```python
import torch

def _softmax(x, axis=-1):
    return torch.softmax(x, dim=axis)

def _gelu(x):
    return torch.nn.functional.gelu(x, approximate='tanh')

def _silu(x):
    return torch.nn.functional.silu(x)

def _sigmoid(x):
    return torch.sigmoid(x)

def _layernorm(x, axis=-1, eps=1e-05):
    m = x.mean(dim=axis, keepdim=True)
    v = x.var(dim=axis, keepdim=True, unbiased=False)
    return (x - m) / torch.sqrt(v + eps)

def _scatter_add(tgt, idx, src, axis=0):
    return tgt.clone().index_add_(axis, idx, src)

def _segment_sum(src, seg, n):
    out = torch.zeros((n,) + src.shape[1:], dtype=src.dtype)
    return out.index_add_(0, seg, src)

def _pad(x, pw):
    flat = tuple((v for pair in reversed(pw) for v in pair))
    return torch.nn.functional.pad(x, flat)
kernel_name = 'recurrent_scan__memory_aug_recurrent_mem_tr__glu_gated_glu__prompt'

def setup_inputs():

    _w = lambda a: (lambda t: t.to(torch.bfloat16) if t.is_floating_point() else t)(torch.as_tensor(a))

    def _A_setup():
        B, S, Dm = (1, 2048, 1024)
        H, Dh = (32, 128)
        M, seg = (8, 32)
        seg = min(seg, S)
        S = S // seg * seg
        return {'x': _w((lambda *s: torch.randn(*s, dtype=torch.bfloat16) * 0.05)(B, S, Dm)), 'mem0': _w((lambda *s: torch.randn(*s, dtype=torch.bfloat16) * 0.2)(B, M, Dm)), 'Wq': _w((lambda *s: torch.randn(*s, dtype=torch.bfloat16) * 0.8)(Dm, H, Dh) * Dm ** (-0.5)), 'Wk': _w((lambda *s: torch.randn(*s, dtype=torch.bfloat16) * 0.8)(Dm, H, Dh) * Dm ** (-0.5)), 'Wv': _w((lambda *s: torch.randn(*s, dtype=torch.bfloat16) * 0.2)(Dm, H, Dh) * Dm ** (-0.5)), 'Wo': _w((lambda *s: torch.randn(*s, dtype=torch.bfloat16) * 0.2)(H, Dh, Dm) * (H * Dh) ** (-0.5)), 'seg_len': seg}
    _A_in = _A_setup()

    def _B_setup():
        B, S, D, F = (1, 2048, 1024, 3584)
        return {'x': _w((lambda *s: torch.randn(*s, dtype=torch.bfloat16) * 0.05)(B, S, D)), 'w_gate': _w((lambda *s: torch.randn(*s, dtype=torch.bfloat16) * 0.2)(D, F)), 'w_up': _w((lambda *s: torch.randn(*s, dtype=torch.bfloat16) * 0.1)(D, F)), 'w_down': _w((lambda *s: torch.randn(*s, dtype=torch.bfloat16) * 0.1)(F, D))}
    _B_in = _B_setup()
    return {'A_x': _A_in['x'], 'A_mem0': _A_in['mem0'], 'A_Wq': _A_in['Wq'], 'A_Wk': _A_in['Wk'], 'A_Wv': _A_in['Wv'], 'A_Wo': _A_in['Wo'], 'A_seg_len': _A_in['seg_len'], 'B_x': _B_in['x'], 'B_w_gate': _B_in['w_gate'], 'B_w_up': _B_in['w_up'], 'B_w_down': _B_in['w_down']}

def reference(A_x, A_mem0, A_Wq, A_Wk, A_Wv, A_Wo, A_seg_len, B_x, B_w_gate, B_w_up, B_w_down):

    def _A_ref(x, mem0, Wq, Wk, Wv, Wo, seg_len):
        B, S, Dm = x.shape
        H, Dh = (Wq.shape[1], Wq.shape[2])
        M = mem0.shape[1]
        scale = Dh ** (-0.5)
        n_seg = S // seg_len
        L = M + seg_len
        idx = torch.arange(L)
        mask = (idx[:, None] < M) | (idx[:, None] >= idx[None, :])
        mem = mem0
        outs = []
        for s in range(n_seg):
            xs = x[:, s * seg_len:(s + 1) * seg_len, :].clone()
            h = torch.concatenate([mem, xs], axis=1)
            q = torch.einsum('btd,dhe->bhte', h, Wq)
            k = torch.einsum('btd,dhe->bhte', h, Wk)
            v = torch.einsum('btd,dhe->bhte', h, Wv)
            scores = torch.einsum('bhqe,bhke->bhqk', q, k) * scale
            scores = torch.where(mask[None, None], scores, -1000000000.0)
            p = _softmax(scores, axis=-1)
            o = torch.einsum('bhqk,bhke->bhqe', p, v)
            o = torch.einsum('bhte,hed->btd', o, Wo)
            mem = o[:, :M, :].clone()
            outs.append(o[:, M:, :])
        return torch.concatenate(outs, axis=1)

    def _B_ref(x, w_gate, w_up, w_down):
        g = torch.einsum('bsd,df->bsf', x, w_gate)
        u = torch.einsum('bsd,df->bsf', x, w_up)
        h = _sigmoid(g) * u
        return torch.einsum('bsf,fd->bsd', h, w_down)
    x = A_x
    xn = _layernorm(x, axis=-1)
    s = _A_ref(xn, A_mem0, A_Wq, A_Wk, A_Wv, A_Wo, A_seg_len)
    g = _silu(_B_ref(xn, B_w_gate, B_w_up, B_w_down))
    return x + s * g
```

```cpp
#include <hip/hip_runtime.h>
#include <hip/hip_bf16.h>
#include <stdint.h>

constexpr int S_LEN = 2048, DM = 1024, NH = 32, DH = 128, NMEM = 8, SEG = 32;
constexpr int NSEG = S_LEN / SEG;
constexpr int HD = NH * DH;
constexpr int FF = 3584;

typedef unsigned short u16;
typedef __attribute__((ext_vector_type(8))) short bf16x8;
typedef __attribute__((ext_vector_type(4))) short s16x4;
typedef __attribute__((ext_vector_type(4))) float f32x4;
typedef __attribute__((ext_vector_type(4))) unsigned int u32x4;
typedef __attribute__((ext_vector_type(2))) unsigned int u32x2;
typedef __attribute__((address_space(3))) s16x4* lds_s16x4_ptr;
typedef __attribute__((address_space(3))) void* lds_void_ptr;
typedef __attribute__((address_space(1))) const void* glb_cvoid_ptr;
typedef __attribute__((address_space(1))) unsigned gu32;
typedef __bf16 bf16x2_t __attribute__((ext_vector_type(2)));
typedef float f32x2_t __attribute__((ext_vector_type(2)));

__device__ __forceinline__ float bf2f(u16 v) { return __uint_as_float(((unsigned)v) << 16); }
__device__ __forceinline__ unsigned cvtpk(float lo, float hi) {
  bf16x2_t r = __builtin_convertvector((f32x2_t){lo, hi}, bf16x2_t);
  return __builtin_bit_cast(unsigned, r);
}
__device__ __forceinline__ float lo_f(unsigned v) { return __uint_as_float(v << 16); }
__device__ __forceinline__ float hi_f(unsigned v) { return __uint_as_float(v & 0xffff0000u); }
__device__ __forceinline__ float rbf(float f) { return lo_f(cvtpk(f, f)); }
__device__ __forceinline__ bf16x8 as_bf16x8(u32x4 v) { return __builtin_bit_cast(bf16x8, v); }
#define MFMA(a, b, c) __builtin_amdgcn_mfma_f32_16x16x32_bf16((a), (b), (c), 0, 0, 0)
typedef __attribute__((ext_vector_type(16))) short bf16x16;
typedef __attribute__((ext_vector_type(8))) __bf16 bfv8;
typedef __attribute__((ext_vector_type(16))) __bf16 bfv16;
#define SMFMA(a, b, c, idx) __builtin_amdgcn_smfmac_f32_16x16x64_bf16(__builtin_bit_cast(bfv8, (a)), __builtin_bit_cast(bfv16, (b)), (c), (idx), 0, 0)
__device__ __forceinline__ bf16x16 cat16(bf16x8 lo, bf16x8 hi) {
  return __builtin_shufflevector(lo, hi, 0, 1, 2, 3, 4, 5, 6, 7, 8, 9, 10, 11, 12, 13, 14, 15);
}

#define KSTR 65
#define XLD 1024
constexpr int PAGE = 2 << 20, NPAGE = 14;
constexpr int T_K = 0, T_Q = 2304, T_V = 4608, T_O = 6912, T_END = T_O + 2048;
constexpr unsigned T_BYTES = T_END * 16384u;
constexpr size_t WS_SY = 0;
constexpr size_t WS_T = (size_t)NPAGE * PAGE;
constexpr size_t WS_XN = WS_T + T_BYTES;
constexpr size_t WS_HF = WS_XN + (size_t)S_LEN * XLD * 2;
constexpr size_t WS_G = WS_HF + (size_t)S_LEN * 16384;
constexpr size_t WS_S = WS_G + (size_t)S_LEN * DM * 2;
constexpr size_t WS_DP = WS_S + (size_t)S_LEN * DM * 2;
constexpr size_t WS_END = WS_DP + (size_t)64 * 65536;
constexpr unsigned SENT = 0xFFFFFFFFu;
constexpr unsigned DSENT = 0xFFBFFFBFu;
constexpr int SY_MEM = 0;
constexpr int SY_O = 2 * PAGE;
constexpr int SY_V = 6 * PAGE;
constexpr int SY_BR = PAGE, SY_BA = SY_BR + 16384;
constexpr int SY_FKQ = SY_BR + 32768;
constexpr int SY_FG = SY_FKQ + 16 * NH * 4 * 128;
constexpr int SY_FO = SY_FG + 16 * 8 * 128;
constexpr int SY_CH = SY_FO + 384 * 128;
constexpr int SY_TK = SY_CH + 16 * 128;
constexpr int SY_Q = SY_TK + 64 * 256;
constexpr int SY_ABORT = SY_Q + 8 * 128;
constexpr int SY_FS = SY_ABORT + 256;
constexpr int SY_FEND = SY_FS + 64 * 128;
constexpr int SY_PAR = SY_FEND;
constexpr int SY_TOTAL = NPAGE * PAGE;
constexpr int PROBE = PAGE - 16384 + 6144;
__device__ __forceinline__ int side(unsigned pm, int page) { return ((pm >> page) & 1u) << 13; }
__device__ __forceinline__ int o_mem(unsigned pm, int s) { return SY_MEM + s * 32768 + side(pm, 0); }
__device__ __forceinline__ int o_o(unsigned pm, int s, int h) {
  return SY_O + s * 131072 + (h >> 3) * 16384 + ((h >> 1) & 3) * 2048 + (h & 1) * 128 + side(pm, 2 + (s >> 4));
}
__device__ __forceinline__ int o_v(unsigned pm, int s, int h) { return SY_V + s * 262144 + (h >> 1) * 16384 + (h & 1) * 4096 + side(pm, 6 + (s >> 3)); }
__device__ __forceinline__ char* ws_align(char* ws) { return ws + ((PAGE - (unsigned)(uintptr_t)ws) & (PAGE - 1)); }
#define NFUSE 8
constexpr int NPRE = 4;
static_assert(WS_END + PAGE <= (size_t)256 << 20 && NH * KSTR <= T_Q && NPAGE % 2 == 0, "");
constexpr int NCHAIN = 128, NGW = 32;
__host__ __device__ constexpr int kseg(int sg, int h) { return h * KSTR + sg; }
__host__ __device__ constexpr int fkq(int blk, int h, int kind) { return SY_FKQ + ((blk * NH + h) * 4 + kind) * 128; }

#define HSKEW 0
#define XPROBE 4
struct Args {
  const u16 *x, *mem0, *Wq, *Wk, *Wv, *Wo, *wg, *wu, *wd;
  char* ws;
  u16* out;
  int s0, pad1;
};
constexpr int HLD = 16384 + HSKEW;
__device__ __forceinline__ int h_off(const Args& p, int rblk) { return rblk * PAGE + (((p.s0 ^ rblk ^ 1) & 1) << 13); }
static_assert(WS_HF % (2 * PAGE) == 0 && 127 * HSKEW + FF * 2 <= 8192, "");
#define TF_KQV 1
#define TF_O 0
__device__ __forceinline__ int t_off(const Args& p, int t) { return t * 16384 + (((p.s0 ^ (t >> 7) ^ (TF_KQV == 2 ? t : TF_KQV)) & 1) << 13); }
__device__ __forceinline__ int t_off_o(const Args& p, int t) { return t * 16384 + (((p.s0 ^ (t >> 7) ^ (TF_O == 2 ? t : TF_O)) & 1) << 13); }

#define LN_AUX 0
__device__ __forceinline__ float wave_sum(float v) {
#pragma unroll
  for (int i = 0; i < 4; ++i) {
    const int x = (int)__float_as_uint(v);
    v += __uint_as_float(i == 0 ? __builtin_amdgcn_update_dpp(0, x, 0x128, 0xf, 0xf, false) : i == 1 ? __builtin_amdgcn_update_dpp(0, x, 0x124, 0xf, 0xf, false)
                         : i == 2 ? __builtin_amdgcn_update_dpp(0, x, 0x122, 0xf, 0xf, false) : __builtin_amdgcn_update_dpp(0, x, 0x121, 0xf, 0xf, false));
  }
  auto r = __builtin_amdgcn_permlane16_swap(__float_as_uint(v), __float_as_uint(v), false, false);
  v = __uint_as_float(r[0]) + __uint_as_float(r[1]);
  r = __builtin_amdgcn_permlane32_swap(__float_as_uint(v), __float_as_uint(v), false, false);
  return __uint_as_float(r[0]) + __uint_as_float(r[1]);
}
__global__ __launch_bounds__(512) void ln_kernel(const u16* __restrict__ x, char* ws_, const u16* __restrict__ mem0) {
  char* ws = ws_align(ws_);
  const int lane = threadIdx.x & 63, w = threadIdx.x >> 6;
  if (blockIdx.x < 8 * NPAGE) {
    if ((blockIdx.x & 7) != XPROBE) return;
    __shared__ unsigned tm[16];
    const __amdgpu_buffer_rsrc_t rs = __builtin_amdgcn_make_buffer_rsrc(ws + (size_t)(blockIdx.x >> 3) * PAGE + PROBE, 0, 16384, 0x00020000);
    if (lane < 32) __builtin_amdgcn_raw_buffer_store_b128((u32x4){DSENT, DSENT, DSENT, DSENT}, rs, (lane >> 4) * 8192 + w * 256 + (lane & 15) * 16, 0, 16);
    unsigned t[4];
#pragma unroll
    for (int i = 0; i < 4; ++i) {
      unsigned long long c0, c1; unsigned v, off = ((i ^ (i >> 1)) & 1) * 8192 + w * 256 + (i >> 1) * 128;
      asm volatile("s_waitcnt vmcnt(0)\n\ts_barrier\n\ts_memtime %0\n\ts_waitcnt lgkmcnt(0)\n\tbuffer_load_dword %2, %3, %4, 0 offen sc1\n\ts_waitcnt vmcnt(0)\n\t"
                   "s_memtime %1\n\ts_waitcnt lgkmcnt(0)" : "=&s"(c0), "=&s"(c1), "=&v"(v) : "v"(off), "s"(rs) : "memory");
      t[i] = (unsigned)(c1 - c0);
    }
    if (lane == 0) { tm[w] = min(t[0], t[3]); tm[8 + w] = min(t[1], t[2]); }
    __syncthreads();
    if (threadIdx.x == 0) {
      unsigned m0 = tm[0], m1 = tm[8];
#pragma unroll
      for (int i = 1; i < 8; ++i) { m0 = min(m0, tm[i]); m1 = min(m1, tm[8 + i]); }
      reinterpret_cast<unsigned*>(ws + SY_PAR)[blockIdx.x >> 3] = min(m0, 30000u) << 16 | min(m1, 30000u);
    }
    return;
  }
  const int bid = blockIdx.x - 8 * NPAGE;
  const int row = bid * 8 + w;
  const u16* xr = x + (size_t)row * DM;
  float v[16];
  u32x4 a = *reinterpret_cast<const u32x4*>(xr + lane * 8);
  u32x4 b = *reinterpret_cast<const u32x4*>(xr + 512 + lane * 8);
  {
    const int i = bid * 512 + threadIdx.x;
    const __amdgpu_buffer_rsrc_t rs = __builtin_amdgcn_make_buffer_rsrc(ws + WS_SY, 0, SY_TOTAL, 0x00020000);
    static_assert(NPRE == 4 && (SY_FEND - SY_BR) / 16 <= 24576, "");
    const int o = i >= 65536 ? SY_V + (i - 65536) * 16 : (i >= 32768 ? SY_O + (i - 32768) * 16 : (i < 4096 ? i * 16
                  : (i >= 8192 && i - 8192 < (SY_FEND - SY_BR) / 16 ? SY_BR + (i - 8192) * 16 : 0x7F000000)));
    const unsigned f = i >= 8192 && i < 32768 ? SENT : DSENT;
    u32x4 m = {f, f, f, f};
    if (i < 2048) {
      const int j = (i >> 10) * 512 + (i & 511);
      const unsigned* m0 = reinterpret_cast<const unsigned*>(mem0 + (2 * ((j >> 2) & 3) + (j & 1)) * DM + (j >> 4) * 16) + ((j >> 1) & 1);
#pragma unroll
      for (int k = 0; k < 4; ++k) { const unsigned t = m0[2 * k]; m[k] = t == DSENT ? 0xFFFFFFFFu : t; }
    }
    __builtin_amdgcn_raw_buffer_store_b128(m, rs, o, 0, LN_AUX);
  }
#pragma unroll
  for (int i = 0; i < 4; ++i) {
    v[2 * i] = lo_f(a[i]); v[2 * i + 1] = hi_f(a[i]);
    v[8 + 2 * i] = lo_f(b[i]); v[8 + 2 * i + 1] = hi_f(b[i]);
  }
  float s = 0.f;
#pragma unroll
  for (int i = 0; i < 16; ++i) s += v[i];
  const float mean = wave_sum(s) * (1.0f / DM);
  float q = 0.f;
#pragma unroll
  for (int i = 0; i < 16; ++i) { float d = v[i] - mean; q += d * d; }
  const float var = rbf(wave_sum(q) * (1.0f / DM));
  const float mb = rbf(mean);
  const float inv = 1.0f / rbf(sqrtf(rbf(var + 1e-5f)));
  u32x4 oa, ob;
#pragma unroll
  for (int i = 0; i < 4; ++i) {
    const unsigned t = cvtpk(v[2 * i] - mb, v[2 * i + 1] - mb), u = cvtpk(v[8 + 2 * i] - mb, v[8 + 2 * i + 1] - mb);
    oa[i] = cvtpk(lo_f(t) * inv, hi_f(t) * inv);
    ob[i] = cvtpk(lo_f(u) * inv, hi_f(u) * inv);
  }
  const __amdgpu_buffer_rsrc_t ro = __builtin_amdgcn_make_buffer_rsrc(ws + WS_XN, 0, S_LEN * XLD * 2, 0x00020000);
  __builtin_amdgcn_raw_buffer_store_b128(oa, ro, row * (XLD * 2) + lane * 16, 0, LN_AUX);
  __builtin_amdgcn_raw_buffer_store_b128(ob, ro, row * (XLD * 2) + 1024 + lane * 16, 0, LN_AUX);
}

__shared__ __attribute__((aligned(16))) char g_smem[163840];
__device__ __forceinline__ unsigned lds_off(const void* p) {
  return (unsigned)(uintptr_t)(lds_void_ptr)g_smem + (unsigned)((const char*)p - g_smem);
}
__device__ __forceinline__ __amdgpu_buffer_rsrc_t mkrsrc(const void* p, unsigned bytes) {
  return __builtin_amdgcn_make_buffer_rsrc((void*)p, 0, bytes, 0x00020000);
}
__device__ __forceinline__ u32x4 ld_sc1(__amdgpu_buffer_rsrc_t rs, int off) {
  return __builtin_amdgcn_raw_buffer_load_b128(rs, off, 0, 16);
}
__device__ __forceinline__ void st_sc1(__amdgpu_buffer_rsrc_t rs, int off, u32x4 v) {
  __builtin_amdgcn_raw_buffer_store_b128(v, rs, off, 0, 16);
}
__device__ __forceinline__ unsigned ror8(unsigned v) {
  return (unsigned)__builtin_amdgcn_update_dpp(0, (int)v, 0x128, 0xf, 0xf, false);
}
__device__ __forceinline__ float ror8f(float v) { return __uint_as_float(ror8(__float_as_uint(v))); }
__device__ __forceinline__ void swap16(unsigned& x, unsigned& y) {
  auto r = __builtin_amdgcn_permlane16_swap(x, y, false, false);
  x = r[0]; y = r[1];
}
__device__ __forceinline__ void swap32(unsigned& x, unsigned& y) {
  auto r = __builtin_amdgcn_permlane32_swap(x, y, false, false);
  x = r[0]; y = r[1];
}
__device__ __forceinline__ f32x4 fold32(f32x4 x, f32x4 y) {
  f32x4 o;
#pragma unroll
  for (int j = 0; j < 4; ++j) {
    unsigned a = __float_as_uint(x[j]), b = __float_as_uint(y[j]);
    swap32(a, b);
    o[j] = __uint_as_float(a) + __uint_as_float(b);
  }
  return o;
}
__device__ __forceinline__ bf16x8 tr_frag(const char* img_lane) {
  s16x4 lo = __builtin_amdgcn_ds_read_tr16_b64_v4i16((lds_s16x4_ptr)img_lane);
  s16x4 hi = __builtin_amdgcn_ds_read_tr16_b64_v4i16((lds_s16x4_ptr)(img_lane + 64));
  return (bf16x8){lo[0], lo[1], lo[2], lo[3], hi[0], hi[1], hi[2], hi[3]};
}
__device__ __forceinline__ float rowq_max(float v) {
  unsigned a = __float_as_uint(v), b = a;
  swap16(a, b);
  v = __builtin_elementwise_maximum(__uint_as_float(a), __uint_as_float(b));
  a = __float_as_uint(v); b = a;
  swap32(a, b);
  return __builtin_elementwise_maximum(__uint_as_float(a), __uint_as_float(b));
}
__device__ __forceinline__ float rowq_sum(float v) {
  unsigned a = __float_as_uint(v), b = a;
  swap16(a, b);
  v = __uint_as_float(a) + __uint_as_float(b);
  a = __float_as_uint(v); b = a;
  swap32(a, b);
  return __uint_as_float(a) + __uint_as_float(b);
}
__device__ __forceinline__ bool spin_fail(unsigned& spins, gu32* abortw) {
  ++spins;
  if ((spins & 63u) == 0u) {
    const __amdgpu_buffer_rsrc_t ra = mkrsrc((const void*)abortw, 4);
    unsigned z = 0;
    asm volatile("" : "+v"(z));
    if (__builtin_amdgcn_readfirstlane(__builtin_amdgcn_raw_buffer_load_b32(ra, z, 0, 16)) == 1u) return true;
    if (spins > (1u << 19)) {
      __builtin_amdgcn_raw_buffer_store_b32(z + 1u, ra, z, 0, 16);
      return true;
    }
  }
  return false;
}
__device__ __forceinline__ unsigned umax4(u32x4 v) { return max(max(v[0], v[1]), max(v[2], v[3])); }
__device__ __forceinline__ bool has_ds(u32x4 v) { return (v[0] == DSENT) | (v[1] == DSENT) | (v[2] == DSENT) | (v[3] == DSENT); }
__device__ __forceinline__ float fmax2(float a, float b) { return __builtin_elementwise_maximum(a, b); }
__device__ __forceinline__ bf16x8 ldsf(const char* p) { return *reinterpret_cast<const bf16x8*>(p); }
#define SCHED() __builtin_amdgcn_sched_barrier(0)
#define LBAR() do { asm volatile("s_waitcnt lgkmcnt(0)" ::: "memory"); __builtin_amdgcn_s_barrier(); asm volatile("" ::: "memory"); } while (0)
#define WAIT_V(n) asm volatile("s_waitcnt vmcnt(%0)" :: "n"(n) : "memory")

__device__ __forceinline__ void wait_flags(__amdgpu_buffer_rsrc_t rs, int off, gu32* abortw) {
  for (unsigned spins = 0;;) {
    asm volatile("" ::: "memory");
    const unsigned v = __builtin_amdgcn_raw_buffer_load_b32(rs, off, 0, 16);
    if (__all(v != SENT)) break;
    __builtin_amdgcn_s_sleep(4);
    if (spin_fail(spins, abortw)) break;
  }
  asm volatile("" ::: "memory");
}

#define LST(i) do {} while (0)
#define RDY(i) do {} while (0)
#define SMP(k, s, v) do {} while (0)
#define LSTC(i) do {} while (0)
__device__ __forceinline__ unsigned offA(unsigned row, unsigned ch) {
  return row * 128u + 16u * (ch ^ (row & 7u));
}
__device__ __forceinline__ unsigned swzB(unsigned row) { return ((row & 3u) << 2) | ((row >> 2) & 3u); }
__device__ __forceinline__ unsigned offB(unsigned row, unsigned ch) {
  return 256u * row + 16u * (ch ^ swzB(row));
}
__device__ __forceinline__ void glds16(const u16* src, char* dst) {
  const unsigned d = __builtin_amdgcn_readfirstlane(lds_off(dst));
  unsigned keep;
  asm volatile("s_mov_b32 %0, m0\n\ts_mov_b32 m0, %2\n\ts_nop 0\n\tglobal_load_lds_dwordx4 %1, off\n\ts_mov_b32 m0, %0"
               : "=&s"(keep) : "v"(src), "s"(d) : "memory");
}
__device__ __forceinline__ bf16x8 ld_bfrag(const char* sb, int ks, int c, int lane) {
  const unsigned g = lane >> 4, qq = (lane & 15) >> 2, pp = lane & 3;
  s16x4 lo = __builtin_amdgcn_ds_read_tr16_b64_v4i16(
      (lds_s16x4_ptr)(sb + offB(32 * ks + 8 * g + qq, 2 * c + (pp >> 1)) + 8 * (pp & 1)));
  s16x4 hi = __builtin_amdgcn_ds_read_tr16_b64_v4i16(
      (lds_s16x4_ptr)(sb + offB(32 * ks + 8 * g + 4 + qq, 2 * c + (pp >> 1)) + 8 * (pp & 1)));
  return (bf16x8){lo[0], lo[1], lo[2], lo[3], hi[0], hi[1], hi[2], hi[3]};
}

__device__ __forceinline__ void blk_dma(const u16* src, size_t ld, char* slot, int lane) {
  const u16* s = src + (size_t)(lane >> 2) * ld + (lane & 3) * 8;
  glds16(s, slot);
  glds16(s + 16 * ld, slot + 1024);
}
__device__ __forceinline__ bf16x8 blk_frag(const char* slot, int T, int lane) {
  const char* a = slot + ((lane >> 4) * 8 + ((lane & 15) >> 2)) * 64 + (lane & 3) * 16 + T * 8;
  s16x4 lo = __builtin_amdgcn_ds_read_tr16_b64_v4i16((lds_s16x4_ptr)a);
  s16x4 hi = __builtin_amdgcn_ds_read_tr16_b64_v4i16((lds_s16x4_ptr)(a + 256));
  return (bf16x8){lo[0], lo[1], lo[2], lo[3], hi[0], hi[1], hi[2], hi[3]};
}
template <int NB, typename ISSUE, typename SINK>
__device__ __forceinline__ void load_blocks(char* slice, ISSUE&& issue, SINK&& sink) {
#pragma unroll
  for (int i = 0; i < 8; ++i) issue(i, slice + i * 2048);
#pragma unroll
  for (int g = 0; g < NB / 4; ++g) {
    if (g + 1 < NB / 4) WAIT_V(8); else WAIT_V(0);
#pragma unroll
    for (int j = 0; j < 4; ++j) sink(4 * g + j, slice + ((4 * g + j) & 7) * 2048);
    asm volatile("s_waitcnt lgkmcnt(0)" ::: "memory");
    if (g + 2 < NB / 4) {
#pragma unroll
      for (int j = 0; j < 4; ++j) issue(4 * g + 8 + j, slice + ((4 * g + j) & 7) * 2048);
    }
  }
}

#define NST_S 3
template <int MT, bool GLU, int NST = 3>
__device__ __forceinline__ void gemm_main(const u16* A, int lda, const u16* B0, const u16* B1, int ldb, int nk, char* smem,
                                          f32x4 (&acc)[MT][4]) {
  constexpr int ABYTES = MT * 8192, STAGE = ABYTES + 16384, NI = MT + 2;
  int tid = threadIdx.x;
  asm volatile("" : "+v"(tid));
  const int lane = tid & 63;
  const int wid = __builtin_amdgcn_readfirstlane(tid >> 6);
  const int wr = wid >> 1, wc = wid & 1;
  const u16* ap = A + (size_t)(tid >> 3) * lda + 8 * ((tid & 7) ^ ((tid >> 3) & 7));
  const unsigned lg = (tid & 15) ^ swzB((tid >> 4) & 15);
  const u16* bp = (lg < 8 ? B0 + 8 * lg : B1 + 8 * (lg - 8)) + (size_t)(tid >> 4) * ldb;
  auto stage = [&](int kt, int buf) {
    char* sa = smem + buf * STAGE + wid * 1024;
#pragma unroll
    for (int i = 0; i < MT; ++i) glds16(ap + (size_t)i * 64 * lda + kt * 64, sa + i * 8192);
#pragma unroll
    for (int i = 0; i < 2; ++i) glds16(bp + (size_t)(kt * 64 + 32 * i) * ldb, sa + ABYTES + i * 8192);
  };
#pragma unroll
  for (int i = 0; i < MT; ++i)
#pragma unroll
    for (int j = 0; j < 4; ++j) acc[i][j] = (f32x4){0.f, 0.f, 0.f, 0.f};
  LBAR();
#pragma unroll
  for (int i = 0; i < NST - 1; ++i) stage(i, i);
  int buf = 0;
  for (int t = 0; t < nk; ++t) {
    if (t + NST - 2 < nk) WAIT_V(NI * (NST - 2)); else WAIT_V(0);
    LBAR();
    if (t + NST - 1 < nk) stage(t + NST - 1, buf == 0 ? NST - 1 : buf - 1);
    const char* sa = smem + buf * STAGE;
    const char* sb = sa + ABYTES;
    auto ldA = [&](int ks, int m) {
      return *reinterpret_cast<const bf16x8*>(sa + offA(wr * (16 * MT) + m * 16 + (lane & 15), ks * 4 + (lane >> 4)));
    };
    auto ldB = [&](int ks, int n) { return ld_bfrag(sb, ks, GLU ? ((n >> 1) * 4 + wc * 2 + (n & 1)) : (wc * 4 + n), lane); };
    bf16x8 af[2][MT], bfr[2][4];
#pragma unroll
    for (int m = 0; m < MT; ++m) af[0][m] = ldA(0, m);
#pragma unroll
    for (int n = 0; n < 4; ++n) bfr[0][n] = ldB(0, n);
    SCHED();
#pragma unroll
    for (int n = 0; n < 4; ++n) {
      if (n < MT) af[1][n] = ldA(1, n);
      bfr[1][n] = ldB(1, n);
#pragma unroll
      for (int m = 0; m < MT; ++m) acc[m][n] = MFMA(bfr[0][n], af[0][m], acc[m][n]);
      SCHED();
    }
#pragma unroll
    for (int n = 0; n < 4; ++n)
#pragma unroll
      for (int m = 0; m < MT; ++m) acc[m][n] = MFMA(bfr[1][n], af[1][m], acc[m][n]);
    buf = buf == NST - 1 ? 0 : buf + 1;
  }
}

__device__ __forceinline__ u32x4 widen32(unsigned a0, unsigned a1, unsigned b0, unsigned b1) {
  swap32(a0, b0); swap16(a0, b0);
  swap32(a1, b1); swap16(a1, b1);
  return (u32x4){a0, a1, b0, b1};
}

__device__ __forceinline__ void publish_flags(__amdgpu_buffer_rsrc_t rs, int off, int stride, int n) {
  WAIT_V(0);
  LBAR();
  int tid = threadIdx.x;
  asm volatile("" : "+v"(tid));
  if (tid < n) __builtin_amdgcn_raw_buffer_store_b32(1u, rs, off + tid * stride, 0, 16);
}

__device__ __forceinline__ void wait_s(const Args& p, __amdgpu_buffer_rsrc_t rsy, int rblk, int c0, int n, bool need) {
  if (need) {
    if (threadIdx.x < 64) {
      for (unsigned spins = 0;;) {
        asm volatile("" ::: "memory");
        const int f = (int)__builtin_amdgcn_raw_buffer_load_b32(rsy, SY_FS + (2 * c0 + (threadIdx.x & (2 * n - 1))) * 128, 0, 16);
        if (__all(f >= rblk)) break;
        __builtin_amdgcn_s_sleep(32);
        if (spin_fail(spins, (gu32*)(p.ws + WS_SY + SY_ABORT))) break;
      }
    }
    LBAR();
  }
}
__device__ __forceinline__ u32x4 combine(u32x4 sv, u32x4 g, u32x4 x) {
  u32x4 o;
#pragma unroll
  for (int j = 0; j < 4; ++j) {
    const unsigned t = cvtpk(lo_f(sv[j]) * lo_f(g[j]), hi_f(sv[j]) * hi_f(g[j]));
    o[j] = cvtpk(lo_f(x[j]) + lo_f(t), hi_f(x[j]) + hi_f(t));
  }
  return o;
}
struct SX { u32x4 s, x; };
__device__ __forceinline__ SX get_sx(const Args& p, int rblk, int e) {
  SX r; r.s = (u32x4){0u, 0u, 0u, 0u}; r.x = r.s;
  if (rblk < NFUSE) {
    r.s = ld_sc1(mkrsrc(p.ws + WS_S, S_LEN * DM * 2), (((e & 1023) >> 5) * S_LEN + (e >> 10)) * 64 + (e & 31) * 2);
    r.x = *reinterpret_cast<const u32x4*>(p.x + e);
  }
  return r;
}
__device__ __forceinline__ void put_g(const Args& p, __amdgpu_buffer_rsrc_t rg, int rblk, int e, u32x4 g, const SX& v) {
  if (rblk < NFUSE) {
    *reinterpret_cast<u32x4*>(p.out + e) = combine(v.s, g, v.x);
  } else st_sc1(rg, e * 2, g);
}

__device__ __forceinline__ void job_comb(const Args& p, int rblk, int ct, __amdgpu_buffer_rsrc_t rsy) {
  int tid = threadIdx.x;
  asm volatile("" : "+v"(tid));
  u32x4 xv[8];
#pragma unroll
  for (int i = 0; i < 8; ++i) xv[i] = *reinterpret_cast<const u32x4*>(p.x + (rblk * 128 + i * 16 + (tid >> 5)) * DM + ct * 256 + (tid & 31) * 8);
  if (tid < 64) wait_flags(rsy, SY_FG + (rblk * 8 + 2 * ct + (tid & 1)) * 128, (gu32*)(p.ws + WS_SY + SY_ABORT));
  wait_s(p, rsy, rblk, ct * 8, 8, true);
  u32x4 sv[8], gv[8];
#pragma unroll
  for (int i = 0; i < 8; ++i) {
    const int e = (rblk * 128 + i * 16 + (tid >> 5)) * DM + ct * 256 + (tid & 31) * 8;
    sv[i] = ld_sc1(mkrsrc(p.ws + WS_S, S_LEN * DM * 2), (((e & 1023) >> 5) * S_LEN + (e >> 10)) * 64 + (e & 31) * 2);
    gv[i] = ld_sc1(mkrsrc(p.ws + WS_G, S_LEN * DM * 2), e * 2);
  }
#pragma unroll
  for (int i = 0; i < 8; ++i)
    *reinterpret_cast<u32x4*>(p.out + (rblk * 128 + i * 16 + (tid >> 5)) * DM + ct * 256 + (tid & 31) * 8) = combine(sv[i], gv[i], xv[i]);
}

constexpr int VT_IMG = 32 * 72;
struct VT2 { u32x4 f[2]; };
__device__ __forceinline__ VT2 turn_vt(f32x4 a00, f32x4 a01, f32x4 a10, f32x4 a11, char* img, int lane) {
  const int r = lane & 15, q = lane >> 4;
  char* wp = img + r * 72 + q * 8;
  asm volatile("" ::: "memory");
  *reinterpret_cast<u32x2*>(wp) = (u32x2){cvtpk(a00[0], a00[1]), cvtpk(a00[2], a00[3])};
  *reinterpret_cast<u32x2*>(wp + 32) = (u32x2){cvtpk(a01[0], a01[1]), cvtpk(a01[2], a01[3])};
  *reinterpret_cast<u32x2*>(wp + 16 * 72) = (u32x2){cvtpk(a10[0], a10[1]), cvtpk(a10[2], a10[3])};
  *reinterpret_cast<u32x2*>(wp + 16 * 72 + 32) = (u32x2){cvtpk(a11[0], a11[1]), cvtpk(a11[2], a11[3])};
  asm volatile("" ::: "memory");
  const char* rp = img + (4 * q + (r >> 2)) * 72 + (r & 3) * 8;
  VT2 o;
#pragma unroll
  for (int n = 0; n < 2; ++n) {
    s16x4 lo = __builtin_amdgcn_ds_read_tr16_b64_v4i16((lds_s16x4_ptr)(rp + n * 32));
    s16x4 hi = __builtin_amdgcn_ds_read_tr16_b64_v4i16((lds_s16x4_ptr)(rp + n * 32 + 16 * 72));
    o.f[n] = __builtin_bit_cast(u32x4, (bf16x8){lo[0], lo[1], lo[2], lo[3], hi[0], hi[1], hi[2], hi[3]});
  }
  asm volatile("" ::: "memory");
  return o;
}
template <int N, typename TURN, typename OFF>
__device__ __forceinline__ void put_vt(__amdgpu_buffer_rsrc_t ro, int lane, TURN&& turn, OFF&& off) {
  VT2 cur = turn(0);
#pragma unroll
  for (int i = 0; i < N; ++i) {
    VT2 nx = cur;
    if (i + 1 < N) nx = turn(i + 1);
#pragma unroll
    for (int n = 0; n < 2; ++n) st_sc1(ro, off(i) + n * 1024 + lane * 16, cur.f[n]);
    cur = nx;
  }
}

__device__ __forceinline__ void job_kq(const Args& p, int which, int blk, int h, char* smem, __amdgpu_buffer_rsrc_t rsy) {
  const u16* xn = (const u16*)(p.ws + WS_XN);
  const u16* W = which == 0 ? p.Wk : p.Wq;
  asm volatile("" : "+s"(W));
  if (which == 2) W = p.Wv;
  W += h * DH;
  f32x4 acc[2][4];
  gemm_main<2, false, NST_S>(xn + (size_t)blk * 128 * XLD, XLD, W, W + 64, HD, DM / 64, smem, acc);
  int tid = threadIdx.x;
  asm volatile("" : "+v"(tid));
  const int lane = tid & 63, wid = __builtin_amdgcn_readfirstlane(tid >> 6), wr = wid >> 1, wc = wid & 1;
  const __amdgpu_buffer_rsrc_t ro = mkrsrc(p.ws + WS_T, T_BYTES);
  const int to = t_off(p, which * T_Q + kseg(blk * 4 + wr, h));
  if (which == 2) {
    put_vt<2>(ro, lane, [&](int nb) { return turn_vt(acc[0][2 * nb], acc[0][2 * nb + 1], acc[1][2 * nb], acc[1][2 * nb + 1], smem + 131072 + wid * VT_IMG, lane); },
              [&](int nb) { return to + (wc * 4 + nb * 2) * 1024; });
  } else {
#pragma unroll
    for (int m = 0; m < 2; ++m)
#pragma unroll
      for (int nb = 0; nb < 2; ++nb) {
        u32x4 o = widen32(cvtpk(acc[m][2 * nb][0], acc[m][2 * nb][1]), cvtpk(acc[m][2 * nb][2], acc[m][2 * nb][3]),
                          cvtpk(acc[m][2 * nb + 1][0], acc[m][2 * nb + 1][1]), cvtpk(acc[m][2 * nb + 1][2], acc[m][2 * nb + 1][3]));
        st_sc1(ro, to + (m * 4 + wc * 2 + nb) * 1024 + lane * 16, o);
      }
  }
  publish_flags(rsy, fkq(blk, h, which), 0, 1);
}

__device__ __forceinline__ unsigned ld_flag0(__amdgpu_buffer_rsrc_t rs, int off) {
  return __builtin_amdgcn_raw_buffer_load_b32(rs, off, 0, 16);
}
__device__ __forceinline__ void glds16_sc1(const char* src, char* dst) {
  const unsigned d = __builtin_amdgcn_readfirstlane(lds_off(dst));
  unsigned keep;
  asm volatile("s_mov_b32 %0, m0\n\ts_mov_b32 m0, %2\n\ts_nop 0\n\tglobal_load_lds_dwordx4 %1, off sc1\n\ts_mov_b32 m0, %0"
               : "=&s"(keep) : "v"(src), "s"(d) : "memory");
}

__device__ __forceinline__ void touch_line(const char* src, char* dst) {
  const unsigned d = __builtin_amdgcn_readfirstlane(lds_off(dst));
  unsigned keep;
  asm volatile("s_mov_b32 %0, m0\n\ts_mov_b32 m0, %2\n\ts_nop 0\n\tglobal_load_lds_dword %1, off sc1\n\ts_mov_b32 m0, %0"
               : "=&s"(keep) : "v"(src), "s"(d) : "memory");
}

#define GPF 1
__device__ __forceinline__ void glds2(const char* base, int step, unsigned voff, unsigned dst) {
  unsigned keep;
  const char* base2 = base + step;
  asm volatile("s_mov_b32 %0, m0\n\ts_mov_b32 m0, %4\n\ts_nop 0\n\tglobal_load_lds_dwordx4 %1, %2\n\t"
               "s_add_u32 m0, m0, 0x2000\n\ts_nop 0\n\tglobal_load_lds_dwordx4 %1, %3\n\ts_mov_b32 m0, %0"
               : "=&s"(keep) : "v"(voff), "s"(base), "s"(base2), "s"(dst) : "memory", "scc");
}

__device__ __forceinline__ void gemm_stream(const char* A, int lda, const char* B, int ldb, int nk, char* smem, f32x4 (&acc)[2][4]) {
  int tid = threadIdx.x;
  asm volatile("" : "+v"(tid));
  const int lane = tid & 63, r = lane & 15, q = lane >> 4;
  const int wid = __builtin_amdgcn_readfirstlane(tid >> 6);
  const int wr = wid >> 1, wc = wid & 1;
  const unsigned va = (tid >> 3) * lda + 16 * ((tid & 7) ^ ((tid >> 3) & 7)), vb = (tid >> 4) * ldb + 16 * ((tid & 15) ^ swzB((tid >> 4) & 15));
  const unsigned lw = lds_off(smem) + wid * 1024;
  auto stage = [&](int kt) {
    glds2(A + kt * 128, 64 * lda, va, lw + (kt & 3) * 32768);
    glds2(B + (size_t)kt * 64 * ldb, 32 * ldb, vb, lw + (kt & 3) * 32768 + 16384);
  };
  unsigned la[2], lb[4][2];
#pragma unroll
  for (int k = 0; k < 2; ++k) { la[k] = lds_off(smem) + offA(wr * 32 + r, k * 4 + q); asm volatile("" : "+v"(la[k])); }
#pragma unroll
  for (int n = 0; n < 4; ++n)
#pragma unroll
    for (int u = 0; u < 2; ++u) {
      lb[n][u] = lds_off(smem) + 16384 + offB(8 * q + 4 * u + (r >> 2), 2 * (wc * 4 + n) + ((r & 3) >> 1)) + 8 * (r & 1);
      asm volatile("" : "+v"(lb[n][u]));
    }
  typedef __attribute__((address_space(3))) const bf16x8* lds_frag_ptr;
  bf16x8 af[2][2], bf[2][4];
  auto ldA = [&](int bo, int ks, int m) { return *(lds_frag_ptr)(uintptr_t)(la[ks] + bo + m * 2048); };
  auto ldB = [&](int bo, int ks, int n) {
    s16x4 lo = __builtin_amdgcn_ds_read_tr16_b64_v4i16((lds_s16x4_ptr)(uintptr_t)(lb[n][0] + bo + ks * 8192));
    s16x4 hi = __builtin_amdgcn_ds_read_tr16_b64_v4i16((lds_s16x4_ptr)(uintptr_t)(lb[n][1] + bo + ks * 8192));
    return (bf16x8){lo[0], lo[1], lo[2], lo[3], hi[0], hi[1], hi[2], hi[3]};
  };
#pragma unroll
  for (int i = 0; i < 8; ++i) acc[i >> 2][i & 3] = (f32x4){0.f, 0.f, 0.f, 0.f};
  LBAR();
  stage(0); stage(1); stage(2);
  WAIT_V(8);
  LBAR();
#pragma unroll
  for (int m = 0; m < 2; ++m) af[0][m] = ldA(0, 0, m);
#pragma unroll
  for (int n = 0; n < 4; ++n) bf[0][n] = ldB(0, 0, n);
  for (int t0 = 0; t0 < nk; t0 += 4) {
#pragma unroll
    for (int b = 0; b < 4; ++b) {
      const int t = t0 + b;
#pragma unroll
      for (int ks = 0; ks < 2; ++ks) {
        if (ks == 1) {
          if (t + 2 < nk) WAIT_V(4); else WAIT_V(0);
          __builtin_amdgcn_s_barrier();
          if (t + 3 < nk) stage(t + 3);
        }
        const int nb = (ks ? (b + 1) & 3 : b) * 32768, nks = ks ^ 1;
#pragma unroll
        for (int n = 0; n < 4; ++n) {
          acc[0][n] = MFMA(bf[ks][n], af[ks][0], acc[0][n]);
          bf[nks][n] = ldB(nb, nks, n);
          acc[1][n] = MFMA(bf[ks][n], af[ks][1], acc[1][n]);
          if (n < 2) af[nks][n] = ldA(nb, nks, n);
          SCHED();
        }
      }
    }
  }
}

#define HSL 40
__device__ __forceinline__ void wait_h(const Args& p, int panel, int half) {
  if (threadIdx.x == 0) {
    gu32* cnt = (gu32*)(p.ws + WS_SY + SY_CH) + panel * 64;
    for (unsigned spins = 0;;) {
      const unsigned c0 = __hip_atomic_load(cnt + (half == 1 ? 32 : 0), __ATOMIC_RELAXED, __HIP_MEMORY_SCOPE_AGENT);
      const unsigned c1 = __hip_atomic_load(cnt + (half == 0 ? 0 : 32), __ATOMIC_RELAXED, __HIP_MEMORY_SCOPE_AGENT);
      if (c0 + 1u >= 14u && c1 + 1u >= 14u) break;
      __builtin_amdgcn_s_sleep(HSL);
      if (++spins > (1u << 20)) { __hip_atomic_store((gu32*)(p.ws + WS_SY + SY_ABORT), 1u, __ATOMIC_RELAXED, __HIP_MEMORY_SCOPE_AGENT); break; }
    }
    __builtin_amdgcn_fence(__ATOMIC_ACQUIRE, "agent");
    asm volatile("s_waitcnt vmcnt(0)" ::: "memory");
  }
  __syncthreads();
}
__device__ __forceinline__ void job_down(const Args& p, int rblk, int ct, int half, char* smem, __amdgpu_buffer_rsrc_t rsy) {
  wait_h(p, rblk >> 1, half);
  f32x4 acc[2][4];
  const int k0 = half == 1 ? FF / 2 : 0;
  gemm_stream(p.ws + WS_HF + h_off(p, rblk) + k0 * 2, HLD, (const char*)(p.wd + (size_t)k0 * DM + ct * 128), DM * 2,
              half < 2 ? FF / 128 : FF / 64, smem, acc);
  if (half < 2) {
    int tid = threadIdx.x;
    asm volatile("" : "+v"(tid));
    const int tile = (rblk & 7) * 8 + ct;
    const __amdgpu_buffer_rsrc_t rp = mkrsrc(p.ws + WS_DP + (size_t)tile * 65536, 65536);
    volatile unsigned* slot = reinterpret_cast<volatile unsigned*>(smem + 163808);
    if (tid == 0) *slot = __hip_atomic_fetch_add((gu32*)(p.ws + WS_SY + SY_TK) + tile * 64, 1u, __ATOMIC_RELAXED, __HIP_MEMORY_SCOPE_AGENT);
    LBAR();
    if (__builtin_amdgcn_readfirstlane(*slot) == SENT) {
#pragma unroll
      for (int i = 0; i < 8; ++i) st_sc1(rp, i * 8192 + tid * 16, __builtin_bit_cast(u32x4, acc[i >> 2][i & 3]));
      publish_flags(rsy, SY_TK + tile * 256 + 128, 0, 1);
      return;
    }
    if (tid < 64) wait_flags(rsy, SY_TK + tile * 256 + 128, (gu32*)(p.ws + WS_SY + SY_ABORT));
    LBAR();
#pragma unroll
    for (int i = 0; i < 8; ++i) acc[i >> 2][i & 3] += __builtin_bit_cast(f32x4, ld_sc1(rp, i * 8192 + tid * 16));
  }
  wait_s(p, rsy, rblk, ct * 4, 4, rblk < NFUSE);
  int tid = threadIdx.x;
  asm volatile("" : "+v"(tid));
  const int lane = tid & 63, wid = tid >> 6, wr = wid >> 1, wc = wid & 1;
  const __amdgpu_buffer_rsrc_t ro = mkrsrc(p.ws + WS_G, S_LEN * DM * 2);
  SX sx[4];
#pragma unroll
  for (int i = 0; i < 4; ++i)
    sx[i] = get_sx(p, rblk, (rblk * 128 + wr * 32 + (i >> 1) * 16 + (lane & 15)) * DM + ct * 128 + wc * 64 + (i & 1) * 32 + (lane >> 4) * 8);
#pragma unroll
  for (int m = 0; m < 2; ++m) {
    const int row = rblk * 128 + wr * 32 + m * 16 + (lane & 15);
#pragma unroll
    for (int nb = 0; nb < 2; ++nb) {
      unsigned pk[2][2];
#pragma unroll
      for (int n = 0; n < 2; ++n) {
        float rr[4];
#pragma unroll
        for (int j = 0; j < 4; ++j) { float d = rbf(acc[m][2 * nb + n][j]); rr[j] = d * __builtin_amdgcn_rcpf(1.0f + __expf(-d)); }
        pk[n][0] = cvtpk(rr[0], rr[1]); pk[n][1] = cvtpk(rr[2], rr[3]);
      }
      u32x4 o = widen32(pk[0][0], pk[0][1], pk[1][0], pk[1][1]);
      put_g(p, ro, rblk, row * DM + ct * 128 + wc * 64 + nb * 32 + (lane >> 4) * 8, o, sx[2 * m + nb]);
    }
  }
  publish_flags(rsy, SY_FG + (rblk * 8 + ct) * 128, 0, 1);
}

__device__ __forceinline__ void med_loop(const u16* A, int lda, const u16* B0, const u16* B1, int ldb, int nk, char* smem,
                                         f32x4 (&acc)[4][4]) {
  int tid = threadIdx.x;
  asm volatile("" : "+v"(tid));
  const int lane = tid & 63;
  const int wid = __builtin_amdgcn_readfirstlane(tid >> 6);
  const int wr = wid >> 2, wc = wid & 3;
  const u16* ap = A + (size_t)(tid >> 3) * lda + 8 * ((tid & 7) ^ ((tid >> 3) & 7));
  const size_t boffs = (size_t)(tid >> 4) * ldb + 8 * ((tid & 15) ^ swzB((tid >> 4) & 15));
  const u16 *bp0 = B0 + boffs, *bp1 = B1 + boffs;
  auto stage = [&](int kt, int buf) {
    char* sa = smem + buf * 49152 + wid * 1024;
#pragma unroll
    for (int i = 0; i < 2; ++i) glds16(ap + (size_t)i * 64 * lda + kt * 64, sa + i * 8192);
#pragma unroll
    for (int i = 0; i < 2; ++i) {
      glds16(bp0 + (size_t)(kt * 64 + 32 * i) * ldb, sa + 16384 + i * 8192);
      glds16(bp1 + (size_t)(kt * 64 + 32 * i) * ldb, sa + 32768 + i * 8192);
    }
  };
#pragma unroll
  for (int i = 0; i < 4; ++i)
#pragma unroll
    for (int j = 0; j < 4; ++j) acc[i][j] = (f32x4){0.f, 0.f, 0.f, 0.f};
  LBAR();
  stage(0, 0);
  stage(1, 1);
  int buf = 0;
  for (int t = 0; t < nk; ++t) {
    if (t + 1 < nk) WAIT_V(6); else WAIT_V(0);
    LBAR();
    if (t + 2 < nk) stage(t + 2, buf == 0 ? 2 : buf - 1);
    const char* sa = smem + buf * 49152;
    const char* sb = sa + 16384 + (wc >> 1) * 16384;
    auto ldB = [&](int ks, int n) { return ld_bfrag(sb, ks, (wc & 1) * 4 + n, lane); };
    auto ldA = [&](int i) {
      return *reinterpret_cast<const bf16x8*>(sa + offA(wr * 64 + (i & 3) * 16 + (lane & 15), (i >> 2) * 4 + (lane >> 4)));
    };
    bf16x8 bfr[2][4], af[4];
    bfr[0][0] = ldB(0, 0); af[0] = ldA(0); bfr[0][1] = ldB(0, 1); bfr[0][2] = ldB(0, 2); bfr[0][3] = ldB(0, 3);
    af[1] = ldA(1); af[2] = ldA(2);
    SCHED();
#pragma unroll
    for (int i = 0; i < 8; ++i) {
      if (i + 3 < 8) af[(i + 3) & 3] = ldA(i + 3);
      if (i < 2) { bfr[1][2 * i] = ldB(1, 2 * i); bfr[1][2 * i + 1] = ldB(1, 2 * i + 1); }
#pragma unroll
      for (int n = 0; n < 4; ++n) acc[i & 3][n] = MFMA(bfr[i >> 2][n], af[i & 3], acc[i & 3][n]);
      SCHED();
    }
    buf = buf == 2 ? 0 : buf + 1;
  }
}

__device__ __forceinline__ void job_kqm(const Args& p, int kind, int blk, int h, char* smem, __amdgpu_buffer_rsrc_t rsy) {
  f32x4 acc[4][4];
  const bool isv = kind == 2;
  const u16* B1 = isv ? p.Wv : p.Wq;
  asm volatile("" : "+s"(B1));
  med_loop((const u16*)(p.ws + WS_XN) + (size_t)blk * 128 * XLD, XLD, (kind == 1 ? p.Wk : B1) + h * DH, B1 + (kind == 1 ? h : h + 8) * DH, HD,
           DM / 64, smem, acc);
  int tid = threadIdx.x;
  asm volatile("" : "+v"(tid));
  const int lane = tid & 63;
  const int wid = __builtin_amdgcn_readfirstlane(tid >> 6);
  const int wr = wid >> 2, wc = wid & 3;
  const __amdgpu_buffer_rsrc_t ro = mkrsrc(p.ws + WS_T, T_BYTES);
  if (isv) {
    LBAR();
    put_vt<4>(ro, lane,
        [&](int i) { const int m = i & 2, n = 2 * (i & 1); return turn_vt(acc[m][n], acc[m][n + 1], acc[m + 1][n], acc[m + 1][n + 1], smem + wid * VT_IMG, lane); },
        [&](int i) { return t_off(p, T_V + kseg(blk * 4 + wr * 2 + (i >> 1), h + (wc >> 1) * 8)) + ((wc & 1) * 4 + 2 * (i & 1)) * 1024; });
    publish_flags(rsy, fkq(blk, h, 2), 8 * 512, 2);
    return;
  }
#pragma unroll
  for (int m = 0; m < 4; ++m) {
#pragma unroll
    for (int nb = 0; nb < 2; ++nb) {
      u32x4 o = widen32(cvtpk(acc[m][2 * nb][0], acc[m][2 * nb][1]), cvtpk(acc[m][2 * nb][2], acc[m][2 * nb][3]),
                        cvtpk(acc[m][2 * nb + 1][0], acc[m][2 * nb + 1][1]), cvtpk(acc[m][2 * nb + 1][2], acc[m][2 * nb + 1][3]));
      st_sc1(ro, t_off(p, (kind ? (wc >> 1) * T_Q : T_Q + (wc >> 1) * 8 * KSTR) + kseg(blk * 4 + wr * 2 + (m >> 1), h)) + ((m & 1) * 4 + (wc & 1) * 2 + nb) * 1024 + lane * 16, o);
    }
  }
  publish_flags(rsy, fkq(blk, h, 1 - kind), kind ? 128 : 8 * 512, 2);
}

__device__ __forceinline__ void job_dnm(const Args& p, int rblk, int ct, char* smem, __amdgpu_buffer_rsrc_t rsy) {
  wait_h(p, rblk >> 1, 2);
  f32x4 acc[4][4];
  med_loop((const u16*)(p.ws + WS_HF + h_off(p, rblk)), HLD / 2, p.wd + ct * 256, p.wd + ct * 256 + 128, DM, FF / 64, smem, acc);
  wait_s(p, rsy, rblk, ct * 8, 8, rblk < NFUSE);
  int tid = threadIdx.x;
  asm volatile("" : "+v"(tid));
  const int lane = tid & 63, wid = tid >> 6, wr = wid >> 2, wc = wid & 3;
  const __amdgpu_buffer_rsrc_t ro = mkrsrc(p.ws + WS_G, S_LEN * DM * 2);
  SX sx[8];
#pragma unroll
  for (int i = 0; i < 8; ++i)
    sx[i] = get_sx(p, rblk, (rblk * 128 + wr * 64 + (i >> 1) * 16 + (lane & 15)) * DM + ct * 256 + wc * 64 + (i & 1) * 32 + (lane >> 4) * 8);
#pragma unroll
  for (int m = 0; m < 4; ++m) {
    const int row = rblk * 128 + wr * 64 + m * 16 + (lane & 15);
#pragma unroll
    for (int nb = 0; nb < 2; ++nb) {
      unsigned pk[2][2];
#pragma unroll
      for (int n = 0; n < 2; ++n) {
        float rr[4];
#pragma unroll
        for (int j = 0; j < 4; ++j) { float d = rbf(acc[m][2 * nb + n][j]); rr[j] = d * __builtin_amdgcn_rcpf(1.0f + __expf(-d)); }
        pk[n][0] = cvtpk(rr[0], rr[1]); pk[n][1] = cvtpk(rr[2], rr[3]);
      }
      u32x4 o = widen32(pk[0][0], pk[0][1], pk[1][0], pk[1][1]);
      put_g(p, ro, rblk, row * DM + ct * 256 + wc * 64 + nb * 32 + (lane >> 4) * 8, o, sx[2 * m + nb]);
    }
  }
  publish_flags(rsy, SY_FG + (rblk * 8 + 2 * ct) * 128, 128, 2);
}

enum { J_NONE = 0, J_KQ = 1, J_VU = 2, J_GU = 3, J_DN = 4, J_KS = 5, J_QS = 6, J_VS = 7, J_CB = 8, J_QQM = 10, J_KQM = 11, J_VUM = 12, J_DNM = 13, J_DNS = 14, J_DNH = 15 };
struct BigJob { int type, panel, idx; };
struct BigPtr { const char *a, *b0, *b1; int lda, ldb; };

__device__ __forceinline__ BigPtr big_ptrs(const Args& p, const BigJob& j) {
  const u16* B0 = j.type == J_KQ ? p.Wk : p.wg;
  const u16* B1 = j.type == J_KQ ? p.Wq : p.wu;
  asm volatile("" : "+s"(B0), "+s"(B1));
  if (j.type == J_VU) { B0 = p.Wv; B1 = p.Wv + 8 * DH; }
  BigPtr r;
  r.lda = XLD * 2;
  r.ldb = j.type == J_GU ? FF * 2 : HD * 2;
  r.a = p.ws + WS_XN + (size_t)j.panel * 128 * r.lda;
  r.b0 = (const char*)(B0 + j.idx * 128); r.b1 = (const char*)(B1 + j.idx * 128);
  return r;
}
__device__ __forceinline__ void big_publish(const Args& p, const BigJob& j, __amdgpu_buffer_rsrc_t rsy) {
  int tid = threadIdx.x;
  asm volatile("" : "+v"(tid));
  if (j.type == J_KQ) {
    if (tid < 4)
      __builtin_amdgcn_raw_buffer_store_b32(1u, rsy, fkq(j.panel + (tid >> 1), j.idx, tid & 1), 0, 16);
  } else if (j.type == J_VU) {
    if (tid < 4)
      __builtin_amdgcn_raw_buffer_store_b32(1u, rsy, fkq(j.panel + (tid >> 1), j.idx + (tid & 1) * 8, 2), 0, 16);
  } else if (j.type == J_GU) {
    if (tid == 0)
      __hip_atomic_fetch_add((gu32*)(p.ws + WS_SY + SY_CH) + j.panel * 32 + (j.idx >= 14 ? 32 : 0), 1u, __ATOMIC_RELAXED, __HIP_MEMORY_SCOPE_AGENT);
  }
}
typedef f32x4 Acc[2][2][4][2];
__device__ __forceinline__ u32x4 pack32(f32x4 a, f32x4 b) {
  return widen32(cvtpk(a[0], a[1]), cvtpk(a[2], a[3]), cvtpk(b[0], b[1]), cvtpk(b[2], b[3]));
}
#define st_epi(r, o, v) st_sc1(r, o, v)
__device__ __forceinline__ void big_epilogue(const Args& p, const BigJob& j, Acc& acc, char* smem) {
  int tid = threadIdx.x;
  asm volatile("" : "+v"(tid));
  const int lane = tid & 63;
  const int wid = __builtin_amdgcn_readfirstlane(tid >> 6);
  const int wr = wid >> 2, wc = wid & 3;
  if (j.type == J_KQ) {
    const __amdgpu_buffer_rsrc_t ro = mkrsrc(p.ws + WS_T, T_BYTES);
#pragma unroll
    for (int bj = 0; bj < 2; ++bj)
#pragma unroll
      for (int i = 0; i < 8; ++i) {
        const int sg = j.panel * 4 + (i >> 2) * 4 + wr * 2 + ((i >> 1) & 1);
        st_epi(ro, t_off(p, bj * T_Q + kseg(sg, j.idx)) + ((i & 1) * 4 + wc) * 1024 + lane * 16,
               pack32(acc[i >> 2][bj][i & 3][0], acc[i >> 2][bj][i & 3][1]));
      }
  } else if (j.type == J_VU) {
    const __amdgpu_buffer_rsrc_t ro = mkrsrc(p.ws + WS_T, T_BYTES);
    put_vt<8>(ro, lane,
        [&](int i) {
          const int ai = (i >> 1) & 1, bj = i >> 2, m = 2 * (i & 1);
          return turn_vt(acc[ai][bj][m][0], acc[ai][bj][m][1], acc[ai][bj][m + 1][0], acc[ai][bj][m + 1][1], smem + 131072 + wid * VT_IMG, lane);
        },
        [&](int i) { return t_off(p, T_V + kseg(j.panel * 4 + (i & 2) * 2 + wr * 2 + (i & 1), j.idx + 8 * (i >> 2))) + wc * 2048; });
  } else {
    const __amdgpu_buffer_rsrc_t ro = mkrsrc(p.ws + WS_HF, S_LEN * 16384);
#pragma unroll
    for (int i = 0; i < 8; ++i) {
      const int row = wr * 64 + (i & 3) * 16 + (lane & 15);
      f32x4 hh[2];
#pragma unroll
      for (int n = 0; n < 2; ++n)
#pragma unroll
        for (int jj = 0; jj < 4; ++jj) {
          const float g = acc[i >> 2][0][i & 3][n][jj], u = acc[i >> 2][1][i & 3][n][jj];
          hh[n][jj] = __builtin_amdgcn_rcpf(1.0f + __builtin_amdgcn_exp2f(g * -1.4426950408889634f)) * u;
        }
      st_epi(ro, h_off(p, j.panel + (i >> 2)) + row * HLD + (j.idx * 128 + wc * 32 + (lane >> 4) * 8) * 2, pack32(hh[0], hh[1]));
    }
  }
}
#define JST(n, i, v) do {} while (0)
constexpr int JOBW = 223;
__constant__ unsigned short JOBS[223] = {
  5120, 5121, 5122, 5123, 5124, 5125, 5126, 5127, 7168, 7169, 7170, 7171, 7172, 7173, 7174, 7175,
  10240, 10241, 10242, 10243, 12304, 12305, 12306, 12307, 11280, 11281, 11282, 11283, 11284, 11285, 11286, 11287,
  12320, 12321, 12322, 12323, 2096, 2097, 2098, 2099, 11296, 11297, 11298, 11299, 11300, 11301, 11302, 11303,
  1072, 1073, 1074, 1075, 1076, 1077, 1078, 1079, 2128, 2129, 2130, 2131, 1104, 1105, 1106, 1107,
  1108, 1109, 1110, 1111, 3264, 3296, 3265, 3297, 3266, 3298, 3267, 3299, 2160, 2161, 2162, 2163,
  1136, 1137, 1138, 1139, 1140, 1141, 1142, 1143, 3268, 3300, 3269, 3301, 3270, 3302, 3200, 3232,
  2192, 2193, 2194, 2195, 1168, 1169, 1170, 1171, 1172, 1173, 1174, 1175, 3201, 3233, 3202, 3234,
  3203, 3235, 3204, 3236, 3205, 3237, 3206, 3238, 3136, 3168, 2224, 2225, 2226, 2227, 1200, 1201,
  1202, 1203, 1204, 1205, 1206, 1207, 13360, 13361, 13362, 13363, 3137, 3169, 2256, 2257, 2258, 2259,
  1232, 1233, 1234, 1235, 1236, 1237, 1238, 1239, 13344, 13345, 13346, 13347, 3138, 3170, 3139, 3171,
  3140, 3172, 3141, 3173, 3142, 3174, 3072, 3104, 3073, 3105, 3074, 3106, 3075, 3107, 3076, 3108,
  3077, 3109, 3078, 3110, 12528, 12529, 12530, 12531, 11504, 11505, 11506, 11507, 11508, 11509, 11510, 11511,
  14352, 14353, 14354, 14355, 14356, 14357, 14358, 14359, 15360, 15361, 15362, 15363, 15364, 15365, 15366, 15367,
  15368, 15369, 15370, 15371, 15372, 15373, 15374, 15375, 8320, 8336, 8352, 8368, 8384, 8400, 0,
};
__device__ __forceinline__ void gemm_role(const Args& p, int g, char* smem) {
  const __amdgpu_buffer_rsrc_t rsy = mkrsrc(p.ws + WS_SY, SY_TOTAL);
  const int x = g & 3, k = g >> 2;
  gu32* queue = (gu32*)(p.ws + WS_SY + SY_Q) + x * 32;
  volatile unsigned* slot = reinterpret_cast<volatile unsigned*>(smem + 163824);
  unsigned nxt = 0;
  auto request = [&]() {
    if (threadIdx.x == 0) nxt = __hip_atomic_fetch_add(queue, 1u, __ATOMIC_RELAXED, __HIP_MEMORY_SCOPE_AGENT) + 1u + NGW;
  };
  auto post = [&]() { if (threadIdx.x == 0) *slot = nxt; };
  auto decode = [&](int j) {
    const unsigned e = JOBS[min(j, JOBW - 1)];
    const int i = e & 15;
    BigJob b;
    b.type = e >> 10; b.panel = (e >> 4) & 63;
    b.idx = (b.type == J_VU || b.type == J_VUM || b.type == J_QQM) ? x + 4 * (i & 1) + 16 * (i >> 1) : x + 4 * i;
    if (b.type >= J_DNM) { b.idx = i; b.panel = 4 * b.panel + x; }
    return b;
  };
  int nj = 0; (void)nj;
  auto claim = [&]() {
    request();
    LBAR();
    post();
    LBAR();
    return decode((int)__builtin_amdgcn_readfirstlane(*slot));
  };
  BigJob cur = decode(k);
  bool first = true;
  if (g < 4 && threadIdx.x == 0) { LST(480 + 4 * g); LSTC(480 + 4 * g + 1); }
  while (cur.type != J_NONE) {
    JST(nj, 0, cur.type | cur.panel << 8); JST(nj, 1, __builtin_amdgcn_s_memrealtime());
    if (cur.type > J_DN) {
      if (cur.type >= J_QQM && cur.type <= J_VUM) job_kqm(p, cur.type - J_QQM, cur.panel, cur.idx, smem, rsy);
      else if (cur.type == J_DNM) job_dnm(p, cur.panel, cur.idx, smem, rsy);
      else if (cur.type >= J_DNS) job_down(p, cur.panel, cur.type == J_DNS ? cur.idx : cur.idx & 7, cur.type == J_DNS ? 2 : cur.idx >> 3, smem, rsy);
      else if (cur.type == J_CB) job_comb(p, cur.panel, x, rsy);
      else job_kq(p, cur.type - J_KS, cur.panel, cur.idx, smem, rsy);
      JST(nj, 3, __builtin_amdgcn_s_memrealtime()); ++nj;
      cur = claim();
      if (GPF && first && threadIdx.x < 448) {
        const size_t o = (size_t)g * 57344 + threadIdx.x * 128;
        touch_line((const char*)p.wd + o, smem + 163072);
        if (GPF == 3) { touch_line((const char*)p.wg + o, smem + 163072); touch_line((const char*)p.wu + o, smem + 163072); }
      }
      first = false;
      continue;
    }
    int tid = threadIdx.x;
    asm volatile("" : "+v"(tid));
    const int lane = tid & 63;
    const int wid = __builtin_amdgcn_readfirstlane(tid >> 6);
    const int wr = wid >> 2, wc = wid & 3;
    const unsigned ra = tid >> 3, ca = 16 * ((tid & 7) ^ ((tid >> 3) & 7)), rb = tid >> 4, cb = 16 * ((tid & 15) ^ swzB((tid >> 4) & 15));
    const unsigned lw = lds_off(smem) + wid * 1024;
#define B8_SA(b, h) (((b) * 2 + (h)) * 16384)
#define B8_SB(b, h) ((4 + (b) * 2 + (h)) * 16384)
#define B8_STA(b, h, P, kt) glds2((P).a + (size_t)(h) * 128 * (P).lda + (kt) * 128, 64 * (P).lda, __umul24(ra, (P).lda) + ca, lw + B8_SA(b, h))
#define B8_STB(b, h, P, kt) glds2(((h) ? (P).b1 : (P).b0) + (size_t)(kt) * 64 * (P).ldb, 32 * (P).ldb, __umul24(rb, (P).ldb) + cb, lw + B8_SB(b, h))
#define B8_LDA(b, h) do { _Pragma("unroll") for (int m = 0; m < 4; ++m) _Pragma("unroll") for (int kk = 0; kk < 2; ++kk) \
      At[m][kk] = ldsf(smem + B8_SA(b, h) + offA(wr * 64 + m * 16 + (lane & 15), kk * 4 + (lane >> 4))); } while (0)
#define B8_LDB(dst, b, h) do { _Pragma("unroll") for (int n = 0; n < 2; ++n) _Pragma("unroll") for (int kk = 0; kk < 2; ++kk) \
      dst[n][kk] = ld_bfrag(smem + B8_SB(b, h), kk, wc * 2 + n, lane); } while (0)
#define B8_MMA(ai, bj, Bt) do { __builtin_amdgcn_s_setprio(1); _Pragma("unroll") for (int m = 0; m < 4; ++m) _Pragma("unroll") for (int n = 0; n < 2; ++n) \
      _Pragma("unroll") for (int kk = 0; kk < 2; ++kk) acc[ai][bj][m][n] = MFMA(Bt[n][kk], At[m][kk], acc[ai][bj][m][n]); __builtin_amdgcn_s_setprio(0); } while (0)
#define WAIT_L(n) asm volatile("s_waitcnt lgkmcnt(%0)" :: "n"(n) : "memory")
#define BAR() __builtin_amdgcn_s_barrier()
#define WAIT_RX(c) asm volatile("s_waitcnt vmcnt(16)\n\ts_cmp_lg_u32 %0, 0\n\ts_cbranch_scc1 .Lrx%=\n\ts_waitcnt vmcnt(8)\n.Lrx%=:" :: "s"(c) : "memory", "scc")
    Acc acc;
    bf16x8 At[4][2], B0[2][2], B1[2][2];
    BigPtr cp = big_ptrs(p, cur);
    BigJob pend; pend.type = J_NONE; pend.panel = 0; pend.idx = 0;
    LBAR();
    B8_STB(0, 0, cp, 0); B8_STB(0, 1, cp, 0); B8_STA(0, 0, cp, 0); B8_STA(0, 1, cp, 0);
    if (wr == 1) BAR();
    WAIT_V(2); BAR();
    B8_STB(1, 0, cp, 1); B8_STA(1, 0, cp, 1); B8_STB(1, 1, cp, 1);
    WAIT_V(6); BAR();
    BigJob nx = pend;
    for (;;) {
      bool chain = false;
      BigPtr np = cp;
      constexpr int nt = DM / 64;
#pragma unroll
      for (int i = 0; i < 32; ++i) acc[i >> 4][(i >> 3) & 1][(i >> 1) & 3][i & 1] = (f32x4){0.f, 0.f, 0.f, 0.f};
#pragma unroll 1
      for (int t = 0; t < nt; t += 2) {
        const bool last = t == nt - 2;
        if (t == nt - 6) request();
        if (t == nt - 4) post();
        if (last) {
          nx = decode((int)__builtin_amdgcn_readfirstlane(*slot));
          chain = nx.type >= J_KQ && nx.type <= J_GU;
          if (chain) np = big_ptrs(p, nx);
        }
        const BigPtr sp = np;
        const int t2 = last ? 0 : t + 2;
        if (t == 2) big_publish(p, pend, rsy);
        const int rx = __builtin_amdgcn_readfirstlane(t == 0 && pend.type != J_NONE);
        B8_LDB(B0, 0, 0); B8_LDB(B1, 0, 1); SCHED(); B8_LDA(0, 0); B8_STA(1, 1, cp, t + 1);
        WAIT_RX(rx);
        WAIT_L(0); BAR(); B8_MMA(0, 0, B0); B8_MMA(0, 1, B1); BAR(); SCHED();
        B8_LDA(0, 1); B8_STB(0, 0, sp, t2); B8_STB(0, 1, sp, t2); B8_STA(0, 0, sp, t2);
        WAIT_RX(rx);
        WAIT_L(0); BAR(); B8_MMA(1, 0, B0); B8_MMA(1, 1, B1); BAR(); SCHED();
        B8_LDB(B0, 1, 0); B8_LDB(B1, 1, 1); SCHED(); B8_LDA(1, 0); B8_STA(0, 1, sp, t2);
        WAIT_V(8); WAIT_L(0); BAR(); B8_MMA(0, 0, B0); B8_MMA(0, 1, B1); BAR(); SCHED();
        B8_LDA(1, 1); B8_STB(1, 0, sp, t2 + 1); B8_STB(1, 1, sp, t2 + 1); B8_STA(1, 0, sp, t2 + 1);
        WAIT_V(8); WAIT_L(0); BAR(); B8_MMA(1, 0, B0); B8_MMA(1, 1, B1); BAR(); SCHED();
      }
      if (wr == 0) BAR();
      JST(nj, 2, __builtin_amdgcn_s_memrealtime());
      big_epilogue(p, cur, acc, smem);
      if (!chain) break;
      JST(nj, 3, __builtin_amdgcn_s_memrealtime()); ++nj;
      pend = cur; cur = nx; cp = np;
      if (wr == 1) BAR();
      JST(nj, 0, cur.type | cur.panel << 8); JST(nj, 1, __builtin_amdgcn_s_memrealtime());
    }
    WAIT_V(0);
    BAR();
    big_publish(p, cur, rsy);
    JST(nj, 3, __builtin_amdgcn_s_memrealtime()); ++nj;
    cur = nx;
  }
  if (threadIdx.x == 0) { LST(128 + g); }
  if (g < 4 && threadIdx.x == 0) { LST(480 + 4 * g + 2); LSTC(480 + 4 * g + 3); }
}

__device__ __forceinline__ unsigned now_q() { return (unsigned)__builtin_amdgcn_s_memrealtime() * 4u; }
__device__ __forceinline__ void pace_until(unsigned t) {
#pragma unroll 1
  for (int i = 0; i < 256; ++i) {
    const int d = (int)(t - now_q());
    if (d <= 0) break;
    if (d > 32) __builtin_amdgcn_s_sleep(1);
  }
}
#define CLB_E "s76", "s77", "s78", "s79", "s80", "s81", "s82", "s83", "s84", "s85", "s86", "s87", "s88", "s89"
#define EDGE_ASM \
  "s_memtime s[76:77]\n\t" \
  "s_memrealtime s[78:79]\n\t" \
  "s_nop 3\n\t" \
  "s_memrealtime s[80:81]\n\t" \
  "s_nop 3\n\t" \
  "s_memrealtime s[82:83]\n\t" \
  "s_nop 3\n\t" \
  "s_memrealtime s[84:85]\n\t" \
  "s_nop 3\n\t" \
  "s_memrealtime s[86:87]\n\t" \
  "s_nop 3\n\t" \
  "s_memrealtime s[88:89]\n\t" \
  "s_mov_b32 s93, 0\n\t" \
  "s_waitcnt lgkmcnt(0)\n\t" \
  "s_cmp_eq_u32 s80, s78\n\t" \
  "s_addc_u32 s93, s93, 0\n\t" \
  "s_cmp_eq_u32 s82, s78\n\t" \
  "s_addc_u32 s93, s93, 0\n\t" \
  "s_cmp_eq_u32 s84, s78\n\t" \
  "s_addc_u32 s93, s93, 0\n\t" \
  "s_cmp_eq_u32 s86, s78\n\t" \
  "s_addc_u32 s93, s93, 0\n\t" \
  "s_cmp_eq_u32 s88, s78\n\t" \
  "s_addc_u32 s93, s93, 0\n\t" \
  "s_mul_i32 s93, s93, 21\n\t" \
  "s_add_u32 s76, s76, 15\n\t" \
  "s_add_u32 %[kc], s76, s93\n\t" \
  "s_lshl2_add_u32 %[kq], s78, 16\n\t"
__device__ __forceinline__ unsigned time_of(unsigned cyc) {
  unsigned c, q;
  asm volatile(EDGE_ASM : [kc] "=&s"(c), [kq] "=&s"(q) :: CLB_E, "s93", "scc");
  return q - (((c - cyc) * 11900u) >> 16);
}
#define DS_RED 58
#define DS_MEM 38
#define LEAD_DN 2
#define LEAD_UP 12
__device__ __forceinline__ u32x2 ld2_sc1(__amdgpu_buffer_rsrc_t rs, int off, int soff = 0) {
  return __builtin_amdgcn_raw_buffer_load_b64(rs, off, soff, 16);
}
__device__ __forceinline__ void st2_sc1(__amdgpu_buffer_rsrc_t rs, int off, u32x2 v) {
  __builtin_amdgcn_raw_buffer_store_b64(v, rs, off, 0, 16);
}
__device__ __forceinline__ int row_max(int v) {
  v = max(v, __builtin_amdgcn_update_dpp(0, v, 0x128, 0xf, 0xf, false));
  v = max(v, __builtin_amdgcn_update_dpp(0, v, 0x124, 0xf, 0xf, false));
  v = max(v, __builtin_amdgcn_update_dpp(0, v, 0x122, 0xf, 0xf, false));
  v = max(v, __builtin_amdgcn_update_dpp(0, v, 0x121, 0xf, 0xf, false));
  return v;
}
__device__ __forceinline__ int wave_max(int v) {
  v = row_max(v);
  return max(max(__builtin_amdgcn_readlane(v, 0), __builtin_amdgcn_readlane(v, 16)),
             max(__builtin_amdgcn_readlane(v, 32), __builtin_amdgcn_readlane(v, 48)));
}
__device__ __forceinline__ int track(int est, int x, int up, int dn, int out) {
  const int d = x - est;
  return est + (d > out ? dn : max(min(d, up), -dn));
}
typedef __attribute__((address_space(3))) volatile unsigned* lds_vu_ptr;
__device__ __forceinline__ lds_vu_ptr lds_at(const void* p) {
  unsigned a = lds_off(p);
  asm volatile("" : "+v"(a));
  return (lds_vu_ptr)(uintptr_t)a;
}
#define WSL 12
#define PLAN_SLEEP 2
__device__ __forceinline__ unsigned latest(unsigned t) {
  const unsigned t0 = __builtin_amdgcn_readfirstlane(t);
  return t0 + wave_max((int)(t - t0));
}
__device__ __forceinline__ void watch(__amdgpu_buffer_rsrc_t rsy, int s, int lane, unsigned t0, const unsigned* tgt_, gu32* abortw, int hs) {
  pace_until(t0);
  u32x2 ra;
  for (unsigned spins = 0;;) {
    asm volatile("" ::: "memory");
    ra = ld2_sc1(rsy, lane * 128, SY_BR + hs);
    const bool all = __all((int)ra[0] >= s);
    SMP(4, s, spins);
    if (all) break;
    if (spins < 3) __builtin_amdgcn_s_sleep(PLAN_SLEEP); else __builtin_amdgcn_s_sleep(WSL);
    if (spin_fail(spins, abortw)) break;
  }
  const unsigned t = latest(ra[0] == (unsigned)s ? ra[1] : t0) + DS_MEM;
  lds_vu_ptr tgt = lds_at(tgt_);
  if (lane == 0) { tgt[1] = t; tgt[0] = s + 1; }
  SMP(0, s, (int)(t - t0)); SMP(1, s, (int)(t - now_q()));
}
template <bool ALL>
__device__ __forceinline__ unsigned side_mask(const char* ws) {
  typedef __attribute__((address_space(4))) const unsigned* cptr;
  cptr q = (cptr)(uintptr_t)(ws + SY_PAR);
  asm volatile("" : "+s"(q));
  unsigned ga = 0xFFFFu, gb = 0xFFFFu;
#pragma unroll
  for (int i = 0; i < NPAGE; ++i) { ga = min(ga, (i & 1) ? q[i] & 0xFFFFu : q[i] >> 16); gb = min(gb, (i & 1) ? q[i] >> 16 : q[i] & 0xFFFFu); }
  unsigned m = gb < ga ? 0x80001555u : 0x2AAAu;
  if (ALL) {
    const unsigned thr = min(ga, gb) + 110;
#pragma unroll
    for (int i = 0; i < NPAGE; ++i) {
      const unsigned m0 = q[i] >> 16, m1 = q[i] & 0xFFFFu, lo = min(m0, m1), hi = max(m0, m1);
      const unsigned keep = (lo < thr ? hi : 0u) > lo + 100 ? ~(1u << i) : ~0u;
      m = (m & keep) | ((m1 < m0 ? ~0u : 0u) & ~keep);
    }
  }
  unsigned r;
  asm volatile("s_nop 1\n\tv_readfirstlane_b32 %0, %1\n\ts_nop 3" : "=s"(r) : "v"(m));
  return r;
}
__device__ __forceinline__ unsigned ld_flag(__amdgpu_buffer_rsrc_t rs, int off) {
  return __builtin_amdgcn_raw_buffer_load_b32(rs, off, 0, 16);
}

#define QK_ASM_0 \
  "s_waitcnt lgkmcnt(0)\n\t" \
  "ds_read_b128 v[216:219], v250 offset:0\n\t" \
  "ds_read_b128 v[220:223], v250 offset:2048\n\t" \
  "ds_read_b128 v[224:227], v250 offset:1024\n\t" \
  "ds_read_b128 v[228:231], v250 offset:3072\n\t" \
  "v_mov_b64 v[192:193], 0\n\t" \
  "v_mov_b64 v[194:195], 0\n\t" \
  "v_mov_b64 v[196:197], 0\n\t" \
  "v_mov_b64 v[198:199], 0\n\t" \
  "s_memrealtime s[90:91]\n\t" \
  "s_waitcnt lgkmcnt(0)\n\t" \
  "s_lshl_b32 s93, s90, 2\n\t" \
  "s_sub_i32 s93, %[tgt], s93\n\t" \
  "s_cmp_le_i32 s93, 0\n\t" \
  "s_cbranch_scc1 .Lgo%=\n\t" \
  "s_min_i32 s93, s93, 8000\n\t" \
  "s_add_u32 s93, s93, 3\n\t" \
  "s_lshr_b32 s93, s93, 2\n\t" \
  "s_add_u32 s93, s93, s90\n\t" \
  "s_memrealtime s[90:91]\n\t" \
  ".Lpace%=:\n\t" \
  "s_memrealtime s[94:95]\n\t" \
  "s_waitcnt lgkmcnt(1)\n\t" \
  "s_sub_i32 s91, s90, s93\n\t" \
  "s_cmp_ge_i32 s91, 0\n\t" \
  "s_cbranch_scc1 .Lgo%=\n\t" \
  "s_memrealtime s[90:91]\n\t" \
  "s_waitcnt lgkmcnt(1)\n\t" \
  "s_sub_i32 s95, s94, s93\n\t" \
  "s_cmp_lt_i32 s95, 0\n\t" \
  "s_cbranch_scc1 .Lpace%=\n\t" \
  ".Lgo%=:\n\t" \
  "s_mov_b32 s92, 0\n\t" \
  ".Lpoll%=:\n\t" \
  "buffer_load_dwordx4 v[200:203], v248, %[rs], %[som] offen sc1\n\t" \
  "buffer_load_dwordx4 v[204:207], v248, %[rs], %[som] offen offset:1024 sc1\n\t" \
  "s_waitcnt vmcnt(1) lgkmcnt(0)\n\t" \
  "ds_write_b128 v249, v[200:203]\n\t" \
  "v_max3_u32 v208, v200, v201, v202\n\t" \
  "s_waitcnt vmcnt(0)\n\t" \
  "ds_write_b128 v249, v[204:207] offset:1024\n\t" \
  "v_max3_u32 v209, v204, v205, v206\n\t" \
  "v_max3_u32 v208, v208, v203, v207\n\t" \
  "v_max_u32 v208, v208, v209\n\t" \
  "v_cmp_le_u32 vcc, %[sent], v208\n\t" \
  "s_cbranch_vccnz .Lslow%=\n\t" \
  ".Lok%=:\n\t" \
  "v_smfmac_f32_16x16x64_bf16 v[192:195], v[200:203], v[0:7], v251\n\t" \
  "v_smfmac_f32_16x16x64_bf16 v[196:199], v[200:203], v[8:15], v251\n\t" \
  "s_waitcnt lgkmcnt(0)\n\t" \
  "s_barrier\n\t" \
  "ds_read_b128 v[208:211], v248 offset:2048\n\t" \
  "ds_read_b128 v[212:215], v248 offset:3072\n\t" \
  "ds_read_b128 v[236:239], v248 offset:4096\n\t" \
  "ds_read_b128 v[240:243], v248 offset:5120\n\t" \
  "ds_read_b128 v[244:247], v248 offset:6144\n\t" \
  "ds_read_b128 v[200:203], v248 offset:7168\n\t" \
  "v_smfmac_f32_16x16x64_bf16 v[192:195], v[204:207], v[16:23], v251\n\t" \
  "v_smfmac_f32_16x16x64_bf16 v[196:199], v[204:207], v[24:31], v251\n\t" \
  "s_waitcnt lgkmcnt(5)\n\t" \
  "v_smfmac_f32_16x16x64_bf16 v[192:195], v[208:211], v[32:39], v251\n\t" \
  "ds_read_b128 v[204:207], v248 offset:8192\n\t" \
  "v_smfmac_f32_16x16x64_bf16 v[196:199], v[208:211], v[40:47], v251\n\t" \
  "s_waitcnt lgkmcnt(5)\n\t" \
  "v_smfmac_f32_16x16x64_bf16 v[192:195], v[212:215], v[216:223], v251\n\t" \
  "ds_read_b128 v[216:219], v250 offset:4096\n\t" \
  "v_smfmac_f32_16x16x64_bf16 v[196:199], v[212:215], v[224:231], v251\n\t" \
  "s_waitcnt lgkmcnt(5)\n\t" \
  "ds_read_b128 v[220:223], v250 offset:6144\n\t" \
  "v_smfmac_f32_16x16x64_bf16 v[192:195], v[236:239], v[48:55], v251\n\t" \
  "ds_read_b128 v[224:227], v250 offset:5120\n\t" \
  "v_smfmac_f32_16x16x64_bf16 v[196:199], v[236:239], v[56:63], v251\n\t" \
  "s_waitcnt lgkmcnt(6)\n\t" \
  "ds_read_b128 v[228:231], v250 offset:7168\n\t" \
  "v_smfmac_f32_16x16x64_bf16 v[192:195], v[240:243], v[64:71], v251\n\t" \
  "ds_read_b128 v[208:211], v248 offset:9216\n\t" \
  "v_smfmac_f32_16x16x64_bf16 v[196:199], v[240:243], v[72:79], v251\n\t" \
  "s_waitcnt lgkmcnt(5)\n\t" \
  "ds_read_b128 v[212:215], v248 offset:10240\n\t" \
  "v_smfmac_f32_16x16x64_bf16 v[192:195], v[244:247], v[80:87], v251\n\t" \
  "ds_read_b128 v[236:239], v248 offset:11264\n\t" \
  "v_smfmac_f32_16x16x64_bf16 v[196:199], v[244:247], v[88:95], v251\n\t" \
  "s_waitcnt lgkmcnt(5)\n\t" \
  "v_smfmac_f32_16x16x64_bf16 v[192:195], v[200:203], v[216:223], v251\n\t" \
  "s_waitcnt lgkmcnt(3)\n\t" \
  "ds_read_b128 v[216:219], v250 offset:8192\n\t" \
  "v_smfmac_f32_16x16x64_bf16 v[196:199], v[200:203], v[224:231], v251\n\t" \
  "ds_read_b128 v[220:223], v250 offset:10240\n\t" \
  "v_smfmac_f32_16x16x64_bf16 v[192:195], v[204:207], v[96:103], v251\n\t" \
  "ds_read_b128 v[224:227], v250 offset:9216\n\t" \
  "v_smfmac_f32_16x16x64_bf16 v[196:199], v[204:207], v[104:111], v251\n\t" \
  "s_waitcnt lgkmcnt(5)\n\t" \
  "ds_read_b128 v[228:231], v250 offset:11264\n\t" \
  "v_smfmac_f32_16x16x64_bf16 v[192:195], v[208:211], v[112:119], v251\n\t" \
  "ds_read_b128 v[240:243], v248 offset:12288\n\t" \
  "v_smfmac_f32_16x16x64_bf16 v[196:199], v[208:211], v[120:127], v251\n\t" \
  "s_waitcnt lgkmcnt(5)\n\t" \
  "ds_read_b128 v[244:247], v248 offset:13312\n\t" \
  "v_smfmac_f32_16x16x64_bf16 v[192:195], v[212:215], v[128:135], v251\n\t" \
  "ds_read_b128 v[200:203], v248 offset:14336\n\t" \
  "v_smfmac_f32_16x16x64_bf16 v[196:199], v[212:215], v[136:143], v251\n\t" \
  "s_waitcnt lgkmcnt(5)\n\t" \
  "v_smfmac_f32_16x16x64_bf16 v[192:195], v[236:239], v[216:223], v251\n\t" \
  "s_waitcnt lgkmcnt(3)\n\t" \
  "ds_read_b128 v[204:207], v248 offset:15360\n\t" \
  "v_smfmac_f32_16x16x64_bf16 v[196:199], v[236:239], v[224:231], v251\n\t" \
  "s_waitcnt lgkmcnt(3)\n\t" \
  "ds_read_b128 v[216:219], v250 offset:12288\n\t" \
  "v_smfmac_f32_16x16x64_bf16 v[192:195], v[240:243], v[144:151], v251\n\t" \
  "ds_read_b128 v[220:223], v250 offset:14336\n\t" \
  "v_smfmac_f32_16x16x64_bf16 v[196:199], v[240:243], v[152:159], v251\n\t" \
  "s_waitcnt lgkmcnt(4)\n\t" \
  "ds_read_b128 v[224:227], v250 offset:13312\n\t" \
  "v_smfmac_f32_16x16x64_bf16 v[192:195], v[244:247], v[160:167], v251\n\t" \
  "ds_read_b128 v[228:231], v250 offset:15360\n\t" \
  "v_smfmac_f32_16x16x64_bf16 v[196:199], v[244:247], v[168:175], v251\n\t" \
  "s_waitcnt lgkmcnt(5)\n\t" \
  "v_smfmac_f32_16x16x64_bf16 v[192:195], v[200:203], v[176:183], v251\n\t" \
  "v_smfmac_f32_16x16x64_bf16 v[196:199], v[200:203], v[184:191], v251\n\t" \
  "s_waitcnt lgkmcnt(2)\n\t" \
  "v_smfmac_f32_16x16x64_bf16 v[192:195], v[204:207], v[216:223], v251\n\t" \
  "s_waitcnt lgkmcnt(0)\n\t" \
  "v_smfmac_f32_16x16x64_bf16 v[196:199], v[204:207], v[224:231], v251\n\t" \
  "ds_read_b128 v[200:203], v248 offset:16384\n\t" \
  "ds_read_b128 v[204:207], v248 offset:20480\n\t" \
  "v_mov_b32 v250, v232\n\t" \
  "v_mov_b32 v249, v233\n\t" \
  "s_nop 2\n\t" \
  "v_add_f32 v192, v192, v194\n\t" \
  "v_add_f32 v193, v193, v195\n\t" \
  "v_add_f32 v196, v196, v198\n\t" \
  "v_add_f32 v197, v197, v199\n\t" \
  "v_cvt_pk_bf16_f32 v192, v192, v193\n\t" \
  "v_cvt_pk_bf16_f32 v196, v196, v197\n\t" \
  "ds_write2_b32 v252, v192, v196 offset1:64\n\t" \
  "v_mov_b32 v251, v234\n\t" \
  "ds_read_b64_tr_b16 v[232:233], v250 offset:0\n\t" \
  "ds_read_b64_tr_b16 v[234:235], v250 offset:64\n\t" \
  "buffer_load_dwordx4 v[192:195], v248, %[rt], %[svt] offen offset:0 sc1\n\t" \
  "buffer_load_dwordx4 v[196:199], v248, %[rt], %[svt] offen offset:1024 sc1\n\t" \
  "s_waitcnt lgkmcnt(0)\n\t" \
  "v_mfma_f32_16x16x32_bf16 v[200:203], v[200:203], v[232:235], 0\n\t" \
  "v_mfma_f32_16x16x32_bf16 v[204:207], v[204:207], v[232:235], 0\n\t" \
  "s_nop 7\n\t" \
  "v_cndmask_b32 v216, v200, v204, %[nh]\n\t" \
  "v_cndmask_b32 v217, v201, v205, %[nh]\n\t" \
  "v_cndmask_b32 v218, v202, v206, %[nh]\n\t" \
  "v_cndmask_b32 v219, v203, v207, %[nh]\n\t" \
  "ds_write_b128 v248, v[216:219] offset:16384\n\t" \
  "s_sleep 2\n\t" \
  "buffer_load_dword v252, v249, %[rs], %[sv] offen offset:0 sc1\n\t" \
  "buffer_load_dword v253, v249, %[rs], %[sv] offen offset:256 sc1\n\t" \
  "s_setprio 3\n\t" \
  "s_waitcnt lgkmcnt(0)\n\t" \
  "s_barrier\n\t" \
  "ds_read_b64_tr_b16 v[200:201], v250 offset:2048\n\t" \
  "ds_read_b64_tr_b16 v[202:203], v250 offset:2112\n\t" \
  "ds_read_b64_tr_b16 v[236:237], v250 offset:512\n\t" \
  "ds_read_b64_tr_b16 v[238:239], v250 offset:576\n\t" \
  "ds_read_b64_tr_b16 v[204:205], v250 offset:2560\n\t" \
  "ds_read_b64_tr_b16 v[206:207], v250 offset:2624\n\t" \
  "ds_read_b64_tr_b16 v[240:241], v250 offset:1024\n\t" \
  "ds_read_b64_tr_b16 v[242:243], v250 offset:1088\n\t" \
  "ds_read_b64_tr_b16 v[208:209], v250 offset:3072\n\t" \
  "ds_read_b64_tr_b16 v[210:211], v250 offset:3136\n\t" \
  "ds_read_b64_tr_b16 v[244:245], v250 offset:1536\n\t" \
  "ds_read_b64_tr_b16 v[246:247], v250 offset:1600\n\t" \
  "ds_read_b64_tr_b16 v[212:213], v250 offset:3584\n\t" \
  "ds_read_b64_tr_b16 v[214:215], v250 offset:3648\n\t" \
  "ds_read_b128 v[220:223], v248 offset:17408\n\t" \
  "s_waitcnt lgkmcnt(13)\n\t" \
  "v_mfma_f32_16x16x32_bf16 v[200:203], v[200:203], v[232:235], 0\n\t" \
  "ds_read_b128 v[224:227], v248 offset:18432\n\t" \
  "ds_read_b128 v[228:231], v248 offset:19456\n\t" \
  "s_waitcnt lgkmcnt(11)\n\t" \
  "v_mfma_f32_16x16x32_bf16 v[200:203], v[204:207], v[236:239], v[200:203]\n\t" \
  "v_mov_b32 v233, 0\n\t" \
  "v_mov_b32 v234, 0\n\t" \
  "s_waitcnt lgkmcnt(7)\n\t" \
  "v_mfma_f32_16x16x32_bf16 v[200:203], v[208:211], v[240:243], v[200:203]\n\t" \
  "s_waitcnt lgkmcnt(3)\n\t" \
  "v_mfma_f32_16x16x32_bf16 v[200:203], v[212:215], v[244:247], v[200:203]\n\t" \
  "s_waitcnt lgkmcnt(2)\n\t" \
  "v_pk_add_f32 v[216:217], v[216:217], v[220:221]\n\t" \
  "v_pk_add_f32 v[218:219], v[218:219], v[222:223]\n\t" \
  "s_waitcnt lgkmcnt(0)\n\t" \
  "v_pk_add_f32 v[224:225], v[224:225], v[228:229]\n\t" \
  "v_pk_add_f32 v[226:227], v[226:227], v[230:231]\n\t" \
  "v_pk_add_f32 v[216:217], v[216:217], v[224:225]\n\t" \
  "v_pk_add_f32 v[218:219], v[218:219], v[226:227]\n\t" \
  "v_max3_f32 v204, v216, v217, v218\n\t" \
  "v_cndmask_b32 v200, v200, v202, %[up]\n\t" \
  "v_cndmask_b32 v201, v201, v203, %[up]\n\t" \
  "v_max_f32 v204, v204, v219\n\t" \
  "v_max3_f32 v204, v204, v200, v201\n\t" \
  "v_mov_b32 v220, 0x3f803f80\n\t" \
  "v_mov_b32 v221, 0x3f803f80\n\t" \
  "v_max_f32_dpp v205, v204, v204 row_ror:8 row_mask:0xf bank_mask:0xf\n\t" \
  "v_max_f32_dpp v206, v204, v204 row_ror:8 row_mask:0xf bank_mask:0xf\n\t" \
  "v_mov_b64 v[222:223], v[220:221]\n\t" \
  "s_nop 0\n\t" \
  "v_permlane16_swap_b32 v205, v206\n\t" \
  "v_max_f32 v204, v205, v206\n\t" \
  "v_max_f32 v207, v205, v206\n\t" \
  "s_nop 1\n\t" \
  "v_permlane32_swap_b32 v204, v207\n\t" \
  "v_max_f32 v204, v204, v207\n\t" \
  "v_mul_f32 v204, %[negc], v204\n\t" \
  "v_fma_f32 v200, v200, %[c], v204\n\t" \
  "v_fma_f32 v201, v201, %[c], v204\n\t" \
  "v_exp_f32 v200, v200\n\t" \
  "v_fma_f32 v216, v216, %[c], v204\n\t" \
  "v_exp_f32 v201, v201\n\t" \
  "v_fma_f32 v217, v217, %[c], v204\n\t" \
  "v_exp_f32 v216, v216\n\t" \
  "v_fma_f32 v218, v218, %[c], v204\n\t" \
  "v_cvt_pk_bf16_f32 v232, v200, v201\n\t" \
  "v_cvt_pk_bf16_f32 v235, v200, v201\n\t" \
  "v_exp_f32 v217, v217\n\t" \
  "v_fma_f32 v219, v219, %[c], v204\n\t" \
  "v_mfma_f32_16x16x16_bf16 v[212:215], v[220:221], v[232:233], 0\n\t" \
  "v_exp_f32 v218, v218\n\t" \
  "s_waitcnt vmcnt(0)\n\t" \
  "v_mfma_f32_16x16x16_bf16 v[224:227], v[252:253], v[232:233], 0\n\t" \
  "v_cvt_pk_bf16_f32 v210, v216, v217\n\t" \
  "v_cvt_pk_bf16_f32 v208, v216, v217\n\t" \
  "v_exp_f32 v219, v219\n\t" \
  "v_mfma_f32_16x16x16_bf16 v[228:231], v[252:253], v[234:235], 0\n\t" \
  "v_cvt_pk_bf16_f32 v211, v218, v219\n\t" \
  "v_cvt_pk_bf16_f32 v209, v218, v219\n\t" \
  "v_mov_b32_dpp v208, v210 row_ror:8 row_mask:0xf bank_mask:0xc\n\t" \
  "v_mov_b32_dpp v210, v210 row_ror:8 row_mask:0xf bank_mask:0x3\n\t" \
  "v_mov_b32_dpp v209, v211 row_ror:8 row_mask:0xf bank_mask:0xc\n\t" \
  "v_mov_b32_dpp v211, v211 row_ror:8 row_mask:0xf bank_mask:0x3\n\t" \
  "v_max_u32 v204, v252, v253\n\t" \
  "v_cmp_le_u32 vcc, %[sent], v204\n\t" \
  "v_mfma_f32_16x16x32_bf16 v[212:215], v[220:223], v[208:211], v[212:215]\n\t" \
  ".Lpv%=:\n\t" \
  "v_mfma_f32_16x16x32_bf16 v[224:227], v[192:195], v[208:211], v[224:227]\n\t" \
  "v_mfma_f32_16x16x32_bf16 v[228:231], v[196:199], v[208:211], v[228:231]\n\t" \
  "s_cbranch_vccnz .Lvs%=\n\t" \
  ".Lvok%=:\n\t" \
  "s_nop 4\n\t" \
  "v_rcp_f32 v204, v212\n\t" \
  "v_cndmask_b32 v224, v224, v226, %[nh]\n\t" \
  "v_cndmask_b32 v225, v225, v227, %[nh]\n\t" \
  "v_mul_f32 v224, v224, v204\n\t" \
  "v_mul_f32 v225, v225, v204\n\t" \
  "v_cvt_pk_bf16_f32 v224, v224, v225\n\t" \
  "buffer_store_dword v224, v251, %[rs], %[so] offen offset:0 sc1\n\t" \
  "v_cndmask_b32 v228, v228, v230, %[nh]\n\t" \
  "v_cndmask_b32 v229, v229, v231, %[nh]\n\t" \
  "v_mul_f32 v228, v228, v204\n\t" \
  "v_mul_f32 v229, v229, v204\n\t" \
  "v_cvt_pk_bf16_f32 v228, v228, v229\n\t" \
  "buffer_store_dword v228, v251, %[rs], %[so] offen offset:256 sc1\n\t" \
  "s_memtime s[90:91]\n\t" \
  "s_setprio 0\n\t" \
  "s_waitcnt lgkmcnt(0)\n\t" \
  "s_mov_b32 %[tout], s90\n\t" \
  "s_mov_b32 %[spins], s92\n\t" \
  "s_branch .Lend%=\n\t" \
  ".Lvs%=:\n\t" \
  "v_cmp_eq_u32 vcc, %[sent], v252\n\t" \
  "v_cmp_eq_u32 s[94:95], %[sent], v253\n\t" \
  "s_or_b64 s[94:95], s[94:95], vcc\n\t" \
  "s_cbranch_scc0 .Lvok%=\n\t" \
  "s_add_u32 s92, s92, 1\n\t" \
  "s_sleep 2\n\t" \
  "s_and_b32 s93, s92, 63\n\t" \
  "s_cbranch_scc1 .Lvr%=\n\t" \
  "buffer_load_dword v204, off, %[rs], %[sab] sc1\n\t" \
  "s_waitcnt vmcnt(0)\n\t" \
  "v_readfirstlane_b32 s93, v204\n\t" \
  "s_nop 3\n\t" \
  "s_cmp_eq_u32 s93, 1\n\t" \
  "s_cbranch_scc1 .Lvok%=\n\t" \
  "s_cmp_lt_u32 s92, 0x80000\n\t" \
  "s_cbranch_scc1 .Lvr%=\n\t" \
  "v_mov_b32 v204, 1\n\t" \
  "buffer_store_dword v204, off, %[rs], %[sab] sc1\n\t" \
  "s_branch .Lvok%=\n\t" \
  ".Lvr%=:\n\t" \
  "buffer_load_dword v252, v249, %[rs], %[sv] offen offset:0 sc1\n\t" \
  "buffer_load_dword v253, v249, %[rs], %[sv] offen offset:256 sc1\n\t" \
  "s_waitcnt vmcnt(0)\n\t" \
  "v_mfma_f32_16x16x16_bf16 v[224:227], v[252:253], v[232:233], 0\n\t" \
  "v_mfma_f32_16x16x16_bf16 v[228:231], v[252:253], v[234:235], 0\n\t" \
  "v_max_u32 v204, v252, v253\n\t" \
  "v_cmp_le_u32 vcc, %[sent], v204\n\t" \
  "s_nop 4\n\t" \
  "s_branch .Lpv%=\n\t" \
  "s_branch .Lend%=\n\t" \
  ".Lslow%=:\n\t" \
  "s_mov_b64 s[94:95], 0\n\t" \
  "v_cmp_eq_u32 vcc, %[sent], v200\n\t" \
  "s_or_b64 s[94:95], s[94:95], vcc\n\t" \
  "v_cmp_eq_u32 vcc, %[sent], v201\n\t" \
  "s_or_b64 s[94:95], s[94:95], vcc\n\t" \
  "v_cmp_eq_u32 vcc, %[sent], v202\n\t" \
  "s_or_b64 s[94:95], s[94:95], vcc\n\t" \
  "v_cmp_eq_u32 vcc, %[sent], v203\n\t" \
  "s_or_b64 s[94:95], s[94:95], vcc\n\t" \
  "v_cmp_eq_u32 vcc, %[sent], v204\n\t" \
  "s_or_b64 s[94:95], s[94:95], vcc\n\t" \
  "v_cmp_eq_u32 vcc, %[sent], v205\n\t" \
  "s_or_b64 s[94:95], s[94:95], vcc\n\t" \
  "v_cmp_eq_u32 vcc, %[sent], v206\n\t" \
  "s_or_b64 s[94:95], s[94:95], vcc\n\t" \
  "v_cmp_eq_u32 vcc, %[sent], v207\n\t" \
  "s_or_b64 s[94:95], s[94:95], vcc\n\t" \
  "s_cmp_eq_u64 s[94:95], 0\n\t" \
  "s_cbranch_scc1 .Lok%=\n\t" \
  "s_add_u32 s92, s92, 1\n\t" \
  "s_cmp_eq_u32 s92, 1\n\t" \
  "s_cbranch_scc1 .Lpoll%=\n\t" \
  "s_sleep 6\n\t" \
  "s_and_b32 s93, s92, 63\n\t" \
  "s_cmp_lg_u32 s93, 0\n\t" \
  "s_cbranch_scc1 .Lpoll%=\n\t" \
  "v_mov_b32 v209, 0\n\t" \
  "buffer_load_dword v208, v209, %[rs], %[sab] offen sc1\n\t" \
  "s_waitcnt vmcnt(0)\n\t" \
  "v_readfirstlane_b32 s93, v208\n\t" \
  "s_nop 3\n\t" \
  "s_cmp_eq_u32 s93, 1\n\t" \
  "s_cbranch_scc1 .Lok%=\n\t" \
  "s_cmp_lt_u32 s92, 0x80000\n\t" \
  "s_cbranch_scc1 .Lpoll%=\n\t" \
  "v_mov_b32 v208, 1\n\t" \
  "buffer_store_dword v208, v209, %[rs], %[sab] offen sc1\n\t" \
  "s_branch .Lok%=\n\t" \
  ".Lend%=:\n\t"
#define QK_ASM_1 \
  "s_waitcnt lgkmcnt(0)\n\t" \
  "ds_read_b128 v[216:219], v250 offset:0\n\t" \
  "ds_read_b128 v[220:223], v250 offset:2048\n\t" \
  "ds_read_b128 v[224:227], v250 offset:1024\n\t" \
  "ds_read_b128 v[228:231], v250 offset:3072\n\t" \
  "v_mov_b64 v[192:193], 0\n\t" \
  "v_mov_b64 v[194:195], 0\n\t" \
  "v_mov_b64 v[196:197], 0\n\t" \
  "v_mov_b64 v[198:199], 0\n\t" \
  "s_memrealtime s[90:91]\n\t" \
  "s_waitcnt lgkmcnt(0)\n\t" \
  "s_lshl_b32 s93, s90, 2\n\t" \
  "s_sub_i32 s93, %[tgt], s93\n\t" \
  "s_cmp_le_i32 s93, 0\n\t" \
  "s_cbranch_scc1 .Lgo%=\n\t" \
  "s_min_i32 s93, s93, 8000\n\t" \
  "s_add_u32 s93, s93, 3\n\t" \
  "s_lshr_b32 s93, s93, 2\n\t" \
  "s_add_u32 s93, s93, s90\n\t" \
  "s_memrealtime s[90:91]\n\t" \
  ".Lpace%=:\n\t" \
  "s_memrealtime s[94:95]\n\t" \
  "s_waitcnt lgkmcnt(1)\n\t" \
  "s_sub_i32 s91, s90, s93\n\t" \
  "s_cmp_ge_i32 s91, 0\n\t" \
  "s_cbranch_scc1 .Lgo%=\n\t" \
  "s_memrealtime s[90:91]\n\t" \
  "s_waitcnt lgkmcnt(1)\n\t" \
  "s_sub_i32 s95, s94, s93\n\t" \
  "s_cmp_lt_i32 s95, 0\n\t" \
  "s_cbranch_scc1 .Lpace%=\n\t" \
  ".Lgo%=:\n\t" \
  "s_mov_b32 s92, 0\n\t" \
  ".Lpoll%=:\n\t" \
  "buffer_load_dwordx4 v[200:203], v248, %[rs], %[som] offen sc1\n\t" \
  "buffer_load_dwordx4 v[204:207], v248, %[rs], %[som] offen offset:1024 sc1\n\t" \
  "s_waitcnt vmcnt(1) lgkmcnt(0)\n\t" \
  "ds_write_b128 v249, v[200:203]\n\t" \
  "v_max3_u32 v208, v200, v201, v202\n\t" \
  "s_waitcnt vmcnt(0)\n\t" \
  "ds_write_b128 v249, v[204:207] offset:1024\n\t" \
  "v_max3_u32 v209, v204, v205, v206\n\t" \
  "v_max3_u32 v208, v208, v203, v207\n\t" \
  "v_max_u32 v208, v208, v209\n\t" \
  "v_cmp_le_u32 vcc, %[sent], v208\n\t" \
  "s_cbranch_vccnz .Lslow%=\n\t" \
  ".Lok%=:\n\t" \
  "v_smfmac_f32_16x16x64_bf16 v[192:195], v[200:203], v[0:7], v251\n\t" \
  "v_smfmac_f32_16x16x64_bf16 v[196:199], v[200:203], v[8:15], v251\n\t" \
  "s_waitcnt lgkmcnt(0)\n\t" \
  "s_barrier\n\t" \
  "ds_read_b128 v[208:211], v248 offset:4096\n\t" \
  "ds_read_b128 v[212:215], v248 offset:5120\n\t" \
  "ds_read_b128 v[236:239], v248 offset:6144\n\t" \
  "ds_read_b128 v[240:243], v248 offset:7168\n\t" \
  "ds_read_b128 v[244:247], v248 offset:8192\n\t" \
  "ds_read_b128 v[200:203], v248 offset:9216\n\t" \
  "v_smfmac_f32_16x16x64_bf16 v[192:195], v[204:207], v[16:23], v251\n\t" \
  "v_smfmac_f32_16x16x64_bf16 v[196:199], v[204:207], v[24:31], v251\n\t" \
  "s_waitcnt lgkmcnt(5)\n\t" \
  "v_smfmac_f32_16x16x64_bf16 v[192:195], v[208:211], v[32:39], v251\n\t" \
  "ds_read_b128 v[204:207], v248 offset:10240\n\t" \
  "v_smfmac_f32_16x16x64_bf16 v[196:199], v[208:211], v[40:47], v251\n\t" \
  "s_waitcnt lgkmcnt(5)\n\t" \
  "v_smfmac_f32_16x16x64_bf16 v[192:195], v[212:215], v[216:223], v251\n\t" \
  "ds_read_b128 v[216:219], v250 offset:4096\n\t" \
  "v_smfmac_f32_16x16x64_bf16 v[196:199], v[212:215], v[224:231], v251\n\t" \
  "s_waitcnt lgkmcnt(5)\n\t" \
  "ds_read_b128 v[220:223], v250 offset:6144\n\t" \
  "v_smfmac_f32_16x16x64_bf16 v[192:195], v[236:239], v[48:55], v251\n\t" \
  "ds_read_b128 v[224:227], v250 offset:5120\n\t" \
  "v_smfmac_f32_16x16x64_bf16 v[196:199], v[236:239], v[56:63], v251\n\t" \
  "s_waitcnt lgkmcnt(6)\n\t" \
  "ds_read_b128 v[228:231], v250 offset:7168\n\t" \
  "v_smfmac_f32_16x16x64_bf16 v[192:195], v[240:243], v[64:71], v251\n\t" \
  "ds_read_b128 v[208:211], v248 offset:11264\n\t" \
  "v_smfmac_f32_16x16x64_bf16 v[196:199], v[240:243], v[72:79], v251\n\t" \
  "s_waitcnt lgkmcnt(5)\n\t" \
  "ds_read_b128 v[212:215], v248 offset:12288\n\t" \
  "v_smfmac_f32_16x16x64_bf16 v[192:195], v[244:247], v[80:87], v251\n\t" \
  "ds_read_b128 v[236:239], v248 offset:13312\n\t" \
  "v_smfmac_f32_16x16x64_bf16 v[196:199], v[244:247], v[88:95], v251\n\t" \
  "s_waitcnt lgkmcnt(5)\n\t" \
  "v_smfmac_f32_16x16x64_bf16 v[192:195], v[200:203], v[216:223], v251\n\t" \
  "s_waitcnt lgkmcnt(3)\n\t" \
  "ds_read_b128 v[216:219], v250 offset:8192\n\t" \
  "v_smfmac_f32_16x16x64_bf16 v[196:199], v[200:203], v[224:231], v251\n\t" \
  "ds_read_b128 v[220:223], v250 offset:10240\n\t" \
  "v_smfmac_f32_16x16x64_bf16 v[192:195], v[204:207], v[96:103], v251\n\t" \
  "ds_read_b128 v[224:227], v250 offset:9216\n\t" \
  "v_smfmac_f32_16x16x64_bf16 v[196:199], v[204:207], v[104:111], v251\n\t" \
  "s_waitcnt lgkmcnt(5)\n\t" \
  "ds_read_b128 v[228:231], v250 offset:11264\n\t" \
  "v_smfmac_f32_16x16x64_bf16 v[192:195], v[208:211], v[112:119], v251\n\t" \
  "ds_read_b128 v[240:243], v248 offset:14336\n\t" \
  "v_smfmac_f32_16x16x64_bf16 v[196:199], v[208:211], v[120:127], v251\n\t" \
  "s_waitcnt lgkmcnt(5)\n\t" \
  "ds_read_b128 v[244:247], v248 offset:15360\n\t" \
  "v_smfmac_f32_16x16x64_bf16 v[192:195], v[212:215], v[128:135], v251\n\t" \
  "ds_read_b128 v[200:203], v248 offset:0\n\t" \
  "v_smfmac_f32_16x16x64_bf16 v[196:199], v[212:215], v[136:143], v251\n\t" \
  "s_waitcnt lgkmcnt(5)\n\t" \
  "v_smfmac_f32_16x16x64_bf16 v[192:195], v[236:239], v[216:223], v251\n\t" \
  "s_waitcnt lgkmcnt(3)\n\t" \
  "ds_read_b128 v[204:207], v248 offset:1024\n\t" \
  "v_smfmac_f32_16x16x64_bf16 v[196:199], v[236:239], v[224:231], v251\n\t" \
  "s_waitcnt lgkmcnt(3)\n\t" \
  "ds_read_b128 v[216:219], v250 offset:12288\n\t" \
  "v_smfmac_f32_16x16x64_bf16 v[192:195], v[240:243], v[144:151], v251\n\t" \
  "ds_read_b128 v[220:223], v250 offset:14336\n\t" \
  "v_smfmac_f32_16x16x64_bf16 v[196:199], v[240:243], v[152:159], v251\n\t" \
  "s_waitcnt lgkmcnt(4)\n\t" \
  "ds_read_b128 v[224:227], v250 offset:13312\n\t" \
  "v_smfmac_f32_16x16x64_bf16 v[192:195], v[244:247], v[160:167], v251\n\t" \
  "ds_read_b128 v[228:231], v250 offset:15360\n\t" \
  "v_smfmac_f32_16x16x64_bf16 v[196:199], v[244:247], v[168:175], v251\n\t" \
  "s_waitcnt lgkmcnt(5)\n\t" \
  "v_smfmac_f32_16x16x64_bf16 v[192:195], v[200:203], v[176:183], v251\n\t" \
  "v_smfmac_f32_16x16x64_bf16 v[196:199], v[200:203], v[184:191], v251\n\t" \
  "s_waitcnt lgkmcnt(2)\n\t" \
  "v_smfmac_f32_16x16x64_bf16 v[192:195], v[204:207], v[216:223], v251\n\t" \
  "s_waitcnt lgkmcnt(0)\n\t" \
  "v_smfmac_f32_16x16x64_bf16 v[196:199], v[204:207], v[224:231], v251\n\t" \
  "ds_read_b128 v[200:203], v248 offset:17408\n\t" \
  "ds_read_b128 v[204:207], v248 offset:21504\n\t" \
  "v_mov_b32 v250, v232\n\t" \
  "v_mov_b32 v249, v233\n\t" \
  "s_nop 2\n\t" \
  "v_add_f32 v192, v192, v194\n\t" \
  "v_add_f32 v193, v193, v195\n\t" \
  "v_add_f32 v196, v196, v198\n\t" \
  "v_add_f32 v197, v197, v199\n\t" \
  "v_cvt_pk_bf16_f32 v192, v192, v193\n\t" \
  "v_cvt_pk_bf16_f32 v196, v196, v197\n\t" \
  "ds_write2_b32 v252, v192, v196 offset1:64\n\t" \
  "v_mov_b32 v251, v234\n\t" \
  "ds_read_b64_tr_b16 v[236:237], v250 offset:512\n\t" \
  "ds_read_b64_tr_b16 v[238:239], v250 offset:576\n\t" \
  "buffer_load_dwordx4 v[192:195], v248, %[rt], %[svt] offen offset:0 sc1\n\t" \
  "buffer_load_dwordx4 v[196:199], v248, %[rt], %[svt] offen offset:1024 sc1\n\t" \
  "s_waitcnt lgkmcnt(0)\n\t" \
  "v_mfma_f32_16x16x32_bf16 v[200:203], v[200:203], v[236:239], 0\n\t" \
  "v_mfma_f32_16x16x32_bf16 v[204:207], v[204:207], v[236:239], 0\n\t" \
  "s_nop 7\n\t" \
  "v_cndmask_b32 v216, v200, v204, %[nh]\n\t" \
  "v_cndmask_b32 v217, v201, v205, %[nh]\n\t" \
  "v_cndmask_b32 v218, v202, v206, %[nh]\n\t" \
  "v_cndmask_b32 v219, v203, v207, %[nh]\n\t" \
  "ds_write_b128 v248, v[216:219] offset:17408\n\t" \
  "s_sleep 2\n\t" \
  "buffer_load_dword v252, v249, %[rs], %[sv] offen offset:0 sc1\n\t" \
  "buffer_load_dword v253, v249, %[rs], %[sv] offen offset:256 sc1\n\t" \
  "s_setprio 3\n\t" \
  "s_waitcnt lgkmcnt(0)\n\t" \
  "s_barrier\n\t" \
  "ds_read_b64_tr_b16 v[204:205], v250 offset:2560\n\t" \
  "ds_read_b64_tr_b16 v[206:207], v250 offset:2624\n\t" \
  "ds_read_b64_tr_b16 v[240:241], v250 offset:1024\n\t" \
  "ds_read_b64_tr_b16 v[242:243], v250 offset:1088\n\t" \
  "ds_read_b64_tr_b16 v[208:209], v250 offset:3072\n\t" \
  "ds_read_b64_tr_b16 v[210:211], v250 offset:3136\n\t" \
  "ds_read_b64_tr_b16 v[244:245], v250 offset:1536\n\t" \
  "ds_read_b64_tr_b16 v[246:247], v250 offset:1600\n\t" \
  "ds_read_b64_tr_b16 v[212:213], v250 offset:3584\n\t" \
  "ds_read_b64_tr_b16 v[214:215], v250 offset:3648\n\t" \
  "ds_read_b64_tr_b16 v[232:233], v250 offset:0\n\t" \
  "ds_read_b64_tr_b16 v[234:235], v250 offset:64\n\t" \
  "ds_read_b64_tr_b16 v[200:201], v250 offset:2048\n\t" \
  "ds_read_b64_tr_b16 v[202:203], v250 offset:2112\n\t" \
  "ds_read_b128 v[220:223], v248 offset:18432\n\t" \
  "s_waitcnt lgkmcnt(13)\n\t" \
  "v_mfma_f32_16x16x32_bf16 v[204:207], v[204:207], v[236:239], 0\n\t" \
  "ds_read_b128 v[224:227], v248 offset:19456\n\t" \
  "ds_read_b128 v[228:231], v248 offset:16384\n\t" \
  "s_waitcnt lgkmcnt(11)\n\t" \
  "v_mfma_f32_16x16x32_bf16 v[204:207], v[208:211], v[240:243], v[204:207]\n\t" \
  "v_mov_b32 v237, 0\n\t" \
  "v_mov_b32 v238, 0\n\t" \
  "s_waitcnt lgkmcnt(7)\n\t" \
  "v_mfma_f32_16x16x32_bf16 v[204:207], v[212:215], v[244:247], v[204:207]\n\t" \
  "s_waitcnt lgkmcnt(3)\n\t" \
  "v_mfma_f32_16x16x32_bf16 v[204:207], v[200:203], v[232:235], v[204:207]\n\t" \
  "s_waitcnt lgkmcnt(2)\n\t" \
  "v_pk_add_f32 v[216:217], v[216:217], v[220:221]\n\t" \
  "v_pk_add_f32 v[218:219], v[218:219], v[222:223]\n\t" \
  "s_waitcnt lgkmcnt(0)\n\t" \
  "v_pk_add_f32 v[224:225], v[224:225], v[228:229]\n\t" \
  "v_pk_add_f32 v[226:227], v[226:227], v[230:231]\n\t" \
  "v_pk_add_f32 v[216:217], v[216:217], v[224:225]\n\t" \
  "v_pk_add_f32 v[218:219], v[218:219], v[226:227]\n\t" \
  "v_max3_f32 v208, v216, v217, v218\n\t" \
  "v_cndmask_b32 v204, v204, v206, %[up]\n\t" \
  "v_cndmask_b32 v205, v205, v207, %[up]\n\t" \
  "v_max_f32 v208, v208, v219\n\t" \
  "v_max3_f32 v208, v208, v204, v205\n\t" \
  "v_mov_b32 v220, 0x3f803f80\n\t" \
  "v_mov_b32 v221, 0x3f803f80\n\t" \
  "v_max_f32_dpp v209, v208, v208 row_ror:8 row_mask:0xf bank_mask:0xf\n\t" \
  "v_max_f32_dpp v210, v208, v208 row_ror:8 row_mask:0xf bank_mask:0xf\n\t" \
  "v_mov_b64 v[222:223], v[220:221]\n\t" \
  "s_nop 0\n\t" \
  "v_permlane16_swap_b32 v209, v210\n\t" \
  "v_max_f32 v208, v209, v210\n\t" \
  "v_max_f32 v211, v209, v210\n\t" \
  "s_nop 1\n\t" \
  "v_permlane32_swap_b32 v208, v211\n\t" \
  "v_max_f32 v208, v208, v211\n\t" \
  "v_mul_f32 v208, %[negc], v208\n\t" \
  "v_fma_f32 v204, v204, %[c], v208\n\t" \
  "v_fma_f32 v205, v205, %[c], v208\n\t" \
  "v_exp_f32 v204, v204\n\t" \
  "v_fma_f32 v216, v216, %[c], v208\n\t" \
  "v_exp_f32 v205, v205\n\t" \
  "v_fma_f32 v217, v217, %[c], v208\n\t" \
  "v_exp_f32 v216, v216\n\t" \
  "v_fma_f32 v218, v218, %[c], v208\n\t" \
  "v_cvt_pk_bf16_f32 v236, v204, v205\n\t" \
  "v_cvt_pk_bf16_f32 v239, v204, v205\n\t" \
  "v_exp_f32 v217, v217\n\t" \
  "v_fma_f32 v219, v219, %[c], v208\n\t" \
  "v_mfma_f32_16x16x16_bf16 v[200:203], v[220:221], v[236:237], 0\n\t" \
  "v_exp_f32 v218, v218\n\t" \
  "s_waitcnt vmcnt(0)\n\t" \
  "v_mfma_f32_16x16x16_bf16 v[224:227], v[252:253], v[236:237], 0\n\t" \
  "v_cvt_pk_bf16_f32 v214, v216, v217\n\t" \
  "v_cvt_pk_bf16_f32 v212, v216, v217\n\t" \
  "v_exp_f32 v219, v219\n\t" \
  "v_mfma_f32_16x16x16_bf16 v[228:231], v[252:253], v[238:239], 0\n\t" \
  "v_cvt_pk_bf16_f32 v215, v218, v219\n\t" \
  "v_cvt_pk_bf16_f32 v213, v218, v219\n\t" \
  "v_mov_b32_dpp v212, v214 row_ror:8 row_mask:0xf bank_mask:0xc\n\t" \
  "v_mov_b32_dpp v214, v214 row_ror:8 row_mask:0xf bank_mask:0x3\n\t" \
  "v_mov_b32_dpp v213, v215 row_ror:8 row_mask:0xf bank_mask:0xc\n\t" \
  "v_mov_b32_dpp v215, v215 row_ror:8 row_mask:0xf bank_mask:0x3\n\t" \
  "v_max_u32 v208, v252, v253\n\t" \
  "v_cmp_le_u32 vcc, %[sent], v208\n\t" \
  "v_mfma_f32_16x16x32_bf16 v[200:203], v[220:223], v[212:215], v[200:203]\n\t" \
  ".Lpv%=:\n\t" \
  "v_mfma_f32_16x16x32_bf16 v[224:227], v[192:195], v[212:215], v[224:227]\n\t" \
  "v_mfma_f32_16x16x32_bf16 v[228:231], v[196:199], v[212:215], v[228:231]\n\t" \
  "s_cbranch_vccnz .Lvs%=\n\t" \
  ".Lvok%=:\n\t" \
  "s_nop 4\n\t" \
  "v_rcp_f32 v208, v200\n\t" \
  "v_cndmask_b32 v224, v224, v226, %[nh]\n\t" \
  "v_cndmask_b32 v225, v225, v227, %[nh]\n\t" \
  "v_mul_f32 v224, v224, v208\n\t" \
  "v_mul_f32 v225, v225, v208\n\t" \
  "v_cvt_pk_bf16_f32 v224, v224, v225\n\t" \
  "buffer_store_dword v224, v251, %[rs], %[so] offen offset:0 sc1\n\t" \
  "v_cndmask_b32 v228, v228, v230, %[nh]\n\t" \
  "v_cndmask_b32 v229, v229, v231, %[nh]\n\t" \
  "v_mul_f32 v228, v228, v208\n\t" \
  "v_mul_f32 v229, v229, v208\n\t" \
  "v_cvt_pk_bf16_f32 v228, v228, v229\n\t" \
  "buffer_store_dword v228, v251, %[rs], %[so] offen offset:256 sc1\n\t" \
  "s_memtime s[90:91]\n\t" \
  "s_setprio 0\n\t" \
  "s_waitcnt lgkmcnt(0)\n\t" \
  "s_mov_b32 %[tout], s90\n\t" \
  "s_mov_b32 %[spins], s92\n\t" \
  "s_branch .Lend%=\n\t" \
  ".Lvs%=:\n\t" \
  "v_cmp_eq_u32 vcc, %[sent], v252\n\t" \
  "v_cmp_eq_u32 s[94:95], %[sent], v253\n\t" \
  "s_or_b64 s[94:95], s[94:95], vcc\n\t" \
  "s_cbranch_scc0 .Lvok%=\n\t" \
  "s_add_u32 s92, s92, 1\n\t" \
  "s_sleep 2\n\t" \
  "s_and_b32 s93, s92, 63\n\t" \
  "s_cbranch_scc1 .Lvr%=\n\t" \
  "buffer_load_dword v208, off, %[rs], %[sab] sc1\n\t" \
  "s_waitcnt vmcnt(0)\n\t" \
  "v_readfirstlane_b32 s93, v208\n\t" \
  "s_nop 3\n\t" \
  "s_cmp_eq_u32 s93, 1\n\t" \
  "s_cbranch_scc1 .Lvok%=\n\t" \
  "s_cmp_lt_u32 s92, 0x80000\n\t" \
  "s_cbranch_scc1 .Lvr%=\n\t" \
  "v_mov_b32 v208, 1\n\t" \
  "buffer_store_dword v208, off, %[rs], %[sab] sc1\n\t" \
  "s_branch .Lvok%=\n\t" \
  ".Lvr%=:\n\t" \
  "buffer_load_dword v252, v249, %[rs], %[sv] offen offset:0 sc1\n\t" \
  "buffer_load_dword v253, v249, %[rs], %[sv] offen offset:256 sc1\n\t" \
  "s_waitcnt vmcnt(0)\n\t" \
  "v_mfma_f32_16x16x16_bf16 v[224:227], v[252:253], v[236:237], 0\n\t" \
  "v_mfma_f32_16x16x16_bf16 v[228:231], v[252:253], v[238:239], 0\n\t" \
  "v_max_u32 v208, v252, v253\n\t" \
  "v_cmp_le_u32 vcc, %[sent], v208\n\t" \
  "s_nop 4\n\t" \
  "s_branch .Lpv%=\n\t" \
  "s_branch .Lend%=\n\t" \
  ".Lslow%=:\n\t" \
  "s_mov_b64 s[94:95], 0\n\t" \
  "v_cmp_eq_u32 vcc, %[sent], v200\n\t" \
  "s_or_b64 s[94:95], s[94:95], vcc\n\t" \
  "v_cmp_eq_u32 vcc, %[sent], v201\n\t" \
  "s_or_b64 s[94:95], s[94:95], vcc\n\t" \
  "v_cmp_eq_u32 vcc, %[sent], v202\n\t" \
  "s_or_b64 s[94:95], s[94:95], vcc\n\t" \
  "v_cmp_eq_u32 vcc, %[sent], v203\n\t" \
  "s_or_b64 s[94:95], s[94:95], vcc\n\t" \
  "v_cmp_eq_u32 vcc, %[sent], v204\n\t" \
  "s_or_b64 s[94:95], s[94:95], vcc\n\t" \
  "v_cmp_eq_u32 vcc, %[sent], v205\n\t" \
  "s_or_b64 s[94:95], s[94:95], vcc\n\t" \
  "v_cmp_eq_u32 vcc, %[sent], v206\n\t" \
  "s_or_b64 s[94:95], s[94:95], vcc\n\t" \
  "v_cmp_eq_u32 vcc, %[sent], v207\n\t" \
  "s_or_b64 s[94:95], s[94:95], vcc\n\t" \
  "s_cmp_eq_u64 s[94:95], 0\n\t" \
  "s_cbranch_scc1 .Lok%=\n\t" \
  "s_add_u32 s92, s92, 1\n\t" \
  "s_cmp_eq_u32 s92, 1\n\t" \
  "s_cbranch_scc1 .Lpoll%=\n\t" \
  "s_sleep 6\n\t" \
  "s_and_b32 s93, s92, 63\n\t" \
  "s_cmp_lg_u32 s93, 0\n\t" \
  "s_cbranch_scc1 .Lpoll%=\n\t" \
  "v_mov_b32 v209, 0\n\t" \
  "buffer_load_dword v208, v209, %[rs], %[sab] offen sc1\n\t" \
  "s_waitcnt vmcnt(0)\n\t" \
  "v_readfirstlane_b32 s93, v208\n\t" \
  "s_nop 3\n\t" \
  "s_cmp_eq_u32 s93, 1\n\t" \
  "s_cbranch_scc1 .Lok%=\n\t" \
  "s_cmp_lt_u32 s92, 0x80000\n\t" \
  "s_cbranch_scc1 .Lpoll%=\n\t" \
  "v_mov_b32 v208, 1\n\t" \
  "buffer_store_dword v208, v209, %[rs], %[sab] offen sc1\n\t" \
  "s_branch .Lok%=\n\t" \
  ".Lend%=:\n\t"
#define QK_ASM_2 \
  "s_waitcnt lgkmcnt(0)\n\t" \
  "ds_read_b128 v[216:219], v250 offset:0\n\t" \
  "ds_read_b128 v[220:223], v250 offset:2048\n\t" \
  "ds_read_b128 v[224:227], v250 offset:1024\n\t" \
  "ds_read_b128 v[228:231], v250 offset:3072\n\t" \
  "v_mov_b64 v[192:193], 0\n\t" \
  "v_mov_b64 v[194:195], 0\n\t" \
  "v_mov_b64 v[196:197], 0\n\t" \
  "v_mov_b64 v[198:199], 0\n\t" \
  "s_memrealtime s[90:91]\n\t" \
  "s_waitcnt lgkmcnt(0)\n\t" \
  "s_lshl_b32 s93, s90, 2\n\t" \
  "s_sub_i32 s93, %[tgt], s93\n\t" \
  "s_cmp_le_i32 s93, 0\n\t" \
  "s_cbranch_scc1 .Lgo%=\n\t" \
  "s_min_i32 s93, s93, 8000\n\t" \
  "s_add_u32 s93, s93, 3\n\t" \
  "s_lshr_b32 s93, s93, 2\n\t" \
  "s_add_u32 s93, s93, s90\n\t" \
  "s_memrealtime s[90:91]\n\t" \
  ".Lpace%=:\n\t" \
  "s_memrealtime s[94:95]\n\t" \
  "s_waitcnt lgkmcnt(1)\n\t" \
  "s_sub_i32 s91, s90, s93\n\t" \
  "s_cmp_ge_i32 s91, 0\n\t" \
  "s_cbranch_scc1 .Lgo%=\n\t" \
  "s_memrealtime s[90:91]\n\t" \
  "s_waitcnt lgkmcnt(1)\n\t" \
  "s_sub_i32 s95, s94, s93\n\t" \
  "s_cmp_lt_i32 s95, 0\n\t" \
  "s_cbranch_scc1 .Lpace%=\n\t" \
  ".Lgo%=:\n\t" \
  "s_mov_b32 s92, 0\n\t" \
  ".Lpoll%=:\n\t" \
  "buffer_load_dwordx4 v[200:203], v248, %[rs], %[som] offen sc1\n\t" \
  "buffer_load_dwordx4 v[204:207], v248, %[rs], %[som] offen offset:1024 sc1\n\t" \
  "s_waitcnt vmcnt(1) lgkmcnt(0)\n\t" \
  "ds_write_b128 v249, v[200:203]\n\t" \
  "v_max3_u32 v208, v200, v201, v202\n\t" \
  "s_waitcnt vmcnt(0)\n\t" \
  "ds_write_b128 v249, v[204:207] offset:1024\n\t" \
  "v_max3_u32 v209, v204, v205, v206\n\t" \
  "v_max3_u32 v208, v208, v203, v207\n\t" \
  "v_max_u32 v208, v208, v209\n\t" \
  "v_cmp_le_u32 vcc, %[sent], v208\n\t" \
  "s_cbranch_vccnz .Lslow%=\n\t" \
  ".Lok%=:\n\t" \
  "v_smfmac_f32_16x16x64_bf16 v[192:195], v[200:203], v[0:7], v251\n\t" \
  "v_smfmac_f32_16x16x64_bf16 v[196:199], v[200:203], v[8:15], v251\n\t" \
  "s_waitcnt lgkmcnt(0)\n\t" \
  "s_barrier\n\t" \
  "ds_read_b128 v[208:211], v248 offset:6144\n\t" \
  "ds_read_b128 v[212:215], v248 offset:7168\n\t" \
  "ds_read_b128 v[236:239], v248 offset:8192\n\t" \
  "ds_read_b128 v[240:243], v248 offset:9216\n\t" \
  "ds_read_b128 v[244:247], v248 offset:10240\n\t" \
  "ds_read_b128 v[200:203], v248 offset:11264\n\t" \
  "v_smfmac_f32_16x16x64_bf16 v[192:195], v[204:207], v[16:23], v251\n\t" \
  "v_smfmac_f32_16x16x64_bf16 v[196:199], v[204:207], v[24:31], v251\n\t" \
  "s_waitcnt lgkmcnt(5)\n\t" \
  "v_smfmac_f32_16x16x64_bf16 v[192:195], v[208:211], v[32:39], v251\n\t" \
  "ds_read_b128 v[204:207], v248 offset:12288\n\t" \
  "v_smfmac_f32_16x16x64_bf16 v[196:199], v[208:211], v[40:47], v251\n\t" \
  "s_waitcnt lgkmcnt(5)\n\t" \
  "v_smfmac_f32_16x16x64_bf16 v[192:195], v[212:215], v[216:223], v251\n\t" \
  "ds_read_b128 v[216:219], v250 offset:4096\n\t" \
  "v_smfmac_f32_16x16x64_bf16 v[196:199], v[212:215], v[224:231], v251\n\t" \
  "s_waitcnt lgkmcnt(5)\n\t" \
  "ds_read_b128 v[220:223], v250 offset:6144\n\t" \
  "v_smfmac_f32_16x16x64_bf16 v[192:195], v[236:239], v[48:55], v251\n\t" \
  "ds_read_b128 v[224:227], v250 offset:5120\n\t" \
  "v_smfmac_f32_16x16x64_bf16 v[196:199], v[236:239], v[56:63], v251\n\t" \
  "s_waitcnt lgkmcnt(6)\n\t" \
  "ds_read_b128 v[228:231], v250 offset:7168\n\t" \
  "v_smfmac_f32_16x16x64_bf16 v[192:195], v[240:243], v[64:71], v251\n\t" \
  "ds_read_b128 v[208:211], v248 offset:13312\n\t" \
  "v_smfmac_f32_16x16x64_bf16 v[196:199], v[240:243], v[72:79], v251\n\t" \
  "s_waitcnt lgkmcnt(5)\n\t" \
  "ds_read_b128 v[212:215], v248 offset:14336\n\t" \
  "v_smfmac_f32_16x16x64_bf16 v[192:195], v[244:247], v[80:87], v251\n\t" \
  "ds_read_b128 v[236:239], v248 offset:15360\n\t" \
  "v_smfmac_f32_16x16x64_bf16 v[196:199], v[244:247], v[88:95], v251\n\t" \
  "s_waitcnt lgkmcnt(5)\n\t" \
  "v_smfmac_f32_16x16x64_bf16 v[192:195], v[200:203], v[216:223], v251\n\t" \
  "s_waitcnt lgkmcnt(3)\n\t" \
  "ds_read_b128 v[216:219], v250 offset:8192\n\t" \
  "v_smfmac_f32_16x16x64_bf16 v[196:199], v[200:203], v[224:231], v251\n\t" \
  "ds_read_b128 v[220:223], v250 offset:10240\n\t" \
  "v_smfmac_f32_16x16x64_bf16 v[192:195], v[204:207], v[96:103], v251\n\t" \
  "ds_read_b128 v[224:227], v250 offset:9216\n\t" \
  "v_smfmac_f32_16x16x64_bf16 v[196:199], v[204:207], v[104:111], v251\n\t" \
  "s_waitcnt lgkmcnt(5)\n\t" \
  "ds_read_b128 v[228:231], v250 offset:11264\n\t" \
  "v_smfmac_f32_16x16x64_bf16 v[192:195], v[208:211], v[112:119], v251\n\t" \
  "ds_read_b128 v[240:243], v248 offset:0\n\t" \
  "v_smfmac_f32_16x16x64_bf16 v[196:199], v[208:211], v[120:127], v251\n\t" \
  "s_waitcnt lgkmcnt(5)\n\t" \
  "ds_read_b128 v[244:247], v248 offset:1024\n\t" \
  "v_smfmac_f32_16x16x64_bf16 v[192:195], v[212:215], v[128:135], v251\n\t" \
  "ds_read_b128 v[200:203], v248 offset:2048\n\t" \
  "v_smfmac_f32_16x16x64_bf16 v[196:199], v[212:215], v[136:143], v251\n\t" \
  "s_waitcnt lgkmcnt(5)\n\t" \
  "v_smfmac_f32_16x16x64_bf16 v[192:195], v[236:239], v[216:223], v251\n\t" \
  "s_waitcnt lgkmcnt(3)\n\t" \
  "ds_read_b128 v[204:207], v248 offset:3072\n\t" \
  "v_smfmac_f32_16x16x64_bf16 v[196:199], v[236:239], v[224:231], v251\n\t" \
  "s_waitcnt lgkmcnt(3)\n\t" \
  "ds_read_b128 v[216:219], v250 offset:12288\n\t" \
  "v_smfmac_f32_16x16x64_bf16 v[192:195], v[240:243], v[144:151], v251\n\t" \
  "ds_read_b128 v[220:223], v250 offset:14336\n\t" \
  "v_smfmac_f32_16x16x64_bf16 v[196:199], v[240:243], v[152:159], v251\n\t" \
  "s_waitcnt lgkmcnt(4)\n\t" \
  "ds_read_b128 v[224:227], v250 offset:13312\n\t" \
  "v_smfmac_f32_16x16x64_bf16 v[192:195], v[244:247], v[160:167], v251\n\t" \
  "ds_read_b128 v[228:231], v250 offset:15360\n\t" \
  "v_smfmac_f32_16x16x64_bf16 v[196:199], v[244:247], v[168:175], v251\n\t" \
  "s_waitcnt lgkmcnt(5)\n\t" \
  "v_smfmac_f32_16x16x64_bf16 v[192:195], v[200:203], v[176:183], v251\n\t" \
  "v_smfmac_f32_16x16x64_bf16 v[196:199], v[200:203], v[184:191], v251\n\t" \
  "s_waitcnt lgkmcnt(2)\n\t" \
  "v_smfmac_f32_16x16x64_bf16 v[192:195], v[204:207], v[216:223], v251\n\t" \
  "s_waitcnt lgkmcnt(0)\n\t" \
  "v_smfmac_f32_16x16x64_bf16 v[196:199], v[204:207], v[224:231], v251\n\t" \
  "ds_read_b128 v[200:203], v248 offset:18432\n\t" \
  "ds_read_b128 v[204:207], v248 offset:22528\n\t" \
  "v_mov_b32 v250, v232\n\t" \
  "v_mov_b32 v249, v233\n\t" \
  "s_nop 2\n\t" \
  "v_add_f32 v192, v192, v194\n\t" \
  "v_add_f32 v193, v193, v195\n\t" \
  "v_add_f32 v196, v196, v198\n\t" \
  "v_add_f32 v197, v197, v199\n\t" \
  "v_cvt_pk_bf16_f32 v192, v192, v193\n\t" \
  "v_cvt_pk_bf16_f32 v196, v196, v197\n\t" \
  "ds_write2_b32 v252, v192, v196 offset1:64\n\t" \
  "v_mov_b32 v251, v234\n\t" \
  "ds_read_b64_tr_b16 v[240:241], v250 offset:1024\n\t" \
  "ds_read_b64_tr_b16 v[242:243], v250 offset:1088\n\t" \
  "buffer_load_dwordx4 v[192:195], v248, %[rt], %[svt] offen offset:0 sc1\n\t" \
  "buffer_load_dwordx4 v[196:199], v248, %[rt], %[svt] offen offset:1024 sc1\n\t" \
  "s_waitcnt lgkmcnt(0)\n\t" \
  "v_mfma_f32_16x16x32_bf16 v[200:203], v[200:203], v[240:243], 0\n\t" \
  "v_mfma_f32_16x16x32_bf16 v[204:207], v[204:207], v[240:243], 0\n\t" \
  "s_nop 7\n\t" \
  "v_cndmask_b32 v216, v200, v204, %[nh]\n\t" \
  "v_cndmask_b32 v217, v201, v205, %[nh]\n\t" \
  "v_cndmask_b32 v218, v202, v206, %[nh]\n\t" \
  "v_cndmask_b32 v219, v203, v207, %[nh]\n\t" \
  "ds_write_b128 v248, v[216:219] offset:18432\n\t" \
  "s_sleep 2\n\t" \
  "buffer_load_dword v252, v249, %[rs], %[sv] offen offset:0 sc1\n\t" \
  "buffer_load_dword v253, v249, %[rs], %[sv] offen offset:256 sc1\n\t" \
  "s_setprio 3\n\t" \
  "s_waitcnt lgkmcnt(0)\n\t" \
  "s_barrier\n\t" \
  "ds_read_b64_tr_b16 v[208:209], v250 offset:3072\n\t" \
  "ds_read_b64_tr_b16 v[210:211], v250 offset:3136\n\t" \
  "ds_read_b64_tr_b16 v[244:245], v250 offset:1536\n\t" \
  "ds_read_b64_tr_b16 v[246:247], v250 offset:1600\n\t" \
  "ds_read_b64_tr_b16 v[212:213], v250 offset:3584\n\t" \
  "ds_read_b64_tr_b16 v[214:215], v250 offset:3648\n\t" \
  "ds_read_b64_tr_b16 v[232:233], v250 offset:0\n\t" \
  "ds_read_b64_tr_b16 v[234:235], v250 offset:64\n\t" \
  "ds_read_b64_tr_b16 v[200:201], v250 offset:2048\n\t" \
  "ds_read_b64_tr_b16 v[202:203], v250 offset:2112\n\t" \
  "ds_read_b64_tr_b16 v[236:237], v250 offset:512\n\t" \
  "ds_read_b64_tr_b16 v[238:239], v250 offset:576\n\t" \
  "ds_read_b64_tr_b16 v[204:205], v250 offset:2560\n\t" \
  "ds_read_b64_tr_b16 v[206:207], v250 offset:2624\n\t" \
  "ds_read_b128 v[220:223], v248 offset:19456\n\t" \
  "s_waitcnt lgkmcnt(13)\n\t" \
  "v_mfma_f32_16x16x32_bf16 v[208:211], v[208:211], v[240:243], 0\n\t" \
  "ds_read_b128 v[224:227], v248 offset:16384\n\t" \
  "ds_read_b128 v[228:231], v248 offset:17408\n\t" \
  "s_waitcnt lgkmcnt(11)\n\t" \
  "v_mfma_f32_16x16x32_bf16 v[208:211], v[212:215], v[244:247], v[208:211]\n\t" \
  "v_mov_b32 v241, 0\n\t" \
  "v_mov_b32 v242, 0\n\t" \
  "s_waitcnt lgkmcnt(7)\n\t" \
  "v_mfma_f32_16x16x32_bf16 v[208:211], v[200:203], v[232:235], v[208:211]\n\t" \
  "s_waitcnt lgkmcnt(3)\n\t" \
  "v_mfma_f32_16x16x32_bf16 v[208:211], v[204:207], v[236:239], v[208:211]\n\t" \
  "s_waitcnt lgkmcnt(2)\n\t" \
  "v_pk_add_f32 v[216:217], v[216:217], v[220:221]\n\t" \
  "v_pk_add_f32 v[218:219], v[218:219], v[222:223]\n\t" \
  "s_waitcnt lgkmcnt(0)\n\t" \
  "v_pk_add_f32 v[224:225], v[224:225], v[228:229]\n\t" \
  "v_pk_add_f32 v[226:227], v[226:227], v[230:231]\n\t" \
  "v_pk_add_f32 v[216:217], v[216:217], v[224:225]\n\t" \
  "v_pk_add_f32 v[218:219], v[218:219], v[226:227]\n\t" \
  "v_max3_f32 v212, v216, v217, v218\n\t" \
  "v_cndmask_b32 v208, v208, v210, %[up]\n\t" \
  "v_cndmask_b32 v209, v209, v211, %[up]\n\t" \
  "v_max_f32 v212, v212, v219\n\t" \
  "v_max3_f32 v212, v212, v208, v209\n\t" \
  "v_mov_b32 v220, 0x3f803f80\n\t" \
  "v_mov_b32 v221, 0x3f803f80\n\t" \
  "v_max_f32_dpp v213, v212, v212 row_ror:8 row_mask:0xf bank_mask:0xf\n\t" \
  "v_max_f32_dpp v214, v212, v212 row_ror:8 row_mask:0xf bank_mask:0xf\n\t" \
  "v_mov_b64 v[222:223], v[220:221]\n\t" \
  "s_nop 0\n\t" \
  "v_permlane16_swap_b32 v213, v214\n\t" \
  "v_max_f32 v212, v213, v214\n\t" \
  "v_max_f32 v215, v213, v214\n\t" \
  "s_nop 1\n\t" \
  "v_permlane32_swap_b32 v212, v215\n\t" \
  "v_max_f32 v212, v212, v215\n\t" \
  "v_mul_f32 v212, %[negc], v212\n\t" \
  "v_fma_f32 v208, v208, %[c], v212\n\t" \
  "v_fma_f32 v209, v209, %[c], v212\n\t" \
  "v_exp_f32 v208, v208\n\t" \
  "v_fma_f32 v216, v216, %[c], v212\n\t" \
  "v_exp_f32 v209, v209\n\t" \
  "v_fma_f32 v217, v217, %[c], v212\n\t" \
  "v_exp_f32 v216, v216\n\t" \
  "v_fma_f32 v218, v218, %[c], v212\n\t" \
  "v_cvt_pk_bf16_f32 v240, v208, v209\n\t" \
  "v_cvt_pk_bf16_f32 v243, v208, v209\n\t" \
  "v_exp_f32 v217, v217\n\t" \
  "v_fma_f32 v219, v219, %[c], v212\n\t" \
  "v_mfma_f32_16x16x16_bf16 v[204:207], v[220:221], v[240:241], 0\n\t" \
  "v_exp_f32 v218, v218\n\t" \
  "s_waitcnt vmcnt(0)\n\t" \
  "v_mfma_f32_16x16x16_bf16 v[224:227], v[252:253], v[240:241], 0\n\t" \
  "v_cvt_pk_bf16_f32 v202, v216, v217\n\t" \
  "v_cvt_pk_bf16_f32 v200, v216, v217\n\t" \
  "v_exp_f32 v219, v219\n\t" \
  "v_mfma_f32_16x16x16_bf16 v[228:231], v[252:253], v[242:243], 0\n\t" \
  "v_cvt_pk_bf16_f32 v203, v218, v219\n\t" \
  "v_cvt_pk_bf16_f32 v201, v218, v219\n\t" \
  "v_mov_b32_dpp v200, v202 row_ror:8 row_mask:0xf bank_mask:0xc\n\t" \
  "v_mov_b32_dpp v202, v202 row_ror:8 row_mask:0xf bank_mask:0x3\n\t" \
  "v_mov_b32_dpp v201, v203 row_ror:8 row_mask:0xf bank_mask:0xc\n\t" \
  "v_mov_b32_dpp v203, v203 row_ror:8 row_mask:0xf bank_mask:0x3\n\t" \
  "v_max_u32 v212, v252, v253\n\t" \
  "v_cmp_le_u32 vcc, %[sent], v212\n\t" \
  "v_mfma_f32_16x16x32_bf16 v[204:207], v[220:223], v[200:203], v[204:207]\n\t" \
  ".Lpv%=:\n\t" \
  "v_mfma_f32_16x16x32_bf16 v[224:227], v[192:195], v[200:203], v[224:227]\n\t" \
  "v_mfma_f32_16x16x32_bf16 v[228:231], v[196:199], v[200:203], v[228:231]\n\t" \
  "s_cbranch_vccnz .Lvs%=\n\t" \
  ".Lvok%=:\n\t" \
  "s_nop 4\n\t" \
  "v_rcp_f32 v212, v204\n\t" \
  "v_cndmask_b32 v224, v224, v226, %[nh]\n\t" \
  "v_cndmask_b32 v225, v225, v227, %[nh]\n\t" \
  "v_mul_f32 v224, v224, v212\n\t" \
  "v_mul_f32 v225, v225, v212\n\t" \
  "v_cvt_pk_bf16_f32 v224, v224, v225\n\t" \
  "buffer_store_dword v224, v251, %[rs], %[so] offen offset:0 sc1\n\t" \
  "v_cndmask_b32 v228, v228, v230, %[nh]\n\t" \
  "v_cndmask_b32 v229, v229, v231, %[nh]\n\t" \
  "v_mul_f32 v228, v228, v212\n\t" \
  "v_mul_f32 v229, v229, v212\n\t" \
  "v_cvt_pk_bf16_f32 v228, v228, v229\n\t" \
  "buffer_store_dword v228, v251, %[rs], %[so] offen offset:256 sc1\n\t" \
  "s_memtime s[90:91]\n\t" \
  "s_setprio 0\n\t" \
  "s_waitcnt lgkmcnt(0)\n\t" \
  "s_mov_b32 %[tout], s90\n\t" \
  "s_mov_b32 %[spins], s92\n\t" \
  "s_branch .Lend%=\n\t" \
  ".Lvs%=:\n\t" \
  "v_cmp_eq_u32 vcc, %[sent], v252\n\t" \
  "v_cmp_eq_u32 s[94:95], %[sent], v253\n\t" \
  "s_or_b64 s[94:95], s[94:95], vcc\n\t" \
  "s_cbranch_scc0 .Lvok%=\n\t" \
  "s_add_u32 s92, s92, 1\n\t" \
  "s_sleep 2\n\t" \
  "s_and_b32 s93, s92, 63\n\t" \
  "s_cbranch_scc1 .Lvr%=\n\t" \
  "buffer_load_dword v212, off, %[rs], %[sab] sc1\n\t" \
  "s_waitcnt vmcnt(0)\n\t" \
  "v_readfirstlane_b32 s93, v212\n\t" \
  "s_nop 3\n\t" \
  "s_cmp_eq_u32 s93, 1\n\t" \
  "s_cbranch_scc1 .Lvok%=\n\t" \
  "s_cmp_lt_u32 s92, 0x80000\n\t" \
  "s_cbranch_scc1 .Lvr%=\n\t" \
  "v_mov_b32 v212, 1\n\t" \
  "buffer_store_dword v212, off, %[rs], %[sab] sc1\n\t" \
  "s_branch .Lvok%=\n\t" \
  ".Lvr%=:\n\t" \
  "buffer_load_dword v252, v249, %[rs], %[sv] offen offset:0 sc1\n\t" \
  "buffer_load_dword v253, v249, %[rs], %[sv] offen offset:256 sc1\n\t" \
  "s_waitcnt vmcnt(0)\n\t" \
  "v_mfma_f32_16x16x16_bf16 v[224:227], v[252:253], v[240:241], 0\n\t" \
  "v_mfma_f32_16x16x16_bf16 v[228:231], v[252:253], v[242:243], 0\n\t" \
  "v_max_u32 v212, v252, v253\n\t" \
  "v_cmp_le_u32 vcc, %[sent], v212\n\t" \
  "s_nop 4\n\t" \
  "s_branch .Lpv%=\n\t" \
  "s_branch .Lend%=\n\t" \
  ".Lslow%=:\n\t" \
  "s_mov_b64 s[94:95], 0\n\t" \
  "v_cmp_eq_u32 vcc, %[sent], v200\n\t" \
  "s_or_b64 s[94:95], s[94:95], vcc\n\t" \
  "v_cmp_eq_u32 vcc, %[sent], v201\n\t" \
  "s_or_b64 s[94:95], s[94:95], vcc\n\t" \
  "v_cmp_eq_u32 vcc, %[sent], v202\n\t" \
  "s_or_b64 s[94:95], s[94:95], vcc\n\t" \
  "v_cmp_eq_u32 vcc, %[sent], v203\n\t" \
  "s_or_b64 s[94:95], s[94:95], vcc\n\t" \
  "v_cmp_eq_u32 vcc, %[sent], v204\n\t" \
  "s_or_b64 s[94:95], s[94:95], vcc\n\t" \
  "v_cmp_eq_u32 vcc, %[sent], v205\n\t" \
  "s_or_b64 s[94:95], s[94:95], vcc\n\t" \
  "v_cmp_eq_u32 vcc, %[sent], v206\n\t" \
  "s_or_b64 s[94:95], s[94:95], vcc\n\t" \
  "v_cmp_eq_u32 vcc, %[sent], v207\n\t" \
  "s_or_b64 s[94:95], s[94:95], vcc\n\t" \
  "s_cmp_eq_u64 s[94:95], 0\n\t" \
  "s_cbranch_scc1 .Lok%=\n\t" \
  "s_add_u32 s92, s92, 1\n\t" \
  "s_cmp_eq_u32 s92, 1\n\t" \
  "s_cbranch_scc1 .Lpoll%=\n\t" \
  "s_sleep 6\n\t" \
  "s_and_b32 s93, s92, 63\n\t" \
  "s_cmp_lg_u32 s93, 0\n\t" \
  "s_cbranch_scc1 .Lpoll%=\n\t" \
  "v_mov_b32 v209, 0\n\t" \
  "buffer_load_dword v208, v209, %[rs], %[sab] offen sc1\n\t" \
  "s_waitcnt vmcnt(0)\n\t" \
  "v_readfirstlane_b32 s93, v208\n\t" \
  "s_nop 3\n\t" \
  "s_cmp_eq_u32 s93, 1\n\t" \
  "s_cbranch_scc1 .Lok%=\n\t" \
  "s_cmp_lt_u32 s92, 0x80000\n\t" \
  "s_cbranch_scc1 .Lpoll%=\n\t" \
  "v_mov_b32 v208, 1\n\t" \
  "buffer_store_dword v208, v209, %[rs], %[sab] offen sc1\n\t" \
  "s_branch .Lok%=\n\t" \
  ".Lend%=:\n\t"
#define QK_ASM_3 \
  "s_waitcnt lgkmcnt(0)\n\t" \
  "ds_read_b128 v[216:219], v250 offset:0\n\t" \
  "ds_read_b128 v[220:223], v250 offset:2048\n\t" \
  "ds_read_b128 v[224:227], v250 offset:1024\n\t" \
  "ds_read_b128 v[228:231], v250 offset:3072\n\t" \
  "v_mov_b64 v[192:193], 0\n\t" \
  "v_mov_b64 v[194:195], 0\n\t" \
  "v_mov_b64 v[196:197], 0\n\t" \
  "v_mov_b64 v[198:199], 0\n\t" \
  "s_memrealtime s[90:91]\n\t" \
  "s_waitcnt lgkmcnt(0)\n\t" \
  "s_lshl_b32 s93, s90, 2\n\t" \
  "s_sub_i32 s93, %[tgt], s93\n\t" \
  "s_cmp_le_i32 s93, 0\n\t" \
  "s_cbranch_scc1 .Lgo%=\n\t" \
  "s_min_i32 s93, s93, 8000\n\t" \
  "s_add_u32 s93, s93, 3\n\t" \
  "s_lshr_b32 s93, s93, 2\n\t" \
  "s_add_u32 s93, s93, s90\n\t" \
  "s_memrealtime s[90:91]\n\t" \
  ".Lpace%=:\n\t" \
  "s_memrealtime s[94:95]\n\t" \
  "s_waitcnt lgkmcnt(1)\n\t" \
  "s_sub_i32 s91, s90, s93\n\t" \
  "s_cmp_ge_i32 s91, 0\n\t" \
  "s_cbranch_scc1 .Lgo%=\n\t" \
  "s_memrealtime s[90:91]\n\t" \
  "s_waitcnt lgkmcnt(1)\n\t" \
  "s_sub_i32 s95, s94, s93\n\t" \
  "s_cmp_lt_i32 s95, 0\n\t" \
  "s_cbranch_scc1 .Lpace%=\n\t" \
  ".Lgo%=:\n\t" \
  "s_mov_b32 s92, 0\n\t" \
  ".Lpoll%=:\n\t" \
  "buffer_load_dwordx4 v[200:203], v248, %[rs], %[som] offen sc1\n\t" \
  "buffer_load_dwordx4 v[204:207], v248, %[rs], %[som] offen offset:1024 sc1\n\t" \
  "s_waitcnt vmcnt(1) lgkmcnt(0)\n\t" \
  "ds_write_b128 v249, v[200:203]\n\t" \
  "v_max3_u32 v208, v200, v201, v202\n\t" \
  "s_waitcnt vmcnt(0)\n\t" \
  "ds_write_b128 v249, v[204:207] offset:1024\n\t" \
  "v_max3_u32 v209, v204, v205, v206\n\t" \
  "v_max3_u32 v208, v208, v203, v207\n\t" \
  "v_max_u32 v208, v208, v209\n\t" \
  "v_cmp_le_u32 vcc, %[sent], v208\n\t" \
  "s_cbranch_vccnz .Lslow%=\n\t" \
  ".Lok%=:\n\t" \
  "v_smfmac_f32_16x16x64_bf16 v[192:195], v[200:203], v[0:7], v251\n\t" \
  "v_smfmac_f32_16x16x64_bf16 v[196:199], v[200:203], v[8:15], v251\n\t" \
  "s_waitcnt lgkmcnt(0)\n\t" \
  "s_barrier\n\t" \
  "s_memrealtime s[90:91]\n\t" \
  "v_mov_b32 v236, %[step]\n\t" \
  "v_lshrrev_b32 v238, 1, v248\n\t" \
  "s_mov_b64 exec, 0xffff\n\t" \
  "s_waitcnt lgkmcnt(0)\n\t" \
  "s_lshl_b32 s90, s90, 2\n\t" \
  "s_sub_i32 s93, s90, %[tgt]\n\t" \
  "s_cmp_gt_i32 s93, 240\n\t" \
  "s_cselect_b32 s93, 60, 0\n\t" \
  "s_add_u32 s93, s93, s90\n\t" \
  "s_add_u32 s93, s93, %[lead]\n\t" \
  "v_mov_b32 v237, s93\n\t" \
  "buffer_store_dwordx2 v[236:237], v238, %[rs], %[sbc] offen sc1\n\t" \
  "s_mov_b64 exec, -1\n\t" \
  "s_mov_b32 %[tb1], s90\n\t" \
  "ds_read_b128 v[208:211], v248 offset:8192\n\t" \
  "ds_read_b128 v[212:215], v248 offset:9216\n\t" \
  "ds_read_b128 v[236:239], v248 offset:10240\n\t" \
  "ds_read_b128 v[240:243], v248 offset:11264\n\t" \
  "ds_read_b128 v[244:247], v248 offset:12288\n\t" \
  "ds_read_b128 v[200:203], v248 offset:13312\n\t" \
  "v_smfmac_f32_16x16x64_bf16 v[192:195], v[204:207], v[16:23], v251\n\t" \
  "v_smfmac_f32_16x16x64_bf16 v[196:199], v[204:207], v[24:31], v251\n\t" \
  "s_waitcnt lgkmcnt(5)\n\t" \
  "v_smfmac_f32_16x16x64_bf16 v[192:195], v[208:211], v[32:39], v251\n\t" \
  "ds_read_b128 v[204:207], v248 offset:14336\n\t" \
  "v_smfmac_f32_16x16x64_bf16 v[196:199], v[208:211], v[40:47], v251\n\t" \
  "s_waitcnt lgkmcnt(5)\n\t" \
  "v_smfmac_f32_16x16x64_bf16 v[192:195], v[212:215], v[216:223], v251\n\t" \
  "ds_read_b128 v[216:219], v250 offset:4096\n\t" \
  "v_smfmac_f32_16x16x64_bf16 v[196:199], v[212:215], v[224:231], v251\n\t" \
  "s_waitcnt lgkmcnt(5)\n\t" \
  "ds_read_b128 v[220:223], v250 offset:6144\n\t" \
  "v_smfmac_f32_16x16x64_bf16 v[192:195], v[236:239], v[48:55], v251\n\t" \
  "ds_read_b128 v[224:227], v250 offset:5120\n\t" \
  "v_smfmac_f32_16x16x64_bf16 v[196:199], v[236:239], v[56:63], v251\n\t" \
  "s_waitcnt lgkmcnt(6)\n\t" \
  "ds_read_b128 v[228:231], v250 offset:7168\n\t" \
  "v_smfmac_f32_16x16x64_bf16 v[192:195], v[240:243], v[64:71], v251\n\t" \
  "ds_read_b128 v[208:211], v248 offset:15360\n\t" \
  "v_smfmac_f32_16x16x64_bf16 v[196:199], v[240:243], v[72:79], v251\n\t" \
  "s_waitcnt lgkmcnt(5)\n\t" \
  "ds_read_b128 v[212:215], v248 offset:0\n\t" \
  "v_smfmac_f32_16x16x64_bf16 v[192:195], v[244:247], v[80:87], v251\n\t" \
  "ds_read_b128 v[236:239], v248 offset:1024\n\t" \
  "v_smfmac_f32_16x16x64_bf16 v[196:199], v[244:247], v[88:95], v251\n\t" \
  "s_waitcnt lgkmcnt(5)\n\t" \
  "v_smfmac_f32_16x16x64_bf16 v[192:195], v[200:203], v[216:223], v251\n\t" \
  "s_waitcnt lgkmcnt(3)\n\t" \
  "ds_read_b128 v[216:219], v250 offset:8192\n\t" \
  "v_smfmac_f32_16x16x64_bf16 v[196:199], v[200:203], v[224:231], v251\n\t" \
  "ds_read_b128 v[220:223], v250 offset:10240\n\t" \
  "v_smfmac_f32_16x16x64_bf16 v[192:195], v[204:207], v[96:103], v251\n\t" \
  "ds_read_b128 v[224:227], v250 offset:9216\n\t" \
  "v_smfmac_f32_16x16x64_bf16 v[196:199], v[204:207], v[104:111], v251\n\t" \
  "s_waitcnt lgkmcnt(5)\n\t" \
  "ds_read_b128 v[228:231], v250 offset:11264\n\t" \
  "v_smfmac_f32_16x16x64_bf16 v[192:195], v[208:211], v[112:119], v251\n\t" \
  "ds_read_b128 v[240:243], v248 offset:2048\n\t" \
  "v_smfmac_f32_16x16x64_bf16 v[196:199], v[208:211], v[120:127], v251\n\t" \
  "s_waitcnt lgkmcnt(5)\n\t" \
  "ds_read_b128 v[244:247], v248 offset:3072\n\t" \
  "v_smfmac_f32_16x16x64_bf16 v[192:195], v[212:215], v[128:135], v251\n\t" \
  "ds_read_b128 v[200:203], v248 offset:4096\n\t" \
  "v_smfmac_f32_16x16x64_bf16 v[196:199], v[212:215], v[136:143], v251\n\t" \
  "s_waitcnt lgkmcnt(5)\n\t" \
  "v_smfmac_f32_16x16x64_bf16 v[192:195], v[236:239], v[216:223], v251\n\t" \
  "s_waitcnt lgkmcnt(3)\n\t" \
  "ds_read_b128 v[204:207], v248 offset:5120\n\t" \
  "v_smfmac_f32_16x16x64_bf16 v[196:199], v[236:239], v[224:231], v251\n\t" \
  "s_waitcnt lgkmcnt(3)\n\t" \
  "ds_read_b128 v[216:219], v250 offset:12288\n\t" \
  "v_smfmac_f32_16x16x64_bf16 v[192:195], v[240:243], v[144:151], v251\n\t" \
  "ds_read_b128 v[220:223], v250 offset:14336\n\t" \
  "v_smfmac_f32_16x16x64_bf16 v[196:199], v[240:243], v[152:159], v251\n\t" \
  "s_waitcnt lgkmcnt(4)\n\t" \
  "ds_read_b128 v[224:227], v250 offset:13312\n\t" \
  "v_smfmac_f32_16x16x64_bf16 v[192:195], v[244:247], v[160:167], v251\n\t" \
  "ds_read_b128 v[228:231], v250 offset:15360\n\t" \
  "v_smfmac_f32_16x16x64_bf16 v[196:199], v[244:247], v[168:175], v251\n\t" \
  "s_waitcnt lgkmcnt(5)\n\t" \
  "v_smfmac_f32_16x16x64_bf16 v[192:195], v[200:203], v[176:183], v251\n\t" \
  "v_smfmac_f32_16x16x64_bf16 v[196:199], v[200:203], v[184:191], v251\n\t" \
  "s_waitcnt lgkmcnt(2)\n\t" \
  "v_smfmac_f32_16x16x64_bf16 v[192:195], v[204:207], v[216:223], v251\n\t" \
  "s_waitcnt lgkmcnt(0)\n\t" \
  "v_smfmac_f32_16x16x64_bf16 v[196:199], v[204:207], v[224:231], v251\n\t" \
  "ds_read_b128 v[200:203], v248 offset:19456\n\t" \
  "ds_read_b128 v[204:207], v248 offset:23552\n\t" \
  "v_mov_b32 v250, v232\n\t" \
  "v_mov_b32 v249, v233\n\t" \
  "s_nop 2\n\t" \
  "v_add_f32 v192, v192, v194\n\t" \
  "v_add_f32 v193, v193, v195\n\t" \
  "v_add_f32 v196, v196, v198\n\t" \
  "v_add_f32 v197, v197, v199\n\t" \
  "v_cvt_pk_bf16_f32 v192, v192, v193\n\t" \
  "v_cvt_pk_bf16_f32 v196, v196, v197\n\t" \
  "ds_write2_b32 v252, v192, v196 offset1:64\n\t" \
  "v_mov_b32 v251, v234\n\t" \
  "ds_read_b64_tr_b16 v[244:245], v250 offset:1536\n\t" \
  "ds_read_b64_tr_b16 v[246:247], v250 offset:1600\n\t" \
  "buffer_load_dwordx4 v[192:195], v248, %[rt], %[svt] offen offset:0 sc1\n\t" \
  "buffer_load_dwordx4 v[196:199], v248, %[rt], %[svt] offen offset:1024 sc1\n\t" \
  "s_waitcnt lgkmcnt(0)\n\t" \
  "v_mfma_f32_16x16x32_bf16 v[200:203], v[200:203], v[244:247], 0\n\t" \
  "v_mfma_f32_16x16x32_bf16 v[204:207], v[204:207], v[244:247], 0\n\t" \
  "s_nop 7\n\t" \
  "v_cndmask_b32 v216, v200, v204, %[nh]\n\t" \
  "v_cndmask_b32 v217, v201, v205, %[nh]\n\t" \
  "v_cndmask_b32 v218, v202, v206, %[nh]\n\t" \
  "v_cndmask_b32 v219, v203, v207, %[nh]\n\t" \
  "ds_write_b128 v248, v[216:219] offset:19456\n\t" \
  "s_sleep 2\n\t" \
  "buffer_load_dword v252, v249, %[rs], %[sv] offen offset:0 sc1\n\t" \
  "buffer_load_dword v253, v249, %[rs], %[sv] offen offset:256 sc1\n\t" \
  "s_setprio 3\n\t" \
  "s_waitcnt lgkmcnt(0)\n\t" \
  "s_barrier\n\t" \
  "ds_read_b64_tr_b16 v[212:213], v250 offset:3584\n\t" \
  "ds_read_b64_tr_b16 v[214:215], v250 offset:3648\n\t" \
  "ds_read_b64_tr_b16 v[232:233], v250 offset:0\n\t" \
  "ds_read_b64_tr_b16 v[234:235], v250 offset:64\n\t" \
  "ds_read_b64_tr_b16 v[200:201], v250 offset:2048\n\t" \
  "ds_read_b64_tr_b16 v[202:203], v250 offset:2112\n\t" \
  "ds_read_b64_tr_b16 v[236:237], v250 offset:512\n\t" \
  "ds_read_b64_tr_b16 v[238:239], v250 offset:576\n\t" \
  "ds_read_b64_tr_b16 v[204:205], v250 offset:2560\n\t" \
  "ds_read_b64_tr_b16 v[206:207], v250 offset:2624\n\t" \
  "ds_read_b64_tr_b16 v[240:241], v250 offset:1024\n\t" \
  "ds_read_b64_tr_b16 v[242:243], v250 offset:1088\n\t" \
  "ds_read_b64_tr_b16 v[208:209], v250 offset:3072\n\t" \
  "ds_read_b64_tr_b16 v[210:211], v250 offset:3136\n\t" \
  "ds_read_b128 v[220:223], v248 offset:16384\n\t" \
  "s_waitcnt lgkmcnt(13)\n\t" \
  "v_mfma_f32_16x16x32_bf16 v[212:215], v[212:215], v[244:247], 0\n\t" \
  "ds_read_b128 v[224:227], v248 offset:17408\n\t" \
  "ds_read_b128 v[228:231], v248 offset:18432\n\t" \
  "s_waitcnt lgkmcnt(11)\n\t" \
  "v_mfma_f32_16x16x32_bf16 v[212:215], v[200:203], v[232:235], v[212:215]\n\t" \
  "v_mov_b32 v245, 0\n\t" \
  "v_mov_b32 v246, 0\n\t" \
  "s_waitcnt lgkmcnt(7)\n\t" \
  "v_mfma_f32_16x16x32_bf16 v[212:215], v[204:207], v[236:239], v[212:215]\n\t" \
  "s_waitcnt lgkmcnt(3)\n\t" \
  "v_mfma_f32_16x16x32_bf16 v[212:215], v[208:211], v[240:243], v[212:215]\n\t" \
  "s_waitcnt lgkmcnt(2)\n\t" \
  "v_pk_add_f32 v[216:217], v[216:217], v[220:221]\n\t" \
  "v_pk_add_f32 v[218:219], v[218:219], v[222:223]\n\t" \
  "s_waitcnt lgkmcnt(0)\n\t" \
  "v_pk_add_f32 v[224:225], v[224:225], v[228:229]\n\t" \
  "v_pk_add_f32 v[226:227], v[226:227], v[230:231]\n\t" \
  "v_pk_add_f32 v[216:217], v[216:217], v[224:225]\n\t" \
  "v_pk_add_f32 v[218:219], v[218:219], v[226:227]\n\t" \
  "v_max3_f32 v200, v216, v217, v218\n\t" \
  "v_cndmask_b32 v212, v212, v214, %[up]\n\t" \
  "v_cndmask_b32 v213, v213, v215, %[up]\n\t" \
  "v_max_f32 v200, v200, v219\n\t" \
  "v_max3_f32 v200, v200, v212, v213\n\t" \
  "v_mov_b32 v220, 0x3f803f80\n\t" \
  "v_mov_b32 v221, 0x3f803f80\n\t" \
  "v_max_f32_dpp v201, v200, v200 row_ror:8 row_mask:0xf bank_mask:0xf\n\t" \
  "v_max_f32_dpp v202, v200, v200 row_ror:8 row_mask:0xf bank_mask:0xf\n\t" \
  "v_mov_b64 v[222:223], v[220:221]\n\t" \
  "s_nop 0\n\t" \
  "v_permlane16_swap_b32 v201, v202\n\t" \
  "v_max_f32 v200, v201, v202\n\t" \
  "v_max_f32 v203, v201, v202\n\t" \
  "s_nop 1\n\t" \
  "v_permlane32_swap_b32 v200, v203\n\t" \
  "v_max_f32 v200, v200, v203\n\t" \
  "v_mul_f32 v200, %[negc], v200\n\t" \
  "v_fma_f32 v212, v212, %[c], v200\n\t" \
  "v_fma_f32 v213, v213, %[c], v200\n\t" \
  "v_exp_f32 v212, v212\n\t" \
  "v_fma_f32 v216, v216, %[c], v200\n\t" \
  "v_exp_f32 v213, v213\n\t" \
  "v_fma_f32 v217, v217, %[c], v200\n\t" \
  "v_exp_f32 v216, v216\n\t" \
  "v_fma_f32 v218, v218, %[c], v200\n\t" \
  "v_cvt_pk_bf16_f32 v244, v212, v213\n\t" \
  "v_cvt_pk_bf16_f32 v247, v212, v213\n\t" \
  "v_exp_f32 v217, v217\n\t" \
  "v_fma_f32 v219, v219, %[c], v200\n\t" \
  "v_mfma_f32_16x16x16_bf16 v[208:211], v[220:221], v[244:245], 0\n\t" \
  "v_exp_f32 v218, v218\n\t" \
  "s_waitcnt vmcnt(0)\n\t" \
  "v_mfma_f32_16x16x16_bf16 v[224:227], v[252:253], v[244:245], 0\n\t" \
  "v_cvt_pk_bf16_f32 v206, v216, v217\n\t" \
  "v_cvt_pk_bf16_f32 v204, v216, v217\n\t" \
  "v_exp_f32 v219, v219\n\t" \
  "v_mfma_f32_16x16x16_bf16 v[228:231], v[252:253], v[246:247], 0\n\t" \
  "v_cvt_pk_bf16_f32 v207, v218, v219\n\t" \
  "v_cvt_pk_bf16_f32 v205, v218, v219\n\t" \
  "v_mov_b32_dpp v204, v206 row_ror:8 row_mask:0xf bank_mask:0xc\n\t" \
  "v_mov_b32_dpp v206, v206 row_ror:8 row_mask:0xf bank_mask:0x3\n\t" \
  "v_mov_b32_dpp v205, v207 row_ror:8 row_mask:0xf bank_mask:0xc\n\t" \
  "v_mov_b32_dpp v207, v207 row_ror:8 row_mask:0xf bank_mask:0x3\n\t" \
  "v_max_u32 v200, v252, v253\n\t" \
  "v_cmp_le_u32 vcc, %[sent], v200\n\t" \
  "v_mfma_f32_16x16x32_bf16 v[208:211], v[220:223], v[204:207], v[208:211]\n\t" \
  ".Lpv%=:\n\t" \
  "v_mfma_f32_16x16x32_bf16 v[224:227], v[192:195], v[204:207], v[224:227]\n\t" \
  "v_mfma_f32_16x16x32_bf16 v[228:231], v[196:199], v[204:207], v[228:231]\n\t" \
  "s_cbranch_vccnz .Lvs%=\n\t" \
  ".Lvok%=:\n\t" \
  "s_nop 4\n\t" \
  "v_rcp_f32 v200, v208\n\t" \
  "v_cndmask_b32 v224, v224, v226, %[nh]\n\t" \
  "v_cndmask_b32 v225, v225, v227, %[nh]\n\t" \
  "v_mul_f32 v224, v224, v200\n\t" \
  "v_mul_f32 v225, v225, v200\n\t" \
  "v_cvt_pk_bf16_f32 v224, v224, v225\n\t" \
  "buffer_store_dword v224, v251, %[rs], %[so] offen offset:0 sc1\n\t" \
  "v_cndmask_b32 v228, v228, v230, %[nh]\n\t" \
  "v_cndmask_b32 v229, v229, v231, %[nh]\n\t" \
  "v_mul_f32 v228, v228, v200\n\t" \
  "v_mul_f32 v229, v229, v200\n\t" \
  "v_cvt_pk_bf16_f32 v228, v228, v229\n\t" \
  "buffer_store_dword v228, v251, %[rs], %[so] offen offset:256 sc1\n\t" \
  "s_memtime s[90:91]\n\t" \
  "s_setprio 0\n\t" \
  "s_waitcnt lgkmcnt(0)\n\t" \
  "s_mov_b32 %[tout], s90\n\t" \
  "s_mov_b32 %[spins], s92\n\t" \
  "s_branch .Lend%=\n\t" \
  ".Lvs%=:\n\t" \
  "v_cmp_eq_u32 vcc, %[sent], v252\n\t" \
  "v_cmp_eq_u32 s[94:95], %[sent], v253\n\t" \
  "s_or_b64 s[94:95], s[94:95], vcc\n\t" \
  "s_cbranch_scc0 .Lvok%=\n\t" \
  "s_add_u32 s92, s92, 1\n\t" \
  "s_sleep 2\n\t" \
  "s_and_b32 s93, s92, 63\n\t" \
  "s_cbranch_scc1 .Lvr%=\n\t" \
  "buffer_load_dword v200, off, %[rs], %[sab] sc1\n\t" \
  "s_waitcnt vmcnt(0)\n\t" \
  "v_readfirstlane_b32 s93, v200\n\t" \
  "s_nop 3\n\t" \
  "s_cmp_eq_u32 s93, 1\n\t" \
  "s_cbranch_scc1 .Lvok%=\n\t" \
  "s_cmp_lt_u32 s92, 0x80000\n\t" \
  "s_cbranch_scc1 .Lvr%=\n\t" \
  "v_mov_b32 v200, 1\n\t" \
  "buffer_store_dword v200, off, %[rs], %[sab] sc1\n\t" \
  "s_branch .Lvok%=\n\t" \
  ".Lvr%=:\n\t" \
  "buffer_load_dword v252, v249, %[rs], %[sv] offen offset:0 sc1\n\t" \
  "buffer_load_dword v253, v249, %[rs], %[sv] offen offset:256 sc1\n\t" \
  "s_waitcnt vmcnt(0)\n\t" \
  "v_mfma_f32_16x16x16_bf16 v[224:227], v[252:253], v[244:245], 0\n\t" \
  "v_mfma_f32_16x16x16_bf16 v[228:231], v[252:253], v[246:247], 0\n\t" \
  "v_max_u32 v200, v252, v253\n\t" \
  "v_cmp_le_u32 vcc, %[sent], v200\n\t" \
  "s_nop 4\n\t" \
  "s_branch .Lpv%=\n\t" \
  "s_branch .Lend%=\n\t" \
  ".Lslow%=:\n\t" \
  "s_mov_b64 s[94:95], 0\n\t" \
  "v_cmp_eq_u32 vcc, %[sent], v200\n\t" \
  "s_or_b64 s[94:95], s[94:95], vcc\n\t" \
  "v_cmp_eq_u32 vcc, %[sent], v201\n\t" \
  "s_or_b64 s[94:95], s[94:95], vcc\n\t" \
  "v_cmp_eq_u32 vcc, %[sent], v202\n\t" \
  "s_or_b64 s[94:95], s[94:95], vcc\n\t" \
  "v_cmp_eq_u32 vcc, %[sent], v203\n\t" \
  "s_or_b64 s[94:95], s[94:95], vcc\n\t" \
  "v_cmp_eq_u32 vcc, %[sent], v204\n\t" \
  "s_or_b64 s[94:95], s[94:95], vcc\n\t" \
  "v_cmp_eq_u32 vcc, %[sent], v205\n\t" \
  "s_or_b64 s[94:95], s[94:95], vcc\n\t" \
  "v_cmp_eq_u32 vcc, %[sent], v206\n\t" \
  "s_or_b64 s[94:95], s[94:95], vcc\n\t" \
  "v_cmp_eq_u32 vcc, %[sent], v207\n\t" \
  "s_or_b64 s[94:95], s[94:95], vcc\n\t" \
  "s_cmp_eq_u64 s[94:95], 0\n\t" \
  "s_cbranch_scc1 .Lok%=\n\t" \
  "s_add_u32 s92, s92, 1\n\t" \
  "s_cmp_eq_u32 s92, 1\n\t" \
  "s_cbranch_scc1 .Lpoll%=\n\t" \
  "s_sleep 6\n\t" \
  "s_and_b32 s93, s92, 63\n\t" \
  "s_cmp_lg_u32 s93, 0\n\t" \
  "s_cbranch_scc1 .Lpoll%=\n\t" \
  "v_mov_b32 v209, 0\n\t" \
  "buffer_load_dword v208, v209, %[rs], %[sab] offen sc1\n\t" \
  "s_waitcnt vmcnt(0)\n\t" \
  "v_readfirstlane_b32 s93, v208\n\t" \
  "s_nop 3\n\t" \
  "s_cmp_eq_u32 s93, 1\n\t" \
  "s_cbranch_scc1 .Lok%=\n\t" \
  "s_cmp_lt_u32 s92, 0x80000\n\t" \
  "s_cbranch_scc1 .Lpoll%=\n\t" \
  "v_mov_b32 v208, 1\n\t" \
  "buffer_store_dword v208, v209, %[rs], %[sab] offen sc1\n\t" \
  "s_branch .Lok%=\n\t" \
  ".Lend%=:\n\t"
#define QK_ASM_4 \
  "s_waitcnt lgkmcnt(0)\n\t" \
  "ds_read_b128 v[216:219], v250 offset:0\n\t" \
  "ds_read_b128 v[220:223], v250 offset:2048\n\t" \
  "ds_read_b128 v[224:227], v250 offset:1024\n\t" \
  "ds_read_b128 v[228:231], v250 offset:3072\n\t" \
  "v_mov_b64 v[192:193], 0\n\t" \
  "v_mov_b64 v[194:195], 0\n\t" \
  "v_mov_b64 v[196:197], 0\n\t" \
  "v_mov_b64 v[198:199], 0\n\t" \
  "s_memrealtime s[90:91]\n\t" \
  "s_waitcnt lgkmcnt(0)\n\t" \
  "s_lshl_b32 s93, s90, 2\n\t" \
  "s_sub_i32 s93, %[tgt], s93\n\t" \
  "s_cmp_le_i32 s93, 0\n\t" \
  "s_cbranch_scc1 .Lgo%=\n\t" \
  "s_min_i32 s93, s93, 8000\n\t" \
  "s_add_u32 s93, s93, 3\n\t" \
  "s_lshr_b32 s93, s93, 2\n\t" \
  "s_add_u32 s93, s93, s90\n\t" \
  "s_memrealtime s[90:91]\n\t" \
  ".Lpace%=:\n\t" \
  "s_memrealtime s[94:95]\n\t" \
  "s_waitcnt lgkmcnt(1)\n\t" \
  "s_sub_i32 s91, s90, s93\n\t" \
  "s_cmp_ge_i32 s91, 0\n\t" \
  "s_cbranch_scc1 .Lgo%=\n\t" \
  "s_memrealtime s[90:91]\n\t" \
  "s_waitcnt lgkmcnt(1)\n\t" \
  "s_sub_i32 s95, s94, s93\n\t" \
  "s_cmp_lt_i32 s95, 0\n\t" \
  "s_cbranch_scc1 .Lpace%=\n\t" \
  ".Lgo%=:\n\t" \
  "s_mov_b32 s92, 0\n\t" \
  ".Lpoll%=:\n\t" \
  "buffer_load_dwordx4 v[200:203], v248, %[rs], %[som] offen sc1\n\t" \
  "buffer_load_dwordx4 v[204:207], v248, %[rs], %[som] offen offset:1024 sc1\n\t" \
  "s_waitcnt vmcnt(1) lgkmcnt(0)\n\t" \
  "ds_write_b128 v249, v[200:203]\n\t" \
  "v_max3_u32 v208, v200, v201, v202\n\t" \
  "s_waitcnt vmcnt(0)\n\t" \
  "ds_write_b128 v249, v[204:207] offset:1024\n\t" \
  "v_max3_u32 v209, v204, v205, v206\n\t" \
  "v_max3_u32 v208, v208, v203, v207\n\t" \
  "v_max_u32 v208, v208, v209\n\t" \
  "v_cmp_le_u32 vcc, %[sent], v208\n\t" \
  "s_cbranch_vccnz .Lslow%=\n\t" \
  ".Lok%=:\n\t" \
  "v_smfmac_f32_16x16x64_bf16 v[192:195], v[200:203], v[0:7], v251\n\t" \
  "v_smfmac_f32_16x16x64_bf16 v[196:199], v[200:203], v[8:15], v251\n\t" \
  "s_waitcnt lgkmcnt(0)\n\t" \
  "s_barrier\n\t" \
  "ds_read_b128 v[208:211], v248 offset:10240\n\t" \
  "ds_read_b128 v[212:215], v248 offset:11264\n\t" \
  "ds_read_b128 v[236:239], v248 offset:12288\n\t" \
  "ds_read_b128 v[240:243], v248 offset:13312\n\t" \
  "ds_read_b128 v[244:247], v248 offset:14336\n\t" \
  "ds_read_b128 v[200:203], v248 offset:15360\n\t" \
  "v_smfmac_f32_16x16x64_bf16 v[192:195], v[204:207], v[16:23], v251\n\t" \
  "v_smfmac_f32_16x16x64_bf16 v[196:199], v[204:207], v[24:31], v251\n\t" \
  "s_waitcnt lgkmcnt(5)\n\t" \
  "v_smfmac_f32_16x16x64_bf16 v[192:195], v[208:211], v[32:39], v251\n\t" \
  "ds_read_b128 v[204:207], v248 offset:0\n\t" \
  "v_smfmac_f32_16x16x64_bf16 v[196:199], v[208:211], v[40:47], v251\n\t" \
  "s_waitcnt lgkmcnt(5)\n\t" \
  "v_smfmac_f32_16x16x64_bf16 v[192:195], v[212:215], v[216:223], v251\n\t" \
  "ds_read_b128 v[216:219], v250 offset:4096\n\t" \
  "v_smfmac_f32_16x16x64_bf16 v[196:199], v[212:215], v[224:231], v251\n\t" \
  "s_waitcnt lgkmcnt(5)\n\t" \
  "ds_read_b128 v[220:223], v250 offset:6144\n\t" \
  "v_smfmac_f32_16x16x64_bf16 v[192:195], v[236:239], v[48:55], v251\n\t" \
  "ds_read_b128 v[224:227], v250 offset:5120\n\t" \
  "v_smfmac_f32_16x16x64_bf16 v[196:199], v[236:239], v[56:63], v251\n\t" \
  "s_waitcnt lgkmcnt(6)\n\t" \
  "ds_read_b128 v[228:231], v250 offset:7168\n\t" \
  "v_smfmac_f32_16x16x64_bf16 v[192:195], v[240:243], v[64:71], v251\n\t" \
  "ds_read_b128 v[208:211], v248 offset:1024\n\t" \
  "v_smfmac_f32_16x16x64_bf16 v[196:199], v[240:243], v[72:79], v251\n\t" \
  "s_waitcnt lgkmcnt(5)\n\t" \
  "ds_read_b128 v[212:215], v248 offset:2048\n\t" \
  "v_smfmac_f32_16x16x64_bf16 v[192:195], v[244:247], v[80:87], v251\n\t" \
  "ds_read_b128 v[236:239], v248 offset:3072\n\t" \
  "v_smfmac_f32_16x16x64_bf16 v[196:199], v[244:247], v[88:95], v251\n\t" \
  "s_waitcnt lgkmcnt(5)\n\t" \
  "v_smfmac_f32_16x16x64_bf16 v[192:195], v[200:203], v[216:223], v251\n\t" \
  "s_waitcnt lgkmcnt(3)\n\t" \
  "ds_read_b128 v[216:219], v250 offset:8192\n\t" \
  "v_smfmac_f32_16x16x64_bf16 v[196:199], v[200:203], v[224:231], v251\n\t" \
  "ds_read_b128 v[220:223], v250 offset:10240\n\t" \
  "v_smfmac_f32_16x16x64_bf16 v[192:195], v[204:207], v[96:103], v251\n\t" \
  "ds_read_b128 v[224:227], v250 offset:9216\n\t" \
  "v_smfmac_f32_16x16x64_bf16 v[196:199], v[204:207], v[104:111], v251\n\t" \
  "s_waitcnt lgkmcnt(5)\n\t" \
  "ds_read_b128 v[228:231], v250 offset:11264\n\t" \
  "v_smfmac_f32_16x16x64_bf16 v[192:195], v[208:211], v[112:119], v251\n\t" \
  "ds_read_b128 v[240:243], v248 offset:4096\n\t" \
  "v_smfmac_f32_16x16x64_bf16 v[196:199], v[208:211], v[120:127], v251\n\t" \
  "s_waitcnt lgkmcnt(5)\n\t" \
  "ds_read_b128 v[244:247], v248 offset:5120\n\t" \
  "v_smfmac_f32_16x16x64_bf16 v[192:195], v[212:215], v[128:135], v251\n\t" \
  "ds_read_b128 v[200:203], v248 offset:6144\n\t" \
  "v_smfmac_f32_16x16x64_bf16 v[196:199], v[212:215], v[136:143], v251\n\t" \
  "s_waitcnt lgkmcnt(5)\n\t" \
  "v_smfmac_f32_16x16x64_bf16 v[192:195], v[236:239], v[216:223], v251\n\t" \
  "s_waitcnt lgkmcnt(3)\n\t" \
  "ds_read_b128 v[204:207], v248 offset:7168\n\t" \
  "v_smfmac_f32_16x16x64_bf16 v[196:199], v[236:239], v[224:231], v251\n\t" \
  "s_waitcnt lgkmcnt(3)\n\t" \
  "ds_read_b128 v[216:219], v250 offset:12288\n\t" \
  "v_smfmac_f32_16x16x64_bf16 v[192:195], v[240:243], v[144:151], v251\n\t" \
  "ds_read_b128 v[220:223], v250 offset:14336\n\t" \
  "v_smfmac_f32_16x16x64_bf16 v[196:199], v[240:243], v[152:159], v251\n\t" \
  "s_waitcnt lgkmcnt(4)\n\t" \
  "ds_read_b128 v[224:227], v250 offset:13312\n\t" \
  "v_smfmac_f32_16x16x64_bf16 v[192:195], v[244:247], v[160:167], v251\n\t" \
  "ds_read_b128 v[228:231], v250 offset:15360\n\t" \
  "v_smfmac_f32_16x16x64_bf16 v[196:199], v[244:247], v[168:175], v251\n\t" \
  "s_waitcnt lgkmcnt(5)\n\t" \
  "v_smfmac_f32_16x16x64_bf16 v[192:195], v[200:203], v[176:183], v251\n\t" \
  "v_smfmac_f32_16x16x64_bf16 v[196:199], v[200:203], v[184:191], v251\n\t" \
  "s_waitcnt lgkmcnt(2)\n\t" \
  "v_smfmac_f32_16x16x64_bf16 v[192:195], v[204:207], v[216:223], v251\n\t" \
  "s_waitcnt lgkmcnt(0)\n\t" \
  "v_smfmac_f32_16x16x64_bf16 v[196:199], v[204:207], v[224:231], v251\n\t" \
  "s_nop 6\n\t" \
  "v_add_f32 v192, v192, v194\n\t" \
  "v_add_f32 v193, v193, v195\n\t" \
  "v_add_f32 v196, v196, v198\n\t" \
  "v_add_f32 v197, v197, v199\n\t" \
  "v_cvt_pk_bf16_f32 v192, v192, v193\n\t" \
  "v_cvt_pk_bf16_f32 v196, v196, v197\n\t" \
  "ds_write2_b32 v252, v192, v196 offset1:64\n\t" \
  "s_waitcnt lgkmcnt(0)\n\t" \
  "s_barrier\n\t" \
  "buffer_store_dword v192, v235, %[rs], %[sk] offen sc1\n\t" \
  "buffer_store_dword v196, v235, %[rs], %[sk] offen offset:256 sc1\n\t" \
  "s_mov_b32 %[spins], s92\n\t" \
  "s_branch .Lend%=\n\t" \
  ".Lslow%=:\n\t" \
  "s_mov_b64 s[94:95], 0\n\t" \
  "v_cmp_eq_u32 vcc, %[sent], v200\n\t" \
  "s_or_b64 s[94:95], s[94:95], vcc\n\t" \
  "v_cmp_eq_u32 vcc, %[sent], v201\n\t" \
  "s_or_b64 s[94:95], s[94:95], vcc\n\t" \
  "v_cmp_eq_u32 vcc, %[sent], v202\n\t" \
  "s_or_b64 s[94:95], s[94:95], vcc\n\t" \
  "v_cmp_eq_u32 vcc, %[sent], v203\n\t" \
  "s_or_b64 s[94:95], s[94:95], vcc\n\t" \
  "v_cmp_eq_u32 vcc, %[sent], v204\n\t" \
  "s_or_b64 s[94:95], s[94:95], vcc\n\t" \
  "v_cmp_eq_u32 vcc, %[sent], v205\n\t" \
  "s_or_b64 s[94:95], s[94:95], vcc\n\t" \
  "v_cmp_eq_u32 vcc, %[sent], v206\n\t" \
  "s_or_b64 s[94:95], s[94:95], vcc\n\t" \
  "v_cmp_eq_u32 vcc, %[sent], v207\n\t" \
  "s_or_b64 s[94:95], s[94:95], vcc\n\t" \
  "s_cmp_eq_u64 s[94:95], 0\n\t" \
  "s_cbranch_scc1 .Lok%=\n\t" \
  "s_add_u32 s92, s92, 1\n\t" \
  "s_cmp_eq_u32 s92, 1\n\t" \
  "s_cbranch_scc1 .Lpoll%=\n\t" \
  "s_sleep 6\n\t" \
  "s_and_b32 s93, s92, 63\n\t" \
  "s_cmp_lg_u32 s93, 0\n\t" \
  "s_cbranch_scc1 .Lpoll%=\n\t" \
  "v_mov_b32 v209, 0\n\t" \
  "buffer_load_dword v208, v209, %[rs], %[sab] offen sc1\n\t" \
  "s_waitcnt vmcnt(0)\n\t" \
  "v_readfirstlane_b32 s93, v208\n\t" \
  "s_nop 3\n\t" \
  "s_cmp_eq_u32 s93, 1\n\t" \
  "s_cbranch_scc1 .Lok%=\n\t" \
  "s_cmp_lt_u32 s92, 0x80000\n\t" \
  "s_cbranch_scc1 .Lpoll%=\n\t" \
  "v_mov_b32 v208, 1\n\t" \
  "buffer_store_dword v208, v209, %[rs], %[sab] offen sc1\n\t" \
  "s_branch .Lok%=\n\t" \
  ".Lend%=:\n\t"
#define QK_ASM_5 \
  "s_waitcnt lgkmcnt(0)\n\t" \
  "ds_read_b128 v[216:219], v250 offset:0\n\t" \
  "ds_read_b128 v[220:223], v250 offset:2048\n\t" \
  "ds_read_b128 v[224:227], v250 offset:1024\n\t" \
  "ds_read_b128 v[228:231], v250 offset:3072\n\t" \
  "v_mov_b64 v[192:193], 0\n\t" \
  "v_mov_b64 v[194:195], 0\n\t" \
  "v_mov_b64 v[196:197], 0\n\t" \
  "v_mov_b64 v[198:199], 0\n\t" \
  "s_memrealtime s[90:91]\n\t" \
  "s_waitcnt lgkmcnt(0)\n\t" \
  "s_lshl_b32 s93, s90, 2\n\t" \
  "s_sub_i32 s93, %[tgt], s93\n\t" \
  "s_cmp_le_i32 s93, 0\n\t" \
  "s_cbranch_scc1 .Lgo%=\n\t" \
  "s_min_i32 s93, s93, 8000\n\t" \
  "s_add_u32 s93, s93, 3\n\t" \
  "s_lshr_b32 s93, s93, 2\n\t" \
  "s_add_u32 s93, s93, s90\n\t" \
  "s_memrealtime s[90:91]\n\t" \
  ".Lpace%=:\n\t" \
  "s_memrealtime s[94:95]\n\t" \
  "s_waitcnt lgkmcnt(1)\n\t" \
  "s_sub_i32 s91, s90, s93\n\t" \
  "s_cmp_ge_i32 s91, 0\n\t" \
  "s_cbranch_scc1 .Lgo%=\n\t" \
  "s_memrealtime s[90:91]\n\t" \
  "s_waitcnt lgkmcnt(1)\n\t" \
  "s_sub_i32 s95, s94, s93\n\t" \
  "s_cmp_lt_i32 s95, 0\n\t" \
  "s_cbranch_scc1 .Lpace%=\n\t" \
  ".Lgo%=:\n\t" \
  "s_mov_b32 s92, 0\n\t" \
  ".Lpoll%=:\n\t" \
  "buffer_load_dwordx4 v[200:203], v248, %[rs], %[som] offen sc1\n\t" \
  "buffer_load_dwordx4 v[204:207], v248, %[rs], %[som] offen offset:1024 sc1\n\t" \
  "s_waitcnt vmcnt(1) lgkmcnt(0)\n\t" \
  "ds_write_b128 v249, v[200:203]\n\t" \
  "v_max3_u32 v208, v200, v201, v202\n\t" \
  "s_waitcnt vmcnt(0)\n\t" \
  "ds_write_b128 v249, v[204:207] offset:1024\n\t" \
  "v_max3_u32 v209, v204, v205, v206\n\t" \
  "v_max3_u32 v208, v208, v203, v207\n\t" \
  "v_max_u32 v208, v208, v209\n\t" \
  "v_cmp_le_u32 vcc, %[sent], v208\n\t" \
  "s_cbranch_vccnz .Lslow%=\n\t" \
  ".Lok%=:\n\t" \
  "v_smfmac_f32_16x16x64_bf16 v[192:195], v[200:203], v[0:7], v251\n\t" \
  "v_smfmac_f32_16x16x64_bf16 v[196:199], v[200:203], v[8:15], v251\n\t" \
  "s_waitcnt lgkmcnt(0)\n\t" \
  "s_barrier\n\t" \
  "ds_read_b128 v[208:211], v248 offset:12288\n\t" \
  "ds_read_b128 v[212:215], v248 offset:13312\n\t" \
  "ds_read_b128 v[236:239], v248 offset:14336\n\t" \
  "ds_read_b128 v[240:243], v248 offset:15360\n\t" \
  "ds_read_b128 v[244:247], v248 offset:0\n\t" \
  "ds_read_b128 v[200:203], v248 offset:1024\n\t" \
  "v_smfmac_f32_16x16x64_bf16 v[192:195], v[204:207], v[16:23], v251\n\t" \
  "v_smfmac_f32_16x16x64_bf16 v[196:199], v[204:207], v[24:31], v251\n\t" \
  "s_waitcnt lgkmcnt(5)\n\t" \
  "v_smfmac_f32_16x16x64_bf16 v[192:195], v[208:211], v[32:39], v251\n\t" \
  "ds_read_b128 v[204:207], v248 offset:2048\n\t" \
  "v_smfmac_f32_16x16x64_bf16 v[196:199], v[208:211], v[40:47], v251\n\t" \
  "s_waitcnt lgkmcnt(5)\n\t" \
  "v_smfmac_f32_16x16x64_bf16 v[192:195], v[212:215], v[216:223], v251\n\t" \
  "ds_read_b128 v[216:219], v250 offset:4096\n\t" \
  "v_smfmac_f32_16x16x64_bf16 v[196:199], v[212:215], v[224:231], v251\n\t" \
  "s_waitcnt lgkmcnt(5)\n\t" \
  "ds_read_b128 v[220:223], v250 offset:6144\n\t" \
  "v_smfmac_f32_16x16x64_bf16 v[192:195], v[236:239], v[48:55], v251\n\t" \
  "ds_read_b128 v[224:227], v250 offset:5120\n\t" \
  "v_smfmac_f32_16x16x64_bf16 v[196:199], v[236:239], v[56:63], v251\n\t" \
  "s_waitcnt lgkmcnt(6)\n\t" \
  "ds_read_b128 v[228:231], v250 offset:7168\n\t" \
  "v_smfmac_f32_16x16x64_bf16 v[192:195], v[240:243], v[64:71], v251\n\t" \
  "ds_read_b128 v[208:211], v248 offset:3072\n\t" \
  "v_smfmac_f32_16x16x64_bf16 v[196:199], v[240:243], v[72:79], v251\n\t" \
  "s_waitcnt lgkmcnt(5)\n\t" \
  "ds_read_b128 v[212:215], v248 offset:4096\n\t" \
  "v_smfmac_f32_16x16x64_bf16 v[192:195], v[244:247], v[80:87], v251\n\t" \
  "ds_read_b128 v[236:239], v248 offset:5120\n\t" \
  "v_smfmac_f32_16x16x64_bf16 v[196:199], v[244:247], v[88:95], v251\n\t" \
  "s_waitcnt lgkmcnt(5)\n\t" \
  "v_smfmac_f32_16x16x64_bf16 v[192:195], v[200:203], v[216:223], v251\n\t" \
  "s_waitcnt lgkmcnt(3)\n\t" \
  "ds_read_b128 v[216:219], v250 offset:8192\n\t" \
  "v_smfmac_f32_16x16x64_bf16 v[196:199], v[200:203], v[224:231], v251\n\t" \
  "ds_read_b128 v[220:223], v250 offset:10240\n\t" \
  "v_smfmac_f32_16x16x64_bf16 v[192:195], v[204:207], v[96:103], v251\n\t" \
  "ds_read_b128 v[224:227], v250 offset:9216\n\t" \
  "v_smfmac_f32_16x16x64_bf16 v[196:199], v[204:207], v[104:111], v251\n\t" \
  "s_waitcnt lgkmcnt(5)\n\t" \
  "ds_read_b128 v[228:231], v250 offset:11264\n\t" \
  "v_smfmac_f32_16x16x64_bf16 v[192:195], v[208:211], v[112:119], v251\n\t" \
  "ds_read_b128 v[240:243], v248 offset:6144\n\t" \
  "v_smfmac_f32_16x16x64_bf16 v[196:199], v[208:211], v[120:127], v251\n\t" \
  "s_waitcnt lgkmcnt(5)\n\t" \
  "ds_read_b128 v[244:247], v248 offset:7168\n\t" \
  "v_smfmac_f32_16x16x64_bf16 v[192:195], v[212:215], v[128:135], v251\n\t" \
  "ds_read_b128 v[200:203], v248 offset:8192\n\t" \
  "v_smfmac_f32_16x16x64_bf16 v[196:199], v[212:215], v[136:143], v251\n\t" \
  "s_waitcnt lgkmcnt(5)\n\t" \
  "v_smfmac_f32_16x16x64_bf16 v[192:195], v[236:239], v[216:223], v251\n\t" \
  "s_waitcnt lgkmcnt(3)\n\t" \
  "ds_read_b128 v[204:207], v248 offset:9216\n\t" \
  "v_smfmac_f32_16x16x64_bf16 v[196:199], v[236:239], v[224:231], v251\n\t" \
  "s_waitcnt lgkmcnt(3)\n\t" \
  "ds_read_b128 v[216:219], v250 offset:12288\n\t" \
  "v_smfmac_f32_16x16x64_bf16 v[192:195], v[240:243], v[144:151], v251\n\t" \
  "ds_read_b128 v[220:223], v250 offset:14336\n\t" \
  "v_smfmac_f32_16x16x64_bf16 v[196:199], v[240:243], v[152:159], v251\n\t" \
  "s_waitcnt lgkmcnt(4)\n\t" \
  "ds_read_b128 v[224:227], v250 offset:13312\n\t" \
  "v_smfmac_f32_16x16x64_bf16 v[192:195], v[244:247], v[160:167], v251\n\t" \
  "ds_read_b128 v[228:231], v250 offset:15360\n\t" \
  "v_smfmac_f32_16x16x64_bf16 v[196:199], v[244:247], v[168:175], v251\n\t" \
  "s_waitcnt lgkmcnt(5)\n\t" \
  "v_smfmac_f32_16x16x64_bf16 v[192:195], v[200:203], v[176:183], v251\n\t" \
  "v_smfmac_f32_16x16x64_bf16 v[196:199], v[200:203], v[184:191], v251\n\t" \
  "s_waitcnt lgkmcnt(2)\n\t" \
  "v_smfmac_f32_16x16x64_bf16 v[192:195], v[204:207], v[216:223], v251\n\t" \
  "s_waitcnt lgkmcnt(0)\n\t" \
  "v_smfmac_f32_16x16x64_bf16 v[196:199], v[204:207], v[224:231], v251\n\t" \
  "s_nop 6\n\t" \
  "v_add_f32 v192, v192, v194\n\t" \
  "v_add_f32 v193, v193, v195\n\t" \
  "v_add_f32 v196, v196, v198\n\t" \
  "v_add_f32 v197, v197, v199\n\t" \
  "v_cvt_pk_bf16_f32 v192, v192, v193\n\t" \
  "v_cvt_pk_bf16_f32 v196, v196, v197\n\t" \
  "ds_write2_b32 v252, v192, v196 offset1:64\n\t" \
  "s_waitcnt lgkmcnt(0)\n\t" \
  "s_barrier\n\t" \
  "buffer_store_dword v192, v235, %[rs], %[sk] offen sc1\n\t" \
  "buffer_store_dword v196, v235, %[rs], %[sk] offen offset:256 sc1\n\t" \
  "s_mov_b32 %[spins], s92\n\t" \
  "s_branch .Lend%=\n\t" \
  ".Lslow%=:\n\t" \
  "s_mov_b64 s[94:95], 0\n\t" \
  "v_cmp_eq_u32 vcc, %[sent], v200\n\t" \
  "s_or_b64 s[94:95], s[94:95], vcc\n\t" \
  "v_cmp_eq_u32 vcc, %[sent], v201\n\t" \
  "s_or_b64 s[94:95], s[94:95], vcc\n\t" \
  "v_cmp_eq_u32 vcc, %[sent], v202\n\t" \
  "s_or_b64 s[94:95], s[94:95], vcc\n\t" \
  "v_cmp_eq_u32 vcc, %[sent], v203\n\t" \
  "s_or_b64 s[94:95], s[94:95], vcc\n\t" \
  "v_cmp_eq_u32 vcc, %[sent], v204\n\t" \
  "s_or_b64 s[94:95], s[94:95], vcc\n\t" \
  "v_cmp_eq_u32 vcc, %[sent], v205\n\t" \
  "s_or_b64 s[94:95], s[94:95], vcc\n\t" \
  "v_cmp_eq_u32 vcc, %[sent], v206\n\t" \
  "s_or_b64 s[94:95], s[94:95], vcc\n\t" \
  "v_cmp_eq_u32 vcc, %[sent], v207\n\t" \
  "s_or_b64 s[94:95], s[94:95], vcc\n\t" \
  "s_cmp_eq_u64 s[94:95], 0\n\t" \
  "s_cbranch_scc1 .Lok%=\n\t" \
  "s_add_u32 s92, s92, 1\n\t" \
  "s_cmp_eq_u32 s92, 1\n\t" \
  "s_cbranch_scc1 .Lpoll%=\n\t" \
  "s_sleep 6\n\t" \
  "s_and_b32 s93, s92, 63\n\t" \
  "s_cmp_lg_u32 s93, 0\n\t" \
  "s_cbranch_scc1 .Lpoll%=\n\t" \
  "v_mov_b32 v209, 0\n\t" \
  "buffer_load_dword v208, v209, %[rs], %[sab] offen sc1\n\t" \
  "s_waitcnt vmcnt(0)\n\t" \
  "v_readfirstlane_b32 s93, v208\n\t" \
  "s_nop 3\n\t" \
  "s_cmp_eq_u32 s93, 1\n\t" \
  "s_cbranch_scc1 .Lok%=\n\t" \
  "s_cmp_lt_u32 s92, 0x80000\n\t" \
  "s_cbranch_scc1 .Lpoll%=\n\t" \
  "v_mov_b32 v208, 1\n\t" \
  "buffer_store_dword v208, v209, %[rs], %[sab] offen sc1\n\t" \
  "s_branch .Lok%=\n\t" \
  ".Lend%=:\n\t"
#define QK_ASM_6 \
  "s_waitcnt lgkmcnt(0)\n\t" \
  "ds_read_b128 v[216:219], v250 offset:0\n\t" \
  "ds_read_b128 v[220:223], v250 offset:2048\n\t" \
  "ds_read_b128 v[224:227], v250 offset:1024\n\t" \
  "ds_read_b128 v[228:231], v250 offset:3072\n\t" \
  "v_mov_b64 v[192:193], 0\n\t" \
  "v_mov_b64 v[194:195], 0\n\t" \
  "v_mov_b64 v[196:197], 0\n\t" \
  "v_mov_b64 v[198:199], 0\n\t" \
  "s_memrealtime s[90:91]\n\t" \
  "s_waitcnt lgkmcnt(0)\n\t" \
  "s_lshl_b32 s93, s90, 2\n\t" \
  "s_sub_i32 s93, %[tgt], s93\n\t" \
  "s_cmp_le_i32 s93, 0\n\t" \
  "s_cbranch_scc1 .Lgo%=\n\t" \
  "s_min_i32 s93, s93, 8000\n\t" \
  "s_add_u32 s93, s93, 3\n\t" \
  "s_lshr_b32 s93, s93, 2\n\t" \
  "s_add_u32 s93, s93, s90\n\t" \
  "s_memrealtime s[90:91]\n\t" \
  ".Lpace%=:\n\t" \
  "s_memrealtime s[94:95]\n\t" \
  "s_waitcnt lgkmcnt(1)\n\t" \
  "s_sub_i32 s91, s90, s93\n\t" \
  "s_cmp_ge_i32 s91, 0\n\t" \
  "s_cbranch_scc1 .Lgo%=\n\t" \
  "s_memrealtime s[90:91]\n\t" \
  "s_waitcnt lgkmcnt(1)\n\t" \
  "s_sub_i32 s95, s94, s93\n\t" \
  "s_cmp_lt_i32 s95, 0\n\t" \
  "s_cbranch_scc1 .Lpace%=\n\t" \
  ".Lgo%=:\n\t" \
  "s_mov_b32 s92, 0\n\t" \
  ".Lpoll%=:\n\t" \
  "buffer_load_dwordx4 v[200:203], v248, %[rs], %[som] offen sc1\n\t" \
  "buffer_load_dwordx4 v[204:207], v248, %[rs], %[som] offen offset:1024 sc1\n\t" \
  "s_waitcnt vmcnt(1) lgkmcnt(0)\n\t" \
  "ds_write_b128 v249, v[200:203]\n\t" \
  "v_max3_u32 v208, v200, v201, v202\n\t" \
  "s_waitcnt vmcnt(0)\n\t" \
  "ds_write_b128 v249, v[204:207] offset:1024\n\t" \
  "v_max3_u32 v209, v204, v205, v206\n\t" \
  "v_max3_u32 v208, v208, v203, v207\n\t" \
  "v_max_u32 v208, v208, v209\n\t" \
  "v_cmp_le_u32 vcc, %[sent], v208\n\t" \
  "s_cbranch_vccnz .Lslow%=\n\t" \
  ".Lok%=:\n\t" \
  "v_smfmac_f32_16x16x64_bf16 v[192:195], v[200:203], v[0:7], v251\n\t" \
  "v_smfmac_f32_16x16x64_bf16 v[196:199], v[200:203], v[8:15], v251\n\t" \
  "s_waitcnt lgkmcnt(0)\n\t" \
  "s_barrier\n\t" \
  "ds_read_b128 v[208:211], v248 offset:14336\n\t" \
  "ds_read_b128 v[212:215], v248 offset:15360\n\t" \
  "ds_read_b128 v[236:239], v248 offset:0\n\t" \
  "ds_read_b128 v[240:243], v248 offset:1024\n\t" \
  "ds_read_b128 v[244:247], v248 offset:2048\n\t" \
  "ds_read_b128 v[200:203], v248 offset:3072\n\t" \
  "v_smfmac_f32_16x16x64_bf16 v[192:195], v[204:207], v[16:23], v251\n\t" \
  "v_smfmac_f32_16x16x64_bf16 v[196:199], v[204:207], v[24:31], v251\n\t" \
  "s_waitcnt lgkmcnt(5)\n\t" \
  "v_smfmac_f32_16x16x64_bf16 v[192:195], v[208:211], v[32:39], v251\n\t" \
  "ds_read_b128 v[204:207], v248 offset:4096\n\t" \
  "v_smfmac_f32_16x16x64_bf16 v[196:199], v[208:211], v[40:47], v251\n\t" \
  "s_waitcnt lgkmcnt(5)\n\t" \
  "v_smfmac_f32_16x16x64_bf16 v[192:195], v[212:215], v[216:223], v251\n\t" \
  "ds_read_b128 v[216:219], v250 offset:4096\n\t" \
  "v_smfmac_f32_16x16x64_bf16 v[196:199], v[212:215], v[224:231], v251\n\t" \
  "s_waitcnt lgkmcnt(5)\n\t" \
  "ds_read_b128 v[220:223], v250 offset:6144\n\t" \
  "v_smfmac_f32_16x16x64_bf16 v[192:195], v[236:239], v[48:55], v251\n\t" \
  "ds_read_b128 v[224:227], v250 offset:5120\n\t" \
  "v_smfmac_f32_16x16x64_bf16 v[196:199], v[236:239], v[56:63], v251\n\t" \
  "s_waitcnt lgkmcnt(6)\n\t" \
  "ds_read_b128 v[228:231], v250 offset:7168\n\t" \
  "v_smfmac_f32_16x16x64_bf16 v[192:195], v[240:243], v[64:71], v251\n\t" \
  "ds_read_b128 v[208:211], v248 offset:5120\n\t" \
  "v_smfmac_f32_16x16x64_bf16 v[196:199], v[240:243], v[72:79], v251\n\t" \
  "s_waitcnt lgkmcnt(5)\n\t" \
  "ds_read_b128 v[212:215], v248 offset:6144\n\t" \
  "v_smfmac_f32_16x16x64_bf16 v[192:195], v[244:247], v[80:87], v251\n\t" \
  "ds_read_b128 v[236:239], v248 offset:7168\n\t" \
  "v_smfmac_f32_16x16x64_bf16 v[196:199], v[244:247], v[88:95], v251\n\t" \
  "s_waitcnt lgkmcnt(5)\n\t" \
  "v_smfmac_f32_16x16x64_bf16 v[192:195], v[200:203], v[216:223], v251\n\t" \
  "s_waitcnt lgkmcnt(3)\n\t" \
  "ds_read_b128 v[216:219], v250 offset:8192\n\t" \
  "v_smfmac_f32_16x16x64_bf16 v[196:199], v[200:203], v[224:231], v251\n\t" \
  "ds_read_b128 v[220:223], v250 offset:10240\n\t" \
  "v_smfmac_f32_16x16x64_bf16 v[192:195], v[204:207], v[96:103], v251\n\t" \
  "ds_read_b128 v[224:227], v250 offset:9216\n\t" \
  "v_smfmac_f32_16x16x64_bf16 v[196:199], v[204:207], v[104:111], v251\n\t" \
  "s_waitcnt lgkmcnt(5)\n\t" \
  "ds_read_b128 v[228:231], v250 offset:11264\n\t" \
  "v_smfmac_f32_16x16x64_bf16 v[192:195], v[208:211], v[112:119], v251\n\t" \
  "ds_read_b128 v[240:243], v248 offset:8192\n\t" \
  "v_smfmac_f32_16x16x64_bf16 v[196:199], v[208:211], v[120:127], v251\n\t" \
  "s_waitcnt lgkmcnt(5)\n\t" \
  "ds_read_b128 v[244:247], v248 offset:9216\n\t" \
  "v_smfmac_f32_16x16x64_bf16 v[192:195], v[212:215], v[128:135], v251\n\t" \
  "ds_read_b128 v[200:203], v248 offset:10240\n\t" \
  "v_smfmac_f32_16x16x64_bf16 v[196:199], v[212:215], v[136:143], v251\n\t" \
  "s_waitcnt lgkmcnt(5)\n\t" \
  "v_smfmac_f32_16x16x64_bf16 v[192:195], v[236:239], v[216:223], v251\n\t" \
  "s_waitcnt lgkmcnt(3)\n\t" \
  "ds_read_b128 v[204:207], v248 offset:11264\n\t" \
  "v_smfmac_f32_16x16x64_bf16 v[196:199], v[236:239], v[224:231], v251\n\t" \
  "s_waitcnt lgkmcnt(3)\n\t" \
  "ds_read_b128 v[216:219], v250 offset:12288\n\t" \
  "v_smfmac_f32_16x16x64_bf16 v[192:195], v[240:243], v[144:151], v251\n\t" \
  "ds_read_b128 v[220:223], v250 offset:14336\n\t" \
  "v_smfmac_f32_16x16x64_bf16 v[196:199], v[240:243], v[152:159], v251\n\t" \
  "s_waitcnt lgkmcnt(4)\n\t" \
  "ds_read_b128 v[224:227], v250 offset:13312\n\t" \
  "v_smfmac_f32_16x16x64_bf16 v[192:195], v[244:247], v[160:167], v251\n\t" \
  "ds_read_b128 v[228:231], v250 offset:15360\n\t" \
  "v_smfmac_f32_16x16x64_bf16 v[196:199], v[244:247], v[168:175], v251\n\t" \
  "s_waitcnt lgkmcnt(5)\n\t" \
  "v_smfmac_f32_16x16x64_bf16 v[192:195], v[200:203], v[176:183], v251\n\t" \
  "v_smfmac_f32_16x16x64_bf16 v[196:199], v[200:203], v[184:191], v251\n\t" \
  "s_waitcnt lgkmcnt(2)\n\t" \
  "v_smfmac_f32_16x16x64_bf16 v[192:195], v[204:207], v[216:223], v251\n\t" \
  "s_waitcnt lgkmcnt(0)\n\t" \
  "v_smfmac_f32_16x16x64_bf16 v[196:199], v[204:207], v[224:231], v251\n\t" \
  "s_nop 6\n\t" \
  "v_add_f32 v192, v192, v194\n\t" \
  "v_add_f32 v193, v193, v195\n\t" \
  "v_add_f32 v196, v196, v198\n\t" \
  "v_add_f32 v197, v197, v199\n\t" \
  "v_cvt_pk_bf16_f32 v192, v192, v193\n\t" \
  "v_cvt_pk_bf16_f32 v196, v196, v197\n\t" \
  "ds_write2_b32 v252, v192, v196 offset1:64\n\t" \
  "s_waitcnt lgkmcnt(0)\n\t" \
  "s_barrier\n\t" \
  "buffer_store_dword v192, v235, %[rs], %[sk] offen sc1\n\t" \
  "buffer_store_dword v196, v235, %[rs], %[sk] offen offset:256 sc1\n\t" \
  "s_mov_b32 %[spins], s92\n\t" \
  "s_branch .Lend%=\n\t" \
  ".Lslow%=:\n\t" \
  "s_mov_b64 s[94:95], 0\n\t" \
  "v_cmp_eq_u32 vcc, %[sent], v200\n\t" \
  "s_or_b64 s[94:95], s[94:95], vcc\n\t" \
  "v_cmp_eq_u32 vcc, %[sent], v201\n\t" \
  "s_or_b64 s[94:95], s[94:95], vcc\n\t" \
  "v_cmp_eq_u32 vcc, %[sent], v202\n\t" \
  "s_or_b64 s[94:95], s[94:95], vcc\n\t" \
  "v_cmp_eq_u32 vcc, %[sent], v203\n\t" \
  "s_or_b64 s[94:95], s[94:95], vcc\n\t" \
  "v_cmp_eq_u32 vcc, %[sent], v204\n\t" \
  "s_or_b64 s[94:95], s[94:95], vcc\n\t" \
  "v_cmp_eq_u32 vcc, %[sent], v205\n\t" \
  "s_or_b64 s[94:95], s[94:95], vcc\n\t" \
  "v_cmp_eq_u32 vcc, %[sent], v206\n\t" \
  "s_or_b64 s[94:95], s[94:95], vcc\n\t" \
  "v_cmp_eq_u32 vcc, %[sent], v207\n\t" \
  "s_or_b64 s[94:95], s[94:95], vcc\n\t" \
  "s_cmp_eq_u64 s[94:95], 0\n\t" \
  "s_cbranch_scc1 .Lok%=\n\t" \
  "s_add_u32 s92, s92, 1\n\t" \
  "s_cmp_eq_u32 s92, 1\n\t" \
  "s_cbranch_scc1 .Lpoll%=\n\t" \
  "s_sleep 6\n\t" \
  "s_and_b32 s93, s92, 63\n\t" \
  "s_cmp_lg_u32 s93, 0\n\t" \
  "s_cbranch_scc1 .Lpoll%=\n\t" \
  "v_mov_b32 v209, 0\n\t" \
  "buffer_load_dword v208, v209, %[rs], %[sab] offen sc1\n\t" \
  "s_waitcnt vmcnt(0)\n\t" \
  "v_readfirstlane_b32 s93, v208\n\t" \
  "s_nop 3\n\t" \
  "s_cmp_eq_u32 s93, 1\n\t" \
  "s_cbranch_scc1 .Lok%=\n\t" \
  "s_cmp_lt_u32 s92, 0x80000\n\t" \
  "s_cbranch_scc1 .Lpoll%=\n\t" \
  "v_mov_b32 v208, 1\n\t" \
  "buffer_store_dword v208, v209, %[rs], %[sab] offen sc1\n\t" \
  "s_branch .Lok%=\n\t" \
  ".Lend%=:\n\t"
#define QK_ASM_7 \
  "s_waitcnt lgkmcnt(0)\n\t" \
  "ds_read_b128 v[216:219], v250 offset:0\n\t" \
  "ds_read_b128 v[220:223], v250 offset:2048\n\t" \
  "ds_read_b128 v[224:227], v250 offset:1024\n\t" \
  "ds_read_b128 v[228:231], v250 offset:3072\n\t" \
  "v_mov_b64 v[192:193], 0\n\t" \
  "v_mov_b64 v[194:195], 0\n\t" \
  "v_mov_b64 v[196:197], 0\n\t" \
  "v_mov_b64 v[198:199], 0\n\t" \
  "s_memrealtime s[90:91]\n\t" \
  "s_waitcnt lgkmcnt(0)\n\t" \
  "s_lshl_b32 s93, s90, 2\n\t" \
  "s_sub_i32 s93, %[tgt], s93\n\t" \
  "s_cmp_le_i32 s93, 0\n\t" \
  "s_cbranch_scc1 .Lgo%=\n\t" \
  "s_min_i32 s93, s93, 8000\n\t" \
  "s_add_u32 s93, s93, 3\n\t" \
  "s_lshr_b32 s93, s93, 2\n\t" \
  "s_add_u32 s93, s93, s90\n\t" \
  "s_memrealtime s[90:91]\n\t" \
  ".Lpace%=:\n\t" \
  "s_memrealtime s[94:95]\n\t" \
  "s_waitcnt lgkmcnt(1)\n\t" \
  "s_sub_i32 s91, s90, s93\n\t" \
  "s_cmp_ge_i32 s91, 0\n\t" \
  "s_cbranch_scc1 .Lgo%=\n\t" \
  "s_memrealtime s[90:91]\n\t" \
  "s_waitcnt lgkmcnt(1)\n\t" \
  "s_sub_i32 s95, s94, s93\n\t" \
  "s_cmp_lt_i32 s95, 0\n\t" \
  "s_cbranch_scc1 .Lpace%=\n\t" \
  ".Lgo%=:\n\t" \
  "s_mov_b32 s92, 0\n\t" \
  ".Lpoll%=:\n\t" \
  "buffer_load_dwordx4 v[200:203], v248, %[rs], %[som] offen sc1\n\t" \
  "buffer_load_dwordx4 v[204:207], v248, %[rs], %[som] offen offset:1024 sc1\n\t" \
  "s_waitcnt vmcnt(1) lgkmcnt(0)\n\t" \
  "ds_write_b128 v249, v[200:203]\n\t" \
  "v_max3_u32 v208, v200, v201, v202\n\t" \
  "s_waitcnt vmcnt(0)\n\t" \
  "ds_write_b128 v249, v[204:207] offset:1024\n\t" \
  "v_max3_u32 v209, v204, v205, v206\n\t" \
  "v_max3_u32 v208, v208, v203, v207\n\t" \
  "v_max_u32 v208, v208, v209\n\t" \
  "v_cmp_le_u32 vcc, %[sent], v208\n\t" \
  "s_cbranch_vccnz .Lslow%=\n\t" \
  ".Lok%=:\n\t" \
  "v_smfmac_f32_16x16x64_bf16 v[192:195], v[200:203], v[0:7], v251\n\t" \
  "v_smfmac_f32_16x16x64_bf16 v[196:199], v[200:203], v[8:15], v251\n\t" \
  "s_waitcnt lgkmcnt(0)\n\t" \
  "s_barrier\n\t" \
  "ds_read_b128 v[208:211], v248 offset:0\n\t" \
  "ds_read_b128 v[212:215], v248 offset:1024\n\t" \
  "ds_read_b128 v[236:239], v248 offset:2048\n\t" \
  "ds_read_b128 v[240:243], v248 offset:3072\n\t" \
  "ds_read_b128 v[244:247], v248 offset:4096\n\t" \
  "ds_read_b128 v[200:203], v248 offset:5120\n\t" \
  "v_smfmac_f32_16x16x64_bf16 v[192:195], v[204:207], v[16:23], v251\n\t" \
  "v_smfmac_f32_16x16x64_bf16 v[196:199], v[204:207], v[24:31], v251\n\t" \
  "s_waitcnt lgkmcnt(5)\n\t" \
  "v_smfmac_f32_16x16x64_bf16 v[192:195], v[208:211], v[32:39], v251\n\t" \
  "ds_read_b128 v[204:207], v248 offset:6144\n\t" \
  "v_smfmac_f32_16x16x64_bf16 v[196:199], v[208:211], v[40:47], v251\n\t" \
  "s_waitcnt lgkmcnt(5)\n\t" \
  "v_smfmac_f32_16x16x64_bf16 v[192:195], v[212:215], v[216:223], v251\n\t" \
  "ds_read_b128 v[216:219], v250 offset:4096\n\t" \
  "v_smfmac_f32_16x16x64_bf16 v[196:199], v[212:215], v[224:231], v251\n\t" \
  "s_waitcnt lgkmcnt(5)\n\t" \
  "ds_read_b128 v[220:223], v250 offset:6144\n\t" \
  "v_smfmac_f32_16x16x64_bf16 v[192:195], v[236:239], v[48:55], v251\n\t" \
  "ds_read_b128 v[224:227], v250 offset:5120\n\t" \
  "v_smfmac_f32_16x16x64_bf16 v[196:199], v[236:239], v[56:63], v251\n\t" \
  "s_waitcnt lgkmcnt(6)\n\t" \
  "ds_read_b128 v[228:231], v250 offset:7168\n\t" \
  "v_smfmac_f32_16x16x64_bf16 v[192:195], v[240:243], v[64:71], v251\n\t" \
  "ds_read_b128 v[208:211], v248 offset:7168\n\t" \
  "v_smfmac_f32_16x16x64_bf16 v[196:199], v[240:243], v[72:79], v251\n\t" \
  "s_waitcnt lgkmcnt(5)\n\t" \
  "ds_read_b128 v[212:215], v248 offset:8192\n\t" \
  "v_smfmac_f32_16x16x64_bf16 v[192:195], v[244:247], v[80:87], v251\n\t" \
  "ds_read_b128 v[236:239], v248 offset:9216\n\t" \
  "v_smfmac_f32_16x16x64_bf16 v[196:199], v[244:247], v[88:95], v251\n\t" \
  "s_waitcnt lgkmcnt(5)\n\t" \
  "v_smfmac_f32_16x16x64_bf16 v[192:195], v[200:203], v[216:223], v251\n\t" \
  "s_waitcnt lgkmcnt(3)\n\t" \
  "ds_read_b128 v[216:219], v250 offset:8192\n\t" \
  "v_smfmac_f32_16x16x64_bf16 v[196:199], v[200:203], v[224:231], v251\n\t" \
  "ds_read_b128 v[220:223], v250 offset:10240\n\t" \
  "v_smfmac_f32_16x16x64_bf16 v[192:195], v[204:207], v[96:103], v251\n\t" \
  "ds_read_b128 v[224:227], v250 offset:9216\n\t" \
  "v_smfmac_f32_16x16x64_bf16 v[196:199], v[204:207], v[104:111], v251\n\t" \
  "s_waitcnt lgkmcnt(5)\n\t" \
  "ds_read_b128 v[228:231], v250 offset:11264\n\t" \
  "v_smfmac_f32_16x16x64_bf16 v[192:195], v[208:211], v[112:119], v251\n\t" \
  "ds_read_b128 v[240:243], v248 offset:10240\n\t" \
  "v_smfmac_f32_16x16x64_bf16 v[196:199], v[208:211], v[120:127], v251\n\t" \
  "s_waitcnt lgkmcnt(5)\n\t" \
  "ds_read_b128 v[244:247], v248 offset:11264\n\t" \
  "v_smfmac_f32_16x16x64_bf16 v[192:195], v[212:215], v[128:135], v251\n\t" \
  "ds_read_b128 v[200:203], v248 offset:12288\n\t" \
  "v_smfmac_f32_16x16x64_bf16 v[196:199], v[212:215], v[136:143], v251\n\t" \
  "s_waitcnt lgkmcnt(5)\n\t" \
  "v_smfmac_f32_16x16x64_bf16 v[192:195], v[236:239], v[216:223], v251\n\t" \
  "s_waitcnt lgkmcnt(3)\n\t" \
  "ds_read_b128 v[204:207], v248 offset:13312\n\t" \
  "v_smfmac_f32_16x16x64_bf16 v[196:199], v[236:239], v[224:231], v251\n\t" \
  "s_waitcnt lgkmcnt(3)\n\t" \
  "ds_read_b128 v[216:219], v250 offset:12288\n\t" \
  "v_smfmac_f32_16x16x64_bf16 v[192:195], v[240:243], v[144:151], v251\n\t" \
  "ds_read_b128 v[220:223], v250 offset:14336\n\t" \
  "v_smfmac_f32_16x16x64_bf16 v[196:199], v[240:243], v[152:159], v251\n\t" \
  "s_waitcnt lgkmcnt(4)\n\t" \
  "ds_read_b128 v[224:227], v250 offset:13312\n\t" \
  "v_smfmac_f32_16x16x64_bf16 v[192:195], v[244:247], v[160:167], v251\n\t" \
  "ds_read_b128 v[228:231], v250 offset:15360\n\t" \
  "v_smfmac_f32_16x16x64_bf16 v[196:199], v[244:247], v[168:175], v251\n\t" \
  "s_waitcnt lgkmcnt(5)\n\t" \
  "v_smfmac_f32_16x16x64_bf16 v[192:195], v[200:203], v[176:183], v251\n\t" \
  "v_smfmac_f32_16x16x64_bf16 v[196:199], v[200:203], v[184:191], v251\n\t" \
  "s_waitcnt lgkmcnt(2)\n\t" \
  "v_smfmac_f32_16x16x64_bf16 v[192:195], v[204:207], v[216:223], v251\n\t" \
  "s_waitcnt lgkmcnt(0)\n\t" \
  "v_smfmac_f32_16x16x64_bf16 v[196:199], v[204:207], v[224:231], v251\n\t" \
  "s_nop 6\n\t" \
  "v_add_f32 v192, v192, v194\n\t" \
  "v_add_f32 v193, v193, v195\n\t" \
  "v_add_f32 v196, v196, v198\n\t" \
  "v_add_f32 v197, v197, v199\n\t" \
  "v_cvt_pk_bf16_f32 v192, v192, v193\n\t" \
  "v_cvt_pk_bf16_f32 v196, v196, v197\n\t" \
  "ds_write2_b32 v252, v192, v196 offset1:64\n\t" \
  "s_waitcnt lgkmcnt(0)\n\t" \
  "s_barrier\n\t" \
  "buffer_store_dword v192, v235, %[rs], %[sk] offen sc1\n\t" \
  "buffer_store_dword v196, v235, %[rs], %[sk] offen offset:256 sc1\n\t" \
  "s_mov_b32 %[spins], s92\n\t" \
  "s_branch .Lend%=\n\t" \
  ".Lslow%=:\n\t" \
  "s_mov_b64 s[94:95], 0\n\t" \
  "v_cmp_eq_u32 vcc, %[sent], v200\n\t" \
  "s_or_b64 s[94:95], s[94:95], vcc\n\t" \
  "v_cmp_eq_u32 vcc, %[sent], v201\n\t" \
  "s_or_b64 s[94:95], s[94:95], vcc\n\t" \
  "v_cmp_eq_u32 vcc, %[sent], v202\n\t" \
  "s_or_b64 s[94:95], s[94:95], vcc\n\t" \
  "v_cmp_eq_u32 vcc, %[sent], v203\n\t" \
  "s_or_b64 s[94:95], s[94:95], vcc\n\t" \
  "v_cmp_eq_u32 vcc, %[sent], v204\n\t" \
  "s_or_b64 s[94:95], s[94:95], vcc\n\t" \
  "v_cmp_eq_u32 vcc, %[sent], v205\n\t" \
  "s_or_b64 s[94:95], s[94:95], vcc\n\t" \
  "v_cmp_eq_u32 vcc, %[sent], v206\n\t" \
  "s_or_b64 s[94:95], s[94:95], vcc\n\t" \
  "v_cmp_eq_u32 vcc, %[sent], v207\n\t" \
  "s_or_b64 s[94:95], s[94:95], vcc\n\t" \
  "s_cmp_eq_u64 s[94:95], 0\n\t" \
  "s_cbranch_scc1 .Lok%=\n\t" \
  "s_add_u32 s92, s92, 1\n\t" \
  "s_cmp_eq_u32 s92, 1\n\t" \
  "s_cbranch_scc1 .Lpoll%=\n\t" \
  "s_sleep 6\n\t" \
  "s_and_b32 s93, s92, 63\n\t" \
  "s_cmp_lg_u32 s93, 0\n\t" \
  "s_cbranch_scc1 .Lpoll%=\n\t" \
  "v_mov_b32 v209, 0\n\t" \
  "buffer_load_dword v208, v209, %[rs], %[sab] offen sc1\n\t" \
  "s_waitcnt vmcnt(0)\n\t" \
  "v_readfirstlane_b32 s93, v208\n\t" \
  "s_nop 3\n\t" \
  "s_cmp_eq_u32 s93, 1\n\t" \
  "s_cbranch_scc1 .Lok%=\n\t" \
  "s_cmp_lt_u32 s92, 0x80000\n\t" \
  "s_cbranch_scc1 .Lpoll%=\n\t" \
  "v_mov_b32 v208, 1\n\t" \
  "buffer_store_dword v208, v209, %[rs], %[sab] offen sc1\n\t" \
  "s_branch .Lok%=\n\t" \
  ".Lend%=:\n\t"
#define VV_ASM \
  "s_waitcnt lgkmcnt(0)\n\t" \
  "v_mov_b64 v[128:129], 0\n\t" \
  "v_mov_b64 v[130:131], 0\n\t" \
  "s_memrealtime s[90:91]\n\t" \
  "s_waitcnt lgkmcnt(0)\n\t" \
  "s_lshl_b32 s93, s90, 2\n\t" \
  "s_sub_i32 s93, %[tgt], s93\n\t" \
  "s_cmp_le_i32 s93, 0\n\t" \
  "s_cbranch_scc1 .Lgo%=\n\t" \
  "s_min_i32 s93, s93, 8000\n\t" \
  "s_add_u32 s93, s93, 3\n\t" \
  "s_lshr_b32 s93, s93, 2\n\t" \
  "s_add_u32 s93, s93, s90\n\t" \
  "s_memrealtime s[90:91]\n\t" \
  ".Lpace%=:\n\t" \
  "s_memrealtime s[94:95]\n\t" \
  "s_waitcnt lgkmcnt(1)\n\t" \
  "s_sub_i32 s91, s90, s93\n\t" \
  "s_cmp_ge_i32 s91, 0\n\t" \
  "s_cbranch_scc1 .Lgo%=\n\t" \
  "s_memrealtime s[90:91]\n\t" \
  "s_waitcnt lgkmcnt(1)\n\t" \
  "s_sub_i32 s95, s94, s93\n\t" \
  "s_cmp_lt_i32 s95, 0\n\t" \
  "s_cbranch_scc1 .Lpace%=\n\t" \
  ".Lgo%=:\n\t" \
  "s_mov_b32 s92, 0\n\t" \
  ".Lpoll%=:\n\t" \
  "buffer_load_dwordx4 v[188:191], v200, %[rs], %[som] offen sc1\n\t" \
  "buffer_load_dwordx4 v[192:195], v200, %[rs], %[som] offen offset:1024 sc1\n\t" \
  "s_waitcnt vmcnt(1) lgkmcnt(0)\n\t" \
  "ds_write_b128 v201, v[188:191]\n\t" \
  "v_max3_u32 v196, v188, v189, v190\n\t" \
  "s_waitcnt vmcnt(0)\n\t" \
  "ds_write_b128 v201, v[192:195] offset:1024\n\t" \
  "v_max3_u32 v197, v192, v193, v194\n\t" \
  "v_max3_u32 v196, v196, v191, v195\n\t" \
  "v_max_u32 v196, v196, v197\n\t" \
  "v_cmp_le_u32 vcc, %[sent], v196\n\t" \
  "s_cbranch_vccnz .Lslow%=\n\t" \
  ".Lok%=:\n\t" \
  "s_waitcnt lgkmcnt(0)\n\t" \
  "s_barrier\n\t" \
  "ds_read_b128 v[132:135], v200 offset:0\n\t" \
  "ds_read_b128 v[136:139], v200 offset:1024\n\t" \
  "ds_read_b128 v[140:143], v200 offset:2048\n\t" \
  "ds_read_b128 v[144:147], v200 offset:3072\n\t" \
  "ds_read_b128 v[148:151], v200 offset:4096\n\t" \
  "s_waitcnt lgkmcnt(4)\n\t" \
  "ds_read_b128 v[152:155], v200 offset:5120\n\t" \
  "v_smfmac_f32_16x16x64_bf16 v[128:131], v[132:135], v[0:7], v202\n\t" \
  "s_waitcnt lgkmcnt(4)\n\t" \
  "ds_read_b128 v[156:159], v200 offset:6144\n\t" \
  "v_smfmac_f32_16x16x64_bf16 v[128:131], v[136:139], v[8:15], v202\n\t" \
  "s_waitcnt lgkmcnt(4)\n\t" \
  "ds_read_b128 v[160:163], v200 offset:7168\n\t" \
  "v_smfmac_f32_16x16x64_bf16 v[128:131], v[140:143], v[16:23], v202\n\t" \
  "s_waitcnt lgkmcnt(4)\n\t" \
  "ds_read_b128 v[164:167], v200 offset:8192\n\t" \
  "v_smfmac_f32_16x16x64_bf16 v[128:131], v[144:147], v[24:31], v202\n\t" \
  "s_waitcnt lgkmcnt(4)\n\t" \
  "ds_read_b128 v[168:171], v200 offset:9216\n\t" \
  "v_smfmac_f32_16x16x64_bf16 v[128:131], v[148:151], v[32:39], v202\n\t" \
  "s_waitcnt lgkmcnt(4)\n\t" \
  "ds_read_b128 v[172:175], v200 offset:10240\n\t" \
  "v_smfmac_f32_16x16x64_bf16 v[128:131], v[152:155], v[40:47], v202\n\t" \
  "s_waitcnt lgkmcnt(4)\n\t" \
  "ds_read_b128 v[176:179], v200 offset:11264\n\t" \
  "v_smfmac_f32_16x16x64_bf16 v[128:131], v[156:159], v[48:55], v202\n\t" \
  "s_waitcnt lgkmcnt(4)\n\t" \
  "ds_read_b128 v[180:183], v200 offset:12288\n\t" \
  "v_smfmac_f32_16x16x64_bf16 v[128:131], v[160:163], v[56:63], v202\n\t" \
  "s_waitcnt lgkmcnt(4)\n\t" \
  "ds_read_b128 v[184:187], v200 offset:13312\n\t" \
  "v_smfmac_f32_16x16x64_bf16 v[128:131], v[164:167], v[64:71], v202\n\t" \
  "s_waitcnt lgkmcnt(4)\n\t" \
  "ds_read_b128 v[188:191], v200 offset:14336\n\t" \
  "v_smfmac_f32_16x16x64_bf16 v[128:131], v[168:171], v[72:79], v202\n\t" \
  "s_waitcnt lgkmcnt(4)\n\t" \
  "ds_read_b128 v[192:195], v200 offset:15360\n\t" \
  "v_smfmac_f32_16x16x64_bf16 v[128:131], v[172:175], v[80:87], v202\n\t" \
  "s_waitcnt lgkmcnt(4)\n\t" \
  "v_smfmac_f32_16x16x64_bf16 v[128:131], v[176:179], v[88:95], v202\n\t" \
  "s_waitcnt lgkmcnt(3)\n\t" \
  "v_smfmac_f32_16x16x64_bf16 v[128:131], v[180:183], v[96:103], v202\n\t" \
  "s_waitcnt lgkmcnt(2)\n\t" \
  "v_smfmac_f32_16x16x64_bf16 v[128:131], v[184:187], v[104:111], v202\n\t" \
  "s_waitcnt lgkmcnt(1)\n\t" \
  "v_smfmac_f32_16x16x64_bf16 v[128:131], v[188:191], v[112:119], v202\n\t" \
  "s_waitcnt lgkmcnt(0)\n\t" \
  "v_smfmac_f32_16x16x64_bf16 v[128:131], v[192:195], v[120:127], v202\n\t" \
  "s_nop 7\n\t" \
  "v_add_f32 v128, v128, v130\n\t" \
  "v_add_f32 v129, v129, v131\n\t" \
  "v_cvt_pk_bf16_f32 v128, v128, v129\n\t" \
  "buffer_store_dword v128, v203, %[rs], %[sv] offen sc1\n\t" \
  "ds_write_b32 v204, v128\n\t" \
  "s_waitcnt lgkmcnt(0)\n\t" \
  "s_barrier\n\t" \
  "s_mov_b32 %[spins], s92\n\t" \
  "s_branch .Lend%=\n\t" \
  ".Lslow%=:\n\t" \
  "s_mov_b64 s[94:95], 0\n\t" \
  "v_cmp_eq_u32 vcc, %[sent], v188\n\t" \
  "s_or_b64 s[94:95], s[94:95], vcc\n\t" \
  "v_cmp_eq_u32 vcc, %[sent], v189\n\t" \
  "s_or_b64 s[94:95], s[94:95], vcc\n\t" \
  "v_cmp_eq_u32 vcc, %[sent], v190\n\t" \
  "s_or_b64 s[94:95], s[94:95], vcc\n\t" \
  "v_cmp_eq_u32 vcc, %[sent], v191\n\t" \
  "s_or_b64 s[94:95], s[94:95], vcc\n\t" \
  "v_cmp_eq_u32 vcc, %[sent], v192\n\t" \
  "s_or_b64 s[94:95], s[94:95], vcc\n\t" \
  "v_cmp_eq_u32 vcc, %[sent], v193\n\t" \
  "s_or_b64 s[94:95], s[94:95], vcc\n\t" \
  "v_cmp_eq_u32 vcc, %[sent], v194\n\t" \
  "s_or_b64 s[94:95], s[94:95], vcc\n\t" \
  "v_cmp_eq_u32 vcc, %[sent], v195\n\t" \
  "s_or_b64 s[94:95], s[94:95], vcc\n\t" \
  "s_cmp_eq_u64 s[94:95], 0\n\t" \
  "s_cbranch_scc1 .Lok%=\n\t" \
  "s_add_u32 s92, s92, 1\n\t" \
  "s_cmp_eq_u32 s92, 1\n\t" \
  "s_cbranch_scc1 .Lpoll%=\n\t" \
  "s_sleep 6\n\t" \
  "s_and_b32 s93, s92, 63\n\t" \
  "s_cmp_lg_u32 s93, 0\n\t" \
  "s_cbranch_scc1 .Lpoll%=\n\t" \
  "v_mov_b32 v197, 0\n\t" \
  "buffer_load_dword v196, v197, %[rs], %[sab] offen sc1\n\t" \
  "s_waitcnt vmcnt(0)\n\t" \
  "v_readfirstlane_b32 s93, v196\n\t" \
  "s_nop 3\n\t" \
  "s_cmp_eq_u32 s93, 1\n\t" \
  "s_cbranch_scc1 .Lok%=\n\t" \
  "s_cmp_lt_u32 s92, 0x80000\n\t" \
  "s_cbranch_scc1 .Lpoll%=\n\t" \
  "v_mov_b32 v196, 1\n\t" \
  "buffer_store_dword v196, v197, %[rs], %[sab] offen sc1\n\t" \
  "s_branch .Lok%=\n\t" \
  ".Lend%=:\n\t"
#define RED_ASM \
  "s_mov_b32 %[step], 0\n\t" \
  "s_lshl_b32 %[sh], %[step], 15\n\t" \
  "s_lshr_b32 s93, %[step], 4\n\t" \
  "s_lshr_b32 s93, %[pm], s93\n\t" \
  "s_and_b32 s93, s93, 4\n\t" \
  "s_lshl_b32 s93, s93, 11\n\t" \
  "s_lshl2_add_u32 %[so], %[sh], s93\n\t" \
  "s_sub_i32 s93, %[step], 3\n\t" \
  "s_lshl_b32 %[st], s93, 19\n\t" \
  "s_lshl_b32 s94, s93, 11\n\t" \
  "s_xor_b32 s94, s94, %[s0]\n\t" \
  "s_and_b32 s94, s94, 0x2000\n\t" \
  "s_or_b32 %[st], %[st], s94\n\t" \
  "s_cmp_lt_i32 s93, 0\n\t" \
  "s_cselect_b32 %[st], 0x70000000, %[st]\n\t" \
  "s_add_u32 %[st4], %[st], 4096\n\t" \
  "buffer_load_dwordx2 v[166:167], v173, %[rs], 0 offen sc1\n\t" \
  "v_mov_b64 v[144:145], 0\n\t" \
  "v_mov_b64 v[146:147], 0\n\t" \
  "v_mov_b64 v[148:149], 0\n\t" \
  "v_mov_b64 v[150:151], 0\n\t" \
  "v_mov_b64 v[152:153], 0\n\t" \
  "v_mov_b64 v[154:155], 0\n\t" \
  "v_mov_b64 v[156:157], 0\n\t" \
  "v_mov_b64 v[158:159], 0\n\t" \
  ".Lstep%=:\n\t" \
  "s_setprio 3\n\t" \
  "s_mov_b32 s92, 0\n\t" \
  ".Lcal%=:\n\t" \
  "s_waitcnt vmcnt(0)\n\t" \
  "s_memrealtime s[76:77]\n\t" \
  "v_cmp_ne_u32 vcc, %[step], v166\n\t" \
  "v_readlane_b32 s90, v167, 0\n\t" \
  "v_readlane_b32 s91, v167, 1\n\t" \
  "v_readlane_b32 s94, v167, 2\n\t" \
  "v_readlane_b32 s95, v167, 3\n\t" \
  "s_sub_i32 s90, s90, %[base]\n\t" \
  "s_sub_i32 s91, s91, %[base]\n\t" \
  "s_sub_i32 s94, s94, %[base]\n\t" \
  "s_sub_i32 s95, s95, %[base]\n\t" \
  "s_max_i32 s90, s90, s91\n\t" \
  "s_max_i32 s94, s94, s95\n\t" \
  "s_max_i32 s90, s90, s94\n\t" \
  "s_add_i32 s90, s90, %[ds]\n\t" \
  "s_waitcnt lgkmcnt(0)\n\t" \
  "s_lshl2_add_u32 s76, s76, %[mn]\n\t" \
  "s_sub_i32 s76, s76, %[base]\n\t" \
  "s_max_i32 s94, s90, s76\n\t" \
  "s_add_u32 %[E], s90, %[base]\n\t" \
  "s_and_b32 s93, vcc_lo, 0xf\n\t" \
  "s_cbranch_scc1 .Lbl%=\n\t" \
  ".Lgo%=:\n\t" \
  "v_mov_b32 v166, 7\n\t" \
  "v_mov_b32 v162, %[step]\n\t" \
  "s_mov_b64 exec, 1\n\t" \
  "v_mov_b32 v164, s94\n\t" \
  "ds_max_u32 v168, v164 offset:4100\n\t" \
  "ds_inc_rtn_u32 v161, v168, v166 offset:4096\n\t" \
  "ds_read_b32 v163, v168 offset:4100\n\t" \
  "s_waitcnt lgkmcnt(0)\n\t" \
  "v_readfirstlane_b32 s94, v161\n\t" \
  "v_readfirstlane_b32 s95, v163\n\t" \
  "s_mov_b64 exec, -1\n\t" \
  "s_cmp_lg_u32 s94, 7\n\t" \
  "s_cbranch_scc1 .Lna%=\n\t" \
  "s_add_u32 s95, s95, %[rl]\n\t" \
  "s_add_u32 s95, s95, %[base]\n\t" \
  "s_mov_b64 exec, 0xffff\n\t" \
  "v_mov_b32 v163, s95\n\t" \
  "buffer_store_dwordx2 v[162:163], v175, %[rs], 0 offen sc1\n\t" \
  "s_mov_b64 exec, -1\n\t" \
  ".Lna%=:\n\t" \
  "s_memrealtime s[90:91]\n\t" \
  "s_waitcnt lgkmcnt(0)\n\t" \
  "s_lshl_b32 s93, s90, 2\n\t" \
  "s_sub_i32 s93, %[E], s93\n\t" \
  "s_cmp_le_i32 s93, 0\n\t" \
  "s_cbranch_scc1 .Lgop%=\n\t" \
  "s_min_i32 s93, s93, 8000\n\t" \
  "s_add_u32 s93, s93, 3\n\t" \
  "s_lshr_b32 s93, s93, 2\n\t" \
  "s_add_u32 s93, s93, s90\n\t" \
  "s_memrealtime s[90:91]\n\t" \
  ".Lpacep%=:\n\t" \
  "s_memrealtime s[94:95]\n\t" \
  "s_waitcnt lgkmcnt(1)\n\t" \
  "s_sub_i32 s91, s90, s93\n\t" \
  "s_cmp_ge_i32 s91, 0\n\t" \
  "s_cbranch_scc1 .Lgop%=\n\t" \
  "s_memrealtime s[90:91]\n\t" \
  "s_waitcnt lgkmcnt(1)\n\t" \
  "s_sub_i32 s95, s94, s93\n\t" \
  "s_cmp_lt_i32 s95, 0\n\t" \
  "s_cbranch_scc1 .Lpacep%=\n\t" \
  ".Lgop%=:\n\t" \
  "buffer_load_dwordx4 v[128:131], v234, %[rs], %[so] offen sc1\n\t" \
  "buffer_load_dwordx4 v[132:135], v235, %[rs], %[so] offen sc1\n\t" \
  "buffer_load_dwordx4 v[136:139], v236, %[rs], %[so] offen sc1\n\t" \
  "buffer_load_dwordx4 v[140:143], v237, %[rs], %[so] offen sc1\n\t" \
  "s_waitcnt lgkmcnt(0)\n\t" \
  ".Lw%=:\n\t" \
  "s_waitcnt vmcnt(3)\n\t" \
  "v_smfmac_f32_16x16x64_bf16 v[144:147], v[128:131], v[0:7], v172\n\t" \
  "v_max3_u32 v164, v128, v129, 0\n\t" \
  "v_smfmac_f32_16x16x64_bf16 v[148:151], v[128:131], v[8:15], v172\n\t" \
  "v_max3_u32 v164, v130, v131, v164\n\t" \
  "v_smfmac_f32_16x16x64_bf16 v[152:155], v[128:131], v[16:23], v172\n\t" \
  "v_smfmac_f32_16x16x64_bf16 v[156:159], v[128:131], v[24:31], v172\n\t" \
  "buffer_load_dwordx4 v[176:179], v238, %[rt], %[st] offen offset:0 sc1\n\t" \
  "s_waitcnt vmcnt(3)\n\t" \
  "v_smfmac_f32_16x16x64_bf16 v[144:147], v[132:135], v[32:39], v172\n\t" \
  "v_max3_u32 v164, v132, v133, v164\n\t" \
  "v_smfmac_f32_16x16x64_bf16 v[148:151], v[132:135], v[40:47], v172\n\t" \
  "v_max3_u32 v164, v134, v135, v164\n\t" \
  "v_smfmac_f32_16x16x64_bf16 v[152:155], v[132:135], v[48:55], v172\n\t" \
  "v_smfmac_f32_16x16x64_bf16 v[156:159], v[132:135], v[56:63], v172\n\t" \
  "buffer_load_dwordx4 v[180:183], v238, %[rt], %[st] offen offset:1024 sc1\n\t" \
  "s_waitcnt vmcnt(3)\n\t" \
  "v_smfmac_f32_16x16x64_bf16 v[144:147], v[136:139], v[64:71], v172\n\t" \
  "v_max3_u32 v164, v136, v137, v164\n\t" \
  "v_smfmac_f32_16x16x64_bf16 v[148:151], v[136:139], v[72:79], v172\n\t" \
  "v_max3_u32 v164, v138, v139, v164\n\t" \
  "v_smfmac_f32_16x16x64_bf16 v[152:155], v[136:139], v[80:87], v172\n\t" \
  "v_smfmac_f32_16x16x64_bf16 v[156:159], v[136:139], v[88:95], v172\n\t" \
  "buffer_load_dwordx4 v[184:187], v238, %[rt], %[st4] offen offset:0 sc1\n\t" \
  "s_waitcnt vmcnt(3)\n\t" \
  "v_smfmac_f32_16x16x64_bf16 v[144:147], v[140:143], v[96:103], v172\n\t" \
  "v_max3_u32 v164, v140, v141, v164\n\t" \
  "v_smfmac_f32_16x16x64_bf16 v[148:151], v[140:143], v[104:111], v172\n\t" \
  "v_max3_u32 v164, v142, v143, v164\n\t" \
  "v_smfmac_f32_16x16x64_bf16 v[152:155], v[140:143], v[112:119], v172\n\t" \
  "v_cmp_le_u32 vcc, %[sent], v164\n\t" \
  "v_smfmac_f32_16x16x64_bf16 v[156:159], v[140:143], v[120:127], v172\n\t" \
  "buffer_load_dwordx4 v[188:191], v238, %[rt], %[st4] offen offset:1024 sc1\n\t" \
  "s_cbranch_vccnz .Lslow%=\n\t" \
  ".Lok%=:\n\t" \
  "v_add_f32_dpp v160, v144, v146 quad_perm:[0,1,2,3] row_mask:0x3 bank_mask:0xf\n\t" \
  "v_add_f32_dpp v161, v145, v147 quad_perm:[0,1,2,3] row_mask:0x3 bank_mask:0xf\n\t" \
  "v_add_f32_dpp v162, v148, v150 quad_perm:[0,1,2,3] row_mask:0x3 bank_mask:0xf\n\t" \
  "v_add_f32_dpp v163, v149, v151 quad_perm:[0,1,2,3] row_mask:0x3 bank_mask:0xf\n\t" \
  "v_add_f32_dpp v160, v152, v154 quad_perm:[0,1,2,3] row_mask:0xc bank_mask:0xf\n\t" \
  "v_add_f32_dpp v161, v153, v155 quad_perm:[0,1,2,3] row_mask:0xc bank_mask:0xf\n\t" \
  "v_add_f32_dpp v162, v156, v158 quad_perm:[0,1,2,3] row_mask:0xc bank_mask:0xf\n\t" \
  "v_add_f32_dpp v163, v157, v159 quad_perm:[0,1,2,3] row_mask:0xc bank_mask:0xf\n\t" \
  "s_nop 0\n\t" \
  "v_permlane32_swap_b32 v160, v162\n\t" \
  "v_permlane32_swap_b32 v161, v163\n\t" \
  "v_add_f32 v160, v160, v162\n\t" \
  "v_add_f32 v161, v161, v163\n\t" \
  "ds_write_b64 v169, v[160:161]\n\t" \
  "s_mov_b64 exec, 1\n\t" \
  "ds_inc_rtn_u32 v164, v168, v166 offset:4104\n\t" \
  "s_mov_b64 exec, -1\n\t" \
  "ds_read_b64 v[128:129], v170 offset:0\n\t" \
  "ds_read_b64 v[130:131], v170 offset:512\n\t" \
  "ds_read_b64 v[132:133], v170 offset:1024\n\t" \
  "ds_read_b64 v[134:135], v170 offset:1536\n\t" \
  "ds_read_b64 v[136:137], v170 offset:2048\n\t" \
  "ds_read_b64 v[138:139], v170 offset:2560\n\t" \
  "ds_read_b64 v[140:141], v170 offset:3072\n\t" \
  "ds_read_b64 v[142:143], v170 offset:3584\n\t" \
  "s_waitcnt lgkmcnt(8)\n\t" \
  "v_readfirstlane_b32 s93, v164\n\t" \
  "s_cmp_lg_u32 s93, 7\n\t" \
  "s_cbranch_scc1 .Lb1%=\n\t" \
  "s_waitcnt lgkmcnt(4)\n\t" \
  "v_pk_add_f32 v[128:129], v[128:129], v[130:131]\n\t" \
  "v_pk_add_f32 v[132:133], v[132:133], v[134:135]\n\t" \
  "s_waitcnt lgkmcnt(0)\n\t" \
  "v_pk_add_f32 v[136:137], v[136:137], v[138:139]\n\t" \
  "v_pk_add_f32 v[140:141], v[140:141], v[142:143]\n\t" \
  "v_pk_add_f32 v[128:129], v[128:129], v[132:133]\n\t" \
  "v_pk_add_f32 v[136:137], v[136:137], v[140:141]\n\t" \
  "v_pk_add_f32 v[128:129], v[128:129], v[136:137]\n\t" \
  "v_cvt_pk_bf16_f32 v128, v128, v129\n\t" \
  "s_nop 1\n\t" \
  "v_mov_b32_dpp v129, v128 quad_perm:[1,0,3,2] row_mask:0xf bank_mask:0xf\n\t" \
  "v_perm_b32 v128, v129, v128, v174\n\t" \
  "buffer_store_dword v128, v171, %[rs], %[sh] offen sc1\n\t" \
  "s_memtime s[90:91]\n\t" \
  "s_barrier\n\t" \
  "s_memtime s[76:77]\n\t" \
  "s_memrealtime s[78:79]\n\t" \
  "s_nop 3\n\t" \
  "s_memrealtime s[80:81]\n\t" \
  "s_nop 3\n\t" \
  "s_memrealtime s[82:83]\n\t" \
  "s_nop 3\n\t" \
  "s_memrealtime s[84:85]\n\t" \
  "s_nop 3\n\t" \
  "s_memrealtime s[86:87]\n\t" \
  "s_nop 3\n\t" \
  "s_memrealtime s[88:89]\n\t" \
  "s_mov_b32 s93, 0\n\t" \
  "s_waitcnt lgkmcnt(0)\n\t" \
  "s_cmp_eq_u32 s80, s78\n\t" \
  "s_addc_u32 s93, s93, 0\n\t" \
  "s_cmp_eq_u32 s82, s78\n\t" \
  "s_addc_u32 s93, s93, 0\n\t" \
  "s_cmp_eq_u32 s84, s78\n\t" \
  "s_addc_u32 s93, s93, 0\n\t" \
  "s_cmp_eq_u32 s86, s78\n\t" \
  "s_addc_u32 s93, s93, 0\n\t" \
  "s_cmp_eq_u32 s88, s78\n\t" \
  "s_addc_u32 s93, s93, 0\n\t" \
  "s_mul_i32 s93, s93, 21\n\t" \
  "s_add_u32 s76, s76, 15\n\t" \
  "s_add_u32 s76, s76, s93\n\t" \
  "s_lshl2_add_u32 s78, s78, 16\n\t" \
  "s_mov_b64 exec, 1\n\t" \
  "s_sub_i32 s90, s76, s90\n\t" \
  "s_mul_i32 s90, s90, 11900\n\t" \
  "s_ashr_i32 s90, s90, 16\n\t" \
  "s_sub_u32 s90, s78, s90\n\t" \
  "s_sub_u32 s90, s90, %[base]\n\t" \
  "v_mov_b32 v164, s90\n\t" \
  "ds_write_b32 v168, v164 offset:4108\n\t" \
  "s_mov_b64 exec, -1\n\t" \
  "s_waitcnt vmcnt(1)\n\t" \
  ".Lb2%=:\n\t" \
  "s_setprio 0\n\t" \
  "s_bitcmp0_b64 %[am], %[step]\n\t" \
  "s_cbranch_scc1 .La1%=\n\t" \
  "buffer_store_dwordx4 v[224:227], v228, %[ra], 0 offen sc1\n\t" \
  ".La1%=:\n\t" \
  "buffer_load_dwordx4 v[192:195], v239, %[rt], %[st] offen offset:0 sc1\n\t" \
  "buffer_load_dwordx4 v[196:199], v239, %[rt], %[st] offen offset:1024 sc1\n\t" \
  "v_mfma_f32_16x16x32_bf16 v[128:131], v[0:3], v[176:179], 0\n\t" \
  "v_mfma_f32_16x16x32_bf16 v[136:139], v[8:11], v[176:179], 0\n\t" \
  "ds_read2st64_b32 v[144:145], v231 offset0:0 offset1:8\n\t" \
  "ds_read2st64_b32 v[146:147], v231 offset0:16 offset1:24\n\t" \
  "ds_read2st64_b32 v[148:149], v231 offset0:32 offset1:40\n\t" \
  "ds_read2st64_b32 v[150:151], v231 offset0:48 offset1:56\n\t" \
  "buffer_load_dwordx4 v[200:203], v239, %[rt], %[st4] offen offset:0 sc1\n\t" \
  "buffer_load_dwordx4 v[204:207], v239, %[rt], %[st4] offen offset:1024 sc1\n\t" \
  "v_mfma_f32_16x16x32_bf16 v[132:135], v[4:7], v[180:183], 0\n\t" \
  "v_mfma_f32_16x16x32_bf16 v[140:143], v[12:15], v[180:183], 0\n\t" \
  "s_waitcnt lgkmcnt(0)\n\t" \
  "v_add_f32 v144, v144, v145\n\t" \
  "v_add_f32 v146, v146, v147\n\t" \
  "v_add_f32 v148, v148, v149\n\t" \
  "v_add_f32 v150, v150, v151\n\t" \
  "v_add_f32 v144, v144, v146\n\t" \
  "v_add_f32 v148, v148, v150\n\t" \
  "buffer_load_dwordx4 v[208:211], v240, %[rt], %[st] offen offset:0 sc1\n\t" \
  "buffer_load_dwordx4 v[212:215], v240, %[rt], %[st] offen offset:1024 sc1\n\t" \
  "v_mfma_f32_16x16x32_bf16 v[128:131], v[16:19], v[184:187], v[128:131]\n\t" \
  "v_mfma_f32_16x16x32_bf16 v[136:139], v[24:27], v[184:187], v[136:139]\n\t" \
  "v_add_f32 v144, v144, v148\n\t" \
  "v_cvt_pk_bf16_f32 v144, v144, v144\n\t" \
  "ds_write_b16 v232, v144\n\t" \
  "ds_read_b128 v[224:227], v229\n\t" \
  "v_add_u32 v228, %[ainc], v228\n\t" \
  "v_add_u32 v229, %[rinc], v229\n\t" \
  "v_add_u32 v232, 1024, v232\n\t" \
  "v_xor_b32 v231, 0x4000, v231\n\t" \
  "buffer_load_dwordx4 v[216:219], v240, %[rt], %[st4] offen offset:0 sc1\n\t" \
  "buffer_load_dwordx4 v[220:223], v240, %[rt], %[st4] offen offset:1024 sc1\n\t" \
  "v_mfma_f32_16x16x32_bf16 v[132:135], v[20:23], v[188:191], v[132:135]\n\t" \
  "v_mfma_f32_16x16x32_bf16 v[140:143], v[28:31], v[188:191], v[140:143]\n\t" \
  "buffer_load_dwordx4 v[176:179], v241, %[rt], %[st] offen offset:0 sc1\n\t" \
  "s_waitcnt vmcnt(8)\n\t" \
  "v_mfma_f32_16x16x32_bf16 v[128:131], v[32:35], v[192:195], v[128:131]\n\t" \
  "v_mfma_f32_16x16x32_bf16 v[136:139], v[40:43], v[192:195], v[136:139]\n\t" \
  "buffer_load_dwordx4 v[180:183], v241, %[rt], %[st] offen offset:1024 sc1\n\t" \
  "s_waitcnt vmcnt(8)\n\t" \
  "v_mfma_f32_16x16x32_bf16 v[132:135], v[36:39], v[196:199], v[132:135]\n\t" \
  "v_mfma_f32_16x16x32_bf16 v[140:143], v[44:47], v[196:199], v[140:143]\n\t" \
  "buffer_load_dwordx4 v[184:187], v241, %[rt], %[st4] offen offset:0 sc1\n\t" \
  "s_waitcnt vmcnt(8)\n\t" \
  "v_mfma_f32_16x16x32_bf16 v[128:131], v[48:51], v[200:203], v[128:131]\n\t" \
  "v_mfma_f32_16x16x32_bf16 v[136:139], v[56:59], v[200:203], v[136:139]\n\t" \
  "ds_read_b128 v[160:163], v168 offset:4096\n\t" \
  "buffer_load_dwordx4 v[188:191], v241, %[rt], %[st4] offen offset:1024 sc1\n\t" \
  "s_waitcnt vmcnt(8)\n\t" \
  "v_mfma_f32_16x16x32_bf16 v[132:135], v[52:55], v[204:207], v[132:135]\n\t" \
  "v_mfma_f32_16x16x32_bf16 v[140:143], v[60:63], v[204:207], v[140:143]\n\t" \
  "s_waitcnt lgkmcnt(0)\n\t" \
  "v_readfirstlane_b32 s90, v161\n\t" \
  "v_readfirstlane_b32 s91, v163\n\t" \
  "s_add_u32 s93, s90, %[rl]\n\t" \
  "s_add_u32 s93, s93, %[base]\n\t" \
  "s_add_u32 %[E], s93, %[lk]\n\t" \
  "s_sub_i32 s91, s91, s90\n\t" \
  "s_sub_i32 s91, s91, %[rl]\n\t" \
  "s_min_i32 s93, s91, %[up]\n\t" \
  "s_max_i32 s93, s93, %[dn]\n\t" \
  "s_cmp_gt_i32 s91, 60\n\t" \
  "s_cselect_b32 s93, 2, s93\n\t" \
  "s_add_i32 %[rl], %[rl], s93\n\t" \
  "s_waitcnt vmcnt(7)\n\t" \
  "v_mfma_f32_16x16x32_bf16 v[128:131], v[64:67], v[208:211], v[128:131]\n\t" \
  "v_mfma_f32_16x16x32_bf16 v[136:139], v[72:75], v[208:211], v[136:139]\n\t" \
  "s_add_u32 %[step], %[step], 1\n\t" \
  "s_lshl_b32 %[sh], %[step], 15\n\t" \
  "s_lshr_b32 s93, %[step], 4\n\t" \
  "s_lshr_b32 s93, %[pm], s93\n\t" \
  "s_and_b32 s93, s93, 4\n\t" \
  "s_lshl_b32 s93, s93, 11\n\t" \
  "s_lshl2_add_u32 %[so], %[sh], s93\n\t" \
  "s_waitcnt vmcnt(6)\n\t" \
  "v_mfma_f32_16x16x32_bf16 v[132:135], v[68:71], v[212:215], v[132:135]\n\t" \
  "v_mfma_f32_16x16x32_bf16 v[140:143], v[76:79], v[212:215], v[140:143]\n\t" \
  "s_sub_i32 s93, %[step], 3\n\t" \
  "s_lshl_b32 %[st], s93, 19\n\t" \
  "s_lshl_b32 s94, s93, 11\n\t" \
  "s_xor_b32 s94, s94, %[s0]\n\t" \
  "s_and_b32 s94, s94, 0x2000\n\t" \
  "s_or_b32 %[st], %[st], s94\n\t" \
  "s_cmp_lt_i32 s93, 0\n\t" \
  "s_cselect_b32 %[st], 0x70000000, %[st]\n\t" \
  "s_add_u32 %[st4], %[st], 4096\n\t" \
  "s_waitcnt vmcnt(5)\n\t" \
  "v_mfma_f32_16x16x32_bf16 v[128:131], v[80:83], v[216:219], v[128:131]\n\t" \
  "v_mfma_f32_16x16x32_bf16 v[136:139], v[88:91], v[216:219], v[136:139]\n\t" \
  "s_waitcnt vmcnt(4)\n\t" \
  "v_mfma_f32_16x16x32_bf16 v[132:135], v[84:87], v[220:223], v[132:135]\n\t" \
  "v_mfma_f32_16x16x32_bf16 v[140:143], v[92:95], v[220:223], v[140:143]\n\t" \
  "s_cmp_eq_u32 %[step], 63\n\t" \
  "s_cbranch_scc1 .Lgol%=\n\t" \
  "s_movk_i32 s93, 600\n\t" \
  ".Lpacel%=:\n\t" \
  "s_memrealtime s[90:91]\n\t" \
  "s_waitcnt lgkmcnt(0)\n\t" \
  "s_lshl_b32 s90, s90, 2\n\t" \
  "s_sub_i32 s91, %[E], s90\n\t" \
  "s_cmp_le_i32 s91, 0\n\t" \
  "s_cbranch_scc1 .Lgol%=\n\t" \
  "s_sub_u32 s93, s93, 1\n\t" \
  "s_cmp_lg_u32 s93, 0\n\t" \
  "s_cbranch_scc1 .Lpacel%=\n\t" \
  ".Lgol%=:\n\t" \
  "buffer_load_dwordx2 v[166:167], v173, %[rs], 0 offen sc1\n\t" \
  "s_waitcnt vmcnt(4)\n\t" \
  "v_mfma_f32_16x16x32_bf16 v[128:131], v[96:99], v[176:179], v[128:131]\n\t" \
  "v_mfma_f32_16x16x32_bf16 v[136:139], v[104:107], v[176:179], v[136:139]\n\t" \
  "s_waitcnt vmcnt(3)\n\t" \
  "v_mfma_f32_16x16x32_bf16 v[132:135], v[100:103], v[180:183], v[132:135]\n\t" \
  "v_mfma_f32_16x16x32_bf16 v[140:143], v[108:111], v[180:183], v[140:143]\n\t" \
  "s_waitcnt vmcnt(2)\n\t" \
  "v_mfma_f32_16x16x32_bf16 v[128:131], v[112:115], v[184:187], v[128:131]\n\t" \
  "v_mfma_f32_16x16x32_bf16 v[136:139], v[120:123], v[184:187], v[136:139]\n\t" \
  "s_waitcnt vmcnt(1)\n\t" \
  "v_mfma_f32_16x16x32_bf16 v[132:135], v[116:119], v[188:191], v[132:135]\n\t" \
  "v_mfma_f32_16x16x32_bf16 v[140:143], v[124:127], v[188:191], v[140:143]\n\t" \
  "v_mov_b64 v[144:145], 0\n\t" \
  "v_mov_b64 v[146:147], 0\n\t" \
  "v_mov_b64 v[148:149], 0\n\t" \
  "v_mov_b64 v[150:151], 0\n\t" \
  "v_mov_b64 v[152:153], 0\n\t" \
  "v_mov_b64 v[154:155], 0\n\t" \
  "v_mov_b64 v[156:157], 0\n\t" \
  "v_mov_b64 v[158:159], 0\n\t" \
  "v_pk_add_f32 v[128:129], v[128:129], v[132:133]\n\t" \
  "v_pk_add_f32 v[130:131], v[130:131], v[134:135]\n\t" \
  "ds_write_b128 v230, v[128:131] offset:0\n\t" \
  "v_pk_add_f32 v[136:137], v[136:137], v[140:141]\n\t" \
  "v_pk_add_f32 v[138:139], v[138:139], v[142:143]\n\t" \
  "ds_write_b128 v230, v[136:139] offset:1024\n\t" \
  "v_xor_b32 v230, 0x4000, v230\n\t" \
  "s_cmp_lg_u32 %[step], 63\n\t" \
  "s_cbranch_scc1 .Lstep%=\n\t" \
  "buffer_load_dwordx4 v[176:179], v238, %[rt], %[st] offen offset:0 sc1\n\t" \
  "buffer_load_dwordx4 v[180:183], v238, %[rt], %[st] offen offset:1024 sc1\n\t" \
  "buffer_load_dwordx4 v[184:187], v238, %[rt], %[st4] offen offset:0 sc1\n\t" \
  "buffer_load_dwordx4 v[188:191], v238, %[rt], %[st4] offen offset:1024 sc1\n\t" \
  "buffer_load_dwordx4 v[192:195], v239, %[rt], %[st] offen offset:0 sc1\n\t" \
  "buffer_load_dwordx4 v[196:199], v239, %[rt], %[st] offen offset:1024 sc1\n\t" \
  "buffer_load_dwordx4 v[200:203], v239, %[rt], %[st4] offen offset:0 sc1\n\t" \
  "buffer_load_dwordx4 v[204:207], v239, %[rt], %[st4] offen offset:1024 sc1\n\t" \
  "v_subrev_u32 v230, 98304, v230\n\t" \
  ".Lt%=:\n\t" \
  "s_waitcnt vmcnt(7)\n\t" \
  "v_mfma_f32_16x16x32_bf16 v[128:131], v[0:3], v[176:179], 0\n\t" \
  "v_mfma_f32_16x16x32_bf16 v[136:139], v[8:11], v[176:179], 0\n\t" \
  "buffer_load_dwordx4 v[176:179], v240, %[rt], %[st] offen offset:0 sc1\n\t" \
  "buffer_load_dword v166, v242, %[rs], 0 offen sc1\n\t" \
  "s_waitcnt vmcnt(8)\n\t" \
  "v_mfma_f32_16x16x32_bf16 v[132:135], v[4:7], v[180:183], 0\n\t" \
  "v_mfma_f32_16x16x32_bf16 v[140:143], v[12:15], v[180:183], 0\n\t" \
  "buffer_load_dwordx4 v[180:183], v240, %[rt], %[st] offen offset:1024 sc1\n\t" \
  "s_waitcnt vmcnt(8)\n\t" \
  "v_mfma_f32_16x16x32_bf16 v[128:131], v[16:19], v[184:187], v[128:131]\n\t" \
  "v_mfma_f32_16x16x32_bf16 v[136:139], v[24:27], v[184:187], v[136:139]\n\t" \
  "buffer_load_dwordx4 v[184:187], v240, %[rt], %[st4] offen offset:0 sc1\n\t" \
  "s_waitcnt vmcnt(8)\n\t" \
  "v_mfma_f32_16x16x32_bf16 v[132:135], v[20:23], v[188:191], v[132:135]\n\t" \
  "v_mfma_f32_16x16x32_bf16 v[140:143], v[28:31], v[188:191], v[140:143]\n\t" \
  "buffer_load_dwordx4 v[188:191], v240, %[rt], %[st4] offen offset:1024 sc1\n\t" \
  "s_waitcnt vmcnt(8)\n\t" \
  "v_mfma_f32_16x16x32_bf16 v[128:131], v[32:35], v[192:195], v[128:131]\n\t" \
  "v_mfma_f32_16x16x32_bf16 v[136:139], v[40:43], v[192:195], v[136:139]\n\t" \
  "buffer_load_dwordx4 v[192:195], v241, %[rt], %[st] offen offset:0 sc1\n\t" \
  "s_waitcnt vmcnt(8)\n\t" \
  "v_mfma_f32_16x16x32_bf16 v[132:135], v[36:39], v[196:199], v[132:135]\n\t" \
  "v_mfma_f32_16x16x32_bf16 v[140:143], v[44:47], v[196:199], v[140:143]\n\t" \
  "buffer_load_dwordx4 v[196:199], v241, %[rt], %[st] offen offset:1024 sc1\n\t" \
  "s_waitcnt vmcnt(8)\n\t" \
  "v_mfma_f32_16x16x32_bf16 v[128:131], v[48:51], v[200:203], v[128:131]\n\t" \
  "v_mfma_f32_16x16x32_bf16 v[136:139], v[56:59], v[200:203], v[136:139]\n\t" \
  "buffer_load_dwordx4 v[200:203], v241, %[rt], %[st4] offen offset:0 sc1\n\t" \
  "s_waitcnt vmcnt(8)\n\t" \
  "v_mfma_f32_16x16x32_bf16 v[132:135], v[52:55], v[204:207], v[132:135]\n\t" \
  "v_mfma_f32_16x16x32_bf16 v[140:143], v[60:63], v[204:207], v[140:143]\n\t" \
  "buffer_load_dwordx4 v[204:207], v241, %[rt], %[st4] offen offset:1024 sc1\n\t" \
  "s_waitcnt vmcnt(8)\n\t" \
  "v_mfma_f32_16x16x32_bf16 v[128:131], v[64:67], v[176:179], v[128:131]\n\t" \
  "v_mfma_f32_16x16x32_bf16 v[136:139], v[72:75], v[176:179], v[136:139]\n\t" \
  "s_waitcnt vmcnt(7)\n\t" \
  ".Ltc%=:\n\t" \
  "v_cmp_eq_u32 vcc, -1, v166\n\t" \
  "s_cbranch_vccnz .Ltw%=\n\t" \
  ".Lto%=:\n\t" \
  "s_cmp_eq_u32 %[step], 66\n\t" \
  "s_cbranch_scc1 .Ltl%=\n\t" \
  "s_add_u32 %[step], %[step], 1\n\t" \
  "s_sub_i32 s93, %[step], 3\n\t" \
  "s_lshl_b32 %[st], s93, 19\n\t" \
  "s_lshl_b32 s94, s93, 11\n\t" \
  "s_xor_b32 s94, s94, %[s0]\n\t" \
  "s_and_b32 s94, s94, 0x2000\n\t" \
  "s_or_b32 %[st], %[st], s94\n\t" \
  "s_cmp_lt_i32 s93, 0\n\t" \
  "s_cselect_b32 %[st], 0x70000000, %[st]\n\t" \
  "s_add_u32 %[st4], %[st], 4096\n\t" \
  "v_add_u32 v242, 16384, v242\n\t" \
  "s_cmp_eq_u32 %[step], 66\n\t" \
  "s_cbranch_scc0 .Ltn%=\n\t" \
  "v_mov_b32 v242, %[fg]\n\t" \
  ".Ltn%=:\n\t" \
  "buffer_load_dwordx4 v[176:179], v238, %[rt], %[st] offen offset:0 sc1\n\t" \
  "s_waitcnt vmcnt(7)\n\t" \
  "v_mfma_f32_16x16x32_bf16 v[132:135], v[68:71], v[180:183], v[132:135]\n\t" \
  "v_mfma_f32_16x16x32_bf16 v[140:143], v[76:79], v[180:183], v[140:143]\n\t" \
  "buffer_load_dwordx4 v[180:183], v238, %[rt], %[st] offen offset:1024 sc1\n\t" \
  "s_waitcnt vmcnt(7)\n\t" \
  "v_mfma_f32_16x16x32_bf16 v[128:131], v[80:83], v[184:187], v[128:131]\n\t" \
  "v_mfma_f32_16x16x32_bf16 v[136:139], v[88:91], v[184:187], v[136:139]\n\t" \
  "buffer_load_dwordx4 v[184:187], v238, %[rt], %[st4] offen offset:0 sc1\n\t" \
  "s_waitcnt vmcnt(7)\n\t" \
  "v_mfma_f32_16x16x32_bf16 v[132:135], v[84:87], v[188:191], v[132:135]\n\t" \
  "v_mfma_f32_16x16x32_bf16 v[140:143], v[92:95], v[188:191], v[140:143]\n\t" \
  "buffer_load_dwordx4 v[188:191], v238, %[rt], %[st4] offen offset:1024 sc1\n\t" \
  "s_waitcnt vmcnt(7)\n\t" \
  "v_mfma_f32_16x16x32_bf16 v[128:131], v[96:99], v[192:195], v[128:131]\n\t" \
  "v_mfma_f32_16x16x32_bf16 v[136:139], v[104:107], v[192:195], v[136:139]\n\t" \
  "buffer_load_dwordx4 v[192:195], v239, %[rt], %[st] offen offset:0 sc1\n\t" \
  "s_waitcnt vmcnt(7)\n\t" \
  "v_mfma_f32_16x16x32_bf16 v[132:135], v[100:103], v[196:199], v[132:135]\n\t" \
  "v_mfma_f32_16x16x32_bf16 v[140:143], v[108:111], v[196:199], v[140:143]\n\t" \
  "buffer_load_dwordx4 v[196:199], v239, %[rt], %[st] offen offset:1024 sc1\n\t" \
  "s_waitcnt vmcnt(7)\n\t" \
  "v_mfma_f32_16x16x32_bf16 v[128:131], v[112:115], v[200:203], v[128:131]\n\t" \
  "v_mfma_f32_16x16x32_bf16 v[136:139], v[120:123], v[200:203], v[136:139]\n\t" \
  "buffer_load_dwordx4 v[200:203], v239, %[rt], %[st4] offen offset:0 sc1\n\t" \
  "s_waitcnt vmcnt(7)\n\t" \
  "v_mfma_f32_16x16x32_bf16 v[132:135], v[116:119], v[204:207], v[132:135]\n\t" \
  "v_mfma_f32_16x16x32_bf16 v[140:143], v[124:127], v[204:207], v[140:143]\n\t" \
  "buffer_load_dwordx4 v[204:207], v239, %[rt], %[st4] offen offset:1024 sc1\n\t" \
  "s_nop 5\n\t" \
  "v_pk_add_f32 v[128:129], v[128:129], v[132:133]\n\t" \
  "v_pk_add_f32 v[130:131], v[130:131], v[134:135]\n\t" \
  "ds_write_b128 v230, v[128:131] offset:0\n\t" \
  "v_pk_add_f32 v[136:137], v[136:137], v[140:141]\n\t" \
  "v_pk_add_f32 v[138:139], v[138:139], v[142:143]\n\t" \
  "ds_write_b128 v230, v[136:139] offset:1024\n\t" \
  "v_add_u32 v230, 16384, v230\n\t" \
  "s_branch .Lt%=\n\t" \
  ".Ltl%=:\n\t" \
  "buffer_load_dwordx4 v[224:227], v243, %[rg], 0 offen sc1\n\t" \
  "s_waitcnt vmcnt(7)\n\t" \
  "v_mfma_f32_16x16x32_bf16 v[132:135], v[68:71], v[180:183], v[132:135]\n\t" \
  "v_mfma_f32_16x16x32_bf16 v[140:143], v[76:79], v[180:183], v[140:143]\n\t" \
  "s_waitcnt vmcnt(6)\n\t" \
  "v_mfma_f32_16x16x32_bf16 v[128:131], v[80:83], v[184:187], v[128:131]\n\t" \
  "v_mfma_f32_16x16x32_bf16 v[136:139], v[88:91], v[184:187], v[136:139]\n\t" \
  "s_waitcnt vmcnt(5)\n\t" \
  "v_mfma_f32_16x16x32_bf16 v[132:135], v[84:87], v[188:191], v[132:135]\n\t" \
  "v_mfma_f32_16x16x32_bf16 v[140:143], v[92:95], v[188:191], v[140:143]\n\t" \
  "s_waitcnt vmcnt(4)\n\t" \
  "v_mfma_f32_16x16x32_bf16 v[128:131], v[96:99], v[192:195], v[128:131]\n\t" \
  "v_mfma_f32_16x16x32_bf16 v[136:139], v[104:107], v[192:195], v[136:139]\n\t" \
  "s_waitcnt vmcnt(3)\n\t" \
  "v_mfma_f32_16x16x32_bf16 v[132:135], v[100:103], v[196:199], v[132:135]\n\t" \
  "v_mfma_f32_16x16x32_bf16 v[140:143], v[108:111], v[196:199], v[140:143]\n\t" \
  "s_waitcnt vmcnt(2)\n\t" \
  "v_mfma_f32_16x16x32_bf16 v[128:131], v[112:115], v[200:203], v[128:131]\n\t" \
  "v_mfma_f32_16x16x32_bf16 v[136:139], v[120:123], v[200:203], v[136:139]\n\t" \
  "s_waitcnt vmcnt(1)\n\t" \
  "v_mfma_f32_16x16x32_bf16 v[132:135], v[116:119], v[204:207], v[132:135]\n\t" \
  "v_mfma_f32_16x16x32_bf16 v[140:143], v[124:127], v[204:207], v[140:143]\n\t" \
  "s_nop 6\n\t" \
  "v_pk_add_f32 v[128:129], v[128:129], v[132:133]\n\t" \
  "v_pk_add_f32 v[130:131], v[130:131], v[134:135]\n\t" \
  "ds_write_b128 v230, v[128:131] offset:0\n\t" \
  "v_pk_add_f32 v[136:137], v[136:137], v[140:141]\n\t" \
  "v_pk_add_f32 v[138:139], v[138:139], v[142:143]\n\t" \
  "ds_write_b128 v230, v[136:139] offset:1024\n\t" \
  "s_waitcnt vmcnt(0) lgkmcnt(0)\n\t" \
  "s_branch .Lend%=\n\t" \
  ".Ltw%=:\n\t" \
  "s_add_u32 s92, s92, 1\n\t" \
  "s_sleep 2\n\t" \
  "s_and_b32 s93, s92, 63\n\t" \
  "s_cbranch_scc1 .Ltr%=\n\t" \
  "buffer_load_dword v164, off, %[rs], %[sab] sc1\n\t" \
  "s_waitcnt vmcnt(0)\n\t" \
  "v_readfirstlane_b32 s93, v164\n\t" \
  "s_nop 3\n\t" \
  "s_cmp_eq_u32 s93, 1\n\t" \
  "s_cbranch_scc1 .Lto%=\n\t" \
  "s_cmp_lt_u32 s92, 0x80000\n\t" \
  "s_cbranch_scc1 .Ltr%=\n\t" \
  "v_mov_b32 v164, 1\n\t" \
  "buffer_store_dword v164, off, %[rs], %[sab] sc1\n\t" \
  "s_waitcnt vmcnt(0)\n\t" \
  "s_branch .Lto%=\n\t" \
  ".Ltr%=:\n\t" \
  "buffer_load_dword v166, v242, %[rs], 0 offen sc1\n\t" \
  "s_waitcnt vmcnt(0)\n\t" \
  "s_branch .Ltc%=\n\t" \
  ".Lb1%=:\n\t" \
  "s_waitcnt vmcnt(0) lgkmcnt(0)\n\t" \
  "s_barrier\n\t" \
  "s_branch .Lb2%=\n\t" \
  ".Lbl%=:\n\t" \
  "s_bitset1_b64 %[ps], %[step]\n\t" \
  "s_cmp_lt_u32 s92, 3\n\t" \
  "s_cbranch_scc1 .Lbm%=\n\t" \
  "s_sleep 12\n\t" \
  ".Lbm%=:\n\t" \
  "s_add_u32 s92, s92, 1\n\t" \
  "s_sleep 2\n\t" \
  "s_and_b32 s93, s92, 63\n\t" \
  "s_cbranch_scc1 .Lba%=\n\t" \
  "buffer_load_dword v164, off, %[rs], %[sab] sc1\n\t" \
  "s_waitcnt vmcnt(0)\n\t" \
  "v_readfirstlane_b32 s93, v164\n\t" \
  "s_nop 3\n\t" \
  "s_cmp_eq_u32 s93, 1\n\t" \
  "s_cbranch_scc1 .Lgo%=\n\t" \
  "s_cmp_lt_u32 s92, 0x80000\n\t" \
  "s_cbranch_scc1 .Lba%=\n\t" \
  "v_mov_b32 v164, 1\n\t" \
  "buffer_store_dword v164, off, %[rs], %[sab] sc1\n\t" \
  "s_waitcnt vmcnt(0)\n\t" \
  "s_branch .Lgo%=\n\t" \
  ".Lba%=:\n\t" \
  "buffer_load_dwordx2 v[166:167], v173, %[rs], 0 offen sc1\n\t" \
  "s_branch .Lcal%=\n\t" \
  ".Lslow%=:\n\t" \
  "s_mov_b64 s[94:95], 0\n\t" \
  "v_cmp_eq_u32 vcc, %[sent], v128\n\t" \
  "s_or_b64 s[94:95], s[94:95], vcc\n\t" \
  "v_cmp_eq_u32 vcc, %[sent], v129\n\t" \
  "s_or_b64 s[94:95], s[94:95], vcc\n\t" \
  "v_cmp_eq_u32 vcc, %[sent], v130\n\t" \
  "s_or_b64 s[94:95], s[94:95], vcc\n\t" \
  "v_cmp_eq_u32 vcc, %[sent], v131\n\t" \
  "s_or_b64 s[94:95], s[94:95], vcc\n\t" \
  "v_cmp_eq_u32 vcc, %[sent], v132\n\t" \
  "s_or_b64 s[94:95], s[94:95], vcc\n\t" \
  "v_cmp_eq_u32 vcc, %[sent], v133\n\t" \
  "s_or_b64 s[94:95], s[94:95], vcc\n\t" \
  "v_cmp_eq_u32 vcc, %[sent], v134\n\t" \
  "s_or_b64 s[94:95], s[94:95], vcc\n\t" \
  "v_cmp_eq_u32 vcc, %[sent], v135\n\t" \
  "s_or_b64 s[94:95], s[94:95], vcc\n\t" \
  "v_cmp_eq_u32 vcc, %[sent], v136\n\t" \
  "s_or_b64 s[94:95], s[94:95], vcc\n\t" \
  "v_cmp_eq_u32 vcc, %[sent], v137\n\t" \
  "s_or_b64 s[94:95], s[94:95], vcc\n\t" \
  "v_cmp_eq_u32 vcc, %[sent], v138\n\t" \
  "s_or_b64 s[94:95], s[94:95], vcc\n\t" \
  "v_cmp_eq_u32 vcc, %[sent], v139\n\t" \
  "s_or_b64 s[94:95], s[94:95], vcc\n\t" \
  "v_cmp_eq_u32 vcc, %[sent], v140\n\t" \
  "s_or_b64 s[94:95], s[94:95], vcc\n\t" \
  "v_cmp_eq_u32 vcc, %[sent], v141\n\t" \
  "s_or_b64 s[94:95], s[94:95], vcc\n\t" \
  "v_cmp_eq_u32 vcc, %[sent], v142\n\t" \
  "s_or_b64 s[94:95], s[94:95], vcc\n\t" \
  "v_cmp_eq_u32 vcc, %[sent], v143\n\t" \
  "s_or_b64 s[94:95], s[94:95], vcc\n\t" \
  "s_cbranch_scc0 .Lok%=\n\t" \
  "s_bitset1_b64 %[fm], %[step]\n\t" \
  "s_add_u32 s92, s92, 1\n\t" \
  "s_sleep 1\n\t" \
  "s_and_b32 s93, s92, 63\n\t" \
  "s_cbranch_scc1 .Lre%=\n\t" \
  "buffer_load_dword v164, off, %[rs], %[sab] sc1\n\t" \
  "s_waitcnt vmcnt(0)\n\t" \
  "v_readfirstlane_b32 s93, v164\n\t" \
  "s_nop 3\n\t" \
  "s_cmp_eq_u32 s93, 1\n\t" \
  "s_cbranch_scc1 .Lok%=\n\t" \
  "s_cmp_lt_u32 s92, 0x80000\n\t" \
  "s_cbranch_scc1 .Lre%=\n\t" \
  "v_mov_b32 v164, 1\n\t" \
  "buffer_store_dword v164, off, %[rs], %[sab] sc1\n\t" \
  "s_waitcnt vmcnt(0)\n\t" \
  "s_branch .Lok%=\n\t" \
  ".Lre%=:\n\t" \
  "buffer_load_dwordx4 v[128:131], v234, %[rs], %[so] offen sc1\n\t" \
  "buffer_load_dwordx4 v[132:135], v235, %[rs], %[so] offen sc1\n\t" \
  "buffer_load_dwordx4 v[136:139], v236, %[rs], %[so] offen sc1\n\t" \
  "buffer_load_dwordx4 v[140:143], v237, %[rs], %[so] offen sc1\n\t" \
  "v_mov_b64 v[144:145], 0\n\t" \
  "v_mov_b64 v[146:147], 0\n\t" \
  "v_mov_b64 v[148:149], 0\n\t" \
  "v_mov_b64 v[150:151], 0\n\t" \
  "v_mov_b64 v[152:153], 0\n\t" \
  "v_mov_b64 v[154:155], 0\n\t" \
  "v_mov_b64 v[156:157], 0\n\t" \
  "v_mov_b64 v[158:159], 0\n\t" \
  "s_waitcnt vmcnt(0)\n\t" \
  "s_branch .Lw%=\n\t" \
  ".Lend%=:\n\t"

typedef __attribute__((ext_vector_type(32))) unsigned u32x32;
__device__ __forceinline__ void put4(u32x32& W, int i, bf16x8 f) {
  const u32x4 v = __builtin_bit_cast(u32x4, f);
#pragma unroll
  for (int j = 0; j < 4; ++j) W[4 * i + j] = v[j];
}
__device__ __forceinline__ bf16x8 get4(const u32x32& W, int i) {
  return as_bf16x8((u32x4){W[4 * i], W[4 * i + 1], W[4 * i + 2], W[4 * i + 3]});
}
__device__ __forceinline__ u32x4 raw_rsrc(const void* p, unsigned bytes) {
  const unsigned long long a = (unsigned long long)p;
  return (u32x4){(unsigned)a, (unsigned)(a >> 32) & 0xffffu, bytes, 0x00020000u};
}
__device__ __forceinline__ unsigned get_target(const char* lds, int s) {
  if (s == 0) return now_q();
  lds_vu_ptr tg = lds_at(lds);
#pragma unroll 1
  for (int i = 0; i < 8192; ++i) {
    if (__builtin_amdgcn_readfirstlane(tg[0]) == (unsigned)s) break;
    __builtin_amdgcn_s_sleep(1);
  }
  const unsigned t = __builtin_amdgcn_readfirstlane(tg[1]);
#pragma unroll 1
  for (int i = 0; i < 256 && (int)(t - now_q()) > 128; ++i) __builtin_amdgcn_s_sleep(2);
  return t;
}
#define CLB8(a, b, c, d, e, f, g, h) "v" #a, "v" #b, "v" #c, "v" #d, "v" #e, "v" #f, "v" #g, "v" #h
#define CLB_S "s90", "s91", "s92", "s93", "s94", "s95", "vcc", "scc", "memory"
#define W_IN "{v[0:31]}"(W[0]), "{v[32:63]}"(W[1]), "{v[64:95]}"(W[2]), "{v[96:127]}"(W[3]), "{v[128:159]}"(W[4]), "{v[160:191]}"(W[5])

#define W_DELAY 60
#define J0 0
constexpr int L_MEM = 0, L_KT = 16384, L_QX = 24576, L_KX = 26624, L_VX = 16384, L_MISC = 30720, L_WL = 32768;

#define QK_STEP(TXT)                                                                                                        \
  asm volatile(TXT                                                                                                          \
      : [tout] "=&s"(tout), [spins] "=&s"(spins), [tb1] "=&s"(tb1), "+{v248}"(aL16), "+{v249}"(aMW), "+{v250}"(aWL),                            \
        "+{v251}"(aIDX), "+{v252}"(aQW), "+{v[232:235]}"(QF)                                                                 \
      : W_IN, [rs] "s"(rs4), [som] "s"(o_mem(pm, s) + (WV >> 2) * 16384 + (WV & 3) * 2048), [tgt] "s"(tgt), [sent] "s"(DSENT), [sab] "s"(SY_ABORT), \
        [lead] "s"(lead), [step] "s"(s), [sbc] "s"(SY_BA + side(pm, 1) + h * 128), [nh] "s"(0xFF00FF00FF00FF00ull), [up] "s"(0xFFFFFFFF00000000ull), [negc] "s"(-cexp),      \
        [c] "s"(cexp), [so] "s"(o_o(pm, s, h) + WV * 512), [sk] "s"(o_v(pm, s, h) + 2048 + (WV & 3) * 512),                    \
        [sv] "s"(o_v(pm, s, h) + WV * 512), [rt] "s"(rt4), [svt] "s"(t_off(p, T_V + kseg(s, h)) + WV * 2048)                  \
      : CLB8(192, 193, 194, 195, 196, 197, 198, 199), CLB8(200, 201, 202, 203, 204, 205, 206, 207),                          \
        CLB8(208, 209, 210, 211, 212, 213, 214, 215), CLB8(216, 217, 218, 219, 220, 221, 222, 223),                          \
        CLB8(224, 225, 226, 227, 228, 229, 230, 231), CLB8(236, 237, 238, 239, 240, 241, 242, 243), "v244", "v245", "v246", "v247", "v253", CLB_S)

template <int WV>
__device__ __forceinline__ void qk_wave(Args& p, int h, char* smem) {
  const int tid = threadIdx.x;
  unsigned pm;
  u32x32 W[6];
  {
    int lane = tid & 63;
    asm volatile("" : "+v"(lane));
    const unsigned vo = (WV * 4 + (lane >> 4)) * (HD * 2) + 16 * ((lane & 15) ^ swzB((WV * 4 + (lane >> 4)) & 15));
    auto stage = [&](int j) {
#pragma unroll
      for (int mm = 0; mm < 2; ++mm) {
        const char* src = (const char*)((mm ? p.Wk : p.Wq) + (size_t)((j + J0) & 7) * 128 * HD + h * DH);
        const unsigned dst = lds_off(smem) + L_WL + (j & 1) * 65536 + mm * 32768 + WV * 1024;
        glds2(src, 32 * HD * 2, vo, dst);
        glds2(src + 64 * HD * 2, 32 * HD * 2, vo, dst + 16384);
      }
    };
    bf16x8 keep[4][4];
    stage(0); stage(1);
    pm = side_mask<true>(p.ws); p.s0 = pm >> 31;
#pragma unroll
    for (int j = 0; j < 8; ++j) {
      if (j < 7) WAIT_V(8); else WAIT_V(0);
      LBAR();
#pragma unroll
      for (int k = 0; k < 8; ++k) {
        const int sl = (2 * (j + J0) + (k >> 2) - 2 * WV) & 15;
        const bf16x8 fr = ld_bfrag(smem + L_WL + (j & 1) * 65536 + (WV >> 2) * 32768 + (k >> 2) * 16384, k & 1, 2 * (WV & 3) + ((k >> 1) & 1), lane);
        const int f = ((sl >> 2) * 3 + (sl & 3)) * 4 + (k & 3);
        if ((sl & 3) != 3) put4(W[f >> 3], f & 7, fr);
        else if (j < 2) *reinterpret_cast<bf16x8*>(smem + WV * 4096 + (k & 3) * 1024 + lane * 16) = fr;
        else keep[sl >> 2][k & 3] = fr;
      }
      LBAR();
      if (j + 2 < 8) stage(j + 2);
    }
    constexpr int PK = (((1 - 2 * WV) & 3) == 3 ? (2 * J0 + 1 - 2 * WV) & 15 : (2 * J0 + 3 - 2 * WV) & 15) >> 2;
#pragma unroll
    for (int u = 0; u < 2; ++u) {
#pragma unroll
      for (int i = 0; i < 16; ++i) {
        if (((i >> 2) == PK) != (u == 1)) continue;
        *reinterpret_cast<bf16x8*>(smem + L_WL + WV * 16384 + (i >> 2) * 4096 + (i & 1) * 2048 + ((i >> 1) & 1) * 1024 + lane * 16) =
            u ? ldsf(smem + WV * 4096 + (i & 3) * 1024 + lane * 16) : keep[i >> 2][i & 3];
      }
      SCHED();
    }
    LBAR();
    RDY(h);
    if (WV == 4 || WV == 5) {
      wait_flags(mkrsrc(p.ws + WS_SY, SY_TOTAL), fkq(0, h, (lane & 1) * 2), (gu32*)(p.ws + WS_SY + SY_ABORT));
      const char* src = p.ws + WS_T + t_off(p, kseg(0, h)) + (WV - 4) * 4096 + lane * 16;
#pragma unroll
      for (int i = 0; i < 4; ++i) glds16_sc1(src + i * 1024, smem + L_KT + (WV - 4) * 4096 + i * 1024);
    }
    if (tid < 8) reinterpret_cast<unsigned*>(smem + L_MISC)[tid] = 0;
  }
  const __amdgpu_buffer_rsrc_t rsy = mkrsrc(p.ws + WS_SY, SY_TOTAL);
  const u32x4 rs4 = raw_rsrc(p.ws + WS_SY, SY_TOTAL);
  const u32x4 rt4 = raw_rsrc(p.ws + WS_T, T_BYTES);
  gu32* abortw = (gu32*)(p.ws + WS_SY + SY_ABORT);
  const float cexp = 0.08838834764831845f * 1.4426950408889634f;
  constexpr int m = WV >> 2, b = WV & 3;
  bool fok = false;
  int lead = 380;
  unsigned long long fm = 0, ps = 0; (void)fm; (void)ps;
  for (int s = 0; s < NSEG; ++s) {
    int lane = tid & 63;
    asm volatile("" : "+v"(lane));
    const int r = lane & 15, q = lane >> 4;
    unsigned aL16 = lane * 16, aMW = aL16 + WV * 2048, aWL = aL16 + L_WL + WV * 16384;
    unsigned aIDX = (lane & 2) ? 0xEEEE : 0x4444;
    unsigned aQW = (32 * b + r) * 16 + q * 4 + (m ? L_KX : L_QX);
    u32x4 QF;
    asm volatile("" : "={v[232:235]}"(QF));
    if (WV < 4) {
      QF[0] = L_QX + (q * 8 + (r >> 2)) * 16 + (r & 1) * 8;
      QF[1] = ((2 * (q & 1) + (q >> 1)) * 16 + r) * 4;
      QF[2] = ((r >> 2) & 1) * 65536 + ((r >> 1) & 1) * 64 + (r >> 3) * 32 + (r & 1) * 16 + q * 4;
    } else QF[3] = r * 16 + q * 4;
    const unsigned tgt = get_target(smem + L_MISC, s);
    unsigned tout, spins, tb1;
    if (WV == 0) QK_STEP(QK_ASM_0); else if (WV == 1) QK_STEP(QK_ASM_1); else if (WV == 2) QK_STEP(QK_ASM_2);
    else if (WV == 3) QK_STEP(QK_ASM_3); else if (WV == 4) QK_STEP(QK_ASM_4); else if (WV == 5) QK_STEP(QK_ASM_5);
    else if (WV == 6) QK_STEP(QK_ASM_6); else QK_STEP(QK_ASM_7);
    tout = __builtin_amdgcn_readfirstlane(tout);
    lane = tid & 63;
    asm volatile("" : "+v"(lane));
    if (WV < 4) {
      lds_at(smem + L_MISC + 8)[WV] = s + 1;
      if (WV == 3) {
        const int x = (int)(time_of(tout) - __builtin_amdgcn_readfirstlane(tb1));
        lead = __builtin_amdgcn_readfirstlane(track(lead, x, LEAD_UP, s < 4 ? 40 : LEAD_DN, 60));
        SMP(2, s, x); SMP(3, s, lead);
      }
    } else if (WV <= 5) {
      if (s + 2 < NSEG) {
        if ((s & 3) == 3 && !fok) wait_flags(rsy, fkq((s + 1) >> 2, h, (lane & 1) * 2), abortw);
        if ((s & 3) == 2) fok = __all(ld_flag(rsy, fkq(((s + 2) >> 2) & 15, h, (lane & 1) * 2)) != SENT);
      }
    }
    if (WV >= 4 && WV <= 6 && s + 1 < NSEG) {
      unsigned t;
      do {
        __builtin_amdgcn_s_sleep(1);
        t = lds_at(smem + L_MISC + 8)[lane & 3];
      } while (!__all(t == (unsigned)(s + 1)));
      if (WV <= 5) {
        if (s + 2 < NSEG) {
          const char* src = p.ws + WS_T + t_off(p, kseg(s + 1, h)) + (WV - 4) * 4096 + lane * 16;
#pragma unroll
          for (int i = 0; i < 4; ++i) glds16_sc1(src + i * 1024, smem + L_KT + (WV - 4) * 4096 + i * 1024);
        }
      } else {
        watch(rsy, s, lane, now_q() + W_DELAY, (const unsigned*)(smem + L_MISC), abortw, side(pm, 1));
      }
    }
  }
}

__device__ __forceinline__ void qk_role(Args& p, int h, char* smem) {
  switch (__builtin_amdgcn_readfirstlane(threadIdx.x >> 6)) {
    case 0: qk_wave<0>(p, h, smem); break;
    case 1: qk_wave<1>(p, h, smem); break;
    case 2: qk_wave<2>(p, h, smem); break;
    case 3: qk_wave<3>(p, h, smem); break;
    case 4: qk_wave<4>(p, h, smem); break;
    case 5: qk_wave<5>(p, h, smem); break;
    case 6: qk_wave<6>(p, h, smem); break;
    default: qk_wave<7>(p, h, smem); break;
  }
}

#define CLB32(a) CLB32_##a
#define CLB32_128 CLB8(128, 129, 130, 131, 132, 133, 134, 135), CLB8(136, 137, 138, 139, 140, 141, 142, 143), CLB8(144, 145, 146, 147, 148, 149, 150, 151), CLB8(152, 153, 154, 155, 156, 157, 158, 159)
#define CLB32_160 CLB8(160, 161, 162, 163, 164, 165, 166, 167), CLB8(168, 169, 170, 171, 172, 173, 174, 175), CLB8(176, 177, 178, 179, 180, 181, 182, 183), CLB8(184, 185, 186, 187, 188, 189, 190, 191)
#define W4_IN "{v[0:31]}"(W[0]), "{v[32:63]}"(W[1]), "{v[64:95]}"(W[2]), "{v[96:127]}"(W[3])
#define LEAD_V 200

constexpr int V_VM = 16384, V_T = 32768, V_KI = 131072;
__device__ __forceinline__ void tok_rows(const Args& p, __amdgpu_buffer_rsrc_t rsy, unsigned pm, int sg, int h, int T, int eh, char* smem, char* img,
                                         int lane, gu32* abortw, unsigned tk) {
  const float scale = 0.08838834764831845f, LOG2E = 1.4426950408889634f;
  const __amdgpu_buffer_rsrc_t rt = mkrsrc(p.ws + WS_T, T_BYTES);
  asm volatile("" : "+v"(lane));
  const int r = lane & 15, q = lane >> 4;
  u32x4 km[2];
  if (tk) pace_until(tk);
#pragma unroll
  for (int i = 0; i < 2; ++i) km[i] = __builtin_amdgcn_raw_buffer_load_b128(rsy, lane * 16 + i * 1024, o_v(pm, sg, h) + 2048, 16);
  SCHED();
  const char* tl = smem + V_T + (sg & 3) * 24576 + lane * 16;
  bf16x8 qt[4], vt[4];
  unsigned vm[4];
  f32x4 d0 = {0.f, 0.f, 0.f, 0.f}, d1 = d0, d2 = d0;
#pragma unroll
  for (int i = 0; i < 4; ++i) qt[i] = ldsf(tl + 8192 + T * 4096 + i * 1024);
  {
    bf16x8 kf[8];
#pragma unroll
    for (int i = 0; i < 8; ++i) kf[i] = ldsf(tl + i * 1024);
    SCHED();
#pragma unroll
    for (int bb = 0; bb < 4; ++bb) { d0 = MFMA(kf[bb], qt[bb], d0); d1 = MFMA(kf[4 + bb], qt[bb], d1); }
  }
#pragma unroll
  for (int i = 0; i < 4; ++i) {
    vt[i] = ldsf(tl + 16384 + eh * 4096 + i * 1024);
    vm[i] = *reinterpret_cast<const unsigned*>(smem + V_VM + (sg & 1) * 2048 + eh * 1024 + i * 256 + lane * 4);
  }
  SCHED();
  for (unsigned sp = 0; !__all(!has_ds(km[0]) && !has_ds(km[1]));) {
    __builtin_amdgcn_s_sleep(4);
    if (spin_fail(sp, abortw)) break;
    asm volatile("" ::: "memory");
#pragma unroll
    for (int i = 0; i < 2; ++i) km[i] = __builtin_amdgcn_raw_buffer_load_b128(rsy, lane * 16 + i * 1024, o_v(pm, sg, h) + 2048, 16);
  }
#pragma unroll
  for (int i = 0; i < 2; ++i)
#pragma unroll
    for (int j = 0; j < 4; ++j) *reinterpret_cast<unsigned*>(img + (i * 64 + lane) * 32 + j * 8) = km[i][j];
  asm volatile("s_waitcnt lgkmcnt(0)" ::: "memory");
  const char* lk = img + (q * 8 + (r >> 2)) * 32 + (r & 3) * 8;
  {
    bf16x8 kk[4];
#pragma unroll
    for (int bb = 0; bb < 4; ++bb) {
      s16x4 lo = __builtin_amdgcn_ds_read_tr16_b64_v4i16((lds_s16x4_ptr)(lk + bb * 1024));
      s16x4 hi = __builtin_amdgcn_ds_read_tr16_b64_v4i16((lds_s16x4_ptr)(lk + bb * 1024 + 128));
      kk[bb] = (bf16x8){lo[0], lo[1], lo[2], lo[3], hi[0], hi[1], hi[2], hi[3]};
    }
    SCHED();
#pragma unroll
    for (int bb = 0; bb < 4; ++bb) d2 = MFMA(kk[bb], qt[bb], d2);
  }
  const int qi = 16 * T + r;
  float v[10];
#pragma unroll
  for (int j = 0; j < 4; ++j) { v[j] = d0[j]; v[4 + j] = d1[j]; }
  v[8] = d2[0]; v[9] = d2[1];
#pragma unroll
  for (int j = 0; j < 5; ++j) {
    unsigned t = cvtpk(v[2 * j], v[2 * j + 1]);
    t = cvtpk(lo_f(t) * scale, hi_f(t) * scale);
    v[2 * j] = lo_f(t); v[2 * j + 1] = hi_f(t);
  }
#pragma unroll
  for (int j = 0; j < 8; ++j) {
    const int key = (j >> 2) * 16 + 4 * q + (j & 3);
    v[j] = key <= qi ? v[j] : -INFINITY;
  }
  float mx = v[0];
#pragma unroll
  for (int j = 1; j < 10; ++j) mx = fmax2(mx, v[j]);
  mx = rowq_max(mx) * LOG2E;
  float sm = 0.f;
#pragma unroll
  for (int j = 0; j < 10; ++j) { v[j] = __builtin_amdgcn_exp2f(__builtin_fmaf(v[j], LOG2E, -mx)); sm += v[j]; }
  const float inv = __builtin_amdgcn_rcpf(rowq_sum(sm));
  u32x4 o = {cvtpk(v[0] * inv, v[1] * inv), cvtpk(v[2] * inv, v[3] * inv), cvtpk(v[4] * inv, v[5] * inv),
             cvtpk(v[6] * inv, v[7] * inv)};
  const s16x4 pmem = __builtin_bit_cast(s16x4, (u32x2){cvtpk(v[8] * inv, v[9] * inv), 0u});
  const int ob = t_off_o(p, T_O + (sg * 2 + T) * 16 + (h >> 1)) + (h & 1) * 4096 + eh * 2048;
#pragma unroll
  for (int j = 0; j < 2; ++j) {
    f32x4 d[2];
#pragma unroll
    for (int u = 0; u < 2; ++u) {
      d[u] = MFMA(vt[2 * j + u], as_bf16x8(o), ((f32x4){0.f, 0.f, 0.f, 0.f}));
      d[u] += __builtin_amdgcn_mfma_f32_16x16x16bf16_1k(__builtin_bit_cast(s16x4, (u32x2){vm[2 * j + u], 0u}), pmem, (f32x4){0.f, 0.f, 0.f, 0.f}, 0, 0, 0);
    }
    st_sc1(rt, ob + lane * 16 + j * 1024, pack32(d[0], d[1]));
  }
}

__device__ __forceinline__ void vu_role(Args& p, int h, char* smem) {
  const int tid = threadIdx.x, lane = tid & 63;
  const int w = __builtin_amdgcn_readfirstlane(tid >> 6);
  u32x32 W[4];
  {
    const unsigned vo = (tid >> 4) * (HD * 2) + 16 * ((lane & 15) ^ swzB((tid >> 4) & 15));
    auto stage = [&](int j) {
      const char* src = (const char*)(p.Wv + (size_t)j * 128 * HD + h * DH);
      const unsigned dst = lds_off(smem) + L_WL + (j & 3) * 32768 + w * 1024;
      glds2(src, 32 * HD * 2, vo, dst);
      glds2(src + 64 * HD * 2, 32 * HD * 2, vo, dst + 16384);
    };
#pragma unroll
    for (int j = 0; j < 4; ++j) stage(j);
#pragma unroll
    for (int j = 0; j < 8; ++j) {
      if (j < 5) WAIT_V(12); else if (j == 5) WAIT_V(8); else if (j == 6) WAIT_V(4); else WAIT_V(0);
      LBAR();
#pragma unroll
      for (int k = 0; k < 4; ++k) put4(W[j >> 1], (j & 1) * 4 + k, ld_bfrag(smem + L_WL + (j & 3) * 32768 + (k >> 1) * 16384, k & 1, w, lane));
      LBAR();
      if (j + 4 < 8) stage(j + 4);
    }
    if (tid < 4) reinterpret_cast<unsigned*>(smem + L_MISC)[tid] = 0;
    RDY(32 + h);
  }
  const __amdgpu_buffer_rsrc_t rsy = mkrsrc(p.ws + WS_SY, SY_TOTAL);
  const u32x4 rs4 = raw_rsrc(p.ws + WS_SY, SY_TOTAL);
  gu32* abortw = (gu32*)(p.ws + WS_SY + SY_ABORT);
  const unsigned pm = side_mask<true>(p.ws);
  p.s0 = pm >> 31;
  unsigned long long fm = 0; (void)fm;
  unsigned fnext = SENT;
  const int fo = SY_FO + ((w & 3) * NH + h) * 128;
#pragma unroll 1
  for (int s = 0; s < NSEG; ++s) {
    const unsigned tgt = get_target(smem + L_MISC, s);
    unsigned spins;
    asm volatile(VV_ASM
        : [spins] "=&s"(spins)
        : W4_IN, "{v200}"(lane * 16), "{v201}"(lane * 16 + w * 2048), "{v202}"((lane & 2) ? 0xEEEE : 0x4444), "{v203}"(lane * 4),
          "{v204}"(tid * 4 + V_VM + (s & 1) * 2048), [rs] "s"(rs4), [som] "s"(o_mem(pm, s) + (w >> 2) * 16384 + (w & 3) * 2048), [tgt] "s"(tgt),
          [sent] "s"(DSENT), [sab] "s"(SY_ABORT), [sv] "s"(o_v(pm, s, h) + w * 256)
        : CLB32(128), CLB32(160), CLB8(192, 193, 194, 195, 196, 197, 198, 199), CLB_S);
    if (w < 4 ? s >= 1 : s == NSEG - 1) {
      if (s == NSEG - 1 && w < 4 && lane == 0) __builtin_amdgcn_raw_buffer_store_b32(1u, rsy, fo, 0, 16);
      tok_rows(p, rsy, pm, s - (w < 4), h, w & 1, (w >> 1) & 1, smem, smem + V_KI + w * 4096, lane, abortw, w < 4 ? 0 : now_q() + 160);
    } else if (w == 7) {
      SMP(6, s, (int)(now_q() - tgt));
      watch(rsy, s, lane, now_q() + LEAD_V + W_DELAY, (const unsigned*)(smem + L_MISC), abortw, side(pm, 1));
    } else if (w >= 4) {
      __builtin_amdgcn_s_sleep(2);
      const int s1 = w == 5 ? (s == NSEG - 2 ? s + 1 : s) : s + 1;
#pragma unroll 1
      for (int sg = (w == 5 || s == 0) ? s : s + 1; sg <= s1; ++sg) {
        if ((sg & 3) == 0 && fnext == SENT) wait_flags(rsy, fkq(sg >> 2, h, w - 4), abortw);
        const char* src = p.ws + WS_T + t_off(p, (w - 4) * T_Q + kseg(sg, h)) + lane * 16;
#pragma unroll
        for (int i = 0; i < 8; ++i) glds16_sc1(src + i * 1024, smem + V_T + (sg & 3) * 24576 + (w - 4) * 8192 + i * 1024);
        if ((sg & 3) == 3) fnext = __builtin_amdgcn_readfirstlane(ld_flag(rsy, fkq(((sg + 1) >> 2) & 15, h, w - 4)));
      }
      if (w == 5 && s + NPRE < NSEG) {
#pragma unroll
        for (int i = 0; i < 4; ++i) st_sc1(rsy, o_v(pm, s + NPRE, h) + i * 1024 + lane * 16, (u32x4){DSENT, DSENT, DSENT, DSENT});
      }
    }
  }
  WAIT_V(0);
  if (lane == 0) __builtin_amdgcn_raw_buffer_store_b32(1u, rsy, fo + (1 + (w >> 2)) * 4 * NH * 128, 0, 16);
  if (h == 3 && (w & 2) == 0) { LST(351 + (w & 1) + (w >> 2) * 2); }
}

#define RL_UP 12
#define RL_DN 4
#define RED_MIN 40
#define BA_LOOK 310
constexpr int TLAG = 3;
constexpr int R_SI = 32768, R_PART = 98304;

__device__ __forceinline__ void red_role(Args& p, int j, char* smem) {
  const int tid = threadIdx.x;
  int lane = tid & 63;
  const int w = __builtin_amdgcn_readfirstlane(tid >> 6);
  const int c = (j & 2) * 8 + (j >> 2), rh = j & 1;
  const int hg = (w + (j >> 2)) & 7, rot = (j >> 2) + (j >> 5);
  const __amdgpu_buffer_rsrc_t rsy = mkrsrc(p.ws + WS_SY, SY_TOTAL);
  const u32x4 rs4 = raw_rsrc(p.ws + WS_SY, SY_TOTAL);
  const __amdgpu_buffer_rsrc_t rt = mkrsrc(p.ws + WS_T, T_BYTES);
  gu32* abortw = (gu32*)(p.ws + WS_SY + SY_ABORT);
  u32x32 W[4];
  {
    const u16* Wo = p.Wo + (size_t)(4 * hg * DH) * DM + 32 * c;
    const int ro = ((lane >> 4) * 8 + ((lane & 15) >> 2)) * 64 + (lane & 3) * 8;
    load_blocks<16>(smem + 32768 + w * 16384,
        [&](int i, char* slot) {
          const int x = ((i >> 2) + rot) & 3;
          blk_dma(Wo + (size_t)((x >> 1) * 256 + (x & 1) * 64 + ((i >> 1) & 1) * 128 + (i & 1) * 32) * DM, DM, slot, lane);
        },
        [&](int i, char* slot) {
#pragma unroll
          for (int t = 0; t < 2; ++t) {
            s16x4 lo = __builtin_amdgcn_ds_read_tr16_b64_v4i16((lds_s16x4_ptr)(slot + ro + t * 32));
            s16x4 hi = __builtin_amdgcn_ds_read_tr16_b64_v4i16((lds_s16x4_ptr)(slot + ro + t * 32 + 256));
            put4(W[i >> 2], (i & 2) * 2 + 2 * t + (i & 1), (bf16x8){lo[0], lo[1], lo[2], lo[3], hi[0], hi[1], hi[2], hi[3]});
          }
        });
  }
  const unsigned pm = side_mask<true>(p.ws);
  p.s0 = pm >> 31;
  const int hs = side(pm, 1);
  if ((c & 14) != 6 && GPF < 3) {
#pragma unroll
    for (int i = 0; i < 4; ++i)
      touch_line((const char*)(i < 2 ? p.wg : p.wu) + (size_t)(j - (j >= 32 ? 8 : 0)) * 131072 + ((i & 1) * 8 + w) * 8192 + lane * 128, smem + w * 256);
  }
  {
    const u32x4 ds = {DSENT, DSENT, DSENT, DSENT};
#pragma unroll
    for (int i = 0; i < 2; ++i) {
      const int s = (w * 2 + i) * 4 + (lane >> 4);
      if (s >= 2) st_sc1(rsy, o_mem(pm, s) + (c >> 4) * 16384 + (c & 15) * 512 + rh * 128 + ((lane >> 3) & 1) * 256 + (lane & 7) * 16, ds);
    }
  }
  if (tid < 4) { reinterpret_cast<unsigned*>(smem + 4096)[tid] = tid == 3 ? now_q() : 0; reinterpret_cast<unsigned*>(smem + 4112)[tid] = DSENT; }
  if (tid < 256) reinterpret_cast<int*>(smem + 5120)[tid] = ((tid >> 2) - TLAG - 6) >> 2;
  LBAR();
  const unsigned base = __builtin_amdgcn_readfirstlane(lds_at(smem + 4096)[3]);
  RDY(64 + j);
  unsigned long long fm = 0, ps = 0;
  const unsigned aL = lane * 16;
  const int e = ((56 + w) * SEG + rh * 16 + (lane >> 2)) * DM + c * 32 + (lane & 3) * 8;
  u32x4 xv = *reinterpret_cast<const u32x4*>(p.x + e), av = {DSENT, DSENT, DSENT, DSENT};
  asm volatile("" : "+v"(xv));
  {
    const int r = lane & 15, q = lane >> 4;
    const int sm0 = o_mem(pm, 0) + (c >> 4) * 16384 + (c & 15) * 512 + rh * 128;
    u32x4 VO, VT;
    unsigned fo = SY_FO + ((rh + (lane & 4) / 2) * NH + 4 * hg + (lane & 3)) * 128;
    unsigned ao = w == 1 ? (c * S_LEN + rh * 16 - (TLAG + 3) * SEG) * 64 + lane * 16
                         : (w >= 4 ? SY_O + NPRE * 131072 + side(pm, w - 2) + (j >> 3) * 16384 + (j & 7) * 1024 + lane * 16
                                   : SY_FS + (2 * c + rh) * 128 + (lane < 8 ? lane * 16 : 0x70000000));
    unsigned ar = w == 1 ? R_SI - (TLAG + 2) * 1024 + lane * 16 : (w == 2 ? 5120 : 4112);
    const unsigned long long am = w == 1 ? (1ull << 62) - 64 : (w == 2 ? 0x4444444444444400ull : (w >= 4 ? (0xFFFFull << (16 * (w - 4))) >> NPRE : 0));
    static_assert(TLAG == 3, "");
    unsigned pw = R_PART + w * 2048 + lane * 16, pr = R_PART + 16384 + tid * 4;
    unsigned sw = R_SI - (TLAG + 1) * 1024 + (lane >> 2) * 64 + w * 8 + (lane & 3) * 2, stm = 8192 + w * 512;
#pragma unroll
    for (int l = 0; l < 4; ++l) {
      const int x = (l + rot) & 3;
      VO[l] = o_o(0, 0, 4 * hg) + rh * 65536 + x * 1024 + aL;
      VT[l] = (T_O + rh * 16 + 2 * hg + (x >> 1)) * 16384 + (x & 1) * 2048 + aL;
    }
    int rl = 280;
    unsigned step, E, so, st, st4, sh;
    asm volatile(RED_ASM
        : [step] "=&s"(step), [E] "=&s"(E), [so] "=&s"(so), [st] "=&s"(st), [st4] "=&s"(st4), [sh] "=&s"(sh),
          [rl] "+s"(rl), [fm] "+s"(fm), [ps] "+s"(ps),
          "+{v[224:227]}"(av), "+{v228}"(ao), "+{v229}"(ar), "+{v230}"(pw), "+{v231}"(pr), "+{v232}"(sw), "+{v233}"(stm), "+{v242}"(fo)
        : W4_IN, "{v168}"(aL), "{v169}"(w * 512 + lane * 8), "{v170}"(lane * 8),
          "{v171}"(sm0 + 32768 + (q >> 1) * 256 + (q & 1) * 64 + ((r >> 1) & 1) * 32 + (r & 1) * 16 + (r >> 2) * 4),
          "{v172}"((lane & 2) ? 0xEEEE : 0x4444), "{v173}"(lane < 4 ? SY_BA + hs + (4 * hg + lane) * 128 : 0x7F000000u),
          "{v174}"((r & 1) ? 0x03020706u : 0x05040100u), "{v175}"(SY_BR + hs + j * 128 + lane * 8), "{v[234:237]}"(VO), "{v[238:241]}"(VT), "{v243}"(e * 2),
          [rg] "s"(raw_rsrc(p.ws + WS_G, S_LEN * DM * 2)), [fg] "s"(SY_FG + ((14 + (w >> 2)) * 8 + (c >> 2)) * 128),
          [rs] "s"(rs4), [ra] "s"(w == 1 ? raw_rsrc(p.ws + WS_S, S_LEN * DM * 2) : rs4), [rt] "s"(raw_rsrc(p.ws + WS_T, T_BYTES)),
          [ds] "n"(DS_RED), [mn] "n"(RED_MIN), [lk] "n"(DS_MEM + BA_LOOK), [up] "n"(RL_UP), [dn] "n"(-RL_DN), [sent] "s"(DSENT), [sab] "s"(SY_ABORT),
          [base] "s"(base), [pm] "s"(pm), [s0] "s"((p.s0 ^ TF_O) << 13), [am] "s"(am), [ainc] "s"(w == 1 ? SEG * 64 : (w >= 4 ? 131072 : 0)),
          [rinc] "s"(w == 1 ? 1024 : (w == 2 ? 16 : 0))
        : CLB32(128), CLB8(160, 161, 162, 163, 164, 165, 166, 167), CLB8(176, 177, 178, 179, 180, 181, 182, 183),
          CLB8(184, 185, 186, 187, 188, 189, 190, 191), CLB8(192, 193, 194, 195, 196, 197, 198, 199), CLB8(200, 201, 202, 203, 204, 205, 206, 207),
          CLB8(208, 209, 210, 211, 212, 213, 214, 215), CLB8(216, 217, 218, 219, 220, 221, 222, 223), CLB_E, "s96", CLB_S);
  }
  if (j == 0 && w == 5) { LST(345); }
  LBAR();
  if (j == 0 && w == 5) { LST(346); }
  u32x4 sv;
  if (w < 3) sv = *reinterpret_cast<const u32x4*>(smem + R_SI + (56 + w) * 1024 + lane * 16);
  else {
    const f32x4* part = reinterpret_cast<const f32x4*>(smem + (w == 3 ? R_PART : (w - 3) * 16384)) +
                        (lane & 2) * 32 + (lane & 1) * 32 + (lane >> 2);
    f32x4 x[8][2];
#pragma unroll
    for (int ww = 0; ww < 8; ++ww) { x[ww][0] = part[ww * 128]; x[ww][1] = part[ww * 128 + 16]; }
#pragma unroll
    for (int u = 0; u < 2; ++u) {
      const f32x4 a = ((x[0][u] + x[1][u]) + (x[2][u] + x[3][u])) + ((x[4][u] + x[5][u]) + (x[6][u] + x[7][u]));
      sv[2 * u] = cvtpk(a[0], a[1]); sv[2 * u + 1] = cvtpk(a[2], a[3]);
    }
  }
  *reinterpret_cast<u32x4*>(p.out + e) = combine(sv, av, xv);
  if (tid == 128) { LST(400 + j); }
}

__global__ __launch_bounds__(512, 2) void main_kernel(Args p) {
  char* smem = g_smem;
  p.ws = ws_align(p.ws);
  const int bx = blockIdx.x & 7, bk = blockIdx.x >> 3;
  const bool isg = (bx ^ XPROBE) & 4;
  const int b = (isg ? NCHAIN : 0) + bk * 4 + (bx & 3);
  if (b == 64 && threadIdx.x == 0) { LST(500); }
  if (b >= NCHAIN) { p.s0 = side_mask<false>(p.ws) >> 31; gemm_role(p, b - NCHAIN, smem); }
  else if (b < 32) qk_role(p, (b & 3) * 8 + bk, smem);
  else if (b < 64) vu_role(p, (b & 3) * 8 + bk - 8, smem);
  else red_role(p, b - 64, smem);
}

extern "C" void kernel_launch(void* const* d_in, const int* in_sizes, int n_in,
                              void* d_out, int out_size, void* d_ws, size_t ws_size,
                              hipStream_t stream) {
  Args a;
  a.x = (const u16*)d_in[0]; a.mem0 = (const u16*)d_in[1];
  a.Wq = (const u16*)d_in[2]; a.Wk = (const u16*)d_in[3]; a.Wv = (const u16*)d_in[4]; a.Wo = (const u16*)d_in[5];
  a.wg = (const u16*)d_in[8]; a.wu = (const u16*)d_in[9]; a.wd = (const u16*)d_in[10];
  a.ws = (char*)d_ws; a.out = (u16*)d_out; a.s0 = 0; a.pad1 = 0;
  ln_kernel<<<S_LEN / 8 + 8 * NPAGE, 512, 0, stream>>>(a.x, a.ws, a.mem0);
  main_kernel<<<256, 512, 0, stream>>>(a);
}
```

```cpp
#include <hip/hip_runtime.h>
#include <hip/hip_bf16.h>
#include <stdint.h>

constexpr int S_LEN = 2048, DM = 1024, NH = 32, DH = 128, NMEM = 8, SEG = 32;
constexpr int NSEG = S_LEN / SEG;
constexpr int HD = NH * DH;
constexpr int FF = 3584;

typedef unsigned short u16;
typedef __attribute__((ext_vector_type(8))) short bf16x8;
typedef __attribute__((ext_vector_type(4))) short s16x4;
typedef __attribute__((ext_vector_type(4))) float f32x4;
typedef __attribute__((ext_vector_type(4))) unsigned int u32x4;
typedef __attribute__((ext_vector_type(2))) unsigned int u32x2;
typedef __attribute__((address_space(3))) s16x4* lds_s16x4_ptr;
typedef __attribute__((address_space(3))) void* lds_void_ptr;
typedef __attribute__((address_space(1))) const void* glb_cvoid_ptr;
typedef __attribute__((address_space(1))) unsigned gu32;
typedef __bf16 bf16x2_t __attribute__((ext_vector_type(2)));
typedef float f32x2_t __attribute__((ext_vector_type(2)));

__device__ __forceinline__ float bf2f(u16 v) { return __uint_as_float(((unsigned)v) << 16); }
__device__ __forceinline__ unsigned cvtpk(float lo, float hi) {
  bf16x2_t r = __builtin_convertvector((f32x2_t){lo, hi}, bf16x2_t);
  return __builtin_bit_cast(unsigned, r);
}
__device__ __forceinline__ float lo_f(unsigned v) { return __uint_as_float(v << 16); }
__device__ __forceinline__ float hi_f(unsigned v) { return __uint_as_float(v & 0xffff0000u); }
__device__ __forceinline__ float rbf(float f) { return lo_f(cvtpk(f, f)); }
__device__ __forceinline__ bf16x8 as_bf16x8(u32x4 v) { return __builtin_bit_cast(bf16x8, v); }
#define MFMA(a, b, c) __builtin_amdgcn_mfma_f32_16x16x32_bf16((a), (b), (c), 0, 0, 0)
typedef __attribute__((ext_vector_type(16))) short bf16x16;
typedef __attribute__((ext_vector_type(8))) __bf16 bfv8;
typedef __attribute__((ext_vector_type(16))) __bf16 bfv16;
#define SMFMA(a, b, c, idx) __builtin_amdgcn_smfmac_f32_16x16x64_bf16(__builtin_bit_cast(bfv8, (a)), __builtin_bit_cast(bfv16, (b)), (c), (idx), 0, 0)
__device__ __forceinline__ bf16x16 cat16(bf16x8 lo, bf16x8 hi) {
  return __builtin_shufflevector(lo, hi, 0, 1, 2, 3, 4, 5, 6, 7, 8, 9, 10, 11, 12, 13, 14, 15);
}

#define KSTR 65
#define XLD 1024
constexpr int PAGE = 2 << 20, NPAGE = 14;
constexpr int T_K = 0, T_Q = 2304, T_V = 4608, T_O = 6912, T_END = T_O + 2048;
constexpr unsigned T_BYTES = T_END * 16384u;
constexpr size_t WS_SY = 0;
constexpr size_t WS_T = (size_t)NPAGE * PAGE;
constexpr size_t WS_XN = WS_T + T_BYTES;
constexpr size_t WS_HF = WS_XN + (size_t)S_LEN * XLD * 2;
constexpr size_t WS_G = WS_HF + (size_t)S_LEN * 16384;
constexpr size_t WS_S = WS_G + (size_t)S_LEN * DM * 2;
constexpr size_t WS_DP = WS_S + (size_t)S_LEN * DM * 2;
constexpr size_t WS_END = WS_DP + (size_t)64 * 65536;
constexpr unsigned SENT = 0xFFFFFFFFu;
constexpr unsigned DSENT = 0xFFBFFFBFu;
constexpr int SY_MEM = 0;
constexpr int SY_O = 2 * PAGE;
constexpr int SY_V = 6 * PAGE;
constexpr int SY_BR = PAGE, SY_BA = SY_BR + 16384;
constexpr int SY_FKQ = SY_BR + 32768;
constexpr int SY_FG = SY_FKQ + 16 * NH * 4 * 128;
constexpr int SY_FO = SY_FG + 16 * 8 * 128;
constexpr int SY_CH = SY_FO + 384 * 128;
constexpr int SY_TK = SY_CH + 16 * 128;
constexpr int SY_Q = SY_TK + 64 * 256;
constexpr int SY_ABORT = SY_Q + 8 * 128;
constexpr int SY_FS = SY_ABORT + 256;
constexpr int SY_FEND = SY_FS + 64 * 128;
constexpr int SY_PAR = SY_FEND;
constexpr int SY_TOTAL = NPAGE * PAGE;
constexpr int PROBE = PAGE - 16384 + 6144;
__device__ __forceinline__ int side(unsigned pm, int page) { return ((pm >> page) & 1u) << 13; }
__device__ __forceinline__ int o_mem(unsigned pm, int s) { return SY_MEM + s * 32768 + side(pm, 0); }
__device__ __forceinline__ int o_o(unsigned pm, int s, int h) {
  return SY_O + s * 131072 + (h >> 3) * 16384 + ((h >> 1) & 3) * 2048 + (h & 1) * 128 + side(pm, 2 + (s >> 4));
}
__device__ __forceinline__ int o_v(unsigned pm, int s, int h) { return SY_V + s * 262144 + (h >> 1) * 16384 + (h & 1) * 4096 + side(pm, 6 + (s >> 3)); }
__device__ __forceinline__ char* ws_align(char* ws) { return ws + ((PAGE - (unsigned)(uintptr_t)ws) & (PAGE - 1)); }
#define NFUSE 8
constexpr int NPRE = 4;
static_assert(WS_END + PAGE <= (size_t)256 << 20 && NH * KSTR <= T_Q && NPAGE % 2 == 0, "");
constexpr int NCHAIN = 128, NGW = 32;
__host__ __device__ constexpr int kseg(int sg, int h) { return h * KSTR + sg; }
__host__ __device__ constexpr int fkq(int blk, int h, int kind) { return SY_FKQ + ((blk * NH + h) * 4 + kind) * 128; }

#define HSKEW 0
#define XPROBE 4
struct Args {
  const u16 *x, *mem0, *Wq, *Wk, *Wv, *Wo, *wg, *wu, *wd;
  char* ws;
  u16* out;
  int s0, pad1;
};
constexpr int HLD = 16384 + HSKEW;
__device__ __forceinline__ int h_off(const Args& p, int rblk) { return rblk * PAGE + (((p.s0 ^ rblk ^ 1) & 1) << 13); }
static_assert(WS_HF % (2 * PAGE) == 0 && 127 * HSKEW + FF * 2 <= 8192, "");
#define TF_KQV 1
#define TF_O 0
__device__ __forceinline__ int t_off(const Args& p, int t) { return t * 16384 + (((p.s0 ^ (t >> 7) ^ (TF_KQV == 2 ? t : TF_KQV)) & 1) << 13); }
__device__ __forceinline__ int t_off_o(const Args& p, int t) { return t * 16384 + (((p.s0 ^ (t >> 7) ^ (TF_O == 2 ? t : TF_O)) & 1) << 13); }

#define LN_AUX 0
__device__ __forceinline__ float wave_sum(float v) {
#pragma unroll
  for (int i = 0; i < 4; ++i) {
    const int x = (int)__float_as_uint(v);
    v += __uint_as_float(i == 0 ? __builtin_amdgcn_update_dpp(0, x, 0x128, 0xf, 0xf, false) : i == 1 ? __builtin_amdgcn_update_dpp(0, x, 0x124, 0xf, 0xf, false)
                         : i == 2 ? __builtin_amdgcn_update_dpp(0, x, 0x122, 0xf, 0xf, false) : __builtin_amdgcn_update_dpp(0, x, 0x121, 0xf, 0xf, false));
  }
  auto r = __builtin_amdgcn_permlane16_swap(__float_as_uint(v), __float_as_uint(v), false, false);
  v = __uint_as_float(r[0]) + __uint_as_float(r[1]);
  r = __builtin_amdgcn_permlane32_swap(__float_as_uint(v), __float_as_uint(v), false, false);
  return __uint_as_float(r[0]) + __uint_as_float(r[1]);
}
__global__ __launch_bounds__(512) void ln_kernel(const u16* __restrict__ x, char* ws_, const u16* __restrict__ mem0) {
  char* ws = ws_align(ws_);
  const int lane = threadIdx.x & 63, w = threadIdx.x >> 6;
  if (blockIdx.x < 8 * NPAGE) {
    if ((blockIdx.x & 7) != XPROBE) return;
    __shared__ unsigned tm[16];
    const __amdgpu_buffer_rsrc_t rs = __builtin_amdgcn_make_buffer_rsrc(ws + (size_t)(blockIdx.x >> 3) * PAGE + PROBE, 0, 16384, 0x00020000);
    if (lane < 32) __builtin_amdgcn_raw_buffer_store_b128((u32x4){DSENT, DSENT, DSENT, DSENT}, rs, (lane >> 4) * 8192 + w * 256 + (lane & 15) * 16, 0, 16);
    unsigned t[4];
#pragma unroll
    for (int i = 0; i < 4; ++i) {
      unsigned long long c0, c1; unsigned v, off = ((i ^ (i >> 1)) & 1) * 8192 + w * 256 + (i >> 1) * 128;
      asm volatile("s_waitcnt vmcnt(0)\n\ts_barrier\n\ts_memtime %0\n\ts_waitcnt lgkmcnt(0)\n\tbuffer_load_dword %2, %3, %4, 0 offen sc1\n\ts_waitcnt vmcnt(0)\n\t"
                   "s_memtime %1\n\ts_waitcnt lgkmcnt(0)" : "=&s"(c0), "=&s"(c1), "=&v"(v) : "v"(off), "s"(rs) : "memory");
      t[i] = (unsigned)(c1 - c0);
    }
    if (lane == 0) { tm[w] = min(t[0], t[3]); tm[8 + w] = min(t[1], t[2]); }
    __syncthreads();
    if (threadIdx.x == 0) {
      unsigned m0 = tm[0], m1 = tm[8];
#pragma unroll
      for (int i = 1; i < 8; ++i) { m0 = min(m0, tm[i]); m1 = min(m1, tm[8 + i]); }
      reinterpret_cast<unsigned*>(ws + SY_PAR)[blockIdx.x >> 3] = min(m0, 30000u) << 16 | min(m1, 30000u);
    }
    return;
  }
  const int bid = blockIdx.x - 8 * NPAGE;
  const int row = bid * 8 + w;
  const u16* xr = x + (size_t)row * DM;
  float v[16];
  u32x4 a = *reinterpret_cast<const u32x4*>(xr + lane * 8);
  u32x4 b = *reinterpret_cast<const u32x4*>(xr + 512 + lane * 8);
  {
    const int i = bid * 512 + threadIdx.x;
    const __amdgpu_buffer_rsrc_t rs = __builtin_amdgcn_make_buffer_rsrc(ws + WS_SY, 0, SY_TOTAL, 0x00020000);
    static_assert(NPRE == 4 && (SY_FEND - SY_BR) / 16 <= 24576, "");
    const int o = i >= 65536 ? SY_V + (i - 65536) * 16 : (i >= 32768 ? SY_O + (i - 32768) * 16 : (i < 4096 ? i * 16
                  : (i >= 8192 && i - 8192 < (SY_FEND - SY_BR) / 16 ? SY_BR + (i - 8192) * 16 : 0x7F000000)));
    const unsigned f = i >= 8192 && i < 32768 ? SENT : DSENT;
    u32x4 m = {f, f, f, f};
    if (i < 2048) {
      const int j = (i >> 10) * 512 + (i & 511);
      const unsigned* m0 = reinterpret_cast<const unsigned*>(mem0 + (2 * ((j >> 2) & 3) + (j & 1)) * DM + (j >> 4) * 16) + ((j >> 1) & 1);
#pragma unroll
      for (int k = 0; k < 4; ++k) { const unsigned t = m0[2 * k]; m[k] = t == DSENT ? 0xFFFFFFFFu : t; }
    }
    __builtin_amdgcn_raw_buffer_store_b128(m, rs, o, 0, LN_AUX);
  }
#pragma unroll
  for (int i = 0; i < 4; ++i) {
    v[2 * i] = lo_f(a[i]); v[2 * i + 1] = hi_f(a[i]);
    v[8 + 2 * i] = lo_f(b[i]); v[8 + 2 * i + 1] = hi_f(b[i]);
  }
  float s = 0.f;
#pragma unroll
  for (int i = 0; i < 16; ++i) s += v[i];
  const float mean = wave_sum(s) * (1.0f / DM);
  float q = 0.f;
#pragma unroll
  for (int i = 0; i < 16; ++i) { float d = v[i] - mean; q += d * d; }
  const float var = rbf(wave_sum(q) * (1.0f / DM));
  const float mb = rbf(mean);
  const float inv = 1.0f / rbf(sqrtf(rbf(var + 1e-5f)));
  u32x4 oa, ob;
#pragma unroll
  for (int i = 0; i < 4; ++i) {
    const unsigned t = cvtpk(v[2 * i] - mb, v[2 * i + 1] - mb), u = cvtpk(v[8 + 2 * i] - mb, v[8 + 2 * i + 1] - mb);
    oa[i] = cvtpk(lo_f(t) * inv, hi_f(t) * inv);
    ob[i] = cvtpk(lo_f(u) * inv, hi_f(u) * inv);
  }
  const __amdgpu_buffer_rsrc_t ro = __builtin_amdgcn_make_buffer_rsrc(ws + WS_XN, 0, S_LEN * XLD * 2, 0x00020000);
  __builtin_amdgcn_raw_buffer_store_b128(oa, ro, row * (XLD * 2) + lane * 16, 0, LN_AUX);
  __builtin_amdgcn_raw_buffer_store_b128(ob, ro, row * (XLD * 2) + 1024 + lane * 16, 0, LN_AUX);
}

__shared__ __attribute__((aligned(16))) char g_smem[163840];
__device__ __forceinline__ unsigned lds_off(const void* p) {
  return (unsigned)(uintptr_t)(lds_void_ptr)g_smem + (unsigned)((const char*)p - g_smem);
}
__device__ __forceinline__ __amdgpu_buffer_rsrc_t mkrsrc(const void* p, unsigned bytes) {
  return __builtin_amdgcn_make_buffer_rsrc((void*)p, 0, bytes, 0x00020000);
}
__device__ __forceinline__ u32x4 ld_sc1(__amdgpu_buffer_rsrc_t rs, int off) {
  return __builtin_amdgcn_raw_buffer_load_b128(rs, off, 0, 16);
}
__device__ __forceinline__ void st_sc1(__amdgpu_buffer_rsrc_t rs, int off, u32x4 v) {
  __builtin_amdgcn_raw_buffer_store_b128(v, rs, off, 0, 16);
}
__device__ __forceinline__ unsigned ror8(unsigned v) {
  return (unsigned)__builtin_amdgcn_update_dpp(0, (int)v, 0x128, 0xf, 0xf, false);
}
__device__ __forceinline__ float ror8f(float v) { return __uint_as_float(ror8(__float_as_uint(v))); }
__device__ __forceinline__ void swap16(unsigned& x, unsigned& y) {
  auto r = __builtin_amdgcn_permlane16_swap(x, y, false, false);
  x = r[0]; y = r[1];
}
__device__ __forceinline__ void swap32(unsigned& x, unsigned& y) {
  auto r = __builtin_amdgcn_permlane32_swap(x, y, false, false);
  x = r[0]; y = r[1];
}
__device__ __forceinline__ f32x4 fold32(f32x4 x, f32x4 y) {
  f32x4 o;
#pragma unroll
  for (int j = 0; j < 4; ++j) {
    unsigned a = __float_as_uint(x[j]), b = __float_as_uint(y[j]);
    swap32(a, b);
    o[j] = __uint_as_float(a) + __uint_as_float(b);
  }
  return o;
}
__device__ __forceinline__ bf16x8 tr_frag(const char* img_lane) {
  s16x4 lo = __builtin_amdgcn_ds_read_tr16_b64_v4i16((lds_s16x4_ptr)img_lane);
  s16x4 hi = __builtin_amdgcn_ds_read_tr16_b64_v4i16((lds_s16x4_ptr)(img_lane + 64));
  return (bf16x8){lo[0], lo[1], lo[2], lo[3], hi[0], hi[1], hi[2], hi[3]};
}
__device__ __forceinline__ float rowq_max(float v) {
  unsigned a = __float_as_uint(v), b = a;
  swap16(a, b);
  v = __builtin_elementwise_maximum(__uint_as_float(a), __uint_as_float(b));
  a = __float_as_uint(v); b = a;
  swap32(a, b);
  return __builtin_elementwise_maximum(__uint_as_float(a), __uint_as_float(b));
}
__device__ __forceinline__ float rowq_sum(float v) {
  unsigned a = __float_as_uint(v), b = a;
  swap16(a, b);
  v = __uint_as_float(a) + __uint_as_float(b);
  a = __float_as_uint(v); b = a;
  swap32(a, b);
  return __uint_as_float(a) + __uint_as_float(b);
}
__device__ __forceinline__ bool spin_fail(unsigned& spins, gu32* abortw) {
  ++spins;
  if ((spins & 63u) == 0u) {
    const __amdgpu_buffer_rsrc_t ra = mkrsrc((const void*)abortw, 4);
    unsigned z = 0;
    asm volatile("" : "+v"(z));
    if (__builtin_amdgcn_readfirstlane(__builtin_amdgcn_raw_buffer_load_b32(ra, z, 0, 16)) == 1u) return true;
    if (spins > (1u << 19)) {
      __builtin_amdgcn_raw_buffer_store_b32(z + 1u, ra, z, 0, 16);
      return true;
    }
  }
  return false;
}
__device__ __forceinline__ unsigned umax4(u32x4 v) { return max(max(v[0], v[1]), max(v[2], v[3])); }
__device__ __forceinline__ bool has_ds(u32x4 v) { return (v[0] == DSENT) | (v[1] == DSENT) | (v[2] == DSENT) | (v[3] == DSENT); }
__device__ __forceinline__ float fmax2(float a, float b) { return __builtin_elementwise_maximum(a, b); }
__device__ __forceinline__ bf16x8 ldsf(const char* p) { return *reinterpret_cast<const bf16x8*>(p); }
#define SCHED() __builtin_amdgcn_sched_barrier(0)
#define LBAR() do { asm volatile("s_waitcnt lgkmcnt(0)" ::: "memory"); __builtin_amdgcn_s_barrier(); asm volatile("" ::: "memory"); } while (0)
#define WAIT_V(n) asm volatile("s_waitcnt vmcnt(%0)" :: "n"(n) : "memory")

__device__ __forceinline__ void wait_flags(__amdgpu_buffer_rsrc_t rs, int off, gu32* abortw) {
  for (unsigned spins = 0;;) {
    asm volatile("" ::: "memory");
    const unsigned v = __builtin_amdgcn_raw_buffer_load_b32(rs, off, 0, 16);
    if (__all(v != SENT)) break;
    __builtin_amdgcn_s_sleep(4);
    if (spin_fail(spins, abortw)) break;
  }
  asm volatile("" ::: "memory");
}

#define LST(i) do {} while (0)
#define RDY(i) do {} while (0)
#define SMP(k, s, v) do {} while (0)
#define LSTC(i) do {} while (0)
__device__ __forceinline__ unsigned offA(unsigned row, unsigned ch) {
  return row * 128u + 16u * (ch ^ (row & 7u));
}
__device__ __forceinline__ unsigned swzB(unsigned row) { return ((row & 3u) << 2) | ((row >> 2) & 3u); }
__device__ __forceinline__ unsigned offB(unsigned row, unsigned ch) {
  return 256u * row + 16u * (ch ^ swzB(row));
}
__device__ __forceinline__ void glds16(const u16* src, char* dst) {
  const unsigned d = __builtin_amdgcn_readfirstlane(lds_off(dst));
  unsigned keep;
  asm volatile("s_mov_b32 %0, m0\n\ts_mov_b32 m0, %2\n\ts_nop 0\n\tglobal_load_lds_dwordx4 %1, off\n\ts_mov_b32 m0, %0"
               : "=&s"(keep) : "v"(src), "s"(d) : "memory");
}
__device__ __forceinline__ bf16x8 ld_bfrag(const char* sb, int ks, int c, int lane) {
  const unsigned g = lane >> 4, qq = (lane & 15) >> 2, pp = lane & 3;
  s16x4 lo = __builtin_amdgcn_ds_read_tr16_b64_v4i16(
      (lds_s16x4_ptr)(sb + offB(32 * ks + 8 * g + qq, 2 * c + (pp >> 1)) + 8 * (pp & 1)));
  s16x4 hi = __builtin_amdgcn_ds_read_tr16_b64_v4i16(
      (lds_s16x4_ptr)(sb + offB(32 * ks + 8 * g + 4 + qq, 2 * c + (pp >> 1)) + 8 * (pp & 1)));
  return (bf16x8){lo[0], lo[1], lo[2], lo[3], hi[0], hi[1], hi[2], hi[3]};
}

__device__ __forceinline__ void blk_dma(const u16* src, size_t ld, char* slot, int lane) {
  const u16* s = src + (size_t)(lane >> 2) * ld + (lane & 3) * 8;
  glds16(s, slot);
  glds16(s + 16 * ld, slot + 1024);
}
__device__ __forceinline__ bf16x8 blk_frag(const char* slot, int T, int lane) {
  const char* a = slot + ((lane >> 4) * 8 + ((lane & 15) >> 2)) * 64 + (lane & 3) * 16 + T * 8;
  s16x4 lo = __builtin_amdgcn_ds_read_tr16_b64_v4i16((lds_s16x4_ptr)a);
  s16x4 hi = __builtin_amdgcn_ds_read_tr16_b64_v4i16((lds_s16x4_ptr)(a + 256));
  return (bf16x8){lo[0], lo[1], lo[2], lo[3], hi[0], hi[1], hi[2], hi[3]};
}
template <int NB, typename ISSUE, typename SINK>
__device__ __forceinline__ void load_blocks(char* slice, ISSUE&& issue, SINK&& sink) {
#pragma unroll
  for (int i = 0; i < 8; ++i) issue(i, slice + i * 2048);
#pragma unroll
  for (int g = 0; g < NB / 4; ++g) {
    if (g + 1 < NB / 4) WAIT_V(8); else WAIT_V(0);
#pragma unroll
    for (int j = 0; j < 4; ++j) sink(4 * g + j, slice + ((4 * g + j) & 7) * 2048);
    asm volatile("s_waitcnt lgkmcnt(0)" ::: "memory");
    if (g + 2 < NB / 4) {
#pragma unroll
      for (int j = 0; j < 4; ++j) issue(4 * g + 8 + j, slice + ((4 * g + j) & 7) * 2048);
    }
  }
}

#define NST_S 3
template <int MT, bool GLU, int NST = 3>
__device__ __forceinline__ void gemm_main(const u16* A, int lda, const u16* B0, const u16* B1, int ldb, int nk, char* smem,
                                          f32x4 (&acc)[MT][4]) {
  constexpr int ABYTES = MT * 8192, STAGE = ABYTES + 16384, NI = MT + 2;
  int tid = threadIdx.x;
  asm volatile("" : "+v"(tid));
  const int lane = tid & 63;
  const int wid = __builtin_amdgcn_readfirstlane(tid >> 6);
  const int wr = wid >> 1, wc = wid & 1;
  const u16* ap = A + (size_t)(tid >> 3) * lda + 8 * ((tid & 7) ^ ((tid >> 3) & 7));
  const unsigned lg = (tid & 15) ^ swzB((tid >> 4) & 15);
  const u16* bp = (lg < 8 ? B0 + 8 * lg : B1 + 8 * (lg - 8)) + (size_t)(tid >> 4) * ldb;
  auto stage = [&](int kt, int buf) {
    char* sa = smem + buf * STAGE + wid * 1024;
#pragma unroll
    for (int i = 0; i < MT; ++i) glds16(ap + (size_t)i * 64 * lda + kt * 64, sa + i * 8192);
#pragma unroll
    for (int i = 0; i < 2; ++i) glds16(bp + (size_t)(kt * 64 + 32 * i) * ldb, sa + ABYTES + i * 8192);
  };
#pragma unroll
  for (int i = 0; i < MT; ++i)
#pragma unroll
    for (int j = 0; j < 4; ++j) acc[i][j] = (f32x4){0.f, 0.f, 0.f, 0.f};
  LBAR();
#pragma unroll
  for (int i = 0; i < NST - 1; ++i) stage(i, i);
  int buf = 0;
  for (int t = 0; t < nk; ++t) {
    if (t + NST - 2 < nk) WAIT_V(NI * (NST - 2)); else WAIT_V(0);
    LBAR();
    if (t + NST - 1 < nk) stage(t + NST - 1, buf == 0 ? NST - 1 : buf - 1);
    const char* sa = smem + buf * STAGE;
    const char* sb = sa + ABYTES;
    auto ldA = [&](int ks, int m) {
      return *reinterpret_cast<const bf16x8*>(sa + offA(wr * (16 * MT) + m * 16 + (lane & 15), ks * 4 + (lane >> 4)));
    };
    auto ldB = [&](int ks, int n) { return ld_bfrag(sb, ks, GLU ? ((n >> 1) * 4 + wc * 2 + (n & 1)) : (wc * 4 + n), lane); };
    bf16x8 af[2][MT], bfr[2][4];
#pragma unroll
    for (int m = 0; m < MT; ++m) af[0][m] = ldA(0, m);
#pragma unroll
    for (int n = 0; n < 4; ++n) bfr[0][n] = ldB(0, n);
    SCHED();
#pragma unroll
    for (int n = 0; n < 4; ++n) {
      if (n < MT) af[1][n] = ldA(1, n);
      bfr[1][n] = ldB(1, n);
#pragma unroll
      for (int m = 0; m < MT; ++m) acc[m][n] = MFMA(bfr[0][n], af[0][m], acc[m][n]);
      SCHED();
    }
#pragma unroll
    for (int n = 0; n < 4; ++n)
#pragma unroll
      for (int m = 0; m < MT; ++m) acc[m][n] = MFMA(bfr[1][n], af[1][m], acc[m][n]);
    buf = buf == NST - 1 ? 0 : buf + 1;
  }
}

__device__ __forceinline__ u32x4 widen32(unsigned a0, unsigned a1, unsigned b0, unsigned b1) {
  swap32(a0, b0); swap16(a0, b0);
  swap32(a1, b1); swap16(a1, b1);
  return (u32x4){a0, a1, b0, b1};
}

__device__ __forceinline__ void publish_flags(__amdgpu_buffer_rsrc_t rs, int off, int stride, int n) {
  WAIT_V(0);
  LBAR();
  int tid = threadIdx.x;
  asm volatile("" : "+v"(tid));
  if (tid < n) __builtin_amdgcn_raw_buffer_store_b32(1u, rs, off + tid * stride, 0, 16);
}

__device__ __forceinline__ void wait_s(const Args& p, __amdgpu_buffer_rsrc_t rsy, int rblk, int c0, int n, bool need) {
  if (need) {
    if (threadIdx.x < 64) {
      for (unsigned spins = 0;;) {
        asm volatile("" ::: "memory");
        const int f = (int)__builtin_amdgcn_raw_buffer_load_b32(rsy, SY_FS + (2 * c0 + (threadIdx.x & (2 * n - 1))) * 128, 0, 16);
        if (__all(f >= rblk)) break;
        __builtin_amdgcn_s_sleep(32);
        if (spin_fail(spins, (gu32*)(p.ws + WS_SY + SY_ABORT))) break;
      }
    }
    LBAR();
  }
}
__device__ __forceinline__ u32x4 combine(u32x4 sv, u32x4 g, u32x4 x) {
  u32x4 o;
#pragma unroll
  for (int j = 0; j < 4; ++j) {
    const unsigned t = cvtpk(lo_f(sv[j]) * lo_f(g[j]), hi_f(sv[j]) * hi_f(g[j]));
    o[j] = cvtpk(lo_f(x[j]) + lo_f(t), hi_f(x[j]) + hi_f(t));
  }
  return o;
}
struct SX { u32x4 s, x; };
__device__ __forceinline__ SX get_sx(const Args& p, int rblk, int e) {
  SX r; r.s = (u32x4){0u, 0u, 0u, 0u}; r.x = r.s;
  if (rblk < NFUSE) {
    r.s = ld_sc1(mkrsrc(p.ws + WS_S, S_LEN * DM * 2), (((e & 1023) >> 5) * S_LEN + (e >> 10)) * 64 + (e & 31) * 2);
    r.x = *reinterpret_cast<const u32x4*>(p.x + e);
  }
  return r;
}
__device__ __forceinline__ void put_g(const Args& p, __amdgpu_buffer_rsrc_t rg, int rblk, int e, u32x4 g, const SX& v) {
  if (rblk < NFUSE) {
    *reinterpret_cast<u32x4*>(p.out + e) = combine(v.s, g, v.x);
  } else st_sc1(rg, e * 2, g);
}

__device__ __forceinline__ void job_comb(const Args& p, int rblk, int ct, __amdgpu_buffer_rsrc_t rsy) {
  int tid = threadIdx.x;
  asm volatile("" : "+v"(tid));
  u32x4 xv[8];
#pragma unroll
  for (int i = 0; i < 8; ++i) xv[i] = *reinterpret_cast<const u32x4*>(p.x + (rblk * 128 + i * 16 + (tid >> 5)) * DM + ct * 256 + (tid & 31) * 8);
  if (tid < 64) wait_flags(rsy, SY_FG + (rblk * 8 + 2 * ct + (tid & 1)) * 128, (gu32*)(p.ws + WS_SY + SY_ABORT));
  wait_s(p, rsy, rblk, ct * 8, 8, true);
  u32x4 sv[8], gv[8];
#pragma unroll
  for (int i = 0; i < 8; ++i) {
    const int e = (rblk * 128 + i * 16 + (tid >> 5)) * DM + ct * 256 + (tid & 31) * 8;
    sv[i] = ld_sc1(mkrsrc(p.ws + WS_S, S_LEN * DM * 2), (((e & 1023) >> 5) * S_LEN + (e >> 10)) * 64 + (e & 31) * 2);
    gv[i] = ld_sc1(mkrsrc(p.ws + WS_G, S_LEN * DM * 2), e * 2);
  }
#pragma unroll
  for (int i = 0; i < 8; ++i)
    *reinterpret_cast<u32x4*>(p.out + (rblk * 128 + i * 16 + (tid >> 5)) * DM + ct * 256 + (tid & 31) * 8) = combine(sv[i], gv[i], xv[i]);
}

constexpr int VT_IMG = 32 * 72;
struct VT2 { u32x4 f[2]; };
__device__ __forceinline__ VT2 turn_vt(f32x4 a00, f32x4 a01, f32x4 a10, f32x4 a11, char* img, int lane) {
  const int r = lane & 15, q = lane >> 4;
  char* wp = img + r * 72 + q * 8;
  asm volatile("" ::: "memory");
  *reinterpret_cast<u32x2*>(wp) = (u32x2){cvtpk(a00[0], a00[1]), cvtpk(a00[2], a00[3])};
  *reinterpret_cast<u32x2*>(wp + 32) = (u32x2){cvtpk(a01[0], a01[1]), cvtpk(a01[2], a01[3])};
  *reinterpret_cast<u32x2*>(wp + 16 * 72) = (u32x2){cvtpk(a10[0], a10[1]), cvtpk(a10[2], a10[3])};
  *reinterpret_cast<u32x2*>(wp + 16 * 72 + 32) = (u32x2){cvtpk(a11[0], a11[1]), cvtpk(a11[2], a11[3])};
  asm volatile("" ::: "memory");
  const char* rp = img + (4 * q + (r >> 2)) * 72 + (r & 3) * 8;
  VT2 o;
#pragma unroll
  for (int n = 0; n < 2; ++n) {
    s16x4 lo = __builtin_amdgcn_ds_read_tr16_b64_v4i16((lds_s16x4_ptr)(rp + n * 32));
    s16x4 hi = __builtin_amdgcn_ds_read_tr16_b64_v4i16((lds_s16x4_ptr)(rp + n * 32 + 16 * 72));
    o.f[n] = __builtin_bit_cast(u32x4, (bf16x8){lo[0], lo[1], lo[2], lo[3], hi[0], hi[1], hi[2], hi[3]});
  }
  asm volatile("" ::: "memory");
  return o;
}
template <int N, typename TURN, typename OFF>
__device__ __forceinline__ void put_vt(__amdgpu_buffer_rsrc_t ro, int lane, TURN&& turn, OFF&& off) {
  VT2 cur = turn(0);
#pragma unroll
  for (int i = 0; i < N; ++i) {
    VT2 nx = cur;
    if (i + 1 < N) nx = turn(i + 1);
#pragma unroll
    for (int n = 0; n < 2; ++n) st_sc1(ro, off(i) + n * 1024 + lane * 16, cur.f[n]);
    cur = nx;
  }
}

__device__ __forceinline__ void job_kq(const Args& p, int which, int blk, int h, char* smem, __amdgpu_buffer_rsrc_t rsy) {
  const u16* xn = (const u16*)(p.ws + WS_XN);
  const u16* W = which == 0 ? p.Wk : p.Wq;
  asm volatile("" : "+s"(W));
  if (which == 2) W = p.Wv;
  W += h * DH;
  f32x4 acc[2][4];
  gemm_main<2, false, NST_S>(xn + (size_t)blk * 128 * XLD, XLD, W, W + 64, HD, DM / 64, smem, acc);
  int tid = threadIdx.x;
  asm volatile("" : "+v"(tid));
  const int lane = tid & 63, wid = __builtin_amdgcn_readfirstlane(tid >> 6), wr = wid >> 1, wc = wid & 1;
  const __amdgpu_buffer_rsrc_t ro = mkrsrc(p.ws + WS_T, T_BYTES);
  const int to = t_off(p, which * T_Q + kseg(blk * 4 + wr, h));
  if (which == 2) {
    put_vt<2>(ro, lane, [&](int nb) { return turn_vt(acc[0][2 * nb], acc[0][2 * nb + 1], acc[1][2 * nb], acc[1][2 * nb + 1], smem + 131072 + wid * VT_IMG, lane); },
              [&](int nb) { return to + (wc * 4 + nb * 2) * 1024; });
  } else {
#pragma unroll
    for (int m = 0; m < 2; ++m)
#pragma unroll
      for (int nb = 0; nb < 2; ++nb) {
        u32x4 o = widen32(cvtpk(acc[m][2 * nb][0], acc[m][2 * nb][1]), cvtpk(acc[m][2 * nb][2], acc[m][2 * nb][3]),
                          cvtpk(acc[m][2 * nb + 1][0], acc[m][2 * nb + 1][1]), cvtpk(acc[m][2 * nb + 1][2], acc[m][2 * nb + 1][3]));
        st_sc1(ro, to + (m * 4 + wc * 2 + nb) * 1024 + lane * 16, o);
      }
  }
  publish_flags(rsy, fkq(blk, h, which), 0, 1);
}

__device__ __forceinline__ unsigned ld_flag0(__amdgpu_buffer_rsrc_t rs, int off) {
  return __builtin_amdgcn_raw_buffer_load_b32(rs, off, 0, 16);
}
__device__ __forceinline__ void glds16_sc1(const char* src, char* dst) {
  const unsigned d = __builtin_amdgcn_readfirstlane(lds_off(dst));
  unsigned keep;
  asm volatile("s_mov_b32 %0, m0\n\ts_mov_b32 m0, %2\n\ts_nop 0\n\tglobal_load_lds_dwordx4 %1, off sc1\n\ts_mov_b32 m0, %0"
               : "=&s"(keep) : "v"(src), "s"(d) : "memory");
}

__device__ __forceinline__ void touch_line(const char* src, char* dst) {
  const unsigned d = __builtin_amdgcn_readfirstlane(lds_off(dst));
  unsigned keep;
  asm volatile("s_mov_b32 %0, m0\n\ts_mov_b32 m0, %2\n\ts_nop 0\n\tglobal_load_lds_dword %1, off sc1\n\ts_mov_b32 m0, %0"
               : "=&s"(keep) : "v"(src), "s"(d) : "memory");
}

#define GPF 1
__device__ __forceinline__ void glds2(const char* base, int step, unsigned voff, unsigned dst) {
  unsigned keep;
  const char* base2 = base + step;
  asm volatile("s_mov_b32 %0, m0\n\ts_mov_b32 m0, %4\n\ts_nop 0\n\tglobal_load_lds_dwordx4 %1, %2\n\t"
               "s_add_u32 m0, m0, 0x2000\n\ts_nop 0\n\tglobal_load_lds_dwordx4 %1, %3\n\ts_mov_b32 m0, %0"
               : "=&s"(keep) : "v"(voff), "s"(base), "s"(base2), "s"(dst) : "memory", "scc");
}

__device__ __forceinline__ void gemm_stream(const char* A, int lda, const char* B, int ldb, int nk, char* smem, f32x4 (&acc)[2][4]) {
  int tid = threadIdx.x;
  asm volatile("" : "+v"(tid));
  const int lane = tid & 63, r = lane & 15, q = lane >> 4;
  const int wid = __builtin_amdgcn_readfirstlane(tid >> 6);
  const int wr = wid >> 1, wc = wid & 1;
  const unsigned va = (tid >> 3) * lda + 16 * ((tid & 7) ^ ((tid >> 3) & 7)), vb = (tid >> 4) * ldb + 16 * ((tid & 15) ^ swzB((tid >> 4) & 15));
  const unsigned lw = lds_off(smem) + wid * 1024;
  auto stage = [&](int kt) {
    glds2(A + kt * 128, 64 * lda, va, lw + (kt & 3) * 32768);
    glds2(B + (size_t)kt * 64 * ldb, 32 * ldb, vb, lw + (kt & 3) * 32768 + 16384);
  };
  unsigned la[2], lb[4][2];
#pragma unroll
  for (int k = 0; k < 2; ++k) { la[k] = lds_off(smem) + offA(wr * 32 + r, k * 4 + q); asm volatile("" : "+v"(la[k])); }
#pragma unroll
  for (int n = 0; n < 4; ++n)
#pragma unroll
    for (int u = 0; u < 2; ++u) {
      lb[n][u] = lds_off(smem) + 16384 + offB(8 * q + 4 * u + (r >> 2), 2 * (wc * 4 + n) + ((r & 3) >> 1)) + 8 * (r & 1);
      asm volatile("" : "+v"(lb[n][u]));
    }
  typedef __attribute__((address_space(3))) const bf16x8* lds_frag_ptr;
  bf16x8 af[2][2], bf[2][4];
  auto ldA = [&](int bo, int ks, int m) { return *(lds_frag_ptr)(uintptr_t)(la[ks] + bo + m * 2048); };
  auto ldB = [&](int bo, int ks, int n) {
    s16x4 lo = __builtin_amdgcn_ds_read_tr16_b64_v4i16((lds_s16x4_ptr)(uintptr_t)(lb[n][0] + bo + ks * 8192));
    s16x4 hi = __builtin_amdgcn_ds_read_tr16_b64_v4i16((lds_s16x4_ptr)(uintptr_t)(lb[n][1] + bo + ks * 8192));
    return (bf16x8){lo[0], lo[1], lo[2], lo[3], hi[0], hi[1], hi[2], hi[3]};
  };
#pragma unroll
  for (int i = 0; i < 8; ++i) acc[i >> 2][i & 3] = (f32x4){0.f, 0.f, 0.f, 0.f};
  LBAR();
  stage(0); stage(1); stage(2);
  WAIT_V(8);
  LBAR();
#pragma unroll
  for (int m = 0; m < 2; ++m) af[0][m] = ldA(0, 0, m);
#pragma unroll
  for (int n = 0; n < 4; ++n) bf[0][n] = ldB(0, 0, n);
  for (int t0 = 0; t0 < nk; t0 += 4) {
#pragma unroll
    for (int b = 0; b < 4; ++b) {
      const int t = t0 + b;
#pragma unroll
      for (int ks = 0; ks < 2; ++ks) {
        if (ks == 1) {
          if (t + 2 < nk) WAIT_V(4); else WAIT_V(0);
          __builtin_amdgcn_s_barrier();
          if (t + 3 < nk) stage(t + 3);
        }
        const int nb = (ks ? (b + 1) & 3 : b) * 32768, nks = ks ^ 1;
#pragma unroll
        for (int n = 0; n < 4; ++n) {
          acc[0][n] = MFMA(bf[ks][n], af[ks][0], acc[0][n]);
          bf[nks][n] = ldB(nb, nks, n);
          acc[1][n] = MFMA(bf[ks][n], af[ks][1], acc[1][n]);
          if (n < 2) af[nks][n] = ldA(nb, nks, n);
          SCHED();
        }
      }
    }
  }
}

#define HSL 40
__device__ __forceinline__ void wait_h(const Args& p, int panel, int half) {
  if (threadIdx.x == 0) {
    gu32* cnt = (gu32*)(p.ws + WS_SY + SY_CH) + panel * 64;
    for (unsigned spins = 0;;) {
      const unsigned c0 = __hip_atomic_load(cnt + (half == 1 ? 32 : 0), __ATOMIC_RELAXED, __HIP_MEMORY_SCOPE_AGENT);
      const unsigned c1 = __hip_atomic_load(cnt + (half == 0 ? 0 : 32), __ATOMIC_RELAXED, __HIP_MEMORY_SCOPE_AGENT);
      if (c0 + 1u >= 14u && c1 + 1u >= 14u) break;
      __builtin_amdgcn_s_sleep(HSL);
      if (++spins > (1u << 20)) { __hip_atomic_store((gu32*)(p.ws + WS_SY + SY_ABORT), 1u, __ATOMIC_RELAXED, __HIP_MEMORY_SCOPE_AGENT); break; }
    }
    __builtin_amdgcn_fence(__ATOMIC_ACQUIRE, "agent");
    asm volatile("s_waitcnt vmcnt(0)" ::: "memory");
  }
  __syncthreads();
}
__device__ __forceinline__ void job_down(const Args& p, int rblk, int ct, int half, char* smem, __amdgpu_buffer_rsrc_t rsy) {
  wait_h(p, rblk >> 1, half);
  f32x4 acc[2][4];
  const int k0 = half == 1 ? FF / 2 : 0;
  gemm_stream(p.ws + WS_HF + h_off(p, rblk) + k0 * 2, HLD, (const char*)(p.wd + (size_t)k0 * DM + ct * 128), DM * 2,
              half < 2 ? FF / 128 : FF / 64, smem, acc);
  if (half < 2) {
    int tid = threadIdx.x;
    asm volatile("" : "+v"(tid));
    const int tile = (rblk & 7) * 8 + ct;
    const __amdgpu_buffer_rsrc_t rp = mkrsrc(p.ws + WS_DP + (size_t)tile * 65536, 65536);
    volatile unsigned* slot = reinterpret_cast<volatile unsigned*>(smem + 163808);
    if (tid == 0) *slot = __hip_atomic_fetch_add((gu32*)(p.ws + WS_SY + SY_TK) + tile * 64, 1u, __ATOMIC_RELAXED, __HIP_MEMORY_SCOPE_AGENT);
    LBAR();
    if (__builtin_amdgcn_readfirstlane(*slot) == SENT) {
#pragma unroll
      for (int i = 0; i < 8; ++i) st_sc1(rp, i * 8192 + tid * 16, __builtin_bit_cast(u32x4, acc[i >> 2][i & 3]));
      publish_flags(rsy, SY_TK + tile * 256 + 128, 0, 1);
      return;
    }
    if (tid < 64) wait_flags(rsy, SY_TK + tile * 256 + 128, (gu32*)(p.ws + WS_SY + SY_ABORT));
    LBAR();
#pragma unroll
    for (int i = 0; i < 8; ++i) acc[i >> 2][i & 3] += __builtin_bit_cast(f32x4, ld_sc1(rp, i * 8192 + tid * 16));
  }
  wait_s(p, rsy, rblk, ct * 4, 4, rblk < NFUSE);
  int tid = threadIdx.x;
  asm volatile("" : "+v"(tid));
  const int lane = tid & 63, wid = tid >> 6, wr = wid >> 1, wc = wid & 1;
  const __amdgpu_buffer_rsrc_t ro = mkrsrc(p.ws + WS_G, S_LEN * DM * 2);
  SX sx[4];
#pragma unroll
  for (int i = 0; i < 4; ++i)
    sx[i] = get_sx(p, rblk, (rblk * 128 + wr * 32 + (i >> 1) * 16 + (lane & 15)) * DM + ct * 128 + wc * 64 + (i & 1) * 32 + (lane >> 4) * 8);
#pragma unroll
  for (int m = 0; m < 2; ++m) {
    const int row = rblk * 128 + wr * 32 + m * 16 + (lane & 15);
#pragma unroll
    for (int nb = 0; nb < 2; ++nb) {
      unsigned pk[2][2];
#pragma unroll
      for (int n = 0; n < 2; ++n) {
        float rr[4];
#pragma unroll
        for (int j = 0; j < 4; ++j) { float d = rbf(acc[m][2 * nb + n][j]); rr[j] = d * __builtin_amdgcn_rcpf(1.0f + __expf(-d)); }
        pk[n][0] = cvtpk(rr[0], rr[1]); pk[n][1] = cvtpk(rr[2], rr[3]);
      }
      u32x4 o = widen32(pk[0][0], pk[0][1], pk[1][0], pk[1][1]);
      put_g(p, ro, rblk, row * DM + ct * 128 + wc * 64 + nb * 32 + (lane >> 4) * 8, o, sx[2 * m + nb]);
    }
  }
  publish_flags(rsy, SY_FG + (rblk * 8 + ct) * 128, 0, 1);
}

__device__ __forceinline__ void med_loop(const u16* A, int lda, const u16* B0, const u16* B1, int ldb, int nk, char* smem,
                                         f32x4 (&acc)[4][4]) {
  int tid = threadIdx.x;
  asm volatile("" : "+v"(tid));
  const int lane = tid & 63;
  const int wid = __builtin_amdgcn_readfirstlane(tid >> 6);
  const int wr = wid >> 2, wc = wid & 3;
  const u16* ap = A + (size_t)(tid >> 3) * lda + 8 * ((tid & 7) ^ ((tid >> 3) & 7));
  const size_t boffs = (size_t)(tid >> 4) * ldb + 8 * ((tid & 15) ^ swzB((tid >> 4) & 15));
  const u16 *bp0 = B0 + boffs, *bp1 = B1 + boffs;
  auto stage = [&](int kt, int buf) {
    char* sa = smem + buf * 49152 + wid * 1024;
#pragma unroll
    for (int i = 0; i < 2; ++i) glds16(ap + (size_t)i * 64 * lda + kt * 64, sa + i * 8192);
#pragma unroll
    for (int i = 0; i < 2; ++i) {
      glds16(bp0 + (size_t)(kt * 64 + 32 * i) * ldb, sa + 16384 + i * 8192);
      glds16(bp1 + (size_t)(kt * 64 + 32 * i) * ldb, sa + 32768 + i * 8192);
    }
  };
#pragma unroll
  for (int i = 0; i < 4; ++i)
#pragma unroll
    for (int j = 0; j < 4; ++j) acc[i][j] = (f32x4){0.f, 0.f, 0.f, 0.f};
  LBAR();
  stage(0, 0);
  stage(1, 1);
  int buf = 0;
  for (int t = 0; t < nk; ++t) {
    if (t + 1 < nk) WAIT_V(6); else WAIT_V(0);
    LBAR();
    if (t + 2 < nk) stage(t + 2, buf == 0 ? 2 : buf - 1);
    const char* sa = smem + buf * 49152;
    const char* sb = sa + 16384 + (wc >> 1) * 16384;
    auto ldB = [&](int ks, int n) { return ld_bfrag(sb, ks, (wc & 1) * 4 + n, lane); };
    auto ldA = [&](int i) {
      return *reinterpret_cast<const bf16x8*>(sa + offA(wr * 64 + (i & 3) * 16 + (lane & 15), (i >> 2) * 4 + (lane >> 4)));
    };
    bf16x8 bfr[2][4], af[4];
    bfr[0][0] = ldB(0, 0); af[0] = ldA(0); bfr[0][1] = ldB(0, 1); bfr[0][2] = ldB(0, 2); bfr[0][3] = ldB(0, 3);
    af[1] = ldA(1); af[2] = ldA(2);
    SCHED();
#pragma unroll
    for (int i = 0; i < 8; ++i) {
      if (i + 3 < 8) af[(i + 3) & 3] = ldA(i + 3);
      if (i < 2) { bfr[1][2 * i] = ldB(1, 2 * i); bfr[1][2 * i + 1] = ldB(1, 2 * i + 1); }
#pragma unroll
      for (int n = 0; n < 4; ++n) acc[i & 3][n] = MFMA(bfr[i >> 2][n], af[i & 3], acc[i & 3][n]);
      SCHED();
    }
    buf = buf == 2 ? 0 : buf + 1;
  }
}

__device__ __forceinline__ void job_kqm(const Args& p, int kind, int blk, int h, char* smem, __amdgpu_buffer_rsrc_t rsy) {
  f32x4 acc[4][4];
  const bool isv = kind == 2;
  const u16* B1 = isv ? p.Wv : p.Wq;
  asm volatile("" : "+s"(B1));
  med_loop((const u16*)(p.ws + WS_XN) + (size_t)blk * 128 * XLD, XLD, (kind == 1 ? p.Wk : B1) + h * DH, B1 + (kind == 1 ? h : h + 8) * DH, HD,
           DM / 64, smem, acc);
  int tid = threadIdx.x;
  asm volatile("" : "+v"(tid));
  const int lane = tid & 63;
  const int wid = __builtin_amdgcn_readfirstlane(tid >> 6);
  const int wr = wid >> 2, wc = wid & 3;
  const __amdgpu_buffer_rsrc_t ro = mkrsrc(p.ws + WS_T, T_BYTES);
  if (isv) {
    LBAR();
    put_vt<4>(ro, lane,
        [&](int i) { const int m = i & 2, n = 2 * (i & 1); return turn_vt(acc[m][n], acc[m][n + 1], acc[m + 1][n], acc[m + 1][n + 1], smem + wid * VT_IMG, lane); },
        [&](int i) { return t_off(p, T_V + kseg(blk * 4 + wr * 2 + (i >> 1), h + (wc >> 1) * 8)) + ((wc & 1) * 4 + 2 * (i & 1)) * 1024; });
    publish_flags(rsy, fkq(blk, h, 2), 8 * 512, 2);
    return;
  }
#pragma unroll
  for (int m = 0; m < 4; ++m) {
#pragma unroll
    for (int nb = 0; nb < 2; ++nb) {
      u32x4 o = widen32(cvtpk(acc[m][2 * nb][0], acc[m][2 * nb][1]), cvtpk(acc[m][2 * nb][2], acc[m][2 * nb][3]),
                        cvtpk(acc[m][2 * nb + 1][0], acc[m][2 * nb + 1][1]), cvtpk(acc[m][2 * nb + 1][2], acc[m][2 * nb + 1][3]));
      st_sc1(ro, t_off(p, (kind ? (wc >> 1) * T_Q : T_Q + (wc >> 1) * 8 * KSTR) + kseg(blk * 4 + wr * 2 + (m >> 1), h)) + ((m & 1) * 4 + (wc & 1) * 2 + nb) * 1024 + lane * 16, o);
    }
  }
  publish_flags(rsy, fkq(blk, h, 1 - kind), kind ? 128 : 8 * 512, 2);
}

__device__ __forceinline__ void job_dnm(const Args& p, int rblk, int ct, char* smem, __amdgpu_buffer_rsrc_t rsy) {
  wait_h(p, rblk >> 1, 2);
  f32x4 acc[4][4];
  med_loop((const u16*)(p.ws + WS_HF + h_off(p, rblk)), HLD / 2, p.wd + ct * 256, p.wd + ct * 256 + 128, DM, FF / 64, smem, acc);
  wait_s(p, rsy, rblk, ct * 8, 8, rblk < NFUSE);
  int tid = threadIdx.x;
  asm volatile("" : "+v"(tid));
  const int lane = tid & 63, wid = tid >> 6, wr = wid >> 2, wc = wid & 3;
  const __amdgpu_buffer_rsrc_t ro = mkrsrc(p.ws + WS_G, S_LEN * DM * 2);
  SX sx[8];
#pragma unroll
  for (int i = 0; i < 8; ++i)
    sx[i] = get_sx(p, rblk, (rblk * 128 + wr * 64 + (i >> 1) * 16 + (lane & 15)) * DM + ct * 256 + wc * 64 + (i & 1) * 32 + (lane >> 4) * 8);
#pragma unroll
  for (int m = 0; m < 4; ++m) {
    const int row = rblk * 128 + wr * 64 + m * 16 + (lane & 15);
#pragma unroll
    for (int nb = 0; nb < 2; ++nb) {
      unsigned pk[2][2];
#pragma unroll
      for (int n = 0; n < 2; ++n) {
        float rr[4];
#pragma unroll
        for (int j = 0; j < 4; ++j) { float d = rbf(acc[m][2 * nb + n][j]); rr[j] = d * __builtin_amdgcn_rcpf(1.0f + __expf(-d)); }
        pk[n][0] = cvtpk(rr[0], rr[1]); pk[n][1] = cvtpk(rr[2], rr[3]);
      }
      u32x4 o = widen32(pk[0][0], pk[0][1], pk[1][0], pk[1][1]);
      put_g(p, ro, rblk, row * DM + ct * 256 + wc * 64 + nb * 32 + (lane >> 4) * 8, o, sx[2 * m + nb]);
    }
  }
  publish_flags(rsy, SY_FG + (rblk * 8 + 2 * ct) * 128, 128, 2);
}

enum { J_NONE = 0, J_KQ = 1, J_VU = 2, J_GU = 3, J_DN = 4, J_KS = 5, J_QS = 6, J_VS = 7, J_CB = 8, J_QQM = 10, J_KQM = 11, J_VUM = 12, J_DNM = 13, J_DNS = 14, J_DNH = 15 };
struct BigJob { int type, panel, idx; };
struct BigPtr { const char *a, *b0, *b1; int lda, ldb; };

__device__ __forceinline__ BigPtr big_ptrs(const Args& p, const BigJob& j) {
  const u16* B0 = j.type == J_KQ ? p.Wk : p.wg;
  const u16* B1 = j.type == J_KQ ? p.Wq : p.wu;
  asm volatile("" : "+s"(B0), "+s"(B1));
  if (j.type == J_VU) { B0 = p.Wv; B1 = p.Wv + 8 * DH; }
  BigPtr r;
  r.lda = XLD * 2;
  r.ldb = j.type == J_GU ? FF * 2 : HD * 2;
  r.a = p.ws + WS_XN + (size_t)j.panel * 128 * r.lda;
  r.b0 = (const char*)(B0 + j.idx * 128); r.b1 = (const char*)(B1 + j.idx * 128);
  return r;
}
__device__ __forceinline__ void big_publish(const Args& p, const BigJob& j, __amdgpu_buffer_rsrc_t rsy) {
  int tid = threadIdx.x;
  asm volatile("" : "+v"(tid));
  if (j.type == J_KQ) {
    if (tid < 4)
      __builtin_amdgcn_raw_buffer_store_b32(1u, rsy, fkq(j.panel + (tid >> 1), j.idx, tid & 1), 0, 16);
  } else if (j.type == J_VU) {
    if (tid < 4)
      __builtin_amdgcn_raw_buffer_store_b32(1u, rsy, fkq(j.panel + (tid >> 1), j.idx + (tid & 1) * 8, 2), 0, 16);
  } else if (j.type == J_GU) {
    if (tid == 0)
      __hip_atomic_fetch_add((gu32*)(p.ws + WS_SY + SY_CH) + j.panel * 32 + (j.idx >= 14 ? 32 : 0), 1u, __ATOMIC_RELAXED, __HIP_MEMORY_SCOPE_AGENT);
  }
}
typedef f32x4 Acc[2][2][4][2];
__device__ __forceinline__ u32x4 pack32(f32x4 a, f32x4 b) {
  return widen32(cvtpk(a[0], a[1]), cvtpk(a[2], a[3]), cvtpk(b[0], b[1]), cvtpk(b[2], b[3]));
}
#define st_epi(r, o, v) st_sc1(r, o, v)
__device__ __forceinline__ void big_epilogue(const Args& p, const BigJob& j, Acc& acc, char* smem) {
  int tid = threadIdx.x;
  asm volatile("" : "+v"(tid));
  const int lane = tid & 63;
  const int wid = __builtin_amdgcn_readfirstlane(tid >> 6);
  const int wr = wid >> 2, wc = wid & 3;
  if (j.type == J_KQ) {
    const __amdgpu_buffer_rsrc_t ro = mkrsrc(p.ws + WS_T, T_BYTES);
#pragma unroll
    for (int bj = 0; bj < 2; ++bj)
#pragma unroll
      for (int i = 0; i < 8; ++i) {
        const int sg = j.panel * 4 + (i >> 2) * 4 + wr * 2 + ((i >> 1) & 1);
        st_epi(ro, t_off(p, bj * T_Q + kseg(sg, j.idx)) + ((i & 1) * 4 + wc) * 1024 + lane * 16,
               pack32(acc[i >> 2][bj][i & 3][0], acc[i >> 2][bj][i & 3][1]));
      }
  } else if (j.type == J_VU) {
    const __amdgpu_buffer_rsrc_t ro = mkrsrc(p.ws + WS_T, T_BYTES);
    put_vt<8>(ro, lane,
        [&](int i) {
          const int ai = (i >> 1) & 1, bj = i >> 2, m = 2 * (i & 1);
          return turn_vt(acc[ai][bj][m][0], acc[ai][bj][m][1], acc[ai][bj][m + 1][0], acc[ai][bj][m + 1][1], smem + 131072 + wid * VT_IMG, lane);
        },
        [&](int i) { return t_off(p, T_V + kseg(j.panel * 4 + (i & 2) * 2 + wr * 2 + (i & 1), j.idx + 8 * (i >> 2))) + wc * 2048; });
  } else {
    const __amdgpu_buffer_rsrc_t ro = mkrsrc(p.ws + WS_HF, S_LEN * 16384);
#pragma unroll
    for (int i = 0; i < 8; ++i) {
      const int row = wr * 64 + (i & 3) * 16 + (lane & 15);
      f32x4 hh[2];
#pragma unroll
      for (int n = 0; n < 2; ++n)
#pragma unroll
        for (int jj = 0; jj < 4; ++jj) {
          const float g = acc[i >> 2][0][i & 3][n][jj], u = acc[i >> 2][1][i & 3][n][jj];
          hh[n][jj] = __builtin_amdgcn_rcpf(1.0f + __builtin_amdgcn_exp2f(g * -1.4426950408889634f)) * u;
        }
      st_epi(ro, h_off(p, j.panel + (i >> 2)) + row * HLD + (j.idx * 128 + wc * 32 + (lane >> 4) * 8) * 2, pack32(hh[0], hh[1]));
    }
  }
}
#define JST(n, i, v) do {} while (0)
constexpr int JOBW = 223;
__constant__ unsigned short JOBS[223] = {
  5120, 5121, 5122, 5123, 5124, 5125, 5126, 5127, 7168, 7169, 7170, 7171, 7172, 7173, 7174, 7175,
  10240, 10241, 10242, 10243, 12304, 12305, 12306, 12307, 11280, 11281, 11282, 11283, 11284, 11285, 11286, 11287,
  12320, 12321, 12322, 12323, 2096, 2097, 2098, 2099, 11296, 11297, 11298, 11299, 11300, 11301, 11302, 11303,
  1072, 1073, 1074, 1075, 1076, 1077, 1078, 1079, 2128, 2129, 2130, 2131, 1104, 1105, 1106, 1107,
  1108, 1109, 1110, 1111, 3264, 3296, 3265, 3297, 3266, 3298, 3267, 3299, 2160, 2161, 2162, 2163,
  1136, 1137, 1138, 1139, 1140, 1141, 1142, 1143, 3268, 3300, 3269, 3301, 3270, 3302, 3200, 3232,
  2192, 2193, 2194, 2195, 1168, 1169, 1170, 1171, 1172, 1173, 1174, 1175, 3201, 3233, 3202, 3234,
  3203, 3235, 3204, 3236, 3205, 3237, 3206, 3238, 3136, 3168, 2224, 2225, 2226, 2227, 1200, 1201,
  1202, 1203, 1204, 1205, 1206, 1207, 13360, 13361, 13362, 13363, 3137, 3169, 2256, 2257, 2258, 2259,
  1232, 1233, 1234, 1235, 1236, 1237, 1238, 1239, 13344, 13345, 13346, 13347, 3138, 3170, 3139, 3171,
  3140, 3172, 3141, 3173, 3142, 3174, 3072, 3104, 3073, 3105, 3074, 3106, 3075, 3107, 3076, 3108,
  3077, 3109, 3078, 3110, 12528, 12529, 12530, 12531, 11504, 11505, 11506, 11507, 11508, 11509, 11510, 11511,
  14352, 14353, 14354, 14355, 14356, 14357, 14358, 14359, 15360, 15361, 15362, 15363, 15364, 15365, 15366, 15367,
  15368, 15369, 15370, 15371, 15372, 15373, 15374, 15375, 8320, 8336, 8352, 8368, 8384, 8400, 0,
};
__device__ __forceinline__ void gemm_role(const Args& p, int g, char* smem) {
  const __amdgpu_buffer_rsrc_t rsy = mkrsrc(p.ws + WS_SY, SY_TOTAL);
  const int x = g & 3, k = g >> 2;
  gu32* queue = (gu32*)(p.ws + WS_SY + SY_Q) + x * 32;
  volatile unsigned* slot = reinterpret_cast<volatile unsigned*>(smem + 163824);
  unsigned nxt = 0;
  auto request = [&]() {
    if (threadIdx.x == 0) nxt = __hip_atomic_fetch_add(queue, 1u, __ATOMIC_RELAXED, __HIP_MEMORY_SCOPE_AGENT) + 1u + NGW;
  };
  auto post = [&]() { if (threadIdx.x == 0) *slot = nxt; };
  auto decode = [&](int j) {
    const unsigned e = JOBS[min(j, JOBW - 1)];
    const int i = e & 15;
    BigJob b;
    b.type = e >> 10; b.panel = (e >> 4) & 63;
    b.idx = (b.type == J_VU || b.type == J_VUM || b.type == J_QQM) ? x + 4 * (i & 1) + 16 * (i >> 1) : x + 4 * i;
    if (b.type >= J_DNM) { b.idx = i; b.panel = 4 * b.panel + x; }
    return b;
  };
  int nj = 0; (void)nj;
  auto claim = [&]() {
    request();
    LBAR();
    post();
    LBAR();
    return decode((int)__builtin_amdgcn_readfirstlane(*slot));
  };
  BigJob cur = decode(k);
  bool first = true;
  if (g < 4 && threadIdx.x == 0) { LST(480 + 4 * g); LSTC(480 + 4 * g + 1); }
  while (cur.type != J_NONE) {
    JST(nj, 0, cur.type | cur.panel << 8); JST(nj, 1, __builtin_amdgcn_s_memrealtime());
    if (cur.type > J_DN) {
      if (cur.type >= J_QQM && cur.type <= J_VUM) job_kqm(p, cur.type - J_QQM, cur.panel, cur.idx, smem, rsy);
      else if (cur.type == J_DNM) job_dnm(p, cur.panel, cur.idx, smem, rsy);
      else if (cur.type >= J_DNS) job_down(p, cur.panel, cur.type == J_DNS ? cur.idx : cur.idx & 7, cur.type == J_DNS ? 2 : cur.idx >> 3, smem, rsy);
      else if (cur.type == J_CB) job_comb(p, cur.panel, x, rsy);
      else job_kq(p, cur.type - J_KS, cur.panel, cur.idx, smem, rsy);
      JST(nj, 3, __builtin_amdgcn_s_memrealtime()); ++nj;
      cur = claim();
      if (GPF && first && threadIdx.x < 448) {
        const size_t o = (size_t)g * 57344 + threadIdx.x * 128;
        touch_line((const char*)p.wd + o, smem + 163072);
        if (GPF == 3) { touch_line((const char*)p.wg + o, smem + 163072); touch_line((const char*)p.wu + o, smem + 163072); }
      }
      first = false;
      continue;
    }
    int tid = threadIdx.x;
    asm volatile("" : "+v"(tid));
    const int lane = tid & 63;
    const int wid = __builtin_amdgcn_readfirstlane(tid >> 6);
    const int wr = wid >> 2, wc = wid & 3;
    const unsigned ra = tid >> 3, ca = 16 * ((tid & 7) ^ ((tid >> 3) & 7)), rb = tid >> 4, cb = 16 * ((tid & 15) ^ swzB((tid >> 4) & 15));
    const unsigned lw = lds_off(smem) + wid * 1024;
#define B8_SA(b, h) (((b) * 2 + (h)) * 16384)
#define B8_SB(b, h) ((4 + (b) * 2 + (h)) * 16384)
#define B8_STA(b, h, P, kt) glds2((P).a + (size_t)(h) * 128 * (P).lda + (kt) * 128, 64 * (P).lda, __umul24(ra, (P).lda) + ca, lw + B8_SA(b, h))
#define B8_STB(b, h, P, kt) glds2(((h) ? (P).b1 : (P).b0) + (size_t)(kt) * 64 * (P).ldb, 32 * (P).ldb, __umul24(rb, (P).ldb) + cb, lw + B8_SB(b, h))
#define B8_LDA(b, h) do { _Pragma("unroll") for (int m = 0; m < 4; ++m) _Pragma("unroll") for (int kk = 0; kk < 2; ++kk) \
      At[m][kk] = ldsf(smem + B8_SA(b, h) + offA(wr * 64 + m * 16 + (lane & 15), kk * 4 + (lane >> 4))); } while (0)
#define B8_LDB(dst, b, h) do { _Pragma("unroll") for (int n = 0; n < 2; ++n) _Pragma("unroll") for (int kk = 0; kk < 2; ++kk) \
      dst[n][kk] = ld_bfrag(smem + B8_SB(b, h), kk, wc * 2 + n, lane); } while (0)
#define B8_MMA(ai, bj, Bt) do { __builtin_amdgcn_s_setprio(1); _Pragma("unroll") for (int m = 0; m < 4; ++m) _Pragma("unroll") for (int n = 0; n < 2; ++n) \
      _Pragma("unroll") for (int kk = 0; kk < 2; ++kk) acc[ai][bj][m][n] = MFMA(Bt[n][kk], At[m][kk], acc[ai][bj][m][n]); __builtin_amdgcn_s_setprio(0); } while (0)
#define WAIT_L(n) asm volatile("s_waitcnt lgkmcnt(%0)" :: "n"(n) : "memory")
#define BAR() __builtin_amdgcn_s_barrier()
#define WAIT_RX(c) asm volatile("s_waitcnt vmcnt(16)\n\ts_cmp_lg_u32 %0, 0\n\ts_cbranch_scc1 .Lrx%=\n\ts_waitcnt vmcnt(8)\n.Lrx%=:" :: "s"(c) : "memory", "scc")
    Acc acc;
    bf16x8 At[4][2], B0[2][2], B1[2][2];
    BigPtr cp = big_ptrs(p, cur);
    BigJob pend; pend.type = J_NONE; pend.panel = 0; pend.idx = 0;
    LBAR();
    B8_STB(0, 0, cp, 0); B8_STB(0, 1, cp, 0); B8_STA(0, 0, cp, 0); B8_STA(0, 1, cp, 0);
    if (wr == 1) BAR();
    WAIT_V(2); BAR();
    B8_STB(1, 0, cp, 1); B8_STA(1, 0, cp, 1); B8_STB(1, 1, cp, 1);
    WAIT_V(6); BAR();
    BigJob nx = pend;
    for (;;) {
      bool chain = false;
      BigPtr np = cp;
      constexpr int nt = DM / 64;
#pragma unroll
      for (int i = 0; i < 32; ++i) acc[i >> 4][(i >> 3) & 1][(i >> 1) & 3][i & 1] = (f32x4){0.f, 0.f, 0.f, 0.f};
#pragma unroll 1
      for (int t = 0; t < nt; t += 2) {
        const bool last = t == nt - 2;
        if (t == nt - 6) request();
        if (t == nt - 4) post();
        if (last) {
          nx = decode((int)__builtin_amdgcn_readfirstlane(*slot));
          chain = nx.type >= J_KQ && nx.type <= J_GU;
          if (chain) np = big_ptrs(p, nx);
        }
        const BigPtr sp = np;
        const int t2 = last ? 0 : t + 2;
        if (t == 2) big_publish(p, pend, rsy);
        const int rx = __builtin_amdgcn_readfirstlane(t == 0 && pend.type != J_NONE);
        B8_LDB(B0, 0, 0); B8_LDB(B1, 0, 1); SCHED(); B8_LDA(0, 0); B8_STA(1, 1, cp, t + 1);
        WAIT_RX(rx);
        WAIT_L(0); BAR(); B8_MMA(0, 0, B0); B8_MMA(0, 1, B1); BAR(); SCHED();
        B8_LDA(0, 1); B8_STB(0, 0, sp, t2); B8_STB(0, 1, sp, t2); B8_STA(0, 0, sp, t2);
        WAIT_RX(rx);
        WAIT_L(0); BAR(); B8_MMA(1, 0, B0); B8_MMA(1, 1, B1); BAR(); SCHED();
        B8_LDB(B0, 1, 0); B8_LDB(B1, 1, 1); SCHED(); B8_LDA(1, 0); B8_STA(0, 1, sp, t2);
        WAIT_V(8); WAIT_L(0); BAR(); B8_MMA(0, 0, B0); B8_MMA(0, 1, B1); BAR(); SCHED();
        B8_LDA(1, 1); B8_STB(1, 0, sp, t2 + 1); B8_STB(1, 1, sp, t2 + 1); B8_STA(1, 0, sp, t2 + 1);
        WAIT_V(8); WAIT_L(0); BAR(); B8_MMA(1, 0, B0); B8_MMA(1, 1, B1); BAR(); SCHED();
      }
      if (wr == 0) BAR();
      JST(nj, 2, __builtin_amdgcn_s_memrealtime());
      big_epilogue(p, cur, acc, smem);
      if (!chain) break;
      JST(nj, 3, __builtin_amdgcn_s_memrealtime()); ++nj;
      pend = cur; cur = nx; cp = np;
      if (wr == 1) BAR();
      JST(nj, 0, cur.type | cur.panel << 8); JST(nj, 1, __builtin_amdgcn_s_memrealtime());
    }
    WAIT_V(0);
    BAR();
    big_publish(p, cur, rsy);
    JST(nj, 3, __builtin_amdgcn_s_memrealtime()); ++nj;
    cur = nx;
  }
  if (threadIdx.x == 0) { LST(128 + g); }
  if (g < 4 && threadIdx.x == 0) { LST(480 + 4 * g + 2); LSTC(480 + 4 * g + 3); }
}

__device__ __forceinline__ unsigned now_q() { return (unsigned)__builtin_amdgcn_s_memrealtime() * 4u; }
__device__ __forceinline__ void pace_until(unsigned t) {
#pragma unroll 1
  for (int i = 0; i < 256; ++i) {
    const int d = (int)(t - now_q());
    if (d <= 0) break;
    if (d > 32) __builtin_amdgcn_s_sleep(1);
  }
}
#define CLB_E "s76", "s77", "s78", "s79", "s80", "s81", "s82", "s83", "s84", "s85", "s86", "s87", "s88", "s89"
#define EDGE_ASM \
  "s_memtime s[76:77]\n\t" \
  "s_memrealtime s[78:79]\n\t" \
  "s_nop 3\n\t" \
  "s_memrealtime s[80:81]\n\t" \
  "s_nop 3\n\t" \
  "s_memrealtime s[82:83]\n\t" \
  "s_nop 3\n\t" \
  "s_memrealtime s[84:85]\n\t" \
  "s_nop 3\n\t" \
  "s_memrealtime s[86:87]\n\t" \
  "s_nop 3\n\t" \
  "s_memrealtime s[88:89]\n\t" \
  "s_mov_b32 s93, 0\n\t" \
  "s_waitcnt lgkmcnt(0)\n\t" \
  "s_cmp_eq_u32 s80, s78\n\t" \
  "s_addc_u32 s93, s93, 0\n\t" \
  "s_cmp_eq_u32 s82, s78\n\t" \
  "s_addc_u32 s93, s93, 0\n\t" \
  "s_cmp_eq_u32 s84, s78\n\t" \
  "s_addc_u32 s93, s93, 0\n\t" \
  "s_cmp_eq_u32 s86, s78\n\t" \
  "s_addc_u32 s93, s93, 0\n\t" \
  "s_cmp_eq_u32 s88, s78\n\t" \
  "s_addc_u32 s93, s93, 0\n\t" \
  "s_mul_i32 s93, s93, 21\n\t" \
  "s_add_u32 s76, s76, 15\n\t" \
  "s_add_u32 %[kc], s76, s93\n\t" \
  "s_lshl2_add_u32 %[kq], s78, 16\n\t"
__device__ __forceinline__ unsigned time_of(unsigned cyc) {
  unsigned c, q;
  asm volatile(EDGE_ASM : [kc] "=&s"(c), [kq] "=&s"(q) :: CLB_E, "s93", "scc");
  return q - (((c - cyc) * 11900u) >> 16);
}
#define DS_RED 58
#define DS_MEM 38
#define LEAD_DN 2
#define LEAD_UP 12
__device__ __forceinline__ u32x2 ld2_sc1(__amdgpu_buffer_rsrc_t rs, int off, int soff = 0) {
  return __builtin_amdgcn_raw_buffer_load_b64(rs, off, soff, 16);
}
__device__ __forceinline__ void st2_sc1(__amdgpu_buffer_rsrc_t rs, int off, u32x2 v) {
  __builtin_amdgcn_raw_buffer_store_b64(v, rs, off, 0, 16);
}
__device__ __forceinline__ int row_max(int v) {
  v = max(v, __builtin_amdgcn_update_dpp(0, v, 0x128, 0xf, 0xf, false));
  v = max(v, __builtin_amdgcn_update_dpp(0, v, 0x124, 0xf, 0xf, false));
  v = max(v, __builtin_amdgcn_update_dpp(0, v, 0x122, 0xf, 0xf, false));
  v = max(v, __builtin_amdgcn_update_dpp(0, v, 0x121, 0xf, 0xf, false));
  return v;
}
__device__ __forceinline__ int wave_max(int v) {
  v = row_max(v);
  return max(max(__builtin_amdgcn_readlane(v, 0), __builtin_amdgcn_readlane(v, 16)),
             max(__builtin_amdgcn_readlane(v, 32), __builtin_amdgcn_readlane(v, 48)));
}
__device__ __forceinline__ int track(int est, int x, int up, int dn, int out) {
  const int d = x - est;
  return est + (d > out ? dn : max(min(d, up), -dn));
}
typedef __attribute__((address_space(3))) volatile unsigned* lds_vu_ptr;
__device__ __forceinline__ lds_vu_ptr lds_at(const void* p) {
  unsigned a = lds_off(p);
  asm volatile("" : "+v"(a));
  return (lds_vu_ptr)(uintptr_t)a;
}
#define WSL 12
#define PLAN_SLEEP 2
__device__ __forceinline__ unsigned latest(unsigned t) {
  const unsigned t0 = __builtin_amdgcn_readfirstlane(t);
  return t0 + wave_max((int)(t - t0));
}
__device__ __forceinline__ void watch(__amdgpu_buffer_rsrc_t rsy, int s, int lane, unsigned t0, const unsigned* tgt_, gu32* abortw, int hs) {
  pace_until(t0);
  u32x2 ra;
  for (unsigned spins = 0;;) {
    asm volatile("" ::: "memory");
    ra = ld2_sc1(rsy, lane * 128, SY_BR + hs);
    const bool all = __all((int)ra[0] >= s);
    SMP(4, s, spins);
    if (all) break;
    if (spins < 3) __builtin_amdgcn_s_sleep(PLAN_SLEEP); else __builtin_amdgcn_s_sleep(WSL);
    if (spin_fail(spins, abortw)) break;
  }
  const unsigned t = latest(ra[0] == (unsigned)s ? ra[1] : t0) + DS_MEM;
  lds_vu_ptr tgt = lds_at(tgt_);
  if (lane == 0) { tgt[1] = t; tgt[0] = s + 1; }
  SMP(0, s, (int)(t - t0)); SMP(1, s, (int)(t - now_q()));
}
template <bool ALL>
__device__ __forceinline__ unsigned side_mask(const char* ws) {
  typedef __attribute__((address_space(4))) const unsigned* cptr;
  cptr q = (cptr)(uintptr_t)(ws + SY_PAR);
  asm volatile("" : "+s"(q));
  unsigned ga = 0xFFFFu, gb = 0xFFFFu;
#pragma unroll
  for (int i = 0; i < NPAGE; ++i) { ga = min(ga, (i & 1) ? q[i] & 0xFFFFu : q[i] >> 16); gb = min(gb, (i & 1) ? q[i] >> 16 : q[i] & 0xFFFFu); }
  unsigned m = gb < ga ? 0x80001555u : 0x2AAAu;
  if (ALL) {
    const unsigned thr = min(ga, gb) + 110;
#pragma unroll
    for (int i = 0; i < NPAGE; ++i) {
      const unsigned m0 = q[i] >> 16, m1 = q[i] & 0xFFFFu, lo = min(m0, m1), hi = max(m0, m1);
      const unsigned keep = (lo < thr ? hi : 0u) > lo + 100 ? ~(1u << i) : ~0u;
      m = (m & keep) | ((m1 < m0 ? ~0u : 0u) & ~keep);
    }
  }
  unsigned r;
  asm volatile("s_nop 1\n\tv_readfirstlane_b32 %0, %1\n\ts_nop 3" : "=s"(r) : "v"(m));
  return r;
}
__device__ __forceinline__ unsigned ld_flag(__amdgpu_buffer_rsrc_t rs, int off) {
  return __builtin_amdgcn_raw_buffer_load_b32(rs, off, 0, 16);
}

#define QK_ASM_0 \
  "s_waitcnt lgkmcnt(0)\n\t" \
  "ds_read_b128 v[216:219], v250 offset:0\n\t" \
  "ds_read_b128 v[220:223], v250 offset:2048\n\t" \
  "ds_read_b128 v[224:227], v250 offset:1024\n\t" \
  "ds_read_b128 v[228:231], v250 offset:3072\n\t" \
  "v_mov_b64 v[192:193], 0\n\t" \
  "v_mov_b64 v[194:195], 0\n\t" \
  "v_mov_b64 v[196:197], 0\n\t" \
  "v_mov_b64 v[198:199], 0\n\t" \
  "s_memrealtime s[90:91]\n\t" \
  "s_waitcnt lgkmcnt(0)\n\t" \
  "s_lshl_b32 s93, s90, 2\n\t" \
  "s_sub_i32 s93, %[tgt], s93\n\t" \
  "s_cmp_le_i32 s93, 0\n\t" \
  "s_cbranch_scc1 .Lgo%=\n\t" \
  "s_min_i32 s93, s93, 8000\n\t" \
  "s_add_u32 s93, s93, 3\n\t" \
  "s_lshr_b32 s93, s93, 2\n\t" \
  "s_add_u32 s93, s93, s90\n\t" \
  "s_memrealtime s[90:91]\n\t" \
  ".Lpace%=:\n\t" \
  "s_memrealtime s[94:95]\n\t" \
  "s_waitcnt lgkmcnt(1)\n\t" \
  "s_sub_i32 s91, s90, s93\n\t" \
  "s_cmp_ge_i32 s91, 0\n\t" \
  "s_cbranch_scc1 .Lgo%=\n\t" \
  "s_memrealtime s[90:91]\n\t" \
  "s_waitcnt lgkmcnt(1)\n\t" \
  "s_sub_i32 s95, s94, s93\n\t" \
  "s_cmp_lt_i32 s95, 0\n\t" \
  "s_cbranch_scc1 .Lpace%=\n\t" \
  ".Lgo%=:\n\t" \
  "s_mov_b32 s92, 0\n\t" \
  ".Lpoll%=:\n\t" \
  "buffer_load_dwordx4 v[200:203], v248, %[rs], %[som] offen sc1\n\t" \
  "buffer_load_dwordx4 v[204:207], v248, %[rs], %[som] offen offset:1024 sc1\n\t" \
  "s_waitcnt vmcnt(1) lgkmcnt(0)\n\t" \
  "ds_write_b128 v249, v[200:203]\n\t" \
  "v_max3_u32 v208, v200, v201, v202\n\t" \
  "s_waitcnt vmcnt(0)\n\t" \
  "ds_write_b128 v249, v[204:207] offset:1024\n\t" \
  "v_max3_u32 v209, v204, v205, v206\n\t" \
  "v_max3_u32 v208, v208, v203, v207\n\t" \
  "v_max_u32 v208, v208, v209\n\t" \
  "v_cmp_le_u32 vcc, %[sent], v208\n\t" \
  "s_cbranch_vccnz .Lslow%=\n\t" \
  ".Lok%=:\n\t" \
  "v_smfmac_f32_16x16x64_bf16 v[192:195], v[200:203], v[0:7], v251\n\t" \
  "v_smfmac_f32_16x16x64_bf16 v[196:199], v[200:203], v[8:15], v251\n\t" \
  "s_waitcnt lgkmcnt(0)\n\t" \
  "s_barrier\n\t" \
  "ds_read_b128 v[208:211], v248 offset:2048\n\t" \
  "ds_read_b128 v[212:215], v248 offset:3072\n\t" \
  "ds_read_b128 v[236:239], v248 offset:4096\n\t" \
  "ds_read_b128 v[240:243], v248 offset:5120\n\t" \
  "ds_read_b128 v[244:247], v248 offset:6144\n\t" \
  "ds_read_b128 v[200:203], v248 offset:7168\n\t" \
  "v_smfmac_f32_16x16x64_bf16 v[192:195], v[204:207], v[16:23], v251\n\t" \
  "v_smfmac_f32_16x16x64_bf16 v[196:199], v[204:207], v[24:31], v251\n\t" \
  "s_waitcnt lgkmcnt(5)\n\t" \
  "v_smfmac_f32_16x16x64_bf16 v[192:195], v[208:211], v[32:39], v251\n\t" \
  "ds_read_b128 v[204:207], v248 offset:8192\n\t" \
  "v_smfmac_f32_16x16x64_bf16 v[196:199], v[208:211], v[40:47], v251\n\t" \
  "s_waitcnt lgkmcnt(5)\n\t" \
  "v_smfmac_f32_16x16x64_bf16 v[192:195], v[212:215], v[216:223], v251\n\t" \
  "ds_read_b128 v[216:219], v250 offset:4096\n\t" \
  "v_smfmac_f32_16x16x64_bf16 v[196:199], v[212:215], v[224:231], v251\n\t" \
  "s_waitcnt lgkmcnt(5)\n\t" \
  "ds_read_b128 v[220:223], v250 offset:6144\n\t" \
  "v_smfmac_f32_16x16x64_bf16 v[192:195], v[236:239], v[48:55], v251\n\t" \
  "ds_read_b128 v[224:227], v250 offset:5120\n\t" \
  "v_smfmac_f32_16x16x64_bf16 v[196:199], v[236:239], v[56:63], v251\n\t" \
  "s_waitcnt lgkmcnt(6)\n\t" \
  "ds_read_b128 v[228:231], v250 offset:7168\n\t" \
  "v_smfmac_f32_16x16x64_bf16 v[192:195], v[240:243], v[64:71], v251\n\t" \
  "ds_read_b128 v[208:211], v248 offset:9216\n\t" \
  "v_smfmac_f32_16x16x64_bf16 v[196:199], v[240:243], v[72:79], v251\n\t" \
  "s_waitcnt lgkmcnt(5)\n\t" \
  "ds_read_b128 v[212:215], v248 offset:10240\n\t" \
  "v_smfmac_f32_16x16x64_bf16 v[192:195], v[244:247], v[80:87], v251\n\t" \
  "ds_read_b128 v[236:239], v248 offset:11264\n\t" \
  "v_smfmac_f32_16x16x64_bf16 v[196:199], v[244:247], v[88:95], v251\n\t" \
  "s_waitcnt lgkmcnt(5)\n\t" \
  "v_smfmac_f32_16x16x64_bf16 v[192:195], v[200:203], v[216:223], v251\n\t" \
  "s_waitcnt lgkmcnt(3)\n\t" \
  "ds_read_b128 v[216:219], v250 offset:8192\n\t" \
  "v_smfmac_f32_16x16x64_bf16 v[196:199], v[200:203], v[224:231], v251\n\t" \
  "ds_read_b128 v[220:223], v250 offset:10240\n\t" \
  "v_smfmac_f32_16x16x64_bf16 v[192:195], v[204:207], v[96:103], v251\n\t" \
  "ds_read_b128 v[224:227], v250 offset:9216\n\t" \
  "v_smfmac_f32_16x16x64_bf16 v[196:199], v[204:207], v[104:111], v251\n\t" \
  "s_waitcnt lgkmcnt(5)\n\t" \
  "ds_read_b128 v[228:231], v250 offset:11264\n\t" \
  "v_smfmac_f32_16x16x64_bf16 v[192:195], v[208:211], v[112:119], v251\n\t" \
  "ds_read_b128 v[240:243], v248 offset:12288\n\t" \
  "v_smfmac_f32_16x16x64_bf16 v[196:199], v[208:211], v[120:127], v251\n\t" \
  "s_waitcnt lgkmcnt(5)\n\t" \
  "ds_read_b128 v[244:247], v248 offset:13312\n\t" \
  "v_smfmac_f32_16x16x64_bf16 v[192:195], v[212:215], v[128:135], v251\n\t" \
  "ds_read_b128 v[200:203], v248 offset:14336\n\t" \
  "v_smfmac_f32_16x16x64_bf16 v[196:199], v[212:215], v[136:143], v251\n\t" \
  "s_waitcnt lgkmcnt(5)\n\t" \
  "v_smfmac_f32_16x16x64_bf16 v[192:195], v[236:239], v[216:223], v251\n\t" \
  "s_waitcnt lgkmcnt(3)\n\t" \
  "ds_read_b128 v[204:207], v248 offset:15360\n\t" \
  "v_smfmac_f32_16x16x64_bf16 v[196:199], v[236:239], v[224:231], v251\n\t" \
  "s_waitcnt lgkmcnt(3)\n\t" \
  "ds_read_b128 v[216:219], v250 offset:12288\n\t" \
  "v_smfmac_f32_16x16x64_bf16 v[192:195], v[240:243], v[144:151], v251\n\t" \
  "ds_read_b128 v[220:223], v250 offset:14336\n\t" \
  "v_smfmac_f32_16x16x64_bf16 v[196:199], v[240:243], v[152:159], v251\n\t" \
  "s_waitcnt lgkmcnt(4)\n\t" \
  "ds_read_b128 v[224:227], v250 offset:13312\n\t" \
  "v_smfmac_f32_16x16x64_bf16 v[192:195], v[244:247], v[160:167], v251\n\t" \
  "ds_read_b128 v[228:231], v250 offset:15360\n\t" \
  "v_smfmac_f32_16x16x64_bf16 v[196:199], v[244:247], v[168:175], v251\n\t" \
  "s_waitcnt lgkmcnt(5)\n\t" \
  "v_smfmac_f32_16x16x64_bf16 v[192:195], v[200:203], v[176:183], v251\n\t" \
  "v_smfmac_f32_16x16x64_bf16 v[196:199], v[200:203], v[184:191], v251\n\t" \
  "s_waitcnt lgkmcnt(2)\n\t" \
  "v_smfmac_f32_16x16x64_bf16 v[192:195], v[204:207], v[216:223], v251\n\t" \
  "s_waitcnt lgkmcnt(0)\n\t" \
  "v_smfmac_f32_16x16x64_bf16 v[196:199], v[204:207], v[224:231], v251\n\t" \
  "ds_read_b128 v[200:203], v248 offset:16384\n\t" \
  "ds_read_b128 v[204:207], v248 offset:20480\n\t" \
  "v_mov_b32 v250, v232\n\t" \
  "v_mov_b32 v249, v233\n\t" \
  "s_nop 2\n\t" \
  "v_add_f32 v192, v192, v194\n\t" \
  "v_add_f32 v193, v193, v195\n\t" \
  "v_add_f32 v196, v196, v198\n\t" \
  "v_add_f32 v197, v197, v199\n\t" \
  "v_cvt_pk_bf16_f32 v192, v192, v193\n\t" \
  "v_cvt_pk_bf16_f32 v196, v196, v197\n\t" \
  "ds_write2_b32 v252, v192, v196 offset1:64\n\t" \
  "v_mov_b32 v251, v234\n\t" \
  "ds_read_b64_tr_b16 v[232:233], v250 offset:0\n\t" \
  "ds_read_b64_tr_b16 v[234:235], v250 offset:64\n\t" \
  "buffer_load_dwordx4 v[192:195], v248, %[rt], %[svt] offen offset:0 sc1\n\t" \
  "buffer_load_dwordx4 v[196:199], v248, %[rt], %[svt] offen offset:1024 sc1\n\t" \
  "s_waitcnt lgkmcnt(0)\n\t" \
  "v_mfma_f32_16x16x32_bf16 v[200:203], v[200:203], v[232:235], 0\n\t" \
  "v_mfma_f32_16x16x32_bf16 v[204:207], v[204:207], v[232:235], 0\n\t" \
  "s_nop 7\n\t" \
  "v_cndmask_b32 v216, v200, v204, %[nh]\n\t" \
  "v_cndmask_b32 v217, v201, v205, %[nh]\n\t" \
  "v_cndmask_b32 v218, v202, v206, %[nh]\n\t" \
  "v_cndmask_b32 v219, v203, v207, %[nh]\n\t" \
  "ds_write_b128 v248, v[216:219] offset:16384\n\t" \
  "s_sleep 2\n\t" \
  "buffer_load_dword v252, v249, %[rs], %[sv] offen offset:0 sc1\n\t" \
  "buffer_load_dword v253, v249, %[rs], %[sv] offen offset:256 sc1\n\t" \
  "s_setprio 3\n\t" \
  "s_waitcnt lgkmcnt(0)\n\t" \
  "s_barrier\n\t" \
  "ds_read_b64_tr_b16 v[200:201], v250 offset:2048\n\t" \
  "ds_read_b64_tr_b16 v[202:203], v250 offset:2112\n\t" \
  "ds_read_b64_tr_b16 v[236:237], v250 offset:512\n\t" \
  "ds_read_b64_tr_b16 v[238:239], v250 offset:576\n\t" \
  "ds_read_b64_tr_b16 v[204:205], v250 offset:2560\n\t" \
  "ds_read_b64_tr_b16 v[206:207], v250 offset:2624\n\t" \
  "ds_read_b64_tr_b16 v[240:241], v250 offset:1024\n\t" \
  "ds_read_b64_tr_b16 v[242:243], v250 offset:1088\n\t" \
  "ds_read_b64_tr_b16 v[208:209], v250 offset:3072\n\t" \
  "ds_read_b64_tr_b16 v[210:211], v250 offset:3136\n\t" \
  "ds_read_b64_tr_b16 v[244:245], v250 offset:1536\n\t" \
  "ds_read_b64_tr_b16 v[246:247], v250 offset:1600\n\t" \
  "ds_read_b64_tr_b16 v[212:213], v250 offset:3584\n\t" \
  "ds_read_b64_tr_b16 v[214:215], v250 offset:3648\n\t" \
  "ds_read_b128 v[220:223], v248 offset:17408\n\t" \
  "s_waitcnt lgkmcnt(13)\n\t" \
  "v_mfma_f32_16x16x32_bf16 v[200:203], v[200:203], v[232:235], 0\n\t" \
  "ds_read_b128 v[224:227], v248 offset:18432\n\t" \
  "ds_read_b128 v[228:231], v248 offset:19456\n\t" \
  "s_waitcnt lgkmcnt(11)\n\t" \
  "v_mfma_f32_16x16x32_bf16 v[200:203], v[204:207], v[236:239], v[200:203]\n\t" \
  "v_mov_b32 v233, 0\n\t" \
  "v_mov_b32 v234, 0\n\t" \
  "s_waitcnt lgkmcnt(7)\n\t" \
  "v_mfma_f32_16x16x32_bf16 v[200:203], v[208:211], v[240:243], v[200:203]\n\t" \
  "s_waitcnt lgkmcnt(3)\n\t" \
  "v_mfma_f32_16x16x32_bf16 v[200:203], v[212:215], v[244:247], v[200:203]\n\t" \
  "s_waitcnt lgkmcnt(2)\n\t" \
  "v_pk_add_f32 v[216:217], v[216:217], v[220:221]\n\t" \
  "v_pk_add_f32 v[218:219], v[218:219], v[222:223]\n\t" \
  "s_waitcnt lgkmcnt(0)\n\t" \
  "v_pk_add_f32 v[224:225], v[224:225], v[228:229]\n\t" \
  "v_pk_add_f32 v[226:227], v[226:227], v[230:231]\n\t" \
  "v_pk_add_f32 v[216:217], v[216:217], v[224:225]\n\t" \
  "v_pk_add_f32 v[218:219], v[218:219], v[226:227]\n\t" \
  "v_max3_f32 v204, v216, v217, v218\n\t" \
  "v_cndmask_b32 v200, v200, v202, %[up]\n\t" \
  "v_cndmask_b32 v201, v201, v203, %[up]\n\t" \
  "v_max_f32 v204, v204, v219\n\t" \
  "v_max3_f32 v204, v204, v200, v201\n\t" \
  "v_mov_b32 v220, 0x3f803f80\n\t" \
  "v_mov_b32 v221, 0x3f803f80\n\t" \
  "v_max_f32_dpp v205, v204, v204 row_ror:8 row_mask:0xf bank_mask:0xf\n\t" \
  "v_max_f32_dpp v206, v204, v204 row_ror:8 row_mask:0xf bank_mask:0xf\n\t" \
  "v_mov_b64 v[222:223], v[220:221]\n\t" \
  "s_nop 0\n\t" \
  "v_permlane16_swap_b32 v205, v206\n\t" \
  "v_max_f32 v204, v205, v206\n\t" \
  "v_max_f32 v207, v205, v206\n\t" \
  "s_nop 1\n\t" \
  "v_permlane32_swap_b32 v204, v207\n\t" \
  "v_max_f32 v204, v204, v207\n\t" \
  "v_mul_f32 v204, %[negc], v204\n\t" \
  "v_fma_f32 v200, v200, %[c], v204\n\t" \
  "v_fma_f32 v201, v201, %[c], v204\n\t" \
  "v_exp_f32 v200, v200\n\t" \
  "v_fma_f32 v216, v216, %[c], v204\n\t" \
  "v_exp_f32 v201, v201\n\t" \
  "v_fma_f32 v217, v217, %[c], v204\n\t" \
  "v_exp_f32 v216, v216\n\t" \
  "v_fma_f32 v218, v218, %[c], v204\n\t" \
  "v_cvt_pk_bf16_f32 v232, v200, v201\n\t" \
  "v_cvt_pk_bf16_f32 v235, v200, v201\n\t" \
  "v_exp_f32 v217, v217\n\t" \
  "v_fma_f32 v219, v219, %[c], v204\n\t" \
  "v_mfma_f32_16x16x16_bf16 v[212:215], v[220:221], v[232:233], 0\n\t" \
  "v_exp_f32 v218, v218\n\t" \
  "s_waitcnt vmcnt(0)\n\t" \
  "v_mfma_f32_16x16x16_bf16 v[224:227], v[252:253], v[232:233], 0\n\t" \
  "v_cvt_pk_bf16_f32 v210, v216, v217\n\t" \
  "v_cvt_pk_bf16_f32 v208, v216, v217\n\t" \
  "v_exp_f32 v219, v219\n\t" \
  "v_mfma_f32_16x16x16_bf16 v[228:231], v[252:253], v[234:235], 0\n\t" \
  "v_cvt_pk_bf16_f32 v211, v218, v219\n\t" \
  "v_cvt_pk_bf16_f32 v209, v218, v219\n\t" \
  "v_mov_b32_dpp v208, v210 row_ror:8 row_mask:0xf bank_mask:0xc\n\t" \
  "v_mov_b32_dpp v210, v210 row_ror:8 row_mask:0xf bank_mask:0x3\n\t" \
  "v_mov_b32_dpp v209, v211 row_ror:8 row_mask:0xf bank_mask:0xc\n\t" \
  "v_mov_b32_dpp v211, v211 row_ror:8 row_mask:0xf bank_mask:0x3\n\t" \
  "v_max_u32 v204, v252, v253\n\t" \
  "v_cmp_le_u32 vcc, %[sent], v204\n\t" \
  "v_mfma_f32_16x16x32_bf16 v[212:215], v[220:223], v[208:211], v[212:215]\n\t" \
  ".Lpv%=:\n\t" \
  "v_mfma_f32_16x16x32_bf16 v[224:227], v[192:195], v[208:211], v[224:227]\n\t" \
  "v_mfma_f32_16x16x32_bf16 v[228:231], v[196:199], v[208:211], v[228:231]\n\t" \
  "s_cbranch_vccnz .Lvs%=\n\t" \
  ".Lvok%=:\n\t" \
  "s_nop 4\n\t" \
  "v_rcp_f32 v204, v212\n\t" \
  "v_cndmask_b32 v224, v224, v226, %[nh]\n\t" \
  "v_cndmask_b32 v225, v225, v227, %[nh]\n\t" \
  "v_mul_f32 v224, v224, v204\n\t" \
  "v_mul_f32 v225, v225, v204\n\t" \
  "v_cvt_pk_bf16_f32 v224, v224, v225\n\t" \
  "buffer_store_dword v224, v251, %[rs], %[so] offen offset:0 sc1\n\t" \
  "v_cndmask_b32 v228, v228, v230, %[nh]\n\t" \
  "v_cndmask_b32 v229, v229, v231, %[nh]\n\t" \
  "v_mul_f32 v228, v228, v204\n\t" \
  "v_mul_f32 v229, v229, v204\n\t" \
  "v_cvt_pk_bf16_f32 v228, v228, v229\n\t" \
  "buffer_store_dword v228, v251, %[rs], %[so] offen offset:256 sc1\n\t" \
  "s_memtime s[90:91]\n\t" \
  "s_setprio 0\n\t" \
  "s_waitcnt lgkmcnt(0)\n\t" \
  "s_mov_b32 %[tout], s90\n\t" \
  "s_mov_b32 %[spins], s92\n\t" \
  "s_branch .Lend%=\n\t" \
  ".Lvs%=:\n\t" \
  "v_cmp_eq_u32 vcc, %[sent], v252\n\t" \
  "v_cmp_eq_u32 s[94:95], %[sent], v253\n\t" \
  "s_or_b64 s[94:95], s[94:95], vcc\n\t" \
  "s_cbranch_scc0 .Lvok%=\n\t" \
  "s_add_u32 s92, s92, 1\n\t" \
  "s_sleep 2\n\t" \
  "s_and_b32 s93, s92, 63\n\t" \
  "s_cbranch_scc1 .Lvr%=\n\t" \
  "buffer_load_dword v204, off, %[rs], %[sab] sc1\n\t" \
  "s_waitcnt vmcnt(0)\n\t" \
  "v_readfirstlane_b32 s93, v204\n\t" \
  "s_nop 3\n\t" \
  "s_cmp_eq_u32 s93, 1\n\t" \
  "s_cbranch_scc1 .Lvok%=\n\t" \
  "s_cmp_lt_u32 s92, 0x80000\n\t" \
  "s_cbranch_scc1 .Lvr%=\n\t" \
  "v_mov_b32 v204, 1\n\t" \
  "buffer_store_dword v204, off, %[rs], %[sab] sc1\n\t" \
  "s_branch .Lvok%=\n\t" \
  ".Lvr%=:\n\t" \
  "buffer_load_dword v252, v249, %[rs], %[sv] offen offset:0 sc1\n\t" \
  "buffer_load_dword v253, v249, %[rs], %[sv] offen offset:256 sc1\n\t" \
  "s_waitcnt vmcnt(0)\n\t" \
  "v_mfma_f32_16x16x16_bf16 v[224:227], v[252:253], v[232:233], 0\n\t" \
  "v_mfma_f32_16x16x16_bf16 v[228:231], v[252:253], v[234:235], 0\n\t" \
  "v_max_u32 v204, v252, v253\n\t" \
  "v_cmp_le_u32 vcc, %[sent], v204\n\t" \
  "s_nop 4\n\t" \
  "s_branch .Lpv%=\n\t" \
  "s_branch .Lend%=\n\t" \
  ".Lslow%=:\n\t" \
  "s_mov_b64 s[94:95], 0\n\t" \
  "v_cmp_eq_u32 vcc, %[sent], v200\n\t" \
  "s_or_b64 s[94:95], s[94:95], vcc\n\t" \
  "v_cmp_eq_u32 vcc, %[sent], v201\n\t" \
  "s_or_b64 s[94:95], s[94:95], vcc\n\t" \
  "v_cmp_eq_u32 vcc, %[sent], v202\n\t" \
  "s_or_b64 s[94:95], s[94:95], vcc\n\t" \
  "v_cmp_eq_u32 vcc, %[sent], v203\n\t" \
  "s_or_b64 s[94:95], s[94:95], vcc\n\t" \
  "v_cmp_eq_u32 vcc, %[sent], v204\n\t" \
  "s_or_b64 s[94:95], s[94:95], vcc\n\t" \
  "v_cmp_eq_u32 vcc, %[sent], v205\n\t" \
  "s_or_b64 s[94:95], s[94:95], vcc\n\t" \
  "v_cmp_eq_u32 vcc, %[sent], v206\n\t" \
  "s_or_b64 s[94:95], s[94:95], vcc\n\t" \
  "v_cmp_eq_u32 vcc, %[sent], v207\n\t" \
  "s_or_b64 s[94:95], s[94:95], vcc\n\t" \
  "s_cmp_eq_u64 s[94:95], 0\n\t" \
  "s_cbranch_scc1 .Lok%=\n\t" \
  "s_add_u32 s92, s92, 1\n\t" \
  "s_cmp_eq_u32 s92, 1\n\t" \
  "s_cbranch_scc1 .Lpoll%=\n\t" \
  "s_sleep 6\n\t" \
  "s_and_b32 s93, s92, 63\n\t" \
  "s_cmp_lg_u32 s93, 0\n\t" \
  "s_cbranch_scc1 .Lpoll%=\n\t" \
  "v_mov_b32 v209, 0\n\t" \
  "buffer_load_dword v208, v209, %[rs], %[sab] offen sc1\n\t" \
  "s_waitcnt vmcnt(0)\n\t" \
  "v_readfirstlane_b32 s93, v208\n\t" \
  "s_nop 3\n\t" \
  "s_cmp_eq_u32 s93, 1\n\t" \
  "s_cbranch_scc1 .Lok%=\n\t" \
  "s_cmp_lt_u32 s92, 0x80000\n\t" \
  "s_cbranch_scc1 .Lpoll%=\n\t" \
  "v_mov_b32 v208, 1\n\t" \
  "buffer_store_dword v208, v209, %[rs], %[sab] offen sc1\n\t" \
  "s_branch .Lok%=\n\t" \
  ".Lend%=:\n\t"
#define QK_ASM_1 \
  "s_waitcnt lgkmcnt(0)\n\t" \
  "ds_read_b128 v[216:219], v250 offset:0\n\t" \
  "ds_read_b128 v[220:223], v250 offset:2048\n\t" \
  "ds_read_b128 v[224:227], v250 offset:1024\n\t" \
  "ds_read_b128 v[228:231], v250 offset:3072\n\t" \
  "v_mov_b64 v[192:193], 0\n\t" \
  "v_mov_b64 v[194:195], 0\n\t" \
  "v_mov_b64 v[196:197], 0\n\t" \
  "v_mov_b64 v[198:199], 0\n\t" \
  "s_memrealtime s[90:91]\n\t" \
  "s_waitcnt lgkmcnt(0)\n\t" \
  "s_lshl_b32 s93, s90, 2\n\t" \
  "s_sub_i32 s93, %[tgt], s93\n\t" \
  "s_cmp_le_i32 s93, 0\n\t" \
  "s_cbranch_scc1 .Lgo%=\n\t" \
  "s_min_i32 s93, s93, 8000\n\t" \
  "s_add_u32 s93, s93, 3\n\t" \
  "s_lshr_b32 s93, s93, 2\n\t" \
  "s_add_u32 s93, s93, s90\n\t" \
  "s_memrealtime s[90:91]\n\t" \
  ".Lpace%=:\n\t" \
  "s_memrealtime s[94:95]\n\t" \
  "s_waitcnt lgkmcnt(1)\n\t" \
  "s_sub_i32 s91, s90, s93\n\t" \
  "s_cmp_ge_i32 s91, 0\n\t" \
  "s_cbranch_scc1 .Lgo%=\n\t" \
  "s_memrealtime s[90:91]\n\t" \
  "s_waitcnt lgkmcnt(1)\n\t" \
  "s_sub_i32 s95, s94, s93\n\t" \
  "s_cmp_lt_i32 s95, 0\n\t" \
  "s_cbranch_scc1 .Lpace%=\n\t" \
  ".Lgo%=:\n\t" \
  "s_mov_b32 s92, 0\n\t" \
  ".Lpoll%=:\n\t" \
  "buffer_load_dwordx4 v[200:203], v248, %[rs], %[som] offen sc1\n\t" \
  "buffer_load_dwordx4 v[204:207], v248, %[rs], %[som] offen offset:1024 sc1\n\t" \
  "s_waitcnt vmcnt(1) lgkmcnt(0)\n\t" \
  "ds_write_b128 v249, v[200:203]\n\t" \
  "v_max3_u32 v208, v200, v201, v202\n\t" \
  "s_waitcnt vmcnt(0)\n\t" \
  "ds_write_b128 v249, v[204:207] offset:1024\n\t" \
  "v_max3_u32 v209, v204, v205, v206\n\t" \
  "v_max3_u32 v208, v208, v203, v207\n\t" \
  "v_max_u32 v208, v208, v209\n\t" \
  "v_cmp_le_u32 vcc, %[sent], v208\n\t" \
  "s_cbranch_vccnz .Lslow%=\n\t" \
  ".Lok%=:\n\t" \
  "v_smfmac_f32_16x16x64_bf16 v[192:195], v[200:203], v[0:7], v251\n\t" \
  "v_smfmac_f32_16x16x64_bf16 v[196:199], v[200:203], v[8:15], v251\n\t" \
  "s_waitcnt lgkmcnt(0)\n\t" \
  "s_barrier\n\t" \
  "ds_read_b128 v[208:211], v248 offset:4096\n\t" \
  "ds_read_b128 v[212:215], v248 offset:5120\n\t" \
  "ds_read_b128 v[236:239], v248 offset:6144\n\t" \
  "ds_read_b128 v[240:243], v248 offset:7168\n\t" \
  "ds_read_b128 v[244:247], v248 offset:8192\n\t" \
  "ds_read_b128 v[200:203], v248 offset:9216\n\t" \
  "v_smfmac_f32_16x16x64_bf16 v[192:195], v[204:207], v[16:23], v251\n\t" \
  "v_smfmac_f32_16x16x64_bf16 v[196:199], v[204:207], v[24:31], v251\n\t" \
  "s_waitcnt lgkmcnt(5)\n\t" \
  "v_smfmac_f32_16x16x64_bf16 v[192:195], v[208:211], v[32:39], v251\n\t" \
  "ds_read_b128 v[204:207], v248 offset:10240\n\t" \
  "v_smfmac_f32_16x16x64_bf16 v[196:199], v[208:211], v[40:47], v251\n\t" \
  "s_waitcnt lgkmcnt(5)\n\t" \
  "v_smfmac_f32_16x16x64_bf16 v[192:195], v[212:215], v[216:223], v251\n\t" \
  "ds_read_b128 v[216:219], v250 offset:4096\n\t" \
  "v_smfmac_f32_16x16x64_bf16 v[196:199], v[212:215], v[224:231], v251\n\t" \
  "s_waitcnt lgkmcnt(5)\n\t" \
  "ds_read_b128 v[220:223], v250 offset:6144\n\t" \
  "v_smfmac_f32_16x16x64_bf16 v[192:195], v[236:239], v[48:55], v251\n\t" \
  "ds_read_b128 v[224:227], v250 offset:5120\n\t" \
  "v_smfmac_f32_16x16x64_bf16 v[196:199], v[236:239], v[56:63], v251\n\t" \
  "s_waitcnt lgkmcnt(6)\n\t" \
  "ds_read_b128 v[228:231], v250 offset:7168\n\t" \
  "v_smfmac_f32_16x16x64_bf16 v[192:195], v[240:243], v[64:71], v251\n\t" \
  "ds_read_b128 v[208:211], v248 offset:11264\n\t" \
  "v_smfmac_f32_16x16x64_bf16 v[196:199], v[240:243], v[72:79], v251\n\t" \
  "s_waitcnt lgkmcnt(5)\n\t" \
  "ds_read_b128 v[212:215], v248 offset:12288\n\t" \
  "v_smfmac_f32_16x16x64_bf16 v[192:195], v[244:247], v[80:87], v251\n\t" \
  "ds_read_b128 v[236:239], v248 offset:13312\n\t" \
  "v_smfmac_f32_16x16x64_bf16 v[196:199], v[244:247], v[88:95], v251\n\t" \
  "s_waitcnt lgkmcnt(5)\n\t" \
  "v_smfmac_f32_16x16x64_bf16 v[192:195], v[200:203], v[216:223], v251\n\t" \
  "s_waitcnt lgkmcnt(3)\n\t" \
  "ds_read_b128 v[216:219], v250 offset:8192\n\t" \
  "v_smfmac_f32_16x16x64_bf16 v[196:199], v[200:203], v[224:231], v251\n\t" \
  "ds_read_b128 v[220:223], v250 offset:10240\n\t" \
  "v_smfmac_f32_16x16x64_bf16 v[192:195], v[204:207], v[96:103], v251\n\t" \
  "ds_read_b128 v[224:227], v250 offset:9216\n\t" \
  "v_smfmac_f32_16x16x64_bf16 v[196:199], v[204:207], v[104:111], v251\n\t" \
  "s_waitcnt lgkmcnt(5)\n\t" \
  "ds_read_b128 v[228:231], v250 offset:11264\n\t" \
  "v_smfmac_f32_16x16x64_bf16 v[192:195], v[208:211], v[112:119], v251\n\t" \
  "ds_read_b128 v[240:243], v248 offset:14336\n\t" \
  "v_smfmac_f32_16x16x64_bf16 v[196:199], v[208:211], v[120:127], v251\n\t" \
  "s_waitcnt lgkmcnt(5)\n\t" \
  "ds_read_b128 v[244:247], v248 offset:15360\n\t" \
  "v_smfmac_f32_16x16x64_bf16 v[192:195], v[212:215], v[128:135], v251\n\t" \
  "ds_read_b128 v[200:203], v248 offset:0\n\t" \
  "v_smfmac_f32_16x16x64_bf16 v[196:199], v[212:215], v[136:143], v251\n\t" \
  "s_waitcnt lgkmcnt(5)\n\t" \
  "v_smfmac_f32_16x16x64_bf16 v[192:195], v[236:239], v[216:223], v251\n\t" \
  "s_waitcnt lgkmcnt(3)\n\t" \
  "ds_read_b128 v[204:207], v248 offset:1024\n\t" \
  "v_smfmac_f32_16x16x64_bf16 v[196:199], v[236:239], v[224:231], v251\n\t" \
  "s_waitcnt lgkmcnt(3)\n\t" \
  "ds_read_b128 v[216:219], v250 offset:12288\n\t" \
  "v_smfmac_f32_16x16x64_bf16 v[192:195], v[240:243], v[144:151], v251\n\t" \
  "ds_read_b128 v[220:223], v250 offset:14336\n\t" \
  "v_smfmac_f32_16x16x64_bf16 v[196:199], v[240:243], v[152:159], v251\n\t" \
  "s_waitcnt lgkmcnt(4)\n\t" \
  "ds_read_b128 v[224:227], v250 offset:13312\n\t" \
  "v_smfmac_f32_16x16x64_bf16 v[192:195], v[244:247], v[160:167], v251\n\t" \
  "ds_read_b128 v[228:231], v250 offset:15360\n\t" \
  "v_smfmac_f32_16x16x64_bf16 v[196:199], v[244:247], v[168:175], v251\n\t" \
  "s_waitcnt lgkmcnt(5)\n\t" \
  "v_smfmac_f32_16x16x64_bf16 v[192:195], v[200:203], v[176:183], v251\n\t" \
  "v_smfmac_f32_16x16x64_bf16 v[196:199], v[200:203], v[184:191], v251\n\t" \
  "s_waitcnt lgkmcnt(2)\n\t" \
  "v_smfmac_f32_16x16x64_bf16 v[192:195], v[204:207], v[216:223], v251\n\t" \
  "s_waitcnt lgkmcnt(0)\n\t" \
  "v_smfmac_f32_16x16x64_bf16 v[196:199], v[204:207], v[224:231], v251\n\t" \
  "ds_read_b128 v[200:203], v248 offset:17408\n\t" \
  "ds_read_b128 v[204:207], v248 offset:21504\n\t" \
  "v_mov_b32 v250, v232\n\t" \
  "v_mov_b32 v249, v233\n\t" \
  "s_nop 2\n\t" \
  "v_add_f32 v192, v192, v194\n\t" \
  "v_add_f32 v193, v193, v195\n\t" \
  "v_add_f32 v196, v196, v198\n\t" \
  "v_add_f32 v197, v197, v199\n\t" \
  "v_cvt_pk_bf16_f32 v192, v192, v193\n\t" \
  "v_cvt_pk_bf16_f32 v196, v196, v197\n\t" \
  "ds_write2_b32 v252, v192, v196 offset1:64\n\t" \
  "v_mov_b32 v251, v234\n\t" \
  "ds_read_b64_tr_b16 v[236:237], v250 offset:512\n\t" \
  "ds_read_b64_tr_b16 v[238:239], v250 offset:576\n\t" \
  "buffer_load_dwordx4 v[192:195], v248, %[rt], %[svt] offen offset:0 sc1\n\t" \
  "buffer_load_dwordx4 v[196:199], v248, %[rt], %[svt] offen offset:1024 sc1\n\t" \
  "s_waitcnt lgkmcnt(0)\n\t" \
  "v_mfma_f32_16x16x32_bf16 v[200:203], v[200:203], v[236:239], 0\n\t" \
  "v_mfma_f32_16x16x32_bf16 v[204:207], v[204:207], v[236:239], 0\n\t" \
  "s_nop 7\n\t" \
  "v_cndmask_b32 v216, v200, v204, %[nh]\n\t" \
  "v_cndmask_b32 v217, v201, v205, %[nh]\n\t" \
  "v_cndmask_b32 v218, v202, v206, %[nh]\n\t" \
  "v_cndmask_b32 v219, v203, v207, %[nh]\n\t" \
  "ds_write_b128 v248, v[216:219] offset:17408\n\t" \
  "s_sleep 2\n\t" \
  "buffer_load_dword v252, v249, %[rs], %[sv] offen offset:0 sc1\n\t" \
  "buffer_load_dword v253, v249, %[rs], %[sv] offen offset:256 sc1\n\t" \
  "s_setprio 3\n\t" \
  "s_waitcnt lgkmcnt(0)\n\t" \
  "s_barrier\n\t" \
  "ds_read_b64_tr_b16 v[204:205], v250 offset:2560\n\t" \
  "ds_read_b64_tr_b16 v[206:207], v250 offset:2624\n\t" \
  "ds_read_b64_tr_b16 v[240:241], v250 offset:1024\n\t" \
  "ds_read_b64_tr_b16 v[242:243], v250 offset:1088\n\t" \
  "ds_read_b64_tr_b16 v[208:209], v250 offset:3072\n\t" \
  "ds_read_b64_tr_b16 v[210:211], v250 offset:3136\n\t" \
  "ds_read_b64_tr_b16 v[244:245], v250 offset:1536\n\t" \
  "ds_read_b64_tr_b16 v[246:247], v250 offset:1600\n\t" \
  "ds_read_b64_tr_b16 v[212:213], v250 offset:3584\n\t" \
  "ds_read_b64_tr_b16 v[214:215], v250 offset:3648\n\t" \
  "ds_read_b64_tr_b16 v[232:233], v250 offset:0\n\t" \
  "ds_read_b64_tr_b16 v[234:235], v250 offset:64\n\t" \
  "ds_read_b64_tr_b16 v[200:201], v250 offset:2048\n\t" \
  "ds_read_b64_tr_b16 v[202:203], v250 offset:2112\n\t" \
  "ds_read_b128 v[220:223], v248 offset:18432\n\t" \
  "s_waitcnt lgkmcnt(13)\n\t" \
  "v_mfma_f32_16x16x32_bf16 v[204:207], v[204:207], v[236:239], 0\n\t" \
  "ds_read_b128 v[224:227], v248 offset:19456\n\t" \
  "ds_read_b128 v[228:231], v248 offset:16384\n\t" \
  "s_waitcnt lgkmcnt(11)\n\t" \
  "v_mfma_f32_16x16x32_bf16 v[204:207], v[208:211], v[240:243], v[204:207]\n\t" \
  "v_mov_b32 v237, 0\n\t" \
  "v_mov_b32 v238, 0\n\t" \
  "s_waitcnt lgkmcnt(7)\n\t" \
  "v_mfma_f32_16x16x32_bf16 v[204:207], v[212:215], v[244:247], v[204:207]\n\t" \
  "s_waitcnt lgkmcnt(3)\n\t" \
  "v_mfma_f32_16x16x32_bf16 v[204:207], v[200:203], v[232:235], v[204:207]\n\t" \
  "s_waitcnt lgkmcnt(2)\n\t" \
  "v_pk_add_f32 v[216:217], v[216:217], v[220:221]\n\t" \
  "v_pk_add_f32 v[218:219], v[218:219], v[222:223]\n\t" \
  "s_waitcnt lgkmcnt(0)\n\t" \
  "v_pk_add_f32 v[224:225], v[224:225], v[228:229]\n\t" \
  "v_pk_add_f32 v[226:227], v[226:227], v[230:231]\n\t" \
  "v_pk_add_f32 v[216:217], v[216:217], v[224:225]\n\t" \
  "v_pk_add_f32 v[218:219], v[218:219], v[226:227]\n\t" \
  "v_max3_f32 v208, v216, v217, v218\n\t" \
  "v_cndmask_b32 v204, v204, v206, %[up]\n\t" \
  "v_cndmask_b32 v205, v205, v207, %[up]\n\t" \
  "v_max_f32 v208, v208, v219\n\t" \
  "v_max3_f32 v208, v208, v204, v205\n\t" \
  "v_mov_b32 v220, 0x3f803f80\n\t" \
  "v_mov_b32 v221, 0x3f803f80\n\t" \
  "v_max_f32_dpp v209, v208, v208 row_ror:8 row_mask:0xf bank_mask:0xf\n\t" \
  "v_max_f32_dpp v210, v208, v208 row_ror:8 row_mask:0xf bank_mask:0xf\n\t" \
  "v_mov_b64 v[222:223], v[220:221]\n\t" \
  "s_nop 0\n\t" \
  "v_permlane16_swap_b32 v209, v210\n\t" \
  "v_max_f32 v208, v209, v210\n\t" \
  "v_max_f32 v211, v209, v210\n\t" \
  "s_nop 1\n\t" \
  "v_permlane32_swap_b32 v208, v211\n\t" \
  "v_max_f32 v208, v208, v211\n\t" \
  "v_mul_f32 v208, %[negc], v208\n\t" \
  "v_fma_f32 v204, v204, %[c], v208\n\t" \
  "v_fma_f32 v205, v205, %[c], v208\n\t" \
  "v_exp_f32 v204, v204\n\t" \
  "v_fma_f32 v216, v216, %[c], v208\n\t" \
  "v_exp_f32 v205, v205\n\t" \
  "v_fma_f32 v217, v217, %[c], v208\n\t" \
  "v_exp_f32 v216, v216\n\t" \
  "v_fma_f32 v218, v218, %[c], v208\n\t" \
  "v_cvt_pk_bf16_f32 v236, v204, v205\n\t" \
  "v_cvt_pk_bf16_f32 v239, v204, v205\n\t" \
  "v_exp_f32 v217, v217\n\t" \
  "v_fma_f32 v219, v219, %[c], v208\n\t" \
  "v_mfma_f32_16x16x16_bf16 v[200:203], v[220:221], v[236:237], 0\n\t" \
  "v_exp_f32 v218, v218\n\t" \
  "s_waitcnt vmcnt(0)\n\t" \
  "v_mfma_f32_16x16x16_bf16 v[224:227], v[252:253], v[236:237], 0\n\t" \
  "v_cvt_pk_bf16_f32 v214, v216, v217\n\t" \
  "v_cvt_pk_bf16_f32 v212, v216, v217\n\t" \
  "v_exp_f32 v219, v219\n\t" \
  "v_mfma_f32_16x16x16_bf16 v[228:231], v[252:253], v[238:239], 0\n\t" \
  "v_cvt_pk_bf16_f32 v215, v218, v219\n\t" \
  "v_cvt_pk_bf16_f32 v213, v218, v219\n\t" \
  "v_mov_b32_dpp v212, v214 row_ror:8 row_mask:0xf bank_mask:0xc\n\t" \
  "v_mov_b32_dpp v214, v214 row_ror:8 row_mask:0xf bank_mask:0x3\n\t" \
  "v_mov_b32_dpp v213, v215 row_ror:8 row_mask:0xf bank_mask:0xc\n\t" \
  "v_mov_b32_dpp v215, v215 row_ror:8 row_mask:0xf bank_mask:0x3\n\t" \
  "v_max_u32 v208, v252, v253\n\t" \
  "v_cmp_le_u32 vcc, %[sent], v208\n\t" \
  "v_mfma_f32_16x16x32_bf16 v[200:203], v[220:223], v[212:215], v[200:203]\n\t" \
  ".Lpv%=:\n\t" \
  "v_mfma_f32_16x16x32_bf16 v[224:227], v[192:195], v[212:215], v[224:227]\n\t" \
  "v_mfma_f32_16x16x32_bf16 v[228:231], v[196:199], v[212:215], v[228:231]\n\t" \
  "s_cbranch_vccnz .Lvs%=\n\t" \
  ".Lvok%=:\n\t" \
  "s_nop 4\n\t" \
  "v_rcp_f32 v208, v200\n\t" \
  "v_cndmask_b32 v224, v224, v226, %[nh]\n\t" \
  "v_cndmask_b32 v225, v225, v227, %[nh]\n\t" \
  "v_mul_f32 v224, v224, v208\n\t" \
  "v_mul_f32 v225, v225, v208\n\t" \
  "v_cvt_pk_bf16_f32 v224, v224, v225\n\t" \
  "buffer_store_dword v224, v251, %[rs], %[so] offen offset:0 sc1\n\t" \
  "v_cndmask_b32 v228, v228, v230, %[nh]\n\t" \
  "v_cndmask_b32 v229, v229, v231, %[nh]\n\t" \
  "v_mul_f32 v228, v228, v208\n\t" \
  "v_mul_f32 v229, v229, v208\n\t" \
  "v_cvt_pk_bf16_f32 v228, v228, v229\n\t" \
  "buffer_store_dword v228, v251, %[rs], %[so] offen offset:256 sc1\n\t" \
  "s_memtime s[90:91]\n\t" \
  "s_setprio 0\n\t" \
  "s_waitcnt lgkmcnt(0)\n\t" \
  "s_mov_b32 %[tout], s90\n\t" \
  "s_mov_b32 %[spins], s92\n\t" \
  "s_branch .Lend%=\n\t" \
  ".Lvs%=:\n\t" \
  "v_cmp_eq_u32 vcc, %[sent], v252\n\t" \
  "v_cmp_eq_u32 s[94:95], %[sent], v253\n\t" \
  "s_or_b64 s[94:95], s[94:95], vcc\n\t" \
  "s_cbranch_scc0 .Lvok%=\n\t" \
  "s_add_u32 s92, s92, 1\n\t" \
  "s_sleep 2\n\t" \
  "s_and_b32 s93, s92, 63\n\t" \
  "s_cbranch_scc1 .Lvr%=\n\t" \
  "buffer_load_dword v208, off, %[rs], %[sab] sc1\n\t" \
  "s_waitcnt vmcnt(0)\n\t" \
  "v_readfirstlane_b32 s93, v208\n\t" \
  "s_nop 3\n\t" \
  "s_cmp_eq_u32 s93, 1\n\t" \
  "s_cbranch_scc1 .Lvok%=\n\t" \
  "s_cmp_lt_u32 s92, 0x80000\n\t" \
  "s_cbranch_scc1 .Lvr%=\n\t" \
  "v_mov_b32 v208, 1\n\t" \
  "buffer_store_dword v208, off, %[rs], %[sab] sc1\n\t" \
  "s_branch .Lvok%=\n\t" \
  ".Lvr%=:\n\t" \
  "buffer_load_dword v252, v249, %[rs], %[sv] offen offset:0 sc1\n\t" \
  "buffer_load_dword v253, v249, %[rs], %[sv] offen offset:256 sc1\n\t" \
  "s_waitcnt vmcnt(0)\n\t" \
  "v_mfma_f32_16x16x16_bf16 v[224:227], v[252:253], v[236:237], 0\n\t" \
  "v_mfma_f32_16x16x16_bf16 v[228:231], v[252:253], v[238:239], 0\n\t" \
  "v_max_u32 v208, v252, v253\n\t" \
  "v_cmp_le_u32 vcc, %[sent], v208\n\t" \
  "s_nop 4\n\t" \
  "s_branch .Lpv%=\n\t" \
  "s_branch .Lend%=\n\t" \
  ".Lslow%=:\n\t" \
  "s_mov_b64 s[94:95], 0\n\t" \
  "v_cmp_eq_u32 vcc, %[sent], v200\n\t" \
  "s_or_b64 s[94:95], s[94:95], vcc\n\t" \
  "v_cmp_eq_u32 vcc, %[sent], v201\n\t" \
  "s_or_b64 s[94:95], s[94:95], vcc\n\t" \
  "v_cmp_eq_u32 vcc, %[sent], v202\n\t" \
  "s_or_b64 s[94:95], s[94:95], vcc\n\t" \
  "v_cmp_eq_u32 vcc, %[sent], v203\n\t" \
  "s_or_b64 s[94:95], s[94:95], vcc\n\t" \
  "v_cmp_eq_u32 vcc, %[sent], v204\n\t" \
  "s_or_b64 s[94:95], s[94:95], vcc\n\t" \
  "v_cmp_eq_u32 vcc, %[sent], v205\n\t" \
  "s_or_b64 s[94:95], s[94:95], vcc\n\t" \
  "v_cmp_eq_u32 vcc, %[sent], v206\n\t" \
  "s_or_b64 s[94:95], s[94:95], vcc\n\t" \
  "v_cmp_eq_u32 vcc, %[sent], v207\n\t" \
  "s_or_b64 s[94:95], s[94:95], vcc\n\t" \
  "s_cmp_eq_u64 s[94:95], 0\n\t" \
  "s_cbranch_scc1 .Lok%=\n\t" \
  "s_add_u32 s92, s92, 1\n\t" \
  "s_cmp_eq_u32 s92, 1\n\t" \
  "s_cbranch_scc1 .Lpoll%=\n\t" \
  "s_sleep 6\n\t" \
  "s_and_b32 s93, s92, 63\n\t" \
  "s_cmp_lg_u32 s93, 0\n\t" \
  "s_cbranch_scc1 .Lpoll%=\n\t" \
  "v_mov_b32 v209, 0\n\t" \
  "buffer_load_dword v208, v209, %[rs], %[sab] offen sc1\n\t" \
  "s_waitcnt vmcnt(0)\n\t" \
  "v_readfirstlane_b32 s93, v208\n\t" \
  "s_nop 3\n\t" \
  "s_cmp_eq_u32 s93, 1\n\t" \
  "s_cbranch_scc1 .Lok%=\n\t" \
  "s_cmp_lt_u32 s92, 0x80000\n\t" \
  "s_cbranch_scc1 .Lpoll%=\n\t" \
  "v_mov_b32 v208, 1\n\t" \
  "buffer_store_dword v208, v209, %[rs], %[sab] offen sc1\n\t" \
  "s_branch .Lok%=\n\t" \
  ".Lend%=:\n\t"
#define QK_ASM_2 \
  "s_waitcnt lgkmcnt(0)\n\t" \
  "ds_read_b128 v[216:219], v250 offset:0\n\t" \
  "ds_read_b128 v[220:223], v250 offset:2048\n\t" \
  "ds_read_b128 v[224:227], v250 offset:1024\n\t" \
  "ds_read_b128 v[228:231], v250 offset:3072\n\t" \
  "v_mov_b64 v[192:193], 0\n\t" \
  "v_mov_b64 v[194:195], 0\n\t" \
  "v_mov_b64 v[196:197], 0\n\t" \
  "v_mov_b64 v[198:199], 0\n\t" \
  "s_memrealtime s[90:91]\n\t" \
  "s_waitcnt lgkmcnt(0)\n\t" \
  "s_lshl_b32 s93, s90, 2\n\t" \
  "s_sub_i32 s93, %[tgt], s93\n\t" \
  "s_cmp_le_i32 s93, 0\n\t" \
  "s_cbranch_scc1 .Lgo%=\n\t" \
  "s_min_i32 s93, s93, 8000\n\t" \
  "s_add_u32 s93, s93, 3\n\t" \
  "s_lshr_b32 s93, s93, 2\n\t" \
  "s_add_u32 s93, s93, s90\n\t" \
  "s_memrealtime s[90:91]\n\t" \
  ".Lpace%=:\n\t" \
  "s_memrealtime s[94:95]\n\t" \
  "s_waitcnt lgkmcnt(1)\n\t" \
  "s_sub_i32 s91, s90, s93\n\t" \
  "s_cmp_ge_i32 s91, 0\n\t" \
  "s_cbranch_scc1 .Lgo%=\n\t" \
  "s_memrealtime s[90:91]\n\t" \
  "s_waitcnt lgkmcnt(1)\n\t" \
  "s_sub_i32 s95, s94, s93\n\t" \
  "s_cmp_lt_i32 s95, 0\n\t" \
  "s_cbranch_scc1 .Lpace%=\n\t" \
  ".Lgo%=:\n\t" \
  "s_mov_b32 s92, 0\n\t" \
  ".Lpoll%=:\n\t" \
  "buffer_load_dwordx4 v[200:203], v248, %[rs], %[som] offen sc1\n\t" \
  "buffer_load_dwordx4 v[204:207], v248, %[rs], %[som] offen offset:1024 sc1\n\t" \
  "s_waitcnt vmcnt(1) lgkmcnt(0)\n\t" \
  "ds_write_b128 v249, v[200:203]\n\t" \
  "v_max3_u32 v208, v200, v201, v202\n\t" \
  "s_waitcnt vmcnt(0)\n\t" \
  "ds_write_b128 v249, v[204:207] offset:1024\n\t" \
  "v_max3_u32 v209, v204, v205, v206\n\t" \
  "v_max3_u32 v208, v208, v203, v207\n\t" \
  "v_max_u32 v208, v208, v209\n\t" \
  "v_cmp_le_u32 vcc, %[sent], v208\n\t" \
  "s_cbranch_vccnz .Lslow%=\n\t" \
  ".Lok%=:\n\t" \
  "v_smfmac_f32_16x16x64_bf16 v[192:195], v[200:203], v[0:7], v251\n\t" \
  "v_smfmac_f32_16x16x64_bf16 v[196:199], v[200:203], v[8:15], v251\n\t" \
  "s_waitcnt lgkmcnt(0)\n\t" \
  "s_barrier\n\t" \
  "ds_read_b128 v[208:211], v248 offset:6144\n\t" \
  "ds_read_b128 v[212:215], v248 offset:7168\n\t" \
  "ds_read_b128 v[236:239], v248 offset:8192\n\t" \
  "ds_read_b128 v[240:243], v248 offset:9216\n\t" \
  "ds_read_b128 v[244:247], v248 offset:10240\n\t" \
  "ds_read_b128 v[200:203], v248 offset:11264\n\t" \
  "v_smfmac_f32_16x16x64_bf16 v[192:195], v[204:207], v[16:23], v251\n\t" \
  "v_smfmac_f32_16x16x64_bf16 v[196:199], v[204:207], v[24:31], v251\n\t" \
  "s_waitcnt lgkmcnt(5)\n\t" \
  "v_smfmac_f32_16x16x64_bf16 v[192:195], v[208:211], v[32:39], v251\n\t" \
  "ds_read_b128 v[204:207], v248 offset:12288\n\t" \
  "v_smfmac_f32_16x16x64_bf16 v[196:199], v[208:211], v[40:47], v251\n\t" \
  "s_waitcnt lgkmcnt(5)\n\t" \
  "v_smfmac_f32_16x16x64_bf16 v[192:195], v[212:215], v[216:223], v251\n\t" \
  "ds_read_b128 v[216:219], v250 offset:4096\n\t" \
  "v_smfmac_f32_16x16x64_bf16 v[196:199], v[212:215], v[224:231], v251\n\t" \
  "s_waitcnt lgkmcnt(5)\n\t" \
  "ds_read_b128 v[220:223], v250 offset:6144\n\t" \
  "v_smfmac_f32_16x16x64_bf16 v[192:195], v[236:239], v[48:55], v251\n\t" \
  "ds_read_b128 v[224:227], v250 offset:5120\n\t" \
  "v_smfmac_f32_16x16x64_bf16 v[196:199], v[236:239], v[56:63], v251\n\t" \
  "s_waitcnt lgkmcnt(6)\n\t" \
  "ds_read_b128 v[228:231], v250 offset:7168\n\t" \
  "v_smfmac_f32_16x16x64_bf16 v[192:195], v[240:243], v[64:71], v251\n\t" \
  "ds_read_b128 v[208:211], v248 offset:13312\n\t" \
  "v_smfmac_f32_16x16x64_bf16 v[196:199], v[240:243], v[72:79], v251\n\t" \
  "s_waitcnt lgkmcnt(5)\n\t" \
  "ds_read_b128 v[212:215], v248 offset:14336\n\t" \
  "v_smfmac_f32_16x16x64_bf16 v[192:195], v[244:247], v[80:87], v251\n\t" \
  "ds_read_b128 v[236:239], v248 offset:15360\n\t" \
  "v_smfmac_f32_16x16x64_bf16 v[196:199], v[244:247], v[88:95], v251\n\t" \
  "s_waitcnt lgkmcnt(5)\n\t" \
  "v_smfmac_f32_16x16x64_bf16 v[192:195], v[200:203], v[216:223], v251\n\t" \
  "s_waitcnt lgkmcnt(3)\n\t" \
  "ds_read_b128 v[216:219], v250 offset:8192\n\t" \
  "v_smfmac_f32_16x16x64_bf16 v[196:199], v[200:203], v[224:231], v251\n\t" \
  "ds_read_b128 v[220:223], v250 offset:10240\n\t" \
  "v_smfmac_f32_16x16x64_bf16 v[192:195], v[204:207], v[96:103], v251\n\t" \
  "ds_read_b128 v[224:227], v250 offset:9216\n\t" \
  "v_smfmac_f32_16x16x64_bf16 v[196:199], v[204:207], v[104:111], v251\n\t" \
  "s_waitcnt lgkmcnt(5)\n\t" \
  "ds_read_b128 v[228:231], v250 offset:11264\n\t" \
  "v_smfmac_f32_16x16x64_bf16 v[192:195], v[208:211], v[112:119], v251\n\t" \
  "ds_read_b128 v[240:243], v248 offset:0\n\t" \
  "v_smfmac_f32_16x16x64_bf16 v[196:199], v[208:211], v[120:127], v251\n\t" \
  "s_waitcnt lgkmcnt(5)\n\t" \
  "ds_read_b128 v[244:247], v248 offset:1024\n\t" \
  "v_smfmac_f32_16x16x64_bf16 v[192:195], v[212:215], v[128:135], v251\n\t" \
  "ds_read_b128 v[200:203], v248 offset:2048\n\t" \
  "v_smfmac_f32_16x16x64_bf16 v[196:199], v[212:215], v[136:143], v251\n\t" \
  "s_waitcnt lgkmcnt(5)\n\t" \
  "v_smfmac_f32_16x16x64_bf16 v[192:195], v[236:239], v[216:223], v251\n\t" \
  "s_waitcnt lgkmcnt(3)\n\t" \
  "ds_read_b128 v[204:207], v248 offset:3072\n\t" \
  "v_smfmac_f32_16x16x64_bf16 v[196:199], v[236:239], v[224:231], v251\n\t" \
  "s_waitcnt lgkmcnt(3)\n\t" \
  "ds_read_b128 v[216:219], v250 offset:12288\n\t" \
  "v_smfmac_f32_16x16x64_bf16 v[192:195], v[240:243], v[144:151], v251\n\t" \
  "ds_read_b128 v[220:223], v250 offset:14336\n\t" \
  "v_smfmac_f32_16x16x64_bf16 v[196:199], v[240:243], v[152:159], v251\n\t" \
  "s_waitcnt lgkmcnt(4)\n\t" \
  "ds_read_b128 v[224:227], v250 offset:13312\n\t" \
  "v_smfmac_f32_16x16x64_bf16 v[192:195], v[244:247], v[160:167], v251\n\t" \
  "ds_read_b128 v[228:231], v250 offset:15360\n\t" \
  "v_smfmac_f32_16x16x64_bf16 v[196:199], v[244:247], v[168:175], v251\n\t" \
  "s_waitcnt lgkmcnt(5)\n\t" \
  "v_smfmac_f32_16x16x64_bf16 v[192:195], v[200:203], v[176:183], v251\n\t" \
  "v_smfmac_f32_16x16x64_bf16 v[196:199], v[200:203], v[184:191], v251\n\t" \
  "s_waitcnt lgkmcnt(2)\n\t" \
  "v_smfmac_f32_16x16x64_bf16 v[192:195], v[204:207], v[216:223], v251\n\t" \
  "s_waitcnt lgkmcnt(0)\n\t" \
  "v_smfmac_f32_16x16x64_bf16 v[196:199], v[204:207], v[224:231], v251\n\t" \
  "ds_read_b128 v[200:203], v248 offset:18432\n\t" \
  "ds_read_b128 v[204:207], v248 offset:22528\n\t" \
  "v_mov_b32 v250, v232\n\t" \
  "v_mov_b32 v249, v233\n\t" \
  "s_nop 2\n\t" \
  "v_add_f32 v192, v192, v194\n\t" \
  "v_add_f32 v193, v193, v195\n\t" \
  "v_add_f32 v196, v196, v198\n\t" \
  "v_add_f32 v197, v197, v199\n\t" \
  "v_cvt_pk_bf16_f32 v192, v192, v193\n\t" \
  "v_cvt_pk_bf16_f32 v196, v196, v197\n\t" \
  "ds_write2_b32 v252, v192, v196 offset1:64\n\t" \
  "v_mov_b32 v251, v234\n\t" \
  "ds_read_b64_tr_b16 v[240:241], v250 offset:1024\n\t" \
  "ds_read_b64_tr_b16 v[242:243], v250 offset:1088\n\t" \
  "buffer_load_dwordx4 v[192:195], v248, %[rt], %[svt] offen offset:0 sc1\n\t" \
  "buffer_load_dwordx4 v[196:199], v248, %[rt], %[svt] offen offset:1024 sc1\n\t" \
  "s_waitcnt lgkmcnt(0)\n\t" \
  "v_mfma_f32_16x16x32_bf16 v[200:203], v[200:203], v[240:243], 0\n\t" \
  "v_mfma_f32_16x16x32_bf16 v[204:207], v[204:207], v[240:243], 0\n\t" \
  "s_nop 7\n\t" \
  "v_cndmask_b32 v216, v200, v204, %[nh]\n\t" \
  "v_cndmask_b32 v217, v201, v205, %[nh]\n\t" \
  "v_cndmask_b32 v218, v202, v206, %[nh]\n\t" \
  "v_cndmask_b32 v219, v203, v207, %[nh]\n\t" \
  "ds_write_b128 v248, v[216:219] offset:18432\n\t" \
  "s_sleep 2\n\t" \
  "buffer_load_dword v252, v249, %[rs], %[sv] offen offset:0 sc1\n\t" \
  "buffer_load_dword v253, v249, %[rs], %[sv] offen offset:256 sc1\n\t" \
  "s_setprio 3\n\t" \
  "s_waitcnt lgkmcnt(0)\n\t" \
  "s_barrier\n\t" \
  "ds_read_b64_tr_b16 v[208:209], v250 offset:3072\n\t" \
  "ds_read_b64_tr_b16 v[210:211], v250 offset:3136\n\t" \
  "ds_read_b64_tr_b16 v[244:245], v250 offset:1536\n\t" \
  "ds_read_b64_tr_b16 v[246:247], v250 offset:1600\n\t" \
  "ds_read_b64_tr_b16 v[212:213], v250 offset:3584\n\t" \
  "ds_read_b64_tr_b16 v[214:215], v250 offset:3648\n\t" \
  "ds_read_b64_tr_b16 v[232:233], v250 offset:0\n\t" \
  "ds_read_b64_tr_b16 v[234:235], v250 offset:64\n\t" \
  "ds_read_b64_tr_b16 v[200:201], v250 offset:2048\n\t" \
  "ds_read_b64_tr_b16 v[202:203], v250 offset:2112\n\t" \
  "ds_read_b64_tr_b16 v[236:237], v250 offset:512\n\t" \
  "ds_read_b64_tr_b16 v[238:239], v250 offset:576\n\t" \
  "ds_read_b64_tr_b16 v[204:205], v250 offset:2560\n\t" \
  "ds_read_b64_tr_b16 v[206:207], v250 offset:2624\n\t" \
  "ds_read_b128 v[220:223], v248 offset:19456\n\t" \
  "s_waitcnt lgkmcnt(13)\n\t" \
  "v_mfma_f32_16x16x32_bf16 v[208:211], v[208:211], v[240:243], 0\n\t" \
  "ds_read_b128 v[224:227], v248 offset:16384\n\t" \
  "ds_read_b128 v[228:231], v248 offset:17408\n\t" \
  "s_waitcnt lgkmcnt(11)\n\t" \
  "v_mfma_f32_16x16x32_bf16 v[208:211], v[212:215], v[244:247], v[208:211]\n\t" \
  "v_mov_b32 v241, 0\n\t" \
  "v_mov_b32 v242, 0\n\t" \
  "s_waitcnt lgkmcnt(7)\n\t" \
  "v_mfma_f32_16x16x32_bf16 v[208:211], v[200:203], v[232:235], v[208:211]\n\t" \
  "s_waitcnt lgkmcnt(3)\n\t" \
  "v_mfma_f32_16x16x32_bf16 v[208:211], v[204:207], v[236:239], v[208:211]\n\t" \
  "s_waitcnt lgkmcnt(2)\n\t" \
  "v_pk_add_f32 v[216:217], v[216:217], v[220:221]\n\t" \
  "v_pk_add_f32 v[218:219], v[218:219], v[222:223]\n\t" \
  "s_waitcnt lgkmcnt(0)\n\t" \
  "v_pk_add_f32 v[224:225], v[224:225], v[228:229]\n\t" \
  "v_pk_add_f32 v[226:227], v[226:227], v[230:231]\n\t" \
  "v_pk_add_f32 v[216:217], v[216:217], v[224:225]\n\t" \
  "v_pk_add_f32 v[218:219], v[218:219], v[226:227]\n\t" \
  "v_max3_f32 v212, v216, v217, v218\n\t" \
  "v_cndmask_b32 v208, v208, v210, %[up]\n\t" \
  "v_cndmask_b32 v209, v209, v211, %[up]\n\t" \
  "v_max_f32 v212, v212, v219\n\t" \
  "v_max3_f32 v212, v212, v208, v209\n\t" \
  "v_mov_b32 v220, 0x3f803f80\n\t" \
  "v_mov_b32 v221, 0x3f803f80\n\t" \
  "v_max_f32_dpp v213, v212, v212 row_ror:8 row_mask:0xf bank_mask:0xf\n\t" \
  "v_max_f32_dpp v214, v212, v212 row_ror:8 row_mask:0xf bank_mask:0xf\n\t" \
  "v_mov_b64 v[222:223], v[220:221]\n\t" \
  "s_nop 0\n\t" \
  "v_permlane16_swap_b32 v213, v214\n\t" \
  "v_max_f32 v212, v213, v214\n\t" \
  "v_max_f32 v215, v213, v214\n\t" \
  "s_nop 1\n\t" \
  "v_permlane32_swap_b32 v212, v215\n\t" \
  "v_max_f32 v212, v212, v215\n\t" \
  "v_mul_f32 v212, %[negc], v212\n\t" \
  "v_fma_f32 v208, v208, %[c], v212\n\t" \
  "v_fma_f32 v209, v209, %[c], v212\n\t" \
  "v_exp_f32 v208, v208\n\t" \
  "v_fma_f32 v216, v216, %[c], v212\n\t" \
  "v_exp_f32 v209, v209\n\t" \
  "v_fma_f32 v217, v217, %[c], v212\n\t" \
  "v_exp_f32 v216, v216\n\t" \
  "v_fma_f32 v218, v218, %[c], v212\n\t" \
  "v_cvt_pk_bf16_f32 v240, v208, v209\n\t" \
  "v_cvt_pk_bf16_f32 v243, v208, v209\n\t" \
  "v_exp_f32 v217, v217\n\t" \
  "v_fma_f32 v219, v219, %[c], v212\n\t" \
  "v_mfma_f32_16x16x16_bf16 v[204:207], v[220:221], v[240:241], 0\n\t" \
  "v_exp_f32 v218, v218\n\t" \
  "s_waitcnt vmcnt(0)\n\t" \
  "v_mfma_f32_16x16x16_bf16 v[224:227], v[252:253], v[240:241], 0\n\t" \
  "v_cvt_pk_bf16_f32 v202, v216, v217\n\t" \
  "v_cvt_pk_bf16_f32 v200, v216, v217\n\t" \
  "v_exp_f32 v219, v219\n\t" \
  "v_mfma_f32_16x16x16_bf16 v[228:231], v[252:253], v[242:243], 0\n\t" \
  "v_cvt_pk_bf16_f32 v203, v218, v219\n\t" \
  "v_cvt_pk_bf16_f32 v201, v218, v219\n\t" \
  "v_mov_b32_dpp v200, v202 row_ror:8 row_mask:0xf bank_mask:0xc\n\t" \
  "v_mov_b32_dpp v202, v202 row_ror:8 row_mask:0xf bank_mask:0x3\n\t" \
  "v_mov_b32_dpp v201, v203 row_ror:8 row_mask:0xf bank_mask:0xc\n\t" \
  "v_mov_b32_dpp v203, v203 row_ror:8 row_mask:0xf bank_mask:0x3\n\t" \
  "v_max_u32 v212, v252, v253\n\t" \
  "v_cmp_le_u32 vcc, %[sent], v212\n\t" \
  "v_mfma_f32_16x16x32_bf16 v[204:207], v[220:223], v[200:203], v[204:207]\n\t" \
  ".Lpv%=:\n\t" \
  "v_mfma_f32_16x16x32_bf16 v[224:227], v[192:195], v[200:203], v[224:227]\n\t" \
  "v_mfma_f32_16x16x32_bf16 v[228:231], v[196:199], v[200:203], v[228:231]\n\t" \
  "s_cbranch_vccnz .Lvs%=\n\t" \
  ".Lvok%=:\n\t" \
  "s_nop 4\n\t" \
  "v_rcp_f32 v212, v204\n\t" \
  "v_cndmask_b32 v224, v224, v226, %[nh]\n\t" \
  "v_cndmask_b32 v225, v225, v227, %[nh]\n\t" \
  "v_mul_f32 v224, v224, v212\n\t" \
  "v_mul_f32 v225, v225, v212\n\t" \
  "v_cvt_pk_bf16_f32 v224, v224, v225\n\t" \
  "buffer_store_dword v224, v251, %[rs], %[so] offen offset:0 sc1\n\t" \
  "v_cndmask_b32 v228, v228, v230, %[nh]\n\t" \
  "v_cndmask_b32 v229, v229, v231, %[nh]\n\t" \
  "v_mul_f32 v228, v228, v212\n\t" \
  "v_mul_f32 v229, v229, v212\n\t" \
  "v_cvt_pk_bf16_f32 v228, v228, v229\n\t" \
  "buffer_store_dword v228, v251, %[rs], %[so] offen offset:256 sc1\n\t" \
  "s_memtime s[90:91]\n\t" \
  "s_setprio 0\n\t" \
  "s_waitcnt lgkmcnt(0)\n\t" \
  "s_mov_b32 %[tout], s90\n\t" \
  "s_mov_b32 %[spins], s92\n\t" \
  "s_branch .Lend%=\n\t" \
  ".Lvs%=:\n\t" \
  "v_cmp_eq_u32 vcc, %[sent], v252\n\t" \
  "v_cmp_eq_u32 s[94:95], %[sent], v253\n\t" \
  "s_or_b64 s[94:95], s[94:95], vcc\n\t" \
  "s_cbranch_scc0 .Lvok%=\n\t" \
  "s_add_u32 s92, s92, 1\n\t" \
  "s_sleep 2\n\t" \
  "s_and_b32 s93, s92, 63\n\t" \
  "s_cbranch_scc1 .Lvr%=\n\t" \
  "buffer_load_dword v212, off, %[rs], %[sab] sc1\n\t" \
  "s_waitcnt vmcnt(0)\n\t" \
  "v_readfirstlane_b32 s93, v212\n\t" \
  "s_nop 3\n\t" \
  "s_cmp_eq_u32 s93, 1\n\t" \
  "s_cbranch_scc1 .Lvok%=\n\t" \
  "s_cmp_lt_u32 s92, 0x80000\n\t" \
  "s_cbranch_scc1 .Lvr%=\n\t" \
  "v_mov_b32 v212, 1\n\t" \
  "buffer_store_dword v212, off, %[rs], %[sab] sc1\n\t" \
  "s_branch .Lvok%=\n\t" \
  ".Lvr%=:\n\t" \
  "buffer_load_dword v252, v249, %[rs], %[sv] offen offset:0 sc1\n\t" \
  "buffer_load_dword v253, v249, %[rs], %[sv] offen offset:256 sc1\n\t" \
  "s_waitcnt vmcnt(0)\n\t" \
  "v_mfma_f32_16x16x16_bf16 v[224:227], v[252:253], v[240:241], 0\n\t" \
  "v_mfma_f32_16x16x16_bf16 v[228:231], v[252:253], v[242:243], 0\n\t" \
  "v_max_u32 v212, v252, v253\n\t" \
  "v_cmp_le_u32 vcc, %[sent], v212\n\t" \
  "s_nop 4\n\t" \
  "s_branch .Lpv%=\n\t" \
  "s_branch .Lend%=\n\t" \
  ".Lslow%=:\n\t" \
  "s_mov_b64 s[94:95], 0\n\t" \
  "v_cmp_eq_u32 vcc, %[sent], v200\n\t" \
  "s_or_b64 s[94:95], s[94:95], vcc\n\t" \
  "v_cmp_eq_u32 vcc, %[sent], v201\n\t" \
  "s_or_b64 s[94:95], s[94:95], vcc\n\t" \
  "v_cmp_eq_u32 vcc, %[sent], v202\n\t" \
  "s_or_b64 s[94:95], s[94:95], vcc\n\t" \
  "v_cmp_eq_u32 vcc, %[sent], v203\n\t" \
  "s_or_b64 s[94:95], s[94:95], vcc\n\t" \
  "v_cmp_eq_u32 vcc, %[sent], v204\n\t" \
  "s_or_b64 s[94:95], s[94:95], vcc\n\t" \
  "v_cmp_eq_u32 vcc, %[sent], v205\n\t" \
  "s_or_b64 s[94:95], s[94:95], vcc\n\t" \
  "v_cmp_eq_u32 vcc, %[sent], v206\n\t" \
  "s_or_b64 s[94:95], s[94:95], vcc\n\t" \
  "v_cmp_eq_u32 vcc, %[sent], v207\n\t" \
  "s_or_b64 s[94:95], s[94:95], vcc\n\t" \
  "s_cmp_eq_u64 s[94:95], 0\n\t" \
  "s_cbranch_scc1 .Lok%=\n\t" \
  "s_add_u32 s92, s92, 1\n\t" \
  "s_cmp_eq_u32 s92, 1\n\t" \
  "s_cbranch_scc1 .Lpoll%=\n\t" \
  "s_sleep 6\n\t" \
  "s_and_b32 s93, s92, 63\n\t" \
  "s_cmp_lg_u32 s93, 0\n\t" \
  "s_cbranch_scc1 .Lpoll%=\n\t" \
  "v_mov_b32 v209, 0\n\t" \
  "buffer_load_dword v208, v209, %[rs], %[sab] offen sc1\n\t" \
  "s_waitcnt vmcnt(0)\n\t" \
  "v_readfirstlane_b32 s93, v208\n\t" \
  "s_nop 3\n\t" \
  "s_cmp_eq_u32 s93, 1\n\t" \
  "s_cbranch_scc1 .Lok%=\n\t" \
  "s_cmp_lt_u32 s92, 0x80000\n\t" \
  "s_cbranch_scc1 .Lpoll%=\n\t" \
  "v_mov_b32 v208, 1\n\t" \
  "buffer_store_dword v208, v209, %[rs], %[sab] offen sc1\n\t" \
  "s_branch .Lok%=\n\t" \
  ".Lend%=:\n\t"
#define QK_ASM_3 \
  "s_waitcnt lgkmcnt(0)\n\t" \
  "ds_read_b128 v[216:219], v250 offset:0\n\t" \
  "ds_read_b128 v[220:223], v250 offset:2048\n\t" \
  "ds_read_b128 v[224:227], v250 offset:1024\n\t" \
  "ds_read_b128 v[228:231], v250 offset:3072\n\t" \
  "v_mov_b64 v[192:193], 0\n\t" \
  "v_mov_b64 v[194:195], 0\n\t" \
  "v_mov_b64 v[196:197], 0\n\t" \
  "v_mov_b64 v[198:199], 0\n\t" \
  "s_memrealtime s[90:91]\n\t" \
  "s_waitcnt lgkmcnt(0)\n\t" \
  "s_lshl_b32 s93, s90, 2\n\t" \
  "s_sub_i32 s93, %[tgt], s93\n\t" \
  "s_cmp_le_i32 s93, 0\n\t" \
  "s_cbranch_scc1 .Lgo%=\n\t" \
  "s_min_i32 s93, s93, 8000\n\t" \
  "s_add_u32 s93, s93, 3\n\t" \
  "s_lshr_b32 s93, s93, 2\n\t" \
  "s_add_u32 s93, s93, s90\n\t" \
  "s_memrealtime s[90:91]\n\t" \
  ".Lpace%=:\n\t" \
  "s_memrealtime s[94:95]\n\t" \
  "s_waitcnt lgkmcnt(1)\n\t" \
  "s_sub_i32 s91, s90, s93\n\t" \
  "s_cmp_ge_i32 s91, 0\n\t" \
  "s_cbranch_scc1 .Lgo%=\n\t" \
  "s_memrealtime s[90:91]\n\t" \
  "s_waitcnt lgkmcnt(1)\n\t" \
  "s_sub_i32 s95, s94, s93\n\t" \
  "s_cmp_lt_i32 s95, 0\n\t" \
  "s_cbranch_scc1 .Lpace%=\n\t" \
  ".Lgo%=:\n\t" \
  "s_mov_b32 s92, 0\n\t" \
  ".Lpoll%=:\n\t" \
  "buffer_load_dwordx4 v[200:203], v248, %[rs], %[som] offen sc1\n\t" \
  "buffer_load_dwordx4 v[204:207], v248, %[rs], %[som] offen offset:1024 sc1\n\t" \
  "s_waitcnt vmcnt(1) lgkmcnt(0)\n\t" \
  "ds_write_b128 v249, v[200:203]\n\t" \
  "v_max3_u32 v208, v200, v201, v202\n\t" \
  "s_waitcnt vmcnt(0)\n\t" \
  "ds_write_b128 v249, v[204:207] offset:1024\n\t" \
  "v_max3_u32 v209, v204, v205, v206\n\t" \
  "v_max3_u32 v208, v208, v203, v207\n\t" \
  "v_max_u32 v208, v208, v209\n\t" \
  "v_cmp_le_u32 vcc, %[sent], v208\n\t" \
  "s_cbranch_vccnz .Lslow%=\n\t" \
  ".Lok%=:\n\t" \
  "v_smfmac_f32_16x16x64_bf16 v[192:195], v[200:203], v[0:7], v251\n\t" \
  "v_smfmac_f32_16x16x64_bf16 v[196:199], v[200:203], v[8:15], v251\n\t" \
  "s_waitcnt lgkmcnt(0)\n\t" \
  "s_barrier\n\t" \
  "s_memrealtime s[90:91]\n\t" \
  "v_mov_b32 v236, %[step]\n\t" \
  "v_lshrrev_b32 v238, 1, v248\n\t" \
  "s_mov_b64 exec, 0xffff\n\t" \
  "s_waitcnt lgkmcnt(0)\n\t" \
  "s_lshl_b32 s90, s90, 2\n\t" \
  "s_sub_i32 s93, s90, %[tgt]\n\t" \
  "s_cmp_gt_i32 s93, 240\n\t" \
  "s_cselect_b32 s93, 60, 0\n\t" \
  "s_add_u32 s93, s93, s90\n\t" \
  "s_add_u32 s93, s93, %[lead]\n\t" \
  "v_mov_b32 v237, s93\n\t" \
  "buffer_store_dwordx2 v[236:237], v238, %[rs], %[sbc] offen sc1\n\t" \
  "s_mov_b64 exec, -1\n\t" \
  "s_mov_b32 %[tb1], s90\n\t" \
  "ds_read_b128 v[208:211], v248 offset:8192\n\t" \
  "ds_read_b128 v[212:215], v248 offset:9216\n\t" \
  "ds_read_b128 v[236:239], v248 offset:10240\n\t" \
  "ds_read_b128 v[240:243], v248 offset:11264\n\t" \
  "ds_read_b128 v[244:247], v248 offset:12288\n\t" \
  "ds_read_b128 v[200:203], v248 offset:13312\n\t" \
  "v_smfmac_f32_16x16x64_bf16 v[192:195], v[204:207], v[16:23], v251\n\t" \
  "v_smfmac_f32_16x16x64_bf16 v[196:199], v[204:207], v[24:31], v251\n\t" \
  "s_waitcnt lgkmcnt(5)\n\t" \
  "v_smfmac_f32_16x16x64_bf16 v[192:195], v[208:211], v[32:39], v251\n\t" \
  "ds_read_b128 v[204:207], v248 offset:14336\n\t" \
  "v_smfmac_f32_16x16x64_bf16 v[196:199], v[208:211], v[40:47], v251\n\t" \
  "s_waitcnt lgkmcnt(5)\n\t" \
  "v_smfmac_f32_16x16x64_bf16 v[192:195], v[212:215], v[216:223], v251\n\t" \
  "ds_read_b128 v[216:219], v250 offset:4096\n\t" \
  "v_smfmac_f32_16x16x64_bf16 v[196:199], v[212:215], v[224:231], v251\n\t" \
  "s_waitcnt lgkmcnt(5)\n\t" \
  "ds_read_b128 v[220:223], v250 offset:6144\n\t" \
  "v_smfmac_f32_16x16x64_bf16 v[192:195], v[236:239], v[48:55], v251\n\t" \
  "ds_read_b128 v[224:227], v250 offset:5120\n\t" \
  "v_smfmac_f32_16x16x64_bf16 v[196:199], v[236:239], v[56:63], v251\n\t" \
  "s_waitcnt lgkmcnt(6)\n\t" \
  "ds_read_b128 v[228:231], v250 offset:7168\n\t" \
  "v_smfmac_f32_16x16x64_bf16 v[192:195], v[240:243], v[64:71], v251\n\t" \
  "ds_read_b128 v[208:211], v248 offset:15360\n\t" \
  "v_smfmac_f32_16x16x64_bf16 v[196:199], v[240:243], v[72:79], v251\n\t" \
  "s_waitcnt lgkmcnt(5)\n\t" \
  "ds_read_b128 v[212:215], v248 offset:0\n\t" \
  "v_smfmac_f32_16x16x64_bf16 v[192:195], v[244:247], v[80:87], v251\n\t" \
  "ds_read_b128 v[236:239], v248 offset:1024\n\t" \
  "v_smfmac_f32_16x16x64_bf16 v[196:199], v[244:247], v[88:95], v251\n\t" \
  "s_waitcnt lgkmcnt(5)\n\t" \
  "v_smfmac_f32_16x16x64_bf16 v[192:195], v[200:203], v[216:223], v251\n\t" \
  "s_waitcnt lgkmcnt(3)\n\t" \
  "ds_read_b128 v[216:219], v250 offset:8192\n\t" \
  "v_smfmac_f32_16x16x64_bf16 v[196:199], v[200:203], v[224:231], v251\n\t" \
  "ds_read_b128 v[220:223], v250 offset:10240\n\t" \
  "v_smfmac_f32_16x16x64_bf16 v[192:195], v[204:207], v[96:103], v251\n\t" \
  "ds_read_b128 v[224:227], v250 offset:9216\n\t" \
  "v_smfmac_f32_16x16x64_bf16 v[196:199], v[204:207], v[104:111], v251\n\t" \
  "s_waitcnt lgkmcnt(5)\n\t" \
  "ds_read_b128 v[228:231], v250 offset:11264\n\t" \
  "v_smfmac_f32_16x16x64_bf16 v[192:195], v[208:211], v[112:119], v251\n\t" \
  "ds_read_b128 v[240:243], v248 offset:2048\n\t" \
  "v_smfmac_f32_16x16x64_bf16 v[196:199], v[208:211], v[120:127], v251\n\t" \
  "s_waitcnt lgkmcnt(5)\n\t" \
  "ds_read_b128 v[244:247], v248 offset:3072\n\t" \
  "v_smfmac_f32_16x16x64_bf16 v[192:195], v[212:215], v[128:135], v251\n\t" \
  "ds_read_b128 v[200:203], v248 offset:4096\n\t" \
  "v_smfmac_f32_16x16x64_bf16 v[196:199], v[212:215], v[136:143], v251\n\t" \
  "s_waitcnt lgkmcnt(5)\n\t" \
  "v_smfmac_f32_16x16x64_bf16 v[192:195], v[236:239], v[216:223], v251\n\t" \
  "s_waitcnt lgkmcnt(3)\n\t" \
  "ds_read_b128 v[204:207], v248 offset:5120\n\t" \
  "v_smfmac_f32_16x16x64_bf16 v[196:199], v[236:239], v[224:231], v251\n\t" \
  "s_waitcnt lgkmcnt(3)\n\t" \
  "ds_read_b128 v[216:219], v250 offset:12288\n\t" \
  "v_smfmac_f32_16x16x64_bf16 v[192:195], v[240:243], v[144:151], v251\n\t" \
  "ds_read_b128 v[220:223], v250 offset:14336\n\t" \
  "v_smfmac_f32_16x16x64_bf16 v[196:199], v[240:243], v[152:159], v251\n\t" \
  "s_waitcnt lgkmcnt(4)\n\t" \
  "ds_read_b128 v[224:227], v250 offset:13312\n\t" \
  "v_smfmac_f32_16x16x64_bf16 v[192:195], v[244:247], v[160:167], v251\n\t" \
  "ds_read_b128 v[228:231], v250 offset:15360\n\t" \
  "v_smfmac_f32_16x16x64_bf16 v[196:199], v[244:247], v[168:175], v251\n\t" \
  "s_waitcnt lgkmcnt(5)\n\t" \
  "v_smfmac_f32_16x16x64_bf16 v[192:195], v[200:203], v[176:183], v251\n\t" \
  "v_smfmac_f32_16x16x64_bf16 v[196:199], v[200:203], v[184:191], v251\n\t" \
  "s_waitcnt lgkmcnt(2)\n\t" \
  "v_smfmac_f32_16x16x64_bf16 v[192:195], v[204:207], v[216:223], v251\n\t" \
  "s_waitcnt lgkmcnt(0)\n\t" \
  "v_smfmac_f32_16x16x64_bf16 v[196:199], v[204:207], v[224:231], v251\n\t" \
  "ds_read_b128 v[200:203], v248 offset:19456\n\t" \
  "ds_read_b128 v[204:207], v248 offset:23552\n\t" \
  "v_mov_b32 v250, v232\n\t" \
  "v_mov_b32 v249, v233\n\t" \
  "s_nop 2\n\t" \
  "v_add_f32 v192, v192, v194\n\t" \
  "v_add_f32 v193, v193, v195\n\t" \
  "v_add_f32 v196, v196, v198\n\t" \
  "v_add_f32 v197, v197, v199\n\t" \
  "v_cvt_pk_bf16_f32 v192, v192, v193\n\t" \
  "v_cvt_pk_bf16_f32 v196, v196, v197\n\t" \
  "ds_write2_b32 v252, v192, v196 offset1:64\n\t" \
  "v_mov_b32 v251, v234\n\t" \
  "ds_read_b64_tr_b16 v[244:245], v250 offset:1536\n\t" \
  "ds_read_b64_tr_b16 v[246:247], v250 offset:1600\n\t" \
  "buffer_load_dwordx4 v[192:195], v248, %[rt], %[svt] offen offset:0 sc1\n\t" \
  "buffer_load_dwordx4 v[196:199], v248, %[rt], %[svt] offen offset:1024 sc1\n\t" \
  "s_waitcnt lgkmcnt(0)\n\t" \
  "v_mfma_f32_16x16x32_bf16 v[200:203], v[200:203], v[244:247], 0\n\t" \
  "v_mfma_f32_16x16x32_bf16 v[204:207], v[204:207], v[244:247], 0\n\t" \
  "s_nop 7\n\t" \
  "v_cndmask_b32 v216, v200, v204, %[nh]\n\t" \
  "v_cndmask_b32 v217, v201, v205, %[nh]\n\t" \
  "v_cndmask_b32 v218, v202, v206, %[nh]\n\t" \
  "v_cndmask_b32 v219, v203, v207, %[nh]\n\t" \
  "ds_write_b128 v248, v[216:219] offset:19456\n\t" \
  "s_sleep 2\n\t" \
  "buffer_load_dword v252, v249, %[rs], %[sv] offen offset:0 sc1\n\t" \
  "buffer_load_dword v253, v249, %[rs], %[sv] offen offset:256 sc1\n\t" \
  "s_setprio 3\n\t" \
  "s_waitcnt lgkmcnt(0)\n\t" \
  "s_barrier\n\t" \
  "ds_read_b64_tr_b16 v[212:213], v250 offset:3584\n\t" \
  "ds_read_b64_tr_b16 v[214:215], v250 offset:3648\n\t" \
  "ds_read_b64_tr_b16 v[232:233], v250 offset:0\n\t" \
  "ds_read_b64_tr_b16 v[234:235], v250 offset:64\n\t" \
  "ds_read_b64_tr_b16 v[200:201], v250 offset:2048\n\t" \
  "ds_read_b64_tr_b16 v[202:203], v250 offset:2112\n\t" \
  "ds_read_b64_tr_b16 v[236:237], v250 offset:512\n\t" \
  "ds_read_b64_tr_b16 v[238:239], v250 offset:576\n\t" \
  "ds_read_b64_tr_b16 v[204:205], v250 offset:2560\n\t" \
  "ds_read_b64_tr_b16 v[206:207], v250 offset:2624\n\t" \
  "ds_read_b64_tr_b16 v[240:241], v250 offset:1024\n\t" \
  "ds_read_b64_tr_b16 v[242:243], v250 offset:1088\n\t" \
  "ds_read_b64_tr_b16 v[208:209], v250 offset:3072\n\t" \
  "ds_read_b64_tr_b16 v[210:211], v250 offset:3136\n\t" \
  "ds_read_b128 v[220:223], v248 offset:16384\n\t" \
  "s_waitcnt lgkmcnt(13)\n\t" \
  "v_mfma_f32_16x16x32_bf16 v[212:215], v[212:215], v[244:247], 0\n\t" \
  "ds_read_b128 v[224:227], v248 offset:17408\n\t" \
  "ds_read_b128 v[228:231], v248 offset:18432\n\t" \
  "s_waitcnt lgkmcnt(11)\n\t" \
  "v_mfma_f32_16x16x32_bf16 v[212:215], v[200:203], v[232:235], v[212:215]\n\t" \
  "v_mov_b32 v245, 0\n\t" \
  "v_mov_b32 v246, 0\n\t" \
  "s_waitcnt lgkmcnt(7)\n\t" \
  "v_mfma_f32_16x16x32_bf16 v[212:215], v[204:207], v[236:239], v[212:215]\n\t" \
  "s_waitcnt lgkmcnt(3)\n\t" \
  "v_mfma_f32_16x16x32_bf16 v[212:215], v[208:211], v[240:243], v[212:215]\n\t" \
  "s_waitcnt lgkmcnt(2)\n\t" \
  "v_pk_add_f32 v[216:217], v[216:217], v[220:221]\n\t" \
  "v_pk_add_f32 v[218:219], v[218:219], v[222:223]\n\t" \
  "s_waitcnt lgkmcnt(0)\n\t" \
  "v_pk_add_f32 v[224:225], v[224:225], v[228:229]\n\t" \
  "v_pk_add_f32 v[226:227], v[226:227], v[230:231]\n\t" \
  "v_pk_add_f32 v[216:217], v[216:217], v[224:225]\n\t" \
  "v_pk_add_f32 v[218:219], v[218:219], v[226:227]\n\t" \
  "v_max3_f32 v200, v216, v217, v218\n\t" \
  "v_cndmask_b32 v212, v212, v214, %[up]\n\t" \
  "v_cndmask_b32 v213, v213, v215, %[up]\n\t" \
  "v_max_f32 v200, v200, v219\n\t" \
  "v_max3_f32 v200, v200, v212, v213\n\t" \
  "v_mov_b32 v220, 0x3f803f80\n\t" \
  "v_mov_b32 v221, 0x3f803f80\n\t" \
  "v_max_f32_dpp v201, v200, v200 row_ror:8 row_mask:0xf bank_mask:0xf\n\t" \
  "v_max_f32_dpp v202, v200, v200 row_ror:8 row_mask:0xf bank_mask:0xf\n\t" \
  "v_mov_b64 v[222:223], v[220:221]\n\t" \
  "s_nop 0\n\t" \
  "v_permlane16_swap_b32 v201, v202\n\t" \
  "v_max_f32 v200, v201, v202\n\t" \
  "v_max_f32 v203, v201, v202\n\t" \
  "s_nop 1\n\t" \
  "v_permlane32_swap_b32 v200, v203\n\t" \
  "v_max_f32 v200, v200, v203\n\t" \
  "v_mul_f32 v200, %[negc], v200\n\t" \
  "v_fma_f32 v212, v212, %[c], v200\n\t" \
  "v_fma_f32 v213, v213, %[c], v200\n\t" \
  "v_exp_f32 v212, v212\n\t" \
  "v_fma_f32 v216, v216, %[c], v200\n\t" \
  "v_exp_f32 v213, v213\n\t" \
  "v_fma_f32 v217, v217, %[c], v200\n\t" \
  "v_exp_f32 v216, v216\n\t" \
  "v_fma_f32 v218, v218, %[c], v200\n\t" \
  "v_cvt_pk_bf16_f32 v244, v212, v213\n\t" \
  "v_cvt_pk_bf16_f32 v247, v212, v213\n\t" \
  "v_exp_f32 v217, v217\n\t" \
  "v_fma_f32 v219, v219, %[c], v200\n\t" \
  "v_mfma_f32_16x16x16_bf16 v[208:211], v[220:221], v[244:245], 0\n\t" \
  "v_exp_f32 v218, v218\n\t" \
  "s_waitcnt vmcnt(0)\n\t" \
  "v_mfma_f32_16x16x16_bf16 v[224:227], v[252:253], v[244:245], 0\n\t" \
  "v_cvt_pk_bf16_f32 v206, v216, v217\n\t" \
  "v_cvt_pk_bf16_f32 v204, v216, v217\n\t" \
  "v_exp_f32 v219, v219\n\t" \
  "v_mfma_f32_16x16x16_bf16 v[228:231], v[252:253], v[246:247], 0\n\t" \
  "v_cvt_pk_bf16_f32 v207, v218, v219\n\t" \
  "v_cvt_pk_bf16_f32 v205, v218, v219\n\t" \
  "v_mov_b32_dpp v204, v206 row_ror:8 row_mask:0xf bank_mask:0xc\n\t" \
  "v_mov_b32_dpp v206, v206 row_ror:8 row_mask:0xf bank_mask:0x3\n\t" \
  "v_mov_b32_dpp v205, v207 row_ror:8 row_mask:0xf bank_mask:0xc\n\t" \
  "v_mov_b32_dpp v207, v207 row_ror:8 row_mask:0xf bank_mask:0x3\n\t" \
  "v_max_u32 v200, v252, v253\n\t" \
  "v_cmp_le_u32 vcc, %[sent], v200\n\t" \
  "v_mfma_f32_16x16x32_bf16 v[208:211], v[220:223], v[204:207], v[208:211]\n\t" \
  ".Lpv%=:\n\t" \
  "v_mfma_f32_16x16x32_bf16 v[224:227], v[192:195], v[204:207], v[224:227]\n\t" \
  "v_mfma_f32_16x16x32_bf16 v[228:231], v[196:199], v[204:207], v[228:231]\n\t" \
  "s_cbranch_vccnz .Lvs%=\n\t" \
  ".Lvok%=:\n\t" \
  "s_nop 4\n\t" \
  "v_rcp_f32 v200, v208\n\t" \
  "v_cndmask_b32 v224, v224, v226, %[nh]\n\t" \
  "v_cndmask_b32 v225, v225, v227, %[nh]\n\t" \
  "v_mul_f32 v224, v224, v200\n\t" \
  "v_mul_f32 v225, v225, v200\n\t" \
  "v_cvt_pk_bf16_f32 v224, v224, v225\n\t" \
  "buffer_store_dword v224, v251, %[rs], %[so] offen offset:0 sc1\n\t" \
  "v_cndmask_b32 v228, v228, v230, %[nh]\n\t" \
  "v_cndmask_b32 v229, v229, v231, %[nh]\n\t" \
  "v_mul_f32 v228, v228, v200\n\t" \
  "v_mul_f32 v229, v229, v200\n\t" \
  "v_cvt_pk_bf16_f32 v228, v228, v229\n\t" \
  "buffer_store_dword v228, v251, %[rs], %[so] offen offset:256 sc1\n\t" \
  "s_memtime s[90:91]\n\t" \
  "s_setprio 0\n\t" \
  "s_waitcnt lgkmcnt(0)\n\t" \
  "s_mov_b32 %[tout], s90\n\t" \
  "s_mov_b32 %[spins], s92\n\t" \
  "s_branch .Lend%=\n\t" \
  ".Lvs%=:\n\t" \
  "v_cmp_eq_u32 vcc, %[sent], v252\n\t" \
  "v_cmp_eq_u32 s[94:95], %[sent], v253\n\t" \
  "s_or_b64 s[94:95], s[94:95], vcc\n\t" \
  "s_cbranch_scc0 .Lvok%=\n\t" \
  "s_add_u32 s92, s92, 1\n\t" \
  "s_sleep 2\n\t" \
  "s_and_b32 s93, s92, 63\n\t" \
  "s_cbranch_scc1 .Lvr%=\n\t" \
  "buffer_load_dword v200, off, %[rs], %[sab] sc1\n\t" \
  "s_waitcnt vmcnt(0)\n\t" \
  "v_readfirstlane_b32 s93, v200\n\t" \
  "s_nop 3\n\t" \
  "s_cmp_eq_u32 s93, 1\n\t" \
  "s_cbranch_scc1 .Lvok%=\n\t" \
  "s_cmp_lt_u32 s92, 0x80000\n\t" \
  "s_cbranch_scc1 .Lvr%=\n\t" \
  "v_mov_b32 v200, 1\n\t" \
  "buffer_store_dword v200, off, %[rs], %[sab] sc1\n\t" \
  "s_branch .Lvok%=\n\t" \
  ".Lvr%=:\n\t" \
  "buffer_load_dword v252, v249, %[rs], %[sv] offen offset:0 sc1\n\t" \
  "buffer_load_dword v253, v249, %[rs], %[sv] offen offset:256 sc1\n\t" \
  "s_waitcnt vmcnt(0)\n\t" \
  "v_mfma_f32_16x16x16_bf16 v[224:227], v[252:253], v[244:245], 0\n\t" \
  "v_mfma_f32_16x16x16_bf16 v[228:231], v[252:253], v[246:247], 0\n\t" \
  "v_max_u32 v200, v252, v253\n\t" \
  "v_cmp_le_u32 vcc, %[sent], v200\n\t" \
  "s_nop 4\n\t" \
  "s_branch .Lpv%=\n\t" \
  "s_branch .Lend%=\n\t" \
  ".Lslow%=:\n\t" \
  "s_mov_b64 s[94:95], 0\n\t" \
  "v_cmp_eq_u32 vcc, %[sent], v200\n\t" \
  "s_or_b64 s[94:95], s[94:95], vcc\n\t" \
  "v_cmp_eq_u32 vcc, %[sent], v201\n\t" \
  "s_or_b64 s[94:95], s[94:95], vcc\n\t" \
  "v_cmp_eq_u32 vcc, %[sent], v202\n\t" \
  "s_or_b64 s[94:95], s[94:95], vcc\n\t" \
  "v_cmp_eq_u32 vcc, %[sent], v203\n\t" \
  "s_or_b64 s[94:95], s[94:95], vcc\n\t" \
  "v_cmp_eq_u32 vcc, %[sent], v204\n\t" \
  "s_or_b64 s[94:95], s[94:95], vcc\n\t" \
  "v_cmp_eq_u32 vcc, %[sent], v205\n\t" \
  "s_or_b64 s[94:95], s[94:95], vcc\n\t" \
  "v_cmp_eq_u32 vcc, %[sent], v206\n\t" \
  "s_or_b64 s[94:95], s[94:95], vcc\n\t" \
  "v_cmp_eq_u32 vcc, %[sent], v207\n\t" \
  "s_or_b64 s[94:95], s[94:95], vcc\n\t" \
  "s_cmp_eq_u64 s[94:95], 0\n\t" \
  "s_cbranch_scc1 .Lok%=\n\t" \
  "s_add_u32 s92, s92, 1\n\t" \
  "s_cmp_eq_u32 s92, 1\n\t" \
  "s_cbranch_scc1 .Lpoll%=\n\t" \
  "s_sleep 6\n\t" \
  "s_and_b32 s93, s92, 63\n\t" \
  "s_cmp_lg_u32 s93, 0\n\t" \
  "s_cbranch_scc1 .Lpoll%=\n\t" \
  "v_mov_b32 v209, 0\n\t" \
  "buffer_load_dword v208, v209, %[rs], %[sab] offen sc1\n\t" \
  "s_waitcnt vmcnt(0)\n\t" \
  "v_readfirstlane_b32 s93, v208\n\t" \
  "s_nop 3\n\t" \
  "s_cmp_eq_u32 s93, 1\n\t" \
  "s_cbranch_scc1 .Lok%=\n\t" \
  "s_cmp_lt_u32 s92, 0x80000\n\t" \
  "s_cbranch_scc1 .Lpoll%=\n\t" \
  "v_mov_b32 v208, 1\n\t" \
  "buffer_store_dword v208, v209, %[rs], %[sab] offen sc1\n\t" \
  "s_branch .Lok%=\n\t" \
  ".Lend%=:\n\t"
#define QK_ASM_4 \
  "s_waitcnt lgkmcnt(0)\n\t" \
  "ds_read_b128 v[216:219], v250 offset:0\n\t" \
  "ds_read_b128 v[220:223], v250 offset:2048\n\t" \
  "ds_read_b128 v[224:227], v250 offset:1024\n\t" \
  "ds_read_b128 v[228:231], v250 offset:3072\n\t" \
  "v_mov_b64 v[192:193], 0\n\t" \
  "v_mov_b64 v[194:195], 0\n\t" \
  "v_mov_b64 v[196:197], 0\n\t" \
  "v_mov_b64 v[198:199], 0\n\t" \
  "s_memrealtime s[90:91]\n\t" \
  "s_waitcnt lgkmcnt(0)\n\t" \
  "s_lshl_b32 s93, s90, 2\n\t" \
  "s_sub_i32 s93, %[tgt], s93\n\t" \
  "s_cmp_le_i32 s93, 0\n\t" \
  "s_cbranch_scc1 .Lgo%=\n\t" \
  "s_min_i32 s93, s93, 8000\n\t" \
  "s_add_u32 s93, s93, 3\n\t" \
  "s_lshr_b32 s93, s93, 2\n\t" \
  "s_add_u32 s93, s93, s90\n\t" \
  "s_memrealtime s[90:91]\n\t" \
  ".Lpace%=:\n\t" \
  "s_memrealtime s[94:95]\n\t" \
  "s_waitcnt lgkmcnt(1)\n\t" \
  "s_sub_i32 s91, s90, s93\n\t" \
  "s_cmp_ge_i32 s91, 0\n\t" \
  "s_cbranch_scc1 .Lgo%=\n\t" \
  "s_memrealtime s[90:91]\n\t" \
  "s_waitcnt lgkmcnt(1)\n\t" \
  "s_sub_i32 s95, s94, s93\n\t" \
  "s_cmp_lt_i32 s95, 0\n\t" \
  "s_cbranch_scc1 .Lpace%=\n\t" \
  ".Lgo%=:\n\t" \
  "s_mov_b32 s92, 0\n\t" \
  ".Lpoll%=:\n\t" \
  "buffer_load_dwordx4 v[200:203], v248, %[rs], %[som] offen sc1\n\t" \
  "buffer_load_dwordx4 v[204:207], v248, %[rs], %[som] offen offset:1024 sc1\n\t" \
  "s_waitcnt vmcnt(1) lgkmcnt(0)\n\t" \
  "ds_write_b128 v249, v[200:203]\n\t" \
  "v_max3_u32 v208, v200, v201, v202\n\t" \
  "s_waitcnt vmcnt(0)\n\t" \
  "ds_write_b128 v249, v[204:207] offset:1024\n\t" \
  "v_max3_u32 v209, v204, v205, v206\n\t" \
  "v_max3_u32 v208, v208, v203, v207\n\t" \
  "v_max_u32 v208, v208, v209\n\t" \
  "v_cmp_le_u32 vcc, %[sent], v208\n\t" \
  "s_cbranch_vccnz .Lslow%=\n\t" \
  ".Lok%=:\n\t" \
  "v_smfmac_f32_16x16x64_bf16 v[192:195], v[200:203], v[0:7], v251\n\t" \
  "v_smfmac_f32_16x16x64_bf16 v[196:199], v[200:203], v[8:15], v251\n\t" \
  "s_waitcnt lgkmcnt(0)\n\t" \
  "s_barrier\n\t" \
  "ds_read_b128 v[208:211], v248 offset:10240\n\t" \
  "ds_read_b128 v[212:215], v248 offset:11264\n\t" \
  "ds_read_b128 v[236:239], v248 offset:12288\n\t" \
  "ds_read_b128 v[240:243], v248 offset:13312\n\t" \
  "ds_read_b128 v[244:247], v248 offset:14336\n\t" \
  "ds_read_b128 v[200:203], v248 offset:15360\n\t" \
  "v_smfmac_f32_16x16x64_bf16 v[192:195], v[204:207], v[16:23], v251\n\t" \
  "v_smfmac_f32_16x16x64_bf16 v[196:199], v[204:207], v[24:31], v251\n\t" \
  "s_waitcnt lgkmcnt(5)\n\t" \
  "v_smfmac_f32_16x16x64_bf16 v[192:195], v[208:211], v[32:39], v251\n\t" \
  "ds_read_b128 v[204:207], v248 offset:0\n\t" \
  "v_smfmac_f32_16x16x64_bf16 v[196:199], v[208:211], v[40:47], v251\n\t" \
  "s_waitcnt lgkmcnt(5)\n\t" \
  "v_smfmac_f32_16x16x64_bf16 v[192:195], v[212:215], v[216:223], v251\n\t" \
  "ds_read_b128 v[216:219], v250 offset:4096\n\t" \
  "v_smfmac_f32_16x16x64_bf16 v[196:199], v[212:215], v[224:231], v251\n\t" \
  "s_waitcnt lgkmcnt(5)\n\t" \
  "ds_read_b128 v[220:223], v250 offset:6144\n\t" \
  "v_smfmac_f32_16x16x64_bf16 v[192:195], v[236:239], v[48:55], v251\n\t" \
  "ds_read_b128 v[224:227], v250 offset:5120\n\t" \
  "v_smfmac_f32_16x16x64_bf16 v[196:199], v[236:239], v[56:63], v251\n\t" \
  "s_waitcnt lgkmcnt(6)\n\t" \
  "ds_read_b128 v[228:231], v250 offset:7168\n\t" \
  "v_smfmac_f32_16x16x64_bf16 v[192:195], v[240:243], v[64:71], v251\n\t" \
  "ds_read_b128 v[208:211], v248 offset:1024\n\t" \
  "v_smfmac_f32_16x16x64_bf16 v[196:199], v[240:243], v[72:79], v251\n\t" \
  "s_waitcnt lgkmcnt(5)\n\t" \
  "ds_read_b128 v[212:215], v248 offset:2048\n\t" \
  "v_smfmac_f32_16x16x64_bf16 v[192:195], v[244:247], v[80:87], v251\n\t" \
  "ds_read_b128 v[236:239], v248 offset:3072\n\t" \
  "v_smfmac_f32_16x16x64_bf16 v[196:199], v[244:247], v[88:95], v251\n\t" \
  "s_waitcnt lgkmcnt(5)\n\t" \
  "v_smfmac_f32_16x16x64_bf16 v[192:195], v[200:203], v[216:223], v251\n\t" \
  "s_waitcnt lgkmcnt(3)\n\t" \
  "ds_read_b128 v[216:219], v250 offset:8192\n\t" \
  "v_smfmac_f32_16x16x64_bf16 v[196:199], v[200:203], v[224:231], v251\n\t" \
  "ds_read_b128 v[220:223], v250 offset:10240\n\t" \
  "v_smfmac_f32_16x16x64_bf16 v[192:195], v[204:207], v[96:103], v251\n\t" \
  "ds_read_b128 v[224:227], v250 offset:9216\n\t" \
  "v_smfmac_f32_16x16x64_bf16 v[196:199], v[204:207], v[104:111], v251\n\t" \
  "s_waitcnt lgkmcnt(5)\n\t" \
  "ds_read_b128 v[228:231], v250 offset:11264\n\t" \
  "v_smfmac_f32_16x16x64_bf16 v[192:195], v[208:211], v[112:119], v251\n\t" \
  "ds_read_b128 v[240:243], v248 offset:4096\n\t" \
  "v_smfmac_f32_16x16x64_bf16 v[196:199], v[208:211], v[120:127], v251\n\t" \
  "s_waitcnt lgkmcnt(5)\n\t" \
  "ds_read_b128 v[244:247], v248 offset:5120\n\t" \
  "v_smfmac_f32_16x16x64_bf16 v[192:195], v[212:215], v[128:135], v251\n\t" \
  "ds_read_b128 v[200:203], v248 offset:6144\n\t" \
  "v_smfmac_f32_16x16x64_bf16 v[196:199], v[212:215], v[136:143], v251\n\t" \
  "s_waitcnt lgkmcnt(5)\n\t" \
  "v_smfmac_f32_16x16x64_bf16 v[192:195], v[236:239], v[216:223], v251\n\t" \
  "s_waitcnt lgkmcnt(3)\n\t" \
  "ds_read_b128 v[204:207], v248 offset:7168\n\t" \
  "v_smfmac_f32_16x16x64_bf16 v[196:199], v[236:239], v[224:231], v251\n\t" \
  "s_waitcnt lgkmcnt(3)\n\t" \
  "ds_read_b128 v[216:219], v250 offset:12288\n\t" \
  "v_smfmac_f32_16x16x64_bf16 v[192:195], v[240:243], v[144:151], v251\n\t" \
  "ds_read_b128 v[220:223], v250 offset:14336\n\t" \
  "v_smfmac_f32_16x16x64_bf16 v[196:199], v[240:243], v[152:159], v251\n\t" \
  "s_waitcnt lgkmcnt(4)\n\t" \
  "ds_read_b128 v[224:227], v250 offset:13312\n\t" \
  "v_smfmac_f32_16x16x64_bf16 v[192:195], v[244:247], v[160:167], v251\n\t" \
  "ds_read_b128 v[228:231], v250 offset:15360\n\t" \
  "v_smfmac_f32_16x16x64_bf16 v[196:199], v[244:247], v[168:175], v251\n\t" \
  "s_waitcnt lgkmcnt(5)\n\t" \
  "v_smfmac_f32_16x16x64_bf16 v[192:195], v[200:203], v[176:183], v251\n\t" \
  "v_smfmac_f32_16x16x64_bf16 v[196:199], v[200:203], v[184:191], v251\n\t" \
  "s_waitcnt lgkmcnt(2)\n\t" \
  "v_smfmac_f32_16x16x64_bf16 v[192:195], v[204:207], v[216:223], v251\n\t" \
  "s_waitcnt lgkmcnt(0)\n\t" \
  "v_smfmac_f32_16x16x64_bf16 v[196:199], v[204:207], v[224:231], v251\n\t" \
  "s_nop 6\n\t" \
  "v_add_f32 v192, v192, v194\n\t" \
  "v_add_f32 v193, v193, v195\n\t" \
  "v_add_f32 v196, v196, v198\n\t" \
  "v_add_f32 v197, v197, v199\n\t" \
  "v_cvt_pk_bf16_f32 v192, v192, v193\n\t" \
  "v_cvt_pk_bf16_f32 v196, v196, v197\n\t" \
  "ds_write2_b32 v252, v192, v196 offset1:64\n\t" \
  "s_waitcnt lgkmcnt(0)\n\t" \
  "s_barrier\n\t" \
  "buffer_store_dword v192, v235, %[rs], %[sk] offen sc1\n\t" \
  "buffer_store_dword v196, v235, %[rs], %[sk] offen offset:256 sc1\n\t" \
  "s_mov_b32 %[spins], s92\n\t" \
  "s_branch .Lend%=\n\t" \
  ".Lslow%=:\n\t" \
  "s_mov_b64 s[94:95], 0\n\t" \
  "v_cmp_eq_u32 vcc, %[sent], v200\n\t" \
  "s_or_b64 s[94:95], s[94:95], vcc\n\t" \
  "v_cmp_eq_u32 vcc, %[sent], v201\n\t" \
  "s_or_b64 s[94:95], s[94:95], vcc\n\t" \
  "v_cmp_eq_u32 vcc, %[sent], v202\n\t" \
  "s_or_b64 s[94:95], s[94:95], vcc\n\t" \
  "v_cmp_eq_u32 vcc, %[sent], v203\n\t" \
  "s_or_b64 s[94:95], s[94:95], vcc\n\t" \
  "v_cmp_eq_u32 vcc, %[sent], v204\n\t" \
  "s_or_b64 s[94:95], s[94:95], vcc\n\t" \
  "v_cmp_eq_u32 vcc, %[sent], v205\n\t" \
  "s_or_b64 s[94:95], s[94:95], vcc\n\t" \
  "v_cmp_eq_u32 vcc, %[sent], v206\n\t" \
  "s_or_b64 s[94:95], s[94:95], vcc\n\t" \
  "v_cmp_eq_u32 vcc, %[sent], v207\n\t" \
  "s_or_b64 s[94:95], s[94:95], vcc\n\t" \
  "s_cmp_eq_u64 s[94:95], 0\n\t" \
  "s_cbranch_scc1 .Lok%=\n\t" \
  "s_add_u32 s92, s92, 1\n\t" \
  "s_cmp_eq_u32 s92, 1\n\t" \
  "s_cbranch_scc1 .Lpoll%=\n\t" \
  "s_sleep 6\n\t" \
  "s_and_b32 s93, s92, 63\n\t" \
  "s_cmp_lg_u32 s93, 0\n\t" \
  "s_cbranch_scc1 .Lpoll%=\n\t" \
  "v_mov_b32 v209, 0\n\t" \
  "buffer_load_dword v208, v209, %[rs], %[sab] offen sc1\n\t" \
  "s_waitcnt vmcnt(0)\n\t" \
  "v_readfirstlane_b32 s93, v208\n\t" \
  "s_nop 3\n\t" \
  "s_cmp_eq_u32 s93, 1\n\t" \
  "s_cbranch_scc1 .Lok%=\n\t" \
  "s_cmp_lt_u32 s92, 0x80000\n\t" \
  "s_cbranch_scc1 .Lpoll%=\n\t" \
  "v_mov_b32 v208, 1\n\t" \
  "buffer_store_dword v208, v209, %[rs], %[sab] offen sc1\n\t" \
  "s_branch .Lok%=\n\t" \
  ".Lend%=:\n\t"
#define QK_ASM_5 \
  "s_waitcnt lgkmcnt(0)\n\t" \
  "ds_read_b128 v[216:219], v250 offset:0\n\t" \
  "ds_read_b128 v[220:223], v250 offset:2048\n\t" \
  "ds_read_b128 v[224:227], v250 offset:1024\n\t" \
  "ds_read_b128 v[228:231], v250 offset:3072\n\t" \
  "v_mov_b64 v[192:193], 0\n\t" \
  "v_mov_b64 v[194:195], 0\n\t" \
  "v_mov_b64 v[196:197], 0\n\t" \
  "v_mov_b64 v[198:199], 0\n\t" \
  "s_memrealtime s[90:91]\n\t" \
  "s_waitcnt lgkmcnt(0)\n\t" \
  "s_lshl_b32 s93, s90, 2\n\t" \
  "s_sub_i32 s93, %[tgt], s93\n\t" \
  "s_cmp_le_i32 s93, 0\n\t" \
  "s_cbranch_scc1 .Lgo%=\n\t" \
  "s_min_i32 s93, s93, 8000\n\t" \
  "s_add_u32 s93, s93, 3\n\t" \
  "s_lshr_b32 s93, s93, 2\n\t" \
  "s_add_u32 s93, s93, s90\n\t" \
  "s_memrealtime s[90:91]\n\t" \
  ".Lpace%=:\n\t" \
  "s_memrealtime s[94:95]\n\t" \
  "s_waitcnt lgkmcnt(1)\n\t" \
  "s_sub_i32 s91, s90, s93\n\t" \
  "s_cmp_ge_i32 s91, 0\n\t" \
  "s_cbranch_scc1 .Lgo%=\n\t" \
  "s_memrealtime s[90:91]\n\t" \
  "s_waitcnt lgkmcnt(1)\n\t" \
  "s_sub_i32 s95, s94, s93\n\t" \
  "s_cmp_lt_i32 s95, 0\n\t" \
  "s_cbranch_scc1 .Lpace%=\n\t" \
  ".Lgo%=:\n\t" \
  "s_mov_b32 s92, 0\n\t" \
  ".Lpoll%=:\n\t" \
  "buffer_load_dwordx4 v[200:203], v248, %[rs], %[som] offen sc1\n\t" \
  "buffer_load_dwordx4 v[204:207], v248, %[rs], %[som] offen offset:1024 sc1\n\t" \
  "s_waitcnt vmcnt(1) lgkmcnt(0)\n\t" \
  "ds_write_b128 v249, v[200:203]\n\t" \
  "v_max3_u32 v208, v200, v201, v202\n\t" \
  "s_waitcnt vmcnt(0)\n\t" \
  "ds_write_b128 v249, v[204:207] offset:1024\n\t" \
  "v_max3_u32 v209, v204, v205, v206\n\t" \
  "v_max3_u32 v208, v208, v203, v207\n\t" \
  "v_max_u32 v208, v208, v209\n\t" \
  "v_cmp_le_u32 vcc, %[sent], v208\n\t" \
  "s_cbranch_vccnz .Lslow%=\n\t" \
  ".Lok%=:\n\t" \
  "v_smfmac_f32_16x16x64_bf16 v[192:195], v[200:203], v[0:7], v251\n\t" \
  "v_smfmac_f32_16x16x64_bf16 v[196:199], v[200:203], v[8:15], v251\n\t" \
  "s_waitcnt lgkmcnt(0)\n\t" \
  "s_barrier\n\t" \
  "ds_read_b128 v[208:211], v248 offset:12288\n\t" \
  "ds_read_b128 v[212:215], v248 offset:13312\n\t" \
  "ds_read_b128 v[236:239], v248 offset:14336\n\t" \
  "ds_read_b128 v[240:243], v248 offset:15360\n\t" \
  "ds_read_b128 v[244:247], v248 offset:0\n\t" \
  "ds_read_b128 v[200:203], v248 offset:1024\n\t" \
  "v_smfmac_f32_16x16x64_bf16 v[192:195], v[204:207], v[16:23], v251\n\t" \
  "v_smfmac_f32_16x16x64_bf16 v[196:199], v[204:207], v[24:31], v251\n\t" \
  "s_waitcnt lgkmcnt(5)\n\t" \
  "v_smfmac_f32_16x16x64_bf16 v[192:195], v[208:211], v[32:39], v251\n\t" \
  "ds_read_b128 v[204:207], v248 offset:2048\n\t" \
  "v_smfmac_f32_16x16x64_bf16 v[196:199], v[208:211], v[40:47], v251\n\t" \
  "s_waitcnt lgkmcnt(5)\n\t" \
  "v_smfmac_f32_16x16x64_bf16 v[192:195], v[212:215], v[216:223], v251\n\t" \
  "ds_read_b128 v[216:219], v250 offset:4096\n\t" \
  "v_smfmac_f32_16x16x64_bf16 v[196:199], v[212:215], v[224:231], v251\n\t" \
  "s_waitcnt lgkmcnt(5)\n\t" \
  "ds_read_b128 v[220:223], v250 offset:6144\n\t" \
  "v_smfmac_f32_16x16x64_bf16 v[192:195], v[236:239], v[48:55], v251\n\t" \
  "ds_read_b128 v[224:227], v250 offset:5120\n\t" \
  "v_smfmac_f32_16x16x64_bf16 v[196:199], v[236:239], v[56:63], v251\n\t" \
  "s_waitcnt lgkmcnt(6)\n\t" \
  "ds_read_b128 v[228:231], v250 offset:7168\n\t" \
  "v_smfmac_f32_16x16x64_bf16 v[192:195], v[240:243], v[64:71], v251\n\t" \
  "ds_read_b128 v[208:211], v248 offset:3072\n\t" \
  "v_smfmac_f32_16x16x64_bf16 v[196:199], v[240:243], v[72:79], v251\n\t" \
  "s_waitcnt lgkmcnt(5)\n\t" \
  "ds_read_b128 v[212:215], v248 offset:4096\n\t" \
  "v_smfmac_f32_16x16x64_bf16 v[192:195], v[244:247], v[80:87], v251\n\t" \
  "ds_read_b128 v[236:239], v248 offset:5120\n\t" \
  "v_smfmac_f32_16x16x64_bf16 v[196:199], v[244:247], v[88:95], v251\n\t" \
  "s_waitcnt lgkmcnt(5)\n\t" \
  "v_smfmac_f32_16x16x64_bf16 v[192:195], v[200:203], v[216:223], v251\n\t" \
  "s_waitcnt lgkmcnt(3)\n\t" \
  "ds_read_b128 v[216:219], v250 offset:8192\n\t" \
  "v_smfmac_f32_16x16x64_bf16 v[196:199], v[200:203], v[224:231], v251\n\t" \
  "ds_read_b128 v[220:223], v250 offset:10240\n\t" \
  "v_smfmac_f32_16x16x64_bf16 v[192:195], v[204:207], v[96:103], v251\n\t" \
  "ds_read_b128 v[224:227], v250 offset:9216\n\t" \
  "v_smfmac_f32_16x16x64_bf16 v[196:199], v[204:207], v[104:111], v251\n\t" \
  "s_waitcnt lgkmcnt(5)\n\t" \
  "ds_read_b128 v[228:231], v250 offset:11264\n\t" \
  "v_smfmac_f32_16x16x64_bf16 v[192:195], v[208:211], v[112:119], v251\n\t" \
  "ds_read_b128 v[240:243], v248 offset:6144\n\t" \
  "v_smfmac_f32_16x16x64_bf16 v[196:199], v[208:211], v[120:127], v251\n\t" \
  "s_waitcnt lgkmcnt(5)\n\t" \
  "ds_read_b128 v[244:247], v248 offset:7168\n\t" \
  "v_smfmac_f32_16x16x64_bf16 v[192:195], v[212:215], v[128:135], v251\n\t" \
  "ds_read_b128 v[200:203], v248 offset:8192\n\t" \
  "v_smfmac_f32_16x16x64_bf16 v[196:199], v[212:215], v[136:143], v251\n\t" \
  "s_waitcnt lgkmcnt(5)\n\t" \
  "v_smfmac_f32_16x16x64_bf16 v[192:195], v[236:239], v[216:223], v251\n\t" \
  "s_waitcnt lgkmcnt(3)\n\t" \
  "ds_read_b128 v[204:207], v248 offset:9216\n\t" \
  "v_smfmac_f32_16x16x64_bf16 v[196:199], v[236:239], v[224:231], v251\n\t" \
  "s_waitcnt lgkmcnt(3)\n\t" \
  "ds_read_b128 v[216:219], v250 offset:12288\n\t" \
  "v_smfmac_f32_16x16x64_bf16 v[192:195], v[240:243], v[144:151], v251\n\t" \
  "ds_read_b128 v[220:223], v250 offset:14336\n\t" \
  "v_smfmac_f32_16x16x64_bf16 v[196:199], v[240:243], v[152:159], v251\n\t" \
  "s_waitcnt lgkmcnt(4)\n\t" \
  "ds_read_b128 v[224:227], v250 offset:13312\n\t" \
  "v_smfmac_f32_16x16x64_bf16 v[192:195], v[244:247], v[160:167], v251\n\t" \
  "ds_read_b128 v[228:231], v250 offset:15360\n\t" \
  "v_smfmac_f32_16x16x64_bf16 v[196:199], v[244:247], v[168:175], v251\n\t" \
  "s_waitcnt lgkmcnt(5)\n\t" \
  "v_smfmac_f32_16x16x64_bf16 v[192:195], v[200:203], v[176:183], v251\n\t" \
  "v_smfmac_f32_16x16x64_bf16 v[196:199], v[200:203], v[184:191], v251\n\t" \
  "s_waitcnt lgkmcnt(2)\n\t" \
  "v_smfmac_f32_16x16x64_bf16 v[192:195], v[204:207], v[216:223], v251\n\t" \
  "s_waitcnt lgkmcnt(0)\n\t" \
  "v_smfmac_f32_16x16x64_bf16 v[196:199], v[204:207], v[224:231], v251\n\t" \
  "s_nop 6\n\t" \
  "v_add_f32 v192, v192, v194\n\t" \
  "v_add_f32 v193, v193, v195\n\t" \
  "v_add_f32 v196, v196, v198\n\t" \
  "v_add_f32 v197, v197, v199\n\t" \
  "v_cvt_pk_bf16_f32 v192, v192, v193\n\t" \
  "v_cvt_pk_bf16_f32 v196, v196, v197\n\t" \
  "ds_write2_b32 v252, v192, v196 offset1:64\n\t" \
  "s_waitcnt lgkmcnt(0)\n\t" \
  "s_barrier\n\t" \
  "buffer_store_dword v192, v235, %[rs], %[sk] offen sc1\n\t" \
  "buffer_store_dword v196, v235, %[rs], %[sk] offen offset:256 sc1\n\t" \
  "s_mov_b32 %[spins], s92\n\t" \
  "s_branch .Lend%=\n\t" \
  ".Lslow%=:\n\t" \
  "s_mov_b64 s[94:95], 0\n\t" \
  "v_cmp_eq_u32 vcc, %[sent], v200\n\t" \
  "s_or_b64 s[94:95], s[94:95], vcc\n\t" \
  "v_cmp_eq_u32 vcc, %[sent], v201\n\t" \
  "s_or_b64 s[94:95], s[94:95], vcc\n\t" \
  "v_cmp_eq_u32 vcc, %[sent], v202\n\t" \
  "s_or_b64 s[94:95], s[94:95], vcc\n\t" \
  "v_cmp_eq_u32 vcc, %[sent], v203\n\t" \
  "s_or_b64 s[94:95], s[94:95], vcc\n\t" \
  "v_cmp_eq_u32 vcc, %[sent], v204\n\t" \
  "s_or_b64 s[94:95], s[94:95], vcc\n\t" \
  "v_cmp_eq_u32 vcc, %[sent], v205\n\t" \
  "s_or_b64 s[94:95], s[94:95], vcc\n\t" \
  "v_cmp_eq_u32 vcc, %[sent], v206\n\t" \
  "s_or_b64 s[94:95], s[94:95], vcc\n\t" \
  "v_cmp_eq_u32 vcc, %[sent], v207\n\t" \
  "s_or_b64 s[94:95], s[94:95], vcc\n\t" \
  "s_cmp_eq_u64 s[94:95], 0\n\t" \
  "s_cbranch_scc1 .Lok%=\n\t" \
  "s_add_u32 s92, s92, 1\n\t" \
  "s_cmp_eq_u32 s92, 1\n\t" \
  "s_cbranch_scc1 .Lpoll%=\n\t" \
  "s_sleep 6\n\t" \
  "s_and_b32 s93, s92, 63\n\t" \
  "s_cmp_lg_u32 s93, 0\n\t" \
  "s_cbranch_scc1 .Lpoll%=\n\t" \
  "v_mov_b32 v209, 0\n\t" \
  "buffer_load_dword v208, v209, %[rs], %[sab] offen sc1\n\t" \
  "s_waitcnt vmcnt(0)\n\t" \
  "v_readfirstlane_b32 s93, v208\n\t" \
  "s_nop 3\n\t" \
  "s_cmp_eq_u32 s93, 1\n\t" \
  "s_cbranch_scc1 .Lok%=\n\t" \
  "s_cmp_lt_u32 s92, 0x80000\n\t" \
  "s_cbranch_scc1 .Lpoll%=\n\t" \
  "v_mov_b32 v208, 1\n\t" \
  "buffer_store_dword v208, v209, %[rs], %[sab] offen sc1\n\t" \
  "s_branch .Lok%=\n\t" \
  ".Lend%=:\n\t"
#define QK_ASM_6 \
  "s_waitcnt lgkmcnt(0)\n\t" \
  "ds_read_b128 v[216:219], v250 offset:0\n\t" \
  "ds_read_b128 v[220:223], v250 offset:2048\n\t" \
  "ds_read_b128 v[224:227], v250 offset:1024\n\t" \
  "ds_read_b128 v[228:231], v250 offset:3072\n\t" \
  "v_mov_b64 v[192:193], 0\n\t" \
  "v_mov_b64 v[194:195], 0\n\t" \
  "v_mov_b64 v[196:197], 0\n\t" \
  "v_mov_b64 v[198:199], 0\n\t" \
  "s_memrealtime s[90:91]\n\t" \
  "s_waitcnt lgkmcnt(0)\n\t" \
  "s_lshl_b32 s93, s90, 2\n\t" \
  "s_sub_i32 s93, %[tgt], s93\n\t" \
  "s_cmp_le_i32 s93, 0\n\t" \
  "s_cbranch_scc1 .Lgo%=\n\t" \
  "s_min_i32 s93, s93, 8000\n\t" \
  "s_add_u32 s93, s93, 3\n\t" \
  "s_lshr_b32 s93, s93, 2\n\t" \
  "s_add_u32 s93, s93, s90\n\t" \
  "s_memrealtime s[90:91]\n\t" \
  ".Lpace%=:\n\t" \
  "s_memrealtime s[94:95]\n\t" \
  "s_waitcnt lgkmcnt(1)\n\t" \
  "s_sub_i32 s91, s90, s93\n\t" \
  "s_cmp_ge_i32 s91, 0\n\t" \
  "s_cbranch_scc1 .Lgo%=\n\t" \
  "s_memrealtime s[90:91]\n\t" \
  "s_waitcnt lgkmcnt(1)\n\t" \
  "s_sub_i32 s95, s94, s93\n\t" \
  "s_cmp_lt_i32 s95, 0\n\t" \
  "s_cbranch_scc1 .Lpace%=\n\t" \
  ".Lgo%=:\n\t" \
  "s_mov_b32 s92, 0\n\t" \
  ".Lpoll%=:\n\t" \
  "buffer_load_dwordx4 v[200:203], v248, %[rs], %[som] offen sc1\n\t" \
  "buffer_load_dwordx4 v[204:207], v248, %[rs], %[som] offen offset:1024 sc1\n\t" \
  "s_waitcnt vmcnt(1) lgkmcnt(0)\n\t" \
  "ds_write_b128 v249, v[200:203]\n\t" \
  "v_max3_u32 v208, v200, v201, v202\n\t" \
  "s_waitcnt vmcnt(0)\n\t" \
  "ds_write_b128 v249, v[204:207] offset:1024\n\t" \
  "v_max3_u32 v209, v204, v205, v206\n\t" \
  "v_max3_u32 v208, v208, v203, v207\n\t" \
  "v_max_u32 v208, v208, v209\n\t" \
  "v_cmp_le_u32 vcc, %[sent], v208\n\t" \
  "s_cbranch_vccnz .Lslow%=\n\t" \
  ".Lok%=:\n\t" \
  "v_smfmac_f32_16x16x64_bf16 v[192:195], v[200:203], v[0:7], v251\n\t" \
  "v_smfmac_f32_16x16x64_bf16 v[196:199], v[200:203], v[8:15], v251\n\t" \
  "s_waitcnt lgkmcnt(0)\n\t" \
  "s_barrier\n\t" \
  "ds_read_b128 v[208:211], v248 offset:14336\n\t" \
  "ds_read_b128 v[212:215], v248 offset:15360\n\t" \
  "ds_read_b128 v[236:239], v248 offset:0\n\t" \
  "ds_read_b128 v[240:243], v248 offset:1024\n\t" \
  "ds_read_b128 v[244:247], v248 offset:2048\n\t" \
  "ds_read_b128 v[200:203], v248 offset:3072\n\t" \
  "v_smfmac_f32_16x16x64_bf16 v[192:195], v[204:207], v[16:23], v251\n\t" \
  "v_smfmac_f32_16x16x64_bf16 v[196:199], v[204:207], v[24:31], v251\n\t" \
  "s_waitcnt lgkmcnt(5)\n\t" \
  "v_smfmac_f32_16x16x64_bf16 v[192:195], v[208:211], v[32:39], v251\n\t" \
  "ds_read_b128 v[204:207], v248 offset:4096\n\t" \
  "v_smfmac_f32_16x16x64_bf16 v[196:199], v[208:211], v[40:47], v251\n\t" \
  "s_waitcnt lgkmcnt(5)\n\t" \
  "v_smfmac_f32_16x16x64_bf16 v[192:195], v[212:215], v[216:223], v251\n\t" \
  "ds_read_b128 v[216:219], v250 offset:4096\n\t" \
  "v_smfmac_f32_16x16x64_bf16 v[196:199], v[212:215], v[224:231], v251\n\t" \
  "s_waitcnt lgkmcnt(5)\n\t" \
  "ds_read_b128 v[220:223], v250 offset:6144\n\t" \
  "v_smfmac_f32_16x16x64_bf16 v[192:195], v[236:239], v[48:55], v251\n\t" \
  "ds_read_b128 v[224:227], v250 offset:5120\n\t" \
  "v_smfmac_f32_16x16x64_bf16 v[196:199], v[236:239], v[56:63], v251\n\t" \
  "s_waitcnt lgkmcnt(6)\n\t" \
  "ds_read_b128 v[228:231], v250 offset:7168\n\t" \
  "v_smfmac_f32_16x16x64_bf16 v[192:195], v[240:243], v[64:71], v251\n\t" \
  "ds_read_b128 v[208:211], v248 offset:5120\n\t" \
  "v_smfmac_f32_16x16x64_bf16 v[196:199], v[240:243], v[72:79], v251\n\t" \
  "s_waitcnt lgkmcnt(5)\n\t" \
  "ds_read_b128 v[212:215], v248 offset:6144\n\t" \
  "v_smfmac_f32_16x16x64_bf16 v[192:195], v[244:247], v[80:87], v251\n\t" \
  "ds_read_b128 v[236:239], v248 offset:7168\n\t" \
  "v_smfmac_f32_16x16x64_bf16 v[196:199], v[244:247], v[88:95], v251\n\t" \
  "s_waitcnt lgkmcnt(5)\n\t" \
  "v_smfmac_f32_16x16x64_bf16 v[192:195], v[200:203], v[216:223], v251\n\t" \
  "s_waitcnt lgkmcnt(3)\n\t" \
  "ds_read_b128 v[216:219], v250 offset:8192\n\t" \
  "v_smfmac_f32_16x16x64_bf16 v[196:199], v[200:203], v[224:231], v251\n\t" \
  "ds_read_b128 v[220:223], v250 offset:10240\n\t" \
  "v_smfmac_f32_16x16x64_bf16 v[192:195], v[204:207], v[96:103], v251\n\t" \
  "ds_read_b128 v[224:227], v250 offset:9216\n\t" \
  "v_smfmac_f32_16x16x64_bf16 v[196:199], v[204:207], v[104:111], v251\n\t" \
  "s_waitcnt lgkmcnt(5)\n\t" \
  "ds_read_b128 v[228:231], v250 offset:11264\n\t" \
  "v_smfmac_f32_16x16x64_bf16 v[192:195], v[208:211], v[112:119], v251\n\t" \
  "ds_read_b128 v[240:243], v248 offset:8192\n\t" \
  "v_smfmac_f32_16x16x64_bf16 v[196:199], v[208:211], v[120:127], v251\n\t" \
  "s_waitcnt lgkmcnt(5)\n\t" \
  "ds_read_b128 v[244:247], v248 offset:9216\n\t" \
  "v_smfmac_f32_16x16x64_bf16 v[192:195], v[212:215], v[128:135], v251\n\t" \
  "ds_read_b128 v[200:203], v248 offset:10240\n\t" \
  "v_smfmac_f32_16x16x64_bf16 v[196:199], v[212:215], v[136:143], v251\n\t" \
  "s_waitcnt lgkmcnt(5)\n\t" \
  "v_smfmac_f32_16x16x64_bf16 v[192:195], v[236:239], v[216:223], v251\n\t" \
  "s_waitcnt lgkmcnt(3)\n\t" \
  "ds_read_b128 v[204:207], v248 offset:11264\n\t" \
  "v_smfmac_f32_16x16x64_bf16 v[196:199], v[236:239], v[224:231], v251\n\t" \
  "s_waitcnt lgkmcnt(3)\n\t" \
  "ds_read_b128 v[216:219], v250 offset:12288\n\t" \
  "v_smfmac_f32_16x16x64_bf16 v[192:195], v[240:243], v[144:151], v251\n\t" \
  "ds_read_b128 v[220:223], v250 offset:14336\n\t" \
  "v_smfmac_f32_16x16x64_bf16 v[196:199], v[240:243], v[152:159], v251\n\t" \
  "s_waitcnt lgkmcnt(4)\n\t" \
  "ds_read_b128 v[224:227], v250 offset:13312\n\t" \
  "v_smfmac_f32_16x16x64_bf16 v[192:195], v[244:247], v[160:167], v251\n\t" \
  "ds_read_b128 v[228:231], v250 offset:15360\n\t" \
  "v_smfmac_f32_16x16x64_bf16 v[196:199], v[244:247], v[168:175], v251\n\t" \
  "s_waitcnt lgkmcnt(5)\n\t" \
  "v_smfmac_f32_16x16x64_bf16 v[192:195], v[200:203], v[176:183], v251\n\t" \
  "v_smfmac_f32_16x16x64_bf16 v[196:199], v[200:203], v[184:191], v251\n\t" \
  "s_waitcnt lgkmcnt(2)\n\t" \
  "v_smfmac_f32_16x16x64_bf16 v[192:195], v[204:207], v[216:223], v251\n\t" \
  "s_waitcnt lgkmcnt(0)\n\t" \
  "v_smfmac_f32_16x16x64_bf16 v[196:199], v[204:207], v[224:231], v251\n\t" \
  "s_nop 6\n\t" \
  "v_add_f32 v192, v192, v194\n\t" \
  "v_add_f32 v193, v193, v195\n\t" \
  "v_add_f32 v196, v196, v198\n\t" \
  "v_add_f32 v197, v197, v199\n\t" \
  "v_cvt_pk_bf16_f32 v192, v192, v193\n\t" \
  "v_cvt_pk_bf16_f32 v196, v196, v197\n\t" \
  "ds_write2_b32 v252, v192, v196 offset1:64\n\t" \
  "s_waitcnt lgkmcnt(0)\n\t" \
  "s_barrier\n\t" \
  "buffer_store_dword v192, v235, %[rs], %[sk] offen sc1\n\t" \
  "buffer_store_dword v196, v235, %[rs], %[sk] offen offset:256 sc1\n\t" \
  "s_mov_b32 %[spins], s92\n\t" \
  "s_branch .Lend%=\n\t" \
  ".Lslow%=:\n\t" \
  "s_mov_b64 s[94:95], 0\n\t" \
  "v_cmp_eq_u32 vcc, %[sent], v200\n\t" \
  "s_or_b64 s[94:95], s[94:95], vcc\n\t" \
  "v_cmp_eq_u32 vcc, %[sent], v201\n\t" \
  "s_or_b64 s[94:95], s[94:95], vcc\n\t" \
  "v_cmp_eq_u32 vcc, %[sent], v202\n\t" \
  "s_or_b64 s[94:95], s[94:95], vcc\n\t" \
  "v_cmp_eq_u32 vcc, %[sent], v203\n\t" \
  "s_or_b64 s[94:95], s[94:95], vcc\n\t" \
  "v_cmp_eq_u32 vcc, %[sent], v204\n\t" \
  "s_or_b64 s[94:95], s[94:95], vcc\n\t" \
  "v_cmp_eq_u32 vcc, %[sent], v205\n\t" \
  "s_or_b64 s[94:95], s[94:95], vcc\n\t" \
  "v_cmp_eq_u32 vcc, %[sent], v206\n\t" \
  "s_or_b64 s[94:95], s[94:95], vcc\n\t" \
  "v_cmp_eq_u32 vcc, %[sent], v207\n\t" \
  "s_or_b64 s[94:95], s[94:95], vcc\n\t" \
  "s_cmp_eq_u64 s[94:95], 0\n\t" \
  "s_cbranch_scc1 .Lok%=\n\t" \
  "s_add_u32 s92, s92, 1\n\t" \
  "s_cmp_eq_u32 s92, 1\n\t" \
  "s_cbranch_scc1 .Lpoll%=\n\t" \
  "s_sleep 6\n\t" \
  "s_and_b32 s93, s92, 63\n\t" \
  "s_cmp_lg_u32 s93, 0\n\t" \
  "s_cbranch_scc1 .Lpoll%=\n\t" \
  "v_mov_b32 v209, 0\n\t" \
  "buffer_load_dword v208, v209, %[rs], %[sab] offen sc1\n\t" \
  "s_waitcnt vmcnt(0)\n\t" \
  "v_readfirstlane_b32 s93, v208\n\t" \
  "s_nop 3\n\t" \
  "s_cmp_eq_u32 s93, 1\n\t" \
  "s_cbranch_scc1 .Lok%=\n\t" \
  "s_cmp_lt_u32 s92, 0x80000\n\t" \
  "s_cbranch_scc1 .Lpoll%=\n\t" \
  "v_mov_b32 v208, 1\n\t" \
  "buffer_store_dword v208, v209, %[rs], %[sab] offen sc1\n\t" \
  "s_branch .Lok%=\n\t" \
  ".Lend%=:\n\t"
#define QK_ASM_7 \
  "s_waitcnt lgkmcnt(0)\n\t" \
  "ds_read_b128 v[216:219], v250 offset:0\n\t" \
  "ds_read_b128 v[220:223], v250 offset:2048\n\t" \
  "ds_read_b128 v[224:227], v250 offset:1024\n\t" \
  "ds_read_b128 v[228:231], v250 offset:3072\n\t" \
  "v_mov_b64 v[192:193], 0\n\t" \
  "v_mov_b64 v[194:195], 0\n\t" \
  "v_mov_b64 v[196:197], 0\n\t" \
  "v_mov_b64 v[198:199], 0\n\t" \
  "s_memrealtime s[90:91]\n\t" \
  "s_waitcnt lgkmcnt(0)\n\t" \
  "s_lshl_b32 s93, s90, 2\n\t" \
  "s_sub_i32 s93, %[tgt], s93\n\t" \
  "s_cmp_le_i32 s93, 0\n\t" \
  "s_cbranch_scc1 .Lgo%=\n\t" \
  "s_min_i32 s93, s93, 8000\n\t" \
  "s_add_u32 s93, s93, 3\n\t" \
  "s_lshr_b32 s93, s93, 2\n\t" \
  "s_add_u32 s93, s93, s90\n\t" \
  "s_memrealtime s[90:91]\n\t" \
  ".Lpace%=:\n\t" \
  "s_memrealtime s[94:95]\n\t" \
  "s_waitcnt lgkmcnt(1)\n\t" \
  "s_sub_i32 s91, s90, s93\n\t" \
  "s_cmp_ge_i32 s91, 0\n\t" \
  "s_cbranch_scc1 .Lgo%=\n\t" \
  "s_memrealtime s[90:91]\n\t" \
  "s_waitcnt lgkmcnt(1)\n\t" \
  "s_sub_i32 s95, s94, s93\n\t" \
  "s_cmp_lt_i32 s95, 0\n\t" \
  "s_cbranch_scc1 .Lpace%=\n\t" \
  ".Lgo%=:\n\t" \
  "s_mov_b32 s92, 0\n\t" \
  ".Lpoll%=:\n\t" \
  "buffer_load_dwordx4 v[200:203], v248, %[rs], %[som] offen sc1\n\t" \
  "buffer_load_dwordx4 v[204:207], v248, %[rs], %[som] offen offset:1024 sc1\n\t" \
  "s_waitcnt vmcnt(1) lgkmcnt(0)\n\t" \
  "ds_write_b128 v249, v[200:203]\n\t" \
  "v_max3_u32 v208, v200, v201, v202\n\t" \
  "s_waitcnt vmcnt(0)\n\t" \
  "ds_write_b128 v249, v[204:207] offset:1024\n\t" \
  "v_max3_u32 v209, v204, v205, v206\n\t" \
  "v_max3_u32 v208, v208, v203, v207\n\t" \
  "v_max_u32 v208, v208, v209\n\t" \
  "v_cmp_le_u32 vcc, %[sent], v208\n\t" \
  "s_cbranch_vccnz .Lslow%=\n\t" \
  ".Lok%=:\n\t" \
  "v_smfmac_f32_16x16x64_bf16 v[192:195], v[200:203], v[0:7], v251\n\t" \
  "v_smfmac_f32_16x16x64_bf16 v[196:199], v[200:203], v[8:15], v251\n\t" \
  "s_waitcnt lgkmcnt(0)\n\t" \
  "s_barrier\n\t" \
  "ds_read_b128 v[208:211], v248 offset:0\n\t" \
  "ds_read_b128 v[212:215], v248 offset:1024\n\t" \
  "ds_read_b128 v[236:239], v248 offset:2048\n\t" \
  "ds_read_b128 v[240:243], v248 offset:3072\n\t" \
  "ds_read_b128 v[244:247], v248 offset:4096\n\t" \
  "ds_read_b128 v[200:203], v248 offset:5120\n\t" \
  "v_smfmac_f32_16x16x64_bf16 v[192:195], v[204:207], v[16:23], v251\n\t" \
  "v_smfmac_f32_16x16x64_bf16 v[196:199], v[204:207], v[24:31], v251\n\t" \
  "s_waitcnt lgkmcnt(5)\n\t" \
  "v_smfmac_f32_16x16x64_bf16 v[192:195], v[208:211], v[32:39], v251\n\t" \
  "ds_read_b128 v[204:207], v248 offset:6144\n\t" \
  "v_smfmac_f32_16x16x64_bf16 v[196:199], v[208:211], v[40:47], v251\n\t" \
  "s_waitcnt lgkmcnt(5)\n\t" \
  "v_smfmac_f32_16x16x64_bf16 v[192:195], v[212:215], v[216:223], v251\n\t" \
  "ds_read_b128 v[216:219], v250 offset:4096\n\t" \
  "v_smfmac_f32_16x16x64_bf16 v[196:199], v[212:215], v[224:231], v251\n\t" \
  "s_waitcnt lgkmcnt(5)\n\t" \
  "ds_read_b128 v[220:223], v250 offset:6144\n\t" \
  "v_smfmac_f32_16x16x64_bf16 v[192:195], v[236:239], v[48:55], v251\n\t" \
  "ds_read_b128 v[224:227], v250 offset:5120\n\t" \
  "v_smfmac_f32_16x16x64_bf16 v[196:199], v[236:239], v[56:63], v251\n\t" \
  "s_waitcnt lgkmcnt(6)\n\t" \
  "ds_read_b128 v[228:231], v250 offset:7168\n\t" \
  "v_smfmac_f32_16x16x64_bf16 v[192:195], v[240:243], v[64:71], v251\n\t" \
  "ds_read_b128 v[208:211], v248 offset:7168\n\t" \
  "v_smfmac_f32_16x16x64_bf16 v[196:199], v[240:243], v[72:79], v251\n\t" \
  "s_waitcnt lgkmcnt(5)\n\t" \
  "ds_read_b128 v[212:215], v248 offset:8192\n\t" \
  "v_smfmac_f32_16x16x64_bf16 v[192:195], v[244:247], v[80:87], v251\n\t" \
  "ds_read_b128 v[236:239], v248 offset:9216\n\t" \
  "v_smfmac_f32_16x16x64_bf16 v[196:199], v[244:247], v[88:95], v251\n\t" \
  "s_waitcnt lgkmcnt(5)\n\t" \
  "v_smfmac_f32_16x16x64_bf16 v[192:195], v[200:203], v[216:223], v251\n\t" \
  "s_waitcnt lgkmcnt(3)\n\t" \
  "ds_read_b128 v[216:219], v250 offset:8192\n\t" \
  "v_smfmac_f32_16x16x64_bf16 v[196:199], v[200:203], v[224:231], v251\n\t" \
  "ds_read_b128 v[220:223], v250 offset:10240\n\t" \
  "v_smfmac_f32_16x16x64_bf16 v[192:195], v[204:207], v[96:103], v251\n\t" \
  "ds_read_b128 v[224:227], v250 offset:9216\n\t" \
  "v_smfmac_f32_16x16x64_bf16 v[196:199], v[204:207], v[104:111], v251\n\t" \
  "s_waitcnt lgkmcnt(5)\n\t" \
  "ds_read_b128 v[228:231], v250 offset:11264\n\t" \
  "v_smfmac_f32_16x16x64_bf16 v[192:195], v[208:211], v[112:119], v251\n\t" \
  "ds_read_b128 v[240:243], v248 offset:10240\n\t" \
  "v_smfmac_f32_16x16x64_bf16 v[196:199], v[208:211], v[120:127], v251\n\t" \
  "s_waitcnt lgkmcnt(5)\n\t" \
  "ds_read_b128 v[244:247], v248 offset:11264\n\t" \
  "v_smfmac_f32_16x16x64_bf16 v[192:195], v[212:215], v[128:135], v251\n\t" \
  "ds_read_b128 v[200:203], v248 offset:12288\n\t" \
  "v_smfmac_f32_16x16x64_bf16 v[196:199], v[212:215], v[136:143], v251\n\t" \
  "s_waitcnt lgkmcnt(5)\n\t" \
  "v_smfmac_f32_16x16x64_bf16 v[192:195], v[236:239], v[216:223], v251\n\t" \
  "s_waitcnt lgkmcnt(3)\n\t" \
  "ds_read_b128 v[204:207], v248 offset:13312\n\t" \
  "v_smfmac_f32_16x16x64_bf16 v[196:199], v[236:239], v[224:231], v251\n\t" \
  "s_waitcnt lgkmcnt(3)\n\t" \
  "ds_read_b128 v[216:219], v250 offset:12288\n\t" \
  "v_smfmac_f32_16x16x64_bf16 v[192:195], v[240:243], v[144:151], v251\n\t" \
  "ds_read_b128 v[220:223], v250 offset:14336\n\t" \
  "v_smfmac_f32_16x16x64_bf16 v[196:199], v[240:243], v[152:159], v251\n\t" \
  "s_waitcnt lgkmcnt(4)\n\t" \
  "ds_read_b128 v[224:227], v250 offset:13312\n\t" \
  "v_smfmac_f32_16x16x64_bf16 v[192:195], v[244:247], v[160:167], v251\n\t" \
  "ds_read_b128 v[228:231], v250 offset:15360\n\t" \
  "v_smfmac_f32_16x16x64_bf16 v[196:199], v[244:247], v[168:175], v251\n\t" \
  "s_waitcnt lgkmcnt(5)\n\t" \
  "v_smfmac_f32_16x16x64_bf16 v[192:195], v[200:203], v[176:183], v251\n\t" \
  "v_smfmac_f32_16x16x64_bf16 v[196:199], v[200:203], v[184:191], v251\n\t" \
  "s_waitcnt lgkmcnt(2)\n\t" \
  "v_smfmac_f32_16x16x64_bf16 v[192:195], v[204:207], v[216:223], v251\n\t" \
  "s_waitcnt lgkmcnt(0)\n\t" \
  "v_smfmac_f32_16x16x64_bf16 v[196:199], v[204:207], v[224:231], v251\n\t" \
  "s_nop 6\n\t" \
  "v_add_f32 v192, v192, v194\n\t" \
  "v_add_f32 v193, v193, v195\n\t" \
  "v_add_f32 v196, v196, v198\n\t" \
  "v_add_f32 v197, v197, v199\n\t" \
  "v_cvt_pk_bf16_f32 v192, v192, v193\n\t" \
  "v_cvt_pk_bf16_f32 v196, v196, v197\n\t" \
  "ds_write2_b32 v252, v192, v196 offset1:64\n\t" \
  "s_waitcnt lgkmcnt(0)\n\t" \
  "s_barrier\n\t" \
  "buffer_store_dword v192, v235, %[rs], %[sk] offen sc1\n\t" \
  "buffer_store_dword v196, v235, %[rs], %[sk] offen offset:256 sc1\n\t" \
  "s_mov_b32 %[spins], s92\n\t" \
  "s_branch .Lend%=\n\t" \
  ".Lslow%=:\n\t" \
  "s_mov_b64 s[94:95], 0\n\t" \
  "v_cmp_eq_u32 vcc, %[sent], v200\n\t" \
  "s_or_b64 s[94:95], s[94:95], vcc\n\t" \
  "v_cmp_eq_u32 vcc, %[sent], v201\n\t" \
  "s_or_b64 s[94:95], s[94:95], vcc\n\t" \
  "v_cmp_eq_u32 vcc, %[sent], v202\n\t" \
  "s_or_b64 s[94:95], s[94:95], vcc\n\t" \
  "v_cmp_eq_u32 vcc, %[sent], v203\n\t" \
  "s_or_b64 s[94:95], s[94:95], vcc\n\t" \
  "v_cmp_eq_u32 vcc, %[sent], v204\n\t" \
  "s_or_b64 s[94:95], s[94:95], vcc\n\t" \
  "v_cmp_eq_u32 vcc, %[sent], v205\n\t" \
  "s_or_b64 s[94:95], s[94:95], vcc\n\t" \
  "v_cmp_eq_u32 vcc, %[sent], v206\n\t" \
  "s_or_b64 s[94:95], s[94:95], vcc\n\t" \
  "v_cmp_eq_u32 vcc, %[sent], v207\n\t" \
  "s_or_b64 s[94:95], s[94:95], vcc\n\t" \
  "s_cmp_eq_u64 s[94:95], 0\n\t" \
  "s_cbranch_scc1 .Lok%=\n\t" \
  "s_add_u32 s92, s92, 1\n\t" \
  "s_cmp_eq_u32 s92, 1\n\t" \
  "s_cbranch_scc1 .Lpoll%=\n\t" \
  "s_sleep 6\n\t" \
  "s_and_b32 s93, s92, 63\n\t" \
  "s_cmp_lg_u32 s93, 0\n\t" \
  "s_cbranch_scc1 .Lpoll%=\n\t" \
  "v_mov_b32 v209, 0\n\t" \
  "buffer_load_dword v208, v209, %[rs], %[sab] offen sc1\n\t" \
  "s_waitcnt vmcnt(0)\n\t" \
  "v_readfirstlane_b32 s93, v208\n\t" \
  "s_nop 3\n\t" \
  "s_cmp_eq_u32 s93, 1\n\t" \
  "s_cbranch_scc1 .Lok%=\n\t" \
  "s_cmp_lt_u32 s92, 0x80000\n\t" \
  "s_cbranch_scc1 .Lpoll%=\n\t" \
  "v_mov_b32 v208, 1\n\t" \
  "buffer_store_dword v208, v209, %[rs], %[sab] offen sc1\n\t" \
  "s_branch .Lok%=\n\t" \
  ".Lend%=:\n\t"
#define VV_ASM \
  "s_waitcnt lgkmcnt(0)\n\t" \
  "v_mov_b64 v[128:129], 0\n\t" \
  "v_mov_b64 v[130:131], 0\n\t" \
  "s_memrealtime s[90:91]\n\t" \
  "s_waitcnt lgkmcnt(0)\n\t" \
  "s_lshl_b32 s93, s90, 2\n\t" \
  "s_sub_i32 s93, %[tgt], s93\n\t" \
  "s_cmp_le_i32 s93, 0\n\t" \
  "s_cbranch_scc1 .Lgo%=\n\t" \
  "s_min_i32 s93, s93, 8000\n\t" \
  "s_add_u32 s93, s93, 3\n\t" \
  "s_lshr_b32 s93, s93, 2\n\t" \
  "s_add_u32 s93, s93, s90\n\t" \
  "s_memrealtime s[90:91]\n\t" \
  ".Lpace%=:\n\t" \
  "s_memrealtime s[94:95]\n\t" \
  "s_waitcnt lgkmcnt(1)\n\t" \
  "s_sub_i32 s91, s90, s93\n\t" \
  "s_cmp_ge_i32 s91, 0\n\t" \
  "s_cbranch_scc1 .Lgo%=\n\t" \
  "s_memrealtime s[90:91]\n\t" \
  "s_waitcnt lgkmcnt(1)\n\t" \
  "s_sub_i32 s95, s94, s93\n\t" \
  "s_cmp_lt_i32 s95, 0\n\t" \
  "s_cbranch_scc1 .Lpace%=\n\t" \
  ".Lgo%=:\n\t" \
  "s_mov_b32 s92, 0\n\t" \
  ".Lpoll%=:\n\t" \
  "buffer_load_dwordx4 v[188:191], v200, %[rs], %[som] offen sc1\n\t" \
  "buffer_load_dwordx4 v[192:195], v200, %[rs], %[som] offen offset:1024 sc1\n\t" \
  "s_waitcnt vmcnt(1) lgkmcnt(0)\n\t" \
  "ds_write_b128 v201, v[188:191]\n\t" \
  "v_max3_u32 v196, v188, v189, v190\n\t" \
  "s_waitcnt vmcnt(0)\n\t" \
  "ds_write_b128 v201, v[192:195] offset:1024\n\t" \
  "v_max3_u32 v197, v192, v193, v194\n\t" \
  "v_max3_u32 v196, v196, v191, v195\n\t" \
  "v_max_u32 v196, v196, v197\n\t" \
  "v_cmp_le_u32 vcc, %[sent], v196\n\t" \
  "s_cbranch_vccnz .Lslow%=\n\t" \
  ".Lok%=:\n\t" \
  "s_waitcnt lgkmcnt(0)\n\t" \
  "s_barrier\n\t" \
  "ds_read_b128 v[132:135], v200 offset:0\n\t" \
  "ds_read_b128 v[136:139], v200 offset:1024\n\t" \
  "ds_read_b128 v[140:143], v200 offset:2048\n\t" \
  "ds_read_b128 v[144:147], v200 offset:3072\n\t" \
  "ds_read_b128 v[148:151], v200 offset:4096\n\t" \
  "s_waitcnt lgkmcnt(4)\n\t" \
  "ds_read_b128 v[152:155], v200 offset:5120\n\t" \
  "v_smfmac_f32_16x16x64_bf16 v[128:131], v[132:135], v[0:7], v202\n\t" \
  "s_waitcnt lgkmcnt(4)\n\t" \
  "ds_read_b128 v[156:159], v200 offset:6144\n\t" \
  "v_smfmac_f32_16x16x64_bf16 v[128:131], v[136:139], v[8:15], v202\n\t" \
  "s_waitcnt lgkmcnt(4)\n\t" \
  "ds_read_b128 v[160:163], v200 offset:7168\n\t" \
  "v_smfmac_f32_16x16x64_bf16 v[128:131], v[140:143], v[16:23], v202\n\t" \
  "s_waitcnt lgkmcnt(4)\n\t" \
  "ds_read_b128 v[164:167], v200 offset:8192\n\t" \
  "v_smfmac_f32_16x16x64_bf16 v[128:131], v[144:147], v[24:31], v202\n\t" \
  "s_waitcnt lgkmcnt(4)\n\t" \
  "ds_read_b128 v[168:171], v200 offset:9216\n\t" \
  "v_smfmac_f32_16x16x64_bf16 v[128:131], v[148:151], v[32:39], v202\n\t" \
  "s_waitcnt lgkmcnt(4)\n\t" \
  "ds_read_b128 v[172:175], v200 offset:10240\n\t" \
  "v_smfmac_f32_16x16x64_bf16 v[128:131], v[152:155], v[40:47], v202\n\t" \
  "s_waitcnt lgkmcnt(4)\n\t" \
  "ds_read_b128 v[176:179], v200 offset:11264\n\t" \
  "v_smfmac_f32_16x16x64_bf16 v[128:131], v[156:159], v[48:55], v202\n\t" \
  "s_waitcnt lgkmcnt(4)\n\t" \
  "ds_read_b128 v[180:183], v200 offset:12288\n\t" \
  "v_smfmac_f32_16x16x64_bf16 v[128:131], v[160:163], v[56:63], v202\n\t" \
  "s_waitcnt lgkmcnt(4)\n\t" \
  "ds_read_b128 v[184:187], v200 offset:13312\n\t" \
  "v_smfmac_f32_16x16x64_bf16 v[128:131], v[164:167], v[64:71], v202\n\t" \
  "s_waitcnt lgkmcnt(4)\n\t" \
  "ds_read_b128 v[188:191], v200 offset:14336\n\t" \
  "v_smfmac_f32_16x16x64_bf16 v[128:131], v[168:171], v[72:79], v202\n\t" \
  "s_waitcnt lgkmcnt(4)\n\t" \
  "ds_read_b128 v[192:195], v200 offset:15360\n\t" \
  "v_smfmac_f32_16x16x64_bf16 v[128:131], v[172:175], v[80:87], v202\n\t" \
  "s_waitcnt lgkmcnt(4)\n\t" \
  "v_smfmac_f32_16x16x64_bf16 v[128:131], v[176:179], v[88:95], v202\n\t" \
  "s_waitcnt lgkmcnt(3)\n\t" \
  "v_smfmac_f32_16x16x64_bf16 v[128:131], v[180:183], v[96:103], v202\n\t" \
  "s_waitcnt lgkmcnt(2)\n\t" \
  "v_smfmac_f32_16x16x64_bf16 v[128:131], v[184:187], v[104:111], v202\n\t" \
  "s_waitcnt lgkmcnt(1)\n\t" \
  "v_smfmac_f32_16x16x64_bf16 v[128:131], v[188:191], v[112:119], v202\n\t" \
  "s_waitcnt lgkmcnt(0)\n\t" \
  "v_smfmac_f32_16x16x64_bf16 v[128:131], v[192:195], v[120:127], v202\n\t" \
  "s_nop 7\n\t" \
  "v_add_f32 v128, v128, v130\n\t" \
  "v_add_f32 v129, v129, v131\n\t" \
  "v_cvt_pk_bf16_f32 v128, v128, v129\n\t" \
  "buffer_store_dword v128, v203, %[rs], %[sv] offen sc1\n\t" \
  "ds_write_b32 v204, v128\n\t" \
  "s_waitcnt lgkmcnt(0)\n\t" \
  "s_barrier\n\t" \
  "s_mov_b32 %[spins], s92\n\t" \
  "s_branch .Lend%=\n\t" \
  ".Lslow%=:\n\t" \
  "s_mov_b64 s[94:95], 0\n\t" \
  "v_cmp_eq_u32 vcc, %[sent], v188\n\t" \
  "s_or_b64 s[94:95], s[94:95], vcc\n\t" \
  "v_cmp_eq_u32 vcc, %[sent], v189\n\t" \
  "s_or_b64 s[94:95], s[94:95], vcc\n\t" \
  "v_cmp_eq_u32 vcc, %[sent], v190\n\t" \
  "s_or_b64 s[94:95], s[94:95], vcc\n\t" \
  "v_cmp_eq_u32 vcc, %[sent], v191\n\t" \
  "s_or_b64 s[94:95], s[94:95], vcc\n\t" \
  "v_cmp_eq_u32 vcc, %[sent], v192\n\t" \
  "s_or_b64 s[94:95], s[94:95], vcc\n\t" \
  "v_cmp_eq_u32 vcc, %[sent], v193\n\t" \
  "s_or_b64 s[94:95], s[94:95], vcc\n\t" \
  "v_cmp_eq_u32 vcc, %[sent], v194\n\t" \
  "s_or_b64 s[94:95], s[94:95], vcc\n\t" \
  "v_cmp_eq_u32 vcc, %[sent], v195\n\t" \
  "s_or_b64 s[94:95], s[94:95], vcc\n\t" \
  "s_cmp_eq_u64 s[94:95], 0\n\t" \
  "s_cbranch_scc1 .Lok%=\n\t" \
  "s_add_u32 s92, s92, 1\n\t" \
  "s_cmp_eq_u32 s92, 1\n\t" \
  "s_cbranch_scc1 .Lpoll%=\n\t" \
  "s_sleep 6\n\t" \
  "s_and_b32 s93, s92, 63\n\t" \
  "s_cmp_lg_u32 s93, 0\n\t" \
  "s_cbranch_scc1 .Lpoll%=\n\t" \
  "v_mov_b32 v197, 0\n\t" \
  "buffer_load_dword v196, v197, %[rs], %[sab] offen sc1\n\t" \
  "s_waitcnt vmcnt(0)\n\t" \
  "v_readfirstlane_b32 s93, v196\n\t" \
  "s_nop 3\n\t" \
  "s_cmp_eq_u32 s93, 1\n\t" \
  "s_cbranch_scc1 .Lok%=\n\t" \
  "s_cmp_lt_u32 s92, 0x80000\n\t" \
  "s_cbranch_scc1 .Lpoll%=\n\t" \
  "v_mov_b32 v196, 1\n\t" \
  "buffer_store_dword v196, v197, %[rs], %[sab] offen sc1\n\t" \
  "s_branch .Lok%=\n\t" \
  ".Lend%=:\n\t"
#define RED_ASM \
  "s_mov_b32 %[step], 0\n\t" \
  "s_lshl_b32 %[sh], %[step], 15\n\t" \
  "s_lshr_b32 s93, %[step], 4\n\t" \
  "s_lshr_b32 s93, %[pm], s93\n\t" \
  "s_and_b32 s93, s93, 4\n\t" \
  "s_lshl_b32 s93, s93, 11\n\t" \
  "s_lshl2_add_u32 %[so], %[sh], s93\n\t" \
  "s_sub_i32 s93, %[step], 3\n\t" \
  "s_lshl_b32 %[st], s93, 19\n\t" \
  "s_lshl_b32 s94, s93, 11\n\t" \
  "s_xor_b32 s94, s94, %[s0]\n\t" \
  "s_and_b32 s94, s94, 0x2000\n\t" \
  "s_or_b32 %[st], %[st], s94\n\t" \
  "s_cmp_lt_i32 s93, 0\n\t" \
  "s_cselect_b32 %[st], 0x70000000, %[st]\n\t" \
  "s_add_u32 %[st4], %[st], 4096\n\t" \
  "buffer_load_dwordx2 v[166:167], v173, %[rs], 0 offen sc1\n\t" \
  "v_mov_b64 v[144:145], 0\n\t" \
  "v_mov_b64 v[146:147], 0\n\t" \
  "v_mov_b64 v[148:149], 0\n\t" \
  "v_mov_b64 v[150:151], 0\n\t" \
  "v_mov_b64 v[152:153], 0\n\t" \
  "v_mov_b64 v[154:155], 0\n\t" \
  "v_mov_b64 v[156:157], 0\n\t" \
  "v_mov_b64 v[158:159], 0\n\t" \
  ".Lstep%=:\n\t" \
  "s_setprio 3\n\t" \
  "s_mov_b32 s92, 0\n\t" \
  ".Lcal%=:\n\t" \
  "s_waitcnt vmcnt(0)\n\t" \
  "s_memrealtime s[76:77]\n\t" \
  "v_cmp_ne_u32 vcc, %[step], v166\n\t" \
  "v_readlane_b32 s90, v167, 0\n\t" \
  "v_readlane_b32 s91, v167, 1\n\t" \
  "v_readlane_b32 s94, v167, 2\n\t" \
  "v_readlane_b32 s95, v167, 3\n\t" \
  "s_sub_i32 s90, s90, %[base]\n\t" \
  "s_sub_i32 s91, s91, %[base]\n\t" \
  "s_sub_i32 s94, s94, %[base]\n\t" \
  "s_sub_i32 s95, s95, %[base]\n\t" \
  "s_max_i32 s90, s90, s91\n\t" \
  "s_max_i32 s94, s94, s95\n\t" \
  "s_max_i32 s90, s90, s94\n\t" \
  "s_add_i32 s90, s90, %[ds]\n\t" \
  "s_waitcnt lgkmcnt(0)\n\t" \
  "s_lshl2_add_u32 s76, s76, %[mn]\n\t" \
  "s_sub_i32 s76, s76, %[base]\n\t" \
  "s_max_i32 s94, s90, s76\n\t" \
  "s_add_u32 %[E], s90, %[base]\n\t" \
  "s_and_b32 s93, vcc_lo, 0xf\n\t" \
  "s_cbranch_scc1 .Lbl%=\n\t" \
  ".Lgo%=:\n\t" \
  "v_mov_b32 v166, 7\n\t" \
  "v_mov_b32 v162, %[step]\n\t" \
  "s_mov_b64 exec, 1\n\t" \
  "v_mov_b32 v164, s94\n\t" \
  "ds_max_u32 v168, v164 offset:4100\n\t" \
  "ds_inc_rtn_u32 v161, v168, v166 offset:4096\n\t" \
  "ds_read_b32 v163, v168 offset:4100\n\t" \
  "s_waitcnt lgkmcnt(0)\n\t" \
  "v_readfirstlane_b32 s94, v161\n\t" \
  "v_readfirstlane_b32 s95, v163\n\t" \
  "s_mov_b64 exec, -1\n\t" \
  "s_cmp_lg_u32 s94, 7\n\t" \
  "s_cbranch_scc1 .Lna%=\n\t" \
  "s_add_u32 s95, s95, %[rl]\n\t" \
  "s_add_u32 s95, s95, %[base]\n\t" \
  "s_mov_b64 exec, 0xffff\n\t" \
  "v_mov_b32 v163, s95\n\t" \
  "buffer_store_dwordx2 v[162:163], v175, %[rs], 0 offen sc1\n\t" \
  "s_mov_b64 exec, -1\n\t" \
  ".Lna%=:\n\t" \
  "s_memrealtime s[90:91]\n\t" \
  "s_waitcnt lgkmcnt(0)\n\t" \
  "s_lshl_b32 s93, s90, 2\n\t" \
  "s_sub_i32 s93, %[E], s93\n\t" \
  "s_cmp_le_i32 s93, 0\n\t" \
  "s_cbranch_scc1 .Lgop%=\n\t" \
  "s_min_i32 s93, s93, 8000\n\t" \
  "s_add_u32 s93, s93, 3\n\t" \
  "s_lshr_b32 s93, s93, 2\n\t" \
  "s_add_u32 s93, s93, s90\n\t" \
  "s_memrealtime s[90:91]\n\t" \
  ".Lpacep%=:\n\t" \
  "s_memrealtime s[94:95]\n\t" \
  "s_waitcnt lgkmcnt(1)\n\t" \
  "s_sub_i32 s91, s90, s93\n\t" \
  "s_cmp_ge_i32 s91, 0\n\t" \
  "s_cbranch_scc1 .Lgop%=\n\t" \
  "s_memrealtime s[90:91]\n\t" \
  "s_waitcnt lgkmcnt(1)\n\t" \
  "s_sub_i32 s95, s94, s93\n\t" \
  "s_cmp_lt_i32 s95, 0\n\t" \
  "s_cbranch_scc1 .Lpacep%=\n\t" \
  ".Lgop%=:\n\t" \
  "buffer_load_dwordx4 v[128:131], v234, %[rs], %[so] offen sc1\n\t" \
  "buffer_load_dwordx4 v[132:135], v235, %[rs], %[so] offen sc1\n\t" \
  "buffer_load_dwordx4 v[136:139], v236, %[rs], %[so] offen sc1\n\t" \
  "buffer_load_dwordx4 v[140:143], v237, %[rs], %[so] offen sc1\n\t" \
  "s_waitcnt lgkmcnt(0)\n\t" \
  ".Lw%=:\n\t" \
  "s_waitcnt vmcnt(3)\n\t" \
  "v_smfmac_f32_16x16x64_bf16 v[144:147], v[128:131], v[0:7], v172\n\t" \
  "v_max3_u32 v164, v128, v129, 0\n\t" \
  "v_smfmac_f32_16x16x64_bf16 v[148:151], v[128:131], v[8:15], v172\n\t" \
  "v_max3_u32 v164, v130, v131, v164\n\t" \
  "v_smfmac_f32_16x16x64_bf16 v[152:155], v[128:131], v[16:23], v172\n\t" \
  "v_smfmac_f32_16x16x64_bf16 v[156:159], v[128:131], v[24:31], v172\n\t" \
  "buffer_load_dwordx4 v[176:179], v238, %[rt], %[st] offen offset:0 sc1\n\t" \
  "s_waitcnt vmcnt(3)\n\t" \
  "v_smfmac_f32_16x16x64_bf16 v[144:147], v[132:135], v[32:39], v172\n\t" \
  "v_max3_u32 v164, v132, v133, v164\n\t" \
  "v_smfmac_f32_16x16x64_bf16 v[148:151], v[132:135], v[40:47], v172\n\t" \
  "v_max3_u32 v164, v134, v135, v164\n\t" \
  "v_smfmac_f32_16x16x64_bf16 v[152:155], v[132:135], v[48:55], v172\n\t" \
  "v_smfmac_f32_16x16x64_bf16 v[156:159], v[132:135], v[56:63], v172\n\t" \
  "buffer_load_dwordx4 v[180:183], v238, %[rt], %[st] offen offset:1024 sc1\n\t" \
  "s_waitcnt vmcnt(3)\n\t" \
  "v_smfmac_f32_16x16x64_bf16 v[144:147], v[136:139], v[64:71], v172\n\t" \
  "v_max3_u32 v164, v136, v137, v164\n\t" \
  "v_smfmac_f32_16x16x64_bf16 v[148:151], v[136:139], v[72:79], v172\n\t" \
  "v_max3_u32 v164, v138, v139, v164\n\t" \
  "v_smfmac_f32_16x16x64_bf16 v[152:155], v[136:139], v[80:87], v172\n\t" \
  "v_smfmac_f32_16x16x64_bf16 v[156:159], v[136:139], v[88:95], v172\n\t" \
  "buffer_load_dwordx4 v[184:187], v238, %[rt], %[st4] offen offset:0 sc1\n\t" \
  "s_waitcnt vmcnt(3)\n\t" \
  "v_smfmac_f32_16x16x64_bf16 v[144:147], v[140:143], v[96:103], v172\n\t" \
  "v_max3_u32 v164, v140, v141, v164\n\t" \
  "v_smfmac_f32_16x16x64_bf16 v[148:151], v[140:143], v[104:111], v172\n\t" \
  "v_max3_u32 v164, v142, v143, v164\n\t" \
  "v_smfmac_f32_16x16x64_bf16 v[152:155], v[140:143], v[112:119], v172\n\t" \
  "v_cmp_le_u32 vcc, %[sent], v164\n\t" \
  "v_smfmac_f32_16x16x64_bf16 v[156:159], v[140:143], v[120:127], v172\n\t" \
  "buffer_load_dwordx4 v[188:191], v238, %[rt], %[st4] offen offset:1024 sc1\n\t" \
  "s_cbranch_vccnz .Lslow%=\n\t" \
  ".Lok%=:\n\t" \
  "v_add_f32_dpp v160, v144, v146 quad_perm:[0,1,2,3] row_mask:0x3 bank_mask:0xf\n\t" \
  "v_add_f32_dpp v161, v145, v147 quad_perm:[0,1,2,3] row_mask:0x3 bank_mask:0xf\n\t" \
  "v_add_f32_dpp v162, v148, v150 quad_perm:[0,1,2,3] row_mask:0x3 bank_mask:0xf\n\t" \
  "v_add_f32_dpp v163, v149, v151 quad_perm:[0,1,2,3] row_mask:0x3 bank_mask:0xf\n\t" \
  "v_add_f32_dpp v160, v152, v154 quad_perm:[0,1,2,3] row_mask:0xc bank_mask:0xf\n\t" \
  "v_add_f32_dpp v161, v153, v155 quad_perm:[0,1,2,3] row_mask:0xc bank_mask:0xf\n\t" \
  "v_add_f32_dpp v162, v156, v158 quad_perm:[0,1,2,3] row_mask:0xc bank_mask:0xf\n\t" \
  "v_add_f32_dpp v163, v157, v159 quad_perm:[0,1,2,3] row_mask:0xc bank_mask:0xf\n\t" \
  "s_nop 0\n\t" \
  "v_permlane32_swap_b32 v160, v162\n\t" \
  "v_permlane32_swap_b32 v161, v163\n\t" \
  "v_add_f32 v160, v160, v162\n\t" \
  "v_add_f32 v161, v161, v163\n\t" \
  "ds_write_b64 v169, v[160:161]\n\t" \
  "s_mov_b64 exec, 1\n\t" \
  "ds_inc_rtn_u32 v164, v168, v166 offset:4104\n\t" \
  "s_mov_b64 exec, -1\n\t" \
  "ds_read_b64 v[128:129], v170 offset:0\n\t" \
  "ds_read_b64 v[130:131], v170 offset:512\n\t" \
  "ds_read_b64 v[132:133], v170 offset:1024\n\t" \
  "ds_read_b64 v[134:135], v170 offset:1536\n\t" \
  "ds_read_b64 v[136:137], v170 offset:2048\n\t" \
  "ds_read_b64 v[138:139], v170 offset:2560\n\t" \
  "ds_read_b64 v[140:141], v170 offset:3072\n\t" \
  "ds_read_b64 v[142:143], v170 offset:3584\n\t" \
  "s_waitcnt lgkmcnt(8)\n\t" \
  "v_readfirstlane_b32 s93, v164\n\t" \
  "s_cmp_lg_u32 s93, 7\n\t" \
  "s_cbranch_scc1 .Lb1%=\n\t" \
  "s_waitcnt lgkmcnt(4)\n\t" \
  "v_pk_add_f32 v[128:129], v[128:129], v[130:131]\n\t" \
  "v_pk_add_f32 v[132:133], v[132:133], v[134:135]\n\t" \
  "s_waitcnt lgkmcnt(0)\n\t" \
  "v_pk_add_f32 v[136:137], v[136:137], v[138:139]\n\t" \
  "v_pk_add_f32 v[140:141], v[140:141], v[142:143]\n\t" \
  "v_pk_add_f32 v[128:129], v[128:129], v[132:133]\n\t" \
  "v_pk_add_f32 v[136:137], v[136:137], v[140:141]\n\t" \
  "v_pk_add_f32 v[128:129], v[128:129], v[136:137]\n\t" \
  "v_cvt_pk_bf16_f32 v128, v128, v129\n\t" \
  "s_nop 1\n\t" \
  "v_mov_b32_dpp v129, v128 quad_perm:[1,0,3,2] row_mask:0xf bank_mask:0xf\n\t" \
  "v_perm_b32 v128, v129, v128, v174\n\t" \
  "buffer_store_dword v128, v171, %[rs], %[sh] offen sc1\n\t" \
  "s_memtime s[90:91]\n\t" \
  "s_barrier\n\t" \
  "s_memtime s[76:77]\n\t" \
  "s_memrealtime s[78:79]\n\t" \
  "s_nop 3\n\t" \
  "s_memrealtime s[80:81]\n\t" \
  "s_nop 3\n\t" \
  "s_memrealtime s[82:83]\n\t" \
  "s_nop 3\n\t" \
  "s_memrealtime s[84:85]\n\t" \
  "s_nop 3\n\t" \
  "s_memrealtime s[86:87]\n\t" \
  "s_nop 3\n\t" \
  "s_memrealtime s[88:89]\n\t" \
  "s_mov_b32 s93, 0\n\t" \
  "s_waitcnt lgkmcnt(0)\n\t" \
  "s_cmp_eq_u32 s80, s78\n\t" \
  "s_addc_u32 s93, s93, 0\n\t" \
  "s_cmp_eq_u32 s82, s78\n\t" \
  "s_addc_u32 s93, s93, 0\n\t" \
  "s_cmp_eq_u32 s84, s78\n\t" \
  "s_addc_u32 s93, s93, 0\n\t" \
  "s_cmp_eq_u32 s86, s78\n\t" \
  "s_addc_u32 s93, s93, 0\n\t" \
  "s_cmp_eq_u32 s88, s78\n\t" \
  "s_addc_u32 s93, s93, 0\n\t" \
  "s_mul_i32 s93, s93, 21\n\t" \
  "s_add_u32 s76, s76, 15\n\t" \
  "s_add_u32 s76, s76, s93\n\t" \
  "s_lshl2_add_u32 s78, s78, 16\n\t" \
  "s_mov_b64 exec, 1\n\t" \
  "s_sub_i32 s90, s76, s90\n\t" \
  "s_mul_i32 s90, s90, 11900\n\t" \
  "s_ashr_i32 s90, s90, 16\n\t" \
  "s_sub_u32 s90, s78, s90\n\t" \
  "s_sub_u32 s90, s90, %[base]\n\t" \
  "v_mov_b32 v164, s90\n\t" \
  "ds_write_b32 v168, v164 offset:4108\n\t" \
  "s_mov_b64 exec, -1\n\t" \
  "s_waitcnt vmcnt(1)\n\t" \
  ".Lb2%=:\n\t" \
  "s_setprio 0\n\t" \
  "s_bitcmp0_b64 %[am], %[step]\n\t" \
  "s_cbranch_scc1 .La1%=\n\t" \
  "buffer_store_dwordx4 v[224:227], v228, %[ra], 0 offen sc1\n\t" \
  ".La1%=:\n\t" \
  "buffer_load_dwordx4 v[192:195], v239, %[rt], %[st] offen offset:0 sc1\n\t" \
  "buffer_load_dwordx4 v[196:199], v239, %[rt], %[st] offen offset:1024 sc1\n\t" \
  "v_mfma_f32_16x16x32_bf16 v[128:131], v[0:3], v[176:179], 0\n\t" \
  "v_mfma_f32_16x16x32_bf16 v[136:139], v[8:11], v[176:179], 0\n\t" \
  "ds_read2st64_b32 v[144:145], v231 offset0:0 offset1:8\n\t" \
  "ds_read2st64_b32 v[146:147], v231 offset0:16 offset1:24\n\t" \
  "ds_read2st64_b32 v[148:149], v231 offset0:32 offset1:40\n\t" \
  "ds_read2st64_b32 v[150:151], v231 offset0:48 offset1:56\n\t" \
  "buffer_load_dwordx4 v[200:203], v239, %[rt], %[st4] offen offset:0 sc1\n\t" \
  "buffer_load_dwordx4 v[204:207], v239, %[rt], %[st4] offen offset:1024 sc1\n\t" \
  "v_mfma_f32_16x16x32_bf16 v[132:135], v[4:7], v[180:183], 0\n\t" \
  "v_mfma_f32_16x16x32_bf16 v[140:143], v[12:15], v[180:183], 0\n\t" \
  "s_waitcnt lgkmcnt(0)\n\t" \
  "v_add_f32 v144, v144, v145\n\t" \
  "v_add_f32 v146, v146, v147\n\t" \
  "v_add_f32 v148, v148, v149\n\t" \
  "v_add_f32 v150, v150, v151\n\t" \
  "v_add_f32 v144, v144, v146\n\t" \
  "v_add_f32 v148, v148, v150\n\t" \
  "buffer_load_dwordx4 v[208:211], v240, %[rt], %[st] offen offset:0 sc1\n\t" \
  "buffer_load_dwordx4 v[212:215], v240, %[rt], %[st] offen offset:1024 sc1\n\t" \
  "v_mfma_f32_16x16x32_bf16 v[128:131], v[16:19], v[184:187], v[128:131]\n\t" \
  "v_mfma_f32_16x16x32_bf16 v[136:139], v[24:27], v[184:187], v[136:139]\n\t" \
  "v_add_f32 v144, v144, v148\n\t" \
  "v_cvt_pk_bf16_f32 v144, v144, v144\n\t" \
  "ds_write_b16 v232, v144\n\t" \
  "ds_read_b128 v[224:227], v229\n\t" \
  "v_add_u32 v228, %[ainc], v228\n\t" \
  "v_add_u32 v229, %[rinc], v229\n\t" \
  "v_add_u32 v232, 1024, v232\n\t" \
  "v_xor_b32 v231, 0x4000, v231\n\t" \
  "buffer_load_dwordx4 v[216:219], v240, %[rt], %[st4] offen offset:0 sc1\n\t" \
  "buffer_load_dwordx4 v[220:223], v240, %[rt], %[st4] offen offset:1024 sc1\n\t" \
  "v_mfma_f32_16x16x32_bf16 v[132:135], v[20:23], v[188:191], v[132:135]\n\t" \
  "v_mfma_f32_16x16x32_bf16 v[140:143], v[28:31], v[188:191], v[140:143]\n\t" \
  "buffer_load_dwordx4 v[176:179], v241, %[rt], %[st] offen offset:0 sc1\n\t" \
  "s_waitcnt vmcnt(8)\n\t" \
  "v_mfma_f32_16x16x32_bf16 v[128:131], v[32:35], v[192:195], v[128:131]\n\t" \
  "v_mfma_f32_16x16x32_bf16 v[136:139], v[40:43], v[192:195], v[136:139]\n\t" \
  "buffer_load_dwordx4 v[180:183], v241, %[rt], %[st] offen offset:1024 sc1\n\t" \
  "s_waitcnt vmcnt(8)\n\t" \
  "v_mfma_f32_16x16x32_bf16 v[132:135], v[36:39], v[196:199], v[132:135]\n\t" \
  "v_mfma_f32_16x16x32_bf16 v[140:143], v[44:47], v[196:199], v[140:143]\n\t" \
  "buffer_load_dwordx4 v[184:187], v241, %[rt], %[st4] offen offset:0 sc1\n\t" \
  "s_waitcnt vmcnt(8)\n\t" \
  "v_mfma_f32_16x16x32_bf16 v[128:131], v[48:51], v[200:203], v[128:131]\n\t" \
  "v_mfma_f32_16x16x32_bf16 v[136:139], v[56:59], v[200:203], v[136:139]\n\t" \
  "ds_read_b128 v[160:163], v168 offset:4096\n\t" \
  "buffer_load_dwordx4 v[188:191], v241, %[rt], %[st4] offen offset:1024 sc1\n\t" \
  "s_waitcnt vmcnt(8)\n\t" \
  "v_mfma_f32_16x16x32_bf16 v[132:135], v[52:55], v[204:207], v[132:135]\n\t" \
  "v_mfma_f32_16x16x32_bf16 v[140:143], v[60:63], v[204:207], v[140:143]\n\t" \
  "s_waitcnt lgkmcnt(0)\n\t" \
  "v_readfirstlane_b32 s90, v161\n\t" \
  "v_readfirstlane_b32 s91, v163\n\t" \
  "s_add_u32 s93, s90, %[rl]\n\t" \
  "s_add_u32 s93, s93, %[base]\n\t" \
  "s_add_u32 %[E], s93, %[lk]\n\t" \
  "s_sub_i32 s91, s91, s90\n\t" \
  "s_sub_i32 s91, s91, %[rl]\n\t" \
  "s_min_i32 s93, s91, %[up]\n\t" \
  "s_max_i32 s93, s93, %[dn]\n\t" \
  "s_cmp_gt_i32 s91, 60\n\t" \
  "s_cselect_b32 s93, 2, s93\n\t" \
  "s_add_i32 %[rl], %[rl], s93\n\t" \
  "s_waitcnt vmcnt(7)\n\t" \
  "v_mfma_f32_16x16x32_bf16 v[128:131], v[64:67], v[208:211], v[128:131]\n\t" \
  "v_mfma_f32_16x16x32_bf16 v[136:139], v[72:75], v[208:211], v[136:139]\n\t" \
  "s_add_u32 %[step], %[step], 1\n\t" \
  "s_lshl_b32 %[sh], %[step], 15\n\t" \
  "s_lshr_b32 s93, %[step], 4\n\t" \
  "s_lshr_b32 s93, %[pm], s93\n\t" \
  "s_and_b32 s93, s93, 4\n\t" \
  "s_lshl_b32 s93, s93, 11\n\t" \
  "s_lshl2_add_u32 %[so], %[sh], s93\n\t" \
  "s_waitcnt vmcnt(6)\n\t" \
  "v_mfma_f32_16x16x32_bf16 v[132:135], v[68:71], v[212:215], v[132:135]\n\t" \
  "v_mfma_f32_16x16x32_bf16 v[140:143], v[76:79], v[212:215], v[140:143]\n\t" \
  "s_sub_i32 s93, %[step], 3\n\t" \
  "s_lshl_b32 %[st], s93, 19\n\t" \
  "s_lshl_b32 s94, s93, 11\n\t" \
  "s_xor_b32 s94, s94, %[s0]\n\t" \
  "s_and_b32 s94, s94, 0x2000\n\t" \
  "s_or_b32 %[st], %[st], s94\n\t" \
  "s_cmp_lt_i32 s93, 0\n\t" \
  "s_cselect_b32 %[st], 0x70000000, %[st]\n\t" \
  "s_add_u32 %[st4], %[st], 4096\n\t" \
  "s_waitcnt vmcnt(5)\n\t" \
  "v_mfma_f32_16x16x32_bf16 v[128:131], v[80:83], v[216:219], v[128:131]\n\t" \
  "v_mfma_f32_16x16x32_bf16 v[136:139], v[88:91], v[216:219], v[136:139]\n\t" \
  "s_waitcnt vmcnt(4)\n\t" \
  "v_mfma_f32_16x16x32_bf16 v[132:135], v[84:87], v[220:223], v[132:135]\n\t" \
  "v_mfma_f32_16x16x32_bf16 v[140:143], v[92:95], v[220:223], v[140:143]\n\t" \
  "s_cmp_eq_u32 %[step], 63\n\t" \
  "s_cbranch_scc1 .Lgol%=\n\t" \
  "s_movk_i32 s93, 600\n\t" \
  ".Lpacel%=:\n\t" \
  "s_memrealtime s[90:91]\n\t" \
  "s_waitcnt lgkmcnt(0)\n\t" \
  "s_lshl_b32 s90, s90, 2\n\t" \
  "s_sub_i32 s91, %[E], s90\n\t" \
  "s_cmp_le_i32 s91, 0\n\t" \
  "s_cbranch_scc1 .Lgol%=\n\t" \
  "s_sub_u32 s93, s93, 1\n\t" \
  "s_cmp_lg_u32 s93, 0\n\t" \
  "s_cbranch_scc1 .Lpacel%=\n\t" \
  ".Lgol%=:\n\t" \
  "buffer_load_dwordx2 v[166:167], v173, %[rs], 0 offen sc1\n\t" \
  "s_waitcnt vmcnt(4)\n\t" \
  "v_mfma_f32_16x16x32_bf16 v[128:131], v[96:99], v[176:179], v[128:131]\n\t" \
  "v_mfma_f32_16x16x32_bf16 v[136:139], v[104:107], v[176:179], v[136:139]\n\t" \
  "s_waitcnt vmcnt(3)\n\t" \
  "v_mfma_f32_16x16x32_bf16 v[132:135], v[100:103], v[180:183], v[132:135]\n\t" \
  "v_mfma_f32_16x16x32_bf16 v[140:143], v[108:111], v[180:183], v[140:143]\n\t" \
  "s_waitcnt vmcnt(2)\n\t" \
  "v_mfma_f32_16x16x32_bf16 v[128:131], v[112:115], v[184:187], v[128:131]\n\t" \
  "v_mfma_f32_16x16x32_bf16 v[136:139], v[120:123], v[184:187], v[136:139]\n\t" \
  "s_waitcnt vmcnt(1)\n\t" \
  "v_mfma_f32_16x16x32_bf16 v[132:135], v[116:119], v[188:191], v[132:135]\n\t" \
  "v_mfma_f32_16x16x32_bf16 v[140:143], v[124:127], v[188:191], v[140:143]\n\t" \
  "v_mov_b64 v[144:145], 0\n\t" \
  "v_mov_b64 v[146:147], 0\n\t" \
  "v_mov_b64 v[148:149], 0\n\t" \
  "v_mov_b64 v[150:151], 0\n\t" \
  "v_mov_b64 v[152:153], 0\n\t" \
  "v_mov_b64 v[154:155], 0\n\t" \
  "v_mov_b64 v[156:157], 0\n\t" \
  "v_mov_b64 v[158:159], 0\n\t" \
  "v_pk_add_f32 v[128:129], v[128:129], v[132:133]\n\t" \
  "v_pk_add_f32 v[130:131], v[130:131], v[134:135]\n\t" \
  "ds_write_b128 v230, v[128:131] offset:0\n\t" \
  "v_pk_add_f32 v[136:137], v[136:137], v[140:141]\n\t" \
  "v_pk_add_f32 v[138:139], v[138:139], v[142:143]\n\t" \
  "ds_write_b128 v230, v[136:139] offset:1024\n\t" \
  "v_xor_b32 v230, 0x4000, v230\n\t" \
  "s_cmp_lg_u32 %[step], 63\n\t" \
  "s_cbranch_scc1 .Lstep%=\n\t" \
  "buffer_load_dwordx4 v[176:179], v238, %[rt], %[st] offen offset:0 sc1\n\t" \
  "buffer_load_dwordx4 v[180:183], v238, %[rt], %[st] offen offset:1024 sc1\n\t" \
  "buffer_load_dwordx4 v[184:187], v238, %[rt], %[st4] offen offset:0 sc1\n\t" \
  "buffer_load_dwordx4 v[188:191], v238, %[rt], %[st4] offen offset:1024 sc1\n\t" \
  "buffer_load_dwordx4 v[192:195], v239, %[rt], %[st] offen offset:0 sc1\n\t" \
  "buffer_load_dwordx4 v[196:199], v239, %[rt], %[st] offen offset:1024 sc1\n\t" \
  "buffer_load_dwordx4 v[200:203], v239, %[rt], %[st4] offen offset:0 sc1\n\t" \
  "buffer_load_dwordx4 v[204:207], v239, %[rt], %[st4] offen offset:1024 sc1\n\t" \
  "v_subrev_u32 v230, 98304, v230\n\t" \
  ".Lt%=:\n\t" \
  "s_waitcnt vmcnt(7)\n\t" \
  "v_mfma_f32_16x16x32_bf16 v[128:131], v[0:3], v[176:179], 0\n\t" \
  "v_mfma_f32_16x16x32_bf16 v[136:139], v[8:11], v[176:179], 0\n\t" \
  "buffer_load_dwordx4 v[176:179], v240, %[rt], %[st] offen offset:0 sc1\n\t" \
  "buffer_load_dword v166, v242, %[rs], 0 offen sc1\n\t" \
  "s_waitcnt vmcnt(8)\n\t" \
  "v_mfma_f32_16x16x32_bf16 v[132:135], v[4:7], v[180:183], 0\n\t" \
  "v_mfma_f32_16x16x32_bf16 v[140:143], v[12:15], v[180:183], 0\n\t" \
  "buffer_load_dwordx4 v[180:183], v240, %[rt], %[st] offen offset:1024 sc1\n\t" \
  "s_waitcnt vmcnt(8)\n\t" \
  "v_mfma_f32_16x16x32_bf16 v[128:131], v[16:19], v[184:187], v[128:131]\n\t" \
  "v_mfma_f32_16x16x32_bf16 v[136:139], v[24:27], v[184:187], v[136:139]\n\t" \
  "buffer_load_dwordx4 v[184:187], v240, %[rt], %[st4] offen offset:0 sc1\n\t" \
  "s_waitcnt vmcnt(8)\n\t" \
  "v_mfma_f32_16x16x32_bf16 v[132:135], v[20:23], v[188:191], v[132:135]\n\t" \
  "v_mfma_f32_16x16x32_bf16 v[140:143], v[28:31], v[188:191], v[140:143]\n\t" \
  "buffer_load_dwordx4 v[188:191], v240, %[rt], %[st4] offen offset:1024 sc1\n\t" \
  "s_waitcnt vmcnt(8)\n\t" \
  "v_mfma_f32_16x16x32_bf16 v[128:131], v[32:35], v[192:195], v[128:131]\n\t" \
  "v_mfma_f32_16x16x32_bf16 v[136:139], v[40:43], v[192:195], v[136:139]\n\t" \
  "buffer_load_dwordx4 v[192:195], v241, %[rt], %[st] offen offset:0 sc1\n\t" \
  "s_waitcnt vmcnt(8)\n\t" \
  "v_mfma_f32_16x16x32_bf16 v[132:135], v[36:39], v[196:199], v[132:135]\n\t" \
  "v_mfma_f32_16x16x32_bf16 v[140:143], v[44:47], v[196:199], v[140:143]\n\t" \
  "buffer_load_dwordx4 v[196:199], v241, %[rt], %[st] offen offset:1024 sc1\n\t" \
  "s_waitcnt vmcnt(8)\n\t" \
  "v_mfma_f32_16x16x32_bf16 v[128:131], v[48:51], v[200:203], v[128:131]\n\t" \
  "v_mfma_f32_16x16x32_bf16 v[136:139], v[56:59], v[200:203], v[136:139]\n\t" \
  "buffer_load_dwordx4 v[200:203], v241, %[rt], %[st4] offen offset:0 sc1\n\t" \
  "s_waitcnt vmcnt(8)\n\t" \
  "v_mfma_f32_16x16x32_bf16 v[132:135], v[52:55], v[204:207], v[132:135]\n\t" \
  "v_mfma_f32_16x16x32_bf16 v[140:143], v[60:63], v[204:207], v[140:143]\n\t" \
  "buffer_load_dwordx4 v[204:207], v241, %[rt], %[st4] offen offset:1024 sc1\n\t" \
  "s_waitcnt vmcnt(8)\n\t" \
  "v_mfma_f32_16x16x32_bf16 v[128:131], v[64:67], v[176:179], v[128:131]\n\t" \
  "v_mfma_f32_16x16x32_bf16 v[136:139], v[72:75], v[176:179], v[136:139]\n\t" \
  "s_waitcnt vmcnt(7)\n\t" \
  ".Ltc%=:\n\t" \
  "v_cmp_eq_u32 vcc, -1, v166\n\t" \
  "s_cbranch_vccnz .Ltw%=\n\t" \
  ".Lto%=:\n\t" \
  "s_cmp_eq_u32 %[step], 66\n\t" \
  "s_cbranch_scc1 .Ltl%=\n\t" \
  "s_add_u32 %[step], %[step], 1\n\t" \
  "s_sub_i32 s93, %[step], 3\n\t" \
  "s_lshl_b32 %[st], s93, 19\n\t" \
  "s_lshl_b32 s94, s93, 11\n\t" \
  "s_xor_b32 s94, s94, %[s0]\n\t" \
  "s_and_b32 s94, s94, 0x2000\n\t" \
  "s_or_b32 %[st], %[st], s94\n\t" \
  "s_cmp_lt_i32 s93, 0\n\t" \
  "s_cselect_b32 %[st], 0x70000000, %[st]\n\t" \
  "s_add_u32 %[st4], %[st], 4096\n\t" \
  "v_add_u32 v242, 16384, v242\n\t" \
  "s_cmp_eq_u32 %[step], 66\n\t" \
  "s_cbranch_scc0 .Ltn%=\n\t" \
  "v_mov_b32 v242, %[fg]\n\t" \
  ".Ltn%=:\n\t" \
  "buffer_load_dwordx4 v[176:179], v238, %[rt], %[st] offen offset:0 sc1\n\t" \
  "s_waitcnt vmcnt(7)\n\t" \
  "v_mfma_f32_16x16x32_bf16 v[132:135], v[68:71], v[180:183], v[132:135]\n\t" \
  "v_mfma_f32_16x16x32_bf16 v[140:143], v[76:79], v[180:183], v[140:143]\n\t" \
  "buffer_load_dwordx4 v[180:183], v238, %[rt], %[st] offen offset:1024 sc1\n\t" \
  "s_waitcnt vmcnt(7)\n\t" \
  "v_mfma_f32_16x16x32_bf16 v[128:131], v[80:83], v[184:187], v[128:131]\n\t" \
  "v_mfma_f32_16x16x32_bf16 v[136:139], v[88:91], v[184:187], v[136:139]\n\t" \
  "buffer_load_dwordx4 v[184:187], v238, %[rt], %[st4] offen offset:0 sc1\n\t" \
  "s_waitcnt vmcnt(7)\n\t" \
  "v_mfma_f32_16x16x32_bf16 v[132:135], v[84:87], v[188:191], v[132:135]\n\t" \
  "v_mfma_f32_16x16x32_bf16 v[140:143], v[92:95], v[188:191], v[140:143]\n\t" \
  "buffer_load_dwordx4 v[188:191], v238, %[rt], %[st4] offen offset:1024 sc1\n\t" \
  "s_waitcnt vmcnt(7)\n\t" \
  "v_mfma_f32_16x16x32_bf16 v[128:131], v[96:99], v[192:195], v[128:131]\n\t" \
  "v_mfma_f32_16x16x32_bf16 v[136:139], v[104:107], v[192:195], v[136:139]\n\t" \
  "buffer_load_dwordx4 v[192:195], v239, %[rt], %[st] offen offset:0 sc1\n\t" \
  "s_waitcnt vmcnt(7)\n\t" \
  "v_mfma_f32_16x16x32_bf16 v[132:135], v[100:103], v[196:199], v[132:135]\n\t" \
  "v_mfma_f32_16x16x32_bf16 v[140:143], v[108:111], v[196:199], v[140:143]\n\t" \
  "buffer_load_dwordx4 v[196:199], v239, %[rt], %[st] offen offset:1024 sc1\n\t" \
  "s_waitcnt vmcnt(7)\n\t" \
  "v_mfma_f32_16x16x32_bf16 v[128:131], v[112:115], v[200:203], v[128:131]\n\t" \
  "v_mfma_f32_16x16x32_bf16 v[136:139], v[120:123], v[200:203], v[136:139]\n\t" \
  "buffer_load_dwordx4 v[200:203], v239, %[rt], %[st4] offen offset:0 sc1\n\t" \
  "s_waitcnt vmcnt(7)\n\t" \
  "v_mfma_f32_16x16x32_bf16 v[132:135], v[116:119], v[204:207], v[132:135]\n\t" \
  "v_mfma_f32_16x16x32_bf16 v[140:143], v[124:127], v[204:207], v[140:143]\n\t" \
  "buffer_load_dwordx4 v[204:207], v239, %[rt], %[st4] offen offset:1024 sc1\n\t" \
  "s_nop 5\n\t" \
  "v_pk_add_f32 v[128:129], v[128:129], v[132:133]\n\t" \
  "v_pk_add_f32 v[130:131], v[130:131], v[134:135]\n\t" \
  "ds_write_b128 v230, v[128:131] offset:0\n\t" \
  "v_pk_add_f32 v[136:137], v[136:137], v[140:141]\n\t" \
  "v_pk_add_f32 v[138:139], v[138:139], v[142:143]\n\t" \
  "ds_write_b128 v230, v[136:139] offset:1024\n\t" \
  "v_add_u32 v230, 16384, v230\n\t" \
  "s_branch .Lt%=\n\t" \
  ".Ltl%=:\n\t" \
  "buffer_load_dwordx4 v[224:227], v243, %[rg], 0 offen sc1\n\t" \
  "s_waitcnt vmcnt(7)\n\t" \
  "v_mfma_f32_16x16x32_bf16 v[132:135], v[68:71], v[180:183], v[132:135]\n\t" \
  "v_mfma_f32_16x16x32_bf16 v[140:143], v[76:79], v[180:183], v[140:143]\n\t" \
  "s_waitcnt vmcnt(6)\n\t" \
  "v_mfma_f32_16x16x32_bf16 v[128:131], v[80:83], v[184:187], v[128:131]\n\t" \
  "v_mfma_f32_16x16x32_bf16 v[136:139], v[88:91], v[184:187], v[136:139]\n\t" \
  "s_waitcnt vmcnt(5)\n\t" \
  "v_mfma_f32_16x16x32_bf16 v[132:135], v[84:87], v[188:191], v[132:135]\n\t" \
  "v_mfma_f32_16x16x32_bf16 v[140:143], v[92:95], v[188:191], v[140:143]\n\t" \
  "s_waitcnt vmcnt(4)\n\t" \
  "v_mfma_f32_16x16x32_bf16 v[128:131], v[96:99], v[192:195], v[128:131]\n\t" \
  "v_mfma_f32_16x16x32_bf16 v[136:139], v[104:107], v[192:195], v[136:139]\n\t" \
  "s_waitcnt vmcnt(3)\n\t" \
  "v_mfma_f32_16x16x32_bf16 v[132:135], v[100:103], v[196:199], v[132:135]\n\t" \
  "v_mfma_f32_16x16x32_bf16 v[140:143], v[108:111], v[196:199], v[140:143]\n\t" \
  "s_waitcnt vmcnt(2)\n\t" \
  "v_mfma_f32_16x16x32_bf16 v[128:131], v[112:115], v[200:203], v[128:131]\n\t" \
  "v_mfma_f32_16x16x32_bf16 v[136:139], v[120:123], v[200:203], v[136:139]\n\t" \
  "s_waitcnt vmcnt(1)\n\t" \
  "v_mfma_f32_16x16x32_bf16 v[132:135], v[116:119], v[204:207], v[132:135]\n\t" \
  "v_mfma_f32_16x16x32_bf16 v[140:143], v[124:127], v[204:207], v[140:143]\n\t" \
  "s_nop 6\n\t" \
  "v_pk_add_f32 v[128:129], v[128:129], v[132:133]\n\t" \
  "v_pk_add_f32 v[130:131], v[130:131], v[134:135]\n\t" \
  "ds_write_b128 v230, v[128:131] offset:0\n\t" \
  "v_pk_add_f32 v[136:137], v[136:137], v[140:141]\n\t" \
  "v_pk_add_f32 v[138:139], v[138:139], v[142:143]\n\t" \
  "ds_write_b128 v230, v[136:139] offset:1024\n\t" \
  "s_waitcnt vmcnt(0) lgkmcnt(0)\n\t" \
  "s_branch .Lend%=\n\t" \
  ".Ltw%=:\n\t" \
  "s_add_u32 s92, s92, 1\n\t" \
  "s_sleep 2\n\t" \
  "s_and_b32 s93, s92, 63\n\t" \
  "s_cbranch_scc1 .Ltr%=\n\t" \
  "buffer_load_dword v164, off, %[rs], %[sab] sc1\n\t" \
  "s_waitcnt vmcnt(0)\n\t" \
  "v_readfirstlane_b32 s93, v164\n\t" \
  "s_nop 3\n\t" \
  "s_cmp_eq_u32 s93, 1\n\t" \
  "s_cbranch_scc1 .Lto%=\n\t" \
  "s_cmp_lt_u32 s92, 0x80000\n\t" \
  "s_cbranch_scc1 .Ltr%=\n\t" \
  "v_mov_b32 v164, 1\n\t" \
  "buffer_store_dword v164, off, %[rs], %[sab] sc1\n\t" \
  "s_waitcnt vmcnt(0)\n\t" \
  "s_branch .Lto%=\n\t" \
  ".Ltr%=:\n\t" \
  "buffer_load_dword v166, v242, %[rs], 0 offen sc1\n\t" \
  "s_waitcnt vmcnt(0)\n\t" \
  "s_branch .Ltc%=\n\t" \
  ".Lb1%=:\n\t" \
  "s_waitcnt vmcnt(0) lgkmcnt(0)\n\t" \
  "s_barrier\n\t" \
  "s_branch .Lb2%=\n\t" \
  ".Lbl%=:\n\t" \
  "s_bitset1_b64 %[ps], %[step]\n\t" \
  "s_cmp_lt_u32 s92, 3\n\t" \
  "s_cbranch_scc1 .Lbm%=\n\t" \
  "s_sleep 12\n\t" \
  ".Lbm%=:\n\t" \
  "s_add_u32 s92, s92, 1\n\t" \
  "s_sleep 2\n\t" \
  "s_and_b32 s93, s92, 63\n\t" \
  "s_cbranch_scc1 .Lba%=\n\t" \
  "buffer_load_dword v164, off, %[rs], %[sab] sc1\n\t" \
  "s_waitcnt vmcnt(0)\n\t" \
  "v_readfirstlane_b32 s93, v164\n\t" \
  "s_nop 3\n\t" \
  "s_cmp_eq_u32 s93, 1\n\t" \
  "s_cbranch_scc1 .Lgo%=\n\t" \
  "s_cmp_lt_u32 s92, 0x80000\n\t" \
  "s_cbranch_scc1 .Lba%=\n\t" \
  "v_mov_b32 v164, 1\n\t" \
  "buffer_store_dword v164, off, %[rs], %[sab] sc1\n\t" \
  "s_waitcnt vmcnt(0)\n\t" \
  "s_branch .Lgo%=\n\t" \
  ".Lba%=:\n\t" \
  "buffer_load_dwordx2 v[166:167], v173, %[rs], 0 offen sc1\n\t" \
  "s_branch .Lcal%=\n\t" \
  ".Lslow%=:\n\t" \
  "s_mov_b64 s[94:95], 0\n\t" \
  "v_cmp_eq_u32 vcc, %[sent], v128\n\t" \
  "s_or_b64 s[94:95], s[94:95], vcc\n\t" \
  "v_cmp_eq_u32 vcc, %[sent], v129\n\t" \
  "s_or_b64 s[94:95], s[94:95], vcc\n\t" \
  "v_cmp_eq_u32 vcc, %[sent], v130\n\t" \
  "s_or_b64 s[94:95], s[94:95], vcc\n\t" \
  "v_cmp_eq_u32 vcc, %[sent], v131\n\t" \
  "s_or_b64 s[94:95], s[94:95], vcc\n\t" \
  "v_cmp_eq_u32 vcc, %[sent], v132\n\t" \
  "s_or_b64 s[94:95], s[94:95], vcc\n\t" \
  "v_cmp_eq_u32 vcc, %[sent], v133\n\t" \
  "s_or_b64 s[94:95], s[94:95], vcc\n\t" \
  "v_cmp_eq_u32 vcc, %[sent], v134\n\t" \
  "s_or_b64 s[94:95], s[94:95], vcc\n\t" \
  "v_cmp_eq_u32 vcc, %[sent], v135\n\t" \
  "s_or_b64 s[94:95], s[94:95], vcc\n\t" \
  "v_cmp_eq_u32 vcc, %[sent], v136\n\t" \
  "s_or_b64 s[94:95], s[94:95], vcc\n\t" \
  "v_cmp_eq_u32 vcc, %[sent], v137\n\t" \
  "s_or_b64 s[94:95], s[94:95], vcc\n\t" \
  "v_cmp_eq_u32 vcc, %[sent], v138\n\t" \
  "s_or_b64 s[94:95], s[94:95], vcc\n\t" \
  "v_cmp_eq_u32 vcc, %[sent], v139\n\t" \
  "s_or_b64 s[94:95], s[94:95], vcc\n\t" \
  "v_cmp_eq_u32 vcc, %[sent], v140\n\t" \
  "s_or_b64 s[94:95], s[94:95], vcc\n\t" \
  "v_cmp_eq_u32 vcc, %[sent], v141\n\t" \
  "s_or_b64 s[94:95], s[94:95], vcc\n\t" \
  "v_cmp_eq_u32 vcc, %[sent], v142\n\t" \
  "s_or_b64 s[94:95], s[94:95], vcc\n\t" \
  "v_cmp_eq_u32 vcc, %[sent], v143\n\t" \
  "s_or_b64 s[94:95], s[94:95], vcc\n\t" \
  "s_cbranch_scc0 .Lok%=\n\t" \
  "s_bitset1_b64 %[fm], %[step]\n\t" \
  "s_add_u32 s92, s92, 1\n\t" \
  "s_sleep 1\n\t" \
  "s_and_b32 s93, s92, 63\n\t" \
  "s_cbranch_scc1 .Lre%=\n\t" \
  "buffer_load_dword v164, off, %[rs], %[sab] sc1\n\t" \
  "s_waitcnt vmcnt(0)\n\t" \
  "v_readfirstlane_b32 s93, v164\n\t" \
  "s_nop 3\n\t" \
  "s_cmp_eq_u32 s93, 1\n\t" \
  "s_cbranch_scc1 .Lok%=\n\t" \
  "s_cmp_lt_u32 s92, 0x80000\n\t" \
  "s_cbranch_scc1 .Lre%=\n\t" \
  "v_mov_b32 v164, 1\n\t" \
  "buffer_store_dword v164, off, %[rs], %[sab] sc1\n\t" \
  "s_waitcnt vmcnt(0)\n\t" \
  "s_branch .Lok%=\n\t" \
  ".Lre%=:\n\t" \
  "buffer_load_dwordx4 v[128:131], v234, %[rs], %[so] offen sc1\n\t" \
  "buffer_load_dwordx4 v[132:135], v235, %[rs], %[so] offen sc1\n\t" \
  "buffer_load_dwordx4 v[136:139], v236, %[rs], %[so] offen sc1\n\t" \
  "buffer_load_dwordx4 v[140:143], v237, %[rs], %[so] offen sc1\n\t" \
  "v_mov_b64 v[144:145], 0\n\t" \
  "v_mov_b64 v[146:147], 0\n\t" \
  "v_mov_b64 v[148:149], 0\n\t" \
  "v_mov_b64 v[150:151], 0\n\t" \
  "v_mov_b64 v[152:153], 0\n\t" \
  "v_mov_b64 v[154:155], 0\n\t" \
  "v_mov_b64 v[156:157], 0\n\t" \
  "v_mov_b64 v[158:159], 0\n\t" \
  "s_waitcnt vmcnt(0)\n\t" \
  "s_branch .Lw%=\n\t" \
  ".Lend%=:\n\t"

typedef __attribute__((ext_vector_type(32))) unsigned u32x32;
__device__ __forceinline__ void put4(u32x32& W, int i, bf16x8 f) {
  const u32x4 v = __builtin_bit_cast(u32x4, f);
#pragma unroll
  for (int j = 0; j < 4; ++j) W[4 * i + j] = v[j];
}
__device__ __forceinline__ bf16x8 get4(const u32x32& W, int i) {
  return as_bf16x8((u32x4){W[4 * i], W[4 * i + 1], W[4 * i + 2], W[4 * i + 3]});
}
__device__ __forceinline__ u32x4 raw_rsrc(const void* p, unsigned bytes) {
  const unsigned long long a = (unsigned long long)p;
  return (u32x4){(unsigned)a, (unsigned)(a >> 32) & 0xffffu, bytes, 0x00020000u};
}
__device__ __forceinline__ unsigned get_target(const char* lds, int s) {
  if (s == 0) return now_q();
  lds_vu_ptr tg = lds_at(lds);
#pragma unroll 1
  for (int i = 0; i < 8192; ++i) {
    if (__builtin_amdgcn_readfirstlane(tg[0]) == (unsigned)s) break;
    __builtin_amdgcn_s_sleep(1);
  }
  const unsigned t = __builtin_amdgcn_readfirstlane(tg[1]);
#pragma unroll 1
  for (int i = 0; i < 256 && (int)(t - now_q()) > 128; ++i) __builtin_amdgcn_s_sleep(2);
  return t;
}
#define CLB8(a, b, c, d, e, f, g, h) "v" #a, "v" #b, "v" #c, "v" #d, "v" #e, "v" #f, "v" #g, "v" #h
#define CLB_S "s90", "s91", "s92", "s93", "s94", "s95", "vcc", "scc", "memory"
#define W_IN "{v[0:31]}"(W[0]), "{v[32:63]}"(W[1]), "{v[64:95]}"(W[2]), "{v[96:127]}"(W[3]), "{v[128:159]}"(W[4]), "{v[160:191]}"(W[5])

#define W_DELAY 60
#define J0 0
constexpr int L_MEM = 0, L_KT = 16384, L_QX = 24576, L_KX = 26624, L_VX = 16384, L_MISC = 30720, L_WL = 32768;

#define QK_STEP(TXT)                                                                                                        \
  asm volatile(TXT                                                                                                          \
      : [tout] "=&s"(tout), [spins] "=&s"(spins), [tb1] "=&s"(tb1), "+{v248}"(aL16), "+{v249}"(aMW), "+{v250}"(aWL),                            \
        "+{v251}"(aIDX), "+{v252}"(aQW), "+{v[232:235]}"(QF)                                                                 \
      : W_IN, [rs] "s"(rs4), [som] "s"(o_mem(pm, s) + (WV >> 2) * 16384 + (WV & 3) * 2048), [tgt] "s"(tgt), [sent] "s"(DSENT), [sab] "s"(SY_ABORT), \
        [lead] "s"(lead), [step] "s"(s), [sbc] "s"(SY_BA + side(pm, 1) + h * 128), [nh] "s"(0xFF00FF00FF00FF00ull), [up] "s"(0xFFFFFFFF00000000ull), [negc] "s"(-cexp),      \
        [c] "s"(cexp), [so] "s"(o_o(pm, s, h) + WV * 512), [sk] "s"(o_v(pm, s, h) + 2048 + (WV & 3) * 512),                    \
        [sv] "s"(o_v(pm, s, h) + WV * 512), [rt] "s"(rt4), [svt] "s"(t_off(p, T_V + kseg(s, h)) + WV * 2048)                  \
      : CLB8(192, 193, 194, 195, 196, 197, 198, 199), CLB8(200, 201, 202, 203, 204, 205, 206, 207),                          \
        CLB8(208, 209, 210, 211, 212, 213, 214, 215), CLB8(216, 217, 218, 219, 220, 221, 222, 223),                          \
        CLB8(224, 225, 226, 227, 228, 229, 230, 231), CLB8(236, 237, 238, 239, 240, 241, 242, 243), "v244", "v245", "v246", "v247", "v253", CLB_S)

template <int WV>
__device__ __forceinline__ void qk_wave(Args& p, int h, char* smem) {
  const int tid = threadIdx.x;
  unsigned pm;
  u32x32 W[6];
  {
    int lane = tid & 63;
    asm volatile("" : "+v"(lane));
    const unsigned vo = (WV * 4 + (lane >> 4)) * (HD * 2) + 16 * ((lane & 15) ^ swzB((WV * 4 + (lane >> 4)) & 15));
    auto stage = [&](int j) {
#pragma unroll
      for (int mm = 0; mm < 2; ++mm) {
        const char* src = (const char*)((mm ? p.Wk : p.Wq) + (size_t)((j + J0) & 7) * 128 * HD + h * DH);
        const unsigned dst = lds_off(smem) + L_WL + (j & 1) * 65536 + mm * 32768 + WV * 1024;
        glds2(src, 32 * HD * 2, vo, dst);
        glds2(src + 64 * HD * 2, 32 * HD * 2, vo, dst + 16384);
      }
    };
    bf16x8 keep[4][4];
    stage(0); stage(1);
    pm = side_mask<true>(p.ws); p.s0 = pm >> 31;
#pragma unroll
    for (int j = 0; j < 8; ++j) {
      if (j < 7) WAIT_V(8); else WAIT_V(0);
      LBAR();
#pragma unroll
      for (int k = 0; k < 8; ++k) {
        const int sl = (2 * (j + J0) + (k >> 2) - 2 * WV) & 15;
        const bf16x8 fr = ld_bfrag(smem + L_WL + (j & 1) * 65536 + (WV >> 2) * 32768 + (k >> 2) * 16384, k & 1, 2 * (WV & 3) + ((k >> 1) & 1), lane);
        const int f = ((sl >> 2) * 3 + (sl & 3)) * 4 + (k & 3);
        if ((sl & 3) != 3) put4(W[f >> 3], f & 7, fr);
        else if (j < 2) *reinterpret_cast<bf16x8*>(smem + WV * 4096 + (k & 3) * 1024 + lane * 16) = fr;
        else keep[sl >> 2][k & 3] = fr;
      }
      LBAR();
      if (j + 2 < 8) stage(j + 2);
    }
    constexpr int PK = (((1 - 2 * WV) & 3) == 3 ? (2 * J0 + 1 - 2 * WV) & 15 : (2 * J0 + 3 - 2 * WV) & 15) >> 2;
#pragma unroll
    for (int u = 0; u < 2; ++u) {
#pragma unroll
      for (int i = 0; i < 16; ++i) {
        if (((i >> 2) == PK) != (u == 1)) continue;
        *reinterpret_cast<bf16x8*>(smem + L_WL + WV * 16384 + (i >> 2) * 4096 + (i & 1) * 2048 + ((i >> 1) & 1) * 1024 + lane * 16) =
            u ? ldsf(smem + WV * 4096 + (i & 3) * 1024 + lane * 16) : keep[i >> 2][i & 3];
      }
      SCHED();
    }
    LBAR();
    RDY(h);
    if (WV == 4 || WV == 5) {
      wait_flags(mkrsrc(p.ws + WS_SY, SY_TOTAL), fkq(0, h, (lane & 1) * 2), (gu32*)(p.ws + WS_SY + SY_ABORT));
      const char* src = p.ws + WS_T + t_off(p, kseg(0, h)) + (WV - 4) * 4096 + lane * 16;
#pragma unroll
      for (int i = 0; i < 4; ++i) glds16_sc1(src + i * 1024, smem + L_KT + (WV - 4) * 4096 + i * 1024);
    }
    if (tid < 8) reinterpret_cast<unsigned*>(smem + L_MISC)[tid] = 0;
  }
  const __amdgpu_buffer_rsrc_t rsy = mkrsrc(p.ws + WS_SY, SY_TOTAL);
  const u32x4 rs4 = raw_rsrc(p.ws + WS_SY, SY_TOTAL);
  const u32x4 rt4 = raw_rsrc(p.ws + WS_T, T_BYTES);
  gu32* abortw = (gu32*)(p.ws + WS_SY + SY_ABORT);
  const float cexp = 0.08838834764831845f * 1.4426950408889634f;
  constexpr int m = WV >> 2, b = WV & 3;
  bool fok = false;
  int lead = 380;
  unsigned long long fm = 0, ps = 0; (void)fm; (void)ps;
  for (int s = 0; s < NSEG; ++s) {
    int lane = tid & 63;
    asm volatile("" : "+v"(lane));
    const int r = lane & 15, q = lane >> 4;
    unsigned aL16 = lane * 16, aMW = aL16 + WV * 2048, aWL = aL16 + L_WL + WV * 16384;
    unsigned aIDX = (lane & 2) ? 0xEEEE : 0x4444;
    unsigned aQW = (32 * b + r) * 16 + q * 4 + (m ? L_KX : L_QX);
    u32x4 QF;
    asm volatile("" : "={v[232:235]}"(QF));
    if (WV < 4) {
      QF[0] = L_QX + (q * 8 + (r >> 2)) * 16 + (r & 1) * 8;
      QF[1] = ((2 * (q & 1) + (q >> 1)) * 16 + r) * 4;
      QF[2] = ((r >> 2) & 1) * 65536 + ((r >> 1) & 1) * 64 + (r >> 3) * 32 + (r & 1) * 16 + q * 4;
    } else QF[3] = r * 16 + q * 4;
    const unsigned tgt = get_target(smem + L_MISC, s);
    unsigned tout, spins, tb1;
    if (WV == 0) QK_STEP(QK_ASM_0); else if (WV == 1) QK_STEP(QK_ASM_1); else if (WV == 2) QK_STEP(QK_ASM_2);
    else if (WV == 3) QK_STEP(QK_ASM_3); else if (WV == 4) QK_STEP(QK_ASM_4); else if (WV == 5) QK_STEP(QK_ASM_5);
    else if (WV == 6) QK_STEP(QK_ASM_6); else QK_STEP(QK_ASM_7);
    tout = __builtin_amdgcn_readfirstlane(tout);
    lane = tid & 63;
    asm volatile("" : "+v"(lane));
    if (WV < 4) {
      lds_at(smem + L_MISC + 8)[WV] = s + 1;
      if (WV == 3) {
        const int x = (int)(time_of(tout) - __builtin_amdgcn_readfirstlane(tb1));
        lead = __builtin_amdgcn_readfirstlane(track(lead, x, LEAD_UP, s < 4 ? 40 : LEAD_DN, 60));
        SMP(2, s, x); SMP(3, s, lead);
      }
    } else if (WV <= 5) {
      if (s + 2 < NSEG) {
        if ((s & 3) == 3 && !fok) wait_flags(rsy, fkq((s + 1) >> 2, h, (lane & 1) * 2), abortw);
        if ((s & 3) == 2) fok = __all(ld_flag(rsy, fkq(((s + 2) >> 2) & 15, h, (lane & 1) * 2)) != SENT);
      }
    }
    if (WV >= 4 && WV <= 6 && s + 1 < NSEG) {
      unsigned t;
      do {
        __builtin_amdgcn_s_sleep(1);
        t = lds_at(smem + L_MISC + 8)[lane & 3];
      } while (!__all(t == (unsigned)(s + 1)));
      if (WV <= 5) {
        if (s + 2 < NSEG) {
          const char* src = p.ws + WS_T + t_off(p, kseg(s + 1, h)) + (WV - 4) * 4096 + lane * 16;
#pragma unroll
          for (int i = 0; i < 4; ++i) glds16_sc1(src + i * 1024, smem + L_KT + (WV - 4) * 4096 + i * 1024);
        }
      } else {
        watch(rsy, s, lane, now_q() + W_DELAY, (const unsigned*)(smem + L_MISC), abortw, side(pm, 1));
      }
    }
  }
}

__device__ __forceinline__ void qk_role(Args& p, int h, char* smem) {
  switch (__builtin_amdgcn_readfirstlane(threadIdx.x >> 6)) {
    case 0: qk_wave<0>(p, h, smem); break;
    case 1: qk_wave<1>(p, h, smem); break;
    case 2: qk_wave<2>(p, h, smem); break;
    case 3: qk_wave<3>(p, h, smem); break;
    case 4: qk_wave<4>(p, h, smem); break;
    case 5: qk_wave<5>(p, h, smem); break;
    case 6: qk_wave<6>(p, h, smem); break;
    default: qk_wave<7>(p, h, smem); break;
  }
}

#define CLB32(a) CLB32_##a
#define CLB32_128 CLB8(128, 129, 130, 131, 132, 133, 134, 135), CLB8(136, 137, 138, 139, 140, 141, 142, 143), CLB8(144, 145, 146, 147, 148, 149, 150, 151), CLB8(152, 153, 154, 155, 156, 157, 158, 159)
#define CLB32_160 CLB8(160, 161, 162, 163, 164, 165, 166, 167), CLB8(168, 169, 170, 171, 172, 173, 174, 175), CLB8(176, 177, 178, 179, 180, 181, 182, 183), CLB8(184, 185, 186, 187, 188, 189, 190, 191)
#define W4_IN "{v[0:31]}"(W[0]), "{v[32:63]}"(W[1]), "{v[64:95]}"(W[2]), "{v[96:127]}"(W[3])
#define LEAD_V 200

constexpr int V_VM = 16384, V_T = 32768, V_KI = 131072;
__device__ __forceinline__ void tok_rows(const Args& p, __amdgpu_buffer_rsrc_t rsy, unsigned pm, int sg, int h, int T, int eh, char* smem, char* img,
                                         int lane, gu32* abortw, unsigned tk) {
  const float scale = 0.08838834764831845f, LOG2E = 1.4426950408889634f;
  const __amdgpu_buffer_rsrc_t rt = mkrsrc(p.ws + WS_T, T_BYTES);
  asm volatile("" : "+v"(lane));
  const int r = lane & 15, q = lane >> 4;
  u32x4 km[2];
  if (tk) pace_until(tk);
#pragma unroll
  for (int i = 0; i < 2; ++i) km[i] = __builtin_amdgcn_raw_buffer_load_b128(rsy, lane * 16 + i * 1024, o_v(pm, sg, h) + 2048, 16);
  SCHED();
  const char* tl = smem + V_T + (sg & 3) * 24576 + lane * 16;
  bf16x8 qt[4], vt[4];
  unsigned vm[4];
  f32x4 d0 = {0.f, 0.f, 0.f, 0.f}, d1 = d0, d2 = d0;
#pragma unroll
  for (int i = 0; i < 4; ++i) qt[i] = ldsf(tl + 8192 + T * 4096 + i * 1024);
  {
    bf16x8 kf[8];
#pragma unroll
    for (int i = 0; i < 8; ++i) kf[i] = ldsf(tl + i * 1024);
    SCHED();
#pragma unroll
    for (int bb = 0; bb < 4; ++bb) { d0 = MFMA(kf[bb], qt[bb], d0); d1 = MFMA(kf[4 + bb], qt[bb], d1); }
  }
#pragma unroll
  for (int i = 0; i < 4; ++i) {
    vt[i] = ldsf(tl + 16384 + eh * 4096 + i * 1024);
    vm[i] = *reinterpret_cast<const unsigned*>(smem + V_VM + (sg & 1) * 2048 + eh * 1024 + i * 256 + lane * 4);
  }
  SCHED();
  for (unsigned sp = 0; !__all(!has_ds(km[0]) && !has_ds(km[1]));) {
    __builtin_amdgcn_s_sleep(4);
    if (spin_fail(sp, abortw)) break;
    asm volatile("" ::: "memory");
#pragma unroll
    for (int i = 0; i < 2; ++i) km[i] = __builtin_amdgcn_raw_buffer_load_b128(rsy, lane * 16 + i * 1024, o_v(pm, sg, h) + 2048, 16);
  }
#pragma unroll
  for (int i = 0; i < 2; ++i)
#pragma unroll
    for (int j = 0; j < 4; ++j) *reinterpret_cast<unsigned*>(img + (i * 64 + lane) * 32 + j * 8) = km[i][j];
  asm volatile("s_waitcnt lgkmcnt(0)" ::: "memory");
  const char* lk = img + (q * 8 + (r >> 2)) * 32 + (r & 3) * 8;
  {
    bf16x8 kk[4];
#pragma unroll
    for (int bb = 0; bb < 4; ++bb) {
      s16x4 lo = __builtin_amdgcn_ds_read_tr16_b64_v4i16((lds_s16x4_ptr)(lk + bb * 1024));
      s16x4 hi = __builtin_amdgcn_ds_read_tr16_b64_v4i16((lds_s16x4_ptr)(lk + bb * 1024 + 128));
      kk[bb] = (bf16x8){lo[0], lo[1], lo[2], lo[3], hi[0], hi[1], hi[2], hi[3]};
    }
    SCHED();
#pragma unroll
    for (int bb = 0; bb < 4; ++bb) d2 = MFMA(kk[bb], qt[bb], d2);
  }
  const int qi = 16 * T + r;
  float v[10];
#pragma unroll
  for (int j = 0; j < 4; ++j) { v[j] = d0[j]; v[4 + j] = d1[j]; }
  v[8] = d2[0]; v[9] = d2[1];
#pragma unroll
  for (int j = 0; j < 5; ++j) {
    unsigned t = cvtpk(v[2 * j], v[2 * j + 1]);
    t = cvtpk(lo_f(t) * scale, hi_f(t) * scale);
    v[2 * j] = lo_f(t); v[2 * j + 1] = hi_f(t);
  }
#pragma unroll
  for (int j = 0; j < 8; ++j) {
    const int key = (j >> 2) * 16 + 4 * q + (j & 3);
    v[j] = key <= qi ? v[j] : -INFINITY;
  }
  float mx = v[0];
#pragma unroll
  for (int j = 1; j < 10; ++j) mx = fmax2(mx, v[j]);
  mx = rowq_max(mx) * LOG2E;
  float sm = 0.f;
#pragma unroll
  for (int j = 0; j < 10; ++j) { v[j] = __builtin_amdgcn_exp2f(__builtin_fmaf(v[j], LOG2E, -mx)); sm += v[j]; }
  const float inv = __builtin_amdgcn_rcpf(rowq_sum(sm));
  u32x4 o = {cvtpk(v[0] * inv, v[1] * inv), cvtpk(v[2] * inv, v[3] * inv), cvtpk(v[4] * inv, v[5] * inv),
             cvtpk(v[6] * inv, v[7] * inv)};
  const s16x4 pmem = __builtin_bit_cast(s16x4, (u32x2){cvtpk(v[8] * inv, v[9] * inv), 0u});
  const int ob = t_off_o(p, T_O + (sg * 2 + T) * 16 + (h >> 1)) + (h & 1) * 4096 + eh * 2048;
#pragma unroll
  for (int j = 0; j < 2; ++j) {
    f32x4 d[2];
#pragma unroll
    for (int u = 0; u < 2; ++u) {
      d[u] = MFMA(vt[2 * j + u], as_bf16x8(o), ((f32x4){0.f, 0.f, 0.f, 0.f}));
      d[u] += __builtin_amdgcn_mfma_f32_16x16x16bf16_1k(__builtin_bit_cast(s16x4, (u32x2){vm[2 * j + u], 0u}), pmem, (f32x4){0.f, 0.f, 0.f, 0.f}, 0, 0, 0);
    }
    st_sc1(rt, ob + lane * 16 + j * 1024, pack32(d[0], d[1]));
  }
}

__device__ __forceinline__ void vu_role(Args& p, int h, char* smem) {
  const int tid = threadIdx.x, lane = tid & 63;
  const int w = __builtin_amdgcn_readfirstlane(tid >> 6);
  u32x32 W[4];
  {
    const unsigned vo = (tid >> 4) * (HD * 2) + 16 * ((lane & 15) ^ swzB((tid >> 4) & 15));
    auto stage = [&](int j) {
      const char* src = (const char*)(p.Wv + (size_t)j * 128 * HD + h * DH);
      const unsigned dst = lds_off(smem) + L_WL + (j & 3) * 32768 + w * 1024;
      glds2(src, 32 * HD * 2, vo, dst);
      glds2(src + 64 * HD * 2, 32 * HD * 2, vo, dst + 16384);
    };
#pragma unroll
    for (int j = 0; j < 4; ++j) stage(j);
#pragma unroll
    for (int j = 0; j < 8; ++j) {
      if (j < 5) WAIT_V(12); else if (j == 5) WAIT_V(8); else if (j == 6) WAIT_V(4); else WAIT_V(0);
      LBAR();
#pragma unroll
      for (int k = 0; k < 4; ++k) put4(W[j >> 1], (j & 1) * 4 + k, ld_bfrag(smem + L_WL + (j & 3) * 32768 + (k >> 1) * 16384, k & 1, w, lane));
      LBAR();
      if (j + 4 < 8) stage(j + 4);
    }
    if (tid < 4) reinterpret_cast<unsigned*>(smem + L_MISC)[tid] = 0;
    RDY(32 + h);
  }
  const __amdgpu_buffer_rsrc_t rsy = mkrsrc(p.ws + WS_SY, SY_TOTAL);
  const u32x4 rs4 = raw_rsrc(p.ws + WS_SY, SY_TOTAL);
  gu32* abortw = (gu32*)(p.ws + WS_SY + SY_ABORT);
  const unsigned pm = side_mask<true>(p.ws);
  p.s0 = pm >> 31;
  unsigned long long fm = 0; (void)fm;
  unsigned fnext = SENT;
  const int fo = SY_FO + ((w & 3) * NH + h) * 128;
#pragma unroll 1
  for (int s = 0; s < NSEG; ++s) {
    const unsigned tgt = get_target(smem + L_MISC, s);
    unsigned spins;
    asm volatile(VV_ASM
        : [spins] "=&s"(spins)
        : W4_IN, "{v200}"(lane * 16), "{v201}"(lane * 16 + w * 2048), "{v202}"((lane & 2) ? 0xEEEE : 0x4444), "{v203}"(lane * 4),
          "{v204}"(tid * 4 + V_VM + (s & 1) * 2048), [rs] "s"(rs4), [som] "s"(o_mem(pm, s) + (w >> 2) * 16384 + (w & 3) * 2048), [tgt] "s"(tgt),
          [sent] "s"(DSENT), [sab] "s"(SY_ABORT), [sv] "s"(o_v(pm, s, h) + w * 256)
        : CLB32(128), CLB32(160), CLB8(192, 193, 194, 195, 196, 197, 198, 199), CLB_S);
    if (w < 4 ? s >= 1 : s == NSEG - 1) {
      if (s == NSEG - 1 && w < 4 && lane == 0) __builtin_amdgcn_raw_buffer_store_b32(1u, rsy, fo, 0, 16);
      tok_rows(p, rsy, pm, s - (w < 4), h, w & 1, (w >> 1) & 1, smem, smem + V_KI + w * 4096, lane, abortw, w < 4 ? 0 : now_q() + 160);
    } else if (w == 7) {
      SMP(6, s, (int)(now_q() - tgt));
      watch(rsy, s, lane, now_q() + LEAD_V + W_DELAY, (const unsigned*)(smem + L_MISC), abortw, side(pm, 1));
    } else if (w >= 4) {
      __builtin_amdgcn_s_sleep(2);
      const int s1 = w == 5 ? (s == NSEG - 2 ? s + 1 : s) : s + 1;
#pragma unroll 1
      for (int sg = (w == 5 || s == 0) ? s : s + 1; sg <= s1; ++sg) {
        if ((sg & 3) == 0 && fnext == SENT) wait_flags(rsy, fkq(sg >> 2, h, w - 4), abortw);
        const char* src = p.ws + WS_T + t_off(p, (w - 4) * T_Q + kseg(sg, h)) + lane * 16;
#pragma unroll
        for (int i = 0; i < 8; ++i) glds16_sc1(src + i * 1024, smem + V_T + (sg & 3) * 24576 + (w - 4) * 8192 + i * 1024);
        if ((sg & 3) == 3) fnext = __builtin_amdgcn_readfirstlane(ld_flag(rsy, fkq(((sg + 1) >> 2) & 15, h, w - 4)));
      }
      if (w == 5 && s + NPRE < NSEG) {
#pragma unroll
        for (int i = 0; i < 4; ++i) st_sc1(rsy, o_v(pm, s + NPRE, h) + i * 1024 + lane * 16, (u32x4){DSENT, DSENT, DSENT, DSENT});
      }
    }
  }
  WAIT_V(0);
  if (lane == 0) __builtin_amdgcn_raw_buffer_store_b32(1u, rsy, fo + (1 + (w >> 2)) * 4 * NH * 128, 0, 16);
  if (h == 3 && (w & 2) == 0) { LST(351 + (w & 1) + (w >> 2) * 2); }
}

#define RL_UP 12
#define RL_DN 4
#define RED_MIN 40
#define BA_LOOK 310
constexpr int TLAG = 3;
constexpr int R_SI = 32768, R_PART = 98304;

__device__ __forceinline__ void red_role(Args& p, int j, char* smem) {
  const int tid = threadIdx.x;
  int lane = tid & 63;
  const int w = __builtin_amdgcn_readfirstlane(tid >> 6);
  const int c = (j & 2) * 8 + (j >> 2), rh = j & 1;
  const int hg = (w + (j >> 2)) & 7, rot = (j >> 2) + (j >> 5);
  const __amdgpu_buffer_rsrc_t rsy = mkrsrc(p.ws + WS_SY, SY_TOTAL);
  const u32x4 rs4 = raw_rsrc(p.ws + WS_SY, SY_TOTAL);
  const __amdgpu_buffer_rsrc_t rt = mkrsrc(p.ws + WS_T, T_BYTES);
  gu32* abortw = (gu32*)(p.ws + WS_SY + SY_ABORT);
  u32x32 W[4];
  {
    const u16* Wo = p.Wo + (size_t)(4 * hg * DH) * DM + 32 * c;
    const int ro = ((lane >> 4) * 8 + ((lane & 15) >> 2)) * 64 + (lane & 3) * 8;
    load_blocks<16>(smem + 32768 + w * 16384,
        [&](int i, char* slot) {
          const int x = ((i >> 2) + rot) & 3;
          blk_dma(Wo + (size_t)((x >> 1) * 256 + (x & 1) * 64 + ((i >> 1) & 1) * 128 + (i & 1) * 32) * DM, DM, slot, lane);
        },
        [&](int i, char* slot) {
#pragma unroll
          for (int t = 0; t < 2; ++t) {
            s16x4 lo = __builtin_amdgcn_ds_read_tr16_b64_v4i16((lds_s16x4_ptr)(slot + ro + t * 32));
            s16x4 hi = __builtin_amdgcn_ds_read_tr16_b64_v4i16((lds_s16x4_ptr)(slot + ro + t * 32 + 256));
            put4(W[i >> 2], (i & 2) * 2 + 2 * t + (i & 1), (bf16x8){lo[0], lo[1], lo[2], lo[3], hi[0], hi[1], hi[2], hi[3]});
          }
        });
  }
  const unsigned pm = side_mask<true>(p.ws);
  p.s0 = pm >> 31;
  const int hs = side(pm, 1);
  if ((c & 14) != 6 && GPF < 3) {
#pragma unroll
    for (int i = 0; i < 4; ++i)
      touch_line((const char*)(i < 2 ? p.wg : p.wu) + (size_t)(j - (j >= 32 ? 8 : 0)) * 131072 + ((i & 1) * 8 + w) * 8192 + lane * 128, smem + w * 256);
  }
  {
    const u32x4 ds = {DSENT, DSENT, DSENT, DSENT};
#pragma unroll
    for (int i = 0; i < 2; ++i) {
      const int s = (w * 2 + i) * 4 + (lane >> 4);
      if (s >= 2) st_sc1(rsy, o_mem(pm, s) + (c >> 4) * 16384 + (c & 15) * 512 + rh * 128 + ((lane >> 3) & 1) * 256 + (lane & 7) * 16, ds);
    }
  }
  if (tid < 4) { reinterpret_cast<unsigned*>(smem + 4096)[tid] = tid == 3 ? now_q() : 0; reinterpret_cast<unsigned*>(smem + 4112)[tid] = DSENT; }
  if (tid < 256) reinterpret_cast<int*>(smem + 5120)[tid] = ((tid >> 2) - TLAG - 6) >> 2;
  LBAR();
  const unsigned base = __builtin_amdgcn_readfirstlane(lds_at(smem + 4096)[3]);
  RDY(64 + j);
  unsigned long long fm = 0, ps = 0;
  const unsigned aL = lane * 16;
  const int e = ((56 + w) * SEG + rh * 16 + (lane >> 2)) * DM + c * 32 + (lane & 3) * 8;
  u32x4 xv = *reinterpret_cast<const u32x4*>(p.x + e), av = {DSENT, DSENT, DSENT, DSENT};
  asm volatile("" : "+v"(xv));
  {
    const int r = lane & 15, q = lane >> 4;
    const int sm0 = o_mem(pm, 0) + (c >> 4) * 16384 + (c & 15) * 512 + rh * 128;
    u32x4 VO, VT;
    unsigned fo = SY_FO + ((rh + (lane & 4) / 2) * NH + 4 * hg + (lane & 3)) * 128;
    unsigned ao = w == 1 ? (c * S_LEN + rh * 16 - (TLAG + 3) * SEG) * 64 + lane * 16
                         : (w >= 4 ? SY_O + NPRE * 131072 + side(pm, w - 2) + (j >> 3) * 16384 + (j & 7) * 1024 + lane * 16
                                   : SY_FS + (2 * c + rh) * 128 + (lane < 8 ? lane * 16 : 0x70000000));
    unsigned ar = w == 1 ? R_SI - (TLAG + 2) * 1024 + lane * 16 : (w == 2 ? 5120 : 4112);
    const unsigned long long am = w == 1 ? (1ull << 62) - 64 : (w == 2 ? 0x4444444444444400ull : (w >= 4 ? (0xFFFFull << (16 * (w - 4))) >> NPRE : 0));
    static_assert(TLAG == 3, "");
    unsigned pw = R_PART + w * 2048 + lane * 16, pr = R_PART + 16384 + tid * 4;
    unsigned sw = R_SI - (TLAG + 1) * 1024 + (lane >> 2) * 64 + w * 8 + (lane & 3) * 2, stm = 8192 + w * 512;
#pragma unroll
    for (int l = 0; l < 4; ++l) {
      const int x = (l + rot) & 3;
      VO[l] = o_o(0, 0, 4 * hg) + rh * 65536 + x * 1024 + aL;
      VT[l] = (T_O + rh * 16 + 2 * hg + (x >> 1)) * 16384 + (x & 1) * 2048 + aL;
    }
    int rl = 280;
    unsigned step, E, so, st, st4, sh;
    asm volatile(RED_ASM
        : [step] "=&s"(step), [E] "=&s"(E), [so] "=&s"(so), [st] "=&s"(st), [st4] "=&s"(st4), [sh] "=&s"(sh),
          [rl] "+s"(rl), [fm] "+s"(fm), [ps] "+s"(ps),
          "+{v[224:227]}"(av), "+{v228}"(ao), "+{v229}"(ar), "+{v230}"(pw), "+{v231}"(pr), "+{v232}"(sw), "+{v233}"(stm), "+{v242}"(fo)
        : W4_IN, "{v168}"(aL), "{v169}"(w * 512 + lane * 8), "{v170}"(lane * 8),
          "{v171}"(sm0 + 32768 + (q >> 1) * 256 + (q & 1) * 64 + ((r >> 1) & 1) * 32 + (r & 1) * 16 + (r >> 2) * 4),
          "{v172}"((lane & 2) ? 0xEEEE : 0x4444), "{v173}"(lane < 4 ? SY_BA + hs + (4 * hg + lane) * 128 : 0x7F000000u),
          "{v174}"((r & 1) ? 0x03020706u : 0x05040100u), "{v175}"(SY_BR + hs + j * 128 + lane * 8), "{v[234:237]}"(VO), "{v[238:241]}"(VT), "{v243}"(e * 2),
          [rg] "s"(raw_rsrc(p.ws + WS_G, S_LEN * DM * 2)), [fg] "s"(SY_FG + ((14 + (w >> 2)) * 8 + (c >> 2)) * 128),
          [rs] "s"(rs4), [ra] "s"(w == 1 ? raw_rsrc(p.ws + WS_S, S_LEN * DM * 2) : rs4), [rt] "s"(raw_rsrc(p.ws + WS_T, T_BYTES)),
          [ds] "n"(DS_RED), [mn] "n"(RED_MIN), [lk] "n"(DS_MEM + BA_LOOK), [up] "n"(RL_UP), [dn] "n"(-RL_DN), [sent] "s"(DSENT), [sab] "s"(SY_ABORT),
          [base] "s"(base), [pm] "s"(pm), [s0] "s"((p.s0 ^ TF_O) << 13), [am] "s"(am), [ainc] "s"(w == 1 ? SEG * 64 : (w >= 4 ? 131072 : 0)),
          [rinc] "s"(w == 1 ? 1024 : (w == 2 ? 16 : 0))
        : CLB32(128), CLB8(160, 161, 162, 163, 164, 165, 166, 167), CLB8(176, 177, 178, 179, 180, 181, 182, 183),
          CLB8(184, 185, 186, 187, 188, 189, 190, 191), CLB8(192, 193, 194, 195, 196, 197, 198, 199), CLB8(200, 201, 202, 203, 204, 205, 206, 207),
          CLB8(208, 209, 210, 211, 212, 213, 214, 215), CLB8(216, 217, 218, 219, 220, 221, 222, 223), CLB_E, "s96", CLB_S);
  }
  if (j == 0 && w == 5) { LST(345); }
  LBAR();
  if (j == 0 && w == 5) { LST(346); }
  u32x4 sv;
  if (w < 3) sv = *reinterpret_cast<const u32x4*>(smem + R_SI + (56 + w) * 1024 + lane * 16);
  else {
    const f32x4* part = reinterpret_cast<const f32x4*>(smem + (w == 3 ? R_PART : (w - 3) * 16384)) +
                        (lane & 2) * 32 + (lane & 1) * 32 + (lane >> 2);
    f32x4 x[8][2];
#pragma unroll
    for (int ww = 0; ww < 8; ++ww) { x[ww][0] = part[ww * 128]; x[ww][1] = part[ww * 128 + 16]; }
#pragma unroll
    for (int u = 0; u < 2; ++u) {
      const f32x4 a = ((x[0][u] + x[1][u]) + (x[2][u] + x[3][u])) + ((x[4][u] + x[5][u]) + (x[6][u] + x[7][u]));
      sv[2 * u] = cvtpk(a[0], a[1]); sv[2 * u + 1] = cvtpk(a[2], a[3]);
    }
  }
  *reinterpret_cast<u32x4*>(p.out + e) = combine(sv, av, xv);
  if (tid == 128) { LST(400 + j); }
}

__global__ __launch_bounds__(512, 2) void main_kernel(Args p) {
  char* smem = g_smem;
  p.ws = ws_align(p.ws);
  const int bx = blockIdx.x & 7, bk = blockIdx.x >> 3;
  const bool isg = (bx ^ XPROBE) & 4;
  const int b = (isg ? NCHAIN : 0) + bk * 4 + (bx & 3);
  if (b == 64 && threadIdx.x == 0) { LST(500); }
  if (b >= NCHAIN) { p.s0 = side_mask<false>(p.ws) >> 31; gemm_role(p, b - NCHAIN, smem); }
  else if (b < 32) qk_role(p, (b & 3) * 8 + bk, smem);
  else if (b < 64) vu_role(p, (b & 3) * 8 + bk - 8, smem);
  else red_role(p, b - 64, smem);
}

__global__ void chase_kernel(const char* p, char* ws) {
  unsigned off = 0, acc = 0;
  for (int i = 0; i < 200; ++i) {
    unsigned v;
    const char* a = p + off;
    asm volatile("global_load_dword %0, %1, off\n\ts_waitcnt vmcnt(0)" : "=v"(v) : "v"(a) : "memory");
    acc += v;
    off += 8192 + 128 * 5;
  }
  if (acc == 0x12345678u) *(unsigned*)(ws + (255u << 20)) = acc;
}
extern "C" void kernel_launch(void* const* d_in, const int* in_sizes, int n_in,
                              void* d_out, int out_size, void* d_ws, size_t ws_size,
                              hipStream_t stream) {
  Args a;
  a.x = (const u16*)d_in[0]; a.mem0 = (const u16*)d_in[1];
  a.Wq = (const u16*)d_in[2]; a.Wk = (const u16*)d_in[3]; a.Wv = (const u16*)d_in[4]; a.Wo = (const u16*)d_in[5];
  a.wg = (const u16*)d_in[8]; a.wu = (const u16*)d_in[9]; a.wd = (const u16*)d_in[10];
  a.ws = (char*)d_ws; a.out = (u16*)d_out; a.s0 = 0; a.pad1 = 0;
  chase_kernel<<<1, 64, 0, stream>>>((const char*)d_in[10], a.ws);
  ln_kernel<<<S_LEN / 8 + 8 * NPAGE, 512, 0, stream>>>(a.x, a.ws, a.mem0);
  main_kernel<<<256, 512, 0, stream>>>(a);
}
```

```cpp
#include <hip/hip_runtime.h>
#include <hip/hip_bf16.h>
#include <stdint.h>

constexpr int S_LEN = 2048, DM = 1024, NH = 32, DH = 128, NMEM = 8, SEG = 32;
constexpr int NSEG = S_LEN / SEG;
constexpr int HD = NH * DH;
constexpr int FF = 3584;

typedef unsigned short u16;
typedef __attribute__((ext_vector_type(8))) short bf16x8;
typedef __attribute__((ext_vector_type(4))) short s16x4;
typedef __attribute__((ext_vector_type(4))) float f32x4;
typedef __attribute__((ext_vector_type(4))) unsigned int u32x4;
typedef __attribute__((ext_vector_type(2))) unsigned int u32x2;
typedef __attribute__((address_space(3))) s16x4* lds_s16x4_ptr;
typedef __attribute__((address_space(3))) void* lds_void_ptr;
typedef __attribute__((address_space(1))) const void* glb_cvoid_ptr;
typedef __attribute__((address_space(1))) unsigned gu32;
typedef __bf16 bf16x2_t __attribute__((ext_vector_type(2)));
typedef float f32x2_t __attribute__((ext_vector_type(2)));

__device__ __forceinline__ float bf2f(u16 v) { return __uint_as_float(((unsigned)v) << 16); }
__device__ __forceinline__ unsigned cvtpk(float lo, float hi) {
  bf16x2_t r = __builtin_convertvector((f32x2_t){lo, hi}, bf16x2_t);
  return __builtin_bit_cast(unsigned, r);
}
__device__ __forceinline__ float lo_f(unsigned v) { return __uint_as_float(v << 16); }
__device__ __forceinline__ float hi_f(unsigned v) { return __uint_as_float(v & 0xffff0000u); }
__device__ __forceinline__ float rbf(float f) { return lo_f(cvtpk(f, f)); }
__device__ __forceinline__ bf16x8 as_bf16x8(u32x4 v) { return __builtin_bit_cast(bf16x8, v); }
#define MFMA(a, b, c) __builtin_amdgcn_mfma_f32_16x16x32_bf16((a), (b), (c), 0, 0, 0)
typedef __attribute__((ext_vector_type(16))) short bf16x16;
typedef __attribute__((ext_vector_type(8))) __bf16 bfv8;
typedef __attribute__((ext_vector_type(16))) __bf16 bfv16;
#define SMFMA(a, b, c, idx) __builtin_amdgcn_smfmac_f32_16x16x64_bf16(__builtin_bit_cast(bfv8, (a)), __builtin_bit_cast(bfv16, (b)), (c), (idx), 0, 0)
__device__ __forceinline__ bf16x16 cat16(bf16x8 lo, bf16x8 hi) {
  return __builtin_shufflevector(lo, hi, 0, 1, 2, 3, 4, 5, 6, 7, 8, 9, 10, 11, 12, 13, 14, 15);
}

#define KSTR 65
#define XLD 1024
constexpr int PAGE = 2 << 20, NPAGE = 14;
constexpr int T_K = 0, T_Q = 2304, T_V = 4608, T_O = 6912, T_END = T_O + 2048;
constexpr unsigned T_BYTES = T_END * 16384u;
constexpr size_t WS_SY = 0;
constexpr size_t WS_T = (size_t)NPAGE * PAGE;
constexpr size_t WS_XN = WS_T + T_BYTES;
constexpr size_t WS_HF = WS_XN + (size_t)S_LEN * XLD * 2;
constexpr size_t WS_G = WS_HF + (size_t)S_LEN * 16384;
constexpr size_t WS_S = WS_G + (size_t)S_LEN * DM * 2;
constexpr size_t WS_END = WS_S + (size_t)S_LEN * DM * 2;
constexpr unsigned SENT = 0xFFFFFFFFu;
constexpr unsigned DSENT = 0xFFBFFFBFu;
constexpr int SY_MEM = 0;
constexpr int SY_O = 2 * PAGE;
constexpr int SY_V = 6 * PAGE;
constexpr int SY_BR = PAGE, SY_BA = SY_BR + 16384;
constexpr int SY_FKQ = SY_BR + 32768;
constexpr int SY_FG = SY_FKQ + NH * 4 * 128;
constexpr int SY_FO = SY_FG + 16 * 8 * 128;
constexpr int SY_CH = SY_FO + 384 * 128;
constexpr int SY_Q = SY_CH + 8 * 128;
constexpr int SY_ABORT = SY_Q + 8 * 128;
constexpr int SY_FS = SY_ABORT + 256;
constexpr int SY_FEND = SY_FS + 64 * 128;
constexpr int SY_EP = SY_FEND, SY_BT = SY_EP + 128;
constexpr int SY_TOTAL = NPAGE * PAGE;
constexpr int PROBE = PAGE - 16384 + 6144;
__device__ __forceinline__ int side(unsigned pm, int page) { return ((pm >> page) & 1u) << 13; }
__device__ __forceinline__ int o_mem(unsigned pm, int s) { return SY_MEM + s * 32768 + side(pm, 0); }
__device__ __forceinline__ int o_o(unsigned pm, int s, int h) {
  return SY_O + s * 131072 + (h >> 3) * 16384 + ((h >> 1) & 3) * 2048 + (h & 1) * 128 + side(pm, 2 + (s >> 4));
}
__device__ __forceinline__ int o_v(unsigned pm, int s, int h) { return SY_V + s * 262144 + (h >> 1) * 16384 + (h & 1) * 4096 + side(pm, 6 + (s >> 3)); }
__device__ __forceinline__ char* ws_align(char* ws) { return ws + ((PAGE - (unsigned)(uintptr_t)ws) & (PAGE - 1)); }
#define NFUSE 12
constexpr int NPRE = 4;
static_assert(WS_END + PAGE <= (size_t)256 << 20 && NH * KSTR <= T_Q && NPAGE % 2 == 0, "");
constexpr int NCHAIN = 128, NGW = 32;
__host__ __device__ constexpr int kseg(int sg, int h) { return h * KSTR + sg; }
__host__ __device__ constexpr int fkq(int h, int kind) { return SY_FKQ + (h * 4 + kind) * 128; }

#define HSKEW 0
#define XPROBE 4
struct Args {
  const u16 *x, *mem0, *Wq, *Wk, *Wv, *Wo, *wg, *wu, *wd;
  char* ws;
  u16* out;
  int s0, pad1;
};
constexpr int HLD = 16384 + HSKEW;
__device__ __forceinline__ int h_off(const Args& p, int rblk) { return rblk * PAGE + (((p.s0 ^ rblk ^ 1) & 1) << 13); }
static_assert(WS_HF % (2 * PAGE) == 0 && 127 * HSKEW + FF * 2 <= 8192, "");
#define TF_KQV 1
#define TF_O 0
__device__ __forceinline__ int t_off(const Args& p, int t) { return t * 16384 + (((p.s0 ^ (t >> 7) ^ (TF_KQV == 2 ? t : TF_KQV)) & 1) << 13); }
__device__ __forceinline__ int t_off_o(const Args& p, int t) { return t * 16384 + (((p.s0 ^ (t >> 7) ^ (TF_O == 2 ? t : TF_O)) & 1) << 13); }

__device__ __forceinline__ float wave_sum(float v) {
#pragma unroll
  for (int i = 0; i < 4; ++i) {
    const int x = (int)__float_as_uint(v);
    v += __uint_as_float(i == 0 ? __builtin_amdgcn_update_dpp(0, x, 0x128, 0xf, 0xf, false) : i == 1 ? __builtin_amdgcn_update_dpp(0, x, 0x124, 0xf, 0xf, false)
                         : i == 2 ? __builtin_amdgcn_update_dpp(0, x, 0x122, 0xf, 0xf, false) : __builtin_amdgcn_update_dpp(0, x, 0x121, 0xf, 0xf, false));
  }
  auto r = __builtin_amdgcn_permlane16_swap(__float_as_uint(v), __float_as_uint(v), false, false);
  v = __uint_as_float(r[0]) + __uint_as_float(r[1]);
  r = __builtin_amdgcn_permlane32_swap(__float_as_uint(v), __float_as_uint(v), false, false);
  return __uint_as_float(r[0]) + __uint_as_float(r[1]);
}

__shared__ __attribute__((aligned(16))) char g_smem[163840];
__device__ __forceinline__ unsigned lds_off(const void* p) {
  return (unsigned)(uintptr_t)(lds_void_ptr)g_smem + (unsigned)((const char*)p - g_smem);
}
__device__ __forceinline__ __amdgpu_buffer_rsrc_t mkrsrc(const void* p, unsigned bytes) {
  return __builtin_amdgcn_make_buffer_rsrc((void*)p, 0, bytes, 0x00020000);
}
__device__ __forceinline__ u32x4 ld_sc1(__amdgpu_buffer_rsrc_t rs, int off) {
  return __builtin_amdgcn_raw_buffer_load_b128(rs, off, 0, 16);
}
__device__ __forceinline__ void st_sc1(__amdgpu_buffer_rsrc_t rs, int off, u32x4 v) {
  __builtin_amdgcn_raw_buffer_store_b128(v, rs, off, 0, 16);
}
__device__ __forceinline__ unsigned ror8(unsigned v) {
  return (unsigned)__builtin_amdgcn_update_dpp(0, (int)v, 0x128, 0xf, 0xf, false);
}
__device__ __forceinline__ float ror8f(float v) { return __uint_as_float(ror8(__float_as_uint(v))); }
__device__ __forceinline__ void swap16(unsigned& x, unsigned& y) {
  auto r = __builtin_amdgcn_permlane16_swap(x, y, false, false);
  x = r[0]; y = r[1];
}
__device__ __forceinline__ void swap32(unsigned& x, unsigned& y) {
  auto r = __builtin_amdgcn_permlane32_swap(x, y, false, false);
  x = r[0]; y = r[1];
}
__device__ __forceinline__ f32x4 fold32(f32x4 x, f32x4 y) {
  f32x4 o;
#pragma unroll
  for (int j = 0; j < 4; ++j) {
    unsigned a = __float_as_uint(x[j]), b = __float_as_uint(y[j]);
    swap32(a, b);
    o[j] = __uint_as_float(a) + __uint_as_float(b);
  }
  return o;
}
__device__ __forceinline__ bf16x8 tr_frag(const char* img_lane) {
  s16x4 lo = __builtin_amdgcn_ds_read_tr16_b64_v4i16((lds_s16x4_ptr)img_lane);
  s16x4 hi = __builtin_amdgcn_ds_read_tr16_b64_v4i16((lds_s16x4_ptr)(img_lane + 64));
  return (bf16x8){lo[0], lo[1], lo[2], lo[3], hi[0], hi[1], hi[2], hi[3]};
}
__device__ __forceinline__ float rowq_max(float v) {
  unsigned a = __float_as_uint(v), b = a;
  swap16(a, b);
  v = __builtin_elementwise_maximum(__uint_as_float(a), __uint_as_float(b));
  a = __float_as_uint(v); b = a;
  swap32(a, b);
  return __builtin_elementwise_maximum(__uint_as_float(a), __uint_as_float(b));
}
__device__ __forceinline__ float rowq_sum(float v) {
  unsigned a = __float_as_uint(v), b = a;
  swap16(a, b);
  v = __uint_as_float(a) + __uint_as_float(b);
  a = __float_as_uint(v); b = a;
  swap32(a, b);
  return __uint_as_float(a) + __uint_as_float(b);
}
__device__ __forceinline__ bool spin_fail(unsigned& spins, gu32* abortw) {
  ++spins;
  if ((spins & 63u) == 0u) {
    const __amdgpu_buffer_rsrc_t ra = mkrsrc((const void*)abortw, 4);
    unsigned z = 0;
    asm volatile("" : "+v"(z));
    if (__builtin_amdgcn_readfirstlane(__builtin_amdgcn_raw_buffer_load_b32(ra, z, 0, 16)) == 1u) return true;
    if (spins > (1u << 19)) {
      __builtin_amdgcn_raw_buffer_store_b32(z + 1u, ra, z, 0, 16);
      return true;
    }
  }
  return false;
}
__device__ __forceinline__ unsigned umax4(u32x4 v) { return max(max(v[0], v[1]), max(v[2], v[3])); }
__device__ __forceinline__ bool has_ds(u32x4 v) { return (v[0] == DSENT) | (v[1] == DSENT) | (v[2] == DSENT) | (v[3] == DSENT); }
__device__ __forceinline__ float fmax2(float a, float b) { return __builtin_elementwise_maximum(a, b); }
__device__ __forceinline__ bf16x8 ldsf(const char* p) { return *reinterpret_cast<const bf16x8*>(p); }
#define SCHED() __builtin_amdgcn_sched_barrier(0)
#define LBAR() do { asm volatile("s_waitcnt lgkmcnt(0)" ::: "memory"); __builtin_amdgcn_s_barrier(); asm volatile("" ::: "memory"); } while (0)
#define WAIT_V(n) asm volatile("s_waitcnt vmcnt(%0)" :: "n"(n) : "memory")

__device__ __forceinline__ void wait_flags(__amdgpu_buffer_rsrc_t rs, int off, gu32* abortw) {
  for (unsigned spins = 0;;) {
    asm volatile("" ::: "memory");
    const unsigned v = __builtin_amdgcn_raw_buffer_load_b32(rs, off, 0, 16);
    if (__all(v != SENT)) break;
    __builtin_amdgcn_s_sleep(4);
    if (spin_fail(spins, abortw)) break;
  }
  asm volatile("" ::: "memory");
}

#define LST(i) do {} while (0)
#define RDY(i) do {} while (0)
#define SMP(k, s, v) do {} while (0)
#define LSTC(i) do {} while (0)
__device__ __forceinline__ unsigned offA(unsigned row, unsigned ch) {
  return row * 128u + 16u * (ch ^ (row & 7u));
}
__device__ __forceinline__ unsigned swzB(unsigned row) { return ((row & 3u) << 2) | ((row >> 2) & 3u); }
__device__ __forceinline__ unsigned offB(unsigned row, unsigned ch) {
  return 256u * row + 16u * (ch ^ swzB(row));
}
__device__ __forceinline__ void glds16(const u16* src, char* dst) {
  const unsigned d = __builtin_amdgcn_readfirstlane(lds_off(dst));
  unsigned keep;
  asm volatile("s_mov_b32 %0, m0\n\ts_mov_b32 m0, %2\n\ts_nop 0\n\tglobal_load_lds_dwordx4 %1, off\n\ts_mov_b32 m0, %0"
               : "=&s"(keep) : "v"(src), "s"(d) : "memory");
}
__device__ __forceinline__ bf16x8 ld_bfrag(const char* sb, int ks, int c, int lane) {
  const unsigned g = lane >> 4, qq = (lane & 15) >> 2, pp = lane & 3;
  s16x4 lo = __builtin_amdgcn_ds_read_tr16_b64_v4i16(
      (lds_s16x4_ptr)(sb + offB(32 * ks + 8 * g + qq, 2 * c + (pp >> 1)) + 8 * (pp & 1)));
  s16x4 hi = __builtin_amdgcn_ds_read_tr16_b64_v4i16(
      (lds_s16x4_ptr)(sb + offB(32 * ks + 8 * g + 4 + qq, 2 * c + (pp >> 1)) + 8 * (pp & 1)));
  return (bf16x8){lo[0], lo[1], lo[2], lo[3], hi[0], hi[1], hi[2], hi[3]};
}

__device__ __forceinline__ void blk_dma(const u16* src, size_t ld, char* slot, int lane) {
  const u16* s = src + (size_t)(lane >> 2) * ld + (lane & 3) * 8;
  glds16(s, slot);
  glds16(s + 16 * ld, slot + 1024);
}
__device__ __forceinline__ bf16x8 blk_frag(const char* slot, int T, int lane) {
  const char* a = slot + ((lane >> 4) * 8 + ((lane & 15) >> 2)) * 64 + (lane & 3) * 16 + T * 8;
  s16x4 lo = __builtin_amdgcn_ds_read_tr16_b64_v4i16((lds_s16x4_ptr)a);
  s16x4 hi = __builtin_amdgcn_ds_read_tr16_b64_v4i16((lds_s16x4_ptr)(a + 256));
  return (bf16x8){lo[0], lo[1], lo[2], lo[3], hi[0], hi[1], hi[2], hi[3]};
}
template <int NB, typename ISSUE, typename SINK>
__device__ __forceinline__ void load_blocks(char* slice, ISSUE&& issue, SINK&& sink) {
#pragma unroll
  for (int i = 0; i < 8; ++i) issue(i, slice + i * 2048);
#pragma unroll
  for (int g = 0; g < NB / 4; ++g) {
    if (g + 1 < NB / 4) WAIT_V(8); else WAIT_V(0);
#pragma unroll
    for (int j = 0; j < 4; ++j) sink(4 * g + j, slice + ((4 * g + j) & 7) * 2048);
    asm volatile("s_waitcnt lgkmcnt(0)" ::: "memory");
    if (g + 2 < NB / 4) {
#pragma unroll
      for (int j = 0; j < 4; ++j) issue(4 * g + 8 + j, slice + ((4 * g + j) & 7) * 2048);
    }
  }
}

__device__ __forceinline__ u32x4 widen32(unsigned a0, unsigned a1, unsigned b0, unsigned b1) {
  swap32(a0, b0); swap16(a0, b0);
  swap32(a1, b1); swap16(a1, b1);
  return (u32x4){a0, a1, b0, b1};
}

__device__ __forceinline__ void publish_flags(__amdgpu_buffer_rsrc_t rs, int off, int stride, int n) {
  WAIT_V(0);
  LBAR();
  int tid = threadIdx.x;
  asm volatile("" : "+v"(tid));
  if (tid < n) __builtin_amdgcn_raw_buffer_store_b32(1u, rs, off + tid * stride, 0, 16);
}

__device__ __forceinline__ void wait_s(const Args& p, __amdgpu_buffer_rsrc_t rsy, int rblk, int c0, int n, bool need) {
  if (need) {
    if (threadIdx.x < 64) {
      for (unsigned spins = 0;;) {
        asm volatile("" ::: "memory");
        const int f = (int)__builtin_amdgcn_raw_buffer_load_b32(rsy, SY_FS + (2 * c0 + (threadIdx.x & (2 * n - 1))) * 128, 0, 16);
        if (__all(f >= rblk)) break;
        __builtin_amdgcn_s_sleep(32);
        if (spin_fail(spins, (gu32*)(p.ws + WS_SY + SY_ABORT))) break;
      }
    }
    LBAR();
  }
}
__device__ __forceinline__ u32x4 combine(u32x4 sv, u32x4 g, u32x4 x) {
  u32x4 o;
#pragma unroll
  for (int j = 0; j < 4; ++j) {
    const unsigned t = cvtpk(lo_f(sv[j]) * lo_f(g[j]), hi_f(sv[j]) * hi_f(g[j]));
    o[j] = cvtpk(lo_f(x[j]) + lo_f(t), hi_f(x[j]) + hi_f(t));
  }
  return o;
}
struct SX { u32x4 s, x; };
__device__ __forceinline__ SX get_sx(const Args& p, int rblk, int e) {
  SX r; r.s = (u32x4){0u, 0u, 0u, 0u}; r.x = r.s;
  if (rblk < NFUSE) {
    r.s = ld_sc1(mkrsrc(p.ws + WS_S, S_LEN * DM * 2), (((e & 1023) >> 5) * S_LEN + (e >> 10)) * 64 + (e & 31) * 2);
    r.x = *reinterpret_cast<const u32x4*>(p.x + e);
  }
  return r;
}
__device__ __forceinline__ void put_g(const Args& p, __amdgpu_buffer_rsrc_t rg, int rblk, int e, u32x4 g, const SX& v) {
  if (rblk < NFUSE) {
    *reinterpret_cast<u32x4*>(p.out + e) = combine(v.s, g, v.x);
  } else st_sc1(rg, e * 2, g);
}

__device__ __forceinline__ void job_comb(const Args& p, int rblk, int ct, __amdgpu_buffer_rsrc_t rsy) {
  int tid = threadIdx.x;
  asm volatile("" : "+v"(tid));
  u32x4 xv[8];
#pragma unroll
  for (int i = 0; i < 8; ++i) xv[i] = *reinterpret_cast<const u32x4*>(p.x + (rblk * 128 + i * 16 + (tid >> 5)) * DM + ct * 256 + (tid & 31) * 8);
  if (tid < 64) wait_flags(rsy, SY_FG + (rblk * 8 + 2 * ct + (tid & 1)) * 128, (gu32*)(p.ws + WS_SY + SY_ABORT));
  wait_s(p, rsy, rblk, ct * 8, 8, true);
  u32x4 sv[8], gv[8];
#pragma unroll
  for (int i = 0; i < 8; ++i) {
    const int e = (rblk * 128 + i * 16 + (tid >> 5)) * DM + ct * 256 + (tid & 31) * 8;
    sv[i] = ld_sc1(mkrsrc(p.ws + WS_S, S_LEN * DM * 2), (((e & 1023) >> 5) * S_LEN + (e >> 10)) * 64 + (e & 31) * 2);
    gv[i] = ld_sc1(mkrsrc(p.ws + WS_G, S_LEN * DM * 2), e * 2);
  }
#pragma unroll
  for (int i = 0; i < 8; ++i)
    *reinterpret_cast<u32x4*>(p.out + (rblk * 128 + i * 16 + (tid >> 5)) * DM + ct * 256 + (tid & 31) * 8) = combine(sv[i], gv[i], xv[i]);
}

__device__ __forceinline__ u32x4 raw_rsrc(const void* p, unsigned bytes) {
  const unsigned long long a = (unsigned long long)p;
  return (u32x4){(unsigned)a, (unsigned)(a >> 32) & 0xffffu, bytes, 0x00020000u};
}
__device__ __forceinline__ u32x2 next_tag(u32x2 e) { return (u32x2){e[0] + 1u, e[1] + (e[0] == 0xFFFFFFFFu)}; }
struct Boot { u32x2 e, a, b; u32x4 c; };
__device__ __forceinline__ void boot_ask(const char* ws, Boot& r) {
  const unsigned o = (threadIdx.x & 63) * 128, oc = (threadIdx.x & 31) * 128;
  asm volatile("buffer_load_dwordx2 %0, off, %6, %7 sc1\n\tbuffer_load_dwordx2 %1, %4, %6, %8 offen sc1\n\t"
               "buffer_load_dwordx2 %2, %4, %6, %9 offen sc1\n\tbuffer_load_dwordx4 %3, %5, %6, %10 offen sc1"
               : "=&v"(r.e), "=&v"(r.a), "=&v"(r.b), "=&v"(r.c)
               : "v"(o), "v"(oc), "s"(raw_rsrc(ws, SY_TOTAL)), "s"(SY_EP), "s"(SY_BT), "s"(SY_BT + 8192), "s"(SY_BT + 16384) : "memory");
}
__device__ __forceinline__ unsigned boot_done(const char* ws, Boot& r, bool own = false, u32x2 tag = (u32x2){0u, 0u}) {
#pragma unroll 1
  for (int i = 0; i < (1 << 17); ++i) {
    asm volatile("" : "+v"(r.e), "+v"(r.a), "+v"(r.b), "+v"(r.c));
    if (!own) tag = next_tag(r.e);
    if (__all(r.a[0] == tag[0] && r.a[1] == tag[1] && r.b[0] == tag[0] && r.b[1] == tag[1] && r.c[0] == tag[0] && r.c[1] == tag[1])) break;
    __builtin_amdgcn_s_sleep(8);
    boot_ask(ws, r);
    WAIT_V(0);
  }
  unsigned q0[NPAGE], q1[NPAGE], ga = 0xFFFFu, gb = 0xFFFFu;
#pragma unroll
  for (int i = 0; i < NPAGE; ++i) {
    const unsigned x = __builtin_amdgcn_readlane(r.c[2], i), y = __builtin_amdgcn_readlane(r.c[2], i + NPAGE);
    q0[i] = min(x >> 16, y >> 16); q1[i] = min(x & 0xFFFFu, y & 0xFFFFu);
    ga = min(ga, (i & 1) ? q1[i] : q0[i]); gb = min(gb, (i & 1) ? q0[i] : q1[i]);
  }
  unsigned m = gb < ga ? 0x80001555u : 0x2AAAu;
  const unsigned thr = min(ga, gb) + 110;
#pragma unroll
  for (int i = 0; i < NPAGE; ++i) {
    const unsigned lo = min(q0[i], q1[i]), hi = max(q0[i], q1[i]);
    const unsigned keep = (lo < thr ? hi : 0u) > lo + 100 ? ~(1u << i) : ~0u;
    m = (m & keep) | ((q1[i] < q0[i] ? ~0u : 0u) & ~keep);
  }
  return m;
}
typedef __attribute__((address_space(3))) volatile unsigned* lds_vu_ptr;
__device__ __forceinline__ lds_vu_ptr lds_at(const void* p) {
  unsigned a = lds_off(p);
  asm volatile("" : "+v"(a));
  return (lds_vu_ptr)(uintptr_t)a;
}
__device__ __forceinline__ unsigned uni(unsigned v) {
  unsigned r;
  asm volatile("s_nop 1\n\tv_readfirstlane_b32 %0, %1\n\ts_nop 3" : "=s"(r) : "v"(v));
  return r;
}
__device__ __forceinline__ void probe_page(char* ws, int i, char* smem) {
  const int lane = threadIdx.x & 63, w = threadIdx.x >> 6;
  const __amdgpu_buffer_rsrc_t rsy = mkrsrc(ws, SY_TOTAL);
  const __amdgpu_buffer_rsrc_t rs = mkrsrc(ws + (size_t)(i % NPAGE) * PAGE + PROBE - (i / NPAGE) * 2048, 16384);
  const u32x2 e = __builtin_amdgcn_raw_buffer_load_b64(rsy, 0, SY_EP, 16);
  if (lane < 32) st_sc1(rs, (lane >> 4) * 8192 + w * 256 + (lane & 15) * 16, (u32x4){DSENT, DSENT, DSENT, DSENT});
  unsigned t[4];
#pragma unroll
  for (int k = 0; k < 4; ++k) {
    unsigned long long c0, c1; unsigned v, off = ((k ^ (k >> 1)) & 1) * 8192 + w * 256 + (k >> 1) * 128;
    asm volatile("s_waitcnt vmcnt(0)\n\ts_barrier\n\ts_memtime %0\n\ts_waitcnt lgkmcnt(0)\n\tbuffer_load_dword %2, %3, %4, 0 offen sc1\n\ts_waitcnt vmcnt(0)\n\t"
                 "s_memtime %1\n\ts_waitcnt lgkmcnt(0)" : "=&s"(c0), "=&s"(c1), "=&v"(v) : "v"(off), "s"(rs) : "memory");
    t[k] = (unsigned)(c1 - c0);
  }
  lds_vu_ptr tm = lds_at(smem);
  if (lane == 0) { tm[w] = min(t[0], t[3]); tm[8 + w] = min(t[1], t[2]); }
  int ro = SY_BT + (128 + i) * 128 + lane * 16;
  asm volatile("" : "+v"(ro));
  LBAR();
  if (threadIdx.x < 8) {
    unsigned m0 = tm[0], m1 = tm[8];
#pragma unroll
    for (int k = 1; k < 8; ++k) { m0 = min(m0, tm[k]); m1 = min(m1, tm[8 + k]); }
    const u32x2 tag = next_tag(e);
    st_sc1(rsy, ro, (u32x4){tag[0], tag[1], min(m0, 30000u) << 16 | min(m1, 30000u), 0u});
  }
}
template <int NB, typename F>
__device__ __forceinline__ u32x2 start_rows(const Args& p, int g, F&& between) {
  const int tid = threadIdx.x, lane = tid & 63, row0 = g * 16 + (tid >> 6) * 2;
  u32x4 a[4];
  u32x2 e;
  asm volatile("global_load_dwordx4 %0, %5, off\n\tglobal_load_dwordx4 %1, %5, off offset:1024\n\tglobal_load_dwordx4 %2, %5, off offset:2048\n\t"
               "global_load_dwordx4 %3, %5, off offset:3072\n\tbuffer_load_dwordx2 %4, off, %6, %7 sc1"
               : "=&v"(a[0]), "=&v"(a[1]), "=&v"(a[2]), "=&v"(a[3]), "=&v"(e)
               : "v"(p.x + (size_t)row0 * DM + lane * 8), "s"(raw_rsrc(p.ws, SY_TOTAL)), "s"(SY_EP) : "memory");
  between();
  const __amdgpu_buffer_rsrc_t rs = mkrsrc(p.ws + WS_SY, SY_TOTAL);
  static_assert(NPRE == 4 && (SY_FEND - SY_BR) / 16 <= 24576, "");
#pragma unroll
  for (int u = 0; u < 2; ++u) {
    const int i = (127 - g) * 1024 + u * 512 + tid;
    if (g >= 126) {
      const int j = (i >> 10) * 512 + (i & 511);
      const unsigned* m0 = reinterpret_cast<const unsigned*>(p.mem0 + (2 * ((j >> 2) & 3) + (j & 1)) * DM + (j >> 4) * 16) + ((j >> 1) & 1);
      u32x4 m;
#pragma unroll
      for (int k = 0; k < 4; ++k) { const unsigned t = m0[2 * k]; m[k] = t == DSENT ? 0xFFFFFFFFu : t; }
      st_sc1(rs, i * 16, m);
    } else {
      const int o = i >= 65536 ? SY_V + (i - 65536) * 16 : (i >= 32768 ? SY_O + (i - 32768) * 16 : (i < 4096 ? i * 16
                    : (i >= 8192 && i - 8192 < (SY_FEND - SY_BR) / 16 ? SY_BR + (i - 8192) * 16 : 0x7F000000)));
      const unsigned f = i >= 8192 && i < 32768 ? SENT : DSENT;
      st_sc1(rs, o, (u32x4){f, f, f, f});
    }
  }
  asm volatile("s_waitcnt vmcnt(%5)" : "+v"(a[0]), "+v"(a[1]), "+v"(a[2]), "+v"(a[3]), "+v"(e) : "n"(NB + 2) : "memory");
  const __amdgpu_buffer_rsrc_t ro = mkrsrc(p.ws + WS_XN, S_LEN * XLD * 2);
#pragma unroll
  for (int u = 0; u < 2; ++u) {
    float v[16];
#pragma unroll
    for (int i = 0; i < 8; ++i) { v[2 * i] = lo_f(a[2 * u + (i >> 2)][i & 3]); v[2 * i + 1] = hi_f(a[2 * u + (i >> 2)][i & 3]); }
    float s = 0.f;
#pragma unroll
    for (int i = 0; i < 16; ++i) s += v[i];
    const float mean = wave_sum(s) * (1.0f / DM);
    float q = 0.f;
#pragma unroll
    for (int i = 0; i < 16; ++i) { float d = v[i] - mean; q += d * d; }
    const float var = rbf(wave_sum(q) * (1.0f / DM));
    const float mb = rbf(mean);
    const float inv = 1.0f / rbf(sqrtf(rbf(var + 1e-5f)));
#pragma unroll
    for (int k = 0; k < 2; ++k) {
      u32x4 o;
#pragma unroll
      for (int i = 0; i < 4; ++i) {
        const unsigned t = cvtpk(v[8 * k + 2 * i] - mb, v[8 * k + 2 * i + 1] - mb);
        o[i] = cvtpk(lo_f(t) * inv, hi_f(t) * inv);
      }
      st_sc1(ro, (row0 + u) * (XLD * 2) + k * 1024 + lane * 16, o);
    }
  }
  return next_tag(e);
}
__device__ __forceinline__ void report_rows(const Args& p, int g, u32x2 tag) {
  if (threadIdx.x < 8) st_sc1(mkrsrc(p.ws + WS_SY, SY_TOTAL), SY_BT + g * 128 + threadIdx.x * 16, (u32x4){tag[0], tag[1], tag[0], tag[1]});
}

__device__ __forceinline__ unsigned ld_flag0(__amdgpu_buffer_rsrc_t rs, int off) {
  return __builtin_amdgcn_raw_buffer_load_b32(rs, off, 0, 16);
}
__device__ __forceinline__ void glds16_sc1(const char* src, char* dst) {
  const unsigned d = __builtin_amdgcn_readfirstlane(lds_off(dst));
  unsigned keep;
  asm volatile("s_mov_b32 %0, m0\n\ts_mov_b32 m0, %2\n\ts_nop 0\n\tglobal_load_lds_dwordx4 %1, off sc1\n\ts_mov_b32 m0, %0"
               : "=&s"(keep) : "v"(src), "s"(d) : "memory");
}

__device__ __forceinline__ void touch_line(const char* src, char* dst) {
  const unsigned d = __builtin_amdgcn_readfirstlane(lds_off(dst));
  unsigned keep;
  asm volatile("s_mov_b32 %0, m0\n\ts_mov_b32 m0, %2\n\ts_nop 0\n\tglobal_load_lds_dword %1, off sc1\n\ts_mov_b32 m0, %0"
               : "=&s"(keep) : "v"(src), "s"(d) : "memory");
}

#define GPF 1
#define WREV 1
__device__ __forceinline__ void glds2(const char* base, int step, unsigned voff, unsigned dst) {
  unsigned keep;
  const char* base2 = base + step;
  asm volatile("s_mov_b32 %0, m0\n\ts_mov_b32 m0, %4\n\ts_nop 0\n\tglobal_load_lds_dwordx4 %1, %2\n\t"
               "s_add_u32 m0, m0, 0x2000\n\ts_nop 0\n\tglobal_load_lds_dwordx4 %1, %3\n\ts_mov_b32 m0, %0"
               : "=&s"(keep) : "v"(voff), "s"(base), "s"(base2), "s"(dst) : "memory", "scc");
}

__device__ __forceinline__ void gemm_stream(const char* A, int lda, const char* B, int ldb, int nk, char* smem, f32x4 (&acc)[2][4]) {
  int tid = threadIdx.x;
  asm volatile("" : "+v"(tid));
  const int lane = tid & 63, r = lane & 15, q = lane >> 4;
  const int wid = __builtin_amdgcn_readfirstlane(tid >> 6);
  const int wr = wid >> 1, wc = wid & 1;
  const unsigned va = (tid >> 3) * lda + 16 * ((tid & 7) ^ ((tid >> 3) & 7)), vb = (tid >> 4) * ldb + 16 * ((tid & 15) ^ swzB((tid >> 4) & 15));
  const unsigned lw = lds_off(smem) + wid * 1024;
  auto stage = [&](int kt) {
    glds2(A + kt * 128, 64 * lda, va, lw + (kt & 3) * 32768);
    glds2(B + (size_t)kt * 64 * ldb, 32 * ldb, vb, lw + (kt & 3) * 32768 + 16384);
  };
  unsigned la[2], lb[4][2];
#pragma unroll
  for (int k = 0; k < 2; ++k) { la[k] = lds_off(smem) + offA(wr * 32 + r, k * 4 + q); asm volatile("" : "+v"(la[k])); }
#pragma unroll
  for (int n = 0; n < 4; ++n)
#pragma unroll
    for (int u = 0; u < 2; ++u) {
      lb[n][u] = lds_off(smem) + 16384 + offB(8 * q + 4 * u + (r >> 2), 2 * (wc * 4 + n) + ((r & 3) >> 1)) + 8 * (r & 1);
      asm volatile("" : "+v"(lb[n][u]));
    }
  typedef __attribute__((address_space(3))) const bf16x8* lds_frag_ptr;
  bf16x8 af[2][2], bf[2][4];
  auto ldA = [&](int bo, int ks, int m) { return *(lds_frag_ptr)(uintptr_t)(la[ks] + bo + m * 2048); };
  auto ldB = [&](int bo, int ks, int n) {
    s16x4 lo = __builtin_amdgcn_ds_read_tr16_b64_v4i16((lds_s16x4_ptr)(uintptr_t)(lb[n][0] + bo + ks * 8192));
    s16x4 hi = __builtin_amdgcn_ds_read_tr16_b64_v4i16((lds_s16x4_ptr)(uintptr_t)(lb[n][1] + bo + ks * 8192));
    return (bf16x8){lo[0], lo[1], lo[2], lo[3], hi[0], hi[1], hi[2], hi[3]};
  };
#pragma unroll
  for (int i = 0; i < 8; ++i) acc[i >> 2][i & 3] = (f32x4){0.f, 0.f, 0.f, 0.f};
  LBAR();
  stage(0); stage(1); stage(2);
  WAIT_V(8);
  LBAR();
#pragma unroll
  for (int m = 0; m < 2; ++m) af[0][m] = ldA(0, 0, m);
#pragma unroll
  for (int n = 0; n < 4; ++n) bf[0][n] = ldB(0, 0, n);
  for (int t0 = 0; t0 < nk; t0 += 4) {
#pragma unroll
    for (int b = 0; b < 4; ++b) {
      const int t = t0 + b;
#pragma unroll
      for (int ks = 0; ks < 2; ++ks) {
        if (ks == 1) {
          if (t + 2 < nk) WAIT_V(4); else WAIT_V(0);
          __builtin_amdgcn_s_barrier();
          if (t + 3 < nk) stage(t + 3);
        }
        const int nb = (ks ? (b + 1) & 3 : b) * 32768, nks = ks ^ 1;
#pragma unroll
        for (int n = 0; n < 4; ++n) {
          acc[0][n] = MFMA(bf[ks][n], af[ks][0], acc[0][n]);
          bf[nks][n] = ldB(nb, nks, n);
          acc[1][n] = MFMA(bf[ks][n], af[ks][1], acc[1][n]);
          if (n < 2) af[nks][n] = ldA(nb, nks, n);
          SCHED();
        }
      }
    }
  }
}

#define HSL 40
__device__ __forceinline__ void wait_h(const Args& p, int panel) {
  if (threadIdx.x == 0) {
    gu32* cnt = (gu32*)(p.ws + WS_SY + SY_CH) + panel * 32;
    for (unsigned spins = 0;;) {
      if (__hip_atomic_load(cnt, __ATOMIC_RELAXED, __HIP_MEMORY_SCOPE_AGENT) + 1u >= 28u) break;
      __builtin_amdgcn_s_sleep(HSL);
      if (++spins > (1u << 20)) { __hip_atomic_store((gu32*)(p.ws + WS_SY + SY_ABORT), 1u, __ATOMIC_RELAXED, __HIP_MEMORY_SCOPE_AGENT); break; }
    }
    __builtin_amdgcn_fence(__ATOMIC_ACQUIRE, "agent");
    asm volatile("s_waitcnt vmcnt(0)" ::: "memory");
  }
  __syncthreads();
}
__device__ __forceinline__ void job_down(const Args& p, int rblk, int ct, char* smem, __amdgpu_buffer_rsrc_t rsy) {
  wait_h(p, rblk >> 1);
  f32x4 acc[2][4];
  gemm_stream(p.ws + WS_HF + h_off(p, rblk), HLD, (const char*)(p.wd + ct * 128), DM * 2, FF / 64, smem, acc);
  wait_s(p, rsy, rblk, ct * 4, 4, rblk < NFUSE);
  int tid = threadIdx.x;
  asm volatile("" : "+v"(tid));
  const int lane = tid & 63, wid = tid >> 6, wr = wid >> 1, wc = wid & 1;
  const __amdgpu_buffer_rsrc_t ro = mkrsrc(p.ws + WS_G, S_LEN * DM * 2);
  SX sx[4];
#pragma unroll
  for (int i = 0; i < 4; ++i)
    sx[i] = get_sx(p, rblk, (rblk * 128 + wr * 32 + (i >> 1) * 16 + (lane & 15)) * DM + ct * 128 + wc * 64 + (i & 1) * 32 + (lane >> 4) * 8);
#pragma unroll
  for (int m = 0; m < 2; ++m) {
    const int row = rblk * 128 + wr * 32 + m * 16 + (lane & 15);
#pragma unroll
    for (int nb = 0; nb < 2; ++nb) {
      unsigned pk[2][2];
#pragma unroll
      for (int n = 0; n < 2; ++n) {
        float rr[4];
#pragma unroll
        for (int j = 0; j < 4; ++j) { float d = rbf(acc[m][2 * nb + n][j]); rr[j] = d * __builtin_amdgcn_rcpf(1.0f + __expf(-d)); }
        pk[n][0] = cvtpk(rr[0], rr[1]); pk[n][1] = cvtpk(rr[2], rr[3]);
      }
      u32x4 o = widen32(pk[0][0], pk[0][1], pk[1][0], pk[1][1]);
      put_g(p, ro, rblk, row * DM + ct * 128 + wc * 64 + nb * 32 + (lane >> 4) * 8, o, sx[2 * m + nb]);
    }
  }
  publish_flags(rsy, SY_FG + (rblk * 8 + ct) * 128, 0, 1);
}

constexpr int W_X = 131072, W_PM = W_X + 16384;
template <bool ISV>
__device__ __forceinline__ void job_proj(Args& p, int g, int which, int h, char* smem, __amdgpu_buffer_rsrc_t rsy) {
  const int tid = threadIdx.x, lane = tid & 63, r = lane & 15, q = lane >> 4;
  const int w = __builtin_amdgcn_readfirstlane(tid >> 6), kh = w >> 2, cg = w & 3;
  bf16x8 W[16][2];
  const unsigned lw = lds_off(smem) + w * 4096;
  unsigned vx[4];
  const char* xn = p.ws + WS_XN;
  auto rows = [&](int t) {
    const char* src = xn + (size_t)(min(t, 127) >> 1) * (SEG * XLD * 2) + (t & 1) * 512;
    unsigned keep;
    asm volatile("s_mov_b32 %0, m0\n\ts_mov_b32 m0, %6\n\ts_nop 0\n\tglobal_load_lds_dwordx4 %1, %5 sc1\n\t"
                 "s_add_u32 m0, m0, 0x400\n\ts_nop 0\n\tglobal_load_lds_dwordx4 %2, %5 sc1\n\t"
                 "s_add_u32 m0, m0, 0x400\n\ts_nop 0\n\tglobal_load_lds_dwordx4 %3, %5 sc1\n\t"
                 "s_add_u32 m0, m0, 0x400\n\ts_nop 0\n\tglobal_load_lds_dwordx4 %4, %5 sc1\n\ts_mov_b32 m0, %0"
                 : "=&s"(keep) : "v"(vx[0]), "v"(vx[1]), "v"(vx[2]), "v"(vx[3]), "s"(src), "s"(lw + (t & 3) * 32768) : "memory", "scc");
  };
  {
    const u16* Wm = which == 0 ? p.Wk : p.Wq;
    asm volatile("" : "+s"(Wm));
    if (ISV) Wm = p.Wv;
    const unsigned vo = (tid >> 4) * (HD * 2) + 16 * ((lane & 15) ^ swzB((tid >> 4) & 15));
    auto stage = [&](int j) {
      const char* src = (const char*)(Wm + (size_t)(WREV ? 7 - j : j) * 64 * HD + h * DH);
      const unsigned dst = lds_off(smem) + (j & 3) * 32768 + w * 1024;
      glds2(src, 32 * HD * 2, vo, dst);
      glds2(src + 512 * HD * 2, 32 * HD * 2, vo, dst + 16384);
    };
    const u32x2 tag = start_rows<8>(p, g, [&]() { stage(0); stage(1); });
    stage(2); stage(3);
#pragma unroll
    for (int i = 0; i < 4; ++i) vx[i] = ((4 * w + i) ^ (ISV ? 0 : (lane >> 1) & 16)) * (XLD * 2) + (lane >> 5) * 1024 + (((lane & 31) ^ ((4 * w + i) & 15)) << 4);
    Boot bt;
#pragma unroll
    for (int j = 0; j < 8; ++j) {
      if (j == 0) WAIT_V(18); else if (j < 5 || j == 7) WAIT_V(12); else if (j == 5) WAIT_V(8); else WAIT_V(4);
      if (j == 6 && w == 0) lds_at(smem + W_PM)[0] = boot_done(p.ws, bt, true, tag);
      LBAR();
      if (j == 1) {
        report_rows(p, g, tag);
      }
      const char* sb = smem + (j & 3) * 32768 + kh * 16384;
#pragma unroll
      for (int k = 0; k < 4; ++k) {
        const int ks = k >> 1, a = k & 1;
        const unsigned ch = ISV ? 2 * (2 * cg + (a ^ kh)) + ((r & 3) >> 1) : 4 * cg + (r & 3), by = ISV ? 8 * (r & 1) : 8 * a;
        s16x4 lo = __builtin_amdgcn_ds_read_tr16_b64_v4i16((lds_s16x4_ptr)(sb + offB(32 * ks + 8 * q + (r >> 2), ch) + by));
        s16x4 hi = __builtin_amdgcn_ds_read_tr16_b64_v4i16((lds_s16x4_ptr)(sb + offB(32 * ks + 8 * q + 4 + (r >> 2), ch) + by));
        W[2 * (WREV ? 7 - j : j) + ks][a] = (bf16x8){lo[0], lo[1], lo[2], lo[3], hi[0], hi[1], hi[2], hi[3]};
      }
      LBAR();
      if (j == 3 && w == 0) boot_ask(p.ws, bt);
      if (j + 4 < 8) stage(j + 4);
      if (j == 6) { rows(0); rows(1); rows(2); }
    }
    p.s0 = uni(lds_at(smem + W_PM)[0] >> 31);
  }
  RDY(128 + g);
  typedef __attribute__((address_space(3))) const bf16x8* lds_frag_ptr;
  typedef __attribute__((address_space(3))) f32x4* lds_f4_ptr;
  unsigned la[2][4];
#pragma unroll
  for (int i = 0; i < 8; ++i) {
    la[i >> 2][i & 3] = lds_off(smem) + (i >> 2) * 65536 + r * 1024 + kh * 512 + (((4 * (i & 3) + q) ^ r) << 4);
    asm volatile("" : "+v"(la[i >> 2][i & 3]));
  }
  unsigned xw = lds_off(smem) + W_X + w * 2048 + lane * 16, xr = xw ^ 8192;
  asm volatile("" : "+v"(xw), "+v"(xr));
  auto ldA = [&](int b, int ks, int m) { return *(lds_frag_ptr)(uintptr_t)(la[b >> 1][ks & 3] + (b & 1) * 32768 + m * 16384 + (ks >> 2) * 256); };
  const __amdgpu_buffer_rsrc_t ro = mkrsrc(p.ws + WS_T, T_BYTES);
  const int t0 = which * T_Q + kseg(0, h), vt = (ISV ? 2 * cg + kh : 4 * kh + cg) * 1024 + lane * 16;
  f32x4 acc[2][2][2], xs[2];
  bf16x8 af[4][2];
#pragma unroll
  for (int i = 0; i < 4; ++i) acc[1][i >> 1][i & 1] = (f32x4){0.f, 0.f, 0.f, 0.f};
  auto put = [&](int sp) {
#pragma unroll
    for (int b = 0; b < 2; ++b) *(lds_f4_ptr)(uintptr_t)(xw + b * 1024) = acc[sp][1][b];
  };
  auto get = [&]() {
#pragma unroll
    for (int b = 0; b < 2; ++b) xs[b] = *(lds_f4_ptr)(uintptr_t)(xr + b * 1024);
  };
  auto fin = [&](int sp, int sg) {
    const f32x4 x = acc[sp][0][0] + xs[0], y = acc[sp][0][1] + xs[1];
    __builtin_amdgcn_raw_buffer_store_b128((u32x4){cvtpk(x[0], x[1]), cvtpk(x[2], x[3]), cvtpk(y[0], y[1]), cvtpk(y[2], y[3])}, ro,
                                           vt + (sg < 0 ? 0x70000000 : t_off(p, t0 + sg)), 0, 16);
  };
  WAIT_V(8);
  LBAR();
#pragma unroll
  for (int i = 0; i < 4; ++i) af[i >> 1][i & 1] = ldA(0, i >> 1, i & 1);
#pragma unroll 1
  for (int tb = 0; tb < 2 * NSEG; tb += 4) {
#pragma unroll
    for (int b = 0; b < 4; ++b) {
      const int sp = b >> 1, u = b & 1, sg = (tb + b) >> 1;
#pragma unroll
      for (int ks = 0; ks < 8; ++ks) {
        if (ks == 4) {
          if (u) WAIT_V(5); else WAIT_V(4);
          __builtin_amdgcn_s_barrier();
          asm volatile("" ::: "memory");
          if (u == 0 && tid == 0 && sg >= 2) __builtin_amdgcn_raw_buffer_store_b32(sg - 2, rsy, fkq(h, which), 0, 16);
          rows(tb + b + 3);
          if (u == 0) get();
        }
#pragma unroll
        for (int m = 0; m < 2; ++m) {
          af[(ks + 2) & 3][m] = ldA(ks < 6 ? b : (b + 1) & 3, (ks + 2) & 7, m);
#pragma unroll
          for (int n = 0; n < 2; ++n) {
            f32x4& d = ISV ? acc[sp][n][m] : acc[sp][m][n];
            const f32x4 c = (u | ks) ? d : (f32x4){0.f, 0.f, 0.f, 0.f};
            d = ISV ? MFMA(af[ks & 3][m], W[8 * u + ks][n], c) : MFMA(W[8 * u + ks][n], af[ks & 3][m], c);
          }
        }
        if (u == 0 && ks == 0) put(sp ^ 1);
        if (u == 0 && ks == 6) fin(sp ^ 1, sg - 1);
        SCHED();
      }
    }
  }
  put(1);
  LBAR();
  get();
  fin(1, NSEG - 1);
  WAIT_V(0);
  LBAR();
  if (tid == 0) __builtin_amdgcn_raw_buffer_store_b32(NSEG - 1, rsy, fkq(h, which), 0, 16);
}

enum { J_NONE = 0, J_GU = 3, J_PR = 5, J_CB = 8, J_DN = 14 };
struct BigJob { int type, panel, idx; };
struct BigPtr { const char *a, *b0, *b1; };
constexpr int G_LDA = XLD * 2, G_LDB = FF * 2;

__device__ __forceinline__ BigPtr big_ptrs(const Args& p, const BigJob& j) {
  BigPtr r;
  r.a = p.ws + WS_XN + (size_t)j.panel * 128 * G_LDA;
  r.b0 = (const char*)(p.wg + j.idx * 128); r.b1 = (const char*)(p.wu + j.idx * 128);
  return r;
}
__device__ __forceinline__ void big_publish(const Args& p, const BigJob& j) {
  int tid = threadIdx.x;
  asm volatile("" : "+v"(tid));
  if (j.type == J_GU && tid == 0)
    __hip_atomic_fetch_add((gu32*)(p.ws + WS_SY + SY_CH) + j.panel * 16, 1u, __ATOMIC_RELAXED, __HIP_MEMORY_SCOPE_AGENT);
}
typedef f32x4 Acc[2][2][4][2];
__device__ __forceinline__ u32x4 pack32(f32x4 a, f32x4 b) {
  return widen32(cvtpk(a[0], a[1]), cvtpk(a[2], a[3]), cvtpk(b[0], b[1]), cvtpk(b[2], b[3]));
}
#define st_epi(r, o, v) st_sc1(r, o, v)
__device__ __forceinline__ void big_epilogue(const Args& p, const BigJob& j, Acc& acc, char* smem) {
  int tid = threadIdx.x;
  asm volatile("" : "+v"(tid));
  const int lane = tid & 63;
  const int wid = __builtin_amdgcn_readfirstlane(tid >> 6);
  const int wr = wid >> 2, wc = wid & 3;
  {
    const __amdgpu_buffer_rsrc_t ro = mkrsrc(p.ws + WS_HF, S_LEN * 16384);
#pragma unroll
    for (int i = 0; i < 8; ++i) {
      const int row = wr * 64 + (i & 3) * 16 + (lane & 15);
      f32x4 hh[2];
#pragma unroll
      for (int n = 0; n < 2; ++n)
#pragma unroll
        for (int jj = 0; jj < 4; ++jj) {
          const float g = acc[i >> 2][0][i & 3][n][jj], u = acc[i >> 2][1][i & 3][n][jj];
          hh[n][jj] = __builtin_amdgcn_rcpf(1.0f + __builtin_amdgcn_exp2f(g * -1.4426950408889634f)) * u;
        }
      st_epi(ro, h_off(p, j.panel + (i >> 2)) + row * HLD + (j.idx * 128 + wc * 32 + (lane >> 4) * 8) * 2, pack32(hh[0], hh[1]));
    }
  }
}
#define JST(n, i, v) do {} while (0)
constexpr int JOBW = 115;
__constant__ unsigned short JOBS[115] = {
  5120, 5120, 5120, 5120, 5120, 5120, 5120, 5120, 5120, 5120, 5120, 5120, 5120, 5120, 5120, 5120,
  5120, 5120, 5120, 5120, 5120, 5120, 5120, 5120, 3296, 3297, 3298, 3299, 3300, 3301, 3302, 3264,
  3265, 3266, 3267, 3268, 3269, 3270, 3232, 3233, 3234, 3235, 3236, 3237, 3238, 3200, 3201, 3202,
  3203, 3204, 3205, 3206, 3168, 3169, 3170, 3171, 3172, 3173, 3174, 3136, 3137, 3138, 3139, 3140,
  3141, 3142, 3104, 3105, 3106, 3107, 3108, 3109, 3110, 3072, 3073, 3074, 3075, 3076, 3077, 3078,
  14384, 14385, 14386, 14387, 14388, 14389, 14390, 14391, 14336, 14337, 14338, 14339, 14340, 14341, 14342, 14343,
  14352, 14353, 14354, 14355, 14356, 14357, 14358, 14359, 14368, 14369, 14370, 14371, 14372, 14373, 14374, 14375,
  8384, 8400, 0,
};
__device__ __forceinline__ void gemm_role(Args& p, int g, char* smem) {
  const __amdgpu_buffer_rsrc_t rsy = mkrsrc(p.ws + WS_SY, SY_TOTAL);
  const int x = g & 3, k = g >> 2;
  gu32* queue = (gu32*)(p.ws + WS_SY + SY_Q) + x * 32;
  volatile unsigned* slot = reinterpret_cast<volatile unsigned*>(smem + 163824);
  unsigned nxt = 0;
  auto request = [&]() {
    if (threadIdx.x == 0) nxt = __hip_atomic_fetch_add(queue, 1u, __ATOMIC_RELAXED, __HIP_MEMORY_SCOPE_AGENT) + 1u + NGW;
  };
  auto post = [&]() { if (threadIdx.x == 0) *slot = nxt; };
  auto decode = [&](int j) {
    const unsigned e = JOBS[min(j, JOBW - 1)];
    const int i = e & 15;
    BigJob b;
    b.type = e >> 10; b.panel = (e >> 4) & 63;
    b.idx = x + 4 * i;
    if (b.type == J_DN) { b.idx = i; b.panel = 4 * b.panel + x; }
    return b;
  };
  int nj = 0; (void)nj;
  auto claim = [&]() {
    request();
    LBAR();
    post();
    LBAR();
    return decode((int)__builtin_amdgcn_readfirstlane(*slot));
  };
  if (g < 4 && threadIdx.x == 0) { LST(480 + 4 * g); LSTC(480 + 4 * g + 1); }
  BigJob cur;
  if (k < 24) {
    JST(nj, 0, J_PR); JST(nj, 1, __builtin_amdgcn_s_memrealtime());
    if (k >= 16) job_proj<true>(p, g, 2, x * 8 + (k & 7), smem, rsy);
    else job_proj<false>(p, g, k >> 3, x * 8 + (k & 7), smem, rsy);
    JST(nj, 3, __builtin_amdgcn_s_memrealtime()); ++nj;
    if (threadIdx.x == 0) __builtin_amdgcn_fence(__ATOMIC_ACQUIRE, "agent");
    cur = claim();
  } else {
    const u32x2 tag = start_rows<0>(p, g, []() {});
    WAIT_V(0);
    LBAR();
    report_rows(p, g, tag);
    if (threadIdx.x < 64) {
      Boot bt;
      boot_ask(p.ws, bt);
      WAIT_V(0);
      *slot = boot_done(p.ws, bt, true, tag);
      __builtin_amdgcn_fence(__ATOMIC_ACQUIRE, "agent");
      WAIT_V(0);
    }
    LBAR();
    p.s0 = *slot >> 31;
    RDY(128 + g);
    cur = decode(k);
  }
  p.s0 = uni(p.s0);
  while (cur.type != J_NONE) {
    JST(nj, 0, cur.type | cur.panel << 8); JST(nj, 1, __builtin_amdgcn_s_memrealtime());
    if (cur.type != J_GU) {
      if (cur.type == J_DN) job_down(p, cur.panel, cur.idx, smem, rsy);
      else job_comb(p, cur.panel, x, rsy);
      JST(nj, 3, __builtin_amdgcn_s_memrealtime()); ++nj;
      cur = claim();
      continue;
    }
    int tid = threadIdx.x;
    asm volatile("" : "+v"(tid));
    const int lane = tid & 63;
    const int wid = __builtin_amdgcn_readfirstlane(tid >> 6);
    const int wr = wid >> 2, wc = wid & 3;
    const unsigned ra = tid >> 3, ca = 16 * ((tid & 7) ^ ((tid >> 3) & 7)), rb = tid >> 4, cb = 16 * ((tid & 15) ^ swzB((tid >> 4) & 15));
    const unsigned lw = lds_off(smem) + wid * 1024;
#define B8_SA(b, h) (((b) * 2 + (h)) * 16384)
#define B8_SB(b, h) ((4 + (b) * 2 + (h)) * 16384)
#define B8_STA(b, h, P, kt) glds2((P).a + (size_t)(h) * 128 * G_LDA + (kt) * 128, 64 * G_LDA, ra * G_LDA + ca, lw + B8_SA(b, h))
#define B8_STB(b, h, P, kt) glds2(((h) ? (P).b1 : (P).b0) + (size_t)(kt) * 64 * G_LDB, 32 * G_LDB, rb * G_LDB + cb, lw + B8_SB(b, h))
#define B8_LDA(b, h) do { _Pragma("unroll") for (int m = 0; m < 4; ++m) _Pragma("unroll") for (int kk = 0; kk < 2; ++kk) \
      At[m][kk] = ldsf(smem + B8_SA(b, h) + offA(wr * 64 + m * 16 + (lane & 15), kk * 4 + (lane >> 4))); } while (0)
#define B8_LDB(dst, b, h) do { _Pragma("unroll") for (int n = 0; n < 2; ++n) _Pragma("unroll") for (int kk = 0; kk < 2; ++kk) \
      dst[n][kk] = ld_bfrag(smem + B8_SB(b, h), kk, wc * 2 + n, lane); } while (0)
#define B8_MMA(ai, bj, Bt) do { __builtin_amdgcn_s_setprio(1); _Pragma("unroll") for (int m = 0; m < 4; ++m) _Pragma("unroll") for (int n = 0; n < 2; ++n) \
      _Pragma("unroll") for (int kk = 0; kk < 2; ++kk) acc[ai][bj][m][n] = MFMA(Bt[n][kk], At[m][kk], acc[ai][bj][m][n]); __builtin_amdgcn_s_setprio(0); } while (0)
#define WAIT_L(n) asm volatile("s_waitcnt lgkmcnt(%0)" :: "n"(n) : "memory")
#define BAR() __builtin_amdgcn_s_barrier()
#define WAIT_RX(c) asm volatile("s_waitcnt vmcnt(16)\n\ts_cmp_lg_u32 %0, 0\n\ts_cbranch_scc1 .Lrx%=\n\ts_waitcnt vmcnt(8)\n.Lrx%=:" :: "s"(c) : "memory", "scc")
    Acc acc;
    bf16x8 At[4][2], B0[2][2], B1[2][2];
    BigPtr cp = big_ptrs(p, cur);
    BigJob pend; pend.type = J_NONE; pend.panel = 0; pend.idx = 0;
    LBAR();
    B8_STB(0, 0, cp, 0); B8_STB(0, 1, cp, 0); B8_STA(0, 0, cp, 0); B8_STA(0, 1, cp, 0);
    if (wr == 1) BAR();
    WAIT_V(2); BAR();
    B8_STB(1, 0, cp, 1); B8_STA(1, 0, cp, 1); B8_STB(1, 1, cp, 1);
    WAIT_V(6); BAR();
    BigJob nx = pend;
    for (;;) {
      bool chain = false;
      BigPtr np = cp;
      constexpr int nt = DM / 64;
#pragma unroll
      for (int i = 0; i < 32; ++i) acc[i >> 4][(i >> 3) & 1][(i >> 1) & 3][i & 1] = (f32x4){0.f, 0.f, 0.f, 0.f};
#pragma unroll 1
      for (int t = 0; t < nt; t += 2) {
        const bool last = t == nt - 2;
        if (t == nt - 6) request();
        if (t == nt - 4) post();
        if (last) {
          nx = decode((int)__builtin_amdgcn_readfirstlane(*slot));
          chain = nx.type == J_GU;
          if (chain) np = big_ptrs(p, nx);
        }
        const BigPtr sp = np;
        const int t2 = last ? 0 : t + 2;
        if (t == 2) big_publish(p, pend);
        const int rx = __builtin_amdgcn_readfirstlane(t == 0 && pend.type != J_NONE);
        B8_LDB(B0, 0, 0); B8_LDB(B1, 0, 1); SCHED(); B8_LDA(0, 0); B8_STA(1, 1, cp, t + 1);
        WAIT_RX(rx);
        WAIT_L(0); BAR(); B8_MMA(0, 0, B0); B8_MMA(0, 1, B1); BAR(); SCHED();
        B8_LDA(0, 1); B8_STB(0, 0, sp, t2); B8_STB(0, 1, sp, t2); B8_STA(0, 0, sp, t2);
        WAIT_RX(rx);
        WAIT_L(0); BAR(); B8_MMA(1, 0, B0); B8_MMA(1, 1, B1); BAR(); SCHED();
        B8_LDB(B0, 1, 0); B8_LDB(B1, 1, 1); SCHED(); B8_LDA(1, 0); B8_STA(0, 1, sp, t2);
        WAIT_V(8); WAIT_L(0); BAR(); B8_MMA(0, 0, B0); B8_MMA(0, 1, B1); BAR(); SCHED();
        B8_LDA(1, 1); B8_STB(1, 0, sp, t2 + 1); B8_STB(1, 1, sp, t2 + 1); B8_STA(1, 0, sp, t2 + 1);
        WAIT_V(8); WAIT_L(0); BAR(); B8_MMA(1, 0, B0); B8_MMA(1, 1, B1); BAR(); SCHED();
      }
      if (wr == 0) BAR();
      JST(nj, 2, __builtin_amdgcn_s_memrealtime());
      big_epilogue(p, cur, acc, smem);
      if (!chain) break;
      JST(nj, 3, __builtin_amdgcn_s_memrealtime()); ++nj;
      pend = cur; cur = nx; cp = np;
      if (wr == 1) BAR();
      JST(nj, 0, cur.type | cur.panel << 8); JST(nj, 1, __builtin_amdgcn_s_memrealtime());
    }
    WAIT_V(0);
    BAR();
    big_publish(p, cur);
    JST(nj, 3, __builtin_amdgcn_s_memrealtime()); ++nj;
    cur = nx;
  }
  if (threadIdx.x == 0) { LST(128 + g); }
  if (g < 4 && threadIdx.x == 0) { LST(480 + 4 * g + 2); LSTC(480 + 4 * g + 3); }
}

__device__ __forceinline__ unsigned now_q() { return (unsigned)__builtin_amdgcn_s_memrealtime() * 4u; }
__device__ __forceinline__ void pace_until(unsigned t) {
#pragma unroll 1
  for (int i = 0; i < 256; ++i) {
    const int d = (int)(t - now_q());
    if (d <= 0) break;
    if (d > 32) __builtin_amdgcn_s_sleep(1);
  }
}
#define CLB_E "s76", "s77", "s78", "s79", "s80", "s81", "s82", "s83", "s84", "s85", "s86", "s87", "s88", "s89"
#define EDGE_ASM \
  "s_memtime s[76:77]\n\t" \
  "s_memrealtime s[78:79]\n\t" \
  "s_nop 3\n\t" \
  "s_memrealtime s[80:81]\n\t" \
  "s_nop 3\n\t" \
  "s_memrealtime s[82:83]\n\t" \
  "s_nop 3\n\t" \
  "s_memrealtime s[84:85]\n\t" \
  "s_nop 3\n\t" \
  "s_memrealtime s[86:87]\n\t" \
  "s_nop 3\n\t" \
  "s_memrealtime s[88:89]\n\t" \
  "s_mov_b32 s93, 0\n\t" \
  "s_waitcnt lgkmcnt(0)\n\t" \
  "s_cmp_eq_u32 s80, s78\n\t" \
  "s_addc_u32 s93, s93, 0\n\t" \
  "s_cmp_eq_u32 s82, s78\n\t" \
  "s_addc_u32 s93, s93, 0\n\t" \
  "s_cmp_eq_u32 s84, s78\n\t" \
  "s_addc_u32 s93, s93, 0\n\t" \
  "s_cmp_eq_u32 s86, s78\n\t" \
  "s_addc_u32 s93, s93, 0\n\t" \
  "s_cmp_eq_u32 s88, s78\n\t" \
  "s_addc_u32 s93, s93, 0\n\t" \
  "s_mul_i32 s93, s93, 21\n\t" \
  "s_add_u32 s76, s76, 15\n\t" \
  "s_add_u32 %[kc], s76, s93\n\t" \
  "s_lshl2_add_u32 %[kq], s78, 16\n\t"
__device__ __forceinline__ unsigned time_of(unsigned cyc) {
  unsigned c, q;
  asm volatile(EDGE_ASM : [kc] "=&s"(c), [kq] "=&s"(q) :: CLB_E, "s93", "scc");
  return q - (((c - cyc) * 11900u) >> 16);
}
#define DS_RED 58
#define DS_MEM 38
#define LEAD_DN 2
#define LEAD_UP 12
__device__ __forceinline__ u32x2 ld2_sc1(__amdgpu_buffer_rsrc_t rs, int off, int soff = 0) {
  return __builtin_amdgcn_raw_buffer_load_b64(rs, off, soff, 16);
}
__device__ __forceinline__ void st2_sc1(__amdgpu_buffer_rsrc_t rs, int off, u32x2 v) {
  __builtin_amdgcn_raw_buffer_store_b64(v, rs, off, 0, 16);
}
__device__ __forceinline__ int row_max(int v) {
  v = max(v, __builtin_amdgcn_update_dpp(0, v, 0x128, 0xf, 0xf, false));
  v = max(v, __builtin_amdgcn_update_dpp(0, v, 0x124, 0xf, 0xf, false));
  v = max(v, __builtin_amdgcn_update_dpp(0, v, 0x122, 0xf, 0xf, false));
  v = max(v, __builtin_amdgcn_update_dpp(0, v, 0x121, 0xf, 0xf, false));
  return v;
}
__device__ __forceinline__ int wave_max(int v) {
  v = row_max(v);
  return max(max(__builtin_amdgcn_readlane(v, 0), __builtin_amdgcn_readlane(v, 16)),
             max(__builtin_amdgcn_readlane(v, 32), __builtin_amdgcn_readlane(v, 48)));
}
__device__ __forceinline__ int track(int est, int x, int up, int dn, int out) {
  const int d = x - est;
  return est + (d > out ? dn : max(min(d, up), -dn));
}
#define WSL 12
#define PLAN_SLEEP 2
__device__ __forceinline__ unsigned latest(unsigned t) {
  const unsigned t0 = __builtin_amdgcn_readfirstlane(t);
  return t0 + wave_max((int)(t - t0));
}
__device__ __forceinline__ void watch(__amdgpu_buffer_rsrc_t rsy, int s, int lane, unsigned t0, const unsigned* tgt_, gu32* abortw, int hs) {
  pace_until(t0);
  u32x2 ra;
  for (unsigned spins = 0;;) {
    asm volatile("" ::: "memory");
    ra = ld2_sc1(rsy, lane * 128, SY_BR + hs);
    const bool all = __all((int)ra[0] >= s);
    SMP(4, s, spins);
    if (all) break;
    if (spins < 3) __builtin_amdgcn_s_sleep(PLAN_SLEEP); else __builtin_amdgcn_s_sleep(WSL);
    if (spin_fail(spins, abortw)) break;
  }
  const unsigned t = latest(ra[0] == (unsigned)s ? ra[1] : t0) + DS_MEM;
  lds_vu_ptr tgt = lds_at(tgt_);
  if (lane == 0) { tgt[1] = t; tgt[0] = s + 1; }
  SMP(0, s, (int)(t - t0)); SMP(1, s, (int)(t - now_q()));
}
__device__ __forceinline__ unsigned ld_flag(__amdgpu_buffer_rsrc_t rs, int off) {
  return __builtin_amdgcn_raw_buffer_load_b32(rs, off, 0, 16);
}

#define QK_ASM_0 \
  "s_waitcnt lgkmcnt(0)\n\t" \
  "ds_read_b128 v[216:219], v250 offset:0\n\t" \
  "ds_read_b128 v[220:223], v250 offset:2048\n\t" \
  "ds_read_b128 v[224:227], v250 offset:1024\n\t" \
  "ds_read_b128 v[228:231], v250 offset:3072\n\t" \
  "v_mov_b64 v[192:193], 0\n\t" \
  "v_mov_b64 v[194:195], 0\n\t" \
  "v_mov_b64 v[196:197], 0\n\t" \
  "v_mov_b64 v[198:199], 0\n\t" \
  "s_memrealtime s[90:91]\n\t" \
  "s_waitcnt lgkmcnt(0)\n\t" \
  "s_lshl_b32 s93, s90, 2\n\t" \
  "s_sub_i32 s93, %[tgt], s93\n\t" \
  "s_cmp_le_i32 s93, 0\n\t" \
  "s_cbranch_scc1 .Lgo%=\n\t" \
  "s_min_i32 s93, s93, 8000\n\t" \
  "s_add_u32 s93, s93, 3\n\t" \
  "s_lshr_b32 s93, s93, 2\n\t" \
  "s_add_u32 s93, s93, s90\n\t" \
  "s_memrealtime s[90:91]\n\t" \
  ".Lpace%=:\n\t" \
  "s_memrealtime s[94:95]\n\t" \
  "s_waitcnt lgkmcnt(1)\n\t" \
  "s_sub_i32 s91, s90, s93\n\t" \
  "s_cmp_ge_i32 s91, 0\n\t" \
  "s_cbranch_scc1 .Lgo%=\n\t" \
  "s_memrealtime s[90:91]\n\t" \
  "s_waitcnt lgkmcnt(1)\n\t" \
  "s_sub_i32 s95, s94, s93\n\t" \
  "s_cmp_lt_i32 s95, 0\n\t" \
  "s_cbranch_scc1 .Lpace%=\n\t" \
  ".Lgo%=:\n\t" \
  "s_mov_b32 s92, 0\n\t" \
  ".Lpoll%=:\n\t" \
  "buffer_load_dwordx4 v[200:203], v248, %[rs], %[som] offen sc1\n\t" \
  "buffer_load_dwordx4 v[204:207], v248, %[rs], %[som] offen offset:1024 sc1\n\t" \
  "s_waitcnt vmcnt(1) lgkmcnt(0)\n\t" \
  "ds_write_b128 v249, v[200:203]\n\t" \
  "v_max3_u32 v208, v200, v201, v202\n\t" \
  "s_waitcnt vmcnt(0)\n\t" \
  "ds_write_b128 v249, v[204:207] offset:1024\n\t" \
  "v_max3_u32 v209, v204, v205, v206\n\t" \
  "v_max3_u32 v208, v208, v203, v207\n\t" \
  "v_max_u32 v208, v208, v209\n\t" \
  "v_cmp_le_u32 vcc, %[sent], v208\n\t" \
  "s_cbranch_vccnz .Lslow%=\n\t" \
  ".Lok%=:\n\t" \
  "v_smfmac_f32_16x16x64_bf16 v[192:195], v[200:203], v[0:7], v251\n\t" \
  "v_smfmac_f32_16x16x64_bf16 v[196:199], v[200:203], v[8:15], v251\n\t" \
  "s_waitcnt lgkmcnt(0)\n\t" \
  "s_barrier\n\t" \
  "ds_read_b128 v[208:211], v248 offset:2048\n\t" \
  "ds_read_b128 v[212:215], v248 offset:3072\n\t" \
  "ds_read_b128 v[236:239], v248 offset:4096\n\t" \
  "ds_read_b128 v[240:243], v248 offset:5120\n\t" \
  "ds_read_b128 v[244:247], v248 offset:6144\n\t" \
  "ds_read_b128 v[200:203], v248 offset:7168\n\t" \
  "v_smfmac_f32_16x16x64_bf16 v[192:195], v[204:207], v[16:23], v251\n\t" \
  "v_smfmac_f32_16x16x64_bf16 v[196:199], v[204:207], v[24:31], v251\n\t" \
  "s_waitcnt lgkmcnt(5)\n\t" \
  "v_smfmac_f32_16x16x64_bf16 v[192:195], v[208:211], v[32:39], v251\n\t" \
  "ds_read_b128 v[204:207], v248 offset:8192\n\t" \
  "v_smfmac_f32_16x16x64_bf16 v[196:199], v[208:211], v[40:47], v251\n\t" \
  "s_waitcnt lgkmcnt(5)\n\t" \
  "v_smfmac_f32_16x16x64_bf16 v[192:195], v[212:215], v[216:223], v251\n\t" \
  "ds_read_b128 v[216:219], v250 offset:4096\n\t" \
  "v_smfmac_f32_16x16x64_bf16 v[196:199], v[212:215], v[224:231], v251\n\t" \
  "s_waitcnt lgkmcnt(5)\n\t" \
  "ds_read_b128 v[220:223], v250 offset:6144\n\t" \
  "v_smfmac_f32_16x16x64_bf16 v[192:195], v[236:239], v[48:55], v251\n\t" \
  "ds_read_b128 v[224:227], v250 offset:5120\n\t" \
  "v_smfmac_f32_16x16x64_bf16 v[196:199], v[236:239], v[56:63], v251\n\t" \
  "s_waitcnt lgkmcnt(6)\n\t" \
  "ds_read_b128 v[228:231], v250 offset:7168\n\t" \
  "v_smfmac_f32_16x16x64_bf16 v[192:195], v[240:243], v[64:71], v251\n\t" \
  "ds_read_b128 v[208:211], v248 offset:9216\n\t" \
  "v_smfmac_f32_16x16x64_bf16 v[196:199], v[240:243], v[72:79], v251\n\t" \
  "s_waitcnt lgkmcnt(5)\n\t" \
  "ds_read_b128 v[212:215], v248 offset:10240\n\t" \
  "v_smfmac_f32_16x16x64_bf16 v[192:195], v[244:247], v[80:87], v251\n\t" \
  "ds_read_b128 v[236:239], v248 offset:11264\n\t" \
  "v_smfmac_f32_16x16x64_bf16 v[196:199], v[244:247], v[88:95], v251\n\t" \
  "s_waitcnt lgkmcnt(5)\n\t" \
  "v_smfmac_f32_16x16x64_bf16 v[192:195], v[200:203], v[216:223], v251\n\t" \
  "s_waitcnt lgkmcnt(3)\n\t" \
  "ds_read_b128 v[216:219], v250 offset:8192\n\t" \
  "v_smfmac_f32_16x16x64_bf16 v[196:199], v[200:203], v[224:231], v251\n\t" \
  "ds_read_b128 v[220:223], v250 offset:10240\n\t" \
  "v_smfmac_f32_16x16x64_bf16 v[192:195], v[204:207], v[96:103], v251\n\t" \
  "ds_read_b128 v[224:227], v250 offset:9216\n\t" \
  "v_smfmac_f32_16x16x64_bf16 v[196:199], v[204:207], v[104:111], v251\n\t" \
  "s_waitcnt lgkmcnt(5)\n\t" \
  "ds_read_b128 v[228:231], v250 offset:11264\n\t" \
  "v_smfmac_f32_16x16x64_bf16 v[192:195], v[208:211], v[112:119], v251\n\t" \
  "ds_read_b128 v[240:243], v248 offset:12288\n\t" \
  "v_smfmac_f32_16x16x64_bf16 v[196:199], v[208:211], v[120:127], v251\n\t" \
  "s_waitcnt lgkmcnt(5)\n\t" \
  "ds_read_b128 v[244:247], v248 offset:13312\n\t" \
  "v_smfmac_f32_16x16x64_bf16 v[192:195], v[212:215], v[128:135], v251\n\t" \
  "ds_read_b128 v[200:203], v248 offset:14336\n\t" \
  "v_smfmac_f32_16x16x64_bf16 v[196:199], v[212:215], v[136:143], v251\n\t" \
  "s_waitcnt lgkmcnt(5)\n\t" \
  "v_smfmac_f32_16x16x64_bf16 v[192:195], v[236:239], v[216:223], v251\n\t" \
  "s_waitcnt lgkmcnt(3)\n\t" \
  "ds_read_b128 v[204:207], v248 offset:15360\n\t" \
  "v_smfmac_f32_16x16x64_bf16 v[196:199], v[236:239], v[224:231], v251\n\t" \
  "s_waitcnt lgkmcnt(3)\n\t" \
  "ds_read_b128 v[216:219], v250 offset:12288\n\t" \
  "v_smfmac_f32_16x16x64_bf16 v[192:195], v[240:243], v[144:151], v251\n\t" \
  "ds_read_b128 v[220:223], v250 offset:14336\n\t" \
  "v_smfmac_f32_16x16x64_bf16 v[196:199], v[240:243], v[152:159], v251\n\t" \
  "s_waitcnt lgkmcnt(4)\n\t" \
  "ds_read_b128 v[224:227], v250 offset:13312\n\t" \
  "v_smfmac_f32_16x16x64_bf16 v[192:195], v[244:247], v[160:167], v251\n\t" \
  "ds_read_b128 v[228:231], v250 offset:15360\n\t" \
  "v_smfmac_f32_16x16x64_bf16 v[196:199], v[244:247], v[168:175], v251\n\t" \
  "s_waitcnt lgkmcnt(5)\n\t" \
  "v_smfmac_f32_16x16x64_bf16 v[192:195], v[200:203], v[176:183], v251\n\t" \
  "v_smfmac_f32_16x16x64_bf16 v[196:199], v[200:203], v[184:191], v251\n\t" \
  "s_waitcnt lgkmcnt(2)\n\t" \
  "v_smfmac_f32_16x16x64_bf16 v[192:195], v[204:207], v[216:223], v251\n\t" \
  "s_waitcnt lgkmcnt(0)\n\t" \
  "v_smfmac_f32_16x16x64_bf16 v[196:199], v[204:207], v[224:231], v251\n\t" \
  "ds_read_b128 v[200:203], v248 offset:16384\n\t" \
  "ds_read_b128 v[204:207], v248 offset:20480\n\t" \
  "v_mov_b32 v250, v232\n\t" \
  "v_mov_b32 v249, v233\n\t" \
  "s_nop 2\n\t" \
  "v_add_f32 v192, v192, v194\n\t" \
  "v_add_f32 v193, v193, v195\n\t" \
  "v_add_f32 v196, v196, v198\n\t" \
  "v_add_f32 v197, v197, v199\n\t" \
  "v_cvt_pk_bf16_f32 v192, v192, v193\n\t" \
  "v_cvt_pk_bf16_f32 v196, v196, v197\n\t" \
  "ds_write2_b32 v252, v192, v196 offset1:64\n\t" \
  "v_mov_b32 v251, v234\n\t" \
  "ds_read_b64_tr_b16 v[232:233], v250 offset:0\n\t" \
  "ds_read_b64_tr_b16 v[234:235], v250 offset:64\n\t" \
  "buffer_load_dwordx4 v[192:195], v248, %[rt], %[svt] offen offset:0 sc1\n\t" \
  "buffer_load_dwordx4 v[196:199], v248, %[rt], %[svt] offen offset:1024 sc1\n\t" \
  "s_waitcnt lgkmcnt(0)\n\t" \
  "v_mfma_f32_16x16x32_bf16 v[200:203], v[200:203], v[232:235], 0\n\t" \
  "v_mfma_f32_16x16x32_bf16 v[204:207], v[204:207], v[232:235], 0\n\t" \
  "s_nop 7\n\t" \
  "v_cndmask_b32 v216, v200, v204, %[nh]\n\t" \
  "v_cndmask_b32 v217, v201, v205, %[nh]\n\t" \
  "v_cndmask_b32 v218, v202, v206, %[nh]\n\t" \
  "v_cndmask_b32 v219, v203, v207, %[nh]\n\t" \
  "ds_write_b128 v248, v[216:219] offset:16384\n\t" \
  "s_sleep 2\n\t" \
  "buffer_load_dword v252, v249, %[rs], %[sv] offen offset:0 sc1\n\t" \
  "buffer_load_dword v253, v249, %[rs], %[sv] offen offset:256 sc1\n\t" \
  "s_setprio 3\n\t" \
  "s_waitcnt lgkmcnt(0)\n\t" \
  "s_barrier\n\t" \
  "ds_read_b64_tr_b16 v[200:201], v250 offset:2048\n\t" \
  "ds_read_b64_tr_b16 v[202:203], v250 offset:2112\n\t" \
  "ds_read_b64_tr_b16 v[236:237], v250 offset:512\n\t" \
  "ds_read_b64_tr_b16 v[238:239], v250 offset:576\n\t" \
  "ds_read_b64_tr_b16 v[204:205], v250 offset:2560\n\t" \
  "ds_read_b64_tr_b16 v[206:207], v250 offset:2624\n\t" \
  "ds_read_b64_tr_b16 v[240:241], v250 offset:1024\n\t" \
  "ds_read_b64_tr_b16 v[242:243], v250 offset:1088\n\t" \
  "ds_read_b64_tr_b16 v[208:209], v250 offset:3072\n\t" \
  "ds_read_b64_tr_b16 v[210:211], v250 offset:3136\n\t" \
  "ds_read_b64_tr_b16 v[244:245], v250 offset:1536\n\t" \
  "ds_read_b64_tr_b16 v[246:247], v250 offset:1600\n\t" \
  "ds_read_b64_tr_b16 v[212:213], v250 offset:3584\n\t" \
  "ds_read_b64_tr_b16 v[214:215], v250 offset:3648\n\t" \
  "ds_read_b128 v[220:223], v248 offset:17408\n\t" \
  "s_waitcnt lgkmcnt(13)\n\t" \
  "v_mfma_f32_16x16x32_bf16 v[200:203], v[200:203], v[232:235], 0\n\t" \
  "ds_read_b128 v[224:227], v248 offset:18432\n\t" \
  "ds_read_b128 v[228:231], v248 offset:19456\n\t" \
  "s_waitcnt lgkmcnt(11)\n\t" \
  "v_mfma_f32_16x16x32_bf16 v[200:203], v[204:207], v[236:239], v[200:203]\n\t" \
  "v_mov_b32 v233, 0\n\t" \
  "v_mov_b32 v234, 0\n\t" \
  "s_waitcnt lgkmcnt(7)\n\t" \
  "v_mfma_f32_16x16x32_bf16 v[200:203], v[208:211], v[240:243], v[200:203]\n\t" \
  "s_waitcnt lgkmcnt(3)\n\t" \
  "v_mfma_f32_16x16x32_bf16 v[200:203], v[212:215], v[244:247], v[200:203]\n\t" \
  "s_waitcnt lgkmcnt(2)\n\t" \
  "v_pk_add_f32 v[216:217], v[216:217], v[220:221]\n\t" \
  "v_pk_add_f32 v[218:219], v[218:219], v[222:223]\n\t" \
  "s_waitcnt lgkmcnt(0)\n\t" \
  "v_pk_add_f32 v[224:225], v[224:225], v[228:229]\n\t" \
  "v_pk_add_f32 v[226:227], v[226:227], v[230:231]\n\t" \
  "v_pk_add_f32 v[216:217], v[216:217], v[224:225]\n\t" \
  "v_pk_add_f32 v[218:219], v[218:219], v[226:227]\n\t" \
  "v_max3_f32 v204, v216, v217, v218\n\t" \
  "v_cndmask_b32 v200, v200, v202, %[up]\n\t" \
  "v_cndmask_b32 v201, v201, v203, %[up]\n\t" \
  "v_max_f32 v204, v204, v219\n\t" \
  "v_max3_f32 v204, v204, v200, v201\n\t" \
  "v_mov_b32 v220, 0x3f803f80\n\t" \
  "v_mov_b32 v221, 0x3f803f80\n\t" \
  "v_max_f32_dpp v205, v204, v204 row_ror:8 row_mask:0xf bank_mask:0xf\n\t" \
  "v_max_f32_dpp v206, v204, v204 row_ror:8 row_mask:0xf bank_mask:0xf\n\t" \
  "v_mov_b64 v[222:223], v[220:221]\n\t" \
  "s_nop 0\n\t" \
  "v_permlane16_swap_b32 v205, v206\n\t" \
  "v_max_f32 v204, v205, v206\n\t" \
  "v_max_f32 v207, v205, v206\n\t" \
  "s_nop 1\n\t" \
  "v_permlane32_swap_b32 v204, v207\n\t" \
  "v_max_f32 v204, v204, v207\n\t" \
  "v_mul_f32 v204, %[negc], v204\n\t" \
  "v_fma_f32 v200, v200, %[c], v204\n\t" \
  "v_fma_f32 v201, v201, %[c], v204\n\t" \
  "v_exp_f32 v200, v200\n\t" \
  "v_fma_f32 v216, v216, %[c], v204\n\t" \
  "v_exp_f32 v201, v201\n\t" \
  "v_fma_f32 v217, v217, %[c], v204\n\t" \
  "v_exp_f32 v216, v216\n\t" \
  "v_fma_f32 v218, v218, %[c], v204\n\t" \
  "v_cvt_pk_bf16_f32 v232, v200, v201\n\t" \
  "v_cvt_pk_bf16_f32 v235, v200, v201\n\t" \
  "v_exp_f32 v217, v217\n\t" \
  "v_fma_f32 v219, v219, %[c], v204\n\t" \
  "v_mfma_f32_16x16x16_bf16 v[212:215], v[220:221], v[232:233], 0\n\t" \
  "v_exp_f32 v218, v218\n\t" \
  "s_waitcnt vmcnt(0)\n\t" \
  "v_mfma_f32_16x16x16_bf16 v[224:227], v[252:253], v[232:233], 0\n\t" \
  "v_cvt_pk_bf16_f32 v210, v216, v217\n\t" \
  "v_cvt_pk_bf16_f32 v208, v216, v217\n\t" \
  "v_exp_f32 v219, v219\n\t" \
  "v_mfma_f32_16x16x16_bf16 v[228:231], v[252:253], v[234:235], 0\n\t" \
  "v_cvt_pk_bf16_f32 v211, v218, v219\n\t" \
  "v_cvt_pk_bf16_f32 v209, v218, v219\n\t" \
  "v_mov_b32_dpp v208, v210 row_ror:8 row_mask:0xf bank_mask:0xc\n\t" \
  "v_mov_b32_dpp v210, v210 row_ror:8 row_mask:0xf bank_mask:0x3\n\t" \
  "v_mov_b32_dpp v209, v211 row_ror:8 row_mask:0xf bank_mask:0xc\n\t" \
  "v_mov_b32_dpp v211, v211 row_ror:8 row_mask:0xf bank_mask:0x3\n\t" \
  "v_max_u32 v204, v252, v253\n\t" \
  "v_cmp_le_u32 vcc, %[sent], v204\n\t" \
  "v_mfma_f32_16x16x32_bf16 v[212:215], v[220:223], v[208:211], v[212:215]\n\t" \
  ".Lpv%=:\n\t" \
  "v_mfma_f32_16x16x32_bf16 v[224:227], v[192:195], v[208:211], v[224:227]\n\t" \
  "v_mfma_f32_16x16x32_bf16 v[228:231], v[196:199], v[208:211], v[228:231]\n\t" \
  "s_cbranch_vccnz .Lvs%=\n\t" \
  ".Lvok%=:\n\t" \
  "s_nop 4\n\t" \
  "v_rcp_f32 v204, v212\n\t" \
  "v_cndmask_b32 v224, v224, v226, %[nh]\n\t" \
  "v_cndmask_b32 v225, v225, v227, %[nh]\n\t" \
  "v_mul_f32 v224, v224, v204\n\t" \
  "v_mul_f32 v225, v225, v204\n\t" \
  "v_cvt_pk_bf16_f32 v224, v224, v225\n\t" \
  "buffer_store_dword v224, v251, %[rs], %[so] offen offset:0 sc1\n\t" \
  "v_cndmask_b32 v228, v228, v230, %[nh]\n\t" \
  "v_cndmask_b32 v229, v229, v231, %[nh]\n\t" \
  "v_mul_f32 v228, v228, v204\n\t" \
  "v_mul_f32 v229, v229, v204\n\t" \
  "v_cvt_pk_bf16_f32 v228, v228, v229\n\t" \
  "buffer_store_dword v228, v251, %[rs], %[so] offen offset:256 sc1\n\t" \
  "s_memtime s[90:91]\n\t" \
  "s_setprio 0\n\t" \
  "s_waitcnt lgkmcnt(0)\n\t" \
  "s_mov_b32 %[tout], s90\n\t" \
  "s_mov_b32 %[spins], s92\n\t" \
  "s_branch .Lend%=\n\t" \
  ".Lvs%=:\n\t" \
  "v_cmp_eq_u32 vcc, %[sent], v252\n\t" \
  "v_cmp_eq_u32 s[94:95], %[sent], v253\n\t" \
  "s_or_b64 s[94:95], s[94:95], vcc\n\t" \
  "s_cbranch_scc0 .Lvok%=\n\t" \
  "s_add_u32 s92, s92, 1\n\t" \
  "s_sleep 2\n\t" \
  "s_and_b32 s93, s92, 63\n\t" \
  "s_cbranch_scc1 .Lvr%=\n\t" \
  "buffer_load_dword v204, off, %[rs], %[sab] sc1\n\t" \
  "s_waitcnt vmcnt(0)\n\t" \
  "v_readfirstlane_b32 s93, v204\n\t" \
  "s_nop 3\n\t" \
  "s_cmp_eq_u32 s93, 1\n\t" \
  "s_cbranch_scc1 .Lvok%=\n\t" \
  "s_cmp_lt_u32 s92, 0x80000\n\t" \
  "s_cbranch_scc1 .Lvr%=\n\t" \
  "v_mov_b32 v204, 1\n\t" \
  "buffer_store_dword v204, off, %[rs], %[sab] sc1\n\t" \
  "s_branch .Lvok%=\n\t" \
  ".Lvr%=:\n\t" \
  "buffer_load_dword v252, v249, %[rs], %[sv] offen offset:0 sc1\n\t" \
  "buffer_load_dword v253, v249, %[rs], %[sv] offen offset:256 sc1\n\t" \
  "s_waitcnt vmcnt(0)\n\t" \
  "v_mfma_f32_16x16x16_bf16 v[224:227], v[252:253], v[232:233], 0\n\t" \
  "v_mfma_f32_16x16x16_bf16 v[228:231], v[252:253], v[234:235], 0\n\t" \
  "v_max_u32 v204, v252, v253\n\t" \
  "v_cmp_le_u32 vcc, %[sent], v204\n\t" \
  "s_nop 4\n\t" \
  "s_branch .Lpv%=\n\t" \
  "s_branch .Lend%=\n\t" \
  ".Lslow%=:\n\t" \
  "s_mov_b64 s[94:95], 0\n\t" \
  "v_cmp_eq_u32 vcc, %[sent], v200\n\t" \
  "s_or_b64 s[94:95], s[94:95], vcc\n\t" \
  "v_cmp_eq_u32 vcc, %[sent], v201\n\t" \
  "s_or_b64 s[94:95], s[94:95], vcc\n\t" \
  "v_cmp_eq_u32 vcc, %[sent], v202\n\t" \
  "s_or_b64 s[94:95], s[94:95], vcc\n\t" \
  "v_cmp_eq_u32 vcc, %[sent], v203\n\t" \
  "s_or_b64 s[94:95], s[94:95], vcc\n\t" \
  "v_cmp_eq_u32 vcc, %[sent], v204\n\t" \
  "s_or_b64 s[94:95], s[94:95], vcc\n\t" \
  "v_cmp_eq_u32 vcc, %[sent], v205\n\t" \
  "s_or_b64 s[94:95], s[94:95], vcc\n\t" \
  "v_cmp_eq_u32 vcc, %[sent], v206\n\t" \
  "s_or_b64 s[94:95], s[94:95], vcc\n\t" \
  "v_cmp_eq_u32 vcc, %[sent], v207\n\t" \
  "s_or_b64 s[94:95], s[94:95], vcc\n\t" \
  "s_cmp_eq_u64 s[94:95], 0\n\t" \
  "s_cbranch_scc1 .Lok%=\n\t" \
  "s_add_u32 s92, s92, 1\n\t" \
  "s_cmp_eq_u32 s92, 1\n\t" \
  "s_cbranch_scc1 .Lpoll%=\n\t" \
  "s_sleep 6\n\t" \
  "s_and_b32 s93, s92, 63\n\t" \
  "s_cmp_lg_u32 s93, 0\n\t" \
  "s_cbranch_scc1 .Lpoll%=\n\t" \
  "v_mov_b32 v209, 0\n\t" \
  "buffer_load_dword v208, v209, %[rs], %[sab] offen sc1\n\t" \
  "s_waitcnt vmcnt(0)\n\t" \
  "v_readfirstlane_b32 s93, v208\n\t" \
  "s_nop 3\n\t" \
  "s_cmp_eq_u32 s93, 1\n\t" \
  "s_cbranch_scc1 .Lok%=\n\t" \
  "s_cmp_lt_u32 s92, 0x80000\n\t" \
  "s_cbranch_scc1 .Lpoll%=\n\t" \
  "v_mov_b32 v208, 1\n\t" \
  "buffer_store_dword v208, v209, %[rs], %[sab] offen sc1\n\t" \
  "s_branch .Lok%=\n\t" \
  ".Lend%=:\n\t"
#define QK_ASM_1 \
  "s_waitcnt lgkmcnt(0)\n\t" \
  "ds_read_b128 v[216:219], v250 offset:0\n\t" \
  "ds_read_b128 v[220:223], v250 offset:2048\n\t" \
  "ds_read_b128 v[224:227], v250 offset:1024\n\t" \
  "ds_read_b128 v[228:231], v250 offset:3072\n\t" \
  "v_mov_b64 v[192:193], 0\n\t" \
  "v_mov_b64 v[194:195], 0\n\t" \
  "v_mov_b64 v[196:197], 0\n\t" \
  "v_mov_b64 v[198:199], 0\n\t" \
  "s_memrealtime s[90:91]\n\t" \
  "s_waitcnt lgkmcnt(0)\n\t" \
  "s_lshl_b32 s93, s90, 2\n\t" \
  "s_sub_i32 s93, %[tgt], s93\n\t" \
  "s_cmp_le_i32 s93, 0\n\t" \
  "s_cbranch_scc1 .Lgo%=\n\t" \
  "s_min_i32 s93, s93, 8000\n\t" \
  "s_add_u32 s93, s93, 3\n\t" \
  "s_lshr_b32 s93, s93, 2\n\t" \
  "s_add_u32 s93, s93, s90\n\t" \
  "s_memrealtime s[90:91]\n\t" \
  ".Lpace%=:\n\t" \
  "s_memrealtime s[94:95]\n\t" \
  "s_waitcnt lgkmcnt(1)\n\t" \
  "s_sub_i32 s91, s90, s93\n\t" \
  "s_cmp_ge_i32 s91, 0\n\t" \
  "s_cbranch_scc1 .Lgo%=\n\t" \
  "s_memrealtime s[90:91]\n\t" \
  "s_waitcnt lgkmcnt(1)\n\t" \
  "s_sub_i32 s95, s94, s93\n\t" \
  "s_cmp_lt_i32 s95, 0\n\t" \
  "s_cbranch_scc1 .Lpace%=\n\t" \
  ".Lgo%=:\n\t" \
  "s_mov_b32 s92, 0\n\t" \
  ".Lpoll%=:\n\t" \
  "buffer_load_dwordx4 v[200:203], v248, %[rs], %[som] offen sc1\n\t" \
  "buffer_load_dwordx4 v[204:207], v248, %[rs], %[som] offen offset:1024 sc1\n\t" \
  "s_waitcnt vmcnt(1) lgkmcnt(0)\n\t" \
  "ds_write_b128 v249, v[200:203]\n\t" \
  "v_max3_u32 v208, v200, v201, v202\n\t" \
  "s_waitcnt vmcnt(0)\n\t" \
  "ds_write_b128 v249, v[204:207] offset:1024\n\t" \
  "v_max3_u32 v209, v204, v205, v206\n\t" \
  "v_max3_u32 v208, v208, v203, v207\n\t" \
  "v_max_u32 v208, v208, v209\n\t" \
  "v_cmp_le_u32 vcc, %[sent], v208\n\t" \
  "s_cbranch_vccnz .Lslow%=\n\t" \
  ".Lok%=:\n\t" \
  "v_smfmac_f32_16x16x64_bf16 v[192:195], v[200:203], v[0:7], v251\n\t" \
  "v_smfmac_f32_16x16x64_bf16 v[196:199], v[200:203], v[8:15], v251\n\t" \
  "s_waitcnt lgkmcnt(0)\n\t" \
  "s_barrier\n\t" \
  "ds_read_b128 v[208:211], v248 offset:4096\n\t" \
  "ds_read_b128 v[212:215], v248 offset:5120\n\t" \
  "ds_read_b128 v[236:239], v248 offset:6144\n\t" \
  "ds_read_b128 v[240:243], v248 offset:7168\n\t" \
  "ds_read_b128 v[244:247], v248 offset:8192\n\t" \
  "ds_read_b128 v[200:203], v248 offset:9216\n\t" \
  "v_smfmac_f32_16x16x64_bf16 v[192:195], v[204:207], v[16:23], v251\n\t" \
  "v_smfmac_f32_16x16x64_bf16 v[196:199], v[204:207], v[24:31], v251\n\t" \
  "s_waitcnt lgkmcnt(5)\n\t" \
  "v_smfmac_f32_16x16x64_bf16 v[192:195], v[208:211], v[32:39], v251\n\t" \
  "ds_read_b128 v[204:207], v248 offset:10240\n\t" \
  "v_smfmac_f32_16x16x64_bf16 v[196:199], v[208:211], v[40:47], v251\n\t" \
  "s_waitcnt lgkmcnt(5)\n\t" \
  "v_smfmac_f32_16x16x64_bf16 v[192:195], v[212:215], v[216:223], v251\n\t" \
  "ds_read_b128 v[216:219], v250 offset:4096\n\t" \
  "v_smfmac_f32_16x16x64_bf16 v[196:199], v[212:215], v[224:231], v251\n\t" \
  "s_waitcnt lgkmcnt(5)\n\t" \
  "ds_read_b128 v[220:223], v250 offset:6144\n\t" \
  "v_smfmac_f32_16x16x64_bf16 v[192:195], v[236:239], v[48:55], v251\n\t" \
  "ds_read_b128 v[224:227], v250 offset:5120\n\t" \
  "v_smfmac_f32_16x16x64_bf16 v[196:199], v[236:239], v[56:63], v251\n\t" \
  "s_waitcnt lgkmcnt(6)\n\t" \
  "ds_read_b128 v[228:231], v250 offset:7168\n\t" \
  "v_smfmac_f32_16x16x64_bf16 v[192:195], v[240:243], v[64:71], v251\n\t" \
  "ds_read_b128 v[208:211], v248 offset:11264\n\t" \
  "v_smfmac_f32_16x16x64_bf16 v[196:199], v[240:243], v[72:79], v251\n\t" \
  "s_waitcnt lgkmcnt(5)\n\t" \
  "ds_read_b128 v[212:215], v248 offset:12288\n\t" \
  "v_smfmac_f32_16x16x64_bf16 v[192:195], v[244:247], v[80:87], v251\n\t" \
  "ds_read_b128 v[236:239], v248 offset:13312\n\t" \
  "v_smfmac_f32_16x16x64_bf16 v[196:199], v[244:247], v[88:95], v251\n\t" \
  "s_waitcnt lgkmcnt(5)\n\t" \
  "v_smfmac_f32_16x16x64_bf16 v[192:195], v[200:203], v[216:223], v251\n\t" \
  "s_waitcnt lgkmcnt(3)\n\t" \
  "ds_read_b128 v[216:219], v250 offset:8192\n\t" \
  "v_smfmac_f32_16x16x64_bf16 v[196:199], v[200:203], v[224:231], v251\n\t" \
  "ds_read_b128 v[220:223], v250 offset:10240\n\t" \
  "v_smfmac_f32_16x16x64_bf16 v[192:195], v[204:207], v[96:103], v251\n\t" \
  "ds_read_b128 v[224:227], v250 offset:9216\n\t" \
  "v_smfmac_f32_16x16x64_bf16 v[196:199], v[204:207], v[104:111], v251\n\t" \
  "s_waitcnt lgkmcnt(5)\n\t" \
  "ds_read_b128 v[228:231], v250 offset:11264\n\t" \
  "v_smfmac_f32_16x16x64_bf16 v[192:195], v[208:211], v[112:119], v251\n\t" \
  "ds_read_b128 v[240:243], v248 offset:14336\n\t" \
  "v_smfmac_f32_16x16x64_bf16 v[196:199], v[208:211], v[120:127], v251\n\t" \
  "s_waitcnt lgkmcnt(5)\n\t" \
  "ds_read_b128 v[244:247], v248 offset:15360\n\t" \
  "v_smfmac_f32_16x16x64_bf16 v[192:195], v[212:215], v[128:135], v251\n\t" \
  "ds_read_b128 v[200:203], v248 offset:0\n\t" \
  "v_smfmac_f32_16x16x64_bf16 v[196:199], v[212:215], v[136:143], v251\n\t" \
  "s_waitcnt lgkmcnt(5)\n\t" \
  "v_smfmac_f32_16x16x64_bf16 v[192:195], v[236:239], v[216:223], v251\n\t" \
  "s_waitcnt lgkmcnt(3)\n\t" \
  "ds_read_b128 v[204:207], v248 offset:1024\n\t" \
  "v_smfmac_f32_16x16x64_bf16 v[196:199], v[236:239], v[224:231], v251\n\t" \
  "s_waitcnt lgkmcnt(3)\n\t" \
  "ds_read_b128 v[216:219], v250 offset:12288\n\t" \
  "v_smfmac_f32_16x16x64_bf16 v[192:195], v[240:243], v[144:151], v251\n\t" \
  "ds_read_b128 v[220:223], v250 offset:14336\n\t" \
  "v_smfmac_f32_16x16x64_bf16 v[196:199], v[240:243], v[152:159], v251\n\t" \
  "s_waitcnt lgkmcnt(4)\n\t" \
  "ds_read_b128 v[224:227], v250 offset:13312\n\t" \
  "v_smfmac_f32_16x16x64_bf16 v[192:195], v[244:247], v[160:167], v251\n\t" \
  "ds_read_b128 v[228:231], v250 offset:15360\n\t" \
  "v_smfmac_f32_16x16x64_bf16 v[196:199], v[244:247], v[168:175], v251\n\t" \
  "s_waitcnt lgkmcnt(5)\n\t" \
  "v_smfmac_f32_16x16x64_bf16 v[192:195], v[200:203], v[176:183], v251\n\t" \
  "v_smfmac_f32_16x16x64_bf16 v[196:199], v[200:203], v[184:191], v251\n\t" \
  "s_waitcnt lgkmcnt(2)\n\t" \
  "v_smfmac_f32_16x16x64_bf16 v[192:195], v[204:207], v[216:223], v251\n\t" \
  "s_waitcnt lgkmcnt(0)\n\t" \
  "v_smfmac_f32_16x16x64_bf16 v[196:199], v[204:207], v[224:231], v251\n\t" \
  "ds_read_b128 v[200:203], v248 offset:17408\n\t" \
  "ds_read_b128 v[204:207], v248 offset:21504\n\t" \
  "v_mov_b32 v250, v232\n\t" \
  "v_mov_b32 v249, v233\n\t" \
  "s_nop 2\n\t" \
  "v_add_f32 v192, v192, v194\n\t" \
  "v_add_f32 v193, v193, v195\n\t" \
  "v_add_f32 v196, v196, v198\n\t" \
  "v_add_f32 v197, v197, v199\n\t" \
  "v_cvt_pk_bf16_f32 v192, v192, v193\n\t" \
  "v_cvt_pk_bf16_f32 v196, v196, v197\n\t" \
  "ds_write2_b32 v252, v192, v196 offset1:64\n\t" \
  "v_mov_b32 v251, v234\n\t" \
  "ds_read_b64_tr_b16 v[236:237], v250 offset:512\n\t" \
  "ds_read_b64_tr_b16 v[238:239], v250 offset:576\n\t" \
  "buffer_load_dwordx4 v[192:195], v248, %[rt], %[svt] offen offset:0 sc1\n\t" \
  "buffer_load_dwordx4 v[196:199], v248, %[rt], %[svt] offen offset:1024 sc1\n\t" \
  "s_waitcnt lgkmcnt(0)\n\t" \
  "v_mfma_f32_16x16x32_bf16 v[200:203], v[200:203], v[236:239], 0\n\t" \
  "v_mfma_f32_16x16x32_bf16 v[204:207], v[204:207], v[236:239], 0\n\t" \
  "s_nop 7\n\t" \
  "v_cndmask_b32 v216, v200, v204, %[nh]\n\t" \
  "v_cndmask_b32 v217, v201, v205, %[nh]\n\t" \
  "v_cndmask_b32 v218, v202, v206, %[nh]\n\t" \
  "v_cndmask_b32 v219, v203, v207, %[nh]\n\t" \
  "ds_write_b128 v248, v[216:219] offset:17408\n\t" \
  "s_sleep 2\n\t" \
  "buffer_load_dword v252, v249, %[rs], %[sv] offen offset:0 sc1\n\t" \
  "buffer_load_dword v253, v249, %[rs], %[sv] offen offset:256 sc1\n\t" \
  "s_setprio 3\n\t" \
  "s_waitcnt lgkmcnt(0)\n\t" \
  "s_barrier\n\t" \
  "ds_read_b64_tr_b16 v[204:205], v250 offset:2560\n\t" \
  "ds_read_b64_tr_b16 v[206:207], v250 offset:2624\n\t" \
  "ds_read_b64_tr_b16 v[240:241], v250 offset:1024\n\t" \
  "ds_read_b64_tr_b16 v[242:243], v250 offset:1088\n\t" \
  "ds_read_b64_tr_b16 v[208:209], v250 offset:3072\n\t" \
  "ds_read_b64_tr_b16 v[210:211], v250 offset:3136\n\t" \
  "ds_read_b64_tr_b16 v[244:245], v250 offset:1536\n\t" \
  "ds_read_b64_tr_b16 v[246:247], v250 offset:1600\n\t" \
  "ds_read_b64_tr_b16 v[212:213], v250 offset:3584\n\t" \
  "ds_read_b64_tr_b16 v[214:215], v250 offset:3648\n\t" \
  "ds_read_b64_tr_b16 v[232:233], v250 offset:0\n\t" \
  "ds_read_b64_tr_b16 v[234:235], v250 offset:64\n\t" \
  "ds_read_b64_tr_b16 v[200:201], v250 offset:2048\n\t" \
  "ds_read_b64_tr_b16 v[202:203], v250 offset:2112\n\t" \
  "ds_read_b128 v[220:223], v248 offset:18432\n\t" \
  "s_waitcnt lgkmcnt(13)\n\t" \
  "v_mfma_f32_16x16x32_bf16 v[204:207], v[204:207], v[236:239], 0\n\t" \
  "ds_read_b128 v[224:227], v248 offset:19456\n\t" \
  "ds_read_b128 v[228:231], v248 offset:16384\n\t" \
  "s_waitcnt lgkmcnt(11)\n\t" \
  "v_mfma_f32_16x16x32_bf16 v[204:207], v[208:211], v[240:243], v[204:207]\n\t" \
  "v_mov_b32 v237, 0\n\t" \
  "v_mov_b32 v238, 0\n\t" \
  "s_waitcnt lgkmcnt(7)\n\t" \
  "v_mfma_f32_16x16x32_bf16 v[204:207], v[212:215], v[244:247], v[204:207]\n\t" \
  "s_waitcnt lgkmcnt(3)\n\t" \
  "v_mfma_f32_16x16x32_bf16 v[204:207], v[200:203], v[232:235], v[204:207]\n\t" \
  "s_waitcnt lgkmcnt(2)\n\t" \
  "v_pk_add_f32 v[216:217], v[216:217], v[220:221]\n\t" \
  "v_pk_add_f32 v[218:219], v[218:219], v[222:223]\n\t" \
  "s_waitcnt lgkmcnt(0)\n\t" \
  "v_pk_add_f32 v[224:225], v[224:225], v[228:229]\n\t" \
  "v_pk_add_f32 v[226:227], v[226:227], v[230:231]\n\t" \
  "v_pk_add_f32 v[216:217], v[216:217], v[224:225]\n\t" \
  "v_pk_add_f32 v[218:219], v[218:219], v[226:227]\n\t" \
  "v_max3_f32 v208, v216, v217, v218\n\t" \
  "v_cndmask_b32 v204, v204, v206, %[up]\n\t" \
  "v_cndmask_b32 v205, v205, v207, %[up]\n\t" \
  "v_max_f32 v208, v208, v219\n\t" \
  "v_max3_f32 v208, v208, v204, v205\n\t" \
  "v_mov_b32 v220, 0x3f803f80\n\t" \
  "v_mov_b32 v221, 0x3f803f80\n\t" \
  "v_max_f32_dpp v209, v208, v208 row_ror:8 row_mask:0xf bank_mask:0xf\n\t" \
  "v_max_f32_dpp v210, v208, v208 row_ror:8 row_mask:0xf bank_mask:0xf\n\t" \
  "v_mov_b64 v[222:223], v[220:221]\n\t" \
  "s_nop 0\n\t" \
  "v_permlane16_swap_b32 v209, v210\n\t" \
  "v_max_f32 v208, v209, v210\n\t" \
  "v_max_f32 v211, v209, v210\n\t" \
  "s_nop 1\n\t" \
  "v_permlane32_swap_b32 v208, v211\n\t" \
  "v_max_f32 v208, v208, v211\n\t" \
  "v_mul_f32 v208, %[negc], v208\n\t" \
  "v_fma_f32 v204, v204, %[c], v208\n\t" \
  "v_fma_f32 v205, v205, %[c], v208\n\t" \
  "v_exp_f32 v204, v204\n\t" \
  "v_fma_f32 v216, v216, %[c], v208\n\t" \
  "v_exp_f32 v205, v205\n\t" \
  "v_fma_f32 v217, v217, %[c], v208\n\t" \
  "v_exp_f32 v216, v216\n\t" \
  "v_fma_f32 v218, v218, %[c], v208\n\t" \
  "v_cvt_pk_bf16_f32 v236, v204, v205\n\t" \
  "v_cvt_pk_bf16_f32 v239, v204, v205\n\t" \
  "v_exp_f32 v217, v217\n\t" \
  "v_fma_f32 v219, v219, %[c], v208\n\t" \
  "v_mfma_f32_16x16x16_bf16 v[200:203], v[220:221], v[236:237], 0\n\t" \
  "v_exp_f32 v218, v218\n\t" \
  "s_waitcnt vmcnt(0)\n\t" \
  "v_mfma_f32_16x16x16_bf16 v[224:227], v[252:253], v[236:237], 0\n\t" \
  "v_cvt_pk_bf16_f32 v214, v216, v217\n\t" \
  "v_cvt_pk_bf16_f32 v212, v216, v217\n\t" \
  "v_exp_f32 v219, v219\n\t" \
  "v_mfma_f32_16x16x16_bf16 v[228:231], v[252:253], v[238:239], 0\n\t" \
  "v_cvt_pk_bf16_f32 v215, v218, v219\n\t" \
  "v_cvt_pk_bf16_f32 v213, v218, v219\n\t" \
  "v_mov_b32_dpp v212, v214 row_ror:8 row_mask:0xf bank_mask:0xc\n\t" \
  "v_mov_b32_dpp v214, v214 row_ror:8 row_mask:0xf bank_mask:0x3\n\t" \
  "v_mov_b32_dpp v213, v215 row_ror:8 row_mask:0xf bank_mask:0xc\n\t" \
  "v_mov_b32_dpp v215, v215 row_ror:8 row_mask:0xf bank_mask:0x3\n\t" \
  "v_max_u32 v208, v252, v253\n\t" \
  "v_cmp_le_u32 vcc, %[sent], v208\n\t" \
  "v_mfma_f32_16x16x32_bf16 v[200:203], v[220:223], v[212:215], v[200:203]\n\t" \
  ".Lpv%=:\n\t" \
  "v_mfma_f32_16x16x32_bf16 v[224:227], v[192:195], v[212:215], v[224:227]\n\t" \
  "v_mfma_f32_16x16x32_bf16 v[228:231], v[196:199], v[212:215], v[228:231]\n\t" \
  "s_cbranch_vccnz .Lvs%=\n\t" \
  ".Lvok%=:\n\t" \
  "s_nop 4\n\t" \
  "v_rcp_f32 v208, v200\n\t" \
  "v_cndmask_b32 v224, v224, v226, %[nh]\n\t" \
  "v_cndmask_b32 v225, v225, v227, %[nh]\n\t" \
  "v_mul_f32 v224, v224, v208\n\t" \
  "v_mul_f32 v225, v225, v208\n\t" \
  "v_cvt_pk_bf16_f32 v224, v224, v225\n\t" \
  "buffer_store_dword v224, v251, %[rs], %[so] offen offset:0 sc1\n\t" \
  "v_cndmask_b32 v228, v228, v230, %[nh]\n\t" \
  "v_cndmask_b32 v229, v229, v231, %[nh]\n\t" \
  "v_mul_f32 v228, v228, v208\n\t" \
  "v_mul_f32 v229, v229, v208\n\t" \
  "v_cvt_pk_bf16_f32 v228, v228, v229\n\t" \
  "buffer_store_dword v228, v251, %[rs], %[so] offen offset:256 sc1\n\t" \
  "s_memtime s[90:91]\n\t" \
  "s_setprio 0\n\t" \
  "s_waitcnt lgkmcnt(0)\n\t" \
  "s_mov_b32 %[tout], s90\n\t" \
  "s_mov_b32 %[spins], s92\n\t" \
  "s_branch .Lend%=\n\t" \
  ".Lvs%=:\n\t" \
  "v_cmp_eq_u32 vcc, %[sent], v252\n\t" \
  "v_cmp_eq_u32 s[94:95], %[sent], v253\n\t" \
  "s_or_b64 s[94:95], s[94:95], vcc\n\t" \
  "s_cbranch_scc0 .Lvok%=\n\t" \
  "s_add_u32 s92, s92, 1\n\t" \
  "s_sleep 2\n\t" \
  "s_and_b32 s93, s92, 63\n\t" \
  "s_cbranch_scc1 .Lvr%=\n\t" \
  "buffer_load_dword v208, off, %[rs], %[sab] sc1\n\t" \
  "s_waitcnt vmcnt(0)\n\t" \
  "v_readfirstlane_b32 s93, v208\n\t" \
  "s_nop 3\n\t" \
  "s_cmp_eq_u32 s93, 1\n\t" \
  "s_cbranch_scc1 .Lvok%=\n\t" \
  "s_cmp_lt_u32 s92, 0x80000\n\t" \
  "s_cbranch_scc1 .Lvr%=\n\t" \
  "v_mov_b32 v208, 1\n\t" \
  "buffer_store_dword v208, off, %[rs], %[sab] sc1\n\t" \
  "s_branch .Lvok%=\n\t" \
  ".Lvr%=:\n\t" \
  "buffer_load_dword v252, v249, %[rs], %[sv] offen offset:0 sc1\n\t" \
  "buffer_load_dword v253, v249, %[rs], %[sv] offen offset:256 sc1\n\t" \
  "s_waitcnt vmcnt(0)\n\t" \
  "v_mfma_f32_16x16x16_bf16 v[224:227], v[252:253], v[236:237], 0\n\t" \
  "v_mfma_f32_16x16x16_bf16 v[228:231], v[252:253], v[238:239], 0\n\t" \
  "v_max_u32 v208, v252, v253\n\t" \
  "v_cmp_le_u32 vcc, %[sent], v208\n\t" \
  "s_nop 4\n\t" \
  "s_branch .Lpv%=\n\t" \
  "s_branch .Lend%=\n\t" \
  ".Lslow%=:\n\t" \
  "s_mov_b64 s[94:95], 0\n\t" \
  "v_cmp_eq_u32 vcc, %[sent], v200\n\t" \
  "s_or_b64 s[94:95], s[94:95], vcc\n\t" \
  "v_cmp_eq_u32 vcc, %[sent], v201\n\t" \
  "s_or_b64 s[94:95], s[94:95], vcc\n\t" \
  "v_cmp_eq_u32 vcc, %[sent], v202\n\t" \
  "s_or_b64 s[94:95], s[94:95], vcc\n\t" \
  "v_cmp_eq_u32 vcc, %[sent], v203\n\t" \
  "s_or_b64 s[94:95], s[94:95], vcc\n\t" \
  "v_cmp_eq_u32 vcc, %[sent], v204\n\t" \
  "s_or_b64 s[94:95], s[94:95], vcc\n\t" \
  "v_cmp_eq_u32 vcc, %[sent], v205\n\t" \
  "s_or_b64 s[94:95], s[94:95], vcc\n\t" \
  "v_cmp_eq_u32 vcc, %[sent], v206\n\t" \
  "s_or_b64 s[94:95], s[94:95], vcc\n\t" \
  "v_cmp_eq_u32 vcc, %[sent], v207\n\t" \
  "s_or_b64 s[94:95], s[94:95], vcc\n\t" \
  "s_cmp_eq_u64 s[94:95], 0\n\t" \
  "s_cbranch_scc1 .Lok%=\n\t" \
  "s_add_u32 s92, s92, 1\n\t" \
  "s_cmp_eq_u32 s92, 1\n\t" \
  "s_cbranch_scc1 .Lpoll%=\n\t" \
  "s_sleep 6\n\t" \
  "s_and_b32 s93, s92, 63\n\t" \
  "s_cmp_lg_u32 s93, 0\n\t" \
  "s_cbranch_scc1 .Lpoll%=\n\t" \
  "v_mov_b32 v209, 0\n\t" \
  "buffer_load_dword v208, v209, %[rs], %[sab] offen sc1\n\t" \
  "s_waitcnt vmcnt(0)\n\t" \
  "v_readfirstlane_b32 s93, v208\n\t" \
  "s_nop 3\n\t" \
  "s_cmp_eq_u32 s93, 1\n\t" \
  "s_cbranch_scc1 .Lok%=\n\t" \
  "s_cmp_lt_u32 s92, 0x80000\n\t" \
  "s_cbranch_scc1 .Lpoll%=\n\t" \
  "v_mov_b32 v208, 1\n\t" \
  "buffer_store_dword v208, v209, %[rs], %[sab] offen sc1\n\t" \
  "s_branch .Lok%=\n\t" \
  ".Lend%=:\n\t"
#define QK_ASM_2 \
  "s_waitcnt lgkmcnt(0)\n\t" \
  "ds_read_b128 v[216:219], v250 offset:0\n\t" \
  "ds_read_b128 v[220:223], v250 offset:2048\n\t" \
  "ds_read_b128 v[224:227], v250 offset:1024\n\t" \
  "ds_read_b128 v[228:231], v250 offset:3072\n\t" \
  "v_mov_b64 v[192:193], 0\n\t" \
  "v_mov_b64 v[194:195], 0\n\t" \
  "v_mov_b64 v[196:197], 0\n\t" \
  "v_mov_b64 v[198:199], 0\n\t" \
  "s_memrealtime s[90:91]\n\t" \
  "s_waitcnt lgkmcnt(0)\n\t" \
  "s_lshl_b32 s93, s90, 2\n\t" \
  "s_sub_i32 s93, %[tgt], s93\n\t" \
  "s_cmp_le_i32 s93, 0\n\t" \
  "s_cbranch_scc1 .Lgo%=\n\t" \
  "s_min_i32 s93, s93, 8000\n\t" \
  "s_add_u32 s93, s93, 3\n\t" \
  "s_lshr_b32 s93, s93, 2\n\t" \
  "s_add_u32 s93, s93, s90\n\t" \
  "s_memrealtime s[90:91]\n\t" \
  ".Lpace%=:\n\t" \
  "s_memrealtime s[94:95]\n\t" \
  "s_waitcnt lgkmcnt(1)\n\t" \
  "s_sub_i32 s91, s90, s93\n\t" \
  "s_cmp_ge_i32 s91, 0\n\t" \
  "s_cbranch_scc1 .Lgo%=\n\t" \
  "s_memrealtime s[90:91]\n\t" \
  "s_waitcnt lgkmcnt(1)\n\t" \
  "s_sub_i32 s95, s94, s93\n\t" \
  "s_cmp_lt_i32 s95, 0\n\t" \
  "s_cbranch_scc1 .Lpace%=\n\t" \
  ".Lgo%=:\n\t" \
  "s_mov_b32 s92, 0\n\t" \
  ".Lpoll%=:\n\t" \
  "buffer_load_dwordx4 v[200:203], v248, %[rs], %[som] offen sc1\n\t" \
  "buffer_load_dwordx4 v[204:207], v248, %[rs], %[som] offen offset:1024 sc1\n\t" \
  "s_waitcnt vmcnt(1) lgkmcnt(0)\n\t" \
  "ds_write_b128 v249, v[200:203]\n\t" \
  "v_max3_u32 v208, v200, v201, v202\n\t" \
  "s_waitcnt vmcnt(0)\n\t" \
  "ds_write_b128 v249, v[204:207] offset:1024\n\t" \
  "v_max3_u32 v209, v204, v205, v206\n\t" \
  "v_max3_u32 v208, v208, v203, v207\n\t" \
  "v_max_u32 v208, v208, v209\n\t" \
  "v_cmp_le_u32 vcc, %[sent], v208\n\t" \
  "s_cbranch_vccnz .Lslow%=\n\t" \
  ".Lok%=:\n\t" \
  "v_smfmac_f32_16x16x64_bf16 v[192:195], v[200:203], v[0:7], v251\n\t" \
  "v_smfmac_f32_16x16x64_bf16 v[196:199], v[200:203], v[8:15], v251\n\t" \
  "s_waitcnt lgkmcnt(0)\n\t" \
  "s_barrier\n\t" \
  "ds_read_b128 v[208:211], v248 offset:6144\n\t" \
  "ds_read_b128 v[212:215], v248 offset:7168\n\t" \
  "ds_read_b128 v[236:239], v248 offset:8192\n\t" \
  "ds_read_b128 v[240:243], v248 offset:9216\n\t" \
  "ds_read_b128 v[244:247], v248 offset:10240\n\t" \
  "ds_read_b128 v[200:203], v248 offset:11264\n\t" \
  "v_smfmac_f32_16x16x64_bf16 v[192:195], v[204:207], v[16:23], v251\n\t" \
  "v_smfmac_f32_16x16x64_bf16 v[196:199], v[204:207], v[24:31], v251\n\t" \
  "s_waitcnt lgkmcnt(5)\n\t" \
  "v_smfmac_f32_16x16x64_bf16 v[192:195], v[208:211], v[32:39], v251\n\t" \
  "ds_read_b128 v[204:207], v248 offset:12288\n\t" \
  "v_smfmac_f32_16x16x64_bf16 v[196:199], v[208:211], v[40:47], v251\n\t" \
  "s_waitcnt lgkmcnt(5)\n\t" \
  "v_smfmac_f32_16x16x64_bf16 v[192:195], v[212:215], v[216:223], v251\n\t" \
  "ds_read_b128 v[216:219], v250 offset:4096\n\t" \
  "v_smfmac_f32_16x16x64_bf16 v[196:199], v[212:215], v[224:231], v251\n\t" \
  "s_waitcnt lgkmcnt(5)\n\t" \
  "ds_read_b128 v[220:223], v250 offset:6144\n\t" \
  "v_smfmac_f32_16x16x64_bf16 v[192:195], v[236:239], v[48:55], v251\n\t" \
  "ds_read_b128 v[224:227], v250 offset:5120\n\t" \
  "v_smfmac_f32_16x16x64_bf16 v[196:199], v[236:239], v[56:63], v251\n\t" \
  "s_waitcnt lgkmcnt(6)\n\t" \
  "ds_read_b128 v[228:231], v250 offset:7168\n\t" \
  "v_smfmac_f32_16x16x64_bf16 v[192:195], v[240:243], v[64:71], v251\n\t" \
  "ds_read_b128 v[208:211], v248 offset:13312\n\t" \
  "v_smfmac_f32_16x16x64_bf16 v[196:199], v[240:243], v[72:79], v251\n\t" \
  "s_waitcnt lgkmcnt(5)\n\t" \
  "ds_read_b128 v[212:215], v248 offset:14336\n\t" \
  "v_smfmac_f32_16x16x64_bf16 v[192:195], v[244:247], v[80:87], v251\n\t" \
  "ds_read_b128 v[236:239], v248 offset:15360\n\t" \
  "v_smfmac_f32_16x16x64_bf16 v[196:199], v[244:247], v[88:95], v251\n\t" \
  "s_waitcnt lgkmcnt(5)\n\t" \
  "v_smfmac_f32_16x16x64_bf16 v[192:195], v[200:203], v[216:223], v251\n\t" \
  "s_waitcnt lgkmcnt(3)\n\t" \
  "ds_read_b128 v[216:219], v250 offset:8192\n\t" \
  "v_smfmac_f32_16x16x64_bf16 v[196:199], v[200:203], v[224:231], v251\n\t" \
  "ds_read_b128 v[220:223], v250 offset:10240\n\t" \
  "v_smfmac_f32_16x16x64_bf16 v[192:195], v[204:207], v[96:103], v251\n\t" \
  "ds_read_b128 v[224:227], v250 offset:9216\n\t" \
  "v_smfmac_f32_16x16x64_bf16 v[196:199], v[204:207], v[104:111], v251\n\t" \
  "s_waitcnt lgkmcnt(5)\n\t" \
  "ds_read_b128 v[228:231], v250 offset:11264\n\t" \
  "v_smfmac_f32_16x16x64_bf16 v[192:195], v[208:211], v[112:119], v251\n\t" \
  "ds_read_b128 v[240:243], v248 offset:0\n\t" \
  "v_smfmac_f32_16x16x64_bf16 v[196:199], v[208:211], v[120:127], v251\n\t" \
  "s_waitcnt lgkmcnt(5)\n\t" \
  "ds_read_b128 v[244:247], v248 offset:1024\n\t" \
  "v_smfmac_f32_16x16x64_bf16 v[192:195], v[212:215], v[128:135], v251\n\t" \
  "ds_read_b128 v[200:203], v248 offset:2048\n\t" \
  "v_smfmac_f32_16x16x64_bf16 v[196:199], v[212:215], v[136:143], v251\n\t" \
  "s_waitcnt lgkmcnt(5)\n\t" \
  "v_smfmac_f32_16x16x64_bf16 v[192:195], v[236:239], v[216:223], v251\n\t" \
  "s_waitcnt lgkmcnt(3)\n\t" \
  "ds_read_b128 v[204:207], v248 offset:3072\n\t" \
  "v_smfmac_f32_16x16x64_bf16 v[196:199], v[236:239], v[224:231], v251\n\t" \
  "s_waitcnt lgkmcnt(3)\n\t" \
  "ds_read_b128 v[216:219], v250 offset:12288\n\t" \
  "v_smfmac_f32_16x16x64_bf16 v[192:195], v[240:243], v[144:151], v251\n\t" \
  "ds_read_b128 v[220:223], v250 offset:14336\n\t" \
  "v_smfmac_f32_16x16x64_bf16 v[196:199], v[240:243], v[152:159], v251\n\t" \
  "s_waitcnt lgkmcnt(4)\n\t" \
  "ds_read_b128 v[224:227], v250 offset:13312\n\t" \
  "v_smfmac_f32_16x16x64_bf16 v[192:195], v[244:247], v[160:167], v251\n\t" \
  "ds_read_b128 v[228:231], v250 offset:15360\n\t" \
  "v_smfmac_f32_16x16x64_bf16 v[196:199], v[244:247], v[168:175], v251\n\t" \
  "s_waitcnt lgkmcnt(5)\n\t" \
  "v_smfmac_f32_16x16x64_bf16 v[192:195], v[200:203], v[176:183], v251\n\t" \
  "v_smfmac_f32_16x16x64_bf16 v[196:199], v[200:203], v[184:191], v251\n\t" \
  "s_waitcnt lgkmcnt(2)\n\t" \
  "v_smfmac_f32_16x16x64_bf16 v[192:195], v[204:207], v[216:223], v251\n\t" \
  "s_waitcnt lgkmcnt(0)\n\t" \
  "v_smfmac_f32_16x16x64_bf16 v[196:199], v[204:207], v[224:231], v251\n\t" \
  "ds_read_b128 v[200:203], v248 offset:18432\n\t" \
  "ds_read_b128 v[204:207], v248 offset:22528\n\t" \
  "v_mov_b32 v250, v232\n\t" \
  "v_mov_b32 v249, v233\n\t" \
  "s_nop 2\n\t" \
  "v_add_f32 v192, v192, v194\n\t" \
  "v_add_f32 v193, v193, v195\n\t" \
  "v_add_f32 v196, v196, v198\n\t" \
  "v_add_f32 v197, v197, v199\n\t" \
  "v_cvt_pk_bf16_f32 v192, v192, v193\n\t" \
  "v_cvt_pk_bf16_f32 v196, v196, v197\n\t" \
  "ds_write2_b32 v252, v192, v196 offset1:64\n\t" \
  "v_mov_b32 v251, v234\n\t" \
  "ds_read_b64_tr_b16 v[240:241], v250 offset:1024\n\t" \
  "ds_read_b64_tr_b16 v[242:243], v250 offset:1088\n\t" \
  "buffer_load_dwordx4 v[192:195], v248, %[rt], %[svt] offen offset:0 sc1\n\t" \
  "buffer_load_dwordx4 v[196:199], v248, %[rt], %[svt] offen offset:1024 sc1\n\t" \
  "s_waitcnt lgkmcnt(0)\n\t" \
  "v_mfma_f32_16x16x32_bf16 v[200:203], v[200:203], v[240:243], 0\n\t" \
  "v_mfma_f32_16x16x32_bf16 v[204:207], v[204:207], v[240:243], 0\n\t" \
  "s_nop 7\n\t" \
  "v_cndmask_b32 v216, v200, v204, %[nh]\n\t" \
  "v_cndmask_b32 v217, v201, v205, %[nh]\n\t" \
  "v_cndmask_b32 v218, v202, v206, %[nh]\n\t" \
  "v_cndmask_b32 v219, v203, v207, %[nh]\n\t" \
  "ds_write_b128 v248, v[216:219] offset:18432\n\t" \
  "s_sleep 2\n\t" \
  "buffer_load_dword v252, v249, %[rs], %[sv] offen offset:0 sc1\n\t" \
  "buffer_load_dword v253, v249, %[rs], %[sv] offen offset:256 sc1\n\t" \
  "s_setprio 3\n\t" \
  "s_waitcnt lgkmcnt(0)\n\t" \
  "s_barrier\n\t" \
  "ds_read_b64_tr_b16 v[208:209], v250 offset:3072\n\t" \
  "ds_read_b64_tr_b16 v[210:211], v250 offset:3136\n\t" \
  "ds_read_b64_tr_b16 v[244:245], v250 offset:1536\n\t" \
  "ds_read_b64_tr_b16 v[246:247], v250 offset:1600\n\t" \
  "ds_read_b64_tr_b16 v[212:213], v250 offset:3584\n\t" \
  "ds_read_b64_tr_b16 v[214:215], v250 offset:3648\n\t" \
  "ds_read_b64_tr_b16 v[232:233], v250 offset:0\n\t" \
  "ds_read_b64_tr_b16 v[234:235], v250 offset:64\n\t" \
  "ds_read_b64_tr_b16 v[200:201], v250 offset:2048\n\t" \
  "ds_read_b64_tr_b16 v[202:203], v250 offset:2112\n\t" \
  "ds_read_b64_tr_b16 v[236:237], v250 offset:512\n\t" \
  "ds_read_b64_tr_b16 v[238:239], v250 offset:576\n\t" \
  "ds_read_b64_tr_b16 v[204:205], v250 offset:2560\n\t" \
  "ds_read_b64_tr_b16 v[206:207], v250 offset:2624\n\t" \
  "ds_read_b128 v[220:223], v248 offset:19456\n\t" \
  "s_waitcnt lgkmcnt(13)\n\t" \
  "v_mfma_f32_16x16x32_bf16 v[208:211], v[208:211], v[240:243], 0\n\t" \
  "ds_read_b128 v[224:227], v248 offset:16384\n\t" \
  "ds_read_b128 v[228:231], v248 offset:17408\n\t" \
  "s_waitcnt lgkmcnt(11)\n\t" \
  "v_mfma_f32_16x16x32_bf16 v[208:211], v[212:215], v[244:247], v[208:211]\n\t" \
  "v_mov_b32 v241, 0\n\t" \
  "v_mov_b32 v242, 0\n\t" \
  "s_waitcnt lgkmcnt(7)\n\t" \
  "v_mfma_f32_16x16x32_bf16 v[208:211], v[200:203], v[232:235], v[208:211]\n\t" \
  "s_waitcnt lgkmcnt(3)\n\t" \
  "v_mfma_f32_16x16x32_bf16 v[208:211], v[204:207], v[236:239], v[208:211]\n\t" \
  "s_waitcnt lgkmcnt(2)\n\t" \
  "v_pk_add_f32 v[216:217], v[216:217], v[220:221]\n\t" \
  "v_pk_add_f32 v[218:219], v[218:219], v[222:223]\n\t" \
  "s_waitcnt lgkmcnt(0)\n\t" \
  "v_pk_add_f32 v[224:225], v[224:225], v[228:229]\n\t" \
  "v_pk_add_f32 v[226:227], v[226:227], v[230:231]\n\t" \
  "v_pk_add_f32 v[216:217], v[216:217], v[224:225]\n\t" \
  "v_pk_add_f32 v[218:219], v[218:219], v[226:227]\n\t" \
  "v_max3_f32 v212, v216, v217, v218\n\t" \
  "v_cndmask_b32 v208, v208, v210, %[up]\n\t" \
  "v_cndmask_b32 v209, v209, v211, %[up]\n\t" \
  "v_max_f32 v212, v212, v219\n\t" \
  "v_max3_f32 v212, v212, v208, v209\n\t" \
  "v_mov_b32 v220, 0x3f803f80\n\t" \
  "v_mov_b32 v221, 0x3f803f80\n\t" \
  "v_max_f32_dpp v213, v212, v212 row_ror:8 row_mask:0xf bank_mask:0xf\n\t" \
  "v_max_f32_dpp v214, v212, v212 row_ror:8 row_mask:0xf bank_mask:0xf\n\t" \
  "v_mov_b64 v[222:223], v[220:221]\n\t" \
  "s_nop 0\n\t" \
  "v_permlane16_swap_b32 v213, v214\n\t" \
  "v_max_f32 v212, v213, v214\n\t" \
  "v_max_f32 v215, v213, v214\n\t" \
  "s_nop 1\n\t" \
  "v_permlane32_swap_b32 v212, v215\n\t" \
  "v_max_f32 v212, v212, v215\n\t" \
  "v_mul_f32 v212, %[negc], v212\n\t" \
  "v_fma_f32 v208, v208, %[c], v212\n\t" \
  "v_fma_f32 v209, v209, %[c], v212\n\t" \
  "v_exp_f32 v208, v208\n\t" \
  "v_fma_f32 v216, v216, %[c], v212\n\t" \
  "v_exp_f32 v209, v209\n\t" \
  "v_fma_f32 v217, v217, %[c], v212\n\t" \
  "v_exp_f32 v216, v216\n\t" \
  "v_fma_f32 v218, v218, %[c], v212\n\t" \
  "v_cvt_pk_bf16_f32 v240, v208, v209\n\t" \
  "v_cvt_pk_bf16_f32 v243, v208, v209\n\t" \
  "v_exp_f32 v217, v217\n\t" \
  "v_fma_f32 v219, v219, %[c], v212\n\t" \
  "v_mfma_f32_16x16x16_bf16 v[204:207], v[220:221], v[240:241], 0\n\t" \
  "v_exp_f32 v218, v218\n\t" \
  "s_waitcnt vmcnt(0)\n\t" \
  "v_mfma_f32_16x16x16_bf16 v[224:227], v[252:253], v[240:241], 0\n\t" \
  "v_cvt_pk_bf16_f32 v202, v216, v217\n\t" \
  "v_cvt_pk_bf16_f32 v200, v216, v217\n\t" \
  "v_exp_f32 v219, v219\n\t" \
  "v_mfma_f32_16x16x16_bf16 v[228:231], v[252:253], v[242:243], 0\n\t" \
  "v_cvt_pk_bf16_f32 v203, v218, v219\n\t" \
  "v_cvt_pk_bf16_f32 v201, v218, v219\n\t" \
  "v_mov_b32_dpp v200, v202 row_ror:8 row_mask:0xf bank_mask:0xc\n\t" \
  "v_mov_b32_dpp v202, v202 row_ror:8 row_mask:0xf bank_mask:0x3\n\t" \
  "v_mov_b32_dpp v201, v203 row_ror:8 row_mask:0xf bank_mask:0xc\n\t" \
  "v_mov_b32_dpp v203, v203 row_ror:8 row_mask:0xf bank_mask:0x3\n\t" \
  "v_max_u32 v212, v252, v253\n\t" \
  "v_cmp_le_u32 vcc, %[sent], v212\n\t" \
  "v_mfma_f32_16x16x32_bf16 v[204:207], v[220:223], v[200:203], v[204:207]\n\t" \
  ".Lpv%=:\n\t" \
  "v_mfma_f32_16x16x32_bf16 v[224:227], v[192:195], v[200:203], v[224:227]\n\t" \
  "v_mfma_f32_16x16x32_bf16 v[228:231], v[196:199], v[200:203], v[228:231]\n\t" \
  "s_cbranch_vccnz .Lvs%=\n\t" \
  ".Lvok%=:\n\t" \
  "s_nop 4\n\t" \
  "v_rcp_f32 v212, v204\n\t" \
  "v_cndmask_b32 v224, v224, v226, %[nh]\n\t" \
  "v_cndmask_b32 v225, v225, v227, %[nh]\n\t" \
  "v_mul_f32 v224, v224, v212\n\t" \
  "v_mul_f32 v225, v225, v212\n\t" \
  "v_cvt_pk_bf16_f32 v224, v224, v225\n\t" \
  "buffer_store_dword v224, v251, %[rs], %[so] offen offset:0 sc1\n\t" \
  "v_cndmask_b32 v228, v228, v230, %[nh]\n\t" \
  "v_cndmask_b32 v229, v229, v231, %[nh]\n\t" \
  "v_mul_f32 v228, v228, v212\n\t" \
  "v_mul_f32 v229, v229, v212\n\t" \
  "v_cvt_pk_bf16_f32 v228, v228, v229\n\t" \
  "buffer_store_dword v228, v251, %[rs], %[so] offen offset:256 sc1\n\t" \
  "s_memtime s[90:91]\n\t" \
  "s_setprio 0\n\t" \
  "s_waitcnt lgkmcnt(0)\n\t" \
  "s_mov_b32 %[tout], s90\n\t" \
  "s_mov_b32 %[spins], s92\n\t" \
  "s_branch .Lend%=\n\t" \
  ".Lvs%=:\n\t" \
  "v_cmp_eq_u32 vcc, %[sent], v252\n\t" \
  "v_cmp_eq_u32 s[94:95], %[sent], v253\n\t" \
  "s_or_b64 s[94:95], s[94:95], vcc\n\t" \
  "s_cbranch_scc0 .Lvok%=\n\t" \
  "s_add_u32 s92, s92, 1\n\t" \
  "s_sleep 2\n\t" \
  "s_and_b32 s93, s92, 63\n\t" \
  "s_cbranch_scc1 .Lvr%=\n\t" \
  "buffer_load_dword v212, off, %[rs], %[sab] sc1\n\t" \
  "s_waitcnt vmcnt(0)\n\t" \
  "v_readfirstlane_b32 s93, v212\n\t" \
  "s_nop 3\n\t" \
  "s_cmp_eq_u32 s93, 1\n\t" \
  "s_cbranch_scc1 .Lvok%=\n\t" \
  "s_cmp_lt_u32 s92, 0x80000\n\t" \
  "s_cbranch_scc1 .Lvr%=\n\t" \
  "v_mov_b32 v212, 1\n\t" \
  "buffer_store_dword v212, off, %[rs], %[sab] sc1\n\t" \
  "s_branch .Lvok%=\n\t" \
  ".Lvr%=:\n\t" \
  "buffer_load_dword v252, v249, %[rs], %[sv] offen offset:0 sc1\n\t" \
  "buffer_load_dword v253, v249, %[rs], %[sv] offen offset:256 sc1\n\t" \
  "s_waitcnt vmcnt(0)\n\t" \
  "v_mfma_f32_16x16x16_bf16 v[224:227], v[252:253], v[240:241], 0\n\t" \
  "v_mfma_f32_16x16x16_bf16 v[228:231], v[252:253], v[242:243], 0\n\t" \
  "v_max_u32 v212, v252, v253\n\t" \
  "v_cmp_le_u32 vcc, %[sent], v212\n\t" \
  "s_nop 4\n\t" \
  "s_branch .Lpv%=\n\t" \
  "s_branch .Lend%=\n\t" \
  ".Lslow%=:\n\t" \
  "s_mov_b64 s[94:95], 0\n\t" \
  "v_cmp_eq_u32 vcc, %[sent], v200\n\t" \
  "s_or_b64 s[94:95], s[94:95], vcc\n\t" \
  "v_cmp_eq_u32 vcc, %[sent], v201\n\t" \
  "s_or_b64 s[94:95], s[94:95], vcc\n\t" \
  "v_cmp_eq_u32 vcc, %[sent], v202\n\t" \
  "s_or_b64 s[94:95], s[94:95], vcc\n\t" \
  "v_cmp_eq_u32 vcc, %[sent], v203\n\t" \
  "s_or_b64 s[94:95], s[94:95], vcc\n\t" \
  "v_cmp_eq_u32 vcc, %[sent], v204\n\t" \
  "s_or_b64 s[94:95], s[94:95], vcc\n\t" \
  "v_cmp_eq_u32 vcc, %[sent], v205\n\t" \
  "s_or_b64 s[94:95], s[94:95], vcc\n\t" \
  "v_cmp_eq_u32 vcc, %[sent], v206\n\t" \
  "s_or_b64 s[94:95], s[94:95], vcc\n\t" \
  "v_cmp_eq_u32 vcc, %[sent], v207\n\t" \
  "s_or_b64 s[94:95], s[94:95], vcc\n\t" \
  "s_cmp_eq_u64 s[94:95], 0\n\t" \
  "s_cbranch_scc1 .Lok%=\n\t" \
  "s_add_u32 s92, s92, 1\n\t" \
  "s_cmp_eq_u32 s92, 1\n\t" \
  "s_cbranch_scc1 .Lpoll%=\n\t" \
  "s_sleep 6\n\t" \
  "s_and_b32 s93, s92, 63\n\t" \
  "s_cmp_lg_u32 s93, 0\n\t" \
  "s_cbranch_scc1 .Lpoll%=\n\t" \
  "v_mov_b32 v209, 0\n\t" \
  "buffer_load_dword v208, v209, %[rs], %[sab] offen sc1\n\t" \
  "s_waitcnt vmcnt(0)\n\t" \
  "v_readfirstlane_b32 s93, v208\n\t" \
  "s_nop 3\n\t" \
  "s_cmp_eq_u32 s93, 1\n\t" \
  "s_cbranch_scc1 .Lok%=\n\t" \
  "s_cmp_lt_u32 s92, 0x80000\n\t" \
  "s_cbranch_scc1 .Lpoll%=\n\t" \
  "v_mov_b32 v208, 1\n\t" \
  "buffer_store_dword v208, v209, %[rs], %[sab] offen sc1\n\t" \
  "s_branch .Lok%=\n\t" \
  ".Lend%=:\n\t"
#define QK_ASM_3 \
  "s_waitcnt lgkmcnt(0)\n\t" \
  "ds_read_b128 v[216:219], v250 offset:0\n\t" \
  "ds_read_b128 v[220:223], v250 offset:2048\n\t" \
  "ds_read_b128 v[224:227], v250 offset:1024\n\t" \
  "ds_read_b128 v[228:231], v250 offset:3072\n\t" \
  "v_mov_b64 v[192:193], 0\n\t" \
  "v_mov_b64 v[194:195], 0\n\t" \
  "v_mov_b64 v[196:197], 0\n\t" \
  "v_mov_b64 v[198:199], 0\n\t" \
  "s_memrealtime s[90:91]\n\t" \
  "s_waitcnt lgkmcnt(0)\n\t" \
  "s_lshl_b32 s93, s90, 2\n\t" \
  "s_sub_i32 s93, %[tgt], s93\n\t" \
  "s_cmp_le_i32 s93, 0\n\t" \
  "s_cbranch_scc1 .Lgo%=\n\t" \
  "s_min_i32 s93, s93, 8000\n\t" \
  "s_add_u32 s93, s93, 3\n\t" \
  "s_lshr_b32 s93, s93, 2\n\t" \
  "s_add_u32 s93, s93, s90\n\t" \
  "s_memrealtime s[90:91]\n\t" \
  ".Lpace%=:\n\t" \
  "s_memrealtime s[94:95]\n\t" \
  "s_waitcnt lgkmcnt(1)\n\t" \
  "s_sub_i32 s91, s90, s93\n\t" \
  "s_cmp_ge_i32 s91, 0\n\t" \
  "s_cbranch_scc1 .Lgo%=\n\t" \
  "s_memrealtime s[90:91]\n\t" \
  "s_waitcnt lgkmcnt(1)\n\t" \
  "s_sub_i32 s95, s94, s93\n\t" \
  "s_cmp_lt_i32 s95, 0\n\t" \
  "s_cbranch_scc1 .Lpace%=\n\t" \
  ".Lgo%=:\n\t" \
  "s_mov_b32 s92, 0\n\t" \
  ".Lpoll%=:\n\t" \
  "buffer_load_dwordx4 v[200:203], v248, %[rs], %[som] offen sc1\n\t" \
  "buffer_load_dwordx4 v[204:207], v248, %[rs], %[som] offen offset:1024 sc1\n\t" \
  "s_waitcnt vmcnt(1) lgkmcnt(0)\n\t" \
  "ds_write_b128 v249, v[200:203]\n\t" \
  "v_max3_u32 v208, v200, v201, v202\n\t" \
  "s_waitcnt vmcnt(0)\n\t" \
  "ds_write_b128 v249, v[204:207] offset:1024\n\t" \
  "v_max3_u32 v209, v204, v205, v206\n\t" \
  "v_max3_u32 v208, v208, v203, v207\n\t" \
  "v_max_u32 v208, v208, v209\n\t" \
  "v_cmp_le_u32 vcc, %[sent], v208\n\t" \
  "s_cbranch_vccnz .Lslow%=\n\t" \
  ".Lok%=:\n\t" \
  "v_smfmac_f32_16x16x64_bf16 v[192:195], v[200:203], v[0:7], v251\n\t" \
  "v_smfmac_f32_16x16x64_bf16 v[196:199], v[200:203], v[8:15], v251\n\t" \
  "s_waitcnt lgkmcnt(0)\n\t" \
  "s_barrier\n\t" \
  "s_memrealtime s[90:91]\n\t" \
  "v_mov_b32 v236, %[step]\n\t" \
  "v_lshrrev_b32 v238, 1, v248\n\t" \
  "s_mov_b64 exec, 0xffff\n\t" \
  "s_waitcnt lgkmcnt(0)\n\t" \
  "s_lshl_b32 s90, s90, 2\n\t" \
  "s_sub_i32 s93, s90, %[tgt]\n\t" \
  "s_cmp_gt_i32 s93, 240\n\t" \
  "s_cselect_b32 s93, 60, 0\n\t" \
  "s_add_u32 s93, s93, s90\n\t" \
  "s_add_u32 s93, s93, %[lead]\n\t" \
  "v_mov_b32 v237, s93\n\t" \
  "buffer_store_dwordx2 v[236:237], v238, %[rs], %[sbc] offen sc1\n\t" \
  "s_mov_b64 exec, -1\n\t" \
  "s_mov_b32 %[tb1], s90\n\t" \
  "ds_read_b128 v[208:211], v248 offset:8192\n\t" \
  "ds_read_b128 v[212:215], v248 offset:9216\n\t" \
  "ds_read_b128 v[236:239], v248 offset:10240\n\t" \
  "ds_read_b128 v[240:243], v248 offset:11264\n\t" \
  "ds_read_b128 v[244:247], v248 offset:12288\n\t" \
  "ds_read_b128 v[200:203], v248 offset:13312\n\t" \
  "v_smfmac_f32_16x16x64_bf16 v[192:195], v[204:207], v[16:23], v251\n\t" \
  "v_smfmac_f32_16x16x64_bf16 v[196:199], v[204:207], v[24:31], v251\n\t" \
  "s_waitcnt lgkmcnt(5)\n\t" \
  "v_smfmac_f32_16x16x64_bf16 v[192:195], v[208:211], v[32:39], v251\n\t" \
  "ds_read_b128 v[204:207], v248 offset:14336\n\t" \
  "v_smfmac_f32_16x16x64_bf16 v[196:199], v[208:211], v[40:47], v251\n\t" \
  "s_waitcnt lgkmcnt(5)\n\t" \
  "v_smfmac_f32_16x16x64_bf16 v[192:195], v[212:215], v[216:223], v251\n\t" \
  "ds_read_b128 v[216:219], v250 offset:4096\n\t" \
  "v_smfmac_f32_16x16x64_bf16 v[196:199], v[212:215], v[224:231], v251\n\t" \
  "s_waitcnt lgkmcnt(5)\n\t" \
  "ds_read_b128 v[220:223], v250 offset:6144\n\t" \
  "v_smfmac_f32_16x16x64_bf16 v[192:195], v[236:239], v[48:55], v251\n\t" \
  "ds_read_b128 v[224:227], v250 offset:5120\n\t" \
  "v_smfmac_f32_16x16x64_bf16 v[196:199], v[236:239], v[56:63], v251\n\t" \
  "s_waitcnt lgkmcnt(6)\n\t" \
  "ds_read_b128 v[228:231], v250 offset:7168\n\t" \
  "v_smfmac_f32_16x16x64_bf16 v[192:195], v[240:243], v[64:71], v251\n\t" \
  "ds_read_b128 v[208:211], v248 offset:15360\n\t" \
  "v_smfmac_f32_16x16x64_bf16 v[196:199], v[240:243], v[72:79], v251\n\t" \
  "s_waitcnt lgkmcnt(5)\n\t" \
  "ds_read_b128 v[212:215], v248 offset:0\n\t" \
  "v_smfmac_f32_16x16x64_bf16 v[192:195], v[244:247], v[80:87], v251\n\t" \
  "ds_read_b128 v[236:239], v248 offset:1024\n\t" \
  "v_smfmac_f32_16x16x64_bf16 v[196:199], v[244:247], v[88:95], v251\n\t" \
  "s_waitcnt lgkmcnt(5)\n\t" \
  "v_smfmac_f32_16x16x64_bf16 v[192:195], v[200:203], v[216:223], v251\n\t" \
  "s_waitcnt lgkmcnt(3)\n\t" \
  "ds_read_b128 v[216:219], v250 offset:8192\n\t" \
  "v_smfmac_f32_16x16x64_bf16 v[196:199], v[200:203], v[224:231], v251\n\t" \
  "ds_read_b128 v[220:223], v250 offset:10240\n\t" \
  "v_smfmac_f32_16x16x64_bf16 v[192:195], v[204:207], v[96:103], v251\n\t" \
  "ds_read_b128 v[224:227], v250 offset:9216\n\t" \
  "v_smfmac_f32_16x16x64_bf16 v[196:199], v[204:207], v[104:111], v251\n\t" \
  "s_waitcnt lgkmcnt(5)\n\t" \
  "ds_read_b128 v[228:231], v250 offset:11264\n\t" \
  "v_smfmac_f32_16x16x64_bf16 v[192:195], v[208:211], v[112:119], v251\n\t" \
  "ds_read_b128 v[240:243], v248 offset:2048\n\t" \
  "v_smfmac_f32_16x16x64_bf16 v[196:199], v[208:211], v[120:127], v251\n\t" \
  "s_waitcnt lgkmcnt(5)\n\t" \
  "ds_read_b128 v[244:247], v248 offset:3072\n\t" \
  "v_smfmac_f32_16x16x64_bf16 v[192:195], v[212:215], v[128:135], v251\n\t" \
  "ds_read_b128 v[200:203], v248 offset:4096\n\t" \
  "v_smfmac_f32_16x16x64_bf16 v[196:199], v[212:215], v[136:143], v251\n\t" \
  "s_waitcnt lgkmcnt(5)\n\t" \
  "v_smfmac_f32_16x16x64_bf16 v[192:195], v[236:239], v[216:223], v251\n\t" \
  "s_waitcnt lgkmcnt(3)\n\t" \
  "ds_read_b128 v[204:207], v248 offset:5120\n\t" \
  "v_smfmac_f32_16x16x64_bf16 v[196:199], v[236:239], v[224:231], v251\n\t" \
  "s_waitcnt lgkmcnt(3)\n\t" \
  "ds_read_b128 v[216:219], v250 offset:12288\n\t" \
  "v_smfmac_f32_16x16x64_bf16 v[192:195], v[240:243], v[144:151], v251\n\t" \
  "ds_read_b128 v[220:223], v250 offset:14336\n\t" \
  "v_smfmac_f32_16x16x64_bf16 v[196:199], v[240:243], v[152:159], v251\n\t" \
  "s_waitcnt lgkmcnt(4)\n\t" \
  "ds_read_b128 v[224:227], v250 offset:13312\n\t" \
  "v_smfmac_f32_16x16x64_bf16 v[192:195], v[244:247], v[160:167], v251\n\t" \
  "ds_read_b128 v[228:231], v250 offset:15360\n\t" \
  "v_smfmac_f32_16x16x64_bf16 v[196:199], v[244:247], v[168:175], v251\n\t" \
  "s_waitcnt lgkmcnt(5)\n\t" \
  "v_smfmac_f32_16x16x64_bf16 v[192:195], v[200:203], v[176:183], v251\n\t" \
  "v_smfmac_f32_16x16x64_bf16 v[196:199], v[200:203], v[184:191], v251\n\t" \
  "s_waitcnt lgkmcnt(2)\n\t" \
  "v_smfmac_f32_16x16x64_bf16 v[192:195], v[204:207], v[216:223], v251\n\t" \
  "s_waitcnt lgkmcnt(0)\n\t" \
  "v_smfmac_f32_16x16x64_bf16 v[196:199], v[204:207], v[224:231], v251\n\t" \
  "ds_read_b128 v[200:203], v248 offset:19456\n\t" \
  "ds_read_b128 v[204:207], v248 offset:23552\n\t" \
  "v_mov_b32 v250, v232\n\t" \
  "v_mov_b32 v249, v233\n\t" \
  "s_nop 2\n\t" \
  "v_add_f32 v192, v192, v194\n\t" \
  "v_add_f32 v193, v193, v195\n\t" \
  "v_add_f32 v196, v196, v198\n\t" \
  "v_add_f32 v197, v197, v199\n\t" \
  "v_cvt_pk_bf16_f32 v192, v192, v193\n\t" \
  "v_cvt_pk_bf16_f32 v196, v196, v197\n\t" \
  "ds_write2_b32 v252, v192, v196 offset1:64\n\t" \
  "v_mov_b32 v251, v234\n\t" \
  "ds_read_b64_tr_b16 v[244:245], v250 offset:1536\n\t" \
  "ds_read_b64_tr_b16 v[246:247], v250 offset:1600\n\t" \
  "buffer_load_dwordx4 v[192:195], v248, %[rt], %[svt] offen offset:0 sc1\n\t" \
  "buffer_load_dwordx4 v[196:199], v248, %[rt], %[svt] offen offset:1024 sc1\n\t" \
  "s_waitcnt lgkmcnt(0)\n\t" \
  "v_mfma_f32_16x16x32_bf16 v[200:203], v[200:203], v[244:247], 0\n\t" \
  "v_mfma_f32_16x16x32_bf16 v[204:207], v[204:207], v[244:247], 0\n\t" \
  "s_nop 7\n\t" \
  "v_cndmask_b32 v216, v200, v204, %[nh]\n\t" \
  "v_cndmask_b32 v217, v201, v205, %[nh]\n\t" \
  "v_cndmask_b32 v218, v202, v206, %[nh]\n\t" \
  "v_cndmask_b32 v219, v203, v207, %[nh]\n\t" \
  "ds_write_b128 v248, v[216:219] offset:19456\n\t" \
  "s_sleep 2\n\t" \
  "buffer_load_dword v252, v249, %[rs], %[sv] offen offset:0 sc1\n\t" \
  "buffer_load_dword v253, v249, %[rs], %[sv] offen offset:256 sc1\n\t" \
  "s_setprio 3\n\t" \
  "s_waitcnt lgkmcnt(0)\n\t" \
  "s_barrier\n\t" \
  "ds_read_b64_tr_b16 v[212:213], v250 offset:3584\n\t" \
  "ds_read_b64_tr_b16 v[214:215], v250 offset:3648\n\t" \
  "ds_read_b64_tr_b16 v[232:233], v250 offset:0\n\t" \
  "ds_read_b64_tr_b16 v[234:235], v250 offset:64\n\t" \
  "ds_read_b64_tr_b16 v[200:201], v250 offset:2048\n\t" \
  "ds_read_b64_tr_b16 v[202:203], v250 offset:2112\n\t" \
  "ds_read_b64_tr_b16 v[236:237], v250 offset:512\n\t" \
  "ds_read_b64_tr_b16 v[238:239], v250 offset:576\n\t" \
  "ds_read_b64_tr_b16 v[204:205], v250 offset:2560\n\t" \
  "ds_read_b64_tr_b16 v[206:207], v250 offset:2624\n\t" \
  "ds_read_b64_tr_b16 v[240:241], v250 offset:1024\n\t" \
  "ds_read_b64_tr_b16 v[242:243], v250 offset:1088\n\t" \
  "ds_read_b64_tr_b16 v[208:209], v250 offset:3072\n\t" \
  "ds_read_b64_tr_b16 v[210:211], v250 offset:3136\n\t" \
  "ds_read_b128 v[220:223], v248 offset:16384\n\t" \
  "s_waitcnt lgkmcnt(13)\n\t" \
  "v_mfma_f32_16x16x32_bf16 v[212:215], v[212:215], v[244:247], 0\n\t" \
  "ds_read_b128 v[224:227], v248 offset:17408\n\t" \
  "ds_read_b128 v[228:231], v248 offset:18432\n\t" \
  "s_waitcnt lgkmcnt(11)\n\t" \
  "v_mfma_f32_16x16x32_bf16 v[212:215], v[200:203], v[232:235], v[212:215]\n\t" \
  "v_mov_b32 v245, 0\n\t" \
  "v_mov_b32 v246, 0\n\t" \
  "s_waitcnt lgkmcnt(7)\n\t" \
  "v_mfma_f32_16x16x32_bf16 v[212:215], v[204:207], v[236:239], v[212:215]\n\t" \
  "s_waitcnt lgkmcnt(3)\n\t" \
  "v_mfma_f32_16x16x32_bf16 v[212:215], v[208:211], v[240:243], v[212:215]\n\t" \
  "s_waitcnt lgkmcnt(2)\n\t" \
  "v_pk_add_f32 v[216:217], v[216:217], v[220:221]\n\t" \
  "v_pk_add_f32 v[218:219], v[218:219], v[222:223]\n\t" \
  "s_waitcnt lgkmcnt(0)\n\t" \
  "v_pk_add_f32 v[224:225], v[224:225], v[228:229]\n\t" \
  "v_pk_add_f32 v[226:227], v[226:227], v[230:231]\n\t" \
  "v_pk_add_f32 v[216:217], v[216:217], v[224:225]\n\t" \
  "v_pk_add_f32 v[218:219], v[218:219], v[226:227]\n\t" \
  "v_max3_f32 v200, v216, v217, v218\n\t" \
  "v_cndmask_b32 v212, v212, v214, %[up]\n\t" \
  "v_cndmask_b32 v213, v213, v215, %[up]\n\t" \
  "v_max_f32 v200, v200, v219\n\t" \
  "v_max3_f32 v200, v200, v212, v213\n\t" \
  "v_mov_b32 v220, 0x3f803f80\n\t" \
  "v_mov_b32 v221, 0x3f803f80\n\t" \
  "v_max_f32_dpp v201, v200, v200 row_ror:8 row_mask:0xf bank_mask:0xf\n\t" \
  "v_max_f32_dpp v202, v200, v200 row_ror:8 row_mask:0xf bank_mask:0xf\n\t" \
  "v_mov_b64 v[222:223], v[220:221]\n\t" \
  "s_nop 0\n\t" \
  "v_permlane16_swap_b32 v201, v202\n\t" \
  "v_max_f32 v200, v201, v202\n\t" \
  "v_max_f32 v203, v201, v202\n\t" \
  "s_nop 1\n\t" \
  "v_permlane32_swap_b32 v200, v203\n\t" \
  "v_max_f32 v200, v200, v203\n\t" \
  "v_mul_f32 v200, %[negc], v200\n\t" \
  "v_fma_f32 v212, v212, %[c], v200\n\t" \
  "v_fma_f32 v213, v213, %[c], v200\n\t" \
  "v_exp_f32 v212, v212\n\t" \
  "v_fma_f32 v216, v216, %[c], v200\n\t" \
  "v_exp_f32 v213, v213\n\t" \
  "v_fma_f32 v217, v217, %[c], v200\n\t" \
  "v_exp_f32 v216, v216\n\t" \
  "v_fma_f32 v218, v218, %[c], v200\n\t" \
  "v_cvt_pk_bf16_f32 v244, v212, v213\n\t" \
  "v_cvt_pk_bf16_f32 v247, v212, v213\n\t" \
  "v_exp_f32 v217, v217\n\t" \
  "v_fma_f32 v219, v219, %[c], v200\n\t" \
  "v_mfma_f32_16x16x16_bf16 v[208:211], v[220:221], v[244:245], 0\n\t" \
  "v_exp_f32 v218, v218\n\t" \
  "s_waitcnt vmcnt(0)\n\t" \
  "v_mfma_f32_16x16x16_bf16 v[224:227], v[252:253], v[244:245], 0\n\t" \
  "v_cvt_pk_bf16_f32 v206, v216, v217\n\t" \
  "v_cvt_pk_bf16_f32 v204, v216, v217\n\t" \
  "v_exp_f32 v219, v219\n\t" \
  "v_mfma_f32_16x16x16_bf16 v[228:231], v[252:253], v[246:247], 0\n\t" \
  "v_cvt_pk_bf16_f32 v207, v218, v219\n\t" \
  "v_cvt_pk_bf16_f32 v205, v218, v219\n\t" \
  "v_mov_b32_dpp v204, v206 row_ror:8 row_mask:0xf bank_mask:0xc\n\t" \
  "v_mov_b32_dpp v206, v206 row_ror:8 row_mask:0xf bank_mask:0x3\n\t" \
  "v_mov_b32_dpp v205, v207 row_ror:8 row_mask:0xf bank_mask:0xc\n\t" \
  "v_mov_b32_dpp v207, v207 row_ror:8 row_mask:0xf bank_mask:0x3\n\t" \
  "v_max_u32 v200, v252, v253\n\t" \
  "v_cmp_le_u32 vcc, %[sent], v200\n\t" \
  "v_mfma_f32_16x16x32_bf16 v[208:211], v[220:223], v[204:207], v[208:211]\n\t" \
  ".Lpv%=:\n\t" \
  "v_mfma_f32_16x16x32_bf16 v[224:227], v[192:195], v[204:207], v[224:227]\n\t" \
  "v_mfma_f32_16x16x32_bf16 v[228:231], v[196:199], v[204:207], v[228:231]\n\t" \
  "s_cbranch_vccnz .Lvs%=\n\t" \
  ".Lvok%=:\n\t" \
  "s_nop 4\n\t" \
  "v_rcp_f32 v200, v208\n\t" \
  "v_cndmask_b32 v224, v224, v226, %[nh]\n\t" \
  "v_cndmask_b32 v225, v225, v227, %[nh]\n\t" \
  "v_mul_f32 v224, v224, v200\n\t" \
  "v_mul_f32 v225, v225, v200\n\t" \
  "v_cvt_pk_bf16_f32 v224, v224, v225\n\t" \
  "buffer_store_dword v224, v251, %[rs], %[so] offen offset:0 sc1\n\t" \
  "v_cndmask_b32 v228, v228, v230, %[nh]\n\t" \
  "v_cndmask_b32 v229, v229, v231, %[nh]\n\t" \
  "v_mul_f32 v228, v228, v200\n\t" \
  "v_mul_f32 v229, v229, v200\n\t" \
  "v_cvt_pk_bf16_f32 v228, v228, v229\n\t" \
  "buffer_store_dword v228, v251, %[rs], %[so] offen offset:256 sc1\n\t" \
  "s_memtime s[90:91]\n\t" \
  "s_setprio 0\n\t" \
  "s_waitcnt lgkmcnt(0)\n\t" \
  "s_mov_b32 %[tout], s90\n\t" \
  "s_mov_b32 %[spins], s92\n\t" \
  "s_branch .Lend%=\n\t" \
  ".Lvs%=:\n\t" \
  "v_cmp_eq_u32 vcc, %[sent], v252\n\t" \
  "v_cmp_eq_u32 s[94:95], %[sent], v253\n\t" \
  "s_or_b64 s[94:95], s[94:95], vcc\n\t" \
  "s_cbranch_scc0 .Lvok%=\n\t" \
  "s_add_u32 s92, s92, 1\n\t" \
  "s_sleep 2\n\t" \
  "s_and_b32 s93, s92, 63\n\t" \
  "s_cbranch_scc1 .Lvr%=\n\t" \
  "buffer_load_dword v200, off, %[rs], %[sab] sc1\n\t" \
  "s_waitcnt vmcnt(0)\n\t" \
  "v_readfirstlane_b32 s93, v200\n\t" \
  "s_nop 3\n\t" \
  "s_cmp_eq_u32 s93, 1\n\t" \
  "s_cbranch_scc1 .Lvok%=\n\t" \
  "s_cmp_lt_u32 s92, 0x80000\n\t" \
  "s_cbranch_scc1 .Lvr%=\n\t" \
  "v_mov_b32 v200, 1\n\t" \
  "buffer_store_dword v200, off, %[rs], %[sab] sc1\n\t" \
  "s_branch .Lvok%=\n\t" \
  ".Lvr%=:\n\t" \
  "buffer_load_dword v252, v249, %[rs], %[sv] offen offset:0 sc1\n\t" \
  "buffer_load_dword v253, v249, %[rs], %[sv] offen offset:256 sc1\n\t" \
  "s_waitcnt vmcnt(0)\n\t" \
  "v_mfma_f32_16x16x16_bf16 v[224:227], v[252:253], v[244:245], 0\n\t" \
  "v_mfma_f32_16x16x16_bf16 v[228:231], v[252:253], v[246:247], 0\n\t" \
  "v_max_u32 v200, v252, v253\n\t" \
  "v_cmp_le_u32 vcc, %[sent], v200\n\t" \
  "s_nop 4\n\t" \
  "s_branch .Lpv%=\n\t" \
  "s_branch .Lend%=\n\t" \
  ".Lslow%=:\n\t" \
  "s_mov_b64 s[94:95], 0\n\t" \
  "v_cmp_eq_u32 vcc, %[sent], v200\n\t" \
  "s_or_b64 s[94:95], s[94:95], vcc\n\t" \
  "v_cmp_eq_u32 vcc, %[sent], v201\n\t" \
  "s_or_b64 s[94:95], s[94:95], vcc\n\t" \
  "v_cmp_eq_u32 vcc, %[sent], v202\n\t" \
  "s_or_b64 s[94:95], s[94:95], vcc\n\t" \
  "v_cmp_eq_u32 vcc, %[sent], v203\n\t" \
  "s_or_b64 s[94:95], s[94:95], vcc\n\t" \
  "v_cmp_eq_u32 vcc, %[sent], v204\n\t" \
  "s_or_b64 s[94:95], s[94:95], vcc\n\t" \
  "v_cmp_eq_u32 vcc, %[sent], v205\n\t" \
  "s_or_b64 s[94:95], s[94:95], vcc\n\t" \
  "v_cmp_eq_u32 vcc, %[sent], v206\n\t" \
  "s_or_b64 s[94:95], s[94:95], vcc\n\t" \
  "v_cmp_eq_u32 vcc, %[sent], v207\n\t" \
  "s_or_b64 s[94:95], s[94:95], vcc\n\t" \
  "s_cmp_eq_u64 s[94:95], 0\n\t" \
  "s_cbranch_scc1 .Lok%=\n\t" \
  "s_add_u32 s92, s92, 1\n\t" \
  "s_cmp_eq_u32 s92, 1\n\t" \
  "s_cbranch_scc1 .Lpoll%=\n\t" \
  "s_sleep 6\n\t" \
  "s_and_b32 s93, s92, 63\n\t" \
  "s_cmp_lg_u32 s93, 0\n\t" \
  "s_cbranch_scc1 .Lpoll%=\n\t" \
  "v_mov_b32 v209, 0\n\t" \
  "buffer_load_dword v208, v209, %[rs], %[sab] offen sc1\n\t" \
  "s_waitcnt vmcnt(0)\n\t" \
  "v_readfirstlane_b32 s93, v208\n\t" \
  "s_nop 3\n\t" \
  "s_cmp_eq_u32 s93, 1\n\t" \
  "s_cbranch_scc1 .Lok%=\n\t" \
  "s_cmp_lt_u32 s92, 0x80000\n\t" \
  "s_cbranch_scc1 .Lpoll%=\n\t" \
  "v_mov_b32 v208, 1\n\t" \
  "buffer_store_dword v208, v209, %[rs], %[sab] offen sc1\n\t" \
  "s_branch .Lok%=\n\t" \
  ".Lend%=:\n\t"
#define QK_ASM_4 \
  "s_waitcnt lgkmcnt(0)\n\t" \
  "ds_read_b128 v[216:219], v250 offset:0\n\t" \
  "ds_read_b128 v[220:223], v250 offset:2048\n\t" \
  "ds_read_b128 v[224:227], v250 offset:1024\n\t" \
  "ds_read_b128 v[228:231], v250 offset:3072\n\t" \
  "v_mov_b64 v[192:193], 0\n\t" \
  "v_mov_b64 v[194:195], 0\n\t" \
  "v_mov_b64 v[196:197], 0\n\t" \
  "v_mov_b64 v[198:199], 0\n\t" \
  "s_memrealtime s[90:91]\n\t" \
  "s_waitcnt lgkmcnt(0)\n\t" \
  "s_lshl_b32 s93, s90, 2\n\t" \
  "s_sub_i32 s93, %[tgt], s93\n\t" \
  "s_cmp_le_i32 s93, 0\n\t" \
  "s_cbranch_scc1 .Lgo%=\n\t" \
  "s_min_i32 s93, s93, 8000\n\t" \
  "s_add_u32 s93, s93, 3\n\t" \
  "s_lshr_b32 s93, s93, 2\n\t" \
  "s_add_u32 s93, s93, s90\n\t" \
  "s_memrealtime s[90:91]\n\t" \
  ".Lpace%=:\n\t" \
  "s_memrealtime s[94:95]\n\t" \
  "s_waitcnt lgkmcnt(1)\n\t" \
  "s_sub_i32 s91, s90, s93\n\t" \
  "s_cmp_ge_i32 s91, 0\n\t" \
  "s_cbranch_scc1 .Lgo%=\n\t" \
  "s_memrealtime s[90:91]\n\t" \
  "s_waitcnt lgkmcnt(1)\n\t" \
  "s_sub_i32 s95, s94, s93\n\t" \
  "s_cmp_lt_i32 s95, 0\n\t" \
  "s_cbranch_scc1 .Lpace%=\n\t" \
  ".Lgo%=:\n\t" \
  "s_mov_b32 s92, 0\n\t" \
  ".Lpoll%=:\n\t" \
  "buffer_load_dwordx4 v[200:203], v248, %[rs], %[som] offen sc1\n\t" \
  "buffer_load_dwordx4 v[204:207], v248, %[rs], %[som] offen offset:1024 sc1\n\t" \
  "s_waitcnt vmcnt(1) lgkmcnt(0)\n\t" \
  "ds_write_b128 v249, v[200:203]\n\t" \
  "v_max3_u32 v208, v200, v201, v202\n\t" \
  "s_waitcnt vmcnt(0)\n\t" \
  "ds_write_b128 v249, v[204:207] offset:1024\n\t" \
  "v_max3_u32 v209, v204, v205, v206\n\t" \
  "v_max3_u32 v208, v208, v203, v207\n\t" \
  "v_max_u32 v208, v208, v209\n\t" \
  "v_cmp_le_u32 vcc, %[sent], v208\n\t" \
  "s_cbranch_vccnz .Lslow%=\n\t" \
  ".Lok%=:\n\t" \
  "v_smfmac_f32_16x16x64_bf16 v[192:195], v[200:203], v[0:7], v251\n\t" \
  "v_smfmac_f32_16x16x64_bf16 v[196:199], v[200:203], v[8:15], v251\n\t" \
  "s_waitcnt lgkmcnt(0)\n\t" \
  "s_barrier\n\t" \
  "ds_read_b128 v[208:211], v248 offset:10240\n\t" \
  "ds_read_b128 v[212:215], v248 offset:11264\n\t" \
  "ds_read_b128 v[236:239], v248 offset:12288\n\t" \
  "ds_read_b128 v[240:243], v248 offset:13312\n\t" \
  "ds_read_b128 v[244:247], v248 offset:14336\n\t" \
  "ds_read_b128 v[200:203], v248 offset:15360\n\t" \
  "v_smfmac_f32_16x16x64_bf16 v[192:195], v[204:207], v[16:23], v251\n\t" \
  "v_smfmac_f32_16x16x64_bf16 v[196:199], v[204:207], v[24:31], v251\n\t" \
  "s_waitcnt lgkmcnt(5)\n\t" \
  "v_smfmac_f32_16x16x64_bf16 v[192:195], v[208:211], v[32:39], v251\n\t" \
  "ds_read_b128 v[204:207], v248 offset:0\n\t" \
  "v_smfmac_f32_16x16x64_bf16 v[196:199], v[208:211], v[40:47], v251\n\t" \
  "s_waitcnt lgkmcnt(5)\n\t" \
  "v_smfmac_f32_16x16x64_bf16 v[192:195], v[212:215], v[216:223], v251\n\t" \
  "ds_read_b128 v[216:219], v250 offset:4096\n\t" \
  "v_smfmac_f32_16x16x64_bf16 v[196:199], v[212:215], v[224:231], v251\n\t" \
  "s_waitcnt lgkmcnt(5)\n\t" \
  "ds_read_b128 v[220:223], v250 offset:6144\n\t" \
  "v_smfmac_f32_16x16x64_bf16 v[192:195], v[236:239], v[48:55], v251\n\t" \
  "ds_read_b128 v[224:227], v250 offset:5120\n\t" \
  "v_smfmac_f32_16x16x64_bf16 v[196:199], v[236:239], v[56:63], v251\n\t" \
  "s_waitcnt lgkmcnt(6)\n\t" \
  "ds_read_b128 v[228:231], v250 offset:7168\n\t" \
  "v_smfmac_f32_16x16x64_bf16 v[192:195], v[240:243], v[64:71], v251\n\t" \
  "ds_read_b128 v[208:211], v248 offset:1024\n\t" \
  "v_smfmac_f32_16x16x64_bf16 v[196:199], v[240:243], v[72:79], v251\n\t" \
  "s_waitcnt lgkmcnt(5)\n\t" \
  "ds_read_b128 v[212:215], v248 offset:2048\n\t" \
  "v_smfmac_f32_16x16x64_bf16 v[192:195], v[244:247], v[80:87], v251\n\t" \
  "ds_read_b128 v[236:239], v248 offset:3072\n\t" \
  "v_smfmac_f32_16x16x64_bf16 v[196:199], v[244:247], v[88:95], v251\n\t" \
  "s_waitcnt lgkmcnt(5)\n\t" \
  "v_smfmac_f32_16x16x64_bf16 v[192:195], v[200:203], v[216:223], v251\n\t" \
  "s_waitcnt lgkmcnt(3)\n\t" \
  "ds_read_b128 v[216:219], v250 offset:8192\n\t" \
  "v_smfmac_f32_16x16x64_bf16 v[196:199], v[200:203], v[224:231], v251\n\t" \
  "ds_read_b128 v[220:223], v250 offset:10240\n\t" \
  "v_smfmac_f32_16x16x64_bf16 v[192:195], v[204:207], v[96:103], v251\n\t" \
  "ds_read_b128 v[224:227], v250 offset:9216\n\t" \
  "v_smfmac_f32_16x16x64_bf16 v[196:199], v[204:207], v[104:111], v251\n\t" \
  "s_waitcnt lgkmcnt(5)\n\t" \
  "ds_read_b128 v[228:231], v250 offset:11264\n\t" \
  "v_smfmac_f32_16x16x64_bf16 v[192:195], v[208:211], v[112:119], v251\n\t" \
  "ds_read_b128 v[240:243], v248 offset:4096\n\t" \
  "v_smfmac_f32_16x16x64_bf16 v[196:199], v[208:211], v[120:127], v251\n\t" \
  "s_waitcnt lgkmcnt(5)\n\t" \
  "ds_read_b128 v[244:247], v248 offset:5120\n\t" \
  "v_smfmac_f32_16x16x64_bf16 v[192:195], v[212:215], v[128:135], v251\n\t" \
  "ds_read_b128 v[200:203], v248 offset:6144\n\t" \
  "v_smfmac_f32_16x16x64_bf16 v[196:199], v[212:215], v[136:143], v251\n\t" \
  "s_waitcnt lgkmcnt(5)\n\t" \
  "v_smfmac_f32_16x16x64_bf16 v[192:195], v[236:239], v[216:223], v251\n\t" \
  "s_waitcnt lgkmcnt(3)\n\t" \
  "ds_read_b128 v[204:207], v248 offset:7168\n\t" \
  "v_smfmac_f32_16x16x64_bf16 v[196:199], v[236:239], v[224:231], v251\n\t" \
  "s_waitcnt lgkmcnt(3)\n\t" \
  "ds_read_b128 v[216:219], v250 offset:12288\n\t" \
  "v_smfmac_f32_16x16x64_bf16 v[192:195], v[240:243], v[144:151], v251\n\t" \
  "ds_read_b128 v[220:223], v250 offset:14336\n\t" \
  "v_smfmac_f32_16x16x64_bf16 v[196:199], v[240:243], v[152:159], v251\n\t" \
  "s_waitcnt lgkmcnt(4)\n\t" \
  "ds_read_b128 v[224:227], v250 offset:13312\n\t" \
  "v_smfmac_f32_16x16x64_bf16 v[192:195], v[244:247], v[160:167], v251\n\t" \
  "ds_read_b128 v[228:231], v250 offset:15360\n\t" \
  "v_smfmac_f32_16x16x64_bf16 v[196:199], v[244:247], v[168:175], v251\n\t" \
  "s_waitcnt lgkmcnt(5)\n\t" \
  "v_smfmac_f32_16x16x64_bf16 v[192:195], v[200:203], v[176:183], v251\n\t" \
  "v_smfmac_f32_16x16x64_bf16 v[196:199], v[200:203], v[184:191], v251\n\t" \
  "s_waitcnt lgkmcnt(2)\n\t" \
  "v_smfmac_f32_16x16x64_bf16 v[192:195], v[204:207], v[216:223], v251\n\t" \
  "s_waitcnt lgkmcnt(0)\n\t" \
  "v_smfmac_f32_16x16x64_bf16 v[196:199], v[204:207], v[224:231], v251\n\t" \
  "s_nop 6\n\t" \
  "v_add_f32 v192, v192, v194\n\t" \
  "v_add_f32 v193, v193, v195\n\t" \
  "v_add_f32 v196, v196, v198\n\t" \
  "v_add_f32 v197, v197, v199\n\t" \
  "v_cvt_pk_bf16_f32 v192, v192, v193\n\t" \
  "v_cvt_pk_bf16_f32 v196, v196, v197\n\t" \
  "ds_write2_b32 v252, v192, v196 offset1:64\n\t" \
  "s_waitcnt lgkmcnt(0)\n\t" \
  "s_barrier\n\t" \
  "buffer_store_dword v192, v235, %[rs], %[sk] offen sc1\n\t" \
  "buffer_store_dword v196, v235, %[rs], %[sk] offen offset:256 sc1\n\t" \
  "s_mov_b32 %[spins], s92\n\t" \
  "s_branch .Lend%=\n\t" \
  ".Lslow%=:\n\t" \
  "s_mov_b64 s[94:95], 0\n\t" \
  "v_cmp_eq_u32 vcc, %[sent], v200\n\t" \
  "s_or_b64 s[94:95], s[94:95], vcc\n\t" \
  "v_cmp_eq_u32 vcc, %[sent], v201\n\t" \
  "s_or_b64 s[94:95], s[94:95], vcc\n\t" \
  "v_cmp_eq_u32 vcc, %[sent], v202\n\t" \
  "s_or_b64 s[94:95], s[94:95], vcc\n\t" \
  "v_cmp_eq_u32 vcc, %[sent], v203\n\t" \
  "s_or_b64 s[94:95], s[94:95], vcc\n\t" \
  "v_cmp_eq_u32 vcc, %[sent], v204\n\t" \
  "s_or_b64 s[94:95], s[94:95], vcc\n\t" \
  "v_cmp_eq_u32 vcc, %[sent], v205\n\t" \
  "s_or_b64 s[94:95], s[94:95], vcc\n\t" \
  "v_cmp_eq_u32 vcc, %[sent], v206\n\t" \
  "s_or_b64 s[94:95], s[94:95], vcc\n\t" \
  "v_cmp_eq_u32 vcc, %[sent], v207\n\t" \
  "s_or_b64 s[94:95], s[94:95], vcc\n\t" \
  "s_cmp_eq_u64 s[94:95], 0\n\t" \
  "s_cbranch_scc1 .Lok%=\n\t" \
  "s_add_u32 s92, s92, 1\n\t" \
  "s_cmp_eq_u32 s92, 1\n\t" \
  "s_cbranch_scc1 .Lpoll%=\n\t" \
  "s_sleep 6\n\t" \
  "s_and_b32 s93, s92, 63\n\t" \
  "s_cmp_lg_u32 s93, 0\n\t" \
  "s_cbranch_scc1 .Lpoll%=\n\t" \
  "v_mov_b32 v209, 0\n\t" \
  "buffer_load_dword v208, v209, %[rs], %[sab] offen sc1\n\t" \
  "s_waitcnt vmcnt(0)\n\t" \
  "v_readfirstlane_b32 s93, v208\n\t" \
  "s_nop 3\n\t" \
  "s_cmp_eq_u32 s93, 1\n\t" \
  "s_cbranch_scc1 .Lok%=\n\t" \
  "s_cmp_lt_u32 s92, 0x80000\n\t" \
  "s_cbranch_scc1 .Lpoll%=\n\t" \
  "v_mov_b32 v208, 1\n\t" \
  "buffer_store_dword v208, v209, %[rs], %[sab] offen sc1\n\t" \
  "s_branch .Lok%=\n\t" \
  ".Lend%=:\n\t"
#define QK_ASM_5 \
  "s_waitcnt lgkmcnt(0)\n\t" \
  "ds_read_b128 v[216:219], v250 offset:0\n\t" \
  "ds_read_b128 v[220:223], v250 offset:2048\n\t" \
  "ds_read_b128 v[224:227], v250 offset:1024\n\t" \
  "ds_read_b128 v[228:231], v250 offset:3072\n\t" \
  "v_mov_b64 v[192:193], 0\n\t" \
  "v_mov_b64 v[194:195], 0\n\t" \
  "v_mov_b64 v[196:197], 0\n\t" \
  "v_mov_b64 v[198:199], 0\n\t" \
  "s_memrealtime s[90:91]\n\t" \
  "s_waitcnt lgkmcnt(0)\n\t" \
  "s_lshl_b32 s93, s90, 2\n\t" \
  "s_sub_i32 s93, %[tgt], s93\n\t" \
  "s_cmp_le_i32 s93, 0\n\t" \
  "s_cbranch_scc1 .Lgo%=\n\t" \
  "s_min_i32 s93, s93, 8000\n\t" \
  "s_add_u32 s93, s93, 3\n\t" \
  "s_lshr_b32 s93, s93, 2\n\t" \
  "s_add_u32 s93, s93, s90\n\t" \
  "s_memrealtime s[90:91]\n\t" \
  ".Lpace%=:\n\t" \
  "s_memrealtime s[94:95]\n\t" \
  "s_waitcnt lgkmcnt(1)\n\t" \
  "s_sub_i32 s91, s90, s93\n\t" \
  "s_cmp_ge_i32 s91, 0\n\t" \
  "s_cbranch_scc1 .Lgo%=\n\t" \
  "s_memrealtime s[90:91]\n\t" \
  "s_waitcnt lgkmcnt(1)\n\t" \
  "s_sub_i32 s95, s94, s93\n\t" \
  "s_cmp_lt_i32 s95, 0\n\t" \
  "s_cbranch_scc1 .Lpace%=\n\t" \
  ".Lgo%=:\n\t" \
  "s_mov_b32 s92, 0\n\t" \
  ".Lpoll%=:\n\t" \
  "buffer_load_dwordx4 v[200:203], v248, %[rs], %[som] offen sc1\n\t" \
  "buffer_load_dwordx4 v[204:207], v248, %[rs], %[som] offen offset:1024 sc1\n\t" \
  "s_waitcnt vmcnt(1) lgkmcnt(0)\n\t" \
  "ds_write_b128 v249, v[200:203]\n\t" \
  "v_max3_u32 v208, v200, v201, v202\n\t" \
  "s_waitcnt vmcnt(0)\n\t" \
  "ds_write_b128 v249, v[204:207] offset:1024\n\t" \
  "v_max3_u32 v209, v204, v205, v206\n\t" \
  "v_max3_u32 v208, v208, v203, v207\n\t" \
  "v_max_u32 v208, v208, v209\n\t" \
  "v_cmp_le_u32 vcc, %[sent], v208\n\t" \
  "s_cbranch_vccnz .Lslow%=\n\t" \
  ".Lok%=:\n\t" \
  "v_smfmac_f32_16x16x64_bf16 v[192:195], v[200:203], v[0:7], v251\n\t" \
  "v_smfmac_f32_16x16x64_bf16 v[196:199], v[200:203], v[8:15], v251\n\t" \
  "s_waitcnt lgkmcnt(0)\n\t" \
  "s_barrier\n\t" \
  "ds_read_b128 v[208:211], v248 offset:12288\n\t" \
  "ds_read_b128 v[212:215], v248 offset:13312\n\t" \
  "ds_read_b128 v[236:239], v248 offset:14336\n\t" \
  "ds_read_b128 v[240:243], v248 offset:15360\n\t" \
  "ds_read_b128 v[244:247], v248 offset:0\n\t" \
  "ds_read_b128 v[200:203], v248 offset:1024\n\t" \
  "v_smfmac_f32_16x16x64_bf16 v[192:195], v[204:207], v[16:23], v251\n\t" \
  "v_smfmac_f32_16x16x64_bf16 v[196:199], v[204:207], v[24:31], v251\n\t" \
  "s_waitcnt lgkmcnt(5)\n\t" \
  "v_smfmac_f32_16x16x64_bf16 v[192:195], v[208:211], v[32:39], v251\n\t" \
  "ds_read_b128 v[204:207], v248 offset:2048\n\t" \
  "v_smfmac_f32_16x16x64_bf16 v[196:199], v[208:211], v[40:47], v251\n\t" \
  "s_waitcnt lgkmcnt(5)\n\t" \
  "v_smfmac_f32_16x16x64_bf16 v[192:195], v[212:215], v[216:223], v251\n\t" \
  "ds_read_b128 v[216:219], v250 offset:4096\n\t" \
  "v_smfmac_f32_16x16x64_bf16 v[196:199], v[212:215], v[224:231], v251\n\t" \
  "s_waitcnt lgkmcnt(5)\n\t" \
  "ds_read_b128 v[220:223], v250 offset:6144\n\t" \
  "v_smfmac_f32_16x16x64_bf16 v[192:195], v[236:239], v[48:55], v251\n\t" \
  "ds_read_b128 v[224:227], v250 offset:5120\n\t" \
  "v_smfmac_f32_16x16x64_bf16 v[196:199], v[236:239], v[56:63], v251\n\t" \
  "s_waitcnt lgkmcnt(6)\n\t" \
  "ds_read_b128 v[228:231], v250 offset:7168\n\t" \
  "v_smfmac_f32_16x16x64_bf16 v[192:195], v[240:243], v[64:71], v251\n\t" \
  "ds_read_b128 v[208:211], v248 offset:3072\n\t" \
  "v_smfmac_f32_16x16x64_bf16 v[196:199], v[240:243], v[72:79], v251\n\t" \
  "s_waitcnt lgkmcnt(5)\n\t" \
  "ds_read_b128 v[212:215], v248 offset:4096\n\t" \
  "v_smfmac_f32_16x16x64_bf16 v[192:195], v[244:247], v[80:87], v251\n\t" \
  "ds_read_b128 v[236:239], v248 offset:5120\n\t" \
  "v_smfmac_f32_16x16x64_bf16 v[196:199], v[244:247], v[88:95], v251\n\t" \
  "s_waitcnt lgkmcnt(5)\n\t" \
  "v_smfmac_f32_16x16x64_bf16 v[192:195], v[200:203], v[216:223], v251\n\t" \
  "s_waitcnt lgkmcnt(3)\n\t" \
  "ds_read_b128 v[216:219], v250 offset:8192\n\t" \
  "v_smfmac_f32_16x16x64_bf16 v[196:199], v[200:203], v[224:231], v251\n\t" \
  "ds_read_b128 v[220:223], v250 offset:10240\n\t" \
  "v_smfmac_f32_16x16x64_bf16 v[192:195], v[204:207], v[96:103], v251\n\t" \
  "ds_read_b128 v[224:227], v250 offset:9216\n\t" \
  "v_smfmac_f32_16x16x64_bf16 v[196:199], v[204:207], v[104:111], v251\n\t" \
  "s_waitcnt lgkmcnt(5)\n\t" \
  "ds_read_b128 v[228:231], v250 offset:11264\n\t" \
  "v_smfmac_f32_16x16x64_bf16 v[192:195], v[208:211], v[112:119], v251\n\t" \
  "ds_read_b128 v[240:243], v248 offset:6144\n\t" \
  "v_smfmac_f32_16x16x64_bf16 v[196:199], v[208:211], v[120:127], v251\n\t" \
  "s_waitcnt lgkmcnt(5)\n\t" \
  "ds_read_b128 v[244:247], v248 offset:7168\n\t" \
  "v_smfmac_f32_16x16x64_bf16 v[192:195], v[212:215], v[128:135], v251\n\t" \
  "ds_read_b128 v[200:203], v248 offset:8192\n\t" \
  "v_smfmac_f32_16x16x64_bf16 v[196:199], v[212:215], v[136:143], v251\n\t" \
  "s_waitcnt lgkmcnt(5)\n\t" \
  "v_smfmac_f32_16x16x64_bf16 v[192:195], v[236:239], v[216:223], v251\n\t" \
  "s_waitcnt lgkmcnt(3)\n\t" \
  "ds_read_b128 v[204:207], v248 offset:9216\n\t" \
  "v_smfmac_f32_16x16x64_bf16 v[196:199], v[236:239], v[224:231], v251\n\t" \
  "s_waitcnt lgkmcnt(3)\n\t" \
  "ds_read_b128 v[216:219], v250 offset:12288\n\t" \
  "v_smfmac_f32_16x16x64_bf16 v[192:195], v[240:243], v[144:151], v251\n\t" \
  "ds_read_b128 v[220:223], v250 offset:14336\n\t" \
  "v_smfmac_f32_16x16x64_bf16 v[196:199], v[240:243], v[152:159], v251\n\t" \
  "s_waitcnt lgkmcnt(4)\n\t" \
  "ds_read_b128 v[224:227], v250 offset:13312\n\t" \
  "v_smfmac_f32_16x16x64_bf16 v[192:195], v[244:247], v[160:167], v251\n\t" \
  "ds_read_b128 v[228:231], v250 offset:15360\n\t" \
  "v_smfmac_f32_16x16x64_bf16 v[196:199], v[244:247], v[168:175], v251\n\t" \
  "s_waitcnt lgkmcnt(5)\n\t" \
  "v_smfmac_f32_16x16x64_bf16 v[192:195], v[200:203], v[176:183], v251\n\t" \
  "v_smfmac_f32_16x16x64_bf16 v[196:199], v[200:203], v[184:191], v251\n\t" \
  "s_waitcnt lgkmcnt(2)\n\t" \
  "v_smfmac_f32_16x16x64_bf16 v[192:195], v[204:207], v[216:223], v251\n\t" \
  "s_waitcnt lgkmcnt(0)\n\t" \
  "v_smfmac_f32_16x16x64_bf16 v[196:199], v[204:207], v[224:231], v251\n\t" \
  "s_nop 6\n\t" \
  "v_add_f32 v192, v192, v194\n\t" \
  "v_add_f32 v193, v193, v195\n\t" \
  "v_add_f32 v196, v196, v198\n\t" \
  "v_add_f32 v197, v197, v199\n\t" \
  "v_cvt_pk_bf16_f32 v192, v192, v193\n\t" \
  "v_cvt_pk_bf16_f32 v196, v196, v197\n\t" \
  "ds_write2_b32 v252, v192, v196 offset1:64\n\t" \
  "s_waitcnt lgkmcnt(0)\n\t" \
  "s_barrier\n\t" \
  "buffer_store_dword v192, v235, %[rs], %[sk] offen sc1\n\t" \
  "buffer_store_dword v196, v235, %[rs], %[sk] offen offset:256 sc1\n\t" \
  "s_mov_b32 %[spins], s92\n\t" \
  "s_branch .Lend%=\n\t" \
  ".Lslow%=:\n\t" \
  "s_mov_b64 s[94:95], 0\n\t" \
  "v_cmp_eq_u32 vcc, %[sent], v200\n\t" \
  "s_or_b64 s[94:95], s[94:95], vcc\n\t" \
  "v_cmp_eq_u32 vcc, %[sent], v201\n\t" \
  "s_or_b64 s[94:95], s[94:95], vcc\n\t" \
  "v_cmp_eq_u32 vcc, %[sent], v202\n\t" \
  "s_or_b64 s[94:95], s[94:95], vcc\n\t" \
  "v_cmp_eq_u32 vcc, %[sent], v203\n\t" \
  "s_or_b64 s[94:95], s[94:95], vcc\n\t" \
  "v_cmp_eq_u32 vcc, %[sent], v204\n\t" \
  "s_or_b64 s[94:95], s[94:95], vcc\n\t" \
  "v_cmp_eq_u32 vcc, %[sent], v205\n\t" \
  "s_or_b64 s[94:95], s[94:95], vcc\n\t" \
  "v_cmp_eq_u32 vcc, %[sent], v206\n\t" \
  "s_or_b64 s[94:95], s[94:95], vcc\n\t" \
  "v_cmp_eq_u32 vcc, %[sent], v207\n\t" \
  "s_or_b64 s[94:95], s[94:95], vcc\n\t" \
  "s_cmp_eq_u64 s[94:95], 0\n\t" \
  "s_cbranch_scc1 .Lok%=\n\t" \
  "s_add_u32 s92, s92, 1\n\t" \
  "s_cmp_eq_u32 s92, 1\n\t" \
  "s_cbranch_scc1 .Lpoll%=\n\t" \
  "s_sleep 6\n\t" \
  "s_and_b32 s93, s92, 63\n\t" \
  "s_cmp_lg_u32 s93, 0\n\t" \
  "s_cbranch_scc1 .Lpoll%=\n\t" \
  "v_mov_b32 v209, 0\n\t" \
  "buffer_load_dword v208, v209, %[rs], %[sab] offen sc1\n\t" \
  "s_waitcnt vmcnt(0)\n\t" \
  "v_readfirstlane_b32 s93, v208\n\t" \
  "s_nop 3\n\t" \
  "s_cmp_eq_u32 s93, 1\n\t" \
  "s_cbranch_scc1 .Lok%=\n\t" \
  "s_cmp_lt_u32 s92, 0x80000\n\t" \
  "s_cbranch_scc1 .Lpoll%=\n\t" \
  "v_mov_b32 v208, 1\n\t" \
  "buffer_store_dword v208, v209, %[rs], %[sab] offen sc1\n\t" \
  "s_branch .Lok%=\n\t" \
  ".Lend%=:\n\t"
#define QK_ASM_6 \
  "s_waitcnt lgkmcnt(0)\n\t" \
  "ds_read_b128 v[216:219], v250 offset:0\n\t" \
  "ds_read_b128 v[220:223], v250 offset:2048\n\t" \
  "ds_read_b128 v[224:227], v250 offset:1024\n\t" \
  "ds_read_b128 v[228:231], v250 offset:3072\n\t" \
  "v_mov_b64 v[192:193], 0\n\t" \
  "v_mov_b64 v[194:195], 0\n\t" \
  "v_mov_b64 v[196:197], 0\n\t" \
  "v_mov_b64 v[198:199], 0\n\t" \
  "s_memrealtime s[90:91]\n\t" \
  "s_waitcnt lgkmcnt(0)\n\t" \
  "s_lshl_b32 s93, s90, 2\n\t" \
  "s_sub_i32 s93, %[tgt], s93\n\t" \
  "s_cmp_le_i32 s93, 0\n\t" \
  "s_cbranch_scc1 .Lgo%=\n\t" \
  "s_min_i32 s93, s93, 8000\n\t" \
  "s_add_u32 s93, s93, 3\n\t" \
  "s_lshr_b32 s93, s93, 2\n\t" \
  "s_add_u32 s93, s93, s90\n\t" \
  "s_memrealtime s[90:91]\n\t" \
  ".Lpace%=:\n\t" \
  "s_memrealtime s[94:95]\n\t" \
  "s_waitcnt lgkmcnt(1)\n\t" \
  "s_sub_i32 s91, s90, s93\n\t" \
  "s_cmp_ge_i32 s91, 0\n\t" \
  "s_cbranch_scc1 .Lgo%=\n\t" \
  "s_memrealtime s[90:91]\n\t" \
  "s_waitcnt lgkmcnt(1)\n\t" \
  "s_sub_i32 s95, s94, s93\n\t" \
  "s_cmp_lt_i32 s95, 0\n\t" \
  "s_cbranch_scc1 .Lpace%=\n\t" \
  ".Lgo%=:\n\t" \
  "s_mov_b32 s92, 0\n\t" \
  ".Lpoll%=:\n\t" \
  "buffer_load_dwordx4 v[200:203], v248, %[rs], %[som] offen sc1\n\t" \
  "buffer_load_dwordx4 v[204:207], v248, %[rs], %[som] offen offset:1024 sc1\n\t" \
  "s_waitcnt vmcnt(1) lgkmcnt(0)\n\t" \
  "ds_write_b128 v249, v[200:203]\n\t" \
  "v_max3_u32 v208, v200, v201, v202\n\t" \
  "s_waitcnt vmcnt(0)\n\t" \
  "ds_write_b128 v249, v[204:207] offset:1024\n\t" \
  "v_max3_u32 v209, v204, v205, v206\n\t" \
  "v_max3_u32 v208, v208, v203, v207\n\t" \
  "v_max_u32 v208, v208, v209\n\t" \
  "v_cmp_le_u32 vcc, %[sent], v208\n\t" \
  "s_cbranch_vccnz .Lslow%=\n\t" \
  ".Lok%=:\n\t" \
  "v_smfmac_f32_16x16x64_bf16 v[192:195], v[200:203], v[0:7], v251\n\t" \
  "v_smfmac_f32_16x16x64_bf16 v[196:199], v[200:203], v[8:15], v251\n\t" \
  "s_waitcnt lgkmcnt(0)\n\t" \
  "s_barrier\n\t" \
  "ds_read_b128 v[208:211], v248 offset:14336\n\t" \
  "ds_read_b128 v[212:215], v248 offset:15360\n\t" \
  "ds_read_b128 v[236:239], v248 offset:0\n\t" \
  "ds_read_b128 v[240:243], v248 offset:1024\n\t" \
  "ds_read_b128 v[244:247], v248 offset:2048\n\t" \
  "ds_read_b128 v[200:203], v248 offset:3072\n\t" \
  "v_smfmac_f32_16x16x64_bf16 v[192:195], v[204:207], v[16:23], v251\n\t" \
  "v_smfmac_f32_16x16x64_bf16 v[196:199], v[204:207], v[24:31], v251\n\t" \
  "s_waitcnt lgkmcnt(5)\n\t" \
  "v_smfmac_f32_16x16x64_bf16 v[192:195], v[208:211], v[32:39], v251\n\t" \
  "ds_read_b128 v[204:207], v248 offset:4096\n\t" \
  "v_smfmac_f32_16x16x64_bf16 v[196:199], v[208:211], v[40:47], v251\n\t" \
  "s_waitcnt lgkmcnt(5)\n\t" \
  "v_smfmac_f32_16x16x64_bf16 v[192:195], v[212:215], v[216:223], v251\n\t" \
  "ds_read_b128 v[216:219], v250 offset:4096\n\t" \
  "v_smfmac_f32_16x16x64_bf16 v[196:199], v[212:215], v[224:231], v251\n\t" \
  "s_waitcnt lgkmcnt(5)\n\t" \
  "ds_read_b128 v[220:223], v250 offset:6144\n\t" \
  "v_smfmac_f32_16x16x64_bf16 v[192:195], v[236:239], v[48:55], v251\n\t" \
  "ds_read_b128 v[224:227], v250 offset:5120\n\t" \
  "v_smfmac_f32_16x16x64_bf16 v[196:199], v[236:239], v[56:63], v251\n\t" \
  "s_waitcnt lgkmcnt(6)\n\t" \
  "ds_read_b128 v[228:231], v250 offset:7168\n\t" \
  "v_smfmac_f32_16x16x64_bf16 v[192:195], v[240:243], v[64:71], v251\n\t" \
  "ds_read_b128 v[208:211], v248 offset:5120\n\t" \
  "v_smfmac_f32_16x16x64_bf16 v[196:199], v[240:243], v[72:79], v251\n\t" \
  "s_waitcnt lgkmcnt(5)\n\t" \
  "ds_read_b128 v[212:215], v248 offset:6144\n\t" \
  "v_smfmac_f32_16x16x64_bf16 v[192:195], v[244:247], v[80:87], v251\n\t" \
  "ds_read_b128 v[236:239], v248 offset:7168\n\t" \
  "v_smfmac_f32_16x16x64_bf16 v[196:199], v[244:247], v[88:95], v251\n\t" \
  "s_waitcnt lgkmcnt(5)\n\t" \
  "v_smfmac_f32_16x16x64_bf16 v[192:195], v[200:203], v[216:223], v251\n\t" \
  "s_waitcnt lgkmcnt(3)\n\t" \
  "ds_read_b128 v[216:219], v250 offset:8192\n\t" \
  "v_smfmac_f32_16x16x64_bf16 v[196:199], v[200:203], v[224:231], v251\n\t" \
  "ds_read_b128 v[220:223], v250 offset:10240\n\t" \
  "v_smfmac_f32_16x16x64_bf16 v[192:195], v[204:207], v[96:103], v251\n\t" \
  "ds_read_b128 v[224:227], v250 offset:9216\n\t" \
  "v_smfmac_f32_16x16x64_bf16 v[196:199], v[204:207], v[104:111], v251\n\t" \
  "s_waitcnt lgkmcnt(5)\n\t" \
  "ds_read_b128 v[228:231], v250 offset:11264\n\t" \
  "v_smfmac_f32_16x16x64_bf16 v[192:195], v[208:211], v[112:119], v251\n\t" \
  "ds_read_b128 v[240:243], v248 offset:8192\n\t" \
  "v_smfmac_f32_16x16x64_bf16 v[196:199], v[208:211], v[120:127], v251\n\t" \
  "s_waitcnt lgkmcnt(5)\n\t" \
  "ds_read_b128 v[244:247], v248 offset:9216\n\t" \
  "v_smfmac_f32_16x16x64_bf16 v[192:195], v[212:215], v[128:135], v251\n\t" \
  "ds_read_b128 v[200:203], v248 offset:10240\n\t" \
  "v_smfmac_f32_16x16x64_bf16 v[196:199], v[212:215], v[136:143], v251\n\t" \
  "s_waitcnt lgkmcnt(5)\n\t" \
  "v_smfmac_f32_16x16x64_bf16 v[192:195], v[236:239], v[216:223], v251\n\t" \
  "s_waitcnt lgkmcnt(3)\n\t" \
  "ds_read_b128 v[204:207], v248 offset:11264\n\t" \
  "v_smfmac_f32_16x16x64_bf16 v[196:199], v[236:239], v[224:231], v251\n\t" \
  "s_waitcnt lgkmcnt(3)\n\t" \
  "ds_read_b128 v[216:219], v250 offset:12288\n\t" \
  "v_smfmac_f32_16x16x64_bf16 v[192:195], v[240:243], v[144:151], v251\n\t" \
  "ds_read_b128 v[220:223], v250 offset:14336\n\t" \
  "v_smfmac_f32_16x16x64_bf16 v[196:199], v[240:243], v[152:159], v251\n\t" \
  "s_waitcnt lgkmcnt(4)\n\t" \
  "ds_read_b128 v[224:227], v250 offset:13312\n\t" \
  "v_smfmac_f32_16x16x64_bf16 v[192:195], v[244:247], v[160:167], v251\n\t" \
  "ds_read_b128 v[228:231], v250 offset:15360\n\t" \
  "v_smfmac_f32_16x16x64_bf16 v[196:199], v[244:247], v[168:175], v251\n\t" \
  "s_waitcnt lgkmcnt(5)\n\t" \
  "v_smfmac_f32_16x16x64_bf16 v[192:195], v[200:203], v[176:183], v251\n\t" \
  "v_smfmac_f32_16x16x64_bf16 v[196:199], v[200:203], v[184:191], v251\n\t" \
  "s_waitcnt lgkmcnt(2)\n\t" \
  "v_smfmac_f32_16x16x64_bf16 v[192:195], v[204:207], v[216:223], v251\n\t" \
  "s_waitcnt lgkmcnt(0)\n\t" \
  "v_smfmac_f32_16x16x64_bf16 v[196:199], v[204:207], v[224:231], v251\n\t" \
  "s_nop 6\n\t" \
  "v_add_f32 v192, v192, v194\n\t" \
  "v_add_f32 v193, v193, v195\n\t" \
  "v_add_f32 v196, v196, v198\n\t" \
  "v_add_f32 v197, v197, v199\n\t" \
  "v_cvt_pk_bf16_f32 v192, v192, v193\n\t" \
  "v_cvt_pk_bf16_f32 v196, v196, v197\n\t" \
  "ds_write2_b32 v252, v192, v196 offset1:64\n\t" \
  "s_waitcnt lgkmcnt(0)\n\t" \
  "s_barrier\n\t" \
  "buffer_store_dword v192, v235, %[rs], %[sk] offen sc1\n\t" \
  "buffer_store_dword v196, v235, %[rs], %[sk] offen offset:256 sc1\n\t" \
  "s_mov_b32 %[spins], s92\n\t" \
  "s_branch .Lend%=\n\t" \
  ".Lslow%=:\n\t" \
  "s_mov_b64 s[94:95], 0\n\t" \
  "v_cmp_eq_u32 vcc, %[sent], v200\n\t" \
  "s_or_b64 s[94:95], s[94:95], vcc\n\t" \
  "v_cmp_eq_u32 vcc, %[sent], v201\n\t" \
  "s_or_b64 s[94:95], s[94:95], vcc\n\t" \
  "v_cmp_eq_u32 vcc, %[sent], v202\n\t" \
  "s_or_b64 s[94:95], s[94:95], vcc\n\t" \
  "v_cmp_eq_u32 vcc, %[sent], v203\n\t" \
  "s_or_b64 s[94:95], s[94:95], vcc\n\t" \
  "v_cmp_eq_u32 vcc, %[sent], v204\n\t" \
  "s_or_b64 s[94:95], s[94:95], vcc\n\t" \
  "v_cmp_eq_u32 vcc, %[sent], v205\n\t" \
  "s_or_b64 s[94:95], s[94:95], vcc\n\t" \
  "v_cmp_eq_u32 vcc, %[sent], v206\n\t" \
  "s_or_b64 s[94:95], s[94:95], vcc\n\t" \
  "v_cmp_eq_u32 vcc, %[sent], v207\n\t" \
  "s_or_b64 s[94:95], s[94:95], vcc\n\t" \
  "s_cmp_eq_u64 s[94:95], 0\n\t" \
  "s_cbranch_scc1 .Lok%=\n\t" \
  "s_add_u32 s92, s92, 1\n\t" \
  "s_cmp_eq_u32 s92, 1\n\t" \
  "s_cbranch_scc1 .Lpoll%=\n\t" \
  "s_sleep 6\n\t" \
  "s_and_b32 s93, s92, 63\n\t" \
  "s_cmp_lg_u32 s93, 0\n\t" \
  "s_cbranch_scc1 .Lpoll%=\n\t" \
  "v_mov_b32 v209, 0\n\t" \
  "buffer_load_dword v208, v209, %[rs], %[sab] offen sc1\n\t" \
  "s_waitcnt vmcnt(0)\n\t" \
  "v_readfirstlane_b32 s93, v208\n\t" \
  "s_nop 3\n\t" \
  "s_cmp_eq_u32 s93, 1\n\t" \
  "s_cbranch_scc1 .Lok%=\n\t" \
  "s_cmp_lt_u32 s92, 0x80000\n\t" \
  "s_cbranch_scc1 .Lpoll%=\n\t" \
  "v_mov_b32 v208, 1\n\t" \
  "buffer_store_dword v208, v209, %[rs], %[sab] offen sc1\n\t" \
  "s_branch .Lok%=\n\t" \
  ".Lend%=:\n\t"
#define QK_ASM_7 \
  "s_waitcnt lgkmcnt(0)\n\t" \
  "ds_read_b128 v[216:219], v250 offset:0\n\t" \
  "ds_read_b128 v[220:223], v250 offset:2048\n\t" \
  "ds_read_b128 v[224:227], v250 offset:1024\n\t" \
  "ds_read_b128 v[228:231], v250 offset:3072\n\t" \
  "v_mov_b64 v[192:193], 0\n\t" \
  "v_mov_b64 v[194:195], 0\n\t" \
  "v_mov_b64 v[196:197], 0\n\t" \
  "v_mov_b64 v[198:199], 0\n\t" \
  "s_memrealtime s[90:91]\n\t" \
  "s_waitcnt lgkmcnt(0)\n\t" \
  "s_lshl_b32 s93, s90, 2\n\t" \
  "s_sub_i32 s93, %[tgt], s93\n\t" \
  "s_cmp_le_i32 s93, 0\n\t" \
  "s_cbranch_scc1 .Lgo%=\n\t" \
  "s_min_i32 s93, s93, 8000\n\t" \
  "s_add_u32 s93, s93, 3\n\t" \
  "s_lshr_b32 s93, s93, 2\n\t" \
  "s_add_u32 s93, s93, s90\n\t" \
  "s_memrealtime s[90:91]\n\t" \
  ".Lpace%=:\n\t" \
  "s_memrealtime s[94:95]\n\t" \
  "s_waitcnt lgkmcnt(1)\n\t" \
  "s_sub_i32 s91, s90, s93\n\t" \
  "s_cmp_ge_i32 s91, 0\n\t" \
  "s_cbranch_scc1 .Lgo%=\n\t" \
  "s_memrealtime s[90:91]\n\t" \
  "s_waitcnt lgkmcnt(1)\n\t" \
  "s_sub_i32 s95, s94, s93\n\t" \
  "s_cmp_lt_i32 s95, 0\n\t" \
  "s_cbranch_scc1 .Lpace%=\n\t" \
  ".Lgo%=:\n\t" \
  "s_mov_b32 s92, 0\n\t" \
  ".Lpoll%=:\n\t" \
  "buffer_load_dwordx4 v[200:203], v248, %[rs], %[som] offen sc1\n\t" \
  "buffer_load_dwordx4 v[204:207], v248, %[rs], %[som] offen offset:1024 sc1\n\t" \
  "s_waitcnt vmcnt(1) lgkmcnt(0)\n\t" \
  "ds_write_b128 v249, v[200:203]\n\t" \
  "v_max3_u32 v208, v200, v201, v202\n\t" \
  "s_waitcnt vmcnt(0)\n\t" \
  "ds_write_b128 v249, v[204:207] offset:1024\n\t" \
  "v_max3_u32 v209, v204, v205, v206\n\t" \
  "v_max3_u32 v208, v208, v203, v207\n\t" \
  "v_max_u32 v208, v208, v209\n\t" \
  "v_cmp_le_u32 vcc, %[sent], v208\n\t" \
  "s_cbranch_vccnz .Lslow%=\n\t" \
  ".Lok%=:\n\t" \
  "v_smfmac_f32_16x16x64_bf16 v[192:195], v[200:203], v[0:7], v251\n\t" \
  "v_smfmac_f32_16x16x64_bf16 v[196:199], v[200:203], v[8:15], v251\n\t" \
  "s_waitcnt lgkmcnt(0)\n\t" \
  "s_barrier\n\t" \
  "ds_read_b128 v[208:211], v248 offset:0\n\t" \
  "ds_read_b128 v[212:215], v248 offset:1024\n\t" \
  "ds_read_b128 v[236:239], v248 offset:2048\n\t" \
  "ds_read_b128 v[240:243], v248 offset:3072\n\t" \
  "ds_read_b128 v[244:247], v248 offset:4096\n\t" \
  "ds_read_b128 v[200:203], v248 offset:5120\n\t" \
  "v_smfmac_f32_16x16x64_bf16 v[192:195], v[204:207], v[16:23], v251\n\t" \
  "v_smfmac_f32_16x16x64_bf16 v[196:199], v[204:207], v[24:31], v251\n\t" \
  "s_waitcnt lgkmcnt(5)\n\t" \
  "v_smfmac_f32_16x16x64_bf16 v[192:195], v[208:211], v[32:39], v251\n\t" \
  "ds_read_b128 v[204:207], v248 offset:6144\n\t" \
  "v_smfmac_f32_16x16x64_bf16 v[196:199], v[208:211], v[40:47], v251\n\t" \
  "s_waitcnt lgkmcnt(5)\n\t" \
  "v_smfmac_f32_16x16x64_bf16 v[192:195], v[212:215], v[216:223], v251\n\t" \
  "ds_read_b128 v[216:219], v250 offset:4096\n\t" \
  "v_smfmac_f32_16x16x64_bf16 v[196:199], v[212:215], v[224:231], v251\n\t" \
  "s_waitcnt lgkmcnt(5)\n\t" \
  "ds_read_b128 v[220:223], v250 offset:6144\n\t" \
  "v_smfmac_f32_16x16x64_bf16 v[192:195], v[236:239], v[48:55], v251\n\t" \
  "ds_read_b128 v[224:227], v250 offset:5120\n\t" \
  "v_smfmac_f32_16x16x64_bf16 v[196:199], v[236:239], v[56:63], v251\n\t" \
  "s_waitcnt lgkmcnt(6)\n\t" \
  "ds_read_b128 v[228:231], v250 offset:7168\n\t" \
  "v_smfmac_f32_16x16x64_bf16 v[192:195], v[240:243], v[64:71], v251\n\t" \
  "ds_read_b128 v[208:211], v248 offset:7168\n\t" \
  "v_smfmac_f32_16x16x64_bf16 v[196:199], v[240:243], v[72:79], v251\n\t" \
  "s_waitcnt lgkmcnt(5)\n\t" \
  "ds_read_b128 v[212:215], v248 offset:8192\n\t" \
  "v_smfmac_f32_16x16x64_bf16 v[192:195], v[244:247], v[80:87], v251\n\t" \
  "ds_read_b128 v[236:239], v248 offset:9216\n\t" \
  "v_smfmac_f32_16x16x64_bf16 v[196:199], v[244:247], v[88:95], v251\n\t" \
  "s_waitcnt lgkmcnt(5)\n\t" \
  "v_smfmac_f32_16x16x64_bf16 v[192:195], v[200:203], v[216:223], v251\n\t" \
  "s_waitcnt lgkmcnt(3)\n\t" \
  "ds_read_b128 v[216:219], v250 offset:8192\n\t" \
  "v_smfmac_f32_16x16x64_bf16 v[196:199], v[200:203], v[224:231], v251\n\t" \
  "ds_read_b128 v[220:223], v250 offset:10240\n\t" \
  "v_smfmac_f32_16x16x64_bf16 v[192:195], v[204:207], v[96:103], v251\n\t" \
  "ds_read_b128 v[224:227], v250 offset:9216\n\t" \
  "v_smfmac_f32_16x16x64_bf16 v[196:199], v[204:207], v[104:111], v251\n\t" \
  "s_waitcnt lgkmcnt(5)\n\t" \
  "ds_read_b128 v[228:231], v250 offset:11264\n\t" \
  "v_smfmac_f32_16x16x64_bf16 v[192:195], v[208:211], v[112:119], v251\n\t" \
  "ds_read_b128 v[240:243], v248 offset:10240\n\t" \
  "v_smfmac_f32_16x16x64_bf16 v[196:199], v[208:211], v[120:127], v251\n\t" \
  "s_waitcnt lgkmcnt(5)\n\t" \
  "ds_read_b128 v[244:247], v248 offset:11264\n\t" \
  "v_smfmac_f32_16x16x64_bf16 v[192:195], v[212:215], v[128:135], v251\n\t" \
  "ds_read_b128 v[200:203], v248 offset:12288\n\t" \
  "v_smfmac_f32_16x16x64_bf16 v[196:199], v[212:215], v[136:143], v251\n\t" \
  "s_waitcnt lgkmcnt(5)\n\t" \
  "v_smfmac_f32_16x16x64_bf16 v[192:195], v[236:239], v[216:223], v251\n\t" \
  "s_waitcnt lgkmcnt(3)\n\t" \
  "ds_read_b128 v[204:207], v248 offset:13312\n\t" \
  "v_smfmac_f32_16x16x64_bf16 v[196:199], v[236:239], v[224:231], v251\n\t" \
  "s_waitcnt lgkmcnt(3)\n\t" \
  "ds_read_b128 v[216:219], v250 offset:12288\n\t" \
  "v_smfmac_f32_16x16x64_bf16 v[192:195], v[240:243], v[144:151], v251\n\t" \
  "ds_read_b128 v[220:223], v250 offset:14336\n\t" \
  "v_smfmac_f32_16x16x64_bf16 v[196:199], v[240:243], v[152:159], v251\n\t" \
  "s_waitcnt lgkmcnt(4)\n\t" \
  "ds_read_b128 v[224:227], v250 offset:13312\n\t" \
  "v_smfmac_f32_16x16x64_bf16 v[192:195], v[244:247], v[160:167], v251\n\t" \
  "ds_read_b128 v[228:231], v250 offset:15360\n\t" \
  "v_smfmac_f32_16x16x64_bf16 v[196:199], v[244:247], v[168:175], v251\n\t" \
  "s_waitcnt lgkmcnt(5)\n\t" \
  "v_smfmac_f32_16x16x64_bf16 v[192:195], v[200:203], v[176:183], v251\n\t" \
  "v_smfmac_f32_16x16x64_bf16 v[196:199], v[200:203], v[184:191], v251\n\t" \
  "s_waitcnt lgkmcnt(2)\n\t" \
  "v_smfmac_f32_16x16x64_bf16 v[192:195], v[204:207], v[216:223], v251\n\t" \
  "s_waitcnt lgkmcnt(0)\n\t" \
  "v_smfmac_f32_16x16x64_bf16 v[196:199], v[204:207], v[224:231], v251\n\t" \
  "s_nop 6\n\t" \
  "v_add_f32 v192, v192, v194\n\t" \
  "v_add_f32 v193, v193, v195\n\t" \
  "v_add_f32 v196, v196, v198\n\t" \
  "v_add_f32 v197, v197, v199\n\t" \
  "v_cvt_pk_bf16_f32 v192, v192, v193\n\t" \
  "v_cvt_pk_bf16_f32 v196, v196, v197\n\t" \
  "ds_write2_b32 v252, v192, v196 offset1:64\n\t" \
  "s_waitcnt lgkmcnt(0)\n\t" \
  "s_barrier\n\t" \
  "buffer_store_dword v192, v235, %[rs], %[sk] offen sc1\n\t" \
  "buffer_store_dword v196, v235, %[rs], %[sk] offen offset:256 sc1\n\t" \
  "s_mov_b32 %[spins], s92\n\t" \
  "s_branch .Lend%=\n\t" \
  ".Lslow%=:\n\t" \
  "s_mov_b64 s[94:95], 0\n\t" \
  "v_cmp_eq_u32 vcc, %[sent], v200\n\t" \
  "s_or_b64 s[94:95], s[94:95], vcc\n\t" \
  "v_cmp_eq_u32 vcc, %[sent], v201\n\t" \
  "s_or_b64 s[94:95], s[94:95], vcc\n\t" \
  "v_cmp_eq_u32 vcc, %[sent], v202\n\t" \
  "s_or_b64 s[94:95], s[94:95], vcc\n\t" \
  "v_cmp_eq_u32 vcc, %[sent], v203\n\t" \
  "s_or_b64 s[94:95], s[94:95], vcc\n\t" \
  "v_cmp_eq_u32 vcc, %[sent], v204\n\t" \
  "s_or_b64 s[94:95], s[94:95], vcc\n\t" \
  "v_cmp_eq_u32 vcc, %[sent], v205\n\t" \
  "s_or_b64 s[94:95], s[94:95], vcc\n\t" \
  "v_cmp_eq_u32 vcc, %[sent], v206\n\t" \
  "s_or_b64 s[94:95], s[94:95], vcc\n\t" \
  "v_cmp_eq_u32 vcc, %[sent], v207\n\t" \
  "s_or_b64 s[94:95], s[94:95], vcc\n\t" \
  "s_cmp_eq_u64 s[94:95], 0\n\t" \
  "s_cbranch_scc1 .Lok%=\n\t" \
  "s_add_u32 s92, s92, 1\n\t" \
  "s_cmp_eq_u32 s92, 1\n\t" \
  "s_cbranch_scc1 .Lpoll%=\n\t" \
  "s_sleep 6\n\t" \
  "s_and_b32 s93, s92, 63\n\t" \
  "s_cmp_lg_u32 s93, 0\n\t" \
  "s_cbranch_scc1 .Lpoll%=\n\t" \
  "v_mov_b32 v209, 0\n\t" \
  "buffer_load_dword v208, v209, %[rs], %[sab] offen sc1\n\t" \
  "s_waitcnt vmcnt(0)\n\t" \
  "v_readfirstlane_b32 s93, v208\n\t" \
  "s_nop 3\n\t" \
  "s_cmp_eq_u32 s93, 1\n\t" \
  "s_cbranch_scc1 .Lok%=\n\t" \
  "s_cmp_lt_u32 s92, 0x80000\n\t" \
  "s_cbranch_scc1 .Lpoll%=\n\t" \
  "v_mov_b32 v208, 1\n\t" \
  "buffer_store_dword v208, v209, %[rs], %[sab] offen sc1\n\t" \
  "s_branch .Lok%=\n\t" \
  ".Lend%=:\n\t"
#define VV_ASM \
  "s_waitcnt lgkmcnt(0)\n\t" \
  "v_mov_b64 v[128:129], 0\n\t" \
  "v_mov_b64 v[130:131], 0\n\t" \
  "s_memrealtime s[90:91]\n\t" \
  "s_waitcnt lgkmcnt(0)\n\t" \
  "s_lshl_b32 s93, s90, 2\n\t" \
  "s_sub_i32 s93, %[tgt], s93\n\t" \
  "s_cmp_le_i32 s93, 0\n\t" \
  "s_cbranch_scc1 .Lgo%=\n\t" \
  "s_min_i32 s93, s93, 8000\n\t" \
  "s_add_u32 s93, s93, 3\n\t" \
  "s_lshr_b32 s93, s93, 2\n\t" \
  "s_add_u32 s93, s93, s90\n\t" \
  "s_memrealtime s[90:91]\n\t" \
  ".Lpace%=:\n\t" \
  "s_memrealtime s[94:95]\n\t" \
  "s_waitcnt lgkmcnt(1)\n\t" \
  "s_sub_i32 s91, s90, s93\n\t" \
  "s_cmp_ge_i32 s91, 0\n\t" \
  "s_cbranch_scc1 .Lgo%=\n\t" \
  "s_memrealtime s[90:91]\n\t" \
  "s_waitcnt lgkmcnt(1)\n\t" \
  "s_sub_i32 s95, s94, s93\n\t" \
  "s_cmp_lt_i32 s95, 0\n\t" \
  "s_cbranch_scc1 .Lpace%=\n\t" \
  ".Lgo%=:\n\t" \
  "s_mov_b32 s92, 0\n\t" \
  ".Lpoll%=:\n\t" \
  "buffer_load_dwordx4 v[188:191], v200, %[rs], %[som] offen sc1\n\t" \
  "buffer_load_dwordx4 v[192:195], v200, %[rs], %[som] offen offset:1024 sc1\n\t" \
  "s_waitcnt vmcnt(1) lgkmcnt(0)\n\t" \
  "ds_write_b128 v201, v[188:191]\n\t" \
  "v_max3_u32 v196, v188, v189, v190\n\t" \
  "s_waitcnt vmcnt(0)\n\t" \
  "ds_write_b128 v201, v[192:195] offset:1024\n\t" \
  "v_max3_u32 v197, v192, v193, v194\n\t" \
  "v_max3_u32 v196, v196, v191, v195\n\t" \
  "v_max_u32 v196, v196, v197\n\t" \
  "v_cmp_le_u32 vcc, %[sent], v196\n\t" \
  "s_cbranch_vccnz .Lslow%=\n\t" \
  ".Lok%=:\n\t" \
  "s_waitcnt lgkmcnt(0)\n\t" \
  "s_barrier\n\t" \
  "ds_read_b128 v[132:135], v200 offset:0\n\t" \
  "ds_read_b128 v[136:139], v200 offset:1024\n\t" \
  "ds_read_b128 v[140:143], v200 offset:2048\n\t" \
  "ds_read_b128 v[144:147], v200 offset:3072\n\t" \
  "ds_read_b128 v[148:151], v200 offset:4096\n\t" \
  "s_waitcnt lgkmcnt(4)\n\t" \
  "ds_read_b128 v[152:155], v200 offset:5120\n\t" \
  "v_smfmac_f32_16x16x64_bf16 v[128:131], v[132:135], v[0:7], v202\n\t" \
  "s_waitcnt lgkmcnt(4)\n\t" \
  "ds_read_b128 v[156:159], v200 offset:6144\n\t" \
  "v_smfmac_f32_16x16x64_bf16 v[128:131], v[136:139], v[8:15], v202\n\t" \
  "s_waitcnt lgkmcnt(4)\n\t" \
  "ds_read_b128 v[160:163], v200 offset:7168\n\t" \
  "v_smfmac_f32_16x16x64_bf16 v[128:131], v[140:143], v[16:23], v202\n\t" \
  "s_waitcnt lgkmcnt(4)\n\t" \
  "ds_read_b128 v[164:167], v200 offset:8192\n\t" \
  "v_smfmac_f32_16x16x64_bf16 v[128:131], v[144:147], v[24:31], v202\n\t" \
  "s_waitcnt lgkmcnt(4)\n\t" \
  "ds_read_b128 v[168:171], v200 offset:9216\n\t" \
  "v_smfmac_f32_16x16x64_bf16 v[128:131], v[148:151], v[32:39], v202\n\t" \
  "s_waitcnt lgkmcnt(4)\n\t" \
  "ds_read_b128 v[172:175], v200 offset:10240\n\t" \
  "v_smfmac_f32_16x16x64_bf16 v[128:131], v[152:155], v[40:47], v202\n\t" \
  "s_waitcnt lgkmcnt(4)\n\t" \
  "ds_read_b128 v[176:179], v200 offset:11264\n\t" \
  "v_smfmac_f32_16x16x64_bf16 v[128:131], v[156:159], v[48:55], v202\n\t" \
  "s_waitcnt lgkmcnt(4)\n\t" \
  "ds_read_b128 v[180:183], v200 offset:12288\n\t" \
  "v_smfmac_f32_16x16x64_bf16 v[128:131], v[160:163], v[56:63], v202\n\t" \
  "s_waitcnt lgkmcnt(4)\n\t" \
  "ds_read_b128 v[184:187], v200 offset:13312\n\t" \
  "v_smfmac_f32_16x16x64_bf16 v[128:131], v[164:167], v[64:71], v202\n\t" \
  "s_waitcnt lgkmcnt(4)\n\t" \
  "ds_read_b128 v[188:191], v200 offset:14336\n\t" \
  "v_smfmac_f32_16x16x64_bf16 v[128:131], v[168:171], v[72:79], v202\n\t" \
  "s_waitcnt lgkmcnt(4)\n\t" \
  "ds_read_b128 v[192:195], v200 offset:15360\n\t" \
  "v_smfmac_f32_16x16x64_bf16 v[128:131], v[172:175], v[80:87], v202\n\t" \
  "s_waitcnt lgkmcnt(4)\n\t" \
  "v_smfmac_f32_16x16x64_bf16 v[128:131], v[176:179], v[88:95], v202\n\t" \
  "s_waitcnt lgkmcnt(3)\n\t" \
  "v_smfmac_f32_16x16x64_bf16 v[128:131], v[180:183], v[96:103], v202\n\t" \
  "s_waitcnt lgkmcnt(2)\n\t" \
  "v_smfmac_f32_16x16x64_bf16 v[128:131], v[184:187], v[104:111], v202\n\t" \
  "s_waitcnt lgkmcnt(1)\n\t" \
  "v_smfmac_f32_16x16x64_bf16 v[128:131], v[188:191], v[112:119], v202\n\t" \
  "s_waitcnt lgkmcnt(0)\n\t" \
  "v_smfmac_f32_16x16x64_bf16 v[128:131], v[192:195], v[120:127], v202\n\t" \
  "s_nop 7\n\t" \
  "v_add_f32 v128, v128, v130\n\t" \
  "v_add_f32 v129, v129, v131\n\t" \
  "v_cvt_pk_bf16_f32 v128, v128, v129\n\t" \
  "buffer_store_dword v128, v203, %[rs], %[sv] offen sc1\n\t" \
  "ds_write_b32 v204, v128\n\t" \
  "s_waitcnt lgkmcnt(0)\n\t" \
  "s_barrier\n\t" \
  "s_mov_b32 %[spins], s92\n\t" \
  "s_branch .Lend%=\n\t" \
  ".Lslow%=:\n\t" \
  "s_mov_b64 s[94:95], 0\n\t" \
  "v_cmp_eq_u32 vcc, %[sent], v188\n\t" \
  "s_or_b64 s[94:95], s[94:95], vcc\n\t" \
  "v_cmp_eq_u32 vcc, %[sent], v189\n\t" \
  "s_or_b64 s[94:95], s[94:95], vcc\n\t" \
  "v_cmp_eq_u32 vcc, %[sent], v190\n\t" \
  "s_or_b64 s[94:95], s[94:95], vcc\n\t" \
  "v_cmp_eq_u32 vcc, %[sent], v191\n\t" \
  "s_or_b64 s[94:95], s[94:95], vcc\n\t" \
  "v_cmp_eq_u32 vcc, %[sent], v192\n\t" \
  "s_or_b64 s[94:95], s[94:95], vcc\n\t" \
  "v_cmp_eq_u32 vcc, %[sent], v193\n\t" \
  "s_or_b64 s[94:95], s[94:95], vcc\n\t" \
  "v_cmp_eq_u32 vcc, %[sent], v194\n\t" \
  "s_or_b64 s[94:95], s[94:95], vcc\n\t" \
  "v_cmp_eq_u32 vcc, %[sent], v195\n\t" \
  "s_or_b64 s[94:95], s[94:95], vcc\n\t" \
  "s_cmp_eq_u64 s[94:95], 0\n\t" \
  "s_cbranch_scc1 .Lok%=\n\t" \
  "s_add_u32 s92, s92, 1\n\t" \
  "s_cmp_eq_u32 s92, 1\n\t" \
  "s_cbranch_scc1 .Lpoll%=\n\t" \
  "s_sleep 6\n\t" \
  "s_and_b32 s93, s92, 63\n\t" \
  "s_cmp_lg_u32 s93, 0\n\t" \
  "s_cbranch_scc1 .Lpoll%=\n\t" \
  "v_mov_b32 v197, 0\n\t" \
  "buffer_load_dword v196, v197, %[rs], %[sab] offen sc1\n\t" \
  "s_waitcnt vmcnt(0)\n\t" \
  "v_readfirstlane_b32 s93, v196\n\t" \
  "s_nop 3\n\t" \
  "s_cmp_eq_u32 s93, 1\n\t" \
  "s_cbranch_scc1 .Lok%=\n\t" \
  "s_cmp_lt_u32 s92, 0x80000\n\t" \
  "s_cbranch_scc1 .Lpoll%=\n\t" \
  "v_mov_b32 v196, 1\n\t" \
  "buffer_store_dword v196, v197, %[rs], %[sab] offen sc1\n\t" \
  "s_branch .Lok%=\n\t" \
  ".Lend%=:\n\t"
#define RED_ASM \
  "s_mov_b32 %[step], 0\n\t" \
  "s_lshl_b32 %[sh], %[step], 15\n\t" \
  "s_lshr_b32 s93, %[step], 4\n\t" \
  "s_lshr_b32 s93, %[pm], s93\n\t" \
  "s_and_b32 s93, s93, 4\n\t" \
  "s_lshl_b32 s93, s93, 11\n\t" \
  "s_lshl2_add_u32 %[so], %[sh], s93\n\t" \
  "s_sub_i32 s93, %[step], 3\n\t" \
  "s_lshl_b32 %[st], s93, 19\n\t" \
  "s_lshl_b32 s94, s93, 11\n\t" \
  "s_xor_b32 s94, s94, %[s0]\n\t" \
  "s_and_b32 s94, s94, 0x2000\n\t" \
  "s_or_b32 %[st], %[st], s94\n\t" \
  "s_cmp_lt_i32 s93, 0\n\t" \
  "s_cselect_b32 %[st], 0x70000000, %[st]\n\t" \
  "s_add_u32 %[st4], %[st], 4096\n\t" \
  "buffer_load_dwordx2 v[166:167], v173, %[rs], 0 offen sc1\n\t" \
  "v_mov_b64 v[144:145], 0\n\t" \
  "v_mov_b64 v[146:147], 0\n\t" \
  "v_mov_b64 v[148:149], 0\n\t" \
  "v_mov_b64 v[150:151], 0\n\t" \
  "v_mov_b64 v[152:153], 0\n\t" \
  "v_mov_b64 v[154:155], 0\n\t" \
  "v_mov_b64 v[156:157], 0\n\t" \
  "v_mov_b64 v[158:159], 0\n\t" \
  ".Lstep%=:\n\t" \
  "s_setprio 3\n\t" \
  "s_mov_b32 s92, 0\n\t" \
  ".Lcal%=:\n\t" \
  "s_waitcnt vmcnt(0)\n\t" \
  "s_memrealtime s[76:77]\n\t" \
  "v_cmp_ne_u32 vcc, %[step], v166\n\t" \
  "v_readlane_b32 s90, v167, 0\n\t" \
  "v_readlane_b32 s91, v167, 1\n\t" \
  "v_readlane_b32 s94, v167, 2\n\t" \
  "v_readlane_b32 s95, v167, 3\n\t" \
  "s_sub_i32 s90, s90, %[base]\n\t" \
  "s_sub_i32 s91, s91, %[base]\n\t" \
  "s_sub_i32 s94, s94, %[base]\n\t" \
  "s_sub_i32 s95, s95, %[base]\n\t" \
  "s_max_i32 s90, s90, s91\n\t" \
  "s_max_i32 s94, s94, s95\n\t" \
  "s_max_i32 s90, s90, s94\n\t" \
  "s_add_i32 s90, s90, %[ds]\n\t" \
  "s_waitcnt lgkmcnt(0)\n\t" \
  "s_lshl2_add_u32 s76, s76, %[mn]\n\t" \
  "s_sub_i32 s76, s76, %[base]\n\t" \
  "s_max_i32 s94, s90, s76\n\t" \
  "s_add_u32 %[E], s90, %[base]\n\t" \
  "s_and_b32 s93, vcc_lo, 0xf\n\t" \
  "s_cbranch_scc1 .Lbl%=\n\t" \
  ".Lgo%=:\n\t" \
  "v_mov_b32 v166, 7\n\t" \
  "v_mov_b32 v162, %[step]\n\t" \
  "s_mov_b64 exec, 1\n\t" \
  "v_mov_b32 v164, s94\n\t" \
  "ds_max_u32 v168, v164 offset:4100\n\t" \
  "ds_inc_rtn_u32 v161, v168, v166 offset:4096\n\t" \
  "ds_read_b32 v163, v168 offset:4100\n\t" \
  "s_waitcnt lgkmcnt(0)\n\t" \
  "v_readfirstlane_b32 s94, v161\n\t" \
  "v_readfirstlane_b32 s95, v163\n\t" \
  "s_mov_b64 exec, -1\n\t" \
  "s_cmp_lg_u32 s94, 7\n\t" \
  "s_cbranch_scc1 .Lna%=\n\t" \
  "s_add_u32 s95, s95, %[rl]\n\t" \
  "s_add_u32 s95, s95, %[base]\n\t" \
  "s_mov_b64 exec, 0xffff\n\t" \
  "v_mov_b32 v163, s95\n\t" \
  "buffer_store_dwordx2 v[162:163], v175, %[rs], 0 offen sc1\n\t" \
  "s_mov_b64 exec, -1\n\t" \
  ".Lna%=:\n\t" \
  "s_memrealtime s[90:91]\n\t" \
  "s_waitcnt lgkmcnt(0)\n\t" \
  "s_lshl_b32 s93, s90, 2\n\t" \
  "s_sub_i32 s93, %[E], s93\n\t" \
  "s_cmp_le_i32 s93, 0\n\t" \
  "s_cbranch_scc1 .Lgop%=\n\t" \
  "s_min_i32 s93, s93, 8000\n\t" \
  "s_add_u32 s93, s93, 3\n\t" \
  "s_lshr_b32 s93, s93, 2\n\t" \
  "s_add_u32 s93, s93, s90\n\t" \
  "s_memrealtime s[90:91]\n\t" \
  ".Lpacep%=:\n\t" \
  "s_memrealtime s[94:95]\n\t" \
  "s_waitcnt lgkmcnt(1)\n\t" \
  "s_sub_i32 s91, s90, s93\n\t" \
  "s_cmp_ge_i32 s91, 0\n\t" \
  "s_cbranch_scc1 .Lgop%=\n\t" \
  "s_memrealtime s[90:91]\n\t" \
  "s_waitcnt lgkmcnt(1)\n\t" \
  "s_sub_i32 s95, s94, s93\n\t" \
  "s_cmp_lt_i32 s95, 0\n\t" \
  "s_cbranch_scc1 .Lpacep%=\n\t" \
  ".Lgop%=:\n\t" \
  "buffer_load_dwordx4 v[128:131], v234, %[rs], %[so] offen sc1\n\t" \
  "buffer_load_dwordx4 v[132:135], v235, %[rs], %[so] offen sc1\n\t" \
  "buffer_load_dwordx4 v[136:139], v236, %[rs], %[so] offen sc1\n\t" \
  "buffer_load_dwordx4 v[140:143], v237, %[rs], %[so] offen sc1\n\t" \
  "s_waitcnt lgkmcnt(0)\n\t" \
  ".Lw%=:\n\t" \
  "s_waitcnt vmcnt(3)\n\t" \
  "v_smfmac_f32_16x16x64_bf16 v[144:147], v[128:131], v[0:7], v172\n\t" \
  "v_max3_u32 v164, v128, v129, 0\n\t" \
  "v_smfmac_f32_16x16x64_bf16 v[148:151], v[128:131], v[8:15], v172\n\t" \
  "v_max3_u32 v164, v130, v131, v164\n\t" \
  "v_smfmac_f32_16x16x64_bf16 v[152:155], v[128:131], v[16:23], v172\n\t" \
  "v_smfmac_f32_16x16x64_bf16 v[156:159], v[128:131], v[24:31], v172\n\t" \
  "buffer_load_dwordx4 v[176:179], v238, %[rt], %[st] offen offset:0 sc1\n\t" \
  "s_waitcnt vmcnt(3)\n\t" \
  "v_smfmac_f32_16x16x64_bf16 v[144:147], v[132:135], v[32:39], v172\n\t" \
  "v_max3_u32 v164, v132, v133, v164\n\t" \
  "v_smfmac_f32_16x16x64_bf16 v[148:151], v[132:135], v[40:47], v172\n\t" \
  "v_max3_u32 v164, v134, v135, v164\n\t" \
  "v_smfmac_f32_16x16x64_bf16 v[152:155], v[132:135], v[48:55], v172\n\t" \
  "v_smfmac_f32_16x16x64_bf16 v[156:159], v[132:135], v[56:63], v172\n\t" \
  "buffer_load_dwordx4 v[180:183], v238, %[rt], %[st] offen offset:1024 sc1\n\t" \
  "s_waitcnt vmcnt(3)\n\t" \
  "v_smfmac_f32_16x16x64_bf16 v[144:147], v[136:139], v[64:71], v172\n\t" \
  "v_max3_u32 v164, v136, v137, v164\n\t" \
  "v_smfmac_f32_16x16x64_bf16 v[148:151], v[136:139], v[72:79], v172\n\t" \
  "v_max3_u32 v164, v138, v139, v164\n\t" \
  "v_smfmac_f32_16x16x64_bf16 v[152:155], v[136:139], v[80:87], v172\n\t" \
  "v_smfmac_f32_16x16x64_bf16 v[156:159], v[136:139], v[88:95], v172\n\t" \
  "buffer_load_dwordx4 v[184:187], v238, %[rt], %[st4] offen offset:0 sc1\n\t" \
  "s_waitcnt vmcnt(3)\n\t" \
  "v_smfmac_f32_16x16x64_bf16 v[144:147], v[140:143], v[96:103], v172\n\t" \
  "v_max3_u32 v164, v140, v141, v164\n\t" \
  "v_smfmac_f32_16x16x64_bf16 v[148:151], v[140:143], v[104:111], v172\n\t" \
  "v_max3_u32 v164, v142, v143, v164\n\t" \
  "v_smfmac_f32_16x16x64_bf16 v[152:155], v[140:143], v[112:119], v172\n\t" \
  "v_cmp_le_u32 vcc, %[sent], v164\n\t" \
  "v_smfmac_f32_16x16x64_bf16 v[156:159], v[140:143], v[120:127], v172\n\t" \
  "buffer_load_dwordx4 v[188:191], v238, %[rt], %[st4] offen offset:1024 sc1\n\t" \
  "s_cbranch_vccnz .Lslow%=\n\t" \
  ".Lok%=:\n\t" \
  "v_add_f32_dpp v160, v144, v146 quad_perm:[0,1,2,3] row_mask:0x3 bank_mask:0xf\n\t" \
  "v_add_f32_dpp v161, v145, v147 quad_perm:[0,1,2,3] row_mask:0x3 bank_mask:0xf\n\t" \
  "v_add_f32_dpp v162, v148, v150 quad_perm:[0,1,2,3] row_mask:0x3 bank_mask:0xf\n\t" \
  "v_add_f32_dpp v163, v149, v151 quad_perm:[0,1,2,3] row_mask:0x3 bank_mask:0xf\n\t" \
  "v_add_f32_dpp v160, v152, v154 quad_perm:[0,1,2,3] row_mask:0xc bank_mask:0xf\n\t" \
  "v_add_f32_dpp v161, v153, v155 quad_perm:[0,1,2,3] row_mask:0xc bank_mask:0xf\n\t" \
  "v_add_f32_dpp v162, v156, v158 quad_perm:[0,1,2,3] row_mask:0xc bank_mask:0xf\n\t" \
  "v_add_f32_dpp v163, v157, v159 quad_perm:[0,1,2,3] row_mask:0xc bank_mask:0xf\n\t" \
  "s_nop 0\n\t" \
  "v_permlane32_swap_b32 v160, v162\n\t" \
  "v_permlane32_swap_b32 v161, v163\n\t" \
  "v_add_f32 v160, v160, v162\n\t" \
  "v_add_f32 v161, v161, v163\n\t" \
  "ds_write_b64 v169, v[160:161]\n\t" \
  "s_mov_b64 exec, 1\n\t" \
  "ds_inc_rtn_u32 v164, v168, v166 offset:4104\n\t" \
  "s_mov_b64 exec, -1\n\t" \
  "ds_read_b64 v[128:129], v170 offset:0\n\t" \
  "ds_read_b64 v[130:131], v170 offset:512\n\t" \
  "ds_read_b64 v[132:133], v170 offset:1024\n\t" \
  "ds_read_b64 v[134:135], v170 offset:1536\n\t" \
  "ds_read_b64 v[136:137], v170 offset:2048\n\t" \
  "ds_read_b64 v[138:139], v170 offset:2560\n\t" \
  "ds_read_b64 v[140:141], v170 offset:3072\n\t" \
  "ds_read_b64 v[142:143], v170 offset:3584\n\t" \
  "s_waitcnt lgkmcnt(8)\n\t" \
  "v_readfirstlane_b32 s93, v164\n\t" \
  "s_cmp_lg_u32 s93, 7\n\t" \
  "s_cbranch_scc1 .Lb1%=\n\t" \
  "s_waitcnt lgkmcnt(4)\n\t" \
  "v_pk_add_f32 v[128:129], v[128:129], v[130:131]\n\t" \
  "v_pk_add_f32 v[132:133], v[132:133], v[134:135]\n\t" \
  "s_waitcnt lgkmcnt(0)\n\t" \
  "v_pk_add_f32 v[136:137], v[136:137], v[138:139]\n\t" \
  "v_pk_add_f32 v[140:141], v[140:141], v[142:143]\n\t" \
  "v_pk_add_f32 v[128:129], v[128:129], v[132:133]\n\t" \
  "v_pk_add_f32 v[136:137], v[136:137], v[140:141]\n\t" \
  "v_pk_add_f32 v[128:129], v[128:129], v[136:137]\n\t" \
  "v_cvt_pk_bf16_f32 v128, v128, v129\n\t" \
  "s_nop 1\n\t" \
  "v_mov_b32_dpp v129, v128 quad_perm:[1,0,3,2] row_mask:0xf bank_mask:0xf\n\t" \
  "v_perm_b32 v128, v129, v128, v174\n\t" \
  "buffer_store_dword v128, v171, %[rs], %[sh] offen sc1\n\t" \
  "s_memtime s[90:91]\n\t" \
  "s_barrier\n\t" \
  "s_memtime s[76:77]\n\t" \
  "s_memrealtime s[78:79]\n\t" \
  "s_nop 3\n\t" \
  "s_memrealtime s[80:81]\n\t" \
  "s_nop 3\n\t" \
  "s_memrealtime s[82:83]\n\t" \
  "s_nop 3\n\t" \
  "s_memrealtime s[84:85]\n\t" \
  "s_nop 3\n\t" \
  "s_memrealtime s[86:87]\n\t" \
  "s_nop 3\n\t" \
  "s_memrealtime s[88:89]\n\t" \
  "s_mov_b32 s93, 0\n\t" \
  "s_waitcnt lgkmcnt(0)\n\t" \
  "s_cmp_eq_u32 s80, s78\n\t" \
  "s_addc_u32 s93, s93, 0\n\t" \
  "s_cmp_eq_u32 s82, s78\n\t" \
  "s_addc_u32 s93, s93, 0\n\t" \
  "s_cmp_eq_u32 s84, s78\n\t" \
  "s_addc_u32 s93, s93, 0\n\t" \
  "s_cmp_eq_u32 s86, s78\n\t" \
  "s_addc_u32 s93, s93, 0\n\t" \
  "s_cmp_eq_u32 s88, s78\n\t" \
  "s_addc_u32 s93, s93, 0\n\t" \
  "s_mul_i32 s93, s93, 21\n\t" \
  "s_add_u32 s76, s76, 15\n\t" \
  "s_add_u32 s76, s76, s93\n\t" \
  "s_lshl2_add_u32 s78, s78, 16\n\t" \
  "s_mov_b64 exec, 1\n\t" \
  "s_sub_i32 s90, s76, s90\n\t" \
  "s_mul_i32 s90, s90, 11900\n\t" \
  "s_ashr_i32 s90, s90, 16\n\t" \
  "s_sub_u32 s90, s78, s90\n\t" \
  "s_sub_u32 s90, s90, %[base]\n\t" \
  "v_mov_b32 v164, s90\n\t" \
  "ds_write_b32 v168, v164 offset:4108\n\t" \
  "s_mov_b64 exec, -1\n\t" \
  "s_waitcnt vmcnt(1)\n\t" \
  ".Lb2%=:\n\t" \
  "s_setprio 0\n\t" \
  "s_bitcmp0_b64 %[am], %[step]\n\t" \
  "s_cbranch_scc1 .La1%=\n\t" \
  "buffer_store_dwordx4 v[224:227], v228, %[ra], 0 offen sc1\n\t" \
  ".La1%=:\n\t" \
  "buffer_load_dwordx4 v[192:195], v239, %[rt], %[st] offen offset:0 sc1\n\t" \
  "buffer_load_dwordx4 v[196:199], v239, %[rt], %[st] offen offset:1024 sc1\n\t" \
  "v_mfma_f32_16x16x32_bf16 v[128:131], v[0:3], v[176:179], 0\n\t" \
  "v_mfma_f32_16x16x32_bf16 v[136:139], v[8:11], v[176:179], 0\n\t" \
  "ds_read2st64_b32 v[144:145], v231 offset0:0 offset1:8\n\t" \
  "ds_read2st64_b32 v[146:147], v231 offset0:16 offset1:24\n\t" \
  "ds_read2st64_b32 v[148:149], v231 offset0:32 offset1:40\n\t" \
  "ds_read2st64_b32 v[150:151], v231 offset0:48 offset1:56\n\t" \
  "buffer_load_dwordx4 v[200:203], v239, %[rt], %[st4] offen offset:0 sc1\n\t" \
  "buffer_load_dwordx4 v[204:207], v239, %[rt], %[st4] offen offset:1024 sc1\n\t" \
  "v_mfma_f32_16x16x32_bf16 v[132:135], v[4:7], v[180:183], 0\n\t" \
  "v_mfma_f32_16x16x32_bf16 v[140:143], v[12:15], v[180:183], 0\n\t" \
  "s_waitcnt lgkmcnt(0)\n\t" \
  "v_add_f32 v144, v144, v145\n\t" \
  "v_add_f32 v146, v146, v147\n\t" \
  "v_add_f32 v148, v148, v149\n\t" \
  "v_add_f32 v150, v150, v151\n\t" \
  "v_add_f32 v144, v144, v146\n\t" \
  "v_add_f32 v148, v148, v150\n\t" \
  "buffer_load_dwordx4 v[208:211], v240, %[rt], %[st] offen offset:0 sc1\n\t" \
  "buffer_load_dwordx4 v[212:215], v240, %[rt], %[st] offen offset:1024 sc1\n\t" \
  "v_mfma_f32_16x16x32_bf16 v[128:131], v[16:19], v[184:187], v[128:131]\n\t" \
  "v_mfma_f32_16x16x32_bf16 v[136:139], v[24:27], v[184:187], v[136:139]\n\t" \
  "v_add_f32 v144, v144, v148\n\t" \
  "v_cvt_pk_bf16_f32 v144, v144, v144\n\t" \
  "ds_write_b16 v232, v144\n\t" \
  "ds_read_b128 v[224:227], v229\n\t" \
  "v_add_u32 v228, %[ainc], v228\n\t" \
  "v_add_u32 v229, %[rinc], v229\n\t" \
  "v_add_u32 v232, 1024, v232\n\t" \
  "v_xor_b32 v231, 0x4000, v231\n\t" \
  "buffer_load_dwordx4 v[216:219], v240, %[rt], %[st4] offen offset:0 sc1\n\t" \
  "buffer_load_dwordx4 v[220:223], v240, %[rt], %[st4] offen offset:1024 sc1\n\t" \
  "v_mfma_f32_16x16x32_bf16 v[132:135], v[20:23], v[188:191], v[132:135]\n\t" \
  "v_mfma_f32_16x16x32_bf16 v[140:143], v[28:31], v[188:191], v[140:143]\n\t" \
  "buffer_load_dwordx4 v[176:179], v241, %[rt], %[st] offen offset:0 sc1\n\t" \
  "s_waitcnt vmcnt(8)\n\t" \
  "v_mfma_f32_16x16x32_bf16 v[128:131], v[32:35], v[192:195], v[128:131]\n\t" \
  "v_mfma_f32_16x16x32_bf16 v[136:139], v[40:43], v[192:195], v[136:139]\n\t" \
  "buffer_load_dwordx4 v[180:183], v241, %[rt], %[st] offen offset:1024 sc1\n\t" \
  "s_waitcnt vmcnt(8)\n\t" \
  "v_mfma_f32_16x16x32_bf16 v[132:135], v[36:39], v[196:199], v[132:135]\n\t" \
  "v_mfma_f32_16x16x32_bf16 v[140:143], v[44:47], v[196:199], v[140:143]\n\t" \
  "buffer_load_dwordx4 v[184:187], v241, %[rt], %[st4] offen offset:0 sc1\n\t" \
  "s_waitcnt vmcnt(8)\n\t" \
  "v_mfma_f32_16x16x32_bf16 v[128:131], v[48:51], v[200:203], v[128:131]\n\t" \
  "v_mfma_f32_16x16x32_bf16 v[136:139], v[56:59], v[200:203], v[136:139]\n\t" \
  "ds_read_b128 v[160:163], v168 offset:4096\n\t" \
  "buffer_load_dwordx4 v[188:191], v241, %[rt], %[st4] offen offset:1024 sc1\n\t" \
  "s_waitcnt vmcnt(8)\n\t" \
  "v_mfma_f32_16x16x32_bf16 v[132:135], v[52:55], v[204:207], v[132:135]\n\t" \
  "v_mfma_f32_16x16x32_bf16 v[140:143], v[60:63], v[204:207], v[140:143]\n\t" \
  "s_waitcnt lgkmcnt(0)\n\t" \
  "v_readfirstlane_b32 s90, v161\n\t" \
  "v_readfirstlane_b32 s91, v163\n\t" \
  "s_add_u32 s93, s90, %[rl]\n\t" \
  "s_add_u32 s93, s93, %[base]\n\t" \
  "s_add_u32 %[E], s93, %[lk]\n\t" \
  "s_sub_i32 s91, s91, s90\n\t" \
  "s_sub_i32 s91, s91, %[rl]\n\t" \
  "s_min_i32 s93, s91, %[up]\n\t" \
  "s_max_i32 s93, s93, %[dn]\n\t" \
  "s_cmp_gt_i32 s91, 60\n\t" \
  "s_cselect_b32 s93, 2, s93\n\t" \
  "s_add_i32 %[rl], %[rl], s93\n\t" \
  "s_waitcnt vmcnt(7)\n\t" \
  "v_mfma_f32_16x16x32_bf16 v[128:131], v[64:67], v[208:211], v[128:131]\n\t" \
  "v_mfma_f32_16x16x32_bf16 v[136:139], v[72:75], v[208:211], v[136:139]\n\t" \
  "s_add_u32 %[step], %[step], 1\n\t" \
  "s_lshl_b32 %[sh], %[step], 15\n\t" \
  "s_lshr_b32 s93, %[step], 4\n\t" \
  "s_lshr_b32 s93, %[pm], s93\n\t" \
  "s_and_b32 s93, s93, 4\n\t" \
  "s_lshl_b32 s93, s93, 11\n\t" \
  "s_lshl2_add_u32 %[so], %[sh], s93\n\t" \
  "s_waitcnt vmcnt(6)\n\t" \
  "v_mfma_f32_16x16x32_bf16 v[132:135], v[68:71], v[212:215], v[132:135]\n\t" \
  "v_mfma_f32_16x16x32_bf16 v[140:143], v[76:79], v[212:215], v[140:143]\n\t" \
  "s_sub_i32 s93, %[step], 3\n\t" \
  "s_lshl_b32 %[st], s93, 19\n\t" \
  "s_lshl_b32 s94, s93, 11\n\t" \
  "s_xor_b32 s94, s94, %[s0]\n\t" \
  "s_and_b32 s94, s94, 0x2000\n\t" \
  "s_or_b32 %[st], %[st], s94\n\t" \
  "s_cmp_lt_i32 s93, 0\n\t" \
  "s_cselect_b32 %[st], 0x70000000, %[st]\n\t" \
  "s_add_u32 %[st4], %[st], 4096\n\t" \
  "s_waitcnt vmcnt(5)\n\t" \
  "v_mfma_f32_16x16x32_bf16 v[128:131], v[80:83], v[216:219], v[128:131]\n\t" \
  "v_mfma_f32_16x16x32_bf16 v[136:139], v[88:91], v[216:219], v[136:139]\n\t" \
  "s_waitcnt vmcnt(4)\n\t" \
  "v_mfma_f32_16x16x32_bf16 v[132:135], v[84:87], v[220:223], v[132:135]\n\t" \
  "v_mfma_f32_16x16x32_bf16 v[140:143], v[92:95], v[220:223], v[140:143]\n\t" \
  "s_cmp_eq_u32 %[step], 63\n\t" \
  "s_cbranch_scc1 .Lgol%=\n\t" \
  "s_movk_i32 s93, 600\n\t" \
  ".Lpacel%=:\n\t" \
  "s_memrealtime s[90:91]\n\t" \
  "s_waitcnt lgkmcnt(0)\n\t" \
  "s_lshl_b32 s90, s90, 2\n\t" \
  "s_sub_i32 s91, %[E], s90\n\t" \
  "s_cmp_le_i32 s91, 0\n\t" \
  "s_cbranch_scc1 .Lgol%=\n\t" \
  "s_sub_u32 s93, s93, 1\n\t" \
  "s_cmp_lg_u32 s93, 0\n\t" \
  "s_cbranch_scc1 .Lpacel%=\n\t" \
  ".Lgol%=:\n\t" \
  "buffer_load_dwordx2 v[166:167], v173, %[rs], 0 offen sc1\n\t" \
  "s_waitcnt vmcnt(4)\n\t" \
  "v_mfma_f32_16x16x32_bf16 v[128:131], v[96:99], v[176:179], v[128:131]\n\t" \
  "v_mfma_f32_16x16x32_bf16 v[136:139], v[104:107], v[176:179], v[136:139]\n\t" \
  "s_waitcnt vmcnt(3)\n\t" \
  "v_mfma_f32_16x16x32_bf16 v[132:135], v[100:103], v[180:183], v[132:135]\n\t" \
  "v_mfma_f32_16x16x32_bf16 v[140:143], v[108:111], v[180:183], v[140:143]\n\t" \
  "s_waitcnt vmcnt(2)\n\t" \
  "v_mfma_f32_16x16x32_bf16 v[128:131], v[112:115], v[184:187], v[128:131]\n\t" \
  "v_mfma_f32_16x16x32_bf16 v[136:139], v[120:123], v[184:187], v[136:139]\n\t" \
  "s_waitcnt vmcnt(1)\n\t" \
  "v_mfma_f32_16x16x32_bf16 v[132:135], v[116:119], v[188:191], v[132:135]\n\t" \
  "v_mfma_f32_16x16x32_bf16 v[140:143], v[124:127], v[188:191], v[140:143]\n\t" \
  "v_mov_b64 v[144:145], 0\n\t" \
  "v_mov_b64 v[146:147], 0\n\t" \
  "v_mov_b64 v[148:149], 0\n\t" \
  "v_mov_b64 v[150:151], 0\n\t" \
  "v_mov_b64 v[152:153], 0\n\t" \
  "v_mov_b64 v[154:155], 0\n\t" \
  "v_mov_b64 v[156:157], 0\n\t" \
  "v_mov_b64 v[158:159], 0\n\t" \
  "v_pk_add_f32 v[128:129], v[128:129], v[132:133]\n\t" \
  "v_pk_add_f32 v[130:131], v[130:131], v[134:135]\n\t" \
  "ds_write_b128 v230, v[128:131] offset:0\n\t" \
  "v_pk_add_f32 v[136:137], v[136:137], v[140:141]\n\t" \
  "v_pk_add_f32 v[138:139], v[138:139], v[142:143]\n\t" \
  "ds_write_b128 v230, v[136:139] offset:1024\n\t" \
  "v_xor_b32 v230, 0x4000, v230\n\t" \
  "s_cmp_lg_u32 %[step], 63\n\t" \
  "s_cbranch_scc1 .Lstep%=\n\t" \
  "buffer_load_dwordx4 v[176:179], v238, %[rt], %[st] offen offset:0 sc1\n\t" \
  "buffer_load_dwordx4 v[180:183], v238, %[rt], %[st] offen offset:1024 sc1\n\t" \
  "buffer_load_dwordx4 v[184:187], v238, %[rt], %[st4] offen offset:0 sc1\n\t" \
  "buffer_load_dwordx4 v[188:191], v238, %[rt], %[st4] offen offset:1024 sc1\n\t" \
  "buffer_load_dwordx4 v[192:195], v239, %[rt], %[st] offen offset:0 sc1\n\t" \
  "buffer_load_dwordx4 v[196:199], v239, %[rt], %[st] offen offset:1024 sc1\n\t" \
  "buffer_load_dwordx4 v[200:203], v239, %[rt], %[st4] offen offset:0 sc1\n\t" \
  "buffer_load_dwordx4 v[204:207], v239, %[rt], %[st4] offen offset:1024 sc1\n\t" \
  "v_subrev_u32 v230, 98304, v230\n\t" \
  ".Lt%=:\n\t" \
  "s_waitcnt vmcnt(7)\n\t" \
  "v_mfma_f32_16x16x32_bf16 v[128:131], v[0:3], v[176:179], 0\n\t" \
  "v_mfma_f32_16x16x32_bf16 v[136:139], v[8:11], v[176:179], 0\n\t" \
  "buffer_load_dwordx4 v[176:179], v240, %[rt], %[st] offen offset:0 sc1\n\t" \
  "buffer_load_dword v166, v242, %[rs], 0 offen sc1\n\t" \
  "s_waitcnt vmcnt(8)\n\t" \
  "v_mfma_f32_16x16x32_bf16 v[132:135], v[4:7], v[180:183], 0\n\t" \
  "v_mfma_f32_16x16x32_bf16 v[140:143], v[12:15], v[180:183], 0\n\t" \
  "buffer_load_dwordx4 v[180:183], v240, %[rt], %[st] offen offset:1024 sc1\n\t" \
  "s_waitcnt vmcnt(8)\n\t" \
  "v_mfma_f32_16x16x32_bf16 v[128:131], v[16:19], v[184:187], v[128:131]\n\t" \
  "v_mfma_f32_16x16x32_bf16 v[136:139], v[24:27], v[184:187], v[136:139]\n\t" \
  "buffer_load_dwordx4 v[184:187], v240, %[rt], %[st4] offen offset:0 sc1\n\t" \
  "s_waitcnt vmcnt(8)\n\t" \
  "v_mfma_f32_16x16x32_bf16 v[132:135], v[20:23], v[188:191], v[132:135]\n\t" \
  "v_mfma_f32_16x16x32_bf16 v[140:143], v[28:31], v[188:191], v[140:143]\n\t" \
  "buffer_load_dwordx4 v[188:191], v240, %[rt], %[st4] offen offset:1024 sc1\n\t" \
  "s_waitcnt vmcnt(8)\n\t" \
  "v_mfma_f32_16x16x32_bf16 v[128:131], v[32:35], v[192:195], v[128:131]\n\t" \
  "v_mfma_f32_16x16x32_bf16 v[136:139], v[40:43], v[192:195], v[136:139]\n\t" \
  "buffer_load_dwordx4 v[192:195], v241, %[rt], %[st] offen offset:0 sc1\n\t" \
  "s_waitcnt vmcnt(8)\n\t" \
  "v_mfma_f32_16x16x32_bf16 v[132:135], v[36:39], v[196:199], v[132:135]\n\t" \
  "v_mfma_f32_16x16x32_bf16 v[140:143], v[44:47], v[196:199], v[140:143]\n\t" \
  "buffer_load_dwordx4 v[196:199], v241, %[rt], %[st] offen offset:1024 sc1\n\t" \
  "s_waitcnt vmcnt(8)\n\t" \
  "v_mfma_f32_16x16x32_bf16 v[128:131], v[48:51], v[200:203], v[128:131]\n\t" \
  "v_mfma_f32_16x16x32_bf16 v[136:139], v[56:59], v[200:203], v[136:139]\n\t" \
  "buffer_load_dwordx4 v[200:203], v241, %[rt], %[st4] offen offset:0 sc1\n\t" \
  "s_waitcnt vmcnt(8)\n\t" \
  "v_mfma_f32_16x16x32_bf16 v[132:135], v[52:55], v[204:207], v[132:135]\n\t" \
  "v_mfma_f32_16x16x32_bf16 v[140:143], v[60:63], v[204:207], v[140:143]\n\t" \
  "buffer_load_dwordx4 v[204:207], v241, %[rt], %[st4] offen offset:1024 sc1\n\t" \
  "s_waitcnt vmcnt(8)\n\t" \
  "v_mfma_f32_16x16x32_bf16 v[128:131], v[64:67], v[176:179], v[128:131]\n\t" \
  "v_mfma_f32_16x16x32_bf16 v[136:139], v[72:75], v[176:179], v[136:139]\n\t" \
  "s_waitcnt vmcnt(7)\n\t" \
  ".Ltc%=:\n\t" \
  "v_cmp_eq_u32 vcc, -1, v166\n\t" \
  "s_cbranch_vccnz .Ltw%=\n\t" \
  ".Lto%=:\n\t" \
  "s_cmp_eq_u32 %[step], 66\n\t" \
  "s_cbranch_scc1 .Ltl%=\n\t" \
  "s_add_u32 %[step], %[step], 1\n\t" \
  "s_sub_i32 s93, %[step], 3\n\t" \
  "s_lshl_b32 %[st], s93, 19\n\t" \
  "s_lshl_b32 s94, s93, 11\n\t" \
  "s_xor_b32 s94, s94, %[s0]\n\t" \
  "s_and_b32 s94, s94, 0x2000\n\t" \
  "s_or_b32 %[st], %[st], s94\n\t" \
  "s_cmp_lt_i32 s93, 0\n\t" \
  "s_cselect_b32 %[st], 0x70000000, %[st]\n\t" \
  "s_add_u32 %[st4], %[st], 4096\n\t" \
  "v_add_u32 v242, 16384, v242\n\t" \
  "s_cmp_eq_u32 %[step], 66\n\t" \
  "s_cbranch_scc0 .Ltn%=\n\t" \
  "v_mov_b32 v242, %[fg]\n\t" \
  ".Ltn%=:\n\t" \
  "buffer_load_dwordx4 v[176:179], v238, %[rt], %[st] offen offset:0 sc1\n\t" \
  "s_waitcnt vmcnt(7)\n\t" \
  "v_mfma_f32_16x16x32_bf16 v[132:135], v[68:71], v[180:183], v[132:135]\n\t" \
  "v_mfma_f32_16x16x32_bf16 v[140:143], v[76:79], v[180:183], v[140:143]\n\t" \
  "buffer_load_dwordx4 v[180:183], v238, %[rt], %[st] offen offset:1024 sc1\n\t" \
  "s_waitcnt vmcnt(7)\n\t" \
  "v_mfma_f32_16x16x32_bf16 v[128:131], v[80:83], v[184:187], v[128:131]\n\t" \
  "v_mfma_f32_16x16x32_bf16 v[136:139], v[88:91], v[184:187], v[136:139]\n\t" \
  "buffer_load_dwordx4 v[184:187], v238, %[rt], %[st4] offen offset:0 sc1\n\t" \
  "s_waitcnt vmcnt(7)\n\t" \
  "v_mfma_f32_16x16x32_bf16 v[132:135], v[84:87], v[188:191], v[132:135]\n\t" \
  "v_mfma_f32_16x16x32_bf16 v[140:143], v[92:95], v[188:191], v[140:143]\n\t" \
  "buffer_load_dwordx4 v[188:191], v238, %[rt], %[st4] offen offset:1024 sc1\n\t" \
  "s_waitcnt vmcnt(7)\n\t" \
  "v_mfma_f32_16x16x32_bf16 v[128:131], v[96:99], v[192:195], v[128:131]\n\t" \
  "v_mfma_f32_16x16x32_bf16 v[136:139], v[104:107], v[192:195], v[136:139]\n\t" \
  "buffer_load_dwordx4 v[192:195], v239, %[rt], %[st] offen offset:0 sc1\n\t" \
  "s_waitcnt vmcnt(7)\n\t" \
  "v_mfma_f32_16x16x32_bf16 v[132:135], v[100:103], v[196:199], v[132:135]\n\t" \
  "v_mfma_f32_16x16x32_bf16 v[140:143], v[108:111], v[196:199], v[140:143]\n\t" \
  "buffer_load_dwordx4 v[196:199], v239, %[rt], %[st] offen offset:1024 sc1\n\t" \
  "s_waitcnt vmcnt(7)\n\t" \
  "v_mfma_f32_16x16x32_bf16 v[128:131], v[112:115], v[200:203], v[128:131]\n\t" \
  "v_mfma_f32_16x16x32_bf16 v[136:139], v[120:123], v[200:203], v[136:139]\n\t" \
  "buffer_load_dwordx4 v[200:203], v239, %[rt], %[st4] offen offset:0 sc1\n\t" \
  "s_waitcnt vmcnt(7)\n\t" \
  "v_mfma_f32_16x16x32_bf16 v[132:135], v[116:119], v[204:207], v[132:135]\n\t" \
  "v_mfma_f32_16x16x32_bf16 v[140:143], v[124:127], v[204:207], v[140:143]\n\t" \
  "buffer_load_dwordx4 v[204:207], v239, %[rt], %[st4] offen offset:1024 sc1\n\t" \
  "s_nop 5\n\t" \
  "v_pk_add_f32 v[128:129], v[128:129], v[132:133]\n\t" \
  "v_pk_add_f32 v[130:131], v[130:131], v[134:135]\n\t" \
  "ds_write_b128 v230, v[128:131] offset:0\n\t" \
  "v_pk_add_f32 v[136:137], v[136:137], v[140:141]\n\t" \
  "v_pk_add_f32 v[138:139], v[138:139], v[142:143]\n\t" \
  "ds_write_b128 v230, v[136:139] offset:1024\n\t" \
  "v_add_u32 v230, 16384, v230\n\t" \
  "s_branch .Lt%=\n\t" \
  ".Ltl%=:\n\t" \
  "buffer_load_dwordx4 v[224:227], v243, %[rg], 0 offen sc1\n\t" \
  "s_waitcnt vmcnt(7)\n\t" \
  "v_mfma_f32_16x16x32_bf16 v[132:135], v[68:71], v[180:183], v[132:135]\n\t" \
  "v_mfma_f32_16x16x32_bf16 v[140:143], v[76:79], v[180:183], v[140:143]\n\t" \
  "s_waitcnt vmcnt(6)\n\t" \
  "v_mfma_f32_16x16x32_bf16 v[128:131], v[80:83], v[184:187], v[128:131]\n\t" \
  "v_mfma_f32_16x16x32_bf16 v[136:139], v[88:91], v[184:187], v[136:139]\n\t" \
  "s_waitcnt vmcnt(5)\n\t" \
  "v_mfma_f32_16x16x32_bf16 v[132:135], v[84:87], v[188:191], v[132:135]\n\t" \
  "v_mfma_f32_16x16x32_bf16 v[140:143], v[92:95], v[188:191], v[140:143]\n\t" \
  "s_waitcnt vmcnt(4)\n\t" \
  "v_mfma_f32_16x16x32_bf16 v[128:131], v[96:99], v[192:195], v[128:131]\n\t" \
  "v_mfma_f32_16x16x32_bf16 v[136:139], v[104:107], v[192:195], v[136:139]\n\t" \
  "s_waitcnt vmcnt(3)\n\t" \
  "v_mfma_f32_16x16x32_bf16 v[132:135], v[100:103], v[196:199], v[132:135]\n\t" \
  "v_mfma_f32_16x16x32_bf16 v[140:143], v[108:111], v[196:199], v[140:143]\n\t" \
  "s_waitcnt vmcnt(2)\n\t" \
  "v_mfma_f32_16x16x32_bf16 v[128:131], v[112:115], v[200:203], v[128:131]\n\t" \
  "v_mfma_f32_16x16x32_bf16 v[136:139], v[120:123], v[200:203], v[136:139]\n\t" \
  "s_waitcnt vmcnt(1)\n\t" \
  "v_mfma_f32_16x16x32_bf16 v[132:135], v[116:119], v[204:207], v[132:135]\n\t" \
  "v_mfma_f32_16x16x32_bf16 v[140:143], v[124:127], v[204:207], v[140:143]\n\t" \
  "s_nop 6\n\t" \
  "v_pk_add_f32 v[128:129], v[128:129], v[132:133]\n\t" \
  "v_pk_add_f32 v[130:131], v[130:131], v[134:135]\n\t" \
  "ds_write_b128 v230, v[128:131] offset:0\n\t" \
  "v_pk_add_f32 v[136:137], v[136:137], v[140:141]\n\t" \
  "v_pk_add_f32 v[138:139], v[138:139], v[142:143]\n\t" \
  "ds_write_b128 v230, v[136:139] offset:1024\n\t" \
  "s_waitcnt vmcnt(0) lgkmcnt(0)\n\t" \
  "s_branch .Lend%=\n\t" \
  ".Ltw%=:\n\t" \
  "s_add_u32 s92, s92, 1\n\t" \
  "s_sleep 2\n\t" \
  "s_and_b32 s93, s92, 63\n\t" \
  "s_cbranch_scc1 .Ltr%=\n\t" \
  "buffer_load_dword v164, off, %[rs], %[sab] sc1\n\t" \
  "s_waitcnt vmcnt(0)\n\t" \
  "v_readfirstlane_b32 s93, v164\n\t" \
  "s_nop 3\n\t" \
  "s_cmp_eq_u32 s93, 1\n\t" \
  "s_cbranch_scc1 .Lto%=\n\t" \
  "s_cmp_lt_u32 s92, 0x80000\n\t" \
  "s_cbranch_scc1 .Ltr%=\n\t" \
  "v_mov_b32 v164, 1\n\t" \
  "buffer_store_dword v164, off, %[rs], %[sab] sc1\n\t" \
  "s_waitcnt vmcnt(0)\n\t" \
  "s_branch .Lto%=\n\t" \
  ".Ltr%=:\n\t" \
  "buffer_load_dword v166, v242, %[rs], 0 offen sc1\n\t" \
  "s_waitcnt vmcnt(0)\n\t" \
  "s_branch .Ltc%=\n\t" \
  ".Lb1%=:\n\t" \
  "s_waitcnt vmcnt(0) lgkmcnt(0)\n\t" \
  "s_barrier\n\t" \
  "s_branch .Lb2%=\n\t" \
  ".Lbl%=:\n\t" \
  "s_bitset1_b64 %[ps], %[step]\n\t" \
  "s_cmp_lt_u32 s92, 3\n\t" \
  "s_cbranch_scc1 .Lbm%=\n\t" \
  "s_sleep 12\n\t" \
  ".Lbm%=:\n\t" \
  "s_add_u32 s92, s92, 1\n\t" \
  "s_sleep 2\n\t" \
  "s_and_b32 s93, s92, 63\n\t" \
  "s_cbranch_scc1 .Lba%=\n\t" \
  "buffer_load_dword v164, off, %[rs], %[sab] sc1\n\t" \
  "s_waitcnt vmcnt(0)\n\t" \
  "v_readfirstlane_b32 s93, v164\n\t" \
  "s_nop 3\n\t" \
  "s_cmp_eq_u32 s93, 1\n\t" \
  "s_cbranch_scc1 .Lgo%=\n\t" \
  "s_cmp_lt_u32 s92, 0x80000\n\t" \
  "s_cbranch_scc1 .Lba%=\n\t" \
  "v_mov_b32 v164, 1\n\t" \
  "buffer_store_dword v164, off, %[rs], %[sab] sc1\n\t" \
  "s_waitcnt vmcnt(0)\n\t" \
  "s_branch .Lgo%=\n\t" \
  ".Lba%=:\n\t" \
  "buffer_load_dwordx2 v[166:167], v173, %[rs], 0 offen sc1\n\t" \
  "s_branch .Lcal%=\n\t" \
  ".Lslow%=:\n\t" \
  "s_mov_b64 s[94:95], 0\n\t" \
  "v_cmp_eq_u32 vcc, %[sent], v128\n\t" \
  "s_or_b64 s[94:95], s[94:95], vcc\n\t" \
  "v_cmp_eq_u32 vcc, %[sent], v129\n\t" \
  "s_or_b64 s[94:95], s[94:95], vcc\n\t" \
  "v_cmp_eq_u32 vcc, %[sent], v130\n\t" \
  "s_or_b64 s[94:95], s[94:95], vcc\n\t" \
  "v_cmp_eq_u32 vcc, %[sent], v131\n\t" \
  "s_or_b64 s[94:95], s[94:95], vcc\n\t" \
  "v_cmp_eq_u32 vcc, %[sent], v132\n\t" \
  "s_or_b64 s[94:95], s[94:95], vcc\n\t" \
  "v_cmp_eq_u32 vcc, %[sent], v133\n\t" \
  "s_or_b64 s[94:95], s[94:95], vcc\n\t" \
  "v_cmp_eq_u32 vcc, %[sent], v134\n\t" \
  "s_or_b64 s[94:95], s[94:95], vcc\n\t" \
  "v_cmp_eq_u32 vcc, %[sent], v135\n\t" \
  "s_or_b64 s[94:95], s[94:95], vcc\n\t" \
  "v_cmp_eq_u32 vcc, %[sent], v136\n\t" \
  "s_or_b64 s[94:95], s[94:95], vcc\n\t" \
  "v_cmp_eq_u32 vcc, %[sent], v137\n\t" \
  "s_or_b64 s[94:95], s[94:95], vcc\n\t" \
  "v_cmp_eq_u32 vcc, %[sent], v138\n\t" \
  "s_or_b64 s[94:95], s[94:95], vcc\n\t" \
  "v_cmp_eq_u32 vcc, %[sent], v139\n\t" \
  "s_or_b64 s[94:95], s[94:95], vcc\n\t" \
  "v_cmp_eq_u32 vcc, %[sent], v140\n\t" \
  "s_or_b64 s[94:95], s[94:95], vcc\n\t" \
  "v_cmp_eq_u32 vcc, %[sent], v141\n\t" \
  "s_or_b64 s[94:95], s[94:95], vcc\n\t" \
  "v_cmp_eq_u32 vcc, %[sent], v142\n\t" \
  "s_or_b64 s[94:95], s[94:95], vcc\n\t" \
  "v_cmp_eq_u32 vcc, %[sent], v143\n\t" \
  "s_or_b64 s[94:95], s[94:95], vcc\n\t" \
  "s_cbranch_scc0 .Lok%=\n\t" \
  "s_bitset1_b64 %[fm], %[step]\n\t" \
  "s_add_u32 s92, s92, 1\n\t" \
  "s_sleep 1\n\t" \
  "s_and_b32 s93, s92, 63\n\t" \
  "s_cbranch_scc1 .Lre%=\n\t" \
  "buffer_load_dword v164, off, %[rs], %[sab] sc1\n\t" \
  "s_waitcnt vmcnt(0)\n\t" \
  "v_readfirstlane_b32 s93, v164\n\t" \
  "s_nop 3\n\t" \
  "s_cmp_eq_u32 s93, 1\n\t" \
  "s_cbranch_scc1 .Lok%=\n\t" \
  "s_cmp_lt_u32 s92, 0x80000\n\t" \
  "s_cbranch_scc1 .Lre%=\n\t" \
  "v_mov_b32 v164, 1\n\t" \
  "buffer_store_dword v164, off, %[rs], %[sab] sc1\n\t" \
  "s_waitcnt vmcnt(0)\n\t" \
  "s_branch .Lok%=\n\t" \
  ".Lre%=:\n\t" \
  "buffer_load_dwordx4 v[128:131], v234, %[rs], %[so] offen sc1\n\t" \
  "buffer_load_dwordx4 v[132:135], v235, %[rs], %[so] offen sc1\n\t" \
  "buffer_load_dwordx4 v[136:139], v236, %[rs], %[so] offen sc1\n\t" \
  "buffer_load_dwordx4 v[140:143], v237, %[rs], %[so] offen sc1\n\t" \
  "v_mov_b64 v[144:145], 0\n\t" \
  "v_mov_b64 v[146:147], 0\n\t" \
  "v_mov_b64 v[148:149], 0\n\t" \
  "v_mov_b64 v[150:151], 0\n\t" \
  "v_mov_b64 v[152:153], 0\n\t" \
  "v_mov_b64 v[154:155], 0\n\t" \
  "v_mov_b64 v[156:157], 0\n\t" \
  "v_mov_b64 v[158:159], 0\n\t" \
  "s_waitcnt vmcnt(0)\n\t" \
  "s_branch .Lw%=\n\t" \
  ".Lend%=:\n\t"

typedef __attribute__((ext_vector_type(32))) unsigned u32x32;
__device__ __forceinline__ void put4(u32x32& W, int i, bf16x8 f) {
  const u32x4 v = __builtin_bit_cast(u32x4, f);
#pragma unroll
  for (int j = 0; j < 4; ++j) W[4 * i + j] = v[j];
}
__device__ __forceinline__ bf16x8 get4(const u32x32& W, int i) {
  return as_bf16x8((u32x4){W[4 * i], W[4 * i + 1], W[4 * i + 2], W[4 * i + 3]});
}
__device__ __forceinline__ int look_prog(__amdgpu_buffer_rsrc_t rs, int off) {
  asm volatile("" ::: "memory");
  const int v = (int)ld_flag(rs, off);
  return min(__builtin_amdgcn_readlane(v, 0), __builtin_amdgcn_readlane(v, 1));
}
__device__ __forceinline__ int wait_prog(__amdgpu_buffer_rsrc_t rs, int off, int need, gu32* abortw) {
  int v;
  for (unsigned spins = 0;;) {
    v = look_prog(rs, off);
    if (v >= need) break;
    __builtin_amdgcn_s_sleep(4);
    if (spin_fail(spins, abortw)) break;
  }
  asm volatile("" ::: "memory");
  return v;
}
#define GSL 1
__device__ __forceinline__ unsigned get_target(const char* lds, int s) {
  if (s == 0) return now_q();
  lds_vu_ptr tg = lds_at(lds);
#pragma unroll 1
  for (int i = 0; i < 8192; ++i) {
    if (__builtin_amdgcn_readfirstlane(tg[0]) == (unsigned)s) break;
    __builtin_amdgcn_s_sleep(GSL);
  }
  const unsigned t = __builtin_amdgcn_readfirstlane(tg[1]);
#pragma unroll 1
  for (int i = 0; i < 256 && (int)(t - now_q()) > 128; ++i) __builtin_amdgcn_s_sleep(2);
  return t;
}
#define CLB8(a, b, c, d, e, f, g, h) "v" #a, "v" #b, "v" #c, "v" #d, "v" #e, "v" #f, "v" #g, "v" #h
#define CLB_S "s90", "s91", "s92", "s93", "s94", "s95", "vcc", "scc", "memory"
#define W_IN "{v[0:31]}"(W[0]), "{v[32:63]}"(W[1]), "{v[64:95]}"(W[2]), "{v[96:127]}"(W[3]), "{v[128:159]}"(W[4]), "{v[160:191]}"(W[5])

#define W_DELAY 60
#define J0 0
#define QBJ 3
constexpr int L_MEM = 0, L_KT = 16384, L_QX = 24576, L_KX = 26624, L_VX = 16384, L_MISC = 30720, L_WL = 32768;

#define QK_STEP(TXT)                                                                                                        \
  asm volatile(TXT                                                                                                          \
      : [tout] "=&s"(tout), [spins] "=&s"(spins), [tb1] "=&s"(tb1), "+{v248}"(aL16), "+{v249}"(aMW), "+{v250}"(aWL),                            \
        "+{v251}"(aIDX), "+{v252}"(aQW), "+{v[232:235]}"(QF)                                                                 \
      : W_IN, [rs] "s"(rs4), [som] "s"(o_mem(pm, s) + (WV >> 2) * 16384 + (WV & 3) * 2048), [tgt] "s"(tgt), [sent] "s"(DSENT), [sab] "s"(SY_ABORT), \
        [lead] "s"(lead), [step] "s"(s), [sbc] "s"(SY_BA + side(pm, 1) + h * 128), [nh] "s"(0xFF00FF00FF00FF00ull), [up] "s"(0xFFFFFFFF00000000ull), [negc] "s"(-cexp),      \
        [c] "s"(cexp), [so] "s"(o_o(pm, s, h) + WV * 512), [sk] "s"(o_v(pm, s, h) + 2048 + (WV & 3) * 512),                    \
        [sv] "s"(o_v(pm, s, h) + WV * 512), [rt] "s"(rt4), [svt] "s"(t_off(p, T_V + kseg(s, h)) + WV * 2048)                  \
      : CLB8(192, 193, 194, 195, 196, 197, 198, 199), CLB8(200, 201, 202, 203, 204, 205, 206, 207),                          \
        CLB8(208, 209, 210, 211, 212, 213, 214, 215), CLB8(216, 217, 218, 219, 220, 221, 222, 223),                          \
        CLB8(224, 225, 226, 227, 228, 229, 230, 231), CLB8(236, 237, 238, 239, 240, 241, 242, 243), "v244", "v245", "v246", "v247", "v253", CLB_S)

template <int WV>
__device__ __forceinline__ void qk_wave(Args& p, int h, char* smem) {
  const int tid = threadIdx.x;
  unsigned pm = 0;
  int have = -1;
  u32x32 W[6];
  {
    int lane = tid & 63;
    asm volatile("" : "+v"(lane));
    const unsigned vo = (WV * 4 + (lane >> 4)) * (HD * 2) + 16 * ((lane & 15) ^ swzB((WV * 4 + (lane >> 4)) & 15));
    auto stage = [&](int j) {
#pragma unroll
      for (int mm = 0; mm < 2; ++mm) {
        const char* src = (const char*)((mm ? p.Wk : p.Wq) + (size_t)((j + J0) & 7) * 128 * HD + h * DH);
        const unsigned dst = lds_off(smem) + L_WL + (j & 1) * 65536 + mm * 32768 + WV * 1024;
        glds2(src, 32 * HD * 2, vo, dst);
        glds2(src + 64 * HD * 2, 32 * HD * 2, vo, dst + 16384);
      }
    };
    bf16x8 keep[4][4];
    stage(0); stage(1);
    Boot bt;
#pragma unroll
    for (int j = 0; j < 8; ++j) {
      if (j < 7) WAIT_V(8); else WAIT_V(0);
      if (WV == 7 && j == QBJ + 1) pm = boot_done(p.ws, bt);
      LBAR();
#pragma unroll
      for (int k = 0; k < 8; ++k) {
        const int sl = (2 * (j + J0) + (k >> 2) - 2 * WV) & 15;
        const bf16x8 fr = ld_bfrag(smem + L_WL + (j & 1) * 65536 + (WV >> 2) * 32768 + (k >> 2) * 16384, k & 1, 2 * (WV & 3) + ((k >> 1) & 1), lane);
        const int f = ((sl >> 2) * 3 + (sl & 3)) * 4 + (k & 3);
        if ((sl & 3) != 3) put4(W[f >> 3], f & 7, fr);
        else if (j < 2) *reinterpret_cast<bf16x8*>(smem + WV * 4096 + (k & 3) * 1024 + lane * 16) = fr;
        else keep[sl >> 2][k & 3] = fr;
      }
      LBAR();
      if (WV == 7 && j == QBJ) boot_ask(p.ws, bt);
      if (j + 2 < 8) stage(j + 2);
    }
    constexpr int PK = (((1 - 2 * WV) & 3) == 3 ? (2 * J0 + 1 - 2 * WV) & 15 : (2 * J0 + 3 - 2 * WV) & 15) >> 2;
#pragma unroll
    for (int u = 0; u < 2; ++u) {
#pragma unroll
      for (int i = 0; i < 16; ++i) {
        if (((i >> 2) == PK) != (u == 1)) continue;
        *reinterpret_cast<bf16x8*>(smem + L_WL + WV * 16384 + (i >> 2) * 4096 + (i & 1) * 2048 + ((i >> 1) & 1) * 1024 + lane * 16) =
            u ? ldsf(smem + WV * 4096 + (i & 3) * 1024 + lane * 16) : keep[i >> 2][i & 3];
      }
      SCHED();
    }
    if (WV == 7 && lane < 8) lds_at(smem + L_MISC)[lane] = lane == 7 ? pm : 0;
    LBAR();
    pm = uni(lds_at(smem + L_MISC)[7]);
    p.s0 = pm >> 31;
    RDY(h);
    if (WV == 4 || WV == 5) {
      have = wait_prog(mkrsrc(p.ws + WS_SY, SY_TOTAL), fkq(h, (lane & 1) * 2), 0, (gu32*)(p.ws + WS_SY + SY_ABORT));
      const char* src = p.ws + WS_T + t_off(p, kseg(0, h)) + (WV - 4) * 4096 + lane * 16;
#pragma unroll
      for (int i = 0; i < 4; ++i) glds16_sc1(src + i * 1024, smem + L_KT + (WV - 4) * 4096 + i * 1024);
    }
  }
  const __amdgpu_buffer_rsrc_t rsy = mkrsrc(p.ws + WS_SY, SY_TOTAL);
  const u32x4 rs4 = raw_rsrc(p.ws + WS_SY, SY_TOTAL);
  const u32x4 rt4 = raw_rsrc(p.ws + WS_T, T_BYTES);
  gu32* abortw = (gu32*)(p.ws + WS_SY + SY_ABORT);
  const float cexp = 0.08838834764831845f * 1.4426950408889634f;
  constexpr int m = WV >> 2, b = WV & 3;
  int lead = 380;
  unsigned long long fm = 0, ps = 0; (void)fm; (void)ps;
  for (int s = 0; s < NSEG; ++s) {
    int lane = tid & 63;
    asm volatile("" : "+v"(lane));
    const int r = lane & 15, q = lane >> 4;
    unsigned aL16 = lane * 16, aMW = aL16 + WV * 2048, aWL = aL16 + L_WL + WV * 16384;
    unsigned aIDX = (lane & 2) ? 0xEEEE : 0x4444;
    unsigned aQW = (32 * b + r) * 16 + q * 4 + (m ? L_KX : L_QX);
    u32x4 QF;
    asm volatile("" : "={v[232:235]}"(QF));
    if (WV < 4) {
      QF[0] = L_QX + (q * 8 + (r >> 2)) * 16 + (r & 1) * 8;
      QF[1] = ((2 * (q & 1) + (q >> 1)) * 16 + r) * 4;
      QF[2] = ((r >> 2) & 1) * 65536 + ((r >> 1) & 1) * 64 + (r >> 3) * 32 + (r & 1) * 16 + q * 4;
    } else QF[3] = r * 16 + q * 4;
    const unsigned tgt = get_target(smem + L_MISC, s);
    unsigned tout, spins, tb1;
    if (WV == 0) QK_STEP(QK_ASM_0); else if (WV == 1) QK_STEP(QK_ASM_1); else if (WV == 2) QK_STEP(QK_ASM_2);
    else if (WV == 3) QK_STEP(QK_ASM_3); else if (WV == 4) QK_STEP(QK_ASM_4); else if (WV == 5) QK_STEP(QK_ASM_5);
    else if (WV == 6) QK_STEP(QK_ASM_6); else QK_STEP(QK_ASM_7);
    tout = __builtin_amdgcn_readfirstlane(tout);
    lane = tid & 63;
    asm volatile("" : "+v"(lane));
    if (WV < 4) {
      lds_at(smem + L_MISC + 8)[WV] = s + 1;
      if (WV == 3) {
        const int x = (int)(time_of(tout) - __builtin_amdgcn_readfirstlane(tb1));
        lead = __builtin_amdgcn_readfirstlane(track(lead, x, LEAD_UP, s < 4 ? 40 : LEAD_DN, 60));
        SMP(2, s, x); SMP(3, s, lead);
      }
    }
    if (WV >= 4 && WV <= 6 && s + 1 < NSEG) {
      unsigned t;
      do {
        __builtin_amdgcn_s_sleep(1);
        t = lds_at(smem + L_MISC + 8)[lane & 3];
      } while (!__all(t == (unsigned)(s + 1)));
      if (WV <= 5) {
        if (s + 2 < NSEG) {
          if (have < s + 1) have = wait_prog(rsy, fkq(h, (lane & 1) * 2), s + 1, abortw);
          const char* src = p.ws + WS_T + t_off(p, kseg(s + 1, h)) + (WV - 4) * 4096 + lane * 16;
#pragma unroll
          for (int i = 0; i < 4; ++i) glds16_sc1(src + i * 1024, smem + L_KT + (WV - 4) * 4096 + i * 1024);
          if (have < s + 2) have = look_prog(rsy, fkq(h, (lane & 1) * 2));
        }
      } else {
        watch(rsy, s, lane, now_q() + W_DELAY, (const unsigned*)(smem + L_MISC), abortw, side(pm, 1));
      }
    }
  }
}

__device__ __forceinline__ void qk_role(Args& p, int h, char* smem) {
  switch (__builtin_amdgcn_readfirstlane(threadIdx.x >> 6)) {
    case 0: qk_wave<0>(p, h, smem); break;
    case 1: qk_wave<1>(p, h, smem); break;
    case 2: qk_wave<2>(p, h, smem); break;
    case 3: qk_wave<3>(p, h, smem); break;
    case 4: qk_wave<4>(p, h, smem); break;
    case 5: qk_wave<5>(p, h, smem); break;
    case 6: qk_wave<6>(p, h, smem); break;
    default: qk_wave<7>(p, h, smem); break;
  }
}

#define CLB32(a) CLB32_##a
#define CLB32_128 CLB8(128, 129, 130, 131, 132, 133, 134, 135), CLB8(136, 137, 138, 139, 140, 141, 142, 143), CLB8(144, 145, 146, 147, 148, 149, 150, 151), CLB8(152, 153, 154, 155, 156, 157, 158, 159)
#define CLB32_160 CLB8(160, 161, 162, 163, 164, 165, 166, 167), CLB8(168, 169, 170, 171, 172, 173, 174, 175), CLB8(176, 177, 178, 179, 180, 181, 182, 183), CLB8(184, 185, 186, 187, 188, 189, 190, 191)
#define W4_IN "{v[0:31]}"(W[0]), "{v[32:63]}"(W[1]), "{v[64:95]}"(W[2]), "{v[96:127]}"(W[3])
#define LEAD_V 200

constexpr int V_VM = 16384, V_T = 32768, V_KI = 131072;
__device__ __forceinline__ void tok_rows(const Args& p, __amdgpu_buffer_rsrc_t rsy, unsigned pm, int sg, int h, int T, int eh, char* smem, char* img,
                                         int lane, gu32* abortw, unsigned tk) {
  const float scale = 0.08838834764831845f, LOG2E = 1.4426950408889634f;
  const __amdgpu_buffer_rsrc_t rt = mkrsrc(p.ws + WS_T, T_BYTES);
  asm volatile("" : "+v"(lane));
  const int r = lane & 15, q = lane >> 4;
  u32x4 km[2];
  if (tk) pace_until(tk);
#pragma unroll
  for (int i = 0; i < 2; ++i) km[i] = __builtin_amdgcn_raw_buffer_load_b128(rsy, lane * 16 + i * 1024, o_v(pm, sg, h) + 2048, 16);
  SCHED();
  const char* tl = smem + V_T + (sg & 3) * 24576 + lane * 16;
  bf16x8 qt[4], vt[4];
  unsigned vm[4];
  f32x4 d0 = {0.f, 0.f, 0.f, 0.f}, d1 = d0, d2 = d0;
#pragma unroll
  for (int i = 0; i < 4; ++i) qt[i] = ldsf(tl + 8192 + T * 4096 + i * 1024);
  {
    bf16x8 kf[8];
#pragma unroll
    for (int i = 0; i < 8; ++i) kf[i] = ldsf(tl + i * 1024);
    SCHED();
#pragma unroll
    for (int bb = 0; bb < 4; ++bb) { d0 = MFMA(kf[bb], qt[bb], d0); d1 = MFMA(kf[4 + bb], qt[bb], d1); }
  }
#pragma unroll
  for (int i = 0; i < 4; ++i) {
    vt[i] = ldsf(tl + 16384 + eh * 4096 + i * 1024);
    vm[i] = *reinterpret_cast<const unsigned*>(smem + V_VM + (sg & 1) * 2048 + eh * 1024 + i * 256 + lane * 4);
  }
  SCHED();
  for (unsigned sp = 0; !__all(!has_ds(km[0]) && !has_ds(km[1]));) {
    __builtin_amdgcn_s_sleep(4);
    if (spin_fail(sp, abortw)) break;
    asm volatile("" ::: "memory");
#pragma unroll
    for (int i = 0; i < 2; ++i) km[i] = __builtin_amdgcn_raw_buffer_load_b128(rsy, lane * 16 + i * 1024, o_v(pm, sg, h) + 2048, 16);
  }
#pragma unroll
  for (int i = 0; i < 2; ++i)
#pragma unroll
    for (int j = 0; j < 4; ++j) *reinterpret_cast<unsigned*>(img + (i * 64 + lane) * 32 + j * 8) = km[i][j];
  asm volatile("s_waitcnt lgkmcnt(0)" ::: "memory");
  const char* lk = img + (q * 8 + (r >> 2)) * 32 + (r & 3) * 8;
  {
    bf16x8 kk[4];
#pragma unroll
    for (int bb = 0; bb < 4; ++bb) {
      s16x4 lo = __builtin_amdgcn_ds_read_tr16_b64_v4i16((lds_s16x4_ptr)(lk + bb * 1024));
      s16x4 hi = __builtin_amdgcn_ds_read_tr16_b64_v4i16((lds_s16x4_ptr)(lk + bb * 1024 + 128));
      kk[bb] = (bf16x8){lo[0], lo[1], lo[2], lo[3], hi[0], hi[1], hi[2], hi[3]};
    }
    SCHED();
#pragma unroll
    for (int bb = 0; bb < 4; ++bb) d2 = MFMA(kk[bb], qt[bb], d2);
  }
  const int qi = 16 * T + r;
  float v[10];
#pragma unroll
  for (int j = 0; j < 4; ++j) { v[j] = d0[j]; v[4 + j] = d1[j]; }
  v[8] = d2[0]; v[9] = d2[1];
#pragma unroll
  for (int j = 0; j < 5; ++j) {
    unsigned t = cvtpk(v[2 * j], v[2 * j + 1]);
    t = cvtpk(lo_f(t) * scale, hi_f(t) * scale);
    v[2 * j] = lo_f(t); v[2 * j + 1] = hi_f(t);
  }
#pragma unroll
  for (int j = 0; j < 8; ++j) {
    const int key = (j >> 2) * 16 + 4 * q + (j & 3);
    v[j] = key <= qi ? v[j] : -INFINITY;
  }
  float mx = v[0];
#pragma unroll
  for (int j = 1; j < 10; ++j) mx = fmax2(mx, v[j]);
  mx = rowq_max(mx) * LOG2E;
  float sm = 0.f;
#pragma unroll
  for (int j = 0; j < 10; ++j) { v[j] = __builtin_amdgcn_exp2f(__builtin_fmaf(v[j], LOG2E, -mx)); sm += v[j]; }
  const float inv = __builtin_amdgcn_rcpf(rowq_sum(sm));
  u32x4 o = {cvtpk(v[0] * inv, v[1] * inv), cvtpk(v[2] * inv, v[3] * inv), cvtpk(v[4] * inv, v[5] * inv),
             cvtpk(v[6] * inv, v[7] * inv)};
  const s16x4 pmem = __builtin_bit_cast(s16x4, (u32x2){cvtpk(v[8] * inv, v[9] * inv), 0u});
  const int ob = t_off_o(p, T_O + (sg * 2 + T) * 16 + (h >> 1)) + (h & 1) * 4096 + eh * 2048;
#pragma unroll
  for (int j = 0; j < 2; ++j) {
    f32x4 d[2];
#pragma unroll
    for (int u = 0; u < 2; ++u) {
      d[u] = MFMA(vt[2 * j + u], as_bf16x8(o), ((f32x4){0.f, 0.f, 0.f, 0.f}));
      d[u] += __builtin_amdgcn_mfma_f32_16x16x16bf16_1k(__builtin_bit_cast(s16x4, (u32x2){vm[2 * j + u], 0u}), pmem, (f32x4){0.f, 0.f, 0.f, 0.f}, 0, 0, 0);
    }
    st_sc1(rt, ob + lane * 16 + j * 1024, pack32(d[0], d[1]));
  }
}

__device__ __forceinline__ void vu_role(Args& p, int h, char* smem) {
  const int tid = threadIdx.x, lane = tid & 63;
  const int w = __builtin_amdgcn_readfirstlane(tid >> 6);
  u32x32 W[4];
  {
    const unsigned vo = (tid >> 4) * (HD * 2) + 16 * ((lane & 15) ^ swzB((tid >> 4) & 15));
    auto stage = [&](int j) {
      const char* src = (const char*)(p.Wv + (size_t)j * 128 * HD + h * DH);
      const unsigned dst = lds_off(smem) + L_WL + (j & 3) * 32768 + w * 1024;
      glds2(src, 32 * HD * 2, vo, dst);
      glds2(src + 64 * HD * 2, 32 * HD * 2, vo, dst + 16384);
    };
    probe_page(p.ws, h, smem);
#pragma unroll
    for (int j = 0; j < 4; ++j) stage(j);
    Boot bt;
#pragma unroll
    for (int j = 0; j < 8; ++j) {
      if (j < 5) WAIT_V(12); else if (j == 5) WAIT_V(8); else if (j == 6) WAIT_V(4); else WAIT_V(0);
      if (j == 7 && w == 7) lds_at(smem + L_MISC)[7] = boot_done(p.ws, bt);
      LBAR();
#pragma unroll
      for (int k = 0; k < 4; ++k) put4(W[j >> 1], (j & 1) * 4 + k, ld_bfrag(smem + L_WL + (j & 3) * 32768 + (k >> 1) * 16384, k & 1, w, lane));
      LBAR();
      if (j + 4 < 8) stage(j + 4);
      if (j == 5 && w == 7) boot_ask(p.ws, bt);
    }
    if (tid < 4) reinterpret_cast<unsigned*>(smem + L_MISC)[tid] = 0;
    RDY(32 + h);
  }
  const __amdgpu_buffer_rsrc_t rsy = mkrsrc(p.ws + WS_SY, SY_TOTAL);
  const u32x4 rs4 = raw_rsrc(p.ws + WS_SY, SY_TOTAL);
  gu32* abortw = (gu32*)(p.ws + WS_SY + SY_ABORT);
  const unsigned pm = uni(lds_at(smem + L_MISC)[7]);
  p.s0 = pm >> 31;
  unsigned long long fm = 0; (void)fm;
  int have = -1;
  const int fo = SY_FO + ((w & 3) * NH + h) * 128;
#pragma unroll 1
  for (int s = 0; s < NSEG; ++s) {
    const unsigned tgt = get_target(smem + L_MISC, s);
    unsigned spins;
    asm volatile(VV_ASM
        : [spins] "=&s"(spins)
        : W4_IN, "{v200}"(lane * 16), "{v201}"(lane * 16 + w * 2048), "{v202}"((lane & 2) ? 0xEEEE : 0x4444), "{v203}"(lane * 4),
          "{v204}"(tid * 4 + V_VM + (s & 1) * 2048), [rs] "s"(rs4), [som] "s"(o_mem(pm, s) + (w >> 2) * 16384 + (w & 3) * 2048), [tgt] "s"(tgt),
          [sent] "s"(DSENT), [sab] "s"(SY_ABORT), [sv] "s"(o_v(pm, s, h) + w * 256)
        : CLB32(128), CLB32(160), CLB8(192, 193, 194, 195, 196, 197, 198, 199), CLB_S);
    if (w < 4 ? s >= 1 : s == NSEG - 1) {
      if (s == NSEG - 1 && w < 4 && lane == 0) __builtin_amdgcn_raw_buffer_store_b32(1u, rsy, fo, 0, 16);
      tok_rows(p, rsy, pm, s - (w < 4), h, w & 1, (w >> 1) & 1, smem, smem + V_KI + w * 4096, lane, abortw, w < 4 ? 0 : now_q() + 160);
    } else if (w == 7) {
      SMP(6, s, (int)(now_q() - tgt));
      watch(rsy, s, lane, now_q() + LEAD_V + W_DELAY, (const unsigned*)(smem + L_MISC), abortw, side(pm, 1));
    } else if (w >= 4) {
      __builtin_amdgcn_s_sleep(2);
      const int s1 = w == 5 ? (s == NSEG - 2 ? s + 1 : s) : s + 1;
#pragma unroll 1
      for (int sg = (w == 5 || s == 0) ? s : s + 1; sg <= s1; ++sg) {
        if (have < sg) have = wait_prog(rsy, fkq(h, w - 4), sg, abortw);
        const char* src = p.ws + WS_T + t_off(p, (w - 4) * T_Q + kseg(sg, h)) + lane * 16;
#pragma unroll
        for (int i = 0; i < 8; ++i) glds16_sc1(src + i * 1024, smem + V_T + (sg & 3) * 24576 + (w - 4) * 8192 + i * 1024);
      }
      if (have < s1 + 1) have = look_prog(rsy, fkq(h, w - 4));
      if (w == 5 && s + NPRE < NSEG) {
#pragma unroll
        for (int i = 0; i < 4; ++i) st_sc1(rsy, o_v(pm, s + NPRE, h) + i * 1024 + lane * 16, (u32x4){DSENT, DSENT, DSENT, DSENT});
      }
    }
  }
  WAIT_V(0);
  if (lane == 0) __builtin_amdgcn_raw_buffer_store_b32(1u, rsy, fo + (1 + (w >> 2)) * 4 * NH * 128, 0, 16);
  if (h == 3 && (w & 2) == 0) { LST(351 + (w & 1) + (w >> 2) * 2); }
}

#define RL_UP 12
#define RL_DN 4
#define RED_MIN 40
#define BA_LOOK 310
constexpr int TLAG = 3;
constexpr int R_SI = 32768, R_PART = 98304;

__device__ __forceinline__ void red_role(Args& p, int j, char* smem) {
  const int tid = threadIdx.x;
  int lane = tid & 63;
  const int w = __builtin_amdgcn_readfirstlane(tid >> 6);
  const int c = (j & 2) * 8 + (j >> 2), rh = j & 1;
  const int hg = (w + (j >> 2)) & 7, rot = (j >> 2) + (j >> 5);
  const __amdgpu_buffer_rsrc_t rsy = mkrsrc(p.ws + WS_SY, SY_TOTAL);
  const u32x4 rs4 = raw_rsrc(p.ws + WS_SY, SY_TOTAL);
  const __amdgpu_buffer_rsrc_t rt = mkrsrc(p.ws + WS_T, T_BYTES);
  gu32* abortw = (gu32*)(p.ws + WS_SY + SY_ABORT);
  u32x32 W[4];
  u32x2 tag = {0u, 0u};
  {
    const u16* Wo = p.Wo + (size_t)(4 * hg * DH) * DM + 32 * c;
    const int ro = ((lane >> 4) * 8 + ((lane & 15) >> 2)) * 64 + (lane & 3) * 8;
    Boot bt;
    load_blocks<16>(smem + 32768 + w * 16384,
        [&](int i, char* slot) {
          if (i == 12 && w == 0) boot_ask(p.ws, bt);
          const int x = ((i >> 2) + rot) & 3;
          blk_dma(Wo + (size_t)((x >> 1) * 256 + (x & 1) * 64 + ((i >> 1) & 1) * 128 + (i & 1) * 32) * DM, DM, slot, lane);
        },
        [&](int i, char* slot) {
#pragma unroll
          for (int t = 0; t < 2; ++t) {
            s16x4 lo = __builtin_amdgcn_ds_read_tr16_b64_v4i16((lds_s16x4_ptr)(slot + ro + t * 32));
            s16x4 hi = __builtin_amdgcn_ds_read_tr16_b64_v4i16((lds_s16x4_ptr)(slot + ro + t * 32 + 256));
            put4(W[i >> 2], (i & 2) * 2 + 2 * t + (i & 1), (bf16x8){lo[0], lo[1], lo[2], lo[3], hi[0], hi[1], hi[2], hi[3]});
          }
        });
    if (w == 0) { lds_at(smem + 4128)[0] = boot_done(p.ws, bt); tag = next_tag(bt.e); }
    LBAR();
  }
  const unsigned pm = uni(lds_at(smem + 4128)[0]);
  p.s0 = pm >> 31;
  const int hs = side(pm, 1);
  if (((j >> 2) & 14) != 6 && GPF == 2) {
#pragma unroll
    for (int i = 0; i < 4; ++i)
      touch_line((const char*)(i < 2 ? p.wg : p.wu) + (size_t)(j - (j >= 32 ? 8 : 0)) * 131072 + ((i & 1) * 8 + w) * 8192 + lane * 128, smem + w * 256);
  }
  {
    const u32x4 ds = {DSENT, DSENT, DSENT, DSENT};
#pragma unroll
    for (int i = 0; i < 2; ++i) {
      const int s = (w * 2 + i) * 4 + (lane >> 4);
      if (s >= 2) st_sc1(rsy, o_mem(pm, s) + (c >> 4) * 16384 + (c & 15) * 512 + rh * 128 + ((lane >> 3) & 1) * 256 + (lane & 7) * 16, ds);
    }
  }
  if (tid < 4) { reinterpret_cast<unsigned*>(smem + 4096)[tid] = tid == 3 ? now_q() : 0; reinterpret_cast<unsigned*>(smem + 4112)[tid] = DSENT; }
  if (tid < 256) reinterpret_cast<int*>(smem + 5120)[tid] = ((tid >> 2) - TLAG - 6) >> 2;
  LBAR();
  const unsigned base = __builtin_amdgcn_readfirstlane(lds_at(smem + 4096)[3]);
  RDY(64 + j);
  unsigned long long fm = 0, ps = 0;
  const unsigned aL = lane * 16;
  const int e = ((56 + w) * SEG + rh * 16 + (lane >> 2)) * DM + c * 32 + (lane & 3) * 8;
  u32x4 xv = *reinterpret_cast<const u32x4*>(p.x + e), av = {DSENT, DSENT, DSENT, DSENT};
  asm volatile("" : "+v"(xv));
  {
    const int r = lane & 15, q = lane >> 4;
    const int sm0 = o_mem(pm, 0) + (c >> 4) * 16384 + (c & 15) * 512 + rh * 128;
    u32x4 VO, VT;
    unsigned fo = SY_FO + ((rh + (lane & 4) / 2) * NH + 4 * hg + (lane & 3)) * 128;
    unsigned ao = w == 1 ? (c * S_LEN + rh * 16 - (TLAG + 3) * SEG) * 64 + lane * 16
                         : (w >= 4 ? SY_O + NPRE * 131072 + side(pm, w - 2) + (j >> 3) * 16384 + (j & 7) * 1024 + lane * 16
                                   : SY_FS + (2 * c + rh) * 128 + (lane < 8 ? lane * 16 : 0x70000000));
    unsigned ar = w == 1 ? R_SI - (TLAG + 2) * 1024 + lane * 16 : (w == 2 ? 5120 : 4112);
    const unsigned long long am = w == 1 ? (1ull << 62) - 64 : (w == 2 ? 0x4444444444444400ull : (w >= 4 ? (0xFFFFull << (16 * (w - 4))) >> NPRE : 0));
    static_assert(TLAG == 3, "");
    unsigned pw = R_PART + w * 2048 + lane * 16, pr = R_PART + 16384 + tid * 4;
    unsigned sw = R_SI - (TLAG + 1) * 1024 + (lane >> 2) * 64 + w * 8 + (lane & 3) * 2, stm = 8192 + w * 512;
#pragma unroll
    for (int l = 0; l < 4; ++l) {
      const int x = (l + rot) & 3;
      VO[l] = o_o(0, 0, 4 * hg) + rh * 65536 + x * 1024 + aL;
      VT[l] = (T_O + rh * 16 + 2 * hg + (x >> 1)) * 16384 + (x & 1) * 2048 + aL;
    }
    int rl = 280;
    unsigned step, E, so, st, st4, sh;
    asm volatile(RED_ASM
        : [step] "=&s"(step), [E] "=&s"(E), [so] "=&s"(so), [st] "=&s"(st), [st4] "=&s"(st4), [sh] "=&s"(sh),
          [rl] "+s"(rl), [fm] "+s"(fm), [ps] "+s"(ps),
          "+{v[224:227]}"(av), "+{v228}"(ao), "+{v229}"(ar), "+{v230}"(pw), "+{v231}"(pr), "+{v232}"(sw), "+{v233}"(stm), "+{v242}"(fo)
        : W4_IN, "{v168}"(aL), "{v169}"(w * 512 + lane * 8), "{v170}"(lane * 8),
          "{v171}"(sm0 + 32768 + (q >> 1) * 256 + (q & 1) * 64 + ((r >> 1) & 1) * 32 + (r & 1) * 16 + (r >> 2) * 4),
          "{v172}"((lane & 2) ? 0xEEEE : 0x4444), "{v173}"(lane < 4 ? SY_BA + hs + (4 * hg + lane) * 128 : 0x7F000000u),
          "{v174}"((r & 1) ? 0x03020706u : 0x05040100u), "{v175}"(SY_BR + hs + j * 128 + lane * 8), "{v[234:237]}"(VO), "{v[238:241]}"(VT), "{v243}"(e * 2),
          [rg] "s"(raw_rsrc(p.ws + WS_G, S_LEN * DM * 2)), [fg] "s"(SY_FG + ((14 + (w >> 2)) * 8 + (c >> 2)) * 128),
          [rs] "s"(rs4), [ra] "s"(w == 1 ? raw_rsrc(p.ws + WS_S, S_LEN * DM * 2) : rs4), [rt] "s"(raw_rsrc(p.ws + WS_T, T_BYTES)),
          [ds] "n"(DS_RED), [mn] "n"(RED_MIN), [lk] "n"(DS_MEM + BA_LOOK), [up] "n"(RL_UP), [dn] "n"(-RL_DN), [sent] "s"(DSENT), [sab] "s"(SY_ABORT),
          [base] "s"(base), [pm] "s"(pm), [s0] "s"((p.s0 ^ TF_O) << 13), [am] "s"(am), [ainc] "s"(w == 1 ? SEG * 64 : (w >= 4 ? 131072 : 0)),
          [rinc] "s"(w == 1 ? 1024 : (w == 2 ? 16 : 0))
        : CLB32(128), CLB8(160, 161, 162, 163, 164, 165, 166, 167), CLB8(176, 177, 178, 179, 180, 181, 182, 183),
          CLB8(184, 185, 186, 187, 188, 189, 190, 191), CLB8(192, 193, 194, 195, 196, 197, 198, 199), CLB8(200, 201, 202, 203, 204, 205, 206, 207),
          CLB8(208, 209, 210, 211, 212, 213, 214, 215), CLB8(216, 217, 218, 219, 220, 221, 222, 223), CLB_E, "s96", CLB_S);
  }
  if (j == 0 && w == 5) { LST(345); }
  LBAR();
  if (j == 0 && w == 5) { LST(346); }
  u32x4 sv;
  if (w < 3) sv = *reinterpret_cast<const u32x4*>(smem + R_SI + (56 + w) * 1024 + lane * 16);
  else {
    const f32x4* part = reinterpret_cast<const f32x4*>(smem + (w == 3 ? R_PART : (w - 3) * 16384)) +
                        (lane & 2) * 32 + (lane & 1) * 32 + (lane >> 2);
    f32x4 x[8][2];
#pragma unroll
    for (int ww = 0; ww < 8; ++ww) { x[ww][0] = part[ww * 128]; x[ww][1] = part[ww * 128 + 16]; }
#pragma unroll
    for (int u = 0; u < 2; ++u) {
      const f32x4 a = ((x[0][u] + x[1][u]) + (x[2][u] + x[3][u])) + ((x[4][u] + x[5][u]) + (x[6][u] + x[7][u]));
      sv[2 * u] = cvtpk(a[0], a[1]); sv[2 * u + 1] = cvtpk(a[2], a[3]);
    }
  }
  *reinterpret_cast<u32x4*>(p.out + e) = combine(sv, av, xv);
  if (j == 0 && tid == 0) st2_sc1(rsy, SY_EP, tag);
  if (tid == 128) { LST(400 + j); }
}

__global__ __launch_bounds__(512, 2) void main_kernel(Args p) {
  char* smem = g_smem;
  p.ws = ws_align(p.ws);
  const int bx = blockIdx.x & 7, bk = blockIdx.x >> 3;
  const bool isg = (bx ^ XPROBE) & 4;
  const int b = (isg ? NCHAIN : 0) + bk * 4 + (bx & 3);
  if (b == 64 && threadIdx.x == 0) { LST(500); }
  if (b >= NCHAIN) gemm_role(p, b - NCHAIN, smem);
  else if (b < 32) qk_role(p, (b & 3) * 8 + bk, smem);
  else if (b < 64) vu_role(p, (b & 3) * 8 + bk - 8, smem);
  else red_role(p, b - 64, smem);
}

extern "C" void kernel_launch(void* const* d_in, const int* in_sizes, int n_in,
                              void* d_out, int out_size, void* d_ws, size_t ws_size,
                              hipStream_t stream) {
  Args a;
  a.x = (const u16*)d_in[0]; a.mem0 = (const u16*)d_in[1];
  a.Wq = (const u16*)d_in[2]; a.Wk = (const u16*)d_in[3]; a.Wv = (const u16*)d_in[4]; a.Wo = (const u16*)d_in[5];
  a.wg = (const u16*)d_in[8]; a.wu = (const u16*)d_in[9]; a.wd = (const u16*)d_in[10];
  a.ws = (char*)d_ws; a.out = (u16*)d_out; a.s0 = 0; a.pad1 = 0;
  main_kernel<<<256, 512, 0, stream>>>(a);
}
```

```cpp
#include <hip/hip_runtime.h>
#include <hip/hip_bf16.h>
#include <stdint.h>
#include <type_traits>

constexpr int S_LEN = 2048, DM = 1024, NH = 32, DH = 128, NMEM = 8, SEG = 32;
constexpr int NSEG = S_LEN / SEG;
constexpr int HD = NH * DH;
constexpr int FF = 3584;

typedef unsigned short u16;
typedef __attribute__((ext_vector_type(8))) short bf16x8;
typedef __attribute__((ext_vector_type(4))) short s16x4;
typedef __attribute__((ext_vector_type(4))) float f32x4;
typedef __attribute__((ext_vector_type(4))) unsigned int u32x4;
typedef __attribute__((ext_vector_type(2))) unsigned int u32x2;
typedef __attribute__((address_space(3))) s16x4* lds_s16x4_ptr;
typedef __attribute__((address_space(3))) void* lds_void_ptr;
typedef __attribute__((address_space(1))) const void* glb_cvoid_ptr;
typedef __attribute__((address_space(1))) unsigned gu32;
typedef __bf16 bf16x2_t __attribute__((ext_vector_type(2)));
typedef float f32x2_t __attribute__((ext_vector_type(2)));

__device__ __forceinline__ float bf2f(u16 v) { return __uint_as_float(((unsigned)v) << 16); }
__device__ __forceinline__ unsigned cvtpk(float lo, float hi) {
  bf16x2_t r = __builtin_convertvector((f32x2_t){lo, hi}, bf16x2_t);
  return __builtin_bit_cast(unsigned, r);
}
__device__ __forceinline__ float lo_f(unsigned v) { return __uint_as_float(v << 16); }
__device__ __forceinline__ float hi_f(unsigned v) { return __uint_as_float(v & 0xffff0000u); }
__device__ __forceinline__ float rbf(float f) { return lo_f(cvtpk(f, f)); }
__device__ __forceinline__ bf16x8 as_bf16x8(u32x4 v) { return __builtin_bit_cast(bf16x8, v); }
#define MFMA(a, b, c) __builtin_amdgcn_mfma_f32_16x16x32_bf16((a), (b), (c), 0, 0, 0)
typedef __attribute__((ext_vector_type(16))) short bf16x16;
typedef __attribute__((ext_vector_type(8))) __bf16 bfv8;
typedef __attribute__((ext_vector_type(16))) __bf16 bfv16;
#define SMFMA(a, b, c, idx) __builtin_amdgcn_smfmac_f32_16x16x64_bf16(__builtin_bit_cast(bfv8, (a)), __builtin_bit_cast(bfv16, (b)), (c), (idx), 0, 0)
__device__ __forceinline__ bf16x16 cat16(bf16x8 lo, bf16x8 hi) {
  return __builtin_shufflevector(lo, hi, 0, 1, 2, 3, 4, 5, 6, 7, 8, 9, 10, 11, 12, 13, 14, 15);
}

#define KSTR 65
#define XLD 1024
constexpr int PAGE = 2 << 20, NPAGE = 14;
constexpr int T_K = 0, T_Q = 2304, T_V = 4608, T_O = 6912, T_END = T_O + 2048;
constexpr unsigned T_BYTES = T_END * 16384u;
constexpr size_t WS_SY = 0;
constexpr size_t WS_T = (size_t)NPAGE * PAGE;
constexpr size_t WS_XN = WS_T + T_BYTES;
constexpr size_t WS_HF = WS_XN + (size_t)S_LEN * XLD * 2;
constexpr size_t WS_G = WS_HF + (size_t)S_LEN * 16384;
constexpr size_t WS_S = WS_G + (size_t)S_LEN * DM * 2;
constexpr size_t WS_END = WS_S + (size_t)S_LEN * DM * 2;
constexpr unsigned SENT = 0xFFFFFFFFu;
constexpr unsigned DSENT = 0xFFBFFFBFu;
constexpr int SY_MEM = 0;
constexpr int SY_O = 2 * PAGE;
constexpr int SY_V = 6 * PAGE;
constexpr int SY_BR = PAGE, SY_BA = SY_BR + 16384;
constexpr int SY_FKQ = SY_BR + 32768;
constexpr int SY_FG = SY_FKQ + NH * 4 * 128;
constexpr int SY_FO = SY_FG + 16 * 8 * 128;
constexpr int SY_CH = SY_FO + 384 * 128;
constexpr int SY_Q = SY_CH + 8 * 128;
constexpr int SY_ABORT = SY_Q + 8 * 128;
constexpr int SY_FS = SY_ABORT + 256;
constexpr int SY_FEND = SY_FS + 64 * 128;
constexpr int SY_EP = SY_FEND, SY_BT = SY_EP + 128;
constexpr int SY_TOTAL = NPAGE * PAGE;
constexpr int PROBE = PAGE - 16384 + 6144;
__device__ __forceinline__ int side(unsigned pm, int page) { return ((pm >> page) & 1u) << 13; }
__device__ __forceinline__ int o_mem(unsigned pm, int s) { return SY_MEM + s * 32768 + side(pm, 0); }
__device__ __forceinline__ int o_o(unsigned pm, int s, int h) {
  return SY_O + s * 131072 + (h >> 3) * 16384 + ((h >> 1) & 3) * 2048 + (h & 1) * 128 + side(pm, 2 + (s >> 4));
}
__device__ __forceinline__ int o_v(unsigned pm, int s, int h) { return SY_V + s * 262144 + (h >> 1) * 16384 + (h & 1) * 4096 + side(pm, 6 + (s >> 3)); }
__device__ __forceinline__ char* ws_align(char* ws) { return ws + ((PAGE - (unsigned)(uintptr_t)ws) & (PAGE - 1)); }
#define NFUSE 12
constexpr int NPRE = 4;
static_assert(WS_END + PAGE <= (size_t)256 << 20 && NH * KSTR <= T_Q && NPAGE % 2 == 0, "");
constexpr int NCHAIN = 128, NGW = 32;
__host__ __device__ constexpr int kseg(int sg, int h) { return h * KSTR + sg; }
__host__ __device__ constexpr int fkq(int h, int kind) { return SY_FKQ + (h * 4 + kind) * 128; }

#define HSKEW 0
#define XPROBE 4
struct Args {
  const u16 *x, *mem0, *Wq, *Wk, *Wv, *Wo, *wg, *wu, *wd;
  char* ws;
  u16* out;
  int s0, pad1;
};
constexpr int HLD = 16384 + HSKEW;
__device__ __forceinline__ int h_off(const Args& p, int rblk) { return rblk * PAGE + (((p.s0 ^ rblk ^ 1) & 1) << 13); }
static_assert(WS_HF % (2 * PAGE) == 0 && 127 * HSKEW + FF * 2 <= 8192, "");
#define TF_KQV 1
#define TF_O 0
__device__ __forceinline__ int t_off(const Args& p, int t) { return t * 16384 + (((p.s0 ^ (t >> 7) ^ (TF_KQV == 2 ? t : TF_KQV)) & 1) << 13); }
__device__ __forceinline__ int t_off_o(const Args& p, int t) { return t * 16384 + (((p.s0 ^ (t >> 7) ^ (TF_O == 2 ? t : TF_O)) & 1) << 13); }

__device__ __forceinline__ float wave_sum(float v) {
#pragma unroll
  for (int i = 0; i < 4; ++i) {
    const int x = (int)__float_as_uint(v);
    v += __uint_as_float(i == 0 ? __builtin_amdgcn_update_dpp(0, x, 0x128, 0xf, 0xf, false) : i == 1 ? __builtin_amdgcn_update_dpp(0, x, 0x124, 0xf, 0xf, false)
                         : i == 2 ? __builtin_amdgcn_update_dpp(0, x, 0x122, 0xf, 0xf, false) : __builtin_amdgcn_update_dpp(0, x, 0x121, 0xf, 0xf, false));
  }
  auto r = __builtin_amdgcn_permlane16_swap(__float_as_uint(v), __float_as_uint(v), false, false);
  v = __uint_as_float(r[0]) + __uint_as_float(r[1]);
  r = __builtin_amdgcn_permlane32_swap(__float_as_uint(v), __float_as_uint(v), false, false);
  return __uint_as_float(r[0]) + __uint_as_float(r[1]);
}

__shared__ __attribute__((aligned(16))) char g_smem[163840];
__device__ __forceinline__ unsigned lds_off(const void* p) {
  return (unsigned)(uintptr_t)(lds_void_ptr)g_smem + (unsigned)((const char*)p - g_smem);
}
__device__ __forceinline__ __amdgpu_buffer_rsrc_t mkrsrc(const void* p, unsigned bytes) {
  return __builtin_amdgcn_make_buffer_rsrc((void*)p, 0, bytes, 0x00020000);
}
__device__ __forceinline__ u32x4 ld_sc1(__amdgpu_buffer_rsrc_t rs, int off) {
  return __builtin_amdgcn_raw_buffer_load_b128(rs, off, 0, 16);
}
__device__ __forceinline__ void st_sc1(__amdgpu_buffer_rsrc_t rs, int off, u32x4 v) {
  __builtin_amdgcn_raw_buffer_store_b128(v, rs, off, 0, 16);
}
__device__ __forceinline__ unsigned ror8(unsigned v) {
  return (unsigned)__builtin_amdgcn_update_dpp(0, (int)v, 0x128, 0xf, 0xf, false);
}
__device__ __forceinline__ float ror8f(float v) { return __uint_as_float(ror8(__float_as_uint(v))); }
__device__ __forceinline__ void swap16(unsigned& x, unsigned& y) {
  auto r = __builtin_amdgcn_permlane16_swap(x, y, false, false);
  x = r[0]; y = r[1];
}
__device__ __forceinline__ void swap32(unsigned& x, unsigned& y) {
  auto r = __builtin_amdgcn_permlane32_swap(x, y, false, false);
  x = r[0]; y = r[1];
}
__device__ __forceinline__ f32x4 fold32(f32x4 x, f32x4 y) {
  f32x4 o;
#pragma unroll
  for (int j = 0; j < 4; ++j) {
    unsigned a = __float_as_uint(x[j]), b = __float_as_uint(y[j]);
    swap32(a, b);
    o[j] = __uint_as_float(a) + __uint_as_float(b);
  }
  return o;
}
__device__ __forceinline__ bf16x8 tr_frag(const char* img_lane) {
  s16x4 lo = __builtin_amdgcn_ds_read_tr16_b64_v4i16((lds_s16x4_ptr)img_lane);
  s16x4 hi = __builtin_amdgcn_ds_read_tr16_b64_v4i16((lds_s16x4_ptr)(img_lane + 64));
  return (bf16x8){lo[0], lo[1], lo[2], lo[3], hi[0], hi[1], hi[2], hi[3]};
}
__device__ __forceinline__ float rowq_max(float v) {
  unsigned a = __float_as_uint(v), b = a;
  swap16(a, b);
  v = __builtin_elementwise_maximum(__uint_as_float(a), __uint_as_float(b));
  a = __float_as_uint(v); b = a;
  swap32(a, b);
  return __builtin_elementwise_maximum(__uint_as_float(a), __uint_as_float(b));
}
__device__ __forceinline__ float rowq_sum(float v) {
  unsigned a = __float_as_uint(v), b = a;
  swap16(a, b);
  v = __uint_as_float(a) + __uint_as_float(b);
  a = __float_as_uint(v); b = a;
  swap32(a, b);
  return __uint_as_float(a) + __uint_as_float(b);
}
__device__ __forceinline__ bool spin_fail(unsigned& spins, gu32* abortw) {
  ++spins;
  if ((spins & 63u) == 0u) {
    const __amdgpu_buffer_rsrc_t ra = mkrsrc((const void*)abortw, 4);
    unsigned z = 0;
    asm volatile("" : "+v"(z));
    if (__builtin_amdgcn_readfirstlane(__builtin_amdgcn_raw_buffer_load_b32(ra, z, 0, 16)) == 1u) return true;
    if (spins > (1u << 19)) {
      __builtin_amdgcn_raw_buffer_store_b32(z + 1u, ra, z, 0, 16);
      return true;
    }
  }
  return false;
}
__device__ __forceinline__ unsigned umax4(u32x4 v) { return max(max(v[0], v[1]), max(v[2], v[3])); }
__device__ __forceinline__ bool has_ds(u32x4 v) { return (v[0] == DSENT) | (v[1] == DSENT) | (v[2] == DSENT) | (v[3] == DSENT); }
__device__ __forceinline__ float fmax2(float a, float b) { return __builtin_elementwise_maximum(a, b); }
__device__ __forceinline__ bf16x8 ldsf(const char* p) { return *reinterpret_cast<const bf16x8*>(p); }
#define SCHED() __builtin_amdgcn_sched_barrier(0)
#define LBAR() do { asm volatile("s_waitcnt lgkmcnt(0)" ::: "memory"); __builtin_amdgcn_s_barrier(); asm volatile("" ::: "memory"); } while (0)
#define WAIT_V(n) asm volatile("s_waitcnt vmcnt(%0)" :: "n"(n) : "memory")

__device__ __forceinline__ void wait_flags(__amdgpu_buffer_rsrc_t rs, int off, gu32* abortw) {
  for (unsigned spins = 0;;) {
    asm volatile("" ::: "memory");
    const unsigned v = __builtin_amdgcn_raw_buffer_load_b32(rs, off, 0, 16);
    if (__all(v != SENT)) break;
    __builtin_amdgcn_s_sleep(4);
    if (spin_fail(spins, abortw)) break;
  }
  asm volatile("" ::: "memory");
}

#define LST(i) do {} while (0)
#define RDY(i) do {} while (0)
#define SMP(k, s, v) do {} while (0)
#define LSTC(i) do {} while (0)
__device__ __forceinline__ unsigned offA(unsigned row, unsigned ch) {
  return row * 128u + 16u * (ch ^ (row & 7u));
}
__device__ __forceinline__ unsigned swzB(unsigned row) { return ((row & 3u) << 2) | ((row >> 2) & 3u); }
__device__ __forceinline__ unsigned offB(unsigned row, unsigned ch) {
  return 256u * row + 16u * (ch ^ swzB(row));
}
__device__ __forceinline__ void glds16(const u16* src, char* dst) {
  const unsigned d = __builtin_amdgcn_readfirstlane(lds_off(dst));
  unsigned keep;
  asm volatile("s_mov_b32 %0, m0\n\ts_mov_b32 m0, %2\n\ts_nop 0\n\tglobal_load_lds_dwordx4 %1, off\n\ts_mov_b32 m0, %0"
               : "=&s"(keep) : "v"(src), "s"(d) : "memory");
}
__device__ __forceinline__ bf16x8 ld_bfrag(const char* sb, int ks, int c, int lane) {
  const unsigned g = lane >> 4, qq = (lane & 15) >> 2, pp = lane & 3;
  s16x4 lo = __builtin_amdgcn_ds_read_tr16_b64_v4i16(
      (lds_s16x4_ptr)(sb + offB(32 * ks + 8 * g + qq, 2 * c + (pp >> 1)) + 8 * (pp & 1)));
  s16x4 hi = __builtin_amdgcn_ds_read_tr16_b64_v4i16(
      (lds_s16x4_ptr)(sb + offB(32 * ks + 8 * g + 4 + qq, 2 * c + (pp >> 1)) + 8 * (pp & 1)));
  return (bf16x8){lo[0], lo[1], lo[2], lo[3], hi[0], hi[1], hi[2], hi[3]};
}

__device__ __forceinline__ void blk_dma(const u16* src, size_t ld, char* slot, int lane) {
  const u16* s = src + (size_t)(lane >> 2) * ld + (lane & 3) * 8;
  glds16(s, slot);
  glds16(s + 16 * ld, slot + 1024);
}
__device__ __forceinline__ bf16x8 blk_frag(const char* slot, int T, int lane) {
  const char* a = slot + ((lane >> 4) * 8 + ((lane & 15) >> 2)) * 64 + (lane & 3) * 16 + T * 8;
  s16x4 lo = __builtin_amdgcn_ds_read_tr16_b64_v4i16((lds_s16x4_ptr)a);
  s16x4 hi = __builtin_amdgcn_ds_read_tr16_b64_v4i16((lds_s16x4_ptr)(a + 256));
  return (bf16x8){lo[0], lo[1], lo[2], lo[3], hi[0], hi[1], hi[2], hi[3]};
}
template <int NB, typename ISSUE, typename SINK>
__device__ __forceinline__ void load_blocks(char* slice, ISSUE&& issue, SINK&& sink) {
#pragma unroll
  for (int i = 0; i < 8; ++i) issue(i, slice + i * 2048);
#pragma unroll
  for (int g = 0; g < NB / 4; ++g) {
    if (g + 1 < NB / 4) WAIT_V(8); else WAIT_V(0);
#pragma unroll
    for (int j = 0; j < 4; ++j) sink(4 * g + j, slice + ((4 * g + j) & 7) * 2048);
    asm volatile("s_waitcnt lgkmcnt(0)" ::: "memory");
    if (g + 2 < NB / 4) {
#pragma unroll
      for (int j = 0; j < 4; ++j) issue(4 * g + 8 + j, slice + ((4 * g + j) & 7) * 2048);
    }
  }
}

__device__ __forceinline__ u32x4 widen32(unsigned a0, unsigned a1, unsigned b0, unsigned b1) {
  swap32(a0, b0); swap16(a0, b0);
  swap32(a1, b1); swap16(a1, b1);
  return (u32x4){a0, a1, b0, b1};
}

__device__ __forceinline__ void publish_flags(__amdgpu_buffer_rsrc_t rs, int off, int stride, int n) {
  WAIT_V(0);
  LBAR();
  int tid = threadIdx.x;
  asm volatile("" : "+v"(tid));
  if (tid < n) __builtin_amdgcn_raw_buffer_store_b32(1u, rs, off + tid * stride, 0, 16);
}

__device__ __forceinline__ void wait_s(const Args& p, __amdgpu_buffer_rsrc_t rsy, int rblk, int c0, int n, bool need) {
  if (need) {
    if (threadIdx.x < 64) {
      for (unsigned spins = 0;;) {
        asm volatile("" ::: "memory");
        const int f = (int)__builtin_amdgcn_raw_buffer_load_b32(rsy, SY_FS + (2 * c0 + (threadIdx.x & (2 * n - 1))) * 128, 0, 16);
        if (__all(f >= rblk)) break;
        __builtin_amdgcn_s_sleep(32);
        if (spin_fail(spins, (gu32*)(p.ws + WS_SY + SY_ABORT))) break;
      }
    }
    LBAR();
  }
}
__device__ __forceinline__ u32x4 combine(u32x4 sv, u32x4 g, u32x4 x) {
  u32x4 o;
#pragma unroll
  for (int j = 0; j < 4; ++j) {
    const unsigned t = cvtpk(lo_f(sv[j]) * lo_f(g[j]), hi_f(sv[j]) * hi_f(g[j]));
    o[j] = cvtpk(lo_f(x[j]) + lo_f(t), hi_f(x[j]) + hi_f(t));
  }
  return o;
}
struct SX { u32x4 s, x; };
__device__ __forceinline__ SX get_sx(const Args& p, int rblk, int e) {
  SX r; r.s = (u32x4){0u, 0u, 0u, 0u}; r.x = r.s;
  if (rblk < NFUSE) {
    r.s = ld_sc1(mkrsrc(p.ws + WS_S, S_LEN * DM * 2), (((e & 1023) >> 5) * S_LEN + (e >> 10)) * 64 + (e & 31) * 2);
    r.x = *reinterpret_cast<const u32x4*>(p.x + e);
  }
  return r;
}
__device__ __forceinline__ void put_g(const Args& p, __amdgpu_buffer_rsrc_t rg, int rblk, int e, u32x4 g, const SX& v) {
  if (rblk < NFUSE) {
    *reinterpret_cast<u32x4*>(p.out + e) = combine(v.s, g, v.x);
  } else st_sc1(rg, e * 2, g);
}

__device__ __forceinline__ void job_comb(const Args& p, int rblk, int ct, __amdgpu_buffer_rsrc_t rsy) {
  int tid = threadIdx.x;
  asm volatile("" : "+v"(tid));
  u32x4 xv[8];
#pragma unroll
  for (int i = 0; i < 8; ++i) xv[i] = *reinterpret_cast<const u32x4*>(p.x + (rblk * 128 + i * 16 + (tid >> 5)) * DM + ct * 256 + (tid & 31) * 8);
  if (tid < 64) wait_flags(rsy, SY_FG + (rblk * 8 + 2 * ct + (tid & 1)) * 128, (gu32*)(p.ws + WS_SY + SY_ABORT));
  wait_s(p, rsy, rblk, ct * 8, 8, true);
  u32x4 sv[8], gv[8];
#pragma unroll
  for (int i = 0; i < 8; ++i) {
    const int e = (rblk * 128 + i * 16 + (tid >> 5)) * DM + ct * 256 + (tid & 31) * 8;
    sv[i] = ld_sc1(mkrsrc(p.ws + WS_S, S_LEN * DM * 2), (((e & 1023) >> 5) * S_LEN + (e >> 10)) * 64 + (e & 31) * 2);
    gv[i] = ld_sc1(mkrsrc(p.ws + WS_G, S_LEN * DM * 2), e * 2);
  }
#pragma unroll
  for (int i = 0; i < 8; ++i)
    *reinterpret_cast<u32x4*>(p.out + (rblk * 128 + i * 16 + (tid >> 5)) * DM + ct * 256 + (tid & 31) * 8) = combine(sv[i], gv[i], xv[i]);
}

__device__ __forceinline__ u32x4 raw_rsrc(const void* p, unsigned bytes) {
  const unsigned long long a = (unsigned long long)p;
  return (u32x4){(unsigned)a, (unsigned)(a >> 32) & 0xffffu, bytes, 0x00020000u};
}
__device__ __forceinline__ u32x2 next_tag(u32x2 e) { return (u32x2){e[0] + 1u, e[1] + (e[0] == 0xFFFFFFFFu)}; }
struct Boot { u32x2 e; u32x4 c; };
__device__ __forceinline__ void boot_ask(const char* ws, Boot& r) {
  const unsigned o = (threadIdx.x & 63) * 128;
  asm volatile("buffer_load_dwordx2 %0, off, %3, %4 sc1\n\tbuffer_load_dwordx4 %1, %2, %3, %5 offen sc1"
               : "=&v"(r.e), "=&v"(r.c) : "v"(o), "s"(raw_rsrc(ws, SY_TOTAL)), "s"(SY_EP), "s"(SY_BT) : "memory");
}
__device__ __forceinline__ void wait_rows(const char* ws, u32x2 tag) {
  const __amdgpu_buffer_rsrc_t rs = mkrsrc(ws, SY_TOTAL);
  int o = (threadIdx.x & 63) * 128;
  asm volatile("" : "+v"(o));
#pragma unroll 1
  for (int i = 0; i < (1 << 17); ++i) {
    asm volatile("" ::: "memory");
    const u32x2 a = __builtin_amdgcn_raw_buffer_load_b64(rs, o, SY_BT + 32 * 128, 16), b = __builtin_amdgcn_raw_buffer_load_b64(rs, o, SY_BT + 96 * 128, 16);
    if (__all(a[0] == tag[0] && a[1] == tag[1] && b[0] == tag[0] && b[1] == tag[1])) break;
    __builtin_amdgcn_s_sleep(8);
  }
  asm volatile("" ::: "memory");
}
__device__ __forceinline__ unsigned boot_done(const char* ws, Boot& r, bool own = false, u32x2 tag = (u32x2){0u, 0u}) {
#pragma unroll 1
  for (int i = 0; i < (1 << 17); ++i) {
    asm volatile("" : "+v"(r.e), "+v"(r.c));
    if (!own) tag = next_tag(r.e);
    if (__all(r.c[0] == tag[0] && r.c[1] == tag[1])) break;
    __builtin_amdgcn_s_sleep(8);
    boot_ask(ws, r);
    WAIT_V(0);
  }
  unsigned q0[NPAGE], q1[NPAGE], ga = 0xFFFFu, gb = 0xFFFFu;
#pragma unroll
  for (int i = 0; i < NPAGE; ++i) {
    const unsigned x = __builtin_amdgcn_readlane(r.c[2], i), y = __builtin_amdgcn_readlane(r.c[2], i + NPAGE);
    q0[i] = min(x >> 16, y >> 16); q1[i] = min(x & 0xFFFFu, y & 0xFFFFu);
    ga = min(ga, (i & 1) ? q1[i] : q0[i]); gb = min(gb, (i & 1) ? q0[i] : q1[i]);
  }
  unsigned m = gb < ga ? 0x80001555u : 0x2AAAu;
  const unsigned thr = min(ga, gb) + 110;
#pragma unroll
  for (int i = 0; i < NPAGE; ++i) {
    const unsigned lo = min(q0[i], q1[i]), hi = max(q0[i], q1[i]);
    const unsigned keep = (lo < thr ? hi : 0u) > lo + 100 ? ~(1u << i) : ~0u;
    m = (m & keep) | ((q1[i] < q0[i] ? ~0u : 0u) & ~keep);
  }
  return m;
}
typedef __attribute__((address_space(3))) volatile unsigned* lds_vu_ptr;
__device__ __forceinline__ lds_vu_ptr lds_at(const void* p) {
  unsigned a = lds_off(p);
  asm volatile("" : "+v"(a));
  return (lds_vu_ptr)(uintptr_t)a;
}
__device__ __forceinline__ unsigned uni(unsigned v) {
  unsigned r;
  asm volatile("s_nop 1\n\tv_readfirstlane_b32 %0, %1\n\ts_nop 3" : "=s"(r) : "v"(v));
  return r;
}
__device__ __forceinline__ void start_sy(char* ws, const u16* mem0, int i, char* smem) {
  const int tid = threadIdx.x, lane = tid & 63, w = __builtin_amdgcn_readfirstlane(tid >> 6);
  const __amdgpu_buffer_rsrc_t rsy = mkrsrc(ws, SY_TOTAL);
  const __amdgpu_buffer_rsrc_t rs = mkrsrc(ws + (size_t)(i % NPAGE) * PAGE + PROBE - (i / NPAGE) * 2048, 16384);
  const u32x2 e = __builtin_amdgcn_raw_buffer_load_b64(rsy, 0, SY_EP, 16);
  const u32x4 ds = {DSENT, DSENT, DSENT, DSENT};
  if (lane < 32) st_sc1(rs, (lane >> 4) * 8192 + w * 256 + (lane & 15) * 16, ds);
  static_assert(NPRE == 4 && SY_FEND - SY_BR <= 32 * 6144, "");
#pragma unroll
  for (int u = 0; u < 6; ++u)
    st_sc1(rsy, (u < 4 ? SY_V + (i * 4 + u) * 8192 : SY_O + (i * 2 + u - 4) * 8192) + tid * 16, ds);
  if (w == 7) {
    const int n = i * 64 + lane, j = (n >> 10) * 512 + (n & 511);
    const unsigned* m0 = reinterpret_cast<const unsigned*>(mem0 + (2 * ((j >> 2) & 3) + (j & 1)) * DM + (j >> 4) * 16) + ((j >> 1) & 1);
    u32x4 m;
#pragma unroll
    for (int k = 0; k < 4; ++k) { const unsigned t = m0[2 * k]; m[k] = t == DSENT ? 0xFFFFFFFFu : t; }
    st_sc1(rsy, n * 16, m);
  } else if (w == 6) st_sc1(rsy, 32768 + i * 1024 + lane * 16, ds);
  else {
    const int o = SY_BR + i * 6144 + tid * 16;
    st_sc1(rsy, o < SY_FEND ? o : 0x7F000000, (u32x4){SENT, SENT, SENT, SENT});
  }
  unsigned t[4];
#pragma unroll
  for (int k = 0; k < 4; ++k) {
    unsigned long long c0, c1; unsigned v, off = ((k ^ (k >> 1)) & 1) * 8192 + w * 256 + (k >> 1) * 128;
    asm volatile("s_waitcnt vmcnt(0)\n\ts_barrier\n\ts_memtime %0\n\ts_waitcnt lgkmcnt(0)\n\tbuffer_load_dword %2, %3, %4, 0 offen sc1\n\ts_waitcnt vmcnt(0)\n\t"
                 "s_memtime %1\n\ts_waitcnt lgkmcnt(0)" : "=&s"(c0), "=&s"(c1), "=&v"(v) : "v"(off), "s"(rs) : "memory");
    t[k] = (unsigned)(c1 - c0);
  }
  lds_vu_ptr tm = lds_at(smem);
  if (lane == 0) { tm[w] = min(t[0], t[3]); tm[8 + w] = min(t[1], t[2]); }
  int ro = SY_BT + i * 128 + lane * 16;
  asm volatile("" : "+v"(ro));
  LBAR();
  RDY(384 + i);
  if (threadIdx.x < 8) {
    unsigned m0 = tm[0], m1 = tm[8];
#pragma unroll
    for (int k = 1; k < 8; ++k) { m0 = min(m0, tm[k]); m1 = min(m1, tm[8 + k]); }
    const u32x2 tag = next_tag(e);
    st_sc1(rsy, ro, (u32x4){tag[0], tag[1], min(m0, 30000u) << 16 | min(m1, 30000u), 0u});
  }
}
template <int NB, typename F>
__device__ __forceinline__ u32x2 start_rows(const Args& p, int g, F&& between) {
  const int tid = threadIdx.x, lane = tid & 63, row0 = g * 16 + (tid >> 6) * 2;
  u32x4 a[4];
  u32x2 e;
  asm volatile("global_load_dwordx4 %0, %5, off\n\tglobal_load_dwordx4 %1, %5, off offset:1024\n\tglobal_load_dwordx4 %2, %5, off offset:2048\n\t"
               "global_load_dwordx4 %3, %5, off offset:3072\n\tbuffer_load_dwordx2 %4, off, %6, %7 sc1"
               : "=&v"(a[0]), "=&v"(a[1]), "=&v"(a[2]), "=&v"(a[3]), "=&v"(e)
               : "v"(p.x + (size_t)row0 * DM + lane * 8), "s"(raw_rsrc(p.ws, SY_TOTAL)), "s"(SY_EP) : "memory");
  between();
  asm volatile("s_waitcnt vmcnt(%5)" : "+v"(a[0]), "+v"(a[1]), "+v"(a[2]), "+v"(a[3]), "+v"(e) : "n"(NB) : "memory");
  RDY(512 + g);
  const __amdgpu_buffer_rsrc_t ro = mkrsrc(p.ws + WS_XN, S_LEN * XLD * 2);
#pragma unroll
  for (int u = 0; u < 2; ++u) {
    float v[16];
#pragma unroll
    for (int i = 0; i < 8; ++i) { v[2 * i] = lo_f(a[2 * u + (i >> 2)][i & 3]); v[2 * i + 1] = hi_f(a[2 * u + (i >> 2)][i & 3]); }
    float s = 0.f;
#pragma unroll
    for (int i = 0; i < 16; ++i) s += v[i];
    const float mean = wave_sum(s) * (1.0f / DM);
    float q = 0.f;
#pragma unroll
    for (int i = 0; i < 16; ++i) { float d = v[i] - mean; q += d * d; }
    const float var = rbf(wave_sum(q) * (1.0f / DM));
    const float mb = rbf(mean);
    const float inv = 1.0f / rbf(sqrtf(rbf(var + 1e-5f)));
#pragma unroll
    for (int k = 0; k < 2; ++k) {
      u32x4 o;
#pragma unroll
      for (int i = 0; i < 4; ++i) {
        const unsigned t = cvtpk(v[8 * k + 2 * i] - mb, v[8 * k + 2 * i + 1] - mb);
        o[i] = cvtpk(lo_f(t) * inv, hi_f(t) * inv);
      }
      st_sc1(ro, (row0 + u) * (XLD * 2) + k * 1024 + lane * 16, o);
    }
  }
  return next_tag(e);
}
__device__ __forceinline__ void report_rows(const Args& p, int g, u32x2 tag) {
  RDY(256 + g);
  if (threadIdx.x < 8) st_sc1(mkrsrc(p.ws + WS_SY, SY_TOTAL), SY_BT + (32 + g) * 128 + threadIdx.x * 16, (u32x4){tag[0], tag[1], tag[0], tag[1]});
}

__device__ __forceinline__ unsigned ld_flag0(__amdgpu_buffer_rsrc_t rs, int off) {
  return __builtin_amdgcn_raw_buffer_load_b32(rs, off, 0, 16);
}
__device__ __forceinline__ void glds16_sc1(const char* src, char* dst) {
  const unsigned d = __builtin_amdgcn_readfirstlane(lds_off(dst));
  unsigned keep;
  asm volatile("s_mov_b32 %0, m0\n\ts_mov_b32 m0, %2\n\ts_nop 0\n\tglobal_load_lds_dwordx4 %1, off sc1\n\ts_mov_b32 m0, %0"
               : "=&s"(keep) : "v"(src), "s"(d) : "memory");
}

__device__ __forceinline__ void touch_line(const char* src, char* dst) {
  const unsigned d = __builtin_amdgcn_readfirstlane(lds_off(dst));
  unsigned keep;
  asm volatile("s_mov_b32 %0, m0\n\ts_mov_b32 m0, %2\n\ts_nop 0\n\tglobal_load_lds_dword %1, off sc1\n\ts_mov_b32 m0, %0"
               : "=&s"(keep) : "v"(src), "s"(d) : "memory");
}

#define GPF 1
#define WREV 1
__device__ __forceinline__ void glds2(const char* base, int step, unsigned voff, unsigned dst) {
  unsigned keep;
  const char* base2 = base + step;
  asm volatile("s_mov_b32 %0, m0\n\ts_mov_b32 m0, %4\n\ts_nop 0\n\tglobal_load_lds_dwordx4 %1, %2\n\t"
               "s_add_u32 m0, m0, 0x2000\n\ts_nop 0\n\tglobal_load_lds_dwordx4 %1, %3\n\ts_mov_b32 m0, %0"
               : "=&s"(keep) : "v"(voff), "s"(base), "s"(base2), "s"(dst) : "memory", "scc");
}

__device__ __forceinline__ void gemm_stream(const char* A, int lda, const char* B, int ldb, int nk, char* smem, f32x4 (&acc)[2][4]) {
  int tid = threadIdx.x;
  asm volatile("" : "+v"(tid));
  const int lane = tid & 63, r = lane & 15, q = lane >> 4;
  const int wid = __builtin_amdgcn_readfirstlane(tid >> 6);
  const int wr = wid >> 1, wc = wid & 1;
  const unsigned va = (tid >> 3) * lda + 16 * ((tid & 7) ^ ((tid >> 3) & 7)), vb = (tid >> 4) * ldb + 16 * ((tid & 15) ^ swzB((tid >> 4) & 15));
  const unsigned lw = lds_off(smem) + wid * 1024;
  auto piece = [&](int kt, int i) {
    const int k = min(kt, nk - 1);
    const char* src = i < 2 ? A + k * 128 + i * 64 * lda : B + (size_t)(k * 64 + (i - 2) * 32) * ldb;
    unsigned keep;
    asm volatile("s_mov_b32 %0, m0\n\ts_mov_b32 m0, %3\n\ts_nop 0\n\tglobal_load_lds_dwordx4 %1, %2\n\ts_mov_b32 m0, %0"
                 : "=&s"(keep) : "v"(i < 2 ? va : vb), "s"(src), "s"(lw + (kt & 3) * 32768 + i * 8192) : "memory");
  };
  unsigned la[2], lb[4][2];
#pragma unroll
  for (int k = 0; k < 2; ++k) { la[k] = lds_off(smem) + offA(wr * 32 + r, k * 4 + q); asm volatile("" : "+v"(la[k])); }
#pragma unroll
  for (int n = 0; n < 4; ++n)
#pragma unroll
    for (int u = 0; u < 2; ++u) {
      lb[n][u] = lds_off(smem) + 16384 + offB(8 * q + 4 * u + (r >> 2), 2 * (wc * 4 + n) + ((r & 3) >> 1)) + 8 * (r & 1);
      asm volatile("" : "+v"(lb[n][u]));
    }
  typedef __attribute__((address_space(3))) const bf16x8* lds_frag_ptr;
  bf16x8 af[2][2], bf[2][4];
  auto ldA = [&](int bo, int ks, int m) { return *(lds_frag_ptr)(uintptr_t)(la[ks] + bo + m * 2048); };
  auto ldB = [&](int bo, int ks, int n) {
    s16x4 lo = __builtin_amdgcn_ds_read_tr16_b64_v4i16((lds_s16x4_ptr)(uintptr_t)(lb[n][0] + bo + ks * 8192));
    s16x4 hi = __builtin_amdgcn_ds_read_tr16_b64_v4i16((lds_s16x4_ptr)(uintptr_t)(lb[n][1] + bo + ks * 8192));
    return (bf16x8){lo[0], lo[1], lo[2], lo[3], hi[0], hi[1], hi[2], hi[3]};
  };
#pragma unroll
  for (int i = 0; i < 8; ++i) acc[i >> 2][i & 3] = (f32x4){0.f, 0.f, 0.f, 0.f};
  LBAR();
#pragma unroll
  for (int i = 0; i < 10; ++i) piece(i >> 2, i & 3);
  WAIT_V(6);
  LBAR();
#pragma unroll
  for (int m = 0; m < 2; ++m) af[0][m] = ldA(0, 0, m);
#pragma unroll
  for (int n = 0; n < 4; ++n) bf[0][n] = ldB(0, 0, n);
  auto loop = [&](auto HF) {
    for (int t0 = 0; t0 < nk; t0 += 4) {
#pragma unroll
      for (int b = 0; b < 4; ++b) {
        const int t = t0 + b;
#pragma unroll
        for (int ks = 0; ks < 2; ++ks) {
          if (ks == 1) {
            WAIT_V(4);
            __builtin_amdgcn_s_barrier();
          }
          const int nb = (ks ? (b + 1) & 3 : b) * 32768, nks = ks ^ 1;
#pragma unroll
          for (int n = 0; n < 4; ++n) {
            acc[0][n] = MFMA(bf[ks][n], af[ks][0], acc[0][n]);
            bf[nks][n] = ldB(nb, nks, n);
            acc[1][n] = MFMA(bf[ks][n], af[ks][1], acc[1][n]);
            if (n < 2) af[nks][n] = ldA(nb, nks, n);
            SCHED();
            if ((n & 1) == HF) { piece(t + 2 + ks, (n >> 1) + 2 * nks); SCHED(); }
          }
        }
      }
    }
  };
  if (wid >> 2) loop(std::integral_constant<int, 1>()); else loop(std::integral_constant<int, 0>());
  WAIT_V(0);
}

#define HSL 40
__device__ __forceinline__ void wait_h(const Args& p, int panel) {
  if (threadIdx.x == 0) {
    gu32* cnt = (gu32*)(p.ws + WS_SY + SY_CH) + panel * 32;
    for (unsigned spins = 0;;) {
      if (__hip_atomic_load(cnt, __ATOMIC_RELAXED, __HIP_MEMORY_SCOPE_AGENT) + 1u >= 28u) break;
      __builtin_amdgcn_s_sleep(HSL);
      if (++spins > (1u << 20)) { __hip_atomic_store((gu32*)(p.ws + WS_SY + SY_ABORT), 1u, __ATOMIC_RELAXED, __HIP_MEMORY_SCOPE_AGENT); break; }
    }
    __builtin_amdgcn_fence(__ATOMIC_ACQUIRE, "agent");
    asm volatile("s_waitcnt vmcnt(0)" ::: "memory");
  }
  __syncthreads();
}
__device__ __forceinline__ void job_down(const Args& p, int rblk, int ct, char* smem, __amdgpu_buffer_rsrc_t rsy) {
  wait_h(p, rblk >> 1);
  f32x4 acc[2][4];
  gemm_stream(p.ws + WS_HF + h_off(p, rblk), HLD, (const char*)(p.wd + ct * 128), DM * 2, FF / 64, smem, acc);
  wait_s(p, rsy, rblk, ct * 4, 4, rblk < NFUSE);
  int tid = threadIdx.x;
  asm volatile("" : "+v"(tid));
  const int lane = tid & 63, wid = tid >> 6, wr = wid >> 1, wc = wid & 1;
  const __amdgpu_buffer_rsrc_t ro = mkrsrc(p.ws + WS_G, S_LEN * DM * 2);
  SX sx[4];
#pragma unroll
  for (int i = 0; i < 4; ++i)
    sx[i] = get_sx(p, rblk, (rblk * 128 + wr * 32 + (i >> 1) * 16 + (lane & 15)) * DM + ct * 128 + wc * 64 + (i & 1) * 32 + (lane >> 4) * 8);
#pragma unroll
  for (int m = 0; m < 2; ++m) {
    const int row = rblk * 128 + wr * 32 + m * 16 + (lane & 15);
#pragma unroll
    for (int nb = 0; nb < 2; ++nb) {
      unsigned pk[2][2];
#pragma unroll
      for (int n = 0; n < 2; ++n) {
        float rr[4];
#pragma unroll
        for (int j = 0; j < 4; ++j) { float d = rbf(acc[m][2 * nb + n][j]); rr[j] = d * __builtin_amdgcn_rcpf(1.0f + __expf(-d)); }
        pk[n][0] = cvtpk(rr[0], rr[1]); pk[n][1] = cvtpk(rr[2], rr[3]);
      }
      u32x4 o = widen32(pk[0][0], pk[0][1], pk[1][0], pk[1][1]);
      put_g(p, ro, rblk, row * DM + ct * 128 + wc * 64 + nb * 32 + (lane >> 4) * 8, o, sx[2 * m + nb]);
    }
  }
  publish_flags(rsy, SY_FG + (rblk * 8 + ct) * 128, 0, 1);
}

constexpr int W_X = 131072, W_PM = W_X + 16384;
template <bool ISV>
__device__ __forceinline__ void job_proj(Args& p, int g, int which, int h, char* smem, __amdgpu_buffer_rsrc_t rsy) {
  const int tid = threadIdx.x, lane = tid & 63, r = lane & 15, q = lane >> 4;
  const int w = __builtin_amdgcn_readfirstlane(tid >> 6), kh = w >> 2, cg = w & 3;
  bf16x8 W[16][2];
  const unsigned lw = lds_off(smem) + w * 4096;
  unsigned vx[4];
  u32x2 tag;
  const char* xn = p.ws + WS_XN;
  auto row = [&](int t, int i) {
    const char* src = xn + (size_t)(min(t, 127) >> 1) * (SEG * XLD * 2) + (t & 1) * 512;
    unsigned keep;
    asm volatile("s_mov_b32 %0, m0\n\ts_mov_b32 m0, %3\n\ts_nop 0\n\tglobal_load_lds_dwordx4 %1, %2 sc1\n\ts_mov_b32 m0, %0"
                 : "=&s"(keep) : "v"(vx[i]), "s"(src), "s"(lw + (t & 3) * 32768 + i * 1024) : "memory");
  };
  {
    const u16* Wm = which == 0 ? p.Wk : p.Wq;
    asm volatile("" : "+s"(Wm));
    if (ISV) Wm = p.Wv;
    const unsigned vo = (tid >> 4) * (HD * 2) + 16 * ((lane & 15) ^ swzB((tid >> 4) & 15));
    auto stage = [&](int j) {
      const char* src = (const char*)(Wm + (size_t)(WREV ? 7 - j : j) * 64 * HD + h * DH);
      const unsigned dst = lds_off(smem) + (j & 3) * 32768 + w * 1024;
      glds2(src, 32 * HD * 2, vo, dst);
      glds2(src + 512 * HD * 2, 32 * HD * 2, vo, dst + 16384);
    };
    tag = start_rows<16>(p, 32 + g, [&]() { stage(0); stage(1); stage(2); stage(3); });
#pragma unroll
    for (int i = 0; i < 4; ++i) vx[i] = ((4 * w + i) ^ (ISV ? 0 : (lane >> 1) & 16)) * (XLD * 2) + (lane >> 5) * 1024 + (((lane & 31) ^ ((4 * w + i) & 15)) << 4);
    Boot bt;
#pragma unroll
    for (int j = 0; j < 8; ++j) {
      if (j < 4) WAIT_V(16); else if (j == 4) WAIT_V(12); else if (j == 5) WAIT_V(8); else if (j == 6) WAIT_V(4); else WAIT_V(10);
      if (j == 6 && w == 0) lds_at(smem + W_PM)[0] = boot_done(p.ws, bt, true, tag);
      LBAR();
      if (j == 4) report_rows(p, 32 + g, tag);
      const char* sb = smem + (j & 3) * 32768 + kh * 16384;
#pragma unroll
      for (int k = 0; k < 4; ++k) {
        const int ks = k >> 1, a = k & 1;
        const unsigned ch = ISV ? 2 * (2 * cg + (a ^ kh)) + ((r & 3) >> 1) : 4 * cg + (r & 3), by = ISV ? 8 * (r & 1) : 8 * a;
        s16x4 lo = __builtin_amdgcn_ds_read_tr16_b64_v4i16((lds_s16x4_ptr)(sb + offB(32 * ks + 8 * q + (r >> 2), ch) + by));
        s16x4 hi = __builtin_amdgcn_ds_read_tr16_b64_v4i16((lds_s16x4_ptr)(sb + offB(32 * ks + 8 * q + 4 + (r >> 2), ch) + by));
        W[2 * (WREV ? 7 - j : j) + ks][a] = (bf16x8){lo[0], lo[1], lo[2], lo[3], hi[0], hi[1], hi[2], hi[3]};
      }
      LBAR();
      if (j == 3 && w == 0) boot_ask(p.ws, bt);
      if (j + 4 < 8) stage(j + 4);
      if (j == 6) {
#pragma unroll
        for (int i = 0; i < 10; ++i) row(i >> 2, i & 3);
      }
    }
    p.s0 = uni(lds_at(smem + W_PM)[0] >> 31);
  }
  RDY(128 + g);
  typedef __attribute__((address_space(3))) const bf16x8* lds_frag_ptr;
  typedef __attribute__((address_space(3))) f32x4* lds_f4_ptr;
  unsigned la[2][4];
#pragma unroll
  for (int i = 0; i < 8; ++i) {
    la[i >> 2][i & 3] = lds_off(smem) + (i >> 2) * 65536 + r * 1024 + kh * 512 + (((4 * (i & 3) + q) ^ r) << 4);
    asm volatile("" : "+v"(la[i >> 2][i & 3]));
  }
  unsigned xw = lds_off(smem) + W_X + w * 2048 + lane * 16, xr = xw ^ 8192;
  asm volatile("" : "+v"(xw), "+v"(xr));
  auto ldA = [&](int b, int ks, int m) { return *(lds_frag_ptr)(uintptr_t)(la[b >> 1][ks & 3] + (b & 1) * 32768 + m * 16384 + (ks >> 2) * 256); };
  const __amdgpu_buffer_rsrc_t ro = mkrsrc(p.ws + WS_T, T_BYTES);
  const int t0 = which * T_Q + kseg(0, h), vt = (ISV ? 2 * cg + kh : 4 * kh + cg) * 1024 + lane * 16;
  f32x4 acc[2][2][2], xs[2];
  bf16x8 af[4][2];
#pragma unroll
  for (int i = 0; i < 4; ++i) acc[1][i >> 1][i & 1] = (f32x4){0.f, 0.f, 0.f, 0.f};
  auto put = [&](int sp) {
#pragma unroll
    for (int b = 0; b < 2; ++b) *(lds_f4_ptr)(uintptr_t)(xw + b * 1024) = acc[sp][1][b];
  };
  auto get = [&]() {
#pragma unroll
    for (int b = 0; b < 2; ++b) xs[b] = *(lds_f4_ptr)(uintptr_t)(xr + b * 1024);
  };
  auto fin = [&](int sp, int sg) {
    const f32x4 x = acc[sp][0][0] + xs[0], y = acc[sp][0][1] + xs[1];
    __builtin_amdgcn_raw_buffer_store_b128((u32x4){cvtpk(x[0], x[1]), cvtpk(x[2], x[3]), cvtpk(y[0], y[1]), cvtpk(y[2], y[3])}, ro,
                                           vt + (sg < 0 ? 0x70000000 : t_off(p, t0 + sg)), 0, 16);
  };
  WAIT_V(6);
  LBAR();
#pragma unroll
  for (int i = 0; i < 4; ++i) af[i >> 1][i & 1] = ldA(0, i >> 1, i & 1);
  auto loop = [&](auto KH) {
#pragma unroll 1
    for (int tb = 0; tb < 2 * NSEG; tb += 4) {
#pragma unroll
      for (int b = 0; b < 4; ++b) {
        const int sp = b >> 1, u = b & 1, sg = (tb + b) >> 1;
#pragma unroll
        for (int ks = 0; ks < 8; ++ks) {
          if (ks == 4) {
            if (KH == 0 && b == 0 && tb == 24 && w == 0) wait_rows(p.ws, tag);
            if (u) WAIT_V(5); else WAIT_V(4);
            __builtin_amdgcn_s_barrier();
            asm volatile("" ::: "memory");
            if (u == 0 && KH == 1 && tid == 256 && sg >= 2) __builtin_amdgcn_raw_buffer_store_b32(sg - 2, rsy, fkq(h, which), 0, 16);
            if (u == 0) get();
          }
#pragma unroll
          for (int m = 0; m < 2; ++m) {
            af[(ks + 2) & 3][m] = ldA(ks < 6 ? b : (b + 1) & 3, (ks + 2) & 7, m);
#pragma unroll
            for (int n = 0; n < 2; ++n) {
              f32x4& d = ISV ? acc[sp][n][m] : acc[sp][m][n];
              const f32x4 c = (u | ks) ? d : (f32x4){0.f, 0.f, 0.f, 0.f};
              d = ISV ? MFMA(af[ks & 3][m], W[8 * u + ks][n], c) : MFMA(W[8 * u + ks][n], af[ks & 3][m], c);
            }
          }
          if ((ks & 1) == KH) { SCHED(); row(tb + b + (ks < 4 ? 2 : 3), ((ks >> 1) + 2) & 3); }
          if (u == 0 && ks == 0) put(sp ^ 1);
          if (u == 0 && ks == 7 - KH) fin(sp ^ 1, sg - 1);
          SCHED();
        }
      }
    }
  };
  if (kh) loop(std::integral_constant<int, 1>()); else loop(std::integral_constant<int, 0>());
  put(1);
  LBAR();
  get();
  fin(1, NSEG - 1);
  WAIT_V(0);
  LBAR();
  if (tid == 0) __builtin_amdgcn_raw_buffer_store_b32(NSEG - 1, rsy, fkq(h, which), 0, 16);
}

enum { J_NONE = 0, J_GU = 3, J_PR = 5, J_CB = 8, J_DN = 14 };
struct BigJob { int type, panel, idx; };
struct BigPtr { const char *a, *b0, *b1; };
constexpr int G_LDA = XLD * 2, G_LDB = FF * 2;

__device__ __forceinline__ BigPtr big_ptrs(const Args& p, const BigJob& j) {
  BigPtr r;
  r.a = p.ws + WS_XN + (size_t)j.panel * 128 * G_LDA;
  r.b0 = (const char*)(p.wg + j.idx * 128); r.b1 = (const char*)(p.wu + j.idx * 128);
  return r;
}
__device__ __forceinline__ void big_publish(const Args& p, const BigJob& j) {
  int tid = threadIdx.x;
  asm volatile("" : "+v"(tid));
  if (j.type == J_GU && tid == 0)
    __hip_atomic_fetch_add((gu32*)(p.ws + WS_SY + SY_CH) + j.panel * 16, 1u, __ATOMIC_RELAXED, __HIP_MEMORY_SCOPE_AGENT);
}
typedef f32x4 Acc[2][2][4][2];
__device__ __forceinline__ u32x4 pack32(f32x4 a, f32x4 b) {
  return widen32(cvtpk(a[0], a[1]), cvtpk(a[2], a[3]), cvtpk(b[0], b[1]), cvtpk(b[2], b[3]));
}
#define st_epi(r, o, v) st_sc1(r, o, v)
__device__ __forceinline__ void big_epilogue(const Args& p, const BigJob& j, Acc& acc, char* smem) {
  int tid = threadIdx.x;
  asm volatile("" : "+v"(tid));
  const int lane = tid & 63;
  const int wid = __builtin_amdgcn_readfirstlane(tid >> 6);
  const int wr = wid >> 2, wc = wid & 3;
  {
    const __amdgpu_buffer_rsrc_t ro = mkrsrc(p.ws + WS_HF, S_LEN * 16384);
#pragma unroll
    for (int i = 0; i < 8; ++i) {
      const int row = wr * 64 + (i & 3) * 16 + (lane & 15);
      f32x4 hh[2];
#pragma unroll
      for (int n = 0; n < 2; ++n)
#pragma unroll
        for (int jj = 0; jj < 4; ++jj) {
          const float g = acc[i >> 2][0][i & 3][n][jj], u = acc[i >> 2][1][i & 3][n][jj];
          hh[n][jj] = __builtin_amdgcn_rcpf(1.0f + __builtin_amdgcn_exp2f(g * -1.4426950408889634f)) * u;
        }
      st_epi(ro, h_off(p, j.panel + (i >> 2)) + row * HLD + (j.idx * 128 + wc * 32 + (lane >> 4) * 8) * 2, pack32(hh[0], hh[1]));
    }
  }
}
#define JST(n, i, v) do {} while (0)
constexpr int JOBW = 115;
__constant__ unsigned short JOBS[115] = {
  5120, 5120, 5120, 5120, 5120, 5120, 5120, 5120, 5120, 5120, 5120, 5120, 5120, 5120, 5120, 5120,
  5120, 5120, 5120, 5120, 5120, 5120, 5120, 5120, 3296, 3297, 3298, 3299, 3300, 3301, 3302, 3264,
  3265, 3266, 3267, 3268, 3269, 3270, 3232, 3233, 3234, 3235, 3236, 3237, 3238, 3200, 3201, 3202,
  3203, 3204, 3205, 3206, 3168, 3169, 3170, 3171, 3172, 3173, 3174, 3136, 3137, 3138, 3139, 3140,
  3141, 3142, 3104, 3105, 3106, 3107, 3108, 3109, 3110, 3072, 3073, 3074, 3075, 3076, 3077, 3078,
  14384, 14385, 14386, 14387, 14388, 14389, 14390, 14391, 14336, 14337, 14338, 14339, 14340, 14341, 14342, 14343,
  14352, 14353, 14354, 14355, 14356, 14357, 14358, 14359, 14368, 14369, 14370, 14371, 14372, 14373, 14374, 14375,
  8384, 8400, 0,
};
__device__ __forceinline__ void gemm_role(Args& p, int g, char* smem) {
  const __amdgpu_buffer_rsrc_t rsy = mkrsrc(p.ws + WS_SY, SY_TOTAL);
  const int x = g & 3, k = g >> 2;
  gu32* queue = (gu32*)(p.ws + WS_SY + SY_Q) + x * 32;
  volatile unsigned* slot = reinterpret_cast<volatile unsigned*>(smem + 163824);
  unsigned nxt = 0;
  auto request = [&]() {
    if (threadIdx.x == 0) nxt = __hip_atomic_fetch_add(queue, 1u, __ATOMIC_RELAXED, __HIP_MEMORY_SCOPE_AGENT) + 1u + NGW;
  };
  auto post = [&]() { if (threadIdx.x == 0) *slot = nxt; };
  auto decode = [&](int j) {
    const unsigned e = JOBS[min(j, JOBW - 1)];
    const int i = e & 15;
    BigJob b;
    b.type = e >> 10; b.panel = (e >> 4) & 63;
    b.idx = x + 4 * i;
    if (b.type == J_DN) { b.idx = i; b.panel = 4 * b.panel + x; }
    return b;
  };
  int nj = 0; (void)nj;
  auto claim = [&]() {
    request();
    LBAR();
    post();
    LBAR();
    return decode((int)__builtin_amdgcn_readfirstlane(*slot));
  };
  if (g < 4 && threadIdx.x == 0) { LST(480 + 4 * g); LSTC(480 + 4 * g + 1); }
  BigJob cur;
  if (k < 24) {
    JST(nj, 0, J_PR); JST(nj, 1, __builtin_amdgcn_s_memrealtime());
    if (k >= 16) job_proj<true>(p, g, 2, x * 8 + (k & 7), smem, rsy);
    else job_proj<false>(p, g, k >> 3, x * 8 + (k & 7), smem, rsy);
    JST(nj, 3, __builtin_amdgcn_s_memrealtime()); ++nj;
    if (threadIdx.x == 0) __builtin_amdgcn_fence(__ATOMIC_ACQUIRE, "agent");
    cur = claim();
  } else {
    const u32x2 tag = start_rows<0>(p, g - 96, []() {});
    WAIT_V(0);
    LBAR();
    report_rows(p, g - 96, tag);
    if (threadIdx.x < 64) {
      wait_rows(p.ws, tag);
      Boot bt;
      boot_ask(p.ws, bt);
      WAIT_V(0);
      *slot = boot_done(p.ws, bt, true, tag);
      __builtin_amdgcn_fence(__ATOMIC_ACQUIRE, "agent");
      WAIT_V(0);
    }
    LBAR();
    p.s0 = *slot >> 31;
    RDY(128 + g);
    cur = decode(k);
  }
  p.s0 = uni(p.s0);
  while (cur.type != J_NONE) {
    JST(nj, 0, cur.type | cur.panel << 8); JST(nj, 1, __builtin_amdgcn_s_memrealtime());
    if (cur.type != J_GU) {
      if (cur.type == J_DN) job_down(p, cur.panel, cur.idx, smem, rsy);
      else job_comb(p, cur.panel, x, rsy);
      JST(nj, 3, __builtin_amdgcn_s_memrealtime()); ++nj;
      cur = claim();
      continue;
    }
    int tid = threadIdx.x;
    asm volatile("" : "+v"(tid));
    const int lane = tid & 63;
    const int wid = __builtin_amdgcn_readfirstlane(tid >> 6);
    const int wr = wid >> 2, wc = wid & 3;
    const unsigned ra = tid >> 3, ca = 16 * ((tid & 7) ^ ((tid >> 3) & 7)), rb = tid >> 4, cb = 16 * ((tid & 15) ^ swzB((tid >> 4) & 15));
    const unsigned lw = lds_off(smem) + wid * 1024;
#define B8_SA(b, h) (((b) * 2 + (h)) * 16384)
#define B8_SB(b, h) ((4 + (b) * 2 + (h)) * 16384)
#define B8_STA(b, h, P, kt) glds2((P).a + (size_t)(h) * 128 * G_LDA + (kt) * 128, 64 * G_LDA, ra * G_LDA + ca, lw + B8_SA(b, h))
#define B8_STB(b, h, P, kt) glds2(((h) ? (P).b1 : (P).b0) + (size_t)(kt) * 64 * G_LDB, 32 * G_LDB, rb * G_LDB + cb, lw + B8_SB(b, h))
#define B8_LDA(b, h) do { _Pragma("unroll") for (int m = 0; m < 4; ++m) _Pragma("unroll") for (int kk = 0; kk < 2; ++kk) \
      At[m][kk] = ldsf(smem + B8_SA(b, h) + offA(wr * 64 + m * 16 + (lane & 15), kk * 4 + (lane >> 4))); } while (0)
#define B8_LDB(dst, b, h) do { _Pragma("unroll") for (int n = 0; n < 2; ++n) _Pragma("unroll") for (int kk = 0; kk < 2; ++kk) \
      dst[n][kk] = ld_bfrag(smem + B8_SB(b, h), kk, wc * 2 + n, lane); } while (0)
#define B8_MMA(ai, bj, Bt) do { __builtin_amdgcn_s_setprio(1); _Pragma("unroll") for (int m = 0; m < 4; ++m) _Pragma("unroll") for (int n = 0; n < 2; ++n) \
      _Pragma("unroll") for (int kk = 0; kk < 2; ++kk) acc[ai][bj][m][n] = MFMA(Bt[n][kk], At[m][kk], acc[ai][bj][m][n]); __builtin_amdgcn_s_setprio(0); } while (0)
#define WAIT_L(n) asm volatile("s_waitcnt lgkmcnt(%0)" :: "n"(n) : "memory")
#define BAR() __builtin_amdgcn_s_barrier()
#define WAIT_RX(c) asm volatile("s_waitcnt vmcnt(16)\n\ts_cmp_lg_u32 %0, 0\n\ts_cbranch_scc1 .Lrx%=\n\ts_waitcnt vmcnt(8)\n.Lrx%=:" :: "s"(c) : "memory", "scc")
    Acc acc;
    bf16x8 At[4][2], B0[2][2], B1[2][2];
    BigPtr cp = big_ptrs(p, cur);
    BigJob pend; pend.type = J_NONE; pend.panel = 0; pend.idx = 0;
    LBAR();
    B8_STB(0, 0, cp, 0); B8_STB(0, 1, cp, 0); B8_STA(0, 0, cp, 0); B8_STA(0, 1, cp, 0);
    if (wr == 1) BAR();
    WAIT_V(2); BAR();
    B8_STB(1, 0, cp, 1); B8_STA(1, 0, cp, 1); B8_STB(1, 1, cp, 1);
    WAIT_V(6); BAR();
    BigJob nx = pend;
    for (;;) {
      bool chain = false;
      BigPtr np = cp;
      constexpr int nt = DM / 64;
#pragma unroll
      for (int i = 0; i < 32; ++i) acc[i >> 4][(i >> 3) & 1][(i >> 1) & 3][i & 1] = (f32x4){0.f, 0.f, 0.f, 0.f};
#pragma unroll 1
      for (int t = 0; t < nt; t += 2) {
        const bool last = t == nt - 2;
        if (t == nt - 6) request();
        if (t == nt - 4) post();
        if (last) {
          nx = decode((int)__builtin_amdgcn_readfirstlane(*slot));
          chain = nx.type == J_GU;
          if (chain) np = big_ptrs(p, nx);
        }
        const BigPtr sp = np;
        const int t2 = last ? 0 : t + 2;
        if (t == 2) big_publish(p, pend);
        const int rx = __builtin_amdgcn_readfirstlane(t == 0 && pend.type != J_NONE);
        B8_LDB(B0, 0, 0); B8_LDB(B1, 0, 1); SCHED(); B8_LDA(0, 0); B8_STA(1, 1, cp, t + 1);
        WAIT_RX(rx);
        WAIT_L(0); BAR(); B8_MMA(0, 0, B0); B8_MMA(0, 1, B1); BAR(); SCHED();
        B8_LDA(0, 1); B8_STB(0, 0, sp, t2); B8_STB(0, 1, sp, t2); B8_STA(0, 0, sp, t2);
        WAIT_RX(rx);
        WAIT_L(0); BAR(); B8_MMA(1, 0, B0); B8_MMA(1, 1, B1); BAR(); SCHED();
        B8_LDB(B0, 1, 0); B8_LDB(B1, 1, 1); SCHED(); B8_LDA(1, 0); B8_STA(0, 1, sp, t2);
        WAIT_V(8); WAIT_L(0); BAR(); B8_MMA(0, 0, B0); B8_MMA(0, 1, B1); BAR(); SCHED();
        B8_LDA(1, 1); B8_STB(1, 0, sp, t2 + 1); B8_STB(1, 1, sp, t2 + 1); B8_STA(1, 0, sp, t2 + 1);
        WAIT_V(8); WAIT_L(0); BAR(); B8_MMA(1, 0, B0); B8_MMA(1, 1, B1); BAR(); SCHED();
      }
      if (wr == 0) BAR();
      JST(nj, 2, __builtin_amdgcn_s_memrealtime());
      big_epilogue(p, cur, acc, smem);
      if (!chain) break;
      JST(nj, 3, __builtin_amdgcn_s_memrealtime()); ++nj;
      pend = cur; cur = nx; cp = np;
      if (wr == 1) BAR();
      JST(nj, 0, cur.type | cur.panel << 8); JST(nj, 1, __builtin_amdgcn_s_memrealtime());
    }
    WAIT_V(0);
    BAR();
    big_publish(p, cur);
    JST(nj, 3, __builtin_amdgcn_s_memrealtime()); ++nj;
    cur = nx;
  }
  if (threadIdx.x == 0) { LST(128 + g); }
  if (g < 4 && threadIdx.x == 0) { LST(480 + 4 * g + 2); LSTC(480 + 4 * g + 3); }
}

__device__ __forceinline__ unsigned now_q() { return (unsigned)__builtin_amdgcn_s_memrealtime() * 4u; }
__device__ __forceinline__ void pace_until(unsigned t) {
#pragma unroll 1
  for (int i = 0; i < 256; ++i) {
    const int d = (int)(t - now_q());
    if (d <= 0) break;
    if (d > 32) __builtin_amdgcn_s_sleep(1);
  }
}
#define CLB_E "s76", "s77", "s78", "s79", "s80", "s81", "s82", "s83", "s84", "s85", "s86", "s87", "s88", "s89"
#define EDGE_ASM \
  "s_memtime s[76:77]\n\t" \
  "s_memrealtime s[78:79]\n\t" \
  "s_nop 3\n\t" \
  "s_memrealtime s[80:81]\n\t" \
  "s_nop 3\n\t" \
  "s_memrealtime s[82:83]\n\t" \
  "s_nop 3\n\t" \
  "s_memrealtime s[84:85]\n\t" \
  "s_nop 3\n\t" \
  "s_memrealtime s[86:87]\n\t" \
  "s_nop 3\n\t" \
  "s_memrealtime s[88:89]\n\t" \
  "s_mov_b32 s93, 0\n\t" \
  "s_waitcnt lgkmcnt(0)\n\t" \
  "s_cmp_eq_u32 s80, s78\n\t" \
  "s_addc_u32 s93, s93, 0\n\t" \
  "s_cmp_eq_u32 s82, s78\n\t" \
  "s_addc_u32 s93, s93, 0\n\t" \
  "s_cmp_eq_u32 s84, s78\n\t" \
  "s_addc_u32 s93, s93, 0\n\t" \
  "s_cmp_eq_u32 s86, s78\n\t" \
  "s_addc_u32 s93, s93, 0\n\t" \
  "s_cmp_eq_u32 s88, s78\n\t" \
  "s_addc_u32 s93, s93, 0\n\t" \
  "s_mul_i32 s93, s93, 21\n\t" \
  "s_add_u32 s76, s76, 15\n\t" \
  "s_add_u32 %[kc], s76, s93\n\t" \
  "s_lshl2_add_u32 %[kq], s78, 16\n\t"
__device__ __forceinline__ unsigned time_of(unsigned cyc) {
  unsigned c, q;
  asm volatile(EDGE_ASM : [kc] "=&s"(c), [kq] "=&s"(q) :: CLB_E, "s93", "scc");
  return q - (((c - cyc) * 11900u) >> 16);
}
#define DS_RED 58
#define DS_MEM 30
#define LEAD_DN 2
#define LEAD_UP 12
__device__ __forceinline__ u32x2 ld2_sc1(__amdgpu_buffer_rsrc_t rs, int off, int soff = 0) {
  return __builtin_amdgcn_raw_buffer_load_b64(rs, off, soff, 16);
}
__device__ __forceinline__ void st2_sc1(__amdgpu_buffer_rsrc_t rs, int off, u32x2 v) {
  __builtin_amdgcn_raw_buffer_store_b64(v, rs, off, 0, 16);
}
__device__ __forceinline__ int row_max(int v) {
  v = max(v, __builtin_amdgcn_update_dpp(0, v, 0x128, 0xf, 0xf, false));
  v = max(v, __builtin_amdgcn_update_dpp(0, v, 0x124, 0xf, 0xf, false));
  v = max(v, __builtin_amdgcn_update_dpp(0, v, 0x122, 0xf, 0xf, false));
  v = max(v, __builtin_amdgcn_update_dpp(0, v, 0x121, 0xf, 0xf, false));
  return v;
}
__device__ __forceinline__ int wave_max(int v) {
  v = row_max(v);
  return max(max(__builtin_amdgcn_readlane(v, 0), __builtin_amdgcn_readlane(v, 16)),
             max(__builtin_amdgcn_readlane(v, 32), __builtin_amdgcn_readlane(v, 48)));
}
__device__ __forceinline__ int track(int est, int x, int up, int dn, int out) {
  const int d = x - est;
  return est + (d > out ? dn : max(min(d, up), -dn));
}
#define WSL 12
#define PLAN_SLEEP 2
__device__ __forceinline__ unsigned latest(unsigned t) {
  const unsigned t0 = __builtin_amdgcn_readfirstlane(t);
  return t0 + wave_max((int)(t - t0));
}
__device__ __forceinline__ void watch(__amdgpu_buffer_rsrc_t rsy, int s, int lane, unsigned t0, const unsigned* tgt_, gu32* abortw, int hs) {
  pace_until(t0);
  u32x2 ra;
  for (unsigned spins = 0;;) {
    asm volatile("" ::: "memory");
    ra = ld2_sc1(rsy, lane * 128, SY_BR + hs);
    const bool all = __all((int)ra[0] >= s);
    SMP(4, s, spins);
    if (all) break;
    if (spins < 3) __builtin_amdgcn_s_sleep(PLAN_SLEEP); else __builtin_amdgcn_s_sleep(WSL);
    if (spin_fail(spins, abortw)) break;
  }
  const unsigned t = latest(ra[0] == (unsigned)s ? ra[1] : t0) + DS_MEM;
  lds_vu_ptr tgt = lds_at(tgt_);
  if (lane < 2) tgt[lane] = lane ? t : s + 1;
  SMP(0, s, (int)(t - t0)); SMP(1, s, (int)(t - now_q()));
}
__device__ __forceinline__ unsigned ld_flag(__amdgpu_buffer_rsrc_t rs, int off) {
  return __builtin_amdgcn_raw_buffer_load_b32(rs, off, 0, 16);
}

constexpr unsigned long long QK_MAP[8] = {0xbaf9876d543210ecull, 0xa98f76d543210bceull, 0x98f76d543210ecbaull, 0x876fd543210bcea9ull, 0x76fd543210ecba98ull, 0x65df43210bcea987ull, 0x5df43210ecba9876ull, 0xd43f210bcea98765ull};
#define QK_ASM_0 \
  "s_waitcnt lgkmcnt(0)\n\t" \
  "v_subrev_u32 v244, 0x8000, v250\n\t" \
  "ds_read_b128 v[216:219], v248 offset:32768\n\t" \
  "ds_read_b128 v[220:223], v248 offset:33792\n\t" \
  "ds_read_b128 v[224:227], v248 offset:34816\n\t" \
  "ds_read_b128 v[228:231], v248 offset:35840\n\t" \
  "ds_read_b128 v[208:211], v244 offset:0\n\t" \
  "ds_read_b128 v[212:215], v244 offset:1024\n\t" \
  "ds_read_b128 v[236:239], v244 offset:2048\n\t" \
  "ds_read_b128 v[240:243], v244 offset:3072\n\t" \
  "v_mov_b64 v[192:193], 0\n\t" \
  "v_mov_b64 v[194:195], 0\n\t" \
  "v_mov_b64 v[196:197], 0\n\t" \
  "v_mov_b64 v[198:199], 0\n\t" \
  "s_movk_i32 s92, 0x2000\n\t" \
  ".Lwt%=:\n\t" \
  "ds_read_b64 v[200:201], v248 offset:30720\n\t" \
  "s_memrealtime s[90:91]\n\t" \
  "s_sub_u32 s92, s92, 1\n\t" \
  "s_waitcnt lgkmcnt(0)\n\t" \
  "v_readfirstlane_b32 s93, v200\n\t" \
  "v_readfirstlane_b32 %[tgt], v201\n\t" \
  "s_cbranch_scc1 .Lgo%=\n\t" \
  "s_cmp_eq_u32 s93, %[step]\n\t" \
  "s_cbranch_scc1 .Lwx%=\n\t" \
  "s_sleep 1\n\t" \
  "s_branch .Lwt%=\n\t" \
  ".Lwx%=:\n\t" \
  "s_lshl_b32 s93, s90, 2\n\t" \
  "s_sub_i32 s93, %[tgt], s93\n\t" \
  "s_cmp_le_i32 s93, 0\n\t" \
  "s_cbranch_scc1 .Lgo%=\n\t" \
  "s_min_i32 s93, s93, 8000\n\t" \
  "s_add_u32 s93, s93, 3\n\t" \
  "s_lshr_b32 s93, s93, 2\n\t" \
  "s_add_u32 s93, s93, s90\n\t" \
  "s_memrealtime s[90:91]\n\t" \
  ".Lpace%=:\n\t" \
  "s_memrealtime s[94:95]\n\t" \
  "s_waitcnt lgkmcnt(1)\n\t" \
  "s_sub_i32 s91, s90, s93\n\t" \
  "s_cmp_ge_i32 s91, 0\n\t" \
  "s_cbranch_scc1 .Lgo%=\n\t" \
  "s_memrealtime s[90:91]\n\t" \
  "s_waitcnt lgkmcnt(1)\n\t" \
  "s_sub_i32 s95, s94, s93\n\t" \
  "s_cmp_lt_i32 s95, 0\n\t" \
  "s_cbranch_scc1 .Lpace%=\n\t" \
  ".Lgo%=:\n\t" \
  "s_mov_b32 s92, 0\n\t" \
  ".Lpoll%=:\n\t" \
  "buffer_load_dwordx4 v[200:203], v248, %[rs], %[som] offen sc1\n\t" \
  "buffer_load_dwordx4 v[204:207], v248, %[rs], %[som2] offen sc1\n\t" \
  "s_waitcnt vmcnt(1) lgkmcnt(0)\n\t" \
  "ds_write_b128 v249, v[200:203]\n\t" \
  "v_smfmac_f32_16x16x64_bf16 v[192:195], v[200:203], v[208:215], v251\n\t" \
  "v_max3_u32 v244, v200, v201, v202\n\t" \
  "v_smfmac_f32_16x16x64_bf16 v[196:199], v[200:203], v[236:243], v251\n\t" \
  "s_waitcnt vmcnt(0)\n\t" \
  "ds_write_b128 v249, v[204:207] offset:8192\n\t" \
  "v_smfmac_f32_16x16x64_bf16 v[192:195], v[204:207], v[216:223], v251\n\t" \
  "v_max3_u32 v244, v244, v203, v204\n\t" \
  "v_max3_u32 v244, v244, v205, v206\n\t" \
  "v_smfmac_f32_16x16x64_bf16 v[196:199], v[204:207], v[224:231], v251\n\t" \
  "v_max_u32 v244, v244, v207\n\t" \
  "v_cmp_le_u32 vcc, %[sent], v244\n\t" \
  "s_cbranch_vccnz .Lslow%=\n\t" \
  ".Lok%=:\n\t" \
  "ds_read_b128 v[216:219], v250 offset:32768\n\t" \
  "ds_read_b128 v[220:223], v250 offset:33792\n\t" \
  "ds_read_b128 v[224:227], v250 offset:34816\n\t" \
  "ds_read_b128 v[228:231], v250 offset:35840\n\t" \
  "s_waitcnt lgkmcnt(4)\n\t" \
  "s_barrier\n\t" \
  "ds_read_b128 v[208:211], v248 offset:1024\n\t" \
  "ds_read_b128 v[212:215], v248 offset:2048\n\t" \
  "ds_read_b128 v[236:239], v248 offset:3072\n\t" \
  "ds_read_b128 v[240:243], v248 offset:4096\n\t" \
  "s_waitcnt lgkmcnt(3)\n\t" \
  "ds_read_b128 v[244:247], v248 offset:13312\n\t" \
  "v_smfmac_f32_16x16x64_bf16 v[192:195], v[208:211], v[216:223], v251\n\t" \
  "ds_read_b128 v[216:219], v250 offset:0\n\t" \
  "v_smfmac_f32_16x16x64_bf16 v[196:199], v[208:211], v[224:231], v251\n\t" \
  "s_waitcnt lgkmcnt(4)\n\t" \
  "ds_read_b128 v[220:223], v250 offset:1024\n\t" \
  "v_smfmac_f32_16x16x64_bf16 v[192:195], v[212:215], v[0:7], v251\n\t" \
  "ds_read_b128 v[224:227], v250 offset:2048\n\t" \
  "v_smfmac_f32_16x16x64_bf16 v[196:199], v[212:215], v[8:15], v251\n\t" \
  "s_waitcnt lgkmcnt(5)\n\t" \
  "ds_read_b128 v[228:231], v250 offset:3072\n\t" \
  "v_smfmac_f32_16x16x64_bf16 v[192:195], v[236:239], v[16:23], v251\n\t" \
  "ds_read_b128 v[200:203], v248 offset:5120\n\t" \
  "v_smfmac_f32_16x16x64_bf16 v[196:199], v[236:239], v[24:31], v251\n\t" \
  "s_waitcnt lgkmcnt(6)\n\t" \
  "ds_read_b128 v[204:207], v248 offset:6144\n\t" \
  "v_smfmac_f32_16x16x64_bf16 v[192:195], v[240:243], v[32:39], v251\n\t" \
  "ds_read_b128 v[208:211], v248 offset:7168\n\t" \
  "v_smfmac_f32_16x16x64_bf16 v[196:199], v[240:243], v[40:47], v251\n\t" \
  "s_waitcnt lgkmcnt(5)\n\t" \
  "ds_read_b128 v[212:215], v248 offset:9216\n\t" \
  "v_smfmac_f32_16x16x64_bf16 v[192:195], v[244:247], v[216:223], v251\n\t" \
  "s_waitcnt lgkmcnt(4)\n\t" \
  "v_smfmac_f32_16x16x64_bf16 v[196:199], v[244:247], v[224:231], v251\n\t" \
  "s_waitcnt lgkmcnt(3)\n\t" \
  "ds_read_b128 v[236:239], v248 offset:10240\n\t" \
  "v_smfmac_f32_16x16x64_bf16 v[192:195], v[200:203], v[48:55], v251\n\t" \
  "ds_read_b128 v[240:243], v248 offset:11264\n\t" \
  "v_smfmac_f32_16x16x64_bf16 v[196:199], v[200:203], v[56:63], v251\n\t" \
  "s_waitcnt lgkmcnt(4)\n\t" \
  "v_smfmac_f32_16x16x64_bf16 v[192:195], v[204:207], v[64:71], v251\n\t" \
  "ds_read_b128 v[244:247], v248 offset:12288\n\t" \
  "v_smfmac_f32_16x16x64_bf16 v[196:199], v[204:207], v[72:79], v251\n\t" \
  "s_waitcnt lgkmcnt(4)\n\t" \
  "v_smfmac_f32_16x16x64_bf16 v[192:195], v[208:211], v[80:87], v251\n\t" \
  "ds_read_b128 v[200:203], v248 offset:14336\n\t" \
  "v_smfmac_f32_16x16x64_bf16 v[196:199], v[208:211], v[88:95], v251\n\t" \
  "s_waitcnt lgkmcnt(4)\n\t" \
  "v_smfmac_f32_16x16x64_bf16 v[192:195], v[212:215], v[96:103], v251\n\t" \
  "ds_read_b128 v[204:207], v248 offset:15360\n\t" \
  "v_smfmac_f32_16x16x64_bf16 v[196:199], v[212:215], v[104:111], v251\n\t" \
  "s_waitcnt lgkmcnt(3)\n\t" \
  "v_smfmac_f32_16x16x64_bf16 v[192:195], v[236:239], v[112:119], v251\n\t" \
  "v_smfmac_f32_16x16x64_bf16 v[196:199], v[236:239], v[120:127], v251\n\t" \
  "v_smfmac_f32_16x16x64_bf16 v[192:195], v[240:243], v[128:135], v251\n\t" \
  "v_smfmac_f32_16x16x64_bf16 v[196:199], v[240:243], v[136:143], v251\n\t" \
  "s_waitcnt lgkmcnt(1)\n\t" \
  "v_smfmac_f32_16x16x64_bf16 v[192:195], v[244:247], v[144:151], v251\n\t" \
  "v_smfmac_f32_16x16x64_bf16 v[196:199], v[244:247], v[152:159], v251\n\t" \
  "v_smfmac_f32_16x16x64_bf16 v[192:195], v[200:203], v[160:167], v251\n\t" \
  "v_smfmac_f32_16x16x64_bf16 v[196:199], v[200:203], v[168:175], v251\n\t" \
  "s_waitcnt lgkmcnt(0)\n\t" \
  "v_smfmac_f32_16x16x64_bf16 v[192:195], v[204:207], v[176:183], v251\n\t" \
  "v_smfmac_f32_16x16x64_bf16 v[196:199], v[204:207], v[184:191], v251\n\t" \
  "ds_read_b128 v[200:203], v248 offset:16384\n\t" \
  "ds_read_b128 v[204:207], v248 offset:20480\n\t" \
  "v_mov_b32 v250, v232\n\t" \
  "v_mov_b32 v249, v233\n\t" \
  "s_nop 2\n\t" \
  "v_add_f32 v192, v192, v194\n\t" \
  "v_add_f32 v193, v193, v195\n\t" \
  "v_add_f32 v196, v196, v198\n\t" \
  "v_add_f32 v197, v197, v199\n\t" \
  "v_cvt_pk_bf16_f32 v192, v192, v193\n\t" \
  "v_cvt_pk_bf16_f32 v196, v196, v197\n\t" \
  "ds_write2_b32 v252, v192, v196 offset1:64\n\t" \
  "v_mov_b32 v251, v234\n\t" \
  "ds_read_b64_tr_b16 v[232:233], v250 offset:0\n\t" \
  "ds_read_b64_tr_b16 v[234:235], v250 offset:64\n\t" \
  "v_mov_b32 v212, %[step]\n\t" \
  "s_mov_b64 exec, 1\n\t" \
  "ds_write_b32 v248, v212 offset:28672\n\t" \
  "s_mov_b64 exec, -1\n\t" \
  "buffer_load_dwordx4 v[192:195], v248, %[rt], %[svt] offen offset:0 sc1\n\t" \
  "buffer_load_dwordx4 v[196:199], v248, %[rt], %[svt] offen offset:1024 sc1\n\t" \
  "s_waitcnt lgkmcnt(1)\n\t" \
  "v_mfma_f32_16x16x32_bf16 v[200:203], v[200:203], v[232:235], 0\n\t" \
  "v_mfma_f32_16x16x32_bf16 v[204:207], v[204:207], v[232:235], 0\n\t" \
  "s_nop 7\n\t" \
  "v_cndmask_b32 v216, v200, v204, %[nh]\n\t" \
  "v_cndmask_b32 v217, v201, v205, %[nh]\n\t" \
  "v_cndmask_b32 v218, v202, v206, %[nh]\n\t" \
  "v_cndmask_b32 v219, v203, v207, %[nh]\n\t" \
  "ds_write_b128 v248, v[216:219] offset:16384\n\t" \
  "buffer_load_dword v252, v249, %[rs], %[sv] offen offset:0 sc1\n\t" \
  "buffer_load_dword v253, v249, %[rs], %[sv] offen offset:256 sc1\n\t" \
  "ds_read_b32 v212, v248 offset:28672\n\t" \
  "ds_read_b64_tr_b16 v[236:237], v250 offset:512\n\t" \
  "ds_read_b64_tr_b16 v[238:239], v250 offset:576\n\t" \
  "ds_read_b64_tr_b16 v[240:241], v250 offset:1024\n\t" \
  "ds_read_b64_tr_b16 v[242:243], v250 offset:1088\n\t" \
  "ds_read_b64_tr_b16 v[244:245], v250 offset:1536\n\t" \
  "ds_read_b64_tr_b16 v[246:247], v250 offset:1600\n\t" \
  "s_setprio 3\n\t" \
  "s_waitcnt lgkmcnt(0)\n\t" \
  "v_cmp_ne_u32 vcc, %[step], v212\n\t" \
  "s_barrier\n\t" \
  "s_and_b32 s93, vcc_lo, 0xf\n\t" \
  "s_cbranch_scc1 .Lre%=\n\t" \
  ".Lrb%=:\n\t" \
  "ds_read_b128 v[220:223], v248 offset:17408\n\t" \
  "ds_read_b128 v[224:227], v248 offset:18432\n\t" \
  "ds_read_b128 v[228:231], v248 offset:19456\n\t" \
  "ds_read_b64_tr_b16 v[200:201], v250 offset:2048\n\t" \
  "ds_read_b64_tr_b16 v[202:203], v250 offset:2112\n\t" \
  "ds_read_b64_tr_b16 v[204:205], v250 offset:2560\n\t" \
  "ds_read_b64_tr_b16 v[206:207], v250 offset:2624\n\t" \
  "ds_read_b64_tr_b16 v[208:209], v250 offset:3072\n\t" \
  "ds_read_b64_tr_b16 v[210:211], v250 offset:3136\n\t" \
  "ds_read_b64_tr_b16 v[212:213], v250 offset:3584\n\t" \
  "ds_read_b64_tr_b16 v[214:215], v250 offset:3648\n\t" \
  "s_waitcnt lgkmcnt(10)\n\t" \
  "v_pk_add_f32 v[216:217], v[216:217], v[220:221]\n\t" \
  "v_pk_add_f32 v[218:219], v[218:219], v[222:223]\n\t" \
  "s_waitcnt lgkmcnt(8)\n\t" \
  "v_pk_add_f32 v[224:225], v[224:225], v[228:229]\n\t" \
  "v_pk_add_f32 v[226:227], v[226:227], v[230:231]\n\t" \
  "v_pk_add_f32 v[216:217], v[216:217], v[224:225]\n\t" \
  "v_pk_add_f32 v[218:219], v[218:219], v[226:227]\n\t" \
  "s_waitcnt lgkmcnt(6)\n\t" \
  "v_mfma_f32_16x16x32_bf16 v[200:203], v[200:203], v[232:235], 0\n\t" \
  "v_max3_f32 v224, v216, v217, v218\n\t" \
  "v_max_f32 v224, v224, v219\n\t" \
  "s_waitcnt lgkmcnt(4)\n\t" \
  "v_mfma_f32_16x16x32_bf16 v[200:203], v[204:207], v[236:239], v[200:203]\n\t" \
  "v_max_f32_dpp v225, v224, v224 row_ror:8 row_mask:0xf bank_mask:0xf\n\t" \
  "v_max_f32_dpp v226, v224, v224 row_ror:8 row_mask:0xf bank_mask:0xf\n\t" \
  "s_waitcnt lgkmcnt(2)\n\t" \
  "v_mfma_f32_16x16x32_bf16 v[200:203], v[208:211], v[240:243], v[200:203]\n\t" \
  "v_permlane32_swap_b32 v225, v226\n\t" \
  "s_waitcnt lgkmcnt(0)\n\t" \
  "v_mfma_f32_16x16x32_bf16 v[200:203], v[212:215], v[244:247], v[200:203]\n\t" \
  "v_max_f32 v231, v225, v226\n\t" \
  "v_mov_b32 v220, 0x3f803f80\n\t" \
  "v_mov_b32 v221, 0x3f803f80\n\t" \
  "v_mov_b64 v[222:223], v[220:221]\n\t" \
  "v_mov_b32 v233, 0\n\t" \
  "v_mov_b32 v234, 0\n\t" \
  "s_nop 1\n\t" \
  "v_max3_f32 v204, v200, v201, v231\n\t" \
  "v_max3_f32 v205, v204, v202, v203\n\t" \
  "v_max3_f32 v206, v204, v202, v203\n\t" \
  "v_cndmask_b32 v200, v200, v202, %[up]\n\t" \
  "v_cndmask_b32 v201, v201, v203, %[up]\n\t" \
  "v_permlane16_swap_b32 v205, v206\n\t" \
  "v_max_f32 v204, v205, v206\n\t" \
  "v_mul_f32 v204, %[negc], v204\n\t" \
  "v_fma_f32 v200, v200, %[c], v204\n\t" \
  "v_fma_f32 v201, v201, %[c], v204\n\t" \
  "v_exp_f32 v200, v200\n\t" \
  "v_fma_f32 v216, v216, %[c], v204\n\t" \
  "v_exp_f32 v201, v201\n\t" \
  "v_fma_f32 v217, v217, %[c], v204\n\t" \
  "v_exp_f32 v216, v216\n\t" \
  "v_fma_f32 v218, v218, %[c], v204\n\t" \
  "v_cvt_pk_bf16_f32 v232, v200, v201\n\t" \
  "v_cvt_pk_bf16_f32 v235, v200, v201\n\t" \
  "v_exp_f32 v217, v217\n\t" \
  "v_fma_f32 v219, v219, %[c], v204\n\t" \
  "v_mfma_f32_16x16x16_bf16 v[212:215], v[220:221], v[232:233], 0\n\t" \
  "v_exp_f32 v218, v218\n\t" \
  "s_waitcnt vmcnt(0)\n\t" \
  "v_mfma_f32_16x16x16_bf16 v[224:227], v[252:253], v[232:233], 0\n\t" \
  "v_cvt_pk_bf16_f32 v210, v216, v217\n\t" \
  "v_cvt_pk_bf16_f32 v208, v216, v217\n\t" \
  "v_exp_f32 v219, v219\n\t" \
  "v_mfma_f32_16x16x16_bf16 v[228:231], v[252:253], v[234:235], 0\n\t" \
  "v_cvt_pk_bf16_f32 v211, v218, v219\n\t" \
  "v_cvt_pk_bf16_f32 v209, v218, v219\n\t" \
  "v_mov_b32_dpp v208, v210 row_ror:8 row_mask:0xf bank_mask:0xc\n\t" \
  "v_mov_b32_dpp v210, v210 row_ror:8 row_mask:0xf bank_mask:0x3\n\t" \
  "v_mov_b32_dpp v209, v211 row_ror:8 row_mask:0xf bank_mask:0xc\n\t" \
  "v_mov_b32_dpp v211, v211 row_ror:8 row_mask:0xf bank_mask:0x3\n\t" \
  "v_max_u32 v204, v252, v253\n\t" \
  "v_cmp_le_u32 vcc, %[sent], v204\n\t" \
  "v_mfma_f32_16x16x32_bf16 v[212:215], v[220:223], v[208:211], v[212:215]\n\t" \
  ".Lpv%=:\n\t" \
  "v_mfma_f32_16x16x32_bf16 v[224:227], v[192:195], v[208:211], v[224:227]\n\t" \
  "v_mfma_f32_16x16x32_bf16 v[228:231], v[196:199], v[208:211], v[228:231]\n\t" \
  "s_cbranch_vccnz .Lvs%=\n\t" \
  ".Lvok%=:\n\t" \
  "s_nop 4\n\t" \
  "v_rcp_f32 v204, v212\n\t" \
  "v_cndmask_b32 v224, v224, v226, %[nh]\n\t" \
  "v_cndmask_b32 v225, v225, v227, %[nh]\n\t" \
  "v_mul_f32 v224, v224, v204\n\t" \
  "v_mul_f32 v225, v225, v204\n\t" \
  "v_cvt_pk_bf16_f32 v224, v224, v225\n\t" \
  "buffer_store_dword v224, v251, %[rs], %[so] offen offset:0 sc1\n\t" \
  "v_cndmask_b32 v228, v228, v230, %[nh]\n\t" \
  "v_cndmask_b32 v229, v229, v231, %[nh]\n\t" \
  "v_mul_f32 v228, v228, v204\n\t" \
  "v_mul_f32 v229, v229, v204\n\t" \
  "v_cvt_pk_bf16_f32 v228, v228, v229\n\t" \
  "buffer_store_dword v228, v251, %[rs], %[so] offen offset:256 sc1\n\t" \
  "s_memtime s[90:91]\n\t" \
  "s_setprio 0\n\t" \
  "s_waitcnt lgkmcnt(0)\n\t" \
  "s_mov_b32 %[tout], s90\n\t" \
  "s_mov_b32 %[spins], s92\n\t" \
  "s_branch .Lend%=\n\t" \
  ".Lre%=:\n\t" \
  "ds_read_b64_tr_b16 v[236:237], v250 offset:512\n\t" \
  "ds_read_b64_tr_b16 v[238:239], v250 offset:576\n\t" \
  "ds_read_b64_tr_b16 v[240:241], v250 offset:1024\n\t" \
  "ds_read_b64_tr_b16 v[242:243], v250 offset:1088\n\t" \
  "ds_read_b64_tr_b16 v[244:245], v250 offset:1536\n\t" \
  "ds_read_b64_tr_b16 v[246:247], v250 offset:1600\n\t" \
  "s_waitcnt lgkmcnt(0)\n\t" \
  "s_branch .Lrb%=\n\t" \
  ".Lvs%=:\n\t" \
  "v_cmp_eq_u32 vcc, %[sent], v252\n\t" \
  "v_cmp_eq_u32 s[94:95], %[sent], v253\n\t" \
  "s_or_b64 s[94:95], s[94:95], vcc\n\t" \
  "s_cbranch_scc0 .Lvok%=\n\t" \
  "s_add_u32 s92, s92, 1\n\t" \
  "s_sleep 2\n\t" \
  "s_and_b32 s93, s92, 63\n\t" \
  "s_cbranch_scc1 .Lvr%=\n\t" \
  "buffer_load_dword v204, off, %[rs], %[sab] sc1\n\t" \
  "s_waitcnt vmcnt(0)\n\t" \
  "v_readfirstlane_b32 s93, v204\n\t" \
  "s_nop 3\n\t" \
  "s_cmp_eq_u32 s93, 1\n\t" \
  "s_cbranch_scc1 .Lvok%=\n\t" \
  "s_cmp_lt_u32 s92, 0x80000\n\t" \
  "s_cbranch_scc1 .Lvr%=\n\t" \
  "v_mov_b32 v204, 1\n\t" \
  "buffer_store_dword v204, off, %[rs], %[sab] sc1\n\t" \
  "s_branch .Lvok%=\n\t" \
  ".Lvr%=:\n\t" \
  "buffer_load_dword v252, v249, %[rs], %[sv] offen offset:0 sc1\n\t" \
  "buffer_load_dword v253, v249, %[rs], %[sv] offen offset:256 sc1\n\t" \
  "s_waitcnt vmcnt(0)\n\t" \
  "v_mfma_f32_16x16x16_bf16 v[224:227], v[252:253], v[232:233], 0\n\t" \
  "v_mfma_f32_16x16x16_bf16 v[228:231], v[252:253], v[234:235], 0\n\t" \
  "v_max_u32 v204, v252, v253\n\t" \
  "v_cmp_le_u32 vcc, %[sent], v204\n\t" \
  "s_nop 4\n\t" \
  "s_branch .Lpv%=\n\t" \
  "s_branch .Lend%=\n\t" \
  ".Lslow%=:\n\t" \
  "s_mov_b64 s[94:95], 0\n\t" \
  "v_cmp_eq_u32 vcc, %[sent], v200\n\t" \
  "s_or_b64 s[94:95], s[94:95], vcc\n\t" \
  "v_cmp_eq_u32 vcc, %[sent], v201\n\t" \
  "s_or_b64 s[94:95], s[94:95], vcc\n\t" \
  "v_cmp_eq_u32 vcc, %[sent], v202\n\t" \
  "s_or_b64 s[94:95], s[94:95], vcc\n\t" \
  "v_cmp_eq_u32 vcc, %[sent], v203\n\t" \
  "s_or_b64 s[94:95], s[94:95], vcc\n\t" \
  "v_cmp_eq_u32 vcc, %[sent], v204\n\t" \
  "s_or_b64 s[94:95], s[94:95], vcc\n\t" \
  "v_cmp_eq_u32 vcc, %[sent], v205\n\t" \
  "s_or_b64 s[94:95], s[94:95], vcc\n\t" \
  "v_cmp_eq_u32 vcc, %[sent], v206\n\t" \
  "s_or_b64 s[94:95], s[94:95], vcc\n\t" \
  "v_cmp_eq_u32 vcc, %[sent], v207\n\t" \
  "s_or_b64 s[94:95], s[94:95], vcc\n\t" \
  "s_cmp_eq_u64 s[94:95], 0\n\t" \
  "s_cbranch_scc1 .Lok%=\n\t" \
  "v_mov_b64 v[192:193], 0\n\t" \
  "v_mov_b64 v[194:195], 0\n\t" \
  "v_mov_b64 v[196:197], 0\n\t" \
  "v_mov_b64 v[198:199], 0\n\t" \
  "s_add_u32 s92, s92, 1\n\t" \
  "s_cmp_eq_u32 s92, 1\n\t" \
  "s_cbranch_scc1 .Lpoll%=\n\t" \
  "s_sleep 6\n\t" \
  "s_and_b32 s93, s92, 63\n\t" \
  "s_cmp_lg_u32 s93, 0\n\t" \
  "s_cbranch_scc1 .Lpoll%=\n\t" \
  "v_mov_b32 v245, 0\n\t" \
  "buffer_load_dword v244, v245, %[rs], %[sab] offen sc1\n\t" \
  "s_waitcnt vmcnt(0)\n\t" \
  "v_readfirstlane_b32 s93, v244\n\t" \
  "s_nop 3\n\t" \
  "s_cmp_eq_u32 s93, 1\n\t" \
  "s_cbranch_scc1 .Lok%=\n\t" \
  "s_cmp_lt_u32 s92, 0x80000\n\t" \
  "s_cbranch_scc1 .Lpoll%=\n\t" \
  "v_mov_b32 v244, 1\n\t" \
  "buffer_store_dword v244, v245, %[rs], %[sab] offen sc1\n\t" \
  "s_branch .Lok%=\n\t" \
  ".Lend%=:\n\t"
#define QK_ASM_1 \
  "s_waitcnt lgkmcnt(0)\n\t" \
  "v_subrev_u32 v244, 0x8000, v250\n\t" \
  "ds_read_b128 v[216:219], v248 offset:36864\n\t" \
  "ds_read_b128 v[220:223], v248 offset:37888\n\t" \
  "ds_read_b128 v[224:227], v248 offset:38912\n\t" \
  "ds_read_b128 v[228:231], v248 offset:39936\n\t" \
  "ds_read_b128 v[208:211], v244 offset:0\n\t" \
  "ds_read_b128 v[212:215], v244 offset:1024\n\t" \
  "ds_read_b128 v[236:239], v244 offset:2048\n\t" \
  "ds_read_b128 v[240:243], v244 offset:3072\n\t" \
  "v_mov_b64 v[192:193], 0\n\t" \
  "v_mov_b64 v[194:195], 0\n\t" \
  "v_mov_b64 v[196:197], 0\n\t" \
  "v_mov_b64 v[198:199], 0\n\t" \
  "s_movk_i32 s92, 0x2000\n\t" \
  ".Lwt%=:\n\t" \
  "ds_read_b64 v[200:201], v248 offset:30720\n\t" \
  "s_memrealtime s[90:91]\n\t" \
  "s_sub_u32 s92, s92, 1\n\t" \
  "s_waitcnt lgkmcnt(0)\n\t" \
  "v_readfirstlane_b32 s93, v200\n\t" \
  "v_readfirstlane_b32 %[tgt], v201\n\t" \
  "s_cbranch_scc1 .Lgo%=\n\t" \
  "s_cmp_eq_u32 s93, %[step]\n\t" \
  "s_cbranch_scc1 .Lwx%=\n\t" \
  "s_sleep 1\n\t" \
  "s_branch .Lwt%=\n\t" \
  ".Lwx%=:\n\t" \
  "s_lshl_b32 s93, s90, 2\n\t" \
  "s_sub_i32 s93, %[tgt], s93\n\t" \
  "s_cmp_le_i32 s93, 0\n\t" \
  "s_cbranch_scc1 .Lgo%=\n\t" \
  "s_min_i32 s93, s93, 8000\n\t" \
  "s_add_u32 s93, s93, 3\n\t" \
  "s_lshr_b32 s93, s93, 2\n\t" \
  "s_add_u32 s93, s93, s90\n\t" \
  "s_memrealtime s[90:91]\n\t" \
  ".Lpace%=:\n\t" \
  "s_memrealtime s[94:95]\n\t" \
  "s_waitcnt lgkmcnt(1)\n\t" \
  "s_sub_i32 s91, s90, s93\n\t" \
  "s_cmp_ge_i32 s91, 0\n\t" \
  "s_cbranch_scc1 .Lgo%=\n\t" \
  "s_memrealtime s[90:91]\n\t" \
  "s_waitcnt lgkmcnt(1)\n\t" \
  "s_sub_i32 s95, s94, s93\n\t" \
  "s_cmp_lt_i32 s95, 0\n\t" \
  "s_cbranch_scc1 .Lpace%=\n\t" \
  ".Lgo%=:\n\t" \
  "s_mov_b32 s92, 0\n\t" \
  ".Lpoll%=:\n\t" \
  "buffer_load_dwordx4 v[200:203], v248, %[rs], %[som] offen sc1\n\t" \
  "buffer_load_dwordx4 v[204:207], v248, %[rs], %[som2] offen sc1\n\t" \
  "s_waitcnt vmcnt(1) lgkmcnt(0)\n\t" \
  "ds_write_b128 v249, v[200:203]\n\t" \
  "v_smfmac_f32_16x16x64_bf16 v[192:195], v[200:203], v[208:215], v251\n\t" \
  "v_max3_u32 v244, v200, v201, v202\n\t" \
  "v_smfmac_f32_16x16x64_bf16 v[196:199], v[200:203], v[236:243], v251\n\t" \
  "s_waitcnt vmcnt(0)\n\t" \
  "ds_write_b128 v249, v[204:207] offset:8192\n\t" \
  "v_smfmac_f32_16x16x64_bf16 v[192:195], v[204:207], v[216:223], v251\n\t" \
  "v_max3_u32 v244, v244, v203, v204\n\t" \
  "v_max3_u32 v244, v244, v205, v206\n\t" \
  "v_smfmac_f32_16x16x64_bf16 v[196:199], v[204:207], v[224:231], v251\n\t" \
  "v_max_u32 v244, v244, v207\n\t" \
  "v_cmp_le_u32 vcc, %[sent], v244\n\t" \
  "s_cbranch_vccnz .Lslow%=\n\t" \
  ".Lok%=:\n\t" \
  "ds_read_b128 v[216:219], v250 offset:32768\n\t" \
  "ds_read_b128 v[220:223], v250 offset:33792\n\t" \
  "ds_read_b128 v[224:227], v250 offset:34816\n\t" \
  "ds_read_b128 v[228:231], v250 offset:35840\n\t" \
  "s_waitcnt lgkmcnt(4)\n\t" \
  "s_barrier\n\t" \
  "ds_read_b128 v[208:211], v248 offset:0\n\t" \
  "ds_read_b128 v[212:215], v248 offset:3072\n\t" \
  "ds_read_b128 v[236:239], v248 offset:4096\n\t" \
  "ds_read_b128 v[240:243], v248 offset:5120\n\t" \
  "s_waitcnt lgkmcnt(3)\n\t" \
  "ds_read_b128 v[244:247], v248 offset:12288\n\t" \
  "v_smfmac_f32_16x16x64_bf16 v[192:195], v[208:211], v[216:223], v251\n\t" \
  "ds_read_b128 v[216:219], v250 offset:0\n\t" \
  "v_smfmac_f32_16x16x64_bf16 v[196:199], v[208:211], v[224:231], v251\n\t" \
  "s_waitcnt lgkmcnt(4)\n\t" \
  "ds_read_b128 v[220:223], v250 offset:1024\n\t" \
  "v_smfmac_f32_16x16x64_bf16 v[192:195], v[212:215], v[0:7], v251\n\t" \
  "ds_read_b128 v[224:227], v250 offset:2048\n\t" \
  "v_smfmac_f32_16x16x64_bf16 v[196:199], v[212:215], v[8:15], v251\n\t" \
  "s_waitcnt lgkmcnt(5)\n\t" \
  "ds_read_b128 v[228:231], v250 offset:3072\n\t" \
  "v_smfmac_f32_16x16x64_bf16 v[192:195], v[236:239], v[16:23], v251\n\t" \
  "ds_read_b128 v[200:203], v248 offset:6144\n\t" \
  "v_smfmac_f32_16x16x64_bf16 v[196:199], v[236:239], v[24:31], v251\n\t" \
  "s_waitcnt lgkmcnt(6)\n\t" \
  "ds_read_b128 v[204:207], v248 offset:7168\n\t" \
  "v_smfmac_f32_16x16x64_bf16 v[192:195], v[240:243], v[32:39], v251\n\t" \
  "ds_read_b128 v[208:211], v248 offset:8192\n\t" \
  "v_smfmac_f32_16x16x64_bf16 v[196:199], v[240:243], v[40:47], v251\n\t" \
  "s_waitcnt lgkmcnt(5)\n\t" \
  "ds_read_b128 v[212:215], v248 offset:10240\n\t" \
  "v_smfmac_f32_16x16x64_bf16 v[192:195], v[244:247], v[216:223], v251\n\t" \
  "s_waitcnt lgkmcnt(4)\n\t" \
  "v_smfmac_f32_16x16x64_bf16 v[196:199], v[244:247], v[224:231], v251\n\t" \
  "s_waitcnt lgkmcnt(3)\n\t" \
  "ds_read_b128 v[236:239], v248 offset:11264\n\t" \
  "v_smfmac_f32_16x16x64_bf16 v[192:195], v[200:203], v[48:55], v251\n\t" \
  "ds_read_b128 v[240:243], v248 offset:13312\n\t" \
  "v_smfmac_f32_16x16x64_bf16 v[196:199], v[200:203], v[56:63], v251\n\t" \
  "s_waitcnt lgkmcnt(4)\n\t" \
  "v_smfmac_f32_16x16x64_bf16 v[192:195], v[204:207], v[64:71], v251\n\t" \
  "ds_read_b128 v[244:247], v248 offset:14336\n\t" \
  "v_smfmac_f32_16x16x64_bf16 v[196:199], v[204:207], v[72:79], v251\n\t" \
  "s_waitcnt lgkmcnt(4)\n\t" \
  "v_smfmac_f32_16x16x64_bf16 v[192:195], v[208:211], v[80:87], v251\n\t" \
  "ds_read_b128 v[200:203], v248 offset:15360\n\t" \
  "v_smfmac_f32_16x16x64_bf16 v[196:199], v[208:211], v[88:95], v251\n\t" \
  "s_waitcnt lgkmcnt(4)\n\t" \
  "v_smfmac_f32_16x16x64_bf16 v[192:195], v[212:215], v[96:103], v251\n\t" \
  "ds_read_b128 v[204:207], v248 offset:2048\n\t" \
  "v_smfmac_f32_16x16x64_bf16 v[196:199], v[212:215], v[104:111], v251\n\t" \
  "s_waitcnt lgkmcnt(3)\n\t" \
  "v_smfmac_f32_16x16x64_bf16 v[192:195], v[236:239], v[112:119], v251\n\t" \
  "v_smfmac_f32_16x16x64_bf16 v[196:199], v[236:239], v[120:127], v251\n\t" \
  "v_smfmac_f32_16x16x64_bf16 v[192:195], v[240:243], v[128:135], v251\n\t" \
  "v_smfmac_f32_16x16x64_bf16 v[196:199], v[240:243], v[136:143], v251\n\t" \
  "s_waitcnt lgkmcnt(1)\n\t" \
  "v_smfmac_f32_16x16x64_bf16 v[192:195], v[244:247], v[144:151], v251\n\t" \
  "v_smfmac_f32_16x16x64_bf16 v[196:199], v[244:247], v[152:159], v251\n\t" \
  "v_smfmac_f32_16x16x64_bf16 v[192:195], v[200:203], v[160:167], v251\n\t" \
  "v_smfmac_f32_16x16x64_bf16 v[196:199], v[200:203], v[168:175], v251\n\t" \
  "s_waitcnt lgkmcnt(0)\n\t" \
  "v_smfmac_f32_16x16x64_bf16 v[192:195], v[204:207], v[176:183], v251\n\t" \
  "v_smfmac_f32_16x16x64_bf16 v[196:199], v[204:207], v[184:191], v251\n\t" \
  "ds_read_b128 v[200:203], v248 offset:17408\n\t" \
  "ds_read_b128 v[204:207], v248 offset:21504\n\t" \
  "v_mov_b32 v250, v232\n\t" \
  "v_mov_b32 v249, v233\n\t" \
  "s_nop 2\n\t" \
  "v_add_f32 v192, v192, v194\n\t" \
  "v_add_f32 v193, v193, v195\n\t" \
  "v_add_f32 v196, v196, v198\n\t" \
  "v_add_f32 v197, v197, v199\n\t" \
  "v_cvt_pk_bf16_f32 v192, v192, v193\n\t" \
  "v_cvt_pk_bf16_f32 v196, v196, v197\n\t" \
  "ds_write2_b32 v252, v192, v196 offset1:64\n\t" \
  "v_mov_b32 v251, v234\n\t" \
  "ds_read_b64_tr_b16 v[236:237], v250 offset:512\n\t" \
  "ds_read_b64_tr_b16 v[238:239], v250 offset:576\n\t" \
  "v_mov_b32 v212, %[step]\n\t" \
  "s_mov_b64 exec, 1\n\t" \
  "ds_write_b32 v248, v212 offset:28688\n\t" \
  "s_mov_b64 exec, -1\n\t" \
  "buffer_load_dwordx4 v[192:195], v248, %[rt], %[svt] offen offset:0 sc1\n\t" \
  "buffer_load_dwordx4 v[196:199], v248, %[rt], %[svt] offen offset:1024 sc1\n\t" \
  "s_waitcnt lgkmcnt(1)\n\t" \
  "v_mfma_f32_16x16x32_bf16 v[200:203], v[200:203], v[236:239], 0\n\t" \
  "v_mfma_f32_16x16x32_bf16 v[204:207], v[204:207], v[236:239], 0\n\t" \
  "s_nop 7\n\t" \
  "v_cndmask_b32 v216, v200, v204, %[nh]\n\t" \
  "v_cndmask_b32 v217, v201, v205, %[nh]\n\t" \
  "v_cndmask_b32 v218, v202, v206, %[nh]\n\t" \
  "v_cndmask_b32 v219, v203, v207, %[nh]\n\t" \
  "ds_write_b128 v248, v[216:219] offset:17408\n\t" \
  "buffer_load_dword v252, v249, %[rs], %[sv] offen offset:0 sc1\n\t" \
  "buffer_load_dword v253, v249, %[rs], %[sv] offen offset:256 sc1\n\t" \
  "ds_read_b32 v212, v248 offset:28672\n\t" \
  "ds_read_b64_tr_b16 v[240:241], v250 offset:1024\n\t" \
  "ds_read_b64_tr_b16 v[242:243], v250 offset:1088\n\t" \
  "ds_read_b64_tr_b16 v[244:245], v250 offset:1536\n\t" \
  "ds_read_b64_tr_b16 v[246:247], v250 offset:1600\n\t" \
  "ds_read_b64_tr_b16 v[232:233], v250 offset:0\n\t" \
  "ds_read_b64_tr_b16 v[234:235], v250 offset:64\n\t" \
  "s_setprio 3\n\t" \
  "s_waitcnt lgkmcnt(0)\n\t" \
  "v_cmp_ne_u32 vcc, %[step], v212\n\t" \
  "s_barrier\n\t" \
  "s_and_b32 s93, vcc_lo, 0xf\n\t" \
  "s_cbranch_scc1 .Lre%=\n\t" \
  ".Lrb%=:\n\t" \
  "ds_read_b128 v[220:223], v248 offset:18432\n\t" \
  "ds_read_b128 v[224:227], v248 offset:19456\n\t" \
  "ds_read_b128 v[228:231], v248 offset:16384\n\t" \
  "ds_read_b64_tr_b16 v[204:205], v250 offset:2560\n\t" \
  "ds_read_b64_tr_b16 v[206:207], v250 offset:2624\n\t" \
  "ds_read_b64_tr_b16 v[208:209], v250 offset:3072\n\t" \
  "ds_read_b64_tr_b16 v[210:211], v250 offset:3136\n\t" \
  "ds_read_b64_tr_b16 v[212:213], v250 offset:3584\n\t" \
  "ds_read_b64_tr_b16 v[214:215], v250 offset:3648\n\t" \
  "ds_read_b64_tr_b16 v[200:201], v250 offset:2048\n\t" \
  "ds_read_b64_tr_b16 v[202:203], v250 offset:2112\n\t" \
  "s_waitcnt lgkmcnt(10)\n\t" \
  "v_pk_add_f32 v[216:217], v[216:217], v[220:221]\n\t" \
  "v_pk_add_f32 v[218:219], v[218:219], v[222:223]\n\t" \
  "s_waitcnt lgkmcnt(8)\n\t" \
  "v_pk_add_f32 v[224:225], v[224:225], v[228:229]\n\t" \
  "v_pk_add_f32 v[226:227], v[226:227], v[230:231]\n\t" \
  "v_pk_add_f32 v[216:217], v[216:217], v[224:225]\n\t" \
  "v_pk_add_f32 v[218:219], v[218:219], v[226:227]\n\t" \
  "s_waitcnt lgkmcnt(6)\n\t" \
  "v_mfma_f32_16x16x32_bf16 v[204:207], v[204:207], v[236:239], 0\n\t" \
  "v_max3_f32 v224, v216, v217, v218\n\t" \
  "v_max_f32 v224, v224, v219\n\t" \
  "s_waitcnt lgkmcnt(4)\n\t" \
  "v_mfma_f32_16x16x32_bf16 v[204:207], v[208:211], v[240:243], v[204:207]\n\t" \
  "v_max_f32_dpp v225, v224, v224 row_ror:8 row_mask:0xf bank_mask:0xf\n\t" \
  "v_max_f32_dpp v226, v224, v224 row_ror:8 row_mask:0xf bank_mask:0xf\n\t" \
  "s_waitcnt lgkmcnt(2)\n\t" \
  "v_mfma_f32_16x16x32_bf16 v[204:207], v[212:215], v[244:247], v[204:207]\n\t" \
  "v_permlane32_swap_b32 v225, v226\n\t" \
  "s_waitcnt lgkmcnt(0)\n\t" \
  "v_mfma_f32_16x16x32_bf16 v[204:207], v[200:203], v[232:235], v[204:207]\n\t" \
  "v_max_f32 v231, v225, v226\n\t" \
  "v_mov_b32 v220, 0x3f803f80\n\t" \
  "v_mov_b32 v221, 0x3f803f80\n\t" \
  "v_mov_b64 v[222:223], v[220:221]\n\t" \
  "v_mov_b32 v237, 0\n\t" \
  "v_mov_b32 v238, 0\n\t" \
  "s_nop 1\n\t" \
  "v_max3_f32 v208, v204, v205, v231\n\t" \
  "v_max3_f32 v209, v208, v206, v207\n\t" \
  "v_max3_f32 v210, v208, v206, v207\n\t" \
  "v_cndmask_b32 v204, v204, v206, %[up]\n\t" \
  "v_cndmask_b32 v205, v205, v207, %[up]\n\t" \
  "v_permlane16_swap_b32 v209, v210\n\t" \
  "v_max_f32 v208, v209, v210\n\t" \
  "v_mul_f32 v208, %[negc], v208\n\t" \
  "v_fma_f32 v204, v204, %[c], v208\n\t" \
  "v_fma_f32 v205, v205, %[c], v208\n\t" \
  "v_exp_f32 v204, v204\n\t" \
  "v_fma_f32 v216, v216, %[c], v208\n\t" \
  "v_exp_f32 v205, v205\n\t" \
  "v_fma_f32 v217, v217, %[c], v208\n\t" \
  "v_exp_f32 v216, v216\n\t" \
  "v_fma_f32 v218, v218, %[c], v208\n\t" \
  "v_cvt_pk_bf16_f32 v236, v204, v205\n\t" \
  "v_cvt_pk_bf16_f32 v239, v204, v205\n\t" \
  "v_exp_f32 v217, v217\n\t" \
  "v_fma_f32 v219, v219, %[c], v208\n\t" \
  "v_mfma_f32_16x16x16_bf16 v[200:203], v[220:221], v[236:237], 0\n\t" \
  "v_exp_f32 v218, v218\n\t" \
  "s_waitcnt vmcnt(0)\n\t" \
  "v_mfma_f32_16x16x16_bf16 v[224:227], v[252:253], v[236:237], 0\n\t" \
  "v_cvt_pk_bf16_f32 v214, v216, v217\n\t" \
  "v_cvt_pk_bf16_f32 v212, v216, v217\n\t" \
  "v_exp_f32 v219, v219\n\t" \
  "v_mfma_f32_16x16x16_bf16 v[228:231], v[252:253], v[238:239], 0\n\t" \
  "v_cvt_pk_bf16_f32 v215, v218, v219\n\t" \
  "v_cvt_pk_bf16_f32 v213, v218, v219\n\t" \
  "v_mov_b32_dpp v212, v214 row_ror:8 row_mask:0xf bank_mask:0xc\n\t" \
  "v_mov_b32_dpp v214, v214 row_ror:8 row_mask:0xf bank_mask:0x3\n\t" \
  "v_mov_b32_dpp v213, v215 row_ror:8 row_mask:0xf bank_mask:0xc\n\t" \
  "v_mov_b32_dpp v215, v215 row_ror:8 row_mask:0xf bank_mask:0x3\n\t" \
  "v_max_u32 v208, v252, v253\n\t" \
  "v_cmp_le_u32 vcc, %[sent], v208\n\t" \
  "v_mfma_f32_16x16x32_bf16 v[200:203], v[220:223], v[212:215], v[200:203]\n\t" \
  ".Lpv%=:\n\t" \
  "v_mfma_f32_16x16x32_bf16 v[224:227], v[192:195], v[212:215], v[224:227]\n\t" \
  "v_mfma_f32_16x16x32_bf16 v[228:231], v[196:199], v[212:215], v[228:231]\n\t" \
  "s_cbranch_vccnz .Lvs%=\n\t" \
  ".Lvok%=:\n\t" \
  "s_nop 4\n\t" \
  "v_rcp_f32 v208, v200\n\t" \
  "v_cndmask_b32 v224, v224, v226, %[nh]\n\t" \
  "v_cndmask_b32 v225, v225, v227, %[nh]\n\t" \
  "v_mul_f32 v224, v224, v208\n\t" \
  "v_mul_f32 v225, v225, v208\n\t" \
  "v_cvt_pk_bf16_f32 v224, v224, v225\n\t" \
  "buffer_store_dword v224, v251, %[rs], %[so] offen offset:0 sc1\n\t" \
  "v_cndmask_b32 v228, v228, v230, %[nh]\n\t" \
  "v_cndmask_b32 v229, v229, v231, %[nh]\n\t" \
  "v_mul_f32 v228, v228, v208\n\t" \
  "v_mul_f32 v229, v229, v208\n\t" \
  "v_cvt_pk_bf16_f32 v228, v228, v229\n\t" \
  "buffer_store_dword v228, v251, %[rs], %[so] offen offset:256 sc1\n\t" \
  "s_memtime s[90:91]\n\t" \
  "s_setprio 0\n\t" \
  "s_waitcnt lgkmcnt(0)\n\t" \
  "s_mov_b32 %[tout], s90\n\t" \
  "s_mov_b32 %[spins], s92\n\t" \
  "s_branch .Lend%=\n\t" \
  ".Lre%=:\n\t" \
  "ds_read_b64_tr_b16 v[240:241], v250 offset:1024\n\t" \
  "ds_read_b64_tr_b16 v[242:243], v250 offset:1088\n\t" \
  "ds_read_b64_tr_b16 v[244:245], v250 offset:1536\n\t" \
  "ds_read_b64_tr_b16 v[246:247], v250 offset:1600\n\t" \
  "ds_read_b64_tr_b16 v[232:233], v250 offset:0\n\t" \
  "ds_read_b64_tr_b16 v[234:235], v250 offset:64\n\t" \
  "s_waitcnt lgkmcnt(0)\n\t" \
  "s_branch .Lrb%=\n\t" \
  ".Lvs%=:\n\t" \
  "v_cmp_eq_u32 vcc, %[sent], v252\n\t" \
  "v_cmp_eq_u32 s[94:95], %[sent], v253\n\t" \
  "s_or_b64 s[94:95], s[94:95], vcc\n\t" \
  "s_cbranch_scc0 .Lvok%=\n\t" \
  "s_add_u32 s92, s92, 1\n\t" \
  "s_sleep 2\n\t" \
  "s_and_b32 s93, s92, 63\n\t" \
  "s_cbranch_scc1 .Lvr%=\n\t" \
  "buffer_load_dword v208, off, %[rs], %[sab] sc1\n\t" \
  "s_waitcnt vmcnt(0)\n\t" \
  "v_readfirstlane_b32 s93, v208\n\t" \
  "s_nop 3\n\t" \
  "s_cmp_eq_u32 s93, 1\n\t" \
  "s_cbranch_scc1 .Lvok%=\n\t" \
  "s_cmp_lt_u32 s92, 0x80000\n\t" \
  "s_cbranch_scc1 .Lvr%=\n\t" \
  "v_mov_b32 v208, 1\n\t" \
  "buffer_store_dword v208, off, %[rs], %[sab] sc1\n\t" \
  "s_branch .Lvok%=\n\t" \
  ".Lvr%=:\n\t" \
  "buffer_load_dword v252, v249, %[rs], %[sv] offen offset:0 sc1\n\t" \
  "buffer_load_dword v253, v249, %[rs], %[sv] offen offset:256 sc1\n\t" \
  "s_waitcnt vmcnt(0)\n\t" \
  "v_mfma_f32_16x16x16_bf16 v[224:227], v[252:253], v[236:237], 0\n\t" \
  "v_mfma_f32_16x16x16_bf16 v[228:231], v[252:253], v[238:239], 0\n\t" \
  "v_max_u32 v208, v252, v253\n\t" \
  "v_cmp_le_u32 vcc, %[sent], v208\n\t" \
  "s_nop 4\n\t" \
  "s_branch .Lpv%=\n\t" \
  "s_branch .Lend%=\n\t" \
  ".Lslow%=:\n\t" \
  "s_mov_b64 s[94:95], 0\n\t" \
  "v_cmp_eq_u32 vcc, %[sent], v200\n\t" \
  "s_or_b64 s[94:95], s[94:95], vcc\n\t" \
  "v_cmp_eq_u32 vcc, %[sent], v201\n\t" \
  "s_or_b64 s[94:95], s[94:95], vcc\n\t" \
  "v_cmp_eq_u32 vcc, %[sent], v202\n\t" \
  "s_or_b64 s[94:95], s[94:95], vcc\n\t" \
  "v_cmp_eq_u32 vcc, %[sent], v203\n\t" \
  "s_or_b64 s[94:95], s[94:95], vcc\n\t" \
  "v_cmp_eq_u32 vcc, %[sent], v204\n\t" \
  "s_or_b64 s[94:95], s[94:95], vcc\n\t" \
  "v_cmp_eq_u32 vcc, %[sent], v205\n\t" \
  "s_or_b64 s[94:95], s[94:95], vcc\n\t" \
  "v_cmp_eq_u32 vcc, %[sent], v206\n\t" \
  "s_or_b64 s[94:95], s[94:95], vcc\n\t" \
  "v_cmp_eq_u32 vcc, %[sent], v207\n\t" \
  "s_or_b64 s[94:95], s[94:95], vcc\n\t" \
  "s_cmp_eq_u64 s[94:95], 0\n\t" \
  "s_cbranch_scc1 .Lok%=\n\t" \
  "v_mov_b64 v[192:193], 0\n\t" \
  "v_mov_b64 v[194:195], 0\n\t" \
  "v_mov_b64 v[196:197], 0\n\t" \
  "v_mov_b64 v[198:199], 0\n\t" \
  "s_add_u32 s92, s92, 1\n\t" \
  "s_cmp_eq_u32 s92, 1\n\t" \
  "s_cbranch_scc1 .Lpoll%=\n\t" \
  "s_sleep 6\n\t" \
  "s_and_b32 s93, s92, 63\n\t" \
  "s_cmp_lg_u32 s93, 0\n\t" \
  "s_cbranch_scc1 .Lpoll%=\n\t" \
  "v_mov_b32 v245, 0\n\t" \
  "buffer_load_dword v244, v245, %[rs], %[sab] offen sc1\n\t" \
  "s_waitcnt vmcnt(0)\n\t" \
  "v_readfirstlane_b32 s93, v244\n\t" \
  "s_nop 3\n\t" \
  "s_cmp_eq_u32 s93, 1\n\t" \
  "s_cbranch_scc1 .Lok%=\n\t" \
  "s_cmp_lt_u32 s92, 0x80000\n\t" \
  "s_cbranch_scc1 .Lpoll%=\n\t" \
  "v_mov_b32 v244, 1\n\t" \
  "buffer_store_dword v244, v245, %[rs], %[sab] offen sc1\n\t" \
  "s_branch .Lok%=\n\t" \
  ".Lend%=:\n\t"
#define QK_ASM_2 \
  "s_waitcnt lgkmcnt(0)\n\t" \
  "v_subrev_u32 v244, 0x8000, v250\n\t" \
  "ds_read_b128 v[216:219], v248 offset:40960\n\t" \
  "ds_read_b128 v[220:223], v248 offset:41984\n\t" \
  "ds_read_b128 v[224:227], v248 offset:43008\n\t" \
  "ds_read_b128 v[228:231], v248 offset:44032\n\t" \
  "ds_read_b128 v[208:211], v244 offset:0\n\t" \
  "ds_read_b128 v[212:215], v244 offset:1024\n\t" \
  "ds_read_b128 v[236:239], v244 offset:2048\n\t" \
  "ds_read_b128 v[240:243], v244 offset:3072\n\t" \
  "v_mov_b64 v[192:193], 0\n\t" \
  "v_mov_b64 v[194:195], 0\n\t" \
  "v_mov_b64 v[196:197], 0\n\t" \
  "v_mov_b64 v[198:199], 0\n\t" \
  "s_movk_i32 s92, 0x2000\n\t" \
  ".Lwt%=:\n\t" \
  "ds_read_b64 v[200:201], v248 offset:30720\n\t" \
  "s_memrealtime s[90:91]\n\t" \
  "s_sub_u32 s92, s92, 1\n\t" \
  "s_waitcnt lgkmcnt(0)\n\t" \
  "v_readfirstlane_b32 s93, v200\n\t" \
  "v_readfirstlane_b32 %[tgt], v201\n\t" \
  "s_cbranch_scc1 .Lgo%=\n\t" \
  "s_cmp_eq_u32 s93, %[step]\n\t" \
  "s_cbranch_scc1 .Lwx%=\n\t" \
  "s_sleep 1\n\t" \
  "s_branch .Lwt%=\n\t" \
  ".Lwx%=:\n\t" \
  "s_lshl_b32 s93, s90, 2\n\t" \
  "s_sub_i32 s93, %[tgt], s93\n\t" \
  "s_cmp_le_i32 s93, 0\n\t" \
  "s_cbranch_scc1 .Lgo%=\n\t" \
  "s_min_i32 s93, s93, 8000\n\t" \
  "s_add_u32 s93, s93, 3\n\t" \
  "s_lshr_b32 s93, s93, 2\n\t" \
  "s_add_u32 s93, s93, s90\n\t" \
  "s_memrealtime s[90:91]\n\t" \
  ".Lpace%=:\n\t" \
  "s_memrealtime s[94:95]\n\t" \
  "s_waitcnt lgkmcnt(1)\n\t" \
  "s_sub_i32 s91, s90, s93\n\t" \
  "s_cmp_ge_i32 s91, 0\n\t" \
  "s_cbranch_scc1 .Lgo%=\n\t" \
  "s_memrealtime s[90:91]\n\t" \
  "s_waitcnt lgkmcnt(1)\n\t" \
  "s_sub_i32 s95, s94, s93\n\t" \
  "s_cmp_lt_i32 s95, 0\n\t" \
  "s_cbranch_scc1 .Lpace%=\n\t" \
  ".Lgo%=:\n\t" \
  "s_mov_b32 s92, 0\n\t" \
  ".Lpoll%=:\n\t" \
  "buffer_load_dwordx4 v[200:203], v248, %[rs], %[som] offen sc1\n\t" \
  "buffer_load_dwordx4 v[204:207], v248, %[rs], %[som2] offen sc1\n\t" \
  "s_waitcnt vmcnt(1) lgkmcnt(0)\n\t" \
  "ds_write_b128 v249, v[200:203]\n\t" \
  "v_smfmac_f32_16x16x64_bf16 v[192:195], v[200:203], v[208:215], v251\n\t" \
  "v_max3_u32 v244, v200, v201, v202\n\t" \
  "v_smfmac_f32_16x16x64_bf16 v[196:199], v[200:203], v[236:243], v251\n\t" \
  "s_waitcnt vmcnt(0)\n\t" \
  "ds_write_b128 v249, v[204:207] offset:8192\n\t" \
  "v_smfmac_f32_16x16x64_bf16 v[192:195], v[204:207], v[216:223], v251\n\t" \
  "v_max3_u32 v244, v244, v203, v204\n\t" \
  "v_max3_u32 v244, v244, v205, v206\n\t" \
  "v_smfmac_f32_16x16x64_bf16 v[196:199], v[204:207], v[224:231], v251\n\t" \
  "v_max_u32 v244, v244, v207\n\t" \
  "v_cmp_le_u32 vcc, %[sent], v244\n\t" \
  "s_cbranch_vccnz .Lslow%=\n\t" \
  ".Lok%=:\n\t" \
  "ds_read_b128 v[216:219], v250 offset:32768\n\t" \
  "ds_read_b128 v[220:223], v250 offset:33792\n\t" \
  "ds_read_b128 v[224:227], v250 offset:34816\n\t" \
  "ds_read_b128 v[228:231], v250 offset:35840\n\t" \
  "s_waitcnt lgkmcnt(4)\n\t" \
  "s_barrier\n\t" \
  "ds_read_b128 v[208:211], v248 offset:3072\n\t" \
  "ds_read_b128 v[212:215], v248 offset:4096\n\t" \
  "ds_read_b128 v[236:239], v248 offset:5120\n\t" \
  "ds_read_b128 v[240:243], v248 offset:6144\n\t" \
  "s_waitcnt lgkmcnt(3)\n\t" \
  "ds_read_b128 v[244:247], v248 offset:13312\n\t" \
  "v_smfmac_f32_16x16x64_bf16 v[192:195], v[208:211], v[216:223], v251\n\t" \
  "ds_read_b128 v[216:219], v250 offset:0\n\t" \
  "v_smfmac_f32_16x16x64_bf16 v[196:199], v[208:211], v[224:231], v251\n\t" \
  "s_waitcnt lgkmcnt(4)\n\t" \
  "ds_read_b128 v[220:223], v250 offset:1024\n\t" \
  "v_smfmac_f32_16x16x64_bf16 v[192:195], v[212:215], v[0:7], v251\n\t" \
  "ds_read_b128 v[224:227], v250 offset:2048\n\t" \
  "v_smfmac_f32_16x16x64_bf16 v[196:199], v[212:215], v[8:15], v251\n\t" \
  "s_waitcnt lgkmcnt(5)\n\t" \
  "ds_read_b128 v[228:231], v250 offset:3072\n\t" \
  "v_smfmac_f32_16x16x64_bf16 v[192:195], v[236:239], v[16:23], v251\n\t" \
  "ds_read_b128 v[200:203], v248 offset:7168\n\t" \
  "v_smfmac_f32_16x16x64_bf16 v[196:199], v[236:239], v[24:31], v251\n\t" \
  "s_waitcnt lgkmcnt(6)\n\t" \
  "ds_read_b128 v[204:207], v248 offset:8192\n\t" \
  "v_smfmac_f32_16x16x64_bf16 v[192:195], v[240:243], v[32:39], v251\n\t" \
  "ds_read_b128 v[208:211], v248 offset:9216\n\t" \
  "v_smfmac_f32_16x16x64_bf16 v[196:199], v[240:243], v[40:47], v251\n\t" \
  "s_waitcnt lgkmcnt(5)\n\t" \
  "ds_read_b128 v[212:215], v248 offset:11264\n\t" \
  "v_smfmac_f32_16x16x64_bf16 v[192:195], v[244:247], v[216:223], v251\n\t" \
  "s_waitcnt lgkmcnt(4)\n\t" \
  "v_smfmac_f32_16x16x64_bf16 v[196:199], v[244:247], v[224:231], v251\n\t" \
  "s_waitcnt lgkmcnt(3)\n\t" \
  "ds_read_b128 v[236:239], v248 offset:12288\n\t" \
  "v_smfmac_f32_16x16x64_bf16 v[192:195], v[200:203], v[48:55], v251\n\t" \
  "ds_read_b128 v[240:243], v248 offset:14336\n\t" \
  "v_smfmac_f32_16x16x64_bf16 v[196:199], v[200:203], v[56:63], v251\n\t" \
  "s_waitcnt lgkmcnt(4)\n\t" \
  "v_smfmac_f32_16x16x64_bf16 v[192:195], v[204:207], v[64:71], v251\n\t" \
  "ds_read_b128 v[244:247], v248 offset:15360\n\t" \
  "v_smfmac_f32_16x16x64_bf16 v[196:199], v[204:207], v[72:79], v251\n\t" \
  "s_waitcnt lgkmcnt(4)\n\t" \
  "v_smfmac_f32_16x16x64_bf16 v[192:195], v[208:211], v[80:87], v251\n\t" \
  "ds_read_b128 v[200:203], v248 offset:0\n\t" \
  "v_smfmac_f32_16x16x64_bf16 v[196:199], v[208:211], v[88:95], v251\n\t" \
  "s_waitcnt lgkmcnt(4)\n\t" \
  "v_smfmac_f32_16x16x64_bf16 v[192:195], v[212:215], v[96:103], v251\n\t" \
  "ds_read_b128 v[204:207], v248 offset:1024\n\t" \
  "v_smfmac_f32_16x16x64_bf16 v[196:199], v[212:215], v[104:111], v251\n\t" \
  "s_waitcnt lgkmcnt(3)\n\t" \
  "v_smfmac_f32_16x16x64_bf16 v[192:195], v[236:239], v[112:119], v251\n\t" \
  "v_smfmac_f32_16x16x64_bf16 v[196:199], v[236:239], v[120:127], v251\n\t" \
  "v_smfmac_f32_16x16x64_bf16 v[192:195], v[240:243], v[128:135], v251\n\t" \
  "v_smfmac_f32_16x16x64_bf16 v[196:199], v[240:243], v[136:143], v251\n\t" \
  "s_waitcnt lgkmcnt(1)\n\t" \
  "v_smfmac_f32_16x16x64_bf16 v[192:195], v[244:247], v[144:151], v251\n\t" \
  "v_smfmac_f32_16x16x64_bf16 v[196:199], v[244:247], v[152:159], v251\n\t" \
  "v_smfmac_f32_16x16x64_bf16 v[192:195], v[200:203], v[160:167], v251\n\t" \
  "v_smfmac_f32_16x16x64_bf16 v[196:199], v[200:203], v[168:175], v251\n\t" \
  "s_waitcnt lgkmcnt(0)\n\t" \
  "v_smfmac_f32_16x16x64_bf16 v[192:195], v[204:207], v[176:183], v251\n\t" \
  "v_smfmac_f32_16x16x64_bf16 v[196:199], v[204:207], v[184:191], v251\n\t" \
  "ds_read_b128 v[200:203], v248 offset:18432\n\t" \
  "ds_read_b128 v[204:207], v248 offset:22528\n\t" \
  "v_mov_b32 v250, v232\n\t" \
  "v_mov_b32 v249, v233\n\t" \
  "s_nop 2\n\t" \
  "v_add_f32 v192, v192, v194\n\t" \
  "v_add_f32 v193, v193, v195\n\t" \
  "v_add_f32 v196, v196, v198\n\t" \
  "v_add_f32 v197, v197, v199\n\t" \
  "v_cvt_pk_bf16_f32 v192, v192, v193\n\t" \
  "v_cvt_pk_bf16_f32 v196, v196, v197\n\t" \
  "ds_write2_b32 v252, v192, v196 offset1:64\n\t" \
  "v_mov_b32 v251, v234\n\t" \
  "ds_read_b64_tr_b16 v[240:241], v250 offset:1024\n\t" \
  "ds_read_b64_tr_b16 v[242:243], v250 offset:1088\n\t" \
  "v_mov_b32 v212, %[step]\n\t" \
  "s_mov_b64 exec, 1\n\t" \
  "ds_write_b32 v248, v212 offset:28704\n\t" \
  "s_mov_b64 exec, -1\n\t" \
  "buffer_load_dwordx4 v[192:195], v248, %[rt], %[svt] offen offset:0 sc1\n\t" \
  "buffer_load_dwordx4 v[196:199], v248, %[rt], %[svt] offen offset:1024 sc1\n\t" \
  "s_waitcnt lgkmcnt(1)\n\t" \
  "v_mfma_f32_16x16x32_bf16 v[200:203], v[200:203], v[240:243], 0\n\t" \
  "v_mfma_f32_16x16x32_bf16 v[204:207], v[204:207], v[240:243], 0\n\t" \
  "s_nop 7\n\t" \
  "v_cndmask_b32 v216, v200, v204, %[nh]\n\t" \
  "v_cndmask_b32 v217, v201, v205, %[nh]\n\t" \
  "v_cndmask_b32 v218, v202, v206, %[nh]\n\t" \
  "v_cndmask_b32 v219, v203, v207, %[nh]\n\t" \
  "ds_write_b128 v248, v[216:219] offset:18432\n\t" \
  "buffer_load_dword v252, v249, %[rs], %[sv] offen offset:0 sc1\n\t" \
  "buffer_load_dword v253, v249, %[rs], %[sv] offen offset:256 sc1\n\t" \
  "ds_read_b32 v212, v248 offset:28672\n\t" \
  "ds_read_b64_tr_b16 v[244:245], v250 offset:1536\n\t" \
  "ds_read_b64_tr_b16 v[246:247], v250 offset:1600\n\t" \
  "ds_read_b64_tr_b16 v[232:233], v250 offset:0\n\t" \
  "ds_read_b64_tr_b16 v[234:235], v250 offset:64\n\t" \
  "ds_read_b64_tr_b16 v[236:237], v250 offset:512\n\t" \
  "ds_read_b64_tr_b16 v[238:239], v250 offset:576\n\t" \
  "s_setprio 3\n\t" \
  "s_waitcnt lgkmcnt(0)\n\t" \
  "v_cmp_ne_u32 vcc, %[step], v212\n\t" \
  "s_barrier\n\t" \
  "s_and_b32 s93, vcc_lo, 0xf\n\t" \
  "s_cbranch_scc1 .Lre%=\n\t" \
  ".Lrb%=:\n\t" \
  "ds_read_b128 v[220:223], v248 offset:19456\n\t" \
  "ds_read_b128 v[224:227], v248 offset:16384\n\t" \
  "ds_read_b128 v[228:231], v248 offset:17408\n\t" \
  "ds_read_b64_tr_b16 v[208:209], v250 offset:3072\n\t" \
  "ds_read_b64_tr_b16 v[210:211], v250 offset:3136\n\t" \
  "ds_read_b64_tr_b16 v[212:213], v250 offset:3584\n\t" \
  "ds_read_b64_tr_b16 v[214:215], v250 offset:3648\n\t" \
  "ds_read_b64_tr_b16 v[200:201], v250 offset:2048\n\t" \
  "ds_read_b64_tr_b16 v[202:203], v250 offset:2112\n\t" \
  "ds_read_b64_tr_b16 v[204:205], v250 offset:2560\n\t" \
  "ds_read_b64_tr_b16 v[206:207], v250 offset:2624\n\t" \
  "s_waitcnt lgkmcnt(10)\n\t" \
  "v_pk_add_f32 v[216:217], v[216:217], v[220:221]\n\t" \
  "v_pk_add_f32 v[218:219], v[218:219], v[222:223]\n\t" \
  "s_waitcnt lgkmcnt(8)\n\t" \
  "v_pk_add_f32 v[224:225], v[224:225], v[228:229]\n\t" \
  "v_pk_add_f32 v[226:227], v[226:227], v[230:231]\n\t" \
  "v_pk_add_f32 v[216:217], v[216:217], v[224:225]\n\t" \
  "v_pk_add_f32 v[218:219], v[218:219], v[226:227]\n\t" \
  "s_waitcnt lgkmcnt(6)\n\t" \
  "v_mfma_f32_16x16x32_bf16 v[208:211], v[208:211], v[240:243], 0\n\t" \
  "v_max3_f32 v224, v216, v217, v218\n\t" \
  "v_max_f32 v224, v224, v219\n\t" \
  "s_waitcnt lgkmcnt(4)\n\t" \
  "v_mfma_f32_16x16x32_bf16 v[208:211], v[212:215], v[244:247], v[208:211]\n\t" \
  "v_max_f32_dpp v225, v224, v224 row_ror:8 row_mask:0xf bank_mask:0xf\n\t" \
  "v_max_f32_dpp v226, v224, v224 row_ror:8 row_mask:0xf bank_mask:0xf\n\t" \
  "s_waitcnt lgkmcnt(2)\n\t" \
  "v_mfma_f32_16x16x32_bf16 v[208:211], v[200:203], v[232:235], v[208:211]\n\t" \
  "v_permlane32_swap_b32 v225, v226\n\t" \
  "s_waitcnt lgkmcnt(0)\n\t" \
  "v_mfma_f32_16x16x32_bf16 v[208:211], v[204:207], v[236:239], v[208:211]\n\t" \
  "v_max_f32 v231, v225, v226\n\t" \
  "v_mov_b32 v220, 0x3f803f80\n\t" \
  "v_mov_b32 v221, 0x3f803f80\n\t" \
  "v_mov_b64 v[222:223], v[220:221]\n\t" \
  "v_mov_b32 v241, 0\n\t" \
  "v_mov_b32 v242, 0\n\t" \
  "s_nop 1\n\t" \
  "v_max3_f32 v212, v208, v209, v231\n\t" \
  "v_max3_f32 v213, v212, v210, v211\n\t" \
  "v_max3_f32 v214, v212, v210, v211\n\t" \
  "v_cndmask_b32 v208, v208, v210, %[up]\n\t" \
  "v_cndmask_b32 v209, v209, v211, %[up]\n\t" \
  "v_permlane16_swap_b32 v213, v214\n\t" \
  "v_max_f32 v212, v213, v214\n\t" \
  "v_mul_f32 v212, %[negc], v212\n\t" \
  "v_fma_f32 v208, v208, %[c], v212\n\t" \
  "v_fma_f32 v209, v209, %[c], v212\n\t" \
  "v_exp_f32 v208, v208\n\t" \
  "v_fma_f32 v216, v216, %[c], v212\n\t" \
  "v_exp_f32 v209, v209\n\t" \
  "v_fma_f32 v217, v217, %[c], v212\n\t" \
  "v_exp_f32 v216, v216\n\t" \
  "v_fma_f32 v218, v218, %[c], v212\n\t" \
  "v_cvt_pk_bf16_f32 v240, v208, v209\n\t" \
  "v_cvt_pk_bf16_f32 v243, v208, v209\n\t" \
  "v_exp_f32 v217, v217\n\t" \
  "v_fma_f32 v219, v219, %[c], v212\n\t" \
  "v_mfma_f32_16x16x16_bf16 v[204:207], v[220:221], v[240:241], 0\n\t" \
  "v_exp_f32 v218, v218\n\t" \
  "s_waitcnt vmcnt(0)\n\t" \
  "v_mfma_f32_16x16x16_bf16 v[224:227], v[252:253], v[240:241], 0\n\t" \
  "v_cvt_pk_bf16_f32 v202, v216, v217\n\t" \
  "v_cvt_pk_bf16_f32 v200, v216, v217\n\t" \
  "v_exp_f32 v219, v219\n\t" \
  "v_mfma_f32_16x16x16_bf16 v[228:231], v[252:253], v[242:243], 0\n\t" \
  "v_cvt_pk_bf16_f32 v203, v218, v219\n\t" \
  "v_cvt_pk_bf16_f32 v201, v218, v219\n\t" \
  "v_mov_b32_dpp v200, v202 row_ror:8 row_mask:0xf bank_mask:0xc\n\t" \
  "v_mov_b32_dpp v202, v202 row_ror:8 row_mask:0xf bank_mask:0x3\n\t" \
  "v_mov_b32_dpp v201, v203 row_ror:8 row_mask:0xf bank_mask:0xc\n\t" \
  "v_mov_b32_dpp v203, v203 row_ror:8 row_mask:0xf bank_mask:0x3\n\t" \
  "v_max_u32 v212, v252, v253\n\t" \
  "v_cmp_le_u32 vcc, %[sent], v212\n\t" \
  "v_mfma_f32_16x16x32_bf16 v[204:207], v[220:223], v[200:203], v[204:207]\n\t" \
  ".Lpv%=:\n\t" \
  "v_mfma_f32_16x16x32_bf16 v[224:227], v[192:195], v[200:203], v[224:227]\n\t" \
  "v_mfma_f32_16x16x32_bf16 v[228:231], v[196:199], v[200:203], v[228:231]\n\t" \
  "s_cbranch_vccnz .Lvs%=\n\t" \
  ".Lvok%=:\n\t" \
  "s_nop 4\n\t" \
  "v_rcp_f32 v212, v204\n\t" \
  "v_cndmask_b32 v224, v224, v226, %[nh]\n\t" \
  "v_cndmask_b32 v225, v225, v227, %[nh]\n\t" \
  "v_mul_f32 v224, v224, v212\n\t" \
  "v_mul_f32 v225, v225, v212\n\t" \
  "v_cvt_pk_bf16_f32 v224, v224, v225\n\t" \
  "buffer_store_dword v224, v251, %[rs], %[so] offen offset:0 sc1\n\t" \
  "v_cndmask_b32 v228, v228, v230, %[nh]\n\t" \
  "v_cndmask_b32 v229, v229, v231, %[nh]\n\t" \
  "v_mul_f32 v228, v228, v212\n\t" \
  "v_mul_f32 v229, v229, v212\n\t" \
  "v_cvt_pk_bf16_f32 v228, v228, v229\n\t" \
  "buffer_store_dword v228, v251, %[rs], %[so] offen offset:256 sc1\n\t" \
  "s_memtime s[90:91]\n\t" \
  "s_setprio 0\n\t" \
  "s_waitcnt lgkmcnt(0)\n\t" \
  "s_mov_b32 %[tout], s90\n\t" \
  "s_mov_b32 %[spins], s92\n\t" \
  "s_branch .Lend%=\n\t" \
  ".Lre%=:\n\t" \
  "ds_read_b64_tr_b16 v[244:245], v250 offset:1536\n\t" \
  "ds_read_b64_tr_b16 v[246:247], v250 offset:1600\n\t" \
  "ds_read_b64_tr_b16 v[232:233], v250 offset:0\n\t" \
  "ds_read_b64_tr_b16 v[234:235], v250 offset:64\n\t" \
  "ds_read_b64_tr_b16 v[236:237], v250 offset:512\n\t" \
  "ds_read_b64_tr_b16 v[238:239], v250 offset:576\n\t" \
  "s_waitcnt lgkmcnt(0)\n\t" \
  "s_branch .Lrb%=\n\t" \
  ".Lvs%=:\n\t" \
  "v_cmp_eq_u32 vcc, %[sent], v252\n\t" \
  "v_cmp_eq_u32 s[94:95], %[sent], v253\n\t" \
  "s_or_b64 s[94:95], s[94:95], vcc\n\t" \
  "s_cbranch_scc0 .Lvok%=\n\t" \
  "s_add_u32 s92, s92, 1\n\t" \
  "s_sleep 2\n\t" \
  "s_and_b32 s93, s92, 63\n\t" \
  "s_cbranch_scc1 .Lvr%=\n\t" \
  "buffer_load_dword v212, off, %[rs], %[sab] sc1\n\t" \
  "s_waitcnt vmcnt(0)\n\t" \
  "v_readfirstlane_b32 s93, v212\n\t" \
  "s_nop 3\n\t" \
  "s_cmp_eq_u32 s93, 1\n\t" \
  "s_cbranch_scc1 .Lvok%=\n\t" \
  "s_cmp_lt_u32 s92, 0x80000\n\t" \
  "s_cbranch_scc1 .Lvr%=\n\t" \
  "v_mov_b32 v212, 1\n\t" \
  "buffer_store_dword v212, off, %[rs], %[sab] sc1\n\t" \
  "s_branch .Lvok%=\n\t" \
  ".Lvr%=:\n\t" \
  "buffer_load_dword v252, v249, %[rs], %[sv] offen offset:0 sc1\n\t" \
  "buffer_load_dword v253, v249, %[rs], %[sv] offen offset:256 sc1\n\t" \
  "s_waitcnt vmcnt(0)\n\t" \
  "v_mfma_f32_16x16x16_bf16 v[224:227], v[252:253], v[240:241], 0\n\t" \
  "v_mfma_f32_16x16x16_bf16 v[228:231], v[252:253], v[242:243], 0\n\t" \
  "v_max_u32 v212, v252, v253\n\t" \
  "v_cmp_le_u32 vcc, %[sent], v212\n\t" \
  "s_nop 4\n\t" \
  "s_branch .Lpv%=\n\t" \
  "s_branch .Lend%=\n\t" \
  ".Lslow%=:\n\t" \
  "s_mov_b64 s[94:95], 0\n\t" \
  "v_cmp_eq_u32 vcc, %[sent], v200\n\t" \
  "s_or_b64 s[94:95], s[94:95], vcc\n\t" \
  "v_cmp_eq_u32 vcc, %[sent], v201\n\t" \
  "s_or_b64 s[94:95], s[94:95], vcc\n\t" \
  "v_cmp_eq_u32 vcc, %[sent], v202\n\t" \
  "s_or_b64 s[94:95], s[94:95], vcc\n\t" \
  "v_cmp_eq_u32 vcc, %[sent], v203\n\t" \
  "s_or_b64 s[94:95], s[94:95], vcc\n\t" \
  "v_cmp_eq_u32 vcc, %[sent], v204\n\t" \
  "s_or_b64 s[94:95], s[94:95], vcc\n\t" \
  "v_cmp_eq_u32 vcc, %[sent], v205\n\t" \
  "s_or_b64 s[94:95], s[94:95], vcc\n\t" \
  "v_cmp_eq_u32 vcc, %[sent], v206\n\t" \
  "s_or_b64 s[94:95], s[94:95], vcc\n\t" \
  "v_cmp_eq_u32 vcc, %[sent], v207\n\t" \
  "s_or_b64 s[94:95], s[94:95], vcc\n\t" \
  "s_cmp_eq_u64 s[94:95], 0\n\t" \
  "s_cbranch_scc1 .Lok%=\n\t" \
  "v_mov_b64 v[192:193], 0\n\t" \
  "v_mov_b64 v[194:195], 0\n\t" \
  "v_mov_b64 v[196:197], 0\n\t" \
  "v_mov_b64 v[198:199], 0\n\t" \
  "s_add_u32 s92, s92, 1\n\t" \
  "s_cmp_eq_u32 s92, 1\n\t" \
  "s_cbranch_scc1 .Lpoll%=\n\t" \
  "s_sleep 6\n\t" \
  "s_and_b32 s93, s92, 63\n\t" \
  "s_cmp_lg_u32 s93, 0\n\t" \
  "s_cbranch_scc1 .Lpoll%=\n\t" \
  "v_mov_b32 v245, 0\n\t" \
  "buffer_load_dword v244, v245, %[rs], %[sab] offen sc1\n\t" \
  "s_waitcnt vmcnt(0)\n\t" \
  "v_readfirstlane_b32 s93, v244\n\t" \
  "s_nop 3\n\t" \
  "s_cmp_eq_u32 s93, 1\n\t" \
  "s_cbranch_scc1 .Lok%=\n\t" \
  "s_cmp_lt_u32 s92, 0x80000\n\t" \
  "s_cbranch_scc1 .Lpoll%=\n\t" \
  "v_mov_b32 v244, 1\n\t" \
  "buffer_store_dword v244, v245, %[rs], %[sab] offen sc1\n\t" \
  "s_branch .Lok%=\n\t" \
  ".Lend%=:\n\t"
#define QK_ASM_3 \
  "s_waitcnt lgkmcnt(0)\n\t" \
  "v_subrev_u32 v244, 0x8000, v250\n\t" \
  "ds_read_b128 v[216:219], v248 offset:45056\n\t" \
  "ds_read_b128 v[220:223], v248 offset:46080\n\t" \
  "ds_read_b128 v[224:227], v248 offset:47104\n\t" \
  "ds_read_b128 v[228:231], v248 offset:48128\n\t" \
  "ds_read_b128 v[208:211], v244 offset:0\n\t" \
  "ds_read_b128 v[212:215], v244 offset:1024\n\t" \
  "ds_read_b128 v[236:239], v244 offset:2048\n\t" \
  "ds_read_b128 v[240:243], v244 offset:3072\n\t" \
  "v_mov_b64 v[192:193], 0\n\t" \
  "v_mov_b64 v[194:195], 0\n\t" \
  "v_mov_b64 v[196:197], 0\n\t" \
  "v_mov_b64 v[198:199], 0\n\t" \
  "s_movk_i32 s92, 0x2000\n\t" \
  ".Lwt%=:\n\t" \
  "ds_read_b64 v[200:201], v248 offset:30720\n\t" \
  "s_memrealtime s[90:91]\n\t" \
  "s_sub_u32 s92, s92, 1\n\t" \
  "s_waitcnt lgkmcnt(0)\n\t" \
  "v_readfirstlane_b32 s93, v200\n\t" \
  "v_readfirstlane_b32 %[tgt], v201\n\t" \
  "s_cbranch_scc1 .Lgo%=\n\t" \
  "s_cmp_eq_u32 s93, %[step]\n\t" \
  "s_cbranch_scc1 .Lwx%=\n\t" \
  "s_sleep 1\n\t" \
  "s_branch .Lwt%=\n\t" \
  ".Lwx%=:\n\t" \
  "s_lshl_b32 s93, s90, 2\n\t" \
  "s_sub_i32 s93, %[tgt], s93\n\t" \
  "s_cmp_le_i32 s93, 0\n\t" \
  "s_cbranch_scc1 .Lgo%=\n\t" \
  "s_min_i32 s93, s93, 8000\n\t" \
  "s_add_u32 s93, s93, 3\n\t" \
  "s_lshr_b32 s93, s93, 2\n\t" \
  "s_add_u32 s93, s93, s90\n\t" \
  "s_memrealtime s[90:91]\n\t" \
  ".Lpace%=:\n\t" \
  "s_memrealtime s[94:95]\n\t" \
  "s_waitcnt lgkmcnt(1)\n\t" \
  "s_sub_i32 s91, s90, s93\n\t" \
  "s_cmp_ge_i32 s91, 0\n\t" \
  "s_cbranch_scc1 .Lgo%=\n\t" \
  "s_memrealtime s[90:91]\n\t" \
  "s_waitcnt lgkmcnt(1)\n\t" \
  "s_sub_i32 s95, s94, s93\n\t" \
  "s_cmp_lt_i32 s95, 0\n\t" \
  "s_cbranch_scc1 .Lpace%=\n\t" \
  ".Lgo%=:\n\t" \
  "s_mov_b32 s92, 0\n\t" \
  ".Lpoll%=:\n\t" \
  "buffer_load_dwordx4 v[200:203], v248, %[rs], %[som] offen sc1\n\t" \
  "buffer_load_dwordx4 v[204:207], v248, %[rs], %[som2] offen sc1\n\t" \
  "s_waitcnt vmcnt(1) lgkmcnt(0)\n\t" \
  "ds_write_b128 v249, v[200:203]\n\t" \
  "v_smfmac_f32_16x16x64_bf16 v[192:195], v[200:203], v[208:215], v251\n\t" \
  "v_max3_u32 v244, v200, v201, v202\n\t" \
  "v_smfmac_f32_16x16x64_bf16 v[196:199], v[200:203], v[236:243], v251\n\t" \
  "s_waitcnt vmcnt(0)\n\t" \
  "ds_write_b128 v249, v[204:207] offset:8192\n\t" \
  "v_smfmac_f32_16x16x64_bf16 v[192:195], v[204:207], v[216:223], v251\n\t" \
  "v_max3_u32 v244, v244, v203, v204\n\t" \
  "v_max3_u32 v244, v244, v205, v206\n\t" \
  "v_smfmac_f32_16x16x64_bf16 v[196:199], v[204:207], v[224:231], v251\n\t" \
  "v_max_u32 v244, v244, v207\n\t" \
  "v_cmp_le_u32 vcc, %[sent], v244\n\t" \
  "s_cbranch_vccnz .Lslow%=\n\t" \
  ".Lok%=:\n\t" \
  "ds_read_b128 v[216:219], v250 offset:32768\n\t" \
  "ds_read_b128 v[220:223], v250 offset:33792\n\t" \
  "ds_read_b128 v[224:227], v250 offset:34816\n\t" \
  "ds_read_b128 v[228:231], v250 offset:35840\n\t" \
  "s_waitcnt lgkmcnt(4)\n\t" \
  "s_barrier\n\t" \
  "ds_read_b128 v[208:211], v248 offset:2048\n\t" \
  "ds_read_b128 v[212:215], v248 offset:5120\n\t" \
  "ds_read_b128 v[236:239], v248 offset:6144\n\t" \
  "ds_read_b128 v[240:243], v248 offset:7168\n\t" \
  "s_waitcnt lgkmcnt(3)\n\t" \
  "ds_read_b128 v[244:247], v248 offset:12288\n\t" \
  "v_smfmac_f32_16x16x64_bf16 v[192:195], v[208:211], v[216:223], v251\n\t" \
  "ds_read_b128 v[216:219], v250 offset:0\n\t" \
  "v_smfmac_f32_16x16x64_bf16 v[196:199], v[208:211], v[224:231], v251\n\t" \
  "s_waitcnt lgkmcnt(4)\n\t" \
  "ds_read_b128 v[220:223], v250 offset:1024\n\t" \
  "v_smfmac_f32_16x16x64_bf16 v[192:195], v[212:215], v[0:7], v251\n\t" \
  "ds_read_b128 v[224:227], v250 offset:2048\n\t" \
  "v_smfmac_f32_16x16x64_bf16 v[196:199], v[212:215], v[8:15], v251\n\t" \
  "s_waitcnt lgkmcnt(5)\n\t" \
  "ds_read_b128 v[228:231], v250 offset:3072\n\t" \
  "v_smfmac_f32_16x16x64_bf16 v[192:195], v[236:239], v[16:23], v251\n\t" \
  "ds_read_b128 v[200:203], v248 offset:8192\n\t" \
  "v_smfmac_f32_16x16x64_bf16 v[196:199], v[236:239], v[24:31], v251\n\t" \
  "s_waitcnt lgkmcnt(6)\n\t" \
  "ds_read_b128 v[204:207], v248 offset:9216\n\t" \
  "v_smfmac_f32_16x16x64_bf16 v[192:195], v[240:243], v[32:39], v251\n\t" \
  "ds_read_b128 v[208:211], v248 offset:10240\n\t" \
  "v_smfmac_f32_16x16x64_bf16 v[196:199], v[240:243], v[40:47], v251\n\t" \
  "s_waitcnt lgkmcnt(5)\n\t" \
  "ds_read_b128 v[212:215], v248 offset:13312\n\t" \
  "v_smfmac_f32_16x16x64_bf16 v[192:195], v[244:247], v[216:223], v251\n\t" \
  "s_waitcnt lgkmcnt(4)\n\t" \
  "v_smfmac_f32_16x16x64_bf16 v[196:199], v[244:247], v[224:231], v251\n\t" \
  "s_waitcnt lgkmcnt(3)\n\t" \
  "ds_read_b128 v[236:239], v248 offset:14336\n\t" \
  "v_smfmac_f32_16x16x64_bf16 v[192:195], v[200:203], v[48:55], v251\n\t" \
  "ds_read_b128 v[240:243], v248 offset:15360\n\t" \
  "v_smfmac_f32_16x16x64_bf16 v[196:199], v[200:203], v[56:63], v251\n\t" \
  "s_waitcnt lgkmcnt(4)\n\t" \
  "v_smfmac_f32_16x16x64_bf16 v[192:195], v[204:207], v[64:71], v251\n\t" \
  "ds_read_b128 v[244:247], v248 offset:0\n\t" \
  "v_smfmac_f32_16x16x64_bf16 v[196:199], v[204:207], v[72:79], v251\n\t" \
  "s_waitcnt lgkmcnt(4)\n\t" \
  "v_smfmac_f32_16x16x64_bf16 v[192:195], v[208:211], v[80:87], v251\n\t" \
  "ds_read_b128 v[200:203], v248 offset:1024\n\t" \
  "v_smfmac_f32_16x16x64_bf16 v[196:199], v[208:211], v[88:95], v251\n\t" \
  "s_waitcnt lgkmcnt(4)\n\t" \
  "v_smfmac_f32_16x16x64_bf16 v[192:195], v[212:215], v[96:103], v251\n\t" \
  "ds_read_b128 v[204:207], v248 offset:4096\n\t" \
  "v_smfmac_f32_16x16x64_bf16 v[196:199], v[212:215], v[104:111], v251\n\t" \
  "s_waitcnt lgkmcnt(3)\n\t" \
  "v_smfmac_f32_16x16x64_bf16 v[192:195], v[236:239], v[112:119], v251\n\t" \
  "v_smfmac_f32_16x16x64_bf16 v[196:199], v[236:239], v[120:127], v251\n\t" \
  "v_smfmac_f32_16x16x64_bf16 v[192:195], v[240:243], v[128:135], v251\n\t" \
  "v_smfmac_f32_16x16x64_bf16 v[196:199], v[240:243], v[136:143], v251\n\t" \
  "s_waitcnt lgkmcnt(1)\n\t" \
  "v_smfmac_f32_16x16x64_bf16 v[192:195], v[244:247], v[144:151], v251\n\t" \
  "v_smfmac_f32_16x16x64_bf16 v[196:199], v[244:247], v[152:159], v251\n\t" \
  "v_smfmac_f32_16x16x64_bf16 v[192:195], v[200:203], v[160:167], v251\n\t" \
  "v_smfmac_f32_16x16x64_bf16 v[196:199], v[200:203], v[168:175], v251\n\t" \
  "s_waitcnt lgkmcnt(0)\n\t" \
  "v_smfmac_f32_16x16x64_bf16 v[192:195], v[204:207], v[176:183], v251\n\t" \
  "v_smfmac_f32_16x16x64_bf16 v[196:199], v[204:207], v[184:191], v251\n\t" \
  "ds_read_b128 v[200:203], v248 offset:19456\n\t" \
  "ds_read_b128 v[204:207], v248 offset:23552\n\t" \
  "v_mov_b32 v250, v232\n\t" \
  "v_mov_b32 v249, v233\n\t" \
  "s_nop 2\n\t" \
  "v_add_f32 v192, v192, v194\n\t" \
  "v_add_f32 v193, v193, v195\n\t" \
  "v_add_f32 v196, v196, v198\n\t" \
  "v_add_f32 v197, v197, v199\n\t" \
  "v_cvt_pk_bf16_f32 v192, v192, v193\n\t" \
  "v_cvt_pk_bf16_f32 v196, v196, v197\n\t" \
  "ds_write2_b32 v252, v192, v196 offset1:64\n\t" \
  "v_mov_b32 v251, v234\n\t" \
  "ds_read_b64_tr_b16 v[244:245], v250 offset:1536\n\t" \
  "ds_read_b64_tr_b16 v[246:247], v250 offset:1600\n\t" \
  "v_mov_b32 v212, %[step]\n\t" \
  "s_mov_b64 exec, 1\n\t" \
  "ds_write_b32 v248, v212 offset:28720\n\t" \
  "s_mov_b64 exec, -1\n\t" \
  "buffer_load_dwordx4 v[192:195], v248, %[rt], %[svt] offen offset:0 sc1\n\t" \
  "buffer_load_dwordx4 v[196:199], v248, %[rt], %[svt] offen offset:1024 sc1\n\t" \
  "s_waitcnt lgkmcnt(1)\n\t" \
  "v_mfma_f32_16x16x32_bf16 v[200:203], v[200:203], v[244:247], 0\n\t" \
  "v_mfma_f32_16x16x32_bf16 v[204:207], v[204:207], v[244:247], 0\n\t" \
  "s_nop 7\n\t" \
  "v_cndmask_b32 v216, v200, v204, %[nh]\n\t" \
  "v_cndmask_b32 v217, v201, v205, %[nh]\n\t" \
  "v_cndmask_b32 v218, v202, v206, %[nh]\n\t" \
  "v_cndmask_b32 v219, v203, v207, %[nh]\n\t" \
  "ds_write_b128 v248, v[216:219] offset:19456\n\t" \
  "buffer_load_dword v252, v249, %[rs], %[sv] offen offset:0 sc1\n\t" \
  "buffer_load_dword v253, v249, %[rs], %[sv] offen offset:256 sc1\n\t" \
  "ds_read_b32 v212, v248 offset:28672\n\t" \
  "ds_read_b64_tr_b16 v[232:233], v250 offset:0\n\t" \
  "ds_read_b64_tr_b16 v[234:235], v250 offset:64\n\t" \
  "ds_read_b64_tr_b16 v[236:237], v250 offset:512\n\t" \
  "ds_read_b64_tr_b16 v[238:239], v250 offset:576\n\t" \
  "ds_read_b64_tr_b16 v[240:241], v250 offset:1024\n\t" \
  "ds_read_b64_tr_b16 v[242:243], v250 offset:1088\n\t" \
  "s_setprio 3\n\t" \
  "s_waitcnt lgkmcnt(0)\n\t" \
  "v_cmp_ne_u32 vcc, %[step], v212\n\t" \
  "s_barrier\n\t" \
  "s_and_b32 s93, vcc_lo, 0xf\n\t" \
  "s_cbranch_scc1 .Lre%=\n\t" \
  ".Lrb%=:\n\t" \
  "ds_read_b128 v[220:223], v248 offset:16384\n\t" \
  "ds_read_b128 v[224:227], v248 offset:17408\n\t" \
  "ds_read_b128 v[228:231], v248 offset:18432\n\t" \
  "ds_read_b64_tr_b16 v[212:213], v250 offset:3584\n\t" \
  "ds_read_b64_tr_b16 v[214:215], v250 offset:3648\n\t" \
  "ds_read_b64_tr_b16 v[200:201], v250 offset:2048\n\t" \
  "ds_read_b64_tr_b16 v[202:203], v250 offset:2112\n\t" \
  "ds_read_b64_tr_b16 v[204:205], v250 offset:2560\n\t" \
  "ds_read_b64_tr_b16 v[206:207], v250 offset:2624\n\t" \
  "ds_read_b64_tr_b16 v[208:209], v250 offset:3072\n\t" \
  "ds_read_b64_tr_b16 v[210:211], v250 offset:3136\n\t" \
  "s_waitcnt lgkmcnt(10)\n\t" \
  "v_pk_add_f32 v[216:217], v[216:217], v[220:221]\n\t" \
  "v_pk_add_f32 v[218:219], v[218:219], v[222:223]\n\t" \
  "s_waitcnt lgkmcnt(8)\n\t" \
  "v_pk_add_f32 v[224:225], v[224:225], v[228:229]\n\t" \
  "v_pk_add_f32 v[226:227], v[226:227], v[230:231]\n\t" \
  "v_pk_add_f32 v[216:217], v[216:217], v[224:225]\n\t" \
  "v_pk_add_f32 v[218:219], v[218:219], v[226:227]\n\t" \
  "s_waitcnt lgkmcnt(6)\n\t" \
  "v_mfma_f32_16x16x32_bf16 v[212:215], v[212:215], v[244:247], 0\n\t" \
  "v_max3_f32 v224, v216, v217, v218\n\t" \
  "v_max_f32 v224, v224, v219\n\t" \
  "s_waitcnt lgkmcnt(4)\n\t" \
  "v_mfma_f32_16x16x32_bf16 v[212:215], v[200:203], v[232:235], v[212:215]\n\t" \
  "v_max_f32_dpp v225, v224, v224 row_ror:8 row_mask:0xf bank_mask:0xf\n\t" \
  "v_max_f32_dpp v226, v224, v224 row_ror:8 row_mask:0xf bank_mask:0xf\n\t" \
  "s_waitcnt lgkmcnt(2)\n\t" \
  "v_mfma_f32_16x16x32_bf16 v[212:215], v[204:207], v[236:239], v[212:215]\n\t" \
  "v_permlane32_swap_b32 v225, v226\n\t" \
  "s_waitcnt lgkmcnt(0)\n\t" \
  "v_mfma_f32_16x16x32_bf16 v[212:215], v[208:211], v[240:243], v[212:215]\n\t" \
  "v_max_f32 v231, v225, v226\n\t" \
  "v_mov_b32 v220, 0x3f803f80\n\t" \
  "v_mov_b32 v221, 0x3f803f80\n\t" \
  "v_mov_b64 v[222:223], v[220:221]\n\t" \
  "v_mov_b32 v245, 0\n\t" \
  "v_mov_b32 v246, 0\n\t" \
  "s_nop 1\n\t" \
  "v_max3_f32 v200, v212, v213, v231\n\t" \
  "v_max3_f32 v201, v200, v214, v215\n\t" \
  "v_max3_f32 v202, v200, v214, v215\n\t" \
  "v_cndmask_b32 v212, v212, v214, %[up]\n\t" \
  "v_cndmask_b32 v213, v213, v215, %[up]\n\t" \
  "v_permlane16_swap_b32 v201, v202\n\t" \
  "v_max_f32 v200, v201, v202\n\t" \
  "v_mul_f32 v200, %[negc], v200\n\t" \
  "v_fma_f32 v212, v212, %[c], v200\n\t" \
  "v_fma_f32 v213, v213, %[c], v200\n\t" \
  "v_exp_f32 v212, v212\n\t" \
  "v_fma_f32 v216, v216, %[c], v200\n\t" \
  "v_exp_f32 v213, v213\n\t" \
  "v_fma_f32 v217, v217, %[c], v200\n\t" \
  "v_exp_f32 v216, v216\n\t" \
  "v_fma_f32 v218, v218, %[c], v200\n\t" \
  "v_cvt_pk_bf16_f32 v244, v212, v213\n\t" \
  "v_cvt_pk_bf16_f32 v247, v212, v213\n\t" \
  "v_exp_f32 v217, v217\n\t" \
  "v_fma_f32 v219, v219, %[c], v200\n\t" \
  "v_mfma_f32_16x16x16_bf16 v[208:211], v[220:221], v[244:245], 0\n\t" \
  "v_exp_f32 v218, v218\n\t" \
  "s_waitcnt vmcnt(0)\n\t" \
  "v_mfma_f32_16x16x16_bf16 v[224:227], v[252:253], v[244:245], 0\n\t" \
  "v_cvt_pk_bf16_f32 v206, v216, v217\n\t" \
  "v_cvt_pk_bf16_f32 v204, v216, v217\n\t" \
  "v_exp_f32 v219, v219\n\t" \
  "v_mfma_f32_16x16x16_bf16 v[228:231], v[252:253], v[246:247], 0\n\t" \
  "v_cvt_pk_bf16_f32 v207, v218, v219\n\t" \
  "v_cvt_pk_bf16_f32 v205, v218, v219\n\t" \
  "v_mov_b32_dpp v204, v206 row_ror:8 row_mask:0xf bank_mask:0xc\n\t" \
  "v_mov_b32_dpp v206, v206 row_ror:8 row_mask:0xf bank_mask:0x3\n\t" \
  "v_mov_b32_dpp v205, v207 row_ror:8 row_mask:0xf bank_mask:0xc\n\t" \
  "v_mov_b32_dpp v207, v207 row_ror:8 row_mask:0xf bank_mask:0x3\n\t" \
  "v_max_u32 v200, v252, v253\n\t" \
  "v_cmp_le_u32 vcc, %[sent], v200\n\t" \
  "v_mfma_f32_16x16x32_bf16 v[208:211], v[220:223], v[204:207], v[208:211]\n\t" \
  ".Lpv%=:\n\t" \
  "v_mfma_f32_16x16x32_bf16 v[224:227], v[192:195], v[204:207], v[224:227]\n\t" \
  "v_mfma_f32_16x16x32_bf16 v[228:231], v[196:199], v[204:207], v[228:231]\n\t" \
  "s_cbranch_vccnz .Lvs%=\n\t" \
  ".Lvok%=:\n\t" \
  "s_nop 4\n\t" \
  "v_rcp_f32 v200, v208\n\t" \
  "v_cndmask_b32 v224, v224, v226, %[nh]\n\t" \
  "v_cndmask_b32 v225, v225, v227, %[nh]\n\t" \
  "v_mul_f32 v224, v224, v200\n\t" \
  "v_mul_f32 v225, v225, v200\n\t" \
  "v_cvt_pk_bf16_f32 v224, v224, v225\n\t" \
  "buffer_store_dword v224, v251, %[rs], %[so] offen offset:0 sc1\n\t" \
  "v_cndmask_b32 v228, v228, v230, %[nh]\n\t" \
  "v_cndmask_b32 v229, v229, v231, %[nh]\n\t" \
  "v_mul_f32 v228, v228, v200\n\t" \
  "v_mul_f32 v229, v229, v200\n\t" \
  "v_cvt_pk_bf16_f32 v228, v228, v229\n\t" \
  "buffer_store_dword v228, v251, %[rs], %[so] offen offset:256 sc1\n\t" \
  "s_memtime s[90:91]\n\t" \
  "s_setprio 0\n\t" \
  "s_waitcnt lgkmcnt(0)\n\t" \
  "s_mov_b32 %[tout], s90\n\t" \
  "s_mov_b32 %[spins], s92\n\t" \
  "s_branch .Lend%=\n\t" \
  ".Lre%=:\n\t" \
  "ds_read_b64_tr_b16 v[232:233], v250 offset:0\n\t" \
  "ds_read_b64_tr_b16 v[234:235], v250 offset:64\n\t" \
  "ds_read_b64_tr_b16 v[236:237], v250 offset:512\n\t" \
  "ds_read_b64_tr_b16 v[238:239], v250 offset:576\n\t" \
  "ds_read_b64_tr_b16 v[240:241], v250 offset:1024\n\t" \
  "ds_read_b64_tr_b16 v[242:243], v250 offset:1088\n\t" \
  "s_waitcnt lgkmcnt(0)\n\t" \
  "s_branch .Lrb%=\n\t" \
  ".Lvs%=:\n\t" \
  "v_cmp_eq_u32 vcc, %[sent], v252\n\t" \
  "v_cmp_eq_u32 s[94:95], %[sent], v253\n\t" \
  "s_or_b64 s[94:95], s[94:95], vcc\n\t" \
  "s_cbranch_scc0 .Lvok%=\n\t" \
  "s_add_u32 s92, s92, 1\n\t" \
  "s_sleep 2\n\t" \
  "s_and_b32 s93, s92, 63\n\t" \
  "s_cbranch_scc1 .Lvr%=\n\t" \
  "buffer_load_dword v200, off, %[rs], %[sab] sc1\n\t" \
  "s_waitcnt vmcnt(0)\n\t" \
  "v_readfirstlane_b32 s93, v200\n\t" \
  "s_nop 3\n\t" \
  "s_cmp_eq_u32 s93, 1\n\t" \
  "s_cbranch_scc1 .Lvok%=\n\t" \
  "s_cmp_lt_u32 s92, 0x80000\n\t" \
  "s_cbranch_scc1 .Lvr%=\n\t" \
  "v_mov_b32 v200, 1\n\t" \
  "buffer_store_dword v200, off, %[rs], %[sab] sc1\n\t" \
  "s_branch .Lvok%=\n\t" \
  ".Lvr%=:\n\t" \
  "buffer_load_dword v252, v249, %[rs], %[sv] offen offset:0 sc1\n\t" \
  "buffer_load_dword v253, v249, %[rs], %[sv] offen offset:256 sc1\n\t" \
  "s_waitcnt vmcnt(0)\n\t" \
  "v_mfma_f32_16x16x16_bf16 v[224:227], v[252:253], v[244:245], 0\n\t" \
  "v_mfma_f32_16x16x16_bf16 v[228:231], v[252:253], v[246:247], 0\n\t" \
  "v_max_u32 v200, v252, v253\n\t" \
  "v_cmp_le_u32 vcc, %[sent], v200\n\t" \
  "s_nop 4\n\t" \
  "s_branch .Lpv%=\n\t" \
  "s_branch .Lend%=\n\t" \
  ".Lslow%=:\n\t" \
  "s_mov_b64 s[94:95], 0\n\t" \
  "v_cmp_eq_u32 vcc, %[sent], v200\n\t" \
  "s_or_b64 s[94:95], s[94:95], vcc\n\t" \
  "v_cmp_eq_u32 vcc, %[sent], v201\n\t" \
  "s_or_b64 s[94:95], s[94:95], vcc\n\t" \
  "v_cmp_eq_u32 vcc, %[sent], v202\n\t" \
  "s_or_b64 s[94:95], s[94:95], vcc\n\t" \
  "v_cmp_eq_u32 vcc, %[sent], v203\n\t" \
  "s_or_b64 s[94:95], s[94:95], vcc\n\t" \
  "v_cmp_eq_u32 vcc, %[sent], v204\n\t" \
  "s_or_b64 s[94:95], s[94:95], vcc\n\t" \
  "v_cmp_eq_u32 vcc, %[sent], v205\n\t" \
  "s_or_b64 s[94:95], s[94:95], vcc\n\t" \
  "v_cmp_eq_u32 vcc, %[sent], v206\n\t" \
  "s_or_b64 s[94:95], s[94:95], vcc\n\t" \
  "v_cmp_eq_u32 vcc, %[sent], v207\n\t" \
  "s_or_b64 s[94:95], s[94:95], vcc\n\t" \
  "s_cmp_eq_u64 s[94:95], 0\n\t" \
  "s_cbranch_scc1 .Lok%=\n\t" \
  "v_mov_b64 v[192:193], 0\n\t" \
  "v_mov_b64 v[194:195], 0\n\t" \
  "v_mov_b64 v[196:197], 0\n\t" \
  "v_mov_b64 v[198:199], 0\n\t" \
  "s_add_u32 s92, s92, 1\n\t" \
  "s_cmp_eq_u32 s92, 1\n\t" \
  "s_cbranch_scc1 .Lpoll%=\n\t" \
  "s_sleep 6\n\t" \
  "s_and_b32 s93, s92, 63\n\t" \
  "s_cmp_lg_u32 s93, 0\n\t" \
  "s_cbranch_scc1 .Lpoll%=\n\t" \
  "v_mov_b32 v245, 0\n\t" \
  "buffer_load_dword v244, v245, %[rs], %[sab] offen sc1\n\t" \
  "s_waitcnt vmcnt(0)\n\t" \
  "v_readfirstlane_b32 s93, v244\n\t" \
  "s_nop 3\n\t" \
  "s_cmp_eq_u32 s93, 1\n\t" \
  "s_cbranch_scc1 .Lok%=\n\t" \
  "s_cmp_lt_u32 s92, 0x80000\n\t" \
  "s_cbranch_scc1 .Lpoll%=\n\t" \
  "v_mov_b32 v244, 1\n\t" \
  "buffer_store_dword v244, v245, %[rs], %[sab] offen sc1\n\t" \
  "s_branch .Lok%=\n\t" \
  ".Lend%=:\n\t"
#define QK_ASM_4 \
  "s_waitcnt lgkmcnt(0)\n\t" \
  "v_subrev_u32 v244, 0x8000, v250\n\t" \
  "ds_read_b128 v[216:219], v248 offset:49152\n\t" \
  "ds_read_b128 v[220:223], v248 offset:50176\n\t" \
  "ds_read_b128 v[224:227], v248 offset:51200\n\t" \
  "ds_read_b128 v[228:231], v248 offset:52224\n\t" \
  "ds_read_b128 v[208:211], v244 offset:0\n\t" \
  "ds_read_b128 v[212:215], v244 offset:1024\n\t" \
  "ds_read_b128 v[236:239], v244 offset:2048\n\t" \
  "ds_read_b128 v[240:243], v244 offset:3072\n\t" \
  "v_mov_b64 v[192:193], 0\n\t" \
  "v_mov_b64 v[194:195], 0\n\t" \
  "v_mov_b64 v[196:197], 0\n\t" \
  "v_mov_b64 v[198:199], 0\n\t" \
  "s_movk_i32 s92, 0x2000\n\t" \
  ".Lwt%=:\n\t" \
  "ds_read_b64 v[200:201], v248 offset:30720\n\t" \
  "s_memrealtime s[90:91]\n\t" \
  "s_sub_u32 s92, s92, 1\n\t" \
  "s_waitcnt lgkmcnt(0)\n\t" \
  "v_readfirstlane_b32 s93, v200\n\t" \
  "v_readfirstlane_b32 %[tgt], v201\n\t" \
  "s_cbranch_scc1 .Lgo%=\n\t" \
  "s_cmp_eq_u32 s93, %[step]\n\t" \
  "s_cbranch_scc1 .Lwx%=\n\t" \
  "s_sleep 1\n\t" \
  "s_branch .Lwt%=\n\t" \
  ".Lwx%=:\n\t" \
  "s_lshl_b32 s93, s90, 2\n\t" \
  "s_sub_i32 s93, %[tgt], s93\n\t" \
  "s_cmp_le_i32 s93, 0\n\t" \
  "s_cbranch_scc1 .Lgo%=\n\t" \
  "s_min_i32 s93, s93, 8000\n\t" \
  "s_add_u32 s93, s93, 3\n\t" \
  "s_lshr_b32 s93, s93, 2\n\t" \
  "s_add_u32 s93, s93, s90\n\t" \
  "s_memrealtime s[90:91]\n\t" \
  ".Lpace%=:\n\t" \
  "s_memrealtime s[94:95]\n\t" \
  "s_waitcnt lgkmcnt(1)\n\t" \
  "s_sub_i32 s91, s90, s93\n\t" \
  "s_cmp_ge_i32 s91, 0\n\t" \
  "s_cbranch_scc1 .Lgo%=\n\t" \
  "s_memrealtime s[90:91]\n\t" \
  "s_waitcnt lgkmcnt(1)\n\t" \
  "s_sub_i32 s95, s94, s93\n\t" \
  "s_cmp_lt_i32 s95, 0\n\t" \
  "s_cbranch_scc1 .Lpace%=\n\t" \
  ".Lgo%=:\n\t" \
  "s_mov_b32 s92, 0\n\t" \
  ".Lpoll%=:\n\t" \
  "buffer_load_dwordx4 v[200:203], v248, %[rs], %[som] offen sc1\n\t" \
  "buffer_load_dwordx4 v[204:207], v248, %[rs], %[som2] offen sc1\n\t" \
  "s_waitcnt vmcnt(1) lgkmcnt(0)\n\t" \
  "ds_write_b128 v249, v[200:203]\n\t" \
  "v_smfmac_f32_16x16x64_bf16 v[192:195], v[200:203], v[208:215], v251\n\t" \
  "v_max3_u32 v244, v200, v201, v202\n\t" \
  "v_smfmac_f32_16x16x64_bf16 v[196:199], v[200:203], v[236:243], v251\n\t" \
  "s_waitcnt vmcnt(0)\n\t" \
  "ds_write_b128 v249, v[204:207] offset:8192\n\t" \
  "v_smfmac_f32_16x16x64_bf16 v[192:195], v[204:207], v[216:223], v251\n\t" \
  "v_max3_u32 v244, v244, v203, v204\n\t" \
  "v_max3_u32 v244, v244, v205, v206\n\t" \
  "v_smfmac_f32_16x16x64_bf16 v[196:199], v[204:207], v[224:231], v251\n\t" \
  "v_max_u32 v244, v244, v207\n\t" \
  "v_cmp_le_u32 vcc, %[sent], v244\n\t" \
  "s_cbranch_vccnz .Lslow%=\n\t" \
  ".Lok%=:\n\t" \
  "ds_read_b128 v[216:219], v250 offset:32768\n\t" \
  "ds_read_b128 v[220:223], v250 offset:33792\n\t" \
  "ds_read_b128 v[224:227], v250 offset:34816\n\t" \
  "ds_read_b128 v[228:231], v250 offset:35840\n\t" \
  "s_waitcnt lgkmcnt(4)\n\t" \
  "s_barrier\n\t" \
  "ds_read_b128 v[208:211], v248 offset:5120\n\t" \
  "ds_read_b128 v[212:215], v248 offset:6144\n\t" \
  "ds_read_b128 v[236:239], v248 offset:7168\n\t" \
  "ds_read_b128 v[240:243], v248 offset:8192\n\t" \
  "s_waitcnt lgkmcnt(3)\n\t" \
  "ds_read_b128 v[244:247], v248 offset:13312\n\t" \
  "v_smfmac_f32_16x16x64_bf16 v[192:195], v[208:211], v[216:223], v251\n\t" \
  "ds_read_b128 v[216:219], v250 offset:0\n\t" \
  "v_smfmac_f32_16x16x64_bf16 v[196:199], v[208:211], v[224:231], v251\n\t" \
  "s_waitcnt lgkmcnt(4)\n\t" \
  "ds_read_b128 v[220:223], v250 offset:1024\n\t" \
  "v_smfmac_f32_16x16x64_bf16 v[192:195], v[212:215], v[0:7], v251\n\t" \
  "ds_read_b128 v[224:227], v250 offset:2048\n\t" \
  "v_smfmac_f32_16x16x64_bf16 v[196:199], v[212:215], v[8:15], v251\n\t" \
  "s_waitcnt lgkmcnt(5)\n\t" \
  "ds_read_b128 v[228:231], v250 offset:3072\n\t" \
  "v_smfmac_f32_16x16x64_bf16 v[192:195], v[236:239], v[16:23], v251\n\t" \
  "ds_read_b128 v[200:203], v248 offset:9216\n\t" \
  "v_smfmac_f32_16x16x64_bf16 v[196:199], v[236:239], v[24:31], v251\n\t" \
  "s_waitcnt lgkmcnt(6)\n\t" \
  "ds_read_b128 v[204:207], v248 offset:10240\n\t" \
  "v_smfmac_f32_16x16x64_bf16 v[192:195], v[240:243], v[32:39], v251\n\t" \
  "ds_read_b128 v[208:211], v248 offset:11264\n\t" \
  "v_smfmac_f32_16x16x64_bf16 v[196:199], v[240:243], v[40:47], v251\n\t" \
  "s_waitcnt lgkmcnt(5)\n\t" \
  "ds_read_b128 v[212:215], v248 offset:14336\n\t" \
  "v_smfmac_f32_16x16x64_bf16 v[192:195], v[244:247], v[216:223], v251\n\t" \
  "s_waitcnt lgkmcnt(4)\n\t" \
  "v_smfmac_f32_16x16x64_bf16 v[196:199], v[244:247], v[224:231], v251\n\t" \
  "s_waitcnt lgkmcnt(3)\n\t" \
  "ds_read_b128 v[236:239], v248 offset:15360\n\t" \
  "v_smfmac_f32_16x16x64_bf16 v[192:195], v[200:203], v[48:55], v251\n\t" \
  "ds_read_b128 v[240:243], v248 offset:0\n\t" \
  "v_smfmac_f32_16x16x64_bf16 v[196:199], v[200:203], v[56:63], v251\n\t" \
  "s_waitcnt lgkmcnt(4)\n\t" \
  "v_smfmac_f32_16x16x64_bf16 v[192:195], v[204:207], v[64:71], v251\n\t" \
  "ds_read_b128 v[244:247], v248 offset:1024\n\t" \
  "v_smfmac_f32_16x16x64_bf16 v[196:199], v[204:207], v[72:79], v251\n\t" \
  "s_waitcnt lgkmcnt(4)\n\t" \
  "v_smfmac_f32_16x16x64_bf16 v[192:195], v[208:211], v[80:87], v251\n\t" \
  "ds_read_b128 v[200:203], v248 offset:2048\n\t" \
  "v_smfmac_f32_16x16x64_bf16 v[196:199], v[208:211], v[88:95], v251\n\t" \
  "s_waitcnt lgkmcnt(4)\n\t" \
  "v_smfmac_f32_16x16x64_bf16 v[192:195], v[212:215], v[96:103], v251\n\t" \
  "ds_read_b128 v[204:207], v248 offset:3072\n\t" \
  "v_smfmac_f32_16x16x64_bf16 v[196:199], v[212:215], v[104:111], v251\n\t" \
  "s_waitcnt lgkmcnt(3)\n\t" \
  "v_smfmac_f32_16x16x64_bf16 v[192:195], v[236:239], v[112:119], v251\n\t" \
  "v_smfmac_f32_16x16x64_bf16 v[196:199], v[236:239], v[120:127], v251\n\t" \
  "v_smfmac_f32_16x16x64_bf16 v[192:195], v[240:243], v[128:135], v251\n\t" \
  "v_smfmac_f32_16x16x64_bf16 v[196:199], v[240:243], v[136:143], v251\n\t" \
  "s_waitcnt lgkmcnt(1)\n\t" \
  "v_smfmac_f32_16x16x64_bf16 v[192:195], v[244:247], v[144:151], v251\n\t" \
  "v_smfmac_f32_16x16x64_bf16 v[196:199], v[244:247], v[152:159], v251\n\t" \
  "v_smfmac_f32_16x16x64_bf16 v[192:195], v[200:203], v[160:167], v251\n\t" \
  "v_smfmac_f32_16x16x64_bf16 v[196:199], v[200:203], v[168:175], v251\n\t" \
  "s_waitcnt lgkmcnt(0)\n\t" \
  "v_smfmac_f32_16x16x64_bf16 v[192:195], v[204:207], v[176:183], v251\n\t" \
  "v_smfmac_f32_16x16x64_bf16 v[196:199], v[204:207], v[184:191], v251\n\t" \
  "s_nop 6\n\t" \
  "v_add_f32 v192, v192, v194\n\t" \
  "v_add_f32 v193, v193, v195\n\t" \
  "v_add_f32 v196, v196, v198\n\t" \
  "v_add_f32 v197, v197, v199\n\t" \
  "v_cvt_pk_bf16_f32 v192, v192, v193\n\t" \
  "v_cvt_pk_bf16_f32 v196, v196, v197\n\t" \
  "ds_write2_b32 v252, v192, v196 offset1:64\n\t" \
  "s_waitcnt lgkmcnt(0)\n\t" \
  "s_barrier\n\t" \
  "buffer_store_dword v192, v235, %[rs], %[sk] offen sc1\n\t" \
  "buffer_store_dword v196, v235, %[rs], %[sk] offen offset:256 sc1\n\t" \
  "s_mov_b32 %[spins], s92\n\t" \
  "s_branch .Lend%=\n\t" \
  ".Lslow%=:\n\t" \
  "s_mov_b64 s[94:95], 0\n\t" \
  "v_cmp_eq_u32 vcc, %[sent], v200\n\t" \
  "s_or_b64 s[94:95], s[94:95], vcc\n\t" \
  "v_cmp_eq_u32 vcc, %[sent], v201\n\t" \
  "s_or_b64 s[94:95], s[94:95], vcc\n\t" \
  "v_cmp_eq_u32 vcc, %[sent], v202\n\t" \
  "s_or_b64 s[94:95], s[94:95], vcc\n\t" \
  "v_cmp_eq_u32 vcc, %[sent], v203\n\t" \
  "s_or_b64 s[94:95], s[94:95], vcc\n\t" \
  "v_cmp_eq_u32 vcc, %[sent], v204\n\t" \
  "s_or_b64 s[94:95], s[94:95], vcc\n\t" \
  "v_cmp_eq_u32 vcc, %[sent], v205\n\t" \
  "s_or_b64 s[94:95], s[94:95], vcc\n\t" \
  "v_cmp_eq_u32 vcc, %[sent], v206\n\t" \
  "s_or_b64 s[94:95], s[94:95], vcc\n\t" \
  "v_cmp_eq_u32 vcc, %[sent], v207\n\t" \
  "s_or_b64 s[94:95], s[94:95], vcc\n\t" \
  "s_cmp_eq_u64 s[94:95], 0\n\t" \
  "s_cbranch_scc1 .Lok%=\n\t" \
  "v_mov_b64 v[192:193], 0\n\t" \
  "v_mov_b64 v[194:195], 0\n\t" \
  "v_mov_b64 v[196:197], 0\n\t" \
  "v_mov_b64 v[198:199], 0\n\t" \
  "s_add_u32 s92, s92, 1\n\t" \
  "s_cmp_eq_u32 s92, 1\n\t" \
  "s_cbranch_scc1 .Lpoll%=\n\t" \
  "s_sleep 6\n\t" \
  "s_and_b32 s93, s92, 63\n\t" \
  "s_cmp_lg_u32 s93, 0\n\t" \
  "s_cbranch_scc1 .Lpoll%=\n\t" \
  "v_mov_b32 v245, 0\n\t" \
  "buffer_load_dword v244, v245, %[rs], %[sab] offen sc1\n\t" \
  "s_waitcnt vmcnt(0)\n\t" \
  "v_readfirstlane_b32 s93, v244\n\t" \
  "s_nop 3\n\t" \
  "s_cmp_eq_u32 s93, 1\n\t" \
  "s_cbranch_scc1 .Lok%=\n\t" \
  "s_cmp_lt_u32 s92, 0x80000\n\t" \
  "s_cbranch_scc1 .Lpoll%=\n\t" \
  "v_mov_b32 v244, 1\n\t" \
  "buffer_store_dword v244, v245, %[rs], %[sab] offen sc1\n\t" \
  "s_branch .Lok%=\n\t" \
  ".Lend%=:\n\t"
#define QK_ASM_5 \
  "s_waitcnt lgkmcnt(0)\n\t" \
  "v_subrev_u32 v244, 0x8000, v250\n\t" \
  "ds_read_b128 v[216:219], v248 offset:53248\n\t" \
  "ds_read_b128 v[220:223], v248 offset:54272\n\t" \
  "ds_read_b128 v[224:227], v248 offset:55296\n\t" \
  "ds_read_b128 v[228:231], v248 offset:56320\n\t" \
  "ds_read_b128 v[208:211], v244 offset:0\n\t" \
  "ds_read_b128 v[212:215], v244 offset:1024\n\t" \
  "ds_read_b128 v[236:239], v244 offset:2048\n\t" \
  "ds_read_b128 v[240:243], v244 offset:3072\n\t" \
  "v_mov_b64 v[192:193], 0\n\t" \
  "v_mov_b64 v[194:195], 0\n\t" \
  "v_mov_b64 v[196:197], 0\n\t" \
  "v_mov_b64 v[198:199], 0\n\t" \
  "s_movk_i32 s92, 0x2000\n\t" \
  ".Lwt%=:\n\t" \
  "ds_read_b64 v[200:201], v248 offset:30720\n\t" \
  "s_memrealtime s[90:91]\n\t" \
  "s_sub_u32 s92, s92, 1\n\t" \
  "s_waitcnt lgkmcnt(0)\n\t" \
  "v_readfirstlane_b32 s93, v200\n\t" \
  "v_readfirstlane_b32 %[tgt], v201\n\t" \
  "s_cbranch_scc1 .Lgo%=\n\t" \
  "s_cmp_eq_u32 s93, %[step]\n\t" \
  "s_cbranch_scc1 .Lwx%=\n\t" \
  "s_sleep 1\n\t" \
  "s_branch .Lwt%=\n\t" \
  ".Lwx%=:\n\t" \
  "s_lshl_b32 s93, s90, 2\n\t" \
  "s_sub_i32 s93, %[tgt], s93\n\t" \
  "s_cmp_le_i32 s93, 0\n\t" \
  "s_cbranch_scc1 .Lgo%=\n\t" \
  "s_min_i32 s93, s93, 8000\n\t" \
  "s_add_u32 s93, s93, 3\n\t" \
  "s_lshr_b32 s93, s93, 2\n\t" \
  "s_add_u32 s93, s93, s90\n\t" \
  "s_memrealtime s[90:91]\n\t" \
  ".Lpace%=:\n\t" \
  "s_memrealtime s[94:95]\n\t" \
  "s_waitcnt lgkmcnt(1)\n\t" \
  "s_sub_i32 s91, s90, s93\n\t" \
  "s_cmp_ge_i32 s91, 0\n\t" \
  "s_cbranch_scc1 .Lgo%=\n\t" \
  "s_memrealtime s[90:91]\n\t" \
  "s_waitcnt lgkmcnt(1)\n\t" \
  "s_sub_i32 s95, s94, s93\n\t" \
  "s_cmp_lt_i32 s95, 0\n\t" \
  "s_cbranch_scc1 .Lpace%=\n\t" \
  ".Lgo%=:\n\t" \
  "s_mov_b32 s92, 0\n\t" \
  ".Lpoll%=:\n\t" \
  "buffer_load_dwordx4 v[200:203], v248, %[rs], %[som] offen sc1\n\t" \
  "buffer_load_dwordx4 v[204:207], v248, %[rs], %[som2] offen sc1\n\t" \
  "s_waitcnt vmcnt(1) lgkmcnt(0)\n\t" \
  "ds_write_b128 v249, v[200:203]\n\t" \
  "v_smfmac_f32_16x16x64_bf16 v[192:195], v[200:203], v[208:215], v251\n\t" \
  "v_max3_u32 v244, v200, v201, v202\n\t" \
  "v_smfmac_f32_16x16x64_bf16 v[196:199], v[200:203], v[236:243], v251\n\t" \
  "s_waitcnt vmcnt(0)\n\t" \
  "ds_write_b128 v249, v[204:207] offset:8192\n\t" \
  "v_smfmac_f32_16x16x64_bf16 v[192:195], v[204:207], v[216:223], v251\n\t" \
  "v_max3_u32 v244, v244, v203, v204\n\t" \
  "v_max3_u32 v244, v244, v205, v206\n\t" \
  "v_smfmac_f32_16x16x64_bf16 v[196:199], v[204:207], v[224:231], v251\n\t" \
  "v_max_u32 v244, v244, v207\n\t" \
  "v_cmp_le_u32 vcc, %[sent], v244\n\t" \
  "s_cbranch_vccnz .Lslow%=\n\t" \
  ".Lok%=:\n\t" \
  "ds_read_b128 v[216:219], v250 offset:32768\n\t" \
  "ds_read_b128 v[220:223], v250 offset:33792\n\t" \
  "ds_read_b128 v[224:227], v250 offset:34816\n\t" \
  "ds_read_b128 v[228:231], v250 offset:35840\n\t" \
  "s_waitcnt lgkmcnt(4)\n\t" \
  "s_barrier\n\t" \
  "ds_read_b128 v[208:211], v248 offset:4096\n\t" \
  "ds_read_b128 v[212:215], v248 offset:7168\n\t" \
  "ds_read_b128 v[236:239], v248 offset:8192\n\t" \
  "ds_read_b128 v[240:243], v248 offset:9216\n\t" \
  "s_waitcnt lgkmcnt(3)\n\t" \
  "ds_read_b128 v[244:247], v248 offset:12288\n\t" \
  "v_smfmac_f32_16x16x64_bf16 v[192:195], v[208:211], v[216:223], v251\n\t" \
  "ds_read_b128 v[216:219], v250 offset:0\n\t" \
  "v_smfmac_f32_16x16x64_bf16 v[196:199], v[208:211], v[224:231], v251\n\t" \
  "s_waitcnt lgkmcnt(4)\n\t" \
  "ds_read_b128 v[220:223], v250 offset:1024\n\t" \
  "v_smfmac_f32_16x16x64_bf16 v[192:195], v[212:215], v[0:7], v251\n\t" \
  "ds_read_b128 v[224:227], v250 offset:2048\n\t" \
  "v_smfmac_f32_16x16x64_bf16 v[196:199], v[212:215], v[8:15], v251\n\t" \
  "s_waitcnt lgkmcnt(5)\n\t" \
  "ds_read_b128 v[228:231], v250 offset:3072\n\t" \
  "v_smfmac_f32_16x16x64_bf16 v[192:195], v[236:239], v[16:23], v251\n\t" \
  "ds_read_b128 v[200:203], v248 offset:10240\n\t" \
  "v_smfmac_f32_16x16x64_bf16 v[196:199], v[236:239], v[24:31], v251\n\t" \
  "s_waitcnt lgkmcnt(6)\n\t" \
  "ds_read_b128 v[204:207], v248 offset:11264\n\t" \
  "v_smfmac_f32_16x16x64_bf16 v[192:195], v[240:243], v[32:39], v251\n\t" \
  "ds_read_b128 v[208:211], v248 offset:14336\n\t" \
  "v_smfmac_f32_16x16x64_bf16 v[196:199], v[240:243], v[40:47], v251\n\t" \
  "s_waitcnt lgkmcnt(5)\n\t" \
  "ds_read_b128 v[212:215], v248 offset:15360\n\t" \
  "v_smfmac_f32_16x16x64_bf16 v[192:195], v[244:247], v[216:223], v251\n\t" \
  "s_waitcnt lgkmcnt(4)\n\t" \
  "v_smfmac_f32_16x16x64_bf16 v[196:199], v[244:247], v[224:231], v251\n\t" \
  "s_waitcnt lgkmcnt(3)\n\t" \
  "ds_read_b128 v[236:239], v248 offset:0\n\t" \
  "v_smfmac_f32_16x16x64_bf16 v[192:195], v[200:203], v[48:55], v251\n\t" \
  "ds_read_b128 v[240:243], v248 offset:1024\n\t" \
  "v_smfmac_f32_16x16x64_bf16 v[196:199], v[200:203], v[56:63], v251\n\t" \
  "s_waitcnt lgkmcnt(4)\n\t" \
  "v_smfmac_f32_16x16x64_bf16 v[192:195], v[204:207], v[64:71], v251\n\t" \
  "ds_read_b128 v[244:247], v248 offset:2048\n\t" \
  "v_smfmac_f32_16x16x64_bf16 v[196:199], v[204:207], v[72:79], v251\n\t" \
  "s_waitcnt lgkmcnt(4)\n\t" \
  "v_smfmac_f32_16x16x64_bf16 v[192:195], v[208:211], v[80:87], v251\n\t" \
  "ds_read_b128 v[200:203], v248 offset:3072\n\t" \
  "v_smfmac_f32_16x16x64_bf16 v[196:199], v[208:211], v[88:95], v251\n\t" \
  "s_waitcnt lgkmcnt(4)\n\t" \
  "v_smfmac_f32_16x16x64_bf16 v[192:195], v[212:215], v[96:103], v251\n\t" \
  "ds_read_b128 v[204:207], v248 offset:6144\n\t" \
  "v_smfmac_f32_16x16x64_bf16 v[196:199], v[212:215], v[104:111], v251\n\t" \
  "s_waitcnt lgkmcnt(3)\n\t" \
  "v_smfmac_f32_16x16x64_bf16 v[192:195], v[236:239], v[112:119], v251\n\t" \
  "v_smfmac_f32_16x16x64_bf16 v[196:199], v[236:239], v[120:127], v251\n\t" \
  "v_smfmac_f32_16x16x64_bf16 v[192:195], v[240:243], v[128:135], v251\n\t" \
  "v_smfmac_f32_16x16x64_bf16 v[196:199], v[240:243], v[136:143], v251\n\t" \
  "s_waitcnt lgkmcnt(1)\n\t" \
  "v_smfmac_f32_16x16x64_bf16 v[192:195], v[244:247], v[144:151], v251\n\t" \
  "v_smfmac_f32_16x16x64_bf16 v[196:199], v[244:247], v[152:159], v251\n\t" \
  "v_smfmac_f32_16x16x64_bf16 v[192:195], v[200:203], v[160:167], v251\n\t" \
  "v_smfmac_f32_16x16x64_bf16 v[196:199], v[200:203], v[168:175], v251\n\t" \
  "s_waitcnt lgkmcnt(0)\n\t" \
  "v_smfmac_f32_16x16x64_bf16 v[192:195], v[204:207], v[176:183], v251\n\t" \
  "v_smfmac_f32_16x16x64_bf16 v[196:199], v[204:207], v[184:191], v251\n\t" \
  "s_nop 6\n\t" \
  "v_add_f32 v192, v192, v194\n\t" \
  "v_add_f32 v193, v193, v195\n\t" \
  "v_add_f32 v196, v196, v198\n\t" \
  "v_add_f32 v197, v197, v199\n\t" \
  "v_cvt_pk_bf16_f32 v192, v192, v193\n\t" \
  "v_cvt_pk_bf16_f32 v196, v196, v197\n\t" \
  "ds_write2_b32 v252, v192, v196 offset1:64\n\t" \
  "s_waitcnt lgkmcnt(0)\n\t" \
  "s_barrier\n\t" \
  "buffer_store_dword v192, v235, %[rs], %[sk] offen sc1\n\t" \
  "buffer_store_dword v196, v235, %[rs], %[sk] offen offset:256 sc1\n\t" \
  "s_mov_b32 %[spins], s92\n\t" \
  "s_branch .Lend%=\n\t" \
  ".Lslow%=:\n\t" \
  "s_mov_b64 s[94:95], 0\n\t" \
  "v_cmp_eq_u32 vcc, %[sent], v200\n\t" \
  "s_or_b64 s[94:95], s[94:95], vcc\n\t" \
  "v_cmp_eq_u32 vcc, %[sent], v201\n\t" \
  "s_or_b64 s[94:95], s[94:95], vcc\n\t" \
  "v_cmp_eq_u32 vcc, %[sent], v202\n\t" \
  "s_or_b64 s[94:95], s[94:95], vcc\n\t" \
  "v_cmp_eq_u32 vcc, %[sent], v203\n\t" \
  "s_or_b64 s[94:95], s[94:95], vcc\n\t" \
  "v_cmp_eq_u32 vcc, %[sent], v204\n\t" \
  "s_or_b64 s[94:95], s[94:95], vcc\n\t" \
  "v_cmp_eq_u32 vcc, %[sent], v205\n\t" \
  "s_or_b64 s[94:95], s[94:95], vcc\n\t" \
  "v_cmp_eq_u32 vcc, %[sent], v206\n\t" \
  "s_or_b64 s[94:95], s[94:95], vcc\n\t" \
  "v_cmp_eq_u32 vcc, %[sent], v207\n\t" \
  "s_or_b64 s[94:95], s[94:95], vcc\n\t" \
  "s_cmp_eq_u64 s[94:95], 0\n\t" \
  "s_cbranch_scc1 .Lok%=\n\t" \
  "v_mov_b64 v[192:193], 0\n\t" \
  "v_mov_b64 v[194:195], 0\n\t" \
  "v_mov_b64 v[196:197], 0\n\t" \
  "v_mov_b64 v[198:199], 0\n\t" \
  "s_add_u32 s92, s92, 1\n\t" \
  "s_cmp_eq_u32 s92, 1\n\t" \
  "s_cbranch_scc1 .Lpoll%=\n\t" \
  "s_sleep 6\n\t" \
  "s_and_b32 s93, s92, 63\n\t" \
  "s_cmp_lg_u32 s93, 0\n\t" \
  "s_cbranch_scc1 .Lpoll%=\n\t" \
  "v_mov_b32 v245, 0\n\t" \
  "buffer_load_dword v244, v245, %[rs], %[sab] offen sc1\n\t" \
  "s_waitcnt vmcnt(0)\n\t" \
  "v_readfirstlane_b32 s93, v244\n\t" \
  "s_nop 3\n\t" \
  "s_cmp_eq_u32 s93, 1\n\t" \
  "s_cbranch_scc1 .Lok%=\n\t" \
  "s_cmp_lt_u32 s92, 0x80000\n\t" \
  "s_cbranch_scc1 .Lpoll%=\n\t" \
  "v_mov_b32 v244, 1\n\t" \
  "buffer_store_dword v244, v245, %[rs], %[sab] offen sc1\n\t" \
  "s_branch .Lok%=\n\t" \
  ".Lend%=:\n\t"
#define QK_ASM_6 \
  "s_waitcnt lgkmcnt(0)\n\t" \
  "v_subrev_u32 v244, 0x8000, v250\n\t" \
  "ds_read_b128 v[216:219], v248 offset:57344\n\t" \
  "ds_read_b128 v[220:223], v248 offset:58368\n\t" \
  "ds_read_b128 v[224:227], v248 offset:59392\n\t" \
  "ds_read_b128 v[228:231], v248 offset:60416\n\t" \
  "ds_read_b128 v[208:211], v244 offset:0\n\t" \
  "ds_read_b128 v[212:215], v244 offset:1024\n\t" \
  "ds_read_b128 v[236:239], v244 offset:2048\n\t" \
  "ds_read_b128 v[240:243], v244 offset:3072\n\t" \
  "v_mov_b64 v[192:193], 0\n\t" \
  "v_mov_b64 v[194:195], 0\n\t" \
  "v_mov_b64 v[196:197], 0\n\t" \
  "v_mov_b64 v[198:199], 0\n\t" \
  "s_movk_i32 s92, 0x2000\n\t" \
  ".Lwt%=:\n\t" \
  "ds_read_b64 v[200:201], v248 offset:30720\n\t" \
  "s_memrealtime s[90:91]\n\t" \
  "s_sub_u32 s92, s92, 1\n\t" \
  "s_waitcnt lgkmcnt(0)\n\t" \
  "v_readfirstlane_b32 s93, v200\n\t" \
  "v_readfirstlane_b32 %[tgt], v201\n\t" \
  "s_cbranch_scc1 .Lgo%=\n\t" \
  "s_cmp_eq_u32 s93, %[step]\n\t" \
  "s_cbranch_scc1 .Lwx%=\n\t" \
  "s_sleep 1\n\t" \
  "s_branch .Lwt%=\n\t" \
  ".Lwx%=:\n\t" \
  "s_lshl_b32 s93, s90, 2\n\t" \
  "s_sub_i32 s93, %[tgt], s93\n\t" \
  "s_cmp_le_i32 s93, 0\n\t" \
  "s_cbranch_scc1 .Lgo%=\n\t" \
  "s_min_i32 s93, s93, 8000\n\t" \
  "s_add_u32 s93, s93, 3\n\t" \
  "s_lshr_b32 s93, s93, 2\n\t" \
  "s_add_u32 s93, s93, s90\n\t" \
  "s_memrealtime s[90:91]\n\t" \
  ".Lpace%=:\n\t" \
  "s_memrealtime s[94:95]\n\t" \
  "s_waitcnt lgkmcnt(1)\n\t" \
  "s_sub_i32 s91, s90, s93\n\t" \
  "s_cmp_ge_i32 s91, 0\n\t" \
  "s_cbranch_scc1 .Lgo%=\n\t" \
  "s_memrealtime s[90:91]\n\t" \
  "s_waitcnt lgkmcnt(1)\n\t" \
  "s_sub_i32 s95, s94, s93\n\t" \
  "s_cmp_lt_i32 s95, 0\n\t" \
  "s_cbranch_scc1 .Lpace%=\n\t" \
  ".Lgo%=:\n\t" \
  "s_mov_b32 s92, 0\n\t" \
  ".Lpoll%=:\n\t" \
  "buffer_load_dwordx4 v[200:203], v248, %[rs], %[som] offen sc1\n\t" \
  "buffer_load_dwordx4 v[204:207], v248, %[rs], %[som2] offen sc1\n\t" \
  "s_waitcnt vmcnt(1) lgkmcnt(0)\n\t" \
  "ds_write_b128 v249, v[200:203]\n\t" \
  "v_smfmac_f32_16x16x64_bf16 v[192:195], v[200:203], v[208:215], v251\n\t" \
  "v_max3_u32 v244, v200, v201, v202\n\t" \
  "v_smfmac_f32_16x16x64_bf16 v[196:199], v[200:203], v[236:243], v251\n\t" \
  "s_waitcnt vmcnt(0)\n\t" \
  "ds_write_b128 v249, v[204:207] offset:8192\n\t" \
  "v_smfmac_f32_16x16x64_bf16 v[192:195], v[204:207], v[216:223], v251\n\t" \
  "v_max3_u32 v244, v244, v203, v204\n\t" \
  "v_max3_u32 v244, v244, v205, v206\n\t" \
  "v_smfmac_f32_16x16x64_bf16 v[196:199], v[204:207], v[224:231], v251\n\t" \
  "v_max_u32 v244, v244, v207\n\t" \
  "v_cmp_le_u32 vcc, %[sent], v244\n\t" \
  "s_cbranch_vccnz .Lslow%=\n\t" \
  ".Lok%=:\n\t" \
  "ds_read_b128 v[216:219], v250 offset:32768\n\t" \
  "ds_read_b128 v[220:223], v250 offset:33792\n\t" \
  "ds_read_b128 v[224:227], v250 offset:34816\n\t" \
  "ds_read_b128 v[228:231], v250 offset:35840\n\t" \
  "s_waitcnt lgkmcnt(4)\n\t" \
  "s_barrier\n\t" \
  "ds_read_b128 v[208:211], v248 offset:7168\n\t" \
  "ds_read_b128 v[212:215], v248 offset:8192\n\t" \
  "ds_read_b128 v[236:239], v248 offset:9216\n\t" \
  "ds_read_b128 v[240:243], v248 offset:10240\n\t" \
  "s_waitcnt lgkmcnt(3)\n\t" \
  "ds_read_b128 v[244:247], v248 offset:13312\n\t" \
  "v_smfmac_f32_16x16x64_bf16 v[192:195], v[208:211], v[216:223], v251\n\t" \
  "ds_read_b128 v[216:219], v250 offset:0\n\t" \
  "v_smfmac_f32_16x16x64_bf16 v[196:199], v[208:211], v[224:231], v251\n\t" \
  "s_waitcnt lgkmcnt(4)\n\t" \
  "ds_read_b128 v[220:223], v250 offset:1024\n\t" \
  "v_smfmac_f32_16x16x64_bf16 v[192:195], v[212:215], v[0:7], v251\n\t" \
  "ds_read_b128 v[224:227], v250 offset:2048\n\t" \
  "v_smfmac_f32_16x16x64_bf16 v[196:199], v[212:215], v[8:15], v251\n\t" \
  "s_waitcnt lgkmcnt(5)\n\t" \
  "ds_read_b128 v[228:231], v250 offset:3072\n\t" \
  "v_smfmac_f32_16x16x64_bf16 v[192:195], v[236:239], v[16:23], v251\n\t" \
  "ds_read_b128 v[200:203], v248 offset:11264\n\t" \
  "v_smfmac_f32_16x16x64_bf16 v[196:199], v[236:239], v[24:31], v251\n\t" \
  "s_waitcnt lgkmcnt(6)\n\t" \
  "ds_read_b128 v[204:207], v248 offset:12288\n\t" \
  "v_smfmac_f32_16x16x64_bf16 v[192:195], v[240:243], v[32:39], v251\n\t" \
  "ds_read_b128 v[208:211], v248 offset:15360\n\t" \
  "v_smfmac_f32_16x16x64_bf16 v[196:199], v[240:243], v[40:47], v251\n\t" \
  "s_waitcnt lgkmcnt(5)\n\t" \
  "ds_read_b128 v[212:215], v248 offset:0\n\t" \
  "v_smfmac_f32_16x16x64_bf16 v[192:195], v[244:247], v[216:223], v251\n\t" \
  "s_waitcnt lgkmcnt(4)\n\t" \
  "v_smfmac_f32_16x16x64_bf16 v[196:199], v[244:247], v[224:231], v251\n\t" \
  "s_waitcnt lgkmcnt(3)\n\t" \
  "ds_read_b128 v[236:239], v248 offset:1024\n\t" \
  "v_smfmac_f32_16x16x64_bf16 v[192:195], v[200:203], v[48:55], v251\n\t" \
  "ds_read_b128 v[240:243], v248 offset:2048\n\t" \
  "v_smfmac_f32_16x16x64_bf16 v[196:199], v[200:203], v[56:63], v251\n\t" \
  "s_waitcnt lgkmcnt(4)\n\t" \
  "v_smfmac_f32_16x16x64_bf16 v[192:195], v[204:207], v[64:71], v251\n\t" \
  "ds_read_b128 v[244:247], v248 offset:3072\n\t" \
  "v_smfmac_f32_16x16x64_bf16 v[196:199], v[204:207], v[72:79], v251\n\t" \
  "s_waitcnt lgkmcnt(4)\n\t" \
  "v_smfmac_f32_16x16x64_bf16 v[192:195], v[208:211], v[80:87], v251\n\t" \
  "ds_read_b128 v[200:203], v248 offset:4096\n\t" \
  "v_smfmac_f32_16x16x64_bf16 v[196:199], v[208:211], v[88:95], v251\n\t" \
  "s_waitcnt lgkmcnt(4)\n\t" \
  "v_smfmac_f32_16x16x64_bf16 v[192:195], v[212:215], v[96:103], v251\n\t" \
  "ds_read_b128 v[204:207], v248 offset:5120\n\t" \
  "v_smfmac_f32_16x16x64_bf16 v[196:199], v[212:215], v[104:111], v251\n\t" \
  "s_waitcnt lgkmcnt(3)\n\t" \
  "v_smfmac_f32_16x16x64_bf16 v[192:195], v[236:239], v[112:119], v251\n\t" \
  "v_smfmac_f32_16x16x64_bf16 v[196:199], v[236:239], v[120:127], v251\n\t" \
  "v_smfmac_f32_16x16x64_bf16 v[192:195], v[240:243], v[128:135], v251\n\t" \
  "v_smfmac_f32_16x16x64_bf16 v[196:199], v[240:243], v[136:143], v251\n\t" \
  "s_waitcnt lgkmcnt(1)\n\t" \
  "v_smfmac_f32_16x16x64_bf16 v[192:195], v[244:247], v[144:151], v251\n\t" \
  "v_smfmac_f32_16x16x64_bf16 v[196:199], v[244:247], v[152:159], v251\n\t" \
  "v_smfmac_f32_16x16x64_bf16 v[192:195], v[200:203], v[160:167], v251\n\t" \
  "v_smfmac_f32_16x16x64_bf16 v[196:199], v[200:203], v[168:175], v251\n\t" \
  "s_waitcnt lgkmcnt(0)\n\t" \
  "v_smfmac_f32_16x16x64_bf16 v[192:195], v[204:207], v[176:183], v251\n\t" \
  "v_smfmac_f32_16x16x64_bf16 v[196:199], v[204:207], v[184:191], v251\n\t" \
  "s_nop 6\n\t" \
  "v_add_f32 v192, v192, v194\n\t" \
  "v_add_f32 v193, v193, v195\n\t" \
  "v_add_f32 v196, v196, v198\n\t" \
  "v_add_f32 v197, v197, v199\n\t" \
  "v_cvt_pk_bf16_f32 v192, v192, v193\n\t" \
  "v_cvt_pk_bf16_f32 v196, v196, v197\n\t" \
  "ds_write2_b32 v252, v192, v196 offset1:64\n\t" \
  "s_waitcnt lgkmcnt(0)\n\t" \
  "s_barrier\n\t" \
  "buffer_store_dword v192, v235, %[rs], %[sk] offen sc1\n\t" \
  "buffer_store_dword v196, v235, %[rs], %[sk] offen offset:256 sc1\n\t" \
  "s_mov_b32 %[spins], s92\n\t" \
  "s_branch .Lend%=\n\t" \
  ".Lslow%=:\n\t" \
  "s_mov_b64 s[94:95], 0\n\t" \
  "v_cmp_eq_u32 vcc, %[sent], v200\n\t" \
  "s_or_b64 s[94:95], s[94:95], vcc\n\t" \
  "v_cmp_eq_u32 vcc, %[sent], v201\n\t" \
  "s_or_b64 s[94:95], s[94:95], vcc\n\t" \
  "v_cmp_eq_u32 vcc, %[sent], v202\n\t" \
  "s_or_b64 s[94:95], s[94:95], vcc\n\t" \
  "v_cmp_eq_u32 vcc, %[sent], v203\n\t" \
  "s_or_b64 s[94:95], s[94:95], vcc\n\t" \
  "v_cmp_eq_u32 vcc, %[sent], v204\n\t" \
  "s_or_b64 s[94:95], s[94:95], vcc\n\t" \
  "v_cmp_eq_u32 vcc, %[sent], v205\n\t" \
  "s_or_b64 s[94:95], s[94:95], vcc\n\t" \
  "v_cmp_eq_u32 vcc, %[sent], v206\n\t" \
  "s_or_b64 s[94:95], s[94:95], vcc\n\t" \
  "v_cmp_eq_u32 vcc, %[sent], v207\n\t" \
  "s_or_b64 s[94:95], s[94:95], vcc\n\t" \
  "s_cmp_eq_u64 s[94:95], 0\n\t" \
  "s_cbranch_scc1 .Lok%=\n\t" \
  "v_mov_b64 v[192:193], 0\n\t" \
  "v_mov_b64 v[194:195], 0\n\t" \
  "v_mov_b64 v[196:197], 0\n\t" \
  "v_mov_b64 v[198:199], 0\n\t" \
  "s_add_u32 s92, s92, 1\n\t" \
  "s_cmp_eq_u32 s92, 1\n\t" \
  "s_cbranch_scc1 .Lpoll%=\n\t" \
  "s_sleep 6\n\t" \
  "s_and_b32 s93, s92, 63\n\t" \
  "s_cmp_lg_u32 s93, 0\n\t" \
  "s_cbranch_scc1 .Lpoll%=\n\t" \
  "v_mov_b32 v245, 0\n\t" \
  "buffer_load_dword v244, v245, %[rs], %[sab] offen sc1\n\t" \
  "s_waitcnt vmcnt(0)\n\t" \
  "v_readfirstlane_b32 s93, v244\n\t" \
  "s_nop 3\n\t" \
  "s_cmp_eq_u32 s93, 1\n\t" \
  "s_cbranch_scc1 .Lok%=\n\t" \
  "s_cmp_lt_u32 s92, 0x80000\n\t" \
  "s_cbranch_scc1 .Lpoll%=\n\t" \
  "v_mov_b32 v244, 1\n\t" \
  "buffer_store_dword v244, v245, %[rs], %[sab] offen sc1\n\t" \
  "s_branch .Lok%=\n\t" \
  ".Lend%=:\n\t"
#define QK_ASM_7 \
  "s_waitcnt lgkmcnt(0)\n\t" \
  "v_subrev_u32 v244, 0x8000, v250\n\t" \
  "ds_read_b128 v[216:219], v248 offset:61440\n\t" \
  "ds_read_b128 v[220:223], v248 offset:62464\n\t" \
  "ds_read_b128 v[224:227], v248 offset:63488\n\t" \
  "ds_read_b128 v[228:231], v248 offset:64512\n\t" \
  "ds_read_b128 v[208:211], v244 offset:0\n\t" \
  "ds_read_b128 v[212:215], v244 offset:1024\n\t" \
  "ds_read_b128 v[236:239], v244 offset:2048\n\t" \
  "ds_read_b128 v[240:243], v244 offset:3072\n\t" \
  "v_mov_b64 v[192:193], 0\n\t" \
  "v_mov_b64 v[194:195], 0\n\t" \
  "v_mov_b64 v[196:197], 0\n\t" \
  "v_mov_b64 v[198:199], 0\n\t" \
  "s_movk_i32 s92, 0x2000\n\t" \
  ".Lwt%=:\n\t" \
  "ds_read_b64 v[200:201], v248 offset:30720\n\t" \
  "s_memrealtime s[90:91]\n\t" \
  "s_sub_u32 s92, s92, 1\n\t" \
  "s_waitcnt lgkmcnt(0)\n\t" \
  "v_readfirstlane_b32 s93, v200\n\t" \
  "v_readfirstlane_b32 %[tgt], v201\n\t" \
  "s_cbranch_scc1 .Lgo%=\n\t" \
  "s_cmp_eq_u32 s93, %[step]\n\t" \
  "s_cbranch_scc1 .Lwx%=\n\t" \
  "s_sleep 1\n\t" \
  "s_branch .Lwt%=\n\t" \
  ".Lwx%=:\n\t" \
  "s_lshl_b32 s93, s90, 2\n\t" \
  "s_sub_i32 s93, %[tgt], s93\n\t" \
  "s_cmp_le_i32 s93, 0\n\t" \
  "s_cbranch_scc1 .Lgo%=\n\t" \
  "s_min_i32 s93, s93, 8000\n\t" \
  "s_add_u32 s93, s93, 3\n\t" \
  "s_lshr_b32 s93, s93, 2\n\t" \
  "s_add_u32 s93, s93, s90\n\t" \
  "s_memrealtime s[90:91]\n\t" \
  ".Lpace%=:\n\t" \
  "s_memrealtime s[94:95]\n\t" \
  "s_waitcnt lgkmcnt(1)\n\t" \
  "s_sub_i32 s91, s90, s93\n\t" \
  "s_cmp_ge_i32 s91, 0\n\t" \
  "s_cbranch_scc1 .Lgo%=\n\t" \
  "s_memrealtime s[90:91]\n\t" \
  "s_waitcnt lgkmcnt(1)\n\t" \
  "s_sub_i32 s95, s94, s93\n\t" \
  "s_cmp_lt_i32 s95, 0\n\t" \
  "s_cbranch_scc1 .Lpace%=\n\t" \
  ".Lgo%=:\n\t" \
  "s_mov_b32 s92, 0\n\t" \
  ".Lpoll%=:\n\t" \
  "buffer_load_dwordx4 v[200:203], v248, %[rs], %[som] offen sc1\n\t" \
  "buffer_load_dwordx4 v[204:207], v248, %[rs], %[som2] offen sc1\n\t" \
  "s_waitcnt vmcnt(1) lgkmcnt(0)\n\t" \
  "ds_write_b128 v249, v[200:203]\n\t" \
  "v_smfmac_f32_16x16x64_bf16 v[192:195], v[200:203], v[208:215], v251\n\t" \
  "v_max3_u32 v244, v200, v201, v202\n\t" \
  "v_smfmac_f32_16x16x64_bf16 v[196:199], v[200:203], v[236:243], v251\n\t" \
  "s_waitcnt vmcnt(0)\n\t" \
  "ds_write_b128 v249, v[204:207] offset:8192\n\t" \
  "v_smfmac_f32_16x16x64_bf16 v[192:195], v[204:207], v[216:223], v251\n\t" \
  "v_max3_u32 v244, v244, v203, v204\n\t" \
  "v_max3_u32 v244, v244, v205, v206\n\t" \
  "v_smfmac_f32_16x16x64_bf16 v[196:199], v[204:207], v[224:231], v251\n\t" \
  "v_max_u32 v244, v244, v207\n\t" \
  "v_cmp_le_u32 vcc, %[sent], v244\n\t" \
  "s_cbranch_vccnz .Lslow%=\n\t" \
  ".Lok%=:\n\t" \
  "ds_read_b128 v[216:219], v250 offset:32768\n\t" \
  "ds_read_b128 v[220:223], v250 offset:33792\n\t" \
  "ds_read_b128 v[224:227], v250 offset:34816\n\t" \
  "ds_read_b128 v[228:231], v250 offset:35840\n\t" \
  "s_waitcnt lgkmcnt(4)\n\t" \
  "s_setprio 3\n\t" \
  "s_barrier\n\t" \
  "s_memrealtime s[90:91]\n\t" \
  "v_mov_b32 v232, %[step]\n\t" \
  "v_lshrrev_b32 v234, 1, v248\n\t" \
  "s_mov_b64 exec, 0xffff\n\t" \
  "s_waitcnt lgkmcnt(0)\n\t" \
  "s_lshl_b32 s90, s90, 2\n\t" \
  "s_sub_i32 s93, s90, %[tgt]\n\t" \
  "s_cmp_gt_i32 s93, 240\n\t" \
  "s_cselect_b32 s93, 60, 0\n\t" \
  "s_add_u32 s93, s93, s90\n\t" \
  "s_add_u32 s93, s93, %[lead]\n\t" \
  "v_mov_b32 v233, s93\n\t" \
  "buffer_store_dwordx2 v[232:233], v234, %[rs], %[sbc] offen sc1\n\t" \
  "s_mov_b64 exec, -1\n\t" \
  "s_mov_b32 %[tb1], s90\n\t" \
  "s_setprio 0\n\t" \
  "ds_read_b128 v[208:211], v248 offset:6144\n\t" \
  "ds_read_b128 v[212:215], v248 offset:9216\n\t" \
  "ds_read_b128 v[236:239], v248 offset:10240\n\t" \
  "ds_read_b128 v[240:243], v248 offset:11264\n\t" \
  "s_waitcnt lgkmcnt(3)\n\t" \
  "ds_read_b128 v[244:247], v248 offset:12288\n\t" \
  "v_smfmac_f32_16x16x64_bf16 v[192:195], v[208:211], v[216:223], v251\n\t" \
  "ds_read_b128 v[216:219], v250 offset:0\n\t" \
  "v_smfmac_f32_16x16x64_bf16 v[196:199], v[208:211], v[224:231], v251\n\t" \
  "s_waitcnt lgkmcnt(4)\n\t" \
  "ds_read_b128 v[220:223], v250 offset:1024\n\t" \
  "v_smfmac_f32_16x16x64_bf16 v[192:195], v[212:215], v[0:7], v251\n\t" \
  "ds_read_b128 v[224:227], v250 offset:2048\n\t" \
  "v_smfmac_f32_16x16x64_bf16 v[196:199], v[212:215], v[8:15], v251\n\t" \
  "s_waitcnt lgkmcnt(5)\n\t" \
  "ds_read_b128 v[228:231], v250 offset:3072\n\t" \
  "v_smfmac_f32_16x16x64_bf16 v[192:195], v[236:239], v[16:23], v251\n\t" \
  "ds_read_b128 v[200:203], v248 offset:13312\n\t" \
  "v_smfmac_f32_16x16x64_bf16 v[196:199], v[236:239], v[24:31], v251\n\t" \
  "s_waitcnt lgkmcnt(6)\n\t" \
  "ds_read_b128 v[204:207], v248 offset:14336\n\t" \
  "v_smfmac_f32_16x16x64_bf16 v[192:195], v[240:243], v[32:39], v251\n\t" \
  "ds_read_b128 v[208:211], v248 offset:0\n\t" \
  "v_smfmac_f32_16x16x64_bf16 v[196:199], v[240:243], v[40:47], v251\n\t" \
  "s_waitcnt lgkmcnt(5)\n\t" \
  "ds_read_b128 v[212:215], v248 offset:1024\n\t" \
  "v_smfmac_f32_16x16x64_bf16 v[192:195], v[244:247], v[216:223], v251\n\t" \
  "s_waitcnt lgkmcnt(4)\n\t" \
  "v_smfmac_f32_16x16x64_bf16 v[196:199], v[244:247], v[224:231], v251\n\t" \
  "s_waitcnt lgkmcnt(3)\n\t" \
  "ds_read_b128 v[236:239], v248 offset:2048\n\t" \
  "v_smfmac_f32_16x16x64_bf16 v[192:195], v[200:203], v[48:55], v251\n\t" \
  "ds_read_b128 v[240:243], v248 offset:3072\n\t" \
  "v_smfmac_f32_16x16x64_bf16 v[196:199], v[200:203], v[56:63], v251\n\t" \
  "s_waitcnt lgkmcnt(4)\n\t" \
  "v_smfmac_f32_16x16x64_bf16 v[192:195], v[204:207], v[64:71], v251\n\t" \
  "ds_read_b128 v[244:247], v248 offset:4096\n\t" \
  "v_smfmac_f32_16x16x64_bf16 v[196:199], v[204:207], v[72:79], v251\n\t" \
  "s_waitcnt lgkmcnt(4)\n\t" \
  "v_smfmac_f32_16x16x64_bf16 v[192:195], v[208:211], v[80:87], v251\n\t" \
  "ds_read_b128 v[200:203], v248 offset:5120\n\t" \
  "v_smfmac_f32_16x16x64_bf16 v[196:199], v[208:211], v[88:95], v251\n\t" \
  "s_waitcnt lgkmcnt(4)\n\t" \
  "v_smfmac_f32_16x16x64_bf16 v[192:195], v[212:215], v[96:103], v251\n\t" \
  "ds_read_b128 v[204:207], v248 offset:8192\n\t" \
  "v_smfmac_f32_16x16x64_bf16 v[196:199], v[212:215], v[104:111], v251\n\t" \
  "s_waitcnt lgkmcnt(3)\n\t" \
  "v_smfmac_f32_16x16x64_bf16 v[192:195], v[236:239], v[112:119], v251\n\t" \
  "v_smfmac_f32_16x16x64_bf16 v[196:199], v[236:239], v[120:127], v251\n\t" \
  "v_smfmac_f32_16x16x64_bf16 v[192:195], v[240:243], v[128:135], v251\n\t" \
  "v_smfmac_f32_16x16x64_bf16 v[196:199], v[240:243], v[136:143], v251\n\t" \
  "s_waitcnt lgkmcnt(1)\n\t" \
  "v_smfmac_f32_16x16x64_bf16 v[192:195], v[244:247], v[144:151], v251\n\t" \
  "v_smfmac_f32_16x16x64_bf16 v[196:199], v[244:247], v[152:159], v251\n\t" \
  "v_smfmac_f32_16x16x64_bf16 v[192:195], v[200:203], v[160:167], v251\n\t" \
  "v_smfmac_f32_16x16x64_bf16 v[196:199], v[200:203], v[168:175], v251\n\t" \
  "s_waitcnt lgkmcnt(0)\n\t" \
  "v_smfmac_f32_16x16x64_bf16 v[192:195], v[204:207], v[176:183], v251\n\t" \
  "v_smfmac_f32_16x16x64_bf16 v[196:199], v[204:207], v[184:191], v251\n\t" \
  "s_nop 6\n\t" \
  "v_add_f32 v192, v192, v194\n\t" \
  "v_add_f32 v193, v193, v195\n\t" \
  "v_add_f32 v196, v196, v198\n\t" \
  "v_add_f32 v197, v197, v199\n\t" \
  "v_cvt_pk_bf16_f32 v192, v192, v193\n\t" \
  "v_cvt_pk_bf16_f32 v196, v196, v197\n\t" \
  "ds_write2_b32 v252, v192, v196 offset1:64\n\t" \
  "s_waitcnt lgkmcnt(0)\n\t" \
  "s_barrier\n\t" \
  "buffer_store_dword v192, v235, %[rs], %[sk] offen sc1\n\t" \
  "buffer_store_dword v196, v235, %[rs], %[sk] offen offset:256 sc1\n\t" \
  "s_mov_b32 %[spins], s92\n\t" \
  "s_branch .Lend%=\n\t" \
  ".Lslow%=:\n\t" \
  "s_mov_b64 s[94:95], 0\n\t" \
  "v_cmp_eq_u32 vcc, %[sent], v200\n\t" \
  "s_or_b64 s[94:95], s[94:95], vcc\n\t" \
  "v_cmp_eq_u32 vcc, %[sent], v201\n\t" \
  "s_or_b64 s[94:95], s[94:95], vcc\n\t" \
  "v_cmp_eq_u32 vcc, %[sent], v202\n\t" \
  "s_or_b64 s[94:95], s[94:95], vcc\n\t" \
  "v_cmp_eq_u32 vcc, %[sent], v203\n\t" \
  "s_or_b64 s[94:95], s[94:95], vcc\n\t" \
  "v_cmp_eq_u32 vcc, %[sent], v204\n\t" \
  "s_or_b64 s[94:95], s[94:95], vcc\n\t" \
  "v_cmp_eq_u32 vcc, %[sent], v205\n\t" \
  "s_or_b64 s[94:95], s[94:95], vcc\n\t" \
  "v_cmp_eq_u32 vcc, %[sent], v206\n\t" \
  "s_or_b64 s[94:95], s[94:95], vcc\n\t" \
  "v_cmp_eq_u32 vcc, %[sent], v207\n\t" \
  "s_or_b64 s[94:95], s[94:95], vcc\n\t" \
  "s_cmp_eq_u64 s[94:95], 0\n\t" \
  "s_cbranch_scc1 .Lok%=\n\t" \
  "v_mov_b64 v[192:193], 0\n\t" \
  "v_mov_b64 v[194:195], 0\n\t" \
  "v_mov_b64 v[196:197], 0\n\t" \
  "v_mov_b64 v[198:199], 0\n\t" \
  "s_add_u32 s92, s92, 1\n\t" \
  "s_cmp_eq_u32 s92, 1\n\t" \
  "s_cbranch_scc1 .Lpoll%=\n\t" \
  "s_sleep 6\n\t" \
  "s_and_b32 s93, s92, 63\n\t" \
  "s_cmp_lg_u32 s93, 0\n\t" \
  "s_cbranch_scc1 .Lpoll%=\n\t" \
  "v_mov_b32 v245, 0\n\t" \
  "buffer_load_dword v244, v245, %[rs], %[sab] offen sc1\n\t" \
  "s_waitcnt vmcnt(0)\n\t" \
  "v_readfirstlane_b32 s93, v244\n\t" \
  "s_nop 3\n\t" \
  "s_cmp_eq_u32 s93, 1\n\t" \
  "s_cbranch_scc1 .Lok%=\n\t" \
  "s_cmp_lt_u32 s92, 0x80000\n\t" \
  "s_cbranch_scc1 .Lpoll%=\n\t" \
  "v_mov_b32 v244, 1\n\t" \
  "buffer_store_dword v244, v245, %[rs], %[sab] offen sc1\n\t" \
  "s_branch .Lok%=\n\t" \
  ".Lend%=:\n\t"
#define VV_ASM \
  "s_waitcnt lgkmcnt(0)\n\t" \
  "v_mov_b64 v[128:129], 0\n\t" \
  "v_mov_b64 v[130:131], 0\n\t" \
  "s_movk_i32 s92, 0x2000\n\t" \
  ".Lwt%=:\n\t" \
  "ds_read_b64 v[188:189], v200 offset:30720\n\t" \
  "s_memrealtime s[90:91]\n\t" \
  "s_sub_u32 s92, s92, 1\n\t" \
  "s_waitcnt lgkmcnt(0)\n\t" \
  "v_readfirstlane_b32 s93, v188\n\t" \
  "v_readfirstlane_b32 %[tgt], v189\n\t" \
  "s_cbranch_scc1 .Lgo%=\n\t" \
  "s_cmp_eq_u32 s93, %[step]\n\t" \
  "s_cbranch_scc1 .Lwx%=\n\t" \
  "s_sleep 1\n\t" \
  "s_branch .Lwt%=\n\t" \
  ".Lwx%=:\n\t" \
  "s_lshl_b32 s93, s90, 2\n\t" \
  "s_sub_i32 s93, %[tgt], s93\n\t" \
  "s_cmp_le_i32 s93, 0\n\t" \
  "s_cbranch_scc1 .Lgo%=\n\t" \
  "s_min_i32 s93, s93, 8000\n\t" \
  "s_add_u32 s93, s93, 3\n\t" \
  "s_lshr_b32 s93, s93, 2\n\t" \
  "s_add_u32 s93, s93, s90\n\t" \
  "s_memrealtime s[90:91]\n\t" \
  ".Lpace%=:\n\t" \
  "s_memrealtime s[94:95]\n\t" \
  "s_waitcnt lgkmcnt(1)\n\t" \
  "s_sub_i32 s91, s90, s93\n\t" \
  "s_cmp_ge_i32 s91, 0\n\t" \
  "s_cbranch_scc1 .Lgo%=\n\t" \
  "s_memrealtime s[90:91]\n\t" \
  "s_waitcnt lgkmcnt(1)\n\t" \
  "s_sub_i32 s95, s94, s93\n\t" \
  "s_cmp_lt_i32 s95, 0\n\t" \
  "s_cbranch_scc1 .Lpace%=\n\t" \
  ".Lgo%=:\n\t" \
  "s_mov_b32 s92, 0\n\t" \
  ".Lpoll%=:\n\t" \
  "buffer_load_dwordx4 v[188:191], v200, %[rs], %[som] offen sc1\n\t" \
  "buffer_load_dwordx4 v[192:195], v200, %[rs], %[som] offen offset:1024 sc1\n\t" \
  "s_waitcnt vmcnt(1) lgkmcnt(0)\n\t" \
  "ds_write_b128 v201, v[188:191]\n\t" \
  "v_max3_u32 v196, v188, v189, v190\n\t" \
  "s_waitcnt vmcnt(0)\n\t" \
  "ds_write_b128 v201, v[192:195] offset:1024\n\t" \
  "v_max3_u32 v196, v196, v191, v192\n\t" \
  "v_max3_u32 v196, v196, v193, v194\n\t" \
  "v_max_u32 v196, v196, v195\n\t" \
  "v_cmp_le_u32 vcc, %[sent], v196\n\t" \
  "s_cbranch_vccnz .Lslow%=\n\t" \
  ".Lok%=:\n\t" \
  "s_waitcnt lgkmcnt(0)\n\t" \
  "s_barrier\n\t" \
  "ds_read_b128 v[132:135], v200 offset:0\n\t" \
  "ds_read_b128 v[136:139], v200 offset:1024\n\t" \
  "ds_read_b128 v[140:143], v200 offset:2048\n\t" \
  "ds_read_b128 v[144:147], v200 offset:3072\n\t" \
  "ds_read_b128 v[148:151], v200 offset:4096\n\t" \
  "s_waitcnt lgkmcnt(4)\n\t" \
  "ds_read_b128 v[152:155], v200 offset:5120\n\t" \
  "v_smfmac_f32_16x16x64_bf16 v[128:131], v[132:135], v[0:7], v202\n\t" \
  "s_waitcnt lgkmcnt(4)\n\t" \
  "ds_read_b128 v[156:159], v200 offset:6144\n\t" \
  "v_smfmac_f32_16x16x64_bf16 v[128:131], v[136:139], v[8:15], v202\n\t" \
  "s_waitcnt lgkmcnt(4)\n\t" \
  "ds_read_b128 v[160:163], v200 offset:7168\n\t" \
  "v_smfmac_f32_16x16x64_bf16 v[128:131], v[140:143], v[16:23], v202\n\t" \
  "s_waitcnt lgkmcnt(4)\n\t" \
  "ds_read_b128 v[164:167], v200 offset:8192\n\t" \
  "v_smfmac_f32_16x16x64_bf16 v[128:131], v[144:147], v[24:31], v202\n\t" \
  "s_waitcnt lgkmcnt(4)\n\t" \
  "ds_read_b128 v[168:171], v200 offset:9216\n\t" \
  "v_smfmac_f32_16x16x64_bf16 v[128:131], v[148:151], v[32:39], v202\n\t" \
  "s_waitcnt lgkmcnt(4)\n\t" \
  "ds_read_b128 v[172:175], v200 offset:10240\n\t" \
  "v_smfmac_f32_16x16x64_bf16 v[128:131], v[152:155], v[40:47], v202\n\t" \
  "s_waitcnt lgkmcnt(4)\n\t" \
  "ds_read_b128 v[176:179], v200 offset:11264\n\t" \
  "v_smfmac_f32_16x16x64_bf16 v[128:131], v[156:159], v[48:55], v202\n\t" \
  "s_waitcnt lgkmcnt(4)\n\t" \
  "ds_read_b128 v[180:183], v200 offset:12288\n\t" \
  "v_smfmac_f32_16x16x64_bf16 v[128:131], v[160:163], v[56:63], v202\n\t" \
  "s_waitcnt lgkmcnt(4)\n\t" \
  "ds_read_b128 v[184:187], v200 offset:13312\n\t" \
  "v_smfmac_f32_16x16x64_bf16 v[128:131], v[164:167], v[64:71], v202\n\t" \
  "s_waitcnt lgkmcnt(4)\n\t" \
  "ds_read_b128 v[188:191], v200 offset:14336\n\t" \
  "v_smfmac_f32_16x16x64_bf16 v[128:131], v[168:171], v[72:79], v202\n\t" \
  "s_waitcnt lgkmcnt(4)\n\t" \
  "ds_read_b128 v[192:195], v200 offset:15360\n\t" \
  "v_smfmac_f32_16x16x64_bf16 v[128:131], v[172:175], v[80:87], v202\n\t" \
  "s_waitcnt lgkmcnt(4)\n\t" \
  "v_smfmac_f32_16x16x64_bf16 v[128:131], v[176:179], v[88:95], v202\n\t" \
  "s_waitcnt lgkmcnt(3)\n\t" \
  "v_smfmac_f32_16x16x64_bf16 v[128:131], v[180:183], v[96:103], v202\n\t" \
  "s_waitcnt lgkmcnt(2)\n\t" \
  "v_smfmac_f32_16x16x64_bf16 v[128:131], v[184:187], v[104:111], v202\n\t" \
  "s_waitcnt lgkmcnt(1)\n\t" \
  "v_smfmac_f32_16x16x64_bf16 v[128:131], v[188:191], v[112:119], v202\n\t" \
  "s_waitcnt lgkmcnt(0)\n\t" \
  "v_smfmac_f32_16x16x64_bf16 v[128:131], v[192:195], v[120:127], v202\n\t" \
  "s_nop 7\n\t" \
  "v_add_f32 v128, v128, v130\n\t" \
  "v_add_f32 v129, v129, v131\n\t" \
  "v_cvt_pk_bf16_f32 v128, v128, v129\n\t" \
  "buffer_store_dword v128, v203, %[rs], %[sv] offen sc1\n\t" \
  "ds_write_b32 v204, v128\n\t" \
  "s_waitcnt lgkmcnt(0)\n\t" \
  "s_barrier\n\t" \
  "s_mov_b32 %[spins], s92\n\t" \
  "s_branch .Lend%=\n\t" \
  ".Lslow%=:\n\t" \
  "s_mov_b64 s[94:95], 0\n\t" \
  "v_cmp_eq_u32 vcc, %[sent], v188\n\t" \
  "s_or_b64 s[94:95], s[94:95], vcc\n\t" \
  "v_cmp_eq_u32 vcc, %[sent], v189\n\t" \
  "s_or_b64 s[94:95], s[94:95], vcc\n\t" \
  "v_cmp_eq_u32 vcc, %[sent], v190\n\t" \
  "s_or_b64 s[94:95], s[94:95], vcc\n\t" \
  "v_cmp_eq_u32 vcc, %[sent], v191\n\t" \
  "s_or_b64 s[94:95], s[94:95], vcc\n\t" \
  "v_cmp_eq_u32 vcc, %[sent], v192\n\t" \
  "s_or_b64 s[94:95], s[94:95], vcc\n\t" \
  "v_cmp_eq_u32 vcc, %[sent], v193\n\t" \
  "s_or_b64 s[94:95], s[94:95], vcc\n\t" \
  "v_cmp_eq_u32 vcc, %[sent], v194\n\t" \
  "s_or_b64 s[94:95], s[94:95], vcc\n\t" \
  "v_cmp_eq_u32 vcc, %[sent], v195\n\t" \
  "s_or_b64 s[94:95], s[94:95], vcc\n\t" \
  "s_cmp_eq_u64 s[94:95], 0\n\t" \
  "s_cbranch_scc1 .Lok%=\n\t" \
  "s_add_u32 s92, s92, 1\n\t" \
  "s_cmp_eq_u32 s92, 1\n\t" \
  "s_cbranch_scc1 .Lpoll%=\n\t" \
  "s_sleep 6\n\t" \
  "s_and_b32 s93, s92, 63\n\t" \
  "s_cmp_lg_u32 s93, 0\n\t" \
  "s_cbranch_scc1 .Lpoll%=\n\t" \
  "v_mov_b32 v197, 0\n\t" \
  "buffer_load_dword v196, v197, %[rs], %[sab] offen sc1\n\t" \
  "s_waitcnt vmcnt(0)\n\t" \
  "v_readfirstlane_b32 s93, v196\n\t" \
  "s_nop 3\n\t" \
  "s_cmp_eq_u32 s93, 1\n\t" \
  "s_cbranch_scc1 .Lok%=\n\t" \
  "s_cmp_lt_u32 s92, 0x80000\n\t" \
  "s_cbranch_scc1 .Lpoll%=\n\t" \
  "v_mov_b32 v196, 1\n\t" \
  "buffer_store_dword v196, v197, %[rs], %[sab] offen sc1\n\t" \
  "s_branch .Lok%=\n\t" \
  ".Lend%=:\n\t"
#define RED_ASM \
  "s_mov_b32 %[step], 0\n\t" \
  "s_lshl_b32 %[sh], %[step], 15\n\t" \
  "s_lshr_b32 s93, %[step], 4\n\t" \
  "s_lshr_b32 s93, %[pm], s93\n\t" \
  "s_and_b32 s93, s93, 4\n\t" \
  "s_lshl_b32 s93, s93, 11\n\t" \
  "s_lshl2_add_u32 %[so], %[sh], s93\n\t" \
  "s_sub_i32 s93, %[step], 3\n\t" \
  "s_lshl_b32 %[st], s93, 19\n\t" \
  "s_lshl_b32 s94, s93, 11\n\t" \
  "s_xor_b32 s94, s94, %[s0]\n\t" \
  "s_and_b32 s94, s94, 0x2000\n\t" \
  "s_or_b32 %[st], %[st], s94\n\t" \
  "s_cmp_lt_i32 s93, 0\n\t" \
  "s_cselect_b32 %[st], 0x70000000, %[st]\n\t" \
  "s_add_u32 %[st4], %[st], 4096\n\t" \
  "buffer_load_dwordx2 v[166:167], v173, %[rs], 0 offen sc1\n\t" \
  "v_mov_b64 v[144:145], 0\n\t" \
  "v_mov_b64 v[146:147], 0\n\t" \
  "v_mov_b64 v[148:149], 0\n\t" \
  "v_mov_b64 v[150:151], 0\n\t" \
  "v_mov_b64 v[152:153], 0\n\t" \
  "v_mov_b64 v[154:155], 0\n\t" \
  "v_mov_b64 v[156:157], 0\n\t" \
  "v_mov_b64 v[158:159], 0\n\t" \
  ".Lstep%=:\n\t" \
  "s_setprio 3\n\t" \
  "s_mov_b32 s92, 0\n\t" \
  ".Lcal%=:\n\t" \
  "s_waitcnt vmcnt(0)\n\t" \
  "s_memrealtime s[76:77]\n\t" \
  "v_cmp_ne_u32 vcc, %[step], v166\n\t" \
  "v_readlane_b32 s90, v167, 0\n\t" \
  "v_readlane_b32 s91, v167, 1\n\t" \
  "v_readlane_b32 s94, v167, 2\n\t" \
  "v_readlane_b32 s95, v167, 3\n\t" \
  "s_sub_i32 s90, s90, %[base]\n\t" \
  "s_sub_i32 s91, s91, %[base]\n\t" \
  "s_sub_i32 s94, s94, %[base]\n\t" \
  "s_sub_i32 s95, s95, %[base]\n\t" \
  "s_max_i32 s90, s90, s91\n\t" \
  "s_max_i32 s94, s94, s95\n\t" \
  "s_max_i32 s90, s90, s94\n\t" \
  "s_add_i32 s90, s90, %[ds]\n\t" \
  "s_waitcnt lgkmcnt(0)\n\t" \
  "s_lshl2_add_u32 s76, s76, %[mn]\n\t" \
  "s_sub_i32 s76, s76, %[base]\n\t" \
  "s_max_i32 s94, s90, s76\n\t" \
  "s_add_u32 %[E], s90, %[base]\n\t" \
  "s_and_b32 s93, vcc_lo, 0xf\n\t" \
  "s_cbranch_scc1 .Lbl%=\n\t" \
  ".Lgo%=:\n\t" \
  "v_mov_b32 v166, 7\n\t" \
  "v_mov_b32 v162, %[step]\n\t" \
  "s_mov_b64 exec, 1\n\t" \
  "v_mov_b32 v164, s94\n\t" \
  "ds_max_u32 v168, v164 offset:4100\n\t" \
  "ds_inc_rtn_u32 v161, v168, v166 offset:4096\n\t" \
  "ds_read_b32 v163, v168 offset:4100\n\t" \
  "s_waitcnt lgkmcnt(0)\n\t" \
  "v_readfirstlane_b32 s94, v161\n\t" \
  "v_readfirstlane_b32 s95, v163\n\t" \
  "s_mov_b64 exec, -1\n\t" \
  "s_cmp_lg_u32 s94, 7\n\t" \
  "s_cbranch_scc1 .Lna%=\n\t" \
  "s_add_u32 s95, s95, %[rl]\n\t" \
  "s_add_u32 s95, s95, %[base]\n\t" \
  "s_mov_b64 exec, 0xffff\n\t" \
  "v_mov_b32 v163, s95\n\t" \
  "buffer_store_dwordx2 v[162:163], v175, %[rs], 0 offen sc1\n\t" \
  "s_mov_b64 exec, -1\n\t" \
  ".Lna%=:\n\t" \
  "s_memrealtime s[90:91]\n\t" \
  "s_waitcnt lgkmcnt(0)\n\t" \
  "s_lshl_b32 s93, s90, 2\n\t" \
  "s_sub_i32 s93, %[E], s93\n\t" \
  "s_cmp_le_i32 s93, 0\n\t" \
  "s_cbranch_scc1 .Lgop%=\n\t" \
  "s_min_i32 s93, s93, 8000\n\t" \
  "s_add_u32 s93, s93, 3\n\t" \
  "s_lshr_b32 s93, s93, 2\n\t" \
  "s_add_u32 s93, s93, s90\n\t" \
  "s_memrealtime s[90:91]\n\t" \
  ".Lpacep%=:\n\t" \
  "s_memrealtime s[94:95]\n\t" \
  "s_waitcnt lgkmcnt(1)\n\t" \
  "s_sub_i32 s91, s90, s93\n\t" \
  "s_cmp_ge_i32 s91, 0\n\t" \
  "s_cbranch_scc1 .Lgop%=\n\t" \
  "s_memrealtime s[90:91]\n\t" \
  "s_waitcnt lgkmcnt(1)\n\t" \
  "s_sub_i32 s95, s94, s93\n\t" \
  "s_cmp_lt_i32 s95, 0\n\t" \
  "s_cbranch_scc1 .Lpacep%=\n\t" \
  ".Lgop%=:\n\t" \
  "buffer_load_dwordx4 v[128:131], v234, %[rs], %[so] offen sc1\n\t" \
  "s_nop 15\n\t" \
  "buffer_load_dwordx4 v[132:135], v235, %[rs], %[so] offen sc1\n\t" \
  "s_nop 15\n\t" \
  "buffer_load_dwordx4 v[136:139], v236, %[rs], %[so] offen sc1\n\t" \
  "s_nop 15\n\t" \
  "buffer_load_dwordx4 v[140:143], v237, %[rs], %[so] offen sc1\n\t" \
  "s_waitcnt lgkmcnt(0)\n\t" \
  ".Lw%=:\n\t" \
  "s_waitcnt vmcnt(3)\n\t" \
  "v_smfmac_f32_16x16x64_bf16 v[144:147], v[128:131], v[0:7], v172\n\t" \
  "v_max3_u32 v164, v128, v129, 0\n\t" \
  "v_smfmac_f32_16x16x64_bf16 v[148:151], v[128:131], v[8:15], v172\n\t" \
  "v_max3_u32 v164, v130, v131, v164\n\t" \
  "v_smfmac_f32_16x16x64_bf16 v[152:155], v[128:131], v[16:23], v172\n\t" \
  "v_smfmac_f32_16x16x64_bf16 v[156:159], v[128:131], v[24:31], v172\n\t" \
  "buffer_load_dwordx4 v[176:179], v238, %[rt], %[st] offen offset:0 sc1\n\t" \
  "s_waitcnt vmcnt(3)\n\t" \
  "v_smfmac_f32_16x16x64_bf16 v[144:147], v[132:135], v[32:39], v172\n\t" \
  "v_max3_u32 v164, v132, v133, v164\n\t" \
  "v_smfmac_f32_16x16x64_bf16 v[148:151], v[132:135], v[40:47], v172\n\t" \
  "v_max3_u32 v164, v134, v135, v164\n\t" \
  "v_smfmac_f32_16x16x64_bf16 v[152:155], v[132:135], v[48:55], v172\n\t" \
  "v_smfmac_f32_16x16x64_bf16 v[156:159], v[132:135], v[56:63], v172\n\t" \
  "buffer_load_dwordx4 v[180:183], v238, %[rt], %[st] offen offset:1024 sc1\n\t" \
  "s_waitcnt vmcnt(3)\n\t" \
  "v_smfmac_f32_16x16x64_bf16 v[144:147], v[136:139], v[64:71], v172\n\t" \
  "v_max3_u32 v164, v136, v137, v164\n\t" \
  "v_smfmac_f32_16x16x64_bf16 v[148:151], v[136:139], v[72:79], v172\n\t" \
  "v_max3_u32 v164, v138, v139, v164\n\t" \
  "v_smfmac_f32_16x16x64_bf16 v[152:155], v[136:139], v[80:87], v172\n\t" \
  "v_smfmac_f32_16x16x64_bf16 v[156:159], v[136:139], v[88:95], v172\n\t" \
  "buffer_load_dwordx4 v[184:187], v238, %[rt], %[st4] offen offset:0 sc1\n\t" \
  "s_waitcnt vmcnt(3)\n\t" \
  "v_smfmac_f32_16x16x64_bf16 v[144:147], v[140:143], v[96:103], v172\n\t" \
  "v_max3_u32 v164, v140, v141, v164\n\t" \
  "v_smfmac_f32_16x16x64_bf16 v[148:151], v[140:143], v[104:111], v172\n\t" \
  "v_max3_u32 v164, v142, v143, v164\n\t" \
  "v_smfmac_f32_16x16x64_bf16 v[152:155], v[140:143], v[112:119], v172\n\t" \
  "v_cmp_le_u32 vcc, %[sent], v164\n\t" \
  "v_smfmac_f32_16x16x64_bf16 v[156:159], v[140:143], v[120:127], v172\n\t" \
  "buffer_load_dwordx4 v[188:191], v238, %[rt], %[st4] offen offset:1024 sc1\n\t" \
  "s_cbranch_vccnz .Lslow%=\n\t" \
  ".Lok%=:\n\t" \
  "v_add_f32_dpp v160, v144, v146 quad_perm:[0,1,2,3] row_mask:0x3 bank_mask:0xf\n\t" \
  "v_add_f32_dpp v161, v145, v147 quad_perm:[0,1,2,3] row_mask:0x3 bank_mask:0xf\n\t" \
  "v_add_f32_dpp v162, v148, v150 quad_perm:[0,1,2,3] row_mask:0x3 bank_mask:0xf\n\t" \
  "v_add_f32_dpp v163, v149, v151 quad_perm:[0,1,2,3] row_mask:0x3 bank_mask:0xf\n\t" \
  "v_add_f32_dpp v160, v152, v154 quad_perm:[0,1,2,3] row_mask:0xc bank_mask:0xf\n\t" \
  "v_add_f32_dpp v161, v153, v155 quad_perm:[0,1,2,3] row_mask:0xc bank_mask:0xf\n\t" \
  "v_add_f32_dpp v162, v156, v158 quad_perm:[0,1,2,3] row_mask:0xc bank_mask:0xf\n\t" \
  "v_add_f32_dpp v163, v157, v159 quad_perm:[0,1,2,3] row_mask:0xc bank_mask:0xf\n\t" \
  "s_nop 0\n\t" \
  "v_permlane32_swap_b32 v160, v162\n\t" \
  "v_permlane32_swap_b32 v161, v163\n\t" \
  "v_add_f32 v160, v160, v162\n\t" \
  "v_add_f32 v161, v161, v163\n\t" \
  "ds_write_b64 v169, v[160:161]\n\t" \
  "s_mov_b64 exec, 1\n\t" \
  "ds_inc_rtn_u32 v164, v168, v166 offset:4104\n\t" \
  "s_mov_b64 exec, -1\n\t" \
  "ds_read_b64 v[128:129], v170 offset:0\n\t" \
  "ds_read_b64 v[130:131], v170 offset:512\n\t" \
  "ds_read_b64 v[132:133], v170 offset:1024\n\t" \
  "ds_read_b64 v[134:135], v170 offset:1536\n\t" \
  "ds_read_b64 v[136:137], v170 offset:2048\n\t" \
  "ds_read_b64 v[138:139], v170 offset:2560\n\t" \
  "ds_read_b64 v[140:141], v170 offset:3072\n\t" \
  "ds_read_b64 v[142:143], v170 offset:3584\n\t" \
  "s_waitcnt lgkmcnt(8)\n\t" \
  "v_readfirstlane_b32 s93, v164\n\t" \
  "s_cmp_lg_u32 s93, 7\n\t" \
  "s_cbranch_scc1 .Lb1%=\n\t" \
  "s_waitcnt lgkmcnt(4)\n\t" \
  "v_pk_add_f32 v[128:129], v[128:129], v[130:131]\n\t" \
  "v_pk_add_f32 v[132:133], v[132:133], v[134:135]\n\t" \
  "s_waitcnt lgkmcnt(0)\n\t" \
  "v_pk_add_f32 v[136:137], v[136:137], v[138:139]\n\t" \
  "v_pk_add_f32 v[140:141], v[140:141], v[142:143]\n\t" \
  "v_pk_add_f32 v[128:129], v[128:129], v[132:133]\n\t" \
  "v_pk_add_f32 v[136:137], v[136:137], v[140:141]\n\t" \
  "v_pk_add_f32 v[128:129], v[128:129], v[136:137]\n\t" \
  "v_cvt_pk_bf16_f32 v128, v128, v129\n\t" \
  "s_nop 1\n\t" \
  "v_mov_b32_dpp v129, v128 quad_perm:[1,0,3,2] row_mask:0xf bank_mask:0xf\n\t" \
  "v_perm_b32 v128, v129, v128, v174\n\t" \
  "buffer_store_dword v128, v171, %[rs], %[sh] offen sc1\n\t" \
  "s_memtime s[90:91]\n\t" \
  "s_barrier\n\t" \
  "s_memtime s[76:77]\n\t" \
  "s_memrealtime s[78:79]\n\t" \
  "s_nop 3\n\t" \
  "s_memrealtime s[80:81]\n\t" \
  "s_nop 3\n\t" \
  "s_memrealtime s[82:83]\n\t" \
  "s_nop 3\n\t" \
  "s_memrealtime s[84:85]\n\t" \
  "s_nop 3\n\t" \
  "s_memrealtime s[86:87]\n\t" \
  "s_nop 3\n\t" \
  "s_memrealtime s[88:89]\n\t" \
  "s_mov_b32 s93, 0\n\t" \
  "s_waitcnt lgkmcnt(0)\n\t" \
  "s_cmp_eq_u32 s80, s78\n\t" \
  "s_addc_u32 s93, s93, 0\n\t" \
  "s_cmp_eq_u32 s82, s78\n\t" \
  "s_addc_u32 s93, s93, 0\n\t" \
  "s_cmp_eq_u32 s84, s78\n\t" \
  "s_addc_u32 s93, s93, 0\n\t" \
  "s_cmp_eq_u32 s86, s78\n\t" \
  "s_addc_u32 s93, s93, 0\n\t" \
  "s_cmp_eq_u32 s88, s78\n\t" \
  "s_addc_u32 s93, s93, 0\n\t" \
  "s_mul_i32 s93, s93, 21\n\t" \
  "s_add_u32 s76, s76, 15\n\t" \
  "s_add_u32 s76, s76, s93\n\t" \
  "s_lshl2_add_u32 s78, s78, 16\n\t" \
  "s_mov_b64 exec, 1\n\t" \
  "s_sub_i32 s90, s76, s90\n\t" \
  "s_mul_i32 s90, s90, 11900\n\t" \
  "s_ashr_i32 s90, s90, 16\n\t" \
  "s_sub_u32 s90, s78, s90\n\t" \
  "s_sub_u32 s90, s90, %[base]\n\t" \
  "v_mov_b32 v164, s90\n\t" \
  "ds_write_b32 v168, v164 offset:4108\n\t" \
  "s_mov_b64 exec, -1\n\t" \
  "s_waitcnt vmcnt(1)\n\t" \
  ".Lb2%=:\n\t" \
  "s_setprio 0\n\t" \
  "s_bitcmp0_b64 %[am], %[step]\n\t" \
  "s_cbranch_scc1 .La1%=\n\t" \
  "buffer_store_dwordx4 v[224:227], v228, %[ra], 0 offen sc1\n\t" \
  ".La1%=:\n\t" \
  "buffer_load_dwordx4 v[192:195], v239, %[rt], %[st] offen offset:0 sc1\n\t" \
  "buffer_load_dwordx4 v[196:199], v239, %[rt], %[st] offen offset:1024 sc1\n\t" \
  "v_mfma_f32_16x16x32_bf16 v[128:131], v[0:3], v[176:179], 0\n\t" \
  "v_mfma_f32_16x16x32_bf16 v[136:139], v[8:11], v[176:179], 0\n\t" \
  "ds_read2st64_b32 v[144:145], v231 offset0:0 offset1:8\n\t" \
  "ds_read2st64_b32 v[146:147], v231 offset0:16 offset1:24\n\t" \
  "ds_read2st64_b32 v[148:149], v231 offset0:32 offset1:40\n\t" \
  "ds_read2st64_b32 v[150:151], v231 offset0:48 offset1:56\n\t" \
  "buffer_load_dwordx4 v[200:203], v239, %[rt], %[st4] offen offset:0 sc1\n\t" \
  "buffer_load_dwordx4 v[204:207], v239, %[rt], %[st4] offen offset:1024 sc1\n\t" \
  "v_mfma_f32_16x16x32_bf16 v[132:135], v[4:7], v[180:183], 0\n\t" \
  "v_mfma_f32_16x16x32_bf16 v[140:143], v[12:15], v[180:183], 0\n\t" \
  "s_waitcnt lgkmcnt(0)\n\t" \
  "v_add_f32 v144, v144, v145\n\t" \
  "v_add_f32 v146, v146, v147\n\t" \
  "v_add_f32 v148, v148, v149\n\t" \
  "v_add_f32 v150, v150, v151\n\t" \
  "v_add_f32 v144, v144, v146\n\t" \
  "v_add_f32 v148, v148, v150\n\t" \
  "buffer_load_dwordx4 v[208:211], v240, %[rt], %[st] offen offset:0 sc1\n\t" \
  "buffer_load_dwordx4 v[212:215], v240, %[rt], %[st] offen offset:1024 sc1\n\t" \
  "v_mfma_f32_16x16x32_bf16 v[128:131], v[16:19], v[184:187], v[128:131]\n\t" \
  "v_mfma_f32_16x16x32_bf16 v[136:139], v[24:27], v[184:187], v[136:139]\n\t" \
  "v_add_f32 v144, v144, v148\n\t" \
  "v_cvt_pk_bf16_f32 v144, v144, v144\n\t" \
  "ds_write_b16 v232, v144\n\t" \
  "ds_read_b128 v[224:227], v229\n\t" \
  "v_add_u32 v228, %[ainc], v228\n\t" \
  "v_add_u32 v229, %[rinc], v229\n\t" \
  "v_add_u32 v232, 1024, v232\n\t" \
  "v_xor_b32 v231, 0x4000, v231\n\t" \
  "buffer_load_dwordx4 v[216:219], v240, %[rt], %[st4] offen offset:0 sc1\n\t" \
  "buffer_load_dwordx4 v[220:223], v240, %[rt], %[st4] offen offset:1024 sc1\n\t" \
  "v_mfma_f32_16x16x32_bf16 v[132:135], v[20:23], v[188:191], v[132:135]\n\t" \
  "v_mfma_f32_16x16x32_bf16 v[140:143], v[28:31], v[188:191], v[140:143]\n\t" \
  "buffer_load_dwordx4 v[176:179], v241, %[rt], %[st] offen offset:0 sc1\n\t" \
  "s_waitcnt vmcnt(8)\n\t" \
  "v_mfma_f32_16x16x32_bf16 v[128:131], v[32:35], v[192:195], v[128:131]\n\t" \
  "v_mfma_f32_16x16x32_bf16 v[136:139], v[40:43], v[192:195], v[136:139]\n\t" \
  "buffer_load_dwordx4 v[180:183], v241, %[rt], %[st] offen offset:1024 sc1\n\t" \
  "s_waitcnt vmcnt(8)\n\t" \
  "v_mfma_f32_16x16x32_bf16 v[132:135], v[36:39], v[196:199], v[132:135]\n\t" \
  "v_mfma_f32_16x16x32_bf16 v[140:143], v[44:47], v[196:199], v[140:143]\n\t" \
  "buffer_load_dwordx4 v[184:187], v241, %[rt], %[st4] offen offset:0 sc1\n\t" \
  "s_waitcnt vmcnt(8)\n\t" \
  "v_mfma_f32_16x16x32_bf16 v[128:131], v[48:51], v[200:203], v[128:131]\n\t" \
  "v_mfma_f32_16x16x32_bf16 v[136:139], v[56:59], v[200:203], v[136:139]\n\t" \
  "ds_read_b128 v[160:163], v168 offset:4096\n\t" \
  "buffer_load_dwordx4 v[188:191], v241, %[rt], %[st4] offen offset:1024 sc1\n\t" \
  "s_waitcnt vmcnt(8)\n\t" \
  "v_mfma_f32_16x16x32_bf16 v[132:135], v[52:55], v[204:207], v[132:135]\n\t" \
  "v_mfma_f32_16x16x32_bf16 v[140:143], v[60:63], v[204:207], v[140:143]\n\t" \
  "s_waitcnt lgkmcnt(0)\n\t" \
  "v_readfirstlane_b32 s90, v161\n\t" \
  "v_readfirstlane_b32 s91, v163\n\t" \
  "s_add_u32 s93, s90, %[rl]\n\t" \
  "s_add_u32 s93, s93, %[base]\n\t" \
  "s_add_u32 %[E], s93, %[lk]\n\t" \
  "s_sub_i32 s91, s91, s90\n\t" \
  "s_sub_i32 s91, s91, %[rl]\n\t" \
  "s_min_i32 s93, s91, %[up]\n\t" \
  "s_max_i32 s93, s93, %[dn]\n\t" \
  "s_cmp_gt_i32 s91, 60\n\t" \
  "s_cselect_b32 s93, 2, s93\n\t" \
  "s_add_i32 %[rl], %[rl], s93\n\t" \
  "s_waitcnt vmcnt(7)\n\t" \
  "v_mfma_f32_16x16x32_bf16 v[128:131], v[64:67], v[208:211], v[128:131]\n\t" \
  "v_mfma_f32_16x16x32_bf16 v[136:139], v[72:75], v[208:211], v[136:139]\n\t" \
  "s_add_u32 %[step], %[step], 1\n\t" \
  "s_lshl_b32 %[sh], %[step], 15\n\t" \
  "s_lshr_b32 s93, %[step], 4\n\t" \
  "s_lshr_b32 s93, %[pm], s93\n\t" \
  "s_and_b32 s93, s93, 4\n\t" \
  "s_lshl_b32 s93, s93, 11\n\t" \
  "s_lshl2_add_u32 %[so], %[sh], s93\n\t" \
  "s_waitcnt vmcnt(6)\n\t" \
  "v_mfma_f32_16x16x32_bf16 v[132:135], v[68:71], v[212:215], v[132:135]\n\t" \
  "v_mfma_f32_16x16x32_bf16 v[140:143], v[76:79], v[212:215], v[140:143]\n\t" \
  "s_sub_i32 s93, %[step], 3\n\t" \
  "s_lshl_b32 %[st], s93, 19\n\t" \
  "s_lshl_b32 s94, s93, 11\n\t" \
  "s_xor_b32 s94, s94, %[s0]\n\t" \
  "s_and_b32 s94, s94, 0x2000\n\t" \
  "s_or_b32 %[st], %[st], s94\n\t" \
  "s_cmp_lt_i32 s93, 0\n\t" \
  "s_cselect_b32 %[st], 0x70000000, %[st]\n\t" \
  "s_add_u32 %[st4], %[st], 4096\n\t" \
  "s_waitcnt vmcnt(5)\n\t" \
  "v_mfma_f32_16x16x32_bf16 v[128:131], v[80:83], v[216:219], v[128:131]\n\t" \
  "v_mfma_f32_16x16x32_bf16 v[136:139], v[88:91], v[216:219], v[136:139]\n\t" \
  "s_waitcnt vmcnt(4)\n\t" \
  "v_mfma_f32_16x16x32_bf16 v[132:135], v[84:87], v[220:223], v[132:135]\n\t" \
  "v_mfma_f32_16x16x32_bf16 v[140:143], v[92:95], v[220:223], v[140:143]\n\t" \
  "s_cmp_eq_u32 %[step], 63\n\t" \
  "s_cbranch_scc1 .Lgol%=\n\t" \
  "s_movk_i32 s93, 600\n\t" \
  ".Lpacel%=:\n\t" \
  "s_memrealtime s[90:91]\n\t" \
  "s_waitcnt lgkmcnt(0)\n\t" \
  "s_lshl_b32 s90, s90, 2\n\t" \
  "s_sub_i32 s91, %[E], s90\n\t" \
  "s_cmp_le_i32 s91, 0\n\t" \
  "s_cbranch_scc1 .Lgol%=\n\t" \
  "s_sub_u32 s93, s93, 1\n\t" \
  "s_cmp_lg_u32 s93, 0\n\t" \
  "s_cbranch_scc1 .Lpacel%=\n\t" \
  ".Lgol%=:\n\t" \
  "buffer_load_dwordx2 v[166:167], v173, %[rs], 0 offen sc1\n\t" \
  "s_waitcnt vmcnt(4)\n\t" \
  "v_mfma_f32_16x16x32_bf16 v[128:131], v[96:99], v[176:179], v[128:131]\n\t" \
  "v_mfma_f32_16x16x32_bf16 v[136:139], v[104:107], v[176:179], v[136:139]\n\t" \
  "s_waitcnt vmcnt(3)\n\t" \
  "v_mfma_f32_16x16x32_bf16 v[132:135], v[100:103], v[180:183], v[132:135]\n\t" \
  "v_mfma_f32_16x16x32_bf16 v[140:143], v[108:111], v[180:183], v[140:143]\n\t" \
  "s_waitcnt vmcnt(2)\n\t" \
  "v_mfma_f32_16x16x32_bf16 v[128:131], v[112:115], v[184:187], v[128:131]\n\t" \
  "v_mfma_f32_16x16x32_bf16 v[136:139], v[120:123], v[184:187], v[136:139]\n\t" \
  "s_waitcnt vmcnt(1)\n\t" \
  "v_mfma_f32_16x16x32_bf16 v[132:135], v[116:119], v[188:191], v[132:135]\n\t" \
  "v_mfma_f32_16x16x32_bf16 v[140:143], v[124:127], v[188:191], v[140:143]\n\t" \
  "v_mov_b64 v[144:145], 0\n\t" \
  "v_mov_b64 v[146:147], 0\n\t" \
  "v_mov_b64 v[148:149], 0\n\t" \
  "v_mov_b64 v[150:151], 0\n\t" \
  "v_mov_b64 v[152:153], 0\n\t" \
  "v_mov_b64 v[154:155], 0\n\t" \
  "v_mov_b64 v[156:157], 0\n\t" \
  "v_mov_b64 v[158:159], 0\n\t" \
  "v_pk_add_f32 v[128:129], v[128:129], v[132:133]\n\t" \
  "v_pk_add_f32 v[130:131], v[130:131], v[134:135]\n\t" \
  "ds_write_b128 v230, v[128:131] offset:0\n\t" \
  "v_pk_add_f32 v[136:137], v[136:137], v[140:141]\n\t" \
  "v_pk_add_f32 v[138:139], v[138:139], v[142:143]\n\t" \
  "ds_write_b128 v230, v[136:139] offset:1024\n\t" \
  "v_xor_b32 v230, 0x4000, v230\n\t" \
  "s_cmp_lg_u32 %[step], 63\n\t" \
  "s_cbranch_scc1 .Lstep%=\n\t" \
  "buffer_load_dwordx4 v[176:179], v238, %[rt], %[st] offen offset:0 sc1\n\t" \
  "buffer_load_dwordx4 v[180:183], v238, %[rt], %[st] offen offset:1024 sc1\n\t" \
  "buffer_load_dwordx4 v[184:187], v238, %[rt], %[st4] offen offset:0 sc1\n\t" \
  "buffer_load_dwordx4 v[188:191], v238, %[rt], %[st4] offen offset:1024 sc1\n\t" \
  "buffer_load_dwordx4 v[192:195], v239, %[rt], %[st] offen offset:0 sc1\n\t" \
  "buffer_load_dwordx4 v[196:199], v239, %[rt], %[st] offen offset:1024 sc1\n\t" \
  "buffer_load_dwordx4 v[200:203], v239, %[rt], %[st4] offen offset:0 sc1\n\t" \
  "buffer_load_dwordx4 v[204:207], v239, %[rt], %[st4] offen offset:1024 sc1\n\t" \
  "v_subrev_u32 v230, 98304, v230\n\t" \
  ".Lt%=:\n\t" \
  "s_waitcnt vmcnt(7)\n\t" \
  "v_mfma_f32_16x16x32_bf16 v[128:131], v[0:3], v[176:179], 0\n\t" \
  "v_mfma_f32_16x16x32_bf16 v[136:139], v[8:11], v[176:179], 0\n\t" \
  "buffer_load_dwordx4 v[176:179], v240, %[rt], %[st] offen offset:0 sc1\n\t" \
  "buffer_load_dword v166, v242, %[rs], 0 offen sc1\n\t" \
  "s_waitcnt vmcnt(8)\n\t" \
  "v_mfma_f32_16x16x32_bf16 v[132:135], v[4:7], v[180:183], 0\n\t" \
  "v_mfma_f32_16x16x32_bf16 v[140:143], v[12:15], v[180:183], 0\n\t" \
  "buffer_load_dwordx4 v[180:183], v240, %[rt], %[st] offen offset:1024 sc1\n\t" \
  "s_waitcnt vmcnt(8)\n\t" \
  "v_mfma_f32_16x16x32_bf16 v[128:131], v[16:19], v[184:187], v[128:131]\n\t" \
  "v_mfma_f32_16x16x32_bf16 v[136:139], v[24:27], v[184:187], v[136:139]\n\t" \
  "buffer_load_dwordx4 v[184:187], v240, %[rt], %[st4] offen offset:0 sc1\n\t" \
  "s_waitcnt vmcnt(8)\n\t" \
  "v_mfma_f32_16x16x32_bf16 v[132:135], v[20:23], v[188:191], v[132:135]\n\t" \
  "v_mfma_f32_16x16x32_bf16 v[140:143], v[28:31], v[188:191], v[140:143]\n\t" \
  "buffer_load_dwordx4 v[188:191], v240, %[rt], %[st4] offen offset:1024 sc1\n\t" \
  "s_waitcnt vmcnt(8)\n\t" \
  "v_mfma_f32_16x16x32_bf16 v[128:131], v[32:35], v[192:195], v[128:131]\n\t" \
  "v_mfma_f32_16x16x32_bf16 v[136:139], v[40:43], v[192:195], v[136:139]\n\t" \
  "buffer_load_dwordx4 v[192:195], v241, %[rt], %[st] offen offset:0 sc1\n\t" \
  "s_waitcnt vmcnt(8)\n\t" \
  "v_mfma_f32_16x16x32_bf16 v[132:135], v[36:39], v[196:199], v[132:135]\n\t" \
  "v_mfma_f32_16x16x32_bf16 v[140:143], v[44:47], v[196:199], v[140:143]\n\t" \
  "buffer_load_dwordx4 v[196:199], v241, %[rt], %[st] offen offset:1024 sc1\n\t" \
  "s_waitcnt vmcnt(8)\n\t" \
  "v_mfma_f32_16x16x32_bf16 v[128:131], v[48:51], v[200:203], v[128:131]\n\t" \
  "v_mfma_f32_16x16x32_bf16 v[136:139], v[56:59], v[200:203], v[136:139]\n\t" \
  "buffer_load_dwordx4 v[200:203], v241, %[rt], %[st4] offen offset:0 sc1\n\t" \
  "s_waitcnt vmcnt(8)\n\t" \
  "v_mfma_f32_16x16x32_bf16 v[132:135], v[52:55], v[204:207], v[132:135]\n\t" \
  "v_mfma_f32_16x16x32_bf16 v[140:143], v[60:63], v[204:207], v[140:143]\n\t" \
  "buffer_load_dwordx4 v[204:207], v241, %[rt], %[st4] offen offset:1024 sc1\n\t" \
  "s_waitcnt vmcnt(8)\n\t" \
  "v_mfma_f32_16x16x32_bf16 v[128:131], v[64:67], v[176:179], v[128:131]\n\t" \
  "v_mfma_f32_16x16x32_bf16 v[136:139], v[72:75], v[176:179], v[136:139]\n\t" \
  "s_waitcnt vmcnt(7)\n\t" \
  ".Ltc%=:\n\t" \
  "v_cmp_eq_u32 vcc, -1, v166\n\t" \
  "s_cbranch_vccnz .Ltw%=\n\t" \
  ".Lto%=:\n\t" \
  "s_cmp_eq_u32 %[step], 66\n\t" \
  "s_cbranch_scc1 .Ltl%=\n\t" \
  "s_add_u32 %[step], %[step], 1\n\t" \
  "s_sub_i32 s93, %[step], 3\n\t" \
  "s_lshl_b32 %[st], s93, 19\n\t" \
  "s_lshl_b32 s94, s93, 11\n\t" \
  "s_xor_b32 s94, s94, %[s0]\n\t" \
  "s_and_b32 s94, s94, 0x2000\n\t" \
  "s_or_b32 %[st], %[st], s94\n\t" \
  "s_cmp_lt_i32 s93, 0\n\t" \
  "s_cselect_b32 %[st], 0x70000000, %[st]\n\t" \
  "s_add_u32 %[st4], %[st], 4096\n\t" \
  "v_add_u32 v242, 16384, v242\n\t" \
  "s_cmp_eq_u32 %[step], 66\n\t" \
  "s_cbranch_scc0 .Ltn%=\n\t" \
  "v_mov_b32 v242, %[fg]\n\t" \
  ".Ltn%=:\n\t" \
  "buffer_load_dwordx4 v[176:179], v238, %[rt], %[st] offen offset:0 sc1\n\t" \
  "s_waitcnt vmcnt(7)\n\t" \
  "v_mfma_f32_16x16x32_bf16 v[132:135], v[68:71], v[180:183], v[132:135]\n\t" \
  "v_mfma_f32_16x16x32_bf16 v[140:143], v[76:79], v[180:183], v[140:143]\n\t" \
  "buffer_load_dwordx4 v[180:183], v238, %[rt], %[st] offen offset:1024 sc1\n\t" \
  "s_waitcnt vmcnt(7)\n\t" \
  "v_mfma_f32_16x16x32_bf16 v[128:131], v[80:83], v[184:187], v[128:131]\n\t" \
  "v_mfma_f32_16x16x32_bf16 v[136:139], v[88:91], v[184:187], v[136:139]\n\t" \
  "buffer_load_dwordx4 v[184:187], v238, %[rt], %[st4] offen offset:0 sc1\n\t" \
  "s_waitcnt vmcnt(7)\n\t" \
  "v_mfma_f32_16x16x32_bf16 v[132:135], v[84:87], v[188:191], v[132:135]\n\t" \
  "v_mfma_f32_16x16x32_bf16 v[140:143], v[92:95], v[188:191], v[140:143]\n\t" \
  "buffer_load_dwordx4 v[188:191], v238, %[rt], %[st4] offen offset:1024 sc1\n\t" \
  "s_waitcnt vmcnt(7)\n\t" \
  "v_mfma_f32_16x16x32_bf16 v[128:131], v[96:99], v[192:195], v[128:131]\n\t" \
  "v_mfma_f32_16x16x32_bf16 v[136:139], v[104:107], v[192:195], v[136:139]\n\t" \
  "buffer_load_dwordx4 v[192:195], v239, %[rt], %[st] offen offset:0 sc1\n\t" \
  "s_waitcnt vmcnt(7)\n\t" \
  "v_mfma_f32_16x16x32_bf16 v[132:135], v[100:103], v[196:199], v[132:135]\n\t" \
  "v_mfma_f32_16x16x32_bf16 v[140:143], v[108:111], v[196:199], v[140:143]\n\t" \
  "buffer_load_dwordx4 v[196:199], v239, %[rt], %[st] offen offset:1024 sc1\n\t" \
  "s_waitcnt vmcnt(7)\n\t" \
  "v_mfma_f32_16x16x32_bf16 v[128:131], v[112:115], v[200:203], v[128:131]\n\t" \
  "v_mfma_f32_16x16x32_bf16 v[136:139], v[120:123], v[200:203], v[136:139]\n\t" \
  "buffer_load_dwordx4 v[200:203], v239, %[rt], %[st4] offen offset:0 sc1\n\t" \
  "s_waitcnt vmcnt(7)\n\t" \
  "v_mfma_f32_16x16x32_bf16 v[132:135], v[116:119], v[204:207], v[132:135]\n\t" \
  "v_mfma_f32_16x16x32_bf16 v[140:143], v[124:127], v[204:207], v[140:143]\n\t" \
  "buffer_load_dwordx4 v[204:207], v239, %[rt], %[st4] offen offset:1024 sc1\n\t" \
  "s_nop 5\n\t" \
  "v_pk_add_f32 v[128:129], v[128:129], v[132:133]\n\t" \
  "v_pk_add_f32 v[130:131], v[130:131], v[134:135]\n\t" \
  "ds_write_b128 v230, v[128:131] offset:0\n\t" \
  "v_pk_add_f32 v[136:137], v[136:137], v[140:141]\n\t" \
  "v_pk_add_f32 v[138:139], v[138:139], v[142:143]\n\t" \
  "ds_write_b128 v230, v[136:139] offset:1024\n\t" \
  "v_add_u32 v230, 16384, v230\n\t" \
  "s_branch .Lt%=\n\t" \
  ".Ltl%=:\n\t" \
  "buffer_load_dwordx4 v[224:227], v243, %[rg], 0 offen sc1\n\t" \
  "s_waitcnt vmcnt(7)\n\t" \
  "v_mfma_f32_16x16x32_bf16 v[132:135], v[68:71], v[180:183], v[132:135]\n\t" \
  "v_mfma_f32_16x16x32_bf16 v[140:143], v[76:79], v[180:183], v[140:143]\n\t" \
  "s_waitcnt vmcnt(6)\n\t" \
  "v_mfma_f32_16x16x32_bf16 v[128:131], v[80:83], v[184:187], v[128:131]\n\t" \
  "v_mfma_f32_16x16x32_bf16 v[136:139], v[88:91], v[184:187], v[136:139]\n\t" \
  "s_waitcnt vmcnt(5)\n\t" \
  "v_mfma_f32_16x16x32_bf16 v[132:135], v[84:87], v[188:191], v[132:135]\n\t" \
  "v_mfma_f32_16x16x32_bf16 v[140:143], v[92:95], v[188:191], v[140:143]\n\t" \
  "s_waitcnt vmcnt(4)\n\t" \
  "v_mfma_f32_16x16x32_bf16 v[128:131], v[96:99], v[192:195], v[128:131]\n\t" \
  "v_mfma_f32_16x16x32_bf16 v[136:139], v[104:107], v[192:195], v[136:139]\n\t" \
  "s_waitcnt vmcnt(3)\n\t" \
  "v_mfma_f32_16x16x32_bf16 v[132:135], v[100:103], v[196:199], v[132:135]\n\t" \
  "v_mfma_f32_16x16x32_bf16 v[140:143], v[108:111], v[196:199], v[140:143]\n\t" \
  "s_waitcnt vmcnt(2)\n\t" \
  "v_mfma_f32_16x16x32_bf16 v[128:131], v[112:115], v[200:203], v[128:131]\n\t" \
  "v_mfma_f32_16x16x32_bf16 v[136:139], v[120:123], v[200:203], v[136:139]\n\t" \
  "s_waitcnt vmcnt(1)\n\t" \
  "v_mfma_f32_16x16x32_bf16 v[132:135], v[116:119], v[204:207], v[132:135]\n\t" \
  "v_mfma_f32_16x16x32_bf16 v[140:143], v[124:127], v[204:207], v[140:143]\n\t" \
  "s_nop 6\n\t" \
  "v_pk_add_f32 v[128:129], v[128:129], v[132:133]\n\t" \
  "v_pk_add_f32 v[130:131], v[130:131], v[134:135]\n\t" \
  "ds_write_b128 v230, v[128:131] offset:0\n\t" \
  "v_pk_add_f32 v[136:137], v[136:137], v[140:141]\n\t" \
  "v_pk_add_f32 v[138:139], v[138:139], v[142:143]\n\t" \
  "ds_write_b128 v230, v[136:139] offset:1024\n\t" \
  "s_waitcnt vmcnt(0) lgkmcnt(0)\n\t" \
  "s_branch .Lend%=\n\t" \
  ".Ltw%=:\n\t" \
  "s_add_u32 s92, s92, 1\n\t" \
  "s_sleep 2\n\t" \
  "s_and_b32 s93, s92, 63\n\t" \
  "s_cbranch_scc1 .Ltr%=\n\t" \
  "buffer_load_dword v164, off, %[rs], %[sab] sc1\n\t" \
  "s_waitcnt vmcnt(0)\n\t" \
  "v_readfirstlane_b32 s93, v164\n\t" \
  "s_nop 3\n\t" \
  "s_cmp_eq_u32 s93, 1\n\t" \
  "s_cbranch_scc1 .Lto%=\n\t" \
  "s_cmp_lt_u32 s92, 0x80000\n\t" \
  "s_cbranch_scc1 .Ltr%=\n\t" \
  "v_mov_b32 v164, 1\n\t" \
  "buffer_store_dword v164, off, %[rs], %[sab] sc1\n\t" \
  "s_waitcnt vmcnt(0)\n\t" \
  "s_branch .Lto%=\n\t" \
  ".Ltr%=:\n\t" \
  "buffer_load_dword v166, v242, %[rs], 0 offen sc1\n\t" \
  "s_waitcnt vmcnt(0)\n\t" \
  "s_branch .Ltc%=\n\t" \
  ".Lb1%=:\n\t" \
  "s_waitcnt vmcnt(0) lgkmcnt(0)\n\t" \
  "s_barrier\n\t" \
  "s_branch .Lb2%=\n\t" \
  ".Lbl%=:\n\t" \
  "s_bitset1_b64 %[ps], %[step]\n\t" \
  "s_cmp_lt_u32 s92, 3\n\t" \
  "s_cbranch_scc1 .Lbm%=\n\t" \
  "s_sleep 12\n\t" \
  ".Lbm%=:\n\t" \
  "s_add_u32 s92, s92, 1\n\t" \
  "s_sleep 2\n\t" \
  "s_and_b32 s93, s92, 63\n\t" \
  "s_cbranch_scc1 .Lba%=\n\t" \
  "buffer_load_dword v164, off, %[rs], %[sab] sc1\n\t" \
  "s_waitcnt vmcnt(0)\n\t" \
  "v_readfirstlane_b32 s93, v164\n\t" \
  "s_nop 3\n\t" \
  "s_cmp_eq_u32 s93, 1\n\t" \
  "s_cbranch_scc1 .Lgo%=\n\t" \
  "s_cmp_lt_u32 s92, 0x80000\n\t" \
  "s_cbranch_scc1 .Lba%=\n\t" \
  "v_mov_b32 v164, 1\n\t" \
  "buffer_store_dword v164, off, %[rs], %[sab] sc1\n\t" \
  "s_waitcnt vmcnt(0)\n\t" \
  "s_branch .Lgo%=\n\t" \
  ".Lba%=:\n\t" \
  "buffer_load_dwordx2 v[166:167], v173, %[rs], 0 offen sc1\n\t" \
  "s_branch .Lcal%=\n\t" \
  ".Lslow%=:\n\t" \
  "s_mov_b64 s[94:95], 0\n\t" \
  "v_cmp_eq_u32 vcc, %[sent], v128\n\t" \
  "s_or_b64 s[94:95], s[94:95], vcc\n\t" \
  "v_cmp_eq_u32 vcc, %[sent], v129\n\t" \
  "s_or_b64 s[94:95], s[94:95], vcc\n\t" \
  "v_cmp_eq_u32 vcc, %[sent], v130\n\t" \
  "s_or_b64 s[94:95], s[94:95], vcc\n\t" \
  "v_cmp_eq_u32 vcc, %[sent], v131\n\t" \
  "s_or_b64 s[94:95], s[94:95], vcc\n\t" \
  "v_cmp_eq_u32 vcc, %[sent], v132\n\t" \
  "s_or_b64 s[94:95], s[94:95], vcc\n\t" \
  "v_cmp_eq_u32 vcc, %[sent], v133\n\t" \
  "s_or_b64 s[94:95], s[94:95], vcc\n\t" \
  "v_cmp_eq_u32 vcc, %[sent], v134\n\t" \
  "s_or_b64 s[94:95], s[94:95], vcc\n\t" \
  "v_cmp_eq_u32 vcc, %[sent], v135\n\t" \
  "s_or_b64 s[94:95], s[94:95], vcc\n\t" \
  "v_cmp_eq_u32 vcc, %[sent], v136\n\t" \
  "s_or_b64 s[94:95], s[94:95], vcc\n\t" \
  "v_cmp_eq_u32 vcc, %[sent], v137\n\t" \
  "s_or_b64 s[94:95], s[94:95], vcc\n\t" \
  "v_cmp_eq_u32 vcc, %[sent], v138\n\t" \
  "s_or_b64 s[94:95], s[94:95], vcc\n\t" \
  "v_cmp_eq_u32 vcc, %[sent], v139\n\t" \
  "s_or_b64 s[94:95], s[94:95], vcc\n\t" \
  "v_cmp_eq_u32 vcc, %[sent], v140\n\t" \
  "s_or_b64 s[94:95], s[94:95], vcc\n\t" \
  "v_cmp_eq_u32 vcc, %[sent], v141\n\t" \
  "s_or_b64 s[94:95], s[94:95], vcc\n\t" \
  "v_cmp_eq_u32 vcc, %[sent], v142\n\t" \
  "s_or_b64 s[94:95], s[94:95], vcc\n\t" \
  "v_cmp_eq_u32 vcc, %[sent], v143\n\t" \
  "s_or_b64 s[94:95], s[94:95], vcc\n\t" \
  "s_cbranch_scc0 .Lok%=\n\t" \
  "s_bitset1_b64 %[fm], %[step]\n\t" \
  "s_add_u32 s92, s92, 1\n\t" \
  "s_sleep 1\n\t" \
  "s_and_b32 s93, s92, 63\n\t" \
  "s_cbranch_scc1 .Lre%=\n\t" \
  "buffer_load_dword v164, off, %[rs], %[sab] sc1\n\t" \
  "s_waitcnt vmcnt(0)\n\t" \
  "v_readfirstlane_b32 s93, v164\n\t" \
  "s_nop 3\n\t" \
  "s_cmp_eq_u32 s93, 1\n\t" \
  "s_cbranch_scc1 .Lok%=\n\t" \
  "s_cmp_lt_u32 s92, 0x80000\n\t" \
  "s_cbranch_scc1 .Lre%=\n\t" \
  "v_mov_b32 v164, 1\n\t" \
  "buffer_store_dword v164, off, %[rs], %[sab] sc1\n\t" \
  "s_waitcnt vmcnt(0)\n\t" \
  "s_branch .Lok%=\n\t" \
  ".Lre%=:\n\t" \
  "buffer_load_dwordx4 v[128:131], v234, %[rs], %[so] offen sc1\n\t" \
  "s_nop 15\n\t" \
  "buffer_load_dwordx4 v[132:135], v235, %[rs], %[so] offen sc1\n\t" \
  "s_nop 15\n\t" \
  "buffer_load_dwordx4 v[136:139], v236, %[rs], %[so] offen sc1\n\t" \
  "s_nop 15\n\t" \
  "buffer_load_dwordx4 v[140:143], v237, %[rs], %[so] offen sc1\n\t" \
  "v_mov_b64 v[144:145], 0\n\t" \
  "v_mov_b64 v[146:147], 0\n\t" \
  "v_mov_b64 v[148:149], 0\n\t" \
  "v_mov_b64 v[150:151], 0\n\t" \
  "v_mov_b64 v[152:153], 0\n\t" \
  "v_mov_b64 v[154:155], 0\n\t" \
  "v_mov_b64 v[156:157], 0\n\t" \
  "v_mov_b64 v[158:159], 0\n\t" \
  "s_waitcnt vmcnt(0)\n\t" \
  "s_branch .Lw%=\n\t" \
  ".Lend%=:\n\t"

typedef __attribute__((ext_vector_type(32))) unsigned u32x32;
__device__ __forceinline__ void put4(u32x32& W, int i, bf16x8 f) {
  const u32x4 v = __builtin_bit_cast(u32x4, f);
#pragma unroll
  for (int j = 0; j < 4; ++j) W[4 * i + j] = v[j];
}
__device__ __forceinline__ bf16x8 get4(const u32x32& W, int i) {
  return as_bf16x8((u32x4){W[4 * i], W[4 * i + 1], W[4 * i + 2], W[4 * i + 3]});
}
__device__ __forceinline__ int look_prog(__amdgpu_buffer_rsrc_t rs, int off) {
  asm volatile("" ::: "memory");
  const int v = (int)ld_flag(rs, off);
  return min(__builtin_amdgcn_readlane(v, 0), __builtin_amdgcn_readlane(v, 1));
}
__device__ __forceinline__ int wait_prog(__amdgpu_buffer_rsrc_t rs, int off, int need, gu32* abortw) {
  int v;
  for (unsigned spins = 0;;) {
    v = look_prog(rs, off);
    if (v >= need) break;
    __builtin_amdgcn_s_sleep(4);
    if (spin_fail(spins, abortw)) break;
  }
  asm volatile("" ::: "memory");
  return v;
}
#define CLB8(a, b, c, d, e, f, g, h) "v" #a, "v" #b, "v" #c, "v" #d, "v" #e, "v" #f, "v" #g, "v" #h
#define CLB_S "s90", "s91", "s92", "s93", "s94", "s95", "vcc", "scc", "memory"
#define W_IN "{v[0:31]}"(W[0]), "{v[32:63]}"(W[1]), "{v[64:95]}"(W[2]), "{v[96:127]}"(W[3]), "{v[128:159]}"(W[4]), "{v[160:191]}"(W[5])

#define W_DELAY 60
#define LEAD0 360
#define QBJ 3
constexpr int L_MEM = 0, L_KT = 16384, L_QX = 24576, L_KX = 26624, L_VX = 16384, L_MISC = 30720, L_WL = 32768;

#define QK_STEP(TXT)                                                                                                        \
  asm volatile(TXT                                                                                                          \
      : [tout] "=&s"(tout), [spins] "=&s"(spins), [tb1] "=&s"(tb1), [tgt] "=&s"(tgt), "+{v248}"(aL16), "+{v249}"(aMW), "+{v250}"(aWL),                            \
        "+{v251}"(aIDX), "+{v252}"(aQW), "+{v[232:235]}"(QF)                                                                 \
      : W_IN, [rs] "s"(rs4), [som] "s"(o_mem(pm, s) + WV * 1024), [som2] "s"(o_mem(pm, s) + WV * 1024 + 16384), [sent] "s"(DSENT), [sab] "s"(SY_ABORT), \
        [lead] "s"(lead), [step] "s"(s), [sbc] "s"(SY_BA + side(pm, 1) + h * 128), [nh] "s"(0xFF00FF00FF00FF00ull), [up] "s"(0xFFFFFFFF00000000ull), [negc] "s"(-cexp),      \
        [c] "s"(cexp), [so] "s"(o_o(pm, s, h) + WV * 512), [sk] "s"(o_v(pm, s, h) + 2048 + (WV & 3) * 512),                    \
        [sv] "s"(o_v(pm, s, h) + WV * 512), [rt] "s"(rt4), [svt] "s"(t_off(p, T_V + kseg(s, h)) + WV * 2048)                  \
      : CLB8(192, 193, 194, 195, 196, 197, 198, 199), CLB8(200, 201, 202, 203, 204, 205, 206, 207),                          \
        CLB8(208, 209, 210, 211, 212, 213, 214, 215), CLB8(216, 217, 218, 219, 220, 221, 222, 223),                          \
        CLB8(224, 225, 226, 227, 228, 229, 230, 231), CLB8(236, 237, 238, 239, 240, 241, 242, 243), "v244", "v245", "v246", "v247", "v253", CLB_S)

template <int WV>
__device__ __forceinline__ void qk_wave(Args& p, int h, char* smem) {
  const int tid = threadIdx.x;
  unsigned pm = 0;
  int have = -1;
  u32x32 W[6];
  {
    int lane = tid & 63;
    asm volatile("" : "+v"(lane));
    const unsigned vo = (WV * 4 + (lane >> 4)) * (HD * 2) + 16 * ((lane & 15) ^ swzB((WV * 4 + (lane >> 4)) & 15));
    auto stage = [&](int j) {
#pragma unroll
      for (int mm = 0; mm < 2; ++mm) {
        const char* src = (const char*)((mm ? p.Wk : p.Wq) + (size_t)j * 128 * HD + h * DH);
        const unsigned dst = lds_off(smem) + (~j & 1) * 65536 + mm * 32768 + WV * 1024;
        glds2(src, 32 * HD * 2, vo, dst);
        glds2(src + 64 * HD * 2, 32 * HD * 2, vo, dst + 16384);
      }
    };
    bf16x8 keep[4][4];
    auto place = [&](int g) {
#pragma unroll
      for (int i = 0; i < 4; ++i)
        *reinterpret_cast<bf16x8*>(smem + (g == 0 ? 65536 : g == 1 ? 32768 : g == 2 ? 131072 : 98304) + WV * 4096 + i * 1024 + lane * 16) = keep[g][i];
    };
    if (h < 4) RDY(640 + h * 16 + 8);
    stage(0); stage(1);
    if (h < 4) RDY(640 + h * 16 + 9);
    Boot bt;
#pragma unroll
    for (int j = 0; j < 8; ++j) {
      if (j < 7) WAIT_V(8); else WAIT_V(0);
      if (WV == 7 && j == QBJ + 1) pm = boot_done(p.ws, bt);
      LBAR();
      if (h < 4) RDY(640 + h * 16 + j);
#pragma unroll
      for (int k = 0; k < 8; ++k) {
        const int P = (QK_MAP[WV] >> (4 * (2 * j + (k >> 2)))) & 15;
        const bf16x8 fr = ld_bfrag(smem + (~j & 1) * 65536 + (WV >> 2) * 32768 + (k >> 2) * 16384, k & 1, 2 * (WV & 3) + ((k >> 1) & 1), lane);
        const int f = P * 4 + (k & 3);
        if (P < 12) put4(W[f >> 3], f & 7, fr);
        else keep[P - 12][k & 3] = fr;
      }
      if (j == (WV >> 1)) place(2);
      LBAR();
      if (WV == 7 && j == QBJ) boot_ask(p.ws, bt);
      if (j + 2 < 8) stage(j + 2);
      if (j == 6) { place(0); place(3); }
    }
    if (h < 4) RDY(640 + h * 16 + 10);
    place(1);
    if (WV == 7 && lane < 8) lds_at(smem + L_MISC)[lane] = lane == 7 ? pm : (lane == 1 ? now_q() : 0);
    if (WV == 6) lds_at(smem + 28672)[lane] = ~0u;
    LBAR();
    pm = uni(lds_at(smem + L_MISC)[7]);
    p.s0 = pm >> 31;
    RDY(h);
    if (WV == 4 || WV == 5) {
      have = wait_prog(mkrsrc(p.ws + WS_SY, SY_TOTAL), fkq(h, (lane & 1) * 2), 0, (gu32*)(p.ws + WS_SY + SY_ABORT));
      const char* src = p.ws + WS_T + t_off(p, kseg(0, h)) + (WV - 4) * 4096 + lane * 16;
#pragma unroll
      for (int i = 0; i < 4; ++i) glds16_sc1(src + i * 1024, smem + L_KT + (WV - 4) * 4096 + i * 1024);
    }
  }
  const __amdgpu_buffer_rsrc_t rsy = mkrsrc(p.ws + WS_SY, SY_TOTAL);
  const u32x4 rs4 = raw_rsrc(p.ws + WS_SY, SY_TOTAL);
  const u32x4 rt4 = raw_rsrc(p.ws + WS_T, T_BYTES);
  gu32* abortw = (gu32*)(p.ws + WS_SY + SY_ABORT);
  const float cexp = 0.08838834764831845f * 1.4426950408889634f;
  constexpr int m = WV >> 2, b = WV & 3;
  int lead = LEAD0;
  unsigned tb1 = 0;
  unsigned long long fm = 0, ps = 0; (void)fm; (void)ps;
  for (int s = 0; s < NSEG; ++s) {
    int lane = tid & 63;
    asm volatile("" : "+v"(lane));
    const int r = lane & 15, q = lane >> 4;
    unsigned aL16 = lane * 16, aMW = aL16 + WV * 1024, aWL = aL16 + 98304 + WV * 4096;
    unsigned aIDX = (lane & 2) ? 0xEEEE : 0x4444;
    unsigned aQW = (32 * b + r) * 16 + q * 4 + (m ? L_KX : L_QX);
    u32x4 QF;
    asm volatile("" : "={v[232:235]}"(QF));
    if (WV < 4) {
      QF[0] = L_QX + (q * 8 + (r >> 2)) * 16 + (r & 1) * 8;
      QF[1] = ((2 * (q & 1) + (q >> 1)) * 16 + r) * 4;
      QF[2] = ((r >> 2) & 1) * 65536 + ((r >> 1) & 1) * 64 + (r >> 3) * 32 + (r & 1) * 16 + q * 4;
    } else QF[3] = r * 16 + q * 4;
    unsigned tout, spins, tgt;
    if (WV == 0) QK_STEP(QK_ASM_0); else if (WV == 1) QK_STEP(QK_ASM_1); else if (WV == 2) QK_STEP(QK_ASM_2);
    else if (WV == 3) QK_STEP(QK_ASM_3); else if (WV == 4) QK_STEP(QK_ASM_4); else if (WV == 5) QK_STEP(QK_ASM_5);
    else if (WV == 6) QK_STEP(QK_ASM_6); else QK_STEP(QK_ASM_7);
    tout = __builtin_amdgcn_readfirstlane(tout);
    tb1 = __builtin_amdgcn_readfirstlane(tb1);
    lane = tid & 63;
    asm volatile("" : "+v"(lane));
    if (WV < 4) {
      lds_at(smem + L_MISC + 8)[WV] = s + 1;
      if (WV == 3) lds_at(smem + L_MISC)[6] = time_of(tout);
    }
    if (WV >= 4 && s + 1 < NSEG) {
      unsigned t;
      do {
        __builtin_amdgcn_s_sleep(1);
        t = lds_at(smem + L_MISC + 8)[lane & 3];
      } while (!__all(t == (unsigned)(s + 1)));
      if (WV <= 5) {
        if (s + 2 < NSEG) {
          if (have < s + 1) have = wait_prog(rsy, fkq(h, (lane & 1) * 2), s + 1, abortw);
          const char* src = p.ws + WS_T + t_off(p, kseg(s + 1, h)) + (WV - 4) * 4096 + lane * 16;
#pragma unroll
          for (int i = 0; i < 4; ++i) glds16_sc1(src + i * 1024, smem + L_KT + (WV - 4) * 4096 + i * 1024);
          if (have < s + 2) have = look_prog(rsy, fkq(h, (lane & 1) * 2));
        }
      } else if (WV == 6) {
        watch(rsy, s, lane, now_q() + W_DELAY, (const unsigned*)(smem + L_MISC), abortw, side(pm, 1));
      } else {
        __builtin_amdgcn_s_sleep(6);
        const int x = (int)(__builtin_amdgcn_readfirstlane(lds_at(smem + L_MISC)[6]) - tb1);
        lead = __builtin_amdgcn_readfirstlane(track(lead, x, LEAD_UP, s < 8 ? 40 : LEAD_DN, 60));
        SMP(2, s, x); SMP(3, s, lead);
      }
    }
  }
}

__device__ __forceinline__ void qk_role(Args& p, int h, char* smem) {
  switch (__builtin_amdgcn_readfirstlane(threadIdx.x >> 6)) {
    case 0: qk_wave<0>(p, h, smem); break;
    case 1: qk_wave<1>(p, h, smem); break;
    case 2: qk_wave<2>(p, h, smem); break;
    case 3: qk_wave<3>(p, h, smem); break;
    case 4: qk_wave<4>(p, h, smem); break;
    case 5: qk_wave<5>(p, h, smem); break;
    case 6: qk_wave<6>(p, h, smem); break;
    default: qk_wave<7>(p, h, smem); break;
  }
}

#define CLB32(a) CLB32_##a
#define CLB32_128 CLB8(128, 129, 130, 131, 132, 133, 134, 135), CLB8(136, 137, 138, 139, 140, 141, 142, 143), CLB8(144, 145, 146, 147, 148, 149, 150, 151), CLB8(152, 153, 154, 155, 156, 157, 158, 159)
#define CLB32_160 CLB8(160, 161, 162, 163, 164, 165, 166, 167), CLB8(168, 169, 170, 171, 172, 173, 174, 175), CLB8(176, 177, 178, 179, 180, 181, 182, 183), CLB8(184, 185, 186, 187, 188, 189, 190, 191)
#define W4_IN "{v[0:31]}"(W[0]), "{v[32:63]}"(W[1]), "{v[64:95]}"(W[2]), "{v[96:127]}"(W[3])
#define LEAD_V 200

constexpr int V_VM = 16384, V_T = 32768, V_KI = 131072;
__device__ __forceinline__ void tok_rows(const Args& p, __amdgpu_buffer_rsrc_t rsy, unsigned pm, int sg, int h, int T, int eh, char* smem, char* img,
                                         int lane, gu32* abortw, unsigned tk) {
  const float scale = 0.08838834764831845f, LOG2E = 1.4426950408889634f;
  const __amdgpu_buffer_rsrc_t rt = mkrsrc(p.ws + WS_T, T_BYTES);
  asm volatile("" : "+v"(lane));
  const int r = lane & 15, q = lane >> 4;
  u32x4 km[2];
  if (tk) pace_until(tk);
#pragma unroll
  for (int i = 0; i < 2; ++i) km[i] = __builtin_amdgcn_raw_buffer_load_b128(rsy, lane * 16 + i * 1024, o_v(pm, sg, h) + 2048, 16);
  SCHED();
  const char* tl = smem + V_T + (sg & 3) * 24576 + lane * 16;
  bf16x8 qt[4], vt[4];
  unsigned vm[4];
  f32x4 d0 = {0.f, 0.f, 0.f, 0.f}, d1 = d0, d2 = d0;
#pragma unroll
  for (int i = 0; i < 4; ++i) qt[i] = ldsf(tl + 8192 + T * 4096 + i * 1024);
  {
    bf16x8 kf[8];
#pragma unroll
    for (int i = 0; i < 8; ++i) kf[i] = ldsf(tl + i * 1024);
    SCHED();
#pragma unroll
    for (int bb = 0; bb < 4; ++bb) { d0 = MFMA(kf[bb], qt[bb], d0); d1 = MFMA(kf[4 + bb], qt[bb], d1); }
  }
#pragma unroll
  for (int i = 0; i < 4; ++i) {
    vt[i] = ldsf(tl + 16384 + eh * 4096 + i * 1024);
    vm[i] = *reinterpret_cast<const unsigned*>(smem + V_VM + (sg & 1) * 2048 + eh * 1024 + i * 256 + lane * 4);
  }
  SCHED();
  for (unsigned sp = 0; !__all(!has_ds(km[0]) && !has_ds(km[1]));) {
    __builtin_amdgcn_s_sleep(4);
    if (spin_fail(sp, abortw)) break;
    asm volatile("" ::: "memory");
#pragma unroll
    for (int i = 0; i < 2; ++i) km[i] = __builtin_amdgcn_raw_buffer_load_b128(rsy, lane * 16 + i * 1024, o_v(pm, sg, h) + 2048, 16);
  }
#pragma unroll
  for (int i = 0; i < 2; ++i)
#pragma unroll
    for (int j = 0; j < 4; ++j) *reinterpret_cast<unsigned*>(img + (i * 64 + lane) * 32 + j * 8) = km[i][j];
  asm volatile("s_waitcnt lgkmcnt(0)" ::: "memory");
  const char* lk = img + (q * 8 + (r >> 2)) * 32 + (r & 3) * 8;
  {
    bf16x8 kk[4];
#pragma unroll
    for (int bb = 0; bb < 4; ++bb) {
      s16x4 lo = __builtin_amdgcn_ds_read_tr16_b64_v4i16((lds_s16x4_ptr)(lk + bb * 1024));
      s16x4 hi = __builtin_amdgcn_ds_read_tr16_b64_v4i16((lds_s16x4_ptr)(lk + bb * 1024 + 128));
      kk[bb] = (bf16x8){lo[0], lo[1], lo[2], lo[3], hi[0], hi[1], hi[2], hi[3]};
    }
    SCHED();
#pragma unroll
    for (int bb = 0; bb < 4; ++bb) d2 = MFMA(kk[bb], qt[bb], d2);
  }
  const int qi = 16 * T + r;
  float v[10];
#pragma unroll
  for (int j = 0; j < 4; ++j) { v[j] = d0[j]; v[4 + j] = d1[j]; }
  v[8] = d2[0]; v[9] = d2[1];
#pragma unroll
  for (int j = 0; j < 5; ++j) {
    unsigned t = cvtpk(v[2 * j], v[2 * j + 1]);
    t = cvtpk(lo_f(t) * scale, hi_f(t) * scale);
    v[2 * j] = lo_f(t); v[2 * j + 1] = hi_f(t);
  }
#pragma unroll
  for (int j = 0; j < 8; ++j) {
    const int key = (j >> 2) * 16 + 4 * q + (j & 3);
    v[j] = key <= qi ? v[j] : -INFINITY;
  }
  float mx = v[0];
#pragma unroll
  for (int j = 1; j < 10; ++j) mx = fmax2(mx, v[j]);
  mx = rowq_max(mx) * LOG2E;
  float sm = 0.f;
#pragma unroll
  for (int j = 0; j < 10; ++j) { v[j] = __builtin_amdgcn_exp2f(__builtin_fmaf(v[j], LOG2E, -mx)); sm += v[j]; }
  const float inv = __builtin_amdgcn_rcpf(rowq_sum(sm));
  u32x4 o = {cvtpk(v[0] * inv, v[1] * inv), cvtpk(v[2] * inv, v[3] * inv), cvtpk(v[4] * inv, v[5] * inv),
             cvtpk(v[6] * inv, v[7] * inv)};
  const s16x4 pmem = __builtin_bit_cast(s16x4, (u32x2){cvtpk(v[8] * inv, v[9] * inv), 0u});
  const int ob = t_off_o(p, T_O + (sg * 2 + T) * 16 + (h >> 1)) + (h & 1) * 4096 + eh * 2048;
#pragma unroll
  for (int j = 0; j < 2; ++j) {
    f32x4 d[2];
#pragma unroll
    for (int u = 0; u < 2; ++u) {
      d[u] = MFMA(vt[2 * j + u], as_bf16x8(o), ((f32x4){0.f, 0.f, 0.f, 0.f}));
      d[u] += __builtin_amdgcn_mfma_f32_16x16x16bf16_1k(__builtin_bit_cast(s16x4, (u32x2){vm[2 * j + u], 0u}), pmem, (f32x4){0.f, 0.f, 0.f, 0.f}, 0, 0, 0);
    }
    st_sc1(rt, ob + lane * 16 + j * 1024, pack32(d[0], d[1]));
  }
}

__device__ __forceinline__ void vu_role(Args& p, int h, char* smem) {
  const int tid = threadIdx.x, lane = tid & 63;
  const int w = __builtin_amdgcn_readfirstlane(tid >> 6);
  u32x32 W[4];
  {
    const unsigned vo = (tid >> 4) * (HD * 2) + 16 * ((lane & 15) ^ swzB((tid >> 4) & 15));
    auto stage = [&](int j) {
      const char* src = (const char*)(p.Wv + (size_t)j * 128 * HD + h * DH);
      const unsigned dst = lds_off(smem) + L_WL + (j & 3) * 32768 + w * 1024;
      glds2(src, 32 * HD * 2, vo, dst);
      glds2(src + 64 * HD * 2, 32 * HD * 2, vo, dst + 16384);
    };
    start_sy(p.ws, p.mem0, h, smem);
#pragma unroll
    for (int j = 0; j < 4; ++j) stage(j);
    Boot bt;
#pragma unroll
    for (int j = 0; j < 8; ++j) {
      if (j < 5) WAIT_V(12); else if (j == 5) WAIT_V(8); else if (j == 6) WAIT_V(4); else WAIT_V(0);
      if (j == 7 && w == 7) {
        const unsigned m = boot_done(p.ws, bt);
        if (lane < 8) lds_at(smem + L_MISC)[lane] = lane == 7 ? m : (lane == 1 ? now_q() : 0);
      }
      LBAR();
#pragma unroll
      for (int k = 0; k < 4; ++k) put4(W[j >> 1], (j & 1) * 4 + k, ld_bfrag(smem + L_WL + (j & 3) * 32768 + (k >> 1) * 16384, k & 1, w, lane));
      LBAR();
      if (j + 4 < 8) stage(j + 4);
      if (j == 5 && w == 7) boot_ask(p.ws, bt);
    }
    RDY(32 + h);
  }
  const __amdgpu_buffer_rsrc_t rsy = mkrsrc(p.ws + WS_SY, SY_TOTAL);
  const u32x4 rs4 = raw_rsrc(p.ws + WS_SY, SY_TOTAL);
  gu32* abortw = (gu32*)(p.ws + WS_SY + SY_ABORT);
  const unsigned pm = uni(lds_at(smem + L_MISC)[7]);
  p.s0 = pm >> 31;
  unsigned long long fm = 0; (void)fm;
  int have = -1;
  const int fo = SY_FO + ((w & 3) * NH + h) * 128;
#pragma unroll 1
  for (int s = 0; s < NSEG; ++s) {
    unsigned spins, tgt;
    asm volatile(VV_ASM
        : [spins] "=&s"(spins), [tgt] "=&s"(tgt)
        : W4_IN, "{v200}"(lane * 16), "{v201}"(lane * 16 + w * 2048), "{v202}"((lane & 2) ? 0xEEEE : 0x4444), "{v203}"(lane * 4),
          "{v204}"(tid * 4 + V_VM + (s & 1) * 2048), [rs] "s"(rs4), [som] "s"(o_mem(pm, s) + (w >> 2) * 16384 + (w & 3) * 2048), [step] "s"(s),
          [sent] "s"(DSENT), [sab] "s"(SY_ABORT), [sv] "s"(o_v(pm, s, h) + w * 256)
        : CLB32(128), CLB32(160), CLB8(192, 193, 194, 195, 196, 197, 198, 199), CLB_S);
    if (w < 4 ? s >= 1 : s == NSEG - 1) {
      if (s == NSEG - 1 && w < 4 && lane == 0) __builtin_amdgcn_raw_buffer_store_b32(1u, rsy, fo, 0, 16);
      tok_rows(p, rsy, pm, s - (w < 4), h, w & 1, (w >> 1) & 1, smem, smem + V_KI + w * 4096, lane, abortw, w < 4 ? 0 : now_q() + 160);
    } else if (w == 7) {
      SMP(6, s, (int)(now_q() - tgt));
      watch(rsy, s, lane, now_q() + LEAD_V + W_DELAY, (const unsigned*)(smem + L_MISC), abortw, side(pm, 1));
    } else if (w >= 4) {
      __builtin_amdgcn_s_sleep(2);
      const int s1 = w == 5 ? (s == NSEG - 2 ? s + 1 : s) : s + 1;
#pragma unroll 1
      for (int sg = (w == 5 || s == 0) ? s : s + 1; sg <= s1; ++sg) {
        if (have < sg) have = wait_prog(rsy, fkq(h, w - 4), sg, abortw);
        const char* src = p.ws + WS_T + t_off(p, (w - 4) * T_Q + kseg(sg, h)) + lane * 16;
#pragma unroll
        for (int i = 0; i < 8; ++i) glds16_sc1(src + i * 1024, smem + V_T + (sg & 3) * 24576 + (w - 4) * 8192 + i * 1024);
      }
      if (have < s1 + 1) have = look_prog(rsy, fkq(h, w - 4));
      if (w == 5 && s + NPRE < NSEG) {
#pragma unroll
        for (int i = 0; i < 4; ++i) st_sc1(rsy, o_v(pm, s + NPRE, h) + i * 1024 + lane * 16, (u32x4){DSENT, DSENT, DSENT, DSENT});
      }
    }
  }
  WAIT_V(0);
  if (lane == 0) __builtin_amdgcn_raw_buffer_store_b32(1u, rsy, fo + (1 + (w >> 2)) * 4 * NH * 128, 0, 16);
  if (h == 3 && (w & 2) == 0) { LST(351 + (w & 1) + (w >> 2) * 2); }
}

#define RL_UP 12
#define RL_DN 4
#define RED_MIN 40
#define BA_LOOK 310
constexpr int TLAG = 3;
constexpr int R_SI = 32768, R_PART = 98304;

__device__ __forceinline__ void red_role(Args& p, int j, char* smem) {
  const int tid = threadIdx.x;
  int lane = tid & 63;
  const int w = __builtin_amdgcn_readfirstlane(tid >> 6);
  const int c = (j & 2) * 8 + (j >> 2), rh = j & 1;
  const int hg = (w + (j >> 2)) & 7, rot = (j >> 2) + (j >> 5);
  const __amdgpu_buffer_rsrc_t rsy = mkrsrc(p.ws + WS_SY, SY_TOTAL);
  const u32x4 rs4 = raw_rsrc(p.ws + WS_SY, SY_TOTAL);
  const __amdgpu_buffer_rsrc_t rt = mkrsrc(p.ws + WS_T, T_BYTES);
  gu32* abortw = (gu32*)(p.ws + WS_SY + SY_ABORT);
  u32x32 W[4];
  u32x2 tag = {0u, 0u};
  {
    const u16* Wo = p.Wo + (size_t)(4 * hg * DH) * DM + 32 * c;
    const int ro = ((lane >> 4) * 8 + ((lane & 15) >> 2)) * 64 + (lane & 3) * 8;
    Boot bt;
    load_blocks<16>(smem + 32768 + w * 16384,
        [&](int i, char* slot) {
          if (i == 12 && w == 0) boot_ask(p.ws, bt);
          if ((i < 2 || i == 4 || i == 8) && (c & 14) != 6 && lane < 10) {
            const int x = i < 2 ? 3 - i : i >> 3, n = min(((c & 15) - ((c & 8) >> 2)) * 10 + lane, 127);
            touch_line((const char*)p.Wo + (size_t)(w * 512 + (x >> 1) * 256 + (x & 1) * 64 + (n & 64) + n) * (DM * 2) + (c & 16) * 64 + 384, smem + w * 256);
          }
          const int x = ((i >> 2) + rot) & 3;
          blk_dma(Wo + (size_t)((x >> 1) * 256 + (x & 1) * 64 + ((i >> 1) & 1) * 128 + (i & 1) * 32) * DM, DM, slot, lane);
        },
        [&](int i, char* slot) {
#pragma unroll
          for (int t = 0; t < 2; ++t) {
            s16x4 lo = __builtin_amdgcn_ds_read_tr16_b64_v4i16((lds_s16x4_ptr)(slot + ro + t * 32));
            s16x4 hi = __builtin_amdgcn_ds_read_tr16_b64_v4i16((lds_s16x4_ptr)(slot + ro + t * 32 + 256));
            put4(W[i >> 2], (i & 2) * 2 + 2 * t + (i & 1), (bf16x8){lo[0], lo[1], lo[2], lo[3], hi[0], hi[1], hi[2], hi[3]});
          }
        });
    if (w == 0) { lds_at(smem + 4128)[0] = boot_done(p.ws, bt); tag = next_tag(bt.e); }
    LBAR();
  }
  const unsigned pm = uni(lds_at(smem + 4128)[0]);
  p.s0 = pm >> 31;
  const int hs = side(pm, 1);
  if (((j >> 2) & 14) != 6 && GPF == 2) {
#pragma unroll
    for (int i = 0; i < 4; ++i)
      touch_line((const char*)(i < 2 ? p.wg : p.wu) + (size_t)(j - (j >= 32 ? 8 : 0)) * 131072 + ((i & 1) * 8 + w) * 8192 + lane * 128, smem + w * 256);
  }
  {
    const u32x4 ds = {DSENT, DSENT, DSENT, DSENT};
#pragma unroll
    for (int i = 0; i < 2; ++i) {
      const int s = (w * 2 + i) * 4 + (lane >> 4);
      if (s >= 2) st_sc1(rsy, o_mem(pm, s) + (c >> 4) * 16384 + (c & 15) * 512 + rh * 128 + ((lane >> 3) & 1) * 256 + (lane & 7) * 16, ds);
    }
  }
  if (tid < 4) { reinterpret_cast<unsigned*>(smem + 4096)[tid] = tid == 3 ? now_q() : 0; reinterpret_cast<unsigned*>(smem + 4112)[tid] = DSENT; }
  if (tid < 256) reinterpret_cast<int*>(smem + 5120)[tid] = ((tid >> 2) - TLAG - 6) >> 2;
  LBAR();
  const unsigned base = __builtin_amdgcn_readfirstlane(lds_at(smem + 4096)[3]);
  RDY(64 + j);
  unsigned long long fm = 0, ps = 0;
  const unsigned aL = lane * 16;
  const int e = ((56 + w) * SEG + rh * 16 + (lane >> 2)) * DM + c * 32 + (lane & 3) * 8;
  u32x4 xv = *reinterpret_cast<const u32x4*>(p.x + e), av = {DSENT, DSENT, DSENT, DSENT};
  asm volatile("" : "+v"(xv));
  {
    const int r = lane & 15, q = lane >> 4;
    const int sm0 = o_mem(pm, 0) + (c >> 4) * 16384 + (c & 15) * 512 + rh * 128;
    u32x4 VO, VT;
    unsigned fo = SY_FO + ((rh + (lane & 4) / 2) * NH + 4 * hg + (lane & 3)) * 128;
    unsigned ao = w == 1 ? (c * S_LEN + rh * 16 - (TLAG + 3) * SEG) * 64 + lane * 16
                         : (w >= 4 ? SY_O + NPRE * 131072 + side(pm, w - 2) + (j >> 3) * 16384 + (j & 7) * 1024 + lane * 16
                                   : SY_FS + (2 * c + rh) * 128 + (lane < 8 ? lane * 16 : 0x70000000));
    unsigned ar = w == 1 ? R_SI - (TLAG + 2) * 1024 + lane * 16 : (w == 2 ? 5120 : 4112);
    const unsigned long long am = w == 1 ? (1ull << 62) - 64 : (w == 2 ? 0x4444444444444400ull : (w >= 4 ? (0xFFFFull << (16 * (w - 4))) >> NPRE : 0));
    static_assert(TLAG == 3, "");
    unsigned pw = R_PART + w * 2048 + lane * 16, pr = R_PART + 16384 + tid * 4;
    unsigned sw = R_SI - (TLAG + 1) * 1024 + (lane >> 2) * 64 + w * 8 + (lane & 3) * 2, stm = 8192 + w * 512;
#pragma unroll
    for (int l = 0; l < 4; ++l) {
      const int x = (l + rot) & 3;
      VO[l] = o_o(0, 0, 4 * hg) + rh * 65536 + x * 1024 + aL;
      VT[l] = (T_O + rh * 16 + 2 * hg + (x >> 1)) * 16384 + (x & 1) * 2048 + aL;
    }
    int rl = 280;
    unsigned step, E, so, st, st4, sh;
    asm volatile(RED_ASM
        : [step] "=&s"(step), [E] "=&s"(E), [so] "=&s"(so), [st] "=&s"(st), [st4] "=&s"(st4), [sh] "=&s"(sh),
          [rl] "+s"(rl), [fm] "+s"(fm), [ps] "+s"(ps),
          "+{v[224:227]}"(av), "+{v228}"(ao), "+{v229}"(ar), "+{v230}"(pw), "+{v231}"(pr), "+{v232}"(sw), "+{v233}"(stm), "+{v242}"(fo)
        : W4_IN, "{v168}"(aL), "{v169}"(w * 512 + lane * 8), "{v170}"(lane * 8),
          "{v171}"(sm0 + 32768 + (q >> 1) * 256 + (q & 1) * 64 + ((r >> 1) & 1) * 32 + (r & 1) * 16 + (r >> 2) * 4),
          "{v172}"((lane & 2) ? 0xEEEE : 0x4444), "{v173}"(lane < 4 ? SY_BA + hs + (4 * hg + lane) * 128 : 0x7F000000u),
          "{v174}"((r & 1) ? 0x03020706u : 0x05040100u), "{v175}"(SY_BR + hs + j * 128 + lane * 8), "{v[234:237]}"(VO), "{v[238:241]}"(VT), "{v243}"(e * 2),
          [rg] "s"(raw_rsrc(p.ws + WS_G, S_LEN * DM * 2)), [fg] "s"(SY_FG + ((14 + (w >> 2)) * 8 + (c >> 2)) * 128),
          [rs] "s"(rs4), [ra] "s"(w == 1 ? raw_rsrc(p.ws + WS_S, S_LEN * DM * 2) : rs4), [rt] "s"(raw_rsrc(p.ws + WS_T, T_BYTES)),
          [ds] "n"(DS_RED), [mn] "n"(RED_MIN), [lk] "n"(DS_MEM + BA_LOOK), [up] "n"(RL_UP), [dn] "n"(-RL_DN), [sent] "s"(DSENT), [sab] "s"(SY_ABORT),
          [base] "s"(base), [pm] "s"(pm), [s0] "s"((p.s0 ^ TF_O) << 13), [am] "s"(am), [ainc] "s"(w == 1 ? SEG * 64 : (w >= 4 ? 131072 : 0)),
          [rinc] "s"(w == 1 ? 1024 : (w == 2 ? 16 : 0))
        : CLB32(128), CLB8(160, 161, 162, 163, 164, 165, 166, 167), CLB8(176, 177, 178, 179, 180, 181, 182, 183),
          CLB8(184, 185, 186, 187, 188, 189, 190, 191), CLB8(192, 193, 194, 195, 196, 197, 198, 199), CLB8(200, 201, 202, 203, 204, 205, 206, 207),
          CLB8(208, 209, 210, 211, 212, 213, 214, 215), CLB8(216, 217, 218, 219, 220, 221, 222, 223), CLB_E, "s96", CLB_S);
  }
  if (j == 0 && w == 5) { LST(345); }
  LBAR();
  if (j == 0 && w == 5) { LST(346); }
  u32x4 sv;
  if (w < 3) sv = *reinterpret_cast<const u32x4*>(smem + R_SI + (56 + w) * 1024 + lane * 16);
  else {
    const f32x4* part = reinterpret_cast<const f32x4*>(smem + (w == 3 ? R_PART : (w - 3) * 16384)) +
                        (lane & 2) * 32 + (lane & 1) * 32 + (lane >> 2);
    f32x4 x[8][2];
#pragma unroll
    for (int ww = 0; ww < 8; ++ww) { x[ww][0] = part[ww * 128]; x[ww][1] = part[ww * 128 + 16]; }
#pragma unroll
    for (int u = 0; u < 2; ++u) {
      const f32x4 a = ((x[0][u] + x[1][u]) + (x[2][u] + x[3][u])) + ((x[4][u] + x[5][u]) + (x[6][u] + x[7][u]));
      sv[2 * u] = cvtpk(a[0], a[1]); sv[2 * u + 1] = cvtpk(a[2], a[3]);
    }
  }
  *reinterpret_cast<u32x4*>(p.out + e) = combine(sv, av, xv);
  if (j == 0 && tid == 0) st2_sc1(rsy, SY_EP, tag);
  if (tid == 128) { LST(400 + j); }
}

__global__ __launch_bounds__(512, 2) void main_kernel(Args p) {
  char* smem = g_smem;
  p.ws = ws_align(p.ws);
  const int bx = blockIdx.x & 7, bk = blockIdx.x >> 3;
  const bool isg = (bx ^ XPROBE) & 4;
  const int b = (isg ? NCHAIN : 0) + bk * 4 + (bx & 3);
  if (b == 64 && threadIdx.x == 0) { LST(500); }
  if (b >= NCHAIN) gemm_role(p, b - NCHAIN, smem);
  else if (b < 32) qk_role(p, (b & 3) * 8 + bk, smem);
  else if (b < 64) vu_role(p, (b & 3) * 8 + bk - 8, smem);
  else red_role(p, b - 64, smem);
}

extern "C" void kernel_launch(void* const* d_in, const int* in_sizes, int n_in,
                              void* d_out, int out_size, void* d_ws, size_t ws_size,
                              hipStream_t stream) {
  Args a;
  a.x = (const u16*)d_in[0]; a.mem0 = (const u16*)d_in[1];
  a.Wq = (const u16*)d_in[2]; a.Wk = (const u16*)d_in[3]; a.Wv = (const u16*)d_in[4]; a.Wo = (const u16*)d_in[5];
  a.wg = (const u16*)d_in[8]; a.wu = (const u16*)d_in[9]; a.wd = (const u16*)d_in[10];
  a.ws = (char*)d_ws; a.out = (u16*)d_out; a.s0 = 0; a.pad1 = 0;
  main_kernel<<<256, 512, 0, stream>>>(a);
}
```

```cpp
#include <hip/hip_runtime.h>
#include <hip/hip_bf16.h>
#include <stdint.h>
#include <type_traits>

constexpr int S_LEN = 2048, DM = 1024, NH = 32, DH = 128, NMEM = 8, SEG = 32;
constexpr int NSEG = S_LEN / SEG;
constexpr int HD = NH * DH;
constexpr int FF = 3584;

typedef unsigned short u16;
typedef __attribute__((ext_vector_type(8))) short bf16x8;
typedef __attribute__((ext_vector_type(4))) short s16x4;
typedef __attribute__((ext_vector_type(4))) float f32x4;
typedef __attribute__((ext_vector_type(4))) unsigned int u32x4;
typedef __attribute__((ext_vector_type(2))) unsigned int u32x2;
typedef __attribute__((address_space(3))) s16x4* lds_s16x4_ptr;
typedef __attribute__((address_space(3))) void* lds_void_ptr;
typedef __attribute__((address_space(1))) const void* glb_cvoid_ptr;
typedef __attribute__((address_space(1))) unsigned gu32;
typedef __bf16 bf16x2_t __attribute__((ext_vector_type(2)));
typedef float f32x2_t __attribute__((ext_vector_type(2)));

__device__ __forceinline__ float bf2f(u16 v) { return __uint_as_float(((unsigned)v) << 16); }
__device__ __forceinline__ unsigned cvtpk(float lo, float hi) {
  bf16x2_t r = __builtin_convertvector((f32x2_t){lo, hi}, bf16x2_t);
  return __builtin_bit_cast(unsigned, r);
}
__device__ __forceinline__ float lo_f(unsigned v) { return __uint_as_float(v << 16); }
__device__ __forceinline__ float hi_f(unsigned v) { return __uint_as_float(v & 0xffff0000u); }
__device__ __forceinline__ float rbf(float f) { return lo_f(cvtpk(f, f)); }
__device__ __forceinline__ bf16x8 as_bf16x8(u32x4 v) { return __builtin_bit_cast(bf16x8, v); }
#define MFMA(a, b, c) __builtin_amdgcn_mfma_f32_16x16x32_bf16((a), (b), (c), 0, 0, 0)
typedef __attribute__((ext_vector_type(16))) short bf16x16;
typedef __attribute__((ext_vector_type(8))) __bf16 bfv8;
typedef __attribute__((ext_vector_type(16))) __bf16 bfv16;
#define SMFMA(a, b, c, idx) __builtin_amdgcn_smfmac_f32_16x16x64_bf16(__builtin_bit_cast(bfv8, (a)), __builtin_bit_cast(bfv16, (b)), (c), (idx), 0, 0)
__device__ __forceinline__ bf16x16 cat16(bf16x8 lo, bf16x8 hi) {
  return __builtin_shufflevector(lo, hi, 0, 1, 2, 3, 4, 5, 6, 7, 8, 9, 10, 11, 12, 13, 14, 15);
}

#define KSTR 65
#define XLD 1024
constexpr int PAGE = 2 << 20, NPAGE = 14;
constexpr int T_K = 0, T_Q = 2304, T_V = 4608, T_O = 6912, T_END = T_O + 2048;
constexpr unsigned T_BYTES = T_END * 16384u;
constexpr size_t WS_SY = 0;
constexpr size_t WS_T = (size_t)NPAGE * PAGE;
constexpr size_t WS_XN = WS_T + T_BYTES;
constexpr size_t WS_HF = WS_XN + (size_t)S_LEN * XLD * 2;
constexpr size_t WS_G = WS_HF + (size_t)S_LEN * 16384;
constexpr size_t WS_S = WS_G + (size_t)S_LEN * DM * 2;
constexpr size_t WS_END = WS_S + (size_t)S_LEN * DM * 2;
constexpr unsigned SENT = 0xFFFFFFFFu;
constexpr unsigned DSENT = 0xFFBFFFBFu;
constexpr int SY_MEM = 0;
constexpr int SY_O = 2 * PAGE;
constexpr int SY_V = 6 * PAGE;
constexpr int SY_BR = PAGE, SY_BA = SY_BR + 16384;
constexpr int SY_FKQ = SY_BR + 32768;
constexpr int SY_FG = SY_FKQ + NH * 4 * 128;
constexpr int SY_FO = SY_FG + 16 * 8 * 128;
constexpr int SY_CH = SY_FO + 384 * 128;
constexpr int SY_Q = SY_CH + 8 * 128;
constexpr int SY_ABORT = SY_Q + 8 * 128;
constexpr int SY_FS = SY_ABORT + 256;
constexpr int SY_FEND = SY_FS + 64 * 128;
constexpr int SY_EP = SY_FEND, SY_BT = SY_EP + 128;
constexpr int SY_TOTAL = NPAGE * PAGE;
constexpr int PROBE = PAGE - 16384 + 6144;
__device__ __forceinline__ int side(unsigned pm, int page) { return ((pm >> page) & 1u) << 13; }
__device__ __forceinline__ int o_mem(unsigned pm, int s) { return SY_MEM + s * 32768 + side(pm, 0); }
__device__ __forceinline__ int o_o(unsigned pm, int s, int h) {
  return SY_O + s * 131072 + (h >> 3) * 16384 + ((h >> 1) & 3) * 2048 + (h & 1) * 128 + side(pm, 2 + (s >> 4));
}
__device__ __forceinline__ int o_v(unsigned pm, int s, int h) { return SY_V + s * 262144 + (h >> 1) * 16384 + (h & 1) * 4096 + side(pm, 6 + (s >> 3)); }
__device__ __forceinline__ char* ws_align(char* ws) { return ws + ((PAGE - (unsigned)(uintptr_t)ws) & (PAGE - 1)); }
#define NFUSE 12
constexpr int NPRE = 4;
static_assert(WS_END + PAGE <= (size_t)256 << 20 && NH * KSTR <= T_Q && NPAGE % 2 == 0, "");
constexpr int NCHAIN = 128, NGW = 32;
__host__ __device__ constexpr int kseg(int sg, int h) { return h * KSTR + sg; }
__host__ __device__ constexpr int fkq(int h, int kind) { return SY_FKQ + (h * 4 + kind) * 128; }

#define HSKEW 0
#define XPROBE 4
struct Args {
  const u16 *x, *mem0, *Wq, *Wk, *Wv, *Wo, *wg, *wu, *wd;
  char* ws;
  u16* out;
  int s0, pad1;
};
constexpr int HLD = 16384 + HSKEW;
__device__ __forceinline__ int h_off(const Args& p, int rblk) { return rblk * PAGE + (((p.s0 ^ rblk ^ 1) & 1) << 13); }
static_assert(WS_HF % (2 * PAGE) == 0 && 127 * HSKEW + FF * 2 <= 8192, "");
#define TF_KQV 1
#define TF_O 0
__device__ __forceinline__ int t_off(const Args& p, int t) { return t * 16384 + (((p.s0 ^ (t >> 7) ^ (TF_KQV == 2 ? t : TF_KQV)) & 1) << 13); }
__device__ __forceinline__ int t_off_o(const Args& p, int t) { return t * 16384 + (((p.s0 ^ (t >> 7) ^ (TF_O == 2 ? t : TF_O)) & 1) << 13); }

__device__ __forceinline__ float wave_sum(float v) {
#pragma unroll
  for (int i = 0; i < 4; ++i) {
    const int x = (int)__float_as_uint(v);
    v += __uint_as_float(i == 0 ? __builtin_amdgcn_update_dpp(0, x, 0x128, 0xf, 0xf, false) : i == 1 ? __builtin_amdgcn_update_dpp(0, x, 0x124, 0xf, 0xf, false)
                         : i == 2 ? __builtin_amdgcn_update_dpp(0, x, 0x122, 0xf, 0xf, false) : __builtin_amdgcn_update_dpp(0, x, 0x121, 0xf, 0xf, false));
  }
  auto r = __builtin_amdgcn_permlane16_swap(__float_as_uint(v), __float_as_uint(v), false, false);
  v = __uint_as_float(r[0]) + __uint_as_float(r[1]);
  r = __builtin_amdgcn_permlane32_swap(__float_as_uint(v), __float_as_uint(v), false, false);
  return __uint_as_float(r[0]) + __uint_as_float(r[1]);
}

__shared__ __attribute__((aligned(16))) char g_smem[163840];
__device__ __forceinline__ unsigned lds_off(const void* p) {
  return (unsigned)(uintptr_t)(lds_void_ptr)g_smem + (unsigned)((const char*)p - g_smem);
}
__device__ __forceinline__ __amdgpu_buffer_rsrc_t mkrsrc(const void* p, unsigned bytes) {
  return __builtin_amdgcn_make_buffer_rsrc((void*)p, 0, bytes, 0x00020000);
}
__device__ __forceinline__ u32x4 ld_sc1(__amdgpu_buffer_rsrc_t rs, int off) {
  return __builtin_amdgcn_raw_buffer_load_b128(rs, off, 0, 16);
}
__device__ __forceinline__ void st_sc1(__amdgpu_buffer_rsrc_t rs, int off, u32x4 v) {
  __builtin_amdgcn_raw_buffer_store_b128(v, rs, off, 0, 16);
}
__device__ __forceinline__ unsigned ror8(unsigned v) {
  return (unsigned)__builtin_amdgcn_update_dpp(0, (int)v, 0x128, 0xf, 0xf, false);
}
__device__ __forceinline__ float ror8f(float v) { return __uint_as_float(ror8(__float_as_uint(v))); }
__device__ __forceinline__ void swap16(unsigned& x, unsigned& y) {
  auto r = __builtin_amdgcn_permlane16_swap(x, y, false, false);
  x = r[0]; y = r[1];
}
__device__ __forceinline__ void swap32(unsigned& x, unsigned& y) {
  auto r = __builtin_amdgcn_permlane32_swap(x, y, false, false);
  x = r[0]; y = r[1];
}
__device__ __forceinline__ f32x4 fold32(f32x4 x, f32x4 y) {
  f32x4 o;
#pragma unroll
  for (int j = 0; j < 4; ++j) {
    unsigned a = __float_as_uint(x[j]), b = __float_as_uint(y[j]);
    swap32(a, b);
    o[j] = __uint_as_float(a) + __uint_as_float(b);
  }
  return o;
}
__device__ __forceinline__ bf16x8 tr_frag(const char* img_lane) {
  s16x4 lo = __builtin_amdgcn_ds_read_tr16_b64_v4i16((lds_s16x4_ptr)img_lane);
  s16x4 hi = __builtin_amdgcn_ds_read_tr16_b64_v4i16((lds_s16x4_ptr)(img_lane + 64));
  return (bf16x8){lo[0], lo[1], lo[2], lo[3], hi[0], hi[1], hi[2], hi[3]};
}
__device__ __forceinline__ float rowq_max(float v) {
  unsigned a = __float_as_uint(v), b = a;
  swap16(a, b);
  v = __builtin_elementwise_maximum(__uint_as_float(a), __uint_as_float(b));
  a = __float_as_uint(v); b = a;
  swap32(a, b);
  return __builtin_elementwise_maximum(__uint_as_float(a), __uint_as_float(b));
}
__device__ __forceinline__ float rowq_sum(float v) {
  unsigned a = __float_as_uint(v), b = a;
  swap16(a, b);
  v = __uint_as_float(a) + __uint_as_float(b);
  a = __float_as_uint(v); b = a;
  swap32(a, b);
  return __uint_as_float(a) + __uint_as_float(b);
}
__device__ __forceinline__ bool spin_fail(unsigned& spins, gu32* abortw) {
  ++spins;
  if ((spins & 63u) == 0u) {
    const __amdgpu_buffer_rsrc_t ra = mkrsrc((const void*)abortw, 4);
    unsigned z = 0;
    asm volatile("" : "+v"(z));
    if (__builtin_amdgcn_readfirstlane(__builtin_amdgcn_raw_buffer_load_b32(ra, z, 0, 16)) == 1u) return true;
    if (spins > (1u << 19)) {
      __builtin_amdgcn_raw_buffer_store_b32(z + 1u, ra, z, 0, 16);
      return true;
    }
  }
  return false;
}
__device__ __forceinline__ unsigned umax4(u32x4 v) { return max(max(v[0], v[1]), max(v[2], v[3])); }
__device__ __forceinline__ bool has_ds(u32x4 v) { return (v[0] == DSENT) | (v[1] == DSENT) | (v[2] == DSENT) | (v[3] == DSENT); }
__device__ __forceinline__ float fmax2(float a, float b) { return __builtin_elementwise_maximum(a, b); }
__device__ __forceinline__ bf16x8 ldsf(const char* p) { return *reinterpret_cast<const bf16x8*>(p); }
#define SCHED() __builtin_amdgcn_sched_barrier(0)
#define LBAR() do { asm volatile("s_waitcnt lgkmcnt(0)" ::: "memory"); __builtin_amdgcn_s_barrier(); asm volatile("" ::: "memory"); } while (0)
#define WAIT_V(n) asm volatile("s_waitcnt vmcnt(%0)" :: "n"(n) : "memory")

__device__ __forceinline__ void wait_flags(__amdgpu_buffer_rsrc_t rs, int off, gu32* abortw) {
  for (unsigned spins = 0;;) {
    asm volatile("" ::: "memory");
    const unsigned v = __builtin_amdgcn_raw_buffer_load_b32(rs, off, 0, 16);
    if (__all(v != SENT)) break;
    __builtin_amdgcn_s_sleep(4);
    if (spin_fail(spins, abortw)) break;
  }
  asm volatile("" ::: "memory");
}

#define LST(i) do {} while (0)
#define RDY(i) do {} while (0)
#define RDW(i) do {} while (0)
#define SMP(k, s, v) do {} while (0)
#define LSTC(i) do {} while (0)
__device__ __forceinline__ unsigned offA(unsigned row, unsigned ch) {
  return row * 128u + 16u * (ch ^ (row & 7u));
}
__device__ __forceinline__ unsigned swzB(unsigned row) { return ((row & 3u) << 2) | ((row >> 2) & 3u); }
__device__ __forceinline__ unsigned offB(unsigned row, unsigned ch) {
  return 256u * row + 16u * (ch ^ swzB(row));
}
__device__ __forceinline__ void glds16(const u16* src, char* dst) {
  const unsigned d = __builtin_amdgcn_readfirstlane(lds_off(dst));
  unsigned keep;
  asm volatile("s_mov_b32 %0, m0\n\ts_mov_b32 m0, %2\n\ts_nop 0\n\tglobal_load_lds_dwordx4 %1, off\n\ts_mov_b32 m0, %0"
               : "=&s"(keep) : "v"(src), "s"(d) : "memory");
}
__device__ __forceinline__ bf16x8 ld_bfrag(const char* sb, int ks, int c, int lane) {
  const unsigned g = lane >> 4, qq = (lane & 15) >> 2, pp = lane & 3;
  s16x4 lo = __builtin_amdgcn_ds_read_tr16_b64_v4i16(
      (lds_s16x4_ptr)(sb + offB(32 * ks + 8 * g + qq, 2 * c + (pp >> 1)) + 8 * (pp & 1)));
  s16x4 hi = __builtin_amdgcn_ds_read_tr16_b64_v4i16(
      (lds_s16x4_ptr)(sb + offB(32 * ks + 8 * g + 4 + qq, 2 * c + (pp >> 1)) + 8 * (pp & 1)));
  return (bf16x8){lo[0], lo[1], lo[2], lo[3], hi[0], hi[1], hi[2], hi[3]};
}

__device__ __forceinline__ void blk_dma(const u16* src, size_t ld, char* slot, int lane) {
  const u16* s = src + (size_t)(lane >> 2) * ld + (lane & 3) * 8;
  glds16(s, slot);
  glds16(s + 16 * ld, slot + 1024);
}
__device__ __forceinline__ bf16x8 blk_frag(const char* slot, int T, int lane) {
  const char* a = slot + ((lane >> 4) * 8 + ((lane & 15) >> 2)) * 64 + (lane & 3) * 16 + T * 8;
  s16x4 lo = __builtin_amdgcn_ds_read_tr16_b64_v4i16((lds_s16x4_ptr)a);
  s16x4 hi = __builtin_amdgcn_ds_read_tr16_b64_v4i16((lds_s16x4_ptr)(a + 256));
  return (bf16x8){lo[0], lo[1], lo[2], lo[3], hi[0], hi[1], hi[2], hi[3]};
}
template <int NB, typename ISSUE, typename SINK>
__device__ __forceinline__ void load_blocks(char* slice, ISSUE&& issue, SINK&& sink) {
#pragma unroll
  for (int i = 0; i < 8; ++i) issue(i, slice + i * 2048);
#pragma unroll
  for (int g = 0; g < NB / 4; ++g) {
    if (g + 1 < NB / 4) WAIT_V(8); else WAIT_V(0);
#pragma unroll
    for (int j = 0; j < 4; ++j) sink(4 * g + j, slice + ((4 * g + j) & 7) * 2048);
    asm volatile("s_waitcnt lgkmcnt(0)" ::: "memory");
    if (g + 2 < NB / 4) {
#pragma unroll
      for (int j = 0; j < 4; ++j) issue(4 * g + 8 + j, slice + ((4 * g + j) & 7) * 2048);
    }
  }
}

__device__ __forceinline__ u32x4 widen32(unsigned a0, unsigned a1, unsigned b0, unsigned b1) {
  swap32(a0, b0); swap16(a0, b0);
  swap32(a1, b1); swap16(a1, b1);
  return (u32x4){a0, a1, b0, b1};
}

__device__ __forceinline__ void publish_flags(__amdgpu_buffer_rsrc_t rs, int off, int stride, int n) {
  WAIT_V(0);
  LBAR();
  int tid = threadIdx.x;
  asm volatile("" : "+v"(tid));
  if (tid < n) __builtin_amdgcn_raw_buffer_store_b32(1u, rs, off + tid * stride, 0, 16);
}

__device__ __forceinline__ void wait_s(const Args& p, __amdgpu_buffer_rsrc_t rsy, int rblk, int c0, int n, bool need) {
  if (need) {
    if (threadIdx.x < 64) {
      for (unsigned spins = 0;;) {
        asm volatile("" ::: "memory");
        const int f = (int)__builtin_amdgcn_raw_buffer_load_b32(rsy, SY_FS + (2 * c0 + (threadIdx.x & (2 * n - 1))) * 128, 0, 16);
        if (__all(f >= rblk)) break;
        __builtin_amdgcn_s_sleep(32);
        if (spin_fail(spins, (gu32*)(p.ws + WS_SY + SY_ABORT))) break;
      }
    }
    LBAR();
  }
}
__device__ __forceinline__ u32x4 combine(u32x4 sv, u32x4 g, u32x4 x) {
  u32x4 o;
#pragma unroll
  for (int j = 0; j < 4; ++j) {
    const unsigned t = cvtpk(lo_f(sv[j]) * lo_f(g[j]), hi_f(sv[j]) * hi_f(g[j]));
    o[j] = cvtpk(lo_f(x[j]) + lo_f(t), hi_f(x[j]) + hi_f(t));
  }
  return o;
}
#define OUT_AUX 16
__device__ __forceinline__ void st_out(const Args& p, int e, u32x4 v) {
  __builtin_amdgcn_raw_buffer_store_b128(v, mkrsrc(p.out, S_LEN * DM * 2), e * 2, 0, OUT_AUX);
}
struct SX { u32x4 s, x; };
__device__ __forceinline__ SX get_sx(const Args& p, int rblk, int e) {
  SX r; r.s = (u32x4){0u, 0u, 0u, 0u}; r.x = r.s;
  if (rblk < NFUSE) {
    r.s = ld_sc1(mkrsrc(p.ws + WS_S, S_LEN * DM * 2), (((e & 1023) >> 5) * S_LEN + (e >> 10)) * 64 + (e & 31) * 2);
    r.x = *reinterpret_cast<const u32x4*>(p.x + e);
  }
  return r;
}
__device__ __forceinline__ void put_g(const Args& p, __amdgpu_buffer_rsrc_t rg, int rblk, int e, u32x4 g, const SX& v) {
  if (rblk < NFUSE) {
    st_out(p, e, combine(v.s, g, v.x));
  } else st_sc1(rg, e * 2, g);
}

__device__ __forceinline__ void job_comb(const Args& p, int rblk, int ct, __amdgpu_buffer_rsrc_t rsy) {
  int tid = threadIdx.x;
  asm volatile("" : "+v"(tid));
  u32x4 xv[8];
#pragma unroll
  for (int i = 0; i < 8; ++i) xv[i] = *reinterpret_cast<const u32x4*>(p.x + (rblk * 128 + i * 16 + (tid >> 5)) * DM + ct * 256 + (tid & 31) * 8);
  if (tid < 64) wait_flags(rsy, SY_FG + (rblk * 8 + 2 * ct + (tid & 1)) * 128, (gu32*)(p.ws + WS_SY + SY_ABORT));
  wait_s(p, rsy, rblk, ct * 8, 8, true);
  u32x4 sv[8], gv[8];
#pragma unroll
  for (int i = 0; i < 8; ++i) {
    const int e = (rblk * 128 + i * 16 + (tid >> 5)) * DM + ct * 256 + (tid & 31) * 8;
    sv[i] = ld_sc1(mkrsrc(p.ws + WS_S, S_LEN * DM * 2), (((e & 1023) >> 5) * S_LEN + (e >> 10)) * 64 + (e & 31) * 2);
    gv[i] = ld_sc1(mkrsrc(p.ws + WS_G, S_LEN * DM * 2), e * 2);
  }
#pragma unroll
  for (int i = 0; i < 8; ++i)
    st_out(p, (rblk * 128 + i * 16 + (tid >> 5)) * DM + ct * 256 + (tid & 31) * 8, combine(sv[i], gv[i], xv[i]));
}

__device__ __forceinline__ u32x4 raw_rsrc(const void* p, unsigned bytes) {
  const unsigned long long a = (unsigned long long)p;
  return (u32x4){(unsigned)a, (unsigned)(a >> 32) & 0xffffu, bytes, 0x00020000u};
}
__device__ __forceinline__ u32x2 next_tag(u32x2 e) { return (u32x2){e[0] + 1u, e[1] + (e[0] == 0xFFFFFFFFu)}; }
struct Boot { u32x2 e; u32x4 c; };
__device__ __forceinline__ void boot_ask(const char* ws, Boot& r) {
  const unsigned o = (threadIdx.x & 63) * 128;
  asm volatile("buffer_load_dwordx2 %0, off, %3, %4 sc1\n\tbuffer_load_dwordx4 %1, %2, %3, %5 offen sc1"
               : "=&v"(r.e), "=&v"(r.c) : "v"(o), "s"(raw_rsrc(ws, SY_TOTAL)), "s"(SY_EP), "s"(SY_BT) : "memory");
}
__device__ __forceinline__ void wait_rows(const char* ws, u32x2 tag) {
  const __amdgpu_buffer_rsrc_t rs = mkrsrc(ws, SY_TOTAL);
  int o = (threadIdx.x & 63) * 128;
  asm volatile("" : "+v"(o));
#pragma unroll 1
  for (int i = 0; i < (1 << 17); ++i) {
    asm volatile("" ::: "memory");
    const u32x2 a = __builtin_amdgcn_raw_buffer_load_b64(rs, o, SY_BT + 32 * 128, 16), b = __builtin_amdgcn_raw_buffer_load_b64(rs, o, SY_BT + 96 * 128, 16);
    if (__all(a[0] == tag[0] && a[1] == tag[1] && b[0] == tag[0] && b[1] == tag[1])) break;
    __builtin_amdgcn_s_sleep(8);
  }
  asm volatile("" ::: "memory");
}
__device__ __forceinline__ unsigned boot_done(const char* ws, Boot& r, bool own = false, u32x2 tag = (u32x2){0u, 0u}) {
#pragma unroll 1
  for (int i = 0; i < (1 << 17); ++i) {
    asm volatile("" : "+v"(r.e), "+v"(r.c));
    if (!own) tag = next_tag(r.e);
    if (__all(r.c[0] == tag[0] && r.c[1] == tag[1])) break;
    __builtin_amdgcn_s_sleep(8);
    boot_ask(ws, r);
    WAIT_V(0);
  }
  unsigned q0[NPAGE], q1[NPAGE], ga = 0xFFFFu, gb = 0xFFFFu;
#pragma unroll
  for (int i = 0; i < NPAGE; ++i) {
    const unsigned x = __builtin_amdgcn_readlane(r.c[2], i), y = __builtin_amdgcn_readlane(r.c[2], i + NPAGE);
    q0[i] = min(x >> 16, y >> 16); q1[i] = min(x & 0xFFFFu, y & 0xFFFFu);
    ga = min(ga, (i & 1) ? q1[i] : q0[i]); gb = min(gb, (i & 1) ? q0[i] : q1[i]);
  }
  unsigned m = gb < ga ? 0x80001555u : 0x2AAAu;
  const unsigned thr = min(ga, gb) + 110;
#pragma unroll
  for (int i = 0; i < NPAGE; ++i) {
    const unsigned lo = min(q0[i], q1[i]), hi = max(q0[i], q1[i]);
    const unsigned keep = (lo < thr ? hi : 0u) > lo + 100 ? ~(1u << i) : ~0u;
    m = (m & keep) | ((q1[i] < q0[i] ? ~0u : 0u) & ~keep);
  }
  return m;
}
typedef __attribute__((address_space(3))) volatile unsigned* lds_vu_ptr;
__device__ __forceinline__ lds_vu_ptr lds_at(const void* p) {
  unsigned a = lds_off(p);
  asm volatile("" : "+v"(a));
  return (lds_vu_ptr)(uintptr_t)a;
}
__device__ __forceinline__ unsigned uni(unsigned v) {
  unsigned r;
  asm volatile("s_nop 1\n\tv_readfirstlane_b32 %0, %1\n\ts_nop 3" : "=s"(r) : "v"(v));
  return r;
}
__device__ __forceinline__ void start_sy(char* ws, const u16* mem0, int i, char* smem) {
  const int tid = threadIdx.x, lane = tid & 63, w = __builtin_amdgcn_readfirstlane(tid >> 6);
  const __amdgpu_buffer_rsrc_t rsy = mkrsrc(ws, SY_TOTAL);
  const __amdgpu_buffer_rsrc_t rs = mkrsrc(ws + (size_t)(i % NPAGE) * PAGE + PROBE - (i / NPAGE) * 2048, 16384);
  const u32x2 e = __builtin_amdgcn_raw_buffer_load_b64(rsy, 0, SY_EP, 16);
  const u32x4 ds = {DSENT, DSENT, DSENT, DSENT};
  if (lane < 32) st_sc1(rs, (lane >> 4) * 8192 + w * 256 + (lane & 15) * 16, ds);
  static_assert(NPRE == 4 && SY_FEND - SY_BR <= 32 * 6144, "");
#pragma unroll
  for (int u = 0; u < 6; ++u)
    st_sc1(rsy, (u < 4 ? SY_V + (i * 4 + u) * 8192 : SY_O + (i * 2 + u - 4) * 8192) + tid * 16, ds);
  if (w == 7) {
    const int n = i * 64 + lane, j = (n >> 10) * 512 + (n & 511);
    const unsigned* m0 = reinterpret_cast<const unsigned*>(mem0 + (2 * ((j >> 2) & 3) + (j & 1)) * DM + (j >> 4) * 16) + ((j >> 1) & 1);
    u32x4 m;
#pragma unroll
    for (int k = 0; k < 4; ++k) { const unsigned t = m0[2 * k]; m[k] = t == DSENT ? 0xFFFFFFFFu : t; }
    st_sc1(rsy, n * 16, m);
  } else if (w == 6) st_sc1(rsy, 32768 + i * 1024 + lane * 16, ds);
  else {
    const int o = SY_BR + i * 6144 + tid * 16;
    st_sc1(rsy, o < SY_FEND ? o : 0x7F000000, (u32x4){SENT, SENT, SENT, SENT});
  }
  unsigned t[4];
#pragma unroll
  for (int k = 0; k < 4; ++k) {
    unsigned long long c0, c1; unsigned v, off = ((k ^ (k >> 1)) & 1) * 8192 + w * 256 + (k >> 1) * 128;
    asm volatile("s_waitcnt vmcnt(0)\n\ts_barrier\n\ts_memtime %0\n\ts_waitcnt lgkmcnt(0)\n\tbuffer_load_dword %2, %3, %4, 0 offen sc1\n\ts_waitcnt vmcnt(0)\n\t"
                 "s_memtime %1\n\ts_waitcnt lgkmcnt(0)" : "=&s"(c0), "=&s"(c1), "=&v"(v) : "v"(off), "s"(rs) : "memory");
    t[k] = (unsigned)(c1 - c0);
  }
  lds_vu_ptr tm = lds_at(smem);
  if (lane == 0) { tm[w] = min(t[0], t[3]); tm[8 + w] = min(t[1], t[2]); }
  int ro = SY_BT + i * 128 + lane * 16;
  asm volatile("" : "+v"(ro));
  LBAR();
  RDY(384 + i);
  if (threadIdx.x < 8) {
    unsigned m0 = tm[0], m1 = tm[8];
#pragma unroll
    for (int k = 1; k < 8; ++k) { m0 = min(m0, tm[k]); m1 = min(m1, tm[8 + k]); }
    const u32x2 tag = next_tag(e);
    st_sc1(rsy, ro, (u32x4){tag[0], tag[1], min(m0, 30000u) << 16 | min(m1, 30000u), 0u});
  }
}
template <int NB, typename F>
__device__ __forceinline__ u32x2 start_rows(const Args& p, int g, F&& between) {
  const int tid = threadIdx.x, lane = tid & 63, row0 = g * 16 + (tid >> 6) * 2;
  u32x4 a[4];
  u32x2 e;
  asm volatile("global_load_dwordx4 %0, %5, off\n\tglobal_load_dwordx4 %1, %5, off offset:1024\n\tglobal_load_dwordx4 %2, %5, off offset:2048\n\t"
               "global_load_dwordx4 %3, %5, off offset:3072\n\tbuffer_load_dwordx2 %4, off, %6, %7 sc1"
               : "=&v"(a[0]), "=&v"(a[1]), "=&v"(a[2]), "=&v"(a[3]), "=&v"(e)
               : "v"(p.x + (size_t)row0 * DM + lane * 8), "s"(raw_rsrc(p.ws, SY_TOTAL)), "s"(SY_EP) : "memory");
  between();
  asm volatile("s_waitcnt vmcnt(%5)" : "+v"(a[0]), "+v"(a[1]), "+v"(a[2]), "+v"(a[3]), "+v"(e) : "n"(NB) : "memory");
  RDY(512 + g);
  const __amdgpu_buffer_rsrc_t ro = mkrsrc(p.ws + WS_XN, S_LEN * XLD * 2);
#pragma unroll
  for (int u = 0; u < 2; ++u) {
    float v[16];
#pragma unroll
    for (int i = 0; i < 8; ++i) { v[2 * i] = lo_f(a[2 * u + (i >> 2)][i & 3]); v[2 * i + 1] = hi_f(a[2 * u + (i >> 2)][i & 3]); }
    float s = 0.f;
#pragma unroll
    for (int i = 0; i < 16; ++i) s += v[i];
    const float mean = wave_sum(s) * (1.0f / DM);
    float q = 0.f;
#pragma unroll
    for (int i = 0; i < 16; ++i) { float d = v[i] - mean; q += d * d; }
    const float var = rbf(wave_sum(q) * (1.0f / DM));
    const float mb = rbf(mean);
    const float inv = 1.0f / rbf(sqrtf(rbf(var + 1e-5f)));
#pragma unroll
    for (int k = 0; k < 2; ++k) {
      u32x4 o;
#pragma unroll
      for (int i = 0; i < 4; ++i) {
        const unsigned t = cvtpk(v[8 * k + 2 * i] - mb, v[8 * k + 2 * i + 1] - mb);
        o[i] = cvtpk(lo_f(t) * inv, hi_f(t) * inv);
      }
      st_sc1(ro, (row0 + u) * (XLD * 2) + k * 1024 + lane * 16, o);
    }
  }
  return next_tag(e);
}
__device__ __forceinline__ void report_rows(const Args& p, int g, u32x2 tag) {
  RDY(256 + g);
  if (threadIdx.x < 8) st_sc1(mkrsrc(p.ws + WS_SY, SY_TOTAL), SY_BT + (32 + g) * 128 + threadIdx.x * 16, (u32x4){tag[0], tag[1], tag[0], tag[1]});
}

__device__ __forceinline__ unsigned ld_flag0(__amdgpu_buffer_rsrc_t rs, int off) {
  return __builtin_amdgcn_raw_buffer_load_b32(rs, off, 0, 16);
}
__device__ __forceinline__ void glds16_sc1(const char* src, char* dst) {
  const unsigned d = __builtin_amdgcn_readfirstlane(lds_off(dst));
  unsigned keep;
  asm volatile("s_mov_b32 %0, m0\n\ts_mov_b32 m0, %2\n\ts_nop 0\n\tglobal_load_lds_dwordx4 %1, off sc1\n\ts_mov_b32 m0, %0"
               : "=&s"(keep) : "v"(src), "s"(d) : "memory");
}

__device__ __forceinline__ void touch_line(const char* src, char* dst) {
  const unsigned d = __builtin_amdgcn_readfirstlane(lds_off(dst));
  unsigned keep;
  asm volatile("s_mov_b32 %0, m0\n\ts_mov_b32 m0, %2\n\ts_nop 0\n\tglobal_load_lds_dword %1, off sc1\n\ts_mov_b32 m0, %0"
               : "=&s"(keep) : "v"(src), "s"(d) : "memory");
}

#define GPF 1
#define GU_DELAY 0
#define Q_DELAY 8
#define PF_T0 400
#define PF_N 12
#define WREV 1
__device__ __forceinline__ void glds2(const char* base, int step, unsigned voff, unsigned dst) {
  unsigned keep;
  const char* base2 = base + step;
  asm volatile("s_mov_b32 %0, m0\n\ts_mov_b32 m0, %4\n\ts_nop 0\n\tglobal_load_lds_dwordx4 %1, %2\n\t"
               "s_add_u32 m0, m0, 0x2000\n\ts_nop 0\n\tglobal_load_lds_dwordx4 %1, %3\n\ts_mov_b32 m0, %0"
               : "=&s"(keep) : "v"(voff), "s"(base), "s"(base2), "s"(dst) : "memory", "scc");
}

__device__ __forceinline__ void gemm_stream(const char* A, int lda, const char* B, int ldb, int nk, char* smem, f32x4 (&acc)[2][4]) {
  int tid = threadIdx.x;
  asm volatile("" : "+v"(tid));
  const int lane = tid & 63, r = lane & 15, q = lane >> 4;
  const int wid = __builtin_amdgcn_readfirstlane(tid >> 6);
  const int wr = wid >> 1, wc = wid & 1;
  const unsigned va = (tid >> 3) * lda + 16 * ((tid & 7) ^ ((tid >> 3) & 7)), vb = (tid >> 4) * ldb + 16 * ((tid & 15) ^ swzB((tid >> 4) & 15));
  const unsigned lw = lds_off(smem) + wid * 1024;
  auto piece = [&](int kt, int i) {
    const int k = min(kt, nk - 1);
    const char* src = i < 2 ? A + k * 128 + i * 64 * lda : B + (size_t)(k * 64 + (i - 2) * 32) * ldb;
    unsigned keep;
    asm volatile("s_mov_b32 %0, m0\n\ts_mov_b32 m0, %3\n\ts_nop 0\n\tglobal_load_lds_dwordx4 %1, %2\n\ts_mov_b32 m0, %0"
                 : "=&s"(keep) : "v"(i < 2 ? va : vb), "s"(src), "s"(lw + (kt & 3) * 32768 + i * 8192) : "memory");
  };
  unsigned la[2], lb[4][2];
#pragma unroll
  for (int k = 0; k < 2; ++k) { la[k] = lds_off(smem) + offA(wr * 32 + r, k * 4 + q); asm volatile("" : "+v"(la[k])); }
#pragma unroll
  for (int n = 0; n < 4; ++n)
#pragma unroll
    for (int u = 0; u < 2; ++u) {
      lb[n][u] = lds_off(smem) + 16384 + offB(8 * q + 4 * u + (r >> 2), 2 * (wc * 4 + n) + ((r & 3) >> 1)) + 8 * (r & 1);
      asm volatile("" : "+v"(lb[n][u]));
    }
  typedef __attribute__((address_space(3))) const bf16x8* lds_frag_ptr;
  bf16x8 af[2][2], bf[2][4];
  auto ldA = [&](int bo, int ks, int m) { return *(lds_frag_ptr)(uintptr_t)(la[ks] + bo + m * 2048); };
  auto ldB = [&](int bo, int ks, int n) {
    s16x4 lo = __builtin_amdgcn_ds_read_tr16_b64_v4i16((lds_s16x4_ptr)(uintptr_t)(lb[n][0] + bo + ks * 8192));
    s16x4 hi = __builtin_amdgcn_ds_read_tr16_b64_v4i16((lds_s16x4_ptr)(uintptr_t)(lb[n][1] + bo + ks * 8192));
    return (bf16x8){lo[0], lo[1], lo[2], lo[3], hi[0], hi[1], hi[2], hi[3]};
  };
#pragma unroll
  for (int i = 0; i < 8; ++i) acc[i >> 2][i & 3] = (f32x4){0.f, 0.f, 0.f, 0.f};
  LBAR();
#pragma unroll
  for (int i = 0; i < 10; ++i) piece(i >> 2, i & 3);
  WAIT_V(6);
  LBAR();
#pragma unroll
  for (int m = 0; m < 2; ++m) af[0][m] = ldA(0, 0, m);
#pragma unroll
  for (int n = 0; n < 4; ++n) bf[0][n] = ldB(0, 0, n);
  auto loop = [&](auto HF) {
    for (int t0 = 0; t0 < nk; t0 += 4) {
#pragma unroll
      for (int b = 0; b < 4; ++b) {
        const int t = t0 + b;
#pragma unroll
        for (int ks = 0; ks < 2; ++ks) {
          if (ks == 1) {
            WAIT_V(4);
            __builtin_amdgcn_s_barrier();
          }
          const int nb = (ks ? (b + 1) & 3 : b) * 32768, nks = ks ^ 1;
#pragma unroll
          for (int n = 0; n < 4; ++n) {
            acc[0][n] = MFMA(bf[ks][n], af[ks][0], acc[0][n]);
            bf[nks][n] = ldB(nb, nks, n);
            acc[1][n] = MFMA(bf[ks][n], af[ks][1], acc[1][n]);
            if (n < 2) af[nks][n] = ldA(nb, nks, n);
            SCHED();
            if ((n & 1) == HF) { piece(t + 2 + ks, (n >> 1) + 2 * nks); SCHED(); }
          }
        }
      }
    }
  };
  if (wid >> 2) loop(std::integral_constant<int, 1>()); else loop(std::integral_constant<int, 0>());
  WAIT_V(0);
}

#define HSL 40
__device__ __forceinline__ void wait_h(const Args& p, int panel) {
  if (threadIdx.x == 0) {
    gu32* cnt = (gu32*)(p.ws + WS_SY + SY_CH) + panel * 32;
    for (unsigned spins = 0;;) {
      if (__hip_atomic_load(cnt, __ATOMIC_RELAXED, __HIP_MEMORY_SCOPE_AGENT) + 1u >= 28u) break;
      __builtin_amdgcn_s_sleep(HSL);
      if (++spins > (1u << 20)) { __hip_atomic_store((gu32*)(p.ws + WS_SY + SY_ABORT), 1u, __ATOMIC_RELAXED, __HIP_MEMORY_SCOPE_AGENT); break; }
    }
    __builtin_amdgcn_fence(__ATOMIC_ACQUIRE, "agent");
    asm volatile("s_waitcnt vmcnt(0)" ::: "memory");
  }
  __syncthreads();
}
__device__ __forceinline__ void job_down(const Args& p, int rblk, int ct, char* smem, __amdgpu_buffer_rsrc_t rsy) {
  wait_h(p, rblk >> 1);
  f32x4 acc[2][4];
  gemm_stream(p.ws + WS_HF + h_off(p, rblk), HLD, (const char*)(p.wd + ct * 128), DM * 2, FF / 64, smem, acc);
  wait_s(p, rsy, rblk, ct * 4, 4, rblk < NFUSE);
  int tid = threadIdx.x;
  asm volatile("" : "+v"(tid));
  const int lane = tid & 63, wid = tid >> 6, wr = wid >> 1, wc = wid & 1;
  const __amdgpu_buffer_rsrc_t ro = mkrsrc(p.ws + WS_G, S_LEN * DM * 2);
  SX sx[4];
#pragma unroll
  for (int i = 0; i < 4; ++i)
    sx[i] = get_sx(p, rblk, (rblk * 128 + wr * 32 + (i >> 1) * 16 + (lane & 15)) * DM + ct * 128 + wc * 64 + (i & 1) * 32 + (lane >> 4) * 8);
#pragma unroll
  for (int m = 0; m < 2; ++m) {
    const int row = rblk * 128 + wr * 32 + m * 16 + (lane & 15);
#pragma unroll
    for (int nb = 0; nb < 2; ++nb) {
      unsigned pk[2][2];
#pragma unroll
      for (int n = 0; n < 2; ++n) {
        float rr[4];
#pragma unroll
        for (int j = 0; j < 4; ++j) { float d = rbf(acc[m][2 * nb + n][j]); rr[j] = d * __builtin_amdgcn_rcpf(1.0f + __expf(-d)); }
        pk[n][0] = cvtpk(rr[0], rr[1]); pk[n][1] = cvtpk(rr[2], rr[3]);
      }
      u32x4 o = widen32(pk[0][0], pk[0][1], pk[1][0], pk[1][1]);
      put_g(p, ro, rblk, row * DM + ct * 128 + wc * 64 + nb * 32 + (lane >> 4) * 8, o, sx[2 * m + nb]);
    }
  }
  publish_flags(rsy, SY_FG + (rblk * 8 + ct) * 128, 0, 1);
}

constexpr int W_X = 131072, W_PM = W_X + 16384;
template <bool ISV>
__device__ __forceinline__ void job_proj(Args& p, int g, int which, int h, char* smem, __amdgpu_buffer_rsrc_t rsy) {
  const int tid = threadIdx.x, lane = tid & 63, r = lane & 15, q = lane >> 4;
  const int w = __builtin_amdgcn_readfirstlane(tid >> 6), kh = w >> 2, cg = w & 3;
  bf16x8 W[16][2];
  const unsigned lw = lds_off(smem) + w * 4096;
  unsigned vx[4];
  u32x2 tag;
  const char* xn = p.ws + WS_XN;
  auto row = [&](int t, int i) {
    const char* src = xn + (size_t)(min(t, 127) >> 1) * (SEG * XLD * 2) + (t & 1) * 512;
    unsigned keep;
    asm volatile("s_mov_b32 %0, m0\n\ts_mov_b32 m0, %3\n\ts_nop 0\n\tglobal_load_lds_dwordx4 %1, %2 sc1\n\ts_mov_b32 m0, %0"
                 : "=&s"(keep) : "v"(vx[i]), "s"(src), "s"(lw + (t & 3) * 32768 + i * 1024) : "memory");
  };
  {
    const u16* Wm = which == 0 ? p.Wk : p.Wq;
    asm volatile("" : "+s"(Wm));
    if (ISV) Wm = p.Wv;
    const unsigned vo = (tid >> 4) * (HD * 2) + 16 * ((lane & 15) ^ swzB((tid >> 4) & 15));
    auto stage = [&](int j) {
      const char* src = (const char*)(Wm + (size_t)(WREV ? 7 - j : j) * 64 * HD + h * DH);
      const unsigned dst = lds_off(smem) + (j & 3) * 32768 + w * 1024;
      glds2(src, 32 * HD * 2, vo, dst);
      glds2(src + 512 * HD * 2, 32 * HD * 2, vo, dst + 16384);
    };
    if (which == 1) { for (int z = 0; z < Q_DELAY; ++z) __builtin_amdgcn_s_sleep(16); }
    tag = start_rows<16>(p, 32 + g, [&]() { stage(0); stage(1); stage(2); stage(3); });
#pragma unroll
    for (int i = 0; i < 4; ++i) vx[i] = ((4 * w + i) ^ (ISV ? 0 : (lane >> 1) & 16)) * (XLD * 2) + (lane >> 5) * 1024 + (((lane & 31) ^ ((4 * w + i) & 15)) << 4);
    Boot bt;
#pragma unroll
    for (int j = 0; j < 8; ++j) {
      if (j < 4) WAIT_V(16); else if (j == 4) WAIT_V(12); else if (j == 5) WAIT_V(8); else if (j == 6) WAIT_V(4); else WAIT_V(10);
      if (j == 6 && w == 0) lds_at(smem + W_PM)[0] = boot_done(p.ws, bt, true, tag);
      LBAR();
      if (j == 4) report_rows(p, 32 + g, tag);
      const char* sb = smem + (j & 3) * 32768 + kh * 16384;
#pragma unroll
      for (int k = 0; k < 4; ++k) {
        const int ks = k >> 1, a = k & 1;
        const unsigned ch = ISV ? 2 * (2 * cg + (a ^ kh)) + ((r & 3) >> 1) : 4 * cg + (r & 3), by = ISV ? 8 * (r & 1) : 8 * a;
        s16x4 lo = __builtin_amdgcn_ds_read_tr16_b64_v4i16((lds_s16x4_ptr)(sb + offB(32 * ks + 8 * q + (r >> 2), ch) + by));
        s16x4 hi = __builtin_amdgcn_ds_read_tr16_b64_v4i16((lds_s16x4_ptr)(sb + offB(32 * ks + 8 * q + 4 + (r >> 2), ch) + by));
        W[2 * (WREV ? 7 - j : j) + ks][a] = (bf16x8){lo[0], lo[1], lo[2], lo[3], hi[0], hi[1], hi[2], hi[3]};
      }
      LBAR();
      if (j == 3 && w == 0) boot_ask(p.ws, bt);
      if (j + 4 < 8) stage(j + 4);
      if (j == 6) {
#pragma unroll
        for (int i = 0; i < 10; ++i) row(i >> 2, i & 3);
      }
    }
    p.s0 = uni(lds_at(smem + W_PM)[0] >> 31);
  }
  RDY(128 + g);
  typedef __attribute__((address_space(3))) const bf16x8* lds_frag_ptr;
  typedef __attribute__((address_space(3))) f32x4* lds_f4_ptr;
  unsigned la[2][4];
#pragma unroll
  for (int i = 0; i < 8; ++i) {
    la[i >> 2][i & 3] = lds_off(smem) + (i >> 2) * 65536 + r * 1024 + kh * 512 + (((4 * (i & 3) + q) ^ r) << 4);
    asm volatile("" : "+v"(la[i >> 2][i & 3]));
  }
  unsigned xw = lds_off(smem) + W_X + w * 2048 + lane * 16, xr = xw ^ 8192;
  asm volatile("" : "+v"(xw), "+v"(xr));
  auto ldA = [&](int b, int ks, int m) { return *(lds_frag_ptr)(uintptr_t)(la[b >> 1][ks & 3] + (b & 1) * 32768 + m * 16384 + (ks >> 2) * 256); };
  const __amdgpu_buffer_rsrc_t ro = mkrsrc(p.ws + WS_T, T_BYTES);
  const int t0 = which * T_Q + kseg(0, h), vt = (ISV ? 2 * cg + kh : 4 * kh + cg) * 1024 + lane * 16;
  f32x4 acc[2][2][2], xs[2];
  bf16x8 af[4][2];
#pragma unroll
  for (int i = 0; i < 4; ++i) acc[1][i >> 1][i & 1] = (f32x4){0.f, 0.f, 0.f, 0.f};
  auto put = [&](int sp) {
#pragma unroll
    for (int b = 0; b < 2; ++b) *(lds_f4_ptr)(uintptr_t)(xw + b * 1024) = acc[sp][1][b];
  };
  auto get = [&]() {
#pragma unroll
    for (int b = 0; b < 2; ++b) xs[b] = *(lds_f4_ptr)(uintptr_t)(xr + b * 1024);
  };
  auto fin = [&](int sp, int sg) {
    const f32x4 x = acc[sp][0][0] + xs[0], y = acc[sp][0][1] + xs[1];
    __builtin_amdgcn_raw_buffer_store_b128((u32x4){cvtpk(x[0], x[1]), cvtpk(x[2], x[3]), cvtpk(y[0], y[1]), cvtpk(y[2], y[3])}, ro,
                                           vt + (sg < 0 ? 0x70000000 : t_off(p, t0 + sg)), 0, 16);
  };
  WAIT_V(6);
  LBAR();
#pragma unroll
  for (int i = 0; i < 4; ++i) af[i >> 1][i & 1] = ldA(0, i >> 1, i & 1);
  auto loop = [&](auto KH) {
#pragma unroll 1
    for (int tb = 0; tb < 2 * NSEG; tb += 4) {
#pragma unroll
      for (int b = 0; b < 4; ++b) {
        const int sp = b >> 1, u = b & 1, sg = (tb + b) >> 1;
#pragma unroll
        for (int ks = 0; ks < 8; ++ks) {
          if (ks == 4) {
            if (KH == 0 && b == 0 && tb == 24 && w == 0) wait_rows(p.ws, tag);
            if (u) WAIT_V(5); else WAIT_V(4);
            __builtin_amdgcn_s_barrier();
            asm volatile("" ::: "memory");
            if (u == 0 && KH == 1 && tid == 256 && sg >= 2) __builtin_amdgcn_raw_buffer_store_b32(sg - 2, rsy, fkq(h, which), 0, 16);
            if (u == 0 && KH == 1 && w == 5 && sg == 2) RDW(928 + g);
            if (u == 0) get();
          }
#pragma unroll
          for (int m = 0; m < 2; ++m) {
            af[(ks + 2) & 3][m] = ldA(ks < 6 ? b : (b + 1) & 3, (ks + 2) & 7, m);
#pragma unroll
            for (int n = 0; n < 2; ++n) {
              f32x4& d = ISV ? acc[sp][n][m] : acc[sp][m][n];
              const f32x4 c = (u | ks) ? d : (f32x4){0.f, 0.f, 0.f, 0.f};
              d = ISV ? MFMA(af[ks & 3][m], W[8 * u + ks][n], c) : MFMA(W[8 * u + ks][n], af[ks & 3][m], c);
            }
          }
          if ((ks & 1) == KH) { SCHED(); row(tb + b + (ks < 4 ? 2 : 3), ((ks >> 1) + 2) & 3); }
          if (u == 0 && ks == 0) put(sp ^ 1);
          if (u == 0 && ks == 7 - KH) fin(sp ^ 1, sg - 1);
          SCHED();
        }
      }
    }
  };
  if (kh) loop(std::integral_constant<int, 1>()); else loop(std::integral_constant<int, 0>());
  put(1);
  LBAR();
  get();
  fin(1, NSEG - 1);
  WAIT_V(0);
  LBAR();
  if (tid == 0) __builtin_amdgcn_raw_buffer_store_b32(NSEG - 1, rsy, fkq(h, which), 0, 16);
}

enum { J_NONE = 0, J_GU = 3, J_PR = 5, J_CB = 8, J_DN = 14 };
struct BigJob { int type, panel, idx; };
struct BigPtr { const char *a, *b0, *b1; };
constexpr int G_LDA = XLD * 2, G_LDB = FF * 2;

__device__ __forceinline__ BigPtr big_ptrs(const Args& p, const BigJob& j) {
  BigPtr r;
  r.a = p.ws + WS_XN + (size_t)j.panel * 128 * G_LDA;
  r.b0 = (const char*)(p.wg + j.idx * 128); r.b1 = (const char*)(p.wu + j.idx * 128);
  return r;
}
__device__ __forceinline__ void big_publish(const Args& p, const BigJob& j) {
  int tid = threadIdx.x;
  asm volatile("" : "+v"(tid));
  if (j.type == J_GU && tid == 0)
    __hip_atomic_fetch_add((gu32*)(p.ws + WS_SY + SY_CH) + j.panel * 16, 1u, __ATOMIC_RELAXED, __HIP_MEMORY_SCOPE_AGENT);
}
typedef f32x4 Acc[2][2][4][2];
__device__ __forceinline__ u32x4 pack32(f32x4 a, f32x4 b) {
  return widen32(cvtpk(a[0], a[1]), cvtpk(a[2], a[3]), cvtpk(b[0], b[1]), cvtpk(b[2], b[3]));
}
#define st_epi(r, o, v) st_sc1(r, o, v)
__device__ __forceinline__ void big_epilogue(const Args& p, const BigJob& j, Acc& acc, char* smem) {
  int tid = threadIdx.x;
  asm volatile("" : "+v"(tid));
  const int lane = tid & 63;
  const int wid = __builtin_amdgcn_readfirstlane(tid >> 6);
  const int wr = wid >> 2, wc = wid & 3;
  {
    const __amdgpu_buffer_rsrc_t ro = mkrsrc(p.ws + WS_HF, S_LEN * 16384);
#pragma unroll
    for (int i = 0; i < 8; ++i) {
      const int row = wr * 64 + (i & 3) * 16 + (lane & 15);
      f32x4 hh[2];
#pragma unroll
      for (int n = 0; n < 2; ++n)
#pragma unroll
        for (int jj = 0; jj < 4; ++jj) {
          const float g = acc[i >> 2][0][i & 3][n][jj], u = acc[i >> 2][1][i & 3][n][jj];
          hh[n][jj] = __builtin_amdgcn_rcpf(1.0f + __builtin_amdgcn_exp2f(g * -1.4426950408889634f)) * u;
        }
      st_epi(ro, h_off(p, j.panel + (i >> 2)) + row * HLD + (j.idx * 128 + wc * 32 + (lane >> 4) * 8) * 2, pack32(hh[0], hh[1]));
    }
  }
}
#define JST(n, i, v) do {} while (0)
constexpr int JOBW = 115;
__constant__ unsigned short JOBS[115] = {
  5120, 5120, 5120, 5120, 5120, 5120, 5120, 5120, 5120, 5120, 5120, 5120, 5120, 5120, 5120, 5120,
  5120, 5120, 5120, 5120, 5120, 5120, 5120, 5120, 3296, 3297, 3298, 3299, 3300, 3301, 3302, 3264,
  3265, 3266, 3267, 3268, 3269, 3270, 3232, 3233, 3234, 3235, 3236, 3237, 3238, 3200, 3201, 3202,
  3203, 3204, 3205, 3206, 3168, 3169, 3170, 3171, 3172, 3173, 3174, 3136, 3137, 3138, 3139, 3140,
  3141, 3142, 3104, 3105, 3106, 3107, 3108, 3109, 3110, 3072, 3073, 3074, 3075, 3076, 3077, 3078,
  14384, 14385, 14386, 14387, 14388, 14389, 14390, 14391, 14336, 14337, 14338, 14339, 14340, 14341, 14342, 14343,
  14352, 14353, 14354, 14355, 14356, 14357, 14358, 14359, 14368, 14369, 14370, 14371, 14372, 14373, 14374, 14375,
  8384, 8400, 0,
};
__device__ __forceinline__ void gemm_role(Args& p, int g, char* smem) {
  const __amdgpu_buffer_rsrc_t rsy = mkrsrc(p.ws + WS_SY, SY_TOTAL);
  const int x = g & 3, k = g >> 2;
  gu32* queue = (gu32*)(p.ws + WS_SY + SY_Q) + x * 32;
  volatile unsigned* slot = reinterpret_cast<volatile unsigned*>(smem + 163824);
  unsigned nxt = 0;
  auto request = [&]() {
    if (threadIdx.x == 0) nxt = __hip_atomic_fetch_add(queue, 1u, __ATOMIC_RELAXED, __HIP_MEMORY_SCOPE_AGENT) + 1u + NGW;
  };
  auto post = [&]() { if (threadIdx.x == 0) *slot = nxt; };
  auto decode = [&](int j) {
    const unsigned e = JOBS[min(j, JOBW - 1)];
    const int i = e & 15;
    BigJob b;
    b.type = e >> 10; b.panel = (e >> 4) & 63;
    b.idx = x + 4 * i;
    if (b.type == J_DN) { b.idx = i; b.panel = 4 * b.panel + x; }
    return b;
  };
  int nj = 0; (void)nj;
  auto claim = [&]() {
    request();
    LBAR();
    post();
    LBAR();
    return decode((int)__builtin_amdgcn_readfirstlane(*slot));
  };
  if (g < 4 && threadIdx.x == 0) { LST(480 + 4 * g); LSTC(480 + 4 * g + 1); }
  BigJob cur;
  if (k < 24) {
    JST(nj, 0, J_PR); JST(nj, 1, __builtin_amdgcn_s_memrealtime());
    if (k >= 16) job_proj<true>(p, g, 2, x * 8 + (k & 7), smem, rsy);
    else job_proj<false>(p, g, k >> 3, x * 8 + (k & 7), smem, rsy);
    JST(nj, 3, __builtin_amdgcn_s_memrealtime()); ++nj;
    if (threadIdx.x == 0) __builtin_amdgcn_fence(__ATOMIC_ACQUIRE, "agent");
    cur = claim();
  } else {
    const unsigned t0 = (unsigned)__builtin_amdgcn_s_memrealtime();
    const u32x2 tag = start_rows<0>(p, g - 96, []() {});
    WAIT_V(0);
    LBAR();
    report_rows(p, g - 96, tag);
    while ((unsigned)__builtin_amdgcn_s_memrealtime() - t0 < PF_T0) __builtin_amdgcn_s_sleep(8);
#pragma unroll 1
    for (int i = 0; i < PF_N; ++i) {
      const int m = i % 3, o = ((i / 3) * 32 + g - 96) * 448 + (int)threadIdx.x;
      if (threadIdx.x < 448) touch_line((const char*)(m == 0 ? p.wg : (m == 1 ? p.wu : p.wd)) + (size_t)o * 128, smem + (threadIdx.x >> 6) * 256);
      WAIT_V(1);
    }
    WAIT_V(0);
    if (threadIdx.x < 64) {
      wait_rows(p.ws, tag);
      Boot bt;
      boot_ask(p.ws, bt);
      WAIT_V(0);
      *slot = boot_done(p.ws, bt, true, tag);
      __builtin_amdgcn_fence(__ATOMIC_ACQUIRE, "agent");
      WAIT_V(0);
      while ((unsigned)__builtin_amdgcn_s_memrealtime() - t0 < GU_DELAY) __builtin_amdgcn_s_sleep(8);
    }
    LBAR();
    p.s0 = *slot >> 31;
    RDY(128 + g);
    cur = decode(k);
  }
  p.s0 = uni(p.s0);
  while (cur.type != J_NONE) {
    JST(nj, 0, cur.type | cur.panel << 8); JST(nj, 1, __builtin_amdgcn_s_memrealtime());
    if (cur.type != J_GU) {
      if (cur.type == J_DN) job_down(p, cur.panel, cur.idx, smem, rsy);
      else job_comb(p, cur.panel, x, rsy);
      JST(nj, 3, __builtin_amdgcn_s_memrealtime()); ++nj;
      cur = claim();
      continue;
    }
    int tid = threadIdx.x;
    asm volatile("" : "+v"(tid));
    const int lane = tid & 63;
    const int wid = __builtin_amdgcn_readfirstlane(tid >> 6);
    const int wr = wid >> 2, wc = wid & 3;
    const unsigned ra = tid >> 3, ca = 16 * ((tid & 7) ^ ((tid >> 3) & 7)), rb = tid >> 4, cb = 16 * ((tid & 15) ^ swzB((tid >> 4) & 15));
    const unsigned lw = lds_off(smem) + wid * 1024;
#define B8_SA(b, h) (((b) * 2 + (h)) * 16384)
#define B8_SB(b, h) ((4 + (b) * 2 + (h)) * 16384)
#define B8_STA(b, h, P, kt) glds2((P).a + (size_t)(h) * 128 * G_LDA + (kt) * 128, 64 * G_LDA, ra * G_LDA + ca, lw + B8_SA(b, h))
#define B8_STB(b, h, P, kt) glds2(((h) ? (P).b1 : (P).b0) + (size_t)(kt) * 64 * G_LDB, 32 * G_LDB, rb * G_LDB + cb, lw + B8_SB(b, h))
#define B8_LDA(b, h) do { _Pragma("unroll") for (int m = 0; m < 4; ++m) _Pragma("unroll") for (int kk = 0; kk < 2; ++kk) \
      At[m][kk] = ldsf(smem + B8_SA(b, h) + offA(wr * 64 + m * 16 + (lane & 15), kk * 4 + (lane >> 4))); } while (0)
#define B8_LDB(dst, b, h) do { _Pragma("unroll") for (int n = 0; n < 2; ++n) _Pragma("unroll") for (int kk = 0; kk < 2; ++kk) \
      dst[n][kk] = ld_bfrag(smem + B8_SB(b, h), kk, wc * 2 + n, lane); } while (0)
#define B8_MMA(ai, bj, Bt) do { __builtin_amdgcn_s_setprio(1); _Pragma("unroll") for (int m = 0; m < 4; ++m) _Pragma("unroll") for (int n = 0; n < 2; ++n) \
      _Pragma("unroll") for (int kk = 0; kk < 2; ++kk) acc[ai][bj][m][n] = MFMA(Bt[n][kk], At[m][kk], acc[ai][bj][m][n]); __builtin_amdgcn_s_setprio(0); } while (0)
#define WAIT_L(n) asm volatile("s_waitcnt lgkmcnt(%0)" :: "n"(n) : "memory")
#define BAR() __builtin_amdgcn_s_barrier()
#define WAIT_RX(c) asm volatile("s_waitcnt vmcnt(16)\n\ts_cmp_lg_u32 %0, 0\n\ts_cbranch_scc1 .Lrx%=\n\ts_waitcnt vmcnt(8)\n.Lrx%=:" :: "s"(c) : "memory", "scc")
    Acc acc;
    bf16x8 At[4][2], B0[2][2], B1[2][2];
    BigPtr cp = big_ptrs(p, cur);
    BigJob pend; pend.type = J_NONE; pend.panel = 0; pend.idx = 0;
    LBAR();
    B8_STB(0, 0, cp, 0); B8_STB(0, 1, cp, 0); B8_STA(0, 0, cp, 0); B8_STA(0, 1, cp, 0);
    if (wr == 1) BAR();
    WAIT_V(2); BAR();
    B8_STB(1, 0, cp, 1); B8_STA(1, 0, cp, 1); B8_STB(1, 1, cp, 1);
    WAIT_V(6); BAR();
    BigJob nx = pend;
    for (;;) {
      bool chain = false;
      BigPtr np = cp;
      constexpr int nt = DM / 64;
#pragma unroll
      for (int i = 0; i < 32; ++i) acc[i >> 4][(i >> 3) & 1][(i >> 1) & 3][i & 1] = (f32x4){0.f, 0.f, 0.f, 0.f};
#pragma unroll 1
      for (int t = 0; t < nt; t += 2) {
        const bool last = t == nt - 2;
        if (t == nt - 6) request();
        if (t == nt - 4) post();
        if (last) {
          nx = decode((int)__builtin_amdgcn_readfirstlane(*slot));
          chain = nx.type == J_GU;
          if (chain) np = big_ptrs(p, nx);
        }
        const BigPtr sp = np;
        const int t2 = last ? 0 : t + 2;
        if (t == 2) big_publish(p, pend);
        const int rx = __builtin_amdgcn_readfirstlane(t == 0 && pend.type != J_NONE);
        B8_LDB(B0, 0, 0); B8_LDB(B1, 0, 1); SCHED(); B8_LDA(0, 0); B8_STA(1, 1, cp, t + 1);
        WAIT_RX(rx);
        WAIT_L(0); BAR(); B8_MMA(0, 0, B0); B8_MMA(0, 1, B1); BAR(); SCHED();
        B8_LDA(0, 1); B8_STB(0, 0, sp, t2); B8_STB(0, 1, sp, t2); B8_STA(0, 0, sp, t2);
        WAIT_RX(rx);
        WAIT_L(0); BAR(); B8_MMA(1, 0, B0); B8_MMA(1, 1, B1); BAR(); SCHED();
        B8_LDB(B0, 1, 0); B8_LDB(B1, 1, 1); SCHED(); B8_LDA(1, 0); B8_STA(0, 1, sp, t2);
        WAIT_V(8); WAIT_L(0); BAR(); B8_MMA(0, 0, B0); B8_MMA(0, 1, B1); BAR(); SCHED();
        B8_LDA(1, 1); B8_STB(1, 0, sp, t2 + 1); B8_STB(1, 1, sp, t2 + 1); B8_STA(1, 0, sp, t2 + 1);
        WAIT_V(8); WAIT_L(0); BAR(); B8_MMA(1, 0, B0); B8_MMA(1, 1, B1); BAR(); SCHED();
      }
      if (wr == 0) BAR();
      JST(nj, 2, __builtin_amdgcn_s_memrealtime());
      big_epilogue(p, cur, acc, smem);
      if (!chain) break;
      JST(nj, 3, __builtin_amdgcn_s_memrealtime()); ++nj;
      pend = cur; cur = nx; cp = np;
      if (wr == 1) BAR();
      JST(nj, 0, cur.type | cur.panel << 8); JST(nj, 1, __builtin_amdgcn_s_memrealtime());
    }
    WAIT_V(0);
    BAR();
    asm volatile(".Lcode_end:");
    big_publish(p, cur);
    JST(nj, 3, __builtin_amdgcn_s_memrealtime()); ++nj;
    cur = nx;
  }
  if (threadIdx.x == 0) { LST(128 + g); }
  if (g < 4 && threadIdx.x == 0) { LST(480 + 4 * g + 2); LSTC(480 + 4 * g + 3); }
}

__device__ __forceinline__ unsigned now_q() { return (unsigned)__builtin_amdgcn_s_memrealtime() * 4u; }
__device__ __forceinline__ void pace_until(unsigned t) {
#pragma unroll 1
  for (int i = 0; i < 256; ++i) {
    const int d = (int)(t - now_q());
    if (d <= 0) break;
    if (d > 32) __builtin_amdgcn_s_sleep(1);
  }
}
#define CLB_E "s76", "s77", "s78", "s79", "s80", "s81", "s82", "s83", "s84", "s85", "s86", "s87", "s88", "s89"
#define EDGE_ASM \
  "s_memtime s[76:77]\n\t" \
  "s_memrealtime s[78:79]\n\t" \
  "s_nop 3\n\t" \
  "s_memrealtime s[80:81]\n\t" \
  "s_nop 3\n\t" \
  "s_memrealtime s[82:83]\n\t" \
  "s_nop 3\n\t" \
  "s_memrealtime s[84:85]\n\t" \
  "s_nop 3\n\t" \
  "s_memrealtime s[86:87]\n\t" \
  "s_nop 3\n\t" \
  "s_memrealtime s[88:89]\n\t" \
  "s_mov_b32 s93, 0\n\t" \
  "s_waitcnt lgkmcnt(0)\n\t" \
  "s_cmp_eq_u32 s80, s78\n\t" \
  "s_addc_u32 s93, s93, 0\n\t" \
  "s_cmp_eq_u32 s82, s78\n\t" \
  "s_addc_u32 s93, s93, 0\n\t" \
  "s_cmp_eq_u32 s84, s78\n\t" \
  "s_addc_u32 s93, s93, 0\n\t" \
  "s_cmp_eq_u32 s86, s78\n\t" \
  "s_addc_u32 s93, s93, 0\n\t" \
  "s_cmp_eq_u32 s88, s78\n\t" \
  "s_addc_u32 s93, s93, 0\n\t" \
  "s_mul_i32 s93, s93, 21\n\t" \
  "s_add_u32 s76, s76, 15\n\t" \
  "s_add_u32 %[kc], s76, s93\n\t" \
  "s_lshl2_add_u32 %[kq], s78, 16\n\t"
__device__ __forceinline__ unsigned time_of(unsigned cyc) {
  unsigned c, q;
  asm volatile(EDGE_ASM : [kc] "=&s"(c), [kq] "=&s"(q) :: CLB_E, "s93", "scc");
  return q - (((c - cyc) * 11900u) >> 16);
}
#define DS_RED 58
#define DS_MEM 30
#define LEAD_DN 2
#define LEAD_UP 12
__device__ __forceinline__ u32x2 ld2_sc1(__amdgpu_buffer_rsrc_t rs, int off, int soff = 0) {
  return __builtin_amdgcn_raw_buffer_load_b64(rs, off, soff, 16);
}
__device__ __forceinline__ void st2_sc1(__amdgpu_buffer_rsrc_t rs, int off, u32x2 v) {
  __builtin_amdgcn_raw_buffer_store_b64(v, rs, off, 0, 16);
}
__device__ __forceinline__ int row_max(int v) {
  v = max(v, __builtin_amdgcn_update_dpp(0, v, 0x128, 0xf, 0xf, false));
  v = max(v, __builtin_amdgcn_update_dpp(0, v, 0x124, 0xf, 0xf, false));
  v = max(v, __builtin_amdgcn_update_dpp(0, v, 0x122, 0xf, 0xf, false));
  v = max(v, __builtin_amdgcn_update_dpp(0, v, 0x121, 0xf, 0xf, false));
  return v;
}
__device__ __forceinline__ int wave_max(int v) {
  v = row_max(v);
  return max(max(__builtin_amdgcn_readlane(v, 0), __builtin_amdgcn_readlane(v, 16)),
             max(__builtin_amdgcn_readlane(v, 32), __builtin_amdgcn_readlane(v, 48)));
}
__device__ __forceinline__ int track(int est, int x, int up, int dn, int out) {
  const int d = x - est;
  return est + (d > out ? 2 : max(min(d, up), -dn));
}
#define WSL 12
#define PLAN_SLEEP 2
__device__ __forceinline__ unsigned latest(unsigned t) {
  const unsigned t0 = __builtin_amdgcn_readfirstlane(t);
  return t0 + wave_max((int)(t - t0));
}
__device__ __forceinline__ void watch(__amdgpu_buffer_rsrc_t rsy, int s, int lane, unsigned t0, const unsigned* tgt_, gu32* abortw, int hs) {
  pace_until(t0);
  u32x2 ra;
  for (unsigned spins = 0;;) {
    asm volatile("" ::: "memory");
    ra = ld2_sc1(rsy, lane * 128, SY_BR + hs);
    const bool all = __all((int)ra[0] >= s);
    SMP(4, s, spins);
    if (all) break;
    if (spins < 3) __builtin_amdgcn_s_sleep(PLAN_SLEEP); else __builtin_amdgcn_s_sleep(WSL);
    if (spin_fail(spins, abortw)) break;
  }
  const unsigned t = latest(ra[0] == (unsigned)s ? ra[1] : t0) + DS_MEM;
  lds_vu_ptr tgt = lds_at(tgt_);
  if (lane < 2) tgt[lane] = lane ? t : s + 1;
  SMP(0, s, (int)(t - t0)); SMP(1, s, (int)(t - now_q()));
}
__device__ __forceinline__ unsigned ld_flag(__amdgpu_buffer_rsrc_t rs, int off) {
  return __builtin_amdgcn_raw_buffer_load_b32(rs, off, 0, 16);
}

constexpr unsigned long long QK_MAP[8] = {0xbaf9876d543210ecull, 0xa98f76d543210bceull, 0x98f76d543210ecbaull, 0x876fd543210bcea9ull, 0x76fd543210ecba98ull, 0x65df43210bcea987ull, 0x5df43210ecba9876ull, 0xd43f210bcea98765ull};
#define QK_ASM_0 \
  "s_waitcnt lgkmcnt(0)\n\t" \
  "v_subrev_u32 v244, 0x8000, v250\n\t" \
  "ds_read_b128 v[216:219], v248 offset:32768\n\t" \
  "ds_read_b128 v[220:223], v248 offset:33792\n\t" \
  "ds_read_b128 v[224:227], v248 offset:34816\n\t" \
  "ds_read_b128 v[228:231], v248 offset:35840\n\t" \
  "ds_read_b128 v[208:211], v244 offset:0\n\t" \
  "ds_read_b128 v[212:215], v244 offset:1024\n\t" \
  "ds_read_b128 v[236:239], v244 offset:2048\n\t" \
  "ds_read_b128 v[240:243], v244 offset:3072\n\t" \
  "v_mov_b64 v[192:193], 0\n\t" \
  "v_mov_b64 v[194:195], 0\n\t" \
  "v_mov_b64 v[196:197], 0\n\t" \
  "v_mov_b64 v[198:199], 0\n\t" \
  "s_movk_i32 s92, 0x2000\n\t" \
  ".Lwt%=:\n\t" \
  "ds_read_b64 v[200:201], v248 offset:30720\n\t" \
  "s_memrealtime s[90:91]\n\t" \
  "s_sub_u32 s92, s92, 1\n\t" \
  "s_waitcnt lgkmcnt(0)\n\t" \
  "v_readfirstlane_b32 s93, v200\n\t" \
  "v_readfirstlane_b32 %[tgt], v201\n\t" \
  "s_cbranch_scc1 .Lgo%=\n\t" \
  "s_cmp_eq_u32 s93, %[step]\n\t" \
  "s_cbranch_scc1 .Lwx%=\n\t" \
  "s_sleep 1\n\t" \
  "s_branch .Lwt%=\n\t" \
  ".Lwx%=:\n\t" \
  "s_lshl_b32 s93, s90, 2\n\t" \
  "s_sub_i32 s93, %[tgt], s93\n\t" \
  "s_cmp_le_i32 s93, 0\n\t" \
  "s_cbranch_scc1 .Lgo%=\n\t" \
  "s_min_i32 s93, s93, 8000\n\t" \
  "s_add_u32 s93, s93, 3\n\t" \
  "s_lshr_b32 s93, s93, 2\n\t" \
  "s_add_u32 s93, s93, s90\n\t" \
  "s_memrealtime s[90:91]\n\t" \
  ".Lpace%=:\n\t" \
  "s_memrealtime s[94:95]\n\t" \
  "s_waitcnt lgkmcnt(1)\n\t" \
  "s_sub_i32 s91, s90, s93\n\t" \
  "s_cmp_ge_i32 s91, 0\n\t" \
  "s_cbranch_scc1 .Lgo%=\n\t" \
  "s_memrealtime s[90:91]\n\t" \
  "s_waitcnt lgkmcnt(1)\n\t" \
  "s_sub_i32 s95, s94, s93\n\t" \
  "s_cmp_lt_i32 s95, 0\n\t" \
  "s_cbranch_scc1 .Lpace%=\n\t" \
  ".Lgo%=:\n\t" \
  "s_mov_b32 s92, 0\n\t" \
  ".Lpoll%=:\n\t" \
  "buffer_load_dwordx4 v[200:203], v248, %[rs], %[som] offen sc1\n\t" \
  "buffer_load_dwordx4 v[204:207], v248, %[rs], %[som2] offen sc1\n\t" \
  "s_waitcnt vmcnt(1) lgkmcnt(0)\n\t" \
  "ds_write_b128 v249, v[200:203]\n\t" \
  "v_smfmac_f32_16x16x64_bf16 v[192:195], v[200:203], v[208:215], v251\n\t" \
  "v_max3_u32 v244, v200, v201, v202\n\t" \
  "v_smfmac_f32_16x16x64_bf16 v[196:199], v[200:203], v[236:243], v251\n\t" \
  "s_waitcnt vmcnt(0)\n\t" \
  "ds_write_b128 v249, v[204:207] offset:8192\n\t" \
  "v_smfmac_f32_16x16x64_bf16 v[192:195], v[204:207], v[216:223], v251\n\t" \
  "v_max3_u32 v244, v244, v203, v204\n\t" \
  "v_max3_u32 v244, v244, v205, v206\n\t" \
  "v_smfmac_f32_16x16x64_bf16 v[196:199], v[204:207], v[224:231], v251\n\t" \
  "v_max_u32 v244, v244, v207\n\t" \
  "v_cmp_le_u32 vcc, %[sent], v244\n\t" \
  "s_cbranch_vccnz .Lslow%=\n\t" \
  ".Lok%=:\n\t" \
  "ds_read_b128 v[216:219], v250 offset:32768\n\t" \
  "ds_read_b128 v[220:223], v250 offset:33792\n\t" \
  "ds_read_b128 v[224:227], v250 offset:34816\n\t" \
  "ds_read_b128 v[228:231], v250 offset:35840\n\t" \
  "s_waitcnt lgkmcnt(4)\n\t" \
  "s_barrier\n\t" \
  "ds_read_b128 v[208:211], v248 offset:1024\n\t" \
  "ds_read_b128 v[212:215], v248 offset:2048\n\t" \
  "ds_read_b128 v[236:239], v248 offset:3072\n\t" \
  "ds_read_b128 v[240:243], v248 offset:4096\n\t" \
  "s_waitcnt lgkmcnt(3)\n\t" \
  "ds_read_b128 v[244:247], v248 offset:13312\n\t" \
  "v_smfmac_f32_16x16x64_bf16 v[192:195], v[208:211], v[216:223], v251\n\t" \
  "ds_read_b128 v[216:219], v250 offset:0\n\t" \
  "v_smfmac_f32_16x16x64_bf16 v[196:199], v[208:211], v[224:231], v251\n\t" \
  "s_waitcnt lgkmcnt(4)\n\t" \
  "ds_read_b128 v[220:223], v250 offset:1024\n\t" \
  "v_smfmac_f32_16x16x64_bf16 v[192:195], v[212:215], v[0:7], v251\n\t" \
  "ds_read_b128 v[224:227], v250 offset:2048\n\t" \
  "v_smfmac_f32_16x16x64_bf16 v[196:199], v[212:215], v[8:15], v251\n\t" \
  "s_waitcnt lgkmcnt(5)\n\t" \
  "ds_read_b128 v[228:231], v250 offset:3072\n\t" \
  "v_smfmac_f32_16x16x64_bf16 v[192:195], v[236:239], v[16:23], v251\n\t" \
  "ds_read_b128 v[200:203], v248 offset:5120\n\t" \
  "v_smfmac_f32_16x16x64_bf16 v[196:199], v[236:239], v[24:31], v251\n\t" \
  "s_waitcnt lgkmcnt(6)\n\t" \
  "ds_read_b128 v[204:207], v248 offset:6144\n\t" \
  "v_smfmac_f32_16x16x64_bf16 v[192:195], v[240:243], v[32:39], v251\n\t" \
  "ds_read_b128 v[208:211], v248 offset:7168\n\t" \
  "v_smfmac_f32_16x16x64_bf16 v[196:199], v[240:243], v[40:47], v251\n\t" \
  "s_waitcnt lgkmcnt(5)\n\t" \
  "ds_read_b128 v[212:215], v248 offset:9216\n\t" \
  "v_smfmac_f32_16x16x64_bf16 v[192:195], v[244:247], v[216:223], v251\n\t" \
  "s_waitcnt lgkmcnt(4)\n\t" \
  "v_smfmac_f32_16x16x64_bf16 v[196:199], v[244:247], v[224:231], v251\n\t" \
  "s_waitcnt lgkmcnt(3)\n\t" \
  "ds_read_b128 v[236:239], v248 offset:10240\n\t" \
  "v_smfmac_f32_16x16x64_bf16 v[192:195], v[200:203], v[48:55], v251\n\t" \
  "ds_read_b128 v[240:243], v248 offset:11264\n\t" \
  "v_smfmac_f32_16x16x64_bf16 v[196:199], v[200:203], v[56:63], v251\n\t" \
  "s_waitcnt lgkmcnt(4)\n\t" \
  "v_smfmac_f32_16x16x64_bf16 v[192:195], v[204:207], v[64:71], v251\n\t" \
  "ds_read_b128 v[244:247], v248 offset:12288\n\t" \
  "v_smfmac_f32_16x16x64_bf16 v[196:199], v[204:207], v[72:79], v251\n\t" \
  "s_waitcnt lgkmcnt(4)\n\t" \
  "v_smfmac_f32_16x16x64_bf16 v[192:195], v[208:211], v[80:87], v251\n\t" \
  "ds_read_b128 v[200:203], v248 offset:14336\n\t" \
  "v_smfmac_f32_16x16x64_bf16 v[196:199], v[208:211], v[88:95], v251\n\t" \
  "s_waitcnt lgkmcnt(4)\n\t" \
  "v_smfmac_f32_16x16x64_bf16 v[192:195], v[212:215], v[96:103], v251\n\t" \
  "ds_read_b128 v[204:207], v248 offset:15360\n\t" \
  "v_smfmac_f32_16x16x64_bf16 v[196:199], v[212:215], v[104:111], v251\n\t" \
  "s_waitcnt lgkmcnt(3)\n\t" \
  "v_smfmac_f32_16x16x64_bf16 v[192:195], v[236:239], v[112:119], v251\n\t" \
  "v_smfmac_f32_16x16x64_bf16 v[196:199], v[236:239], v[120:127], v251\n\t" \
  "v_smfmac_f32_16x16x64_bf16 v[192:195], v[240:243], v[128:135], v251\n\t" \
  "v_smfmac_f32_16x16x64_bf16 v[196:199], v[240:243], v[136:143], v251\n\t" \
  "s_waitcnt lgkmcnt(1)\n\t" \
  "v_smfmac_f32_16x16x64_bf16 v[192:195], v[244:247], v[144:151], v251\n\t" \
  "v_smfmac_f32_16x16x64_bf16 v[196:199], v[244:247], v[152:159], v251\n\t" \
  "v_smfmac_f32_16x16x64_bf16 v[192:195], v[200:203], v[160:167], v251\n\t" \
  "v_smfmac_f32_16x16x64_bf16 v[196:199], v[200:203], v[168:175], v251\n\t" \
  "s_waitcnt lgkmcnt(0)\n\t" \
  "v_smfmac_f32_16x16x64_bf16 v[192:195], v[204:207], v[176:183], v251\n\t" \
  "v_smfmac_f32_16x16x64_bf16 v[196:199], v[204:207], v[184:191], v251\n\t" \
  "ds_read_b128 v[200:203], v248 offset:16384\n\t" \
  "ds_read_b128 v[204:207], v248 offset:20480\n\t" \
  "v_mov_b32 v250, v232\n\t" \
  "v_mov_b32 v249, v233\n\t" \
  "s_nop 2\n\t" \
  "v_add_f32 v192, v192, v194\n\t" \
  "v_add_f32 v193, v193, v195\n\t" \
  "v_add_f32 v196, v196, v198\n\t" \
  "v_add_f32 v197, v197, v199\n\t" \
  "v_cvt_pk_bf16_f32 v192, v192, v193\n\t" \
  "v_cvt_pk_bf16_f32 v196, v196, v197\n\t" \
  "ds_write2_b32 v252, v192, v196 offset1:64\n\t" \
  "v_mov_b32 v251, v234\n\t" \
  "ds_read_b64_tr_b16 v[232:233], v250 offset:0\n\t" \
  "ds_read_b64_tr_b16 v[234:235], v250 offset:64\n\t" \
  "v_mov_b32 v212, %[step]\n\t" \
  "s_mov_b64 exec, 1\n\t" \
  "ds_write_b32 v248, v212 offset:28672\n\t" \
  "s_mov_b64 exec, -1\n\t" \
  "buffer_load_dwordx4 v[192:195], v248, %[rt], %[svt] offen offset:0 sc1\n\t" \
  "buffer_load_dwordx4 v[196:199], v248, %[rt], %[svt] offen offset:1024 sc1\n\t" \
  "s_waitcnt lgkmcnt(1)\n\t" \
  "v_mfma_f32_16x16x32_bf16 v[200:203], v[200:203], v[232:235], 0\n\t" \
  "v_mfma_f32_16x16x32_bf16 v[204:207], v[204:207], v[232:235], 0\n\t" \
  "s_nop 7\n\t" \
  "v_cndmask_b32 v216, v200, v204, %[nh]\n\t" \
  "v_cndmask_b32 v217, v201, v205, %[nh]\n\t" \
  "v_cndmask_b32 v218, v202, v206, %[nh]\n\t" \
  "v_cndmask_b32 v219, v203, v207, %[nh]\n\t" \
  "ds_write_b128 v248, v[216:219] offset:16384\n\t" \
  "buffer_load_dword v252, v249, %[rs], %[sv] offen offset:0 sc1\n\t" \
  "buffer_load_dword v253, v249, %[rs], %[sv] offen offset:256 sc1\n\t" \
  "ds_read_b32 v212, v248 offset:28672\n\t" \
  "ds_read_b64_tr_b16 v[236:237], v250 offset:512\n\t" \
  "ds_read_b64_tr_b16 v[238:239], v250 offset:576\n\t" \
  "ds_read_b64_tr_b16 v[240:241], v250 offset:1024\n\t" \
  "ds_read_b64_tr_b16 v[242:243], v250 offset:1088\n\t" \
  "ds_read_b64_tr_b16 v[244:245], v250 offset:1536\n\t" \
  "ds_read_b64_tr_b16 v[246:247], v250 offset:1600\n\t" \
  "s_setprio 3\n\t" \
  "s_waitcnt lgkmcnt(0)\n\t" \
  "v_cmp_ne_u32 vcc, %[step], v212\n\t" \
  "s_barrier\n\t" \
  "s_and_b32 s93, vcc_lo, 0xf\n\t" \
  "s_cbranch_scc1 .Lre%=\n\t" \
  ".Lrb%=:\n\t" \
  "ds_read_b128 v[220:223], v248 offset:17408\n\t" \
  "ds_read_b128 v[224:227], v248 offset:18432\n\t" \
  "ds_read_b128 v[228:231], v248 offset:19456\n\t" \
  "ds_read_b64_tr_b16 v[200:201], v250 offset:2048\n\t" \
  "ds_read_b64_tr_b16 v[202:203], v250 offset:2112\n\t" \
  "ds_read_b64_tr_b16 v[204:205], v250 offset:2560\n\t" \
  "ds_read_b64_tr_b16 v[206:207], v250 offset:2624\n\t" \
  "ds_read_b64_tr_b16 v[208:209], v250 offset:3072\n\t" \
  "ds_read_b64_tr_b16 v[210:211], v250 offset:3136\n\t" \
  "ds_read_b64_tr_b16 v[212:213], v250 offset:3584\n\t" \
  "ds_read_b64_tr_b16 v[214:215], v250 offset:3648\n\t" \
  "s_waitcnt lgkmcnt(10)\n\t" \
  "v_pk_add_f32 v[216:217], v[216:217], v[220:221]\n\t" \
  "v_pk_add_f32 v[218:219], v[218:219], v[222:223]\n\t" \
  "s_waitcnt lgkmcnt(8)\n\t" \
  "v_pk_add_f32 v[224:225], v[224:225], v[228:229]\n\t" \
  "v_pk_add_f32 v[226:227], v[226:227], v[230:231]\n\t" \
  "v_pk_add_f32 v[216:217], v[216:217], v[224:225]\n\t" \
  "v_pk_add_f32 v[218:219], v[218:219], v[226:227]\n\t" \
  "s_waitcnt lgkmcnt(6)\n\t" \
  "v_mfma_f32_16x16x32_bf16 v[200:203], v[200:203], v[232:235], 0\n\t" \
  "v_max3_f32 v224, v216, v217, v218\n\t" \
  "v_max_f32 v224, v224, v219\n\t" \
  "s_waitcnt lgkmcnt(4)\n\t" \
  "v_mfma_f32_16x16x32_bf16 v[200:203], v[204:207], v[236:239], v[200:203]\n\t" \
  "v_max_f32_dpp v225, v224, v224 row_ror:8 row_mask:0xf bank_mask:0xf\n\t" \
  "v_max_f32_dpp v226, v224, v224 row_ror:8 row_mask:0xf bank_mask:0xf\n\t" \
  "s_waitcnt lgkmcnt(2)\n\t" \
  "v_mfma_f32_16x16x32_bf16 v[200:203], v[208:211], v[240:243], v[200:203]\n\t" \
  "v_permlane32_swap_b32 v225, v226\n\t" \
  "s_waitcnt lgkmcnt(0)\n\t" \
  "v_mfma_f32_16x16x32_bf16 v[200:203], v[212:215], v[244:247], v[200:203]\n\t" \
  "v_max_f32 v231, v225, v226\n\t" \
  "v_mov_b32 v220, 0x3f803f80\n\t" \
  "v_mov_b32 v221, 0x3f803f80\n\t" \
  "v_mov_b64 v[222:223], v[220:221]\n\t" \
  "v_mov_b32 v233, 0\n\t" \
  "v_mov_b32 v234, 0\n\t" \
  "s_nop 1\n\t" \
  "v_max3_f32 v204, v200, v201, v231\n\t" \
  "v_max3_f32 v205, v204, v202, v203\n\t" \
  "v_max3_f32 v206, v204, v202, v203\n\t" \
  "v_cndmask_b32 v200, v200, v202, %[up]\n\t" \
  "v_cndmask_b32 v201, v201, v203, %[up]\n\t" \
  "v_permlane16_swap_b32 v205, v206\n\t" \
  "v_max_f32 v204, v205, v206\n\t" \
  "v_mul_f32 v204, %[negc], v204\n\t" \
  "v_fma_f32 v200, v200, %[c], v204\n\t" \
  "v_fma_f32 v201, v201, %[c], v204\n\t" \
  "v_exp_f32 v200, v200\n\t" \
  "v_fma_f32 v216, v216, %[c], v204\n\t" \
  "v_exp_f32 v201, v201\n\t" \
  "v_fma_f32 v217, v217, %[c], v204\n\t" \
  "v_exp_f32 v216, v216\n\t" \
  "v_fma_f32 v218, v218, %[c], v204\n\t" \
  "v_cvt_pk_bf16_f32 v232, v200, v201\n\t" \
  "v_cvt_pk_bf16_f32 v235, v200, v201\n\t" \
  "v_exp_f32 v217, v217\n\t" \
  "v_fma_f32 v219, v219, %[c], v204\n\t" \
  "v_mfma_f32_16x16x16_bf16 v[212:215], v[220:221], v[232:233], 0\n\t" \
  "v_exp_f32 v218, v218\n\t" \
  "s_waitcnt vmcnt(0)\n\t" \
  "v_mfma_f32_16x16x16_bf16 v[224:227], v[252:253], v[232:233], 0\n\t" \
  "v_cvt_pk_bf16_f32 v210, v216, v217\n\t" \
  "v_cvt_pk_bf16_f32 v208, v216, v217\n\t" \
  "v_exp_f32 v219, v219\n\t" \
  "v_mfma_f32_16x16x16_bf16 v[228:231], v[252:253], v[234:235], 0\n\t" \
  "v_cvt_pk_bf16_f32 v211, v218, v219\n\t" \
  "v_cvt_pk_bf16_f32 v209, v218, v219\n\t" \
  "v_mov_b32_dpp v208, v210 row_ror:8 row_mask:0xf bank_mask:0xc\n\t" \
  "v_mov_b32_dpp v210, v210 row_ror:8 row_mask:0xf bank_mask:0x3\n\t" \
  "v_mov_b32_dpp v209, v211 row_ror:8 row_mask:0xf bank_mask:0xc\n\t" \
  "v_mov_b32_dpp v211, v211 row_ror:8 row_mask:0xf bank_mask:0x3\n\t" \
  "v_max_u32 v204, v252, v253\n\t" \
  "v_cmp_le_u32 vcc, %[sent], v204\n\t" \
  "v_mfma_f32_16x16x32_bf16 v[212:215], v[220:223], v[208:211], v[212:215]\n\t" \
  ".Lpv%=:\n\t" \
  "v_mfma_f32_16x16x32_bf16 v[224:227], v[192:195], v[208:211], v[224:227]\n\t" \
  "v_mfma_f32_16x16x32_bf16 v[228:231], v[196:199], v[208:211], v[228:231]\n\t" \
  "s_cbranch_vccnz .Lvs%=\n\t" \
  ".Lvok%=:\n\t" \
  "s_nop 4\n\t" \
  "v_rcp_f32 v204, v212\n\t" \
  "v_cndmask_b32 v224, v224, v226, %[nh]\n\t" \
  "v_cndmask_b32 v225, v225, v227, %[nh]\n\t" \
  "v_mul_f32 v224, v224, v204\n\t" \
  "v_mul_f32 v225, v225, v204\n\t" \
  "v_cvt_pk_bf16_f32 v224, v224, v225\n\t" \
  "buffer_store_dword v224, v251, %[rs], %[so] offen offset:0 sc1\n\t" \
  "v_cndmask_b32 v228, v228, v230, %[nh]\n\t" \
  "v_cndmask_b32 v229, v229, v231, %[nh]\n\t" \
  "v_mul_f32 v228, v228, v204\n\t" \
  "v_mul_f32 v229, v229, v204\n\t" \
  "v_cvt_pk_bf16_f32 v228, v228, v229\n\t" \
  "buffer_store_dword v228, v251, %[rs], %[so] offen offset:256 sc1\n\t" \
  "s_memtime s[90:91]\n\t" \
  "s_setprio 0\n\t" \
  "s_waitcnt lgkmcnt(0)\n\t" \
  "s_mov_b32 %[tout], s90\n\t" \
  "s_mov_b32 %[spins], s92\n\t" \
  "s_branch .Lend%=\n\t" \
  ".Lre%=:\n\t" \
  "ds_read_b64_tr_b16 v[236:237], v250 offset:512\n\t" \
  "ds_read_b64_tr_b16 v[238:239], v250 offset:576\n\t" \
  "ds_read_b64_tr_b16 v[240:241], v250 offset:1024\n\t" \
  "ds_read_b64_tr_b16 v[242:243], v250 offset:1088\n\t" \
  "ds_read_b64_tr_b16 v[244:245], v250 offset:1536\n\t" \
  "ds_read_b64_tr_b16 v[246:247], v250 offset:1600\n\t" \
  "s_waitcnt lgkmcnt(0)\n\t" \
  "s_branch .Lrb%=\n\t" \
  ".Lvs%=:\n\t" \
  "v_cmp_eq_u32 vcc, %[sent], v252\n\t" \
  "v_cmp_eq_u32 s[94:95], %[sent], v253\n\t" \
  "s_or_b64 s[94:95], s[94:95], vcc\n\t" \
  "s_cbranch_scc0 .Lvok%=\n\t" \
  "s_add_u32 s92, s92, 1\n\t" \
  "s_sleep 2\n\t" \
  "s_and_b32 s93, s92, 63\n\t" \
  "s_cbranch_scc1 .Lvr%=\n\t" \
  "buffer_load_dword v204, off, %[rs], %[sab] sc1\n\t" \
  "s_waitcnt vmcnt(0)\n\t" \
  "v_readfirstlane_b32 s93, v204\n\t" \
  "s_nop 3\n\t" \
  "s_cmp_eq_u32 s93, 1\n\t" \
  "s_cbranch_scc1 .Lvok%=\n\t" \
  "s_cmp_lt_u32 s92, 0x80000\n\t" \
  "s_cbranch_scc1 .Lvr%=\n\t" \
  "v_mov_b32 v204, 1\n\t" \
  "buffer_store_dword v204, off, %[rs], %[sab] sc1\n\t" \
  "s_branch .Lvok%=\n\t" \
  ".Lvr%=:\n\t" \
  "buffer_load_dword v252, v249, %[rs], %[sv] offen offset:0 sc1\n\t" \
  "buffer_load_dword v253, v249, %[rs], %[sv] offen offset:256 sc1\n\t" \
  "s_waitcnt vmcnt(0)\n\t" \
  "v_mfma_f32_16x16x16_bf16 v[224:227], v[252:253], v[232:233], 0\n\t" \
  "v_mfma_f32_16x16x16_bf16 v[228:231], v[252:253], v[234:235], 0\n\t" \
  "v_max_u32 v204, v252, v253\n\t" \
  "v_cmp_le_u32 vcc, %[sent], v204\n\t" \
  "s_nop 4\n\t" \
  "s_branch .Lpv%=\n\t" \
  "s_branch .Lend%=\n\t" \
  ".Lslow%=:\n\t" \
  "s_mov_b64 s[94:95], 0\n\t" \
  "v_cmp_eq_u32 vcc, %[sent], v200\n\t" \
  "s_or_b64 s[94:95], s[94:95], vcc\n\t" \
  "v_cmp_eq_u32 vcc, %[sent], v201\n\t" \
  "s_or_b64 s[94:95], s[94:95], vcc\n\t" \
  "v_cmp_eq_u32 vcc, %[sent], v202\n\t" \
  "s_or_b64 s[94:95], s[94:95], vcc\n\t" \
  "v_cmp_eq_u32 vcc, %[sent], v203\n\t" \
  "s_or_b64 s[94:95], s[94:95], vcc\n\t" \
  "v_cmp_eq_u32 vcc, %[sent], v204\n\t" \
  "s_or_b64 s[94:95], s[94:95], vcc\n\t" \
  "v_cmp_eq_u32 vcc, %[sent], v205\n\t" \
  "s_or_b64 s[94:95], s[94:95], vcc\n\t" \
  "v_cmp_eq_u32 vcc, %[sent], v206\n\t" \
  "s_or_b64 s[94:95], s[94:95], vcc\n\t" \
  "v_cmp_eq_u32 vcc, %[sent], v207\n\t" \
  "s_or_b64 s[94:95], s[94:95], vcc\n\t" \
  "s_cmp_eq_u64 s[94:95], 0\n\t" \
  "s_cbranch_scc1 .Lok%=\n\t" \
  "v_mov_b64 v[192:193], 0\n\t" \
  "v_mov_b64 v[194:195], 0\n\t" \
  "v_mov_b64 v[196:197], 0\n\t" \
  "v_mov_b64 v[198:199], 0\n\t" \
  "s_add_u32 s92, s92, 1\n\t" \
  "s_sleep 6\n\t" \
  "s_cmp_eq_u32 s92, 1\n\t" \
  "s_cbranch_scc1 .Lpoll%=\n\t" \
  "s_sleep 1\n\t" \
  "s_and_b32 s93, s92, 63\n\t" \
  "s_cmp_lg_u32 s93, 0\n\t" \
  "s_cbranch_scc1 .Lpoll%=\n\t" \
  "v_mov_b32 v245, 0\n\t" \
  "buffer_load_dword v244, v245, %[rs], %[sab] offen sc1\n\t" \
  "s_waitcnt vmcnt(0)\n\t" \
  "v_readfirstlane_b32 s93, v244\n\t" \
  "s_nop 3\n\t" \
  "s_cmp_eq_u32 s93, 1\n\t" \
  "s_cbranch_scc1 .Lok%=\n\t" \
  "s_cmp_lt_u32 s92, 0x80000\n\t" \
  "s_cbranch_scc1 .Lpoll%=\n\t" \
  "v_mov_b32 v244, 1\n\t" \
  "buffer_store_dword v244, v245, %[rs], %[sab] offen sc1\n\t" \
  "s_branch .Lok%=\n\t" \
  ".Lend%=:\n\t"
#define QK_ASM_1 \
  "s_waitcnt lgkmcnt(0)\n\t" \
  "v_subrev_u32 v244, 0x8000, v250\n\t" \
  "ds_read_b128 v[216:219], v248 offset:36864\n\t" \
  "ds_read_b128 v[220:223], v248 offset:37888\n\t" \
  "ds_read_b128 v[224:227], v248 offset:38912\n\t" \
  "ds_read_b128 v[228:231], v248 offset:39936\n\t" \
  "ds_read_b128 v[208:211], v244 offset:0\n\t" \
  "ds_read_b128 v[212:215], v244 offset:1024\n\t" \
  "ds_read_b128 v[236:239], v244 offset:2048\n\t" \
  "ds_read_b128 v[240:243], v244 offset:3072\n\t" \
  "v_mov_b64 v[192:193], 0\n\t" \
  "v_mov_b64 v[194:195], 0\n\t" \
  "v_mov_b64 v[196:197], 0\n\t" \
  "v_mov_b64 v[198:199], 0\n\t" \
  "s_movk_i32 s92, 0x2000\n\t" \
  ".Lwt%=:\n\t" \
  "ds_read_b64 v[200:201], v248 offset:30720\n\t" \
  "s_memrealtime s[90:91]\n\t" \
  "s_sub_u32 s92, s92, 1\n\t" \
  "s_waitcnt lgkmcnt(0)\n\t" \
  "v_readfirstlane_b32 s93, v200\n\t" \
  "v_readfirstlane_b32 %[tgt], v201\n\t" \
  "s_cbranch_scc1 .Lgo%=\n\t" \
  "s_cmp_eq_u32 s93, %[step]\n\t" \
  "s_cbranch_scc1 .Lwx%=\n\t" \
  "s_sleep 1\n\t" \
  "s_branch .Lwt%=\n\t" \
  ".Lwx%=:\n\t" \
  "s_lshl_b32 s93, s90, 2\n\t" \
  "s_sub_i32 s93, %[tgt], s93\n\t" \
  "s_cmp_le_i32 s93, 0\n\t" \
  "s_cbranch_scc1 .Lgo%=\n\t" \
  "s_min_i32 s93, s93, 8000\n\t" \
  "s_add_u32 s93, s93, 3\n\t" \
  "s_lshr_b32 s93, s93, 2\n\t" \
  "s_add_u32 s93, s93, s90\n\t" \
  "s_memrealtime s[90:91]\n\t" \
  ".Lpace%=:\n\t" \
  "s_memrealtime s[94:95]\n\t" \
  "s_waitcnt lgkmcnt(1)\n\t" \
  "s_sub_i32 s91, s90, s93\n\t" \
  "s_cmp_ge_i32 s91, 0\n\t" \
  "s_cbranch_scc1 .Lgo%=\n\t" \
  "s_memrealtime s[90:91]\n\t" \
  "s_waitcnt lgkmcnt(1)\n\t" \
  "s_sub_i32 s95, s94, s93\n\t" \
  "s_cmp_lt_i32 s95, 0\n\t" \
  "s_cbranch_scc1 .Lpace%=\n\t" \
  ".Lgo%=:\n\t" \
  "s_mov_b32 s92, 0\n\t" \
  ".Lpoll%=:\n\t" \
  "buffer_load_dwordx4 v[200:203], v248, %[rs], %[som] offen sc1\n\t" \
  "buffer_load_dwordx4 v[204:207], v248, %[rs], %[som2] offen sc1\n\t" \
  "s_waitcnt vmcnt(1) lgkmcnt(0)\n\t" \
  "ds_write_b128 v249, v[200:203]\n\t" \
  "v_smfmac_f32_16x16x64_bf16 v[192:195], v[200:203], v[208:215], v251\n\t" \
  "v_max3_u32 v244, v200, v201, v202\n\t" \
  "v_smfmac_f32_16x16x64_bf16 v[196:199], v[200:203], v[236:243], v251\n\t" \
  "s_waitcnt vmcnt(0)\n\t" \
  "ds_write_b128 v249, v[204:207] offset:8192\n\t" \
  "v_smfmac_f32_16x16x64_bf16 v[192:195], v[204:207], v[216:223], v251\n\t" \
  "v_max3_u32 v244, v244, v203, v204\n\t" \
  "v_max3_u32 v244, v244, v205, v206\n\t" \
  "v_smfmac_f32_16x16x64_bf16 v[196:199], v[204:207], v[224:231], v251\n\t" \
  "v_max_u32 v244, v244, v207\n\t" \
  "v_cmp_le_u32 vcc, %[sent], v244\n\t" \
  "s_cbranch_vccnz .Lslow%=\n\t" \
  ".Lok%=:\n\t" \
  "ds_read_b128 v[216:219], v250 offset:32768\n\t" \
  "ds_read_b128 v[220:223], v250 offset:33792\n\t" \
  "ds_read_b128 v[224:227], v250 offset:34816\n\t" \
  "ds_read_b128 v[228:231], v250 offset:35840\n\t" \
  "s_waitcnt lgkmcnt(4)\n\t" \
  "s_barrier\n\t" \
  "ds_read_b128 v[208:211], v248 offset:0\n\t" \
  "ds_read_b128 v[212:215], v248 offset:3072\n\t" \
  "ds_read_b128 v[236:239], v248 offset:4096\n\t" \
  "ds_read_b128 v[240:243], v248 offset:5120\n\t" \
  "s_waitcnt lgkmcnt(3)\n\t" \
  "ds_read_b128 v[244:247], v248 offset:12288\n\t" \
  "v_smfmac_f32_16x16x64_bf16 v[192:195], v[208:211], v[216:223], v251\n\t" \
  "ds_read_b128 v[216:219], v250 offset:0\n\t" \
  "v_smfmac_f32_16x16x64_bf16 v[196:199], v[208:211], v[224:231], v251\n\t" \
  "s_waitcnt lgkmcnt(4)\n\t" \
  "ds_read_b128 v[220:223], v250 offset:1024\n\t" \
  "v_smfmac_f32_16x16x64_bf16 v[192:195], v[212:215], v[0:7], v251\n\t" \
  "ds_read_b128 v[224:227], v250 offset:2048\n\t" \
  "v_smfmac_f32_16x16x64_bf16 v[196:199], v[212:215], v[8:15], v251\n\t" \
  "s_waitcnt lgkmcnt(5)\n\t" \
  "ds_read_b128 v[228:231], v250 offset:3072\n\t" \
  "v_smfmac_f32_16x16x64_bf16 v[192:195], v[236:239], v[16:23], v251\n\t" \
  "ds_read_b128 v[200:203], v248 offset:6144\n\t" \
  "v_smfmac_f32_16x16x64_bf16 v[196:199], v[236:239], v[24:31], v251\n\t" \
  "s_waitcnt lgkmcnt(6)\n\t" \
  "ds_read_b128 v[204:207], v248 offset:7168\n\t" \
  "v_smfmac_f32_16x16x64_bf16 v[192:195], v[240:243], v[32:39], v251\n\t" \
  "ds_read_b128 v[208:211], v248 offset:8192\n\t" \
  "v_smfmac_f32_16x16x64_bf16 v[196:199], v[240:243], v[40:47], v251\n\t" \
  "s_waitcnt lgkmcnt(5)\n\t" \
  "ds_read_b128 v[212:215], v248 offset:10240\n\t" \
  "v_smfmac_f32_16x16x64_bf16 v[192:195], v[244:247], v[216:223], v251\n\t" \
  "s_waitcnt lgkmcnt(4)\n\t" \
  "v_smfmac_f32_16x16x64_bf16 v[196:199], v[244:247], v[224:231], v251\n\t" \
  "s_waitcnt lgkmcnt(3)\n\t" \
  "ds_read_b128 v[236:239], v248 offset:11264\n\t" \
  "v_smfmac_f32_16x16x64_bf16 v[192:195], v[200:203], v[48:55], v251\n\t" \
  "ds_read_b128 v[240:243], v248 offset:13312\n\t" \
  "v_smfmac_f32_16x16x64_bf16 v[196:199], v[200:203], v[56:63], v251\n\t" \
  "s_waitcnt lgkmcnt(4)\n\t" \
  "v_smfmac_f32_16x16x64_bf16 v[192:195], v[204:207], v[64:71], v251\n\t" \
  "ds_read_b128 v[244:247], v248 offset:14336\n\t" \
  "v_smfmac_f32_16x16x64_bf16 v[196:199], v[204:207], v[72:79], v251\n\t" \
  "s_waitcnt lgkmcnt(4)\n\t" \
  "v_smfmac_f32_16x16x64_bf16 v[192:195], v[208:211], v[80:87], v251\n\t" \
  "ds_read_b128 v[200:203], v248 offset:15360\n\t" \
  "v_smfmac_f32_16x16x64_bf16 v[196:199], v[208:211], v[88:95], v251\n\t" \
  "s_waitcnt lgkmcnt(4)\n\t" \
  "v_smfmac_f32_16x16x64_bf16 v[192:195], v[212:215], v[96:103], v251\n\t" \
  "ds_read_b128 v[204:207], v248 offset:2048\n\t" \
  "v_smfmac_f32_16x16x64_bf16 v[196:199], v[212:215], v[104:111], v251\n\t" \
  "s_waitcnt lgkmcnt(3)\n\t" \
  "v_smfmac_f32_16x16x64_bf16 v[192:195], v[236:239], v[112:119], v251\n\t" \
  "v_smfmac_f32_16x16x64_bf16 v[196:199], v[236:239], v[120:127], v251\n\t" \
  "v_smfmac_f32_16x16x64_bf16 v[192:195], v[240:243], v[128:135], v251\n\t" \
  "v_smfmac_f32_16x16x64_bf16 v[196:199], v[240:243], v[136:143], v251\n\t" \
  "s_waitcnt lgkmcnt(1)\n\t" \
  "v_smfmac_f32_16x16x64_bf16 v[192:195], v[244:247], v[144:151], v251\n\t" \
  "v_smfmac_f32_16x16x64_bf16 v[196:199], v[244:247], v[152:159], v251\n\t" \
  "v_smfmac_f32_16x16x64_bf16 v[192:195], v[200:203], v[160:167], v251\n\t" \
  "v_smfmac_f32_16x16x64_bf16 v[196:199], v[200:203], v[168:175], v251\n\t" \
  "s_waitcnt lgkmcnt(0)\n\t" \
  "v_smfmac_f32_16x16x64_bf16 v[192:195], v[204:207], v[176:183], v251\n\t" \
  "v_smfmac_f32_16x16x64_bf16 v[196:199], v[204:207], v[184:191], v251\n\t" \
  "ds_read_b128 v[200:203], v248 offset:17408\n\t" \
  "ds_read_b128 v[204:207], v248 offset:21504\n\t" \
  "v_mov_b32 v250, v232\n\t" \
  "v_mov_b32 v249, v233\n\t" \
  "s_nop 2\n\t" \
  "v_add_f32 v192, v192, v194\n\t" \
  "v_add_f32 v193, v193, v195\n\t" \
  "v_add_f32 v196, v196, v198\n\t" \
  "v_add_f32 v197, v197, v199\n\t" \
  "v_cvt_pk_bf16_f32 v192, v192, v193\n\t" \
  "v_cvt_pk_bf16_f32 v196, v196, v197\n\t" \
  "ds_write2_b32 v252, v192, v196 offset1:64\n\t" \
  "v_mov_b32 v251, v234\n\t" \
  "ds_read_b64_tr_b16 v[236:237], v250 offset:512\n\t" \
  "ds_read_b64_tr_b16 v[238:239], v250 offset:576\n\t" \
  "v_mov_b32 v212, %[step]\n\t" \
  "s_mov_b64 exec, 1\n\t" \
  "ds_write_b32 v248, v212 offset:28688\n\t" \
  "s_mov_b64 exec, -1\n\t" \
  "buffer_load_dwordx4 v[192:195], v248, %[rt], %[svt] offen offset:0 sc1\n\t" \
  "buffer_load_dwordx4 v[196:199], v248, %[rt], %[svt] offen offset:1024 sc1\n\t" \
  "s_waitcnt lgkmcnt(1)\n\t" \
  "v_mfma_f32_16x16x32_bf16 v[200:203], v[200:203], v[236:239], 0\n\t" \
  "v_mfma_f32_16x16x32_bf16 v[204:207], v[204:207], v[236:239], 0\n\t" \
  "s_nop 7\n\t" \
  "v_cndmask_b32 v216, v200, v204, %[nh]\n\t" \
  "v_cndmask_b32 v217, v201, v205, %[nh]\n\t" \
  "v_cndmask_b32 v218, v202, v206, %[nh]\n\t" \
  "v_cndmask_b32 v219, v203, v207, %[nh]\n\t" \
  "ds_write_b128 v248, v[216:219] offset:17408\n\t" \
  "buffer_load_dword v252, v249, %[rs], %[sv] offen offset:0 sc1\n\t" \
  "buffer_load_dword v253, v249, %[rs], %[sv] offen offset:256 sc1\n\t" \
  "ds_read_b32 v212, v248 offset:28672\n\t" \
  "ds_read_b64_tr_b16 v[240:241], v250 offset:1024\n\t" \
  "ds_read_b64_tr_b16 v[242:243], v250 offset:1088\n\t" \
  "ds_read_b64_tr_b16 v[244:245], v250 offset:1536\n\t" \
  "ds_read_b64_tr_b16 v[246:247], v250 offset:1600\n\t" \
  "ds_read_b64_tr_b16 v[232:233], v250 offset:0\n\t" \
  "ds_read_b64_tr_b16 v[234:235], v250 offset:64\n\t" \
  "s_setprio 3\n\t" \
  "s_waitcnt lgkmcnt(0)\n\t" \
  "v_cmp_ne_u32 vcc, %[step], v212\n\t" \
  "s_barrier\n\t" \
  "s_and_b32 s93, vcc_lo, 0xf\n\t" \
  "s_cbranch_scc1 .Lre%=\n\t" \
  ".Lrb%=:\n\t" \
  "ds_read_b128 v[220:223], v248 offset:18432\n\t" \
  "ds_read_b128 v[224:227], v248 offset:19456\n\t" \
  "ds_read_b128 v[228:231], v248 offset:16384\n\t" \
  "ds_read_b64_tr_b16 v[204:205], v250 offset:2560\n\t" \
  "ds_read_b64_tr_b16 v[206:207], v250 offset:2624\n\t" \
  "ds_read_b64_tr_b16 v[208:209], v250 offset:3072\n\t" \
  "ds_read_b64_tr_b16 v[210:211], v250 offset:3136\n\t" \
  "ds_read_b64_tr_b16 v[212:213], v250 offset:3584\n\t" \
  "ds_read_b64_tr_b16 v[214:215], v250 offset:3648\n\t" \
  "ds_read_b64_tr_b16 v[200:201], v250 offset:2048\n\t" \
  "ds_read_b64_tr_b16 v[202:203], v250 offset:2112\n\t" \
  "s_waitcnt lgkmcnt(10)\n\t" \
  "v_pk_add_f32 v[216:217], v[216:217], v[220:221]\n\t" \
  "v_pk_add_f32 v[218:219], v[218:219], v[222:223]\n\t" \
  "s_waitcnt lgkmcnt(8)\n\t" \
  "v_pk_add_f32 v[224:225], v[224:225], v[228:229]\n\t" \
  "v_pk_add_f32 v[226:227], v[226:227], v[230:231]\n\t" \
  "v_pk_add_f32 v[216:217], v[216:217], v[224:225]\n\t" \
  "v_pk_add_f32 v[218:219], v[218:219], v[226:227]\n\t" \
  "s_waitcnt lgkmcnt(6)\n\t" \
  "v_mfma_f32_16x16x32_bf16 v[204:207], v[204:207], v[236:239], 0\n\t" \
  "v_max3_f32 v224, v216, v217, v218\n\t" \
  "v_max_f32 v224, v224, v219\n\t" \
  "s_waitcnt lgkmcnt(4)\n\t" \
  "v_mfma_f32_16x16x32_bf16 v[204:207], v[208:211], v[240:243], v[204:207]\n\t" \
  "v_max_f32_dpp v225, v224, v224 row_ror:8 row_mask:0xf bank_mask:0xf\n\t" \
  "v_max_f32_dpp v226, v224, v224 row_ror:8 row_mask:0xf bank_mask:0xf\n\t" \
  "s_waitcnt lgkmcnt(2)\n\t" \
  "v_mfma_f32_16x16x32_bf16 v[204:207], v[212:215], v[244:247], v[204:207]\n\t" \
  "v_permlane32_swap_b32 v225, v226\n\t" \
  "s_waitcnt lgkmcnt(0)\n\t" \
  "v_mfma_f32_16x16x32_bf16 v[204:207], v[200:203], v[232:235], v[204:207]\n\t" \
  "v_max_f32 v231, v225, v226\n\t" \
  "v_mov_b32 v220, 0x3f803f80\n\t" \
  "v_mov_b32 v221, 0x3f803f80\n\t" \
  "v_mov_b64 v[222:223], v[220:221]\n\t" \
  "v_mov_b32 v237, 0\n\t" \
  "v_mov_b32 v238, 0\n\t" \
  "s_nop 1\n\t" \
  "v_max3_f32 v208, v204, v205, v231\n\t" \
  "v_max3_f32 v209, v208, v206, v207\n\t" \
  "v_max3_f32 v210, v208, v206, v207\n\t" \
  "v_cndmask_b32 v204, v204, v206, %[up]\n\t" \
  "v_cndmask_b32 v205, v205, v207, %[up]\n\t" \
  "v_permlane16_swap_b32 v209, v210\n\t" \
  "v_max_f32 v208, v209, v210\n\t" \
  "v_mul_f32 v208, %[negc], v208\n\t" \
  "v_fma_f32 v204, v204, %[c], v208\n\t" \
  "v_fma_f32 v205, v205, %[c], v208\n\t" \
  "v_exp_f32 v204, v204\n\t" \
  "v_fma_f32 v216, v216, %[c], v208\n\t" \
  "v_exp_f32 v205, v205\n\t" \
  "v_fma_f32 v217, v217, %[c], v208\n\t" \
  "v_exp_f32 v216, v216\n\t" \
  "v_fma_f32 v218, v218, %[c], v208\n\t" \
  "v_cvt_pk_bf16_f32 v236, v204, v205\n\t" \
  "v_cvt_pk_bf16_f32 v239, v204, v205\n\t" \
  "v_exp_f32 v217, v217\n\t" \
  "v_fma_f32 v219, v219, %[c], v208\n\t" \
  "v_mfma_f32_16x16x16_bf16 v[200:203], v[220:221], v[236:237], 0\n\t" \
  "v_exp_f32 v218, v218\n\t" \
  "s_waitcnt vmcnt(0)\n\t" \
  "v_mfma_f32_16x16x16_bf16 v[224:227], v[252:253], v[236:237], 0\n\t" \
  "v_cvt_pk_bf16_f32 v214, v216, v217\n\t" \
  "v_cvt_pk_bf16_f32 v212, v216, v217\n\t" \
  "v_exp_f32 v219, v219\n\t" \
  "v_mfma_f32_16x16x16_bf16 v[228:231], v[252:253], v[238:239], 0\n\t" \
  "v_cvt_pk_bf16_f32 v215, v218, v219\n\t" \
  "v_cvt_pk_bf16_f32 v213, v218, v219\n\t" \
  "v_mov_b32_dpp v212, v214 row_ror:8 row_mask:0xf bank_mask:0xc\n\t" \
  "v_mov_b32_dpp v214, v214 row_ror:8 row_mask:0xf bank_mask:0x3\n\t" \
  "v_mov_b32_dpp v213, v215 row_ror:8 row_mask:0xf bank_mask:0xc\n\t" \
  "v_mov_b32_dpp v215, v215 row_ror:8 row_mask:0xf bank_mask:0x3\n\t" \
  "v_max_u32 v208, v252, v253\n\t" \
  "v_cmp_le_u32 vcc, %[sent], v208\n\t" \
  "v_mfma_f32_16x16x32_bf16 v[200:203], v[220:223], v[212:215], v[200:203]\n\t" \
  ".Lpv%=:\n\t" \
  "v_mfma_f32_16x16x32_bf16 v[224:227], v[192:195], v[212:215], v[224:227]\n\t" \
  "v_mfma_f32_16x16x32_bf16 v[228:231], v[196:199], v[212:215], v[228:231]\n\t" \
  "s_cbranch_vccnz .Lvs%=\n\t" \
  ".Lvok%=:\n\t" \
  "s_nop 4\n\t" \
  "v_rcp_f32 v208, v200\n\t" \
  "v_cndmask_b32 v224, v224, v226, %[nh]\n\t" \
  "v_cndmask_b32 v225, v225, v227, %[nh]\n\t" \
  "v_mul_f32 v224, v224, v208\n\t" \
  "v_mul_f32 v225, v225, v208\n\t" \
  "v_cvt_pk_bf16_f32 v224, v224, v225\n\t" \
  "buffer_store_dword v224, v251, %[rs], %[so] offen offset:0 sc1\n\t" \
  "v_cndmask_b32 v228, v228, v230, %[nh]\n\t" \
  "v_cndmask_b32 v229, v229, v231, %[nh]\n\t" \
  "v_mul_f32 v228, v228, v208\n\t" \
  "v_mul_f32 v229, v229, v208\n\t" \
  "v_cvt_pk_bf16_f32 v228, v228, v229\n\t" \
  "buffer_store_dword v228, v251, %[rs], %[so] offen offset:256 sc1\n\t" \
  "s_memtime s[90:91]\n\t" \
  "s_setprio 0\n\t" \
  "s_waitcnt lgkmcnt(0)\n\t" \
  "s_mov_b32 %[tout], s90\n\t" \
  "s_mov_b32 %[spins], s92\n\t" \
  "s_branch .Lend%=\n\t" \
  ".Lre%=:\n\t" \
  "ds_read_b64_tr_b16 v[240:241], v250 offset:1024\n\t" \
  "ds_read_b64_tr_b16 v[242:243], v250 offset:1088\n\t" \
  "ds_read_b64_tr_b16 v[244:245], v250 offset:1536\n\t" \
  "ds_read_b64_tr_b16 v[246:247], v250 offset:1600\n\t" \
  "ds_read_b64_tr_b16 v[232:233], v250 offset:0\n\t" \
  "ds_read_b64_tr_b16 v[234:235], v250 offset:64\n\t" \
  "s_waitcnt lgkmcnt(0)\n\t" \
  "s_branch .Lrb%=\n\t" \
  ".Lvs%=:\n\t" \
  "v_cmp_eq_u32 vcc, %[sent], v252\n\t" \
  "v_cmp_eq_u32 s[94:95], %[sent], v253\n\t" \
  "s_or_b64 s[94:95], s[94:95], vcc\n\t" \
  "s_cbranch_scc0 .Lvok%=\n\t" \
  "s_add_u32 s92, s92, 1\n\t" \
  "s_sleep 2\n\t" \
  "s_and_b32 s93, s92, 63\n\t" \
  "s_cbranch_scc1 .Lvr%=\n\t" \
  "buffer_load_dword v208, off, %[rs], %[sab] sc1\n\t" \
  "s_waitcnt vmcnt(0)\n\t" \
  "v_readfirstlane_b32 s93, v208\n\t" \
  "s_nop 3\n\t" \
  "s_cmp_eq_u32 s93, 1\n\t" \
  "s_cbranch_scc1 .Lvok%=\n\t" \
  "s_cmp_lt_u32 s92, 0x80000\n\t" \
  "s_cbranch_scc1 .Lvr%=\n\t" \
  "v_mov_b32 v208, 1\n\t" \
  "buffer_store_dword v208, off, %[rs], %[sab] sc1\n\t" \
  "s_branch .Lvok%=\n\t" \
  ".Lvr%=:\n\t" \
  "buffer_load_dword v252, v249, %[rs], %[sv] offen offset:0 sc1\n\t" \
  "buffer_load_dword v253, v249, %[rs], %[sv] offen offset:256 sc1\n\t" \
  "s_waitcnt vmcnt(0)\n\t" \
  "v_mfma_f32_16x16x16_bf16 v[224:227], v[252:253], v[236:237], 0\n\t" \
  "v_mfma_f32_16x16x16_bf16 v[228:231], v[252:253], v[238:239], 0\n\t" \
  "v_max_u32 v208, v252, v253\n\t" \
  "v_cmp_le_u32 vcc, %[sent], v208\n\t" \
  "s_nop 4\n\t" \
  "s_branch .Lpv%=\n\t" \
  "s_branch .Lend%=\n\t" \
  ".Lslow%=:\n\t" \
  "s_mov_b64 s[94:95], 0\n\t" \
  "v_cmp_eq_u32 vcc, %[sent], v200\n\t" \
  "s_or_b64 s[94:95], s[94:95], vcc\n\t" \
  "v_cmp_eq_u32 vcc, %[sent], v201\n\t" \
  "s_or_b64 s[94:95], s[94:95], vcc\n\t" \
  "v_cmp_eq_u32 vcc, %[sent], v202\n\t" \
  "s_or_b64 s[94:95], s[94:95], vcc\n\t" \
  "v_cmp_eq_u32 vcc, %[sent], v203\n\t" \
  "s_or_b64 s[94:95], s[94:95], vcc\n\t" \
  "v_cmp_eq_u32 vcc, %[sent], v204\n\t" \
  "s_or_b64 s[94:95], s[94:95], vcc\n\t" \
  "v_cmp_eq_u32 vcc, %[sent], v205\n\t" \
  "s_or_b64 s[94:95], s[94:95], vcc\n\t" \
  "v_cmp_eq_u32 vcc, %[sent], v206\n\t" \
  "s_or_b64 s[94:95], s[94:95], vcc\n\t" \
  "v_cmp_eq_u32 vcc, %[sent], v207\n\t" \
  "s_or_b64 s[94:95], s[94:95], vcc\n\t" \
  "s_cmp_eq_u64 s[94:95], 0\n\t" \
  "s_cbranch_scc1 .Lok%=\n\t" \
  "v_mov_b64 v[192:193], 0\n\t" \
  "v_mov_b64 v[194:195], 0\n\t" \
  "v_mov_b64 v[196:197], 0\n\t" \
  "v_mov_b64 v[198:199], 0\n\t" \
  "s_add_u32 s92, s92, 1\n\t" \
  "s_sleep 6\n\t" \
  "s_cmp_eq_u32 s92, 1\n\t" \
  "s_cbranch_scc1 .Lpoll%=\n\t" \
  "s_sleep 1\n\t" \
  "s_and_b32 s93, s92, 63\n\t" \
  "s_cmp_lg_u32 s93, 0\n\t" \
  "s_cbranch_scc1 .Lpoll%=\n\t" \
  "v_mov_b32 v245, 0\n\t" \
  "buffer_load_dword v244, v245, %[rs], %[sab] offen sc1\n\t" \
  "s_waitcnt vmcnt(0)\n\t" \
  "v_readfirstlane_b32 s93, v244\n\t" \
  "s_nop 3\n\t" \
  "s_cmp_eq_u32 s93, 1\n\t" \
  "s_cbranch_scc1 .Lok%=\n\t" \
  "s_cmp_lt_u32 s92, 0x80000\n\t" \
  "s_cbranch_scc1 .Lpoll%=\n\t" \
  "v_mov_b32 v244, 1\n\t" \
  "buffer_store_dword v244, v245, %[rs], %[sab] offen sc1\n\t" \
  "s_branch .Lok%=\n\t" \
  ".Lend%=:\n\t"
#define QK_ASM_2 \
  "s_waitcnt lgkmcnt(0)\n\t" \
  "v_subrev_u32 v244, 0x8000, v250\n\t" \
  "ds_read_b128 v[216:219], v248 offset:40960\n\t" \
  "ds_read_b128 v[220:223], v248 offset:41984\n\t" \
  "ds_read_b128 v[224:227], v248 offset:43008\n\t" \
  "ds_read_b128 v[228:231], v248 offset:44032\n\t" \
  "ds_read_b128 v[208:211], v244 offset:0\n\t" \
  "ds_read_b128 v[212:215], v244 offset:1024\n\t" \
  "ds_read_b128 v[236:239], v244 offset:2048\n\t" \
  "ds_read_b128 v[240:243], v244 offset:3072\n\t" \
  "v_mov_b64 v[192:193], 0\n\t" \
  "v_mov_b64 v[194:195], 0\n\t" \
  "v_mov_b64 v[196:197], 0\n\t" \
  "v_mov_b64 v[198:199], 0\n\t" \
  "s_movk_i32 s92, 0x2000\n\t" \
  ".Lwt%=:\n\t" \
  "ds_read_b64 v[200:201], v248 offset:30720\n\t" \
  "s_memrealtime s[90:91]\n\t" \
  "s_sub_u32 s92, s92, 1\n\t" \
  "s_waitcnt lgkmcnt(0)\n\t" \
  "v_readfirstlane_b32 s93, v200\n\t" \
  "v_readfirstlane_b32 %[tgt], v201\n\t" \
  "s_cbranch_scc1 .Lgo%=\n\t" \
  "s_cmp_eq_u32 s93, %[step]\n\t" \
  "s_cbranch_scc1 .Lwx%=\n\t" \
  "s_sleep 1\n\t" \
  "s_branch .Lwt%=\n\t" \
  ".Lwx%=:\n\t" \
  "s_lshl_b32 s93, s90, 2\n\t" \
  "s_sub_i32 s93, %[tgt], s93\n\t" \
  "s_cmp_le_i32 s93, 0\n\t" \
  "s_cbranch_scc1 .Lgo%=\n\t" \
  "s_min_i32 s93, s93, 8000\n\t" \
  "s_add_u32 s93, s93, 3\n\t" \
  "s_lshr_b32 s93, s93, 2\n\t" \
  "s_add_u32 s93, s93, s90\n\t" \
  "s_memrealtime s[90:91]\n\t" \
  ".Lpace%=:\n\t" \
  "s_memrealtime s[94:95]\n\t" \
  "s_waitcnt lgkmcnt(1)\n\t" \
  "s_sub_i32 s91, s90, s93\n\t" \
  "s_cmp_ge_i32 s91, 0\n\t" \
  "s_cbranch_scc1 .Lgo%=\n\t" \
  "s_memrealtime s[90:91]\n\t" \
  "s_waitcnt lgkmcnt(1)\n\t" \
  "s_sub_i32 s95, s94, s93\n\t" \
  "s_cmp_lt_i32 s95, 0\n\t" \
  "s_cbranch_scc1 .Lpace%=\n\t" \
  ".Lgo%=:\n\t" \
  "s_mov_b32 s92, 0\n\t" \
  ".Lpoll%=:\n\t" \
  "buffer_load_dwordx4 v[200:203], v248, %[rs], %[som] offen sc1\n\t" \
  "buffer_load_dwordx4 v[204:207], v248, %[rs], %[som2] offen sc1\n\t" \
  "s_waitcnt vmcnt(1) lgkmcnt(0)\n\t" \
  "ds_write_b128 v249, v[200:203]\n\t" \
  "v_smfmac_f32_16x16x64_bf16 v[192:195], v[200:203], v[208:215], v251\n\t" \
  "v_max3_u32 v244, v200, v201, v202\n\t" \
  "v_smfmac_f32_16x16x64_bf16 v[196:199], v[200:203], v[236:243], v251\n\t" \
  "s_waitcnt vmcnt(0)\n\t" \
  "ds_write_b128 v249, v[204:207] offset:8192\n\t" \
  "v_smfmac_f32_16x16x64_bf16 v[192:195], v[204:207], v[216:223], v251\n\t" \
  "v_max3_u32 v244, v244, v203, v204\n\t" \
  "v_max3_u32 v244, v244, v205, v206\n\t" \
  "v_smfmac_f32_16x16x64_bf16 v[196:199], v[204:207], v[224:231], v251\n\t" \
  "v_max_u32 v244, v244, v207\n\t" \
  "v_cmp_le_u32 vcc, %[sent], v244\n\t" \
  "s_cbranch_vccnz .Lslow%=\n\t" \
  ".Lok%=:\n\t" \
  "ds_read_b128 v[216:219], v250 offset:32768\n\t" \
  "ds_read_b128 v[220:223], v250 offset:33792\n\t" \
  "ds_read_b128 v[224:227], v250 offset:34816\n\t" \
  "ds_read_b128 v[228:231], v250 offset:35840\n\t" \
  "s_waitcnt lgkmcnt(4)\n\t" \
  "s_barrier\n\t" \
  "ds_read_b128 v[208:211], v248 offset:3072\n\t" \
  "ds_read_b128 v[212:215], v248 offset:4096\n\t" \
  "ds_read_b128 v[236:239], v248 offset:5120\n\t" \
  "ds_read_b128 v[240:243], v248 offset:6144\n\t" \
  "s_waitcnt lgkmcnt(3)\n\t" \
  "ds_read_b128 v[244:247], v248 offset:13312\n\t" \
  "v_smfmac_f32_16x16x64_bf16 v[192:195], v[208:211], v[216:223], v251\n\t" \
  "ds_read_b128 v[216:219], v250 offset:0\n\t" \
  "v_smfmac_f32_16x16x64_bf16 v[196:199], v[208:211], v[224:231], v251\n\t" \
  "s_waitcnt lgkmcnt(4)\n\t" \
  "ds_read_b128 v[220:223], v250 offset:1024\n\t" \
  "v_smfmac_f32_16x16x64_bf16 v[192:195], v[212:215], v[0:7], v251\n\t" \
  "ds_read_b128 v[224:227], v250 offset:2048\n\t" \
  "v_smfmac_f32_16x16x64_bf16 v[196:199], v[212:215], v[8:15], v251\n\t" \
  "s_waitcnt lgkmcnt(5)\n\t" \
  "ds_read_b128 v[228:231], v250 offset:3072\n\t" \
  "v_smfmac_f32_16x16x64_bf16 v[192:195], v[236:239], v[16:23], v251\n\t" \
  "ds_read_b128 v[200:203], v248 offset:7168\n\t" \
  "v_smfmac_f32_16x16x64_bf16 v[196:199], v[236:239], v[24:31], v251\n\t" \
  "s_waitcnt lgkmcnt(6)\n\t" \
  "ds_read_b128 v[204:207], v248 offset:8192\n\t" \
  "v_smfmac_f32_16x16x64_bf16 v[192:195], v[240:243], v[32:39], v251\n\t" \
  "ds_read_b128 v[208:211], v248 offset:9216\n\t" \
  "v_smfmac_f32_16x16x64_bf16 v[196:199], v[240:243], v[40:47], v251\n\t" \
  "s_waitcnt lgkmcnt(5)\n\t" \
  "ds_read_b128 v[212:215], v248 offset:11264\n\t" \
  "v_smfmac_f32_16x16x64_bf16 v[192:195], v[244:247], v[216:223], v251\n\t" \
  "s_waitcnt lgkmcnt(4)\n\t" \
  "v_smfmac_f32_16x16x64_bf16 v[196:199], v[244:247], v[224:231], v251\n\t" \
  "s_waitcnt lgkmcnt(3)\n\t" \
  "ds_read_b128 v[236:239], v248 offset:12288\n\t" \
  "v_smfmac_f32_16x16x64_bf16 v[192:195], v[200:203], v[48:55], v251\n\t" \
  "ds_read_b128 v[240:243], v248 offset:14336\n\t" \
  "v_smfmac_f32_16x16x64_bf16 v[196:199], v[200:203], v[56:63], v251\n\t" \
  "s_waitcnt lgkmcnt(4)\n\t" \
  "v_smfmac_f32_16x16x64_bf16 v[192:195], v[204:207], v[64:71], v251\n\t" \
  "ds_read_b128 v[244:247], v248 offset:15360\n\t" \
  "v_smfmac_f32_16x16x64_bf16 v[196:199], v[204:207], v[72:79], v251\n\t" \
  "s_waitcnt lgkmcnt(4)\n\t" \
  "v_smfmac_f32_16x16x64_bf16 v[192:195], v[208:211], v[80:87], v251\n\t" \
  "ds_read_b128 v[200:203], v248 offset:0\n\t" \
  "v_smfmac_f32_16x16x64_bf16 v[196:199], v[208:211], v[88:95], v251\n\t" \
  "s_waitcnt lgkmcnt(4)\n\t" \
  "v_smfmac_f32_16x16x64_bf16 v[192:195], v[212:215], v[96:103], v251\n\t" \
  "ds_read_b128 v[204:207], v248 offset:1024\n\t" \
  "v_smfmac_f32_16x16x64_bf16 v[196:199], v[212:215], v[104:111], v251\n\t" \
  "s_waitcnt lgkmcnt(3)\n\t" \
  "v_smfmac_f32_16x16x64_bf16 v[192:195], v[236:239], v[112:119], v251\n\t" \
  "v_smfmac_f32_16x16x64_bf16 v[196:199], v[236:239], v[120:127], v251\n\t" \
  "v_smfmac_f32_16x16x64_bf16 v[192:195], v[240:243], v[128:135], v251\n\t" \
  "v_smfmac_f32_16x16x64_bf16 v[196:199], v[240:243], v[136:143], v251\n\t" \
  "s_waitcnt lgkmcnt(1)\n\t" \
  "v_smfmac_f32_16x16x64_bf16 v[192:195], v[244:247], v[144:151], v251\n\t" \
  "v_smfmac_f32_16x16x64_bf16 v[196:199], v[244:247], v[152:159], v251\n\t" \
  "v_smfmac_f32_16x16x64_bf16 v[192:195], v[200:203], v[160:167], v251\n\t" \
  "v_smfmac_f32_16x16x64_bf16 v[196:199], v[200:203], v[168:175], v251\n\t" \
  "s_waitcnt lgkmcnt(0)\n\t" \
  "v_smfmac_f32_16x16x64_bf16 v[192:195], v[204:207], v[176:183], v251\n\t" \
  "v_smfmac_f32_16x16x64_bf16 v[196:199], v[204:207], v[184:191], v251\n\t" \
  "ds_read_b128 v[200:203], v248 offset:18432\n\t" \
  "ds_read_b128 v[204:207], v248 offset:22528\n\t" \
  "v_mov_b32 v250, v232\n\t" \
  "v_mov_b32 v249, v233\n\t" \
  "s_nop 2\n\t" \
  "v_add_f32 v192, v192, v194\n\t" \
  "v_add_f32 v193, v193, v195\n\t" \
  "v_add_f32 v196, v196, v198\n\t" \
  "v_add_f32 v197, v197, v199\n\t" \
  "v_cvt_pk_bf16_f32 v192, v192, v193\n\t" \
  "v_cvt_pk_bf16_f32 v196, v196, v197\n\t" \
  "ds_write2_b32 v252, v192, v196 offset1:64\n\t" \
  "v_mov_b32 v251, v234\n\t" \
  "ds_read_b64_tr_b16 v[240:241], v250 offset:1024\n\t" \
  "ds_read_b64_tr_b16 v[242:243], v250 offset:1088\n\t" \
  "v_mov_b32 v212, %[step]\n\t" \
  "s_mov_b64 exec, 1\n\t" \
  "ds_write_b32 v248, v212 offset:28704\n\t" \
  "s_mov_b64 exec, -1\n\t" \
  "buffer_load_dwordx4 v[192:195], v248, %[rt], %[svt] offen offset:0 sc1\n\t" \
  "buffer_load_dwordx4 v[196:199], v248, %[rt], %[svt] offen offset:1024 sc1\n\t" \
  "s_waitcnt lgkmcnt(1)\n\t" \
  "v_mfma_f32_16x16x32_bf16 v[200:203], v[200:203], v[240:243], 0\n\t" \
  "v_mfma_f32_16x16x32_bf16 v[204:207], v[204:207], v[240:243], 0\n\t" \
  "s_nop 7\n\t" \
  "v_cndmask_b32 v216, v200, v204, %[nh]\n\t" \
  "v_cndmask_b32 v217, v201, v205, %[nh]\n\t" \
  "v_cndmask_b32 v218, v202, v206, %[nh]\n\t" \
  "v_cndmask_b32 v219, v203, v207, %[nh]\n\t" \
  "ds_write_b128 v248, v[216:219] offset:18432\n\t" \
  "buffer_load_dword v252, v249, %[rs], %[sv] offen offset:0 sc1\n\t" \
  "buffer_load_dword v253, v249, %[rs], %[sv] offen offset:256 sc1\n\t" \
  "ds_read_b32 v212, v248 offset:28672\n\t" \
  "ds_read_b64_tr_b16 v[244:245], v250 offset:1536\n\t" \
  "ds_read_b64_tr_b16 v[246:247], v250 offset:1600\n\t" \
  "ds_read_b64_tr_b16 v[232:233], v250 offset:0\n\t" \
  "ds_read_b64_tr_b16 v[234:235], v250 offset:64\n\t" \
  "ds_read_b64_tr_b16 v[236:237], v250 offset:512\n\t" \
  "ds_read_b64_tr_b16 v[238:239], v250 offset:576\n\t" \
  "s_setprio 3\n\t" \
  "s_waitcnt lgkmcnt(0)\n\t" \
  "v_cmp_ne_u32 vcc, %[step], v212\n\t" \
  "s_barrier\n\t" \
  "s_and_b32 s93, vcc_lo, 0xf\n\t" \
  "s_cbranch_scc1 .Lre%=\n\t" \
  ".Lrb%=:\n\t" \
  "ds_read_b128 v[220:223], v248 offset:19456\n\t" \
  "ds_read_b128 v[224:227], v248 offset:16384\n\t" \
  "ds_read_b128 v[228:231], v248 offset:17408\n\t" \
  "ds_read_b64_tr_b16 v[208:209], v250 offset:3072\n\t" \
  "ds_read_b64_tr_b16 v[210:211], v250 offset:3136\n\t" \
  "ds_read_b64_tr_b16 v[212:213], v250 offset:3584\n\t" \
  "ds_read_b64_tr_b16 v[214:215], v250 offset:3648\n\t" \
  "ds_read_b64_tr_b16 v[200:201], v250 offset:2048\n\t" \
  "ds_read_b64_tr_b16 v[202:203], v250 offset:2112\n\t" \
  "ds_read_b64_tr_b16 v[204:205], v250 offset:2560\n\t" \
  "ds_read_b64_tr_b16 v[206:207], v250 offset:2624\n\t" \
  "s_waitcnt lgkmcnt(10)\n\t" \
  "v_pk_add_f32 v[216:217], v[216:217], v[220:221]\n\t" \
  "v_pk_add_f32 v[218:219], v[218:219], v[222:223]\n\t" \
  "s_waitcnt lgkmcnt(8)\n\t" \
  "v_pk_add_f32 v[224:225], v[224:225], v[228:229]\n\t" \
  "v_pk_add_f32 v[226:227], v[226:227], v[230:231]\n\t" \
  "v_pk_add_f32 v[216:217], v[216:217], v[224:225]\n\t" \
  "v_pk_add_f32 v[218:219], v[218:219], v[226:227]\n\t" \
  "s_waitcnt lgkmcnt(6)\n\t" \
  "v_mfma_f32_16x16x32_bf16 v[208:211], v[208:211], v[240:243], 0\n\t" \
  "v_max3_f32 v224, v216, v217, v218\n\t" \
  "v_max_f32 v224, v224, v219\n\t" \
  "s_waitcnt lgkmcnt(4)\n\t" \
  "v_mfma_f32_16x16x32_bf16 v[208:211], v[212:215], v[244:247], v[208:211]\n\t" \
  "v_max_f32_dpp v225, v224, v224 row_ror:8 row_mask:0xf bank_mask:0xf\n\t" \
  "v_max_f32_dpp v226, v224, v224 row_ror:8 row_mask:0xf bank_mask:0xf\n\t" \
  "s_waitcnt lgkmcnt(2)\n\t" \
  "v_mfma_f32_16x16x32_bf16 v[208:211], v[200:203], v[232:235], v[208:211]\n\t" \
  "v_permlane32_swap_b32 v225, v226\n\t" \
  "s_waitcnt lgkmcnt(0)\n\t" \
  "v_mfma_f32_16x16x32_bf16 v[208:211], v[204:207], v[236:239], v[208:211]\n\t" \
  "v_max_f32 v231, v225, v226\n\t" \
  "v_mov_b32 v220, 0x3f803f80\n\t" \
  "v_mov_b32 v221, 0x3f803f80\n\t" \
  "v_mov_b64 v[222:223], v[220:221]\n\t" \
  "v_mov_b32 v241, 0\n\t" \
  "v_mov_b32 v242, 0\n\t" \
  "s_nop 1\n\t" \
  "v_max3_f32 v212, v208, v209, v231\n\t" \
  "v_max3_f32 v213, v212, v210, v211\n\t" \
  "v_max3_f32 v214, v212, v210, v211\n\t" \
  "v_cndmask_b32 v208, v208, v210, %[up]\n\t" \
  "v_cndmask_b32 v209, v209, v211, %[up]\n\t" \
  "v_permlane16_swap_b32 v213, v214\n\t" \
  "v_max_f32 v212, v213, v214\n\t" \
  "v_mul_f32 v212, %[negc], v212\n\t" \
  "v_fma_f32 v208, v208, %[c], v212\n\t" \
  "v_fma_f32 v209, v209, %[c], v212\n\t" \
  "v_exp_f32 v208, v208\n\t" \
  "v_fma_f32 v216, v216, %[c], v212\n\t" \
  "v_exp_f32 v209, v209\n\t" \
  "v_fma_f32 v217, v217, %[c], v212\n\t" \
  "v_exp_f32 v216, v216\n\t" \
  "v_fma_f32 v218, v218, %[c], v212\n\t" \
  "v_cvt_pk_bf16_f32 v240, v208, v209\n\t" \
  "v_cvt_pk_bf16_f32 v243, v208, v209\n\t" \
  "v_exp_f32 v217, v217\n\t" \
  "v_fma_f32 v219, v219, %[c], v212\n\t" \
  "v_mfma_f32_16x16x16_bf16 v[204:207], v[220:221], v[240:241], 0\n\t" \
  "v_exp_f32 v218, v218\n\t" \
  "s_waitcnt vmcnt(0)\n\t" \
  "v_mfma_f32_16x16x16_bf16 v[224:227], v[252:253], v[240:241], 0\n\t" \
  "v_cvt_pk_bf16_f32 v202, v216, v217\n\t" \
  "v_cvt_pk_bf16_f32 v200, v216, v217\n\t" \
  "v_exp_f32 v219, v219\n\t" \
  "v_mfma_f32_16x16x16_bf16 v[228:231], v[252:253], v[242:243], 0\n\t" \
  "v_cvt_pk_bf16_f32 v203, v218, v219\n\t" \
  "v_cvt_pk_bf16_f32 v201, v218, v219\n\t" \
  "v_mov_b32_dpp v200, v202 row_ror:8 row_mask:0xf bank_mask:0xc\n\t" \
  "v_mov_b32_dpp v202, v202 row_ror:8 row_mask:0xf bank_mask:0x3\n\t" \
  "v_mov_b32_dpp v201, v203 row_ror:8 row_mask:0xf bank_mask:0xc\n\t" \
  "v_mov_b32_dpp v203, v203 row_ror:8 row_mask:0xf bank_mask:0x3\n\t" \
  "v_max_u32 v212, v252, v253\n\t" \
  "v_cmp_le_u32 vcc, %[sent], v212\n\t" \
  "v_mfma_f32_16x16x32_bf16 v[204:207], v[220:223], v[200:203], v[204:207]\n\t" \
  ".Lpv%=:\n\t" \
  "v_mfma_f32_16x16x32_bf16 v[224:227], v[192:195], v[200:203], v[224:227]\n\t" \
  "v_mfma_f32_16x16x32_bf16 v[228:231], v[196:199], v[200:203], v[228:231]\n\t" \
  "s_cbranch_vccnz .Lvs%=\n\t" \
  ".Lvok%=:\n\t" \
  "s_nop 4\n\t" \
  "v_rcp_f32 v212, v204\n\t" \
  "v_cndmask_b32 v224, v224, v226, %[nh]\n\t" \
  "v_cndmask_b32 v225, v225, v227, %[nh]\n\t" \
  "v_mul_f32 v224, v224, v212\n\t" \
  "v_mul_f32 v225, v225, v212\n\t" \
  "v_cvt_pk_bf16_f32 v224, v224, v225\n\t" \
  "buffer_store_dword v224, v251, %[rs], %[so] offen offset:0 sc1\n\t" \
  "v_cndmask_b32 v228, v228, v230, %[nh]\n\t" \
  "v_cndmask_b32 v229, v229, v231, %[nh]\n\t" \
  "v_mul_f32 v228, v228, v212\n\t" \
  "v_mul_f32 v229, v229, v212\n\t" \
  "v_cvt_pk_bf16_f32 v228, v228, v229\n\t" \
  "buffer_store_dword v228, v251, %[rs], %[so] offen offset:256 sc1\n\t" \
  "s_memtime s[90:91]\n\t" \
  "s_setprio 0\n\t" \
  "s_waitcnt lgkmcnt(0)\n\t" \
  "s_mov_b32 %[tout], s90\n\t" \
  "s_mov_b32 %[spins], s92\n\t" \
  "s_branch .Lend%=\n\t" \
  ".Lre%=:\n\t" \
  "ds_read_b64_tr_b16 v[244:245], v250 offset:1536\n\t" \
  "ds_read_b64_tr_b16 v[246:247], v250 offset:1600\n\t" \
  "ds_read_b64_tr_b16 v[232:233], v250 offset:0\n\t" \
  "ds_read_b64_tr_b16 v[234:235], v250 offset:64\n\t" \
  "ds_read_b64_tr_b16 v[236:237], v250 offset:512\n\t" \
  "ds_read_b64_tr_b16 v[238:239], v250 offset:576\n\t" \
  "s_waitcnt lgkmcnt(0)\n\t" \
  "s_branch .Lrb%=\n\t" \
  ".Lvs%=:\n\t" \
  "v_cmp_eq_u32 vcc, %[sent], v252\n\t" \
  "v_cmp_eq_u32 s[94:95], %[sent], v253\n\t" \
  "s_or_b64 s[94:95], s[94:95], vcc\n\t" \
  "s_cbranch_scc0 .Lvok%=\n\t" \
  "s_add_u32 s92, s92, 1\n\t" \
  "s_sleep 2\n\t" \
  "s_and_b32 s93, s92, 63\n\t" \
  "s_cbranch_scc1 .Lvr%=\n\t" \
  "buffer_load_dword v212, off, %[rs], %[sab] sc1\n\t" \
  "s_waitcnt vmcnt(0)\n\t" \
  "v_readfirstlane_b32 s93, v212\n\t" \
  "s_nop 3\n\t" \
  "s_cmp_eq_u32 s93, 1\n\t" \
  "s_cbranch_scc1 .Lvok%=\n\t" \
  "s_cmp_lt_u32 s92, 0x80000\n\t" \
  "s_cbranch_scc1 .Lvr%=\n\t" \
  "v_mov_b32 v212, 1\n\t" \
  "buffer_store_dword v212, off, %[rs], %[sab] sc1\n\t" \
  "s_branch .Lvok%=\n\t" \
  ".Lvr%=:\n\t" \
  "buffer_load_dword v252, v249, %[rs], %[sv] offen offset:0 sc1\n\t" \
  "buffer_load_dword v253, v249, %[rs], %[sv] offen offset:256 sc1\n\t" \
  "s_waitcnt vmcnt(0)\n\t" \
  "v_mfma_f32_16x16x16_bf16 v[224:227], v[252:253], v[240:241], 0\n\t" \
  "v_mfma_f32_16x16x16_bf16 v[228:231], v[252:253], v[242:243], 0\n\t" \
  "v_max_u32 v212, v252, v253\n\t" \
  "v_cmp_le_u32 vcc, %[sent], v212\n\t" \
  "s_nop 4\n\t" \
  "s_branch .Lpv%=\n\t" \
  "s_branch .Lend%=\n\t" \
  ".Lslow%=:\n\t" \
  "s_mov_b64 s[94:95], 0\n\t" \
  "v_cmp_eq_u32 vcc, %[sent], v200\n\t" \
  "s_or_b64 s[94:95], s[94:95], vcc\n\t" \
  "v_cmp_eq_u32 vcc, %[sent], v201\n\t" \
  "s_or_b64 s[94:95], s[94:95], vcc\n\t" \
  "v_cmp_eq_u32 vcc, %[sent], v202\n\t" \
  "s_or_b64 s[94:95], s[94:95], vcc\n\t" \
  "v_cmp_eq_u32 vcc, %[sent], v203\n\t" \
  "s_or_b64 s[94:95], s[94:95], vcc\n\t" \
  "v_cmp_eq_u32 vcc, %[sent], v204\n\t" \
  "s_or_b64 s[94:95], s[94:95], vcc\n\t" \
  "v_cmp_eq_u32 vcc, %[sent], v205\n\t" \
  "s_or_b64 s[94:95], s[94:95], vcc\n\t" \
  "v_cmp_eq_u32 vcc, %[sent], v206\n\t" \
  "s_or_b64 s[94:95], s[94:95], vcc\n\t" \
  "v_cmp_eq_u32 vcc, %[sent], v207\n\t" \
  "s_or_b64 s[94:95], s[94:95], vcc\n\t" \
  "s_cmp_eq_u64 s[94:95], 0\n\t" \
  "s_cbranch_scc1 .Lok%=\n\t" \
  "v_mov_b64 v[192:193], 0\n\t" \
  "v_mov_b64 v[194:195], 0\n\t" \
  "v_mov_b64 v[196:197], 0\n\t" \
  "v_mov_b64 v[198:199], 0\n\t" \
  "s_add_u32 s92, s92, 1\n\t" \
  "s_sleep 6\n\t" \
  "s_cmp_eq_u32 s92, 1\n\t" \
  "s_cbranch_scc1 .Lpoll%=\n\t" \
  "s_sleep 1\n\t" \
  "s_and_b32 s93, s92, 63\n\t" \
  "s_cmp_lg_u32 s93, 0\n\t" \
  "s_cbranch_scc1 .Lpoll%=\n\t" \
  "v_mov_b32 v245, 0\n\t" \
  "buffer_load_dword v244, v245, %[rs], %[sab] offen sc1\n\t" \
  "s_waitcnt vmcnt(0)\n\t" \
  "v_readfirstlane_b32 s93, v244\n\t" \
  "s_nop 3\n\t" \
  "s_cmp_eq_u32 s93, 1\n\t" \
  "s_cbranch_scc1 .Lok%=\n\t" \
  "s_cmp_lt_u32 s92, 0x80000\n\t" \
  "s_cbranch_scc1 .Lpoll%=\n\t" \
  "v_mov_b32 v244, 1\n\t" \
  "buffer_store_dword v244, v245, %[rs], %[sab] offen sc1\n\t" \
  "s_branch .Lok%=\n\t" \
  ".Lend%=:\n\t"
#define QK_ASM_3 \
  "s_waitcnt lgkmcnt(0)\n\t" \
  "v_subrev_u32 v244, 0x8000, v250\n\t" \
  "ds_read_b128 v[216:219], v248 offset:45056\n\t" \
  "ds_read_b128 v[220:223], v248 offset:46080\n\t" \
  "ds_read_b128 v[224:227], v248 offset:47104\n\t" \
  "ds_read_b128 v[228:231], v248 offset:48128\n\t" \
  "ds_read_b128 v[208:211], v244 offset:0\n\t" \
  "ds_read_b128 v[212:215], v244 offset:1024\n\t" \
  "ds_read_b128 v[236:239], v244 offset:2048\n\t" \
  "ds_read_b128 v[240:243], v244 offset:3072\n\t" \
  "v_mov_b64 v[192:193], 0\n\t" \
  "v_mov_b64 v[194:195], 0\n\t" \
  "v_mov_b64 v[196:197], 0\n\t" \
  "v_mov_b64 v[198:199], 0\n\t" \
  "s_movk_i32 s92, 0x2000\n\t" \
  ".Lwt%=:\n\t" \
  "ds_read_b64 v[200:201], v248 offset:30720\n\t" \
  "s_memrealtime s[90:91]\n\t" \
  "s_sub_u32 s92, s92, 1\n\t" \
  "s_waitcnt lgkmcnt(0)\n\t" \
  "v_readfirstlane_b32 s93, v200\n\t" \
  "v_readfirstlane_b32 %[tgt], v201\n\t" \
  "s_cbranch_scc1 .Lgo%=\n\t" \
  "s_cmp_eq_u32 s93, %[step]\n\t" \
  "s_cbranch_scc1 .Lwx%=\n\t" \
  "s_sleep 1\n\t" \
  "s_branch .Lwt%=\n\t" \
  ".Lwx%=:\n\t" \
  "s_lshl_b32 s93, s90, 2\n\t" \
  "s_sub_i32 s93, %[tgt], s93\n\t" \
  "s_cmp_le_i32 s93, 0\n\t" \
  "s_cbranch_scc1 .Lgo%=\n\t" \
  "s_min_i32 s93, s93, 8000\n\t" \
  "s_add_u32 s93, s93, 3\n\t" \
  "s_lshr_b32 s93, s93, 2\n\t" \
  "s_add_u32 s93, s93, s90\n\t" \
  "s_memrealtime s[90:91]\n\t" \
  ".Lpace%=:\n\t" \
  "s_memrealtime s[94:95]\n\t" \
  "s_waitcnt lgkmcnt(1)\n\t" \
  "s_sub_i32 s91, s90, s93\n\t" \
  "s_cmp_ge_i32 s91, 0\n\t" \
  "s_cbranch_scc1 .Lgo%=\n\t" \
  "s_memrealtime s[90:91]\n\t" \
  "s_waitcnt lgkmcnt(1)\n\t" \
  "s_sub_i32 s95, s94, s93\n\t" \
  "s_cmp_lt_i32 s95, 0\n\t" \
  "s_cbranch_scc1 .Lpace%=\n\t" \
  ".Lgo%=:\n\t" \
  "s_mov_b32 s92, 0\n\t" \
  ".Lpoll%=:\n\t" \
  "buffer_load_dwordx4 v[200:203], v248, %[rs], %[som] offen sc1\n\t" \
  "buffer_load_dwordx4 v[204:207], v248, %[rs], %[som2] offen sc1\n\t" \
  "s_waitcnt vmcnt(1) lgkmcnt(0)\n\t" \
  "ds_write_b128 v249, v[200:203]\n\t" \
  "v_smfmac_f32_16x16x64_bf16 v[192:195], v[200:203], v[208:215], v251\n\t" \
  "v_max3_u32 v244, v200, v201, v202\n\t" \
  "v_smfmac_f32_16x16x64_bf16 v[196:199], v[200:203], v[236:243], v251\n\t" \
  "s_waitcnt vmcnt(0)\n\t" \
  "ds_write_b128 v249, v[204:207] offset:8192\n\t" \
  "v_smfmac_f32_16x16x64_bf16 v[192:195], v[204:207], v[216:223], v251\n\t" \
  "v_max3_u32 v244, v244, v203, v204\n\t" \
  "v_max3_u32 v244, v244, v205, v206\n\t" \
  "v_smfmac_f32_16x16x64_bf16 v[196:199], v[204:207], v[224:231], v251\n\t" \
  "v_max_u32 v244, v244, v207\n\t" \
  "v_cmp_le_u32 vcc, %[sent], v244\n\t" \
  "s_cbranch_vccnz .Lslow%=\n\t" \
  ".Lok%=:\n\t" \
  "ds_read_b128 v[216:219], v250 offset:32768\n\t" \
  "ds_read_b128 v[220:223], v250 offset:33792\n\t" \
  "ds_read_b128 v[224:227], v250 offset:34816\n\t" \
  "ds_read_b128 v[228:231], v250 offset:35840\n\t" \
  "s_waitcnt lgkmcnt(4)\n\t" \
  "s_barrier\n\t" \
  "ds_read_b128 v[208:211], v248 offset:2048\n\t" \
  "ds_read_b128 v[212:215], v248 offset:5120\n\t" \
  "ds_read_b128 v[236:239], v248 offset:6144\n\t" \
  "ds_read_b128 v[240:243], v248 offset:7168\n\t" \
  "s_waitcnt lgkmcnt(3)\n\t" \
  "ds_read_b128 v[244:247], v248 offset:12288\n\t" \
  "v_smfmac_f32_16x16x64_bf16 v[192:195], v[208:211], v[216:223], v251\n\t" \
  "ds_read_b128 v[216:219], v250 offset:0\n\t" \
  "v_smfmac_f32_16x16x64_bf16 v[196:199], v[208:211], v[224:231], v251\n\t" \
  "s_waitcnt lgkmcnt(4)\n\t" \
  "ds_read_b128 v[220:223], v250 offset:1024\n\t" \
  "v_smfmac_f32_16x16x64_bf16 v[192:195], v[212:215], v[0:7], v251\n\t" \
  "ds_read_b128 v[224:227], v250 offset:2048\n\t" \
  "v_smfmac_f32_16x16x64_bf16 v[196:199], v[212:215], v[8:15], v251\n\t" \
  "s_waitcnt lgkmcnt(5)\n\t" \
  "ds_read_b128 v[228:231], v250 offset:3072\n\t" \
  "v_smfmac_f32_16x16x64_bf16 v[192:195], v[236:239], v[16:23], v251\n\t" \
  "ds_read_b128 v[200:203], v248 offset:8192\n\t" \
  "v_smfmac_f32_16x16x64_bf16 v[196:199], v[236:239], v[24:31], v251\n\t" \
  "s_waitcnt lgkmcnt(6)\n\t" \
  "ds_read_b128 v[204:207], v248 offset:9216\n\t" \
  "v_smfmac_f32_16x16x64_bf16 v[192:195], v[240:243], v[32:39], v251\n\t" \
  "ds_read_b128 v[208:211], v248 offset:10240\n\t" \
  "v_smfmac_f32_16x16x64_bf16 v[196:199], v[240:243], v[40:47], v251\n\t" \
  "s_waitcnt lgkmcnt(5)\n\t" \
  "ds_read_b128 v[212:215], v248 offset:13312\n\t" \
  "v_smfmac_f32_16x16x64_bf16 v[192:195], v[244:247], v[216:223], v251\n\t" \
  "s_waitcnt lgkmcnt(4)\n\t" \
  "v_smfmac_f32_16x16x64_bf16 v[196:199], v[244:247], v[224:231], v251\n\t" \
  "s_waitcnt lgkmcnt(3)\n\t" \
  "ds_read_b128 v[236:239], v248 offset:14336\n\t" \
  "v_smfmac_f32_16x16x64_bf16 v[192:195], v[200:203], v[48:55], v251\n\t" \
  "ds_read_b128 v[240:243], v248 offset:15360\n\t" \
  "v_smfmac_f32_16x16x64_bf16 v[196:199], v[200:203], v[56:63], v251\n\t" \
  "s_waitcnt lgkmcnt(4)\n\t" \
  "v_smfmac_f32_16x16x64_bf16 v[192:195], v[204:207], v[64:71], v251\n\t" \
  "ds_read_b128 v[244:247], v248 offset:0\n\t" \
  "v_smfmac_f32_16x16x64_bf16 v[196:199], v[204:207], v[72:79], v251\n\t" \
  "s_waitcnt lgkmcnt(4)\n\t" \
  "v_smfmac_f32_16x16x64_bf16 v[192:195], v[208:211], v[80:87], v251\n\t" \
  "ds_read_b128 v[200:203], v248 offset:1024\n\t" \
  "v_smfmac_f32_16x16x64_bf16 v[196:199], v[208:211], v[88:95], v251\n\t" \
  "s_waitcnt lgkmcnt(4)\n\t" \
  "v_smfmac_f32_16x16x64_bf16 v[192:195], v[212:215], v[96:103], v251\n\t" \
  "ds_read_b128 v[204:207], v248 offset:4096\n\t" \
  "v_smfmac_f32_16x16x64_bf16 v[196:199], v[212:215], v[104:111], v251\n\t" \
  "s_waitcnt lgkmcnt(3)\n\t" \
  "v_smfmac_f32_16x16x64_bf16 v[192:195], v[236:239], v[112:119], v251\n\t" \
  "v_smfmac_f32_16x16x64_bf16 v[196:199], v[236:239], v[120:127], v251\n\t" \
  "v_smfmac_f32_16x16x64_bf16 v[192:195], v[240:243], v[128:135], v251\n\t" \
  "v_smfmac_f32_16x16x64_bf16 v[196:199], v[240:243], v[136:143], v251\n\t" \
  "s_waitcnt lgkmcnt(1)\n\t" \
  "v_smfmac_f32_16x16x64_bf16 v[192:195], v[244:247], v[144:151], v251\n\t" \
  "v_smfmac_f32_16x16x64_bf16 v[196:199], v[244:247], v[152:159], v251\n\t" \
  "v_smfmac_f32_16x16x64_bf16 v[192:195], v[200:203], v[160:167], v251\n\t" \
  "v_smfmac_f32_16x16x64_bf16 v[196:199], v[200:203], v[168:175], v251\n\t" \
  "s_waitcnt lgkmcnt(0)\n\t" \
  "v_smfmac_f32_16x16x64_bf16 v[192:195], v[204:207], v[176:183], v251\n\t" \
  "v_smfmac_f32_16x16x64_bf16 v[196:199], v[204:207], v[184:191], v251\n\t" \
  "ds_read_b128 v[200:203], v248 offset:19456\n\t" \
  "ds_read_b128 v[204:207], v248 offset:23552\n\t" \
  "v_mov_b32 v250, v232\n\t" \
  "v_mov_b32 v249, v233\n\t" \
  "s_nop 2\n\t" \
  "v_add_f32 v192, v192, v194\n\t" \
  "v_add_f32 v193, v193, v195\n\t" \
  "v_add_f32 v196, v196, v198\n\t" \
  "v_add_f32 v197, v197, v199\n\t" \
  "v_cvt_pk_bf16_f32 v192, v192, v193\n\t" \
  "v_cvt_pk_bf16_f32 v196, v196, v197\n\t" \
  "ds_write2_b32 v252, v192, v196 offset1:64\n\t" \
  "v_mov_b32 v251, v234\n\t" \
  "ds_read_b64_tr_b16 v[244:245], v250 offset:1536\n\t" \
  "ds_read_b64_tr_b16 v[246:247], v250 offset:1600\n\t" \
  "v_mov_b32 v212, %[step]\n\t" \
  "s_mov_b64 exec, 1\n\t" \
  "ds_write_b32 v248, v212 offset:28720\n\t" \
  "s_mov_b64 exec, -1\n\t" \
  "buffer_load_dwordx4 v[192:195], v248, %[rt], %[svt] offen offset:0 sc1\n\t" \
  "buffer_load_dwordx4 v[196:199], v248, %[rt], %[svt] offen offset:1024 sc1\n\t" \
  "s_waitcnt lgkmcnt(1)\n\t" \
  "v_mfma_f32_16x16x32_bf16 v[200:203], v[200:203], v[244:247], 0\n\t" \
  "v_mfma_f32_16x16x32_bf16 v[204:207], v[204:207], v[244:247], 0\n\t" \
  "s_nop 7\n\t" \
  "v_cndmask_b32 v216, v200, v204, %[nh]\n\t" \
  "v_cndmask_b32 v217, v201, v205, %[nh]\n\t" \
  "v_cndmask_b32 v218, v202, v206, %[nh]\n\t" \
  "v_cndmask_b32 v219, v203, v207, %[nh]\n\t" \
  "ds_write_b128 v248, v[216:219] offset:19456\n\t" \
  "buffer_load_dword v252, v249, %[rs], %[sv] offen offset:0 sc1\n\t" \
  "buffer_load_dword v253, v249, %[rs], %[sv] offen offset:256 sc1\n\t" \
  "ds_read_b32 v212, v248 offset:28672\n\t" \
  "ds_read_b64_tr_b16 v[232:233], v250 offset:0\n\t" \
  "ds_read_b64_tr_b16 v[234:235], v250 offset:64\n\t" \
  "ds_read_b64_tr_b16 v[236:237], v250 offset:512\n\t" \
  "ds_read_b64_tr_b16 v[238:239], v250 offset:576\n\t" \
  "ds_read_b64_tr_b16 v[240:241], v250 offset:1024\n\t" \
  "ds_read_b64_tr_b16 v[242:243], v250 offset:1088\n\t" \
  "s_setprio 3\n\t" \
  "s_waitcnt lgkmcnt(0)\n\t" \
  "v_cmp_ne_u32 vcc, %[step], v212\n\t" \
  "s_barrier\n\t" \
  "s_and_b32 s93, vcc_lo, 0xf\n\t" \
  "s_cbranch_scc1 .Lre%=\n\t" \
  ".Lrb%=:\n\t" \
  "ds_read_b128 v[220:223], v248 offset:16384\n\t" \
  "ds_read_b128 v[224:227], v248 offset:17408\n\t" \
  "ds_read_b128 v[228:231], v248 offset:18432\n\t" \
  "ds_read_b64_tr_b16 v[212:213], v250 offset:3584\n\t" \
  "ds_read_b64_tr_b16 v[214:215], v250 offset:3648\n\t" \
  "ds_read_b64_tr_b16 v[200:201], v250 offset:2048\n\t" \
  "ds_read_b64_tr_b16 v[202:203], v250 offset:2112\n\t" \
  "ds_read_b64_tr_b16 v[204:205], v250 offset:2560\n\t" \
  "ds_read_b64_tr_b16 v[206:207], v250 offset:2624\n\t" \
  "ds_read_b64_tr_b16 v[208:209], v250 offset:3072\n\t" \
  "ds_read_b64_tr_b16 v[210:211], v250 offset:3136\n\t" \
  "s_waitcnt lgkmcnt(10)\n\t" \
  "v_pk_add_f32 v[216:217], v[216:217], v[220:221]\n\t" \
  "v_pk_add_f32 v[218:219], v[218:219], v[222:223]\n\t" \
  "s_waitcnt lgkmcnt(8)\n\t" \
  "v_pk_add_f32 v[224:225], v[224:225], v[228:229]\n\t" \
  "v_pk_add_f32 v[226:227], v[226:227], v[230:231]\n\t" \
  "v_pk_add_f32 v[216:217], v[216:217], v[224:225]\n\t" \
  "v_pk_add_f32 v[218:219], v[218:219], v[226:227]\n\t" \
  "s_waitcnt lgkmcnt(6)\n\t" \
  "v_mfma_f32_16x16x32_bf16 v[212:215], v[212:215], v[244:247], 0\n\t" \
  "v_max3_f32 v224, v216, v217, v218\n\t" \
  "v_max_f32 v224, v224, v219\n\t" \
  "s_waitcnt lgkmcnt(4)\n\t" \
  "v_mfma_f32_16x16x32_bf16 v[212:215], v[200:203], v[232:235], v[212:215]\n\t" \
  "v_max_f32_dpp v225, v224, v224 row_ror:8 row_mask:0xf bank_mask:0xf\n\t" \
  "v_max_f32_dpp v226, v224, v224 row_ror:8 row_mask:0xf bank_mask:0xf\n\t" \
  "s_waitcnt lgkmcnt(2)\n\t" \
  "v_mfma_f32_16x16x32_bf16 v[212:215], v[204:207], v[236:239], v[212:215]\n\t" \
  "v_permlane32_swap_b32 v225, v226\n\t" \
  "s_waitcnt lgkmcnt(0)\n\t" \
  "v_mfma_f32_16x16x32_bf16 v[212:215], v[208:211], v[240:243], v[212:215]\n\t" \
  "v_max_f32 v231, v225, v226\n\t" \
  "v_mov_b32 v220, 0x3f803f80\n\t" \
  "v_mov_b32 v221, 0x3f803f80\n\t" \
  "v_mov_b64 v[222:223], v[220:221]\n\t" \
  "v_mov_b32 v245, 0\n\t" \
  "v_mov_b32 v246, 0\n\t" \
  "s_nop 1\n\t" \
  "v_max3_f32 v200, v212, v213, v231\n\t" \
  "v_max3_f32 v201, v200, v214, v215\n\t" \
  "v_max3_f32 v202, v200, v214, v215\n\t" \
  "v_cndmask_b32 v212, v212, v214, %[up]\n\t" \
  "v_cndmask_b32 v213, v213, v215, %[up]\n\t" \
  "v_permlane16_swap_b32 v201, v202\n\t" \
  "v_max_f32 v200, v201, v202\n\t" \
  "v_mul_f32 v200, %[negc], v200\n\t" \
  "v_fma_f32 v212, v212, %[c], v200\n\t" \
  "v_fma_f32 v213, v213, %[c], v200\n\t" \
  "v_exp_f32 v212, v212\n\t" \
  "v_fma_f32 v216, v216, %[c], v200\n\t" \
  "v_exp_f32 v213, v213\n\t" \
  "v_fma_f32 v217, v217, %[c], v200\n\t" \
  "v_exp_f32 v216, v216\n\t" \
  "v_fma_f32 v218, v218, %[c], v200\n\t" \
  "v_cvt_pk_bf16_f32 v244, v212, v213\n\t" \
  "v_cvt_pk_bf16_f32 v247, v212, v213\n\t" \
  "v_exp_f32 v217, v217\n\t" \
  "v_fma_f32 v219, v219, %[c], v200\n\t" \
  "v_mfma_f32_16x16x16_bf16 v[208:211], v[220:221], v[244:245], 0\n\t" \
  "v_exp_f32 v218, v218\n\t" \
  "s_waitcnt vmcnt(0)\n\t" \
  "v_mfma_f32_16x16x16_bf16 v[224:227], v[252:253], v[244:245], 0\n\t" \
  "v_cvt_pk_bf16_f32 v206, v216, v217\n\t" \
  "v_cvt_pk_bf16_f32 v204, v216, v217\n\t" \
  "v_exp_f32 v219, v219\n\t" \
  "v_mfma_f32_16x16x16_bf16 v[228:231], v[252:253], v[246:247], 0\n\t" \
  "v_cvt_pk_bf16_f32 v207, v218, v219\n\t" \
  "v_cvt_pk_bf16_f32 v205, v218, v219\n\t" \
  "v_mov_b32_dpp v204, v206 row_ror:8 row_mask:0xf bank_mask:0xc\n\t" \
  "v_mov_b32_dpp v206, v206 row_ror:8 row_mask:0xf bank_mask:0x3\n\t" \
  "v_mov_b32_dpp v205, v207 row_ror:8 row_mask:0xf bank_mask:0xc\n\t" \
  "v_mov_b32_dpp v207, v207 row_ror:8 row_mask:0xf bank_mask:0x3\n\t" \
  "v_max_u32 v200, v252, v253\n\t" \
  "v_cmp_le_u32 vcc, %[sent], v200\n\t" \
  "v_mfma_f32_16x16x32_bf16 v[208:211], v[220:223], v[204:207], v[208:211]\n\t" \
  ".Lpv%=:\n\t" \
  "v_mfma_f32_16x16x32_bf16 v[224:227], v[192:195], v[204:207], v[224:227]\n\t" \
  "v_mfma_f32_16x16x32_bf16 v[228:231], v[196:199], v[204:207], v[228:231]\n\t" \
  "s_cbranch_vccnz .Lvs%=\n\t" \
  ".Lvok%=:\n\t" \
  "s_nop 4\n\t" \
  "v_rcp_f32 v200, v208\n\t" \
  "v_cndmask_b32 v224, v224, v226, %[nh]\n\t" \
  "v_cndmask_b32 v225, v225, v227, %[nh]\n\t" \
  "v_mul_f32 v224, v224, v200\n\t" \
  "v_mul_f32 v225, v225, v200\n\t" \
  "v_cvt_pk_bf16_f32 v224, v224, v225\n\t" \
  "buffer_store_dword v224, v251, %[rs], %[so] offen offset:0 sc1\n\t" \
  "v_cndmask_b32 v228, v228, v230, %[nh]\n\t" \
  "v_cndmask_b32 v229, v229, v231, %[nh]\n\t" \
  "v_mul_f32 v228, v228, v200\n\t" \
  "v_mul_f32 v229, v229, v200\n\t" \
  "v_cvt_pk_bf16_f32 v228, v228, v229\n\t" \
  "buffer_store_dword v228, v251, %[rs], %[so] offen offset:256 sc1\n\t" \
  "s_memtime s[90:91]\n\t" \
  "s_setprio 0\n\t" \
  "s_waitcnt lgkmcnt(0)\n\t" \
  "s_mov_b32 %[tout], s90\n\t" \
  "s_mov_b32 %[spins], s92\n\t" \
  "s_branch .Lend%=\n\t" \
  ".Lre%=:\n\t" \
  "ds_read_b64_tr_b16 v[232:233], v250 offset:0\n\t" \
  "ds_read_b64_tr_b16 v[234:235], v250 offset:64\n\t" \
  "ds_read_b64_tr_b16 v[236:237], v250 offset:512\n\t" \
  "ds_read_b64_tr_b16 v[238:239], v250 offset:576\n\t" \
  "ds_read_b64_tr_b16 v[240:241], v250 offset:1024\n\t" \
  "ds_read_b64_tr_b16 v[242:243], v250 offset:1088\n\t" \
  "s_waitcnt lgkmcnt(0)\n\t" \
  "s_branch .Lrb%=\n\t" \
  ".Lvs%=:\n\t" \
  "v_cmp_eq_u32 vcc, %[sent], v252\n\t" \
  "v_cmp_eq_u32 s[94:95], %[sent], v253\n\t" \
  "s_or_b64 s[94:95], s[94:95], vcc\n\t" \
  "s_cbranch_scc0 .Lvok%=\n\t" \
  "s_add_u32 s92, s92, 1\n\t" \
  "s_sleep 2\n\t" \
  "s_and_b32 s93, s92, 63\n\t" \
  "s_cbranch_scc1 .Lvr%=\n\t" \
  "buffer_load_dword v200, off, %[rs], %[sab] sc1\n\t" \
  "s_waitcnt vmcnt(0)\n\t" \
  "v_readfirstlane_b32 s93, v200\n\t" \
  "s_nop 3\n\t" \
  "s_cmp_eq_u32 s93, 1\n\t" \
  "s_cbranch_scc1 .Lvok%=\n\t" \
  "s_cmp_lt_u32 s92, 0x80000\n\t" \
  "s_cbranch_scc1 .Lvr%=\n\t" \
  "v_mov_b32 v200, 1\n\t" \
  "buffer_store_dword v200, off, %[rs], %[sab] sc1\n\t" \
  "s_branch .Lvok%=\n\t" \
  ".Lvr%=:\n\t" \
  "buffer_load_dword v252, v249, %[rs], %[sv] offen offset:0 sc1\n\t" \
  "buffer_load_dword v253, v249, %[rs], %[sv] offen offset:256 sc1\n\t" \
  "s_waitcnt vmcnt(0)\n\t" \
  "v_mfma_f32_16x16x16_bf16 v[224:227], v[252:253], v[244:245], 0\n\t" \
  "v_mfma_f32_16x16x16_bf16 v[228:231], v[252:253], v[246:247], 0\n\t" \
  "v_max_u32 v200, v252, v253\n\t" \
  "v_cmp_le_u32 vcc, %[sent], v200\n\t" \
  "s_nop 4\n\t" \
  "s_branch .Lpv%=\n\t" \
  "s_branch .Lend%=\n\t" \
  ".Lslow%=:\n\t" \
  "s_mov_b64 s[94:95], 0\n\t" \
  "v_cmp_eq_u32 vcc, %[sent], v200\n\t" \
  "s_or_b64 s[94:95], s[94:95], vcc\n\t" \
  "v_cmp_eq_u32 vcc, %[sent], v201\n\t" \
  "s_or_b64 s[94:95], s[94:95], vcc\n\t" \
  "v_cmp_eq_u32 vcc, %[sent], v202\n\t" \
  "s_or_b64 s[94:95], s[94:95], vcc\n\t" \
  "v_cmp_eq_u32 vcc, %[sent], v203\n\t" \
  "s_or_b64 s[94:95], s[94:95], vcc\n\t" \
  "v_cmp_eq_u32 vcc, %[sent], v204\n\t" \
  "s_or_b64 s[94:95], s[94:95], vcc\n\t" \
  "v_cmp_eq_u32 vcc, %[sent], v205\n\t" \
  "s_or_b64 s[94:95], s[94:95], vcc\n\t" \
  "v_cmp_eq_u32 vcc, %[sent], v206\n\t" \
  "s_or_b64 s[94:95], s[94:95], vcc\n\t" \
  "v_cmp_eq_u32 vcc, %[sent], v207\n\t" \
  "s_or_b64 s[94:95], s[94:95], vcc\n\t" \
  "s_cmp_eq_u64 s[94:95], 0\n\t" \
  "s_cbranch_scc1 .Lok%=\n\t" \
  "v_mov_b64 v[192:193], 0\n\t" \
  "v_mov_b64 v[194:195], 0\n\t" \
  "v_mov_b64 v[196:197], 0\n\t" \
  "v_mov_b64 v[198:199], 0\n\t" \
  "s_add_u32 s92, s92, 1\n\t" \
  "s_sleep 6\n\t" \
  "s_cmp_eq_u32 s92, 1\n\t" \
  "s_cbranch_scc1 .Lpoll%=\n\t" \
  "s_sleep 1\n\t" \
  "s_and_b32 s93, s92, 63\n\t" \
  "s_cmp_lg_u32 s93, 0\n\t" \
  "s_cbranch_scc1 .Lpoll%=\n\t" \
  "v_mov_b32 v245, 0\n\t" \
  "buffer_load_dword v244, v245, %[rs], %[sab] offen sc1\n\t" \
  "s_waitcnt vmcnt(0)\n\t" \
  "v_readfirstlane_b32 s93, v244\n\t" \
  "s_nop 3\n\t" \
  "s_cmp_eq_u32 s93, 1\n\t" \
  "s_cbranch_scc1 .Lok%=\n\t" \
  "s_cmp_lt_u32 s92, 0x80000\n\t" \
  "s_cbranch_scc1 .Lpoll%=\n\t" \
  "v_mov_b32 v244, 1\n\t" \
  "buffer_store_dword v244, v245, %[rs], %[sab] offen sc1\n\t" \
  "s_branch .Lok%=\n\t" \
  ".Lend%=:\n\t"
#define QK_ASM_4 \
  "s_waitcnt lgkmcnt(0)\n\t" \
  "v_subrev_u32 v244, 0x8000, v250\n\t" \
  "ds_read_b128 v[216:219], v248 offset:49152\n\t" \
  "ds_read_b128 v[220:223], v248 offset:50176\n\t" \
  "ds_read_b128 v[224:227], v248 offset:51200\n\t" \
  "ds_read_b128 v[228:231], v248 offset:52224\n\t" \
  "ds_read_b128 v[208:211], v244 offset:0\n\t" \
  "ds_read_b128 v[212:215], v244 offset:1024\n\t" \
  "ds_read_b128 v[236:239], v244 offset:2048\n\t" \
  "ds_read_b128 v[240:243], v244 offset:3072\n\t" \
  "v_mov_b64 v[192:193], 0\n\t" \
  "v_mov_b64 v[194:195], 0\n\t" \
  "v_mov_b64 v[196:197], 0\n\t" \
  "v_mov_b64 v[198:199], 0\n\t" \
  "s_movk_i32 s92, 0x2000\n\t" \
  ".Lwt%=:\n\t" \
  "ds_read_b64 v[200:201], v248 offset:30720\n\t" \
  "s_memrealtime s[90:91]\n\t" \
  "s_sub_u32 s92, s92, 1\n\t" \
  "s_waitcnt lgkmcnt(0)\n\t" \
  "v_readfirstlane_b32 s93, v200\n\t" \
  "v_readfirstlane_b32 %[tgt], v201\n\t" \
  "s_cbranch_scc1 .Lgo%=\n\t" \
  "s_cmp_eq_u32 s93, %[step]\n\t" \
  "s_cbranch_scc1 .Lwx%=\n\t" \
  "s_sleep 1\n\t" \
  "s_branch .Lwt%=\n\t" \
  ".Lwx%=:\n\t" \
  "s_lshl_b32 s93, s90, 2\n\t" \
  "s_sub_i32 s93, %[tgt], s93\n\t" \
  "s_cmp_le_i32 s93, 0\n\t" \
  "s_cbranch_scc1 .Lgo%=\n\t" \
  "s_min_i32 s93, s93, 8000\n\t" \
  "s_add_u32 s93, s93, 3\n\t" \
  "s_lshr_b32 s93, s93, 2\n\t" \
  "s_add_u32 s93, s93, s90\n\t" \
  "s_memrealtime s[90:91]\n\t" \
  ".Lpace%=:\n\t" \
  "s_memrealtime s[94:95]\n\t" \
  "s_waitcnt lgkmcnt(1)\n\t" \
  "s_sub_i32 s91, s90, s93\n\t" \
  "s_cmp_ge_i32 s91, 0\n\t" \
  "s_cbranch_scc1 .Lgo%=\n\t" \
  "s_memrealtime s[90:91]\n\t" \
  "s_waitcnt lgkmcnt(1)\n\t" \
  "s_sub_i32 s95, s94, s93\n\t" \
  "s_cmp_lt_i32 s95, 0\n\t" \
  "s_cbranch_scc1 .Lpace%=\n\t" \
  ".Lgo%=:\n\t" \
  "s_mov_b32 s92, 0\n\t" \
  ".Lpoll%=:\n\t" \
  "buffer_load_dwordx4 v[200:203], v248, %[rs], %[som] offen sc1\n\t" \
  "buffer_load_dwordx4 v[204:207], v248, %[rs], %[som2] offen sc1\n\t" \
  "s_waitcnt vmcnt(1) lgkmcnt(0)\n\t" \
  "ds_write_b128 v249, v[200:203]\n\t" \
  "v_smfmac_f32_16x16x64_bf16 v[192:195], v[200:203], v[208:215], v251\n\t" \
  "v_max3_u32 v244, v200, v201, v202\n\t" \
  "v_smfmac_f32_16x16x64_bf16 v[196:199], v[200:203], v[236:243], v251\n\t" \
  "s_waitcnt vmcnt(0)\n\t" \
  "ds_write_b128 v249, v[204:207] offset:8192\n\t" \
  "v_smfmac_f32_16x16x64_bf16 v[192:195], v[204:207], v[216:223], v251\n\t" \
  "v_max3_u32 v244, v244, v203, v204\n\t" \
  "v_max3_u32 v244, v244, v205, v206\n\t" \
  "v_smfmac_f32_16x16x64_bf16 v[196:199], v[204:207], v[224:231], v251\n\t" \
  "v_max_u32 v244, v244, v207\n\t" \
  "v_cmp_le_u32 vcc, %[sent], v244\n\t" \
  "s_cbranch_vccnz .Lslow%=\n\t" \
  ".Lok%=:\n\t" \
  "ds_read_b128 v[216:219], v250 offset:32768\n\t" \
  "ds_read_b128 v[220:223], v250 offset:33792\n\t" \
  "ds_read_b128 v[224:227], v250 offset:34816\n\t" \
  "ds_read_b128 v[228:231], v250 offset:35840\n\t" \
  "s_waitcnt lgkmcnt(4)\n\t" \
  "s_barrier\n\t" \
  "ds_read_b128 v[208:211], v248 offset:5120\n\t" \
  "ds_read_b128 v[212:215], v248 offset:6144\n\t" \
  "ds_read_b128 v[236:239], v248 offset:7168\n\t" \
  "ds_read_b128 v[240:243], v248 offset:8192\n\t" \
  "s_waitcnt lgkmcnt(3)\n\t" \
  "ds_read_b128 v[244:247], v248 offset:13312\n\t" \
  "v_smfmac_f32_16x16x64_bf16 v[192:195], v[208:211], v[216:223], v251\n\t" \
  "ds_read_b128 v[216:219], v250 offset:0\n\t" \
  "v_smfmac_f32_16x16x64_bf16 v[196:199], v[208:211], v[224:231], v251\n\t" \
  "s_waitcnt lgkmcnt(4)\n\t" \
  "ds_read_b128 v[220:223], v250 offset:1024\n\t" \
  "v_smfmac_f32_16x16x64_bf16 v[192:195], v[212:215], v[0:7], v251\n\t" \
  "ds_read_b128 v[224:227], v250 offset:2048\n\t" \
  "v_smfmac_f32_16x16x64_bf16 v[196:199], v[212:215], v[8:15], v251\n\t" \
  "s_waitcnt lgkmcnt(5)\n\t" \
  "ds_read_b128 v[228:231], v250 offset:3072\n\t" \
  "v_smfmac_f32_16x16x64_bf16 v[192:195], v[236:239], v[16:23], v251\n\t" \
  "ds_read_b128 v[200:203], v248 offset:9216\n\t" \
  "v_smfmac_f32_16x16x64_bf16 v[196:199], v[236:239], v[24:31], v251\n\t" \
  "s_waitcnt lgkmcnt(6)\n\t" \
  "ds_read_b128 v[204:207], v248 offset:10240\n\t" \
  "v_smfmac_f32_16x16x64_bf16 v[192:195], v[240:243], v[32:39], v251\n\t" \
  "ds_read_b128 v[208:211], v248 offset:11264\n\t" \
  "v_smfmac_f32_16x16x64_bf16 v[196:199], v[240:243], v[40:47], v251\n\t" \
  "s_waitcnt lgkmcnt(5)\n\t" \
  "ds_read_b128 v[212:215], v248 offset:14336\n\t" \
  "v_smfmac_f32_16x16x64_bf16 v[192:195], v[244:247], v[216:223], v251\n\t" \
  "s_waitcnt lgkmcnt(4)\n\t" \
  "v_smfmac_f32_16x16x64_bf16 v[196:199], v[244:247], v[224:231], v251\n\t" \
  "s_waitcnt lgkmcnt(3)\n\t" \
  "ds_read_b128 v[236:239], v248 offset:15360\n\t" \
  "v_smfmac_f32_16x16x64_bf16 v[192:195], v[200:203], v[48:55], v251\n\t" \
  "ds_read_b128 v[240:243], v248 offset:0\n\t" \
  "v_smfmac_f32_16x16x64_bf16 v[196:199], v[200:203], v[56:63], v251\n\t" \
  "s_waitcnt lgkmcnt(4)\n\t" \
  "v_smfmac_f32_16x16x64_bf16 v[192:195], v[204:207], v[64:71], v251\n\t" \
  "ds_read_b128 v[244:247], v248 offset:1024\n\t" \
  "v_smfmac_f32_16x16x64_bf16 v[196:199], v[204:207], v[72:79], v251\n\t" \
  "s_waitcnt lgkmcnt(4)\n\t" \
  "v_smfmac_f32_16x16x64_bf16 v[192:195], v[208:211], v[80:87], v251\n\t" \
  "ds_read_b128 v[200:203], v248 offset:2048\n\t" \
  "v_smfmac_f32_16x16x64_bf16 v[196:199], v[208:211], v[88:95], v251\n\t" \
  "s_waitcnt lgkmcnt(4)\n\t" \
  "v_smfmac_f32_16x16x64_bf16 v[192:195], v[212:215], v[96:103], v251\n\t" \
  "ds_read_b128 v[204:207], v248 offset:3072\n\t" \
  "v_smfmac_f32_16x16x64_bf16 v[196:199], v[212:215], v[104:111], v251\n\t" \
  "s_waitcnt lgkmcnt(3)\n\t" \
  "v_smfmac_f32_16x16x64_bf16 v[192:195], v[236:239], v[112:119], v251\n\t" \
  "v_smfmac_f32_16x16x64_bf16 v[196:199], v[236:239], v[120:127], v251\n\t" \
  "v_smfmac_f32_16x16x64_bf16 v[192:195], v[240:243], v[128:135], v251\n\t" \
  "v_smfmac_f32_16x16x64_bf16 v[196:199], v[240:243], v[136:143], v251\n\t" \
  "s_waitcnt lgkmcnt(1)\n\t" \
  "v_smfmac_f32_16x16x64_bf16 v[192:195], v[244:247], v[144:151], v251\n\t" \
  "v_smfmac_f32_16x16x64_bf16 v[196:199], v[244:247], v[152:159], v251\n\t" \
  "v_smfmac_f32_16x16x64_bf16 v[192:195], v[200:203], v[160:167], v251\n\t" \
  "v_smfmac_f32_16x16x64_bf16 v[196:199], v[200:203], v[168:175], v251\n\t" \
  "s_waitcnt lgkmcnt(0)\n\t" \
  "v_smfmac_f32_16x16x64_bf16 v[192:195], v[204:207], v[176:183], v251\n\t" \
  "v_smfmac_f32_16x16x64_bf16 v[196:199], v[204:207], v[184:191], v251\n\t" \
  "s_nop 6\n\t" \
  "v_add_f32 v192, v192, v194\n\t" \
  "v_add_f32 v193, v193, v195\n\t" \
  "v_add_f32 v196, v196, v198\n\t" \
  "v_add_f32 v197, v197, v199\n\t" \
  "v_cvt_pk_bf16_f32 v192, v192, v193\n\t" \
  "v_cvt_pk_bf16_f32 v196, v196, v197\n\t" \
  "ds_write2_b32 v252, v192, v196 offset1:64\n\t" \
  "s_waitcnt lgkmcnt(0)\n\t" \
  "s_barrier\n\t" \
  "buffer_store_dword v192, v235, %[rs], %[sk] offen sc1\n\t" \
  "buffer_store_dword v196, v235, %[rs], %[sk] offen offset:256 sc1\n\t" \
  "s_mov_b32 %[spins], s92\n\t" \
  "s_branch .Lend%=\n\t" \
  ".Lslow%=:\n\t" \
  "s_mov_b64 s[94:95], 0\n\t" \
  "v_cmp_eq_u32 vcc, %[sent], v200\n\t" \
  "s_or_b64 s[94:95], s[94:95], vcc\n\t" \
  "v_cmp_eq_u32 vcc, %[sent], v201\n\t" \
  "s_or_b64 s[94:95], s[94:95], vcc\n\t" \
  "v_cmp_eq_u32 vcc, %[sent], v202\n\t" \
  "s_or_b64 s[94:95], s[94:95], vcc\n\t" \
  "v_cmp_eq_u32 vcc, %[sent], v203\n\t" \
  "s_or_b64 s[94:95], s[94:95], vcc\n\t" \
  "v_cmp_eq_u32 vcc, %[sent], v204\n\t" \
  "s_or_b64 s[94:95], s[94:95], vcc\n\t" \
  "v_cmp_eq_u32 vcc, %[sent], v205\n\t" \
  "s_or_b64 s[94:95], s[94:95], vcc\n\t" \
  "v_cmp_eq_u32 vcc, %[sent], v206\n\t" \
  "s_or_b64 s[94:95], s[94:95], vcc\n\t" \
  "v_cmp_eq_u32 vcc, %[sent], v207\n\t" \
  "s_or_b64 s[94:95], s[94:95], vcc\n\t" \
  "s_cmp_eq_u64 s[94:95], 0\n\t" \
  "s_cbranch_scc1 .Lok%=\n\t" \
  "v_mov_b64 v[192:193], 0\n\t" \
  "v_mov_b64 v[194:195], 0\n\t" \
  "v_mov_b64 v[196:197], 0\n\t" \
  "v_mov_b64 v[198:199], 0\n\t" \
  "s_add_u32 s92, s92, 1\n\t" \
  "s_sleep 6\n\t" \
  "s_cmp_eq_u32 s92, 1\n\t" \
  "s_cbranch_scc1 .Lpoll%=\n\t" \
  "s_sleep 1\n\t" \
  "s_and_b32 s93, s92, 63\n\t" \
  "s_cmp_lg_u32 s93, 0\n\t" \
  "s_cbranch_scc1 .Lpoll%=\n\t" \
  "v_mov_b32 v245, 0\n\t" \
  "buffer_load_dword v244, v245, %[rs], %[sab] offen sc1\n\t" \
  "s_waitcnt vmcnt(0)\n\t" \
  "v_readfirstlane_b32 s93, v244\n\t" \
  "s_nop 3\n\t" \
  "s_cmp_eq_u32 s93, 1\n\t" \
  "s_cbranch_scc1 .Lok%=\n\t" \
  "s_cmp_lt_u32 s92, 0x80000\n\t" \
  "s_cbranch_scc1 .Lpoll%=\n\t" \
  "v_mov_b32 v244, 1\n\t" \
  "buffer_store_dword v244, v245, %[rs], %[sab] offen sc1\n\t" \
  "s_branch .Lok%=\n\t" \
  ".Lend%=:\n\t"
#define QK_ASM_5 \
  "s_waitcnt lgkmcnt(0)\n\t" \
  "v_subrev_u32 v244, 0x8000, v250\n\t" \
  "ds_read_b128 v[216:219], v248 offset:53248\n\t" \
  "ds_read_b128 v[220:223], v248 offset:54272\n\t" \
  "ds_read_b128 v[224:227], v248 offset:55296\n\t" \
  "ds_read_b128 v[228:231], v248 offset:56320\n\t" \
  "ds_read_b128 v[208:211], v244 offset:0\n\t" \
  "ds_read_b128 v[212:215], v244 offset:1024\n\t" \
  "ds_read_b128 v[236:239], v244 offset:2048\n\t" \
  "ds_read_b128 v[240:243], v244 offset:3072\n\t" \
  "v_mov_b64 v[192:193], 0\n\t" \
  "v_mov_b64 v[194:195], 0\n\t" \
  "v_mov_b64 v[196:197], 0\n\t" \
  "v_mov_b64 v[198:199], 0\n\t" \
  "s_movk_i32 s92, 0x2000\n\t" \
  ".Lwt%=:\n\t" \
  "ds_read_b64 v[200:201], v248 offset:30720\n\t" \
  "s_memrealtime s[90:91]\n\t" \
  "s_sub_u32 s92, s92, 1\n\t" \
  "s_waitcnt lgkmcnt(0)\n\t" \
  "v_readfirstlane_b32 s93, v200\n\t" \
  "v_readfirstlane_b32 %[tgt], v201\n\t" \
  "s_cbranch_scc1 .Lgo%=\n\t" \
  "s_cmp_eq_u32 s93, %[step]\n\t" \
  "s_cbranch_scc1 .Lwx%=\n\t" \
  "s_sleep 1\n\t" \
  "s_branch .Lwt%=\n\t" \
  ".Lwx%=:\n\t" \
  "s_lshl_b32 s93, s90, 2\n\t" \
  "s_sub_i32 s93, %[tgt], s93\n\t" \
  "s_cmp_le_i32 s93, 0\n\t" \
  "s_cbranch_scc1 .Lgo%=\n\t" \
  "s_min_i32 s93, s93, 8000\n\t" \
  "s_add_u32 s93, s93, 3\n\t" \
  "s_lshr_b32 s93, s93, 2\n\t" \
  "s_add_u32 s93, s93, s90\n\t" \
  "s_memrealtime s[90:91]\n\t" \
  ".Lpace%=:\n\t" \
  "s_memrealtime s[94:95]\n\t" \
  "s_waitcnt lgkmcnt(1)\n\t" \
  "s_sub_i32 s91, s90, s93\n\t" \
  "s_cmp_ge_i32 s91, 0\n\t" \
  "s_cbranch_scc1 .Lgo%=\n\t" \
  "s_memrealtime s[90:91]\n\t" \
  "s_waitcnt lgkmcnt(1)\n\t" \
  "s_sub_i32 s95, s94, s93\n\t" \
  "s_cmp_lt_i32 s95, 0\n\t" \
  "s_cbranch_scc1 .Lpace%=\n\t" \
  ".Lgo%=:\n\t" \
  "s_mov_b32 s92, 0\n\t" \
  ".Lpoll%=:\n\t" \
  "buffer_load_dwordx4 v[200:203], v248, %[rs], %[som] offen sc1\n\t" \
  "buffer_load_dwordx4 v[204:207], v248, %[rs], %[som2] offen sc1\n\t" \
  "s_waitcnt vmcnt(1) lgkmcnt(0)\n\t" \
  "ds_write_b128 v249, v[200:203]\n\t" \
  "v_smfmac_f32_16x16x64_bf16 v[192:195], v[200:203], v[208:215], v251\n\t" \
  "v_max3_u32 v244, v200, v201, v202\n\t" \
  "v_smfmac_f32_16x16x64_bf16 v[196:199], v[200:203], v[236:243], v251\n\t" \
  "s_waitcnt vmcnt(0)\n\t" \
  "ds_write_b128 v249, v[204:207] offset:8192\n\t" \
  "v_smfmac_f32_16x16x64_bf16 v[192:195], v[204:207], v[216:223], v251\n\t" \
  "v_max3_u32 v244, v244, v203, v204\n\t" \
  "v_max3_u32 v244, v244, v205, v206\n\t" \
  "v_smfmac_f32_16x16x64_bf16 v[196:199], v[204:207], v[224:231], v251\n\t" \
  "v_max_u32 v244, v244, v207\n\t" \
  "v_cmp_le_u32 vcc, %[sent], v244\n\t" \
  "s_cbranch_vccnz .Lslow%=\n\t" \
  ".Lok%=:\n\t" \
  "ds_read_b128 v[216:219], v250 offset:32768\n\t" \
  "ds_read_b128 v[220:223], v250 offset:33792\n\t" \
  "ds_read_b128 v[224:227], v250 offset:34816\n\t" \
  "ds_read_b128 v[228:231], v250 offset:35840\n\t" \
  "s_waitcnt lgkmcnt(4)\n\t" \
  "s_barrier\n\t" \
  "ds_read_b128 v[208:211], v248 offset:4096\n\t" \
  "ds_read_b128 v[212:215], v248 offset:7168\n\t" \
  "ds_read_b128 v[236:239], v248 offset:8192\n\t" \
  "ds_read_b128 v[240:243], v248 offset:9216\n\t" \
  "s_waitcnt lgkmcnt(3)\n\t" \
  "ds_read_b128 v[244:247], v248 offset:12288\n\t" \
  "v_smfmac_f32_16x16x64_bf16 v[192:195], v[208:211], v[216:223], v251\n\t" \
  "ds_read_b128 v[216:219], v250 offset:0\n\t" \
  "v_smfmac_f32_16x16x64_bf16 v[196:199], v[208:211], v[224:231], v251\n\t" \
  "s_waitcnt lgkmcnt(4)\n\t" \
  "ds_read_b128 v[220:223], v250 offset:1024\n\t" \
  "v_smfmac_f32_16x16x64_bf16 v[192:195], v[212:215], v[0:7], v251\n\t" \
  "ds_read_b128 v[224:227], v250 offset:2048\n\t" \
  "v_smfmac_f32_16x16x64_bf16 v[196:199], v[212:215], v[8:15], v251\n\t" \
  "s_waitcnt lgkmcnt(5)\n\t" \
  "ds_read_b128 v[228:231], v250 offset:3072\n\t" \
  "v_smfmac_f32_16x16x64_bf16 v[192:195], v[236:239], v[16:23], v251\n\t" \
  "ds_read_b128 v[200:203], v248 offset:10240\n\t" \
  "v_smfmac_f32_16x16x64_bf16 v[196:199], v[236:239], v[24:31], v251\n\t" \
  "s_waitcnt lgkmcnt(6)\n\t" \
  "ds_read_b128 v[204:207], v248 offset:11264\n\t" \
  "v_smfmac_f32_16x16x64_bf16 v[192:195], v[240:243], v[32:39], v251\n\t" \
  "ds_read_b128 v[208:211], v248 offset:14336\n\t" \
  "v_smfmac_f32_16x16x64_bf16 v[196:199], v[240:243], v[40:47], v251\n\t" \
  "s_waitcnt lgkmcnt(5)\n\t" \
  "ds_read_b128 v[212:215], v248 offset:15360\n\t" \
  "v_smfmac_f32_16x16x64_bf16 v[192:195], v[244:247], v[216:223], v251\n\t" \
  "s_waitcnt lgkmcnt(4)\n\t" \
  "v_smfmac_f32_16x16x64_bf16 v[196:199], v[244:247], v[224:231], v251\n\t" \
  "s_waitcnt lgkmcnt(3)\n\t" \
  "ds_read_b128 v[236:239], v248 offset:0\n\t" \
  "v_smfmac_f32_16x16x64_bf16 v[192:195], v[200:203], v[48:55], v251\n\t" \
  "ds_read_b128 v[240:243], v248 offset:1024\n\t" \
  "v_smfmac_f32_16x16x64_bf16 v[196:199], v[200:203], v[56:63], v251\n\t" \
  "s_waitcnt lgkmcnt(4)\n\t" \
  "v_smfmac_f32_16x16x64_bf16 v[192:195], v[204:207], v[64:71], v251\n\t" \
  "ds_read_b128 v[244:247], v248 offset:2048\n\t" \
  "v_smfmac_f32_16x16x64_bf16 v[196:199], v[204:207], v[72:79], v251\n\t" \
  "s_waitcnt lgkmcnt(4)\n\t" \
  "v_smfmac_f32_16x16x64_bf16 v[192:195], v[208:211], v[80:87], v251\n\t" \
  "ds_read_b128 v[200:203], v248 offset:3072\n\t" \
  "v_smfmac_f32_16x16x64_bf16 v[196:199], v[208:211], v[88:95], v251\n\t" \
  "s_waitcnt lgkmcnt(4)\n\t" \
  "v_smfmac_f32_16x16x64_bf16 v[192:195], v[212:215], v[96:103], v251\n\t" \
  "ds_read_b128 v[204:207], v248 offset:6144\n\t" \
  "v_smfmac_f32_16x16x64_bf16 v[196:199], v[212:215], v[104:111], v251\n\t" \
  "s_waitcnt lgkmcnt(3)\n\t" \
  "v_smfmac_f32_16x16x64_bf16 v[192:195], v[236:239], v[112:119], v251\n\t" \
  "v_smfmac_f32_16x16x64_bf16 v[196:199], v[236:239], v[120:127], v251\n\t" \
  "v_smfmac_f32_16x16x64_bf16 v[192:195], v[240:243], v[128:135], v251\n\t" \
  "v_smfmac_f32_16x16x64_bf16 v[196:199], v[240:243], v[136:143], v251\n\t" \
  "s_waitcnt lgkmcnt(1)\n\t" \
  "v_smfmac_f32_16x16x64_bf16 v[192:195], v[244:247], v[144:151], v251\n\t" \
  "v_smfmac_f32_16x16x64_bf16 v[196:199], v[244:247], v[152:159], v251\n\t" \
  "v_smfmac_f32_16x16x64_bf16 v[192:195], v[200:203], v[160:167], v251\n\t" \
  "v_smfmac_f32_16x16x64_bf16 v[196:199], v[200:203], v[168:175], v251\n\t" \
  "s_waitcnt lgkmcnt(0)\n\t" \
  "v_smfmac_f32_16x16x64_bf16 v[192:195], v[204:207], v[176:183], v251\n\t" \
  "v_smfmac_f32_16x16x64_bf16 v[196:199], v[204:207], v[184:191], v251\n\t" \
  "s_nop 6\n\t" \
  "v_add_f32 v192, v192, v194\n\t" \
  "v_add_f32 v193, v193, v195\n\t" \
  "v_add_f32 v196, v196, v198\n\t" \
  "v_add_f32 v197, v197, v199\n\t" \
  "v_cvt_pk_bf16_f32 v192, v192, v193\n\t" \
  "v_cvt_pk_bf16_f32 v196, v196, v197\n\t" \
  "ds_write2_b32 v252, v192, v196 offset1:64\n\t" \
  "s_waitcnt lgkmcnt(0)\n\t" \
  "s_barrier\n\t" \
  "buffer_store_dword v192, v235, %[rs], %[sk] offen sc1\n\t" \
  "buffer_store_dword v196, v235, %[rs], %[sk] offen offset:256 sc1\n\t" \
  "s_mov_b32 %[spins], s92\n\t" \
  "s_branch .Lend%=\n\t" \
  ".Lslow%=:\n\t" \
  "s_mov_b64 s[94:95], 0\n\t" \
  "v_cmp_eq_u32 vcc, %[sent], v200\n\t" \
  "s_or_b64 s[94:95], s[94:95], vcc\n\t" \
  "v_cmp_eq_u32 vcc, %[sent], v201\n\t" \
  "s_or_b64 s[94:95], s[94:95], vcc\n\t" \
  "v_cmp_eq_u32 vcc, %[sent], v202\n\t" \
  "s_or_b64 s[94:95], s[94:95], vcc\n\t" \
  "v_cmp_eq_u32 vcc, %[sent], v203\n\t" \
  "s_or_b64 s[94:95], s[94:95], vcc\n\t" \
  "v_cmp_eq_u32 vcc, %[sent], v204\n\t" \
  "s_or_b64 s[94:95], s[94:95], vcc\n\t" \
  "v_cmp_eq_u32 vcc, %[sent], v205\n\t" \
  "s_or_b64 s[94:95], s[94:95], vcc\n\t" \
  "v_cmp_eq_u32 vcc, %[sent], v206\n\t" \
  "s_or_b64 s[94:95], s[94:95], vcc\n\t" \
  "v_cmp_eq_u32 vcc, %[sent], v207\n\t" \
  "s_or_b64 s[94:95], s[94:95], vcc\n\t" \
  "s_cmp_eq_u64 s[94:95], 0\n\t" \
  "s_cbranch_scc1 .Lok%=\n\t" \
  "v_mov_b64 v[192:193], 0\n\t" \
  "v_mov_b64 v[194:195], 0\n\t" \
  "v_mov_b64 v[196:197], 0\n\t" \
  "v_mov_b64 v[198:199], 0\n\t" \
  "s_add_u32 s92, s92, 1\n\t" \
  "s_sleep 6\n\t" \
  "s_cmp_eq_u32 s92, 1\n\t" \
  "s_cbranch_scc1 .Lpoll%=\n\t" \
  "s_sleep 1\n\t" \
  "s_and_b32 s93, s92, 63\n\t" \
  "s_cmp_lg_u32 s93, 0\n\t" \
  "s_cbranch_scc1 .Lpoll%=\n\t" \
  "v_mov_b32 v245, 0\n\t" \
  "buffer_load_dword v244, v245, %[rs], %[sab] offen sc1\n\t" \
  "s_waitcnt vmcnt(0)\n\t" \
  "v_readfirstlane_b32 s93, v244\n\t" \
  "s_nop 3\n\t" \
  "s_cmp_eq_u32 s93, 1\n\t" \
  "s_cbranch_scc1 .Lok%=\n\t" \
  "s_cmp_lt_u32 s92, 0x80000\n\t" \
  "s_cbranch_scc1 .Lpoll%=\n\t" \
  "v_mov_b32 v244, 1\n\t" \
  "buffer_store_dword v244, v245, %[rs], %[sab] offen sc1\n\t" \
  "s_branch .Lok%=\n\t" \
  ".Lend%=:\n\t"
#define QK_ASM_6 \
  "s_waitcnt lgkmcnt(0)\n\t" \
  "v_subrev_u32 v244, 0x8000, v250\n\t" \
  "ds_read_b128 v[216:219], v248 offset:57344\n\t" \
  "ds_read_b128 v[220:223], v248 offset:58368\n\t" \
  "ds_read_b128 v[224:227], v248 offset:59392\n\t" \
  "ds_read_b128 v[228:231], v248 offset:60416\n\t" \
  "ds_read_b128 v[208:211], v244 offset:0\n\t" \
  "ds_read_b128 v[212:215], v244 offset:1024\n\t" \
  "ds_read_b128 v[236:239], v244 offset:2048\n\t" \
  "ds_read_b128 v[240:243], v244 offset:3072\n\t" \
  "v_mov_b64 v[192:193], 0\n\t" \
  "v_mov_b64 v[194:195], 0\n\t" \
  "v_mov_b64 v[196:197], 0\n\t" \
  "v_mov_b64 v[198:199], 0\n\t" \
  "s_movk_i32 s92, 0x2000\n\t" \
  ".Lwt%=:\n\t" \
  "ds_read_b64 v[200:201], v248 offset:30720\n\t" \
  "s_memrealtime s[90:91]\n\t" \
  "s_sub_u32 s92, s92, 1\n\t" \
  "s_waitcnt lgkmcnt(0)\n\t" \
  "v_readfirstlane_b32 s93, v200\n\t" \
  "v_readfirstlane_b32 %[tgt], v201\n\t" \
  "s_cbranch_scc1 .Lgo%=\n\t" \
  "s_cmp_eq_u32 s93, %[step]\n\t" \
  "s_cbranch_scc1 .Lwx%=\n\t" \
  "s_sleep 1\n\t" \
  "s_branch .Lwt%=\n\t" \
  ".Lwx%=:\n\t" \
  "s_lshl_b32 s93, s90, 2\n\t" \
  "s_sub_i32 s93, %[tgt], s93\n\t" \
  "s_cmp_le_i32 s93, 0\n\t" \
  "s_cbranch_scc1 .Lgo%=\n\t" \
  "s_min_i32 s93, s93, 8000\n\t" \
  "s_add_u32 s93, s93, 3\n\t" \
  "s_lshr_b32 s93, s93, 2\n\t" \
  "s_add_u32 s93, s93, s90\n\t" \
  "s_memrealtime s[90:91]\n\t" \
  ".Lpace%=:\n\t" \
  "s_memrealtime s[94:95]\n\t" \
  "s_waitcnt lgkmcnt(1)\n\t" \
  "s_sub_i32 s91, s90, s93\n\t" \
  "s_cmp_ge_i32 s91, 0\n\t" \
  "s_cbranch_scc1 .Lgo%=\n\t" \
  "s_memrealtime s[90:91]\n\t" \
  "s_waitcnt lgkmcnt(1)\n\t" \
  "s_sub_i32 s95, s94, s93\n\t" \
  "s_cmp_lt_i32 s95, 0\n\t" \
  "s_cbranch_scc1 .Lpace%=\n\t" \
  ".Lgo%=:\n\t" \
  "s_mov_b32 s92, 0\n\t" \
  ".Lpoll%=:\n\t" \
  "buffer_load_dwordx4 v[200:203], v248, %[rs], %[som] offen sc1\n\t" \
  "buffer_load_dwordx4 v[204:207], v248, %[rs], %[som2] offen sc1\n\t" \
  "s_waitcnt vmcnt(1) lgkmcnt(0)\n\t" \
  "ds_write_b128 v249, v[200:203]\n\t" \
  "v_smfmac_f32_16x16x64_bf16 v[192:195], v[200:203], v[208:215], v251\n\t" \
  "v_max3_u32 v244, v200, v201, v202\n\t" \
  "v_smfmac_f32_16x16x64_bf16 v[196:199], v[200:203], v[236:243], v251\n\t" \
  "s_waitcnt vmcnt(0)\n\t" \
  "ds_write_b128 v249, v[204:207] offset:8192\n\t" \
  "v_smfmac_f32_16x16x64_bf16 v[192:195], v[204:207], v[216:223], v251\n\t" \
  "v_max3_u32 v244, v244, v203, v204\n\t" \
  "v_max3_u32 v244, v244, v205, v206\n\t" \
  "v_smfmac_f32_16x16x64_bf16 v[196:199], v[204:207], v[224:231], v251\n\t" \
  "v_max_u32 v244, v244, v207\n\t" \
  "v_cmp_le_u32 vcc, %[sent], v244\n\t" \
  "s_cbranch_vccnz .Lslow%=\n\t" \
  ".Lok%=:\n\t" \
  "ds_read_b128 v[216:219], v250 offset:32768\n\t" \
  "ds_read_b128 v[220:223], v250 offset:33792\n\t" \
  "ds_read_b128 v[224:227], v250 offset:34816\n\t" \
  "ds_read_b128 v[228:231], v250 offset:35840\n\t" \
  "s_waitcnt lgkmcnt(4)\n\t" \
  "s_barrier\n\t" \
  "ds_read_b128 v[208:211], v248 offset:7168\n\t" \
  "ds_read_b128 v[212:215], v248 offset:8192\n\t" \
  "ds_read_b128 v[236:239], v248 offset:9216\n\t" \
  "ds_read_b128 v[240:243], v248 offset:10240\n\t" \
  "s_waitcnt lgkmcnt(3)\n\t" \
  "ds_read_b128 v[244:247], v248 offset:13312\n\t" \
  "v_smfmac_f32_16x16x64_bf16 v[192:195], v[208:211], v[216:223], v251\n\t" \
  "ds_read_b128 v[216:219], v250 offset:0\n\t" \
  "v_smfmac_f32_16x16x64_bf16 v[196:199], v[208:211], v[224:231], v251\n\t" \
  "s_waitcnt lgkmcnt(4)\n\t" \
  "ds_read_b128 v[220:223], v250 offset:1024\n\t" \
  "v_smfmac_f32_16x16x64_bf16 v[192:195], v[212:215], v[0:7], v251\n\t" \
  "ds_read_b128 v[224:227], v250 offset:2048\n\t" \
  "v_smfmac_f32_16x16x64_bf16 v[196:199], v[212:215], v[8:15], v251\n\t" \
  "s_waitcnt lgkmcnt(5)\n\t" \
  "ds_read_b128 v[228:231], v250 offset:3072\n\t" \
  "v_smfmac_f32_16x16x64_bf16 v[192:195], v[236:239], v[16:23], v251\n\t" \
  "ds_read_b128 v[200:203], v248 offset:11264\n\t" \
  "v_smfmac_f32_16x16x64_bf16 v[196:199], v[236:239], v[24:31], v251\n\t" \
  "s_waitcnt lgkmcnt(6)\n\t" \
  "ds_read_b128 v[204:207], v248 offset:12288\n\t" \
  "v_smfmac_f32_16x16x64_bf16 v[192:195], v[240:243], v[32:39], v251\n\t" \
  "ds_read_b128 v[208:211], v248 offset:15360\n\t" \
  "v_smfmac_f32_16x16x64_bf16 v[196:199], v[240:243], v[40:47], v251\n\t" \
  "s_waitcnt lgkmcnt(5)\n\t" \
  "ds_read_b128 v[212:215], v248 offset:0\n\t" \
  "v_smfmac_f32_16x16x64_bf16 v[192:195], v[244:247], v[216:223], v251\n\t" \
  "s_waitcnt lgkmcnt(4)\n\t" \
  "v_smfmac_f32_16x16x64_bf16 v[196:199], v[244:247], v[224:231], v251\n\t" \
  "s_waitcnt lgkmcnt(3)\n\t" \
  "ds_read_b128 v[236:239], v248 offset:1024\n\t" \
  "v_smfmac_f32_16x16x64_bf16 v[192:195], v[200:203], v[48:55], v251\n\t" \
  "ds_read_b128 v[240:243], v248 offset:2048\n\t" \
  "v_smfmac_f32_16x16x64_bf16 v[196:199], v[200:203], v[56:63], v251\n\t" \
  "s_waitcnt lgkmcnt(4)\n\t" \
  "v_smfmac_f32_16x16x64_bf16 v[192:195], v[204:207], v[64:71], v251\n\t" \
  "ds_read_b128 v[244:247], v248 offset:3072\n\t" \
  "v_smfmac_f32_16x16x64_bf16 v[196:199], v[204:207], v[72:79], v251\n\t" \
  "s_waitcnt lgkmcnt(4)\n\t" \
  "v_smfmac_f32_16x16x64_bf16 v[192:195], v[208:211], v[80:87], v251\n\t" \
  "ds_read_b128 v[200:203], v248 offset:4096\n\t" \
  "v_smfmac_f32_16x16x64_bf16 v[196:199], v[208:211], v[88:95], v251\n\t" \
  "s_waitcnt lgkmcnt(4)\n\t" \
  "v_smfmac_f32_16x16x64_bf16 v[192:195], v[212:215], v[96:103], v251\n\t" \
  "ds_read_b128 v[204:207], v248 offset:5120\n\t" \
  "v_smfmac_f32_16x16x64_bf16 v[196:199], v[212:215], v[104:111], v251\n\t" \
  "s_waitcnt lgkmcnt(3)\n\t" \
  "v_smfmac_f32_16x16x64_bf16 v[192:195], v[236:239], v[112:119], v251\n\t" \
  "v_smfmac_f32_16x16x64_bf16 v[196:199], v[236:239], v[120:127], v251\n\t" \
  "v_smfmac_f32_16x16x64_bf16 v[192:195], v[240:243], v[128:135], v251\n\t" \
  "v_smfmac_f32_16x16x64_bf16 v[196:199], v[240:243], v[136:143], v251\n\t" \
  "s_waitcnt lgkmcnt(1)\n\t" \
  "v_smfmac_f32_16x16x64_bf16 v[192:195], v[244:247], v[144:151], v251\n\t" \
  "v_smfmac_f32_16x16x64_bf16 v[196:199], v[244:247], v[152:159], v251\n\t" \
  "v_smfmac_f32_16x16x64_bf16 v[192:195], v[200:203], v[160:167], v251\n\t" \
  "v_smfmac_f32_16x16x64_bf16 v[196:199], v[200:203], v[168:175], v251\n\t" \
  "s_waitcnt lgkmcnt(0)\n\t" \
  "v_smfmac_f32_16x16x64_bf16 v[192:195], v[204:207], v[176:183], v251\n\t" \
  "v_smfmac_f32_16x16x64_bf16 v[196:199], v[204:207], v[184:191], v251\n\t" \
  "s_nop 6\n\t" \
  "v_add_f32 v192, v192, v194\n\t" \
  "v_add_f32 v193, v193, v195\n\t" \
  "v_add_f32 v196, v196, v198\n\t" \
  "v_add_f32 v197, v197, v199\n\t" \
  "v_cvt_pk_bf16_f32 v192, v192, v193\n\t" \
  "v_cvt_pk_bf16_f32 v196, v196, v197\n\t" \
  "ds_write2_b32 v252, v192, v196 offset1:64\n\t" \
  "s_waitcnt lgkmcnt(0)\n\t" \
  "s_barrier\n\t" \
  "buffer_store_dword v192, v235, %[rs], %[sk] offen sc1\n\t" \
  "buffer_store_dword v196, v235, %[rs], %[sk] offen offset:256 sc1\n\t" \
  "s_mov_b32 %[spins], s92\n\t" \
  "s_branch .Lend%=\n\t" \
  ".Lslow%=:\n\t" \
  "s_mov_b64 s[94:95], 0\n\t" \
  "v_cmp_eq_u32 vcc, %[sent], v200\n\t" \
  "s_or_b64 s[94:95], s[94:95], vcc\n\t" \
  "v_cmp_eq_u32 vcc, %[sent], v201\n\t" \
  "s_or_b64 s[94:95], s[94:95], vcc\n\t" \
  "v_cmp_eq_u32 vcc, %[sent], v202\n\t" \
  "s_or_b64 s[94:95], s[94:95], vcc\n\t" \
  "v_cmp_eq_u32 vcc, %[sent], v203\n\t" \
  "s_or_b64 s[94:95], s[94:95], vcc\n\t" \
  "v_cmp_eq_u32 vcc, %[sent], v204\n\t" \
  "s_or_b64 s[94:95], s[94:95], vcc\n\t" \
  "v_cmp_eq_u32 vcc, %[sent], v205\n\t" \
  "s_or_b64 s[94:95], s[94:95], vcc\n\t" \
  "v_cmp_eq_u32 vcc, %[sent], v206\n\t" \
  "s_or_b64 s[94:95], s[94:95], vcc\n\t" \
  "v_cmp_eq_u32 vcc, %[sent], v207\n\t" \
  "s_or_b64 s[94:95], s[94:95], vcc\n\t" \
  "s_cmp_eq_u64 s[94:95], 0\n\t" \
  "s_cbranch_scc1 .Lok%=\n\t" \
  "v_mov_b64 v[192:193], 0\n\t" \
  "v_mov_b64 v[194:195], 0\n\t" \
  "v_mov_b64 v[196:197], 0\n\t" \
  "v_mov_b64 v[198:199], 0\n\t" \
  "s_add_u32 s92, s92, 1\n\t" \
  "s_sleep 6\n\t" \
  "s_cmp_eq_u32 s92, 1\n\t" \
  "s_cbranch_scc1 .Lpoll%=\n\t" \
  "s_sleep 1\n\t" \
  "s_and_b32 s93, s92, 63\n\t" \
  "s_cmp_lg_u32 s93, 0\n\t" \
  "s_cbranch_scc1 .Lpoll%=\n\t" \
  "v_mov_b32 v245, 0\n\t" \
  "buffer_load_dword v244, v245, %[rs], %[sab] offen sc1\n\t" \
  "s_waitcnt vmcnt(0)\n\t" \
  "v_readfirstlane_b32 s93, v244\n\t" \
  "s_nop 3\n\t" \
  "s_cmp_eq_u32 s93, 1\n\t" \
  "s_cbranch_scc1 .Lok%=\n\t" \
  "s_cmp_lt_u32 s92, 0x80000\n\t" \
  "s_cbranch_scc1 .Lpoll%=\n\t" \
  "v_mov_b32 v244, 1\n\t" \
  "buffer_store_dword v244, v245, %[rs], %[sab] offen sc1\n\t" \
  "s_branch .Lok%=\n\t" \
  ".Lend%=:\n\t"
#define QK_ASM_7 \
  "s_waitcnt lgkmcnt(0)\n\t" \
  "v_subrev_u32 v244, 0x8000, v250\n\t" \
  "ds_read_b128 v[216:219], v248 offset:61440\n\t" \
  "ds_read_b128 v[220:223], v248 offset:62464\n\t" \
  "ds_read_b128 v[224:227], v248 offset:63488\n\t" \
  "ds_read_b128 v[228:231], v248 offset:64512\n\t" \
  "ds_read_b128 v[208:211], v244 offset:0\n\t" \
  "ds_read_b128 v[212:215], v244 offset:1024\n\t" \
  "ds_read_b128 v[236:239], v244 offset:2048\n\t" \
  "ds_read_b128 v[240:243], v244 offset:3072\n\t" \
  "v_mov_b64 v[192:193], 0\n\t" \
  "v_mov_b64 v[194:195], 0\n\t" \
  "v_mov_b64 v[196:197], 0\n\t" \
  "v_mov_b64 v[198:199], 0\n\t" \
  "s_movk_i32 s92, 0x2000\n\t" \
  ".Lwt%=:\n\t" \
  "ds_read_b64 v[200:201], v248 offset:30720\n\t" \
  "s_memrealtime s[90:91]\n\t" \
  "s_sub_u32 s92, s92, 1\n\t" \
  "s_waitcnt lgkmcnt(0)\n\t" \
  "v_readfirstlane_b32 s93, v200\n\t" \
  "v_readfirstlane_b32 %[tgt], v201\n\t" \
  "s_cbranch_scc1 .Lgo%=\n\t" \
  "s_cmp_eq_u32 s93, %[step]\n\t" \
  "s_cbranch_scc1 .Lwx%=\n\t" \
  "s_sleep 1\n\t" \
  "s_branch .Lwt%=\n\t" \
  ".Lwx%=:\n\t" \
  "s_lshl_b32 s93, s90, 2\n\t" \
  "s_sub_i32 s93, %[tgt], s93\n\t" \
  "s_cmp_le_i32 s93, 0\n\t" \
  "s_cbranch_scc1 .Lgo%=\n\t" \
  "s_min_i32 s93, s93, 8000\n\t" \
  "s_add_u32 s93, s93, 3\n\t" \
  "s_lshr_b32 s93, s93, 2\n\t" \
  "s_add_u32 s93, s93, s90\n\t" \
  "s_memrealtime s[90:91]\n\t" \
  ".Lpace%=:\n\t" \
  "s_memrealtime s[94:95]\n\t" \
  "s_waitcnt lgkmcnt(1)\n\t" \
  "s_sub_i32 s91, s90, s93\n\t" \
  "s_cmp_ge_i32 s91, 0\n\t" \
  "s_cbranch_scc1 .Lgo%=\n\t" \
  "s_memrealtime s[90:91]\n\t" \
  "s_waitcnt lgkmcnt(1)\n\t" \
  "s_sub_i32 s95, s94, s93\n\t" \
  "s_cmp_lt_i32 s95, 0\n\t" \
  "s_cbranch_scc1 .Lpace%=\n\t" \
  ".Lgo%=:\n\t" \
  "s_mov_b32 s92, 0\n\t" \
  ".Lpoll%=:\n\t" \
  "buffer_load_dwordx4 v[200:203], v248, %[rs], %[som] offen sc1\n\t" \
  "buffer_load_dwordx4 v[204:207], v248, %[rs], %[som2] offen sc1\n\t" \
  "s_waitcnt vmcnt(1) lgkmcnt(0)\n\t" \
  "ds_write_b128 v249, v[200:203]\n\t" \
  "v_smfmac_f32_16x16x64_bf16 v[192:195], v[200:203], v[208:215], v251\n\t" \
  "v_max3_u32 v244, v200, v201, v202\n\t" \
  "v_smfmac_f32_16x16x64_bf16 v[196:199], v[200:203], v[236:243], v251\n\t" \
  "s_waitcnt vmcnt(0)\n\t" \
  "ds_write_b128 v249, v[204:207] offset:8192\n\t" \
  "v_smfmac_f32_16x16x64_bf16 v[192:195], v[204:207], v[216:223], v251\n\t" \
  "v_max3_u32 v244, v244, v203, v204\n\t" \
  "v_max3_u32 v244, v244, v205, v206\n\t" \
  "v_smfmac_f32_16x16x64_bf16 v[196:199], v[204:207], v[224:231], v251\n\t" \
  "v_max_u32 v244, v244, v207\n\t" \
  "v_cmp_le_u32 vcc, %[sent], v244\n\t" \
  "s_cbranch_vccnz .Lslow%=\n\t" \
  ".Lok%=:\n\t" \
  "ds_read_b128 v[216:219], v250 offset:32768\n\t" \
  "ds_read_b128 v[220:223], v250 offset:33792\n\t" \
  "ds_read_b128 v[224:227], v250 offset:34816\n\t" \
  "ds_read_b128 v[228:231], v250 offset:35840\n\t" \
  "s_waitcnt lgkmcnt(4)\n\t" \
  "s_setprio 3\n\t" \
  "s_barrier\n\t" \
  "s_memrealtime s[90:91]\n\t" \
  "v_mov_b32 v232, %[step]\n\t" \
  "v_lshrrev_b32 v234, 1, v248\n\t" \
  "s_mov_b64 exec, 0xffff\n\t" \
  "s_waitcnt lgkmcnt(0)\n\t" \
  "s_lshl_b32 s90, s90, 2\n\t" \
  "s_sub_i32 s93, s90, %[tgt]\n\t" \
  "s_cmp_gt_i32 s93, 240\n\t" \
  "s_cselect_b32 s93, 90, 0\n\t" \
  "s_add_u32 s93, s93, s90\n\t" \
  "s_add_u32 s93, s93, %[lead]\n\t" \
  "v_mov_b32 v233, s93\n\t" \
  "buffer_store_dwordx2 v[232:233], v234, %[rs], %[sbc] offen sc1\n\t" \
  "s_mov_b64 exec, -1\n\t" \
  "s_mov_b32 %[tb1], s90\n\t" \
  "s_setprio 0\n\t" \
  "ds_read_b128 v[208:211], v248 offset:6144\n\t" \
  "ds_read_b128 v[212:215], v248 offset:9216\n\t" \
  "ds_read_b128 v[236:239], v248 offset:10240\n\t" \
  "ds_read_b128 v[240:243], v248 offset:11264\n\t" \
  "s_waitcnt lgkmcnt(3)\n\t" \
  "ds_read_b128 v[244:247], v248 offset:12288\n\t" \
  "v_smfmac_f32_16x16x64_bf16 v[192:195], v[208:211], v[216:223], v251\n\t" \
  "ds_read_b128 v[216:219], v250 offset:0\n\t" \
  "v_smfmac_f32_16x16x64_bf16 v[196:199], v[208:211], v[224:231], v251\n\t" \
  "s_waitcnt lgkmcnt(4)\n\t" \
  "ds_read_b128 v[220:223], v250 offset:1024\n\t" \
  "v_smfmac_f32_16x16x64_bf16 v[192:195], v[212:215], v[0:7], v251\n\t" \
  "ds_read_b128 v[224:227], v250 offset:2048\n\t" \
  "v_smfmac_f32_16x16x64_bf16 v[196:199], v[212:215], v[8:15], v251\n\t" \
  "s_waitcnt lgkmcnt(5)\n\t" \
  "ds_read_b128 v[228:231], v250 offset:3072\n\t" \
  "v_smfmac_f32_16x16x64_bf16 v[192:195], v[236:239], v[16:23], v251\n\t" \
  "ds_read_b128 v[200:203], v248 offset:13312\n\t" \
  "v_smfmac_f32_16x16x64_bf16 v[196:199], v[236:239], v[24:31], v251\n\t" \
  "s_waitcnt lgkmcnt(6)\n\t" \
  "ds_read_b128 v[204:207], v248 offset:14336\n\t" \
  "v_smfmac_f32_16x16x64_bf16 v[192:195], v[240:243], v[32:39], v251\n\t" \
  "ds_read_b128 v[208:211], v248 offset:0\n\t" \
  "v_smfmac_f32_16x16x64_bf16 v[196:199], v[240:243], v[40:47], v251\n\t" \
  "s_waitcnt lgkmcnt(5)\n\t" \
  "ds_read_b128 v[212:215], v248 offset:1024\n\t" \
  "v_smfmac_f32_16x16x64_bf16 v[192:195], v[244:247], v[216:223], v251\n\t" \
  "s_waitcnt lgkmcnt(4)\n\t" \
  "v_smfmac_f32_16x16x64_bf16 v[196:199], v[244:247], v[224:231], v251\n\t" \
  "s_waitcnt lgkmcnt(3)\n\t" \
  "ds_read_b128 v[236:239], v248 offset:2048\n\t" \
  "v_smfmac_f32_16x16x64_bf16 v[192:195], v[200:203], v[48:55], v251\n\t" \
  "ds_read_b128 v[240:243], v248 offset:3072\n\t" \
  "v_smfmac_f32_16x16x64_bf16 v[196:199], v[200:203], v[56:63], v251\n\t" \
  "s_waitcnt lgkmcnt(4)\n\t" \
  "v_smfmac_f32_16x16x64_bf16 v[192:195], v[204:207], v[64:71], v251\n\t" \
  "ds_read_b128 v[244:247], v248 offset:4096\n\t" \
  "v_smfmac_f32_16x16x64_bf16 v[196:199], v[204:207], v[72:79], v251\n\t" \
  "s_waitcnt lgkmcnt(4)\n\t" \
  "v_smfmac_f32_16x16x64_bf16 v[192:195], v[208:211], v[80:87], v251\n\t" \
  "ds_read_b128 v[200:203], v248 offset:5120\n\t" \
  "v_smfmac_f32_16x16x64_bf16 v[196:199], v[208:211], v[88:95], v251\n\t" \
  "s_waitcnt lgkmcnt(4)\n\t" \
  "v_smfmac_f32_16x16x64_bf16 v[192:195], v[212:215], v[96:103], v251\n\t" \
  "ds_read_b128 v[204:207], v248 offset:8192\n\t" \
  "v_smfmac_f32_16x16x64_bf16 v[196:199], v[212:215], v[104:111], v251\n\t" \
  "s_waitcnt lgkmcnt(3)\n\t" \
  "v_smfmac_f32_16x16x64_bf16 v[192:195], v[236:239], v[112:119], v251\n\t" \
  "v_smfmac_f32_16x16x64_bf16 v[196:199], v[236:239], v[120:127], v251\n\t" \
  "v_smfmac_f32_16x16x64_bf16 v[192:195], v[240:243], v[128:135], v251\n\t" \
  "v_smfmac_f32_16x16x64_bf16 v[196:199], v[240:243], v[136:143], v251\n\t" \
  "s_waitcnt lgkmcnt(1)\n\t" \
  "v_smfmac_f32_16x16x64_bf16 v[192:195], v[244:247], v[144:151], v251\n\t" \
  "v_smfmac_f32_16x16x64_bf16 v[196:199], v[244:247], v[152:159], v251\n\t" \
  "v_smfmac_f32_16x16x64_bf16 v[192:195], v[200:203], v[160:167], v251\n\t" \
  "v_smfmac_f32_16x16x64_bf16 v[196:199], v[200:203], v[168:175], v251\n\t" \
  "s_waitcnt lgkmcnt(0)\n\t" \
  "v_smfmac_f32_16x16x64_bf16 v[192:195], v[204:207], v[176:183], v251\n\t" \
  "v_smfmac_f32_16x16x64_bf16 v[196:199], v[204:207], v[184:191], v251\n\t" \
  "s_nop 6\n\t" \
  "v_add_f32 v192, v192, v194\n\t" \
  "v_add_f32 v193, v193, v195\n\t" \
  "v_add_f32 v196, v196, v198\n\t" \
  "v_add_f32 v197, v197, v199\n\t" \
  "v_cvt_pk_bf16_f32 v192, v192, v193\n\t" \
  "v_cvt_pk_bf16_f32 v196, v196, v197\n\t" \
  "ds_write2_b32 v252, v192, v196 offset1:64\n\t" \
  "s_waitcnt lgkmcnt(0)\n\t" \
  "s_barrier\n\t" \
  "buffer_store_dword v192, v235, %[rs], %[sk] offen sc1\n\t" \
  "buffer_store_dword v196, v235, %[rs], %[sk] offen offset:256 sc1\n\t" \
  "s_mov_b32 %[spins], s92\n\t" \
  "s_branch .Lend%=\n\t" \
  ".Lslow%=:\n\t" \
  "s_mov_b64 s[94:95], 0\n\t" \
  "v_cmp_eq_u32 vcc, %[sent], v200\n\t" \
  "s_or_b64 s[94:95], s[94:95], vcc\n\t" \
  "v_cmp_eq_u32 vcc, %[sent], v201\n\t" \
  "s_or_b64 s[94:95], s[94:95], vcc\n\t" \
  "v_cmp_eq_u32 vcc, %[sent], v202\n\t" \
  "s_or_b64 s[94:95], s[94:95], vcc\n\t" \
  "v_cmp_eq_u32 vcc, %[sent], v203\n\t" \
  "s_or_b64 s[94:95], s[94:95], vcc\n\t" \
  "v_cmp_eq_u32 vcc, %[sent], v204\n\t" \
  "s_or_b64 s[94:95], s[94:95], vcc\n\t" \
  "v_cmp_eq_u32 vcc, %[sent], v205\n\t" \
  "s_or_b64 s[94:95], s[94:95], vcc\n\t" \
  "v_cmp_eq_u32 vcc, %[sent], v206\n\t" \
  "s_or_b64 s[94:95], s[94:95], vcc\n\t" \
  "v_cmp_eq_u32 vcc, %[sent], v207\n\t" \
  "s_or_b64 s[94:95], s[94:95], vcc\n\t" \
  "s_cmp_eq_u64 s[94:95], 0\n\t" \
  "s_cbranch_scc1 .Lok%=\n\t" \
  "v_mov_b64 v[192:193], 0\n\t" \
  "v_mov_b64 v[194:195], 0\n\t" \
  "v_mov_b64 v[196:197], 0\n\t" \
  "v_mov_b64 v[198:199], 0\n\t" \
  "s_add_u32 s92, s92, 1\n\t" \
  "s_sleep 6\n\t" \
  "s_cmp_eq_u32 s92, 1\n\t" \
  "s_cbranch_scc1 .Lpoll%=\n\t" \
  "s_sleep 1\n\t" \
  "s_and_b32 s93, s92, 63\n\t" \
  "s_cmp_lg_u32 s93, 0\n\t" \
  "s_cbranch_scc1 .Lpoll%=\n\t" \
  "v_mov_b32 v245, 0\n\t" \
  "buffer_load_dword v244, v245, %[rs], %[sab] offen sc1\n\t" \
  "s_waitcnt vmcnt(0)\n\t" \
  "v_readfirstlane_b32 s93, v244\n\t" \
  "s_nop 3\n\t" \
  "s_cmp_eq_u32 s93, 1\n\t" \
  "s_cbranch_scc1 .Lok%=\n\t" \
  "s_cmp_lt_u32 s92, 0x80000\n\t" \
  "s_cbranch_scc1 .Lpoll%=\n\t" \
  "v_mov_b32 v244, 1\n\t" \
  "buffer_store_dword v244, v245, %[rs], %[sab] offen sc1\n\t" \
  "s_branch .Lok%=\n\t" \
  ".Lend%=:\n\t"
#define VV_ASM \
  "s_waitcnt lgkmcnt(0)\n\t" \
  "v_mov_b64 v[128:129], 0\n\t" \
  "v_mov_b64 v[130:131], 0\n\t" \
  "s_movk_i32 s92, 0x2000\n\t" \
  ".Lwt%=:\n\t" \
  "ds_read_b64 v[188:189], v200 offset:30720\n\t" \
  "s_memrealtime s[90:91]\n\t" \
  "s_sub_u32 s92, s92, 1\n\t" \
  "s_waitcnt lgkmcnt(0)\n\t" \
  "v_readfirstlane_b32 s93, v188\n\t" \
  "v_readfirstlane_b32 %[tgt], v189\n\t" \
  "s_cbranch_scc1 .Lgo%=\n\t" \
  "s_cmp_eq_u32 s93, %[step]\n\t" \
  "s_cbranch_scc1 .Lwx%=\n\t" \
  "s_sleep 1\n\t" \
  "s_branch .Lwt%=\n\t" \
  ".Lwx%=:\n\t" \
  "s_lshl_b32 s93, s90, 2\n\t" \
  "s_sub_i32 s93, %[tgt], s93\n\t" \
  "s_cmp_le_i32 s93, 0\n\t" \
  "s_cbranch_scc1 .Lgo%=\n\t" \
  "s_min_i32 s93, s93, 8000\n\t" \
  "s_add_u32 s93, s93, 3\n\t" \
  "s_lshr_b32 s93, s93, 2\n\t" \
  "s_add_u32 s93, s93, s90\n\t" \
  "s_memrealtime s[90:91]\n\t" \
  ".Lpace%=:\n\t" \
  "s_memrealtime s[94:95]\n\t" \
  "s_waitcnt lgkmcnt(1)\n\t" \
  "s_sub_i32 s91, s90, s93\n\t" \
  "s_cmp_ge_i32 s91, 0\n\t" \
  "s_cbranch_scc1 .Lgo%=\n\t" \
  "s_memrealtime s[90:91]\n\t" \
  "s_waitcnt lgkmcnt(1)\n\t" \
  "s_sub_i32 s95, s94, s93\n\t" \
  "s_cmp_lt_i32 s95, 0\n\t" \
  "s_cbranch_scc1 .Lpace%=\n\t" \
  ".Lgo%=:\n\t" \
  "s_mov_b32 s92, 0\n\t" \
  ".Lpoll%=:\n\t" \
  "buffer_load_dwordx4 v[188:191], v200, %[rs], %[som] offen sc1\n\t" \
  "buffer_load_dwordx4 v[192:195], v200, %[rs], %[som] offen offset:1024 sc1\n\t" \
  "s_waitcnt vmcnt(1) lgkmcnt(0)\n\t" \
  "ds_write_b128 v201, v[188:191]\n\t" \
  "v_max3_u32 v196, v188, v189, v190\n\t" \
  "s_waitcnt vmcnt(0)\n\t" \
  "ds_write_b128 v201, v[192:195] offset:1024\n\t" \
  "v_max3_u32 v196, v196, v191, v192\n\t" \
  "v_max3_u32 v196, v196, v193, v194\n\t" \
  "v_max_u32 v196, v196, v195\n\t" \
  "v_cmp_le_u32 vcc, %[sent], v196\n\t" \
  "s_cbranch_vccnz .Lslow%=\n\t" \
  ".Lok%=:\n\t" \
  "s_waitcnt lgkmcnt(0)\n\t" \
  "s_barrier\n\t" \
  "ds_read_b128 v[132:135], v200 offset:0\n\t" \
  "ds_read_b128 v[136:139], v200 offset:1024\n\t" \
  "ds_read_b128 v[140:143], v200 offset:2048\n\t" \
  "ds_read_b128 v[144:147], v200 offset:3072\n\t" \
  "ds_read_b128 v[148:151], v200 offset:4096\n\t" \
  "s_waitcnt lgkmcnt(4)\n\t" \
  "ds_read_b128 v[152:155], v200 offset:5120\n\t" \
  "v_smfmac_f32_16x16x64_bf16 v[128:131], v[132:135], v[0:7], v202\n\t" \
  "s_waitcnt lgkmcnt(4)\n\t" \
  "ds_read_b128 v[156:159], v200 offset:6144\n\t" \
  "v_smfmac_f32_16x16x64_bf16 v[128:131], v[136:139], v[8:15], v202\n\t" \
  "s_waitcnt lgkmcnt(4)\n\t" \
  "ds_read_b128 v[160:163], v200 offset:7168\n\t" \
  "v_smfmac_f32_16x16x64_bf16 v[128:131], v[140:143], v[16:23], v202\n\t" \
  "s_waitcnt lgkmcnt(4)\n\t" \
  "ds_read_b128 v[164:167], v200 offset:8192\n\t" \
  "v_smfmac_f32_16x16x64_bf16 v[128:131], v[144:147], v[24:31], v202\n\t" \
  "s_waitcnt lgkmcnt(4)\n\t" \
  "ds_read_b128 v[168:171], v200 offset:9216\n\t" \
  "v_smfmac_f32_16x16x64_bf16 v[128:131], v[148:151], v[32:39], v202\n\t" \
  "s_waitcnt lgkmcnt(4)\n\t" \
  "ds_read_b128 v[172:175], v200 offset:10240\n\t" \
  "v_smfmac_f32_16x16x64_bf16 v[128:131], v[152:155], v[40:47], v202\n\t" \
  "s_waitcnt lgkmcnt(4)\n\t" \
  "ds_read_b128 v[176:179], v200 offset:11264\n\t" \
  "v_smfmac_f32_16x16x64_bf16 v[128:131], v[156:159], v[48:55], v202\n\t" \
  "s_waitcnt lgkmcnt(4)\n\t" \
  "ds_read_b128 v[180:183], v200 offset:12288\n\t" \
  "v_smfmac_f32_16x16x64_bf16 v[128:131], v[160:163], v[56:63], v202\n\t" \
  "s_waitcnt lgkmcnt(4)\n\t" \
  "ds_read_b128 v[184:187], v200 offset:13312\n\t" \
  "v_smfmac_f32_16x16x64_bf16 v[128:131], v[164:167], v[64:71], v202\n\t" \
  "s_waitcnt lgkmcnt(4)\n\t" \
  "ds_read_b128 v[188:191], v200 offset:14336\n\t" \
  "v_smfmac_f32_16x16x64_bf16 v[128:131], v[168:171], v[72:79], v202\n\t" \
  "s_waitcnt lgkmcnt(4)\n\t" \
  "ds_read_b128 v[192:195], v200 offset:15360\n\t" \
  "v_smfmac_f32_16x16x64_bf16 v[128:131], v[172:175], v[80:87], v202\n\t" \
  "s_waitcnt lgkmcnt(4)\n\t" \
  "v_smfmac_f32_16x16x64_bf16 v[128:131], v[176:179], v[88:95], v202\n\t" \
  "s_waitcnt lgkmcnt(3)\n\t" \
  "v_smfmac_f32_16x16x64_bf16 v[128:131], v[180:183], v[96:103], v202\n\t" \
  "s_waitcnt lgkmcnt(2)\n\t" \
  "v_smfmac_f32_16x16x64_bf16 v[128:131], v[184:187], v[104:111], v202\n\t" \
  "s_waitcnt lgkmcnt(1)\n\t" \
  "v_smfmac_f32_16x16x64_bf16 v[128:131], v[188:191], v[112:119], v202\n\t" \
  "s_waitcnt lgkmcnt(0)\n\t" \
  "v_smfmac_f32_16x16x64_bf16 v[128:131], v[192:195], v[120:127], v202\n\t" \
  "s_nop 7\n\t" \
  "v_add_f32 v128, v128, v130\n\t" \
  "v_add_f32 v129, v129, v131\n\t" \
  "v_cvt_pk_bf16_f32 v128, v128, v129\n\t" \
  "buffer_store_dword v128, v203, %[rs], %[sv] offen sc1\n\t" \
  "ds_write_b32 v204, v128\n\t" \
  "s_waitcnt lgkmcnt(0)\n\t" \
  "s_barrier\n\t" \
  "s_mov_b32 %[spins], s92\n\t" \
  "s_branch .Lend%=\n\t" \
  ".Lslow%=:\n\t" \
  "s_mov_b64 s[94:95], 0\n\t" \
  "v_cmp_eq_u32 vcc, %[sent], v188\n\t" \
  "s_or_b64 s[94:95], s[94:95], vcc\n\t" \
  "v_cmp_eq_u32 vcc, %[sent], v189\n\t" \
  "s_or_b64 s[94:95], s[94:95], vcc\n\t" \
  "v_cmp_eq_u32 vcc, %[sent], v190\n\t" \
  "s_or_b64 s[94:95], s[94:95], vcc\n\t" \
  "v_cmp_eq_u32 vcc, %[sent], v191\n\t" \
  "s_or_b64 s[94:95], s[94:95], vcc\n\t" \
  "v_cmp_eq_u32 vcc, %[sent], v192\n\t" \
  "s_or_b64 s[94:95], s[94:95], vcc\n\t" \
  "v_cmp_eq_u32 vcc, %[sent], v193\n\t" \
  "s_or_b64 s[94:95], s[94:95], vcc\n\t" \
  "v_cmp_eq_u32 vcc, %[sent], v194\n\t" \
  "s_or_b64 s[94:95], s[94:95], vcc\n\t" \
  "v_cmp_eq_u32 vcc, %[sent], v195\n\t" \
  "s_or_b64 s[94:95], s[94:95], vcc\n\t" \
  "s_cmp_eq_u64 s[94:95], 0\n\t" \
  "s_cbranch_scc1 .Lok%=\n\t" \
  "s_add_u32 s92, s92, 1\n\t" \
  "s_sleep 6\n\t" \
  "s_cmp_eq_u32 s92, 1\n\t" \
  "s_cbranch_scc1 .Lpoll%=\n\t" \
  "s_sleep 1\n\t" \
  "s_and_b32 s93, s92, 63\n\t" \
  "s_cmp_lg_u32 s93, 0\n\t" \
  "s_cbranch_scc1 .Lpoll%=\n\t" \
  "v_mov_b32 v197, 0\n\t" \
  "buffer_load_dword v196, v197, %[rs], %[sab] offen sc1\n\t" \
  "s_waitcnt vmcnt(0)\n\t" \
  "v_readfirstlane_b32 s93, v196\n\t" \
  "s_nop 3\n\t" \
  "s_cmp_eq_u32 s93, 1\n\t" \
  "s_cbranch_scc1 .Lok%=\n\t" \
  "s_cmp_lt_u32 s92, 0x80000\n\t" \
  "s_cbranch_scc1 .Lpoll%=\n\t" \
  "v_mov_b32 v196, 1\n\t" \
  "buffer_store_dword v196, v197, %[rs], %[sab] offen sc1\n\t" \
  "s_branch .Lok%=\n\t" \
  ".Lend%=:\n\t"
#define TK_ASM \
  "buffer_load_dwordx4 v[224:227], v200, %[rs], %[sk] offen offset:0 sc1\n\t" \
  "buffer_load_dwordx4 v[228:231], v200, %[rs], %[sk] offen offset:1024 sc1\n\t" \
  "s_memrealtime s[88:89]\n\t" \
  "v_add_u32 v205, %[tl], v200\n\t" \
  "v_add_u32 v206, %[tq], v205\n\t" \
  "v_add_u32 v207, %[tv], v205\n\t" \
  "s_waitcnt lgkmcnt(0)\n\t" \
  "ds_read_b128 v[128:131], v206 offset:8192\n\t" \
  "ds_read_b128 v[144:147], v205 offset:0\n\t" \
  "ds_read_b128 v[160:163], v205 offset:4096\n\t" \
  "ds_read_b128 v[132:135], v206 offset:9216\n\t" \
  "ds_read_b128 v[148:151], v205 offset:1024\n\t" \
  "ds_read_b128 v[164:167], v205 offset:5120\n\t" \
  "ds_read_b128 v[136:139], v206 offset:10240\n\t" \
  "ds_read_b128 v[152:155], v205 offset:2048\n\t" \
  "ds_read_b128 v[168:171], v205 offset:6144\n\t" \
  "ds_read_b128 v[140:143], v206 offset:11264\n\t" \
  "ds_read_b128 v[156:159], v205 offset:3072\n\t" \
  "ds_read_b128 v[172:175], v205 offset:7168\n\t" \
  "v_add_u32 v206, %[vp], v218\n\t" \
  "s_lshl_b32 s88, s88, 2\n\t" \
  "s_add_u32 s89, s88, %[od]\n\t" \
  "s_waitcnt lgkmcnt(10)\n\t" \
  "v_mfma_f32_16x16x32_bf16 v[196:199], v[144:147], v[128:131], 0\n\t" \
  "s_waitcnt lgkmcnt(9)\n\t" \
  "v_mfma_f32_16x16x32_bf16 v[144:147], v[160:163], v[128:131], 0\n\t" \
  "s_waitcnt lgkmcnt(7)\n\t" \
  "v_mfma_f32_16x16x32_bf16 v[196:199], v[148:151], v[132:135], v[196:199]\n\t" \
  "s_waitcnt lgkmcnt(6)\n\t" \
  "v_mfma_f32_16x16x32_bf16 v[144:147], v[164:167], v[132:135], v[144:147]\n\t" \
  "s_waitcnt lgkmcnt(4)\n\t" \
  "v_mfma_f32_16x16x32_bf16 v[196:199], v[152:155], v[136:139], v[196:199]\n\t" \
  "s_waitcnt lgkmcnt(3)\n\t" \
  "v_mfma_f32_16x16x32_bf16 v[144:147], v[168:171], v[136:139], v[144:147]\n\t" \
  "s_waitcnt lgkmcnt(1)\n\t" \
  "v_mfma_f32_16x16x32_bf16 v[196:199], v[156:159], v[140:143], v[196:199]\n\t" \
  "s_waitcnt lgkmcnt(0)\n\t" \
  "v_mfma_f32_16x16x32_bf16 v[144:147], v[172:175], v[140:143], v[144:147]\n\t" \
  "ds_read_b128 v[176:179], v207 offset:16384\n\t" \
  "ds_read_b128 v[180:183], v207 offset:17408\n\t" \
  "ds_read_b128 v[184:187], v207 offset:18432\n\t" \
  "ds_read_b128 v[188:191], v207 offset:19456\n\t" \
  "ds_read2st64_b32 v[192:193], v206 offset0:0 offset1:1\n\t" \
  "ds_read2st64_b32 v[194:195], v206 offset0:2 offset1:3\n\t" \
  "v_min_f32 v196, v196, v208\n\t" \
  "v_min_f32 v197, v197, v209\n\t" \
  "v_min_f32 v198, v198, v210\n\t" \
  "v_min_f32 v199, v199, v211\n\t" \
  "v_min_f32 v144, v144, v212\n\t" \
  "v_min_f32 v145, v145, v213\n\t" \
  "v_min_f32 v146, v146, v214\n\t" \
  "v_min_f32 v147, v147, v215\n\t" \
  "v_max3_f32 v148, v196, v197, v198\n\t" \
  "v_max3_f32 v149, v199, v144, v145\n\t" \
  "v_max3_f32 v148, v148, v146, v147\n\t" \
  "v_max_f32 v150, v148, v149\n\t" \
  "v_max_f32 v151, v148, v149\n\t" \
  "s_nop 1\n\t" \
  "v_permlane32_swap_b32 v150, v151\n\t" \
  "v_max_f32 v150, v150, v151\n\t" \
  "s_mov_b32 s92, 0\n\t" \
  ".Ltake%=:\n\t" \
  "s_waitcnt vmcnt(1) lgkmcnt(0)\n\t" \
  "ds_write_b128 v216, v[224:227]\n\t" \
  "v_max3_u32 v148, v224, v225, v226\n\t" \
  "s_waitcnt vmcnt(0)\n\t" \
  "ds_write_b128 v216, v[228:231] offset:1024\n\t" \
  "ds_read_b64_tr_b16 v[160:161], v217 offset:0\n\t" \
  "ds_read_b64_tr_b16 v[162:163], v217 offset:64\n\t" \
  "ds_read_b64_tr_b16 v[164:165], v217 offset:512\n\t" \
  "ds_read_b64_tr_b16 v[166:167], v217 offset:576\n\t" \
  "ds_read_b64_tr_b16 v[168:169], v217 offset:1024\n\t" \
  "ds_read_b64_tr_b16 v[170:171], v217 offset:1088\n\t" \
  "ds_read_b64_tr_b16 v[172:173], v217 offset:1536\n\t" \
  "ds_read_b64_tr_b16 v[174:175], v217 offset:1600\n\t" \
  "v_max3_u32 v148, v148, v227, v228\n\t" \
  "v_max3_u32 v148, v148, v229, v230\n\t" \
  "v_max_u32 v148, v148, v231\n\t" \
  "v_cmp_le_u32 vcc, %[sent], v148\n\t" \
  "s_cbranch_vccnz .Lslow%=\n\t" \
  ".Lok%=:\n\t" \
  "s_waitcnt lgkmcnt(6)\n\t" \
  "v_mfma_f32_16x16x32_bf16 v[152:155], v[160:163], v[128:131], 0\n\t" \
  "s_waitcnt lgkmcnt(4)\n\t" \
  "v_mfma_f32_16x16x32_bf16 v[152:155], v[164:167], v[132:135], v[152:155]\n\t" \
  "s_waitcnt lgkmcnt(2)\n\t" \
  "v_mfma_f32_16x16x32_bf16 v[152:155], v[168:171], v[136:139], v[152:155]\n\t" \
  "s_waitcnt lgkmcnt(0)\n\t" \
  "v_mfma_f32_16x16x32_bf16 v[152:155], v[172:175], v[140:143], v[152:155]\n\t" \
  "v_mov_b32 v157, 0\n\t" \
  "v_mov_b32 v158, 0\n\t" \
  "s_nop 5\n\t" \
  "v_max3_f32 v148, v152, v153, v150\n\t" \
  "v_max3_f32 v149, v148, v154, v155\n\t" \
  "v_max3_f32 v150, v148, v154, v155\n\t" \
  "v_cndmask_b32 v152, v152, v154, %[up]\n\t" \
  "v_cndmask_b32 v153, v153, v155, %[up]\n\t" \
  "v_permlane16_swap_b32 v149, v150\n\t" \
  "v_max_f32 v148, v149, v150\n\t" \
  "v_mul_f32 v148, %[negc], v148\n\t" \
  "v_fma_f32 v152, v152, %[c], v148\n\t" \
  "v_fma_f32 v153, v153, %[c], v148\n\t" \
  "v_exp_f32 v152, v152\n\t" \
  "v_fma_f32 v196, v196, %[c], v148\n\t" \
  "v_exp_f32 v153, v153\n\t" \
  "v_fma_f32 v197, v197, %[c], v148\n\t" \
  "v_exp_f32 v196, v196\n\t" \
  "v_fma_f32 v198, v198, %[c], v148\n\t" \
  "v_cvt_pk_bf16_f32 v156, v152, v153\n\t" \
  "v_cvt_pk_bf16_f32 v159, v152, v153\n\t" \
  "v_exp_f32 v197, v197\n\t" \
  "v_fma_f32 v199, v199, %[c], v148\n\t" \
  "v_mfma_f32_16x16x16_bf16 v[132:135], v[220:221], v[156:157], 0\n\t" \
  "v_exp_f32 v198, v198\n\t" \
  "v_fma_f32 v144, v144, %[c], v148\n\t" \
  "v_mfma_f32_16x16x16_bf16 v[160:163], v[192:193], v[156:157], 0\n\t" \
  "v_cvt_pk_bf16_f32 v128, v196, v197\n\t" \
  "v_exp_f32 v199, v199\n\t" \
  "v_fma_f32 v145, v145, %[c], v148\n\t" \
  "v_mfma_f32_16x16x16_bf16 v[164:167], v[192:193], v[158:159], 0\n\t" \
  "v_exp_f32 v144, v144\n\t" \
  "v_fma_f32 v146, v146, %[c], v148\n\t" \
  "v_mfma_f32_16x16x16_bf16 v[168:171], v[194:195], v[156:157], 0\n\t" \
  "v_cvt_pk_bf16_f32 v129, v198, v199\n\t" \
  "v_exp_f32 v145, v145\n\t" \
  "v_fma_f32 v147, v147, %[c], v148\n\t" \
  "v_mfma_f32_16x16x16_bf16 v[172:175], v[194:195], v[158:159], 0\n\t" \
  "v_exp_f32 v146, v146\n\t" \
  "v_cvt_pk_bf16_f32 v130, v144, v145\n\t" \
  "v_exp_f32 v147, v147\n\t" \
  "s_nop 0\n\t" \
  "v_cvt_pk_bf16_f32 v131, v146, v147\n\t" \
  "s_nop 1\n\t" \
  "v_mfma_f32_16x16x32_bf16 v[132:135], v[220:223], v[128:131], v[132:135]\n\t" \
  "v_mfma_f32_16x16x32_bf16 v[160:163], v[176:179], v[128:131], v[160:163]\n\t" \
  "v_mfma_f32_16x16x32_bf16 v[164:167], v[180:183], v[128:131], v[164:167]\n\t" \
  "v_mfma_f32_16x16x32_bf16 v[168:171], v[184:187], v[128:131], v[168:171]\n\t" \
  "v_mfma_f32_16x16x32_bf16 v[172:175], v[188:191], v[128:131], v[172:175]\n\t" \
  "s_nop 3\n\t" \
  "v_rcp_f32 v148, v132\n\t" \
  "s_nop 0\n\t" \
  "v_pk_mul_f32 v[160:161], v[160:161], v[148:149] op_sel_hi:[1,0]\n\t" \
  "v_pk_mul_f32 v[162:163], v[162:163], v[148:149] op_sel_hi:[1,0]\n\t" \
  "v_cvt_pk_bf16_f32 v136, v160, v161\n\t" \
  "v_cvt_pk_bf16_f32 v137, v162, v163\n\t" \
  "v_pk_mul_f32 v[164:165], v[164:165], v[148:149] op_sel_hi:[1,0]\n\t" \
  "v_pk_mul_f32 v[166:167], v[166:167], v[148:149] op_sel_hi:[1,0]\n\t" \
  "v_cvt_pk_bf16_f32 v138, v164, v165\n\t" \
  "v_cvt_pk_bf16_f32 v139, v166, v167\n\t" \
  "v_pk_mul_f32 v[168:169], v[168:169], v[148:149] op_sel_hi:[1,0]\n\t" \
  "v_pk_mul_f32 v[170:171], v[170:171], v[148:149] op_sel_hi:[1,0]\n\t" \
  "v_cvt_pk_bf16_f32 v140, v168, v169\n\t" \
  "v_cvt_pk_bf16_f32 v141, v170, v171\n\t" \
  "v_pk_mul_f32 v[172:173], v[172:173], v[148:149] op_sel_hi:[1,0]\n\t" \
  "v_pk_mul_f32 v[174:175], v[174:175], v[148:149] op_sel_hi:[1,0]\n\t" \
  "v_cvt_pk_bf16_f32 v142, v172, v173\n\t" \
  "v_cvt_pk_bf16_f32 v143, v174, v175\n\t" \
  "v_permlane32_swap_b32 v136, v138\n\t" \
  "v_permlane32_swap_b32 v137, v139\n\t" \
  "v_permlane32_swap_b32 v140, v142\n\t" \
  "v_permlane32_swap_b32 v141, v143\n\t" \
  "v_permlane16_swap_b32 v136, v138\n\t" \
  "v_permlane16_swap_b32 v137, v139\n\t" \
  "v_permlane16_swap_b32 v140, v142\n\t" \
  "v_permlane16_swap_b32 v141, v143\n\t" \
  "s_memrealtime s[90:91]\n\t" \
  "s_waitcnt lgkmcnt(0)\n\t" \
  "s_lshl_b32 s93, s90, 2\n\t" \
  "s_sub_i32 s93, s89, s93\n\t" \
  "s_cmp_le_i32 s93, 0\n\t" \
  "s_cbranch_scc1 .Lgoo%=\n\t" \
  "s_min_i32 s93, s93, 8000\n\t" \
  "s_add_u32 s93, s93, 3\n\t" \
  "s_lshr_b32 s93, s93, 2\n\t" \
  "s_add_u32 s93, s93, s90\n\t" \
  "s_memrealtime s[90:91]\n\t" \
  ".Lpaceo%=:\n\t" \
  "s_memrealtime s[94:95]\n\t" \
  "s_waitcnt lgkmcnt(1)\n\t" \
  "s_sub_i32 s91, s90, s93\n\t" \
  "s_cmp_ge_i32 s91, 0\n\t" \
  "s_cbranch_scc1 .Lgoo%=\n\t" \
  "s_memrealtime s[90:91]\n\t" \
  "s_waitcnt lgkmcnt(1)\n\t" \
  "s_sub_i32 s95, s94, s93\n\t" \
  "s_cmp_lt_i32 s95, 0\n\t" \
  "s_cbranch_scc1 .Lpaceo%=\n\t" \
  ".Lgoo%=:\n\t" \
  "buffer_store_dwordx4 v[136:139], v200, %[rt], %[so] offen offset:0 sc1\n\t" \
  "buffer_store_dwordx4 v[140:143], v200, %[rt], %[so] offen offset:1024 sc1\n\t" \
  "s_waitcnt lgkmcnt(0)\n\t" \
  "s_branch .Lend%=\n\t" \
  ".Lpoll%=:\n\t" \
  "buffer_load_dwordx4 v[224:227], v200, %[rs], %[sk] offen offset:0 sc1\n\t" \
  "buffer_load_dwordx4 v[228:231], v200, %[rs], %[sk] offen offset:1024 sc1\n\t" \
  "s_branch .Ltake%=\n\t" \
  "s_branch .Lend%=\n\t" \
  ".Lslow%=:\n\t" \
  "s_mov_b64 s[94:95], 0\n\t" \
  "v_cmp_eq_u32 vcc, %[sent], v224\n\t" \
  "s_or_b64 s[94:95], s[94:95], vcc\n\t" \
  "v_cmp_eq_u32 vcc, %[sent], v225\n\t" \
  "s_or_b64 s[94:95], s[94:95], vcc\n\t" \
  "v_cmp_eq_u32 vcc, %[sent], v226\n\t" \
  "s_or_b64 s[94:95], s[94:95], vcc\n\t" \
  "v_cmp_eq_u32 vcc, %[sent], v227\n\t" \
  "s_or_b64 s[94:95], s[94:95], vcc\n\t" \
  "v_cmp_eq_u32 vcc, %[sent], v228\n\t" \
  "s_or_b64 s[94:95], s[94:95], vcc\n\t" \
  "v_cmp_eq_u32 vcc, %[sent], v229\n\t" \
  "s_or_b64 s[94:95], s[94:95], vcc\n\t" \
  "v_cmp_eq_u32 vcc, %[sent], v230\n\t" \
  "s_or_b64 s[94:95], s[94:95], vcc\n\t" \
  "v_cmp_eq_u32 vcc, %[sent], v231\n\t" \
  "s_or_b64 s[94:95], s[94:95], vcc\n\t" \
  "s_cmp_eq_u64 s[94:95], 0\n\t" \
  "s_cbranch_scc1 .Lok%=\n\t" \
  "s_add_u32 s92, s92, 1\n\t" \
  "s_sleep 6\n\t" \
  "s_cmp_eq_u32 s92, 1\n\t" \
  "s_cbranch_scc1 .Lpoll%=\n\t" \
  "s_sleep 1\n\t" \
  "s_and_b32 s93, s92, 63\n\t" \
  "s_cmp_lg_u32 s93, 0\n\t" \
  "s_cbranch_scc1 .Lpoll%=\n\t" \
  "v_mov_b32 v149, 0\n\t" \
  "buffer_load_dword v148, v149, %[rs], %[sab] offen sc1\n\t" \
  "s_waitcnt vmcnt(0)\n\t" \
  "v_readfirstlane_b32 s93, v148\n\t" \
  "s_nop 3\n\t" \
  "s_cmp_eq_u32 s93, 1\n\t" \
  "s_cbranch_scc1 .Lok%=\n\t" \
  "s_cmp_lt_u32 s92, 0x80000\n\t" \
  "s_cbranch_scc1 .Lpoll%=\n\t" \
  "v_mov_b32 v148, 1\n\t" \
  "buffer_store_dword v148, v149, %[rs], %[sab] offen sc1\n\t" \
  "s_branch .Lok%=\n\t" \
  ".Lend%=:\n\t"
constexpr int TLAG = 2;
#define RED_ASM \
  "s_mov_b32 %[step], 0\n\t" \
  "s_lshl_b32 %[sh], %[step], 15\n\t" \
  "s_lshr_b32 s93, %[step], 4\n\t" \
  "s_lshr_b32 s93, %[pm], s93\n\t" \
  "s_and_b32 s93, s93, 4\n\t" \
  "s_lshl_b32 s93, s93, 11\n\t" \
  "s_lshl2_add_u32 %[so], %[sh], s93\n\t" \
  "s_sub_i32 s93, %[step], 2\n\t" \
  "s_lshl_b32 %[st], s93, 19\n\t" \
  "s_lshl_b32 s94, s93, 11\n\t" \
  "s_xor_b32 s94, s94, %[s0]\n\t" \
  "s_and_b32 s94, s94, 0x2000\n\t" \
  "s_or_b32 %[st], %[st], s94\n\t" \
  "s_cmp_lt_i32 s93, 0\n\t" \
  "s_cselect_b32 %[st], 0x70000000, %[st]\n\t" \
  "s_add_u32 %[st4], %[st], 4096\n\t" \
  "buffer_load_dwordx2 v[166:167], v173, %[rs], 0 offen sc1\n\t" \
  "v_mov_b64 v[144:145], 0\n\t" \
  "v_mov_b64 v[146:147], 0\n\t" \
  "v_mov_b64 v[148:149], 0\n\t" \
  "v_mov_b64 v[150:151], 0\n\t" \
  "v_mov_b64 v[152:153], 0\n\t" \
  "v_mov_b64 v[154:155], 0\n\t" \
  "v_mov_b64 v[156:157], 0\n\t" \
  "v_mov_b64 v[158:159], 0\n\t" \
  ".Lstep%=:\n\t" \
  "s_setprio 3\n\t" \
  "s_mov_b32 s92, 0\n\t" \
  ".Lcal%=:\n\t" \
  "s_waitcnt vmcnt(0)\n\t" \
  "s_memrealtime s[76:77]\n\t" \
  "v_cmp_ne_u32 vcc, %[step], v166\n\t" \
  "v_readlane_b32 s90, v167, 0\n\t" \
  "v_readlane_b32 s91, v167, 1\n\t" \
  "v_readlane_b32 s94, v167, 2\n\t" \
  "v_readlane_b32 s95, v167, 3\n\t" \
  "s_sub_i32 s90, s90, %[base]\n\t" \
  "s_sub_i32 s91, s91, %[base]\n\t" \
  "s_sub_i32 s94, s94, %[base]\n\t" \
  "s_sub_i32 s95, s95, %[base]\n\t" \
  "s_max_i32 s90, s90, s91\n\t" \
  "s_max_i32 s94, s94, s95\n\t" \
  "s_max_i32 s90, s90, s94\n\t" \
  "s_add_i32 s90, s90, %[ds]\n\t" \
  "s_waitcnt lgkmcnt(0)\n\t" \
  "s_lshl2_add_u32 s76, s76, %[mn]\n\t" \
  "s_sub_i32 s76, s76, %[base]\n\t" \
  "s_max_i32 s94, s90, s76\n\t" \
  "s_add_u32 %[E], s90, %[base]\n\t" \
  "s_and_b32 s93, vcc_lo, 0xf\n\t" \
  "s_cbranch_scc1 .Lbl%=\n\t" \
  ".Lgo%=:\n\t" \
  "v_mov_b32 v166, 7\n\t" \
  "v_mov_b32 v162, %[step]\n\t" \
  "s_mov_b64 exec, 1\n\t" \
  "v_mov_b32 v164, s94\n\t" \
  "ds_max_u32 v168, v164 offset:4100\n\t" \
  "ds_inc_rtn_u32 v161, v168, v166 offset:4096\n\t" \
  "ds_read_b32 v163, v168 offset:4100\n\t" \
  "s_waitcnt lgkmcnt(0)\n\t" \
  "v_readfirstlane_b32 s94, v161\n\t" \
  "v_readfirstlane_b32 s95, v163\n\t" \
  "s_mov_b64 exec, -1\n\t" \
  "s_cmp_lg_u32 s94, 7\n\t" \
  "s_cbranch_scc1 .Lna%=\n\t" \
  "s_add_u32 s95, s95, %[rl]\n\t" \
  "s_add_u32 s95, s95, %[base]\n\t" \
  "s_mov_b64 exec, 0xffff\n\t" \
  "v_mov_b32 v163, s95\n\t" \
  "buffer_store_dwordx2 v[162:163], v175, %[rs], 0 offen sc1\n\t" \
  "s_mov_b64 exec, -1\n\t" \
  ".Lna%=:\n\t" \
  "s_memrealtime s[90:91]\n\t" \
  "s_waitcnt lgkmcnt(0)\n\t" \
  "s_lshl_b32 s93, s90, 2\n\t" \
  "s_sub_i32 s93, %[E], s93\n\t" \
  "s_cmp_le_i32 s93, 0\n\t" \
  "s_cbranch_scc1 .Lgop%=\n\t" \
  "s_min_i32 s93, s93, 8000\n\t" \
  "s_add_u32 s93, s93, 3\n\t" \
  "s_lshr_b32 s93, s93, 2\n\t" \
  "s_add_u32 s93, s93, s90\n\t" \
  "s_memrealtime s[90:91]\n\t" \
  ".Lpacep%=:\n\t" \
  "s_memrealtime s[94:95]\n\t" \
  "s_waitcnt lgkmcnt(1)\n\t" \
  "s_sub_i32 s91, s90, s93\n\t" \
  "s_cmp_ge_i32 s91, 0\n\t" \
  "s_cbranch_scc1 .Lgop%=\n\t" \
  "s_memrealtime s[90:91]\n\t" \
  "s_waitcnt lgkmcnt(1)\n\t" \
  "s_sub_i32 s95, s94, s93\n\t" \
  "s_cmp_lt_i32 s95, 0\n\t" \
  "s_cbranch_scc1 .Lpacep%=\n\t" \
  ".Lgop%=:\n\t" \
  "buffer_load_dwordx4 v[128:131], v234, %[rs], %[so] offen sc1\n\t" \
  "s_nop 15\n\t" \
  "buffer_load_dwordx4 v[132:135], v235, %[rs], %[so] offen sc1\n\t" \
  "s_nop 15\n\t" \
  "buffer_load_dwordx4 v[136:139], v236, %[rs], %[so] offen sc1\n\t" \
  "s_nop 15\n\t" \
  "buffer_load_dwordx4 v[140:143], v237, %[rs], %[so] offen sc1\n\t" \
  "s_waitcnt lgkmcnt(0)\n\t" \
  ".Lw%=:\n\t" \
  "s_waitcnt vmcnt(3)\n\t" \
  "v_smfmac_f32_16x16x64_bf16 v[144:147], v[128:131], v[0:7], v172\n\t" \
  "v_max3_u32 v164, v128, v129, 0\n\t" \
  "v_smfmac_f32_16x16x64_bf16 v[148:151], v[128:131], v[8:15], v172\n\t" \
  "v_max3_u32 v164, v130, v131, v164\n\t" \
  "v_smfmac_f32_16x16x64_bf16 v[152:155], v[128:131], v[16:23], v172\n\t" \
  "v_smfmac_f32_16x16x64_bf16 v[156:159], v[128:131], v[24:31], v172\n\t" \
  "buffer_load_dwordx4 v[176:179], v238, %[rt], %[st] offen offset:0 sc1\n\t" \
  "s_waitcnt vmcnt(3)\n\t" \
  "v_smfmac_f32_16x16x64_bf16 v[144:147], v[132:135], v[32:39], v172\n\t" \
  "v_max3_u32 v164, v132, v133, v164\n\t" \
  "v_smfmac_f32_16x16x64_bf16 v[148:151], v[132:135], v[40:47], v172\n\t" \
  "v_max3_u32 v164, v134, v135, v164\n\t" \
  "v_smfmac_f32_16x16x64_bf16 v[152:155], v[132:135], v[48:55], v172\n\t" \
  "v_smfmac_f32_16x16x64_bf16 v[156:159], v[132:135], v[56:63], v172\n\t" \
  "buffer_load_dwordx4 v[180:183], v238, %[rt], %[st] offen offset:1024 sc1\n\t" \
  "s_waitcnt vmcnt(3)\n\t" \
  "v_smfmac_f32_16x16x64_bf16 v[144:147], v[136:139], v[64:71], v172\n\t" \
  "v_max3_u32 v164, v136, v137, v164\n\t" \
  "v_smfmac_f32_16x16x64_bf16 v[148:151], v[136:139], v[72:79], v172\n\t" \
  "v_max3_u32 v164, v138, v139, v164\n\t" \
  "v_smfmac_f32_16x16x64_bf16 v[152:155], v[136:139], v[80:87], v172\n\t" \
  "v_smfmac_f32_16x16x64_bf16 v[156:159], v[136:139], v[88:95], v172\n\t" \
  "buffer_load_dwordx4 v[184:187], v238, %[rt], %[st4] offen offset:0 sc1\n\t" \
  "s_waitcnt vmcnt(3)\n\t" \
  "v_smfmac_f32_16x16x64_bf16 v[144:147], v[140:143], v[96:103], v172\n\t" \
  "v_max3_u32 v164, v140, v141, v164\n\t" \
  "v_smfmac_f32_16x16x64_bf16 v[148:151], v[140:143], v[104:111], v172\n\t" \
  "v_max3_u32 v164, v142, v143, v164\n\t" \
  "v_smfmac_f32_16x16x64_bf16 v[152:155], v[140:143], v[112:119], v172\n\t" \
  "v_cmp_le_u32 vcc, %[sent], v164\n\t" \
  "v_smfmac_f32_16x16x64_bf16 v[156:159], v[140:143], v[120:127], v172\n\t" \
  "buffer_load_dwordx4 v[188:191], v238, %[rt], %[st4] offen offset:1024 sc1\n\t" \
  "s_cbranch_vccnz .Lslow%=\n\t" \
  ".Lok%=:\n\t" \
  "v_add_f32_dpp v160, v144, v146 quad_perm:[0,1,2,3] row_mask:0x3 bank_mask:0xf\n\t" \
  "v_add_f32_dpp v161, v145, v147 quad_perm:[0,1,2,3] row_mask:0x3 bank_mask:0xf\n\t" \
  "v_add_f32_dpp v162, v148, v150 quad_perm:[0,1,2,3] row_mask:0x3 bank_mask:0xf\n\t" \
  "v_add_f32_dpp v163, v149, v151 quad_perm:[0,1,2,3] row_mask:0x3 bank_mask:0xf\n\t" \
  "v_add_f32_dpp v160, v152, v154 quad_perm:[0,1,2,3] row_mask:0xc bank_mask:0xf\n\t" \
  "v_add_f32_dpp v161, v153, v155 quad_perm:[0,1,2,3] row_mask:0xc bank_mask:0xf\n\t" \
  "v_add_f32_dpp v162, v156, v158 quad_perm:[0,1,2,3] row_mask:0xc bank_mask:0xf\n\t" \
  "v_add_f32_dpp v163, v157, v159 quad_perm:[0,1,2,3] row_mask:0xc bank_mask:0xf\n\t" \
  "s_nop 0\n\t" \
  "v_permlane32_swap_b32 v160, v162\n\t" \
  "v_permlane32_swap_b32 v161, v163\n\t" \
  "v_add_f32 v160, v160, v162\n\t" \
  "v_add_f32 v161, v161, v163\n\t" \
  "ds_write_b64 v169, v[160:161]\n\t" \
  "s_mov_b64 exec, 1\n\t" \
  "ds_inc_rtn_u32 v164, v168, v166 offset:4104\n\t" \
  "s_mov_b64 exec, -1\n\t" \
  "ds_read_b64 v[128:129], v170 offset:0\n\t" \
  "ds_read_b64 v[130:131], v170 offset:512\n\t" \
  "ds_read_b64 v[132:133], v170 offset:1024\n\t" \
  "ds_read_b64 v[134:135], v170 offset:1536\n\t" \
  "ds_read_b64 v[136:137], v170 offset:2048\n\t" \
  "ds_read_b64 v[138:139], v170 offset:2560\n\t" \
  "ds_read_b64 v[140:141], v170 offset:3072\n\t" \
  "ds_read_b64 v[142:143], v170 offset:3584\n\t" \
  "s_waitcnt lgkmcnt(8)\n\t" \
  "v_readfirstlane_b32 s93, v164\n\t" \
  "s_cmp_lg_u32 s93, 7\n\t" \
  "s_cbranch_scc1 .Lb1%=\n\t" \
  "s_waitcnt lgkmcnt(4)\n\t" \
  "v_pk_add_f32 v[128:129], v[128:129], v[130:131]\n\t" \
  "v_pk_add_f32 v[132:133], v[132:133], v[134:135]\n\t" \
  "s_waitcnt lgkmcnt(0)\n\t" \
  "v_pk_add_f32 v[136:137], v[136:137], v[138:139]\n\t" \
  "v_pk_add_f32 v[140:141], v[140:141], v[142:143]\n\t" \
  "v_pk_add_f32 v[128:129], v[128:129], v[132:133]\n\t" \
  "v_pk_add_f32 v[136:137], v[136:137], v[140:141]\n\t" \
  "v_pk_add_f32 v[128:129], v[128:129], v[136:137]\n\t" \
  "v_cvt_pk_bf16_f32 v128, v128, v129\n\t" \
  "s_nop 1\n\t" \
  "v_mov_b32_dpp v129, v128 quad_perm:[1,0,3,2] row_mask:0xf bank_mask:0xf\n\t" \
  "v_perm_b32 v128, v129, v128, v174\n\t" \
  "buffer_store_dword v128, v171, %[rs], %[sh] offen sc1\n\t" \
  "s_memtime s[90:91]\n\t" \
  "s_barrier\n\t" \
  "s_memtime s[76:77]\n\t" \
  "s_memrealtime s[78:79]\n\t" \
  "s_nop 3\n\t" \
  "s_memrealtime s[80:81]\n\t" \
  "s_nop 3\n\t" \
  "s_memrealtime s[82:83]\n\t" \
  "s_nop 3\n\t" \
  "s_memrealtime s[84:85]\n\t" \
  "s_nop 3\n\t" \
  "s_memrealtime s[86:87]\n\t" \
  "s_nop 3\n\t" \
  "s_memrealtime s[88:89]\n\t" \
  "s_mov_b32 s93, 0\n\t" \
  "s_waitcnt lgkmcnt(0)\n\t" \
  "s_cmp_eq_u32 s80, s78\n\t" \
  "s_addc_u32 s93, s93, 0\n\t" \
  "s_cmp_eq_u32 s82, s78\n\t" \
  "s_addc_u32 s93, s93, 0\n\t" \
  "s_cmp_eq_u32 s84, s78\n\t" \
  "s_addc_u32 s93, s93, 0\n\t" \
  "s_cmp_eq_u32 s86, s78\n\t" \
  "s_addc_u32 s93, s93, 0\n\t" \
  "s_cmp_eq_u32 s88, s78\n\t" \
  "s_addc_u32 s93, s93, 0\n\t" \
  "s_mul_i32 s93, s93, 21\n\t" \
  "s_add_u32 s76, s76, 15\n\t" \
  "s_add_u32 s76, s76, s93\n\t" \
  "s_lshl2_add_u32 s78, s78, 16\n\t" \
  "s_mov_b64 exec, 1\n\t" \
  "s_sub_i32 s90, s76, s90\n\t" \
  "s_mul_i32 s90, s90, 11900\n\t" \
  "s_ashr_i32 s90, s90, 16\n\t" \
  "s_sub_u32 s90, s78, s90\n\t" \
  "s_sub_u32 s90, s90, %[base]\n\t" \
  "v_mov_b32 v164, s90\n\t" \
  "ds_write_b32 v168, v164 offset:4108\n\t" \
  "s_mov_b64 exec, -1\n\t" \
  "s_waitcnt vmcnt(1)\n\t" \
  ".Lb2%=:\n\t" \
  "s_setprio 0\n\t" \
  "s_bitcmp0_b64 %[am], %[step]\n\t" \
  "s_cbranch_scc1 .La1%=\n\t" \
  "buffer_store_dwordx4 v[224:227], v228, %[ra], 0 offen sc1\n\t" \
  ".La1%=:\n\t" \
  "buffer_load_dwordx4 v[192:195], v239, %[rt], %[st] offen offset:0 sc1\n\t" \
  "buffer_load_dwordx4 v[196:199], v239, %[rt], %[st] offen offset:1024 sc1\n\t" \
  "v_mfma_f32_16x16x32_bf16 v[128:131], v[0:3], v[176:179], 0\n\t" \
  "v_mfma_f32_16x16x32_bf16 v[136:139], v[8:11], v[176:179], 0\n\t" \
  "ds_read2st64_b32 v[144:145], v231 offset0:0 offset1:8\n\t" \
  "ds_read2st64_b32 v[146:147], v231 offset0:16 offset1:24\n\t" \
  "ds_read2st64_b32 v[148:149], v231 offset0:32 offset1:40\n\t" \
  "ds_read2st64_b32 v[150:151], v231 offset0:48 offset1:56\n\t" \
  "buffer_load_dwordx4 v[200:203], v239, %[rt], %[st4] offen offset:0 sc1\n\t" \
  "buffer_load_dwordx4 v[204:207], v239, %[rt], %[st4] offen offset:1024 sc1\n\t" \
  "v_mfma_f32_16x16x32_bf16 v[132:135], v[4:7], v[180:183], 0\n\t" \
  "v_mfma_f32_16x16x32_bf16 v[140:143], v[12:15], v[180:183], 0\n\t" \
  "s_waitcnt lgkmcnt(0)\n\t" \
  "v_add_f32 v144, v144, v145\n\t" \
  "v_add_f32 v146, v146, v147\n\t" \
  "v_add_f32 v148, v148, v149\n\t" \
  "v_add_f32 v150, v150, v151\n\t" \
  "v_add_f32 v144, v144, v146\n\t" \
  "v_add_f32 v148, v148, v150\n\t" \
  "buffer_load_dwordx4 v[208:211], v240, %[rt], %[st] offen offset:0 sc1\n\t" \
  "buffer_load_dwordx4 v[212:215], v240, %[rt], %[st] offen offset:1024 sc1\n\t" \
  "v_mfma_f32_16x16x32_bf16 v[128:131], v[16:19], v[184:187], v[128:131]\n\t" \
  "v_mfma_f32_16x16x32_bf16 v[136:139], v[24:27], v[184:187], v[136:139]\n\t" \
  "v_add_f32 v144, v144, v148\n\t" \
  "v_cvt_pk_bf16_f32 v144, v144, v144\n\t" \
  "ds_write_b16 v232, v144\n\t" \
  "ds_read_b128 v[224:227], v229\n\t" \
  "v_add_u32 v228, %[ainc], v228\n\t" \
  "v_add_u32 v229, %[rinc], v229\n\t" \
  "v_add_u32 v232, 1024, v232\n\t" \
  "v_xor_b32 v231, 0x4000, v231\n\t" \
  "buffer_load_dwordx4 v[216:219], v240, %[rt], %[st4] offen offset:0 sc1\n\t" \
  "buffer_load_dwordx4 v[220:223], v240, %[rt], %[st4] offen offset:1024 sc1\n\t" \
  "v_mfma_f32_16x16x32_bf16 v[132:135], v[20:23], v[188:191], v[132:135]\n\t" \
  "v_mfma_f32_16x16x32_bf16 v[140:143], v[28:31], v[188:191], v[140:143]\n\t" \
  "buffer_load_dwordx4 v[176:179], v241, %[rt], %[st] offen offset:0 sc1\n\t" \
  "s_waitcnt vmcnt(8)\n\t" \
  "v_mfma_f32_16x16x32_bf16 v[128:131], v[32:35], v[192:195], v[128:131]\n\t" \
  "v_mfma_f32_16x16x32_bf16 v[136:139], v[40:43], v[192:195], v[136:139]\n\t" \
  "buffer_load_dwordx4 v[180:183], v241, %[rt], %[st] offen offset:1024 sc1\n\t" \
  "s_waitcnt vmcnt(8)\n\t" \
  "v_mfma_f32_16x16x32_bf16 v[132:135], v[36:39], v[196:199], v[132:135]\n\t" \
  "v_mfma_f32_16x16x32_bf16 v[140:143], v[44:47], v[196:199], v[140:143]\n\t" \
  "buffer_load_dwordx4 v[184:187], v241, %[rt], %[st4] offen offset:0 sc1\n\t" \
  "s_waitcnt vmcnt(8)\n\t" \
  "v_mfma_f32_16x16x32_bf16 v[128:131], v[48:51], v[200:203], v[128:131]\n\t" \
  "v_mfma_f32_16x16x32_bf16 v[136:139], v[56:59], v[200:203], v[136:139]\n\t" \
  "ds_read_b128 v[160:163], v168 offset:4096\n\t" \
  "buffer_load_dwordx4 v[188:191], v241, %[rt], %[st4] offen offset:1024 sc1\n\t" \
  "s_waitcnt vmcnt(8)\n\t" \
  "v_mfma_f32_16x16x32_bf16 v[132:135], v[52:55], v[204:207], v[132:135]\n\t" \
  "v_mfma_f32_16x16x32_bf16 v[140:143], v[60:63], v[204:207], v[140:143]\n\t" \
  "s_waitcnt lgkmcnt(0)\n\t" \
  "v_readfirstlane_b32 s90, v161\n\t" \
  "v_readfirstlane_b32 s91, v163\n\t" \
  "s_add_u32 s93, s90, %[rl]\n\t" \
  "s_add_u32 s93, s93, %[base]\n\t" \
  "s_add_u32 %[E], s93, %[lk]\n\t" \
  "s_sub_i32 s91, s91, s90\n\t" \
  "s_sub_i32 s91, s91, %[rl]\n\t" \
  "s_min_i32 s93, s91, %[up]\n\t" \
  "s_max_i32 s93, s93, %[dn]\n\t" \
  "s_cmp_gt_i32 s91, 60\n\t" \
  "s_cselect_b32 s93, 2, s93\n\t" \
  "s_add_i32 %[rl], %[rl], s93\n\t" \
  "s_waitcnt vmcnt(7)\n\t" \
  "v_mfma_f32_16x16x32_bf16 v[128:131], v[64:67], v[208:211], v[128:131]\n\t" \
  "v_mfma_f32_16x16x32_bf16 v[136:139], v[72:75], v[208:211], v[136:139]\n\t" \
  "s_add_u32 %[step], %[step], 1\n\t" \
  "s_lshl_b32 %[sh], %[step], 15\n\t" \
  "s_lshr_b32 s93, %[step], 4\n\t" \
  "s_lshr_b32 s93, %[pm], s93\n\t" \
  "s_and_b32 s93, s93, 4\n\t" \
  "s_lshl_b32 s93, s93, 11\n\t" \
  "s_lshl2_add_u32 %[so], %[sh], s93\n\t" \
  "s_waitcnt vmcnt(6)\n\t" \
  "v_mfma_f32_16x16x32_bf16 v[132:135], v[68:71], v[212:215], v[132:135]\n\t" \
  "v_mfma_f32_16x16x32_bf16 v[140:143], v[76:79], v[212:215], v[140:143]\n\t" \
  "s_sub_i32 s93, %[step], 2\n\t" \
  "s_lshl_b32 %[st], s93, 19\n\t" \
  "s_lshl_b32 s94, s93, 11\n\t" \
  "s_xor_b32 s94, s94, %[s0]\n\t" \
  "s_and_b32 s94, s94, 0x2000\n\t" \
  "s_or_b32 %[st], %[st], s94\n\t" \
  "s_cmp_lt_i32 s93, 0\n\t" \
  "s_cselect_b32 %[st], 0x70000000, %[st]\n\t" \
  "s_add_u32 %[st4], %[st], 4096\n\t" \
  "s_waitcnt vmcnt(5)\n\t" \
  "v_mfma_f32_16x16x32_bf16 v[128:131], v[80:83], v[216:219], v[128:131]\n\t" \
  "v_mfma_f32_16x16x32_bf16 v[136:139], v[88:91], v[216:219], v[136:139]\n\t" \
  "s_waitcnt vmcnt(4)\n\t" \
  "v_mfma_f32_16x16x32_bf16 v[132:135], v[84:87], v[220:223], v[132:135]\n\t" \
  "v_mfma_f32_16x16x32_bf16 v[140:143], v[92:95], v[220:223], v[140:143]\n\t" \
  "s_cmp_eq_u32 %[step], 63\n\t" \
  "s_cbranch_scc1 .Llast%=\n\t" \
  "s_movk_i32 s93, 600\n\t" \
  ".Lpacel%=:\n\t" \
  "s_memrealtime s[90:91]\n\t" \
  "s_waitcnt lgkmcnt(0)\n\t" \
  "s_lshl_b32 s90, s90, 2\n\t" \
  "s_sub_i32 s91, %[E], s90\n\t" \
  "s_cmp_le_i32 s91, 0\n\t" \
  "s_cbranch_scc1 .Lgol%=\n\t" \
  "s_sub_u32 s93, s93, 1\n\t" \
  "s_cmp_lg_u32 s93, 0\n\t" \
  "s_cbranch_scc1 .Lpacel%=\n\t" \
  ".Lgol%=:\n\t" \
  "buffer_load_dwordx2 v[166:167], v173, %[rs], 0 offen sc1\n\t" \
  "s_waitcnt vmcnt(4)\n\t" \
  "v_mfma_f32_16x16x32_bf16 v[128:131], v[96:99], v[176:179], v[128:131]\n\t" \
  "v_mfma_f32_16x16x32_bf16 v[136:139], v[104:107], v[176:179], v[136:139]\n\t" \
  "s_waitcnt vmcnt(3)\n\t" \
  "v_mfma_f32_16x16x32_bf16 v[132:135], v[100:103], v[180:183], v[132:135]\n\t" \
  "v_mfma_f32_16x16x32_bf16 v[140:143], v[108:111], v[180:183], v[140:143]\n\t" \
  "s_waitcnt vmcnt(2)\n\t" \
  "v_mfma_f32_16x16x32_bf16 v[128:131], v[112:115], v[184:187], v[128:131]\n\t" \
  "v_mfma_f32_16x16x32_bf16 v[136:139], v[120:123], v[184:187], v[136:139]\n\t" \
  "s_waitcnt vmcnt(1)\n\t" \
  "v_mfma_f32_16x16x32_bf16 v[132:135], v[116:119], v[188:191], v[132:135]\n\t" \
  "v_mfma_f32_16x16x32_bf16 v[140:143], v[124:127], v[188:191], v[140:143]\n\t" \
  "v_mov_b64 v[144:145], 0\n\t" \
  "v_mov_b64 v[146:147], 0\n\t" \
  "v_mov_b64 v[148:149], 0\n\t" \
  "v_mov_b64 v[150:151], 0\n\t" \
  "v_mov_b64 v[152:153], 0\n\t" \
  "v_mov_b64 v[154:155], 0\n\t" \
  "v_mov_b64 v[156:157], 0\n\t" \
  "v_mov_b64 v[158:159], 0\n\t" \
  "v_pk_add_f32 v[128:129], v[128:129], v[132:133]\n\t" \
  "v_pk_add_f32 v[130:131], v[130:131], v[134:135]\n\t" \
  "ds_write_b128 v230, v[128:131] offset:0\n\t" \
  "v_pk_add_f32 v[136:137], v[136:137], v[140:141]\n\t" \
  "v_pk_add_f32 v[138:139], v[138:139], v[142:143]\n\t" \
  "ds_write_b128 v230, v[136:139] offset:1024\n\t" \
  "v_xor_b32 v230, 0x4000, v230\n\t" \
  "s_cmp_lg_u32 %[step], 63\n\t" \
  "s_cbranch_scc1 .Lstep%=\n\t" \
  ".Lfc%=:\n\t" \
  "s_waitcnt vmcnt(0)\n\t" \
  "v_cmp_eq_u32 vcc, -1, v166\n\t" \
  "s_cbranch_vccnz .Lfw%=\n\t" \
  ".Lfo%=:\n\t" \
  "v_add_u32 v242, 16384, v242\n\t" \
  "buffer_load_dwordx4 v[176:179], v238, %[rt], %[st] offen offset:0 sc1\n\t" \
  "buffer_load_dwordx4 v[180:183], v238, %[rt], %[st] offen offset:1024 sc1\n\t" \
  "buffer_load_dwordx4 v[184:187], v238, %[rt], %[st4] offen offset:0 sc1\n\t" \
  "buffer_load_dwordx4 v[188:191], v238, %[rt], %[st4] offen offset:1024 sc1\n\t" \
  "buffer_load_dwordx4 v[192:195], v239, %[rt], %[st] offen offset:0 sc1\n\t" \
  "buffer_load_dwordx4 v[196:199], v239, %[rt], %[st] offen offset:1024 sc1\n\t" \
  "buffer_load_dwordx4 v[200:203], v239, %[rt], %[st4] offen offset:0 sc1\n\t" \
  "buffer_load_dwordx4 v[204:207], v239, %[rt], %[st4] offen offset:1024 sc1\n\t" \
  "v_subrev_u32 v230, 98304, v230\n\t" \
  ".Lt%=:\n\t" \
  "s_waitcnt vmcnt(7)\n\t" \
  "v_mfma_f32_16x16x32_bf16 v[128:131], v[0:3], v[176:179], 0\n\t" \
  "v_mfma_f32_16x16x32_bf16 v[136:139], v[8:11], v[176:179], 0\n\t" \
  "buffer_load_dwordx4 v[176:179], v240, %[rt], %[st] offen offset:0 sc1\n\t" \
  "buffer_load_dword v166, v242, %[rs], 0 offen sc1\n\t" \
  "s_waitcnt vmcnt(8)\n\t" \
  "v_mfma_f32_16x16x32_bf16 v[132:135], v[4:7], v[180:183], 0\n\t" \
  "v_mfma_f32_16x16x32_bf16 v[140:143], v[12:15], v[180:183], 0\n\t" \
  "buffer_load_dwordx4 v[180:183], v240, %[rt], %[st] offen offset:1024 sc1\n\t" \
  "s_waitcnt vmcnt(8)\n\t" \
  "v_mfma_f32_16x16x32_bf16 v[128:131], v[16:19], v[184:187], v[128:131]\n\t" \
  "v_mfma_f32_16x16x32_bf16 v[136:139], v[24:27], v[184:187], v[136:139]\n\t" \
  "buffer_load_dwordx4 v[184:187], v240, %[rt], %[st4] offen offset:0 sc1\n\t" \
  "s_waitcnt vmcnt(8)\n\t" \
  "v_mfma_f32_16x16x32_bf16 v[132:135], v[20:23], v[188:191], v[132:135]\n\t" \
  "v_mfma_f32_16x16x32_bf16 v[140:143], v[28:31], v[188:191], v[140:143]\n\t" \
  "buffer_load_dwordx4 v[188:191], v240, %[rt], %[st4] offen offset:1024 sc1\n\t" \
  "s_waitcnt vmcnt(8)\n\t" \
  "v_mfma_f32_16x16x32_bf16 v[128:131], v[32:35], v[192:195], v[128:131]\n\t" \
  "v_mfma_f32_16x16x32_bf16 v[136:139], v[40:43], v[192:195], v[136:139]\n\t" \
  "buffer_load_dwordx4 v[192:195], v241, %[rt], %[st] offen offset:0 sc1\n\t" \
  "s_waitcnt vmcnt(8)\n\t" \
  "v_mfma_f32_16x16x32_bf16 v[132:135], v[36:39], v[196:199], v[132:135]\n\t" \
  "v_mfma_f32_16x16x32_bf16 v[140:143], v[44:47], v[196:199], v[140:143]\n\t" \
  "buffer_load_dwordx4 v[196:199], v241, %[rt], %[st] offen offset:1024 sc1\n\t" \
  "s_waitcnt vmcnt(8)\n\t" \
  "v_mfma_f32_16x16x32_bf16 v[128:131], v[48:51], v[200:203], v[128:131]\n\t" \
  "v_mfma_f32_16x16x32_bf16 v[136:139], v[56:59], v[200:203], v[136:139]\n\t" \
  "buffer_load_dwordx4 v[200:203], v241, %[rt], %[st4] offen offset:0 sc1\n\t" \
  "s_waitcnt vmcnt(8)\n\t" \
  "v_mfma_f32_16x16x32_bf16 v[132:135], v[52:55], v[204:207], v[132:135]\n\t" \
  "v_mfma_f32_16x16x32_bf16 v[140:143], v[60:63], v[204:207], v[140:143]\n\t" \
  "buffer_load_dwordx4 v[204:207], v241, %[rt], %[st4] offen offset:1024 sc1\n\t" \
  "s_waitcnt vmcnt(8)\n\t" \
  "v_mfma_f32_16x16x32_bf16 v[128:131], v[64:67], v[176:179], v[128:131]\n\t" \
  "v_mfma_f32_16x16x32_bf16 v[136:139], v[72:75], v[176:179], v[136:139]\n\t" \
  "s_waitcnt vmcnt(7)\n\t" \
  ".Ltc%=:\n\t" \
  "v_cmp_eq_u32 vcc, -1, v166\n\t" \
  "s_cbranch_vccnz .Ltw%=\n\t" \
  ".Lto%=:\n\t" \
  "s_cmp_eq_u32 %[step], 65\n\t" \
  "s_cbranch_scc1 .Ltl%=\n\t" \
  "s_add_u32 %[step], %[step], 1\n\t" \
  "s_sub_i32 s93, %[step], 2\n\t" \
  "s_lshl_b32 %[st], s93, 19\n\t" \
  "s_lshl_b32 s94, s93, 11\n\t" \
  "s_xor_b32 s94, s94, %[s0]\n\t" \
  "s_and_b32 s94, s94, 0x2000\n\t" \
  "s_or_b32 %[st], %[st], s94\n\t" \
  "s_cmp_lt_i32 s93, 0\n\t" \
  "s_cselect_b32 %[st], 0x70000000, %[st]\n\t" \
  "s_add_u32 %[st4], %[st], 4096\n\t" \
  "v_add_u32 v242, 16384, v242\n\t" \
  "s_cmp_eq_u32 %[step], 65\n\t" \
  "s_cbranch_scc0 .Ltn%=\n\t" \
  "v_mov_b32 v242, %[fg]\n\t" \
  ".Ltn%=:\n\t" \
  "buffer_load_dwordx4 v[176:179], v238, %[rt], %[st] offen offset:0 sc1\n\t" \
  "s_waitcnt vmcnt(7)\n\t" \
  "v_mfma_f32_16x16x32_bf16 v[132:135], v[68:71], v[180:183], v[132:135]\n\t" \
  "v_mfma_f32_16x16x32_bf16 v[140:143], v[76:79], v[180:183], v[140:143]\n\t" \
  "buffer_load_dwordx4 v[180:183], v238, %[rt], %[st] offen offset:1024 sc1\n\t" \
  "s_waitcnt vmcnt(7)\n\t" \
  "v_mfma_f32_16x16x32_bf16 v[128:131], v[80:83], v[184:187], v[128:131]\n\t" \
  "v_mfma_f32_16x16x32_bf16 v[136:139], v[88:91], v[184:187], v[136:139]\n\t" \
  "buffer_load_dwordx4 v[184:187], v238, %[rt], %[st4] offen offset:0 sc1\n\t" \
  "s_waitcnt vmcnt(7)\n\t" \
  "v_mfma_f32_16x16x32_bf16 v[132:135], v[84:87], v[188:191], v[132:135]\n\t" \
  "v_mfma_f32_16x16x32_bf16 v[140:143], v[92:95], v[188:191], v[140:143]\n\t" \
  "buffer_load_dwordx4 v[188:191], v238, %[rt], %[st4] offen offset:1024 sc1\n\t" \
  "s_waitcnt vmcnt(7)\n\t" \
  "v_mfma_f32_16x16x32_bf16 v[128:131], v[96:99], v[192:195], v[128:131]\n\t" \
  "v_mfma_f32_16x16x32_bf16 v[136:139], v[104:107], v[192:195], v[136:139]\n\t" \
  "buffer_load_dwordx4 v[192:195], v239, %[rt], %[st] offen offset:0 sc1\n\t" \
  "s_waitcnt vmcnt(7)\n\t" \
  "v_mfma_f32_16x16x32_bf16 v[132:135], v[100:103], v[196:199], v[132:135]\n\t" \
  "v_mfma_f32_16x16x32_bf16 v[140:143], v[108:111], v[196:199], v[140:143]\n\t" \
  "buffer_load_dwordx4 v[196:199], v239, %[rt], %[st] offen offset:1024 sc1\n\t" \
  "s_waitcnt vmcnt(7)\n\t" \
  "v_mfma_f32_16x16x32_bf16 v[128:131], v[112:115], v[200:203], v[128:131]\n\t" \
  "v_mfma_f32_16x16x32_bf16 v[136:139], v[120:123], v[200:203], v[136:139]\n\t" \
  "buffer_load_dwordx4 v[200:203], v239, %[rt], %[st4] offen offset:0 sc1\n\t" \
  "s_waitcnt vmcnt(7)\n\t" \
  "v_mfma_f32_16x16x32_bf16 v[132:135], v[116:119], v[204:207], v[132:135]\n\t" \
  "v_mfma_f32_16x16x32_bf16 v[140:143], v[124:127], v[204:207], v[140:143]\n\t" \
  "buffer_load_dwordx4 v[204:207], v239, %[rt], %[st4] offen offset:1024 sc1\n\t" \
  "s_nop 5\n\t" \
  "v_pk_add_f32 v[128:129], v[128:129], v[132:133]\n\t" \
  "v_pk_add_f32 v[130:131], v[130:131], v[134:135]\n\t" \
  "ds_write_b128 v230, v[128:131] offset:0\n\t" \
  "v_pk_add_f32 v[136:137], v[136:137], v[140:141]\n\t" \
  "v_pk_add_f32 v[138:139], v[138:139], v[142:143]\n\t" \
  "ds_write_b128 v230, v[136:139] offset:1024\n\t" \
  "v_add_u32 v230, 16384, v230\n\t" \
  "s_branch .Lt%=\n\t" \
  ".Ltl%=:\n\t" \
  "buffer_load_dwordx4 v[224:227], v243, %[rg], 0 offen sc1\n\t" \
  "s_waitcnt vmcnt(7)\n\t" \
  "v_mfma_f32_16x16x32_bf16 v[132:135], v[68:71], v[180:183], v[132:135]\n\t" \
  "v_mfma_f32_16x16x32_bf16 v[140:143], v[76:79], v[180:183], v[140:143]\n\t" \
  "s_waitcnt vmcnt(6)\n\t" \
  "v_mfma_f32_16x16x32_bf16 v[128:131], v[80:83], v[184:187], v[128:131]\n\t" \
  "v_mfma_f32_16x16x32_bf16 v[136:139], v[88:91], v[184:187], v[136:139]\n\t" \
  "s_waitcnt vmcnt(5)\n\t" \
  "v_mfma_f32_16x16x32_bf16 v[132:135], v[84:87], v[188:191], v[132:135]\n\t" \
  "v_mfma_f32_16x16x32_bf16 v[140:143], v[92:95], v[188:191], v[140:143]\n\t" \
  "s_waitcnt vmcnt(4)\n\t" \
  "v_mfma_f32_16x16x32_bf16 v[128:131], v[96:99], v[192:195], v[128:131]\n\t" \
  "v_mfma_f32_16x16x32_bf16 v[136:139], v[104:107], v[192:195], v[136:139]\n\t" \
  "s_waitcnt vmcnt(3)\n\t" \
  "v_mfma_f32_16x16x32_bf16 v[132:135], v[100:103], v[196:199], v[132:135]\n\t" \
  "v_mfma_f32_16x16x32_bf16 v[140:143], v[108:111], v[196:199], v[140:143]\n\t" \
  "s_waitcnt vmcnt(2)\n\t" \
  "v_mfma_f32_16x16x32_bf16 v[128:131], v[112:115], v[200:203], v[128:131]\n\t" \
  "v_mfma_f32_16x16x32_bf16 v[136:139], v[120:123], v[200:203], v[136:139]\n\t" \
  "s_waitcnt vmcnt(1)\n\t" \
  "v_mfma_f32_16x16x32_bf16 v[132:135], v[116:119], v[204:207], v[132:135]\n\t" \
  "v_mfma_f32_16x16x32_bf16 v[140:143], v[124:127], v[204:207], v[140:143]\n\t" \
  "s_nop 6\n\t" \
  "v_pk_add_f32 v[128:129], v[128:129], v[132:133]\n\t" \
  "v_pk_add_f32 v[130:131], v[130:131], v[134:135]\n\t" \
  "ds_write_b128 v230, v[128:131] offset:0\n\t" \
  "v_pk_add_f32 v[136:137], v[136:137], v[140:141]\n\t" \
  "v_pk_add_f32 v[138:139], v[138:139], v[142:143]\n\t" \
  "ds_write_b128 v230, v[136:139] offset:1024\n\t" \
  "s_waitcnt vmcnt(0) lgkmcnt(0)\n\t" \
  "s_branch .Lend%=\n\t" \
  ".Ltw%=:\n\t" \
  "s_add_u32 s92, s92, 1\n\t" \
  "s_sleep 2\n\t" \
  "s_and_b32 s93, s92, 63\n\t" \
  "s_cbranch_scc1 .Ltr%=\n\t" \
  "buffer_load_dword v164, off, %[rs], %[sab] sc1\n\t" \
  "s_waitcnt vmcnt(0)\n\t" \
  "v_readfirstlane_b32 s93, v164\n\t" \
  "s_nop 3\n\t" \
  "s_cmp_eq_u32 s93, 1\n\t" \
  "s_cbranch_scc1 .Lto%=\n\t" \
  "s_cmp_lt_u32 s92, 0x80000\n\t" \
  "s_cbranch_scc1 .Ltr%=\n\t" \
  "v_mov_b32 v164, 1\n\t" \
  "buffer_store_dword v164, off, %[rs], %[sab] sc1\n\t" \
  "s_waitcnt vmcnt(0)\n\t" \
  "s_branch .Lto%=\n\t" \
  ".Ltr%=:\n\t" \
  "buffer_load_dword v166, v242, %[rs], 0 offen sc1\n\t" \
  "s_waitcnt vmcnt(0)\n\t" \
  "s_branch .Ltc%=\n\t" \
  ".Llast%=:\n\t" \
  "v_mov_b32 v173, v242\n\t" \
  "s_branch .Lgol%=\n\t" \
  ".Lfw%=:\n\t" \
  "s_add_u32 s92, s92, 1\n\t" \
  "s_sleep 2\n\t" \
  "s_and_b32 s93, s92, 63\n\t" \
  "s_cbranch_scc1 .Lfr%=\n\t" \
  "buffer_load_dword v164, off, %[rs], %[sab] sc1\n\t" \
  "s_waitcnt vmcnt(0)\n\t" \
  "v_readfirstlane_b32 s93, v164\n\t" \
  "s_nop 3\n\t" \
  "s_cmp_eq_u32 s93, 1\n\t" \
  "s_cbranch_scc1 .Lfo%=\n\t" \
  "s_cmp_lt_u32 s92, 0x80000\n\t" \
  "s_cbranch_scc1 .Lfr%=\n\t" \
  "v_mov_b32 v164, 1\n\t" \
  "buffer_store_dword v164, off, %[rs], %[sab] sc1\n\t" \
  "s_waitcnt vmcnt(0)\n\t" \
  "s_branch .Lfo%=\n\t" \
  ".Lfr%=:\n\t" \
  "buffer_load_dwordx2 v[166:167], v173, %[rs], 0 offen sc1\n\t" \
  "s_branch .Lfc%=\n\t" \
  ".Lb1%=:\n\t" \
  "s_waitcnt vmcnt(0) lgkmcnt(0)\n\t" \
  "s_barrier\n\t" \
  "s_branch .Lb2%=\n\t" \
  ".Lbl%=:\n\t" \
  "s_bitset1_b64 %[ps], %[step]\n\t" \
  "s_cmp_lt_u32 s92, 3\n\t" \
  "s_cbranch_scc1 .Lbm%=\n\t" \
  "s_sleep 12\n\t" \
  ".Lbm%=:\n\t" \
  "s_add_u32 s92, s92, 1\n\t" \
  "s_sleep 2\n\t" \
  "s_and_b32 s93, s92, 63\n\t" \
  "s_cbranch_scc1 .Lba%=\n\t" \
  "buffer_load_dword v164, off, %[rs], %[sab] sc1\n\t" \
  "s_waitcnt vmcnt(0)\n\t" \
  "v_readfirstlane_b32 s93, v164\n\t" \
  "s_nop 3\n\t" \
  "s_cmp_eq_u32 s93, 1\n\t" \
  "s_cbranch_scc1 .Lgo%=\n\t" \
  "s_cmp_lt_u32 s92, 0x80000\n\t" \
  "s_cbranch_scc1 .Lba%=\n\t" \
  "v_mov_b32 v164, 1\n\t" \
  "buffer_store_dword v164, off, %[rs], %[sab] sc1\n\t" \
  "s_waitcnt vmcnt(0)\n\t" \
  "s_branch .Lgo%=\n\t" \
  ".Lba%=:\n\t" \
  "buffer_load_dwordx2 v[166:167], v173, %[rs], 0 offen sc1\n\t" \
  "s_branch .Lcal%=\n\t" \
  ".Lslow%=:\n\t" \
  "s_mov_b64 s[94:95], 0\n\t" \
  "v_cmp_eq_u32 vcc, %[sent], v128\n\t" \
  "s_or_b64 s[94:95], s[94:95], vcc\n\t" \
  "v_cmp_eq_u32 vcc, %[sent], v129\n\t" \
  "s_or_b64 s[94:95], s[94:95], vcc\n\t" \
  "v_cmp_eq_u32 vcc, %[sent], v130\n\t" \
  "s_or_b64 s[94:95], s[94:95], vcc\n\t" \
  "v_cmp_eq_u32 vcc, %[sent], v131\n\t" \
  "s_or_b64 s[94:95], s[94:95], vcc\n\t" \
  "v_cmp_eq_u32 vcc, %[sent], v132\n\t" \
  "s_or_b64 s[94:95], s[94:95], vcc\n\t" \
  "v_cmp_eq_u32 vcc, %[sent], v133\n\t" \
  "s_or_b64 s[94:95], s[94:95], vcc\n\t" \
  "v_cmp_eq_u32 vcc, %[sent], v134\n\t" \
  "s_or_b64 s[94:95], s[94:95], vcc\n\t" \
  "v_cmp_eq_u32 vcc, %[sent], v135\n\t" \
  "s_or_b64 s[94:95], s[94:95], vcc\n\t" \
  "v_cmp_eq_u32 vcc, %[sent], v136\n\t" \
  "s_or_b64 s[94:95], s[94:95], vcc\n\t" \
  "v_cmp_eq_u32 vcc, %[sent], v137\n\t" \
  "s_or_b64 s[94:95], s[94:95], vcc\n\t" \
  "v_cmp_eq_u32 vcc, %[sent], v138\n\t" \
  "s_or_b64 s[94:95], s[94:95], vcc\n\t" \
  "v_cmp_eq_u32 vcc, %[sent], v139\n\t" \
  "s_or_b64 s[94:95], s[94:95], vcc\n\t" \
  "v_cmp_eq_u32 vcc, %[sent], v140\n\t" \
  "s_or_b64 s[94:95], s[94:95], vcc\n\t" \
  "v_cmp_eq_u32 vcc, %[sent], v141\n\t" \
  "s_or_b64 s[94:95], s[94:95], vcc\n\t" \
  "v_cmp_eq_u32 vcc, %[sent], v142\n\t" \
  "s_or_b64 s[94:95], s[94:95], vcc\n\t" \
  "v_cmp_eq_u32 vcc, %[sent], v143\n\t" \
  "s_or_b64 s[94:95], s[94:95], vcc\n\t" \
  "s_cbranch_scc0 .Lok%=\n\t" \
  "s_bitset1_b64 %[fm], %[step]\n\t" \
  "s_add_u32 s92, s92, 1\n\t" \
  "s_sleep 1\n\t" \
  "s_and_b32 s93, s92, 63\n\t" \
  "s_cbranch_scc1 .Lre%=\n\t" \
  "buffer_load_dword v164, off, %[rs], %[sab] sc1\n\t" \
  "s_waitcnt vmcnt(0)\n\t" \
  "v_readfirstlane_b32 s93, v164\n\t" \
  "s_nop 3\n\t" \
  "s_cmp_eq_u32 s93, 1\n\t" \
  "s_cbranch_scc1 .Lok%=\n\t" \
  "s_cmp_lt_u32 s92, 0x80000\n\t" \
  "s_cbranch_scc1 .Lre%=\n\t" \
  "v_mov_b32 v164, 1\n\t" \
  "buffer_store_dword v164, off, %[rs], %[sab] sc1\n\t" \
  "s_waitcnt vmcnt(0)\n\t" \
  "s_branch .Lok%=\n\t" \
  ".Lre%=:\n\t" \
  "buffer_load_dwordx4 v[128:131], v234, %[rs], %[so] offen sc1\n\t" \
  "s_nop 15\n\t" \
  "buffer_load_dwordx4 v[132:135], v235, %[rs], %[so] offen sc1\n\t" \
  "s_nop 15\n\t" \
  "buffer_load_dwordx4 v[136:139], v236, %[rs], %[so] offen sc1\n\t" \
  "s_nop 15\n\t" \
  "buffer_load_dwordx4 v[140:143], v237, %[rs], %[so] offen sc1\n\t" \
  "v_mov_b64 v[144:145], 0\n\t" \
  "v_mov_b64 v[146:147], 0\n\t" \
  "v_mov_b64 v[148:149], 0\n\t" \
  "v_mov_b64 v[150:151], 0\n\t" \
  "v_mov_b64 v[152:153], 0\n\t" \
  "v_mov_b64 v[154:155], 0\n\t" \
  "v_mov_b64 v[156:157], 0\n\t" \
  "v_mov_b64 v[158:159], 0\n\t" \
  "s_waitcnt vmcnt(0)\n\t" \
  "s_branch .Lw%=\n\t" \
  ".Lend%=:\n\t"

typedef __attribute__((ext_vector_type(32))) unsigned u32x32;
__device__ __forceinline__ void put4(u32x32& W, int i, bf16x8 f) {
  const u32x4 v = __builtin_bit_cast(u32x4, f);
#pragma unroll
  for (int j = 0; j < 4; ++j) W[4 * i + j] = v[j];
}
__device__ __forceinline__ bf16x8 get4(const u32x32& W, int i) {
  return as_bf16x8((u32x4){W[4 * i], W[4 * i + 1], W[4 * i + 2], W[4 * i + 3]});
}
__device__ __forceinline__ int look_prog(__amdgpu_buffer_rsrc_t rs, int off) {
  asm volatile("" ::: "memory");
  const int v = (int)ld_flag(rs, off);
  return min(__builtin_amdgcn_readlane(v, 0), __builtin_amdgcn_readlane(v, 1));
}
__device__ __forceinline__ int wait_prog(__amdgpu_buffer_rsrc_t rs, int off, int need, gu32* abortw) {
  int v;
  for (unsigned spins = 0;;) {
    v = look_prog(rs, off);
    if (v >= need) break;
    __builtin_amdgcn_s_sleep(4);
    if (spin_fail(spins, abortw)) break;
  }
  asm volatile("" ::: "memory");
  return v;
}
#define CLB8(a, b, c, d, e, f, g, h) "v" #a, "v" #b, "v" #c, "v" #d, "v" #e, "v" #f, "v" #g, "v" #h
#define CLB_S "s90", "s91", "s92", "s93", "s94", "s95", "vcc", "scc", "memory"
#define W_IN "{v[0:31]}"(W[0]), "{v[32:63]}"(W[1]), "{v[64:95]}"(W[2]), "{v[96:127]}"(W[3]), "{v[128:159]}"(W[4]), "{v[160:191]}"(W[5])

#define W_DELAY 60
#define LEAD0 378
#define LEAD_D0 6
#define QBJ 3
constexpr int L_MEM = 0, L_KT = 16384, L_QX = 24576, L_KX = 26624, L_VX = 16384, L_MISC = 30720, L_WL = 32768;

#define QK_STEP(TXT)                                                                                                        \
  asm volatile(TXT                                                                                                          \
      : [tout] "=&s"(tout), [spins] "=&s"(spins), [tb1] "=&s"(tb1), [tgt] "=&s"(tgt), "+{v248}"(aL16), "+{v249}"(aMW), "+{v250}"(aWL),                            \
        "+{v251}"(aIDX), "+{v252}"(aQW), "+{v[232:235]}"(QF)                                                                 \
      : W_IN, [rs] "s"(rs4), [som] "s"(o_mem(pm, s) + WV * 1024), [som2] "s"(o_mem(pm, s) + WV * 1024 + 16384), [sent] "s"(DSENT), [sab] "s"(SY_ABORT), \
        [lead] "s"(lead), [step] "s"(s), [sbc] "s"(SY_BA + side(pm, 1) + h * 128), [nh] "s"(0xFF00FF00FF00FF00ull), [up] "s"(0xFFFFFFFF00000000ull), [negc] "s"(-cexp),      \
        [c] "s"(cexp), [so] "s"(o_o(pm, s, h) + WV * 512), [sk] "s"(o_v(pm, s, h) + 2048 + (WV & 3) * 512),                    \
        [sv] "s"(o_v(pm, s, h) + WV * 512), [rt] "s"(rt4), [svt] "s"(t_off(p, T_V + kseg(s, h)) + WV * 2048)                  \
      : CLB8(192, 193, 194, 195, 196, 197, 198, 199), CLB8(200, 201, 202, 203, 204, 205, 206, 207),                          \
        CLB8(208, 209, 210, 211, 212, 213, 214, 215), CLB8(216, 217, 218, 219, 220, 221, 222, 223),                          \
        CLB8(224, 225, 226, 227, 228, 229, 230, 231), CLB8(236, 237, 238, 239, 240, 241, 242, 243), "v244", "v245", "v246", "v247", "v253", CLB_S)

template <int WV>
__device__ __forceinline__ void qk_wave(Args& p, int h, char* smem) {
  const int tid = threadIdx.x;
  unsigned pm = 0;
  int have = -1;
  u32x32 W[6];
  {
    int lane = tid & 63;
    asm volatile("" : "+v"(lane));
    const unsigned vo = (WV * 4 + (lane >> 4)) * (HD * 2) + 16 * ((lane & 15) ^ swzB((WV * 4 + (lane >> 4)) & 15));
    auto stage = [&](int j) {
#pragma unroll
      for (int mm = 0; mm < 2; ++mm) {
        const char* src = (const char*)((mm ? p.Wk : p.Wq) + (size_t)j * 128 * HD + h * DH);
        const unsigned dst = lds_off(smem) + (~j & 1) * 65536 + mm * 32768 + WV * 1024;
        glds2(src, 32 * HD * 2, vo, dst);
        glds2(src + 64 * HD * 2, 32 * HD * 2, vo, dst + 16384);
      }
    };
    bf16x8 keep[4][4];
    auto place = [&](int g) {
#pragma unroll
      for (int i = 0; i < 4; ++i)
        *reinterpret_cast<bf16x8*>(smem + (g == 0 ? 65536 : g == 1 ? 32768 : g == 2 ? 131072 : 98304) + WV * 4096 + i * 1024 + lane * 16) = keep[g][i];
    };
    stage(0); stage(1);
    Boot bt;
#pragma unroll
    for (int j = 0; j < 8; ++j) {
      if (j < 7) WAIT_V(8); else WAIT_V(0);
      if (WV == 7 && j == QBJ + 1) pm = boot_done(p.ws, bt);
      LBAR();
#pragma unroll
      for (int k = 0; k < 8; ++k) {
        const int P = (QK_MAP[WV] >> (4 * (2 * j + (k >> 2)))) & 15;
        const bf16x8 fr = ld_bfrag(smem + (~j & 1) * 65536 + (WV >> 2) * 32768 + (k >> 2) * 16384, k & 1, 2 * (WV & 3) + ((k >> 1) & 1), lane);
        const int f = P * 4 + (k & 3);
        if (P < 12) put4(W[f >> 3], f & 7, fr);
        else keep[P - 12][k & 3] = fr;
      }
      if (j == (WV >> 1)) place(2);
      LBAR();
      if (WV == 7 && j == QBJ) boot_ask(p.ws, bt);
      if (j + 2 < 8) stage(j + 2);
      if (j == 6) { place(0); place(3); }
    }
    place(1);
    if (WV == 7 && lane < 8) lds_at(smem + L_MISC)[lane] = lane == 7 ? pm : (lane == 1 ? now_q() : 0);
    if (WV == 6) lds_at(smem + 28672)[lane] = ~0u;
    LBAR();
    pm = uni(lds_at(smem + L_MISC)[7]);
    p.s0 = pm >> 31;
    if (WV == 6) RDW(h);
    if (WV == 4 || WV == 5) {
      have = wait_prog(mkrsrc(p.ws + WS_SY, SY_TOTAL), fkq(h, (lane & 1) * 2), 0, (gu32*)(p.ws + WS_SY + SY_ABORT));
      if (WV == 4) RDW(768 + h);
      const char* src = p.ws + WS_T + t_off(p, kseg(0, h)) + (WV - 4) * 4096 + lane * 16;
#pragma unroll
      for (int i = 0; i < 4; ++i) glds16_sc1(src + i * 1024, smem + L_KT + (WV - 4) * 4096 + i * 1024);
    }
  }
  const __amdgpu_buffer_rsrc_t rsy = mkrsrc(p.ws + WS_SY, SY_TOTAL);
  const u32x4 rs4 = raw_rsrc(p.ws + WS_SY, SY_TOTAL);
  const u32x4 rt4 = raw_rsrc(p.ws + WS_T, T_BYTES);
  gu32* abortw = (gu32*)(p.ws + WS_SY + SY_ABORT);
  const float cexp = 0.08838834764831845f * 1.4426950408889634f;
  constexpr int m = WV >> 2, b = WV & 3;
  int lead = LEAD0;
  unsigned tb1 = 0;
  unsigned long long fm = 0, ps = 0; (void)fm; (void)ps;
  for (int s = 0; s < NSEG; ++s) {
    int lane = tid & 63;
    asm volatile("" : "+v"(lane));
    const int r = lane & 15, q = lane >> 4;
    unsigned aL16 = lane * 16, aMW = aL16 + WV * 1024, aWL = aL16 + 98304 + WV * 4096;
    unsigned aIDX = (lane & 2) ? 0xEEEE : 0x4444;
    unsigned aQW = (32 * b + r) * 16 + q * 4 + (m ? L_KX : L_QX);
    u32x4 QF;
    asm volatile("" : "={v[232:235]}"(QF));
    if (WV < 4) {
      QF[0] = L_QX + (q * 8 + (r >> 2)) * 16 + (r & 1) * 8;
      QF[1] = ((2 * (q & 1) + (q >> 1)) * 16 + r) * 4;
      QF[2] = ((r >> 2) & 1) * 65536 + ((r >> 1) & 1) * 64 + (r >> 3) * 32 + (r & 1) * 16 + q * 4;
    } else QF[3] = r * 16 + q * 4;
    unsigned tout, spins, tgt;
    if (WV == 0) QK_STEP(QK_ASM_0); else if (WV == 1) QK_STEP(QK_ASM_1); else if (WV == 2) QK_STEP(QK_ASM_2);
    else if (WV == 3) QK_STEP(QK_ASM_3); else if (WV == 4) QK_STEP(QK_ASM_4); else if (WV == 5) QK_STEP(QK_ASM_5);
    else if (WV == 6) QK_STEP(QK_ASM_6); else QK_STEP(QK_ASM_7);
    tout = __builtin_amdgcn_readfirstlane(tout);
    tb1 = __builtin_amdgcn_readfirstlane(tb1);
    lane = tid & 63;
    asm volatile("" : "+v"(lane));
    if (WV < 4) {
      lds_at(smem + L_MISC + 8)[WV] = s + 1;
      if (WV == 3) lds_at(smem + L_MISC)[6] = time_of(tout);
      if (WV == 2 && s < 2) RDW(800 + s * 32 + h);
    }
    if (WV >= 4 && s + 1 < NSEG) {
      unsigned t;
      do {
        __builtin_amdgcn_s_sleep(1);
        t = lds_at(smem + L_MISC + 8)[lane & 3];
      } while (!__all(t == (unsigned)(s + 1)));
      if (WV <= 5) {
        if (s + 2 < NSEG) {
          if (have < s + 1) have = wait_prog(rsy, fkq(h, (lane & 1) * 2), s + 1, abortw);
          const char* src = p.ws + WS_T + t_off(p, kseg(s + 1, h)) + (WV - 4) * 4096 + lane * 16;
#pragma unroll
          for (int i = 0; i < 4; ++i) glds16_sc1(src + i * 1024, smem + L_KT + (WV - 4) * 4096 + i * 1024);
          if (have < s + 2) have = look_prog(rsy, fkq(h, (lane & 1) * 2));
        }
      } else if (WV == 6) {
        watch(rsy, s, lane, now_q() + W_DELAY, (const unsigned*)(smem + L_MISC), abortw, side(pm, 1));
      } else {
        __builtin_amdgcn_s_sleep(6);
        const int x = (int)(__builtin_amdgcn_readfirstlane(lds_at(smem + L_MISC)[6]) - tb1);
        lead = __builtin_amdgcn_readfirstlane(track(lead, x, LEAD_UP, s < 8 ? LEAD_D0 : LEAD_DN, 60));
        SMP(2, s, x); SMP(3, s, lead);
      }
    }
  }
}

__device__ __forceinline__ void qk_role(Args& p, int h, char* smem) {
  switch (__builtin_amdgcn_readfirstlane(threadIdx.x >> 6)) {
    case 0: qk_wave<0>(p, h, smem); break;
    case 1: qk_wave<1>(p, h, smem); break;
    case 2: qk_wave<2>(p, h, smem); break;
    case 3: qk_wave<3>(p, h, smem); break;
    case 4: qk_wave<4>(p, h, smem); break;
    case 5: qk_wave<5>(p, h, smem); break;
    case 6: qk_wave<6>(p, h, smem); break;
    default: qk_wave<7>(p, h, smem); break;
  }
}

#define CLB32(a) CLB32_##a
#define CLB32_128 CLB8(128, 129, 130, 131, 132, 133, 134, 135), CLB8(136, 137, 138, 139, 140, 141, 142, 143), CLB8(144, 145, 146, 147, 148, 149, 150, 151), CLB8(152, 153, 154, 155, 156, 157, 158, 159)
#define CLB32_160 CLB8(160, 161, 162, 163, 164, 165, 166, 167), CLB8(168, 169, 170, 171, 172, 173, 174, 175), CLB8(176, 177, 178, 179, 180, 181, 182, 183), CLB8(184, 185, 186, 187, 188, 189, 190, 191)
#define W4_IN "{v[0:31]}"(W[0]), "{v[32:63]}"(W[1]), "{v[64:95]}"(W[2]), "{v[96:127]}"(W[3])
#define K_DUE 160
#define T_SL 2
#define O_DUE 400
#define LEAD_V 200

constexpr int V_VM = 16384, V_T = 32768, V_KI = 131072;
__device__ __forceinline__ void vu_role(Args& p, int h, char* smem) {
  const int tid = threadIdx.x, lane = tid & 63;
  const int w = __builtin_amdgcn_readfirstlane(tid >> 6);
  u32x32 W[4];
  {
    const unsigned vo = (tid >> 4) * (HD * 2) + 16 * ((lane & 15) ^ swzB((tid >> 4) & 15));
    auto stage = [&](int j) {
      const char* src = (const char*)(p.Wv + (size_t)j * 128 * HD + h * DH);
      const unsigned dst = lds_off(smem) + L_WL + (j & 3) * 32768 + w * 1024;
      glds2(src, 32 * HD * 2, vo, dst);
      glds2(src + 64 * HD * 2, 32 * HD * 2, vo, dst + 16384);
    };
    start_sy(p.ws, p.mem0, h, smem);
#pragma unroll
    for (int j = 0; j < 4; ++j) stage(j);
    Boot bt;
#pragma unroll
    for (int j = 0; j < 8; ++j) {
      if (j < 5) WAIT_V(12); else if (j == 5) WAIT_V(8); else if (j == 6) WAIT_V(4); else WAIT_V(0);
      if (j == 7 && w == 7) {
        const unsigned m = boot_done(p.ws, bt);
        if (lane < 8) lds_at(smem + L_MISC)[lane] = lane == 7 ? m : (lane == 1 ? now_q() : 0);
      }
      LBAR();
#pragma unroll
      for (int k = 0; k < 4; ++k) put4(W[j >> 1], (j & 1) * 4 + k, ld_bfrag(smem + L_WL + (j & 3) * 32768 + (k >> 1) * 16384, k & 1, w, lane));
      LBAR();
      if (j + 4 < 8) stage(j + 4);
      if (j == 5 && w == 7) boot_ask(p.ws, bt);
    }
    RDY(32 + h);
  }
  const __amdgpu_buffer_rsrc_t rsy = mkrsrc(p.ws + WS_SY, SY_TOTAL);
  const u32x4 rs4 = raw_rsrc(p.ws + WS_SY, SY_TOTAL);
  gu32* abortw = (gu32*)(p.ws + WS_SY + SY_ABORT);
  const unsigned pm = uni(lds_at(smem + L_MISC)[7]);
  p.s0 = pm >> 31;
  unsigned long long fm = 0; (void)fm;
  int have = -1;
  auto fetch = [&](int sg) {
    if (have < sg) have = wait_prog(rsy, fkq(h, w - 4), sg, abortw);
    const char* src = p.ws + WS_T + t_off(p, (w - 4) * T_Q + kseg(sg, h)) + lane * 16;
#pragma unroll
    for (int i = 0; i < 8; ++i) glds16_sc1(src + i * 1024, smem + V_T + (sg & 3) * 24576 + (w - 4) * 8192 + i * 1024);
  };
  const int fo = SY_FO + ((w & 3) * NH + h) * 128;
  const float cexp = 0.08838834764831845f * 1.4426950408889634f;
  typedef __attribute__((ext_vector_type(8))) float f32x8;
  f32x8 bias;
  u32x4 ta;
  {
    const int r = lane & 15, q = lane >> 4;
#pragma unroll
    for (int j = 0; j < 8; ++j) bias[j] = (j >> 2) * 16 + 4 * q + (j & 3) <= 16 * (w & 1) + r ? INFINITY : -INFINITY;
    ta[0] = V_KI + w * 2048 + lane * 16;
    ta[1] = V_KI + w * 2048 + (q * 8 + (r >> 2)) * 16 + (r & 1) * 8;
    ta[2] = V_VM + (w & 2) * 512 + ((2 * (q & 1) + (q >> 1)) * 16 + r) * 4;
    ta[3] = 0;
  }
#pragma unroll 1
  for (int s = 0; s < NSEG; ++s) {
    unsigned spins, tgt;
    asm volatile(VV_ASM
        : [spins] "=&s"(spins), [tgt] "=&s"(tgt)
        : W4_IN, "{v200}"(lane * 16), "{v201}"(lane * 16 + w * 2048), "{v202}"((lane & 2) ? 0xEEEE : 0x4444), "{v203}"(lane * 4),
          "{v204}"(tid * 4 + V_VM + (s & 1) * 2048), [rs] "s"(rs4), [som] "s"(o_mem(pm, s) + (w >> 2) * 16384 + (w & 3) * 2048), [step] "s"(s),
          [sent] "s"(DSENT), [sab] "s"(SY_ABORT), [sv] "s"(o_v(pm, s, h) + w * 256)
        : CLB32(128), CLB32(160), CLB8(192, 193, 194, 195, 196, 197, 198, 199), CLB_S);
    if (w == 3 && s < 2) RDW(864 + s * 32 + h);
    if (w < 4 || s == NSEG - 1) {
      const int sg = s - (w < 4);
      if (w >= 4) pace_until(now_q() + K_DUE);
      if (sg >= 0) {
        asm volatile(TK_ASM
            :
            : "{v200}"(lane * 16), "{v[208:215]}"(bias), "{v[216:219]}"(ta), "{v[220:223]}"((u32x4){0x3f803f80u, 0x3f803f80u, 0x3f803f80u, 0x3f803f80u}),
              [rs] "s"(rs4), [rt] "s"(raw_rsrc(p.ws + WS_T, T_BYTES)), [sent] "s"(DSENT), [sab] "s"(SY_ABORT), [sk] "s"(o_v(pm, sg, h) + 2048),
              [tl] "s"(V_T + (sg & 3) * 24576), [vp] "s"((sg & 1) * 2048), [tq] "s"((w & 1) * 4096), [tv] "s"((w & 2) * 2048),
              [so] "s"(t_off_o(p, T_O + (sg * 2 + (w & 1)) * 16 + (h >> 1)) + (h & 1) * 4096 + (w & 2) * 1024),
              [up] "s"(0xFFFFFFFF00000000ull), [c] "s"(cexp), [negc] "s"(-cexp), [od] "s"(s < NSEG - 1 ? O_DUE : 0)
            : CLB32(128), CLB32(160), CLB8(192, 193, 194, 195, 196, 197, 198, 199), CLB8(224, 225, 226, 227, 228, 229, 230, 231),
              "v205", "v206", "v207", "s88", "s89", CLB_S);
        if (sg >= NSEG - 3) {
          static_assert(TLAG == 2, "");
          WAIT_V(0);
          if (lane == 0) __builtin_amdgcn_raw_buffer_store_b32(1u, rsy, fo + (sg - NSEG + 3) * 4 * NH * 128, 0, 16);
        }
      }
    } else if (w == 7) {
      SMP(6, s, (int)(now_q() - tgt));
      watch(rsy, s, lane, now_q() + LEAD_V + W_DELAY, (const unsigned*)(smem + L_MISC), abortw, side(pm, 1));
    } else if (w >= 4) {
      __builtin_amdgcn_s_sleep(T_SL);
      const int s1 = w == 5 ? (s == NSEG - 2 ? s + 1 : s) : s + 1;
#pragma unroll 1
      for (int sg = (w == 5 || s == 0) ? s : s + 1; sg <= s1; ++sg) fetch(sg);
      if (have < s1 + 1 && s1 + 1 < NSEG) have = look_prog(rsy, fkq(h, w - 4));
      if (w == 5 && s + NPRE < NSEG) {
#pragma unroll
        for (int i = 0; i < 4; ++i) st_sc1(rsy, o_v(pm, s + NPRE, h) + i * 1024 + lane * 16, (u32x4){DSENT, DSENT, DSENT, DSENT});
      }
    }
  }
  if (h == 3 && (w & 2) == 0) { LST(351 + (w & 1) + (w >> 2) * 2); }
}

#define RL_UP 12
#define RL_DN 4
#define RED_MIN 40
#define BA_LOOK 310
constexpr int R_SI = 32768, R_PART = 98304;

__device__ __forceinline__ void red_role(Args& p, int j, char* smem) {
  const int tid = threadIdx.x;
  int lane = tid & 63;
  const int w = __builtin_amdgcn_readfirstlane(tid >> 6);
  const int c = (j & 2) * 8 + (j >> 2), rh = j & 1;
  const int hg = (w + (j >> 2)) & 7, rot = (j >> 2) + (j >> 5);
  const __amdgpu_buffer_rsrc_t rsy = mkrsrc(p.ws + WS_SY, SY_TOTAL);
  const u32x4 rs4 = raw_rsrc(p.ws + WS_SY, SY_TOTAL);
  const __amdgpu_buffer_rsrc_t rt = mkrsrc(p.ws + WS_T, T_BYTES);
  gu32* abortw = (gu32*)(p.ws + WS_SY + SY_ABORT);
  u32x32 W[4];
  u32x2 tag = {0u, 0u};
  {
    const u16* Wo = p.Wo + (size_t)(4 * hg * DH) * DM + 32 * c;
    const int ro = ((lane >> 4) * 8 + ((lane & 15) >> 2)) * 64 + (lane & 3) * 8;
    Boot bt;
    load_blocks<16>(smem + 32768 + w * 16384,
        [&](int i, char* slot) {
          if (i == 12 && w == 0) boot_ask(p.ws, bt);
          if ((i < 2 || i == 4 || i == 8) && (c & 14) != 6 && lane < 10) {
            const int x = i < 2 ? 3 - i : i >> 3, n = min(((c & 15) - ((c & 8) >> 2)) * 10 + lane, 127);
            touch_line((const char*)p.Wo + (size_t)(w * 512 + (x >> 1) * 256 + (x & 1) * 64 + (n & 64) + n) * (DM * 2) + (c & 16) * 64 + 384, smem + w * 256);
          }
          const int x = ((i >> 2) + rot) & 3;
          blk_dma(Wo + (size_t)((x >> 1) * 256 + (x & 1) * 64 + ((i >> 1) & 1) * 128 + (i & 1) * 32) * DM, DM, slot, lane);
        },
        [&](int i, char* slot) {
#pragma unroll
          for (int t = 0; t < 2; ++t) {
            s16x4 lo = __builtin_amdgcn_ds_read_tr16_b64_v4i16((lds_s16x4_ptr)(slot + ro + t * 32));
            s16x4 hi = __builtin_amdgcn_ds_read_tr16_b64_v4i16((lds_s16x4_ptr)(slot + ro + t * 32 + 256));
            put4(W[i >> 2], (i & 2) * 2 + 2 * t + (i & 1), (bf16x8){lo[0], lo[1], lo[2], lo[3], hi[0], hi[1], hi[2], hi[3]});
          }
        });
    if (w == 0) { lds_at(smem + 4128)[0] = boot_done(p.ws, bt); tag = next_tag(bt.e); }
    LBAR();
  }
  const unsigned pm = uni(lds_at(smem + 4128)[0]);
  p.s0 = pm >> 31;
  const int hs = side(pm, 1);
  if (((j >> 2) & 14) != 6 && GPF == 2) {
#pragma unroll
    for (int i = 0; i < 4; ++i)
      touch_line((const char*)(i < 2 ? p.wg : p.wu) + (size_t)(j - (j >= 32 ? 8 : 0)) * 131072 + ((i & 1) * 8 + w) * 8192 + lane * 128, smem + w * 256);
  }
  {
    const u32x4 ds = {DSENT, DSENT, DSENT, DSENT};
#pragma unroll
    for (int i = 0; i < 2; ++i) {
      const int s = (w * 2 + i) * 4 + (lane >> 4);
      if (s >= 2) st_sc1(rsy, o_mem(pm, s) + (c >> 4) * 16384 + (c & 15) * 512 + rh * 128 + ((lane >> 3) & 1) * 256 + (lane & 7) * 16, ds);
    }
  }
  if (tid < 4) { reinterpret_cast<unsigned*>(smem + 4096)[tid] = tid == 3 ? now_q() : 0; reinterpret_cast<unsigned*>(smem + 4112)[tid] = DSENT; }
  if (tid < 256) reinterpret_cast<int*>(smem + 5120)[tid] = ((tid >> 2) - TLAG - 6) >> 2;
  LBAR();
  const unsigned base = __builtin_amdgcn_readfirstlane(lds_at(smem + 4096)[3]);
  RDY(64 + j);
  unsigned long long fm = 0, ps = 0;
  const unsigned aL = lane * 16;
  const int e = ((56 + w) * SEG + rh * 16 + (lane >> 2)) * DM + c * 32 + (lane & 3) * 8;
  u32x4 xv = *reinterpret_cast<const u32x4*>(p.x + e), av = {DSENT, DSENT, DSENT, DSENT};
  asm volatile("" : "+v"(xv));
  {
    const int r = lane & 15, q = lane >> 4;
    const int sm0 = o_mem(pm, 0) + (c >> 4) * 16384 + (c & 15) * 512 + rh * 128;
    u32x4 VO, VT;
    unsigned fo = SY_FO + ((rh + (lane & 4) / 2) * NH + 4 * hg + (lane & 3)) * 128;
    unsigned ao = w == 1 ? (c * S_LEN + rh * 16 - (TLAG + 3) * SEG) * 64 + lane * 16
                         : (w >= 4 ? SY_O + NPRE * 131072 + side(pm, w - 2) + (j >> 3) * 16384 + (j & 7) * 1024 + lane * 16
                                   : SY_FS + (2 * c + rh) * 128 + (lane < 8 ? lane * 16 : 0x70000000));
    unsigned ar = w == 1 ? R_SI - (TLAG + 2) * 1024 + lane * 16 : (w == 2 ? 5120 : 4112);
    const unsigned long long am = w == 1 ? ((1ull << 56) - 1) << (TLAG + 3) : (w == 2 ? 0x11111111111111ull << (TLAG + 7) : (w >= 4 ? (0xFFFFull << (16 * (w - 4))) >> NPRE : 0));
    unsigned pw = R_PART + w * 2048 + lane * 16, pr = R_PART + 16384 + tid * 4;
    unsigned sw = R_SI - (TLAG + 1) * 1024 + (lane >> 2) * 64 + w * 8 + (lane & 3) * 2, stm = 8192 + w * 512;
#pragma unroll
    for (int l = 0; l < 4; ++l) {
      const int x = (l + rot) & 3;
      VO[l] = o_o(0, 0, 4 * hg) + rh * 65536 + x * 1024 + aL;
      VT[l] = (T_O + rh * 16 + 2 * hg + (x >> 1)) * 16384 + (x & 1) * 2048 + aL;
    }
    unsigned ba = lane < 4 ? SY_BA + hs + (4 * hg + lane) * 128 : 0x7F000000u;
    int rl = 280;
    unsigned step, E, so, st, st4, sh;
    asm volatile(RED_ASM
        : [step] "=&s"(step), [E] "=&s"(E), [so] "=&s"(so), [st] "=&s"(st), [st4] "=&s"(st4), [sh] "=&s"(sh),
          [rl] "+s"(rl), [fm] "+s"(fm), [ps] "+s"(ps),
          "+{v[224:227]}"(av), "+{v228}"(ao), "+{v229}"(ar), "+{v230}"(pw), "+{v231}"(pr), "+{v232}"(sw), "+{v233}"(stm), "+{v242}"(fo), "+{v173}"(ba)
        : W4_IN, "{v168}"(aL), "{v169}"(w * 512 + lane * 8), "{v170}"(lane * 8),
          "{v171}"(sm0 + 32768 + (q >> 1) * 256 + (q & 1) * 64 + ((r >> 1) & 1) * 32 + (r & 1) * 16 + (r >> 2) * 4),
          "{v172}"((lane & 2) ? 0xEEEE : 0x4444),
          "{v174}"((r & 1) ? 0x03020706u : 0x05040100u), "{v175}"(SY_BR + hs + j * 128 + lane * 8), "{v[234:237]}"(VO), "{v[238:241]}"(VT), "{v243}"(e * 2),
          [rg] "s"(raw_rsrc(p.ws + WS_G, S_LEN * DM * 2)), [fg] "s"(SY_FG + ((14 + (w >> 2)) * 8 + (c >> 2)) * 128),
          [rs] "s"(rs4), [ra] "s"(w == 1 ? raw_rsrc(p.ws + WS_S, S_LEN * DM * 2) : rs4), [rt] "s"(raw_rsrc(p.ws + WS_T, T_BYTES)),
          [ds] "n"(DS_RED), [mn] "n"(RED_MIN), [lk] "n"(DS_MEM + BA_LOOK), [up] "n"(RL_UP), [dn] "n"(-RL_DN), [sent] "s"(DSENT), [sab] "s"(SY_ABORT),
          [base] "s"(base), [pm] "s"(pm), [s0] "s"((p.s0 ^ TF_O) << 13), [am] "s"(am), [ainc] "s"(w == 1 ? SEG * 64 : (w >= 4 ? 131072 : 0)),
          [rinc] "s"(w == 1 ? 1024 : (w == 2 ? 16 : 0))
        : CLB32(128), CLB8(160, 161, 162, 163, 164, 165, 166, 167), CLB8(176, 177, 178, 179, 180, 181, 182, 183),
          CLB8(184, 185, 186, 187, 188, 189, 190, 191), CLB8(192, 193, 194, 195, 196, 197, 198, 199), CLB8(200, 201, 202, 203, 204, 205, 206, 207),
          CLB8(208, 209, 210, 211, 212, 213, 214, 215), CLB8(216, 217, 218, 219, 220, 221, 222, 223), CLB_E, "s96", CLB_S);
  }
  if (j == 0 && w == 5) { LST(345); }
  LBAR();
  if (j == 0 && w == 5) { LST(346); }
  u32x4 sv;
  if (w < 6 - TLAG) sv = *reinterpret_cast<const u32x4*>(smem + R_SI + (56 + w) * 1024 + lane * 16);
  else {
    const f32x4* part = reinterpret_cast<const f32x4*>(smem + (w == 6 - TLAG ? R_PART : (w - 6 + TLAG) * 16384)) +
                        (lane & 2) * 32 + (lane & 1) * 32 + (lane >> 2);
    f32x4 x[8][2];
#pragma unroll
    for (int ww = 0; ww < 8; ++ww) { x[ww][0] = part[ww * 128]; x[ww][1] = part[ww * 128 + 16]; }
#pragma unroll
    for (int u = 0; u < 2; ++u) {
      const f32x4 a = ((x[0][u] + x[1][u]) + (x[2][u] + x[3][u])) + ((x[4][u] + x[5][u]) + (x[6][u] + x[7][u]));
      sv[2 * u] = cvtpk(a[0], a[1]); sv[2 * u + 1] = cvtpk(a[2], a[3]);
    }
  }
  st_out(p, e, combine(sv, av, xv));
  if (j == 0 && tid == 0) st2_sc1(rsy, SY_EP, tag);
  if (tid == 128) { LST(400 + j); }
}

__global__ __launch_bounds__(512, 2) void main_kernel(Args p) {
  char* smem = g_smem;
  p.ws = ws_align(p.ws);
  const int bx = blockIdx.x & 7, bk = blockIdx.x >> 3;
  const bool isg = (bx ^ XPROBE) & 4;
  const int b = (isg ? NCHAIN : 0) + bk * 4 + (bx & 3);
  if (b == 64 && threadIdx.x == 0) { LST(500); }
  if (threadIdx.x < 64) {
    unsigned long long pc; unsigned len, keep;
    asm volatile("s_getpc_b64 %0\n.Lpc%=:\n\ts_mov_b32 %1, .Lcode_end-.Lpc%=" : "=s"(pc), "=s"(len));
    asm volatile("s_mov_b32 %0, m0\n\ts_mov_b32 m0, %3\n\ts_nop 0\n\tbuffer_load_dword %1, %2, 0 offen lds\n\ts_mov_b32 m0, %0"
                 : "=&s"(keep) : "v"((bk * 40 + threadIdx.x) * 128), "s"(raw_rsrc((const void*)pc, len)), "s"((b >> 6) == 1 ? 2048 : 163328) : "memory");
  }
  if (b >= NCHAIN) gemm_role(p, b - NCHAIN, smem);
  else if (b < 32) qk_role(p, (b & 3) * 8 + bk, smem);
  else if (b < 64) vu_role(p, (b & 3) * 8 + bk - 8, smem);
  else red_role(p, b - 64, smem);
}

extern "C" void kernel_launch(void* const* d_in, const int* in_sizes, int n_in,
                              void* d_out, int out_size, void* d_ws, size_t ws_size,
                              hipStream_t stream) {
  Args a;
  a.x = (const u16*)d_in[0]; a.mem0 = (const u16*)d_in[1];
  a.Wq = (const u16*)d_in[2]; a.Wk = (const u16*)d_in[3]; a.Wv = (const u16*)d_in[4]; a.Wo = (const u16*)d_in[5];
  a.wg = (const u16*)d_in[8]; a.wu = (const u16*)d_in[9]; a.wd = (const u16*)d_in[10];
  a.ws = (char*)d_ws; a.out = (u16*)d_out; a.s0 = 0; a.pad1 = 0;
  main_kernel<<<256, 512, 0, stream>>>(a);
}
```

```cpp
#include <hip/hip_runtime.h>
#include <hip/hip_bf16.h>
#include <stdint.h>
#include <type_traits>

constexpr int S_LEN = 2048, DM = 1024, NH = 32, DH = 128, NMEM = 8, SEG = 32;
constexpr int NSEG = S_LEN / SEG;
constexpr int HD = NH * DH;
constexpr int FF = 3584;

typedef unsigned short u16;
typedef __attribute__((ext_vector_type(8))) short bf16x8;
typedef __attribute__((ext_vector_type(4))) short s16x4;
typedef __attribute__((ext_vector_type(4))) float f32x4;
typedef __attribute__((ext_vector_type(4))) unsigned int u32x4;
typedef __attribute__((ext_vector_type(2))) unsigned int u32x2;
typedef __attribute__((address_space(3))) s16x4* lds_s16x4_ptr;
typedef __attribute__((address_space(3))) void* lds_void_ptr;
typedef __attribute__((address_space(1))) const void* glb_cvoid_ptr;
typedef __attribute__((address_space(1))) unsigned gu32;
typedef __bf16 bf16x2_t __attribute__((ext_vector_type(2)));
typedef float f32x2_t __attribute__((ext_vector_type(2)));

__device__ __forceinline__ float bf2f(u16 v) { return __uint_as_float(((unsigned)v) << 16); }
__device__ __forceinline__ unsigned cvtpk(float lo, float hi) {
  bf16x2_t r = __builtin_convertvector((f32x2_t){lo, hi}, bf16x2_t);
  return __builtin_bit_cast(unsigned, r);
}
__device__ __forceinline__ float lo_f(unsigned v) { return __uint_as_float(v << 16); }
__device__ __forceinline__ float hi_f(unsigned v) { return __uint_as_float(v & 0xffff0000u); }
__device__ __forceinline__ float rbf(float f) { return lo_f(cvtpk(f, f)); }
__device__ __forceinline__ bf16x8 as_bf16x8(u32x4 v) { return __builtin_bit_cast(bf16x8, v); }
#define MFMA(a, b, c) __builtin_amdgcn_mfma_f32_16x16x32_bf16((a), (b), (c), 0, 0, 0)
typedef __attribute__((ext_vector_type(16))) short bf16x16;
typedef __attribute__((ext_vector_type(8))) __bf16 bfv8;
typedef __attribute__((ext_vector_type(16))) __bf16 bfv16;
#define SMFMA(a, b, c, idx) __builtin_amdgcn_smfmac_f32_16x16x64_bf16(__builtin_bit_cast(bfv8, (a)), __builtin_bit_cast(bfv16, (b)), (c), (idx), 0, 0)
__device__ __forceinline__ bf16x16 cat16(bf16x8 lo, bf16x8 hi) {
  return __builtin_shufflevector(lo, hi, 0, 1, 2, 3, 4, 5, 6, 7, 8, 9, 10, 11, 12, 13, 14, 15);
}

#define KSTR 65
#define XLD 1024
constexpr int PAGE = 2 << 20, NPAGE = 14;
constexpr int T_K = 0, T_Q = 2304, T_V = 4608, T_O = 6912, T_END = T_O + 2048;
constexpr unsigned T_BYTES = T_END * 16384u;
constexpr size_t WS_SY = 0;
constexpr size_t WS_T = (size_t)NPAGE * PAGE;
constexpr size_t WS_XN = WS_T + T_BYTES;
constexpr size_t WS_HF = WS_XN + (size_t)S_LEN * XLD * 2;
constexpr size_t WS_G = WS_HF + (size_t)S_LEN * 16384;
constexpr size_t WS_S = WS_G + (size_t)S_LEN * DM * 2;
constexpr size_t WS_END = WS_S + (size_t)S_LEN * DM * 2;
constexpr unsigned SENT = 0xFFFFFFFFu;
constexpr unsigned DSENT = 0xFFBFFFBFu;
constexpr int SY_MEM = 0;
constexpr int SY_O = 2 * PAGE;
constexpr int SY_V = 6 * PAGE;
constexpr int SY_BR = PAGE, SY_BA = SY_BR + 16384;
constexpr int SY_FKQ = SY_BR + 32768;
constexpr int SY_FG = SY_FKQ + NH * 4 * 128;
constexpr int SY_FO = SY_FG + 16 * 8 * 128;
constexpr int SY_CH = SY_FO + 384 * 128;
constexpr int SY_Q = SY_CH + 8 * 128;
constexpr int SY_ABORT = SY_Q + 8 * 128;
constexpr int SY_FS = SY_ABORT + 256;
constexpr int SY_FEND = SY_FS + 64 * 128;
constexpr int SY_EP = SY_FEND, SY_BT = SY_EP + 128;
constexpr int SY_TOTAL = NPAGE * PAGE;
constexpr int PROBE = PAGE - 16384 + 6144;
__device__ __forceinline__ int side(unsigned pm, int page) { return ((pm >> page) & 1u) << 13; }
__device__ __forceinline__ int o_mem(unsigned pm, int s) { return SY_MEM + s * 32768 + side(pm, 0); }
__device__ __forceinline__ int o_o(unsigned pm, int s, int h) {
  return SY_O + s * 131072 + (h >> 3) * 16384 + ((h >> 1) & 3) * 2048 + (h & 1) * 128 + side(pm, 2 + (s >> 4));
}
__device__ __forceinline__ int o_v(unsigned pm, int s, int h) { return SY_V + s * 262144 + (h >> 1) * 16384 + (h & 1) * 4096 + side(pm, 6 + (s >> 3)); }
__device__ __forceinline__ char* ws_align(char* ws) { return ws + ((PAGE - (unsigned)(uintptr_t)ws) & (PAGE - 1)); }
#define NFUSE 12
constexpr int NPRE = 4;
static_assert(WS_END + PAGE <= (size_t)256 << 20 && NH * KSTR <= T_Q && NPAGE % 2 == 0, "");
constexpr int NCHAIN = 128, NGW = 32;
__host__ __device__ constexpr int kseg(int sg, int h) { return h * KSTR + sg; }
__host__ __device__ constexpr int fkq(int h, int kind) { return SY_FKQ + (h * 4 + kind) * 128; }

#define HSKEW 0
#define XPROBE 0
struct Args {
  const u16 *x, *mem0, *Wq, *Wk, *Wv, *Wo, *wg, *wu, *wd;
  char* ws;
  u16* out;
  int s0, pad1;
};
constexpr int HLD = 16384 + HSKEW;
__device__ __forceinline__ int h_off(const Args& p, int rblk) { return rblk * PAGE + (((p.s0 ^ rblk ^ 1) & 1) << 13); }
static_assert(WS_HF % (2 * PAGE) == 0 && 127 * HSKEW + FF * 2 <= 8192, "");
#define TF_KQV 1
#define TF_O 0
__device__ __forceinline__ int t_off(const Args& p, int t) { return t * 16384 + (((p.s0 ^ (t >> 7) ^ (TF_KQV == 2 ? t : TF_KQV)) & 1) << 13); }
__device__ __forceinline__ int t_off_o(const Args& p, int t) { return t * 16384 + (((p.s0 ^ (t >> 7) ^ (TF_O == 2 ? t : TF_O)) & 1) << 13); }

__device__ __forceinline__ float wave_sum(float v) {
#pragma unroll
  for (int i = 0; i < 4; ++i) {
    const int x = (int)__float_as_uint(v);
    v += __uint_as_float(i == 0 ? __builtin_amdgcn_update_dpp(0, x, 0x128, 0xf, 0xf, false) : i == 1 ? __builtin_amdgcn_update_dpp(0, x, 0x124, 0xf, 0xf, false)
                         : i == 2 ? __builtin_amdgcn_update_dpp(0, x, 0x122, 0xf, 0xf, false) : __builtin_amdgcn_update_dpp(0, x, 0x121, 0xf, 0xf, false));
  }
  auto r = __builtin_amdgcn_permlane16_swap(__float_as_uint(v), __float_as_uint(v), false, false);
  v = __uint_as_float(r[0]) + __uint_as_float(r[1]);
  r = __builtin_amdgcn_permlane32_swap(__float_as_uint(v), __float_as_uint(v), false, false);
  return __uint_as_float(r[0]) + __uint_as_float(r[1]);
}

__shared__ __attribute__((aligned(16))) char g_smem[163840];
__device__ __forceinline__ unsigned lds_off(const void* p) {
  return (unsigned)(uintptr_t)(lds_void_ptr)g_smem + (unsigned)((const char*)p - g_smem);
}
__device__ __forceinline__ __amdgpu_buffer_rsrc_t mkrsrc(const void* p, unsigned bytes) {
  return __builtin_amdgcn_make_buffer_rsrc((void*)p, 0, bytes, 0x00020000);
}
__device__ __forceinline__ u32x4 ld_sc1(__amdgpu_buffer_rsrc_t rs, int off) {
  return __builtin_amdgcn_raw_buffer_load_b128(rs, off, 0, 16);
}
__device__ __forceinline__ void st_sc1(__amdgpu_buffer_rsrc_t rs, int off, u32x4 v) {
  __builtin_amdgcn_raw_buffer_store_b128(v, rs, off, 0, 16);
}
__device__ __forceinline__ unsigned ror8(unsigned v) {
  return (unsigned)__builtin_amdgcn_update_dpp(0, (int)v, 0x128, 0xf, 0xf, false);
}
__device__ __forceinline__ float ror8f(float v) { return __uint_as_float(ror8(__float_as_uint(v))); }
__device__ __forceinline__ void swap16(unsigned& x, unsigned& y) {
  auto r = __builtin_amdgcn_permlane16_swap(x, y, false, false);
  x = r[0]; y = r[1];
}
__device__ __forceinline__ void swap32(unsigned& x, unsigned& y) {
  auto r = __builtin_amdgcn_permlane32_swap(x, y, false, false);
  x = r[0]; y = r[1];
}
__device__ __forceinline__ f32x4 fold32(f32x4 x, f32x4 y) {
  f32x4 o;
#pragma unroll
  for (int j = 0; j < 4; ++j) {
    unsigned a = __float_as_uint(x[j]), b = __float_as_uint(y[j]);
    swap32(a, b);
    o[j] = __uint_as_float(a) + __uint_as_float(b);
  }
  return o;
}
__device__ __forceinline__ bf16x8 tr_frag(const char* img_lane) {
  s16x4 lo = __builtin_amdgcn_ds_read_tr16_b64_v4i16((lds_s16x4_ptr)img_lane);
  s16x4 hi = __builtin_amdgcn_ds_read_tr16_b64_v4i16((lds_s16x4_ptr)(img_lane + 64));
  return (bf16x8){lo[0], lo[1], lo[2], lo[3], hi[0], hi[1], hi[2], hi[3]};
}
__device__ __forceinline__ float rowq_max(float v) {
  unsigned a = __float_as_uint(v), b = a;
  swap16(a, b);
  v = __builtin_elementwise_maximum(__uint_as_float(a), __uint_as_float(b));
  a = __float_as_uint(v); b = a;
  swap32(a, b);
  return __builtin_elementwise_maximum(__uint_as_float(a), __uint_as_float(b));
}
__device__ __forceinline__ float rowq_sum(float v) {
  unsigned a = __float_as_uint(v), b = a;
  swap16(a, b);
  v = __uint_as_float(a) + __uint_as_float(b);
  a = __float_as_uint(v); b = a;
  swap32(a, b);
  return __uint_as_float(a) + __uint_as_float(b);
}
__device__ __forceinline__ bool spin_fail(unsigned& spins, gu32* abortw) {
  ++spins;
  if ((spins & 63u) == 0u) {
    const __amdgpu_buffer_rsrc_t ra = mkrsrc((const void*)abortw, 4);
    unsigned z = 0;
    asm volatile("" : "+v"(z));
    if (__builtin_amdgcn_readfirstlane(__builtin_amdgcn_raw_buffer_load_b32(ra, z, 0, 16)) == 1u) return true;
    if (spins > (1u << 19)) {
      __builtin_amdgcn_raw_buffer_store_b32(z + 1u, ra, z, 0, 16);
      return true;
    }
  }
  return false;
}
__device__ __forceinline__ unsigned umax4(u32x4 v) { return max(max(v[0], v[1]), max(v[2], v[3])); }
__device__ __forceinline__ bool has_ds(u32x4 v) { return (v[0] == DSENT) | (v[1] == DSENT) | (v[2] == DSENT) | (v[3] == DSENT); }
__device__ __forceinline__ float fmax2(float a, float b) { return __builtin_elementwise_maximum(a, b); }
__device__ __forceinline__ bf16x8 ldsf(const char* p) { return *reinterpret_cast<const bf16x8*>(p); }
#define SCHED() __builtin_amdgcn_sched_barrier(0)
#define LBAR() do { asm volatile("s_waitcnt lgkmcnt(0)" ::: "memory"); __builtin_amdgcn_s_barrier(); asm volatile("" ::: "memory"); } while (0)
#define WAIT_V(n) asm volatile("s_waitcnt vmcnt(%0)" :: "n"(n) : "memory")

__device__ __forceinline__ void wait_flags(__amdgpu_buffer_rsrc_t rs, int off, gu32* abortw) {
  for (unsigned spins = 0;;) {
    asm volatile("" ::: "memory");
    const unsigned v = __builtin_amdgcn_raw_buffer_load_b32(rs, off, 0, 16);
    if (__all(v != SENT)) break;
    __builtin_amdgcn_s_sleep(4);
    if (spin_fail(spins, abortw)) break;
  }
  asm volatile("" ::: "memory");
}

#define LST(i) do {} while (0)
#define RDY(i) do {} while (0)
#define RDW(i) do {} while (0)
#define SMP(k, s, v) do {} while (0)
#define LSTC(i) do {} while (0)
__device__ __forceinline__ unsigned offA(unsigned row, unsigned ch) {
  return row * 128u + 16u * (ch ^ (row & 7u));
}
__device__ __forceinline__ unsigned swzB(unsigned row) { return ((row & 3u) << 2) | ((row >> 2) & 3u); }
__device__ __forceinline__ unsigned offB(unsigned row, unsigned ch) {
  return 256u * row + 16u * (ch ^ swzB(row));
}
__device__ __forceinline__ void glds16(const u16* src, char* dst) {
  const unsigned d = __builtin_amdgcn_readfirstlane(lds_off(dst));
  unsigned keep;
  asm volatile("s_mov_b32 %0, m0\n\ts_mov_b32 m0, %2\n\ts_nop 0\n\tglobal_load_lds_dwordx4 %1, off\n\ts_mov_b32 m0, %0"
               : "=&s"(keep) : "v"(src), "s"(d) : "memory");
}
__device__ __forceinline__ bf16x8 ld_bfrag(const char* sb, int ks, int c, int lane) {
  const unsigned g = lane >> 4, qq = (lane & 15) >> 2, pp = lane & 3;
  s16x4 lo = __builtin_amdgcn_ds_read_tr16_b64_v4i16(
      (lds_s16x4_ptr)(sb + offB(32 * ks + 8 * g + qq, 2 * c + (pp >> 1)) + 8 * (pp & 1)));
  s16x4 hi = __builtin_amdgcn_ds_read_tr16_b64_v4i16(
      (lds_s16x4_ptr)(sb + offB(32 * ks + 8 * g + 4 + qq, 2 * c + (pp >> 1)) + 8 * (pp & 1)));
  return (bf16x8){lo[0], lo[1], lo[2], lo[3], hi[0], hi[1], hi[2], hi[3]};
}

__device__ __forceinline__ void blk_dma(const u16* src, size_t ld, char* slot, int lane) {
  const u16* s = src + (size_t)(lane >> 2) * ld + (lane & 3) * 8;
  glds16(s, slot);
  glds16(s + 16 * ld, slot + 1024);
}
__device__ __forceinline__ bf16x8 blk_frag(const char* slot, int T, int lane) {
  const char* a = slot + ((lane >> 4) * 8 + ((lane & 15) >> 2)) * 64 + (lane & 3) * 16 + T * 8;
  s16x4 lo = __builtin_amdgcn_ds_read_tr16_b64_v4i16((lds_s16x4_ptr)a);
  s16x4 hi = __builtin_amdgcn_ds_read_tr16_b64_v4i16((lds_s16x4_ptr)(a + 256));
  return (bf16x8){lo[0], lo[1], lo[2], lo[3], hi[0], hi[1], hi[2], hi[3]};
}
template <int NB, typename ISSUE, typename SINK>
__device__ __forceinline__ void load_blocks(char* slice, ISSUE&& issue, SINK&& sink) {
#pragma unroll
  for (int i = 0; i < 8; ++i) issue(i, slice + i * 2048);
#pragma unroll
  for (int g = 0; g < NB / 4; ++g) {
    if (g + 1 < NB / 4) WAIT_V(8); else WAIT_V(0);
#pragma unroll
    for (int j = 0; j < 4; ++j) sink(4 * g + j, slice + ((4 * g + j) & 7) * 2048);
    asm volatile("s_waitcnt lgkmcnt(0)" ::: "memory");
    if (g + 2 < NB / 4) {
#pragma unroll
      for (int j = 0; j < 4; ++j) issue(4 * g + 8 + j, slice + ((4 * g + j) & 7) * 2048);
    }
  }
}

__device__ __forceinline__ u32x4 widen32(unsigned a0, unsigned a1, unsigned b0, unsigned b1) {
  swap32(a0, b0); swap16(a0, b0);
  swap32(a1, b1); swap16(a1, b1);
  return (u32x4){a0, a1, b0, b1};
}

__device__ __forceinline__ void publish_flags(__amdgpu_buffer_rsrc_t rs, int off, int stride, int n) {
  WAIT_V(0);
  LBAR();
  int tid = threadIdx.x;
  asm volatile("" : "+v"(tid));
  if (tid < n) __builtin_amdgcn_raw_buffer_store_b32(1u, rs, off + tid * stride, 0, 16);
}

__device__ __forceinline__ void wait_s(const Args& p, __amdgpu_buffer_rsrc_t rsy, int rblk, int c0, int n, bool need) {
  if (need) {
    if (threadIdx.x < 64) {
      for (unsigned spins = 0;;) {
        asm volatile("" ::: "memory");
        const int f = (int)__builtin_amdgcn_raw_buffer_load_b32(rsy, SY_FS + (2 * c0 + (threadIdx.x & (2 * n - 1))) * 128, 0, 16);
        if (__all(f >= rblk)) break;
        __builtin_amdgcn_s_sleep(32);
        if (spin_fail(spins, (gu32*)(p.ws + WS_SY + SY_ABORT))) break;
      }
    }
    LBAR();
  }
}
__device__ __forceinline__ u32x4 combine(u32x4 sv, u32x4 g, u32x4 x) {
  u32x4 o;
#pragma unroll
  for (int j = 0; j < 4; ++j) {
    const unsigned t = cvtpk(lo_f(sv[j]) * lo_f(g[j]), hi_f(sv[j]) * hi_f(g[j]));
    o[j] = cvtpk(lo_f(x[j]) + lo_f(t), hi_f(x[j]) + hi_f(t));
  }
  return o;
}
#define OUT_AUX 16
__device__ __forceinline__ void st_out(const Args& p, int e, u32x4 v) {
  __builtin_amdgcn_raw_buffer_store_b128(v, mkrsrc(p.out, S_LEN * DM * 2), e * 2, 0, OUT_AUX);
}
struct SX { u32x4 s, x; };
__device__ __forceinline__ SX get_sx(const Args& p, int rblk, int e) {
  SX r; r.s = (u32x4){0u, 0u, 0u, 0u}; r.x = r.s;
  if (rblk < NFUSE) {
    r.s = ld_sc1(mkrsrc(p.ws + WS_S, S_LEN * DM * 2), (((e & 1023) >> 5) * S_LEN + (e >> 10)) * 64 + (e & 31) * 2);
    r.x = *reinterpret_cast<const u32x4*>(p.x + e);
  }
  return r;
}
__device__ __forceinline__ void put_g(const Args& p, __amdgpu_buffer_rsrc_t rg, int rblk, int e, u32x4 g, const SX& v) {
  if (rblk < NFUSE) {
    st_out(p, e, combine(v.s, g, v.x));
  } else st_sc1(rg, e * 2, g);
}

__device__ __forceinline__ void job_comb(const Args& p, int rblk, int ct, __amdgpu_buffer_rsrc_t rsy) {
  int tid = threadIdx.x;
  asm volatile("" : "+v"(tid));
  u32x4 xv[8];
#pragma unroll
  for (int i = 0; i < 8; ++i) xv[i] = *reinterpret_cast<const u32x4*>(p.x + (rblk * 128 + i * 16 + (tid >> 5)) * DM + ct * 256 + (tid & 31) * 8);
  if (tid < 64) wait_flags(rsy, SY_FG + (rblk * 8 + 2 * ct + (tid & 1)) * 128, (gu32*)(p.ws + WS_SY + SY_ABORT));
  wait_s(p, rsy, rblk, ct * 8, 8, true);
  u32x4 sv[8], gv[8];
#pragma unroll
  for (int i = 0; i < 8; ++i) {
    const int e = (rblk * 128 + i * 16 + (tid >> 5)) * DM + ct * 256 + (tid & 31) * 8;
    sv[i] = ld_sc1(mkrsrc(p.ws + WS_S, S_LEN * DM * 2), (((e & 1023) >> 5) * S_LEN + (e >> 10)) * 64 + (e & 31) * 2);
    gv[i] = ld_sc1(mkrsrc(p.ws + WS_G, S_LEN * DM * 2), e * 2);
  }
#pragma unroll
  for (int i = 0; i < 8; ++i)
    st_out(p, (rblk * 128 + i * 16 + (tid >> 5)) * DM + ct * 256 + (tid & 31) * 8, combine(sv[i], gv[i], xv[i]));
}

__device__ __forceinline__ u32x4 raw_rsrc(const void* p, unsigned bytes) {
  const unsigned long long a = (unsigned long long)p;
  return (u32x4){(unsigned)a, (unsigned)(a >> 32) & 0xffffu, bytes, 0x00020000u};
}
__device__ __forceinline__ u32x2 next_tag(u32x2 e) { return (u32x2){e[0] + 1u, e[1] + (e[0] == 0xFFFFFFFFu)}; }
struct Boot { u32x2 e; u32x4 c; };
__device__ __forceinline__ void boot_ask(const char* ws, Boot& r) {
  const unsigned o = (threadIdx.x & 63) * 128;
  asm volatile("buffer_load_dwordx2 %0, off, %3, %4 sc1\n\tbuffer_load_dwordx4 %1, %2, %3, %5 offen sc1"
               : "=&v"(r.e), "=&v"(r.c) : "v"(o), "s"(raw_rsrc(ws, SY_TOTAL)), "s"(SY_EP), "s"(SY_BT) : "memory");
}
__device__ __forceinline__ void wait_rows(const char* ws, u32x2 tag) {
  const __amdgpu_buffer_rsrc_t rs = mkrsrc(ws, SY_TOTAL);
  int o = (threadIdx.x & 63) * 128;
  asm volatile("" : "+v"(o));
#pragma unroll 1
  for (int i = 0; i < (1 << 17); ++i) {
    asm volatile("" ::: "memory");
    const u32x2 a = __builtin_amdgcn_raw_buffer_load_b64(rs, o, SY_BT + 32 * 128, 16), b = __builtin_amdgcn_raw_buffer_load_b64(rs, o, SY_BT + 96 * 128, 16);
    if (__all(a[0] == tag[0] && a[1] == tag[1] && b[0] == tag[0] && b[1] == tag[1])) break;
    __builtin_amdgcn_s_sleep(8);
  }
  asm volatile("" ::: "memory");
}
__device__ __forceinline__ unsigned boot_done(const char* ws, Boot& r, bool own = false, u32x2 tag = (u32x2){0u, 0u}) {
#pragma unroll 1
  for (int i = 0; i < (1 << 17); ++i) {
    asm volatile("" : "+v"(r.e), "+v"(r.c));
    if (!own) tag = next_tag(r.e);
    if (__all(r.c[0] == tag[0] && r.c[1] == tag[1])) break;
    __builtin_amdgcn_s_sleep(8);
    boot_ask(ws, r);
    WAIT_V(0);
  }
  unsigned q0[NPAGE], q1[NPAGE], ga = 0xFFFFu, gb = 0xFFFFu;
#pragma unroll
  for (int i = 0; i < NPAGE; ++i) {
    const unsigned x = __builtin_amdgcn_readlane(r.c[2], i), y = __builtin_amdgcn_readlane(r.c[2], i + NPAGE);
    q0[i] = min(x >> 16, y >> 16); q1[i] = min(x & 0xFFFFu, y & 0xFFFFu);
    ga = min(ga, (i & 1) ? q1[i] : q0[i]); gb = min(gb, (i & 1) ? q0[i] : q1[i]);
  }
  unsigned m = gb < ga ? 0x80001555u : 0x2AAAu;
  const unsigned thr = min(ga, gb) + 110;
#pragma unroll
  for (int i = 0; i < NPAGE; ++i) {
    const unsigned lo = min(q0[i], q1[i]), hi = max(q0[i], q1[i]);
    const unsigned keep = (lo < thr ? hi : 0u) > lo + 100 ? ~(1u << i) : ~0u;
    m = (m & keep) | ((q1[i] < q0[i] ? ~0u : 0u) & ~keep);
  }
  return m;
}
typedef __attribute__((address_space(3))) volatile unsigned* lds_vu_ptr;
__device__ __forceinline__ lds_vu_ptr lds_at(const void* p) {
  unsigned a = lds_off(p);
  asm volatile("" : "+v"(a));
  return (lds_vu_ptr)(uintptr_t)a;
}
__device__ __forceinline__ unsigned uni(unsigned v) {
  unsigned r;
  asm volatile("s_nop 1\n\tv_readfirstlane_b32 %0, %1\n\ts_nop 3" : "=s"(r) : "v"(v));
  return r;
}
__device__ __forceinline__ void start_sy(char* ws, const u16* mem0, int i, char* smem) {
  const int tid = threadIdx.x, lane = tid & 63, w = __builtin_amdgcn_readfirstlane(tid >> 6);
  const __amdgpu_buffer_rsrc_t rsy = mkrsrc(ws, SY_TOTAL);
  const __amdgpu_buffer_rsrc_t rs = mkrsrc(ws + (size_t)(i % NPAGE) * PAGE + PROBE - (i / NPAGE) * 2048, 16384);
  const u32x2 e = __builtin_amdgcn_raw_buffer_load_b64(rsy, 0, SY_EP, 16);
  const u32x4 ds = {DSENT, DSENT, DSENT, DSENT};
  if (lane < 32) st_sc1(rs, (lane >> 4) * 8192 + w * 256 + (lane & 15) * 16, ds);
  static_assert(NPRE == 4 && SY_FEND - SY_BR <= 32 * 6144, "");
#pragma unroll
  for (int u = 0; u < 6; ++u)
    st_sc1(rsy, (u < 4 ? SY_V + (i * 4 + u) * 8192 : SY_O + (i * 2 + u - 4) * 8192) + tid * 16, ds);
  if (w == 7) {
    const int n = i * 64 + lane, j = (n >> 10) * 512 + (n & 511);
    const unsigned* m0 = reinterpret_cast<const unsigned*>(mem0 + (2 * ((j >> 2) & 3) + (j & 1)) * DM + (j >> 4) * 16) + ((j >> 1) & 1);
    u32x4 m;
#pragma unroll
    for (int k = 0; k < 4; ++k) { const unsigned t = m0[2 * k]; m[k] = t == DSENT ? 0xFFFFFFFFu : t; }
    st_sc1(rsy, n * 16, m);
  } else if (w == 6) st_sc1(rsy, 32768 + i * 1024 + lane * 16, ds);
  else {
    const int o = SY_BR + i * 6144 + tid * 16;
    st_sc1(rsy, o < SY_FEND ? o : 0x7F000000, (u32x4){SENT, SENT, SENT, SENT});
  }
  unsigned t[4];
#pragma unroll
  for (int k = 0; k < 4; ++k) {
    unsigned long long c0, c1; unsigned v, off = ((k ^ (k >> 1)) & 1) * 8192 + w * 256 + (k >> 1) * 128;
    asm volatile("s_waitcnt vmcnt(0)\n\ts_barrier\n\ts_memtime %0\n\ts_waitcnt lgkmcnt(0)\n\tbuffer_load_dword %2, %3, %4, 0 offen sc1\n\ts_waitcnt vmcnt(0)\n\t"
                 "s_memtime %1\n\ts_waitcnt lgkmcnt(0)" : "=&s"(c0), "=&s"(c1), "=&v"(v) : "v"(off), "s"(rs) : "memory");
    t[k] = (unsigned)(c1 - c0);
  }
  lds_vu_ptr tm = lds_at(smem);
  if (lane == 0) { tm[w] = min(t[0], t[3]); tm[8 + w] = min(t[1], t[2]); }
  int ro = SY_BT + i * 128 + lane * 16;
  asm volatile("" : "+v"(ro));
  LBAR();
  RDY(384 + i);
  if (threadIdx.x < 8) {
    unsigned m0 = tm[0], m1 = tm[8];
#pragma unroll
    for (int k = 1; k < 8; ++k) { m0 = min(m0, tm[k]); m1 = min(m1, tm[8 + k]); }
    const u32x2 tag = next_tag(e);
    st_sc1(rsy, ro, (u32x4){tag[0], tag[1], min(m0, 30000u) << 16 | min(m1, 30000u), 0u});
  }
}
template <int NB, typename F>
__device__ __forceinline__ u32x2 start_rows(const Args& p, int g, F&& between) {
  const int tid = threadIdx.x, lane = tid & 63, row0 = g * 16 + (tid >> 6) * 2;
  u32x4 a[4];
  u32x2 e;
  asm volatile("global_load_dwordx4 %0, %5, off\n\tglobal_load_dwordx4 %1, %5, off offset:1024\n\tglobal_load_dwordx4 %2, %5, off offset:2048\n\t"
               "global_load_dwordx4 %3, %5, off offset:3072\n\tbuffer_load_dwordx2 %4, off, %6, %7 sc1"
               : "=&v"(a[0]), "=&v"(a[1]), "=&v"(a[2]), "=&v"(a[3]), "=&v"(e)
               : "v"(p.x + (size_t)row0 * DM + lane * 8), "s"(raw_rsrc(p.ws, SY_TOTAL)), "s"(SY_EP) : "memory");
  between();
  asm volatile("s_waitcnt vmcnt(%5)" : "+v"(a[0]), "+v"(a[1]), "+v"(a[2]), "+v"(a[3]), "+v"(e) : "n"(NB) : "memory");
  RDY(512 + g);
  const __amdgpu_buffer_rsrc_t ro = mkrsrc(p.ws + WS_XN, S_LEN * XLD * 2);
#pragma unroll
  for (int u = 0; u < 2; ++u) {
    float v[16];
#pragma unroll
    for (int i = 0; i < 8; ++i) { v[2 * i] = lo_f(a[2 * u + (i >> 2)][i & 3]); v[2 * i + 1] = hi_f(a[2 * u + (i >> 2)][i & 3]); }
    float s = 0.f;
#pragma unroll
    for (int i = 0; i < 16; ++i) s += v[i];
    const float mean = wave_sum(s) * (1.0f / DM);
    float q = 0.f;
#pragma unroll
    for (int i = 0; i < 16; ++i) { float d = v[i] - mean; q += d * d; }
    const float var = rbf(wave_sum(q) * (1.0f / DM));
    const float mb = rbf(mean);
    const float inv = 1.0f / rbf(sqrtf(rbf(var + 1e-5f)));
#pragma unroll
    for (int k = 0; k < 2; ++k) {
      u32x4 o;
#pragma unroll
      for (int i = 0; i < 4; ++i) {
        const unsigned t = cvtpk(v[8 * k + 2 * i] - mb, v[8 * k + 2 * i + 1] - mb);
        o[i] = cvtpk(lo_f(t) * inv, hi_f(t) * inv);
      }
      st_sc1(ro, (row0 + u) * (XLD * 2) + k * 1024 + lane * 16, o);
    }
  }
  return next_tag(e);
}
__device__ __forceinline__ void report_rows(const Args& p, int g, u32x2 tag) {
  RDY(256 + g);
  if (threadIdx.x < 8) st_sc1(mkrsrc(p.ws + WS_SY, SY_TOTAL), SY_BT + (32 + g) * 128 + threadIdx.x * 16, (u32x4){tag[0], tag[1], tag[0], tag[1]});
}

__device__ __forceinline__ unsigned ld_flag0(__amdgpu_buffer_rsrc_t rs, int off) {
  return __builtin_amdgcn_raw_buffer_load_b32(rs, off, 0, 16);
}
__device__ __forceinline__ void glds16_sc1(const char* src, char* dst) {
  const unsigned d = __builtin_amdgcn_readfirstlane(lds_off(dst));
  unsigned keep;
  asm volatile("s_mov_b32 %0, m0\n\ts_mov_b32 m0, %2\n\ts_nop 0\n\tglobal_load_lds_dwordx4 %1, off sc1\n\ts_mov_b32 m0, %0"
               : "=&s"(keep) : "v"(src), "s"(d) : "memory");
}

__device__ __forceinline__ void touch_line(const char* src, char* dst) {
  const unsigned d = __builtin_amdgcn_readfirstlane(lds_off(dst));
  unsigned keep;
  asm volatile("s_mov_b32 %0, m0\n\ts_mov_b32 m0, %2\n\ts_nop 0\n\tglobal_load_lds_dword %1, off sc1\n\ts_mov_b32 m0, %0"
               : "=&s"(keep) : "v"(src), "s"(d) : "memory");
}

#define GPF 1
#define GU_DELAY 0
#define Q_DELAY 0
#define PF_T0 400
#define PF_N 12
#define WREV 1
__device__ __forceinline__ void glds2(const char* base, int step, unsigned voff, unsigned dst) {
  unsigned keep;
  const char* base2 = base + step;
  asm volatile("s_mov_b32 %0, m0\n\ts_mov_b32 m0, %4\n\ts_nop 0\n\tglobal_load_lds_dwordx4 %1, %2\n\t"
               "s_add_u32 m0, m0, 0x2000\n\ts_nop 0\n\tglobal_load_lds_dwordx4 %1, %3\n\ts_mov_b32 m0, %0"
               : "=&s"(keep) : "v"(voff), "s"(base), "s"(base2), "s"(dst) : "memory", "scc");
}

__device__ __forceinline__ void gemm_stream(const char* A, int lda, const char* B, int ldb, int nk, char* smem, f32x4 (&acc)[2][4]) {
  int tid = threadIdx.x;
  asm volatile("" : "+v"(tid));
  const int lane = tid & 63, r = lane & 15, q = lane >> 4;
  const int wid = __builtin_amdgcn_readfirstlane(tid >> 6);
  const int wr = wid >> 1, wc = wid & 1;
  const unsigned va = (tid >> 3) * lda + 16 * ((tid & 7) ^ ((tid >> 3) & 7)), vb = (tid >> 4) * ldb + 16 * ((tid & 15) ^ swzB((tid >> 4) & 15));
  const unsigned lw = lds_off(smem) + wid * 1024;
  auto piece = [&](int kt, int i) {
    const int k = min(kt, nk - 1);
    const char* src = i < 2 ? A + k * 128 + i * 64 * lda : B + (size_t)(k * 64 + (i - 2) * 32) * ldb;
    unsigned keep;
    asm volatile("s_mov_b32 %0, m0\n\ts_mov_b32 m0, %3\n\ts_nop 0\n\tglobal_load_lds_dwordx4 %1, %2\n\ts_mov_b32 m0, %0"
                 : "=&s"(keep) : "v"(i < 2 ? va : vb), "s"(src), "s"(lw + (kt & 3) * 32768 + i * 8192) : "memory");
  };
  unsigned la[2], lb[4][2];
#pragma unroll
  for (int k = 0; k < 2; ++k) { la[k] = lds_off(smem) + offA(wr * 32 + r, k * 4 + q); asm volatile("" : "+v"(la[k])); }
#pragma unroll
  for (int n = 0; n < 4; ++n)
#pragma unroll
    for (int u = 0; u < 2; ++u) {
      lb[n][u] = lds_off(smem) + 16384 + offB(8 * q + 4 * u + (r >> 2), 2 * (wc * 4 + n) + ((r & 3) >> 1)) + 8 * (r & 1);
      asm volatile("" : "+v"(lb[n][u]));
    }
  typedef __attribute__((address_space(3))) const bf16x8* lds_frag_ptr;
  bf16x8 af[2][2], bf[2][4];
  auto ldA = [&](int bo, int ks, int m) { return *(lds_frag_ptr)(uintptr_t)(la[ks] + bo + m * 2048); };
  auto ldB = [&](int bo, int ks, int n) {
    s16x4 lo = __builtin_amdgcn_ds_read_tr16_b64_v4i16((lds_s16x4_ptr)(uintptr_t)(lb[n][0] + bo + ks * 8192));
    s16x4 hi = __builtin_amdgcn_ds_read_tr16_b64_v4i16((lds_s16x4_ptr)(uintptr_t)(lb[n][1] + bo + ks * 8192));
    return (bf16x8){lo[0], lo[1], lo[2], lo[3], hi[0], hi[1], hi[2], hi[3]};
  };
#pragma unroll
  for (int i = 0; i < 8; ++i) acc[i >> 2][i & 3] = (f32x4){0.f, 0.f, 0.f, 0.f};
  LBAR();
#pragma unroll
  for (int i = 0; i < 10; ++i) piece(i >> 2, i & 3);
  WAIT_V(6);
  LBAR();
#pragma unroll
  for (int m = 0; m < 2; ++m) af[0][m] = ldA(0, 0, m);
#pragma unroll
  for (int n = 0; n < 4; ++n) bf[0][n] = ldB(0, 0, n);
  auto loop = [&](auto HF) {
    for (int t0 = 0; t0 < nk; t0 += 4) {
#pragma unroll
      for (int b = 0; b < 4; ++b) {
        const int t = t0 + b;
#pragma unroll
        for (int ks = 0; ks < 2; ++ks) {
          if (ks == 1) {
            WAIT_V(4);
            __builtin_amdgcn_s_barrier();
          }
          const int nb = (ks ? (b + 1) & 3 : b) * 32768, nks = ks ^ 1;
#pragma unroll
          for (int n = 0; n < 4; ++n) {
            acc[0][n] = MFMA(bf[ks][n], af[ks][0], acc[0][n]);
            bf[nks][n] = ldB(nb, nks, n);
            acc[1][n] = MFMA(bf[ks][n], af[ks][1], acc[1][n]);
            if (n < 2) af[nks][n] = ldA(nb, nks, n);
            SCHED();
            if ((n & 1) == HF) { piece(t + 2 + ks, (n >> 1) + 2 * nks); SCHED(); }
          }
        }
      }
    }
  };
  if (wid >> 2) loop(std::integral_constant<int, 1>()); else loop(std::integral_constant<int, 0>());
  WAIT_V(0);
}

#define HSL 40
__device__ __forceinline__ void wait_h(const Args& p, int panel) {
  if (threadIdx.x == 0) {
    gu32* cnt = (gu32*)(p.ws + WS_SY + SY_CH) + panel * 32;
    for (unsigned spins = 0;;) {
      if (__hip_atomic_load(cnt, __ATOMIC_RELAXED, __HIP_MEMORY_SCOPE_AGENT) + 1u >= 28u) break;
      __builtin_amdgcn_s_sleep(HSL);
      if (++spins > (1u << 20)) { __hip_atomic_store((gu32*)(p.ws + WS_SY + SY_ABORT), 1u, __ATOMIC_RELAXED, __HIP_MEMORY_SCOPE_AGENT); break; }
    }
    __builtin_amdgcn_fence(__ATOMIC_ACQUIRE, "agent");
    asm volatile("s_waitcnt vmcnt(0)" ::: "memory");
  }
  __syncthreads();
}
__device__ __forceinline__ void job_down(const Args& p, int rblk, int ct, char* smem, __amdgpu_buffer_rsrc_t rsy) {
  wait_h(p, rblk >> 1);
  f32x4 acc[2][4];
  gemm_stream(p.ws + WS_HF + h_off(p, rblk), HLD, (const char*)(p.wd + ct * 128), DM * 2, FF / 64, smem, acc);
  wait_s(p, rsy, rblk, ct * 4, 4, rblk < NFUSE);
  int tid = threadIdx.x;
  asm volatile("" : "+v"(tid));
  const int lane = tid & 63, wid = tid >> 6, wr = wid >> 1, wc = wid & 1;
  const __amdgpu_buffer_rsrc_t ro = mkrsrc(p.ws + WS_G, S_LEN * DM * 2);
  SX sx[4];
#pragma unroll
  for (int i = 0; i < 4; ++i)
    sx[i] = get_sx(p, rblk, (rblk * 128 + wr * 32 + (i >> 1) * 16 + (lane & 15)) * DM + ct * 128 + wc * 64 + (i & 1) * 32 + (lane >> 4) * 8);
#pragma unroll
  for (int m = 0; m < 2; ++m) {
    const int row = rblk * 128 + wr * 32 + m * 16 + (lane & 15);
#pragma unroll
    for (int nb = 0; nb < 2; ++nb) {
      unsigned pk[2][2];
#pragma unroll
      for (int n = 0; n < 2; ++n) {
        float rr[4];
#pragma unroll
        for (int j = 0; j < 4; ++j) { float d = rbf(acc[m][2 * nb + n][j]); rr[j] = d * __builtin_amdgcn_rcpf(1.0f + __expf(-d)); }
        pk[n][0] = cvtpk(rr[0], rr[1]); pk[n][1] = cvtpk(rr[2], rr[3]);
      }
      u32x4 o = widen32(pk[0][0], pk[0][1], pk[1][0], pk[1][1]);
      put_g(p, ro, rblk, row * DM + ct * 128 + wc * 64 + nb * 32 + (lane >> 4) * 8, o, sx[2 * m + nb]);
    }
  }
  publish_flags(rsy, SY_FG + (rblk * 8 + ct) * 128, 0, 1);
}

constexpr int W_X = 131072, W_PM = W_X + 16384;
template <bool ISV>
__device__ __forceinline__ void job_proj(Args& p, int g, int which, int h, char* smem, __amdgpu_buffer_rsrc_t rsy) {
  const int tid = threadIdx.x, lane = tid & 63, r = lane & 15, q = lane >> 4;
  const int w = __builtin_amdgcn_readfirstlane(tid >> 6), kh = w >> 2, cg = w & 3;
  bf16x8 W[16][2];
  const unsigned lw = lds_off(smem) + w * 4096;
  unsigned vx[4];
  u32x2 tag;
  const char* xn = p.ws + WS_XN;
  auto row = [&](int t, int i) {
    const char* src = xn + (size_t)(min(t, 127) >> 1) * (SEG * XLD * 2) + (t & 1) * 512;
    unsigned keep;
    asm volatile("s_mov_b32 %0, m0\n\ts_mov_b32 m0, %3\n\ts_nop 0\n\tglobal_load_lds_dwordx4 %1, %2 sc1\n\ts_mov_b32 m0, %0"
                 : "=&s"(keep) : "v"(vx[i]), "s"(src), "s"(lw + (t & 3) * 32768 + i * 1024) : "memory");
  };
  {
    const u16* Wm = which == 0 ? p.Wk : p.Wq;
    asm volatile("" : "+s"(Wm));
    if (ISV) Wm = p.Wv;
    const unsigned vo = (tid >> 4) * (HD * 2) + 16 * ((lane & 15) ^ swzB((tid >> 4) & 15));
    auto stage = [&](int j) {
      const char* src = (const char*)(Wm + (size_t)(WREV ? 7 - j : j) * 64 * HD + h * DH);
      const unsigned dst = lds_off(smem) + (j & 3) * 32768 + w * 1024;
      glds2(src, 32 * HD * 2, vo, dst);
      glds2(src + 512 * HD * 2, 32 * HD * 2, vo, dst + 16384);
    };
    if (which == 1) { for (int z = 0; z < Q_DELAY; ++z) __builtin_amdgcn_s_sleep(16); }
    tag = start_rows<16>(p, 32 + g, [&]() { stage(0); stage(1); stage(2); stage(3); });
#pragma unroll
    for (int i = 0; i < 4; ++i) vx[i] = ((4 * w + i) ^ (ISV ? 0 : (lane >> 1) & 16)) * (XLD * 2) + (lane >> 5) * 1024 + (((lane & 31) ^ ((4 * w + i) & 15)) << 4);
    Boot bt;
#pragma unroll
    for (int j = 0; j < 8; ++j) {
      if (j < 4) WAIT_V(16); else if (j == 4) WAIT_V(12); else if (j == 5) WAIT_V(8); else if (j == 6) WAIT_V(4); else WAIT_V(10);
      if (j == 6 && w == 0) lds_at(smem + W_PM)[0] = boot_done(p.ws, bt, true, tag);
      LBAR();
      if (j == 4) report_rows(p, 32 + g, tag);
      const char* sb = smem + (j & 3) * 32768 + kh * 16384;
#pragma unroll
      for (int k = 0; k < 4; ++k) {
        const int ks = k >> 1, a = k & 1;
        const unsigned ch = ISV ? 2 * (2 * cg + (a ^ kh)) + ((r & 3) >> 1) : 4 * cg + (r & 3), by = ISV ? 8 * (r & 1) : 8 * a;
        s16x4 lo = __builtin_amdgcn_ds_read_tr16_b64_v4i16((lds_s16x4_ptr)(sb + offB(32 * ks + 8 * q + (r >> 2), ch) + by));
        s16x4 hi = __builtin_amdgcn_ds_read_tr16_b64_v4i16((lds_s16x4_ptr)(sb + offB(32 * ks + 8 * q + 4 + (r >> 2), ch) + by));
        W[2 * (WREV ? 7 - j : j) + ks][a] = (bf16x8){lo[0], lo[1], lo[2], lo[3], hi[0], hi[1], hi[2], hi[3]};
      }
      LBAR();
      if (j == 3 && w == 0) boot_ask(p.ws, bt);
      if (j + 4 < 8) stage(j + 4);
      if (j == 6) {
#pragma unroll
        for (int i = 0; i < 10; ++i) row(i >> 2, i & 3);
      }
    }
    p.s0 = uni(lds_at(smem + W_PM)[0] >> 31);
  }
  RDY(128 + g);
  typedef __attribute__((address_space(3))) const bf16x8* lds_frag_ptr;
  typedef __attribute__((address_space(3))) f32x4* lds_f4_ptr;
  unsigned la[2][4];
#pragma unroll
  for (int i = 0; i < 8; ++i) {
    la[i >> 2][i & 3] = lds_off(smem) + (i >> 2) * 65536 + r * 1024 + kh * 512 + (((4 * (i & 3) + q) ^ r) << 4);
    asm volatile("" : "+v"(la[i >> 2][i & 3]));
  }
  unsigned xw = lds_off(smem) + W_X + w * 2048 + lane * 16, xr = xw ^ 8192;
  asm volatile("" : "+v"(xw), "+v"(xr));
  auto ldA = [&](int b, int ks, int m) { return *(lds_frag_ptr)(uintptr_t)(la[b >> 1][ks & 3] + (b & 1) * 32768 + m * 16384 + (ks >> 2) * 256); };
  const __amdgpu_buffer_rsrc_t ro = mkrsrc(p.ws + WS_T, T_BYTES);
  const int t0 = which * T_Q + kseg(0, h), vt = (ISV ? 2 * cg + kh : 4 * kh + cg) * 1024 + lane * 16;
  f32x4 acc[2][2][2], xs[2];
  bf16x8 af[4][2];
#pragma unroll
  for (int i = 0; i < 4; ++i) acc[1][i >> 1][i & 1] = (f32x4){0.f, 0.f, 0.f, 0.f};
  auto put = [&](int sp) {
#pragma unroll
    for (int b = 0; b < 2; ++b) *(lds_f4_ptr)(uintptr_t)(xw + b * 1024) = acc[sp][1][b];
  };
  auto get = [&]() {
#pragma unroll
    for (int b = 0; b < 2; ++b) xs[b] = *(lds_f4_ptr)(uintptr_t)(xr + b * 1024);
  };
  auto fin = [&](int sp, int sg) {
    const f32x4 x = acc[sp][0][0] + xs[0], y = acc[sp][0][1] + xs[1];
    __builtin_amdgcn_raw_buffer_store_b128((u32x4){cvtpk(x[0], x[1]), cvtpk(x[2], x[3]), cvtpk(y[0], y[1]), cvtpk(y[2], y[3])}, ro,
                                           vt + (sg < 0 ? 0x70000000 : t_off(p, t0 + sg)), 0, 16);
  };
  WAIT_V(6);
  LBAR();
#pragma unroll
  for (int i = 0; i < 4; ++i) af[i >> 1][i & 1] = ldA(0, i >> 1, i & 1);
  auto loop = [&](auto KH) {
#pragma unroll 1
    for (int tb = 0; tb < 2 * NSEG; tb += 4) {
#pragma unroll
      for (int b = 0; b < 4; ++b) {
        const int sp = b >> 1, u = b & 1, sg = (tb + b) >> 1;
#pragma unroll
        for (int ks = 0; ks < 8; ++ks) {
          if (ks == 4) {
            if (KH == 0 && b == 0 && tb == 24 && w == 0) wait_rows(p.ws, tag);
            if (u) WAIT_V(5); else WAIT_V(4);
            __builtin_amdgcn_s_barrier();
            asm volatile("" ::: "memory");
            if (u == 0 && KH == 1 && tid == 256 && sg >= 2) __builtin_amdgcn_raw_buffer_store_b32(sg - 2, rsy, fkq(h, which), 0, 16);
            if (u == 0 && KH == 1 && w == 5 && sg == 2) RDW(928 + g);
            if (u == 0) get();
          }
#pragma unroll
          for (int m = 0; m < 2; ++m) {
            af[(ks + 2) & 3][m] = ldA(ks < 6 ? b : (b + 1) & 3, (ks + 2) & 7, m);
#pragma unroll
            for (int n = 0; n < 2; ++n) {
              f32x4& d = ISV ? acc[sp][n][m] : acc[sp][m][n];
              const f32x4 c = (u | ks) ? d : (f32x4){0.f, 0.f, 0.f, 0.f};
              d = ISV ? MFMA(af[ks & 3][m], W[8 * u + ks][n], c) : MFMA(W[8 * u + ks][n], af[ks & 3][m], c);
            }
          }
          if ((ks & 1) == KH) { SCHED(); row(tb + b + (ks < 4 ? 2 : 3), ((ks >> 1) + 2) & 3); }
          if (u == 0 && ks == 0) put(sp ^ 1);
          if (u == 0 && ks == 7 - KH) fin(sp ^ 1, sg - 1);
          SCHED();
        }
      }
    }
  };
  if (kh) loop(std::integral_constant<int, 1>()); else loop(std::integral_constant<int, 0>());
  put(1);
  LBAR();
  get();
  fin(1, NSEG - 1);
  WAIT_V(0);
  LBAR();
  if (tid == 0) __builtin_amdgcn_raw_buffer_store_b32(NSEG - 1, rsy, fkq(h, which), 0, 16);
}

enum { J_NONE = 0, J_GU = 3, J_PR = 5, J_CB = 8, J_DN = 14 };
struct BigJob { int type, panel, idx; };
struct BigPtr { const char *a, *b0, *b1; };
constexpr int G_LDA = XLD * 2, G_LDB = FF * 2;

__device__ __forceinline__ BigPtr big_ptrs(const Args& p, const BigJob& j) {
  BigPtr r;
  r.a = p.ws + WS_XN + (size_t)j.panel * 128 * G_LDA;
  r.b0 = (const char*)(p.wg + j.idx * 128); r.b1 = (const char*)(p.wu + j.idx * 128);
  return r;
}
__device__ __forceinline__ void big_publish(const Args& p, const BigJob& j) {
  int tid = threadIdx.x;
  asm volatile("" : "+v"(tid));
  if (j.type == J_GU && tid == 0)
    __hip_atomic_fetch_add((gu32*)(p.ws + WS_SY + SY_CH) + j.panel * 16, 1u, __ATOMIC_RELAXED, __HIP_MEMORY_SCOPE_AGENT);
}
typedef f32x4 Acc[2][2][4][2];
__device__ __forceinline__ u32x4 pack32(f32x4 a, f32x4 b) {
  return widen32(cvtpk(a[0], a[1]), cvtpk(a[2], a[3]), cvtpk(b[0], b[1]), cvtpk(b[2], b[3]));
}
#define st_epi(r, o, v) st_sc1(r, o, v)
__device__ __forceinline__ void big_epilogue(const Args& p, const BigJob& j, Acc& acc, char* smem) {
  int tid = threadIdx.x;
  asm volatile("" : "+v"(tid));
  const int lane = tid & 63;
  const int wid = __builtin_amdgcn_readfirstlane(tid >> 6);
  const int wr = wid >> 2, wc = wid & 3;
  {
    const __amdgpu_buffer_rsrc_t ro = mkrsrc(p.ws + WS_HF, S_LEN * 16384);
#pragma unroll
    for (int i = 0; i < 8; ++i) {
      const int row = wr * 64 + (i & 3) * 16 + (lane & 15);
      f32x4 hh[2];
#pragma unroll
      for (int n = 0; n < 2; ++n)
#pragma unroll
        for (int jj = 0; jj < 4; ++jj) {
          const float g = acc[i >> 2][0][i & 3][n][jj], u = acc[i >> 2][1][i & 3][n][jj];
          hh[n][jj] = __builtin_amdgcn_rcpf(1.0f + __builtin_amdgcn_exp2f(g * -1.4426950408889634f)) * u;
        }
      st_epi(ro, h_off(p, j.panel + (i >> 2)) + row * HLD + (j.idx * 128 + wc * 32 + (lane >> 4) * 8) * 2, pack32(hh[0], hh[1]));
    }
  }
}
#define JST(n, i, v) do {} while (0)
constexpr int JOBQ = 56, JOBW = 105;
__constant__ unsigned short JOBS[105] = {
  3296, 3297, 3298, 3299, 3300, 3301, 3302, 3264, 3265, 3266, 3267, 3268, 3269, 3270, 3232, 3233,
  3234, 3235, 3236, 3237, 3238, 3200, 3201, 3202, 3203, 3204, 3205, 3206, 3168, 3169, 3170, 3171,
  14384, 14385, 14386, 14387, 14388, 14389, 14390, 14391, 8384, 8400, 0, 0, 0, 0, 0, 0,
  0, 0, 0, 0, 0, 0, 0, 0, 3172, 3173, 3174, 3136, 3137, 3138, 3139, 3140,
  3141, 3142, 3104, 3105, 3106, 3107, 3108, 3109, 3110, 3072, 3073, 3074, 3075, 3076, 3077, 3078,
  14368, 14369, 14370, 14371, 14372, 14373, 14374, 14375, 14352, 14353, 14354, 14355, 14356, 14357, 14358, 14359,
  14336, 14337, 14338, 14339, 14340, 14341, 14342, 14343, 0,
};
__device__ __forceinline__ void gemm_role(Args& p, int g, char* smem) {
  const __amdgpu_buffer_rsrc_t rsy = mkrsrc(p.ws + WS_SY, SY_TOTAL);
  const int x = g & 3, k = g >> 2;
  gu32* queue = (gu32*)(p.ws + WS_SY + SY_Q) + x * 32;
  volatile unsigned* slot = reinterpret_cast<volatile unsigned*>(smem + 163824);
  unsigned nxt = k - 24;
  auto request = [&]() {
    if (k >= 24) nxt += 8;
    else if (threadIdx.x == 0) nxt = __hip_atomic_fetch_add(queue, 1u, __ATOMIC_RELAXED, __HIP_MEMORY_SCOPE_AGENT) + 1u + JOBQ;
  };
  auto post = [&]() { if (threadIdx.x == 0) *slot = nxt; };
  auto decode = [&](int j) {
    const unsigned e = JOBS[min(j, JOBW - 1)];
    const int i = e & 15;
    BigJob b;
    b.type = e >> 10; b.panel = (e >> 4) & 63;
    b.idx = x + 4 * i;
    if (b.type == J_DN) { b.idx = i; b.panel = 4 * b.panel + x; }
    return b;
  };
  int nj = 0; (void)nj;
  auto claim = [&]() {
    request();
    LBAR();
    post();
    LBAR();
    return decode((int)__builtin_amdgcn_readfirstlane(*slot));
  };
  if (g < 4 && threadIdx.x == 0) { LST(480 + 4 * g); LSTC(480 + 4 * g + 1); }
  BigJob cur;
  if (k < 24) {
    JST(nj, 0, J_PR); JST(nj, 1, __builtin_amdgcn_s_memrealtime());
    if (k >= 16) job_proj<true>(p, g, 2, x * 8 + (k & 7), smem, rsy);
    else job_proj<false>(p, g, k >> 3, x * 8 + (k & 7), smem, rsy);
    JST(nj, 3, __builtin_amdgcn_s_memrealtime()); ++nj;
    if (threadIdx.x == 0) __builtin_amdgcn_fence(__ATOMIC_ACQUIRE, "agent");
    cur = claim();
  } else {
    const unsigned t0 = (unsigned)__builtin_amdgcn_s_memrealtime();
    const u32x2 tag = start_rows<0>(p, g - 96, []() {});
    WAIT_V(0);
    LBAR();
    report_rows(p, g - 96, tag);
    while ((unsigned)__builtin_amdgcn_s_memrealtime() - t0 < PF_T0) __builtin_amdgcn_s_sleep(8);
#pragma unroll 1
    for (int i = 0; i < PF_N; ++i) {
      const int m = i % 3, o = ((i / 3) * 32 + g - 96) * 448 + (int)threadIdx.x;
      if (threadIdx.x < 448) touch_line((const char*)(m == 0 ? p.wg : (m == 1 ? p.wu : p.wd)) + (size_t)o * 128, smem + (threadIdx.x >> 6) * 256);
      WAIT_V(1);
    }
    WAIT_V(0);
    if (threadIdx.x < 64) {
      wait_rows(p.ws, tag);
      Boot bt;
      boot_ask(p.ws, bt);
      WAIT_V(0);
      *slot = boot_done(p.ws, bt, true, tag);
      __builtin_amdgcn_fence(__ATOMIC_ACQUIRE, "agent");
      WAIT_V(0);
      while ((unsigned)__builtin_amdgcn_s_memrealtime() - t0 < GU_DELAY) __builtin_amdgcn_s_sleep(8);
    }
    LBAR();
    p.s0 = *slot >> 31;
    RDY(128 + g);
    cur = decode(k - 24);
  }
  p.s0 = uni(p.s0);
  while (cur.type != J_NONE) {
    JST(nj, 0, cur.type | cur.panel << 8); JST(nj, 1, __builtin_amdgcn_s_memrealtime());
    if (cur.type != J_GU) {
      if (cur.type == J_DN) job_down(p, cur.panel, cur.idx, smem, rsy);
      else job_comb(p, cur.panel, x, rsy);
      JST(nj, 3, __builtin_amdgcn_s_memrealtime()); ++nj;
      cur = claim();
      continue;
    }
    int tid = threadIdx.x;
    asm volatile("" : "+v"(tid));
    const int lane = tid & 63;
    const int wid = __builtin_amdgcn_readfirstlane(tid >> 6);
    const int wr = wid >> 2, wc = wid & 3;
    const unsigned ra = tid >> 3, ca = 16 * ((tid & 7) ^ ((tid >> 3) & 7)), rb = tid >> 4, cb = 16 * ((tid & 15) ^ swzB((tid >> 4) & 15));
    const unsigned lw = lds_off(smem) + wid * 1024;
#define B8_SA(b, h) (((b) * 2 + (h)) * 16384)
#define B8_SB(b, h) ((4 + (b) * 2 + (h)) * 16384)
#define B8_STA(b, h, P, kt) glds2((P).a + (size_t)(h) * 128 * G_LDA + (kt) * 128, 64 * G_LDA, ra * G_LDA + ca, lw + B8_SA(b, h))
#define B8_STB(b, h, P, kt) glds2(((h) ? (P).b1 : (P).b0) + (size_t)(kt) * 64 * G_LDB, 32 * G_LDB, rb * G_LDB + cb, lw + B8_SB(b, h))
#define B8_LDA(b, h) do { _Pragma("unroll") for (int m = 0; m < 4; ++m) _Pragma("unroll") for (int kk = 0; kk < 2; ++kk) \
      At[m][kk] = ldsf(smem + B8_SA(b, h) + offA(wr * 64 + m * 16 + (lane & 15), kk * 4 + (lane >> 4))); } while (0)
#define B8_LDB(dst, b, h) do { _Pragma("unroll") for (int n = 0; n < 2; ++n) _Pragma("unroll") for (int kk = 0; kk < 2; ++kk) \
      dst[n][kk] = ld_bfrag(smem + B8_SB(b, h), kk, wc * 2 + n, lane); } while (0)
#define B8_MMA(ai, bj, Bt) do { __builtin_amdgcn_s_setprio(1); _Pragma("unroll") for (int m = 0; m < 4; ++m) _Pragma("unroll") for (int n = 0; n < 2; ++n) \
      _Pragma("unroll") for (int kk = 0; kk < 2; ++kk) acc[ai][bj][m][n] = MFMA(Bt[n][kk], At[m][kk], acc[ai][bj][m][n]); __builtin_amdgcn_s_setprio(0); } while (0)
#define WAIT_L(n) asm volatile("s_waitcnt lgkmcnt(%0)" :: "n"(n) : "memory")
#define BAR() __builtin_amdgcn_s_barrier()
#define WAIT_RX(c) asm volatile("s_waitcnt vmcnt(16)\n\ts_cmp_lg_u32 %0, 0\n\ts_cbranch_scc1 .Lrx%=\n\ts_waitcnt vmcnt(8)\n.Lrx%=:" :: "s"(c) : "memory", "scc")
    Acc acc;
    bf16x8 At[4][2], B0[2][2], B1[2][2];
    BigPtr cp = big_ptrs(p, cur);
    BigJob pend; pend.type = J_NONE; pend.panel = 0; pend.idx = 0;
    LBAR();
    B8_STB(0, 0, cp, 0); B8_STB(0, 1, cp, 0); B8_STA(0, 0, cp, 0); B8_STA(0, 1, cp, 0);
    if (wr == 1) BAR();
    WAIT_V(2); BAR();
    B8_STB(1, 0, cp, 1); B8_STA(1, 0, cp, 1); B8_STB(1, 1, cp, 1);
    WAIT_V(6); BAR();
    BigJob nx = pend;
    for (;;) {
      bool chain = false;
      BigPtr np = cp;
      constexpr int nt = DM / 64;
#pragma unroll
      for (int i = 0; i < 32; ++i) acc[i >> 4][(i >> 3) & 1][(i >> 1) & 3][i & 1] = (f32x4){0.f, 0.f, 0.f, 0.f};
#pragma unroll 1
      for (int t = 0; t < nt; t += 2) {
        const bool last = t == nt - 2;
        if (t == nt - 6) request();
        if (t == nt - 4) post();
        if (last) {
          nx = decode((int)__builtin_amdgcn_readfirstlane(*slot));
          chain = nx.type == J_GU;
          if (chain) np = big_ptrs(p, nx);
        }
        const BigPtr sp = np;
        const int t2 = last ? 0 : t + 2;
        if (t == 2) big_publish(p, pend);
        const int rx = __builtin_amdgcn_readfirstlane(t == 0 && pend.type != J_NONE);
        B8_LDB(B0, 0, 0); B8_LDB(B1, 0, 1); SCHED(); B8_LDA(0, 0); B8_STA(1, 1, cp, t + 1);
        WAIT_RX(rx);
        WAIT_L(0); BAR(); B8_MMA(0, 0, B0); B8_MMA(0, 1, B1); BAR(); SCHED();
        B8_LDA(0, 1); B8_STB(0, 0, sp, t2); B8_STB(0, 1, sp, t2); B8_STA(0, 0, sp, t2);
        WAIT_RX(rx);
        WAIT_L(0); BAR(); B8_MMA(1, 0, B0); B8_MMA(1, 1, B1); BAR(); SCHED();
        B8_LDB(B0, 1, 0); B8_LDB(B1, 1, 1); SCHED(); B8_LDA(1, 0); B8_STA(0, 1, sp, t2);
        WAIT_V(8); WAIT_L(0); BAR(); B8_MMA(0, 0, B0); B8_MMA(0, 1, B1); BAR(); SCHED();
        B8_LDA(1, 1); B8_STB(1, 0, sp, t2 + 1); B8_STB(1, 1, sp, t2 + 1); B8_STA(1, 0, sp, t2 + 1);
        WAIT_V(8); WAIT_L(0); BAR(); B8_MMA(1, 0, B0); B8_MMA(1, 1, B1); BAR(); SCHED();
      }
      if (wr == 0) BAR();
      JST(nj, 2, __builtin_amdgcn_s_memrealtime());
      big_epilogue(p, cur, acc, smem);
      if (!chain) break;
      JST(nj, 3, __builtin_amdgcn_s_memrealtime()); ++nj;
      pend = cur; cur = nx; cp = np;
      if (wr == 1) BAR();
      JST(nj, 0, cur.type | cur.panel << 8); JST(nj, 1, __builtin_amdgcn_s_memrealtime());
    }
    WAIT_V(0);
    BAR();
    asm volatile(".Lcode_end:");
    big_publish(p, cur);
    JST(nj, 3, __builtin_amdgcn_s_memrealtime()); ++nj;
    cur = nx;
  }
  if (threadIdx.x == 0) { LST(128 + g); }
  if (g < 4 && threadIdx.x == 0) { LST(480 + 4 * g + 2); LSTC(480 + 4 * g + 3); }
}

__device__ __forceinline__ unsigned now_q() { return (unsigned)__builtin_amdgcn_s_memrealtime() * 4u; }
__device__ __forceinline__ void pace_until(unsigned t) {
#pragma unroll 1
  for (int i = 0; i < 256; ++i) {
    const int d = (int)(t - now_q());
    if (d <= 0) break;
    if (d > 32) __builtin_amdgcn_s_sleep(1);
  }
}
#define CLB_E "s76", "s77", "s78", "s79", "s80", "s81", "s82", "s83", "s84", "s85", "s86", "s87", "s88", "s89"
#define EDGE_ASM \
  "s_memtime s[76:77]\n\t" \
  "s_memrealtime s[78:79]\n\t" \
  "s_nop 3\n\t" \
  "s_memrealtime s[80:81]\n\t" \
  "s_nop 3\n\t" \
  "s_memrealtime s[82:83]\n\t" \
  "s_nop 3\n\t" \
  "s_memrealtime s[84:85]\n\t" \
  "s_nop 3\n\t" \
  "s_memrealtime s[86:87]\n\t" \
  "s_nop 3\n\t" \
  "s_memrealtime s[88:89]\n\t" \
  "s_mov_b32 s93, 0\n\t" \
  "s_waitcnt lgkmcnt(0)\n\t" \
  "s_cmp_eq_u32 s80, s78\n\t" \
  "s_addc_u32 s93, s93, 0\n\t" \
  "s_cmp_eq_u32 s82, s78\n\t" \
  "s_addc_u32 s93, s93, 0\n\t" \
  "s_cmp_eq_u32 s84, s78\n\t" \
  "s_addc_u32 s93, s93, 0\n\t" \
  "s_cmp_eq_u32 s86, s78\n\t" \
  "s_addc_u32 s93, s93, 0\n\t" \
  "s_cmp_eq_u32 s88, s78\n\t" \
  "s_addc_u32 s93, s93, 0\n\t" \
  "s_mul_i32 s93, s93, 21\n\t" \
  "s_add_u32 s76, s76, 15\n\t" \
  "s_add_u32 %[kc], s76, s93\n\t" \
  "s_lshl2_add_u32 %[kq], s78, 16\n\t"
__device__ __forceinline__ unsigned time_of(unsigned cyc) {
  unsigned c, q;
  asm volatile(EDGE_ASM : [kc] "=&s"(c), [kq] "=&s"(q) :: CLB_E, "s93", "scc");
  return q - (((c - cyc) * 11900u) >> 16);
}
#define DS_RED 58
#define DS_MEM 30
#define LEAD_DN 2
#define LEAD_UP 12
__device__ __forceinline__ u32x2 ld2_sc1(__amdgpu_buffer_rsrc_t rs, int off, int soff = 0) {
  return __builtin_amdgcn_raw_buffer_load_b64(rs, off, soff, 16);
}
__device__ __forceinline__ void st2_sc1(__amdgpu_buffer_rsrc_t rs, int off, u32x2 v) {
  __builtin_amdgcn_raw_buffer_store_b64(v, rs, off, 0, 16);
}
__device__ __forceinline__ int row_max(int v) {
  v = max(v, __builtin_amdgcn_update_dpp(0, v, 0x128, 0xf, 0xf, false));
  v = max(v, __builtin_amdgcn_update_dpp(0, v, 0x124, 0xf, 0xf, false));
  v = max(v, __builtin_amdgcn_update_dpp(0, v, 0x122, 0xf, 0xf, false));
  v = max(v, __builtin_amdgcn_update_dpp(0, v, 0x121, 0xf, 0xf, false));
  return v;
}
__device__ __forceinline__ int wave_max(int v) {
  v = row_max(v);
  return max(max(__builtin_amdgcn_readlane(v, 0), __builtin_amdgcn_readlane(v, 16)),
             max(__builtin_amdgcn_readlane(v, 32), __builtin_amdgcn_readlane(v, 48)));
}
__device__ __forceinline__ int track(int est, int x, int up, int dn, int out) {
  const int d = x - est;
  return est + (d > out ? 2 : max(min(d, up), -dn));
}
#define WSL 12
#define PLAN_SLEEP 2
__device__ __forceinline__ unsigned latest(unsigned t) {
  const unsigned t0 = __builtin_amdgcn_readfirstlane(t);
  return t0 + wave_max((int)(t - t0));
}
__device__ __forceinline__ void watch(__amdgpu_buffer_rsrc_t rsy, int s, int lane, unsigned t0, const unsigned* tgt_, gu32* abortw, int hs) {
  pace_until(t0);
  u32x2 ra;
  for (unsigned spins = 0;;) {
    asm volatile("" ::: "memory");
    ra = ld2_sc1(rsy, lane * 128, SY_BR + hs);
    const bool all = __all((int)ra[0] >= s);
    SMP(4, s, spins);
    if (all) break;
    if (spins < 3) __builtin_amdgcn_s_sleep(PLAN_SLEEP); else __builtin_amdgcn_s_sleep(WSL);
    if (spin_fail(spins, abortw)) break;
  }
  const unsigned t = latest(ra[0] == (unsigned)s ? ra[1] : t0) + DS_MEM;
  lds_vu_ptr tgt = lds_at(tgt_);
  if (lane < 2) tgt[lane] = lane ? t : s + 1;
  SMP(0, s, (int)(t - t0)); SMP(1, s, (int)(t - now_q()));
}
__device__ __forceinline__ unsigned ld_flag(__amdgpu_buffer_rsrc_t rs, int off) {
  return __builtin_amdgcn_raw_buffer_load_b32(rs, off, 0, 16);
}

constexpr unsigned long long QK_MAP[8] = {0xbaf9876d543210ecull, 0xa98f76d543210bceull, 0x98f76d543210ecbaull, 0x876fd543210bcea9ull, 0x76fd543210ecba98ull, 0x65df43210bcea987ull, 0x5df43210ecba9876ull, 0xd43f210bcea98765ull};
#define QK_ASM_0 \
  "s_waitcnt lgkmcnt(0)\n\t" \
  "v_subrev_u32 v244, 0x8000, v250\n\t" \
  "ds_read_b128 v[216:219], v248 offset:32768\n\t" \
  "ds_read_b128 v[220:223], v248 offset:33792\n\t" \
  "ds_read_b128 v[224:227], v248 offset:34816\n\t" \
  "ds_read_b128 v[228:231], v248 offset:35840\n\t" \
  "ds_read_b128 v[208:211], v244 offset:0\n\t" \
  "ds_read_b128 v[212:215], v244 offset:1024\n\t" \
  "ds_read_b128 v[236:239], v244 offset:2048\n\t" \
  "ds_read_b128 v[240:243], v244 offset:3072\n\t" \
  "v_mov_b64 v[192:193], 0\n\t" \
  "v_mov_b64 v[194:195], 0\n\t" \
  "v_mov_b64 v[196:197], 0\n\t" \
  "v_mov_b64 v[198:199], 0\n\t" \
  "s_movk_i32 s92, 0x2000\n\t" \
  ".Lwt%=:\n\t" \
  "ds_read_b64 v[200:201], v248 offset:30720\n\t" \
  "s_memrealtime s[90:91]\n\t" \
  "s_sub_u32 s92, s92, 1\n\t" \
  "s_waitcnt lgkmcnt(0)\n\t" \
  "v_readfirstlane_b32 s93, v200\n\t" \
  "v_readfirstlane_b32 %[tgt], v201\n\t" \
  "s_cbranch_scc1 .Lgo%=\n\t" \
  "s_cmp_eq_u32 s93, %[step]\n\t" \
  "s_cbranch_scc1 .Lwx%=\n\t" \
  "s_sleep 1\n\t" \
  "s_branch .Lwt%=\n\t" \
  ".Lwx%=:\n\t" \
  "s_lshl_b32 s93, s90, 2\n\t" \
  "s_sub_i32 s93, %[tgt], s93\n\t" \
  "s_cmp_le_i32 s93, 0\n\t" \
  "s_cbranch_scc1 .Lgo%=\n\t" \
  "s_min_i32 s93, s93, 8000\n\t" \
  "s_add_u32 s93, s93, 3\n\t" \
  "s_lshr_b32 s93, s93, 2\n\t" \
  "s_add_u32 s93, s93, s90\n\t" \
  "s_memrealtime s[90:91]\n\t" \
  ".Lpace%=:\n\t" \
  "s_memrealtime s[94:95]\n\t" \
  "s_waitcnt lgkmcnt(1)\n\t" \
  "s_sub_i32 s91, s90, s93\n\t" \
  "s_cmp_ge_i32 s91, 0\n\t" \
  "s_cbranch_scc1 .Lgo%=\n\t" \
  "s_memrealtime s[90:91]\n\t" \
  "s_waitcnt lgkmcnt(1)\n\t" \
  "s_sub_i32 s95, s94, s93\n\t" \
  "s_cmp_lt_i32 s95, 0\n\t" \
  "s_cbranch_scc1 .Lpace%=\n\t" \
  ".Lgo%=:\n\t" \
  "s_mov_b32 s92, 0\n\t" \
  ".Lpoll%=:\n\t" \
  "buffer_load_dwordx4 v[200:203], v248, %[rs], %[som] offen sc1\n\t" \
  "buffer_load_dwordx4 v[204:207], v248, %[rs], %[som2] offen sc1\n\t" \
  "s_waitcnt vmcnt(1) lgkmcnt(0)\n\t" \
  "ds_write_b128 v249, v[200:203]\n\t" \
  "v_smfmac_f32_16x16x64_bf16 v[192:195], v[200:203], v[208:215], v251\n\t" \
  "v_max3_u32 v244, v200, v201, v202\n\t" \
  "v_smfmac_f32_16x16x64_bf16 v[196:199], v[200:203], v[236:243], v251\n\t" \
  "s_waitcnt vmcnt(0)\n\t" \
  "ds_write_b128 v249, v[204:207] offset:8192\n\t" \
  "v_smfmac_f32_16x16x64_bf16 v[192:195], v[204:207], v[216:223], v251\n\t" \
  "v_max3_u32 v244, v244, v203, v204\n\t" \
  "v_max3_u32 v244, v244, v205, v206\n\t" \
  "v_smfmac_f32_16x16x64_bf16 v[196:199], v[204:207], v[224:231], v251\n\t" \
  "v_max_u32 v244, v244, v207\n\t" \
  "v_cmp_le_u32 vcc, %[sent], v244\n\t" \
  "s_cbranch_vccnz .Lslow%=\n\t" \
  ".Lok%=:\n\t" \
  "ds_read_b128 v[216:219], v250 offset:32768\n\t" \
  "ds_read_b128 v[220:223], v250 offset:33792\n\t" \
  "ds_read_b128 v[224:227], v250 offset:34816\n\t" \
  "ds_read_b128 v[228:231], v250 offset:35840\n\t" \
  "s_waitcnt lgkmcnt(4)\n\t" \
  "s_barrier\n\t" \
  "ds_read_b128 v[208:211], v248 offset:1024\n\t" \
  "ds_read_b128 v[212:215], v248 offset:2048\n\t" \
  "ds_read_b128 v[236:239], v248 offset:3072\n\t" \
  "ds_read_b128 v[240:243], v248 offset:4096\n\t" \
  "s_waitcnt lgkmcnt(3)\n\t" \
  "ds_read_b128 v[244:247], v248 offset:13312\n\t" \
  "v_smfmac_f32_16x16x64_bf16 v[192:195], v[208:211], v[216:223], v251\n\t" \
  "ds_read_b128 v[216:219], v250 offset:0\n\t" \
  "v_smfmac_f32_16x16x64_bf16 v[196:199], v[208:211], v[224:231], v251\n\t" \
  "s_waitcnt lgkmcnt(4)\n\t" \
  "ds_read_b128 v[220:223], v250 offset:1024\n\t" \
  "v_smfmac_f32_16x16x64_bf16 v[192:195], v[212:215], v[0:7], v251\n\t" \
  "ds_read_b128 v[224:227], v250 offset:2048\n\t" \
  "v_smfmac_f32_16x16x64_bf16 v[196:199], v[212:215], v[8:15], v251\n\t" \
  "s_waitcnt lgkmcnt(5)\n\t" \
  "ds_read_b128 v[228:231], v250 offset:3072\n\t" \
  "v_smfmac_f32_16x16x64_bf16 v[192:195], v[236:239], v[16:23], v251\n\t" \
  "ds_read_b128 v[200:203], v248 offset:5120\n\t" \
  "v_smfmac_f32_16x16x64_bf16 v[196:199], v[236:239], v[24:31], v251\n\t" \
  "s_waitcnt lgkmcnt(6)\n\t" \
  "ds_read_b128 v[204:207], v248 offset:6144\n\t" \
  "v_smfmac_f32_16x16x64_bf16 v[192:195], v[240:243], v[32:39], v251\n\t" \
  "ds_read_b128 v[208:211], v248 offset:7168\n\t" \
  "v_smfmac_f32_16x16x64_bf16 v[196:199], v[240:243], v[40:47], v251\n\t" \
  "s_waitcnt lgkmcnt(5)\n\t" \
  "ds_read_b128 v[212:215], v248 offset:9216\n\t" \
  "v_smfmac_f32_16x16x64_bf16 v[192:195], v[244:247], v[216:223], v251\n\t" \
  "s_waitcnt lgkmcnt(4)\n\t" \
  "v_smfmac_f32_16x16x64_bf16 v[196:199], v[244:247], v[224:231], v251\n\t" \
  "s_waitcnt lgkmcnt(3)\n\t" \
  "ds_read_b128 v[236:239], v248 offset:10240\n\t" \
  "v_smfmac_f32_16x16x64_bf16 v[192:195], v[200:203], v[48:55], v251\n\t" \
  "ds_read_b128 v[240:243], v248 offset:11264\n\t" \
  "v_smfmac_f32_16x16x64_bf16 v[196:199], v[200:203], v[56:63], v251\n\t" \
  "s_waitcnt lgkmcnt(4)\n\t" \
  "v_smfmac_f32_16x16x64_bf16 v[192:195], v[204:207], v[64:71], v251\n\t" \
  "ds_read_b128 v[244:247], v248 offset:12288\n\t" \
  "v_smfmac_f32_16x16x64_bf16 v[196:199], v[204:207], v[72:79], v251\n\t" \
  "s_waitcnt lgkmcnt(4)\n\t" \
  "v_smfmac_f32_16x16x64_bf16 v[192:195], v[208:211], v[80:87], v251\n\t" \
  "ds_read_b128 v[200:203], v248 offset:14336\n\t" \
  "v_smfmac_f32_16x16x64_bf16 v[196:199], v[208:211], v[88:95], v251\n\t" \
  "s_waitcnt lgkmcnt(4)\n\t" \
  "v_smfmac_f32_16x16x64_bf16 v[192:195], v[212:215], v[96:103], v251\n\t" \
  "ds_read_b128 v[204:207], v248 offset:15360\n\t" \
  "v_smfmac_f32_16x16x64_bf16 v[196:199], v[212:215], v[104:111], v251\n\t" \
  "s_waitcnt lgkmcnt(3)\n\t" \
  "v_smfmac_f32_16x16x64_bf16 v[192:195], v[236:239], v[112:119], v251\n\t" \
  "v_smfmac_f32_16x16x64_bf16 v[196:199], v[236:239], v[120:127], v251\n\t" \
  "v_smfmac_f32_16x16x64_bf16 v[192:195], v[240:243], v[128:135], v251\n\t" \
  "v_smfmac_f32_16x16x64_bf16 v[196:199], v[240:243], v[136:143], v251\n\t" \
  "s_waitcnt lgkmcnt(1)\n\t" \
  "v_smfmac_f32_16x16x64_bf16 v[192:195], v[244:247], v[144:151], v251\n\t" \
  "v_smfmac_f32_16x16x64_bf16 v[196:199], v[244:247], v[152:159], v251\n\t" \
  "v_smfmac_f32_16x16x64_bf16 v[192:195], v[200:203], v[160:167], v251\n\t" \
  "v_smfmac_f32_16x16x64_bf16 v[196:199], v[200:203], v[168:175], v251\n\t" \
  "s_waitcnt lgkmcnt(0)\n\t" \
  "v_smfmac_f32_16x16x64_bf16 v[192:195], v[204:207], v[176:183], v251\n\t" \
  "v_smfmac_f32_16x16x64_bf16 v[196:199], v[204:207], v[184:191], v251\n\t" \
  "ds_read_b128 v[200:203], v248 offset:16384\n\t" \
  "ds_read_b128 v[204:207], v248 offset:20480\n\t" \
  "v_mov_b32 v250, v232\n\t" \
  "v_mov_b32 v249, v233\n\t" \
  "s_nop 2\n\t" \
  "v_add_f32 v192, v192, v194\n\t" \
  "v_add_f32 v193, v193, v195\n\t" \
  "v_add_f32 v196, v196, v198\n\t" \
  "v_add_f32 v197, v197, v199\n\t" \
  "v_cvt_pk_bf16_f32 v192, v192, v193\n\t" \
  "v_cvt_pk_bf16_f32 v196, v196, v197\n\t" \
  "ds_write2_b32 v252, v192, v196 offset1:64\n\t" \
  "v_mov_b32 v251, v234\n\t" \
  "ds_read_b64_tr_b16 v[232:233], v250 offset:0\n\t" \
  "ds_read_b64_tr_b16 v[234:235], v250 offset:64\n\t" \
  "v_mov_b32 v212, %[step]\n\t" \
  "s_mov_b64 exec, 1\n\t" \
  "ds_write_b32 v248, v212 offset:28672\n\t" \
  "s_mov_b64 exec, -1\n\t" \
  "buffer_load_dwordx4 v[192:195], v248, %[rt], %[svt] offen offset:0 sc1\n\t" \
  "buffer_load_dwordx4 v[196:199], v248, %[rt], %[svt] offen offset:1024 sc1\n\t" \
  "s_waitcnt lgkmcnt(1)\n\t" \
  "v_mfma_f32_16x16x32_bf16 v[200:203], v[200:203], v[232:235], 0\n\t" \
  "v_mfma_f32_16x16x32_bf16 v[204:207], v[204:207], v[232:235], 0\n\t" \
  "s_nop 7\n\t" \
  "v_cndmask_b32 v216, v200, v204, %[nh]\n\t" \
  "v_cndmask_b32 v217, v201, v205, %[nh]\n\t" \
  "v_cndmask_b32 v218, v202, v206, %[nh]\n\t" \
  "v_cndmask_b32 v219, v203, v207, %[nh]\n\t" \
  "ds_write_b128 v248, v[216:219] offset:16384\n\t" \
  "buffer_load_dword v252, v249, %[rs], %[sv] offen offset:0 sc1\n\t" \
  "buffer_load_dword v253, v249, %[rs], %[sv] offen offset:256 sc1\n\t" \
  "ds_read_b32 v212, v248 offset:28672\n\t" \
  "ds_read_b64_tr_b16 v[236:237], v250 offset:512\n\t" \
  "ds_read_b64_tr_b16 v[238:239], v250 offset:576\n\t" \
  "ds_read_b64_tr_b16 v[240:241], v250 offset:1024\n\t" \
  "ds_read_b64_tr_b16 v[242:243], v250 offset:1088\n\t" \
  "ds_read_b64_tr_b16 v[244:245], v250 offset:1536\n\t" \
  "ds_read_b64_tr_b16 v[246:247], v250 offset:1600\n\t" \
  "s_setprio 3\n\t" \
  "s_waitcnt lgkmcnt(0)\n\t" \
  "v_cmp_ne_u32 vcc, %[step], v212\n\t" \
  "s_barrier\n\t" \
  "s_and_b32 s93, vcc_lo, 0xf\n\t" \
  "s_cbranch_scc1 .Lre%=\n\t" \
  ".Lrb%=:\n\t" \
  "ds_read_b128 v[220:223], v248 offset:17408\n\t" \
  "ds_read_b128 v[224:227], v248 offset:18432\n\t" \
  "ds_read_b128 v[228:231], v248 offset:19456\n\t" \
  "ds_read_b64_tr_b16 v[200:201], v250 offset:2048\n\t" \
  "ds_read_b64_tr_b16 v[202:203], v250 offset:2112\n\t" \
  "ds_read_b64_tr_b16 v[204:205], v250 offset:2560\n\t" \
  "ds_read_b64_tr_b16 v[206:207], v250 offset:2624\n\t" \
  "ds_read_b64_tr_b16 v[208:209], v250 offset:3072\n\t" \
  "ds_read_b64_tr_b16 v[210:211], v250 offset:3136\n\t" \
  "ds_read_b64_tr_b16 v[212:213], v250 offset:3584\n\t" \
  "ds_read_b64_tr_b16 v[214:215], v250 offset:3648\n\t" \
  "s_waitcnt lgkmcnt(10)\n\t" \
  "v_pk_add_f32 v[216:217], v[216:217], v[220:221]\n\t" \
  "v_pk_add_f32 v[218:219], v[218:219], v[222:223]\n\t" \
  "s_waitcnt lgkmcnt(8)\n\t" \
  "v_pk_add_f32 v[224:225], v[224:225], v[228:229]\n\t" \
  "v_pk_add_f32 v[226:227], v[226:227], v[230:231]\n\t" \
  "v_pk_add_f32 v[216:217], v[216:217], v[224:225]\n\t" \
  "v_pk_add_f32 v[218:219], v[218:219], v[226:227]\n\t" \
  "s_waitcnt lgkmcnt(6)\n\t" \
  "v_mfma_f32_16x16x32_bf16 v[200:203], v[200:203], v[232:235], 0\n\t" \
  "v_max3_f32 v224, v216, v217, v218\n\t" \
  "v_max_f32 v224, v224, v219\n\t" \
  "s_waitcnt lgkmcnt(4)\n\t" \
  "v_mfma_f32_16x16x32_bf16 v[200:203], v[204:207], v[236:239], v[200:203]\n\t" \
  "v_max_f32_dpp v225, v224, v224 row_ror:8 row_mask:0xf bank_mask:0xf\n\t" \
  "v_max_f32_dpp v226, v224, v224 row_ror:8 row_mask:0xf bank_mask:0xf\n\t" \
  "s_waitcnt lgkmcnt(2)\n\t" \
  "v_mfma_f32_16x16x32_bf16 v[200:203], v[208:211], v[240:243], v[200:203]\n\t" \
  "v_permlane32_swap_b32 v225, v226\n\t" \
  "s_waitcnt lgkmcnt(0)\n\t" \
  "v_mfma_f32_16x16x32_bf16 v[200:203], v[212:215], v[244:247], v[200:203]\n\t" \
  "v_max_f32 v231, v225, v226\n\t" \
  "v_mov_b32 v220, 0x3f803f80\n\t" \
  "v_mov_b32 v221, 0x3f803f80\n\t" \
  "v_mov_b64 v[222:223], v[220:221]\n\t" \
  "v_mov_b32 v233, 0\n\t" \
  "v_mov_b32 v234, 0\n\t" \
  "s_nop 1\n\t" \
  "v_max3_f32 v204, v200, v201, v231\n\t" \
  "v_max3_f32 v205, v204, v202, v203\n\t" \
  "v_max3_f32 v206, v204, v202, v203\n\t" \
  "v_cndmask_b32 v200, v200, v202, %[up]\n\t" \
  "v_cndmask_b32 v201, v201, v203, %[up]\n\t" \
  "v_permlane16_swap_b32 v205, v206\n\t" \
  "v_max_f32 v204, v205, v206\n\t" \
  "v_mul_f32 v204, %[negc], v204\n\t" \
  "v_fma_f32 v200, v200, %[c], v204\n\t" \
  "v_fma_f32 v201, v201, %[c], v204\n\t" \
  "v_exp_f32 v200, v200\n\t" \
  "v_fma_f32 v216, v216, %[c], v204\n\t" \
  "v_exp_f32 v201, v201\n\t" \
  "v_fma_f32 v217, v217, %[c], v204\n\t" \
  "v_exp_f32 v216, v216\n\t" \
  "v_fma_f32 v218, v218, %[c], v204\n\t" \
  "v_cvt_pk_bf16_f32 v232, v200, v201\n\t" \
  "v_cvt_pk_bf16_f32 v235, v200, v201\n\t" \
  "v_exp_f32 v217, v217\n\t" \
  "v_fma_f32 v219, v219, %[c], v204\n\t" \
  "v_mfma_f32_16x16x16_bf16 v[212:215], v[220:221], v[232:233], 0\n\t" \
  "v_exp_f32 v218, v218\n\t" \
  "s_waitcnt vmcnt(0)\n\t" \
  "v_mfma_f32_16x16x16_bf16 v[224:227], v[252:253], v[232:233], 0\n\t" \
  "v_cvt_pk_bf16_f32 v210, v216, v217\n\t" \
  "v_cvt_pk_bf16_f32 v208, v216, v217\n\t" \
  "v_exp_f32 v219, v219\n\t" \
  "v_mfma_f32_16x16x16_bf16 v[228:231], v[252:253], v[234:235], 0\n\t" \
  "v_cvt_pk_bf16_f32 v211, v218, v219\n\t" \
  "v_cvt_pk_bf16_f32 v209, v218, v219\n\t" \
  "v_mov_b32_dpp v208, v210 row_ror:8 row_mask:0xf bank_mask:0xc\n\t" \
  "v_mov_b32_dpp v210, v210 row_ror:8 row_mask:0xf bank_mask:0x3\n\t" \
  "v_mov_b32_dpp v209, v211 row_ror:8 row_mask:0xf bank_mask:0xc\n\t" \
  "v_mov_b32_dpp v211, v211 row_ror:8 row_mask:0xf bank_mask:0x3\n\t" \
  "v_max_u32 v204, v252, v253\n\t" \
  "v_cmp_le_u32 vcc, %[sent], v204\n\t" \
  "v_mfma_f32_16x16x32_bf16 v[212:215], v[220:223], v[208:211], v[212:215]\n\t" \
  ".Lpv%=:\n\t" \
  "v_mfma_f32_16x16x32_bf16 v[224:227], v[192:195], v[208:211], v[224:227]\n\t" \
  "v_mfma_f32_16x16x32_bf16 v[228:231], v[196:199], v[208:211], v[228:231]\n\t" \
  "s_cbranch_vccnz .Lvs%=\n\t" \
  ".Lvok%=:\n\t" \
  "s_nop 4\n\t" \
  "v_rcp_f32 v204, v212\n\t" \
  "v_cndmask_b32 v224, v224, v226, %[nh]\n\t" \
  "v_cndmask_b32 v225, v225, v227, %[nh]\n\t" \
  "v_mul_f32 v224, v224, v204\n\t" \
  "v_mul_f32 v225, v225, v204\n\t" \
  "v_cvt_pk_bf16_f32 v224, v224, v225\n\t" \
  "buffer_store_dword v224, v251, %[rs], %[so] offen offset:0 sc1\n\t" \
  "v_cndmask_b32 v228, v228, v230, %[nh]\n\t" \
  "v_cndmask_b32 v229, v229, v231, %[nh]\n\t" \
  "v_mul_f32 v228, v228, v204\n\t" \
  "v_mul_f32 v229, v229, v204\n\t" \
  "v_cvt_pk_bf16_f32 v228, v228, v229\n\t" \
  "buffer_store_dword v228, v251, %[rs], %[so] offen offset:256 sc1\n\t" \
  "s_memtime s[90:91]\n\t" \
  "s_setprio 0\n\t" \
  "s_waitcnt lgkmcnt(0)\n\t" \
  "s_mov_b32 %[tout], s90\n\t" \
  "s_mov_b32 %[spins], s92\n\t" \
  "s_branch .Lend%=\n\t" \
  ".Lre%=:\n\t" \
  "ds_read_b64_tr_b16 v[236:237], v250 offset:512\n\t" \
  "ds_read_b64_tr_b16 v[238:239], v250 offset:576\n\t" \
  "ds_read_b64_tr_b16 v[240:241], v250 offset:1024\n\t" \
  "ds_read_b64_tr_b16 v[242:243], v250 offset:1088\n\t" \
  "ds_read_b64_tr_b16 v[244:245], v250 offset:1536\n\t" \
  "ds_read_b64_tr_b16 v[246:247], v250 offset:1600\n\t" \
  "s_waitcnt lgkmcnt(0)\n\t" \
  "s_branch .Lrb%=\n\t" \
  ".Lvs%=:\n\t" \
  "v_cmp_eq_u32 vcc, %[sent], v252\n\t" \
  "v_cmp_eq_u32 s[94:95], %[sent], v253\n\t" \
  "s_or_b64 s[94:95], s[94:95], vcc\n\t" \
  "s_cbranch_scc0 .Lvok%=\n\t" \
  "s_add_u32 s92, s92, 1\n\t" \
  "s_sleep 2\n\t" \
  "s_and_b32 s93, s92, 63\n\t" \
  "s_cbranch_scc1 .Lvr%=\n\t" \
  "buffer_load_dword v204, off, %[rs], %[sab] sc1\n\t" \
  "s_waitcnt vmcnt(0)\n\t" \
  "v_readfirstlane_b32 s93, v204\n\t" \
  "s_nop 3\n\t" \
  "s_cmp_eq_u32 s93, 1\n\t" \
  "s_cbranch_scc1 .Lvok%=\n\t" \
  "s_cmp_lt_u32 s92, 0x80000\n\t" \
  "s_cbranch_scc1 .Lvr%=\n\t" \
  "v_mov_b32 v204, 1\n\t" \
  "buffer_store_dword v204, off, %[rs], %[sab] sc1\n\t" \
  "s_branch .Lvok%=\n\t" \
  ".Lvr%=:\n\t" \
  "buffer_load_dword v252, v249, %[rs], %[sv] offen offset:0 sc1\n\t" \
  "buffer_load_dword v253, v249, %[rs], %[sv] offen offset:256 sc1\n\t" \
  "s_waitcnt vmcnt(0)\n\t" \
  "v_mfma_f32_16x16x16_bf16 v[224:227], v[252:253], v[232:233], 0\n\t" \
  "v_mfma_f32_16x16x16_bf16 v[228:231], v[252:253], v[234:235], 0\n\t" \
  "v_max_u32 v204, v252, v253\n\t" \
  "v_cmp_le_u32 vcc, %[sent], v204\n\t" \
  "s_nop 4\n\t" \
  "s_branch .Lpv%=\n\t" \
  "s_branch .Lend%=\n\t" \
  ".Lslow%=:\n\t" \
  "s_mov_b64 s[94:95], 0\n\t" \
  "v_cmp_eq_u32 vcc, %[sent], v200\n\t" \
  "s_or_b64 s[94:95], s[94:95], vcc\n\t" \
  "v_cmp_eq_u32 vcc, %[sent], v201\n\t" \
  "s_or_b64 s[94:95], s[94:95], vcc\n\t" \
  "v_cmp_eq_u32 vcc, %[sent], v202\n\t" \
  "s_or_b64 s[94:95], s[94:95], vcc\n\t" \
  "v_cmp_eq_u32 vcc, %[sent], v203\n\t" \
  "s_or_b64 s[94:95], s[94:95], vcc\n\t" \
  "v_cmp_eq_u32 vcc, %[sent], v204\n\t" \
  "s_or_b64 s[94:95], s[94:95], vcc\n\t" \
  "v_cmp_eq_u32 vcc, %[sent], v205\n\t" \
  "s_or_b64 s[94:95], s[94:95], vcc\n\t" \
  "v_cmp_eq_u32 vcc, %[sent], v206\n\t" \
  "s_or_b64 s[94:95], s[94:95], vcc\n\t" \
  "v_cmp_eq_u32 vcc, %[sent], v207\n\t" \
  "s_or_b64 s[94:95], s[94:95], vcc\n\t" \
  "s_cmp_eq_u64 s[94:95], 0\n\t" \
  "s_cbranch_scc1 .Lok%=\n\t" \
  "v_mov_b64 v[192:193], 0\n\t" \
  "v_mov_b64 v[194:195], 0\n\t" \
  "v_mov_b64 v[196:197], 0\n\t" \
  "v_mov_b64 v[198:199], 0\n\t" \
  "s_add_u32 s92, s92, 1\n\t" \
  "s_sleep 6\n\t" \
  "s_cmp_eq_u32 s92, 1\n\t" \
  "s_cbranch_scc1 .Lpoll%=\n\t" \
  "s_sleep 1\n\t" \
  "s_and_b32 s93, s92, 63\n\t" \
  "s_cmp_lg_u32 s93, 0\n\t" \
  "s_cbranch_scc1 .Lpoll%=\n\t" \
  "v_mov_b32 v245, 0\n\t" \
  "buffer_load_dword v244, v245, %[rs], %[sab] offen sc1\n\t" \
  "s_waitcnt vmcnt(0)\n\t" \
  "v_readfirstlane_b32 s93, v244\n\t" \
  "s_nop 3\n\t" \
  "s_cmp_eq_u32 s93, 1\n\t" \
  "s_cbranch_scc1 .Lok%=\n\t" \
  "s_cmp_lt_u32 s92, 0x80000\n\t" \
  "s_cbranch_scc1 .Lpoll%=\n\t" \
  "v_mov_b32 v244, 1\n\t" \
  "buffer_store_dword v244, v245, %[rs], %[sab] offen sc1\n\t" \
  "s_branch .Lok%=\n\t" \
  ".Lend%=:\n\t"
#define QK_ASM_1 \
  "s_waitcnt lgkmcnt(0)\n\t" \
  "v_subrev_u32 v244, 0x8000, v250\n\t" \
  "ds_read_b128 v[216:219], v248 offset:36864\n\t" \
  "ds_read_b128 v[220:223], v248 offset:37888\n\t" \
  "ds_read_b128 v[224:227], v248 offset:38912\n\t" \
  "ds_read_b128 v[228:231], v248 offset:39936\n\t" \
  "ds_read_b128 v[208:211], v244 offset:0\n\t" \
  "ds_read_b128 v[212:215], v244 offset:1024\n\t" \
  "ds_read_b128 v[236:239], v244 offset:2048\n\t" \
  "ds_read_b128 v[240:243], v244 offset:3072\n\t" \
  "v_mov_b64 v[192:193], 0\n\t" \
  "v_mov_b64 v[194:195], 0\n\t" \
  "v_mov_b64 v[196:197], 0\n\t" \
  "v_mov_b64 v[198:199], 0\n\t" \
  "s_movk_i32 s92, 0x2000\n\t" \
  ".Lwt%=:\n\t" \
  "ds_read_b64 v[200:201], v248 offset:30720\n\t" \
  "s_memrealtime s[90:91]\n\t" \
  "s_sub_u32 s92, s92, 1\n\t" \
  "s_waitcnt lgkmcnt(0)\n\t" \
  "v_readfirstlane_b32 s93, v200\n\t" \
  "v_readfirstlane_b32 %[tgt], v201\n\t" \
  "s_cbranch_scc1 .Lgo%=\n\t" \
  "s_cmp_eq_u32 s93, %[step]\n\t" \
  "s_cbranch_scc1 .Lwx%=\n\t" \
  "s_sleep 1\n\t" \
  "s_branch .Lwt%=\n\t" \
  ".Lwx%=:\n\t" \
  "s_lshl_b32 s93, s90, 2\n\t" \
  "s_sub_i32 s93, %[tgt], s93\n\t" \
  "s_cmp_le_i32 s93, 0\n\t" \
  "s_cbranch_scc1 .Lgo%=\n\t" \
  "s_min_i32 s93, s93, 8000\n\t" \
  "s_add_u32 s93, s93, 3\n\t" \
  "s_lshr_b32 s93, s93, 2\n\t" \
  "s_add_u32 s93, s93, s90\n\t" \
  "s_memrealtime s[90:91]\n\t" \
  ".Lpace%=:\n\t" \
  "s_memrealtime s[94:95]\n\t" \
  "s_waitcnt lgkmcnt(1)\n\t" \
  "s_sub_i32 s91, s90, s93\n\t" \
  "s_cmp_ge_i32 s91, 0\n\t" \
  "s_cbranch_scc1 .Lgo%=\n\t" \
  "s_memrealtime s[90:91]\n\t" \
  "s_waitcnt lgkmcnt(1)\n\t" \
  "s_sub_i32 s95, s94, s93\n\t" \
  "s_cmp_lt_i32 s95, 0\n\t" \
  "s_cbranch_scc1 .Lpace%=\n\t" \
  ".Lgo%=:\n\t" \
  "s_mov_b32 s92, 0\n\t" \
  ".Lpoll%=:\n\t" \
  "buffer_load_dwordx4 v[200:203], v248, %[rs], %[som] offen sc1\n\t" \
  "buffer_load_dwordx4 v[204:207], v248, %[rs], %[som2] offen sc1\n\t" \
  "s_waitcnt vmcnt(1) lgkmcnt(0)\n\t" \
  "ds_write_b128 v249, v[200:203]\n\t" \
  "v_smfmac_f32_16x16x64_bf16 v[192:195], v[200:203], v[208:215], v251\n\t" \
  "v_max3_u32 v244, v200, v201, v202\n\t" \
  "v_smfmac_f32_16x16x64_bf16 v[196:199], v[200:203], v[236:243], v251\n\t" \
  "s_waitcnt vmcnt(0)\n\t" \
  "ds_write_b128 v249, v[204:207] offset:8192\n\t" \
  "v_smfmac_f32_16x16x64_bf16 v[192:195], v[204:207], v[216:223], v251\n\t" \
  "v_max3_u32 v244, v244, v203, v204\n\t" \
  "v_max3_u32 v244, v244, v205, v206\n\t" \
  "v_smfmac_f32_16x16x64_bf16 v[196:199], v[204:207], v[224:231], v251\n\t" \
  "v_max_u32 v244, v244, v207\n\t" \
  "v_cmp_le_u32 vcc, %[sent], v244\n\t" \
  "s_cbranch_vccnz .Lslow%=\n\t" \
  ".Lok%=:\n\t" \
  "ds_read_b128 v[216:219], v250 offset:32768\n\t" \
  "ds_read_b128 v[220:223], v250 offset:33792\n\t" \
  "ds_read_b128 v[224:227], v250 offset:34816\n\t" \
  "ds_read_b128 v[228:231], v250 offset:35840\n\t" \
  "s_waitcnt lgkmcnt(4)\n\t" \
  "s_barrier\n\t" \
  "ds_read_b128 v[208:211], v248 offset:0\n\t" \
  "ds_read_b128 v[212:215], v248 offset:3072\n\t" \
  "ds_read_b128 v[236:239], v248 offset:4096\n\t" \
  "ds_read_b128 v[240:243], v248 offset:5120\n\t" \
  "s_waitcnt lgkmcnt(3)\n\t" \
  "ds_read_b128 v[244:247], v248 offset:12288\n\t" \
  "v_smfmac_f32_16x16x64_bf16 v[192:195], v[208:211], v[216:223], v251\n\t" \
  "ds_read_b128 v[216:219], v250 offset:0\n\t" \
  "v_smfmac_f32_16x16x64_bf16 v[196:199], v[208:211], v[224:231], v251\n\t" \
  "s_waitcnt lgkmcnt(4)\n\t" \
  "ds_read_b128 v[220:223], v250 offset:1024\n\t" \
  "v_smfmac_f32_16x16x64_bf16 v[192:195], v[212:215], v[0:7], v251\n\t" \
  "ds_read_b128 v[224:227], v250 offset:2048\n\t" \
  "v_smfmac_f32_16x16x64_bf16 v[196:199], v[212:215], v[8:15], v251\n\t" \
  "s_waitcnt lgkmcnt(5)\n\t" \
  "ds_read_b128 v[228:231], v250 offset:3072\n\t" \
  "v_smfmac_f32_16x16x64_bf16 v[192:195], v[236:239], v[16:23], v251\n\t" \
  "ds_read_b128 v[200:203], v248 offset:6144\n\t" \
  "v_smfmac_f32_16x16x64_bf16 v[196:199], v[236:239], v[24:31], v251\n\t" \
  "s_waitcnt lgkmcnt(6)\n\t" \
  "ds_read_b128 v[204:207], v248 offset:7168\n\t" \
  "v_smfmac_f32_16x16x64_bf16 v[192:195], v[240:243], v[32:39], v251\n\t" \
  "ds_read_b128 v[208:211], v248 offset:8192\n\t" \
  "v_smfmac_f32_16x16x64_bf16 v[196:199], v[240:243], v[40:47], v251\n\t" \
  "s_waitcnt lgkmcnt(5)\n\t" \
  "ds_read_b128 v[212:215], v248 offset:10240\n\t" \
  "v_smfmac_f32_16x16x64_bf16 v[192:195], v[244:247], v[216:223], v251\n\t" \
  "s_waitcnt lgkmcnt(4)\n\t" \
  "v_smfmac_f32_16x16x64_bf16 v[196:199], v[244:247], v[224:231], v251\n\t" \
  "s_waitcnt lgkmcnt(3)\n\t" \
  "ds_read_b128 v[236:239], v248 offset:11264\n\t" \
  "v_smfmac_f32_16x16x64_bf16 v[192:195], v[200:203], v[48:55], v251\n\t" \
  "ds_read_b128 v[240:243], v248 offset:13312\n\t" \
  "v_smfmac_f32_16x16x64_bf16 v[196:199], v[200:203], v[56:63], v251\n\t" \
  "s_waitcnt lgkmcnt(4)\n\t" \
  "v_smfmac_f32_16x16x64_bf16 v[192:195], v[204:207], v[64:71], v251\n\t" \
  "ds_read_b128 v[244:247], v248 offset:14336\n\t" \
  "v_smfmac_f32_16x16x64_bf16 v[196:199], v[204:207], v[72:79], v251\n\t" \
  "s_waitcnt lgkmcnt(4)\n\t" \
  "v_smfmac_f32_16x16x64_bf16 v[192:195], v[208:211], v[80:87], v251\n\t" \
  "ds_read_b128 v[200:203], v248 offset:15360\n\t" \
  "v_smfmac_f32_16x16x64_bf16 v[196:199], v[208:211], v[88:95], v251\n\t" \
  "s_waitcnt lgkmcnt(4)\n\t" \
  "v_smfmac_f32_16x16x64_bf16 v[192:195], v[212:215], v[96:103], v251\n\t" \
  "ds_read_b128 v[204:207], v248 offset:2048\n\t" \
  "v_smfmac_f32_16x16x64_bf16 v[196:199], v[212:215], v[104:111], v251\n\t" \
  "s_waitcnt lgkmcnt(3)\n\t" \
  "v_smfmac_f32_16x16x64_bf16 v[192:195], v[236:239], v[112:119], v251\n\t" \
  "v_smfmac_f32_16x16x64_bf16 v[196:199], v[236:239], v[120:127], v251\n\t" \
  "v_smfmac_f32_16x16x64_bf16 v[192:195], v[240:243], v[128:135], v251\n\t" \
  "v_smfmac_f32_16x16x64_bf16 v[196:199], v[240:243], v[136:143], v251\n\t" \
  "s_waitcnt lgkmcnt(1)\n\t" \
  "v_smfmac_f32_16x16x64_bf16 v[192:195], v[244:247], v[144:151], v251\n\t" \
  "v_smfmac_f32_16x16x64_bf16 v[196:199], v[244:247], v[152:159], v251\n\t" \
  "v_smfmac_f32_16x16x64_bf16 v[192:195], v[200:203], v[160:167], v251\n\t" \
  "v_smfmac_f32_16x16x64_bf16 v[196:199], v[200:203], v[168:175], v251\n\t" \
  "s_waitcnt lgkmcnt(0)\n\t" \
  "v_smfmac_f32_16x16x64_bf16 v[192:195], v[204:207], v[176:183], v251\n\t" \
  "v_smfmac_f32_16x16x64_bf16 v[196:199], v[204:207], v[184:191], v251\n\t" \
  "ds_read_b128 v[200:203], v248 offset:17408\n\t" \
  "ds_read_b128 v[204:207], v248 offset:21504\n\t" \
  "v_mov_b32 v250, v232\n\t" \
  "v_mov_b32 v249, v233\n\t" \
  "s_nop 2\n\t" \
  "v_add_f32 v192, v192, v194\n\t" \
  "v_add_f32 v193, v193, v195\n\t" \
  "v_add_f32 v196, v196, v198\n\t" \
  "v_add_f32 v197, v197, v199\n\t" \
  "v_cvt_pk_bf16_f32 v192, v192, v193\n\t" \
  "v_cvt_pk_bf16_f32 v196, v196, v197\n\t" \
  "ds_write2_b32 v252, v192, v196 offset1:64\n\t" \
  "v_mov_b32 v251, v234\n\t" \
  "ds_read_b64_tr_b16 v[236:237], v250 offset:512\n\t" \
  "ds_read_b64_tr_b16 v[238:239], v250 offset:576\n\t" \
  "v_mov_b32 v212, %[step]\n\t" \
  "s_mov_b64 exec, 1\n\t" \
  "ds_write_b32 v248, v212 offset:28688\n\t" \
  "s_mov_b64 exec, -1\n\t" \
  "buffer_load_dwordx4 v[192:195], v248, %[rt], %[svt] offen offset:0 sc1\n\t" \
  "buffer_load_dwordx4 v[196:199], v248, %[rt], %[svt] offen offset:1024 sc1\n\t" \
  "s_waitcnt lgkmcnt(1)\n\t" \
  "v_mfma_f32_16x16x32_bf16 v[200:203], v[200:203], v[236:239], 0\n\t" \
  "v_mfma_f32_16x16x32_bf16 v[204:207], v[204:207], v[236:239], 0\n\t" \
  "s_nop 7\n\t" \
  "v_cndmask_b32 v216, v200, v204, %[nh]\n\t" \
  "v_cndmask_b32 v217, v201, v205, %[nh]\n\t" \
  "v_cndmask_b32 v218, v202, v206, %[nh]\n\t" \
  "v_cndmask_b32 v219, v203, v207, %[nh]\n\t" \
  "ds_write_b128 v248, v[216:219] offset:17408\n\t" \
  "buffer_load_dword v252, v249, %[rs], %[sv] offen offset:0 sc1\n\t" \
  "buffer_load_dword v253, v249, %[rs], %[sv] offen offset:256 sc1\n\t" \
  "ds_read_b32 v212, v248 offset:28672\n\t" \
  "ds_read_b64_tr_b16 v[240:241], v250 offset:1024\n\t" \
  "ds_read_b64_tr_b16 v[242:243], v250 offset:1088\n\t" \
  "ds_read_b64_tr_b16 v[244:245], v250 offset:1536\n\t" \
  "ds_read_b64_tr_b16 v[246:247], v250 offset:1600\n\t" \
  "ds_read_b64_tr_b16 v[232:233], v250 offset:0\n\t" \
  "ds_read_b64_tr_b16 v[234:235], v250 offset:64\n\t" \
  "s_setprio 3\n\t" \
  "s_waitcnt lgkmcnt(0)\n\t" \
  "v_cmp_ne_u32 vcc, %[step], v212\n\t" \
  "s_barrier\n\t" \
  "s_and_b32 s93, vcc_lo, 0xf\n\t" \
  "s_cbranch_scc1 .Lre%=\n\t" \
  ".Lrb%=:\n\t" \
  "ds_read_b128 v[220:223], v248 offset:18432\n\t" \
  "ds_read_b128 v[224:227], v248 offset:19456\n\t" \
  "ds_read_b128 v[228:231], v248 offset:16384\n\t" \
  "ds_read_b64_tr_b16 v[204:205], v250 offset:2560\n\t" \
  "ds_read_b64_tr_b16 v[206:207], v250 offset:2624\n\t" \
  "ds_read_b64_tr_b16 v[208:209], v250 offset:3072\n\t" \
  "ds_read_b64_tr_b16 v[210:211], v250 offset:3136\n\t" \
  "ds_read_b64_tr_b16 v[212:213], v250 offset:3584\n\t" \
  "ds_read_b64_tr_b16 v[214:215], v250 offset:3648\n\t" \
  "ds_read_b64_tr_b16 v[200:201], v250 offset:2048\n\t" \
  "ds_read_b64_tr_b16 v[202:203], v250 offset:2112\n\t" \
  "s_waitcnt lgkmcnt(10)\n\t" \
  "v_pk_add_f32 v[216:217], v[216:217], v[220:221]\n\t" \
  "v_pk_add_f32 v[218:219], v[218:219], v[222:223]\n\t" \
  "s_waitcnt lgkmcnt(8)\n\t" \
  "v_pk_add_f32 v[224:225], v[224:225], v[228:229]\n\t" \
  "v_pk_add_f32 v[226:227], v[226:227], v[230:231]\n\t" \
  "v_pk_add_f32 v[216:217], v[216:217], v[224:225]\n\t" \
  "v_pk_add_f32 v[218:219], v[218:219], v[226:227]\n\t" \
  "s_waitcnt lgkmcnt(6)\n\t" \
  "v_mfma_f32_16x16x32_bf16 v[204:207], v[204:207], v[236:239], 0\n\t" \
  "v_max3_f32 v224, v216, v217, v218\n\t" \
  "v_max_f32 v224, v224, v219\n\t" \
  "s_waitcnt lgkmcnt(4)\n\t" \
  "v_mfma_f32_16x16x32_bf16 v[204:207], v[208:211], v[240:243], v[204:207]\n\t" \
  "v_max_f32_dpp v225, v224, v224 row_ror:8 row_mask:0xf bank_mask:0xf\n\t" \
  "v_max_f32_dpp v226, v224, v224 row_ror:8 row_mask:0xf bank_mask:0xf\n\t" \
  "s_waitcnt lgkmcnt(2)\n\t" \
  "v_mfma_f32_16x16x32_bf16 v[204:207], v[212:215], v[244:247], v[204:207]\n\t" \
  "v_permlane32_swap_b32 v225, v226\n\t" \
  "s_waitcnt lgkmcnt(0)\n\t" \
  "v_mfma_f32_16x16x32_bf16 v[204:207], v[200:203], v[232:235], v[204:207]\n\t" \
  "v_max_f32 v231, v225, v226\n\t" \
  "v_mov_b32 v220, 0x3f803f80\n\t" \
  "v_mov_b32 v221, 0x3f803f80\n\t" \
  "v_mov_b64 v[222:223], v[220:221]\n\t" \
  "v_mov_b32 v237, 0\n\t" \
  "v_mov_b32 v238, 0\n\t" \
  "s_nop 1\n\t" \
  "v_max3_f32 v208, v204, v205, v231\n\t" \
  "v_max3_f32 v209, v208, v206, v207\n\t" \
  "v_max3_f32 v210, v208, v206, v207\n\t" \
  "v_cndmask_b32 v204, v204, v206, %[up]\n\t" \
  "v_cndmask_b32 v205, v205, v207, %[up]\n\t" \
  "v_permlane16_swap_b32 v209, v210\n\t" \
  "v_max_f32 v208, v209, v210\n\t" \
  "v_mul_f32 v208, %[negc], v208\n\t" \
  "v_fma_f32 v204, v204, %[c], v208\n\t" \
  "v_fma_f32 v205, v205, %[c], v208\n\t" \
  "v_exp_f32 v204, v204\n\t" \
  "v_fma_f32 v216, v216, %[c], v208\n\t" \
  "v_exp_f32 v205, v205\n\t" \
  "v_fma_f32 v217, v217, %[c], v208\n\t" \
  "v_exp_f32 v216, v216\n\t" \
  "v_fma_f32 v218, v218, %[c], v208\n\t" \
  "v_cvt_pk_bf16_f32 v236, v204, v205\n\t" \
  "v_cvt_pk_bf16_f32 v239, v204, v205\n\t" \
  "v_exp_f32 v217, v217\n\t" \
  "v_fma_f32 v219, v219, %[c], v208\n\t" \
  "v_mfma_f32_16x16x16_bf16 v[200:203], v[220:221], v[236:237], 0\n\t" \
  "v_exp_f32 v218, v218\n\t" \
  "s_waitcnt vmcnt(0)\n\t" \
  "v_mfma_f32_16x16x16_bf16 v[224:227], v[252:253], v[236:237], 0\n\t" \
  "v_cvt_pk_bf16_f32 v214, v216, v217\n\t" \
  "v_cvt_pk_bf16_f32 v212, v216, v217\n\t" \
  "v_exp_f32 v219, v219\n\t" \
  "v_mfma_f32_16x16x16_bf16 v[228:231], v[252:253], v[238:239], 0\n\t" \
  "v_cvt_pk_bf16_f32 v215, v218, v219\n\t" \
  "v_cvt_pk_bf16_f32 v213, v218, v219\n\t" \
  "v_mov_b32_dpp v212, v214 row_ror:8 row_mask:0xf bank_mask:0xc\n\t" \
  "v_mov_b32_dpp v214, v214 row_ror:8 row_mask:0xf bank_mask:0x3\n\t" \
  "v_mov_b32_dpp v213, v215 row_ror:8 row_mask:0xf bank_mask:0xc\n\t" \
  "v_mov_b32_dpp v215, v215 row_ror:8 row_mask:0xf bank_mask:0x3\n\t" \
  "v_max_u32 v208, v252, v253\n\t" \
  "v_cmp_le_u32 vcc, %[sent], v208\n\t" \
  "v_mfma_f32_16x16x32_bf16 v[200:203], v[220:223], v[212:215], v[200:203]\n\t" \
  ".Lpv%=:\n\t" \
  "v_mfma_f32_16x16x32_bf16 v[224:227], v[192:195], v[212:215], v[224:227]\n\t" \
  "v_mfma_f32_16x16x32_bf16 v[228:231], v[196:199], v[212:215], v[228:231]\n\t" \
  "s_cbranch_vccnz .Lvs%=\n\t" \
  ".Lvok%=:\n\t" \
  "s_nop 4\n\t" \
  "v_rcp_f32 v208, v200\n\t" \
  "v_cndmask_b32 v224, v224, v226, %[nh]\n\t" \
  "v_cndmask_b32 v225, v225, v227, %[nh]\n\t" \
  "v_mul_f32 v224, v224, v208\n\t" \
  "v_mul_f32 v225, v225, v208\n\t" \
  "v_cvt_pk_bf16_f32 v224, v224, v225\n\t" \
  "buffer_store_dword v224, v251, %[rs], %[so] offen offset:0 sc1\n\t" \
  "v_cndmask_b32 v228, v228, v230, %[nh]\n\t" \
  "v_cndmask_b32 v229, v229, v231, %[nh]\n\t" \
  "v_mul_f32 v228, v228, v208\n\t" \
  "v_mul_f32 v229, v229, v208\n\t" \
  "v_cvt_pk_bf16_f32 v228, v228, v229\n\t" \
  "buffer_store_dword v228, v251, %[rs], %[so] offen offset:256 sc1\n\t" \
  "s_memtime s[90:91]\n\t" \
  "s_setprio 0\n\t" \
  "s_waitcnt lgkmcnt(0)\n\t" \
  "s_mov_b32 %[tout], s90\n\t" \
  "s_mov_b32 %[spins], s92\n\t" \
  "s_branch .Lend%=\n\t" \
  ".Lre%=:\n\t" \
  "ds_read_b64_tr_b16 v[240:241], v250 offset:1024\n\t" \
  "ds_read_b64_tr_b16 v[242:243], v250 offset:1088\n\t" \
  "ds_read_b64_tr_b16 v[244:245], v250 offset:1536\n\t" \
  "ds_read_b64_tr_b16 v[246:247], v250 offset:1600\n\t" \
  "ds_read_b64_tr_b16 v[232:233], v250 offset:0\n\t" \
  "ds_read_b64_tr_b16 v[234:235], v250 offset:64\n\t" \
  "s_waitcnt lgkmcnt(0)\n\t" \
  "s_branch .Lrb%=\n\t" \
  ".Lvs%=:\n\t" \
  "v_cmp_eq_u32 vcc, %[sent], v252\n\t" \
  "v_cmp_eq_u32 s[94:95], %[sent], v253\n\t" \
  "s_or_b64 s[94:95], s[94:95], vcc\n\t" \
  "s_cbranch_scc0 .Lvok%=\n\t" \
  "s_add_u32 s92, s92, 1\n\t" \
  "s_sleep 2\n\t" \
  "s_and_b32 s93, s92, 63\n\t" \
  "s_cbranch_scc1 .Lvr%=\n\t" \
  "buffer_load_dword v208, off, %[rs], %[sab] sc1\n\t" \
  "s_waitcnt vmcnt(0)\n\t" \
  "v_readfirstlane_b32 s93, v208\n\t" \
  "s_nop 3\n\t" \
  "s_cmp_eq_u32 s93, 1\n\t" \
  "s_cbranch_scc1 .Lvok%=\n\t" \
  "s_cmp_lt_u32 s92, 0x80000\n\t" \
  "s_cbranch_scc1 .Lvr%=\n\t" \
  "v_mov_b32 v208, 1\n\t" \
  "buffer_store_dword v208, off, %[rs], %[sab] sc1\n\t" \
  "s_branch .Lvok%=\n\t" \
  ".Lvr%=:\n\t" \
  "buffer_load_dword v252, v249, %[rs], %[sv] offen offset:0 sc1\n\t" \
  "buffer_load_dword v253, v249, %[rs], %[sv] offen offset:256 sc1\n\t" \
  "s_waitcnt vmcnt(0)\n\t" \
  "v_mfma_f32_16x16x16_bf16 v[224:227], v[252:253], v[236:237], 0\n\t" \
  "v_mfma_f32_16x16x16_bf16 v[228:231], v[252:253], v[238:239], 0\n\t" \
  "v_max_u32 v208, v252, v253\n\t" \
  "v_cmp_le_u32 vcc, %[sent], v208\n\t" \
  "s_nop 4\n\t" \
  "s_branch .Lpv%=\n\t" \
  "s_branch .Lend%=\n\t" \
  ".Lslow%=:\n\t" \
  "s_mov_b64 s[94:95], 0\n\t" \
  "v_cmp_eq_u32 vcc, %[sent], v200\n\t" \
  "s_or_b64 s[94:95], s[94:95], vcc\n\t" \
  "v_cmp_eq_u32 vcc, %[sent], v201\n\t" \
  "s_or_b64 s[94:95], s[94:95], vcc\n\t" \
  "v_cmp_eq_u32 vcc, %[sent], v202\n\t" \
  "s_or_b64 s[94:95], s[94:95], vcc\n\t" \
  "v_cmp_eq_u32 vcc, %[sent], v203\n\t" \
  "s_or_b64 s[94:95], s[94:95], vcc\n\t" \
  "v_cmp_eq_u32 vcc, %[sent], v204\n\t" \
  "s_or_b64 s[94:95], s[94:95], vcc\n\t" \
  "v_cmp_eq_u32 vcc, %[sent], v205\n\t" \
  "s_or_b64 s[94:95], s[94:95], vcc\n\t" \
  "v_cmp_eq_u32 vcc, %[sent], v206\n\t" \
  "s_or_b64 s[94:95], s[94:95], vcc\n\t" \
  "v_cmp_eq_u32 vcc, %[sent], v207\n\t" \
  "s_or_b64 s[94:95], s[94:95], vcc\n\t" \
  "s_cmp_eq_u64 s[94:95], 0\n\t" \
  "s_cbranch_scc1 .Lok%=\n\t" \
  "v_mov_b64 v[192:193], 0\n\t" \
  "v_mov_b64 v[194:195], 0\n\t" \
  "v_mov_b64 v[196:197], 0\n\t" \
  "v_mov_b64 v[198:199], 0\n\t" \
  "s_add_u32 s92, s92, 1\n\t" \
  "s_sleep 6\n\t" \
  "s_cmp_eq_u32 s92, 1\n\t" \
  "s_cbranch_scc1 .Lpoll%=\n\t" \
  "s_sleep 1\n\t" \
  "s_and_b32 s93, s92, 63\n\t" \
  "s_cmp_lg_u32 s93, 0\n\t" \
  "s_cbranch_scc1 .Lpoll%=\n\t" \
  "v_mov_b32 v245, 0\n\t" \
  "buffer_load_dword v244, v245, %[rs], %[sab] offen sc1\n\t" \
  "s_waitcnt vmcnt(0)\n\t" \
  "v_readfirstlane_b32 s93, v244\n\t" \
  "s_nop 3\n\t" \
  "s_cmp_eq_u32 s93, 1\n\t" \
  "s_cbranch_scc1 .Lok%=\n\t" \
  "s_cmp_lt_u32 s92, 0x80000\n\t" \
  "s_cbranch_scc1 .Lpoll%=\n\t" \
  "v_mov_b32 v244, 1\n\t" \
  "buffer_store_dword v244, v245, %[rs], %[sab] offen sc1\n\t" \
  "s_branch .Lok%=\n\t" \
  ".Lend%=:\n\t"
#define QK_ASM_2 \
  "s_waitcnt lgkmcnt(0)\n\t" \
  "v_subrev_u32 v244, 0x8000, v250\n\t" \
  "ds_read_b128 v[216:219], v248 offset:40960\n\t" \
  "ds_read_b128 v[220:223], v248 offset:41984\n\t" \
  "ds_read_b128 v[224:227], v248 offset:43008\n\t" \
  "ds_read_b128 v[228:231], v248 offset:44032\n\t" \
  "ds_read_b128 v[208:211], v244 offset:0\n\t" \
  "ds_read_b128 v[212:215], v244 offset:1024\n\t" \
  "ds_read_b128 v[236:239], v244 offset:2048\n\t" \
  "ds_read_b128 v[240:243], v244 offset:3072\n\t" \
  "v_mov_b64 v[192:193], 0\n\t" \
  "v_mov_b64 v[194:195], 0\n\t" \
  "v_mov_b64 v[196:197], 0\n\t" \
  "v_mov_b64 v[198:199], 0\n\t" \
  "s_movk_i32 s92, 0x2000\n\t" \
  ".Lwt%=:\n\t" \
  "ds_read_b64 v[200:201], v248 offset:30720\n\t" \
  "s_memrealtime s[90:91]\n\t" \
  "s_sub_u32 s92, s92, 1\n\t" \
  "s_waitcnt lgkmcnt(0)\n\t" \
  "v_readfirstlane_b32 s93, v200\n\t" \
  "v_readfirstlane_b32 %[tgt], v201\n\t" \
  "s_cbranch_scc1 .Lgo%=\n\t" \
  "s_cmp_eq_u32 s93, %[step]\n\t" \
  "s_cbranch_scc1 .Lwx%=\n\t" \
  "s_sleep 1\n\t" \
  "s_branch .Lwt%=\n\t" \
  ".Lwx%=:\n\t" \
  "s_lshl_b32 s93, s90, 2\n\t" \
  "s_sub_i32 s93, %[tgt], s93\n\t" \
  "s_cmp_le_i32 s93, 0\n\t" \
  "s_cbranch_scc1 .Lgo%=\n\t" \
  "s_min_i32 s93, s93, 8000\n\t" \
  "s_add_u32 s93, s93, 3\n\t" \
  "s_lshr_b32 s93, s93, 2\n\t" \
  "s_add_u32 s93, s93, s90\n\t" \
  "s_memrealtime s[90:91]\n\t" \
  ".Lpace%=:\n\t" \
  "s_memrealtime s[94:95]\n\t" \
  "s_waitcnt lgkmcnt(1)\n\t" \
  "s_sub_i32 s91, s90, s93\n\t" \
  "s_cmp_ge_i32 s91, 0\n\t" \
  "s_cbranch_scc1 .Lgo%=\n\t" \
  "s_memrealtime s[90:91]\n\t" \
  "s_waitcnt lgkmcnt(1)\n\t" \
  "s_sub_i32 s95, s94, s93\n\t" \
  "s_cmp_lt_i32 s95, 0\n\t" \
  "s_cbranch_scc1 .Lpace%=\n\t" \
  ".Lgo%=:\n\t" \
  "s_mov_b32 s92, 0\n\t" \
  ".Lpoll%=:\n\t" \
  "buffer_load_dwordx4 v[200:203], v248, %[rs], %[som] offen sc1\n\t" \
  "buffer_load_dwordx4 v[204:207], v248, %[rs], %[som2] offen sc1\n\t" \
  "s_waitcnt vmcnt(1) lgkmcnt(0)\n\t" \
  "ds_write_b128 v249, v[200:203]\n\t" \
  "v_smfmac_f32_16x16x64_bf16 v[192:195], v[200:203], v[208:215], v251\n\t" \
  "v_max3_u32 v244, v200, v201, v202\n\t" \
  "v_smfmac_f32_16x16x64_bf16 v[196:199], v[200:203], v[236:243], v251\n\t" \
  "s_waitcnt vmcnt(0)\n\t" \
  "ds_write_b128 v249, v[204:207] offset:8192\n\t" \
  "v_smfmac_f32_16x16x64_bf16 v[192:195], v[204:207], v[216:223], v251\n\t" \
  "v_max3_u32 v244, v244, v203, v204\n\t" \
  "v_max3_u32 v244, v244, v205, v206\n\t" \
  "v_smfmac_f32_16x16x64_bf16 v[196:199], v[204:207], v[224:231], v251\n\t" \
  "v_max_u32 v244, v244, v207\n\t" \
  "v_cmp_le_u32 vcc, %[sent], v244\n\t" \
  "s_cbranch_vccnz .Lslow%=\n\t" \
  ".Lok%=:\n\t" \
  "ds_read_b128 v[216:219], v250 offset:32768\n\t" \
  "ds_read_b128 v[220:223], v250 offset:33792\n\t" \
  "ds_read_b128 v[224:227], v250 offset:34816\n\t" \
  "ds_read_b128 v[228:231], v250 offset:35840\n\t" \
  "s_waitcnt lgkmcnt(4)\n\t" \
  "s_barrier\n\t" \
  "ds_read_b128 v[208:211], v248 offset:3072\n\t" \
  "ds_read_b128 v[212:215], v248 offset:4096\n\t" \
  "ds_read_b128 v[236:239], v248 offset:5120\n\t" \
  "ds_read_b128 v[240:243], v248 offset:6144\n\t" \
  "s_waitcnt lgkmcnt(3)\n\t" \
  "ds_read_b128 v[244:247], v248 offset:13312\n\t" \
  "v_smfmac_f32_16x16x64_bf16 v[192:195], v[208:211], v[216:223], v251\n\t" \
  "ds_read_b128 v[216:219], v250 offset:0\n\t" \
  "v_smfmac_f32_16x16x64_bf16 v[196:199], v[208:211], v[224:231], v251\n\t" \
  "s_waitcnt lgkmcnt(4)\n\t" \
  "ds_read_b128 v[220:223], v250 offset:1024\n\t" \
  "v_smfmac_f32_16x16x64_bf16 v[192:195], v[212:215], v[0:7], v251\n\t" \
  "ds_read_b128 v[224:227], v250 offset:2048\n\t" \
  "v_smfmac_f32_16x16x64_bf16 v[196:199], v[212:215], v[8:15], v251\n\t" \
  "s_waitcnt lgkmcnt(5)\n\t" \
  "ds_read_b128 v[228:231], v250 offset:3072\n\t" \
  "v_smfmac_f32_16x16x64_bf16 v[192:195], v[236:239], v[16:23], v251\n\t" \
  "ds_read_b128 v[200:203], v248 offset:7168\n\t" \
  "v_smfmac_f32_16x16x64_bf16 v[196:199], v[236:239], v[24:31], v251\n\t" \
  "s_waitcnt lgkmcnt(6)\n\t" \
  "ds_read_b128 v[204:207], v248 offset:8192\n\t" \
  "v_smfmac_f32_16x16x64_bf16 v[192:195], v[240:243], v[32:39], v251\n\t" \
  "ds_read_b128 v[208:211], v248 offset:9216\n\t" \
  "v_smfmac_f32_16x16x64_bf16 v[196:199], v[240:243], v[40:47], v251\n\t" \
  "s_waitcnt lgkmcnt(5)\n\t" \
  "ds_read_b128 v[212:215], v248 offset:11264\n\t" \
  "v_smfmac_f32_16x16x64_bf16 v[192:195], v[244:247], v[216:223], v251\n\t" \
  "s_waitcnt lgkmcnt(4)\n\t" \
  "v_smfmac_f32_16x16x64_bf16 v[196:199], v[244:247], v[224:231], v251\n\t" \
  "s_waitcnt lgkmcnt(3)\n\t" \
  "ds_read_b128 v[236:239], v248 offset:12288\n\t" \
  "v_smfmac_f32_16x16x64_bf16 v[192:195], v[200:203], v[48:55], v251\n\t" \
  "ds_read_b128 v[240:243], v248 offset:14336\n\t" \
  "v_smfmac_f32_16x16x64_bf16 v[196:199], v[200:203], v[56:63], v251\n\t" \
  "s_waitcnt lgkmcnt(4)\n\t" \
  "v_smfmac_f32_16x16x64_bf16 v[192:195], v[204:207], v[64:71], v251\n\t" \
  "ds_read_b128 v[244:247], v248 offset:15360\n\t" \
  "v_smfmac_f32_16x16x64_bf16 v[196:199], v[204:207], v[72:79], v251\n\t" \
  "s_waitcnt lgkmcnt(4)\n\t" \
  "v_smfmac_f32_16x16x64_bf16 v[192:195], v[208:211], v[80:87], v251\n\t" \
  "ds_read_b128 v[200:203], v248 offset:0\n\t" \
  "v_smfmac_f32_16x16x64_bf16 v[196:199], v[208:211], v[88:95], v251\n\t" \
  "s_waitcnt lgkmcnt(4)\n\t" \
  "v_smfmac_f32_16x16x64_bf16 v[192:195], v[212:215], v[96:103], v251\n\t" \
  "ds_read_b128 v[204:207], v248 offset:1024\n\t" \
  "v_smfmac_f32_16x16x64_bf16 v[196:199], v[212:215], v[104:111], v251\n\t" \
  "s_waitcnt lgkmcnt(3)\n\t" \
  "v_smfmac_f32_16x16x64_bf16 v[192:195], v[236:239], v[112:119], v251\n\t" \
  "v_smfmac_f32_16x16x64_bf16 v[196:199], v[236:239], v[120:127], v251\n\t" \
  "v_smfmac_f32_16x16x64_bf16 v[192:195], v[240:243], v[128:135], v251\n\t" \
  "v_smfmac_f32_16x16x64_bf16 v[196:199], v[240:243], v[136:143], v251\n\t" \
  "s_waitcnt lgkmcnt(1)\n\t" \
  "v_smfmac_f32_16x16x64_bf16 v[192:195], v[244:247], v[144:151], v251\n\t" \
  "v_smfmac_f32_16x16x64_bf16 v[196:199], v[244:247], v[152:159], v251\n\t" \
  "v_smfmac_f32_16x16x64_bf16 v[192:195], v[200:203], v[160:167], v251\n\t" \
  "v_smfmac_f32_16x16x64_bf16 v[196:199], v[200:203], v[168:175], v251\n\t" \
  "s_waitcnt lgkmcnt(0)\n\t" \
  "v_smfmac_f32_16x16x64_bf16 v[192:195], v[204:207], v[176:183], v251\n\t" \
  "v_smfmac_f32_16x16x64_bf16 v[196:199], v[204:207], v[184:191], v251\n\t" \
  "ds_read_b128 v[200:203], v248 offset:18432\n\t" \
  "ds_read_b128 v[204:207], v248 offset:22528\n\t" \
  "v_mov_b32 v250, v232\n\t" \
  "v_mov_b32 v249, v233\n\t" \
  "s_nop 2\n\t" \
  "v_add_f32 v192, v192, v194\n\t" \
  "v_add_f32 v193, v193, v195\n\t" \
  "v_add_f32 v196, v196, v198\n\t" \
  "v_add_f32 v197, v197, v199\n\t" \
  "v_cvt_pk_bf16_f32 v192, v192, v193\n\t" \
  "v_cvt_pk_bf16_f32 v196, v196, v197\n\t" \
  "ds_write2_b32 v252, v192, v196 offset1:64\n\t" \
  "v_mov_b32 v251, v234\n\t" \
  "ds_read_b64_tr_b16 v[240:241], v250 offset:1024\n\t" \
  "ds_read_b64_tr_b16 v[242:243], v250 offset:1088\n\t" \
  "v_mov_b32 v212, %[step]\n\t" \
  "s_mov_b64 exec, 1\n\t" \
  "ds_write_b32 v248, v212 offset:28704\n\t" \
  "s_mov_b64 exec, -1\n\t" \
  "buffer_load_dwordx4 v[192:195], v248, %[rt], %[svt] offen offset:0 sc1\n\t" \
  "buffer_load_dwordx4 v[196:199], v248, %[rt], %[svt] offen offset:1024 sc1\n\t" \
  "s_waitcnt lgkmcnt(1)\n\t" \
  "v_mfma_f32_16x16x32_bf16 v[200:203], v[200:203], v[240:243], 0\n\t" \
  "v_mfma_f32_16x16x32_bf16 v[204:207], v[204:207], v[240:243], 0\n\t" \
  "s_nop 7\n\t" \
  "v_cndmask_b32 v216, v200, v204, %[nh]\n\t" \
  "v_cndmask_b32 v217, v201, v205, %[nh]\n\t" \
  "v_cndmask_b32 v218, v202, v206, %[nh]\n\t" \
  "v_cndmask_b32 v219, v203, v207, %[nh]\n\t" \
  "ds_write_b128 v248, v[216:219] offset:18432\n\t" \
  "buffer_load_dword v252, v249, %[rs], %[sv] offen offset:0 sc1\n\t" \
  "buffer_load_dword v253, v249, %[rs], %[sv] offen offset:256 sc1\n\t" \
  "ds_read_b32 v212, v248 offset:28672\n\t" \
  "ds_read_b64_tr_b16 v[244:245], v250 offset:1536\n\t" \
  "ds_read_b64_tr_b16 v[246:247], v250 offset:1600\n\t" \
  "ds_read_b64_tr_b16 v[232:233], v250 offset:0\n\t" \
  "ds_read_b64_tr_b16 v[234:235], v250 offset:64\n\t" \
  "ds_read_b64_tr_b16 v[236:237], v250 offset:512\n\t" \
  "ds_read_b64_tr_b16 v[238:239], v250 offset:576\n\t" \
  "s_setprio 3\n\t" \
  "s_waitcnt lgkmcnt(0)\n\t" \
  "v_cmp_ne_u32 vcc, %[step], v212\n\t" \
  "s_barrier\n\t" \
  "s_and_b32 s93, vcc_lo, 0xf\n\t" \
  "s_cbranch_scc1 .Lre%=\n\t" \
  ".Lrb%=:\n\t" \
  "ds_read_b128 v[220:223], v248 offset:19456\n\t" \
  "ds_read_b128 v[224:227], v248 offset:16384\n\t" \
  "ds_read_b128 v[228:231], v248 offset:17408\n\t" \
  "ds_read_b64_tr_b16 v[208:209], v250 offset:3072\n\t" \
  "ds_read_b64_tr_b16 v[210:211], v250 offset:3136\n\t" \
  "ds_read_b64_tr_b16 v[212:213], v250 offset:3584\n\t" \
  "ds_read_b64_tr_b16 v[214:215], v250 offset:3648\n\t" \
  "ds_read_b64_tr_b16 v[200:201], v250 offset:2048\n\t" \
  "ds_read_b64_tr_b16 v[202:203], v250 offset:2112\n\t" \
  "ds_read_b64_tr_b16 v[204:205], v250 offset:2560\n\t" \
  "ds_read_b64_tr_b16 v[206:207], v250 offset:2624\n\t" \
  "s_waitcnt lgkmcnt(10)\n\t" \
  "v_pk_add_f32 v[216:217], v[216:217], v[220:221]\n\t" \
  "v_pk_add_f32 v[218:219], v[218:219], v[222:223]\n\t" \
  "s_waitcnt lgkmcnt(8)\n\t" \
  "v_pk_add_f32 v[224:225], v[224:225], v[228:229]\n\t" \
  "v_pk_add_f32 v[226:227], v[226:227], v[230:231]\n\t" \
  "v_pk_add_f32 v[216:217], v[216:217], v[224:225]\n\t" \
  "v_pk_add_f32 v[218:219], v[218:219], v[226:227]\n\t" \
  "s_waitcnt lgkmcnt(6)\n\t" \
  "v_mfma_f32_16x16x32_bf16 v[208:211], v[208:211], v[240:243], 0\n\t" \
  "v_max3_f32 v224, v216, v217, v218\n\t" \
  "v_max_f32 v224, v224, v219\n\t" \
  "s_waitcnt lgkmcnt(4)\n\t" \
  "v_mfma_f32_16x16x32_bf16 v[208:211], v[212:215], v[244:247], v[208:211]\n\t" \
  "v_max_f32_dpp v225, v224, v224 row_ror:8 row_mask:0xf bank_mask:0xf\n\t" \
  "v_max_f32_dpp v226, v224, v224 row_ror:8 row_mask:0xf bank_mask:0xf\n\t" \
  "s_waitcnt lgkmcnt(2)\n\t" \
  "v_mfma_f32_16x16x32_bf16 v[208:211], v[200:203], v[232:235], v[208:211]\n\t" \
  "v_permlane32_swap_b32 v225, v226\n\t" \
  "s_waitcnt lgkmcnt(0)\n\t" \
  "v_mfma_f32_16x16x32_bf16 v[208:211], v[204:207], v[236:239], v[208:211]\n\t" \
  "v_max_f32 v231, v225, v226\n\t" \
  "v_mov_b32 v220, 0x3f803f80\n\t" \
  "v_mov_b32 v221, 0x3f803f80\n\t" \
  "v_mov_b64 v[222:223], v[220:221]\n\t" \
  "v_mov_b32 v241, 0\n\t" \
  "v_mov_b32 v242, 0\n\t" \
  "s_nop 1\n\t" \
  "v_max3_f32 v212, v208, v209, v231\n\t" \
  "v_max3_f32 v213, v212, v210, v211\n\t" \
  "v_max3_f32 v214, v212, v210, v211\n\t" \
  "v_cndmask_b32 v208, v208, v210, %[up]\n\t" \
  "v_cndmask_b32 v209, v209, v211, %[up]\n\t" \
  "v_permlane16_swap_b32 v213, v214\n\t" \
  "v_max_f32 v212, v213, v214\n\t" \
  "v_mul_f32 v212, %[negc], v212\n\t" \
  "v_fma_f32 v208, v208, %[c], v212\n\t" \
  "v_fma_f32 v209, v209, %[c], v212\n\t" \
  "v_exp_f32 v208, v208\n\t" \
  "v_fma_f32 v216, v216, %[c], v212\n\t" \
  "v_exp_f32 v209, v209\n\t" \
  "v_fma_f32 v217, v217, %[c], v212\n\t" \
  "v_exp_f32 v216, v216\n\t" \
  "v_fma_f32 v218, v218, %[c], v212\n\t" \
  "v_cvt_pk_bf16_f32 v240, v208, v209\n\t" \
  "v_cvt_pk_bf16_f32 v243, v208, v209\n\t" \
  "v_exp_f32 v217, v217\n\t" \
  "v_fma_f32 v219, v219, %[c], v212\n\t" \
  "v_mfma_f32_16x16x16_bf16 v[204:207], v[220:221], v[240:241], 0\n\t" \
  "v_exp_f32 v218, v218\n\t" \
  "s_waitcnt vmcnt(0)\n\t" \
  "v_mfma_f32_16x16x16_bf16 v[224:227], v[252:253], v[240:241], 0\n\t" \
  "v_cvt_pk_bf16_f32 v202, v216, v217\n\t" \
  "v_cvt_pk_bf16_f32 v200, v216, v217\n\t" \
  "v_exp_f32 v219, v219\n\t" \
  "v_mfma_f32_16x16x16_bf16 v[228:231], v[252:253], v[242:243], 0\n\t" \
  "v_cvt_pk_bf16_f32 v203, v218, v219\n\t" \
  "v_cvt_pk_bf16_f32 v201, v218, v219\n\t" \
  "v_mov_b32_dpp v200, v202 row_ror:8 row_mask:0xf bank_mask:0xc\n\t" \
  "v_mov_b32_dpp v202, v202 row_ror:8 row_mask:0xf bank_mask:0x3\n\t" \
  "v_mov_b32_dpp v201, v203 row_ror:8 row_mask:0xf bank_mask:0xc\n\t" \
  "v_mov_b32_dpp v203, v203 row_ror:8 row_mask:0xf bank_mask:0x3\n\t" \
  "v_max_u32 v212, v252, v253\n\t" \
  "v_cmp_le_u32 vcc, %[sent], v212\n\t" \
  "v_mfma_f32_16x16x32_bf16 v[204:207], v[220:223], v[200:203], v[204:207]\n\t" \
  ".Lpv%=:\n\t" \
  "v_mfma_f32_16x16x32_bf16 v[224:227], v[192:195], v[200:203], v[224:227]\n\t" \
  "v_mfma_f32_16x16x32_bf16 v[228:231], v[196:199], v[200:203], v[228:231]\n\t" \
  "s_cbranch_vccnz .Lvs%=\n\t" \
  ".Lvok%=:\n\t" \
  "s_nop 4\n\t" \
  "v_rcp_f32 v212, v204\n\t" \
  "v_cndmask_b32 v224, v224, v226, %[nh]\n\t" \
  "v_cndmask_b32 v225, v225, v227, %[nh]\n\t" \
  "v_mul_f32 v224, v224, v212\n\t" \
  "v_mul_f32 v225, v225, v212\n\t" \
  "v_cvt_pk_bf16_f32 v224, v224, v225\n\t" \
  "buffer_store_dword v224, v251, %[rs], %[so] offen offset:0 sc1\n\t" \
  "v_cndmask_b32 v228, v228, v230, %[nh]\n\t" \
  "v_cndmask_b32 v229, v229, v231, %[nh]\n\t" \
  "v_mul_f32 v228, v228, v212\n\t" \
  "v_mul_f32 v229, v229, v212\n\t" \
  "v_cvt_pk_bf16_f32 v228, v228, v229\n\t" \
  "buffer_store_dword v228, v251, %[rs], %[so] offen offset:256 sc1\n\t" \
  "s_memtime s[90:91]\n\t" \
  "s_setprio 0\n\t" \
  "s_waitcnt lgkmcnt(0)\n\t" \
  "s_mov_b32 %[tout], s90\n\t" \
  "s_mov_b32 %[spins], s92\n\t" \
  "s_branch .Lend%=\n\t" \
  ".Lre%=:\n\t" \
  "ds_read_b64_tr_b16 v[244:245], v250 offset:1536\n\t" \
  "ds_read_b64_tr_b16 v[246:247], v250 offset:1600\n\t" \
  "ds_read_b64_tr_b16 v[232:233], v250 offset:0\n\t" \
  "ds_read_b64_tr_b16 v[234:235], v250 offset:64\n\t" \
  "ds_read_b64_tr_b16 v[236:237], v250 offset:512\n\t" \
  "ds_read_b64_tr_b16 v[238:239], v250 offset:576\n\t" \
  "s_waitcnt lgkmcnt(0)\n\t" \
  "s_branch .Lrb%=\n\t" \
  ".Lvs%=:\n\t" \
  "v_cmp_eq_u32 vcc, %[sent], v252\n\t" \
  "v_cmp_eq_u32 s[94:95], %[sent], v253\n\t" \
  "s_or_b64 s[94:95], s[94:95], vcc\n\t" \
  "s_cbranch_scc0 .Lvok%=\n\t" \
  "s_add_u32 s92, s92, 1\n\t" \
  "s_sleep 2\n\t" \
  "s_and_b32 s93, s92, 63\n\t" \
  "s_cbranch_scc1 .Lvr%=\n\t" \
  "buffer_load_dword v212, off, %[rs], %[sab] sc1\n\t" \
  "s_waitcnt vmcnt(0)\n\t" \
  "v_readfirstlane_b32 s93, v212\n\t" \
  "s_nop 3\n\t" \
  "s_cmp_eq_u32 s93, 1\n\t" \
  "s_cbranch_scc1 .Lvok%=\n\t" \
  "s_cmp_lt_u32 s92, 0x80000\n\t" \
  "s_cbranch_scc1 .Lvr%=\n\t" \
  "v_mov_b32 v212, 1\n\t" \
  "buffer_store_dword v212, off, %[rs], %[sab] sc1\n\t" \
  "s_branch .Lvok%=\n\t" \
  ".Lvr%=:\n\t" \
  "buffer_load_dword v252, v249, %[rs], %[sv] offen offset:0 sc1\n\t" \
  "buffer_load_dword v253, v249, %[rs], %[sv] offen offset:256 sc1\n\t" \
  "s_waitcnt vmcnt(0)\n\t" \
  "v_mfma_f32_16x16x16_bf16 v[224:227], v[252:253], v[240:241], 0\n\t" \
  "v_mfma_f32_16x16x16_bf16 v[228:231], v[252:253], v[242:243], 0\n\t" \
  "v_max_u32 v212, v252, v253\n\t" \
  "v_cmp_le_u32 vcc, %[sent], v212\n\t" \
  "s_nop 4\n\t" \
  "s_branch .Lpv%=\n\t" \
  "s_branch .Lend%=\n\t" \
  ".Lslow%=:\n\t" \
  "s_mov_b64 s[94:95], 0\n\t" \
  "v_cmp_eq_u32 vcc, %[sent], v200\n\t" \
  "s_or_b64 s[94:95], s[94:95], vcc\n\t" \
  "v_cmp_eq_u32 vcc, %[sent], v201\n\t" \
  "s_or_b64 s[94:95], s[94:95], vcc\n\t" \
  "v_cmp_eq_u32 vcc, %[sent], v202\n\t" \
  "s_or_b64 s[94:95], s[94:95], vcc\n\t" \
  "v_cmp_eq_u32 vcc, %[sent], v203\n\t" \
  "s_or_b64 s[94:95], s[94:95], vcc\n\t" \
  "v_cmp_eq_u32 vcc, %[sent], v204\n\t" \
  "s_or_b64 s[94:95], s[94:95], vcc\n\t" \
  "v_cmp_eq_u32 vcc, %[sent], v205\n\t" \
  "s_or_b64 s[94:95], s[94:95], vcc\n\t" \
  "v_cmp_eq_u32 vcc, %[sent], v206\n\t" \
  "s_or_b64 s[94:95], s[94:95], vcc\n\t" \
  "v_cmp_eq_u32 vcc, %[sent], v207\n\t" \
  "s_or_b64 s[94:95], s[94:95], vcc\n\t" \
  "s_cmp_eq_u64 s[94:95], 0\n\t" \
  "s_cbranch_scc1 .Lok%=\n\t" \
  "v_mov_b64 v[192:193], 0\n\t" \
  "v_mov_b64 v[194:195], 0\n\t" \
  "v_mov_b64 v[196:197], 0\n\t" \
  "v_mov_b64 v[198:199], 0\n\t" \
  "s_add_u32 s92, s92, 1\n\t" \
  "s_sleep 6\n\t" \
  "s_cmp_eq_u32 s92, 1\n\t" \
  "s_cbranch_scc1 .Lpoll%=\n\t" \
  "s_sleep 1\n\t" \
  "s_and_b32 s93, s92, 63\n\t" \
  "s_cmp_lg_u32 s93, 0\n\t" \
  "s_cbranch_scc1 .Lpoll%=\n\t" \
  "v_mov_b32 v245, 0\n\t" \
  "buffer_load_dword v244, v245, %[rs], %[sab] offen sc1\n\t" \
  "s_waitcnt vmcnt(0)\n\t" \
  "v_readfirstlane_b32 s93, v244\n\t" \
  "s_nop 3\n\t" \
  "s_cmp_eq_u32 s93, 1\n\t" \
  "s_cbranch_scc1 .Lok%=\n\t" \
  "s_cmp_lt_u32 s92, 0x80000\n\t" \
  "s_cbranch_scc1 .Lpoll%=\n\t" \
  "v_mov_b32 v244, 1\n\t" \
  "buffer_store_dword v244, v245, %[rs], %[sab] offen sc1\n\t" \
  "s_branch .Lok%=\n\t" \
  ".Lend%=:\n\t"
#define QK_ASM_3 \
  "s_waitcnt lgkmcnt(0)\n\t" \
  "v_subrev_u32 v244, 0x8000, v250\n\t" \
  "ds_read_b128 v[216:219], v248 offset:45056\n\t" \
  "ds_read_b128 v[220:223], v248 offset:46080\n\t" \
  "ds_read_b128 v[224:227], v248 offset:47104\n\t" \
  "ds_read_b128 v[228:231], v248 offset:48128\n\t" \
  "ds_read_b128 v[208:211], v244 offset:0\n\t" \
  "ds_read_b128 v[212:215], v244 offset:1024\n\t" \
  "ds_read_b128 v[236:239], v244 offset:2048\n\t" \
  "ds_read_b128 v[240:243], v244 offset:3072\n\t" \
  "v_mov_b64 v[192:193], 0\n\t" \
  "v_mov_b64 v[194:195], 0\n\t" \
  "v_mov_b64 v[196:197], 0\n\t" \
  "v_mov_b64 v[198:199], 0\n\t" \
  "s_movk_i32 s92, 0x2000\n\t" \
  ".Lwt%=:\n\t" \
  "ds_read_b64 v[200:201], v248 offset:30720\n\t" \
  "s_memrealtime s[90:91]\n\t" \
  "s_sub_u32 s92, s92, 1\n\t" \
  "s_waitcnt lgkmcnt(0)\n\t" \
  "v_readfirstlane_b32 s93, v200\n\t" \
  "v_readfirstlane_b32 %[tgt], v201\n\t" \
  "s_cbranch_scc1 .Lgo%=\n\t" \
  "s_cmp_eq_u32 s93, %[step]\n\t" \
  "s_cbranch_scc1 .Lwx%=\n\t" \
  "s_sleep 1\n\t" \
  "s_branch .Lwt%=\n\t" \
  ".Lwx%=:\n\t" \
  "s_lshl_b32 s93, s90, 2\n\t" \
  "s_sub_i32 s93, %[tgt], s93\n\t" \
  "s_cmp_le_i32 s93, 0\n\t" \
  "s_cbranch_scc1 .Lgo%=\n\t" \
  "s_min_i32 s93, s93, 8000\n\t" \
  "s_add_u32 s93, s93, 3\n\t" \
  "s_lshr_b32 s93, s93, 2\n\t" \
  "s_add_u32 s93, s93, s90\n\t" \
  "s_memrealtime s[90:91]\n\t" \
  ".Lpace%=:\n\t" \
  "s_memrealtime s[94:95]\n\t" \
  "s_waitcnt lgkmcnt(1)\n\t" \
  "s_sub_i32 s91, s90, s93\n\t" \
  "s_cmp_ge_i32 s91, 0\n\t" \
  "s_cbranch_scc1 .Lgo%=\n\t" \
  "s_memrealtime s[90:91]\n\t" \
  "s_waitcnt lgkmcnt(1)\n\t" \
  "s_sub_i32 s95, s94, s93\n\t" \
  "s_cmp_lt_i32 s95, 0\n\t" \
  "s_cbranch_scc1 .Lpace%=\n\t" \
  ".Lgo%=:\n\t" \
  "s_mov_b32 s92, 0\n\t" \
  ".Lpoll%=:\n\t" \
  "buffer_load_dwordx4 v[200:203], v248, %[rs], %[som] offen sc1\n\t" \
  "buffer_load_dwordx4 v[204:207], v248, %[rs], %[som2] offen sc1\n\t" \
  "s_waitcnt vmcnt(1) lgkmcnt(0)\n\t" \
  "ds_write_b128 v249, v[200:203]\n\t" \
  "v_smfmac_f32_16x16x64_bf16 v[192:195], v[200:203], v[208:215], v251\n\t" \
  "v_max3_u32 v244, v200, v201, v202\n\t" \
  "v_smfmac_f32_16x16x64_bf16 v[196:199], v[200:203], v[236:243], v251\n\t" \
  "s_waitcnt vmcnt(0)\n\t" \
  "ds_write_b128 v249, v[204:207] offset:8192\n\t" \
  "v_smfmac_f32_16x16x64_bf16 v[192:195], v[204:207], v[216:223], v251\n\t" \
  "v_max3_u32 v244, v244, v203, v204\n\t" \
  "v_max3_u32 v244, v244, v205, v206\n\t" \
  "v_smfmac_f32_16x16x64_bf16 v[196:199], v[204:207], v[224:231], v251\n\t" \
  "v_max_u32 v244, v244, v207\n\t" \
  "v_cmp_le_u32 vcc, %[sent], v244\n\t" \
  "s_cbranch_vccnz .Lslow%=\n\t" \
  ".Lok%=:\n\t" \
  "ds_read_b128 v[216:219], v250 offset:32768\n\t" \
  "ds_read_b128 v[220:223], v250 offset:33792\n\t" \
  "ds_read_b128 v[224:227], v250 offset:34816\n\t" \
  "ds_read_b128 v[228:231], v250 offset:35840\n\t" \
  "s_waitcnt lgkmcnt(4)\n\t" \
  "s_barrier\n\t" \
  "ds_read_b128 v[208:211], v248 offset:2048\n\t" \
  "ds_read_b128 v[212:215], v248 offset:5120\n\t" \
  "ds_read_b128 v[236:239], v248 offset:6144\n\t" \
  "ds_read_b128 v[240:243], v248 offset:7168\n\t" \
  "s_waitcnt lgkmcnt(3)\n\t" \
  "ds_read_b128 v[244:247], v248 offset:12288\n\t" \
  "v_smfmac_f32_16x16x64_bf16 v[192:195], v[208:211], v[216:223], v251\n\t" \
  "ds_read_b128 v[216:219], v250 offset:0\n\t" \
  "v_smfmac_f32_16x16x64_bf16 v[196:199], v[208:211], v[224:231], v251\n\t" \
  "s_waitcnt lgkmcnt(4)\n\t" \
  "ds_read_b128 v[220:223], v250 offset:1024\n\t" \
  "v_smfmac_f32_16x16x64_bf16 v[192:195], v[212:215], v[0:7], v251\n\t" \
  "ds_read_b128 v[224:227], v250 offset:2048\n\t" \
  "v_smfmac_f32_16x16x64_bf16 v[196:199], v[212:215], v[8:15], v251\n\t" \
  "s_waitcnt lgkmcnt(5)\n\t" \
  "ds_read_b128 v[228:231], v250 offset:3072\n\t" \
  "v_smfmac_f32_16x16x64_bf16 v[192:195], v[236:239], v[16:23], v251\n\t" \
  "ds_read_b128 v[200:203], v248 offset:8192\n\t" \
  "v_smfmac_f32_16x16x64_bf16 v[196:199], v[236:239], v[24:31], v251\n\t" \
  "s_waitcnt lgkmcnt(6)\n\t" \
  "ds_read_b128 v[204:207], v248 offset:9216\n\t" \
  "v_smfmac_f32_16x16x64_bf16 v[192:195], v[240:243], v[32:39], v251\n\t" \
  "ds_read_b128 v[208:211], v248 offset:10240\n\t" \
  "v_smfmac_f32_16x16x64_bf16 v[196:199], v[240:243], v[40:47], v251\n\t" \
  "s_waitcnt lgkmcnt(5)\n\t" \
  "ds_read_b128 v[212:215], v248 offset:13312\n\t" \
  "v_smfmac_f32_16x16x64_bf16 v[192:195], v[244:247], v[216:223], v251\n\t" \
  "s_waitcnt lgkmcnt(4)\n\t" \
  "v_smfmac_f32_16x16x64_bf16 v[196:199], v[244:247], v[224:231], v251\n\t" \
  "s_waitcnt lgkmcnt(3)\n\t" \
  "ds_read_b128 v[236:239], v248 offset:14336\n\t" \
  "v_smfmac_f32_16x16x64_bf16 v[192:195], v[200:203], v[48:55], v251\n\t" \
  "ds_read_b128 v[240:243], v248 offset:15360\n\t" \
  "v_smfmac_f32_16x16x64_bf16 v[196:199], v[200:203], v[56:63], v251\n\t" \
  "s_waitcnt lgkmcnt(4)\n\t" \
  "v_smfmac_f32_16x16x64_bf16 v[192:195], v[204:207], v[64:71], v251\n\t" \
  "ds_read_b128 v[244:247], v248 offset:0\n\t" \
  "v_smfmac_f32_16x16x64_bf16 v[196:199], v[204:207], v[72:79], v251\n\t" \
  "s_waitcnt lgkmcnt(4)\n\t" \
  "v_smfmac_f32_16x16x64_bf16 v[192:195], v[208:211], v[80:87], v251\n\t" \
  "ds_read_b128 v[200:203], v248 offset:1024\n\t" \
  "v_smfmac_f32_16x16x64_bf16 v[196:199], v[208:211], v[88:95], v251\n\t" \
  "s_waitcnt lgkmcnt(4)\n\t" \
  "v_smfmac_f32_16x16x64_bf16 v[192:195], v[212:215], v[96:103], v251\n\t" \
  "ds_read_b128 v[204:207], v248 offset:4096\n\t" \
  "v_smfmac_f32_16x16x64_bf16 v[196:199], v[212:215], v[104:111], v251\n\t" \
  "s_waitcnt lgkmcnt(3)\n\t" \
  "v_smfmac_f32_16x16x64_bf16 v[192:195], v[236:239], v[112:119], v251\n\t" \
  "v_smfmac_f32_16x16x64_bf16 v[196:199], v[236:239], v[120:127], v251\n\t" \
  "v_smfmac_f32_16x16x64_bf16 v[192:195], v[240:243], v[128:135], v251\n\t" \
  "v_smfmac_f32_16x16x64_bf16 v[196:199], v[240:243], v[136:143], v251\n\t" \
  "s_waitcnt lgkmcnt(1)\n\t" \
  "v_smfmac_f32_16x16x64_bf16 v[192:195], v[244:247], v[144:151], v251\n\t" \
  "v_smfmac_f32_16x16x64_bf16 v[196:199], v[244:247], v[152:159], v251\n\t" \
  "v_smfmac_f32_16x16x64_bf16 v[192:195], v[200:203], v[160:167], v251\n\t" \
  "v_smfmac_f32_16x16x64_bf16 v[196:199], v[200:203], v[168:175], v251\n\t" \
  "s_waitcnt lgkmcnt(0)\n\t" \
  "v_smfmac_f32_16x16x64_bf16 v[192:195], v[204:207], v[176:183], v251\n\t" \
  "v_smfmac_f32_16x16x64_bf16 v[196:199], v[204:207], v[184:191], v251\n\t" \
  "ds_read_b128 v[200:203], v248 offset:19456\n\t" \
  "ds_read_b128 v[204:207], v248 offset:23552\n\t" \
  "v_mov_b32 v250, v232\n\t" \
  "v_mov_b32 v249, v233\n\t" \
  "s_nop 2\n\t" \
  "v_add_f32 v192, v192, v194\n\t" \
  "v_add_f32 v193, v193, v195\n\t" \
  "v_add_f32 v196, v196, v198\n\t" \
  "v_add_f32 v197, v197, v199\n\t" \
  "v_cvt_pk_bf16_f32 v192, v192, v193\n\t" \
  "v_cvt_pk_bf16_f32 v196, v196, v197\n\t" \
  "ds_write2_b32 v252, v192, v196 offset1:64\n\t" \
  "v_mov_b32 v251, v234\n\t" \
  "ds_read_b64_tr_b16 v[244:245], v250 offset:1536\n\t" \
  "ds_read_b64_tr_b16 v[246:247], v250 offset:1600\n\t" \
  "v_mov_b32 v212, %[step]\n\t" \
  "s_mov_b64 exec, 1\n\t" \
  "ds_write_b32 v248, v212 offset:28720\n\t" \
  "s_mov_b64 exec, -1\n\t" \
  "buffer_load_dwordx4 v[192:195], v248, %[rt], %[svt] offen offset:0 sc1\n\t" \
  "buffer_load_dwordx4 v[196:199], v248, %[rt], %[svt] offen offset:1024 sc1\n\t" \
  "s_waitcnt lgkmcnt(1)\n\t" \
  "v_mfma_f32_16x16x32_bf16 v[200:203], v[200:203], v[244:247], 0\n\t" \
  "v_mfma_f32_16x16x32_bf16 v[204:207], v[204:207], v[244:247], 0\n\t" \
  "s_nop 7\n\t" \
  "v_cndmask_b32 v216, v200, v204, %[nh]\n\t" \
  "v_cndmask_b32 v217, v201, v205, %[nh]\n\t" \
  "v_cndmask_b32 v218, v202, v206, %[nh]\n\t" \
  "v_cndmask_b32 v219, v203, v207, %[nh]\n\t" \
  "ds_write_b128 v248, v[216:219] offset:19456\n\t" \
  "buffer_load_dword v252, v249, %[rs], %[sv] offen offset:0 sc1\n\t" \
  "buffer_load_dword v253, v249, %[rs], %[sv] offen offset:256 sc1\n\t" \
  "ds_read_b32 v212, v248 offset:28672\n\t" \
  "ds_read_b64_tr_b16 v[232:233], v250 offset:0\n\t" \
  "ds_read_b64_tr_b16 v[234:235], v250 offset:64\n\t" \
  "ds_read_b64_tr_b16 v[236:237], v250 offset:512\n\t" \
  "ds_read_b64_tr_b16 v[238:239], v250 offset:576\n\t" \
  "ds_read_b64_tr_b16 v[240:241], v250 offset:1024\n\t" \
  "ds_read_b64_tr_b16 v[242:243], v250 offset:1088\n\t" \
  "s_setprio 3\n\t" \
  "s_waitcnt lgkmcnt(0)\n\t" \
  "v_cmp_ne_u32 vcc, %[step], v212\n\t" \
  "s_barrier\n\t" \
  "s_and_b32 s93, vcc_lo, 0xf\n\t" \
  "s_cbranch_scc1 .Lre%=\n\t" \
  ".Lrb%=:\n\t" \
  "ds_read_b128 v[220:223], v248 offset:16384\n\t" \
  "ds_read_b128 v[224:227], v248 offset:17408\n\t" \
  "ds_read_b128 v[228:231], v248 offset:18432\n\t" \
  "ds_read_b64_tr_b16 v[212:213], v250 offset:3584\n\t" \
  "ds_read_b64_tr_b16 v[214:215], v250 offset:3648\n\t" \
  "ds_read_b64_tr_b16 v[200:201], v250 offset:2048\n\t" \
  "ds_read_b64_tr_b16 v[202:203], v250 offset:2112\n\t" \
  "ds_read_b64_tr_b16 v[204:205], v250 offset:2560\n\t" \
  "ds_read_b64_tr_b16 v[206:207], v250 offset:2624\n\t" \
  "ds_read_b64_tr_b16 v[208:209], v250 offset:3072\n\t" \
  "ds_read_b64_tr_b16 v[210:211], v250 offset:3136\n\t" \
  "s_waitcnt lgkmcnt(10)\n\t" \
  "v_pk_add_f32 v[216:217], v[216:217], v[220:221]\n\t" \
  "v_pk_add_f32 v[218:219], v[218:219], v[222:223]\n\t" \
  "s_waitcnt lgkmcnt(8)\n\t" \
  "v_pk_add_f32 v[224:225], v[224:225], v[228:229]\n\t" \
  "v_pk_add_f32 v[226:227], v[226:227], v[230:231]\n\t" \
  "v_pk_add_f32 v[216:217], v[216:217], v[224:225]\n\t" \
  "v_pk_add_f32 v[218:219], v[218:219], v[226:227]\n\t" \
  "s_waitcnt lgkmcnt(6)\n\t" \
  "v_mfma_f32_16x16x32_bf16 v[212:215], v[212:215], v[244:247], 0\n\t" \
  "v_max3_f32 v224, v216, v217, v218\n\t" \
  "v_max_f32 v224, v224, v219\n\t" \
  "s_waitcnt lgkmcnt(4)\n\t" \
  "v_mfma_f32_16x16x32_bf16 v[212:215], v[200:203], v[232:235], v[212:215]\n\t" \
  "v_max_f32_dpp v225, v224, v224 row_ror:8 row_mask:0xf bank_mask:0xf\n\t" \
  "v_max_f32_dpp v226, v224, v224 row_ror:8 row_mask:0xf bank_mask:0xf\n\t" \
  "s_waitcnt lgkmcnt(2)\n\t" \
  "v_mfma_f32_16x16x32_bf16 v[212:215], v[204:207], v[236:239], v[212:215]\n\t" \
  "v_permlane32_swap_b32 v225, v226\n\t" \
  "s_waitcnt lgkmcnt(0)\n\t" \
  "v_mfma_f32_16x16x32_bf16 v[212:215], v[208:211], v[240:243], v[212:215]\n\t" \
  "v_max_f32 v231, v225, v226\n\t" \
  "v_mov_b32 v220, 0x3f803f80\n\t" \
  "v_mov_b32 v221, 0x3f803f80\n\t" \
  "v_mov_b64 v[222:223], v[220:221]\n\t" \
  "v_mov_b32 v245, 0\n\t" \
  "v_mov_b32 v246, 0\n\t" \
  "s_nop 1\n\t" \
  "v_max3_f32 v200, v212, v213, v231\n\t" \
  "v_max3_f32 v201, v200, v214, v215\n\t" \
  "v_max3_f32 v202, v200, v214, v215\n\t" \
  "v_cndmask_b32 v212, v212, v214, %[up]\n\t" \
  "v_cndmask_b32 v213, v213, v215, %[up]\n\t" \
  "v_permlane16_swap_b32 v201, v202\n\t" \
  "v_max_f32 v200, v201, v202\n\t" \
  "v_mul_f32 v200, %[negc], v200\n\t" \
  "v_fma_f32 v212, v212, %[c], v200\n\t" \
  "v_fma_f32 v213, v213, %[c], v200\n\t" \
  "v_exp_f32 v212, v212\n\t" \
  "v_fma_f32 v216, v216, %[c], v200\n\t" \
  "v_exp_f32 v213, v213\n\t" \
  "v_fma_f32 v217, v217, %[c], v200\n\t" \
  "v_exp_f32 v216, v216\n\t" \
  "v_fma_f32 v218, v218, %[c], v200\n\t" \
  "v_cvt_pk_bf16_f32 v244, v212, v213\n\t" \
  "v_cvt_pk_bf16_f32 v247, v212, v213\n\t" \
  "v_exp_f32 v217, v217\n\t" \
  "v_fma_f32 v219, v219, %[c], v200\n\t" \
  "v_mfma_f32_16x16x16_bf16 v[208:211], v[220:221], v[244:245], 0\n\t" \
  "v_exp_f32 v218, v218\n\t" \
  "s_waitcnt vmcnt(0)\n\t" \
  "v_mfma_f32_16x16x16_bf16 v[224:227], v[252:253], v[244:245], 0\n\t" \
  "v_cvt_pk_bf16_f32 v206, v216, v217\n\t" \
  "v_cvt_pk_bf16_f32 v204, v216, v217\n\t" \
  "v_exp_f32 v219, v219\n\t" \
  "v_mfma_f32_16x16x16_bf16 v[228:231], v[252:253], v[246:247], 0\n\t" \
  "v_cvt_pk_bf16_f32 v207, v218, v219\n\t" \
  "v_cvt_pk_bf16_f32 v205, v218, v219\n\t" \
  "v_mov_b32_dpp v204, v206 row_ror:8 row_mask:0xf bank_mask:0xc\n\t" \
  "v_mov_b32_dpp v206, v206 row_ror:8 row_mask:0xf bank_mask:0x3\n\t" \
  "v_mov_b32_dpp v205, v207 row_ror:8 row_mask:0xf bank_mask:0xc\n\t" \
  "v_mov_b32_dpp v207, v207 row_ror:8 row_mask:0xf bank_mask:0x3\n\t" \
  "v_max_u32 v200, v252, v253\n\t" \
  "v_cmp_le_u32 vcc, %[sent], v200\n\t" \
  "v_mfma_f32_16x16x32_bf16 v[208:211], v[220:223], v[204:207], v[208:211]\n\t" \
  ".Lpv%=:\n\t" \
  "v_mfma_f32_16x16x32_bf16 v[224:227], v[192:195], v[204:207], v[224:227]\n\t" \
  "v_mfma_f32_16x16x32_bf16 v[228:231], v[196:199], v[204:207], v[228:231]\n\t" \
  "s_cbranch_vccnz .Lvs%=\n\t" \
  ".Lvok%=:\n\t" \
  "s_nop 4\n\t" \
  "v_rcp_f32 v200, v208\n\t" \
  "v_cndmask_b32 v224, v224, v226, %[nh]\n\t" \
  "v_cndmask_b32 v225, v225, v227, %[nh]\n\t" \
  "v_mul_f32 v224, v224, v200\n\t" \
  "v_mul_f32 v225, v225, v200\n\t" \
  "v_cvt_pk_bf16_f32 v224, v224, v225\n\t" \
  "buffer_store_dword v224, v251, %[rs], %[so] offen offset:0 sc1\n\t" \
  "v_cndmask_b32 v228, v228, v230, %[nh]\n\t" \
  "v_cndmask_b32 v229, v229, v231, %[nh]\n\t" \
  "v_mul_f32 v228, v228, v200\n\t" \
  "v_mul_f32 v229, v229, v200\n\t" \
  "v_cvt_pk_bf16_f32 v228, v228, v229\n\t" \
  "buffer_store_dword v228, v251, %[rs], %[so] offen offset:256 sc1\n\t" \
  "s_memtime s[90:91]\n\t" \
  "s_setprio 0\n\t" \
  "s_waitcnt lgkmcnt(0)\n\t" \
  "s_mov_b32 %[tout], s90\n\t" \
  "s_mov_b32 %[spins], s92\n\t" \
  "s_branch .Lend%=\n\t" \
  ".Lre%=:\n\t" \
  "ds_read_b64_tr_b16 v[232:233], v250 offset:0\n\t" \
  "ds_read_b64_tr_b16 v[234:235], v250 offset:64\n\t" \
  "ds_read_b64_tr_b16 v[236:237], v250 offset:512\n\t" \
  "ds_read_b64_tr_b16 v[238:239], v250 offset:576\n\t" \
  "ds_read_b64_tr_b16 v[240:241], v250 offset:1024\n\t" \
  "ds_read_b64_tr_b16 v[242:243], v250 offset:1088\n\t" \
  "s_waitcnt lgkmcnt(0)\n\t" \
  "s_branch .Lrb%=\n\t" \
  ".Lvs%=:\n\t" \
  "v_cmp_eq_u32 vcc, %[sent], v252\n\t" \
  "v_cmp_eq_u32 s[94:95], %[sent], v253\n\t" \
  "s_or_b64 s[94:95], s[94:95], vcc\n\t" \
  "s_cbranch_scc0 .Lvok%=\n\t" \
  "s_add_u32 s92, s92, 1\n\t" \
  "s_sleep 2\n\t" \
  "s_and_b32 s93, s92, 63\n\t" \
  "s_cbranch_scc1 .Lvr%=\n\t" \
  "buffer_load_dword v200, off, %[rs], %[sab] sc1\n\t" \
  "s_waitcnt vmcnt(0)\n\t" \
  "v_readfirstlane_b32 s93, v200\n\t" \
  "s_nop 3\n\t" \
  "s_cmp_eq_u32 s93, 1\n\t" \
  "s_cbranch_scc1 .Lvok%=\n\t" \
  "s_cmp_lt_u32 s92, 0x80000\n\t" \
  "s_cbranch_scc1 .Lvr%=\n\t" \
  "v_mov_b32 v200, 1\n\t" \
  "buffer_store_dword v200, off, %[rs], %[sab] sc1\n\t" \
  "s_branch .Lvok%=\n\t" \
  ".Lvr%=:\n\t" \
  "buffer_load_dword v252, v249, %[rs], %[sv] offen offset:0 sc1\n\t" \
  "buffer_load_dword v253, v249, %[rs], %[sv] offen offset:256 sc1\n\t" \
  "s_waitcnt vmcnt(0)\n\t" \
  "v_mfma_f32_16x16x16_bf16 v[224:227], v[252:253], v[244:245], 0\n\t" \
  "v_mfma_f32_16x16x16_bf16 v[228:231], v[252:253], v[246:247], 0\n\t" \
  "v_max_u32 v200, v252, v253\n\t" \
  "v_cmp_le_u32 vcc, %[sent], v200\n\t" \
  "s_nop 4\n\t" \
  "s_branch .Lpv%=\n\t" \
  "s_branch .Lend%=\n\t" \
  ".Lslow%=:\n\t" \
  "s_mov_b64 s[94:95], 0\n\t" \
  "v_cmp_eq_u32 vcc, %[sent], v200\n\t" \
  "s_or_b64 s[94:95], s[94:95], vcc\n\t" \
  "v_cmp_eq_u32 vcc, %[sent], v201\n\t" \
  "s_or_b64 s[94:95], s[94:95], vcc\n\t" \
  "v_cmp_eq_u32 vcc, %[sent], v202\n\t" \
  "s_or_b64 s[94:95], s[94:95], vcc\n\t" \
  "v_cmp_eq_u32 vcc, %[sent], v203\n\t" \
  "s_or_b64 s[94:95], s[94:95], vcc\n\t" \
  "v_cmp_eq_u32 vcc, %[sent], v204\n\t" \
  "s_or_b64 s[94:95], s[94:95], vcc\n\t" \
  "v_cmp_eq_u32 vcc, %[sent], v205\n\t" \
  "s_or_b64 s[94:95], s[94:95], vcc\n\t" \
  "v_cmp_eq_u32 vcc, %[sent], v206\n\t" \
  "s_or_b64 s[94:95], s[94:95], vcc\n\t" \
  "v_cmp_eq_u32 vcc, %[sent], v207\n\t" \
  "s_or_b64 s[94:95], s[94:95], vcc\n\t" \
  "s_cmp_eq_u64 s[94:95], 0\n\t" \
  "s_cbranch_scc1 .Lok%=\n\t" \
  "v_mov_b64 v[192:193], 0\n\t" \
  "v_mov_b64 v[194:195], 0\n\t" \
  "v_mov_b64 v[196:197], 0\n\t" \
  "v_mov_b64 v[198:199], 0\n\t" \
  "s_add_u32 s92, s92, 1\n\t" \
  "s_sleep 6\n\t" \
  "s_cmp_eq_u32 s92, 1\n\t" \
  "s_cbranch_scc1 .Lpoll%=\n\t" \
  "s_sleep 1\n\t" \
  "s_and_b32 s93, s92, 63\n\t" \
  "s_cmp_lg_u32 s93, 0\n\t" \
  "s_cbranch_scc1 .Lpoll%=\n\t" \
  "v_mov_b32 v245, 0\n\t" \
  "buffer_load_dword v244, v245, %[rs], %[sab] offen sc1\n\t" \
  "s_waitcnt vmcnt(0)\n\t" \
  "v_readfirstlane_b32 s93, v244\n\t" \
  "s_nop 3\n\t" \
  "s_cmp_eq_u32 s93, 1\n\t" \
  "s_cbranch_scc1 .Lok%=\n\t" \
  "s_cmp_lt_u32 s92, 0x80000\n\t" \
  "s_cbranch_scc1 .Lpoll%=\n\t" \
  "v_mov_b32 v244, 1\n\t" \
  "buffer_store_dword v244, v245, %[rs], %[sab] offen sc1\n\t" \
  "s_branch .Lok%=\n\t" \
  ".Lend%=:\n\t"
#define QK_ASM_4 \
  "s_waitcnt lgkmcnt(0)\n\t" \
  "v_subrev_u32 v244, 0x8000, v250\n\t" \
  "ds_read_b128 v[216:219], v248 offset:49152\n\t" \
  "ds_read_b128 v[220:223], v248 offset:50176\n\t" \
  "ds_read_b128 v[224:227], v248 offset:51200\n\t" \
  "ds_read_b128 v[228:231], v248 offset:52224\n\t" \
  "ds_read_b128 v[208:211], v244 offset:0\n\t" \
  "ds_read_b128 v[212:215], v244 offset:1024\n\t" \
  "ds_read_b128 v[236:239], v244 offset:2048\n\t" \
  "ds_read_b128 v[240:243], v244 offset:3072\n\t" \
  "v_mov_b64 v[192:193], 0\n\t" \
  "v_mov_b64 v[194:195], 0\n\t" \
  "v_mov_b64 v[196:197], 0\n\t" \
  "v_mov_b64 v[198:199], 0\n\t" \
  "s_movk_i32 s92, 0x2000\n\t" \
  ".Lwt%=:\n\t" \
  "ds_read_b64 v[200:201], v248 offset:30720\n\t" \
  "s_memrealtime s[90:91]\n\t" \
  "s_sub_u32 s92, s92, 1\n\t" \
  "s_waitcnt lgkmcnt(0)\n\t" \
  "v_readfirstlane_b32 s93, v200\n\t" \
  "v_readfirstlane_b32 %[tgt], v201\n\t" \
  "s_cbranch_scc1 .Lgo%=\n\t" \
  "s_cmp_eq_u32 s93, %[step]\n\t" \
  "s_cbranch_scc1 .Lwx%=\n\t" \
  "s_sleep 1\n\t" \
  "s_branch .Lwt%=\n\t" \
  ".Lwx%=:\n\t" \
  "s_lshl_b32 s93, s90, 2\n\t" \
  "s_sub_i32 s93, %[tgt], s93\n\t" \
  "s_cmp_le_i32 s93, 0\n\t" \
  "s_cbranch_scc1 .Lgo%=\n\t" \
  "s_min_i32 s93, s93, 8000\n\t" \
  "s_add_u32 s93, s93, 3\n\t" \
  "s_lshr_b32 s93, s93, 2\n\t" \
  "s_add_u32 s93, s93, s90\n\t" \
  "s_memrealtime s[90:91]\n\t" \
  ".Lpace%=:\n\t" \
  "s_memrealtime s[94:95]\n\t" \
  "s_waitcnt lgkmcnt(1)\n\t" \
  "s_sub_i32 s91, s90, s93\n\t" \
  "s_cmp_ge_i32 s91, 0\n\t" \
  "s_cbranch_scc1 .Lgo%=\n\t" \
  "s_memrealtime s[90:91]\n\t" \
  "s_waitcnt lgkmcnt(1)\n\t" \
  "s_sub_i32 s95, s94, s93\n\t" \
  "s_cmp_lt_i32 s95, 0\n\t" \
  "s_cbranch_scc1 .Lpace%=\n\t" \
  ".Lgo%=:\n\t" \
  "s_mov_b32 s92, 0\n\t" \
  ".Lpoll%=:\n\t" \
  "buffer_load_dwordx4 v[200:203], v248, %[rs], %[som] offen sc1\n\t" \
  "buffer_load_dwordx4 v[204:207], v248, %[rs], %[som2] offen sc1\n\t" \
  "s_waitcnt vmcnt(1) lgkmcnt(0)\n\t" \
  "ds_write_b128 v249, v[200:203]\n\t" \
  "v_smfmac_f32_16x16x64_bf16 v[192:195], v[200:203], v[208:215], v251\n\t" \
  "v_max3_u32 v244, v200, v201, v202\n\t" \
  "v_smfmac_f32_16x16x64_bf16 v[196:199], v[200:203], v[236:243], v251\n\t" \
  "s_waitcnt vmcnt(0)\n\t" \
  "ds_write_b128 v249, v[204:207] offset:8192\n\t" \
  "v_smfmac_f32_16x16x64_bf16 v[192:195], v[204:207], v[216:223], v251\n\t" \
  "v_max3_u32 v244, v244, v203, v204\n\t" \
  "v_max3_u32 v244, v244, v205, v206\n\t" \
  "v_smfmac_f32_16x16x64_bf16 v[196:199], v[204:207], v[224:231], v251\n\t" \
  "v_max_u32 v244, v244, v207\n\t" \
  "v_cmp_le_u32 vcc, %[sent], v244\n\t" \
  "s_cbranch_vccnz .Lslow%=\n\t" \
  ".Lok%=:\n\t" \
  "ds_read_b128 v[216:219], v250 offset:32768\n\t" \
  "ds_read_b128 v[220:223], v250 offset:33792\n\t" \
  "ds_read_b128 v[224:227], v250 offset:34816\n\t" \
  "ds_read_b128 v[228:231], v250 offset:35840\n\t" \
  "s_waitcnt lgkmcnt(4)\n\t" \
  "s_barrier\n\t" \
  "ds_read_b128 v[208:211], v248 offset:5120\n\t" \
  "ds_read_b128 v[212:215], v248 offset:6144\n\t" \
  "ds_read_b128 v[236:239], v248 offset:7168\n\t" \
  "ds_read_b128 v[240:243], v248 offset:8192\n\t" \
  "s_waitcnt lgkmcnt(3)\n\t" \
  "ds_read_b128 v[244:247], v248 offset:13312\n\t" \
  "v_smfmac_f32_16x16x64_bf16 v[192:195], v[208:211], v[216:223], v251\n\t" \
  "ds_read_b128 v[216:219], v250 offset:0\n\t" \
  "v_smfmac_f32_16x16x64_bf16 v[196:199], v[208:211], v[224:231], v251\n\t" \
  "s_waitcnt lgkmcnt(4)\n\t" \
  "ds_read_b128 v[220:223], v250 offset:1024\n\t" \
  "v_smfmac_f32_16x16x64_bf16 v[192:195], v[212:215], v[0:7], v251\n\t" \
  "ds_read_b128 v[224:227], v250 offset:2048\n\t" \
  "v_smfmac_f32_16x16x64_bf16 v[196:199], v[212:215], v[8:15], v251\n\t" \
  "s_waitcnt lgkmcnt(5)\n\t" \
  "ds_read_b128 v[228:231], v250 offset:3072\n\t" \
  "v_smfmac_f32_16x16x64_bf16 v[192:195], v[236:239], v[16:23], v251\n\t" \
  "ds_read_b128 v[200:203], v248 offset:9216\n\t" \
  "v_smfmac_f32_16x16x64_bf16 v[196:199], v[236:239], v[24:31], v251\n\t" \
  "s_waitcnt lgkmcnt(6)\n\t" \
  "ds_read_b128 v[204:207], v248 offset:10240\n\t" \
  "v_smfmac_f32_16x16x64_bf16 v[192:195], v[240:243], v[32:39], v251\n\t" \
  "ds_read_b128 v[208:211], v248 offset:11264\n\t" \
  "v_smfmac_f32_16x16x64_bf16 v[196:199], v[240:243], v[40:47], v251\n\t" \
  "s_waitcnt lgkmcnt(5)\n\t" \
  "ds_read_b128 v[212:215], v248 offset:14336\n\t" \
  "v_smfmac_f32_16x16x64_bf16 v[192:195], v[244:247], v[216:223], v251\n\t" \
  "s_waitcnt lgkmcnt(4)\n\t" \
  "v_smfmac_f32_16x16x64_bf16 v[196:199], v[244:247], v[224:231], v251\n\t" \
  "s_waitcnt lgkmcnt(3)\n\t" \
  "ds_read_b128 v[236:239], v248 offset:15360\n\t" \
  "v_smfmac_f32_16x16x64_bf16 v[192:195], v[200:203], v[48:55], v251\n\t" \
  "ds_read_b128 v[240:243], v248 offset:0\n\t" \
  "v_smfmac_f32_16x16x64_bf16 v[196:199], v[200:203], v[56:63], v251\n\t" \
  "s_waitcnt lgkmcnt(4)\n\t" \
  "v_smfmac_f32_16x16x64_bf16 v[192:195], v[204:207], v[64:71], v251\n\t" \
  "ds_read_b128 v[244:247], v248 offset:1024\n\t" \
  "v_smfmac_f32_16x16x64_bf16 v[196:199], v[204:207], v[72:79], v251\n\t" \
  "s_waitcnt lgkmcnt(4)\n\t" \
  "v_smfmac_f32_16x16x64_bf16 v[192:195], v[208:211], v[80:87], v251\n\t" \
  "ds_read_b128 v[200:203], v248 offset:2048\n\t" \
  "v_smfmac_f32_16x16x64_bf16 v[196:199], v[208:211], v[88:95], v251\n\t" \
  "s_waitcnt lgkmcnt(4)\n\t" \
  "v_smfmac_f32_16x16x64_bf16 v[192:195], v[212:215], v[96:103], v251\n\t" \
  "ds_read_b128 v[204:207], v248 offset:3072\n\t" \
  "v_smfmac_f32_16x16x64_bf16 v[196:199], v[212:215], v[104:111], v251\n\t" \
  "s_waitcnt lgkmcnt(3)\n\t" \
  "v_smfmac_f32_16x16x64_bf16 v[192:195], v[236:239], v[112:119], v251\n\t" \
  "v_smfmac_f32_16x16x64_bf16 v[196:199], v[236:239], v[120:127], v251\n\t" \
  "v_smfmac_f32_16x16x64_bf16 v[192:195], v[240:243], v[128:135], v251\n\t" \
  "v_smfmac_f32_16x16x64_bf16 v[196:199], v[240:243], v[136:143], v251\n\t" \
  "s_waitcnt lgkmcnt(1)\n\t" \
  "v_smfmac_f32_16x16x64_bf16 v[192:195], v[244:247], v[144:151], v251\n\t" \
  "v_smfmac_f32_16x16x64_bf16 v[196:199], v[244:247], v[152:159], v251\n\t" \
  "v_smfmac_f32_16x16x64_bf16 v[192:195], v[200:203], v[160:167], v251\n\t" \
  "v_smfmac_f32_16x16x64_bf16 v[196:199], v[200:203], v[168:175], v251\n\t" \
  "s_waitcnt lgkmcnt(0)\n\t" \
  "v_smfmac_f32_16x16x64_bf16 v[192:195], v[204:207], v[176:183], v251\n\t" \
  "v_smfmac_f32_16x16x64_bf16 v[196:199], v[204:207], v[184:191], v251\n\t" \
  "s_nop 6\n\t" \
  "v_add_f32 v192, v192, v194\n\t" \
  "v_add_f32 v193, v193, v195\n\t" \
  "v_add_f32 v196, v196, v198\n\t" \
  "v_add_f32 v197, v197, v199\n\t" \
  "v_cvt_pk_bf16_f32 v192, v192, v193\n\t" \
  "v_cvt_pk_bf16_f32 v196, v196, v197\n\t" \
  "ds_write2_b32 v252, v192, v196 offset1:64\n\t" \
  "s_waitcnt lgkmcnt(0)\n\t" \
  "s_barrier\n\t" \
  "buffer_store_dword v192, v235, %[rs], %[sk] offen sc1\n\t" \
  "buffer_store_dword v196, v235, %[rs], %[sk] offen offset:256 sc1\n\t" \
  "s_mov_b32 %[spins], s92\n\t" \
  "s_branch .Lend%=\n\t" \
  ".Lslow%=:\n\t" \
  "s_mov_b64 s[94:95], 0\n\t" \
  "v_cmp_eq_u32 vcc, %[sent], v200\n\t" \
  "s_or_b64 s[94:95], s[94:95], vcc\n\t" \
  "v_cmp_eq_u32 vcc, %[sent], v201\n\t" \
  "s_or_b64 s[94:95], s[94:95], vcc\n\t" \
  "v_cmp_eq_u32 vcc, %[sent], v202\n\t" \
  "s_or_b64 s[94:95], s[94:95], vcc\n\t" \
  "v_cmp_eq_u32 vcc, %[sent], v203\n\t" \
  "s_or_b64 s[94:95], s[94:95], vcc\n\t" \
  "v_cmp_eq_u32 vcc, %[sent], v204\n\t" \
  "s_or_b64 s[94:95], s[94:95], vcc\n\t" \
  "v_cmp_eq_u32 vcc, %[sent], v205\n\t" \
  "s_or_b64 s[94:95], s[94:95], vcc\n\t" \
  "v_cmp_eq_u32 vcc, %[sent], v206\n\t" \
  "s_or_b64 s[94:95], s[94:95], vcc\n\t" \
  "v_cmp_eq_u32 vcc, %[sent], v207\n\t" \
  "s_or_b64 s[94:95], s[94:95], vcc\n\t" \
  "s_cmp_eq_u64 s[94:95], 0\n\t" \
  "s_cbranch_scc1 .Lok%=\n\t" \
  "v_mov_b64 v[192:193], 0\n\t" \
  "v_mov_b64 v[194:195], 0\n\t" \
  "v_mov_b64 v[196:197], 0\n\t" \
  "v_mov_b64 v[198:199], 0\n\t" \
  "s_add_u32 s92, s92, 1\n\t" \
  "s_sleep 6\n\t" \
  "s_cmp_eq_u32 s92, 1\n\t" \
  "s_cbranch_scc1 .Lpoll%=\n\t" \
  "s_sleep 1\n\t" \
  "s_and_b32 s93, s92, 63\n\t" \
  "s_cmp_lg_u32 s93, 0\n\t" \
  "s_cbranch_scc1 .Lpoll%=\n\t" \
  "v_mov_b32 v245, 0\n\t" \
  "buffer_load_dword v244, v245, %[rs], %[sab] offen sc1\n\t" \
  "s_waitcnt vmcnt(0)\n\t" \
  "v_readfirstlane_b32 s93, v244\n\t" \
  "s_nop 3\n\t" \
  "s_cmp_eq_u32 s93, 1\n\t" \
  "s_cbranch_scc1 .Lok%=\n\t" \
  "s_cmp_lt_u32 s92, 0x80000\n\t" \
  "s_cbranch_scc1 .Lpoll%=\n\t" \
  "v_mov_b32 v244, 1\n\t" \
  "buffer_store_dword v244, v245, %[rs], %[sab] offen sc1\n\t" \
  "s_branch .Lok%=\n\t" \
  ".Lend%=:\n\t"
#define QK_ASM_5 \
  "s_waitcnt lgkmcnt(0)\n\t" \
  "v_subrev_u32 v244, 0x8000, v250\n\t" \
  "ds_read_b128 v[216:219], v248 offset:53248\n\t" \
  "ds_read_b128 v[220:223], v248 offset:54272\n\t" \
  "ds_read_b128 v[224:227], v248 offset:55296\n\t" \
  "ds_read_b128 v[228:231], v248 offset:56320\n\t" \
  "ds_read_b128 v[208:211], v244 offset:0\n\t" \
  "ds_read_b128 v[212:215], v244 offset:1024\n\t" \
  "ds_read_b128 v[236:239], v244 offset:2048\n\t" \
  "ds_read_b128 v[240:243], v244 offset:3072\n\t" \
  "v_mov_b64 v[192:193], 0\n\t" \
  "v_mov_b64 v[194:195], 0\n\t" \
  "v_mov_b64 v[196:197], 0\n\t" \
  "v_mov_b64 v[198:199], 0\n\t" \
  "s_movk_i32 s92, 0x2000\n\t" \
  ".Lwt%=:\n\t" \
  "ds_read_b64 v[200:201], v248 offset:30720\n\t" \
  "s_memrealtime s[90:91]\n\t" \
  "s_sub_u32 s92, s92, 1\n\t" \
  "s_waitcnt lgkmcnt(0)\n\t" \
  "v_readfirstlane_b32 s93, v200\n\t" \
  "v_readfirstlane_b32 %[tgt], v201\n\t" \
  "s_cbranch_scc1 .Lgo%=\n\t" \
  "s_cmp_eq_u32 s93, %[step]\n\t" \
  "s_cbranch_scc1 .Lwx%=\n\t" \
  "s_sleep 1\n\t" \
  "s_branch .Lwt%=\n\t" \
  ".Lwx%=:\n\t" \
  "s_lshl_b32 s93, s90, 2\n\t" \
  "s_sub_i32 s93, %[tgt], s93\n\t" \
  "s_cmp_le_i32 s93, 0\n\t" \
  "s_cbranch_scc1 .Lgo%=\n\t" \
  "s_min_i32 s93, s93, 8000\n\t" \
  "s_add_u32 s93, s93, 3\n\t" \
  "s_lshr_b32 s93, s93, 2\n\t" \
  "s_add_u32 s93, s93, s90\n\t" \
  "s_memrealtime s[90:91]\n\t" \
  ".Lpace%=:\n\t" \
  "s_memrealtime s[94:95]\n\t" \
  "s_waitcnt lgkmcnt(1)\n\t" \
  "s_sub_i32 s91, s90, s93\n\t" \
  "s_cmp_ge_i32 s91, 0\n\t" \
  "s_cbranch_scc1 .Lgo%=\n\t" \
  "s_memrealtime s[90:91]\n\t" \
  "s_waitcnt lgkmcnt(1)\n\t" \
  "s_sub_i32 s95, s94, s93\n\t" \
  "s_cmp_lt_i32 s95, 0\n\t" \
  "s_cbranch_scc1 .Lpace%=\n\t" \
  ".Lgo%=:\n\t" \
  "s_mov_b32 s92, 0\n\t" \
  ".Lpoll%=:\n\t" \
  "buffer_load_dwordx4 v[200:203], v248, %[rs], %[som] offen sc1\n\t" \
  "buffer_load_dwordx4 v[204:207], v248, %[rs], %[som2] offen sc1\n\t" \
  "s_waitcnt vmcnt(1) lgkmcnt(0)\n\t" \
  "ds_write_b128 v249, v[200:203]\n\t" \
  "v_smfmac_f32_16x16x64_bf16 v[192:195], v[200:203], v[208:215], v251\n\t" \
  "v_max3_u32 v244, v200, v201, v202\n\t" \
  "v_smfmac_f32_16x16x64_bf16 v[196:199], v[200:203], v[236:243], v251\n\t" \
  "s_waitcnt vmcnt(0)\n\t" \
  "ds_write_b128 v249, v[204:207] offset:8192\n\t" \
  "v_smfmac_f32_16x16x64_bf16 v[192:195], v[204:207], v[216:223], v251\n\t" \
  "v_max3_u32 v244, v244, v203, v204\n\t" \
  "v_max3_u32 v244, v244, v205, v206\n\t" \
  "v_smfmac_f32_16x16x64_bf16 v[196:199], v[204:207], v[224:231], v251\n\t" \
  "v_max_u32 v244, v244, v207\n\t" \
  "v_cmp_le_u32 vcc, %[sent], v244\n\t" \
  "s_cbranch_vccnz .Lslow%=\n\t" \
  ".Lok%=:\n\t" \
  "ds_read_b128 v[216:219], v250 offset:32768\n\t" \
  "ds_read_b128 v[220:223], v250 offset:33792\n\t" \
  "ds_read_b128 v[224:227], v250 offset:34816\n\t" \
  "ds_read_b128 v[228:231], v250 offset:35840\n\t" \
  "s_waitcnt lgkmcnt(4)\n\t" \
  "s_barrier\n\t" \
  "ds_read_b128 v[208:211], v248 offset:4096\n\t" \
  "ds_read_b128 v[212:215], v248 offset:7168\n\t" \
  "ds_read_b128 v[236:239], v248 offset:8192\n\t" \
  "ds_read_b128 v[240:243], v248 offset:9216\n\t" \
  "s_waitcnt lgkmcnt(3)\n\t" \
  "ds_read_b128 v[244:247], v248 offset:12288\n\t" \
  "v_smfmac_f32_16x16x64_bf16 v[192:195], v[208:211], v[216:223], v251\n\t" \
  "ds_read_b128 v[216:219], v250 offset:0\n\t" \
  "v_smfmac_f32_16x16x64_bf16 v[196:199], v[208:211], v[224:231], v251\n\t" \
  "s_waitcnt lgkmcnt(4)\n\t" \
  "ds_read_b128 v[220:223], v250 offset:1024\n\t" \
  "v_smfmac_f32_16x16x64_bf16 v[192:195], v[212:215], v[0:7], v251\n\t" \
  "ds_read_b128 v[224:227], v250 offset:2048\n\t" \
  "v_smfmac_f32_16x16x64_bf16 v[196:199], v[212:215], v[8:15], v251\n\t" \
  "s_waitcnt lgkmcnt(5)\n\t" \
  "ds_read_b128 v[228:231], v250 offset:3072\n\t" \
  "v_smfmac_f32_16x16x64_bf16 v[192:195], v[236:239], v[16:23], v251\n\t" \
  "ds_read_b128 v[200:203], v248 offset:10240\n\t" \
  "v_smfmac_f32_16x16x64_bf16 v[196:199], v[236:239], v[24:31], v251\n\t" \
  "s_waitcnt lgkmcnt(6)\n\t" \
  "ds_read_b128 v[204:207], v248 offset:11264\n\t" \
  "v_smfmac_f32_16x16x64_bf16 v[192:195], v[240:243], v[32:39], v251\n\t" \
  "ds_read_b128 v[208:211], v248 offset:14336\n\t" \
  "v_smfmac_f32_16x16x64_bf16 v[196:199], v[240:243], v[40:47], v251\n\t" \
  "s_waitcnt lgkmcnt(5)\n\t" \
  "ds_read_b128 v[212:215], v248 offset:15360\n\t" \
  "v_smfmac_f32_16x16x64_bf16 v[192:195], v[244:247], v[216:223], v251\n\t" \
  "s_waitcnt lgkmcnt(4)\n\t" \
  "v_smfmac_f32_16x16x64_bf16 v[196:199], v[244:247], v[224:231], v251\n\t" \
  "s_waitcnt lgkmcnt(3)\n\t" \
  "ds_read_b128 v[236:239], v248 offset:0\n\t" \
  "v_smfmac_f32_16x16x64_bf16 v[192:195], v[200:203], v[48:55], v251\n\t" \
  "ds_read_b128 v[240:243], v248 offset:1024\n\t" \
  "v_smfmac_f32_16x16x64_bf16 v[196:199], v[200:203], v[56:63], v251\n\t" \
  "s_waitcnt lgkmcnt(4)\n\t" \
  "v_smfmac_f32_16x16x64_bf16 v[192:195], v[204:207], v[64:71], v251\n\t" \
  "ds_read_b128 v[244:247], v248 offset:2048\n\t" \
  "v_smfmac_f32_16x16x64_bf16 v[196:199], v[204:207], v[72:79], v251\n\t" \
  "s_waitcnt lgkmcnt(4)\n\t" \
  "v_smfmac_f32_16x16x64_bf16 v[192:195], v[208:211], v[80:87], v251\n\t" \
  "ds_read_b128 v[200:203], v248 offset:3072\n\t" \
  "v_smfmac_f32_16x16x64_bf16 v[196:199], v[208:211], v[88:95], v251\n\t" \
  "s_waitcnt lgkmcnt(4)\n\t" \
  "v_smfmac_f32_16x16x64_bf16 v[192:195], v[212:215], v[96:103], v251\n\t" \
  "ds_read_b128 v[204:207], v248 offset:6144\n\t" \
  "v_smfmac_f32_16x16x64_bf16 v[196:199], v[212:215], v[104:111], v251\n\t" \
  "s_waitcnt lgkmcnt(3)\n\t" \
  "v_smfmac_f32_16x16x64_bf16 v[192:195], v[236:239], v[112:119], v251\n\t" \
  "v_smfmac_f32_16x16x64_bf16 v[196:199], v[236:239], v[120:127], v251\n\t" \
  "v_smfmac_f32_16x16x64_bf16 v[192:195], v[240:243], v[128:135], v251\n\t" \
  "v_smfmac_f32_16x16x64_bf16 v[196:199], v[240:243], v[136:143], v251\n\t" \
  "s_waitcnt lgkmcnt(1)\n\t" \
  "v_smfmac_f32_16x16x64_bf16 v[192:195], v[244:247], v[144:151], v251\n\t" \
  "v_smfmac_f32_16x16x64_bf16 v[196:199], v[244:247], v[152:159], v251\n\t" \
  "v_smfmac_f32_16x16x64_bf16 v[192:195], v[200:203], v[160:167], v251\n\t" \
  "v_smfmac_f32_16x16x64_bf16 v[196:199], v[200:203], v[168:175], v251\n\t" \
  "s_waitcnt lgkmcnt(0)\n\t" \
  "v_smfmac_f32_16x16x64_bf16 v[192:195], v[204:207], v[176:183], v251\n\t" \
  "v_smfmac_f32_16x16x64_bf16 v[196:199], v[204:207], v[184:191], v251\n\t" \
  "s_nop 6\n\t" \
  "v_add_f32 v192, v192, v194\n\t" \
  "v_add_f32 v193, v193, v195\n\t" \
  "v_add_f32 v196, v196, v198\n\t" \
  "v_add_f32 v197, v197, v199\n\t" \
  "v_cvt_pk_bf16_f32 v192, v192, v193\n\t" \
  "v_cvt_pk_bf16_f32 v196, v196, v197\n\t" \
  "ds_write2_b32 v252, v192, v196 offset1:64\n\t" \
  "s_waitcnt lgkmcnt(0)\n\t" \
  "s_barrier\n\t" \
  "buffer_store_dword v192, v235, %[rs], %[sk] offen sc1\n\t" \
  "buffer_store_dword v196, v235, %[rs], %[sk] offen offset:256 sc1\n\t" \
  "s_mov_b32 %[spins], s92\n\t" \
  "s_branch .Lend%=\n\t" \
  ".Lslow%=:\n\t" \
  "s_mov_b64 s[94:95], 0\n\t" \
  "v_cmp_eq_u32 vcc, %[sent], v200\n\t" \
  "s_or_b64 s[94:95], s[94:95], vcc\n\t" \
  "v_cmp_eq_u32 vcc, %[sent], v201\n\t" \
  "s_or_b64 s[94:95], s[94:95], vcc\n\t" \
  "v_cmp_eq_u32 vcc, %[sent], v202\n\t" \
  "s_or_b64 s[94:95], s[94:95], vcc\n\t" \
  "v_cmp_eq_u32 vcc, %[sent], v203\n\t" \
  "s_or_b64 s[94:95], s[94:95], vcc\n\t" \
  "v_cmp_eq_u32 vcc, %[sent], v204\n\t" \
  "s_or_b64 s[94:95], s[94:95], vcc\n\t" \
  "v_cmp_eq_u32 vcc, %[sent], v205\n\t" \
  "s_or_b64 s[94:95], s[94:95], vcc\n\t" \
  "v_cmp_eq_u32 vcc, %[sent], v206\n\t" \
  "s_or_b64 s[94:95], s[94:95], vcc\n\t" \
  "v_cmp_eq_u32 vcc, %[sent], v207\n\t" \
  "s_or_b64 s[94:95], s[94:95], vcc\n\t" \
  "s_cmp_eq_u64 s[94:95], 0\n\t" \
  "s_cbranch_scc1 .Lok%=\n\t" \
  "v_mov_b64 v[192:193], 0\n\t" \
  "v_mov_b64 v[194:195], 0\n\t" \
  "v_mov_b64 v[196:197], 0\n\t" \
  "v_mov_b64 v[198:199], 0\n\t" \
  "s_add_u32 s92, s92, 1\n\t" \
  "s_sleep 6\n\t" \
  "s_cmp_eq_u32 s92, 1\n\t" \
  "s_cbranch_scc1 .Lpoll%=\n\t" \
  "s_sleep 1\n\t" \
  "s_and_b32 s93, s92, 63\n\t" \
  "s_cmp_lg_u32 s93, 0\n\t" \
  "s_cbranch_scc1 .Lpoll%=\n\t" \
  "v_mov_b32 v245, 0\n\t" \
  "buffer_load_dword v244, v245, %[rs], %[sab] offen sc1\n\t" \
  "s_waitcnt vmcnt(0)\n\t" \
  "v_readfirstlane_b32 s93, v244\n\t" \
  "s_nop 3\n\t" \
  "s_cmp_eq_u32 s93, 1\n\t" \
  "s_cbranch_scc1 .Lok%=\n\t" \
  "s_cmp_lt_u32 s92, 0x80000\n\t" \
  "s_cbranch_scc1 .Lpoll%=\n\t" \
  "v_mov_b32 v244, 1\n\t" \
  "buffer_store_dword v244, v245, %[rs], %[sab] offen sc1\n\t" \
  "s_branch .Lok%=\n\t" \
  ".Lend%=:\n\t"
#define QK_ASM_6 \
  "s_waitcnt lgkmcnt(0)\n\t" \
  "v_subrev_u32 v244, 0x8000, v250\n\t" \
  "ds_read_b128 v[216:219], v248 offset:57344\n\t" \
  "ds_read_b128 v[220:223], v248 offset:58368\n\t" \
  "ds_read_b128 v[224:227], v248 offset:59392\n\t" \
  "ds_read_b128 v[228:231], v248 offset:60416\n\t" \
  "ds_read_b128 v[208:211], v244 offset:0\n\t" \
  "ds_read_b128 v[212:215], v244 offset:1024\n\t" \
  "ds_read_b128 v[236:239], v244 offset:2048\n\t" \
  "ds_read_b128 v[240:243], v244 offset:3072\n\t" \
  "v_mov_b64 v[192:193], 0\n\t" \
  "v_mov_b64 v[194:195], 0\n\t" \
  "v_mov_b64 v[196:197], 0\n\t" \
  "v_mov_b64 v[198:199], 0\n\t" \
  "s_movk_i32 s92, 0x2000\n\t" \
  ".Lwt%=:\n\t" \
  "ds_read_b64 v[200:201], v248 offset:30720\n\t" \
  "s_memrealtime s[90:91]\n\t" \
  "s_sub_u32 s92, s92, 1\n\t" \
  "s_waitcnt lgkmcnt(0)\n\t" \
  "v_readfirstlane_b32 s93, v200\n\t" \
  "v_readfirstlane_b32 %[tgt], v201\n\t" \
  "s_cbranch_scc1 .Lgo%=\n\t" \
  "s_cmp_eq_u32 s93, %[step]\n\t" \
  "s_cbranch_scc1 .Lwx%=\n\t" \
  "s_sleep 1\n\t" \
  "s_branch .Lwt%=\n\t" \
  ".Lwx%=:\n\t" \
  "s_lshl_b32 s93, s90, 2\n\t" \
  "s_sub_i32 s93, %[tgt], s93\n\t" \
  "s_cmp_le_i32 s93, 0\n\t" \
  "s_cbranch_scc1 .Lgo%=\n\t" \
  "s_min_i32 s93, s93, 8000\n\t" \
  "s_add_u32 s93, s93, 3\n\t" \
  "s_lshr_b32 s93, s93, 2\n\t" \
  "s_add_u32 s93, s93, s90\n\t" \
  "s_memrealtime s[90:91]\n\t" \
  ".Lpace%=:\n\t" \
  "s_memrealtime s[94:95]\n\t" \
  "s_waitcnt lgkmcnt(1)\n\t" \
  "s_sub_i32 s91, s90, s93\n\t" \
  "s_cmp_ge_i32 s91, 0\n\t" \
  "s_cbranch_scc1 .Lgo%=\n\t" \
  "s_memrealtime s[90:91]\n\t" \
  "s_waitcnt lgkmcnt(1)\n\t" \
  "s_sub_i32 s95, s94, s93\n\t" \
  "s_cmp_lt_i32 s95, 0\n\t" \
  "s_cbranch_scc1 .Lpace%=\n\t" \
  ".Lgo%=:\n\t" \
  "s_mov_b32 s92, 0\n\t" \
  ".Lpoll%=:\n\t" \
  "buffer_load_dwordx4 v[200:203], v248, %[rs], %[som] offen sc1\n\t" \
  "buffer_load_dwordx4 v[204:207], v248, %[rs], %[som2] offen sc1\n\t" \
  "s_waitcnt vmcnt(1) lgkmcnt(0)\n\t" \
  "ds_write_b128 v249, v[200:203]\n\t" \
  "v_smfmac_f32_16x16x64_bf16 v[192:195], v[200:203], v[208:215], v251\n\t" \
  "v_max3_u32 v244, v200, v201, v202\n\t" \
  "v_smfmac_f32_16x16x64_bf16 v[196:199], v[200:203], v[236:243], v251\n\t" \
  "s_waitcnt vmcnt(0)\n\t" \
  "ds_write_b128 v249, v[204:207] offset:8192\n\t" \
  "v_smfmac_f32_16x16x64_bf16 v[192:195], v[204:207], v[216:223], v251\n\t" \
  "v_max3_u32 v244, v244, v203, v204\n\t" \
  "v_max3_u32 v244, v244, v205, v206\n\t" \
  "v_smfmac_f32_16x16x64_bf16 v[196:199], v[204:207], v[224:231], v251\n\t" \
  "v_max_u32 v244, v244, v207\n\t" \
  "v_cmp_le_u32 vcc, %[sent], v244\n\t" \
  "s_cbranch_vccnz .Lslow%=\n\t" \
  ".Lok%=:\n\t" \
  "ds_read_b128 v[216:219], v250 offset:32768\n\t" \
  "ds_read_b128 v[220:223], v250 offset:33792\n\t" \
  "ds_read_b128 v[224:227], v250 offset:34816\n\t" \
  "ds_read_b128 v[228:231], v250 offset:35840\n\t" \
  "s_waitcnt lgkmcnt(4)\n\t" \
  "s_barrier\n\t" \
  "ds_read_b128 v[208:211], v248 offset:7168\n\t" \
  "ds_read_b128 v[212:215], v248 offset:8192\n\t" \
  "ds_read_b128 v[236:239], v248 offset:9216\n\t" \
  "ds_read_b128 v[240:243], v248 offset:10240\n\t" \
  "s_waitcnt lgkmcnt(3)\n\t" \
  "ds_read_b128 v[244:247], v248 offset:13312\n\t" \
  "v_smfmac_f32_16x16x64_bf16 v[192:195], v[208:211], v[216:223], v251\n\t" \
  "ds_read_b128 v[216:219], v250 offset:0\n\t" \
  "v_smfmac_f32_16x16x64_bf16 v[196:199], v[208:211], v[224:231], v251\n\t" \
  "s_waitcnt lgkmcnt(4)\n\t" \
  "ds_read_b128 v[220:223], v250 offset:1024\n\t" \
  "v_smfmac_f32_16x16x64_bf16 v[192:195], v[212:215], v[0:7], v251\n\t" \
  "ds_read_b128 v[224:227], v250 offset:2048\n\t" \
  "v_smfmac_f32_16x16x64_bf16 v[196:199], v[212:215], v[8:15], v251\n\t" \
  "s_waitcnt lgkmcnt(5)\n\t" \
  "ds_read_b128 v[228:231], v250 offset:3072\n\t" \
  "v_smfmac_f32_16x16x64_bf16 v[192:195], v[236:239], v[16:23], v251\n\t" \
  "ds_read_b128 v[200:203], v248 offset:11264\n\t" \
  "v_smfmac_f32_16x16x64_bf16 v[196:199], v[236:239], v[24:31], v251\n\t" \
  "s_waitcnt lgkmcnt(6)\n\t" \
  "ds_read_b128 v[204:207], v248 offset:12288\n\t" \
  "v_smfmac_f32_16x16x64_bf16 v[192:195], v[240:243], v[32:39], v251\n\t" \
  "ds_read_b128 v[208:211], v248 offset:15360\n\t" \
  "v_smfmac_f32_16x16x64_bf16 v[196:199], v[240:243], v[40:47], v251\n\t" \
  "s_waitcnt lgkmcnt(5)\n\t" \
  "ds_read_b128 v[212:215], v248 offset:0\n\t" \
  "v_smfmac_f32_16x16x64_bf16 v[192:195], v[244:247], v[216:223], v251\n\t" \
  "s_waitcnt lgkmcnt(4)\n\t" \
  "v_smfmac_f32_16x16x64_bf16 v[196:199], v[244:247], v[224:231], v251\n\t" \
  "s_waitcnt lgkmcnt(3)\n\t" \
  "ds_read_b128 v[236:239], v248 offset:1024\n\t" \
  "v_smfmac_f32_16x16x64_bf16 v[192:195], v[200:203], v[48:55], v251\n\t" \
  "ds_read_b128 v[240:243], v248 offset:2048\n\t" \
  "v_smfmac_f32_16x16x64_bf16 v[196:199], v[200:203], v[56:63], v251\n\t" \
  "s_waitcnt lgkmcnt(4)\n\t" \
  "v_smfmac_f32_16x16x64_bf16 v[192:195], v[204:207], v[64:71], v251\n\t" \
  "ds_read_b128 v[244:247], v248 offset:3072\n\t" \
  "v_smfmac_f32_16x16x64_bf16 v[196:199], v[204:207], v[72:79], v251\n\t" \
  "s_waitcnt lgkmcnt(4)\n\t" \
  "v_smfmac_f32_16x16x64_bf16 v[192:195], v[208:211], v[80:87], v251\n\t" \
  "ds_read_b128 v[200:203], v248 offset:4096\n\t" \
  "v_smfmac_f32_16x16x64_bf16 v[196:199], v[208:211], v[88:95], v251\n\t" \
  "s_waitcnt lgkmcnt(4)\n\t" \
  "v_smfmac_f32_16x16x64_bf16 v[192:195], v[212:215], v[96:103], v251\n\t" \
  "ds_read_b128 v[204:207], v248 offset:5120\n\t" \
  "v_smfmac_f32_16x16x64_bf16 v[196:199], v[212:215], v[104:111], v251\n\t" \
  "s_waitcnt lgkmcnt(3)\n\t" \
  "v_smfmac_f32_16x16x64_bf16 v[192:195], v[236:239], v[112:119], v251\n\t" \
  "v_smfmac_f32_16x16x64_bf16 v[196:199], v[236:239], v[120:127], v251\n\t" \
  "v_smfmac_f32_16x16x64_bf16 v[192:195], v[240:243], v[128:135], v251\n\t" \
  "v_smfmac_f32_16x16x64_bf16 v[196:199], v[240:243], v[136:143], v251\n\t" \
  "s_waitcnt lgkmcnt(1)\n\t" \
  "v_smfmac_f32_16x16x64_bf16 v[192:195], v[244:247], v[144:151], v251\n\t" \
  "v_smfmac_f32_16x16x64_bf16 v[196:199], v[244:247], v[152:159], v251\n\t" \
  "v_smfmac_f32_16x16x64_bf16 v[192:195], v[200:203], v[160:167], v251\n\t" \
  "v_smfmac_f32_16x16x64_bf16 v[196:199], v[200:203], v[168:175], v251\n\t" \
  "s_waitcnt lgkmcnt(0)\n\t" \
  "v_smfmac_f32_16x16x64_bf16 v[192:195], v[204:207], v[176:183], v251\n\t" \
  "v_smfmac_f32_16x16x64_bf16 v[196:199], v[204:207], v[184:191], v251\n\t" \
  "s_nop 6\n\t" \
  "v_add_f32 v192, v192, v194\n\t" \
  "v_add_f32 v193, v193, v195\n\t" \
  "v_add_f32 v196, v196, v198\n\t" \
  "v_add_f32 v197, v197, v199\n\t" \
  "v_cvt_pk_bf16_f32 v192, v192, v193\n\t" \
  "v_cvt_pk_bf16_f32 v196, v196, v197\n\t" \
  "ds_write2_b32 v252, v192, v196 offset1:64\n\t" \
  "s_waitcnt lgkmcnt(0)\n\t" \
  "s_barrier\n\t" \
  "buffer_store_dword v192, v235, %[rs], %[sk] offen sc1\n\t" \
  "buffer_store_dword v196, v235, %[rs], %[sk] offen offset:256 sc1\n\t" \
  "s_mov_b32 %[spins], s92\n\t" \
  "s_branch .Lend%=\n\t" \
  ".Lslow%=:\n\t" \
  "s_mov_b64 s[94:95], 0\n\t" \
  "v_cmp_eq_u32 vcc, %[sent], v200\n\t" \
  "s_or_b64 s[94:95], s[94:95], vcc\n\t" \
  "v_cmp_eq_u32 vcc, %[sent], v201\n\t" \
  "s_or_b64 s[94:95], s[94:95], vcc\n\t" \
  "v_cmp_eq_u32 vcc, %[sent], v202\n\t" \
  "s_or_b64 s[94:95], s[94:95], vcc\n\t" \
  "v_cmp_eq_u32 vcc, %[sent], v203\n\t" \
  "s_or_b64 s[94:95], s[94:95], vcc\n\t" \
  "v_cmp_eq_u32 vcc, %[sent], v204\n\t" \
  "s_or_b64 s[94:95], s[94:95], vcc\n\t" \
  "v_cmp_eq_u32 vcc, %[sent], v205\n\t" \
  "s_or_b64 s[94:95], s[94:95], vcc\n\t" \
  "v_cmp_eq_u32 vcc, %[sent], v206\n\t" \
  "s_or_b64 s[94:95], s[94:95], vcc\n\t" \
  "v_cmp_eq_u32 vcc, %[sent], v207\n\t" \
  "s_or_b64 s[94:95], s[94:95], vcc\n\t" \
  "s_cmp_eq_u64 s[94:95], 0\n\t" \
  "s_cbranch_scc1 .Lok%=\n\t" \
  "v_mov_b64 v[192:193], 0\n\t" \
  "v_mov_b64 v[194:195], 0\n\t" \
  "v_mov_b64 v[196:197], 0\n\t" \
  "v_mov_b64 v[198:199], 0\n\t" \
  "s_add_u32 s92, s92, 1\n\t" \
  "s_sleep 6\n\t" \
  "s_cmp_eq_u32 s92, 1\n\t" \
  "s_cbranch_scc1 .Lpoll%=\n\t" \
  "s_sleep 1\n\t" \
  "s_and_b32 s93, s92, 63\n\t" \
  "s_cmp_lg_u32 s93, 0\n\t" \
  "s_cbranch_scc1 .Lpoll%=\n\t" \
  "v_mov_b32 v245, 0\n\t" \
  "buffer_load_dword v244, v245, %[rs], %[sab] offen sc1\n\t" \
  "s_waitcnt vmcnt(0)\n\t" \
  "v_readfirstlane_b32 s93, v244\n\t" \
  "s_nop 3\n\t" \
  "s_cmp_eq_u32 s93, 1\n\t" \
  "s_cbranch_scc1 .Lok%=\n\t" \
  "s_cmp_lt_u32 s92, 0x80000\n\t" \
  "s_cbranch_scc1 .Lpoll%=\n\t" \
  "v_mov_b32 v244, 1\n\t" \
  "buffer_store_dword v244, v245, %[rs], %[sab] offen sc1\n\t" \
  "s_branch .Lok%=\n\t" \
  ".Lend%=:\n\t"
#define QK_ASM_7 \
  "s_waitcnt lgkmcnt(0)\n\t" \
  "v_subrev_u32 v244, 0x8000, v250\n\t" \
  "ds_read_b128 v[216:219], v248 offset:61440\n\t" \
  "ds_read_b128 v[220:223], v248 offset:62464\n\t" \
  "ds_read_b128 v[224:227], v248 offset:63488\n\t" \
  "ds_read_b128 v[228:231], v248 offset:64512\n\t" \
  "ds_read_b128 v[208:211], v244 offset:0\n\t" \
  "ds_read_b128 v[212:215], v244 offset:1024\n\t" \
  "ds_read_b128 v[236:239], v244 offset:2048\n\t" \
  "ds_read_b128 v[240:243], v244 offset:3072\n\t" \
  "v_mov_b64 v[192:193], 0\n\t" \
  "v_mov_b64 v[194:195], 0\n\t" \
  "v_mov_b64 v[196:197], 0\n\t" \
  "v_mov_b64 v[198:199], 0\n\t" \
  "s_movk_i32 s92, 0x2000\n\t" \
  ".Lwt%=:\n\t" \
  "ds_read_b64 v[200:201], v248 offset:30720\n\t" \
  "s_memrealtime s[90:91]\n\t" \
  "s_sub_u32 s92, s92, 1\n\t" \
  "s_waitcnt lgkmcnt(0)\n\t" \
  "v_readfirstlane_b32 s93, v200\n\t" \
  "v_readfirstlane_b32 %[tgt], v201\n\t" \
  "s_cbranch_scc1 .Lgo%=\n\t" \
  "s_cmp_eq_u32 s93, %[step]\n\t" \
  "s_cbranch_scc1 .Lwx%=\n\t" \
  "s_sleep 1\n\t" \
  "s_branch .Lwt%=\n\t" \
  ".Lwx%=:\n\t" \
  "s_lshl_b32 s93, s90, 2\n\t" \
  "s_sub_i32 s93, %[tgt], s93\n\t" \
  "s_cmp_le_i32 s93, 0\n\t" \
  "s_cbranch_scc1 .Lgo%=\n\t" \
  "s_min_i32 s93, s93, 8000\n\t" \
  "s_add_u32 s93, s93, 3\n\t" \
  "s_lshr_b32 s93, s93, 2\n\t" \
  "s_add_u32 s93, s93, s90\n\t" \
  "s_memrealtime s[90:91]\n\t" \
  ".Lpace%=:\n\t" \
  "s_memrealtime s[94:95]\n\t" \
  "s_waitcnt lgkmcnt(1)\n\t" \
  "s_sub_i32 s91, s90, s93\n\t" \
  "s_cmp_ge_i32 s91, 0\n\t" \
  "s_cbranch_scc1 .Lgo%=\n\t" \
  "s_memrealtime s[90:91]\n\t" \
  "s_waitcnt lgkmcnt(1)\n\t" \
  "s_sub_i32 s95, s94, s93\n\t" \
  "s_cmp_lt_i32 s95, 0\n\t" \
  "s_cbranch_scc1 .Lpace%=\n\t" \
  ".Lgo%=:\n\t" \
  "s_mov_b32 s92, 0\n\t" \
  ".Lpoll%=:\n\t" \
  "buffer_load_dwordx4 v[200:203], v248, %[rs], %[som] offen sc1\n\t" \
  "buffer_load_dwordx4 v[204:207], v248, %[rs], %[som2] offen sc1\n\t" \
  "s_waitcnt vmcnt(1) lgkmcnt(0)\n\t" \
  "ds_write_b128 v249, v[200:203]\n\t" \
  "v_smfmac_f32_16x16x64_bf16 v[192:195], v[200:203], v[208:215], v251\n\t" \
  "v_max3_u32 v244, v200, v201, v202\n\t" \
  "v_smfmac_f32_16x16x64_bf16 v[196:199], v[200:203], v[236:243], v251\n\t" \
  "s_waitcnt vmcnt(0)\n\t" \
  "ds_write_b128 v249, v[204:207] offset:8192\n\t" \
  "v_smfmac_f32_16x16x64_bf16 v[192:195], v[204:207], v[216:223], v251\n\t" \
  "v_max3_u32 v244, v244, v203, v204\n\t" \
  "v_max3_u32 v244, v244, v205, v206\n\t" \
  "v_smfmac_f32_16x16x64_bf16 v[196:199], v[204:207], v[224:231], v251\n\t" \
  "v_max_u32 v244, v244, v207\n\t" \
  "v_cmp_le_u32 vcc, %[sent], v244\n\t" \
  "s_cbranch_vccnz .Lslow%=\n\t" \
  ".Lok%=:\n\t" \
  "ds_read_b128 v[216:219], v250 offset:32768\n\t" \
  "ds_read_b128 v[220:223], v250 offset:33792\n\t" \
  "ds_read_b128 v[224:227], v250 offset:34816\n\t" \
  "ds_read_b128 v[228:231], v250 offset:35840\n\t" \
  "s_waitcnt lgkmcnt(4)\n\t" \
  "s_setprio 3\n\t" \
  "s_barrier\n\t" \
  "s_memrealtime s[90:91]\n\t" \
  "v_mov_b32 v232, %[step]\n\t" \
  "v_lshrrev_b32 v234, 1, v248\n\t" \
  "s_mov_b64 exec, 0xffff\n\t" \
  "s_waitcnt lgkmcnt(0)\n\t" \
  "s_lshl_b32 s90, s90, 2\n\t" \
  "s_sub_i32 s93, s90, %[tgt]\n\t" \
  "s_cmp_gt_i32 s93, 270\n\t" \
  "s_cselect_b32 s93, 90, 0\n\t" \
  "s_add_u32 s93, s93, s90\n\t" \
  "s_add_u32 s93, s93, %[lead]\n\t" \
  "v_mov_b32 v233, s93\n\t" \
  "buffer_store_dwordx2 v[232:233], v234, %[rs], %[sbc] offen sc1\n\t" \
  "s_mov_b64 exec, -1\n\t" \
  "s_mov_b32 %[tb1], s90\n\t" \
  "s_setprio 0\n\t" \
  "ds_read_b128 v[208:211], v248 offset:6144\n\t" \
  "ds_read_b128 v[212:215], v248 offset:9216\n\t" \
  "ds_read_b128 v[236:239], v248 offset:10240\n\t" \
  "ds_read_b128 v[240:243], v248 offset:11264\n\t" \
  "s_waitcnt lgkmcnt(3)\n\t" \
  "ds_read_b128 v[244:247], v248 offset:12288\n\t" \
  "v_smfmac_f32_16x16x64_bf16 v[192:195], v[208:211], v[216:223], v251\n\t" \
  "ds_read_b128 v[216:219], v250 offset:0\n\t" \
  "v_smfmac_f32_16x16x64_bf16 v[196:199], v[208:211], v[224:231], v251\n\t" \
  "s_waitcnt lgkmcnt(4)\n\t" \
  "ds_read_b128 v[220:223], v250 offset:1024\n\t" \
  "v_smfmac_f32_16x16x64_bf16 v[192:195], v[212:215], v[0:7], v251\n\t" \
  "ds_read_b128 v[224:227], v250 offset:2048\n\t" \
  "v_smfmac_f32_16x16x64_bf16 v[196:199], v[212:215], v[8:15], v251\n\t" \
  "s_waitcnt lgkmcnt(5)\n\t" \
  "ds_read_b128 v[228:231], v250 offset:3072\n\t" \
  "v_smfmac_f32_16x16x64_bf16 v[192:195], v[236:239], v[16:23], v251\n\t" \
  "ds_read_b128 v[200:203], v248 offset:13312\n\t" \
  "v_smfmac_f32_16x16x64_bf16 v[196:199], v[236:239], v[24:31], v251\n\t" \
  "s_waitcnt lgkmcnt(6)\n\t" \
  "ds_read_b128 v[204:207], v248 offset:14336\n\t" \
  "v_smfmac_f32_16x16x64_bf16 v[192:195], v[240:243], v[32:39], v251\n\t" \
  "ds_read_b128 v[208:211], v248 offset:0\n\t" \
  "v_smfmac_f32_16x16x64_bf16 v[196:199], v[240:243], v[40:47], v251\n\t" \
  "s_waitcnt lgkmcnt(5)\n\t" \
  "ds_read_b128 v[212:215], v248 offset:1024\n\t" \
  "v_smfmac_f32_16x16x64_bf16 v[192:195], v[244:247], v[216:223], v251\n\t" \
  "s_waitcnt lgkmcnt(4)\n\t" \
  "v_smfmac_f32_16x16x64_bf16 v[196:199], v[244:247], v[224:231], v251\n\t" \
  "s_waitcnt lgkmcnt(3)\n\t" \
  "ds_read_b128 v[236:239], v248 offset:2048\n\t" \
  "v_smfmac_f32_16x16x64_bf16 v[192:195], v[200:203], v[48:55], v251\n\t" \
  "ds_read_b128 v[240:243], v248 offset:3072\n\t" \
  "v_smfmac_f32_16x16x64_bf16 v[196:199], v[200:203], v[56:63], v251\n\t" \
  "s_waitcnt lgkmcnt(4)\n\t" \
  "v_smfmac_f32_16x16x64_bf16 v[192:195], v[204:207], v[64:71], v251\n\t" \
  "ds_read_b128 v[244:247], v248 offset:4096\n\t" \
  "v_smfmac_f32_16x16x64_bf16 v[196:199], v[204:207], v[72:79], v251\n\t" \
  "s_waitcnt lgkmcnt(4)\n\t" \
  "v_smfmac_f32_16x16x64_bf16 v[192:195], v[208:211], v[80:87], v251\n\t" \
  "ds_read_b128 v[200:203], v248 offset:5120\n\t" \
  "v_smfmac_f32_16x16x64_bf16 v[196:199], v[208:211], v[88:95], v251\n\t" \
  "s_waitcnt lgkmcnt(4)\n\t" \
  "v_smfmac_f32_16x16x64_bf16 v[192:195], v[212:215], v[96:103], v251\n\t" \
  "ds_read_b128 v[204:207], v248 offset:8192\n\t" \
  "v_smfmac_f32_16x16x64_bf16 v[196:199], v[212:215], v[104:111], v251\n\t" \
  "s_waitcnt lgkmcnt(3)\n\t" \
  "v_smfmac_f32_16x16x64_bf16 v[192:195], v[236:239], v[112:119], v251\n\t" \
  "v_smfmac_f32_16x16x64_bf16 v[196:199], v[236:239], v[120:127], v251\n\t" \
  "v_smfmac_f32_16x16x64_bf16 v[192:195], v[240:243], v[128:135], v251\n\t" \
  "v_smfmac_f32_16x16x64_bf16 v[196:199], v[240:243], v[136:143], v251\n\t" \
  "s_waitcnt lgkmcnt(1)\n\t" \
  "v_smfmac_f32_16x16x64_bf16 v[192:195], v[244:247], v[144:151], v251\n\t" \
  "v_smfmac_f32_16x16x64_bf16 v[196:199], v[244:247], v[152:159], v251\n\t" \
  "v_smfmac_f32_16x16x64_bf16 v[192:195], v[200:203], v[160:167], v251\n\t" \
  "v_smfmac_f32_16x16x64_bf16 v[196:199], v[200:203], v[168:175], v251\n\t" \
  "s_waitcnt lgkmcnt(0)\n\t" \
  "v_smfmac_f32_16x16x64_bf16 v[192:195], v[204:207], v[176:183], v251\n\t" \
  "v_smfmac_f32_16x16x64_bf16 v[196:199], v[204:207], v[184:191], v251\n\t" \
  "s_nop 6\n\t" \
  "v_add_f32 v192, v192, v194\n\t" \
  "v_add_f32 v193, v193, v195\n\t" \
  "v_add_f32 v196, v196, v198\n\t" \
  "v_add_f32 v197, v197, v199\n\t" \
  "v_cvt_pk_bf16_f32 v192, v192, v193\n\t" \
  "v_cvt_pk_bf16_f32 v196, v196, v197\n\t" \
  "ds_write2_b32 v252, v192, v196 offset1:64\n\t" \
  "s_waitcnt lgkmcnt(0)\n\t" \
  "s_barrier\n\t" \
  "buffer_store_dword v192, v235, %[rs], %[sk] offen sc1\n\t" \
  "buffer_store_dword v196, v235, %[rs], %[sk] offen offset:256 sc1\n\t" \
  "s_mov_b32 %[spins], s92\n\t" \
  "s_branch .Lend%=\n\t" \
  ".Lslow%=:\n\t" \
  "s_mov_b64 s[94:95], 0\n\t" \
  "v_cmp_eq_u32 vcc, %[sent], v200\n\t" \
  "s_or_b64 s[94:95], s[94:95], vcc\n\t" \
  "v_cmp_eq_u32 vcc, %[sent], v201\n\t" \
  "s_or_b64 s[94:95], s[94:95], vcc\n\t" \
  "v_cmp_eq_u32 vcc, %[sent], v202\n\t" \
  "s_or_b64 s[94:95], s[94:95], vcc\n\t" \
  "v_cmp_eq_u32 vcc, %[sent], v203\n\t" \
  "s_or_b64 s[94:95], s[94:95], vcc\n\t" \
  "v_cmp_eq_u32 vcc, %[sent], v204\n\t" \
  "s_or_b64 s[94:95], s[94:95], vcc\n\t" \
  "v_cmp_eq_u32 vcc, %[sent], v205\n\t" \
  "s_or_b64 s[94:95], s[94:95], vcc\n\t" \
  "v_cmp_eq_u32 vcc, %[sent], v206\n\t" \
  "s_or_b64 s[94:95], s[94:95], vcc\n\t" \
  "v_cmp_eq_u32 vcc, %[sent], v207\n\t" \
  "s_or_b64 s[94:95], s[94:95], vcc\n\t" \
  "s_cmp_eq_u64 s[94:95], 0\n\t" \
  "s_cbranch_scc1 .Lok%=\n\t" \
  "v_mov_b64 v[192:193], 0\n\t" \
  "v_mov_b64 v[194:195], 0\n\t" \
  "v_mov_b64 v[196:197], 0\n\t" \
  "v_mov_b64 v[198:199], 0\n\t" \
  "s_add_u32 s92, s92, 1\n\t" \
  "s_sleep 6\n\t" \
  "s_cmp_eq_u32 s92, 1\n\t" \
  "s_cbranch_scc1 .Lpoll%=\n\t" \
  "s_sleep 1\n\t" \
  "s_and_b32 s93, s92, 63\n\t" \
  "s_cmp_lg_u32 s93, 0\n\t" \
  "s_cbranch_scc1 .Lpoll%=\n\t" \
  "v_mov_b32 v245, 0\n\t" \
  "buffer_load_dword v244, v245, %[rs], %[sab] offen sc1\n\t" \
  "s_waitcnt vmcnt(0)\n\t" \
  "v_readfirstlane_b32 s93, v244\n\t" \
  "s_nop 3\n\t" \
  "s_cmp_eq_u32 s93, 1\n\t" \
  "s_cbranch_scc1 .Lok%=\n\t" \
  "s_cmp_lt_u32 s92, 0x80000\n\t" \
  "s_cbranch_scc1 .Lpoll%=\n\t" \
  "v_mov_b32 v244, 1\n\t" \
  "buffer_store_dword v244, v245, %[rs], %[sab] offen sc1\n\t" \
  "s_branch .Lok%=\n\t" \
  ".Lend%=:\n\t"
#define VV_ASM \
  "s_waitcnt lgkmcnt(0)\n\t" \
  "v_mov_b64 v[128:129], 0\n\t" \
  "v_mov_b64 v[130:131], 0\n\t" \
  "s_movk_i32 s92, 0x2000\n\t" \
  ".Lwt%=:\n\t" \
  "ds_read_b64 v[188:189], v200 offset:30720\n\t" \
  "s_memrealtime s[90:91]\n\t" \
  "s_sub_u32 s92, s92, 1\n\t" \
  "s_waitcnt lgkmcnt(0)\n\t" \
  "v_readfirstlane_b32 s93, v188\n\t" \
  "v_readfirstlane_b32 %[tgt], v189\n\t" \
  "s_cbranch_scc1 .Lgo%=\n\t" \
  "s_cmp_eq_u32 s93, %[step]\n\t" \
  "s_cbranch_scc1 .Lwx%=\n\t" \
  "s_sleep 1\n\t" \
  "s_branch .Lwt%=\n\t" \
  ".Lwx%=:\n\t" \
  "s_lshl_b32 s93, s90, 2\n\t" \
  "s_sub_i32 s93, %[tgt], s93\n\t" \
  "s_cmp_le_i32 s93, 0\n\t" \
  "s_cbranch_scc1 .Lgo%=\n\t" \
  "s_min_i32 s93, s93, 8000\n\t" \
  "s_add_u32 s93, s93, 3\n\t" \
  "s_lshr_b32 s93, s93, 2\n\t" \
  "s_add_u32 s93, s93, s90\n\t" \
  "s_memrealtime s[90:91]\n\t" \
  ".Lpace%=:\n\t" \
  "s_memrealtime s[94:95]\n\t" \
  "s_waitcnt lgkmcnt(1)\n\t" \
  "s_sub_i32 s91, s90, s93\n\t" \
  "s_cmp_ge_i32 s91, 0\n\t" \
  "s_cbranch_scc1 .Lgo%=\n\t" \
  "s_memrealtime s[90:91]\n\t" \
  "s_waitcnt lgkmcnt(1)\n\t" \
  "s_sub_i32 s95, s94, s93\n\t" \
  "s_cmp_lt_i32 s95, 0\n\t" \
  "s_cbranch_scc1 .Lpace%=\n\t" \
  ".Lgo%=:\n\t" \
  "s_mov_b32 s92, 0\n\t" \
  ".Lpoll%=:\n\t" \
  "buffer_load_dwordx4 v[188:191], v200, %[rs], %[som] offen sc1\n\t" \
  "buffer_load_dwordx4 v[192:195], v200, %[rs], %[som] offen offset:1024 sc1\n\t" \
  "s_waitcnt vmcnt(1) lgkmcnt(0)\n\t" \
  "ds_write_b128 v201, v[188:191]\n\t" \
  "v_max3_u32 v196, v188, v189, v190\n\t" \
  "s_waitcnt vmcnt(0)\n\t" \
  "ds_write_b128 v201, v[192:195] offset:1024\n\t" \
  "v_max3_u32 v196, v196, v191, v192\n\t" \
  "v_max3_u32 v196, v196, v193, v194\n\t" \
  "v_max_u32 v196, v196, v195\n\t" \
  "v_cmp_le_u32 vcc, %[sent], v196\n\t" \
  "s_cbranch_vccnz .Lslow%=\n\t" \
  ".Lok%=:\n\t" \
  "s_waitcnt lgkmcnt(0)\n\t" \
  "s_barrier\n\t" \
  "ds_read_b128 v[132:135], v200 offset:0\n\t" \
  "ds_read_b128 v[136:139], v200 offset:1024\n\t" \
  "ds_read_b128 v[140:143], v200 offset:2048\n\t" \
  "ds_read_b128 v[144:147], v200 offset:3072\n\t" \
  "ds_read_b128 v[148:151], v200 offset:4096\n\t" \
  "s_waitcnt lgkmcnt(4)\n\t" \
  "ds_read_b128 v[152:155], v200 offset:5120\n\t" \
  "v_smfmac_f32_16x16x64_bf16 v[128:131], v[132:135], v[0:7], v202\n\t" \
  "s_waitcnt lgkmcnt(4)\n\t" \
  "ds_read_b128 v[156:159], v200 offset:6144\n\t" \
  "v_smfmac_f32_16x16x64_bf16 v[128:131], v[136:139], v[8:15], v202\n\t" \
  "s_waitcnt lgkmcnt(4)\n\t" \
  "ds_read_b128 v[160:163], v200 offset:7168\n\t" \
  "v_smfmac_f32_16x16x64_bf16 v[128:131], v[140:143], v[16:23], v202\n\t" \
  "s_waitcnt lgkmcnt(4)\n\t" \
  "ds_read_b128 v[164:167], v200 offset:8192\n\t" \
  "v_smfmac_f32_16x16x64_bf16 v[128:131], v[144:147], v[24:31], v202\n\t" \
  "s_waitcnt lgkmcnt(4)\n\t" \
  "ds_read_b128 v[168:171], v200 offset:9216\n\t" \
  "v_smfmac_f32_16x16x64_bf16 v[128:131], v[148:151], v[32:39], v202\n\t" \
  "s_waitcnt lgkmcnt(4)\n\t" \
  "ds_read_b128 v[172:175], v200 offset:10240\n\t" \
  "v_smfmac_f32_16x16x64_bf16 v[128:131], v[152:155], v[40:47], v202\n\t" \
  "s_waitcnt lgkmcnt(4)\n\t" \
  "ds_read_b128 v[176:179], v200 offset:11264\n\t" \
  "v_smfmac_f32_16x16x64_bf16 v[128:131], v[156:159], v[48:55], v202\n\t" \
  "s_waitcnt lgkmcnt(4)\n\t" \
  "ds_read_b128 v[180:183], v200 offset:12288\n\t" \
  "v_smfmac_f32_16x16x64_bf16 v[128:131], v[160:163], v[56:63], v202\n\t" \
  "s_waitcnt lgkmcnt(4)\n\t" \
  "ds_read_b128 v[184:187], v200 offset:13312\n\t" \
  "v_smfmac_f32_16x16x64_bf16 v[128:131], v[164:167], v[64:71], v202\n\t" \
  "s_waitcnt lgkmcnt(4)\n\t" \
  "ds_read_b128 v[188:191], v200 offset:14336\n\t" \
  "v_smfmac_f32_16x16x64_bf16 v[128:131], v[168:171], v[72:79], v202\n\t" \
  "s_waitcnt lgkmcnt(4)\n\t" \
  "ds_read_b128 v[192:195], v200 offset:15360\n\t" \
  "v_smfmac_f32_16x16x64_bf16 v[128:131], v[172:175], v[80:87], v202\n\t" \
  "s_waitcnt lgkmcnt(4)\n\t" \
  "v_smfmac_f32_16x16x64_bf16 v[128:131], v[176:179], v[88:95], v202\n\t" \
  "s_waitcnt lgkmcnt(3)\n\t" \
  "v_smfmac_f32_16x16x64_bf16 v[128:131], v[180:183], v[96:103], v202\n\t" \
  "s_waitcnt lgkmcnt(2)\n\t" \
  "v_smfmac_f32_16x16x64_bf16 v[128:131], v[184:187], v[104:111], v202\n\t" \
  "s_waitcnt lgkmcnt(1)\n\t" \
  "v_smfmac_f32_16x16x64_bf16 v[128:131], v[188:191], v[112:119], v202\n\t" \
  "s_waitcnt lgkmcnt(0)\n\t" \
  "v_smfmac_f32_16x16x64_bf16 v[128:131], v[192:195], v[120:127], v202\n\t" \
  "s_nop 7\n\t" \
  "v_add_f32 v128, v128, v130\n\t" \
  "v_add_f32 v129, v129, v131\n\t" \
  "v_cvt_pk_bf16_f32 v128, v128, v129\n\t" \
  "buffer_store_dword v128, v203, %[rs], %[sv] offen sc1\n\t" \
  "ds_write_b32 v204, v128\n\t" \
  "s_waitcnt lgkmcnt(0)\n\t" \
  "s_barrier\n\t" \
  "s_mov_b32 %[spins], s92\n\t" \
  "s_branch .Lend%=\n\t" \
  ".Lslow%=:\n\t" \
  "s_mov_b64 s[94:95], 0\n\t" \
  "v_cmp_eq_u32 vcc, %[sent], v188\n\t" \
  "s_or_b64 s[94:95], s[94:95], vcc\n\t" \
  "v_cmp_eq_u32 vcc, %[sent], v189\n\t" \
  "s_or_b64 s[94:95], s[94:95], vcc\n\t" \
  "v_cmp_eq_u32 vcc, %[sent], v190\n\t" \
  "s_or_b64 s[94:95], s[94:95], vcc\n\t" \
  "v_cmp_eq_u32 vcc, %[sent], v191\n\t" \
  "s_or_b64 s[94:95], s[94:95], vcc\n\t" \
  "v_cmp_eq_u32 vcc, %[sent], v192\n\t" \
  "s_or_b64 s[94:95], s[94:95], vcc\n\t" \
  "v_cmp_eq_u32 vcc, %[sent], v193\n\t" \
  "s_or_b64 s[94:95], s[94:95], vcc\n\t" \
  "v_cmp_eq_u32 vcc, %[sent], v194\n\t" \
  "s_or_b64 s[94:95], s[94:95], vcc\n\t" \
  "v_cmp_eq_u32 vcc, %[sent], v195\n\t" \
  "s_or_b64 s[94:95], s[94:95], vcc\n\t" \
  "s_cmp_eq_u64 s[94:95], 0\n\t" \
  "s_cbranch_scc1 .Lok%=\n\t" \
  "s_add_u32 s92, s92, 1\n\t" \
  "s_sleep 6\n\t" \
  "s_cmp_eq_u32 s92, 1\n\t" \
  "s_cbranch_scc1 .Lpoll%=\n\t" \
  "s_sleep 1\n\t" \
  "s_and_b32 s93, s92, 63\n\t" \
  "s_cmp_lg_u32 s93, 0\n\t" \
  "s_cbranch_scc1 .Lpoll%=\n\t" \
  "v_mov_b32 v197, 0\n\t" \
  "buffer_load_dword v196, v197, %[rs], %[sab] offen sc1\n\t" \
  "s_waitcnt vmcnt(0)\n\t" \
  "v_readfirstlane_b32 s93, v196\n\t" \
  "s_nop 3\n\t" \
  "s_cmp_eq_u32 s93, 1\n\t" \
  "s_cbranch_scc1 .Lok%=\n\t" \
  "s_cmp_lt_u32 s92, 0x80000\n\t" \
  "s_cbranch_scc1 .Lpoll%=\n\t" \
  "v_mov_b32 v196, 1\n\t" \
  "buffer_store_dword v196, v197, %[rs], %[sab] offen sc1\n\t" \
  "s_branch .Lok%=\n\t" \
  ".Lend%=:\n\t"
#define TK_ASM \
  "buffer_load_dwordx4 v[224:227], v200, %[rs], %[sk] offen offset:0 sc1\n\t" \
  "buffer_load_dwordx4 v[228:231], v200, %[rs], %[sk] offen offset:1024 sc1\n\t" \
  "s_memrealtime s[88:89]\n\t" \
  "v_add_u32 v205, %[tl], v200\n\t" \
  "v_add_u32 v206, %[tq], v205\n\t" \
  "v_add_u32 v207, %[tv], v205\n\t" \
  "s_waitcnt lgkmcnt(0)\n\t" \
  "ds_read_b128 v[128:131], v206 offset:8192\n\t" \
  "ds_read_b128 v[144:147], v205 offset:0\n\t" \
  "ds_read_b128 v[160:163], v205 offset:4096\n\t" \
  "ds_read_b128 v[132:135], v206 offset:9216\n\t" \
  "ds_read_b128 v[148:151], v205 offset:1024\n\t" \
  "ds_read_b128 v[164:167], v205 offset:5120\n\t" \
  "ds_read_b128 v[136:139], v206 offset:10240\n\t" \
  "ds_read_b128 v[152:155], v205 offset:2048\n\t" \
  "ds_read_b128 v[168:171], v205 offset:6144\n\t" \
  "ds_read_b128 v[140:143], v206 offset:11264\n\t" \
  "ds_read_b128 v[156:159], v205 offset:3072\n\t" \
  "ds_read_b128 v[172:175], v205 offset:7168\n\t" \
  "v_add_u32 v206, %[vp], v218\n\t" \
  "s_lshl_b32 s88, s88, 2\n\t" \
  "s_add_u32 s89, s88, %[od]\n\t" \
  "s_waitcnt lgkmcnt(10)\n\t" \
  "v_mfma_f32_16x16x32_bf16 v[196:199], v[144:147], v[128:131], 0\n\t" \
  "s_waitcnt lgkmcnt(9)\n\t" \
  "v_mfma_f32_16x16x32_bf16 v[144:147], v[160:163], v[128:131], 0\n\t" \
  "s_waitcnt lgkmcnt(7)\n\t" \
  "v_mfma_f32_16x16x32_bf16 v[196:199], v[148:151], v[132:135], v[196:199]\n\t" \
  "s_waitcnt lgkmcnt(6)\n\t" \
  "v_mfma_f32_16x16x32_bf16 v[144:147], v[164:167], v[132:135], v[144:147]\n\t" \
  "s_waitcnt lgkmcnt(4)\n\t" \
  "v_mfma_f32_16x16x32_bf16 v[196:199], v[152:155], v[136:139], v[196:199]\n\t" \
  "s_waitcnt lgkmcnt(3)\n\t" \
  "v_mfma_f32_16x16x32_bf16 v[144:147], v[168:171], v[136:139], v[144:147]\n\t" \
  "s_waitcnt lgkmcnt(1)\n\t" \
  "v_mfma_f32_16x16x32_bf16 v[196:199], v[156:159], v[140:143], v[196:199]\n\t" \
  "s_waitcnt lgkmcnt(0)\n\t" \
  "v_mfma_f32_16x16x32_bf16 v[144:147], v[172:175], v[140:143], v[144:147]\n\t" \
  "ds_read_b128 v[176:179], v207 offset:16384\n\t" \
  "ds_read_b128 v[180:183], v207 offset:17408\n\t" \
  "ds_read_b128 v[184:187], v207 offset:18432\n\t" \
  "ds_read_b128 v[188:191], v207 offset:19456\n\t" \
  "ds_read2st64_b32 v[192:193], v206 offset0:0 offset1:1\n\t" \
  "ds_read2st64_b32 v[194:195], v206 offset0:2 offset1:3\n\t" \
  "v_min_f32 v196, v196, v208\n\t" \
  "v_min_f32 v197, v197, v209\n\t" \
  "v_min_f32 v198, v198, v210\n\t" \
  "v_min_f32 v199, v199, v211\n\t" \
  "v_min_f32 v144, v144, v212\n\t" \
  "v_min_f32 v145, v145, v213\n\t" \
  "v_min_f32 v146, v146, v214\n\t" \
  "v_min_f32 v147, v147, v215\n\t" \
  "v_max3_f32 v148, v196, v197, v198\n\t" \
  "v_max3_f32 v149, v199, v144, v145\n\t" \
  "v_max3_f32 v148, v148, v146, v147\n\t" \
  "v_max_f32 v150, v148, v149\n\t" \
  "v_max_f32 v151, v148, v149\n\t" \
  "s_nop 1\n\t" \
  "v_permlane32_swap_b32 v150, v151\n\t" \
  "v_max_f32 v150, v150, v151\n\t" \
  "s_mov_b32 s92, 0\n\t" \
  ".Ltake%=:\n\t" \
  "s_waitcnt vmcnt(1) lgkmcnt(0)\n\t" \
  "ds_write_b128 v216, v[224:227]\n\t" \
  "v_max3_u32 v148, v224, v225, v226\n\t" \
  "s_waitcnt vmcnt(0)\n\t" \
  "ds_write_b128 v216, v[228:231] offset:1024\n\t" \
  "ds_read_b64_tr_b16 v[160:161], v217 offset:0\n\t" \
  "ds_read_b64_tr_b16 v[162:163], v217 offset:64\n\t" \
  "ds_read_b64_tr_b16 v[164:165], v217 offset:512\n\t" \
  "ds_read_b64_tr_b16 v[166:167], v217 offset:576\n\t" \
  "ds_read_b64_tr_b16 v[168:169], v217 offset:1024\n\t" \
  "ds_read_b64_tr_b16 v[170:171], v217 offset:1088\n\t" \
  "ds_read_b64_tr_b16 v[172:173], v217 offset:1536\n\t" \
  "ds_read_b64_tr_b16 v[174:175], v217 offset:1600\n\t" \
  "v_max3_u32 v148, v148, v227, v228\n\t" \
  "v_max3_u32 v148, v148, v229, v230\n\t" \
  "v_max_u32 v148, v148, v231\n\t" \
  "v_cmp_le_u32 vcc, %[sent], v148\n\t" \
  "s_cbranch_vccnz .Lslow%=\n\t" \
  ".Lok%=:\n\t" \
  "s_waitcnt lgkmcnt(6)\n\t" \
  "v_mfma_f32_16x16x32_bf16 v[152:155], v[160:163], v[128:131], 0\n\t" \
  "s_waitcnt lgkmcnt(4)\n\t" \
  "v_mfma_f32_16x16x32_bf16 v[152:155], v[164:167], v[132:135], v[152:155]\n\t" \
  "s_waitcnt lgkmcnt(2)\n\t" \
  "v_mfma_f32_16x16x32_bf16 v[152:155], v[168:171], v[136:139], v[152:155]\n\t" \
  "s_waitcnt lgkmcnt(0)\n\t" \
  "v_mfma_f32_16x16x32_bf16 v[152:155], v[172:175], v[140:143], v[152:155]\n\t" \
  "v_mov_b32 v157, 0\n\t" \
  "v_mov_b32 v158, 0\n\t" \
  "s_nop 5\n\t" \
  "v_max3_f32 v148, v152, v153, v150\n\t" \
  "v_max3_f32 v149, v148, v154, v155\n\t" \
  "v_max3_f32 v150, v148, v154, v155\n\t" \
  "v_cndmask_b32 v152, v152, v154, %[up]\n\t" \
  "v_cndmask_b32 v153, v153, v155, %[up]\n\t" \
  "v_permlane16_swap_b32 v149, v150\n\t" \
  "v_max_f32 v148, v149, v150\n\t" \
  "v_mul_f32 v148, %[negc], v148\n\t" \
  "v_fma_f32 v152, v152, %[c], v148\n\t" \
  "v_fma_f32 v153, v153, %[c], v148\n\t" \
  "v_exp_f32 v152, v152\n\t" \
  "v_fma_f32 v196, v196, %[c], v148\n\t" \
  "v_exp_f32 v153, v153\n\t" \
  "v_fma_f32 v197, v197, %[c], v148\n\t" \
  "v_exp_f32 v196, v196\n\t" \
  "v_fma_f32 v198, v198, %[c], v148\n\t" \
  "v_cvt_pk_bf16_f32 v156, v152, v153\n\t" \
  "v_cvt_pk_bf16_f32 v159, v152, v153\n\t" \
  "v_exp_f32 v197, v197\n\t" \
  "v_fma_f32 v199, v199, %[c], v148\n\t" \
  "v_mfma_f32_16x16x16_bf16 v[132:135], v[220:221], v[156:157], 0\n\t" \
  "v_exp_f32 v198, v198\n\t" \
  "v_fma_f32 v144, v144, %[c], v148\n\t" \
  "v_mfma_f32_16x16x16_bf16 v[160:163], v[192:193], v[156:157], 0\n\t" \
  "v_cvt_pk_bf16_f32 v128, v196, v197\n\t" \
  "v_exp_f32 v199, v199\n\t" \
  "v_fma_f32 v145, v145, %[c], v148\n\t" \
  "v_mfma_f32_16x16x16_bf16 v[164:167], v[192:193], v[158:159], 0\n\t" \
  "v_exp_f32 v144, v144\n\t" \
  "v_fma_f32 v146, v146, %[c], v148\n\t" \
  "v_mfma_f32_16x16x16_bf16 v[168:171], v[194:195], v[156:157], 0\n\t" \
  "v_cvt_pk_bf16_f32 v129, v198, v199\n\t" \
  "v_exp_f32 v145, v145\n\t" \
  "v_fma_f32 v147, v147, %[c], v148\n\t" \
  "v_mfma_f32_16x16x16_bf16 v[172:175], v[194:195], v[158:159], 0\n\t" \
  "v_exp_f32 v146, v146\n\t" \
  "v_cvt_pk_bf16_f32 v130, v144, v145\n\t" \
  "v_exp_f32 v147, v147\n\t" \
  "s_nop 0\n\t" \
  "v_cvt_pk_bf16_f32 v131, v146, v147\n\t" \
  "s_nop 1\n\t" \
  "v_mfma_f32_16x16x32_bf16 v[132:135], v[220:223], v[128:131], v[132:135]\n\t" \
  "v_mfma_f32_16x16x32_bf16 v[160:163], v[176:179], v[128:131], v[160:163]\n\t" \
  "v_mfma_f32_16x16x32_bf16 v[164:167], v[180:183], v[128:131], v[164:167]\n\t" \
  "v_mfma_f32_16x16x32_bf16 v[168:171], v[184:187], v[128:131], v[168:171]\n\t" \
  "v_mfma_f32_16x16x32_bf16 v[172:175], v[188:191], v[128:131], v[172:175]\n\t" \
  "s_nop 3\n\t" \
  "v_rcp_f32 v148, v132\n\t" \
  "s_nop 0\n\t" \
  "v_pk_mul_f32 v[160:161], v[160:161], v[148:149] op_sel_hi:[1,0]\n\t" \
  "v_pk_mul_f32 v[162:163], v[162:163], v[148:149] op_sel_hi:[1,0]\n\t" \
  "v_cvt_pk_bf16_f32 v136, v160, v161\n\t" \
  "v_cvt_pk_bf16_f32 v137, v162, v163\n\t" \
  "v_pk_mul_f32 v[164:165], v[164:165], v[148:149] op_sel_hi:[1,0]\n\t" \
  "v_pk_mul_f32 v[166:167], v[166:167], v[148:149] op_sel_hi:[1,0]\n\t" \
  "v_cvt_pk_bf16_f32 v138, v164, v165\n\t" \
  "v_cvt_pk_bf16_f32 v139, v166, v167\n\t" \
  "v_pk_mul_f32 v[168:169], v[168:169], v[148:149] op_sel_hi:[1,0]\n\t" \
  "v_pk_mul_f32 v[170:171], v[170:171], v[148:149] op_sel_hi:[1,0]\n\t" \
  "v_cvt_pk_bf16_f32 v140, v168, v169\n\t" \
  "v_cvt_pk_bf16_f32 v141, v170, v171\n\t" \
  "v_pk_mul_f32 v[172:173], v[172:173], v[148:149] op_sel_hi:[1,0]\n\t" \
  "v_pk_mul_f32 v[174:175], v[174:175], v[148:149] op_sel_hi:[1,0]\n\t" \
  "v_cvt_pk_bf16_f32 v142, v172, v173\n\t" \
  "v_cvt_pk_bf16_f32 v143, v174, v175\n\t" \
  "v_permlane32_swap_b32 v136, v138\n\t" \
  "v_permlane32_swap_b32 v137, v139\n\t" \
  "v_permlane32_swap_b32 v140, v142\n\t" \
  "v_permlane32_swap_b32 v141, v143\n\t" \
  "v_permlane16_swap_b32 v136, v138\n\t" \
  "v_permlane16_swap_b32 v137, v139\n\t" \
  "v_permlane16_swap_b32 v140, v142\n\t" \
  "v_permlane16_swap_b32 v141, v143\n\t" \
  "s_memrealtime s[90:91]\n\t" \
  "s_waitcnt lgkmcnt(0)\n\t" \
  "s_lshl_b32 s93, s90, 2\n\t" \
  "s_sub_i32 s93, s89, s93\n\t" \
  "s_cmp_le_i32 s93, 0\n\t" \
  "s_cbranch_scc1 .Lgoo%=\n\t" \
  "s_min_i32 s93, s93, 8000\n\t" \
  "s_add_u32 s93, s93, 3\n\t" \
  "s_lshr_b32 s93, s93, 2\n\t" \
  "s_add_u32 s93, s93, s90\n\t" \
  "s_memrealtime s[90:91]\n\t" \
  ".Lpaceo%=:\n\t" \
  "s_memrealtime s[94:95]\n\t" \
  "s_waitcnt lgkmcnt(1)\n\t" \
  "s_sub_i32 s91, s90, s93\n\t" \
  "s_cmp_ge_i32 s91, 0\n\t" \
  "s_cbranch_scc1 .Lgoo%=\n\t" \
  "s_memrealtime s[90:91]\n\t" \
  "s_waitcnt lgkmcnt(1)\n\t" \
  "s_sub_i32 s95, s94, s93\n\t" \
  "s_cmp_lt_i32 s95, 0\n\t" \
  "s_cbranch_scc1 .Lpaceo%=\n\t" \
  ".Lgoo%=:\n\t" \
  "buffer_store_dwordx4 v[136:139], v200, %[rt], %[so] offen offset:0 sc1\n\t" \
  "buffer_store_dwordx4 v[140:143], v200, %[rt], %[so] offen offset:1024 sc1\n\t" \
  "s_waitcnt lgkmcnt(0)\n\t" \
  "s_branch .Lend%=\n\t" \
  ".Lpoll%=:\n\t" \
  "buffer_load_dwordx4 v[224:227], v200, %[rs], %[sk] offen offset:0 sc1\n\t" \
  "buffer_load_dwordx4 v[228:231], v200, %[rs], %[sk] offen offset:1024 sc1\n\t" \
  "s_branch .Ltake%=\n\t" \
  "s_branch .Lend%=\n\t" \
  ".Lslow%=:\n\t" \
  "s_mov_b64 s[94:95], 0\n\t" \
  "v_cmp_eq_u32 vcc, %[sent], v224\n\t" \
  "s_or_b64 s[94:95], s[94:95], vcc\n\t" \
  "v_cmp_eq_u32 vcc, %[sent], v225\n\t" \
  "s_or_b64 s[94:95], s[94:95], vcc\n\t" \
  "v_cmp_eq_u32 vcc, %[sent], v226\n\t" \
  "s_or_b64 s[94:95], s[94:95], vcc\n\t" \
  "v_cmp_eq_u32 vcc, %[sent], v227\n\t" \
  "s_or_b64 s[94:95], s[94:95], vcc\n\t" \
  "v_cmp_eq_u32 vcc, %[sent], v228\n\t" \
  "s_or_b64 s[94:95], s[94:95], vcc\n\t" \
  "v_cmp_eq_u32 vcc, %[sent], v229\n\t" \
  "s_or_b64 s[94:95], s[94:95], vcc\n\t" \
  "v_cmp_eq_u32 vcc, %[sent], v230\n\t" \
  "s_or_b64 s[94:95], s[94:95], vcc\n\t" \
  "v_cmp_eq_u32 vcc, %[sent], v231\n\t" \
  "s_or_b64 s[94:95], s[94:95], vcc\n\t" \
  "s_cmp_eq_u64 s[94:95], 0\n\t" \
  "s_cbranch_scc1 .Lok%=\n\t" \
  "s_add_u32 s92, s92, 1\n\t" \
  "s_sleep 6\n\t" \
  "s_cmp_eq_u32 s92, 1\n\t" \
  "s_cbranch_scc1 .Lpoll%=\n\t" \
  "s_sleep 1\n\t" \
  "s_and_b32 s93, s92, 63\n\t" \
  "s_cmp_lg_u32 s93, 0\n\t" \
  "s_cbranch_scc1 .Lpoll%=\n\t" \
  "v_mov_b32 v149, 0\n\t" \
  "buffer_load_dword v148, v149, %[rs], %[sab] offen sc1\n\t" \
  "s_waitcnt vmcnt(0)\n\t" \
  "v_readfirstlane_b32 s93, v148\n\t" \
  "s_nop 3\n\t" \
  "s_cmp_eq_u32 s93, 1\n\t" \
  "s_cbranch_scc1 .Lok%=\n\t" \
  "s_cmp_lt_u32 s92, 0x80000\n\t" \
  "s_cbranch_scc1 .Lpoll%=\n\t" \
  "v_mov_b32 v148, 1\n\t" \
  "buffer_store_dword v148, v149, %[rs], %[sab] offen sc1\n\t" \
  "s_branch .Lok%=\n\t" \
  ".Lend%=:\n\t"
constexpr int TLAG = 2;
#define RED_ASM \
  "s_mov_b32 %[step], 0\n\t" \
  "s_lshl_b32 %[sh], %[step], 15\n\t" \
  "s_lshr_b32 s93, %[step], 4\n\t" \
  "s_lshr_b32 s93, %[pm], s93\n\t" \
  "s_and_b32 s93, s93, 4\n\t" \
  "s_lshl_b32 s93, s93, 11\n\t" \
  "s_lshl2_add_u32 %[so], %[sh], s93\n\t" \
  "s_sub_i32 s93, %[step], 2\n\t" \
  "s_lshl_b32 %[st], s93, 19\n\t" \
  "s_lshl_b32 s94, s93, 11\n\t" \
  "s_xor_b32 s94, s94, %[s0]\n\t" \
  "s_and_b32 s94, s94, 0x2000\n\t" \
  "s_or_b32 %[st], %[st], s94\n\t" \
  "s_cmp_lt_i32 s93, 0\n\t" \
  "s_cselect_b32 %[st], 0x70000000, %[st]\n\t" \
  "s_add_u32 %[st4], %[st], 4096\n\t" \
  "buffer_load_dwordx2 v[166:167], v173, %[rs], 0 offen sc1\n\t" \
  "v_mov_b64 v[144:145], 0\n\t" \
  "v_mov_b64 v[146:147], 0\n\t" \
  "v_mov_b64 v[148:149], 0\n\t" \
  "v_mov_b64 v[150:151], 0\n\t" \
  "v_mov_b64 v[152:153], 0\n\t" \
  "v_mov_b64 v[154:155], 0\n\t" \
  "v_mov_b64 v[156:157], 0\n\t" \
  "v_mov_b64 v[158:159], 0\n\t" \
  ".Lstep%=:\n\t" \
  "s_setprio 3\n\t" \
  "s_mov_b32 s92, 0\n\t" \
  ".Lcal%=:\n\t" \
  "s_waitcnt vmcnt(0)\n\t" \
  "s_memrealtime s[76:77]\n\t" \
  "v_cmp_ne_u32 vcc, %[step], v166\n\t" \
  "v_readlane_b32 s90, v167, 0\n\t" \
  "v_readlane_b32 s91, v167, 1\n\t" \
  "v_readlane_b32 s94, v167, 2\n\t" \
  "v_readlane_b32 s95, v167, 3\n\t" \
  "s_sub_i32 s90, s90, %[base]\n\t" \
  "s_sub_i32 s91, s91, %[base]\n\t" \
  "s_sub_i32 s94, s94, %[base]\n\t" \
  "s_sub_i32 s95, s95, %[base]\n\t" \
  "s_max_i32 s90, s90, s91\n\t" \
  "s_max_i32 s94, s94, s95\n\t" \
  "s_max_i32 s90, s90, s94\n\t" \
  "s_add_i32 s90, s90, %[ds]\n\t" \
  "s_waitcnt lgkmcnt(0)\n\t" \
  "s_lshl2_add_u32 s76, s76, %[mn]\n\t" \
  "s_sub_i32 s76, s76, %[base]\n\t" \
  "s_max_i32 s94, s90, s76\n\t" \
  "s_add_u32 %[E], s90, %[base]\n\t" \
  "s_and_b32 s93, vcc_lo, 0xf\n\t" \
  "s_cbranch_scc1 .Lbl%=\n\t" \
  ".Lgo%=:\n\t" \
  "v_mov_b32 v166, 7\n\t" \
  "v_mov_b32 v162, %[step]\n\t" \
  "s_mov_b64 exec, 1\n\t" \
  "v_mov_b32 v164, s94\n\t" \
  "ds_max_u32 v168, v164 offset:4100\n\t" \
  "ds_inc_rtn_u32 v161, v168, v166 offset:4096\n\t" \
  "ds_read_b32 v163, v168 offset:4100\n\t" \
  "s_waitcnt lgkmcnt(0)\n\t" \
  "v_readfirstlane_b32 s94, v161\n\t" \
  "v_readfirstlane_b32 s95, v163\n\t" \
  "s_mov_b64 exec, -1\n\t" \
  "s_cmp_lg_u32 s94, 7\n\t" \
  "s_cbranch_scc1 .Lna%=\n\t" \
  "s_add_u32 s95, s95, %[rl]\n\t" \
  "s_add_u32 s95, s95, %[base]\n\t" \
  "s_mov_b64 exec, 0xffff\n\t" \
  "v_mov_b32 v163, s95\n\t" \
  "buffer_store_dwordx2 v[162:163], v175, %[rs], 0 offen sc1\n\t" \
  "s_mov_b64 exec, -1\n\t" \
  ".Lna%=:\n\t" \
  "s_memrealtime s[90:91]\n\t" \
  "s_waitcnt lgkmcnt(0)\n\t" \
  "s_lshl_b32 s93, s90, 2\n\t" \
  "s_sub_i32 s93, %[E], s93\n\t" \
  "s_cmp_le_i32 s93, 0\n\t" \
  "s_cbranch_scc1 .Lgop%=\n\t" \
  "s_min_i32 s93, s93, 8000\n\t" \
  "s_add_u32 s93, s93, 3\n\t" \
  "s_lshr_b32 s93, s93, 2\n\t" \
  "s_add_u32 s93, s93, s90\n\t" \
  "s_memrealtime s[90:91]\n\t" \
  ".Lpacep%=:\n\t" \
  "s_memrealtime s[94:95]\n\t" \
  "s_waitcnt lgkmcnt(1)\n\t" \
  "s_sub_i32 s91, s90, s93\n\t" \
  "s_cmp_ge_i32 s91, 0\n\t" \
  "s_cbranch_scc1 .Lgop%=\n\t" \
  "s_memrealtime s[90:91]\n\t" \
  "s_waitcnt lgkmcnt(1)\n\t" \
  "s_sub_i32 s95, s94, s93\n\t" \
  "s_cmp_lt_i32 s95, 0\n\t" \
  "s_cbranch_scc1 .Lpacep%=\n\t" \
  ".Lgop%=:\n\t" \
  "buffer_load_dwordx4 v[128:131], v234, %[rs], %[so] offen sc1\n\t" \
  "s_nop 15\n\t" \
  "buffer_load_dwordx4 v[132:135], v235, %[rs], %[so] offen sc1\n\t" \
  "s_nop 15\n\t" \
  "buffer_load_dwordx4 v[136:139], v236, %[rs], %[so] offen sc1\n\t" \
  "s_nop 15\n\t" \
  "buffer_load_dwordx4 v[140:143], v237, %[rs], %[so] offen sc1\n\t" \
  "s_waitcnt lgkmcnt(0)\n\t" \
  ".Lw%=:\n\t" \
  "s_waitcnt vmcnt(3)\n\t" \
  "v_smfmac_f32_16x16x64_bf16 v[144:147], v[128:131], v[0:7], v172\n\t" \
  "v_max3_u32 v164, v128, v129, 0\n\t" \
  "v_smfmac_f32_16x16x64_bf16 v[148:151], v[128:131], v[8:15], v172\n\t" \
  "v_max3_u32 v164, v130, v131, v164\n\t" \
  "v_smfmac_f32_16x16x64_bf16 v[152:155], v[128:131], v[16:23], v172\n\t" \
  "v_smfmac_f32_16x16x64_bf16 v[156:159], v[128:131], v[24:31], v172\n\t" \
  "buffer_load_dwordx4 v[176:179], v238, %[rt], %[st] offen offset:0 sc1\n\t" \
  "s_waitcnt vmcnt(3)\n\t" \
  "v_smfmac_f32_16x16x64_bf16 v[144:147], v[132:135], v[32:39], v172\n\t" \
  "v_max3_u32 v164, v132, v133, v164\n\t" \
  "v_smfmac_f32_16x16x64_bf16 v[148:151], v[132:135], v[40:47], v172\n\t" \
  "v_max3_u32 v164, v134, v135, v164\n\t" \
  "v_smfmac_f32_16x16x64_bf16 v[152:155], v[132:135], v[48:55], v172\n\t" \
  "v_smfmac_f32_16x16x64_bf16 v[156:159], v[132:135], v[56:63], v172\n\t" \
  "buffer_load_dwordx4 v[180:183], v238, %[rt], %[st] offen offset:1024 sc1\n\t" \
  "s_waitcnt vmcnt(3)\n\t" \
  "v_smfmac_f32_16x16x64_bf16 v[144:147], v[136:139], v[64:71], v172\n\t" \
  "v_max3_u32 v164, v136, v137, v164\n\t" \
  "v_smfmac_f32_16x16x64_bf16 v[148:151], v[136:139], v[72:79], v172\n\t" \
  "v_max3_u32 v164, v138, v139, v164\n\t" \
  "v_smfmac_f32_16x16x64_bf16 v[152:155], v[136:139], v[80:87], v172\n\t" \
  "v_smfmac_f32_16x16x64_bf16 v[156:159], v[136:139], v[88:95], v172\n\t" \
  "buffer_load_dwordx4 v[184:187], v238, %[rt], %[st4] offen offset:0 sc1\n\t" \
  "s_waitcnt vmcnt(3)\n\t" \
  "v_smfmac_f32_16x16x64_bf16 v[144:147], v[140:143], v[96:103], v172\n\t" \
  "v_max3_u32 v164, v140, v141, v164\n\t" \
  "v_smfmac_f32_16x16x64_bf16 v[148:151], v[140:143], v[104:111], v172\n\t" \
  "v_max3_u32 v164, v142, v143, v164\n\t" \
  "v_smfmac_f32_16x16x64_bf16 v[152:155], v[140:143], v[112:119], v172\n\t" \
  "v_cmp_le_u32 vcc, %[sent], v164\n\t" \
  "v_smfmac_f32_16x16x64_bf16 v[156:159], v[140:143], v[120:127], v172\n\t" \
  "buffer_load_dwordx4 v[188:191], v238, %[rt], %[st4] offen offset:1024 sc1\n\t" \
  "s_cbranch_vccnz .Lslow%=\n\t" \
  ".Lok%=:\n\t" \
  "v_add_f32_dpp v160, v144, v146 quad_perm:[0,1,2,3] row_mask:0x3 bank_mask:0xf\n\t" \
  "v_add_f32_dpp v161, v145, v147 quad_perm:[0,1,2,3] row_mask:0x3 bank_mask:0xf\n\t" \
  "v_add_f32_dpp v162, v148, v150 quad_perm:[0,1,2,3] row_mask:0x3 bank_mask:0xf\n\t" \
  "v_add_f32_dpp v163, v149, v151 quad_perm:[0,1,2,3] row_mask:0x3 bank_mask:0xf\n\t" \
  "v_add_f32_dpp v160, v152, v154 quad_perm:[0,1,2,3] row_mask:0xc bank_mask:0xf\n\t" \
  "v_add_f32_dpp v161, v153, v155 quad_perm:[0,1,2,3] row_mask:0xc bank_mask:0xf\n\t" \
  "v_add_f32_dpp v162, v156, v158 quad_perm:[0,1,2,3] row_mask:0xc bank_mask:0xf\n\t" \
  "v_add_f32_dpp v163, v157, v159 quad_perm:[0,1,2,3] row_mask:0xc bank_mask:0xf\n\t" \
  "s_nop 0\n\t" \
  "v_permlane32_swap_b32 v160, v162\n\t" \
  "v_permlane32_swap_b32 v161, v163\n\t" \
  "v_add_f32 v160, v160, v162\n\t" \
  "v_add_f32 v161, v161, v163\n\t" \
  "ds_write_b64 v169, v[160:161]\n\t" \
  "s_mov_b64 exec, 1\n\t" \
  "ds_inc_rtn_u32 v164, v168, v166 offset:4104\n\t" \
  "s_mov_b64 exec, -1\n\t" \
  "ds_read_b64 v[128:129], v170 offset:0\n\t" \
  "ds_read_b64 v[130:131], v170 offset:512\n\t" \
  "ds_read_b64 v[132:133], v170 offset:1024\n\t" \
  "ds_read_b64 v[134:135], v170 offset:1536\n\t" \
  "ds_read_b64 v[136:137], v170 offset:2048\n\t" \
  "ds_read_b64 v[138:139], v170 offset:2560\n\t" \
  "ds_read_b64 v[140:141], v170 offset:3072\n\t" \
  "ds_read_b64 v[142:143], v170 offset:3584\n\t" \
  "s_waitcnt lgkmcnt(8)\n\t" \
  "v_readfirstlane_b32 s93, v164\n\t" \
  "s_cmp_lg_u32 s93, 7\n\t" \
  "s_cbranch_scc1 .Lb1%=\n\t" \
  "s_waitcnt lgkmcnt(4)\n\t" \
  "v_pk_add_f32 v[128:129], v[128:129], v[130:131]\n\t" \
  "v_pk_add_f32 v[132:133], v[132:133], v[134:135]\n\t" \
  "s_waitcnt lgkmcnt(0)\n\t" \
  "v_pk_add_f32 v[136:137], v[136:137], v[138:139]\n\t" \
  "v_pk_add_f32 v[140:141], v[140:141], v[142:143]\n\t" \
  "v_pk_add_f32 v[128:129], v[128:129], v[132:133]\n\t" \
  "v_pk_add_f32 v[136:137], v[136:137], v[140:141]\n\t" \
  "v_pk_add_f32 v[128:129], v[128:129], v[136:137]\n\t" \
  "v_cvt_pk_bf16_f32 v128, v128, v129\n\t" \
  "s_nop 1\n\t" \
  "v_mov_b32_dpp v129, v128 quad_perm:[1,0,3,2] row_mask:0xf bank_mask:0xf\n\t" \
  "v_perm_b32 v128, v129, v128, v174\n\t" \
  "buffer_store_dword v128, v171, %[rs], %[sh] offen sc1\n\t" \
  "s_memtime s[90:91]\n\t" \
  "s_barrier\n\t" \
  "s_memtime s[76:77]\n\t" \
  "s_memrealtime s[78:79]\n\t" \
  "s_nop 3\n\t" \
  "s_memrealtime s[80:81]\n\t" \
  "s_nop 3\n\t" \
  "s_memrealtime s[82:83]\n\t" \
  "s_nop 3\n\t" \
  "s_memrealtime s[84:85]\n\t" \
  "s_nop 3\n\t" \
  "s_memrealtime s[86:87]\n\t" \
  "s_nop 3\n\t" \
  "s_memrealtime s[88:89]\n\t" \
  "s_mov_b32 s93, 0\n\t" \
  "s_waitcnt lgkmcnt(0)\n\t" \
  "s_cmp_eq_u32 s80, s78\n\t" \
  "s_addc_u32 s93, s93, 0\n\t" \
  "s_cmp_eq_u32 s82, s78\n\t" \
  "s_addc_u32 s93, s93, 0\n\t" \
  "s_cmp_eq_u32 s84, s78\n\t" \
  "s_addc_u32 s93, s93, 0\n\t" \
  "s_cmp_eq_u32 s86, s78\n\t" \
  "s_addc_u32 s93, s93, 0\n\t" \
  "s_cmp_eq_u32 s88, s78\n\t" \
  "s_addc_u32 s93, s93, 0\n\t" \
  "s_mul_i32 s93, s93, 21\n\t" \
  "s_add_u32 s76, s76, 15\n\t" \
  "s_add_u32 s76, s76, s93\n\t" \
  "s_lshl2_add_u32 s78, s78, 16\n\t" \
  "s_mov_b64 exec, 1\n\t" \
  "s_sub_i32 s90, s76, s90\n\t" \
  "s_mul_i32 s90, s90, 11900\n\t" \
  "s_ashr_i32 s90, s90, 16\n\t" \
  "s_sub_u32 s90, s78, s90\n\t" \
  "s_sub_u32 s90, s90, %[base]\n\t" \
  "v_mov_b32 v164, s90\n\t" \
  "ds_write_b32 v168, v164 offset:4108\n\t" \
  "s_mov_b64 exec, -1\n\t" \
  "s_waitcnt vmcnt(1)\n\t" \
  ".Lb2%=:\n\t" \
  "s_setprio 0\n\t" \
  "s_bitcmp0_b64 %[am], %[step]\n\t" \
  "s_cbranch_scc1 .La1%=\n\t" \
  "buffer_store_dwordx4 v[224:227], v228, %[ra], 0 offen sc1\n\t" \
  ".La1%=:\n\t" \
  "buffer_load_dwordx4 v[192:195], v239, %[rt], %[st] offen offset:0 sc1\n\t" \
  "buffer_load_dwordx4 v[196:199], v239, %[rt], %[st] offen offset:1024 sc1\n\t" \
  "v_mfma_f32_16x16x32_bf16 v[128:131], v[0:3], v[176:179], 0\n\t" \
  "v_mfma_f32_16x16x32_bf16 v[136:139], v[8:11], v[176:179], 0\n\t" \
  "ds_read2st64_b32 v[144:145], v231 offset0:0 offset1:8\n\t" \
  "ds_read2st64_b32 v[146:147], v231 offset0:16 offset1:24\n\t" \
  "ds_read2st64_b32 v[148:149], v231 offset0:32 offset1:40\n\t" \
  "ds_read2st64_b32 v[150:151], v231 offset0:48 offset1:56\n\t" \
  "buffer_load_dwordx4 v[200:203], v239, %[rt], %[st4] offen offset:0 sc1\n\t" \
  "buffer_load_dwordx4 v[204:207], v239, %[rt], %[st4] offen offset:1024 sc1\n\t" \
  "v_mfma_f32_16x16x32_bf16 v[132:135], v[4:7], v[180:183], 0\n\t" \
  "v_mfma_f32_16x16x32_bf16 v[140:143], v[12:15], v[180:183], 0\n\t" \
  "s_waitcnt lgkmcnt(0)\n\t" \
  "v_add_f32 v144, v144, v145\n\t" \
  "v_add_f32 v146, v146, v147\n\t" \
  "v_add_f32 v148, v148, v149\n\t" \
  "v_add_f32 v150, v150, v151\n\t" \
  "v_add_f32 v144, v144, v146\n\t" \
  "v_add_f32 v148, v148, v150\n\t" \
  "buffer_load_dwordx4 v[208:211], v240, %[rt], %[st] offen offset:0 sc1\n\t" \
  "buffer_load_dwordx4 v[212:215], v240, %[rt], %[st] offen offset:1024 sc1\n\t" \
  "v_mfma_f32_16x16x32_bf16 v[128:131], v[16:19], v[184:187], v[128:131]\n\t" \
  "v_mfma_f32_16x16x32_bf16 v[136:139], v[24:27], v[184:187], v[136:139]\n\t" \
  "v_add_f32 v144, v144, v148\n\t" \
  "v_cvt_pk_bf16_f32 v144, v144, v144\n\t" \
  "ds_write_b16 v232, v144\n\t" \
  "ds_read_b128 v[224:227], v229\n\t" \
  "v_add_u32 v228, %[ainc], v228\n\t" \
  "v_add_u32 v229, %[rinc], v229\n\t" \
  "v_add_u32 v232, 1024, v232\n\t" \
  "v_xor_b32 v231, 0x4000, v231\n\t" \
  "buffer_load_dwordx4 v[216:219], v240, %[rt], %[st4] offen offset:0 sc1\n\t" \
  "buffer_load_dwordx4 v[220:223], v240, %[rt], %[st4] offen offset:1024 sc1\n\t" \
  "v_mfma_f32_16x16x32_bf16 v[132:135], v[20:23], v[188:191], v[132:135]\n\t" \
  "v_mfma_f32_16x16x32_bf16 v[140:143], v[28:31], v[188:191], v[140:143]\n\t" \
  "buffer_load_dwordx4 v[176:179], v241, %[rt], %[st] offen offset:0 sc1\n\t" \
  "s_waitcnt vmcnt(8)\n\t" \
  "v_mfma_f32_16x16x32_bf16 v[128:131], v[32:35], v[192:195], v[128:131]\n\t" \
  "v_mfma_f32_16x16x32_bf16 v[136:139], v[40:43], v[192:195], v[136:139]\n\t" \
  "buffer_load_dwordx4 v[180:183], v241, %[rt], %[st] offen offset:1024 sc1\n\t" \
  "s_waitcnt vmcnt(8)\n\t" \
  "v_mfma_f32_16x16x32_bf16 v[132:135], v[36:39], v[196:199], v[132:135]\n\t" \
  "v_mfma_f32_16x16x32_bf16 v[140:143], v[44:47], v[196:199], v[140:143]\n\t" \
  "buffer_load_dwordx4 v[184:187], v241, %[rt], %[st4] offen offset:0 sc1\n\t" \
  "s_waitcnt vmcnt(8)\n\t" \
  "v_mfma_f32_16x16x32_bf16 v[128:131], v[48:51], v[200:203], v[128:131]\n\t" \
  "v_mfma_f32_16x16x32_bf16 v[136:139], v[56:59], v[200:203], v[136:139]\n\t" \
  "ds_read_b128 v[160:163], v168 offset:4096\n\t" \
  "buffer_load_dwordx4 v[188:191], v241, %[rt], %[st4] offen offset:1024 sc1\n\t" \
  "s_waitcnt vmcnt(8)\n\t" \
  "v_mfma_f32_16x16x32_bf16 v[132:135], v[52:55], v[204:207], v[132:135]\n\t" \
  "v_mfma_f32_16x16x32_bf16 v[140:143], v[60:63], v[204:207], v[140:143]\n\t" \
  "s_waitcnt lgkmcnt(0)\n\t" \
  "v_readfirstlane_b32 s90, v161\n\t" \
  "v_readfirstlane_b32 s91, v163\n\t" \
  "s_add_u32 s93, s90, %[rl]\n\t" \
  "s_add_u32 s93, s93, %[base]\n\t" \
  "s_add_u32 %[E], s93, %[lk]\n\t" \
  "s_sub_i32 s91, s91, s90\n\t" \
  "s_sub_i32 s91, s91, %[rl]\n\t" \
  "s_min_i32 s93, s91, %[up]\n\t" \
  "s_max_i32 s93, s93, %[dn]\n\t" \
  "s_cmp_gt_i32 s91, 60\n\t" \
  "s_cselect_b32 s93, 2, s93\n\t" \
  "s_add_i32 %[rl], %[rl], s93\n\t" \
  "s_waitcnt vmcnt(7)\n\t" \
  "v_mfma_f32_16x16x32_bf16 v[128:131], v[64:67], v[208:211], v[128:131]\n\t" \
  "v_mfma_f32_16x16x32_bf16 v[136:139], v[72:75], v[208:211], v[136:139]\n\t" \
  "s_add_u32 %[step], %[step], 1\n\t" \
  "s_lshl_b32 %[sh], %[step], 15\n\t" \
  "s_lshr_b32 s93, %[step], 4\n\t" \
  "s_lshr_b32 s93, %[pm], s93\n\t" \
  "s_and_b32 s93, s93, 4\n\t" \
  "s_lshl_b32 s93, s93, 11\n\t" \
  "s_lshl2_add_u32 %[so], %[sh], s93\n\t" \
  "s_waitcnt vmcnt(6)\n\t" \
  "v_mfma_f32_16x16x32_bf16 v[132:135], v[68:71], v[212:215], v[132:135]\n\t" \
  "v_mfma_f32_16x16x32_bf16 v[140:143], v[76:79], v[212:215], v[140:143]\n\t" \
  "s_sub_i32 s93, %[step], 2\n\t" \
  "s_lshl_b32 %[st], s93, 19\n\t" \
  "s_lshl_b32 s94, s93, 11\n\t" \
  "s_xor_b32 s94, s94, %[s0]\n\t" \
  "s_and_b32 s94, s94, 0x2000\n\t" \
  "s_or_b32 %[st], %[st], s94\n\t" \
  "s_cmp_lt_i32 s93, 0\n\t" \
  "s_cselect_b32 %[st], 0x70000000, %[st]\n\t" \
  "s_add_u32 %[st4], %[st], 4096\n\t" \
  "s_waitcnt vmcnt(5)\n\t" \
  "v_mfma_f32_16x16x32_bf16 v[128:131], v[80:83], v[216:219], v[128:131]\n\t" \
  "v_mfma_f32_16x16x32_bf16 v[136:139], v[88:91], v[216:219], v[136:139]\n\t" \
  "s_waitcnt vmcnt(4)\n\t" \
  "v_mfma_f32_16x16x32_bf16 v[132:135], v[84:87], v[220:223], v[132:135]\n\t" \
  "v_mfma_f32_16x16x32_bf16 v[140:143], v[92:95], v[220:223], v[140:143]\n\t" \
  "s_cmp_eq_u32 %[step], 63\n\t" \
  "s_cbranch_scc1 .Llast%=\n\t" \
  "s_movk_i32 s93, 600\n\t" \
  ".Lpacel%=:\n\t" \
  "s_memrealtime s[90:91]\n\t" \
  "s_waitcnt lgkmcnt(0)\n\t" \
  "s_lshl_b32 s90, s90, 2\n\t" \
  "s_sub_i32 s91, %[E], s90\n\t" \
  "s_cmp_le_i32 s91, 0\n\t" \
  "s_cbranch_scc1 .Lgol%=\n\t" \
  "s_sub_u32 s93, s93, 1\n\t" \
  "s_cmp_lg_u32 s93, 0\n\t" \
  "s_cbranch_scc1 .Lpacel%=\n\t" \
  ".Lgol%=:\n\t" \
  "buffer_load_dwordx2 v[166:167], v173, %[rs], 0 offen sc1\n\t" \
  "s_waitcnt vmcnt(4)\n\t" \
  "v_mfma_f32_16x16x32_bf16 v[128:131], v[96:99], v[176:179], v[128:131]\n\t" \
  "v_mfma_f32_16x16x32_bf16 v[136:139], v[104:107], v[176:179], v[136:139]\n\t" \
  "s_waitcnt vmcnt(3)\n\t" \
  "v_mfma_f32_16x16x32_bf16 v[132:135], v[100:103], v[180:183], v[132:135]\n\t" \
  "v_mfma_f32_16x16x32_bf16 v[140:143], v[108:111], v[180:183], v[140:143]\n\t" \
  "s_waitcnt vmcnt(2)\n\t" \
  "v_mfma_f32_16x16x32_bf16 v[128:131], v[112:115], v[184:187], v[128:131]\n\t" \
  "v_mfma_f32_16x16x32_bf16 v[136:139], v[120:123], v[184:187], v[136:139]\n\t" \
  "s_waitcnt vmcnt(1)\n\t" \
  "v_mfma_f32_16x16x32_bf16 v[132:135], v[116:119], v[188:191], v[132:135]\n\t" \
  "v_mfma_f32_16x16x32_bf16 v[140:143], v[124:127], v[188:191], v[140:143]\n\t" \
  "v_mov_b64 v[144:145], 0\n\t" \
  "v_mov_b64 v[146:147], 0\n\t" \
  "v_mov_b64 v[148:149], 0\n\t" \
  "v_mov_b64 v[150:151], 0\n\t" \
  "v_mov_b64 v[152:153], 0\n\t" \
  "v_mov_b64 v[154:155], 0\n\t" \
  "v_mov_b64 v[156:157], 0\n\t" \
  "v_mov_b64 v[158:159], 0\n\t" \
  "v_pk_add_f32 v[128:129], v[128:129], v[132:133]\n\t" \
  "v_pk_add_f32 v[130:131], v[130:131], v[134:135]\n\t" \
  "ds_write_b128 v230, v[128:131] offset:0\n\t" \
  "v_pk_add_f32 v[136:137], v[136:137], v[140:141]\n\t" \
  "v_pk_add_f32 v[138:139], v[138:139], v[142:143]\n\t" \
  "ds_write_b128 v230, v[136:139] offset:1024\n\t" \
  "v_xor_b32 v230, 0x4000, v230\n\t" \
  "s_cmp_lg_u32 %[step], 63\n\t" \
  "s_cbranch_scc1 .Lstep%=\n\t" \
  ".Lfc%=:\n\t" \
  "s_waitcnt vmcnt(0)\n\t" \
  "v_cmp_eq_u32 vcc, -1, v166\n\t" \
  "s_cbranch_vccnz .Lfw%=\n\t" \
  ".Lfo%=:\n\t" \
  "v_add_u32 v242, 16384, v242\n\t" \
  "buffer_load_dwordx4 v[176:179], v238, %[rt], %[st] offen offset:0 sc1\n\t" \
  "buffer_load_dwordx4 v[180:183], v238, %[rt], %[st] offen offset:1024 sc1\n\t" \
  "buffer_load_dwordx4 v[184:187], v238, %[rt], %[st4] offen offset:0 sc1\n\t" \
  "buffer_load_dwordx4 v[188:191], v238, %[rt], %[st4] offen offset:1024 sc1\n\t" \
  "buffer_load_dwordx4 v[192:195], v239, %[rt], %[st] offen offset:0 sc1\n\t" \
  "buffer_load_dwordx4 v[196:199], v239, %[rt], %[st] offen offset:1024 sc1\n\t" \
  "buffer_load_dwordx4 v[200:203], v239, %[rt], %[st4] offen offset:0 sc1\n\t" \
  "buffer_load_dwordx4 v[204:207], v239, %[rt], %[st4] offen offset:1024 sc1\n\t" \
  "v_subrev_u32 v230, 98304, v230\n\t" \
  ".Lt%=:\n\t" \
  "s_waitcnt vmcnt(7)\n\t" \
  "v_mfma_f32_16x16x32_bf16 v[128:131], v[0:3], v[176:179], 0\n\t" \
  "v_mfma_f32_16x16x32_bf16 v[136:139], v[8:11], v[176:179], 0\n\t" \
  "buffer_load_dwordx4 v[176:179], v240, %[rt], %[st] offen offset:0 sc1\n\t" \
  "buffer_load_dword v166, v242, %[rs], 0 offen sc1\n\t" \
  "s_waitcnt vmcnt(8)\n\t" \
  "v_mfma_f32_16x16x32_bf16 v[132:135], v[4:7], v[180:183], 0\n\t" \
  "v_mfma_f32_16x16x32_bf16 v[140:143], v[12:15], v[180:183], 0\n\t" \
  "buffer_load_dwordx4 v[180:183], v240, %[rt], %[st] offen offset:1024 sc1\n\t" \
  "s_waitcnt vmcnt(8)\n\t" \
  "v_mfma_f32_16x16x32_bf16 v[128:131], v[16:19], v[184:187], v[128:131]\n\t" \
  "v_mfma_f32_16x16x32_bf16 v[136:139], v[24:27], v[184:187], v[136:139]\n\t" \
  "buffer_load_dwordx4 v[184:187], v240, %[rt], %[st4] offen offset:0 sc1\n\t" \
  "s_waitcnt vmcnt(8)\n\t" \
  "v_mfma_f32_16x16x32_bf16 v[132:135], v[20:23], v[188:191], v[132:135]\n\t" \
  "v_mfma_f32_16x16x32_bf16 v[140:143], v[28:31], v[188:191], v[140:143]\n\t" \
  "buffer_load_dwordx4 v[188:191], v240, %[rt], %[st4] offen offset:1024 sc1\n\t" \
  "s_waitcnt vmcnt(8)\n\t" \
  "v_mfma_f32_16x16x32_bf16 v[128:131], v[32:35], v[192:195], v[128:131]\n\t" \
  "v_mfma_f32_16x16x32_bf16 v[136:139], v[40:43], v[192:195], v[136:139]\n\t" \
  "buffer_load_dwordx4 v[192:195], v241, %[rt], %[st] offen offset:0 sc1\n\t" \
  "s_waitcnt vmcnt(8)\n\t" \
  "v_mfma_f32_16x16x32_bf16 v[132:135], v[36:39], v[196:199], v[132:135]\n\t" \
  "v_mfma_f32_16x16x32_bf16 v[140:143], v[44:47], v[196:199], v[140:143]\n\t" \
  "buffer_load_dwordx4 v[196:199], v241, %[rt], %[st] offen offset:1024 sc1\n\t" \
  "s_waitcnt vmcnt(8)\n\t" \
  "v_mfma_f32_16x16x32_bf16 v[128:131], v[48:51], v[200:203], v[128:131]\n\t" \
  "v_mfma_f32_16x16x32_bf16 v[136:139], v[56:59], v[200:203], v[136:139]\n\t" \
  "buffer_load_dwordx4 v[200:203], v241, %[rt], %[st4] offen offset:0 sc1\n\t" \
  "s_waitcnt vmcnt(8)\n\t" \
  "v_mfma_f32_16x16x32_bf16 v[132:135], v[52:55], v[204:207], v[132:135]\n\t" \
  "v_mfma_f32_16x16x32_bf16 v[140:143], v[60:63], v[204:207], v[140:143]\n\t" \
  "buffer_load_dwordx4 v[204:207], v241, %[rt], %[st4] offen offset:1024 sc1\n\t" \
  "s_waitcnt vmcnt(8)\n\t" \
  "v_mfma_f32_16x16x32_bf16 v[128:131], v[64:67], v[176:179], v[128:131]\n\t" \
  "v_mfma_f32_16x16x32_bf16 v[136:139], v[72:75], v[176:179], v[136:139]\n\t" \
  "s_waitcnt vmcnt(7)\n\t" \
  ".Ltc%=:\n\t" \
  "v_cmp_eq_u32 vcc, -1, v166\n\t" \
  "s_cbranch_vccnz .Ltw%=\n\t" \
  ".Lto%=:\n\t" \
  "s_cmp_eq_u32 %[step], 65\n\t" \
  "s_cbranch_scc1 .Ltl%=\n\t" \
  "s_add_u32 %[step], %[step], 1\n\t" \
  "s_sub_i32 s93, %[step], 2\n\t" \
  "s_lshl_b32 %[st], s93, 19\n\t" \
  "s_lshl_b32 s94, s93, 11\n\t" \
  "s_xor_b32 s94, s94, %[s0]\n\t" \
  "s_and_b32 s94, s94, 0x2000\n\t" \
  "s_or_b32 %[st], %[st], s94\n\t" \
  "s_cmp_lt_i32 s93, 0\n\t" \
  "s_cselect_b32 %[st], 0x70000000, %[st]\n\t" \
  "s_add_u32 %[st4], %[st], 4096\n\t" \
  "v_add_u32 v242, 16384, v242\n\t" \
  "s_cmp_eq_u32 %[step], 65\n\t" \
  "s_cbranch_scc0 .Ltn%=\n\t" \
  "v_mov_b32 v242, %[fg]\n\t" \
  ".Ltn%=:\n\t" \
  "buffer_load_dwordx4 v[176:179], v238, %[rt], %[st] offen offset:0 sc1\n\t" \
  "s_waitcnt vmcnt(7)\n\t" \
  "v_mfma_f32_16x16x32_bf16 v[132:135], v[68:71], v[180:183], v[132:135]\n\t" \
  "v_mfma_f32_16x16x32_bf16 v[140:143], v[76:79], v[180:183], v[140:143]\n\t" \
  "buffer_load_dwordx4 v[180:183], v238, %[rt], %[st] offen offset:1024 sc1\n\t" \
  "s_waitcnt vmcnt(7)\n\t" \
  "v_mfma_f32_16x16x32_bf16 v[128:131], v[80:83], v[184:187], v[128:131]\n\t" \
  "v_mfma_f32_16x16x32_bf16 v[136:139], v[88:91], v[184:187], v[136:139]\n\t" \
  "buffer_load_dwordx4 v[184:187], v238, %[rt], %[st4] offen offset:0 sc1\n\t" \
  "s_waitcnt vmcnt(7)\n\t" \
  "v_mfma_f32_16x16x32_bf16 v[132:135], v[84:87], v[188:191], v[132:135]\n\t" \
  "v_mfma_f32_16x16x32_bf16 v[140:143], v[92:95], v[188:191], v[140:143]\n\t" \
  "buffer_load_dwordx4 v[188:191], v238, %[rt], %[st4] offen offset:1024 sc1\n\t" \
  "s_waitcnt vmcnt(7)\n\t" \
  "v_mfma_f32_16x16x32_bf16 v[128:131], v[96:99], v[192:195], v[128:131]\n\t" \
  "v_mfma_f32_16x16x32_bf16 v[136:139], v[104:107], v[192:195], v[136:139]\n\t" \
  "buffer_load_dwordx4 v[192:195], v239, %[rt], %[st] offen offset:0 sc1\n\t" \
  "s_waitcnt vmcnt(7)\n\t" \
  "v_mfma_f32_16x16x32_bf16 v[132:135], v[100:103], v[196:199], v[132:135]\n\t" \
  "v_mfma_f32_16x16x32_bf16 v[140:143], v[108:111], v[196:199], v[140:143]\n\t" \
  "buffer_load_dwordx4 v[196:199], v239, %[rt], %[st] offen offset:1024 sc1\n\t" \
  "s_waitcnt vmcnt(7)\n\t" \
  "v_mfma_f32_16x16x32_bf16 v[128:131], v[112:115], v[200:203], v[128:131]\n\t" \
  "v_mfma_f32_16x16x32_bf16 v[136:139], v[120:123], v[200:203], v[136:139]\n\t" \
  "buffer_load_dwordx4 v[200:203], v239, %[rt], %[st4] offen offset:0 sc1\n\t" \
  "s_waitcnt vmcnt(7)\n\t" \
  "v_mfma_f32_16x16x32_bf16 v[132:135], v[116:119], v[204:207], v[132:135]\n\t" \
  "v_mfma_f32_16x16x32_bf16 v[140:143], v[124:127], v[204:207], v[140:143]\n\t" \
  "buffer_load_dwordx4 v[204:207], v239, %[rt], %[st4] offen offset:1024 sc1\n\t" \
  "s_nop 5\n\t" \
  "v_pk_add_f32 v[128:129], v[128:129], v[132:133]\n\t" \
  "v_pk_add_f32 v[130:131], v[130:131], v[134:135]\n\t" \
  "ds_write_b128 v230, v[128:131] offset:0\n\t" \
  "v_pk_add_f32 v[136:137], v[136:137], v[140:141]\n\t" \
  "v_pk_add_f32 v[138:139], v[138:139], v[142:143]\n\t" \
  "ds_write_b128 v230, v[136:139] offset:1024\n\t" \
  "v_add_u32 v230, 16384, v230\n\t" \
  "s_branch .Lt%=\n\t" \
  ".Ltl%=:\n\t" \
  "buffer_load_dwordx4 v[224:227], v243, %[rg], 0 offen sc1\n\t" \
  "s_waitcnt vmcnt(7)\n\t" \
  "v_mfma_f32_16x16x32_bf16 v[132:135], v[68:71], v[180:183], v[132:135]\n\t" \
  "v_mfma_f32_16x16x32_bf16 v[140:143], v[76:79], v[180:183], v[140:143]\n\t" \
  "s_waitcnt vmcnt(6)\n\t" \
  "v_mfma_f32_16x16x32_bf16 v[128:131], v[80:83], v[184:187], v[128:131]\n\t" \
  "v_mfma_f32_16x16x32_bf16 v[136:139], v[88:91], v[184:187], v[136:139]\n\t" \
  "s_waitcnt vmcnt(5)\n\t" \
  "v_mfma_f32_16x16x32_bf16 v[132:135], v[84:87], v[188:191], v[132:135]\n\t" \
  "v_mfma_f32_16x16x32_bf16 v[140:143], v[92:95], v[188:191], v[140:143]\n\t" \
  "s_waitcnt vmcnt(4)\n\t" \
  "v_mfma_f32_16x16x32_bf16 v[128:131], v[96:99], v[192:195], v[128:131]\n\t" \
  "v_mfma_f32_16x16x32_bf16 v[136:139], v[104:107], v[192:195], v[136:139]\n\t" \
  "s_waitcnt vmcnt(3)\n\t" \
  "v_mfma_f32_16x16x32_bf16 v[132:135], v[100:103], v[196:199], v[132:135]\n\t" \
  "v_mfma_f32_16x16x32_bf16 v[140:143], v[108:111], v[196:199], v[140:143]\n\t" \
  "s_waitcnt vmcnt(2)\n\t" \
  "v_mfma_f32_16x16x32_bf16 v[128:131], v[112:115], v[200:203], v[128:131]\n\t" \
  "v_mfma_f32_16x16x32_bf16 v[136:139], v[120:123], v[200:203], v[136:139]\n\t" \
  "s_waitcnt vmcnt(1)\n\t" \
  "v_mfma_f32_16x16x32_bf16 v[132:135], v[116:119], v[204:207], v[132:135]\n\t" \
  "v_mfma_f32_16x16x32_bf16 v[140:143], v[124:127], v[204:207], v[140:143]\n\t" \
  "s_nop 6\n\t" \
  "v_pk_add_f32 v[128:129], v[128:129], v[132:133]\n\t" \
  "v_pk_add_f32 v[130:131], v[130:131], v[134:135]\n\t" \
  "ds_write_b128 v230, v[128:131] offset:0\n\t" \
  "v_pk_add_f32 v[136:137], v[136:137], v[140:141]\n\t" \
  "v_pk_add_f32 v[138:139], v[138:139], v[142:143]\n\t" \
  "ds_write_b128 v230, v[136:139] offset:1024\n\t" \
  "s_waitcnt vmcnt(0) lgkmcnt(0)\n\t" \
  "s_branch .Lend%=\n\t" \
  ".Ltw%=:\n\t" \
  "s_add_u32 s92, s92, 1\n\t" \
  "s_sleep 2\n\t" \
  "s_and_b32 s93, s92, 63\n\t" \
  "s_cbranch_scc1 .Ltr%=\n\t" \
  "buffer_load_dword v164, off, %[rs], %[sab] sc1\n\t" \
  "s_waitcnt vmcnt(0)\n\t" \
  "v_readfirstlane_b32 s93, v164\n\t" \
  "s_nop 3\n\t" \
  "s_cmp_eq_u32 s93, 1\n\t" \
  "s_cbranch_scc1 .Lto%=\n\t" \
  "s_cmp_lt_u32 s92, 0x80000\n\t" \
  "s_cbranch_scc1 .Ltr%=\n\t" \
  "v_mov_b32 v164, 1\n\t" \
  "buffer_store_dword v164, off, %[rs], %[sab] sc1\n\t" \
  "s_waitcnt vmcnt(0)\n\t" \
  "s_branch .Lto%=\n\t" \
  ".Ltr%=:\n\t" \
  "buffer_load_dword v166, v242, %[rs], 0 offen sc1\n\t" \
  "s_waitcnt vmcnt(0)\n\t" \
  "s_branch .Ltc%=\n\t" \
  ".Llast%=:\n\t" \
  "v_mov_b32 v173, v242\n\t" \
  "s_branch .Lgol%=\n\t" \
  ".Lfw%=:\n\t" \
  "s_add_u32 s92, s92, 1\n\t" \
  "s_sleep 2\n\t" \
  "s_and_b32 s93, s92, 63\n\t" \
  "s_cbranch_scc1 .Lfr%=\n\t" \
  "buffer_load_dword v164, off, %[rs], %[sab] sc1\n\t" \
  "s_waitcnt vmcnt(0)\n\t" \
  "v_readfirstlane_b32 s93, v164\n\t" \
  "s_nop 3\n\t" \
  "s_cmp_eq_u32 s93, 1\n\t" \
  "s_cbranch_scc1 .Lfo%=\n\t" \
  "s_cmp_lt_u32 s92, 0x80000\n\t" \
  "s_cbranch_scc1 .Lfr%=\n\t" \
  "v_mov_b32 v164, 1\n\t" \
  "buffer_store_dword v164, off, %[rs], %[sab] sc1\n\t" \
  "s_waitcnt vmcnt(0)\n\t" \
  "s_branch .Lfo%=\n\t" \
  ".Lfr%=:\n\t" \
  "buffer_load_dwordx2 v[166:167], v173, %[rs], 0 offen sc1\n\t" \
  "s_branch .Lfc%=\n\t" \
  ".Lb1%=:\n\t" \
  "s_waitcnt vmcnt(0) lgkmcnt(0)\n\t" \
  "s_barrier\n\t" \
  "s_branch .Lb2%=\n\t" \
  ".Lbl%=:\n\t" \
  "s_bitset1_b64 %[ps], %[step]\n\t" \
  "s_cmp_lt_u32 s92, 3\n\t" \
  "s_cbranch_scc1 .Lbm%=\n\t" \
  "s_sleep 12\n\t" \
  ".Lbm%=:\n\t" \
  "s_add_u32 s92, s92, 1\n\t" \
  "s_sleep 2\n\t" \
  "s_and_b32 s93, s92, 63\n\t" \
  "s_cbranch_scc1 .Lba%=\n\t" \
  "buffer_load_dword v164, off, %[rs], %[sab] sc1\n\t" \
  "s_waitcnt vmcnt(0)\n\t" \
  "v_readfirstlane_b32 s93, v164\n\t" \
  "s_nop 3\n\t" \
  "s_cmp_eq_u32 s93, 1\n\t" \
  "s_cbranch_scc1 .Lgo%=\n\t" \
  "s_cmp_lt_u32 s92, 0x80000\n\t" \
  "s_cbranch_scc1 .Lba%=\n\t" \
  "v_mov_b32 v164, 1\n\t" \
  "buffer_store_dword v164, off, %[rs], %[sab] sc1\n\t" \
  "s_waitcnt vmcnt(0)\n\t" \
  "s_branch .Lgo%=\n\t" \
  ".Lba%=:\n\t" \
  "buffer_load_dwordx2 v[166:167], v173, %[rs], 0 offen sc1\n\t" \
  "s_branch .Lcal%=\n\t" \
  ".Lslow%=:\n\t" \
  "s_mov_b64 s[94:95], 0\n\t" \
  "v_cmp_eq_u32 vcc, %[sent], v128\n\t" \
  "s_or_b64 s[94:95], s[94:95], vcc\n\t" \
  "v_cmp_eq_u32 vcc, %[sent], v129\n\t" \
  "s_or_b64 s[94:95], s[94:95], vcc\n\t" \
  "v_cmp_eq_u32 vcc, %[sent], v130\n\t" \
  "s_or_b64 s[94:95], s[94:95], vcc\n\t" \
  "v_cmp_eq_u32 vcc, %[sent], v131\n\t" \
  "s_or_b64 s[94:95], s[94:95], vcc\n\t" \
  "v_cmp_eq_u32 vcc, %[sent], v132\n\t" \
  "s_or_b64 s[94:95], s[94:95], vcc\n\t" \
  "v_cmp_eq_u32 vcc, %[sent], v133\n\t" \
  "s_or_b64 s[94:95], s[94:95], vcc\n\t" \
  "v_cmp_eq_u32 vcc, %[sent], v134\n\t" \
  "s_or_b64 s[94:95], s[94:95], vcc\n\t" \
  "v_cmp_eq_u32 vcc, %[sent], v135\n\t" \
  "s_or_b64 s[94:95], s[94:95], vcc\n\t" \
  "v_cmp_eq_u32 vcc, %[sent], v136\n\t" \
  "s_or_b64 s[94:95], s[94:95], vcc\n\t" \
  "v_cmp_eq_u32 vcc, %[sent], v137\n\t" \
  "s_or_b64 s[94:95], s[94:95], vcc\n\t" \
  "v_cmp_eq_u32 vcc, %[sent], v138\n\t" \
  "s_or_b64 s[94:95], s[94:95], vcc\n\t" \
  "v_cmp_eq_u32 vcc, %[sent], v139\n\t" \
  "s_or_b64 s[94:95], s[94:95], vcc\n\t" \
  "v_cmp_eq_u32 vcc, %[sent], v140\n\t" \
  "s_or_b64 s[94:95], s[94:95], vcc\n\t" \
  "v_cmp_eq_u32 vcc, %[sent], v141\n\t" \
  "s_or_b64 s[94:95], s[94:95], vcc\n\t" \
  "v_cmp_eq_u32 vcc, %[sent], v142\n\t" \
  "s_or_b64 s[94:95], s[94:95], vcc\n\t" \
  "v_cmp_eq_u32 vcc, %[sent], v143\n\t" \
  "s_or_b64 s[94:95], s[94:95], vcc\n\t" \
  "s_cbranch_scc0 .Lok%=\n\t" \
  "s_bitset1_b64 %[fm], %[step]\n\t" \
  "s_add_u32 s92, s92, 1\n\t" \
  "s_sleep 1\n\t" \
  "s_and_b32 s93, s92, 63\n\t" \
  "s_cbranch_scc1 .Lre%=\n\t" \
  "buffer_load_dword v164, off, %[rs], %[sab] sc1\n\t" \
  "s_waitcnt vmcnt(0)\n\t" \
  "v_readfirstlane_b32 s93, v164\n\t" \
  "s_nop 3\n\t" \
  "s_cmp_eq_u32 s93, 1\n\t" \
  "s_cbranch_scc1 .Lok%=\n\t" \
  "s_cmp_lt_u32 s92, 0x80000\n\t" \
  "s_cbranch_scc1 .Lre%=\n\t" \
  "v_mov_b32 v164, 1\n\t" \
  "buffer_store_dword v164, off, %[rs], %[sab] sc1\n\t" \
  "s_waitcnt vmcnt(0)\n\t" \
  "s_branch .Lok%=\n\t" \
  ".Lre%=:\n\t" \
  "buffer_load_dwordx4 v[128:131], v234, %[rs], %[so] offen sc1\n\t" \
  "s_nop 15\n\t" \
  "buffer_load_dwordx4 v[132:135], v235, %[rs], %[so] offen sc1\n\t" \
  "s_nop 15\n\t" \
  "buffer_load_dwordx4 v[136:139], v236, %[rs], %[so] offen sc1\n\t" \
  "s_nop 15\n\t" \
  "buffer_load_dwordx4 v[140:143], v237, %[rs], %[so] offen sc1\n\t" \
  "v_mov_b64 v[144:145], 0\n\t" \
  "v_mov_b64 v[146:147], 0\n\t" \
  "v_mov_b64 v[148:149], 0\n\t" \
  "v_mov_b64 v[150:151], 0\n\t" \
  "v_mov_b64 v[152:153], 0\n\t" \
  "v_mov_b64 v[154:155], 0\n\t" \
  "v_mov_b64 v[156:157], 0\n\t" \
  "v_mov_b64 v[158:159], 0\n\t" \
  "s_waitcnt vmcnt(0)\n\t" \
  "s_branch .Lw%=\n\t" \
  ".Lend%=:\n\t"

typedef __attribute__((ext_vector_type(32))) unsigned u32x32;
__device__ __forceinline__ void put4(u32x32& W, int i, bf16x8 f) {
  const u32x4 v = __builtin_bit_cast(u32x4, f);
#pragma unroll
  for (int j = 0; j < 4; ++j) W[4 * i + j] = v[j];
}
__device__ __forceinline__ bf16x8 get4(const u32x32& W, int i) {
  return as_bf16x8((u32x4){W[4 * i], W[4 * i + 1], W[4 * i + 2], W[4 * i + 3]});
}
__device__ __forceinline__ int look_prog(__amdgpu_buffer_rsrc_t rs, int off) {
  asm volatile("" ::: "memory");
  const int v = (int)ld_flag(rs, off);
  return min(__builtin_amdgcn_readlane(v, 0), __builtin_amdgcn_readlane(v, 1));
}
__device__ __forceinline__ int wait_prog(__amdgpu_buffer_rsrc_t rs, int off, int need, gu32* abortw) {
  int v;
  for (unsigned spins = 0;;) {
    v = look_prog(rs, off);
    if (v >= need) break;
    __builtin_amdgcn_s_sleep(4);
    if (spin_fail(spins, abortw)) break;
  }
  asm volatile("" ::: "memory");
  return v;
}
#define CLB8(a, b, c, d, e, f, g, h) "v" #a, "v" #b, "v" #c, "v" #d, "v" #e, "v" #f, "v" #g, "v" #h
#define CLB_S "s90", "s91", "s92", "s93", "s94", "s95", "vcc", "scc", "memory"
#define W_IN "{v[0:31]}"(W[0]), "{v[32:63]}"(W[1]), "{v[64:95]}"(W[2]), "{v[96:127]}"(W[3]), "{v[128:159]}"(W[4]), "{v[160:191]}"(W[5])

#define W_DELAY 60
#define LEAD0 378
#define LEAD_D0 6
#define QBJ 3
constexpr int L_MEM = 0, L_KT = 16384, L_QX = 24576, L_KX = 26624, L_VX = 16384, L_MISC = 30720, L_WL = 32768;

#define QK_STEP(TXT)                                                                                                        \
  asm volatile(TXT                                                                                                          \
      : [tout] "=&s"(tout), [spins] "=&s"(spins), [tb1] "=&s"(tb1), [tgt] "=&s"(tgt), "+{v248}"(aL16), "+{v249}"(aMW), "+{v250}"(aWL),                            \
        "+{v251}"(aIDX), "+{v252}"(aQW), "+{v[232:235]}"(QF)                                                                 \
      : W_IN, [rs] "s"(rs4), [som] "s"(o_mem(pm, s) + WV * 1024), [som2] "s"(o_mem(pm, s) + WV * 1024 + 16384), [sent] "s"(DSENT), [sab] "s"(SY_ABORT), \
        [lead] "s"(lead), [step] "s"(s), [sbc] "s"(SY_BA + side(pm, 1) + h * 128), [nh] "s"(0xFF00FF00FF00FF00ull), [up] "s"(0xFFFFFFFF00000000ull), [negc] "s"(-cexp),      \
        [c] "s"(cexp), [so] "s"(o_o(pm, s, h) + WV * 512), [sk] "s"(o_v(pm, s, h) + 2048 + (WV & 3) * 512),                    \
        [sv] "s"(o_v(pm, s, h) + WV * 512), [rt] "s"(rt4), [svt] "s"(t_off(p, T_V + kseg(s, h)) + WV * 2048)                  \
      : CLB8(192, 193, 194, 195, 196, 197, 198, 199), CLB8(200, 201, 202, 203, 204, 205, 206, 207),                          \
        CLB8(208, 209, 210, 211, 212, 213, 214, 215), CLB8(216, 217, 218, 219, 220, 221, 222, 223),                          \
        CLB8(224, 225, 226, 227, 228, 229, 230, 231), CLB8(236, 237, 238, 239, 240, 241, 242, 243), "v244", "v245", "v246", "v247", "v253", CLB_S)

template <int WV>
__device__ __forceinline__ void qk_wave(Args& p, int h, char* smem) {
  const int tid = threadIdx.x;
  unsigned pm = 0;
  int have = -1;
  u32x32 W[6];
  {
    int lane = tid & 63;
    asm volatile("" : "+v"(lane));
    const unsigned vo = (WV * 4 + (lane >> 4)) * (HD * 2) + 16 * ((lane & 15) ^ swzB((WV * 4 + (lane >> 4)) & 15));
    auto stage = [&](int j) {
#pragma unroll
      for (int mm = 0; mm < 2; ++mm) {
        const char* src = (const char*)((mm ? p.Wk : p.Wq) + (size_t)j * 128 * HD + h * DH);
        const unsigned dst = lds_off(smem) + (~j & 1) * 65536 + mm * 32768 + WV * 1024;
        glds2(src, 32 * HD * 2, vo, dst);
        glds2(src + 64 * HD * 2, 32 * HD * 2, vo, dst + 16384);
      }
    };
    bf16x8 keep[4][4];
    auto place = [&](int g) {
#pragma unroll
      for (int i = 0; i < 4; ++i)
        *reinterpret_cast<bf16x8*>(smem + (g == 0 ? 65536 : g == 1 ? 32768 : g == 2 ? 131072 : 98304) + WV * 4096 + i * 1024 + lane * 16) = keep[g][i];
    };
    stage(0); stage(1);
    Boot bt;
#pragma unroll
    for (int j = 0; j < 8; ++j) {
      if (j < 7) WAIT_V(8); else WAIT_V(0);
      if (WV == 7 && j == QBJ + 1) pm = boot_done(p.ws, bt);
      LBAR();
#pragma unroll
      for (int k = 0; k < 8; ++k) {
        const int P = (QK_MAP[WV] >> (4 * (2 * j + (k >> 2)))) & 15;
        const bf16x8 fr = ld_bfrag(smem + (~j & 1) * 65536 + (WV >> 2) * 32768 + (k >> 2) * 16384, k & 1, 2 * (WV & 3) + ((k >> 1) & 1), lane);
        const int f = P * 4 + (k & 3);
        if (P < 12) put4(W[f >> 3], f & 7, fr);
        else keep[P - 12][k & 3] = fr;
      }
      if (j == (WV >> 1)) place(2);
      LBAR();
      if (WV == 7 && j == QBJ) boot_ask(p.ws, bt);
      if (j + 2 < 8) stage(j + 2);
      if (j == 6) { place(0); place(3); }
    }
    place(1);
    if (WV == 7 && lane < 8) lds_at(smem + L_MISC)[lane] = lane == 7 ? pm : (lane == 1 ? now_q() : 0);
    if (WV == 6) lds_at(smem + 28672)[lane] = ~0u;
    LBAR();
    pm = uni(lds_at(smem + L_MISC)[7]);
    p.s0 = pm >> 31;
    if (WV == 6) RDW(h);
    if (WV == 4 || WV == 5) {
      have = wait_prog(mkrsrc(p.ws + WS_SY, SY_TOTAL), fkq(h, (lane & 1) * 2), 0, (gu32*)(p.ws + WS_SY + SY_ABORT));
      const char* src = p.ws + WS_T + t_off(p, kseg(0, h)) + (WV - 4) * 4096 + lane * 16;
#pragma unroll
      for (int i = 0; i < 4; ++i) glds16_sc1(src + i * 1024, smem + L_KT + (WV - 4) * 4096 + i * 1024);
    }
  }
  const __amdgpu_buffer_rsrc_t rsy = mkrsrc(p.ws + WS_SY, SY_TOTAL);
  const u32x4 rs4 = raw_rsrc(p.ws + WS_SY, SY_TOTAL);
  const u32x4 rt4 = raw_rsrc(p.ws + WS_T, T_BYTES);
  gu32* abortw = (gu32*)(p.ws + WS_SY + SY_ABORT);
  const float cexp = 0.08838834764831845f * 1.4426950408889634f;
  constexpr int m = WV >> 2, b = WV & 3;
  int lead = LEAD0;
  unsigned tb1 = 0;
  unsigned long long fm = 0, ps = 0; (void)fm; (void)ps;
  for (int s = 0; s < NSEG; ++s) {
    int lane = tid & 63;
    asm volatile("" : "+v"(lane));
    const int r = lane & 15, q = lane >> 4;
    unsigned aL16 = lane * 16, aMW = aL16 + WV * 1024, aWL = aL16 + 98304 + WV * 4096;
    unsigned aIDX = (lane & 2) ? 0xEEEE : 0x4444;
    unsigned aQW = (32 * b + r) * 16 + q * 4 + (m ? L_KX : L_QX);
    u32x4 QF;
    asm volatile("" : "={v[232:235]}"(QF));
    if (WV < 4) {
      QF[0] = L_QX + (q * 8 + (r >> 2)) * 16 + (r & 1) * 8;
      QF[1] = ((2 * (q & 1) + (q >> 1)) * 16 + r) * 4;
      QF[2] = ((r >> 2) & 1) * 65536 + ((r >> 1) & 1) * 64 + (r >> 3) * 32 + (r & 1) * 16 + q * 4;
    } else QF[3] = r * 16 + q * 4;
    unsigned tout, spins, tgt;
    if (WV == 0) QK_STEP(QK_ASM_0); else if (WV == 1) QK_STEP(QK_ASM_1); else if (WV == 2) QK_STEP(QK_ASM_2);
    else if (WV == 3) QK_STEP(QK_ASM_3); else if (WV == 4) QK_STEP(QK_ASM_4); else if (WV == 5) QK_STEP(QK_ASM_5);
    else if (WV == 6) QK_STEP(QK_ASM_6); else QK_STEP(QK_ASM_7);
    tout = __builtin_amdgcn_readfirstlane(tout);
    tb1 = __builtin_amdgcn_readfirstlane(tb1);
    lane = tid & 63;
    asm volatile("" : "+v"(lane));
    if (WV < 4) {
      lds_at(smem + L_MISC + 8)[WV] = s + 1;
      if (WV == 3) lds_at(smem + L_MISC)[6] = time_of(tout);
    }
    if (WV >= 4 && s + 1 < NSEG) {
      unsigned t;
      do {
        __builtin_amdgcn_s_sleep(1);
        t = lds_at(smem + L_MISC + 8)[lane & 3];
      } while (!__all(t == (unsigned)(s + 1)));
      if (WV <= 5) {
        if (s + 2 < NSEG) {
          if (have < s + 1) have = wait_prog(rsy, fkq(h, (lane & 1) * 2), s + 1, abortw);
          const char* src = p.ws + WS_T + t_off(p, kseg(s + 1, h)) + (WV - 4) * 4096 + lane * 16;
#pragma unroll
          for (int i = 0; i < 4; ++i) glds16_sc1(src + i * 1024, smem + L_KT + (WV - 4) * 4096 + i * 1024);
          if (have < s + 2) have = look_prog(rsy, fkq(h, (lane & 1) * 2));
        }
      } else if (WV == 6) {
        watch(rsy, s, lane, now_q() + W_DELAY, (const unsigned*)(smem + L_MISC), abortw, side(pm, 1));
      } else {
        __builtin_amdgcn_s_sleep(6);
        const int x = (int)(__builtin_amdgcn_readfirstlane(lds_at(smem + L_MISC)[6]) - tb1);
        lead = __builtin_amdgcn_readfirstlane(track(lead, x, LEAD_UP, s < 8 ? LEAD_D0 : LEAD_DN, 60));
        SMP(2, s, x); SMP(3, s, lead);
      }
    }
  }
}

__device__ __forceinline__ void qk_role(Args& p, int h, char* smem) {
  switch (__builtin_amdgcn_readfirstlane(threadIdx.x >> 6)) {
    case 0: qk_wave<0>(p, h, smem); break;
    case 1: qk_wave<1>(p, h, smem); break;
    case 2: qk_wave<2>(p, h, smem); break;
    case 3: qk_wave<3>(p, h, smem); break;
    case 4: qk_wave<4>(p, h, smem); break;
    case 5: qk_wave<5>(p, h, smem); break;
    case 6: qk_wave<6>(p, h, smem); break;
    default: qk_wave<7>(p, h, smem); break;
  }
}

#define CLB32(a) CLB32_##a
#define CLB32_128 CLB8(128, 129, 130, 131, 132, 133, 134, 135), CLB8(136, 137, 138, 139, 140, 141, 142, 143), CLB8(144, 145, 146, 147, 148, 149, 150, 151), CLB8(152, 153, 154, 155, 156, 157, 158, 159)
#define CLB32_160 CLB8(160, 161, 162, 163, 164, 165, 166, 167), CLB8(168, 169, 170, 171, 172, 173, 174, 175), CLB8(176, 177, 178, 179, 180, 181, 182, 183), CLB8(184, 185, 186, 187, 188, 189, 190, 191)
#define W4_IN "{v[0:31]}"(W[0]), "{v[32:63]}"(W[1]), "{v[64:95]}"(W[2]), "{v[96:127]}"(W[3])
#define K_DUE 160
#define T_SL 2
#define O_DUE 400
#define LEAD_V 200

constexpr int V_VM = 16384, V_T = 32768, V_KI = 131072;
__device__ __forceinline__ void vu_role(Args& p, int h, char* smem) {
  const int tid = threadIdx.x, lane = tid & 63;
  const int w = __builtin_amdgcn_readfirstlane(tid >> 6);
  u32x32 W[4];
  {
    const unsigned vo = (tid >> 4) * (HD * 2) + 16 * ((lane & 15) ^ swzB((tid >> 4) & 15));
    auto stage = [&](int j) {
      const char* src = (const char*)(p.Wv + (size_t)j * 128 * HD + h * DH);
      const unsigned dst = lds_off(smem) + L_WL + (j & 3) * 32768 + w * 1024;
      glds2(src, 32 * HD * 2, vo, dst);
      glds2(src + 64 * HD * 2, 32 * HD * 2, vo, dst + 16384);
    };
    start_sy(p.ws, p.mem0, h, smem);
#pragma unroll
    for (int j = 0; j < 4; ++j) stage(j);
    Boot bt;
#pragma unroll
    for (int j = 0; j < 8; ++j) {
      if (j < 5) WAIT_V(12); else if (j == 5) WAIT_V(8); else if (j == 6) WAIT_V(4); else WAIT_V(0);
      if (j == 7 && w == 7) {
        const unsigned m = boot_done(p.ws, bt);
        if (lane < 8) lds_at(smem + L_MISC)[lane] = lane == 7 ? m : (lane == 1 ? now_q() : 0);
      }
      LBAR();
#pragma unroll
      for (int k = 0; k < 4; ++k) put4(W[j >> 1], (j & 1) * 4 + k, ld_bfrag(smem + L_WL + (j & 3) * 32768 + (k >> 1) * 16384, k & 1, w, lane));
      LBAR();
      if (j + 4 < 8) stage(j + 4);
      if (j == 5 && w == 7) boot_ask(p.ws, bt);
    }
    RDY(32 + h);
  }
  const __amdgpu_buffer_rsrc_t rsy = mkrsrc(p.ws + WS_SY, SY_TOTAL);
  const u32x4 rs4 = raw_rsrc(p.ws + WS_SY, SY_TOTAL);
  gu32* abortw = (gu32*)(p.ws + WS_SY + SY_ABORT);
  const unsigned pm = uni(lds_at(smem + L_MISC)[7]);
  p.s0 = pm >> 31;
  unsigned long long fm = 0; (void)fm;
  int have = -1;
  auto fetch = [&](int sg) {
    if (have < sg) have = wait_prog(rsy, fkq(h, w - 4), sg, abortw);
    const char* src = p.ws + WS_T + t_off(p, (w - 4) * T_Q + kseg(sg, h)) + lane * 16;
#pragma unroll
    for (int i = 0; i < 8; ++i) glds16_sc1(src + i * 1024, smem + V_T + (sg & 3) * 24576 + (w - 4) * 8192 + i * 1024);
  };
  const int fo = SY_FO + ((w & 3) * NH + h) * 128;
  const float cexp = 0.08838834764831845f * 1.4426950408889634f;
  typedef __attribute__((ext_vector_type(8))) float f32x8;
  f32x8 bias;
  u32x4 ta;
  {
    const int r = lane & 15, q = lane >> 4;
#pragma unroll
    for (int j = 0; j < 8; ++j) bias[j] = (j >> 2) * 16 + 4 * q + (j & 3) <= 16 * (w & 1) + r ? INFINITY : -INFINITY;
    ta[0] = V_KI + w * 2048 + lane * 16;
    ta[1] = V_KI + w * 2048 + (q * 8 + (r >> 2)) * 16 + (r & 1) * 8;
    ta[2] = V_VM + (w & 2) * 512 + ((2 * (q & 1) + (q >> 1)) * 16 + r) * 4;
    ta[3] = 0;
  }
#pragma unroll 1
  for (int s = 0; s < NSEG; ++s) {
    unsigned spins, tgt;
    asm volatile(VV_ASM
        : [spins] "=&s"(spins), [tgt] "=&s"(tgt)
        : W4_IN, "{v200}"(lane * 16), "{v201}"(lane * 16 + w * 2048), "{v202}"((lane & 2) ? 0xEEEE : 0x4444), "{v203}"(lane * 4),
          "{v204}"(tid * 4 + V_VM + (s & 1) * 2048), [rs] "s"(rs4), [som] "s"(o_mem(pm, s) + (w >> 2) * 16384 + (w & 3) * 2048), [step] "s"(s),
          [sent] "s"(DSENT), [sab] "s"(SY_ABORT), [sv] "s"(o_v(pm, s, h) + w * 256)
        : CLB32(128), CLB32(160), CLB8(192, 193, 194, 195, 196, 197, 198, 199), CLB_S);
    if (w < 4 || s == NSEG - 1) {
      const int sg = s - (w < 4);
      if (w >= 4) pace_until(now_q() + K_DUE);
      if (sg >= 0) {
        asm volatile(TK_ASM
            :
            : "{v200}"(lane * 16), "{v[208:215]}"(bias), "{v[216:219]}"(ta), "{v[220:223]}"((u32x4){0x3f803f80u, 0x3f803f80u, 0x3f803f80u, 0x3f803f80u}),
              [rs] "s"(rs4), [rt] "s"(raw_rsrc(p.ws + WS_T, T_BYTES)), [sent] "s"(DSENT), [sab] "s"(SY_ABORT), [sk] "s"(o_v(pm, sg, h) + 2048),
              [tl] "s"(V_T + (sg & 3) * 24576), [vp] "s"((sg & 1) * 2048), [tq] "s"((w & 1) * 4096), [tv] "s"((w & 2) * 2048),
              [so] "s"(t_off_o(p, T_O + (sg * 2 + (w & 1)) * 16 + (h >> 1)) + (h & 1) * 4096 + (w & 2) * 1024),
              [up] "s"(0xFFFFFFFF00000000ull), [c] "s"(cexp), [negc] "s"(-cexp), [od] "s"(s < NSEG - 1 ? O_DUE : 0)
            : CLB32(128), CLB32(160), CLB8(192, 193, 194, 195, 196, 197, 198, 199), CLB8(224, 225, 226, 227, 228, 229, 230, 231),
              "v205", "v206", "v207", "s88", "s89", CLB_S);
        if (sg >= NSEG - 3) {
          static_assert(TLAG == 2, "");
          WAIT_V(0);
          if (lane == 0) __builtin_amdgcn_raw_buffer_store_b32(1u, rsy, fo + (sg - NSEG + 3) * 4 * NH * 128, 0, 16);
        }
      }
    } else if (w == 7) {
      SMP(6, s, (int)(now_q() - tgt));
      watch(rsy, s, lane, now_q() + LEAD_V + W_DELAY, (const unsigned*)(smem + L_MISC), abortw, side(pm, 1));
    } else if (w >= 4) {
      __builtin_amdgcn_s_sleep(T_SL);
      const int s1 = w == 5 ? (s == NSEG - 2 ? s + 1 : s) : s + 1;
#pragma unroll 1
      for (int sg = (w == 5 || s == 0) ? s : s + 1; sg <= s1; ++sg) fetch(sg);
      if (have < s1 + 1 && s1 + 1 < NSEG) have = look_prog(rsy, fkq(h, w - 4));
      if (w == 5 && s + NPRE < NSEG) {
#pragma unroll
        for (int i = 0; i < 4; ++i) st_sc1(rsy, o_v(pm, s + NPRE, h) + i * 1024 + lane * 16, (u32x4){DSENT, DSENT, DSENT, DSENT});
      }
    }
  }
  if (h == 3 && (w & 2) == 0) { LST(351 + (w & 1) + (w >> 2) * 2); }
}

#define RL_UP 12
#define RL_DN 4
#define RED_MIN 40
#define BA_LOOK 310
constexpr int R_SI = 32768, R_PART = 98304;

__device__ __forceinline__ void red_role(Args& p, int j, char* smem) {
  const int tid = threadIdx.x;
  int lane = tid & 63;
  const int w = __builtin_amdgcn_readfirstlane(tid >> 6);
  const int c = (j & 2) * 8 + (j >> 2), rh = j & 1;
  const int hg = (w + (j >> 2)) & 7, rot = (j >> 2) + (j >> 5);
  const __amdgpu_buffer_rsrc_t rsy = mkrsrc(p.ws + WS_SY, SY_TOTAL);
  const u32x4 rs4 = raw_rsrc(p.ws + WS_SY, SY_TOTAL);
  const __amdgpu_buffer_rsrc_t rt = mkrsrc(p.ws + WS_T, T_BYTES);
  gu32* abortw = (gu32*)(p.ws + WS_SY + SY_ABORT);
  u32x32 W[4];
  u32x2 tag = {0u, 0u};
  {
    const u16* Wo = p.Wo + (size_t)(4 * hg * DH) * DM + 32 * c;
    const int ro = ((lane >> 4) * 8 + ((lane & 15) >> 2)) * 64 + (lane & 3) * 8;
    Boot bt;
    load_blocks<16>(smem + 32768 + w * 16384,
        [&](int i, char* slot) {
          if (i == 12 && w == 0) boot_ask(p.ws, bt);
          if ((i < 2 || i == 4 || i == 8) && (c & 14) != 6 && lane < 10) {
            const int x = i < 2 ? 3 - i : i >> 3, n = min(((c & 15) - ((c & 8) >> 2)) * 10 + lane, 127);
            touch_line((const char*)p.Wo + (size_t)(w * 512 + (x >> 1) * 256 + (x & 1) * 64 + (n & 64) + n) * (DM * 2) + (c & 16) * 64 + 384, smem + w * 256);
          }
          const int x = ((i >> 2) + rot) & 3;
          blk_dma(Wo + (size_t)((x >> 1) * 256 + (x & 1) * 64 + ((i >> 1) & 1) * 128 + (i & 1) * 32) * DM, DM, slot, lane);
        },
        [&](int i, char* slot) {
#pragma unroll
          for (int t = 0; t < 2; ++t) {
            s16x4 lo = __builtin_amdgcn_ds_read_tr16_b64_v4i16((lds_s16x4_ptr)(slot + ro + t * 32));
            s16x4 hi = __builtin_amdgcn_ds_read_tr16_b64_v4i16((lds_s16x4_ptr)(slot + ro + t * 32 + 256));
            put4(W[i >> 2], (i & 2) * 2 + 2 * t + (i & 1), (bf16x8){lo[0], lo[1], lo[2], lo[3], hi[0], hi[1], hi[2], hi[3]});
          }
        });
    if (w == 0) { lds_at(smem + 4128)[0] = boot_done(p.ws, bt); tag = next_tag(bt.e); }
    LBAR();
  }
  const unsigned pm = uni(lds_at(smem + 4128)[0]);
  p.s0 = pm >> 31;
  const int hs = side(pm, 1);
  if (((j >> 2) & 14) != 6 && GPF == 2) {
#pragma unroll
    for (int i = 0; i < 4; ++i)
      touch_line((const char*)(i < 2 ? p.wg : p.wu) + (size_t)(j - (j >= 32 ? 8 : 0)) * 131072 + ((i & 1) * 8 + w) * 8192 + lane * 128, smem + w * 256);
  }
  {
    const u32x4 ds = {DSENT, DSENT, DSENT, DSENT};
#pragma unroll
    for (int i = 0; i < 2; ++i) {
      const int s = (w * 2 + i) * 4 + (lane >> 4);
      if (s >= 2) st_sc1(rsy, o_mem(pm, s) + (c >> 4) * 16384 + (c & 15) * 512 + rh * 128 + ((lane >> 3) & 1) * 256 + (lane & 7) * 16, ds);
    }
  }
  if (tid < 4) { reinterpret_cast<unsigned*>(smem + 4096)[tid] = tid == 3 ? now_q() : 0; reinterpret_cast<unsigned*>(smem + 4112)[tid] = DSENT; }
  if (tid < 256) reinterpret_cast<int*>(smem + 5120)[tid] = ((tid >> 2) - TLAG - 6) >> 2;
  LBAR();
  const unsigned base = __builtin_amdgcn_readfirstlane(lds_at(smem + 4096)[3]);
  RDY(64 + j);
  unsigned long long fm = 0, ps = 0;
  const unsigned aL = lane * 16;
  const int e = ((56 + w) * SEG + rh * 16 + (lane >> 2)) * DM + c * 32 + (lane & 3) * 8;
  u32x4 xv = *reinterpret_cast<const u32x4*>(p.x + e), av = {DSENT, DSENT, DSENT, DSENT};
  asm volatile("" : "+v"(xv));
  {
    const int r = lane & 15, q = lane >> 4;
    const int sm0 = o_mem(pm, 0) + (c >> 4) * 16384 + (c & 15) * 512 + rh * 128;
    u32x4 VO, VT;
    unsigned fo = SY_FO + ((rh + (lane & 4) / 2) * NH + 4 * hg + (lane & 3)) * 128;
    unsigned ao = w == 1 ? (c * S_LEN + rh * 16 - (TLAG + 3) * SEG) * 64 + lane * 16
                         : (w >= 4 ? SY_O + NPRE * 131072 + side(pm, w - 2) + (j >> 3) * 16384 + (j & 7) * 1024 + lane * 16
                                   : SY_FS + (2 * c + rh) * 128 + (lane < 8 ? lane * 16 : 0x70000000));
    unsigned ar = w == 1 ? R_SI - (TLAG + 2) * 1024 + lane * 16 : (w == 2 ? 5120 : 4112);
    const unsigned long long am = w == 1 ? ((1ull << 56) - 1) << (TLAG + 3) : (w == 2 ? 0x11111111111111ull << (TLAG + 7) : (w >= 4 ? (0xFFFFull << (16 * (w - 4))) >> NPRE : 0));
    unsigned pw = R_PART + w * 2048 + lane * 16, pr = R_PART + 16384 + tid * 4;
    unsigned sw = R_SI - (TLAG + 1) * 1024 + (lane >> 2) * 64 + w * 8 + (lane & 3) * 2, stm = 8192 + w * 512;
#pragma unroll
    for (int l = 0; l < 4; ++l) {
      const int x = (l + rot) & 3;
      VO[l] = o_o(0, 0, 4 * hg) + rh * 65536 + x * 1024 + aL;
      VT[l] = (T_O + rh * 16 + 2 * hg + (x >> 1)) * 16384 + (x & 1) * 2048 + aL;
    }
    unsigned ba = lane < 4 ? SY_BA + hs + (4 * hg + lane) * 128 : 0x7F000000u;
    int rl = 280;
    unsigned step, E, so, st, st4, sh;
    asm volatile(RED_ASM
        : [step] "=&s"(step), [E] "=&s"(E), [so] "=&s"(so), [st] "=&s"(st), [st4] "=&s"(st4), [sh] "=&s"(sh),
          [rl] "+s"(rl), [fm] "+s"(fm), [ps] "+s"(ps),
          "+{v[224:227]}"(av), "+{v228}"(ao), "+{v229}"(ar), "+{v230}"(pw), "+{v231}"(pr), "+{v232}"(sw), "+{v233}"(stm), "+{v242}"(fo), "+{v173}"(ba)
        : W4_IN, "{v168}"(aL), "{v169}"(w * 512 + lane * 8), "{v170}"(lane * 8),
          "{v171}"(sm0 + 32768 + (q >> 1) * 256 + (q & 1) * 64 + ((r >> 1) & 1) * 32 + (r & 1) * 16 + (r >> 2) * 4),
          "{v172}"((lane & 2) ? 0xEEEE : 0x4444),
          "{v174}"((r & 1) ? 0x03020706u : 0x05040100u), "{v175}"(SY_BR + hs + j * 128 + lane * 8), "{v[234:237]}"(VO), "{v[238:241]}"(VT), "{v243}"(e * 2),
          [rg] "s"(raw_rsrc(p.ws + WS_G, S_LEN * DM * 2)), [fg] "s"(SY_FG + ((14 + (w >> 2)) * 8 + (c >> 2)) * 128),
          [rs] "s"(rs4), [ra] "s"(w == 1 ? raw_rsrc(p.ws + WS_S, S_LEN * DM * 2) : rs4), [rt] "s"(raw_rsrc(p.ws + WS_T, T_BYTES)),
          [ds] "n"(DS_RED), [mn] "n"(RED_MIN), [lk] "n"(DS_MEM + BA_LOOK), [up] "n"(RL_UP), [dn] "n"(-RL_DN), [sent] "s"(DSENT), [sab] "s"(SY_ABORT),
          [base] "s"(base), [pm] "s"(pm), [s0] "s"((p.s0 ^ TF_O) << 13), [am] "s"(am), [ainc] "s"(w == 1 ? SEG * 64 : (w >= 4 ? 131072 : 0)),
          [rinc] "s"(w == 1 ? 1024 : (w == 2 ? 16 : 0))
        : CLB32(128), CLB8(160, 161, 162, 163, 164, 165, 166, 167), CLB8(176, 177, 178, 179, 180, 181, 182, 183),
          CLB8(184, 185, 186, 187, 188, 189, 190, 191), CLB8(192, 193, 194, 195, 196, 197, 198, 199), CLB8(200, 201, 202, 203, 204, 205, 206, 207),
          CLB8(208, 209, 210, 211, 212, 213, 214, 215), CLB8(216, 217, 218, 219, 220, 221, 222, 223), CLB_E, "s96", CLB_S);
  }
  if (j == 0 && w == 5) { LST(345); }
  LBAR();
  if (j == 0 && w == 5) { LST(346); }
  u32x4 sv;
  if (w < 6 - TLAG) sv = *reinterpret_cast<const u32x4*>(smem + R_SI + (56 + w) * 1024 + lane * 16);
  else {
    const f32x4* part = reinterpret_cast<const f32x4*>(smem + (w == 6 - TLAG ? R_PART : (w - 6 + TLAG) * 16384)) +
                        (lane & 2) * 32 + (lane & 1) * 32 + (lane >> 2);
    f32x4 x[8][2];
#pragma unroll
    for (int ww = 0; ww < 8; ++ww) { x[ww][0] = part[ww * 128]; x[ww][1] = part[ww * 128 + 16]; }
#pragma unroll
    for (int u = 0; u < 2; ++u) {
      const f32x4 a = ((x[0][u] + x[1][u]) + (x[2][u] + x[3][u])) + ((x[4][u] + x[5][u]) + (x[6][u] + x[7][u]));
      sv[2 * u] = cvtpk(a[0], a[1]); sv[2 * u + 1] = cvtpk(a[2], a[3]);
    }
  }
  st_out(p, e, combine(sv, av, xv));
  if (j == 0 && tid == 0) st2_sc1(rsy, SY_EP, tag);
  if (tid == 128) { LST(400 + j); }
}

__global__ __launch_bounds__(512, 2) void main_kernel(Args p) {
  char* smem = g_smem;
  p.ws = ws_align(p.ws);
  const int bx = blockIdx.x & 7, bk = blockIdx.x >> 3;
  const bool isg = (bx ^ XPROBE) & 4;
  const int b = (isg ? NCHAIN : 0) + bk * 4 + (bx & 3);
  if (b == 64 && threadIdx.x == 0) { LST(500); }
  if (threadIdx.x < 64) {
    unsigned long long pc; unsigned len, keep;
    asm volatile("s_getpc_b64 %0\n.Lpc%=:\n\ts_mov_b32 %1, .Lcode_end-.Lpc%=" : "=s"(pc), "=s"(len));
    asm volatile("s_mov_b32 %0, m0\n\ts_mov_b32 m0, %3\n\ts_nop 0\n\tbuffer_load_dword %1, %2, 0 offen lds\n\ts_mov_b32 m0, %0"
                 : "=&s"(keep) : "v"((bk * 40 + threadIdx.x) * 128), "s"(raw_rsrc((const void*)pc, len)), "s"((b >> 6) == 1 ? 2048 : 163328) : "memory");
  }
  if (b >= NCHAIN) gemm_role(p, b - NCHAIN, smem);
  else if (b < 32) qk_role(p, (b & 3) * 8 + bk, smem);
  else if (b < 64) vu_role(p, (b & 3) * 8 + bk - 8, smem);
  else red_role(p, b - 64, smem);
}

extern "C" void kernel_launch(void* const* d_in, const int* in_sizes, int n_in,
                              void* d_out, int out_size, void* d_ws, size_t ws_size,
                              hipStream_t stream) {
  Args a;
  a.x = (const u16*)d_in[0]; a.mem0 = (const u16*)d_in[1];
  a.Wq = (const u16*)d_in[2]; a.Wk = (const u16*)d_in[3]; a.Wv = (const u16*)d_in[4]; a.Wo = (const u16*)d_in[5];
  a.wg = (const u16*)d_in[8]; a.wu = (const u16*)d_in[9]; a.wd = (const u16*)d_in[10];
  a.ws = (char*)d_ws; a.out = (u16*)d_out; a.s0 = 0; a.pad1 = 0;
  main_kernel<<<256, 512, 0, stream>>>(a);
}
```

```cpp
#include <hip/hip_runtime.h>
#include <hip/hip_bf16.h>
#include <stdint.h>
#include <type_traits>

constexpr int S_LEN = 2048, DM = 1024, NH = 32, DH = 128, NMEM = 8, SEG = 32;
constexpr int NSEG = S_LEN / SEG;
constexpr int HD = NH * DH;
constexpr int FF = 3584;

typedef unsigned short u16;
typedef __attribute__((ext_vector_type(8))) short bf16x8;
typedef __attribute__((ext_vector_type(4))) short s16x4;
typedef __attribute__((ext_vector_type(4))) float f32x4;
typedef __attribute__((ext_vector_type(4))) unsigned int u32x4;
typedef __attribute__((ext_vector_type(2))) unsigned int u32x2;
typedef __attribute__((address_space(3))) s16x4* lds_s16x4_ptr;
typedef __attribute__((address_space(3))) void* lds_void_ptr;
typedef __attribute__((address_space(1))) const void* glb_cvoid_ptr;
typedef __attribute__((address_space(1))) unsigned gu32;
typedef __bf16 bf16x2_t __attribute__((ext_vector_type(2)));
typedef float f32x2_t __attribute__((ext_vector_type(2)));

__device__ __forceinline__ float bf2f(u16 v) { return __uint_as_float(((unsigned)v) << 16); }
__device__ __forceinline__ unsigned cvtpk(float lo, float hi) {
  bf16x2_t r = __builtin_convertvector((f32x2_t){lo, hi}, bf16x2_t);
  return __builtin_bit_cast(unsigned, r);
}
__device__ __forceinline__ float lo_f(unsigned v) { return __uint_as_float(v << 16); }
__device__ __forceinline__ float hi_f(unsigned v) { return __uint_as_float(v & 0xffff0000u); }
__device__ __forceinline__ float rbf(float f) { return lo_f(cvtpk(f, f)); }
__device__ __forceinline__ bf16x8 as_bf16x8(u32x4 v) { return __builtin_bit_cast(bf16x8, v); }
#define MFMA(a, b, c) __builtin_amdgcn_mfma_f32_16x16x32_bf16((a), (b), (c), 0, 0, 0)
typedef __attribute__((ext_vector_type(16))) short bf16x16;
typedef __attribute__((ext_vector_type(8))) __bf16 bfv8;
typedef __attribute__((ext_vector_type(16))) __bf16 bfv16;
#define SMFMA(a, b, c, idx) __builtin_amdgcn_smfmac_f32_16x16x64_bf16(__builtin_bit_cast(bfv8, (a)), __builtin_bit_cast(bfv16, (b)), (c), (idx), 0, 0)
__device__ __forceinline__ bf16x16 cat16(bf16x8 lo, bf16x8 hi) {
  return __builtin_shufflevector(lo, hi, 0, 1, 2, 3, 4, 5, 6, 7, 8, 9, 10, 11, 12, 13, 14, 15);
}

#define KSTR 65
#define XLD 1024
constexpr int PAGE = 2 << 20, NPAGE = 14;
constexpr int T_K = 0, T_Q = 2304, T_V = 4608, T_O = 6912, T_END = T_O + 2048;
constexpr unsigned T_BYTES = T_END * 16384u;
constexpr size_t WS_SY = 0;
constexpr size_t WS_T = (size_t)NPAGE * PAGE;
constexpr size_t WS_XN = WS_T + T_BYTES;
constexpr size_t WS_HF = WS_XN + (size_t)S_LEN * XLD * 2;
constexpr size_t WS_G = WS_HF + (size_t)S_LEN * 16384;
constexpr size_t WS_S = WS_G + (size_t)S_LEN * DM * 2;
constexpr size_t WS_END = WS_S + (size_t)S_LEN * DM * 2;
constexpr unsigned SENT = 0xFFFFFFFFu;
constexpr unsigned DSENT = 0xFFBFFFBFu;
constexpr int SY_MEM = 0;
constexpr int SY_O = 2 * PAGE;
constexpr int SY_V = 6 * PAGE;
constexpr int SY_BR = PAGE, SY_BA = SY_BR + 16384;
constexpr int SY_FKQ = SY_BR + 32768;
constexpr int SY_FG = SY_FKQ + NH * 4 * 128;
constexpr int SY_FO = SY_FG + 16 * 8 * 128;
constexpr int SY_CH = SY_FO + 384 * 128;
constexpr int SY_ABORT = SY_CH + 8 * 128;
constexpr int SY_FS = SY_ABORT + 256;
constexpr int SY_FEND = SY_FS + 64 * 128;
constexpr int SY_EP = SY_FEND, SY_BT = SY_EP + 128;
constexpr int SY_TOTAL = NPAGE * PAGE;
constexpr int PROBE = PAGE - 16384 + 6144;
__device__ __forceinline__ int side(unsigned pm, int page) { return ((pm >> page) & 1u) << 13; }
__device__ __forceinline__ int o_mem(unsigned pm, int s) { return SY_MEM + s * 32768 + side(pm, 0); }
__device__ __forceinline__ int o_o(unsigned pm, int s, int h) {
  return SY_O + s * 131072 + (h >> 3) * 16384 + ((h >> 1) & 3) * 2048 + (h & 1) * 128 + side(pm, 2 + (s >> 4));
}
__device__ __forceinline__ int o_v(unsigned pm, int s, int h) { return SY_V + s * 262144 + (h >> 1) * 16384 + (h & 1) * 4096 + side(pm, 6 + (s >> 3)); }
__device__ __forceinline__ char* ws_align(char* ws) { return ws + ((PAGE - (unsigned)(uintptr_t)ws) & (PAGE - 1)); }
#define NFUSE 12
constexpr int NPRE = 4;
static_assert(WS_END + PAGE <= (size_t)256 << 20 && NH * KSTR <= T_Q && NPAGE % 2 == 0, "");
constexpr int NCHAIN = 128, NGW = 32;
__host__ __device__ constexpr int kseg(int sg, int h) { return h * KSTR + sg; }
__host__ __device__ constexpr int fkq(int h, int kind) { return SY_FKQ + (h * 4 + kind) * 128; }

#define HSKEW 0
#define XPROBE 0
struct Args {
  const u16 *x, *mem0, *Wq, *Wk, *Wv, *Wo, *wg, *wu, *wd;
  char* ws;
  u16* out;
  int s0, pad1;
};
constexpr int HLD = 16384 + HSKEW;
__device__ __forceinline__ int h_off(const Args& p, int rblk) { return rblk * PAGE + (((p.s0 ^ rblk ^ 1) & 1) << 13); }
static_assert(WS_HF % (2 * PAGE) == 0 && 127 * HSKEW + FF * 2 <= 8192, "");
#define TF_KQV 1
#define TF_O 0
__device__ __forceinline__ int t_off(const Args& p, int t) { return t * 16384 + (((p.s0 ^ (t >> 7) ^ (TF_KQV == 2 ? t : TF_KQV)) & 1) << 13); }
__device__ __forceinline__ int t_off_o(const Args& p, int t) { return t * 16384 + (((p.s0 ^ (t >> 7) ^ (TF_O == 2 ? t : TF_O)) & 1) << 13); }

__device__ __forceinline__ float wave_sum(float v) {
#pragma unroll
  for (int i = 0; i < 4; ++i) {
    const int x = (int)__float_as_uint(v);
    v += __uint_as_float(i == 0 ? __builtin_amdgcn_update_dpp(0, x, 0x128, 0xf, 0xf, false) : i == 1 ? __builtin_amdgcn_update_dpp(0, x, 0x124, 0xf, 0xf, false)
                         : i == 2 ? __builtin_amdgcn_update_dpp(0, x, 0x122, 0xf, 0xf, false) : __builtin_amdgcn_update_dpp(0, x, 0x121, 0xf, 0xf, false));
  }
  auto r = __builtin_amdgcn_permlane16_swap(__float_as_uint(v), __float_as_uint(v), false, false);
  v = __uint_as_float(r[0]) + __uint_as_float(r[1]);
  r = __builtin_amdgcn_permlane32_swap(__float_as_uint(v), __float_as_uint(v), false, false);
  return __uint_as_float(r[0]) + __uint_as_float(r[1]);
}

__shared__ __attribute__((aligned(16))) char g_smem[163840];
__device__ __forceinline__ unsigned lds_off(const void* p) {
  return (unsigned)(uintptr_t)(lds_void_ptr)g_smem + (unsigned)((const char*)p - g_smem);
}
__device__ __forceinline__ __amdgpu_buffer_rsrc_t mkrsrc(const void* p, unsigned bytes) {
  return __builtin_amdgcn_make_buffer_rsrc((void*)p, 0, bytes, 0x00020000);
}
__device__ __forceinline__ u32x4 ld_sc1(__amdgpu_buffer_rsrc_t rs, int off) {
  return __builtin_amdgcn_raw_buffer_load_b128(rs, off, 0, 16);
}
__device__ __forceinline__ void st_sc1(__amdgpu_buffer_rsrc_t rs, int off, u32x4 v) {
  __builtin_amdgcn_raw_buffer_store_b128(v, rs, off, 0, 16);
}
__device__ __forceinline__ unsigned ror8(unsigned v) {
  return (unsigned)__builtin_amdgcn_update_dpp(0, (int)v, 0x128, 0xf, 0xf, false);
}
__device__ __forceinline__ float ror8f(float v) { return __uint_as_float(ror8(__float_as_uint(v))); }
__device__ __forceinline__ void swap16(unsigned& x, unsigned& y) {
  auto r = __builtin_amdgcn_permlane16_swap(x, y, false, false);
  x = r[0]; y = r[1];
}
__device__ __forceinline__ void swap32(unsigned& x, unsigned& y) {
  auto r = __builtin_amdgcn_permlane32_swap(x, y, false, false);
  x = r[0]; y = r[1];
}
__device__ __forceinline__ f32x4 fold32(f32x4 x, f32x4 y) {
  f32x4 o;
#pragma unroll
  for (int j = 0; j < 4; ++j) {
    unsigned a = __float_as_uint(x[j]), b = __float_as_uint(y[j]);
    swap32(a, b);
    o[j] = __uint_as_float(a) + __uint_as_float(b);
  }
  return o;
}
__device__ __forceinline__ bf16x8 tr_frag(const char* img_lane) {
  s16x4 lo = __builtin_amdgcn_ds_read_tr16_b64_v4i16((lds_s16x4_ptr)img_lane);
  s16x4 hi = __builtin_amdgcn_ds_read_tr16_b64_v4i16((lds_s16x4_ptr)(img_lane + 64));
  return (bf16x8){lo[0], lo[1], lo[2], lo[3], hi[0], hi[1], hi[2], hi[3]};
}
__device__ __forceinline__ float rowq_max(float v) {
  unsigned a = __float_as_uint(v), b = a;
  swap16(a, b);
  v = __builtin_elementwise_maximum(__uint_as_float(a), __uint_as_float(b));
  a = __float_as_uint(v); b = a;
  swap32(a, b);
  return __builtin_elementwise_maximum(__uint_as_float(a), __uint_as_float(b));
}
__device__ __forceinline__ float rowq_sum(float v) {
  unsigned a = __float_as_uint(v), b = a;
  swap16(a, b);
  v = __uint_as_float(a) + __uint_as_float(b);
  a = __float_as_uint(v); b = a;
  swap32(a, b);
  return __uint_as_float(a) + __uint_as_float(b);
}
__device__ __forceinline__ bool spin_fail(unsigned& spins, gu32* abortw) {
  ++spins;
  if ((spins & 63u) == 0u) {
    const __amdgpu_buffer_rsrc_t ra = mkrsrc((const void*)abortw, 4);
    unsigned z = 0;
    asm volatile("" : "+v"(z));
    if (__builtin_amdgcn_readfirstlane(__builtin_amdgcn_raw_buffer_load_b32(ra, z, 0, 16)) == 1u) return true;
    if (spins > (1u << 19)) {
      __builtin_amdgcn_raw_buffer_store_b32(z + 1u, ra, z, 0, 16);
      return true;
    }
  }
  return false;
}
__device__ __forceinline__ unsigned umax4(u32x4 v) { return max(max(v[0], v[1]), max(v[2], v[3])); }
__device__ __forceinline__ bool has_ds(u32x4 v) { return (v[0] == DSENT) | (v[1] == DSENT) | (v[2] == DSENT) | (v[3] == DSENT); }
__device__ __forceinline__ float fmax2(float a, float b) { return __builtin_elementwise_maximum(a, b); }
__device__ __forceinline__ bf16x8 ldsf(const char* p) { return *reinterpret_cast<const bf16x8*>(p); }
#define SCHED() __builtin_amdgcn_sched_barrier(0)
#define LBAR() do { asm volatile("s_waitcnt lgkmcnt(0)" ::: "memory"); __builtin_amdgcn_s_barrier(); asm volatile("" ::: "memory"); } while (0)
#define WAIT_V(n) asm volatile("s_waitcnt vmcnt(%0)" :: "n"(n) : "memory")

__device__ __forceinline__ void wait_flags(__amdgpu_buffer_rsrc_t rs, int off, gu32* abortw) {
  for (unsigned spins = 0;;) {
    asm volatile("" ::: "memory");
    const unsigned v = __builtin_amdgcn_raw_buffer_load_b32(rs, off, 0, 16);
    if (__all(v != SENT)) break;
    __builtin_amdgcn_s_sleep(4);
    if (spin_fail(spins, abortw)) break;
  }
  asm volatile("" ::: "memory");
}

#define LST(i) do {} while (0)
#define RDY(i) do {} while (0)
#define RDW(i) do {} while (0)
#define SMP(k, s, v) do {} while (0)
#define LSTC(i) do {} while (0)
__device__ __forceinline__ unsigned offA(unsigned row, unsigned ch) {
  return row * 128u + 16u * (ch ^ (row & 7u));
}
__device__ __forceinline__ unsigned swzB(unsigned row) { return ((row & 3u) << 2) | ((row >> 2) & 3u); }
__device__ __forceinline__ unsigned offB(unsigned row, unsigned ch) {
  return 256u * row + 16u * (ch ^ swzB(row));
}
__device__ __forceinline__ void glds16(const u16* src, char* dst) {
  const unsigned d = __builtin_amdgcn_readfirstlane(lds_off(dst));
  unsigned keep;
  asm volatile("s_mov_b32 %0, m0\n\ts_mov_b32 m0, %2\n\ts_nop 0\n\tglobal_load_lds_dwordx4 %1, off\n\ts_mov_b32 m0, %0"
               : "=&s"(keep) : "v"(src), "s"(d) : "memory");
}
__device__ __forceinline__ bf16x8 ld_bfrag(const char* sb, int ks, int c, int lane) {
  const unsigned g = lane >> 4, qq = (lane & 15) >> 2, pp = lane & 3;
  s16x4 lo = __builtin_amdgcn_ds_read_tr16_b64_v4i16(
      (lds_s16x4_ptr)(sb + offB(32 * ks + 8 * g + qq, 2 * c + (pp >> 1)) + 8 * (pp & 1)));
  s16x4 hi = __builtin_amdgcn_ds_read_tr16_b64_v4i16(
      (lds_s16x4_ptr)(sb + offB(32 * ks + 8 * g + 4 + qq, 2 * c + (pp >> 1)) + 8 * (pp & 1)));
  return (bf16x8){lo[0], lo[1], lo[2], lo[3], hi[0], hi[1], hi[2], hi[3]};
}

__device__ __forceinline__ void blk_dma(const u16* src, size_t ld, char* slot, int lane) {
  const u16* s = src + (size_t)(lane >> 2) * ld + (lane & 3) * 8;
  glds16(s, slot);
  glds16(s + 16 * ld, slot + 1024);
}
__device__ __forceinline__ bf16x8 blk_frag(const char* slot, int T, int lane) {
  const char* a = slot + ((lane >> 4) * 8 + ((lane & 15) >> 2)) * 64 + (lane & 3) * 16 + T * 8;
  s16x4 lo = __builtin_amdgcn_ds_read_tr16_b64_v4i16((lds_s16x4_ptr)a);
  s16x4 hi = __builtin_amdgcn_ds_read_tr16_b64_v4i16((lds_s16x4_ptr)(a + 256));
  return (bf16x8){lo[0], lo[1], lo[2], lo[3], hi[0], hi[1], hi[2], hi[3]};
}
template <int NB, typename ISSUE, typename SINK>
__device__ __forceinline__ void load_blocks(char* slice, ISSUE&& issue, SINK&& sink) {
#pragma unroll
  for (int i = 0; i < 8; ++i) issue(i, slice + i * 2048);
#pragma unroll
  for (int g = 0; g < NB / 4; ++g) {
    if (g + 1 < NB / 4) WAIT_V(8); else WAIT_V(0);
#pragma unroll
    for (int j = 0; j < 4; ++j) sink(4 * g + j, slice + ((4 * g + j) & 7) * 2048);
    asm volatile("s_waitcnt lgkmcnt(0)" ::: "memory");
    if (g + 2 < NB / 4) {
#pragma unroll
      for (int j = 0; j < 4; ++j) issue(4 * g + 8 + j, slice + ((4 * g + j) & 7) * 2048);
    }
  }
}

__device__ __forceinline__ u32x4 widen32(unsigned a0, unsigned a1, unsigned b0, unsigned b1) {
  swap32(a0, b0); swap16(a0, b0);
  swap32(a1, b1); swap16(a1, b1);
  return (u32x4){a0, a1, b0, b1};
}

__device__ __forceinline__ void publish_flags(__amdgpu_buffer_rsrc_t rs, int off, int stride, int n) {
  WAIT_V(0);
  LBAR();
  int tid = threadIdx.x;
  asm volatile("" : "+v"(tid));
  if (tid < n) __builtin_amdgcn_raw_buffer_store_b32(1u, rs, off + tid * stride, 0, 16);
}

__device__ __forceinline__ void wait_s(const Args& p, __amdgpu_buffer_rsrc_t rsy, int rblk, int c0, int n, bool need) {
  if (need) {
    if (threadIdx.x < 64) {
      for (unsigned spins = 0;;) {
        asm volatile("" ::: "memory");
        const int f = (int)__builtin_amdgcn_raw_buffer_load_b32(rsy, SY_FS + (2 * c0 + (threadIdx.x & (2 * n - 1))) * 128, 0, 16);
        if (__all(f >= rblk)) break;
        __builtin_amdgcn_s_sleep(32);
        if (spin_fail(spins, (gu32*)(p.ws + WS_SY + SY_ABORT))) break;
      }
    }
    LBAR();
  }
}
__device__ __forceinline__ u32x4 combine(u32x4 sv, u32x4 g, u32x4 x) {
  u32x4 o;
#pragma unroll
  for (int j = 0; j < 4; ++j) {
    const unsigned t = cvtpk(lo_f(sv[j]) * lo_f(g[j]), hi_f(sv[j]) * hi_f(g[j]));
    o[j] = cvtpk(lo_f(x[j]) + lo_f(t), hi_f(x[j]) + hi_f(t));
  }
  return o;
}
#define OUT_AUX 16
__device__ __forceinline__ void st_out(const Args& p, int e, u32x4 v) {
  __builtin_amdgcn_raw_buffer_store_b128(v, mkrsrc(p.out, S_LEN * DM * 2), e * 2, 0, OUT_AUX);
}
struct SX { u32x4 s, x; };
__device__ __forceinline__ SX get_sx(const Args& p, int rblk, int e) {
  SX r; r.s = (u32x4){0u, 0u, 0u, 0u}; r.x = r.s;
  if (rblk < NFUSE) {
    r.s = ld_sc1(mkrsrc(p.ws + WS_S, S_LEN * DM * 2), (((e & 1023) >> 5) * S_LEN + (e >> 10)) * 64 + (e & 31) * 2);
    r.x = *reinterpret_cast<const u32x4*>(p.x + e);
  }
  return r;
}
__device__ __forceinline__ void put_g(const Args& p, __amdgpu_buffer_rsrc_t rg, int rblk, int e, u32x4 g, const SX& v) {
  if (rblk < NFUSE) {
    st_out(p, e, combine(v.s, g, v.x));
  } else st_sc1(rg, e * 2, g);
}

__device__ __forceinline__ void job_comb(const Args& p, int rblk, int ct, __amdgpu_buffer_rsrc_t rsy) {
  int tid = threadIdx.x;
  asm volatile("" : "+v"(tid));
  u32x4 xv[8];
#pragma unroll
  for (int i = 0; i < 8; ++i) xv[i] = *reinterpret_cast<const u32x4*>(p.x + (rblk * 128 + i * 16 + (tid >> 5)) * DM + ct * 256 + (tid & 31) * 8);
  if (tid < 64) wait_flags(rsy, SY_FG + (rblk * 8 + 2 * ct + (tid & 1)) * 128, (gu32*)(p.ws + WS_SY + SY_ABORT));
  wait_s(p, rsy, rblk, ct * 8, 8, true);
  u32x4 sv[8], gv[8];
#pragma unroll
  for (int i = 0; i < 8; ++i) {
    const int e = (rblk * 128 + i * 16 + (tid >> 5)) * DM + ct * 256 + (tid & 31) * 8;
    sv[i] = ld_sc1(mkrsrc(p.ws + WS_S, S_LEN * DM * 2), (((e & 1023) >> 5) * S_LEN + (e >> 10)) * 64 + (e & 31) * 2);
    gv[i] = ld_sc1(mkrsrc(p.ws + WS_G, S_LEN * DM * 2), e * 2);
  }
#pragma unroll
  for (int i = 0; i < 8; ++i)
    st_out(p, (rblk * 128 + i * 16 + (tid >> 5)) * DM + ct * 256 + (tid & 31) * 8, combine(sv[i], gv[i], xv[i]));
}

__device__ __forceinline__ u32x4 raw_rsrc(const void* p, unsigned bytes) {
  const unsigned long long a = (unsigned long long)p;
  return (u32x4){(unsigned)a, (unsigned)(a >> 32) & 0xffffu, bytes, 0x00020000u};
}
__device__ __forceinline__ u32x2 next_tag(u32x2 e) { return (u32x2){e[0] + 1u, e[1] + (e[0] == 0xFFFFFFFFu)}; }
struct Boot { u32x2 e; u32x4 c; };
__device__ __forceinline__ void boot_ask(const char* ws, Boot& r) {
  const unsigned o = (threadIdx.x & 63) * 128;
  asm volatile("buffer_load_dwordx2 %0, off, %3, %4 sc1\n\tbuffer_load_dwordx4 %1, %2, %3, %5 offen sc1"
               : "=&v"(r.e), "=&v"(r.c) : "v"(o), "s"(raw_rsrc(ws, SY_TOTAL)), "s"(SY_EP), "s"(SY_BT) : "memory");
}
__device__ __forceinline__ void wait_rows(const char* ws, u32x2 tag) {
  const __amdgpu_buffer_rsrc_t rs = mkrsrc(ws, SY_TOTAL);
  int o = (threadIdx.x & 63) * 128;
  asm volatile("" : "+v"(o));
#pragma unroll 1
  for (int i = 0; i < (1 << 17); ++i) {
    asm volatile("" ::: "memory");
    const u32x2 a = __builtin_amdgcn_raw_buffer_load_b64(rs, o, SY_BT + 32 * 128, 16), b = __builtin_amdgcn_raw_buffer_load_b64(rs, o, SY_BT + 96 * 128, 16);
    if (__all(a[0] == tag[0] && a[1] == tag[1] && b[0] == tag[0] && b[1] == tag[1])) break;
    __builtin_amdgcn_s_sleep(8);
  }
  asm volatile("" ::: "memory");
}
__device__ __forceinline__ unsigned boot_done(const char* ws, Boot& r, bool own = false, u32x2 tag = (u32x2){0u, 0u}) {
#pragma unroll 1
  for (int i = 0; i < (1 << 17); ++i) {
    asm volatile("" : "+v"(r.e), "+v"(r.c));
    if (!own) tag = next_tag(r.e);
    if (__all(r.c[0] == tag[0] && r.c[1] == tag[1])) break;
    __builtin_amdgcn_s_sleep(8);
    boot_ask(ws, r);
    WAIT_V(0);
  }
  unsigned q0[NPAGE], q1[NPAGE], ga = 0xFFFFu, gb = 0xFFFFu;
#pragma unroll
  for (int i = 0; i < NPAGE; ++i) {
    const unsigned x = __builtin_amdgcn_readlane(r.c[2], i), y = __builtin_amdgcn_readlane(r.c[2], i + NPAGE);
    q0[i] = min(x >> 16, y >> 16); q1[i] = min(x & 0xFFFFu, y & 0xFFFFu);
    ga = min(ga, (i & 1) ? q1[i] : q0[i]); gb = min(gb, (i & 1) ? q0[i] : q1[i]);
  }
  unsigned m = gb < ga ? 0x80001555u : 0x2AAAu;
  const unsigned thr = min(ga, gb) + 110;
#pragma unroll
  for (int i = 0; i < NPAGE; ++i) {
    const unsigned lo = min(q0[i], q1[i]), hi = max(q0[i], q1[i]);
    const unsigned keep = (lo < thr ? hi : 0u) > lo + 100 ? ~(1u << i) : ~0u;
    m = (m & keep) | ((q1[i] < q0[i] ? ~0u : 0u) & ~keep);
  }
  return m;
}
typedef __attribute__((address_space(3))) volatile unsigned* lds_vu_ptr;
__device__ __forceinline__ lds_vu_ptr lds_at(const void* p) {
  unsigned a = lds_off(p);
  asm volatile("" : "+v"(a));
  return (lds_vu_ptr)(uintptr_t)a;
}
__device__ __forceinline__ unsigned uni(unsigned v) {
  unsigned r;
  asm volatile("s_nop 1\n\tv_readfirstlane_b32 %0, %1\n\ts_nop 3" : "=s"(r) : "v"(v));
  return r;
}
__device__ __forceinline__ void start_sy(char* ws, const u16* mem0, int i, char* smem) {
  const int tid = threadIdx.x, lane = tid & 63, w = __builtin_amdgcn_readfirstlane(tid >> 6);
  const __amdgpu_buffer_rsrc_t rsy = mkrsrc(ws, SY_TOTAL);
  const __amdgpu_buffer_rsrc_t rs = mkrsrc(ws + (size_t)(i % NPAGE) * PAGE + PROBE - (i / NPAGE) * 2048, 16384);
  const u32x2 e = __builtin_amdgcn_raw_buffer_load_b64(rsy, 0, SY_EP, 16);
  const u32x4 ds = {DSENT, DSENT, DSENT, DSENT};
  if (lane < 32) st_sc1(rs, (lane >> 4) * 8192 + w * 256 + (lane & 15) * 16, ds);
  static_assert(NPRE == 4 && SY_FEND - SY_BR <= 32 * 6144, "");
#pragma unroll
  for (int u = 0; u < 6; ++u)
    st_sc1(rsy, (u < 4 ? SY_V + (i * 4 + u) * 8192 : SY_O + (i * 2 + u - 4) * 8192) + tid * 16, ds);
  if (w == 7) {
    const int n = i * 64 + lane, j = (n >> 10) * 512 + (n & 511);
    const unsigned* m0 = reinterpret_cast<const unsigned*>(mem0 + (2 * ((j >> 2) & 3) + (j & 1)) * DM + (j >> 4) * 16) + ((j >> 1) & 1);
    u32x4 m;
#pragma unroll
    for (int k = 0; k < 4; ++k) { const unsigned t = m0[2 * k]; m[k] = t == DSENT ? 0xFFFFFFFFu : t; }
    st_sc1(rsy, n * 16, m);
  } else if (w == 6) st_sc1(rsy, 32768 + i * 1024 + lane * 16, ds);
  else {
    const int o = SY_BR + i * 6144 + tid * 16;
    st_sc1(rsy, o < SY_FEND ? o : 0x7F000000, (u32x4){SENT, SENT, SENT, SENT});
  }
  unsigned t[4];
#pragma unroll
  for (int k = 0; k < 4; ++k) {
    unsigned long long c0, c1; unsigned v, off = ((k ^ (k >> 1)) & 1) * 8192 + w * 256 + (k >> 1) * 128;
    asm volatile("s_waitcnt vmcnt(0)\n\ts_barrier\n\ts_memtime %0\n\ts_waitcnt lgkmcnt(0)\n\tbuffer_load_dword %2, %3, %4, 0 offen sc1\n\ts_waitcnt vmcnt(0)\n\t"
                 "s_memtime %1\n\ts_waitcnt lgkmcnt(0)" : "=&s"(c0), "=&s"(c1), "=&v"(v) : "v"(off), "s"(rs) : "memory");
    t[k] = (unsigned)(c1 - c0);
  }
  lds_vu_ptr tm = lds_at(smem);
  if (lane == 0) { tm[w] = min(t[0], t[3]); tm[8 + w] = min(t[1], t[2]); }
  int ro = SY_BT + i * 128 + lane * 16;
  asm volatile("" : "+v"(ro));
  LBAR();
  RDY(384 + i);
  if (threadIdx.x < 8) {
    unsigned m0 = tm[0], m1 = tm[8];
#pragma unroll
    for (int k = 1; k < 8; ++k) { m0 = min(m0, tm[k]); m1 = min(m1, tm[8 + k]); }
    const u32x2 tag = next_tag(e);
    st_sc1(rsy, ro, (u32x4){tag[0], tag[1], min(m0, 30000u) << 16 | min(m1, 30000u), 0u});
  }
}
template <int NB, typename F>
__device__ __forceinline__ u32x2 start_rows(const Args& p, int g, F&& between) {
  const int tid = threadIdx.x, lane = tid & 63, row0 = g * 16 + (tid >> 6) * 2;
  u32x4 a[4];
  u32x2 e;
  asm volatile("global_load_dwordx4 %0, %5, off\n\tglobal_load_dwordx4 %1, %5, off offset:1024\n\tglobal_load_dwordx4 %2, %5, off offset:2048\n\t"
               "global_load_dwordx4 %3, %5, off offset:3072\n\tbuffer_load_dwordx2 %4, off, %6, %7 sc1"
               : "=&v"(a[0]), "=&v"(a[1]), "=&v"(a[2]), "=&v"(a[3]), "=&v"(e)
               : "v"(p.x + (size_t)row0 * DM + lane * 8), "s"(raw_rsrc(p.ws, SY_TOTAL)), "s"(SY_EP) : "memory");
  between();
  asm volatile("s_waitcnt vmcnt(%5)" : "+v"(a[0]), "+v"(a[1]), "+v"(a[2]), "+v"(a[3]), "+v"(e) : "n"(NB) : "memory");
  RDY(512 + g);
  const __amdgpu_buffer_rsrc_t ro = mkrsrc(p.ws + WS_XN, S_LEN * XLD * 2);
#pragma unroll
  for (int u = 0; u < 2; ++u) {
    float v[16];
#pragma unroll
    for (int i = 0; i < 8; ++i) { v[2 * i] = lo_f(a[2 * u + (i >> 2)][i & 3]); v[2 * i + 1] = hi_f(a[2 * u + (i >> 2)][i & 3]); }
    float s = 0.f;
#pragma unroll
    for (int i = 0; i < 16; ++i) s += v[i];
    const float mean = wave_sum(s) * (1.0f / DM);
    float q = 0.f;
#pragma unroll
    for (int i = 0; i < 16; ++i) { float d = v[i] - mean; q += d * d; }
    const float var = rbf(wave_sum(q) * (1.0f / DM));
    const float mb = rbf(mean);
    const float inv = 1.0f / rbf(sqrtf(rbf(var + 1e-5f)));
#pragma unroll
    for (int k = 0; k < 2; ++k) {
      u32x4 o;
#pragma unroll
      for (int i = 0; i < 4; ++i) {
        const unsigned t = cvtpk(v[8 * k + 2 * i] - mb, v[8 * k + 2 * i + 1] - mb);
        o[i] = cvtpk(lo_f(t) * inv, hi_f(t) * inv);
      }
      st_sc1(ro, (row0 + u) * (XLD * 2) + k * 1024 + lane * 16, o);
    }
  }
  return next_tag(e);
}
__device__ __forceinline__ void report_rows(const Args& p, int g, u32x2 tag) {
  RDY(256 + g);
  if (threadIdx.x < 8) st_sc1(mkrsrc(p.ws + WS_SY, SY_TOTAL), SY_BT + (32 + g) * 128 + threadIdx.x * 16, (u32x4){tag[0], tag[1], tag[0], tag[1]});
}

typedef __attribute__((ext_vector_type(32))) unsigned u32x32;
__device__ __forceinline__ void put4(u32x32& W, int i, bf16x8 f) {
  const u32x4 v = __builtin_bit_cast(u32x4, f);
#pragma unroll
  for (int j = 0; j < 4; ++j) W[4 * i + j] = v[j];
}
#define CLB8(a, b, c, d, e, f, g, h) "v" #a, "v" #b, "v" #c, "v" #d, "v" #e, "v" #f, "v" #g, "v" #h
#define CLB32(a) CLB32_##a
#define CLB32_128 CLB8(128, 129, 130, 131, 132, 133, 134, 135), CLB8(136, 137, 138, 139, 140, 141, 142, 143), CLB8(144, 145, 146, 147, 148, 149, 150, 151), CLB8(152, 153, 154, 155, 156, 157, 158, 159)
#define CLB32_160 CLB8(160, 161, 162, 163, 164, 165, 166, 167), CLB8(168, 169, 170, 171, 172, 173, 174, 175), CLB8(176, 177, 178, 179, 180, 181, 182, 183), CLB8(184, 185, 186, 187, 188, 189, 190, 191)
#define W4_IN "{v[0:31]}"(W[0]), "{v[32:63]}"(W[1]), "{v[64:95]}"(W[2]), "{v[96:127]}"(W[3])
#define CLB32_192 CLB8(192, 193, 194, 195, 196, 197, 198, 199), CLB8(200, 201, 202, 203, 204, 205, 206, 207), CLB8(208, 209, 210, 211, 212, 213, 214, 215), CLB8(216, 217, 218, 219, 220, 221, 222, 223)
__device__ __forceinline__ unsigned ld_flag0(__amdgpu_buffer_rsrc_t rs, int off) {
  return __builtin_amdgcn_raw_buffer_load_b32(rs, off, 0, 16);
}
__device__ __forceinline__ void glds16_sc1(const char* src, char* dst) {
  const unsigned d = __builtin_amdgcn_readfirstlane(lds_off(dst));
  unsigned keep;
  asm volatile("s_mov_b32 %0, m0\n\ts_mov_b32 m0, %2\n\ts_nop 0\n\tglobal_load_lds_dwordx4 %1, off sc1\n\ts_mov_b32 m0, %0"
               : "=&s"(keep) : "v"(src), "s"(d) : "memory");
}

__device__ __forceinline__ void touch_line(const char* src, char* dst) {
  const unsigned d = __builtin_amdgcn_readfirstlane(lds_off(dst));
  unsigned keep;
  asm volatile("s_mov_b32 %0, m0\n\ts_mov_b32 m0, %2\n\ts_nop 0\n\tglobal_load_lds_dword %1, off sc1\n\ts_mov_b32 m0, %0"
               : "=&s"(keep) : "v"(src), "s"(d) : "memory");
}

#define GPF 1
#define GU_DELAY 0
#define Q_DELAY 0
#define PF_T0 100
#define PF_N 12
#define A_AUX " sc1"
#define WREV 1
template <bool DEV = false>
__device__ __forceinline__ void glds2(const char* base, int step, unsigned voff, unsigned dst) {
  unsigned keep;
  const char* base2 = base + step;
  if (DEV)
    asm volatile("s_mov_b32 %0, m0\n\ts_mov_b32 m0, %4\n\ts_nop 0\n\tglobal_load_lds_dwordx4 %1, %2 sc1\n\t"
                 "s_add_u32 m0, m0, 0x2000\n\ts_nop 0\n\tglobal_load_lds_dwordx4 %1, %3 sc1\n\ts_mov_b32 m0, %0"
                 : "=&s"(keep) : "v"(voff), "s"(base), "s"(base2), "s"(dst) : "memory", "scc");
  else
  asm volatile("s_mov_b32 %0, m0\n\ts_mov_b32 m0, %4\n\ts_nop 0\n\tglobal_load_lds_dwordx4 %1, %2\n\t"
               "s_add_u32 m0, m0, 0x2000\n\ts_nop 0\n\tglobal_load_lds_dwordx4 %1, %3\n\ts_mov_b32 m0, %0"
               : "=&s"(keep) : "v"(voff), "s"(base), "s"(base2), "s"(dst) : "memory", "scc");
}

__device__ __forceinline__ void gemm_stream(const char* A, int lda, const char* B, int ldb, int nk, char* smem, f32x4 (&acc)[2][4]) {
  int tid = threadIdx.x;
  asm volatile("" : "+v"(tid));
  const int lane = tid & 63, r = lane & 15, q = lane >> 4;
  const int wid = __builtin_amdgcn_readfirstlane(tid >> 6);
  const int wr = wid >> 1, wc = wid & 1;
  const unsigned va = (tid >> 3) * lda + 16 * ((tid & 7) ^ ((tid >> 3) & 7)), vb = (tid >> 4) * ldb + 16 * ((tid & 15) ^ swzB((tid >> 4) & 15));
  const unsigned lw = lds_off(smem) + wid * 1024;
  auto piece = [&](int kt, int i) {
    const int k = min(kt, nk - 1);
    const char* src = i < 2 ? A + k * 128 + i * 64 * lda : B + (size_t)(k * 64 + (i - 2) * 32) * ldb;
    unsigned keep;
    if (i < 2)
      asm volatile("s_mov_b32 %0, m0\n\ts_mov_b32 m0, %3\n\ts_nop 0\n\tglobal_load_lds_dwordx4 %1, %2" A_AUX "\n\ts_mov_b32 m0, %0"
                   : "=&s"(keep) : "v"(va), "s"(src), "s"(lw + (kt & 3) * 32768 + i * 8192) : "memory");
    else
      asm volatile("s_mov_b32 %0, m0\n\ts_mov_b32 m0, %3\n\ts_nop 0\n\tglobal_load_lds_dwordx4 %1, %2\n\ts_mov_b32 m0, %0"
                   : "=&s"(keep) : "v"(vb), "s"(src), "s"(lw + (kt & 3) * 32768 + i * 8192) : "memory");
  };
  unsigned la[2], lb[4][2];
#pragma unroll
  for (int k = 0; k < 2; ++k) { la[k] = lds_off(smem) + offA(wr * 32 + r, k * 4 + q); asm volatile("" : "+v"(la[k])); }
#pragma unroll
  for (int n = 0; n < 4; ++n)
#pragma unroll
    for (int u = 0; u < 2; ++u) {
      lb[n][u] = lds_off(smem) + 16384 + offB(8 * q + 4 * u + (r >> 2), 2 * (wc * 4 + n) + ((r & 3) >> 1)) + 8 * (r & 1);
      asm volatile("" : "+v"(lb[n][u]));
    }
  typedef __attribute__((address_space(3))) const bf16x8* lds_frag_ptr;
  bf16x8 af[2][2], bf[2][4];
  auto ldA = [&](int bo, int ks, int m) { return *(lds_frag_ptr)(uintptr_t)(la[ks] + bo + m * 2048); };
  auto ldB = [&](int bo, int ks, int n) {
    s16x4 lo = __builtin_amdgcn_ds_read_tr16_b64_v4i16((lds_s16x4_ptr)(uintptr_t)(lb[n][0] + bo + ks * 8192));
    s16x4 hi = __builtin_amdgcn_ds_read_tr16_b64_v4i16((lds_s16x4_ptr)(uintptr_t)(lb[n][1] + bo + ks * 8192));
    return (bf16x8){lo[0], lo[1], lo[2], lo[3], hi[0], hi[1], hi[2], hi[3]};
  };
#pragma unroll
  for (int i = 0; i < 8; ++i) acc[i >> 2][i & 3] = (f32x4){0.f, 0.f, 0.f, 0.f};
  LBAR();
#pragma unroll
  for (int i = 0; i < 10; ++i) piece(i >> 2, i & 3);
  WAIT_V(6);
  LBAR();
#pragma unroll
  for (int m = 0; m < 2; ++m) af[0][m] = ldA(0, 0, m);
#pragma unroll
  for (int n = 0; n < 4; ++n) bf[0][n] = ldB(0, 0, n);
  auto loop = [&](auto HF) {
    for (int t0 = 0; t0 < nk; t0 += 4) {
#pragma unroll
      for (int b = 0; b < 4; ++b) {
        const int t = t0 + b;
#pragma unroll
        for (int ks = 0; ks < 2; ++ks) {
          if (ks == 1) {
            WAIT_V(4);
            __builtin_amdgcn_s_barrier();
          }
          const int nb = (ks ? (b + 1) & 3 : b) * 32768, nks = ks ^ 1;
#pragma unroll
          for (int n = 0; n < 4; ++n) {
            acc[0][n] = MFMA(bf[ks][n], af[ks][0], acc[0][n]);
            bf[nks][n] = ldB(nb, nks, n);
            acc[1][n] = MFMA(bf[ks][n], af[ks][1], acc[1][n]);
            if (n < 2) af[nks][n] = ldA(nb, nks, n);
            SCHED();
            if ((n & 1) == HF) { piece(t + 2 + ks, (n >> 1) + 2 * nks); SCHED(); }
          }
        }
      }
    }
  };
  if (wid >> 2) loop(std::integral_constant<int, 1>()); else loop(std::integral_constant<int, 0>());
  WAIT_V(0);
}

#define HSL 16
__device__ __forceinline__ void job_down(const Args& p, int rblk, int ct, char* smem, __amdgpu_buffer_rsrc_t rsy) {
  if (threadIdx.x < 64) {
    for (unsigned spins = 0;;) {
      asm volatile("" ::: "memory");
      if (__builtin_amdgcn_readfirstlane((int)ld_flag0(rsy, SY_CH + (rblk >> 1) * 128)) >= 27) break;
      __builtin_amdgcn_s_sleep(HSL);
      if (spin_fail(spins, (gu32*)(p.ws + WS_SY + SY_ABORT))) break;
    }
  }
  f32x4 acc[2][4];
  gemm_stream(p.ws + WS_HF + h_off(p, rblk), HLD, (const char*)(p.wd + ct * 128), DM * 2, FF / 64, smem, acc);
  wait_s(p, rsy, rblk, ct * 4, 4, rblk < NFUSE);
  int tid = threadIdx.x;
  asm volatile("" : "+v"(tid));
  const int lane = tid & 63, wid = tid >> 6, wr = wid >> 1, wc = wid & 1;
  SX sx[4];
#pragma unroll
  for (int i = 0; i < 4; ++i)
    sx[i] = get_sx(p, rblk, (rblk * 128 + wr * 32 + (i >> 1) * 16 + (lane & 15)) * DM + ct * 128 + wc * 64 + (i & 1) * 32 + (lane >> 4) * 8);
  const __amdgpu_buffer_rsrc_t ro = mkrsrc(p.ws + WS_G, S_LEN * DM * 2);
#pragma unroll
  for (int m = 0; m < 2; ++m) {
    const int row = rblk * 128 + wr * 32 + m * 16 + (lane & 15);
#pragma unroll
    for (int nb = 0; nb < 2; ++nb) {
      unsigned pk[2][2];
#pragma unroll
      for (int n = 0; n < 2; ++n) {
        float rr[4];
#pragma unroll
        for (int j = 0; j < 4; ++j) { float d = rbf(acc[m][2 * nb + n][j]); rr[j] = d * __builtin_amdgcn_rcpf(1.0f + __expf(-d)); }
        pk[n][0] = cvtpk(rr[0], rr[1]); pk[n][1] = cvtpk(rr[2], rr[3]);
      }
      u32x4 o = widen32(pk[0][0], pk[0][1], pk[1][0], pk[1][1]);
      put_g(p, ro, rblk, row * DM + ct * 128 + wc * 64 + nb * 32 + (lane >> 4) * 8, o, sx[2 * m + nb]);
    }
  }
  if (rblk >= NFUSE) publish_flags(rsy, SY_FG + (rblk * 8 + ct) * 128, 0, 1);
}

#define PR_ASM_0 \
  "s_cmp_lg_u32 %[kh], 0\n\t" \
  "s_cbranch_scc1 .Lk%=\n\t" \
  "s_mov_b32 s94, m0\n\t" \
  "s_mov_b64 s[92:93], %[x0]\n\t" \
  "s_mov_b32 s86, %[g0]\n\t" \
  "s_add_u32 s87, s86, %[t0]\n\t" \
  "s_sub_i32 s88, s86, 3\n\t" \
  "ds_read_b128 v[176:179], v208 offset:0\n\t" \
  "ds_read_b128 v[180:183], v208 offset:16384\n\t" \
  "ds_read_b128 v[184:187], v209 offset:0\n\t" \
  "ds_read_b128 v[188:191], v209 offset:16384\n\t" \
  "ds_read_b128 v[192:195], v210 offset:0\n\t" \
  "ds_read_b128 v[196:199], v210 offset:16384\n\t" \
  "ds_read_b128 v[200:203], v211 offset:0\n\t" \
  "ds_read_b128 v[204:207], v211 offset:16384\n\t" \
  ".Lp0%=:\n\t" \
  "s_waitcnt lgkmcnt(6)\n\t" \
  "v_mfma_f32_16x16x32_bf16 v[128:131], v[0:3], v[176:179], 0\n\t" \
  "v_mfma_f32_16x16x32_bf16 v[132:135], v[4:7], v[176:179], 0\n\t" \
  "ds_read_b128 v[176:179], v208 offset:256\n\t" \
  "s_add_u32 m0, %[lw], 67584\n\t" \
  "v_mfma_f32_16x16x32_bf16 v[136:139], v[0:3], v[180:183], 0\n\t" \
  "v_mfma_f32_16x16x32_bf16 v[140:143], v[4:7], v[180:183], 0\n\t" \
  "ds_read_b128 v[180:183], v208 offset:16640\n\t" \
  "global_load_lds_dwordx4 v218, s[92:93] sc1\n\t" \
  "s_waitcnt lgkmcnt(6)\n\t" \
  "v_mfma_f32_16x16x32_bf16 v[128:131], v[8:11], v[184:187], v[128:131]\n\t" \
  "v_mfma_f32_16x16x32_bf16 v[132:135], v[12:15], v[184:187], v[132:135]\n\t" \
  "ds_read_b128 v[184:187], v209 offset:256\n\t" \
  "v_mfma_f32_16x16x32_bf16 v[136:139], v[8:11], v[188:191], v[136:139]\n\t" \
  "v_mfma_f32_16x16x32_bf16 v[140:143], v[12:15], v[188:191], v[140:143]\n\t" \
  "ds_read_b128 v[188:191], v209 offset:16640\n\t" \
  "ds_write_b128 v220, v[152:155] offset:0\n\t" \
  "ds_write_b128 v220, v[156:159] offset:1024\n\t" \
  "s_waitcnt lgkmcnt(8)\n\t" \
  "v_mfma_f32_16x16x32_bf16 v[128:131], v[16:19], v[192:195], v[128:131]\n\t" \
  "v_mfma_f32_16x16x32_bf16 v[132:135], v[20:23], v[192:195], v[132:135]\n\t" \
  "ds_read_b128 v[192:195], v210 offset:256\n\t" \
  "s_add_u32 m0, %[lw], 68608\n\t" \
  "v_mfma_f32_16x16x32_bf16 v[136:139], v[16:19], v[196:199], v[136:139]\n\t" \
  "v_mfma_f32_16x16x32_bf16 v[140:143], v[20:23], v[196:199], v[140:143]\n\t" \
  "ds_read_b128 v[196:199], v210 offset:16640\n\t" \
  "global_load_lds_dwordx4 v219, s[92:93] sc1\n\t" \
  "s_lshr_b32 s90, s87, 7\n\t" \
  "s_xor_b32 s90, s90, %[s0]\n\t" \
  "s_and_b32 s90, s90, 1\n\t" \
  "s_lshl1_add_u32 s90, s87, s90\n\t" \
  "s_lshl_b32 s90, s90, 13\n\t" \
  "s_cmp_lt_i32 s86, 3\n\t" \
  "s_cselect_b32 s90, 0x70000000, s90\n\t" \
  "s_waitcnt lgkmcnt(8)\n\t" \
  "v_mfma_f32_16x16x32_bf16 v[128:131], v[24:27], v[200:203], v[128:131]\n\t" \
  "v_mfma_f32_16x16x32_bf16 v[132:135], v[28:31], v[200:203], v[132:135]\n\t" \
  "ds_read_b128 v[200:203], v211 offset:256\n\t" \
  "v_mfma_f32_16x16x32_bf16 v[136:139], v[24:27], v[204:207], v[136:139]\n\t" \
  "v_mfma_f32_16x16x32_bf16 v[140:143], v[28:31], v[204:207], v[140:143]\n\t" \
  "ds_read_b128 v[204:207], v211 offset:16640\n\t" \
  "s_waitcnt lgkmcnt(4)\n\t" \
  "s_waitcnt vmcnt(4)\n\t" \
  "s_barrier\n\t" \
  ".Lg00%=:\n\t" \
  "s_add_u32 s92, s92, 512\n\t" \
  "s_addc_u32 s93, s93, 0\n\t" \
  "v_mfma_f32_16x16x32_bf16 v[128:131], v[32:35], v[176:179], v[128:131]\n\t" \
  "v_mfma_f32_16x16x32_bf16 v[132:135], v[36:39], v[176:179], v[132:135]\n\t" \
  "ds_read_b128 v[176:179], v208 offset:32768\n\t" \
  "s_add_u32 m0, %[lw], 98304\n\t" \
  "v_mfma_f32_16x16x32_bf16 v[136:139], v[32:35], v[180:183], v[136:139]\n\t" \
  "v_mfma_f32_16x16x32_bf16 v[140:143], v[36:39], v[180:183], v[140:143]\n\t" \
  "ds_read_b128 v[180:183], v208 offset:49152\n\t" \
  "global_load_lds_dwordx4 v216, s[92:93] sc1\n\t" \
  "ds_read_b128 v[160:163], v221 offset:0\n\t" \
  "ds_read_b128 v[164:167], v221 offset:1024\n\t" \
  "v_mfma_f32_16x16x32_bf16 v[128:131], v[40:43], v[184:187], v[128:131]\n\t" \
  "v_mfma_f32_16x16x32_bf16 v[132:135], v[44:47], v[184:187], v[132:135]\n\t" \
  "ds_read_b128 v[184:187], v209 offset:32768\n\t" \
  "v_mfma_f32_16x16x32_bf16 v[136:139], v[40:43], v[188:191], v[136:139]\n\t" \
  "v_mfma_f32_16x16x32_bf16 v[140:143], v[44:47], v[188:191], v[140:143]\n\t" \
  "ds_read_b128 v[188:191], v209 offset:49152\n\t" \
  "s_waitcnt lgkmcnt(8)\n\t" \
  "v_mfma_f32_16x16x32_bf16 v[128:131], v[48:51], v[192:195], v[128:131]\n\t" \
  "v_mfma_f32_16x16x32_bf16 v[132:135], v[52:55], v[192:195], v[132:135]\n\t" \
  "ds_read_b128 v[192:195], v210 offset:32768\n\t" \
  "s_add_u32 m0, %[lw], 99328\n\t" \
  "v_mfma_f32_16x16x32_bf16 v[136:139], v[48:51], v[196:199], v[136:139]\n\t" \
  "v_mfma_f32_16x16x32_bf16 v[140:143], v[52:55], v[196:199], v[140:143]\n\t" \
  "ds_read_b128 v[196:199], v210 offset:49152\n\t" \
  "global_load_lds_dwordx4 v217, s[92:93] sc1\n\t" \
  "s_waitcnt lgkmcnt(4)\n\t" \
  "v_add_f32 v160, v160, v144\n\t" \
  "v_add_f32 v161, v161, v145\n\t" \
  "v_add_f32 v162, v162, v146\n\t" \
  "v_add_f32 v163, v163, v147\n\t" \
  "v_add_f32 v164, v164, v148\n\t" \
  "v_add_f32 v165, v165, v149\n\t" \
  "v_add_f32 v166, v166, v150\n\t" \
  "v_add_f32 v167, v167, v151\n\t" \
  "v_mfma_f32_16x16x32_bf16 v[128:131], v[56:59], v[200:203], v[128:131]\n\t" \
  "v_mfma_f32_16x16x32_bf16 v[132:135], v[60:63], v[200:203], v[132:135]\n\t" \
  "ds_read_b128 v[200:203], v211 offset:32768\n\t" \
  "v_mfma_f32_16x16x32_bf16 v[136:139], v[56:59], v[204:207], v[136:139]\n\t" \
  "v_mfma_f32_16x16x32_bf16 v[140:143], v[60:63], v[204:207], v[140:143]\n\t" \
  "ds_read_b128 v[204:207], v211 offset:49152\n\t" \
  "v_cvt_pk_bf16_f32 v168, v160, v161\n\t" \
  "v_cvt_pk_bf16_f32 v169, v162, v163\n\t" \
  "v_cvt_pk_bf16_f32 v170, v164, v165\n\t" \
  "v_cvt_pk_bf16_f32 v171, v166, v167\n\t" \
  "buffer_store_dwordx4 v[168:171], v172, %[rt], s90 offen sc1\n\t" \
  "v_mfma_f32_16x16x32_bf16 v[128:131], v[64:67], v[176:179], v[128:131]\n\t" \
  "v_mfma_f32_16x16x32_bf16 v[132:135], v[68:71], v[176:179], v[132:135]\n\t" \
  "ds_read_b128 v[176:179], v208 offset:33024\n\t" \
  "s_add_u32 m0, %[lw], 100352\n\t" \
  "v_mfma_f32_16x16x32_bf16 v[136:139], v[64:67], v[180:183], v[136:139]\n\t" \
  "v_mfma_f32_16x16x32_bf16 v[140:143], v[68:71], v[180:183], v[140:143]\n\t" \
  "ds_read_b128 v[180:183], v208 offset:49408\n\t" \
  "global_load_lds_dwordx4 v218, s[92:93] sc1\n\t" \
  "s_waitcnt lgkmcnt(6)\n\t" \
  "v_mfma_f32_16x16x32_bf16 v[128:131], v[72:75], v[184:187], v[128:131]\n\t" \
  "v_mfma_f32_16x16x32_bf16 v[132:135], v[76:79], v[184:187], v[132:135]\n\t" \
  "ds_read_b128 v[184:187], v209 offset:33024\n\t" \
  "v_mfma_f32_16x16x32_bf16 v[136:139], v[72:75], v[188:191], v[136:139]\n\t" \
  "v_mfma_f32_16x16x32_bf16 v[140:143], v[76:79], v[188:191], v[140:143]\n\t" \
  "ds_read_b128 v[188:191], v209 offset:49408\n\t" \
  "s_waitcnt lgkmcnt(6)\n\t" \
  "v_mfma_f32_16x16x32_bf16 v[128:131], v[80:83], v[192:195], v[128:131]\n\t" \
  "v_mfma_f32_16x16x32_bf16 v[132:135], v[84:87], v[192:195], v[132:135]\n\t" \
  "ds_read_b128 v[192:195], v210 offset:33024\n\t" \
  "s_add_u32 m0, %[lw], 101376\n\t" \
  "v_mfma_f32_16x16x32_bf16 v[136:139], v[80:83], v[196:199], v[136:139]\n\t" \
  "v_mfma_f32_16x16x32_bf16 v[140:143], v[84:87], v[196:199], v[140:143]\n\t" \
  "ds_read_b128 v[196:199], v210 offset:49408\n\t" \
  "global_load_lds_dwordx4 v219, s[92:93] sc1\n\t" \
  "s_waitcnt lgkmcnt(6)\n\t" \
  "v_mfma_f32_16x16x32_bf16 v[128:131], v[88:91], v[200:203], v[128:131]\n\t" \
  "v_mfma_f32_16x16x32_bf16 v[132:135], v[92:95], v[200:203], v[132:135]\n\t" \
  "ds_read_b128 v[200:203], v211 offset:33024\n\t" \
  "v_mfma_f32_16x16x32_bf16 v[136:139], v[88:91], v[204:207], v[136:139]\n\t" \
  "v_mfma_f32_16x16x32_bf16 v[140:143], v[92:95], v[204:207], v[140:143]\n\t" \
  "ds_read_b128 v[204:207], v211 offset:49408\n\t" \
  "s_cmp_lt_u32 s86, 6\n\t" \
  "s_cbranch_scc1 .Lw10%=\n\t" \
  "s_waitcnt vmcnt(5)\n\t" \
  "s_barrier\n\t" \
  ".Lg10%=:\n\t" \
  "s_add_u32 s92, s92, 65024\n\t" \
  "s_addc_u32 s93, s93, 0\n\t" \
  "s_waitcnt lgkmcnt(6)\n\t" \
  "v_mfma_f32_16x16x32_bf16 v[128:131], v[96:99], v[176:179], v[128:131]\n\t" \
  "v_mfma_f32_16x16x32_bf16 v[132:135], v[100:103], v[176:179], v[132:135]\n\t" \
  "ds_read_b128 v[176:179], v212 offset:0\n\t" \
  "s_add_u32 m0, %[lw], 0\n\t" \
  "v_mfma_f32_16x16x32_bf16 v[136:139], v[96:99], v[180:183], v[136:139]\n\t" \
  "v_mfma_f32_16x16x32_bf16 v[140:143], v[100:103], v[180:183], v[140:143]\n\t" \
  "ds_read_b128 v[180:183], v212 offset:16384\n\t" \
  "global_load_lds_dwordx4 v216, s[92:93] sc1\n\t" \
  "s_waitcnt lgkmcnt(6)\n\t" \
  "v_mfma_f32_16x16x32_bf16 v[128:131], v[104:107], v[184:187], v[128:131]\n\t" \
  "v_mfma_f32_16x16x32_bf16 v[132:135], v[108:111], v[184:187], v[132:135]\n\t" \
  "ds_read_b128 v[184:187], v213 offset:0\n\t" \
  "v_mfma_f32_16x16x32_bf16 v[136:139], v[104:107], v[188:191], v[136:139]\n\t" \
  "v_mfma_f32_16x16x32_bf16 v[140:143], v[108:111], v[188:191], v[140:143]\n\t" \
  "ds_read_b128 v[188:191], v213 offset:16384\n\t" \
  "s_waitcnt lgkmcnt(6)\n\t" \
  "v_mfma_f32_16x16x32_bf16 v[128:131], v[112:115], v[192:195], v[128:131]\n\t" \
  "v_mfma_f32_16x16x32_bf16 v[132:135], v[116:119], v[192:195], v[132:135]\n\t" \
  "ds_read_b128 v[192:195], v214 offset:0\n\t" \
  "s_add_u32 m0, %[lw], 1024\n\t" \
  "v_mfma_f32_16x16x32_bf16 v[136:139], v[112:115], v[196:199], v[136:139]\n\t" \
  "v_mfma_f32_16x16x32_bf16 v[140:143], v[116:119], v[196:199], v[140:143]\n\t" \
  "ds_read_b128 v[196:199], v214 offset:16384\n\t" \
  "global_load_lds_dwordx4 v217, s[92:93] sc1\n\t" \
  "s_waitcnt lgkmcnt(6)\n\t" \
  "v_mfma_f32_16x16x32_bf16 v[128:131], v[120:123], v[200:203], v[128:131]\n\t" \
  "v_mfma_f32_16x16x32_bf16 v[132:135], v[124:127], v[200:203], v[132:135]\n\t" \
  "ds_read_b128 v[200:203], v215 offset:0\n\t" \
  "v_mfma_f32_16x16x32_bf16 v[136:139], v[120:123], v[204:207], v[136:139]\n\t" \
  "v_mfma_f32_16x16x32_bf16 v[140:143], v[124:127], v[204:207], v[140:143]\n\t" \
  "ds_read_b128 v[204:207], v215 offset:16384\n\t" \
  "s_add_u32 s86, s86, 1\n\t" \
  "s_add_u32 s87, s87, 1\n\t" \
  "s_cmp_eq_u32 s86, 1\n\t" \
  "s_cbranch_scc1 .Lf0%=\n\t" \
  ".Lfb0%=:\n\t" \
  "s_waitcnt lgkmcnt(6)\n\t" \
  "v_mfma_f32_16x16x32_bf16 v[144:147], v[0:3], v[176:179], 0\n\t" \
  "v_mfma_f32_16x16x32_bf16 v[148:151], v[4:7], v[176:179], 0\n\t" \
  "ds_read_b128 v[176:179], v212 offset:256\n\t" \
  "s_add_u32 m0, %[lw], 2048\n\t" \
  "v_mfma_f32_16x16x32_bf16 v[152:155], v[0:3], v[180:183], 0\n\t" \
  "v_mfma_f32_16x16x32_bf16 v[156:159], v[4:7], v[180:183], 0\n\t" \
  "ds_read_b128 v[180:183], v212 offset:16640\n\t" \
  "global_load_lds_dwordx4 v218, s[92:93] sc1\n\t" \
  "s_waitcnt lgkmcnt(6)\n\t" \
  "v_mfma_f32_16x16x32_bf16 v[144:147], v[8:11], v[184:187], v[144:147]\n\t" \
  "v_mfma_f32_16x16x32_bf16 v[148:151], v[12:15], v[184:187], v[148:151]\n\t" \
  "ds_read_b128 v[184:187], v213 offset:256\n\t" \
  "v_mfma_f32_16x16x32_bf16 v[152:155], v[8:11], v[188:191], v[152:155]\n\t" \
  "v_mfma_f32_16x16x32_bf16 v[156:159], v[12:15], v[188:191], v[156:159]\n\t" \
  "ds_read_b128 v[188:191], v213 offset:16640\n\t" \
  "ds_write_b128 v220, v[136:139] offset:0\n\t" \
  "ds_write_b128 v220, v[140:143] offset:1024\n\t" \
  "s_waitcnt lgkmcnt(8)\n\t" \
  "v_mfma_f32_16x16x32_bf16 v[144:147], v[16:19], v[192:195], v[144:147]\n\t" \
  "v_mfma_f32_16x16x32_bf16 v[148:151], v[20:23], v[192:195], v[148:151]\n\t" \
  "ds_read_b128 v[192:195], v214 offset:256\n\t" \
  "s_add_u32 m0, %[lw], 3072\n\t" \
  "v_mfma_f32_16x16x32_bf16 v[152:155], v[16:19], v[196:199], v[152:155]\n\t" \
  "v_mfma_f32_16x16x32_bf16 v[156:159], v[20:23], v[196:199], v[156:159]\n\t" \
  "ds_read_b128 v[196:199], v214 offset:16640\n\t" \
  "global_load_lds_dwordx4 v219, s[92:93] sc1\n\t" \
  "s_lshr_b32 s90, s87, 7\n\t" \
  "s_xor_b32 s90, s90, %[s0]\n\t" \
  "s_and_b32 s90, s90, 1\n\t" \
  "s_lshl1_add_u32 s90, s87, s90\n\t" \
  "s_lshl_b32 s90, s90, 13\n\t" \
  "s_cmp_lt_i32 s86, 3\n\t" \
  "s_cselect_b32 s90, 0x70000000, s90\n\t" \
  "s_waitcnt lgkmcnt(8)\n\t" \
  "v_mfma_f32_16x16x32_bf16 v[144:147], v[24:27], v[200:203], v[144:147]\n\t" \
  "v_mfma_f32_16x16x32_bf16 v[148:151], v[28:31], v[200:203], v[148:151]\n\t" \
  "ds_read_b128 v[200:203], v215 offset:256\n\t" \
  "v_mfma_f32_16x16x32_bf16 v[152:155], v[24:27], v[204:207], v[152:155]\n\t" \
  "v_mfma_f32_16x16x32_bf16 v[156:159], v[28:31], v[204:207], v[156:159]\n\t" \
  "ds_read_b128 v[204:207], v215 offset:16640\n\t" \
  "s_waitcnt lgkmcnt(4)\n\t" \
  "s_waitcnt vmcnt(4)\n\t" \
  "s_barrier\n\t" \
  ".Lg20%=:\n\t" \
  "s_add_u32 s92, s92, 512\n\t" \
  "s_addc_u32 s93, s93, 0\n\t" \
  "v_mfma_f32_16x16x32_bf16 v[144:147], v[32:35], v[176:179], v[144:147]\n\t" \
  "v_mfma_f32_16x16x32_bf16 v[148:151], v[36:39], v[176:179], v[148:151]\n\t" \
  "ds_read_b128 v[176:179], v212 offset:32768\n\t" \
  "s_add_u32 m0, %[lw], 32768\n\t" \
  "v_mfma_f32_16x16x32_bf16 v[152:155], v[32:35], v[180:183], v[152:155]\n\t" \
  "v_mfma_f32_16x16x32_bf16 v[156:159], v[36:39], v[180:183], v[156:159]\n\t" \
  "ds_read_b128 v[180:183], v212 offset:49152\n\t" \
  "global_load_lds_dwordx4 v216, s[92:93] sc1\n\t" \
  "ds_read_b128 v[160:163], v221 offset:0\n\t" \
  "ds_read_b128 v[164:167], v221 offset:1024\n\t" \
  "v_mfma_f32_16x16x32_bf16 v[144:147], v[40:43], v[184:187], v[144:147]\n\t" \
  "v_mfma_f32_16x16x32_bf16 v[148:151], v[44:47], v[184:187], v[148:151]\n\t" \
  "ds_read_b128 v[184:187], v213 offset:32768\n\t" \
  "v_mfma_f32_16x16x32_bf16 v[152:155], v[40:43], v[188:191], v[152:155]\n\t" \
  "v_mfma_f32_16x16x32_bf16 v[156:159], v[44:47], v[188:191], v[156:159]\n\t" \
  "ds_read_b128 v[188:191], v213 offset:49152\n\t" \
  "s_waitcnt lgkmcnt(8)\n\t" \
  "v_mfma_f32_16x16x32_bf16 v[144:147], v[48:51], v[192:195], v[144:147]\n\t" \
  "v_mfma_f32_16x16x32_bf16 v[148:151], v[52:55], v[192:195], v[148:151]\n\t" \
  "ds_read_b128 v[192:195], v214 offset:32768\n\t" \
  "s_add_u32 m0, %[lw], 33792\n\t" \
  "v_mfma_f32_16x16x32_bf16 v[152:155], v[48:51], v[196:199], v[152:155]\n\t" \
  "v_mfma_f32_16x16x32_bf16 v[156:159], v[52:55], v[196:199], v[156:159]\n\t" \
  "ds_read_b128 v[196:199], v214 offset:49152\n\t" \
  "global_load_lds_dwordx4 v217, s[92:93] sc1\n\t" \
  "s_waitcnt lgkmcnt(4)\n\t" \
  "v_add_f32 v160, v160, v128\n\t" \
  "v_add_f32 v161, v161, v129\n\t" \
  "v_add_f32 v162, v162, v130\n\t" \
  "v_add_f32 v163, v163, v131\n\t" \
  "v_add_f32 v164, v164, v132\n\t" \
  "v_add_f32 v165, v165, v133\n\t" \
  "v_add_f32 v166, v166, v134\n\t" \
  "v_add_f32 v167, v167, v135\n\t" \
  "v_mfma_f32_16x16x32_bf16 v[144:147], v[56:59], v[200:203], v[144:147]\n\t" \
  "v_mfma_f32_16x16x32_bf16 v[148:151], v[60:63], v[200:203], v[148:151]\n\t" \
  "ds_read_b128 v[200:203], v215 offset:32768\n\t" \
  "v_mfma_f32_16x16x32_bf16 v[152:155], v[56:59], v[204:207], v[152:155]\n\t" \
  "v_mfma_f32_16x16x32_bf16 v[156:159], v[60:63], v[204:207], v[156:159]\n\t" \
  "ds_read_b128 v[204:207], v215 offset:49152\n\t" \
  "v_cvt_pk_bf16_f32 v168, v160, v161\n\t" \
  "v_cvt_pk_bf16_f32 v169, v162, v163\n\t" \
  "v_cvt_pk_bf16_f32 v170, v164, v165\n\t" \
  "v_cvt_pk_bf16_f32 v171, v166, v167\n\t" \
  "buffer_store_dwordx4 v[168:171], v172, %[rt], s90 offen sc1\n\t" \
  "v_mfma_f32_16x16x32_bf16 v[144:147], v[64:67], v[176:179], v[144:147]\n\t" \
  "v_mfma_f32_16x16x32_bf16 v[148:151], v[68:71], v[176:179], v[148:151]\n\t" \
  "ds_read_b128 v[176:179], v212 offset:33024\n\t" \
  "s_add_u32 m0, %[lw], 34816\n\t" \
  "v_mfma_f32_16x16x32_bf16 v[152:155], v[64:67], v[180:183], v[152:155]\n\t" \
  "v_mfma_f32_16x16x32_bf16 v[156:159], v[68:71], v[180:183], v[156:159]\n\t" \
  "ds_read_b128 v[180:183], v212 offset:49408\n\t" \
  "global_load_lds_dwordx4 v218, s[92:93] sc1\n\t" \
  "s_waitcnt lgkmcnt(6)\n\t" \
  "v_mfma_f32_16x16x32_bf16 v[144:147], v[72:75], v[184:187], v[144:147]\n\t" \
  "v_mfma_f32_16x16x32_bf16 v[148:151], v[76:79], v[184:187], v[148:151]\n\t" \
  "ds_read_b128 v[184:187], v213 offset:33024\n\t" \
  "v_mfma_f32_16x16x32_bf16 v[152:155], v[72:75], v[188:191], v[152:155]\n\t" \
  "v_mfma_f32_16x16x32_bf16 v[156:159], v[76:79], v[188:191], v[156:159]\n\t" \
  "ds_read_b128 v[188:191], v213 offset:49408\n\t" \
  "s_waitcnt lgkmcnt(6)\n\t" \
  "v_mfma_f32_16x16x32_bf16 v[144:147], v[80:83], v[192:195], v[144:147]\n\t" \
  "v_mfma_f32_16x16x32_bf16 v[148:151], v[84:87], v[192:195], v[148:151]\n\t" \
  "ds_read_b128 v[192:195], v214 offset:33024\n\t" \
  "s_add_u32 m0, %[lw], 35840\n\t" \
  "v_mfma_f32_16x16x32_bf16 v[152:155], v[80:83], v[196:199], v[152:155]\n\t" \
  "v_mfma_f32_16x16x32_bf16 v[156:159], v[84:87], v[196:199], v[156:159]\n\t" \
  "ds_read_b128 v[196:199], v214 offset:49408\n\t" \
  "global_load_lds_dwordx4 v219, s[92:93] sc1\n\t" \
  "s_waitcnt lgkmcnt(6)\n\t" \
  "v_mfma_f32_16x16x32_bf16 v[144:147], v[88:91], v[200:203], v[144:147]\n\t" \
  "v_mfma_f32_16x16x32_bf16 v[148:151], v[92:95], v[200:203], v[148:151]\n\t" \
  "ds_read_b128 v[200:203], v215 offset:33024\n\t" \
  "v_mfma_f32_16x16x32_bf16 v[152:155], v[88:91], v[204:207], v[152:155]\n\t" \
  "v_mfma_f32_16x16x32_bf16 v[156:159], v[92:95], v[204:207], v[156:159]\n\t" \
  "ds_read_b128 v[204:207], v215 offset:49408\n\t" \
  "s_cmp_lt_u32 s86, 6\n\t" \
  "s_cbranch_scc1 .Lw30%=\n\t" \
  "s_waitcnt vmcnt(5)\n\t" \
  "s_barrier\n\t" \
  ".Lg30%=:\n\t" \
  "s_add_u32 s92, s92, 65024\n\t" \
  "s_addc_u32 s93, s93, 0\n\t" \
  "s_waitcnt lgkmcnt(6)\n\t" \
  "v_mfma_f32_16x16x32_bf16 v[144:147], v[96:99], v[176:179], v[144:147]\n\t" \
  "v_mfma_f32_16x16x32_bf16 v[148:151], v[100:103], v[176:179], v[148:151]\n\t" \
  "ds_read_b128 v[176:179], v208 offset:0\n\t" \
  "s_add_u32 m0, %[lw], 65536\n\t" \
  "v_mfma_f32_16x16x32_bf16 v[152:155], v[96:99], v[180:183], v[152:155]\n\t" \
  "v_mfma_f32_16x16x32_bf16 v[156:159], v[100:103], v[180:183], v[156:159]\n\t" \
  "ds_read_b128 v[180:183], v208 offset:16384\n\t" \
  "global_load_lds_dwordx4 v216, s[92:93] sc1\n\t" \
  "s_waitcnt lgkmcnt(6)\n\t" \
  "v_mfma_f32_16x16x32_bf16 v[144:147], v[104:107], v[184:187], v[144:147]\n\t" \
  "v_mfma_f32_16x16x32_bf16 v[148:151], v[108:111], v[184:187], v[148:151]\n\t" \
  "ds_read_b128 v[184:187], v209 offset:0\n\t" \
  "v_mfma_f32_16x16x32_bf16 v[152:155], v[104:107], v[188:191], v[152:155]\n\t" \
  "v_mfma_f32_16x16x32_bf16 v[156:159], v[108:111], v[188:191], v[156:159]\n\t" \
  "ds_read_b128 v[188:191], v209 offset:16384\n\t" \
  "s_waitcnt lgkmcnt(6)\n\t" \
  "v_mfma_f32_16x16x32_bf16 v[144:147], v[112:115], v[192:195], v[144:147]\n\t" \
  "v_mfma_f32_16x16x32_bf16 v[148:151], v[116:119], v[192:195], v[148:151]\n\t" \
  "ds_read_b128 v[192:195], v210 offset:0\n\t" \
  "s_add_u32 m0, %[lw], 66560\n\t" \
  "v_mfma_f32_16x16x32_bf16 v[152:155], v[112:115], v[196:199], v[152:155]\n\t" \
  "v_mfma_f32_16x16x32_bf16 v[156:159], v[116:119], v[196:199], v[156:159]\n\t" \
  "ds_read_b128 v[196:199], v210 offset:16384\n\t" \
  "global_load_lds_dwordx4 v217, s[92:93] sc1\n\t" \
  "s_waitcnt lgkmcnt(6)\n\t" \
  "v_mfma_f32_16x16x32_bf16 v[144:147], v[120:123], v[200:203], v[144:147]\n\t" \
  "v_mfma_f32_16x16x32_bf16 v[148:151], v[124:127], v[200:203], v[148:151]\n\t" \
  "ds_read_b128 v[200:203], v211 offset:0\n\t" \
  "v_mfma_f32_16x16x32_bf16 v[152:155], v[120:123], v[204:207], v[152:155]\n\t" \
  "v_mfma_f32_16x16x32_bf16 v[156:159], v[124:127], v[204:207], v[156:159]\n\t" \
  "ds_read_b128 v[204:207], v211 offset:16384\n\t" \
  "s_add_u32 s86, s86, 1\n\t" \
  "s_add_u32 s87, s87, 1\n\t" \
  "s_cmp_eq_u32 s86, 2\n\t" \
  "s_cbranch_scc1 .Ll0%=\n\t" \
  "s_cmp_lt_u32 s86, %[end]\n\t" \
  "s_cbranch_scc1 .Lp0%=\n\t" \
  "s_cmp_lg_u32 s86, 64\n\t" \
  "s_cbranch_scc1 .Lx0%=\n\t" \
  ".Ll0%=:\n\t" \
  "s_nop 7\n\t" \
  "ds_write_b128 v220, v[152:155] offset:0\n\t" \
  "ds_write_b128 v220, v[156:159] offset:1024\n\t" \
  "s_lshr_b32 s90, s87, 7\n\t" \
  "s_xor_b32 s90, s90, %[s0]\n\t" \
  "s_and_b32 s90, s90, 1\n\t" \
  "s_lshl1_add_u32 s90, s87, s90\n\t" \
  "s_lshl_b32 s90, s90, 13\n\t" \
  "s_cmp_lt_i32 s86, 1\n\t" \
  "s_cselect_b32 s90, 0x70000000, s90\n\t" \
  "s_waitcnt lgkmcnt(0)\n\t" \
  "s_barrier\n\t" \
  "ds_read_b128 v[160:163], v221 offset:0\n\t" \
  "ds_read_b128 v[164:167], v221 offset:1024\n\t" \
  "s_waitcnt lgkmcnt(0)\n\t" \
  "v_add_f32 v160, v160, v144\n\t" \
  "v_add_f32 v161, v161, v145\n\t" \
  "v_add_f32 v162, v162, v146\n\t" \
  "v_add_f32 v163, v163, v147\n\t" \
  "v_add_f32 v164, v164, v148\n\t" \
  "v_add_f32 v165, v165, v149\n\t" \
  "v_add_f32 v166, v166, v150\n\t" \
  "v_add_f32 v167, v167, v151\n\t" \
  "v_cvt_pk_bf16_f32 v168, v160, v161\n\t" \
  "v_cvt_pk_bf16_f32 v169, v162, v163\n\t" \
  "v_cvt_pk_bf16_f32 v170, v164, v165\n\t" \
  "v_cvt_pk_bf16_f32 v171, v166, v167\n\t" \
  "buffer_store_dwordx4 v[168:171], v172, %[rt], s90 offen sc1\n\t" \
  "s_waitcnt vmcnt(0)\n\t" \
  "s_barrier\n\t" \
  "s_cmp_eq_u32 s86, 2\n\t" \
  "s_cbranch_scc1 .Lp0%=\n\t" \
  "s_branch .Lx0%=\n\t" \
  ".Lf0%=:\n\t" \
  "s_nop 7\n\t" \
  "ds_write_b128 v220, v[136:139] offset:0\n\t" \
  "ds_write_b128 v220, v[140:143] offset:1024\n\t" \
  "s_lshr_b32 s90, s87, 7\n\t" \
  "s_xor_b32 s90, s90, %[s0]\n\t" \
  "s_and_b32 s90, s90, 1\n\t" \
  "s_lshl1_add_u32 s90, s87, s90\n\t" \
  "s_lshl_b32 s90, s90, 13\n\t" \
  "s_cmp_lt_i32 s86, 1\n\t" \
  "s_cselect_b32 s90, 0x70000000, s90\n\t" \
  "s_waitcnt lgkmcnt(0)\n\t" \
  "s_barrier\n\t" \
  "ds_read_b128 v[160:163], v221 offset:0\n\t" \
  "ds_read_b128 v[164:167], v221 offset:1024\n\t" \
  "s_waitcnt lgkmcnt(0)\n\t" \
  "v_add_f32 v160, v160, v128\n\t" \
  "v_add_f32 v161, v161, v129\n\t" \
  "v_add_f32 v162, v162, v130\n\t" \
  "v_add_f32 v163, v163, v131\n\t" \
  "v_add_f32 v164, v164, v132\n\t" \
  "v_add_f32 v165, v165, v133\n\t" \
  "v_add_f32 v166, v166, v134\n\t" \
  "v_add_f32 v167, v167, v135\n\t" \
  "v_cvt_pk_bf16_f32 v168, v160, v161\n\t" \
  "v_cvt_pk_bf16_f32 v169, v162, v163\n\t" \
  "v_cvt_pk_bf16_f32 v170, v164, v165\n\t" \
  "v_cvt_pk_bf16_f32 v171, v166, v167\n\t" \
  "buffer_store_dwordx4 v[168:171], v172, %[rt], s90 offen sc1\n\t" \
  "s_waitcnt vmcnt(0)\n\t" \
  "s_barrier\n\t" \
  "s_branch .Lfb0%=\n\t" \
  ".Lw10%=:\n\t" \
  "s_waitcnt vmcnt(2)\n\t" \
  "s_barrier\n\t" \
  "s_branch .Lg10%=\n\t" \
  ".Lw30%=:\n\t" \
  "s_waitcnt vmcnt(2)\n\t" \
  "s_barrier\n\t" \
  "s_branch .Lg30%=\n\t" \
  ".Lx0%=:\n\t" \
  "s_waitcnt lgkmcnt(0)\n\t" \
  "s_setprio 0\n\t" \
  "s_mov_b32 m0, s94\n\t" \
  "s_branch .Le%=\n\t" \
  ".Lk%=:\n\t" \
  "s_mov_b32 s94, m0\n\t" \
  "s_mov_b64 s[92:93], %[x0]\n\t" \
  "s_mov_b32 s86, %[g0]\n\t" \
  "s_add_u32 s87, s86, %[t0]\n\t" \
  "s_sub_i32 s88, s86, 3\n\t" \
  "ds_read_b128 v[176:179], v208 offset:0\n\t" \
  "ds_read_b128 v[180:183], v208 offset:16384\n\t" \
  "ds_read_b128 v[184:187], v209 offset:0\n\t" \
  "ds_read_b128 v[188:191], v209 offset:16384\n\t" \
  "ds_read_b128 v[192:195], v210 offset:0\n\t" \
  "ds_read_b128 v[196:199], v210 offset:16384\n\t" \
  "ds_read_b128 v[200:203], v211 offset:0\n\t" \
  "ds_read_b128 v[204:207], v211 offset:16384\n\t" \
  ".Lp1%=:\n\t" \
  "s_waitcnt lgkmcnt(6)\n\t" \
  "v_mfma_f32_16x16x32_bf16 v[128:131], v[0:3], v[176:179], 0\n\t" \
  "v_mfma_f32_16x16x32_bf16 v[132:135], v[4:7], v[176:179], 0\n\t" \
  "ds_read_b128 v[176:179], v208 offset:256\n\t" \
  "v_mfma_f32_16x16x32_bf16 v[136:139], v[0:3], v[180:183], 0\n\t" \
  "v_mfma_f32_16x16x32_bf16 v[140:143], v[4:7], v[180:183], 0\n\t" \
  "ds_read_b128 v[180:183], v208 offset:16640\n\t" \
  "s_waitcnt lgkmcnt(6)\n\t" \
  "v_mfma_f32_16x16x32_bf16 v[128:131], v[8:11], v[184:187], v[128:131]\n\t" \
  "v_mfma_f32_16x16x32_bf16 v[132:135], v[12:15], v[184:187], v[132:135]\n\t" \
  "ds_read_b128 v[184:187], v209 offset:256\n\t" \
  "s_add_u32 m0, %[lw], 67584\n\t" \
  "v_mfma_f32_16x16x32_bf16 v[136:139], v[8:11], v[188:191], v[136:139]\n\t" \
  "v_mfma_f32_16x16x32_bf16 v[140:143], v[12:15], v[188:191], v[140:143]\n\t" \
  "ds_read_b128 v[188:191], v209 offset:16640\n\t" \
  "global_load_lds_dwordx4 v218, s[92:93] sc1\n\t" \
  "ds_write_b128 v220, v[152:155] offset:0\n\t" \
  "ds_write_b128 v220, v[156:159] offset:1024\n\t" \
  "s_waitcnt lgkmcnt(8)\n\t" \
  "v_mfma_f32_16x16x32_bf16 v[128:131], v[16:19], v[192:195], v[128:131]\n\t" \
  "v_mfma_f32_16x16x32_bf16 v[132:135], v[20:23], v[192:195], v[132:135]\n\t" \
  "ds_read_b128 v[192:195], v210 offset:256\n\t" \
  "v_mfma_f32_16x16x32_bf16 v[136:139], v[16:19], v[196:199], v[136:139]\n\t" \
  "v_mfma_f32_16x16x32_bf16 v[140:143], v[20:23], v[196:199], v[140:143]\n\t" \
  "ds_read_b128 v[196:199], v210 offset:16640\n\t" \
  "s_lshr_b32 s90, s87, 7\n\t" \
  "s_xor_b32 s90, s90, %[s0]\n\t" \
  "s_and_b32 s90, s90, 1\n\t" \
  "s_lshl1_add_u32 s90, s87, s90\n\t" \
  "s_lshl_b32 s90, s90, 13\n\t" \
  "s_cmp_lt_i32 s86, 3\n\t" \
  "s_cselect_b32 s90, 0x70000000, s90\n\t" \
  "s_waitcnt lgkmcnt(8)\n\t" \
  "v_mfma_f32_16x16x32_bf16 v[128:131], v[24:27], v[200:203], v[128:131]\n\t" \
  "v_mfma_f32_16x16x32_bf16 v[132:135], v[28:31], v[200:203], v[132:135]\n\t" \
  "ds_read_b128 v[200:203], v211 offset:256\n\t" \
  "s_add_u32 m0, %[lw], 68608\n\t" \
  "v_mfma_f32_16x16x32_bf16 v[136:139], v[24:27], v[204:207], v[136:139]\n\t" \
  "v_mfma_f32_16x16x32_bf16 v[140:143], v[28:31], v[204:207], v[140:143]\n\t" \
  "ds_read_b128 v[204:207], v211 offset:16640\n\t" \
  "global_load_lds_dwordx4 v219, s[92:93] sc1\n\t" \
  "s_waitcnt lgkmcnt(4)\n\t" \
  "s_waitcnt vmcnt(4)\n\t" \
  "s_barrier\n\t" \
  ".Lg01%=:\n\t" \
  "s_add_u32 s92, s92, 512\n\t" \
  "s_addc_u32 s93, s93, 0\n\t" \
  "v_mfma_f32_16x16x32_bf16 v[128:131], v[32:35], v[176:179], v[128:131]\n\t" \
  "v_mfma_f32_16x16x32_bf16 v[132:135], v[36:39], v[176:179], v[132:135]\n\t" \
  "ds_read_b128 v[176:179], v208 offset:32768\n\t" \
  "v_mfma_f32_16x16x32_bf16 v[136:139], v[32:35], v[180:183], v[136:139]\n\t" \
  "v_mfma_f32_16x16x32_bf16 v[140:143], v[36:39], v[180:183], v[140:143]\n\t" \
  "ds_read_b128 v[180:183], v208 offset:49152\n\t" \
  "ds_read_b128 v[160:163], v221 offset:0\n\t" \
  "ds_read_b128 v[164:167], v221 offset:1024\n\t" \
  "s_sub_i32 s91, s86, 2\n\t" \
  "s_max_i32 s88, s88, s91\n\t" \
  "v_mov_b32 v173, s88\n\t" \
  "buffer_store_dword v173, v174, %[rs], 0 offen sc1\n\t" \
  "v_mfma_f32_16x16x32_bf16 v[128:131], v[40:43], v[184:187], v[128:131]\n\t" \
  "v_mfma_f32_16x16x32_bf16 v[132:135], v[44:47], v[184:187], v[132:135]\n\t" \
  "ds_read_b128 v[184:187], v209 offset:32768\n\t" \
  "s_add_u32 m0, %[lw], 98304\n\t" \
  "v_mfma_f32_16x16x32_bf16 v[136:139], v[40:43], v[188:191], v[136:139]\n\t" \
  "v_mfma_f32_16x16x32_bf16 v[140:143], v[44:47], v[188:191], v[140:143]\n\t" \
  "ds_read_b128 v[188:191], v209 offset:49152\n\t" \
  "global_load_lds_dwordx4 v216, s[92:93] sc1\n\t" \
  "s_waitcnt lgkmcnt(8)\n\t" \
  "v_mfma_f32_16x16x32_bf16 v[128:131], v[48:51], v[192:195], v[128:131]\n\t" \
  "v_mfma_f32_16x16x32_bf16 v[132:135], v[52:55], v[192:195], v[132:135]\n\t" \
  "ds_read_b128 v[192:195], v210 offset:32768\n\t" \
  "v_mfma_f32_16x16x32_bf16 v[136:139], v[48:51], v[196:199], v[136:139]\n\t" \
  "v_mfma_f32_16x16x32_bf16 v[140:143], v[52:55], v[196:199], v[140:143]\n\t" \
  "ds_read_b128 v[196:199], v210 offset:49152\n\t" \
  "s_waitcnt lgkmcnt(4)\n\t" \
  "v_add_f32 v160, v160, v144\n\t" \
  "v_add_f32 v161, v161, v145\n\t" \
  "v_add_f32 v162, v162, v146\n\t" \
  "v_add_f32 v163, v163, v147\n\t" \
  "v_add_f32 v164, v164, v148\n\t" \
  "v_add_f32 v165, v165, v149\n\t" \
  "v_add_f32 v166, v166, v150\n\t" \
  "v_add_f32 v167, v167, v151\n\t" \
  "v_mfma_f32_16x16x32_bf16 v[128:131], v[56:59], v[200:203], v[128:131]\n\t" \
  "v_mfma_f32_16x16x32_bf16 v[132:135], v[60:63], v[200:203], v[132:135]\n\t" \
  "ds_read_b128 v[200:203], v211 offset:32768\n\t" \
  "s_add_u32 m0, %[lw], 99328\n\t" \
  "v_mfma_f32_16x16x32_bf16 v[136:139], v[56:59], v[204:207], v[136:139]\n\t" \
  "v_mfma_f32_16x16x32_bf16 v[140:143], v[60:63], v[204:207], v[140:143]\n\t" \
  "ds_read_b128 v[204:207], v211 offset:49152\n\t" \
  "global_load_lds_dwordx4 v217, s[92:93] sc1\n\t" \
  "v_cvt_pk_bf16_f32 v168, v160, v161\n\t" \
  "v_cvt_pk_bf16_f32 v169, v162, v163\n\t" \
  "v_cvt_pk_bf16_f32 v170, v164, v165\n\t" \
  "v_cvt_pk_bf16_f32 v171, v166, v167\n\t" \
  "buffer_store_dwordx4 v[168:171], v172, %[rt], s90 offen sc1\n\t" \
  "v_mfma_f32_16x16x32_bf16 v[128:131], v[64:67], v[176:179], v[128:131]\n\t" \
  "v_mfma_f32_16x16x32_bf16 v[132:135], v[68:71], v[176:179], v[132:135]\n\t" \
  "ds_read_b128 v[176:179], v208 offset:33024\n\t" \
  "v_mfma_f32_16x16x32_bf16 v[136:139], v[64:67], v[180:183], v[136:139]\n\t" \
  "v_mfma_f32_16x16x32_bf16 v[140:143], v[68:71], v[180:183], v[140:143]\n\t" \
  "ds_read_b128 v[180:183], v208 offset:49408\n\t" \
  "s_waitcnt lgkmcnt(6)\n\t" \
  "v_mfma_f32_16x16x32_bf16 v[128:131], v[72:75], v[184:187], v[128:131]\n\t" \
  "v_mfma_f32_16x16x32_bf16 v[132:135], v[76:79], v[184:187], v[132:135]\n\t" \
  "ds_read_b128 v[184:187], v209 offset:33024\n\t" \
  "s_add_u32 m0, %[lw], 100352\n\t" \
  "v_mfma_f32_16x16x32_bf16 v[136:139], v[72:75], v[188:191], v[136:139]\n\t" \
  "v_mfma_f32_16x16x32_bf16 v[140:143], v[76:79], v[188:191], v[140:143]\n\t" \
  "ds_read_b128 v[188:191], v209 offset:49408\n\t" \
  "global_load_lds_dwordx4 v218, s[92:93] sc1\n\t" \
  "s_waitcnt lgkmcnt(6)\n\t" \
  "v_mfma_f32_16x16x32_bf16 v[128:131], v[80:83], v[192:195], v[128:131]\n\t" \
  "v_mfma_f32_16x16x32_bf16 v[132:135], v[84:87], v[192:195], v[132:135]\n\t" \
  "ds_read_b128 v[192:195], v210 offset:33024\n\t" \
  "v_mfma_f32_16x16x32_bf16 v[136:139], v[80:83], v[196:199], v[136:139]\n\t" \
  "v_mfma_f32_16x16x32_bf16 v[140:143], v[84:87], v[196:199], v[140:143]\n\t" \
  "ds_read_b128 v[196:199], v210 offset:49408\n\t" \
  "s_waitcnt lgkmcnt(6)\n\t" \
  "v_mfma_f32_16x16x32_bf16 v[128:131], v[88:91], v[200:203], v[128:131]\n\t" \
  "v_mfma_f32_16x16x32_bf16 v[132:135], v[92:95], v[200:203], v[132:135]\n\t" \
  "ds_read_b128 v[200:203], v211 offset:33024\n\t" \
  "s_add_u32 m0, %[lw], 101376\n\t" \
  "v_mfma_f32_16x16x32_bf16 v[136:139], v[88:91], v[204:207], v[136:139]\n\t" \
  "v_mfma_f32_16x16x32_bf16 v[140:143], v[92:95], v[204:207], v[140:143]\n\t" \
  "ds_read_b128 v[204:207], v211 offset:49408\n\t" \
  "global_load_lds_dwordx4 v219, s[92:93] sc1\n\t" \
  "s_cmp_lt_u32 s86, 6\n\t" \
  "s_cbranch_scc1 .Lw11%=\n\t" \
  "s_waitcnt vmcnt(6)\n\t" \
  "s_barrier\n\t" \
  ".Lg11%=:\n\t" \
  "s_add_u32 s92, s92, 65024\n\t" \
  "s_addc_u32 s93, s93, 0\n\t" \
  "s_waitcnt lgkmcnt(6)\n\t" \
  "v_mfma_f32_16x16x32_bf16 v[128:131], v[96:99], v[176:179], v[128:131]\n\t" \
  "v_mfma_f32_16x16x32_bf16 v[132:135], v[100:103], v[176:179], v[132:135]\n\t" \
  "ds_read_b128 v[176:179], v212 offset:0\n\t" \
  "v_mfma_f32_16x16x32_bf16 v[136:139], v[96:99], v[180:183], v[136:139]\n\t" \
  "v_mfma_f32_16x16x32_bf16 v[140:143], v[100:103], v[180:183], v[140:143]\n\t" \
  "ds_read_b128 v[180:183], v212 offset:16384\n\t" \
  "s_waitcnt lgkmcnt(6)\n\t" \
  "v_mfma_f32_16x16x32_bf16 v[128:131], v[104:107], v[184:187], v[128:131]\n\t" \
  "v_mfma_f32_16x16x32_bf16 v[132:135], v[108:111], v[184:187], v[132:135]\n\t" \
  "ds_read_b128 v[184:187], v213 offset:0\n\t" \
  "s_add_u32 m0, %[lw], 0\n\t" \
  "v_mfma_f32_16x16x32_bf16 v[136:139], v[104:107], v[188:191], v[136:139]\n\t" \
  "v_mfma_f32_16x16x32_bf16 v[140:143], v[108:111], v[188:191], v[140:143]\n\t" \
  "ds_read_b128 v[188:191], v213 offset:16384\n\t" \
  "global_load_lds_dwordx4 v216, s[92:93] sc1\n\t" \
  "s_waitcnt lgkmcnt(6)\n\t" \
  "v_mfma_f32_16x16x32_bf16 v[128:131], v[112:115], v[192:195], v[128:131]\n\t" \
  "v_mfma_f32_16x16x32_bf16 v[132:135], v[116:119], v[192:195], v[132:135]\n\t" \
  "ds_read_b128 v[192:195], v214 offset:0\n\t" \
  "v_mfma_f32_16x16x32_bf16 v[136:139], v[112:115], v[196:199], v[136:139]\n\t" \
  "v_mfma_f32_16x16x32_bf16 v[140:143], v[116:119], v[196:199], v[140:143]\n\t" \
  "ds_read_b128 v[196:199], v214 offset:16384\n\t" \
  "s_waitcnt lgkmcnt(6)\n\t" \
  "v_mfma_f32_16x16x32_bf16 v[128:131], v[120:123], v[200:203], v[128:131]\n\t" \
  "v_mfma_f32_16x16x32_bf16 v[132:135], v[124:127], v[200:203], v[132:135]\n\t" \
  "ds_read_b128 v[200:203], v215 offset:0\n\t" \
  "s_add_u32 m0, %[lw], 1024\n\t" \
  "v_mfma_f32_16x16x32_bf16 v[136:139], v[120:123], v[204:207], v[136:139]\n\t" \
  "v_mfma_f32_16x16x32_bf16 v[140:143], v[124:127], v[204:207], v[140:143]\n\t" \
  "ds_read_b128 v[204:207], v215 offset:16384\n\t" \
  "global_load_lds_dwordx4 v217, s[92:93] sc1\n\t" \
  "s_add_u32 s86, s86, 1\n\t" \
  "s_add_u32 s87, s87, 1\n\t" \
  "s_cmp_eq_u32 s86, 1\n\t" \
  "s_cbranch_scc1 .Lf1%=\n\t" \
  ".Lfb1%=:\n\t" \
  "s_waitcnt lgkmcnt(6)\n\t" \
  "v_mfma_f32_16x16x32_bf16 v[144:147], v[0:3], v[176:179], 0\n\t" \
  "v_mfma_f32_16x16x32_bf16 v[148:151], v[4:7], v[176:179], 0\n\t" \
  "ds_read_b128 v[176:179], v212 offset:256\n\t" \
  "v_mfma_f32_16x16x32_bf16 v[152:155], v[0:3], v[180:183], 0\n\t" \
  "v_mfma_f32_16x16x32_bf16 v[156:159], v[4:7], v[180:183], 0\n\t" \
  "ds_read_b128 v[180:183], v212 offset:16640\n\t" \
  "s_waitcnt lgkmcnt(6)\n\t" \
  "v_mfma_f32_16x16x32_bf16 v[144:147], v[8:11], v[184:187], v[144:147]\n\t" \
  "v_mfma_f32_16x16x32_bf16 v[148:151], v[12:15], v[184:187], v[148:151]\n\t" \
  "ds_read_b128 v[184:187], v213 offset:256\n\t" \
  "s_add_u32 m0, %[lw], 2048\n\t" \
  "v_mfma_f32_16x16x32_bf16 v[152:155], v[8:11], v[188:191], v[152:155]\n\t" \
  "v_mfma_f32_16x16x32_bf16 v[156:159], v[12:15], v[188:191], v[156:159]\n\t" \
  "ds_read_b128 v[188:191], v213 offset:16640\n\t" \
  "global_load_lds_dwordx4 v218, s[92:93] sc1\n\t" \
  "ds_write_b128 v220, v[136:139] offset:0\n\t" \
  "ds_write_b128 v220, v[140:143] offset:1024\n\t" \
  "s_waitcnt lgkmcnt(8)\n\t" \
  "v_mfma_f32_16x16x32_bf16 v[144:147], v[16:19], v[192:195], v[144:147]\n\t" \
  "v_mfma_f32_16x16x32_bf16 v[148:151], v[20:23], v[192:195], v[148:151]\n\t" \
  "ds_read_b128 v[192:195], v214 offset:256\n\t" \
  "v_mfma_f32_16x16x32_bf16 v[152:155], v[16:19], v[196:199], v[152:155]\n\t" \
  "v_mfma_f32_16x16x32_bf16 v[156:159], v[20:23], v[196:199], v[156:159]\n\t" \
  "ds_read_b128 v[196:199], v214 offset:16640\n\t" \
  "s_lshr_b32 s90, s87, 7\n\t" \
  "s_xor_b32 s90, s90, %[s0]\n\t" \
  "s_and_b32 s90, s90, 1\n\t" \
  "s_lshl1_add_u32 s90, s87, s90\n\t" \
  "s_lshl_b32 s90, s90, 13\n\t" \
  "s_cmp_lt_i32 s86, 3\n\t" \
  "s_cselect_b32 s90, 0x70000000, s90\n\t" \
  "s_waitcnt lgkmcnt(8)\n\t" \
  "v_mfma_f32_16x16x32_bf16 v[144:147], v[24:27], v[200:203], v[144:147]\n\t" \
  "v_mfma_f32_16x16x32_bf16 v[148:151], v[28:31], v[200:203], v[148:151]\n\t" \
  "ds_read_b128 v[200:203], v215 offset:256\n\t" \
  "s_add_u32 m0, %[lw], 3072\n\t" \
  "v_mfma_f32_16x16x32_bf16 v[152:155], v[24:27], v[204:207], v[152:155]\n\t" \
  "v_mfma_f32_16x16x32_bf16 v[156:159], v[28:31], v[204:207], v[156:159]\n\t" \
  "ds_read_b128 v[204:207], v215 offset:16640\n\t" \
  "global_load_lds_dwordx4 v219, s[92:93] sc1\n\t" \
  "s_waitcnt lgkmcnt(4)\n\t" \
  "s_waitcnt vmcnt(4)\n\t" \
  "s_barrier\n\t" \
  ".Lg21%=:\n\t" \
  "s_add_u32 s92, s92, 512\n\t" \
  "s_addc_u32 s93, s93, 0\n\t" \
  "v_mfma_f32_16x16x32_bf16 v[144:147], v[32:35], v[176:179], v[144:147]\n\t" \
  "v_mfma_f32_16x16x32_bf16 v[148:151], v[36:39], v[176:179], v[148:151]\n\t" \
  "ds_read_b128 v[176:179], v212 offset:32768\n\t" \
  "v_mfma_f32_16x16x32_bf16 v[152:155], v[32:35], v[180:183], v[152:155]\n\t" \
  "v_mfma_f32_16x16x32_bf16 v[156:159], v[36:39], v[180:183], v[156:159]\n\t" \
  "ds_read_b128 v[180:183], v212 offset:49152\n\t" \
  "ds_read_b128 v[160:163], v221 offset:0\n\t" \
  "ds_read_b128 v[164:167], v221 offset:1024\n\t" \
  "s_sub_i32 s91, s86, 2\n\t" \
  "s_max_i32 s88, s88, s91\n\t" \
  "v_mov_b32 v173, s88\n\t" \
  "buffer_store_dword v173, v174, %[rs], 0 offen sc1\n\t" \
  "v_mfma_f32_16x16x32_bf16 v[144:147], v[40:43], v[184:187], v[144:147]\n\t" \
  "v_mfma_f32_16x16x32_bf16 v[148:151], v[44:47], v[184:187], v[148:151]\n\t" \
  "ds_read_b128 v[184:187], v213 offset:32768\n\t" \
  "s_add_u32 m0, %[lw], 32768\n\t" \
  "v_mfma_f32_16x16x32_bf16 v[152:155], v[40:43], v[188:191], v[152:155]\n\t" \
  "v_mfma_f32_16x16x32_bf16 v[156:159], v[44:47], v[188:191], v[156:159]\n\t" \
  "ds_read_b128 v[188:191], v213 offset:49152\n\t" \
  "global_load_lds_dwordx4 v216, s[92:93] sc1\n\t" \
  "s_waitcnt lgkmcnt(8)\n\t" \
  "v_mfma_f32_16x16x32_bf16 v[144:147], v[48:51], v[192:195], v[144:147]\n\t" \
  "v_mfma_f32_16x16x32_bf16 v[148:151], v[52:55], v[192:195], v[148:151]\n\t" \
  "ds_read_b128 v[192:195], v214 offset:32768\n\t" \
  "v_mfma_f32_16x16x32_bf16 v[152:155], v[48:51], v[196:199], v[152:155]\n\t" \
  "v_mfma_f32_16x16x32_bf16 v[156:159], v[52:55], v[196:199], v[156:159]\n\t" \
  "ds_read_b128 v[196:199], v214 offset:49152\n\t" \
  "s_waitcnt lgkmcnt(4)\n\t" \
  "v_add_f32 v160, v160, v128\n\t" \
  "v_add_f32 v161, v161, v129\n\t" \
  "v_add_f32 v162, v162, v130\n\t" \
  "v_add_f32 v163, v163, v131\n\t" \
  "v_add_f32 v164, v164, v132\n\t" \
  "v_add_f32 v165, v165, v133\n\t" \
  "v_add_f32 v166, v166, v134\n\t" \
  "v_add_f32 v167, v167, v135\n\t" \
  "v_mfma_f32_16x16x32_bf16 v[144:147], v[56:59], v[200:203], v[144:147]\n\t" \
  "v_mfma_f32_16x16x32_bf16 v[148:151], v[60:63], v[200:203], v[148:151]\n\t" \
  "ds_read_b128 v[200:203], v215 offset:32768\n\t" \
  "s_add_u32 m0, %[lw], 33792\n\t" \
  "v_mfma_f32_16x16x32_bf16 v[152:155], v[56:59], v[204:207], v[152:155]\n\t" \
  "v_mfma_f32_16x16x32_bf16 v[156:159], v[60:63], v[204:207], v[156:159]\n\t" \
  "ds_read_b128 v[204:207], v215 offset:49152\n\t" \
  "global_load_lds_dwordx4 v217, s[92:93] sc1\n\t" \
  "v_cvt_pk_bf16_f32 v168, v160, v161\n\t" \
  "v_cvt_pk_bf16_f32 v169, v162, v163\n\t" \
  "v_cvt_pk_bf16_f32 v170, v164, v165\n\t" \
  "v_cvt_pk_bf16_f32 v171, v166, v167\n\t" \
  "buffer_store_dwordx4 v[168:171], v172, %[rt], s90 offen sc1\n\t" \
  "v_mfma_f32_16x16x32_bf16 v[144:147], v[64:67], v[176:179], v[144:147]\n\t" \
  "v_mfma_f32_16x16x32_bf16 v[148:151], v[68:71], v[176:179], v[148:151]\n\t" \
  "ds_read_b128 v[176:179], v212 offset:33024\n\t" \
  "v_mfma_f32_16x16x32_bf16 v[152:155], v[64:67], v[180:183], v[152:155]\n\t" \
  "v_mfma_f32_16x16x32_bf16 v[156:159], v[68:71], v[180:183], v[156:159]\n\t" \
  "ds_read_b128 v[180:183], v212 offset:49408\n\t" \
  "s_waitcnt lgkmcnt(6)\n\t" \
  "v_mfma_f32_16x16x32_bf16 v[144:147], v[72:75], v[184:187], v[144:147]\n\t" \
  "v_mfma_f32_16x16x32_bf16 v[148:151], v[76:79], v[184:187], v[148:151]\n\t" \
  "ds_read_b128 v[184:187], v213 offset:33024\n\t" \
  "s_add_u32 m0, %[lw], 34816\n\t" \
  "v_mfma_f32_16x16x32_bf16 v[152:155], v[72:75], v[188:191], v[152:155]\n\t" \
  "v_mfma_f32_16x16x32_bf16 v[156:159], v[76:79], v[188:191], v[156:159]\n\t" \
  "ds_read_b128 v[188:191], v213 offset:49408\n\t" \
  "global_load_lds_dwordx4 v218, s[92:93] sc1\n\t" \
  "s_waitcnt lgkmcnt(6)\n\t" \
  "v_mfma_f32_16x16x32_bf16 v[144:147], v[80:83], v[192:195], v[144:147]\n\t" \
  "v_mfma_f32_16x16x32_bf16 v[148:151], v[84:87], v[192:195], v[148:151]\n\t" \
  "ds_read_b128 v[192:195], v214 offset:33024\n\t" \
  "v_mfma_f32_16x16x32_bf16 v[152:155], v[80:83], v[196:199], v[152:155]\n\t" \
  "v_mfma_f32_16x16x32_bf16 v[156:159], v[84:87], v[196:199], v[156:159]\n\t" \
  "ds_read_b128 v[196:199], v214 offset:49408\n\t" \
  "s_waitcnt lgkmcnt(6)\n\t" \
  "v_mfma_f32_16x16x32_bf16 v[144:147], v[88:91], v[200:203], v[144:147]\n\t" \
  "v_mfma_f32_16x16x32_bf16 v[148:151], v[92:95], v[200:203], v[148:151]\n\t" \
  "ds_read_b128 v[200:203], v215 offset:33024\n\t" \
  "s_add_u32 m0, %[lw], 35840\n\t" \
  "v_mfma_f32_16x16x32_bf16 v[152:155], v[88:91], v[204:207], v[152:155]\n\t" \
  "v_mfma_f32_16x16x32_bf16 v[156:159], v[92:95], v[204:207], v[156:159]\n\t" \
  "ds_read_b128 v[204:207], v215 offset:49408\n\t" \
  "global_load_lds_dwordx4 v219, s[92:93] sc1\n\t" \
  "s_cmp_lt_u32 s86, 6\n\t" \
  "s_cbranch_scc1 .Lw31%=\n\t" \
  "s_waitcnt vmcnt(6)\n\t" \
  "s_barrier\n\t" \
  ".Lg31%=:\n\t" \
  "s_add_u32 s92, s92, 65024\n\t" \
  "s_addc_u32 s93, s93, 0\n\t" \
  "s_waitcnt lgkmcnt(6)\n\t" \
  "v_mfma_f32_16x16x32_bf16 v[144:147], v[96:99], v[176:179], v[144:147]\n\t" \
  "v_mfma_f32_16x16x32_bf16 v[148:151], v[100:103], v[176:179], v[148:151]\n\t" \
  "ds_read_b128 v[176:179], v208 offset:0\n\t" \
  "v_mfma_f32_16x16x32_bf16 v[152:155], v[96:99], v[180:183], v[152:155]\n\t" \
  "v_mfma_f32_16x16x32_bf16 v[156:159], v[100:103], v[180:183], v[156:159]\n\t" \
  "ds_read_b128 v[180:183], v208 offset:16384\n\t" \
  "s_waitcnt lgkmcnt(6)\n\t" \
  "v_mfma_f32_16x16x32_bf16 v[144:147], v[104:107], v[184:187], v[144:147]\n\t" \
  "v_mfma_f32_16x16x32_bf16 v[148:151], v[108:111], v[184:187], v[148:151]\n\t" \
  "ds_read_b128 v[184:187], v209 offset:0\n\t" \
  "s_add_u32 m0, %[lw], 65536\n\t" \
  "v_mfma_f32_16x16x32_bf16 v[152:155], v[104:107], v[188:191], v[152:155]\n\t" \
  "v_mfma_f32_16x16x32_bf16 v[156:159], v[108:111], v[188:191], v[156:159]\n\t" \
  "ds_read_b128 v[188:191], v209 offset:16384\n\t" \
  "global_load_lds_dwordx4 v216, s[92:93] sc1\n\t" \
  "s_waitcnt lgkmcnt(6)\n\t" \
  "v_mfma_f32_16x16x32_bf16 v[144:147], v[112:115], v[192:195], v[144:147]\n\t" \
  "v_mfma_f32_16x16x32_bf16 v[148:151], v[116:119], v[192:195], v[148:151]\n\t" \
  "ds_read_b128 v[192:195], v210 offset:0\n\t" \
  "v_mfma_f32_16x16x32_bf16 v[152:155], v[112:115], v[196:199], v[152:155]\n\t" \
  "v_mfma_f32_16x16x32_bf16 v[156:159], v[116:119], v[196:199], v[156:159]\n\t" \
  "ds_read_b128 v[196:199], v210 offset:16384\n\t" \
  "s_waitcnt lgkmcnt(6)\n\t" \
  "v_mfma_f32_16x16x32_bf16 v[144:147], v[120:123], v[200:203], v[144:147]\n\t" \
  "v_mfma_f32_16x16x32_bf16 v[148:151], v[124:127], v[200:203], v[148:151]\n\t" \
  "ds_read_b128 v[200:203], v211 offset:0\n\t" \
  "s_add_u32 m0, %[lw], 66560\n\t" \
  "v_mfma_f32_16x16x32_bf16 v[152:155], v[120:123], v[204:207], v[152:155]\n\t" \
  "v_mfma_f32_16x16x32_bf16 v[156:159], v[124:127], v[204:207], v[156:159]\n\t" \
  "ds_read_b128 v[204:207], v211 offset:16384\n\t" \
  "global_load_lds_dwordx4 v217, s[92:93] sc1\n\t" \
  "s_add_u32 s86, s86, 1\n\t" \
  "s_add_u32 s87, s87, 1\n\t" \
  "s_cmp_eq_u32 s86, 2\n\t" \
  "s_cbranch_scc1 .Ll1%=\n\t" \
  "s_cmp_lt_u32 s86, %[end]\n\t" \
  "s_cbranch_scc1 .Lp1%=\n\t" \
  "s_cmp_lg_u32 s86, 64\n\t" \
  "s_cbranch_scc1 .Lx1%=\n\t" \
  ".Ll1%=:\n\t" \
  "s_nop 7\n\t" \
  "ds_write_b128 v220, v[152:155] offset:0\n\t" \
  "ds_write_b128 v220, v[156:159] offset:1024\n\t" \
  "s_lshr_b32 s90, s87, 7\n\t" \
  "s_xor_b32 s90, s90, %[s0]\n\t" \
  "s_and_b32 s90, s90, 1\n\t" \
  "s_lshl1_add_u32 s90, s87, s90\n\t" \
  "s_lshl_b32 s90, s90, 13\n\t" \
  "s_cmp_lt_i32 s86, 1\n\t" \
  "s_cselect_b32 s90, 0x70000000, s90\n\t" \
  "s_waitcnt lgkmcnt(0)\n\t" \
  "s_barrier\n\t" \
  "ds_read_b128 v[160:163], v221 offset:0\n\t" \
  "ds_read_b128 v[164:167], v221 offset:1024\n\t" \
  "s_waitcnt lgkmcnt(0)\n\t" \
  "v_add_f32 v160, v160, v144\n\t" \
  "v_add_f32 v161, v161, v145\n\t" \
  "v_add_f32 v162, v162, v146\n\t" \
  "v_add_f32 v163, v163, v147\n\t" \
  "v_add_f32 v164, v164, v148\n\t" \
  "v_add_f32 v165, v165, v149\n\t" \
  "v_add_f32 v166, v166, v150\n\t" \
  "v_add_f32 v167, v167, v151\n\t" \
  "v_cvt_pk_bf16_f32 v168, v160, v161\n\t" \
  "v_cvt_pk_bf16_f32 v169, v162, v163\n\t" \
  "v_cvt_pk_bf16_f32 v170, v164, v165\n\t" \
  "v_cvt_pk_bf16_f32 v171, v166, v167\n\t" \
  "buffer_store_dwordx4 v[168:171], v172, %[rt], s90 offen sc1\n\t" \
  "s_waitcnt vmcnt(0)\n\t" \
  "s_barrier\n\t" \
  "s_sub_i32 s91, s86, 1\n\t" \
  "s_max_i32 s88, s88, s91\n\t" \
  "v_mov_b32 v173, s88\n\t" \
  "buffer_store_dword v173, v174, %[rs], 0 offen sc1\n\t" \
  "s_cmp_eq_u32 s86, 2\n\t" \
  "s_cbranch_scc1 .Lp1%=\n\t" \
  "s_branch .Lx1%=\n\t" \
  ".Lf1%=:\n\t" \
  "s_nop 7\n\t" \
  "ds_write_b128 v220, v[136:139] offset:0\n\t" \
  "ds_write_b128 v220, v[140:143] offset:1024\n\t" \
  "s_lshr_b32 s90, s87, 7\n\t" \
  "s_xor_b32 s90, s90, %[s0]\n\t" \
  "s_and_b32 s90, s90, 1\n\t" \
  "s_lshl1_add_u32 s90, s87, s90\n\t" \
  "s_lshl_b32 s90, s90, 13\n\t" \
  "s_cmp_lt_i32 s86, 1\n\t" \
  "s_cselect_b32 s90, 0x70000000, s90\n\t" \
  "s_waitcnt lgkmcnt(0)\n\t" \
  "s_barrier\n\t" \
  "ds_read_b128 v[160:163], v221 offset:0\n\t" \
  "ds_read_b128 v[164:167], v221 offset:1024\n\t" \
  "s_waitcnt lgkmcnt(0)\n\t" \
  "v_add_f32 v160, v160, v128\n\t" \
  "v_add_f32 v161, v161, v129\n\t" \
  "v_add_f32 v162, v162, v130\n\t" \
  "v_add_f32 v163, v163, v131\n\t" \
  "v_add_f32 v164, v164, v132\n\t" \
  "v_add_f32 v165, v165, v133\n\t" \
  "v_add_f32 v166, v166, v134\n\t" \
  "v_add_f32 v167, v167, v135\n\t" \
  "v_cvt_pk_bf16_f32 v168, v160, v161\n\t" \
  "v_cvt_pk_bf16_f32 v169, v162, v163\n\t" \
  "v_cvt_pk_bf16_f32 v170, v164, v165\n\t" \
  "v_cvt_pk_bf16_f32 v171, v166, v167\n\t" \
  "buffer_store_dwordx4 v[168:171], v172, %[rt], s90 offen sc1\n\t" \
  "s_waitcnt vmcnt(0)\n\t" \
  "s_barrier\n\t" \
  "s_sub_i32 s91, s86, 1\n\t" \
  "s_max_i32 s88, s88, s91\n\t" \
  "v_mov_b32 v173, s88\n\t" \
  "buffer_store_dword v173, v174, %[rs], 0 offen sc1\n\t" \
  "s_branch .Lfb1%=\n\t" \
  ".Lw11%=:\n\t" \
  "s_waitcnt vmcnt(2)\n\t" \
  "s_barrier\n\t" \
  "s_sub_i32 s91, s86, 1\n\t" \
  "s_max_i32 s88, s88, s91\n\t" \
  "v_mov_b32 v173, s88\n\t" \
  "buffer_store_dword v173, v174, %[rs], 0 offen sc1\n\t" \
  "s_branch .Lg11%=\n\t" \
  ".Lw31%=:\n\t" \
  "s_waitcnt vmcnt(2)\n\t" \
  "s_barrier\n\t" \
  "s_sub_i32 s91, s86, 1\n\t" \
  "s_max_i32 s88, s88, s91\n\t" \
  "v_mov_b32 v173, s88\n\t" \
  "buffer_store_dword v173, v174, %[rs], 0 offen sc1\n\t" \
  "s_branch .Lg31%=\n\t" \
  ".Lx1%=:\n\t" \
  "s_waitcnt lgkmcnt(0)\n\t" \
  "s_setprio 0\n\t" \
  "s_mov_b32 m0, s94\n\t" \
  ".Le%=:\n\t"
#define PR_ASM_1 \
  "s_cmp_lg_u32 %[kh], 0\n\t" \
  "s_cbranch_scc1 .Lk%=\n\t" \
  "s_mov_b32 s94, m0\n\t" \
  "s_mov_b64 s[92:93], %[x0]\n\t" \
  "s_mov_b32 s86, %[g0]\n\t" \
  "s_add_u32 s87, s86, %[t0]\n\t" \
  "s_sub_i32 s88, s86, 3\n\t" \
  "ds_read_b128 v[176:179], v208 offset:0\n\t" \
  "ds_read_b128 v[180:183], v208 offset:16384\n\t" \
  "ds_read_b128 v[184:187], v209 offset:0\n\t" \
  "ds_read_b128 v[188:191], v209 offset:16384\n\t" \
  "ds_read_b128 v[192:195], v210 offset:0\n\t" \
  "ds_read_b128 v[196:199], v210 offset:16384\n\t" \
  "ds_read_b128 v[200:203], v211 offset:0\n\t" \
  "ds_read_b128 v[204:207], v211 offset:16384\n\t" \
  ".Lp0%=:\n\t" \
  "s_waitcnt lgkmcnt(6)\n\t" \
  "v_mfma_f32_16x16x32_bf16 v[128:131], v[176:179], v[0:3], 0\n\t" \
  "v_mfma_f32_16x16x32_bf16 v[136:139], v[176:179], v[4:7], 0\n\t" \
  "ds_read_b128 v[176:179], v208 offset:256\n\t" \
  "s_add_u32 m0, %[lw], 67584\n\t" \
  "v_mfma_f32_16x16x32_bf16 v[132:135], v[180:183], v[0:3], 0\n\t" \
  "v_mfma_f32_16x16x32_bf16 v[140:143], v[180:183], v[4:7], 0\n\t" \
  "ds_read_b128 v[180:183], v208 offset:16640\n\t" \
  "global_load_lds_dwordx4 v218, s[92:93] sc1\n\t" \
  "s_waitcnt lgkmcnt(6)\n\t" \
  "v_mfma_f32_16x16x32_bf16 v[128:131], v[184:187], v[8:11], v[128:131]\n\t" \
  "v_mfma_f32_16x16x32_bf16 v[136:139], v[184:187], v[12:15], v[136:139]\n\t" \
  "ds_read_b128 v[184:187], v209 offset:256\n\t" \
  "v_mfma_f32_16x16x32_bf16 v[132:135], v[188:191], v[8:11], v[132:135]\n\t" \
  "v_mfma_f32_16x16x32_bf16 v[140:143], v[188:191], v[12:15], v[140:143]\n\t" \
  "ds_read_b128 v[188:191], v209 offset:16640\n\t" \
  "ds_write_b128 v220, v[152:155] offset:0\n\t" \
  "ds_write_b128 v220, v[156:159] offset:1024\n\t" \
  "s_waitcnt lgkmcnt(8)\n\t" \
  "v_mfma_f32_16x16x32_bf16 v[128:131], v[192:195], v[16:19], v[128:131]\n\t" \
  "v_mfma_f32_16x16x32_bf16 v[136:139], v[192:195], v[20:23], v[136:139]\n\t" \
  "ds_read_b128 v[192:195], v210 offset:256\n\t" \
  "s_add_u32 m0, %[lw], 68608\n\t" \
  "v_mfma_f32_16x16x32_bf16 v[132:135], v[196:199], v[16:19], v[132:135]\n\t" \
  "v_mfma_f32_16x16x32_bf16 v[140:143], v[196:199], v[20:23], v[140:143]\n\t" \
  "ds_read_b128 v[196:199], v210 offset:16640\n\t" \
  "global_load_lds_dwordx4 v219, s[92:93] sc1\n\t" \
  "s_lshr_b32 s90, s87, 7\n\t" \
  "s_xor_b32 s90, s90, %[s0]\n\t" \
  "s_and_b32 s90, s90, 1\n\t" \
  "s_lshl1_add_u32 s90, s87, s90\n\t" \
  "s_lshl_b32 s90, s90, 13\n\t" \
  "s_cmp_lt_i32 s86, 3\n\t" \
  "s_cselect_b32 s90, 0x70000000, s90\n\t" \
  "s_waitcnt lgkmcnt(8)\n\t" \
  "v_mfma_f32_16x16x32_bf16 v[128:131], v[200:203], v[24:27], v[128:131]\n\t" \
  "v_mfma_f32_16x16x32_bf16 v[136:139], v[200:203], v[28:31], v[136:139]\n\t" \
  "ds_read_b128 v[200:203], v211 offset:256\n\t" \
  "v_mfma_f32_16x16x32_bf16 v[132:135], v[204:207], v[24:27], v[132:135]\n\t" \
  "v_mfma_f32_16x16x32_bf16 v[140:143], v[204:207], v[28:31], v[140:143]\n\t" \
  "ds_read_b128 v[204:207], v211 offset:16640\n\t" \
  "s_waitcnt lgkmcnt(4)\n\t" \
  "s_waitcnt vmcnt(4)\n\t" \
  "s_barrier\n\t" \
  ".Lg00%=:\n\t" \
  "s_add_u32 s92, s92, 512\n\t" \
  "s_addc_u32 s93, s93, 0\n\t" \
  "v_mfma_f32_16x16x32_bf16 v[128:131], v[176:179], v[32:35], v[128:131]\n\t" \
  "v_mfma_f32_16x16x32_bf16 v[136:139], v[176:179], v[36:39], v[136:139]\n\t" \
  "ds_read_b128 v[176:179], v208 offset:32768\n\t" \
  "s_add_u32 m0, %[lw], 98304\n\t" \
  "v_mfma_f32_16x16x32_bf16 v[132:135], v[180:183], v[32:35], v[132:135]\n\t" \
  "v_mfma_f32_16x16x32_bf16 v[140:143], v[180:183], v[36:39], v[140:143]\n\t" \
  "ds_read_b128 v[180:183], v208 offset:49152\n\t" \
  "global_load_lds_dwordx4 v216, s[92:93] sc1\n\t" \
  "ds_read_b128 v[160:163], v221 offset:0\n\t" \
  "ds_read_b128 v[164:167], v221 offset:1024\n\t" \
  "v_mfma_f32_16x16x32_bf16 v[128:131], v[184:187], v[40:43], v[128:131]\n\t" \
  "v_mfma_f32_16x16x32_bf16 v[136:139], v[184:187], v[44:47], v[136:139]\n\t" \
  "ds_read_b128 v[184:187], v209 offset:32768\n\t" \
  "v_mfma_f32_16x16x32_bf16 v[132:135], v[188:191], v[40:43], v[132:135]\n\t" \
  "v_mfma_f32_16x16x32_bf16 v[140:143], v[188:191], v[44:47], v[140:143]\n\t" \
  "ds_read_b128 v[188:191], v209 offset:49152\n\t" \
  "s_waitcnt lgkmcnt(8)\n\t" \
  "v_mfma_f32_16x16x32_bf16 v[128:131], v[192:195], v[48:51], v[128:131]\n\t" \
  "v_mfma_f32_16x16x32_bf16 v[136:139], v[192:195], v[52:55], v[136:139]\n\t" \
  "ds_read_b128 v[192:195], v210 offset:32768\n\t" \
  "s_add_u32 m0, %[lw], 99328\n\t" \
  "v_mfma_f32_16x16x32_bf16 v[132:135], v[196:199], v[48:51], v[132:135]\n\t" \
  "v_mfma_f32_16x16x32_bf16 v[140:143], v[196:199], v[52:55], v[140:143]\n\t" \
  "ds_read_b128 v[196:199], v210 offset:49152\n\t" \
  "global_load_lds_dwordx4 v217, s[92:93] sc1\n\t" \
  "s_waitcnt lgkmcnt(4)\n\t" \
  "v_add_f32 v160, v160, v144\n\t" \
  "v_add_f32 v161, v161, v145\n\t" \
  "v_add_f32 v162, v162, v146\n\t" \
  "v_add_f32 v163, v163, v147\n\t" \
  "v_add_f32 v164, v164, v148\n\t" \
  "v_add_f32 v165, v165, v149\n\t" \
  "v_add_f32 v166, v166, v150\n\t" \
  "v_add_f32 v167, v167, v151\n\t" \
  "v_mfma_f32_16x16x32_bf16 v[128:131], v[200:203], v[56:59], v[128:131]\n\t" \
  "v_mfma_f32_16x16x32_bf16 v[136:139], v[200:203], v[60:63], v[136:139]\n\t" \
  "ds_read_b128 v[200:203], v211 offset:32768\n\t" \
  "v_mfma_f32_16x16x32_bf16 v[132:135], v[204:207], v[56:59], v[132:135]\n\t" \
  "v_mfma_f32_16x16x32_bf16 v[140:143], v[204:207], v[60:63], v[140:143]\n\t" \
  "ds_read_b128 v[204:207], v211 offset:49152\n\t" \
  "v_cvt_pk_bf16_f32 v168, v160, v161\n\t" \
  "v_cvt_pk_bf16_f32 v169, v162, v163\n\t" \
  "v_cvt_pk_bf16_f32 v170, v164, v165\n\t" \
  "v_cvt_pk_bf16_f32 v171, v166, v167\n\t" \
  "buffer_store_dwordx4 v[168:171], v172, %[rt], s90 offen sc1\n\t" \
  "v_mfma_f32_16x16x32_bf16 v[128:131], v[176:179], v[64:67], v[128:131]\n\t" \
  "v_mfma_f32_16x16x32_bf16 v[136:139], v[176:179], v[68:71], v[136:139]\n\t" \
  "ds_read_b128 v[176:179], v208 offset:33024\n\t" \
  "s_add_u32 m0, %[lw], 100352\n\t" \
  "v_mfma_f32_16x16x32_bf16 v[132:135], v[180:183], v[64:67], v[132:135]\n\t" \
  "v_mfma_f32_16x16x32_bf16 v[140:143], v[180:183], v[68:71], v[140:143]\n\t" \
  "ds_read_b128 v[180:183], v208 offset:49408\n\t" \
  "global_load_lds_dwordx4 v218, s[92:93] sc1\n\t" \
  "s_waitcnt lgkmcnt(6)\n\t" \
  "v_mfma_f32_16x16x32_bf16 v[128:131], v[184:187], v[72:75], v[128:131]\n\t" \
  "v_mfma_f32_16x16x32_bf16 v[136:139], v[184:187], v[76:79], v[136:139]\n\t" \
  "ds_read_b128 v[184:187], v209 offset:33024\n\t" \
  "v_mfma_f32_16x16x32_bf16 v[132:135], v[188:191], v[72:75], v[132:135]\n\t" \
  "v_mfma_f32_16x16x32_bf16 v[140:143], v[188:191], v[76:79], v[140:143]\n\t" \
  "ds_read_b128 v[188:191], v209 offset:49408\n\t" \
  "s_waitcnt lgkmcnt(6)\n\t" \
  "v_mfma_f32_16x16x32_bf16 v[128:131], v[192:195], v[80:83], v[128:131]\n\t" \
  "v_mfma_f32_16x16x32_bf16 v[136:139], v[192:195], v[84:87], v[136:139]\n\t" \
  "ds_read_b128 v[192:195], v210 offset:33024\n\t" \
  "s_add_u32 m0, %[lw], 101376\n\t" \
  "v_mfma_f32_16x16x32_bf16 v[132:135], v[196:199], v[80:83], v[132:135]\n\t" \
  "v_mfma_f32_16x16x32_bf16 v[140:143], v[196:199], v[84:87], v[140:143]\n\t" \
  "ds_read_b128 v[196:199], v210 offset:49408\n\t" \
  "global_load_lds_dwordx4 v219, s[92:93] sc1\n\t" \
  "s_waitcnt lgkmcnt(6)\n\t" \
  "v_mfma_f32_16x16x32_bf16 v[128:131], v[200:203], v[88:91], v[128:131]\n\t" \
  "v_mfma_f32_16x16x32_bf16 v[136:139], v[200:203], v[92:95], v[136:139]\n\t" \
  "ds_read_b128 v[200:203], v211 offset:33024\n\t" \
  "v_mfma_f32_16x16x32_bf16 v[132:135], v[204:207], v[88:91], v[132:135]\n\t" \
  "v_mfma_f32_16x16x32_bf16 v[140:143], v[204:207], v[92:95], v[140:143]\n\t" \
  "ds_read_b128 v[204:207], v211 offset:49408\n\t" \
  "s_cmp_lt_u32 s86, 6\n\t" \
  "s_cbranch_scc1 .Lw10%=\n\t" \
  "s_waitcnt vmcnt(5)\n\t" \
  "s_barrier\n\t" \
  ".Lg10%=:\n\t" \
  "s_add_u32 s92, s92, 65024\n\t" \
  "s_addc_u32 s93, s93, 0\n\t" \
  "s_waitcnt lgkmcnt(6)\n\t" \
  "v_mfma_f32_16x16x32_bf16 v[128:131], v[176:179], v[96:99], v[128:131]\n\t" \
  "v_mfma_f32_16x16x32_bf16 v[136:139], v[176:179], v[100:103], v[136:139]\n\t" \
  "ds_read_b128 v[176:179], v212 offset:0\n\t" \
  "s_add_u32 m0, %[lw], 0\n\t" \
  "v_mfma_f32_16x16x32_bf16 v[132:135], v[180:183], v[96:99], v[132:135]\n\t" \
  "v_mfma_f32_16x16x32_bf16 v[140:143], v[180:183], v[100:103], v[140:143]\n\t" \
  "ds_read_b128 v[180:183], v212 offset:16384\n\t" \
  "global_load_lds_dwordx4 v216, s[92:93] sc1\n\t" \
  "s_waitcnt lgkmcnt(6)\n\t" \
  "v_mfma_f32_16x16x32_bf16 v[128:131], v[184:187], v[104:107], v[128:131]\n\t" \
  "v_mfma_f32_16x16x32_bf16 v[136:139], v[184:187], v[108:111], v[136:139]\n\t" \
  "ds_read_b128 v[184:187], v213 offset:0\n\t" \
  "v_mfma_f32_16x16x32_bf16 v[132:135], v[188:191], v[104:107], v[132:135]\n\t" \
  "v_mfma_f32_16x16x32_bf16 v[140:143], v[188:191], v[108:111], v[140:143]\n\t" \
  "ds_read_b128 v[188:191], v213 offset:16384\n\t" \
  "s_waitcnt lgkmcnt(6)\n\t" \
  "v_mfma_f32_16x16x32_bf16 v[128:131], v[192:195], v[112:115], v[128:131]\n\t" \
  "v_mfma_f32_16x16x32_bf16 v[136:139], v[192:195], v[116:119], v[136:139]\n\t" \
  "ds_read_b128 v[192:195], v214 offset:0\n\t" \
  "s_add_u32 m0, %[lw], 1024\n\t" \
  "v_mfma_f32_16x16x32_bf16 v[132:135], v[196:199], v[112:115], v[132:135]\n\t" \
  "v_mfma_f32_16x16x32_bf16 v[140:143], v[196:199], v[116:119], v[140:143]\n\t" \
  "ds_read_b128 v[196:199], v214 offset:16384\n\t" \
  "global_load_lds_dwordx4 v217, s[92:93] sc1\n\t" \
  "s_waitcnt lgkmcnt(6)\n\t" \
  "v_mfma_f32_16x16x32_bf16 v[128:131], v[200:203], v[120:123], v[128:131]\n\t" \
  "v_mfma_f32_16x16x32_bf16 v[136:139], v[200:203], v[124:127], v[136:139]\n\t" \
  "ds_read_b128 v[200:203], v215 offset:0\n\t" \
  "v_mfma_f32_16x16x32_bf16 v[132:135], v[204:207], v[120:123], v[132:135]\n\t" \
  "v_mfma_f32_16x16x32_bf16 v[140:143], v[204:207], v[124:127], v[140:143]\n\t" \
  "ds_read_b128 v[204:207], v215 offset:16384\n\t" \
  "s_add_u32 s86, s86, 1\n\t" \
  "s_add_u32 s87, s87, 1\n\t" \
  "s_cmp_eq_u32 s86, 1\n\t" \
  "s_cbranch_scc1 .Lf0%=\n\t" \
  ".Lfb0%=:\n\t" \
  "s_waitcnt lgkmcnt(6)\n\t" \
  "v_mfma_f32_16x16x32_bf16 v[144:147], v[176:179], v[0:3], 0\n\t" \
  "v_mfma_f32_16x16x32_bf16 v[152:155], v[176:179], v[4:7], 0\n\t" \
  "ds_read_b128 v[176:179], v212 offset:256\n\t" \
  "s_add_u32 m0, %[lw], 2048\n\t" \
  "v_mfma_f32_16x16x32_bf16 v[148:151], v[180:183], v[0:3], 0\n\t" \
  "v_mfma_f32_16x16x32_bf16 v[156:159], v[180:183], v[4:7], 0\n\t" \
  "ds_read_b128 v[180:183], v212 offset:16640\n\t" \
  "global_load_lds_dwordx4 v218, s[92:93] sc1\n\t" \
  "s_waitcnt lgkmcnt(6)\n\t" \
  "v_mfma_f32_16x16x32_bf16 v[144:147], v[184:187], v[8:11], v[144:147]\n\t" \
  "v_mfma_f32_16x16x32_bf16 v[152:155], v[184:187], v[12:15], v[152:155]\n\t" \
  "ds_read_b128 v[184:187], v213 offset:256\n\t" \
  "v_mfma_f32_16x16x32_bf16 v[148:151], v[188:191], v[8:11], v[148:151]\n\t" \
  "v_mfma_f32_16x16x32_bf16 v[156:159], v[188:191], v[12:15], v[156:159]\n\t" \
  "ds_read_b128 v[188:191], v213 offset:16640\n\t" \
  "ds_write_b128 v220, v[136:139] offset:0\n\t" \
  "ds_write_b128 v220, v[140:143] offset:1024\n\t" \
  "s_waitcnt lgkmcnt(8)\n\t" \
  "v_mfma_f32_16x16x32_bf16 v[144:147], v[192:195], v[16:19], v[144:147]\n\t" \
  "v_mfma_f32_16x16x32_bf16 v[152:155], v[192:195], v[20:23], v[152:155]\n\t" \
  "ds_read_b128 v[192:195], v214 offset:256\n\t" \
  "s_add_u32 m0, %[lw], 3072\n\t" \
  "v_mfma_f32_16x16x32_bf16 v[148:151], v[196:199], v[16:19], v[148:151]\n\t" \
  "v_mfma_f32_16x16x32_bf16 v[156:159], v[196:199], v[20:23], v[156:159]\n\t" \
  "ds_read_b128 v[196:199], v214 offset:16640\n\t" \
  "global_load_lds_dwordx4 v219, s[92:93] sc1\n\t" \
  "s_lshr_b32 s90, s87, 7\n\t" \
  "s_xor_b32 s90, s90, %[s0]\n\t" \
  "s_and_b32 s90, s90, 1\n\t" \
  "s_lshl1_add_u32 s90, s87, s90\n\t" \
  "s_lshl_b32 s90, s90, 13\n\t" \
  "s_cmp_lt_i32 s86, 3\n\t" \
  "s_cselect_b32 s90, 0x70000000, s90\n\t" \
  "s_waitcnt lgkmcnt(8)\n\t" \
  "v_mfma_f32_16x16x32_bf16 v[144:147], v[200:203], v[24:27], v[144:147]\n\t" \
  "v_mfma_f32_16x16x32_bf16 v[152:155], v[200:203], v[28:31], v[152:155]\n\t" \
  "ds_read_b128 v[200:203], v215 offset:256\n\t" \
  "v_mfma_f32_16x16x32_bf16 v[148:151], v[204:207], v[24:27], v[148:151]\n\t" \
  "v_mfma_f32_16x16x32_bf16 v[156:159], v[204:207], v[28:31], v[156:159]\n\t" \
  "ds_read_b128 v[204:207], v215 offset:16640\n\t" \
  "s_waitcnt lgkmcnt(4)\n\t" \
  "s_waitcnt vmcnt(4)\n\t" \
  "s_barrier\n\t" \
  ".Lg20%=:\n\t" \
  "s_add_u32 s92, s92, 512\n\t" \
  "s_addc_u32 s93, s93, 0\n\t" \
  "v_mfma_f32_16x16x32_bf16 v[144:147], v[176:179], v[32:35], v[144:147]\n\t" \
  "v_mfma_f32_16x16x32_bf16 v[152:155], v[176:179], v[36:39], v[152:155]\n\t" \
  "ds_read_b128 v[176:179], v212 offset:32768\n\t" \
  "s_add_u32 m0, %[lw], 32768\n\t" \
  "v_mfma_f32_16x16x32_bf16 v[148:151], v[180:183], v[32:35], v[148:151]\n\t" \
  "v_mfma_f32_16x16x32_bf16 v[156:159], v[180:183], v[36:39], v[156:159]\n\t" \
  "ds_read_b128 v[180:183], v212 offset:49152\n\t" \
  "global_load_lds_dwordx4 v216, s[92:93] sc1\n\t" \
  "ds_read_b128 v[160:163], v221 offset:0\n\t" \
  "ds_read_b128 v[164:167], v221 offset:1024\n\t" \
  "v_mfma_f32_16x16x32_bf16 v[144:147], v[184:187], v[40:43], v[144:147]\n\t" \
  "v_mfma_f32_16x16x32_bf16 v[152:155], v[184:187], v[44:47], v[152:155]\n\t" \
  "ds_read_b128 v[184:187], v213 offset:32768\n\t" \
  "v_mfma_f32_16x16x32_bf16 v[148:151], v[188:191], v[40:43], v[148:151]\n\t" \
  "v_mfma_f32_16x16x32_bf16 v[156:159], v[188:191], v[44:47], v[156:159]\n\t" \
  "ds_read_b128 v[188:191], v213 offset:49152\n\t" \
  "s_waitcnt lgkmcnt(8)\n\t" \
  "v_mfma_f32_16x16x32_bf16 v[144:147], v[192:195], v[48:51], v[144:147]\n\t" \
  "v_mfma_f32_16x16x32_bf16 v[152:155], v[192:195], v[52:55], v[152:155]\n\t" \
  "ds_read_b128 v[192:195], v214 offset:32768\n\t" \
  "s_add_u32 m0, %[lw], 33792\n\t" \
  "v_mfma_f32_16x16x32_bf16 v[148:151], v[196:199], v[48:51], v[148:151]\n\t" \
  "v_mfma_f32_16x16x32_bf16 v[156:159], v[196:199], v[52:55], v[156:159]\n\t" \
  "ds_read_b128 v[196:199], v214 offset:49152\n\t" \
  "global_load_lds_dwordx4 v217, s[92:93] sc1\n\t" \
  "s_waitcnt lgkmcnt(4)\n\t" \
  "v_add_f32 v160, v160, v128\n\t" \
  "v_add_f32 v161, v161, v129\n\t" \
  "v_add_f32 v162, v162, v130\n\t" \
  "v_add_f32 v163, v163, v131\n\t" \
  "v_add_f32 v164, v164, v132\n\t" \
  "v_add_f32 v165, v165, v133\n\t" \
  "v_add_f32 v166, v166, v134\n\t" \
  "v_add_f32 v167, v167, v135\n\t" \
  "v_mfma_f32_16x16x32_bf16 v[144:147], v[200:203], v[56:59], v[144:147]\n\t" \
  "v_mfma_f32_16x16x32_bf16 v[152:155], v[200:203], v[60:63], v[152:155]\n\t" \
  "ds_read_b128 v[200:203], v215 offset:32768\n\t" \
  "v_mfma_f32_16x16x32_bf16 v[148:151], v[204:207], v[56:59], v[148:151]\n\t" \
  "v_mfma_f32_16x16x32_bf16 v[156:159], v[204:207], v[60:63], v[156:159]\n\t" \
  "ds_read_b128 v[204:207], v215 offset:49152\n\t" \
  "v_cvt_pk_bf16_f32 v168, v160, v161\n\t" \
  "v_cvt_pk_bf16_f32 v169, v162, v163\n\t" \
  "v_cvt_pk_bf16_f32 v170, v164, v165\n\t" \
  "v_cvt_pk_bf16_f32 v171, v166, v167\n\t" \
  "buffer_store_dwordx4 v[168:171], v172, %[rt], s90 offen sc1\n\t" \
  "v_mfma_f32_16x16x32_bf16 v[144:147], v[176:179], v[64:67], v[144:147]\n\t" \
  "v_mfma_f32_16x16x32_bf16 v[152:155], v[176:179], v[68:71], v[152:155]\n\t" \
  "ds_read_b128 v[176:179], v212 offset:33024\n\t" \
  "s_add_u32 m0, %[lw], 34816\n\t" \
  "v_mfma_f32_16x16x32_bf16 v[148:151], v[180:183], v[64:67], v[148:151]\n\t" \
  "v_mfma_f32_16x16x32_bf16 v[156:159], v[180:183], v[68:71], v[156:159]\n\t" \
  "ds_read_b128 v[180:183], v212 offset:49408\n\t" \
  "global_load_lds_dwordx4 v218, s[92:93] sc1\n\t" \
  "s_waitcnt lgkmcnt(6)\n\t" \
  "v_mfma_f32_16x16x32_bf16 v[144:147], v[184:187], v[72:75], v[144:147]\n\t" \
  "v_mfma_f32_16x16x32_bf16 v[152:155], v[184:187], v[76:79], v[152:155]\n\t" \
  "ds_read_b128 v[184:187], v213 offset:33024\n\t" \
  "v_mfma_f32_16x16x32_bf16 v[148:151], v[188:191], v[72:75], v[148:151]\n\t" \
  "v_mfma_f32_16x16x32_bf16 v[156:159], v[188:191], v[76:79], v[156:159]\n\t" \
  "ds_read_b128 v[188:191], v213 offset:49408\n\t" \
  "s_waitcnt lgkmcnt(6)\n\t" \
  "v_mfma_f32_16x16x32_bf16 v[144:147], v[192:195], v[80:83], v[144:147]\n\t" \
  "v_mfma_f32_16x16x32_bf16 v[152:155], v[192:195], v[84:87], v[152:155]\n\t" \
  "ds_read_b128 v[192:195], v214 offset:33024\n\t" \
  "s_add_u32 m0, %[lw], 35840\n\t" \
  "v_mfma_f32_16x16x32_bf16 v[148:151], v[196:199], v[80:83], v[148:151]\n\t" \
  "v_mfma_f32_16x16x32_bf16 v[156:159], v[196:199], v[84:87], v[156:159]\n\t" \
  "ds_read_b128 v[196:199], v214 offset:49408\n\t" \
  "global_load_lds_dwordx4 v219, s[92:93] sc1\n\t" \
  "s_waitcnt lgkmcnt(6)\n\t" \
  "v_mfma_f32_16x16x32_bf16 v[144:147], v[200:203], v[88:91], v[144:147]\n\t" \
  "v_mfma_f32_16x16x32_bf16 v[152:155], v[200:203], v[92:95], v[152:155]\n\t" \
  "ds_read_b128 v[200:203], v215 offset:33024\n\t" \
  "v_mfma_f32_16x16x32_bf16 v[148:151], v[204:207], v[88:91], v[148:151]\n\t" \
  "v_mfma_f32_16x16x32_bf16 v[156:159], v[204:207], v[92:95], v[156:159]\n\t" \
  "ds_read_b128 v[204:207], v215 offset:49408\n\t" \
  "s_cmp_lt_u32 s86, 6\n\t" \
  "s_cbranch_scc1 .Lw30%=\n\t" \
  "s_waitcnt vmcnt(5)\n\t" \
  "s_barrier\n\t" \
  ".Lg30%=:\n\t" \
  "s_add_u32 s92, s92, 65024\n\t" \
  "s_addc_u32 s93, s93, 0\n\t" \
  "s_waitcnt lgkmcnt(6)\n\t" \
  "v_mfma_f32_16x16x32_bf16 v[144:147], v[176:179], v[96:99], v[144:147]\n\t" \
  "v_mfma_f32_16x16x32_bf16 v[152:155], v[176:179], v[100:103], v[152:155]\n\t" \
  "ds_read_b128 v[176:179], v208 offset:0\n\t" \
  "s_add_u32 m0, %[lw], 65536\n\t" \
  "v_mfma_f32_16x16x32_bf16 v[148:151], v[180:183], v[96:99], v[148:151]\n\t" \
  "v_mfma_f32_16x16x32_bf16 v[156:159], v[180:183], v[100:103], v[156:159]\n\t" \
  "ds_read_b128 v[180:183], v208 offset:16384\n\t" \
  "global_load_lds_dwordx4 v216, s[92:93] sc1\n\t" \
  "s_waitcnt lgkmcnt(6)\n\t" \
  "v_mfma_f32_16x16x32_bf16 v[144:147], v[184:187], v[104:107], v[144:147]\n\t" \
  "v_mfma_f32_16x16x32_bf16 v[152:155], v[184:187], v[108:111], v[152:155]\n\t" \
  "ds_read_b128 v[184:187], v209 offset:0\n\t" \
  "v_mfma_f32_16x16x32_bf16 v[148:151], v[188:191], v[104:107], v[148:151]\n\t" \
  "v_mfma_f32_16x16x32_bf16 v[156:159], v[188:191], v[108:111], v[156:159]\n\t" \
  "ds_read_b128 v[188:191], v209 offset:16384\n\t" \
  "s_waitcnt lgkmcnt(6)\n\t" \
  "v_mfma_f32_16x16x32_bf16 v[144:147], v[192:195], v[112:115], v[144:147]\n\t" \
  "v_mfma_f32_16x16x32_bf16 v[152:155], v[192:195], v[116:119], v[152:155]\n\t" \
  "ds_read_b128 v[192:195], v210 offset:0\n\t" \
  "s_add_u32 m0, %[lw], 66560\n\t" \
  "v_mfma_f32_16x16x32_bf16 v[148:151], v[196:199], v[112:115], v[148:151]\n\t" \
  "v_mfma_f32_16x16x32_bf16 v[156:159], v[196:199], v[116:119], v[156:159]\n\t" \
  "ds_read_b128 v[196:199], v210 offset:16384\n\t" \
  "global_load_lds_dwordx4 v217, s[92:93] sc1\n\t" \
  "s_waitcnt lgkmcnt(6)\n\t" \
  "v_mfma_f32_16x16x32_bf16 v[144:147], v[200:203], v[120:123], v[144:147]\n\t" \
  "v_mfma_f32_16x16x32_bf16 v[152:155], v[200:203], v[124:127], v[152:155]\n\t" \
  "ds_read_b128 v[200:203], v211 offset:0\n\t" \
  "v_mfma_f32_16x16x32_bf16 v[148:151], v[204:207], v[120:123], v[148:151]\n\t" \
  "v_mfma_f32_16x16x32_bf16 v[156:159], v[204:207], v[124:127], v[156:159]\n\t" \
  "ds_read_b128 v[204:207], v211 offset:16384\n\t" \
  "s_add_u32 s86, s86, 1\n\t" \
  "s_add_u32 s87, s87, 1\n\t" \
  "s_cmp_eq_u32 s86, 2\n\t" \
  "s_cbranch_scc1 .Ll0%=\n\t" \
  "s_cmp_lt_u32 s86, %[end]\n\t" \
  "s_cbranch_scc1 .Lp0%=\n\t" \
  "s_cmp_lg_u32 s86, 64\n\t" \
  "s_cbranch_scc1 .Lx0%=\n\t" \
  ".Ll0%=:\n\t" \
  "s_nop 7\n\t" \
  "ds_write_b128 v220, v[152:155] offset:0\n\t" \
  "ds_write_b128 v220, v[156:159] offset:1024\n\t" \
  "s_lshr_b32 s90, s87, 7\n\t" \
  "s_xor_b32 s90, s90, %[s0]\n\t" \
  "s_and_b32 s90, s90, 1\n\t" \
  "s_lshl1_add_u32 s90, s87, s90\n\t" \
  "s_lshl_b32 s90, s90, 13\n\t" \
  "s_cmp_lt_i32 s86, 1\n\t" \
  "s_cselect_b32 s90, 0x70000000, s90\n\t" \
  "s_waitcnt lgkmcnt(0)\n\t" \
  "s_barrier\n\t" \
  "ds_read_b128 v[160:163], v221 offset:0\n\t" \
  "ds_read_b128 v[164:167], v221 offset:1024\n\t" \
  "s_waitcnt lgkmcnt(0)\n\t" \
  "v_add_f32 v160, v160, v144\n\t" \
  "v_add_f32 v161, v161, v145\n\t" \
  "v_add_f32 v162, v162, v146\n\t" \
  "v_add_f32 v163, v163, v147\n\t" \
  "v_add_f32 v164, v164, v148\n\t" \
  "v_add_f32 v165, v165, v149\n\t" \
  "v_add_f32 v166, v166, v150\n\t" \
  "v_add_f32 v167, v167, v151\n\t" \
  "v_cvt_pk_bf16_f32 v168, v160, v161\n\t" \
  "v_cvt_pk_bf16_f32 v169, v162, v163\n\t" \
  "v_cvt_pk_bf16_f32 v170, v164, v165\n\t" \
  "v_cvt_pk_bf16_f32 v171, v166, v167\n\t" \
  "buffer_store_dwordx4 v[168:171], v172, %[rt], s90 offen sc1\n\t" \
  "s_waitcnt vmcnt(0)\n\t" \
  "s_barrier\n\t" \
  "s_cmp_eq_u32 s86, 2\n\t" \
  "s_cbranch_scc1 .Lp0%=\n\t" \
  "s_branch .Lx0%=\n\t" \
  ".Lf0%=:\n\t" \
  "s_nop 7\n\t" \
  "ds_write_b128 v220, v[136:139] offset:0\n\t" \
  "ds_write_b128 v220, v[140:143] offset:1024\n\t" \
  "s_lshr_b32 s90, s87, 7\n\t" \
  "s_xor_b32 s90, s90, %[s0]\n\t" \
  "s_and_b32 s90, s90, 1\n\t" \
  "s_lshl1_add_u32 s90, s87, s90\n\t" \
  "s_lshl_b32 s90, s90, 13\n\t" \
  "s_cmp_lt_i32 s86, 1\n\t" \
  "s_cselect_b32 s90, 0x70000000, s90\n\t" \
  "s_waitcnt lgkmcnt(0)\n\t" \
  "s_barrier\n\t" \
  "ds_read_b128 v[160:163], v221 offset:0\n\t" \
  "ds_read_b128 v[164:167], v221 offset:1024\n\t" \
  "s_waitcnt lgkmcnt(0)\n\t" \
  "v_add_f32 v160, v160, v128\n\t" \
  "v_add_f32 v161, v161, v129\n\t" \
  "v_add_f32 v162, v162, v130\n\t" \
  "v_add_f32 v163, v163, v131\n\t" \
  "v_add_f32 v164, v164, v132\n\t" \
  "v_add_f32 v165, v165, v133\n\t" \
  "v_add_f32 v166, v166, v134\n\t" \
  "v_add_f32 v167, v167, v135\n\t" \
  "v_cvt_pk_bf16_f32 v168, v160, v161\n\t" \
  "v_cvt_pk_bf16_f32 v169, v162, v163\n\t" \
  "v_cvt_pk_bf16_f32 v170, v164, v165\n\t" \
  "v_cvt_pk_bf16_f32 v171, v166, v167\n\t" \
  "buffer_store_dwordx4 v[168:171], v172, %[rt], s90 offen sc1\n\t" \
  "s_waitcnt vmcnt(0)\n\t" \
  "s_barrier\n\t" \
  "s_branch .Lfb0%=\n\t" \
  ".Lw10%=:\n\t" \
  "s_waitcnt vmcnt(2)\n\t" \
  "s_barrier\n\t" \
  "s_branch .Lg10%=\n\t" \
  ".Lw30%=:\n\t" \
  "s_waitcnt vmcnt(2)\n\t" \
  "s_barrier\n\t" \
  "s_branch .Lg30%=\n\t" \
  ".Lx0%=:\n\t" \
  "s_waitcnt lgkmcnt(0)\n\t" \
  "s_setprio 0\n\t" \
  "s_mov_b32 m0, s94\n\t" \
  "s_branch .Le%=\n\t" \
  ".Lk%=:\n\t" \
  "s_mov_b32 s94, m0\n\t" \
  "s_mov_b64 s[92:93], %[x0]\n\t" \
  "s_mov_b32 s86, %[g0]\n\t" \
  "s_add_u32 s87, s86, %[t0]\n\t" \
  "s_sub_i32 s88, s86, 3\n\t" \
  "ds_read_b128 v[176:179], v208 offset:0\n\t" \
  "ds_read_b128 v[180:183], v208 offset:16384\n\t" \
  "ds_read_b128 v[184:187], v209 offset:0\n\t" \
  "ds_read_b128 v[188:191], v209 offset:16384\n\t" \
  "ds_read_b128 v[192:195], v210 offset:0\n\t" \
  "ds_read_b128 v[196:199], v210 offset:16384\n\t" \
  "ds_read_b128 v[200:203], v211 offset:0\n\t" \
  "ds_read_b128 v[204:207], v211 offset:16384\n\t" \
  ".Lp1%=:\n\t" \
  "s_waitcnt lgkmcnt(6)\n\t" \
  "v_mfma_f32_16x16x32_bf16 v[128:131], v[176:179], v[0:3], 0\n\t" \
  "v_mfma_f32_16x16x32_bf16 v[136:139], v[176:179], v[4:7], 0\n\t" \
  "ds_read_b128 v[176:179], v208 offset:256\n\t" \
  "v_mfma_f32_16x16x32_bf16 v[132:135], v[180:183], v[0:3], 0\n\t" \
  "v_mfma_f32_16x16x32_bf16 v[140:143], v[180:183], v[4:7], 0\n\t" \
  "ds_read_b128 v[180:183], v208 offset:16640\n\t" \
  "s_waitcnt lgkmcnt(6)\n\t" \
  "v_mfma_f32_16x16x32_bf16 v[128:131], v[184:187], v[8:11], v[128:131]\n\t" \
  "v_mfma_f32_16x16x32_bf16 v[136:139], v[184:187], v[12:15], v[136:139]\n\t" \
  "ds_read_b128 v[184:187], v209 offset:256\n\t" \
  "s_add_u32 m0, %[lw], 67584\n\t" \
  "v_mfma_f32_16x16x32_bf16 v[132:135], v[188:191], v[8:11], v[132:135]\n\t" \
  "v_mfma_f32_16x16x32_bf16 v[140:143], v[188:191], v[12:15], v[140:143]\n\t" \
  "ds_read_b128 v[188:191], v209 offset:16640\n\t" \
  "global_load_lds_dwordx4 v218, s[92:93] sc1\n\t" \
  "ds_write_b128 v220, v[152:155] offset:0\n\t" \
  "ds_write_b128 v220, v[156:159] offset:1024\n\t" \
  "s_waitcnt lgkmcnt(8)\n\t" \
  "v_mfma_f32_16x16x32_bf16 v[128:131], v[192:195], v[16:19], v[128:131]\n\t" \
  "v_mfma_f32_16x16x32_bf16 v[136:139], v[192:195], v[20:23], v[136:139]\n\t" \
  "ds_read_b128 v[192:195], v210 offset:256\n\t" \
  "v_mfma_f32_16x16x32_bf16 v[132:135], v[196:199], v[16:19], v[132:135]\n\t" \
  "v_mfma_f32_16x16x32_bf16 v[140:143], v[196:199], v[20:23], v[140:143]\n\t" \
  "ds_read_b128 v[196:199], v210 offset:16640\n\t" \
  "s_lshr_b32 s90, s87, 7\n\t" \
  "s_xor_b32 s90, s90, %[s0]\n\t" \
  "s_and_b32 s90, s90, 1\n\t" \
  "s_lshl1_add_u32 s90, s87, s90\n\t" \
  "s_lshl_b32 s90, s90, 13\n\t" \
  "s_cmp_lt_i32 s86, 3\n\t" \
  "s_cselect_b32 s90, 0x70000000, s90\n\t" \
  "s_waitcnt lgkmcnt(8)\n\t" \
  "v_mfma_f32_16x16x32_bf16 v[128:131], v[200:203], v[24:27], v[128:131]\n\t" \
  "v_mfma_f32_16x16x32_bf16 v[136:139], v[200:203], v[28:31], v[136:139]\n\t" \
  "ds_read_b128 v[200:203], v211 offset:256\n\t" \
  "s_add_u32 m0, %[lw], 68608\n\t" \
  "v_mfma_f32_16x16x32_bf16 v[132:135], v[204:207], v[24:27], v[132:135]\n\t" \
  "v_mfma_f32_16x16x32_bf16 v[140:143], v[204:207], v[28:31], v[140:143]\n\t" \
  "ds_read_b128 v[204:207], v211 offset:16640\n\t" \
  "global_load_lds_dwordx4 v219, s[92:93] sc1\n\t" \
  "s_waitcnt lgkmcnt(4)\n\t" \
  "s_waitcnt vmcnt(4)\n\t" \
  "s_barrier\n\t" \
  ".Lg01%=:\n\t" \
  "s_add_u32 s92, s92, 512\n\t" \
  "s_addc_u32 s93, s93, 0\n\t" \
  "v_mfma_f32_16x16x32_bf16 v[128:131], v[176:179], v[32:35], v[128:131]\n\t" \
  "v_mfma_f32_16x16x32_bf16 v[136:139], v[176:179], v[36:39], v[136:139]\n\t" \
  "ds_read_b128 v[176:179], v208 offset:32768\n\t" \
  "v_mfma_f32_16x16x32_bf16 v[132:135], v[180:183], v[32:35], v[132:135]\n\t" \
  "v_mfma_f32_16x16x32_bf16 v[140:143], v[180:183], v[36:39], v[140:143]\n\t" \
  "ds_read_b128 v[180:183], v208 offset:49152\n\t" \
  "ds_read_b128 v[160:163], v221 offset:0\n\t" \
  "ds_read_b128 v[164:167], v221 offset:1024\n\t" \
  "s_sub_i32 s91, s86, 2\n\t" \
  "s_max_i32 s88, s88, s91\n\t" \
  "v_mov_b32 v173, s88\n\t" \
  "buffer_store_dword v173, v174, %[rs], 0 offen sc1\n\t" \
  "v_mfma_f32_16x16x32_bf16 v[128:131], v[184:187], v[40:43], v[128:131]\n\t" \
  "v_mfma_f32_16x16x32_bf16 v[136:139], v[184:187], v[44:47], v[136:139]\n\t" \
  "ds_read_b128 v[184:187], v209 offset:32768\n\t" \
  "s_add_u32 m0, %[lw], 98304\n\t" \
  "v_mfma_f32_16x16x32_bf16 v[132:135], v[188:191], v[40:43], v[132:135]\n\t" \
  "v_mfma_f32_16x16x32_bf16 v[140:143], v[188:191], v[44:47], v[140:143]\n\t" \
  "ds_read_b128 v[188:191], v209 offset:49152\n\t" \
  "global_load_lds_dwordx4 v216, s[92:93] sc1\n\t" \
  "s_waitcnt lgkmcnt(8)\n\t" \
  "v_mfma_f32_16x16x32_bf16 v[128:131], v[192:195], v[48:51], v[128:131]\n\t" \
  "v_mfma_f32_16x16x32_bf16 v[136:139], v[192:195], v[52:55], v[136:139]\n\t" \
  "ds_read_b128 v[192:195], v210 offset:32768\n\t" \
  "v_mfma_f32_16x16x32_bf16 v[132:135], v[196:199], v[48:51], v[132:135]\n\t" \
  "v_mfma_f32_16x16x32_bf16 v[140:143], v[196:199], v[52:55], v[140:143]\n\t" \
  "ds_read_b128 v[196:199], v210 offset:49152\n\t" \
  "s_waitcnt lgkmcnt(4)\n\t" \
  "v_add_f32 v160, v160, v144\n\t" \
  "v_add_f32 v161, v161, v145\n\t" \
  "v_add_f32 v162, v162, v146\n\t" \
  "v_add_f32 v163, v163, v147\n\t" \
  "v_add_f32 v164, v164, v148\n\t" \
  "v_add_f32 v165, v165, v149\n\t" \
  "v_add_f32 v166, v166, v150\n\t" \
  "v_add_f32 v167, v167, v151\n\t" \
  "v_mfma_f32_16x16x32_bf16 v[128:131], v[200:203], v[56:59], v[128:131]\n\t" \
  "v_mfma_f32_16x16x32_bf16 v[136:139], v[200:203], v[60:63], v[136:139]\n\t" \
  "ds_read_b128 v[200:203], v211 offset:32768\n\t" \
  "s_add_u32 m0, %[lw], 99328\n\t" \
  "v_mfma_f32_16x16x32_bf16 v[132:135], v[204:207], v[56:59], v[132:135]\n\t" \
  "v_mfma_f32_16x16x32_bf16 v[140:143], v[204:207], v[60:63], v[140:143]\n\t" \
  "ds_read_b128 v[204:207], v211 offset:49152\n\t" \
  "global_load_lds_dwordx4 v217, s[92:93] sc1\n\t" \
  "v_cvt_pk_bf16_f32 v168, v160, v161\n\t" \
  "v_cvt_pk_bf16_f32 v169, v162, v163\n\t" \
  "v_cvt_pk_bf16_f32 v170, v164, v165\n\t" \
  "v_cvt_pk_bf16_f32 v171, v166, v167\n\t" \
  "buffer_store_dwordx4 v[168:171], v172, %[rt], s90 offen sc1\n\t" \
  "v_mfma_f32_16x16x32_bf16 v[128:131], v[176:179], v[64:67], v[128:131]\n\t" \
  "v_mfma_f32_16x16x32_bf16 v[136:139], v[176:179], v[68:71], v[136:139]\n\t" \
  "ds_read_b128 v[176:179], v208 offset:33024\n\t" \
  "v_mfma_f32_16x16x32_bf16 v[132:135], v[180:183], v[64:67], v[132:135]\n\t" \
  "v_mfma_f32_16x16x32_bf16 v[140:143], v[180:183], v[68:71], v[140:143]\n\t" \
  "ds_read_b128 v[180:183], v208 offset:49408\n\t" \
  "s_waitcnt lgkmcnt(6)\n\t" \
  "v_mfma_f32_16x16x32_bf16 v[128:131], v[184:187], v[72:75], v[128:131]\n\t" \
  "v_mfma_f32_16x16x32_bf16 v[136:139], v[184:187], v[76:79], v[136:139]\n\t" \
  "ds_read_b128 v[184:187], v209 offset:33024\n\t" \
  "s_add_u32 m0, %[lw], 100352\n\t" \
  "v_mfma_f32_16x16x32_bf16 v[132:135], v[188:191], v[72:75], v[132:135]\n\t" \
  "v_mfma_f32_16x16x32_bf16 v[140:143], v[188:191], v[76:79], v[140:143]\n\t" \
  "ds_read_b128 v[188:191], v209 offset:49408\n\t" \
  "global_load_lds_dwordx4 v218, s[92:93] sc1\n\t" \
  "s_waitcnt lgkmcnt(6)\n\t" \
  "v_mfma_f32_16x16x32_bf16 v[128:131], v[192:195], v[80:83], v[128:131]\n\t" \
  "v_mfma_f32_16x16x32_bf16 v[136:139], v[192:195], v[84:87], v[136:139]\n\t" \
  "ds_read_b128 v[192:195], v210 offset:33024\n\t" \
  "v_mfma_f32_16x16x32_bf16 v[132:135], v[196:199], v[80:83], v[132:135]\n\t" \
  "v_mfma_f32_16x16x32_bf16 v[140:143], v[196:199], v[84:87], v[140:143]\n\t" \
  "ds_read_b128 v[196:199], v210 offset:49408\n\t" \
  "s_waitcnt lgkmcnt(6)\n\t" \
  "v_mfma_f32_16x16x32_bf16 v[128:131], v[200:203], v[88:91], v[128:131]\n\t" \
  "v_mfma_f32_16x16x32_bf16 v[136:139], v[200:203], v[92:95], v[136:139]\n\t" \
  "ds_read_b128 v[200:203], v211 offset:33024\n\t" \
  "s_add_u32 m0, %[lw], 101376\n\t" \
  "v_mfma_f32_16x16x32_bf16 v[132:135], v[204:207], v[88:91], v[132:135]\n\t" \
  "v_mfma_f32_16x16x32_bf16 v[140:143], v[204:207], v[92:95], v[140:143]\n\t" \
  "ds_read_b128 v[204:207], v211 offset:49408\n\t" \
  "global_load_lds_dwordx4 v219, s[92:93] sc1\n\t" \
  "s_cmp_lt_u32 s86, 6\n\t" \
  "s_cbranch_scc1 .Lw11%=\n\t" \
  "s_waitcnt vmcnt(6)\n\t" \
  "s_barrier\n\t" \
  ".Lg11%=:\n\t" \
  "s_add_u32 s92, s92, 65024\n\t" \
  "s_addc_u32 s93, s93, 0\n\t" \
  "s_waitcnt lgkmcnt(6)\n\t" \
  "v_mfma_f32_16x16x32_bf16 v[128:131], v[176:179], v[96:99], v[128:131]\n\t" \
  "v_mfma_f32_16x16x32_bf16 v[136:139], v[176:179], v[100:103], v[136:139]\n\t" \
  "ds_read_b128 v[176:179], v212 offset:0\n\t" \
  "v_mfma_f32_16x16x32_bf16 v[132:135], v[180:183], v[96:99], v[132:135]\n\t" \
  "v_mfma_f32_16x16x32_bf16 v[140:143], v[180:183], v[100:103], v[140:143]\n\t" \
  "ds_read_b128 v[180:183], v212 offset:16384\n\t" \
  "s_waitcnt lgkmcnt(6)\n\t" \
  "v_mfma_f32_16x16x32_bf16 v[128:131], v[184:187], v[104:107], v[128:131]\n\t" \
  "v_mfma_f32_16x16x32_bf16 v[136:139], v[184:187], v[108:111], v[136:139]\n\t" \
  "ds_read_b128 v[184:187], v213 offset:0\n\t" \
  "s_add_u32 m0, %[lw], 0\n\t" \
  "v_mfma_f32_16x16x32_bf16 v[132:135], v[188:191], v[104:107], v[132:135]\n\t" \
  "v_mfma_f32_16x16x32_bf16 v[140:143], v[188:191], v[108:111], v[140:143]\n\t" \
  "ds_read_b128 v[188:191], v213 offset:16384\n\t" \
  "global_load_lds_dwordx4 v216, s[92:93] sc1\n\t" \
  "s_waitcnt lgkmcnt(6)\n\t" \
  "v_mfma_f32_16x16x32_bf16 v[128:131], v[192:195], v[112:115], v[128:131]\n\t" \
  "v_mfma_f32_16x16x32_bf16 v[136:139], v[192:195], v[116:119], v[136:139]\n\t" \
  "ds_read_b128 v[192:195], v214 offset:0\n\t" \
  "v_mfma_f32_16x16x32_bf16 v[132:135], v[196:199], v[112:115], v[132:135]\n\t" \
  "v_mfma_f32_16x16x32_bf16 v[140:143], v[196:199], v[116:119], v[140:143]\n\t" \
  "ds_read_b128 v[196:199], v214 offset:16384\n\t" \
  "s_waitcnt lgkmcnt(6)\n\t" \
  "v_mfma_f32_16x16x32_bf16 v[128:131], v[200:203], v[120:123], v[128:131]\n\t" \
  "v_mfma_f32_16x16x32_bf16 v[136:139], v[200:203], v[124:127], v[136:139]\n\t" \
  "ds_read_b128 v[200:203], v215 offset:0\n\t" \
  "s_add_u32 m0, %[lw], 1024\n\t" \
  "v_mfma_f32_16x16x32_bf16 v[132:135], v[204:207], v[120:123], v[132:135]\n\t" \
  "v_mfma_f32_16x16x32_bf16 v[140:143], v[204:207], v[124:127], v[140:143]\n\t" \
  "ds_read_b128 v[204:207], v215 offset:16384\n\t" \
  "global_load_lds_dwordx4 v217, s[92:93] sc1\n\t" \
  "s_add_u32 s86, s86, 1\n\t" \
  "s_add_u32 s87, s87, 1\n\t" \
  "s_cmp_eq_u32 s86, 1\n\t" \
  "s_cbranch_scc1 .Lf1%=\n\t" \
  ".Lfb1%=:\n\t" \
  "s_waitcnt lgkmcnt(6)\n\t" \
  "v_mfma_f32_16x16x32_bf16 v[144:147], v[176:179], v[0:3], 0\n\t" \
  "v_mfma_f32_16x16x32_bf16 v[152:155], v[176:179], v[4:7], 0\n\t" \
  "ds_read_b128 v[176:179], v212 offset:256\n\t" \
  "v_mfma_f32_16x16x32_bf16 v[148:151], v[180:183], v[0:3], 0\n\t" \
  "v_mfma_f32_16x16x32_bf16 v[156:159], v[180:183], v[4:7], 0\n\t" \
  "ds_read_b128 v[180:183], v212 offset:16640\n\t" \
  "s_waitcnt lgkmcnt(6)\n\t" \
  "v_mfma_f32_16x16x32_bf16 v[144:147], v[184:187], v[8:11], v[144:147]\n\t" \
  "v_mfma_f32_16x16x32_bf16 v[152:155], v[184:187], v[12:15], v[152:155]\n\t" \
  "ds_read_b128 v[184:187], v213 offset:256\n\t" \
  "s_add_u32 m0, %[lw], 2048\n\t" \
  "v_mfma_f32_16x16x32_bf16 v[148:151], v[188:191], v[8:11], v[148:151]\n\t" \
  "v_mfma_f32_16x16x32_bf16 v[156:159], v[188:191], v[12:15], v[156:159]\n\t" \
  "ds_read_b128 v[188:191], v213 offset:16640\n\t" \
  "global_load_lds_dwordx4 v218, s[92:93] sc1\n\t" \
  "ds_write_b128 v220, v[136:139] offset:0\n\t" \
  "ds_write_b128 v220, v[140:143] offset:1024\n\t" \
  "s_waitcnt lgkmcnt(8)\n\t" \
  "v_mfma_f32_16x16x32_bf16 v[144:147], v[192:195], v[16:19], v[144:147]\n\t" \
  "v_mfma_f32_16x16x32_bf16 v[152:155], v[192:195], v[20:23], v[152:155]\n\t" \
  "ds_read_b128 v[192:195], v214 offset:256\n\t" \
  "v_mfma_f32_16x16x32_bf16 v[148:151], v[196:199], v[16:19], v[148:151]\n\t" \
  "v_mfma_f32_16x16x32_bf16 v[156:159], v[196:199], v[20:23], v[156:159]\n\t" \
  "ds_read_b128 v[196:199], v214 offset:16640\n\t" \
  "s_lshr_b32 s90, s87, 7\n\t" \
  "s_xor_b32 s90, s90, %[s0]\n\t" \
  "s_and_b32 s90, s90, 1\n\t" \
  "s_lshl1_add_u32 s90, s87, s90\n\t" \
  "s_lshl_b32 s90, s90, 13\n\t" \
  "s_cmp_lt_i32 s86, 3\n\t" \
  "s_cselect_b32 s90, 0x70000000, s90\n\t" \
  "s_waitcnt lgkmcnt(8)\n\t" \
  "v_mfma_f32_16x16x32_bf16 v[144:147], v[200:203], v[24:27], v[144:147]\n\t" \
  "v_mfma_f32_16x16x32_bf16 v[152:155], v[200:203], v[28:31], v[152:155]\n\t" \
  "ds_read_b128 v[200:203], v215 offset:256\n\t" \
  "s_add_u32 m0, %[lw], 3072\n\t" \
  "v_mfma_f32_16x16x32_bf16 v[148:151], v[204:207], v[24:27], v[148:151]\n\t" \
  "v_mfma_f32_16x16x32_bf16 v[156:159], v[204:207], v[28:31], v[156:159]\n\t" \
  "ds_read_b128 v[204:207], v215 offset:16640\n\t" \
  "global_load_lds_dwordx4 v219, s[92:93] sc1\n\t" \
  "s_waitcnt lgkmcnt(4)\n\t" \
  "s_waitcnt vmcnt(4)\n\t" \
  "s_barrier\n\t" \
  ".Lg21%=:\n\t" \
  "s_add_u32 s92, s92, 512\n\t" \
  "s_addc_u32 s93, s93, 0\n\t" \
  "v_mfma_f32_16x16x32_bf16 v[144:147], v[176:179], v[32:35], v[144:147]\n\t" \
  "v_mfma_f32_16x16x32_bf16 v[152:155], v[176:179], v[36:39], v[152:155]\n\t" \
  "ds_read_b128 v[176:179], v212 offset:32768\n\t" \
  "v_mfma_f32_16x16x32_bf16 v[148:151], v[180:183], v[32:35], v[148:151]\n\t" \
  "v_mfma_f32_16x16x32_bf16 v[156:159], v[180:183], v[36:39], v[156:159]\n\t" \
  "ds_read_b128 v[180:183], v212 offset:49152\n\t" \
  "ds_read_b128 v[160:163], v221 offset:0\n\t" \
  "ds_read_b128 v[164:167], v221 offset:1024\n\t" \
  "s_sub_i32 s91, s86, 2\n\t" \
  "s_max_i32 s88, s88, s91\n\t" \
  "v_mov_b32 v173, s88\n\t" \
  "buffer_store_dword v173, v174, %[rs], 0 offen sc1\n\t" \
  "v_mfma_f32_16x16x32_bf16 v[144:147], v[184:187], v[40:43], v[144:147]\n\t" \
  "v_mfma_f32_16x16x32_bf16 v[152:155], v[184:187], v[44:47], v[152:155]\n\t" \
  "ds_read_b128 v[184:187], v213 offset:32768\n\t" \
  "s_add_u32 m0, %[lw], 32768\n\t" \
  "v_mfma_f32_16x16x32_bf16 v[148:151], v[188:191], v[40:43], v[148:151]\n\t" \
  "v_mfma_f32_16x16x32_bf16 v[156:159], v[188:191], v[44:47], v[156:159]\n\t" \
  "ds_read_b128 v[188:191], v213 offset:49152\n\t" \
  "global_load_lds_dwordx4 v216, s[92:93] sc1\n\t" \
  "s_waitcnt lgkmcnt(8)\n\t" \
  "v_mfma_f32_16x16x32_bf16 v[144:147], v[192:195], v[48:51], v[144:147]\n\t" \
  "v_mfma_f32_16x16x32_bf16 v[152:155], v[192:195], v[52:55], v[152:155]\n\t" \
  "ds_read_b128 v[192:195], v214 offset:32768\n\t" \
  "v_mfma_f32_16x16x32_bf16 v[148:151], v[196:199], v[48:51], v[148:151]\n\t" \
  "v_mfma_f32_16x16x32_bf16 v[156:159], v[196:199], v[52:55], v[156:159]\n\t" \
  "ds_read_b128 v[196:199], v214 offset:49152\n\t" \
  "s_waitcnt lgkmcnt(4)\n\t" \
  "v_add_f32 v160, v160, v128\n\t" \
  "v_add_f32 v161, v161, v129\n\t" \
  "v_add_f32 v162, v162, v130\n\t" \
  "v_add_f32 v163, v163, v131\n\t" \
  "v_add_f32 v164, v164, v132\n\t" \
  "v_add_f32 v165, v165, v133\n\t" \
  "v_add_f32 v166, v166, v134\n\t" \
  "v_add_f32 v167, v167, v135\n\t" \
  "v_mfma_f32_16x16x32_bf16 v[144:147], v[200:203], v[56:59], v[144:147]\n\t" \
  "v_mfma_f32_16x16x32_bf16 v[152:155], v[200:203], v[60:63], v[152:155]\n\t" \
  "ds_read_b128 v[200:203], v215 offset:32768\n\t" \
  "s_add_u32 m0, %[lw], 33792\n\t" \
  "v_mfma_f32_16x16x32_bf16 v[148:151], v[204:207], v[56:59], v[148:151]\n\t" \
  "v_mfma_f32_16x16x32_bf16 v[156:159], v[204:207], v[60:63], v[156:159]\n\t" \
  "ds_read_b128 v[204:207], v215 offset:49152\n\t" \
  "global_load_lds_dwordx4 v217, s[92:93] sc1\n\t" \
  "v_cvt_pk_bf16_f32 v168, v160, v161\n\t" \
  "v_cvt_pk_bf16_f32 v169, v162, v163\n\t" \
  "v_cvt_pk_bf16_f32 v170, v164, v165\n\t" \
  "v_cvt_pk_bf16_f32 v171, v166, v167\n\t" \
  "buffer_store_dwordx4 v[168:171], v172, %[rt], s90 offen sc1\n\t" \
  "v_mfma_f32_16x16x32_bf16 v[144:147], v[176:179], v[64:67], v[144:147]\n\t" \
  "v_mfma_f32_16x16x32_bf16 v[152:155], v[176:179], v[68:71], v[152:155]\n\t" \
  "ds_read_b128 v[176:179], v212 offset:33024\n\t" \
  "v_mfma_f32_16x16x32_bf16 v[148:151], v[180:183], v[64:67], v[148:151]\n\t" \
  "v_mfma_f32_16x16x32_bf16 v[156:159], v[180:183], v[68:71], v[156:159]\n\t" \
  "ds_read_b128 v[180:183], v212 offset:49408\n\t" \
  "s_waitcnt lgkmcnt(6)\n\t" \
  "v_mfma_f32_16x16x32_bf16 v[144:147], v[184:187], v[72:75], v[144:147]\n\t" \
  "v_mfma_f32_16x16x32_bf16 v[152:155], v[184:187], v[76:79], v[152:155]\n\t" \
  "ds_read_b128 v[184:187], v213 offset:33024\n\t" \
  "s_add_u32 m0, %[lw], 34816\n\t" \
  "v_mfma_f32_16x16x32_bf16 v[148:151], v[188:191], v[72:75], v[148:151]\n\t" \
  "v_mfma_f32_16x16x32_bf16 v[156:159], v[188:191], v[76:79], v[156:159]\n\t" \
  "ds_read_b128 v[188:191], v213 offset:49408\n\t" \
  "global_load_lds_dwordx4 v218, s[92:93] sc1\n\t" \
  "s_waitcnt lgkmcnt(6)\n\t" \
  "v_mfma_f32_16x16x32_bf16 v[144:147], v[192:195], v[80:83], v[144:147]\n\t" \
  "v_mfma_f32_16x16x32_bf16 v[152:155], v[192:195], v[84:87], v[152:155]\n\t" \
  "ds_read_b128 v[192:195], v214 offset:33024\n\t" \
  "v_mfma_f32_16x16x32_bf16 v[148:151], v[196:199], v[80:83], v[148:151]\n\t" \
  "v_mfma_f32_16x16x32_bf16 v[156:159], v[196:199], v[84:87], v[156:159]\n\t" \
  "ds_read_b128 v[196:199], v214 offset:49408\n\t" \
  "s_waitcnt lgkmcnt(6)\n\t" \
  "v_mfma_f32_16x16x32_bf16 v[144:147], v[200:203], v[88:91], v[144:147]\n\t" \
  "v_mfma_f32_16x16x32_bf16 v[152:155], v[200:203], v[92:95], v[152:155]\n\t" \
  "ds_read_b128 v[200:203], v215 offset:33024\n\t" \
  "s_add_u32 m0, %[lw], 35840\n\t" \
  "v_mfma_f32_16x16x32_bf16 v[148:151], v[204:207], v[88:91], v[148:151]\n\t" \
  "v_mfma_f32_16x16x32_bf16 v[156:159], v[204:207], v[92:95], v[156:159]\n\t" \
  "ds_read_b128 v[204:207], v215 offset:49408\n\t" \
  "global_load_lds_dwordx4 v219, s[92:93] sc1\n\t" \
  "s_cmp_lt_u32 s86, 6\n\t" \
  "s_cbranch_scc1 .Lw31%=\n\t" \
  "s_waitcnt vmcnt(6)\n\t" \
  "s_barrier\n\t" \
  ".Lg31%=:\n\t" \
  "s_add_u32 s92, s92, 65024\n\t" \
  "s_addc_u32 s93, s93, 0\n\t" \
  "s_waitcnt lgkmcnt(6)\n\t" \
  "v_mfma_f32_16x16x32_bf16 v[144:147], v[176:179], v[96:99], v[144:147]\n\t" \
  "v_mfma_f32_16x16x32_bf16 v[152:155], v[176:179], v[100:103], v[152:155]\n\t" \
  "ds_read_b128 v[176:179], v208 offset:0\n\t" \
  "v_mfma_f32_16x16x32_bf16 v[148:151], v[180:183], v[96:99], v[148:151]\n\t" \
  "v_mfma_f32_16x16x32_bf16 v[156:159], v[180:183], v[100:103], v[156:159]\n\t" \
  "ds_read_b128 v[180:183], v208 offset:16384\n\t" \
  "s_waitcnt lgkmcnt(6)\n\t" \
  "v_mfma_f32_16x16x32_bf16 v[144:147], v[184:187], v[104:107], v[144:147]\n\t" \
  "v_mfma_f32_16x16x32_bf16 v[152:155], v[184:187], v[108:111], v[152:155]\n\t" \
  "ds_read_b128 v[184:187], v209 offset:0\n\t" \
  "s_add_u32 m0, %[lw], 65536\n\t" \
  "v_mfma_f32_16x16x32_bf16 v[148:151], v[188:191], v[104:107], v[148:151]\n\t" \
  "v_mfma_f32_16x16x32_bf16 v[156:159], v[188:191], v[108:111], v[156:159]\n\t" \
  "ds_read_b128 v[188:191], v209 offset:16384\n\t" \
  "global_load_lds_dwordx4 v216, s[92:93] sc1\n\t" \
  "s_waitcnt lgkmcnt(6)\n\t" \
  "v_mfma_f32_16x16x32_bf16 v[144:147], v[192:195], v[112:115], v[144:147]\n\t" \
  "v_mfma_f32_16x16x32_bf16 v[152:155], v[192:195], v[116:119], v[152:155]\n\t" \
  "ds_read_b128 v[192:195], v210 offset:0\n\t" \
  "v_mfma_f32_16x16x32_bf16 v[148:151], v[196:199], v[112:115], v[148:151]\n\t" \
  "v_mfma_f32_16x16x32_bf16 v[156:159], v[196:199], v[116:119], v[156:159]\n\t" \
  "ds_read_b128 v[196:199], v210 offset:16384\n\t" \
  "s_waitcnt lgkmcnt(6)\n\t" \
  "v_mfma_f32_16x16x32_bf16 v[144:147], v[200:203], v[120:123], v[144:147]\n\t" \
  "v_mfma_f32_16x16x32_bf16 v[152:155], v[200:203], v[124:127], v[152:155]\n\t" \
  "ds_read_b128 v[200:203], v211 offset:0\n\t" \
  "s_add_u32 m0, %[lw], 66560\n\t" \
  "v_mfma_f32_16x16x32_bf16 v[148:151], v[204:207], v[120:123], v[148:151]\n\t" \
  "v_mfma_f32_16x16x32_bf16 v[156:159], v[204:207], v[124:127], v[156:159]\n\t" \
  "ds_read_b128 v[204:207], v211 offset:16384\n\t" \
  "global_load_lds_dwordx4 v217, s[92:93] sc1\n\t" \
  "s_add_u32 s86, s86, 1\n\t" \
  "s_add_u32 s87, s87, 1\n\t" \
  "s_cmp_eq_u32 s86, 2\n\t" \
  "s_cbranch_scc1 .Ll1%=\n\t" \
  "s_cmp_lt_u32 s86, %[end]\n\t" \
  "s_cbranch_scc1 .Lp1%=\n\t" \
  "s_cmp_lg_u32 s86, 64\n\t" \
  "s_cbranch_scc1 .Lx1%=\n\t" \
  ".Ll1%=:\n\t" \
  "s_nop 7\n\t" \
  "ds_write_b128 v220, v[152:155] offset:0\n\t" \
  "ds_write_b128 v220, v[156:159] offset:1024\n\t" \
  "s_lshr_b32 s90, s87, 7\n\t" \
  "s_xor_b32 s90, s90, %[s0]\n\t" \
  "s_and_b32 s90, s90, 1\n\t" \
  "s_lshl1_add_u32 s90, s87, s90\n\t" \
  "s_lshl_b32 s90, s90, 13\n\t" \
  "s_cmp_lt_i32 s86, 1\n\t" \
  "s_cselect_b32 s90, 0x70000000, s90\n\t" \
  "s_waitcnt lgkmcnt(0)\n\t" \
  "s_barrier\n\t" \
  "ds_read_b128 v[160:163], v221 offset:0\n\t" \
  "ds_read_b128 v[164:167], v221 offset:1024\n\t" \
  "s_waitcnt lgkmcnt(0)\n\t" \
  "v_add_f32 v160, v160, v144\n\t" \
  "v_add_f32 v161, v161, v145\n\t" \
  "v_add_f32 v162, v162, v146\n\t" \
  "v_add_f32 v163, v163, v147\n\t" \
  "v_add_f32 v164, v164, v148\n\t" \
  "v_add_f32 v165, v165, v149\n\t" \
  "v_add_f32 v166, v166, v150\n\t" \
  "v_add_f32 v167, v167, v151\n\t" \
  "v_cvt_pk_bf16_f32 v168, v160, v161\n\t" \
  "v_cvt_pk_bf16_f32 v169, v162, v163\n\t" \
  "v_cvt_pk_bf16_f32 v170, v164, v165\n\t" \
  "v_cvt_pk_bf16_f32 v171, v166, v167\n\t" \
  "buffer_store_dwordx4 v[168:171], v172, %[rt], s90 offen sc1\n\t" \
  "s_waitcnt vmcnt(0)\n\t" \
  "s_barrier\n\t" \
  "s_sub_i32 s91, s86, 1\n\t" \
  "s_max_i32 s88, s88, s91\n\t" \
  "v_mov_b32 v173, s88\n\t" \
  "buffer_store_dword v173, v174, %[rs], 0 offen sc1\n\t" \
  "s_cmp_eq_u32 s86, 2\n\t" \
  "s_cbranch_scc1 .Lp1%=\n\t" \
  "s_branch .Lx1%=\n\t" \
  ".Lf1%=:\n\t" \
  "s_nop 7\n\t" \
  "ds_write_b128 v220, v[136:139] offset:0\n\t" \
  "ds_write_b128 v220, v[140:143] offset:1024\n\t" \
  "s_lshr_b32 s90, s87, 7\n\t" \
  "s_xor_b32 s90, s90, %[s0]\n\t" \
  "s_and_b32 s90, s90, 1\n\t" \
  "s_lshl1_add_u32 s90, s87, s90\n\t" \
  "s_lshl_b32 s90, s90, 13\n\t" \
  "s_cmp_lt_i32 s86, 1\n\t" \
  "s_cselect_b32 s90, 0x70000000, s90\n\t" \
  "s_waitcnt lgkmcnt(0)\n\t" \
  "s_barrier\n\t" \
  "ds_read_b128 v[160:163], v221 offset:0\n\t" \
  "ds_read_b128 v[164:167], v221 offset:1024\n\t" \
  "s_waitcnt lgkmcnt(0)\n\t" \
  "v_add_f32 v160, v160, v128\n\t" \
  "v_add_f32 v161, v161, v129\n\t" \
  "v_add_f32 v162, v162, v130\n\t" \
  "v_add_f32 v163, v163, v131\n\t" \
  "v_add_f32 v164, v164, v132\n\t" \
  "v_add_f32 v165, v165, v133\n\t" \
  "v_add_f32 v166, v166, v134\n\t" \
  "v_add_f32 v167, v167, v135\n\t" \
  "v_cvt_pk_bf16_f32 v168, v160, v161\n\t" \
  "v_cvt_pk_bf16_f32 v169, v162, v163\n\t" \
  "v_cvt_pk_bf16_f32 v170, v164, v165\n\t" \
  "v_cvt_pk_bf16_f32 v171, v166, v167\n\t" \
  "buffer_store_dwordx4 v[168:171], v172, %[rt], s90 offen sc1\n\t" \
  "s_waitcnt vmcnt(0)\n\t" \
  "s_barrier\n\t" \
  "s_sub_i32 s91, s86, 1\n\t" \
  "s_max_i32 s88, s88, s91\n\t" \
  "v_mov_b32 v173, s88\n\t" \
  "buffer_store_dword v173, v174, %[rs], 0 offen sc1\n\t" \
  "s_branch .Lfb1%=\n\t" \
  ".Lw11%=:\n\t" \
  "s_waitcnt vmcnt(2)\n\t" \
  "s_barrier\n\t" \
  "s_sub_i32 s91, s86, 1\n\t" \
  "s_max_i32 s88, s88, s91\n\t" \
  "v_mov_b32 v173, s88\n\t" \
  "buffer_store_dword v173, v174, %[rs], 0 offen sc1\n\t" \
  "s_branch .Lg11%=\n\t" \
  ".Lw31%=:\n\t" \
  "s_waitcnt vmcnt(2)\n\t" \
  "s_barrier\n\t" \
  "s_sub_i32 s91, s86, 1\n\t" \
  "s_max_i32 s88, s88, s91\n\t" \
  "v_mov_b32 v173, s88\n\t" \
  "buffer_store_dword v173, v174, %[rs], 0 offen sc1\n\t" \
  "s_branch .Lg31%=\n\t" \
  ".Lx1%=:\n\t" \
  "s_waitcnt lgkmcnt(0)\n\t" \
  "s_setprio 0\n\t" \
  "s_mov_b32 m0, s94\n\t" \
  ".Le%=:\n\t"
constexpr int W_X = 131072, W_PM = W_X + 16384;
template <bool ISV>
__device__ __forceinline__ void job_proj(Args& p, int g, int which, int h, char* smem, __amdgpu_buffer_rsrc_t rsy) {
  const int tid = threadIdx.x, lane = tid & 63, r = lane & 15, q = lane >> 4;
  const int w = __builtin_amdgcn_readfirstlane(tid >> 6), kh = w >> 2, cg = w & 3;
  u32x32 W[4];
  const unsigned lw = lds_off(smem) + w * 4096;
  u32x4 vx;
  u32x2 tag;
  const char* xn = p.ws + WS_XN;
  auto row = [&](int t, int i) {
    const char* src = xn + (size_t)(min(t, 127) >> 1) * (SEG * XLD * 2) + (t & 1) * 512;
    unsigned keep;
    asm volatile("s_mov_b32 %0, m0\n\ts_mov_b32 m0, %3\n\ts_nop 0\n\tglobal_load_lds_dwordx4 %1, %2 sc1\n\ts_mov_b32 m0, %0"
                 : "=&s"(keep) : "v"(vx[i]), "s"(src), "s"(lw + (t & 3) * 32768 + i * 1024) : "memory");
  };
  {
    const u16* Wm = which == 0 ? p.Wk : p.Wq;
    asm volatile("" : "+s"(Wm));
    if (ISV) Wm = p.Wv;
    const unsigned vo = (tid >> 4) * (HD * 2) + 16 * ((lane & 15) ^ swzB((tid >> 4) & 15));
    auto stage = [&](int j) {
      const char* src = (const char*)(Wm + (size_t)(WREV ? 7 - j : j) * 64 * HD + h * DH);
      const unsigned dst = lds_off(smem) + (j & 3) * 32768 + w * 1024;
      glds2(src, 32 * HD * 2, vo, dst);
      glds2(src + 512 * HD * 2, 32 * HD * 2, vo, dst + 16384);
    };
    if (which == 1) { for (int z = 0; z < Q_DELAY; ++z) __builtin_amdgcn_s_sleep(16); }
    tag = start_rows<16>(p, 32 + g, [&]() { stage(0); stage(1); stage(2); stage(3); });
#pragma unroll
    for (int i = 0; i < 4; ++i) vx[i] = ((4 * w + i) ^ (ISV ? 0 : (lane >> 1) & 16)) * (XLD * 2) + (lane >> 5) * 1024 + (((lane & 31) ^ ((4 * w + i) & 15)) << 4);
    Boot bt;
#pragma unroll
    for (int j = 0; j < 8; ++j) {
      if (j < 4) WAIT_V(16); else if (j == 4) WAIT_V(12); else if (j == 5) WAIT_V(8); else if (j == 6) WAIT_V(4); else WAIT_V(10);
      if (j == 6 && w == 0) lds_at(smem + W_PM)[0] = boot_done(p.ws, bt, true, tag);
      LBAR();
      if (j == 4) report_rows(p, 32 + g, tag);
      const char* sb = smem + (j & 3) * 32768 + kh * 16384;
#pragma unroll
      for (int k = 0; k < 4; ++k) {
        const int ks = k >> 1, a = k & 1;
        const unsigned ch = ISV ? 2 * (2 * cg + (a ^ kh)) + ((r & 3) >> 1) : 4 * cg + (r & 3), by = ISV ? 8 * (r & 1) : 8 * a;
        s16x4 lo = __builtin_amdgcn_ds_read_tr16_b64_v4i16((lds_s16x4_ptr)(sb + offB(32 * ks + 8 * q + (r >> 2), ch) + by));
        s16x4 hi = __builtin_amdgcn_ds_read_tr16_b64_v4i16((lds_s16x4_ptr)(sb + offB(32 * ks + 8 * q + 4 + (r >> 2), ch) + by));
        const int f = 4 * (WREV ? 7 - j : j) + k;
        put4(W[f >> 3], f & 7, (bf16x8){lo[0], lo[1], lo[2], lo[3], hi[0], hi[1], hi[2], hi[3]});
      }
      LBAR();
      if (j == 3 && w == 0) boot_ask(p.ws, bt);
      if (j + 4 < 8) stage(j + 4);
      if (j == 6) {
#pragma unroll
        for (int i = 0; i < 10; ++i) row(i >> 2, i & 3);
      }
    }
    p.s0 = uni(lds_at(smem + W_PM)[0] >> 31);
  }
  RDY(128 + g);
  typedef __attribute__((ext_vector_type(8))) unsigned u32x8;
  u32x8 la;
#pragma unroll
  for (int i = 0; i < 8; ++i) la[i] = (i >> 2) * 65536 + r * 1024 + kh * 512 + (((4 * (i & 3) + q) ^ r) << 4);
  const unsigned xw = W_X + w * 2048 + lane * 16;
  u32x32 acc;
  asm volatile("" : "={v[128:159]}"(acc));
  WAIT_V(6);
  LBAR();
#pragma unroll 1
  for (int g0 = 0; g0 <= 12; g0 += 12) {
#define PR_LOOP(TXT)                                                                                                              \
    asm volatile(TXT                                                                                                              \
        : "+{v[128:159]}"(acc)                                                                                                    \
        : W4_IN, "{v[208:215]}"(la), "{v[216:219]}"(vx), "{v220}"(xw), "{v221}"(xw ^ 8192),                                       \
          "{v172}"((ISV ? 2 * cg + kh : 4 * kh + cg) * 1024 + lane * 16), "{v174}"(tid == 256 ? fkq(h, which) : 0x7F000000),      \
          [rt] "s"(raw_rsrc(p.ws + WS_T, T_BYTES)), [rs] "s"(raw_rsrc(p.ws + WS_SY, SY_TOTAL)), [s0] "s"(p.s0 ^ TF_KQV),          \
          [lw] "s"(lw), [kh] "s"(kh), [g0] "s"(g0), [end] "s"(g0 ? NSEG : 12), [x0] "s"(xn + (g0 + 1) * (SEG * XLD * 2)),                       \
          [t0] "s"(which * T_Q + kseg(0, h) - 1)                                                                                  \
        : CLB8(160, 161, 162, 163, 164, 165, 166, 167), "v168", "v169", "v170", "v171", "v173", CLB8(176, 177, 178, 179, 180, 181, 182, 183), \
          CLB8(184, 185, 186, 187, 188, 189, 190, 191), CLB8(192, 193, 194, 195, 196, 197, 198, 199),                             \
          CLB8(200, 201, 202, 203, 204, 205, 206, 207), "s86", "s87", "s88", "s90", "s91", "s92", "s93", "s94", "scc", "memory")
    if (ISV) PR_LOOP(PR_ASM_1); else PR_LOOP(PR_ASM_0);
    if (w == 0 && g0 == 0) wait_rows(p.ws, tag);
  }
}

enum { J_NONE = 0, J_GU = 3, J_PR = 5, J_CB = 8, J_DN = 14 };
struct BigJob { int type, panel, idx; };
struct BigPtr { const char *a, *b0, *b1; };
constexpr int G_LDA = XLD * 2, G_LDB = FF * 2;

__device__ __forceinline__ BigPtr big_ptrs(const Args& p, const BigJob& j) {
  BigPtr r;
  r.a = p.ws + WS_XN + (size_t)j.panel * 128 * G_LDA;
  r.b0 = (const char*)(p.wg + j.idx * 128); r.b1 = (const char*)(p.wu + j.idx * 128);
  return r;
}
__device__ __forceinline__ void big_publish(const Args& p, const BigJob& j) {
  int tid = threadIdx.x;
  asm volatile("" : "+v"(tid));
  if (j.type == J_GU && tid == 0)
    __hip_atomic_fetch_add((gu32*)(p.ws + WS_SY + SY_CH) + j.panel * 16, 1u, __ATOMIC_RELAXED, __HIP_MEMORY_SCOPE_AGENT);
}
typedef f32x4 Acc[2][2][4][2];
__device__ __forceinline__ u32x4 pack32(f32x4 a, f32x4 b) {
  return widen32(cvtpk(a[0], a[1]), cvtpk(a[2], a[3]), cvtpk(b[0], b[1]), cvtpk(b[2], b[3]));
}
#define st_epi(r, o, v) st_sc1(r, o, v)
__device__ __forceinline__ void big_epilogue(const Args& p, const BigJob& j, Acc& acc, char* smem) {
  int tid = threadIdx.x;
  asm volatile("" : "+v"(tid));
  const int lane = tid & 63;
  const int wid = __builtin_amdgcn_readfirstlane(tid >> 6);
  const int wr = wid >> 2, wc = wid & 3;
  {
    const __amdgpu_buffer_rsrc_t ro = mkrsrc(p.ws + WS_HF, S_LEN * 16384);
#pragma unroll
    for (int i = 0; i < 8; ++i) {
      const int row = wr * 64 + (i & 3) * 16 + (lane & 15);
      f32x4 hh[2];
#pragma unroll
      for (int n = 0; n < 2; ++n)
#pragma unroll
        for (int jj = 0; jj < 4; ++jj) {
          const float g = acc[i >> 2][0][i & 3][n][jj], u = acc[i >> 2][1][i & 3][n][jj];
          hh[n][jj] = __builtin_amdgcn_rcpf(1.0f + __builtin_amdgcn_exp2f(g * -1.4426950408889634f)) * u;
        }
      st_epi(ro, h_off(p, j.panel + (i >> 2)) + row * HLD + (j.idx * 128 + wc * 32 + (lane >> 4) * 8) * 2, pack32(hh[0], hh[1]));
    }
  }
}
#define JST(n, i, v) do {} while (0)
__device__ __forceinline__ void gemm_role(Args& p, int g, char* smem) {
  const __amdgpu_buffer_rsrc_t rsy = mkrsrc(p.ws + WS_SY, SY_TOTAL);
  const int x = g & 3, k = g >> 2;
  volatile unsigned* slot = reinterpret_cast<volatile unsigned*>(smem + 163824);
  int rnd = k < 24 ? 2 : -1;
  int nj = 0; (void)nj;
  auto claim = [&]() {
    ++rnd;
    const int n = k < 24 ? 32 + k : rnd * 8 + k - 24, pn = (n * 37) >> 8;
    BigJob b;
    b.type = rnd < 4 ? J_GU : (rnd == 4 ? J_DN : (rnd == 5 && k >= 24 && k - 24 < 14 - NFUSE ? J_CB : J_NONE));
    b.panel = rnd < 4 ? 14 - 2 * pn : (rnd == 4 ? 4 * ((2 - (k >> 3)) & 3) + x : NFUSE + k - 24);
    b.idx = rnd < 4 ? x + 4 * (n - 7 * pn) : k & 7;
    return b;
  };
  if (g < 4 && threadIdx.x == 0) { LST(480 + 4 * g); LSTC(480 + 4 * g + 1); }
  if (k < 24) {
    JST(nj, 0, J_PR); JST(nj, 1, __builtin_amdgcn_s_memrealtime());
    if (k >= 16) job_proj<true>(p, g, 2, x * 8 + (k & 7), smem, rsy);
    else job_proj<false>(p, g, k >> 3, x * 8 + (k & 7), smem, rsy);
    JST(nj, 3, __builtin_amdgcn_s_memrealtime()); ++nj;
  } else {
    const unsigned t0 = (unsigned)__builtin_amdgcn_s_memrealtime();
    const u32x2 tag = start_rows<0>(p, g - 96, []() {});
    WAIT_V(0);
    LBAR();
    report_rows(p, g - 96, tag);
    while ((unsigned)__builtin_amdgcn_s_memrealtime() - t0 < PF_T0) __builtin_amdgcn_s_sleep(8);
#pragma unroll 1
    for (int i = 0; i < PF_N; ++i) {
      const int m = i % 3, o = ((i / 3) * 32 + g - 96) * 448 + (int)threadIdx.x;
      if (threadIdx.x < 448) touch_line((const char*)(m == 0 ? p.wg : (m == 1 ? p.wu : p.wd)) + (size_t)o * 128, smem + (threadIdx.x >> 6) * 256);
      WAIT_V(1);
    }
    WAIT_V(0);
    if (threadIdx.x < 64) {
      wait_rows(p.ws, tag);
      Boot bt;
      boot_ask(p.ws, bt);
      WAIT_V(0);
      *slot = boot_done(p.ws, bt, true, tag);
      while ((unsigned)__builtin_amdgcn_s_memrealtime() - t0 < GU_DELAY) __builtin_amdgcn_s_sleep(8);
    }
    LBAR();
    p.s0 = *slot >> 31;
    RDY(128 + g);
  }
  p.s0 = uni(p.s0);
  BigJob cur = claim();
  while (cur.type != J_NONE) {
    JST(nj, 0, cur.type | cur.panel << 8); JST(nj, 1, __builtin_amdgcn_s_memrealtime());
    if (cur.type != J_GU) {
      if (cur.type == J_DN) job_down(p, cur.panel, cur.idx, smem, rsy);
      else job_comb(p, cur.panel, x, rsy);
      JST(nj, 3, __builtin_amdgcn_s_memrealtime()); ++nj;
      cur = claim();
      continue;
    }
    int tid = threadIdx.x;
    asm volatile("" : "+v"(tid));
    const int lane = tid & 63;
    const int wid = __builtin_amdgcn_readfirstlane(tid >> 6);
    const int wr = wid >> 2, wc = wid & 3;
    const unsigned ra = tid >> 3, ca = 16 * ((tid & 7) ^ ((tid >> 3) & 7)), rb = tid >> 4, cb = 16 * ((tid & 15) ^ swzB((tid >> 4) & 15));
    const unsigned lw = lds_off(smem) + wid * 1024;
#define B8_SA(b, h) (((b) * 2 + (h)) * 16384)
#define B8_SB(b, h) ((4 + (b) * 2 + (h)) * 16384)
#define B8_STA(b, h, P, kt) glds2<true>((P).a + (size_t)(h) * 128 * G_LDA + (kt) * 128, 64 * G_LDA, ra * G_LDA + ca, lw + B8_SA(b, h))
#define B8_STB(b, h, P, kt) glds2(((h) ? (P).b1 : (P).b0) + (size_t)(kt) * 64 * G_LDB, 32 * G_LDB, rb * G_LDB + cb, lw + B8_SB(b, h))
#define B8_LDA(b, h) do { _Pragma("unroll") for (int m = 0; m < 4; ++m) _Pragma("unroll") for (int kk = 0; kk < 2; ++kk) \
      At[m][kk] = ldsf(smem + B8_SA(b, h) + offA(wr * 64 + m * 16 + (lane & 15), kk * 4 + (lane >> 4))); } while (0)
#define B8_LDB(dst, b, h) do { _Pragma("unroll") for (int n = 0; n < 2; ++n) _Pragma("unroll") for (int kk = 0; kk < 2; ++kk) \
      dst[n][kk] = ld_bfrag(smem + B8_SB(b, h), kk, wc * 2 + n, lane); } while (0)
#define B8_MMA(ai, bj, Bt) do { __builtin_amdgcn_s_setprio(1); _Pragma("unroll") for (int m = 0; m < 4; ++m) _Pragma("unroll") for (int n = 0; n < 2; ++n) \
      _Pragma("unroll") for (int kk = 0; kk < 2; ++kk) acc[ai][bj][m][n] = MFMA(Bt[n][kk], At[m][kk], acc[ai][bj][m][n]); __builtin_amdgcn_s_setprio(0); } while (0)
#define WAIT_L(n) asm volatile("s_waitcnt lgkmcnt(%0)" :: "n"(n) : "memory")
#define BAR() __builtin_amdgcn_s_barrier()
#define WAIT_RX(c) asm volatile("s_waitcnt vmcnt(16)\n\ts_cmp_lg_u32 %0, 0\n\ts_cbranch_scc1 .Lrx%=\n\ts_waitcnt vmcnt(8)\n.Lrx%=:" :: "s"(c) : "memory", "scc")
    Acc acc;
    bf16x8 At[4][2], B0[2][2], B1[2][2];
    BigPtr cp = big_ptrs(p, cur);
    BigJob pend; pend.type = J_NONE; pend.panel = 0; pend.idx = 0;
    LBAR();
    B8_STB(0, 0, cp, 0); B8_STB(0, 1, cp, 0); B8_STA(0, 0, cp, 0); B8_STA(0, 1, cp, 0);
    if (wr == 1) BAR();
    WAIT_V(2); BAR();
    B8_STB(1, 0, cp, 1); B8_STA(1, 0, cp, 1); B8_STB(1, 1, cp, 1);
    WAIT_V(6); BAR();
    BigJob nx = pend;
    for (;;) {
      bool chain = false;
      BigPtr np = cp;
      constexpr int nt = DM / 64;
#pragma unroll
      for (int i = 0; i < 32; ++i) acc[i >> 4][(i >> 3) & 1][(i >> 1) & 3][i & 1] = (f32x4){0.f, 0.f, 0.f, 0.f};
#pragma unroll 1
      for (int t = 0; t < nt; t += 2) {
        const bool last = t == nt - 2;
        if (last) {
          nx = claim();
          chain = nx.type == J_GU;
          if (chain) np = big_ptrs(p, nx);
        }
        const BigPtr sp = np;
        const int t2 = last ? 0 : t + 2;
        if (t == 2) big_publish(p, pend);
        const int rx = __builtin_amdgcn_readfirstlane(t == 0 && pend.type != J_NONE);
        B8_LDB(B0, 0, 0); B8_LDB(B1, 0, 1); SCHED(); B8_LDA(0, 0); B8_STA(1, 1, cp, t + 1);
        WAIT_RX(rx);
        WAIT_L(0); BAR(); B8_MMA(0, 0, B0); B8_MMA(0, 1, B1); BAR(); SCHED();
        B8_LDA(0, 1); B8_STB(0, 0, sp, t2); B8_STB(0, 1, sp, t2); B8_STA(0, 0, sp, t2);
        WAIT_RX(rx);
        WAIT_L(0); BAR(); B8_MMA(1, 0, B0); B8_MMA(1, 1, B1); BAR(); SCHED();
        B8_LDB(B0, 1, 0); B8_LDB(B1, 1, 1); SCHED(); B8_LDA(1, 0); B8_STA(0, 1, sp, t2);
        WAIT_V(8); WAIT_L(0); BAR(); B8_MMA(0, 0, B0); B8_MMA(0, 1, B1); BAR(); SCHED();
        B8_LDA(1, 1); B8_STB(1, 0, sp, t2 + 1); B8_STB(1, 1, sp, t2 + 1); B8_STA(1, 0, sp, t2 + 1);
        WAIT_V(8); WAIT_L(0); BAR(); B8_MMA(1, 0, B0); B8_MMA(1, 1, B1); BAR(); SCHED();
      }
      if (wr == 0) BAR();
      JST(nj, 2, __builtin_amdgcn_s_memrealtime());
      big_epilogue(p, cur, acc, smem);
      if (!chain) break;
      JST(nj, 3, __builtin_amdgcn_s_memrealtime()); ++nj;
      pend = cur; cur = nx; cp = np;
      if (wr == 1) BAR();
      JST(nj, 0, cur.type | cur.panel << 8); JST(nj, 1, __builtin_amdgcn_s_memrealtime());
    }
    WAIT_V(0);
    BAR();
    asm volatile(".Lcode_end:");
    big_publish(p, cur);
    JST(nj, 3, __builtin_amdgcn_s_memrealtime()); ++nj;
    cur = nx;
  }
  if (threadIdx.x == 0) { LST(128 + g); }
  if (g < 4 && threadIdx.x == 0) { LST(480 + 4 * g + 2); LSTC(480 + 4 * g + 3); }
}

__device__ __forceinline__ unsigned now_q() { return (unsigned)__builtin_amdgcn_s_memrealtime() * 4u; }
__device__ __forceinline__ void pace_until(unsigned t) {
#pragma unroll 1
  for (int i = 0; i < 256; ++i) {
    const int d = (int)(t - now_q());
    if (d <= 0) break;
    if (d > 32) __builtin_amdgcn_s_sleep(1);
  }
}
#define CLB_E "s76", "s77", "s78", "s79", "s80", "s81", "s82", "s83", "s84", "s85", "s86", "s87", "s88", "s89"
#define EDGE_ASM \
  "s_memtime s[76:77]\n\t" \
  "s_memrealtime s[78:79]\n\t" \
  "s_nop 3\n\t" \
  "s_memrealtime s[80:81]\n\t" \
  "s_nop 3\n\t" \
  "s_memrealtime s[82:83]\n\t" \
  "s_nop 3\n\t" \
  "s_memrealtime s[84:85]\n\t" \
  "s_nop 3\n\t" \
  "s_memrealtime s[86:87]\n\t" \
  "s_nop 3\n\t" \
  "s_memrealtime s[88:89]\n\t" \
  "s_mov_b32 s93, 0\n\t" \
  "s_waitcnt lgkmcnt(0)\n\t" \
  "s_cmp_eq_u32 s80, s78\n\t" \
  "s_addc_u32 s93, s93, 0\n\t" \
  "s_cmp_eq_u32 s82, s78\n\t" \
  "s_addc_u32 s93, s93, 0\n\t" \
  "s_cmp_eq_u32 s84, s78\n\t" \
  "s_addc_u32 s93, s93, 0\n\t" \
  "s_cmp_eq_u32 s86, s78\n\t" \
  "s_addc_u32 s93, s93, 0\n\t" \
  "s_cmp_eq_u32 s88, s78\n\t" \
  "s_addc_u32 s93, s93, 0\n\t" \
  "s_mul_i32 s93, s93, 21\n\t" \
  "s_add_u32 s76, s76, 15\n\t" \
  "s_add_u32 %[kc], s76, s93\n\t" \
  "s_lshl2_add_u32 %[kq], s78, 16\n\t"
__device__ __forceinline__ unsigned time_of(unsigned cyc) {
  unsigned c, q;
  asm volatile(EDGE_ASM : [kc] "=&s"(c), [kq] "=&s"(q) :: CLB_E, "s93", "scc");
  return q - (((c - cyc) * 11900u) >> 16);
}
#define DS_RED 58
#define DS_MEM 30
#define LEAD_DN 2
#define LEAD_UP 12
__device__ __forceinline__ u32x2 ld2_sc1(__amdgpu_buffer_rsrc_t rs, int off, int soff = 0) {
  return __builtin_amdgcn_raw_buffer_load_b64(rs, off, soff, 16);
}
__device__ __forceinline__ void st2_sc1(__amdgpu_buffer_rsrc_t rs, int off, u32x2 v) {
  __builtin_amdgcn_raw_buffer_store_b64(v, rs, off, 0, 16);
}
__device__ __forceinline__ int row_max(int v) {
  v = max(v, __builtin_amdgcn_update_dpp(0, v, 0x128, 0xf, 0xf, false));
  v = max(v, __builtin_amdgcn_update_dpp(0, v, 0x124, 0xf, 0xf, false));
  v = max(v, __builtin_amdgcn_update_dpp(0, v, 0x122, 0xf, 0xf, false));
  v = max(v, __builtin_amdgcn_update_dpp(0, v, 0x121, 0xf, 0xf, false));
  return v;
}
__device__ __forceinline__ int wave_max(int v) {
  v = row_max(v);
  return max(max(__builtin_amdgcn_readlane(v, 0), __builtin_amdgcn_readlane(v, 16)),
             max(__builtin_amdgcn_readlane(v, 32), __builtin_amdgcn_readlane(v, 48)));
}
__device__ __forceinline__ int track(int est, int x, int up, int dn, int out) {
  const int d = x - est;
  return est + (d > out ? 2 : max(min(d, up), -dn));
}
#define WSL 12
#define PLAN_SLEEP 2
__device__ __forceinline__ unsigned latest(unsigned t) {
  const unsigned t0 = __builtin_amdgcn_readfirstlane(t);
  return t0 + wave_max((int)(t - t0));
}
__device__ __forceinline__ void watch(__amdgpu_buffer_rsrc_t rsy, int s, int lane, unsigned t0, const unsigned* tgt_, gu32* abortw, int hs) {
  pace_until(t0);
  u32x2 ra;
  for (unsigned spins = 0;;) {
    asm volatile("" ::: "memory");
    ra = ld2_sc1(rsy, lane * 128, SY_BR + hs);
    const bool all = __all((int)ra[0] >= s);
    SMP(4, s, spins);
    if (all) break;
    if (spins < 3) __builtin_amdgcn_s_sleep(PLAN_SLEEP); else __builtin_amdgcn_s_sleep(WSL);
    if (spin_fail(spins, abortw)) break;
  }
  const unsigned t = latest(ra[0] == (unsigned)s ? ra[1] : t0) + DS_MEM;
  lds_vu_ptr tgt = lds_at(tgt_);
  if (lane < 2) tgt[lane] = lane ? t : s + 1;
  SMP(0, s, (int)(t - t0)); SMP(1, s, (int)(t - now_q()));
}
__device__ __forceinline__ unsigned ld_flag(__amdgpu_buffer_rsrc_t rs, int off) {
  return __builtin_amdgcn_raw_buffer_load_b32(rs, off, 0, 16);
}

constexpr unsigned long long QK_MAP[8] = {0xbaf9876d543210ecull, 0xa98f76d543210bceull, 0x98f76d543210ecbaull, 0x876fd543210bcea9ull, 0x76fd543210ecba98ull, 0x65df43210bcea987ull, 0x5df43210ecba9876ull, 0xd43f210bcea98765ull};
#define QK_ASM_0 \
  "s_waitcnt lgkmcnt(0)\n\t" \
  "v_subrev_u32 v244, 0x8000, v250\n\t" \
  "ds_read_b128 v[216:219], v248 offset:32768\n\t" \
  "ds_read_b128 v[220:223], v248 offset:33792\n\t" \
  "ds_read_b128 v[224:227], v248 offset:34816\n\t" \
  "ds_read_b128 v[228:231], v248 offset:35840\n\t" \
  "ds_read_b128 v[208:211], v244 offset:0\n\t" \
  "ds_read_b128 v[212:215], v244 offset:1024\n\t" \
  "ds_read_b128 v[236:239], v244 offset:2048\n\t" \
  "ds_read_b128 v[240:243], v244 offset:3072\n\t" \
  "v_mov_b64 v[192:193], 0\n\t" \
  "v_mov_b64 v[194:195], 0\n\t" \
  "v_mov_b64 v[196:197], 0\n\t" \
  "v_mov_b64 v[198:199], 0\n\t" \
  "s_movk_i32 s92, 0x2000\n\t" \
  ".Lwt%=:\n\t" \
  "ds_read_b64 v[200:201], v248 offset:30720\n\t" \
  "s_memrealtime s[90:91]\n\t" \
  "s_sub_u32 s92, s92, 1\n\t" \
  "s_waitcnt lgkmcnt(0)\n\t" \
  "v_readfirstlane_b32 s93, v200\n\t" \
  "v_readfirstlane_b32 %[tgt], v201\n\t" \
  "s_cbranch_scc1 .Lgo%=\n\t" \
  "s_cmp_eq_u32 s93, %[step]\n\t" \
  "s_cbranch_scc1 .Lwx%=\n\t" \
  "s_sleep 1\n\t" \
  "s_branch .Lwt%=\n\t" \
  ".Lwx%=:\n\t" \
  "s_lshl_b32 s93, s90, 2\n\t" \
  "s_sub_i32 s93, %[tgt], s93\n\t" \
  "s_cmp_le_i32 s93, 0\n\t" \
  "s_cbranch_scc1 .Lgo%=\n\t" \
  "s_min_i32 s93, s93, 8000\n\t" \
  "s_add_u32 s93, s93, 3\n\t" \
  "s_lshr_b32 s93, s93, 2\n\t" \
  "s_add_u32 s93, s93, s90\n\t" \
  "s_memrealtime s[90:91]\n\t" \
  ".Lpace%=:\n\t" \
  "s_memrealtime s[94:95]\n\t" \
  "s_waitcnt lgkmcnt(1)\n\t" \
  "s_sub_i32 s91, s90, s93\n\t" \
  "s_cmp_ge_i32 s91, 0\n\t" \
  "s_cbranch_scc1 .Lgo%=\n\t" \
  "s_memrealtime s[90:91]\n\t" \
  "s_waitcnt lgkmcnt(1)\n\t" \
  "s_sub_i32 s95, s94, s93\n\t" \
  "s_cmp_lt_i32 s95, 0\n\t" \
  "s_cbranch_scc1 .Lpace%=\n\t" \
  ".Lgo%=:\n\t" \
  "s_mov_b32 s92, 0\n\t" \
  ".Lpoll%=:\n\t" \
  "buffer_load_dwordx4 v[200:203], v248, %[rs], %[som] offen sc1\n\t" \
  "buffer_load_dwordx4 v[204:207], v248, %[rs], %[som2] offen sc1\n\t" \
  "s_waitcnt vmcnt(1) lgkmcnt(0)\n\t" \
  "ds_write_b128 v249, v[200:203]\n\t" \
  "v_smfmac_f32_16x16x64_bf16 v[192:195], v[200:203], v[208:215], v251\n\t" \
  "v_max3_u32 v244, v200, v201, v202\n\t" \
  "v_smfmac_f32_16x16x64_bf16 v[196:199], v[200:203], v[236:243], v251\n\t" \
  "s_waitcnt vmcnt(0)\n\t" \
  "ds_write_b128 v249, v[204:207] offset:8192\n\t" \
  "v_smfmac_f32_16x16x64_bf16 v[192:195], v[204:207], v[216:223], v251\n\t" \
  "v_max3_u32 v244, v244, v203, v204\n\t" \
  "v_max3_u32 v244, v244, v205, v206\n\t" \
  "v_smfmac_f32_16x16x64_bf16 v[196:199], v[204:207], v[224:231], v251\n\t" \
  "v_max_u32 v244, v244, v207\n\t" \
  "v_cmp_le_u32 vcc, %[sent], v244\n\t" \
  "s_cbranch_vccnz .Lslow%=\n\t" \
  ".Lok%=:\n\t" \
  "ds_read_b128 v[216:219], v250 offset:32768\n\t" \
  "ds_read_b128 v[220:223], v250 offset:33792\n\t" \
  "ds_read_b128 v[224:227], v250 offset:34816\n\t" \
  "ds_read_b128 v[228:231], v250 offset:35840\n\t" \
  "s_waitcnt lgkmcnt(4)\n\t" \
  "s_barrier\n\t" \
  "ds_read_b128 v[208:211], v248 offset:1024\n\t" \
  "ds_read_b128 v[212:215], v248 offset:2048\n\t" \
  "ds_read_b128 v[236:239], v248 offset:3072\n\t" \
  "ds_read_b128 v[240:243], v248 offset:4096\n\t" \
  "s_waitcnt lgkmcnt(3)\n\t" \
  "ds_read_b128 v[244:247], v248 offset:13312\n\t" \
  "v_smfmac_f32_16x16x64_bf16 v[192:195], v[208:211], v[216:223], v251\n\t" \
  "ds_read_b128 v[216:219], v250 offset:0\n\t" \
  "v_smfmac_f32_16x16x64_bf16 v[196:199], v[208:211], v[224:231], v251\n\t" \
  "s_waitcnt lgkmcnt(4)\n\t" \
  "ds_read_b128 v[220:223], v250 offset:1024\n\t" \
  "v_smfmac_f32_16x16x64_bf16 v[192:195], v[212:215], v[0:7], v251\n\t" \
  "ds_read_b128 v[224:227], v250 offset:2048\n\t" \
  "v_smfmac_f32_16x16x64_bf16 v[196:199], v[212:215], v[8:15], v251\n\t" \
  "s_waitcnt lgkmcnt(5)\n\t" \
  "ds_read_b128 v[228:231], v250 offset:3072\n\t" \
  "v_smfmac_f32_16x16x64_bf16 v[192:195], v[236:239], v[16:23], v251\n\t" \
  "ds_read_b128 v[200:203], v248 offset:5120\n\t" \
  "v_smfmac_f32_16x16x64_bf16 v[196:199], v[236:239], v[24:31], v251\n\t" \
  "s_waitcnt lgkmcnt(6)\n\t" \
  "ds_read_b128 v[204:207], v248 offset:6144\n\t" \
  "v_smfmac_f32_16x16x64_bf16 v[192:195], v[240:243], v[32:39], v251\n\t" \
  "ds_read_b128 v[208:211], v248 offset:7168\n\t" \
  "v_smfmac_f32_16x16x64_bf16 v[196:199], v[240:243], v[40:47], v251\n\t" \
  "s_waitcnt lgkmcnt(5)\n\t" \
  "ds_read_b128 v[212:215], v248 offset:9216\n\t" \
  "v_smfmac_f32_16x16x64_bf16 v[192:195], v[244:247], v[216:223], v251\n\t" \
  "s_waitcnt lgkmcnt(4)\n\t" \
  "v_smfmac_f32_16x16x64_bf16 v[196:199], v[244:247], v[224:231], v251\n\t" \
  "s_waitcnt lgkmcnt(3)\n\t" \
  "ds_read_b128 v[236:239], v248 offset:10240\n\t" \
  "v_smfmac_f32_16x16x64_bf16 v[192:195], v[200:203], v[48:55], v251\n\t" \
  "ds_read_b128 v[240:243], v248 offset:11264\n\t" \
  "v_smfmac_f32_16x16x64_bf16 v[196:199], v[200:203], v[56:63], v251\n\t" \
  "s_waitcnt lgkmcnt(4)\n\t" \
  "v_smfmac_f32_16x16x64_bf16 v[192:195], v[204:207], v[64:71], v251\n\t" \
  "ds_read_b128 v[244:247], v248 offset:12288\n\t" \
  "v_smfmac_f32_16x16x64_bf16 v[196:199], v[204:207], v[72:79], v251\n\t" \
  "s_waitcnt lgkmcnt(4)\n\t" \
  "v_smfmac_f32_16x16x64_bf16 v[192:195], v[208:211], v[80:87], v251\n\t" \
  "ds_read_b128 v[200:203], v248 offset:14336\n\t" \
  "v_smfmac_f32_16x16x64_bf16 v[196:199], v[208:211], v[88:95], v251\n\t" \
  "s_waitcnt lgkmcnt(4)\n\t" \
  "v_smfmac_f32_16x16x64_bf16 v[192:195], v[212:215], v[96:103], v251\n\t" \
  "ds_read_b128 v[204:207], v248 offset:15360\n\t" \
  "v_smfmac_f32_16x16x64_bf16 v[196:199], v[212:215], v[104:111], v251\n\t" \
  "s_waitcnt lgkmcnt(3)\n\t" \
  "v_smfmac_f32_16x16x64_bf16 v[192:195], v[236:239], v[112:119], v251\n\t" \
  "v_smfmac_f32_16x16x64_bf16 v[196:199], v[236:239], v[120:127], v251\n\t" \
  "v_smfmac_f32_16x16x64_bf16 v[192:195], v[240:243], v[128:135], v251\n\t" \
  "v_smfmac_f32_16x16x64_bf16 v[196:199], v[240:243], v[136:143], v251\n\t" \
  "s_waitcnt lgkmcnt(1)\n\t" \
  "v_smfmac_f32_16x16x64_bf16 v[192:195], v[244:247], v[144:151], v251\n\t" \
  "v_smfmac_f32_16x16x64_bf16 v[196:199], v[244:247], v[152:159], v251\n\t" \
  "v_smfmac_f32_16x16x64_bf16 v[192:195], v[200:203], v[160:167], v251\n\t" \
  "v_smfmac_f32_16x16x64_bf16 v[196:199], v[200:203], v[168:175], v251\n\t" \
  "s_waitcnt lgkmcnt(0)\n\t" \
  "v_smfmac_f32_16x16x64_bf16 v[192:195], v[204:207], v[176:183], v251\n\t" \
  "v_smfmac_f32_16x16x64_bf16 v[196:199], v[204:207], v[184:191], v251\n\t" \
  "ds_read_b128 v[200:203], v248 offset:16384\n\t" \
  "ds_read_b128 v[204:207], v248 offset:20480\n\t" \
  "v_mov_b32 v250, v232\n\t" \
  "v_mov_b32 v249, v233\n\t" \
  "s_nop 2\n\t" \
  "v_add_f32 v192, v192, v194\n\t" \
  "v_add_f32 v193, v193, v195\n\t" \
  "v_add_f32 v196, v196, v198\n\t" \
  "v_add_f32 v197, v197, v199\n\t" \
  "v_cvt_pk_bf16_f32 v192, v192, v193\n\t" \
  "v_cvt_pk_bf16_f32 v196, v196, v197\n\t" \
  "ds_write2_b32 v252, v192, v196 offset1:64\n\t" \
  "v_mov_b32 v251, v234\n\t" \
  "ds_read_b64_tr_b16 v[232:233], v250 offset:0\n\t" \
  "ds_read_b64_tr_b16 v[234:235], v250 offset:64\n\t" \
  "v_mov_b32 v212, %[step]\n\t" \
  "s_mov_b64 exec, 1\n\t" \
  "ds_write_b32 v248, v212 offset:28672\n\t" \
  "s_mov_b64 exec, -1\n\t" \
  "buffer_load_dwordx4 v[192:195], v248, %[rt], %[svt] offen offset:0 sc1\n\t" \
  "buffer_load_dwordx4 v[196:199], v248, %[rt], %[svt] offen offset:1024 sc1\n\t" \
  "s_waitcnt lgkmcnt(1)\n\t" \
  "v_mfma_f32_16x16x32_bf16 v[200:203], v[200:203], v[232:235], 0\n\t" \
  "v_mfma_f32_16x16x32_bf16 v[204:207], v[204:207], v[232:235], 0\n\t" \
  "s_nop 7\n\t" \
  "v_cndmask_b32 v216, v200, v204, %[nh]\n\t" \
  "v_cndmask_b32 v217, v201, v205, %[nh]\n\t" \
  "v_cndmask_b32 v218, v202, v206, %[nh]\n\t" \
  "v_cndmask_b32 v219, v203, v207, %[nh]\n\t" \
  "ds_write_b128 v248, v[216:219] offset:16384\n\t" \
  "buffer_load_dword v252, v249, %[rs], %[sv] offen offset:0 sc1\n\t" \
  "buffer_load_dword v253, v249, %[rs], %[sv] offen offset:256 sc1\n\t" \
  "ds_read_b32 v212, v248 offset:28672\n\t" \
  "ds_read_b64_tr_b16 v[236:237], v250 offset:512\n\t" \
  "ds_read_b64_tr_b16 v[238:239], v250 offset:576\n\t" \
  "ds_read_b64_tr_b16 v[240:241], v250 offset:1024\n\t" \
  "ds_read_b64_tr_b16 v[242:243], v250 offset:1088\n\t" \
  "ds_read_b64_tr_b16 v[244:245], v250 offset:1536\n\t" \
  "ds_read_b64_tr_b16 v[246:247], v250 offset:1600\n\t" \
  "s_setprio 3\n\t" \
  "s_waitcnt lgkmcnt(0)\n\t" \
  "v_cmp_ne_u32 vcc, %[step], v212\n\t" \
  "s_barrier\n\t" \
  "s_and_b32 s93, vcc_lo, 0xf\n\t" \
  "s_cbranch_scc1 .Lre%=\n\t" \
  ".Lrb%=:\n\t" \
  "ds_read_b128 v[220:223], v248 offset:17408\n\t" \
  "ds_read_b128 v[224:227], v248 offset:18432\n\t" \
  "ds_read_b128 v[228:231], v248 offset:19456\n\t" \
  "ds_read_b64_tr_b16 v[200:201], v250 offset:2048\n\t" \
  "ds_read_b64_tr_b16 v[202:203], v250 offset:2112\n\t" \
  "ds_read_b64_tr_b16 v[204:205], v250 offset:2560\n\t" \
  "ds_read_b64_tr_b16 v[206:207], v250 offset:2624\n\t" \
  "ds_read_b64_tr_b16 v[208:209], v250 offset:3072\n\t" \
  "ds_read_b64_tr_b16 v[210:211], v250 offset:3136\n\t" \
  "ds_read_b64_tr_b16 v[212:213], v250 offset:3584\n\t" \
  "ds_read_b64_tr_b16 v[214:215], v250 offset:3648\n\t" \
  "s_waitcnt lgkmcnt(10)\n\t" \
  "v_pk_add_f32 v[216:217], v[216:217], v[220:221]\n\t" \
  "v_pk_add_f32 v[218:219], v[218:219], v[222:223]\n\t" \
  "s_waitcnt lgkmcnt(8)\n\t" \
  "v_pk_add_f32 v[224:225], v[224:225], v[228:229]\n\t" \
  "v_pk_add_f32 v[226:227], v[226:227], v[230:231]\n\t" \
  "v_pk_add_f32 v[216:217], v[216:217], v[224:225]\n\t" \
  "v_pk_add_f32 v[218:219], v[218:219], v[226:227]\n\t" \
  "s_waitcnt lgkmcnt(6)\n\t" \
  "v_mfma_f32_16x16x32_bf16 v[200:203], v[200:203], v[232:235], 0\n\t" \
  "v_max3_f32 v224, v216, v217, v218\n\t" \
  "v_max_f32 v224, v224, v219\n\t" \
  "s_waitcnt lgkmcnt(4)\n\t" \
  "v_mfma_f32_16x16x32_bf16 v[200:203], v[204:207], v[236:239], v[200:203]\n\t" \
  "v_max_f32_dpp v225, v224, v224 row_ror:8 row_mask:0xf bank_mask:0xf\n\t" \
  "v_max_f32_dpp v226, v224, v224 row_ror:8 row_mask:0xf bank_mask:0xf\n\t" \
  "s_waitcnt lgkmcnt(2)\n\t" \
  "v_mfma_f32_16x16x32_bf16 v[200:203], v[208:211], v[240:243], v[200:203]\n\t" \
  "v_permlane32_swap_b32 v225, v226\n\t" \
  "s_waitcnt lgkmcnt(0)\n\t" \
  "v_mfma_f32_16x16x32_bf16 v[200:203], v[212:215], v[244:247], v[200:203]\n\t" \
  "v_max_f32 v231, v225, v226\n\t" \
  "v_mov_b32 v220, 0x3f803f80\n\t" \
  "v_mov_b32 v221, 0x3f803f80\n\t" \
  "v_mov_b64 v[222:223], v[220:221]\n\t" \
  "v_mov_b32 v233, 0\n\t" \
  "v_mov_b32 v234, 0\n\t" \
  "s_nop 1\n\t" \
  "v_max3_f32 v204, v200, v201, v231\n\t" \
  "v_max3_f32 v205, v204, v202, v203\n\t" \
  "v_max3_f32 v206, v204, v202, v203\n\t" \
  "v_cndmask_b32 v200, v200, v202, %[up]\n\t" \
  "v_cndmask_b32 v201, v201, v203, %[up]\n\t" \
  "v_permlane16_swap_b32 v205, v206\n\t" \
  "v_max_f32 v204, v205, v206\n\t" \
  "v_mul_f32 v204, %[negc], v204\n\t" \
  "v_fma_f32 v200, v200, %[c], v204\n\t" \
  "v_fma_f32 v201, v201, %[c], v204\n\t" \
  "v_exp_f32 v200, v200\n\t" \
  "v_fma_f32 v216, v216, %[c], v204\n\t" \
  "v_exp_f32 v201, v201\n\t" \
  "v_fma_f32 v217, v217, %[c], v204\n\t" \
  "v_exp_f32 v216, v216\n\t" \
  "v_fma_f32 v218, v218, %[c], v204\n\t" \
  "v_cvt_pk_bf16_f32 v232, v200, v201\n\t" \
  "v_cvt_pk_bf16_f32 v235, v200, v201\n\t" \
  "v_exp_f32 v217, v217\n\t" \
  "v_fma_f32 v219, v219, %[c], v204\n\t" \
  "v_mfma_f32_16x16x16_bf16 v[212:215], v[220:221], v[232:233], 0\n\t" \
  "v_exp_f32 v218, v218\n\t" \
  "s_waitcnt vmcnt(0)\n\t" \
  "v_mfma_f32_16x16x16_bf16 v[224:227], v[252:253], v[232:233], 0\n\t" \
  "v_cvt_pk_bf16_f32 v210, v216, v217\n\t" \
  "v_cvt_pk_bf16_f32 v208, v216, v217\n\t" \
  "v_exp_f32 v219, v219\n\t" \
  "v_mfma_f32_16x16x16_bf16 v[228:231], v[252:253], v[234:235], 0\n\t" \
  "v_cvt_pk_bf16_f32 v211, v218, v219\n\t" \
  "v_cvt_pk_bf16_f32 v209, v218, v219\n\t" \
  "v_mov_b32_dpp v208, v210 row_ror:8 row_mask:0xf bank_mask:0xc\n\t" \
  "v_mov_b32_dpp v210, v210 row_ror:8 row_mask:0xf bank_mask:0x3\n\t" \
  "v_mov_b32_dpp v209, v211 row_ror:8 row_mask:0xf bank_mask:0xc\n\t" \
  "v_mov_b32_dpp v211, v211 row_ror:8 row_mask:0xf bank_mask:0x3\n\t" \
  "v_max_u32 v204, v252, v253\n\t" \
  "v_cmp_le_u32 vcc, %[sent], v204\n\t" \
  "v_mfma_f32_16x16x32_bf16 v[212:215], v[220:223], v[208:211], v[212:215]\n\t" \
  ".Lpv%=:\n\t" \
  "v_mfma_f32_16x16x32_bf16 v[224:227], v[192:195], v[208:211], v[224:227]\n\t" \
  "v_mfma_f32_16x16x32_bf16 v[228:231], v[196:199], v[208:211], v[228:231]\n\t" \
  "s_cbranch_vccnz .Lvs%=\n\t" \
  ".Lvok%=:\n\t" \
  "s_nop 4\n\t" \
  "v_rcp_f32 v204, v212\n\t" \
  "v_cndmask_b32 v224, v224, v226, %[nh]\n\t" \
  "v_cndmask_b32 v225, v225, v227, %[nh]\n\t" \
  "v_mul_f32 v224, v224, v204\n\t" \
  "v_mul_f32 v225, v225, v204\n\t" \
  "v_cvt_pk_bf16_f32 v224, v224, v225\n\t" \
  "buffer_store_dword v224, v251, %[rs], %[so] offen offset:0 sc1\n\t" \
  "v_cndmask_b32 v228, v228, v230, %[nh]\n\t" \
  "v_cndmask_b32 v229, v229, v231, %[nh]\n\t" \
  "v_mul_f32 v228, v228, v204\n\t" \
  "v_mul_f32 v229, v229, v204\n\t" \
  "v_cvt_pk_bf16_f32 v228, v228, v229\n\t" \
  "buffer_store_dword v228, v251, %[rs], %[so] offen offset:256 sc1\n\t" \
  "s_memtime s[90:91]\n\t" \
  "s_setprio 0\n\t" \
  "s_waitcnt lgkmcnt(0)\n\t" \
  "s_mov_b32 %[tout], s90\n\t" \
  "s_mov_b32 %[spins], s92\n\t" \
  "s_branch .Lend%=\n\t" \
  ".Lre%=:\n\t" \
  "ds_read_b64_tr_b16 v[236:237], v250 offset:512\n\t" \
  "ds_read_b64_tr_b16 v[238:239], v250 offset:576\n\t" \
  "ds_read_b64_tr_b16 v[240:241], v250 offset:1024\n\t" \
  "ds_read_b64_tr_b16 v[242:243], v250 offset:1088\n\t" \
  "ds_read_b64_tr_b16 v[244:245], v250 offset:1536\n\t" \
  "ds_read_b64_tr_b16 v[246:247], v250 offset:1600\n\t" \
  "s_waitcnt lgkmcnt(0)\n\t" \
  "s_branch .Lrb%=\n\t" \
  ".Lvs%=:\n\t" \
  "v_cmp_eq_u32 vcc, %[sent], v252\n\t" \
  "v_cmp_eq_u32 s[94:95], %[sent], v253\n\t" \
  "s_or_b64 s[94:95], s[94:95], vcc\n\t" \
  "s_cbranch_scc0 .Lvok%=\n\t" \
  "s_add_u32 s92, s92, 1\n\t" \
  "s_sleep 2\n\t" \
  "s_and_b32 s93, s92, 63\n\t" \
  "s_cbranch_scc1 .Lvr%=\n\t" \
  "buffer_load_dword v204, off, %[rs], %[sab] sc1\n\t" \
  "s_waitcnt vmcnt(0)\n\t" \
  "v_readfirstlane_b32 s93, v204\n\t" \
  "s_nop 3\n\t" \
  "s_cmp_eq_u32 s93, 1\n\t" \
  "s_cbranch_scc1 .Lvok%=\n\t" \
  "s_cmp_lt_u32 s92, 0x80000\n\t" \
  "s_cbranch_scc1 .Lvr%=\n\t" \
  "v_mov_b32 v204, 1\n\t" \
  "buffer_store_dword v204, off, %[rs], %[sab] sc1\n\t" \
  "s_branch .Lvok%=\n\t" \
  ".Lvr%=:\n\t" \
  "buffer_load_dword v252, v249, %[rs], %[sv] offen offset:0 sc1\n\t" \
  "buffer_load_dword v253, v249, %[rs], %[sv] offen offset:256 sc1\n\t" \
  "s_waitcnt vmcnt(0)\n\t" \
  "v_mfma_f32_16x16x16_bf16 v[224:227], v[252:253], v[232:233], 0\n\t" \
  "v_mfma_f32_16x16x16_bf16 v[228:231], v[252:253], v[234:235], 0\n\t" \
  "v_max_u32 v204, v252, v253\n\t" \
  "v_cmp_le_u32 vcc, %[sent], v204\n\t" \
  "s_nop 4\n\t" \
  "s_branch .Lpv%=\n\t" \
  "s_branch .Lend%=\n\t" \
  ".Lslow%=:\n\t" \
  "s_mov_b64 s[94:95], 0\n\t" \
  "v_cmp_eq_u32 vcc, %[sent], v200\n\t" \
  "s_or_b64 s[94:95], s[94:95], vcc\n\t" \
  "v_cmp_eq_u32 vcc, %[sent], v201\n\t" \
  "s_or_b64 s[94:95], s[94:95], vcc\n\t" \
  "v_cmp_eq_u32 vcc, %[sent], v202\n\t" \
  "s_or_b64 s[94:95], s[94:95], vcc\n\t" \
  "v_cmp_eq_u32 vcc, %[sent], v203\n\t" \
  "s_or_b64 s[94:95], s[94:95], vcc\n\t" \
  "v_cmp_eq_u32 vcc, %[sent], v204\n\t" \
  "s_or_b64 s[94:95], s[94:95], vcc\n\t" \
  "v_cmp_eq_u32 vcc, %[sent], v205\n\t" \
  "s_or_b64 s[94:95], s[94:95], vcc\n\t" \
  "v_cmp_eq_u32 vcc, %[sent], v206\n\t" \
  "s_or_b64 s[94:95], s[94:95], vcc\n\t" \
  "v_cmp_eq_u32 vcc, %[sent], v207\n\t" \
  "s_or_b64 s[94:95], s[94:95], vcc\n\t" \
  "s_cmp_eq_u64 s[94:95], 0\n\t" \
  "s_cbranch_scc1 .Lok%=\n\t" \
  "v_mov_b64 v[192:193], 0\n\t" \
  "v_mov_b64 v[194:195], 0\n\t" \
  "v_mov_b64 v[196:197], 0\n\t" \
  "v_mov_b64 v[198:199], 0\n\t" \
  "s_add_u32 s92, s92, 1\n\t" \
  "s_sleep 6\n\t" \
  "s_cmp_eq_u32 s92, 1\n\t" \
  "s_cbranch_scc1 .Lpoll%=\n\t" \
  "s_sleep 1\n\t" \
  "s_and_b32 s93, s92, 63\n\t" \
  "s_cmp_lg_u32 s93, 0\n\t" \
  "s_cbranch_scc1 .Lpoll%=\n\t" \
  "v_mov_b32 v245, 0\n\t" \
  "buffer_load_dword v244, v245, %[rs], %[sab] offen sc1\n\t" \
  "s_waitcnt vmcnt(0)\n\t" \
  "v_readfirstlane_b32 s93, v244\n\t" \
  "s_nop 3\n\t" \
  "s_cmp_eq_u32 s93, 1\n\t" \
  "s_cbranch_scc1 .Lok%=\n\t" \
  "s_cmp_lt_u32 s92, 0x80000\n\t" \
  "s_cbranch_scc1 .Lpoll%=\n\t" \
  "v_mov_b32 v244, 1\n\t" \
  "buffer_store_dword v244, v245, %[rs], %[sab] offen sc1\n\t" \
  "s_branch .Lok%=\n\t" \
  ".Lend%=:\n\t"
#define QK_ASM_1 \
  "s_waitcnt lgkmcnt(0)\n\t" \
  "v_subrev_u32 v244, 0x8000, v250\n\t" \
  "ds_read_b128 v[216:219], v248 offset:36864\n\t" \
  "ds_read_b128 v[220:223], v248 offset:37888\n\t" \
  "ds_read_b128 v[224:227], v248 offset:38912\n\t" \
  "ds_read_b128 v[228:231], v248 offset:39936\n\t" \
  "ds_read_b128 v[208:211], v244 offset:0\n\t" \
  "ds_read_b128 v[212:215], v244 offset:1024\n\t" \
  "ds_read_b128 v[236:239], v244 offset:2048\n\t" \
  "ds_read_b128 v[240:243], v244 offset:3072\n\t" \
  "v_mov_b64 v[192:193], 0\n\t" \
  "v_mov_b64 v[194:195], 0\n\t" \
  "v_mov_b64 v[196:197], 0\n\t" \
  "v_mov_b64 v[198:199], 0\n\t" \
  "s_movk_i32 s92, 0x2000\n\t" \
  ".Lwt%=:\n\t" \
  "ds_read_b64 v[200:201], v248 offset:30720\n\t" \
  "s_memrealtime s[90:91]\n\t" \
  "s_sub_u32 s92, s92, 1\n\t" \
  "s_waitcnt lgkmcnt(0)\n\t" \
  "v_readfirstlane_b32 s93, v200\n\t" \
  "v_readfirstlane_b32 %[tgt], v201\n\t" \
  "s_cbranch_scc1 .Lgo%=\n\t" \
  "s_cmp_eq_u32 s93, %[step]\n\t" \
  "s_cbranch_scc1 .Lwx%=\n\t" \
  "s_sleep 1\n\t" \
  "s_branch .Lwt%=\n\t" \
  ".Lwx%=:\n\t" \
  "s_lshl_b32 s93, s90, 2\n\t" \
  "s_sub_i32 s93, %[tgt], s93\n\t" \
  "s_cmp_le_i32 s93, 0\n\t" \
  "s_cbranch_scc1 .Lgo%=\n\t" \
  "s_min_i32 s93, s93, 8000\n\t" \
  "s_add_u32 s93, s93, 3\n\t" \
  "s_lshr_b32 s93, s93, 2\n\t" \
  "s_add_u32 s93, s93, s90\n\t" \
  "s_memrealtime s[90:91]\n\t" \
  ".Lpace%=:\n\t" \
  "s_memrealtime s[94:95]\n\t" \
  "s_waitcnt lgkmcnt(1)\n\t" \
  "s_sub_i32 s91, s90, s93\n\t" \
  "s_cmp_ge_i32 s91, 0\n\t" \
  "s_cbranch_scc1 .Lgo%=\n\t" \
  "s_memrealtime s[90:91]\n\t" \
  "s_waitcnt lgkmcnt(1)\n\t" \
  "s_sub_i32 s95, s94, s93\n\t" \
  "s_cmp_lt_i32 s95, 0\n\t" \
  "s_cbranch_scc1 .Lpace%=\n\t" \
  ".Lgo%=:\n\t" \
  "s_mov_b32 s92, 0\n\t" \
  ".Lpoll%=:\n\t" \
  "buffer_load_dwordx4 v[200:203], v248, %[rs], %[som] offen sc1\n\t" \
  "buffer_load_dwordx4 v[204:207], v248, %[rs], %[som2] offen sc1\n\t" \
  "s_waitcnt vmcnt(1) lgkmcnt(0)\n\t" \
  "ds_write_b128 v249, v[200:203]\n\t" \
  "v_smfmac_f32_16x16x64_bf16 v[192:195], v[200:203], v[208:215], v251\n\t" \
  "v_max3_u32 v244, v200, v201, v202\n\t" \
  "v_smfmac_f32_16x16x64_bf16 v[196:199], v[200:203], v[236:243], v251\n\t" \
  "s_waitcnt vmcnt(0)\n\t" \
  "ds_write_b128 v249, v[204:207] offset:8192\n\t" \
  "v_smfmac_f32_16x16x64_bf16 v[192:195], v[204:207], v[216:223], v251\n\t" \
  "v_max3_u32 v244, v244, v203, v204\n\t" \
  "v_max3_u32 v244, v244, v205, v206\n\t" \
  "v_smfmac_f32_16x16x64_bf16 v[196:199], v[204:207], v[224:231], v251\n\t" \
  "v_max_u32 v244, v244, v207\n\t" \
  "v_cmp_le_u32 vcc, %[sent], v244\n\t" \
  "s_cbranch_vccnz .Lslow%=\n\t" \
  ".Lok%=:\n\t" \
  "ds_read_b128 v[216:219], v250 offset:32768\n\t" \
  "ds_read_b128 v[220:223], v250 offset:33792\n\t" \
  "ds_read_b128 v[224:227], v250 offset:34816\n\t" \
  "ds_read_b128 v[228:231], v250 offset:35840\n\t" \
  "s_waitcnt lgkmcnt(4)\n\t" \
  "s_barrier\n\t" \
  "ds_read_b128 v[208:211], v248 offset:0\n\t" \
  "ds_read_b128 v[212:215], v248 offset:3072\n\t" \
  "ds_read_b128 v[236:239], v248 offset:4096\n\t" \
  "ds_read_b128 v[240:243], v248 offset:5120\n\t" \
  "s_waitcnt lgkmcnt(3)\n\t" \
  "ds_read_b128 v[244:247], v248 offset:12288\n\t" \
  "v_smfmac_f32_16x16x64_bf16 v[192:195], v[208:211], v[216:223], v251\n\t" \
  "ds_read_b128 v[216:219], v250 offset:0\n\t" \
  "v_smfmac_f32_16x16x64_bf16 v[196:199], v[208:211], v[224:231], v251\n\t" \
  "s_waitcnt lgkmcnt(4)\n\t" \
  "ds_read_b128 v[220:223], v250 offset:1024\n\t" \
  "v_smfmac_f32_16x16x64_bf16 v[192:195], v[212:215], v[0:7], v251\n\t" \
  "ds_read_b128 v[224:227], v250 offset:2048\n\t" \
  "v_smfmac_f32_16x16x64_bf16 v[196:199], v[212:215], v[8:15], v251\n\t" \
  "s_waitcnt lgkmcnt(5)\n\t" \
  "ds_read_b128 v[228:231], v250 offset:3072\n\t" \
  "v_smfmac_f32_16x16x64_bf16 v[192:195], v[236:239], v[16:23], v251\n\t" \
  "ds_read_b128 v[200:203], v248 offset:6144\n\t" \
  "v_smfmac_f32_16x16x64_bf16 v[196:199], v[236:239], v[24:31], v251\n\t" \
  "s_waitcnt lgkmcnt(6)\n\t" \
  "ds_read_b128 v[204:207], v248 offset:7168\n\t" \
  "v_smfmac_f32_16x16x64_bf16 v[192:195], v[240:243], v[32:39], v251\n\t" \
  "ds_read_b128 v[208:211], v248 offset:8192\n\t" \
  "v_smfmac_f32_16x16x64_bf16 v[196:199], v[240:243], v[40:47], v251\n\t" \
  "s_waitcnt lgkmcnt(5)\n\t" \
  "ds_read_b128 v[212:215], v248 offset:10240\n\t" \
  "v_smfmac_f32_16x16x64_bf16 v[192:195], v[244:247], v[216:223], v251\n\t" \
  "s_waitcnt lgkmcnt(4)\n\t" \
  "v_smfmac_f32_16x16x64_bf16 v[196:199], v[244:247], v[224:231], v251\n\t" \
  "s_waitcnt lgkmcnt(3)\n\t" \
  "ds_read_b128 v[236:239], v248 offset:11264\n\t" \
  "v_smfmac_f32_16x16x64_bf16 v[192:195], v[200:203], v[48:55], v251\n\t" \
  "ds_read_b128 v[240:243], v248 offset:13312\n\t" \
  "v_smfmac_f32_16x16x64_bf16 v[196:199], v[200:203], v[56:63], v251\n\t" \
  "s_waitcnt lgkmcnt(4)\n\t" \
  "v_smfmac_f32_16x16x64_bf16 v[192:195], v[204:207], v[64:71], v251\n\t" \
  "ds_read_b128 v[244:247], v248 offset:14336\n\t" \
  "v_smfmac_f32_16x16x64_bf16 v[196:199], v[204:207], v[72:79], v251\n\t" \
  "s_waitcnt lgkmcnt(4)\n\t" \
  "v_smfmac_f32_16x16x64_bf16 v[192:195], v[208:211], v[80:87], v251\n\t" \
  "ds_read_b128 v[200:203], v248 offset:15360\n\t" \
  "v_smfmac_f32_16x16x64_bf16 v[196:199], v[208:211], v[88:95], v251\n\t" \
  "s_waitcnt lgkmcnt(4)\n\t" \
  "v_smfmac_f32_16x16x64_bf16 v[192:195], v[212:215], v[96:103], v251\n\t" \
  "ds_read_b128 v[204:207], v248 offset:2048\n\t" \
  "v_smfmac_f32_16x16x64_bf16 v[196:199], v[212:215], v[104:111], v251\n\t" \
  "s_waitcnt lgkmcnt(3)\n\t" \
  "v_smfmac_f32_16x16x64_bf16 v[192:195], v[236:239], v[112:119], v251\n\t" \
  "v_smfmac_f32_16x16x64_bf16 v[196:199], v[236:239], v[120:127], v251\n\t" \
  "v_smfmac_f32_16x16x64_bf16 v[192:195], v[240:243], v[128:135], v251\n\t" \
  "v_smfmac_f32_16x16x64_bf16 v[196:199], v[240:243], v[136:143], v251\n\t" \
  "s_waitcnt lgkmcnt(1)\n\t" \
  "v_smfmac_f32_16x16x64_bf16 v[192:195], v[244:247], v[144:151], v251\n\t" \
  "v_smfmac_f32_16x16x64_bf16 v[196:199], v[244:247], v[152:159], v251\n\t" \
  "v_smfmac_f32_16x16x64_bf16 v[192:195], v[200:203], v[160:167], v251\n\t" \
  "v_smfmac_f32_16x16x64_bf16 v[196:199], v[200:203], v[168:175], v251\n\t" \
  "s_waitcnt lgkmcnt(0)\n\t" \
  "v_smfmac_f32_16x16x64_bf16 v[192:195], v[204:207], v[176:183], v251\n\t" \
  "v_smfmac_f32_16x16x64_bf16 v[196:199], v[204:207], v[184:191], v251\n\t" \
  "ds_read_b128 v[200:203], v248 offset:17408\n\t" \
  "ds_read_b128 v[204:207], v248 offset:21504\n\t" \
  "v_mov_b32 v250, v232\n\t" \
  "v_mov_b32 v249, v233\n\t" \
  "s_nop 2\n\t" \
  "v_add_f32 v192, v192, v194\n\t" \
  "v_add_f32 v193, v193, v195\n\t" \
  "v_add_f32 v196, v196, v198\n\t" \
  "v_add_f32 v197, v197, v199\n\t" \
  "v_cvt_pk_bf16_f32 v192, v192, v193\n\t" \
  "v_cvt_pk_bf16_f32 v196, v196, v197\n\t" \
  "ds_write2_b32 v252, v192, v196 offset1:64\n\t" \
  "v_mov_b32 v251, v234\n\t" \
  "ds_read_b64_tr_b16 v[236:237], v250 offset:512\n\t" \
  "ds_read_b64_tr_b16 v[238:239], v250 offset:576\n\t" \
  "v_mov_b32 v212, %[step]\n\t" \
  "s_mov_b64 exec, 1\n\t" \
  "ds_write_b32 v248, v212 offset:28688\n\t" \
  "s_mov_b64 exec, -1\n\t" \
  "buffer_load_dwordx4 v[192:195], v248, %[rt], %[svt] offen offset:0 sc1\n\t" \
  "buffer_load_dwordx4 v[196:199], v248, %[rt], %[svt] offen offset:1024 sc1\n\t" \
  "s_waitcnt lgkmcnt(1)\n\t" \
  "v_mfma_f32_16x16x32_bf16 v[200:203], v[200:203], v[236:239], 0\n\t" \
  "v_mfma_f32_16x16x32_bf16 v[204:207], v[204:207], v[236:239], 0\n\t" \
  "s_nop 7\n\t" \
  "v_cndmask_b32 v216, v200, v204, %[nh]\n\t" \
  "v_cndmask_b32 v217, v201, v205, %[nh]\n\t" \
  "v_cndmask_b32 v218, v202, v206, %[nh]\n\t" \
  "v_cndmask_b32 v219, v203, v207, %[nh]\n\t" \
  "ds_write_b128 v248, v[216:219] offset:17408\n\t" \
  "buffer_load_dword v252, v249, %[rs], %[sv] offen offset:0 sc1\n\t" \
  "buffer_load_dword v253, v249, %[rs], %[sv] offen offset:256 sc1\n\t" \
  "ds_read_b32 v212, v248 offset:28672\n\t" \
  "ds_read_b64_tr_b16 v[240:241], v250 offset:1024\n\t" \
  "ds_read_b64_tr_b16 v[242:243], v250 offset:1088\n\t" \
  "ds_read_b64_tr_b16 v[244:245], v250 offset:1536\n\t" \
  "ds_read_b64_tr_b16 v[246:247], v250 offset:1600\n\t" \
  "ds_read_b64_tr_b16 v[232:233], v250 offset:0\n\t" \
  "ds_read_b64_tr_b16 v[234:235], v250 offset:64\n\t" \
  "s_setprio 3\n\t" \
  "s_waitcnt lgkmcnt(0)\n\t" \
  "v_cmp_ne_u32 vcc, %[step], v212\n\t" \
  "s_barrier\n\t" \
  "s_and_b32 s93, vcc_lo, 0xf\n\t" \
  "s_cbranch_scc1 .Lre%=\n\t" \
  ".Lrb%=:\n\t" \
  "ds_read_b128 v[220:223], v248 offset:18432\n\t" \
  "ds_read_b128 v[224:227], v248 offset:19456\n\t" \
  "ds_read_b128 v[228:231], v248 offset:16384\n\t" \
  "ds_read_b64_tr_b16 v[204:205], v250 offset:2560\n\t" \
  "ds_read_b64_tr_b16 v[206:207], v250 offset:2624\n\t" \
  "ds_read_b64_tr_b16 v[208:209], v250 offset:3072\n\t" \
  "ds_read_b64_tr_b16 v[210:211], v250 offset:3136\n\t" \
  "ds_read_b64_tr_b16 v[212:213], v250 offset:3584\n\t" \
  "ds_read_b64_tr_b16 v[214:215], v250 offset:3648\n\t" \
  "ds_read_b64_tr_b16 v[200:201], v250 offset:2048\n\t" \
  "ds_read_b64_tr_b16 v[202:203], v250 offset:2112\n\t" \
  "s_waitcnt lgkmcnt(10)\n\t" \
  "v_pk_add_f32 v[216:217], v[216:217], v[220:221]\n\t" \
  "v_pk_add_f32 v[218:219], v[218:219], v[222:223]\n\t" \
  "s_waitcnt lgkmcnt(8)\n\t" \
  "v_pk_add_f32 v[224:225], v[224:225], v[228:229]\n\t" \
  "v_pk_add_f32 v[226:227], v[226:227], v[230:231]\n\t" \
  "v_pk_add_f32 v[216:217], v[216:217], v[224:225]\n\t" \
  "v_pk_add_f32 v[218:219], v[218:219], v[226:227]\n\t" \
  "s_waitcnt lgkmcnt(6)\n\t" \
  "v_mfma_f32_16x16x32_bf16 v[204:207], v[204:207], v[236:239], 0\n\t" \
  "v_max3_f32 v224, v216, v217, v218\n\t" \
  "v_max_f32 v224, v224, v219\n\t" \
  "s_waitcnt lgkmcnt(4)\n\t" \
  "v_mfma_f32_16x16x32_bf16 v[204:207], v[208:211], v[240:243], v[204:207]\n\t" \
  "v_max_f32_dpp v225, v224, v224 row_ror:8 row_mask:0xf bank_mask:0xf\n\t" \
  "v_max_f32_dpp v226, v224, v224 row_ror:8 row_mask:0xf bank_mask:0xf\n\t" \
  "s_waitcnt lgkmcnt(2)\n\t" \
  "v_mfma_f32_16x16x32_bf16 v[204:207], v[212:215], v[244:247], v[204:207]\n\t" \
  "v_permlane32_swap_b32 v225, v226\n\t" \
  "s_waitcnt lgkmcnt(0)\n\t" \
  "v_mfma_f32_16x16x32_bf16 v[204:207], v[200:203], v[232:235], v[204:207]\n\t" \
  "v_max_f32 v231, v225, v226\n\t" \
  "v_mov_b32 v220, 0x3f803f80\n\t" \
  "v_mov_b32 v221, 0x3f803f80\n\t" \
  "v_mov_b64 v[222:223], v[220:221]\n\t" \
  "v_mov_b32 v237, 0\n\t" \
  "v_mov_b32 v238, 0\n\t" \
  "s_nop 1\n\t" \
  "v_max3_f32 v208, v204, v205, v231\n\t" \
  "v_max3_f32 v209, v208, v206, v207\n\t" \
  "v_max3_f32 v210, v208, v206, v207\n\t" \
  "v_cndmask_b32 v204, v204, v206, %[up]\n\t" \
  "v_cndmask_b32 v205, v205, v207, %[up]\n\t" \
  "v_permlane16_swap_b32 v209, v210\n\t" \
  "v_max_f32 v208, v209, v210\n\t" \
  "v_mul_f32 v208, %[negc], v208\n\t" \
  "v_fma_f32 v204, v204, %[c], v208\n\t" \
  "v_fma_f32 v205, v205, %[c], v208\n\t" \
  "v_exp_f32 v204, v204\n\t" \
  "v_fma_f32 v216, v216, %[c], v208\n\t" \
  "v_exp_f32 v205, v205\n\t" \
  "v_fma_f32 v217, v217, %[c], v208\n\t" \
  "v_exp_f32 v216, v216\n\t" \
  "v_fma_f32 v218, v218, %[c], v208\n\t" \
  "v_cvt_pk_bf16_f32 v236, v204, v205\n\t" \
  "v_cvt_pk_bf16_f32 v239, v204, v205\n\t" \
  "v_exp_f32 v217, v217\n\t" \
  "v_fma_f32 v219, v219, %[c], v208\n\t" \
  "v_mfma_f32_16x16x16_bf16 v[200:203], v[220:221], v[236:237], 0\n\t" \
  "v_exp_f32 v218, v218\n\t" \
  "s_waitcnt vmcnt(0)\n\t" \
  "v_mfma_f32_16x16x16_bf16 v[224:227], v[252:253], v[236:237], 0\n\t" \
  "v_cvt_pk_bf16_f32 v214, v216, v217\n\t" \
  "v_cvt_pk_bf16_f32 v212, v216, v217\n\t" \
  "v_exp_f32 v219, v219\n\t" \
  "v_mfma_f32_16x16x16_bf16 v[228:231], v[252:253], v[238:239], 0\n\t" \
  "v_cvt_pk_bf16_f32 v215, v218, v219\n\t" \
  "v_cvt_pk_bf16_f32 v213, v218, v219\n\t" \
  "v_mov_b32_dpp v212, v214 row_ror:8 row_mask:0xf bank_mask:0xc\n\t" \
  "v_mov_b32_dpp v214, v214 row_ror:8 row_mask:0xf bank_mask:0x3\n\t" \
  "v_mov_b32_dpp v213, v215 row_ror:8 row_mask:0xf bank_mask:0xc\n\t" \
  "v_mov_b32_dpp v215, v215 row_ror:8 row_mask:0xf bank_mask:0x3\n\t" \
  "v_max_u32 v208, v252, v253\n\t" \
  "v_cmp_le_u32 vcc, %[sent], v208\n\t" \
  "v_mfma_f32_16x16x32_bf16 v[200:203], v[220:223], v[212:215], v[200:203]\n\t" \
  ".Lpv%=:\n\t" \
  "v_mfma_f32_16x16x32_bf16 v[224:227], v[192:195], v[212:215], v[224:227]\n\t" \
  "v_mfma_f32_16x16x32_bf16 v[228:231], v[196:199], v[212:215], v[228:231]\n\t" \
  "s_cbranch_vccnz .Lvs%=\n\t" \
  ".Lvok%=:\n\t" \
  "s_nop 4\n\t" \
  "v_rcp_f32 v208, v200\n\t" \
  "v_cndmask_b32 v224, v224, v226, %[nh]\n\t" \
  "v_cndmask_b32 v225, v225, v227, %[nh]\n\t" \
  "v_mul_f32 v224, v224, v208\n\t" \
  "v_mul_f32 v225, v225, v208\n\t" \
  "v_cvt_pk_bf16_f32 v224, v224, v225\n\t" \
  "buffer_store_dword v224, v251, %[rs], %[so] offen offset:0 sc1\n\t" \
  "v_cndmask_b32 v228, v228, v230, %[nh]\n\t" \
  "v_cndmask_b32 v229, v229, v231, %[nh]\n\t" \
  "v_mul_f32 v228, v228, v208\n\t" \
  "v_mul_f32 v229, v229, v208\n\t" \
  "v_cvt_pk_bf16_f32 v228, v228, v229\n\t" \
  "buffer_store_dword v228, v251, %[rs], %[so] offen offset:256 sc1\n\t" \
  "s_memtime s[90:91]\n\t" \
  "s_setprio 0\n\t" \
  "s_waitcnt lgkmcnt(0)\n\t" \
  "s_mov_b32 %[tout], s90\n\t" \
  "s_mov_b32 %[spins], s92\n\t" \
  "s_branch .Lend%=\n\t" \
  ".Lre%=:\n\t" \
  "ds_read_b64_tr_b16 v[240:241], v250 offset:1024\n\t" \
  "ds_read_b64_tr_b16 v[242:243], v250 offset:1088\n\t" \
  "ds_read_b64_tr_b16 v[244:245], v250 offset:1536\n\t" \
  "ds_read_b64_tr_b16 v[246:247], v250 offset:1600\n\t" \
  "ds_read_b64_tr_b16 v[232:233], v250 offset:0\n\t" \
  "ds_read_b64_tr_b16 v[234:235], v250 offset:64\n\t" \
  "s_waitcnt lgkmcnt(0)\n\t" \
  "s_branch .Lrb%=\n\t" \
  ".Lvs%=:\n\t" \
  "v_cmp_eq_u32 vcc, %[sent], v252\n\t" \
  "v_cmp_eq_u32 s[94:95], %[sent], v253\n\t" \
  "s_or_b64 s[94:95], s[94:95], vcc\n\t" \
  "s_cbranch_scc0 .Lvok%=\n\t" \
  "s_add_u32 s92, s92, 1\n\t" \
  "s_sleep 2\n\t" \
  "s_and_b32 s93, s92, 63\n\t" \
  "s_cbranch_scc1 .Lvr%=\n\t" \
  "buffer_load_dword v208, off, %[rs], %[sab] sc1\n\t" \
  "s_waitcnt vmcnt(0)\n\t" \
  "v_readfirstlane_b32 s93, v208\n\t" \
  "s_nop 3\n\t" \
  "s_cmp_eq_u32 s93, 1\n\t" \
  "s_cbranch_scc1 .Lvok%=\n\t" \
  "s_cmp_lt_u32 s92, 0x80000\n\t" \
  "s_cbranch_scc1 .Lvr%=\n\t" \
  "v_mov_b32 v208, 1\n\t" \
  "buffer_store_dword v208, off, %[rs], %[sab] sc1\n\t" \
  "s_branch .Lvok%=\n\t" \
  ".Lvr%=:\n\t" \
  "buffer_load_dword v252, v249, %[rs], %[sv] offen offset:0 sc1\n\t" \
  "buffer_load_dword v253, v249, %[rs], %[sv] offen offset:256 sc1\n\t" \
  "s_waitcnt vmcnt(0)\n\t" \
  "v_mfma_f32_16x16x16_bf16 v[224:227], v[252:253], v[236:237], 0\n\t" \
  "v_mfma_f32_16x16x16_bf16 v[228:231], v[252:253], v[238:239], 0\n\t" \
  "v_max_u32 v208, v252, v253\n\t" \
  "v_cmp_le_u32 vcc, %[sent], v208\n\t" \
  "s_nop 4\n\t" \
  "s_branch .Lpv%=\n\t" \
  "s_branch .Lend%=\n\t" \
  ".Lslow%=:\n\t" \
  "s_mov_b64 s[94:95], 0\n\t" \
  "v_cmp_eq_u32 vcc, %[sent], v200\n\t" \
  "s_or_b64 s[94:95], s[94:95], vcc\n\t" \
  "v_cmp_eq_u32 vcc, %[sent], v201\n\t" \
  "s_or_b64 s[94:95], s[94:95], vcc\n\t" \
  "v_cmp_eq_u32 vcc, %[sent], v202\n\t" \
  "s_or_b64 s[94:95], s[94:95], vcc\n\t" \
  "v_cmp_eq_u32 vcc, %[sent], v203\n\t" \
  "s_or_b64 s[94:95], s[94:95], vcc\n\t" \
  "v_cmp_eq_u32 vcc, %[sent], v204\n\t" \
  "s_or_b64 s[94:95], s[94:95], vcc\n\t" \
  "v_cmp_eq_u32 vcc, %[sent], v205\n\t" \
  "s_or_b64 s[94:95], s[94:95], vcc\n\t" \
  "v_cmp_eq_u32 vcc, %[sent], v206\n\t" \
  "s_or_b64 s[94:95], s[94:95], vcc\n\t" \
  "v_cmp_eq_u32 vcc, %[sent], v207\n\t" \
  "s_or_b64 s[94:95], s[94:95], vcc\n\t" \
  "s_cmp_eq_u64 s[94:95], 0\n\t" \
  "s_cbranch_scc1 .Lok%=\n\t" \
  "v_mov_b64 v[192:193], 0\n\t" \
  "v_mov_b64 v[194:195], 0\n\t" \
  "v_mov_b64 v[196:197], 0\n\t" \
  "v_mov_b64 v[198:199], 0\n\t" \
  "s_add_u32 s92, s92, 1\n\t" \
  "s_sleep 6\n\t" \
  "s_cmp_eq_u32 s92, 1\n\t" \
  "s_cbranch_scc1 .Lpoll%=\n\t" \
  "s_sleep 1\n\t" \
  "s_and_b32 s93, s92, 63\n\t" \
  "s_cmp_lg_u32 s93, 0\n\t" \
  "s_cbranch_scc1 .Lpoll%=\n\t" \
  "v_mov_b32 v245, 0\n\t" \
  "buffer_load_dword v244, v245, %[rs], %[sab] offen sc1\n\t" \
  "s_waitcnt vmcnt(0)\n\t" \
  "v_readfirstlane_b32 s93, v244\n\t" \
  "s_nop 3\n\t" \
  "s_cmp_eq_u32 s93, 1\n\t" \
  "s_cbranch_scc1 .Lok%=\n\t" \
  "s_cmp_lt_u32 s92, 0x80000\n\t" \
  "s_cbranch_scc1 .Lpoll%=\n\t" \
  "v_mov_b32 v244, 1\n\t" \
  "buffer_store_dword v244, v245, %[rs], %[sab] offen sc1\n\t" \
  "s_branch .Lok%=\n\t" \
  ".Lend%=:\n\t"
#define QK_ASM_2 \
  "s_waitcnt lgkmcnt(0)\n\t" \
  "v_subrev_u32 v244, 0x8000, v250\n\t" \
  "ds_read_b128 v[216:219], v248 offset:40960\n\t" \
  "ds_read_b128 v[220:223], v248 offset:41984\n\t" \
  "ds_read_b128 v[224:227], v248 offset:43008\n\t" \
  "ds_read_b128 v[228:231], v248 offset:44032\n\t" \
  "ds_read_b128 v[208:211], v244 offset:0\n\t" \
  "ds_read_b128 v[212:215], v244 offset:1024\n\t" \
  "ds_read_b128 v[236:239], v244 offset:2048\n\t" \
  "ds_read_b128 v[240:243], v244 offset:3072\n\t" \
  "v_mov_b64 v[192:193], 0\n\t" \
  "v_mov_b64 v[194:195], 0\n\t" \
  "v_mov_b64 v[196:197], 0\n\t" \
  "v_mov_b64 v[198:199], 0\n\t" \
  "s_movk_i32 s92, 0x2000\n\t" \
  ".Lwt%=:\n\t" \
  "ds_read_b64 v[200:201], v248 offset:30720\n\t" \
  "s_memrealtime s[90:91]\n\t" \
  "s_sub_u32 s92, s92, 1\n\t" \
  "s_waitcnt lgkmcnt(0)\n\t" \
  "v_readfirstlane_b32 s93, v200\n\t" \
  "v_readfirstlane_b32 %[tgt], v201\n\t" \
  "s_cbranch_scc1 .Lgo%=\n\t" \
  "s_cmp_eq_u32 s93, %[step]\n\t" \
  "s_cbranch_scc1 .Lwx%=\n\t" \
  "s_sleep 1\n\t" \
  "s_branch .Lwt%=\n\t" \
  ".Lwx%=:\n\t" \
  "s_lshl_b32 s93, s90, 2\n\t" \
  "s_sub_i32 s93, %[tgt], s93\n\t" \
  "s_cmp_le_i32 s93, 0\n\t" \
  "s_cbranch_scc1 .Lgo%=\n\t" \
  "s_min_i32 s93, s93, 8000\n\t" \
  "s_add_u32 s93, s93, 3\n\t" \
  "s_lshr_b32 s93, s93, 2\n\t" \
  "s_add_u32 s93, s93, s90\n\t" \
  "s_memrealtime s[90:91]\n\t" \
  ".Lpace%=:\n\t" \
  "s_memrealtime s[94:95]\n\t" \
  "s_waitcnt lgkmcnt(1)\n\t" \
  "s_sub_i32 s91, s90, s93\n\t" \
  "s_cmp_ge_i32 s91, 0\n\t" \
  "s_cbranch_scc1 .Lgo%=\n\t" \
  "s_memrealtime s[90:91]\n\t" \
  "s_waitcnt lgkmcnt(1)\n\t" \
  "s_sub_i32 s95, s94, s93\n\t" \
  "s_cmp_lt_i32 s95, 0\n\t" \
  "s_cbranch_scc1 .Lpace%=\n\t" \
  ".Lgo%=:\n\t" \
  "s_mov_b32 s92, 0\n\t" \
  ".Lpoll%=:\n\t" \
  "buffer_load_dwordx4 v[200:203], v248, %[rs], %[som] offen sc1\n\t" \
  "buffer_load_dwordx4 v[204:207], v248, %[rs], %[som2] offen sc1\n\t" \
  "s_waitcnt vmcnt(1) lgkmcnt(0)\n\t" \
  "ds_write_b128 v249, v[200:203]\n\t" \
  "v_smfmac_f32_16x16x64_bf16 v[192:195], v[200:203], v[208:215], v251\n\t" \
  "v_max3_u32 v244, v200, v201, v202\n\t" \
  "v_smfmac_f32_16x16x64_bf16 v[196:199], v[200:203], v[236:243], v251\n\t" \
  "s_waitcnt vmcnt(0)\n\t" \
  "ds_write_b128 v249, v[204:207] offset:8192\n\t" \
  "v_smfmac_f32_16x16x64_bf16 v[192:195], v[204:207], v[216:223], v251\n\t" \
  "v_max3_u32 v244, v244, v203, v204\n\t" \
  "v_max3_u32 v244, v244, v205, v206\n\t" \
  "v_smfmac_f32_16x16x64_bf16 v[196:199], v[204:207], v[224:231], v251\n\t" \
  "v_max_u32 v244, v244, v207\n\t" \
  "v_cmp_le_u32 vcc, %[sent], v244\n\t" \
  "s_cbranch_vccnz .Lslow%=\n\t" \
  ".Lok%=:\n\t" \
  "ds_read_b128 v[216:219], v250 offset:32768\n\t" \
  "ds_read_b128 v[220:223], v250 offset:33792\n\t" \
  "ds_read_b128 v[224:227], v250 offset:34816\n\t" \
  "ds_read_b128 v[228:231], v250 offset:35840\n\t" \
  "s_waitcnt lgkmcnt(4)\n\t" \
  "s_barrier\n\t" \
  "ds_read_b128 v[208:211], v248 offset:3072\n\t" \
  "ds_read_b128 v[212:215], v248 offset:4096\n\t" \
  "ds_read_b128 v[236:239], v248 offset:5120\n\t" \
  "ds_read_b128 v[240:243], v248 offset:6144\n\t" \
  "s_waitcnt lgkmcnt(3)\n\t" \
  "ds_read_b128 v[244:247], v248 offset:13312\n\t" \
  "v_smfmac_f32_16x16x64_bf16 v[192:195], v[208:211], v[216:223], v251\n\t" \
  "ds_read_b128 v[216:219], v250 offset:0\n\t" \
  "v_smfmac_f32_16x16x64_bf16 v[196:199], v[208:211], v[224:231], v251\n\t" \
  "s_waitcnt lgkmcnt(4)\n\t" \
  "ds_read_b128 v[220:223], v250 offset:1024\n\t" \
  "v_smfmac_f32_16x16x64_bf16 v[192:195], v[212:215], v[0:7], v251\n\t" \
  "ds_read_b128 v[224:227], v250 offset:2048\n\t" \
  "v_smfmac_f32_16x16x64_bf16 v[196:199], v[212:215], v[8:15], v251\n\t" \
  "s_waitcnt lgkmcnt(5)\n\t" \
  "ds_read_b128 v[228:231], v250 offset:3072\n\t" \
  "v_smfmac_f32_16x16x64_bf16 v[192:195], v[236:239], v[16:23], v251\n\t" \
  "ds_read_b128 v[200:203], v248 offset:7168\n\t" \
  "v_smfmac_f32_16x16x64_bf16 v[196:199], v[236:239], v[24:31], v251\n\t" \
  "s_waitcnt lgkmcnt(6)\n\t" \
  "ds_read_b128 v[204:207], v248 offset:8192\n\t" \
  "v_smfmac_f32_16x16x64_bf16 v[192:195], v[240:243], v[32:39], v251\n\t" \
  "ds_read_b128 v[208:211], v248 offset:9216\n\t" \
  "v_smfmac_f32_16x16x64_bf16 v[196:199], v[240:243], v[40:47], v251\n\t" \
  "s_waitcnt lgkmcnt(5)\n\t" \
  "ds_read_b128 v[212:215], v248 offset:11264\n\t" \
  "v_smfmac_f32_16x16x64_bf16 v[192:195], v[244:247], v[216:223], v251\n\t" \
  "s_waitcnt lgkmcnt(4)\n\t" \
  "v_smfmac_f32_16x16x64_bf16 v[196:199], v[244:247], v[224:231], v251\n\t" \
  "s_waitcnt lgkmcnt(3)\n\t" \
  "ds_read_b128 v[236:239], v248 offset:12288\n\t" \
  "v_smfmac_f32_16x16x64_bf16 v[192:195], v[200:203], v[48:55], v251\n\t" \
  "ds_read_b128 v[240:243], v248 offset:14336\n\t" \
  "v_smfmac_f32_16x16x64_bf16 v[196:199], v[200:203], v[56:63], v251\n\t" \
  "s_waitcnt lgkmcnt(4)\n\t" \
  "v_smfmac_f32_16x16x64_bf16 v[192:195], v[204:207], v[64:71], v251\n\t" \
  "ds_read_b128 v[244:247], v248 offset:15360\n\t" \
  "v_smfmac_f32_16x16x64_bf16 v[196:199], v[204:207], v[72:79], v251\n\t" \
  "s_waitcnt lgkmcnt(4)\n\t" \
  "v_smfmac_f32_16x16x64_bf16 v[192:195], v[208:211], v[80:87], v251\n\t" \
  "ds_read_b128 v[200:203], v248 offset:0\n\t" \
  "v_smfmac_f32_16x16x64_bf16 v[196:199], v[208:211], v[88:95], v251\n\t" \
  "s_waitcnt lgkmcnt(4)\n\t" \
  "v_smfmac_f32_16x16x64_bf16 v[192:195], v[212:215], v[96:103], v251\n\t" \
  "ds_read_b128 v[204:207], v248 offset:1024\n\t" \
  "v_smfmac_f32_16x16x64_bf16 v[196:199], v[212:215], v[104:111], v251\n\t" \
  "s_waitcnt lgkmcnt(3)\n\t" \
  "v_smfmac_f32_16x16x64_bf16 v[192:195], v[236:239], v[112:119], v251\n\t" \
  "v_smfmac_f32_16x16x64_bf16 v[196:199], v[236:239], v[120:127], v251\n\t" \
  "v_smfmac_f32_16x16x64_bf16 v[192:195], v[240:243], v[128:135], v251\n\t" \
  "v_smfmac_f32_16x16x64_bf16 v[196:199], v[240:243], v[136:143], v251\n\t" \
  "s_waitcnt lgkmcnt(1)\n\t" \
  "v_smfmac_f32_16x16x64_bf16 v[192:195], v[244:247], v[144:151], v251\n\t" \
  "v_smfmac_f32_16x16x64_bf16 v[196:199], v[244:247], v[152:159], v251\n\t" \
  "v_smfmac_f32_16x16x64_bf16 v[192:195], v[200:203], v[160:167], v251\n\t" \
  "v_smfmac_f32_16x16x64_bf16 v[196:199], v[200:203], v[168:175], v251\n\t" \
  "s_waitcnt lgkmcnt(0)\n\t" \
  "v_smfmac_f32_16x16x64_bf16 v[192:195], v[204:207], v[176:183], v251\n\t" \
  "v_smfmac_f32_16x16x64_bf16 v[196:199], v[204:207], v[184:191], v251\n\t" \
  "ds_read_b128 v[200:203], v248 offset:18432\n\t" \
  "ds_read_b128 v[204:207], v248 offset:22528\n\t" \
  "v_mov_b32 v250, v232\n\t" \
  "v_mov_b32 v249, v233\n\t" \
  "s_nop 2\n\t" \
  "v_add_f32 v192, v192, v194\n\t" \
  "v_add_f32 v193, v193, v195\n\t" \
  "v_add_f32 v196, v196, v198\n\t" \
  "v_add_f32 v197, v197, v199\n\t" \
  "v_cvt_pk_bf16_f32 v192, v192, v193\n\t" \
  "v_cvt_pk_bf16_f32 v196, v196, v197\n\t" \
  "ds_write2_b32 v252, v192, v196 offset1:64\n\t" \
  "v_mov_b32 v251, v234\n\t" \
  "ds_read_b64_tr_b16 v[240:241], v250 offset:1024\n\t" \
  "ds_read_b64_tr_b16 v[242:243], v250 offset:1088\n\t" \
  "v_mov_b32 v212, %[step]\n\t" \
  "s_mov_b64 exec, 1\n\t" \
  "ds_write_b32 v248, v212 offset:28704\n\t" \
  "s_mov_b64 exec, -1\n\t" \
  "buffer_load_dwordx4 v[192:195], v248, %[rt], %[svt] offen offset:0 sc1\n\t" \
  "buffer_load_dwordx4 v[196:199], v248, %[rt], %[svt] offen offset:1024 sc1\n\t" \
  "s_waitcnt lgkmcnt(1)\n\t" \
  "v_mfma_f32_16x16x32_bf16 v[200:203], v[200:203], v[240:243], 0\n\t" \
  "v_mfma_f32_16x16x32_bf16 v[204:207], v[204:207], v[240:243], 0\n\t" \
  "s_nop 7\n\t" \
  "v_cndmask_b32 v216, v200, v204, %[nh]\n\t" \
  "v_cndmask_b32 v217, v201, v205, %[nh]\n\t" \
  "v_cndmask_b32 v218, v202, v206, %[nh]\n\t" \
  "v_cndmask_b32 v219, v203, v207, %[nh]\n\t" \
  "ds_write_b128 v248, v[216:219] offset:18432\n\t" \
  "buffer_load_dword v252, v249, %[rs], %[sv] offen offset:0 sc1\n\t" \
  "buffer_load_dword v253, v249, %[rs], %[sv] offen offset:256 sc1\n\t" \
  "ds_read_b32 v212, v248 offset:28672\n\t" \
  "ds_read_b64_tr_b16 v[244:245], v250 offset:1536\n\t" \
  "ds_read_b64_tr_b16 v[246:247], v250 offset:1600\n\t" \
  "ds_read_b64_tr_b16 v[232:233], v250 offset:0\n\t" \
  "ds_read_b64_tr_b16 v[234:235], v250 offset:64\n\t" \
  "ds_read_b64_tr_b16 v[236:237], v250 offset:512\n\t" \
  "ds_read_b64_tr_b16 v[238:239], v250 offset:576\n\t" \
  "s_setprio 3\n\t" \
  "s_waitcnt lgkmcnt(0)\n\t" \
  "v_cmp_ne_u32 vcc, %[step], v212\n\t" \
  "s_barrier\n\t" \
  "s_and_b32 s93, vcc_lo, 0xf\n\t" \
  "s_cbranch_scc1 .Lre%=\n\t" \
  ".Lrb%=:\n\t" \
  "ds_read_b128 v[220:223], v248 offset:19456\n\t" \
  "ds_read_b128 v[224:227], v248 offset:16384\n\t" \
  "ds_read_b128 v[228:231], v248 offset:17408\n\t" \
  "ds_read_b64_tr_b16 v[208:209], v250 offset:3072\n\t" \
  "ds_read_b64_tr_b16 v[210:211], v250 offset:3136\n\t" \
  "ds_read_b64_tr_b16 v[212:213], v250 offset:3584\n\t" \
  "ds_read_b64_tr_b16 v[214:215], v250 offset:3648\n\t" \
  "ds_read_b64_tr_b16 v[200:201], v250 offset:2048\n\t" \
  "ds_read_b64_tr_b16 v[202:203], v250 offset:2112\n\t" \
  "ds_read_b64_tr_b16 v[204:205], v250 offset:2560\n\t" \
  "ds_read_b64_tr_b16 v[206:207], v250 offset:2624\n\t" \
  "s_waitcnt lgkmcnt(10)\n\t" \
  "v_pk_add_f32 v[216:217], v[216:217], v[220:221]\n\t" \
  "v_pk_add_f32 v[218:219], v[218:219], v[222:223]\n\t" \
  "s_waitcnt lgkmcnt(8)\n\t" \
  "v_pk_add_f32 v[224:225], v[224:225], v[228:229]\n\t" \
  "v_pk_add_f32 v[226:227], v[226:227], v[230:231]\n\t" \
  "v_pk_add_f32 v[216:217], v[216:217], v[224:225]\n\t" \
  "v_pk_add_f32 v[218:219], v[218:219], v[226:227]\n\t" \
  "s_waitcnt lgkmcnt(6)\n\t" \
  "v_mfma_f32_16x16x32_bf16 v[208:211], v[208:211], v[240:243], 0\n\t" \
  "v_max3_f32 v224, v216, v217, v218\n\t" \
  "v_max_f32 v224, v224, v219\n\t" \
  "s_waitcnt lgkmcnt(4)\n\t" \
  "v_mfma_f32_16x16x32_bf16 v[208:211], v[212:215], v[244:247], v[208:211]\n\t" \
  "v_max_f32_dpp v225, v224, v224 row_ror:8 row_mask:0xf bank_mask:0xf\n\t" \
  "v_max_f32_dpp v226, v224, v224 row_ror:8 row_mask:0xf bank_mask:0xf\n\t" \
  "s_waitcnt lgkmcnt(2)\n\t" \
  "v_mfma_f32_16x16x32_bf16 v[208:211], v[200:203], v[232:235], v[208:211]\n\t" \
  "v_permlane32_swap_b32 v225, v226\n\t" \
  "s_waitcnt lgkmcnt(0)\n\t" \
  "v_mfma_f32_16x16x32_bf16 v[208:211], v[204:207], v[236:239], v[208:211]\n\t" \
  "v_max_f32 v231, v225, v226\n\t" \
  "v_mov_b32 v220, 0x3f803f80\n\t" \
  "v_mov_b32 v221, 0x3f803f80\n\t" \
  "v_mov_b64 v[222:223], v[220:221]\n\t" \
  "v_mov_b32 v241, 0\n\t" \
  "v_mov_b32 v242, 0\n\t" \
  "s_nop 1\n\t" \
  "v_max3_f32 v212, v208, v209, v231\n\t" \
  "v_max3_f32 v213, v212, v210, v211\n\t" \
  "v_max3_f32 v214, v212, v210, v211\n\t" \
  "v_cndmask_b32 v208, v208, v210, %[up]\n\t" \
  "v_cndmask_b32 v209, v209, v211, %[up]\n\t" \
  "v_permlane16_swap_b32 v213, v214\n\t" \
  "v_max_f32 v212, v213, v214\n\t" \
  "v_mul_f32 v212, %[negc], v212\n\t" \
  "v_fma_f32 v208, v208, %[c], v212\n\t" \
  "v_fma_f32 v209, v209, %[c], v212\n\t" \
  "v_exp_f32 v208, v208\n\t" \
  "v_fma_f32 v216, v216, %[c], v212\n\t" \
  "v_exp_f32 v209, v209\n\t" \
  "v_fma_f32 v217, v217, %[c], v212\n\t" \
  "v_exp_f32 v216, v216\n\t" \
  "v_fma_f32 v218, v218, %[c], v212\n\t" \
  "v_cvt_pk_bf16_f32 v240, v208, v209\n\t" \
  "v_cvt_pk_bf16_f32 v243, v208, v209\n\t" \
  "v_exp_f32 v217, v217\n\t" \
  "v_fma_f32 v219, v219, %[c], v212\n\t" \
  "v_mfma_f32_16x16x16_bf16 v[204:207], v[220:221], v[240:241], 0\n\t" \
  "v_exp_f32 v218, v218\n\t" \
  "s_waitcnt vmcnt(0)\n\t" \
  "v_mfma_f32_16x16x16_bf16 v[224:227], v[252:253], v[240:241], 0\n\t" \
  "v_cvt_pk_bf16_f32 v202, v216, v217\n\t" \
  "v_cvt_pk_bf16_f32 v200, v216, v217\n\t" \
  "v_exp_f32 v219, v219\n\t" \
  "v_mfma_f32_16x16x16_bf16 v[228:231], v[252:253], v[242:243], 0\n\t" \
  "v_cvt_pk_bf16_f32 v203, v218, v219\n\t" \
  "v_cvt_pk_bf16_f32 v201, v218, v219\n\t" \
  "v_mov_b32_dpp v200, v202 row_ror:8 row_mask:0xf bank_mask:0xc\n\t" \
  "v_mov_b32_dpp v202, v202 row_ror:8 row_mask:0xf bank_mask:0x3\n\t" \
  "v_mov_b32_dpp v201, v203 row_ror:8 row_mask:0xf bank_mask:0xc\n\t" \
  "v_mov_b32_dpp v203, v203 row_ror:8 row_mask:0xf bank_mask:0x3\n\t" \
  "v_max_u32 v212, v252, v253\n\t" \
  "v_cmp_le_u32 vcc, %[sent], v212\n\t" \
  "v_mfma_f32_16x16x32_bf16 v[204:207], v[220:223], v[200:203], v[204:207]\n\t" \
  ".Lpv%=:\n\t" \
  "v_mfma_f32_16x16x32_bf16 v[224:227], v[192:195], v[200:203], v[224:227]\n\t" \
  "v_mfma_f32_16x16x32_bf16 v[228:231], v[196:199], v[200:203], v[228:231]\n\t" \
  "s_cbranch_vccnz .Lvs%=\n\t" \
  ".Lvok%=:\n\t" \
  "s_nop 4\n\t" \
  "v_rcp_f32 v212, v204\n\t" \
  "v_cndmask_b32 v224, v224, v226, %[nh]\n\t" \
  "v_cndmask_b32 v225, v225, v227, %[nh]\n\t" \
  "v_mul_f32 v224, v224, v212\n\t" \
  "v_mul_f32 v225, v225, v212\n\t" \
  "v_cvt_pk_bf16_f32 v224, v224, v225\n\t" \
  "buffer_store_dword v224, v251, %[rs], %[so] offen offset:0 sc1\n\t" \
  "v_cndmask_b32 v228, v228, v230, %[nh]\n\t" \
  "v_cndmask_b32 v229, v229, v231, %[nh]\n\t" \
  "v_mul_f32 v228, v228, v212\n\t" \
  "v_mul_f32 v229, v229, v212\n\t" \
  "v_cvt_pk_bf16_f32 v228, v228, v229\n\t" \
  "buffer_store_dword v228, v251, %[rs], %[so] offen offset:256 sc1\n\t" \
  "s_memtime s[90:91]\n\t" \
  "s_setprio 0\n\t" \
  "s_waitcnt lgkmcnt(0)\n\t" \
  "s_mov_b32 %[tout], s90\n\t" \
  "s_mov_b32 %[spins], s92\n\t" \
  "s_branch .Lend%=\n\t" \
  ".Lre%=:\n\t" \
  "ds_read_b64_tr_b16 v[244:245], v250 offset:1536\n\t" \
  "ds_read_b64_tr_b16 v[246:247], v250 offset:1600\n\t" \
  "ds_read_b64_tr_b16 v[232:233], v250 offset:0\n\t" \
  "ds_read_b64_tr_b16 v[234:235], v250 offset:64\n\t" \
  "ds_read_b64_tr_b16 v[236:237], v250 offset:512\n\t" \
  "ds_read_b64_tr_b16 v[238:239], v250 offset:576\n\t" \
  "s_waitcnt lgkmcnt(0)\n\t" \
  "s_branch .Lrb%=\n\t" \
  ".Lvs%=:\n\t" \
  "v_cmp_eq_u32 vcc, %[sent], v252\n\t" \
  "v_cmp_eq_u32 s[94:95], %[sent], v253\n\t" \
  "s_or_b64 s[94:95], s[94:95], vcc\n\t" \
  "s_cbranch_scc0 .Lvok%=\n\t" \
  "s_add_u32 s92, s92, 1\n\t" \
  "s_sleep 2\n\t" \
  "s_and_b32 s93, s92, 63\n\t" \
  "s_cbranch_scc1 .Lvr%=\n\t" \
  "buffer_load_dword v212, off, %[rs], %[sab] sc1\n\t" \
  "s_waitcnt vmcnt(0)\n\t" \
  "v_readfirstlane_b32 s93, v212\n\t" \
  "s_nop 3\n\t" \
  "s_cmp_eq_u32 s93, 1\n\t" \
  "s_cbranch_scc1 .Lvok%=\n\t" \
  "s_cmp_lt_u32 s92, 0x80000\n\t" \
  "s_cbranch_scc1 .Lvr%=\n\t" \
  "v_mov_b32 v212, 1\n\t" \
  "buffer_store_dword v212, off, %[rs], %[sab] sc1\n\t" \
  "s_branch .Lvok%=\n\t" \
  ".Lvr%=:\n\t" \
  "buffer_load_dword v252, v249, %[rs], %[sv] offen offset:0 sc1\n\t" \
  "buffer_load_dword v253, v249, %[rs], %[sv] offen offset:256 sc1\n\t" \
  "s_waitcnt vmcnt(0)\n\t" \
  "v_mfma_f32_16x16x16_bf16 v[224:227], v[252:253], v[240:241], 0\n\t" \
  "v_mfma_f32_16x16x16_bf16 v[228:231], v[252:253], v[242:243], 0\n\t" \
  "v_max_u32 v212, v252, v253\n\t" \
  "v_cmp_le_u32 vcc, %[sent], v212\n\t" \
  "s_nop 4\n\t" \
  "s_branch .Lpv%=\n\t" \
  "s_branch .Lend%=\n\t" \
  ".Lslow%=:\n\t" \
  "s_mov_b64 s[94:95], 0\n\t" \
  "v_cmp_eq_u32 vcc, %[sent], v200\n\t" \
  "s_or_b64 s[94:95], s[94:95], vcc\n\t" \
  "v_cmp_eq_u32 vcc, %[sent], v201\n\t" \
  "s_or_b64 s[94:95], s[94:95], vcc\n\t" \
  "v_cmp_eq_u32 vcc, %[sent], v202\n\t" \
  "s_or_b64 s[94:95], s[94:95], vcc\n\t" \
  "v_cmp_eq_u32 vcc, %[sent], v203\n\t" \
  "s_or_b64 s[94:95], s[94:95], vcc\n\t" \
  "v_cmp_eq_u32 vcc, %[sent], v204\n\t" \
  "s_or_b64 s[94:95], s[94:95], vcc\n\t" \
  "v_cmp_eq_u32 vcc, %[sent], v205\n\t" \
  "s_or_b64 s[94:95], s[94:95], vcc\n\t" \
  "v_cmp_eq_u32 vcc, %[sent], v206\n\t" \
  "s_or_b64 s[94:95], s[94:95], vcc\n\t" \
  "v_cmp_eq_u32 vcc, %[sent], v207\n\t" \
  "s_or_b64 s[94:95], s[94:95], vcc\n\t" \
  "s_cmp_eq_u64 s[94:95], 0\n\t" \
  "s_cbranch_scc1 .Lok%=\n\t" \
  "v_mov_b64 v[192:193], 0\n\t" \
  "v_mov_b64 v[194:195], 0\n\t" \
  "v_mov_b64 v[196:197], 0\n\t" \
  "v_mov_b64 v[198:199], 0\n\t" \
  "s_add_u32 s92, s92, 1\n\t" \
  "s_sleep 6\n\t" \
  "s_cmp_eq_u32 s92, 1\n\t" \
  "s_cbranch_scc1 .Lpoll%=\n\t" \
  "s_sleep 1\n\t" \
  "s_and_b32 s93, s92, 63\n\t" \
  "s_cmp_lg_u32 s93, 0\n\t" \
  "s_cbranch_scc1 .Lpoll%=\n\t" \
  "v_mov_b32 v245, 0\n\t" \
  "buffer_load_dword v244, v245, %[rs], %[sab] offen sc1\n\t" \
  "s_waitcnt vmcnt(0)\n\t" \
  "v_readfirstlane_b32 s93, v244\n\t" \
  "s_nop 3\n\t" \
  "s_cmp_eq_u32 s93, 1\n\t" \
  "s_cbranch_scc1 .Lok%=\n\t" \
  "s_cmp_lt_u32 s92, 0x80000\n\t" \
  "s_cbranch_scc1 .Lpoll%=\n\t" \
  "v_mov_b32 v244, 1\n\t" \
  "buffer_store_dword v244, v245, %[rs], %[sab] offen sc1\n\t" \
  "s_branch .Lok%=\n\t" \
  ".Lend%=:\n\t"
#define QK_ASM_3 \
  "s_waitcnt lgkmcnt(0)\n\t" \
  "v_subrev_u32 v244, 0x8000, v250\n\t" \
  "ds_read_b128 v[216:219], v248 offset:45056\n\t" \
  "ds_read_b128 v[220:223], v248 offset:46080\n\t" \
  "ds_read_b128 v[224:227], v248 offset:47104\n\t" \
  "ds_read_b128 v[228:231], v248 offset:48128\n\t" \
  "ds_read_b128 v[208:211], v244 offset:0\n\t" \
  "ds_read_b128 v[212:215], v244 offset:1024\n\t" \
  "ds_read_b128 v[236:239], v244 offset:2048\n\t" \
  "ds_read_b128 v[240:243], v244 offset:3072\n\t" \
  "v_mov_b64 v[192:193], 0\n\t" \
  "v_mov_b64 v[194:195], 0\n\t" \
  "v_mov_b64 v[196:197], 0\n\t" \
  "v_mov_b64 v[198:199], 0\n\t" \
  "s_movk_i32 s92, 0x2000\n\t" \
  ".Lwt%=:\n\t" \
  "ds_read_b64 v[200:201], v248 offset:30720\n\t" \
  "s_memrealtime s[90:91]\n\t" \
  "s_sub_u32 s92, s92, 1\n\t" \
  "s_waitcnt lgkmcnt(0)\n\t" \
  "v_readfirstlane_b32 s93, v200\n\t" \
  "v_readfirstlane_b32 %[tgt], v201\n\t" \
  "s_cbranch_scc1 .Lgo%=\n\t" \
  "s_cmp_eq_u32 s93, %[step]\n\t" \
  "s_cbranch_scc1 .Lwx%=\n\t" \
  "s_sleep 1\n\t" \
  "s_branch .Lwt%=\n\t" \
  ".Lwx%=:\n\t" \
  "s_lshl_b32 s93, s90, 2\n\t" \
  "s_sub_i32 s93, %[tgt], s93\n\t" \
  "s_cmp_le_i32 s93, 0\n\t" \
  "s_cbranch_scc1 .Lgo%=\n\t" \
  "s_min_i32 s93, s93, 8000\n\t" \
  "s_add_u32 s93, s93, 3\n\t" \
  "s_lshr_b32 s93, s93, 2\n\t" \
  "s_add_u32 s93, s93, s90\n\t" \
  "s_memrealtime s[90:91]\n\t" \
  ".Lpace%=:\n\t" \
  "s_memrealtime s[94:95]\n\t" \
  "s_waitcnt lgkmcnt(1)\n\t" \
  "s_sub_i32 s91, s90, s93\n\t" \
  "s_cmp_ge_i32 s91, 0\n\t" \
  "s_cbranch_scc1 .Lgo%=\n\t" \
  "s_memrealtime s[90:91]\n\t" \
  "s_waitcnt lgkmcnt(1)\n\t" \
  "s_sub_i32 s95, s94, s93\n\t" \
  "s_cmp_lt_i32 s95, 0\n\t" \
  "s_cbranch_scc1 .Lpace%=\n\t" \
  ".Lgo%=:\n\t" \
  "s_mov_b32 s92, 0\n\t" \
  ".Lpoll%=:\n\t" \
  "buffer_load_dwordx4 v[200:203], v248, %[rs], %[som] offen sc1\n\t" \
  "buffer_load_dwordx4 v[204:207], v248, %[rs], %[som2] offen sc1\n\t" \
  "s_waitcnt vmcnt(1) lgkmcnt(0)\n\t" \
  "ds_write_b128 v249, v[200:203]\n\t" \
  "v_smfmac_f32_16x16x64_bf16 v[192:195], v[200:203], v[208:215], v251\n\t" \
  "v_max3_u32 v244, v200, v201, v202\n\t" \
  "v_smfmac_f32_16x16x64_bf16 v[196:199], v[200:203], v[236:243], v251\n\t" \
  "s_waitcnt vmcnt(0)\n\t" \
  "ds_write_b128 v249, v[204:207] offset:8192\n\t" \
  "v_smfmac_f32_16x16x64_bf16 v[192:195], v[204:207], v[216:223], v251\n\t" \
  "v_max3_u32 v244, v244, v203, v204\n\t" \
  "v_max3_u32 v244, v244, v205, v206\n\t" \
  "v_smfmac_f32_16x16x64_bf16 v[196:199], v[204:207], v[224:231], v251\n\t" \
  "v_max_u32 v244, v244, v207\n\t" \
  "v_cmp_le_u32 vcc, %[sent], v244\n\t" \
  "s_cbranch_vccnz .Lslow%=\n\t" \
  ".Lok%=:\n\t" \
  "ds_read_b128 v[216:219], v250 offset:32768\n\t" \
  "ds_read_b128 v[220:223], v250 offset:33792\n\t" \
  "ds_read_b128 v[224:227], v250 offset:34816\n\t" \
  "ds_read_b128 v[228:231], v250 offset:35840\n\t" \
  "s_waitcnt lgkmcnt(4)\n\t" \
  "s_barrier\n\t" \
  "ds_read_b128 v[208:211], v248 offset:2048\n\t" \
  "ds_read_b128 v[212:215], v248 offset:5120\n\t" \
  "ds_read_b128 v[236:239], v248 offset:6144\n\t" \
  "ds_read_b128 v[240:243], v248 offset:7168\n\t" \
  "s_waitcnt lgkmcnt(3)\n\t" \
  "ds_read_b128 v[244:247], v248 offset:12288\n\t" \
  "v_smfmac_f32_16x16x64_bf16 v[192:195], v[208:211], v[216:223], v251\n\t" \
  "ds_read_b128 v[216:219], v250 offset:0\n\t" \
  "v_smfmac_f32_16x16x64_bf16 v[196:199], v[208:211], v[224:231], v251\n\t" \
  "s_waitcnt lgkmcnt(4)\n\t" \
  "ds_read_b128 v[220:223], v250 offset:1024\n\t" \
  "v_smfmac_f32_16x16x64_bf16 v[192:195], v[212:215], v[0:7], v251\n\t" \
  "ds_read_b128 v[224:227], v250 offset:2048\n\t" \
  "v_smfmac_f32_16x16x64_bf16 v[196:199], v[212:215], v[8:15], v251\n\t" \
  "s_waitcnt lgkmcnt(5)\n\t" \
  "ds_read_b128 v[228:231], v250 offset:3072\n\t" \
  "v_smfmac_f32_16x16x64_bf16 v[192:195], v[236:239], v[16:23], v251\n\t" \
  "ds_read_b128 v[200:203], v248 offset:8192\n\t" \
  "v_smfmac_f32_16x16x64_bf16 v[196:199], v[236:239], v[24:31], v251\n\t" \
  "s_waitcnt lgkmcnt(6)\n\t" \
  "ds_read_b128 v[204:207], v248 offset:9216\n\t" \
  "v_smfmac_f32_16x16x64_bf16 v[192:195], v[240:243], v[32:39], v251\n\t" \
  "ds_read_b128 v[208:211], v248 offset:10240\n\t" \
  "v_smfmac_f32_16x16x64_bf16 v[196:199], v[240:243], v[40:47], v251\n\t" \
  "s_waitcnt lgkmcnt(5)\n\t" \
  "ds_read_b128 v[212:215], v248 offset:13312\n\t" \
  "v_smfmac_f32_16x16x64_bf16 v[192:195], v[244:247], v[216:223], v251\n\t" \
  "s_waitcnt lgkmcnt(4)\n\t" \
  "v_smfmac_f32_16x16x64_bf16 v[196:199], v[244:247], v[224:231], v251\n\t" \
  "s_waitcnt lgkmcnt(3)\n\t" \
  "ds_read_b128 v[236:239], v248 offset:14336\n\t" \
  "v_smfmac_f32_16x16x64_bf16 v[192:195], v[200:203], v[48:55], v251\n\t" \
  "ds_read_b128 v[240:243], v248 offset:15360\n\t" \
  "v_smfmac_f32_16x16x64_bf16 v[196:199], v[200:203], v[56:63], v251\n\t" \
  "s_waitcnt lgkmcnt(4)\n\t" \
  "v_smfmac_f32_16x16x64_bf16 v[192:195], v[204:207], v[64:71], v251\n\t" \
  "ds_read_b128 v[244:247], v248 offset:0\n\t" \
  "v_smfmac_f32_16x16x64_bf16 v[196:199], v[204:207], v[72:79], v251\n\t" \
  "s_waitcnt lgkmcnt(4)\n\t" \
  "v_smfmac_f32_16x16x64_bf16 v[192:195], v[208:211], v[80:87], v251\n\t" \
  "ds_read_b128 v[200:203], v248 offset:1024\n\t" \
  "v_smfmac_f32_16x16x64_bf16 v[196:199], v[208:211], v[88:95], v251\n\t" \
  "s_waitcnt lgkmcnt(4)\n\t" \
  "v_smfmac_f32_16x16x64_bf16 v[192:195], v[212:215], v[96:103], v251\n\t" \
  "ds_read_b128 v[204:207], v248 offset:4096\n\t" \
  "v_smfmac_f32_16x16x64_bf16 v[196:199], v[212:215], v[104:111], v251\n\t" \
  "s_waitcnt lgkmcnt(3)\n\t" \
  "v_smfmac_f32_16x16x64_bf16 v[192:195], v[236:239], v[112:119], v251\n\t" \
  "v_smfmac_f32_16x16x64_bf16 v[196:199], v[236:239], v[120:127], v251\n\t" \
  "v_smfmac_f32_16x16x64_bf16 v[192:195], v[240:243], v[128:135], v251\n\t" \
  "v_smfmac_f32_16x16x64_bf16 v[196:199], v[240:243], v[136:143], v251\n\t" \
  "s_waitcnt lgkmcnt(1)\n\t" \
  "v_smfmac_f32_16x16x64_bf16 v[192:195], v[244:247], v[144:151], v251\n\t" \
  "v_smfmac_f32_16x16x64_bf16 v[196:199], v[244:247], v[152:159], v251\n\t" \
  "v_smfmac_f32_16x16x64_bf16 v[192:195], v[200:203], v[160:167], v251\n\t" \
  "v_smfmac_f32_16x16x64_bf16 v[196:199], v[200:203], v[168:175], v251\n\t" \
  "s_waitcnt lgkmcnt(0)\n\t" \
  "v_smfmac_f32_16x16x64_bf16 v[192:195], v[204:207], v[176:183], v251\n\t" \
  "v_smfmac_f32_16x16x64_bf16 v[196:199], v[204:207], v[184:191], v251\n\t" \
  "ds_read_b128 v[200:203], v248 offset:19456\n\t" \
  "ds_read_b128 v[204:207], v248 offset:23552\n\t" \
  "v_mov_b32 v250, v232\n\t" \
  "v_mov_b32 v249, v233\n\t" \
  "s_nop 2\n\t" \
  "v_add_f32 v192, v192, v194\n\t" \
  "v_add_f32 v193, v193, v195\n\t" \
  "v_add_f32 v196, v196, v198\n\t" \
  "v_add_f32 v197, v197, v199\n\t" \
  "v_cvt_pk_bf16_f32 v192, v192, v193\n\t" \
  "v_cvt_pk_bf16_f32 v196, v196, v197\n\t" \
  "ds_write2_b32 v252, v192, v196 offset1:64\n\t" \
  "v_mov_b32 v251, v234\n\t" \
  "ds_read_b64_tr_b16 v[244:245], v250 offset:1536\n\t" \
  "ds_read_b64_tr_b16 v[246:247], v250 offset:1600\n\t" \
  "v_mov_b32 v212, %[step]\n\t" \
  "s_mov_b64 exec, 1\n\t" \
  "ds_write_b32 v248, v212 offset:28720\n\t" \
  "s_mov_b64 exec, -1\n\t" \
  "buffer_load_dwordx4 v[192:195], v248, %[rt], %[svt] offen offset:0 sc1\n\t" \
  "buffer_load_dwordx4 v[196:199], v248, %[rt], %[svt] offen offset:1024 sc1\n\t" \
  "s_waitcnt lgkmcnt(1)\n\t" \
  "v_mfma_f32_16x16x32_bf16 v[200:203], v[200:203], v[244:247], 0\n\t" \
  "v_mfma_f32_16x16x32_bf16 v[204:207], v[204:207], v[244:247], 0\n\t" \
  "s_nop 7\n\t" \
  "v_cndmask_b32 v216, v200, v204, %[nh]\n\t" \
  "v_cndmask_b32 v217, v201, v205, %[nh]\n\t" \
  "v_cndmask_b32 v218, v202, v206, %[nh]\n\t" \
  "v_cndmask_b32 v219, v203, v207, %[nh]\n\t" \
  "ds_write_b128 v248, v[216:219] offset:19456\n\t" \
  "buffer_load_dword v252, v249, %[rs], %[sv] offen offset:0 sc1\n\t" \
  "buffer_load_dword v253, v249, %[rs], %[sv] offen offset:256 sc1\n\t" \
  "ds_read_b32 v212, v248 offset:28672\n\t" \
  "ds_read_b64_tr_b16 v[232:233], v250 offset:0\n\t" \
  "ds_read_b64_tr_b16 v[234:235], v250 offset:64\n\t" \
  "ds_read_b64_tr_b16 v[236:237], v250 offset:512\n\t" \
  "ds_read_b64_tr_b16 v[238:239], v250 offset:576\n\t" \
  "ds_read_b64_tr_b16 v[240:241], v250 offset:1024\n\t" \
  "ds_read_b64_tr_b16 v[242:243], v250 offset:1088\n\t" \
  "s_setprio 3\n\t" \
  "s_waitcnt lgkmcnt(0)\n\t" \
  "v_cmp_ne_u32 vcc, %[step], v212\n\t" \
  "s_barrier\n\t" \
  "s_and_b32 s93, vcc_lo, 0xf\n\t" \
  "s_cbranch_scc1 .Lre%=\n\t" \
  ".Lrb%=:\n\t" \
  "ds_read_b128 v[220:223], v248 offset:16384\n\t" \
  "ds_read_b128 v[224:227], v248 offset:17408\n\t" \
  "ds_read_b128 v[228:231], v248 offset:18432\n\t" \
  "ds_read_b64_tr_b16 v[212:213], v250 offset:3584\n\t" \
  "ds_read_b64_tr_b16 v[214:215], v250 offset:3648\n\t" \
  "ds_read_b64_tr_b16 v[200:201], v250 offset:2048\n\t" \
  "ds_read_b64_tr_b16 v[202:203], v250 offset:2112\n\t" \
  "ds_read_b64_tr_b16 v[204:205], v250 offset:2560\n\t" \
  "ds_read_b64_tr_b16 v[206:207], v250 offset:2624\n\t" \
  "ds_read_b64_tr_b16 v[208:209], v250 offset:3072\n\t" \
  "ds_read_b64_tr_b16 v[210:211], v250 offset:3136\n\t" \
  "s_waitcnt lgkmcnt(10)\n\t" \
  "v_pk_add_f32 v[216:217], v[216:217], v[220:221]\n\t" \
  "v_pk_add_f32 v[218:219], v[218:219], v[222:223]\n\t" \
  "s_waitcnt lgkmcnt(8)\n\t" \
  "v_pk_add_f32 v[224:225], v[224:225], v[228:229]\n\t" \
  "v_pk_add_f32 v[226:227], v[226:227], v[230:231]\n\t" \
  "v_pk_add_f32 v[216:217], v[216:217], v[224:225]\n\t" \
  "v_pk_add_f32 v[218:219], v[218:219], v[226:227]\n\t" \
  "s_waitcnt lgkmcnt(6)\n\t" \
  "v_mfma_f32_16x16x32_bf16 v[212:215], v[212:215], v[244:247], 0\n\t" \
  "v_max3_f32 v224, v216, v217, v218\n\t" \
  "v_max_f32 v224, v224, v219\n\t" \
  "s_waitcnt lgkmcnt(4)\n\t" \
  "v_mfma_f32_16x16x32_bf16 v[212:215], v[200:203], v[232:235], v[212:215]\n\t" \
  "v_max_f32_dpp v225, v224, v224 row_ror:8 row_mask:0xf bank_mask:0xf\n\t" \
  "v_max_f32_dpp v226, v224, v224 row_ror:8 row_mask:0xf bank_mask:0xf\n\t" \
  "s_waitcnt lgkmcnt(2)\n\t" \
  "v_mfma_f32_16x16x32_bf16 v[212:215], v[204:207], v[236:239], v[212:215]\n\t" \
  "v_permlane32_swap_b32 v225, v226\n\t" \
  "s_waitcnt lgkmcnt(0)\n\t" \
  "v_mfma_f32_16x16x32_bf16 v[212:215], v[208:211], v[240:243], v[212:215]\n\t" \
  "v_max_f32 v231, v225, v226\n\t" \
  "v_mov_b32 v220, 0x3f803f80\n\t" \
  "v_mov_b32 v221, 0x3f803f80\n\t" \
  "v_mov_b64 v[222:223], v[220:221]\n\t" \
  "v_mov_b32 v245, 0\n\t" \
  "v_mov_b32 v246, 0\n\t" \
  "s_nop 1\n\t" \
  "v_max3_f32 v200, v212, v213, v231\n\t" \
  "v_max3_f32 v201, v200, v214, v215\n\t" \
  "v_max3_f32 v202, v200, v214, v215\n\t" \
  "v_cndmask_b32 v212, v212, v214, %[up]\n\t" \
  "v_cndmask_b32 v213, v213, v215, %[up]\n\t" \
  "v_permlane16_swap_b32 v201, v202\n\t" \
  "v_max_f32 v200, v201, v202\n\t" \
  "v_mul_f32 v200, %[negc], v200\n\t" \
  "v_fma_f32 v212, v212, %[c], v200\n\t" \
  "v_fma_f32 v213, v213, %[c], v200\n\t" \
  "v_exp_f32 v212, v212\n\t" \
  "v_fma_f32 v216, v216, %[c], v200\n\t" \
  "v_exp_f32 v213, v213\n\t" \
  "v_fma_f32 v217, v217, %[c], v200\n\t" \
  "v_exp_f32 v216, v216\n\t" \
  "v_fma_f32 v218, v218, %[c], v200\n\t" \
  "v_cvt_pk_bf16_f32 v244, v212, v213\n\t" \
  "v_cvt_pk_bf16_f32 v247, v212, v213\n\t" \
  "v_exp_f32 v217, v217\n\t" \
  "v_fma_f32 v219, v219, %[c], v200\n\t" \
  "v_mfma_f32_16x16x16_bf16 v[208:211], v[220:221], v[244:245], 0\n\t" \
  "v_exp_f32 v218, v218\n\t" \
  "s_waitcnt vmcnt(0)\n\t" \
  "v_mfma_f32_16x16x16_bf16 v[224:227], v[252:253], v[244:245], 0\n\t" \
  "v_cvt_pk_bf16_f32 v206, v216, v217\n\t" \
  "v_cvt_pk_bf16_f32 v204, v216, v217\n\t" \
  "v_exp_f32 v219, v219\n\t" \
  "v_mfma_f32_16x16x16_bf16 v[228:231], v[252:253], v[246:247], 0\n\t" \
  "v_cvt_pk_bf16_f32 v207, v218, v219\n\t" \
  "v_cvt_pk_bf16_f32 v205, v218, v219\n\t" \
  "v_mov_b32_dpp v204, v206 row_ror:8 row_mask:0xf bank_mask:0xc\n\t" \
  "v_mov_b32_dpp v206, v206 row_ror:8 row_mask:0xf bank_mask:0x3\n\t" \
  "v_mov_b32_dpp v205, v207 row_ror:8 row_mask:0xf bank_mask:0xc\n\t" \
  "v_mov_b32_dpp v207, v207 row_ror:8 row_mask:0xf bank_mask:0x3\n\t" \
  "v_max_u32 v200, v252, v253\n\t" \
  "v_cmp_le_u32 vcc, %[sent], v200\n\t" \
  "v_mfma_f32_16x16x32_bf16 v[208:211], v[220:223], v[204:207], v[208:211]\n\t" \
  ".Lpv%=:\n\t" \
  "v_mfma_f32_16x16x32_bf16 v[224:227], v[192:195], v[204:207], v[224:227]\n\t" \
  "v_mfma_f32_16x16x32_bf16 v[228:231], v[196:199], v[204:207], v[228:231]\n\t" \
  "s_cbranch_vccnz .Lvs%=\n\t" \
  ".Lvok%=:\n\t" \
  "s_nop 4\n\t" \
  "v_rcp_f32 v200, v208\n\t" \
  "v_cndmask_b32 v224, v224, v226, %[nh]\n\t" \
  "v_cndmask_b32 v225, v225, v227, %[nh]\n\t" \
  "v_mul_f32 v224, v224, v200\n\t" \
  "v_mul_f32 v225, v225, v200\n\t" \
  "v_cvt_pk_bf16_f32 v224, v224, v225\n\t" \
  "buffer_store_dword v224, v251, %[rs], %[so] offen offset:0 sc1\n\t" \
  "v_cndmask_b32 v228, v228, v230, %[nh]\n\t" \
  "v_cndmask_b32 v229, v229, v231, %[nh]\n\t" \
  "v_mul_f32 v228, v228, v200\n\t" \
  "v_mul_f32 v229, v229, v200\n\t" \
  "v_cvt_pk_bf16_f32 v228, v228, v229\n\t" \
  "buffer_store_dword v228, v251, %[rs], %[so] offen offset:256 sc1\n\t" \
  "s_memtime s[90:91]\n\t" \
  "s_setprio 0\n\t" \
  "s_waitcnt lgkmcnt(0)\n\t" \
  "s_mov_b32 %[tout], s90\n\t" \
  "s_mov_b32 %[spins], s92\n\t" \
  "s_branch .Lend%=\n\t" \
  ".Lre%=:\n\t" \
  "ds_read_b64_tr_b16 v[232:233], v250 offset:0\n\t" \
  "ds_read_b64_tr_b16 v[234:235], v250 offset:64\n\t" \
  "ds_read_b64_tr_b16 v[236:237], v250 offset:512\n\t" \
  "ds_read_b64_tr_b16 v[238:239], v250 offset:576\n\t" \
  "ds_read_b64_tr_b16 v[240:241], v250 offset:1024\n\t" \
  "ds_read_b64_tr_b16 v[242:243], v250 offset:1088\n\t" \
  "s_waitcnt lgkmcnt(0)\n\t" \
  "s_branch .Lrb%=\n\t" \
  ".Lvs%=:\n\t" \
  "v_cmp_eq_u32 vcc, %[sent], v252\n\t" \
  "v_cmp_eq_u32 s[94:95], %[sent], v253\n\t" \
  "s_or_b64 s[94:95], s[94:95], vcc\n\t" \
  "s_cbranch_scc0 .Lvok%=\n\t" \
  "s_add_u32 s92, s92, 1\n\t" \
  "s_sleep 2\n\t" \
  "s_and_b32 s93, s92, 63\n\t" \
  "s_cbranch_scc1 .Lvr%=\n\t" \
  "buffer_load_dword v200, off, %[rs], %[sab] sc1\n\t" \
  "s_waitcnt vmcnt(0)\n\t" \
  "v_readfirstlane_b32 s93, v200\n\t" \
  "s_nop 3\n\t" \
  "s_cmp_eq_u32 s93, 1\n\t" \
  "s_cbranch_scc1 .Lvok%=\n\t" \
  "s_cmp_lt_u32 s92, 0x80000\n\t" \
  "s_cbranch_scc1 .Lvr%=\n\t" \
  "v_mov_b32 v200, 1\n\t" \
  "buffer_store_dword v200, off, %[rs], %[sab] sc1\n\t" \
  "s_branch .Lvok%=\n\t" \
  ".Lvr%=:\n\t" \
  "buffer_load_dword v252, v249, %[rs], %[sv] offen offset:0 sc1\n\t" \
  "buffer_load_dword v253, v249, %[rs], %[sv] offen offset:256 sc1\n\t" \
  "s_waitcnt vmcnt(0)\n\t" \
  "v_mfma_f32_16x16x16_bf16 v[224:227], v[252:253], v[244:245], 0\n\t" \
  "v_mfma_f32_16x16x16_bf16 v[228:231], v[252:253], v[246:247], 0\n\t" \
  "v_max_u32 v200, v252, v253\n\t" \
  "v_cmp_le_u32 vcc, %[sent], v200\n\t" \
  "s_nop 4\n\t" \
  "s_branch .Lpv%=\n\t" \
  "s_branch .Lend%=\n\t" \
  ".Lslow%=:\n\t" \
  "s_mov_b64 s[94:95], 0\n\t" \
  "v_cmp_eq_u32 vcc, %[sent], v200\n\t" \
  "s_or_b64 s[94:95], s[94:95], vcc\n\t" \
  "v_cmp_eq_u32 vcc, %[sent], v201\n\t" \
  "s_or_b64 s[94:95], s[94:95], vcc\n\t" \
  "v_cmp_eq_u32 vcc, %[sent], v202\n\t" \
  "s_or_b64 s[94:95], s[94:95], vcc\n\t" \
  "v_cmp_eq_u32 vcc, %[sent], v203\n\t" \
  "s_or_b64 s[94:95], s[94:95], vcc\n\t" \
  "v_cmp_eq_u32 vcc, %[sent], v204\n\t" \
  "s_or_b64 s[94:95], s[94:95], vcc\n\t" \
  "v_cmp_eq_u32 vcc, %[sent], v205\n\t" \
  "s_or_b64 s[94:95], s[94:95], vcc\n\t" \
  "v_cmp_eq_u32 vcc, %[sent], v206\n\t" \
  "s_or_b64 s[94:95], s[94:95], vcc\n\t" \
  "v_cmp_eq_u32 vcc, %[sent], v207\n\t" \
  "s_or_b64 s[94:95], s[94:95], vcc\n\t" \
  "s_cmp_eq_u64 s[94:95], 0\n\t" \
  "s_cbranch_scc1 .Lok%=\n\t" \
  "v_mov_b64 v[192:193], 0\n\t" \
  "v_mov_b64 v[194:195], 0\n\t" \
  "v_mov_b64 v[196:197], 0\n\t" \
  "v_mov_b64 v[198:199], 0\n\t" \
  "s_add_u32 s92, s92, 1\n\t" \
  "s_sleep 6\n\t" \
  "s_cmp_eq_u32 s92, 1\n\t" \
  "s_cbranch_scc1 .Lpoll%=\n\t" \
  "s_sleep 1\n\t" \
  "s_and_b32 s93, s92, 63\n\t" \
  "s_cmp_lg_u32 s93, 0\n\t" \
  "s_cbranch_scc1 .Lpoll%=\n\t" \
  "v_mov_b32 v245, 0\n\t" \
  "buffer_load_dword v244, v245, %[rs], %[sab] offen sc1\n\t" \
  "s_waitcnt vmcnt(0)\n\t" \
  "v_readfirstlane_b32 s93, v244\n\t" \
  "s_nop 3\n\t" \
  "s_cmp_eq_u32 s93, 1\n\t" \
  "s_cbranch_scc1 .Lok%=\n\t" \
  "s_cmp_lt_u32 s92, 0x80000\n\t" \
  "s_cbranch_scc1 .Lpoll%=\n\t" \
  "v_mov_b32 v244, 1\n\t" \
  "buffer_store_dword v244, v245, %[rs], %[sab] offen sc1\n\t" \
  "s_branch .Lok%=\n\t" \
  ".Lend%=:\n\t"
#define QK_ASM_4 \
  "s_waitcnt lgkmcnt(0)\n\t" \
  "v_subrev_u32 v244, 0x8000, v250\n\t" \
  "ds_read_b128 v[216:219], v248 offset:49152\n\t" \
  "ds_read_b128 v[220:223], v248 offset:50176\n\t" \
  "ds_read_b128 v[224:227], v248 offset:51200\n\t" \
  "ds_read_b128 v[228:231], v248 offset:52224\n\t" \
  "ds_read_b128 v[208:211], v244 offset:0\n\t" \
  "ds_read_b128 v[212:215], v244 offset:1024\n\t" \
  "ds_read_b128 v[236:239], v244 offset:2048\n\t" \
  "ds_read_b128 v[240:243], v244 offset:3072\n\t" \
  "v_mov_b64 v[192:193], 0\n\t" \
  "v_mov_b64 v[194:195], 0\n\t" \
  "v_mov_b64 v[196:197], 0\n\t" \
  "v_mov_b64 v[198:199], 0\n\t" \
  "s_movk_i32 s92, 0x2000\n\t" \
  ".Lwt%=:\n\t" \
  "ds_read_b64 v[200:201], v248 offset:30720\n\t" \
  "s_memrealtime s[90:91]\n\t" \
  "s_sub_u32 s92, s92, 1\n\t" \
  "s_waitcnt lgkmcnt(0)\n\t" \
  "v_readfirstlane_b32 s93, v200\n\t" \
  "v_readfirstlane_b32 %[tgt], v201\n\t" \
  "s_cbranch_scc1 .Lgo%=\n\t" \
  "s_cmp_eq_u32 s93, %[step]\n\t" \
  "s_cbranch_scc1 .Lwx%=\n\t" \
  "s_sleep 1\n\t" \
  "s_branch .Lwt%=\n\t" \
  ".Lwx%=:\n\t" \
  "s_lshl_b32 s93, s90, 2\n\t" \
  "s_sub_i32 s93, %[tgt], s93\n\t" \
  "s_cmp_le_i32 s93, 0\n\t" \
  "s_cbranch_scc1 .Lgo%=\n\t" \
  "s_min_i32 s93, s93, 8000\n\t" \
  "s_add_u32 s93, s93, 3\n\t" \
  "s_lshr_b32 s93, s93, 2\n\t" \
  "s_add_u32 s93, s93, s90\n\t" \
  "s_memrealtime s[90:91]\n\t" \
  ".Lpace%=:\n\t" \
  "s_memrealtime s[94:95]\n\t" \
  "s_waitcnt lgkmcnt(1)\n\t" \
  "s_sub_i32 s91, s90, s93\n\t" \
  "s_cmp_ge_i32 s91, 0\n\t" \
  "s_cbranch_scc1 .Lgo%=\n\t" \
  "s_memrealtime s[90:91]\n\t" \
  "s_waitcnt lgkmcnt(1)\n\t" \
  "s_sub_i32 s95, s94, s93\n\t" \
  "s_cmp_lt_i32 s95, 0\n\t" \
  "s_cbranch_scc1 .Lpace%=\n\t" \
  ".Lgo%=:\n\t" \
  "s_mov_b32 s92, 0\n\t" \
  ".Lpoll%=:\n\t" \
  "buffer_load_dwordx4 v[200:203], v248, %[rs], %[som] offen sc1\n\t" \
  "buffer_load_dwordx4 v[204:207], v248, %[rs], %[som2] offen sc1\n\t" \
  "s_waitcnt vmcnt(1) lgkmcnt(0)\n\t" \
  "ds_write_b128 v249, v[200:203]\n\t" \
  "v_smfmac_f32_16x16x64_bf16 v[192:195], v[200:203], v[208:215], v251\n\t" \
  "v_max3_u32 v244, v200, v201, v202\n\t" \
  "v_smfmac_f32_16x16x64_bf16 v[196:199], v[200:203], v[236:243], v251\n\t" \
  "s_waitcnt vmcnt(0)\n\t" \
  "ds_write_b128 v249, v[204:207] offset:8192\n\t" \
  "v_smfmac_f32_16x16x64_bf16 v[192:195], v[204:207], v[216:223], v251\n\t" \
  "v_max3_u32 v244, v244, v203, v204\n\t" \
  "v_max3_u32 v244, v244, v205, v206\n\t" \
  "v_smfmac_f32_16x16x64_bf16 v[196:199], v[204:207], v[224:231], v251\n\t" \
  "v_max_u32 v244, v244, v207\n\t" \
  "v_cmp_le_u32 vcc, %[sent], v244\n\t" \
  "s_cbranch_vccnz .Lslow%=\n\t" \
  ".Lok%=:\n\t" \
  "ds_read_b128 v[216:219], v250 offset:32768\n\t" \
  "ds_read_b128 v[220:223], v250 offset:33792\n\t" \
  "ds_read_b128 v[224:227], v250 offset:34816\n\t" \
  "ds_read_b128 v[228:231], v250 offset:35840\n\t" \
  "s_waitcnt lgkmcnt(4)\n\t" \
  "s_barrier\n\t" \
  "ds_read_b128 v[208:211], v248 offset:5120\n\t" \
  "ds_read_b128 v[212:215], v248 offset:6144\n\t" \
  "ds_read_b128 v[236:239], v248 offset:7168\n\t" \
  "ds_read_b128 v[240:243], v248 offset:8192\n\t" \
  "s_waitcnt lgkmcnt(3)\n\t" \
  "ds_read_b128 v[244:247], v248 offset:13312\n\t" \
  "v_smfmac_f32_16x16x64_bf16 v[192:195], v[208:211], v[216:223], v251\n\t" \
  "ds_read_b128 v[216:219], v250 offset:0\n\t" \
  "v_smfmac_f32_16x16x64_bf16 v[196:199], v[208:211], v[224:231], v251\n\t" \
  "s_waitcnt lgkmcnt(4)\n\t" \
  "ds_read_b128 v[220:223], v250 offset:1024\n\t" \
  "v_smfmac_f32_16x16x64_bf16 v[192:195], v[212:215], v[0:7], v251\n\t" \
  "ds_read_b128 v[224:227], v250 offset:2048\n\t" \
  "v_smfmac_f32_16x16x64_bf16 v[196:199], v[212:215], v[8:15], v251\n\t" \
  "s_waitcnt lgkmcnt(5)\n\t" \
  "ds_read_b128 v[228:231], v250 offset:3072\n\t" \
  "v_smfmac_f32_16x16x64_bf16 v[192:195], v[236:239], v[16:23], v251\n\t" \
  "ds_read_b128 v[200:203], v248 offset:9216\n\t" \
  "v_smfmac_f32_16x16x64_bf16 v[196:199], v[236:239], v[24:31], v251\n\t" \
  "s_waitcnt lgkmcnt(6)\n\t" \
  "ds_read_b128 v[204:207], v248 offset:10240\n\t" \
  "v_smfmac_f32_16x16x64_bf16 v[192:195], v[240:243], v[32:39], v251\n\t" \
  "ds_read_b128 v[208:211], v248 offset:11264\n\t" \
  "v_smfmac_f32_16x16x64_bf16 v[196:199], v[240:243], v[40:47], v251\n\t" \
  "s_waitcnt lgkmcnt(5)\n\t" \
  "ds_read_b128 v[212:215], v248 offset:14336\n\t" \
  "v_smfmac_f32_16x16x64_bf16 v[192:195], v[244:247], v[216:223], v251\n\t" \
  "s_waitcnt lgkmcnt(4)\n\t" \
  "v_smfmac_f32_16x16x64_bf16 v[196:199], v[244:247], v[224:231], v251\n\t" \
  "s_waitcnt lgkmcnt(3)\n\t" \
  "ds_read_b128 v[236:239], v248 offset:15360\n\t" \
  "v_smfmac_f32_16x16x64_bf16 v[192:195], v[200:203], v[48:55], v251\n\t" \
  "ds_read_b128 v[240:243], v248 offset:0\n\t" \
  "v_smfmac_f32_16x16x64_bf16 v[196:199], v[200:203], v[56:63], v251\n\t" \
  "s_waitcnt lgkmcnt(4)\n\t" \
  "v_smfmac_f32_16x16x64_bf16 v[192:195], v[204:207], v[64:71], v251\n\t" \
  "ds_read_b128 v[244:247], v248 offset:1024\n\t" \
  "v_smfmac_f32_16x16x64_bf16 v[196:199], v[204:207], v[72:79], v251\n\t" \
  "s_waitcnt lgkmcnt(4)\n\t" \
  "v_smfmac_f32_16x16x64_bf16 v[192:195], v[208:211], v[80:87], v251\n\t" \
  "ds_read_b128 v[200:203], v248 offset:2048\n\t" \
  "v_smfmac_f32_16x16x64_bf16 v[196:199], v[208:211], v[88:95], v251\n\t" \
  "s_waitcnt lgkmcnt(4)\n\t" \
  "v_smfmac_f32_16x16x64_bf16 v[192:195], v[212:215], v[96:103], v251\n\t" \
  "ds_read_b128 v[204:207], v248 offset:3072\n\t" \
  "v_smfmac_f32_16x16x64_bf16 v[196:199], v[212:215], v[104:111], v251\n\t" \
  "s_waitcnt lgkmcnt(3)\n\t" \
  "v_smfmac_f32_16x16x64_bf16 v[192:195], v[236:239], v[112:119], v251\n\t" \
  "v_smfmac_f32_16x16x64_bf16 v[196:199], v[236:239], v[120:127], v251\n\t" \
  "v_smfmac_f32_16x16x64_bf16 v[192:195], v[240:243], v[128:135], v251\n\t" \
  "v_smfmac_f32_16x16x64_bf16 v[196:199], v[240:243], v[136:143], v251\n\t" \
  "s_waitcnt lgkmcnt(1)\n\t" \
  "v_smfmac_f32_16x16x64_bf16 v[192:195], v[244:247], v[144:151], v251\n\t" \
  "v_smfmac_f32_16x16x64_bf16 v[196:199], v[244:247], v[152:159], v251\n\t" \
  "v_smfmac_f32_16x16x64_bf16 v[192:195], v[200:203], v[160:167], v251\n\t" \
  "v_smfmac_f32_16x16x64_bf16 v[196:199], v[200:203], v[168:175], v251\n\t" \
  "s_waitcnt lgkmcnt(0)\n\t" \
  "v_smfmac_f32_16x16x64_bf16 v[192:195], v[204:207], v[176:183], v251\n\t" \
  "v_smfmac_f32_16x16x64_bf16 v[196:199], v[204:207], v[184:191], v251\n\t" \
  "s_nop 6\n\t" \
  "v_add_f32 v192, v192, v194\n\t" \
  "v_add_f32 v193, v193, v195\n\t" \
  "v_add_f32 v196, v196, v198\n\t" \
  "v_add_f32 v197, v197, v199\n\t" \
  "v_cvt_pk_bf16_f32 v192, v192, v193\n\t" \
  "v_cvt_pk_bf16_f32 v196, v196, v197\n\t" \
  "ds_write2_b32 v252, v192, v196 offset1:64\n\t" \
  "s_waitcnt lgkmcnt(0)\n\t" \
  "s_barrier\n\t" \
  "buffer_store_dword v192, v235, %[rs], %[sk] offen sc1\n\t" \
  "buffer_store_dword v196, v235, %[rs], %[sk] offen offset:256 sc1\n\t" \
  "s_mov_b32 %[spins], s92\n\t" \
  "s_branch .Lend%=\n\t" \
  ".Lslow%=:\n\t" \
  "s_mov_b64 s[94:95], 0\n\t" \
  "v_cmp_eq_u32 vcc, %[sent], v200\n\t" \
  "s_or_b64 s[94:95], s[94:95], vcc\n\t" \
  "v_cmp_eq_u32 vcc, %[sent], v201\n\t" \
  "s_or_b64 s[94:95], s[94:95], vcc\n\t" \
  "v_cmp_eq_u32 vcc, %[sent], v202\n\t" \
  "s_or_b64 s[94:95], s[94:95], vcc\n\t" \
  "v_cmp_eq_u32 vcc, %[sent], v203\n\t" \
  "s_or_b64 s[94:95], s[94:95], vcc\n\t" \
  "v_cmp_eq_u32 vcc, %[sent], v204\n\t" \
  "s_or_b64 s[94:95], s[94:95], vcc\n\t" \
  "v_cmp_eq_u32 vcc, %[sent], v205\n\t" \
  "s_or_b64 s[94:95], s[94:95], vcc\n\t" \
  "v_cmp_eq_u32 vcc, %[sent], v206\n\t" \
  "s_or_b64 s[94:95], s[94:95], vcc\n\t" \
  "v_cmp_eq_u32 vcc, %[sent], v207\n\t" \
  "s_or_b64 s[94:95], s[94:95], vcc\n\t" \
  "s_cmp_eq_u64 s[94:95], 0\n\t" \
  "s_cbranch_scc1 .Lok%=\n\t" \
  "v_mov_b64 v[192:193], 0\n\t" \
  "v_mov_b64 v[194:195], 0\n\t" \
  "v_mov_b64 v[196:197], 0\n\t" \
  "v_mov_b64 v[198:199], 0\n\t" \
  "s_add_u32 s92, s92, 1\n\t" \
  "s_sleep 6\n\t" \
  "s_cmp_eq_u32 s92, 1\n\t" \
  "s_cbranch_scc1 .Lpoll%=\n\t" \
  "s_sleep 1\n\t" \
  "s_and_b32 s93, s92, 63\n\t" \
  "s_cmp_lg_u32 s93, 0\n\t" \
  "s_cbranch_scc1 .Lpoll%=\n\t" \
  "v_mov_b32 v245, 0\n\t" \
  "buffer_load_dword v244, v245, %[rs], %[sab] offen sc1\n\t" \
  "s_waitcnt vmcnt(0)\n\t" \
  "v_readfirstlane_b32 s93, v244\n\t" \
  "s_nop 3\n\t" \
  "s_cmp_eq_u32 s93, 1\n\t" \
  "s_cbranch_scc1 .Lok%=\n\t" \
  "s_cmp_lt_u32 s92, 0x80000\n\t" \
  "s_cbranch_scc1 .Lpoll%=\n\t" \
  "v_mov_b32 v244, 1\n\t" \
  "buffer_store_dword v244, v245, %[rs], %[sab] offen sc1\n\t" \
  "s_branch .Lok%=\n\t" \
  ".Lend%=:\n\t"
#define QK_ASM_5 \
  "s_waitcnt lgkmcnt(0)\n\t" \
  "v_subrev_u32 v244, 0x8000, v250\n\t" \
  "ds_read_b128 v[216:219], v248 offset:53248\n\t" \
  "ds_read_b128 v[220:223], v248 offset:54272\n\t" \
  "ds_read_b128 v[224:227], v248 offset:55296\n\t" \
  "ds_read_b128 v[228:231], v248 offset:56320\n\t" \
  "ds_read_b128 v[208:211], v244 offset:0\n\t" \
  "ds_read_b128 v[212:215], v244 offset:1024\n\t" \
  "ds_read_b128 v[236:239], v244 offset:2048\n\t" \
  "ds_read_b128 v[240:243], v244 offset:3072\n\t" \
  "v_mov_b64 v[192:193], 0\n\t" \
  "v_mov_b64 v[194:195], 0\n\t" \
  "v_mov_b64 v[196:197], 0\n\t" \
  "v_mov_b64 v[198:199], 0\n\t" \
  "s_movk_i32 s92, 0x2000\n\t" \
  ".Lwt%=:\n\t" \
  "ds_read_b64 v[200:201], v248 offset:30720\n\t" \
  "s_memrealtime s[90:91]\n\t" \
  "s_sub_u32 s92, s92, 1\n\t" \
  "s_waitcnt lgkmcnt(0)\n\t" \
  "v_readfirstlane_b32 s93, v200\n\t" \
  "v_readfirstlane_b32 %[tgt], v201\n\t" \
  "s_cbranch_scc1 .Lgo%=\n\t" \
  "s_cmp_eq_u32 s93, %[step]\n\t" \
  "s_cbranch_scc1 .Lwx%=\n\t" \
  "s_sleep 1\n\t" \
  "s_branch .Lwt%=\n\t" \
  ".Lwx%=:\n\t" \
  "s_lshl_b32 s93, s90, 2\n\t" \
  "s_sub_i32 s93, %[tgt], s93\n\t" \
  "s_cmp_le_i32 s93, 0\n\t" \
  "s_cbranch_scc1 .Lgo%=\n\t" \
  "s_min_i32 s93, s93, 8000\n\t" \
  "s_add_u32 s93, s93, 3\n\t" \
  "s_lshr_b32 s93, s93, 2\n\t" \
  "s_add_u32 s93, s93, s90\n\t" \
  "s_memrealtime s[90:91]\n\t" \
  ".Lpace%=:\n\t" \
  "s_memrealtime s[94:95]\n\t" \
  "s_waitcnt lgkmcnt(1)\n\t" \
  "s_sub_i32 s91, s90, s93\n\t" \
  "s_cmp_ge_i32 s91, 0\n\t" \
  "s_cbranch_scc1 .Lgo%=\n\t" \
  "s_memrealtime s[90:91]\n\t" \
  "s_waitcnt lgkmcnt(1)\n\t" \
  "s_sub_i32 s95, s94, s93\n\t" \
  "s_cmp_lt_i32 s95, 0\n\t" \
  "s_cbranch_scc1 .Lpace%=\n\t" \
  ".Lgo%=:\n\t" \
  "s_mov_b32 s92, 0\n\t" \
  ".Lpoll%=:\n\t" \
  "buffer_load_dwordx4 v[200:203], v248, %[rs], %[som] offen sc1\n\t" \
  "buffer_load_dwordx4 v[204:207], v248, %[rs], %[som2] offen sc1\n\t" \
  "s_waitcnt vmcnt(1) lgkmcnt(0)\n\t" \
  "ds_write_b128 v249, v[200:203]\n\t" \
  "v_smfmac_f32_16x16x64_bf16 v[192:195], v[200:203], v[208:215], v251\n\t" \
  "v_max3_u32 v244, v200, v201, v202\n\t" \
  "v_smfmac_f32_16x16x64_bf16 v[196:199], v[200:203], v[236:243], v251\n\t" \
  "s_waitcnt vmcnt(0)\n\t" \
  "ds_write_b128 v249, v[204:207] offset:8192\n\t" \
  "v_smfmac_f32_16x16x64_bf16 v[192:195], v[204:207], v[216:223], v251\n\t" \
  "v_max3_u32 v244, v244, v203, v204\n\t" \
  "v_max3_u32 v244, v244, v205, v206\n\t" \
  "v_smfmac_f32_16x16x64_bf16 v[196:199], v[204:207], v[224:231], v251\n\t" \
  "v_max_u32 v244, v244, v207\n\t" \
  "v_cmp_le_u32 vcc, %[sent], v244\n\t" \
  "s_cbranch_vccnz .Lslow%=\n\t" \
  ".Lok%=:\n\t" \
  "ds_read_b128 v[216:219], v250 offset:32768\n\t" \
  "ds_read_b128 v[220:223], v250 offset:33792\n\t" \
  "ds_read_b128 v[224:227], v250 offset:34816\n\t" \
  "ds_read_b128 v[228:231], v250 offset:35840\n\t" \
  "s_waitcnt lgkmcnt(4)\n\t" \
  "s_barrier\n\t" \
  "ds_read_b128 v[208:211], v248 offset:4096\n\t" \
  "ds_read_b128 v[212:215], v248 offset:7168\n\t" \
  "ds_read_b128 v[236:239], v248 offset:8192\n\t" \
  "ds_read_b128 v[240:243], v248 offset:9216\n\t" \
  "s_waitcnt lgkmcnt(3)\n\t" \
  "ds_read_b128 v[244:247], v248 offset:12288\n\t" \
  "v_smfmac_f32_16x16x64_bf16 v[192:195], v[208:211], v[216:223], v251\n\t" \
  "ds_read_b128 v[216:219], v250 offset:0\n\t" \
  "v_smfmac_f32_16x16x64_bf16 v[196:199], v[208:211], v[224:231], v251\n\t" \
  "s_waitcnt lgkmcnt(4)\n\t" \
  "ds_read_b128 v[220:223], v250 offset:1024\n\t" \
  "v_smfmac_f32_16x16x64_bf16 v[192:195], v[212:215], v[0:7], v251\n\t" \
  "ds_read_b128 v[224:227], v250 offset:2048\n\t" \
  "v_smfmac_f32_16x16x64_bf16 v[196:199], v[212:215], v[8:15], v251\n\t" \
  "s_waitcnt lgkmcnt(5)\n\t" \
  "ds_read_b128 v[228:231], v250 offset:3072\n\t" \
  "v_smfmac_f32_16x16x64_bf16 v[192:195], v[236:239], v[16:23], v251\n\t" \
  "ds_read_b128 v[200:203], v248 offset:10240\n\t" \
  "v_smfmac_f32_16x16x64_bf16 v[196:199], v[236:239], v[24:31], v251\n\t" \
  "s_waitcnt lgkmcnt(6)\n\t" \
  "ds_read_b128 v[204:207], v248 offset:11264\n\t" \
  "v_smfmac_f32_16x16x64_bf16 v[192:195], v[240:243], v[32:39], v251\n\t" \
  "ds_read_b128 v[208:211], v248 offset:14336\n\t" \
  "v_smfmac_f32_16x16x64_bf16 v[196:199], v[240:243], v[40:47], v251\n\t" \
  "s_waitcnt lgkmcnt(5)\n\t" \
  "ds_read_b128 v[212:215], v248 offset:15360\n\t" \
  "v_smfmac_f32_16x16x64_bf16 v[192:195], v[244:247], v[216:223], v251\n\t" \
  "s_waitcnt lgkmcnt(4)\n\t" \
  "v_smfmac_f32_16x16x64_bf16 v[196:199], v[244:247], v[224:231], v251\n\t" \
  "s_waitcnt lgkmcnt(3)\n\t" \
  "ds_read_b128 v[236:239], v248 offset:0\n\t" \
  "v_smfmac_f32_16x16x64_bf16 v[192:195], v[200:203], v[48:55], v251\n\t" \
  "ds_read_b128 v[240:243], v248 offset:1024\n\t" \
  "v_smfmac_f32_16x16x64_bf16 v[196:199], v[200:203], v[56:63], v251\n\t" \
  "s_waitcnt lgkmcnt(4)\n\t" \
  "v_smfmac_f32_16x16x64_bf16 v[192:195], v[204:207], v[64:71], v251\n\t" \
  "ds_read_b128 v[244:247], v248 offset:2048\n\t" \
  "v_smfmac_f32_16x16x64_bf16 v[196:199], v[204:207], v[72:79], v251\n\t" \
  "s_waitcnt lgkmcnt(4)\n\t" \
  "v_smfmac_f32_16x16x64_bf16 v[192:195], v[208:211], v[80:87], v251\n\t" \
  "ds_read_b128 v[200:203], v248 offset:3072\n\t" \
  "v_smfmac_f32_16x16x64_bf16 v[196:199], v[208:211], v[88:95], v251\n\t" \
  "s_waitcnt lgkmcnt(4)\n\t" \
  "v_smfmac_f32_16x16x64_bf16 v[192:195], v[212:215], v[96:103], v251\n\t" \
  "ds_read_b128 v[204:207], v248 offset:6144\n\t" \
  "v_smfmac_f32_16x16x64_bf16 v[196:199], v[212:215], v[104:111], v251\n\t" \
  "s_waitcnt lgkmcnt(3)\n\t" \
  "v_smfmac_f32_16x16x64_bf16 v[192:195], v[236:239], v[112:119], v251\n\t" \
  "v_smfmac_f32_16x16x64_bf16 v[196:199], v[236:239], v[120:127], v251\n\t" \
  "v_smfmac_f32_16x16x64_bf16 v[192:195], v[240:243], v[128:135], v251\n\t" \
  "v_smfmac_f32_16x16x64_bf16 v[196:199], v[240:243], v[136:143], v251\n\t" \
  "s_waitcnt lgkmcnt(1)\n\t" \
  "v_smfmac_f32_16x16x64_bf16 v[192:195], v[244:247], v[144:151], v251\n\t" \
  "v_smfmac_f32_16x16x64_bf16 v[196:199], v[244:247], v[152:159], v251\n\t" \
  "v_smfmac_f32_16x16x64_bf16 v[192:195], v[200:203], v[160:167], v251\n\t" \
  "v_smfmac_f32_16x16x64_bf16 v[196:199], v[200:203], v[168:175], v251\n\t" \
  "s_waitcnt lgkmcnt(0)\n\t" \
  "v_smfmac_f32_16x16x64_bf16 v[192:195], v[204:207], v[176:183], v251\n\t" \
  "v_smfmac_f32_16x16x64_bf16 v[196:199], v[204:207], v[184:191], v251\n\t" \
  "s_nop 6\n\t" \
  "v_add_f32 v192, v192, v194\n\t" \
  "v_add_f32 v193, v193, v195\n\t" \
  "v_add_f32 v196, v196, v198\n\t" \
  "v_add_f32 v197, v197, v199\n\t" \
  "v_cvt_pk_bf16_f32 v192, v192, v193\n\t" \
  "v_cvt_pk_bf16_f32 v196, v196, v197\n\t" \
  "ds_write2_b32 v252, v192, v196 offset1:64\n\t" \
  "s_waitcnt lgkmcnt(0)\n\t" \
  "s_barrier\n\t" \
  "buffer_store_dword v192, v235, %[rs], %[sk] offen sc1\n\t" \
  "buffer_store_dword v196, v235, %[rs], %[sk] offen offset:256 sc1\n\t" \
  "s_mov_b32 %[spins], s92\n\t" \
  "s_branch .Lend%=\n\t" \
  ".Lslow%=:\n\t" \
  "s_mov_b64 s[94:95], 0\n\t" \
  "v_cmp_eq_u32 vcc, %[sent], v200\n\t" \
  "s_or_b64 s[94:95], s[94:95], vcc\n\t" \
  "v_cmp_eq_u32 vcc, %[sent], v201\n\t" \
  "s_or_b64 s[94:95], s[94:95], vcc\n\t" \
  "v_cmp_eq_u32 vcc, %[sent], v202\n\t" \
  "s_or_b64 s[94:95], s[94:95], vcc\n\t" \
  "v_cmp_eq_u32 vcc, %[sent], v203\n\t" \
  "s_or_b64 s[94:95], s[94:95], vcc\n\t" \
  "v_cmp_eq_u32 vcc, %[sent], v204\n\t" \
  "s_or_b64 s[94:95], s[94:95], vcc\n\t" \
  "v_cmp_eq_u32 vcc, %[sent], v205\n\t" \
  "s_or_b64 s[94:95], s[94:95], vcc\n\t" \
  "v_cmp_eq_u32 vcc, %[sent], v206\n\t" \
  "s_or_b64 s[94:95], s[94:95], vcc\n\t" \
  "v_cmp_eq_u32 vcc, %[sent], v207\n\t" \
  "s_or_b64 s[94:95], s[94:95], vcc\n\t" \
  "s_cmp_eq_u64 s[94:95], 0\n\t" \
  "s_cbranch_scc1 .Lok%=\n\t" \
  "v_mov_b64 v[192:193], 0\n\t" \
  "v_mov_b64 v[194:195], 0\n\t" \
  "v_mov_b64 v[196:197], 0\n\t" \
  "v_mov_b64 v[198:199], 0\n\t" \
  "s_add_u32 s92, s92, 1\n\t" \
  "s_sleep 6\n\t" \
  "s_cmp_eq_u32 s92, 1\n\t" \
  "s_cbranch_scc1 .Lpoll%=\n\t" \
  "s_sleep 1\n\t" \
  "s_and_b32 s93, s92, 63\n\t" \
  "s_cmp_lg_u32 s93, 0\n\t" \
  "s_cbranch_scc1 .Lpoll%=\n\t" \
  "v_mov_b32 v245, 0\n\t" \
  "buffer_load_dword v244, v245, %[rs], %[sab] offen sc1\n\t" \
  "s_waitcnt vmcnt(0)\n\t" \
  "v_readfirstlane_b32 s93, v244\n\t" \
  "s_nop 3\n\t" \
  "s_cmp_eq_u32 s93, 1\n\t" \
  "s_cbranch_scc1 .Lok%=\n\t" \
  "s_cmp_lt_u32 s92, 0x80000\n\t" \
  "s_cbranch_scc1 .Lpoll%=\n\t" \
  "v_mov_b32 v244, 1\n\t" \
  "buffer_store_dword v244, v245, %[rs], %[sab] offen sc1\n\t" \
  "s_branch .Lok%=\n\t" \
  ".Lend%=:\n\t"
#define QK_ASM_6 \
  "s_waitcnt lgkmcnt(0)\n\t" \
  "v_subrev_u32 v244, 0x8000, v250\n\t" \
  "ds_read_b128 v[216:219], v248 offset:57344\n\t" \
  "ds_read_b128 v[220:223], v248 offset:58368\n\t" \
  "ds_read_b128 v[224:227], v248 offset:59392\n\t" \
  "ds_read_b128 v[228:231], v248 offset:60416\n\t" \
  "ds_read_b128 v[208:211], v244 offset:0\n\t" \
  "ds_read_b128 v[212:215], v244 offset:1024\n\t" \
  "ds_read_b128 v[236:239], v244 offset:2048\n\t" \
  "ds_read_b128 v[240:243], v244 offset:3072\n\t" \
  "v_mov_b64 v[192:193], 0\n\t" \
  "v_mov_b64 v[194:195], 0\n\t" \
  "v_mov_b64 v[196:197], 0\n\t" \
  "v_mov_b64 v[198:199], 0\n\t" \
  "s_movk_i32 s92, 0x2000\n\t" \
  ".Lwt%=:\n\t" \
  "ds_read_b64 v[200:201], v248 offset:30720\n\t" \
  "s_memrealtime s[90:91]\n\t" \
  "s_sub_u32 s92, s92, 1\n\t" \
  "s_waitcnt lgkmcnt(0)\n\t" \
  "v_readfirstlane_b32 s93, v200\n\t" \
  "v_readfirstlane_b32 %[tgt], v201\n\t" \
  "s_cbranch_scc1 .Lgo%=\n\t" \
  "s_cmp_eq_u32 s93, %[step]\n\t" \
  "s_cbranch_scc1 .Lwx%=\n\t" \
  "s_sleep 1\n\t" \
  "s_branch .Lwt%=\n\t" \
  ".Lwx%=:\n\t" \
  "s_lshl_b32 s93, s90, 2\n\t" \
  "s_sub_i32 s93, %[tgt], s93\n\t" \
  "s_cmp_le_i32 s93, 0\n\t" \
  "s_cbranch_scc1 .Lgo%=\n\t" \
  "s_min_i32 s93, s93, 8000\n\t" \
  "s_add_u32 s93, s93, 3\n\t" \
  "s_lshr_b32 s93, s93, 2\n\t" \
  "s_add_u32 s93, s93, s90\n\t" \
  "s_memrealtime s[90:91]\n\t" \
  ".Lpace%=:\n\t" \
  "s_memrealtime s[94:95]\n\t" \
  "s_waitcnt lgkmcnt(1)\n\t" \
  "s_sub_i32 s91, s90, s93\n\t" \
  "s_cmp_ge_i32 s91, 0\n\t" \
  "s_cbranch_scc1 .Lgo%=\n\t" \
  "s_memrealtime s[90:91]\n\t" \
  "s_waitcnt lgkmcnt(1)\n\t" \
  "s_sub_i32 s95, s94, s93\n\t" \
  "s_cmp_lt_i32 s95, 0\n\t" \
  "s_cbranch_scc1 .Lpace%=\n\t" \
  ".Lgo%=:\n\t" \
  "s_mov_b32 s92, 0\n\t" \
  ".Lpoll%=:\n\t" \
  "buffer_load_dwordx4 v[200:203], v248, %[rs], %[som] offen sc1\n\t" \
  "buffer_load_dwordx4 v[204:207], v248, %[rs], %[som2] offen sc1\n\t" \
  "s_waitcnt vmcnt(1) lgkmcnt(0)\n\t" \
  "ds_write_b128 v249, v[200:203]\n\t" \
  "v_smfmac_f32_16x16x64_bf16 v[192:195], v[200:203], v[208:215], v251\n\t" \
  "v_max3_u32 v244, v200, v201, v202\n\t" \
  "v_smfmac_f32_16x16x64_bf16 v[196:199], v[200:203], v[236:243], v251\n\t" \
  "s_waitcnt vmcnt(0)\n\t" \
  "ds_write_b128 v249, v[204:207] offset:8192\n\t" \
  "v_smfmac_f32_16x16x64_bf16 v[192:195], v[204:207], v[216:223], v251\n\t" \
  "v_max3_u32 v244, v244, v203, v204\n\t" \
  "v_max3_u32 v244, v244, v205, v206\n\t" \
  "v_smfmac_f32_16x16x64_bf16 v[196:199], v[204:207], v[224:231], v251\n\t" \
  "v_max_u32 v244, v244, v207\n\t" \
  "v_cmp_le_u32 vcc, %[sent], v244\n\t" \
  "s_cbranch_vccnz .Lslow%=\n\t" \
  ".Lok%=:\n\t" \
  "ds_read_b128 v[216:219], v250 offset:32768\n\t" \
  "ds_read_b128 v[220:223], v250 offset:33792\n\t" \
  "ds_read_b128 v[224:227], v250 offset:34816\n\t" \
  "ds_read_b128 v[228:231], v250 offset:35840\n\t" \
  "s_waitcnt lgkmcnt(4)\n\t" \
  "s_barrier\n\t" \
  "ds_read_b128 v[208:211], v248 offset:7168\n\t" \
  "ds_read_b128 v[212:215], v248 offset:8192\n\t" \
  "ds_read_b128 v[236:239], v248 offset:9216\n\t" \
  "ds_read_b128 v[240:243], v248 offset:10240\n\t" \
  "s_waitcnt lgkmcnt(3)\n\t" \
  "ds_read_b128 v[244:247], v248 offset:13312\n\t" \
  "v_smfmac_f32_16x16x64_bf16 v[192:195], v[208:211], v[216:223], v251\n\t" \
  "ds_read_b128 v[216:219], v250 offset:0\n\t" \
  "v_smfmac_f32_16x16x64_bf16 v[196:199], v[208:211], v[224:231], v251\n\t" \
  "s_waitcnt lgkmcnt(4)\n\t" \
  "ds_read_b128 v[220:223], v250 offset:1024\n\t" \
  "v_smfmac_f32_16x16x64_bf16 v[192:195], v[212:215], v[0:7], v251\n\t" \
  "ds_read_b128 v[224:227], v250 offset:2048\n\t" \
  "v_smfmac_f32_16x16x64_bf16 v[196:199], v[212:215], v[8:15], v251\n\t" \
  "s_waitcnt lgkmcnt(5)\n\t" \
  "ds_read_b128 v[228:231], v250 offset:3072\n\t" \
  "v_smfmac_f32_16x16x64_bf16 v[192:195], v[236:239], v[16:23], v251\n\t" \
  "ds_read_b128 v[200:203], v248 offset:11264\n\t" \
  "v_smfmac_f32_16x16x64_bf16 v[196:199], v[236:239], v[24:31], v251\n\t" \
  "s_waitcnt lgkmcnt(6)\n\t" \
  "ds_read_b128 v[204:207], v248 offset:12288\n\t" \
  "v_smfmac_f32_16x16x64_bf16 v[192:195], v[240:243], v[32:39], v251\n\t" \
  "ds_read_b128 v[208:211], v248 offset:15360\n\t" \
  "v_smfmac_f32_16x16x64_bf16 v[196:199], v[240:243], v[40:47], v251\n\t" \
  "s_waitcnt lgkmcnt(5)\n\t" \
  "ds_read_b128 v[212:215], v248 offset:0\n\t" \
  "v_smfmac_f32_16x16x64_bf16 v[192:195], v[244:247], v[216:223], v251\n\t" \
  "s_waitcnt lgkmcnt(4)\n\t" \
  "v_smfmac_f32_16x16x64_bf16 v[196:199], v[244:247], v[224:231], v251\n\t" \
  "s_waitcnt lgkmcnt(3)\n\t" \
  "ds_read_b128 v[236:239], v248 offset:1024\n\t" \
  "v_smfmac_f32_16x16x64_bf16 v[192:195], v[200:203], v[48:55], v251\n\t" \
  "ds_read_b128 v[240:243], v248 offset:2048\n\t" \
  "v_smfmac_f32_16x16x64_bf16 v[196:199], v[200:203], v[56:63], v251\n\t" \
  "s_waitcnt lgkmcnt(4)\n\t" \
  "v_smfmac_f32_16x16x64_bf16 v[192:195], v[204:207], v[64:71], v251\n\t" \
  "ds_read_b128 v[244:247], v248 offset:3072\n\t" \
  "v_smfmac_f32_16x16x64_bf16 v[196:199], v[204:207], v[72:79], v251\n\t" \
  "s_waitcnt lgkmcnt(4)\n\t" \
  "v_smfmac_f32_16x16x64_bf16 v[192:195], v[208:211], v[80:87], v251\n\t" \
  "ds_read_b128 v[200:203], v248 offset:4096\n\t" \
  "v_smfmac_f32_16x16x64_bf16 v[196:199], v[208:211], v[88:95], v251\n\t" \
  "s_waitcnt lgkmcnt(4)\n\t" \
  "v_smfmac_f32_16x16x64_bf16 v[192:195], v[212:215], v[96:103], v251\n\t" \
  "ds_read_b128 v[204:207], v248 offset:5120\n\t" \
  "v_smfmac_f32_16x16x64_bf16 v[196:199], v[212:215], v[104:111], v251\n\t" \
  "s_waitcnt lgkmcnt(3)\n\t" \
  "v_smfmac_f32_16x16x64_bf16 v[192:195], v[236:239], v[112:119], v251\n\t" \
  "v_smfmac_f32_16x16x64_bf16 v[196:199], v[236:239], v[120:127], v251\n\t" \
  "v_smfmac_f32_16x16x64_bf16 v[192:195], v[240:243], v[128:135], v251\n\t" \
  "v_smfmac_f32_16x16x64_bf16 v[196:199], v[240:243], v[136:143], v251\n\t" \
  "s_waitcnt lgkmcnt(1)\n\t" \
  "v_smfmac_f32_16x16x64_bf16 v[192:195], v[244:247], v[144:151], v251\n\t" \
  "v_smfmac_f32_16x16x64_bf16 v[196:199], v[244:247], v[152:159], v251\n\t" \
  "v_smfmac_f32_16x16x64_bf16 v[192:195], v[200:203], v[160:167], v251\n\t" \
  "v_smfmac_f32_16x16x64_bf16 v[196:199], v[200:203], v[168:175], v251\n\t" \
  "s_waitcnt lgkmcnt(0)\n\t" \
  "v_smfmac_f32_16x16x64_bf16 v[192:195], v[204:207], v[176:183], v251\n\t" \
  "v_smfmac_f32_16x16x64_bf16 v[196:199], v[204:207], v[184:191], v251\n\t" \
  "s_nop 6\n\t" \
  "v_add_f32 v192, v192, v194\n\t" \
  "v_add_f32 v193, v193, v195\n\t" \
  "v_add_f32 v196, v196, v198\n\t" \
  "v_add_f32 v197, v197, v199\n\t" \
  "v_cvt_pk_bf16_f32 v192, v192, v193\n\t" \
  "v_cvt_pk_bf16_f32 v196, v196, v197\n\t" \
  "ds_write2_b32 v252, v192, v196 offset1:64\n\t" \
  "s_waitcnt lgkmcnt(0)\n\t" \
  "s_barrier\n\t" \
  "buffer_store_dword v192, v235, %[rs], %[sk] offen sc1\n\t" \
  "buffer_store_dword v196, v235, %[rs], %[sk] offen offset:256 sc1\n\t" \
  "s_mov_b32 %[spins], s92\n\t" \
  "s_branch .Lend%=\n\t" \
  ".Lslow%=:\n\t" \
  "s_mov_b64 s[94:95], 0\n\t" \
  "v_cmp_eq_u32 vcc, %[sent], v200\n\t" \
  "s_or_b64 s[94:95], s[94:95], vcc\n\t" \
  "v_cmp_eq_u32 vcc, %[sent], v201\n\t" \
  "s_or_b64 s[94:95], s[94:95], vcc\n\t" \
  "v_cmp_eq_u32 vcc, %[sent], v202\n\t" \
  "s_or_b64 s[94:95], s[94:95], vcc\n\t" \
  "v_cmp_eq_u32 vcc, %[sent], v203\n\t" \
  "s_or_b64 s[94:95], s[94:95], vcc\n\t" \
  "v_cmp_eq_u32 vcc, %[sent], v204\n\t" \
  "s_or_b64 s[94:95], s[94:95], vcc\n\t" \
  "v_cmp_eq_u32 vcc, %[sent], v205\n\t" \
  "s_or_b64 s[94:95], s[94:95], vcc\n\t" \
  "v_cmp_eq_u32 vcc, %[sent], v206\n\t" \
  "s_or_b64 s[94:95], s[94:95], vcc\n\t" \
  "v_cmp_eq_u32 vcc, %[sent], v207\n\t" \
  "s_or_b64 s[94:95], s[94:95], vcc\n\t" \
  "s_cmp_eq_u64 s[94:95], 0\n\t" \
  "s_cbranch_scc1 .Lok%=\n\t" \
  "v_mov_b64 v[192:193], 0\n\t" \
  "v_mov_b64 v[194:195], 0\n\t" \
  "v_mov_b64 v[196:197], 0\n\t" \
  "v_mov_b64 v[198:199], 0\n\t" \
  "s_add_u32 s92, s92, 1\n\t" \
  "s_sleep 6\n\t" \
  "s_cmp_eq_u32 s92, 1\n\t" \
  "s_cbranch_scc1 .Lpoll%=\n\t" \
  "s_sleep 1\n\t" \
  "s_and_b32 s93, s92, 63\n\t" \
  "s_cmp_lg_u32 s93, 0\n\t" \
  "s_cbranch_scc1 .Lpoll%=\n\t" \
  "v_mov_b32 v245, 0\n\t" \
  "buffer_load_dword v244, v245, %[rs], %[sab] offen sc1\n\t" \
  "s_waitcnt vmcnt(0)\n\t" \
  "v_readfirstlane_b32 s93, v244\n\t" \
  "s_nop 3\n\t" \
  "s_cmp_eq_u32 s93, 1\n\t" \
  "s_cbranch_scc1 .Lok%=\n\t" \
  "s_cmp_lt_u32 s92, 0x80000\n\t" \
  "s_cbranch_scc1 .Lpoll%=\n\t" \
  "v_mov_b32 v244, 1\n\t" \
  "buffer_store_dword v244, v245, %[rs], %[sab] offen sc1\n\t" \
  "s_branch .Lok%=\n\t" \
  ".Lend%=:\n\t"
#define QK_ASM_7 \
  "s_waitcnt lgkmcnt(0)\n\t" \
  "v_subrev_u32 v244, 0x8000, v250\n\t" \
  "ds_read_b128 v[216:219], v248 offset:61440\n\t" \
  "ds_read_b128 v[220:223], v248 offset:62464\n\t" \
  "ds_read_b128 v[224:227], v248 offset:63488\n\t" \
  "ds_read_b128 v[228:231], v248 offset:64512\n\t" \
  "ds_read_b128 v[208:211], v244 offset:0\n\t" \
  "ds_read_b128 v[212:215], v244 offset:1024\n\t" \
  "ds_read_b128 v[236:239], v244 offset:2048\n\t" \
  "ds_read_b128 v[240:243], v244 offset:3072\n\t" \
  "v_mov_b64 v[192:193], 0\n\t" \
  "v_mov_b64 v[194:195], 0\n\t" \
  "v_mov_b64 v[196:197], 0\n\t" \
  "v_mov_b64 v[198:199], 0\n\t" \
  "s_movk_i32 s92, 0x2000\n\t" \
  ".Lwt%=:\n\t" \
  "ds_read_b64 v[200:201], v248 offset:30720\n\t" \
  "s_memrealtime s[90:91]\n\t" \
  "s_sub_u32 s92, s92, 1\n\t" \
  "s_waitcnt lgkmcnt(0)\n\t" \
  "v_readfirstlane_b32 s93, v200\n\t" \
  "v_readfirstlane_b32 %[tgt], v201\n\t" \
  "s_cbranch_scc1 .Lgo%=\n\t" \
  "s_cmp_eq_u32 s93, %[step]\n\t" \
  "s_cbranch_scc1 .Lwx%=\n\t" \
  "s_sleep 1\n\t" \
  "s_branch .Lwt%=\n\t" \
  ".Lwx%=:\n\t" \
  "s_lshl_b32 s93, s90, 2\n\t" \
  "s_sub_i32 s93, %[tgt], s93\n\t" \
  "s_cmp_le_i32 s93, 0\n\t" \
  "s_cbranch_scc1 .Lgo%=\n\t" \
  "s_min_i32 s93, s93, 8000\n\t" \
  "s_add_u32 s93, s93, 3\n\t" \
  "s_lshr_b32 s93, s93, 2\n\t" \
  "s_add_u32 s93, s93, s90\n\t" \
  "s_memrealtime s[90:91]\n\t" \
  ".Lpace%=:\n\t" \
  "s_memrealtime s[94:95]\n\t" \
  "s_waitcnt lgkmcnt(1)\n\t" \
  "s_sub_i32 s91, s90, s93\n\t" \
  "s_cmp_ge_i32 s91, 0\n\t" \
  "s_cbranch_scc1 .Lgo%=\n\t" \
  "s_memrealtime s[90:91]\n\t" \
  "s_waitcnt lgkmcnt(1)\n\t" \
  "s_sub_i32 s95, s94, s93\n\t" \
  "s_cmp_lt_i32 s95, 0\n\t" \
  "s_cbranch_scc1 .Lpace%=\n\t" \
  ".Lgo%=:\n\t" \
  "s_mov_b32 s92, 0\n\t" \
  ".Lpoll%=:\n\t" \
  "buffer_load_dwordx4 v[200:203], v248, %[rs], %[som] offen sc1\n\t" \
  "buffer_load_dwordx4 v[204:207], v248, %[rs], %[som2] offen sc1\n\t" \
  "s_waitcnt vmcnt(1) lgkmcnt(0)\n\t" \
  "ds_write_b128 v249, v[200:203]\n\t" \
  "v_smfmac_f32_16x16x64_bf16 v[192:195], v[200:203], v[208:215], v251\n\t" \
  "v_max3_u32 v244, v200, v201, v202\n\t" \
  "v_smfmac_f32_16x16x64_bf16 v[196:199], v[200:203], v[236:243], v251\n\t" \
  "s_waitcnt vmcnt(0)\n\t" \
  "ds_write_b128 v249, v[204:207] offset:8192\n\t" \
  "v_smfmac_f32_16x16x64_bf16 v[192:195], v[204:207], v[216:223], v251\n\t" \
  "v_max3_u32 v244, v244, v203, v204\n\t" \
  "v_max3_u32 v244, v244, v205, v206\n\t" \
  "v_smfmac_f32_16x16x64_bf16 v[196:199], v[204:207], v[224:231], v251\n\t" \
  "v_max_u32 v244, v244, v207\n\t" \
  "v_cmp_le_u32 vcc, %[sent], v244\n\t" \
  "s_cbranch_vccnz .Lslow%=\n\t" \
  ".Lok%=:\n\t" \
  "ds_read_b128 v[216:219], v250 offset:32768\n\t" \
  "ds_read_b128 v[220:223], v250 offset:33792\n\t" \
  "ds_read_b128 v[224:227], v250 offset:34816\n\t" \
  "ds_read_b128 v[228:231], v250 offset:35840\n\t" \
  "s_waitcnt lgkmcnt(4)\n\t" \
  "s_setprio 3\n\t" \
  "s_barrier\n\t" \
  "s_memrealtime s[90:91]\n\t" \
  "v_mov_b32 v232, %[step]\n\t" \
  "v_lshrrev_b32 v234, 1, v248\n\t" \
  "s_mov_b64 exec, 0xffff\n\t" \
  "s_waitcnt lgkmcnt(0)\n\t" \
  "s_lshl_b32 s90, s90, 2\n\t" \
  "s_sub_i32 s93, s90, %[tgt]\n\t" \
  "s_cmp_gt_i32 s93, 270\n\t" \
  "s_cselect_b32 s93, 90, 0\n\t" \
  "s_add_u32 s93, s93, s90\n\t" \
  "s_add_u32 s93, s93, %[lead]\n\t" \
  "v_mov_b32 v233, s93\n\t" \
  "buffer_store_dwordx2 v[232:233], v234, %[rs], %[sbc] offen sc1\n\t" \
  "s_mov_b64 exec, -1\n\t" \
  "s_mov_b32 %[tb1], s90\n\t" \
  "s_setprio 0\n\t" \
  "ds_read_b128 v[208:211], v248 offset:6144\n\t" \
  "ds_read_b128 v[212:215], v248 offset:9216\n\t" \
  "ds_read_b128 v[236:239], v248 offset:10240\n\t" \
  "ds_read_b128 v[240:243], v248 offset:11264\n\t" \
  "s_waitcnt lgkmcnt(3)\n\t" \
  "ds_read_b128 v[244:247], v248 offset:12288\n\t" \
  "v_smfmac_f32_16x16x64_bf16 v[192:195], v[208:211], v[216:223], v251\n\t" \
  "ds_read_b128 v[216:219], v250 offset:0\n\t" \
  "v_smfmac_f32_16x16x64_bf16 v[196:199], v[208:211], v[224:231], v251\n\t" \
  "s_waitcnt lgkmcnt(4)\n\t" \
  "ds_read_b128 v[220:223], v250 offset:1024\n\t" \
  "v_smfmac_f32_16x16x64_bf16 v[192:195], v[212:215], v[0:7], v251\n\t" \
  "ds_read_b128 v[224:227], v250 offset:2048\n\t" \
  "v_smfmac_f32_16x16x64_bf16 v[196:199], v[212:215], v[8:15], v251\n\t" \
  "s_waitcnt lgkmcnt(5)\n\t" \
  "ds_read_b128 v[228:231], v250 offset:3072\n\t" \
  "v_smfmac_f32_16x16x64_bf16 v[192:195], v[236:239], v[16:23], v251\n\t" \
  "ds_read_b128 v[200:203], v248 offset:13312\n\t" \
  "v_smfmac_f32_16x16x64_bf16 v[196:199], v[236:239], v[24:31], v251\n\t" \
  "s_waitcnt lgkmcnt(6)\n\t" \
  "ds_read_b128 v[204:207], v248 offset:14336\n\t" \
  "v_smfmac_f32_16x16x64_bf16 v[192:195], v[240:243], v[32:39], v251\n\t" \
  "ds_read_b128 v[208:211], v248 offset:0\n\t" \
  "v_smfmac_f32_16x16x64_bf16 v[196:199], v[240:243], v[40:47], v251\n\t" \
  "s_waitcnt lgkmcnt(5)\n\t" \
  "ds_read_b128 v[212:215], v248 offset:1024\n\t" \
  "v_smfmac_f32_16x16x64_bf16 v[192:195], v[244:247], v[216:223], v251\n\t" \
  "s_waitcnt lgkmcnt(4)\n\t" \
  "v_smfmac_f32_16x16x64_bf16 v[196:199], v[244:247], v[224:231], v251\n\t" \
  "s_waitcnt lgkmcnt(3)\n\t" \
  "ds_read_b128 v[236:239], v248 offset:2048\n\t" \
  "v_smfmac_f32_16x16x64_bf16 v[192:195], v[200:203], v[48:55], v251\n\t" \
  "ds_read_b128 v[240:243], v248 offset:3072\n\t" \
  "v_smfmac_f32_16x16x64_bf16 v[196:199], v[200:203], v[56:63], v251\n\t" \
  "s_waitcnt lgkmcnt(4)\n\t" \
  "v_smfmac_f32_16x16x64_bf16 v[192:195], v[204:207], v[64:71], v251\n\t" \
  "ds_read_b128 v[244:247], v248 offset:4096\n\t" \
  "v_smfmac_f32_16x16x64_bf16 v[196:199], v[204:207], v[72:79], v251\n\t" \
  "s_waitcnt lgkmcnt(4)\n\t" \
  "v_smfmac_f32_16x16x64_bf16 v[192:195], v[208:211], v[80:87], v251\n\t" \
  "ds_read_b128 v[200:203], v248 offset:5120\n\t" \
  "v_smfmac_f32_16x16x64_bf16 v[196:199], v[208:211], v[88:95], v251\n\t" \
  "s_waitcnt lgkmcnt(4)\n\t" \
  "v_smfmac_f32_16x16x64_bf16 v[192:195], v[212:215], v[96:103], v251\n\t" \
  "ds_read_b128 v[204:207], v248 offset:8192\n\t" \
  "v_smfmac_f32_16x16x64_bf16 v[196:199], v[212:215], v[104:111], v251\n\t" \
  "s_waitcnt lgkmcnt(3)\n\t" \
  "v_smfmac_f32_16x16x64_bf16 v[192:195], v[236:239], v[112:119], v251\n\t" \
  "v_smfmac_f32_16x16x64_bf16 v[196:199], v[236:239], v[120:127], v251\n\t" \
  "v_smfmac_f32_16x16x64_bf16 v[192:195], v[240:243], v[128:135], v251\n\t" \
  "v_smfmac_f32_16x16x64_bf16 v[196:199], v[240:243], v[136:143], v251\n\t" \
  "s_waitcnt lgkmcnt(1)\n\t" \
  "v_smfmac_f32_16x16x64_bf16 v[192:195], v[244:247], v[144:151], v251\n\t" \
  "v_smfmac_f32_16x16x64_bf16 v[196:199], v[244:247], v[152:159], v251\n\t" \
  "v_smfmac_f32_16x16x64_bf16 v[192:195], v[200:203], v[160:167], v251\n\t" \
  "v_smfmac_f32_16x16x64_bf16 v[196:199], v[200:203], v[168:175], v251\n\t" \
  "s_waitcnt lgkmcnt(0)\n\t" \
  "v_smfmac_f32_16x16x64_bf16 v[192:195], v[204:207], v[176:183], v251\n\t" \
  "v_smfmac_f32_16x16x64_bf16 v[196:199], v[204:207], v[184:191], v251\n\t" \
  "s_nop 6\n\t" \
  "v_add_f32 v192, v192, v194\n\t" \
  "v_add_f32 v193, v193, v195\n\t" \
  "v_add_f32 v196, v196, v198\n\t" \
  "v_add_f32 v197, v197, v199\n\t" \
  "v_cvt_pk_bf16_f32 v192, v192, v193\n\t" \
  "v_cvt_pk_bf16_f32 v196, v196, v197\n\t" \
  "ds_write2_b32 v252, v192, v196 offset1:64\n\t" \
  "s_waitcnt lgkmcnt(0)\n\t" \
  "s_barrier\n\t" \
  "buffer_store_dword v192, v235, %[rs], %[sk] offen sc1\n\t" \
  "buffer_store_dword v196, v235, %[rs], %[sk] offen offset:256 sc1\n\t" \
  "s_mov_b32 %[spins], s92\n\t" \
  "s_branch .Lend%=\n\t" \
  ".Lslow%=:\n\t" \
  "s_mov_b64 s[94:95], 0\n\t" \
  "v_cmp_eq_u32 vcc, %[sent], v200\n\t" \
  "s_or_b64 s[94:95], s[94:95], vcc\n\t" \
  "v_cmp_eq_u32 vcc, %[sent], v201\n\t" \
  "s_or_b64 s[94:95], s[94:95], vcc\n\t" \
  "v_cmp_eq_u32 vcc, %[sent], v202\n\t" \
  "s_or_b64 s[94:95], s[94:95], vcc\n\t" \
  "v_cmp_eq_u32 vcc, %[sent], v203\n\t" \
  "s_or_b64 s[94:95], s[94:95], vcc\n\t" \
  "v_cmp_eq_u32 vcc, %[sent], v204\n\t" \
  "s_or_b64 s[94:95], s[94:95], vcc\n\t" \
  "v_cmp_eq_u32 vcc, %[sent], v205\n\t" \
  "s_or_b64 s[94:95], s[94:95], vcc\n\t" \
  "v_cmp_eq_u32 vcc, %[sent], v206\n\t" \
  "s_or_b64 s[94:95], s[94:95], vcc\n\t" \
  "v_cmp_eq_u32 vcc, %[sent], v207\n\t" \
  "s_or_b64 s[94:95], s[94:95], vcc\n\t" \
  "s_cmp_eq_u64 s[94:95], 0\n\t" \
  "s_cbranch_scc1 .Lok%=\n\t" \
  "v_mov_b64 v[192:193], 0\n\t" \
  "v_mov_b64 v[194:195], 0\n\t" \
  "v_mov_b64 v[196:197], 0\n\t" \
  "v_mov_b64 v[198:199], 0\n\t" \
  "s_add_u32 s92, s92, 1\n\t" \
  "s_sleep 6\n\t" \
  "s_cmp_eq_u32 s92, 1\n\t" \
  "s_cbranch_scc1 .Lpoll%=\n\t" \
  "s_sleep 1\n\t" \
  "s_and_b32 s93, s92, 63\n\t" \
  "s_cmp_lg_u32 s93, 0\n\t" \
  "s_cbranch_scc1 .Lpoll%=\n\t" \
  "v_mov_b32 v245, 0\n\t" \
  "buffer_load_dword v244, v245, %[rs], %[sab] offen sc1\n\t" \
  "s_waitcnt vmcnt(0)\n\t" \
  "v_readfirstlane_b32 s93, v244\n\t" \
  "s_nop 3\n\t" \
  "s_cmp_eq_u32 s93, 1\n\t" \
  "s_cbranch_scc1 .Lok%=\n\t" \
  "s_cmp_lt_u32 s92, 0x80000\n\t" \
  "s_cbranch_scc1 .Lpoll%=\n\t" \
  "v_mov_b32 v244, 1\n\t" \
  "buffer_store_dword v244, v245, %[rs], %[sab] offen sc1\n\t" \
  "s_branch .Lok%=\n\t" \
  ".Lend%=:\n\t"
#define VV_ASM \
  "s_waitcnt lgkmcnt(0)\n\t" \
  "v_mov_b64 v[128:129], 0\n\t" \
  "v_mov_b64 v[130:131], 0\n\t" \
  "s_movk_i32 s92, 0x2000\n\t" \
  ".Lwt%=:\n\t" \
  "ds_read_b64 v[188:189], v200 offset:30720\n\t" \
  "s_memrealtime s[90:91]\n\t" \
  "s_sub_u32 s92, s92, 1\n\t" \
  "s_waitcnt lgkmcnt(0)\n\t" \
  "v_readfirstlane_b32 s93, v188\n\t" \
  "v_readfirstlane_b32 %[tgt], v189\n\t" \
  "s_cbranch_scc1 .Lgo%=\n\t" \
  "s_cmp_eq_u32 s93, %[step]\n\t" \
  "s_cbranch_scc1 .Lwx%=\n\t" \
  "s_sleep 1\n\t" \
  "s_branch .Lwt%=\n\t" \
  ".Lwx%=:\n\t" \
  "s_lshl_b32 s93, s90, 2\n\t" \
  "s_sub_i32 s93, %[tgt], s93\n\t" \
  "s_cmp_le_i32 s93, 0\n\t" \
  "s_cbranch_scc1 .Lgo%=\n\t" \
  "s_min_i32 s93, s93, 8000\n\t" \
  "s_add_u32 s93, s93, 3\n\t" \
  "s_lshr_b32 s93, s93, 2\n\t" \
  "s_add_u32 s93, s93, s90\n\t" \
  "s_memrealtime s[90:91]\n\t" \
  ".Lpace%=:\n\t" \
  "s_memrealtime s[94:95]\n\t" \
  "s_waitcnt lgkmcnt(1)\n\t" \
  "s_sub_i32 s91, s90, s93\n\t" \
  "s_cmp_ge_i32 s91, 0\n\t" \
  "s_cbranch_scc1 .Lgo%=\n\t" \
  "s_memrealtime s[90:91]\n\t" \
  "s_waitcnt lgkmcnt(1)\n\t" \
  "s_sub_i32 s95, s94, s93\n\t" \
  "s_cmp_lt_i32 s95, 0\n\t" \
  "s_cbranch_scc1 .Lpace%=\n\t" \
  ".Lgo%=:\n\t" \
  "s_mov_b32 s92, 0\n\t" \
  ".Lpoll%=:\n\t" \
  "buffer_load_dwordx4 v[188:191], v200, %[rs], %[som] offen sc1\n\t" \
  "buffer_load_dwordx4 v[192:195], v200, %[rs], %[som] offen offset:1024 sc1\n\t" \
  "s_waitcnt vmcnt(1) lgkmcnt(0)\n\t" \
  "ds_write_b128 v201, v[188:191]\n\t" \
  "v_max3_u32 v196, v188, v189, v190\n\t" \
  "s_waitcnt vmcnt(0)\n\t" \
  "ds_write_b128 v201, v[192:195] offset:1024\n\t" \
  "v_max3_u32 v196, v196, v191, v192\n\t" \
  "v_max3_u32 v196, v196, v193, v194\n\t" \
  "v_max_u32 v196, v196, v195\n\t" \
  "v_cmp_le_u32 vcc, %[sent], v196\n\t" \
  "s_cbranch_vccnz .Lslow%=\n\t" \
  ".Lok%=:\n\t" \
  "s_waitcnt lgkmcnt(0)\n\t" \
  "s_barrier\n\t" \
  "ds_read_b128 v[132:135], v200 offset:0\n\t" \
  "ds_read_b128 v[136:139], v200 offset:1024\n\t" \
  "ds_read_b128 v[140:143], v200 offset:2048\n\t" \
  "ds_read_b128 v[144:147], v200 offset:3072\n\t" \
  "ds_read_b128 v[148:151], v200 offset:4096\n\t" \
  "s_waitcnt lgkmcnt(4)\n\t" \
  "ds_read_b128 v[152:155], v200 offset:5120\n\t" \
  "v_smfmac_f32_16x16x64_bf16 v[128:131], v[132:135], v[0:7], v202\n\t" \
  "s_waitcnt lgkmcnt(4)\n\t" \
  "ds_read_b128 v[156:159], v200 offset:6144\n\t" \
  "v_smfmac_f32_16x16x64_bf16 v[128:131], v[136:139], v[8:15], v202\n\t" \
  "s_waitcnt lgkmcnt(4)\n\t" \
  "ds_read_b128 v[160:163], v200 offset:7168\n\t" \
  "v_smfmac_f32_16x16x64_bf16 v[128:131], v[140:143], v[16:23], v202\n\t" \
  "s_waitcnt lgkmcnt(4)\n\t" \
  "ds_read_b128 v[164:167], v200 offset:8192\n\t" \
  "v_smfmac_f32_16x16x64_bf16 v[128:131], v[144:147], v[24:31], v202\n\t" \
  "s_waitcnt lgkmcnt(4)\n\t" \
  "ds_read_b128 v[168:171], v200 offset:9216\n\t" \
  "v_smfmac_f32_16x16x64_bf16 v[128:131], v[148:151], v[32:39], v202\n\t" \
  "s_waitcnt lgkmcnt(4)\n\t" \
  "ds_read_b128 v[172:175], v200 offset:10240\n\t" \
  "v_smfmac_f32_16x16x64_bf16 v[128:131], v[152:155], v[40:47], v202\n\t" \
  "s_waitcnt lgkmcnt(4)\n\t" \
  "ds_read_b128 v[176:179], v200 offset:11264\n\t" \
  "v_smfmac_f32_16x16x64_bf16 v[128:131], v[156:159], v[48:55], v202\n\t" \
  "s_waitcnt lgkmcnt(4)\n\t" \
  "ds_read_b128 v[180:183], v200 offset:12288\n\t" \
  "v_smfmac_f32_16x16x64_bf16 v[128:131], v[160:163], v[56:63], v202\n\t" \
  "s_waitcnt lgkmcnt(4)\n\t" \
  "ds_read_b128 v[184:187], v200 offset:13312\n\t" \
  "v_smfmac_f32_16x16x64_bf16 v[128:131], v[164:167], v[64:71], v202\n\t" \
  "s_waitcnt lgkmcnt(4)\n\t" \
  "ds_read_b128 v[188:191], v200 offset:14336\n\t" \
  "v_smfmac_f32_16x16x64_bf16 v[128:131], v[168:171], v[72:79], v202\n\t" \
  "s_waitcnt lgkmcnt(4)\n\t" \
  "ds_read_b128 v[192:195], v200 offset:15360\n\t" \
  "v_smfmac_f32_16x16x64_bf16 v[128:131], v[172:175], v[80:87], v202\n\t" \
  "s_waitcnt lgkmcnt(4)\n\t" \
  "v_smfmac_f32_16x16x64_bf16 v[128:131], v[176:179], v[88:95], v202\n\t" \
  "s_waitcnt lgkmcnt(3)\n\t" \
  "v_smfmac_f32_16x16x64_bf16 v[128:131], v[180:183], v[96:103], v202\n\t" \
  "s_waitcnt lgkmcnt(2)\n\t" \
  "v_smfmac_f32_16x16x64_bf16 v[128:131], v[184:187], v[104:111], v202\n\t" \
  "s_waitcnt lgkmcnt(1)\n\t" \
  "v_smfmac_f32_16x16x64_bf16 v[128:131], v[188:191], v[112:119], v202\n\t" \
  "s_waitcnt lgkmcnt(0)\n\t" \
  "v_smfmac_f32_16x16x64_bf16 v[128:131], v[192:195], v[120:127], v202\n\t" \
  "s_nop 7\n\t" \
  "v_add_f32 v128, v128, v130\n\t" \
  "v_add_f32 v129, v129, v131\n\t" \
  "v_cvt_pk_bf16_f32 v128, v128, v129\n\t" \
  "buffer_store_dword v128, v203, %[rs], %[sv] offen sc1\n\t" \
  "ds_write_b32 v204, v128\n\t" \
  "s_waitcnt lgkmcnt(0)\n\t" \
  "s_barrier\n\t" \
  "s_mov_b32 %[spins], s92\n\t" \
  "s_branch .Lend%=\n\t" \
  ".Lslow%=:\n\t" \
  "s_mov_b64 s[94:95], 0\n\t" \
  "v_cmp_eq_u32 vcc, %[sent], v188\n\t" \
  "s_or_b64 s[94:95], s[94:95], vcc\n\t" \
  "v_cmp_eq_u32 vcc, %[sent], v189\n\t" \
  "s_or_b64 s[94:95], s[94:95], vcc\n\t" \
  "v_cmp_eq_u32 vcc, %[sent], v190\n\t" \
  "s_or_b64 s[94:95], s[94:95], vcc\n\t" \
  "v_cmp_eq_u32 vcc, %[sent], v191\n\t" \
  "s_or_b64 s[94:95], s[94:95], vcc\n\t" \
  "v_cmp_eq_u32 vcc, %[sent], v192\n\t" \
  "s_or_b64 s[94:95], s[94:95], vcc\n\t" \
  "v_cmp_eq_u32 vcc, %[sent], v193\n\t" \
  "s_or_b64 s[94:95], s[94:95], vcc\n\t" \
  "v_cmp_eq_u32 vcc, %[sent], v194\n\t" \
  "s_or_b64 s[94:95], s[94:95], vcc\n\t" \
  "v_cmp_eq_u32 vcc, %[sent], v195\n\t" \
  "s_or_b64 s[94:95], s[94:95], vcc\n\t" \
  "s_cmp_eq_u64 s[94:95], 0\n\t" \
  "s_cbranch_scc1 .Lok%=\n\t" \
  "s_add_u32 s92, s92, 1\n\t" \
  "s_sleep 6\n\t" \
  "s_cmp_eq_u32 s92, 1\n\t" \
  "s_cbranch_scc1 .Lpoll%=\n\t" \
  "s_sleep 1\n\t" \
  "s_and_b32 s93, s92, 63\n\t" \
  "s_cmp_lg_u32 s93, 0\n\t" \
  "s_cbranch_scc1 .Lpoll%=\n\t" \
  "v_mov_b32 v197, 0\n\t" \
  "buffer_load_dword v196, v197, %[rs], %[sab] offen sc1\n\t" \
  "s_waitcnt vmcnt(0)\n\t" \
  "v_readfirstlane_b32 s93, v196\n\t" \
  "s_nop 3\n\t" \
  "s_cmp_eq_u32 s93, 1\n\t" \
  "s_cbranch_scc1 .Lok%=\n\t" \
  "s_cmp_lt_u32 s92, 0x80000\n\t" \
  "s_cbranch_scc1 .Lpoll%=\n\t" \
  "v_mov_b32 v196, 1\n\t" \
  "buffer_store_dword v196, v197, %[rs], %[sab] offen sc1\n\t" \
  "s_branch .Lok%=\n\t" \
  ".Lend%=:\n\t"
#define TK_ASM \
  "buffer_load_dwordx4 v[224:227], v200, %[rs], %[sk] offen offset:0 sc1\n\t" \
  "buffer_load_dwordx4 v[228:231], v200, %[rs], %[sk] offen offset:1024 sc1\n\t" \
  "s_memrealtime s[88:89]\n\t" \
  "v_add_u32 v205, %[tl], v200\n\t" \
  "v_add_u32 v206, %[tq], v205\n\t" \
  "v_add_u32 v207, %[tv], v205\n\t" \
  "s_waitcnt lgkmcnt(0)\n\t" \
  "ds_read_b128 v[128:131], v206 offset:8192\n\t" \
  "ds_read_b128 v[144:147], v205 offset:0\n\t" \
  "ds_read_b128 v[160:163], v205 offset:4096\n\t" \
  "ds_read_b128 v[132:135], v206 offset:9216\n\t" \
  "ds_read_b128 v[148:151], v205 offset:1024\n\t" \
  "ds_read_b128 v[164:167], v205 offset:5120\n\t" \
  "ds_read_b128 v[136:139], v206 offset:10240\n\t" \
  "ds_read_b128 v[152:155], v205 offset:2048\n\t" \
  "ds_read_b128 v[168:171], v205 offset:6144\n\t" \
  "ds_read_b128 v[140:143], v206 offset:11264\n\t" \
  "ds_read_b128 v[156:159], v205 offset:3072\n\t" \
  "ds_read_b128 v[172:175], v205 offset:7168\n\t" \
  "v_add_u32 v206, %[vp], v218\n\t" \
  "s_lshl_b32 s88, s88, 2\n\t" \
  "s_add_u32 s89, s88, %[od]\n\t" \
  "s_waitcnt lgkmcnt(10)\n\t" \
  "v_mfma_f32_16x16x32_bf16 v[196:199], v[144:147], v[128:131], 0\n\t" \
  "s_waitcnt lgkmcnt(9)\n\t" \
  "v_mfma_f32_16x16x32_bf16 v[144:147], v[160:163], v[128:131], 0\n\t" \
  "s_waitcnt lgkmcnt(7)\n\t" \
  "v_mfma_f32_16x16x32_bf16 v[196:199], v[148:151], v[132:135], v[196:199]\n\t" \
  "s_waitcnt lgkmcnt(6)\n\t" \
  "v_mfma_f32_16x16x32_bf16 v[144:147], v[164:167], v[132:135], v[144:147]\n\t" \
  "s_waitcnt lgkmcnt(4)\n\t" \
  "v_mfma_f32_16x16x32_bf16 v[196:199], v[152:155], v[136:139], v[196:199]\n\t" \
  "s_waitcnt lgkmcnt(3)\n\t" \
  "v_mfma_f32_16x16x32_bf16 v[144:147], v[168:171], v[136:139], v[144:147]\n\t" \
  "s_waitcnt lgkmcnt(1)\n\t" \
  "v_mfma_f32_16x16x32_bf16 v[196:199], v[156:159], v[140:143], v[196:199]\n\t" \
  "s_waitcnt lgkmcnt(0)\n\t" \
  "v_mfma_f32_16x16x32_bf16 v[144:147], v[172:175], v[140:143], v[144:147]\n\t" \
  "ds_read_b128 v[176:179], v207 offset:16384\n\t" \
  "ds_read_b128 v[180:183], v207 offset:17408\n\t" \
  "ds_read_b128 v[184:187], v207 offset:18432\n\t" \
  "ds_read_b128 v[188:191], v207 offset:19456\n\t" \
  "ds_read2st64_b32 v[192:193], v206 offset0:0 offset1:1\n\t" \
  "ds_read2st64_b32 v[194:195], v206 offset0:2 offset1:3\n\t" \
  "v_cvt_pk_bf16_f32 v197, v196, v197\n\t" \
  "v_cvt_pk_bf16_f32 v199, v198, v199\n\t" \
  "v_cvt_pk_bf16_f32 v145, v144, v145\n\t" \
  "v_cvt_pk_bf16_f32 v147, v146, v147\n\t" \
  "v_lshlrev_b32 v196, 16, v197\n\t" \
  "v_lshlrev_b32 v198, 16, v199\n\t" \
  "v_lshlrev_b32 v144, 16, v145\n\t" \
  "v_lshlrev_b32 v146, 16, v147\n\t" \
  "v_and_b32 v197, 0xffff0000, v197\n\t" \
  "v_and_b32 v199, 0xffff0000, v199\n\t" \
  "v_and_b32 v145, 0xffff0000, v145\n\t" \
  "v_and_b32 v147, 0xffff0000, v147\n\t" \
  "v_pk_mul_f32 v[196:197], v[196:197], %[sc] op_sel_hi:[1,0]\n\t" \
  "v_pk_mul_f32 v[198:199], v[198:199], %[sc] op_sel_hi:[1,0]\n\t" \
  "v_pk_mul_f32 v[144:145], v[144:145], %[sc] op_sel_hi:[1,0]\n\t" \
  "v_pk_mul_f32 v[146:147], v[146:147], %[sc] op_sel_hi:[1,0]\n\t" \
  "v_cvt_pk_bf16_f32 v197, v196, v197\n\t" \
  "v_cvt_pk_bf16_f32 v199, v198, v199\n\t" \
  "v_cvt_pk_bf16_f32 v145, v144, v145\n\t" \
  "v_cvt_pk_bf16_f32 v147, v146, v147\n\t" \
  "v_lshlrev_b32 v196, 16, v197\n\t" \
  "v_lshlrev_b32 v198, 16, v199\n\t" \
  "v_lshlrev_b32 v144, 16, v145\n\t" \
  "v_lshlrev_b32 v146, 16, v147\n\t" \
  "v_and_b32 v197, 0xffff0000, v197\n\t" \
  "v_and_b32 v199, 0xffff0000, v199\n\t" \
  "v_and_b32 v145, 0xffff0000, v145\n\t" \
  "v_and_b32 v147, 0xffff0000, v147\n\t" \
  "v_min_f32 v196, v196, v208\n\t" \
  "v_min_f32 v197, v197, v209\n\t" \
  "v_min_f32 v198, v198, v210\n\t" \
  "v_min_f32 v199, v199, v211\n\t" \
  "v_min_f32 v144, v144, v212\n\t" \
  "v_min_f32 v145, v145, v213\n\t" \
  "v_min_f32 v146, v146, v214\n\t" \
  "v_min_f32 v147, v147, v215\n\t" \
  "v_max3_f32 v148, v196, v197, v198\n\t" \
  "v_max3_f32 v149, v199, v144, v145\n\t" \
  "v_max3_f32 v148, v148, v146, v147\n\t" \
  "v_max_f32 v150, v148, v149\n\t" \
  "v_max_f32 v151, v148, v149\n\t" \
  "s_nop 1\n\t" \
  "v_permlane32_swap_b32 v150, v151\n\t" \
  "v_max_f32 v150, v150, v151\n\t" \
  "s_mov_b32 s92, 0\n\t" \
  ".Ltake%=:\n\t" \
  "s_waitcnt vmcnt(1) lgkmcnt(0)\n\t" \
  "ds_write_b128 v216, v[224:227]\n\t" \
  "v_max3_u32 v148, v224, v225, v226\n\t" \
  "s_waitcnt vmcnt(0)\n\t" \
  "ds_write_b128 v216, v[228:231] offset:1024\n\t" \
  "ds_read_b64_tr_b16 v[160:161], v217 offset:0\n\t" \
  "ds_read_b64_tr_b16 v[162:163], v217 offset:64\n\t" \
  "ds_read_b64_tr_b16 v[164:165], v217 offset:512\n\t" \
  "ds_read_b64_tr_b16 v[166:167], v217 offset:576\n\t" \
  "ds_read_b64_tr_b16 v[168:169], v217 offset:1024\n\t" \
  "ds_read_b64_tr_b16 v[170:171], v217 offset:1088\n\t" \
  "ds_read_b64_tr_b16 v[172:173], v217 offset:1536\n\t" \
  "ds_read_b64_tr_b16 v[174:175], v217 offset:1600\n\t" \
  "v_max3_u32 v148, v148, v227, v228\n\t" \
  "v_max3_u32 v148, v148, v229, v230\n\t" \
  "v_max_u32 v148, v148, v231\n\t" \
  "v_cmp_le_u32 vcc, %[sent], v148\n\t" \
  "s_cbranch_vccnz .Lslow%=\n\t" \
  ".Lok%=:\n\t" \
  "s_waitcnt lgkmcnt(6)\n\t" \
  "v_mfma_f32_16x16x32_bf16 v[152:155], v[160:163], v[128:131], 0\n\t" \
  "s_waitcnt lgkmcnt(4)\n\t" \
  "v_mfma_f32_16x16x32_bf16 v[152:155], v[164:167], v[132:135], v[152:155]\n\t" \
  "s_waitcnt lgkmcnt(2)\n\t" \
  "v_mfma_f32_16x16x32_bf16 v[152:155], v[168:171], v[136:139], v[152:155]\n\t" \
  "s_waitcnt lgkmcnt(0)\n\t" \
  "v_mfma_f32_16x16x32_bf16 v[152:155], v[172:175], v[140:143], v[152:155]\n\t" \
  "v_mov_b32 v157, 0\n\t" \
  "v_mov_b32 v158, 0\n\t" \
  "s_nop 5\n\t" \
  "v_cvt_pk_bf16_f32 v153, v152, v153\n\t" \
  "v_cvt_pk_bf16_f32 v155, v154, v155\n\t" \
  "v_lshlrev_b32 v152, 16, v153\n\t" \
  "v_lshlrev_b32 v154, 16, v155\n\t" \
  "v_and_b32 v153, 0xffff0000, v153\n\t" \
  "v_and_b32 v155, 0xffff0000, v155\n\t" \
  "v_pk_mul_f32 v[152:153], v[152:153], %[sc] op_sel_hi:[1,0]\n\t" \
  "v_pk_mul_f32 v[154:155], v[154:155], %[sc] op_sel_hi:[1,0]\n\t" \
  "v_cvt_pk_bf16_f32 v153, v152, v153\n\t" \
  "v_cvt_pk_bf16_f32 v155, v154, v155\n\t" \
  "v_lshlrev_b32 v152, 16, v153\n\t" \
  "v_lshlrev_b32 v154, 16, v155\n\t" \
  "v_and_b32 v153, 0xffff0000, v153\n\t" \
  "v_and_b32 v155, 0xffff0000, v155\n\t" \
  "v_max3_f32 v148, v152, v153, v150\n\t" \
  "v_max3_f32 v149, v148, v154, v155\n\t" \
  "v_max3_f32 v150, v148, v154, v155\n\t" \
  "v_cndmask_b32 v152, v152, v154, %[up]\n\t" \
  "v_cndmask_b32 v153, v153, v155, %[up]\n\t" \
  "v_permlane16_swap_b32 v149, v150\n\t" \
  "v_max_f32 v148, v149, v150\n\t" \
  "v_mul_f32 v148, %[negc], v148\n\t" \
  "v_fma_f32 v152, v152, %[c], v148\n\t" \
  "v_fma_f32 v153, v153, %[c], v148\n\t" \
  "v_exp_f32 v152, v152\n\t" \
  "v_fma_f32 v196, v196, %[c], v148\n\t" \
  "v_exp_f32 v153, v153\n\t" \
  "v_fma_f32 v197, v197, %[c], v148\n\t" \
  "v_exp_f32 v196, v196\n\t" \
  "v_fma_f32 v198, v198, %[c], v148\n\t" \
  "v_exp_f32 v197, v197\n\t" \
  "v_fma_f32 v199, v199, %[c], v148\n\t" \
  "v_exp_f32 v198, v198\n\t" \
  "v_fma_f32 v144, v144, %[c], v148\n\t" \
  "v_pk_add_f32 v[132:133], v[152:153], v[196:197]\n\t" \
  "v_exp_f32 v199, v199\n\t" \
  "v_fma_f32 v145, v145, %[c], v148\n\t" \
  "v_exp_f32 v144, v144\n\t" \
  "v_fma_f32 v146, v146, %[c], v148\n\t" \
  "v_pk_add_f32 v[132:133], v[132:133], v[198:199]\n\t" \
  "v_exp_f32 v145, v145\n\t" \
  "v_fma_f32 v147, v147, %[c], v148\n\t" \
  "v_exp_f32 v146, v146\n\t" \
  "v_pk_add_f32 v[132:133], v[132:133], v[144:145]\n\t" \
  "v_exp_f32 v147, v147\n\t" \
  "s_nop 0\n\t" \
  "v_pk_add_f32 v[132:133], v[132:133], v[146:147]\n\t" \
  "v_add_f32 v134, v132, v133\n\t" \
  "v_add_f32 v135, v132, v133\n\t" \
  "s_nop 1\n\t" \
  "v_permlane16_swap_b32 v134, v135\n\t" \
  "v_add_f32 v132, v134, v135\n\t" \
  "v_add_f32 v133, v134, v135\n\t" \
  "s_nop 1\n\t" \
  "v_permlane32_swap_b32 v132, v133\n\t" \
  "v_add_f32 v132, v132, v133\n\t" \
  "v_rcp_f32 v148, v132\n\t" \
  "s_nop 0\n\t" \
  "v_pk_mul_f32 v[152:153], v[152:153], v[148:149] op_sel_hi:[1,0]\n\t" \
  "v_pk_mul_f32 v[196:197], v[196:197], v[148:149] op_sel_hi:[1,0]\n\t" \
  "v_pk_mul_f32 v[198:199], v[198:199], v[148:149] op_sel_hi:[1,0]\n\t" \
  "v_pk_mul_f32 v[144:145], v[144:145], v[148:149] op_sel_hi:[1,0]\n\t" \
  "v_pk_mul_f32 v[146:147], v[146:147], v[148:149] op_sel_hi:[1,0]\n\t" \
  "v_cvt_pk_bf16_f32 v156, v152, v153\n\t" \
  "v_cvt_pk_bf16_f32 v159, v152, v153\n\t" \
  "v_cvt_pk_bf16_f32 v128, v196, v197\n\t" \
  "v_cvt_pk_bf16_f32 v129, v198, v199\n\t" \
  "v_mfma_f32_16x16x16_bf16 v[160:163], v[192:193], v[156:157], 0\n\t" \
  "v_cvt_pk_bf16_f32 v130, v144, v145\n\t" \
  "v_mfma_f32_16x16x16_bf16 v[164:167], v[192:193], v[158:159], 0\n\t" \
  "v_cvt_pk_bf16_f32 v131, v146, v147\n\t" \
  "v_mfma_f32_16x16x16_bf16 v[168:171], v[194:195], v[156:157], 0\n\t" \
  "v_mfma_f32_16x16x16_bf16 v[172:175], v[194:195], v[158:159], 0\n\t" \
  "s_nop 0\n\t" \
  "v_mfma_f32_16x16x32_bf16 v[160:163], v[176:179], v[128:131], v[160:163]\n\t" \
  "s_nop 0\n\t" \
  "v_mfma_f32_16x16x32_bf16 v[164:167], v[180:183], v[128:131], v[164:167]\n\t" \
  "s_nop 0\n\t" \
  "v_mfma_f32_16x16x32_bf16 v[168:171], v[184:187], v[128:131], v[168:171]\n\t" \
  "v_mfma_f32_16x16x32_bf16 v[172:175], v[188:191], v[128:131], v[172:175]\n\t" \
  "s_nop 2\n\t" \
  "v_cvt_pk_bf16_f32 v136, v160, v161\n\t" \
  "v_cvt_pk_bf16_f32 v137, v162, v163\n\t" \
  "v_cvt_pk_bf16_f32 v138, v164, v165\n\t" \
  "v_cvt_pk_bf16_f32 v139, v166, v167\n\t" \
  "v_cvt_pk_bf16_f32 v140, v168, v169\n\t" \
  "v_cvt_pk_bf16_f32 v141, v170, v171\n\t" \
  "v_cvt_pk_bf16_f32 v142, v172, v173\n\t" \
  "v_cvt_pk_bf16_f32 v143, v174, v175\n\t" \
  "v_permlane32_swap_b32 v136, v138\n\t" \
  "v_permlane32_swap_b32 v137, v139\n\t" \
  "v_permlane32_swap_b32 v140, v142\n\t" \
  "v_permlane32_swap_b32 v141, v143\n\t" \
  "v_permlane16_swap_b32 v136, v138\n\t" \
  "v_permlane16_swap_b32 v137, v139\n\t" \
  "v_permlane16_swap_b32 v140, v142\n\t" \
  "v_permlane16_swap_b32 v141, v143\n\t" \
  "s_memrealtime s[90:91]\n\t" \
  "s_waitcnt lgkmcnt(0)\n\t" \
  "s_lshl_b32 s93, s90, 2\n\t" \
  "s_sub_i32 s93, s89, s93\n\t" \
  "s_cmp_le_i32 s93, 0\n\t" \
  "s_cbranch_scc1 .Lgoo%=\n\t" \
  "s_min_i32 s93, s93, 8000\n\t" \
  "s_add_u32 s93, s93, 3\n\t" \
  "s_lshr_b32 s93, s93, 2\n\t" \
  "s_add_u32 s93, s93, s90\n\t" \
  "s_memrealtime s[90:91]\n\t" \
  ".Lpaceo%=:\n\t" \
  "s_memrealtime s[94:95]\n\t" \
  "s_waitcnt lgkmcnt(1)\n\t" \
  "s_sub_i32 s91, s90, s93\n\t" \
  "s_cmp_ge_i32 s91, 0\n\t" \
  "s_cbranch_scc1 .Lgoo%=\n\t" \
  "s_memrealtime s[90:91]\n\t" \
  "s_waitcnt lgkmcnt(1)\n\t" \
  "s_sub_i32 s95, s94, s93\n\t" \
  "s_cmp_lt_i32 s95, 0\n\t" \
  "s_cbranch_scc1 .Lpaceo%=\n\t" \
  ".Lgoo%=:\n\t" \
  "buffer_store_dwordx4 v[136:139], v200, %[rt], %[so] offen offset:0 sc1\n\t" \
  "buffer_store_dwordx4 v[140:143], v200, %[rt], %[so] offen offset:1024 sc1\n\t" \
  "s_waitcnt lgkmcnt(0)\n\t" \
  "s_branch .Lend%=\n\t" \
  ".Lpoll%=:\n\t" \
  "buffer_load_dwordx4 v[224:227], v200, %[rs], %[sk] offen offset:0 sc1\n\t" \
  "buffer_load_dwordx4 v[228:231], v200, %[rs], %[sk] offen offset:1024 sc1\n\t" \
  "s_branch .Ltake%=\n\t" \
  "s_branch .Lend%=\n\t" \
  ".Lslow%=:\n\t" \
  "s_mov_b64 s[94:95], 0\n\t" \
  "v_cmp_eq_u32 vcc, %[sent], v224\n\t" \
  "s_or_b64 s[94:95], s[94:95], vcc\n\t" \
  "v_cmp_eq_u32 vcc, %[sent], v225\n\t" \
  "s_or_b64 s[94:95], s[94:95], vcc\n\t" \
  "v_cmp_eq_u32 vcc, %[sent], v226\n\t" \
  "s_or_b64 s[94:95], s[94:95], vcc\n\t" \
  "v_cmp_eq_u32 vcc, %[sent], v227\n\t" \
  "s_or_b64 s[94:95], s[94:95], vcc\n\t" \
  "v_cmp_eq_u32 vcc, %[sent], v228\n\t" \
  "s_or_b64 s[94:95], s[94:95], vcc\n\t" \
  "v_cmp_eq_u32 vcc, %[sent], v229\n\t" \
  "s_or_b64 s[94:95], s[94:95], vcc\n\t" \
  "v_cmp_eq_u32 vcc, %[sent], v230\n\t" \
  "s_or_b64 s[94:95], s[94:95], vcc\n\t" \
  "v_cmp_eq_u32 vcc, %[sent], v231\n\t" \
  "s_or_b64 s[94:95], s[94:95], vcc\n\t" \
  "s_cmp_eq_u64 s[94:95], 0\n\t" \
  "s_cbranch_scc1 .Lok%=\n\t" \
  "s_add_u32 s92, s92, 1\n\t" \
  "s_sleep 6\n\t" \
  "s_cmp_eq_u32 s92, 1\n\t" \
  "s_cbranch_scc1 .Lpoll%=\n\t" \
  "s_sleep 1\n\t" \
  "s_and_b32 s93, s92, 63\n\t" \
  "s_cmp_lg_u32 s93, 0\n\t" \
  "s_cbranch_scc1 .Lpoll%=\n\t" \
  "v_mov_b32 v149, 0\n\t" \
  "buffer_load_dword v148, v149, %[rs], %[sab] offen sc1\n\t" \
  "s_waitcnt vmcnt(0)\n\t" \
  "v_readfirstlane_b32 s93, v148\n\t" \
  "s_nop 3\n\t" \
  "s_cmp_eq_u32 s93, 1\n\t" \
  "s_cbranch_scc1 .Lok%=\n\t" \
  "s_cmp_lt_u32 s92, 0x80000\n\t" \
  "s_cbranch_scc1 .Lpoll%=\n\t" \
  "v_mov_b32 v148, 1\n\t" \
  "buffer_store_dword v148, v149, %[rs], %[sab] offen sc1\n\t" \
  "s_branch .Lok%=\n\t" \
  ".Lend%=:\n\t"
constexpr int TLAG = 2;
#define RED_ASM \
  "s_mov_b32 %[step], 0\n\t" \
  "s_lshl_b32 %[sh], %[step], 15\n\t" \
  "s_lshr_b32 s93, %[step], 4\n\t" \
  "s_lshr_b32 s93, %[pm], s93\n\t" \
  "s_and_b32 s93, s93, 4\n\t" \
  "s_lshl_b32 s93, s93, 11\n\t" \
  "s_lshl2_add_u32 %[so], %[sh], s93\n\t" \
  "s_sub_i32 s93, %[step], 2\n\t" \
  "s_lshl_b32 %[st], s93, 19\n\t" \
  "s_lshl_b32 s94, s93, 11\n\t" \
  "s_xor_b32 s94, s94, %[s0]\n\t" \
  "s_and_b32 s94, s94, 0x2000\n\t" \
  "s_or_b32 %[st], %[st], s94\n\t" \
  "s_cmp_lt_i32 s93, 0\n\t" \
  "s_cselect_b32 %[st], 0x70000000, %[st]\n\t" \
  "s_add_u32 %[st4], %[st], 4096\n\t" \
  "buffer_load_dwordx2 v[166:167], v173, %[rs], 0 offen sc1\n\t" \
  "v_mov_b64 v[144:145], 0\n\t" \
  "v_mov_b64 v[146:147], 0\n\t" \
  "v_mov_b64 v[148:149], 0\n\t" \
  "v_mov_b64 v[150:151], 0\n\t" \
  "v_mov_b64 v[152:153], 0\n\t" \
  "v_mov_b64 v[154:155], 0\n\t" \
  "v_mov_b64 v[156:157], 0\n\t" \
  "v_mov_b64 v[158:159], 0\n\t" \
  ".Lstep%=:\n\t" \
  "s_setprio 3\n\t" \
  "s_mov_b32 s92, 0\n\t" \
  ".Lcal%=:\n\t" \
  "s_waitcnt vmcnt(0)\n\t" \
  "s_memrealtime s[76:77]\n\t" \
  "v_cmp_ne_u32 vcc, %[step], v166\n\t" \
  "v_readlane_b32 s90, v167, 0\n\t" \
  "v_readlane_b32 s91, v167, 1\n\t" \
  "v_readlane_b32 s94, v167, 2\n\t" \
  "v_readlane_b32 s95, v167, 3\n\t" \
  "s_sub_i32 s90, s90, %[base]\n\t" \
  "s_sub_i32 s91, s91, %[base]\n\t" \
  "s_sub_i32 s94, s94, %[base]\n\t" \
  "s_sub_i32 s95, s95, %[base]\n\t" \
  "s_max_i32 s90, s90, s91\n\t" \
  "s_max_i32 s94, s94, s95\n\t" \
  "s_max_i32 s90, s90, s94\n\t" \
  "s_add_i32 s90, s90, %[ds]\n\t" \
  "s_waitcnt lgkmcnt(0)\n\t" \
  "s_lshl2_add_u32 s76, s76, %[mn]\n\t" \
  "s_sub_i32 s76, s76, %[base]\n\t" \
  "s_max_i32 s94, s90, s76\n\t" \
  "s_add_u32 %[E], s90, %[base]\n\t" \
  "s_and_b32 s93, vcc_lo, 0xf\n\t" \
  "s_cbranch_scc1 .Lbl%=\n\t" \
  ".Lgo%=:\n\t" \
  "v_mov_b32 v166, 7\n\t" \
  "v_mov_b32 v162, %[step]\n\t" \
  "s_mov_b64 exec, 1\n\t" \
  "v_mov_b32 v164, s94\n\t" \
  "ds_max_u32 v168, v164 offset:4100\n\t" \
  "ds_inc_rtn_u32 v161, v168, v166 offset:4096\n\t" \
  "ds_read_b32 v163, v168 offset:4100\n\t" \
  "s_waitcnt lgkmcnt(0)\n\t" \
  "v_readfirstlane_b32 s94, v161\n\t" \
  "v_readfirstlane_b32 s95, v163\n\t" \
  "s_mov_b64 exec, -1\n\t" \
  "s_cmp_lg_u32 s94, 7\n\t" \
  "s_cbranch_scc1 .Lna%=\n\t" \
  "s_add_u32 s95, s95, %[rl]\n\t" \
  "s_add_u32 s95, s95, %[base]\n\t" \
  "s_mov_b64 exec, 0xffff\n\t" \
  "v_mov_b32 v163, s95\n\t" \
  "buffer_store_dwordx2 v[162:163], v175, %[rs], 0 offen sc1\n\t" \
  "s_mov_b64 exec, -1\n\t" \
  ".Lna%=:\n\t" \
  "s_memrealtime s[90:91]\n\t" \
  "s_waitcnt lgkmcnt(0)\n\t" \
  "s_lshl_b32 s93, s90, 2\n\t" \
  "s_sub_i32 s93, %[E], s93\n\t" \
  "s_cmp_le_i32 s93, 0\n\t" \
  "s_cbranch_scc1 .Lgop%=\n\t" \
  "s_min_i32 s93, s93, 8000\n\t" \
  "s_add_u32 s93, s93, 3\n\t" \
  "s_lshr_b32 s93, s93, 2\n\t" \
  "s_add_u32 s93, s93, s90\n\t" \
  "s_memrealtime s[90:91]\n\t" \
  ".Lpacep%=:\n\t" \
  "s_memrealtime s[94:95]\n\t" \
  "s_waitcnt lgkmcnt(1)\n\t" \
  "s_sub_i32 s91, s90, s93\n\t" \
  "s_cmp_ge_i32 s91, 0\n\t" \
  "s_cbranch_scc1 .Lgop%=\n\t" \
  "s_memrealtime s[90:91]\n\t" \
  "s_waitcnt lgkmcnt(1)\n\t" \
  "s_sub_i32 s95, s94, s93\n\t" \
  "s_cmp_lt_i32 s95, 0\n\t" \
  "s_cbranch_scc1 .Lpacep%=\n\t" \
  ".Lgop%=:\n\t" \
  "buffer_load_dwordx4 v[128:131], v234, %[rs], %[so] offen sc1\n\t" \
  "s_nop 15\n\t" \
  "buffer_load_dwordx4 v[132:135], v235, %[rs], %[so] offen sc1\n\t" \
  "s_nop 15\n\t" \
  "buffer_load_dwordx4 v[136:139], v236, %[rs], %[so] offen sc1\n\t" \
  "s_nop 15\n\t" \
  "buffer_load_dwordx4 v[140:143], v237, %[rs], %[so] offen sc1\n\t" \
  "s_waitcnt lgkmcnt(0)\n\t" \
  ".Lw%=:\n\t" \
  "s_waitcnt vmcnt(3)\n\t" \
  "v_smfmac_f32_16x16x64_bf16 v[144:147], v[128:131], v[0:7], v172\n\t" \
  "v_max3_u32 v164, v128, v129, 0\n\t" \
  "v_smfmac_f32_16x16x64_bf16 v[148:151], v[128:131], v[8:15], v172\n\t" \
  "v_max3_u32 v164, v130, v131, v164\n\t" \
  "v_smfmac_f32_16x16x64_bf16 v[152:155], v[128:131], v[16:23], v172\n\t" \
  "v_smfmac_f32_16x16x64_bf16 v[156:159], v[128:131], v[24:31], v172\n\t" \
  "buffer_load_dwordx4 v[176:179], v238, %[rt], %[st] offen offset:0 sc1\n\t" \
  "s_waitcnt vmcnt(3)\n\t" \
  "v_smfmac_f32_16x16x64_bf16 v[144:147], v[132:135], v[32:39], v172\n\t" \
  "v_max3_u32 v164, v132, v133, v164\n\t" \
  "v_smfmac_f32_16x16x64_bf16 v[148:151], v[132:135], v[40:47], v172\n\t" \
  "v_max3_u32 v164, v134, v135, v164\n\t" \
  "v_smfmac_f32_16x16x64_bf16 v[152:155], v[132:135], v[48:55], v172\n\t" \
  "v_smfmac_f32_16x16x64_bf16 v[156:159], v[132:135], v[56:63], v172\n\t" \
  "buffer_load_dwordx4 v[180:183], v238, %[rt], %[st] offen offset:1024 sc1\n\t" \
  "s_waitcnt vmcnt(3)\n\t" \
  "v_smfmac_f32_16x16x64_bf16 v[144:147], v[136:139], v[64:71], v172\n\t" \
  "v_max3_u32 v164, v136, v137, v164\n\t" \
  "v_smfmac_f32_16x16x64_bf16 v[148:151], v[136:139], v[72:79], v172\n\t" \
  "v_max3_u32 v164, v138, v139, v164\n\t" \
  "v_smfmac_f32_16x16x64_bf16 v[152:155], v[136:139], v[80:87], v172\n\t" \
  "v_smfmac_f32_16x16x64_bf16 v[156:159], v[136:139], v[88:95], v172\n\t" \
  "buffer_load_dwordx4 v[184:187], v238, %[rt], %[st4] offen offset:0 sc1\n\t" \
  "s_waitcnt vmcnt(3)\n\t" \
  "v_smfmac_f32_16x16x64_bf16 v[144:147], v[140:143], v[96:103], v172\n\t" \
  "v_max3_u32 v164, v140, v141, v164\n\t" \
  "v_smfmac_f32_16x16x64_bf16 v[148:151], v[140:143], v[104:111], v172\n\t" \
  "v_max3_u32 v164, v142, v143, v164\n\t" \
  "v_smfmac_f32_16x16x64_bf16 v[152:155], v[140:143], v[112:119], v172\n\t" \
  "v_cmp_le_u32 vcc, %[sent], v164\n\t" \
  "v_smfmac_f32_16x16x64_bf16 v[156:159], v[140:143], v[120:127], v172\n\t" \
  "buffer_load_dwordx4 v[188:191], v238, %[rt], %[st4] offen offset:1024 sc1\n\t" \
  "s_cbranch_vccnz .Lslow%=\n\t" \
  ".Lok%=:\n\t" \
  "v_add_f32_dpp v160, v144, v146 quad_perm:[0,1,2,3] row_mask:0x3 bank_mask:0xf\n\t" \
  "v_add_f32_dpp v161, v145, v147 quad_perm:[0,1,2,3] row_mask:0x3 bank_mask:0xf\n\t" \
  "v_add_f32_dpp v162, v148, v150 quad_perm:[0,1,2,3] row_mask:0x3 bank_mask:0xf\n\t" \
  "v_add_f32_dpp v163, v149, v151 quad_perm:[0,1,2,3] row_mask:0x3 bank_mask:0xf\n\t" \
  "v_add_f32_dpp v160, v152, v154 quad_perm:[0,1,2,3] row_mask:0xc bank_mask:0xf\n\t" \
  "v_add_f32_dpp v161, v153, v155 quad_perm:[0,1,2,3] row_mask:0xc bank_mask:0xf\n\t" \
  "v_add_f32_dpp v162, v156, v158 quad_perm:[0,1,2,3] row_mask:0xc bank_mask:0xf\n\t" \
  "v_add_f32_dpp v163, v157, v159 quad_perm:[0,1,2,3] row_mask:0xc bank_mask:0xf\n\t" \
  "s_nop 0\n\t" \
  "v_permlane32_swap_b32 v160, v162\n\t" \
  "v_permlane32_swap_b32 v161, v163\n\t" \
  "v_add_f32 v160, v160, v162\n\t" \
  "v_add_f32 v161, v161, v163\n\t" \
  "ds_write_b64 v169, v[160:161]\n\t" \
  "s_mov_b64 exec, 1\n\t" \
  "ds_inc_rtn_u32 v164, v168, v166 offset:4104\n\t" \
  "s_mov_b64 exec, -1\n\t" \
  "ds_read_b64 v[128:129], v170 offset:0\n\t" \
  "ds_read_b64 v[130:131], v170 offset:512\n\t" \
  "ds_read_b64 v[132:133], v170 offset:1024\n\t" \
  "ds_read_b64 v[134:135], v170 offset:1536\n\t" \
  "ds_read_b64 v[136:137], v170 offset:2048\n\t" \
  "ds_read_b64 v[138:139], v170 offset:2560\n\t" \
  "ds_read_b64 v[140:141], v170 offset:3072\n\t" \
  "ds_read_b64 v[142:143], v170 offset:3584\n\t" \
  "s_waitcnt lgkmcnt(8)\n\t" \
  "v_readfirstlane_b32 s93, v164\n\t" \
  "s_cmp_lg_u32 s93, 7\n\t" \
  "s_cbranch_scc1 .Lb1%=\n\t" \
  "s_waitcnt lgkmcnt(4)\n\t" \
  "v_pk_add_f32 v[128:129], v[128:129], v[130:131]\n\t" \
  "v_pk_add_f32 v[132:133], v[132:133], v[134:135]\n\t" \
  "s_waitcnt lgkmcnt(0)\n\t" \
  "v_pk_add_f32 v[136:137], v[136:137], v[138:139]\n\t" \
  "v_pk_add_f32 v[140:141], v[140:141], v[142:143]\n\t" \
  "v_pk_add_f32 v[128:129], v[128:129], v[132:133]\n\t" \
  "v_pk_add_f32 v[136:137], v[136:137], v[140:141]\n\t" \
  "v_pk_add_f32 v[128:129], v[128:129], v[136:137]\n\t" \
  "v_cvt_pk_bf16_f32 v128, v128, v129\n\t" \
  "s_nop 1\n\t" \
  "v_mov_b32_dpp v129, v128 quad_perm:[1,0,3,2] row_mask:0xf bank_mask:0xf\n\t" \
  "v_perm_b32 v128, v129, v128, v174\n\t" \
  "buffer_store_dword v128, v171, %[rs], %[sh] offen sc1\n\t" \
  "s_memtime s[90:91]\n\t" \
  "s_barrier\n\t" \
  "s_memtime s[76:77]\n\t" \
  "s_memrealtime s[78:79]\n\t" \
  "s_nop 3\n\t" \
  "s_memrealtime s[80:81]\n\t" \
  "s_nop 3\n\t" \
  "s_memrealtime s[82:83]\n\t" \
  "s_nop 3\n\t" \
  "s_memrealtime s[84:85]\n\t" \
  "s_nop 3\n\t" \
  "s_memrealtime s[86:87]\n\t" \
  "s_nop 3\n\t" \
  "s_memrealtime s[88:89]\n\t" \
  "s_mov_b32 s93, 0\n\t" \
  "s_waitcnt lgkmcnt(0)\n\t" \
  "s_cmp_eq_u32 s80, s78\n\t" \
  "s_addc_u32 s93, s93, 0\n\t" \
  "s_cmp_eq_u32 s82, s78\n\t" \
  "s_addc_u32 s93, s93, 0\n\t" \
  "s_cmp_eq_u32 s84, s78\n\t" \
  "s_addc_u32 s93, s93, 0\n\t" \
  "s_cmp_eq_u32 s86, s78\n\t" \
  "s_addc_u32 s93, s93, 0\n\t" \
  "s_cmp_eq_u32 s88, s78\n\t" \
  "s_addc_u32 s93, s93, 0\n\t" \
  "s_mul_i32 s93, s93, 21\n\t" \
  "s_add_u32 s76, s76, 15\n\t" \
  "s_add_u32 s76, s76, s93\n\t" \
  "s_lshl2_add_u32 s78, s78, 16\n\t" \
  "s_mov_b64 exec, 1\n\t" \
  "s_sub_i32 s90, s76, s90\n\t" \
  "s_mul_i32 s90, s90, 11900\n\t" \
  "s_ashr_i32 s90, s90, 16\n\t" \
  "s_sub_u32 s90, s78, s90\n\t" \
  "s_sub_u32 s90, s90, %[base]\n\t" \
  "v_mov_b32 v164, s90\n\t" \
  "ds_write_b32 v168, v164 offset:4108\n\t" \
  "s_mov_b64 exec, -1\n\t" \
  "s_waitcnt vmcnt(1)\n\t" \
  ".Lb2%=:\n\t" \
  "s_setprio 0\n\t" \
  "s_bitcmp0_b64 %[am], %[step]\n\t" \
  "s_cbranch_scc1 .La1%=\n\t" \
  "buffer_store_dwordx4 v[224:227], v228, %[ra], 0 offen sc1\n\t" \
  ".La1%=:\n\t" \
  "buffer_load_dwordx4 v[192:195], v239, %[rt], %[st] offen offset:0 sc1\n\t" \
  "buffer_load_dwordx4 v[196:199], v239, %[rt], %[st] offen offset:1024 sc1\n\t" \
  "v_mfma_f32_16x16x32_bf16 v[128:131], v[0:3], v[176:179], 0\n\t" \
  "v_mfma_f32_16x16x32_bf16 v[136:139], v[8:11], v[176:179], 0\n\t" \
  "ds_read2st64_b32 v[144:145], v231 offset0:0 offset1:8\n\t" \
  "ds_read2st64_b32 v[146:147], v231 offset0:16 offset1:24\n\t" \
  "ds_read2st64_b32 v[148:149], v231 offset0:32 offset1:40\n\t" \
  "ds_read2st64_b32 v[150:151], v231 offset0:48 offset1:56\n\t" \
  "buffer_load_dwordx4 v[200:203], v239, %[rt], %[st4] offen offset:0 sc1\n\t" \
  "buffer_load_dwordx4 v[204:207], v239, %[rt], %[st4] offen offset:1024 sc1\n\t" \
  "v_mfma_f32_16x16x32_bf16 v[132:135], v[4:7], v[180:183], 0\n\t" \
  "v_mfma_f32_16x16x32_bf16 v[140:143], v[12:15], v[180:183], 0\n\t" \
  "s_waitcnt lgkmcnt(0)\n\t" \
  "v_add_f32 v144, v144, v145\n\t" \
  "v_add_f32 v146, v146, v147\n\t" \
  "v_add_f32 v148, v148, v149\n\t" \
  "v_add_f32 v150, v150, v151\n\t" \
  "v_add_f32 v144, v144, v146\n\t" \
  "v_add_f32 v148, v148, v150\n\t" \
  "buffer_load_dwordx4 v[208:211], v240, %[rt], %[st] offen offset:0 sc1\n\t" \
  "buffer_load_dwordx4 v[212:215], v240, %[rt], %[st] offen offset:1024 sc1\n\t" \
  "v_mfma_f32_16x16x32_bf16 v[128:131], v[16:19], v[184:187], v[128:131]\n\t" \
  "v_mfma_f32_16x16x32_bf16 v[136:139], v[24:27], v[184:187], v[136:139]\n\t" \
  "v_add_f32 v144, v144, v148\n\t" \
  "v_cvt_pk_bf16_f32 v144, v144, v144\n\t" \
  "ds_write_b16 v232, v144\n\t" \
  "ds_read_b128 v[224:227], v229\n\t" \
  "v_add_u32 v228, %[ainc], v228\n\t" \
  "v_add_u32 v229, %[rinc], v229\n\t" \
  "v_add_u32 v232, 1024, v232\n\t" \
  "v_xor_b32 v231, 0x4000, v231\n\t" \
  "buffer_load_dwordx4 v[216:219], v240, %[rt], %[st4] offen offset:0 sc1\n\t" \
  "buffer_load_dwordx4 v[220:223], v240, %[rt], %[st4] offen offset:1024 sc1\n\t" \
  "v_mfma_f32_16x16x32_bf16 v[132:135], v[20:23], v[188:191], v[132:135]\n\t" \
  "v_mfma_f32_16x16x32_bf16 v[140:143], v[28:31], v[188:191], v[140:143]\n\t" \
  "buffer_load_dwordx4 v[176:179], v241, %[rt], %[st] offen offset:0 sc1\n\t" \
  "s_waitcnt vmcnt(8)\n\t" \
  "v_mfma_f32_16x16x32_bf16 v[128:131], v[32:35], v[192:195], v[128:131]\n\t" \
  "v_mfma_f32_16x16x32_bf16 v[136:139], v[40:43], v[192:195], v[136:139]\n\t" \
  "buffer_load_dwordx4 v[180:183], v241, %[rt], %[st] offen offset:1024 sc1\n\t" \
  "s_waitcnt vmcnt(8)\n\t" \
  "v_mfma_f32_16x16x32_bf16 v[132:135], v[36:39], v[196:199], v[132:135]\n\t" \
  "v_mfma_f32_16x16x32_bf16 v[140:143], v[44:47], v[196:199], v[140:143]\n\t" \
  "buffer_load_dwordx4 v[184:187], v241, %[rt], %[st4] offen offset:0 sc1\n\t" \
  "s_waitcnt vmcnt(8)\n\t" \
  "v_mfma_f32_16x16x32_bf16 v[128:131], v[48:51], v[200:203], v[128:131]\n\t" \
  "v_mfma_f32_16x16x32_bf16 v[136:139], v[56:59], v[200:203], v[136:139]\n\t" \
  "ds_read_b128 v[160:163], v168 offset:4096\n\t" \
  "buffer_load_dwordx4 v[188:191], v241, %[rt], %[st4] offen offset:1024 sc1\n\t" \
  "s_waitcnt vmcnt(8)\n\t" \
  "v_mfma_f32_16x16x32_bf16 v[132:135], v[52:55], v[204:207], v[132:135]\n\t" \
  "v_mfma_f32_16x16x32_bf16 v[140:143], v[60:63], v[204:207], v[140:143]\n\t" \
  "s_waitcnt lgkmcnt(0)\n\t" \
  "v_readfirstlane_b32 s90, v161\n\t" \
  "v_readfirstlane_b32 s91, v163\n\t" \
  "s_add_u32 s93, s90, %[rl]\n\t" \
  "s_add_u32 s93, s93, %[base]\n\t" \
  "s_add_u32 %[E], s93, %[lk]\n\t" \
  "s_sub_i32 s91, s91, s90\n\t" \
  "s_sub_i32 s91, s91, %[rl]\n\t" \
  "s_min_i32 s93, s91, %[up]\n\t" \
  "s_max_i32 s93, s93, %[dn]\n\t" \
  "s_cmp_gt_i32 s91, 60\n\t" \
  "s_cselect_b32 s93, 2, s93\n\t" \
  "s_add_i32 %[rl], %[rl], s93\n\t" \
  "s_waitcnt vmcnt(7)\n\t" \
  "v_mfma_f32_16x16x32_bf16 v[128:131], v[64:67], v[208:211], v[128:131]\n\t" \
  "v_mfma_f32_16x16x32_bf16 v[136:139], v[72:75], v[208:211], v[136:139]\n\t" \
  "s_add_u32 %[step], %[step], 1\n\t" \
  "s_lshl_b32 %[sh], %[step], 15\n\t" \
  "s_lshr_b32 s93, %[step], 4\n\t" \
  "s_lshr_b32 s93, %[pm], s93\n\t" \
  "s_and_b32 s93, s93, 4\n\t" \
  "s_lshl_b32 s93, s93, 11\n\t" \
  "s_lshl2_add_u32 %[so], %[sh], s93\n\t" \
  "s_waitcnt vmcnt(6)\n\t" \
  "v_mfma_f32_16x16x32_bf16 v[132:135], v[68:71], v[212:215], v[132:135]\n\t" \
  "v_mfma_f32_16x16x32_bf16 v[140:143], v[76:79], v[212:215], v[140:143]\n\t" \
  "s_sub_i32 s93, %[step], 2\n\t" \
  "s_lshl_b32 %[st], s93, 19\n\t" \
  "s_lshl_b32 s94, s93, 11\n\t" \
  "s_xor_b32 s94, s94, %[s0]\n\t" \
  "s_and_b32 s94, s94, 0x2000\n\t" \
  "s_or_b32 %[st], %[st], s94\n\t" \
  "s_cmp_lt_i32 s93, 0\n\t" \
  "s_cselect_b32 %[st], 0x70000000, %[st]\n\t" \
  "s_add_u32 %[st4], %[st], 4096\n\t" \
  "s_waitcnt vmcnt(5)\n\t" \
  "v_mfma_f32_16x16x32_bf16 v[128:131], v[80:83], v[216:219], v[128:131]\n\t" \
  "v_mfma_f32_16x16x32_bf16 v[136:139], v[88:91], v[216:219], v[136:139]\n\t" \
  "s_waitcnt vmcnt(4)\n\t" \
  "v_mfma_f32_16x16x32_bf16 v[132:135], v[84:87], v[220:223], v[132:135]\n\t" \
  "v_mfma_f32_16x16x32_bf16 v[140:143], v[92:95], v[220:223], v[140:143]\n\t" \
  "s_cmp_eq_u32 %[step], 63\n\t" \
  "s_cbranch_scc1 .Llast%=\n\t" \
  "s_movk_i32 s93, 600\n\t" \
  ".Lpacel%=:\n\t" \
  "s_memrealtime s[90:91]\n\t" \
  "s_waitcnt lgkmcnt(0)\n\t" \
  "s_lshl_b32 s90, s90, 2\n\t" \
  "s_sub_i32 s91, %[E], s90\n\t" \
  "s_cmp_le_i32 s91, 0\n\t" \
  "s_cbranch_scc1 .Lgol%=\n\t" \
  "s_sub_u32 s93, s93, 1\n\t" \
  "s_cmp_lg_u32 s93, 0\n\t" \
  "s_cbranch_scc1 .Lpacel%=\n\t" \
  ".Lgol%=:\n\t" \
  "buffer_load_dwordx2 v[166:167], v173, %[rs], 0 offen sc1\n\t" \
  "s_waitcnt vmcnt(4)\n\t" \
  "v_mfma_f32_16x16x32_bf16 v[128:131], v[96:99], v[176:179], v[128:131]\n\t" \
  "v_mfma_f32_16x16x32_bf16 v[136:139], v[104:107], v[176:179], v[136:139]\n\t" \
  "s_waitcnt vmcnt(3)\n\t" \
  "v_mfma_f32_16x16x32_bf16 v[132:135], v[100:103], v[180:183], v[132:135]\n\t" \
  "v_mfma_f32_16x16x32_bf16 v[140:143], v[108:111], v[180:183], v[140:143]\n\t" \
  "s_waitcnt vmcnt(2)\n\t" \
  "v_mfma_f32_16x16x32_bf16 v[128:131], v[112:115], v[184:187], v[128:131]\n\t" \
  "v_mfma_f32_16x16x32_bf16 v[136:139], v[120:123], v[184:187], v[136:139]\n\t" \
  "s_waitcnt vmcnt(1)\n\t" \
  "v_mfma_f32_16x16x32_bf16 v[132:135], v[116:119], v[188:191], v[132:135]\n\t" \
  "v_mfma_f32_16x16x32_bf16 v[140:143], v[124:127], v[188:191], v[140:143]\n\t" \
  "v_mov_b64 v[144:145], 0\n\t" \
  "v_mov_b64 v[146:147], 0\n\t" \
  "v_mov_b64 v[148:149], 0\n\t" \
  "v_mov_b64 v[150:151], 0\n\t" \
  "v_mov_b64 v[152:153], 0\n\t" \
  "v_mov_b64 v[154:155], 0\n\t" \
  "v_mov_b64 v[156:157], 0\n\t" \
  "v_mov_b64 v[158:159], 0\n\t" \
  "v_pk_add_f32 v[128:129], v[128:129], v[132:133]\n\t" \
  "v_pk_add_f32 v[130:131], v[130:131], v[134:135]\n\t" \
  "ds_write_b128 v230, v[128:131] offset:0\n\t" \
  "v_pk_add_f32 v[136:137], v[136:137], v[140:141]\n\t" \
  "v_pk_add_f32 v[138:139], v[138:139], v[142:143]\n\t" \
  "ds_write_b128 v230, v[136:139] offset:1024\n\t" \
  "v_xor_b32 v230, 0x4000, v230\n\t" \
  "s_cmp_lg_u32 %[step], 63\n\t" \
  "s_cbranch_scc1 .Lstep%=\n\t" \
  ".Lfc%=:\n\t" \
  "s_waitcnt vmcnt(0)\n\t" \
  "v_cmp_eq_u32 vcc, -1, v166\n\t" \
  "s_cbranch_vccnz .Lfw%=\n\t" \
  ".Lfo%=:\n\t" \
  "v_add_u32 v242, 16384, v242\n\t" \
  "buffer_load_dwordx4 v[176:179], v238, %[rt], %[st] offen offset:0 sc1\n\t" \
  "buffer_load_dwordx4 v[180:183], v238, %[rt], %[st] offen offset:1024 sc1\n\t" \
  "buffer_load_dwordx4 v[184:187], v238, %[rt], %[st4] offen offset:0 sc1\n\t" \
  "buffer_load_dwordx4 v[188:191], v238, %[rt], %[st4] offen offset:1024 sc1\n\t" \
  "buffer_load_dwordx4 v[192:195], v239, %[rt], %[st] offen offset:0 sc1\n\t" \
  "buffer_load_dwordx4 v[196:199], v239, %[rt], %[st] offen offset:1024 sc1\n\t" \
  "buffer_load_dwordx4 v[200:203], v239, %[rt], %[st4] offen offset:0 sc1\n\t" \
  "buffer_load_dwordx4 v[204:207], v239, %[rt], %[st4] offen offset:1024 sc1\n\t" \
  "v_subrev_u32 v230, 98304, v230\n\t" \
  ".Lt%=:\n\t" \
  "s_waitcnt vmcnt(7)\n\t" \
  "v_mfma_f32_16x16x32_bf16 v[128:131], v[0:3], v[176:179], 0\n\t" \
  "v_mfma_f32_16x16x32_bf16 v[136:139], v[8:11], v[176:179], 0\n\t" \
  "buffer_load_dwordx4 v[176:179], v240, %[rt], %[st] offen offset:0 sc1\n\t" \
  "buffer_load_dword v166, v242, %[rs], 0 offen sc1\n\t" \
  "s_waitcnt vmcnt(8)\n\t" \
  "v_mfma_f32_16x16x32_bf16 v[132:135], v[4:7], v[180:183], 0\n\t" \
  "v_mfma_f32_16x16x32_bf16 v[140:143], v[12:15], v[180:183], 0\n\t" \
  "buffer_load_dwordx4 v[180:183], v240, %[rt], %[st] offen offset:1024 sc1\n\t" \
  "s_waitcnt vmcnt(8)\n\t" \
  "v_mfma_f32_16x16x32_bf16 v[128:131], v[16:19], v[184:187], v[128:131]\n\t" \
  "v_mfma_f32_16x16x32_bf16 v[136:139], v[24:27], v[184:187], v[136:139]\n\t" \
  "buffer_load_dwordx4 v[184:187], v240, %[rt], %[st4] offen offset:0 sc1\n\t" \
  "s_waitcnt vmcnt(8)\n\t" \
  "v_mfma_f32_16x16x32_bf16 v[132:135], v[20:23], v[188:191], v[132:135]\n\t" \
  "v_mfma_f32_16x16x32_bf16 v[140:143], v[28:31], v[188:191], v[140:143]\n\t" \
  "buffer_load_dwordx4 v[188:191], v240, %[rt], %[st4] offen offset:1024 sc1\n\t" \
  "s_waitcnt vmcnt(8)\n\t" \
  "v_mfma_f32_16x16x32_bf16 v[128:131], v[32:35], v[192:195], v[128:131]\n\t" \
  "v_mfma_f32_16x16x32_bf16 v[136:139], v[40:43], v[192:195], v[136:139]\n\t" \
  "buffer_load_dwordx4 v[192:195], v241, %[rt], %[st] offen offset:0 sc1\n\t" \
  "s_waitcnt vmcnt(8)\n\t" \
  "v_mfma_f32_16x16x32_bf16 v[132:135], v[36:39], v[196:199], v[132:135]\n\t" \
  "v_mfma_f32_16x16x32_bf16 v[140:143], v[44:47], v[196:199], v[140:143]\n\t" \
  "buffer_load_dwordx4 v[196:199], v241, %[rt], %[st] offen offset:1024 sc1\n\t" \
  "s_waitcnt vmcnt(8)\n\t" \
  "v_mfma_f32_16x16x32_bf16 v[128:131], v[48:51], v[200:203], v[128:131]\n\t" \
  "v_mfma_f32_16x16x32_bf16 v[136:139], v[56:59], v[200:203], v[136:139]\n\t" \
  "buffer_load_dwordx4 v[200:203], v241, %[rt], %[st4] offen offset:0 sc1\n\t" \
  "s_waitcnt vmcnt(8)\n\t" \
  "v_mfma_f32_16x16x32_bf16 v[132:135], v[52:55], v[204:207], v[132:135]\n\t" \
  "v_mfma_f32_16x16x32_bf16 v[140:143], v[60:63], v[204:207], v[140:143]\n\t" \
  "buffer_load_dwordx4 v[204:207], v241, %[rt], %[st4] offen offset:1024 sc1\n\t" \
  "s_waitcnt vmcnt(8)\n\t" \
  "v_mfma_f32_16x16x32_bf16 v[128:131], v[64:67], v[176:179], v[128:131]\n\t" \
  "v_mfma_f32_16x16x32_bf16 v[136:139], v[72:75], v[176:179], v[136:139]\n\t" \
  "s_waitcnt vmcnt(7)\n\t" \
  ".Ltc%=:\n\t" \
  "v_cmp_eq_u32 vcc, -1, v166\n\t" \
  "s_cbranch_vccnz .Ltw%=\n\t" \
  ".Lto%=:\n\t" \
  "s_cmp_eq_u32 %[step], 65\n\t" \
  "s_cbranch_scc1 .Ltl%=\n\t" \
  "s_add_u32 %[step], %[step], 1\n\t" \
  "s_sub_i32 s93, %[step], 2\n\t" \
  "s_lshl_b32 %[st], s93, 19\n\t" \
  "s_lshl_b32 s94, s93, 11\n\t" \
  "s_xor_b32 s94, s94, %[s0]\n\t" \
  "s_and_b32 s94, s94, 0x2000\n\t" \
  "s_or_b32 %[st], %[st], s94\n\t" \
  "s_cmp_lt_i32 s93, 0\n\t" \
  "s_cselect_b32 %[st], 0x70000000, %[st]\n\t" \
  "s_add_u32 %[st4], %[st], 4096\n\t" \
  "v_add_u32 v242, 16384, v242\n\t" \
  "s_cmp_eq_u32 %[step], 65\n\t" \
  "s_cbranch_scc0 .Ltn%=\n\t" \
  "v_mov_b32 v242, %[fg]\n\t" \
  ".Ltn%=:\n\t" \
  "buffer_load_dwordx4 v[176:179], v238, %[rt], %[st] offen offset:0 sc1\n\t" \
  "s_waitcnt vmcnt(7)\n\t" \
  "v_mfma_f32_16x16x32_bf16 v[132:135], v[68:71], v[180:183], v[132:135]\n\t" \
  "v_mfma_f32_16x16x32_bf16 v[140:143], v[76:79], v[180:183], v[140:143]\n\t" \
  "buffer_load_dwordx4 v[180:183], v238, %[rt], %[st] offen offset:1024 sc1\n\t" \
  "s_waitcnt vmcnt(7)\n\t" \
  "v_mfma_f32_16x16x32_bf16 v[128:131], v[80:83], v[184:187], v[128:131]\n\t" \
  "v_mfma_f32_16x16x32_bf16 v[136:139], v[88:91], v[184:187], v[136:139]\n\t" \
  "buffer_load_dwordx4 v[184:187], v238, %[rt], %[st4] offen offset:0 sc1\n\t" \
  "s_waitcnt vmcnt(7)\n\t" \
  "v_mfma_f32_16x16x32_bf16 v[132:135], v[84:87], v[188:191], v[132:135]\n\t" \
  "v_mfma_f32_16x16x32_bf16 v[140:143], v[92:95], v[188:191], v[140:143]\n\t" \
  "buffer_load_dwordx4 v[188:191], v238, %[rt], %[st4] offen offset:1024 sc1\n\t" \
  "s_waitcnt vmcnt(7)\n\t" \
  "v_mfma_f32_16x16x32_bf16 v[128:131], v[96:99], v[192:195], v[128:131]\n\t" \
  "v_mfma_f32_16x16x32_bf16 v[136:139], v[104:107], v[192:195], v[136:139]\n\t" \
  "buffer_load_dwordx4 v[192:195], v239, %[rt], %[st] offen offset:0 sc1\n\t" \
  "s_waitcnt vmcnt(7)\n\t" \
  "v_mfma_f32_16x16x32_bf16 v[132:135], v[100:103], v[196:199], v[132:135]\n\t" \
  "v_mfma_f32_16x16x32_bf16 v[140:143], v[108:111], v[196:199], v[140:143]\n\t" \
  "buffer_load_dwordx4 v[196:199], v239, %[rt], %[st] offen offset:1024 sc1\n\t" \
  "s_waitcnt vmcnt(7)\n\t" \
  "v_mfma_f32_16x16x32_bf16 v[128:131], v[112:115], v[200:203], v[128:131]\n\t" \
  "v_mfma_f32_16x16x32_bf16 v[136:139], v[120:123], v[200:203], v[136:139]\n\t" \
  "buffer_load_dwordx4 v[200:203], v239, %[rt], %[st4] offen offset:0 sc1\n\t" \
  "s_waitcnt vmcnt(7)\n\t" \
  "v_mfma_f32_16x16x32_bf16 v[132:135], v[116:119], v[204:207], v[132:135]\n\t" \
  "v_mfma_f32_16x16x32_bf16 v[140:143], v[124:127], v[204:207], v[140:143]\n\t" \
  "buffer_load_dwordx4 v[204:207], v239, %[rt], %[st4] offen offset:1024 sc1\n\t" \
  "s_nop 5\n\t" \
  "v_pk_add_f32 v[128:129], v[128:129], v[132:133]\n\t" \
  "v_pk_add_f32 v[130:131], v[130:131], v[134:135]\n\t" \
  "ds_write_b128 v230, v[128:131] offset:0\n\t" \
  "v_pk_add_f32 v[136:137], v[136:137], v[140:141]\n\t" \
  "v_pk_add_f32 v[138:139], v[138:139], v[142:143]\n\t" \
  "ds_write_b128 v230, v[136:139] offset:1024\n\t" \
  "v_add_u32 v230, 16384, v230\n\t" \
  "s_branch .Lt%=\n\t" \
  ".Ltl%=:\n\t" \
  "buffer_load_dwordx4 v[224:227], v243, %[rg], 0 offen sc1\n\t" \
  "s_waitcnt vmcnt(7)\n\t" \
  "v_mfma_f32_16x16x32_bf16 v[132:135], v[68:71], v[180:183], v[132:135]\n\t" \
  "v_mfma_f32_16x16x32_bf16 v[140:143], v[76:79], v[180:183], v[140:143]\n\t" \
  "s_waitcnt vmcnt(6)\n\t" \
  "v_mfma_f32_16x16x32_bf16 v[128:131], v[80:83], v[184:187], v[128:131]\n\t" \
  "v_mfma_f32_16x16x32_bf16 v[136:139], v[88:91], v[184:187], v[136:139]\n\t" \
  "s_waitcnt vmcnt(5)\n\t" \
  "v_mfma_f32_16x16x32_bf16 v[132:135], v[84:87], v[188:191], v[132:135]\n\t" \
  "v_mfma_f32_16x16x32_bf16 v[140:143], v[92:95], v[188:191], v[140:143]\n\t" \
  "s_waitcnt vmcnt(4)\n\t" \
  "v_mfma_f32_16x16x32_bf16 v[128:131], v[96:99], v[192:195], v[128:131]\n\t" \
  "v_mfma_f32_16x16x32_bf16 v[136:139], v[104:107], v[192:195], v[136:139]\n\t" \
  "s_waitcnt vmcnt(3)\n\t" \
  "v_mfma_f32_16x16x32_bf16 v[132:135], v[100:103], v[196:199], v[132:135]\n\t" \
  "v_mfma_f32_16x16x32_bf16 v[140:143], v[108:111], v[196:199], v[140:143]\n\t" \
  "s_waitcnt vmcnt(2)\n\t" \
  "v_mfma_f32_16x16x32_bf16 v[128:131], v[112:115], v[200:203], v[128:131]\n\t" \
  "v_mfma_f32_16x16x32_bf16 v[136:139], v[120:123], v[200:203], v[136:139]\n\t" \
  "s_waitcnt vmcnt(1)\n\t" \
  "v_mfma_f32_16x16x32_bf16 v[132:135], v[116:119], v[204:207], v[132:135]\n\t" \
  "v_mfma_f32_16x16x32_bf16 v[140:143], v[124:127], v[204:207], v[140:143]\n\t" \
  "s_nop 6\n\t" \
  "v_pk_add_f32 v[128:129], v[128:129], v[132:133]\n\t" \
  "v_pk_add_f32 v[130:131], v[130:131], v[134:135]\n\t" \
  "ds_write_b128 v230, v[128:131] offset:0\n\t" \
  "v_pk_add_f32 v[136:137], v[136:137], v[140:141]\n\t" \
  "v_pk_add_f32 v[138:139], v[138:139], v[142:143]\n\t" \
  "ds_write_b128 v230, v[136:139] offset:1024\n\t" \
  "s_waitcnt vmcnt(0) lgkmcnt(0)\n\t" \
  "s_branch .Lend%=\n\t" \
  ".Ltw%=:\n\t" \
  "s_add_u32 s92, s92, 1\n\t" \
  "s_sleep 2\n\t" \
  "s_and_b32 s93, s92, 63\n\t" \
  "s_cbranch_scc1 .Ltr%=\n\t" \
  "buffer_load_dword v164, off, %[rs], %[sab] sc1\n\t" \
  "s_waitcnt vmcnt(0)\n\t" \
  "v_readfirstlane_b32 s93, v164\n\t" \
  "s_nop 3\n\t" \
  "s_cmp_eq_u32 s93, 1\n\t" \
  "s_cbranch_scc1 .Lto%=\n\t" \
  "s_cmp_lt_u32 s92, 0x80000\n\t" \
  "s_cbranch_scc1 .Ltr%=\n\t" \
  "v_mov_b32 v164, 1\n\t" \
  "buffer_store_dword v164, off, %[rs], %[sab] sc1\n\t" \
  "s_waitcnt vmcnt(0)\n\t" \
  "s_branch .Lto%=\n\t" \
  ".Ltr%=:\n\t" \
  "buffer_load_dword v166, v242, %[rs], 0 offen sc1\n\t" \
  "s_waitcnt vmcnt(0)\n\t" \
  "s_branch .Ltc%=\n\t" \
  ".Llast%=:\n\t" \
  "v_mov_b32 v173, v242\n\t" \
  "s_branch .Lgol%=\n\t" \
  ".Lfw%=:\n\t" \
  "s_add_u32 s92, s92, 1\n\t" \
  "s_sleep 2\n\t" \
  "s_and_b32 s93, s92, 63\n\t" \
  "s_cbranch_scc1 .Lfr%=\n\t" \
  "buffer_load_dword v164, off, %[rs], %[sab] sc1\n\t" \
  "s_waitcnt vmcnt(0)\n\t" \
  "v_readfirstlane_b32 s93, v164\n\t" \
  "s_nop 3\n\t" \
  "s_cmp_eq_u32 s93, 1\n\t" \
  "s_cbranch_scc1 .Lfo%=\n\t" \
  "s_cmp_lt_u32 s92, 0x80000\n\t" \
  "s_cbranch_scc1 .Lfr%=\n\t" \
  "v_mov_b32 v164, 1\n\t" \
  "buffer_store_dword v164, off, %[rs], %[sab] sc1\n\t" \
  "s_waitcnt vmcnt(0)\n\t" \
  "s_branch .Lfo%=\n\t" \
  ".Lfr%=:\n\t" \
  "buffer_load_dwordx2 v[166:167], v173, %[rs], 0 offen sc1\n\t" \
  "s_branch .Lfc%=\n\t" \
  ".Lb1%=:\n\t" \
  "s_waitcnt vmcnt(0) lgkmcnt(0)\n\t" \
  "s_barrier\n\t" \
  "s_branch .Lb2%=\n\t" \
  ".Lbl%=:\n\t" \
  "s_bitset1_b64 %[ps], %[step]\n\t" \
  "s_cmp_lt_u32 s92, 3\n\t" \
  "s_cbranch_scc1 .Lbm%=\n\t" \
  "s_sleep 12\n\t" \
  ".Lbm%=:\n\t" \
  "s_add_u32 s92, s92, 1\n\t" \
  "s_sleep 2\n\t" \
  "s_and_b32 s93, s92, 63\n\t" \
  "s_cbranch_scc1 .Lba%=\n\t" \
  "buffer_load_dword v164, off, %[rs], %[sab] sc1\n\t" \
  "s_waitcnt vmcnt(0)\n\t" \
  "v_readfirstlane_b32 s93, v164\n\t" \
  "s_nop 3\n\t" \
  "s_cmp_eq_u32 s93, 1\n\t" \
  "s_cbranch_scc1 .Lgo%=\n\t" \
  "s_cmp_lt_u32 s92, 0x80000\n\t" \
  "s_cbranch_scc1 .Lba%=\n\t" \
  "v_mov_b32 v164, 1\n\t" \
  "buffer_store_dword v164, off, %[rs], %[sab] sc1\n\t" \
  "s_waitcnt vmcnt(0)\n\t" \
  "s_branch .Lgo%=\n\t" \
  ".Lba%=:\n\t" \
  "buffer_load_dwordx2 v[166:167], v173, %[rs], 0 offen sc1\n\t" \
  "s_branch .Lcal%=\n\t" \
  ".Lslow%=:\n\t" \
  "s_mov_b64 s[94:95], 0\n\t" \
  "v_cmp_eq_u32 vcc, %[sent], v128\n\t" \
  "s_or_b64 s[94:95], s[94:95], vcc\n\t" \
  "v_cmp_eq_u32 vcc, %[sent], v129\n\t" \
  "s_or_b64 s[94:95], s[94:95], vcc\n\t" \
  "v_cmp_eq_u32 vcc, %[sent], v130\n\t" \
  "s_or_b64 s[94:95], s[94:95], vcc\n\t" \
  "v_cmp_eq_u32 vcc, %[sent], v131\n\t" \
  "s_or_b64 s[94:95], s[94:95], vcc\n\t" \
  "v_cmp_eq_u32 vcc, %[sent], v132\n\t" \
  "s_or_b64 s[94:95], s[94:95], vcc\n\t" \
  "v_cmp_eq_u32 vcc, %[sent], v133\n\t" \
  "s_or_b64 s[94:95], s[94:95], vcc\n\t" \
  "v_cmp_eq_u32 vcc, %[sent], v134\n\t" \
  "s_or_b64 s[94:95], s[94:95], vcc\n\t" \
  "v_cmp_eq_u32 vcc, %[sent], v135\n\t" \
  "s_or_b64 s[94:95], s[94:95], vcc\n\t" \
  "v_cmp_eq_u32 vcc, %[sent], v136\n\t" \
  "s_or_b64 s[94:95], s[94:95], vcc\n\t" \
  "v_cmp_eq_u32 vcc, %[sent], v137\n\t" \
  "s_or_b64 s[94:95], s[94:95], vcc\n\t" \
  "v_cmp_eq_u32 vcc, %[sent], v138\n\t" \
  "s_or_b64 s[94:95], s[94:95], vcc\n\t" \
  "v_cmp_eq_u32 vcc, %[sent], v139\n\t" \
  "s_or_b64 s[94:95], s[94:95], vcc\n\t" \
  "v_cmp_eq_u32 vcc, %[sent], v140\n\t" \
  "s_or_b64 s[94:95], s[94:95], vcc\n\t" \
  "v_cmp_eq_u32 vcc, %[sent], v141\n\t" \
  "s_or_b64 s[94:95], s[94:95], vcc\n\t" \
  "v_cmp_eq_u32 vcc, %[sent], v142\n\t" \
  "s_or_b64 s[94:95], s[94:95], vcc\n\t" \
  "v_cmp_eq_u32 vcc, %[sent], v143\n\t" \
  "s_or_b64 s[94:95], s[94:95], vcc\n\t" \
  "s_cbranch_scc0 .Lok%=\n\t" \
  "s_bitset1_b64 %[fm], %[step]\n\t" \
  "s_add_u32 s92, s92, 1\n\t" \
  "s_sleep 1\n\t" \
  "s_and_b32 s93, s92, 63\n\t" \
  "s_cbranch_scc1 .Lre%=\n\t" \
  "buffer_load_dword v164, off, %[rs], %[sab] sc1\n\t" \
  "s_waitcnt vmcnt(0)\n\t" \
  "v_readfirstlane_b32 s93, v164\n\t" \
  "s_nop 3\n\t" \
  "s_cmp_eq_u32 s93, 1\n\t" \
  "s_cbranch_scc1 .Lok%=\n\t" \
  "s_cmp_lt_u32 s92, 0x80000\n\t" \
  "s_cbranch_scc1 .Lre%=\n\t" \
  "v_mov_b32 v164, 1\n\t" \
  "buffer_store_dword v164, off, %[rs], %[sab] sc1\n\t" \
  "s_waitcnt vmcnt(0)\n\t" \
  "s_branch .Lok%=\n\t" \
  ".Lre%=:\n\t" \
  "buffer_load_dwordx4 v[128:131], v234, %[rs], %[so] offen sc1\n\t" \
  "s_nop 15\n\t" \
  "buffer_load_dwordx4 v[132:135], v235, %[rs], %[so] offen sc1\n\t" \
  "s_nop 15\n\t" \
  "buffer_load_dwordx4 v[136:139], v236, %[rs], %[so] offen sc1\n\t" \
  "s_nop 15\n\t" \
  "buffer_load_dwordx4 v[140:143], v237, %[rs], %[so] offen sc1\n\t" \
  "v_mov_b64 v[144:145], 0\n\t" \
  "v_mov_b64 v[146:147], 0\n\t" \
  "v_mov_b64 v[148:149], 0\n\t" \
  "v_mov_b64 v[150:151], 0\n\t" \
  "v_mov_b64 v[152:153], 0\n\t" \
  "v_mov_b64 v[154:155], 0\n\t" \
  "v_mov_b64 v[156:157], 0\n\t" \
  "v_mov_b64 v[158:159], 0\n\t" \
  "s_waitcnt vmcnt(0)\n\t" \
  "s_branch .Lw%=\n\t" \
  ".Lend%=:\n\t"

__device__ __forceinline__ bf16x8 get4(const u32x32& W, int i) {
  return as_bf16x8((u32x4){W[4 * i], W[4 * i + 1], W[4 * i + 2], W[4 * i + 3]});
}
__device__ __forceinline__ int look_prog(__amdgpu_buffer_rsrc_t rs, int off) {
  asm volatile("" ::: "memory");
  const int v = (int)ld_flag(rs, off);
  return min(__builtin_amdgcn_readlane(v, 0), __builtin_amdgcn_readlane(v, 1));
}
__device__ __forceinline__ int wait_prog(__amdgpu_buffer_rsrc_t rs, int off, int need, gu32* abortw) {
  int v;
  for (unsigned spins = 0;;) {
    v = look_prog(rs, off);
    if (v >= need) break;
    __builtin_amdgcn_s_sleep(4);
    if (spin_fail(spins, abortw)) break;
  }
  asm volatile("" ::: "memory");
  return v;
}
#define CLB_S "s90", "s91", "s92", "s93", "s94", "s95", "vcc", "scc", "memory"
#define W_IN "{v[0:31]}"(W[0]), "{v[32:63]}"(W[1]), "{v[64:95]}"(W[2]), "{v[96:127]}"(W[3]), "{v[128:159]}"(W[4]), "{v[160:191]}"(W[5])

#define W_DELAY 60
#define LEAD0 378
#define LEAD_D0 6
#define QBJ 3
constexpr int L_MEM = 0, L_KT = 16384, L_QX = 24576, L_KX = 26624, L_VX = 16384, L_MISC = 30720, L_WL = 32768;

#define QK_STEP(TXT)                                                                                                        \
  asm volatile(TXT                                                                                                          \
      : [tout] "=&s"(tout), [spins] "=&s"(spins), [tb1] "=&s"(tb1), [tgt] "=&s"(tgt), "+{v248}"(aL16), "+{v249}"(aMW), "+{v250}"(aWL),                            \
        "+{v251}"(aIDX), "+{v252}"(aQW), "+{v[232:235]}"(QF)                                                                 \
      : W_IN, [rs] "s"(rs4), [som] "s"(o_mem(pm, s) + WV * 1024), [som2] "s"(o_mem(pm, s) + WV * 1024 + 16384), [sent] "s"(DSENT), [sab] "s"(SY_ABORT), \
        [lead] "s"(lead), [step] "s"(s), [sbc] "s"(SY_BA + side(pm, 1) + h * 128), [nh] "s"(0xFF00FF00FF00FF00ull), [up] "s"(0xFFFFFFFF00000000ull), [negc] "s"(-cexp),      \
        [c] "s"(cexp), [so] "s"(o_o(pm, s, h) + WV * 512), [sk] "s"(o_v(pm, s, h) + 2048 + (WV & 3) * 512),                    \
        [sv] "s"(o_v(pm, s, h) + WV * 512), [rt] "s"(rt4), [svt] "s"(t_off(p, T_V + kseg(s, h)) + WV * 2048)                  \
      : CLB8(192, 193, 194, 195, 196, 197, 198, 199), CLB8(200, 201, 202, 203, 204, 205, 206, 207),                          \
        CLB8(208, 209, 210, 211, 212, 213, 214, 215), CLB8(216, 217, 218, 219, 220, 221, 222, 223),                          \
        CLB8(224, 225, 226, 227, 228, 229, 230, 231), CLB8(236, 237, 238, 239, 240, 241, 242, 243), "v244", "v245", "v246", "v247", "v253", CLB_S)

template <int WV>
__device__ __forceinline__ void qk_wave(Args& p, int h, char* smem) {
  const int tid = threadIdx.x;
  unsigned pm = 0;
  int have = -1;
  u32x32 W[6];
  {
    int lane = tid & 63;
    asm volatile("" : "+v"(lane));
    const unsigned vo = (WV * 4 + (lane >> 4)) * (HD * 2) + 16 * ((lane & 15) ^ swzB((WV * 4 + (lane >> 4)) & 15));
    auto stage = [&](int j) {
#pragma unroll
      for (int mm = 0; mm < 2; ++mm) {
        const char* src = (const char*)((mm ? p.Wk : p.Wq) + (size_t)j * 128 * HD + h * DH);
        const unsigned dst = lds_off(smem) + (~j & 1) * 65536 + mm * 32768 + WV * 1024;
        glds2(src, 32 * HD * 2, vo, dst);
        glds2(src + 64 * HD * 2, 32 * HD * 2, vo, dst + 16384);
      }
    };
    bf16x8 keep[4][4];
    auto place = [&](int g) {
#pragma unroll
      for (int i = 0; i < 4; ++i)
        *reinterpret_cast<bf16x8*>(smem + (g == 0 ? 65536 : g == 1 ? 32768 : g == 2 ? 131072 : 98304) + WV * 4096 + i * 1024 + lane * 16) = keep[g][i];
    };
    stage(0); stage(1);
    Boot bt;
#pragma unroll
    for (int j = 0; j < 8; ++j) {
      if (j < 7) WAIT_V(8); else WAIT_V(0);
      if (WV == 7 && j == QBJ + 1) pm = boot_done(p.ws, bt);
      LBAR();
#pragma unroll
      for (int k = 0; k < 8; ++k) {
        const int P = (QK_MAP[WV] >> (4 * (2 * j + (k >> 2)))) & 15;
        const bf16x8 fr = ld_bfrag(smem + (~j & 1) * 65536 + (WV >> 2) * 32768 + (k >> 2) * 16384, k & 1, 2 * (WV & 3) + ((k >> 1) & 1), lane);
        const int f = P * 4 + (k & 3);
        if (P < 12) put4(W[f >> 3], f & 7, fr);
        else keep[P - 12][k & 3] = fr;
      }
      if (j == (WV >> 1)) place(2);
      LBAR();
      if (WV == 7 && j == QBJ) boot_ask(p.ws, bt);
      if (j + 2 < 8) stage(j + 2);
      if (j == 6) { place(0); place(3); }
    }
    place(1);
    if (WV == 7 && lane < 8) lds_at(smem + L_MISC)[lane] = lane == 7 ? pm : (lane == 1 ? now_q() : 0);
    if (WV == 6) lds_at(smem + 28672)[lane] = ~0u;
    LBAR();
    pm = uni(lds_at(smem + L_MISC)[7]);
    p.s0 = pm >> 31;
    if (WV == 6) RDW(h);
    if (WV == 4 || WV == 5) {
      have = wait_prog(mkrsrc(p.ws + WS_SY, SY_TOTAL), fkq(h, (lane & 1) * 2), 0, (gu32*)(p.ws + WS_SY + SY_ABORT));
      const char* src = p.ws + WS_T + t_off(p, kseg(0, h)) + (WV - 4) * 4096 + lane * 16;
#pragma unroll
      for (int i = 0; i < 4; ++i) glds16_sc1(src + i * 1024, smem + L_KT + (WV - 4) * 4096 + i * 1024);
    }
  }
  const __amdgpu_buffer_rsrc_t rsy = mkrsrc(p.ws + WS_SY, SY_TOTAL);
  const u32x4 rs4 = raw_rsrc(p.ws + WS_SY, SY_TOTAL);
  const u32x4 rt4 = raw_rsrc(p.ws + WS_T, T_BYTES);
  gu32* abortw = (gu32*)(p.ws + WS_SY + SY_ABORT);
  const float cexp = 0.08838834764831845f * 1.4426950408889634f;
  constexpr int m = WV >> 2, b = WV & 3;
  int lead = LEAD0;
  unsigned tb1 = 0;
  unsigned long long fm = 0, ps = 0; (void)fm; (void)ps;
  for (int s = 0; s < NSEG; ++s) {
    int lane = tid & 63;
    asm volatile("" : "+v"(lane));
    const int r = lane & 15, q = lane >> 4;
    unsigned aL16 = lane * 16, aMW = aL16 + WV * 1024, aWL = aL16 + 98304 + WV * 4096;
    unsigned aIDX = (lane & 2) ? 0xEEEE : 0x4444;
    unsigned aQW = (32 * b + r) * 16 + q * 4 + (m ? L_KX : L_QX);
    u32x4 QF;
    asm volatile("" : "={v[232:235]}"(QF));
    if (WV < 4) {
      QF[0] = L_QX + (q * 8 + (r >> 2)) * 16 + (r & 1) * 8;
      QF[1] = ((2 * (q & 1) + (q >> 1)) * 16 + r) * 4;
      QF[2] = ((r >> 2) & 1) * 65536 + ((r >> 1) & 1) * 64 + (r >> 3) * 32 + (r & 1) * 16 + q * 4;
    } else QF[3] = r * 16 + q * 4;
    unsigned tout, spins, tgt;
    if (WV == 0) QK_STEP(QK_ASM_0); else if (WV == 1) QK_STEP(QK_ASM_1); else if (WV == 2) QK_STEP(QK_ASM_2);
    else if (WV == 3) QK_STEP(QK_ASM_3); else if (WV == 4) QK_STEP(QK_ASM_4); else if (WV == 5) QK_STEP(QK_ASM_5);
    else if (WV == 6) QK_STEP(QK_ASM_6); else QK_STEP(QK_ASM_7);
    tout = __builtin_amdgcn_readfirstlane(tout);
    tb1 = __builtin_amdgcn_readfirstlane(tb1);
    lane = tid & 63;
    asm volatile("" : "+v"(lane));
    if (WV < 4) {
      lds_at(smem + L_MISC + 8)[WV] = s + 1;
      if (WV == 3) lds_at(smem + L_MISC)[6] = time_of(tout);
    }
    if (WV >= 4 && s + 1 < NSEG) {
      unsigned t;
      do {
        __builtin_amdgcn_s_sleep(1);
        t = lds_at(smem + L_MISC + 8)[lane & 3];
      } while (!__all(t == (unsigned)(s + 1)));
      if (WV <= 5) {
        if (s + 2 < NSEG) {
          if (have < s + 1) have = wait_prog(rsy, fkq(h, (lane & 1) * 2), s + 1, abortw);
          const char* src = p.ws + WS_T + t_off(p, kseg(s + 1, h)) + (WV - 4) * 4096 + lane * 16;
#pragma unroll
          for (int i = 0; i < 4; ++i) glds16_sc1(src + i * 1024, smem + L_KT + (WV - 4) * 4096 + i * 1024);
          if (have < s + 2) have = look_prog(rsy, fkq(h, (lane & 1) * 2));
        }
      } else if (WV == 6) {
        watch(rsy, s, lane, now_q() + W_DELAY, (const unsigned*)(smem + L_MISC), abortw, side(pm, 1));
      } else {
        __builtin_amdgcn_s_sleep(6);
        const int x = (int)(__builtin_amdgcn_readfirstlane(lds_at(smem + L_MISC)[6]) - tb1);
        lead = __builtin_amdgcn_readfirstlane(track(lead, x, LEAD_UP, s < 8 ? LEAD_D0 : LEAD_DN, 60));
        SMP(2, s, x); SMP(3, s, lead);
      }
    }
  }
}

__device__ __forceinline__ void qk_role(Args& p, int h, char* smem) {
  switch (__builtin_amdgcn_readfirstlane(threadIdx.x >> 6)) {
    case 0: qk_wave<0>(p, h, smem); break;
    case 1: qk_wave<1>(p, h, smem); break;
    case 2: qk_wave<2>(p, h, smem); break;
    case 3: qk_wave<3>(p, h, smem); break;
    case 4: qk_wave<4>(p, h, smem); break;
    case 5: qk_wave<5>(p, h, smem); break;
    case 6: qk_wave<6>(p, h, smem); break;
    default: qk_wave<7>(p, h, smem); break;
  }
}

#define K_DUE 160
#define T_SL 2
#define O_DUE 400
#define LEAD_V 200

constexpr int V_VM = 16384, V_T = 32768, V_KI = 131072;
__device__ __forceinline__ void vu_role(Args& p, int h, char* smem) {
  const int tid = threadIdx.x, lane = tid & 63;
  const int w = __builtin_amdgcn_readfirstlane(tid >> 6);
  u32x32 W[4];
  {
    const unsigned vo = (tid >> 4) * (HD * 2) + 16 * ((lane & 15) ^ swzB((tid >> 4) & 15));
    auto stage = [&](int j) {
      const char* src = (const char*)(p.Wv + (size_t)j * 128 * HD + h * DH);
      const unsigned dst = lds_off(smem) + L_WL + (j & 3) * 32768 + w * 1024;
      glds2(src, 32 * HD * 2, vo, dst);
      glds2(src + 64 * HD * 2, 32 * HD * 2, vo, dst + 16384);
    };
    start_sy(p.ws, p.mem0, h, smem);
#pragma unroll
    for (int j = 0; j < 4; ++j) stage(j);
    Boot bt;
#pragma unroll
    for (int j = 0; j < 8; ++j) {
      if (j < 5) WAIT_V(12); else if (j == 5) WAIT_V(8); else if (j == 6) WAIT_V(4); else WAIT_V(0);
      if (j == 7 && w == 7) {
        const unsigned m = boot_done(p.ws, bt);
        if (lane < 8) lds_at(smem + L_MISC)[lane] = lane == 7 ? m : (lane == 1 ? now_q() : 0);
      }
      LBAR();
#pragma unroll
      for (int k = 0; k < 4; ++k) put4(W[j >> 1], (j & 1) * 4 + k, ld_bfrag(smem + L_WL + (j & 3) * 32768 + (k >> 1) * 16384, k & 1, w, lane));
      LBAR();
      if (j + 4 < 8) stage(j + 4);
      if (j == 5 && w == 7) boot_ask(p.ws, bt);
    }
    RDY(32 + h);
  }
  const __amdgpu_buffer_rsrc_t rsy = mkrsrc(p.ws + WS_SY, SY_TOTAL);
  const u32x4 rs4 = raw_rsrc(p.ws + WS_SY, SY_TOTAL);
  gu32* abortw = (gu32*)(p.ws + WS_SY + SY_ABORT);
  const unsigned pm = uni(lds_at(smem + L_MISC)[7]);
  p.s0 = pm >> 31;
  unsigned long long fm = 0; (void)fm;
  int have = -1;
  auto fetch = [&](int sg) {
    if (have < sg) have = wait_prog(rsy, fkq(h, w - 4), sg, abortw);
    const char* src = p.ws + WS_T + t_off(p, (w - 4) * T_Q + kseg(sg, h)) + lane * 16;
#pragma unroll
    for (int i = 0; i < 8; ++i) glds16_sc1(src + i * 1024, smem + V_T + (sg & 3) * 24576 + (w - 4) * 8192 + i * 1024);
  };
  const int fo = SY_FO + ((w & 3) * NH + h) * 128;
  typedef __attribute__((ext_vector_type(8))) float f32x8;
  f32x8 bias;
  u32x4 ta;
  {
    const int r = lane & 15, q = lane >> 4;
#pragma unroll
    for (int j = 0; j < 8; ++j) bias[j] = (j >> 2) * 16 + 4 * q + (j & 3) <= 16 * (w & 1) + r ? INFINITY : -INFINITY;
    ta[0] = V_KI + w * 2048 + lane * 16;
    ta[1] = V_KI + w * 2048 + (q * 8 + (r >> 2)) * 16 + (r & 1) * 8;
    ta[2] = V_VM + (w & 2) * 512 + ((2 * (q & 1) + (q >> 1)) * 16 + r) * 4;
    ta[3] = 0;
  }
#pragma unroll 1
  for (int s = 0; s < NSEG; ++s) {
    unsigned spins, tgt;
    asm volatile(VV_ASM
        : [spins] "=&s"(spins), [tgt] "=&s"(tgt)
        : W4_IN, "{v200}"(lane * 16), "{v201}"(lane * 16 + w * 2048), "{v202}"((lane & 2) ? 0xEEEE : 0x4444), "{v203}"(lane * 4),
          "{v204}"(tid * 4 + V_VM + (s & 1) * 2048), [rs] "s"(rs4), [som] "s"(o_mem(pm, s) + (w >> 2) * 16384 + (w & 3) * 2048), [step] "s"(s),
          [sent] "s"(DSENT), [sab] "s"(SY_ABORT), [sv] "s"(o_v(pm, s, h) + w * 256)
        : CLB32(128), CLB32(160), CLB8(192, 193, 194, 195, 196, 197, 198, 199), CLB_S);
    if (w < 4 || s == NSEG - 1) {
      const int sg = s - (w < 4);
      if (w >= 4) pace_until(now_q() + K_DUE);
      if (sg >= 0) {
        asm volatile(TK_ASM
            :
            : "{v200}"(lane * 16), "{v[208:215]}"(bias), "{v[216:219]}"(ta),
              [rs] "s"(rs4), [rt] "s"(raw_rsrc(p.ws + WS_T, T_BYTES)), [sent] "s"(DSENT), [sab] "s"(SY_ABORT), [sk] "s"(o_v(pm, sg, h) + 2048),
              [tl] "s"(V_T + (sg & 3) * 24576), [vp] "s"((sg & 1) * 2048), [tq] "s"((w & 1) * 4096), [tv] "s"((w & 2) * 2048),
              [so] "s"(t_off_o(p, T_O + (sg * 2 + (w & 1)) * 16 + (h >> 1)) + (h & 1) * 4096 + (w & 2) * 1024),
              [up] "s"(0xFFFFFFFF00000000ull), [c] "s"(1.4426950408889634f), [negc] "s"(-1.4426950408889634f),
              [sc] "s"((unsigned long long)__float_as_uint(0.08838834764831845f)), [od] "s"(s < NSEG - 1 ? O_DUE : 0)
            : CLB32(128), CLB32(160), CLB8(192, 193, 194, 195, 196, 197, 198, 199), CLB8(224, 225, 226, 227, 228, 229, 230, 231),
              "v205", "v206", "v207", "s88", "s89", CLB_S);
        if (sg >= NSEG - 3) {
          static_assert(TLAG == 2, "");
          WAIT_V(0);
          if (lane == 0) __builtin_amdgcn_raw_buffer_store_b32(1u, rsy, fo + (sg - NSEG + 3) * 4 * NH * 128, 0, 16);
        }
      }
    } else if (w == 7) {
      SMP(6, s, (int)(now_q() - tgt));
      watch(rsy, s, lane, now_q() + LEAD_V + W_DELAY, (const unsigned*)(smem + L_MISC), abortw, side(pm, 1));
    } else if (w >= 4) {
      __builtin_amdgcn_s_sleep(T_SL);
      const int s1 = w == 5 ? (s == NSEG - 2 ? s + 1 : s) : s + 1;
#pragma unroll 1
      for (int sg = (w == 5 || s == 0) ? s : s + 1; sg <= s1; ++sg) fetch(sg);
      if (have < s1 + 1 && s1 + 1 < NSEG) have = look_prog(rsy, fkq(h, w - 4));
      if (w == 5 && s + NPRE < NSEG) {
#pragma unroll
        for (int i = 0; i < 4; ++i) st_sc1(rsy, o_v(pm, s + NPRE, h) + i * 1024 + lane * 16, (u32x4){DSENT, DSENT, DSENT, DSENT});
      }
    }
  }
  if (h == 3 && (w & 2) == 0) { LST(351 + (w & 1) + (w >> 2) * 2); }
}

#define RL_UP 12
#define RL_DN 4
#define RED_MIN 40
#define BA_LOOK 310
constexpr int R_SI = 32768, R_PART = 98304;

__device__ __forceinline__ void red_role(Args& p, int j, char* smem) {
  const int tid = threadIdx.x;
  int lane = tid & 63;
  const int w = __builtin_amdgcn_readfirstlane(tid >> 6);
  const int c = (j & 2) * 8 + (j >> 2), rh = j & 1;
  const int hg = (w + (j >> 2)) & 7, rot = (j >> 2) + (j >> 5);
  const __amdgpu_buffer_rsrc_t rsy = mkrsrc(p.ws + WS_SY, SY_TOTAL);
  const u32x4 rs4 = raw_rsrc(p.ws + WS_SY, SY_TOTAL);
  const __amdgpu_buffer_rsrc_t rt = mkrsrc(p.ws + WS_T, T_BYTES);
  gu32* abortw = (gu32*)(p.ws + WS_SY + SY_ABORT);
  u32x32 W[4];
  u32x2 tag = {0u, 0u};
  {
    const u16* Wo = p.Wo + (size_t)(4 * hg * DH) * DM + 32 * c;
    const int ro = ((lane >> 4) * 8 + ((lane & 15) >> 2)) * 64 + (lane & 3) * 8;
    Boot bt;
    load_blocks<16>(smem + 32768 + w * 16384,
        [&](int i, char* slot) {
          if (i == 12 && w == 0) boot_ask(p.ws, bt);
          if ((i < 2 || i == 4 || i == 8) && (c & 14) != 6 && lane < 10) {
            const int x = i < 2 ? 3 - i : i >> 3, n = min(((c & 15) - ((c & 8) >> 2)) * 10 + lane, 127);
            touch_line((const char*)p.Wo + (size_t)(w * 512 + (x >> 1) * 256 + (x & 1) * 64 + (n & 64) + n) * (DM * 2) + (c & 16) * 64 + 384, smem + w * 256);
          }
          const int x = ((i >> 2) + rot) & 3;
          blk_dma(Wo + (size_t)((x >> 1) * 256 + (x & 1) * 64 + ((i >> 1) & 1) * 128 + (i & 1) * 32) * DM, DM, slot, lane);
        },
        [&](int i, char* slot) {
#pragma unroll
          for (int t = 0; t < 2; ++t) {
            s16x4 lo = __builtin_amdgcn_ds_read_tr16_b64_v4i16((lds_s16x4_ptr)(slot + ro + t * 32));
            s16x4 hi = __builtin_amdgcn_ds_read_tr16_b64_v4i16((lds_s16x4_ptr)(slot + ro + t * 32 + 256));
            put4(W[i >> 2], (i & 2) * 2 + 2 * t + (i & 1), (bf16x8){lo[0], lo[1], lo[2], lo[3], hi[0], hi[1], hi[2], hi[3]});
          }
        });
    if (w == 0) { lds_at(smem + 4128)[0] = boot_done(p.ws, bt); tag = next_tag(bt.e); }
    LBAR();
  }
  const unsigned pm = uni(lds_at(smem + 4128)[0]);
  p.s0 = pm >> 31;
  const int hs = side(pm, 1);
  if (((j >> 2) & 14) != 6 && GPF == 2) {
#pragma unroll
    for (int i = 0; i < 4; ++i)
      touch_line((const char*)(i < 2 ? p.wg : p.wu) + (size_t)(j - (j >= 32 ? 8 : 0)) * 131072 + ((i & 1) * 8 + w) * 8192 + lane * 128, smem + w * 256);
  }
  {
    const u32x4 ds = {DSENT, DSENT, DSENT, DSENT};
#pragma unroll
    for (int i = 0; i < 2; ++i) {
      const int s = (w * 2 + i) * 4 + (lane >> 4);
      if (s >= 2) st_sc1(rsy, o_mem(pm, s) + (c >> 4) * 16384 + (c & 15) * 512 + rh * 128 + ((lane >> 3) & 1) * 256 + (lane & 7) * 16, ds);
    }
  }
  if (tid < 4) { reinterpret_cast<unsigned*>(smem + 4096)[tid] = tid == 3 ? now_q() : 0; reinterpret_cast<unsigned*>(smem + 4112)[tid] = DSENT; }
  if (tid < 256) reinterpret_cast<int*>(smem + 5120)[tid] = ((tid >> 2) - TLAG - 6) >> 2;
  LBAR();
  const unsigned base = __builtin_amdgcn_readfirstlane(lds_at(smem + 4096)[3]);
  RDY(64 + j);
  unsigned long long fm = 0, ps = 0;
  const unsigned aL = lane * 16;
  const int e = ((56 + w) * SEG + rh * 16 + (lane >> 2)) * DM + c * 32 + (lane & 3) * 8;
  u32x4 xv = *reinterpret_cast<const u32x4*>(p.x + e), av = {DSENT, DSENT, DSENT, DSENT};
  asm volatile("" : "+v"(xv));
  {
    const int r = lane & 15, q = lane >> 4;
    const int sm0 = o_mem(pm, 0) + (c >> 4) * 16384 + (c & 15) * 512 + rh * 128;
    u32x4 VO, VT;
    unsigned fo = SY_FO + ((rh + (lane & 4) / 2) * NH + 4 * hg + (lane & 3)) * 128;
    unsigned ao = w == 1 ? (c * S_LEN + rh * 16 - (TLAG + 3) * SEG) * 64 + lane * 16
                         : (w >= 4 ? SY_O + NPRE * 131072 + side(pm, w - 2) + (j >> 3) * 16384 + (j & 7) * 1024 + lane * 16
                                   : SY_FS + (2 * c + rh) * 128 + (lane < 8 ? lane * 16 : 0x70000000));
    unsigned ar = w == 1 ? R_SI - (TLAG + 2) * 1024 + lane * 16 : (w == 2 ? 5120 : 4112);
    const unsigned long long am = w == 1 ? ((1ull << 56) - 1) << (TLAG + 3) : (w == 2 ? 0x11111111111111ull << (TLAG + 7) : (w >= 4 ? (0xFFFFull << (16 * (w - 4))) >> NPRE : 0));
    unsigned pw = R_PART + w * 2048 + lane * 16, pr = R_PART + 16384 + tid * 4;
    unsigned sw = R_SI - (TLAG + 1) * 1024 + (lane >> 2) * 64 + w * 8 + (lane & 3) * 2, stm = 8192 + w * 512;
#pragma unroll
    for (int l = 0; l < 4; ++l) {
      const int x = (l + rot) & 3;
      VO[l] = o_o(0, 0, 4 * hg) + rh * 65536 + x * 1024 + aL;
      VT[l] = (T_O + rh * 16 + 2 * hg + (x >> 1)) * 16384 + (x & 1) * 2048 + aL;
    }
    unsigned ba = lane < 4 ? SY_BA + hs + (4 * hg + lane) * 128 : 0x7F000000u;
    int rl = 280;
    unsigned step, E, so, st, st4, sh;
    asm volatile(RED_ASM
        : [step] "=&s"(step), [E] "=&s"(E), [so] "=&s"(so), [st] "=&s"(st), [st4] "=&s"(st4), [sh] "=&s"(sh),
          [rl] "+s"(rl), [fm] "+s"(fm), [ps] "+s"(ps),
          "+{v[224:227]}"(av), "+{v228}"(ao), "+{v229}"(ar), "+{v230}"(pw), "+{v231}"(pr), "+{v232}"(sw), "+{v233}"(stm), "+{v242}"(fo), "+{v173}"(ba)
        : W4_IN, "{v168}"(aL), "{v169}"(w * 512 + lane * 8), "{v170}"(lane * 8),
          "{v171}"(sm0 + 32768 + (q >> 1) * 256 + (q & 1) * 64 + ((r >> 1) & 1) * 32 + (r & 1) * 16 + (r >> 2) * 4),
          "{v172}"((lane & 2) ? 0xEEEE : 0x4444),
          "{v174}"((r & 1) ? 0x03020706u : 0x05040100u), "{v175}"(SY_BR + hs + j * 128 + lane * 8), "{v[234:237]}"(VO), "{v[238:241]}"(VT), "{v243}"(e * 2),
          [rg] "s"(raw_rsrc(p.ws + WS_G, S_LEN * DM * 2)), [fg] "s"(SY_FG + ((14 + (w >> 2)) * 8 + (c >> 2)) * 128),
          [rs] "s"(rs4), [ra] "s"(w == 1 ? raw_rsrc(p.ws + WS_S, S_LEN * DM * 2) : rs4), [rt] "s"(raw_rsrc(p.ws + WS_T, T_BYTES)),
          [ds] "n"(DS_RED), [mn] "n"(RED_MIN), [lk] "n"(DS_MEM + BA_LOOK), [up] "n"(RL_UP), [dn] "n"(-RL_DN), [sent] "s"(DSENT), [sab] "s"(SY_ABORT),
          [base] "s"(base), [pm] "s"(pm), [s0] "s"((p.s0 ^ TF_O) << 13), [am] "s"(am), [ainc] "s"(w == 1 ? SEG * 64 : (w >= 4 ? 131072 : 0)),
          [rinc] "s"(w == 1 ? 1024 : (w == 2 ? 16 : 0))
        : CLB32(128), CLB8(160, 161, 162, 163, 164, 165, 166, 167), CLB8(176, 177, 178, 179, 180, 181, 182, 183),
          CLB8(184, 185, 186, 187, 188, 189, 190, 191), CLB8(192, 193, 194, 195, 196, 197, 198, 199), CLB8(200, 201, 202, 203, 204, 205, 206, 207),
          CLB8(208, 209, 210, 211, 212, 213, 214, 215), CLB8(216, 217, 218, 219, 220, 221, 222, 223), CLB_E, "s96", CLB_S);
  }
  if (j == 0 && w == 5) { LST(345); }
  LBAR();
  if (j == 0 && w == 5) { LST(346); }
  u32x4 sv;
  if (w < 6 - TLAG) sv = *reinterpret_cast<const u32x4*>(smem + R_SI + (56 + w) * 1024 + lane * 16);
  else {
    const f32x4* part = reinterpret_cast<const f32x4*>(smem + (w == 6 - TLAG ? R_PART : (w - 6 + TLAG) * 16384)) +
                        (lane & 2) * 32 + (lane & 1) * 32 + (lane >> 2);
    f32x4 x[8][2];
#pragma unroll
    for (int ww = 0; ww < 8; ++ww) { x[ww][0] = part[ww * 128]; x[ww][1] = part[ww * 128 + 16]; }
#pragma unroll
    for (int u = 0; u < 2; ++u) {
      const f32x4 a = ((x[0][u] + x[1][u]) + (x[2][u] + x[3][u])) + ((x[4][u] + x[5][u]) + (x[6][u] + x[7][u]));
      sv[2 * u] = cvtpk(a[0], a[1]); sv[2 * u + 1] = cvtpk(a[2], a[3]);
    }
  }
  st_out(p, e, combine(sv, av, xv));
  if (j == 0 && tid == 0) st2_sc1(rsy, SY_EP, tag);
  if (tid == 128) { LST(400 + j); }
}

__global__ __launch_bounds__(512, 2) void main_kernel(Args p) {
  char* smem = g_smem;
  p.ws = ws_align(p.ws);
  const int bx = blockIdx.x & 7, bk = blockIdx.x >> 3;
  const bool isg = (bx ^ XPROBE) & 4;
  const int b = (isg ? NCHAIN : 0) + bk * 4 + (bx & 3);
  if (b == 64 && threadIdx.x == 0) { LST(500); }
  if (threadIdx.x < 64) {
    unsigned long long pc; unsigned len, keep;
    asm volatile("s_getpc_b64 %0\n.Lpc%=:\n\ts_mov_b32 %1, .Lcode_end-.Lpc%=" : "=s"(pc), "=s"(len));
    asm volatile("s_mov_b32 %0, m0\n\ts_mov_b32 m0, %3\n\ts_nop 0\n\tbuffer_load_dword %1, %2, 0 offen lds\n\ts_mov_b32 m0, %0"
                 : "=&s"(keep) : "v"((bk * 40 + threadIdx.x) * 128), "s"(raw_rsrc((const void*)pc, len)), "s"((b >> 6) == 1 ? 2048 : 163328) : "memory");
  }
  if (b >= NCHAIN) gemm_role(p, b - NCHAIN, smem);
  else if (b < 32) qk_role(p, (b & 3) * 8 + bk, smem);
  else if (b < 64) vu_role(p, (b & 3) * 8 + bk - 8, smem);
  else red_role(p, b - 64, smem);
}

extern "C" void kernel_launch(void* const* d_in, const int* in_sizes, int n_in,
                              void* d_out, int out_size, void* d_ws, size_t ws_size,
                              hipStream_t stream) {
  Args a;
  a.x = (const u16*)d_in[0]; a.mem0 = (const u16*)d_in[1];
  a.Wq = (const u16*)d_in[2]; a.Wk = (const u16*)d_in[3]; a.Wv = (const u16*)d_in[4]; a.Wo = (const u16*)d_in[5];
  a.wg = (const u16*)d_in[8]; a.wu = (const u16*)d_in[9]; a.wd = (const u16*)d_in[10];
  a.ws = (char*)d_ws; a.out = (u16*)d_out; a.s0 = 0; a.pad1 = 0;
  main_kernel<<<256, 512, 0, stream>>>(a);
}
```

```cpp
#include <hip/hip_runtime.h>
#include <hip/hip_bf16.h>
#include <stdint.h>
#include <type_traits>

constexpr int S_LEN = 2048, DM = 1024, NH = 32, DH = 128, NMEM = 8, SEG = 32;
constexpr int NSEG = S_LEN / SEG;
constexpr int HD = NH * DH;
constexpr int FF = 3584;

typedef unsigned short u16;
typedef __attribute__((ext_vector_type(8))) short bf16x8;
typedef __attribute__((ext_vector_type(4))) short s16x4;
typedef __attribute__((ext_vector_type(4))) float f32x4;
typedef __attribute__((ext_vector_type(4))) unsigned int u32x4;
typedef __attribute__((ext_vector_type(2))) unsigned int u32x2;
typedef __attribute__((address_space(3))) s16x4* lds_s16x4_ptr;
typedef __attribute__((address_space(3))) void* lds_void_ptr;
typedef __attribute__((address_space(1))) const void* glb_cvoid_ptr;
typedef __attribute__((address_space(1))) unsigned gu32;
typedef __bf16 bf16x2_t __attribute__((ext_vector_type(2)));
typedef float f32x2_t __attribute__((ext_vector_type(2)));

__device__ __forceinline__ float bf2f(u16 v) { return __uint_as_float(((unsigned)v) << 16); }
__device__ __forceinline__ unsigned cvtpk(float lo, float hi) {
  bf16x2_t r = __builtin_convertvector((f32x2_t){lo, hi}, bf16x2_t);
  return __builtin_bit_cast(unsigned, r);
}
__device__ __forceinline__ float lo_f(unsigned v) { return __uint_as_float(v << 16); }
__device__ __forceinline__ float hi_f(unsigned v) { return __uint_as_float(v & 0xffff0000u); }
__device__ __forceinline__ float rbf(float f) { return lo_f(cvtpk(f, f)); }
__device__ __forceinline__ bf16x8 as_bf16x8(u32x4 v) { return __builtin_bit_cast(bf16x8, v); }
#define MFMA(a, b, c) __builtin_amdgcn_mfma_f32_16x16x32_bf16((a), (b), (c), 0, 0, 0)
typedef __attribute__((ext_vector_type(16))) short bf16x16;
typedef __attribute__((ext_vector_type(8))) __bf16 bfv8;
typedef __attribute__((ext_vector_type(16))) __bf16 bfv16;
#define SMFMA(a, b, c, idx) __builtin_amdgcn_smfmac_f32_16x16x64_bf16(__builtin_bit_cast(bfv8, (a)), __builtin_bit_cast(bfv16, (b)), (c), (idx), 0, 0)
__device__ __forceinline__ bf16x16 cat16(bf16x8 lo, bf16x8 hi) {
  return __builtin_shufflevector(lo, hi, 0, 1, 2, 3, 4, 5, 6, 7, 8, 9, 10, 11, 12, 13, 14, 15);
}

#define KSTR 65
#define XLD 1024
constexpr int PAGE = 2 << 20, NPAGE = 14;
constexpr int T_K = 0, T_Q = 2304, T_V = 4608, T_O = 6912, T_END = T_O + 2048;
constexpr unsigned T_BYTES = T_END * 16384u;
constexpr size_t WS_SY = 0;
constexpr size_t WS_T = (size_t)NPAGE * PAGE;
constexpr size_t WS_XN = WS_T + T_BYTES;
constexpr size_t WS_HF = WS_XN + (size_t)S_LEN * XLD * 2;
constexpr size_t WS_G = WS_HF + (size_t)S_LEN * 16384;
constexpr size_t WS_S = WS_G + (size_t)S_LEN * DM * 2;
constexpr size_t WS_END = WS_S + (size_t)S_LEN * DM * 2;
constexpr unsigned SENT = 0xFFFFFFFFu;
constexpr unsigned DSENT = 0xFFBFFFBFu;
constexpr int SY_MEM = 0;
constexpr int SY_O = 2 * PAGE;
constexpr int SY_V = 6 * PAGE;
constexpr int SY_BR = PAGE, SY_BA = SY_BR + 16384;
constexpr int SY_FKQ = SY_BR + 32768;
constexpr int SY_FG = SY_FKQ + NH * 4 * 128;
constexpr int SY_FO = SY_FG + 16 * 8 * 128;
constexpr int SY_CH = SY_FO + 384 * 128;
constexpr int SY_ABORT = SY_CH + 8 * 128;
constexpr int SY_FS = SY_ABORT + 256;
constexpr int SY_FEND = SY_FS + 64 * 128;
constexpr int SY_EP = SY_FEND, SY_BT = SY_EP + 128;
constexpr int SY_TOTAL = NPAGE * PAGE;
constexpr int PROBE = PAGE - 16384 + 6144;
__device__ __forceinline__ int side(unsigned pm, int page) { return ((pm >> page) & 1u) << 13; }
__device__ __forceinline__ int o_mem(unsigned pm, int s) { return SY_MEM + s * 32768 + side(pm, 0); }
__device__ __forceinline__ int o_o(unsigned pm, int s, int h) {
  return SY_O + s * 131072 + (h >> 3) * 16384 + ((h >> 1) & 3) * 2048 + (h & 1) * 128 + side(pm, 2 + (s >> 4));
}
__device__ __forceinline__ int o_v(unsigned pm, int s, int h) { return SY_V + s * 262144 + (h >> 1) * 16384 + (h & 1) * 4096 + side(pm, 6 + (s >> 3)); }
__device__ __forceinline__ char* ws_align(char* ws) { return ws + ((PAGE - (unsigned)(uintptr_t)ws) & (PAGE - 1)); }
#define NFUSE 12
constexpr int NPRE = 4;
static_assert(WS_END + PAGE <= (size_t)256 << 20 && NH * KSTR <= T_Q && NPAGE % 2 == 0, "");
constexpr int NCHAIN = 128, NGW = 32;
__host__ __device__ constexpr int kseg(int sg, int h) { return h * KSTR + sg; }
__host__ __device__ constexpr int fkq(int h, int kind) { return SY_FKQ + (h * 4 + kind) * 128; }

#define HSKEW 0
#define XPROBE 0
struct Args {
  const u16 *x, *mem0, *Wq, *Wk, *Wv, *Wo, *wg, *wu, *wd;
  char* ws;
  u16* out;
  int s0, pad1;
};
constexpr int HLD = 16384 + HSKEW;
__device__ __forceinline__ int h_off(const Args& p, int rblk) { return rblk * PAGE + (((p.s0 ^ rblk ^ 1) & 1) << 13); }
static_assert(WS_HF % (2 * PAGE) == 0 && 127 * HSKEW + FF * 2 <= 8192, "");
#define TF_KQV 1
#define TF_O 0
__device__ __forceinline__ int t_off(const Args& p, int t) { return t * 16384 + (((p.s0 ^ (t >> 7) ^ (TF_KQV == 2 ? t : TF_KQV)) & 1) << 13); }
__device__ __forceinline__ int t_off_o(const Args& p, int t) { return t * 16384 + (((p.s0 ^ (t >> 7) ^ (TF_O == 2 ? t : TF_O)) & 1) << 13); }

__device__ __forceinline__ float wave_sum(float v) {
#pragma unroll
  for (int i = 0; i < 4; ++i) {
    const int x = (int)__float_as_uint(v);
    v += __uint_as_float(i == 0 ? __builtin_amdgcn_update_dpp(0, x, 0x128, 0xf, 0xf, false) : i == 1 ? __builtin_amdgcn_update_dpp(0, x, 0x124, 0xf, 0xf, false)
                         : i == 2 ? __builtin_amdgcn_update_dpp(0, x, 0x122, 0xf, 0xf, false) : __builtin_amdgcn_update_dpp(0, x, 0x121, 0xf, 0xf, false));
  }
  auto r = __builtin_amdgcn_permlane16_swap(__float_as_uint(v), __float_as_uint(v), false, false);
  v = __uint_as_float(r[0]) + __uint_as_float(r[1]);
  r = __builtin_amdgcn_permlane32_swap(__float_as_uint(v), __float_as_uint(v), false, false);
  return __uint_as_float(r[0]) + __uint_as_float(r[1]);
}

__shared__ __attribute__((aligned(16))) char g_smem[163840];
__device__ __forceinline__ unsigned lds_off(const void* p) {
  return (unsigned)(uintptr_t)(lds_void_ptr)g_smem + (unsigned)((const char*)p - g_smem);
}
__device__ __forceinline__ __amdgpu_buffer_rsrc_t mkrsrc(const void* p, unsigned bytes) {
  return __builtin_amdgcn_make_buffer_rsrc((void*)p, 0, bytes, 0x00020000);
}
__device__ __forceinline__ u32x4 ld_sc1(__amdgpu_buffer_rsrc_t rs, int off) {
  return __builtin_amdgcn_raw_buffer_load_b128(rs, off, 0, 16);
}
__device__ __forceinline__ void st_sc1(__amdgpu_buffer_rsrc_t rs, int off, u32x4 v) {
  __builtin_amdgcn_raw_buffer_store_b128(v, rs, off, 0, 16);
}
__device__ __forceinline__ unsigned ror8(unsigned v) {
  return (unsigned)__builtin_amdgcn_update_dpp(0, (int)v, 0x128, 0xf, 0xf, false);
}
__device__ __forceinline__ float ror8f(float v) { return __uint_as_float(ror8(__float_as_uint(v))); }
__device__ __forceinline__ void swap16(unsigned& x, unsigned& y) {
  auto r = __builtin_amdgcn_permlane16_swap(x, y, false, false);
  x = r[0]; y = r[1];
}
__device__ __forceinline__ void swap32(unsigned& x, unsigned& y) {
  auto r = __builtin_amdgcn_permlane32_swap(x, y, false, false);
  x = r[0]; y = r[1];
}
__device__ __forceinline__ f32x4 fold32(f32x4 x, f32x4 y) {
  f32x4 o;
#pragma unroll
  for (int j = 0; j < 4; ++j) {
    unsigned a = __float_as_uint(x[j]), b = __float_as_uint(y[j]);
    swap32(a, b);
    o[j] = __uint_as_float(a) + __uint_as_float(b);
  }
  return o;
}
__device__ __forceinline__ bf16x8 tr_frag(const char* img_lane) {
  s16x4 lo = __builtin_amdgcn_ds_read_tr16_b64_v4i16((lds_s16x4_ptr)img_lane);
  s16x4 hi = __builtin_amdgcn_ds_read_tr16_b64_v4i16((lds_s16x4_ptr)(img_lane + 64));
  return (bf16x8){lo[0], lo[1], lo[2], lo[3], hi[0], hi[1], hi[2], hi[3]};
}
__device__ __forceinline__ float rowq_max(float v) {
  unsigned a = __float_as_uint(v), b = a;
  swap16(a, b);
  v = __builtin_elementwise_maximum(__uint_as_float(a), __uint_as_float(b));
  a = __float_as_uint(v); b = a;
  swap32(a, b);
  return __builtin_elementwise_maximum(__uint_as_float(a), __uint_as_float(b));
}
__device__ __forceinline__ float rowq_sum(float v) {
  unsigned a = __float_as_uint(v), b = a;
  swap16(a, b);
  v = __uint_as_float(a) + __uint_as_float(b);
  a = __float_as_uint(v); b = a;
  swap32(a, b);
  return __uint_as_float(a) + __uint_as_float(b);
}
__device__ __forceinline__ bool spin_fail(unsigned& spins, gu32* abortw) {
  ++spins;
  if ((spins & 63u) == 0u) {
    const __amdgpu_buffer_rsrc_t ra = mkrsrc((const void*)abortw, 4);
    unsigned z = 0;
    asm volatile("" : "+v"(z));
    if (__builtin_amdgcn_readfirstlane(__builtin_amdgcn_raw_buffer_load_b32(ra, z, 0, 16)) == 1u) return true;
    if (spins > (1u << 19)) {
      __builtin_amdgcn_raw_buffer_store_b32(z + 1u, ra, z, 0, 16);
      return true;
    }
  }
  return false;
}
__device__ __forceinline__ unsigned umax4(u32x4 v) { return max(max(v[0], v[1]), max(v[2], v[3])); }
__device__ __forceinline__ bool has_ds(u32x4 v) { return (v[0] == DSENT) | (v[1] == DSENT) | (v[2] == DSENT) | (v[3] == DSENT); }
__device__ __forceinline__ float fmax2(float a, float b) { return __builtin_elementwise_maximum(a, b); }
__device__ __forceinline__ bf16x8 ldsf(const char* p) { return *reinterpret_cast<const bf16x8*>(p); }
#define SCHED() __builtin_amdgcn_sched_barrier(0)
#define LBAR() do { asm volatile("s_waitcnt lgkmcnt(0)" ::: "memory"); __builtin_amdgcn_s_barrier(); asm volatile("" ::: "memory"); } while (0)
#define WAIT_V(n) asm volatile("s_waitcnt vmcnt(%0)" :: "n"(n) : "memory")

__device__ __forceinline__ void wait_flags(__amdgpu_buffer_rsrc_t rs, int off, gu32* abortw) {
  for (unsigned spins = 0;;) {
    asm volatile("" ::: "memory");
    const unsigned v = __builtin_amdgcn_raw_buffer_load_b32(rs, off, 0, 16);
    if (__all(v != SENT)) break;
    __builtin_amdgcn_s_sleep(4);
    if (spin_fail(spins, abortw)) break;
  }
  asm volatile("" ::: "memory");
}

#define LST(i) do {} while (0)
#define RDY(i) do {} while (0)
#define RDW(i) do {} while (0)
#define SMP(k, s, v) do {} while (0)
#define LSTC(i) do {} while (0)
__device__ __forceinline__ unsigned offA(unsigned row, unsigned ch) {
  return row * 128u + 16u * (ch ^ (row & 7u));
}
__device__ __forceinline__ unsigned swzB(unsigned row) { return ((row & 3u) << 2) | ((row >> 2) & 3u); }
__device__ __forceinline__ unsigned offB(unsigned row, unsigned ch) {
  return 256u * row + 16u * (ch ^ swzB(row));
}
__device__ __forceinline__ void glds16(const u16* src, char* dst) {
  const unsigned d = __builtin_amdgcn_readfirstlane(lds_off(dst));
  unsigned keep;
  asm volatile("s_mov_b32 %0, m0\n\ts_mov_b32 m0, %2\n\ts_nop 0\n\tglobal_load_lds_dwordx4 %1, off\n\ts_mov_b32 m0, %0"
               : "=&s"(keep) : "v"(src), "s"(d) : "memory");
}
__device__ __forceinline__ bf16x8 ld_bfrag(const char* sb, int ks, int c, int lane) {
  const unsigned g = lane >> 4, qq = (lane & 15) >> 2, pp = lane & 3;
  s16x4 lo = __builtin_amdgcn_ds_read_tr16_b64_v4i16(
      (lds_s16x4_ptr)(sb + offB(32 * ks + 8 * g + qq, 2 * c + (pp >> 1)) + 8 * (pp & 1)));
  s16x4 hi = __builtin_amdgcn_ds_read_tr16_b64_v4i16(
      (lds_s16x4_ptr)(sb + offB(32 * ks + 8 * g + 4 + qq, 2 * c + (pp >> 1)) + 8 * (pp & 1)));
  return (bf16x8){lo[0], lo[1], lo[2], lo[3], hi[0], hi[1], hi[2], hi[3]};
}

__device__ __forceinline__ void blk_dma(const u16* src, size_t ld, char* slot, int lane) {
  const u16* s = src + (size_t)(lane >> 2) * ld + (lane & 3) * 8;
  glds16(s, slot);
  glds16(s + 16 * ld, slot + 1024);
}
__device__ __forceinline__ bf16x8 blk_frag(const char* slot, int T, int lane) {
  const char* a = slot + ((lane >> 4) * 8 + ((lane & 15) >> 2)) * 64 + (lane & 3) * 16 + T * 8;
  s16x4 lo = __builtin_amdgcn_ds_read_tr16_b64_v4i16((lds_s16x4_ptr)a);
  s16x4 hi = __builtin_amdgcn_ds_read_tr16_b64_v4i16((lds_s16x4_ptr)(a + 256));
  return (bf16x8){lo[0], lo[1], lo[2], lo[3], hi[0], hi[1], hi[2], hi[3]};
}
template <int NB, typename ISSUE, typename SINK>
__device__ __forceinline__ void load_blocks(char* slice, ISSUE&& issue, SINK&& sink) {
#pragma unroll
  for (int i = 0; i < 8; ++i) issue(i, slice + i * 2048);
#pragma unroll
  for (int g = 0; g < NB / 4; ++g) {
    if (g + 1 < NB / 4) WAIT_V(8); else WAIT_V(0);
#pragma unroll
    for (int j = 0; j < 4; ++j) sink(4 * g + j, slice + ((4 * g + j) & 7) * 2048);
    asm volatile("s_waitcnt lgkmcnt(0)" ::: "memory");
    if (g + 2 < NB / 4) {
#pragma unroll
      for (int j = 0; j < 4; ++j) issue(4 * g + 8 + j, slice + ((4 * g + j) & 7) * 2048);
    }
  }
}

__device__ __forceinline__ u32x4 widen32(unsigned a0, unsigned a1, unsigned b0, unsigned b1) {
  swap32(a0, b0); swap16(a0, b0);
  swap32(a1, b1); swap16(a1, b1);
  return (u32x4){a0, a1, b0, b1};
}

__device__ __forceinline__ void publish_flags(__amdgpu_buffer_rsrc_t rs, int off, int stride, int n) {
  WAIT_V(0);
  LBAR();
  int tid = threadIdx.x;
  asm volatile("" : "+v"(tid));
  if (tid < n) __builtin_amdgcn_raw_buffer_store_b32(1u, rs, off + tid * stride, 0, 16);
}

__device__ __forceinline__ void wait_s(const Args& p, __amdgpu_buffer_rsrc_t rsy, int rblk, int c0, int n, bool need) {
  if (need) {
    if (threadIdx.x < 64) {
      for (unsigned spins = 0;;) {
        asm volatile("" ::: "memory");
        const int f = (int)__builtin_amdgcn_raw_buffer_load_b32(rsy, SY_FS + (2 * c0 + (threadIdx.x & (2 * n - 1))) * 128, 0, 16);
        if (__all(f >= rblk)) break;
        __builtin_amdgcn_s_sleep(32);
        if (spin_fail(spins, (gu32*)(p.ws + WS_SY + SY_ABORT))) break;
      }
    }
    LBAR();
  }
}
__device__ __forceinline__ u32x4 combine(u32x4 sv, u32x4 g, u32x4 x) {
  u32x4 o;
#pragma unroll
  for (int j = 0; j < 4; ++j) {
    const unsigned t = cvtpk(lo_f(sv[j]) * lo_f(g[j]), hi_f(sv[j]) * hi_f(g[j]));
    o[j] = cvtpk(lo_f(x[j]) + lo_f(t), hi_f(x[j]) + hi_f(t));
  }
  return o;
}
#define OUT_AUX 16
__device__ __forceinline__ void st_out(const Args& p, int e, u32x4 v) {
  __builtin_amdgcn_raw_buffer_store_b128(v, mkrsrc(p.out, S_LEN * DM * 2), e * 2, 0, OUT_AUX);
}
struct SX { u32x4 s, x; };
__device__ __forceinline__ SX get_sx(const Args& p, int rblk, int e) {
  SX r; r.s = (u32x4){0u, 0u, 0u, 0u}; r.x = r.s;
  if (rblk < NFUSE) {
    r.s = ld_sc1(mkrsrc(p.ws + WS_S, S_LEN * DM * 2), (((e & 1023) >> 5) * S_LEN + (e >> 10)) * 64 + (e & 31) * 2);
    r.x = *reinterpret_cast<const u32x4*>(p.x + e);
  }
  return r;
}
__device__ __forceinline__ void put_g(const Args& p, __amdgpu_buffer_rsrc_t rg, int rblk, int e, u32x4 g, const SX& v) {
  if (rblk < NFUSE) {
    st_out(p, e, combine(v.s, g, v.x));
  } else st_sc1(rg, e * 2, g);
}

__device__ __forceinline__ void job_comb(const Args& p, int rblk, int ct, __amdgpu_buffer_rsrc_t rsy) {
  int tid = threadIdx.x;
  asm volatile("" : "+v"(tid));
  u32x4 xv[8];
#pragma unroll
  for (int i = 0; i < 8; ++i) xv[i] = *reinterpret_cast<const u32x4*>(p.x + (rblk * 128 + i * 16 + (tid >> 5)) * DM + ct * 256 + (tid & 31) * 8);
  if (tid < 64) wait_flags(rsy, SY_FG + (rblk * 8 + 2 * ct + (tid & 1)) * 128, (gu32*)(p.ws + WS_SY + SY_ABORT));
  wait_s(p, rsy, rblk, ct * 8, 8, true);
  u32x4 sv[8], gv[8];
#pragma unroll
  for (int i = 0; i < 8; ++i) {
    const int e = (rblk * 128 + i * 16 + (tid >> 5)) * DM + ct * 256 + (tid & 31) * 8;
    sv[i] = ld_sc1(mkrsrc(p.ws + WS_S, S_LEN * DM * 2), (((e & 1023) >> 5) * S_LEN + (e >> 10)) * 64 + (e & 31) * 2);
    gv[i] = ld_sc1(mkrsrc(p.ws + WS_G, S_LEN * DM * 2), e * 2);
  }
#pragma unroll
  for (int i = 0; i < 8; ++i)
    st_out(p, (rblk * 128 + i * 16 + (tid >> 5)) * DM + ct * 256 + (tid & 31) * 8, combine(sv[i], gv[i], xv[i]));
}

__device__ __forceinline__ u32x4 raw_rsrc(const void* p, unsigned bytes) {
  const unsigned long long a = (unsigned long long)p;
  return (u32x4){(unsigned)a, (unsigned)(a >> 32) & 0xffffu, bytes, 0x00020000u};
}
__device__ __forceinline__ u32x2 next_tag(u32x2 e) { return (u32x2){e[0] + 1u, e[1] + (e[0] == 0xFFFFFFFFu)}; }
struct Boot { u32x2 e; u32x4 c; };
__device__ __forceinline__ void boot_ask(const char* ws, Boot& r) {
  const unsigned o = (threadIdx.x & 63) * 128;
  asm volatile("buffer_load_dwordx2 %0, off, %3, %4 sc1\n\tbuffer_load_dwordx4 %1, %2, %3, %5 offen sc1"
               : "=&v"(r.e), "=&v"(r.c) : "v"(o), "s"(raw_rsrc(ws, SY_TOTAL)), "s"(SY_EP), "s"(SY_BT) : "memory");
}
__device__ __forceinline__ void wait_rows(const char* ws, u32x2 tag) {
  const __amdgpu_buffer_rsrc_t rs = mkrsrc(ws, SY_TOTAL);
  int o = (threadIdx.x & 63) * 128;
  asm volatile("" : "+v"(o));
#pragma unroll 1
  for (int i = 0; i < (1 << 17); ++i) {
    asm volatile("" ::: "memory");
    const u32x2 a = __builtin_amdgcn_raw_buffer_load_b64(rs, o, SY_BT + 32 * 128, 16), b = __builtin_amdgcn_raw_buffer_load_b64(rs, o, SY_BT + 96 * 128, 16);
    if (__all(a[0] == tag[0] && a[1] == tag[1] && b[0] == tag[0] && b[1] == tag[1])) break;
    __builtin_amdgcn_s_sleep(8);
  }
  asm volatile("" ::: "memory");
}
__device__ __forceinline__ unsigned boot_done(const char* ws, Boot& r, bool own = false, u32x2 tag = (u32x2){0u, 0u}) {
#pragma unroll 1
  for (int i = 0; i < (1 << 17); ++i) {
    asm volatile("" : "+v"(r.e), "+v"(r.c));
    if (!own) tag = next_tag(r.e);
    if (__all(r.c[0] == tag[0] && r.c[1] == tag[1])) break;
    __builtin_amdgcn_s_sleep(8);
    boot_ask(ws, r);
    WAIT_V(0);
  }
  unsigned q0[NPAGE], q1[NPAGE], ga = 0xFFFFu, gb = 0xFFFFu;
#pragma unroll
  for (int i = 0; i < NPAGE; ++i) {
    const unsigned x = __builtin_amdgcn_readlane(r.c[2], i), y = __builtin_amdgcn_readlane(r.c[2], i + NPAGE);
    q0[i] = min(x >> 16, y >> 16); q1[i] = min(x & 0xFFFFu, y & 0xFFFFu);
    ga = min(ga, (i & 1) ? q1[i] : q0[i]); gb = min(gb, (i & 1) ? q0[i] : q1[i]);
  }
  unsigned m = gb < ga ? 0x80001555u : 0x2AAAu;
  const unsigned thr = min(ga, gb) + 110;
#pragma unroll
  for (int i = 0; i < NPAGE; ++i) {
    const unsigned lo = min(q0[i], q1[i]), hi = max(q0[i], q1[i]);
    const unsigned keep = (lo < thr ? hi : 0u) > lo + 100 ? ~(1u << i) : ~0u;
    m = (m & keep) | ((q1[i] < q0[i] ? ~0u : 0u) & ~keep);
  }
  return m;
}
typedef __attribute__((address_space(3))) volatile unsigned* lds_vu_ptr;
__device__ __forceinline__ lds_vu_ptr lds_at(const void* p) {
  unsigned a = lds_off(p);
  asm volatile("" : "+v"(a));
  return (lds_vu_ptr)(uintptr_t)a;
}
__device__ __forceinline__ unsigned uni(unsigned v) {
  unsigned r;
  asm volatile("s_nop 1\n\tv_readfirstlane_b32 %0, %1\n\ts_nop 3" : "=s"(r) : "v"(v));
  return r;
}
__device__ __forceinline__ void start_sy(char* ws, const u16* mem0, int i, char* smem) {
  const int tid = threadIdx.x, lane = tid & 63, w = __builtin_amdgcn_readfirstlane(tid >> 6);
  const __amdgpu_buffer_rsrc_t rsy = mkrsrc(ws, SY_TOTAL);
  const __amdgpu_buffer_rsrc_t rs = mkrsrc(ws + (size_t)(i % NPAGE) * PAGE + PROBE - (i / NPAGE) * 2048, 16384);
  const u32x2 e = __builtin_amdgcn_raw_buffer_load_b64(rsy, 0, SY_EP, 16);
  const u32x4 ds = {DSENT, DSENT, DSENT, DSENT};
  if (lane < 32) st_sc1(rs, (lane >> 4) * 8192 + w * 256 + (lane & 15) * 16, ds);
  static_assert(NPRE == 4 && SY_FEND - SY_BR <= 32 * 6144, "");
#pragma unroll
  for (int u = 0; u < 6; ++u)
    st_sc1(rsy, (u < 4 ? SY_V + (i * 4 + u) * 8192 : SY_O + (i * 2 + u - 4) * 8192) + tid * 16, ds);
  if (w == 7) {
    const int n = i * 64 + lane, j = (n >> 10) * 512 + (n & 511);
    const unsigned* m0 = reinterpret_cast<const unsigned*>(mem0 + (2 * ((j >> 2) & 3) + (j & 1)) * DM + (j >> 4) * 16) + ((j >> 1) & 1);
    u32x4 m;
#pragma unroll
    for (int k = 0; k < 4; ++k) { const unsigned t = m0[2 * k]; m[k] = t == DSENT ? 0xFFFFFFFFu : t; }
    st_sc1(rsy, n * 16, m);
  } else if (w == 6) st_sc1(rsy, 32768 + i * 1024 + lane * 16, ds);
  else {
    const int o = SY_BR + i * 6144 + tid * 16;
    st_sc1(rsy, o < SY_FEND ? o : 0x7F000000, (u32x4){SENT, SENT, SENT, SENT});
  }
  unsigned t[4];
#pragma unroll
  for (int k = 0; k < 4; ++k) {
    unsigned long long c0, c1; unsigned v, off = ((k ^ (k >> 1)) & 1) * 8192 + w * 256 + (k >> 1) * 128;
    asm volatile("s_waitcnt vmcnt(0)\n\ts_barrier\n\ts_memtime %0\n\ts_waitcnt lgkmcnt(0)\n\tbuffer_load_dword %2, %3, %4, 0 offen sc1\n\ts_waitcnt vmcnt(0)\n\t"
                 "s_memtime %1\n\ts_waitcnt lgkmcnt(0)" : "=&s"(c0), "=&s"(c1), "=&v"(v) : "v"(off), "s"(rs) : "memory");
    t[k] = (unsigned)(c1 - c0);
  }
  lds_vu_ptr tm = lds_at(smem);
  if (lane == 0) { tm[w] = min(t[0], t[3]); tm[8 + w] = min(t[1], t[2]); }
  int ro = SY_BT + i * 128 + lane * 16;
  asm volatile("" : "+v"(ro));
  LBAR();
  RDY(384 + i);
  if (threadIdx.x < 8) {
    unsigned m0 = tm[0], m1 = tm[8];
#pragma unroll
    for (int k = 1; k < 8; ++k) { m0 = min(m0, tm[k]); m1 = min(m1, tm[8 + k]); }
    const u32x2 tag = next_tag(e);
    st_sc1(rsy, ro, (u32x4){tag[0], tag[1], min(m0, 30000u) << 16 | min(m1, 30000u), 0u});
  }
}
template <int NB, typename F>
__device__ __forceinline__ u32x2 start_rows(const Args& p, int g, F&& between) {
  const int tid = threadIdx.x, lane = tid & 63, row0 = g * 16 + (tid >> 6) * 2;
  u32x4 a[4];
  u32x2 e;
  asm volatile("global_load_dwordx4 %0, %5, off\n\tglobal_load_dwordx4 %1, %5, off offset:1024\n\tglobal_load_dwordx4 %2, %5, off offset:2048\n\t"
               "global_load_dwordx4 %3, %5, off offset:3072\n\tbuffer_load_dwordx2 %4, off, %6, %7 sc1"
               : "=&v"(a[0]), "=&v"(a[1]), "=&v"(a[2]), "=&v"(a[3]), "=&v"(e)
               : "v"(p.x + (size_t)row0 * DM + lane * 8), "s"(raw_rsrc(p.ws, SY_TOTAL)), "s"(SY_EP) : "memory");
  between();
  asm volatile("s_waitcnt vmcnt(%5)" : "+v"(a[0]), "+v"(a[1]), "+v"(a[2]), "+v"(a[3]), "+v"(e) : "n"(NB) : "memory");
  RDY(512 + g);
  const __amdgpu_buffer_rsrc_t ro = mkrsrc(p.ws + WS_XN, S_LEN * XLD * 2);
#pragma unroll
  for (int u = 0; u < 2; ++u) {
    float v[16];
#pragma unroll
    for (int i = 0; i < 8; ++i) { v[2 * i] = lo_f(a[2 * u + (i >> 2)][i & 3]); v[2 * i + 1] = hi_f(a[2 * u + (i >> 2)][i & 3]); }
    float s = 0.f;
#pragma unroll
    for (int i = 0; i < 16; ++i) s += v[i];
    const float mean = wave_sum(s) * (1.0f / DM);
    float q = 0.f;
#pragma unroll
    for (int i = 0; i < 16; ++i) { float d = v[i] - mean; q += d * d; }
    const float var = rbf(wave_sum(q) * (1.0f / DM));
    const float mb = rbf(mean);
    const float inv = 1.0f / rbf(sqrtf(rbf(var + 1e-5f)));
#pragma unroll
    for (int k = 0; k < 2; ++k) {
      u32x4 o;
#pragma unroll
      for (int i = 0; i < 4; ++i) {
        const unsigned t = cvtpk(v[8 * k + 2 * i] - mb, v[8 * k + 2 * i + 1] - mb);
        o[i] = cvtpk(lo_f(t) * inv, hi_f(t) * inv);
      }
      st_sc1(ro, (row0 + u) * (XLD * 2) + k * 1024 + lane * 16, o);
    }
  }
  return next_tag(e);
}
__device__ __forceinline__ void report_rows(const Args& p, int g, u32x2 tag) {
  RDY(256 + g);
  if (threadIdx.x < 8) st_sc1(mkrsrc(p.ws + WS_SY, SY_TOTAL), SY_BT + (32 + g) * 128 + threadIdx.x * 16, (u32x4){tag[0], tag[1], tag[0], tag[1]});
}

typedef __attribute__((ext_vector_type(32))) unsigned u32x32;
__device__ __forceinline__ void put4(u32x32& W, int i, bf16x8 f) {
  const u32x4 v = __builtin_bit_cast(u32x4, f);
#pragma unroll
  for (int j = 0; j < 4; ++j) W[4 * i + j] = v[j];
}
#define CLB8(a, b, c, d, e, f, g, h) "v" #a, "v" #b, "v" #c, "v" #d, "v" #e, "v" #f, "v" #g, "v" #h
#define CLB32(a) CLB32_##a
#define CLB32_128 CLB8(128, 129, 130, 131, 132, 133, 134, 135), CLB8(136, 137, 138, 139, 140, 141, 142, 143), CLB8(144, 145, 146, 147, 148, 149, 150, 151), CLB8(152, 153, 154, 155, 156, 157, 158, 159)
#define CLB32_160 CLB8(160, 161, 162, 163, 164, 165, 166, 167), CLB8(168, 169, 170, 171, 172, 173, 174, 175), CLB8(176, 177, 178, 179, 180, 181, 182, 183), CLB8(184, 185, 186, 187, 188, 189, 190, 191)
#define W4_IN "{v[0:31]}"(W[0]), "{v[32:63]}"(W[1]), "{v[64:95]}"(W[2]), "{v[96:127]}"(W[3])
#define CLB32_192 CLB8(192, 193, 194, 195, 196, 197, 198, 199), CLB8(200, 201, 202, 203, 204, 205, 206, 207), CLB8(208, 209, 210, 211, 212, 213, 214, 215), CLB8(216, 217, 218, 219, 220, 221, 222, 223)
__device__ __forceinline__ unsigned ld_flag0(__amdgpu_buffer_rsrc_t rs, int off) {
  return __builtin_amdgcn_raw_buffer_load_b32(rs, off, 0, 16);
}
__device__ __forceinline__ void glds16_sc1(const char* src, char* dst) {
  const unsigned d = __builtin_amdgcn_readfirstlane(lds_off(dst));
  unsigned keep;
  asm volatile("s_mov_b32 %0, m0\n\ts_mov_b32 m0, %2\n\ts_nop 0\n\tglobal_load_lds_dwordx4 %1, off sc1\n\ts_mov_b32 m0, %0"
               : "=&s"(keep) : "v"(src), "s"(d) : "memory");
}

__device__ __forceinline__ void touch_line(const char* src, char* dst) {
  const unsigned d = __builtin_amdgcn_readfirstlane(lds_off(dst));
  unsigned keep;
  asm volatile("s_mov_b32 %0, m0\n\ts_mov_b32 m0, %2\n\ts_nop 0\n\tglobal_load_lds_dword %1, off sc1\n\ts_mov_b32 m0, %0"
               : "=&s"(keep) : "v"(src), "s"(d) : "memory");
}

#define GPF 1
#define GU_DELAY 0
#define Q_DELAY 0
#define PF_T0 100
#define PF_N 12
#define A_AUX " sc1"
#define WREV 1
template <bool DEV = false>
__device__ __forceinline__ void glds2(const char* base, int step, unsigned voff, unsigned dst) {
  unsigned keep;
  const char* base2 = base + step;
  if (DEV)
    asm volatile("s_mov_b32 %0, m0\n\ts_mov_b32 m0, %4\n\ts_nop 0\n\tglobal_load_lds_dwordx4 %1, %2 sc1\n\t"
                 "s_add_u32 m0, m0, 0x2000\n\ts_nop 0\n\tglobal_load_lds_dwordx4 %1, %3 sc1\n\ts_mov_b32 m0, %0"
                 : "=&s"(keep) : "v"(voff), "s"(base), "s"(base2), "s"(dst) : "memory", "scc");
  else
  asm volatile("s_mov_b32 %0, m0\n\ts_mov_b32 m0, %4\n\ts_nop 0\n\tglobal_load_lds_dwordx4 %1, %2\n\t"
               "s_add_u32 m0, m0, 0x2000\n\ts_nop 0\n\tglobal_load_lds_dwordx4 %1, %3\n\ts_mov_b32 m0, %0"
               : "=&s"(keep) : "v"(voff), "s"(base), "s"(base2), "s"(dst) : "memory", "scc");
}

__device__ __forceinline__ void gemm_stream(const char* A, int lda, const char* B, int ldb, int nk, char* smem, f32x4 (&acc)[2][4]) {
  int tid = threadIdx.x;
  asm volatile("" : "+v"(tid));
  const int lane = tid & 63, r = lane & 15, q = lane >> 4;
  const int wid = __builtin_amdgcn_readfirstlane(tid >> 6);
  const int wr = wid >> 1, wc = wid & 1;
  const unsigned va = (tid >> 3) * lda + 16 * ((tid & 7) ^ ((tid >> 3) & 7)), vb = (tid >> 4) * ldb + 16 * ((tid & 15) ^ swzB((tid >> 4) & 15));
  const unsigned lw = lds_off(smem) + wid * 1024;
  auto piece = [&](int kt, int i) {
    const int k = min(kt, nk - 1);
    const char* src = i < 2 ? A + k * 128 + i * 64 * lda : B + (size_t)(k * 64 + (i - 2) * 32) * ldb;
    unsigned keep;
    if (i < 2)
      asm volatile("s_mov_b32 %0, m0\n\ts_mov_b32 m0, %3\n\ts_nop 0\n\tglobal_load_lds_dwordx4 %1, %2" A_AUX "\n\ts_mov_b32 m0, %0"
                   : "=&s"(keep) : "v"(va), "s"(src), "s"(lw + (kt & 3) * 32768 + i * 8192) : "memory");
    else
      asm volatile("s_mov_b32 %0, m0\n\ts_mov_b32 m0, %3\n\ts_nop 0\n\tglobal_load_lds_dwordx4 %1, %2\n\ts_mov_b32 m0, %0"
                   : "=&s"(keep) : "v"(vb), "s"(src), "s"(lw + (kt & 3) * 32768 + i * 8192) : "memory");
  };
  unsigned la[2], lb[4][2];
#pragma unroll
  for (int k = 0; k < 2; ++k) { la[k] = lds_off(smem) + offA(wr * 32 + r, k * 4 + q); asm volatile("" : "+v"(la[k])); }
#pragma unroll
  for (int n = 0; n < 4; ++n)
#pragma unroll
    for (int u = 0; u < 2; ++u) {
      lb[n][u] = lds_off(smem) + 16384 + offB(8 * q + 4 * u + (r >> 2), 2 * (wc * 4 + n) + ((r & 3) >> 1)) + 8 * (r & 1);
      asm volatile("" : "+v"(lb[n][u]));
    }
  typedef __attribute__((address_space(3))) const bf16x8* lds_frag_ptr;
  bf16x8 af[2][2], bf[2][4];
  auto ldA = [&](int bo, int ks, int m) { return *(lds_frag_ptr)(uintptr_t)(la[ks] + bo + m * 2048); };
  auto ldB = [&](int bo, int ks, int n) {
    s16x4 lo = __builtin_amdgcn_ds_read_tr16_b64_v4i16((lds_s16x4_ptr)(uintptr_t)(lb[n][0] + bo + ks * 8192));
    s16x4 hi = __builtin_amdgcn_ds_read_tr16_b64_v4i16((lds_s16x4_ptr)(uintptr_t)(lb[n][1] + bo + ks * 8192));
    return (bf16x8){lo[0], lo[1], lo[2], lo[3], hi[0], hi[1], hi[2], hi[3]};
  };
#pragma unroll
  for (int i = 0; i < 8; ++i) acc[i >> 2][i & 3] = (f32x4){0.f, 0.f, 0.f, 0.f};
  LBAR();
#pragma unroll
  for (int i = 0; i < 10; ++i) piece(i >> 2, i & 3);
  WAIT_V(6);
  LBAR();
#pragma unroll
  for (int m = 0; m < 2; ++m) af[0][m] = ldA(0, 0, m);
#pragma unroll
  for (int n = 0; n < 4; ++n) bf[0][n] = ldB(0, 0, n);
  auto loop = [&](auto HF) {
    for (int t0 = 0; t0 < nk; t0 += 4) {
#pragma unroll
      for (int b = 0; b < 4; ++b) {
        const int t = t0 + b;
#pragma unroll
        for (int ks = 0; ks < 2; ++ks) {
          if (ks == 1) {
            WAIT_V(4);
            __builtin_amdgcn_s_barrier();
          }
          const int nb = (ks ? (b + 1) & 3 : b) * 32768, nks = ks ^ 1;
#pragma unroll
          for (int n = 0; n < 4; ++n) {
            acc[0][n] = MFMA(bf[ks][n], af[ks][0], acc[0][n]);
            bf[nks][n] = ldB(nb, nks, n);
            acc[1][n] = MFMA(bf[ks][n], af[ks][1], acc[1][n]);
            if (n < 2) af[nks][n] = ldA(nb, nks, n);
            SCHED();
            if ((n & 1) == HF) { piece(t + 2 + ks, (n >> 1) + 2 * nks); SCHED(); }
          }
        }
      }
    }
  };
  if (wid >> 2) loop(std::integral_constant<int, 1>()); else loop(std::integral_constant<int, 0>());
  WAIT_V(0);
}

#define HSL 16
__device__ __forceinline__ void job_down(const Args& p, int rblk, int ct, char* smem, __amdgpu_buffer_rsrc_t rsy) {
  if (threadIdx.x < 64) {
    for (unsigned spins = 0;;) {
      asm volatile("" ::: "memory");
      if (__builtin_amdgcn_readfirstlane((int)ld_flag0(rsy, SY_CH + (rblk >> 1) * 128)) >= 27) break;
      __builtin_amdgcn_s_sleep(HSL);
      if (spin_fail(spins, (gu32*)(p.ws + WS_SY + SY_ABORT))) break;
    }
  }
  f32x4 acc[2][4];
  gemm_stream(p.ws + WS_HF + h_off(p, rblk), HLD, (const char*)(p.wd + ct * 128), DM * 2, FF / 64, smem, acc);
  wait_s(p, rsy, rblk, ct * 4, 4, rblk < NFUSE);
  int tid = threadIdx.x;
  asm volatile("" : "+v"(tid));
  const int lane = tid & 63, wid = tid >> 6, wr = wid >> 1, wc = wid & 1;
  SX sx[4];
#pragma unroll
  for (int i = 0; i < 4; ++i)
    sx[i] = get_sx(p, rblk, (rblk * 128 + wr * 32 + (i >> 1) * 16 + (lane & 15)) * DM + ct * 128 + wc * 64 + (i & 1) * 32 + (lane >> 4) * 8);
  const __amdgpu_buffer_rsrc_t ro = mkrsrc(p.ws + WS_G, S_LEN * DM * 2);
#pragma unroll
  for (int m = 0; m < 2; ++m) {
    const int row = rblk * 128 + wr * 32 + m * 16 + (lane & 15);
#pragma unroll
    for (int nb = 0; nb < 2; ++nb) {
      unsigned pk[2][2];
#pragma unroll
      for (int n = 0; n < 2; ++n) {
        float rr[4];
#pragma unroll
        for (int j = 0; j < 4; ++j) { float d = rbf(acc[m][2 * nb + n][j]); rr[j] = d * __builtin_amdgcn_rcpf(1.0f + __expf(-d)); }
        pk[n][0] = cvtpk(rr[0], rr[1]); pk[n][1] = cvtpk(rr[2], rr[3]);
      }
      u32x4 o = widen32(pk[0][0], pk[0][1], pk[1][0], pk[1][1]);
      put_g(p, ro, rblk, row * DM + ct * 128 + wc * 64 + nb * 32 + (lane >> 4) * 8, o, sx[2 * m + nb]);
    }
  }
  if (rblk >= NFUSE) publish_flags(rsy, SY_FG + (rblk * 8 + ct) * 128, 0, 1);
}

#define PR_ASM_0 \
  "s_cmp_lg_u32 %[kh], 0\n\t" \
  "s_cbranch_scc1 .Lk%=\n\t" \
  "s_mov_b32 s94, m0\n\t" \
  "s_mov_b64 s[92:93], %[x0]\n\t" \
  "s_mov_b32 s86, %[g0]\n\t" \
  "s_add_u32 s87, s86, %[t0]\n\t" \
  "s_sub_i32 s88, s86, 3\n\t" \
  "ds_read_b128 v[176:179], v208 offset:0\n\t" \
  "ds_read_b128 v[180:183], v208 offset:16384\n\t" \
  "ds_read_b128 v[184:187], v209 offset:0\n\t" \
  "ds_read_b128 v[188:191], v209 offset:16384\n\t" \
  "ds_read_b128 v[192:195], v210 offset:0\n\t" \
  "ds_read_b128 v[196:199], v210 offset:16384\n\t" \
  "ds_read_b128 v[200:203], v211 offset:0\n\t" \
  "ds_read_b128 v[204:207], v211 offset:16384\n\t" \
  ".Lp0%=:\n\t" \
  "s_waitcnt lgkmcnt(6)\n\t" \
  "v_mfma_f32_16x16x32_bf16 v[128:131], v[0:3], v[176:179], 0\n\t" \
  "v_mfma_f32_16x16x32_bf16 v[132:135], v[4:7], v[176:179], 0\n\t" \
  "ds_read_b128 v[176:179], v208 offset:256\n\t" \
  "s_add_u32 m0, %[lw], 67584\n\t" \
  "v_mfma_f32_16x16x32_bf16 v[136:139], v[0:3], v[180:183], 0\n\t" \
  "v_mfma_f32_16x16x32_bf16 v[140:143], v[4:7], v[180:183], 0\n\t" \
  "ds_read_b128 v[180:183], v208 offset:16640\n\t" \
  "global_load_lds_dwordx4 v218, s[92:93] sc1\n\t" \
  "s_waitcnt lgkmcnt(6)\n\t" \
  "v_mfma_f32_16x16x32_bf16 v[128:131], v[8:11], v[184:187], v[128:131]\n\t" \
  "v_mfma_f32_16x16x32_bf16 v[132:135], v[12:15], v[184:187], v[132:135]\n\t" \
  "ds_read_b128 v[184:187], v209 offset:256\n\t" \
  "v_mfma_f32_16x16x32_bf16 v[136:139], v[8:11], v[188:191], v[136:139]\n\t" \
  "v_mfma_f32_16x16x32_bf16 v[140:143], v[12:15], v[188:191], v[140:143]\n\t" \
  "ds_read_b128 v[188:191], v209 offset:16640\n\t" \
  "ds_write_b128 v220, v[152:155] offset:0\n\t" \
  "ds_write_b128 v220, v[156:159] offset:1024\n\t" \
  "s_waitcnt lgkmcnt(8)\n\t" \
  "v_mfma_f32_16x16x32_bf16 v[128:131], v[16:19], v[192:195], v[128:131]\n\t" \
  "v_mfma_f32_16x16x32_bf16 v[132:135], v[20:23], v[192:195], v[132:135]\n\t" \
  "ds_read_b128 v[192:195], v210 offset:256\n\t" \
  "s_add_u32 m0, %[lw], 68608\n\t" \
  "v_mfma_f32_16x16x32_bf16 v[136:139], v[16:19], v[196:199], v[136:139]\n\t" \
  "v_mfma_f32_16x16x32_bf16 v[140:143], v[20:23], v[196:199], v[140:143]\n\t" \
  "ds_read_b128 v[196:199], v210 offset:16640\n\t" \
  "global_load_lds_dwordx4 v219, s[92:93] sc1\n\t" \
  "s_lshr_b32 s90, s87, 7\n\t" \
  "s_xor_b32 s90, s90, %[s0]\n\t" \
  "s_and_b32 s90, s90, 1\n\t" \
  "s_lshl1_add_u32 s90, s87, s90\n\t" \
  "s_lshl_b32 s90, s90, 13\n\t" \
  "s_cmp_lt_i32 s86, 3\n\t" \
  "s_cselect_b32 s90, 0x70000000, s90\n\t" \
  "s_waitcnt lgkmcnt(8)\n\t" \
  "v_mfma_f32_16x16x32_bf16 v[128:131], v[24:27], v[200:203], v[128:131]\n\t" \
  "v_mfma_f32_16x16x32_bf16 v[132:135], v[28:31], v[200:203], v[132:135]\n\t" \
  "ds_read_b128 v[200:203], v211 offset:256\n\t" \
  "v_mfma_f32_16x16x32_bf16 v[136:139], v[24:27], v[204:207], v[136:139]\n\t" \
  "v_mfma_f32_16x16x32_bf16 v[140:143], v[28:31], v[204:207], v[140:143]\n\t" \
  "ds_read_b128 v[204:207], v211 offset:16640\n\t" \
  "s_waitcnt lgkmcnt(4)\n\t" \
  "s_waitcnt vmcnt(4)\n\t" \
  "s_barrier\n\t" \
  ".Lg00%=:\n\t" \
  "s_add_u32 s92, s92, 512\n\t" \
  "s_addc_u32 s93, s93, 0\n\t" \
  "v_mfma_f32_16x16x32_bf16 v[128:131], v[32:35], v[176:179], v[128:131]\n\t" \
  "v_mfma_f32_16x16x32_bf16 v[132:135], v[36:39], v[176:179], v[132:135]\n\t" \
  "ds_read_b128 v[176:179], v208 offset:32768\n\t" \
  "s_add_u32 m0, %[lw], 98304\n\t" \
  "v_mfma_f32_16x16x32_bf16 v[136:139], v[32:35], v[180:183], v[136:139]\n\t" \
  "v_mfma_f32_16x16x32_bf16 v[140:143], v[36:39], v[180:183], v[140:143]\n\t" \
  "ds_read_b128 v[180:183], v208 offset:49152\n\t" \
  "global_load_lds_dwordx4 v216, s[92:93] sc1\n\t" \
  "ds_read_b128 v[160:163], v221 offset:0\n\t" \
  "ds_read_b128 v[164:167], v221 offset:1024\n\t" \
  "v_mfma_f32_16x16x32_bf16 v[128:131], v[40:43], v[184:187], v[128:131]\n\t" \
  "v_mfma_f32_16x16x32_bf16 v[132:135], v[44:47], v[184:187], v[132:135]\n\t" \
  "ds_read_b128 v[184:187], v209 offset:32768\n\t" \
  "v_mfma_f32_16x16x32_bf16 v[136:139], v[40:43], v[188:191], v[136:139]\n\t" \
  "v_mfma_f32_16x16x32_bf16 v[140:143], v[44:47], v[188:191], v[140:143]\n\t" \
  "ds_read_b128 v[188:191], v209 offset:49152\n\t" \
  "s_waitcnt lgkmcnt(8)\n\t" \
  "v_mfma_f32_16x16x32_bf16 v[128:131], v[48:51], v[192:195], v[128:131]\n\t" \
  "v_mfma_f32_16x16x32_bf16 v[132:135], v[52:55], v[192:195], v[132:135]\n\t" \
  "ds_read_b128 v[192:195], v210 offset:32768\n\t" \
  "s_add_u32 m0, %[lw], 99328\n\t" \
  "v_mfma_f32_16x16x32_bf16 v[136:139], v[48:51], v[196:199], v[136:139]\n\t" \
  "v_mfma_f32_16x16x32_bf16 v[140:143], v[52:55], v[196:199], v[140:143]\n\t" \
  "ds_read_b128 v[196:199], v210 offset:49152\n\t" \
  "global_load_lds_dwordx4 v217, s[92:93] sc1\n\t" \
  "s_waitcnt lgkmcnt(4)\n\t" \
  "v_add_f32 v160, v160, v144\n\t" \
  "v_add_f32 v161, v161, v145\n\t" \
  "v_add_f32 v162, v162, v146\n\t" \
  "v_add_f32 v163, v163, v147\n\t" \
  "v_add_f32 v164, v164, v148\n\t" \
  "v_add_f32 v165, v165, v149\n\t" \
  "v_add_f32 v166, v166, v150\n\t" \
  "v_add_f32 v167, v167, v151\n\t" \
  "v_mfma_f32_16x16x32_bf16 v[128:131], v[56:59], v[200:203], v[128:131]\n\t" \
  "v_mfma_f32_16x16x32_bf16 v[132:135], v[60:63], v[200:203], v[132:135]\n\t" \
  "ds_read_b128 v[200:203], v211 offset:32768\n\t" \
  "v_mfma_f32_16x16x32_bf16 v[136:139], v[56:59], v[204:207], v[136:139]\n\t" \
  "v_mfma_f32_16x16x32_bf16 v[140:143], v[60:63], v[204:207], v[140:143]\n\t" \
  "ds_read_b128 v[204:207], v211 offset:49152\n\t" \
  "v_cvt_pk_bf16_f32 v168, v160, v161\n\t" \
  "v_cvt_pk_bf16_f32 v169, v162, v163\n\t" \
  "v_cvt_pk_bf16_f32 v170, v164, v165\n\t" \
  "v_cvt_pk_bf16_f32 v171, v166, v167\n\t" \
  "buffer_store_dwordx4 v[168:171], v172, %[rt], s90 offen sc1\n\t" \
  "v_mfma_f32_16x16x32_bf16 v[128:131], v[64:67], v[176:179], v[128:131]\n\t" \
  "v_mfma_f32_16x16x32_bf16 v[132:135], v[68:71], v[176:179], v[132:135]\n\t" \
  "ds_read_b128 v[176:179], v208 offset:33024\n\t" \
  "s_add_u32 m0, %[lw], 100352\n\t" \
  "v_mfma_f32_16x16x32_bf16 v[136:139], v[64:67], v[180:183], v[136:139]\n\t" \
  "v_mfma_f32_16x16x32_bf16 v[140:143], v[68:71], v[180:183], v[140:143]\n\t" \
  "ds_read_b128 v[180:183], v208 offset:49408\n\t" \
  "global_load_lds_dwordx4 v218, s[92:93] sc1\n\t" \
  "s_waitcnt lgkmcnt(6)\n\t" \
  "v_mfma_f32_16x16x32_bf16 v[128:131], v[72:75], v[184:187], v[128:131]\n\t" \
  "v_mfma_f32_16x16x32_bf16 v[132:135], v[76:79], v[184:187], v[132:135]\n\t" \
  "ds_read_b128 v[184:187], v209 offset:33024\n\t" \
  "v_mfma_f32_16x16x32_bf16 v[136:139], v[72:75], v[188:191], v[136:139]\n\t" \
  "v_mfma_f32_16x16x32_bf16 v[140:143], v[76:79], v[188:191], v[140:143]\n\t" \
  "ds_read_b128 v[188:191], v209 offset:49408\n\t" \
  "s_waitcnt lgkmcnt(6)\n\t" \
  "v_mfma_f32_16x16x32_bf16 v[128:131], v[80:83], v[192:195], v[128:131]\n\t" \
  "v_mfma_f32_16x16x32_bf16 v[132:135], v[84:87], v[192:195], v[132:135]\n\t" \
  "ds_read_b128 v[192:195], v210 offset:33024\n\t" \
  "s_add_u32 m0, %[lw], 101376\n\t" \
  "v_mfma_f32_16x16x32_bf16 v[136:139], v[80:83], v[196:199], v[136:139]\n\t" \
  "v_mfma_f32_16x16x32_bf16 v[140:143], v[84:87], v[196:199], v[140:143]\n\t" \
  "ds_read_b128 v[196:199], v210 offset:49408\n\t" \
  "global_load_lds_dwordx4 v219, s[92:93] sc1\n\t" \
  "s_waitcnt lgkmcnt(6)\n\t" \
  "v_mfma_f32_16x16x32_bf16 v[128:131], v[88:91], v[200:203], v[128:131]\n\t" \
  "v_mfma_f32_16x16x32_bf16 v[132:135], v[92:95], v[200:203], v[132:135]\n\t" \
  "ds_read_b128 v[200:203], v211 offset:33024\n\t" \
  "v_mfma_f32_16x16x32_bf16 v[136:139], v[88:91], v[204:207], v[136:139]\n\t" \
  "v_mfma_f32_16x16x32_bf16 v[140:143], v[92:95], v[204:207], v[140:143]\n\t" \
  "ds_read_b128 v[204:207], v211 offset:49408\n\t" \
  "s_cmp_lt_u32 s86, 6\n\t" \
  "s_cbranch_scc1 .Lw10%=\n\t" \
  "s_waitcnt vmcnt(5)\n\t" \
  "s_barrier\n\t" \
  ".Lg10%=:\n\t" \
  "s_add_u32 s92, s92, 65024\n\t" \
  "s_addc_u32 s93, s93, 0\n\t" \
  "s_waitcnt lgkmcnt(6)\n\t" \
  "v_mfma_f32_16x16x32_bf16 v[128:131], v[96:99], v[176:179], v[128:131]\n\t" \
  "v_mfma_f32_16x16x32_bf16 v[132:135], v[100:103], v[176:179], v[132:135]\n\t" \
  "ds_read_b128 v[176:179], v212 offset:0\n\t" \
  "s_add_u32 m0, %[lw], 0\n\t" \
  "v_mfma_f32_16x16x32_bf16 v[136:139], v[96:99], v[180:183], v[136:139]\n\t" \
  "v_mfma_f32_16x16x32_bf16 v[140:143], v[100:103], v[180:183], v[140:143]\n\t" \
  "ds_read_b128 v[180:183], v212 offset:16384\n\t" \
  "global_load_lds_dwordx4 v216, s[92:93] sc1\n\t" \
  "s_waitcnt lgkmcnt(6)\n\t" \
  "v_mfma_f32_16x16x32_bf16 v[128:131], v[104:107], v[184:187], v[128:131]\n\t" \
  "v_mfma_f32_16x16x32_bf16 v[132:135], v[108:111], v[184:187], v[132:135]\n\t" \
  "ds_read_b128 v[184:187], v213 offset:0\n\t" \
  "v_mfma_f32_16x16x32_bf16 v[136:139], v[104:107], v[188:191], v[136:139]\n\t" \
  "v_mfma_f32_16x16x32_bf16 v[140:143], v[108:111], v[188:191], v[140:143]\n\t" \
  "ds_read_b128 v[188:191], v213 offset:16384\n\t" \
  "s_waitcnt lgkmcnt(6)\n\t" \
  "v_mfma_f32_16x16x32_bf16 v[128:131], v[112:115], v[192:195], v[128:131]\n\t" \
  "v_mfma_f32_16x16x32_bf16 v[132:135], v[116:119], v[192:195], v[132:135]\n\t" \
  "ds_read_b128 v[192:195], v214 offset:0\n\t" \
  "s_add_u32 m0, %[lw], 1024\n\t" \
  "v_mfma_f32_16x16x32_bf16 v[136:139], v[112:115], v[196:199], v[136:139]\n\t" \
  "v_mfma_f32_16x16x32_bf16 v[140:143], v[116:119], v[196:199], v[140:143]\n\t" \
  "ds_read_b128 v[196:199], v214 offset:16384\n\t" \
  "global_load_lds_dwordx4 v217, s[92:93] sc1\n\t" \
  "s_waitcnt lgkmcnt(6)\n\t" \
  "v_mfma_f32_16x16x32_bf16 v[128:131], v[120:123], v[200:203], v[128:131]\n\t" \
  "v_mfma_f32_16x16x32_bf16 v[132:135], v[124:127], v[200:203], v[132:135]\n\t" \
  "ds_read_b128 v[200:203], v215 offset:0\n\t" \
  "v_mfma_f32_16x16x32_bf16 v[136:139], v[120:123], v[204:207], v[136:139]\n\t" \
  "v_mfma_f32_16x16x32_bf16 v[140:143], v[124:127], v[204:207], v[140:143]\n\t" \
  "ds_read_b128 v[204:207], v215 offset:16384\n\t" \
  "s_add_u32 s86, s86, 1\n\t" \
  "s_add_u32 s87, s87, 1\n\t" \
  "s_cmp_eq_u32 s86, 1\n\t" \
  "s_cbranch_scc1 .Lf0%=\n\t" \
  ".Lfb0%=:\n\t" \
  "s_waitcnt lgkmcnt(6)\n\t" \
  "v_mfma_f32_16x16x32_bf16 v[144:147], v[0:3], v[176:179], 0\n\t" \
  "v_mfma_f32_16x16x32_bf16 v[148:151], v[4:7], v[176:179], 0\n\t" \
  "ds_read_b128 v[176:179], v212 offset:256\n\t" \
  "s_add_u32 m0, %[lw], 2048\n\t" \
  "v_mfma_f32_16x16x32_bf16 v[152:155], v[0:3], v[180:183], 0\n\t" \
  "v_mfma_f32_16x16x32_bf16 v[156:159], v[4:7], v[180:183], 0\n\t" \
  "ds_read_b128 v[180:183], v212 offset:16640\n\t" \
  "global_load_lds_dwordx4 v218, s[92:93] sc1\n\t" \
  "s_waitcnt lgkmcnt(6)\n\t" \
  "v_mfma_f32_16x16x32_bf16 v[144:147], v[8:11], v[184:187], v[144:147]\n\t" \
  "v_mfma_f32_16x16x32_bf16 v[148:151], v[12:15], v[184:187], v[148:151]\n\t" \
  "ds_read_b128 v[184:187], v213 offset:256\n\t" \
  "v_mfma_f32_16x16x32_bf16 v[152:155], v[8:11], v[188:191], v[152:155]\n\t" \
  "v_mfma_f32_16x16x32_bf16 v[156:159], v[12:15], v[188:191], v[156:159]\n\t" \
  "ds_read_b128 v[188:191], v213 offset:16640\n\t" \
  "ds_write_b128 v220, v[136:139] offset:0\n\t" \
  "ds_write_b128 v220, v[140:143] offset:1024\n\t" \
  "s_waitcnt lgkmcnt(8)\n\t" \
  "v_mfma_f32_16x16x32_bf16 v[144:147], v[16:19], v[192:195], v[144:147]\n\t" \
  "v_mfma_f32_16x16x32_bf16 v[148:151], v[20:23], v[192:195], v[148:151]\n\t" \
  "ds_read_b128 v[192:195], v214 offset:256\n\t" \
  "s_add_u32 m0, %[lw], 3072\n\t" \
  "v_mfma_f32_16x16x32_bf16 v[152:155], v[16:19], v[196:199], v[152:155]\n\t" \
  "v_mfma_f32_16x16x32_bf16 v[156:159], v[20:23], v[196:199], v[156:159]\n\t" \
  "ds_read_b128 v[196:199], v214 offset:16640\n\t" \
  "global_load_lds_dwordx4 v219, s[92:93] sc1\n\t" \
  "s_lshr_b32 s90, s87, 7\n\t" \
  "s_xor_b32 s90, s90, %[s0]\n\t" \
  "s_and_b32 s90, s90, 1\n\t" \
  "s_lshl1_add_u32 s90, s87, s90\n\t" \
  "s_lshl_b32 s90, s90, 13\n\t" \
  "s_cmp_lt_i32 s86, 3\n\t" \
  "s_cselect_b32 s90, 0x70000000, s90\n\t" \
  "s_waitcnt lgkmcnt(8)\n\t" \
  "v_mfma_f32_16x16x32_bf16 v[144:147], v[24:27], v[200:203], v[144:147]\n\t" \
  "v_mfma_f32_16x16x32_bf16 v[148:151], v[28:31], v[200:203], v[148:151]\n\t" \
  "ds_read_b128 v[200:203], v215 offset:256\n\t" \
  "v_mfma_f32_16x16x32_bf16 v[152:155], v[24:27], v[204:207], v[152:155]\n\t" \
  "v_mfma_f32_16x16x32_bf16 v[156:159], v[28:31], v[204:207], v[156:159]\n\t" \
  "ds_read_b128 v[204:207], v215 offset:16640\n\t" \
  "s_waitcnt lgkmcnt(4)\n\t" \
  "s_waitcnt vmcnt(4)\n\t" \
  "s_barrier\n\t" \
  ".Lg20%=:\n\t" \
  "s_add_u32 s92, s92, 512\n\t" \
  "s_addc_u32 s93, s93, 0\n\t" \
  "v_mfma_f32_16x16x32_bf16 v[144:147], v[32:35], v[176:179], v[144:147]\n\t" \
  "v_mfma_f32_16x16x32_bf16 v[148:151], v[36:39], v[176:179], v[148:151]\n\t" \
  "ds_read_b128 v[176:179], v212 offset:32768\n\t" \
  "s_add_u32 m0, %[lw], 32768\n\t" \
  "v_mfma_f32_16x16x32_bf16 v[152:155], v[32:35], v[180:183], v[152:155]\n\t" \
  "v_mfma_f32_16x16x32_bf16 v[156:159], v[36:39], v[180:183], v[156:159]\n\t" \
  "ds_read_b128 v[180:183], v212 offset:49152\n\t" \
  "global_load_lds_dwordx4 v216, s[92:93] sc1\n\t" \
  "ds_read_b128 v[160:163], v221 offset:0\n\t" \
  "ds_read_b128 v[164:167], v221 offset:1024\n\t" \
  "v_mfma_f32_16x16x32_bf16 v[144:147], v[40:43], v[184:187], v[144:147]\n\t" \
  "v_mfma_f32_16x16x32_bf16 v[148:151], v[44:47], v[184:187], v[148:151]\n\t" \
  "ds_read_b128 v[184:187], v213 offset:32768\n\t" \
  "v_mfma_f32_16x16x32_bf16 v[152:155], v[40:43], v[188:191], v[152:155]\n\t" \
  "v_mfma_f32_16x16x32_bf16 v[156:159], v[44:47], v[188:191], v[156:159]\n\t" \
  "ds_read_b128 v[188:191], v213 offset:49152\n\t" \
  "s_waitcnt lgkmcnt(8)\n\t" \
  "v_mfma_f32_16x16x32_bf16 v[144:147], v[48:51], v[192:195], v[144:147]\n\t" \
  "v_mfma_f32_16x16x32_bf16 v[148:151], v[52:55], v[192:195], v[148:151]\n\t" \
  "ds_read_b128 v[192:195], v214 offset:32768\n\t" \
  "s_add_u32 m0, %[lw], 33792\n\t" \
  "v_mfma_f32_16x16x32_bf16 v[152:155], v[48:51], v[196:199], v[152:155]\n\t" \
  "v_mfma_f32_16x16x32_bf16 v[156:159], v[52:55], v[196:199], v[156:159]\n\t" \
  "ds_read_b128 v[196:199], v214 offset:49152\n\t" \
  "global_load_lds_dwordx4 v217, s[92:93] sc1\n\t" \
  "s_waitcnt lgkmcnt(4)\n\t" \
  "v_add_f32 v160, v160, v128\n\t" \
  "v_add_f32 v161, v161, v129\n\t" \
  "v_add_f32 v162, v162, v130\n\t" \
  "v_add_f32 v163, v163, v131\n\t" \
  "v_add_f32 v164, v164, v132\n\t" \
  "v_add_f32 v165, v165, v133\n\t" \
  "v_add_f32 v166, v166, v134\n\t" \
  "v_add_f32 v167, v167, v135\n\t" \
  "v_mfma_f32_16x16x32_bf16 v[144:147], v[56:59], v[200:203], v[144:147]\n\t" \
  "v_mfma_f32_16x16x32_bf16 v[148:151], v[60:63], v[200:203], v[148:151]\n\t" \
  "ds_read_b128 v[200:203], v215 offset:32768\n\t" \
  "v_mfma_f32_16x16x32_bf16 v[152:155], v[56:59], v[204:207], v[152:155]\n\t" \
  "v_mfma_f32_16x16x32_bf16 v[156:159], v[60:63], v[204:207], v[156:159]\n\t" \
  "ds_read_b128 v[204:207], v215 offset:49152\n\t" \
  "v_cvt_pk_bf16_f32 v168, v160, v161\n\t" \
  "v_cvt_pk_bf16_f32 v169, v162, v163\n\t" \
  "v_cvt_pk_bf16_f32 v170, v164, v165\n\t" \
  "v_cvt_pk_bf16_f32 v171, v166, v167\n\t" \
  "buffer_store_dwordx4 v[168:171], v172, %[rt], s90 offen sc1\n\t" \
  "v_mfma_f32_16x16x32_bf16 v[144:147], v[64:67], v[176:179], v[144:147]\n\t" \
  "v_mfma_f32_16x16x32_bf16 v[148:151], v[68:71], v[176:179], v[148:151]\n\t" \
  "ds_read_b128 v[176:179], v212 offset:33024\n\t" \
  "s_add_u32 m0, %[lw], 34816\n\t" \
  "v_mfma_f32_16x16x32_bf16 v[152:155], v[64:67], v[180:183], v[152:155]\n\t" \
  "v_mfma_f32_16x16x32_bf16 v[156:159], v[68:71], v[180:183], v[156:159]\n\t" \
  "ds_read_b128 v[180:183], v212 offset:49408\n\t" \
  "global_load_lds_dwordx4 v218, s[92:93] sc1\n\t" \
  "s_waitcnt lgkmcnt(6)\n\t" \
  "v_mfma_f32_16x16x32_bf16 v[144:147], v[72:75], v[184:187], v[144:147]\n\t" \
  "v_mfma_f32_16x16x32_bf16 v[148:151], v[76:79], v[184:187], v[148:151]\n\t" \
  "ds_read_b128 v[184:187], v213 offset:33024\n\t" \
  "v_mfma_f32_16x16x32_bf16 v[152:155], v[72:75], v[188:191], v[152:155]\n\t" \
  "v_mfma_f32_16x16x32_bf16 v[156:159], v[76:79], v[188:191], v[156:159]\n\t" \
  "ds_read_b128 v[188:191], v213 offset:49408\n\t" \
  "s_waitcnt lgkmcnt(6)\n\t" \
  "v_mfma_f32_16x16x32_bf16 v[144:147], v[80:83], v[192:195], v[144:147]\n\t" \
  "v_mfma_f32_16x16x32_bf16 v[148:151], v[84:87], v[192:195], v[148:151]\n\t" \
  "ds_read_b128 v[192:195], v214 offset:33024\n\t" \
  "s_add_u32 m0, %[lw], 35840\n\t" \
  "v_mfma_f32_16x16x32_bf16 v[152:155], v[80:83], v[196:199], v[152:155]\n\t" \
  "v_mfma_f32_16x16x32_bf16 v[156:159], v[84:87], v[196:199], v[156:159]\n\t" \
  "ds_read_b128 v[196:199], v214 offset:49408\n\t" \
  "global_load_lds_dwordx4 v219, s[92:93] sc1\n\t" \
  "s_waitcnt lgkmcnt(6)\n\t" \
  "v_mfma_f32_16x16x32_bf16 v[144:147], v[88:91], v[200:203], v[144:147]\n\t" \
  "v_mfma_f32_16x16x32_bf16 v[148:151], v[92:95], v[200:203], v[148:151]\n\t" \
  "ds_read_b128 v[200:203], v215 offset:33024\n\t" \
  "v_mfma_f32_16x16x32_bf16 v[152:155], v[88:91], v[204:207], v[152:155]\n\t" \
  "v_mfma_f32_16x16x32_bf16 v[156:159], v[92:95], v[204:207], v[156:159]\n\t" \
  "ds_read_b128 v[204:207], v215 offset:49408\n\t" \
  "s_cmp_lt_u32 s86, 6\n\t" \
  "s_cbranch_scc1 .Lw30%=\n\t" \
  "s_waitcnt vmcnt(5)\n\t" \
  "s_barrier\n\t" \
  ".Lg30%=:\n\t" \
  "s_add_u32 s92, s92, 65024\n\t" \
  "s_addc_u32 s93, s93, 0\n\t" \
  "s_waitcnt lgkmcnt(6)\n\t" \
  "v_mfma_f32_16x16x32_bf16 v[144:147], v[96:99], v[176:179], v[144:147]\n\t" \
  "v_mfma_f32_16x16x32_bf16 v[148:151], v[100:103], v[176:179], v[148:151]\n\t" \
  "ds_read_b128 v[176:179], v208 offset:0\n\t" \
  "s_add_u32 m0, %[lw], 65536\n\t" \
  "v_mfma_f32_16x16x32_bf16 v[152:155], v[96:99], v[180:183], v[152:155]\n\t" \
  "v_mfma_f32_16x16x32_bf16 v[156:159], v[100:103], v[180:183], v[156:159]\n\t" \
  "ds_read_b128 v[180:183], v208 offset:16384\n\t" \
  "global_load_lds_dwordx4 v216, s[92:93] sc1\n\t" \
  "s_waitcnt lgkmcnt(6)\n\t" \
  "v_mfma_f32_16x16x32_bf16 v[144:147], v[104:107], v[184:187], v[144:147]\n\t" \
  "v_mfma_f32_16x16x32_bf16 v[148:151], v[108:111], v[184:187], v[148:151]\n\t" \
  "ds_read_b128 v[184:187], v209 offset:0\n\t" \
  "v_mfma_f32_16x16x32_bf16 v[152:155], v[104:107], v[188:191], v[152:155]\n\t" \
  "v_mfma_f32_16x16x32_bf16 v[156:159], v[108:111], v[188:191], v[156:159]\n\t" \
  "ds_read_b128 v[188:191], v209 offset:16384\n\t" \
  "s_waitcnt lgkmcnt(6)\n\t" \
  "v_mfma_f32_16x16x32_bf16 v[144:147], v[112:115], v[192:195], v[144:147]\n\t" \
  "v_mfma_f32_16x16x32_bf16 v[148:151], v[116:119], v[192:195], v[148:151]\n\t" \
  "ds_read_b128 v[192:195], v210 offset:0\n\t" \
  "s_add_u32 m0, %[lw], 66560\n\t" \
  "v_mfma_f32_16x16x32_bf16 v[152:155], v[112:115], v[196:199], v[152:155]\n\t" \
  "v_mfma_f32_16x16x32_bf16 v[156:159], v[116:119], v[196:199], v[156:159]\n\t" \
  "ds_read_b128 v[196:199], v210 offset:16384\n\t" \
  "global_load_lds_dwordx4 v217, s[92:93] sc1\n\t" \
  "s_waitcnt lgkmcnt(6)\n\t" \
  "v_mfma_f32_16x16x32_bf16 v[144:147], v[120:123], v[200:203], v[144:147]\n\t" \
  "v_mfma_f32_16x16x32_bf16 v[148:151], v[124:127], v[200:203], v[148:151]\n\t" \
  "ds_read_b128 v[200:203], v211 offset:0\n\t" \
  "v_mfma_f32_16x16x32_bf16 v[152:155], v[120:123], v[204:207], v[152:155]\n\t" \
  "v_mfma_f32_16x16x32_bf16 v[156:159], v[124:127], v[204:207], v[156:159]\n\t" \
  "ds_read_b128 v[204:207], v211 offset:16384\n\t" \
  "s_add_u32 s86, s86, 1\n\t" \
  "s_add_u32 s87, s87, 1\n\t" \
  "s_cmp_eq_u32 s86, 2\n\t" \
  "s_cbranch_scc1 .Ll0%=\n\t" \
  "s_cmp_lt_u32 s86, %[end]\n\t" \
  "s_cbranch_scc1 .Lp0%=\n\t" \
  "s_cmp_lg_u32 s86, 64\n\t" \
  "s_cbranch_scc1 .Lx0%=\n\t" \
  ".Ll0%=:\n\t" \
  "s_nop 7\n\t" \
  "ds_write_b128 v220, v[152:155] offset:0\n\t" \
  "ds_write_b128 v220, v[156:159] offset:1024\n\t" \
  "s_lshr_b32 s90, s87, 7\n\t" \
  "s_xor_b32 s90, s90, %[s0]\n\t" \
  "s_and_b32 s90, s90, 1\n\t" \
  "s_lshl1_add_u32 s90, s87, s90\n\t" \
  "s_lshl_b32 s90, s90, 13\n\t" \
  "s_cmp_lt_i32 s86, 1\n\t" \
  "s_cselect_b32 s90, 0x70000000, s90\n\t" \
  "s_waitcnt lgkmcnt(0)\n\t" \
  "s_barrier\n\t" \
  "ds_read_b128 v[160:163], v221 offset:0\n\t" \
  "ds_read_b128 v[164:167], v221 offset:1024\n\t" \
  "s_waitcnt lgkmcnt(0)\n\t" \
  "v_add_f32 v160, v160, v144\n\t" \
  "v_add_f32 v161, v161, v145\n\t" \
  "v_add_f32 v162, v162, v146\n\t" \
  "v_add_f32 v163, v163, v147\n\t" \
  "v_add_f32 v164, v164, v148\n\t" \
  "v_add_f32 v165, v165, v149\n\t" \
  "v_add_f32 v166, v166, v150\n\t" \
  "v_add_f32 v167, v167, v151\n\t" \
  "v_cvt_pk_bf16_f32 v168, v160, v161\n\t" \
  "v_cvt_pk_bf16_f32 v169, v162, v163\n\t" \
  "v_cvt_pk_bf16_f32 v170, v164, v165\n\t" \
  "v_cvt_pk_bf16_f32 v171, v166, v167\n\t" \
  "buffer_store_dwordx4 v[168:171], v172, %[rt], s90 offen sc1\n\t" \
  "s_waitcnt vmcnt(0)\n\t" \
  "s_barrier\n\t" \
  "s_cmp_eq_u32 s86, 2\n\t" \
  "s_cbranch_scc1 .Lp0%=\n\t" \
  "s_branch .Lx0%=\n\t" \
  ".Lf0%=:\n\t" \
  "s_nop 7\n\t" \
  "ds_write_b128 v220, v[136:139] offset:0\n\t" \
  "ds_write_b128 v220, v[140:143] offset:1024\n\t" \
  "s_lshr_b32 s90, s87, 7\n\t" \
  "s_xor_b32 s90, s90, %[s0]\n\t" \
  "s_and_b32 s90, s90, 1\n\t" \
  "s_lshl1_add_u32 s90, s87, s90\n\t" \
  "s_lshl_b32 s90, s90, 13\n\t" \
  "s_cmp_lt_i32 s86, 1\n\t" \
  "s_cselect_b32 s90, 0x70000000, s90\n\t" \
  "s_waitcnt lgkmcnt(0)\n\t" \
  "s_barrier\n\t" \
  "ds_read_b128 v[160:163], v221 offset:0\n\t" \
  "ds_read_b128 v[164:167], v221 offset:1024\n\t" \
  "s_waitcnt lgkmcnt(0)\n\t" \
  "v_add_f32 v160, v160, v128\n\t" \
  "v_add_f32 v161, v161, v129\n\t" \
  "v_add_f32 v162, v162, v130\n\t" \
  "v_add_f32 v163, v163, v131\n\t" \
  "v_add_f32 v164, v164, v132\n\t" \
  "v_add_f32 v165, v165, v133\n\t" \
  "v_add_f32 v166, v166, v134\n\t" \
  "v_add_f32 v167, v167, v135\n\t" \
  "v_cvt_pk_bf16_f32 v168, v160, v161\n\t" \
  "v_cvt_pk_bf16_f32 v169, v162, v163\n\t" \
  "v_cvt_pk_bf16_f32 v170, v164, v165\n\t" \
  "v_cvt_pk_bf16_f32 v171, v166, v167\n\t" \
  "buffer_store_dwordx4 v[168:171], v172, %[rt], s90 offen sc1\n\t" \
  "s_waitcnt vmcnt(0)\n\t" \
  "s_barrier\n\t" \
  "s_branch .Lfb0%=\n\t" \
  ".Lw10%=:\n\t" \
  "s_waitcnt vmcnt(2)\n\t" \
  "s_barrier\n\t" \
  "s_branch .Lg10%=\n\t" \
  ".Lw30%=:\n\t" \
  "s_waitcnt vmcnt(2)\n\t" \
  "s_barrier\n\t" \
  "s_branch .Lg30%=\n\t" \
  ".Lx0%=:\n\t" \
  "s_waitcnt lgkmcnt(0)\n\t" \
  "s_setprio 0\n\t" \
  "s_mov_b32 m0, s94\n\t" \
  "s_branch .Le%=\n\t" \
  ".Lk%=:\n\t" \
  "s_mov_b32 s94, m0\n\t" \
  "s_mov_b64 s[92:93], %[x0]\n\t" \
  "s_mov_b32 s86, %[g0]\n\t" \
  "s_add_u32 s87, s86, %[t0]\n\t" \
  "s_sub_i32 s88, s86, 3\n\t" \
  "ds_read_b128 v[176:179], v208 offset:0\n\t" \
  "ds_read_b128 v[180:183], v208 offset:16384\n\t" \
  "ds_read_b128 v[184:187], v209 offset:0\n\t" \
  "ds_read_b128 v[188:191], v209 offset:16384\n\t" \
  "ds_read_b128 v[192:195], v210 offset:0\n\t" \
  "ds_read_b128 v[196:199], v210 offset:16384\n\t" \
  "ds_read_b128 v[200:203], v211 offset:0\n\t" \
  "ds_read_b128 v[204:207], v211 offset:16384\n\t" \
  ".Lp1%=:\n\t" \
  "s_waitcnt lgkmcnt(6)\n\t" \
  "v_mfma_f32_16x16x32_bf16 v[128:131], v[0:3], v[176:179], 0\n\t" \
  "v_mfma_f32_16x16x32_bf16 v[132:135], v[4:7], v[176:179], 0\n\t" \
  "ds_read_b128 v[176:179], v208 offset:256\n\t" \
  "v_mfma_f32_16x16x32_bf16 v[136:139], v[0:3], v[180:183], 0\n\t" \
  "v_mfma_f32_16x16x32_bf16 v[140:143], v[4:7], v[180:183], 0\n\t" \
  "ds_read_b128 v[180:183], v208 offset:16640\n\t" \
  "s_waitcnt lgkmcnt(6)\n\t" \
  "v_mfma_f32_16x16x32_bf16 v[128:131], v[8:11], v[184:187], v[128:131]\n\t" \
  "v_mfma_f32_16x16x32_bf16 v[132:135], v[12:15], v[184:187], v[132:135]\n\t" \
  "ds_read_b128 v[184:187], v209 offset:256\n\t" \
  "s_add_u32 m0, %[lw], 67584\n\t" \
  "v_mfma_f32_16x16x32_bf16 v[136:139], v[8:11], v[188:191], v[136:139]\n\t" \
  "v_mfma_f32_16x16x32_bf16 v[140:143], v[12:15], v[188:191], v[140:143]\n\t" \
  "ds_read_b128 v[188:191], v209 offset:16640\n\t" \
  "global_load_lds_dwordx4 v218, s[92:93] sc1\n\t" \
  "ds_write_b128 v220, v[152:155] offset:0\n\t" \
  "ds_write_b128 v220, v[156:159] offset:1024\n\t" \
  "s_waitcnt lgkmcnt(8)\n\t" \
  "v_mfma_f32_16x16x32_bf16 v[128:131], v[16:19], v[192:195], v[128:131]\n\t" \
  "v_mfma_f32_16x16x32_bf16 v[132:135], v[20:23], v[192:195], v[132:135]\n\t" \
  "ds_read_b128 v[192:195], v210 offset:256\n\t" \
  "v_mfma_f32_16x16x32_bf16 v[136:139], v[16:19], v[196:199], v[136:139]\n\t" \
  "v_mfma_f32_16x16x32_bf16 v[140:143], v[20:23], v[196:199], v[140:143]\n\t" \
  "ds_read_b128 v[196:199], v210 offset:16640\n\t" \
  "s_lshr_b32 s90, s87, 7\n\t" \
  "s_xor_b32 s90, s90, %[s0]\n\t" \
  "s_and_b32 s90, s90, 1\n\t" \
  "s_lshl1_add_u32 s90, s87, s90\n\t" \
  "s_lshl_b32 s90, s90, 13\n\t" \
  "s_cmp_lt_i32 s86, 3\n\t" \
  "s_cselect_b32 s90, 0x70000000, s90\n\t" \
  "s_waitcnt lgkmcnt(8)\n\t" \
  "v_mfma_f32_16x16x32_bf16 v[128:131], v[24:27], v[200:203], v[128:131]\n\t" \
  "v_mfma_f32_16x16x32_bf16 v[132:135], v[28:31], v[200:203], v[132:135]\n\t" \
  "ds_read_b128 v[200:203], v211 offset:256\n\t" \
  "s_add_u32 m0, %[lw], 68608\n\t" \
  "v_mfma_f32_16x16x32_bf16 v[136:139], v[24:27], v[204:207], v[136:139]\n\t" \
  "v_mfma_f32_16x16x32_bf16 v[140:143], v[28:31], v[204:207], v[140:143]\n\t" \
  "ds_read_b128 v[204:207], v211 offset:16640\n\t" \
  "global_load_lds_dwordx4 v219, s[92:93] sc1\n\t" \
  "s_waitcnt lgkmcnt(4)\n\t" \
  "s_waitcnt vmcnt(4)\n\t" \
  "s_barrier\n\t" \
  ".Lg01%=:\n\t" \
  "s_add_u32 s92, s92, 512\n\t" \
  "s_addc_u32 s93, s93, 0\n\t" \
  "v_mfma_f32_16x16x32_bf16 v[128:131], v[32:35], v[176:179], v[128:131]\n\t" \
  "v_mfma_f32_16x16x32_bf16 v[132:135], v[36:39], v[176:179], v[132:135]\n\t" \
  "ds_read_b128 v[176:179], v208 offset:32768\n\t" \
  "v_mfma_f32_16x16x32_bf16 v[136:139], v[32:35], v[180:183], v[136:139]\n\t" \
  "v_mfma_f32_16x16x32_bf16 v[140:143], v[36:39], v[180:183], v[140:143]\n\t" \
  "ds_read_b128 v[180:183], v208 offset:49152\n\t" \
  "ds_read_b128 v[160:163], v221 offset:0\n\t" \
  "ds_read_b128 v[164:167], v221 offset:1024\n\t" \
  "s_sub_i32 s91, s86, 2\n\t" \
  "s_max_i32 s88, s88, s91\n\t" \
  "v_mov_b32 v173, s88\n\t" \
  "buffer_store_dword v173, v174, %[rs], 0 offen sc1\n\t" \
  "v_mfma_f32_16x16x32_bf16 v[128:131], v[40:43], v[184:187], v[128:131]\n\t" \
  "v_mfma_f32_16x16x32_bf16 v[132:135], v[44:47], v[184:187], v[132:135]\n\t" \
  "ds_read_b128 v[184:187], v209 offset:32768\n\t" \
  "s_add_u32 m0, %[lw], 98304\n\t" \
  "v_mfma_f32_16x16x32_bf16 v[136:139], v[40:43], v[188:191], v[136:139]\n\t" \
  "v_mfma_f32_16x16x32_bf16 v[140:143], v[44:47], v[188:191], v[140:143]\n\t" \
  "ds_read_b128 v[188:191], v209 offset:49152\n\t" \
  "global_load_lds_dwordx4 v216, s[92:93] sc1\n\t" \
  "s_waitcnt lgkmcnt(8)\n\t" \
  "v_mfma_f32_16x16x32_bf16 v[128:131], v[48:51], v[192:195], v[128:131]\n\t" \
  "v_mfma_f32_16x16x32_bf16 v[132:135], v[52:55], v[192:195], v[132:135]\n\t" \
  "ds_read_b128 v[192:195], v210 offset:32768\n\t" \
  "v_mfma_f32_16x16x32_bf16 v[136:139], v[48:51], v[196:199], v[136:139]\n\t" \
  "v_mfma_f32_16x16x32_bf16 v[140:143], v[52:55], v[196:199], v[140:143]\n\t" \
  "ds_read_b128 v[196:199], v210 offset:49152\n\t" \
  "s_waitcnt lgkmcnt(4)\n\t" \
  "v_add_f32 v160, v160, v144\n\t" \
  "v_add_f32 v161, v161, v145\n\t" \
  "v_add_f32 v162, v162, v146\n\t" \
  "v_add_f32 v163, v163, v147\n\t" \
  "v_add_f32 v164, v164, v148\n\t" \
  "v_add_f32 v165, v165, v149\n\t" \
  "v_add_f32 v166, v166, v150\n\t" \
  "v_add_f32 v167, v167, v151\n\t" \
  "v_mfma_f32_16x16x32_bf16 v[128:131], v[56:59], v[200:203], v[128:131]\n\t" \
  "v_mfma_f32_16x16x32_bf16 v[132:135], v[60:63], v[200:203], v[132:135]\n\t" \
  "ds_read_b128 v[200:203], v211 offset:32768\n\t" \
  "s_add_u32 m0, %[lw], 99328\n\t" \
  "v_mfma_f32_16x16x32_bf16 v[136:139], v[56:59], v[204:207], v[136:139]\n\t" \
  "v_mfma_f32_16x16x32_bf16 v[140:143], v[60:63], v[204:207], v[140:143]\n\t" \
  "ds_read_b128 v[204:207], v211 offset:49152\n\t" \
  "global_load_lds_dwordx4 v217, s[92:93] sc1\n\t" \
  "v_cvt_pk_bf16_f32 v168, v160, v161\n\t" \
  "v_cvt_pk_bf16_f32 v169, v162, v163\n\t" \
  "v_cvt_pk_bf16_f32 v170, v164, v165\n\t" \
  "v_cvt_pk_bf16_f32 v171, v166, v167\n\t" \
  "buffer_store_dwordx4 v[168:171], v172, %[rt], s90 offen sc1\n\t" \
  "v_mfma_f32_16x16x32_bf16 v[128:131], v[64:67], v[176:179], v[128:131]\n\t" \
  "v_mfma_f32_16x16x32_bf16 v[132:135], v[68:71], v[176:179], v[132:135]\n\t" \
  "ds_read_b128 v[176:179], v208 offset:33024\n\t" \
  "v_mfma_f32_16x16x32_bf16 v[136:139], v[64:67], v[180:183], v[136:139]\n\t" \
  "v_mfma_f32_16x16x32_bf16 v[140:143], v[68:71], v[180:183], v[140:143]\n\t" \
  "ds_read_b128 v[180:183], v208 offset:49408\n\t" \
  "s_waitcnt lgkmcnt(6)\n\t" \
  "v_mfma_f32_16x16x32_bf16 v[128:131], v[72:75], v[184:187], v[128:131]\n\t" \
  "v_mfma_f32_16x16x32_bf16 v[132:135], v[76:79], v[184:187], v[132:135]\n\t" \
  "ds_read_b128 v[184:187], v209 offset:33024\n\t" \
  "s_add_u32 m0, %[lw], 100352\n\t" \
  "v_mfma_f32_16x16x32_bf16 v[136:139], v[72:75], v[188:191], v[136:139]\n\t" \
  "v_mfma_f32_16x16x32_bf16 v[140:143], v[76:79], v[188:191], v[140:143]\n\t" \
  "ds_read_b128 v[188:191], v209 offset:49408\n\t" \
  "global_load_lds_dwordx4 v218, s[92:93] sc1\n\t" \
  "s_waitcnt lgkmcnt(6)\n\t" \
  "v_mfma_f32_16x16x32_bf16 v[128:131], v[80:83], v[192:195], v[128:131]\n\t" \
  "v_mfma_f32_16x16x32_bf16 v[132:135], v[84:87], v[192:195], v[132:135]\n\t" \
  "ds_read_b128 v[192:195], v210 offset:33024\n\t" \
  "v_mfma_f32_16x16x32_bf16 v[136:139], v[80:83], v[196:199], v[136:139]\n\t" \
  "v_mfma_f32_16x16x32_bf16 v[140:143], v[84:87], v[196:199], v[140:143]\n\t" \
  "ds_read_b128 v[196:199], v210 offset:49408\n\t" \
  "s_waitcnt lgkmcnt(6)\n\t" \
  "v_mfma_f32_16x16x32_bf16 v[128:131], v[88:91], v[200:203], v[128:131]\n\t" \
  "v_mfma_f32_16x16x32_bf16 v[132:135], v[92:95], v[200:203], v[132:135]\n\t" \
  "ds_read_b128 v[200:203], v211 offset:33024\n\t" \
  "s_add_u32 m0, %[lw], 101376\n\t" \
  "v_mfma_f32_16x16x32_bf16 v[136:139], v[88:91], v[204:207], v[136:139]\n\t" \
  "v_mfma_f32_16x16x32_bf16 v[140:143], v[92:95], v[204:207], v[140:143]\n\t" \
  "ds_read_b128 v[204:207], v211 offset:49408\n\t" \
  "global_load_lds_dwordx4 v219, s[92:93] sc1\n\t" \
  "s_cmp_lt_u32 s86, 6\n\t" \
  "s_cbranch_scc1 .Lw11%=\n\t" \
  "s_waitcnt vmcnt(6)\n\t" \
  "s_barrier\n\t" \
  ".Lg11%=:\n\t" \
  "s_add_u32 s92, s92, 65024\n\t" \
  "s_addc_u32 s93, s93, 0\n\t" \
  "s_waitcnt lgkmcnt(6)\n\t" \
  "v_mfma_f32_16x16x32_bf16 v[128:131], v[96:99], v[176:179], v[128:131]\n\t" \
  "v_mfma_f32_16x16x32_bf16 v[132:135], v[100:103], v[176:179], v[132:135]\n\t" \
  "ds_read_b128 v[176:179], v212 offset:0\n\t" \
  "v_mfma_f32_16x16x32_bf16 v[136:139], v[96:99], v[180:183], v[136:139]\n\t" \
  "v_mfma_f32_16x16x32_bf16 v[140:143], v[100:103], v[180:183], v[140:143]\n\t" \
  "ds_read_b128 v[180:183], v212 offset:16384\n\t" \
  "s_waitcnt lgkmcnt(6)\n\t" \
  "v_mfma_f32_16x16x32_bf16 v[128:131], v[104:107], v[184:187], v[128:131]\n\t" \
  "v_mfma_f32_16x16x32_bf16 v[132:135], v[108:111], v[184:187], v[132:135]\n\t" \
  "ds_read_b128 v[184:187], v213 offset:0\n\t" \
  "s_add_u32 m0, %[lw], 0\n\t" \
  "v_mfma_f32_16x16x32_bf16 v[136:139], v[104:107], v[188:191], v[136:139]\n\t" \
  "v_mfma_f32_16x16x32_bf16 v[140:143], v[108:111], v[188:191], v[140:143]\n\t" \
  "ds_read_b128 v[188:191], v213 offset:16384\n\t" \
  "global_load_lds_dwordx4 v216, s[92:93] sc1\n\t" \
  "s_waitcnt lgkmcnt(6)\n\t" \
  "v_mfma_f32_16x16x32_bf16 v[128:131], v[112:115], v[192:195], v[128:131]\n\t" \
  "v_mfma_f32_16x16x32_bf16 v[132:135], v[116:119], v[192:195], v[132:135]\n\t" \
  "ds_read_b128 v[192:195], v214 offset:0\n\t" \
  "v_mfma_f32_16x16x32_bf16 v[136:139], v[112:115], v[196:199], v[136:139]\n\t" \
  "v_mfma_f32_16x16x32_bf16 v[140:143], v[116:119], v[196:199], v[140:143]\n\t" \
  "ds_read_b128 v[196:199], v214 offset:16384\n\t" \
  "s_waitcnt lgkmcnt(6)\n\t" \
  "v_mfma_f32_16x16x32_bf16 v[128:131], v[120:123], v[200:203], v[128:131]\n\t" \
  "v_mfma_f32_16x16x32_bf16 v[132:135], v[124:127], v[200:203], v[132:135]\n\t" \
  "ds_read_b128 v[200:203], v215 offset:0\n\t" \
  "s_add_u32 m0, %[lw], 1024\n\t" \
  "v_mfma_f32_16x16x32_bf16 v[136:139], v[120:123], v[204:207], v[136:139]\n\t" \
  "v_mfma_f32_16x16x32_bf16 v[140:143], v[124:127], v[204:207], v[140:143]\n\t" \
  "ds_read_b128 v[204:207], v215 offset:16384\n\t" \
  "global_load_lds_dwordx4 v217, s[92:93] sc1\n\t" \
  "s_add_u32 s86, s86, 1\n\t" \
  "s_add_u32 s87, s87, 1\n\t" \
  "s_cmp_eq_u32 s86, 1\n\t" \
  "s_cbranch_scc1 .Lf1%=\n\t" \
  ".Lfb1%=:\n\t" \
  "s_waitcnt lgkmcnt(6)\n\t" \
  "v_mfma_f32_16x16x32_bf16 v[144:147], v[0:3], v[176:179], 0\n\t" \
  "v_mfma_f32_16x16x32_bf16 v[148:151], v[4:7], v[176:179], 0\n\t" \
  "ds_read_b128 v[176:179], v212 offset:256\n\t" \
  "v_mfma_f32_16x16x32_bf16 v[152:155], v[0:3], v[180:183], 0\n\t" \
  "v_mfma_f32_16x16x32_bf16 v[156:159], v[4:7], v[180:183], 0\n\t" \
  "ds_read_b128 v[180:183], v212 offset:16640\n\t" \
  "s_waitcnt lgkmcnt(6)\n\t" \
  "v_mfma_f32_16x16x32_bf16 v[144:147], v[8:11], v[184:187], v[144:147]\n\t" \
  "v_mfma_f32_16x16x32_bf16 v[148:151], v[12:15], v[184:187], v[148:151]\n\t" \
  "ds_read_b128 v[184:187], v213 offset:256\n\t" \
  "s_add_u32 m0, %[lw], 2048\n\t" \
  "v_mfma_f32_16x16x32_bf16 v[152:155], v[8:11], v[188:191], v[152:155]\n\t" \
  "v_mfma_f32_16x16x32_bf16 v[156:159], v[12:15], v[188:191], v[156:159]\n\t" \
  "ds_read_b128 v[188:191], v213 offset:16640\n\t" \
  "global_load_lds_dwordx4 v218, s[92:93] sc1\n\t" \
  "ds_write_b128 v220, v[136:139] offset:0\n\t" \
  "ds_write_b128 v220, v[140:143] offset:1024\n\t" \
  "s_waitcnt lgkmcnt(8)\n\t" \
  "v_mfma_f32_16x16x32_bf16 v[144:147], v[16:19], v[192:195], v[144:147]\n\t" \
  "v_mfma_f32_16x16x32_bf16 v[148:151], v[20:23], v[192:195], v[148:151]\n\t" \
  "ds_read_b128 v[192:195], v214 offset:256\n\t" \
  "v_mfma_f32_16x16x32_bf16 v[152:155], v[16:19], v[196:199], v[152:155]\n\t" \
  "v_mfma_f32_16x16x32_bf16 v[156:159], v[20:23], v[196:199], v[156:159]\n\t" \
  "ds_read_b128 v[196:199], v214 offset:16640\n\t" \
  "s_lshr_b32 s90, s87, 7\n\t" \
  "s_xor_b32 s90, s90, %[s0]\n\t" \
  "s_and_b32 s90, s90, 1\n\t" \
  "s_lshl1_add_u32 s90, s87, s90\n\t" \
  "s_lshl_b32 s90, s90, 13\n\t" \
  "s_cmp_lt_i32 s86, 3\n\t" \
  "s_cselect_b32 s90, 0x70000000, s90\n\t" \
  "s_waitcnt lgkmcnt(8)\n\t" \
  "v_mfma_f32_16x16x32_bf16 v[144:147], v[24:27], v[200:203], v[144:147]\n\t" \
  "v_mfma_f32_16x16x32_bf16 v[148:151], v[28:31], v[200:203], v[148:151]\n\t" \
  "ds_read_b128 v[200:203], v215 offset:256\n\t" \
  "s_add_u32 m0, %[lw], 3072\n\t" \
  "v_mfma_f32_16x16x32_bf16 v[152:155], v[24:27], v[204:207], v[152:155]\n\t" \
  "v_mfma_f32_16x16x32_bf16 v[156:159], v[28:31], v[204:207], v[156:159]\n\t" \
  "ds_read_b128 v[204:207], v215 offset:16640\n\t" \
  "global_load_lds_dwordx4 v219, s[92:93] sc1\n\t" \
  "s_waitcnt lgkmcnt(4)\n\t" \
  "s_waitcnt vmcnt(4)\n\t" \
  "s_barrier\n\t" \
  ".Lg21%=:\n\t" \
  "s_add_u32 s92, s92, 512\n\t" \
  "s_addc_u32 s93, s93, 0\n\t" \
  "v_mfma_f32_16x16x32_bf16 v[144:147], v[32:35], v[176:179], v[144:147]\n\t" \
  "v_mfma_f32_16x16x32_bf16 v[148:151], v[36:39], v[176:179], v[148:151]\n\t" \
  "ds_read_b128 v[176:179], v212 offset:32768\n\t" \
  "v_mfma_f32_16x16x32_bf16 v[152:155], v[32:35], v[180:183], v[152:155]\n\t" \
  "v_mfma_f32_16x16x32_bf16 v[156:159], v[36:39], v[180:183], v[156:159]\n\t" \
  "ds_read_b128 v[180:183], v212 offset:49152\n\t" \
  "ds_read_b128 v[160:163], v221 offset:0\n\t" \
  "ds_read_b128 v[164:167], v221 offset:1024\n\t" \
  "s_sub_i32 s91, s86, 2\n\t" \
  "s_max_i32 s88, s88, s91\n\t" \
  "v_mov_b32 v173, s88\n\t" \
  "buffer_store_dword v173, v174, %[rs], 0 offen sc1\n\t" \
  "v_mfma_f32_16x16x32_bf16 v[144:147], v[40:43], v[184:187], v[144:147]\n\t" \
  "v_mfma_f32_16x16x32_bf16 v[148:151], v[44:47], v[184:187], v[148:151]\n\t" \
  "ds_read_b128 v[184:187], v213 offset:32768\n\t" \
  "s_add_u32 m0, %[lw], 32768\n\t" \
  "v_mfma_f32_16x16x32_bf16 v[152:155], v[40:43], v[188:191], v[152:155]\n\t" \
  "v_mfma_f32_16x16x32_bf16 v[156:159], v[44:47], v[188:191], v[156:159]\n\t" \
  "ds_read_b128 v[188:191], v213 offset:49152\n\t" \
  "global_load_lds_dwordx4 v216, s[92:93] sc1\n\t" \
  "s_waitcnt lgkmcnt(8)\n\t" \
  "v_mfma_f32_16x16x32_bf16 v[144:147], v[48:51], v[192:195], v[144:147]\n\t" \
  "v_mfma_f32_16x16x32_bf16 v[148:151], v[52:55], v[192:195], v[148:151]\n\t" \
  "ds_read_b128 v[192:195], v214 offset:32768\n\t" \
  "v_mfma_f32_16x16x32_bf16 v[152:155], v[48:51], v[196:199], v[152:155]\n\t" \
  "v_mfma_f32_16x16x32_bf16 v[156:159], v[52:55], v[196:199], v[156:159]\n\t" \
  "ds_read_b128 v[196:199], v214 offset:49152\n\t" \
  "s_waitcnt lgkmcnt(4)\n\t" \
  "v_add_f32 v160, v160, v128\n\t" \
  "v_add_f32 v161, v161, v129\n\t" \
  "v_add_f32 v162, v162, v130\n\t" \
  "v_add_f32 v163, v163, v131\n\t" \
  "v_add_f32 v164, v164, v132\n\t" \
  "v_add_f32 v165, v165, v133\n\t" \
  "v_add_f32 v166, v166, v134\n\t" \
  "v_add_f32 v167, v167, v135\n\t" \
  "v_mfma_f32_16x16x32_bf16 v[144:147], v[56:59], v[200:203], v[144:147]\n\t" \
  "v_mfma_f32_16x16x32_bf16 v[148:151], v[60:63], v[200:203], v[148:151]\n\t" \
  "ds_read_b128 v[200:203], v215 offset:32768\n\t" \
  "s_add_u32 m0, %[lw], 33792\n\t" \
  "v_mfma_f32_16x16x32_bf16 v[152:155], v[56:59], v[204:207], v[152:155]\n\t" \
  "v_mfma_f32_16x16x32_bf16 v[156:159], v[60:63], v[204:207], v[156:159]\n\t" \
  "ds_read_b128 v[204:207], v215 offset:49152\n\t" \
  "global_load_lds_dwordx4 v217, s[92:93] sc1\n\t" \
  "v_cvt_pk_bf16_f32 v168, v160, v161\n\t" \
  "v_cvt_pk_bf16_f32 v169, v162, v163\n\t" \
  "v_cvt_pk_bf16_f32 v170, v164, v165\n\t" \
  "v_cvt_pk_bf16_f32 v171, v166, v167\n\t" \
  "buffer_store_dwordx4 v[168:171], v172, %[rt], s90 offen sc1\n\t" \
  "v_mfma_f32_16x16x32_bf16 v[144:147], v[64:67], v[176:179], v[144:147]\n\t" \
  "v_mfma_f32_16x16x32_bf16 v[148:151], v[68:71], v[176:179], v[148:151]\n\t" \
  "ds_read_b128 v[176:179], v212 offset:33024\n\t" \
  "v_mfma_f32_16x16x32_bf16 v[152:155], v[64:67], v[180:183], v[152:155]\n\t" \
  "v_mfma_f32_16x16x32_bf16 v[156:159], v[68:71], v[180:183], v[156:159]\n\t" \
  "ds_read_b128 v[180:183], v212 offset:49408\n\t" \
  "s_waitcnt lgkmcnt(6)\n\t" \
  "v_mfma_f32_16x16x32_bf16 v[144:147], v[72:75], v[184:187], v[144:147]\n\t" \
  "v_mfma_f32_16x16x32_bf16 v[148:151], v[76:79], v[184:187], v[148:151]\n\t" \
  "ds_read_b128 v[184:187], v213 offset:33024\n\t" \
  "s_add_u32 m0, %[lw], 34816\n\t" \
  "v_mfma_f32_16x16x32_bf16 v[152:155], v[72:75], v[188:191], v[152:155]\n\t" \
  "v_mfma_f32_16x16x32_bf16 v[156:159], v[76:79], v[188:191], v[156:159]\n\t" \
  "ds_read_b128 v[188:191], v213 offset:49408\n\t" \
  "global_load_lds_dwordx4 v218, s[92:93] sc1\n\t" \
  "s_waitcnt lgkmcnt(6)\n\t" \
  "v_mfma_f32_16x16x32_bf16 v[144:147], v[80:83], v[192:195], v[144:147]\n\t" \
  "v_mfma_f32_16x16x32_bf16 v[148:151], v[84:87], v[192:195], v[148:151]\n\t" \
  "ds_read_b128 v[192:195], v214 offset:33024\n\t" \
  "v_mfma_f32_16x16x32_bf16 v[152:155], v[80:83], v[196:199], v[152:155]\n\t" \
  "v_mfma_f32_16x16x32_bf16 v[156:159], v[84:87], v[196:199], v[156:159]\n\t" \
  "ds_read_b128 v[196:199], v214 offset:49408\n\t" \
  "s_waitcnt lgkmcnt(6)\n\t" \
  "v_mfma_f32_16x16x32_bf16 v[144:147], v[88:91], v[200:203], v[144:147]\n\t" \
  "v_mfma_f32_16x16x32_bf16 v[148:151], v[92:95], v[200:203], v[148:151]\n\t" \
  "ds_read_b128 v[200:203], v215 offset:33024\n\t" \
  "s_add_u32 m0, %[lw], 35840\n\t" \
  "v_mfma_f32_16x16x32_bf16 v[152:155], v[88:91], v[204:207], v[152:155]\n\t" \
  "v_mfma_f32_16x16x32_bf16 v[156:159], v[92:95], v[204:207], v[156:159]\n\t" \
  "ds_read_b128 v[204:207], v215 offset:49408\n\t" \
  "global_load_lds_dwordx4 v219, s[92:93] sc1\n\t" \
  "s_cmp_lt_u32 s86, 6\n\t" \
  "s_cbranch_scc1 .Lw31%=\n\t" \
  "s_waitcnt vmcnt(6)\n\t" \
  "s_barrier\n\t" \
  ".Lg31%=:\n\t" \
  "s_add_u32 s92, s92, 65024\n\t" \
  "s_addc_u32 s93, s93, 0\n\t" \
  "s_waitcnt lgkmcnt(6)\n\t" \
  "v_mfma_f32_16x16x32_bf16 v[144:147], v[96:99], v[176:179], v[144:147]\n\t" \
  "v_mfma_f32_16x16x32_bf16 v[148:151], v[100:103], v[176:179], v[148:151]\n\t" \
  "ds_read_b128 v[176:179], v208 offset:0\n\t" \
  "v_mfma_f32_16x16x32_bf16 v[152:155], v[96:99], v[180:183], v[152:155]\n\t" \
  "v_mfma_f32_16x16x32_bf16 v[156:159], v[100:103], v[180:183], v[156:159]\n\t" \
  "ds_read_b128 v[180:183], v208 offset:16384\n\t" \
  "s_waitcnt lgkmcnt(6)\n\t" \
  "v_mfma_f32_16x16x32_bf16 v[144:147], v[104:107], v[184:187], v[144:147]\n\t" \
  "v_mfma_f32_16x16x32_bf16 v[148:151], v[108:111], v[184:187], v[148:151]\n\t" \
  "ds_read_b128 v[184:187], v209 offset:0\n\t" \
  "s_add_u32 m0, %[lw], 65536\n\t" \
  "v_mfma_f32_16x16x32_bf16 v[152:155], v[104:107], v[188:191], v[152:155]\n\t" \
  "v_mfma_f32_16x16x32_bf16 v[156:159], v[108:111], v[188:191], v[156:159]\n\t" \
  "ds_read_b128 v[188:191], v209 offset:16384\n\t" \
  "global_load_lds_dwordx4 v216, s[92:93] sc1\n\t" \
  "s_waitcnt lgkmcnt(6)\n\t" \
  "v_mfma_f32_16x16x32_bf16 v[144:147], v[112:115], v[192:195], v[144:147]\n\t" \
  "v_mfma_f32_16x16x32_bf16 v[148:151], v[116:119], v[192:195], v[148:151]\n\t" \
  "ds_read_b128 v[192:195], v210 offset:0\n\t" \
  "v_mfma_f32_16x16x32_bf16 v[152:155], v[112:115], v[196:199], v[152:155]\n\t" \
  "v_mfma_f32_16x16x32_bf16 v[156:159], v[116:119], v[196:199], v[156:159]\n\t" \
  "ds_read_b128 v[196:199], v210 offset:16384\n\t" \
  "s_waitcnt lgkmcnt(6)\n\t" \
  "v_mfma_f32_16x16x32_bf16 v[144:147], v[120:123], v[200:203], v[144:147]\n\t" \
  "v_mfma_f32_16x16x32_bf16 v[148:151], v[124:127], v[200:203], v[148:151]\n\t" \
  "ds_read_b128 v[200:203], v211 offset:0\n\t" \
  "s_add_u32 m0, %[lw], 66560\n\t" \
  "v_mfma_f32_16x16x32_bf16 v[152:155], v[120:123], v[204:207], v[152:155]\n\t" \
  "v_mfma_f32_16x16x32_bf16 v[156:159], v[124:127], v[204:207], v[156:159]\n\t" \
  "ds_read_b128 v[204:207], v211 offset:16384\n\t" \
  "global_load_lds_dwordx4 v217, s[92:93] sc1\n\t" \
  "s_add_u32 s86, s86, 1\n\t" \
  "s_add_u32 s87, s87, 1\n\t" \
  "s_cmp_eq_u32 s86, 2\n\t" \
  "s_cbranch_scc1 .Ll1%=\n\t" \
  "s_cmp_lt_u32 s86, %[end]\n\t" \
  "s_cbranch_scc1 .Lp1%=\n\t" \
  "s_cmp_lg_u32 s86, 64\n\t" \
  "s_cbranch_scc1 .Lx1%=\n\t" \
  ".Ll1%=:\n\t" \
  "s_nop 7\n\t" \
  "ds_write_b128 v220, v[152:155] offset:0\n\t" \
  "ds_write_b128 v220, v[156:159] offset:1024\n\t" \
  "s_lshr_b32 s90, s87, 7\n\t" \
  "s_xor_b32 s90, s90, %[s0]\n\t" \
  "s_and_b32 s90, s90, 1\n\t" \
  "s_lshl1_add_u32 s90, s87, s90\n\t" \
  "s_lshl_b32 s90, s90, 13\n\t" \
  "s_cmp_lt_i32 s86, 1\n\t" \
  "s_cselect_b32 s90, 0x70000000, s90\n\t" \
  "s_waitcnt lgkmcnt(0)\n\t" \
  "s_barrier\n\t" \
  "ds_read_b128 v[160:163], v221 offset:0\n\t" \
  "ds_read_b128 v[164:167], v221 offset:1024\n\t" \
  "s_waitcnt lgkmcnt(0)\n\t" \
  "v_add_f32 v160, v160, v144\n\t" \
  "v_add_f32 v161, v161, v145\n\t" \
  "v_add_f32 v162, v162, v146\n\t" \
  "v_add_f32 v163, v163, v147\n\t" \
  "v_add_f32 v164, v164, v148\n\t" \
  "v_add_f32 v165, v165, v149\n\t" \
  "v_add_f32 v166, v166, v150\n\t" \
  "v_add_f32 v167, v167, v151\n\t" \
  "v_cvt_pk_bf16_f32 v168, v160, v161\n\t" \
  "v_cvt_pk_bf16_f32 v169, v162, v163\n\t" \
  "v_cvt_pk_bf16_f32 v170, v164, v165\n\t" \
  "v_cvt_pk_bf16_f32 v171, v166, v167\n\t" \
  "buffer_store_dwordx4 v[168:171], v172, %[rt], s90 offen sc1\n\t" \
  "s_waitcnt vmcnt(0)\n\t" \
  "s_barrier\n\t" \
  "s_sub_i32 s91, s86, 1\n\t" \
  "s_max_i32 s88, s88, s91\n\t" \
  "v_mov_b32 v173, s88\n\t" \
  "buffer_store_dword v173, v174, %[rs], 0 offen sc1\n\t" \
  "s_cmp_eq_u32 s86, 2\n\t" \
  "s_cbranch_scc1 .Lp1%=\n\t" \
  "s_branch .Lx1%=\n\t" \
  ".Lf1%=:\n\t" \
  "s_nop 7\n\t" \
  "ds_write_b128 v220, v[136:139] offset:0\n\t" \
  "ds_write_b128 v220, v[140:143] offset:1024\n\t" \
  "s_lshr_b32 s90, s87, 7\n\t" \
  "s_xor_b32 s90, s90, %[s0]\n\t" \
  "s_and_b32 s90, s90, 1\n\t" \
  "s_lshl1_add_u32 s90, s87, s90\n\t" \
  "s_lshl_b32 s90, s90, 13\n\t" \
  "s_cmp_lt_i32 s86, 1\n\t" \
  "s_cselect_b32 s90, 0x70000000, s90\n\t" \
  "s_waitcnt lgkmcnt(0)\n\t" \
  "s_barrier\n\t" \
  "ds_read_b128 v[160:163], v221 offset:0\n\t" \
  "ds_read_b128 v[164:167], v221 offset:1024\n\t" \
  "s_waitcnt lgkmcnt(0)\n\t" \
  "v_add_f32 v160, v160, v128\n\t" \
  "v_add_f32 v161, v161, v129\n\t" \
  "v_add_f32 v162, v162, v130\n\t" \
  "v_add_f32 v163, v163, v131\n\t" \
  "v_add_f32 v164, v164, v132\n\t" \
  "v_add_f32 v165, v165, v133\n\t" \
  "v_add_f32 v166, v166, v134\n\t" \
  "v_add_f32 v167, v167, v135\n\t" \
  "v_cvt_pk_bf16_f32 v168, v160, v161\n\t" \
  "v_cvt_pk_bf16_f32 v169, v162, v163\n\t" \
  "v_cvt_pk_bf16_f32 v170, v164, v165\n\t" \
  "v_cvt_pk_bf16_f32 v171, v166, v167\n\t" \
  "buffer_store_dwordx4 v[168:171], v172, %[rt], s90 offen sc1\n\t" \
  "s_waitcnt vmcnt(0)\n\t" \
  "s_barrier\n\t" \
  "s_sub_i32 s91, s86, 1\n\t" \
  "s_max_i32 s88, s88, s91\n\t" \
  "v_mov_b32 v173, s88\n\t" \
  "buffer_store_dword v173, v174, %[rs], 0 offen sc1\n\t" \
  "s_branch .Lfb1%=\n\t" \
  ".Lw11%=:\n\t" \
  "s_waitcnt vmcnt(2)\n\t" \
  "s_barrier\n\t" \
  "s_sub_i32 s91, s86, 1\n\t" \
  "s_max_i32 s88, s88, s91\n\t" \
  "v_mov_b32 v173, s88\n\t" \
  "buffer_store_dword v173, v174, %[rs], 0 offen sc1\n\t" \
  "s_branch .Lg11%=\n\t" \
  ".Lw31%=:\n\t" \
  "s_waitcnt vmcnt(2)\n\t" \
  "s_barrier\n\t" \
  "s_sub_i32 s91, s86, 1\n\t" \
  "s_max_i32 s88, s88, s91\n\t" \
  "v_mov_b32 v173, s88\n\t" \
  "buffer_store_dword v173, v174, %[rs], 0 offen sc1\n\t" \
  "s_branch .Lg31%=\n\t" \
  ".Lx1%=:\n\t" \
  "s_waitcnt lgkmcnt(0)\n\t" \
  "s_setprio 0\n\t" \
  "s_mov_b32 m0, s94\n\t" \
  ".Le%=:\n\t"
#define PR_ASM_1 \
  "s_cmp_lg_u32 %[kh], 0\n\t" \
  "s_cbranch_scc1 .Lk%=\n\t" \
  "s_mov_b32 s94, m0\n\t" \
  "s_mov_b64 s[92:93], %[x0]\n\t" \
  "s_mov_b32 s86, %[g0]\n\t" \
  "s_add_u32 s87, s86, %[t0]\n\t" \
  "s_sub_i32 s88, s86, 3\n\t" \
  "ds_read_b128 v[176:179], v208 offset:0\n\t" \
  "ds_read_b128 v[180:183], v208 offset:16384\n\t" \
  "ds_read_b128 v[184:187], v209 offset:0\n\t" \
  "ds_read_b128 v[188:191], v209 offset:16384\n\t" \
  "ds_read_b128 v[192:195], v210 offset:0\n\t" \
  "ds_read_b128 v[196:199], v210 offset:16384\n\t" \
  "ds_read_b128 v[200:203], v211 offset:0\n\t" \
  "ds_read_b128 v[204:207], v211 offset:16384\n\t" \
  ".Lp0%=:\n\t" \
  "s_waitcnt lgkmcnt(6)\n\t" \
  "v_mfma_f32_16x16x32_bf16 v[128:131], v[176:179], v[0:3], 0\n\t" \
  "v_mfma_f32_16x16x32_bf16 v[136:139], v[176:179], v[4:7], 0\n\t" \
  "ds_read_b128 v[176:179], v208 offset:256\n\t" \
  "s_add_u32 m0, %[lw], 67584\n\t" \
  "v_mfma_f32_16x16x32_bf16 v[132:135], v[180:183], v[0:3], 0\n\t" \
  "v_mfma_f32_16x16x32_bf16 v[140:143], v[180:183], v[4:7], 0\n\t" \
  "ds_read_b128 v[180:183], v208 offset:16640\n\t" \
  "global_load_lds_dwordx4 v218, s[92:93] sc1\n\t" \
  "s_waitcnt lgkmcnt(6)\n\t" \
  "v_mfma_f32_16x16x32_bf16 v[128:131], v[184:187], v[8:11], v[128:131]\n\t" \
  "v_mfma_f32_16x16x32_bf16 v[136:139], v[184:187], v[12:15], v[136:139]\n\t" \
  "ds_read_b128 v[184:187], v209 offset:256\n\t" \
  "v_mfma_f32_16x16x32_bf16 v[132:135], v[188:191], v[8:11], v[132:135]\n\t" \
  "v_mfma_f32_16x16x32_bf16 v[140:143], v[188:191], v[12:15], v[140:143]\n\t" \
  "ds_read_b128 v[188:191], v209 offset:16640\n\t" \
  "ds_write_b128 v220, v[152:155] offset:0\n\t" \
  "ds_write_b128 v220, v[156:159] offset:1024\n\t" \
  "s_waitcnt lgkmcnt(8)\n\t" \
  "v_mfma_f32_16x16x32_bf16 v[128:131], v[192:195], v[16:19], v[128:131]\n\t" \
  "v_mfma_f32_16x16x32_bf16 v[136:139], v[192:195], v[20:23], v[136:139]\n\t" \
  "ds_read_b128 v[192:195], v210 offset:256\n\t" \
  "s_add_u32 m0, %[lw], 68608\n\t" \
  "v_mfma_f32_16x16x32_bf16 v[132:135], v[196:199], v[16:19], v[132:135]\n\t" \
  "v_mfma_f32_16x16x32_bf16 v[140:143], v[196:199], v[20:23], v[140:143]\n\t" \
  "ds_read_b128 v[196:199], v210 offset:16640\n\t" \
  "global_load_lds_dwordx4 v219, s[92:93] sc1\n\t" \
  "s_lshr_b32 s90, s87, 7\n\t" \
  "s_xor_b32 s90, s90, %[s0]\n\t" \
  "s_and_b32 s90, s90, 1\n\t" \
  "s_lshl1_add_u32 s90, s87, s90\n\t" \
  "s_lshl_b32 s90, s90, 13\n\t" \
  "s_cmp_lt_i32 s86, 3\n\t" \
  "s_cselect_b32 s90, 0x70000000, s90\n\t" \
  "s_waitcnt lgkmcnt(8)\n\t" \
  "v_mfma_f32_16x16x32_bf16 v[128:131], v[200:203], v[24:27], v[128:131]\n\t" \
  "v_mfma_f32_16x16x32_bf16 v[136:139], v[200:203], v[28:31], v[136:139]\n\t" \
  "ds_read_b128 v[200:203], v211 offset:256\n\t" \
  "v_mfma_f32_16x16x32_bf16 v[132:135], v[204:207], v[24:27], v[132:135]\n\t" \
  "v_mfma_f32_16x16x32_bf16 v[140:143], v[204:207], v[28:31], v[140:143]\n\t" \
  "ds_read_b128 v[204:207], v211 offset:16640\n\t" \
  "s_waitcnt lgkmcnt(4)\n\t" \
  "s_waitcnt vmcnt(4)\n\t" \
  "s_barrier\n\t" \
  ".Lg00%=:\n\t" \
  "s_add_u32 s92, s92, 512\n\t" \
  "s_addc_u32 s93, s93, 0\n\t" \
  "v_mfma_f32_16x16x32_bf16 v[128:131], v[176:179], v[32:35], v[128:131]\n\t" \
  "v_mfma_f32_16x16x32_bf16 v[136:139], v[176:179], v[36:39], v[136:139]\n\t" \
  "ds_read_b128 v[176:179], v208 offset:32768\n\t" \
  "s_add_u32 m0, %[lw], 98304\n\t" \
  "v_mfma_f32_16x16x32_bf16 v[132:135], v[180:183], v[32:35], v[132:135]\n\t" \
  "v_mfma_f32_16x16x32_bf16 v[140:143], v[180:183], v[36:39], v[140:143]\n\t" \
  "ds_read_b128 v[180:183], v208 offset:49152\n\t" \
  "global_load_lds_dwordx4 v216, s[92:93] sc1\n\t" \
  "ds_read_b128 v[160:163], v221 offset:0\n\t" \
  "ds_read_b128 v[164:167], v221 offset:1024\n\t" \
  "v_mfma_f32_16x16x32_bf16 v[128:131], v[184:187], v[40:43], v[128:131]\n\t" \
  "v_mfma_f32_16x16x32_bf16 v[136:139], v[184:187], v[44:47], v[136:139]\n\t" \
  "ds_read_b128 v[184:187], v209 offset:32768\n\t" \
  "v_mfma_f32_16x16x32_bf16 v[132:135], v[188:191], v[40:43], v[132:135]\n\t" \
  "v_mfma_f32_16x16x32_bf16 v[140:143], v[188:191], v[44:47], v[140:143]\n\t" \
  "ds_read_b128 v[188:191], v209 offset:49152\n\t" \
  "s_waitcnt lgkmcnt(8)\n\t" \
  "v_mfma_f32_16x16x32_bf16 v[128:131], v[192:195], v[48:51], v[128:131]\n\t" \
  "v_mfma_f32_16x16x32_bf16 v[136:139], v[192:195], v[52:55], v[136:139]\n\t" \
  "ds_read_b128 v[192:195], v210 offset:32768\n\t" \
  "s_add_u32 m0, %[lw], 99328\n\t" \
  "v_mfma_f32_16x16x32_bf16 v[132:135], v[196:199], v[48:51], v[132:135]\n\t" \
  "v_mfma_f32_16x16x32_bf16 v[140:143], v[196:199], v[52:55], v[140:143]\n\t" \
  "ds_read_b128 v[196:199], v210 offset:49152\n\t" \
  "global_load_lds_dwordx4 v217, s[92:93] sc1\n\t" \
  "s_waitcnt lgkmcnt(4)\n\t" \
  "v_add_f32 v160, v160, v144\n\t" \
  "v_add_f32 v161, v161, v145\n\t" \
  "v_add_f32 v162, v162, v146\n\t" \
  "v_add_f32 v163, v163, v147\n\t" \
  "v_add_f32 v164, v164, v148\n\t" \
  "v_add_f32 v165, v165, v149\n\t" \
  "v_add_f32 v166, v166, v150\n\t" \
  "v_add_f32 v167, v167, v151\n\t" \
  "v_mfma_f32_16x16x32_bf16 v[128:131], v[200:203], v[56:59], v[128:131]\n\t" \
  "v_mfma_f32_16x16x32_bf16 v[136:139], v[200:203], v[60:63], v[136:139]\n\t" \
  "ds_read_b128 v[200:203], v211 offset:32768\n\t" \
  "v_mfma_f32_16x16x32_bf16 v[132:135], v[204:207], v[56:59], v[132:135]\n\t" \
  "v_mfma_f32_16x16x32_bf16 v[140:143], v[204:207], v[60:63], v[140:143]\n\t" \
  "ds_read_b128 v[204:207], v211 offset:49152\n\t" \
  "v_cvt_pk_bf16_f32 v168, v160, v161\n\t" \
  "v_cvt_pk_bf16_f32 v169, v162, v163\n\t" \
  "v_cvt_pk_bf16_f32 v170, v164, v165\n\t" \
  "v_cvt_pk_bf16_f32 v171, v166, v167\n\t" \
  "buffer_store_dwordx4 v[168:171], v172, %[rt], s90 offen sc1\n\t" \
  "v_mfma_f32_16x16x32_bf16 v[128:131], v[176:179], v[64:67], v[128:131]\n\t" \
  "v_mfma_f32_16x16x32_bf16 v[136:139], v[176:179], v[68:71], v[136:139]\n\t" \
  "ds_read_b128 v[176:179], v208 offset:33024\n\t" \
  "s_add_u32 m0, %[lw], 100352\n\t" \
  "v_mfma_f32_16x16x32_bf16 v[132:135], v[180:183], v[64:67], v[132:135]\n\t" \
  "v_mfma_f32_16x16x32_bf16 v[140:143], v[180:183], v[68:71], v[140:143]\n\t" \
  "ds_read_b128 v[180:183], v208 offset:49408\n\t" \
  "global_load_lds_dwordx4 v218, s[92:93] sc1\n\t" \
  "s_waitcnt lgkmcnt(6)\n\t" \
  "v_mfma_f32_16x16x32_bf16 v[128:131], v[184:187], v[72:75], v[128:131]\n\t" \
  "v_mfma_f32_16x16x32_bf16 v[136:139], v[184:187], v[76:79], v[136:139]\n\t" \
  "ds_read_b128 v[184:187], v209 offset:33024\n\t" \
  "v_mfma_f32_16x16x32_bf16 v[132:135], v[188:191], v[72:75], v[132:135]\n\t" \
  "v_mfma_f32_16x16x32_bf16 v[140:143], v[188:191], v[76:79], v[140:143]\n\t" \
  "ds_read_b128 v[188:191], v209 offset:49408\n\t" \
  "s_waitcnt lgkmcnt(6)\n\t" \
  "v_mfma_f32_16x16x32_bf16 v[128:131], v[192:195], v[80:83], v[128:131]\n\t" \
  "v_mfma_f32_16x16x32_bf16 v[136:139], v[192:195], v[84:87], v[136:139]\n\t" \
  "ds_read_b128 v[192:195], v210 offset:33024\n\t" \
  "s_add_u32 m0, %[lw], 101376\n\t" \
  "v_mfma_f32_16x16x32_bf16 v[132:135], v[196:199], v[80:83], v[132:135]\n\t" \
  "v_mfma_f32_16x16x32_bf16 v[140:143], v[196:199], v[84:87], v[140:143]\n\t" \
  "ds_read_b128 v[196:199], v210 offset:49408\n\t" \
  "global_load_lds_dwordx4 v219, s[92:93] sc1\n\t" \
  "s_waitcnt lgkmcnt(6)\n\t" \
  "v_mfma_f32_16x16x32_bf16 v[128:131], v[200:203], v[88:91], v[128:131]\n\t" \
  "v_mfma_f32_16x16x32_bf16 v[136:139], v[200:203], v[92:95], v[136:139]\n\t" \
  "ds_read_b128 v[200:203], v211 offset:33024\n\t" \
  "v_mfma_f32_16x16x32_bf16 v[132:135], v[204:207], v[88:91], v[132:135]\n\t" \
  "v_mfma_f32_16x16x32_bf16 v[140:143], v[204:207], v[92:95], v[140:143]\n\t" \
  "ds_read_b128 v[204:207], v211 offset:49408\n\t" \
  "s_cmp_lt_u32 s86, 6\n\t" \
  "s_cbranch_scc1 .Lw10%=\n\t" \
  "s_waitcnt vmcnt(5)\n\t" \
  "s_barrier\n\t" \
  ".Lg10%=:\n\t" \
  "s_add_u32 s92, s92, 65024\n\t" \
  "s_addc_u32 s93, s93, 0\n\t" \
  "s_waitcnt lgkmcnt(6)\n\t" \
  "v_mfma_f32_16x16x32_bf16 v[128:131], v[176:179], v[96:99], v[128:131]\n\t" \
  "v_mfma_f32_16x16x32_bf16 v[136:139], v[176:179], v[100:103], v[136:139]\n\t" \
  "ds_read_b128 v[176:179], v212 offset:0\n\t" \
  "s_add_u32 m0, %[lw], 0\n\t" \
  "v_mfma_f32_16x16x32_bf16 v[132:135], v[180:183], v[96:99], v[132:135]\n\t" \
  "v_mfma_f32_16x16x32_bf16 v[140:143], v[180:183], v[100:103], v[140:143]\n\t" \
  "ds_read_b128 v[180:183], v212 offset:16384\n\t" \
  "global_load_lds_dwordx4 v216, s[92:93] sc1\n\t" \
  "s_waitcnt lgkmcnt(6)\n\t" \
  "v_mfma_f32_16x16x32_bf16 v[128:131], v[184:187], v[104:107], v[128:131]\n\t" \
  "v_mfma_f32_16x16x32_bf16 v[136:139], v[184:187], v[108:111], v[136:139]\n\t" \
  "ds_read_b128 v[184:187], v213 offset:0\n\t" \
  "v_mfma_f32_16x16x32_bf16 v[132:135], v[188:191], v[104:107], v[132:135]\n\t" \
  "v_mfma_f32_16x16x32_bf16 v[140:143], v[188:191], v[108:111], v[140:143]\n\t" \
  "ds_read_b128 v[188:191], v213 offset:16384\n\t" \
  "s_waitcnt lgkmcnt(6)\n\t" \
  "v_mfma_f32_16x16x32_bf16 v[128:131], v[192:195], v[112:115], v[128:131]\n\t" \
  "v_mfma_f32_16x16x32_bf16 v[136:139], v[192:195], v[116:119], v[136:139]\n\t" \
  "ds_read_b128 v[192:195], v214 offset:0\n\t" \
  "s_add_u32 m0, %[lw], 1024\n\t" \
  "v_mfma_f32_16x16x32_bf16 v[132:135], v[196:199], v[112:115], v[132:135]\n\t" \
  "v_mfma_f32_16x16x32_bf16 v[140:143], v[196:199], v[116:119], v[140:143]\n\t" \
  "ds_read_b128 v[196:199], v214 offset:16384\n\t" \
  "global_load_lds_dwordx4 v217, s[92:93] sc1\n\t" \
  "s_waitcnt lgkmcnt(6)\n\t" \
  "v_mfma_f32_16x16x32_bf16 v[128:131], v[200:203], v[120:123], v[128:131]\n\t" \
  "v_mfma_f32_16x16x32_bf16 v[136:139], v[200:203], v[124:127], v[136:139]\n\t" \
  "ds_read_b128 v[200:203], v215 offset:0\n\t" \
  "v_mfma_f32_16x16x32_bf16 v[132:135], v[204:207], v[120:123], v[132:135]\n\t" \
  "v_mfma_f32_16x16x32_bf16 v[140:143], v[204:207], v[124:127], v[140:143]\n\t" \
  "ds_read_b128 v[204:207], v215 offset:16384\n\t" \
  "s_add_u32 s86, s86, 1\n\t" \
  "s_add_u32 s87, s87, 1\n\t" \
  "s_cmp_eq_u32 s86, 1\n\t" \
  "s_cbranch_scc1 .Lf0%=\n\t" \
  ".Lfb0%=:\n\t" \
  "s_waitcnt lgkmcnt(6)\n\t" \
  "v_mfma_f32_16x16x32_bf16 v[144:147], v[176:179], v[0:3], 0\n\t" \
  "v_mfma_f32_16x16x32_bf16 v[152:155], v[176:179], v[4:7], 0\n\t" \
  "ds_read_b128 v[176:179], v212 offset:256\n\t" \
  "s_add_u32 m0, %[lw], 2048\n\t" \
  "v_mfma_f32_16x16x32_bf16 v[148:151], v[180:183], v[0:3], 0\n\t" \
  "v_mfma_f32_16x16x32_bf16 v[156:159], v[180:183], v[4:7], 0\n\t" \
  "ds_read_b128 v[180:183], v212 offset:16640\n\t" \
  "global_load_lds_dwordx4 v218, s[92:93] sc1\n\t" \
  "s_waitcnt lgkmcnt(6)\n\t" \
  "v_mfma_f32_16x16x32_bf16 v[144:147], v[184:187], v[8:11], v[144:147]\n\t" \
  "v_mfma_f32_16x16x32_bf16 v[152:155], v[184:187], v[12:15], v[152:155]\n\t" \
  "ds_read_b128 v[184:187], v213 offset:256\n\t" \
  "v_mfma_f32_16x16x32_bf16 v[148:151], v[188:191], v[8:11], v[148:151]\n\t" \
  "v_mfma_f32_16x16x32_bf16 v[156:159], v[188:191], v[12:15], v[156:159]\n\t" \
  "ds_read_b128 v[188:191], v213 offset:16640\n\t" \
  "ds_write_b128 v220, v[136:139] offset:0\n\t" \
  "ds_write_b128 v220, v[140:143] offset:1024\n\t" \
  "s_waitcnt lgkmcnt(8)\n\t" \
  "v_mfma_f32_16x16x32_bf16 v[144:147], v[192:195], v[16:19], v[144:147]\n\t" \
  "v_mfma_f32_16x16x32_bf16 v[152:155], v[192:195], v[20:23], v[152:155]\n\t" \
  "ds_read_b128 v[192:195], v214 offset:256\n\t" \
  "s_add_u32 m0, %[lw], 3072\n\t" \
  "v_mfma_f32_16x16x32_bf16 v[148:151], v[196:199], v[16:19], v[148:151]\n\t" \
  "v_mfma_f32_16x16x32_bf16 v[156:159], v[196:199], v[20:23], v[156:159]\n\t" \
  "ds_read_b128 v[196:199], v214 offset:16640\n\t" \
  "global_load_lds_dwordx4 v219, s[92:93] sc1\n\t" \
  "s_lshr_b32 s90, s87, 7\n\t" \
  "s_xor_b32 s90, s90, %[s0]\n\t" \
  "s_and_b32 s90, s90, 1\n\t" \
  "s_lshl1_add_u32 s90, s87, s90\n\t" \
  "s_lshl_b32 s90, s90, 13\n\t" \
  "s_cmp_lt_i32 s86, 3\n\t" \
  "s_cselect_b32 s90, 0x70000000, s90\n\t" \
  "s_waitcnt lgkmcnt(8)\n\t" \
  "v_mfma_f32_16x16x32_bf16 v[144:147], v[200:203], v[24:27], v[144:147]\n\t" \
  "v_mfma_f32_16x16x32_bf16 v[152:155], v[200:203], v[28:31], v[152:155]\n\t" \
  "ds_read_b128 v[200:203], v215 offset:256\n\t" \
  "v_mfma_f32_16x16x32_bf16 v[148:151], v[204:207], v[24:27], v[148:151]\n\t" \
  "v_mfma_f32_16x16x32_bf16 v[156:159], v[204:207], v[28:31], v[156:159]\n\t" \
  "ds_read_b128 v[204:207], v215 offset:16640\n\t" \
  "s_waitcnt lgkmcnt(4)\n\t" \
  "s_waitcnt vmcnt(4)\n\t" \
  "s_barrier\n\t" \
  ".Lg20%=:\n\t" \
  "s_add_u32 s92, s92, 512\n\t" \
  "s_addc_u32 s93, s93, 0\n\t" \
  "v_mfma_f32_16x16x32_bf16 v[144:147], v[176:179], v[32:35], v[144:147]\n\t" \
  "v_mfma_f32_16x16x32_bf16 v[152:155], v[176:179], v[36:39], v[152:155]\n\t" \
  "ds_read_b128 v[176:179], v212 offset:32768\n\t" \
  "s_add_u32 m0, %[lw], 32768\n\t" \
  "v_mfma_f32_16x16x32_bf16 v[148:151], v[180:183], v[32:35], v[148:151]\n\t" \
  "v_mfma_f32_16x16x32_bf16 v[156:159], v[180:183], v[36:39], v[156:159]\n\t" \
  "ds_read_b128 v[180:183], v212 offset:49152\n\t" \
  "global_load_lds_dwordx4 v216, s[92:93] sc1\n\t" \
  "ds_read_b128 v[160:163], v221 offset:0\n\t" \
  "ds_read_b128 v[164:167], v221 offset:1024\n\t" \
  "v_mfma_f32_16x16x32_bf16 v[144:147], v[184:187], v[40:43], v[144:147]\n\t" \
  "v_mfma_f32_16x16x32_bf16 v[152:155], v[184:187], v[44:47], v[152:155]\n\t" \
  "ds_read_b128 v[184:187], v213 offset:32768\n\t" \
  "v_mfma_f32_16x16x32_bf16 v[148:151], v[188:191], v[40:43], v[148:151]\n\t" \
  "v_mfma_f32_16x16x32_bf16 v[156:159], v[188:191], v[44:47], v[156:159]\n\t" \
  "ds_read_b128 v[188:191], v213 offset:49152\n\t" \
  "s_waitcnt lgkmcnt(8)\n\t" \
  "v_mfma_f32_16x16x32_bf16 v[144:147], v[192:195], v[48:51], v[144:147]\n\t" \
  "v_mfma_f32_16x16x32_bf16 v[152:155], v[192:195], v[52:55], v[152:155]\n\t" \
  "ds_read_b128 v[192:195], v214 offset:32768\n\t" \
  "s_add_u32 m0, %[lw], 33792\n\t" \
  "v_mfma_f32_16x16x32_bf16 v[148:151], v[196:199], v[48:51], v[148:151]\n\t" \
  "v_mfma_f32_16x16x32_bf16 v[156:159], v[196:199], v[52:55], v[156:159]\n\t" \
  "ds_read_b128 v[196:199], v214 offset:49152\n\t" \
  "global_load_lds_dwordx4 v217, s[92:93] sc1\n\t" \
  "s_waitcnt lgkmcnt(4)\n\t" \
  "v_add_f32 v160, v160, v128\n\t" \
  "v_add_f32 v161, v161, v129\n\t" \
  "v_add_f32 v162, v162, v130\n\t" \
  "v_add_f32 v163, v163, v131\n\t" \
  "v_add_f32 v164, v164, v132\n\t" \
  "v_add_f32 v165, v165, v133\n\t" \
  "v_add_f32 v166, v166, v134\n\t" \
  "v_add_f32 v167, v167, v135\n\t" \
  "v_mfma_f32_16x16x32_bf16 v[144:147], v[200:203], v[56:59], v[144:147]\n\t" \
  "v_mfma_f32_16x16x32_bf16 v[152:155], v[200:203], v[60:63], v[152:155]\n\t" \
  "ds_read_b128 v[200:203], v215 offset:32768\n\t" \
  "v_mfma_f32_16x16x32_bf16 v[148:151], v[204:207], v[56:59], v[148:151]\n\t" \
  "v_mfma_f32_16x16x32_bf16 v[156:159], v[204:207], v[60:63], v[156:159]\n\t" \
  "ds_read_b128 v[204:207], v215 offset:49152\n\t" \
  "v_cvt_pk_bf16_f32 v168, v160, v161\n\t" \
  "v_cvt_pk_bf16_f32 v169, v162, v163\n\t" \
  "v_cvt_pk_bf16_f32 v170, v164, v165\n\t" \
  "v_cvt_pk_bf16_f32 v171, v166, v167\n\t" \
  "buffer_store_dwordx4 v[168:171], v172, %[rt], s90 offen sc1\n\t" \
  "v_mfma_f32_16x16x32_bf16 v[144:147], v[176:179], v[64:67], v[144:147]\n\t" \
  "v_mfma_f32_16x16x32_bf16 v[152:155], v[176:179], v[68:71], v[152:155]\n\t" \
  "ds_read_b128 v[176:179], v212 offset:33024\n\t" \
  "s_add_u32 m0, %[lw], 34816\n\t" \
  "v_mfma_f32_16x16x32_bf16 v[148:151], v[180:183], v[64:67], v[148:151]\n\t" \
  "v_mfma_f32_16x16x32_bf16 v[156:159], v[180:183], v[68:71], v[156:159]\n\t" \
  "ds_read_b128 v[180:183], v212 offset:49408\n\t" \
  "global_load_lds_dwordx4 v218, s[92:93] sc1\n\t" \
  "s_waitcnt lgkmcnt(6)\n\t" \
  "v_mfma_f32_16x16x32_bf16 v[144:147], v[184:187], v[72:75], v[144:147]\n\t" \
  "v_mfma_f32_16x16x32_bf16 v[152:155], v[184:187], v[76:79], v[152:155]\n\t" \
  "ds_read_b128 v[184:187], v213 offset:33024\n\t" \
  "v_mfma_f32_16x16x32_bf16 v[148:151], v[188:191], v[72:75], v[148:151]\n\t" \
  "v_mfma_f32_16x16x32_bf16 v[156:159], v[188:191], v[76:79], v[156:159]\n\t" \
  "ds_read_b128 v[188:191], v213 offset:49408\n\t" \
  "s_waitcnt lgkmcnt(6)\n\t" \
  "v_mfma_f32_16x16x32_bf16 v[144:147], v[192:195], v[80:83], v[144:147]\n\t" \
  "v_mfma_f32_16x16x32_bf16 v[152:155], v[192:195], v[84:87], v[152:155]\n\t" \
  "ds_read_b128 v[192:195], v214 offset:33024\n\t" \
  "s_add_u32 m0, %[lw], 35840\n\t" \
  "v_mfma_f32_16x16x32_bf16 v[148:151], v[196:199], v[80:83], v[148:151]\n\t" \
  "v_mfma_f32_16x16x32_bf16 v[156:159], v[196:199], v[84:87], v[156:159]\n\t" \
  "ds_read_b128 v[196:199], v214 offset:49408\n\t" \
  "global_load_lds_dwordx4 v219, s[92:93] sc1\n\t" \
  "s_waitcnt lgkmcnt(6)\n\t" \
  "v_mfma_f32_16x16x32_bf16 v[144:147], v[200:203], v[88:91], v[144:147]\n\t" \
  "v_mfma_f32_16x16x32_bf16 v[152:155], v[200:203], v[92:95], v[152:155]\n\t" \
  "ds_read_b128 v[200:203], v215 offset:33024\n\t" \
  "v_mfma_f32_16x16x32_bf16 v[148:151], v[204:207], v[88:91], v[148:151]\n\t" \
  "v_mfma_f32_16x16x32_bf16 v[156:159], v[204:207], v[92:95], v[156:159]\n\t" \
  "ds_read_b128 v[204:207], v215 offset:49408\n\t" \
  "s_cmp_lt_u32 s86, 6\n\t" \
  "s_cbranch_scc1 .Lw30%=\n\t" \
  "s_waitcnt vmcnt(5)\n\t" \
  "s_barrier\n\t" \
  ".Lg30%=:\n\t" \
  "s_add_u32 s92, s92, 65024\n\t" \
  "s_addc_u32 s93, s93, 0\n\t" \
  "s_waitcnt lgkmcnt(6)\n\t" \
  "v_mfma_f32_16x16x32_bf16 v[144:147], v[176:179], v[96:99], v[144:147]\n\t" \
  "v_mfma_f32_16x16x32_bf16 v[152:155], v[176:179], v[100:103], v[152:155]\n\t" \
  "ds_read_b128 v[176:179], v208 offset:0\n\t" \
  "s_add_u32 m0, %[lw], 65536\n\t" \
  "v_mfma_f32_16x16x32_bf16 v[148:151], v[180:183], v[96:99], v[148:151]\n\t" \
  "v_mfma_f32_16x16x32_bf16 v[156:159], v[180:183], v[100:103], v[156:159]\n\t" \
  "ds_read_b128 v[180:183], v208 offset:16384\n\t" \
  "global_load_lds_dwordx4 v216, s[92:93] sc1\n\t" \
  "s_waitcnt lgkmcnt(6)\n\t" \
  "v_mfma_f32_16x16x32_bf16 v[144:147], v[184:187], v[104:107], v[144:147]\n\t" \
  "v_mfma_f32_16x16x32_bf16 v[152:155], v[184:187], v[108:111], v[152:155]\n\t" \
  "ds_read_b128 v[184:187], v209 offset:0\n\t" \
  "v_mfma_f32_16x16x32_bf16 v[148:151], v[188:191], v[104:107], v[148:151]\n\t" \
  "v_mfma_f32_16x16x32_bf16 v[156:159], v[188:191], v[108:111], v[156:159]\n\t" \
  "ds_read_b128 v[188:191], v209 offset:16384\n\t" \
  "s_waitcnt lgkmcnt(6)\n\t" \
  "v_mfma_f32_16x16x32_bf16 v[144:147], v[192:195], v[112:115], v[144:147]\n\t" \
  "v_mfma_f32_16x16x32_bf16 v[152:155], v[192:195], v[116:119], v[152:155]\n\t" \
  "ds_read_b128 v[192:195], v210 offset:0\n\t" \
  "s_add_u32 m0, %[lw], 66560\n\t" \
  "v_mfma_f32_16x16x32_bf16 v[148:151], v[196:199], v[112:115], v[148:151]\n\t" \
  "v_mfma_f32_16x16x32_bf16 v[156:159], v[196:199], v[116:119], v[156:159]\n\t" \
  "ds_read_b128 v[196:199], v210 offset:16384\n\t" \
  "global_load_lds_dwordx4 v217, s[92:93] sc1\n\t" \
  "s_waitcnt lgkmcnt(6)\n\t" \
  "v_mfma_f32_16x16x32_bf16 v[144:147], v[200:203], v[120:123], v[144:147]\n\t" \
  "v_mfma_f32_16x16x32_bf16 v[152:155], v[200:203], v[124:127], v[152:155]\n\t" \
  "ds_read_b128 v[200:203], v211 offset:0\n\t" \
  "v_mfma_f32_16x16x32_bf16 v[148:151], v[204:207], v[120:123], v[148:151]\n\t" \
  "v_mfma_f32_16x16x32_bf16 v[156:159], v[204:207], v[124:127], v[156:159]\n\t" \
  "ds_read_b128 v[204:207], v211 offset:16384\n\t" \
  "s_add_u32 s86, s86, 1\n\t" \
  "s_add_u32 s87, s87, 1\n\t" \
  "s_cmp_eq_u32 s86, 2\n\t" \
  "s_cbranch_scc1 .Ll0%=\n\t" \
  "s_cmp_lt_u32 s86, %[end]\n\t" \
  "s_cbranch_scc1 .Lp0%=\n\t" \
  "s_cmp_lg_u32 s86, 64\n\t" \
  "s_cbranch_scc1 .Lx0%=\n\t" \
  ".Ll0%=:\n\t" \
  "s_nop 7\n\t" \
  "ds_write_b128 v220, v[152:155] offset:0\n\t" \
  "ds_write_b128 v220, v[156:159] offset:1024\n\t" \
  "s_lshr_b32 s90, s87, 7\n\t" \
  "s_xor_b32 s90, s90, %[s0]\n\t" \
  "s_and_b32 s90, s90, 1\n\t" \
  "s_lshl1_add_u32 s90, s87, s90\n\t" \
  "s_lshl_b32 s90, s90, 13\n\t" \
  "s_cmp_lt_i32 s86, 1\n\t" \
  "s_cselect_b32 s90, 0x70000000, s90\n\t" \
  "s_waitcnt lgkmcnt(0)\n\t" \
  "s_barrier\n\t" \
  "ds_read_b128 v[160:163], v221 offset:0\n\t" \
  "ds_read_b128 v[164:167], v221 offset:1024\n\t" \
  "s_waitcnt lgkmcnt(0)\n\t" \
  "v_add_f32 v160, v160, v144\n\t" \
  "v_add_f32 v161, v161, v145\n\t" \
  "v_add_f32 v162, v162, v146\n\t" \
  "v_add_f32 v163, v163, v147\n\t" \
  "v_add_f32 v164, v164, v148\n\t" \
  "v_add_f32 v165, v165, v149\n\t" \
  "v_add_f32 v166, v166, v150\n\t" \
  "v_add_f32 v167, v167, v151\n\t" \
  "v_cvt_pk_bf16_f32 v168, v160, v161\n\t" \
  "v_cvt_pk_bf16_f32 v169, v162, v163\n\t" \
  "v_cvt_pk_bf16_f32 v170, v164, v165\n\t" \
  "v_cvt_pk_bf16_f32 v171, v166, v167\n\t" \
  "buffer_store_dwordx4 v[168:171], v172, %[rt], s90 offen sc1\n\t" \
  "s_waitcnt vmcnt(0)\n\t" \
  "s_barrier\n\t" \
  "s_cmp_eq_u32 s86, 2\n\t" \
  "s_cbranch_scc1 .Lp0%=\n\t" \
  "s_branch .Lx0%=\n\t" \
  ".Lf0%=:\n\t" \
  "s_nop 7\n\t" \
  "ds_write_b128 v220, v[136:139] offset:0\n\t" \
  "ds_write_b128 v220, v[140:143] offset:1024\n\t" \
  "s_lshr_b32 s90, s87, 7\n\t" \
  "s_xor_b32 s90, s90, %[s0]\n\t" \
  "s_and_b32 s90, s90, 1\n\t" \
  "s_lshl1_add_u32 s90, s87, s90\n\t" \
  "s_lshl_b32 s90, s90, 13\n\t" \
  "s_cmp_lt_i32 s86, 1\n\t" \
  "s_cselect_b32 s90, 0x70000000, s90\n\t" \
  "s_waitcnt lgkmcnt(0)\n\t" \
  "s_barrier\n\t" \
  "ds_read_b128 v[160:163], v221 offset:0\n\t" \
  "ds_read_b128 v[164:167], v221 offset:1024\n\t" \
  "s_waitcnt lgkmcnt(0)\n\t" \
  "v_add_f32 v160, v160, v128\n\t" \
  "v_add_f32 v161, v161, v129\n\t" \
  "v_add_f32 v162, v162, v130\n\t" \
  "v_add_f32 v163, v163, v131\n\t" \
  "v_add_f32 v164, v164, v132\n\t" \
  "v_add_f32 v165, v165, v133\n\t" \
  "v_add_f32 v166, v166, v134\n\t" \
  "v_add_f32 v167, v167, v135\n\t" \
  "v_cvt_pk_bf16_f32 v168, v160, v161\n\t" \
  "v_cvt_pk_bf16_f32 v169, v162, v163\n\t" \
  "v_cvt_pk_bf16_f32 v170, v164, v165\n\t" \
  "v_cvt_pk_bf16_f32 v171, v166, v167\n\t" \
  "buffer_store_dwordx4 v[168:171], v172, %[rt], s90 offen sc1\n\t" \
  "s_waitcnt vmcnt(0)\n\t" \
  "s_barrier\n\t" \
  "s_branch .Lfb0%=\n\t" \
  ".Lw10%=:\n\t" \
  "s_waitcnt vmcnt(2)\n\t" \
  "s_barrier\n\t" \
  "s_branch .Lg10%=\n\t" \
  ".Lw30%=:\n\t" \
  "s_waitcnt vmcnt(2)\n\t" \
  "s_barrier\n\t" \
  "s_branch .Lg30%=\n\t" \
  ".Lx0%=:\n\t" \
  "s_waitcnt lgkmcnt(0)\n\t" \
  "s_setprio 0\n\t" \
  "s_mov_b32 m0, s94\n\t" \
  "s_branch .Le%=\n\t" \
  ".Lk%=:\n\t" \
  "s_mov_b32 s94, m0\n\t" \
  "s_mov_b64 s[92:93], %[x0]\n\t" \
  "s_mov_b32 s86, %[g0]\n\t" \
  "s_add_u32 s87, s86, %[t0]\n\t" \
  "s_sub_i32 s88, s86, 3\n\t" \
  "ds_read_b128 v[176:179], v208 offset:0\n\t" \
  "ds_read_b128 v[180:183], v208 offset:16384\n\t" \
  "ds_read_b128 v[184:187], v209 offset:0\n\t" \
  "ds_read_b128 v[188:191], v209 offset:16384\n\t" \
  "ds_read_b128 v[192:195], v210 offset:0\n\t" \
  "ds_read_b128 v[196:199], v210 offset:16384\n\t" \
  "ds_read_b128 v[200:203], v211 offset:0\n\t" \
  "ds_read_b128 v[204:207], v211 offset:16384\n\t" \
  ".Lp1%=:\n\t" \
  "s_waitcnt lgkmcnt(6)\n\t" \
  "v_mfma_f32_16x16x32_bf16 v[128:131], v[176:179], v[0:3], 0\n\t" \
  "v_mfma_f32_16x16x32_bf16 v[136:139], v[176:179], v[4:7], 0\n\t" \
  "ds_read_b128 v[176:179], v208 offset:256\n\t" \
  "v_mfma_f32_16x16x32_bf16 v[132:135], v[180:183], v[0:3], 0\n\t" \
  "v_mfma_f32_16x16x32_bf16 v[140:143], v[180:183], v[4:7], 0\n\t" \
  "ds_read_b128 v[180:183], v208 offset:16640\n\t" \
  "s_waitcnt lgkmcnt(6)\n\t" \
  "v_mfma_f32_16x16x32_bf16 v[128:131], v[184:187], v[8:11], v[128:131]\n\t" \
  "v_mfma_f32_16x16x32_bf16 v[136:139], v[184:187], v[12:15], v[136:139]\n\t" \
  "ds_read_b128 v[184:187], v209 offset:256\n\t" \
  "s_add_u32 m0, %[lw], 67584\n\t" \
  "v_mfma_f32_16x16x32_bf16 v[132:135], v[188:191], v[8:11], v[132:135]\n\t" \
  "v_mfma_f32_16x16x32_bf16 v[140:143], v[188:191], v[12:15], v[140:143]\n\t" \
  "ds_read_b128 v[188:191], v209 offset:16640\n\t" \
  "global_load_lds_dwordx4 v218, s[92:93] sc1\n\t" \
  "ds_write_b128 v220, v[152:155] offset:0\n\t" \
  "ds_write_b128 v220, v[156:159] offset:1024\n\t" \
  "s_waitcnt lgkmcnt(8)\n\t" \
  "v_mfma_f32_16x16x32_bf16 v[128:131], v[192:195], v[16:19], v[128:131]\n\t" \
  "v_mfma_f32_16x16x32_bf16 v[136:139], v[192:195], v[20:23], v[136:139]\n\t" \
  "ds_read_b128 v[192:195], v210 offset:256\n\t" \
  "v_mfma_f32_16x16x32_bf16 v[132:135], v[196:199], v[16:19], v[132:135]\n\t" \
  "v_mfma_f32_16x16x32_bf16 v[140:143], v[196:199], v[20:23], v[140:143]\n\t" \
  "ds_read_b128 v[196:199], v210 offset:16640\n\t" \
  "s_lshr_b32 s90, s87, 7\n\t" \
  "s_xor_b32 s90, s90, %[s0]\n\t" \
  "s_and_b32 s90, s90, 1\n\t" \
  "s_lshl1_add_u32 s90, s87, s90\n\t" \
  "s_lshl_b32 s90, s90, 13\n\t" \
  "s_cmp_lt_i32 s86, 3\n\t" \
  "s_cselect_b32 s90, 0x70000000, s90\n\t" \
  "s_waitcnt lgkmcnt(8)\n\t" \
  "v_mfma_f32_16x16x32_bf16 v[128:131], v[200:203], v[24:27], v[128:131]\n\t" \
  "v_mfma_f32_16x16x32_bf16 v[136:139], v[200:203], v[28:31], v[136:139]\n\t" \
  "ds_read_b128 v[200:203], v211 offset:256\n\t" \
  "s_add_u32 m0, %[lw], 68608\n\t" \
  "v_mfma_f32_16x16x32_bf16 v[132:135], v[204:207], v[24:27], v[132:135]\n\t" \
  "v_mfma_f32_16x16x32_bf16 v[140:143], v[204:207], v[28:31], v[140:143]\n\t" \
  "ds_read_b128 v[204:207], v211 offset:16640\n\t" \
  "global_load_lds_dwordx4 v219, s[92:93] sc1\n\t" \
  "s_waitcnt lgkmcnt(4)\n\t" \
  "s_waitcnt vmcnt(4)\n\t" \
  "s_barrier\n\t" \
  ".Lg01%=:\n\t" \
  "s_add_u32 s92, s92, 512\n\t" \
  "s_addc_u32 s93, s93, 0\n\t" \
  "v_mfma_f32_16x16x32_bf16 v[128:131], v[176:179], v[32:35], v[128:131]\n\t" \
  "v_mfma_f32_16x16x32_bf16 v[136:139], v[176:179], v[36:39], v[136:139]\n\t" \
  "ds_read_b128 v[176:179], v208 offset:32768\n\t" \
  "v_mfma_f32_16x16x32_bf16 v[132:135], v[180:183], v[32:35], v[132:135]\n\t" \
  "v_mfma_f32_16x16x32_bf16 v[140:143], v[180:183], v[36:39], v[140:143]\n\t" \
  "ds_read_b128 v[180:183], v208 offset:49152\n\t" \
  "ds_read_b128 v[160:163], v221 offset:0\n\t" \
  "ds_read_b128 v[164:167], v221 offset:1024\n\t" \
  "s_sub_i32 s91, s86, 2\n\t" \
  "s_max_i32 s88, s88, s91\n\t" \
  "v_mov_b32 v173, s88\n\t" \
  "buffer_store_dword v173, v174, %[rs], 0 offen sc1\n\t" \
  "v_mfma_f32_16x16x32_bf16 v[128:131], v[184:187], v[40:43], v[128:131]\n\t" \
  "v_mfma_f32_16x16x32_bf16 v[136:139], v[184:187], v[44:47], v[136:139]\n\t" \
  "ds_read_b128 v[184:187], v209 offset:32768\n\t" \
  "s_add_u32 m0, %[lw], 98304\n\t" \
  "v_mfma_f32_16x16x32_bf16 v[132:135], v[188:191], v[40:43], v[132:135]\n\t" \
  "v_mfma_f32_16x16x32_bf16 v[140:143], v[188:191], v[44:47], v[140:143]\n\t" \
  "ds_read_b128 v[188:191], v209 offset:49152\n\t" \
  "global_load_lds_dwordx4 v216, s[92:93] sc1\n\t" \
  "s_waitcnt lgkmcnt(8)\n\t" \
  "v_mfma_f32_16x16x32_bf16 v[128:131], v[192:195], v[48:51], v[128:131]\n\t" \
  "v_mfma_f32_16x16x32_bf16 v[136:139], v[192:195], v[52:55], v[136:139]\n\t" \
  "ds_read_b128 v[192:195], v210 offset:32768\n\t" \
  "v_mfma_f32_16x16x32_bf16 v[132:135], v[196:199], v[48:51], v[132:135]\n\t" \
  "v_mfma_f32_16x16x32_bf16 v[140:143], v[196:199], v[52:55], v[140:143]\n\t" \
  "ds_read_b128 v[196:199], v210 offset:49152\n\t" \
  "s_waitcnt lgkmcnt(4)\n\t" \
  "v_add_f32 v160, v160, v144\n\t" \
  "v_add_f32 v161, v161, v145\n\t" \
  "v_add_f32 v162, v162, v146\n\t" \
  "v_add_f32 v163, v163, v147\n\t" \
  "v_add_f32 v164, v164, v148\n\t" \
  "v_add_f32 v165, v165, v149\n\t" \
  "v_add_f32 v166, v166, v150\n\t" \
  "v_add_f32 v167, v167, v151\n\t" \
  "v_mfma_f32_16x16x32_bf16 v[128:131], v[200:203], v[56:59], v[128:131]\n\t" \
  "v_mfma_f32_16x16x32_bf16 v[136:139], v[200:203], v[60:63], v[136:139]\n\t" \
  "ds_read_b128 v[200:203], v211 offset:32768\n\t" \
  "s_add_u32 m0, %[lw], 99328\n\t" \
  "v_mfma_f32_16x16x32_bf16 v[132:135], v[204:207], v[56:59], v[132:135]\n\t" \
  "v_mfma_f32_16x16x32_bf16 v[140:143], v[204:207], v[60:63], v[140:143]\n\t" \
  "ds_read_b128 v[204:207], v211 offset:49152\n\t" \
  "global_load_lds_dwordx4 v217, s[92:93] sc1\n\t" \
  "v_cvt_pk_bf16_f32 v168, v160, v161\n\t" \
  "v_cvt_pk_bf16_f32 v169, v162, v163\n\t" \
  "v_cvt_pk_bf16_f32 v170, v164, v165\n\t" \
  "v_cvt_pk_bf16_f32 v171, v166, v167\n\t" \
  "buffer_store_dwordx4 v[168:171], v172, %[rt], s90 offen sc1\n\t" \
  "v_mfma_f32_16x16x32_bf16 v[128:131], v[176:179], v[64:67], v[128:131]\n\t" \
  "v_mfma_f32_16x16x32_bf16 v[136:139], v[176:179], v[68:71], v[136:139]\n\t" \
  "ds_read_b128 v[176:179], v208 offset:33024\n\t" \
  "v_mfma_f32_16x16x32_bf16 v[132:135], v[180:183], v[64:67], v[132:135]\n\t" \
  "v_mfma_f32_16x16x32_bf16 v[140:143], v[180:183], v[68:71], v[140:143]\n\t" \
  "ds_read_b128 v[180:183], v208 offset:49408\n\t" \
  "s_waitcnt lgkmcnt(6)\n\t" \
  "v_mfma_f32_16x16x32_bf16 v[128:131], v[184:187], v[72:75], v[128:131]\n\t" \
  "v_mfma_f32_16x16x32_bf16 v[136:139], v[184:187], v[76:79], v[136:139]\n\t" \
  "ds_read_b128 v[184:187], v209 offset:33024\n\t" \
  "s_add_u32 m0, %[lw], 100352\n\t" \
  "v_mfma_f32_16x16x32_bf16 v[132:135], v[188:191], v[72:75], v[132:135]\n\t" \
  "v_mfma_f32_16x16x32_bf16 v[140:143], v[188:191], v[76:79], v[140:143]\n\t" \
  "ds_read_b128 v[188:191], v209 offset:49408\n\t" \
  "global_load_lds_dwordx4 v218, s[92:93] sc1\n\t" \
  "s_waitcnt lgkmcnt(6)\n\t" \
  "v_mfma_f32_16x16x32_bf16 v[128:131], v[192:195], v[80:83], v[128:131]\n\t" \
  "v_mfma_f32_16x16x32_bf16 v[136:139], v[192:195], v[84:87], v[136:139]\n\t" \
  "ds_read_b128 v[192:195], v210 offset:33024\n\t" \
  "v_mfma_f32_16x16x32_bf16 v[132:135], v[196:199], v[80:83], v[132:135]\n\t" \
  "v_mfma_f32_16x16x32_bf16 v[140:143], v[196:199], v[84:87], v[140:143]\n\t" \
  "ds_read_b128 v[196:199], v210 offset:49408\n\t" \
  "s_waitcnt lgkmcnt(6)\n\t" \
  "v_mfma_f32_16x16x32_bf16 v[128:131], v[200:203], v[88:91], v[128:131]\n\t" \
  "v_mfma_f32_16x16x32_bf16 v[136:139], v[200:203], v[92:95], v[136:139]\n\t" \
  "ds_read_b128 v[200:203], v211 offset:33024\n\t" \
  "s_add_u32 m0, %[lw], 101376\n\t" \
  "v_mfma_f32_16x16x32_bf16 v[132:135], v[204:207], v[88:91], v[132:135]\n\t" \
  "v_mfma_f32_16x16x32_bf16 v[140:143], v[204:207], v[92:95], v[140:143]\n\t" \
  "ds_read_b128 v[204:207], v211 offset:49408\n\t" \
  "global_load_lds_dwordx4 v219, s[92:93] sc1\n\t" \
  "s_cmp_lt_u32 s86, 6\n\t" \
  "s_cbranch_scc1 .Lw11%=\n\t" \
  "s_waitcnt vmcnt(6)\n\t" \
  "s_barrier\n\t" \
  ".Lg11%=:\n\t" \
  "s_add_u32 s92, s92, 65024\n\t" \
  "s_addc_u32 s93, s93, 0\n\t" \
  "s_waitcnt lgkmcnt(6)\n\t" \
  "v_mfma_f32_16x16x32_bf16 v[128:131], v[176:179], v[96:99], v[128:131]\n\t" \
  "v_mfma_f32_16x16x32_bf16 v[136:139], v[176:179], v[100:103], v[136:139]\n\t" \
  "ds_read_b128 v[176:179], v212 offset:0\n\t" \
  "v_mfma_f32_16x16x32_bf16 v[132:135], v[180:183], v[96:99], v[132:135]\n\t" \
  "v_mfma_f32_16x16x32_bf16 v[140:143], v[180:183], v[100:103], v[140:143]\n\t" \
  "ds_read_b128 v[180:183], v212 offset:16384\n\t" \
  "s_waitcnt lgkmcnt(6)\n\t" \
  "v_mfma_f32_16x16x32_bf16 v[128:131], v[184:187], v[104:107], v[128:131]\n\t" \
  "v_mfma_f32_16x16x32_bf16 v[136:139], v[184:187], v[108:111], v[136:139]\n\t" \
  "ds_read_b128 v[184:187], v213 offset:0\n\t" \
  "s_add_u32 m0, %[lw], 0\n\t" \
  "v_mfma_f32_16x16x32_bf16 v[132:135], v[188:191], v[104:107], v[132:135]\n\t" \
  "v_mfma_f32_16x16x32_bf16 v[140:143], v[188:191], v[108:111], v[140:143]\n\t" \
  "ds_read_b128 v[188:191], v213 offset:16384\n\t" \
  "global_load_lds_dwordx4 v216, s[92:93] sc1\n\t" \
  "s_waitcnt lgkmcnt(6)\n\t" \
  "v_mfma_f32_16x16x32_bf16 v[128:131], v[192:195], v[112:115], v[128:131]\n\t" \
  "v_mfma_f32_16x16x32_bf16 v[136:139], v[192:195], v[116:119], v[136:139]\n\t" \
  "ds_read_b128 v[192:195], v214 offset:0\n\t" \
  "v_mfma_f32_16x16x32_bf16 v[132:135], v[196:199], v[112:115], v[132:135]\n\t" \
  "v_mfma_f32_16x16x32_bf16 v[140:143], v[196:199], v[116:119], v[140:143]\n\t" \
  "ds_read_b128 v[196:199], v214 offset:16384\n\t" \
  "s_waitcnt lgkmcnt(6)\n\t" \
  "v_mfma_f32_16x16x32_bf16 v[128:131], v[200:203], v[120:123], v[128:131]\n\t" \
  "v_mfma_f32_16x16x32_bf16 v[136:139], v[200:203], v[124:127], v[136:139]\n\t" \
  "ds_read_b128 v[200:203], v215 offset:0\n\t" \
  "s_add_u32 m0, %[lw], 1024\n\t" \
  "v_mfma_f32_16x16x32_bf16 v[132:135], v[204:207], v[120:123], v[132:135]\n\t" \
  "v_mfma_f32_16x16x32_bf16 v[140:143], v[204:207], v[124:127], v[140:143]\n\t" \
  "ds_read_b128 v[204:207], v215 offset:16384\n\t" \
  "global_load_lds_dwordx4 v217, s[92:93] sc1\n\t" \
  "s_add_u32 s86, s86, 1\n\t" \
  "s_add_u32 s87, s87, 1\n\t" \
  "s_cmp_eq_u32 s86, 1\n\t" \
  "s_cbranch_scc1 .Lf1%=\n\t" \
  ".Lfb1%=:\n\t" \
  "s_waitcnt lgkmcnt(6)\n\t" \
  "v_mfma_f32_16x16x32_bf16 v[144:147], v[176:179], v[0:3], 0\n\t" \
  "v_mfma_f32_16x16x32_bf16 v[152:155], v[176:179], v[4:7], 0\n\t" \
  "ds_read_b128 v[176:179], v212 offset:256\n\t" \
  "v_mfma_f32_16x16x32_bf16 v[148:151], v[180:183], v[0:3], 0\n\t" \
  "v_mfma_f32_16x16x32_bf16 v[156:159], v[180:183], v[4:7], 0\n\t" \
  "ds_read_b128 v[180:183], v212 offset:16640\n\t" \
  "s_waitcnt lgkmcnt(6)\n\t" \
  "v_mfma_f32_16x16x32_bf16 v[144:147], v[184:187], v[8:11], v[144:147]\n\t" \
  "v_mfma_f32_16x16x32_bf16 v[152:155], v[184:187], v[12:15], v[152:155]\n\t" \
  "ds_read_b128 v[184:187], v213 offset:256\n\t" \
  "s_add_u32 m0, %[lw], 2048\n\t" \
  "v_mfma_f32_16x16x32_bf16 v[148:151], v[188:191], v[8:11], v[148:151]\n\t" \
  "v_mfma_f32_16x16x32_bf16 v[156:159], v[188:191], v[12:15], v[156:159]\n\t" \
  "ds_read_b128 v[188:191], v213 offset:16640\n\t" \
  "global_load_lds_dwordx4 v218, s[92:93] sc1\n\t" \
  "ds_write_b128 v220, v[136:139] offset:0\n\t" \
  "ds_write_b128 v220, v[140:143] offset:1024\n\t" \
  "s_waitcnt lgkmcnt(8)\n\t" \
  "v_mfma_f32_16x16x32_bf16 v[144:147], v[192:195], v[16:19], v[144:147]\n\t" \
  "v_mfma_f32_16x16x32_bf16 v[152:155], v[192:195], v[20:23], v[152:155]\n\t" \
  "ds_read_b128 v[192:195], v214 offset:256\n\t" \
  "v_mfma_f32_16x16x32_bf16 v[148:151], v[196:199], v[16:19], v[148:151]\n\t" \
  "v_mfma_f32_16x16x32_bf16 v[156:159], v[196:199], v[20:23], v[156:159]\n\t" \
  "ds_read_b128 v[196:199], v214 offset:16640\n\t" \
  "s_lshr_b32 s90, s87, 7\n\t" \
  "s_xor_b32 s90, s90, %[s0]\n\t" \
  "s_and_b32 s90, s90, 1\n\t" \
  "s_lshl1_add_u32 s90, s87, s90\n\t" \
  "s_lshl_b32 s90, s90, 13\n\t" \
  "s_cmp_lt_i32 s86, 3\n\t" \
  "s_cselect_b32 s90, 0x70000000, s90\n\t" \
  "s_waitcnt lgkmcnt(8)\n\t" \
  "v_mfma_f32_16x16x32_bf16 v[144:147], v[200:203], v[24:27], v[144:147]\n\t" \
  "v_mfma_f32_16x16x32_bf16 v[152:155], v[200:203], v[28:31], v[152:155]\n\t" \
  "ds_read_b128 v[200:203], v215 offset:256\n\t" \
  "s_add_u32 m0, %[lw], 3072\n\t" \
  "v_mfma_f32_16x16x32_bf16 v[148:151], v[204:207], v[24:27], v[148:151]\n\t" \
  "v_mfma_f32_16x16x32_bf16 v[156:159], v[204:207], v[28:31], v[156:159]\n\t" \
  "ds_read_b128 v[204:207], v215 offset:16640\n\t" \
  "global_load_lds_dwordx4 v219, s[92:93] sc1\n\t" \
  "s_waitcnt lgkmcnt(4)\n\t" \
  "s_waitcnt vmcnt(4)\n\t" \
  "s_barrier\n\t" \
  ".Lg21%=:\n\t" \
  "s_add_u32 s92, s92, 512\n\t" \
  "s_addc_u32 s93, s93, 0\n\t" \
  "v_mfma_f32_16x16x32_bf16 v[144:147], v[176:179], v[32:35], v[144:147]\n\t" \
  "v_mfma_f32_16x16x32_bf16 v[152:155], v[176:179], v[36:39], v[152:155]\n\t" \
  "ds_read_b128 v[176:179], v212 offset:32768\n\t" \
  "v_mfma_f32_16x16x32_bf16 v[148:151], v[180:183], v[32:35], v[148:151]\n\t" \
  "v_mfma_f32_16x16x32_bf16 v[156:159], v[180:183], v[36:39], v[156:159]\n\t" \
  "ds_read_b128 v[180:183], v212 offset:49152\n\t" \
  "ds_read_b128 v[160:163], v221 offset:0\n\t" \
  "ds_read_b128 v[164:167], v221 offset:1024\n\t" \
  "s_sub_i32 s91, s86, 2\n\t" \
  "s_max_i32 s88, s88, s91\n\t" \
  "v_mov_b32 v173, s88\n\t" \
  "buffer_store_dword v173, v174, %[rs], 0 offen sc1\n\t" \
  "v_mfma_f32_16x16x32_bf16 v[144:147], v[184:187], v[40:43], v[144:147]\n\t" \
  "v_mfma_f32_16x16x32_bf16 v[152:155], v[184:187], v[44:47], v[152:155]\n\t" \
  "ds_read_b128 v[184:187], v213 offset:32768\n\t" \
  "s_add_u32 m0, %[lw], 32768\n\t" \
  "v_mfma_f32_16x16x32_bf16 v[148:151], v[188:191], v[40:43], v[148:151]\n\t" \
  "v_mfma_f32_16x16x32_bf16 v[156:159], v[188:191], v[44:47], v[156:159]\n\t" \
  "ds_read_b128 v[188:191], v213 offset:49152\n\t" \
  "global_load_lds_dwordx4 v216, s[92:93] sc1\n\t" \
  "s_waitcnt lgkmcnt(8)\n\t" \
  "v_mfma_f32_16x16x32_bf16 v[144:147], v[192:195], v[48:51], v[144:147]\n\t" \
  "v_mfma_f32_16x16x32_bf16 v[152:155], v[192:195], v[52:55], v[152:155]\n\t" \
  "ds_read_b128 v[192:195], v214 offset:32768\n\t" \
  "v_mfma_f32_16x16x32_bf16 v[148:151], v[196:199], v[48:51], v[148:151]\n\t" \
  "v_mfma_f32_16x16x32_bf16 v[156:159], v[196:199], v[52:55], v[156:159]\n\t" \
  "ds_read_b128 v[196:199], v214 offset:49152\n\t" \
  "s_waitcnt lgkmcnt(4)\n\t" \
  "v_add_f32 v160, v160, v128\n\t" \
  "v_add_f32 v161, v161, v129\n\t" \
  "v_add_f32 v162, v162, v130\n\t" \
  "v_add_f32 v163, v163, v131\n\t" \
  "v_add_f32 v164, v164, v132\n\t" \
  "v_add_f32 v165, v165, v133\n\t" \
  "v_add_f32 v166, v166, v134\n\t" \
  "v_add_f32 v167, v167, v135\n\t" \
  "v_mfma_f32_16x16x32_bf16 v[144:147], v[200:203], v[56:59], v[144:147]\n\t" \
  "v_mfma_f32_16x16x32_bf16 v[152:155], v[200:203], v[60:63], v[152:155]\n\t" \
  "ds_read_b128 v[200:203], v215 offset:32768\n\t" \
  "s_add_u32 m0, %[lw], 33792\n\t" \
  "v_mfma_f32_16x16x32_bf16 v[148:151], v[204:207], v[56:59], v[148:151]\n\t" \
  "v_mfma_f32_16x16x32_bf16 v[156:159], v[204:207], v[60:63], v[156:159]\n\t" \
  "ds_read_b128 v[204:207], v215 offset:49152\n\t" \
  "global_load_lds_dwordx4 v217, s[92:93] sc1\n\t" \
  "v_cvt_pk_bf16_f32 v168, v160, v161\n\t" \
  "v_cvt_pk_bf16_f32 v169, v162, v163\n\t" \
  "v_cvt_pk_bf16_f32 v170, v164, v165\n\t" \
  "v_cvt_pk_bf16_f32 v171, v166, v167\n\t" \
  "buffer_store_dwordx4 v[168:171], v172, %[rt], s90 offen sc1\n\t" \
  "v_mfma_f32_16x16x32_bf16 v[144:147], v[176:179], v[64:67], v[144:147]\n\t" \
  "v_mfma_f32_16x16x32_bf16 v[152:155], v[176:179], v[68:71], v[152:155]\n\t" \
  "ds_read_b128 v[176:179], v212 offset:33024\n\t" \
  "v_mfma_f32_16x16x32_bf16 v[148:151], v[180:183], v[64:67], v[148:151]\n\t" \
  "v_mfma_f32_16x16x32_bf16 v[156:159], v[180:183], v[68:71], v[156:159]\n\t" \
  "ds_read_b128 v[180:183], v212 offset:49408\n\t" \
  "s_waitcnt lgkmcnt(6)\n\t" \
  "v_mfma_f32_16x16x32_bf16 v[144:147], v[184:187], v[72:75], v[144:147]\n\t" \
  "v_mfma_f32_16x16x32_bf16 v[152:155], v[184:187], v[76:79], v[152:155]\n\t" \
  "ds_read_b128 v[184:187], v213 offset:33024\n\t" \
  "s_add_u32 m0, %[lw], 34816\n\t" \
  "v_mfma_f32_16x16x32_bf16 v[148:151], v[188:191], v[72:75], v[148:151]\n\t" \
  "v_mfma_f32_16x16x32_bf16 v[156:159], v[188:191], v[76:79], v[156:159]\n\t" \
  "ds_read_b128 v[188:191], v213 offset:49408\n\t" \
  "global_load_lds_dwordx4 v218, s[92:93] sc1\n\t" \
  "s_waitcnt lgkmcnt(6)\n\t" \
  "v_mfma_f32_16x16x32_bf16 v[144:147], v[192:195], v[80:83], v[144:147]\n\t" \
  "v_mfma_f32_16x16x32_bf16 v[152:155], v[192:195], v[84:87], v[152:155]\n\t" \
  "ds_read_b128 v[192:195], v214 offset:33024\n\t" \
  "v_mfma_f32_16x16x32_bf16 v[148:151], v[196:199], v[80:83], v[148:151]\n\t" \
  "v_mfma_f32_16x16x32_bf16 v[156:159], v[196:199], v[84:87], v[156:159]\n\t" \
  "ds_read_b128 v[196:199], v214 offset:49408\n\t" \
  "s_waitcnt lgkmcnt(6)\n\t" \
  "v_mfma_f32_16x16x32_bf16 v[144:147], v[200:203], v[88:91], v[144:147]\n\t" \
  "v_mfma_f32_16x16x32_bf16 v[152:155], v[200:203], v[92:95], v[152:155]\n\t" \
  "ds_read_b128 v[200:203], v215 offset:33024\n\t" \
  "s_add_u32 m0, %[lw], 35840\n\t" \
  "v_mfma_f32_16x16x32_bf16 v[148:151], v[204:207], v[88:91], v[148:151]\n\t" \
  "v_mfma_f32_16x16x32_bf16 v[156:159], v[204:207], v[92:95], v[156:159]\n\t" \
  "ds_read_b128 v[204:207], v215 offset:49408\n\t" \
  "global_load_lds_dwordx4 v219, s[92:93] sc1\n\t" \
  "s_cmp_lt_u32 s86, 6\n\t" \
  "s_cbranch_scc1 .Lw31%=\n\t" \
  "s_waitcnt vmcnt(6)\n\t" \
  "s_barrier\n\t" \
  ".Lg31%=:\n\t" \
  "s_add_u32 s92, s92, 65024\n\t" \
  "s_addc_u32 s93, s93, 0\n\t" \
  "s_waitcnt lgkmcnt(6)\n\t" \
  "v_mfma_f32_16x16x32_bf16 v[144:147], v[176:179], v[96:99], v[144:147]\n\t" \
  "v_mfma_f32_16x16x32_bf16 v[152:155], v[176:179], v[100:103], v[152:155]\n\t" \
  "ds_read_b128 v[176:179], v208 offset:0\n\t" \
  "v_mfma_f32_16x16x32_bf16 v[148:151], v[180:183], v[96:99], v[148:151]\n\t" \
  "v_mfma_f32_16x16x32_bf16 v[156:159], v[180:183], v[100:103], v[156:159]\n\t" \
  "ds_read_b128 v[180:183], v208 offset:16384\n\t" \
  "s_waitcnt lgkmcnt(6)\n\t" \
  "v_mfma_f32_16x16x32_bf16 v[144:147], v[184:187], v[104:107], v[144:147]\n\t" \
  "v_mfma_f32_16x16x32_bf16 v[152:155], v[184:187], v[108:111], v[152:155]\n\t" \
  "ds_read_b128 v[184:187], v209 offset:0\n\t" \
  "s_add_u32 m0, %[lw], 65536\n\t" \
  "v_mfma_f32_16x16x32_bf16 v[148:151], v[188:191], v[104:107], v[148:151]\n\t" \
  "v_mfma_f32_16x16x32_bf16 v[156:159], v[188:191], v[108:111], v[156:159]\n\t" \
  "ds_read_b128 v[188:191], v209 offset:16384\n\t" \
  "global_load_lds_dwordx4 v216, s[92:93] sc1\n\t" \
  "s_waitcnt lgkmcnt(6)\n\t" \
  "v_mfma_f32_16x16x32_bf16 v[144:147], v[192:195], v[112:115], v[144:147]\n\t" \
  "v_mfma_f32_16x16x32_bf16 v[152:155], v[192:195], v[116:119], v[152:155]\n\t" \
  "ds_read_b128 v[192:195], v210 offset:0\n\t" \
  "v_mfma_f32_16x16x32_bf16 v[148:151], v[196:199], v[112:115], v[148:151]\n\t" \
  "v_mfma_f32_16x16x32_bf16 v[156:159], v[196:199], v[116:119], v[156:159]\n\t" \
  "ds_read_b128 v[196:199], v210 offset:16384\n\t" \
  "s_waitcnt lgkmcnt(6)\n\t" \
  "v_mfma_f32_16x16x32_bf16 v[144:147], v[200:203], v[120:123], v[144:147]\n\t" \
  "v_mfma_f32_16x16x32_bf16 v[152:155], v[200:203], v[124:127], v[152:155]\n\t" \
  "ds_read_b128 v[200:203], v211 offset:0\n\t" \
  "s_add_u32 m0, %[lw], 66560\n\t" \
  "v_mfma_f32_16x16x32_bf16 v[148:151], v[204:207], v[120:123], v[148:151]\n\t" \
  "v_mfma_f32_16x16x32_bf16 v[156:159], v[204:207], v[124:127], v[156:159]\n\t" \
  "ds_read_b128 v[204:207], v211 offset:16384\n\t" \
  "global_load_lds_dwordx4 v217, s[92:93] sc1\n\t" \
  "s_add_u32 s86, s86, 1\n\t" \
  "s_add_u32 s87, s87, 1\n\t" \
  "s_cmp_eq_u32 s86, 2\n\t" \
  "s_cbranch_scc1 .Ll1%=\n\t" \
  "s_cmp_lt_u32 s86, %[end]\n\t" \
  "s_cbranch_scc1 .Lp1%=\n\t" \
  "s_cmp_lg_u32 s86, 64\n\t" \
  "s_cbranch_scc1 .Lx1%=\n\t" \
  ".Ll1%=:\n\t" \
  "s_nop 7\n\t" \
  "ds_write_b128 v220, v[152:155] offset:0\n\t" \
  "ds_write_b128 v220, v[156:159] offset:1024\n\t" \
  "s_lshr_b32 s90, s87, 7\n\t" \
  "s_xor_b32 s90, s90, %[s0]\n\t" \
  "s_and_b32 s90, s90, 1\n\t" \
  "s_lshl1_add_u32 s90, s87, s90\n\t" \
  "s_lshl_b32 s90, s90, 13\n\t" \
  "s_cmp_lt_i32 s86, 1\n\t" \
  "s_cselect_b32 s90, 0x70000000, s90\n\t" \
  "s_waitcnt lgkmcnt(0)\n\t" \
  "s_barrier\n\t" \
  "ds_read_b128 v[160:163], v221 offset:0\n\t" \
  "ds_read_b128 v[164:167], v221 offset:1024\n\t" \
  "s_waitcnt lgkmcnt(0)\n\t" \
  "v_add_f32 v160, v160, v144\n\t" \
  "v_add_f32 v161, v161, v145\n\t" \
  "v_add_f32 v162, v162, v146\n\t" \
  "v_add_f32 v163, v163, v147\n\t" \
  "v_add_f32 v164, v164, v148\n\t" \
  "v_add_f32 v165, v165, v149\n\t" \
  "v_add_f32 v166, v166, v150\n\t" \
  "v_add_f32 v167, v167, v151\n\t" \
  "v_cvt_pk_bf16_f32 v168, v160, v161\n\t" \
  "v_cvt_pk_bf16_f32 v169, v162, v163\n\t" \
  "v_cvt_pk_bf16_f32 v170, v164, v165\n\t" \
  "v_cvt_pk_bf16_f32 v171, v166, v167\n\t" \
  "buffer_store_dwordx4 v[168:171], v172, %[rt], s90 offen sc1\n\t" \
  "s_waitcnt vmcnt(0)\n\t" \
  "s_barrier\n\t" \
  "s_sub_i32 s91, s86, 1\n\t" \
  "s_max_i32 s88, s88, s91\n\t" \
  "v_mov_b32 v173, s88\n\t" \
  "buffer_store_dword v173, v174, %[rs], 0 offen sc1\n\t" \
  "s_cmp_eq_u32 s86, 2\n\t" \
  "s_cbranch_scc1 .Lp1%=\n\t" \
  "s_branch .Lx1%=\n\t" \
  ".Lf1%=:\n\t" \
  "s_nop 7\n\t" \
  "ds_write_b128 v220, v[136:139] offset:0\n\t" \
  "ds_write_b128 v220, v[140:143] offset:1024\n\t" \
  "s_lshr_b32 s90, s87, 7\n\t" \
  "s_xor_b32 s90, s90, %[s0]\n\t" \
  "s_and_b32 s90, s90, 1\n\t" \
  "s_lshl1_add_u32 s90, s87, s90\n\t" \
  "s_lshl_b32 s90, s90, 13\n\t" \
  "s_cmp_lt_i32 s86, 1\n\t" \
  "s_cselect_b32 s90, 0x70000000, s90\n\t" \
  "s_waitcnt lgkmcnt(0)\n\t" \
  "s_barrier\n\t" \
  "ds_read_b128 v[160:163], v221 offset:0\n\t" \
  "ds_read_b128 v[164:167], v221 offset:1024\n\t" \
  "s_waitcnt lgkmcnt(0)\n\t" \
  "v_add_f32 v160, v160, v128\n\t" \
  "v_add_f32 v161, v161, v129\n\t" \
  "v_add_f32 v162, v162, v130\n\t" \
  "v_add_f32 v163, v163, v131\n\t" \
  "v_add_f32 v164, v164, v132\n\t" \
  "v_add_f32 v165, v165, v133\n\t" \
  "v_add_f32 v166, v166, v134\n\t" \
  "v_add_f32 v167, v167, v135\n\t" \
  "v_cvt_pk_bf16_f32 v168, v160, v161\n\t" \
  "v_cvt_pk_bf16_f32 v169, v162, v163\n\t" \
  "v_cvt_pk_bf16_f32 v170, v164, v165\n\t" \
  "v_cvt_pk_bf16_f32 v171, v166, v167\n\t" \
  "buffer_store_dwordx4 v[168:171], v172, %[rt], s90 offen sc1\n\t" \
  "s_waitcnt vmcnt(0)\n\t" \
  "s_barrier\n\t" \
  "s_sub_i32 s91, s86, 1\n\t" \
  "s_max_i32 s88, s88, s91\n\t" \
  "v_mov_b32 v173, s88\n\t" \
  "buffer_store_dword v173, v174, %[rs], 0 offen sc1\n\t" \
  "s_branch .Lfb1%=\n\t" \
  ".Lw11%=:\n\t" \
  "s_waitcnt vmcnt(2)\n\t" \
  "s_barrier\n\t" \
  "s_sub_i32 s91, s86, 1\n\t" \
  "s_max_i32 s88, s88, s91\n\t" \
  "v_mov_b32 v173, s88\n\t" \
  "buffer_store_dword v173, v174, %[rs], 0 offen sc1\n\t" \
  "s_branch .Lg11%=\n\t" \
  ".Lw31%=:\n\t" \
  "s_waitcnt vmcnt(2)\n\t" \
  "s_barrier\n\t" \
  "s_sub_i32 s91, s86, 1\n\t" \
  "s_max_i32 s88, s88, s91\n\t" \
  "v_mov_b32 v173, s88\n\t" \
  "buffer_store_dword v173, v174, %[rs], 0 offen sc1\n\t" \
  "s_branch .Lg31%=\n\t" \
  ".Lx1%=:\n\t" \
  "s_waitcnt lgkmcnt(0)\n\t" \
  "s_setprio 0\n\t" \
  "s_mov_b32 m0, s94\n\t" \
  ".Le%=:\n\t"
constexpr int W_X = 131072, W_PM = W_X + 16384;
template <bool ISV>
__device__ __forceinline__ void job_proj(Args& p, int g, int which, int h, char* smem, __amdgpu_buffer_rsrc_t rsy) {
  const int tid = threadIdx.x, lane = tid & 63, r = lane & 15, q = lane >> 4;
  const int w = __builtin_amdgcn_readfirstlane(tid >> 6), kh = w >> 2, cg = w & 3;
  u32x32 W[4];
  const unsigned lw = lds_off(smem) + w * 4096;
  u32x4 vx;
  u32x2 tag;
  const char* xn = p.ws + WS_XN;
  auto row = [&](int t, int i) {
    const char* src = xn + (size_t)(min(t, 127) >> 1) * (SEG * XLD * 2) + (t & 1) * 512;
    unsigned keep;
    asm volatile("s_mov_b32 %0, m0\n\ts_mov_b32 m0, %3\n\ts_nop 0\n\tglobal_load_lds_dwordx4 %1, %2 sc1\n\ts_mov_b32 m0, %0"
                 : "=&s"(keep) : "v"(vx[i]), "s"(src), "s"(lw + (t & 3) * 32768 + i * 1024) : "memory");
  };
  {
    const u16* Wm = which == 0 ? p.Wk : p.Wq;
    asm volatile("" : "+s"(Wm));
    if (ISV) Wm = p.Wv;
    const unsigned vo = (tid >> 4) * (HD * 2) + 16 * ((lane & 15) ^ swzB((tid >> 4) & 15));
    auto stage = [&](int j) {
      const char* src = (const char*)(Wm + (size_t)(WREV ? 7 - j : j) * 64 * HD + h * DH);
      const unsigned dst = lds_off(smem) + (j & 3) * 32768 + w * 1024;
      glds2(src, 32 * HD * 2, vo, dst);
      glds2(src + 512 * HD * 2, 32 * HD * 2, vo, dst + 16384);
    };
    if (which == 1) { for (int z = 0; z < Q_DELAY; ++z) __builtin_amdgcn_s_sleep(16); }
    tag = start_rows<16>(p, 32 + g, [&]() { stage(0); stage(1); stage(2); stage(3); });
#pragma unroll
    for (int i = 0; i < 4; ++i) vx[i] = ((4 * w + i) ^ (ISV ? 0 : (lane >> 1) & 16)) * (XLD * 2) + (lane >> 5) * 1024 + (((lane & 31) ^ ((4 * w + i) & 15)) << 4);
    Boot bt;
#pragma unroll
    for (int j = 0; j < 8; ++j) {
      if (j < 4) WAIT_V(16); else if (j == 4) WAIT_V(12); else if (j == 5) WAIT_V(8); else if (j == 6) WAIT_V(4); else WAIT_V(10);
      if (j == 6 && w == 0) lds_at(smem + W_PM)[0] = boot_done(p.ws, bt, true, tag);
      LBAR();
      if (j == 4) report_rows(p, 32 + g, tag);
      const char* sb = smem + (j & 3) * 32768 + kh * 16384;
#pragma unroll
      for (int k = 0; k < 4; ++k) {
        const int ks = k >> 1, a = k & 1;
        const unsigned ch = ISV ? 2 * (2 * cg + (a ^ kh)) + ((r & 3) >> 1) : 4 * cg + (r & 3), by = ISV ? 8 * (r & 1) : 8 * a;
        s16x4 lo = __builtin_amdgcn_ds_read_tr16_b64_v4i16((lds_s16x4_ptr)(sb + offB(32 * ks + 8 * q + (r >> 2), ch) + by));
        s16x4 hi = __builtin_amdgcn_ds_read_tr16_b64_v4i16((lds_s16x4_ptr)(sb + offB(32 * ks + 8 * q + 4 + (r >> 2), ch) + by));
        const int f = 4 * (WREV ? 7 - j : j) + k;
        put4(W[f >> 3], f & 7, (bf16x8){lo[0], lo[1], lo[2], lo[3], hi[0], hi[1], hi[2], hi[3]});
      }
      LBAR();
      if (j == 3 && w == 0) boot_ask(p.ws, bt);
      if (j + 4 < 8) stage(j + 4);
      if (j == 6) {
#pragma unroll
        for (int i = 0; i < 10; ++i) row(i >> 2, i & 3);
      }
    }
    p.s0 = uni(lds_at(smem + W_PM)[0] >> 31);
  }
  RDY(128 + g);
  typedef __attribute__((ext_vector_type(8))) unsigned u32x8;
  u32x8 la;
#pragma unroll
  for (int i = 0; i < 8; ++i) la[i] = (i >> 2) * 65536 + r * 1024 + kh * 512 + (((4 * (i & 3) + q) ^ r) << 4);
  const unsigned xw = W_X + w * 2048 + lane * 16;
  u32x32 acc;
  asm volatile("" : "={v[128:159]}"(acc));
  WAIT_V(6);
  LBAR();
#pragma unroll 1
  for (int g0 = 0; g0 <= 12; g0 += 12) {
#define PR_LOOP(TXT)                                                                                                              \
    asm volatile(TXT                                                                                                              \
        : "+{v[128:159]}"(acc)                                                                                                    \
        : W4_IN, "{v[208:215]}"(la), "{v[216:219]}"(vx), "{v220}"(xw), "{v221}"(xw ^ 8192),                                       \
          "{v172}"((ISV ? 2 * cg + kh : 4 * kh + cg) * 1024 + lane * 16), "{v174}"(tid == 256 ? fkq(h, which) : 0x7F000000),      \
          [rt] "s"(raw_rsrc(p.ws + WS_T, T_BYTES)), [rs] "s"(raw_rsrc(p.ws + WS_SY, SY_TOTAL)), [s0] "s"(p.s0 ^ TF_KQV),          \
          [lw] "s"(lw), [kh] "s"(kh), [g0] "s"(g0), [end] "s"(g0 ? NSEG : 12), [x0] "s"(xn + (g0 + 1) * (SEG * XLD * 2)),                       \
          [t0] "s"(which * T_Q + kseg(0, h) - 1)                                                                                  \
        : CLB8(160, 161, 162, 163, 164, 165, 166, 167), "v168", "v169", "v170", "v171", "v173", CLB8(176, 177, 178, 179, 180, 181, 182, 183), \
          CLB8(184, 185, 186, 187, 188, 189, 190, 191), CLB8(192, 193, 194, 195, 196, 197, 198, 199),                             \
          CLB8(200, 201, 202, 203, 204, 205, 206, 207), "s86", "s87", "s88", "s90", "s91", "s92", "s93", "s94", "scc", "memory")
    if (ISV) PR_LOOP(PR_ASM_1); else PR_LOOP(PR_ASM_0);
    if (w == 0 && g0 == 0) wait_rows(p.ws, tag);
  }
}

enum { J_NONE = 0, J_GU = 3, J_PR = 5, J_CB = 8, J_DN = 14 };
struct BigJob { int type, panel, idx; };
struct BigPtr { const char *a, *b0, *b1; };
constexpr int G_LDA = XLD * 2, G_LDB = FF * 2;

__device__ __forceinline__ BigPtr big_ptrs(const Args& p, const BigJob& j) {
  BigPtr r;
  r.a = p.ws + WS_XN + (size_t)j.panel * 128 * G_LDA;
  r.b0 = (const char*)(p.wg + j.idx * 128); r.b1 = (const char*)(p.wu + j.idx * 128);
  return r;
}
__device__ __forceinline__ void big_publish(const Args& p, const BigJob& j) {
  int tid = threadIdx.x;
  asm volatile("" : "+v"(tid));
  if (j.type == J_GU && tid == 0)
    __hip_atomic_fetch_add((gu32*)(p.ws + WS_SY + SY_CH) + j.panel * 16, 1u, __ATOMIC_RELAXED, __HIP_MEMORY_SCOPE_AGENT);
}
typedef f32x4 Acc[2][2][4][2];
__device__ __forceinline__ u32x4 pack32(f32x4 a, f32x4 b) {
  return widen32(cvtpk(a[0], a[1]), cvtpk(a[2], a[3]), cvtpk(b[0], b[1]), cvtpk(b[2], b[3]));
}
#define st_epi(r, o, v) st_sc1(r, o, v)
__device__ __forceinline__ void big_epilogue(const Args& p, const BigJob& j, Acc& acc, char* smem) {
  int tid = threadIdx.x;
  asm volatile("" : "+v"(tid));
  const int lane = tid & 63;
  const int wid = __builtin_amdgcn_readfirstlane(tid >> 6);
  const int wr = wid >> 2, wc = wid & 3;
  {
    const __amdgpu_buffer_rsrc_t ro = mkrsrc(p.ws + WS_HF, S_LEN * 16384);
#pragma unroll
    for (int i = 0; i < 8; ++i) {
      const int row = wr * 64 + (i & 3) * 16 + (lane & 15);
      f32x4 hh[2];
#pragma unroll
      for (int n = 0; n < 2; ++n)
#pragma unroll
        for (int jj = 0; jj < 4; ++jj) {
          const float g = acc[i >> 2][0][i & 3][n][jj], u = acc[i >> 2][1][i & 3][n][jj];
          hh[n][jj] = __builtin_amdgcn_rcpf(1.0f + __builtin_amdgcn_exp2f(g * -1.4426950408889634f)) * u;
        }
      st_epi(ro, h_off(p, j.panel + (i >> 2)) + row * HLD + (j.idx * 128 + wc * 32 + (lane >> 4) * 8) * 2, pack32(hh[0], hh[1]));
    }
  }
}
#define JST(n, i, v) do {} while (0)
__device__ __forceinline__ void gemm_role(Args& p, int g, char* smem) {
  const __amdgpu_buffer_rsrc_t rsy = mkrsrc(p.ws + WS_SY, SY_TOTAL);
  const int x = g & 3, k = g >> 2;
  volatile unsigned* slot = reinterpret_cast<volatile unsigned*>(smem + 163824);
  int rnd = k < 24 ? 2 : -1;
  int nj = 0; (void)nj;
  auto claim = [&]() {
    ++rnd;
    const int n = k < 24 ? 32 + k : rnd * 8 + k - 24, pn = (n * 37) >> 8;
    BigJob b;
    b.type = rnd < 4 ? J_GU : (rnd == 4 ? J_DN : (rnd == 5 && k >= 24 && k - 24 < 14 - NFUSE ? J_CB : J_NONE));
    b.panel = rnd < 4 ? 14 - 2 * pn : (rnd == 4 ? 4 * ((2 - (k >> 3)) & 3) + x : NFUSE + k - 24);
    b.idx = rnd < 4 ? x + 4 * (n - 7 * pn) : k & 7;
    return b;
  };
  if (g < 4 && threadIdx.x == 0) { LST(480 + 4 * g); LSTC(480 + 4 * g + 1); }
  if (k < 24) {
    JST(nj, 0, J_PR); JST(nj, 1, __builtin_amdgcn_s_memrealtime());
    if (k >= 16) job_proj<true>(p, g, 2, x * 8 + (k & 7), smem, rsy);
    else job_proj<false>(p, g, k >> 3, x * 8 + (k & 7), smem, rsy);
    JST(nj, 3, __builtin_amdgcn_s_memrealtime()); ++nj;
  } else {
    const unsigned t0 = (unsigned)__builtin_amdgcn_s_memrealtime();
    const u32x2 tag = start_rows<0>(p, g - 96, []() {});
    WAIT_V(0);
    LBAR();
    report_rows(p, g - 96, tag);
    while ((unsigned)__builtin_amdgcn_s_memrealtime() - t0 < PF_T0) __builtin_amdgcn_s_sleep(8);
#pragma unroll 1
    for (int i = 0; i < PF_N; ++i) {
      const int m = i % 3, o = ((i / 3) * 32 + g - 96) * 448 + (int)threadIdx.x;
      if (threadIdx.x < 448) touch_line((const char*)(m == 0 ? p.wg : (m == 1 ? p.wu : p.wd)) + (size_t)o * 128, smem + (threadIdx.x >> 6) * 256);
      WAIT_V(1);
    }
    WAIT_V(0);
    if (threadIdx.x < 64) {
      wait_rows(p.ws, tag);
      Boot bt;
      boot_ask(p.ws, bt);
      WAIT_V(0);
      *slot = boot_done(p.ws, bt, true, tag);
      while ((unsigned)__builtin_amdgcn_s_memrealtime() - t0 < GU_DELAY) __builtin_amdgcn_s_sleep(8);
    }
    LBAR();
    p.s0 = *slot >> 31;
    RDY(128 + g);
  }
  p.s0 = uni(p.s0);
  BigJob cur = claim();
  while (cur.type != J_NONE) {
    JST(nj, 0, cur.type | cur.panel << 8); JST(nj, 1, __builtin_amdgcn_s_memrealtime());
    if (cur.type != J_GU) {
      if (cur.type == J_DN) job_down(p, cur.panel, cur.idx, smem, rsy);
      else job_comb(p, cur.panel, x, rsy);
      JST(nj, 3, __builtin_amdgcn_s_memrealtime()); ++nj;
      cur = claim();
      continue;
    }
    int tid = threadIdx.x;
    asm volatile("" : "+v"(tid));
    const int lane = tid & 63;
    const int wid = __builtin_amdgcn_readfirstlane(tid >> 6);
    const int wr = wid >> 2, wc = wid & 3;
    const unsigned ra = tid >> 3, ca = 16 * ((tid & 7) ^ ((tid >> 3) & 7)), rb = tid >> 4, cb = 16 * ((tid & 15) ^ swzB((tid >> 4) & 15));
    const unsigned lw = lds_off(smem) + wid * 1024;
#define B8_SA(b, h) (((b) * 2 + (h)) * 16384)
#define B8_SB(b, h) ((4 + (b) * 2 + (h)) * 16384)
#define B8_STA(b, h, P, kt) glds2<true>((P).a + (size_t)(h) * 128 * G_LDA + (kt) * 128, 64 * G_LDA, ra * G_LDA + ca, lw + B8_SA(b, h))
#define B8_STB(b, h, P, kt) glds2(((h) ? (P).b1 : (P).b0) + (size_t)(kt) * 64 * G_LDB, 32 * G_LDB, rb * G_LDB + cb, lw + B8_SB(b, h))
#define B8_LDA(b, h) do { _Pragma("unroll") for (int m = 0; m < 4; ++m) _Pragma("unroll") for (int kk = 0; kk < 2; ++kk) \
      At[m][kk] = ldsf(smem + B8_SA(b, h) + offA(wr * 64 + m * 16 + (lane & 15), kk * 4 + (lane >> 4))); } while (0)
#define B8_LDB(dst, b, h) do { _Pragma("unroll") for (int n = 0; n < 2; ++n) _Pragma("unroll") for (int kk = 0; kk < 2; ++kk) \
      dst[n][kk] = ld_bfrag(smem + B8_SB(b, h), kk, wc * 2 + n, lane); } while (0)
#define B8_MMA(ai, bj, Bt) do { __builtin_amdgcn_s_setprio(1); _Pragma("unroll") for (int m = 0; m < 4; ++m) _Pragma("unroll") for (int n = 0; n < 2; ++n) \
      _Pragma("unroll") for (int kk = 0; kk < 2; ++kk) acc[ai][bj][m][n] = MFMA(Bt[n][kk], At[m][kk], acc[ai][bj][m][n]); __builtin_amdgcn_s_setprio(0); } while (0)
#define WAIT_L(n) asm volatile("s_waitcnt lgkmcnt(%0)" :: "n"(n) : "memory")
#define BAR() __builtin_amdgcn_s_barrier()
#define WAIT_RX(c) asm volatile("s_waitcnt vmcnt(16)\n\ts_cmp_lg_u32 %0, 0\n\ts_cbranch_scc1 .Lrx%=\n\ts_waitcnt vmcnt(8)\n.Lrx%=:" :: "s"(c) : "memory", "scc")
    Acc acc;
    bf16x8 At[4][2], B0[2][2], B1[2][2];
    BigPtr cp = big_ptrs(p, cur);
    BigJob pend; pend.type = J_NONE; pend.panel = 0; pend.idx = 0;
    LBAR();
    B8_STB(0, 0, cp, 0); B8_STB(0, 1, cp, 0); B8_STA(0, 0, cp, 0); B8_STA(0, 1, cp, 0);
    if (wr == 1) BAR();
    WAIT_V(2); BAR();
    B8_STB(1, 0, cp, 1); B8_STA(1, 0, cp, 1); B8_STB(1, 1, cp, 1);
    WAIT_V(6); BAR();
    BigJob nx = pend;
    for (;;) {
      bool chain = false;
      BigPtr np = cp;
      constexpr int nt = DM / 64;
#pragma unroll
      for (int i = 0; i < 32; ++i) acc[i >> 4][(i >> 3) & 1][(i >> 1) & 3][i & 1] = (f32x4){0.f, 0.f, 0.f, 0.f};
#pragma unroll 1
      for (int t = 0; t < nt; t += 2) {
        const bool last = t == nt - 2;
        if (last) {
          nx = claim();
          chain = nx.type == J_GU;
          if (chain) np = big_ptrs(p, nx);
        }
        const BigPtr sp = np;
        const int t2 = last ? 0 : t + 2;
        if (t == 2) big_publish(p, pend);
        const int rx = __builtin_amdgcn_readfirstlane(t == 0 && pend.type != J_NONE);
        B8_LDB(B0, 0, 0); B8_LDB(B1, 0, 1); SCHED(); B8_LDA(0, 0); B8_STA(1, 1, cp, t + 1);
        WAIT_RX(rx);
        WAIT_L(0); BAR(); B8_MMA(0, 0, B0); B8_MMA(0, 1, B1); BAR(); SCHED();
        B8_LDA(0, 1); B8_STB(0, 0, sp, t2); B8_STB(0, 1, sp, t2); B8_STA(0, 0, sp, t2);
        WAIT_RX(rx);
        WAIT_L(0); BAR(); B8_MMA(1, 0, B0); B8_MMA(1, 1, B1); BAR(); SCHED();
        B8_LDB(B0, 1, 0); B8_LDB(B1, 1, 1); SCHED(); B8_LDA(1, 0); B8_STA(0, 1, sp, t2);
        WAIT_V(8); WAIT_L(0); BAR(); B8_MMA(0, 0, B0); B8_MMA(0, 1, B1); BAR(); SCHED();
        B8_LDA(1, 1); B8_STB(1, 0, sp, t2 + 1); B8_STB(1, 1, sp, t2 + 1); B8_STA(1, 0, sp, t2 + 1);
        WAIT_V(8); WAIT_L(0); BAR(); B8_MMA(1, 0, B0); B8_MMA(1, 1, B1); BAR(); SCHED();
      }
      if (wr == 0) BAR();
      JST(nj, 2, __builtin_amdgcn_s_memrealtime());
      big_epilogue(p, cur, acc, smem);
      if (!chain) break;
      JST(nj, 3, __builtin_amdgcn_s_memrealtime()); ++nj;
      pend = cur; cur = nx; cp = np;
      if (wr == 1) BAR();
      JST(nj, 0, cur.type | cur.panel << 8); JST(nj, 1, __builtin_amdgcn_s_memrealtime());
    }
    WAIT_V(0);
    BAR();
    asm volatile(".Lcode_end:");
    big_publish(p, cur);
    JST(nj, 3, __builtin_amdgcn_s_memrealtime()); ++nj;
    cur = nx;
  }
  if (threadIdx.x == 0) { LST(128 + g); }
  if (g < 4 && threadIdx.x == 0) { LST(480 + 4 * g + 2); LSTC(480 + 4 * g + 3); }
}

__device__ __forceinline__ unsigned now_q() { return (unsigned)__builtin_amdgcn_s_memrealtime() * 4u; }
__device__ __forceinline__ void pace_until(unsigned t) {
#pragma unroll 1
  for (int i = 0; i < 256; ++i) {
    const int d = (int)(t - now_q());
    if (d <= 0) break;
    if (d > 32) __builtin_amdgcn_s_sleep(1);
  }
}
#define CLB_E "s76", "s77", "s78", "s79", "s80", "s81", "s82", "s83", "s84", "s85", "s86", "s87", "s88", "s89"
#define EDGE_ASM \
  "s_memtime s[76:77]\n\t" \
  "s_memrealtime s[78:79]\n\t" \
  "s_nop 3\n\t" \
  "s_memrealtime s[80:81]\n\t" \
  "s_nop 3\n\t" \
  "s_memrealtime s[82:83]\n\t" \
  "s_nop 3\n\t" \
  "s_memrealtime s[84:85]\n\t" \
  "s_nop 3\n\t" \
  "s_memrealtime s[86:87]\n\t" \
  "s_nop 3\n\t" \
  "s_memrealtime s[88:89]\n\t" \
  "s_mov_b32 s93, 0\n\t" \
  "s_waitcnt lgkmcnt(0)\n\t" \
  "s_cmp_eq_u32 s80, s78\n\t" \
  "s_addc_u32 s93, s93, 0\n\t" \
  "s_cmp_eq_u32 s82, s78\n\t" \
  "s_addc_u32 s93, s93, 0\n\t" \
  "s_cmp_eq_u32 s84, s78\n\t" \
  "s_addc_u32 s93, s93, 0\n\t" \
  "s_cmp_eq_u32 s86, s78\n\t" \
  "s_addc_u32 s93, s93, 0\n\t" \
  "s_cmp_eq_u32 s88, s78\n\t" \
  "s_addc_u32 s93, s93, 0\n\t" \
  "s_mul_i32 s93, s93, 21\n\t" \
  "s_add_u32 s76, s76, 15\n\t" \
  "s_add_u32 %[kc], s76, s93\n\t" \
  "s_lshl2_add_u32 %[kq], s78, 16\n\t"
__device__ __forceinline__ unsigned time_of(unsigned cyc) {
  unsigned c, q;
  asm volatile(EDGE_ASM : [kc] "=&s"(c), [kq] "=&s"(q) :: CLB_E, "s93", "scc");
  return q - (((c - cyc) * 11900u) >> 16);
}
#define DS_RED 20
#define DS_MEM 14
#define LEAD_DN 2
#define LEAD_UP 12
__device__ __forceinline__ u32x2 ld2_sc1(__amdgpu_buffer_rsrc_t rs, int off, int soff = 0) {
  return __builtin_amdgcn_raw_buffer_load_b64(rs, off, soff, 16);
}
__device__ __forceinline__ void st2_sc1(__amdgpu_buffer_rsrc_t rs, int off, u32x2 v) {
  __builtin_amdgcn_raw_buffer_store_b64(v, rs, off, 0, 16);
}
__device__ __forceinline__ int row_max(int v) {
  v = max(v, __builtin_amdgcn_update_dpp(0, v, 0x128, 0xf, 0xf, false));
  v = max(v, __builtin_amdgcn_update_dpp(0, v, 0x124, 0xf, 0xf, false));
  v = max(v, __builtin_amdgcn_update_dpp(0, v, 0x122, 0xf, 0xf, false));
  v = max(v, __builtin_amdgcn_update_dpp(0, v, 0x121, 0xf, 0xf, false));
  return v;
}
__device__ __forceinline__ int wave_max(int v) {
  v = row_max(v);
  return max(max(__builtin_amdgcn_readlane(v, 0), __builtin_amdgcn_readlane(v, 16)),
             max(__builtin_amdgcn_readlane(v, 32), __builtin_amdgcn_readlane(v, 48)));
}
__device__ __forceinline__ int track(int est, int x, int up, int dn, int out) {
  const int d = x - est;
  return est + (d > out ? 2 : max(min(d, up), -dn));
}
#define WSL 12
#define PLAN_SLEEP 2
__device__ __forceinline__ unsigned latest(unsigned t) {
  const unsigned t0 = __builtin_amdgcn_readfirstlane(t);
  return t0 + wave_max((int)(t - t0));
}
#define RL0 300
__device__ __forceinline__ void watch(__amdgpu_buffer_rsrc_t rsy, int s, int lane, unsigned t0, const unsigned* tgt_, gu32* abortw, int hs) {
  pace_until(t0);
  const int rl = (int)__builtin_amdgcn_raw_buffer_load_b32(rsy, lane * 128, SY_BR + hs, 16);
  u32x2 ra;
  for (unsigned spins = 0;;) {
    asm volatile("" ::: "memory");
    ra = ld2_sc1(rsy, (lane & 31) * 128, SY_BA + hs);
    const bool all = __all((int)ra[0] >= s);
    SMP(4, s, spins);
    if (all) break;
    if (spins < 3) __builtin_amdgcn_s_sleep(PLAN_SLEEP); else __builtin_amdgcn_s_sleep(WSL);
    if (spin_fail(spins, abortw)) break;
  }
  const unsigned t = latest(ra[0] == (unsigned)s ? ra[1] : t0) + wave_max(rl < 0 ? RL0 : rl) + (DS_RED + DS_MEM);
  lds_vu_ptr tgt = lds_at(tgt_);
  if (lane < 2) tgt[lane] = lane ? t : s + 1;
  SMP(0, s, (int)(t - t0)); SMP(1, s, (int)(t - now_q()));
}
__device__ __forceinline__ unsigned ld_flag(__amdgpu_buffer_rsrc_t rs, int off) {
  return __builtin_amdgcn_raw_buffer_load_b32(rs, off, 0, 16);
}

constexpr unsigned long long QK_MAP[8] = {0xbaf9876d543210ecull, 0xa98f76d543210bceull, 0x98f76d543210ecbaull, 0x876fd543210bcea9ull, 0x76fd543210ecba98ull, 0x65df43210bcea987ull, 0x5df43210ecba9876ull, 0xd43f210bcea98765ull};
#define QK_ASM_0 \
  "s_waitcnt lgkmcnt(0)\n\t" \
  "v_subrev_u32 v244, 0x8000, v250\n\t" \
  "ds_read_b128 v[216:219], v248 offset:32768\n\t" \
  "ds_read_b128 v[220:223], v248 offset:33792\n\t" \
  "ds_read_b128 v[224:227], v248 offset:34816\n\t" \
  "ds_read_b128 v[228:231], v248 offset:35840\n\t" \
  "ds_read_b128 v[208:211], v244 offset:0\n\t" \
  "ds_read_b128 v[212:215], v244 offset:1024\n\t" \
  "ds_read_b128 v[236:239], v244 offset:2048\n\t" \
  "ds_read_b128 v[240:243], v244 offset:3072\n\t" \
  "v_mov_b64 v[192:193], 0\n\t" \
  "v_mov_b64 v[194:195], 0\n\t" \
  "v_mov_b64 v[196:197], 0\n\t" \
  "v_mov_b64 v[198:199], 0\n\t" \
  "s_movk_i32 s92, 0x2000\n\t" \
  ".Lwt%=:\n\t" \
  "ds_read_b64 v[200:201], v248 offset:30720\n\t" \
  "s_memrealtime s[90:91]\n\t" \
  "s_sub_u32 s92, s92, 1\n\t" \
  "s_waitcnt lgkmcnt(0)\n\t" \
  "v_readfirstlane_b32 s93, v200\n\t" \
  "v_readfirstlane_b32 %[tgt], v201\n\t" \
  "s_cbranch_scc1 .Lgo%=\n\t" \
  "s_cmp_eq_u32 s93, %[step]\n\t" \
  "s_cbranch_scc1 .Lwx%=\n\t" \
  "s_sleep 1\n\t" \
  "s_branch .Lwt%=\n\t" \
  ".Lwx%=:\n\t" \
  "s_lshl_b32 s93, s90, 2\n\t" \
  "s_sub_i32 s93, %[tgt], s93\n\t" \
  "s_cmp_le_i32 s93, 0\n\t" \
  "s_cbranch_scc1 .Lgo%=\n\t" \
  "s_min_i32 s93, s93, 8000\n\t" \
  "s_mov_b32 s94, s93\n\t" \
  ".Lsl%=:\n\t" \
  "s_sub_i32 s94, s94, 18\n\t" \
  "s_cmp_lt_i32 s94, 30\n\t" \
  "s_cbranch_scc1 .Lsx%=\n\t" \
  "s_sleep 1\n\t" \
  "s_branch .Lsl%=\n\t" \
  ".Lsx%=:\n\t" \
  "s_sub_i32 s93, s93, 12\n\t" \
  "s_max_i32 s93, s93, 4\n\t" \
  "s_lshr_b32 s93, s93, 2\n\t" \
  "s_add_u32 s93, s93, s90\n\t" \
  "s_memrealtime s[90:91]\n\t" \
  ".Lpace%=:\n\t" \
  "s_memrealtime s[94:95]\n\t" \
  "s_waitcnt lgkmcnt(1)\n\t" \
  "s_sub_i32 s92, s90, s93\n\t" \
  "s_cmp_ge_i32 s92, 0\n\t" \
  "s_cbranch_scc1 .Le%=\n\t" \
  "s_memrealtime s[90:91]\n\t" \
  "s_waitcnt lgkmcnt(1)\n\t" \
  "s_sub_i32 s92, s94, s93\n\t" \
  "s_cmp_lt_i32 s92, 0\n\t" \
  "s_cbranch_scc1 .Lpace%=\n\t" \
  "s_nop 4\n\t" \
  ".Le%=:\n\t" \
  "s_add_u32 s92, s92, s93\n\t" \
  "s_lshl_b32 s92, s92, 2\n\t" \
  "s_sub_i32 s92, %[tgt], s92\n\t" \
  "s_max_i32 s92, s92, 0\n\t" \
  "s_bitcmp0_b32 s92, 3\n\t" \
  "s_cbranch_scc1 .Lf3%=\n\t" \
  "s_nop 15\n\t" \
  ".Lf3%=:\n\t" \
  "s_bitcmp0_b32 s92, 2\n\t" \
  "s_cbranch_scc1 .Lf2%=\n\t" \
  "s_nop 10\n\t" \
  ".Lf2%=:\n\t" \
  ".Lgo%=:\n\t" \
  "s_mov_b32 s92, 0\n\t" \
  ".Lpoll%=:\n\t" \
  "buffer_load_dwordx4 v[200:203], v248, %[rs], %[som] offen sc1\n\t" \
  "buffer_load_dwordx4 v[204:207], v248, %[rs], %[som2] offen sc1\n\t" \
  "s_waitcnt vmcnt(1) lgkmcnt(0)\n\t" \
  "ds_write_b128 v249, v[200:203]\n\t" \
  "v_smfmac_f32_16x16x64_bf16 v[192:195], v[200:203], v[208:215], v251\n\t" \
  "v_max3_u32 v244, v200, v201, v202\n\t" \
  "v_smfmac_f32_16x16x64_bf16 v[196:199], v[200:203], v[236:243], v251\n\t" \
  "s_waitcnt vmcnt(0)\n\t" \
  "ds_write_b128 v249, v[204:207] offset:8192\n\t" \
  "v_smfmac_f32_16x16x64_bf16 v[192:195], v[204:207], v[216:223], v251\n\t" \
  "v_max3_u32 v244, v244, v203, v204\n\t" \
  "v_max3_u32 v244, v244, v205, v206\n\t" \
  "v_smfmac_f32_16x16x64_bf16 v[196:199], v[204:207], v[224:231], v251\n\t" \
  "v_max_u32 v244, v244, v207\n\t" \
  "v_cmp_le_u32 vcc, %[sent], v244\n\t" \
  "s_cbranch_vccnz .Lslow%=\n\t" \
  ".Lok%=:\n\t" \
  "ds_read_b128 v[216:219], v250 offset:32768\n\t" \
  "ds_read_b128 v[220:223], v250 offset:33792\n\t" \
  "ds_read_b128 v[224:227], v250 offset:34816\n\t" \
  "ds_read_b128 v[228:231], v250 offset:35840\n\t" \
  "s_waitcnt lgkmcnt(4)\n\t" \
  "s_barrier\n\t" \
  "ds_read_b128 v[208:211], v248 offset:1024\n\t" \
  "ds_read_b128 v[212:215], v248 offset:2048\n\t" \
  "ds_read_b128 v[236:239], v248 offset:3072\n\t" \
  "ds_read_b128 v[240:243], v248 offset:4096\n\t" \
  "s_waitcnt lgkmcnt(3)\n\t" \
  "ds_read_b128 v[244:247], v248 offset:13312\n\t" \
  "v_smfmac_f32_16x16x64_bf16 v[192:195], v[208:211], v[216:223], v251\n\t" \
  "ds_read_b128 v[216:219], v250 offset:0\n\t" \
  "v_smfmac_f32_16x16x64_bf16 v[196:199], v[208:211], v[224:231], v251\n\t" \
  "s_waitcnt lgkmcnt(4)\n\t" \
  "ds_read_b128 v[220:223], v250 offset:1024\n\t" \
  "v_smfmac_f32_16x16x64_bf16 v[192:195], v[212:215], v[0:7], v251\n\t" \
  "ds_read_b128 v[224:227], v250 offset:2048\n\t" \
  "v_smfmac_f32_16x16x64_bf16 v[196:199], v[212:215], v[8:15], v251\n\t" \
  "s_waitcnt lgkmcnt(5)\n\t" \
  "ds_read_b128 v[228:231], v250 offset:3072\n\t" \
  "v_smfmac_f32_16x16x64_bf16 v[192:195], v[236:239], v[16:23], v251\n\t" \
  "ds_read_b128 v[200:203], v248 offset:5120\n\t" \
  "v_smfmac_f32_16x16x64_bf16 v[196:199], v[236:239], v[24:31], v251\n\t" \
  "s_waitcnt lgkmcnt(6)\n\t" \
  "ds_read_b128 v[204:207], v248 offset:6144\n\t" \
  "v_smfmac_f32_16x16x64_bf16 v[192:195], v[240:243], v[32:39], v251\n\t" \
  "ds_read_b128 v[208:211], v248 offset:7168\n\t" \
  "v_smfmac_f32_16x16x64_bf16 v[196:199], v[240:243], v[40:47], v251\n\t" \
  "s_waitcnt lgkmcnt(5)\n\t" \
  "ds_read_b128 v[212:215], v248 offset:9216\n\t" \
  "v_smfmac_f32_16x16x64_bf16 v[192:195], v[244:247], v[216:223], v251\n\t" \
  "s_waitcnt lgkmcnt(4)\n\t" \
  "v_smfmac_f32_16x16x64_bf16 v[196:199], v[244:247], v[224:231], v251\n\t" \
  "s_waitcnt lgkmcnt(3)\n\t" \
  "ds_read_b128 v[236:239], v248 offset:10240\n\t" \
  "v_smfmac_f32_16x16x64_bf16 v[192:195], v[200:203], v[48:55], v251\n\t" \
  "ds_read_b128 v[240:243], v248 offset:11264\n\t" \
  "v_smfmac_f32_16x16x64_bf16 v[196:199], v[200:203], v[56:63], v251\n\t" \
  "s_waitcnt lgkmcnt(4)\n\t" \
  "v_smfmac_f32_16x16x64_bf16 v[192:195], v[204:207], v[64:71], v251\n\t" \
  "ds_read_b128 v[244:247], v248 offset:12288\n\t" \
  "v_smfmac_f32_16x16x64_bf16 v[196:199], v[204:207], v[72:79], v251\n\t" \
  "s_waitcnt lgkmcnt(4)\n\t" \
  "v_smfmac_f32_16x16x64_bf16 v[192:195], v[208:211], v[80:87], v251\n\t" \
  "ds_read_b128 v[200:203], v248 offset:14336\n\t" \
  "v_smfmac_f32_16x16x64_bf16 v[196:199], v[208:211], v[88:95], v251\n\t" \
  "s_waitcnt lgkmcnt(4)\n\t" \
  "v_smfmac_f32_16x16x64_bf16 v[192:195], v[212:215], v[96:103], v251\n\t" \
  "ds_read_b128 v[204:207], v248 offset:15360\n\t" \
  "v_smfmac_f32_16x16x64_bf16 v[196:199], v[212:215], v[104:111], v251\n\t" \
  "s_waitcnt lgkmcnt(3)\n\t" \
  "v_smfmac_f32_16x16x64_bf16 v[192:195], v[236:239], v[112:119], v251\n\t" \
  "v_smfmac_f32_16x16x64_bf16 v[196:199], v[236:239], v[120:127], v251\n\t" \
  "v_smfmac_f32_16x16x64_bf16 v[192:195], v[240:243], v[128:135], v251\n\t" \
  "v_smfmac_f32_16x16x64_bf16 v[196:199], v[240:243], v[136:143], v251\n\t" \
  "s_waitcnt lgkmcnt(1)\n\t" \
  "v_smfmac_f32_16x16x64_bf16 v[192:195], v[244:247], v[144:151], v251\n\t" \
  "v_smfmac_f32_16x16x64_bf16 v[196:199], v[244:247], v[152:159], v251\n\t" \
  "v_smfmac_f32_16x16x64_bf16 v[192:195], v[200:203], v[160:167], v251\n\t" \
  "v_smfmac_f32_16x16x64_bf16 v[196:199], v[200:203], v[168:175], v251\n\t" \
  "s_waitcnt lgkmcnt(0)\n\t" \
  "v_smfmac_f32_16x16x64_bf16 v[192:195], v[204:207], v[176:183], v251\n\t" \
  "v_smfmac_f32_16x16x64_bf16 v[196:199], v[204:207], v[184:191], v251\n\t" \
  "ds_read_b128 v[200:203], v248 offset:16384\n\t" \
  "ds_read_b128 v[204:207], v248 offset:20480\n\t" \
  "v_mov_b32 v250, v232\n\t" \
  "v_mov_b32 v249, v233\n\t" \
  "s_nop 2\n\t" \
  "v_add_f32 v192, v192, v194\n\t" \
  "v_add_f32 v193, v193, v195\n\t" \
  "v_add_f32 v196, v196, v198\n\t" \
  "v_add_f32 v197, v197, v199\n\t" \
  "v_cvt_pk_bf16_f32 v192, v192, v193\n\t" \
  "v_cvt_pk_bf16_f32 v196, v196, v197\n\t" \
  "ds_write2_b32 v252, v192, v196 offset1:64\n\t" \
  "v_mov_b32 v251, v234\n\t" \
  "ds_read_b64_tr_b16 v[232:233], v250 offset:0\n\t" \
  "ds_read_b64_tr_b16 v[234:235], v250 offset:64\n\t" \
  "v_mov_b32 v212, %[step]\n\t" \
  "s_mov_b64 exec, 1\n\t" \
  "ds_write_b32 v248, v212 offset:28672\n\t" \
  "s_mov_b64 exec, -1\n\t" \
  "buffer_load_dwordx4 v[192:195], v248, %[rt], %[svt] offen offset:0 sc1\n\t" \
  "buffer_load_dwordx4 v[196:199], v248, %[rt], %[svt] offen offset:1024 sc1\n\t" \
  "s_waitcnt lgkmcnt(1)\n\t" \
  "v_mfma_f32_16x16x32_bf16 v[200:203], v[200:203], v[232:235], 0\n\t" \
  "v_mfma_f32_16x16x32_bf16 v[204:207], v[204:207], v[232:235], 0\n\t" \
  "s_nop 7\n\t" \
  "v_cndmask_b32 v216, v200, v204, %[nh]\n\t" \
  "v_cndmask_b32 v217, v201, v205, %[nh]\n\t" \
  "v_cndmask_b32 v218, v202, v206, %[nh]\n\t" \
  "v_cndmask_b32 v219, v203, v207, %[nh]\n\t" \
  "ds_write_b128 v248, v[216:219] offset:16384\n\t" \
  "buffer_load_dword v252, v249, %[rs], %[sv] offen offset:0 sc1\n\t" \
  "buffer_load_dword v253, v249, %[rs], %[sv] offen offset:256 sc1\n\t" \
  "ds_read_b32 v212, v248 offset:28672\n\t" \
  "ds_read_b64_tr_b16 v[236:237], v250 offset:512\n\t" \
  "ds_read_b64_tr_b16 v[238:239], v250 offset:576\n\t" \
  "ds_read_b64_tr_b16 v[240:241], v250 offset:1024\n\t" \
  "ds_read_b64_tr_b16 v[242:243], v250 offset:1088\n\t" \
  "ds_read_b64_tr_b16 v[244:245], v250 offset:1536\n\t" \
  "ds_read_b64_tr_b16 v[246:247], v250 offset:1600\n\t" \
  "s_setprio 3\n\t" \
  "s_waitcnt lgkmcnt(0)\n\t" \
  "v_cmp_ne_u32 vcc, %[step], v212\n\t" \
  "s_barrier\n\t" \
  "s_and_b32 s93, vcc_lo, 0xf\n\t" \
  "s_cbranch_scc1 .Lre%=\n\t" \
  ".Lrb%=:\n\t" \
  "ds_read_b128 v[220:223], v248 offset:17408\n\t" \
  "ds_read_b128 v[224:227], v248 offset:18432\n\t" \
  "ds_read_b128 v[228:231], v248 offset:19456\n\t" \
  "ds_read_b64_tr_b16 v[200:201], v250 offset:2048\n\t" \
  "ds_read_b64_tr_b16 v[202:203], v250 offset:2112\n\t" \
  "ds_read_b64_tr_b16 v[204:205], v250 offset:2560\n\t" \
  "ds_read_b64_tr_b16 v[206:207], v250 offset:2624\n\t" \
  "ds_read_b64_tr_b16 v[208:209], v250 offset:3072\n\t" \
  "ds_read_b64_tr_b16 v[210:211], v250 offset:3136\n\t" \
  "ds_read_b64_tr_b16 v[212:213], v250 offset:3584\n\t" \
  "ds_read_b64_tr_b16 v[214:215], v250 offset:3648\n\t" \
  "s_waitcnt lgkmcnt(10)\n\t" \
  "v_pk_add_f32 v[216:217], v[216:217], v[220:221]\n\t" \
  "v_pk_add_f32 v[218:219], v[218:219], v[222:223]\n\t" \
  "s_waitcnt lgkmcnt(8)\n\t" \
  "v_pk_add_f32 v[224:225], v[224:225], v[228:229]\n\t" \
  "v_pk_add_f32 v[226:227], v[226:227], v[230:231]\n\t" \
  "v_pk_add_f32 v[216:217], v[216:217], v[224:225]\n\t" \
  "v_pk_add_f32 v[218:219], v[218:219], v[226:227]\n\t" \
  "s_waitcnt lgkmcnt(6)\n\t" \
  "v_mfma_f32_16x16x32_bf16 v[200:203], v[200:203], v[232:235], 0\n\t" \
  "v_max3_f32 v224, v216, v217, v218\n\t" \
  "v_max_f32 v224, v224, v219\n\t" \
  "s_waitcnt lgkmcnt(4)\n\t" \
  "v_mfma_f32_16x16x32_bf16 v[200:203], v[204:207], v[236:239], v[200:203]\n\t" \
  "v_max_f32_dpp v225, v224, v224 row_ror:8 row_mask:0xf bank_mask:0xf\n\t" \
  "v_max_f32_dpp v226, v224, v224 row_ror:8 row_mask:0xf bank_mask:0xf\n\t" \
  "s_waitcnt lgkmcnt(2)\n\t" \
  "v_mfma_f32_16x16x32_bf16 v[200:203], v[208:211], v[240:243], v[200:203]\n\t" \
  "v_permlane32_swap_b32 v225, v226\n\t" \
  "s_waitcnt lgkmcnt(0)\n\t" \
  "v_mfma_f32_16x16x32_bf16 v[200:203], v[212:215], v[244:247], v[200:203]\n\t" \
  "v_max_f32 v231, v225, v226\n\t" \
  "v_mov_b32 v220, 0x3f803f80\n\t" \
  "v_mov_b32 v221, 0x3f803f80\n\t" \
  "v_mov_b64 v[222:223], v[220:221]\n\t" \
  "v_mov_b32 v233, 0\n\t" \
  "v_mov_b32 v234, 0\n\t" \
  "s_nop 1\n\t" \
  "v_max3_f32 v204, v200, v201, v231\n\t" \
  "v_max3_f32 v205, v204, v202, v203\n\t" \
  "v_max3_f32 v206, v204, v202, v203\n\t" \
  "v_cndmask_b32 v200, v200, v202, %[up]\n\t" \
  "v_cndmask_b32 v201, v201, v203, %[up]\n\t" \
  "v_permlane16_swap_b32 v205, v206\n\t" \
  "v_max_f32 v204, v205, v206\n\t" \
  "v_mul_f32 v204, %[negc], v204\n\t" \
  "v_fma_f32 v200, v200, %[c], v204\n\t" \
  "v_fma_f32 v201, v201, %[c], v204\n\t" \
  "v_exp_f32 v200, v200\n\t" \
  "v_fma_f32 v216, v216, %[c], v204\n\t" \
  "v_exp_f32 v201, v201\n\t" \
  "v_fma_f32 v217, v217, %[c], v204\n\t" \
  "v_exp_f32 v216, v216\n\t" \
  "v_fma_f32 v218, v218, %[c], v204\n\t" \
  "v_cvt_pk_bf16_f32 v232, v200, v201\n\t" \
  "v_cvt_pk_bf16_f32 v235, v200, v201\n\t" \
  "v_exp_f32 v217, v217\n\t" \
  "v_fma_f32 v219, v219, %[c], v204\n\t" \
  "v_mfma_f32_16x16x16_bf16 v[212:215], v[220:221], v[232:233], 0\n\t" \
  "v_exp_f32 v218, v218\n\t" \
  "s_waitcnt vmcnt(0)\n\t" \
  "v_mfma_f32_16x16x16_bf16 v[224:227], v[252:253], v[232:233], 0\n\t" \
  "v_cvt_pk_bf16_f32 v210, v216, v217\n\t" \
  "v_cvt_pk_bf16_f32 v208, v216, v217\n\t" \
  "v_exp_f32 v219, v219\n\t" \
  "v_mfma_f32_16x16x16_bf16 v[228:231], v[252:253], v[234:235], 0\n\t" \
  "v_cvt_pk_bf16_f32 v211, v218, v219\n\t" \
  "v_cvt_pk_bf16_f32 v209, v218, v219\n\t" \
  "v_mov_b32_dpp v208, v210 row_ror:8 row_mask:0xf bank_mask:0xc\n\t" \
  "v_mov_b32_dpp v210, v210 row_ror:8 row_mask:0xf bank_mask:0x3\n\t" \
  "v_mov_b32_dpp v209, v211 row_ror:8 row_mask:0xf bank_mask:0xc\n\t" \
  "v_mov_b32_dpp v211, v211 row_ror:8 row_mask:0xf bank_mask:0x3\n\t" \
  "v_max_u32 v204, v252, v253\n\t" \
  "v_cmp_le_u32 vcc, %[sent], v204\n\t" \
  "v_mfma_f32_16x16x32_bf16 v[212:215], v[220:223], v[208:211], v[212:215]\n\t" \
  ".Lpv%=:\n\t" \
  "v_mfma_f32_16x16x32_bf16 v[224:227], v[192:195], v[208:211], v[224:227]\n\t" \
  "v_mfma_f32_16x16x32_bf16 v[228:231], v[196:199], v[208:211], v[228:231]\n\t" \
  "s_cbranch_vccnz .Lvs%=\n\t" \
  ".Lvok%=:\n\t" \
  "s_nop 4\n\t" \
  "v_rcp_f32 v204, v212\n\t" \
  "v_cndmask_b32 v224, v224, v226, %[nh]\n\t" \
  "v_cndmask_b32 v225, v225, v227, %[nh]\n\t" \
  "v_cndmask_b32 v228, v228, v230, %[nh]\n\t" \
  "v_cndmask_b32 v229, v229, v231, %[nh]\n\t" \
  "v_pk_mul_f32 v[224:225], v[224:225], v[204:205] op_sel_hi:[1,0]\n\t" \
  "v_pk_mul_f32 v[228:229], v[228:229], v[204:205] op_sel_hi:[1,0]\n\t" \
  "v_cvt_pk_bf16_f32 v224, v224, v225\n\t" \
  "v_cvt_pk_bf16_f32 v225, v228, v229\n\t" \
  "buffer_store_dwordx2 v[224:225], v251, %[rs], %[so] offen sc1\n\t" \
  "s_memtime s[90:91]\n\t" \
  "s_setprio 0\n\t" \
  "s_waitcnt lgkmcnt(0)\n\t" \
  "s_mov_b32 %[tout], s90\n\t" \
  "s_mov_b32 %[spins], s92\n\t" \
  "s_branch .Lend%=\n\t" \
  ".Lre%=:\n\t" \
  "ds_read_b64_tr_b16 v[236:237], v250 offset:512\n\t" \
  "ds_read_b64_tr_b16 v[238:239], v250 offset:576\n\t" \
  "ds_read_b64_tr_b16 v[240:241], v250 offset:1024\n\t" \
  "ds_read_b64_tr_b16 v[242:243], v250 offset:1088\n\t" \
  "ds_read_b64_tr_b16 v[244:245], v250 offset:1536\n\t" \
  "ds_read_b64_tr_b16 v[246:247], v250 offset:1600\n\t" \
  "s_waitcnt lgkmcnt(0)\n\t" \
  "s_branch .Lrb%=\n\t" \
  ".Lvs%=:\n\t" \
  "v_cmp_eq_u32 vcc, %[sent], v252\n\t" \
  "v_cmp_eq_u32 s[94:95], %[sent], v253\n\t" \
  "s_or_b64 s[94:95], s[94:95], vcc\n\t" \
  "s_cbranch_scc0 .Lvok%=\n\t" \
  "s_add_u32 s92, s92, 1\n\t" \
  "s_sleep 2\n\t" \
  "s_and_b32 s93, s92, 63\n\t" \
  "s_cbranch_scc1 .Lvr%=\n\t" \
  "buffer_load_dword v204, off, %[rs], %[sab] sc1\n\t" \
  "s_waitcnt vmcnt(0)\n\t" \
  "v_readfirstlane_b32 s93, v204\n\t" \
  "s_nop 3\n\t" \
  "s_cmp_eq_u32 s93, 1\n\t" \
  "s_cbranch_scc1 .Lvok%=\n\t" \
  "s_cmp_lt_u32 s92, 0x80000\n\t" \
  "s_cbranch_scc1 .Lvr%=\n\t" \
  "v_mov_b32 v204, 1\n\t" \
  "buffer_store_dword v204, off, %[rs], %[sab] sc1\n\t" \
  "s_branch .Lvok%=\n\t" \
  ".Lvr%=:\n\t" \
  "buffer_load_dword v252, v249, %[rs], %[sv] offen offset:0 sc1\n\t" \
  "buffer_load_dword v253, v249, %[rs], %[sv] offen offset:256 sc1\n\t" \
  "s_waitcnt vmcnt(0)\n\t" \
  "v_mfma_f32_16x16x16_bf16 v[224:227], v[252:253], v[232:233], 0\n\t" \
  "v_mfma_f32_16x16x16_bf16 v[228:231], v[252:253], v[234:235], 0\n\t" \
  "v_max_u32 v204, v252, v253\n\t" \
  "v_cmp_le_u32 vcc, %[sent], v204\n\t" \
  "s_nop 4\n\t" \
  "s_branch .Lpv%=\n\t" \
  "s_branch .Lend%=\n\t" \
  ".Lslow%=:\n\t" \
  "s_mov_b64 s[94:95], 0\n\t" \
  "v_cmp_eq_u32 vcc, %[sent], v200\n\t" \
  "s_or_b64 s[94:95], s[94:95], vcc\n\t" \
  "v_cmp_eq_u32 vcc, %[sent], v201\n\t" \
  "s_or_b64 s[94:95], s[94:95], vcc\n\t" \
  "v_cmp_eq_u32 vcc, %[sent], v202\n\t" \
  "s_or_b64 s[94:95], s[94:95], vcc\n\t" \
  "v_cmp_eq_u32 vcc, %[sent], v203\n\t" \
  "s_or_b64 s[94:95], s[94:95], vcc\n\t" \
  "v_cmp_eq_u32 vcc, %[sent], v204\n\t" \
  "s_or_b64 s[94:95], s[94:95], vcc\n\t" \
  "v_cmp_eq_u32 vcc, %[sent], v205\n\t" \
  "s_or_b64 s[94:95], s[94:95], vcc\n\t" \
  "v_cmp_eq_u32 vcc, %[sent], v206\n\t" \
  "s_or_b64 s[94:95], s[94:95], vcc\n\t" \
  "v_cmp_eq_u32 vcc, %[sent], v207\n\t" \
  "s_or_b64 s[94:95], s[94:95], vcc\n\t" \
  "s_cmp_eq_u64 s[94:95], 0\n\t" \
  "s_cbranch_scc1 .Lok%=\n\t" \
  "v_mov_b64 v[192:193], 0\n\t" \
  "v_mov_b64 v[194:195], 0\n\t" \
  "v_mov_b64 v[196:197], 0\n\t" \
  "v_mov_b64 v[198:199], 0\n\t" \
  "s_add_u32 s92, s92, 1\n\t" \
  "s_sleep 6\n\t" \
  "s_cmp_eq_u32 s92, 1\n\t" \
  "s_cbranch_scc1 .Lpoll%=\n\t" \
  "s_sleep 1\n\t" \
  "s_and_b32 s93, s92, 63\n\t" \
  "s_cmp_lg_u32 s93, 0\n\t" \
  "s_cbranch_scc1 .Lpoll%=\n\t" \
  "v_mov_b32 v245, 0\n\t" \
  "buffer_load_dword v244, v245, %[rs], %[sab] offen sc1\n\t" \
  "s_waitcnt vmcnt(0)\n\t" \
  "v_readfirstlane_b32 s93, v244\n\t" \
  "s_nop 3\n\t" \
  "s_cmp_eq_u32 s93, 1\n\t" \
  "s_cbranch_scc1 .Lok%=\n\t" \
  "s_cmp_lt_u32 s92, 0x80000\n\t" \
  "s_cbranch_scc1 .Lpoll%=\n\t" \
  "v_mov_b32 v244, 1\n\t" \
  "buffer_store_dword v244, v245, %[rs], %[sab] offen sc1\n\t" \
  "s_branch .Lok%=\n\t" \
  ".Lend%=:\n\t"
#define QK_ASM_1 \
  "s_waitcnt lgkmcnt(0)\n\t" \
  "v_subrev_u32 v244, 0x8000, v250\n\t" \
  "ds_read_b128 v[216:219], v248 offset:36864\n\t" \
  "ds_read_b128 v[220:223], v248 offset:37888\n\t" \
  "ds_read_b128 v[224:227], v248 offset:38912\n\t" \
  "ds_read_b128 v[228:231], v248 offset:39936\n\t" \
  "ds_read_b128 v[208:211], v244 offset:0\n\t" \
  "ds_read_b128 v[212:215], v244 offset:1024\n\t" \
  "ds_read_b128 v[236:239], v244 offset:2048\n\t" \
  "ds_read_b128 v[240:243], v244 offset:3072\n\t" \
  "v_mov_b64 v[192:193], 0\n\t" \
  "v_mov_b64 v[194:195], 0\n\t" \
  "v_mov_b64 v[196:197], 0\n\t" \
  "v_mov_b64 v[198:199], 0\n\t" \
  "s_movk_i32 s92, 0x2000\n\t" \
  ".Lwt%=:\n\t" \
  "ds_read_b64 v[200:201], v248 offset:30720\n\t" \
  "s_memrealtime s[90:91]\n\t" \
  "s_sub_u32 s92, s92, 1\n\t" \
  "s_waitcnt lgkmcnt(0)\n\t" \
  "v_readfirstlane_b32 s93, v200\n\t" \
  "v_readfirstlane_b32 %[tgt], v201\n\t" \
  "s_cbranch_scc1 .Lgo%=\n\t" \
  "s_cmp_eq_u32 s93, %[step]\n\t" \
  "s_cbranch_scc1 .Lwx%=\n\t" \
  "s_sleep 1\n\t" \
  "s_branch .Lwt%=\n\t" \
  ".Lwx%=:\n\t" \
  "s_lshl_b32 s93, s90, 2\n\t" \
  "s_sub_i32 s93, %[tgt], s93\n\t" \
  "s_cmp_le_i32 s93, 0\n\t" \
  "s_cbranch_scc1 .Lgo%=\n\t" \
  "s_min_i32 s93, s93, 8000\n\t" \
  "s_mov_b32 s94, s93\n\t" \
  ".Lsl%=:\n\t" \
  "s_sub_i32 s94, s94, 18\n\t" \
  "s_cmp_lt_i32 s94, 30\n\t" \
  "s_cbranch_scc1 .Lsx%=\n\t" \
  "s_sleep 1\n\t" \
  "s_branch .Lsl%=\n\t" \
  ".Lsx%=:\n\t" \
  "s_sub_i32 s93, s93, 12\n\t" \
  "s_max_i32 s93, s93, 4\n\t" \
  "s_lshr_b32 s93, s93, 2\n\t" \
  "s_add_u32 s93, s93, s90\n\t" \
  "s_memrealtime s[90:91]\n\t" \
  ".Lpace%=:\n\t" \
  "s_memrealtime s[94:95]\n\t" \
  "s_waitcnt lgkmcnt(1)\n\t" \
  "s_sub_i32 s92, s90, s93\n\t" \
  "s_cmp_ge_i32 s92, 0\n\t" \
  "s_cbranch_scc1 .Le%=\n\t" \
  "s_memrealtime s[90:91]\n\t" \
  "s_waitcnt lgkmcnt(1)\n\t" \
  "s_sub_i32 s92, s94, s93\n\t" \
  "s_cmp_lt_i32 s92, 0\n\t" \
  "s_cbranch_scc1 .Lpace%=\n\t" \
  "s_nop 4\n\t" \
  ".Le%=:\n\t" \
  "s_add_u32 s92, s92, s93\n\t" \
  "s_lshl_b32 s92, s92, 2\n\t" \
  "s_sub_i32 s92, %[tgt], s92\n\t" \
  "s_max_i32 s92, s92, 0\n\t" \
  "s_bitcmp0_b32 s92, 3\n\t" \
  "s_cbranch_scc1 .Lf3%=\n\t" \
  "s_nop 15\n\t" \
  ".Lf3%=:\n\t" \
  "s_bitcmp0_b32 s92, 2\n\t" \
  "s_cbranch_scc1 .Lf2%=\n\t" \
  "s_nop 10\n\t" \
  ".Lf2%=:\n\t" \
  ".Lgo%=:\n\t" \
  "s_mov_b32 s92, 0\n\t" \
  ".Lpoll%=:\n\t" \
  "buffer_load_dwordx4 v[200:203], v248, %[rs], %[som] offen sc1\n\t" \
  "buffer_load_dwordx4 v[204:207], v248, %[rs], %[som2] offen sc1\n\t" \
  "s_waitcnt vmcnt(1) lgkmcnt(0)\n\t" \
  "ds_write_b128 v249, v[200:203]\n\t" \
  "v_smfmac_f32_16x16x64_bf16 v[192:195], v[200:203], v[208:215], v251\n\t" \
  "v_max3_u32 v244, v200, v201, v202\n\t" \
  "v_smfmac_f32_16x16x64_bf16 v[196:199], v[200:203], v[236:243], v251\n\t" \
  "s_waitcnt vmcnt(0)\n\t" \
  "ds_write_b128 v249, v[204:207] offset:8192\n\t" \
  "v_smfmac_f32_16x16x64_bf16 v[192:195], v[204:207], v[216:223], v251\n\t" \
  "v_max3_u32 v244, v244, v203, v204\n\t" \
  "v_max3_u32 v244, v244, v205, v206\n\t" \
  "v_smfmac_f32_16x16x64_bf16 v[196:199], v[204:207], v[224:231], v251\n\t" \
  "v_max_u32 v244, v244, v207\n\t" \
  "v_cmp_le_u32 vcc, %[sent], v244\n\t" \
  "s_cbranch_vccnz .Lslow%=\n\t" \
  ".Lok%=:\n\t" \
  "ds_read_b128 v[216:219], v250 offset:32768\n\t" \
  "ds_read_b128 v[220:223], v250 offset:33792\n\t" \
  "ds_read_b128 v[224:227], v250 offset:34816\n\t" \
  "ds_read_b128 v[228:231], v250 offset:35840\n\t" \
  "s_waitcnt lgkmcnt(4)\n\t" \
  "s_barrier\n\t" \
  "ds_read_b128 v[208:211], v248 offset:0\n\t" \
  "ds_read_b128 v[212:215], v248 offset:3072\n\t" \
  "ds_read_b128 v[236:239], v248 offset:4096\n\t" \
  "ds_read_b128 v[240:243], v248 offset:5120\n\t" \
  "s_waitcnt lgkmcnt(3)\n\t" \
  "ds_read_b128 v[244:247], v248 offset:12288\n\t" \
  "v_smfmac_f32_16x16x64_bf16 v[192:195], v[208:211], v[216:223], v251\n\t" \
  "ds_read_b128 v[216:219], v250 offset:0\n\t" \
  "v_smfmac_f32_16x16x64_bf16 v[196:199], v[208:211], v[224:231], v251\n\t" \
  "s_waitcnt lgkmcnt(4)\n\t" \
  "ds_read_b128 v[220:223], v250 offset:1024\n\t" \
  "v_smfmac_f32_16x16x64_bf16 v[192:195], v[212:215], v[0:7], v251\n\t" \
  "ds_read_b128 v[224:227], v250 offset:2048\n\t" \
  "v_smfmac_f32_16x16x64_bf16 v[196:199], v[212:215], v[8:15], v251\n\t" \
  "s_waitcnt lgkmcnt(5)\n\t" \
  "ds_read_b128 v[228:231], v250 offset:3072\n\t" \
  "v_smfmac_f32_16x16x64_bf16 v[192:195], v[236:239], v[16:23], v251\n\t" \
  "ds_read_b128 v[200:203], v248 offset:6144\n\t" \
  "v_smfmac_f32_16x16x64_bf16 v[196:199], v[236:239], v[24:31], v251\n\t" \
  "s_waitcnt lgkmcnt(6)\n\t" \
  "ds_read_b128 v[204:207], v248 offset:7168\n\t" \
  "v_smfmac_f32_16x16x64_bf16 v[192:195], v[240:243], v[32:39], v251\n\t" \
  "ds_read_b128 v[208:211], v248 offset:8192\n\t" \
  "v_smfmac_f32_16x16x64_bf16 v[196:199], v[240:243], v[40:47], v251\n\t" \
  "s_waitcnt lgkmcnt(5)\n\t" \
  "ds_read_b128 v[212:215], v248 offset:10240\n\t" \
  "v_smfmac_f32_16x16x64_bf16 v[192:195], v[244:247], v[216:223], v251\n\t" \
  "s_waitcnt lgkmcnt(4)\n\t" \
  "v_smfmac_f32_16x16x64_bf16 v[196:199], v[244:247], v[224:231], v251\n\t" \
  "s_waitcnt lgkmcnt(3)\n\t" \
  "ds_read_b128 v[236:239], v248 offset:11264\n\t" \
  "v_smfmac_f32_16x16x64_bf16 v[192:195], v[200:203], v[48:55], v251\n\t" \
  "ds_read_b128 v[240:243], v248 offset:13312\n\t" \
  "v_smfmac_f32_16x16x64_bf16 v[196:199], v[200:203], v[56:63], v251\n\t" \
  "s_waitcnt lgkmcnt(4)\n\t" \
  "v_smfmac_f32_16x16x64_bf16 v[192:195], v[204:207], v[64:71], v251\n\t" \
  "ds_read_b128 v[244:247], v248 offset:14336\n\t" \
  "v_smfmac_f32_16x16x64_bf16 v[196:199], v[204:207], v[72:79], v251\n\t" \
  "s_waitcnt lgkmcnt(4)\n\t" \
  "v_smfmac_f32_16x16x64_bf16 v[192:195], v[208:211], v[80:87], v251\n\t" \
  "ds_read_b128 v[200:203], v248 offset:15360\n\t" \
  "v_smfmac_f32_16x16x64_bf16 v[196:199], v[208:211], v[88:95], v251\n\t" \
  "s_waitcnt lgkmcnt(4)\n\t" \
  "v_smfmac_f32_16x16x64_bf16 v[192:195], v[212:215], v[96:103], v251\n\t" \
  "ds_read_b128 v[204:207], v248 offset:2048\n\t" \
  "v_smfmac_f32_16x16x64_bf16 v[196:199], v[212:215], v[104:111], v251\n\t" \
  "s_waitcnt lgkmcnt(3)\n\t" \
  "v_smfmac_f32_16x16x64_bf16 v[192:195], v[236:239], v[112:119], v251\n\t" \
  "v_smfmac_f32_16x16x64_bf16 v[196:199], v[236:239], v[120:127], v251\n\t" \
  "v_smfmac_f32_16x16x64_bf16 v[192:195], v[240:243], v[128:135], v251\n\t" \
  "v_smfmac_f32_16x16x64_bf16 v[196:199], v[240:243], v[136:143], v251\n\t" \
  "s_waitcnt lgkmcnt(1)\n\t" \
  "v_smfmac_f32_16x16x64_bf16 v[192:195], v[244:247], v[144:151], v251\n\t" \
  "v_smfmac_f32_16x16x64_bf16 v[196:199], v[244:247], v[152:159], v251\n\t" \
  "v_smfmac_f32_16x16x64_bf16 v[192:195], v[200:203], v[160:167], v251\n\t" \
  "v_smfmac_f32_16x16x64_bf16 v[196:199], v[200:203], v[168:175], v251\n\t" \
  "s_waitcnt lgkmcnt(0)\n\t" \
  "v_smfmac_f32_16x16x64_bf16 v[192:195], v[204:207], v[176:183], v251\n\t" \
  "v_smfmac_f32_16x16x64_bf16 v[196:199], v[204:207], v[184:191], v251\n\t" \
  "ds_read_b128 v[200:203], v248 offset:17408\n\t" \
  "ds_read_b128 v[204:207], v248 offset:21504\n\t" \
  "v_mov_b32 v250, v232\n\t" \
  "v_mov_b32 v249, v233\n\t" \
  "s_nop 2\n\t" \
  "v_add_f32 v192, v192, v194\n\t" \
  "v_add_f32 v193, v193, v195\n\t" \
  "v_add_f32 v196, v196, v198\n\t" \
  "v_add_f32 v197, v197, v199\n\t" \
  "v_cvt_pk_bf16_f32 v192, v192, v193\n\t" \
  "v_cvt_pk_bf16_f32 v196, v196, v197\n\t" \
  "ds_write2_b32 v252, v192, v196 offset1:64\n\t" \
  "v_mov_b32 v251, v234\n\t" \
  "ds_read_b64_tr_b16 v[236:237], v250 offset:512\n\t" \
  "ds_read_b64_tr_b16 v[238:239], v250 offset:576\n\t" \
  "v_mov_b32 v212, %[step]\n\t" \
  "s_mov_b64 exec, 1\n\t" \
  "ds_write_b32 v248, v212 offset:28688\n\t" \
  "s_mov_b64 exec, -1\n\t" \
  "buffer_load_dwordx4 v[192:195], v248, %[rt], %[svt] offen offset:0 sc1\n\t" \
  "buffer_load_dwordx4 v[196:199], v248, %[rt], %[svt] offen offset:1024 sc1\n\t" \
  "s_waitcnt lgkmcnt(1)\n\t" \
  "v_mfma_f32_16x16x32_bf16 v[200:203], v[200:203], v[236:239], 0\n\t" \
  "v_mfma_f32_16x16x32_bf16 v[204:207], v[204:207], v[236:239], 0\n\t" \
  "s_nop 7\n\t" \
  "v_cndmask_b32 v216, v200, v204, %[nh]\n\t" \
  "v_cndmask_b32 v217, v201, v205, %[nh]\n\t" \
  "v_cndmask_b32 v218, v202, v206, %[nh]\n\t" \
  "v_cndmask_b32 v219, v203, v207, %[nh]\n\t" \
  "ds_write_b128 v248, v[216:219] offset:17408\n\t" \
  "buffer_load_dword v252, v249, %[rs], %[sv] offen offset:0 sc1\n\t" \
  "buffer_load_dword v253, v249, %[rs], %[sv] offen offset:256 sc1\n\t" \
  "ds_read_b32 v212, v248 offset:28672\n\t" \
  "ds_read_b64_tr_b16 v[240:241], v250 offset:1024\n\t" \
  "ds_read_b64_tr_b16 v[242:243], v250 offset:1088\n\t" \
  "ds_read_b64_tr_b16 v[244:245], v250 offset:1536\n\t" \
  "ds_read_b64_tr_b16 v[246:247], v250 offset:1600\n\t" \
  "ds_read_b64_tr_b16 v[232:233], v250 offset:0\n\t" \
  "ds_read_b64_tr_b16 v[234:235], v250 offset:64\n\t" \
  "s_setprio 3\n\t" \
  "s_waitcnt lgkmcnt(0)\n\t" \
  "v_cmp_ne_u32 vcc, %[step], v212\n\t" \
  "s_barrier\n\t" \
  "s_and_b32 s93, vcc_lo, 0xf\n\t" \
  "s_cbranch_scc1 .Lre%=\n\t" \
  ".Lrb%=:\n\t" \
  "ds_read_b128 v[220:223], v248 offset:18432\n\t" \
  "ds_read_b128 v[224:227], v248 offset:19456\n\t" \
  "ds_read_b128 v[228:231], v248 offset:16384\n\t" \
  "ds_read_b64_tr_b16 v[204:205], v250 offset:2560\n\t" \
  "ds_read_b64_tr_b16 v[206:207], v250 offset:2624\n\t" \
  "ds_read_b64_tr_b16 v[208:209], v250 offset:3072\n\t" \
  "ds_read_b64_tr_b16 v[210:211], v250 offset:3136\n\t" \
  "ds_read_b64_tr_b16 v[212:213], v250 offset:3584\n\t" \
  "ds_read_b64_tr_b16 v[214:215], v250 offset:3648\n\t" \
  "ds_read_b64_tr_b16 v[200:201], v250 offset:2048\n\t" \
  "ds_read_b64_tr_b16 v[202:203], v250 offset:2112\n\t" \
  "s_waitcnt lgkmcnt(10)\n\t" \
  "v_pk_add_f32 v[216:217], v[216:217], v[220:221]\n\t" \
  "v_pk_add_f32 v[218:219], v[218:219], v[222:223]\n\t" \
  "s_waitcnt lgkmcnt(8)\n\t" \
  "v_pk_add_f32 v[224:225], v[224:225], v[228:229]\n\t" \
  "v_pk_add_f32 v[226:227], v[226:227], v[230:231]\n\t" \
  "v_pk_add_f32 v[216:217], v[216:217], v[224:225]\n\t" \
  "v_pk_add_f32 v[218:219], v[218:219], v[226:227]\n\t" \
  "s_waitcnt lgkmcnt(6)\n\t" \
  "v_mfma_f32_16x16x32_bf16 v[204:207], v[204:207], v[236:239], 0\n\t" \
  "v_max3_f32 v224, v216, v217, v218\n\t" \
  "v_max_f32 v224, v224, v219\n\t" \
  "s_waitcnt lgkmcnt(4)\n\t" \
  "v_mfma_f32_16x16x32_bf16 v[204:207], v[208:211], v[240:243], v[204:207]\n\t" \
  "v_max_f32_dpp v225, v224, v224 row_ror:8 row_mask:0xf bank_mask:0xf\n\t" \
  "v_max_f32_dpp v226, v224, v224 row_ror:8 row_mask:0xf bank_mask:0xf\n\t" \
  "s_waitcnt lgkmcnt(2)\n\t" \
  "v_mfma_f32_16x16x32_bf16 v[204:207], v[212:215], v[244:247], v[204:207]\n\t" \
  "v_permlane32_swap_b32 v225, v226\n\t" \
  "s_waitcnt lgkmcnt(0)\n\t" \
  "v_mfma_f32_16x16x32_bf16 v[204:207], v[200:203], v[232:235], v[204:207]\n\t" \
  "v_max_f32 v231, v225, v226\n\t" \
  "v_mov_b32 v220, 0x3f803f80\n\t" \
  "v_mov_b32 v221, 0x3f803f80\n\t" \
  "v_mov_b64 v[222:223], v[220:221]\n\t" \
  "v_mov_b32 v237, 0\n\t" \
  "v_mov_b32 v238, 0\n\t" \
  "s_nop 1\n\t" \
  "v_max3_f32 v208, v204, v205, v231\n\t" \
  "v_max3_f32 v209, v208, v206, v207\n\t" \
  "v_max3_f32 v210, v208, v206, v207\n\t" \
  "v_cndmask_b32 v204, v204, v206, %[up]\n\t" \
  "v_cndmask_b32 v205, v205, v207, %[up]\n\t" \
  "v_permlane16_swap_b32 v209, v210\n\t" \
  "v_max_f32 v208, v209, v210\n\t" \
  "v_mul_f32 v208, %[negc], v208\n\t" \
  "v_fma_f32 v204, v204, %[c], v208\n\t" \
  "v_fma_f32 v205, v205, %[c], v208\n\t" \
  "v_exp_f32 v204, v204\n\t" \
  "v_fma_f32 v216, v216, %[c], v208\n\t" \
  "v_exp_f32 v205, v205\n\t" \
  "v_fma_f32 v217, v217, %[c], v208\n\t" \
  "v_exp_f32 v216, v216\n\t" \
  "v_fma_f32 v218, v218, %[c], v208\n\t" \
  "v_cvt_pk_bf16_f32 v236, v204, v205\n\t" \
  "v_cvt_pk_bf16_f32 v239, v204, v205\n\t" \
  "v_exp_f32 v217, v217\n\t" \
  "v_fma_f32 v219, v219, %[c], v208\n\t" \
  "v_mfma_f32_16x16x16_bf16 v[200:203], v[220:221], v[236:237], 0\n\t" \
  "v_exp_f32 v218, v218\n\t" \
  "s_waitcnt vmcnt(0)\n\t" \
  "v_mfma_f32_16x16x16_bf16 v[224:227], v[252:253], v[236:237], 0\n\t" \
  "v_cvt_pk_bf16_f32 v214, v216, v217\n\t" \
  "v_cvt_pk_bf16_f32 v212, v216, v217\n\t" \
  "v_exp_f32 v219, v219\n\t" \
  "v_mfma_f32_16x16x16_bf16 v[228:231], v[252:253], v[238:239], 0\n\t" \
  "v_cvt_pk_bf16_f32 v215, v218, v219\n\t" \
  "v_cvt_pk_bf16_f32 v213, v218, v219\n\t" \
  "v_mov_b32_dpp v212, v214 row_ror:8 row_mask:0xf bank_mask:0xc\n\t" \
  "v_mov_b32_dpp v214, v214 row_ror:8 row_mask:0xf bank_mask:0x3\n\t" \
  "v_mov_b32_dpp v213, v215 row_ror:8 row_mask:0xf bank_mask:0xc\n\t" \
  "v_mov_b32_dpp v215, v215 row_ror:8 row_mask:0xf bank_mask:0x3\n\t" \
  "v_max_u32 v208, v252, v253\n\t" \
  "v_cmp_le_u32 vcc, %[sent], v208\n\t" \
  "v_mfma_f32_16x16x32_bf16 v[200:203], v[220:223], v[212:215], v[200:203]\n\t" \
  ".Lpv%=:\n\t" \
  "v_mfma_f32_16x16x32_bf16 v[224:227], v[192:195], v[212:215], v[224:227]\n\t" \
  "v_mfma_f32_16x16x32_bf16 v[228:231], v[196:199], v[212:215], v[228:231]\n\t" \
  "s_cbranch_vccnz .Lvs%=\n\t" \
  ".Lvok%=:\n\t" \
  "s_nop 4\n\t" \
  "v_rcp_f32 v208, v200\n\t" \
  "v_cndmask_b32 v224, v224, v226, %[nh]\n\t" \
  "v_cndmask_b32 v225, v225, v227, %[nh]\n\t" \
  "v_cndmask_b32 v228, v228, v230, %[nh]\n\t" \
  "v_cndmask_b32 v229, v229, v231, %[nh]\n\t" \
  "v_pk_mul_f32 v[224:225], v[224:225], v[208:209] op_sel_hi:[1,0]\n\t" \
  "v_pk_mul_f32 v[228:229], v[228:229], v[208:209] op_sel_hi:[1,0]\n\t" \
  "v_cvt_pk_bf16_f32 v224, v224, v225\n\t" \
  "v_cvt_pk_bf16_f32 v225, v228, v229\n\t" \
  "buffer_store_dwordx2 v[224:225], v251, %[rs], %[so] offen sc1\n\t" \
  "s_memtime s[90:91]\n\t" \
  "s_setprio 0\n\t" \
  "s_waitcnt lgkmcnt(0)\n\t" \
  "s_mov_b32 %[tout], s90\n\t" \
  "s_mov_b32 %[spins], s92\n\t" \
  "s_branch .Lend%=\n\t" \
  ".Lre%=:\n\t" \
  "ds_read_b64_tr_b16 v[240:241], v250 offset:1024\n\t" \
  "ds_read_b64_tr_b16 v[242:243], v250 offset:1088\n\t" \
  "ds_read_b64_tr_b16 v[244:245], v250 offset:1536\n\t" \
  "ds_read_b64_tr_b16 v[246:247], v250 offset:1600\n\t" \
  "ds_read_b64_tr_b16 v[232:233], v250 offset:0\n\t" \
  "ds_read_b64_tr_b16 v[234:235], v250 offset:64\n\t" \
  "s_waitcnt lgkmcnt(0)\n\t" \
  "s_branch .Lrb%=\n\t" \
  ".Lvs%=:\n\t" \
  "v_cmp_eq_u32 vcc, %[sent], v252\n\t" \
  "v_cmp_eq_u32 s[94:95], %[sent], v253\n\t" \
  "s_or_b64 s[94:95], s[94:95], vcc\n\t" \
  "s_cbranch_scc0 .Lvok%=\n\t" \
  "s_add_u32 s92, s92, 1\n\t" \
  "s_sleep 2\n\t" \
  "s_and_b32 s93, s92, 63\n\t" \
  "s_cbranch_scc1 .Lvr%=\n\t" \
  "buffer_load_dword v208, off, %[rs], %[sab] sc1\n\t" \
  "s_waitcnt vmcnt(0)\n\t" \
  "v_readfirstlane_b32 s93, v208\n\t" \
  "s_nop 3\n\t" \
  "s_cmp_eq_u32 s93, 1\n\t" \
  "s_cbranch_scc1 .Lvok%=\n\t" \
  "s_cmp_lt_u32 s92, 0x80000\n\t" \
  "s_cbranch_scc1 .Lvr%=\n\t" \
  "v_mov_b32 v208, 1\n\t" \
  "buffer_store_dword v208, off, %[rs], %[sab] sc1\n\t" \
  "s_branch .Lvok%=\n\t" \
  ".Lvr%=:\n\t" \
  "buffer_load_dword v252, v249, %[rs], %[sv] offen offset:0 sc1\n\t" \
  "buffer_load_dword v253, v249, %[rs], %[sv] offen offset:256 sc1\n\t" \
  "s_waitcnt vmcnt(0)\n\t" \
  "v_mfma_f32_16x16x16_bf16 v[224:227], v[252:253], v[236:237], 0\n\t" \
  "v_mfma_f32_16x16x16_bf16 v[228:231], v[252:253], v[238:239], 0\n\t" \
  "v_max_u32 v208, v252, v253\n\t" \
  "v_cmp_le_u32 vcc, %[sent], v208\n\t" \
  "s_nop 4\n\t" \
  "s_branch .Lpv%=\n\t" \
  "s_branch .Lend%=\n\t" \
  ".Lslow%=:\n\t" \
  "s_mov_b64 s[94:95], 0\n\t" \
  "v_cmp_eq_u32 vcc, %[sent], v200\n\t" \
  "s_or_b64 s[94:95], s[94:95], vcc\n\t" \
  "v_cmp_eq_u32 vcc, %[sent], v201\n\t" \
  "s_or_b64 s[94:95], s[94:95], vcc\n\t" \
  "v_cmp_eq_u32 vcc, %[sent], v202\n\t" \
  "s_or_b64 s[94:95], s[94:95], vcc\n\t" \
  "v_cmp_eq_u32 vcc, %[sent], v203\n\t" \
  "s_or_b64 s[94:95], s[94:95], vcc\n\t" \
  "v_cmp_eq_u32 vcc, %[sent], v204\n\t" \
  "s_or_b64 s[94:95], s[94:95], vcc\n\t" \
  "v_cmp_eq_u32 vcc, %[sent], v205\n\t" \
  "s_or_b64 s[94:95], s[94:95], vcc\n\t" \
  "v_cmp_eq_u32 vcc, %[sent], v206\n\t" \
  "s_or_b64 s[94:95], s[94:95], vcc\n\t" \
  "v_cmp_eq_u32 vcc, %[sent], v207\n\t" \
  "s_or_b64 s[94:95], s[94:95], vcc\n\t" \
  "s_cmp_eq_u64 s[94:95], 0\n\t" \
  "s_cbranch_scc1 .Lok%=\n\t" \
  "v_mov_b64 v[192:193], 0\n\t" \
  "v_mov_b64 v[194:195], 0\n\t" \
  "v_mov_b64 v[196:197], 0\n\t" \
  "v_mov_b64 v[198:199], 0\n\t" \
  "s_add_u32 s92, s92, 1\n\t" \
  "s_sleep 6\n\t" \
  "s_cmp_eq_u32 s92, 1\n\t" \
  "s_cbranch_scc1 .Lpoll%=\n\t" \
  "s_sleep 1\n\t" \
  "s_and_b32 s93, s92, 63\n\t" \
  "s_cmp_lg_u32 s93, 0\n\t" \
  "s_cbranch_scc1 .Lpoll%=\n\t" \
  "v_mov_b32 v245, 0\n\t" \
  "buffer_load_dword v244, v245, %[rs], %[sab] offen sc1\n\t" \
  "s_waitcnt vmcnt(0)\n\t" \
  "v_readfirstlane_b32 s93, v244\n\t" \
  "s_nop 3\n\t" \
  "s_cmp_eq_u32 s93, 1\n\t" \
  "s_cbranch_scc1 .Lok%=\n\t" \
  "s_cmp_lt_u32 s92, 0x80000\n\t" \
  "s_cbranch_scc1 .Lpoll%=\n\t" \
  "v_mov_b32 v244, 1\n\t" \
  "buffer_store_dword v244, v245, %[rs], %[sab] offen sc1\n\t" \
  "s_branch .Lok%=\n\t" \
  ".Lend%=:\n\t"
#define QK_ASM_2 \
  "s_waitcnt lgkmcnt(0)\n\t" \
  "v_subrev_u32 v244, 0x8000, v250\n\t" \
  "ds_read_b128 v[216:219], v248 offset:40960\n\t" \
  "ds_read_b128 v[220:223], v248 offset:41984\n\t" \
  "ds_read_b128 v[224:227], v248 offset:43008\n\t" \
  "ds_read_b128 v[228:231], v248 offset:44032\n\t" \
  "ds_read_b128 v[208:211], v244 offset:0\n\t" \
  "ds_read_b128 v[212:215], v244 offset:1024\n\t" \
  "ds_read_b128 v[236:239], v244 offset:2048\n\t" \
  "ds_read_b128 v[240:243], v244 offset:3072\n\t" \
  "v_mov_b64 v[192:193], 0\n\t" \
  "v_mov_b64 v[194:195], 0\n\t" \
  "v_mov_b64 v[196:197], 0\n\t" \
  "v_mov_b64 v[198:199], 0\n\t" \
  "s_movk_i32 s92, 0x2000\n\t" \
  ".Lwt%=:\n\t" \
  "ds_read_b64 v[200:201], v248 offset:30720\n\t" \
  "s_memrealtime s[90:91]\n\t" \
  "s_sub_u32 s92, s92, 1\n\t" \
  "s_waitcnt lgkmcnt(0)\n\t" \
  "v_readfirstlane_b32 s93, v200\n\t" \
  "v_readfirstlane_b32 %[tgt], v201\n\t" \
  "s_cbranch_scc1 .Lgo%=\n\t" \
  "s_cmp_eq_u32 s93, %[step]\n\t" \
  "s_cbranch_scc1 .Lwx%=\n\t" \
  "s_sleep 1\n\t" \
  "s_branch .Lwt%=\n\t" \
  ".Lwx%=:\n\t" \
  "s_lshl_b32 s93, s90, 2\n\t" \
  "s_sub_i32 s93, %[tgt], s93\n\t" \
  "s_cmp_le_i32 s93, 0\n\t" \
  "s_cbranch_scc1 .Lgo%=\n\t" \
  "s_min_i32 s93, s93, 8000\n\t" \
  "s_mov_b32 s94, s93\n\t" \
  ".Lsl%=:\n\t" \
  "s_sub_i32 s94, s94, 18\n\t" \
  "s_cmp_lt_i32 s94, 30\n\t" \
  "s_cbranch_scc1 .Lsx%=\n\t" \
  "s_sleep 1\n\t" \
  "s_branch .Lsl%=\n\t" \
  ".Lsx%=:\n\t" \
  "s_sub_i32 s93, s93, 12\n\t" \
  "s_max_i32 s93, s93, 4\n\t" \
  "s_lshr_b32 s93, s93, 2\n\t" \
  "s_add_u32 s93, s93, s90\n\t" \
  "s_memrealtime s[90:91]\n\t" \
  ".Lpace%=:\n\t" \
  "s_memrealtime s[94:95]\n\t" \
  "s_waitcnt lgkmcnt(1)\n\t" \
  "s_sub_i32 s92, s90, s93\n\t" \
  "s_cmp_ge_i32 s92, 0\n\t" \
  "s_cbranch_scc1 .Le%=\n\t" \
  "s_memrealtime s[90:91]\n\t" \
  "s_waitcnt lgkmcnt(1)\n\t" \
  "s_sub_i32 s92, s94, s93\n\t" \
  "s_cmp_lt_i32 s92, 0\n\t" \
  "s_cbranch_scc1 .Lpace%=\n\t" \
  "s_nop 4\n\t" \
  ".Le%=:\n\t" \
  "s_add_u32 s92, s92, s93\n\t" \
  "s_lshl_b32 s92, s92, 2\n\t" \
  "s_sub_i32 s92, %[tgt], s92\n\t" \
  "s_max_i32 s92, s92, 0\n\t" \
  "s_bitcmp0_b32 s92, 3\n\t" \
  "s_cbranch_scc1 .Lf3%=\n\t" \
  "s_nop 15\n\t" \
  ".Lf3%=:\n\t" \
  "s_bitcmp0_b32 s92, 2\n\t" \
  "s_cbranch_scc1 .Lf2%=\n\t" \
  "s_nop 10\n\t" \
  ".Lf2%=:\n\t" \
  ".Lgo%=:\n\t" \
  "s_mov_b32 s92, 0\n\t" \
  ".Lpoll%=:\n\t" \
  "buffer_load_dwordx4 v[200:203], v248, %[rs], %[som] offen sc1\n\t" \
  "buffer_load_dwordx4 v[204:207], v248, %[rs], %[som2] offen sc1\n\t" \
  "s_waitcnt vmcnt(1) lgkmcnt(0)\n\t" \
  "ds_write_b128 v249, v[200:203]\n\t" \
  "v_smfmac_f32_16x16x64_bf16 v[192:195], v[200:203], v[208:215], v251\n\t" \
  "v_max3_u32 v244, v200, v201, v202\n\t" \
  "v_smfmac_f32_16x16x64_bf16 v[196:199], v[200:203], v[236:243], v251\n\t" \
  "s_waitcnt vmcnt(0)\n\t" \
  "ds_write_b128 v249, v[204:207] offset:8192\n\t" \
  "v_smfmac_f32_16x16x64_bf16 v[192:195], v[204:207], v[216:223], v251\n\t" \
  "v_max3_u32 v244, v244, v203, v204\n\t" \
  "v_max3_u32 v244, v244, v205, v206\n\t" \
  "v_smfmac_f32_16x16x64_bf16 v[196:199], v[204:207], v[224:231], v251\n\t" \
  "v_max_u32 v244, v244, v207\n\t" \
  "v_cmp_le_u32 vcc, %[sent], v244\n\t" \
  "s_cbranch_vccnz .Lslow%=\n\t" \
  ".Lok%=:\n\t" \
  "ds_read_b128 v[216:219], v250 offset:32768\n\t" \
  "ds_read_b128 v[220:223], v250 offset:33792\n\t" \
  "ds_read_b128 v[224:227], v250 offset:34816\n\t" \
  "ds_read_b128 v[228:231], v250 offset:35840\n\t" \
  "s_waitcnt lgkmcnt(4)\n\t" \
  "s_barrier\n\t" \
  "ds_read_b128 v[208:211], v248 offset:3072\n\t" \
  "ds_read_b128 v[212:215], v248 offset:4096\n\t" \
  "ds_read_b128 v[236:239], v248 offset:5120\n\t" \
  "ds_read_b128 v[240:243], v248 offset:6144\n\t" \
  "s_waitcnt lgkmcnt(3)\n\t" \
  "ds_read_b128 v[244:247], v248 offset:13312\n\t" \
  "v_smfmac_f32_16x16x64_bf16 v[192:195], v[208:211], v[216:223], v251\n\t" \
  "ds_read_b128 v[216:219], v250 offset:0\n\t" \
  "v_smfmac_f32_16x16x64_bf16 v[196:199], v[208:211], v[224:231], v251\n\t" \
  "s_waitcnt lgkmcnt(4)\n\t" \
  "ds_read_b128 v[220:223], v250 offset:1024\n\t" \
  "v_smfmac_f32_16x16x64_bf16 v[192:195], v[212:215], v[0:7], v251\n\t" \
  "ds_read_b128 v[224:227], v250 offset:2048\n\t" \
  "v_smfmac_f32_16x16x64_bf16 v[196:199], v[212:215], v[8:15], v251\n\t" \
  "s_waitcnt lgkmcnt(5)\n\t" \
  "ds_read_b128 v[228:231], v250 offset:3072\n\t" \
  "v_smfmac_f32_16x16x64_bf16 v[192:195], v[236:239], v[16:23], v251\n\t" \
  "ds_read_b128 v[200:203], v248 offset:7168\n\t" \
  "v_smfmac_f32_16x16x64_bf16 v[196:199], v[236:239], v[24:31], v251\n\t" \
  "s_waitcnt lgkmcnt(6)\n\t" \
  "ds_read_b128 v[204:207], v248 offset:8192\n\t" \
  "v_smfmac_f32_16x16x64_bf16 v[192:195], v[240:243], v[32:39], v251\n\t" \
  "ds_read_b128 v[208:211], v248 offset:9216\n\t" \
  "v_smfmac_f32_16x16x64_bf16 v[196:199], v[240:243], v[40:47], v251\n\t" \
  "s_waitcnt lgkmcnt(5)\n\t" \
  "ds_read_b128 v[212:215], v248 offset:11264\n\t" \
  "v_smfmac_f32_16x16x64_bf16 v[192:195], v[244:247], v[216:223], v251\n\t" \
  "s_waitcnt lgkmcnt(4)\n\t" \
  "v_smfmac_f32_16x16x64_bf16 v[196:199], v[244:247], v[224:231], v251\n\t" \
  "s_waitcnt lgkmcnt(3)\n\t" \
  "ds_read_b128 v[236:239], v248 offset:12288\n\t" \
  "v_smfmac_f32_16x16x64_bf16 v[192:195], v[200:203], v[48:55], v251\n\t" \
  "ds_read_b128 v[240:243], v248 offset:14336\n\t" \
  "v_smfmac_f32_16x16x64_bf16 v[196:199], v[200:203], v[56:63], v251\n\t" \
  "s_waitcnt lgkmcnt(4)\n\t" \
  "v_smfmac_f32_16x16x64_bf16 v[192:195], v[204:207], v[64:71], v251\n\t" \
  "ds_read_b128 v[244:247], v248 offset:15360\n\t" \
  "v_smfmac_f32_16x16x64_bf16 v[196:199], v[204:207], v[72:79], v251\n\t" \
  "s_waitcnt lgkmcnt(4)\n\t" \
  "v_smfmac_f32_16x16x64_bf16 v[192:195], v[208:211], v[80:87], v251\n\t" \
  "ds_read_b128 v[200:203], v248 offset:0\n\t" \
  "v_smfmac_f32_16x16x64_bf16 v[196:199], v[208:211], v[88:95], v251\n\t" \
  "s_waitcnt lgkmcnt(4)\n\t" \
  "v_smfmac_f32_16x16x64_bf16 v[192:195], v[212:215], v[96:103], v251\n\t" \
  "ds_read_b128 v[204:207], v248 offset:1024\n\t" \
  "v_smfmac_f32_16x16x64_bf16 v[196:199], v[212:215], v[104:111], v251\n\t" \
  "s_waitcnt lgkmcnt(3)\n\t" \
  "v_smfmac_f32_16x16x64_bf16 v[192:195], v[236:239], v[112:119], v251\n\t" \
  "v_smfmac_f32_16x16x64_bf16 v[196:199], v[236:239], v[120:127], v251\n\t" \
  "v_smfmac_f32_16x16x64_bf16 v[192:195], v[240:243], v[128:135], v251\n\t" \
  "v_smfmac_f32_16x16x64_bf16 v[196:199], v[240:243], v[136:143], v251\n\t" \
  "s_waitcnt lgkmcnt(1)\n\t" \
  "v_smfmac_f32_16x16x64_bf16 v[192:195], v[244:247], v[144:151], v251\n\t" \
  "v_smfmac_f32_16x16x64_bf16 v[196:199], v[244:247], v[152:159], v251\n\t" \
  "v_smfmac_f32_16x16x64_bf16 v[192:195], v[200:203], v[160:167], v251\n\t" \
  "v_smfmac_f32_16x16x64_bf16 v[196:199], v[200:203], v[168:175], v251\n\t" \
  "s_waitcnt lgkmcnt(0)\n\t" \
  "v_smfmac_f32_16x16x64_bf16 v[192:195], v[204:207], v[176:183], v251\n\t" \
  "v_smfmac_f32_16x16x64_bf16 v[196:199], v[204:207], v[184:191], v251\n\t" \
  "ds_read_b128 v[200:203], v248 offset:18432\n\t" \
  "ds_read_b128 v[204:207], v248 offset:22528\n\t" \
  "v_mov_b32 v250, v232\n\t" \
  "v_mov_b32 v249, v233\n\t" \
  "s_nop 2\n\t" \
  "v_add_f32 v192, v192, v194\n\t" \
  "v_add_f32 v193, v193, v195\n\t" \
  "v_add_f32 v196, v196, v198\n\t" \
  "v_add_f32 v197, v197, v199\n\t" \
  "v_cvt_pk_bf16_f32 v192, v192, v193\n\t" \
  "v_cvt_pk_bf16_f32 v196, v196, v197\n\t" \
  "ds_write2_b32 v252, v192, v196 offset1:64\n\t" \
  "v_mov_b32 v251, v234\n\t" \
  "ds_read_b64_tr_b16 v[240:241], v250 offset:1024\n\t" \
  "ds_read_b64_tr_b16 v[242:243], v250 offset:1088\n\t" \
  "v_mov_b32 v212, %[step]\n\t" \
  "s_mov_b64 exec, 1\n\t" \
  "ds_write_b32 v248, v212 offset:28704\n\t" \
  "s_mov_b64 exec, -1\n\t" \
  "buffer_load_dwordx4 v[192:195], v248, %[rt], %[svt] offen offset:0 sc1\n\t" \
  "buffer_load_dwordx4 v[196:199], v248, %[rt], %[svt] offen offset:1024 sc1\n\t" \
  "s_waitcnt lgkmcnt(1)\n\t" \
  "v_mfma_f32_16x16x32_bf16 v[200:203], v[200:203], v[240:243], 0\n\t" \
  "v_mfma_f32_16x16x32_bf16 v[204:207], v[204:207], v[240:243], 0\n\t" \
  "s_nop 7\n\t" \
  "v_cndmask_b32 v216, v200, v204, %[nh]\n\t" \
  "v_cndmask_b32 v217, v201, v205, %[nh]\n\t" \
  "v_cndmask_b32 v218, v202, v206, %[nh]\n\t" \
  "v_cndmask_b32 v219, v203, v207, %[nh]\n\t" \
  "ds_write_b128 v248, v[216:219] offset:18432\n\t" \
  "buffer_load_dword v252, v249, %[rs], %[sv] offen offset:0 sc1\n\t" \
  "buffer_load_dword v253, v249, %[rs], %[sv] offen offset:256 sc1\n\t" \
  "ds_read_b32 v212, v248 offset:28672\n\t" \
  "ds_read_b64_tr_b16 v[244:245], v250 offset:1536\n\t" \
  "ds_read_b64_tr_b16 v[246:247], v250 offset:1600\n\t" \
  "ds_read_b64_tr_b16 v[232:233], v250 offset:0\n\t" \
  "ds_read_b64_tr_b16 v[234:235], v250 offset:64\n\t" \
  "ds_read_b64_tr_b16 v[236:237], v250 offset:512\n\t" \
  "ds_read_b64_tr_b16 v[238:239], v250 offset:576\n\t" \
  "s_setprio 3\n\t" \
  "s_waitcnt lgkmcnt(0)\n\t" \
  "v_cmp_ne_u32 vcc, %[step], v212\n\t" \
  "s_barrier\n\t" \
  "s_and_b32 s93, vcc_lo, 0xf\n\t" \
  "s_cbranch_scc1 .Lre%=\n\t" \
  ".Lrb%=:\n\t" \
  "ds_read_b128 v[220:223], v248 offset:19456\n\t" \
  "ds_read_b128 v[224:227], v248 offset:16384\n\t" \
  "ds_read_b128 v[228:231], v248 offset:17408\n\t" \
  "ds_read_b64_tr_b16 v[208:209], v250 offset:3072\n\t" \
  "ds_read_b64_tr_b16 v[210:211], v250 offset:3136\n\t" \
  "ds_read_b64_tr_b16 v[212:213], v250 offset:3584\n\t" \
  "ds_read_b64_tr_b16 v[214:215], v250 offset:3648\n\t" \
  "ds_read_b64_tr_b16 v[200:201], v250 offset:2048\n\t" \
  "ds_read_b64_tr_b16 v[202:203], v250 offset:2112\n\t" \
  "ds_read_b64_tr_b16 v[204:205], v250 offset:2560\n\t" \
  "ds_read_b64_tr_b16 v[206:207], v250 offset:2624\n\t" \
  "s_waitcnt lgkmcnt(10)\n\t" \
  "v_pk_add_f32 v[216:217], v[216:217], v[220:221]\n\t" \
  "v_pk_add_f32 v[218:219], v[218:219], v[222:223]\n\t" \
  "s_waitcnt lgkmcnt(8)\n\t" \
  "v_pk_add_f32 v[224:225], v[224:225], v[228:229]\n\t" \
  "v_pk_add_f32 v[226:227], v[226:227], v[230:231]\n\t" \
  "v_pk_add_f32 v[216:217], v[216:217], v[224:225]\n\t" \
  "v_pk_add_f32 v[218:219], v[218:219], v[226:227]\n\t" \
  "s_waitcnt lgkmcnt(6)\n\t" \
  "v_mfma_f32_16x16x32_bf16 v[208:211], v[208:211], v[240:243], 0\n\t" \
  "v_max3_f32 v224, v216, v217, v218\n\t" \
  "v_max_f32 v224, v224, v219\n\t" \
  "s_waitcnt lgkmcnt(4)\n\t" \
  "v_mfma_f32_16x16x32_bf16 v[208:211], v[212:215], v[244:247], v[208:211]\n\t" \
  "v_max_f32_dpp v225, v224, v224 row_ror:8 row_mask:0xf bank_mask:0xf\n\t" \
  "v_max_f32_dpp v226, v224, v224 row_ror:8 row_mask:0xf bank_mask:0xf\n\t" \
  "s_waitcnt lgkmcnt(2)\n\t" \
  "v_mfma_f32_16x16x32_bf16 v[208:211], v[200:203], v[232:235], v[208:211]\n\t" \
  "v_permlane32_swap_b32 v225, v226\n\t" \
  "s_waitcnt lgkmcnt(0)\n\t" \
  "v_mfma_f32_16x16x32_bf16 v[208:211], v[204:207], v[236:239], v[208:211]\n\t" \
  "v_max_f32 v231, v225, v226\n\t" \
  "v_mov_b32 v220, 0x3f803f80\n\t" \
  "v_mov_b32 v221, 0x3f803f80\n\t" \
  "v_mov_b64 v[222:223], v[220:221]\n\t" \
  "v_mov_b32 v241, 0\n\t" \
  "v_mov_b32 v242, 0\n\t" \
  "s_nop 1\n\t" \
  "v_max3_f32 v212, v208, v209, v231\n\t" \
  "v_max3_f32 v213, v212, v210, v211\n\t" \
  "v_max3_f32 v214, v212, v210, v211\n\t" \
  "v_cndmask_b32 v208, v208, v210, %[up]\n\t" \
  "v_cndmask_b32 v209, v209, v211, %[up]\n\t" \
  "v_permlane16_swap_b32 v213, v214\n\t" \
  "v_max_f32 v212, v213, v214\n\t" \
  "v_mul_f32 v212, %[negc], v212\n\t" \
  "v_fma_f32 v208, v208, %[c], v212\n\t" \
  "v_fma_f32 v209, v209, %[c], v212\n\t" \
  "v_exp_f32 v208, v208\n\t" \
  "v_fma_f32 v216, v216, %[c], v212\n\t" \
  "v_exp_f32 v209, v209\n\t" \
  "v_fma_f32 v217, v217, %[c], v212\n\t" \
  "v_exp_f32 v216, v216\n\t" \
  "v_fma_f32 v218, v218, %[c], v212\n\t" \
  "v_cvt_pk_bf16_f32 v240, v208, v209\n\t" \
  "v_cvt_pk_bf16_f32 v243, v208, v209\n\t" \
  "v_exp_f32 v217, v217\n\t" \
  "v_fma_f32 v219, v219, %[c], v212\n\t" \
  "v_mfma_f32_16x16x16_bf16 v[204:207], v[220:221], v[240:241], 0\n\t" \
  "v_exp_f32 v218, v218\n\t" \
  "s_waitcnt vmcnt(0)\n\t" \
  "v_mfma_f32_16x16x16_bf16 v[224:227], v[252:253], v[240:241], 0\n\t" \
  "v_cvt_pk_bf16_f32 v202, v216, v217\n\t" \
  "v_cvt_pk_bf16_f32 v200, v216, v217\n\t" \
  "v_exp_f32 v219, v219\n\t" \
  "v_mfma_f32_16x16x16_bf16 v[228:231], v[252:253], v[242:243], 0\n\t" \
  "v_cvt_pk_bf16_f32 v203, v218, v219\n\t" \
  "v_cvt_pk_bf16_f32 v201, v218, v219\n\t" \
  "v_mov_b32_dpp v200, v202 row_ror:8 row_mask:0xf bank_mask:0xc\n\t" \
  "v_mov_b32_dpp v202, v202 row_ror:8 row_mask:0xf bank_mask:0x3\n\t" \
  "v_mov_b32_dpp v201, v203 row_ror:8 row_mask:0xf bank_mask:0xc\n\t" \
  "v_mov_b32_dpp v203, v203 row_ror:8 row_mask:0xf bank_mask:0x3\n\t" \
  "v_max_u32 v212, v252, v253\n\t" \
  "v_cmp_le_u32 vcc, %[sent], v212\n\t" \
  "v_mfma_f32_16x16x32_bf16 v[204:207], v[220:223], v[200:203], v[204:207]\n\t" \
  ".Lpv%=:\n\t" \
  "v_mfma_f32_16x16x32_bf16 v[224:227], v[192:195], v[200:203], v[224:227]\n\t" \
  "v_mfma_f32_16x16x32_bf16 v[228:231], v[196:199], v[200:203], v[228:231]\n\t" \
  "s_cbranch_vccnz .Lvs%=\n\t" \
  ".Lvok%=:\n\t" \
  "s_nop 4\n\t" \
  "v_rcp_f32 v212, v204\n\t" \
  "v_cndmask_b32 v224, v224, v226, %[nh]\n\t" \
  "v_cndmask_b32 v225, v225, v227, %[nh]\n\t" \
  "v_cndmask_b32 v228, v228, v230, %[nh]\n\t" \
  "v_cndmask_b32 v229, v229, v231, %[nh]\n\t" \
  "v_pk_mul_f32 v[224:225], v[224:225], v[212:213] op_sel_hi:[1,0]\n\t" \
  "v_pk_mul_f32 v[228:229], v[228:229], v[212:213] op_sel_hi:[1,0]\n\t" \
  "v_cvt_pk_bf16_f32 v224, v224, v225\n\t" \
  "v_cvt_pk_bf16_f32 v225, v228, v229\n\t" \
  "buffer_store_dwordx2 v[224:225], v251, %[rs], %[so] offen sc1\n\t" \
  "s_memtime s[90:91]\n\t" \
  "s_setprio 0\n\t" \
  "s_waitcnt lgkmcnt(0)\n\t" \
  "s_mov_b32 %[tout], s90\n\t" \
  "s_mov_b32 %[spins], s92\n\t" \
  "s_branch .Lend%=\n\t" \
  ".Lre%=:\n\t" \
  "ds_read_b64_tr_b16 v[244:245], v250 offset:1536\n\t" \
  "ds_read_b64_tr_b16 v[246:247], v250 offset:1600\n\t" \
  "ds_read_b64_tr_b16 v[232:233], v250 offset:0\n\t" \
  "ds_read_b64_tr_b16 v[234:235], v250 offset:64\n\t" \
  "ds_read_b64_tr_b16 v[236:237], v250 offset:512\n\t" \
  "ds_read_b64_tr_b16 v[238:239], v250 offset:576\n\t" \
  "s_waitcnt lgkmcnt(0)\n\t" \
  "s_branch .Lrb%=\n\t" \
  ".Lvs%=:\n\t" \
  "v_cmp_eq_u32 vcc, %[sent], v252\n\t" \
  "v_cmp_eq_u32 s[94:95], %[sent], v253\n\t" \
  "s_or_b64 s[94:95], s[94:95], vcc\n\t" \
  "s_cbranch_scc0 .Lvok%=\n\t" \
  "s_add_u32 s92, s92, 1\n\t" \
  "s_sleep 2\n\t" \
  "s_and_b32 s93, s92, 63\n\t" \
  "s_cbranch_scc1 .Lvr%=\n\t" \
  "buffer_load_dword v212, off, %[rs], %[sab] sc1\n\t" \
  "s_waitcnt vmcnt(0)\n\t" \
  "v_readfirstlane_b32 s93, v212\n\t" \
  "s_nop 3\n\t" \
  "s_cmp_eq_u32 s93, 1\n\t" \
  "s_cbranch_scc1 .Lvok%=\n\t" \
  "s_cmp_lt_u32 s92, 0x80000\n\t" \
  "s_cbranch_scc1 .Lvr%=\n\t" \
  "v_mov_b32 v212, 1\n\t" \
  "buffer_store_dword v212, off, %[rs], %[sab] sc1\n\t" \
  "s_branch .Lvok%=\n\t" \
  ".Lvr%=:\n\t" \
  "buffer_load_dword v252, v249, %[rs], %[sv] offen offset:0 sc1\n\t" \
  "buffer_load_dword v253, v249, %[rs], %[sv] offen offset:256 sc1\n\t" \
  "s_waitcnt vmcnt(0)\n\t" \
  "v_mfma_f32_16x16x16_bf16 v[224:227], v[252:253], v[240:241], 0\n\t" \
  "v_mfma_f32_16x16x16_bf16 v[228:231], v[252:253], v[242:243], 0\n\t" \
  "v_max_u32 v212, v252, v253\n\t" \
  "v_cmp_le_u32 vcc, %[sent], v212\n\t" \
  "s_nop 4\n\t" \
  "s_branch .Lpv%=\n\t" \
  "s_branch .Lend%=\n\t" \
  ".Lslow%=:\n\t" \
  "s_mov_b64 s[94:95], 0\n\t" \
  "v_cmp_eq_u32 vcc, %[sent], v200\n\t" \
  "s_or_b64 s[94:95], s[94:95], vcc\n\t" \
  "v_cmp_eq_u32 vcc, %[sent], v201\n\t" \
  "s_or_b64 s[94:95], s[94:95], vcc\n\t" \
  "v_cmp_eq_u32 vcc, %[sent], v202\n\t" \
  "s_or_b64 s[94:95], s[94:95], vcc\n\t" \
  "v_cmp_eq_u32 vcc, %[sent], v203\n\t" \
  "s_or_b64 s[94:95], s[94:95], vcc\n\t" \
  "v_cmp_eq_u32 vcc, %[sent], v204\n\t" \
  "s_or_b64 s[94:95], s[94:95], vcc\n\t" \
  "v_cmp_eq_u32 vcc, %[sent], v205\n\t" \
  "s_or_b64 s[94:95], s[94:95], vcc\n\t" \
  "v_cmp_eq_u32 vcc, %[sent], v206\n\t" \
  "s_or_b64 s[94:95], s[94:95], vcc\n\t" \
  "v_cmp_eq_u32 vcc, %[sent], v207\n\t" \
  "s_or_b64 s[94:95], s[94:95], vcc\n\t" \
  "s_cmp_eq_u64 s[94:95], 0\n\t" \
  "s_cbranch_scc1 .Lok%=\n\t" \
  "v_mov_b64 v[192:193], 0\n\t" \
  "v_mov_b64 v[194:195], 0\n\t" \
  "v_mov_b64 v[196:197], 0\n\t" \
  "v_mov_b64 v[198:199], 0\n\t" \
  "s_add_u32 s92, s92, 1\n\t" \
  "s_sleep 6\n\t" \
  "s_cmp_eq_u32 s92, 1\n\t" \
  "s_cbranch_scc1 .Lpoll%=\n\t" \
  "s_sleep 1\n\t" \
  "s_and_b32 s93, s92, 63\n\t" \
  "s_cmp_lg_u32 s93, 0\n\t" \
  "s_cbranch_scc1 .Lpoll%=\n\t" \
  "v_mov_b32 v245, 0\n\t" \
  "buffer_load_dword v244, v245, %[rs], %[sab] offen sc1\n\t" \
  "s_waitcnt vmcnt(0)\n\t" \
  "v_readfirstlane_b32 s93, v244\n\t" \
  "s_nop 3\n\t" \
  "s_cmp_eq_u32 s93, 1\n\t" \
  "s_cbranch_scc1 .Lok%=\n\t" \
  "s_cmp_lt_u32 s92, 0x80000\n\t" \
  "s_cbranch_scc1 .Lpoll%=\n\t" \
  "v_mov_b32 v244, 1\n\t" \
  "buffer_store_dword v244, v245, %[rs], %[sab] offen sc1\n\t" \
  "s_branch .Lok%=\n\t" \
  ".Lend%=:\n\t"
#define QK_ASM_3 \
  "s_waitcnt lgkmcnt(0)\n\t" \
  "v_subrev_u32 v244, 0x8000, v250\n\t" \
  "ds_read_b128 v[216:219], v248 offset:45056\n\t" \
  "ds_read_b128 v[220:223], v248 offset:46080\n\t" \
  "ds_read_b128 v[224:227], v248 offset:47104\n\t" \
  "ds_read_b128 v[228:231], v248 offset:48128\n\t" \
  "ds_read_b128 v[208:211], v244 offset:0\n\t" \
  "ds_read_b128 v[212:215], v244 offset:1024\n\t" \
  "ds_read_b128 v[236:239], v244 offset:2048\n\t" \
  "ds_read_b128 v[240:243], v244 offset:3072\n\t" \
  "v_mov_b64 v[192:193], 0\n\t" \
  "v_mov_b64 v[194:195], 0\n\t" \
  "v_mov_b64 v[196:197], 0\n\t" \
  "v_mov_b64 v[198:199], 0\n\t" \
  "s_movk_i32 s92, 0x2000\n\t" \
  ".Lwt%=:\n\t" \
  "ds_read_b64 v[200:201], v248 offset:30720\n\t" \
  "s_memrealtime s[90:91]\n\t" \
  "s_sub_u32 s92, s92, 1\n\t" \
  "s_waitcnt lgkmcnt(0)\n\t" \
  "v_readfirstlane_b32 s93, v200\n\t" \
  "v_readfirstlane_b32 %[tgt], v201\n\t" \
  "s_cbranch_scc1 .Lgo%=\n\t" \
  "s_cmp_eq_u32 s93, %[step]\n\t" \
  "s_cbranch_scc1 .Lwx%=\n\t" \
  "s_sleep 1\n\t" \
  "s_branch .Lwt%=\n\t" \
  ".Lwx%=:\n\t" \
  "s_lshl_b32 s93, s90, 2\n\t" \
  "s_sub_i32 s93, %[tgt], s93\n\t" \
  "s_cmp_le_i32 s93, 0\n\t" \
  "s_cbranch_scc1 .Lgo%=\n\t" \
  "s_min_i32 s93, s93, 8000\n\t" \
  "s_mov_b32 s94, s93\n\t" \
  ".Lsl%=:\n\t" \
  "s_sub_i32 s94, s94, 18\n\t" \
  "s_cmp_lt_i32 s94, 30\n\t" \
  "s_cbranch_scc1 .Lsx%=\n\t" \
  "s_sleep 1\n\t" \
  "s_branch .Lsl%=\n\t" \
  ".Lsx%=:\n\t" \
  "s_sub_i32 s93, s93, 12\n\t" \
  "s_max_i32 s93, s93, 4\n\t" \
  "s_lshr_b32 s93, s93, 2\n\t" \
  "s_add_u32 s93, s93, s90\n\t" \
  "s_memrealtime s[90:91]\n\t" \
  ".Lpace%=:\n\t" \
  "s_memrealtime s[94:95]\n\t" \
  "s_waitcnt lgkmcnt(1)\n\t" \
  "s_sub_i32 s92, s90, s93\n\t" \
  "s_cmp_ge_i32 s92, 0\n\t" \
  "s_cbranch_scc1 .Le%=\n\t" \
  "s_memrealtime s[90:91]\n\t" \
  "s_waitcnt lgkmcnt(1)\n\t" \
  "s_sub_i32 s92, s94, s93\n\t" \
  "s_cmp_lt_i32 s92, 0\n\t" \
  "s_cbranch_scc1 .Lpace%=\n\t" \
  "s_nop 4\n\t" \
  ".Le%=:\n\t" \
  "s_add_u32 s92, s92, s93\n\t" \
  "s_lshl_b32 s92, s92, 2\n\t" \
  "s_sub_i32 s92, %[tgt], s92\n\t" \
  "s_max_i32 s92, s92, 0\n\t" \
  "s_bitcmp0_b32 s92, 3\n\t" \
  "s_cbranch_scc1 .Lf3%=\n\t" \
  "s_nop 15\n\t" \
  ".Lf3%=:\n\t" \
  "s_bitcmp0_b32 s92, 2\n\t" \
  "s_cbranch_scc1 .Lf2%=\n\t" \
  "s_nop 10\n\t" \
  ".Lf2%=:\n\t" \
  ".Lgo%=:\n\t" \
  "s_mov_b32 s92, 0\n\t" \
  ".Lpoll%=:\n\t" \
  "buffer_load_dwordx4 v[200:203], v248, %[rs], %[som] offen sc1\n\t" \
  "buffer_load_dwordx4 v[204:207], v248, %[rs], %[som2] offen sc1\n\t" \
  "s_waitcnt vmcnt(1) lgkmcnt(0)\n\t" \
  "ds_write_b128 v249, v[200:203]\n\t" \
  "v_smfmac_f32_16x16x64_bf16 v[192:195], v[200:203], v[208:215], v251\n\t" \
  "v_max3_u32 v244, v200, v201, v202\n\t" \
  "v_smfmac_f32_16x16x64_bf16 v[196:199], v[200:203], v[236:243], v251\n\t" \
  "s_waitcnt vmcnt(0)\n\t" \
  "ds_write_b128 v249, v[204:207] offset:8192\n\t" \
  "v_smfmac_f32_16x16x64_bf16 v[192:195], v[204:207], v[216:223], v251\n\t" \
  "v_max3_u32 v244, v244, v203, v204\n\t" \
  "v_max3_u32 v244, v244, v205, v206\n\t" \
  "v_smfmac_f32_16x16x64_bf16 v[196:199], v[204:207], v[224:231], v251\n\t" \
  "v_max_u32 v244, v244, v207\n\t" \
  "v_cmp_le_u32 vcc, %[sent], v244\n\t" \
  "s_cbranch_vccnz .Lslow%=\n\t" \
  ".Lok%=:\n\t" \
  "ds_read_b128 v[216:219], v250 offset:32768\n\t" \
  "ds_read_b128 v[220:223], v250 offset:33792\n\t" \
  "ds_read_b128 v[224:227], v250 offset:34816\n\t" \
  "ds_read_b128 v[228:231], v250 offset:35840\n\t" \
  "s_waitcnt lgkmcnt(4)\n\t" \
  "s_barrier\n\t" \
  "ds_read_b128 v[208:211], v248 offset:2048\n\t" \
  "ds_read_b128 v[212:215], v248 offset:5120\n\t" \
  "ds_read_b128 v[236:239], v248 offset:6144\n\t" \
  "ds_read_b128 v[240:243], v248 offset:7168\n\t" \
  "s_waitcnt lgkmcnt(3)\n\t" \
  "ds_read_b128 v[244:247], v248 offset:12288\n\t" \
  "v_smfmac_f32_16x16x64_bf16 v[192:195], v[208:211], v[216:223], v251\n\t" \
  "ds_read_b128 v[216:219], v250 offset:0\n\t" \
  "v_smfmac_f32_16x16x64_bf16 v[196:199], v[208:211], v[224:231], v251\n\t" \
  "s_waitcnt lgkmcnt(4)\n\t" \
  "ds_read_b128 v[220:223], v250 offset:1024\n\t" \
  "v_smfmac_f32_16x16x64_bf16 v[192:195], v[212:215], v[0:7], v251\n\t" \
  "ds_read_b128 v[224:227], v250 offset:2048\n\t" \
  "v_smfmac_f32_16x16x64_bf16 v[196:199], v[212:215], v[8:15], v251\n\t" \
  "s_waitcnt lgkmcnt(5)\n\t" \
  "ds_read_b128 v[228:231], v250 offset:3072\n\t" \
  "v_smfmac_f32_16x16x64_bf16 v[192:195], v[236:239], v[16:23], v251\n\t" \
  "ds_read_b128 v[200:203], v248 offset:8192\n\t" \
  "v_smfmac_f32_16x16x64_bf16 v[196:199], v[236:239], v[24:31], v251\n\t" \
  "s_waitcnt lgkmcnt(6)\n\t" \
  "ds_read_b128 v[204:207], v248 offset:9216\n\t" \
  "v_smfmac_f32_16x16x64_bf16 v[192:195], v[240:243], v[32:39], v251\n\t" \
  "ds_read_b128 v[208:211], v248 offset:10240\n\t" \
  "v_smfmac_f32_16x16x64_bf16 v[196:199], v[240:243], v[40:47], v251\n\t" \
  "s_waitcnt lgkmcnt(5)\n\t" \
  "ds_read_b128 v[212:215], v248 offset:13312\n\t" \
  "v_smfmac_f32_16x16x64_bf16 v[192:195], v[244:247], v[216:223], v251\n\t" \
  "s_waitcnt lgkmcnt(4)\n\t" \
  "v_smfmac_f32_16x16x64_bf16 v[196:199], v[244:247], v[224:231], v251\n\t" \
  "s_waitcnt lgkmcnt(3)\n\t" \
  "ds_read_b128 v[236:239], v248 offset:14336\n\t" \
  "v_smfmac_f32_16x16x64_bf16 v[192:195], v[200:203], v[48:55], v251\n\t" \
  "ds_read_b128 v[240:243], v248 offset:15360\n\t" \
  "v_smfmac_f32_16x16x64_bf16 v[196:199], v[200:203], v[56:63], v251\n\t" \
  "s_waitcnt lgkmcnt(4)\n\t" \
  "v_smfmac_f32_16x16x64_bf16 v[192:195], v[204:207], v[64:71], v251\n\t" \
  "ds_read_b128 v[244:247], v248 offset:0\n\t" \
  "v_smfmac_f32_16x16x64_bf16 v[196:199], v[204:207], v[72:79], v251\n\t" \
  "s_waitcnt lgkmcnt(4)\n\t" \
  "v_smfmac_f32_16x16x64_bf16 v[192:195], v[208:211], v[80:87], v251\n\t" \
  "ds_read_b128 v[200:203], v248 offset:1024\n\t" \
  "v_smfmac_f32_16x16x64_bf16 v[196:199], v[208:211], v[88:95], v251\n\t" \
  "s_waitcnt lgkmcnt(4)\n\t" \
  "v_smfmac_f32_16x16x64_bf16 v[192:195], v[212:215], v[96:103], v251\n\t" \
  "ds_read_b128 v[204:207], v248 offset:4096\n\t" \
  "v_smfmac_f32_16x16x64_bf16 v[196:199], v[212:215], v[104:111], v251\n\t" \
  "s_waitcnt lgkmcnt(3)\n\t" \
  "v_smfmac_f32_16x16x64_bf16 v[192:195], v[236:239], v[112:119], v251\n\t" \
  "v_smfmac_f32_16x16x64_bf16 v[196:199], v[236:239], v[120:127], v251\n\t" \
  "v_smfmac_f32_16x16x64_bf16 v[192:195], v[240:243], v[128:135], v251\n\t" \
  "v_smfmac_f32_16x16x64_bf16 v[196:199], v[240:243], v[136:143], v251\n\t" \
  "s_waitcnt lgkmcnt(1)\n\t" \
  "v_smfmac_f32_16x16x64_bf16 v[192:195], v[244:247], v[144:151], v251\n\t" \
  "v_smfmac_f32_16x16x64_bf16 v[196:199], v[244:247], v[152:159], v251\n\t" \
  "v_smfmac_f32_16x16x64_bf16 v[192:195], v[200:203], v[160:167], v251\n\t" \
  "v_smfmac_f32_16x16x64_bf16 v[196:199], v[200:203], v[168:175], v251\n\t" \
  "s_waitcnt lgkmcnt(0)\n\t" \
  "v_smfmac_f32_16x16x64_bf16 v[192:195], v[204:207], v[176:183], v251\n\t" \
  "v_smfmac_f32_16x16x64_bf16 v[196:199], v[204:207], v[184:191], v251\n\t" \
  "ds_read_b128 v[200:203], v248 offset:19456\n\t" \
  "ds_read_b128 v[204:207], v248 offset:23552\n\t" \
  "v_mov_b32 v250, v232\n\t" \
  "v_mov_b32 v249, v233\n\t" \
  "s_nop 2\n\t" \
  "v_add_f32 v192, v192, v194\n\t" \
  "v_add_f32 v193, v193, v195\n\t" \
  "v_add_f32 v196, v196, v198\n\t" \
  "v_add_f32 v197, v197, v199\n\t" \
  "v_cvt_pk_bf16_f32 v192, v192, v193\n\t" \
  "v_cvt_pk_bf16_f32 v196, v196, v197\n\t" \
  "ds_write2_b32 v252, v192, v196 offset1:64\n\t" \
  "v_mov_b32 v251, v234\n\t" \
  "ds_read_b64_tr_b16 v[244:245], v250 offset:1536\n\t" \
  "ds_read_b64_tr_b16 v[246:247], v250 offset:1600\n\t" \
  "v_mov_b32 v212, %[step]\n\t" \
  "s_mov_b64 exec, 1\n\t" \
  "ds_write_b32 v248, v212 offset:28720\n\t" \
  "s_mov_b64 exec, -1\n\t" \
  "buffer_load_dwordx4 v[192:195], v248, %[rt], %[svt] offen offset:0 sc1\n\t" \
  "buffer_load_dwordx4 v[196:199], v248, %[rt], %[svt] offen offset:1024 sc1\n\t" \
  "s_waitcnt lgkmcnt(1)\n\t" \
  "v_mfma_f32_16x16x32_bf16 v[200:203], v[200:203], v[244:247], 0\n\t" \
  "v_mfma_f32_16x16x32_bf16 v[204:207], v[204:207], v[244:247], 0\n\t" \
  "s_nop 7\n\t" \
  "v_cndmask_b32 v216, v200, v204, %[nh]\n\t" \
  "v_cndmask_b32 v217, v201, v205, %[nh]\n\t" \
  "v_cndmask_b32 v218, v202, v206, %[nh]\n\t" \
  "v_cndmask_b32 v219, v203, v207, %[nh]\n\t" \
  "ds_write_b128 v248, v[216:219] offset:19456\n\t" \
  "buffer_load_dword v252, v249, %[rs], %[sv] offen offset:0 sc1\n\t" \
  "buffer_load_dword v253, v249, %[rs], %[sv] offen offset:256 sc1\n\t" \
  "ds_read_b32 v212, v248 offset:28672\n\t" \
  "ds_read_b64_tr_b16 v[232:233], v250 offset:0\n\t" \
  "ds_read_b64_tr_b16 v[234:235], v250 offset:64\n\t" \
  "ds_read_b64_tr_b16 v[236:237], v250 offset:512\n\t" \
  "ds_read_b64_tr_b16 v[238:239], v250 offset:576\n\t" \
  "ds_read_b64_tr_b16 v[240:241], v250 offset:1024\n\t" \
  "ds_read_b64_tr_b16 v[242:243], v250 offset:1088\n\t" \
  "s_setprio 3\n\t" \
  "s_waitcnt lgkmcnt(0)\n\t" \
  "v_cmp_ne_u32 vcc, %[step], v212\n\t" \
  "s_barrier\n\t" \
  "s_and_b32 s93, vcc_lo, 0xf\n\t" \
  "s_cbranch_scc1 .Lre%=\n\t" \
  ".Lrb%=:\n\t" \
  "ds_read_b128 v[220:223], v248 offset:16384\n\t" \
  "ds_read_b128 v[224:227], v248 offset:17408\n\t" \
  "ds_read_b128 v[228:231], v248 offset:18432\n\t" \
  "ds_read_b64_tr_b16 v[212:213], v250 offset:3584\n\t" \
  "ds_read_b64_tr_b16 v[214:215], v250 offset:3648\n\t" \
  "ds_read_b64_tr_b16 v[200:201], v250 offset:2048\n\t" \
  "ds_read_b64_tr_b16 v[202:203], v250 offset:2112\n\t" \
  "ds_read_b64_tr_b16 v[204:205], v250 offset:2560\n\t" \
  "ds_read_b64_tr_b16 v[206:207], v250 offset:2624\n\t" \
  "ds_read_b64_tr_b16 v[208:209], v250 offset:3072\n\t" \
  "ds_read_b64_tr_b16 v[210:211], v250 offset:3136\n\t" \
  "s_waitcnt lgkmcnt(10)\n\t" \
  "v_pk_add_f32 v[216:217], v[216:217], v[220:221]\n\t" \
  "v_pk_add_f32 v[218:219], v[218:219], v[222:223]\n\t" \
  "s_waitcnt lgkmcnt(8)\n\t" \
  "v_pk_add_f32 v[224:225], v[224:225], v[228:229]\n\t" \
  "v_pk_add_f32 v[226:227], v[226:227], v[230:231]\n\t" \
  "v_pk_add_f32 v[216:217], v[216:217], v[224:225]\n\t" \
  "v_pk_add_f32 v[218:219], v[218:219], v[226:227]\n\t" \
  "s_waitcnt lgkmcnt(6)\n\t" \
  "v_mfma_f32_16x16x32_bf16 v[212:215], v[212:215], v[244:247], 0\n\t" \
  "v_max3_f32 v224, v216, v217, v218\n\t" \
  "v_max_f32 v224, v224, v219\n\t" \
  "s_waitcnt lgkmcnt(4)\n\t" \
  "v_mfma_f32_16x16x32_bf16 v[212:215], v[200:203], v[232:235], v[212:215]\n\t" \
  "v_max_f32_dpp v225, v224, v224 row_ror:8 row_mask:0xf bank_mask:0xf\n\t" \
  "v_max_f32_dpp v226, v224, v224 row_ror:8 row_mask:0xf bank_mask:0xf\n\t" \
  "s_waitcnt lgkmcnt(2)\n\t" \
  "v_mfma_f32_16x16x32_bf16 v[212:215], v[204:207], v[236:239], v[212:215]\n\t" \
  "v_permlane32_swap_b32 v225, v226\n\t" \
  "s_waitcnt lgkmcnt(0)\n\t" \
  "v_mfma_f32_16x16x32_bf16 v[212:215], v[208:211], v[240:243], v[212:215]\n\t" \
  "v_max_f32 v231, v225, v226\n\t" \
  "v_mov_b32 v220, 0x3f803f80\n\t" \
  "v_mov_b32 v221, 0x3f803f80\n\t" \
  "v_mov_b64 v[222:223], v[220:221]\n\t" \
  "v_mov_b32 v245, 0\n\t" \
  "v_mov_b32 v246, 0\n\t" \
  "s_nop 1\n\t" \
  "v_max3_f32 v200, v212, v213, v231\n\t" \
  "v_max3_f32 v201, v200, v214, v215\n\t" \
  "v_max3_f32 v202, v200, v214, v215\n\t" \
  "v_cndmask_b32 v212, v212, v214, %[up]\n\t" \
  "v_cndmask_b32 v213, v213, v215, %[up]\n\t" \
  "v_permlane16_swap_b32 v201, v202\n\t" \
  "v_max_f32 v200, v201, v202\n\t" \
  "v_mul_f32 v200, %[negc], v200\n\t" \
  "v_fma_f32 v212, v212, %[c], v200\n\t" \
  "v_fma_f32 v213, v213, %[c], v200\n\t" \
  "v_exp_f32 v212, v212\n\t" \
  "v_fma_f32 v216, v216, %[c], v200\n\t" \
  "v_exp_f32 v213, v213\n\t" \
  "v_fma_f32 v217, v217, %[c], v200\n\t" \
  "v_exp_f32 v216, v216\n\t" \
  "v_fma_f32 v218, v218, %[c], v200\n\t" \
  "v_cvt_pk_bf16_f32 v244, v212, v213\n\t" \
  "v_cvt_pk_bf16_f32 v247, v212, v213\n\t" \
  "v_exp_f32 v217, v217\n\t" \
  "v_fma_f32 v219, v219, %[c], v200\n\t" \
  "v_mfma_f32_16x16x16_bf16 v[208:211], v[220:221], v[244:245], 0\n\t" \
  "v_exp_f32 v218, v218\n\t" \
  "s_waitcnt vmcnt(0)\n\t" \
  "v_mfma_f32_16x16x16_bf16 v[224:227], v[252:253], v[244:245], 0\n\t" \
  "v_cvt_pk_bf16_f32 v206, v216, v217\n\t" \
  "v_cvt_pk_bf16_f32 v204, v216, v217\n\t" \
  "v_exp_f32 v219, v219\n\t" \
  "v_mfma_f32_16x16x16_bf16 v[228:231], v[252:253], v[246:247], 0\n\t" \
  "v_cvt_pk_bf16_f32 v207, v218, v219\n\t" \
  "v_cvt_pk_bf16_f32 v205, v218, v219\n\t" \
  "v_mov_b32_dpp v204, v206 row_ror:8 row_mask:0xf bank_mask:0xc\n\t" \
  "v_mov_b32_dpp v206, v206 row_ror:8 row_mask:0xf bank_mask:0x3\n\t" \
  "v_mov_b32_dpp v205, v207 row_ror:8 row_mask:0xf bank_mask:0xc\n\t" \
  "v_mov_b32_dpp v207, v207 row_ror:8 row_mask:0xf bank_mask:0x3\n\t" \
  "v_max_u32 v200, v252, v253\n\t" \
  "v_cmp_le_u32 vcc, %[sent], v200\n\t" \
  "v_mfma_f32_16x16x32_bf16 v[208:211], v[220:223], v[204:207], v[208:211]\n\t" \
  ".Lpv%=:\n\t" \
  "v_mfma_f32_16x16x32_bf16 v[224:227], v[192:195], v[204:207], v[224:227]\n\t" \
  "v_mfma_f32_16x16x32_bf16 v[228:231], v[196:199], v[204:207], v[228:231]\n\t" \
  "s_cbranch_vccnz .Lvs%=\n\t" \
  ".Lvok%=:\n\t" \
  "s_nop 4\n\t" \
  "v_rcp_f32 v200, v208\n\t" \
  "v_cndmask_b32 v224, v224, v226, %[nh]\n\t" \
  "v_cndmask_b32 v225, v225, v227, %[nh]\n\t" \
  "v_cndmask_b32 v228, v228, v230, %[nh]\n\t" \
  "v_cndmask_b32 v229, v229, v231, %[nh]\n\t" \
  "v_pk_mul_f32 v[224:225], v[224:225], v[200:201] op_sel_hi:[1,0]\n\t" \
  "v_pk_mul_f32 v[228:229], v[228:229], v[200:201] op_sel_hi:[1,0]\n\t" \
  "v_cvt_pk_bf16_f32 v224, v224, v225\n\t" \
  "v_cvt_pk_bf16_f32 v225, v228, v229\n\t" \
  "buffer_store_dwordx2 v[224:225], v251, %[rs], %[so] offen sc1\n\t" \
  "s_memtime s[90:91]\n\t" \
  "s_setprio 0\n\t" \
  "s_waitcnt lgkmcnt(0)\n\t" \
  "s_mov_b32 %[tout], s90\n\t" \
  "s_mov_b32 %[spins], s92\n\t" \
  "s_branch .Lend%=\n\t" \
  ".Lre%=:\n\t" \
  "ds_read_b64_tr_b16 v[232:233], v250 offset:0\n\t" \
  "ds_read_b64_tr_b16 v[234:235], v250 offset:64\n\t" \
  "ds_read_b64_tr_b16 v[236:237], v250 offset:512\n\t" \
  "ds_read_b64_tr_b16 v[238:239], v250 offset:576\n\t" \
  "ds_read_b64_tr_b16 v[240:241], v250 offset:1024\n\t" \
  "ds_read_b64_tr_b16 v[242:243], v250 offset:1088\n\t" \
  "s_waitcnt lgkmcnt(0)\n\t" \
  "s_branch .Lrb%=\n\t" \
  ".Lvs%=:\n\t" \
  "v_cmp_eq_u32 vcc, %[sent], v252\n\t" \
  "v_cmp_eq_u32 s[94:95], %[sent], v253\n\t" \
  "s_or_b64 s[94:95], s[94:95], vcc\n\t" \
  "s_cbranch_scc0 .Lvok%=\n\t" \
  "s_add_u32 s92, s92, 1\n\t" \
  "s_sleep 2\n\t" \
  "s_and_b32 s93, s92, 63\n\t" \
  "s_cbranch_scc1 .Lvr%=\n\t" \
  "buffer_load_dword v200, off, %[rs], %[sab] sc1\n\t" \
  "s_waitcnt vmcnt(0)\n\t" \
  "v_readfirstlane_b32 s93, v200\n\t" \
  "s_nop 3\n\t" \
  "s_cmp_eq_u32 s93, 1\n\t" \
  "s_cbranch_scc1 .Lvok%=\n\t" \
  "s_cmp_lt_u32 s92, 0x80000\n\t" \
  "s_cbranch_scc1 .Lvr%=\n\t" \
  "v_mov_b32 v200, 1\n\t" \
  "buffer_store_dword v200, off, %[rs], %[sab] sc1\n\t" \
  "s_branch .Lvok%=\n\t" \
  ".Lvr%=:\n\t" \
  "buffer_load_dword v252, v249, %[rs], %[sv] offen offset:0 sc1\n\t" \
  "buffer_load_dword v253, v249, %[rs], %[sv] offen offset:256 sc1\n\t" \
  "s_waitcnt vmcnt(0)\n\t" \
  "v_mfma_f32_16x16x16_bf16 v[224:227], v[252:253], v[244:245], 0\n\t" \
  "v_mfma_f32_16x16x16_bf16 v[228:231], v[252:253], v[246:247], 0\n\t" \
  "v_max_u32 v200, v252, v253\n\t" \
  "v_cmp_le_u32 vcc, %[sent], v200\n\t" \
  "s_nop 4\n\t" \
  "s_branch .Lpv%=\n\t" \
  "s_branch .Lend%=\n\t" \
  ".Lslow%=:\n\t" \
  "s_mov_b64 s[94:95], 0\n\t" \
  "v_cmp_eq_u32 vcc, %[sent], v200\n\t" \
  "s_or_b64 s[94:95], s[94:95], vcc\n\t" \
  "v_cmp_eq_u32 vcc, %[sent], v201\n\t" \
  "s_or_b64 s[94:95], s[94:95], vcc\n\t" \
  "v_cmp_eq_u32 vcc, %[sent], v202\n\t" \
  "s_or_b64 s[94:95], s[94:95], vcc\n\t" \
  "v_cmp_eq_u32 vcc, %[sent], v203\n\t" \
  "s_or_b64 s[94:95], s[94:95], vcc\n\t" \
  "v_cmp_eq_u32 vcc, %[sent], v204\n\t" \
  "s_or_b64 s[94:95], s[94:95], vcc\n\t" \
  "v_cmp_eq_u32 vcc, %[sent], v205\n\t" \
  "s_or_b64 s[94:95], s[94:95], vcc\n\t" \
  "v_cmp_eq_u32 vcc, %[sent], v206\n\t" \
  "s_or_b64 s[94:95], s[94:95], vcc\n\t" \
  "v_cmp_eq_u32 vcc, %[sent], v207\n\t" \
  "s_or_b64 s[94:95], s[94:95], vcc\n\t" \
  "s_cmp_eq_u64 s[94:95], 0\n\t" \
  "s_cbranch_scc1 .Lok%=\n\t" \
  "v_mov_b64 v[192:193], 0\n\t" \
  "v_mov_b64 v[194:195], 0\n\t" \
  "v_mov_b64 v[196:197], 0\n\t" \
  "v_mov_b64 v[198:199], 0\n\t" \
  "s_add_u32 s92, s92, 1\n\t" \
  "s_sleep 6\n\t" \
  "s_cmp_eq_u32 s92, 1\n\t" \
  "s_cbranch_scc1 .Lpoll%=\n\t" \
  "s_sleep 1\n\t" \
  "s_and_b32 s93, s92, 63\n\t" \
  "s_cmp_lg_u32 s93, 0\n\t" \
  "s_cbranch_scc1 .Lpoll%=\n\t" \
  "v_mov_b32 v245, 0\n\t" \
  "buffer_load_dword v244, v245, %[rs], %[sab] offen sc1\n\t" \
  "s_waitcnt vmcnt(0)\n\t" \
  "v_readfirstlane_b32 s93, v244\n\t" \
  "s_nop 3\n\t" \
  "s_cmp_eq_u32 s93, 1\n\t" \
  "s_cbranch_scc1 .Lok%=\n\t" \
  "s_cmp_lt_u32 s92, 0x80000\n\t" \
  "s_cbranch_scc1 .Lpoll%=\n\t" \
  "v_mov_b32 v244, 1\n\t" \
  "buffer_store_dword v244, v245, %[rs], %[sab] offen sc1\n\t" \
  "s_branch .Lok%=\n\t" \
  ".Lend%=:\n\t"
#define QK_ASM_4 \
  "s_waitcnt lgkmcnt(0)\n\t" \
  "v_subrev_u32 v244, 0x8000, v250\n\t" \
  "ds_read_b128 v[216:219], v248 offset:49152\n\t" \
  "ds_read_b128 v[220:223], v248 offset:50176\n\t" \
  "ds_read_b128 v[224:227], v248 offset:51200\n\t" \
  "ds_read_b128 v[228:231], v248 offset:52224\n\t" \
  "ds_read_b128 v[208:211], v244 offset:0\n\t" \
  "ds_read_b128 v[212:215], v244 offset:1024\n\t" \
  "ds_read_b128 v[236:239], v244 offset:2048\n\t" \
  "ds_read_b128 v[240:243], v244 offset:3072\n\t" \
  "v_mov_b64 v[192:193], 0\n\t" \
  "v_mov_b64 v[194:195], 0\n\t" \
  "v_mov_b64 v[196:197], 0\n\t" \
  "v_mov_b64 v[198:199], 0\n\t" \
  "s_movk_i32 s92, 0x2000\n\t" \
  ".Lwt%=:\n\t" \
  "ds_read_b64 v[200:201], v248 offset:30720\n\t" \
  "s_memrealtime s[90:91]\n\t" \
  "s_sub_u32 s92, s92, 1\n\t" \
  "s_waitcnt lgkmcnt(0)\n\t" \
  "v_readfirstlane_b32 s93, v200\n\t" \
  "v_readfirstlane_b32 %[tgt], v201\n\t" \
  "s_cbranch_scc1 .Lgo%=\n\t" \
  "s_cmp_eq_u32 s93, %[step]\n\t" \
  "s_cbranch_scc1 .Lwx%=\n\t" \
  "s_sleep 1\n\t" \
  "s_branch .Lwt%=\n\t" \
  ".Lwx%=:\n\t" \
  "s_lshl_b32 s93, s90, 2\n\t" \
  "s_sub_i32 s93, %[tgt], s93\n\t" \
  "s_cmp_le_i32 s93, 0\n\t" \
  "s_cbranch_scc1 .Lgo%=\n\t" \
  "s_min_i32 s93, s93, 8000\n\t" \
  "s_mov_b32 s94, s93\n\t" \
  ".Lsl%=:\n\t" \
  "s_sub_i32 s94, s94, 18\n\t" \
  "s_cmp_lt_i32 s94, 30\n\t" \
  "s_cbranch_scc1 .Lsx%=\n\t" \
  "s_sleep 1\n\t" \
  "s_branch .Lsl%=\n\t" \
  ".Lsx%=:\n\t" \
  "s_sub_i32 s93, s93, 12\n\t" \
  "s_max_i32 s93, s93, 4\n\t" \
  "s_lshr_b32 s93, s93, 2\n\t" \
  "s_add_u32 s93, s93, s90\n\t" \
  "s_memrealtime s[90:91]\n\t" \
  ".Lpace%=:\n\t" \
  "s_memrealtime s[94:95]\n\t" \
  "s_waitcnt lgkmcnt(1)\n\t" \
  "s_sub_i32 s92, s90, s93\n\t" \
  "s_cmp_ge_i32 s92, 0\n\t" \
  "s_cbranch_scc1 .Le%=\n\t" \
  "s_memrealtime s[90:91]\n\t" \
  "s_waitcnt lgkmcnt(1)\n\t" \
  "s_sub_i32 s92, s94, s93\n\t" \
  "s_cmp_lt_i32 s92, 0\n\t" \
  "s_cbranch_scc1 .Lpace%=\n\t" \
  "s_nop 4\n\t" \
  ".Le%=:\n\t" \
  "s_add_u32 s92, s92, s93\n\t" \
  "s_lshl_b32 s92, s92, 2\n\t" \
  "s_sub_i32 s92, %[tgt], s92\n\t" \
  "s_max_i32 s92, s92, 0\n\t" \
  "s_bitcmp0_b32 s92, 3\n\t" \
  "s_cbranch_scc1 .Lf3%=\n\t" \
  "s_nop 15\n\t" \
  ".Lf3%=:\n\t" \
  "s_bitcmp0_b32 s92, 2\n\t" \
  "s_cbranch_scc1 .Lf2%=\n\t" \
  "s_nop 10\n\t" \
  ".Lf2%=:\n\t" \
  ".Lgo%=:\n\t" \
  "s_mov_b32 s92, 0\n\t" \
  ".Lpoll%=:\n\t" \
  "buffer_load_dwordx4 v[200:203], v248, %[rs], %[som] offen sc1\n\t" \
  "buffer_load_dwordx4 v[204:207], v248, %[rs], %[som2] offen sc1\n\t" \
  "s_waitcnt vmcnt(1) lgkmcnt(0)\n\t" \
  "ds_write_b128 v249, v[200:203]\n\t" \
  "v_smfmac_f32_16x16x64_bf16 v[192:195], v[200:203], v[208:215], v251\n\t" \
  "v_max3_u32 v244, v200, v201, v202\n\t" \
  "v_smfmac_f32_16x16x64_bf16 v[196:199], v[200:203], v[236:243], v251\n\t" \
  "s_waitcnt vmcnt(0)\n\t" \
  "ds_write_b128 v249, v[204:207] offset:8192\n\t" \
  "v_smfmac_f32_16x16x64_bf16 v[192:195], v[204:207], v[216:223], v251\n\t" \
  "v_max3_u32 v244, v244, v203, v204\n\t" \
  "v_max3_u32 v244, v244, v205, v206\n\t" \
  "v_smfmac_f32_16x16x64_bf16 v[196:199], v[204:207], v[224:231], v251\n\t" \
  "v_max_u32 v244, v244, v207\n\t" \
  "v_cmp_le_u32 vcc, %[sent], v244\n\t" \
  "s_cbranch_vccnz .Lslow%=\n\t" \
  ".Lok%=:\n\t" \
  "ds_read_b128 v[216:219], v250 offset:32768\n\t" \
  "ds_read_b128 v[220:223], v250 offset:33792\n\t" \
  "ds_read_b128 v[224:227], v250 offset:34816\n\t" \
  "ds_read_b128 v[228:231], v250 offset:35840\n\t" \
  "s_waitcnt lgkmcnt(4)\n\t" \
  "s_barrier\n\t" \
  "ds_read_b128 v[208:211], v248 offset:5120\n\t" \
  "ds_read_b128 v[212:215], v248 offset:6144\n\t" \
  "ds_read_b128 v[236:239], v248 offset:7168\n\t" \
  "ds_read_b128 v[240:243], v248 offset:8192\n\t" \
  "s_waitcnt lgkmcnt(3)\n\t" \
  "ds_read_b128 v[244:247], v248 offset:13312\n\t" \
  "v_smfmac_f32_16x16x64_bf16 v[192:195], v[208:211], v[216:223], v251\n\t" \
  "ds_read_b128 v[216:219], v250 offset:0\n\t" \
  "v_smfmac_f32_16x16x64_bf16 v[196:199], v[208:211], v[224:231], v251\n\t" \
  "s_waitcnt lgkmcnt(4)\n\t" \
  "ds_read_b128 v[220:223], v250 offset:1024\n\t" \
  "v_smfmac_f32_16x16x64_bf16 v[192:195], v[212:215], v[0:7], v251\n\t" \
  "ds_read_b128 v[224:227], v250 offset:2048\n\t" \
  "v_smfmac_f32_16x16x64_bf16 v[196:199], v[212:215], v[8:15], v251\n\t" \
  "s_waitcnt lgkmcnt(5)\n\t" \
  "ds_read_b128 v[228:231], v250 offset:3072\n\t" \
  "v_smfmac_f32_16x16x64_bf16 v[192:195], v[236:239], v[16:23], v251\n\t" \
  "ds_read_b128 v[200:203], v248 offset:9216\n\t" \
  "v_smfmac_f32_16x16x64_bf16 v[196:199], v[236:239], v[24:31], v251\n\t" \
  "s_waitcnt lgkmcnt(6)\n\t" \
  "ds_read_b128 v[204:207], v248 offset:10240\n\t" \
  "v_smfmac_f32_16x16x64_bf16 v[192:195], v[240:243], v[32:39], v251\n\t" \
  "ds_read_b128 v[208:211], v248 offset:11264\n\t" \
  "v_smfmac_f32_16x16x64_bf16 v[196:199], v[240:243], v[40:47], v251\n\t" \
  "s_waitcnt lgkmcnt(5)\n\t" \
  "ds_read_b128 v[212:215], v248 offset:14336\n\t" \
  "v_smfmac_f32_16x16x64_bf16 v[192:195], v[244:247], v[216:223], v251\n\t" \
  "s_waitcnt lgkmcnt(4)\n\t" \
  "v_smfmac_f32_16x16x64_bf16 v[196:199], v[244:247], v[224:231], v251\n\t" \
  "s_waitcnt lgkmcnt(3)\n\t" \
  "ds_read_b128 v[236:239], v248 offset:15360\n\t" \
  "v_smfmac_f32_16x16x64_bf16 v[192:195], v[200:203], v[48:55], v251\n\t" \
  "ds_read_b128 v[240:243], v248 offset:0\n\t" \
  "v_smfmac_f32_16x16x64_bf16 v[196:199], v[200:203], v[56:63], v251\n\t" \
  "s_waitcnt lgkmcnt(4)\n\t" \
  "v_smfmac_f32_16x16x64_bf16 v[192:195], v[204:207], v[64:71], v251\n\t" \
  "ds_read_b128 v[244:247], v248 offset:1024\n\t" \
  "v_smfmac_f32_16x16x64_bf16 v[196:199], v[204:207], v[72:79], v251\n\t" \
  "s_waitcnt lgkmcnt(4)\n\t" \
  "v_smfmac_f32_16x16x64_bf16 v[192:195], v[208:211], v[80:87], v251\n\t" \
  "ds_read_b128 v[200:203], v248 offset:2048\n\t" \
  "v_smfmac_f32_16x16x64_bf16 v[196:199], v[208:211], v[88:95], v251\n\t" \
  "s_waitcnt lgkmcnt(4)\n\t" \
  "v_smfmac_f32_16x16x64_bf16 v[192:195], v[212:215], v[96:103], v251\n\t" \
  "ds_read_b128 v[204:207], v248 offset:3072\n\t" \
  "v_smfmac_f32_16x16x64_bf16 v[196:199], v[212:215], v[104:111], v251\n\t" \
  "s_waitcnt lgkmcnt(3)\n\t" \
  "v_smfmac_f32_16x16x64_bf16 v[192:195], v[236:239], v[112:119], v251\n\t" \
  "v_smfmac_f32_16x16x64_bf16 v[196:199], v[236:239], v[120:127], v251\n\t" \
  "v_smfmac_f32_16x16x64_bf16 v[192:195], v[240:243], v[128:135], v251\n\t" \
  "v_smfmac_f32_16x16x64_bf16 v[196:199], v[240:243], v[136:143], v251\n\t" \
  "s_waitcnt lgkmcnt(1)\n\t" \
  "v_smfmac_f32_16x16x64_bf16 v[192:195], v[244:247], v[144:151], v251\n\t" \
  "v_smfmac_f32_16x16x64_bf16 v[196:199], v[244:247], v[152:159], v251\n\t" \
  "v_smfmac_f32_16x16x64_bf16 v[192:195], v[200:203], v[160:167], v251\n\t" \
  "v_smfmac_f32_16x16x64_bf16 v[196:199], v[200:203], v[168:175], v251\n\t" \
  "s_waitcnt lgkmcnt(0)\n\t" \
  "v_smfmac_f32_16x16x64_bf16 v[192:195], v[204:207], v[176:183], v251\n\t" \
  "v_smfmac_f32_16x16x64_bf16 v[196:199], v[204:207], v[184:191], v251\n\t" \
  "s_nop 6\n\t" \
  "v_add_f32 v192, v192, v194\n\t" \
  "v_add_f32 v193, v193, v195\n\t" \
  "v_add_f32 v196, v196, v198\n\t" \
  "v_add_f32 v197, v197, v199\n\t" \
  "v_cvt_pk_bf16_f32 v192, v192, v193\n\t" \
  "v_cvt_pk_bf16_f32 v196, v196, v197\n\t" \
  "ds_write2_b32 v252, v192, v196 offset1:64\n\t" \
  "s_waitcnt lgkmcnt(0)\n\t" \
  "s_barrier\n\t" \
  "buffer_store_dword v192, v235, %[rs], %[sk] offen sc1\n\t" \
  "buffer_store_dword v196, v235, %[rs], %[sk] offen offset:256 sc1\n\t" \
  "s_mov_b32 %[spins], s92\n\t" \
  "s_branch .Lend%=\n\t" \
  ".Lslow%=:\n\t" \
  "s_mov_b64 s[94:95], 0\n\t" \
  "v_cmp_eq_u32 vcc, %[sent], v200\n\t" \
  "s_or_b64 s[94:95], s[94:95], vcc\n\t" \
  "v_cmp_eq_u32 vcc, %[sent], v201\n\t" \
  "s_or_b64 s[94:95], s[94:95], vcc\n\t" \
  "v_cmp_eq_u32 vcc, %[sent], v202\n\t" \
  "s_or_b64 s[94:95], s[94:95], vcc\n\t" \
  "v_cmp_eq_u32 vcc, %[sent], v203\n\t" \
  "s_or_b64 s[94:95], s[94:95], vcc\n\t" \
  "v_cmp_eq_u32 vcc, %[sent], v204\n\t" \
  "s_or_b64 s[94:95], s[94:95], vcc\n\t" \
  "v_cmp_eq_u32 vcc, %[sent], v205\n\t" \
  "s_or_b64 s[94:95], s[94:95], vcc\n\t" \
  "v_cmp_eq_u32 vcc, %[sent], v206\n\t" \
  "s_or_b64 s[94:95], s[94:95], vcc\n\t" \
  "v_cmp_eq_u32 vcc, %[sent], v207\n\t" \
  "s_or_b64 s[94:95], s[94:95], vcc\n\t" \
  "s_cmp_eq_u64 s[94:95], 0\n\t" \
  "s_cbranch_scc1 .Lok%=\n\t" \
  "v_mov_b64 v[192:193], 0\n\t" \
  "v_mov_b64 v[194:195], 0\n\t" \
  "v_mov_b64 v[196:197], 0\n\t" \
  "v_mov_b64 v[198:199], 0\n\t" \
  "s_add_u32 s92, s92, 1\n\t" \
  "s_sleep 6\n\t" \
  "s_cmp_eq_u32 s92, 1\n\t" \
  "s_cbranch_scc1 .Lpoll%=\n\t" \
  "s_sleep 1\n\t" \
  "s_and_b32 s93, s92, 63\n\t" \
  "s_cmp_lg_u32 s93, 0\n\t" \
  "s_cbranch_scc1 .Lpoll%=\n\t" \
  "v_mov_b32 v245, 0\n\t" \
  "buffer_load_dword v244, v245, %[rs], %[sab] offen sc1\n\t" \
  "s_waitcnt vmcnt(0)\n\t" \
  "v_readfirstlane_b32 s93, v244\n\t" \
  "s_nop 3\n\t" \
  "s_cmp_eq_u32 s93, 1\n\t" \
  "s_cbranch_scc1 .Lok%=\n\t" \
  "s_cmp_lt_u32 s92, 0x80000\n\t" \
  "s_cbranch_scc1 .Lpoll%=\n\t" \
  "v_mov_b32 v244, 1\n\t" \
  "buffer_store_dword v244, v245, %[rs], %[sab] offen sc1\n\t" \
  "s_branch .Lok%=\n\t" \
  ".Lend%=:\n\t"
#define QK_ASM_5 \
  "s_waitcnt lgkmcnt(0)\n\t" \
  "v_subrev_u32 v244, 0x8000, v250\n\t" \
  "ds_read_b128 v[216:219], v248 offset:53248\n\t" \
  "ds_read_b128 v[220:223], v248 offset:54272\n\t" \
  "ds_read_b128 v[224:227], v248 offset:55296\n\t" \
  "ds_read_b128 v[228:231], v248 offset:56320\n\t" \
  "ds_read_b128 v[208:211], v244 offset:0\n\t" \
  "ds_read_b128 v[212:215], v244 offset:1024\n\t" \
  "ds_read_b128 v[236:239], v244 offset:2048\n\t" \
  "ds_read_b128 v[240:243], v244 offset:3072\n\t" \
  "v_mov_b64 v[192:193], 0\n\t" \
  "v_mov_b64 v[194:195], 0\n\t" \
  "v_mov_b64 v[196:197], 0\n\t" \
  "v_mov_b64 v[198:199], 0\n\t" \
  "s_movk_i32 s92, 0x2000\n\t" \
  ".Lwt%=:\n\t" \
  "ds_read_b64 v[200:201], v248 offset:30720\n\t" \
  "s_memrealtime s[90:91]\n\t" \
  "s_sub_u32 s92, s92, 1\n\t" \
  "s_waitcnt lgkmcnt(0)\n\t" \
  "v_readfirstlane_b32 s93, v200\n\t" \
  "v_readfirstlane_b32 %[tgt], v201\n\t" \
  "s_cbranch_scc1 .Lgo%=\n\t" \
  "s_cmp_eq_u32 s93, %[step]\n\t" \
  "s_cbranch_scc1 .Lwx%=\n\t" \
  "s_sleep 1\n\t" \
  "s_branch .Lwt%=\n\t" \
  ".Lwx%=:\n\t" \
  "s_lshl_b32 s93, s90, 2\n\t" \
  "s_sub_i32 s93, %[tgt], s93\n\t" \
  "s_cmp_le_i32 s93, 0\n\t" \
  "s_cbranch_scc1 .Lgo%=\n\t" \
  "s_min_i32 s93, s93, 8000\n\t" \
  "s_mov_b32 s94, s93\n\t" \
  ".Lsl%=:\n\t" \
  "s_sub_i32 s94, s94, 18\n\t" \
  "s_cmp_lt_i32 s94, 30\n\t" \
  "s_cbranch_scc1 .Lsx%=\n\t" \
  "s_sleep 1\n\t" \
  "s_branch .Lsl%=\n\t" \
  ".Lsx%=:\n\t" \
  "s_sub_i32 s93, s93, 12\n\t" \
  "s_max_i32 s93, s93, 4\n\t" \
  "s_lshr_b32 s93, s93, 2\n\t" \
  "s_add_u32 s93, s93, s90\n\t" \
  "s_memrealtime s[90:91]\n\t" \
  ".Lpace%=:\n\t" \
  "s_memrealtime s[94:95]\n\t" \
  "s_waitcnt lgkmcnt(1)\n\t" \
  "s_sub_i32 s92, s90, s93\n\t" \
  "s_cmp_ge_i32 s92, 0\n\t" \
  "s_cbranch_scc1 .Le%=\n\t" \
  "s_memrealtime s[90:91]\n\t" \
  "s_waitcnt lgkmcnt(1)\n\t" \
  "s_sub_i32 s92, s94, s93\n\t" \
  "s_cmp_lt_i32 s92, 0\n\t" \
  "s_cbranch_scc1 .Lpace%=\n\t" \
  "s_nop 4\n\t" \
  ".Le%=:\n\t" \
  "s_add_u32 s92, s92, s93\n\t" \
  "s_lshl_b32 s92, s92, 2\n\t" \
  "s_sub_i32 s92, %[tgt], s92\n\t" \
  "s_max_i32 s92, s92, 0\n\t" \
  "s_bitcmp0_b32 s92, 3\n\t" \
  "s_cbranch_scc1 .Lf3%=\n\t" \
  "s_nop 15\n\t" \
  ".Lf3%=:\n\t" \
  "s_bitcmp0_b32 s92, 2\n\t" \
  "s_cbranch_scc1 .Lf2%=\n\t" \
  "s_nop 10\n\t" \
  ".Lf2%=:\n\t" \
  ".Lgo%=:\n\t" \
  "s_mov_b32 s92, 0\n\t" \
  ".Lpoll%=:\n\t" \
  "buffer_load_dwordx4 v[200:203], v248, %[rs], %[som] offen sc1\n\t" \
  "buffer_load_dwordx4 v[204:207], v248, %[rs], %[som2] offen sc1\n\t" \
  "s_waitcnt vmcnt(1) lgkmcnt(0)\n\t" \
  "ds_write_b128 v249, v[200:203]\n\t" \
  "v_smfmac_f32_16x16x64_bf16 v[192:195], v[200:203], v[208:215], v251\n\t" \
  "v_max3_u32 v244, v200, v201, v202\n\t" \
  "v_smfmac_f32_16x16x64_bf16 v[196:199], v[200:203], v[236:243], v251\n\t" \
  "s_waitcnt vmcnt(0)\n\t" \
  "ds_write_b128 v249, v[204:207] offset:8192\n\t" \
  "v_smfmac_f32_16x16x64_bf16 v[192:195], v[204:207], v[216:223], v251\n\t" \
  "v_max3_u32 v244, v244, v203, v204\n\t" \
  "v_max3_u32 v244, v244, v205, v206\n\t" \
  "v_smfmac_f32_16x16x64_bf16 v[196:199], v[204:207], v[224:231], v251\n\t" \
  "v_max_u32 v244, v244, v207\n\t" \
  "v_cmp_le_u32 vcc, %[sent], v244\n\t" \
  "s_cbranch_vccnz .Lslow%=\n\t" \
  ".Lok%=:\n\t" \
  "ds_read_b128 v[216:219], v250 offset:32768\n\t" \
  "ds_read_b128 v[220:223], v250 offset:33792\n\t" \
  "ds_read_b128 v[224:227], v250 offset:34816\n\t" \
  "ds_read_b128 v[228:231], v250 offset:35840\n\t" \
  "s_waitcnt lgkmcnt(4)\n\t" \
  "s_barrier\n\t" \
  "ds_read_b128 v[208:211], v248 offset:4096\n\t" \
  "ds_read_b128 v[212:215], v248 offset:7168\n\t" \
  "ds_read_b128 v[236:239], v248 offset:8192\n\t" \
  "ds_read_b128 v[240:243], v248 offset:9216\n\t" \
  "s_waitcnt lgkmcnt(3)\n\t" \
  "ds_read_b128 v[244:247], v248 offset:12288\n\t" \
  "v_smfmac_f32_16x16x64_bf16 v[192:195], v[208:211], v[216:223], v251\n\t" \
  "ds_read_b128 v[216:219], v250 offset:0\n\t" \
  "v_smfmac_f32_16x16x64_bf16 v[196:199], v[208:211], v[224:231], v251\n\t" \
  "s_waitcnt lgkmcnt(4)\n\t" \
  "ds_read_b128 v[220:223], v250 offset:1024\n\t" \
  "v_smfmac_f32_16x16x64_bf16 v[192:195], v[212:215], v[0:7], v251\n\t" \
  "ds_read_b128 v[224:227], v250 offset:2048\n\t" \
  "v_smfmac_f32_16x16x64_bf16 v[196:199], v[212:215], v[8:15], v251\n\t" \
  "s_waitcnt lgkmcnt(5)\n\t" \
  "ds_read_b128 v[228:231], v250 offset:3072\n\t" \
  "v_smfmac_f32_16x16x64_bf16 v[192:195], v[236:239], v[16:23], v251\n\t" \
  "ds_read_b128 v[200:203], v248 offset:10240\n\t" \
  "v_smfmac_f32_16x16x64_bf16 v[196:199], v[236:239], v[24:31], v251\n\t" \
  "s_waitcnt lgkmcnt(6)\n\t" \
  "ds_read_b128 v[204:207], v248 offset:11264\n\t" \
  "v_smfmac_f32_16x16x64_bf16 v[192:195], v[240:243], v[32:39], v251\n\t" \
  "ds_read_b128 v[208:211], v248 offset:14336\n\t" \
  "v_smfmac_f32_16x16x64_bf16 v[196:199], v[240:243], v[40:47], v251\n\t" \
  "s_waitcnt lgkmcnt(5)\n\t" \
  "ds_read_b128 v[212:215], v248 offset:15360\n\t" \
  "v_smfmac_f32_16x16x64_bf16 v[192:195], v[244:247], v[216:223], v251\n\t" \
  "s_waitcnt lgkmcnt(4)\n\t" \
  "v_smfmac_f32_16x16x64_bf16 v[196:199], v[244:247], v[224:231], v251\n\t" \
  "s_waitcnt lgkmcnt(3)\n\t" \
  "ds_read_b128 v[236:239], v248 offset:0\n\t" \
  "v_smfmac_f32_16x16x64_bf16 v[192:195], v[200:203], v[48:55], v251\n\t" \
  "ds_read_b128 v[240:243], v248 offset:1024\n\t" \
  "v_smfmac_f32_16x16x64_bf16 v[196:199], v[200:203], v[56:63], v251\n\t" \
  "s_waitcnt lgkmcnt(4)\n\t" \
  "v_smfmac_f32_16x16x64_bf16 v[192:195], v[204:207], v[64:71], v251\n\t" \
  "ds_read_b128 v[244:247], v248 offset:2048\n\t" \
  "v_smfmac_f32_16x16x64_bf16 v[196:199], v[204:207], v[72:79], v251\n\t" \
  "s_waitcnt lgkmcnt(4)\n\t" \
  "v_smfmac_f32_16x16x64_bf16 v[192:195], v[208:211], v[80:87], v251\n\t" \
  "ds_read_b128 v[200:203], v248 offset:3072\n\t" \
  "v_smfmac_f32_16x16x64_bf16 v[196:199], v[208:211], v[88:95], v251\n\t" \
  "s_waitcnt lgkmcnt(4)\n\t" \
  "v_smfmac_f32_16x16x64_bf16 v[192:195], v[212:215], v[96:103], v251\n\t" \
  "ds_read_b128 v[204:207], v248 offset:6144\n\t" \
  "v_smfmac_f32_16x16x64_bf16 v[196:199], v[212:215], v[104:111], v251\n\t" \
  "s_waitcnt lgkmcnt(3)\n\t" \
  "v_smfmac_f32_16x16x64_bf16 v[192:195], v[236:239], v[112:119], v251\n\t" \
  "v_smfmac_f32_16x16x64_bf16 v[196:199], v[236:239], v[120:127], v251\n\t" \
  "v_smfmac_f32_16x16x64_bf16 v[192:195], v[240:243], v[128:135], v251\n\t" \
  "v_smfmac_f32_16x16x64_bf16 v[196:199], v[240:243], v[136:143], v251\n\t" \
  "s_waitcnt lgkmcnt(1)\n\t" \
  "v_smfmac_f32_16x16x64_bf16 v[192:195], v[244:247], v[144:151], v251\n\t" \
  "v_smfmac_f32_16x16x64_bf16 v[196:199], v[244:247], v[152:159], v251\n\t" \
  "v_smfmac_f32_16x16x64_bf16 v[192:195], v[200:203], v[160:167], v251\n\t" \
  "v_smfmac_f32_16x16x64_bf16 v[196:199], v[200:203], v[168:175], v251\n\t" \
  "s_waitcnt lgkmcnt(0)\n\t" \
  "v_smfmac_f32_16x16x64_bf16 v[192:195], v[204:207], v[176:183], v251\n\t" \
  "v_smfmac_f32_16x16x64_bf16 v[196:199], v[204:207], v[184:191], v251\n\t" \
  "s_nop 6\n\t" \
  "v_add_f32 v192, v192, v194\n\t" \
  "v_add_f32 v193, v193, v195\n\t" \
  "v_add_f32 v196, v196, v198\n\t" \
  "v_add_f32 v197, v197, v199\n\t" \
  "v_cvt_pk_bf16_f32 v192, v192, v193\n\t" \
  "v_cvt_pk_bf16_f32 v196, v196, v197\n\t" \
  "ds_write2_b32 v252, v192, v196 offset1:64\n\t" \
  "s_waitcnt lgkmcnt(0)\n\t" \
  "s_barrier\n\t" \
  "buffer_store_dword v192, v235, %[rs], %[sk] offen sc1\n\t" \
  "buffer_store_dword v196, v235, %[rs], %[sk] offen offset:256 sc1\n\t" \
  "s_mov_b32 %[spins], s92\n\t" \
  "s_branch .Lend%=\n\t" \
  ".Lslow%=:\n\t" \
  "s_mov_b64 s[94:95], 0\n\t" \
  "v_cmp_eq_u32 vcc, %[sent], v200\n\t" \
  "s_or_b64 s[94:95], s[94:95], vcc\n\t" \
  "v_cmp_eq_u32 vcc, %[sent], v201\n\t" \
  "s_or_b64 s[94:95], s[94:95], vcc\n\t" \
  "v_cmp_eq_u32 vcc, %[sent], v202\n\t" \
  "s_or_b64 s[94:95], s[94:95], vcc\n\t" \
  "v_cmp_eq_u32 vcc, %[sent], v203\n\t" \
  "s_or_b64 s[94:95], s[94:95], vcc\n\t" \
  "v_cmp_eq_u32 vcc, %[sent], v204\n\t" \
  "s_or_b64 s[94:95], s[94:95], vcc\n\t" \
  "v_cmp_eq_u32 vcc, %[sent], v205\n\t" \
  "s_or_b64 s[94:95], s[94:95], vcc\n\t" \
  "v_cmp_eq_u32 vcc, %[sent], v206\n\t" \
  "s_or_b64 s[94:95], s[94:95], vcc\n\t" \
  "v_cmp_eq_u32 vcc, %[sent], v207\n\t" \
  "s_or_b64 s[94:95], s[94:95], vcc\n\t" \
  "s_cmp_eq_u64 s[94:95], 0\n\t" \
  "s_cbranch_scc1 .Lok%=\n\t" \
  "v_mov_b64 v[192:193], 0\n\t" \
  "v_mov_b64 v[194:195], 0\n\t" \
  "v_mov_b64 v[196:197], 0\n\t" \
  "v_mov_b64 v[198:199], 0\n\t" \
  "s_add_u32 s92, s92, 1\n\t" \
  "s_sleep 6\n\t" \
  "s_cmp_eq_u32 s92, 1\n\t" \
  "s_cbranch_scc1 .Lpoll%=\n\t" \
  "s_sleep 1\n\t" \
  "s_and_b32 s93, s92, 63\n\t" \
  "s_cmp_lg_u32 s93, 0\n\t" \
  "s_cbranch_scc1 .Lpoll%=\n\t" \
  "v_mov_b32 v245, 0\n\t" \
  "buffer_load_dword v244, v245, %[rs], %[sab] offen sc1\n\t" \
  "s_waitcnt vmcnt(0)\n\t" \
  "v_readfirstlane_b32 s93, v244\n\t" \
  "s_nop 3\n\t" \
  "s_cmp_eq_u32 s93, 1\n\t" \
  "s_cbranch_scc1 .Lok%=\n\t" \
  "s_cmp_lt_u32 s92, 0x80000\n\t" \
  "s_cbranch_scc1 .Lpoll%=\n\t" \
  "v_mov_b32 v244, 1\n\t" \
  "buffer_store_dword v244, v245, %[rs], %[sab] offen sc1\n\t" \
  "s_branch .Lok%=\n\t" \
  ".Lend%=:\n\t"
#define QK_ASM_6 \
  "s_waitcnt lgkmcnt(0)\n\t" \
  "v_subrev_u32 v244, 0x8000, v250\n\t" \
  "ds_read_b128 v[216:219], v248 offset:57344\n\t" \
  "ds_read_b128 v[220:223], v248 offset:58368\n\t" \
  "ds_read_b128 v[224:227], v248 offset:59392\n\t" \
  "ds_read_b128 v[228:231], v248 offset:60416\n\t" \
  "ds_read_b128 v[208:211], v244 offset:0\n\t" \
  "ds_read_b128 v[212:215], v244 offset:1024\n\t" \
  "ds_read_b128 v[236:239], v244 offset:2048\n\t" \
  "ds_read_b128 v[240:243], v244 offset:3072\n\t" \
  "v_mov_b64 v[192:193], 0\n\t" \
  "v_mov_b64 v[194:195], 0\n\t" \
  "v_mov_b64 v[196:197], 0\n\t" \
  "v_mov_b64 v[198:199], 0\n\t" \
  "s_movk_i32 s92, 0x2000\n\t" \
  ".Lwt%=:\n\t" \
  "ds_read_b64 v[200:201], v248 offset:30720\n\t" \
  "s_memrealtime s[90:91]\n\t" \
  "s_sub_u32 s92, s92, 1\n\t" \
  "s_waitcnt lgkmcnt(0)\n\t" \
  "v_readfirstlane_b32 s93, v200\n\t" \
  "v_readfirstlane_b32 %[tgt], v201\n\t" \
  "s_cbranch_scc1 .Lgo%=\n\t" \
  "s_cmp_eq_u32 s93, %[step]\n\t" \
  "s_cbranch_scc1 .Lwx%=\n\t" \
  "s_sleep 1\n\t" \
  "s_branch .Lwt%=\n\t" \
  ".Lwx%=:\n\t" \
  "s_lshl_b32 s93, s90, 2\n\t" \
  "s_sub_i32 s93, %[tgt], s93\n\t" \
  "s_cmp_le_i32 s93, 0\n\t" \
  "s_cbranch_scc1 .Lgo%=\n\t" \
  "s_min_i32 s93, s93, 8000\n\t" \
  "s_mov_b32 s94, s93\n\t" \
  ".Lsl%=:\n\t" \
  "s_sub_i32 s94, s94, 18\n\t" \
  "s_cmp_lt_i32 s94, 30\n\t" \
  "s_cbranch_scc1 .Lsx%=\n\t" \
  "s_sleep 1\n\t" \
  "s_branch .Lsl%=\n\t" \
  ".Lsx%=:\n\t" \
  "s_sub_i32 s93, s93, 12\n\t" \
  "s_max_i32 s93, s93, 4\n\t" \
  "s_lshr_b32 s93, s93, 2\n\t" \
  "s_add_u32 s93, s93, s90\n\t" \
  "s_memrealtime s[90:91]\n\t" \
  ".Lpace%=:\n\t" \
  "s_memrealtime s[94:95]\n\t" \
  "s_waitcnt lgkmcnt(1)\n\t" \
  "s_sub_i32 s92, s90, s93\n\t" \
  "s_cmp_ge_i32 s92, 0\n\t" \
  "s_cbranch_scc1 .Le%=\n\t" \
  "s_memrealtime s[90:91]\n\t" \
  "s_waitcnt lgkmcnt(1)\n\t" \
  "s_sub_i32 s92, s94, s93\n\t" \
  "s_cmp_lt_i32 s92, 0\n\t" \
  "s_cbranch_scc1 .Lpace%=\n\t" \
  "s_nop 4\n\t" \
  ".Le%=:\n\t" \
  "s_add_u32 s92, s92, s93\n\t" \
  "s_lshl_b32 s92, s92, 2\n\t" \
  "s_sub_i32 s92, %[tgt], s92\n\t" \
  "s_max_i32 s92, s92, 0\n\t" \
  "s_bitcmp0_b32 s92, 3\n\t" \
  "s_cbranch_scc1 .Lf3%=\n\t" \
  "s_nop 15\n\t" \
  ".Lf3%=:\n\t" \
  "s_bitcmp0_b32 s92, 2\n\t" \
  "s_cbranch_scc1 .Lf2%=\n\t" \
  "s_nop 10\n\t" \
  ".Lf2%=:\n\t" \
  ".Lgo%=:\n\t" \
  "s_mov_b32 s92, 0\n\t" \
  ".Lpoll%=:\n\t" \
  "buffer_load_dwordx4 v[200:203], v248, %[rs], %[som] offen sc1\n\t" \
  "buffer_load_dwordx4 v[204:207], v248, %[rs], %[som2] offen sc1\n\t" \
  "s_waitcnt vmcnt(1) lgkmcnt(0)\n\t" \
  "ds_write_b128 v249, v[200:203]\n\t" \
  "v_smfmac_f32_16x16x64_bf16 v[192:195], v[200:203], v[208:215], v251\n\t" \
  "v_max3_u32 v244, v200, v201, v202\n\t" \
  "v_smfmac_f32_16x16x64_bf16 v[196:199], v[200:203], v[236:243], v251\n\t" \
  "s_waitcnt vmcnt(0)\n\t" \
  "ds_write_b128 v249, v[204:207] offset:8192\n\t" \
  "v_smfmac_f32_16x16x64_bf16 v[192:195], v[204:207], v[216:223], v251\n\t" \
  "v_max3_u32 v244, v244, v203, v204\n\t" \
  "v_max3_u32 v244, v244, v205, v206\n\t" \
  "v_smfmac_f32_16x16x64_bf16 v[196:199], v[204:207], v[224:231], v251\n\t" \
  "v_max_u32 v244, v244, v207\n\t" \
  "v_cmp_le_u32 vcc, %[sent], v244\n\t" \
  "s_cbranch_vccnz .Lslow%=\n\t" \
  ".Lok%=:\n\t" \
  "ds_read_b128 v[216:219], v250 offset:32768\n\t" \
  "ds_read_b128 v[220:223], v250 offset:33792\n\t" \
  "ds_read_b128 v[224:227], v250 offset:34816\n\t" \
  "ds_read_b128 v[228:231], v250 offset:35840\n\t" \
  "s_waitcnt lgkmcnt(4)\n\t" \
  "s_barrier\n\t" \
  "ds_read_b128 v[208:211], v248 offset:7168\n\t" \
  "ds_read_b128 v[212:215], v248 offset:8192\n\t" \
  "ds_read_b128 v[236:239], v248 offset:9216\n\t" \
  "ds_read_b128 v[240:243], v248 offset:10240\n\t" \
  "s_waitcnt lgkmcnt(3)\n\t" \
  "ds_read_b128 v[244:247], v248 offset:13312\n\t" \
  "v_smfmac_f32_16x16x64_bf16 v[192:195], v[208:211], v[216:223], v251\n\t" \
  "ds_read_b128 v[216:219], v250 offset:0\n\t" \
  "v_smfmac_f32_16x16x64_bf16 v[196:199], v[208:211], v[224:231], v251\n\t" \
  "s_waitcnt lgkmcnt(4)\n\t" \
  "ds_read_b128 v[220:223], v250 offset:1024\n\t" \
  "v_smfmac_f32_16x16x64_bf16 v[192:195], v[212:215], v[0:7], v251\n\t" \
  "ds_read_b128 v[224:227], v250 offset:2048\n\t" \
  "v_smfmac_f32_16x16x64_bf16 v[196:199], v[212:215], v[8:15], v251\n\t" \
  "s_waitcnt lgkmcnt(5)\n\t" \
  "ds_read_b128 v[228:231], v250 offset:3072\n\t" \
  "v_smfmac_f32_16x16x64_bf16 v[192:195], v[236:239], v[16:23], v251\n\t" \
  "ds_read_b128 v[200:203], v248 offset:11264\n\t" \
  "v_smfmac_f32_16x16x64_bf16 v[196:199], v[236:239], v[24:31], v251\n\t" \
  "s_waitcnt lgkmcnt(6)\n\t" \
  "ds_read_b128 v[204:207], v248 offset:12288\n\t" \
  "v_smfmac_f32_16x16x64_bf16 v[192:195], v[240:243], v[32:39], v251\n\t" \
  "ds_read_b128 v[208:211], v248 offset:15360\n\t" \
  "v_smfmac_f32_16x16x64_bf16 v[196:199], v[240:243], v[40:47], v251\n\t" \
  "s_waitcnt lgkmcnt(5)\n\t" \
  "ds_read_b128 v[212:215], v248 offset:0\n\t" \
  "v_smfmac_f32_16x16x64_bf16 v[192:195], v[244:247], v[216:223], v251\n\t" \
  "s_waitcnt lgkmcnt(4)\n\t" \
  "v_smfmac_f32_16x16x64_bf16 v[196:199], v[244:247], v[224:231], v251\n\t" \
  "s_waitcnt lgkmcnt(3)\n\t" \
  "ds_read_b128 v[236:239], v248 offset:1024\n\t" \
  "v_smfmac_f32_16x16x64_bf16 v[192:195], v[200:203], v[48:55], v251\n\t" \
  "ds_read_b128 v[240:243], v248 offset:2048\n\t" \
  "v_smfmac_f32_16x16x64_bf16 v[196:199], v[200:203], v[56:63], v251\n\t" \
  "s_waitcnt lgkmcnt(4)\n\t" \
  "v_smfmac_f32_16x16x64_bf16 v[192:195], v[204:207], v[64:71], v251\n\t" \
  "ds_read_b128 v[244:247], v248 offset:3072\n\t" \
  "v_smfmac_f32_16x16x64_bf16 v[196:199], v[204:207], v[72:79], v251\n\t" \
  "s_waitcnt lgkmcnt(4)\n\t" \
  "v_smfmac_f32_16x16x64_bf16 v[192:195], v[208:211], v[80:87], v251\n\t" \
  "ds_read_b128 v[200:203], v248 offset:4096\n\t" \
  "v_smfmac_f32_16x16x64_bf16 v[196:199], v[208:211], v[88:95], v251\n\t" \
  "s_waitcnt lgkmcnt(4)\n\t" \
  "v_smfmac_f32_16x16x64_bf16 v[192:195], v[212:215], v[96:103], v251\n\t" \
  "ds_read_b128 v[204:207], v248 offset:5120\n\t" \
  "v_smfmac_f32_16x16x64_bf16 v[196:199], v[212:215], v[104:111], v251\n\t" \
  "s_waitcnt lgkmcnt(3)\n\t" \
  "v_smfmac_f32_16x16x64_bf16 v[192:195], v[236:239], v[112:119], v251\n\t" \
  "v_smfmac_f32_16x16x64_bf16 v[196:199], v[236:239], v[120:127], v251\n\t" \
  "v_smfmac_f32_16x16x64_bf16 v[192:195], v[240:243], v[128:135], v251\n\t" \
  "v_smfmac_f32_16x16x64_bf16 v[196:199], v[240:243], v[136:143], v251\n\t" \
  "s_waitcnt lgkmcnt(1)\n\t" \
  "v_smfmac_f32_16x16x64_bf16 v[192:195], v[244:247], v[144:151], v251\n\t" \
  "v_smfmac_f32_16x16x64_bf16 v[196:199], v[244:247], v[152:159], v251\n\t" \
  "v_smfmac_f32_16x16x64_bf16 v[192:195], v[200:203], v[160:167], v251\n\t" \
  "v_smfmac_f32_16x16x64_bf16 v[196:199], v[200:203], v[168:175], v251\n\t" \
  "s_waitcnt lgkmcnt(0)\n\t" \
  "v_smfmac_f32_16x16x64_bf16 v[192:195], v[204:207], v[176:183], v251\n\t" \
  "v_smfmac_f32_16x16x64_bf16 v[196:199], v[204:207], v[184:191], v251\n\t" \
  "s_nop 6\n\t" \
  "v_add_f32 v192, v192, v194\n\t" \
  "v_add_f32 v193, v193, v195\n\t" \
  "v_add_f32 v196, v196, v198\n\t" \
  "v_add_f32 v197, v197, v199\n\t" \
  "v_cvt_pk_bf16_f32 v192, v192, v193\n\t" \
  "v_cvt_pk_bf16_f32 v196, v196, v197\n\t" \
  "ds_write2_b32 v252, v192, v196 offset1:64\n\t" \
  "s_waitcnt lgkmcnt(0)\n\t" \
  "s_barrier\n\t" \
  "buffer_store_dword v192, v235, %[rs], %[sk] offen sc1\n\t" \
  "buffer_store_dword v196, v235, %[rs], %[sk] offen offset:256 sc1\n\t" \
  "s_mov_b32 %[spins], s92\n\t" \
  "s_branch .Lend%=\n\t" \
  ".Lslow%=:\n\t" \
  "s_mov_b64 s[94:95], 0\n\t" \
  "v_cmp_eq_u32 vcc, %[sent], v200\n\t" \
  "s_or_b64 s[94:95], s[94:95], vcc\n\t" \
  "v_cmp_eq_u32 vcc, %[sent], v201\n\t" \
  "s_or_b64 s[94:95], s[94:95], vcc\n\t" \
  "v_cmp_eq_u32 vcc, %[sent], v202\n\t" \
  "s_or_b64 s[94:95], s[94:95], vcc\n\t" \
  "v_cmp_eq_u32 vcc, %[sent], v203\n\t" \
  "s_or_b64 s[94:95], s[94:95], vcc\n\t" \
  "v_cmp_eq_u32 vcc, %[sent], v204\n\t" \
  "s_or_b64 s[94:95], s[94:95], vcc\n\t" \
  "v_cmp_eq_u32 vcc, %[sent], v205\n\t" \
  "s_or_b64 s[94:95], s[94:95], vcc\n\t" \
  "v_cmp_eq_u32 vcc, %[sent], v206\n\t" \
  "s_or_b64 s[94:95], s[94:95], vcc\n\t" \
  "v_cmp_eq_u32 vcc, %[sent], v207\n\t" \
  "s_or_b64 s[94:95], s[94:95], vcc\n\t" \
  "s_cmp_eq_u64 s[94:95], 0\n\t" \
  "s_cbranch_scc1 .Lok%=\n\t" \
  "v_mov_b64 v[192:193], 0\n\t" \
  "v_mov_b64 v[194:195], 0\n\t" \
  "v_mov_b64 v[196:197], 0\n\t" \
  "v_mov_b64 v[198:199], 0\n\t" \
  "s_add_u32 s92, s92, 1\n\t" \
  "s_sleep 6\n\t" \
  "s_cmp_eq_u32 s92, 1\n\t" \
  "s_cbranch_scc1 .Lpoll%=\n\t" \
  "s_sleep 1\n\t" \
  "s_and_b32 s93, s92, 63\n\t" \
  "s_cmp_lg_u32 s93, 0\n\t" \
  "s_cbranch_scc1 .Lpoll%=\n\t" \
  "v_mov_b32 v245, 0\n\t" \
  "buffer_load_dword v244, v245, %[rs], %[sab] offen sc1\n\t" \
  "s_waitcnt vmcnt(0)\n\t" \
  "v_readfirstlane_b32 s93, v244\n\t" \
  "s_nop 3\n\t" \
  "s_cmp_eq_u32 s93, 1\n\t" \
  "s_cbranch_scc1 .Lok%=\n\t" \
  "s_cmp_lt_u32 s92, 0x80000\n\t" \
  "s_cbranch_scc1 .Lpoll%=\n\t" \
  "v_mov_b32 v244, 1\n\t" \
  "buffer_store_dword v244, v245, %[rs], %[sab] offen sc1\n\t" \
  "s_branch .Lok%=\n\t" \
  ".Lend%=:\n\t"
#define QK_ASM_7 \
  "s_waitcnt lgkmcnt(0)\n\t" \
  "v_subrev_u32 v244, 0x8000, v250\n\t" \
  "ds_read_b128 v[216:219], v248 offset:61440\n\t" \
  "ds_read_b128 v[220:223], v248 offset:62464\n\t" \
  "ds_read_b128 v[224:227], v248 offset:63488\n\t" \
  "ds_read_b128 v[228:231], v248 offset:64512\n\t" \
  "ds_read_b128 v[208:211], v244 offset:0\n\t" \
  "ds_read_b128 v[212:215], v244 offset:1024\n\t" \
  "ds_read_b128 v[236:239], v244 offset:2048\n\t" \
  "ds_read_b128 v[240:243], v244 offset:3072\n\t" \
  "v_mov_b64 v[192:193], 0\n\t" \
  "v_mov_b64 v[194:195], 0\n\t" \
  "v_mov_b64 v[196:197], 0\n\t" \
  "v_mov_b64 v[198:199], 0\n\t" \
  "s_movk_i32 s92, 0x2000\n\t" \
  ".Lwt%=:\n\t" \
  "ds_read_b64 v[200:201], v248 offset:30720\n\t" \
  "s_memrealtime s[90:91]\n\t" \
  "s_sub_u32 s92, s92, 1\n\t" \
  "s_waitcnt lgkmcnt(0)\n\t" \
  "v_readfirstlane_b32 s93, v200\n\t" \
  "v_readfirstlane_b32 %[tgt], v201\n\t" \
  "s_cbranch_scc1 .Lgo%=\n\t" \
  "s_cmp_eq_u32 s93, %[step]\n\t" \
  "s_cbranch_scc1 .Lwx%=\n\t" \
  "s_sleep 1\n\t" \
  "s_branch .Lwt%=\n\t" \
  ".Lwx%=:\n\t" \
  "s_lshl_b32 s93, s90, 2\n\t" \
  "s_sub_i32 s93, %[tgt], s93\n\t" \
  "s_cmp_le_i32 s93, 0\n\t" \
  "s_cbranch_scc1 .Lgo%=\n\t" \
  "s_min_i32 s93, s93, 8000\n\t" \
  "s_mov_b32 s94, s93\n\t" \
  ".Lsl%=:\n\t" \
  "s_sub_i32 s94, s94, 18\n\t" \
  "s_cmp_lt_i32 s94, 30\n\t" \
  "s_cbranch_scc1 .Lsx%=\n\t" \
  "s_sleep 1\n\t" \
  "s_branch .Lsl%=\n\t" \
  ".Lsx%=:\n\t" \
  "s_sub_i32 s93, s93, 12\n\t" \
  "s_max_i32 s93, s93, 4\n\t" \
  "s_lshr_b32 s93, s93, 2\n\t" \
  "s_add_u32 s93, s93, s90\n\t" \
  "s_memrealtime s[90:91]\n\t" \
  ".Lpace%=:\n\t" \
  "s_memrealtime s[94:95]\n\t" \
  "s_waitcnt lgkmcnt(1)\n\t" \
  "s_sub_i32 s92, s90, s93\n\t" \
  "s_cmp_ge_i32 s92, 0\n\t" \
  "s_cbranch_scc1 .Le%=\n\t" \
  "s_memrealtime s[90:91]\n\t" \
  "s_waitcnt lgkmcnt(1)\n\t" \
  "s_sub_i32 s92, s94, s93\n\t" \
  "s_cmp_lt_i32 s92, 0\n\t" \
  "s_cbranch_scc1 .Lpace%=\n\t" \
  "s_nop 4\n\t" \
  ".Le%=:\n\t" \
  "s_add_u32 s92, s92, s93\n\t" \
  "s_lshl_b32 s92, s92, 2\n\t" \
  "s_sub_i32 s92, %[tgt], s92\n\t" \
  "s_max_i32 s92, s92, 0\n\t" \
  "s_bitcmp0_b32 s92, 3\n\t" \
  "s_cbranch_scc1 .Lf3%=\n\t" \
  "s_nop 15\n\t" \
  ".Lf3%=:\n\t" \
  "s_bitcmp0_b32 s92, 2\n\t" \
  "s_cbranch_scc1 .Lf2%=\n\t" \
  "s_nop 10\n\t" \
  ".Lf2%=:\n\t" \
  ".Lgo%=:\n\t" \
  "s_mov_b32 s92, 0\n\t" \
  "s_memtime s[88:89]\n\t" \
  ".Lpoll%=:\n\t" \
  "buffer_load_dwordx4 v[200:203], v248, %[rs], %[som] offen sc1\n\t" \
  "buffer_load_dwordx4 v[204:207], v248, %[rs], %[som2] offen sc1\n\t" \
  "s_waitcnt vmcnt(1) lgkmcnt(0)\n\t" \
  "ds_write_b128 v249, v[200:203]\n\t" \
  "v_smfmac_f32_16x16x64_bf16 v[192:195], v[200:203], v[208:215], v251\n\t" \
  "v_max3_u32 v244, v200, v201, v202\n\t" \
  "v_smfmac_f32_16x16x64_bf16 v[196:199], v[200:203], v[236:243], v251\n\t" \
  "s_waitcnt vmcnt(0)\n\t" \
  "ds_write_b128 v249, v[204:207] offset:8192\n\t" \
  "v_smfmac_f32_16x16x64_bf16 v[192:195], v[204:207], v[216:223], v251\n\t" \
  "v_max3_u32 v244, v244, v203, v204\n\t" \
  "v_max3_u32 v244, v244, v205, v206\n\t" \
  "v_smfmac_f32_16x16x64_bf16 v[196:199], v[204:207], v[224:231], v251\n\t" \
  "v_max_u32 v244, v244, v207\n\t" \
  "v_cmp_le_u32 vcc, %[sent], v244\n\t" \
  "s_cbranch_vccnz .Lslow%=\n\t" \
  ".Lok%=:\n\t" \
  "ds_read_b128 v[216:219], v250 offset:32768\n\t" \
  "ds_read_b128 v[220:223], v250 offset:33792\n\t" \
  "ds_read_b128 v[224:227], v250 offset:34816\n\t" \
  "ds_read_b128 v[228:231], v250 offset:35840\n\t" \
  "s_waitcnt lgkmcnt(4)\n\t" \
  "s_setprio 3\n\t" \
  "s_barrier\n\t" \
  "s_memrealtime s[90:91]\n\t" \
  "s_memtime s[94:95]\n\t" \
  "v_mov_b32 v232, %[step]\n\t" \
  "v_lshrrev_b32 v234, 1, v248\n\t" \
  "s_mov_b64 exec, 0xffff\n\t" \
  "s_waitcnt lgkmcnt(0)\n\t" \
  "s_lshl_b32 s90, s90, 2\n\t" \
  "s_sub_u32 s94, s94, s88\n\t" \
  "s_mul_i32 s94, s94, 12200\n\t" \
  "s_lshr_b32 s94, s94, 16\n\t" \
  "s_add_u32 s94, s94, %[tgt]\n\t" \
  "s_sub_i32 s93, s90, s94\n\t" \
  "s_sub_i32 s93, s93, 30\n\t" \
  "s_max_i32 s93, s93, 0\n\t" \
  "s_add_u32 s90, s94, s93\n\t" \
  "s_sub_i32 s93, s90, %[tgt]\n\t" \
  "s_cmp_gt_i32 s93, 270\n\t" \
  "s_cselect_b32 s93, 90, 0\n\t" \
  "s_add_u32 s93, s93, s90\n\t" \
  "s_add_u32 s93, s93, %[lead]\n\t" \
  "v_mov_b32 v233, s93\n\t" \
  "buffer_store_dwordx2 v[232:233], v234, %[rs], %[sbc] offen sc1\n\t" \
  "s_mov_b64 exec, -1\n\t" \
  "s_mov_b32 %[tb1], s90\n\t" \
  "s_setprio 0\n\t" \
  "ds_read_b128 v[208:211], v248 offset:6144\n\t" \
  "ds_read_b128 v[212:215], v248 offset:9216\n\t" \
  "ds_read_b128 v[236:239], v248 offset:10240\n\t" \
  "ds_read_b128 v[240:243], v248 offset:11264\n\t" \
  "s_waitcnt lgkmcnt(3)\n\t" \
  "ds_read_b128 v[244:247], v248 offset:12288\n\t" \
  "v_smfmac_f32_16x16x64_bf16 v[192:195], v[208:211], v[216:223], v251\n\t" \
  "ds_read_b128 v[216:219], v250 offset:0\n\t" \
  "v_smfmac_f32_16x16x64_bf16 v[196:199], v[208:211], v[224:231], v251\n\t" \
  "s_waitcnt lgkmcnt(4)\n\t" \
  "ds_read_b128 v[220:223], v250 offset:1024\n\t" \
  "v_smfmac_f32_16x16x64_bf16 v[192:195], v[212:215], v[0:7], v251\n\t" \
  "ds_read_b128 v[224:227], v250 offset:2048\n\t" \
  "v_smfmac_f32_16x16x64_bf16 v[196:199], v[212:215], v[8:15], v251\n\t" \
  "s_waitcnt lgkmcnt(5)\n\t" \
  "ds_read_b128 v[228:231], v250 offset:3072\n\t" \
  "v_smfmac_f32_16x16x64_bf16 v[192:195], v[236:239], v[16:23], v251\n\t" \
  "ds_read_b128 v[200:203], v248 offset:13312\n\t" \
  "v_smfmac_f32_16x16x64_bf16 v[196:199], v[236:239], v[24:31], v251\n\t" \
  "s_waitcnt lgkmcnt(6)\n\t" \
  "ds_read_b128 v[204:207], v248 offset:14336\n\t" \
  "v_smfmac_f32_16x16x64_bf16 v[192:195], v[240:243], v[32:39], v251\n\t" \
  "ds_read_b128 v[208:211], v248 offset:0\n\t" \
  "v_smfmac_f32_16x16x64_bf16 v[196:199], v[240:243], v[40:47], v251\n\t" \
  "s_waitcnt lgkmcnt(5)\n\t" \
  "ds_read_b128 v[212:215], v248 offset:1024\n\t" \
  "v_smfmac_f32_16x16x64_bf16 v[192:195], v[244:247], v[216:223], v251\n\t" \
  "s_waitcnt lgkmcnt(4)\n\t" \
  "v_smfmac_f32_16x16x64_bf16 v[196:199], v[244:247], v[224:231], v251\n\t" \
  "s_waitcnt lgkmcnt(3)\n\t" \
  "ds_read_b128 v[236:239], v248 offset:2048\n\t" \
  "v_smfmac_f32_16x16x64_bf16 v[192:195], v[200:203], v[48:55], v251\n\t" \
  "ds_read_b128 v[240:243], v248 offset:3072\n\t" \
  "v_smfmac_f32_16x16x64_bf16 v[196:199], v[200:203], v[56:63], v251\n\t" \
  "s_waitcnt lgkmcnt(4)\n\t" \
  "v_smfmac_f32_16x16x64_bf16 v[192:195], v[204:207], v[64:71], v251\n\t" \
  "ds_read_b128 v[244:247], v248 offset:4096\n\t" \
  "v_smfmac_f32_16x16x64_bf16 v[196:199], v[204:207], v[72:79], v251\n\t" \
  "s_waitcnt lgkmcnt(4)\n\t" \
  "v_smfmac_f32_16x16x64_bf16 v[192:195], v[208:211], v[80:87], v251\n\t" \
  "ds_read_b128 v[200:203], v248 offset:5120\n\t" \
  "v_smfmac_f32_16x16x64_bf16 v[196:199], v[208:211], v[88:95], v251\n\t" \
  "s_waitcnt lgkmcnt(4)\n\t" \
  "v_smfmac_f32_16x16x64_bf16 v[192:195], v[212:215], v[96:103], v251\n\t" \
  "ds_read_b128 v[204:207], v248 offset:8192\n\t" \
  "v_smfmac_f32_16x16x64_bf16 v[196:199], v[212:215], v[104:111], v251\n\t" \
  "s_waitcnt lgkmcnt(3)\n\t" \
  "v_smfmac_f32_16x16x64_bf16 v[192:195], v[236:239], v[112:119], v251\n\t" \
  "v_smfmac_f32_16x16x64_bf16 v[196:199], v[236:239], v[120:127], v251\n\t" \
  "v_smfmac_f32_16x16x64_bf16 v[192:195], v[240:243], v[128:135], v251\n\t" \
  "v_smfmac_f32_16x16x64_bf16 v[196:199], v[240:243], v[136:143], v251\n\t" \
  "s_waitcnt lgkmcnt(1)\n\t" \
  "v_smfmac_f32_16x16x64_bf16 v[192:195], v[244:247], v[144:151], v251\n\t" \
  "v_smfmac_f32_16x16x64_bf16 v[196:199], v[244:247], v[152:159], v251\n\t" \
  "v_smfmac_f32_16x16x64_bf16 v[192:195], v[200:203], v[160:167], v251\n\t" \
  "v_smfmac_f32_16x16x64_bf16 v[196:199], v[200:203], v[168:175], v251\n\t" \
  "s_waitcnt lgkmcnt(0)\n\t" \
  "v_smfmac_f32_16x16x64_bf16 v[192:195], v[204:207], v[176:183], v251\n\t" \
  "v_smfmac_f32_16x16x64_bf16 v[196:199], v[204:207], v[184:191], v251\n\t" \
  "s_nop 6\n\t" \
  "v_add_f32 v192, v192, v194\n\t" \
  "v_add_f32 v193, v193, v195\n\t" \
  "v_add_f32 v196, v196, v198\n\t" \
  "v_add_f32 v197, v197, v199\n\t" \
  "v_cvt_pk_bf16_f32 v192, v192, v193\n\t" \
  "v_cvt_pk_bf16_f32 v196, v196, v197\n\t" \
  "ds_write2_b32 v252, v192, v196 offset1:64\n\t" \
  "s_waitcnt lgkmcnt(0)\n\t" \
  "s_barrier\n\t" \
  "buffer_store_dword v192, v235, %[rs], %[sk] offen sc1\n\t" \
  "buffer_store_dword v196, v235, %[rs], %[sk] offen offset:256 sc1\n\t" \
  "s_mov_b32 %[spins], s92\n\t" \
  "s_branch .Lend%=\n\t" \
  ".Lslow%=:\n\t" \
  "s_mov_b64 s[94:95], 0\n\t" \
  "v_cmp_eq_u32 vcc, %[sent], v200\n\t" \
  "s_or_b64 s[94:95], s[94:95], vcc\n\t" \
  "v_cmp_eq_u32 vcc, %[sent], v201\n\t" \
  "s_or_b64 s[94:95], s[94:95], vcc\n\t" \
  "v_cmp_eq_u32 vcc, %[sent], v202\n\t" \
  "s_or_b64 s[94:95], s[94:95], vcc\n\t" \
  "v_cmp_eq_u32 vcc, %[sent], v203\n\t" \
  "s_or_b64 s[94:95], s[94:95], vcc\n\t" \
  "v_cmp_eq_u32 vcc, %[sent], v204\n\t" \
  "s_or_b64 s[94:95], s[94:95], vcc\n\t" \
  "v_cmp_eq_u32 vcc, %[sent], v205\n\t" \
  "s_or_b64 s[94:95], s[94:95], vcc\n\t" \
  "v_cmp_eq_u32 vcc, %[sent], v206\n\t" \
  "s_or_b64 s[94:95], s[94:95], vcc\n\t" \
  "v_cmp_eq_u32 vcc, %[sent], v207\n\t" \
  "s_or_b64 s[94:95], s[94:95], vcc\n\t" \
  "s_cmp_eq_u64 s[94:95], 0\n\t" \
  "s_cbranch_scc1 .Lok%=\n\t" \
  "v_mov_b64 v[192:193], 0\n\t" \
  "v_mov_b64 v[194:195], 0\n\t" \
  "v_mov_b64 v[196:197], 0\n\t" \
  "v_mov_b64 v[198:199], 0\n\t" \
  "s_add_u32 s92, s92, 1\n\t" \
  "s_sleep 6\n\t" \
  "s_cmp_eq_u32 s92, 1\n\t" \
  "s_cbranch_scc1 .Lpoll%=\n\t" \
  "s_sleep 1\n\t" \
  "s_and_b32 s93, s92, 63\n\t" \
  "s_cmp_lg_u32 s93, 0\n\t" \
  "s_cbranch_scc1 .Lpoll%=\n\t" \
  "v_mov_b32 v245, 0\n\t" \
  "buffer_load_dword v244, v245, %[rs], %[sab] offen sc1\n\t" \
  "s_waitcnt vmcnt(0)\n\t" \
  "v_readfirstlane_b32 s93, v244\n\t" \
  "s_nop 3\n\t" \
  "s_cmp_eq_u32 s93, 1\n\t" \
  "s_cbranch_scc1 .Lok%=\n\t" \
  "s_cmp_lt_u32 s92, 0x80000\n\t" \
  "s_cbranch_scc1 .Lpoll%=\n\t" \
  "v_mov_b32 v244, 1\n\t" \
  "buffer_store_dword v244, v245, %[rs], %[sab] offen sc1\n\t" \
  "s_branch .Lok%=\n\t" \
  ".Lend%=:\n\t"
#define VV_ASM \
  "s_waitcnt lgkmcnt(0)\n\t" \
  "v_mov_b64 v[128:129], 0\n\t" \
  "v_mov_b64 v[130:131], 0\n\t" \
  "s_movk_i32 s92, 0x2000\n\t" \
  ".Lwt%=:\n\t" \
  "ds_read_b64 v[188:189], v200 offset:30720\n\t" \
  "s_memrealtime s[90:91]\n\t" \
  "s_sub_u32 s92, s92, 1\n\t" \
  "s_waitcnt lgkmcnt(0)\n\t" \
  "v_readfirstlane_b32 s93, v188\n\t" \
  "v_readfirstlane_b32 %[tgt], v189\n\t" \
  "s_cbranch_scc1 .Lgo%=\n\t" \
  "s_cmp_eq_u32 s93, %[step]\n\t" \
  "s_cbranch_scc1 .Lwx%=\n\t" \
  "s_sleep 1\n\t" \
  "s_branch .Lwt%=\n\t" \
  ".Lwx%=:\n\t" \
  "s_lshl_b32 s93, s90, 2\n\t" \
  "s_sub_i32 s93, %[tgt], s93\n\t" \
  "s_cmp_le_i32 s93, 0\n\t" \
  "s_cbranch_scc1 .Lgo%=\n\t" \
  "s_min_i32 s93, s93, 8000\n\t" \
  "s_mov_b32 s94, s93\n\t" \
  ".Lsl%=:\n\t" \
  "s_sub_i32 s94, s94, 18\n\t" \
  "s_cmp_lt_i32 s94, 30\n\t" \
  "s_cbranch_scc1 .Lsx%=\n\t" \
  "s_sleep 1\n\t" \
  "s_branch .Lsl%=\n\t" \
  ".Lsx%=:\n\t" \
  "s_sub_i32 s93, s93, 12\n\t" \
  "s_max_i32 s93, s93, 4\n\t" \
  "s_lshr_b32 s93, s93, 2\n\t" \
  "s_add_u32 s93, s93, s90\n\t" \
  "s_memrealtime s[90:91]\n\t" \
  ".Lpace%=:\n\t" \
  "s_memrealtime s[94:95]\n\t" \
  "s_waitcnt lgkmcnt(1)\n\t" \
  "s_sub_i32 s92, s90, s93\n\t" \
  "s_cmp_ge_i32 s92, 0\n\t" \
  "s_cbranch_scc1 .Le%=\n\t" \
  "s_memrealtime s[90:91]\n\t" \
  "s_waitcnt lgkmcnt(1)\n\t" \
  "s_sub_i32 s92, s94, s93\n\t" \
  "s_cmp_lt_i32 s92, 0\n\t" \
  "s_cbranch_scc1 .Lpace%=\n\t" \
  "s_nop 4\n\t" \
  ".Le%=:\n\t" \
  "s_add_u32 s92, s92, s93\n\t" \
  "s_lshl_b32 s92, s92, 2\n\t" \
  "s_sub_i32 s92, %[tgt], s92\n\t" \
  "s_max_i32 s92, s92, 0\n\t" \
  "s_bitcmp0_b32 s92, 3\n\t" \
  "s_cbranch_scc1 .Lf3%=\n\t" \
  "s_nop 15\n\t" \
  ".Lf3%=:\n\t" \
  "s_bitcmp0_b32 s92, 2\n\t" \
  "s_cbranch_scc1 .Lf2%=\n\t" \
  "s_nop 10\n\t" \
  ".Lf2%=:\n\t" \
  ".Lgo%=:\n\t" \
  "s_mov_b32 s92, 0\n\t" \
  ".Lpoll%=:\n\t" \
  "buffer_load_dwordx4 v[188:191], v200, %[rs], %[som] offen sc1\n\t" \
  "buffer_load_dwordx4 v[192:195], v200, %[rs], %[som] offen offset:1024 sc1\n\t" \
  "s_waitcnt vmcnt(1) lgkmcnt(0)\n\t" \
  "ds_write_b128 v201, v[188:191]\n\t" \
  "v_max3_u32 v196, v188, v189, v190\n\t" \
  "s_waitcnt vmcnt(0)\n\t" \
  "ds_write_b128 v201, v[192:195] offset:1024\n\t" \
  "v_max3_u32 v196, v196, v191, v192\n\t" \
  "v_max3_u32 v196, v196, v193, v194\n\t" \
  "v_max_u32 v196, v196, v195\n\t" \
  "v_cmp_le_u32 vcc, %[sent], v196\n\t" \
  "s_cbranch_vccnz .Lslow%=\n\t" \
  ".Lok%=:\n\t" \
  "s_waitcnt lgkmcnt(0)\n\t" \
  "s_barrier\n\t" \
  "ds_read_b128 v[132:135], v200 offset:0\n\t" \
  "ds_read_b128 v[136:139], v200 offset:1024\n\t" \
  "ds_read_b128 v[140:143], v200 offset:2048\n\t" \
  "ds_read_b128 v[144:147], v200 offset:3072\n\t" \
  "ds_read_b128 v[148:151], v200 offset:4096\n\t" \
  "s_waitcnt lgkmcnt(4)\n\t" \
  "ds_read_b128 v[152:155], v200 offset:5120\n\t" \
  "v_smfmac_f32_16x16x64_bf16 v[128:131], v[132:135], v[0:7], v202\n\t" \
  "s_waitcnt lgkmcnt(4)\n\t" \
  "ds_read_b128 v[156:159], v200 offset:6144\n\t" \
  "v_smfmac_f32_16x16x64_bf16 v[128:131], v[136:139], v[8:15], v202\n\t" \
  "s_waitcnt lgkmcnt(4)\n\t" \
  "ds_read_b128 v[160:163], v200 offset:7168\n\t" \
  "v_smfmac_f32_16x16x64_bf16 v[128:131], v[140:143], v[16:23], v202\n\t" \
  "s_waitcnt lgkmcnt(4)\n\t" \
  "ds_read_b128 v[164:167], v200 offset:8192\n\t" \
  "v_smfmac_f32_16x16x64_bf16 v[128:131], v[144:147], v[24:31], v202\n\t" \
  "s_waitcnt lgkmcnt(4)\n\t" \
  "ds_read_b128 v[168:171], v200 offset:9216\n\t" \
  "v_smfmac_f32_16x16x64_bf16 v[128:131], v[148:151], v[32:39], v202\n\t" \
  "s_waitcnt lgkmcnt(4)\n\t" \
  "ds_read_b128 v[172:175], v200 offset:10240\n\t" \
  "v_smfmac_f32_16x16x64_bf16 v[128:131], v[152:155], v[40:47], v202\n\t" \
  "s_waitcnt lgkmcnt(4)\n\t" \
  "ds_read_b128 v[176:179], v200 offset:11264\n\t" \
  "v_smfmac_f32_16x16x64_bf16 v[128:131], v[156:159], v[48:55], v202\n\t" \
  "s_waitcnt lgkmcnt(4)\n\t" \
  "ds_read_b128 v[180:183], v200 offset:12288\n\t" \
  "v_smfmac_f32_16x16x64_bf16 v[128:131], v[160:163], v[56:63], v202\n\t" \
  "s_waitcnt lgkmcnt(4)\n\t" \
  "ds_read_b128 v[184:187], v200 offset:13312\n\t" \
  "v_smfmac_f32_16x16x64_bf16 v[128:131], v[164:167], v[64:71], v202\n\t" \
  "s_waitcnt lgkmcnt(4)\n\t" \
  "ds_read_b128 v[188:191], v200 offset:14336\n\t" \
  "v_smfmac_f32_16x16x64_bf16 v[128:131], v[168:171], v[72:79], v202\n\t" \
  "s_waitcnt lgkmcnt(4)\n\t" \
  "ds_read_b128 v[192:195], v200 offset:15360\n\t" \
  "v_smfmac_f32_16x16x64_bf16 v[128:131], v[172:175], v[80:87], v202\n\t" \
  "s_waitcnt lgkmcnt(4)\n\t" \
  "v_smfmac_f32_16x16x64_bf16 v[128:131], v[176:179], v[88:95], v202\n\t" \
  "s_waitcnt lgkmcnt(3)\n\t" \
  "v_smfmac_f32_16x16x64_bf16 v[128:131], v[180:183], v[96:103], v202\n\t" \
  "s_waitcnt lgkmcnt(2)\n\t" \
  "v_smfmac_f32_16x16x64_bf16 v[128:131], v[184:187], v[104:111], v202\n\t" \
  "s_waitcnt lgkmcnt(1)\n\t" \
  "v_smfmac_f32_16x16x64_bf16 v[128:131], v[188:191], v[112:119], v202\n\t" \
  "s_waitcnt lgkmcnt(0)\n\t" \
  "v_smfmac_f32_16x16x64_bf16 v[128:131], v[192:195], v[120:127], v202\n\t" \
  "s_nop 7\n\t" \
  "v_add_f32 v128, v128, v130\n\t" \
  "v_add_f32 v129, v129, v131\n\t" \
  "v_cvt_pk_bf16_f32 v128, v128, v129\n\t" \
  "buffer_store_dword v128, v203, %[rs], %[sv] offen sc1\n\t" \
  "ds_write_b32 v204, v128\n\t" \
  "s_waitcnt lgkmcnt(0)\n\t" \
  "s_barrier\n\t" \
  "s_mov_b32 %[spins], s92\n\t" \
  "s_branch .Lend%=\n\t" \
  ".Lslow%=:\n\t" \
  "s_mov_b64 s[94:95], 0\n\t" \
  "v_cmp_eq_u32 vcc, %[sent], v188\n\t" \
  "s_or_b64 s[94:95], s[94:95], vcc\n\t" \
  "v_cmp_eq_u32 vcc, %[sent], v189\n\t" \
  "s_or_b64 s[94:95], s[94:95], vcc\n\t" \
  "v_cmp_eq_u32 vcc, %[sent], v190\n\t" \
  "s_or_b64 s[94:95], s[94:95], vcc\n\t" \
  "v_cmp_eq_u32 vcc, %[sent], v191\n\t" \
  "s_or_b64 s[94:95], s[94:95], vcc\n\t" \
  "v_cmp_eq_u32 vcc, %[sent], v192\n\t" \
  "s_or_b64 s[94:95], s[94:95], vcc\n\t" \
  "v_cmp_eq_u32 vcc, %[sent], v193\n\t" \
  "s_or_b64 s[94:95], s[94:95], vcc\n\t" \
  "v_cmp_eq_u32 vcc, %[sent], v194\n\t" \
  "s_or_b64 s[94:95], s[94:95], vcc\n\t" \
  "v_cmp_eq_u32 vcc, %[sent], v195\n\t" \
  "s_or_b64 s[94:95], s[94:95], vcc\n\t" \
  "s_cmp_eq_u64 s[94:95], 0\n\t" \
  "s_cbranch_scc1 .Lok%=\n\t" \
  "s_add_u32 s92, s92, 1\n\t" \
  "s_sleep 6\n\t" \
  "s_cmp_eq_u32 s92, 1\n\t" \
  "s_cbranch_scc1 .Lpoll%=\n\t" \
  "s_sleep 1\n\t" \
  "s_and_b32 s93, s92, 63\n\t" \
  "s_cmp_lg_u32 s93, 0\n\t" \
  "s_cbranch_scc1 .Lpoll%=\n\t" \
  "v_mov_b32 v197, 0\n\t" \
  "buffer_load_dword v196, v197, %[rs], %[sab] offen sc1\n\t" \
  "s_waitcnt vmcnt(0)\n\t" \
  "v_readfirstlane_b32 s93, v196\n\t" \
  "s_nop 3\n\t" \
  "s_cmp_eq_u32 s93, 1\n\t" \
  "s_cbranch_scc1 .Lok%=\n\t" \
  "s_cmp_lt_u32 s92, 0x80000\n\t" \
  "s_cbranch_scc1 .Lpoll%=\n\t" \
  "v_mov_b32 v196, 1\n\t" \
  "buffer_store_dword v196, v197, %[rs], %[sab] offen sc1\n\t" \
  "s_branch .Lok%=\n\t" \
  ".Lend%=:\n\t"
#define TK_ASM \
  "buffer_load_dwordx4 v[224:227], v200, %[rs], %[sk] offen offset:0 sc1\n\t" \
  "buffer_load_dwordx4 v[228:231], v200, %[rs], %[sk] offen offset:1024 sc1\n\t" \
  "s_memrealtime s[88:89]\n\t" \
  "v_add_u32 v205, %[tl], v200\n\t" \
  "v_add_u32 v206, %[tq], v205\n\t" \
  "v_add_u32 v207, %[tv], v205\n\t" \
  "s_waitcnt lgkmcnt(0)\n\t" \
  "ds_read_b128 v[128:131], v206 offset:8192\n\t" \
  "ds_read_b128 v[144:147], v205 offset:0\n\t" \
  "ds_read_b128 v[160:163], v205 offset:4096\n\t" \
  "ds_read_b128 v[132:135], v206 offset:9216\n\t" \
  "ds_read_b128 v[148:151], v205 offset:1024\n\t" \
  "ds_read_b128 v[164:167], v205 offset:5120\n\t" \
  "ds_read_b128 v[136:139], v206 offset:10240\n\t" \
  "ds_read_b128 v[152:155], v205 offset:2048\n\t" \
  "ds_read_b128 v[168:171], v205 offset:6144\n\t" \
  "ds_read_b128 v[140:143], v206 offset:11264\n\t" \
  "ds_read_b128 v[156:159], v205 offset:3072\n\t" \
  "ds_read_b128 v[172:175], v205 offset:7168\n\t" \
  "v_add_u32 v206, %[vp], v218\n\t" \
  "s_lshl_b32 s88, s88, 2\n\t" \
  "s_add_u32 s89, s88, %[od]\n\t" \
  "s_waitcnt lgkmcnt(10)\n\t" \
  "v_mfma_f32_16x16x32_bf16 v[196:199], v[144:147], v[128:131], 0\n\t" \
  "s_waitcnt lgkmcnt(9)\n\t" \
  "v_mfma_f32_16x16x32_bf16 v[144:147], v[160:163], v[128:131], 0\n\t" \
  "s_waitcnt lgkmcnt(7)\n\t" \
  "v_mfma_f32_16x16x32_bf16 v[196:199], v[148:151], v[132:135], v[196:199]\n\t" \
  "s_waitcnt lgkmcnt(6)\n\t" \
  "v_mfma_f32_16x16x32_bf16 v[144:147], v[164:167], v[132:135], v[144:147]\n\t" \
  "s_waitcnt lgkmcnt(4)\n\t" \
  "v_mfma_f32_16x16x32_bf16 v[196:199], v[152:155], v[136:139], v[196:199]\n\t" \
  "s_waitcnt lgkmcnt(3)\n\t" \
  "v_mfma_f32_16x16x32_bf16 v[144:147], v[168:171], v[136:139], v[144:147]\n\t" \
  "s_waitcnt lgkmcnt(1)\n\t" \
  "v_mfma_f32_16x16x32_bf16 v[196:199], v[156:159], v[140:143], v[196:199]\n\t" \
  "s_waitcnt lgkmcnt(0)\n\t" \
  "v_mfma_f32_16x16x32_bf16 v[144:147], v[172:175], v[140:143], v[144:147]\n\t" \
  "ds_read_b128 v[176:179], v207 offset:16384\n\t" \
  "ds_read_b128 v[180:183], v207 offset:17408\n\t" \
  "ds_read_b128 v[184:187], v207 offset:18432\n\t" \
  "ds_read_b128 v[188:191], v207 offset:19456\n\t" \
  "ds_read2st64_b32 v[192:193], v206 offset0:0 offset1:1\n\t" \
  "ds_read2st64_b32 v[194:195], v206 offset0:2 offset1:3\n\t" \
  "v_cvt_pk_bf16_f32 v197, v196, v197\n\t" \
  "v_cvt_pk_bf16_f32 v199, v198, v199\n\t" \
  "v_cvt_pk_bf16_f32 v145, v144, v145\n\t" \
  "v_cvt_pk_bf16_f32 v147, v146, v147\n\t" \
  "v_lshlrev_b32 v196, 16, v197\n\t" \
  "v_lshlrev_b32 v198, 16, v199\n\t" \
  "v_lshlrev_b32 v144, 16, v145\n\t" \
  "v_lshlrev_b32 v146, 16, v147\n\t" \
  "v_and_b32 v197, 0xffff0000, v197\n\t" \
  "v_and_b32 v199, 0xffff0000, v199\n\t" \
  "v_and_b32 v145, 0xffff0000, v145\n\t" \
  "v_and_b32 v147, 0xffff0000, v147\n\t" \
  "v_pk_mul_f32 v[196:197], v[196:197], %[sc] op_sel_hi:[1,0]\n\t" \
  "v_pk_mul_f32 v[198:199], v[198:199], %[sc] op_sel_hi:[1,0]\n\t" \
  "v_pk_mul_f32 v[144:145], v[144:145], %[sc] op_sel_hi:[1,0]\n\t" \
  "v_pk_mul_f32 v[146:147], v[146:147], %[sc] op_sel_hi:[1,0]\n\t" \
  "v_cvt_pk_bf16_f32 v197, v196, v197\n\t" \
  "v_cvt_pk_bf16_f32 v199, v198, v199\n\t" \
  "v_cvt_pk_bf16_f32 v145, v144, v145\n\t" \
  "v_cvt_pk_bf16_f32 v147, v146, v147\n\t" \
  "v_lshlrev_b32 v196, 16, v197\n\t" \
  "v_lshlrev_b32 v198, 16, v199\n\t" \
  "v_lshlrev_b32 v144, 16, v145\n\t" \
  "v_lshlrev_b32 v146, 16, v147\n\t" \
  "v_and_b32 v197, 0xffff0000, v197\n\t" \
  "v_and_b32 v199, 0xffff0000, v199\n\t" \
  "v_and_b32 v145, 0xffff0000, v145\n\t" \
  "v_and_b32 v147, 0xffff0000, v147\n\t" \
  "v_min_f32 v196, v196, v208\n\t" \
  "v_min_f32 v197, v197, v209\n\t" \
  "v_min_f32 v198, v198, v210\n\t" \
  "v_min_f32 v199, v199, v211\n\t" \
  "v_min_f32 v144, v144, v212\n\t" \
  "v_min_f32 v145, v145, v213\n\t" \
  "v_min_f32 v146, v146, v214\n\t" \
  "v_min_f32 v147, v147, v215\n\t" \
  "v_max3_f32 v148, v196, v197, v198\n\t" \
  "v_max3_f32 v149, v199, v144, v145\n\t" \
  "v_max3_f32 v148, v148, v146, v147\n\t" \
  "v_max_f32 v150, v148, v149\n\t" \
  "v_max_f32 v151, v148, v149\n\t" \
  "s_nop 1\n\t" \
  "v_permlane32_swap_b32 v150, v151\n\t" \
  "v_max_f32 v150, v150, v151\n\t" \
  "s_mov_b32 s92, 0\n\t" \
  ".Ltake%=:\n\t" \
  "s_waitcnt vmcnt(1) lgkmcnt(0)\n\t" \
  "ds_write_b128 v216, v[224:227]\n\t" \
  "v_max3_u32 v148, v224, v225, v226\n\t" \
  "s_waitcnt vmcnt(0)\n\t" \
  "ds_write_b128 v216, v[228:231] offset:1024\n\t" \
  "ds_read_b64_tr_b16 v[160:161], v217 offset:0\n\t" \
  "ds_read_b64_tr_b16 v[162:163], v217 offset:64\n\t" \
  "ds_read_b64_tr_b16 v[164:165], v217 offset:512\n\t" \
  "ds_read_b64_tr_b16 v[166:167], v217 offset:576\n\t" \
  "ds_read_b64_tr_b16 v[168:169], v217 offset:1024\n\t" \
  "ds_read_b64_tr_b16 v[170:171], v217 offset:1088\n\t" \
  "ds_read_b64_tr_b16 v[172:173], v217 offset:1536\n\t" \
  "ds_read_b64_tr_b16 v[174:175], v217 offset:1600\n\t" \
  "v_max3_u32 v148, v148, v227, v228\n\t" \
  "v_max3_u32 v148, v148, v229, v230\n\t" \
  "v_max_u32 v148, v148, v231\n\t" \
  "v_cmp_le_u32 vcc, %[sent], v148\n\t" \
  "s_cbranch_vccnz .Lslow%=\n\t" \
  ".Lok%=:\n\t" \
  "s_waitcnt lgkmcnt(6)\n\t" \
  "v_mfma_f32_16x16x32_bf16 v[152:155], v[160:163], v[128:131], 0\n\t" \
  "s_waitcnt lgkmcnt(4)\n\t" \
  "v_mfma_f32_16x16x32_bf16 v[152:155], v[164:167], v[132:135], v[152:155]\n\t" \
  "s_waitcnt lgkmcnt(2)\n\t" \
  "v_mfma_f32_16x16x32_bf16 v[152:155], v[168:171], v[136:139], v[152:155]\n\t" \
  "s_waitcnt lgkmcnt(0)\n\t" \
  "v_mfma_f32_16x16x32_bf16 v[152:155], v[172:175], v[140:143], v[152:155]\n\t" \
  "v_mov_b32 v157, 0\n\t" \
  "v_mov_b32 v158, 0\n\t" \
  "s_nop 5\n\t" \
  "v_cvt_pk_bf16_f32 v153, v152, v153\n\t" \
  "v_cvt_pk_bf16_f32 v155, v154, v155\n\t" \
  "v_lshlrev_b32 v152, 16, v153\n\t" \
  "v_lshlrev_b32 v154, 16, v155\n\t" \
  "v_and_b32 v153, 0xffff0000, v153\n\t" \
  "v_and_b32 v155, 0xffff0000, v155\n\t" \
  "v_pk_mul_f32 v[152:153], v[152:153], %[sc] op_sel_hi:[1,0]\n\t" \
  "v_pk_mul_f32 v[154:155], v[154:155], %[sc] op_sel_hi:[1,0]\n\t" \
  "v_cvt_pk_bf16_f32 v153, v152, v153\n\t" \
  "v_cvt_pk_bf16_f32 v155, v154, v155\n\t" \
  "v_lshlrev_b32 v152, 16, v153\n\t" \
  "v_lshlrev_b32 v154, 16, v155\n\t" \
  "v_and_b32 v153, 0xffff0000, v153\n\t" \
  "v_and_b32 v155, 0xffff0000, v155\n\t" \
  "v_max3_f32 v148, v152, v153, v150\n\t" \
  "v_max3_f32 v149, v148, v154, v155\n\t" \
  "v_max3_f32 v150, v148, v154, v155\n\t" \
  "v_cndmask_b32 v152, v152, v154, %[up]\n\t" \
  "v_cndmask_b32 v153, v153, v155, %[up]\n\t" \
  "v_permlane16_swap_b32 v149, v150\n\t" \
  "v_max_f32 v148, v149, v150\n\t" \
  "v_mul_f32 v148, %[negc], v148\n\t" \
  "v_fma_f32 v152, v152, %[c], v148\n\t" \
  "v_fma_f32 v153, v153, %[c], v148\n\t" \
  "v_exp_f32 v152, v152\n\t" \
  "v_fma_f32 v196, v196, %[c], v148\n\t" \
  "v_exp_f32 v153, v153\n\t" \
  "v_fma_f32 v197, v197, %[c], v148\n\t" \
  "v_exp_f32 v196, v196\n\t" \
  "v_fma_f32 v198, v198, %[c], v148\n\t" \
  "v_exp_f32 v197, v197\n\t" \
  "v_fma_f32 v199, v199, %[c], v148\n\t" \
  "v_exp_f32 v198, v198\n\t" \
  "v_fma_f32 v144, v144, %[c], v148\n\t" \
  "v_pk_add_f32 v[132:133], v[152:153], v[196:197]\n\t" \
  "v_exp_f32 v199, v199\n\t" \
  "v_fma_f32 v145, v145, %[c], v148\n\t" \
  "v_exp_f32 v144, v144\n\t" \
  "v_fma_f32 v146, v146, %[c], v148\n\t" \
  "v_pk_add_f32 v[132:133], v[132:133], v[198:199]\n\t" \
  "v_exp_f32 v145, v145\n\t" \
  "v_fma_f32 v147, v147, %[c], v148\n\t" \
  "v_exp_f32 v146, v146\n\t" \
  "v_pk_add_f32 v[132:133], v[132:133], v[144:145]\n\t" \
  "v_exp_f32 v147, v147\n\t" \
  "s_nop 0\n\t" \
  "v_pk_add_f32 v[132:133], v[132:133], v[146:147]\n\t" \
  "v_add_f32 v134, v132, v133\n\t" \
  "v_add_f32 v135, v132, v133\n\t" \
  "s_nop 1\n\t" \
  "v_permlane16_swap_b32 v134, v135\n\t" \
  "v_add_f32 v132, v134, v135\n\t" \
  "v_add_f32 v133, v134, v135\n\t" \
  "s_nop 1\n\t" \
  "v_permlane32_swap_b32 v132, v133\n\t" \
  "v_add_f32 v132, v132, v133\n\t" \
  "v_rcp_f32 v148, v132\n\t" \
  "s_nop 0\n\t" \
  "v_pk_mul_f32 v[152:153], v[152:153], v[148:149] op_sel_hi:[1,0]\n\t" \
  "v_pk_mul_f32 v[196:197], v[196:197], v[148:149] op_sel_hi:[1,0]\n\t" \
  "v_pk_mul_f32 v[198:199], v[198:199], v[148:149] op_sel_hi:[1,0]\n\t" \
  "v_pk_mul_f32 v[144:145], v[144:145], v[148:149] op_sel_hi:[1,0]\n\t" \
  "v_pk_mul_f32 v[146:147], v[146:147], v[148:149] op_sel_hi:[1,0]\n\t" \
  "v_cvt_pk_bf16_f32 v156, v152, v153\n\t" \
  "v_cvt_pk_bf16_f32 v159, v152, v153\n\t" \
  "v_cvt_pk_bf16_f32 v128, v196, v197\n\t" \
  "v_cvt_pk_bf16_f32 v129, v198, v199\n\t" \
  "v_mfma_f32_16x16x16_bf16 v[160:163], v[192:193], v[156:157], 0\n\t" \
  "v_cvt_pk_bf16_f32 v130, v144, v145\n\t" \
  "v_mfma_f32_16x16x16_bf16 v[164:167], v[192:193], v[158:159], 0\n\t" \
  "v_cvt_pk_bf16_f32 v131, v146, v147\n\t" \
  "v_mfma_f32_16x16x16_bf16 v[168:171], v[194:195], v[156:157], 0\n\t" \
  "v_mfma_f32_16x16x16_bf16 v[172:175], v[194:195], v[158:159], 0\n\t" \
  "s_nop 0\n\t" \
  "v_mfma_f32_16x16x32_bf16 v[160:163], v[176:179], v[128:131], v[160:163]\n\t" \
  "s_nop 0\n\t" \
  "v_mfma_f32_16x16x32_bf16 v[164:167], v[180:183], v[128:131], v[164:167]\n\t" \
  "s_nop 0\n\t" \
  "v_mfma_f32_16x16x32_bf16 v[168:171], v[184:187], v[128:131], v[168:171]\n\t" \
  "v_mfma_f32_16x16x32_bf16 v[172:175], v[188:191], v[128:131], v[172:175]\n\t" \
  "s_nop 2\n\t" \
  "v_cvt_pk_bf16_f32 v136, v160, v161\n\t" \
  "v_cvt_pk_bf16_f32 v137, v162, v163\n\t" \
  "v_cvt_pk_bf16_f32 v138, v164, v165\n\t" \
  "v_cvt_pk_bf16_f32 v139, v166, v167\n\t" \
  "v_cvt_pk_bf16_f32 v140, v168, v169\n\t" \
  "v_cvt_pk_bf16_f32 v141, v170, v171\n\t" \
  "v_cvt_pk_bf16_f32 v142, v172, v173\n\t" \
  "v_cvt_pk_bf16_f32 v143, v174, v175\n\t" \
  "s_memrealtime s[90:91]\n\t" \
  "s_waitcnt lgkmcnt(0)\n\t" \
  "s_lshl_b32 s93, s90, 2\n\t" \
  "s_sub_i32 s93, s89, s93\n\t" \
  "s_cmp_le_i32 s93, 0\n\t" \
  "s_cbranch_scc1 .Lgoo%=\n\t" \
  "s_min_i32 s93, s93, 8000\n\t" \
  "s_mov_b32 s94, s93\n\t" \
  ".Lslo%=:\n\t" \
  "s_sub_i32 s94, s94, 18\n\t" \
  "s_cmp_lt_i32 s94, 30\n\t" \
  "s_cbranch_scc1 .Lsxo%=\n\t" \
  "s_sleep 1\n\t" \
  "s_branch .Lslo%=\n\t" \
  ".Lsxo%=:\n\t" \
  "s_add_u32 s93, s93, 3\n\t" \
  "s_lshr_b32 s93, s93, 2\n\t" \
  "s_add_u32 s93, s93, s90\n\t" \
  "s_memrealtime s[90:91]\n\t" \
  ".Lpaceo%=:\n\t" \
  "s_memrealtime s[94:95]\n\t" \
  "s_waitcnt lgkmcnt(1)\n\t" \
  "s_sub_i32 s92, s90, s93\n\t" \
  "s_cmp_ge_i32 s92, 0\n\t" \
  "s_cbranch_scc1 .Leo%=\n\t" \
  "s_memrealtime s[90:91]\n\t" \
  "s_waitcnt lgkmcnt(1)\n\t" \
  "s_sub_i32 s92, s94, s93\n\t" \
  "s_cmp_lt_i32 s92, 0\n\t" \
  "s_cbranch_scc1 .Lpaceo%=\n\t" \
  ".Leo%=:\n\t" \
  ".Lgoo%=:\n\t" \
  "buffer_store_dwordx4 v[136:139], v200, %[rt], %[so] offen offset:0 sc1\n\t" \
  "buffer_store_dwordx4 v[140:143], v200, %[rt], %[so] offen offset:1024 sc1\n\t" \
  "s_waitcnt lgkmcnt(0)\n\t" \
  "s_branch .Lend%=\n\t" \
  ".Lpoll%=:\n\t" \
  "buffer_load_dwordx4 v[224:227], v200, %[rs], %[sk] offen offset:0 sc1\n\t" \
  "buffer_load_dwordx4 v[228:231], v200, %[rs], %[sk] offen offset:1024 sc1\n\t" \
  "s_branch .Ltake%=\n\t" \
  "s_branch .Lend%=\n\t" \
  ".Lslow%=:\n\t" \
  "s_mov_b64 s[94:95], 0\n\t" \
  "v_cmp_eq_u32 vcc, %[sent], v224\n\t" \
  "s_or_b64 s[94:95], s[94:95], vcc\n\t" \
  "v_cmp_eq_u32 vcc, %[sent], v225\n\t" \
  "s_or_b64 s[94:95], s[94:95], vcc\n\t" \
  "v_cmp_eq_u32 vcc, %[sent], v226\n\t" \
  "s_or_b64 s[94:95], s[94:95], vcc\n\t" \
  "v_cmp_eq_u32 vcc, %[sent], v227\n\t" \
  "s_or_b64 s[94:95], s[94:95], vcc\n\t" \
  "v_cmp_eq_u32 vcc, %[sent], v228\n\t" \
  "s_or_b64 s[94:95], s[94:95], vcc\n\t" \
  "v_cmp_eq_u32 vcc, %[sent], v229\n\t" \
  "s_or_b64 s[94:95], s[94:95], vcc\n\t" \
  "v_cmp_eq_u32 vcc, %[sent], v230\n\t" \
  "s_or_b64 s[94:95], s[94:95], vcc\n\t" \
  "v_cmp_eq_u32 vcc, %[sent], v231\n\t" \
  "s_or_b64 s[94:95], s[94:95], vcc\n\t" \
  "s_cmp_eq_u64 s[94:95], 0\n\t" \
  "s_cbranch_scc1 .Lok%=\n\t" \
  "s_add_u32 s92, s92, 1\n\t" \
  "s_sleep 6\n\t" \
  "s_cmp_eq_u32 s92, 1\n\t" \
  "s_cbranch_scc1 .Lpoll%=\n\t" \
  "s_sleep 1\n\t" \
  "s_and_b32 s93, s92, 63\n\t" \
  "s_cmp_lg_u32 s93, 0\n\t" \
  "s_cbranch_scc1 .Lpoll%=\n\t" \
  "v_mov_b32 v149, 0\n\t" \
  "buffer_load_dword v148, v149, %[rs], %[sab] offen sc1\n\t" \
  "s_waitcnt vmcnt(0)\n\t" \
  "v_readfirstlane_b32 s93, v148\n\t" \
  "s_nop 3\n\t" \
  "s_cmp_eq_u32 s93, 1\n\t" \
  "s_cbranch_scc1 .Lok%=\n\t" \
  "s_cmp_lt_u32 s92, 0x80000\n\t" \
  "s_cbranch_scc1 .Lpoll%=\n\t" \
  "v_mov_b32 v148, 1\n\t" \
  "buffer_store_dword v148, v149, %[rs], %[sab] offen sc1\n\t" \
  "s_branch .Lok%=\n\t" \
  ".Lend%=:\n\t"
constexpr int TLAG = 2;
#define RED_ASM \
  "s_mov_b32 %[step], 0\n\t" \
  "s_lshl_b32 %[sh], %[step], 15\n\t" \
  "s_lshr_b32 s93, %[step], 4\n\t" \
  "s_lshr_b32 s93, %[pm], s93\n\t" \
  "s_and_b32 s93, s93, 4\n\t" \
  "s_lshl_b32 s93, s93, 11\n\t" \
  "s_lshl2_add_u32 %[so], %[sh], s93\n\t" \
  "s_sub_i32 s93, %[step], 2\n\t" \
  "s_lshl_b32 %[st], s93, 19\n\t" \
  "s_lshl_b32 s94, s93, 11\n\t" \
  "s_xor_b32 s94, s94, %[s0]\n\t" \
  "s_and_b32 s94, s94, 0x2000\n\t" \
  "s_or_b32 %[st], %[st], s94\n\t" \
  "s_cmp_lt_i32 s93, 0\n\t" \
  "s_cselect_b32 %[st], 0x70000000, %[st]\n\t" \
  "s_add_u32 %[st4], %[st], 4096\n\t" \
  "buffer_load_dwordx2 v[166:167], v173, %[rs], 0 offen sc1\n\t" \
  "v_mov_b64 v[144:145], 0\n\t" \
  "v_mov_b64 v[146:147], 0\n\t" \
  "v_mov_b64 v[148:149], 0\n\t" \
  "v_mov_b64 v[150:151], 0\n\t" \
  "v_mov_b64 v[152:153], 0\n\t" \
  "v_mov_b64 v[154:155], 0\n\t" \
  "v_mov_b64 v[156:157], 0\n\t" \
  "v_mov_b64 v[158:159], 0\n\t" \
  ".Lstep%=:\n\t" \
  "s_setprio 3\n\t" \
  "s_mov_b32 s92, 0\n\t" \
  ".Lcal%=:\n\t" \
  "s_waitcnt vmcnt(0)\n\t" \
  "s_memrealtime s[76:77]\n\t" \
  "v_cmp_ne_u32 vcc, %[step], v166\n\t" \
  "v_readlane_b32 s90, v167, 0\n\t" \
  "v_readlane_b32 s91, v167, 1\n\t" \
  "v_readlane_b32 s94, v167, 2\n\t" \
  "v_readlane_b32 s95, v167, 3\n\t" \
  "s_sub_i32 s90, s90, %[base]\n\t" \
  "s_sub_i32 s91, s91, %[base]\n\t" \
  "s_sub_i32 s94, s94, %[base]\n\t" \
  "s_sub_i32 s95, s95, %[base]\n\t" \
  "s_max_i32 s90, s90, s91\n\t" \
  "s_max_i32 s94, s94, s95\n\t" \
  "s_max_i32 s90, s90, s94\n\t" \
  "s_add_i32 s90, s90, %[ds]\n\t" \
  "s_waitcnt lgkmcnt(0)\n\t" \
  "s_lshl2_add_u32 s76, s76, %[mn]\n\t" \
  "s_sub_i32 s76, s76, %[base]\n\t" \
  "s_add_u32 %[E], s90, %[base]\n\t" \
  "s_and_b32 s93, vcc_lo, 0xf\n\t" \
  "s_cbranch_scc1 .Lbl%=\n\t" \
  ".Lgo%=:\n\t" \
  "v_mov_b32 v166, 7\n\t" \
  "s_mov_b64 exec, 1\n\t" \
  "v_mov_b32 v164, s90\n\t" \
  "ds_max_u32 v168, v164 offset:4100\n\t" \
  "s_mov_b64 exec, -1\n\t" \
  "s_memrealtime s[90:91]\n\t" \
  "s_waitcnt lgkmcnt(0)\n\t" \
  "s_lshl_b32 s93, s90, 2\n\t" \
  "s_sub_i32 s93, %[E], s93\n\t" \
  "s_cmp_le_i32 s93, 0\n\t" \
  "s_cbranch_scc1 .Lgop%=\n\t" \
  "s_min_i32 s93, s93, 8000\n\t" \
  "s_mov_b32 s94, s93\n\t" \
  ".Lslp%=:\n\t" \
  "s_sub_i32 s94, s94, 18\n\t" \
  "s_cmp_lt_i32 s94, 30\n\t" \
  "s_cbranch_scc1 .Lsxp%=\n\t" \
  "s_sleep 1\n\t" \
  "s_branch .Lslp%=\n\t" \
  ".Lsxp%=:\n\t" \
  "s_sub_i32 s93, s93, 12\n\t" \
  "s_max_i32 s93, s93, 4\n\t" \
  "s_lshr_b32 s93, s93, 2\n\t" \
  "s_add_u32 s93, s93, s90\n\t" \
  "s_memrealtime s[90:91]\n\t" \
  ".Lpacep%=:\n\t" \
  "s_memrealtime s[94:95]\n\t" \
  "s_waitcnt lgkmcnt(1)\n\t" \
  "s_sub_i32 s92, s90, s93\n\t" \
  "s_cmp_ge_i32 s92, 0\n\t" \
  "s_cbranch_scc1 .Lep%=\n\t" \
  "s_memrealtime s[90:91]\n\t" \
  "s_waitcnt lgkmcnt(1)\n\t" \
  "s_sub_i32 s92, s94, s93\n\t" \
  "s_cmp_lt_i32 s92, 0\n\t" \
  "s_cbranch_scc1 .Lpacep%=\n\t" \
  "s_nop 4\n\t" \
  ".Lep%=:\n\t" \
  "s_add_u32 s92, s92, s93\n\t" \
  "s_lshl_b32 s92, s92, 2\n\t" \
  "s_sub_i32 s92, %[E], s92\n\t" \
  "s_max_i32 s92, s92, 0\n\t" \
  "s_bitcmp0_b32 s92, 3\n\t" \
  "s_cbranch_scc1 .Lf3p%=\n\t" \
  "s_nop 15\n\t" \
  ".Lf3p%=:\n\t" \
  "s_bitcmp0_b32 s92, 2\n\t" \
  "s_cbranch_scc1 .Lf2p%=\n\t" \
  "s_nop 10\n\t" \
  ".Lf2p%=:\n\t" \
  ".Lgop%=:\n\t" \
  "s_mov_b32 s92, 0\n\t" \
  "buffer_load_dwordx4 v[128:131], v234, %[rs], %[so] offen sc1\n\t" \
  "s_nop 15\n\t" \
  "buffer_load_dwordx4 v[132:135], v235, %[rs], %[so] offen sc1\n\t" \
  "s_nop 15\n\t" \
  "buffer_load_dwordx4 v[136:139], v236, %[rs], %[so] offen sc1\n\t" \
  "s_nop 15\n\t" \
  "buffer_load_dwordx4 v[140:143], v237, %[rs], %[so] offen sc1\n\t" \
  "s_waitcnt lgkmcnt(0)\n\t" \
  ".Lw%=:\n\t" \
  "s_waitcnt vmcnt(3)\n\t" \
  "v_smfmac_f32_16x16x64_bf16 v[144:147], v[128:131], v[0:7], v172\n\t" \
  "v_max3_u32 v164, v128, v129, 0\n\t" \
  "v_smfmac_f32_16x16x64_bf16 v[148:151], v[128:131], v[8:15], v172\n\t" \
  "v_max3_u32 v164, v130, v131, v164\n\t" \
  "v_smfmac_f32_16x16x64_bf16 v[152:155], v[128:131], v[16:23], v172\n\t" \
  "v_smfmac_f32_16x16x64_bf16 v[156:159], v[128:131], v[24:31], v172\n\t" \
  "buffer_load_dwordx4 v[176:179], v238, %[rt], %[st] offen offset:0 sc1\n\t" \
  "s_waitcnt vmcnt(3)\n\t" \
  "v_smfmac_f32_16x16x64_bf16 v[144:147], v[132:135], v[32:39], v172\n\t" \
  "v_max3_u32 v164, v132, v133, v164\n\t" \
  "v_smfmac_f32_16x16x64_bf16 v[148:151], v[132:135], v[40:47], v172\n\t" \
  "v_max3_u32 v164, v134, v135, v164\n\t" \
  "v_smfmac_f32_16x16x64_bf16 v[152:155], v[132:135], v[48:55], v172\n\t" \
  "v_smfmac_f32_16x16x64_bf16 v[156:159], v[132:135], v[56:63], v172\n\t" \
  "buffer_load_dwordx4 v[180:183], v238, %[rt], %[st] offen offset:1024 sc1\n\t" \
  "s_waitcnt vmcnt(3)\n\t" \
  "v_smfmac_f32_16x16x64_bf16 v[144:147], v[136:139], v[64:71], v172\n\t" \
  "v_max3_u32 v164, v136, v137, v164\n\t" \
  "v_smfmac_f32_16x16x64_bf16 v[148:151], v[136:139], v[72:79], v172\n\t" \
  "v_max3_u32 v164, v138, v139, v164\n\t" \
  "v_smfmac_f32_16x16x64_bf16 v[152:155], v[136:139], v[80:87], v172\n\t" \
  "v_smfmac_f32_16x16x64_bf16 v[156:159], v[136:139], v[88:95], v172\n\t" \
  "buffer_load_dwordx4 v[184:187], v238, %[rt], %[st4] offen offset:0 sc1\n\t" \
  "s_waitcnt vmcnt(3)\n\t" \
  "v_smfmac_f32_16x16x64_bf16 v[144:147], v[140:143], v[96:103], v172\n\t" \
  "v_max3_u32 v164, v140, v141, v164\n\t" \
  "v_smfmac_f32_16x16x64_bf16 v[148:151], v[140:143], v[104:111], v172\n\t" \
  "v_max3_u32 v164, v142, v143, v164\n\t" \
  "v_smfmac_f32_16x16x64_bf16 v[152:155], v[140:143], v[112:119], v172\n\t" \
  "v_cmp_le_u32 vcc, %[sent], v164\n\t" \
  "v_smfmac_f32_16x16x64_bf16 v[156:159], v[140:143], v[120:127], v172\n\t" \
  "buffer_load_dwordx4 v[188:191], v238, %[rt], %[st4] offen offset:1024 sc1\n\t" \
  "s_cbranch_vccnz .Lslow%=\n\t" \
  ".Lok%=:\n\t" \
  "v_add_f32_dpp v160, v144, v146 quad_perm:[0,1,2,3] row_mask:0x3 bank_mask:0xf\n\t" \
  "v_add_f32_dpp v161, v145, v147 quad_perm:[0,1,2,3] row_mask:0x3 bank_mask:0xf\n\t" \
  "v_add_f32_dpp v162, v148, v150 quad_perm:[0,1,2,3] row_mask:0x3 bank_mask:0xf\n\t" \
  "v_add_f32_dpp v163, v149, v151 quad_perm:[0,1,2,3] row_mask:0x3 bank_mask:0xf\n\t" \
  "v_add_f32_dpp v160, v152, v154 quad_perm:[0,1,2,3] row_mask:0xc bank_mask:0xf\n\t" \
  "v_add_f32_dpp v161, v153, v155 quad_perm:[0,1,2,3] row_mask:0xc bank_mask:0xf\n\t" \
  "v_add_f32_dpp v162, v156, v158 quad_perm:[0,1,2,3] row_mask:0xc bank_mask:0xf\n\t" \
  "v_add_f32_dpp v163, v157, v159 quad_perm:[0,1,2,3] row_mask:0xc bank_mask:0xf\n\t" \
  "s_nop 0\n\t" \
  "v_permlane32_swap_b32 v160, v162\n\t" \
  "v_permlane32_swap_b32 v161, v163\n\t" \
  "v_add_f32 v160, v160, v162\n\t" \
  "v_add_f32 v161, v161, v163\n\t" \
  "ds_write_b64 v169, v[160:161]\n\t" \
  "s_mov_b64 exec, 1\n\t" \
  "ds_inc_rtn_u32 v164, v168, v166 offset:4104\n\t" \
  "s_mov_b64 exec, -1\n\t" \
  "ds_read_b64 v[128:129], v170 offset:0\n\t" \
  "ds_read_b64 v[130:131], v170 offset:512\n\t" \
  "ds_read_b64 v[132:133], v170 offset:1024\n\t" \
  "ds_read_b64 v[134:135], v170 offset:1536\n\t" \
  "ds_read_b64 v[136:137], v170 offset:2048\n\t" \
  "ds_read_b64 v[138:139], v170 offset:2560\n\t" \
  "ds_read_b64 v[140:141], v170 offset:3072\n\t" \
  "ds_read_b64 v[142:143], v170 offset:3584\n\t" \
  "s_waitcnt lgkmcnt(8)\n\t" \
  "v_readfirstlane_b32 s93, v164\n\t" \
  "s_cmp_lg_u32 s93, 7\n\t" \
  "s_cbranch_scc1 .Lb1%=\n\t" \
  "s_waitcnt lgkmcnt(4)\n\t" \
  "v_pk_add_f32 v[128:129], v[128:129], v[130:131]\n\t" \
  "v_pk_add_f32 v[132:133], v[132:133], v[134:135]\n\t" \
  "s_waitcnt lgkmcnt(0)\n\t" \
  "v_pk_add_f32 v[136:137], v[136:137], v[138:139]\n\t" \
  "v_pk_add_f32 v[140:141], v[140:141], v[142:143]\n\t" \
  "v_pk_add_f32 v[128:129], v[128:129], v[132:133]\n\t" \
  "v_pk_add_f32 v[136:137], v[136:137], v[140:141]\n\t" \
  "v_pk_add_f32 v[128:129], v[128:129], v[136:137]\n\t" \
  "v_cvt_pk_bf16_f32 v128, v128, v129\n\t" \
  "s_nop 1\n\t" \
  "v_mov_b32_dpp v129, v128 quad_perm:[1,0,3,2] row_mask:0xf bank_mask:0xf\n\t" \
  "v_perm_b32 v128, v129, v128, v174\n\t" \
  "buffer_store_dword v128, v171, %[rs], %[sh] offen sc1\n\t" \
  "s_memtime s[90:91]\n\t" \
  "s_barrier\n\t" \
  "s_memtime s[76:77]\n\t" \
  "s_memrealtime s[78:79]\n\t" \
  "s_nop 3\n\t" \
  "s_memrealtime s[80:81]\n\t" \
  "s_nop 3\n\t" \
  "s_memrealtime s[82:83]\n\t" \
  "s_nop 3\n\t" \
  "s_memrealtime s[84:85]\n\t" \
  "s_nop 3\n\t" \
  "s_memrealtime s[86:87]\n\t" \
  "s_nop 3\n\t" \
  "s_memrealtime s[88:89]\n\t" \
  "s_mov_b32 s93, 0\n\t" \
  "s_waitcnt lgkmcnt(0)\n\t" \
  "s_cmp_eq_u32 s80, s78\n\t" \
  "s_addc_u32 s93, s93, 0\n\t" \
  "s_cmp_eq_u32 s82, s78\n\t" \
  "s_addc_u32 s93, s93, 0\n\t" \
  "s_cmp_eq_u32 s84, s78\n\t" \
  "s_addc_u32 s93, s93, 0\n\t" \
  "s_cmp_eq_u32 s86, s78\n\t" \
  "s_addc_u32 s93, s93, 0\n\t" \
  "s_cmp_eq_u32 s88, s78\n\t" \
  "s_addc_u32 s93, s93, 0\n\t" \
  "s_mul_i32 s93, s93, 21\n\t" \
  "s_add_u32 s76, s76, 15\n\t" \
  "s_add_u32 s76, s76, s93\n\t" \
  "s_lshl2_add_u32 s78, s78, 16\n\t" \
  "s_mov_b64 exec, 1\n\t" \
  "s_sub_i32 s90, s76, s90\n\t" \
  "s_mul_i32 s90, s90, 11900\n\t" \
  "s_ashr_i32 s90, s90, 16\n\t" \
  "s_sub_u32 s90, s78, s90\n\t" \
  "s_sub_u32 s90, s90, %[base]\n\t" \
  "v_mov_b32 v164, s90\n\t" \
  "ds_write_b32 v168, v164 offset:4108\n\t" \
  "s_mov_b64 exec, -1\n\t" \
  "s_waitcnt vmcnt(1)\n\t" \
  ".Lb2%=:\n\t" \
  "s_setprio 0\n\t" \
  "s_bitcmp0_b64 %[am], %[step]\n\t" \
  "s_cbranch_scc1 .La1%=\n\t" \
  "buffer_store_dwordx4 v[224:227], v228, %[ra], 0 offen sc1\n\t" \
  ".La1%=:\n\t" \
  "buffer_load_dwordx4 v[192:195], v239, %[rt], %[st] offen offset:0 sc1\n\t" \
  "buffer_load_dwordx4 v[196:199], v239, %[rt], %[st] offen offset:1024 sc1\n\t" \
  "v_mfma_f32_16x16x32_bf16 v[128:131], v[0:3], v[176:179], 0\n\t" \
  "v_mfma_f32_16x16x32_bf16 v[136:139], v[8:11], v[176:179], 0\n\t" \
  "ds_read2st64_b32 v[144:145], v231 offset0:0 offset1:8\n\t" \
  "ds_read2st64_b32 v[146:147], v231 offset0:16 offset1:24\n\t" \
  "ds_read2st64_b32 v[148:149], v231 offset0:32 offset1:40\n\t" \
  "ds_read2st64_b32 v[150:151], v231 offset0:48 offset1:56\n\t" \
  "buffer_load_dwordx4 v[200:203], v239, %[rt], %[st4] offen offset:0 sc1\n\t" \
  "buffer_load_dwordx4 v[204:207], v239, %[rt], %[st4] offen offset:1024 sc1\n\t" \
  "v_mfma_f32_16x16x32_bf16 v[132:135], v[4:7], v[180:183], 0\n\t" \
  "v_mfma_f32_16x16x32_bf16 v[140:143], v[12:15], v[180:183], 0\n\t" \
  "s_waitcnt lgkmcnt(0)\n\t" \
  "v_add_f32 v144, v144, v145\n\t" \
  "v_add_f32 v146, v146, v147\n\t" \
  "v_add_f32 v148, v148, v149\n\t" \
  "v_add_f32 v150, v150, v151\n\t" \
  "v_add_f32 v144, v144, v146\n\t" \
  "v_add_f32 v148, v148, v150\n\t" \
  "buffer_load_dwordx4 v[208:211], v240, %[rt], %[st] offen offset:0 sc1\n\t" \
  "buffer_load_dwordx4 v[212:215], v240, %[rt], %[st] offen offset:1024 sc1\n\t" \
  "v_mfma_f32_16x16x32_bf16 v[128:131], v[16:19], v[184:187], v[128:131]\n\t" \
  "v_mfma_f32_16x16x32_bf16 v[136:139], v[24:27], v[184:187], v[136:139]\n\t" \
  "v_add_f32 v144, v144, v148\n\t" \
  "v_cvt_pk_bf16_f32 v144, v144, v144\n\t" \
  "ds_write_b16 v232, v144\n\t" \
  "ds_read_b128 v[224:227], v229\n\t" \
  "v_add_u32 v228, %[ainc], v228\n\t" \
  "v_add_u32 v229, %[rinc], v229\n\t" \
  "v_add_u32 v232, 1024, v232\n\t" \
  "v_xor_b32 v231, 0x4000, v231\n\t" \
  "buffer_load_dwordx4 v[216:219], v240, %[rt], %[st4] offen offset:0 sc1\n\t" \
  "buffer_load_dwordx4 v[220:223], v240, %[rt], %[st4] offen offset:1024 sc1\n\t" \
  "v_mfma_f32_16x16x32_bf16 v[132:135], v[20:23], v[188:191], v[132:135]\n\t" \
  "v_mfma_f32_16x16x32_bf16 v[140:143], v[28:31], v[188:191], v[140:143]\n\t" \
  "buffer_load_dwordx4 v[176:179], v241, %[rt], %[st] offen offset:0 sc1\n\t" \
  "s_waitcnt vmcnt(8)\n\t" \
  "v_mfma_f32_16x16x32_bf16 v[128:131], v[32:35], v[192:195], v[128:131]\n\t" \
  "v_mfma_f32_16x16x32_bf16 v[136:139], v[40:43], v[192:195], v[136:139]\n\t" \
  "buffer_load_dwordx4 v[180:183], v241, %[rt], %[st] offen offset:1024 sc1\n\t" \
  "s_waitcnt vmcnt(8)\n\t" \
  "v_mfma_f32_16x16x32_bf16 v[132:135], v[36:39], v[196:199], v[132:135]\n\t" \
  "v_mfma_f32_16x16x32_bf16 v[140:143], v[44:47], v[196:199], v[140:143]\n\t" \
  "buffer_load_dwordx4 v[184:187], v241, %[rt], %[st4] offen offset:0 sc1\n\t" \
  "s_waitcnt vmcnt(8)\n\t" \
  "v_mfma_f32_16x16x32_bf16 v[128:131], v[48:51], v[200:203], v[128:131]\n\t" \
  "v_mfma_f32_16x16x32_bf16 v[136:139], v[56:59], v[200:203], v[136:139]\n\t" \
  "ds_read_b128 v[160:163], v168 offset:4096\n\t" \
  "buffer_load_dwordx4 v[188:191], v241, %[rt], %[st4] offen offset:1024 sc1\n\t" \
  "s_waitcnt vmcnt(8)\n\t" \
  "v_mfma_f32_16x16x32_bf16 v[132:135], v[52:55], v[204:207], v[132:135]\n\t" \
  "v_mfma_f32_16x16x32_bf16 v[140:143], v[60:63], v[204:207], v[140:143]\n\t" \
  "s_waitcnt lgkmcnt(0)\n\t" \
  "v_readfirstlane_b32 s90, v161\n\t" \
  "v_readfirstlane_b32 s91, v163\n\t" \
  "s_add_u32 s93, s90, %[rl]\n\t" \
  "s_add_u32 s93, s93, %[base]\n\t" \
  "s_add_u32 %[E], s93, %[lk]\n\t" \
  "s_sub_i32 s91, s91, s90\n\t" \
  "s_sub_i32 s91, s91, %[rl]\n\t" \
  "s_min_i32 s93, s91, %[up]\n\t" \
  "s_max_i32 s93, s93, %[dn]\n\t" \
  "s_cmp_gt_i32 s91, 60\n\t" \
  "s_cselect_b32 s93, 2, s93\n\t" \
  "s_add_i32 %[rl], %[rl], s93\n\t" \
  "v_mov_b32 v160, %[rl]\n\t" \
  "buffer_store_dword v160, v175, %[rs], 0 offen sc1\n\t" \
  "s_waitcnt vmcnt(8)\n\t" \
  "v_mfma_f32_16x16x32_bf16 v[128:131], v[64:67], v[208:211], v[128:131]\n\t" \
  "v_mfma_f32_16x16x32_bf16 v[136:139], v[72:75], v[208:211], v[136:139]\n\t" \
  "s_add_u32 %[step], %[step], 1\n\t" \
  "s_lshl_b32 %[sh], %[step], 15\n\t" \
  "s_lshr_b32 s93, %[step], 4\n\t" \
  "s_lshr_b32 s93, %[pm], s93\n\t" \
  "s_and_b32 s93, s93, 4\n\t" \
  "s_lshl_b32 s93, s93, 11\n\t" \
  "s_lshl2_add_u32 %[so], %[sh], s93\n\t" \
  "s_waitcnt vmcnt(7)\n\t" \
  "v_mfma_f32_16x16x32_bf16 v[132:135], v[68:71], v[212:215], v[132:135]\n\t" \
  "v_mfma_f32_16x16x32_bf16 v[140:143], v[76:79], v[212:215], v[140:143]\n\t" \
  "s_sub_i32 s93, %[step], 2\n\t" \
  "s_lshl_b32 %[st], s93, 19\n\t" \
  "s_lshl_b32 s94, s93, 11\n\t" \
  "s_xor_b32 s94, s94, %[s0]\n\t" \
  "s_and_b32 s94, s94, 0x2000\n\t" \
  "s_or_b32 %[st], %[st], s94\n\t" \
  "s_cmp_lt_i32 s93, 0\n\t" \
  "s_cselect_b32 %[st], 0x70000000, %[st]\n\t" \
  "s_add_u32 %[st4], %[st], 4096\n\t" \
  "s_waitcnt vmcnt(6)\n\t" \
  "v_mfma_f32_16x16x32_bf16 v[128:131], v[80:83], v[216:219], v[128:131]\n\t" \
  "v_mfma_f32_16x16x32_bf16 v[136:139], v[88:91], v[216:219], v[136:139]\n\t" \
  "s_waitcnt vmcnt(5)\n\t" \
  "v_mfma_f32_16x16x32_bf16 v[132:135], v[84:87], v[220:223], v[132:135]\n\t" \
  "v_mfma_f32_16x16x32_bf16 v[140:143], v[92:95], v[220:223], v[140:143]\n\t" \
  "s_cmp_eq_u32 %[step], 63\n\t" \
  "s_cbranch_scc1 .Llast%=\n\t" \
  "s_movk_i32 s93, 600\n\t" \
  ".Lpacel%=:\n\t" \
  "s_memrealtime s[90:91]\n\t" \
  "s_waitcnt lgkmcnt(0)\n\t" \
  "s_lshl_b32 s90, s90, 2\n\t" \
  "s_sub_i32 s91, %[E], s90\n\t" \
  "s_cmp_le_i32 s91, 0\n\t" \
  "s_cbranch_scc1 .Lgol%=\n\t" \
  "s_sub_u32 s93, s93, 1\n\t" \
  "s_cmp_lg_u32 s93, 0\n\t" \
  "s_cbranch_scc1 .Lpacel%=\n\t" \
  ".Lgol%=:\n\t" \
  "buffer_load_dwordx2 v[166:167], v173, %[rs], 0 offen sc1\n\t" \
  "s_waitcnt vmcnt(5)\n\t" \
  "v_mfma_f32_16x16x32_bf16 v[128:131], v[96:99], v[176:179], v[128:131]\n\t" \
  "v_mfma_f32_16x16x32_bf16 v[136:139], v[104:107], v[176:179], v[136:139]\n\t" \
  "s_waitcnt vmcnt(4)\n\t" \
  "v_mfma_f32_16x16x32_bf16 v[132:135], v[100:103], v[180:183], v[132:135]\n\t" \
  "v_mfma_f32_16x16x32_bf16 v[140:143], v[108:111], v[180:183], v[140:143]\n\t" \
  "s_waitcnt vmcnt(3)\n\t" \
  "v_mfma_f32_16x16x32_bf16 v[128:131], v[112:115], v[184:187], v[128:131]\n\t" \
  "v_mfma_f32_16x16x32_bf16 v[136:139], v[120:123], v[184:187], v[136:139]\n\t" \
  "s_waitcnt vmcnt(2)\n\t" \
  "v_mfma_f32_16x16x32_bf16 v[132:135], v[116:119], v[188:191], v[132:135]\n\t" \
  "v_mfma_f32_16x16x32_bf16 v[140:143], v[124:127], v[188:191], v[140:143]\n\t" \
  "v_mov_b64 v[144:145], 0\n\t" \
  "v_mov_b64 v[146:147], 0\n\t" \
  "v_mov_b64 v[148:149], 0\n\t" \
  "v_mov_b64 v[150:151], 0\n\t" \
  "v_mov_b64 v[152:153], 0\n\t" \
  "v_mov_b64 v[154:155], 0\n\t" \
  "v_mov_b64 v[156:157], 0\n\t" \
  "v_mov_b64 v[158:159], 0\n\t" \
  "v_pk_add_f32 v[128:129], v[128:129], v[132:133]\n\t" \
  "v_pk_add_f32 v[130:131], v[130:131], v[134:135]\n\t" \
  "ds_write_b128 v230, v[128:131] offset:0\n\t" \
  "v_pk_add_f32 v[136:137], v[136:137], v[140:141]\n\t" \
  "v_pk_add_f32 v[138:139], v[138:139], v[142:143]\n\t" \
  "ds_write_b128 v230, v[136:139] offset:1024\n\t" \
  "v_xor_b32 v230, 0x4000, v230\n\t" \
  "s_cmp_lg_u32 %[step], 63\n\t" \
  "s_cbranch_scc1 .Lstep%=\n\t" \
  ".Lfc%=:\n\t" \
  "s_waitcnt vmcnt(0)\n\t" \
  "v_cmp_eq_u32 vcc, -1, v166\n\t" \
  "s_cbranch_vccnz .Lfw%=\n\t" \
  ".Lfo%=:\n\t" \
  "v_add_u32 v242, 16384, v242\n\t" \
  "buffer_load_dwordx4 v[176:179], v238, %[rt], %[st] offen offset:0 sc1\n\t" \
  "buffer_load_dwordx4 v[180:183], v238, %[rt], %[st] offen offset:1024 sc1\n\t" \
  "buffer_load_dwordx4 v[184:187], v238, %[rt], %[st4] offen offset:0 sc1\n\t" \
  "buffer_load_dwordx4 v[188:191], v238, %[rt], %[st4] offen offset:1024 sc1\n\t" \
  "buffer_load_dwordx4 v[192:195], v239, %[rt], %[st] offen offset:0 sc1\n\t" \
  "buffer_load_dwordx4 v[196:199], v239, %[rt], %[st] offen offset:1024 sc1\n\t" \
  "buffer_load_dwordx4 v[200:203], v239, %[rt], %[st4] offen offset:0 sc1\n\t" \
  "buffer_load_dwordx4 v[204:207], v239, %[rt], %[st4] offen offset:1024 sc1\n\t" \
  "v_subrev_u32 v230, 98304, v230\n\t" \
  ".Lt%=:\n\t" \
  "s_waitcnt vmcnt(7)\n\t" \
  "v_mfma_f32_16x16x32_bf16 v[128:131], v[0:3], v[176:179], 0\n\t" \
  "v_mfma_f32_16x16x32_bf16 v[136:139], v[8:11], v[176:179], 0\n\t" \
  "buffer_load_dwordx4 v[176:179], v240, %[rt], %[st] offen offset:0 sc1\n\t" \
  "buffer_load_dword v166, v242, %[rs], 0 offen sc1\n\t" \
  "s_waitcnt vmcnt(8)\n\t" \
  "v_mfma_f32_16x16x32_bf16 v[132:135], v[4:7], v[180:183], 0\n\t" \
  "v_mfma_f32_16x16x32_bf16 v[140:143], v[12:15], v[180:183], 0\n\t" \
  "buffer_load_dwordx4 v[180:183], v240, %[rt], %[st] offen offset:1024 sc1\n\t" \
  "s_waitcnt vmcnt(8)\n\t" \
  "v_mfma_f32_16x16x32_bf16 v[128:131], v[16:19], v[184:187], v[128:131]\n\t" \
  "v_mfma_f32_16x16x32_bf16 v[136:139], v[24:27], v[184:187], v[136:139]\n\t" \
  "buffer_load_dwordx4 v[184:187], v240, %[rt], %[st4] offen offset:0 sc1\n\t" \
  "s_waitcnt vmcnt(8)\n\t" \
  "v_mfma_f32_16x16x32_bf16 v[132:135], v[20:23], v[188:191], v[132:135]\n\t" \
  "v_mfma_f32_16x16x32_bf16 v[140:143], v[28:31], v[188:191], v[140:143]\n\t" \
  "buffer_load_dwordx4 v[188:191], v240, %[rt], %[st4] offen offset:1024 sc1\n\t" \
  "s_waitcnt vmcnt(8)\n\t" \
  "v_mfma_f32_16x16x32_bf16 v[128:131], v[32:35], v[192:195], v[128:131]\n\t" \
  "v_mfma_f32_16x16x32_bf16 v[136:139], v[40:43], v[192:195], v[136:139]\n\t" \
  "buffer_load_dwordx4 v[192:195], v241, %[rt], %[st] offen offset:0 sc1\n\t" \
  "s_waitcnt vmcnt(8)\n\t" \
  "v_mfma_f32_16x16x32_bf16 v[132:135], v[36:39], v[196:199], v[132:135]\n\t" \
  "v_mfma_f32_16x16x32_bf16 v[140:143], v[44:47], v[196:199], v[140:143]\n\t" \
  "buffer_load_dwordx4 v[196:199], v241, %[rt], %[st] offen offset:1024 sc1\n\t" \
  "s_waitcnt vmcnt(8)\n\t" \
  "v_mfma_f32_16x16x32_bf16 v[128:131], v[48:51], v[200:203], v[128:131]\n\t" \
  "v_mfma_f32_16x16x32_bf16 v[136:139], v[56:59], v[200:203], v[136:139]\n\t" \
  "buffer_load_dwordx4 v[200:203], v241, %[rt], %[st4] offen offset:0 sc1\n\t" \
  "s_waitcnt vmcnt(8)\n\t" \
  "v_mfma_f32_16x16x32_bf16 v[132:135], v[52:55], v[204:207], v[132:135]\n\t" \
  "v_mfma_f32_16x16x32_bf16 v[140:143], v[60:63], v[204:207], v[140:143]\n\t" \
  "buffer_load_dwordx4 v[204:207], v241, %[rt], %[st4] offen offset:1024 sc1\n\t" \
  "s_waitcnt vmcnt(8)\n\t" \
  "v_mfma_f32_16x16x32_bf16 v[128:131], v[64:67], v[176:179], v[128:131]\n\t" \
  "v_mfma_f32_16x16x32_bf16 v[136:139], v[72:75], v[176:179], v[136:139]\n\t" \
  "s_waitcnt vmcnt(7)\n\t" \
  ".Ltc%=:\n\t" \
  "v_cmp_eq_u32 vcc, -1, v166\n\t" \
  "s_cbranch_vccnz .Ltw%=\n\t" \
  ".Lto%=:\n\t" \
  "s_cmp_eq_u32 %[step], 65\n\t" \
  "s_cbranch_scc1 .Ltl%=\n\t" \
  "s_add_u32 %[step], %[step], 1\n\t" \
  "s_sub_i32 s93, %[step], 2\n\t" \
  "s_lshl_b32 %[st], s93, 19\n\t" \
  "s_lshl_b32 s94, s93, 11\n\t" \
  "s_xor_b32 s94, s94, %[s0]\n\t" \
  "s_and_b32 s94, s94, 0x2000\n\t" \
  "s_or_b32 %[st], %[st], s94\n\t" \
  "s_cmp_lt_i32 s93, 0\n\t" \
  "s_cselect_b32 %[st], 0x70000000, %[st]\n\t" \
  "s_add_u32 %[st4], %[st], 4096\n\t" \
  "v_add_u32 v242, 16384, v242\n\t" \
  "s_cmp_eq_u32 %[step], 65\n\t" \
  "s_cbranch_scc0 .Ltn%=\n\t" \
  "v_mov_b32 v242, %[fg]\n\t" \
  ".Ltn%=:\n\t" \
  "buffer_load_dwordx4 v[176:179], v238, %[rt], %[st] offen offset:0 sc1\n\t" \
  "s_waitcnt vmcnt(7)\n\t" \
  "v_mfma_f32_16x16x32_bf16 v[132:135], v[68:71], v[180:183], v[132:135]\n\t" \
  "v_mfma_f32_16x16x32_bf16 v[140:143], v[76:79], v[180:183], v[140:143]\n\t" \
  "buffer_load_dwordx4 v[180:183], v238, %[rt], %[st] offen offset:1024 sc1\n\t" \
  "s_waitcnt vmcnt(7)\n\t" \
  "v_mfma_f32_16x16x32_bf16 v[128:131], v[80:83], v[184:187], v[128:131]\n\t" \
  "v_mfma_f32_16x16x32_bf16 v[136:139], v[88:91], v[184:187], v[136:139]\n\t" \
  "buffer_load_dwordx4 v[184:187], v238, %[rt], %[st4] offen offset:0 sc1\n\t" \
  "s_waitcnt vmcnt(7)\n\t" \
  "v_mfma_f32_16x16x32_bf16 v[132:135], v[84:87], v[188:191], v[132:135]\n\t" \
  "v_mfma_f32_16x16x32_bf16 v[140:143], v[92:95], v[188:191], v[140:143]\n\t" \
  "buffer_load_dwordx4 v[188:191], v238, %[rt], %[st4] offen offset:1024 sc1\n\t" \
  "s_waitcnt vmcnt(7)\n\t" \
  "v_mfma_f32_16x16x32_bf16 v[128:131], v[96:99], v[192:195], v[128:131]\n\t" \
  "v_mfma_f32_16x16x32_bf16 v[136:139], v[104:107], v[192:195], v[136:139]\n\t" \
  "buffer_load_dwordx4 v[192:195], v239, %[rt], %[st] offen offset:0 sc1\n\t" \
  "s_waitcnt vmcnt(7)\n\t" \
  "v_mfma_f32_16x16x32_bf16 v[132:135], v[100:103], v[196:199], v[132:135]\n\t" \
  "v_mfma_f32_16x16x32_bf16 v[140:143], v[108:111], v[196:199], v[140:143]\n\t" \
  "buffer_load_dwordx4 v[196:199], v239, %[rt], %[st] offen offset:1024 sc1\n\t" \
  "s_waitcnt vmcnt(7)\n\t" \
  "v_mfma_f32_16x16x32_bf16 v[128:131], v[112:115], v[200:203], v[128:131]\n\t" \
  "v_mfma_f32_16x16x32_bf16 v[136:139], v[120:123], v[200:203], v[136:139]\n\t" \
  "buffer_load_dwordx4 v[200:203], v239, %[rt], %[st4] offen offset:0 sc1\n\t" \
  "s_waitcnt vmcnt(7)\n\t" \
  "v_mfma_f32_16x16x32_bf16 v[132:135], v[116:119], v[204:207], v[132:135]\n\t" \
  "v_mfma_f32_16x16x32_bf16 v[140:143], v[124:127], v[204:207], v[140:143]\n\t" \
  "buffer_load_dwordx4 v[204:207], v239, %[rt], %[st4] offen offset:1024 sc1\n\t" \
  "s_nop 5\n\t" \
  "v_pk_add_f32 v[128:129], v[128:129], v[132:133]\n\t" \
  "v_pk_add_f32 v[130:131], v[130:131], v[134:135]\n\t" \
  "ds_write_b128 v230, v[128:131] offset:0\n\t" \
  "v_pk_add_f32 v[136:137], v[136:137], v[140:141]\n\t" \
  "v_pk_add_f32 v[138:139], v[138:139], v[142:143]\n\t" \
  "ds_write_b128 v230, v[136:139] offset:1024\n\t" \
  "v_add_u32 v230, 16384, v230\n\t" \
  "s_branch .Lt%=\n\t" \
  ".Ltl%=:\n\t" \
  "buffer_load_dwordx4 v[224:227], v243, %[rg], 0 offen sc1\n\t" \
  "s_waitcnt vmcnt(7)\n\t" \
  "v_mfma_f32_16x16x32_bf16 v[132:135], v[68:71], v[180:183], v[132:135]\n\t" \
  "v_mfma_f32_16x16x32_bf16 v[140:143], v[76:79], v[180:183], v[140:143]\n\t" \
  "s_waitcnt vmcnt(6)\n\t" \
  "v_mfma_f32_16x16x32_bf16 v[128:131], v[80:83], v[184:187], v[128:131]\n\t" \
  "v_mfma_f32_16x16x32_bf16 v[136:139], v[88:91], v[184:187], v[136:139]\n\t" \
  "s_waitcnt vmcnt(5)\n\t" \
  "v_mfma_f32_16x16x32_bf16 v[132:135], v[84:87], v[188:191], v[132:135]\n\t" \
  "v_mfma_f32_16x16x32_bf16 v[140:143], v[92:95], v[188:191], v[140:143]\n\t" \
  "s_waitcnt vmcnt(4)\n\t" \
  "v_mfma_f32_16x16x32_bf16 v[128:131], v[96:99], v[192:195], v[128:131]\n\t" \
  "v_mfma_f32_16x16x32_bf16 v[136:139], v[104:107], v[192:195], v[136:139]\n\t" \
  "s_waitcnt vmcnt(3)\n\t" \
  "v_mfma_f32_16x16x32_bf16 v[132:135], v[100:103], v[196:199], v[132:135]\n\t" \
  "v_mfma_f32_16x16x32_bf16 v[140:143], v[108:111], v[196:199], v[140:143]\n\t" \
  "s_waitcnt vmcnt(2)\n\t" \
  "v_mfma_f32_16x16x32_bf16 v[128:131], v[112:115], v[200:203], v[128:131]\n\t" \
  "v_mfma_f32_16x16x32_bf16 v[136:139], v[120:123], v[200:203], v[136:139]\n\t" \
  "s_waitcnt vmcnt(1)\n\t" \
  "v_mfma_f32_16x16x32_bf16 v[132:135], v[116:119], v[204:207], v[132:135]\n\t" \
  "v_mfma_f32_16x16x32_bf16 v[140:143], v[124:127], v[204:207], v[140:143]\n\t" \
  "s_nop 6\n\t" \
  "v_pk_add_f32 v[128:129], v[128:129], v[132:133]\n\t" \
  "v_pk_add_f32 v[130:131], v[130:131], v[134:135]\n\t" \
  "ds_write_b128 v230, v[128:131] offset:0\n\t" \
  "v_pk_add_f32 v[136:137], v[136:137], v[140:141]\n\t" \
  "v_pk_add_f32 v[138:139], v[138:139], v[142:143]\n\t" \
  "ds_write_b128 v230, v[136:139] offset:1024\n\t" \
  "s_waitcnt vmcnt(0) lgkmcnt(0)\n\t" \
  "s_branch .Lend%=\n\t" \
  ".Ltw%=:\n\t" \
  "s_add_u32 s92, s92, 1\n\t" \
  "s_sleep 2\n\t" \
  "s_and_b32 s93, s92, 63\n\t" \
  "s_cbranch_scc1 .Ltr%=\n\t" \
  "buffer_load_dword v164, off, %[rs], %[sab] sc1\n\t" \
  "s_waitcnt vmcnt(0)\n\t" \
  "v_readfirstlane_b32 s93, v164\n\t" \
  "s_nop 3\n\t" \
  "s_cmp_eq_u32 s93, 1\n\t" \
  "s_cbranch_scc1 .Lto%=\n\t" \
  "s_cmp_lt_u32 s92, 0x80000\n\t" \
  "s_cbranch_scc1 .Ltr%=\n\t" \
  "v_mov_b32 v164, 1\n\t" \
  "buffer_store_dword v164, off, %[rs], %[sab] sc1\n\t" \
  "s_waitcnt vmcnt(0)\n\t" \
  "s_branch .Lto%=\n\t" \
  ".Ltr%=:\n\t" \
  "buffer_load_dword v166, v242, %[rs], 0 offen sc1\n\t" \
  "s_waitcnt vmcnt(0)\n\t" \
  "s_branch .Ltc%=\n\t" \
  ".Llast%=:\n\t" \
  "v_mov_b32 v173, v242\n\t" \
  "s_branch .Lgol%=\n\t" \
  ".Lfw%=:\n\t" \
  "s_add_u32 s92, s92, 1\n\t" \
  "s_sleep 2\n\t" \
  "s_and_b32 s93, s92, 63\n\t" \
  "s_cbranch_scc1 .Lfr%=\n\t" \
  "buffer_load_dword v164, off, %[rs], %[sab] sc1\n\t" \
  "s_waitcnt vmcnt(0)\n\t" \
  "v_readfirstlane_b32 s93, v164\n\t" \
  "s_nop 3\n\t" \
  "s_cmp_eq_u32 s93, 1\n\t" \
  "s_cbranch_scc1 .Lfo%=\n\t" \
  "s_cmp_lt_u32 s92, 0x80000\n\t" \
  "s_cbranch_scc1 .Lfr%=\n\t" \
  "v_mov_b32 v164, 1\n\t" \
  "buffer_store_dword v164, off, %[rs], %[sab] sc1\n\t" \
  "s_waitcnt vmcnt(0)\n\t" \
  "s_branch .Lfo%=\n\t" \
  ".Lfr%=:\n\t" \
  "buffer_load_dwordx2 v[166:167], v173, %[rs], 0 offen sc1\n\t" \
  "s_branch .Lfc%=\n\t" \
  ".Lb1%=:\n\t" \
  "s_waitcnt vmcnt(0) lgkmcnt(0)\n\t" \
  "s_barrier\n\t" \
  "s_branch .Lb2%=\n\t" \
  ".Lbl%=:\n\t" \
  "s_bitset1_b64 %[ps], %[step]\n\t" \
  "s_cmp_lt_u32 s92, 3\n\t" \
  "s_cbranch_scc1 .Lbm%=\n\t" \
  "s_sleep 12\n\t" \
  ".Lbm%=:\n\t" \
  "s_add_u32 s92, s92, 1\n\t" \
  "s_sleep 2\n\t" \
  "s_and_b32 s93, s92, 63\n\t" \
  "s_cbranch_scc1 .Lba%=\n\t" \
  "buffer_load_dword v164, off, %[rs], %[sab] sc1\n\t" \
  "s_waitcnt vmcnt(0)\n\t" \
  "v_readfirstlane_b32 s93, v164\n\t" \
  "s_nop 3\n\t" \
  "s_cmp_eq_u32 s93, 1\n\t" \
  "s_cbranch_scc1 .Lgo%=\n\t" \
  "s_cmp_lt_u32 s92, 0x80000\n\t" \
  "s_cbranch_scc1 .Lba%=\n\t" \
  "v_mov_b32 v164, 1\n\t" \
  "buffer_store_dword v164, off, %[rs], %[sab] sc1\n\t" \
  "s_waitcnt vmcnt(0)\n\t" \
  "s_branch .Lgo%=\n\t" \
  ".Lba%=:\n\t" \
  "buffer_load_dwordx2 v[166:167], v173, %[rs], 0 offen sc1\n\t" \
  "s_branch .Lcal%=\n\t" \
  ".Lslow%=:\n\t" \
  "s_mov_b64 s[94:95], 0\n\t" \
  "v_cmp_eq_u32 vcc, %[sent], v128\n\t" \
  "s_or_b64 s[94:95], s[94:95], vcc\n\t" \
  "v_cmp_eq_u32 vcc, %[sent], v129\n\t" \
  "s_or_b64 s[94:95], s[94:95], vcc\n\t" \
  "v_cmp_eq_u32 vcc, %[sent], v130\n\t" \
  "s_or_b64 s[94:95], s[94:95], vcc\n\t" \
  "v_cmp_eq_u32 vcc, %[sent], v131\n\t" \
  "s_or_b64 s[94:95], s[94:95], vcc\n\t" \
  "v_cmp_eq_u32 vcc, %[sent], v132\n\t" \
  "s_or_b64 s[94:95], s[94:95], vcc\n\t" \
  "v_cmp_eq_u32 vcc, %[sent], v133\n\t" \
  "s_or_b64 s[94:95], s[94:95], vcc\n\t" \
  "v_cmp_eq_u32 vcc, %[sent], v134\n\t" \
  "s_or_b64 s[94:95], s[94:95], vcc\n\t" \
  "v_cmp_eq_u32 vcc, %[sent], v135\n\t" \
  "s_or_b64 s[94:95], s[94:95], vcc\n\t" \
  "v_cmp_eq_u32 vcc, %[sent], v136\n\t" \
  "s_or_b64 s[94:95], s[94:95], vcc\n\t" \
  "v_cmp_eq_u32 vcc, %[sent], v137\n\t" \
  "s_or_b64 s[94:95], s[94:95], vcc\n\t" \
  "v_cmp_eq_u32 vcc, %[sent], v138\n\t" \
  "s_or_b64 s[94:95], s[94:95], vcc\n\t" \
  "v_cmp_eq_u32 vcc, %[sent], v139\n\t" \
  "s_or_b64 s[94:95], s[94:95], vcc\n\t" \
  "v_cmp_eq_u32 vcc, %[sent], v140\n\t" \
  "s_or_b64 s[94:95], s[94:95], vcc\n\t" \
  "v_cmp_eq_u32 vcc, %[sent], v141\n\t" \
  "s_or_b64 s[94:95], s[94:95], vcc\n\t" \
  "v_cmp_eq_u32 vcc, %[sent], v142\n\t" \
  "s_or_b64 s[94:95], s[94:95], vcc\n\t" \
  "v_cmp_eq_u32 vcc, %[sent], v143\n\t" \
  "s_or_b64 s[94:95], s[94:95], vcc\n\t" \
  "s_cbranch_scc0 .Lok%=\n\t" \
  "s_bitset1_b64 %[fm], %[step]\n\t" \
  "s_add_u32 s92, s92, 1\n\t" \
  "s_sleep 1\n\t" \
  "s_and_b32 s93, s92, 63\n\t" \
  "s_cbranch_scc1 .Lre%=\n\t" \
  "buffer_load_dword v164, off, %[rs], %[sab] sc1\n\t" \
  "s_waitcnt vmcnt(0)\n\t" \
  "v_readfirstlane_b32 s93, v164\n\t" \
  "s_nop 3\n\t" \
  "s_cmp_eq_u32 s93, 1\n\t" \
  "s_cbranch_scc1 .Lok%=\n\t" \
  "s_cmp_lt_u32 s92, 0x80000\n\t" \
  "s_cbranch_scc1 .Lre%=\n\t" \
  "v_mov_b32 v164, 1\n\t" \
  "buffer_store_dword v164, off, %[rs], %[sab] sc1\n\t" \
  "s_waitcnt vmcnt(0)\n\t" \
  "s_branch .Lok%=\n\t" \
  ".Lre%=:\n\t" \
  "buffer_load_dwordx4 v[128:131], v234, %[rs], %[so] offen sc1\n\t" \
  "s_nop 15\n\t" \
  "buffer_load_dwordx4 v[132:135], v235, %[rs], %[so] offen sc1\n\t" \
  "s_nop 15\n\t" \
  "buffer_load_dwordx4 v[136:139], v236, %[rs], %[so] offen sc1\n\t" \
  "s_nop 15\n\t" \
  "buffer_load_dwordx4 v[140:143], v237, %[rs], %[so] offen sc1\n\t" \
  "v_mov_b64 v[144:145], 0\n\t" \
  "v_mov_b64 v[146:147], 0\n\t" \
  "v_mov_b64 v[148:149], 0\n\t" \
  "v_mov_b64 v[150:151], 0\n\t" \
  "v_mov_b64 v[152:153], 0\n\t" \
  "v_mov_b64 v[154:155], 0\n\t" \
  "v_mov_b64 v[156:157], 0\n\t" \
  "v_mov_b64 v[158:159], 0\n\t" \
  "s_waitcnt vmcnt(0)\n\t" \
  "s_branch .Lw%=\n\t" \
  ".Lend%=:\n\t"

__device__ __forceinline__ bf16x8 get4(const u32x32& W, int i) {
  return as_bf16x8((u32x4){W[4 * i], W[4 * i + 1], W[4 * i + 2], W[4 * i + 3]});
}
__device__ __forceinline__ int look_prog(__amdgpu_buffer_rsrc_t rs, int off) {
  asm volatile("" ::: "memory");
  const int v = (int)ld_flag(rs, off);
  return min(__builtin_amdgcn_readlane(v, 0), __builtin_amdgcn_readlane(v, 1));
}
__device__ __forceinline__ int wait_prog(__amdgpu_buffer_rsrc_t rs, int off, int need, gu32* abortw) {
  int v;
  for (unsigned spins = 0;;) {
    v = look_prog(rs, off);
    if (v >= need) break;
    __builtin_amdgcn_s_sleep(4);
    if (spin_fail(spins, abortw)) break;
  }
  asm volatile("" ::: "memory");
  return v;
}
#define CLB_S "s90", "s91", "s92", "s93", "s94", "s95", "vcc", "scc", "memory"
#define W_IN "{v[0:31]}"(W[0]), "{v[32:63]}"(W[1]), "{v[64:95]}"(W[2]), "{v[96:127]}"(W[3]), "{v[128:159]}"(W[4]), "{v[160:191]}"(W[5])

#define W_DELAY 0
#define LEAD0 395
#define LEAD_D0 6
#define QBJ 3
constexpr int L_MEM = 0, L_KT = 16384, L_QX = 24576, L_KX = 26624, L_VX = 16384, L_MISC = 30720, L_WL = 32768;

#define QK_STEP(TXT)                                                                                                        \
  asm volatile(TXT                                                                                                          \
      : [tout] "=&s"(tout), [spins] "=&s"(spins), [tb1] "=&s"(tb1), [tgt] "=&s"(tgt), "+{v248}"(aL16), "+{v249}"(aMW), "+{v250}"(aWL),                            \
        "+{v251}"(aIDX), "+{v252}"(aQW), "+{v[232:235]}"(QF)                                                                 \
      : W_IN, [rs] "s"(rs4), [som] "s"(o_mem(pm, s) + WV * 1024), [som2] "s"(o_mem(pm, s) + WV * 1024 + 16384), [sent] "s"(DSENT), [sab] "s"(SY_ABORT), \
        [lead] "s"(lead), [step] "s"(s), [sbc] "s"(SY_BA + side(pm, 1) + h * 128), [nh] "s"(0xFF00FF00FF00FF00ull), [up] "s"(0xFFFFFFFF00000000ull), [negc] "s"(-cexp),      \
        [c] "s"(cexp), [so] "s"(o_o(pm, s, h) + WV * 512), [sk] "s"(o_v(pm, s, h) + 2048 + (WV & 3) * 512),                    \
        [sv] "s"(o_v(pm, s, h) + WV * 512), [rt] "s"(rt4), [svt] "s"(t_off(p, T_V + kseg(s, h)) + WV * 2048)                  \
      : CLB8(192, 193, 194, 195, 196, 197, 198, 199), CLB8(200, 201, 202, 203, 204, 205, 206, 207),                          \
        CLB8(208, 209, 210, 211, 212, 213, 214, 215), CLB8(216, 217, 218, 219, 220, 221, 222, 223),                          \
        CLB8(224, 225, 226, 227, 228, 229, 230, 231), CLB8(236, 237, 238, 239, 240, 241, 242, 243), "v244", "v245", "v246", "v247", "v253", "s88", "s89", CLB_S)

template <int WV>
__device__ __forceinline__ void qk_wave(Args& p, int h, char* smem) {
  const int tid = threadIdx.x;
  unsigned pm = 0;
  int have = -1;
  u32x32 W[6];
  {
    int lane = tid & 63;
    asm volatile("" : "+v"(lane));
    const unsigned vo = (WV * 4 + (lane >> 4)) * (HD * 2) + 16 * ((lane & 15) ^ swzB((WV * 4 + (lane >> 4)) & 15));
    auto stage = [&](int j) {
#pragma unroll
      for (int mm = 0; mm < 2; ++mm) {
        const char* src = (const char*)((mm ? p.Wk : p.Wq) + (size_t)j * 128 * HD + h * DH);
        const unsigned dst = lds_off(smem) + (~j & 1) * 65536 + mm * 32768 + WV * 1024;
        glds2(src, 32 * HD * 2, vo, dst);
        glds2(src + 64 * HD * 2, 32 * HD * 2, vo, dst + 16384);
      }
    };
    bf16x8 keep[4][4];
    auto place = [&](int g) {
#pragma unroll
      for (int i = 0; i < 4; ++i)
        *reinterpret_cast<bf16x8*>(smem + (g == 0 ? 65536 : g == 1 ? 32768 : g == 2 ? 131072 : 98304) + WV * 4096 + i * 1024 + lane * 16) = keep[g][i];
    };
    stage(0); stage(1);
    Boot bt;
#pragma unroll
    for (int j = 0; j < 8; ++j) {
      if (j < 7) WAIT_V(8); else WAIT_V(0);
      if (WV == 7 && j == QBJ + 1) pm = boot_done(p.ws, bt);
      LBAR();
#pragma unroll
      for (int k = 0; k < 8; ++k) {
        const int P = (QK_MAP[WV] >> (4 * (2 * j + (k >> 2)))) & 15;
        const bf16x8 fr = ld_bfrag(smem + (~j & 1) * 65536 + (WV >> 2) * 32768 + (k >> 2) * 16384, k & 1, 2 * (WV & 3) + ((k >> 1) & 1), lane);
        const int f = P * 4 + (k & 3);
        if (P < 12) put4(W[f >> 3], f & 7, fr);
        else keep[P - 12][k & 3] = fr;
      }
      if (j == (WV >> 1)) place(2);
      LBAR();
      if (WV == 7 && j == QBJ) boot_ask(p.ws, bt);
      if (j + 2 < 8) stage(j + 2);
      if (j == 6) { place(0); place(3); }
    }
    place(1);
    if (WV == 7 && lane < 8) lds_at(smem + L_MISC)[lane] = lane == 7 ? pm : (lane == 1 ? now_q() : 0);
    if (WV == 6) lds_at(smem + 28672)[lane] = ~0u;
    LBAR();
    pm = uni(lds_at(smem + L_MISC)[7]);
    p.s0 = pm >> 31;
    if (WV == 6) RDW(h);
    if (WV == 4 || WV == 5) {
      have = wait_prog(mkrsrc(p.ws + WS_SY, SY_TOTAL), fkq(h, (lane & 1) * 2), 0, (gu32*)(p.ws + WS_SY + SY_ABORT));
      const char* src = p.ws + WS_T + t_off(p, kseg(0, h)) + (WV - 4) * 4096 + lane * 16;
#pragma unroll
      for (int i = 0; i < 4; ++i) glds16_sc1(src + i * 1024, smem + L_KT + (WV - 4) * 4096 + i * 1024);
    }
  }
  const __amdgpu_buffer_rsrc_t rsy = mkrsrc(p.ws + WS_SY, SY_TOTAL);
  const u32x4 rs4 = raw_rsrc(p.ws + WS_SY, SY_TOTAL);
  const u32x4 rt4 = raw_rsrc(p.ws + WS_T, T_BYTES);
  gu32* abortw = (gu32*)(p.ws + WS_SY + SY_ABORT);
  const float cexp = 0.08838834764831845f * 1.4426950408889634f;
  constexpr int m = WV >> 2, b = WV & 3;
  int lead = LEAD0;
  unsigned tb1 = 0;
  unsigned long long fm = 0, ps = 0; (void)fm; (void)ps;
  for (int s = 0; s < NSEG; ++s) {
    int lane = tid & 63;
    asm volatile("" : "+v"(lane));
    const int r = lane & 15, q = lane >> 4;
    unsigned aL16 = lane * 16, aMW = aL16 + WV * 1024, aWL = aL16 + 98304 + WV * 4096;
    unsigned aIDX = (lane & 2) ? 0xEEEE : 0x4444;
    unsigned aQW = (32 * b + r) * 16 + q * 4 + (m ? L_KX : L_QX);
    u32x4 QF;
    asm volatile("" : "={v[232:235]}"(QF));
    if (WV < 4) {
      QF[0] = L_QX + (q * 8 + (r >> 2)) * 16 + (r & 1) * 8;
      QF[1] = ((2 * (q & 1) + (q >> 1)) * 16 + r) * 4;
      QF[2] = ((r >> 2) & 1) * 65536 + ((r >> 1) & 1) * 64 + (r >> 3) * 32 + (r & 1) * 16 + (q >> 1) * 256 + (q & 1) * 8;
    } else QF[3] = r * 16 + q * 4;
    unsigned tout, spins, tgt;
    if (WV == 0) QK_STEP(QK_ASM_0); else if (WV == 1) QK_STEP(QK_ASM_1); else if (WV == 2) QK_STEP(QK_ASM_2);
    else if (WV == 3) QK_STEP(QK_ASM_3); else if (WV == 4) QK_STEP(QK_ASM_4); else if (WV == 5) QK_STEP(QK_ASM_5);
    else if (WV == 6) QK_STEP(QK_ASM_6); else QK_STEP(QK_ASM_7);
    tout = __builtin_amdgcn_readfirstlane(tout);
    tb1 = __builtin_amdgcn_readfirstlane(tb1);
    lane = tid & 63;
    asm volatile("" : "+v"(lane));
    if (WV < 4) {
      lds_at(smem + L_MISC + 8)[WV] = s + 1;
      if (WV == 3) lds_at(smem + L_MISC)[6] = time_of(tout);
    }
    if (WV == 6) {
      if (s + 1 < NSEG) watch(rsy, s, lane, now_q() + W_DELAY, (const unsigned*)(smem + L_MISC), abortw, side(pm, 1));
    } else if (WV >= 4 && s + 1 < NSEG) {
      unsigned t;
      do {
        __builtin_amdgcn_s_sleep(1);
        t = lds_at(smem + L_MISC + 8)[lane & 3];
      } while (!__all(t == (unsigned)(s + 1)));
      if (WV <= 5) {
        if (s + 2 < NSEG) {
          if (have < s + 1) have = wait_prog(rsy, fkq(h, (lane & 1) * 2), s + 1, abortw);
          const char* src = p.ws + WS_T + t_off(p, kseg(s + 1, h)) + (WV - 4) * 4096 + lane * 16;
#pragma unroll
          for (int i = 0; i < 4; ++i) glds16_sc1(src + i * 1024, smem + L_KT + (WV - 4) * 4096 + i * 1024);
          if (have < s + 2) have = look_prog(rsy, fkq(h, (lane & 1) * 2));
        }
      } else {
        __builtin_amdgcn_s_sleep(6);
        const int x = (int)(__builtin_amdgcn_readfirstlane(lds_at(smem + L_MISC)[6]) - tb1);
        lead = __builtin_amdgcn_readfirstlane(track(lead, x, LEAD_UP, s < 8 ? LEAD_D0 : LEAD_DN, 60));
        SMP(2, s, x); SMP(3, s, lead);
      }
    }
  }
}

__device__ __forceinline__ void qk_role(Args& p, int h, char* smem) {
  switch (__builtin_amdgcn_readfirstlane(threadIdx.x >> 6)) {
    case 0: qk_wave<0>(p, h, smem); break;
    case 1: qk_wave<1>(p, h, smem); break;
    case 2: qk_wave<2>(p, h, smem); break;
    case 3: qk_wave<3>(p, h, smem); break;
    case 4: qk_wave<4>(p, h, smem); break;
    case 5: qk_wave<5>(p, h, smem); break;
    case 6: qk_wave<6>(p, h, smem); break;
    default: qk_wave<7>(p, h, smem); break;
  }
}

#define K_DUE 160
#define T_SL 2
#define O_DUE 400
#define LEAD_V 0

constexpr int V_VM = 16384, V_T = 32768, V_KI = 131072;
__device__ __forceinline__ void vu_role(Args& p, int h, char* smem) {
  const int tid = threadIdx.x, lane = tid & 63;
  const int w = __builtin_amdgcn_readfirstlane(tid >> 6);
  u32x32 W[4];
  {
    const unsigned vo = (tid >> 4) * (HD * 2) + 16 * ((lane & 15) ^ swzB((tid >> 4) & 15));
    auto stage = [&](int j) {
      const char* src = (const char*)(p.Wv + (size_t)j * 128 * HD + h * DH);
      const unsigned dst = lds_off(smem) + L_WL + (j & 3) * 32768 + w * 1024;
      glds2(src, 32 * HD * 2, vo, dst);
      glds2(src + 64 * HD * 2, 32 * HD * 2, vo, dst + 16384);
    };
    start_sy(p.ws, p.mem0, h, smem);
#pragma unroll
    for (int j = 0; j < 4; ++j) stage(j);
    Boot bt;
#pragma unroll
    for (int j = 0; j < 8; ++j) {
      if (j < 5) WAIT_V(12); else if (j == 5) WAIT_V(8); else if (j == 6) WAIT_V(4); else WAIT_V(0);
      if (j == 7 && w == 7) {
        const unsigned m = boot_done(p.ws, bt);
        if (lane < 8) lds_at(smem + L_MISC)[lane] = lane == 7 ? m : (lane == 1 ? now_q() : 0);
      }
      LBAR();
#pragma unroll
      for (int k = 0; k < 4; ++k) put4(W[j >> 1], (j & 1) * 4 + k, ld_bfrag(smem + L_WL + (j & 3) * 32768 + (k >> 1) * 16384, k & 1, w, lane));
      LBAR();
      if (j + 4 < 8) stage(j + 4);
      if (j == 5 && w == 7) boot_ask(p.ws, bt);
    }
    RDY(32 + h);
  }
  const __amdgpu_buffer_rsrc_t rsy = mkrsrc(p.ws + WS_SY, SY_TOTAL);
  const u32x4 rs4 = raw_rsrc(p.ws + WS_SY, SY_TOTAL);
  gu32* abortw = (gu32*)(p.ws + WS_SY + SY_ABORT);
  const unsigned pm = uni(lds_at(smem + L_MISC)[7]);
  p.s0 = pm >> 31;
  unsigned long long fm = 0; (void)fm;
  int have = -1;
  auto fetch = [&](int sg) {
    if (have < sg) have = wait_prog(rsy, fkq(h, w - 4), sg, abortw);
    const char* src = p.ws + WS_T + t_off(p, (w - 4) * T_Q + kseg(sg, h)) + lane * 16;
#pragma unroll
    for (int i = 0; i < 8; ++i) glds16_sc1(src + i * 1024, smem + V_T + (sg & 3) * 24576 + (w - 4) * 8192 + i * 1024);
  };
  const int fo = SY_FO + ((w & 3) * NH + h) * 128;
  typedef __attribute__((ext_vector_type(8))) float f32x8;
  f32x8 bias;
  u32x4 ta;
  {
    const int r = lane & 15, q = lane >> 4;
#pragma unroll
    for (int j = 0; j < 8; ++j) bias[j] = (j >> 2) * 16 + 4 * q + (j & 3) <= 16 * (w & 1) + r ? INFINITY : -INFINITY;
    ta[0] = V_KI + w * 2048 + lane * 16;
    ta[1] = V_KI + w * 2048 + (q * 8 + (r >> 2)) * 16 + (r & 1) * 8;
    ta[2] = V_VM + (w & 2) * 512 + ((2 * (q & 1) + (q >> 1)) * 16 + r) * 4;
    ta[3] = 0;
  }
#pragma unroll 1
  for (int s = 0; s < NSEG; ++s) {
    unsigned spins, tgt;
    asm volatile(VV_ASM
        : [spins] "=&s"(spins), [tgt] "=&s"(tgt)
        : W4_IN, "{v200}"(lane * 16), "{v201}"(lane * 16 + w * 2048), "{v202}"((lane & 2) ? 0xEEEE : 0x4444), "{v203}"(lane * 4),
          "{v204}"(tid * 4 + V_VM + (s & 1) * 2048), [rs] "s"(rs4), [som] "s"(o_mem(pm, s) + (w >> 2) * 16384 + (w & 3) * 2048), [step] "s"(s),
          [sent] "s"(DSENT), [sab] "s"(SY_ABORT), [sv] "s"(o_v(pm, s, h) + w * 256)
        : CLB32(128), CLB32(160), CLB8(192, 193, 194, 195, 196, 197, 198, 199), CLB_S);
    if (w < 4 || s == NSEG - 1) {
      const int sg = s - (w < 4);
      if (w >= 4) pace_until(now_q() + K_DUE);
      if (sg >= 0) {
        asm volatile(TK_ASM
            :
            : "{v200}"(lane * 16), "{v[208:215]}"(bias), "{v[216:219]}"(ta),
              [rs] "s"(rs4), [rt] "s"(raw_rsrc(p.ws + WS_T, T_BYTES)), [sent] "s"(DSENT), [sab] "s"(SY_ABORT), [sk] "s"(o_v(pm, sg, h) + 2048),
              [tl] "s"(V_T + (sg & 3) * 24576), [vp] "s"((sg & 1) * 2048), [tq] "s"((w & 1) * 4096), [tv] "s"((w & 2) * 2048),
              [so] "s"(t_off_o(p, T_O + (sg * 2 + (w & 1)) * 16 + (h >> 1)) + (h & 1) * 4096 + (w & 2) * 1024),
              [up] "s"(0xFFFFFFFF00000000ull), [c] "s"(1.4426950408889634f), [negc] "s"(-1.4426950408889634f),
              [sc] "s"((unsigned long long)__float_as_uint(0.08838834764831845f)), [od] "s"(s < NSEG - 1 ? O_DUE : 0)
            : CLB32(128), CLB32(160), CLB8(192, 193, 194, 195, 196, 197, 198, 199), CLB8(224, 225, 226, 227, 228, 229, 230, 231),
              "v205", "v206", "v207", "s88", "s89", CLB_S);
        if (sg >= NSEG - 3) {
          static_assert(TLAG == 2, "");
          WAIT_V(0);
          if (lane == 0) __builtin_amdgcn_raw_buffer_store_b32(1u, rsy, fo + (sg - NSEG + 3) * 4 * NH * 128, 0, 16);
        }
      }
    } else if (w == 7) {
      SMP(6, s, (int)(now_q() - tgt));
      watch(rsy, s, lane, now_q() + LEAD_V + W_DELAY, (const unsigned*)(smem + L_MISC), abortw, side(pm, 1));
    } else if (w >= 4) {
      __builtin_amdgcn_s_sleep(T_SL);
      const int s1 = w == 5 ? (s == NSEG - 2 ? s + 1 : s) : s + 1;
#pragma unroll 1
      for (int sg = (w == 5 || s == 0) ? s : s + 1; sg <= s1; ++sg) fetch(sg);
      if (have < s1 + 1 && s1 + 1 < NSEG) have = look_prog(rsy, fkq(h, w - 4));
      if (w == 5 && s + NPRE < NSEG) {
#pragma unroll
        for (int i = 0; i < 4; ++i) st_sc1(rsy, o_v(pm, s + NPRE, h) + i * 1024 + lane * 16, (u32x4){DSENT, DSENT, DSENT, DSENT});
      }
    }
  }
  if (h == 3 && (w & 2) == 0) { LST(351 + (w & 1) + (w >> 2) * 2); }
}

#define RL_UP 6
#define RL_DN 4
#define RED_MIN 40
#define BA_LOOK 290
constexpr int R_SI = 32768, R_PART = 98304;

__device__ __forceinline__ void red_role(Args& p, int j, char* smem) {
  const int tid = threadIdx.x;
  int lane = tid & 63;
  const int w = __builtin_amdgcn_readfirstlane(tid >> 6);
  const int c = (j & 2) * 8 + (j >> 2), rh = j & 1;
  const int hg = (w + (j >> 2)) & 7, rot = (j >> 2) + (j >> 5);
  const __amdgpu_buffer_rsrc_t rsy = mkrsrc(p.ws + WS_SY, SY_TOTAL);
  const u32x4 rs4 = raw_rsrc(p.ws + WS_SY, SY_TOTAL);
  const __amdgpu_buffer_rsrc_t rt = mkrsrc(p.ws + WS_T, T_BYTES);
  gu32* abortw = (gu32*)(p.ws + WS_SY + SY_ABORT);
  u32x32 W[4];
  u32x2 tag = {0u, 0u};
  {
    const u16* Wo = p.Wo + (size_t)(4 * hg * DH) * DM + 32 * c;
    const int ro = (lane >> 2) * 64 + (lane & 3) * 8;
    Boot bt;
    load_blocks<16>(smem + 32768 + w * 16384,
        [&](int i, char* slot) {
          if (i == 12 && w == 0) boot_ask(p.ws, bt);
          if ((i < 2 || i == 4 || i == 8) && (c & 14) != 6 && lane < 10) {
            const int x = i < 2 ? 3 - i : i >> 3, n = min(((c & 15) - ((c & 8) >> 2)) * 10 + lane, 127);
            touch_line((const char*)p.Wo + (size_t)(w * 512 + (x >> 1) * 256 + (x & 1) * 64 + (n & 64) + n) * (DM * 2) + (c & 16) * 64 + 384, smem + w * 256);
          }
          const int x = ((i >> 2) + rot) & 3;
          blk_dma(Wo + (size_t)((x >> 1) * 256 + (x & 1) * 64 + ((i >> 1) & 1) * 128 + (i & 1) * 32) * DM, DM, slot, lane);
        },
        [&](int i, char* slot) {
#pragma unroll
          for (int t = 0; t < 2; ++t) {
            s16x4 lo = __builtin_amdgcn_ds_read_tr16_b64_v4i16((lds_s16x4_ptr)(slot + ro + t * 32));
            s16x4 hi = __builtin_amdgcn_ds_read_tr16_b64_v4i16((lds_s16x4_ptr)(slot + ro + t * 32 + 1024));
            put4(W[i >> 2], (i & 2) * 2 + 2 * t + (i & 1), (bf16x8){lo[0], lo[1], lo[2], lo[3], hi[0], hi[1], hi[2], hi[3]});
          }
        });
    if (w == 0) { lds_at(smem + 4128)[0] = boot_done(p.ws, bt); tag = next_tag(bt.e); }
    LBAR();
  }
  const unsigned pm = uni(lds_at(smem + 4128)[0]);
  p.s0 = pm >> 31;
  const int hs = side(pm, 1);
  if (((j >> 2) & 14) != 6 && GPF == 2) {
#pragma unroll
    for (int i = 0; i < 4; ++i)
      touch_line((const char*)(i < 2 ? p.wg : p.wu) + (size_t)(j - (j >= 32 ? 8 : 0)) * 131072 + ((i & 1) * 8 + w) * 8192 + lane * 128, smem + w * 256);
  }
  {
    const u32x4 ds = {DSENT, DSENT, DSENT, DSENT};
#pragma unroll
    for (int i = 0; i < 2; ++i) {
      const int s = (w * 2 + i) * 4 + (lane >> 4);
      if (s >= 2) st_sc1(rsy, o_mem(pm, s) + (c >> 4) * 16384 + (c & 15) * 512 + rh * 128 + ((lane >> 3) & 1) * 256 + (lane & 7) * 16, ds);
    }
  }
  if (tid < 4) { reinterpret_cast<unsigned*>(smem + 4096)[tid] = tid == 3 ? now_q() : 0; reinterpret_cast<unsigned*>(smem + 4112)[tid] = DSENT; }
  if (tid < 256) reinterpret_cast<int*>(smem + 5120)[tid] = ((tid >> 2) - TLAG - 6) >> 2;
  LBAR();
  const unsigned base = __builtin_amdgcn_readfirstlane(lds_at(smem + 4096)[3]);
  RDY(64 + j);
  unsigned long long fm = 0, ps = 0;
  const unsigned aL = lane * 16;
  const int e = ((56 + w) * SEG + rh * 16 + (lane >> 2)) * DM + c * 32 + (lane & 3) * 8;
  u32x4 xv = *reinterpret_cast<const u32x4*>(p.x + e), av = {DSENT, DSENT, DSENT, DSENT};
  asm volatile("" : "+v"(xv));
  {
    const int r = lane & 15, q = lane >> 4;
    const int sm0 = o_mem(pm, 0) + (c >> 4) * 16384 + (c & 15) * 512 + rh * 128;
    u32x4 VO, VT;
    unsigned fo = SY_FO + ((rh + (lane & 4) / 2) * NH + 4 * hg + (lane & 3)) * 128;
    unsigned ao = w == 1 ? (c * S_LEN + rh * 16 - (TLAG + 3) * SEG) * 64 + lane * 16
                         : (w >= 4 ? SY_O + NPRE * 131072 + side(pm, w - 2) + (j >> 3) * 16384 + (j & 7) * 1024 + lane * 16
                                   : SY_FS + (2 * c + rh) * 128 + (lane < 8 ? lane * 16 : 0x70000000));
    unsigned ar = w == 1 ? R_SI - (TLAG + 2) * 1024 + lane * 16 : (w == 2 ? 5120 : 4112);
    const unsigned long long am = w == 1 ? ((1ull << 56) - 1) << (TLAG + 3) : (w == 2 ? 0x11111111111111ull << (TLAG + 7) : (w >= 4 ? (0xFFFFull << (16 * (w - 4))) >> NPRE : 0));
    unsigned pw = R_PART + w * 2048 + lane * 16, pr = R_PART + 16384 + tid * 4;
    unsigned sw = R_SI - (TLAG + 1) * 1024 + (lane >> 2) * 64 + w * 8 + (lane & 3) * 2, stm = 8192 + w * 512;
#pragma unroll
    for (int l = 0; l < 4; ++l) {
      const int x = (l + rot) & 3;
      VO[l] = o_o(0, 0, 4 * hg) + rh * 65536 + x * 1024 + aL;
      VT[l] = (T_O + rh * 16 + 2 * hg + (x >> 1)) * 16384 + (x & 1) * 2048 + aL;
    }
    unsigned ba = lane < 4 ? SY_BA + hs + (4 * hg + lane) * 128 : 0x7F000000u;
    int rl = RL0;
    unsigned step, E, so, st, st4, sh;
    asm volatile(RED_ASM
        : [step] "=&s"(step), [E] "=&s"(E), [so] "=&s"(so), [st] "=&s"(st), [st4] "=&s"(st4), [sh] "=&s"(sh),
          [rl] "+s"(rl), [fm] "+s"(fm), [ps] "+s"(ps),
          "+{v[224:227]}"(av), "+{v228}"(ao), "+{v229}"(ar), "+{v230}"(pw), "+{v231}"(pr), "+{v232}"(sw), "+{v233}"(stm), "+{v242}"(fo), "+{v173}"(ba)
        : W4_IN, "{v168}"(aL), "{v169}"(w * 512 + lane * 8), "{v170}"(lane * 8), "{v174}"((r & 1) ? 0x03020706u : 0x05040100u),
          "{v171}"(sm0 + 32768 + (q >> 1) * 256 + (q & 1) * 64 + ((r >> 1) & 1) * 32 + (r & 1) * 16 + (r >> 2) * 4),
          "{v172}"((lane & 2) ? 0xEEEE : 0x4444),
          "{v175}"(w == 3 && lane < 32 ? SY_BR + hs + j * 128 + lane * 4 : 0x7F000000u), "{v[234:237]}"(VO), "{v[238:241]}"(VT), "{v243}"(e * 2),
          [rg] "s"(raw_rsrc(p.ws + WS_G, S_LEN * DM * 2)), [fg] "s"(SY_FG + ((14 + (w >> 2)) * 8 + (c >> 2)) * 128),
          [rs] "s"(rs4), [ra] "s"(w == 1 ? raw_rsrc(p.ws + WS_S, S_LEN * DM * 2) : rs4), [rt] "s"(raw_rsrc(p.ws + WS_T, T_BYTES)),
          [ds] "n"(DS_RED), [mn] "n"(RED_MIN), [lk] "n"(DS_MEM + BA_LOOK), [up] "n"(RL_UP), [dn] "n"(-RL_DN), [sent] "s"(DSENT), [sab] "s"(SY_ABORT),
          [base] "s"(base), [pm] "s"(pm), [s0] "s"((p.s0 ^ TF_O) << 13), [am] "s"(am), [ainc] "s"(w == 1 ? SEG * 64 : (w >= 4 ? 131072 : 0)),
          [rinc] "s"(w == 1 ? 1024 : (w == 2 ? 16 : 0))
        : CLB32(128), CLB8(160, 161, 162, 163, 164, 165, 166, 167), CLB8(176, 177, 178, 179, 180, 181, 182, 183),
          CLB8(184, 185, 186, 187, 188, 189, 190, 191), CLB8(192, 193, 194, 195, 196, 197, 198, 199), CLB8(200, 201, 202, 203, 204, 205, 206, 207),
          CLB8(208, 209, 210, 211, 212, 213, 214, 215), CLB8(216, 217, 218, 219, 220, 221, 222, 223), CLB_E, "s96", CLB_S);
  }
  if (j == 0 && w == 5) { LST(345); }
  LBAR();
  if (j == 0 && w == 5) { LST(346); }
  u32x4 sv;
  if (w < 6 - TLAG) sv = *reinterpret_cast<const u32x4*>(smem + R_SI + (56 + w) * 1024 + lane * 16);
  else {
    const f32x4* part = reinterpret_cast<const f32x4*>(smem + (w == 6 - TLAG ? R_PART : (w - 6 + TLAG) * 16384)) +
                        (lane & 2) * 32 + (lane & 1) * 32 + (lane >> 2);
    f32x4 x[8][2];
#pragma unroll
    for (int ww = 0; ww < 8; ++ww) { x[ww][0] = part[ww * 128]; x[ww][1] = part[ww * 128 + 16]; }
#pragma unroll
    for (int u = 0; u < 2; ++u) {
      const f32x4 a = ((x[0][u] + x[1][u]) + (x[2][u] + x[3][u])) + ((x[4][u] + x[5][u]) + (x[6][u] + x[7][u]));
      sv[2 * u] = cvtpk(a[0], a[1]); sv[2 * u + 1] = cvtpk(a[2], a[3]);
    }
  }
  st_out(p, e, combine(sv, av, xv));
  if (j == 0 && tid == 0) st2_sc1(rsy, SY_EP, tag);
  if (tid == 128) { LST(400 + j); }
}

__global__ __launch_bounds__(512, 2) void main_kernel(Args p) {
  char* smem = g_smem;
  p.ws = ws_align(p.ws);
  const int bx = blockIdx.x & 7, bk = blockIdx.x >> 3;
  const bool isg = (bx ^ XPROBE) & 4;
  const int b = (isg ? NCHAIN : 0) + bk * 4 + (bx & 3);
  if (b == 64 && threadIdx.x == 0) { LST(500); }
  if (threadIdx.x < 64) {
    unsigned long long pc; unsigned len, keep;
    asm volatile("s_getpc_b64 %0\n.Lpc%=:\n\ts_mov_b32 %1, .Lcode_end-.Lpc%=" : "=s"(pc), "=s"(len));
    asm volatile("s_mov_b32 %0, m0\n\ts_mov_b32 m0, %3\n\ts_nop 0\n\tbuffer_load_dword %1, %2, 0 offen lds\n\ts_mov_b32 m0, %0"
                 : "=&s"(keep) : "v"((bk * 40 + threadIdx.x) * 128), "s"(raw_rsrc((const void*)pc, len)), "s"((b >> 6) == 1 ? 2048 : 163328) : "memory");
  }
  if (b >= NCHAIN) gemm_role(p, b - NCHAIN, smem);
  else if (b < 32) qk_role(p, (b & 3) * 8 + bk, smem);
  else if (b < 64) vu_role(p, (b & 3) * 8 + bk - 8, smem);
  else red_role(p, b - 64, smem);
}

extern "C" void kernel_launch(void* const* d_in, const int* in_sizes, int n_in,
                              void* d_out, int out_size, void* d_ws, size_t ws_size,
                              hipStream_t stream) {
  Args a;
  a.x = (const u16*)d_in[0]; a.mem0 = (const u16*)d_in[1];
  a.Wq = (const u16*)d_in[2]; a.Wk = (const u16*)d_in[3]; a.Wv = (const u16*)d_in[4]; a.Wo = (const u16*)d_in[5];
  a.wg = (const u16*)d_in[8]; a.wu = (const u16*)d_in[9]; a.wd = (const u16*)d_in[10];
  a.ws = (char*)d_ws; a.out = (u16*)d_out; a.s0 = 0; a.pad1 = 0;
  main_kernel<<<256, 512, 0, stream>>>(a);
}
```

```cpp
#include <hip/hip_runtime.h>
#include <hip/hip_bf16.h>
#include <stdint.h>
#include <type_traits>

constexpr int S_LEN = 2048, DM = 1024, NH = 32, DH = 128, NMEM = 8, SEG = 32;
constexpr int NSEG = S_LEN / SEG;
constexpr int HD = NH * DH;
constexpr int FF = 3584;

typedef unsigned short u16;
typedef __attribute__((ext_vector_type(8))) short bf16x8;
typedef __attribute__((ext_vector_type(4))) short s16x4;
typedef __attribute__((ext_vector_type(4))) float f32x4;
typedef __attribute__((ext_vector_type(4))) unsigned int u32x4;
typedef __attribute__((ext_vector_type(2))) unsigned int u32x2;
typedef __attribute__((address_space(3))) s16x4* lds_s16x4_ptr;
typedef __attribute__((address_space(3))) void* lds_void_ptr;
typedef __attribute__((address_space(1))) const void* glb_cvoid_ptr;
typedef __attribute__((address_space(1))) unsigned gu32;
typedef __bf16 bf16x2_t __attribute__((ext_vector_type(2)));
typedef float f32x2_t __attribute__((ext_vector_type(2)));

__device__ __forceinline__ float bf2f(u16 v) { return __uint_as_float(((unsigned)v) << 16); }
__device__ __forceinline__ unsigned cvtpk(float lo, float hi) {
  bf16x2_t r = __builtin_convertvector((f32x2_t){lo, hi}, bf16x2_t);
  return __builtin_bit_cast(unsigned, r);
}
__device__ __forceinline__ float lo_f(unsigned v) { return __uint_as_float(v << 16); }
__device__ __forceinline__ float hi_f(unsigned v) { return __uint_as_float(v & 0xffff0000u); }
__device__ __forceinline__ float rbf(float f) { return lo_f(cvtpk(f, f)); }
__device__ __forceinline__ bf16x8 as_bf16x8(u32x4 v) { return __builtin_bit_cast(bf16x8, v); }
#define MFMA(a, b, c) __builtin_amdgcn_mfma_f32_16x16x32_bf16((a), (b), (c), 0, 0, 0)
typedef __attribute__((ext_vector_type(16))) short bf16x16;
typedef __attribute__((ext_vector_type(8))) __bf16 bfv8;
typedef __attribute__((ext_vector_type(16))) __bf16 bfv16;
#define SMFMA(a, b, c, idx) __builtin_amdgcn_smfmac_f32_16x16x64_bf16(__builtin_bit_cast(bfv8, (a)), __builtin_bit_cast(bfv16, (b)), (c), (idx), 0, 0)
__device__ __forceinline__ bf16x16 cat16(bf16x8 lo, bf16x8 hi) {
  return __builtin_shufflevector(lo, hi, 0, 1, 2, 3, 4, 5, 6, 7, 8, 9, 10, 11, 12, 13, 14, 15);
}

#define KSTR 65
#define XLD 1024
constexpr int PAGE = 2 << 20, NPAGE = 14;
constexpr int T_K = 0, T_Q = 2304, T_V = 4608, T_O = 6912, T_END = T_O + 2048;
constexpr unsigned T_BYTES = T_END * 16384u;
constexpr size_t WS_SY = 0;
constexpr size_t WS_T = (size_t)NPAGE * PAGE;
constexpr size_t WS_XN = WS_T + T_BYTES;
constexpr size_t WS_HF = WS_XN + (size_t)S_LEN * XLD * 2;
constexpr size_t WS_G = WS_HF + (size_t)S_LEN * 16384;
constexpr size_t WS_S = WS_G + (size_t)S_LEN * DM * 2;
constexpr size_t WS_END = WS_S + (size_t)S_LEN * DM * 2;
constexpr unsigned SENT = 0xFFFFFFFFu;
constexpr unsigned DSENT = 0xFFBFFFBFu;
constexpr int SY_MEM = 0;
constexpr int SY_O = 2 * PAGE;
constexpr int SY_V = 6 * PAGE;
constexpr int SY_BR = PAGE, SY_BA = SY_BR + 16384;
constexpr int SY_FKQ = SY_BR + 32768;
constexpr int SY_FG = SY_FKQ + NH * 4 * 128;
constexpr int SY_FO = SY_FG + 16 * 8 * 128;
constexpr int SY_CH = SY_FO + 384 * 128;
constexpr int SY_ABORT = SY_CH + 8 * 128;
constexpr int SY_FS = SY_ABORT + 256;
constexpr int SY_FEND = SY_FS + 64 * 128;
constexpr int SY_EP = SY_FEND, SY_BT = SY_EP + 128;
constexpr int SY_TOTAL = NPAGE * PAGE;
constexpr int PROBE = PAGE - 16384 + 6144;
__device__ __forceinline__ int side(unsigned pm, int page) { return ((pm >> page) & 1u) << 13; }
__device__ __forceinline__ int o_mem(unsigned pm, int s) { return SY_MEM + s * 32768 + side(pm, 0); }
__device__ __forceinline__ int o_o(unsigned pm, int s, int h) {
  return SY_O + s * 131072 + (h >> 3) * 16384 + ((h >> 1) & 3) * 2048 + (h & 1) * 128 + side(pm, 2 + (s >> 4));
}
__device__ __forceinline__ int o_v(unsigned pm, int s, int h) { return SY_V + s * 262144 + (h >> 1) * 16384 + (h & 1) * 4096 + side(pm, 6 + (s >> 3)); }
__device__ __forceinline__ char* ws_align(char* ws) { return ws + ((PAGE - (unsigned)(uintptr_t)ws) & (PAGE - 1)); }
#define NFUSE 12
constexpr int NPRE = 4;
static_assert(WS_END + PAGE <= (size_t)256 << 20 && NH * KSTR <= T_Q && NPAGE % 2 == 0, "");
constexpr int NCHAIN = 128, NGW = 32;
__host__ __device__ constexpr int kseg(int sg, int h) { return h * KSTR + sg; }
__host__ __device__ constexpr int fkq(int h, int kind) { return SY_FKQ + (h * 4 + kind) * 128; }

#define XPROBE 0
struct Args {
  const u16 *x, *mem0, *Wq, *Wk, *Wv, *Wo, *wg, *wu, *wd;
  char* ws;
  u16* out;
  int s0, pad1;
};
__device__ __forceinline__ int h_off(const Args& p, int rblk) { return rblk * PAGE + (((p.s0 ^ rblk ^ 1) & 1) << 13); }
static_assert(WS_HF % (2 * PAGE) == 0, "");
#define TF_KQV 1
#define TF_O 0
__device__ __forceinline__ int t_off(const Args& p, int t) { return t * 16384 + (((p.s0 ^ (t >> 7) ^ (TF_KQV == 2 ? t : TF_KQV)) & 1) << 13); }
__device__ __forceinline__ int t_off_o(const Args& p, int t) { return t * 16384 + (((p.s0 ^ (t >> 7) ^ (TF_O == 2 ? t : TF_O)) & 1) << 13); }

__device__ __forceinline__ float wave_sum(float v) {
#pragma unroll
  for (int i = 0; i < 4; ++i) {
    const int x = (int)__float_as_uint(v);
    v += __uint_as_float(i == 0 ? __builtin_amdgcn_update_dpp(0, x, 0x128, 0xf, 0xf, false) : i == 1 ? __builtin_amdgcn_update_dpp(0, x, 0x124, 0xf, 0xf, false)
                         : i == 2 ? __builtin_amdgcn_update_dpp(0, x, 0x122, 0xf, 0xf, false) : __builtin_amdgcn_update_dpp(0, x, 0x121, 0xf, 0xf, false));
  }
  auto r = __builtin_amdgcn_permlane16_swap(__float_as_uint(v), __float_as_uint(v), false, false);
  v = __uint_as_float(r[0]) + __uint_as_float(r[1]);
  r = __builtin_amdgcn_permlane32_swap(__float_as_uint(v), __float_as_uint(v), false, false);
  return __uint_as_float(r[0]) + __uint_as_float(r[1]);
}

__shared__ __attribute__((aligned(16))) char g_smem[163840];
__device__ __forceinline__ unsigned lds_off(const void* p) {
  return (unsigned)(uintptr_t)(lds_void_ptr)g_smem + (unsigned)((const char*)p - g_smem);
}
__device__ __forceinline__ __amdgpu_buffer_rsrc_t mkrsrc(const void* p, unsigned bytes) {
  return __builtin_amdgcn_make_buffer_rsrc((void*)p, 0, bytes, 0x00020000);
}
__device__ __forceinline__ u32x4 ld_sc1(__amdgpu_buffer_rsrc_t rs, int off) {
  return __builtin_amdgcn_raw_buffer_load_b128(rs, off, 0, 16);
}
__device__ __forceinline__ void st_sc1(__amdgpu_buffer_rsrc_t rs, int off, u32x4 v) {
  __builtin_amdgcn_raw_buffer_store_b128(v, rs, off, 0, 16);
}
__device__ __forceinline__ unsigned ror8(unsigned v) {
  return (unsigned)__builtin_amdgcn_update_dpp(0, (int)v, 0x128, 0xf, 0xf, false);
}
__device__ __forceinline__ float ror8f(float v) { return __uint_as_float(ror8(__float_as_uint(v))); }
__device__ __forceinline__ void swap16(unsigned& x, unsigned& y) {
  auto r = __builtin_amdgcn_permlane16_swap(x, y, false, false);
  x = r[0]; y = r[1];
}
__device__ __forceinline__ void swap32(unsigned& x, unsigned& y) {
  auto r = __builtin_amdgcn_permlane32_swap(x, y, false, false);
  x = r[0]; y = r[1];
}
__device__ __forceinline__ f32x4 fold32(f32x4 x, f32x4 y) {
  f32x4 o;
#pragma unroll
  for (int j = 0; j < 4; ++j) {
    unsigned a = __float_as_uint(x[j]), b = __float_as_uint(y[j]);
    swap32(a, b);
    o[j] = __uint_as_float(a) + __uint_as_float(b);
  }
  return o;
}
__device__ __forceinline__ bf16x8 tr_frag(const char* img_lane) {
  s16x4 lo = __builtin_amdgcn_ds_read_tr16_b64_v4i16((lds_s16x4_ptr)img_lane);
  s16x4 hi = __builtin_amdgcn_ds_read_tr16_b64_v4i16((lds_s16x4_ptr)(img_lane + 64));
  return (bf16x8){lo[0], lo[1], lo[2], lo[3], hi[0], hi[1], hi[2], hi[3]};
}
__device__ __forceinline__ float rowq_max(float v) {
  unsigned a = __float_as_uint(v), b = a;
  swap16(a, b);
  v = __builtin_elementwise_maximum(__uint_as_float(a), __uint_as_float(b));
  a = __float_as_uint(v); b = a;
  swap32(a, b);
  return __builtin_elementwise_maximum(__uint_as_float(a), __uint_as_float(b));
}
__device__ __forceinline__ float rowq_sum(float v) {
  unsigned a = __float_as_uint(v), b = a;
  swap16(a, b);
  v = __uint_as_float(a) + __uint_as_float(b);
  a = __float_as_uint(v); b = a;
  swap32(a, b);
  return __uint_as_float(a) + __uint_as_float(b);
}
__device__ __forceinline__ bool spin_fail(unsigned& spins, gu32* abortw) {
  ++spins;
  if ((spins & 63u) == 0u) {
    const __amdgpu_buffer_rsrc_t ra = mkrsrc((const void*)abortw, 4);
    unsigned z = 0;
    asm volatile("" : "+v"(z));
    if (__builtin_amdgcn_readfirstlane(__builtin_amdgcn_raw_buffer_load_b32(ra, z, 0, 16)) == 1u) return true;
    if (spins > (1u << 19)) {
      __builtin_amdgcn_raw_buffer_store_b32(z + 1u, ra, z, 0, 16);
      return true;
    }
  }
  return false;
}
__device__ __forceinline__ unsigned umax4(u32x4 v) { return max(max(v[0], v[1]), max(v[2], v[3])); }
__device__ __forceinline__ bool has_ds(u32x4 v) { return (v[0] == DSENT) | (v[1] == DSENT) | (v[2] == DSENT) | (v[3] == DSENT); }
__device__ __forceinline__ float fmax2(float a, float b) { return __builtin_elementwise_maximum(a, b); }
__device__ __forceinline__ bf16x8 ldsf(const char* p) { return *reinterpret_cast<const bf16x8*>(p); }
#define SCHED() __builtin_amdgcn_sched_barrier(0)
#define LBAR() do { asm volatile("s_waitcnt lgkmcnt(0)" ::: "memory"); __builtin_amdgcn_s_barrier(); asm volatile("" ::: "memory"); } while (0)
#define WAIT_V(n) asm volatile("s_waitcnt vmcnt(%0)" :: "n"(n) : "memory")

#define HOLD_V(c, n) asm volatile("s_cmp_eq_u32 %0, 0\n\ts_cbranch_scc1 .Lh%=\n\ts_waitcnt vmcnt(%1)\n.Lh%=:" :: "s"((int)(c)), "n"(n) : "memory", "scc")
#define TH_Q 2
#define TH_V 63
#define TH_R 63
#define TH_P 63
__device__ __forceinline__ void wait_flags(__amdgpu_buffer_rsrc_t rs, int off, gu32* abortw) {
  for (unsigned spins = 0;;) {
    asm volatile("" ::: "memory");
    const unsigned v = __builtin_amdgcn_raw_buffer_load_b32(rs, off, 0, 16);
    if (__all(v != SENT)) break;
    __builtin_amdgcn_s_sleep(4);
    if (spin_fail(spins, abortw)) break;
  }
  asm volatile("" ::: "memory");
}

#define LST(i) do {} while (0)
#define RDY(i) do {} while (0)
#define RDW(i) do {} while (0)
#define SMP(k, s, v) do {} while (0)
#define LSTC(i) do {} while (0)
__device__ __forceinline__ unsigned offA(unsigned row, unsigned ch) {
  return row * 128u + 16u * (ch ^ (row & 7u));
}
__device__ __forceinline__ unsigned swzB(unsigned row) { return ((row & 3u) << 2) | ((row >> 2) & 3u); }
__device__ __forceinline__ unsigned offB(unsigned row, unsigned ch) {
  return 256u * row + 16u * (ch ^ swzB(row));
}
__device__ __forceinline__ void glds16(const u16* src, char* dst) {
  const unsigned d = __builtin_amdgcn_readfirstlane(lds_off(dst));
  unsigned keep;
  asm volatile("s_mov_b32 %0, m0\n\ts_mov_b32 m0, %2\n\ts_nop 0\n\tglobal_load_lds_dwordx4 %1, off\n\ts_mov_b32 m0, %0"
               : "=&s"(keep) : "v"(src), "s"(d) : "memory");
}
__device__ __forceinline__ bf16x8 ld_bfrag(const char* sb, int ks, int c, int lane) {
  const unsigned g = lane >> 4, qq = (lane & 15) >> 2, pp = lane & 3;
  s16x4 lo = __builtin_amdgcn_ds_read_tr16_b64_v4i16(
      (lds_s16x4_ptr)(sb + offB(32 * ks + 8 * g + qq, 2 * c + (pp >> 1)) + 8 * (pp & 1)));
  s16x4 hi = __builtin_amdgcn_ds_read_tr16_b64_v4i16(
      (lds_s16x4_ptr)(sb + offB(32 * ks + 8 * g + 4 + qq, 2 * c + (pp >> 1)) + 8 * (pp & 1)));
  return (bf16x8){lo[0], lo[1], lo[2], lo[3], hi[0], hi[1], hi[2], hi[3]};
}

__device__ __forceinline__ void blk_dma(const u16* src, size_t ld, char* slot, int lane) {
  const u16* s = src + (size_t)(lane >> 2) * ld + (lane & 3) * 8;
  glds16(s, slot);
  glds16(s + 16 * ld, slot + 1024);
}
__device__ __forceinline__ bf16x8 blk_frag(const char* slot, int T, int lane) {
  const char* a = slot + ((lane >> 4) * 8 + ((lane & 15) >> 2)) * 64 + (lane & 3) * 16 + T * 8;
  s16x4 lo = __builtin_amdgcn_ds_read_tr16_b64_v4i16((lds_s16x4_ptr)a);
  s16x4 hi = __builtin_amdgcn_ds_read_tr16_b64_v4i16((lds_s16x4_ptr)(a + 256));
  return (bf16x8){lo[0], lo[1], lo[2], lo[3], hi[0], hi[1], hi[2], hi[3]};
}
template <int NB, typename ISSUE, typename SINK>
__device__ __forceinline__ void load_blocks(char* slice, ISSUE&& issue, SINK&& sink) {
#pragma unroll
  for (int i = 0; i < 8; ++i) issue(i, slice + i * 2048);
#pragma unroll
  for (int g = 0; g < NB / 4; ++g) {
    if (g + 1 < NB / 4) WAIT_V(8); else WAIT_V(0);
#pragma unroll
    for (int j = 0; j < 4; ++j) sink(4 * g + j, slice + ((4 * g + j) & 7) * 2048);
    asm volatile("s_waitcnt lgkmcnt(0)" ::: "memory");
    if (g + 2 < NB / 4) {
#pragma unroll
      for (int j = 0; j < 4; ++j) issue(4 * g + 8 + j, slice + ((4 * g + j) & 7) * 2048);
    }
  }
}

__device__ __forceinline__ u32x4 widen32(unsigned a0, unsigned a1, unsigned b0, unsigned b1) {
  swap32(a0, b0); swap16(a0, b0);
  swap32(a1, b1); swap16(a1, b1);
  return (u32x4){a0, a1, b0, b1};
}

__device__ __forceinline__ void publish_flags(__amdgpu_buffer_rsrc_t rs, int off, int stride, int n) {
  WAIT_V(0);
  LBAR();
  int tid = threadIdx.x;
  asm volatile("" : "+v"(tid));
  if (tid < n) __builtin_amdgcn_raw_buffer_store_b32(1u, rs, off + tid * stride, 0, 16);
}

__device__ __forceinline__ void wait_s(const Args& p, __amdgpu_buffer_rsrc_t rsy, int rblk, int c0, int n, bool need) {
  if (need) {
    if (threadIdx.x < 64) {
      for (unsigned spins = 0;;) {
        asm volatile("" ::: "memory");
        const int f = (int)__builtin_amdgcn_raw_buffer_load_b32(rsy, SY_FS + (2 * c0 + (threadIdx.x & (2 * n - 1))) * 128, 0, 16);
        if (__all(f >= rblk)) break;
        __builtin_amdgcn_s_sleep(32);
        if (spin_fail(spins, (gu32*)(p.ws + WS_SY + SY_ABORT))) break;
      }
    }
    LBAR();
  }
}
__device__ __forceinline__ u32x4 combine(u32x4 sv, u32x4 g, u32x4 x) {
  u32x4 o;
#pragma unroll
  for (int j = 0; j < 4; ++j) {
    const unsigned t = cvtpk(lo_f(sv[j]) * lo_f(g[j]), hi_f(sv[j]) * hi_f(g[j]));
    o[j] = cvtpk(lo_f(x[j]) + lo_f(t), hi_f(x[j]) + hi_f(t));
  }
  return o;
}
#define OUT_AUX 0
__device__ __forceinline__ void st_out(const Args& p, int e, u32x4 v) {
  __builtin_amdgcn_raw_buffer_store_b128(v, mkrsrc(p.out, S_LEN * DM * 2), e * 2, 0, OUT_AUX);
}
struct SX { u32x4 s, x; };
__device__ __forceinline__ SX get_sx(const Args& p, int rblk, int e) {
  SX r; r.s = (u32x4){0u, 0u, 0u, 0u}; r.x = r.s;
  if (rblk < NFUSE) {
    r.s = ld_sc1(mkrsrc(p.ws + WS_S, S_LEN * DM * 2), (((e & 1023) >> 5) * S_LEN + (e >> 10)) * 64 + (e & 31) * 2);
    r.x = *reinterpret_cast<const u32x4*>(p.x + e);
  }
  return r;
}
__device__ __forceinline__ void put_g(const Args& p, __amdgpu_buffer_rsrc_t rg, int rblk, int e, u32x4 g, const SX& v) {
  if (rblk < NFUSE) {
    st_out(p, e, combine(v.s, g, v.x));
  } else st_sc1(rg, e * 2, g);
}

__device__ __forceinline__ void job_comb(const Args& p, int rblk, int ct, __amdgpu_buffer_rsrc_t rsy) {
  int tid = threadIdx.x;
  asm volatile("" : "+v"(tid));
  u32x4 xv[8];
#pragma unroll
  for (int i = 0; i < 8; ++i) xv[i] = *reinterpret_cast<const u32x4*>(p.x + (rblk * 128 + i * 16 + (tid >> 5)) * DM + ct * 256 + (tid & 31) * 8);
  if (tid < 64) wait_flags(rsy, SY_FG + (rblk * 8 + 2 * ct + (tid & 1)) * 128, (gu32*)(p.ws + WS_SY + SY_ABORT));
  wait_s(p, rsy, rblk, ct * 8, 8, true);
  u32x4 sv[8], gv[8];
#pragma unroll
  for (int i = 0; i < 8; ++i) {
    const int e = (rblk * 128 + i * 16 + (tid >> 5)) * DM + ct * 256 + (tid & 31) * 8;
    sv[i] = ld_sc1(mkrsrc(p.ws + WS_S, S_LEN * DM * 2), (((e & 1023) >> 5) * S_LEN + (e >> 10)) * 64 + (e & 31) * 2);
    gv[i] = ld_sc1(mkrsrc(p.ws + WS_G, S_LEN * DM * 2), e * 2);
  }
#pragma unroll
  for (int i = 0; i < 8; ++i)
    st_out(p, (rblk * 128 + i * 16 + (tid >> 5)) * DM + ct * 256 + (tid & 31) * 8, combine(sv[i], gv[i], xv[i]));
}

__device__ __forceinline__ u32x4 raw_rsrc(const void* p, unsigned bytes) {
  const unsigned long long a = (unsigned long long)p;
  return (u32x4){(unsigned)a, (unsigned)(a >> 32) & 0xffffu, bytes, 0x00020000u};
}
__device__ __forceinline__ u32x2 next_tag(u32x2 e) { return (u32x2){e[0] + 1u, e[1] + (e[0] == 0xFFFFFFFFu)}; }
struct Boot { u32x2 e; u32x4 c; };
__device__ __forceinline__ void boot_ask(const char* ws, Boot& r) {
  const unsigned o = (threadIdx.x & 63) * 128;
  asm volatile("buffer_load_dwordx2 %0, off, %3, %4 sc1\n\tbuffer_load_dwordx4 %1, %2, %3, %5 offen sc1"
               : "=&v"(r.e), "=&v"(r.c) : "v"(o), "s"(raw_rsrc(ws, SY_TOTAL)), "s"(SY_EP), "s"(SY_BT) : "memory");
}
__device__ __forceinline__ void wait_rows(const char* ws, u32x2 tag) {
  const __amdgpu_buffer_rsrc_t rs = mkrsrc(ws, SY_TOTAL);
  int o = (threadIdx.x & 63) * 128;
  asm volatile("" : "+v"(o));
#pragma unroll 1
  for (int i = 0; i < (1 << 17); ++i) {
    asm volatile("" ::: "memory");
    const u32x2 a = __builtin_amdgcn_raw_buffer_load_b64(rs, o, SY_BT + 32 * 128, 16), b = __builtin_amdgcn_raw_buffer_load_b64(rs, o, SY_BT + 96 * 128, 16);
    if (__all(a[0] == tag[0] && a[1] == tag[1] && b[0] == tag[0] && b[1] == tag[1])) break;
    __builtin_amdgcn_s_sleep(8);
  }
  asm volatile("" ::: "memory");
}
__device__ __forceinline__ unsigned boot_done(const char* ws, Boot& r, bool own = false, u32x2 tag = (u32x2){0u, 0u}) {
#pragma unroll 1
  for (int i = 0; i < (1 << 17); ++i) {
    asm volatile("" : "+v"(r.e), "+v"(r.c));
    if (!own) tag = next_tag(r.e);
    if (__all(r.c[0] == tag[0] && r.c[1] == tag[1])) break;
    __builtin_amdgcn_s_sleep(8);
    boot_ask(ws, r);
    WAIT_V(0);
  }
  unsigned q0[NPAGE], q1[NPAGE], ga = 0xFFFFu, gb = 0xFFFFu;
#pragma unroll
  for (int i = 0; i < NPAGE; ++i) {
    const unsigned x = __builtin_amdgcn_readlane(r.c[2], i), y = __builtin_amdgcn_readlane(r.c[2], i + NPAGE);
    q0[i] = min(x >> 16, y >> 16); q1[i] = min(x & 0xFFFFu, y & 0xFFFFu);
    ga = min(ga, (i & 1) ? q1[i] : q0[i]); gb = min(gb, (i & 1) ? q0[i] : q1[i]);
  }
  unsigned m = gb < ga ? 0x80001555u : 0x2AAAu;
  const unsigned thr = min(ga, gb) + 110;
#pragma unroll
  for (int i = 0; i < NPAGE; ++i) {
    const unsigned lo = min(q0[i], q1[i]), hi = max(q0[i], q1[i]);
    const unsigned keep = (lo < thr ? hi : 0u) > lo + 100 ? ~(1u << i) : ~0u;
    m = (m & keep) | ((q1[i] < q0[i] ? ~0u : 0u) & ~keep);
  }
  return m;
}
typedef __attribute__((address_space(3))) volatile unsigned* lds_vu_ptr;
__device__ __forceinline__ lds_vu_ptr lds_at(const void* p) {
  unsigned a = lds_off(p);
  asm volatile("" : "+v"(a));
  return (lds_vu_ptr)(uintptr_t)a;
}
__device__ __forceinline__ unsigned uni(unsigned v) {
  unsigned r;
  asm volatile("s_nop 1\n\tv_readfirstlane_b32 %0, %1\n\ts_nop 3" : "=s"(r) : "v"(v));
  return r;
}
__device__ __forceinline__ void start_sy(char* ws, const u16* mem0, int i, char* smem) {
  const int tid = threadIdx.x, lane = tid & 63, w = __builtin_amdgcn_readfirstlane(tid >> 6);
  const __amdgpu_buffer_rsrc_t rsy = mkrsrc(ws, SY_TOTAL);
  const __amdgpu_buffer_rsrc_t rs = mkrsrc(ws + (size_t)(i % NPAGE) * PAGE + PROBE - (i / NPAGE) * 2048, 16384);
  const u32x2 e = __builtin_amdgcn_raw_buffer_load_b64(rsy, 0, SY_EP, 16);
  const u32x4 ds = {DSENT, DSENT, DSENT, DSENT};
  if (lane < 32) st_sc1(rs, (lane >> 4) * 8192 + w * 256 + (lane & 15) * 16, ds);
  static_assert(NPRE == 4 && SY_FEND - SY_BR <= 32 * 6144, "");
#pragma unroll
  for (int u = 0; u < 6; ++u)
    st_sc1(rsy, (u < 4 ? SY_V + (i * 4 + u) * 8192 : SY_O + (i * 2 + u - 4) * 8192) + tid * 16, ds);
  if (w == 7) {
    const int n = i * 64 + lane, j = (n >> 10) * 512 + (n & 511);
    const unsigned* m0 = reinterpret_cast<const unsigned*>(mem0 + (2 * ((j >> 2) & 3) + (j & 1)) * DM + (j >> 4) * 16) + ((j >> 1) & 1);
    u32x4 m;
#pragma unroll
    for (int k = 0; k < 4; ++k) { const unsigned t = m0[2 * k]; m[k] = t == DSENT ? 0xFFFFFFFFu : t; }
    st_sc1(rsy, n * 16, m);
  } else if (w == 6) st_sc1(rsy, 32768 + i * 1024 + lane * 16, ds);
  else {
    const int o = SY_BR + i * 6144 + tid * 16;
    st_sc1(rsy, o < SY_FEND ? o : 0x7F000000, (u32x4){SENT, SENT, SENT, SENT});
  }
  unsigned t[4];
#pragma unroll
  for (int k = 0; k < 4; ++k) {
    unsigned long long c0, c1; unsigned v, off = ((k ^ (k >> 1)) & 1) * 8192 + w * 256 + (k >> 1) * 128;
    asm volatile("s_waitcnt vmcnt(0)\n\ts_barrier\n\ts_memtime %0\n\ts_waitcnt lgkmcnt(0)\n\tbuffer_load_dword %2, %3, %4, 0 offen sc1\n\ts_waitcnt vmcnt(0)\n\t"
                 "s_memtime %1\n\ts_waitcnt lgkmcnt(0)" : "=&s"(c0), "=&s"(c1), "=&v"(v) : "v"(off), "s"(rs) : "memory");
    t[k] = (unsigned)(c1 - c0);
  }
  lds_vu_ptr tm = lds_at(smem);
  if (lane == 0) { tm[w] = min(t[0], t[3]); tm[8 + w] = min(t[1], t[2]); }
  int ro = SY_BT + i * 128 + lane * 16;
  asm volatile("" : "+v"(ro));
  LBAR();
  RDY(384 + i);
  if (threadIdx.x < 8) {
    unsigned m0 = tm[0], m1 = tm[8];
#pragma unroll
    for (int k = 1; k < 8; ++k) { m0 = min(m0, tm[k]); m1 = min(m1, tm[8 + k]); }
    const u32x2 tag = next_tag(e);
    st_sc1(rsy, ro, (u32x4){tag[0], tag[1], min(m0, 30000u) << 16 | min(m1, 30000u), 0u});
  }
}
template <int NB, typename F>
__device__ __forceinline__ u32x2 start_rows(const Args& p, int g, F&& between) {
  const int tid = threadIdx.x, lane = tid & 63, row0 = g * 16 + (tid >> 6) * 2;
  u32x4 a[4];
  u32x2 e;
  asm volatile("global_load_dwordx4 %0, %5, off\n\tglobal_load_dwordx4 %1, %5, off offset:1024\n\tglobal_load_dwordx4 %2, %5, off offset:2048\n\t"
               "global_load_dwordx4 %3, %5, off offset:3072\n\tbuffer_load_dwordx2 %4, off, %6, %7 sc1"
               : "=&v"(a[0]), "=&v"(a[1]), "=&v"(a[2]), "=&v"(a[3]), "=&v"(e)
               : "v"(p.x + (size_t)row0 * DM + lane * 8), "s"(raw_rsrc(p.ws, SY_TOTAL)), "s"(SY_EP) : "memory");
  between();
  asm volatile("s_waitcnt vmcnt(%5)" : "+v"(a[0]), "+v"(a[1]), "+v"(a[2]), "+v"(a[3]), "+v"(e) : "n"(NB) : "memory");
  RDY(512 + g);
  const __amdgpu_buffer_rsrc_t ro = mkrsrc(p.ws + WS_XN, S_LEN * XLD * 2);
#pragma unroll
  for (int u = 0; u < 2; ++u) {
    float v[16];
#pragma unroll
    for (int i = 0; i < 8; ++i) { v[2 * i] = lo_f(a[2 * u + (i >> 2)][i & 3]); v[2 * i + 1] = hi_f(a[2 * u + (i >> 2)][i & 3]); }
    float s = 0.f;
#pragma unroll
    for (int i = 0; i < 16; ++i) s += v[i];
    const float mean = wave_sum(s) * (1.0f / DM);
    float q = 0.f;
#pragma unroll
    for (int i = 0; i < 16; ++i) { float d = v[i] - mean; q += d * d; }
    const float var = rbf(wave_sum(q) * (1.0f / DM));
    const float mb = rbf(mean);
    const float inv = 1.0f / rbf(sqrtf(rbf(var + 1e-5f)));
#pragma unroll
    for (int k = 0; k < 2; ++k) {
      u32x4 o;
#pragma unroll
      for (int i = 0; i < 4; ++i) {
        const unsigned t = cvtpk(v[8 * k + 2 * i] - mb, v[8 * k + 2 * i + 1] - mb);
        o[i] = cvtpk(lo_f(t) * inv, hi_f(t) * inv);
      }
      st_sc1(ro, (row0 + u) * (XLD * 2) + k * 1024 + lane * 16, o);
    }
  }
  return next_tag(e);
}
__device__ __forceinline__ void report_rows(const Args& p, int g, u32x2 tag) {
  RDY(256 + g);
  if (threadIdx.x < 8) st_sc1(mkrsrc(p.ws + WS_SY, SY_TOTAL), SY_BT + (32 + g) * 128 + threadIdx.x * 16, (u32x4){tag[0], tag[1], tag[0], tag[1]});
}

typedef __attribute__((ext_vector_type(32))) unsigned u32x32;
__device__ __forceinline__ void put4(u32x32& W, int i, bf16x8 f) {
  const u32x4 v = __builtin_bit_cast(u32x4, f);
#pragma unroll
  for (int j = 0; j < 4; ++j) W[4 * i + j] = v[j];
}
#define CLB8(a, b, c, d, e, f, g, h) "v" #a, "v" #b, "v" #c, "v" #d, "v" #e, "v" #f, "v" #g, "v" #h
#define CLB32(a) CLB32_##a
#define CLB32_128 CLB8(128, 129, 130, 131, 132, 133, 134, 135), CLB8(136, 137, 138, 139, 140, 141, 142, 143), CLB8(144, 145, 146, 147, 148, 149, 150, 151), CLB8(152, 153, 154, 155, 156, 157, 158, 159)
#define CLB32_160 CLB8(160, 161, 162, 163, 164, 165, 166, 167), CLB8(168, 169, 170, 171, 172, 173, 174, 175), CLB8(176, 177, 178, 179, 180, 181, 182, 183), CLB8(184, 185, 186, 187, 188, 189, 190, 191)
#define W4_IN "{v[0:31]}"(W[0]), "{v[32:63]}"(W[1]), "{v[64:95]}"(W[2]), "{v[96:127]}"(W[3])
#define CLB32_192 CLB8(192, 193, 194, 195, 196, 197, 198, 199), CLB8(200, 201, 202, 203, 204, 205, 206, 207), CLB8(208, 209, 210, 211, 212, 213, 214, 215), CLB8(216, 217, 218, 219, 220, 221, 222, 223)
__device__ __forceinline__ unsigned ld_flag0(__amdgpu_buffer_rsrc_t rs, int off) {
  return __builtin_amdgcn_raw_buffer_load_b32(rs, off, 0, 16);
}
__device__ __forceinline__ void glds16_sc1(const char* src, char* dst) {
  const unsigned d = __builtin_amdgcn_readfirstlane(lds_off(dst));
  unsigned keep;
  asm volatile("s_mov_b32 %0, m0\n\ts_mov_b32 m0, %2\n\ts_nop 0\n\tglobal_load_lds_dwordx4 %1, off sc1\n\ts_mov_b32 m0, %0"
               : "=&s"(keep) : "v"(src), "s"(d) : "memory");
}

__device__ __forceinline__ void touch_line(const char* src, char* dst) {
  const unsigned d = __builtin_amdgcn_readfirstlane(lds_off(dst));
  unsigned keep;
  asm volatile("s_mov_b32 %0, m0\n\ts_mov_b32 m0, %2\n\ts_nop 0\n\tglobal_load_lds_dword %1, off sc1\n\ts_mov_b32 m0, %0"
               : "=&s"(keep) : "v"(src), "s"(d) : "memory");
}

#define GPF 1
#define GU_DELAY 0
#define Q_DELAY 0
#define PF_T0 100
#define PF_N 12
#define A_AUX " sc1"
#define WREV 1
template <bool DEV = false>
__device__ __forceinline__ void glds2(const char* base, int step, unsigned voff, unsigned dst) {
  unsigned keep;
  const char* base2 = base + step;
  if (DEV)
    asm volatile("s_mov_b32 %0, m0\n\ts_mov_b32 m0, %4\n\ts_nop 0\n\tglobal_load_lds_dwordx4 %1, %2 sc1\n\t"
                 "s_add_u32 m0, m0, 0x2000\n\ts_nop 0\n\tglobal_load_lds_dwordx4 %1, %3 sc1\n\ts_mov_b32 m0, %0"
                 : "=&s"(keep) : "v"(voff), "s"(base), "s"(base2), "s"(dst) : "memory", "scc");
  else
  asm volatile("s_mov_b32 %0, m0\n\ts_mov_b32 m0, %4\n\ts_nop 0\n\tglobal_load_lds_dwordx4 %1, %2\n\t"
               "s_add_u32 m0, m0, 0x2000\n\ts_nop 0\n\tglobal_load_lds_dwordx4 %1, %3\n\ts_mov_b32 m0, %0"
               : "=&s"(keep) : "v"(voff), "s"(base), "s"(base2), "s"(dst) : "memory", "scc");
}

__device__ __forceinline__ void gemm_stream(const char* A, const char* B, int ldb, int nk, char* smem, f32x4 (&acc)[2][4]) {
  int tid = threadIdx.x;
  asm volatile("" : "+v"(tid));
  const int lane = tid & 63, r = lane & 15, q = lane >> 4;
  const int wid = __builtin_amdgcn_readfirstlane(tid >> 6);
  const int wr = wid >> 1, wc = wid & 1;
  const unsigned rb = tid >> 4, va = tid * 16, vb = ((rb & 4) * 4 + (rb >> 3) * 4 + (rb & 3)) * ldb + 16 * ((tid & 15) ^ swzB((tid >> 4) & 15));
  const unsigned lw = lds_off(smem) + wid * 1024;
  auto piece = [&](int kt, int i) {
    const int k = min(kt, nk - 1);
    const char* src = i < 2 ? A + k * 32768 + i * 16384 : B + (size_t)(k * 64 + (i - 2) * 32) * ldb;
    unsigned keep;
    if (i < 2)
      asm volatile("s_mov_b32 %0, m0\n\ts_mov_b32 m0, %3\n\ts_nop 0\n\tglobal_load_lds_dwordx4 %1, %2" A_AUX "\n\ts_mov_b32 m0, %0"
                   : "=&s"(keep) : "v"(va), "s"(src), "s"(lw + (kt & 3) * 32768 + i * 8192) : "memory");
    else
      asm volatile("s_mov_b32 %0, m0\n\ts_mov_b32 m0, %3\n\ts_nop 0\n\tglobal_load_lds_dwordx4 %1, %2\n\ts_mov_b32 m0, %0"
                   : "=&s"(keep) : "v"(vb), "s"(src), "s"(lw + (kt & 3) * 32768 + i * 8192) : "memory");
  };
  unsigned la = lds_off(smem) + wr * 2048 + lane * 16, lb[4][2];
  asm volatile("" : "+v"(la));
#pragma unroll
  for (int n = 0; n < 4; ++n)
#pragma unroll
    for (int u = 0; u < 2; ++u) {
      lb[n][u] = lds_off(smem) + 16384 + offB(8 * q + 4 * u + (r >> 2), 2 * (wc * 4 + n) + ((r & 3) >> 1)) + 8 * (r & 1);
      asm volatile("" : "+v"(lb[n][u]));
    }
  typedef __attribute__((address_space(3))) const bf16x8* lds_frag_ptr;
  bf16x8 af[2][2], bf[2][4];
  auto ldA = [&](int bo, int ks, int m) { return *(lds_frag_ptr)(uintptr_t)(la + bo + ks * 8192 + m * 1024); };
  auto ldB = [&](int bo, int ks, int n) {
    s16x4 lo = __builtin_amdgcn_ds_read_tr16_b64_v4i16((lds_s16x4_ptr)(uintptr_t)(lb[n][0] + bo + ks * 8192));
    s16x4 hi = __builtin_amdgcn_ds_read_tr16_b64_v4i16((lds_s16x4_ptr)(uintptr_t)(lb[n][1] + bo + ks * 8192));
    return (bf16x8){lo[0], lo[1], lo[2], lo[3], hi[0], hi[1], hi[2], hi[3]};
  };
#pragma unroll
  for (int i = 0; i < 8; ++i) acc[i >> 2][i & 3] = (f32x4){0.f, 0.f, 0.f, 0.f};
  LBAR();
#pragma unroll
  for (int i = 0; i < 10; ++i) piece(i >> 2, i & 3);
  WAIT_V(6);
  LBAR();
#pragma unroll
  for (int m = 0; m < 2; ++m) af[0][m] = ldA(0, 0, m);
#pragma unroll
  for (int n = 0; n < 4; ++n) bf[0][n] = ldB(0, 0, n);
  auto loop = [&](auto HF) {
    for (int t0 = 0; t0 < nk; t0 += 4) {
#pragma unroll
      for (int b = 0; b < 4; ++b) {
        const int t = t0 + b;
#pragma unroll
        for (int ks = 0; ks < 2; ++ks) {
          if (ks == 1) {
            WAIT_V(4);
            __builtin_amdgcn_s_barrier();
          }
          const int nb = (ks ? (b + 1) & 3 : b) * 32768, nks = ks ^ 1;
#pragma unroll
          for (int n = 0; n < 4; ++n) {
            acc[0][n] = MFMA(bf[ks][n], af[ks][0], acc[0][n]);
            bf[nks][n] = ldB(nb, nks, n);
            acc[1][n] = MFMA(bf[ks][n], af[ks][1], acc[1][n]);
            if (n < 2) af[nks][n] = ldA(nb, nks, n);
            SCHED();
            if ((n & 1) == HF) { piece(t + 2 + ks, (n >> 1) + 2 * nks); SCHED(); }
          }
        }
      }
    }
  };
  if (wid >> 2) loop(std::integral_constant<int, 1>()); else loop(std::integral_constant<int, 0>());
  WAIT_V(0);
}

#define HSL 16
__device__ __forceinline__ void job_down(const Args& p, int rblk, int ct, char* smem, __amdgpu_buffer_rsrc_t rsy) {
  if (threadIdx.x < 64) {
    for (unsigned spins = 0;;) {
      asm volatile("" ::: "memory");
      if (__builtin_amdgcn_readfirstlane((int)ld_flag0(rsy, SY_CH + (rblk >> 1) * 128)) >= 27) break;
      __builtin_amdgcn_s_sleep(HSL);
      if (spin_fail(spins, (gu32*)(p.ws + WS_SY + SY_ABORT))) break;
    }
  }
  f32x4 acc[2][4];
  gemm_stream(p.ws + WS_HF + h_off(p, rblk), (const char*)(p.wd + ct * 128), DM * 2, FF / 64, smem, acc);
  wait_s(p, rsy, rblk, ct * 4, 4, rblk < NFUSE);
  int tid = threadIdx.x;
  asm volatile("" : "+v"(tid));
  const int lane = tid & 63, wid = tid >> 6, wr = wid >> 1, wc = wid & 1;
  SX sx[4];
#pragma unroll
  for (int i = 0; i < 4; ++i)
    sx[i] = get_sx(p, rblk, (rblk * 128 + wr * 32 + (i >> 1) * 16 + (lane & 15)) * DM + ct * 128 + wc * 64 + (i & 1) * 32 + (lane >> 4) * 8);
  const __amdgpu_buffer_rsrc_t ro = mkrsrc(p.ws + WS_G, S_LEN * DM * 2);
#pragma unroll
  for (int m = 0; m < 2; ++m) {
    const int row = rblk * 128 + wr * 32 + m * 16 + (lane & 15);
#pragma unroll
    for (int nb = 0; nb < 2; ++nb) {
      unsigned pk[2][2];
#pragma unroll
      for (int n = 0; n < 2; ++n) {
        float rr[4];
#pragma unroll
        for (int j = 0; j < 4; ++j) { float d = rbf(acc[m][2 * nb + n][j]); rr[j] = d * __builtin_amdgcn_rcpf(1.0f + __expf(-d)); }
        pk[n][0] = cvtpk(rr[0], rr[1]); pk[n][1] = cvtpk(rr[2], rr[3]);
      }
      u32x4 o = widen32(pk[0][0], pk[0][1], pk[1][0], pk[1][1]);
      put_g(p, ro, rblk, row * DM + ct * 128 + wc * 64 + nb * 32 + (lane >> 4) * 8, o, sx[2 * m + nb]);
    }
  }
  if (rblk >= NFUSE) publish_flags(rsy, SY_FG + (rblk * 8 + ct) * 128, 0, 1);
}

#define PR_ASM_0 \
  "s_cmp_lg_u32 %[kh], 0\n\t" \
  "s_cbranch_scc1 .Lk%=\n\t" \
  "s_mov_b32 s94, m0\n\t" \
  "s_mov_b64 s[92:93], %[x0]\n\t" \
  "s_mov_b32 s86, %[g0]\n\t" \
  "s_add_u32 s87, s86, %[t0]\n\t" \
  "s_sub_i32 s88, s86, 3\n\t" \
  "ds_read_b128 v[176:179], v208 offset:0\n\t" \
  "ds_read_b128 v[180:183], v208 offset:16384\n\t" \
  "ds_read_b128 v[184:187], v209 offset:0\n\t" \
  "ds_read_b128 v[188:191], v209 offset:16384\n\t" \
  "ds_read_b128 v[192:195], v210 offset:0\n\t" \
  "ds_read_b128 v[196:199], v210 offset:16384\n\t" \
  "ds_read_b128 v[200:203], v211 offset:0\n\t" \
  "ds_read_b128 v[204:207], v211 offset:16384\n\t" \
  ".Lp0%=:\n\t" \
  "s_waitcnt lgkmcnt(6)\n\t" \
  "v_mfma_f32_16x16x32_bf16 v[128:131], v[0:3], v[176:179], 0\n\t" \
  "v_mfma_f32_16x16x32_bf16 v[132:135], v[4:7], v[176:179], 0\n\t" \
  "ds_read_b128 v[176:179], v208 offset:256\n\t" \
  "s_add_u32 m0, %[lw], 67584\n\t" \
  "v_mfma_f32_16x16x32_bf16 v[136:139], v[0:3], v[180:183], 0\n\t" \
  "v_mfma_f32_16x16x32_bf16 v[140:143], v[4:7], v[180:183], 0\n\t" \
  "ds_read_b128 v[180:183], v208 offset:16640\n\t" \
  "global_load_lds_dwordx4 v218, s[92:93] sc1\n\t" \
  "s_waitcnt lgkmcnt(6)\n\t" \
  "v_mfma_f32_16x16x32_bf16 v[128:131], v[8:11], v[184:187], v[128:131]\n\t" \
  "v_mfma_f32_16x16x32_bf16 v[132:135], v[12:15], v[184:187], v[132:135]\n\t" \
  "ds_read_b128 v[184:187], v209 offset:256\n\t" \
  "v_mfma_f32_16x16x32_bf16 v[136:139], v[8:11], v[188:191], v[136:139]\n\t" \
  "v_mfma_f32_16x16x32_bf16 v[140:143], v[12:15], v[188:191], v[140:143]\n\t" \
  "ds_read_b128 v[188:191], v209 offset:16640\n\t" \
  "ds_write_b128 v220, v[152:155] offset:0\n\t" \
  "ds_write_b128 v220, v[156:159] offset:1024\n\t" \
  "s_waitcnt lgkmcnt(8)\n\t" \
  "v_mfma_f32_16x16x32_bf16 v[128:131], v[16:19], v[192:195], v[128:131]\n\t" \
  "v_mfma_f32_16x16x32_bf16 v[132:135], v[20:23], v[192:195], v[132:135]\n\t" \
  "ds_read_b128 v[192:195], v210 offset:256\n\t" \
  "s_add_u32 m0, %[lw], 68608\n\t" \
  "v_mfma_f32_16x16x32_bf16 v[136:139], v[16:19], v[196:199], v[136:139]\n\t" \
  "v_mfma_f32_16x16x32_bf16 v[140:143], v[20:23], v[196:199], v[140:143]\n\t" \
  "ds_read_b128 v[196:199], v210 offset:16640\n\t" \
  "global_load_lds_dwordx4 v219, s[92:93] sc1\n\t" \
  "s_lshr_b32 s90, s87, 7\n\t" \
  "s_xor_b32 s90, s90, %[s0]\n\t" \
  "s_and_b32 s90, s90, 1\n\t" \
  "s_lshl1_add_u32 s90, s87, s90\n\t" \
  "s_lshl_b32 s90, s90, 13\n\t" \
  "s_cmp_lt_i32 s86, 3\n\t" \
  "s_cselect_b32 s90, 0x70000000, s90\n\t" \
  "s_waitcnt lgkmcnt(8)\n\t" \
  "v_mfma_f32_16x16x32_bf16 v[128:131], v[24:27], v[200:203], v[128:131]\n\t" \
  "v_mfma_f32_16x16x32_bf16 v[132:135], v[28:31], v[200:203], v[132:135]\n\t" \
  "ds_read_b128 v[200:203], v211 offset:256\n\t" \
  "v_mfma_f32_16x16x32_bf16 v[136:139], v[24:27], v[204:207], v[136:139]\n\t" \
  "v_mfma_f32_16x16x32_bf16 v[140:143], v[28:31], v[204:207], v[140:143]\n\t" \
  "ds_read_b128 v[204:207], v211 offset:16640\n\t" \
  "s_waitcnt lgkmcnt(4)\n\t" \
  "s_waitcnt vmcnt(4)\n\t" \
  "s_barrier\n\t" \
  ".Lg00%=:\n\t" \
  "s_add_u32 s92, s92, 512\n\t" \
  "s_addc_u32 s93, s93, 0\n\t" \
  "v_mfma_f32_16x16x32_bf16 v[128:131], v[32:35], v[176:179], v[128:131]\n\t" \
  "v_mfma_f32_16x16x32_bf16 v[132:135], v[36:39], v[176:179], v[132:135]\n\t" \
  "ds_read_b128 v[176:179], v208 offset:32768\n\t" \
  "s_add_u32 m0, %[lw], 98304\n\t" \
  "v_mfma_f32_16x16x32_bf16 v[136:139], v[32:35], v[180:183], v[136:139]\n\t" \
  "v_mfma_f32_16x16x32_bf16 v[140:143], v[36:39], v[180:183], v[140:143]\n\t" \
  "ds_read_b128 v[180:183], v208 offset:49152\n\t" \
  "global_load_lds_dwordx4 v216, s[92:93] sc1\n\t" \
  "ds_read_b128 v[160:163], v221 offset:0\n\t" \
  "ds_read_b128 v[164:167], v221 offset:1024\n\t" \
  "v_mfma_f32_16x16x32_bf16 v[128:131], v[40:43], v[184:187], v[128:131]\n\t" \
  "v_mfma_f32_16x16x32_bf16 v[132:135], v[44:47], v[184:187], v[132:135]\n\t" \
  "ds_read_b128 v[184:187], v209 offset:32768\n\t" \
  "v_mfma_f32_16x16x32_bf16 v[136:139], v[40:43], v[188:191], v[136:139]\n\t" \
  "v_mfma_f32_16x16x32_bf16 v[140:143], v[44:47], v[188:191], v[140:143]\n\t" \
  "ds_read_b128 v[188:191], v209 offset:49152\n\t" \
  "s_waitcnt lgkmcnt(8)\n\t" \
  "v_mfma_f32_16x16x32_bf16 v[128:131], v[48:51], v[192:195], v[128:131]\n\t" \
  "v_mfma_f32_16x16x32_bf16 v[132:135], v[52:55], v[192:195], v[132:135]\n\t" \
  "ds_read_b128 v[192:195], v210 offset:32768\n\t" \
  "s_add_u32 m0, %[lw], 99328\n\t" \
  "v_mfma_f32_16x16x32_bf16 v[136:139], v[48:51], v[196:199], v[136:139]\n\t" \
  "v_mfma_f32_16x16x32_bf16 v[140:143], v[52:55], v[196:199], v[140:143]\n\t" \
  "ds_read_b128 v[196:199], v210 offset:49152\n\t" \
  "global_load_lds_dwordx4 v217, s[92:93] sc1\n\t" \
  "s_waitcnt lgkmcnt(4)\n\t" \
  "v_add_f32 v160, v160, v144\n\t" \
  "v_add_f32 v161, v161, v145\n\t" \
  "v_add_f32 v162, v162, v146\n\t" \
  "v_add_f32 v163, v163, v147\n\t" \
  "v_add_f32 v164, v164, v148\n\t" \
  "v_add_f32 v165, v165, v149\n\t" \
  "v_add_f32 v166, v166, v150\n\t" \
  "v_add_f32 v167, v167, v151\n\t" \
  "v_mfma_f32_16x16x32_bf16 v[128:131], v[56:59], v[200:203], v[128:131]\n\t" \
  "v_mfma_f32_16x16x32_bf16 v[132:135], v[60:63], v[200:203], v[132:135]\n\t" \
  "ds_read_b128 v[200:203], v211 offset:32768\n\t" \
  "v_mfma_f32_16x16x32_bf16 v[136:139], v[56:59], v[204:207], v[136:139]\n\t" \
  "v_mfma_f32_16x16x32_bf16 v[140:143], v[60:63], v[204:207], v[140:143]\n\t" \
  "ds_read_b128 v[204:207], v211 offset:49152\n\t" \
  "v_cvt_pk_bf16_f32 v168, v160, v161\n\t" \
  "v_cvt_pk_bf16_f32 v169, v162, v163\n\t" \
  "v_cvt_pk_bf16_f32 v170, v164, v165\n\t" \
  "v_cvt_pk_bf16_f32 v171, v166, v167\n\t" \
  "buffer_store_dwordx4 v[168:171], v172, %[rt], s90 offen sc1\n\t" \
  "v_mfma_f32_16x16x32_bf16 v[128:131], v[64:67], v[176:179], v[128:131]\n\t" \
  "v_mfma_f32_16x16x32_bf16 v[132:135], v[68:71], v[176:179], v[132:135]\n\t" \
  "ds_read_b128 v[176:179], v208 offset:33024\n\t" \
  "s_add_u32 m0, %[lw], 100352\n\t" \
  "v_mfma_f32_16x16x32_bf16 v[136:139], v[64:67], v[180:183], v[136:139]\n\t" \
  "v_mfma_f32_16x16x32_bf16 v[140:143], v[68:71], v[180:183], v[140:143]\n\t" \
  "ds_read_b128 v[180:183], v208 offset:49408\n\t" \
  "global_load_lds_dwordx4 v218, s[92:93] sc1\n\t" \
  "s_waitcnt lgkmcnt(6)\n\t" \
  "v_mfma_f32_16x16x32_bf16 v[128:131], v[72:75], v[184:187], v[128:131]\n\t" \
  "v_mfma_f32_16x16x32_bf16 v[132:135], v[76:79], v[184:187], v[132:135]\n\t" \
  "ds_read_b128 v[184:187], v209 offset:33024\n\t" \
  "v_mfma_f32_16x16x32_bf16 v[136:139], v[72:75], v[188:191], v[136:139]\n\t" \
  "v_mfma_f32_16x16x32_bf16 v[140:143], v[76:79], v[188:191], v[140:143]\n\t" \
  "ds_read_b128 v[188:191], v209 offset:49408\n\t" \
  "s_waitcnt lgkmcnt(6)\n\t" \
  "v_mfma_f32_16x16x32_bf16 v[128:131], v[80:83], v[192:195], v[128:131]\n\t" \
  "v_mfma_f32_16x16x32_bf16 v[132:135], v[84:87], v[192:195], v[132:135]\n\t" \
  "ds_read_b128 v[192:195], v210 offset:33024\n\t" \
  "s_add_u32 m0, %[lw], 101376\n\t" \
  "v_mfma_f32_16x16x32_bf16 v[136:139], v[80:83], v[196:199], v[136:139]\n\t" \
  "v_mfma_f32_16x16x32_bf16 v[140:143], v[84:87], v[196:199], v[140:143]\n\t" \
  "ds_read_b128 v[196:199], v210 offset:49408\n\t" \
  "global_load_lds_dwordx4 v219, s[92:93] sc1\n\t" \
  "s_waitcnt lgkmcnt(6)\n\t" \
  "v_mfma_f32_16x16x32_bf16 v[128:131], v[88:91], v[200:203], v[128:131]\n\t" \
  "v_mfma_f32_16x16x32_bf16 v[132:135], v[92:95], v[200:203], v[132:135]\n\t" \
  "ds_read_b128 v[200:203], v211 offset:33024\n\t" \
  "v_mfma_f32_16x16x32_bf16 v[136:139], v[88:91], v[204:207], v[136:139]\n\t" \
  "v_mfma_f32_16x16x32_bf16 v[140:143], v[92:95], v[204:207], v[140:143]\n\t" \
  "ds_read_b128 v[204:207], v211 offset:49408\n\t" \
  "s_cmp_lt_u32 s86, 6\n\t" \
  "s_cbranch_scc1 .Lw10%=\n\t" \
  "s_waitcnt vmcnt(5)\n\t" \
  "s_barrier\n\t" \
  ".Lg10%=:\n\t" \
  "s_add_u32 s92, s92, 65024\n\t" \
  "s_addc_u32 s93, s93, 0\n\t" \
  "s_waitcnt lgkmcnt(6)\n\t" \
  "v_mfma_f32_16x16x32_bf16 v[128:131], v[96:99], v[176:179], v[128:131]\n\t" \
  "v_mfma_f32_16x16x32_bf16 v[132:135], v[100:103], v[176:179], v[132:135]\n\t" \
  "ds_read_b128 v[176:179], v212 offset:0\n\t" \
  "s_add_u32 m0, %[lw], 0\n\t" \
  "v_mfma_f32_16x16x32_bf16 v[136:139], v[96:99], v[180:183], v[136:139]\n\t" \
  "v_mfma_f32_16x16x32_bf16 v[140:143], v[100:103], v[180:183], v[140:143]\n\t" \
  "ds_read_b128 v[180:183], v212 offset:16384\n\t" \
  "global_load_lds_dwordx4 v216, s[92:93] sc1\n\t" \
  "s_waitcnt lgkmcnt(6)\n\t" \
  "v_mfma_f32_16x16x32_bf16 v[128:131], v[104:107], v[184:187], v[128:131]\n\t" \
  "v_mfma_f32_16x16x32_bf16 v[132:135], v[108:111], v[184:187], v[132:135]\n\t" \
  "ds_read_b128 v[184:187], v213 offset:0\n\t" \
  "v_mfma_f32_16x16x32_bf16 v[136:139], v[104:107], v[188:191], v[136:139]\n\t" \
  "v_mfma_f32_16x16x32_bf16 v[140:143], v[108:111], v[188:191], v[140:143]\n\t" \
  "ds_read_b128 v[188:191], v213 offset:16384\n\t" \
  "s_waitcnt lgkmcnt(6)\n\t" \
  "v_mfma_f32_16x16x32_bf16 v[128:131], v[112:115], v[192:195], v[128:131]\n\t" \
  "v_mfma_f32_16x16x32_bf16 v[132:135], v[116:119], v[192:195], v[132:135]\n\t" \
  "ds_read_b128 v[192:195], v214 offset:0\n\t" \
  "s_add_u32 m0, %[lw], 1024\n\t" \
  "v_mfma_f32_16x16x32_bf16 v[136:139], v[112:115], v[196:199], v[136:139]\n\t" \
  "v_mfma_f32_16x16x32_bf16 v[140:143], v[116:119], v[196:199], v[140:143]\n\t" \
  "ds_read_b128 v[196:199], v214 offset:16384\n\t" \
  "global_load_lds_dwordx4 v217, s[92:93] sc1\n\t" \
  "s_waitcnt lgkmcnt(6)\n\t" \
  "v_mfma_f32_16x16x32_bf16 v[128:131], v[120:123], v[200:203], v[128:131]\n\t" \
  "v_mfma_f32_16x16x32_bf16 v[132:135], v[124:127], v[200:203], v[132:135]\n\t" \
  "ds_read_b128 v[200:203], v215 offset:0\n\t" \
  "v_mfma_f32_16x16x32_bf16 v[136:139], v[120:123], v[204:207], v[136:139]\n\t" \
  "v_mfma_f32_16x16x32_bf16 v[140:143], v[124:127], v[204:207], v[140:143]\n\t" \
  "ds_read_b128 v[204:207], v215 offset:16384\n\t" \
  "s_add_u32 s86, s86, 1\n\t" \
  "s_add_u32 s87, s87, 1\n\t" \
  "s_cmp_eq_u32 s86, 1\n\t" \
  "s_cbranch_scc1 .Lf0%=\n\t" \
  ".Lfb0%=:\n\t" \
  "s_waitcnt lgkmcnt(6)\n\t" \
  "v_mfma_f32_16x16x32_bf16 v[144:147], v[0:3], v[176:179], 0\n\t" \
  "v_mfma_f32_16x16x32_bf16 v[148:151], v[4:7], v[176:179], 0\n\t" \
  "ds_read_b128 v[176:179], v212 offset:256\n\t" \
  "s_add_u32 m0, %[lw], 2048\n\t" \
  "v_mfma_f32_16x16x32_bf16 v[152:155], v[0:3], v[180:183], 0\n\t" \
  "v_mfma_f32_16x16x32_bf16 v[156:159], v[4:7], v[180:183], 0\n\t" \
  "ds_read_b128 v[180:183], v212 offset:16640\n\t" \
  "global_load_lds_dwordx4 v218, s[92:93] sc1\n\t" \
  "s_waitcnt lgkmcnt(6)\n\t" \
  "v_mfma_f32_16x16x32_bf16 v[144:147], v[8:11], v[184:187], v[144:147]\n\t" \
  "v_mfma_f32_16x16x32_bf16 v[148:151], v[12:15], v[184:187], v[148:151]\n\t" \
  "ds_read_b128 v[184:187], v213 offset:256\n\t" \
  "v_mfma_f32_16x16x32_bf16 v[152:155], v[8:11], v[188:191], v[152:155]\n\t" \
  "v_mfma_f32_16x16x32_bf16 v[156:159], v[12:15], v[188:191], v[156:159]\n\t" \
  "ds_read_b128 v[188:191], v213 offset:16640\n\t" \
  "ds_write_b128 v220, v[136:139] offset:0\n\t" \
  "ds_write_b128 v220, v[140:143] offset:1024\n\t" \
  "s_waitcnt lgkmcnt(8)\n\t" \
  "v_mfma_f32_16x16x32_bf16 v[144:147], v[16:19], v[192:195], v[144:147]\n\t" \
  "v_mfma_f32_16x16x32_bf16 v[148:151], v[20:23], v[192:195], v[148:151]\n\t" \
  "ds_read_b128 v[192:195], v214 offset:256\n\t" \
  "s_add_u32 m0, %[lw], 3072\n\t" \
  "v_mfma_f32_16x16x32_bf16 v[152:155], v[16:19], v[196:199], v[152:155]\n\t" \
  "v_mfma_f32_16x16x32_bf16 v[156:159], v[20:23], v[196:199], v[156:159]\n\t" \
  "ds_read_b128 v[196:199], v214 offset:16640\n\t" \
  "global_load_lds_dwordx4 v219, s[92:93] sc1\n\t" \
  "s_lshr_b32 s90, s87, 7\n\t" \
  "s_xor_b32 s90, s90, %[s0]\n\t" \
  "s_and_b32 s90, s90, 1\n\t" \
  "s_lshl1_add_u32 s90, s87, s90\n\t" \
  "s_lshl_b32 s90, s90, 13\n\t" \
  "s_cmp_lt_i32 s86, 3\n\t" \
  "s_cselect_b32 s90, 0x70000000, s90\n\t" \
  "s_waitcnt lgkmcnt(8)\n\t" \
  "v_mfma_f32_16x16x32_bf16 v[144:147], v[24:27], v[200:203], v[144:147]\n\t" \
  "v_mfma_f32_16x16x32_bf16 v[148:151], v[28:31], v[200:203], v[148:151]\n\t" \
  "ds_read_b128 v[200:203], v215 offset:256\n\t" \
  "v_mfma_f32_16x16x32_bf16 v[152:155], v[24:27], v[204:207], v[152:155]\n\t" \
  "v_mfma_f32_16x16x32_bf16 v[156:159], v[28:31], v[204:207], v[156:159]\n\t" \
  "ds_read_b128 v[204:207], v215 offset:16640\n\t" \
  "s_waitcnt lgkmcnt(4)\n\t" \
  "s_waitcnt vmcnt(4)\n\t" \
  "s_barrier\n\t" \
  ".Lg20%=:\n\t" \
  "s_add_u32 s92, s92, 512\n\t" \
  "s_addc_u32 s93, s93, 0\n\t" \
  "v_mfma_f32_16x16x32_bf16 v[144:147], v[32:35], v[176:179], v[144:147]\n\t" \
  "v_mfma_f32_16x16x32_bf16 v[148:151], v[36:39], v[176:179], v[148:151]\n\t" \
  "ds_read_b128 v[176:179], v212 offset:32768\n\t" \
  "s_add_u32 m0, %[lw], 32768\n\t" \
  "v_mfma_f32_16x16x32_bf16 v[152:155], v[32:35], v[180:183], v[152:155]\n\t" \
  "v_mfma_f32_16x16x32_bf16 v[156:159], v[36:39], v[180:183], v[156:159]\n\t" \
  "ds_read_b128 v[180:183], v212 offset:49152\n\t" \
  "global_load_lds_dwordx4 v216, s[92:93] sc1\n\t" \
  "ds_read_b128 v[160:163], v221 offset:0\n\t" \
  "ds_read_b128 v[164:167], v221 offset:1024\n\t" \
  "v_mfma_f32_16x16x32_bf16 v[144:147], v[40:43], v[184:187], v[144:147]\n\t" \
  "v_mfma_f32_16x16x32_bf16 v[148:151], v[44:47], v[184:187], v[148:151]\n\t" \
  "ds_read_b128 v[184:187], v213 offset:32768\n\t" \
  "v_mfma_f32_16x16x32_bf16 v[152:155], v[40:43], v[188:191], v[152:155]\n\t" \
  "v_mfma_f32_16x16x32_bf16 v[156:159], v[44:47], v[188:191], v[156:159]\n\t" \
  "ds_read_b128 v[188:191], v213 offset:49152\n\t" \
  "s_waitcnt lgkmcnt(8)\n\t" \
  "v_mfma_f32_16x16x32_bf16 v[144:147], v[48:51], v[192:195], v[144:147]\n\t" \
  "v_mfma_f32_16x16x32_bf16 v[148:151], v[52:55], v[192:195], v[148:151]\n\t" \
  "ds_read_b128 v[192:195], v214 offset:32768\n\t" \
  "s_add_u32 m0, %[lw], 33792\n\t" \
  "v_mfma_f32_16x16x32_bf16 v[152:155], v[48:51], v[196:199], v[152:155]\n\t" \
  "v_mfma_f32_16x16x32_bf16 v[156:159], v[52:55], v[196:199], v[156:159]\n\t" \
  "ds_read_b128 v[196:199], v214 offset:49152\n\t" \
  "global_load_lds_dwordx4 v217, s[92:93] sc1\n\t" \
  "s_waitcnt lgkmcnt(4)\n\t" \
  "v_add_f32 v160, v160, v128\n\t" \
  "v_add_f32 v161, v161, v129\n\t" \
  "v_add_f32 v162, v162, v130\n\t" \
  "v_add_f32 v163, v163, v131\n\t" \
  "v_add_f32 v164, v164, v132\n\t" \
  "v_add_f32 v165, v165, v133\n\t" \
  "v_add_f32 v166, v166, v134\n\t" \
  "v_add_f32 v167, v167, v135\n\t" \
  "v_mfma_f32_16x16x32_bf16 v[144:147], v[56:59], v[200:203], v[144:147]\n\t" \
  "v_mfma_f32_16x16x32_bf16 v[148:151], v[60:63], v[200:203], v[148:151]\n\t" \
  "ds_read_b128 v[200:203], v215 offset:32768\n\t" \
  "v_mfma_f32_16x16x32_bf16 v[152:155], v[56:59], v[204:207], v[152:155]\n\t" \
  "v_mfma_f32_16x16x32_bf16 v[156:159], v[60:63], v[204:207], v[156:159]\n\t" \
  "ds_read_b128 v[204:207], v215 offset:49152\n\t" \
  "v_cvt_pk_bf16_f32 v168, v160, v161\n\t" \
  "v_cvt_pk_bf16_f32 v169, v162, v163\n\t" \
  "v_cvt_pk_bf16_f32 v170, v164, v165\n\t" \
  "v_cvt_pk_bf16_f32 v171, v166, v167\n\t" \
  "buffer_store_dwordx4 v[168:171], v172, %[rt], s90 offen sc1\n\t" \
  "v_mfma_f32_16x16x32_bf16 v[144:147], v[64:67], v[176:179], v[144:147]\n\t" \
  "v_mfma_f32_16x16x32_bf16 v[148:151], v[68:71], v[176:179], v[148:151]\n\t" \
  "ds_read_b128 v[176:179], v212 offset:33024\n\t" \
  "s_add_u32 m0, %[lw], 34816\n\t" \
  "v_mfma_f32_16x16x32_bf16 v[152:155], v[64:67], v[180:183], v[152:155]\n\t" \
  "v_mfma_f32_16x16x32_bf16 v[156:159], v[68:71], v[180:183], v[156:159]\n\t" \
  "ds_read_b128 v[180:183], v212 offset:49408\n\t" \
  "global_load_lds_dwordx4 v218, s[92:93] sc1\n\t" \
  "s_waitcnt lgkmcnt(6)\n\t" \
  "v_mfma_f32_16x16x32_bf16 v[144:147], v[72:75], v[184:187], v[144:147]\n\t" \
  "v_mfma_f32_16x16x32_bf16 v[148:151], v[76:79], v[184:187], v[148:151]\n\t" \
  "ds_read_b128 v[184:187], v213 offset:33024\n\t" \
  "v_mfma_f32_16x16x32_bf16 v[152:155], v[72:75], v[188:191], v[152:155]\n\t" \
  "v_mfma_f32_16x16x32_bf16 v[156:159], v[76:79], v[188:191], v[156:159]\n\t" \
  "ds_read_b128 v[188:191], v213 offset:49408\n\t" \
  "s_waitcnt lgkmcnt(6)\n\t" \
  "v_mfma_f32_16x16x32_bf16 v[144:147], v[80:83], v[192:195], v[144:147]\n\t" \
  "v_mfma_f32_16x16x32_bf16 v[148:151], v[84:87], v[192:195], v[148:151]\n\t" \
  "ds_read_b128 v[192:195], v214 offset:33024\n\t" \
  "s_add_u32 m0, %[lw], 35840\n\t" \
  "v_mfma_f32_16x16x32_bf16 v[152:155], v[80:83], v[196:199], v[152:155]\n\t" \
  "v_mfma_f32_16x16x32_bf16 v[156:159], v[84:87], v[196:199], v[156:159]\n\t" \
  "ds_read_b128 v[196:199], v214 offset:49408\n\t" \
  "global_load_lds_dwordx4 v219, s[92:93] sc1\n\t" \
  "s_waitcnt lgkmcnt(6)\n\t" \
  "v_mfma_f32_16x16x32_bf16 v[144:147], v[88:91], v[200:203], v[144:147]\n\t" \
  "v_mfma_f32_16x16x32_bf16 v[148:151], v[92:95], v[200:203], v[148:151]\n\t" \
  "ds_read_b128 v[200:203], v215 offset:33024\n\t" \
  "v_mfma_f32_16x16x32_bf16 v[152:155], v[88:91], v[204:207], v[152:155]\n\t" \
  "v_mfma_f32_16x16x32_bf16 v[156:159], v[92:95], v[204:207], v[156:159]\n\t" \
  "ds_read_b128 v[204:207], v215 offset:49408\n\t" \
  "s_cmp_lt_u32 s86, 6\n\t" \
  "s_cbranch_scc1 .Lw30%=\n\t" \
  "s_waitcnt vmcnt(5)\n\t" \
  "s_barrier\n\t" \
  ".Lg30%=:\n\t" \
  "s_add_u32 s92, s92, 65024\n\t" \
  "s_addc_u32 s93, s93, 0\n\t" \
  "s_waitcnt lgkmcnt(6)\n\t" \
  "v_mfma_f32_16x16x32_bf16 v[144:147], v[96:99], v[176:179], v[144:147]\n\t" \
  "v_mfma_f32_16x16x32_bf16 v[148:151], v[100:103], v[176:179], v[148:151]\n\t" \
  "ds_read_b128 v[176:179], v208 offset:0\n\t" \
  "s_add_u32 m0, %[lw], 65536\n\t" \
  "v_mfma_f32_16x16x32_bf16 v[152:155], v[96:99], v[180:183], v[152:155]\n\t" \
  "v_mfma_f32_16x16x32_bf16 v[156:159], v[100:103], v[180:183], v[156:159]\n\t" \
  "ds_read_b128 v[180:183], v208 offset:16384\n\t" \
  "global_load_lds_dwordx4 v216, s[92:93] sc1\n\t" \
  "s_waitcnt lgkmcnt(6)\n\t" \
  "v_mfma_f32_16x16x32_bf16 v[144:147], v[104:107], v[184:187], v[144:147]\n\t" \
  "v_mfma_f32_16x16x32_bf16 v[148:151], v[108:111], v[184:187], v[148:151]\n\t" \
  "ds_read_b128 v[184:187], v209 offset:0\n\t" \
  "v_mfma_f32_16x16x32_bf16 v[152:155], v[104:107], v[188:191], v[152:155]\n\t" \
  "v_mfma_f32_16x16x32_bf16 v[156:159], v[108:111], v[188:191], v[156:159]\n\t" \
  "ds_read_b128 v[188:191], v209 offset:16384\n\t" \
  "s_waitcnt lgkmcnt(6)\n\t" \
  "v_mfma_f32_16x16x32_bf16 v[144:147], v[112:115], v[192:195], v[144:147]\n\t" \
  "v_mfma_f32_16x16x32_bf16 v[148:151], v[116:119], v[192:195], v[148:151]\n\t" \
  "ds_read_b128 v[192:195], v210 offset:0\n\t" \
  "s_add_u32 m0, %[lw], 66560\n\t" \
  "v_mfma_f32_16x16x32_bf16 v[152:155], v[112:115], v[196:199], v[152:155]\n\t" \
  "v_mfma_f32_16x16x32_bf16 v[156:159], v[116:119], v[196:199], v[156:159]\n\t" \
  "ds_read_b128 v[196:199], v210 offset:16384\n\t" \
  "global_load_lds_dwordx4 v217, s[92:93] sc1\n\t" \
  "s_waitcnt lgkmcnt(6)\n\t" \
  "v_mfma_f32_16x16x32_bf16 v[144:147], v[120:123], v[200:203], v[144:147]\n\t" \
  "v_mfma_f32_16x16x32_bf16 v[148:151], v[124:127], v[200:203], v[148:151]\n\t" \
  "ds_read_b128 v[200:203], v211 offset:0\n\t" \
  "v_mfma_f32_16x16x32_bf16 v[152:155], v[120:123], v[204:207], v[152:155]\n\t" \
  "v_mfma_f32_16x16x32_bf16 v[156:159], v[124:127], v[204:207], v[156:159]\n\t" \
  "ds_read_b128 v[204:207], v211 offset:16384\n\t" \
  "s_add_u32 s86, s86, 1\n\t" \
  "s_add_u32 s87, s87, 1\n\t" \
  "s_cmp_eq_u32 s86, 2\n\t" \
  "s_cbranch_scc1 .Ll0%=\n\t" \
  "s_cmp_lt_u32 s86, %[end]\n\t" \
  "s_cbranch_scc1 .Lp0%=\n\t" \
  "s_cmp_lg_u32 s86, 64\n\t" \
  "s_cbranch_scc1 .Lx0%=\n\t" \
  ".Ll0%=:\n\t" \
  "s_nop 7\n\t" \
  "ds_write_b128 v220, v[152:155] offset:0\n\t" \
  "ds_write_b128 v220, v[156:159] offset:1024\n\t" \
  "s_lshr_b32 s90, s87, 7\n\t" \
  "s_xor_b32 s90, s90, %[s0]\n\t" \
  "s_and_b32 s90, s90, 1\n\t" \
  "s_lshl1_add_u32 s90, s87, s90\n\t" \
  "s_lshl_b32 s90, s90, 13\n\t" \
  "s_cmp_lt_i32 s86, 1\n\t" \
  "s_cselect_b32 s90, 0x70000000, s90\n\t" \
  "s_waitcnt lgkmcnt(0)\n\t" \
  "s_barrier\n\t" \
  "ds_read_b128 v[160:163], v221 offset:0\n\t" \
  "ds_read_b128 v[164:167], v221 offset:1024\n\t" \
  "s_waitcnt lgkmcnt(0)\n\t" \
  "v_add_f32 v160, v160, v144\n\t" \
  "v_add_f32 v161, v161, v145\n\t" \
  "v_add_f32 v162, v162, v146\n\t" \
  "v_add_f32 v163, v163, v147\n\t" \
  "v_add_f32 v164, v164, v148\n\t" \
  "v_add_f32 v165, v165, v149\n\t" \
  "v_add_f32 v166, v166, v150\n\t" \
  "v_add_f32 v167, v167, v151\n\t" \
  "v_cvt_pk_bf16_f32 v168, v160, v161\n\t" \
  "v_cvt_pk_bf16_f32 v169, v162, v163\n\t" \
  "v_cvt_pk_bf16_f32 v170, v164, v165\n\t" \
  "v_cvt_pk_bf16_f32 v171, v166, v167\n\t" \
  "buffer_store_dwordx4 v[168:171], v172, %[rt], s90 offen sc1\n\t" \
  "s_waitcnt vmcnt(0)\n\t" \
  "s_barrier\n\t" \
  "s_cmp_eq_u32 s86, 2\n\t" \
  "s_cbranch_scc1 .Lp0%=\n\t" \
  "s_branch .Lx0%=\n\t" \
  ".Lf0%=:\n\t" \
  "s_nop 7\n\t" \
  "ds_write_b128 v220, v[136:139] offset:0\n\t" \
  "ds_write_b128 v220, v[140:143] offset:1024\n\t" \
  "s_lshr_b32 s90, s87, 7\n\t" \
  "s_xor_b32 s90, s90, %[s0]\n\t" \
  "s_and_b32 s90, s90, 1\n\t" \
  "s_lshl1_add_u32 s90, s87, s90\n\t" \
  "s_lshl_b32 s90, s90, 13\n\t" \
  "s_cmp_lt_i32 s86, 1\n\t" \
  "s_cselect_b32 s90, 0x70000000, s90\n\t" \
  "s_waitcnt lgkmcnt(0)\n\t" \
  "s_barrier\n\t" \
  "ds_read_b128 v[160:163], v221 offset:0\n\t" \
  "ds_read_b128 v[164:167], v221 offset:1024\n\t" \
  "s_waitcnt lgkmcnt(0)\n\t" \
  "v_add_f32 v160, v160, v128\n\t" \
  "v_add_f32 v161, v161, v129\n\t" \
  "v_add_f32 v162, v162, v130\n\t" \
  "v_add_f32 v163, v163, v131\n\t" \
  "v_add_f32 v164, v164, v132\n\t" \
  "v_add_f32 v165, v165, v133\n\t" \
  "v_add_f32 v166, v166, v134\n\t" \
  "v_add_f32 v167, v167, v135\n\t" \
  "v_cvt_pk_bf16_f32 v168, v160, v161\n\t" \
  "v_cvt_pk_bf16_f32 v169, v162, v163\n\t" \
  "v_cvt_pk_bf16_f32 v170, v164, v165\n\t" \
  "v_cvt_pk_bf16_f32 v171, v166, v167\n\t" \
  "buffer_store_dwordx4 v[168:171], v172, %[rt], s90 offen sc1\n\t" \
  "s_waitcnt vmcnt(0)\n\t" \
  "s_barrier\n\t" \
  "s_branch .Lfb0%=\n\t" \
  ".Lw10%=:\n\t" \
  "s_waitcnt vmcnt(2)\n\t" \
  "s_barrier\n\t" \
  "s_branch .Lg10%=\n\t" \
  ".Lw30%=:\n\t" \
  "s_waitcnt vmcnt(2)\n\t" \
  "s_barrier\n\t" \
  "s_branch .Lg30%=\n\t" \
  ".Lx0%=:\n\t" \
  "s_waitcnt lgkmcnt(0)\n\t" \
  "s_setprio 0\n\t" \
  "s_mov_b32 m0, s94\n\t" \
  "s_branch .Le%=\n\t" \
  ".Lk%=:\n\t" \
  "s_mov_b32 s94, m0\n\t" \
  "s_mov_b64 s[92:93], %[x0]\n\t" \
  "s_mov_b32 s86, %[g0]\n\t" \
  "s_add_u32 s87, s86, %[t0]\n\t" \
  "s_sub_i32 s88, s86, 3\n\t" \
  "ds_read_b128 v[176:179], v208 offset:0\n\t" \
  "ds_read_b128 v[180:183], v208 offset:16384\n\t" \
  "ds_read_b128 v[184:187], v209 offset:0\n\t" \
  "ds_read_b128 v[188:191], v209 offset:16384\n\t" \
  "ds_read_b128 v[192:195], v210 offset:0\n\t" \
  "ds_read_b128 v[196:199], v210 offset:16384\n\t" \
  "ds_read_b128 v[200:203], v211 offset:0\n\t" \
  "ds_read_b128 v[204:207], v211 offset:16384\n\t" \
  ".Lp1%=:\n\t" \
  "s_waitcnt lgkmcnt(6)\n\t" \
  "v_mfma_f32_16x16x32_bf16 v[128:131], v[0:3], v[176:179], 0\n\t" \
  "v_mfma_f32_16x16x32_bf16 v[132:135], v[4:7], v[176:179], 0\n\t" \
  "ds_read_b128 v[176:179], v208 offset:256\n\t" \
  "v_mfma_f32_16x16x32_bf16 v[136:139], v[0:3], v[180:183], 0\n\t" \
  "v_mfma_f32_16x16x32_bf16 v[140:143], v[4:7], v[180:183], 0\n\t" \
  "ds_read_b128 v[180:183], v208 offset:16640\n\t" \
  "s_waitcnt lgkmcnt(6)\n\t" \
  "v_mfma_f32_16x16x32_bf16 v[128:131], v[8:11], v[184:187], v[128:131]\n\t" \
  "v_mfma_f32_16x16x32_bf16 v[132:135], v[12:15], v[184:187], v[132:135]\n\t" \
  "ds_read_b128 v[184:187], v209 offset:256\n\t" \
  "s_add_u32 m0, %[lw], 67584\n\t" \
  "v_mfma_f32_16x16x32_bf16 v[136:139], v[8:11], v[188:191], v[136:139]\n\t" \
  "v_mfma_f32_16x16x32_bf16 v[140:143], v[12:15], v[188:191], v[140:143]\n\t" \
  "ds_read_b128 v[188:191], v209 offset:16640\n\t" \
  "global_load_lds_dwordx4 v218, s[92:93] sc1\n\t" \
  "ds_write_b128 v220, v[152:155] offset:0\n\t" \
  "ds_write_b128 v220, v[156:159] offset:1024\n\t" \
  "s_waitcnt lgkmcnt(8)\n\t" \
  "v_mfma_f32_16x16x32_bf16 v[128:131], v[16:19], v[192:195], v[128:131]\n\t" \
  "v_mfma_f32_16x16x32_bf16 v[132:135], v[20:23], v[192:195], v[132:135]\n\t" \
  "ds_read_b128 v[192:195], v210 offset:256\n\t" \
  "v_mfma_f32_16x16x32_bf16 v[136:139], v[16:19], v[196:199], v[136:139]\n\t" \
  "v_mfma_f32_16x16x32_bf16 v[140:143], v[20:23], v[196:199], v[140:143]\n\t" \
  "ds_read_b128 v[196:199], v210 offset:16640\n\t" \
  "s_lshr_b32 s90, s87, 7\n\t" \
  "s_xor_b32 s90, s90, %[s0]\n\t" \
  "s_and_b32 s90, s90, 1\n\t" \
  "s_lshl1_add_u32 s90, s87, s90\n\t" \
  "s_lshl_b32 s90, s90, 13\n\t" \
  "s_cmp_lt_i32 s86, 3\n\t" \
  "s_cselect_b32 s90, 0x70000000, s90\n\t" \
  "s_waitcnt lgkmcnt(8)\n\t" \
  "v_mfma_f32_16x16x32_bf16 v[128:131], v[24:27], v[200:203], v[128:131]\n\t" \
  "v_mfma_f32_16x16x32_bf16 v[132:135], v[28:31], v[200:203], v[132:135]\n\t" \
  "ds_read_b128 v[200:203], v211 offset:256\n\t" \
  "s_add_u32 m0, %[lw], 68608\n\t" \
  "v_mfma_f32_16x16x32_bf16 v[136:139], v[24:27], v[204:207], v[136:139]\n\t" \
  "v_mfma_f32_16x16x32_bf16 v[140:143], v[28:31], v[204:207], v[140:143]\n\t" \
  "ds_read_b128 v[204:207], v211 offset:16640\n\t" \
  "global_load_lds_dwordx4 v219, s[92:93] sc1\n\t" \
  "s_waitcnt lgkmcnt(4)\n\t" \
  "s_waitcnt vmcnt(4)\n\t" \
  "s_barrier\n\t" \
  ".Lg01%=:\n\t" \
  "s_add_u32 s92, s92, 512\n\t" \
  "s_addc_u32 s93, s93, 0\n\t" \
  "v_mfma_f32_16x16x32_bf16 v[128:131], v[32:35], v[176:179], v[128:131]\n\t" \
  "v_mfma_f32_16x16x32_bf16 v[132:135], v[36:39], v[176:179], v[132:135]\n\t" \
  "ds_read_b128 v[176:179], v208 offset:32768\n\t" \
  "v_mfma_f32_16x16x32_bf16 v[136:139], v[32:35], v[180:183], v[136:139]\n\t" \
  "v_mfma_f32_16x16x32_bf16 v[140:143], v[36:39], v[180:183], v[140:143]\n\t" \
  "ds_read_b128 v[180:183], v208 offset:49152\n\t" \
  "ds_read_b128 v[160:163], v221 offset:0\n\t" \
  "ds_read_b128 v[164:167], v221 offset:1024\n\t" \
  "s_sub_i32 s91, s86, 2\n\t" \
  "s_max_i32 s88, s88, s91\n\t" \
  "v_mov_b32 v173, s88\n\t" \
  "buffer_store_dword v173, v174, %[rs], 0 offen sc1\n\t" \
  "v_mfma_f32_16x16x32_bf16 v[128:131], v[40:43], v[184:187], v[128:131]\n\t" \
  "v_mfma_f32_16x16x32_bf16 v[132:135], v[44:47], v[184:187], v[132:135]\n\t" \
  "ds_read_b128 v[184:187], v209 offset:32768\n\t" \
  "s_add_u32 m0, %[lw], 98304\n\t" \
  "v_mfma_f32_16x16x32_bf16 v[136:139], v[40:43], v[188:191], v[136:139]\n\t" \
  "v_mfma_f32_16x16x32_bf16 v[140:143], v[44:47], v[188:191], v[140:143]\n\t" \
  "ds_read_b128 v[188:191], v209 offset:49152\n\t" \
  "global_load_lds_dwordx4 v216, s[92:93] sc1\n\t" \
  "s_waitcnt lgkmcnt(8)\n\t" \
  "v_mfma_f32_16x16x32_bf16 v[128:131], v[48:51], v[192:195], v[128:131]\n\t" \
  "v_mfma_f32_16x16x32_bf16 v[132:135], v[52:55], v[192:195], v[132:135]\n\t" \
  "ds_read_b128 v[192:195], v210 offset:32768\n\t" \
  "v_mfma_f32_16x16x32_bf16 v[136:139], v[48:51], v[196:199], v[136:139]\n\t" \
  "v_mfma_f32_16x16x32_bf16 v[140:143], v[52:55], v[196:199], v[140:143]\n\t" \
  "ds_read_b128 v[196:199], v210 offset:49152\n\t" \
  "s_waitcnt lgkmcnt(4)\n\t" \
  "v_add_f32 v160, v160, v144\n\t" \
  "v_add_f32 v161, v161, v145\n\t" \
  "v_add_f32 v162, v162, v146\n\t" \
  "v_add_f32 v163, v163, v147\n\t" \
  "v_add_f32 v164, v164, v148\n\t" \
  "v_add_f32 v165, v165, v149\n\t" \
  "v_add_f32 v166, v166, v150\n\t" \
  "v_add_f32 v167, v167, v151\n\t" \
  "v_mfma_f32_16x16x32_bf16 v[128:131], v[56:59], v[200:203], v[128:131]\n\t" \
  "v_mfma_f32_16x16x32_bf16 v[132:135], v[60:63], v[200:203], v[132:135]\n\t" \
  "ds_read_b128 v[200:203], v211 offset:32768\n\t" \
  "s_add_u32 m0, %[lw], 99328\n\t" \
  "v_mfma_f32_16x16x32_bf16 v[136:139], v[56:59], v[204:207], v[136:139]\n\t" \
  "v_mfma_f32_16x16x32_bf16 v[140:143], v[60:63], v[204:207], v[140:143]\n\t" \
  "ds_read_b128 v[204:207], v211 offset:49152\n\t" \
  "global_load_lds_dwordx4 v217, s[92:93] sc1\n\t" \
  "v_cvt_pk_bf16_f32 v168, v160, v161\n\t" \
  "v_cvt_pk_bf16_f32 v169, v162, v163\n\t" \
  "v_cvt_pk_bf16_f32 v170, v164, v165\n\t" \
  "v_cvt_pk_bf16_f32 v171, v166, v167\n\t" \
  "buffer_store_dwordx4 v[168:171], v172, %[rt], s90 offen sc1\n\t" \
  "v_mfma_f32_16x16x32_bf16 v[128:131], v[64:67], v[176:179], v[128:131]\n\t" \
  "v_mfma_f32_16x16x32_bf16 v[132:135], v[68:71], v[176:179], v[132:135]\n\t" \
  "ds_read_b128 v[176:179], v208 offset:33024\n\t" \
  "v_mfma_f32_16x16x32_bf16 v[136:139], v[64:67], v[180:183], v[136:139]\n\t" \
  "v_mfma_f32_16x16x32_bf16 v[140:143], v[68:71], v[180:183], v[140:143]\n\t" \
  "ds_read_b128 v[180:183], v208 offset:49408\n\t" \
  "s_waitcnt lgkmcnt(6)\n\t" \
  "v_mfma_f32_16x16x32_bf16 v[128:131], v[72:75], v[184:187], v[128:131]\n\t" \
  "v_mfma_f32_16x16x32_bf16 v[132:135], v[76:79], v[184:187], v[132:135]\n\t" \
  "ds_read_b128 v[184:187], v209 offset:33024\n\t" \
  "s_add_u32 m0, %[lw], 100352\n\t" \
  "v_mfma_f32_16x16x32_bf16 v[136:139], v[72:75], v[188:191], v[136:139]\n\t" \
  "v_mfma_f32_16x16x32_bf16 v[140:143], v[76:79], v[188:191], v[140:143]\n\t" \
  "ds_read_b128 v[188:191], v209 offset:49408\n\t" \
  "global_load_lds_dwordx4 v218, s[92:93] sc1\n\t" \
  "s_waitcnt lgkmcnt(6)\n\t" \
  "v_mfma_f32_16x16x32_bf16 v[128:131], v[80:83], v[192:195], v[128:131]\n\t" \
  "v_mfma_f32_16x16x32_bf16 v[132:135], v[84:87], v[192:195], v[132:135]\n\t" \
  "ds_read_b128 v[192:195], v210 offset:33024\n\t" \
  "v_mfma_f32_16x16x32_bf16 v[136:139], v[80:83], v[196:199], v[136:139]\n\t" \
  "v_mfma_f32_16x16x32_bf16 v[140:143], v[84:87], v[196:199], v[140:143]\n\t" \
  "ds_read_b128 v[196:199], v210 offset:49408\n\t" \
  "s_waitcnt lgkmcnt(6)\n\t" \
  "v_mfma_f32_16x16x32_bf16 v[128:131], v[88:91], v[200:203], v[128:131]\n\t" \
  "v_mfma_f32_16x16x32_bf16 v[132:135], v[92:95], v[200:203], v[132:135]\n\t" \
  "ds_read_b128 v[200:203], v211 offset:33024\n\t" \
  "s_add_u32 m0, %[lw], 101376\n\t" \
  "v_mfma_f32_16x16x32_bf16 v[136:139], v[88:91], v[204:207], v[136:139]\n\t" \
  "v_mfma_f32_16x16x32_bf16 v[140:143], v[92:95], v[204:207], v[140:143]\n\t" \
  "ds_read_b128 v[204:207], v211 offset:49408\n\t" \
  "global_load_lds_dwordx4 v219, s[92:93] sc1\n\t" \
  "s_cmp_lt_u32 s86, 6\n\t" \
  "s_cbranch_scc1 .Lw11%=\n\t" \
  "s_waitcnt vmcnt(6)\n\t" \
  "s_barrier\n\t" \
  ".Lg11%=:\n\t" \
  "s_add_u32 s92, s92, 65024\n\t" \
  "s_addc_u32 s93, s93, 0\n\t" \
  "s_waitcnt lgkmcnt(6)\n\t" \
  "v_mfma_f32_16x16x32_bf16 v[128:131], v[96:99], v[176:179], v[128:131]\n\t" \
  "v_mfma_f32_16x16x32_bf16 v[132:135], v[100:103], v[176:179], v[132:135]\n\t" \
  "ds_read_b128 v[176:179], v212 offset:0\n\t" \
  "v_mfma_f32_16x16x32_bf16 v[136:139], v[96:99], v[180:183], v[136:139]\n\t" \
  "v_mfma_f32_16x16x32_bf16 v[140:143], v[100:103], v[180:183], v[140:143]\n\t" \
  "ds_read_b128 v[180:183], v212 offset:16384\n\t" \
  "s_waitcnt lgkmcnt(6)\n\t" \
  "v_mfma_f32_16x16x32_bf16 v[128:131], v[104:107], v[184:187], v[128:131]\n\t" \
  "v_mfma_f32_16x16x32_bf16 v[132:135], v[108:111], v[184:187], v[132:135]\n\t" \
  "ds_read_b128 v[184:187], v213 offset:0\n\t" \
  "s_add_u32 m0, %[lw], 0\n\t" \
  "v_mfma_f32_16x16x32_bf16 v[136:139], v[104:107], v[188:191], v[136:139]\n\t" \
  "v_mfma_f32_16x16x32_bf16 v[140:143], v[108:111], v[188:191], v[140:143]\n\t" \
  "ds_read_b128 v[188:191], v213 offset:16384\n\t" \
  "global_load_lds_dwordx4 v216, s[92:93] sc1\n\t" \
  "s_waitcnt lgkmcnt(6)\n\t" \
  "v_mfma_f32_16x16x32_bf16 v[128:131], v[112:115], v[192:195], v[128:131]\n\t" \
  "v_mfma_f32_16x16x32_bf16 v[132:135], v[116:119], v[192:195], v[132:135]\n\t" \
  "ds_read_b128 v[192:195], v214 offset:0\n\t" \
  "v_mfma_f32_16x16x32_bf16 v[136:139], v[112:115], v[196:199], v[136:139]\n\t" \
  "v_mfma_f32_16x16x32_bf16 v[140:143], v[116:119], v[196:199], v[140:143]\n\t" \
  "ds_read_b128 v[196:199], v214 offset:16384\n\t" \
  "s_waitcnt lgkmcnt(6)\n\t" \
  "v_mfma_f32_16x16x32_bf16 v[128:131], v[120:123], v[200:203], v[128:131]\n\t" \
  "v_mfma_f32_16x16x32_bf16 v[132:135], v[124:127], v[200:203], v[132:135]\n\t" \
  "ds_read_b128 v[200:203], v215 offset:0\n\t" \
  "s_add_u32 m0, %[lw], 1024\n\t" \
  "v_mfma_f32_16x16x32_bf16 v[136:139], v[120:123], v[204:207], v[136:139]\n\t" \
  "v_mfma_f32_16x16x32_bf16 v[140:143], v[124:127], v[204:207], v[140:143]\n\t" \
  "ds_read_b128 v[204:207], v215 offset:16384\n\t" \
  "global_load_lds_dwordx4 v217, s[92:93] sc1\n\t" \
  "s_add_u32 s86, s86, 1\n\t" \
  "s_add_u32 s87, s87, 1\n\t" \
  "s_cmp_eq_u32 s86, 1\n\t" \
  "s_cbranch_scc1 .Lf1%=\n\t" \
  ".Lfb1%=:\n\t" \
  "s_waitcnt lgkmcnt(6)\n\t" \
  "v_mfma_f32_16x16x32_bf16 v[144:147], v[0:3], v[176:179], 0\n\t" \
  "v_mfma_f32_16x16x32_bf16 v[148:151], v[4:7], v[176:179], 0\n\t" \
  "ds_read_b128 v[176:179], v212 offset:256\n\t" \
  "v_mfma_f32_16x16x32_bf16 v[152:155], v[0:3], v[180:183], 0\n\t" \
  "v_mfma_f32_16x16x32_bf16 v[156:159], v[4:7], v[180:183], 0\n\t" \
  "ds_read_b128 v[180:183], v212 offset:16640\n\t" \
  "s_waitcnt lgkmcnt(6)\n\t" \
  "v_mfma_f32_16x16x32_bf16 v[144:147], v[8:11], v[184:187], v[144:147]\n\t" \
  "v_mfma_f32_16x16x32_bf16 v[148:151], v[12:15], v[184:187], v[148:151]\n\t" \
  "ds_read_b128 v[184:187], v213 offset:256\n\t" \
  "s_add_u32 m0, %[lw], 2048\n\t" \
  "v_mfma_f32_16x16x32_bf16 v[152:155], v[8:11], v[188:191], v[152:155]\n\t" \
  "v_mfma_f32_16x16x32_bf16 v[156:159], v[12:15], v[188:191], v[156:159]\n\t" \
  "ds_read_b128 v[188:191], v213 offset:16640\n\t" \
  "global_load_lds_dwordx4 v218, s[92:93] sc1\n\t" \
  "ds_write_b128 v220, v[136:139] offset:0\n\t" \
  "ds_write_b128 v220, v[140:143] offset:1024\n\t" \
  "s_waitcnt lgkmcnt(8)\n\t" \
  "v_mfma_f32_16x16x32_bf16 v[144:147], v[16:19], v[192:195], v[144:147]\n\t" \
  "v_mfma_f32_16x16x32_bf16 v[148:151], v[20:23], v[192:195], v[148:151]\n\t" \
  "ds_read_b128 v[192:195], v214 offset:256\n\t" \
  "v_mfma_f32_16x16x32_bf16 v[152:155], v[16:19], v[196:199], v[152:155]\n\t" \
  "v_mfma_f32_16x16x32_bf16 v[156:159], v[20:23], v[196:199], v[156:159]\n\t" \
  "ds_read_b128 v[196:199], v214 offset:16640\n\t" \
  "s_lshr_b32 s90, s87, 7\n\t" \
  "s_xor_b32 s90, s90, %[s0]\n\t" \
  "s_and_b32 s90, s90, 1\n\t" \
  "s_lshl1_add_u32 s90, s87, s90\n\t" \
  "s_lshl_b32 s90, s90, 13\n\t" \
  "s_cmp_lt_i32 s86, 3\n\t" \
  "s_cselect_b32 s90, 0x70000000, s90\n\t" \
  "s_waitcnt lgkmcnt(8)\n\t" \
  "v_mfma_f32_16x16x32_bf16 v[144:147], v[24:27], v[200:203], v[144:147]\n\t" \
  "v_mfma_f32_16x16x32_bf16 v[148:151], v[28:31], v[200:203], v[148:151]\n\t" \
  "ds_read_b128 v[200:203], v215 offset:256\n\t" \
  "s_add_u32 m0, %[lw], 3072\n\t" \
  "v_mfma_f32_16x16x32_bf16 v[152:155], v[24:27], v[204:207], v[152:155]\n\t" \
  "v_mfma_f32_16x16x32_bf16 v[156:159], v[28:31], v[204:207], v[156:159]\n\t" \
  "ds_read_b128 v[204:207], v215 offset:16640\n\t" \
  "global_load_lds_dwordx4 v219, s[92:93] sc1\n\t" \
  "s_waitcnt lgkmcnt(4)\n\t" \
  "s_waitcnt vmcnt(4)\n\t" \
  "s_barrier\n\t" \
  ".Lg21%=:\n\t" \
  "s_add_u32 s92, s92, 512\n\t" \
  "s_addc_u32 s93, s93, 0\n\t" \
  "v_mfma_f32_16x16x32_bf16 v[144:147], v[32:35], v[176:179], v[144:147]\n\t" \
  "v_mfma_f32_16x16x32_bf16 v[148:151], v[36:39], v[176:179], v[148:151]\n\t" \
  "ds_read_b128 v[176:179], v212 offset:32768\n\t" \
  "v_mfma_f32_16x16x32_bf16 v[152:155], v[32:35], v[180:183], v[152:155]\n\t" \
  "v_mfma_f32_16x16x32_bf16 v[156:159], v[36:39], v[180:183], v[156:159]\n\t" \
  "ds_read_b128 v[180:183], v212 offset:49152\n\t" \
  "ds_read_b128 v[160:163], v221 offset:0\n\t" \
  "ds_read_b128 v[164:167], v221 offset:1024\n\t" \
  "s_sub_i32 s91, s86, 2\n\t" \
  "s_max_i32 s88, s88, s91\n\t" \
  "v_mov_b32 v173, s88\n\t" \
  "buffer_store_dword v173, v174, %[rs], 0 offen sc1\n\t" \
  "v_mfma_f32_16x16x32_bf16 v[144:147], v[40:43], v[184:187], v[144:147]\n\t" \
  "v_mfma_f32_16x16x32_bf16 v[148:151], v[44:47], v[184:187], v[148:151]\n\t" \
  "ds_read_b128 v[184:187], v213 offset:32768\n\t" \
  "s_add_u32 m0, %[lw], 32768\n\t" \
  "v_mfma_f32_16x16x32_bf16 v[152:155], v[40:43], v[188:191], v[152:155]\n\t" \
  "v_mfma_f32_16x16x32_bf16 v[156:159], v[44:47], v[188:191], v[156:159]\n\t" \
  "ds_read_b128 v[188:191], v213 offset:49152\n\t" \
  "global_load_lds_dwordx4 v216, s[92:93] sc1\n\t" \
  "s_waitcnt lgkmcnt(8)\n\t" \
  "v_mfma_f32_16x16x32_bf16 v[144:147], v[48:51], v[192:195], v[144:147]\n\t" \
  "v_mfma_f32_16x16x32_bf16 v[148:151], v[52:55], v[192:195], v[148:151]\n\t" \
  "ds_read_b128 v[192:195], v214 offset:32768\n\t" \
  "v_mfma_f32_16x16x32_bf16 v[152:155], v[48:51], v[196:199], v[152:155]\n\t" \
  "v_mfma_f32_16x16x32_bf16 v[156:159], v[52:55], v[196:199], v[156:159]\n\t" \
  "ds_read_b128 v[196:199], v214 offset:49152\n\t" \
  "s_waitcnt lgkmcnt(4)\n\t" \
  "v_add_f32 v160, v160, v128\n\t" \
  "v_add_f32 v161, v161, v129\n\t" \
  "v_add_f32 v162, v162, v130\n\t" \
  "v_add_f32 v163, v163, v131\n\t" \
  "v_add_f32 v164, v164, v132\n\t" \
  "v_add_f32 v165, v165, v133\n\t" \
  "v_add_f32 v166, v166, v134\n\t" \
  "v_add_f32 v167, v167, v135\n\t" \
  "v_mfma_f32_16x16x32_bf16 v[144:147], v[56:59], v[200:203], v[144:147]\n\t" \
  "v_mfma_f32_16x16x32_bf16 v[148:151], v[60:63], v[200:203], v[148:151]\n\t" \
  "ds_read_b128 v[200:203], v215 offset:32768\n\t" \
  "s_add_u32 m0, %[lw], 33792\n\t" \
  "v_mfma_f32_16x16x32_bf16 v[152:155], v[56:59], v[204:207], v[152:155]\n\t" \
  "v_mfma_f32_16x16x32_bf16 v[156:159], v[60:63], v[204:207], v[156:159]\n\t" \
  "ds_read_b128 v[204:207], v215 offset:49152\n\t" \
  "global_load_lds_dwordx4 v217, s[92:93] sc1\n\t" \
  "v_cvt_pk_bf16_f32 v168, v160, v161\n\t" \
  "v_cvt_pk_bf16_f32 v169, v162, v163\n\t" \
  "v_cvt_pk_bf16_f32 v170, v164, v165\n\t" \
  "v_cvt_pk_bf16_f32 v171, v166, v167\n\t" \
  "buffer_store_dwordx4 v[168:171], v172, %[rt], s90 offen sc1\n\t" \
  "v_mfma_f32_16x16x32_bf16 v[144:147], v[64:67], v[176:179], v[144:147]\n\t" \
  "v_mfma_f32_16x16x32_bf16 v[148:151], v[68:71], v[176:179], v[148:151]\n\t" \
  "ds_read_b128 v[176:179], v212 offset:33024\n\t" \
  "v_mfma_f32_16x16x32_bf16 v[152:155], v[64:67], v[180:183], v[152:155]\n\t" \
  "v_mfma_f32_16x16x32_bf16 v[156:159], v[68:71], v[180:183], v[156:159]\n\t" \
  "ds_read_b128 v[180:183], v212 offset:49408\n\t" \
  "s_waitcnt lgkmcnt(6)\n\t" \
  "v_mfma_f32_16x16x32_bf16 v[144:147], v[72:75], v[184:187], v[144:147]\n\t" \
  "v_mfma_f32_16x16x32_bf16 v[148:151], v[76:79], v[184:187], v[148:151]\n\t" \
  "ds_read_b128 v[184:187], v213 offset:33024\n\t" \
  "s_add_u32 m0, %[lw], 34816\n\t" \
  "v_mfma_f32_16x16x32_bf16 v[152:155], v[72:75], v[188:191], v[152:155]\n\t" \
  "v_mfma_f32_16x16x32_bf16 v[156:159], v[76:79], v[188:191], v[156:159]\n\t" \
  "ds_read_b128 v[188:191], v213 offset:49408\n\t" \
  "global_load_lds_dwordx4 v218, s[92:93] sc1\n\t" \
  "s_waitcnt lgkmcnt(6)\n\t" \
  "v_mfma_f32_16x16x32_bf16 v[144:147], v[80:83], v[192:195], v[144:147]\n\t" \
  "v_mfma_f32_16x16x32_bf16 v[148:151], v[84:87], v[192:195], v[148:151]\n\t" \
  "ds_read_b128 v[192:195], v214 offset:33024\n\t" \
  "v_mfma_f32_16x16x32_bf16 v[152:155], v[80:83], v[196:199], v[152:155]\n\t" \
  "v_mfma_f32_16x16x32_bf16 v[156:159], v[84:87], v[196:199], v[156:159]\n\t" \
  "ds_read_b128 v[196:199], v214 offset:49408\n\t" \
  "s_waitcnt lgkmcnt(6)\n\t" \
  "v_mfma_f32_16x16x32_bf16 v[144:147], v[88:91], v[200:203], v[144:147]\n\t" \
  "v_mfma_f32_16x16x32_bf16 v[148:151], v[92:95], v[200:203], v[148:151]\n\t" \
  "ds_read_b128 v[200:203], v215 offset:33024\n\t" \
  "s_add_u32 m0, %[lw], 35840\n\t" \
  "v_mfma_f32_16x16x32_bf16 v[152:155], v[88:91], v[204:207], v[152:155]\n\t" \
  "v_mfma_f32_16x16x32_bf16 v[156:159], v[92:95], v[204:207], v[156:159]\n\t" \
  "ds_read_b128 v[204:207], v215 offset:49408\n\t" \
  "global_load_lds_dwordx4 v219, s[92:93] sc1\n\t" \
  "s_cmp_lt_u32 s86, 6\n\t" \
  "s_cbranch_scc1 .Lw31%=\n\t" \
  "s_waitcnt vmcnt(6)\n\t" \
  "s_barrier\n\t" \
  ".Lg31%=:\n\t" \
  "s_add_u32 s92, s92, 65024\n\t" \
  "s_addc_u32 s93, s93, 0\n\t" \
  "s_waitcnt lgkmcnt(6)\n\t" \
  "v_mfma_f32_16x16x32_bf16 v[144:147], v[96:99], v[176:179], v[144:147]\n\t" \
  "v_mfma_f32_16x16x32_bf16 v[148:151], v[100:103], v[176:179], v[148:151]\n\t" \
  "ds_read_b128 v[176:179], v208 offset:0\n\t" \
  "v_mfma_f32_16x16x32_bf16 v[152:155], v[96:99], v[180:183], v[152:155]\n\t" \
  "v_mfma_f32_16x16x32_bf16 v[156:159], v[100:103], v[180:183], v[156:159]\n\t" \
  "ds_read_b128 v[180:183], v208 offset:16384\n\t" \
  "s_waitcnt lgkmcnt(6)\n\t" \
  "v_mfma_f32_16x16x32_bf16 v[144:147], v[104:107], v[184:187], v[144:147]\n\t" \
  "v_mfma_f32_16x16x32_bf16 v[148:151], v[108:111], v[184:187], v[148:151]\n\t" \
  "ds_read_b128 v[184:187], v209 offset:0\n\t" \
  "s_add_u32 m0, %[lw], 65536\n\t" \
  "v_mfma_f32_16x16x32_bf16 v[152:155], v[104:107], v[188:191], v[152:155]\n\t" \
  "v_mfma_f32_16x16x32_bf16 v[156:159], v[108:111], v[188:191], v[156:159]\n\t" \
  "ds_read_b128 v[188:191], v209 offset:16384\n\t" \
  "global_load_lds_dwordx4 v216, s[92:93] sc1\n\t" \
  "s_waitcnt lgkmcnt(6)\n\t" \
  "v_mfma_f32_16x16x32_bf16 v[144:147], v[112:115], v[192:195], v[144:147]\n\t" \
  "v_mfma_f32_16x16x32_bf16 v[148:151], v[116:119], v[192:195], v[148:151]\n\t" \
  "ds_read_b128 v[192:195], v210 offset:0\n\t" \
  "v_mfma_f32_16x16x32_bf16 v[152:155], v[112:115], v[196:199], v[152:155]\n\t" \
  "v_mfma_f32_16x16x32_bf16 v[156:159], v[116:119], v[196:199], v[156:159]\n\t" \
  "ds_read_b128 v[196:199], v210 offset:16384\n\t" \
  "s_waitcnt lgkmcnt(6)\n\t" \
  "v_mfma_f32_16x16x32_bf16 v[144:147], v[120:123], v[200:203], v[144:147]\n\t" \
  "v_mfma_f32_16x16x32_bf16 v[148:151], v[124:127], v[200:203], v[148:151]\n\t" \
  "ds_read_b128 v[200:203], v211 offset:0\n\t" \
  "s_add_u32 m0, %[lw], 66560\n\t" \
  "v_mfma_f32_16x16x32_bf16 v[152:155], v[120:123], v[204:207], v[152:155]\n\t" \
  "v_mfma_f32_16x16x32_bf16 v[156:159], v[124:127], v[204:207], v[156:159]\n\t" \
  "ds_read_b128 v[204:207], v211 offset:16384\n\t" \
  "global_load_lds_dwordx4 v217, s[92:93] sc1\n\t" \
  "s_add_u32 s86, s86, 1\n\t" \
  "s_add_u32 s87, s87, 1\n\t" \
  "s_cmp_eq_u32 s86, 2\n\t" \
  "s_cbranch_scc1 .Ll1%=\n\t" \
  "s_cmp_lt_u32 s86, %[end]\n\t" \
  "s_cbranch_scc1 .Lp1%=\n\t" \
  "s_cmp_lg_u32 s86, 64\n\t" \
  "s_cbranch_scc1 .Lx1%=\n\t" \
  ".Ll1%=:\n\t" \
  "s_nop 7\n\t" \
  "ds_write_b128 v220, v[152:155] offset:0\n\t" \
  "ds_write_b128 v220, v[156:159] offset:1024\n\t" \
  "s_lshr_b32 s90, s87, 7\n\t" \
  "s_xor_b32 s90, s90, %[s0]\n\t" \
  "s_and_b32 s90, s90, 1\n\t" \
  "s_lshl1_add_u32 s90, s87, s90\n\t" \
  "s_lshl_b32 s90, s90, 13\n\t" \
  "s_cmp_lt_i32 s86, 1\n\t" \
  "s_cselect_b32 s90, 0x70000000, s90\n\t" \
  "s_waitcnt lgkmcnt(0)\n\t" \
  "s_barrier\n\t" \
  "ds_read_b128 v[160:163], v221 offset:0\n\t" \
  "ds_read_b128 v[164:167], v221 offset:1024\n\t" \
  "s_waitcnt lgkmcnt(0)\n\t" \
  "v_add_f32 v160, v160, v144\n\t" \
  "v_add_f32 v161, v161, v145\n\t" \
  "v_add_f32 v162, v162, v146\n\t" \
  "v_add_f32 v163, v163, v147\n\t" \
  "v_add_f32 v164, v164, v148\n\t" \
  "v_add_f32 v165, v165, v149\n\t" \
  "v_add_f32 v166, v166, v150\n\t" \
  "v_add_f32 v167, v167, v151\n\t" \
  "v_cvt_pk_bf16_f32 v168, v160, v161\n\t" \
  "v_cvt_pk_bf16_f32 v169, v162, v163\n\t" \
  "v_cvt_pk_bf16_f32 v170, v164, v165\n\t" \
  "v_cvt_pk_bf16_f32 v171, v166, v167\n\t" \
  "buffer_store_dwordx4 v[168:171], v172, %[rt], s90 offen sc1\n\t" \
  "s_waitcnt vmcnt(0)\n\t" \
  "s_barrier\n\t" \
  "s_sub_i32 s91, s86, 1\n\t" \
  "s_max_i32 s88, s88, s91\n\t" \
  "v_mov_b32 v173, s88\n\t" \
  "buffer_store_dword v173, v174, %[rs], 0 offen sc1\n\t" \
  "s_cmp_eq_u32 s86, 2\n\t" \
  "s_cbranch_scc1 .Lp1%=\n\t" \
  "s_branch .Lx1%=\n\t" \
  ".Lf1%=:\n\t" \
  "s_nop 7\n\t" \
  "ds_write_b128 v220, v[136:139] offset:0\n\t" \
  "ds_write_b128 v220, v[140:143] offset:1024\n\t" \
  "s_lshr_b32 s90, s87, 7\n\t" \
  "s_xor_b32 s90, s90, %[s0]\n\t" \
  "s_and_b32 s90, s90, 1\n\t" \
  "s_lshl1_add_u32 s90, s87, s90\n\t" \
  "s_lshl_b32 s90, s90, 13\n\t" \
  "s_cmp_lt_i32 s86, 1\n\t" \
  "s_cselect_b32 s90, 0x70000000, s90\n\t" \
  "s_waitcnt lgkmcnt(0)\n\t" \
  "s_barrier\n\t" \
  "ds_read_b128 v[160:163], v221 offset:0\n\t" \
  "ds_read_b128 v[164:167], v221 offset:1024\n\t" \
  "s_waitcnt lgkmcnt(0)\n\t" \
  "v_add_f32 v160, v160, v128\n\t" \
  "v_add_f32 v161, v161, v129\n\t" \
  "v_add_f32 v162, v162, v130\n\t" \
  "v_add_f32 v163, v163, v131\n\t" \
  "v_add_f32 v164, v164, v132\n\t" \
  "v_add_f32 v165, v165, v133\n\t" \
  "v_add_f32 v166, v166, v134\n\t" \
  "v_add_f32 v167, v167, v135\n\t" \
  "v_cvt_pk_bf16_f32 v168, v160, v161\n\t" \
  "v_cvt_pk_bf16_f32 v169, v162, v163\n\t" \
  "v_cvt_pk_bf16_f32 v170, v164, v165\n\t" \
  "v_cvt_pk_bf16_f32 v171, v166, v167\n\t" \
  "buffer_store_dwordx4 v[168:171], v172, %[rt], s90 offen sc1\n\t" \
  "s_waitcnt vmcnt(0)\n\t" \
  "s_barrier\n\t" \
  "s_sub_i32 s91, s86, 1\n\t" \
  "s_max_i32 s88, s88, s91\n\t" \
  "v_mov_b32 v173, s88\n\t" \
  "buffer_store_dword v173, v174, %[rs], 0 offen sc1\n\t" \
  "s_branch .Lfb1%=\n\t" \
  ".Lw11%=:\n\t" \
  "s_waitcnt vmcnt(2)\n\t" \
  "s_barrier\n\t" \
  "s_sub_i32 s91, s86, 1\n\t" \
  "s_max_i32 s88, s88, s91\n\t" \
  "v_mov_b32 v173, s88\n\t" \
  "buffer_store_dword v173, v174, %[rs], 0 offen sc1\n\t" \
  "s_branch .Lg11%=\n\t" \
  ".Lw31%=:\n\t" \
  "s_waitcnt vmcnt(2)\n\t" \
  "s_barrier\n\t" \
  "s_sub_i32 s91, s86, 1\n\t" \
  "s_max_i32 s88, s88, s91\n\t" \
  "v_mov_b32 v173, s88\n\t" \
  "buffer_store_dword v173, v174, %[rs], 0 offen sc1\n\t" \
  "s_branch .Lg31%=\n\t" \
  ".Lx1%=:\n\t" \
  "s_waitcnt lgkmcnt(0)\n\t" \
  "s_setprio 0\n\t" \
  "s_mov_b32 m0, s94\n\t" \
  ".Le%=:\n\t"
#define PR_ASM_1 \
  "s_cmp_lg_u32 %[kh], 0\n\t" \
  "s_cbranch_scc1 .Lk%=\n\t" \
  "s_mov_b32 s94, m0\n\t" \
  "s_mov_b64 s[92:93], %[x0]\n\t" \
  "s_mov_b32 s86, %[g0]\n\t" \
  "s_add_u32 s87, s86, %[t0]\n\t" \
  "s_sub_i32 s88, s86, 3\n\t" \
  "ds_read_b128 v[176:179], v208 offset:0\n\t" \
  "ds_read_b128 v[180:183], v208 offset:16384\n\t" \
  "ds_read_b128 v[184:187], v209 offset:0\n\t" \
  "ds_read_b128 v[188:191], v209 offset:16384\n\t" \
  "ds_read_b128 v[192:195], v210 offset:0\n\t" \
  "ds_read_b128 v[196:199], v210 offset:16384\n\t" \
  "ds_read_b128 v[200:203], v211 offset:0\n\t" \
  "ds_read_b128 v[204:207], v211 offset:16384\n\t" \
  ".Lp0%=:\n\t" \
  "s_waitcnt lgkmcnt(6)\n\t" \
  "v_mfma_f32_16x16x32_bf16 v[128:131], v[176:179], v[0:3], 0\n\t" \
  "v_mfma_f32_16x16x32_bf16 v[136:139], v[176:179], v[4:7], 0\n\t" \
  "ds_read_b128 v[176:179], v208 offset:256\n\t" \
  "s_add_u32 m0, %[lw], 67584\n\t" \
  "v_mfma_f32_16x16x32_bf16 v[132:135], v[180:183], v[0:3], 0\n\t" \
  "v_mfma_f32_16x16x32_bf16 v[140:143], v[180:183], v[4:7], 0\n\t" \
  "ds_read_b128 v[180:183], v208 offset:16640\n\t" \
  "global_load_lds_dwordx4 v218, s[92:93] sc1\n\t" \
  "s_waitcnt lgkmcnt(6)\n\t" \
  "v_mfma_f32_16x16x32_bf16 v[128:131], v[184:187], v[8:11], v[128:131]\n\t" \
  "v_mfma_f32_16x16x32_bf16 v[136:139], v[184:187], v[12:15], v[136:139]\n\t" \
  "ds_read_b128 v[184:187], v209 offset:256\n\t" \
  "v_mfma_f32_16x16x32_bf16 v[132:135], v[188:191], v[8:11], v[132:135]\n\t" \
  "v_mfma_f32_16x16x32_bf16 v[140:143], v[188:191], v[12:15], v[140:143]\n\t" \
  "ds_read_b128 v[188:191], v209 offset:16640\n\t" \
  "ds_write_b128 v220, v[152:155] offset:0\n\t" \
  "ds_write_b128 v220, v[156:159] offset:1024\n\t" \
  "s_waitcnt lgkmcnt(8)\n\t" \
  "v_mfma_f32_16x16x32_bf16 v[128:131], v[192:195], v[16:19], v[128:131]\n\t" \
  "v_mfma_f32_16x16x32_bf16 v[136:139], v[192:195], v[20:23], v[136:139]\n\t" \
  "ds_read_b128 v[192:195], v210 offset:256\n\t" \
  "s_add_u32 m0, %[lw], 68608\n\t" \
  "v_mfma_f32_16x16x32_bf16 v[132:135], v[196:199], v[16:19], v[132:135]\n\t" \
  "v_mfma_f32_16x16x32_bf16 v[140:143], v[196:199], v[20:23], v[140:143]\n\t" \
  "ds_read_b128 v[196:199], v210 offset:16640\n\t" \
  "global_load_lds_dwordx4 v219, s[92:93] sc1\n\t" \
  "s_lshr_b32 s90, s87, 7\n\t" \
  "s_xor_b32 s90, s90, %[s0]\n\t" \
  "s_and_b32 s90, s90, 1\n\t" \
  "s_lshl1_add_u32 s90, s87, s90\n\t" \
  "s_lshl_b32 s90, s90, 13\n\t" \
  "s_cmp_lt_i32 s86, 3\n\t" \
  "s_cselect_b32 s90, 0x70000000, s90\n\t" \
  "s_waitcnt lgkmcnt(8)\n\t" \
  "v_mfma_f32_16x16x32_bf16 v[128:131], v[200:203], v[24:27], v[128:131]\n\t" \
  "v_mfma_f32_16x16x32_bf16 v[136:139], v[200:203], v[28:31], v[136:139]\n\t" \
  "ds_read_b128 v[200:203], v211 offset:256\n\t" \
  "v_mfma_f32_16x16x32_bf16 v[132:135], v[204:207], v[24:27], v[132:135]\n\t" \
  "v_mfma_f32_16x16x32_bf16 v[140:143], v[204:207], v[28:31], v[140:143]\n\t" \
  "ds_read_b128 v[204:207], v211 offset:16640\n\t" \
  "s_waitcnt lgkmcnt(4)\n\t" \
  "s_waitcnt vmcnt(4)\n\t" \
  "s_barrier\n\t" \
  ".Lg00%=:\n\t" \
  "s_add_u32 s92, s92, 512\n\t" \
  "s_addc_u32 s93, s93, 0\n\t" \
  "v_mfma_f32_16x16x32_bf16 v[128:131], v[176:179], v[32:35], v[128:131]\n\t" \
  "v_mfma_f32_16x16x32_bf16 v[136:139], v[176:179], v[36:39], v[136:139]\n\t" \
  "ds_read_b128 v[176:179], v208 offset:32768\n\t" \
  "s_add_u32 m0, %[lw], 98304\n\t" \
  "v_mfma_f32_16x16x32_bf16 v[132:135], v[180:183], v[32:35], v[132:135]\n\t" \
  "v_mfma_f32_16x16x32_bf16 v[140:143], v[180:183], v[36:39], v[140:143]\n\t" \
  "ds_read_b128 v[180:183], v208 offset:49152\n\t" \
  "global_load_lds_dwordx4 v216, s[92:93] sc1\n\t" \
  "ds_read_b128 v[160:163], v221 offset:0\n\t" \
  "ds_read_b128 v[164:167], v221 offset:1024\n\t" \
  "v_mfma_f32_16x16x32_bf16 v[128:131], v[184:187], v[40:43], v[128:131]\n\t" \
  "v_mfma_f32_16x16x32_bf16 v[136:139], v[184:187], v[44:47], v[136:139]\n\t" \
  "ds_read_b128 v[184:187], v209 offset:32768\n\t" \
  "v_mfma_f32_16x16x32_bf16 v[132:135], v[188:191], v[40:43], v[132:135]\n\t" \
  "v_mfma_f32_16x16x32_bf16 v[140:143], v[188:191], v[44:47], v[140:143]\n\t" \
  "ds_read_b128 v[188:191], v209 offset:49152\n\t" \
  "s_waitcnt lgkmcnt(8)\n\t" \
  "v_mfma_f32_16x16x32_bf16 v[128:131], v[192:195], v[48:51], v[128:131]\n\t" \
  "v_mfma_f32_16x16x32_bf16 v[136:139], v[192:195], v[52:55], v[136:139]\n\t" \
  "ds_read_b128 v[192:195], v210 offset:32768\n\t" \
  "s_add_u32 m0, %[lw], 99328\n\t" \
  "v_mfma_f32_16x16x32_bf16 v[132:135], v[196:199], v[48:51], v[132:135]\n\t" \
  "v_mfma_f32_16x16x32_bf16 v[140:143], v[196:199], v[52:55], v[140:143]\n\t" \
  "ds_read_b128 v[196:199], v210 offset:49152\n\t" \
  "global_load_lds_dwordx4 v217, s[92:93] sc1\n\t" \
  "s_waitcnt lgkmcnt(4)\n\t" \
  "v_add_f32 v160, v160, v144\n\t" \
  "v_add_f32 v161, v161, v145\n\t" \
  "v_add_f32 v162, v162, v146\n\t" \
  "v_add_f32 v163, v163, v147\n\t" \
  "v_add_f32 v164, v164, v148\n\t" \
  "v_add_f32 v165, v165, v149\n\t" \
  "v_add_f32 v166, v166, v150\n\t" \
  "v_add_f32 v167, v167, v151\n\t" \
  "v_mfma_f32_16x16x32_bf16 v[128:131], v[200:203], v[56:59], v[128:131]\n\t" \
  "v_mfma_f32_16x16x32_bf16 v[136:139], v[200:203], v[60:63], v[136:139]\n\t" \
  "ds_read_b128 v[200:203], v211 offset:32768\n\t" \
  "v_mfma_f32_16x16x32_bf16 v[132:135], v[204:207], v[56:59], v[132:135]\n\t" \
  "v_mfma_f32_16x16x32_bf16 v[140:143], v[204:207], v[60:63], v[140:143]\n\t" \
  "ds_read_b128 v[204:207], v211 offset:49152\n\t" \
  "v_cvt_pk_bf16_f32 v168, v160, v161\n\t" \
  "v_cvt_pk_bf16_f32 v169, v162, v163\n\t" \
  "v_cvt_pk_bf16_f32 v170, v164, v165\n\t" \
  "v_cvt_pk_bf16_f32 v171, v166, v167\n\t" \
  "buffer_store_dwordx4 v[168:171], v172, %[rt], s90 offen sc1\n\t" \
  "v_mfma_f32_16x16x32_bf16 v[128:131], v[176:179], v[64:67], v[128:131]\n\t" \
  "v_mfma_f32_16x16x32_bf16 v[136:139], v[176:179], v[68:71], v[136:139]\n\t" \
  "ds_read_b128 v[176:179], v208 offset:33024\n\t" \
  "s_add_u32 m0, %[lw], 100352\n\t" \
  "v_mfma_f32_16x16x32_bf16 v[132:135], v[180:183], v[64:67], v[132:135]\n\t" \
  "v_mfma_f32_16x16x32_bf16 v[140:143], v[180:183], v[68:71], v[140:143]\n\t" \
  "ds_read_b128 v[180:183], v208 offset:49408\n\t" \
  "global_load_lds_dwordx4 v218, s[92:93] sc1\n\t" \
  "s_waitcnt lgkmcnt(6)\n\t" \
  "v_mfma_f32_16x16x32_bf16 v[128:131], v[184:187], v[72:75], v[128:131]\n\t" \
  "v_mfma_f32_16x16x32_bf16 v[136:139], v[184:187], v[76:79], v[136:139]\n\t" \
  "ds_read_b128 v[184:187], v209 offset:33024\n\t" \
  "v_mfma_f32_16x16x32_bf16 v[132:135], v[188:191], v[72:75], v[132:135]\n\t" \
  "v_mfma_f32_16x16x32_bf16 v[140:143], v[188:191], v[76:79], v[140:143]\n\t" \
  "ds_read_b128 v[188:191], v209 offset:49408\n\t" \
  "s_waitcnt lgkmcnt(6)\n\t" \
  "v_mfma_f32_16x16x32_bf16 v[128:131], v[192:195], v[80:83], v[128:131]\n\t" \
  "v_mfma_f32_16x16x32_bf16 v[136:139], v[192:195], v[84:87], v[136:139]\n\t" \
  "ds_read_b128 v[192:195], v210 offset:33024\n\t" \
  "s_add_u32 m0, %[lw], 101376\n\t" \
  "v_mfma_f32_16x16x32_bf16 v[132:135], v[196:199], v[80:83], v[132:135]\n\t" \
  "v_mfma_f32_16x16x32_bf16 v[140:143], v[196:199], v[84:87], v[140:143]\n\t" \
  "ds_read_b128 v[196:199], v210 offset:49408\n\t" \
  "global_load_lds_dwordx4 v219, s[92:93] sc1\n\t" \
  "s_waitcnt lgkmcnt(6)\n\t" \
  "v_mfma_f32_16x16x32_bf16 v[128:131], v[200:203], v[88:91], v[128:131]\n\t" \
  "v_mfma_f32_16x16x32_bf16 v[136:139], v[200:203], v[92:95], v[136:139]\n\t" \
  "ds_read_b128 v[200:203], v211 offset:33024\n\t" \
  "v_mfma_f32_16x16x32_bf16 v[132:135], v[204:207], v[88:91], v[132:135]\n\t" \
  "v_mfma_f32_16x16x32_bf16 v[140:143], v[204:207], v[92:95], v[140:143]\n\t" \
  "ds_read_b128 v[204:207], v211 offset:49408\n\t" \
  "s_cmp_lt_u32 s86, 6\n\t" \
  "s_cbranch_scc1 .Lw10%=\n\t" \
  "s_waitcnt vmcnt(5)\n\t" \
  "s_barrier\n\t" \
  ".Lg10%=:\n\t" \
  "s_add_u32 s92, s92, 65024\n\t" \
  "s_addc_u32 s93, s93, 0\n\t" \
  "s_waitcnt lgkmcnt(6)\n\t" \
  "v_mfma_f32_16x16x32_bf16 v[128:131], v[176:179], v[96:99], v[128:131]\n\t" \
  "v_mfma_f32_16x16x32_bf16 v[136:139], v[176:179], v[100:103], v[136:139]\n\t" \
  "ds_read_b128 v[176:179], v212 offset:0\n\t" \
  "s_add_u32 m0, %[lw], 0\n\t" \
  "v_mfma_f32_16x16x32_bf16 v[132:135], v[180:183], v[96:99], v[132:135]\n\t" \
  "v_mfma_f32_16x16x32_bf16 v[140:143], v[180:183], v[100:103], v[140:143]\n\t" \
  "ds_read_b128 v[180:183], v212 offset:16384\n\t" \
  "global_load_lds_dwordx4 v216, s[92:93] sc1\n\t" \
  "s_waitcnt lgkmcnt(6)\n\t" \
  "v_mfma_f32_16x16x32_bf16 v[128:131], v[184:187], v[104:107], v[128:131]\n\t" \
  "v_mfma_f32_16x16x32_bf16 v[136:139], v[184:187], v[108:111], v[136:139]\n\t" \
  "ds_read_b128 v[184:187], v213 offset:0\n\t" \
  "v_mfma_f32_16x16x32_bf16 v[132:135], v[188:191], v[104:107], v[132:135]\n\t" \
  "v_mfma_f32_16x16x32_bf16 v[140:143], v[188:191], v[108:111], v[140:143]\n\t" \
  "ds_read_b128 v[188:191], v213 offset:16384\n\t" \
  "s_waitcnt lgkmcnt(6)\n\t" \
  "v_mfma_f32_16x16x32_bf16 v[128:131], v[192:195], v[112:115], v[128:131]\n\t" \
  "v_mfma_f32_16x16x32_bf16 v[136:139], v[192:195], v[116:119], v[136:139]\n\t" \
  "ds_read_b128 v[192:195], v214 offset:0\n\t" \
  "s_add_u32 m0, %[lw], 1024\n\t" \
  "v_mfma_f32_16x16x32_bf16 v[132:135], v[196:199], v[112:115], v[132:135]\n\t" \
  "v_mfma_f32_16x16x32_bf16 v[140:143], v[196:199], v[116:119], v[140:143]\n\t" \
  "ds_read_b128 v[196:199], v214 offset:16384\n\t" \
  "global_load_lds_dwordx4 v217, s[92:93] sc1\n\t" \
  "s_waitcnt lgkmcnt(6)\n\t" \
  "v_mfma_f32_16x16x32_bf16 v[128:131], v[200:203], v[120:123], v[128:131]\n\t" \
  "v_mfma_f32_16x16x32_bf16 v[136:139], v[200:203], v[124:127], v[136:139]\n\t" \
  "ds_read_b128 v[200:203], v215 offset:0\n\t" \
  "v_mfma_f32_16x16x32_bf16 v[132:135], v[204:207], v[120:123], v[132:135]\n\t" \
  "v_mfma_f32_16x16x32_bf16 v[140:143], v[204:207], v[124:127], v[140:143]\n\t" \
  "ds_read_b128 v[204:207], v215 offset:16384\n\t" \
  "s_add_u32 s86, s86, 1\n\t" \
  "s_add_u32 s87, s87, 1\n\t" \
  "s_cmp_eq_u32 s86, 1\n\t" \
  "s_cbranch_scc1 .Lf0%=\n\t" \
  ".Lfb0%=:\n\t" \
  "s_waitcnt lgkmcnt(6)\n\t" \
  "v_mfma_f32_16x16x32_bf16 v[144:147], v[176:179], v[0:3], 0\n\t" \
  "v_mfma_f32_16x16x32_bf16 v[152:155], v[176:179], v[4:7], 0\n\t" \
  "ds_read_b128 v[176:179], v212 offset:256\n\t" \
  "s_add_u32 m0, %[lw], 2048\n\t" \
  "v_mfma_f32_16x16x32_bf16 v[148:151], v[180:183], v[0:3], 0\n\t" \
  "v_mfma_f32_16x16x32_bf16 v[156:159], v[180:183], v[4:7], 0\n\t" \
  "ds_read_b128 v[180:183], v212 offset:16640\n\t" \
  "global_load_lds_dwordx4 v218, s[92:93] sc1\n\t" \
  "s_waitcnt lgkmcnt(6)\n\t" \
  "v_mfma_f32_16x16x32_bf16 v[144:147], v[184:187], v[8:11], v[144:147]\n\t" \
  "v_mfma_f32_16x16x32_bf16 v[152:155], v[184:187], v[12:15], v[152:155]\n\t" \
  "ds_read_b128 v[184:187], v213 offset:256\n\t" \
  "v_mfma_f32_16x16x32_bf16 v[148:151], v[188:191], v[8:11], v[148:151]\n\t" \
  "v_mfma_f32_16x16x32_bf16 v[156:159], v[188:191], v[12:15], v[156:159]\n\t" \
  "ds_read_b128 v[188:191], v213 offset:16640\n\t" \
  "ds_write_b128 v220, v[136:139] offset:0\n\t" \
  "ds_write_b128 v220, v[140:143] offset:1024\n\t" \
  "s_waitcnt lgkmcnt(8)\n\t" \
  "v_mfma_f32_16x16x32_bf16 v[144:147], v[192:195], v[16:19], v[144:147]\n\t" \
  "v_mfma_f32_16x16x32_bf16 v[152:155], v[192:195], v[20:23], v[152:155]\n\t" \
  "ds_read_b128 v[192:195], v214 offset:256\n\t" \
  "s_add_u32 m0, %[lw], 3072\n\t" \
  "v_mfma_f32_16x16x32_bf16 v[148:151], v[196:199], v[16:19], v[148:151]\n\t" \
  "v_mfma_f32_16x16x32_bf16 v[156:159], v[196:199], v[20:23], v[156:159]\n\t" \
  "ds_read_b128 v[196:199], v214 offset:16640\n\t" \
  "global_load_lds_dwordx4 v219, s[92:93] sc1\n\t" \
  "s_lshr_b32 s90, s87, 7\n\t" \
  "s_xor_b32 s90, s90, %[s0]\n\t" \
  "s_and_b32 s90, s90, 1\n\t" \
  "s_lshl1_add_u32 s90, s87, s90\n\t" \
  "s_lshl_b32 s90, s90, 13\n\t" \
  "s_cmp_lt_i32 s86, 3\n\t" \
  "s_cselect_b32 s90, 0x70000000, s90\n\t" \
  "s_waitcnt lgkmcnt(8)\n\t" \
  "v_mfma_f32_16x16x32_bf16 v[144:147], v[200:203], v[24:27], v[144:147]\n\t" \
  "v_mfma_f32_16x16x32_bf16 v[152:155], v[200:203], v[28:31], v[152:155]\n\t" \
  "ds_read_b128 v[200:203], v215 offset:256\n\t" \
  "v_mfma_f32_16x16x32_bf16 v[148:151], v[204:207], v[24:27], v[148:151]\n\t" \
  "v_mfma_f32_16x16x32_bf16 v[156:159], v[204:207], v[28:31], v[156:159]\n\t" \
  "ds_read_b128 v[204:207], v215 offset:16640\n\t" \
  "s_waitcnt lgkmcnt(4)\n\t" \
  "s_waitcnt vmcnt(4)\n\t" \
  "s_barrier\n\t" \
  ".Lg20%=:\n\t" \
  "s_add_u32 s92, s92, 512\n\t" \
  "s_addc_u32 s93, s93, 0\n\t" \
  "v_mfma_f32_16x16x32_bf16 v[144:147], v[176:179], v[32:35], v[144:147]\n\t" \
  "v_mfma_f32_16x16x32_bf16 v[152:155], v[176:179], v[36:39], v[152:155]\n\t" \
  "ds_read_b128 v[176:179], v212 offset:32768\n\t" \
  "s_add_u32 m0, %[lw], 32768\n\t" \
  "v_mfma_f32_16x16x32_bf16 v[148:151], v[180:183], v[32:35], v[148:151]\n\t" \
  "v_mfma_f32_16x16x32_bf16 v[156:159], v[180:183], v[36:39], v[156:159]\n\t" \
  "ds_read_b128 v[180:183], v212 offset:49152\n\t" \
  "global_load_lds_dwordx4 v216, s[92:93] sc1\n\t" \
  "ds_read_b128 v[160:163], v221 offset:0\n\t" \
  "ds_read_b128 v[164:167], v221 offset:1024\n\t" \
  "v_mfma_f32_16x16x32_bf16 v[144:147], v[184:187], v[40:43], v[144:147]\n\t" \
  "v_mfma_f32_16x16x32_bf16 v[152:155], v[184:187], v[44:47], v[152:155]\n\t" \
  "ds_read_b128 v[184:187], v213 offset:32768\n\t" \
  "v_mfma_f32_16x16x32_bf16 v[148:151], v[188:191], v[40:43], v[148:151]\n\t" \
  "v_mfma_f32_16x16x32_bf16 v[156:159], v[188:191], v[44:47], v[156:159]\n\t" \
  "ds_read_b128 v[188:191], v213 offset:49152\n\t" \
  "s_waitcnt lgkmcnt(8)\n\t" \
  "v_mfma_f32_16x16x32_bf16 v[144:147], v[192:195], v[48:51], v[144:147]\n\t" \
  "v_mfma_f32_16x16x32_bf16 v[152:155], v[192:195], v[52:55], v[152:155]\n\t" \
  "ds_read_b128 v[192:195], v214 offset:32768\n\t" \
  "s_add_u32 m0, %[lw], 33792\n\t" \
  "v_mfma_f32_16x16x32_bf16 v[148:151], v[196:199], v[48:51], v[148:151]\n\t" \
  "v_mfma_f32_16x16x32_bf16 v[156:159], v[196:199], v[52:55], v[156:159]\n\t" \
  "ds_read_b128 v[196:199], v214 offset:49152\n\t" \
  "global_load_lds_dwordx4 v217, s[92:93] sc1\n\t" \
  "s_waitcnt lgkmcnt(4)\n\t" \
  "v_add_f32 v160, v160, v128\n\t" \
  "v_add_f32 v161, v161, v129\n\t" \
  "v_add_f32 v162, v162, v130\n\t" \
  "v_add_f32 v163, v163, v131\n\t" \
  "v_add_f32 v164, v164, v132\n\t" \
  "v_add_f32 v165, v165, v133\n\t" \
  "v_add_f32 v166, v166, v134\n\t" \
  "v_add_f32 v167, v167, v135\n\t" \
  "v_mfma_f32_16x16x32_bf16 v[144:147], v[200:203], v[56:59], v[144:147]\n\t" \
  "v_mfma_f32_16x16x32_bf16 v[152:155], v[200:203], v[60:63], v[152:155]\n\t" \
  "ds_read_b128 v[200:203], v215 offset:32768\n\t" \
  "v_mfma_f32_16x16x32_bf16 v[148:151], v[204:207], v[56:59], v[148:151]\n\t" \
  "v_mfma_f32_16x16x32_bf16 v[156:159], v[204:207], v[60:63], v[156:159]\n\t" \
  "ds_read_b128 v[204:207], v215 offset:49152\n\t" \
  "v_cvt_pk_bf16_f32 v168, v160, v161\n\t" \
  "v_cvt_pk_bf16_f32 v169, v162, v163\n\t" \
  "v_cvt_pk_bf16_f32 v170, v164, v165\n\t" \
  "v_cvt_pk_bf16_f32 v171, v166, v167\n\t" \
  "buffer_store_dwordx4 v[168:171], v172, %[rt], s90 offen sc1\n\t" \
  "v_mfma_f32_16x16x32_bf16 v[144:147], v[176:179], v[64:67], v[144:147]\n\t" \
  "v_mfma_f32_16x16x32_bf16 v[152:155], v[176:179], v[68:71], v[152:155]\n\t" \
  "ds_read_b128 v[176:179], v212 offset:33024\n\t" \
  "s_add_u32 m0, %[lw], 34816\n\t" \
  "v_mfma_f32_16x16x32_bf16 v[148:151], v[180:183], v[64:67], v[148:151]\n\t" \
  "v_mfma_f32_16x16x32_bf16 v[156:159], v[180:183], v[68:71], v[156:159]\n\t" \
  "ds_read_b128 v[180:183], v212 offset:49408\n\t" \
  "global_load_lds_dwordx4 v218, s[92:93] sc1\n\t" \
  "s_waitcnt lgkmcnt(6)\n\t" \
  "v_mfma_f32_16x16x32_bf16 v[144:147], v[184:187], v[72:75], v[144:147]\n\t" \
  "v_mfma_f32_16x16x32_bf16 v[152:155], v[184:187], v[76:79], v[152:155]\n\t" \
  "ds_read_b128 v[184:187], v213 offset:33024\n\t" \
  "v_mfma_f32_16x16x32_bf16 v[148:151], v[188:191], v[72:75], v[148:151]\n\t" \
  "v_mfma_f32_16x16x32_bf16 v[156:159], v[188:191], v[76:79], v[156:159]\n\t" \
  "ds_read_b128 v[188:191], v213 offset:49408\n\t" \
  "s_waitcnt lgkmcnt(6)\n\t" \
  "v_mfma_f32_16x16x32_bf16 v[144:147], v[192:195], v[80:83], v[144:147]\n\t" \
  "v_mfma_f32_16x16x32_bf16 v[152:155], v[192:195], v[84:87], v[152:155]\n\t" \
  "ds_read_b128 v[192:195], v214 offset:33024\n\t" \
  "s_add_u32 m0, %[lw], 35840\n\t" \
  "v_mfma_f32_16x16x32_bf16 v[148:151], v[196:199], v[80:83], v[148:151]\n\t" \
  "v_mfma_f32_16x16x32_bf16 v[156:159], v[196:199], v[84:87], v[156:159]\n\t" \
  "ds_read_b128 v[196:199], v214 offset:49408\n\t" \
  "global_load_lds_dwordx4 v219, s[92:93] sc1\n\t" \
  "s_waitcnt lgkmcnt(6)\n\t" \
  "v_mfma_f32_16x16x32_bf16 v[144:147], v[200:203], v[88:91], v[144:147]\n\t" \
  "v_mfma_f32_16x16x32_bf16 v[152:155], v[200:203], v[92:95], v[152:155]\n\t" \
  "ds_read_b128 v[200:203], v215 offset:33024\n\t" \
  "v_mfma_f32_16x16x32_bf16 v[148:151], v[204:207], v[88:91], v[148:151]\n\t" \
  "v_mfma_f32_16x16x32_bf16 v[156:159], v[204:207], v[92:95], v[156:159]\n\t" \
  "ds_read_b128 v[204:207], v215 offset:49408\n\t" \
  "s_cmp_lt_u32 s86, 6\n\t" \
  "s_cbranch_scc1 .Lw30%=\n\t" \
  "s_waitcnt vmcnt(5)\n\t" \
  "s_barrier\n\t" \
  ".Lg30%=:\n\t" \
  "s_add_u32 s92, s92, 65024\n\t" \
  "s_addc_u32 s93, s93, 0\n\t" \
  "s_waitcnt lgkmcnt(6)\n\t" \
  "v_mfma_f32_16x16x32_bf16 v[144:147], v[176:179], v[96:99], v[144:147]\n\t" \
  "v_mfma_f32_16x16x32_bf16 v[152:155], v[176:179], v[100:103], v[152:155]\n\t" \
  "ds_read_b128 v[176:179], v208 offset:0\n\t" \
  "s_add_u32 m0, %[lw], 65536\n\t" \
  "v_mfma_f32_16x16x32_bf16 v[148:151], v[180:183], v[96:99], v[148:151]\n\t" \
  "v_mfma_f32_16x16x32_bf16 v[156:159], v[180:183], v[100:103], v[156:159]\n\t" \
  "ds_read_b128 v[180:183], v208 offset:16384\n\t" \
  "global_load_lds_dwordx4 v216, s[92:93] sc1\n\t" \
  "s_waitcnt lgkmcnt(6)\n\t" \
  "v_mfma_f32_16x16x32_bf16 v[144:147], v[184:187], v[104:107], v[144:147]\n\t" \
  "v_mfma_f32_16x16x32_bf16 v[152:155], v[184:187], v[108:111], v[152:155]\n\t" \
  "ds_read_b128 v[184:187], v209 offset:0\n\t" \
  "v_mfma_f32_16x16x32_bf16 v[148:151], v[188:191], v[104:107], v[148:151]\n\t" \
  "v_mfma_f32_16x16x32_bf16 v[156:159], v[188:191], v[108:111], v[156:159]\n\t" \
  "ds_read_b128 v[188:191], v209 offset:16384\n\t" \
  "s_waitcnt lgkmcnt(6)\n\t" \
  "v_mfma_f32_16x16x32_bf16 v[144:147], v[192:195], v[112:115], v[144:147]\n\t" \
  "v_mfma_f32_16x16x32_bf16 v[152:155], v[192:195], v[116:119], v[152:155]\n\t" \
  "ds_read_b128 v[192:195], v210 offset:0\n\t" \
  "s_add_u32 m0, %[lw], 66560\n\t" \
  "v_mfma_f32_16x16x32_bf16 v[148:151], v[196:199], v[112:115], v[148:151]\n\t" \
  "v_mfma_f32_16x16x32_bf16 v[156:159], v[196:199], v[116:119], v[156:159]\n\t" \
  "ds_read_b128 v[196:199], v210 offset:16384\n\t" \
  "global_load_lds_dwordx4 v217, s[92:93] sc1\n\t" \
  "s_waitcnt lgkmcnt(6)\n\t" \
  "v_mfma_f32_16x16x32_bf16 v[144:147], v[200:203], v[120:123], v[144:147]\n\t" \
  "v_mfma_f32_16x16x32_bf16 v[152:155], v[200:203], v[124:127], v[152:155]\n\t" \
  "ds_read_b128 v[200:203], v211 offset:0\n\t" \
  "v_mfma_f32_16x16x32_bf16 v[148:151], v[204:207], v[120:123], v[148:151]\n\t" \
  "v_mfma_f32_16x16x32_bf16 v[156:159], v[204:207], v[124:127], v[156:159]\n\t" \
  "ds_read_b128 v[204:207], v211 offset:16384\n\t" \
  "s_add_u32 s86, s86, 1\n\t" \
  "s_add_u32 s87, s87, 1\n\t" \
  "s_cmp_eq_u32 s86, 2\n\t" \
  "s_cbranch_scc1 .Ll0%=\n\t" \
  "s_cmp_lt_u32 s86, %[end]\n\t" \
  "s_cbranch_scc1 .Lp0%=\n\t" \
  "s_cmp_lg_u32 s86, 64\n\t" \
  "s_cbranch_scc1 .Lx0%=\n\t" \
  ".Ll0%=:\n\t" \
  "s_nop 7\n\t" \
  "ds_write_b128 v220, v[152:155] offset:0\n\t" \
  "ds_write_b128 v220, v[156:159] offset:1024\n\t" \
  "s_lshr_b32 s90, s87, 7\n\t" \
  "s_xor_b32 s90, s90, %[s0]\n\t" \
  "s_and_b32 s90, s90, 1\n\t" \
  "s_lshl1_add_u32 s90, s87, s90\n\t" \
  "s_lshl_b32 s90, s90, 13\n\t" \
  "s_cmp_lt_i32 s86, 1\n\t" \
  "s_cselect_b32 s90, 0x70000000, s90\n\t" \
  "s_waitcnt lgkmcnt(0)\n\t" \
  "s_barrier\n\t" \
  "ds_read_b128 v[160:163], v221 offset:0\n\t" \
  "ds_read_b128 v[164:167], v221 offset:1024\n\t" \
  "s_waitcnt lgkmcnt(0)\n\t" \
  "v_add_f32 v160, v160, v144\n\t" \
  "v_add_f32 v161, v161, v145\n\t" \
  "v_add_f32 v162, v162, v146\n\t" \
  "v_add_f32 v163, v163, v147\n\t" \
  "v_add_f32 v164, v164, v148\n\t" \
  "v_add_f32 v165, v165, v149\n\t" \
  "v_add_f32 v166, v166, v150\n\t" \
  "v_add_f32 v167, v167, v151\n\t" \
  "v_cvt_pk_bf16_f32 v168, v160, v161\n\t" \
  "v_cvt_pk_bf16_f32 v169, v162, v163\n\t" \
  "v_cvt_pk_bf16_f32 v170, v164, v165\n\t" \
  "v_cvt_pk_bf16_f32 v171, v166, v167\n\t" \
  "buffer_store_dwordx4 v[168:171], v172, %[rt], s90 offen sc1\n\t" \
  "s_waitcnt vmcnt(0)\n\t" \
  "s_barrier\n\t" \
  "s_cmp_eq_u32 s86, 2\n\t" \
  "s_cbranch_scc1 .Lp0%=\n\t" \
  "s_branch .Lx0%=\n\t" \
  ".Lf0%=:\n\t" \
  "s_nop 7\n\t" \
  "ds_write_b128 v220, v[136:139] offset:0\n\t" \
  "ds_write_b128 v220, v[140:143] offset:1024\n\t" \
  "s_lshr_b32 s90, s87, 7\n\t" \
  "s_xor_b32 s90, s90, %[s0]\n\t" \
  "s_and_b32 s90, s90, 1\n\t" \
  "s_lshl1_add_u32 s90, s87, s90\n\t" \
  "s_lshl_b32 s90, s90, 13\n\t" \
  "s_cmp_lt_i32 s86, 1\n\t" \
  "s_cselect_b32 s90, 0x70000000, s90\n\t" \
  "s_waitcnt lgkmcnt(0)\n\t" \
  "s_barrier\n\t" \
  "ds_read_b128 v[160:163], v221 offset:0\n\t" \
  "ds_read_b128 v[164:167], v221 offset:1024\n\t" \
  "s_waitcnt lgkmcnt(0)\n\t" \
  "v_add_f32 v160, v160, v128\n\t" \
  "v_add_f32 v161, v161, v129\n\t" \
  "v_add_f32 v162, v162, v130\n\t" \
  "v_add_f32 v163, v163, v131\n\t" \
  "v_add_f32 v164, v164, v132\n\t" \
  "v_add_f32 v165, v165, v133\n\t" \
  "v_add_f32 v166, v166, v134\n\t" \
  "v_add_f32 v167, v167, v135\n\t" \
  "v_cvt_pk_bf16_f32 v168, v160, v161\n\t" \
  "v_cvt_pk_bf16_f32 v169, v162, v163\n\t" \
  "v_cvt_pk_bf16_f32 v170, v164, v165\n\t" \
  "v_cvt_pk_bf16_f32 v171, v166, v167\n\t" \
  "buffer_store_dwordx4 v[168:171], v172, %[rt], s90 offen sc1\n\t" \
  "s_waitcnt vmcnt(0)\n\t" \
  "s_barrier\n\t" \
  "s_branch .Lfb0%=\n\t" \
  ".Lw10%=:\n\t" \
  "s_waitcnt vmcnt(2)\n\t" \
  "s_barrier\n\t" \
  "s_branch .Lg10%=\n\t" \
  ".Lw30%=:\n\t" \
  "s_waitcnt vmcnt(2)\n\t" \
  "s_barrier\n\t" \
  "s_branch .Lg30%=\n\t" \
  ".Lx0%=:\n\t" \
  "s_waitcnt lgkmcnt(0)\n\t" \
  "s_setprio 0\n\t" \
  "s_mov_b32 m0, s94\n\t" \
  "s_branch .Le%=\n\t" \
  ".Lk%=:\n\t" \
  "s_mov_b32 s94, m0\n\t" \
  "s_mov_b64 s[92:93], %[x0]\n\t" \
  "s_mov_b32 s86, %[g0]\n\t" \
  "s_add_u32 s87, s86, %[t0]\n\t" \
  "s_sub_i32 s88, s86, 3\n\t" \
  "ds_read_b128 v[176:179], v208 offset:0\n\t" \
  "ds_read_b128 v[180:183], v208 offset:16384\n\t" \
  "ds_read_b128 v[184:187], v209 offset:0\n\t" \
  "ds_read_b128 v[188:191], v209 offset:16384\n\t" \
  "ds_read_b128 v[192:195], v210 offset:0\n\t" \
  "ds_read_b128 v[196:199], v210 offset:16384\n\t" \
  "ds_read_b128 v[200:203], v211 offset:0\n\t" \
  "ds_read_b128 v[204:207], v211 offset:16384\n\t" \
  ".Lp1%=:\n\t" \
  "s_waitcnt lgkmcnt(6)\n\t" \
  "v_mfma_f32_16x16x32_bf16 v[128:131], v[176:179], v[0:3], 0\n\t" \
  "v_mfma_f32_16x16x32_bf16 v[136:139], v[176:179], v[4:7], 0\n\t" \
  "ds_read_b128 v[176:179], v208 offset:256\n\t" \
  "v_mfma_f32_16x16x32_bf16 v[132:135], v[180:183], v[0:3], 0\n\t" \
  "v_mfma_f32_16x16x32_bf16 v[140:143], v[180:183], v[4:7], 0\n\t" \
  "ds_read_b128 v[180:183], v208 offset:16640\n\t" \
  "s_waitcnt lgkmcnt(6)\n\t" \
  "v_mfma_f32_16x16x32_bf16 v[128:131], v[184:187], v[8:11], v[128:131]\n\t" \
  "v_mfma_f32_16x16x32_bf16 v[136:139], v[184:187], v[12:15], v[136:139]\n\t" \
  "ds_read_b128 v[184:187], v209 offset:256\n\t" \
  "s_add_u32 m0, %[lw], 67584\n\t" \
  "v_mfma_f32_16x16x32_bf16 v[132:135], v[188:191], v[8:11], v[132:135]\n\t" \
  "v_mfma_f32_16x16x32_bf16 v[140:143], v[188:191], v[12:15], v[140:143]\n\t" \
  "ds_read_b128 v[188:191], v209 offset:16640\n\t" \
  "global_load_lds_dwordx4 v218, s[92:93] sc1\n\t" \
  "ds_write_b128 v220, v[152:155] offset:0\n\t" \
  "ds_write_b128 v220, v[156:159] offset:1024\n\t" \
  "s_waitcnt lgkmcnt(8)\n\t" \
  "v_mfma_f32_16x16x32_bf16 v[128:131], v[192:195], v[16:19], v[128:131]\n\t" \
  "v_mfma_f32_16x16x32_bf16 v[136:139], v[192:195], v[20:23], v[136:139]\n\t" \
  "ds_read_b128 v[192:195], v210 offset:256\n\t" \
  "v_mfma_f32_16x16x32_bf16 v[132:135], v[196:199], v[16:19], v[132:135]\n\t" \
  "v_mfma_f32_16x16x32_bf16 v[140:143], v[196:199], v[20:23], v[140:143]\n\t" \
  "ds_read_b128 v[196:199], v210 offset:16640\n\t" \
  "s_lshr_b32 s90, s87, 7\n\t" \
  "s_xor_b32 s90, s90, %[s0]\n\t" \
  "s_and_b32 s90, s90, 1\n\t" \
  "s_lshl1_add_u32 s90, s87, s90\n\t" \
  "s_lshl_b32 s90, s90, 13\n\t" \
  "s_cmp_lt_i32 s86, 3\n\t" \
  "s_cselect_b32 s90, 0x70000000, s90\n\t" \
  "s_waitcnt lgkmcnt(8)\n\t" \
  "v_mfma_f32_16x16x32_bf16 v[128:131], v[200:203], v[24:27], v[128:131]\n\t" \
  "v_mfma_f32_16x16x32_bf16 v[136:139], v[200:203], v[28:31], v[136:139]\n\t" \
  "ds_read_b128 v[200:203], v211 offset:256\n\t" \
  "s_add_u32 m0, %[lw], 68608\n\t" \
  "v_mfma_f32_16x16x32_bf16 v[132:135], v[204:207], v[24:27], v[132:135]\n\t" \
  "v_mfma_f32_16x16x32_bf16 v[140:143], v[204:207], v[28:31], v[140:143]\n\t" \
  "ds_read_b128 v[204:207], v211 offset:16640\n\t" \
  "global_load_lds_dwordx4 v219, s[92:93] sc1\n\t" \
  "s_waitcnt lgkmcnt(4)\n\t" \
  "s_waitcnt vmcnt(4)\n\t" \
  "s_barrier\n\t" \
  ".Lg01%=:\n\t" \
  "s_add_u32 s92, s92, 512\n\t" \
  "s_addc_u32 s93, s93, 0\n\t" \
  "v_mfma_f32_16x16x32_bf16 v[128:131], v[176:179], v[32:35], v[128:131]\n\t" \
  "v_mfma_f32_16x16x32_bf16 v[136:139], v[176:179], v[36:39], v[136:139]\n\t" \
  "ds_read_b128 v[176:179], v208 offset:32768\n\t" \
  "v_mfma_f32_16x16x32_bf16 v[132:135], v[180:183], v[32:35], v[132:135]\n\t" \
  "v_mfma_f32_16x16x32_bf16 v[140:143], v[180:183], v[36:39], v[140:143]\n\t" \
  "ds_read_b128 v[180:183], v208 offset:49152\n\t" \
  "ds_read_b128 v[160:163], v221 offset:0\n\t" \
  "ds_read_b128 v[164:167], v221 offset:1024\n\t" \
  "s_sub_i32 s91, s86, 2\n\t" \
  "s_max_i32 s88, s88, s91\n\t" \
  "v_mov_b32 v173, s88\n\t" \
  "buffer_store_dword v173, v174, %[rs], 0 offen sc1\n\t" \
  "v_mfma_f32_16x16x32_bf16 v[128:131], v[184:187], v[40:43], v[128:131]\n\t" \
  "v_mfma_f32_16x16x32_bf16 v[136:139], v[184:187], v[44:47], v[136:139]\n\t" \
  "ds_read_b128 v[184:187], v209 offset:32768\n\t" \
  "s_add_u32 m0, %[lw], 98304\n\t" \
  "v_mfma_f32_16x16x32_bf16 v[132:135], v[188:191], v[40:43], v[132:135]\n\t" \
  "v_mfma_f32_16x16x32_bf16 v[140:143], v[188:191], v[44:47], v[140:143]\n\t" \
  "ds_read_b128 v[188:191], v209 offset:49152\n\t" \
  "global_load_lds_dwordx4 v216, s[92:93] sc1\n\t" \
  "s_waitcnt lgkmcnt(8)\n\t" \
  "v_mfma_f32_16x16x32_bf16 v[128:131], v[192:195], v[48:51], v[128:131]\n\t" \
  "v_mfma_f32_16x16x32_bf16 v[136:139], v[192:195], v[52:55], v[136:139]\n\t" \
  "ds_read_b128 v[192:195], v210 offset:32768\n\t" \
  "v_mfma_f32_16x16x32_bf16 v[132:135], v[196:199], v[48:51], v[132:135]\n\t" \
  "v_mfma_f32_16x16x32_bf16 v[140:143], v[196:199], v[52:55], v[140:143]\n\t" \
  "ds_read_b128 v[196:199], v210 offset:49152\n\t" \
  "s_waitcnt lgkmcnt(4)\n\t" \
  "v_add_f32 v160, v160, v144\n\t" \
  "v_add_f32 v161, v161, v145\n\t" \
  "v_add_f32 v162, v162, v146\n\t" \
  "v_add_f32 v163, v163, v147\n\t" \
  "v_add_f32 v164, v164, v148\n\t" \
  "v_add_f32 v165, v165, v149\n\t" \
  "v_add_f32 v166, v166, v150\n\t" \
  "v_add_f32 v167, v167, v151\n\t" \
  "v_mfma_f32_16x16x32_bf16 v[128:131], v[200:203], v[56:59], v[128:131]\n\t" \
  "v_mfma_f32_16x16x32_bf16 v[136:139], v[200:203], v[60:63], v[136:139]\n\t" \
  "ds_read_b128 v[200:203], v211 offset:32768\n\t" \
  "s_add_u32 m0, %[lw], 99328\n\t" \
  "v_mfma_f32_16x16x32_bf16 v[132:135], v[204:207], v[56:59], v[132:135]\n\t" \
  "v_mfma_f32_16x16x32_bf16 v[140:143], v[204:207], v[60:63], v[140:143]\n\t" \
  "ds_read_b128 v[204:207], v211 offset:49152\n\t" \
  "global_load_lds_dwordx4 v217, s[92:93] sc1\n\t" \
  "v_cvt_pk_bf16_f32 v168, v160, v161\n\t" \
  "v_cvt_pk_bf16_f32 v169, v162, v163\n\t" \
  "v_cvt_pk_bf16_f32 v170, v164, v165\n\t" \
  "v_cvt_pk_bf16_f32 v171, v166, v167\n\t" \
  "buffer_store_dwordx4 v[168:171], v172, %[rt], s90 offen sc1\n\t" \
  "v_mfma_f32_16x16x32_bf16 v[128:131], v[176:179], v[64:67], v[128:131]\n\t" \
  "v_mfma_f32_16x16x32_bf16 v[136:139], v[176:179], v[68:71], v[136:139]\n\t" \
  "ds_read_b128 v[176:179], v208 offset:33024\n\t" \
  "v_mfma_f32_16x16x32_bf16 v[132:135], v[180:183], v[64:67], v[132:135]\n\t" \
  "v_mfma_f32_16x16x32_bf16 v[140:143], v[180:183], v[68:71], v[140:143]\n\t" \
  "ds_read_b128 v[180:183], v208 offset:49408\n\t" \
  "s_waitcnt lgkmcnt(6)\n\t" \
  "v_mfma_f32_16x16x32_bf16 v[128:131], v[184:187], v[72:75], v[128:131]\n\t" \
  "v_mfma_f32_16x16x32_bf16 v[136:139], v[184:187], v[76:79], v[136:139]\n\t" \
  "ds_read_b128 v[184:187], v209 offset:33024\n\t" \
  "s_add_u32 m0, %[lw], 100352\n\t" \
  "v_mfma_f32_16x16x32_bf16 v[132:135], v[188:191], v[72:75], v[132:135]\n\t" \
  "v_mfma_f32_16x16x32_bf16 v[140:143], v[188:191], v[76:79], v[140:143]\n\t" \
  "ds_read_b128 v[188:191], v209 offset:49408\n\t" \
  "global_load_lds_dwordx4 v218, s[92:93] sc1\n\t" \
  "s_waitcnt lgkmcnt(6)\n\t" \
  "v_mfma_f32_16x16x32_bf16 v[128:131], v[192:195], v[80:83], v[128:131]\n\t" \
  "v_mfma_f32_16x16x32_bf16 v[136:139], v[192:195], v[84:87], v[136:139]\n\t" \
  "ds_read_b128 v[192:195], v210 offset:33024\n\t" \
  "v_mfma_f32_16x16x32_bf16 v[132:135], v[196:199], v[80:83], v[132:135]\n\t" \
  "v_mfma_f32_16x16x32_bf16 v[140:143], v[196:199], v[84:87], v[140:143]\n\t" \
  "ds_read_b128 v[196:199], v210 offset:49408\n\t" \
  "s_waitcnt lgkmcnt(6)\n\t" \
  "v_mfma_f32_16x16x32_bf16 v[128:131], v[200:203], v[88:91], v[128:131]\n\t" \
  "v_mfma_f32_16x16x32_bf16 v[136:139], v[200:203], v[92:95], v[136:139]\n\t" \
  "ds_read_b128 v[200:203], v211 offset:33024\n\t" \
  "s_add_u32 m0, %[lw], 101376\n\t" \
  "v_mfma_f32_16x16x32_bf16 v[132:135], v[204:207], v[88:91], v[132:135]\n\t" \
  "v_mfma_f32_16x16x32_bf16 v[140:143], v[204:207], v[92:95], v[140:143]\n\t" \
  "ds_read_b128 v[204:207], v211 offset:49408\n\t" \
  "global_load_lds_dwordx4 v219, s[92:93] sc1\n\t" \
  "s_cmp_lt_u32 s86, 6\n\t" \
  "s_cbranch_scc1 .Lw11%=\n\t" \
  "s_waitcnt vmcnt(6)\n\t" \
  "s_barrier\n\t" \
  ".Lg11%=:\n\t" \
  "s_add_u32 s92, s92, 65024\n\t" \
  "s_addc_u32 s93, s93, 0\n\t" \
  "s_waitcnt lgkmcnt(6)\n\t" \
  "v_mfma_f32_16x16x32_bf16 v[128:131], v[176:179], v[96:99], v[128:131]\n\t" \
  "v_mfma_f32_16x16x32_bf16 v[136:139], v[176:179], v[100:103], v[136:139]\n\t" \
  "ds_read_b128 v[176:179], v212 offset:0\n\t" \
  "v_mfma_f32_16x16x32_bf16 v[132:135], v[180:183], v[96:99], v[132:135]\n\t" \
  "v_mfma_f32_16x16x32_bf16 v[140:143], v[180:183], v[100:103], v[140:143]\n\t" \
  "ds_read_b128 v[180:183], v212 offset:16384\n\t" \
  "s_waitcnt lgkmcnt(6)\n\t" \
  "v_mfma_f32_16x16x32_bf16 v[128:131], v[184:187], v[104:107], v[128:131]\n\t" \
  "v_mfma_f32_16x16x32_bf16 v[136:139], v[184:187], v[108:111], v[136:139]\n\t" \
  "ds_read_b128 v[184:187], v213 offset:0\n\t" \
  "s_add_u32 m0, %[lw], 0\n\t" \
  "v_mfma_f32_16x16x32_bf16 v[132:135], v[188:191], v[104:107], v[132:135]\n\t" \
  "v_mfma_f32_16x16x32_bf16 v[140:143], v[188:191], v[108:111], v[140:143]\n\t" \
  "ds_read_b128 v[188:191], v213 offset:16384\n\t" \
  "global_load_lds_dwordx4 v216, s[92:93] sc1\n\t" \
  "s_waitcnt lgkmcnt(6)\n\t" \
  "v_mfma_f32_16x16x32_bf16 v[128:131], v[192:195], v[112:115], v[128:131]\n\t" \
  "v_mfma_f32_16x16x32_bf16 v[136:139], v[192:195], v[116:119], v[136:139]\n\t" \
  "ds_read_b128 v[192:195], v214 offset:0\n\t" \
  "v_mfma_f32_16x16x32_bf16 v[132:135], v[196:199], v[112:115], v[132:135]\n\t" \
  "v_mfma_f32_16x16x32_bf16 v[140:143], v[196:199], v[116:119], v[140:143]\n\t" \
  "ds_read_b128 v[196:199], v214 offset:16384\n\t" \
  "s_waitcnt lgkmcnt(6)\n\t" \
  "v_mfma_f32_16x16x32_bf16 v[128:131], v[200:203], v[120:123], v[128:131]\n\t" \
  "v_mfma_f32_16x16x32_bf16 v[136:139], v[200:203], v[124:127], v[136:139]\n\t" \
  "ds_read_b128 v[200:203], v215 offset:0\n\t" \
  "s_add_u32 m0, %[lw], 1024\n\t" \
  "v_mfma_f32_16x16x32_bf16 v[132:135], v[204:207], v[120:123], v[132:135]\n\t" \
  "v_mfma_f32_16x16x32_bf16 v[140:143], v[204:207], v[124:127], v[140:143]\n\t" \
  "ds_read_b128 v[204:207], v215 offset:16384\n\t" \
  "global_load_lds_dwordx4 v217, s[92:93] sc1\n\t" \
  "s_add_u32 s86, s86, 1\n\t" \
  "s_add_u32 s87, s87, 1\n\t" \
  "s_cmp_eq_u32 s86, 1\n\t" \
  "s_cbranch_scc1 .Lf1%=\n\t" \
  ".Lfb1%=:\n\t" \
  "s_waitcnt lgkmcnt(6)\n\t" \
  "v_mfma_f32_16x16x32_bf16 v[144:147], v[176:179], v[0:3], 0\n\t" \
  "v_mfma_f32_16x16x32_bf16 v[152:155], v[176:179], v[4:7], 0\n\t" \
  "ds_read_b128 v[176:179], v212 offset:256\n\t" \
  "v_mfma_f32_16x16x32_bf16 v[148:151], v[180:183], v[0:3], 0\n\t" \
  "v_mfma_f32_16x16x32_bf16 v[156:159], v[180:183], v[4:7], 0\n\t" \
  "ds_read_b128 v[180:183], v212 offset:16640\n\t" \
  "s_waitcnt lgkmcnt(6)\n\t" \
  "v_mfma_f32_16x16x32_bf16 v[144:147], v[184:187], v[8:11], v[144:147]\n\t" \
  "v_mfma_f32_16x16x32_bf16 v[152:155], v[184:187], v[12:15], v[152:155]\n\t" \
  "ds_read_b128 v[184:187], v213 offset:256\n\t" \
  "s_add_u32 m0, %[lw], 2048\n\t" \
  "v_mfma_f32_16x16x32_bf16 v[148:151], v[188:191], v[8:11], v[148:151]\n\t" \
  "v_mfma_f32_16x16x32_bf16 v[156:159], v[188:191], v[12:15], v[156:159]\n\t" \
  "ds_read_b128 v[188:191], v213 offset:16640\n\t" \
  "global_load_lds_dwordx4 v218, s[92:93] sc1\n\t" \
  "ds_write_b128 v220, v[136:139] offset:0\n\t" \
  "ds_write_b128 v220, v[140:143] offset:1024\n\t" \
  "s_waitcnt lgkmcnt(8)\n\t" \
  "v_mfma_f32_16x16x32_bf16 v[144:147], v[192:195], v[16:19], v[144:147]\n\t" \
  "v_mfma_f32_16x16x32_bf16 v[152:155], v[192:195], v[20:23], v[152:155]\n\t" \
  "ds_read_b128 v[192:195], v214 offset:256\n\t" \
  "v_mfma_f32_16x16x32_bf16 v[148:151], v[196:199], v[16:19], v[148:151]\n\t" \
  "v_mfma_f32_16x16x32_bf16 v[156:159], v[196:199], v[20:23], v[156:159]\n\t" \
  "ds_read_b128 v[196:199], v214 offset:16640\n\t" \
  "s_lshr_b32 s90, s87, 7\n\t" \
  "s_xor_b32 s90, s90, %[s0]\n\t" \
  "s_and_b32 s90, s90, 1\n\t" \
  "s_lshl1_add_u32 s90, s87, s90\n\t" \
  "s_lshl_b32 s90, s90, 13\n\t" \
  "s_cmp_lt_i32 s86, 3\n\t" \
  "s_cselect_b32 s90, 0x70000000, s90\n\t" \
  "s_waitcnt lgkmcnt(8)\n\t" \
  "v_mfma_f32_16x16x32_bf16 v[144:147], v[200:203], v[24:27], v[144:147]\n\t" \
  "v_mfma_f32_16x16x32_bf16 v[152:155], v[200:203], v[28:31], v[152:155]\n\t" \
  "ds_read_b128 v[200:203], v215 offset:256\n\t" \
  "s_add_u32 m0, %[lw], 3072\n\t" \
  "v_mfma_f32_16x16x32_bf16 v[148:151], v[204:207], v[24:27], v[148:151]\n\t" \
  "v_mfma_f32_16x16x32_bf16 v[156:159], v[204:207], v[28:31], v[156:159]\n\t" \
  "ds_read_b128 v[204:207], v215 offset:16640\n\t" \
  "global_load_lds_dwordx4 v219, s[92:93] sc1\n\t" \
  "s_waitcnt lgkmcnt(4)\n\t" \
  "s_waitcnt vmcnt(4)\n\t" \
  "s_barrier\n\t" \
  ".Lg21%=:\n\t" \
  "s_add_u32 s92, s92, 512\n\t" \
  "s_addc_u32 s93, s93, 0\n\t" \
  "v_mfma_f32_16x16x32_bf16 v[144:147], v[176:179], v[32:35], v[144:147]\n\t" \
  "v_mfma_f32_16x16x32_bf16 v[152:155], v[176:179], v[36:39], v[152:155]\n\t" \
  "ds_read_b128 v[176:179], v212 offset:32768\n\t" \
  "v_mfma_f32_16x16x32_bf16 v[148:151], v[180:183], v[32:35], v[148:151]\n\t" \
  "v_mfma_f32_16x16x32_bf16 v[156:159], v[180:183], v[36:39], v[156:159]\n\t" \
  "ds_read_b128 v[180:183], v212 offset:49152\n\t" \
  "ds_read_b128 v[160:163], v221 offset:0\n\t" \
  "ds_read_b128 v[164:167], v221 offset:1024\n\t" \
  "s_sub_i32 s91, s86, 2\n\t" \
  "s_max_i32 s88, s88, s91\n\t" \
  "v_mov_b32 v173, s88\n\t" \
  "buffer_store_dword v173, v174, %[rs], 0 offen sc1\n\t" \
  "v_mfma_f32_16x16x32_bf16 v[144:147], v[184:187], v[40:43], v[144:147]\n\t" \
  "v_mfma_f32_16x16x32_bf16 v[152:155], v[184:187], v[44:47], v[152:155]\n\t" \
  "ds_read_b128 v[184:187], v213 offset:32768\n\t" \
  "s_add_u32 m0, %[lw], 32768\n\t" \
  "v_mfma_f32_16x16x32_bf16 v[148:151], v[188:191], v[40:43], v[148:151]\n\t" \
  "v_mfma_f32_16x16x32_bf16 v[156:159], v[188:191], v[44:47], v[156:159]\n\t" \
  "ds_read_b128 v[188:191], v213 offset:49152\n\t" \
  "global_load_lds_dwordx4 v216, s[92:93] sc1\n\t" \
  "s_waitcnt lgkmcnt(8)\n\t" \
  "v_mfma_f32_16x16x32_bf16 v[144:147], v[192:195], v[48:51], v[144:147]\n\t" \
  "v_mfma_f32_16x16x32_bf16 v[152:155], v[192:195], v[52:55], v[152:155]\n\t" \
  "ds_read_b128 v[192:195], v214 offset:32768\n\t" \
  "v_mfma_f32_16x16x32_bf16 v[148:151], v[196:199], v[48:51], v[148:151]\n\t" \
  "v_mfma_f32_16x16x32_bf16 v[156:159], v[196:199], v[52:55], v[156:159]\n\t" \
  "ds_read_b128 v[196:199], v214 offset:49152\n\t" \
  "s_waitcnt lgkmcnt(4)\n\t" \
  "v_add_f32 v160, v160, v128\n\t" \
  "v_add_f32 v161, v161, v129\n\t" \
  "v_add_f32 v162, v162, v130\n\t" \
  "v_add_f32 v163, v163, v131\n\t" \
  "v_add_f32 v164, v164, v132\n\t" \
  "v_add_f32 v165, v165, v133\n\t" \
  "v_add_f32 v166, v166, v134\n\t" \
  "v_add_f32 v167, v167, v135\n\t" \
  "v_mfma_f32_16x16x32_bf16 v[144:147], v[200:203], v[56:59], v[144:147]\n\t" \
  "v_mfma_f32_16x16x32_bf16 v[152:155], v[200:203], v[60:63], v[152:155]\n\t" \
  "ds_read_b128 v[200:203], v215 offset:32768\n\t" \
  "s_add_u32 m0, %[lw], 33792\n\t" \
  "v_mfma_f32_16x16x32_bf16 v[148:151], v[204:207], v[56:59], v[148:151]\n\t" \
  "v_mfma_f32_16x16x32_bf16 v[156:159], v[204:207], v[60:63], v[156:159]\n\t" \
  "ds_read_b128 v[204:207], v215 offset:49152\n\t" \
  "global_load_lds_dwordx4 v217, s[92:93] sc1\n\t" \
  "v_cvt_pk_bf16_f32 v168, v160, v161\n\t" \
  "v_cvt_pk_bf16_f32 v169, v162, v163\n\t" \
  "v_cvt_pk_bf16_f32 v170, v164, v165\n\t" \
  "v_cvt_pk_bf16_f32 v171, v166, v167\n\t" \
  "buffer_store_dwordx4 v[168:171], v172, %[rt], s90 offen sc1\n\t" \
  "v_mfma_f32_16x16x32_bf16 v[144:147], v[176:179], v[64:67], v[144:147]\n\t" \
  "v_mfma_f32_16x16x32_bf16 v[152:155], v[176:179], v[68:71], v[152:155]\n\t" \
  "ds_read_b128 v[176:179], v212 offset:33024\n\t" \
  "v_mfma_f32_16x16x32_bf16 v[148:151], v[180:183], v[64:67], v[148:151]\n\t" \
  "v_mfma_f32_16x16x32_bf16 v[156:159], v[180:183], v[68:71], v[156:159]\n\t" \
  "ds_read_b128 v[180:183], v212 offset:49408\n\t" \
  "s_waitcnt lgkmcnt(6)\n\t" \
  "v_mfma_f32_16x16x32_bf16 v[144:147], v[184:187], v[72:75], v[144:147]\n\t" \
  "v_mfma_f32_16x16x32_bf16 v[152:155], v[184:187], v[76:79], v[152:155]\n\t" \
  "ds_read_b128 v[184:187], v213 offset:33024\n\t" \
  "s_add_u32 m0, %[lw], 34816\n\t" \
  "v_mfma_f32_16x16x32_bf16 v[148:151], v[188:191], v[72:75], v[148:151]\n\t" \
  "v_mfma_f32_16x16x32_bf16 v[156:159], v[188:191], v[76:79], v[156:159]\n\t" \
  "ds_read_b128 v[188:191], v213 offset:49408\n\t" \
  "global_load_lds_dwordx4 v218, s[92:93] sc1\n\t" \
  "s_waitcnt lgkmcnt(6)\n\t" \
  "v_mfma_f32_16x16x32_bf16 v[144:147], v[192:195], v[80:83], v[144:147]\n\t" \
  "v_mfma_f32_16x16x32_bf16 v[152:155], v[192:195], v[84:87], v[152:155]\n\t" \
  "ds_read_b128 v[192:195], v214 offset:33024\n\t" \
  "v_mfma_f32_16x16x32_bf16 v[148:151], v[196:199], v[80:83], v[148:151]\n\t" \
  "v_mfma_f32_16x16x32_bf16 v[156:159], v[196:199], v[84:87], v[156:159]\n\t" \
  "ds_read_b128 v[196:199], v214 offset:49408\n\t" \
  "s_waitcnt lgkmcnt(6)\n\t" \
  "v_mfma_f32_16x16x32_bf16 v[144:147], v[200:203], v[88:91], v[144:147]\n\t" \
  "v_mfma_f32_16x16x32_bf16 v[152:155], v[200:203], v[92:95], v[152:155]\n\t" \
  "ds_read_b128 v[200:203], v215 offset:33024\n\t" \
  "s_add_u32 m0, %[lw], 35840\n\t" \
  "v_mfma_f32_16x16x32_bf16 v[148:151], v[204:207], v[88:91], v[148:151]\n\t" \
  "v_mfma_f32_16x16x32_bf16 v[156:159], v[204:207], v[92:95], v[156:159]\n\t" \
  "ds_read_b128 v[204:207], v215 offset:49408\n\t" \
  "global_load_lds_dwordx4 v219, s[92:93] sc1\n\t" \
  "s_cmp_lt_u32 s86, 6\n\t" \
  "s_cbranch_scc1 .Lw31%=\n\t" \
  "s_waitcnt vmcnt(6)\n\t" \
  "s_barrier\n\t" \
  ".Lg31%=:\n\t" \
  "s_add_u32 s92, s92, 65024\n\t" \
  "s_addc_u32 s93, s93, 0\n\t" \
  "s_waitcnt lgkmcnt(6)\n\t" \
  "v_mfma_f32_16x16x32_bf16 v[144:147], v[176:179], v[96:99], v[144:147]\n\t" \
  "v_mfma_f32_16x16x32_bf16 v[152:155], v[176:179], v[100:103], v[152:155]\n\t" \
  "ds_read_b128 v[176:179], v208 offset:0\n\t" \
  "v_mfma_f32_16x16x32_bf16 v[148:151], v[180:183], v[96:99], v[148:151]\n\t" \
  "v_mfma_f32_16x16x32_bf16 v[156:159], v[180:183], v[100:103], v[156:159]\n\t" \
  "ds_read_b128 v[180:183], v208 offset:16384\n\t" \
  "s_waitcnt lgkmcnt(6)\n\t" \
  "v_mfma_f32_16x16x32_bf16 v[144:147], v[184:187], v[104:107], v[144:147]\n\t" \
  "v_mfma_f32_16x16x32_bf16 v[152:155], v[184:187], v[108:111], v[152:155]\n\t" \
  "ds_read_b128 v[184:187], v209 offset:0\n\t" \
  "s_add_u32 m0, %[lw], 65536\n\t" \
  "v_mfma_f32_16x16x32_bf16 v[148:151], v[188:191], v[104:107], v[148:151]\n\t" \
  "v_mfma_f32_16x16x32_bf16 v[156:159], v[188:191], v[108:111], v[156:159]\n\t" \
  "ds_read_b128 v[188:191], v209 offset:16384\n\t" \
  "global_load_lds_dwordx4 v216, s[92:93] sc1\n\t" \
  "s_waitcnt lgkmcnt(6)\n\t" \
  "v_mfma_f32_16x16x32_bf16 v[144:147], v[192:195], v[112:115], v[144:147]\n\t" \
  "v_mfma_f32_16x16x32_bf16 v[152:155], v[192:195], v[116:119], v[152:155]\n\t" \
  "ds_read_b128 v[192:195], v210 offset:0\n\t" \
  "v_mfma_f32_16x16x32_bf16 v[148:151], v[196:199], v[112:115], v[148:151]\n\t" \
  "v_mfma_f32_16x16x32_bf16 v[156:159], v[196:199], v[116:119], v[156:159]\n\t" \
  "ds_read_b128 v[196:199], v210 offset:16384\n\t" \
  "s_waitcnt lgkmcnt(6)\n\t" \
  "v_mfma_f32_16x16x32_bf16 v[144:147], v[200:203], v[120:123], v[144:147]\n\t" \
  "v_mfma_f32_16x16x32_bf16 v[152:155], v[200:203], v[124:127], v[152:155]\n\t" \
  "ds_read_b128 v[200:203], v211 offset:0\n\t" \
  "s_add_u32 m0, %[lw], 66560\n\t" \
  "v_mfma_f32_16x16x32_bf16 v[148:151], v[204:207], v[120:123], v[148:151]\n\t" \
  "v_mfma_f32_16x16x32_bf16 v[156:159], v[204:207], v[124:127], v[156:159]\n\t" \
  "ds_read_b128 v[204:207], v211 offset:16384\n\t" \
  "global_load_lds_dwordx4 v217, s[92:93] sc1\n\t" \
  "s_add_u32 s86, s86, 1\n\t" \
  "s_add_u32 s87, s87, 1\n\t" \
  "s_cmp_eq_u32 s86, 2\n\t" \
  "s_cbranch_scc1 .Ll1%=\n\t" \
  "s_cmp_lt_u32 s86, %[end]\n\t" \
  "s_cbranch_scc1 .Lp1%=\n\t" \
  "s_cmp_lg_u32 s86, 64\n\t" \
  "s_cbranch_scc1 .Lx1%=\n\t" \
  ".Ll1%=:\n\t" \
  "s_nop 7\n\t" \
  "ds_write_b128 v220, v[152:155] offset:0\n\t" \
  "ds_write_b128 v220, v[156:159] offset:1024\n\t" \
  "s_lshr_b32 s90, s87, 7\n\t" \
  "s_xor_b32 s90, s90, %[s0]\n\t" \
  "s_and_b32 s90, s90, 1\n\t" \
  "s_lshl1_add_u32 s90, s87, s90\n\t" \
  "s_lshl_b32 s90, s90, 13\n\t" \
  "s_cmp_lt_i32 s86, 1\n\t" \
  "s_cselect_b32 s90, 0x70000000, s90\n\t" \
  "s_waitcnt lgkmcnt(0)\n\t" \
  "s_barrier\n\t" \
  "ds_read_b128 v[160:163], v221 offset:0\n\t" \
  "ds_read_b128 v[164:167], v221 offset:1024\n\t" \
  "s_waitcnt lgkmcnt(0)\n\t" \
  "v_add_f32 v160, v160, v144\n\t" \
  "v_add_f32 v161, v161, v145\n\t" \
  "v_add_f32 v162, v162, v146\n\t" \
  "v_add_f32 v163, v163, v147\n\t" \
  "v_add_f32 v164, v164, v148\n\t" \
  "v_add_f32 v165, v165, v149\n\t" \
  "v_add_f32 v166, v166, v150\n\t" \
  "v_add_f32 v167, v167, v151\n\t" \
  "v_cvt_pk_bf16_f32 v168, v160, v161\n\t" \
  "v_cvt_pk_bf16_f32 v169, v162, v163\n\t" \
  "v_cvt_pk_bf16_f32 v170, v164, v165\n\t" \
  "v_cvt_pk_bf16_f32 v171, v166, v167\n\t" \
  "buffer_store_dwordx4 v[168:171], v172, %[rt], s90 offen sc1\n\t" \
  "s_waitcnt vmcnt(0)\n\t" \
  "s_barrier\n\t" \
  "s_sub_i32 s91, s86, 1\n\t" \
  "s_max_i32 s88, s88, s91\n\t" \
  "v_mov_b32 v173, s88\n\t" \
  "buffer_store_dword v173, v174, %[rs], 0 offen sc1\n\t" \
  "s_cmp_eq_u32 s86, 2\n\t" \
  "s_cbranch_scc1 .Lp1%=\n\t" \
  "s_branch .Lx1%=\n\t" \
  ".Lf1%=:\n\t" \
  "s_nop 7\n\t" \
  "ds_write_b128 v220, v[136:139] offset:0\n\t" \
  "ds_write_b128 v220, v[140:143] offset:1024\n\t" \
  "s_lshr_b32 s90, s87, 7\n\t" \
  "s_xor_b32 s90, s90, %[s0]\n\t" \
  "s_and_b32 s90, s90, 1\n\t" \
  "s_lshl1_add_u32 s90, s87, s90\n\t" \
  "s_lshl_b32 s90, s90, 13\n\t" \
  "s_cmp_lt_i32 s86, 1\n\t" \
  "s_cselect_b32 s90, 0x70000000, s90\n\t" \
  "s_waitcnt lgkmcnt(0)\n\t" \
  "s_barrier\n\t" \
  "ds_read_b128 v[160:163], v221 offset:0\n\t" \
  "ds_read_b128 v[164:167], v221 offset:1024\n\t" \
  "s_waitcnt lgkmcnt(0)\n\t" \
  "v_add_f32 v160, v160, v128\n\t" \
  "v_add_f32 v161, v161, v129\n\t" \
  "v_add_f32 v162, v162, v130\n\t" \
  "v_add_f32 v163, v163, v131\n\t" \
  "v_add_f32 v164, v164, v132\n\t" \
  "v_add_f32 v165, v165, v133\n\t" \
  "v_add_f32 v166, v166, v134\n\t" \
  "v_add_f32 v167, v167, v135\n\t" \
  "v_cvt_pk_bf16_f32 v168, v160, v161\n\t" \
  "v_cvt_pk_bf16_f32 v169, v162, v163\n\t" \
  "v_cvt_pk_bf16_f32 v170, v164, v165\n\t" \
  "v_cvt_pk_bf16_f32 v171, v166, v167\n\t" \
  "buffer_store_dwordx4 v[168:171], v172, %[rt], s90 offen sc1\n\t" \
  "s_waitcnt vmcnt(0)\n\t" \
  "s_barrier\n\t" \
  "s_sub_i32 s91, s86, 1\n\t" \
  "s_max_i32 s88, s88, s91\n\t" \
  "v_mov_b32 v173, s88\n\t" \
  "buffer_store_dword v173, v174, %[rs], 0 offen sc1\n\t" \
  "s_branch .Lfb1%=\n\t" \
  ".Lw11%=:\n\t" \
  "s_waitcnt vmcnt(2)\n\t" \
  "s_barrier\n\t" \
  "s_sub_i32 s91, s86, 1\n\t" \
  "s_max_i32 s88, s88, s91\n\t" \
  "v_mov_b32 v173, s88\n\t" \
  "buffer_store_dword v173, v174, %[rs], 0 offen sc1\n\t" \
  "s_branch .Lg11%=\n\t" \
  ".Lw31%=:\n\t" \
  "s_waitcnt vmcnt(2)\n\t" \
  "s_barrier\n\t" \
  "s_sub_i32 s91, s86, 1\n\t" \
  "s_max_i32 s88, s88, s91\n\t" \
  "v_mov_b32 v173, s88\n\t" \
  "buffer_store_dword v173, v174, %[rs], 0 offen sc1\n\t" \
  "s_branch .Lg31%=\n\t" \
  ".Lx1%=:\n\t" \
  "s_waitcnt lgkmcnt(0)\n\t" \
  "s_setprio 0\n\t" \
  "s_mov_b32 m0, s94\n\t" \
  ".Le%=:\n\t"
constexpr int W_X = 131072, W_PM = W_X + 16384;
template <bool ISV>
__device__ __forceinline__ void job_proj(Args& p, int g, int which, int h, char* smem, __amdgpu_buffer_rsrc_t rsy) {
  const int tid = threadIdx.x, lane = tid & 63, r = lane & 15, q = lane >> 4;
  const int w = __builtin_amdgcn_readfirstlane(tid >> 6), kh = w >> 2, cg = w & 3;
  u32x32 W[4];
  const unsigned lw = lds_off(smem) + w * 4096;
  u32x4 vx;
  u32x2 tag;
  const char* xn = p.ws + WS_XN;
  auto row = [&](int t, int i) {
    const char* src = xn + (size_t)(min(t, 127) >> 1) * (SEG * XLD * 2) + (t & 1) * 512;
    unsigned keep;
    asm volatile("s_mov_b32 %0, m0\n\ts_mov_b32 m0, %3\n\ts_nop 0\n\tglobal_load_lds_dwordx4 %1, %2 sc1\n\ts_mov_b32 m0, %0"
                 : "=&s"(keep) : "v"(vx[i]), "s"(src), "s"(lw + (t & 3) * 32768 + i * 1024) : "memory");
  };
  {
    const u16* Wm = which == 0 ? p.Wk : p.Wq;
    asm volatile("" : "+s"(Wm));
    if (ISV) Wm = p.Wv;
    const unsigned vo = (tid >> 4) * (HD * 2) + 16 * ((lane & 15) ^ swzB((tid >> 4) & 15));
    auto stage = [&](int j) {
      const char* src = (const char*)(Wm + (size_t)(WREV ? 7 - j : j) * 64 * HD + h * DH);
      const unsigned dst = lds_off(smem) + (j & 3) * 32768 + w * 1024;
      glds2(src, 32 * HD * 2, vo, dst);
      if (TH_P < 63) HOLD_V((h & 3) ^ 1, TH_P);
      glds2(src + 512 * HD * 2, 32 * HD * 2, vo, dst + 16384);
      if (TH_P < 63) HOLD_V((h & 3) ^ 1, TH_P);
    };
    if (which == 1) { for (int z = 0; z < Q_DELAY; ++z) __builtin_amdgcn_s_sleep(16); }
    tag = start_rows<16>(p, 32 + g, [&]() { stage(0); stage(1); stage(2); stage(3); });
#pragma unroll
    for (int i = 0; i < 4; ++i) vx[i] = ((4 * w + i) ^ (ISV ? 0 : (lane >> 1) & 16)) * (XLD * 2) + (lane >> 5) * 1024 + (((lane & 31) ^ ((4 * w + i) & 15)) << 4);
    Boot bt;
#pragma unroll
    for (int j = 0; j < 8; ++j) {
      if (j < 4) WAIT_V(16); else if (j == 4) WAIT_V(12); else if (j == 5) WAIT_V(8); else if (j == 6) WAIT_V(4); else WAIT_V(10);
      if (j == 6 && w == 0) lds_at(smem + W_PM)[0] = boot_done(p.ws, bt, true, tag);
      LBAR();
      if (j == 4) report_rows(p, 32 + g, tag);
      const char* sb = smem + (j & 3) * 32768 + kh * 16384;
#pragma unroll
      for (int k = 0; k < 4; ++k) {
        const int ks = k >> 1, a = k & 1;
        const unsigned ch = ISV ? 2 * (2 * cg + (a ^ kh)) + ((r & 3) >> 1) : 4 * cg + (r & 3), by = ISV ? 8 * (r & 1) : 8 * a;
        s16x4 lo = __builtin_amdgcn_ds_read_tr16_b64_v4i16((lds_s16x4_ptr)(sb + offB(32 * ks + 8 * q + (r >> 2), ch) + by));
        s16x4 hi = __builtin_amdgcn_ds_read_tr16_b64_v4i16((lds_s16x4_ptr)(sb + offB(32 * ks + 8 * q + 4 + (r >> 2), ch) + by));
        const int f = 4 * (WREV ? 7 - j : j) + k;
        put4(W[f >> 3], f & 7, (bf16x8){lo[0], lo[1], lo[2], lo[3], hi[0], hi[1], hi[2], hi[3]});
      }
      LBAR();
      if (j == 3 && w == 0) boot_ask(p.ws, bt);
      if (j + 4 < 8) stage(j + 4);
      if (j == 6) {
#pragma unroll
        for (int i = 0; i < 10; ++i) row(i >> 2, i & 3);
      }
    }
    p.s0 = uni(lds_at(smem + W_PM)[0] >> 31);
  }
  RDY(128 + g);
  typedef __attribute__((ext_vector_type(8))) unsigned u32x8;
  u32x8 la;
#pragma unroll
  for (int i = 0; i < 8; ++i) la[i] = (i >> 2) * 65536 + r * 1024 + kh * 512 + (((4 * (i & 3) + q) ^ r) << 4);
  const unsigned xw = W_X + w * 2048 + lane * 16;
  u32x32 acc;
  asm volatile("" : "={v[128:159]}"(acc));
  WAIT_V(6);
  LBAR();
#pragma unroll 1
  for (int g0 = 0; g0 <= 12; g0 += 12) {
#define PR_LOOP(TXT)                                                                                                              \
    asm volatile(TXT                                                                                                              \
        : "+{v[128:159]}"(acc)                                                                                                    \
        : W4_IN, "{v[208:215]}"(la), "{v[216:219]}"(vx), "{v220}"(xw), "{v221}"(xw ^ 8192),                                       \
          "{v172}"((ISV ? 2 * cg + kh : 4 * kh + cg) * 1024 + lane * 16), "{v174}"(tid == 256 ? fkq(h, which) : 0x7F000000),      \
          [rt] "s"(raw_rsrc(p.ws + WS_T, T_BYTES)), [rs] "s"(raw_rsrc(p.ws + WS_SY, SY_TOTAL)), [s0] "s"(p.s0 ^ TF_KQV),          \
          [lw] "s"(lw), [kh] "s"(kh), [g0] "s"(g0), [end] "s"(g0 ? NSEG : 12), [x0] "s"(xn + (g0 + 1) * (SEG * XLD * 2)),                       \
          [t0] "s"(which * T_Q + kseg(0, h) - 1)                                                                                  \
        : CLB8(160, 161, 162, 163, 164, 165, 166, 167), "v168", "v169", "v170", "v171", "v173", CLB8(176, 177, 178, 179, 180, 181, 182, 183), \
          CLB8(184, 185, 186, 187, 188, 189, 190, 191), CLB8(192, 193, 194, 195, 196, 197, 198, 199),                             \
          CLB8(200, 201, 202, 203, 204, 205, 206, 207), "s86", "s87", "s88", "s90", "s91", "s92", "s93", "s94", "scc", "memory")
    if (ISV) PR_LOOP(PR_ASM_1); else PR_LOOP(PR_ASM_0);
    if (w == 0 && g0 == 0) wait_rows(p.ws, tag);
  }
}

enum { J_NONE = 0, J_GU = 3, J_PR = 5, J_CB = 8, J_DN = 14 };
struct BigJob { int type, panel, idx; };
struct BigPtr { const char *a, *b0, *b1; };
constexpr int G_LDA = XLD * 2, G_LDB = FF * 2;

__device__ __forceinline__ BigPtr big_ptrs(const Args& p, const BigJob& j) {
  BigPtr r;
  r.a = p.ws + WS_XN + (size_t)j.panel * 128 * G_LDA;
  r.b0 = (const char*)(p.wg + j.idx * 128); r.b1 = (const char*)(p.wu + j.idx * 128);
  return r;
}
__device__ __forceinline__ void big_publish(const Args& p, const BigJob& j) {
  int tid = threadIdx.x;
  asm volatile("" : "+v"(tid));
  if (j.type == J_GU && tid == 0)
    __hip_atomic_fetch_add((gu32*)(p.ws + WS_SY + SY_CH) + j.panel * 16, 1u, __ATOMIC_RELAXED, __HIP_MEMORY_SCOPE_AGENT);
}
typedef f32x4 Acc[2][2][4][2];
__device__ __forceinline__ u32x4 pack32(f32x4 a, f32x4 b) {
  return widen32(cvtpk(a[0], a[1]), cvtpk(a[2], a[3]), cvtpk(b[0], b[1]), cvtpk(b[2], b[3]));
}
#define st_epi(r, o, v) st_sc1(r, o, v)
__device__ __forceinline__ void big_epilogue(const Args& p, const BigJob& j, Acc& acc, char* smem) {
  int tid = threadIdx.x;
  asm volatile("" : "+v"(tid));
  const int lane = tid & 63;
  const int wid = __builtin_amdgcn_readfirstlane(tid >> 6);
  const int wr = wid >> 2, wc = wid & 3;
  {
    const __amdgpu_buffer_rsrc_t ro = mkrsrc(p.ws + WS_HF, S_LEN * 16384);
#pragma unroll
    for (int i = 0; i < 8; ++i) {
      f32x4 hh[2];
#pragma unroll
      for (int n = 0; n < 2; ++n)
#pragma unroll
        for (int jj = 0; jj < 4; ++jj) {
          const float g = acc[i >> 2][0][i & 3][n][jj], u = acc[i >> 2][1][i & 3][n][jj];
          hh[n][jj] = __builtin_amdgcn_rcpf(1.0f + __builtin_amdgcn_exp2f(g * -1.4426950408889634f)) * u;
        }
      st_epi(ro, h_off(p, j.panel + (i >> 2)) + (j.idx * 4 + wc) * 16384 + (wr * 4 + (i & 3)) * 1024 + lane * 16,
             ((u32x4){cvtpk(hh[0][0], hh[0][1]), cvtpk(hh[0][2], hh[0][3]), cvtpk(hh[1][0], hh[1][1]), cvtpk(hh[1][2], hh[1][3])}));
    }
  }
}
#define JST(n, i, v) do {} while (0)
__device__ __forceinline__ void gemm_role(Args& p, int g, char* smem) {
  const __amdgpu_buffer_rsrc_t rsy = mkrsrc(p.ws + WS_SY, SY_TOTAL);
  const int x = g & 3, k = g >> 2;
  volatile unsigned* slot = reinterpret_cast<volatile unsigned*>(smem + 163824);
  int rnd = k < 24 ? 2 : -1;
  int nj = 0; (void)nj;
  auto claim = [&]() {
    ++rnd;
    const int n = k < 24 ? 32 + k : rnd * 8 + k - 24, pn = (n * 37) >> 8;
    BigJob b;
    b.type = rnd < 4 ? J_GU : (rnd == 4 ? J_DN : (rnd == 5 && k >= 24 && k - 24 < 14 - NFUSE ? J_CB : J_NONE));
    b.panel = rnd < 4 ? 14 - 2 * pn : (rnd == 4 ? 4 * ((2 - (k >> 3)) & 3) + x : NFUSE + k - 24);
    b.idx = rnd < 4 ? x + 4 * (n - 7 * pn) : k & 7;
    return b;
  };
  if (g < 4 && threadIdx.x == 0) { LST(480 + 4 * g); LSTC(480 + 4 * g + 1); }
  if (k < 24) {
    JST(nj, 0, J_PR); JST(nj, 1, __builtin_amdgcn_s_memrealtime());
    if (k >= 16) job_proj<true>(p, g, 2, x * 8 + (k & 7), smem, rsy);
    else job_proj<false>(p, g, k >> 3, x * 8 + (k & 7), smem, rsy);
    JST(nj, 3, __builtin_amdgcn_s_memrealtime()); ++nj;
  } else {
    const unsigned t0 = (unsigned)__builtin_amdgcn_s_memrealtime();
    const u32x2 tag = start_rows<0>(p, g - 96, []() {});
    WAIT_V(0);
    LBAR();
    report_rows(p, g - 96, tag);
    while ((unsigned)__builtin_amdgcn_s_memrealtime() - t0 < PF_T0) __builtin_amdgcn_s_sleep(8);
#pragma unroll 1
    for (int i = GPF == 2 ? 2 : 0; i < PF_N; i += GPF == 2 ? 3 : 1) {
      const int m = i % 3, o = ((i / 3) * 32 + g - 96) * 448 + (int)threadIdx.x;
      if (threadIdx.x < 448) touch_line((const char*)(m == 0 ? p.wg : (m == 1 ? p.wu : p.wd)) + (size_t)o * 128, smem + (threadIdx.x >> 6) * 256);
      WAIT_V(1);
    }
    WAIT_V(0);
    if (threadIdx.x < 64) {
      wait_rows(p.ws, tag);
      Boot bt;
      boot_ask(p.ws, bt);
      WAIT_V(0);
      *slot = boot_done(p.ws, bt, true, tag);
      while ((unsigned)__builtin_amdgcn_s_memrealtime() - t0 < GU_DELAY) __builtin_amdgcn_s_sleep(8);
    }
    LBAR();
    p.s0 = *slot >> 31;
    RDY(128 + g);
  }
  p.s0 = uni(p.s0);
  BigJob cur = claim();
  while (cur.type != J_NONE) {
    JST(nj, 0, cur.type | cur.panel << 8); JST(nj, 1, __builtin_amdgcn_s_memrealtime());
    if (cur.type != J_GU) {
      if (cur.type == J_DN) job_down(p, cur.panel, cur.idx, smem, rsy);
      else job_comb(p, cur.panel, x, rsy);
      JST(nj, 3, __builtin_amdgcn_s_memrealtime()); ++nj;
      cur = claim();
      continue;
    }
    int tid = threadIdx.x;
    asm volatile("" : "+v"(tid));
    const int lane = tid & 63;
    const int wid = __builtin_amdgcn_readfirstlane(tid >> 6);
    const int wr = wid >> 2, wc = wid & 3;
    const unsigned ra = tid >> 3, ca = 16 * ((tid & 7) ^ ((tid >> 3) & 7)), rb = tid >> 4, cb = 16 * ((tid & 15) ^ swzB((tid >> 4) & 15));
    const unsigned lw = lds_off(smem) + wid * 1024;
#define B8_SA(b, h) (((b) * 2 + (h)) * 16384)
#define B8_SB(b, h) ((4 + (b) * 2 + (h)) * 16384)
#define B8_STA(b, h, P, kt) glds2<true>((P).a + (size_t)(h) * 128 * G_LDA + (kt) * 128, 64 * G_LDA, ra * G_LDA + ca, lw + B8_SA(b, h))
#define B8_STB(b, h, P, kt) glds2(((h) ? (P).b1 : (P).b0) + (size_t)(kt) * 64 * G_LDB, 32 * G_LDB, rb * G_LDB + cb, lw + B8_SB(b, h))
#define B8_LDA(b, h) do { _Pragma("unroll") for (int m = 0; m < 4; ++m) _Pragma("unroll") for (int kk = 0; kk < 2; ++kk) \
      At[m][kk] = ldsf(smem + B8_SA(b, h) + offA(wr * 64 + m * 16 + (lane & 15), kk * 4 + (lane >> 4))); } while (0)
#define B8_LDB(dst, b, h) do { _Pragma("unroll") for (int n = 0; n < 2; ++n) _Pragma("unroll") for (int kk = 0; kk < 2; ++kk) \
      dst[n][kk] = ld_bfrag(smem + B8_SB(b, h), kk, wc * 2 + n, lane); } while (0)
#define B8_MMA(ai, bj, Bt) do { __builtin_amdgcn_s_setprio(1); _Pragma("unroll") for (int m = 0; m < 4; ++m) _Pragma("unroll") for (int n = 0; n < 2; ++n) \
      _Pragma("unroll") for (int kk = 0; kk < 2; ++kk) acc[ai][bj][m][n] = MFMA(Bt[n][kk], At[m][kk], acc[ai][bj][m][n]); __builtin_amdgcn_s_setprio(0); } while (0)
#define WAIT_L(n) asm volatile("s_waitcnt lgkmcnt(%0)" :: "n"(n) : "memory")
#define BAR() __builtin_amdgcn_s_barrier()
#define WAIT_RX(c) asm volatile("s_waitcnt vmcnt(16)\n\ts_cmp_lg_u32 %0, 0\n\ts_cbranch_scc1 .Lrx%=\n\ts_waitcnt vmcnt(8)\n.Lrx%=:" :: "s"(c) : "memory", "scc")
    Acc acc;
    bf16x8 At[4][2], B0[2][2], B1[2][2];
    BigPtr cp = big_ptrs(p, cur);
    BigJob pend; pend.type = J_NONE; pend.panel = 0; pend.idx = 0;
    LBAR();
    B8_STB(0, 0, cp, 0); B8_STB(0, 1, cp, 0); B8_STA(0, 0, cp, 0); B8_STA(0, 1, cp, 0);
    if (wr == 1) BAR();
    WAIT_V(2); BAR();
    B8_STB(1, 0, cp, 1); B8_STA(1, 0, cp, 1); B8_STB(1, 1, cp, 1);
    WAIT_V(6); BAR();
    BigJob nx = pend;
    for (;;) {
      bool chain = false;
      BigPtr np = cp;
      constexpr int nt = DM / 64;
#pragma unroll
      for (int i = 0; i < 32; ++i) acc[i >> 4][(i >> 3) & 1][(i >> 1) & 3][i & 1] = (f32x4){0.f, 0.f, 0.f, 0.f};
#pragma unroll 1
      for (int t = 0; t < nt; t += 2) {
        const bool last = t == nt - 2;
        if (last) {
          nx = claim();
          chain = nx.type == J_GU;
          if (chain) np = big_ptrs(p, nx);
        }
        const BigPtr sp = np;
        const int t2 = last ? 0 : t + 2;
        if (t == 2) big_publish(p, pend);
        const int rx = __builtin_amdgcn_readfirstlane(t == 0 && pend.type != J_NONE);
        B8_LDB(B0, 0, 0); B8_LDB(B1, 0, 1); SCHED(); B8_LDA(0, 0); B8_STA(1, 1, cp, t + 1);
        WAIT_RX(rx);
        WAIT_L(0); BAR(); B8_MMA(0, 0, B0); B8_MMA(0, 1, B1); BAR(); SCHED();
        B8_LDA(0, 1); B8_STB(0, 0, sp, t2); B8_STB(0, 1, sp, t2); B8_STA(0, 0, sp, t2);
        WAIT_RX(rx);
        WAIT_L(0); BAR(); B8_MMA(1, 0, B0); B8_MMA(1, 1, B1); BAR(); SCHED();
        B8_LDB(B0, 1, 0); B8_LDB(B1, 1, 1); SCHED(); B8_LDA(1, 0); B8_STA(0, 1, sp, t2);
        WAIT_V(8); WAIT_L(0); BAR(); B8_MMA(0, 0, B0); B8_MMA(0, 1, B1); BAR(); SCHED();
        B8_LDA(1, 1); B8_STB(1, 0, sp, t2 + 1); B8_STB(1, 1, sp, t2 + 1); B8_STA(1, 0, sp, t2 + 1);
        WAIT_V(8); WAIT_L(0); BAR(); B8_MMA(1, 0, B0); B8_MMA(1, 1, B1); BAR(); SCHED();
      }
      if (wr == 0) BAR();
      JST(nj, 2, __builtin_amdgcn_s_memrealtime());
      big_epilogue(p, cur, acc, smem);
      if (!chain) break;
      JST(nj, 3, __builtin_amdgcn_s_memrealtime()); ++nj;
      pend = cur; cur = nx; cp = np;
      if (wr == 1) BAR();
      JST(nj, 0, cur.type | cur.panel << 8); JST(nj, 1, __builtin_amdgcn_s_memrealtime());
    }
    WAIT_V(0);
    BAR();
    asm volatile(".Lcode_end:");
    big_publish(p, cur);
    JST(nj, 3, __builtin_amdgcn_s_memrealtime()); ++nj;
    cur = nx;
  }
  if (threadIdx.x == 0) { LST(128 + g); }
  if (g < 4 && threadIdx.x == 0) { LST(480 + 4 * g + 2); LSTC(480 + 4 * g + 3); }
}

__device__ __forceinline__ unsigned now_q() { return (unsigned)__builtin_amdgcn_s_memrealtime() * 4u; }
__device__ __forceinline__ void pace_until(unsigned t) {
#pragma unroll 1
  for (int i = 0; i < 256; ++i) {
    const int d = (int)(t - now_q());
    if (d <= 0) break;
    if (d > 32) __builtin_amdgcn_s_sleep(1);
  }
}
#define CLB_E "s76", "s77", "s78", "s79", "s80", "s81", "s82", "s83", "s84", "s85", "s86", "s87", "s88", "s89"
#define EDGE_ASM \
  "s_memtime s[76:77]\n\t" \
  "s_memrealtime s[78:79]\n\t" \
  "s_nop 3\n\t" \
  "s_memrealtime s[80:81]\n\t" \
  "s_nop 3\n\t" \
  "s_memrealtime s[82:83]\n\t" \
  "s_nop 3\n\t" \
  "s_memrealtime s[84:85]\n\t" \
  "s_nop 3\n\t" \
  "s_memrealtime s[86:87]\n\t" \
  "s_nop 3\n\t" \
  "s_memrealtime s[88:89]\n\t" \
  "s_mov_b32 s93, 0\n\t" \
  "s_waitcnt lgkmcnt(0)\n\t" \
  "s_cmp_eq_u32 s80, s78\n\t" \
  "s_addc_u32 s93, s93, 0\n\t" \
  "s_cmp_eq_u32 s82, s78\n\t" \
  "s_addc_u32 s93, s93, 0\n\t" \
  "s_cmp_eq_u32 s84, s78\n\t" \
  "s_addc_u32 s93, s93, 0\n\t" \
  "s_cmp_eq_u32 s86, s78\n\t" \
  "s_addc_u32 s93, s93, 0\n\t" \
  "s_cmp_eq_u32 s88, s78\n\t" \
  "s_addc_u32 s93, s93, 0\n\t" \
  "s_mul_i32 s93, s93, 21\n\t" \
  "s_add_u32 s76, s76, 15\n\t" \
  "s_add_u32 %[kc], s76, s93\n\t" \
  "s_lshl2_add_u32 %[kq], s78, 16\n\t"
__device__ __forceinline__ unsigned time_of(unsigned cyc) {
  unsigned c, q;
  asm volatile(EDGE_ASM : [kc] "=&s"(c), [kq] "=&s"(q) :: CLB_E, "s93", "scc");
  return q - (((c - cyc) * 11900u) >> 16);
}
#define DS_RED 20
#define DS_MEM 18
#define LEAD_DN 2
#define LEAD_UP 12
__device__ __forceinline__ u32x2 ld2_sc1(__amdgpu_buffer_rsrc_t rs, int off, int soff = 0) {
  return __builtin_amdgcn_raw_buffer_load_b64(rs, off, soff, 16);
}
__device__ __forceinline__ void st2_sc1(__amdgpu_buffer_rsrc_t rs, int off, u32x2 v) {
  __builtin_amdgcn_raw_buffer_store_b64(v, rs, off, 0, 16);
}
__device__ __forceinline__ int row_max(int v) {
  v = max(v, __builtin_amdgcn_update_dpp(0, v, 0x128, 0xf, 0xf, false));
  v = max(v, __builtin_amdgcn_update_dpp(0, v, 0x124, 0xf, 0xf, false));
  v = max(v, __builtin_amdgcn_update_dpp(0, v, 0x122, 0xf, 0xf, false));
  v = max(v, __builtin_amdgcn_update_dpp(0, v, 0x121, 0xf, 0xf, false));
  return v;
}
__device__ __forceinline__ int wave_max(int v) {
  v = row_max(v);
  return max(max(__builtin_amdgcn_readlane(v, 0), __builtin_amdgcn_readlane(v, 16)),
             max(__builtin_amdgcn_readlane(v, 32), __builtin_amdgcn_readlane(v, 48)));
}
__device__ __forceinline__ int track(int est, int x, int up, int dn, int out) {
  const int d = x - est;
  return est + (d > out ? 2 : max(min(d, up), -dn));
}
#define WSL 12
#define PLAN_SLEEP 2
__device__ __forceinline__ unsigned latest(unsigned t) {
  const unsigned t0 = __builtin_amdgcn_readfirstlane(t);
  return t0 + wave_max((int)(t - t0));
}
#define RL0 300
__device__ __forceinline__ void watch(__amdgpu_buffer_rsrc_t rsy, int s, int lane, unsigned t0, const unsigned* tgt_, gu32* abortw, int hs) {
  pace_until(t0);
  const int rl = (int)__builtin_amdgcn_raw_buffer_load_b32(rsy, lane * 128, SY_BR + hs, 16);
  u32x2 ra;
  for (unsigned spins = 0;;) {
    asm volatile("" ::: "memory");
    ra = ld2_sc1(rsy, (lane & 31) * 128, SY_BA + hs);
    const bool all = __all((int)ra[0] >= s);
    SMP(4, s, spins);
    if (all) break;
    if (spins < 3) __builtin_amdgcn_s_sleep(PLAN_SLEEP); else __builtin_amdgcn_s_sleep(WSL);
    if (spin_fail(spins, abortw)) break;
  }
  const unsigned t = latest(ra[0] == (unsigned)s ? ra[1] : t0) + wave_max(rl < 0 ? RL0 : rl) + (DS_RED + DS_MEM);
  lds_vu_ptr tgt = lds_at(tgt_);
  if (lane < 2) tgt[lane] = lane ? t : s + 1;
  SMP(0, s, (int)(t - t0)); SMP(1, s, (int)(t - now_q()));
}
__device__ __forceinline__ unsigned ld_flag(__amdgpu_buffer_rsrc_t rs, int off) {
  return __builtin_amdgcn_raw_buffer_load_b32(rs, off, 0, 16);
}

constexpr unsigned long long QK_MAP[8] = {0xbaf9876d543210ecull, 0xa98f76d543210bceull, 0x98f76d543210ecbaull, 0x876fd543210bcea9ull, 0x76fd543210ecba98ull, 0x65df43210bcea987ull, 0x5df43210ecba9876ull, 0xd43f210bcea98765ull};
#define QK_ASM_0 \
  "s_waitcnt lgkmcnt(0)\n\t" \
  "v_subrev_u32 v244, 0x8000, v250\n\t" \
  "ds_read_b128 v[216:219], v248 offset:32768\n\t" \
  "ds_read_b128 v[220:223], v248 offset:33792\n\t" \
  "ds_read_b128 v[224:227], v248 offset:34816\n\t" \
  "ds_read_b128 v[228:231], v248 offset:35840\n\t" \
  "ds_read_b128 v[208:211], v244 offset:0\n\t" \
  "ds_read_b128 v[212:215], v244 offset:1024\n\t" \
  "ds_read_b128 v[236:239], v244 offset:2048\n\t" \
  "ds_read_b128 v[240:243], v244 offset:3072\n\t" \
  "v_mov_b64 v[192:193], 0\n\t" \
  "v_mov_b64 v[194:195], 0\n\t" \
  "v_mov_b64 v[196:197], 0\n\t" \
  "v_mov_b64 v[198:199], 0\n\t" \
  "s_movk_i32 s92, 0x2000\n\t" \
  ".Lwt%=:\n\t" \
  "ds_read_b64 v[200:201], v248 offset:30720\n\t" \
  "s_memrealtime s[90:91]\n\t" \
  "s_sub_u32 s92, s92, 1\n\t" \
  "s_waitcnt lgkmcnt(0)\n\t" \
  "v_readfirstlane_b32 s93, v200\n\t" \
  "v_readfirstlane_b32 %[tgt], v201\n\t" \
  "s_cbranch_scc1 .Lgo%=\n\t" \
  "s_cmp_eq_u32 s93, %[step]\n\t" \
  "s_cbranch_scc1 .Lwx%=\n\t" \
  "s_sleep 1\n\t" \
  "s_branch .Lwt%=\n\t" \
  ".Lwx%=:\n\t" \
  "s_lshl_b32 s93, s90, 2\n\t" \
  "s_sub_i32 s93, %[tgt], s93\n\t" \
  "s_cmp_le_i32 s93, 0\n\t" \
  "s_cbranch_scc1 .Lgo%=\n\t" \
  "s_min_i32 s93, s93, 8000\n\t" \
  "s_mov_b32 s94, s93\n\t" \
  ".Lsl%=:\n\t" \
  "s_sub_i32 s94, s94, 18\n\t" \
  "s_cmp_lt_i32 s94, 30\n\t" \
  "s_cbranch_scc1 .Lsx%=\n\t" \
  "s_sleep 1\n\t" \
  "s_branch .Lsl%=\n\t" \
  ".Lsx%=:\n\t" \
  "s_sub_i32 s93, s93, 12\n\t" \
  "s_max_i32 s93, s93, 4\n\t" \
  "s_lshr_b32 s93, s93, 2\n\t" \
  "s_add_u32 s93, s93, s90\n\t" \
  "s_memrealtime s[90:91]\n\t" \
  ".Lpace%=:\n\t" \
  "s_memrealtime s[94:95]\n\t" \
  "s_waitcnt lgkmcnt(1)\n\t" \
  "s_sub_i32 s92, s90, s93\n\t" \
  "s_cmp_ge_i32 s92, 0\n\t" \
  "s_cbranch_scc1 .Le%=\n\t" \
  "s_memrealtime s[90:91]\n\t" \
  "s_waitcnt lgkmcnt(1)\n\t" \
  "s_sub_i32 s92, s94, s93\n\t" \
  "s_cmp_lt_i32 s92, 0\n\t" \
  "s_cbranch_scc1 .Lpace%=\n\t" \
  "s_nop 4\n\t" \
  ".Le%=:\n\t" \
  "s_add_u32 s92, s92, s93\n\t" \
  "s_lshl_b32 s92, s92, 2\n\t" \
  "s_sub_i32 s92, %[tgt], s92\n\t" \
  "s_max_i32 s92, s92, 0\n\t" \
  "s_bitcmp0_b32 s92, 3\n\t" \
  "s_cbranch_scc1 .Lf3%=\n\t" \
  "s_nop 15\n\t" \
  ".Lf3%=:\n\t" \
  "s_bitcmp0_b32 s92, 2\n\t" \
  "s_cbranch_scc1 .Lf2%=\n\t" \
  "s_nop 10\n\t" \
  ".Lf2%=:\n\t" \
  ".Lgo%=:\n\t" \
  "s_mov_b32 s92, 0\n\t" \
  ".Lpoll%=:\n\t" \
  "buffer_load_dwordx4 v[200:203], v248, %[rs], %[som] offen sc1\n\t" \
  "buffer_load_dwordx4 v[204:207], v248, %[rs], %[som2] offen sc1\n\t" \
  "s_waitcnt vmcnt(1) lgkmcnt(0)\n\t" \
  "ds_write_b128 v249, v[200:203]\n\t" \
  "v_smfmac_f32_16x16x64_bf16 v[192:195], v[200:203], v[208:215], v251\n\t" \
  "v_max3_u32 v244, v200, v201, v202\n\t" \
  "v_smfmac_f32_16x16x64_bf16 v[196:199], v[200:203], v[236:243], v251\n\t" \
  "s_waitcnt vmcnt(0)\n\t" \
  "ds_write_b128 v249, v[204:207] offset:8192\n\t" \
  "v_smfmac_f32_16x16x64_bf16 v[192:195], v[204:207], v[216:223], v251\n\t" \
  "v_max3_u32 v244, v244, v203, v204\n\t" \
  "v_max3_u32 v244, v244, v205, v206\n\t" \
  "v_smfmac_f32_16x16x64_bf16 v[196:199], v[204:207], v[224:231], v251\n\t" \
  "v_max_u32 v244, v244, v207\n\t" \
  "v_cmp_le_u32 vcc, %[sent], v244\n\t" \
  "s_cbranch_vccnz .Lslow%=\n\t" \
  ".Lok%=:\n\t" \
  "ds_read_b128 v[216:219], v250 offset:32768\n\t" \
  "ds_read_b128 v[220:223], v250 offset:33792\n\t" \
  "ds_read_b128 v[224:227], v250 offset:34816\n\t" \
  "ds_read_b128 v[228:231], v250 offset:35840\n\t" \
  "s_waitcnt lgkmcnt(4)\n\t" \
  "s_barrier\n\t" \
  "ds_read_b128 v[208:211], v248 offset:1024\n\t" \
  "ds_read_b128 v[212:215], v248 offset:2048\n\t" \
  "ds_read_b128 v[236:239], v248 offset:3072\n\t" \
  "ds_read_b128 v[240:243], v248 offset:4096\n\t" \
  "s_waitcnt lgkmcnt(3)\n\t" \
  "ds_read_b128 v[244:247], v248 offset:13312\n\t" \
  "v_smfmac_f32_16x16x64_bf16 v[192:195], v[208:211], v[216:223], v251\n\t" \
  "ds_read_b128 v[216:219], v250 offset:0\n\t" \
  "v_smfmac_f32_16x16x64_bf16 v[196:199], v[208:211], v[224:231], v251\n\t" \
  "s_waitcnt lgkmcnt(4)\n\t" \
  "ds_read_b128 v[220:223], v250 offset:1024\n\t" \
  "v_smfmac_f32_16x16x64_bf16 v[192:195], v[212:215], v[0:7], v251\n\t" \
  "ds_read_b128 v[224:227], v250 offset:2048\n\t" \
  "v_smfmac_f32_16x16x64_bf16 v[196:199], v[212:215], v[8:15], v251\n\t" \
  "s_waitcnt lgkmcnt(5)\n\t" \
  "ds_read_b128 v[228:231], v250 offset:3072\n\t" \
  "v_smfmac_f32_16x16x64_bf16 v[192:195], v[236:239], v[16:23], v251\n\t" \
  "ds_read_b128 v[200:203], v248 offset:5120\n\t" \
  "v_smfmac_f32_16x16x64_bf16 v[196:199], v[236:239], v[24:31], v251\n\t" \
  "s_waitcnt lgkmcnt(6)\n\t" \
  "ds_read_b128 v[204:207], v248 offset:6144\n\t" \
  "v_smfmac_f32_16x16x64_bf16 v[192:195], v[240:243], v[32:39], v251\n\t" \
  "ds_read_b128 v[208:211], v248 offset:7168\n\t" \
  "v_smfmac_f32_16x16x64_bf16 v[196:199], v[240:243], v[40:47], v251\n\t" \
  "s_waitcnt lgkmcnt(5)\n\t" \
  "ds_read_b128 v[212:215], v248 offset:9216\n\t" \
  "v_smfmac_f32_16x16x64_bf16 v[192:195], v[244:247], v[216:223], v251\n\t" \
  "s_waitcnt lgkmcnt(4)\n\t" \
  "v_smfmac_f32_16x16x64_bf16 v[196:199], v[244:247], v[224:231], v251\n\t" \
  "s_waitcnt lgkmcnt(3)\n\t" \
  "ds_read_b128 v[236:239], v248 offset:10240\n\t" \
  "v_smfmac_f32_16x16x64_bf16 v[192:195], v[200:203], v[48:55], v251\n\t" \
  "ds_read_b128 v[240:243], v248 offset:11264\n\t" \
  "v_smfmac_f32_16x16x64_bf16 v[196:199], v[200:203], v[56:63], v251\n\t" \
  "s_waitcnt lgkmcnt(4)\n\t" \
  "v_smfmac_f32_16x16x64_bf16 v[192:195], v[204:207], v[64:71], v251\n\t" \
  "ds_read_b128 v[244:247], v248 offset:12288\n\t" \
  "v_smfmac_f32_16x16x64_bf16 v[196:199], v[204:207], v[72:79], v251\n\t" \
  "s_waitcnt lgkmcnt(4)\n\t" \
  "v_smfmac_f32_16x16x64_bf16 v[192:195], v[208:211], v[80:87], v251\n\t" \
  "ds_read_b128 v[200:203], v248 offset:14336\n\t" \
  "v_smfmac_f32_16x16x64_bf16 v[196:199], v[208:211], v[88:95], v251\n\t" \
  "s_waitcnt lgkmcnt(4)\n\t" \
  "v_smfmac_f32_16x16x64_bf16 v[192:195], v[212:215], v[96:103], v251\n\t" \
  "ds_read_b128 v[204:207], v248 offset:15360\n\t" \
  "v_smfmac_f32_16x16x64_bf16 v[196:199], v[212:215], v[104:111], v251\n\t" \
  "s_waitcnt lgkmcnt(3)\n\t" \
  "v_smfmac_f32_16x16x64_bf16 v[192:195], v[236:239], v[112:119], v251\n\t" \
  "v_smfmac_f32_16x16x64_bf16 v[196:199], v[236:239], v[120:127], v251\n\t" \
  "v_smfmac_f32_16x16x64_bf16 v[192:195], v[240:243], v[128:135], v251\n\t" \
  "v_smfmac_f32_16x16x64_bf16 v[196:199], v[240:243], v[136:143], v251\n\t" \
  "s_waitcnt lgkmcnt(1)\n\t" \
  "v_smfmac_f32_16x16x64_bf16 v[192:195], v[244:247], v[144:151], v251\n\t" \
  "v_smfmac_f32_16x16x64_bf16 v[196:199], v[244:247], v[152:159], v251\n\t" \
  "v_smfmac_f32_16x16x64_bf16 v[192:195], v[200:203], v[160:167], v251\n\t" \
  "v_smfmac_f32_16x16x64_bf16 v[196:199], v[200:203], v[168:175], v251\n\t" \
  "s_waitcnt lgkmcnt(0)\n\t" \
  "v_smfmac_f32_16x16x64_bf16 v[192:195], v[204:207], v[176:183], v251\n\t" \
  "v_smfmac_f32_16x16x64_bf16 v[196:199], v[204:207], v[184:191], v251\n\t" \
  "ds_read_b128 v[200:203], v248 offset:16384\n\t" \
  "ds_read_b128 v[204:207], v248 offset:20480\n\t" \
  "v_mov_b32 v250, v232\n\t" \
  "v_mov_b32 v249, v233\n\t" \
  "s_nop 2\n\t" \
  "v_add_f32 v192, v192, v194\n\t" \
  "v_add_f32 v193, v193, v195\n\t" \
  "v_add_f32 v196, v196, v198\n\t" \
  "v_add_f32 v197, v197, v199\n\t" \
  "v_cvt_pk_bf16_f32 v192, v192, v193\n\t" \
  "v_cvt_pk_bf16_f32 v196, v196, v197\n\t" \
  "ds_write2_b32 v252, v192, v196 offset1:64\n\t" \
  "v_mov_b32 v251, v234\n\t" \
  "ds_read_b64_tr_b16 v[232:233], v250 offset:0\n\t" \
  "ds_read_b64_tr_b16 v[234:235], v250 offset:64\n\t" \
  "v_mov_b32 v212, %[step]\n\t" \
  "s_mov_b64 exec, 1\n\t" \
  "ds_write_b32 v248, v212 offset:28672\n\t" \
  "s_mov_b64 exec, -1\n\t" \
  "buffer_load_dwordx4 v[192:195], v248, %[rt], %[svt] offen offset:0 sc1\n\t" \
  "buffer_load_dwordx4 v[196:199], v248, %[rt], %[svt] offen offset:1024 sc1\n\t" \
  "s_waitcnt lgkmcnt(1)\n\t" \
  "v_mfma_f32_16x16x32_bf16 v[200:203], v[200:203], v[232:235], 0\n\t" \
  "v_mfma_f32_16x16x32_bf16 v[204:207], v[204:207], v[232:235], 0\n\t" \
  "s_nop 7\n\t" \
  "v_cndmask_b32 v216, v200, v204, %[nh]\n\t" \
  "v_cndmask_b32 v217, v201, v205, %[nh]\n\t" \
  "v_cndmask_b32 v218, v202, v206, %[nh]\n\t" \
  "v_cndmask_b32 v219, v203, v207, %[nh]\n\t" \
  "ds_write_b128 v248, v[216:219] offset:16384\n\t" \
  "buffer_load_dword v252, v249, %[rs], %[sv] offen offset:0 sc1\n\t" \
  "buffer_load_dword v253, v249, %[rs], %[sv] offen offset:256 sc1\n\t" \
  "ds_read_b32 v212, v248 offset:28672\n\t" \
  "ds_read_b64_tr_b16 v[236:237], v250 offset:512\n\t" \
  "ds_read_b64_tr_b16 v[238:239], v250 offset:576\n\t" \
  "ds_read_b64_tr_b16 v[240:241], v250 offset:1024\n\t" \
  "ds_read_b64_tr_b16 v[242:243], v250 offset:1088\n\t" \
  "ds_read_b64_tr_b16 v[244:245], v250 offset:1536\n\t" \
  "ds_read_b64_tr_b16 v[246:247], v250 offset:1600\n\t" \
  "s_setprio 3\n\t" \
  "s_waitcnt lgkmcnt(0)\n\t" \
  "v_cmp_ne_u32 vcc, %[step], v212\n\t" \
  "s_barrier\n\t" \
  "s_and_b32 s93, vcc_lo, 0xf\n\t" \
  "s_cbranch_scc1 .Lre%=\n\t" \
  ".Lrb%=:\n\t" \
  "ds_read_b64_tr_b16 v[200:201], v250 offset:2048\n\t" \
  "ds_read_b64_tr_b16 v[202:203], v250 offset:2112\n\t" \
  "ds_read_b64_tr_b16 v[204:205], v250 offset:2560\n\t" \
  "ds_read_b64_tr_b16 v[206:207], v250 offset:2624\n\t" \
  "ds_read_b128 v[220:223], v248 offset:17408\n\t" \
  "ds_read_b128 v[224:227], v248 offset:18432\n\t" \
  "ds_read_b128 v[228:231], v248 offset:19456\n\t" \
  "ds_read_b64_tr_b16 v[208:209], v250 offset:3072\n\t" \
  "ds_read_b64_tr_b16 v[210:211], v250 offset:3136\n\t" \
  "ds_read_b64_tr_b16 v[212:213], v250 offset:3584\n\t" \
  "ds_read_b64_tr_b16 v[214:215], v250 offset:3648\n\t" \
  "s_waitcnt lgkmcnt(9)\n\t" \
  "v_mfma_f32_16x16x32_bf16 v[200:203], v[200:203], v[232:235], 0\n\t" \
  "s_waitcnt lgkmcnt(7)\n\t" \
  "v_mfma_f32_16x16x32_bf16 v[200:203], v[204:207], v[236:239], v[200:203]\n\t" \
  "s_waitcnt lgkmcnt(6)\n\t" \
  "v_pk_add_f32 v[216:217], v[216:217], v[220:221]\n\t" \
  "v_pk_add_f32 v[218:219], v[218:219], v[222:223]\n\t" \
  "s_waitcnt lgkmcnt(4)\n\t" \
  "v_pk_add_f32 v[224:225], v[224:225], v[228:229]\n\t" \
  "v_pk_add_f32 v[226:227], v[226:227], v[230:231]\n\t" \
  "v_pk_add_f32 v[216:217], v[216:217], v[224:225]\n\t" \
  "v_pk_add_f32 v[218:219], v[218:219], v[226:227]\n\t" \
  "s_waitcnt lgkmcnt(2)\n\t" \
  "v_mfma_f32_16x16x32_bf16 v[200:203], v[208:211], v[240:243], v[200:203]\n\t" \
  "v_max3_f32 v224, v216, v217, v218\n\t" \
  "v_max_f32 v224, v224, v219\n\t" \
  "s_waitcnt lgkmcnt(0)\n\t" \
  "v_mfma_f32_16x16x32_bf16 v[200:203], v[212:215], v[244:247], v[200:203]\n\t" \
  "v_max_f32_dpp v225, v224, v224 row_ror:8 row_mask:0xf bank_mask:0xf\n\t" \
  "v_max_f32_dpp v226, v224, v224 row_ror:8 row_mask:0xf bank_mask:0xf\n\t" \
  "s_nop 1\n\t" \
  "v_permlane32_swap_b32 v225, v226\n\t" \
  "v_max_f32 v231, v225, v226\n\t" \
  "v_mov_b32 v220, 0x3f803f80\n\t" \
  "v_mov_b32 v221, 0x3f803f80\n\t" \
  "v_mov_b64 v[222:223], v[220:221]\n\t" \
  "v_mov_b32 v233, 0\n\t" \
  "v_mov_b32 v234, 0\n\t" \
  "v_max3_f32 v204, v200, v201, v231\n\t" \
  "v_max3_f32 v205, v204, v202, v203\n\t" \
  "v_max3_f32 v206, v204, v202, v203\n\t" \
  "v_cndmask_b32 v200, v200, v202, %[up]\n\t" \
  "v_cndmask_b32 v201, v201, v203, %[up]\n\t" \
  "v_permlane16_swap_b32 v205, v206\n\t" \
  "v_max_f32 v204, v205, v206\n\t" \
  "v_mul_f32 v204, %[negc], v204\n\t" \
  "v_fma_f32 v200, v200, %[c], v204\n\t" \
  "v_fma_f32 v201, v201, %[c], v204\n\t" \
  "v_exp_f32 v200, v200\n\t" \
  "v_fma_f32 v216, v216, %[c], v204\n\t" \
  "v_exp_f32 v201, v201\n\t" \
  "v_fma_f32 v217, v217, %[c], v204\n\t" \
  "v_exp_f32 v216, v216\n\t" \
  "v_fma_f32 v218, v218, %[c], v204\n\t" \
  "v_cvt_pk_bf16_f32 v232, v200, v201\n\t" \
  "v_cvt_pk_bf16_f32 v235, v200, v201\n\t" \
  "v_exp_f32 v217, v217\n\t" \
  "v_fma_f32 v219, v219, %[c], v204\n\t" \
  "v_mfma_f32_16x16x16_bf16 v[212:215], v[220:221], v[232:233], 0\n\t" \
  "v_exp_f32 v218, v218\n\t" \
  "s_waitcnt vmcnt(0)\n\t" \
  "v_mfma_f32_16x16x16_bf16 v[224:227], v[252:253], v[232:233], 0\n\t" \
  "v_cvt_pk_bf16_f32 v210, v216, v217\n\t" \
  "v_cvt_pk_bf16_f32 v208, v216, v217\n\t" \
  "v_exp_f32 v219, v219\n\t" \
  "v_mfma_f32_16x16x16_bf16 v[228:231], v[252:253], v[234:235], 0\n\t" \
  "v_cvt_pk_bf16_f32 v211, v218, v219\n\t" \
  "v_cvt_pk_bf16_f32 v209, v218, v219\n\t" \
  "v_mov_b32_dpp v208, v210 row_ror:8 row_mask:0xf bank_mask:0xc\n\t" \
  "v_mov_b32_dpp v210, v210 row_ror:8 row_mask:0xf bank_mask:0x3\n\t" \
  "v_mov_b32_dpp v209, v211 row_ror:8 row_mask:0xf bank_mask:0xc\n\t" \
  "v_mov_b32_dpp v211, v211 row_ror:8 row_mask:0xf bank_mask:0x3\n\t" \
  "v_max_u32 v204, v252, v253\n\t" \
  "v_cmp_le_u32 vcc, %[sent], v204\n\t" \
  "v_mfma_f32_16x16x32_bf16 v[212:215], v[220:223], v[208:211], v[212:215]\n\t" \
  ".Lpv%=:\n\t" \
  "v_mfma_f32_16x16x32_bf16 v[224:227], v[192:195], v[208:211], v[224:227]\n\t" \
  "v_mfma_f32_16x16x32_bf16 v[228:231], v[196:199], v[208:211], v[228:231]\n\t" \
  "s_cbranch_vccnz .Lvs%=\n\t" \
  ".Lvok%=:\n\t" \
  "s_nop 4\n\t" \
  "v_rcp_f32 v204, v212\n\t" \
  "v_cndmask_b32 v224, v224, v226, %[nh]\n\t" \
  "v_cndmask_b32 v225, v225, v227, %[nh]\n\t" \
  "v_cndmask_b32 v228, v228, v230, %[nh]\n\t" \
  "v_cndmask_b32 v229, v229, v231, %[nh]\n\t" \
  "v_pk_mul_f32 v[224:225], v[224:225], v[204:205] op_sel_hi:[1,0]\n\t" \
  "v_pk_mul_f32 v[228:229], v[228:229], v[204:205] op_sel_hi:[1,0]\n\t" \
  "v_cvt_pk_bf16_f32 v224, v224, v225\n\t" \
  "v_cvt_pk_bf16_f32 v225, v228, v229\n\t" \
  "buffer_store_dwordx2 v[224:225], v251, %[rs], %[so] offen sc1\n\t" \
  "s_memtime s[90:91]\n\t" \
  "s_setprio 0\n\t" \
  "s_waitcnt lgkmcnt(0)\n\t" \
  "s_mov_b32 %[tout], s90\n\t" \
  "s_mov_b32 %[spins], s92\n\t" \
  "s_branch .Lend%=\n\t" \
  ".Lre%=:\n\t" \
  "ds_read_b64_tr_b16 v[236:237], v250 offset:512\n\t" \
  "ds_read_b64_tr_b16 v[238:239], v250 offset:576\n\t" \
  "ds_read_b64_tr_b16 v[240:241], v250 offset:1024\n\t" \
  "ds_read_b64_tr_b16 v[242:243], v250 offset:1088\n\t" \
  "ds_read_b64_tr_b16 v[244:245], v250 offset:1536\n\t" \
  "ds_read_b64_tr_b16 v[246:247], v250 offset:1600\n\t" \
  "s_waitcnt lgkmcnt(0)\n\t" \
  "s_branch .Lrb%=\n\t" \
  ".Lvs%=:\n\t" \
  "v_cmp_eq_u32 vcc, %[sent], v252\n\t" \
  "v_cmp_eq_u32 s[94:95], %[sent], v253\n\t" \
  "s_or_b64 s[94:95], s[94:95], vcc\n\t" \
  "s_cbranch_scc0 .Lvok%=\n\t" \
  "s_add_u32 s92, s92, 1\n\t" \
  "s_sleep 2\n\t" \
  "s_and_b32 s93, s92, 63\n\t" \
  "s_cbranch_scc1 .Lvr%=\n\t" \
  "buffer_load_dword v204, off, %[rs], %[sab] sc1\n\t" \
  "s_waitcnt vmcnt(0)\n\t" \
  "v_readfirstlane_b32 s93, v204\n\t" \
  "s_nop 3\n\t" \
  "s_cmp_eq_u32 s93, 1\n\t" \
  "s_cbranch_scc1 .Lvok%=\n\t" \
  "s_cmp_lt_u32 s92, 0x80000\n\t" \
  "s_cbranch_scc1 .Lvr%=\n\t" \
  "v_mov_b32 v204, 1\n\t" \
  "buffer_store_dword v204, off, %[rs], %[sab] sc1\n\t" \
  "s_branch .Lvok%=\n\t" \
  ".Lvr%=:\n\t" \
  "buffer_load_dword v252, v249, %[rs], %[sv] offen offset:0 sc1\n\t" \
  "buffer_load_dword v253, v249, %[rs], %[sv] offen offset:256 sc1\n\t" \
  "s_waitcnt vmcnt(0)\n\t" \
  "v_mfma_f32_16x16x16_bf16 v[224:227], v[252:253], v[232:233], 0\n\t" \
  "v_mfma_f32_16x16x16_bf16 v[228:231], v[252:253], v[234:235], 0\n\t" \
  "v_max_u32 v204, v252, v253\n\t" \
  "v_cmp_le_u32 vcc, %[sent], v204\n\t" \
  "s_nop 4\n\t" \
  "s_branch .Lpv%=\n\t" \
  "s_branch .Lend%=\n\t" \
  ".Lslow%=:\n\t" \
  "s_mov_b64 s[94:95], 0\n\t" \
  "v_cmp_eq_u32 vcc, %[sent], v200\n\t" \
  "s_or_b64 s[94:95], s[94:95], vcc\n\t" \
  "v_cmp_eq_u32 vcc, %[sent], v201\n\t" \
  "s_or_b64 s[94:95], s[94:95], vcc\n\t" \
  "v_cmp_eq_u32 vcc, %[sent], v202\n\t" \
  "s_or_b64 s[94:95], s[94:95], vcc\n\t" \
  "v_cmp_eq_u32 vcc, %[sent], v203\n\t" \
  "s_or_b64 s[94:95], s[94:95], vcc\n\t" \
  "v_cmp_eq_u32 vcc, %[sent], v204\n\t" \
  "s_or_b64 s[94:95], s[94:95], vcc\n\t" \
  "v_cmp_eq_u32 vcc, %[sent], v205\n\t" \
  "s_or_b64 s[94:95], s[94:95], vcc\n\t" \
  "v_cmp_eq_u32 vcc, %[sent], v206\n\t" \
  "s_or_b64 s[94:95], s[94:95], vcc\n\t" \
  "v_cmp_eq_u32 vcc, %[sent], v207\n\t" \
  "s_or_b64 s[94:95], s[94:95], vcc\n\t" \
  "s_cmp_eq_u64 s[94:95], 0\n\t" \
  "s_cbranch_scc1 .Lok%=\n\t" \
  "v_mov_b64 v[192:193], 0\n\t" \
  "v_mov_b64 v[194:195], 0\n\t" \
  "v_mov_b64 v[196:197], 0\n\t" \
  "v_mov_b64 v[198:199], 0\n\t" \
  "s_add_u32 s92, s92, 1\n\t" \
  "s_sleep 6\n\t" \
  "s_cmp_eq_u32 s92, 1\n\t" \
  "s_cbranch_scc1 .Lpoll%=\n\t" \
  "s_sleep 1\n\t" \
  "s_and_b32 s93, s92, 63\n\t" \
  "s_cmp_lg_u32 s93, 0\n\t" \
  "s_cbranch_scc1 .Lpoll%=\n\t" \
  "v_mov_b32 v245, 0\n\t" \
  "buffer_load_dword v244, v245, %[rs], %[sab] offen sc1\n\t" \
  "s_waitcnt vmcnt(0)\n\t" \
  "v_readfirstlane_b32 s93, v244\n\t" \
  "s_nop 3\n\t" \
  "s_cmp_eq_u32 s93, 1\n\t" \
  "s_cbranch_scc1 .Lok%=\n\t" \
  "s_cmp_lt_u32 s92, 0x80000\n\t" \
  "s_cbranch_scc1 .Lpoll%=\n\t" \
  "v_mov_b32 v244, 1\n\t" \
  "buffer_store_dword v244, v245, %[rs], %[sab] offen sc1\n\t" \
  "s_branch .Lok%=\n\t" \
  ".Lend%=:\n\t"
#define QK_ASM_1 \
  "s_waitcnt lgkmcnt(0)\n\t" \
  "v_subrev_u32 v244, 0x8000, v250\n\t" \
  "ds_read_b128 v[216:219], v248 offset:36864\n\t" \
  "ds_read_b128 v[220:223], v248 offset:37888\n\t" \
  "ds_read_b128 v[224:227], v248 offset:38912\n\t" \
  "ds_read_b128 v[228:231], v248 offset:39936\n\t" \
  "ds_read_b128 v[208:211], v244 offset:0\n\t" \
  "ds_read_b128 v[212:215], v244 offset:1024\n\t" \
  "ds_read_b128 v[236:239], v244 offset:2048\n\t" \
  "ds_read_b128 v[240:243], v244 offset:3072\n\t" \
  "v_mov_b64 v[192:193], 0\n\t" \
  "v_mov_b64 v[194:195], 0\n\t" \
  "v_mov_b64 v[196:197], 0\n\t" \
  "v_mov_b64 v[198:199], 0\n\t" \
  "s_movk_i32 s92, 0x2000\n\t" \
  ".Lwt%=:\n\t" \
  "ds_read_b64 v[200:201], v248 offset:30720\n\t" \
  "s_memrealtime s[90:91]\n\t" \
  "s_sub_u32 s92, s92, 1\n\t" \
  "s_waitcnt lgkmcnt(0)\n\t" \
  "v_readfirstlane_b32 s93, v200\n\t" \
  "v_readfirstlane_b32 %[tgt], v201\n\t" \
  "s_cbranch_scc1 .Lgo%=\n\t" \
  "s_cmp_eq_u32 s93, %[step]\n\t" \
  "s_cbranch_scc1 .Lwx%=\n\t" \
  "s_sleep 1\n\t" \
  "s_branch .Lwt%=\n\t" \
  ".Lwx%=:\n\t" \
  "s_lshl_b32 s93, s90, 2\n\t" \
  "s_sub_i32 s93, %[tgt], s93\n\t" \
  "s_cmp_le_i32 s93, 0\n\t" \
  "s_cbranch_scc1 .Lgo%=\n\t" \
  "s_min_i32 s93, s93, 8000\n\t" \
  "s_mov_b32 s94, s93\n\t" \
  ".Lsl%=:\n\t" \
  "s_sub_i32 s94, s94, 18\n\t" \
  "s_cmp_lt_i32 s94, 30\n\t" \
  "s_cbranch_scc1 .Lsx%=\n\t" \
  "s_sleep 1\n\t" \
  "s_branch .Lsl%=\n\t" \
  ".Lsx%=:\n\t" \
  "s_sub_i32 s93, s93, 12\n\t" \
  "s_max_i32 s93, s93, 4\n\t" \
  "s_lshr_b32 s93, s93, 2\n\t" \
  "s_add_u32 s93, s93, s90\n\t" \
  "s_memrealtime s[90:91]\n\t" \
  ".Lpace%=:\n\t" \
  "s_memrealtime s[94:95]\n\t" \
  "s_waitcnt lgkmcnt(1)\n\t" \
  "s_sub_i32 s92, s90, s93\n\t" \
  "s_cmp_ge_i32 s92, 0\n\t" \
  "s_cbranch_scc1 .Le%=\n\t" \
  "s_memrealtime s[90:91]\n\t" \
  "s_waitcnt lgkmcnt(1)\n\t" \
  "s_sub_i32 s92, s94, s93\n\t" \
  "s_cmp_lt_i32 s92, 0\n\t" \
  "s_cbranch_scc1 .Lpace%=\n\t" \
  "s_nop 4\n\t" \
  ".Le%=:\n\t" \
  "s_add_u32 s92, s92, s93\n\t" \
  "s_lshl_b32 s92, s92, 2\n\t" \
  "s_sub_i32 s92, %[tgt], s92\n\t" \
  "s_max_i32 s92, s92, 0\n\t" \
  "s_bitcmp0_b32 s92, 3\n\t" \
  "s_cbranch_scc1 .Lf3%=\n\t" \
  "s_nop 15\n\t" \
  ".Lf3%=:\n\t" \
  "s_bitcmp0_b32 s92, 2\n\t" \
  "s_cbranch_scc1 .Lf2%=\n\t" \
  "s_nop 10\n\t" \
  ".Lf2%=:\n\t" \
  ".Lgo%=:\n\t" \
  "s_mov_b32 s92, 0\n\t" \
  ".Lpoll%=:\n\t" \
  "buffer_load_dwordx4 v[200:203], v248, %[rs], %[som] offen sc1\n\t" \
  "buffer_load_dwordx4 v[204:207], v248, %[rs], %[som2] offen sc1\n\t" \
  "s_waitcnt vmcnt(1) lgkmcnt(0)\n\t" \
  "ds_write_b128 v249, v[200:203]\n\t" \
  "v_smfmac_f32_16x16x64_bf16 v[192:195], v[200:203], v[208:215], v251\n\t" \
  "v_max3_u32 v244, v200, v201, v202\n\t" \
  "v_smfmac_f32_16x16x64_bf16 v[196:199], v[200:203], v[236:243], v251\n\t" \
  "s_waitcnt vmcnt(0)\n\t" \
  "ds_write_b128 v249, v[204:207] offset:8192\n\t" \
  "v_smfmac_f32_16x16x64_bf16 v[192:195], v[204:207], v[216:223], v251\n\t" \
  "v_max3_u32 v244, v244, v203, v204\n\t" \
  "v_max3_u32 v244, v244, v205, v206\n\t" \
  "v_smfmac_f32_16x16x64_bf16 v[196:199], v[204:207], v[224:231], v251\n\t" \
  "v_max_u32 v244, v244, v207\n\t" \
  "v_cmp_le_u32 vcc, %[sent], v244\n\t" \
  "s_cbranch_vccnz .Lslow%=\n\t" \
  ".Lok%=:\n\t" \
  "ds_read_b128 v[216:219], v250 offset:32768\n\t" \
  "ds_read_b128 v[220:223], v250 offset:33792\n\t" \
  "ds_read_b128 v[224:227], v250 offset:34816\n\t" \
  "ds_read_b128 v[228:231], v250 offset:35840\n\t" \
  "s_waitcnt lgkmcnt(4)\n\t" \
  "s_barrier\n\t" \
  "ds_read_b128 v[208:211], v248 offset:0\n\t" \
  "ds_read_b128 v[212:215], v248 offset:3072\n\t" \
  "ds_read_b128 v[236:239], v248 offset:4096\n\t" \
  "ds_read_b128 v[240:243], v248 offset:5120\n\t" \
  "s_waitcnt lgkmcnt(3)\n\t" \
  "ds_read_b128 v[244:247], v248 offset:12288\n\t" \
  "v_smfmac_f32_16x16x64_bf16 v[192:195], v[208:211], v[216:223], v251\n\t" \
  "ds_read_b128 v[216:219], v250 offset:0\n\t" \
  "v_smfmac_f32_16x16x64_bf16 v[196:199], v[208:211], v[224:231], v251\n\t" \
  "s_waitcnt lgkmcnt(4)\n\t" \
  "ds_read_b128 v[220:223], v250 offset:1024\n\t" \
  "v_smfmac_f32_16x16x64_bf16 v[192:195], v[212:215], v[0:7], v251\n\t" \
  "ds_read_b128 v[224:227], v250 offset:2048\n\t" \
  "v_smfmac_f32_16x16x64_bf16 v[196:199], v[212:215], v[8:15], v251\n\t" \
  "s_waitcnt lgkmcnt(5)\n\t" \
  "ds_read_b128 v[228:231], v250 offset:3072\n\t" \
  "v_smfmac_f32_16x16x64_bf16 v[192:195], v[236:239], v[16:23], v251\n\t" \
  "ds_read_b128 v[200:203], v248 offset:6144\n\t" \
  "v_smfmac_f32_16x16x64_bf16 v[196:199], v[236:239], v[24:31], v251\n\t" \
  "s_waitcnt lgkmcnt(6)\n\t" \
  "ds_read_b128 v[204:207], v248 offset:7168\n\t" \
  "v_smfmac_f32_16x16x64_bf16 v[192:195], v[240:243], v[32:39], v251\n\t" \
  "ds_read_b128 v[208:211], v248 offset:8192\n\t" \
  "v_smfmac_f32_16x16x64_bf16 v[196:199], v[240:243], v[40:47], v251\n\t" \
  "s_waitcnt lgkmcnt(5)\n\t" \
  "ds_read_b128 v[212:215], v248 offset:10240\n\t" \
  "v_smfmac_f32_16x16x64_bf16 v[192:195], v[244:247], v[216:223], v251\n\t" \
  "s_waitcnt lgkmcnt(4)\n\t" \
  "v_smfmac_f32_16x16x64_bf16 v[196:199], v[244:247], v[224:231], v251\n\t" \
  "s_waitcnt lgkmcnt(3)\n\t" \
  "ds_read_b128 v[236:239], v248 offset:11264\n\t" \
  "v_smfmac_f32_16x16x64_bf16 v[192:195], v[200:203], v[48:55], v251\n\t" \
  "ds_read_b128 v[240:243], v248 offset:13312\n\t" \
  "v_smfmac_f32_16x16x64_bf16 v[196:199], v[200:203], v[56:63], v251\n\t" \
  "s_waitcnt lgkmcnt(4)\n\t" \
  "v_smfmac_f32_16x16x64_bf16 v[192:195], v[204:207], v[64:71], v251\n\t" \
  "ds_read_b128 v[244:247], v248 offset:14336\n\t" \
  "v_smfmac_f32_16x16x64_bf16 v[196:199], v[204:207], v[72:79], v251\n\t" \
  "s_waitcnt lgkmcnt(4)\n\t" \
  "v_smfmac_f32_16x16x64_bf16 v[192:195], v[208:211], v[80:87], v251\n\t" \
  "ds_read_b128 v[200:203], v248 offset:15360\n\t" \
  "v_smfmac_f32_16x16x64_bf16 v[196:199], v[208:211], v[88:95], v251\n\t" \
  "s_waitcnt lgkmcnt(4)\n\t" \
  "v_smfmac_f32_16x16x64_bf16 v[192:195], v[212:215], v[96:103], v251\n\t" \
  "ds_read_b128 v[204:207], v248 offset:2048\n\t" \
  "v_smfmac_f32_16x16x64_bf16 v[196:199], v[212:215], v[104:111], v251\n\t" \
  "s_waitcnt lgkmcnt(3)\n\t" \
  "v_smfmac_f32_16x16x64_bf16 v[192:195], v[236:239], v[112:119], v251\n\t" \
  "v_smfmac_f32_16x16x64_bf16 v[196:199], v[236:239], v[120:127], v251\n\t" \
  "v_smfmac_f32_16x16x64_bf16 v[192:195], v[240:243], v[128:135], v251\n\t" \
  "v_smfmac_f32_16x16x64_bf16 v[196:199], v[240:243], v[136:143], v251\n\t" \
  "s_waitcnt lgkmcnt(1)\n\t" \
  "v_smfmac_f32_16x16x64_bf16 v[192:195], v[244:247], v[144:151], v251\n\t" \
  "v_smfmac_f32_16x16x64_bf16 v[196:199], v[244:247], v[152:159], v251\n\t" \
  "v_smfmac_f32_16x16x64_bf16 v[192:195], v[200:203], v[160:167], v251\n\t" \
  "v_smfmac_f32_16x16x64_bf16 v[196:199], v[200:203], v[168:175], v251\n\t" \
  "s_waitcnt lgkmcnt(0)\n\t" \
  "v_smfmac_f32_16x16x64_bf16 v[192:195], v[204:207], v[176:183], v251\n\t" \
  "v_smfmac_f32_16x16x64_bf16 v[196:199], v[204:207], v[184:191], v251\n\t" \
  "ds_read_b128 v[200:203], v248 offset:17408\n\t" \
  "ds_read_b128 v[204:207], v248 offset:21504\n\t" \
  "v_mov_b32 v250, v232\n\t" \
  "v_mov_b32 v249, v233\n\t" \
  "s_nop 2\n\t" \
  "v_add_f32 v192, v192, v194\n\t" \
  "v_add_f32 v193, v193, v195\n\t" \
  "v_add_f32 v196, v196, v198\n\t" \
  "v_add_f32 v197, v197, v199\n\t" \
  "v_cvt_pk_bf16_f32 v192, v192, v193\n\t" \
  "v_cvt_pk_bf16_f32 v196, v196, v197\n\t" \
  "ds_write2_b32 v252, v192, v196 offset1:64\n\t" \
  "v_mov_b32 v251, v234\n\t" \
  "ds_read_b64_tr_b16 v[236:237], v250 offset:512\n\t" \
  "ds_read_b64_tr_b16 v[238:239], v250 offset:576\n\t" \
  "v_mov_b32 v212, %[step]\n\t" \
  "s_mov_b64 exec, 1\n\t" \
  "ds_write_b32 v248, v212 offset:28688\n\t" \
  "s_mov_b64 exec, -1\n\t" \
  "buffer_load_dwordx4 v[192:195], v248, %[rt], %[svt] offen offset:0 sc1\n\t" \
  "buffer_load_dwordx4 v[196:199], v248, %[rt], %[svt] offen offset:1024 sc1\n\t" \
  "s_waitcnt lgkmcnt(1)\n\t" \
  "v_mfma_f32_16x16x32_bf16 v[200:203], v[200:203], v[236:239], 0\n\t" \
  "v_mfma_f32_16x16x32_bf16 v[204:207], v[204:207], v[236:239], 0\n\t" \
  "s_nop 7\n\t" \
  "v_cndmask_b32 v216, v200, v204, %[nh]\n\t" \
  "v_cndmask_b32 v217, v201, v205, %[nh]\n\t" \
  "v_cndmask_b32 v218, v202, v206, %[nh]\n\t" \
  "v_cndmask_b32 v219, v203, v207, %[nh]\n\t" \
  "ds_write_b128 v248, v[216:219] offset:17408\n\t" \
  "buffer_load_dword v252, v249, %[rs], %[sv] offen offset:0 sc1\n\t" \
  "buffer_load_dword v253, v249, %[rs], %[sv] offen offset:256 sc1\n\t" \
  "ds_read_b32 v212, v248 offset:28672\n\t" \
  "ds_read_b64_tr_b16 v[240:241], v250 offset:1024\n\t" \
  "ds_read_b64_tr_b16 v[242:243], v250 offset:1088\n\t" \
  "ds_read_b64_tr_b16 v[244:245], v250 offset:1536\n\t" \
  "ds_read_b64_tr_b16 v[246:247], v250 offset:1600\n\t" \
  "ds_read_b64_tr_b16 v[232:233], v250 offset:0\n\t" \
  "ds_read_b64_tr_b16 v[234:235], v250 offset:64\n\t" \
  "s_setprio 3\n\t" \
  "s_waitcnt lgkmcnt(0)\n\t" \
  "v_cmp_ne_u32 vcc, %[step], v212\n\t" \
  "s_barrier\n\t" \
  "s_and_b32 s93, vcc_lo, 0xf\n\t" \
  "s_cbranch_scc1 .Lre%=\n\t" \
  ".Lrb%=:\n\t" \
  "ds_read_b64_tr_b16 v[204:205], v250 offset:2560\n\t" \
  "ds_read_b64_tr_b16 v[206:207], v250 offset:2624\n\t" \
  "ds_read_b64_tr_b16 v[208:209], v250 offset:3072\n\t" \
  "ds_read_b64_tr_b16 v[210:211], v250 offset:3136\n\t" \
  "ds_read_b128 v[220:223], v248 offset:18432\n\t" \
  "ds_read_b128 v[224:227], v248 offset:19456\n\t" \
  "ds_read_b128 v[228:231], v248 offset:16384\n\t" \
  "ds_read_b64_tr_b16 v[212:213], v250 offset:3584\n\t" \
  "ds_read_b64_tr_b16 v[214:215], v250 offset:3648\n\t" \
  "ds_read_b64_tr_b16 v[200:201], v250 offset:2048\n\t" \
  "ds_read_b64_tr_b16 v[202:203], v250 offset:2112\n\t" \
  "s_waitcnt lgkmcnt(9)\n\t" \
  "v_mfma_f32_16x16x32_bf16 v[204:207], v[204:207], v[236:239], 0\n\t" \
  "s_waitcnt lgkmcnt(7)\n\t" \
  "v_mfma_f32_16x16x32_bf16 v[204:207], v[208:211], v[240:243], v[204:207]\n\t" \
  "s_waitcnt lgkmcnt(6)\n\t" \
  "v_pk_add_f32 v[216:217], v[216:217], v[220:221]\n\t" \
  "v_pk_add_f32 v[218:219], v[218:219], v[222:223]\n\t" \
  "s_waitcnt lgkmcnt(4)\n\t" \
  "v_pk_add_f32 v[224:225], v[224:225], v[228:229]\n\t" \
  "v_pk_add_f32 v[226:227], v[226:227], v[230:231]\n\t" \
  "v_pk_add_f32 v[216:217], v[216:217], v[224:225]\n\t" \
  "v_pk_add_f32 v[218:219], v[218:219], v[226:227]\n\t" \
  "s_waitcnt lgkmcnt(2)\n\t" \
  "v_mfma_f32_16x16x32_bf16 v[204:207], v[212:215], v[244:247], v[204:207]\n\t" \
  "v_max3_f32 v224, v216, v217, v218\n\t" \
  "v_max_f32 v224, v224, v219\n\t" \
  "s_waitcnt lgkmcnt(0)\n\t" \
  "v_mfma_f32_16x16x32_bf16 v[204:207], v[200:203], v[232:235], v[204:207]\n\t" \
  "v_max_f32_dpp v225, v224, v224 row_ror:8 row_mask:0xf bank_mask:0xf\n\t" \
  "v_max_f32_dpp v226, v224, v224 row_ror:8 row_mask:0xf bank_mask:0xf\n\t" \
  "s_nop 1\n\t" \
  "v_permlane32_swap_b32 v225, v226\n\t" \
  "v_max_f32 v231, v225, v226\n\t" \
  "v_mov_b32 v220, 0x3f803f80\n\t" \
  "v_mov_b32 v221, 0x3f803f80\n\t" \
  "v_mov_b64 v[222:223], v[220:221]\n\t" \
  "v_mov_b32 v237, 0\n\t" \
  "v_mov_b32 v238, 0\n\t" \
  "v_max3_f32 v208, v204, v205, v231\n\t" \
  "v_max3_f32 v209, v208, v206, v207\n\t" \
  "v_max3_f32 v210, v208, v206, v207\n\t" \
  "v_cndmask_b32 v204, v204, v206, %[up]\n\t" \
  "v_cndmask_b32 v205, v205, v207, %[up]\n\t" \
  "v_permlane16_swap_b32 v209, v210\n\t" \
  "v_max_f32 v208, v209, v210\n\t" \
  "v_mul_f32 v208, %[negc], v208\n\t" \
  "v_fma_f32 v204, v204, %[c], v208\n\t" \
  "v_fma_f32 v205, v205, %[c], v208\n\t" \
  "v_exp_f32 v204, v204\n\t" \
  "v_fma_f32 v216, v216, %[c], v208\n\t" \
  "v_exp_f32 v205, v205\n\t" \
  "v_fma_f32 v217, v217, %[c], v208\n\t" \
  "v_exp_f32 v216, v216\n\t" \
  "v_fma_f32 v218, v218, %[c], v208\n\t" \
  "v_cvt_pk_bf16_f32 v236, v204, v205\n\t" \
  "v_cvt_pk_bf16_f32 v239, v204, v205\n\t" \
  "v_exp_f32 v217, v217\n\t" \
  "v_fma_f32 v219, v219, %[c], v208\n\t" \
  "v_mfma_f32_16x16x16_bf16 v[200:203], v[220:221], v[236:237], 0\n\t" \
  "v_exp_f32 v218, v218\n\t" \
  "s_waitcnt vmcnt(0)\n\t" \
  "v_mfma_f32_16x16x16_bf16 v[224:227], v[252:253], v[236:237], 0\n\t" \
  "v_cvt_pk_bf16_f32 v214, v216, v217\n\t" \
  "v_cvt_pk_bf16_f32 v212, v216, v217\n\t" \
  "v_exp_f32 v219, v219\n\t" \
  "v_mfma_f32_16x16x16_bf16 v[228:231], v[252:253], v[238:239], 0\n\t" \
  "v_cvt_pk_bf16_f32 v215, v218, v219\n\t" \
  "v_cvt_pk_bf16_f32 v213, v218, v219\n\t" \
  "v_mov_b32_dpp v212, v214 row_ror:8 row_mask:0xf bank_mask:0xc\n\t" \
  "v_mov_b32_dpp v214, v214 row_ror:8 row_mask:0xf bank_mask:0x3\n\t" \
  "v_mov_b32_dpp v213, v215 row_ror:8 row_mask:0xf bank_mask:0xc\n\t" \
  "v_mov_b32_dpp v215, v215 row_ror:8 row_mask:0xf bank_mask:0x3\n\t" \
  "v_max_u32 v208, v252, v253\n\t" \
  "v_cmp_le_u32 vcc, %[sent], v208\n\t" \
  "v_mfma_f32_16x16x32_bf16 v[200:203], v[220:223], v[212:215], v[200:203]\n\t" \
  ".Lpv%=:\n\t" \
  "v_mfma_f32_16x16x32_bf16 v[224:227], v[192:195], v[212:215], v[224:227]\n\t" \
  "v_mfma_f32_16x16x32_bf16 v[228:231], v[196:199], v[212:215], v[228:231]\n\t" \
  "s_cbranch_vccnz .Lvs%=\n\t" \
  ".Lvok%=:\n\t" \
  "s_nop 4\n\t" \
  "v_rcp_f32 v208, v200\n\t" \
  "v_cndmask_b32 v224, v224, v226, %[nh]\n\t" \
  "v_cndmask_b32 v225, v225, v227, %[nh]\n\t" \
  "v_cndmask_b32 v228, v228, v230, %[nh]\n\t" \
  "v_cndmask_b32 v229, v229, v231, %[nh]\n\t" \
  "v_pk_mul_f32 v[224:225], v[224:225], v[208:209] op_sel_hi:[1,0]\n\t" \
  "v_pk_mul_f32 v[228:229], v[228:229], v[208:209] op_sel_hi:[1,0]\n\t" \
  "v_cvt_pk_bf16_f32 v224, v224, v225\n\t" \
  "v_cvt_pk_bf16_f32 v225, v228, v229\n\t" \
  "buffer_store_dwordx2 v[224:225], v251, %[rs], %[so] offen sc1\n\t" \
  "s_memtime s[90:91]\n\t" \
  "s_setprio 0\n\t" \
  "s_waitcnt lgkmcnt(0)\n\t" \
  "s_mov_b32 %[tout], s90\n\t" \
  "s_mov_b32 %[spins], s92\n\t" \
  "s_branch .Lend%=\n\t" \
  ".Lre%=:\n\t" \
  "ds_read_b64_tr_b16 v[240:241], v250 offset:1024\n\t" \
  "ds_read_b64_tr_b16 v[242:243], v250 offset:1088\n\t" \
  "ds_read_b64_tr_b16 v[244:245], v250 offset:1536\n\t" \
  "ds_read_b64_tr_b16 v[246:247], v250 offset:1600\n\t" \
  "ds_read_b64_tr_b16 v[232:233], v250 offset:0\n\t" \
  "ds_read_b64_tr_b16 v[234:235], v250 offset:64\n\t" \
  "s_waitcnt lgkmcnt(0)\n\t" \
  "s_branch .Lrb%=\n\t" \
  ".Lvs%=:\n\t" \
  "v_cmp_eq_u32 vcc, %[sent], v252\n\t" \
  "v_cmp_eq_u32 s[94:95], %[sent], v253\n\t" \
  "s_or_b64 s[94:95], s[94:95], vcc\n\t" \
  "s_cbranch_scc0 .Lvok%=\n\t" \
  "s_add_u32 s92, s92, 1\n\t" \
  "s_sleep 2\n\t" \
  "s_and_b32 s93, s92, 63\n\t" \
  "s_cbranch_scc1 .Lvr%=\n\t" \
  "buffer_load_dword v208, off, %[rs], %[sab] sc1\n\t" \
  "s_waitcnt vmcnt(0)\n\t" \
  "v_readfirstlane_b32 s93, v208\n\t" \
  "s_nop 3\n\t" \
  "s_cmp_eq_u32 s93, 1\n\t" \
  "s_cbranch_scc1 .Lvok%=\n\t" \
  "s_cmp_lt_u32 s92, 0x80000\n\t" \
  "s_cbranch_scc1 .Lvr%=\n\t" \
  "v_mov_b32 v208, 1\n\t" \
  "buffer_store_dword v208, off, %[rs], %[sab] sc1\n\t" \
  "s_branch .Lvok%=\n\t" \
  ".Lvr%=:\n\t" \
  "buffer_load_dword v252, v249, %[rs], %[sv] offen offset:0 sc1\n\t" \
  "buffer_load_dword v253, v249, %[rs], %[sv] offen offset:256 sc1\n\t" \
  "s_waitcnt vmcnt(0)\n\t" \
  "v_mfma_f32_16x16x16_bf16 v[224:227], v[252:253], v[236:237], 0\n\t" \
  "v_mfma_f32_16x16x16_bf16 v[228:231], v[252:253], v[238:239], 0\n\t" \
  "v_max_u32 v208, v252, v253\n\t" \
  "v_cmp_le_u32 vcc, %[sent], v208\n\t" \
  "s_nop 4\n\t" \
  "s_branch .Lpv%=\n\t" \
  "s_branch .Lend%=\n\t" \
  ".Lslow%=:\n\t" \
  "s_mov_b64 s[94:95], 0\n\t" \
  "v_cmp_eq_u32 vcc, %[sent], v200\n\t" \
  "s_or_b64 s[94:95], s[94:95], vcc\n\t" \
  "v_cmp_eq_u32 vcc, %[sent], v201\n\t" \
  "s_or_b64 s[94:95], s[94:95], vcc\n\t" \
  "v_cmp_eq_u32 vcc, %[sent], v202\n\t" \
  "s_or_b64 s[94:95], s[94:95], vcc\n\t" \
  "v_cmp_eq_u32 vcc, %[sent], v203\n\t" \
  "s_or_b64 s[94:95], s[94:95], vcc\n\t" \
  "v_cmp_eq_u32 vcc, %[sent], v204\n\t" \
  "s_or_b64 s[94:95], s[94:95], vcc\n\t" \
  "v_cmp_eq_u32 vcc, %[sent], v205\n\t" \
  "s_or_b64 s[94:95], s[94:95], vcc\n\t" \
  "v_cmp_eq_u32 vcc, %[sent], v206\n\t" \
  "s_or_b64 s[94:95], s[94:95], vcc\n\t" \
  "v_cmp_eq_u32 vcc, %[sent], v207\n\t" \
  "s_or_b64 s[94:95], s[94:95], vcc\n\t" \
  "s_cmp_eq_u64 s[94:95], 0\n\t" \
  "s_cbranch_scc1 .Lok%=\n\t" \
  "v_mov_b64 v[192:193], 0\n\t" \
  "v_mov_b64 v[194:195], 0\n\t" \
  "v_mov_b64 v[196:197], 0\n\t" \
  "v_mov_b64 v[198:199], 0\n\t" \
  "s_add_u32 s92, s92, 1\n\t" \
  "s_sleep 6\n\t" \
  "s_cmp_eq_u32 s92, 1\n\t" \
  "s_cbranch_scc1 .Lpoll%=\n\t" \
  "s_sleep 1\n\t" \
  "s_and_b32 s93, s92, 63\n\t" \
  "s_cmp_lg_u32 s93, 0\n\t" \
  "s_cbranch_scc1 .Lpoll%=\n\t" \
  "v_mov_b32 v245, 0\n\t" \
  "buffer_load_dword v244, v245, %[rs], %[sab] offen sc1\n\t" \
  "s_waitcnt vmcnt(0)\n\t" \
  "v_readfirstlane_b32 s93, v244\n\t" \
  "s_nop 3\n\t" \
  "s_cmp_eq_u32 s93, 1\n\t" \
  "s_cbranch_scc1 .Lok%=\n\t" \
  "s_cmp_lt_u32 s92, 0x80000\n\t" \
  "s_cbranch_scc1 .Lpoll%=\n\t" \
  "v_mov_b32 v244, 1\n\t" \
  "buffer_store_dword v244, v245, %[rs], %[sab] offen sc1\n\t" \
  "s_branch .Lok%=\n\t" \
  ".Lend%=:\n\t"
#define QK_ASM_2 \
  "s_waitcnt lgkmcnt(0)\n\t" \
  "v_subrev_u32 v244, 0x8000, v250\n\t" \
  "ds_read_b128 v[216:219], v248 offset:40960\n\t" \
  "ds_read_b128 v[220:223], v248 offset:41984\n\t" \
  "ds_read_b128 v[224:227], v248 offset:43008\n\t" \
  "ds_read_b128 v[228:231], v248 offset:44032\n\t" \
  "ds_read_b128 v[208:211], v244 offset:0\n\t" \
  "ds_read_b128 v[212:215], v244 offset:1024\n\t" \
  "ds_read_b128 v[236:239], v244 offset:2048\n\t" \
  "ds_read_b128 v[240:243], v244 offset:3072\n\t" \
  "v_mov_b64 v[192:193], 0\n\t" \
  "v_mov_b64 v[194:195], 0\n\t" \
  "v_mov_b64 v[196:197], 0\n\t" \
  "v_mov_b64 v[198:199], 0\n\t" \
  "s_movk_i32 s92, 0x2000\n\t" \
  ".Lwt%=:\n\t" \
  "ds_read_b64 v[200:201], v248 offset:30720\n\t" \
  "s_memrealtime s[90:91]\n\t" \
  "s_sub_u32 s92, s92, 1\n\t" \
  "s_waitcnt lgkmcnt(0)\n\t" \
  "v_readfirstlane_b32 s93, v200\n\t" \
  "v_readfirstlane_b32 %[tgt], v201\n\t" \
  "s_cbranch_scc1 .Lgo%=\n\t" \
  "s_cmp_eq_u32 s93, %[step]\n\t" \
  "s_cbranch_scc1 .Lwx%=\n\t" \
  "s_sleep 1\n\t" \
  "s_branch .Lwt%=\n\t" \
  ".Lwx%=:\n\t" \
  "s_lshl_b32 s93, s90, 2\n\t" \
  "s_sub_i32 s93, %[tgt], s93\n\t" \
  "s_cmp_le_i32 s93, 0\n\t" \
  "s_cbranch_scc1 .Lgo%=\n\t" \
  "s_min_i32 s93, s93, 8000\n\t" \
  "s_mov_b32 s94, s93\n\t" \
  ".Lsl%=:\n\t" \
  "s_sub_i32 s94, s94, 18\n\t" \
  "s_cmp_lt_i32 s94, 30\n\t" \
  "s_cbranch_scc1 .Lsx%=\n\t" \
  "s_sleep 1\n\t" \
  "s_branch .Lsl%=\n\t" \
  ".Lsx%=:\n\t" \
  "s_sub_i32 s93, s93, 12\n\t" \
  "s_max_i32 s93, s93, 4\n\t" \
  "s_lshr_b32 s93, s93, 2\n\t" \
  "s_add_u32 s93, s93, s90\n\t" \
  "s_memrealtime s[90:91]\n\t" \
  ".Lpace%=:\n\t" \
  "s_memrealtime s[94:95]\n\t" \
  "s_waitcnt lgkmcnt(1)\n\t" \
  "s_sub_i32 s92, s90, s93\n\t" \
  "s_cmp_ge_i32 s92, 0\n\t" \
  "s_cbranch_scc1 .Le%=\n\t" \
  "s_memrealtime s[90:91]\n\t" \
  "s_waitcnt lgkmcnt(1)\n\t" \
  "s_sub_i32 s92, s94, s93\n\t" \
  "s_cmp_lt_i32 s92, 0\n\t" \
  "s_cbranch_scc1 .Lpace%=\n\t" \
  "s_nop 4\n\t" \
  ".Le%=:\n\t" \
  "s_add_u32 s92, s92, s93\n\t" \
  "s_lshl_b32 s92, s92, 2\n\t" \
  "s_sub_i32 s92, %[tgt], s92\n\t" \
  "s_max_i32 s92, s92, 0\n\t" \
  "s_bitcmp0_b32 s92, 3\n\t" \
  "s_cbranch_scc1 .Lf3%=\n\t" \
  "s_nop 15\n\t" \
  ".Lf3%=:\n\t" \
  "s_bitcmp0_b32 s92, 2\n\t" \
  "s_cbranch_scc1 .Lf2%=\n\t" \
  "s_nop 10\n\t" \
  ".Lf2%=:\n\t" \
  ".Lgo%=:\n\t" \
  "s_mov_b32 s92, 0\n\t" \
  ".Lpoll%=:\n\t" \
  "buffer_load_dwordx4 v[200:203], v248, %[rs], %[som] offen sc1\n\t" \
  "buffer_load_dwordx4 v[204:207], v248, %[rs], %[som2] offen sc1\n\t" \
  "s_waitcnt vmcnt(1) lgkmcnt(0)\n\t" \
  "ds_write_b128 v249, v[200:203]\n\t" \
  "v_smfmac_f32_16x16x64_bf16 v[192:195], v[200:203], v[208:215], v251\n\t" \
  "v_max3_u32 v244, v200, v201, v202\n\t" \
  "v_smfmac_f32_16x16x64_bf16 v[196:199], v[200:203], v[236:243], v251\n\t" \
  "s_waitcnt vmcnt(0)\n\t" \
  "ds_write_b128 v249, v[204:207] offset:8192\n\t" \
  "v_smfmac_f32_16x16x64_bf16 v[192:195], v[204:207], v[216:223], v251\n\t" \
  "v_max3_u32 v244, v244, v203, v204\n\t" \
  "v_max3_u32 v244, v244, v205, v206\n\t" \
  "v_smfmac_f32_16x16x64_bf16 v[196:199], v[204:207], v[224:231], v251\n\t" \
  "v_max_u32 v244, v244, v207\n\t" \
  "v_cmp_le_u32 vcc, %[sent], v244\n\t" \
  "s_cbranch_vccnz .Lslow%=\n\t" \
  ".Lok%=:\n\t" \
  "ds_read_b128 v[216:219], v250 offset:32768\n\t" \
  "ds_read_b128 v[220:223], v250 offset:33792\n\t" \
  "ds_read_b128 v[224:227], v250 offset:34816\n\t" \
  "ds_read_b128 v[228:231], v250 offset:35840\n\t" \
  "s_waitcnt lgkmcnt(4)\n\t" \
  "s_barrier\n\t" \
  "ds_read_b128 v[208:211], v248 offset:3072\n\t" \
  "ds_read_b128 v[212:215], v248 offset:4096\n\t" \
  "ds_read_b128 v[236:239], v248 offset:5120\n\t" \
  "ds_read_b128 v[240:243], v248 offset:6144\n\t" \
  "s_waitcnt lgkmcnt(3)\n\t" \
  "ds_read_b128 v[244:247], v248 offset:13312\n\t" \
  "v_smfmac_f32_16x16x64_bf16 v[192:195], v[208:211], v[216:223], v251\n\t" \
  "ds_read_b128 v[216:219], v250 offset:0\n\t" \
  "v_smfmac_f32_16x16x64_bf16 v[196:199], v[208:211], v[224:231], v251\n\t" \
  "s_waitcnt lgkmcnt(4)\n\t" \
  "ds_read_b128 v[220:223], v250 offset:1024\n\t" \
  "v_smfmac_f32_16x16x64_bf16 v[192:195], v[212:215], v[0:7], v251\n\t" \
  "ds_read_b128 v[224:227], v250 offset:2048\n\t" \
  "v_smfmac_f32_16x16x64_bf16 v[196:199], v[212:215], v[8:15], v251\n\t" \
  "s_waitcnt lgkmcnt(5)\n\t" \
  "ds_read_b128 v[228:231], v250 offset:3072\n\t" \
  "v_smfmac_f32_16x16x64_bf16 v[192:195], v[236:239], v[16:23], v251\n\t" \
  "ds_read_b128 v[200:203], v248 offset:7168\n\t" \
  "v_smfmac_f32_16x16x64_bf16 v[196:199], v[236:239], v[24:31], v251\n\t" \
  "s_waitcnt lgkmcnt(6)\n\t" \
  "ds_read_b128 v[204:207], v248 offset:8192\n\t" \
  "v_smfmac_f32_16x16x64_bf16 v[192:195], v[240:243], v[32:39], v251\n\t" \
  "ds_read_b128 v[208:211], v248 offset:9216\n\t" \
  "v_smfmac_f32_16x16x64_bf16 v[196:199], v[240:243], v[40:47], v251\n\t" \
  "s_waitcnt lgkmcnt(5)\n\t" \
  "ds_read_b128 v[212:215], v248 offset:11264\n\t" \
  "v_smfmac_f32_16x16x64_bf16 v[192:195], v[244:247], v[216:223], v251\n\t" \
  "s_waitcnt lgkmcnt(4)\n\t" \
  "v_smfmac_f32_16x16x64_bf16 v[196:199], v[244:247], v[224:231], v251\n\t" \
  "s_waitcnt lgkmcnt(3)\n\t" \
  "ds_read_b128 v[236:239], v248 offset:12288\n\t" \
  "v_smfmac_f32_16x16x64_bf16 v[192:195], v[200:203], v[48:55], v251\n\t" \
  "ds_read_b128 v[240:243], v248 offset:14336\n\t" \
  "v_smfmac_f32_16x16x64_bf16 v[196:199], v[200:203], v[56:63], v251\n\t" \
  "s_waitcnt lgkmcnt(4)\n\t" \
  "v_smfmac_f32_16x16x64_bf16 v[192:195], v[204:207], v[64:71], v251\n\t" \
  "ds_read_b128 v[244:247], v248 offset:15360\n\t" \
  "v_smfmac_f32_16x16x64_bf16 v[196:199], v[204:207], v[72:79], v251\n\t" \
  "s_waitcnt lgkmcnt(4)\n\t" \
  "v_smfmac_f32_16x16x64_bf16 v[192:195], v[208:211], v[80:87], v251\n\t" \
  "ds_read_b128 v[200:203], v248 offset:0\n\t" \
  "v_smfmac_f32_16x16x64_bf16 v[196:199], v[208:211], v[88:95], v251\n\t" \
  "s_waitcnt lgkmcnt(4)\n\t" \
  "v_smfmac_f32_16x16x64_bf16 v[192:195], v[212:215], v[96:103], v251\n\t" \
  "ds_read_b128 v[204:207], v248 offset:1024\n\t" \
  "v_smfmac_f32_16x16x64_bf16 v[196:199], v[212:215], v[104:111], v251\n\t" \
  "s_waitcnt lgkmcnt(3)\n\t" \
  "v_smfmac_f32_16x16x64_bf16 v[192:195], v[236:239], v[112:119], v251\n\t" \
  "v_smfmac_f32_16x16x64_bf16 v[196:199], v[236:239], v[120:127], v251\n\t" \
  "v_smfmac_f32_16x16x64_bf16 v[192:195], v[240:243], v[128:135], v251\n\t" \
  "v_smfmac_f32_16x16x64_bf16 v[196:199], v[240:243], v[136:143], v251\n\t" \
  "s_waitcnt lgkmcnt(1)\n\t" \
  "v_smfmac_f32_16x16x64_bf16 v[192:195], v[244:247], v[144:151], v251\n\t" \
  "v_smfmac_f32_16x16x64_bf16 v[196:199], v[244:247], v[152:159], v251\n\t" \
  "v_smfmac_f32_16x16x64_bf16 v[192:195], v[200:203], v[160:167], v251\n\t" \
  "v_smfmac_f32_16x16x64_bf16 v[196:199], v[200:203], v[168:175], v251\n\t" \
  "s_waitcnt lgkmcnt(0)\n\t" \
  "v_smfmac_f32_16x16x64_bf16 v[192:195], v[204:207], v[176:183], v251\n\t" \
  "v_smfmac_f32_16x16x64_bf16 v[196:199], v[204:207], v[184:191], v251\n\t" \
  "ds_read_b128 v[200:203], v248 offset:18432\n\t" \
  "ds_read_b128 v[204:207], v248 offset:22528\n\t" \
  "v_mov_b32 v250, v232\n\t" \
  "v_mov_b32 v249, v233\n\t" \
  "s_nop 2\n\t" \
  "v_add_f32 v192, v192, v194\n\t" \
  "v_add_f32 v193, v193, v195\n\t" \
  "v_add_f32 v196, v196, v198\n\t" \
  "v_add_f32 v197, v197, v199\n\t" \
  "v_cvt_pk_bf16_f32 v192, v192, v193\n\t" \
  "v_cvt_pk_bf16_f32 v196, v196, v197\n\t" \
  "ds_write2_b32 v252, v192, v196 offset1:64\n\t" \
  "v_mov_b32 v251, v234\n\t" \
  "ds_read_b64_tr_b16 v[240:241], v250 offset:1024\n\t" \
  "ds_read_b64_tr_b16 v[242:243], v250 offset:1088\n\t" \
  "v_mov_b32 v212, %[step]\n\t" \
  "s_mov_b64 exec, 1\n\t" \
  "ds_write_b32 v248, v212 offset:28704\n\t" \
  "s_mov_b64 exec, -1\n\t" \
  "buffer_load_dwordx4 v[192:195], v248, %[rt], %[svt] offen offset:0 sc1\n\t" \
  "buffer_load_dwordx4 v[196:199], v248, %[rt], %[svt] offen offset:1024 sc1\n\t" \
  "s_waitcnt lgkmcnt(1)\n\t" \
  "v_mfma_f32_16x16x32_bf16 v[200:203], v[200:203], v[240:243], 0\n\t" \
  "v_mfma_f32_16x16x32_bf16 v[204:207], v[204:207], v[240:243], 0\n\t" \
  "s_nop 7\n\t" \
  "v_cndmask_b32 v216, v200, v204, %[nh]\n\t" \
  "v_cndmask_b32 v217, v201, v205, %[nh]\n\t" \
  "v_cndmask_b32 v218, v202, v206, %[nh]\n\t" \
  "v_cndmask_b32 v219, v203, v207, %[nh]\n\t" \
  "ds_write_b128 v248, v[216:219] offset:18432\n\t" \
  "buffer_load_dword v252, v249, %[rs], %[sv] offen offset:0 sc1\n\t" \
  "buffer_load_dword v253, v249, %[rs], %[sv] offen offset:256 sc1\n\t" \
  "ds_read_b32 v212, v248 offset:28672\n\t" \
  "ds_read_b64_tr_b16 v[244:245], v250 offset:1536\n\t" \
  "ds_read_b64_tr_b16 v[246:247], v250 offset:1600\n\t" \
  "ds_read_b64_tr_b16 v[232:233], v250 offset:0\n\t" \
  "ds_read_b64_tr_b16 v[234:235], v250 offset:64\n\t" \
  "ds_read_b64_tr_b16 v[236:237], v250 offset:512\n\t" \
  "ds_read_b64_tr_b16 v[238:239], v250 offset:576\n\t" \
  "s_setprio 3\n\t" \
  "s_waitcnt lgkmcnt(0)\n\t" \
  "v_cmp_ne_u32 vcc, %[step], v212\n\t" \
  "s_barrier\n\t" \
  "s_and_b32 s93, vcc_lo, 0xf\n\t" \
  "s_cbranch_scc1 .Lre%=\n\t" \
  ".Lrb%=:\n\t" \
  "ds_read_b64_tr_b16 v[208:209], v250 offset:3072\n\t" \
  "ds_read_b64_tr_b16 v[210:211], v250 offset:3136\n\t" \
  "ds_read_b64_tr_b16 v[212:213], v250 offset:3584\n\t" \
  "ds_read_b64_tr_b16 v[214:215], v250 offset:3648\n\t" \
  "ds_read_b128 v[220:223], v248 offset:19456\n\t" \
  "ds_read_b128 v[224:227], v248 offset:16384\n\t" \
  "ds_read_b128 v[228:231], v248 offset:17408\n\t" \
  "ds_read_b64_tr_b16 v[200:201], v250 offset:2048\n\t" \
  "ds_read_b64_tr_b16 v[202:203], v250 offset:2112\n\t" \
  "ds_read_b64_tr_b16 v[204:205], v250 offset:2560\n\t" \
  "ds_read_b64_tr_b16 v[206:207], v250 offset:2624\n\t" \
  "s_waitcnt lgkmcnt(9)\n\t" \
  "v_mfma_f32_16x16x32_bf16 v[208:211], v[208:211], v[240:243], 0\n\t" \
  "s_waitcnt lgkmcnt(7)\n\t" \
  "v_mfma_f32_16x16x32_bf16 v[208:211], v[212:215], v[244:247], v[208:211]\n\t" \
  "s_waitcnt lgkmcnt(6)\n\t" \
  "v_pk_add_f32 v[216:217], v[216:217], v[220:221]\n\t" \
  "v_pk_add_f32 v[218:219], v[218:219], v[222:223]\n\t" \
  "s_waitcnt lgkmcnt(4)\n\t" \
  "v_pk_add_f32 v[224:225], v[224:225], v[228:229]\n\t" \
  "v_pk_add_f32 v[226:227], v[226:227], v[230:231]\n\t" \
  "v_pk_add_f32 v[216:217], v[216:217], v[224:225]\n\t" \
  "v_pk_add_f32 v[218:219], v[218:219], v[226:227]\n\t" \
  "s_waitcnt lgkmcnt(2)\n\t" \
  "v_mfma_f32_16x16x32_bf16 v[208:211], v[200:203], v[232:235], v[208:211]\n\t" \
  "v_max3_f32 v224, v216, v217, v218\n\t" \
  "v_max_f32 v224, v224, v219\n\t" \
  "s_waitcnt lgkmcnt(0)\n\t" \
  "v_mfma_f32_16x16x32_bf16 v[208:211], v[204:207], v[236:239], v[208:211]\n\t" \
  "v_max_f32_dpp v225, v224, v224 row_ror:8 row_mask:0xf bank_mask:0xf\n\t" \
  "v_max_f32_dpp v226, v224, v224 row_ror:8 row_mask:0xf bank_mask:0xf\n\t" \
  "s_nop 1\n\t" \
  "v_permlane32_swap_b32 v225, v226\n\t" \
  "v_max_f32 v231, v225, v226\n\t" \
  "v_mov_b32 v220, 0x3f803f80\n\t" \
  "v_mov_b32 v221, 0x3f803f80\n\t" \
  "v_mov_b64 v[222:223], v[220:221]\n\t" \
  "v_mov_b32 v241, 0\n\t" \
  "v_mov_b32 v242, 0\n\t" \
  "v_max3_f32 v212, v208, v209, v231\n\t" \
  "v_max3_f32 v213, v212, v210, v211\n\t" \
  "v_max3_f32 v214, v212, v210, v211\n\t" \
  "v_cndmask_b32 v208, v208, v210, %[up]\n\t" \
  "v_cndmask_b32 v209, v209, v211, %[up]\n\t" \
  "v_permlane16_swap_b32 v213, v214\n\t" \
  "v_max_f32 v212, v213, v214\n\t" \
  "v_mul_f32 v212, %[negc], v212\n\t" \
  "v_fma_f32 v208, v208, %[c], v212\n\t" \
  "v_fma_f32 v209, v209, %[c], v212\n\t" \
  "v_exp_f32 v208, v208\n\t" \
  "v_fma_f32 v216, v216, %[c], v212\n\t" \
  "v_exp_f32 v209, v209\n\t" \
  "v_fma_f32 v217, v217, %[c], v212\n\t" \
  "v_exp_f32 v216, v216\n\t" \
  "v_fma_f32 v218, v218, %[c], v212\n\t" \
  "v_cvt_pk_bf16_f32 v240, v208, v209\n\t" \
  "v_cvt_pk_bf16_f32 v243, v208, v209\n\t" \
  "v_exp_f32 v217, v217\n\t" \
  "v_fma_f32 v219, v219, %[c], v212\n\t" \
  "v_mfma_f32_16x16x16_bf16 v[204:207], v[220:221], v[240:241], 0\n\t" \
  "v_exp_f32 v218, v218\n\t" \
  "s_waitcnt vmcnt(0)\n\t" \
  "v_mfma_f32_16x16x16_bf16 v[224:227], v[252:253], v[240:241], 0\n\t" \
  "v_cvt_pk_bf16_f32 v202, v216, v217\n\t" \
  "v_cvt_pk_bf16_f32 v200, v216, v217\n\t" \
  "v_exp_f32 v219, v219\n\t" \
  "v_mfma_f32_16x16x16_bf16 v[228:231], v[252:253], v[242:243], 0\n\t" \
  "v_cvt_pk_bf16_f32 v203, v218, v219\n\t" \
  "v_cvt_pk_bf16_f32 v201, v218, v219\n\t" \
  "v_mov_b32_dpp v200, v202 row_ror:8 row_mask:0xf bank_mask:0xc\n\t" \
  "v_mov_b32_dpp v202, v202 row_ror:8 row_mask:0xf bank_mask:0x3\n\t" \
  "v_mov_b32_dpp v201, v203 row_ror:8 row_mask:0xf bank_mask:0xc\n\t" \
  "v_mov_b32_dpp v203, v203 row_ror:8 row_mask:0xf bank_mask:0x3\n\t" \
  "v_max_u32 v212, v252, v253\n\t" \
  "v_cmp_le_u32 vcc, %[sent], v212\n\t" \
  "v_mfma_f32_16x16x32_bf16 v[204:207], v[220:223], v[200:203], v[204:207]\n\t" \
  ".Lpv%=:\n\t" \
  "v_mfma_f32_16x16x32_bf16 v[224:227], v[192:195], v[200:203], v[224:227]\n\t" \
  "v_mfma_f32_16x16x32_bf16 v[228:231], v[196:199], v[200:203], v[228:231]\n\t" \
  "s_cbranch_vccnz .Lvs%=\n\t" \
  ".Lvok%=:\n\t" \
  "s_nop 4\n\t" \
  "v_rcp_f32 v212, v204\n\t" \
  "v_cndmask_b32 v224, v224, v226, %[nh]\n\t" \
  "v_cndmask_b32 v225, v225, v227, %[nh]\n\t" \
  "v_cndmask_b32 v228, v228, v230, %[nh]\n\t" \
  "v_cndmask_b32 v229, v229, v231, %[nh]\n\t" \
  "v_pk_mul_f32 v[224:225], v[224:225], v[212:213] op_sel_hi:[1,0]\n\t" \
  "v_pk_mul_f32 v[228:229], v[228:229], v[212:213] op_sel_hi:[1,0]\n\t" \
  "v_cvt_pk_bf16_f32 v224, v224, v225\n\t" \
  "v_cvt_pk_bf16_f32 v225, v228, v229\n\t" \
  "buffer_store_dwordx2 v[224:225], v251, %[rs], %[so] offen sc1\n\t" \
  "s_memtime s[90:91]\n\t" \
  "s_setprio 0\n\t" \
  "s_waitcnt lgkmcnt(0)\n\t" \
  "s_mov_b32 %[tout], s90\n\t" \
  "s_mov_b32 %[spins], s92\n\t" \
  "s_branch .Lend%=\n\t" \
  ".Lre%=:\n\t" \
  "ds_read_b64_tr_b16 v[244:245], v250 offset:1536\n\t" \
  "ds_read_b64_tr_b16 v[246:247], v250 offset:1600\n\t" \
  "ds_read_b64_tr_b16 v[232:233], v250 offset:0\n\t" \
  "ds_read_b64_tr_b16 v[234:235], v250 offset:64\n\t" \
  "ds_read_b64_tr_b16 v[236:237], v250 offset:512\n\t" \
  "ds_read_b64_tr_b16 v[238:239], v250 offset:576\n\t" \
  "s_waitcnt lgkmcnt(0)\n\t" \
  "s_branch .Lrb%=\n\t" \
  ".Lvs%=:\n\t" \
  "v_cmp_eq_u32 vcc, %[sent], v252\n\t" \
  "v_cmp_eq_u32 s[94:95], %[sent], v253\n\t" \
  "s_or_b64 s[94:95], s[94:95], vcc\n\t" \
  "s_cbranch_scc0 .Lvok%=\n\t" \
  "s_add_u32 s92, s92, 1\n\t" \
  "s_sleep 2\n\t" \
  "s_and_b32 s93, s92, 63\n\t" \
  "s_cbranch_scc1 .Lvr%=\n\t" \
  "buffer_load_dword v212, off, %[rs], %[sab] sc1\n\t" \
  "s_waitcnt vmcnt(0)\n\t" \
  "v_readfirstlane_b32 s93, v212\n\t" \
  "s_nop 3\n\t" \
  "s_cmp_eq_u32 s93, 1\n\t" \
  "s_cbranch_scc1 .Lvok%=\n\t" \
  "s_cmp_lt_u32 s92, 0x80000\n\t" \
  "s_cbranch_scc1 .Lvr%=\n\t" \
  "v_mov_b32 v212, 1\n\t" \
  "buffer_store_dword v212, off, %[rs], %[sab] sc1\n\t" \
  "s_branch .Lvok%=\n\t" \
  ".Lvr%=:\n\t" \
  "buffer_load_dword v252, v249, %[rs], %[sv] offen offset:0 sc1\n\t" \
  "buffer_load_dword v253, v249, %[rs], %[sv] offen offset:256 sc1\n\t" \
  "s_waitcnt vmcnt(0)\n\t" \
  "v_mfma_f32_16x16x16_bf16 v[224:227], v[252:253], v[240:241], 0\n\t" \
  "v_mfma_f32_16x16x16_bf16 v[228:231], v[252:253], v[242:243], 0\n\t" \
  "v_max_u32 v212, v252, v253\n\t" \
  "v_cmp_le_u32 vcc, %[sent], v212\n\t" \
  "s_nop 4\n\t" \
  "s_branch .Lpv%=\n\t" \
  "s_branch .Lend%=\n\t" \
  ".Lslow%=:\n\t" \
  "s_mov_b64 s[94:95], 0\n\t" \
  "v_cmp_eq_u32 vcc, %[sent], v200\n\t" \
  "s_or_b64 s[94:95], s[94:95], vcc\n\t" \
  "v_cmp_eq_u32 vcc, %[sent], v201\n\t" \
  "s_or_b64 s[94:95], s[94:95], vcc\n\t" \
  "v_cmp_eq_u32 vcc, %[sent], v202\n\t" \
  "s_or_b64 s[94:95], s[94:95], vcc\n\t" \
  "v_cmp_eq_u32 vcc, %[sent], v203\n\t" \
  "s_or_b64 s[94:95], s[94:95], vcc\n\t" \
  "v_cmp_eq_u32 vcc, %[sent], v204\n\t" \
  "s_or_b64 s[94:95], s[94:95], vcc\n\t" \
  "v_cmp_eq_u32 vcc, %[sent], v205\n\t" \
  "s_or_b64 s[94:95], s[94:95], vcc\n\t" \
  "v_cmp_eq_u32 vcc, %[sent], v206\n\t" \
  "s_or_b64 s[94:95], s[94:95], vcc\n\t" \
  "v_cmp_eq_u32 vcc, %[sent], v207\n\t" \
  "s_or_b64 s[94:95], s[94:95], vcc\n\t" \
  "s_cmp_eq_u64 s[94:95], 0\n\t" \
  "s_cbranch_scc1 .Lok%=\n\t" \
  "v_mov_b64 v[192:193], 0\n\t" \
  "v_mov_b64 v[194:195], 0\n\t" \
  "v_mov_b64 v[196:197], 0\n\t" \
  "v_mov_b64 v[198:199], 0\n\t" \
  "s_add_u32 s92, s92, 1\n\t" \
  "s_sleep 6\n\t" \
  "s_cmp_eq_u32 s92, 1\n\t" \
  "s_cbranch_scc1 .Lpoll%=\n\t" \
  "s_sleep 1\n\t" \
  "s_and_b32 s93, s92, 63\n\t" \
  "s_cmp_lg_u32 s93, 0\n\t" \
  "s_cbranch_scc1 .Lpoll%=\n\t" \
  "v_mov_b32 v245, 0\n\t" \
  "buffer_load_dword v244, v245, %[rs], %[sab] offen sc1\n\t" \
  "s_waitcnt vmcnt(0)\n\t" \
  "v_readfirstlane_b32 s93, v244\n\t" \
  "s_nop 3\n\t" \
  "s_cmp_eq_u32 s93, 1\n\t" \
  "s_cbranch_scc1 .Lok%=\n\t" \
  "s_cmp_lt_u32 s92, 0x80000\n\t" \
  "s_cbranch_scc1 .Lpoll%=\n\t" \
  "v_mov_b32 v244, 1\n\t" \
  "buffer_store_dword v244, v245, %[rs], %[sab] offen sc1\n\t" \
  "s_branch .Lok%=\n\t" \
  ".Lend%=:\n\t"
#define QK_ASM_3 \
  "s_waitcnt lgkmcnt(0)\n\t" \
  "v_subrev_u32 v244, 0x8000, v250\n\t" \
  "ds_read_b128 v[216:219], v248 offset:45056\n\t" \
  "ds_read_b128 v[220:223], v248 offset:46080\n\t" \
  "ds_read_b128 v[224:227], v248 offset:47104\n\t" \
  "ds_read_b128 v[228:231], v248 offset:48128\n\t" \
  "ds_read_b128 v[208:211], v244 offset:0\n\t" \
  "ds_read_b128 v[212:215], v244 offset:1024\n\t" \
  "ds_read_b128 v[236:239], v244 offset:2048\n\t" \
  "ds_read_b128 v[240:243], v244 offset:3072\n\t" \
  "v_mov_b64 v[192:193], 0\n\t" \
  "v_mov_b64 v[194:195], 0\n\t" \
  "v_mov_b64 v[196:197], 0\n\t" \
  "v_mov_b64 v[198:199], 0\n\t" \
  "s_movk_i32 s92, 0x2000\n\t" \
  ".Lwt%=:\n\t" \
  "ds_read_b64 v[200:201], v248 offset:30720\n\t" \
  "s_memrealtime s[90:91]\n\t" \
  "s_sub_u32 s92, s92, 1\n\t" \
  "s_waitcnt lgkmcnt(0)\n\t" \
  "v_readfirstlane_b32 s93, v200\n\t" \
  "v_readfirstlane_b32 %[tgt], v201\n\t" \
  "s_cbranch_scc1 .Lgo%=\n\t" \
  "s_cmp_eq_u32 s93, %[step]\n\t" \
  "s_cbranch_scc1 .Lwx%=\n\t" \
  "s_sleep 1\n\t" \
  "s_branch .Lwt%=\n\t" \
  ".Lwx%=:\n\t" \
  "s_lshl_b32 s93, s90, 2\n\t" \
  "s_sub_i32 s93, %[tgt], s93\n\t" \
  "s_cmp_le_i32 s93, 0\n\t" \
  "s_cbranch_scc1 .Lgo%=\n\t" \
  "s_min_i32 s93, s93, 8000\n\t" \
  "s_mov_b32 s94, s93\n\t" \
  ".Lsl%=:\n\t" \
  "s_sub_i32 s94, s94, 18\n\t" \
  "s_cmp_lt_i32 s94, 30\n\t" \
  "s_cbranch_scc1 .Lsx%=\n\t" \
  "s_sleep 1\n\t" \
  "s_branch .Lsl%=\n\t" \
  ".Lsx%=:\n\t" \
  "s_sub_i32 s93, s93, 12\n\t" \
  "s_max_i32 s93, s93, 4\n\t" \
  "s_lshr_b32 s93, s93, 2\n\t" \
  "s_add_u32 s93, s93, s90\n\t" \
  "s_memrealtime s[90:91]\n\t" \
  ".Lpace%=:\n\t" \
  "s_memrealtime s[94:95]\n\t" \
  "s_waitcnt lgkmcnt(1)\n\t" \
  "s_sub_i32 s92, s90, s93\n\t" \
  "s_cmp_ge_i32 s92, 0\n\t" \
  "s_cbranch_scc1 .Le%=\n\t" \
  "s_memrealtime s[90:91]\n\t" \
  "s_waitcnt lgkmcnt(1)\n\t" \
  "s_sub_i32 s92, s94, s93\n\t" \
  "s_cmp_lt_i32 s92, 0\n\t" \
  "s_cbranch_scc1 .Lpace%=\n\t" \
  "s_nop 4\n\t" \
  ".Le%=:\n\t" \
  "s_add_u32 s92, s92, s93\n\t" \
  "s_lshl_b32 s92, s92, 2\n\t" \
  "s_sub_i32 s92, %[tgt], s92\n\t" \
  "s_max_i32 s92, s92, 0\n\t" \
  "s_bitcmp0_b32 s92, 3\n\t" \
  "s_cbranch_scc1 .Lf3%=\n\t" \
  "s_nop 15\n\t" \
  ".Lf3%=:\n\t" \
  "s_bitcmp0_b32 s92, 2\n\t" \
  "s_cbranch_scc1 .Lf2%=\n\t" \
  "s_nop 10\n\t" \
  ".Lf2%=:\n\t" \
  ".Lgo%=:\n\t" \
  "s_mov_b32 s92, 0\n\t" \
  ".Lpoll%=:\n\t" \
  "buffer_load_dwordx4 v[200:203], v248, %[rs], %[som] offen sc1\n\t" \
  "buffer_load_dwordx4 v[204:207], v248, %[rs], %[som2] offen sc1\n\t" \
  "s_waitcnt vmcnt(1) lgkmcnt(0)\n\t" \
  "ds_write_b128 v249, v[200:203]\n\t" \
  "v_smfmac_f32_16x16x64_bf16 v[192:195], v[200:203], v[208:215], v251\n\t" \
  "v_max3_u32 v244, v200, v201, v202\n\t" \
  "v_smfmac_f32_16x16x64_bf16 v[196:199], v[200:203], v[236:243], v251\n\t" \
  "s_waitcnt vmcnt(0)\n\t" \
  "ds_write_b128 v249, v[204:207] offset:8192\n\t" \
  "v_smfmac_f32_16x16x64_bf16 v[192:195], v[204:207], v[216:223], v251\n\t" \
  "v_max3_u32 v244, v244, v203, v204\n\t" \
  "v_max3_u32 v244, v244, v205, v206\n\t" \
  "v_smfmac_f32_16x16x64_bf16 v[196:199], v[204:207], v[224:231], v251\n\t" \
  "v_max_u32 v244, v244, v207\n\t" \
  "v_cmp_le_u32 vcc, %[sent], v244\n\t" \
  "s_cbranch_vccnz .Lslow%=\n\t" \
  ".Lok%=:\n\t" \
  "ds_read_b128 v[216:219], v250 offset:32768\n\t" \
  "ds_read_b128 v[220:223], v250 offset:33792\n\t" \
  "ds_read_b128 v[224:227], v250 offset:34816\n\t" \
  "ds_read_b128 v[228:231], v250 offset:35840\n\t" \
  "s_waitcnt lgkmcnt(4)\n\t" \
  "s_barrier\n\t" \
  "ds_read_b128 v[208:211], v248 offset:2048\n\t" \
  "ds_read_b128 v[212:215], v248 offset:5120\n\t" \
  "ds_read_b128 v[236:239], v248 offset:6144\n\t" \
  "ds_read_b128 v[240:243], v248 offset:7168\n\t" \
  "s_waitcnt lgkmcnt(3)\n\t" \
  "ds_read_b128 v[244:247], v248 offset:12288\n\t" \
  "v_smfmac_f32_16x16x64_bf16 v[192:195], v[208:211], v[216:223], v251\n\t" \
  "ds_read_b128 v[216:219], v250 offset:0\n\t" \
  "v_smfmac_f32_16x16x64_bf16 v[196:199], v[208:211], v[224:231], v251\n\t" \
  "s_waitcnt lgkmcnt(4)\n\t" \
  "ds_read_b128 v[220:223], v250 offset:1024\n\t" \
  "v_smfmac_f32_16x16x64_bf16 v[192:195], v[212:215], v[0:7], v251\n\t" \
  "ds_read_b128 v[224:227], v250 offset:2048\n\t" \
  "v_smfmac_f32_16x16x64_bf16 v[196:199], v[212:215], v[8:15], v251\n\t" \
  "s_waitcnt lgkmcnt(5)\n\t" \
  "ds_read_b128 v[228:231], v250 offset:3072\n\t" \
  "v_smfmac_f32_16x16x64_bf16 v[192:195], v[236:239], v[16:23], v251\n\t" \
  "ds_read_b128 v[200:203], v248 offset:8192\n\t" \
  "v_smfmac_f32_16x16x64_bf16 v[196:199], v[236:239], v[24:31], v251\n\t" \
  "s_waitcnt lgkmcnt(6)\n\t" \
  "ds_read_b128 v[204:207], v248 offset:9216\n\t" \
  "v_smfmac_f32_16x16x64_bf16 v[192:195], v[240:243], v[32:39], v251\n\t" \
  "ds_read_b128 v[208:211], v248 offset:10240\n\t" \
  "v_smfmac_f32_16x16x64_bf16 v[196:199], v[240:243], v[40:47], v251\n\t" \
  "s_waitcnt lgkmcnt(5)\n\t" \
  "ds_read_b128 v[212:215], v248 offset:13312\n\t" \
  "v_smfmac_f32_16x16x64_bf16 v[192:195], v[244:247], v[216:223], v251\n\t" \
  "s_waitcnt lgkmcnt(4)\n\t" \
  "v_smfmac_f32_16x16x64_bf16 v[196:199], v[244:247], v[224:231], v251\n\t" \
  "s_waitcnt lgkmcnt(3)\n\t" \
  "ds_read_b128 v[236:239], v248 offset:14336\n\t" \
  "v_smfmac_f32_16x16x64_bf16 v[192:195], v[200:203], v[48:55], v251\n\t" \
  "ds_read_b128 v[240:243], v248 offset:15360\n\t" \
  "v_smfmac_f32_16x16x64_bf16 v[196:199], v[200:203], v[56:63], v251\n\t" \
  "s_waitcnt lgkmcnt(4)\n\t" \
  "v_smfmac_f32_16x16x64_bf16 v[192:195], v[204:207], v[64:71], v251\n\t" \
  "ds_read_b128 v[244:247], v248 offset:0\n\t" \
  "v_smfmac_f32_16x16x64_bf16 v[196:199], v[204:207], v[72:79], v251\n\t" \
  "s_waitcnt lgkmcnt(4)\n\t" \
  "v_smfmac_f32_16x16x64_bf16 v[192:195], v[208:211], v[80:87], v251\n\t" \
  "ds_read_b128 v[200:203], v248 offset:1024\n\t" \
  "v_smfmac_f32_16x16x64_bf16 v[196:199], v[208:211], v[88:95], v251\n\t" \
  "s_waitcnt lgkmcnt(4)\n\t" \
  "v_smfmac_f32_16x16x64_bf16 v[192:195], v[212:215], v[96:103], v251\n\t" \
  "ds_read_b128 v[204:207], v248 offset:4096\n\t" \
  "v_smfmac_f32_16x16x64_bf16 v[196:199], v[212:215], v[104:111], v251\n\t" \
  "s_waitcnt lgkmcnt(3)\n\t" \
  "v_smfmac_f32_16x16x64_bf16 v[192:195], v[236:239], v[112:119], v251\n\t" \
  "v_smfmac_f32_16x16x64_bf16 v[196:199], v[236:239], v[120:127], v251\n\t" \
  "v_smfmac_f32_16x16x64_bf16 v[192:195], v[240:243], v[128:135], v251\n\t" \
  "v_smfmac_f32_16x16x64_bf16 v[196:199], v[240:243], v[136:143], v251\n\t" \
  "s_waitcnt lgkmcnt(1)\n\t" \
  "v_smfmac_f32_16x16x64_bf16 v[192:195], v[244:247], v[144:151], v251\n\t" \
  "v_smfmac_f32_16x16x64_bf16 v[196:199], v[244:247], v[152:159], v251\n\t" \
  "v_smfmac_f32_16x16x64_bf16 v[192:195], v[200:203], v[160:167], v251\n\t" \
  "v_smfmac_f32_16x16x64_bf16 v[196:199], v[200:203], v[168:175], v251\n\t" \
  "s_waitcnt lgkmcnt(0)\n\t" \
  "v_smfmac_f32_16x16x64_bf16 v[192:195], v[204:207], v[176:183], v251\n\t" \
  "v_smfmac_f32_16x16x64_bf16 v[196:199], v[204:207], v[184:191], v251\n\t" \
  "ds_read_b128 v[200:203], v248 offset:19456\n\t" \
  "ds_read_b128 v[204:207], v248 offset:23552\n\t" \
  "v_mov_b32 v250, v232\n\t" \
  "v_mov_b32 v249, v233\n\t" \
  "s_nop 2\n\t" \
  "v_add_f32 v192, v192, v194\n\t" \
  "v_add_f32 v193, v193, v195\n\t" \
  "v_add_f32 v196, v196, v198\n\t" \
  "v_add_f32 v197, v197, v199\n\t" \
  "v_cvt_pk_bf16_f32 v192, v192, v193\n\t" \
  "v_cvt_pk_bf16_f32 v196, v196, v197\n\t" \
  "ds_write2_b32 v252, v192, v196 offset1:64\n\t" \
  "v_mov_b32 v251, v234\n\t" \
  "ds_read_b64_tr_b16 v[244:245], v250 offset:1536\n\t" \
  "ds_read_b64_tr_b16 v[246:247], v250 offset:1600\n\t" \
  "v_mov_b32 v212, %[step]\n\t" \
  "s_mov_b64 exec, 1\n\t" \
  "ds_write_b32 v248, v212 offset:28720\n\t" \
  "s_mov_b64 exec, -1\n\t" \
  "buffer_load_dwordx4 v[192:195], v248, %[rt], %[svt] offen offset:0 sc1\n\t" \
  "buffer_load_dwordx4 v[196:199], v248, %[rt], %[svt] offen offset:1024 sc1\n\t" \
  "s_waitcnt lgkmcnt(1)\n\t" \
  "v_mfma_f32_16x16x32_bf16 v[200:203], v[200:203], v[244:247], 0\n\t" \
  "v_mfma_f32_16x16x32_bf16 v[204:207], v[204:207], v[244:247], 0\n\t" \
  "s_nop 7\n\t" \
  "v_cndmask_b32 v216, v200, v204, %[nh]\n\t" \
  "v_cndmask_b32 v217, v201, v205, %[nh]\n\t" \
  "v_cndmask_b32 v218, v202, v206, %[nh]\n\t" \
  "v_cndmask_b32 v219, v203, v207, %[nh]\n\t" \
  "ds_write_b128 v248, v[216:219] offset:19456\n\t" \
  "buffer_load_dword v252, v249, %[rs], %[sv] offen offset:0 sc1\n\t" \
  "buffer_load_dword v253, v249, %[rs], %[sv] offen offset:256 sc1\n\t" \
  "ds_read_b32 v212, v248 offset:28672\n\t" \
  "ds_read_b64_tr_b16 v[232:233], v250 offset:0\n\t" \
  "ds_read_b64_tr_b16 v[234:235], v250 offset:64\n\t" \
  "ds_read_b64_tr_b16 v[236:237], v250 offset:512\n\t" \
  "ds_read_b64_tr_b16 v[238:239], v250 offset:576\n\t" \
  "ds_read_b64_tr_b16 v[240:241], v250 offset:1024\n\t" \
  "ds_read_b64_tr_b16 v[242:243], v250 offset:1088\n\t" \
  "s_setprio 3\n\t" \
  "s_waitcnt lgkmcnt(0)\n\t" \
  "v_cmp_ne_u32 vcc, %[step], v212\n\t" \
  "s_barrier\n\t" \
  "s_and_b32 s93, vcc_lo, 0xf\n\t" \
  "s_cbranch_scc1 .Lre%=\n\t" \
  ".Lrb%=:\n\t" \
  "ds_read_b64_tr_b16 v[212:213], v250 offset:3584\n\t" \
  "ds_read_b64_tr_b16 v[214:215], v250 offset:3648\n\t" \
  "ds_read_b64_tr_b16 v[200:201], v250 offset:2048\n\t" \
  "ds_read_b64_tr_b16 v[202:203], v250 offset:2112\n\t" \
  "ds_read_b128 v[220:223], v248 offset:16384\n\t" \
  "ds_read_b128 v[224:227], v248 offset:17408\n\t" \
  "ds_read_b128 v[228:231], v248 offset:18432\n\t" \
  "ds_read_b64_tr_b16 v[204:205], v250 offset:2560\n\t" \
  "ds_read_b64_tr_b16 v[206:207], v250 offset:2624\n\t" \
  "ds_read_b64_tr_b16 v[208:209], v250 offset:3072\n\t" \
  "ds_read_b64_tr_b16 v[210:211], v250 offset:3136\n\t" \
  "s_waitcnt lgkmcnt(9)\n\t" \
  "v_mfma_f32_16x16x32_bf16 v[212:215], v[212:215], v[244:247], 0\n\t" \
  "s_waitcnt lgkmcnt(7)\n\t" \
  "v_mfma_f32_16x16x32_bf16 v[212:215], v[200:203], v[232:235], v[212:215]\n\t" \
  "s_waitcnt lgkmcnt(6)\n\t" \
  "v_pk_add_f32 v[216:217], v[216:217], v[220:221]\n\t" \
  "v_pk_add_f32 v[218:219], v[218:219], v[222:223]\n\t" \
  "s_waitcnt lgkmcnt(4)\n\t" \
  "v_pk_add_f32 v[224:225], v[224:225], v[228:229]\n\t" \
  "v_pk_add_f32 v[226:227], v[226:227], v[230:231]\n\t" \
  "v_pk_add_f32 v[216:217], v[216:217], v[224:225]\n\t" \
  "v_pk_add_f32 v[218:219], v[218:219], v[226:227]\n\t" \
  "s_waitcnt lgkmcnt(2)\n\t" \
  "v_mfma_f32_16x16x32_bf16 v[212:215], v[204:207], v[236:239], v[212:215]\n\t" \
  "v_max3_f32 v224, v216, v217, v218\n\t" \
  "v_max_f32 v224, v224, v219\n\t" \
  "s_waitcnt lgkmcnt(0)\n\t" \
  "v_mfma_f32_16x16x32_bf16 v[212:215], v[208:211], v[240:243], v[212:215]\n\t" \
  "v_max_f32_dpp v225, v224, v224 row_ror:8 row_mask:0xf bank_mask:0xf\n\t" \
  "v_max_f32_dpp v226, v224, v224 row_ror:8 row_mask:0xf bank_mask:0xf\n\t" \
  "s_nop 1\n\t" \
  "v_permlane32_swap_b32 v225, v226\n\t" \
  "v_max_f32 v231, v225, v226\n\t" \
  "v_mov_b32 v220, 0x3f803f80\n\t" \
  "v_mov_b32 v221, 0x3f803f80\n\t" \
  "v_mov_b64 v[222:223], v[220:221]\n\t" \
  "v_mov_b32 v245, 0\n\t" \
  "v_mov_b32 v246, 0\n\t" \
  "v_max3_f32 v200, v212, v213, v231\n\t" \
  "v_max3_f32 v201, v200, v214, v215\n\t" \
  "v_max3_f32 v202, v200, v214, v215\n\t" \
  "v_cndmask_b32 v212, v212, v214, %[up]\n\t" \
  "v_cndmask_b32 v213, v213, v215, %[up]\n\t" \
  "v_permlane16_swap_b32 v201, v202\n\t" \
  "v_max_f32 v200, v201, v202\n\t" \
  "v_mul_f32 v200, %[negc], v200\n\t" \
  "v_fma_f32 v212, v212, %[c], v200\n\t" \
  "v_fma_f32 v213, v213, %[c], v200\n\t" \
  "v_exp_f32 v212, v212\n\t" \
  "v_fma_f32 v216, v216, %[c], v200\n\t" \
  "v_exp_f32 v213, v213\n\t" \
  "v_fma_f32 v217, v217, %[c], v200\n\t" \
  "v_exp_f32 v216, v216\n\t" \
  "v_fma_f32 v218, v218, %[c], v200\n\t" \
  "v_cvt_pk_bf16_f32 v244, v212, v213\n\t" \
  "v_cvt_pk_bf16_f32 v247, v212, v213\n\t" \
  "v_exp_f32 v217, v217\n\t" \
  "v_fma_f32 v219, v219, %[c], v200\n\t" \
  "v_mfma_f32_16x16x16_bf16 v[208:211], v[220:221], v[244:245], 0\n\t" \
  "v_exp_f32 v218, v218\n\t" \
  "s_waitcnt vmcnt(0)\n\t" \
  "v_mfma_f32_16x16x16_bf16 v[224:227], v[252:253], v[244:245], 0\n\t" \
  "v_cvt_pk_bf16_f32 v206, v216, v217\n\t" \
  "v_cvt_pk_bf16_f32 v204, v216, v217\n\t" \
  "v_exp_f32 v219, v219\n\t" \
  "v_mfma_f32_16x16x16_bf16 v[228:231], v[252:253], v[246:247], 0\n\t" \
  "v_cvt_pk_bf16_f32 v207, v218, v219\n\t" \
  "v_cvt_pk_bf16_f32 v205, v218, v219\n\t" \
  "v_mov_b32_dpp v204, v206 row_ror:8 row_mask:0xf bank_mask:0xc\n\t" \
  "v_mov_b32_dpp v206, v206 row_ror:8 row_mask:0xf bank_mask:0x3\n\t" \
  "v_mov_b32_dpp v205, v207 row_ror:8 row_mask:0xf bank_mask:0xc\n\t" \
  "v_mov_b32_dpp v207, v207 row_ror:8 row_mask:0xf bank_mask:0x3\n\t" \
  "v_max_u32 v200, v252, v253\n\t" \
  "v_cmp_le_u32 vcc, %[sent], v200\n\t" \
  "v_mfma_f32_16x16x32_bf16 v[208:211], v[220:223], v[204:207], v[208:211]\n\t" \
  ".Lpv%=:\n\t" \
  "v_mfma_f32_16x16x32_bf16 v[224:227], v[192:195], v[204:207], v[224:227]\n\t" \
  "v_mfma_f32_16x16x32_bf16 v[228:231], v[196:199], v[204:207], v[228:231]\n\t" \
  "s_cbranch_vccnz .Lvs%=\n\t" \
  ".Lvok%=:\n\t" \
  "s_nop 4\n\t" \
  "v_rcp_f32 v200, v208\n\t" \
  "v_cndmask_b32 v224, v224, v226, %[nh]\n\t" \
  "v_cndmask_b32 v225, v225, v227, %[nh]\n\t" \
  "v_cndmask_b32 v228, v228, v230, %[nh]\n\t" \
  "v_cndmask_b32 v229, v229, v231, %[nh]\n\t" \
  "v_pk_mul_f32 v[224:225], v[224:225], v[200:201] op_sel_hi:[1,0]\n\t" \
  "v_pk_mul_f32 v[228:229], v[228:229], v[200:201] op_sel_hi:[1,0]\n\t" \
  "v_cvt_pk_bf16_f32 v224, v224, v225\n\t" \
  "v_cvt_pk_bf16_f32 v225, v228, v229\n\t" \
  "buffer_store_dwordx2 v[224:225], v251, %[rs], %[so] offen sc1\n\t" \
  "s_memtime s[90:91]\n\t" \
  "s_setprio 0\n\t" \
  "s_waitcnt lgkmcnt(0)\n\t" \
  "s_mov_b32 %[tout], s90\n\t" \
  "s_mov_b32 %[spins], s92\n\t" \
  "s_branch .Lend%=\n\t" \
  ".Lre%=:\n\t" \
  "ds_read_b64_tr_b16 v[232:233], v250 offset:0\n\t" \
  "ds_read_b64_tr_b16 v[234:235], v250 offset:64\n\t" \
  "ds_read_b64_tr_b16 v[236:237], v250 offset:512\n\t" \
  "ds_read_b64_tr_b16 v[238:239], v250 offset:576\n\t" \
  "ds_read_b64_tr_b16 v[240:241], v250 offset:1024\n\t" \
  "ds_read_b64_tr_b16 v[242:243], v250 offset:1088\n\t" \
  "s_waitcnt lgkmcnt(0)\n\t" \
  "s_branch .Lrb%=\n\t" \
  ".Lvs%=:\n\t" \
  "v_cmp_eq_u32 vcc, %[sent], v252\n\t" \
  "v_cmp_eq_u32 s[94:95], %[sent], v253\n\t" \
  "s_or_b64 s[94:95], s[94:95], vcc\n\t" \
  "s_cbranch_scc0 .Lvok%=\n\t" \
  "s_add_u32 s92, s92, 1\n\t" \
  "s_sleep 2\n\t" \
  "s_and_b32 s93, s92, 63\n\t" \
  "s_cbranch_scc1 .Lvr%=\n\t" \
  "buffer_load_dword v200, off, %[rs], %[sab] sc1\n\t" \
  "s_waitcnt vmcnt(0)\n\t" \
  "v_readfirstlane_b32 s93, v200\n\t" \
  "s_nop 3\n\t" \
  "s_cmp_eq_u32 s93, 1\n\t" \
  "s_cbranch_scc1 .Lvok%=\n\t" \
  "s_cmp_lt_u32 s92, 0x80000\n\t" \
  "s_cbranch_scc1 .Lvr%=\n\t" \
  "v_mov_b32 v200, 1\n\t" \
  "buffer_store_dword v200, off, %[rs], %[sab] sc1\n\t" \
  "s_branch .Lvok%=\n\t" \
  ".Lvr%=:\n\t" \
  "buffer_load_dword v252, v249, %[rs], %[sv] offen offset:0 sc1\n\t" \
  "buffer_load_dword v253, v249, %[rs], %[sv] offen offset:256 sc1\n\t" \
  "s_waitcnt vmcnt(0)\n\t" \
  "v_mfma_f32_16x16x16_bf16 v[224:227], v[252:253], v[244:245], 0\n\t" \
  "v_mfma_f32_16x16x16_bf16 v[228:231], v[252:253], v[246:247], 0\n\t" \
  "v_max_u32 v200, v252, v253\n\t" \
  "v_cmp_le_u32 vcc, %[sent], v200\n\t" \
  "s_nop 4\n\t" \
  "s_branch .Lpv%=\n\t" \
  "s_branch .Lend%=\n\t" \
  ".Lslow%=:\n\t" \
  "s_mov_b64 s[94:95], 0\n\t" \
  "v_cmp_eq_u32 vcc, %[sent], v200\n\t" \
  "s_or_b64 s[94:95], s[94:95], vcc\n\t" \
  "v_cmp_eq_u32 vcc, %[sent], v201\n\t" \
  "s_or_b64 s[94:95], s[94:95], vcc\n\t" \
  "v_cmp_eq_u32 vcc, %[sent], v202\n\t" \
  "s_or_b64 s[94:95], s[94:95], vcc\n\t" \
  "v_cmp_eq_u32 vcc, %[sent], v203\n\t" \
  "s_or_b64 s[94:95], s[94:95], vcc\n\t" \
  "v_cmp_eq_u32 vcc, %[sent], v204\n\t" \
  "s_or_b64 s[94:95], s[94:95], vcc\n\t" \
  "v_cmp_eq_u32 vcc, %[sent], v205\n\t" \
  "s_or_b64 s[94:95], s[94:95], vcc\n\t" \
  "v_cmp_eq_u32 vcc, %[sent], v206\n\t" \
  "s_or_b64 s[94:95], s[94:95], vcc\n\t" \
  "v_cmp_eq_u32 vcc, %[sent], v207\n\t" \
  "s_or_b64 s[94:95], s[94:95], vcc\n\t" \
  "s_cmp_eq_u64 s[94:95], 0\n\t" \
  "s_cbranch_scc1 .Lok%=\n\t" \
  "v_mov_b64 v[192:193], 0\n\t" \
  "v_mov_b64 v[194:195], 0\n\t" \
  "v_mov_b64 v[196:197], 0\n\t" \
  "v_mov_b64 v[198:199], 0\n\t" \
  "s_add_u32 s92, s92, 1\n\t" \
  "s_sleep 6\n\t" \
  "s_cmp_eq_u32 s92, 1\n\t" \
  "s_cbranch_scc1 .Lpoll%=\n\t" \
  "s_sleep 1\n\t" \
  "s_and_b32 s93, s92, 63\n\t" \
  "s_cmp_lg_u32 s93, 0\n\t" \
  "s_cbranch_scc1 .Lpoll%=\n\t" \
  "v_mov_b32 v245, 0\n\t" \
  "buffer_load_dword v244, v245, %[rs], %[sab] offen sc1\n\t" \
  "s_waitcnt vmcnt(0)\n\t" \
  "v_readfirstlane_b32 s93, v244\n\t" \
  "s_nop 3\n\t" \
  "s_cmp_eq_u32 s93, 1\n\t" \
  "s_cbranch_scc1 .Lok%=\n\t" \
  "s_cmp_lt_u32 s92, 0x80000\n\t" \
  "s_cbranch_scc1 .Lpoll%=\n\t" \
  "v_mov_b32 v244, 1\n\t" \
  "buffer_store_dword v244, v245, %[rs], %[sab] offen sc1\n\t" \
  "s_branch .Lok%=\n\t" \
  ".Lend%=:\n\t"
#define QK_ASM_4 \
  "s_waitcnt lgkmcnt(0)\n\t" \
  "v_subrev_u32 v244, 0x8000, v250\n\t" \
  "ds_read_b128 v[216:219], v248 offset:49152\n\t" \
  "ds_read_b128 v[220:223], v248 offset:50176\n\t" \
  "ds_read_b128 v[224:227], v248 offset:51200\n\t" \
  "ds_read_b128 v[228:231], v248 offset:52224\n\t" \
  "ds_read_b128 v[208:211], v244 offset:0\n\t" \
  "ds_read_b128 v[212:215], v244 offset:1024\n\t" \
  "ds_read_b128 v[236:239], v244 offset:2048\n\t" \
  "ds_read_b128 v[240:243], v244 offset:3072\n\t" \
  "v_mov_b64 v[192:193], 0\n\t" \
  "v_mov_b64 v[194:195], 0\n\t" \
  "v_mov_b64 v[196:197], 0\n\t" \
  "v_mov_b64 v[198:199], 0\n\t" \
  "s_movk_i32 s92, 0x2000\n\t" \
  ".Lwt%=:\n\t" \
  "ds_read_b64 v[200:201], v248 offset:30720\n\t" \
  "s_memrealtime s[90:91]\n\t" \
  "s_sub_u32 s92, s92, 1\n\t" \
  "s_waitcnt lgkmcnt(0)\n\t" \
  "v_readfirstlane_b32 s93, v200\n\t" \
  "v_readfirstlane_b32 %[tgt], v201\n\t" \
  "s_cbranch_scc1 .Lgo%=\n\t" \
  "s_cmp_eq_u32 s93, %[step]\n\t" \
  "s_cbranch_scc1 .Lwx%=\n\t" \
  "s_sleep 1\n\t" \
  "s_branch .Lwt%=\n\t" \
  ".Lwx%=:\n\t" \
  "s_lshl_b32 s93, s90, 2\n\t" \
  "s_sub_i32 s93, %[tgt], s93\n\t" \
  "s_cmp_le_i32 s93, 0\n\t" \
  "s_cbranch_scc1 .Lgo%=\n\t" \
  "s_min_i32 s93, s93, 8000\n\t" \
  "s_mov_b32 s94, s93\n\t" \
  ".Lsl%=:\n\t" \
  "s_sub_i32 s94, s94, 18\n\t" \
  "s_cmp_lt_i32 s94, 30\n\t" \
  "s_cbranch_scc1 .Lsx%=\n\t" \
  "s_sleep 1\n\t" \
  "s_branch .Lsl%=\n\t" \
  ".Lsx%=:\n\t" \
  "s_sub_i32 s93, s93, 12\n\t" \
  "s_max_i32 s93, s93, 4\n\t" \
  "s_lshr_b32 s93, s93, 2\n\t" \
  "s_add_u32 s93, s93, s90\n\t" \
  "s_memrealtime s[90:91]\n\t" \
  ".Lpace%=:\n\t" \
  "s_memrealtime s[94:95]\n\t" \
  "s_waitcnt lgkmcnt(1)\n\t" \
  "s_sub_i32 s92, s90, s93\n\t" \
  "s_cmp_ge_i32 s92, 0\n\t" \
  "s_cbranch_scc1 .Le%=\n\t" \
  "s_memrealtime s[90:91]\n\t" \
  "s_waitcnt lgkmcnt(1)\n\t" \
  "s_sub_i32 s92, s94, s93\n\t" \
  "s_cmp_lt_i32 s92, 0\n\t" \
  "s_cbranch_scc1 .Lpace%=\n\t" \
  "s_nop 4\n\t" \
  ".Le%=:\n\t" \
  "s_add_u32 s92, s92, s93\n\t" \
  "s_lshl_b32 s92, s92, 2\n\t" \
  "s_sub_i32 s92, %[tgt], s92\n\t" \
  "s_max_i32 s92, s92, 0\n\t" \
  "s_bitcmp0_b32 s92, 3\n\t" \
  "s_cbranch_scc1 .Lf3%=\n\t" \
  "s_nop 15\n\t" \
  ".Lf3%=:\n\t" \
  "s_bitcmp0_b32 s92, 2\n\t" \
  "s_cbranch_scc1 .Lf2%=\n\t" \
  "s_nop 10\n\t" \
  ".Lf2%=:\n\t" \
  ".Lgo%=:\n\t" \
  "s_mov_b32 s92, 0\n\t" \
  ".Lpoll%=:\n\t" \
  "buffer_load_dwordx4 v[200:203], v248, %[rs], %[som] offen sc1\n\t" \
  "buffer_load_dwordx4 v[204:207], v248, %[rs], %[som2] offen sc1\n\t" \
  "s_waitcnt vmcnt(1) lgkmcnt(0)\n\t" \
  "ds_write_b128 v249, v[200:203]\n\t" \
  "v_smfmac_f32_16x16x64_bf16 v[192:195], v[200:203], v[208:215], v251\n\t" \
  "v_max3_u32 v244, v200, v201, v202\n\t" \
  "v_smfmac_f32_16x16x64_bf16 v[196:199], v[200:203], v[236:243], v251\n\t" \
  "s_waitcnt vmcnt(0)\n\t" \
  "ds_write_b128 v249, v[204:207] offset:8192\n\t" \
  "v_smfmac_f32_16x16x64_bf16 v[192:195], v[204:207], v[216:223], v251\n\t" \
  "v_max3_u32 v244, v244, v203, v204\n\t" \
  "v_max3_u32 v244, v244, v205, v206\n\t" \
  "v_smfmac_f32_16x16x64_bf16 v[196:199], v[204:207], v[224:231], v251\n\t" \
  "v_max_u32 v244, v244, v207\n\t" \
  "v_cmp_le_u32 vcc, %[sent], v244\n\t" \
  "s_cbranch_vccnz .Lslow%=\n\t" \
  ".Lok%=:\n\t" \
  "ds_read_b128 v[216:219], v250 offset:32768\n\t" \
  "ds_read_b128 v[220:223], v250 offset:33792\n\t" \
  "ds_read_b128 v[224:227], v250 offset:34816\n\t" \
  "ds_read_b128 v[228:231], v250 offset:35840\n\t" \
  "s_waitcnt lgkmcnt(4)\n\t" \
  "s_barrier\n\t" \
  "ds_read_b128 v[208:211], v248 offset:5120\n\t" \
  "ds_read_b128 v[212:215], v248 offset:6144\n\t" \
  "ds_read_b128 v[236:239], v248 offset:7168\n\t" \
  "ds_read_b128 v[240:243], v248 offset:8192\n\t" \
  "s_waitcnt lgkmcnt(3)\n\t" \
  "ds_read_b128 v[244:247], v248 offset:13312\n\t" \
  "v_smfmac_f32_16x16x64_bf16 v[192:195], v[208:211], v[216:223], v251\n\t" \
  "ds_read_b128 v[216:219], v250 offset:0\n\t" \
  "v_smfmac_f32_16x16x64_bf16 v[196:199], v[208:211], v[224:231], v251\n\t" \
  "s_waitcnt lgkmcnt(4)\n\t" \
  "ds_read_b128 v[220:223], v250 offset:1024\n\t" \
  "v_smfmac_f32_16x16x64_bf16 v[192:195], v[212:215], v[0:7], v251\n\t" \
  "ds_read_b128 v[224:227], v250 offset:2048\n\t" \
  "v_smfmac_f32_16x16x64_bf16 v[196:199], v[212:215], v[8:15], v251\n\t" \
  "s_waitcnt lgkmcnt(5)\n\t" \
  "ds_read_b128 v[228:231], v250 offset:3072\n\t" \
  "v_smfmac_f32_16x16x64_bf16 v[192:195], v[236:239], v[16:23], v251\n\t" \
  "ds_read_b128 v[200:203], v248 offset:9216\n\t" \
  "v_smfmac_f32_16x16x64_bf16 v[196:199], v[236:239], v[24:31], v251\n\t" \
  "s_waitcnt lgkmcnt(6)\n\t" \
  "ds_read_b128 v[204:207], v248 offset:10240\n\t" \
  "v_smfmac_f32_16x16x64_bf16 v[192:195], v[240:243], v[32:39], v251\n\t" \
  "ds_read_b128 v[208:211], v248 offset:11264\n\t" \
  "v_smfmac_f32_16x16x64_bf16 v[196:199], v[240:243], v[40:47], v251\n\t" \
  "s_waitcnt lgkmcnt(5)\n\t" \
  "ds_read_b128 v[212:215], v248 offset:14336\n\t" \
  "v_smfmac_f32_16x16x64_bf16 v[192:195], v[244:247], v[216:223], v251\n\t" \
  "s_waitcnt lgkmcnt(4)\n\t" \
  "v_smfmac_f32_16x16x64_bf16 v[196:199], v[244:247], v[224:231], v251\n\t" \
  "s_waitcnt lgkmcnt(3)\n\t" \
  "ds_read_b128 v[236:239], v248 offset:15360\n\t" \
  "v_smfmac_f32_16x16x64_bf16 v[192:195], v[200:203], v[48:55], v251\n\t" \
  "ds_read_b128 v[240:243], v248 offset:0\n\t" \
  "v_smfmac_f32_16x16x64_bf16 v[196:199], v[200:203], v[56:63], v251\n\t" \
  "s_waitcnt lgkmcnt(4)\n\t" \
  "v_smfmac_f32_16x16x64_bf16 v[192:195], v[204:207], v[64:71], v251\n\t" \
  "ds_read_b128 v[244:247], v248 offset:1024\n\t" \
  "v_smfmac_f32_16x16x64_bf16 v[196:199], v[204:207], v[72:79], v251\n\t" \
  "s_waitcnt lgkmcnt(4)\n\t" \
  "v_smfmac_f32_16x16x64_bf16 v[192:195], v[208:211], v[80:87], v251\n\t" \
  "ds_read_b128 v[200:203], v248 offset:2048\n\t" \
  "v_smfmac_f32_16x16x64_bf16 v[196:199], v[208:211], v[88:95], v251\n\t" \
  "s_waitcnt lgkmcnt(4)\n\t" \
  "v_smfmac_f32_16x16x64_bf16 v[192:195], v[212:215], v[96:103], v251\n\t" \
  "ds_read_b128 v[204:207], v248 offset:3072\n\t" \
  "v_smfmac_f32_16x16x64_bf16 v[196:199], v[212:215], v[104:111], v251\n\t" \
  "s_waitcnt lgkmcnt(3)\n\t" \
  "v_smfmac_f32_16x16x64_bf16 v[192:195], v[236:239], v[112:119], v251\n\t" \
  "v_smfmac_f32_16x16x64_bf16 v[196:199], v[236:239], v[120:127], v251\n\t" \
  "v_smfmac_f32_16x16x64_bf16 v[192:195], v[240:243], v[128:135], v251\n\t" \
  "v_smfmac_f32_16x16x64_bf16 v[196:199], v[240:243], v[136:143], v251\n\t" \
  "s_waitcnt lgkmcnt(1)\n\t" \
  "v_smfmac_f32_16x16x64_bf16 v[192:195], v[244:247], v[144:151], v251\n\t" \
  "v_smfmac_f32_16x16x64_bf16 v[196:199], v[244:247], v[152:159], v251\n\t" \
  "v_smfmac_f32_16x16x64_bf16 v[192:195], v[200:203], v[160:167], v251\n\t" \
  "v_smfmac_f32_16x16x64_bf16 v[196:199], v[200:203], v[168:175], v251\n\t" \
  "s_waitcnt lgkmcnt(0)\n\t" \
  "v_smfmac_f32_16x16x64_bf16 v[192:195], v[204:207], v[176:183], v251\n\t" \
  "v_smfmac_f32_16x16x64_bf16 v[196:199], v[204:207], v[184:191], v251\n\t" \
  "s_nop 6\n\t" \
  "v_add_f32 v192, v192, v194\n\t" \
  "v_add_f32 v193, v193, v195\n\t" \
  "v_add_f32 v196, v196, v198\n\t" \
  "v_add_f32 v197, v197, v199\n\t" \
  "v_cvt_pk_bf16_f32 v192, v192, v193\n\t" \
  "v_cvt_pk_bf16_f32 v196, v196, v197\n\t" \
  "ds_write2_b32 v252, v192, v196 offset1:64\n\t" \
  "s_waitcnt lgkmcnt(0)\n\t" \
  "s_barrier\n\t" \
  "buffer_store_dword v192, v235, %[rs], %[sk] offen sc1\n\t" \
  "buffer_store_dword v196, v235, %[rs], %[sk] offen offset:256 sc1\n\t" \
  "s_mov_b32 %[spins], s92\n\t" \
  "s_branch .Lend%=\n\t" \
  ".Lslow%=:\n\t" \
  "s_mov_b64 s[94:95], 0\n\t" \
  "v_cmp_eq_u32 vcc, %[sent], v200\n\t" \
  "s_or_b64 s[94:95], s[94:95], vcc\n\t" \
  "v_cmp_eq_u32 vcc, %[sent], v201\n\t" \
  "s_or_b64 s[94:95], s[94:95], vcc\n\t" \
  "v_cmp_eq_u32 vcc, %[sent], v202\n\t" \
  "s_or_b64 s[94:95], s[94:95], vcc\n\t" \
  "v_cmp_eq_u32 vcc, %[sent], v203\n\t" \
  "s_or_b64 s[94:95], s[94:95], vcc\n\t" \
  "v_cmp_eq_u32 vcc, %[sent], v204\n\t" \
  "s_or_b64 s[94:95], s[94:95], vcc\n\t" \
  "v_cmp_eq_u32 vcc, %[sent], v205\n\t" \
  "s_or_b64 s[94:95], s[94:95], vcc\n\t" \
  "v_cmp_eq_u32 vcc, %[sent], v206\n\t" \
  "s_or_b64 s[94:95], s[94:95], vcc\n\t" \
  "v_cmp_eq_u32 vcc, %[sent], v207\n\t" \
  "s_or_b64 s[94:95], s[94:95], vcc\n\t" \
  "s_cmp_eq_u64 s[94:95], 0\n\t" \
  "s_cbranch_scc1 .Lok%=\n\t" \
  "v_mov_b64 v[192:193], 0\n\t" \
  "v_mov_b64 v[194:195], 0\n\t" \
  "v_mov_b64 v[196:197], 0\n\t" \
  "v_mov_b64 v[198:199], 0\n\t" \
  "s_add_u32 s92, s92, 1\n\t" \
  "s_sleep 6\n\t" \
  "s_cmp_eq_u32 s92, 1\n\t" \
  "s_cbranch_scc1 .Lpoll%=\n\t" \
  "s_sleep 1\n\t" \
  "s_and_b32 s93, s92, 63\n\t" \
  "s_cmp_lg_u32 s93, 0\n\t" \
  "s_cbranch_scc1 .Lpoll%=\n\t" \
  "v_mov_b32 v245, 0\n\t" \
  "buffer_load_dword v244, v245, %[rs], %[sab] offen sc1\n\t" \
  "s_waitcnt vmcnt(0)\n\t" \
  "v_readfirstlane_b32 s93, v244\n\t" \
  "s_nop 3\n\t" \
  "s_cmp_eq_u32 s93, 1\n\t" \
  "s_cbranch_scc1 .Lok%=\n\t" \
  "s_cmp_lt_u32 s92, 0x80000\n\t" \
  "s_cbranch_scc1 .Lpoll%=\n\t" \
  "v_mov_b32 v244, 1\n\t" \
  "buffer_store_dword v244, v245, %[rs], %[sab] offen sc1\n\t" \
  "s_branch .Lok%=\n\t" \
  ".Lend%=:\n\t"
#define QK_ASM_5 \
  "s_waitcnt lgkmcnt(0)\n\t" \
  "v_subrev_u32 v244, 0x8000, v250\n\t" \
  "ds_read_b128 v[216:219], v248 offset:53248\n\t" \
  "ds_read_b128 v[220:223], v248 offset:54272\n\t" \
  "ds_read_b128 v[224:227], v248 offset:55296\n\t" \
  "ds_read_b128 v[228:231], v248 offset:56320\n\t" \
  "ds_read_b128 v[208:211], v244 offset:0\n\t" \
  "ds_read_b128 v[212:215], v244 offset:1024\n\t" \
  "ds_read_b128 v[236:239], v244 offset:2048\n\t" \
  "ds_read_b128 v[240:243], v244 offset:3072\n\t" \
  "v_mov_b64 v[192:193], 0\n\t" \
  "v_mov_b64 v[194:195], 0\n\t" \
  "v_mov_b64 v[196:197], 0\n\t" \
  "v_mov_b64 v[198:199], 0\n\t" \
  "s_movk_i32 s92, 0x2000\n\t" \
  ".Lwt%=:\n\t" \
  "ds_read_b64 v[200:201], v248 offset:30720\n\t" \
  "s_memrealtime s[90:91]\n\t" \
  "s_sub_u32 s92, s92, 1\n\t" \
  "s_waitcnt lgkmcnt(0)\n\t" \
  "v_readfirstlane_b32 s93, v200\n\t" \
  "v_readfirstlane_b32 %[tgt], v201\n\t" \
  "s_cbranch_scc1 .Lgo%=\n\t" \
  "s_cmp_eq_u32 s93, %[step]\n\t" \
  "s_cbranch_scc1 .Lwx%=\n\t" \
  "s_sleep 1\n\t" \
  "s_branch .Lwt%=\n\t" \
  ".Lwx%=:\n\t" \
  "s_lshl_b32 s93, s90, 2\n\t" \
  "s_sub_i32 s93, %[tgt], s93\n\t" \
  "s_cmp_le_i32 s93, 0\n\t" \
  "s_cbranch_scc1 .Lgo%=\n\t" \
  "s_min_i32 s93, s93, 8000\n\t" \
  "s_mov_b32 s94, s93\n\t" \
  ".Lsl%=:\n\t" \
  "s_sub_i32 s94, s94, 18\n\t" \
  "s_cmp_lt_i32 s94, 30\n\t" \
  "s_cbranch_scc1 .Lsx%=\n\t" \
  "s_sleep 1\n\t" \
  "s_branch .Lsl%=\n\t" \
  ".Lsx%=:\n\t" \
  "s_sub_i32 s93, s93, 12\n\t" \
  "s_max_i32 s93, s93, 4\n\t" \
  "s_lshr_b32 s93, s93, 2\n\t" \
  "s_add_u32 s93, s93, s90\n\t" \
  "s_memrealtime s[90:91]\n\t" \
  ".Lpace%=:\n\t" \
  "s_memrealtime s[94:95]\n\t" \
  "s_waitcnt lgkmcnt(1)\n\t" \
  "s_sub_i32 s92, s90, s93\n\t" \
  "s_cmp_ge_i32 s92, 0\n\t" \
  "s_cbranch_scc1 .Le%=\n\t" \
  "s_memrealtime s[90:91]\n\t" \
  "s_waitcnt lgkmcnt(1)\n\t" \
  "s_sub_i32 s92, s94, s93\n\t" \
  "s_cmp_lt_i32 s92, 0\n\t" \
  "s_cbranch_scc1 .Lpace%=\n\t" \
  "s_nop 4\n\t" \
  ".Le%=:\n\t" \
  "s_add_u32 s92, s92, s93\n\t" \
  "s_lshl_b32 s92, s92, 2\n\t" \
  "s_sub_i32 s92, %[tgt], s92\n\t" \
  "s_max_i32 s92, s92, 0\n\t" \
  "s_bitcmp0_b32 s92, 3\n\t" \
  "s_cbranch_scc1 .Lf3%=\n\t" \
  "s_nop 15\n\t" \
  ".Lf3%=:\n\t" \
  "s_bitcmp0_b32 s92, 2\n\t" \
  "s_cbranch_scc1 .Lf2%=\n\t" \
  "s_nop 10\n\t" \
  ".Lf2%=:\n\t" \
  ".Lgo%=:\n\t" \
  "s_mov_b32 s92, 0\n\t" \
  ".Lpoll%=:\n\t" \
  "buffer_load_dwordx4 v[200:203], v248, %[rs], %[som] offen sc1\n\t" \
  "buffer_load_dwordx4 v[204:207], v248, %[rs], %[som2] offen sc1\n\t" \
  "s_waitcnt vmcnt(1) lgkmcnt(0)\n\t" \
  "ds_write_b128 v249, v[200:203]\n\t" \
  "v_smfmac_f32_16x16x64_bf16 v[192:195], v[200:203], v[208:215], v251\n\t" \
  "v_max3_u32 v244, v200, v201, v202\n\t" \
  "v_smfmac_f32_16x16x64_bf16 v[196:199], v[200:203], v[236:243], v251\n\t" \
  "s_waitcnt vmcnt(0)\n\t" \
  "ds_write_b128 v249, v[204:207] offset:8192\n\t" \
  "v_smfmac_f32_16x16x64_bf16 v[192:195], v[204:207], v[216:223], v251\n\t" \
  "v_max3_u32 v244, v244, v203, v204\n\t" \
  "v_max3_u32 v244, v244, v205, v206\n\t" \
  "v_smfmac_f32_16x16x64_bf16 v[196:199], v[204:207], v[224:231], v251\n\t" \
  "v_max_u32 v244, v244, v207\n\t" \
  "v_cmp_le_u32 vcc, %[sent], v244\n\t" \
  "s_cbranch_vccnz .Lslow%=\n\t" \
  ".Lok%=:\n\t" \
  "ds_read_b128 v[216:219], v250 offset:32768\n\t" \
  "ds_read_b128 v[220:223], v250 offset:33792\n\t" \
  "ds_read_b128 v[224:227], v250 offset:34816\n\t" \
  "ds_read_b128 v[228:231], v250 offset:35840\n\t" \
  "s_waitcnt lgkmcnt(4)\n\t" \
  "s_barrier\n\t" \
  "ds_read_b128 v[208:211], v248 offset:4096\n\t" \
  "ds_read_b128 v[212:215], v248 offset:7168\n\t" \
  "ds_read_b128 v[236:239], v248 offset:8192\n\t" \
  "ds_read_b128 v[240:243], v248 offset:9216\n\t" \
  "s_waitcnt lgkmcnt(3)\n\t" \
  "ds_read_b128 v[244:247], v248 offset:12288\n\t" \
  "v_smfmac_f32_16x16x64_bf16 v[192:195], v[208:211], v[216:223], v251\n\t" \
  "ds_read_b128 v[216:219], v250 offset:0\n\t" \
  "v_smfmac_f32_16x16x64_bf16 v[196:199], v[208:211], v[224:231], v251\n\t" \
  "s_waitcnt lgkmcnt(4)\n\t" \
  "ds_read_b128 v[220:223], v250 offset:1024\n\t" \
  "v_smfmac_f32_16x16x64_bf16 v[192:195], v[212:215], v[0:7], v251\n\t" \
  "ds_read_b128 v[224:227], v250 offset:2048\n\t" \
  "v_smfmac_f32_16x16x64_bf16 v[196:199], v[212:215], v[8:15], v251\n\t" \
  "s_waitcnt lgkmcnt(5)\n\t" \
  "ds_read_b128 v[228:231], v250 offset:3072\n\t" \
  "v_smfmac_f32_16x16x64_bf16 v[192:195], v[236:239], v[16:23], v251\n\t" \
  "ds_read_b128 v[200:203], v248 offset:10240\n\t" \
  "v_smfmac_f32_16x16x64_bf16 v[196:199], v[236:239], v[24:31], v251\n\t" \
  "s_waitcnt lgkmcnt(6)\n\t" \
  "ds_read_b128 v[204:207], v248 offset:11264\n\t" \
  "v_smfmac_f32_16x16x64_bf16 v[192:195], v[240:243], v[32:39], v251\n\t" \
  "ds_read_b128 v[208:211], v248 offset:14336\n\t" \
  "v_smfmac_f32_16x16x64_bf16 v[196:199], v[240:243], v[40:47], v251\n\t" \
  "s_waitcnt lgkmcnt(5)\n\t" \
  "ds_read_b128 v[212:215], v248 offset:15360\n\t" \
  "v_smfmac_f32_16x16x64_bf16 v[192:195], v[244:247], v[216:223], v251\n\t" \
  "s_waitcnt lgkmcnt(4)\n\t" \
  "v_smfmac_f32_16x16x64_bf16 v[196:199], v[244:247], v[224:231], v251\n\t" \
  "s_waitcnt lgkmcnt(3)\n\t" \
  "ds_read_b128 v[236:239], v248 offset:0\n\t" \
  "v_smfmac_f32_16x16x64_bf16 v[192:195], v[200:203], v[48:55], v251\n\t" \
  "ds_read_b128 v[240:243], v248 offset:1024\n\t" \
  "v_smfmac_f32_16x16x64_bf16 v[196:199], v[200:203], v[56:63], v251\n\t" \
  "s_waitcnt lgkmcnt(4)\n\t" \
  "v_smfmac_f32_16x16x64_bf16 v[192:195], v[204:207], v[64:71], v251\n\t" \
  "ds_read_b128 v[244:247], v248 offset:2048\n\t" \
  "v_smfmac_f32_16x16x64_bf16 v[196:199], v[204:207], v[72:79], v251\n\t" \
  "s_waitcnt lgkmcnt(4)\n\t" \
  "v_smfmac_f32_16x16x64_bf16 v[192:195], v[208:211], v[80:87], v251\n\t" \
  "ds_read_b128 v[200:203], v248 offset:3072\n\t" \
  "v_smfmac_f32_16x16x64_bf16 v[196:199], v[208:211], v[88:95], v251\n\t" \
  "s_waitcnt lgkmcnt(4)\n\t" \
  "v_smfmac_f32_16x16x64_bf16 v[192:195], v[212:215], v[96:103], v251\n\t" \
  "ds_read_b128 v[204:207], v248 offset:6144\n\t" \
  "v_smfmac_f32_16x16x64_bf16 v[196:199], v[212:215], v[104:111], v251\n\t" \
  "s_waitcnt lgkmcnt(3)\n\t" \
  "v_smfmac_f32_16x16x64_bf16 v[192:195], v[236:239], v[112:119], v251\n\t" \
  "v_smfmac_f32_16x16x64_bf16 v[196:199], v[236:239], v[120:127], v251\n\t" \
  "v_smfmac_f32_16x16x64_bf16 v[192:195], v[240:243], v[128:135], v251\n\t" \
  "v_smfmac_f32_16x16x64_bf16 v[196:199], v[240:243], v[136:143], v251\n\t" \
  "s_waitcnt lgkmcnt(1)\n\t" \
  "v_smfmac_f32_16x16x64_bf16 v[192:195], v[244:247], v[144:151], v251\n\t" \
  "v_smfmac_f32_16x16x64_bf16 v[196:199], v[244:247], v[152:159], v251\n\t" \
  "v_smfmac_f32_16x16x64_bf16 v[192:195], v[200:203], v[160:167], v251\n\t" \
  "v_smfmac_f32_16x16x64_bf16 v[196:199], v[200:203], v[168:175], v251\n\t" \
  "s_waitcnt lgkmcnt(0)\n\t" \
  "v_smfmac_f32_16x16x64_bf16 v[192:195], v[204:207], v[176:183], v251\n\t" \
  "v_smfmac_f32_16x16x64_bf16 v[196:199], v[204:207], v[184:191], v251\n\t" \
  "s_nop 6\n\t" \
  "v_add_f32 v192, v192, v194\n\t" \
  "v_add_f32 v193, v193, v195\n\t" \
  "v_add_f32 v196, v196, v198\n\t" \
  "v_add_f32 v197, v197, v199\n\t" \
  "v_cvt_pk_bf16_f32 v192, v192, v193\n\t" \
  "v_cvt_pk_bf16_f32 v196, v196, v197\n\t" \
  "ds_write2_b32 v252, v192, v196 offset1:64\n\t" \
  "s_waitcnt lgkmcnt(0)\n\t" \
  "s_barrier\n\t" \
  "buffer_store_dword v192, v235, %[rs], %[sk] offen sc1\n\t" \
  "buffer_store_dword v196, v235, %[rs], %[sk] offen offset:256 sc1\n\t" \
  "s_mov_b32 %[spins], s92\n\t" \
  "s_branch .Lend%=\n\t" \
  ".Lslow%=:\n\t" \
  "s_mov_b64 s[94:95], 0\n\t" \
  "v_cmp_eq_u32 vcc, %[sent], v200\n\t" \
  "s_or_b64 s[94:95], s[94:95], vcc\n\t" \
  "v_cmp_eq_u32 vcc, %[sent], v201\n\t" \
  "s_or_b64 s[94:95], s[94:95], vcc\n\t" \
  "v_cmp_eq_u32 vcc, %[sent], v202\n\t" \
  "s_or_b64 s[94:95], s[94:95], vcc\n\t" \
  "v_cmp_eq_u32 vcc, %[sent], v203\n\t" \
  "s_or_b64 s[94:95], s[94:95], vcc\n\t" \
  "v_cmp_eq_u32 vcc, %[sent], v204\n\t" \
  "s_or_b64 s[94:95], s[94:95], vcc\n\t" \
  "v_cmp_eq_u32 vcc, %[sent], v205\n\t" \
  "s_or_b64 s[94:95], s[94:95], vcc\n\t" \
  "v_cmp_eq_u32 vcc, %[sent], v206\n\t" \
  "s_or_b64 s[94:95], s[94:95], vcc\n\t" \
  "v_cmp_eq_u32 vcc, %[sent], v207\n\t" \
  "s_or_b64 s[94:95], s[94:95], vcc\n\t" \
  "s_cmp_eq_u64 s[94:95], 0\n\t" \
  "s_cbranch_scc1 .Lok%=\n\t" \
  "v_mov_b64 v[192:193], 0\n\t" \
  "v_mov_b64 v[194:195], 0\n\t" \
  "v_mov_b64 v[196:197], 0\n\t" \
  "v_mov_b64 v[198:199], 0\n\t" \
  "s_add_u32 s92, s92, 1\n\t" \
  "s_sleep 6\n\t" \
  "s_cmp_eq_u32 s92, 1\n\t" \
  "s_cbranch_scc1 .Lpoll%=\n\t" \
  "s_sleep 1\n\t" \
  "s_and_b32 s93, s92, 63\n\t" \
  "s_cmp_lg_u32 s93, 0\n\t" \
  "s_cbranch_scc1 .Lpoll%=\n\t" \
  "v_mov_b32 v245, 0\n\t" \
  "buffer_load_dword v244, v245, %[rs], %[sab] offen sc1\n\t" \
  "s_waitcnt vmcnt(0)\n\t" \
  "v_readfirstlane_b32 s93, v244\n\t" \
  "s_nop 3\n\t" \
  "s_cmp_eq_u32 s93, 1\n\t" \
  "s_cbranch_scc1 .Lok%=\n\t" \
  "s_cmp_lt_u32 s92, 0x80000\n\t" \
  "s_cbranch_scc1 .Lpoll%=\n\t" \
  "v_mov_b32 v244, 1\n\t" \
  "buffer_store_dword v244, v245, %[rs], %[sab] offen sc1\n\t" \
  "s_branch .Lok%=\n\t" \
  ".Lend%=:\n\t"
#define QK_ASM_6 \
  "s_waitcnt lgkmcnt(0)\n\t" \
  "v_subrev_u32 v244, 0x8000, v250\n\t" \
  "ds_read_b128 v[216:219], v248 offset:57344\n\t" \
  "ds_read_b128 v[220:223], v248 offset:58368\n\t" \
  "ds_read_b128 v[224:227], v248 offset:59392\n\t" \
  "ds_read_b128 v[228:231], v248 offset:60416\n\t" \
  "ds_read_b128 v[208:211], v244 offset:0\n\t" \
  "ds_read_b128 v[212:215], v244 offset:1024\n\t" \
  "ds_read_b128 v[236:239], v244 offset:2048\n\t" \
  "ds_read_b128 v[240:243], v244 offset:3072\n\t" \
  "v_mov_b64 v[192:193], 0\n\t" \
  "v_mov_b64 v[194:195], 0\n\t" \
  "v_mov_b64 v[196:197], 0\n\t" \
  "v_mov_b64 v[198:199], 0\n\t" \
  "s_movk_i32 s92, 0x2000\n\t" \
  ".Lwt%=:\n\t" \
  "ds_read_b64 v[200:201], v248 offset:30720\n\t" \
  "s_memrealtime s[90:91]\n\t" \
  "s_sub_u32 s92, s92, 1\n\t" \
  "s_waitcnt lgkmcnt(0)\n\t" \
  "v_readfirstlane_b32 s93, v200\n\t" \
  "v_readfirstlane_b32 %[tgt], v201\n\t" \
  "s_cbranch_scc1 .Lgo%=\n\t" \
  "s_cmp_eq_u32 s93, %[step]\n\t" \
  "s_cbranch_scc1 .Lwx%=\n\t" \
  "s_sleep 1\n\t" \
  "s_branch .Lwt%=\n\t" \
  ".Lwx%=:\n\t" \
  "s_lshl_b32 s93, s90, 2\n\t" \
  "s_sub_i32 s93, %[tgt], s93\n\t" \
  "s_cmp_le_i32 s93, 0\n\t" \
  "s_cbranch_scc1 .Lgo%=\n\t" \
  "s_min_i32 s93, s93, 8000\n\t" \
  "s_mov_b32 s94, s93\n\t" \
  ".Lsl%=:\n\t" \
  "s_sub_i32 s94, s94, 18\n\t" \
  "s_cmp_lt_i32 s94, 30\n\t" \
  "s_cbranch_scc1 .Lsx%=\n\t" \
  "s_sleep 1\n\t" \
  "s_branch .Lsl%=\n\t" \
  ".Lsx%=:\n\t" \
  "s_sub_i32 s93, s93, 12\n\t" \
  "s_max_i32 s93, s93, 4\n\t" \
  "s_lshr_b32 s93, s93, 2\n\t" \
  "s_add_u32 s93, s93, s90\n\t" \
  "s_memrealtime s[90:91]\n\t" \
  ".Lpace%=:\n\t" \
  "s_memrealtime s[94:95]\n\t" \
  "s_waitcnt lgkmcnt(1)\n\t" \
  "s_sub_i32 s92, s90, s93\n\t" \
  "s_cmp_ge_i32 s92, 0\n\t" \
  "s_cbranch_scc1 .Le%=\n\t" \
  "s_memrealtime s[90:91]\n\t" \
  "s_waitcnt lgkmcnt(1)\n\t" \
  "s_sub_i32 s92, s94, s93\n\t" \
  "s_cmp_lt_i32 s92, 0\n\t" \
  "s_cbranch_scc1 .Lpace%=\n\t" \
  "s_nop 4\n\t" \
  ".Le%=:\n\t" \
  "s_add_u32 s92, s92, s93\n\t" \
  "s_lshl_b32 s92, s92, 2\n\t" \
  "s_sub_i32 s92, %[tgt], s92\n\t" \
  "s_max_i32 s92, s92, 0\n\t" \
  "s_bitcmp0_b32 s92, 3\n\t" \
  "s_cbranch_scc1 .Lf3%=\n\t" \
  "s_nop 15\n\t" \
  ".Lf3%=:\n\t" \
  "s_bitcmp0_b32 s92, 2\n\t" \
  "s_cbranch_scc1 .Lf2%=\n\t" \
  "s_nop 10\n\t" \
  ".Lf2%=:\n\t" \
  ".Lgo%=:\n\t" \
  "s_mov_b32 s92, 0\n\t" \
  ".Lpoll%=:\n\t" \
  "buffer_load_dwordx4 v[200:203], v248, %[rs], %[som] offen sc1\n\t" \
  "buffer_load_dwordx4 v[204:207], v248, %[rs], %[som2] offen sc1\n\t" \
  "s_waitcnt vmcnt(1) lgkmcnt(0)\n\t" \
  "ds_write_b128 v249, v[200:203]\n\t" \
  "v_smfmac_f32_16x16x64_bf16 v[192:195], v[200:203], v[208:215], v251\n\t" \
  "v_max3_u32 v244, v200, v201, v202\n\t" \
  "v_smfmac_f32_16x16x64_bf16 v[196:199], v[200:203], v[236:243], v251\n\t" \
  "s_waitcnt vmcnt(0)\n\t" \
  "ds_write_b128 v249, v[204:207] offset:8192\n\t" \
  "v_smfmac_f32_16x16x64_bf16 v[192:195], v[204:207], v[216:223], v251\n\t" \
  "v_max3_u32 v244, v244, v203, v204\n\t" \
  "v_max3_u32 v244, v244, v205, v206\n\t" \
  "v_smfmac_f32_16x16x64_bf16 v[196:199], v[204:207], v[224:231], v251\n\t" \
  "v_max_u32 v244, v244, v207\n\t" \
  "v_cmp_le_u32 vcc, %[sent], v244\n\t" \
  "s_cbranch_vccnz .Lslow%=\n\t" \
  ".Lok%=:\n\t" \
  "ds_read_b128 v[216:219], v250 offset:32768\n\t" \
  "ds_read_b128 v[220:223], v250 offset:33792\n\t" \
  "ds_read_b128 v[224:227], v250 offset:34816\n\t" \
  "ds_read_b128 v[228:231], v250 offset:35840\n\t" \
  "s_waitcnt lgkmcnt(4)\n\t" \
  "s_barrier\n\t" \
  "ds_read_b128 v[208:211], v248 offset:7168\n\t" \
  "ds_read_b128 v[212:215], v248 offset:8192\n\t" \
  "ds_read_b128 v[236:239], v248 offset:9216\n\t" \
  "ds_read_b128 v[240:243], v248 offset:10240\n\t" \
  "s_waitcnt lgkmcnt(3)\n\t" \
  "ds_read_b128 v[244:247], v248 offset:13312\n\t" \
  "v_smfmac_f32_16x16x64_bf16 v[192:195], v[208:211], v[216:223], v251\n\t" \
  "ds_read_b128 v[216:219], v250 offset:0\n\t" \
  "v_smfmac_f32_16x16x64_bf16 v[196:199], v[208:211], v[224:231], v251\n\t" \
  "s_waitcnt lgkmcnt(4)\n\t" \
  "ds_read_b128 v[220:223], v250 offset:1024\n\t" \
  "v_smfmac_f32_16x16x64_bf16 v[192:195], v[212:215], v[0:7], v251\n\t" \
  "ds_read_b128 v[224:227], v250 offset:2048\n\t" \
  "v_smfmac_f32_16x16x64_bf16 v[196:199], v[212:215], v[8:15], v251\n\t" \
  "s_waitcnt lgkmcnt(5)\n\t" \
  "ds_read_b128 v[228:231], v250 offset:3072\n\t" \
  "v_smfmac_f32_16x16x64_bf16 v[192:195], v[236:239], v[16:23], v251\n\t" \
  "ds_read_b128 v[200:203], v248 offset:11264\n\t" \
  "v_smfmac_f32_16x16x64_bf16 v[196:199], v[236:239], v[24:31], v251\n\t" \
  "s_waitcnt lgkmcnt(6)\n\t" \
  "ds_read_b128 v[204:207], v248 offset:12288\n\t" \
  "v_smfmac_f32_16x16x64_bf16 v[192:195], v[240:243], v[32:39], v251\n\t" \
  "ds_read_b128 v[208:211], v248 offset:15360\n\t" \
  "v_smfmac_f32_16x16x64_bf16 v[196:199], v[240:243], v[40:47], v251\n\t" \
  "s_waitcnt lgkmcnt(5)\n\t" \
  "ds_read_b128 v[212:215], v248 offset:0\n\t" \
  "v_smfmac_f32_16x16x64_bf16 v[192:195], v[244:247], v[216:223], v251\n\t" \
  "s_waitcnt lgkmcnt(4)\n\t" \
  "v_smfmac_f32_16x16x64_bf16 v[196:199], v[244:247], v[224:231], v251\n\t" \
  "s_waitcnt lgkmcnt(3)\n\t" \
  "ds_read_b128 v[236:239], v248 offset:1024\n\t" \
  "v_smfmac_f32_16x16x64_bf16 v[192:195], v[200:203], v[48:55], v251\n\t" \
  "ds_read_b128 v[240:243], v248 offset:2048\n\t" \
  "v_smfmac_f32_16x16x64_bf16 v[196:199], v[200:203], v[56:63], v251\n\t" \
  "s_waitcnt lgkmcnt(4)\n\t" \
  "v_smfmac_f32_16x16x64_bf16 v[192:195], v[204:207], v[64:71], v251\n\t" \
  "ds_read_b128 v[244:247], v248 offset:3072\n\t" \
  "v_smfmac_f32_16x16x64_bf16 v[196:199], v[204:207], v[72:79], v251\n\t" \
  "s_waitcnt lgkmcnt(4)\n\t" \
  "v_smfmac_f32_16x16x64_bf16 v[192:195], v[208:211], v[80:87], v251\n\t" \
  "ds_read_b128 v[200:203], v248 offset:4096\n\t" \
  "v_smfmac_f32_16x16x64_bf16 v[196:199], v[208:211], v[88:95], v251\n\t" \
  "s_waitcnt lgkmcnt(4)\n\t" \
  "v_smfmac_f32_16x16x64_bf16 v[192:195], v[212:215], v[96:103], v251\n\t" \
  "ds_read_b128 v[204:207], v248 offset:5120\n\t" \
  "v_smfmac_f32_16x16x64_bf16 v[196:199], v[212:215], v[104:111], v251\n\t" \
  "s_waitcnt lgkmcnt(3)\n\t" \
  "v_smfmac_f32_16x16x64_bf16 v[192:195], v[236:239], v[112:119], v251\n\t" \
  "v_smfmac_f32_16x16x64_bf16 v[196:199], v[236:239], v[120:127], v251\n\t" \
  "v_smfmac_f32_16x16x64_bf16 v[192:195], v[240:243], v[128:135], v251\n\t" \
  "v_smfmac_f32_16x16x64_bf16 v[196:199], v[240:243], v[136:143], v251\n\t" \
  "s_waitcnt lgkmcnt(1)\n\t" \
  "v_smfmac_f32_16x16x64_bf16 v[192:195], v[244:247], v[144:151], v251\n\t" \
  "v_smfmac_f32_16x16x64_bf16 v[196:199], v[244:247], v[152:159], v251\n\t" \
  "v_smfmac_f32_16x16x64_bf16 v[192:195], v[200:203], v[160:167], v251\n\t" \
  "v_smfmac_f32_16x16x64_bf16 v[196:199], v[200:203], v[168:175], v251\n\t" \
  "s_waitcnt lgkmcnt(0)\n\t" \
  "v_smfmac_f32_16x16x64_bf16 v[192:195], v[204:207], v[176:183], v251\n\t" \
  "v_smfmac_f32_16x16x64_bf16 v[196:199], v[204:207], v[184:191], v251\n\t" \
  "s_nop 6\n\t" \
  "v_add_f32 v192, v192, v194\n\t" \
  "v_add_f32 v193, v193, v195\n\t" \
  "v_add_f32 v196, v196, v198\n\t" \
  "v_add_f32 v197, v197, v199\n\t" \
  "v_cvt_pk_bf16_f32 v192, v192, v193\n\t" \
  "v_cvt_pk_bf16_f32 v196, v196, v197\n\t" \
  "ds_write2_b32 v252, v192, v196 offset1:64\n\t" \
  "s_waitcnt lgkmcnt(0)\n\t" \
  "s_barrier\n\t" \
  "buffer_store_dword v192, v235, %[rs], %[sk] offen sc1\n\t" \
  "buffer_store_dword v196, v235, %[rs], %[sk] offen offset:256 sc1\n\t" \
  "s_mov_b32 %[spins], s92\n\t" \
  "s_branch .Lend%=\n\t" \
  ".Lslow%=:\n\t" \
  "s_mov_b64 s[94:95], 0\n\t" \
  "v_cmp_eq_u32 vcc, %[sent], v200\n\t" \
  "s_or_b64 s[94:95], s[94:95], vcc\n\t" \
  "v_cmp_eq_u32 vcc, %[sent], v201\n\t" \
  "s_or_b64 s[94:95], s[94:95], vcc\n\t" \
  "v_cmp_eq_u32 vcc, %[sent], v202\n\t" \
  "s_or_b64 s[94:95], s[94:95], vcc\n\t" \
  "v_cmp_eq_u32 vcc, %[sent], v203\n\t" \
  "s_or_b64 s[94:95], s[94:95], vcc\n\t" \
  "v_cmp_eq_u32 vcc, %[sent], v204\n\t" \
  "s_or_b64 s[94:95], s[94:95], vcc\n\t" \
  "v_cmp_eq_u32 vcc, %[sent], v205\n\t" \
  "s_or_b64 s[94:95], s[94:95], vcc\n\t" \
  "v_cmp_eq_u32 vcc, %[sent], v206\n\t" \
  "s_or_b64 s[94:95], s[94:95], vcc\n\t" \
  "v_cmp_eq_u32 vcc, %[sent], v207\n\t" \
  "s_or_b64 s[94:95], s[94:95], vcc\n\t" \
  "s_cmp_eq_u64 s[94:95], 0\n\t" \
  "s_cbranch_scc1 .Lok%=\n\t" \
  "v_mov_b64 v[192:193], 0\n\t" \
  "v_mov_b64 v[194:195], 0\n\t" \
  "v_mov_b64 v[196:197], 0\n\t" \
  "v_mov_b64 v[198:199], 0\n\t" \
  "s_add_u32 s92, s92, 1\n\t" \
  "s_sleep 6\n\t" \
  "s_cmp_eq_u32 s92, 1\n\t" \
  "s_cbranch_scc1 .Lpoll%=\n\t" \
  "s_sleep 1\n\t" \
  "s_and_b32 s93, s92, 63\n\t" \
  "s_cmp_lg_u32 s93, 0\n\t" \
  "s_cbranch_scc1 .Lpoll%=\n\t" \
  "v_mov_b32 v245, 0\n\t" \
  "buffer_load_dword v244, v245, %[rs], %[sab] offen sc1\n\t" \
  "s_waitcnt vmcnt(0)\n\t" \
  "v_readfirstlane_b32 s93, v244\n\t" \
  "s_nop 3\n\t" \
  "s_cmp_eq_u32 s93, 1\n\t" \
  "s_cbranch_scc1 .Lok%=\n\t" \
  "s_cmp_lt_u32 s92, 0x80000\n\t" \
  "s_cbranch_scc1 .Lpoll%=\n\t" \
  "v_mov_b32 v244, 1\n\t" \
  "buffer_store_dword v244, v245, %[rs], %[sab] offen sc1\n\t" \
  "s_branch .Lok%=\n\t" \
  ".Lend%=:\n\t"
#define QK_ASM_7 \
  "s_waitcnt lgkmcnt(0)\n\t" \
  "v_subrev_u32 v244, 0x8000, v250\n\t" \
  "ds_read_b128 v[216:219], v248 offset:61440\n\t" \
  "ds_read_b128 v[220:223], v248 offset:62464\n\t" \
  "ds_read_b128 v[224:227], v248 offset:63488\n\t" \
  "ds_read_b128 v[228:231], v248 offset:64512\n\t" \
  "ds_read_b128 v[208:211], v244 offset:0\n\t" \
  "ds_read_b128 v[212:215], v244 offset:1024\n\t" \
  "ds_read_b128 v[236:239], v244 offset:2048\n\t" \
  "ds_read_b128 v[240:243], v244 offset:3072\n\t" \
  "v_mov_b64 v[192:193], 0\n\t" \
  "v_mov_b64 v[194:195], 0\n\t" \
  "v_mov_b64 v[196:197], 0\n\t" \
  "v_mov_b64 v[198:199], 0\n\t" \
  "s_movk_i32 s92, 0x2000\n\t" \
  ".Lwt%=:\n\t" \
  "ds_read_b64 v[200:201], v248 offset:30720\n\t" \
  "s_memrealtime s[90:91]\n\t" \
  "s_sub_u32 s92, s92, 1\n\t" \
  "s_waitcnt lgkmcnt(0)\n\t" \
  "v_readfirstlane_b32 s93, v200\n\t" \
  "v_readfirstlane_b32 %[tgt], v201\n\t" \
  "s_cbranch_scc1 .Lgo%=\n\t" \
  "s_cmp_eq_u32 s93, %[step]\n\t" \
  "s_cbranch_scc1 .Lwx%=\n\t" \
  "s_sleep 1\n\t" \
  "s_branch .Lwt%=\n\t" \
  ".Lwx%=:\n\t" \
  "s_lshl_b32 s93, s90, 2\n\t" \
  "s_sub_i32 s93, %[tgt], s93\n\t" \
  "s_cmp_le_i32 s93, 0\n\t" \
  "s_cbranch_scc1 .Lgo%=\n\t" \
  "s_min_i32 s93, s93, 8000\n\t" \
  "s_mov_b32 s94, s93\n\t" \
  ".Lsl%=:\n\t" \
  "s_sub_i32 s94, s94, 18\n\t" \
  "s_cmp_lt_i32 s94, 30\n\t" \
  "s_cbranch_scc1 .Lsx%=\n\t" \
  "s_sleep 1\n\t" \
  "s_branch .Lsl%=\n\t" \
  ".Lsx%=:\n\t" \
  "s_sub_i32 s93, s93, 12\n\t" \
  "s_max_i32 s93, s93, 4\n\t" \
  "s_lshr_b32 s93, s93, 2\n\t" \
  "s_add_u32 s93, s93, s90\n\t" \
  "s_memrealtime s[90:91]\n\t" \
  ".Lpace%=:\n\t" \
  "s_memrealtime s[94:95]\n\t" \
  "s_waitcnt lgkmcnt(1)\n\t" \
  "s_sub_i32 s92, s90, s93\n\t" \
  "s_cmp_ge_i32 s92, 0\n\t" \
  "s_cbranch_scc1 .Le%=\n\t" \
  "s_memrealtime s[90:91]\n\t" \
  "s_waitcnt lgkmcnt(1)\n\t" \
  "s_sub_i32 s92, s94, s93\n\t" \
  "s_cmp_lt_i32 s92, 0\n\t" \
  "s_cbranch_scc1 .Lpace%=\n\t" \
  "s_nop 4\n\t" \
  ".Le%=:\n\t" \
  "s_add_u32 s92, s92, s93\n\t" \
  "s_lshl_b32 s92, s92, 2\n\t" \
  "s_sub_i32 s92, %[tgt], s92\n\t" \
  "s_max_i32 s92, s92, 0\n\t" \
  "s_bitcmp0_b32 s92, 3\n\t" \
  "s_cbranch_scc1 .Lf3%=\n\t" \
  "s_nop 15\n\t" \
  ".Lf3%=:\n\t" \
  "s_bitcmp0_b32 s92, 2\n\t" \
  "s_cbranch_scc1 .Lf2%=\n\t" \
  "s_nop 10\n\t" \
  ".Lf2%=:\n\t" \
  ".Lgo%=:\n\t" \
  "s_mov_b32 s92, 0\n\t" \
  "s_memtime s[88:89]\n\t" \
  ".Lpoll%=:\n\t" \
  "buffer_load_dwordx4 v[200:203], v248, %[rs], %[som] offen sc1\n\t" \
  "buffer_load_dwordx4 v[204:207], v248, %[rs], %[som2] offen sc1\n\t" \
  "s_waitcnt vmcnt(1) lgkmcnt(0)\n\t" \
  "ds_write_b128 v249, v[200:203]\n\t" \
  "v_smfmac_f32_16x16x64_bf16 v[192:195], v[200:203], v[208:215], v251\n\t" \
  "v_max3_u32 v244, v200, v201, v202\n\t" \
  "v_smfmac_f32_16x16x64_bf16 v[196:199], v[200:203], v[236:243], v251\n\t" \
  "s_waitcnt vmcnt(0)\n\t" \
  "ds_write_b128 v249, v[204:207] offset:8192\n\t" \
  "v_smfmac_f32_16x16x64_bf16 v[192:195], v[204:207], v[216:223], v251\n\t" \
  "v_max3_u32 v244, v244, v203, v204\n\t" \
  "v_max3_u32 v244, v244, v205, v206\n\t" \
  "v_smfmac_f32_16x16x64_bf16 v[196:199], v[204:207], v[224:231], v251\n\t" \
  "v_max_u32 v244, v244, v207\n\t" \
  "v_cmp_le_u32 vcc, %[sent], v244\n\t" \
  "s_cbranch_vccnz .Lslow%=\n\t" \
  ".Lok%=:\n\t" \
  "ds_read_b128 v[216:219], v250 offset:32768\n\t" \
  "ds_read_b128 v[220:223], v250 offset:33792\n\t" \
  "ds_read_b128 v[224:227], v250 offset:34816\n\t" \
  "ds_read_b128 v[228:231], v250 offset:35840\n\t" \
  "s_waitcnt lgkmcnt(4)\n\t" \
  "s_setprio 3\n\t" \
  "s_barrier\n\t" \
  "s_memrealtime s[90:91]\n\t" \
  "s_memtime s[94:95]\n\t" \
  "v_mov_b32 v232, %[step]\n\t" \
  "v_lshrrev_b32 v234, 1, v248\n\t" \
  "s_mov_b64 exec, 0xffff\n\t" \
  "s_waitcnt lgkmcnt(0)\n\t" \
  "s_lshl_b32 s90, s90, 2\n\t" \
  "s_sub_u32 s94, s94, s88\n\t" \
  "s_mul_i32 s94, s94, 12200\n\t" \
  "s_lshr_b32 s94, s94, 16\n\t" \
  "s_add_u32 s94, s94, %[tgt]\n\t" \
  "s_sub_i32 s93, s90, s94\n\t" \
  "s_sub_i32 s93, s93, 30\n\t" \
  "s_max_i32 s93, s93, 0\n\t" \
  "s_add_u32 s90, s94, s93\n\t" \
  "s_sub_i32 s93, s90, %[tgt]\n\t" \
  "s_cmp_gt_i32 s93, 270\n\t" \
  "s_cselect_b32 s93, 90, 0\n\t" \
  "s_add_u32 s93, s93, s90\n\t" \
  "s_add_u32 s93, s93, %[lead]\n\t" \
  "v_mov_b32 v233, s93\n\t" \
  "buffer_store_dwordx2 v[232:233], v234, %[rs], %[sbc] offen sc1\n\t" \
  "s_mov_b64 exec, -1\n\t" \
  "s_mov_b32 %[tb1], s90\n\t" \
  "s_setprio 0\n\t" \
  "ds_read_b128 v[208:211], v248 offset:6144\n\t" \
  "ds_read_b128 v[212:215], v248 offset:9216\n\t" \
  "ds_read_b128 v[236:239], v248 offset:10240\n\t" \
  "ds_read_b128 v[240:243], v248 offset:11264\n\t" \
  "s_waitcnt lgkmcnt(3)\n\t" \
  "ds_read_b128 v[244:247], v248 offset:12288\n\t" \
  "v_smfmac_f32_16x16x64_bf16 v[192:195], v[208:211], v[216:223], v251\n\t" \
  "ds_read_b128 v[216:219], v250 offset:0\n\t" \
  "v_smfmac_f32_16x16x64_bf16 v[196:199], v[208:211], v[224:231], v251\n\t" \
  "s_waitcnt lgkmcnt(4)\n\t" \
  "ds_read_b128 v[220:223], v250 offset:1024\n\t" \
  "v_smfmac_f32_16x16x64_bf16 v[192:195], v[212:215], v[0:7], v251\n\t" \
  "ds_read_b128 v[224:227], v250 offset:2048\n\t" \
  "v_smfmac_f32_16x16x64_bf16 v[196:199], v[212:215], v[8:15], v251\n\t" \
  "s_waitcnt lgkmcnt(5)\n\t" \
  "ds_read_b128 v[228:231], v250 offset:3072\n\t" \
  "v_smfmac_f32_16x16x64_bf16 v[192:195], v[236:239], v[16:23], v251\n\t" \
  "ds_read_b128 v[200:203], v248 offset:13312\n\t" \
  "v_smfmac_f32_16x16x64_bf16 v[196:199], v[236:239], v[24:31], v251\n\t" \
  "s_waitcnt lgkmcnt(6)\n\t" \
  "ds_read_b128 v[204:207], v248 offset:14336\n\t" \
  "v_smfmac_f32_16x16x64_bf16 v[192:195], v[240:243], v[32:39], v251\n\t" \
  "ds_read_b128 v[208:211], v248 offset:0\n\t" \
  "v_smfmac_f32_16x16x64_bf16 v[196:199], v[240:243], v[40:47], v251\n\t" \
  "s_waitcnt lgkmcnt(5)\n\t" \
  "ds_read_b128 v[212:215], v248 offset:1024\n\t" \
  "v_smfmac_f32_16x16x64_bf16 v[192:195], v[244:247], v[216:223], v251\n\t" \
  "s_waitcnt lgkmcnt(4)\n\t" \
  "v_smfmac_f32_16x16x64_bf16 v[196:199], v[244:247], v[224:231], v251\n\t" \
  "s_waitcnt lgkmcnt(3)\n\t" \
  "ds_read_b128 v[236:239], v248 offset:2048\n\t" \
  "v_smfmac_f32_16x16x64_bf16 v[192:195], v[200:203], v[48:55], v251\n\t" \
  "ds_read_b128 v[240:243], v248 offset:3072\n\t" \
  "v_smfmac_f32_16x16x64_bf16 v[196:199], v[200:203], v[56:63], v251\n\t" \
  "s_waitcnt lgkmcnt(4)\n\t" \
  "v_smfmac_f32_16x16x64_bf16 v[192:195], v[204:207], v[64:71], v251\n\t" \
  "ds_read_b128 v[244:247], v248 offset:4096\n\t" \
  "v_smfmac_f32_16x16x64_bf16 v[196:199], v[204:207], v[72:79], v251\n\t" \
  "s_waitcnt lgkmcnt(4)\n\t" \
  "v_smfmac_f32_16x16x64_bf16 v[192:195], v[208:211], v[80:87], v251\n\t" \
  "ds_read_b128 v[200:203], v248 offset:5120\n\t" \
  "v_smfmac_f32_16x16x64_bf16 v[196:199], v[208:211], v[88:95], v251\n\t" \
  "s_waitcnt lgkmcnt(4)\n\t" \
  "v_smfmac_f32_16x16x64_bf16 v[192:195], v[212:215], v[96:103], v251\n\t" \
  "ds_read_b128 v[204:207], v248 offset:8192\n\t" \
  "v_smfmac_f32_16x16x64_bf16 v[196:199], v[212:215], v[104:111], v251\n\t" \
  "s_waitcnt lgkmcnt(3)\n\t" \
  "v_smfmac_f32_16x16x64_bf16 v[192:195], v[236:239], v[112:119], v251\n\t" \
  "v_smfmac_f32_16x16x64_bf16 v[196:199], v[236:239], v[120:127], v251\n\t" \
  "v_smfmac_f32_16x16x64_bf16 v[192:195], v[240:243], v[128:135], v251\n\t" \
  "v_smfmac_f32_16x16x64_bf16 v[196:199], v[240:243], v[136:143], v251\n\t" \
  "s_waitcnt lgkmcnt(1)\n\t" \
  "v_smfmac_f32_16x16x64_bf16 v[192:195], v[244:247], v[144:151], v251\n\t" \
  "v_smfmac_f32_16x16x64_bf16 v[196:199], v[244:247], v[152:159], v251\n\t" \
  "v_smfmac_f32_16x16x64_bf16 v[192:195], v[200:203], v[160:167], v251\n\t" \
  "v_smfmac_f32_16x16x64_bf16 v[196:199], v[200:203], v[168:175], v251\n\t" \
  "s_waitcnt lgkmcnt(0)\n\t" \
  "v_smfmac_f32_16x16x64_bf16 v[192:195], v[204:207], v[176:183], v251\n\t" \
  "v_smfmac_f32_16x16x64_bf16 v[196:199], v[204:207], v[184:191], v251\n\t" \
  "s_nop 6\n\t" \
  "v_add_f32 v192, v192, v194\n\t" \
  "v_add_f32 v193, v193, v195\n\t" \
  "v_add_f32 v196, v196, v198\n\t" \
  "v_add_f32 v197, v197, v199\n\t" \
  "v_cvt_pk_bf16_f32 v192, v192, v193\n\t" \
  "v_cvt_pk_bf16_f32 v196, v196, v197\n\t" \
  "ds_write2_b32 v252, v192, v196 offset1:64\n\t" \
  "s_waitcnt lgkmcnt(0)\n\t" \
  "s_barrier\n\t" \
  "buffer_store_dword v192, v235, %[rs], %[sk] offen sc1\n\t" \
  "buffer_store_dword v196, v235, %[rs], %[sk] offen offset:256 sc1\n\t" \
  "s_mov_b32 %[spins], s92\n\t" \
  "s_branch .Lend%=\n\t" \
  ".Lslow%=:\n\t" \
  "s_mov_b64 s[94:95], 0\n\t" \
  "v_cmp_eq_u32 vcc, %[sent], v200\n\t" \
  "s_or_b64 s[94:95], s[94:95], vcc\n\t" \
  "v_cmp_eq_u32 vcc, %[sent], v201\n\t" \
  "s_or_b64 s[94:95], s[94:95], vcc\n\t" \
  "v_cmp_eq_u32 vcc, %[sent], v202\n\t" \
  "s_or_b64 s[94:95], s[94:95], vcc\n\t" \
  "v_cmp_eq_u32 vcc, %[sent], v203\n\t" \
  "s_or_b64 s[94:95], s[94:95], vcc\n\t" \
  "v_cmp_eq_u32 vcc, %[sent], v204\n\t" \
  "s_or_b64 s[94:95], s[94:95], vcc\n\t" \
  "v_cmp_eq_u32 vcc, %[sent], v205\n\t" \
  "s_or_b64 s[94:95], s[94:95], vcc\n\t" \
  "v_cmp_eq_u32 vcc, %[sent], v206\n\t" \
  "s_or_b64 s[94:95], s[94:95], vcc\n\t" \
  "v_cmp_eq_u32 vcc, %[sent], v207\n\t" \
  "s_or_b64 s[94:95], s[94:95], vcc\n\t" \
  "s_cmp_eq_u64 s[94:95], 0\n\t" \
  "s_cbranch_scc1 .Lok%=\n\t" \
  "v_mov_b64 v[192:193], 0\n\t" \
  "v_mov_b64 v[194:195], 0\n\t" \
  "v_mov_b64 v[196:197], 0\n\t" \
  "v_mov_b64 v[198:199], 0\n\t" \
  "s_add_u32 s92, s92, 1\n\t" \
  "s_sleep 6\n\t" \
  "s_cmp_eq_u32 s92, 1\n\t" \
  "s_cbranch_scc1 .Lpoll%=\n\t" \
  "s_sleep 1\n\t" \
  "s_and_b32 s93, s92, 63\n\t" \
  "s_cmp_lg_u32 s93, 0\n\t" \
  "s_cbranch_scc1 .Lpoll%=\n\t" \
  "v_mov_b32 v245, 0\n\t" \
  "buffer_load_dword v244, v245, %[rs], %[sab] offen sc1\n\t" \
  "s_waitcnt vmcnt(0)\n\t" \
  "v_readfirstlane_b32 s93, v244\n\t" \
  "s_nop 3\n\t" \
  "s_cmp_eq_u32 s93, 1\n\t" \
  "s_cbranch_scc1 .Lok%=\n\t" \
  "s_cmp_lt_u32 s92, 0x80000\n\t" \
  "s_cbranch_scc1 .Lpoll%=\n\t" \
  "v_mov_b32 v244, 1\n\t" \
  "buffer_store_dword v244, v245, %[rs], %[sab] offen sc1\n\t" \
  "s_branch .Lok%=\n\t" \
  ".Lend%=:\n\t"
#define VV_ASM \
  "s_waitcnt lgkmcnt(0)\n\t" \
  "v_mov_b64 v[128:129], 0\n\t" \
  "v_mov_b64 v[130:131], 0\n\t" \
  "s_movk_i32 s92, 0x2000\n\t" \
  ".Lwt%=:\n\t" \
  "ds_read_b64 v[188:189], v200 offset:30720\n\t" \
  "s_memrealtime s[90:91]\n\t" \
  "s_sub_u32 s92, s92, 1\n\t" \
  "s_waitcnt lgkmcnt(0)\n\t" \
  "v_readfirstlane_b32 s93, v188\n\t" \
  "v_readfirstlane_b32 %[tgt], v189\n\t" \
  "s_cbranch_scc1 .Lgo%=\n\t" \
  "s_cmp_eq_u32 s93, %[step]\n\t" \
  "s_cbranch_scc1 .Lwx%=\n\t" \
  "s_sleep 1\n\t" \
  "s_branch .Lwt%=\n\t" \
  ".Lwx%=:\n\t" \
  "s_lshl_b32 s93, s90, 2\n\t" \
  "s_sub_i32 s93, %[tgt], s93\n\t" \
  "s_cmp_le_i32 s93, 0\n\t" \
  "s_cbranch_scc1 .Lgo%=\n\t" \
  "s_min_i32 s93, s93, 8000\n\t" \
  "s_mov_b32 s94, s93\n\t" \
  ".Lsl%=:\n\t" \
  "s_sub_i32 s94, s94, 18\n\t" \
  "s_cmp_lt_i32 s94, 30\n\t" \
  "s_cbranch_scc1 .Lsx%=\n\t" \
  "s_sleep 1\n\t" \
  "s_branch .Lsl%=\n\t" \
  ".Lsx%=:\n\t" \
  "s_sub_i32 s93, s93, 12\n\t" \
  "s_max_i32 s93, s93, 4\n\t" \
  "s_lshr_b32 s93, s93, 2\n\t" \
  "s_add_u32 s93, s93, s90\n\t" \
  "s_memrealtime s[90:91]\n\t" \
  ".Lpace%=:\n\t" \
  "s_memrealtime s[94:95]\n\t" \
  "s_waitcnt lgkmcnt(1)\n\t" \
  "s_sub_i32 s92, s90, s93\n\t" \
  "s_cmp_ge_i32 s92, 0\n\t" \
  "s_cbranch_scc1 .Le%=\n\t" \
  "s_memrealtime s[90:91]\n\t" \
  "s_waitcnt lgkmcnt(1)\n\t" \
  "s_sub_i32 s92, s94, s93\n\t" \
  "s_cmp_lt_i32 s92, 0\n\t" \
  "s_cbranch_scc1 .Lpace%=\n\t" \
  "s_nop 4\n\t" \
  ".Le%=:\n\t" \
  "s_add_u32 s92, s92, s93\n\t" \
  "s_lshl_b32 s92, s92, 2\n\t" \
  "s_sub_i32 s92, %[tgt], s92\n\t" \
  "s_max_i32 s92, s92, 0\n\t" \
  "s_bitcmp0_b32 s92, 3\n\t" \
  "s_cbranch_scc1 .Lf3%=\n\t" \
  "s_nop 15\n\t" \
  ".Lf3%=:\n\t" \
  "s_bitcmp0_b32 s92, 2\n\t" \
  "s_cbranch_scc1 .Lf2%=\n\t" \
  "s_nop 10\n\t" \
  ".Lf2%=:\n\t" \
  ".Lgo%=:\n\t" \
  "s_mov_b32 s92, 0\n\t" \
  ".Lpoll%=:\n\t" \
  "buffer_load_dwordx4 v[188:191], v200, %[rs], %[som] offen sc1\n\t" \
  "buffer_load_dwordx4 v[192:195], v200, %[rs], %[som] offen offset:1024 sc1\n\t" \
  "s_waitcnt vmcnt(1) lgkmcnt(0)\n\t" \
  "ds_write_b128 v201, v[188:191]\n\t" \
  "v_max3_u32 v196, v188, v189, v190\n\t" \
  "s_waitcnt vmcnt(0)\n\t" \
  "ds_write_b128 v201, v[192:195] offset:1024\n\t" \
  "v_max3_u32 v196, v196, v191, v192\n\t" \
  "v_max3_u32 v196, v196, v193, v194\n\t" \
  "v_max_u32 v196, v196, v195\n\t" \
  "v_cmp_le_u32 vcc, %[sent], v196\n\t" \
  "s_cbranch_vccnz .Lslow%=\n\t" \
  ".Lok%=:\n\t" \
  "s_waitcnt lgkmcnt(0)\n\t" \
  "s_barrier\n\t" \
  "ds_read_b128 v[132:135], v200 offset:0\n\t" \
  "ds_read_b128 v[136:139], v200 offset:1024\n\t" \
  "ds_read_b128 v[140:143], v200 offset:2048\n\t" \
  "ds_read_b128 v[144:147], v200 offset:3072\n\t" \
  "ds_read_b128 v[148:151], v200 offset:4096\n\t" \
  "s_waitcnt lgkmcnt(4)\n\t" \
  "ds_read_b128 v[152:155], v200 offset:5120\n\t" \
  "v_smfmac_f32_16x16x64_bf16 v[128:131], v[132:135], v[0:7], v202\n\t" \
  "s_waitcnt lgkmcnt(4)\n\t" \
  "ds_read_b128 v[156:159], v200 offset:6144\n\t" \
  "v_smfmac_f32_16x16x64_bf16 v[128:131], v[136:139], v[8:15], v202\n\t" \
  "s_waitcnt lgkmcnt(4)\n\t" \
  "ds_read_b128 v[160:163], v200 offset:7168\n\t" \
  "v_smfmac_f32_16x16x64_bf16 v[128:131], v[140:143], v[16:23], v202\n\t" \
  "s_waitcnt lgkmcnt(4)\n\t" \
  "ds_read_b128 v[164:167], v200 offset:8192\n\t" \
  "v_smfmac_f32_16x16x64_bf16 v[128:131], v[144:147], v[24:31], v202\n\t" \
  "s_waitcnt lgkmcnt(4)\n\t" \
  "ds_read_b128 v[168:171], v200 offset:9216\n\t" \
  "v_smfmac_f32_16x16x64_bf16 v[128:131], v[148:151], v[32:39], v202\n\t" \
  "s_waitcnt lgkmcnt(4)\n\t" \
  "ds_read_b128 v[172:175], v200 offset:10240\n\t" \
  "v_smfmac_f32_16x16x64_bf16 v[128:131], v[152:155], v[40:47], v202\n\t" \
  "s_waitcnt lgkmcnt(4)\n\t" \
  "ds_read_b128 v[176:179], v200 offset:11264\n\t" \
  "v_smfmac_f32_16x16x64_bf16 v[128:131], v[156:159], v[48:55], v202\n\t" \
  "s_waitcnt lgkmcnt(4)\n\t" \
  "ds_read_b128 v[180:183], v200 offset:12288\n\t" \
  "v_smfmac_f32_16x16x64_bf16 v[128:131], v[160:163], v[56:63], v202\n\t" \
  "s_waitcnt lgkmcnt(4)\n\t" \
  "ds_read_b128 v[184:187], v200 offset:13312\n\t" \
  "v_smfmac_f32_16x16x64_bf16 v[128:131], v[164:167], v[64:71], v202\n\t" \
  "s_waitcnt lgkmcnt(4)\n\t" \
  "ds_read_b128 v[188:191], v200 offset:14336\n\t" \
  "v_smfmac_f32_16x16x64_bf16 v[128:131], v[168:171], v[72:79], v202\n\t" \
  "s_waitcnt lgkmcnt(4)\n\t" \
  "ds_read_b128 v[192:195], v200 offset:15360\n\t" \
  "v_smfmac_f32_16x16x64_bf16 v[128:131], v[172:175], v[80:87], v202\n\t" \
  "s_waitcnt lgkmcnt(4)\n\t" \
  "v_smfmac_f32_16x16x64_bf16 v[128:131], v[176:179], v[88:95], v202\n\t" \
  "s_waitcnt lgkmcnt(3)\n\t" \
  "v_smfmac_f32_16x16x64_bf16 v[128:131], v[180:183], v[96:103], v202\n\t" \
  "s_waitcnt lgkmcnt(2)\n\t" \
  "v_smfmac_f32_16x16x64_bf16 v[128:131], v[184:187], v[104:111], v202\n\t" \
  "s_waitcnt lgkmcnt(1)\n\t" \
  "v_smfmac_f32_16x16x64_bf16 v[128:131], v[188:191], v[112:119], v202\n\t" \
  "s_waitcnt lgkmcnt(0)\n\t" \
  "v_smfmac_f32_16x16x64_bf16 v[128:131], v[192:195], v[120:127], v202\n\t" \
  "s_nop 7\n\t" \
  "v_add_f32 v128, v128, v130\n\t" \
  "v_add_f32 v129, v129, v131\n\t" \
  "v_cvt_pk_bf16_f32 v128, v128, v129\n\t" \
  "buffer_store_dword v128, v203, %[rs], %[sv] offen sc1\n\t" \
  "ds_write_b32 v204, v128\n\t" \
  "s_waitcnt lgkmcnt(0)\n\t" \
  "s_barrier\n\t" \
  "s_mov_b32 %[spins], s92\n\t" \
  "s_branch .Lend%=\n\t" \
  ".Lslow%=:\n\t" \
  "s_mov_b64 s[94:95], 0\n\t" \
  "v_cmp_eq_u32 vcc, %[sent], v188\n\t" \
  "s_or_b64 s[94:95], s[94:95], vcc\n\t" \
  "v_cmp_eq_u32 vcc, %[sent], v189\n\t" \
  "s_or_b64 s[94:95], s[94:95], vcc\n\t" \
  "v_cmp_eq_u32 vcc, %[sent], v190\n\t" \
  "s_or_b64 s[94:95], s[94:95], vcc\n\t" \
  "v_cmp_eq_u32 vcc, %[sent], v191\n\t" \
  "s_or_b64 s[94:95], s[94:95], vcc\n\t" \
  "v_cmp_eq_u32 vcc, %[sent], v192\n\t" \
  "s_or_b64 s[94:95], s[94:95], vcc\n\t" \
  "v_cmp_eq_u32 vcc, %[sent], v193\n\t" \
  "s_or_b64 s[94:95], s[94:95], vcc\n\t" \
  "v_cmp_eq_u32 vcc, %[sent], v194\n\t" \
  "s_or_b64 s[94:95], s[94:95], vcc\n\t" \
  "v_cmp_eq_u32 vcc, %[sent], v195\n\t" \
  "s_or_b64 s[94:95], s[94:95], vcc\n\t" \
  "s_cmp_eq_u64 s[94:95], 0\n\t" \
  "s_cbranch_scc1 .Lok%=\n\t" \
  "s_add_u32 s92, s92, 1\n\t" \
  "s_sleep 6\n\t" \
  "s_cmp_eq_u32 s92, 1\n\t" \
  "s_cbranch_scc1 .Lpoll%=\n\t" \
  "s_sleep 1\n\t" \
  "s_and_b32 s93, s92, 63\n\t" \
  "s_cmp_lg_u32 s93, 0\n\t" \
  "s_cbranch_scc1 .Lpoll%=\n\t" \
  "v_mov_b32 v197, 0\n\t" \
  "buffer_load_dword v196, v197, %[rs], %[sab] offen sc1\n\t" \
  "s_waitcnt vmcnt(0)\n\t" \
  "v_readfirstlane_b32 s93, v196\n\t" \
  "s_nop 3\n\t" \
  "s_cmp_eq_u32 s93, 1\n\t" \
  "s_cbranch_scc1 .Lok%=\n\t" \
  "s_cmp_lt_u32 s92, 0x80000\n\t" \
  "s_cbranch_scc1 .Lpoll%=\n\t" \
  "v_mov_b32 v196, 1\n\t" \
  "buffer_store_dword v196, v197, %[rs], %[sab] offen sc1\n\t" \
  "s_branch .Lok%=\n\t" \
  ".Lend%=:\n\t"
#define TK_ASM \
  "buffer_load_dwordx4 v[224:227], v200, %[rs], %[sk] offen offset:0 sc1\n\t" \
  "buffer_load_dwordx4 v[228:231], v200, %[rs], %[sk] offen offset:1024 sc1\n\t" \
  "s_memrealtime s[88:89]\n\t" \
  "v_add_u32 v205, %[tl], v200\n\t" \
  "v_add_u32 v206, %[tq], v205\n\t" \
  "v_add_u32 v207, %[tv], v205\n\t" \
  "s_waitcnt lgkmcnt(0)\n\t" \
  "ds_read_b128 v[128:131], v206 offset:8192\n\t" \
  "ds_read_b128 v[144:147], v205 offset:0\n\t" \
  "ds_read_b128 v[160:163], v205 offset:4096\n\t" \
  "ds_read_b128 v[132:135], v206 offset:9216\n\t" \
  "ds_read_b128 v[148:151], v205 offset:1024\n\t" \
  "ds_read_b128 v[164:167], v205 offset:5120\n\t" \
  "ds_read_b128 v[136:139], v206 offset:10240\n\t" \
  "ds_read_b128 v[152:155], v205 offset:2048\n\t" \
  "ds_read_b128 v[168:171], v205 offset:6144\n\t" \
  "ds_read_b128 v[140:143], v206 offset:11264\n\t" \
  "ds_read_b128 v[156:159], v205 offset:3072\n\t" \
  "ds_read_b128 v[172:175], v205 offset:7168\n\t" \
  "v_add_u32 v206, %[vp], v218\n\t" \
  "s_lshl_b32 s88, s88, 2\n\t" \
  "s_add_u32 s89, s88, %[od]\n\t" \
  "s_waitcnt lgkmcnt(10)\n\t" \
  "v_mfma_f32_16x16x32_bf16 v[196:199], v[144:147], v[128:131], 0\n\t" \
  "s_waitcnt lgkmcnt(9)\n\t" \
  "v_mfma_f32_16x16x32_bf16 v[144:147], v[160:163], v[128:131], 0\n\t" \
  "s_waitcnt lgkmcnt(7)\n\t" \
  "v_mfma_f32_16x16x32_bf16 v[196:199], v[148:151], v[132:135], v[196:199]\n\t" \
  "s_waitcnt lgkmcnt(6)\n\t" \
  "v_mfma_f32_16x16x32_bf16 v[144:147], v[164:167], v[132:135], v[144:147]\n\t" \
  "s_waitcnt lgkmcnt(4)\n\t" \
  "v_mfma_f32_16x16x32_bf16 v[196:199], v[152:155], v[136:139], v[196:199]\n\t" \
  "s_waitcnt lgkmcnt(3)\n\t" \
  "v_mfma_f32_16x16x32_bf16 v[144:147], v[168:171], v[136:139], v[144:147]\n\t" \
  "s_waitcnt lgkmcnt(1)\n\t" \
  "v_mfma_f32_16x16x32_bf16 v[196:199], v[156:159], v[140:143], v[196:199]\n\t" \
  "s_waitcnt lgkmcnt(0)\n\t" \
  "v_mfma_f32_16x16x32_bf16 v[144:147], v[172:175], v[140:143], v[144:147]\n\t" \
  "ds_read_b128 v[176:179], v207 offset:16384\n\t" \
  "ds_read_b128 v[180:183], v207 offset:17408\n\t" \
  "ds_read_b128 v[184:187], v207 offset:18432\n\t" \
  "ds_read_b128 v[188:191], v207 offset:19456\n\t" \
  "ds_read2st64_b32 v[192:193], v206 offset0:0 offset1:1\n\t" \
  "ds_read2st64_b32 v[194:195], v206 offset0:2 offset1:3\n\t" \
  "v_cvt_pk_bf16_f32 v197, v196, v197\n\t" \
  "v_cvt_pk_bf16_f32 v199, v198, v199\n\t" \
  "v_cvt_pk_bf16_f32 v145, v144, v145\n\t" \
  "v_cvt_pk_bf16_f32 v147, v146, v147\n\t" \
  "v_lshlrev_b32 v196, 16, v197\n\t" \
  "v_lshlrev_b32 v198, 16, v199\n\t" \
  "v_lshlrev_b32 v144, 16, v145\n\t" \
  "v_lshlrev_b32 v146, 16, v147\n\t" \
  "v_and_b32 v197, 0xffff0000, v197\n\t" \
  "v_and_b32 v199, 0xffff0000, v199\n\t" \
  "v_and_b32 v145, 0xffff0000, v145\n\t" \
  "v_and_b32 v147, 0xffff0000, v147\n\t" \
  "v_pk_mul_f32 v[196:197], v[196:197], %[sc] op_sel_hi:[1,0]\n\t" \
  "v_pk_mul_f32 v[198:199], v[198:199], %[sc] op_sel_hi:[1,0]\n\t" \
  "v_pk_mul_f32 v[144:145], v[144:145], %[sc] op_sel_hi:[1,0]\n\t" \
  "v_pk_mul_f32 v[146:147], v[146:147], %[sc] op_sel_hi:[1,0]\n\t" \
  "v_cvt_pk_bf16_f32 v197, v196, v197\n\t" \
  "v_cvt_pk_bf16_f32 v199, v198, v199\n\t" \
  "v_cvt_pk_bf16_f32 v145, v144, v145\n\t" \
  "v_cvt_pk_bf16_f32 v147, v146, v147\n\t" \
  "v_lshlrev_b32 v196, 16, v197\n\t" \
  "v_lshlrev_b32 v198, 16, v199\n\t" \
  "v_lshlrev_b32 v144, 16, v145\n\t" \
  "v_lshlrev_b32 v146, 16, v147\n\t" \
  "v_and_b32 v197, 0xffff0000, v197\n\t" \
  "v_and_b32 v199, 0xffff0000, v199\n\t" \
  "v_and_b32 v145, 0xffff0000, v145\n\t" \
  "v_and_b32 v147, 0xffff0000, v147\n\t" \
  "v_min_f32 v196, v196, v208\n\t" \
  "v_min_f32 v197, v197, v209\n\t" \
  "v_min_f32 v198, v198, v210\n\t" \
  "v_min_f32 v199, v199, v211\n\t" \
  "v_min_f32 v144, v144, v212\n\t" \
  "v_min_f32 v145, v145, v213\n\t" \
  "v_min_f32 v146, v146, v214\n\t" \
  "v_min_f32 v147, v147, v215\n\t" \
  "v_max3_f32 v148, v196, v197, v198\n\t" \
  "v_max3_f32 v149, v199, v144, v145\n\t" \
  "v_max3_f32 v148, v148, v146, v147\n\t" \
  "v_max_f32 v150, v148, v149\n\t" \
  "v_max_f32 v151, v148, v149\n\t" \
  "s_nop 1\n\t" \
  "v_permlane32_swap_b32 v150, v151\n\t" \
  "v_max_f32 v150, v150, v151\n\t" \
  "s_mov_b32 s92, 0\n\t" \
  ".Ltake%=:\n\t" \
  "s_waitcnt vmcnt(1) lgkmcnt(0)\n\t" \
  "ds_write_b128 v216, v[224:227]\n\t" \
  "v_max3_u32 v148, v224, v225, v226\n\t" \
  "s_waitcnt vmcnt(0)\n\t" \
  "ds_write_b128 v216, v[228:231] offset:1024\n\t" \
  "ds_read_b64_tr_b16 v[160:161], v217 offset:0\n\t" \
  "ds_read_b64_tr_b16 v[162:163], v217 offset:64\n\t" \
  "ds_read_b64_tr_b16 v[164:165], v217 offset:512\n\t" \
  "ds_read_b64_tr_b16 v[166:167], v217 offset:576\n\t" \
  "ds_read_b64_tr_b16 v[168:169], v217 offset:1024\n\t" \
  "ds_read_b64_tr_b16 v[170:171], v217 offset:1088\n\t" \
  "ds_read_b64_tr_b16 v[172:173], v217 offset:1536\n\t" \
  "ds_read_b64_tr_b16 v[174:175], v217 offset:1600\n\t" \
  "v_max3_u32 v148, v148, v227, v228\n\t" \
  "v_max3_u32 v148, v148, v229, v230\n\t" \
  "v_max_u32 v148, v148, v231\n\t" \
  "v_cmp_le_u32 vcc, %[sent], v148\n\t" \
  "s_cbranch_vccnz .Lslow%=\n\t" \
  ".Lok%=:\n\t" \
  "s_waitcnt lgkmcnt(6)\n\t" \
  "v_mfma_f32_16x16x32_bf16 v[152:155], v[160:163], v[128:131], 0\n\t" \
  "s_waitcnt lgkmcnt(4)\n\t" \
  "v_mfma_f32_16x16x32_bf16 v[152:155], v[164:167], v[132:135], v[152:155]\n\t" \
  "s_waitcnt lgkmcnt(2)\n\t" \
  "v_mfma_f32_16x16x32_bf16 v[152:155], v[168:171], v[136:139], v[152:155]\n\t" \
  "s_waitcnt lgkmcnt(0)\n\t" \
  "v_mfma_f32_16x16x32_bf16 v[152:155], v[172:175], v[140:143], v[152:155]\n\t" \
  "v_mov_b32 v157, 0\n\t" \
  "v_mov_b32 v158, 0\n\t" \
  "s_nop 5\n\t" \
  "v_cvt_pk_bf16_f32 v153, v152, v153\n\t" \
  "v_cvt_pk_bf16_f32 v155, v154, v155\n\t" \
  "v_lshlrev_b32 v152, 16, v153\n\t" \
  "v_lshlrev_b32 v154, 16, v155\n\t" \
  "v_and_b32 v153, 0xffff0000, v153\n\t" \
  "v_and_b32 v155, 0xffff0000, v155\n\t" \
  "v_pk_mul_f32 v[152:153], v[152:153], %[sc] op_sel_hi:[1,0]\n\t" \
  "v_pk_mul_f32 v[154:155], v[154:155], %[sc] op_sel_hi:[1,0]\n\t" \
  "v_cvt_pk_bf16_f32 v153, v152, v153\n\t" \
  "v_cvt_pk_bf16_f32 v155, v154, v155\n\t" \
  "v_lshlrev_b32 v152, 16, v153\n\t" \
  "v_lshlrev_b32 v154, 16, v155\n\t" \
  "v_and_b32 v153, 0xffff0000, v153\n\t" \
  "v_and_b32 v155, 0xffff0000, v155\n\t" \
  "v_max3_f32 v148, v152, v153, v150\n\t" \
  "v_max3_f32 v149, v148, v154, v155\n\t" \
  "v_max3_f32 v150, v148, v154, v155\n\t" \
  "v_cndmask_b32 v152, v152, v154, %[up]\n\t" \
  "v_cndmask_b32 v153, v153, v155, %[up]\n\t" \
  "v_permlane16_swap_b32 v149, v150\n\t" \
  "v_max_f32 v148, v149, v150\n\t" \
  "v_mul_f32 v148, %[negc], v148\n\t" \
  "v_fma_f32 v152, v152, %[c], v148\n\t" \
  "v_fma_f32 v153, v153, %[c], v148\n\t" \
  "v_exp_f32 v152, v152\n\t" \
  "v_fma_f32 v196, v196, %[c], v148\n\t" \
  "v_exp_f32 v153, v153\n\t" \
  "v_fma_f32 v197, v197, %[c], v148\n\t" \
  "v_exp_f32 v196, v196\n\t" \
  "v_fma_f32 v198, v198, %[c], v148\n\t" \
  "v_exp_f32 v197, v197\n\t" \
  "v_fma_f32 v199, v199, %[c], v148\n\t" \
  "v_exp_f32 v198, v198\n\t" \
  "v_fma_f32 v144, v144, %[c], v148\n\t" \
  "v_pk_add_f32 v[132:133], v[152:153], v[196:197]\n\t" \
  "v_exp_f32 v199, v199\n\t" \
  "v_fma_f32 v145, v145, %[c], v148\n\t" \
  "v_exp_f32 v144, v144\n\t" \
  "v_fma_f32 v146, v146, %[c], v148\n\t" \
  "v_pk_add_f32 v[132:133], v[132:133], v[198:199]\n\t" \
  "v_exp_f32 v145, v145\n\t" \
  "v_fma_f32 v147, v147, %[c], v148\n\t" \
  "v_exp_f32 v146, v146\n\t" \
  "v_pk_add_f32 v[132:133], v[132:133], v[144:145]\n\t" \
  "v_exp_f32 v147, v147\n\t" \
  "s_nop 0\n\t" \
  "v_pk_add_f32 v[132:133], v[132:133], v[146:147]\n\t" \
  "v_add_f32 v134, v132, v133\n\t" \
  "v_add_f32 v135, v132, v133\n\t" \
  "s_nop 1\n\t" \
  "v_permlane16_swap_b32 v134, v135\n\t" \
  "v_add_f32 v132, v134, v135\n\t" \
  "v_add_f32 v133, v134, v135\n\t" \
  "s_nop 1\n\t" \
  "v_permlane32_swap_b32 v132, v133\n\t" \
  "v_add_f32 v132, v132, v133\n\t" \
  "v_rcp_f32 v148, v132\n\t" \
  "s_nop 0\n\t" \
  "v_pk_mul_f32 v[152:153], v[152:153], v[148:149] op_sel_hi:[1,0]\n\t" \
  "v_pk_mul_f32 v[196:197], v[196:197], v[148:149] op_sel_hi:[1,0]\n\t" \
  "v_pk_mul_f32 v[198:199], v[198:199], v[148:149] op_sel_hi:[1,0]\n\t" \
  "v_pk_mul_f32 v[144:145], v[144:145], v[148:149] op_sel_hi:[1,0]\n\t" \
  "v_pk_mul_f32 v[146:147], v[146:147], v[148:149] op_sel_hi:[1,0]\n\t" \
  "v_cvt_pk_bf16_f32 v156, v152, v153\n\t" \
  "v_cvt_pk_bf16_f32 v159, v152, v153\n\t" \
  "v_cvt_pk_bf16_f32 v128, v196, v197\n\t" \
  "v_cvt_pk_bf16_f32 v129, v198, v199\n\t" \
  "v_mfma_f32_16x16x16_bf16 v[160:163], v[192:193], v[156:157], 0\n\t" \
  "v_cvt_pk_bf16_f32 v130, v144, v145\n\t" \
  "v_mfma_f32_16x16x16_bf16 v[164:167], v[192:193], v[158:159], 0\n\t" \
  "v_cvt_pk_bf16_f32 v131, v146, v147\n\t" \
  "v_mfma_f32_16x16x16_bf16 v[168:171], v[194:195], v[156:157], 0\n\t" \
  "v_mfma_f32_16x16x16_bf16 v[172:175], v[194:195], v[158:159], 0\n\t" \
  "s_nop 0\n\t" \
  "v_mfma_f32_16x16x32_bf16 v[160:163], v[176:179], v[128:131], v[160:163]\n\t" \
  "s_nop 0\n\t" \
  "v_mfma_f32_16x16x32_bf16 v[164:167], v[180:183], v[128:131], v[164:167]\n\t" \
  "s_nop 0\n\t" \
  "v_mfma_f32_16x16x32_bf16 v[168:171], v[184:187], v[128:131], v[168:171]\n\t" \
  "v_mfma_f32_16x16x32_bf16 v[172:175], v[188:191], v[128:131], v[172:175]\n\t" \
  "s_nop 2\n\t" \
  "v_cvt_pk_bf16_f32 v136, v160, v161\n\t" \
  "v_cvt_pk_bf16_f32 v137, v162, v163\n\t" \
  "v_cvt_pk_bf16_f32 v138, v164, v165\n\t" \
  "v_cvt_pk_bf16_f32 v139, v166, v167\n\t" \
  "v_cvt_pk_bf16_f32 v140, v168, v169\n\t" \
  "v_cvt_pk_bf16_f32 v141, v170, v171\n\t" \
  "v_cvt_pk_bf16_f32 v142, v172, v173\n\t" \
  "v_cvt_pk_bf16_f32 v143, v174, v175\n\t" \
  "s_memrealtime s[90:91]\n\t" \
  "s_waitcnt lgkmcnt(0)\n\t" \
  "s_lshl_b32 s93, s90, 2\n\t" \
  "s_sub_i32 s93, s89, s93\n\t" \
  "s_cmp_le_i32 s93, 0\n\t" \
  "s_cbranch_scc1 .Lgoo%=\n\t" \
  "s_min_i32 s93, s93, 8000\n\t" \
  "s_mov_b32 s94, s93\n\t" \
  ".Lslo%=:\n\t" \
  "s_sub_i32 s94, s94, 18\n\t" \
  "s_cmp_lt_i32 s94, 30\n\t" \
  "s_cbranch_scc1 .Lsxo%=\n\t" \
  "s_sleep 1\n\t" \
  "s_branch .Lslo%=\n\t" \
  ".Lsxo%=:\n\t" \
  "s_add_u32 s93, s93, 3\n\t" \
  "s_lshr_b32 s93, s93, 2\n\t" \
  "s_add_u32 s93, s93, s90\n\t" \
  "s_memrealtime s[90:91]\n\t" \
  ".Lpaceo%=:\n\t" \
  "s_memrealtime s[94:95]\n\t" \
  "s_waitcnt lgkmcnt(1)\n\t" \
  "s_sub_i32 s92, s90, s93\n\t" \
  "s_cmp_ge_i32 s92, 0\n\t" \
  "s_cbranch_scc1 .Leo%=\n\t" \
  "s_memrealtime s[90:91]\n\t" \
  "s_waitcnt lgkmcnt(1)\n\t" \
  "s_sub_i32 s92, s94, s93\n\t" \
  "s_cmp_lt_i32 s92, 0\n\t" \
  "s_cbranch_scc1 .Lpaceo%=\n\t" \
  ".Leo%=:\n\t" \
  ".Lgoo%=:\n\t" \
  "buffer_store_dwordx4 v[136:139], v200, %[rt], %[so] offen offset:0 sc1\n\t" \
  "buffer_store_dwordx4 v[140:143], v200, %[rt], %[so] offen offset:1024 sc1\n\t" \
  "s_waitcnt lgkmcnt(0)\n\t" \
  "s_branch .Lend%=\n\t" \
  ".Lpoll%=:\n\t" \
  "buffer_load_dwordx4 v[224:227], v200, %[rs], %[sk] offen offset:0 sc1\n\t" \
  "buffer_load_dwordx4 v[228:231], v200, %[rs], %[sk] offen offset:1024 sc1\n\t" \
  "s_branch .Ltake%=\n\t" \
  "s_branch .Lend%=\n\t" \
  ".Lslow%=:\n\t" \
  "s_mov_b64 s[94:95], 0\n\t" \
  "v_cmp_eq_u32 vcc, %[sent], v224\n\t" \
  "s_or_b64 s[94:95], s[94:95], vcc\n\t" \
  "v_cmp_eq_u32 vcc, %[sent], v225\n\t" \
  "s_or_b64 s[94:95], s[94:95], vcc\n\t" \
  "v_cmp_eq_u32 vcc, %[sent], v226\n\t" \
  "s_or_b64 s[94:95], s[94:95], vcc\n\t" \
  "v_cmp_eq_u32 vcc, %[sent], v227\n\t" \
  "s_or_b64 s[94:95], s[94:95], vcc\n\t" \
  "v_cmp_eq_u32 vcc, %[sent], v228\n\t" \
  "s_or_b64 s[94:95], s[94:95], vcc\n\t" \
  "v_cmp_eq_u32 vcc, %[sent], v229\n\t" \
  "s_or_b64 s[94:95], s[94:95], vcc\n\t" \
  "v_cmp_eq_u32 vcc, %[sent], v230\n\t" \
  "s_or_b64 s[94:95], s[94:95], vcc\n\t" \
  "v_cmp_eq_u32 vcc, %[sent], v231\n\t" \
  "s_or_b64 s[94:95], s[94:95], vcc\n\t" \
  "s_cmp_eq_u64 s[94:95], 0\n\t" \
  "s_cbranch_scc1 .Lok%=\n\t" \
  "s_add_u32 s92, s92, 1\n\t" \
  "s_sleep 6\n\t" \
  "s_cmp_eq_u32 s92, 1\n\t" \
  "s_cbranch_scc1 .Lpoll%=\n\t" \
  "s_sleep 1\n\t" \
  "s_and_b32 s93, s92, 63\n\t" \
  "s_cmp_lg_u32 s93, 0\n\t" \
  "s_cbranch_scc1 .Lpoll%=\n\t" \
  "v_mov_b32 v149, 0\n\t" \
  "buffer_load_dword v148, v149, %[rs], %[sab] offen sc1\n\t" \
  "s_waitcnt vmcnt(0)\n\t" \
  "v_readfirstlane_b32 s93, v148\n\t" \
  "s_nop 3\n\t" \
  "s_cmp_eq_u32 s93, 1\n\t" \
  "s_cbranch_scc1 .Lok%=\n\t" \
  "s_cmp_lt_u32 s92, 0x80000\n\t" \
  "s_cbranch_scc1 .Lpoll%=\n\t" \
  "v_mov_b32 v148, 1\n\t" \
  "buffer_store_dword v148, v149, %[rs], %[sab] offen sc1\n\t" \
  "s_branch .Lok%=\n\t" \
  ".Lend%=:\n\t"
constexpr int TLAG = 2;
#define RED_ASM \
  "s_mov_b32 %[step], 0\n\t" \
  "s_lshl_b32 %[sh], %[step], 15\n\t" \
  "s_lshr_b32 s93, %[step], 4\n\t" \
  "s_lshr_b32 s93, %[pm], s93\n\t" \
  "s_and_b32 s93, s93, 4\n\t" \
  "s_lshl_b32 s93, s93, 11\n\t" \
  "s_lshl2_add_u32 %[so], %[sh], s93\n\t" \
  "s_sub_i32 s93, %[step], 2\n\t" \
  "s_lshl_b32 %[st], s93, 19\n\t" \
  "s_lshl_b32 s94, s93, 11\n\t" \
  "s_xor_b32 s94, s94, %[s0]\n\t" \
  "s_and_b32 s94, s94, 0x2000\n\t" \
  "s_or_b32 %[st], %[st], s94\n\t" \
  "s_cmp_lt_i32 s93, 0\n\t" \
  "s_cselect_b32 %[st], 0x70000000, %[st]\n\t" \
  "s_add_u32 %[st4], %[st], 4096\n\t" \
  "buffer_load_dwordx2 v[166:167], v173, %[rs], 0 offen sc1\n\t" \
  "v_mov_b64 v[144:145], 0\n\t" \
  "v_mov_b64 v[146:147], 0\n\t" \
  "v_mov_b64 v[148:149], 0\n\t" \
  "v_mov_b64 v[150:151], 0\n\t" \
  "v_mov_b64 v[152:153], 0\n\t" \
  "v_mov_b64 v[154:155], 0\n\t" \
  "v_mov_b64 v[156:157], 0\n\t" \
  "v_mov_b64 v[158:159], 0\n\t" \
  ".Lstep%=:\n\t" \
  "s_setprio 3\n\t" \
  "s_mov_b32 s92, 0\n\t" \
  ".Lcal%=:\n\t" \
  "s_waitcnt vmcnt(0)\n\t" \
  "s_memrealtime s[76:77]\n\t" \
  "v_cmp_ne_u32 vcc, %[step], v166\n\t" \
  "v_readlane_b32 s90, v167, 0\n\t" \
  "v_readlane_b32 s91, v167, 1\n\t" \
  "v_readlane_b32 s94, v167, 2\n\t" \
  "v_readlane_b32 s95, v167, 3\n\t" \
  "s_sub_i32 s90, s90, %[base]\n\t" \
  "s_sub_i32 s91, s91, %[base]\n\t" \
  "s_sub_i32 s94, s94, %[base]\n\t" \
  "s_sub_i32 s95, s95, %[base]\n\t" \
  "s_max_i32 s90, s90, s91\n\t" \
  "s_max_i32 s94, s94, s95\n\t" \
  "s_max_i32 s90, s90, s94\n\t" \
  "s_add_i32 s90, s90, %[ds]\n\t" \
  "s_waitcnt lgkmcnt(0)\n\t" \
  "s_lshl2_add_u32 s76, s76, %[mn]\n\t" \
  "s_sub_i32 s76, s76, %[base]\n\t" \
  "s_add_u32 %[E], s90, %[base]\n\t" \
  "s_and_b32 s93, vcc_lo, 0xf\n\t" \
  "s_cbranch_scc1 .Lbl%=\n\t" \
  ".Lgo%=:\n\t" \
  "v_mov_b32 v166, 7\n\t" \
  "s_mov_b64 exec, 1\n\t" \
  "v_mov_b32 v164, s90\n\t" \
  "ds_max_u32 v168, v164 offset:4100\n\t" \
  "s_mov_b64 exec, -1\n\t" \
  "s_memrealtime s[90:91]\n\t" \
  "s_waitcnt lgkmcnt(0)\n\t" \
  "s_lshl_b32 s93, s90, 2\n\t" \
  "s_sub_i32 s93, %[E], s93\n\t" \
  "s_cmp_le_i32 s93, 0\n\t" \
  "s_cbranch_scc1 .Lgop%=\n\t" \
  "s_min_i32 s93, s93, 8000\n\t" \
  "s_mov_b32 s94, s93\n\t" \
  ".Lslp%=:\n\t" \
  "s_sub_i32 s94, s94, 18\n\t" \
  "s_cmp_lt_i32 s94, 30\n\t" \
  "s_cbranch_scc1 .Lsxp%=\n\t" \
  "s_sleep 1\n\t" \
  "s_branch .Lslp%=\n\t" \
  ".Lsxp%=:\n\t" \
  "s_sub_i32 s93, s93, 12\n\t" \
  "s_max_i32 s93, s93, 4\n\t" \
  "s_lshr_b32 s93, s93, 2\n\t" \
  "s_add_u32 s93, s93, s90\n\t" \
  "s_memrealtime s[90:91]\n\t" \
  ".Lpacep%=:\n\t" \
  "s_memrealtime s[94:95]\n\t" \
  "s_waitcnt lgkmcnt(1)\n\t" \
  "s_sub_i32 s92, s90, s93\n\t" \
  "s_cmp_ge_i32 s92, 0\n\t" \
  "s_cbranch_scc1 .Lep%=\n\t" \
  "s_memrealtime s[90:91]\n\t" \
  "s_waitcnt lgkmcnt(1)\n\t" \
  "s_sub_i32 s92, s94, s93\n\t" \
  "s_cmp_lt_i32 s92, 0\n\t" \
  "s_cbranch_scc1 .Lpacep%=\n\t" \
  "s_nop 4\n\t" \
  ".Lep%=:\n\t" \
  "s_add_u32 s92, s92, s93\n\t" \
  "s_lshl_b32 s92, s92, 2\n\t" \
  "s_sub_i32 s92, %[E], s92\n\t" \
  "s_max_i32 s92, s92, 0\n\t" \
  "s_bitcmp0_b32 s92, 3\n\t" \
  "s_cbranch_scc1 .Lf3p%=\n\t" \
  "s_nop 15\n\t" \
  ".Lf3p%=:\n\t" \
  "s_bitcmp0_b32 s92, 2\n\t" \
  "s_cbranch_scc1 .Lf2p%=\n\t" \
  "s_nop 10\n\t" \
  ".Lf2p%=:\n\t" \
  ".Lgop%=:\n\t" \
  "s_mov_b32 s92, 0\n\t" \
  "buffer_load_dwordx4 v[128:131], v234, %[rs], %[so] offen sc1\n\t" \
  "s_nop 15\n\t" \
  "buffer_load_dwordx4 v[132:135], v235, %[rs], %[so] offen sc1\n\t" \
  "s_nop 15\n\t" \
  "buffer_load_dwordx4 v[136:139], v236, %[rs], %[so] offen sc1\n\t" \
  "s_nop 15\n\t" \
  "buffer_load_dwordx4 v[140:143], v237, %[rs], %[so] offen sc1\n\t" \
  "s_waitcnt lgkmcnt(0)\n\t" \
  ".Lw%=:\n\t" \
  "s_waitcnt vmcnt(3)\n\t" \
  "v_smfmac_f32_16x16x64_bf16 v[144:147], v[128:131], v[0:7], v172\n\t" \
  "v_max3_u32 v164, v128, v129, 0\n\t" \
  "v_smfmac_f32_16x16x64_bf16 v[148:151], v[128:131], v[8:15], v172\n\t" \
  "v_max3_u32 v164, v130, v131, v164\n\t" \
  "v_smfmac_f32_16x16x64_bf16 v[152:155], v[128:131], v[16:23], v172\n\t" \
  "v_smfmac_f32_16x16x64_bf16 v[156:159], v[128:131], v[24:31], v172\n\t" \
  "buffer_load_dwordx4 v[176:179], v238, %[rt], %[st] offen offset:0 sc1\n\t" \
  "s_waitcnt vmcnt(3)\n\t" \
  "v_smfmac_f32_16x16x64_bf16 v[144:147], v[132:135], v[32:39], v172\n\t" \
  "v_max3_u32 v164, v132, v133, v164\n\t" \
  "v_smfmac_f32_16x16x64_bf16 v[148:151], v[132:135], v[40:47], v172\n\t" \
  "v_max3_u32 v164, v134, v135, v164\n\t" \
  "v_smfmac_f32_16x16x64_bf16 v[152:155], v[132:135], v[48:55], v172\n\t" \
  "v_smfmac_f32_16x16x64_bf16 v[156:159], v[132:135], v[56:63], v172\n\t" \
  "buffer_load_dwordx4 v[180:183], v238, %[rt], %[st] offen offset:1024 sc1\n\t" \
  "s_waitcnt vmcnt(3)\n\t" \
  "v_smfmac_f32_16x16x64_bf16 v[144:147], v[136:139], v[64:71], v172\n\t" \
  "v_max3_u32 v164, v136, v137, v164\n\t" \
  "v_smfmac_f32_16x16x64_bf16 v[148:151], v[136:139], v[72:79], v172\n\t" \
  "v_max3_u32 v164, v138, v139, v164\n\t" \
  "v_smfmac_f32_16x16x64_bf16 v[152:155], v[136:139], v[80:87], v172\n\t" \
  "v_smfmac_f32_16x16x64_bf16 v[156:159], v[136:139], v[88:95], v172\n\t" \
  "buffer_load_dwordx4 v[184:187], v238, %[rt], %[st4] offen offset:0 sc1\n\t" \
  "s_waitcnt vmcnt(3)\n\t" \
  "v_smfmac_f32_16x16x64_bf16 v[144:147], v[140:143], v[96:103], v172\n\t" \
  "v_max3_u32 v164, v140, v141, v164\n\t" \
  "v_smfmac_f32_16x16x64_bf16 v[148:151], v[140:143], v[104:111], v172\n\t" \
  "v_max3_u32 v164, v142, v143, v164\n\t" \
  "v_smfmac_f32_16x16x64_bf16 v[152:155], v[140:143], v[112:119], v172\n\t" \
  "v_cmp_le_u32 vcc, %[sent], v164\n\t" \
  "v_smfmac_f32_16x16x64_bf16 v[156:159], v[140:143], v[120:127], v172\n\t" \
  "buffer_load_dwordx4 v[188:191], v238, %[rt], %[st4] offen offset:1024 sc1\n\t" \
  "s_cbranch_vccnz .Lslow%=\n\t" \
  ".Lok%=:\n\t" \
  "v_add_f32_dpp v160, v144, v146 quad_perm:[0,1,2,3] row_mask:0x3 bank_mask:0xf\n\t" \
  "v_add_f32_dpp v161, v145, v147 quad_perm:[0,1,2,3] row_mask:0x3 bank_mask:0xf\n\t" \
  "v_add_f32_dpp v162, v148, v150 quad_perm:[0,1,2,3] row_mask:0x3 bank_mask:0xf\n\t" \
  "v_add_f32_dpp v163, v149, v151 quad_perm:[0,1,2,3] row_mask:0x3 bank_mask:0xf\n\t" \
  "v_add_f32_dpp v160, v152, v154 quad_perm:[0,1,2,3] row_mask:0xc bank_mask:0xf\n\t" \
  "v_add_f32_dpp v161, v153, v155 quad_perm:[0,1,2,3] row_mask:0xc bank_mask:0xf\n\t" \
  "v_add_f32_dpp v162, v156, v158 quad_perm:[0,1,2,3] row_mask:0xc bank_mask:0xf\n\t" \
  "v_add_f32_dpp v163, v157, v159 quad_perm:[0,1,2,3] row_mask:0xc bank_mask:0xf\n\t" \
  "s_nop 0\n\t" \
  "v_permlane32_swap_b32 v160, v162\n\t" \
  "v_permlane32_swap_b32 v161, v163\n\t" \
  "v_add_f32 v160, v160, v162\n\t" \
  "v_add_f32 v161, v161, v163\n\t" \
  "ds_write_b64 v169, v[160:161]\n\t" \
  "s_mov_b64 exec, 1\n\t" \
  "ds_inc_rtn_u32 v164, v168, v166 offset:4104\n\t" \
  "s_mov_b64 exec, -1\n\t" \
  "ds_read_b64 v[128:129], v170 offset:0\n\t" \
  "ds_read_b64 v[130:131], v170 offset:512\n\t" \
  "ds_read_b64 v[132:133], v170 offset:1024\n\t" \
  "ds_read_b64 v[134:135], v170 offset:1536\n\t" \
  "ds_read_b64 v[136:137], v170 offset:2048\n\t" \
  "ds_read_b64 v[138:139], v170 offset:2560\n\t" \
  "ds_read_b64 v[140:141], v170 offset:3072\n\t" \
  "ds_read_b64 v[142:143], v170 offset:3584\n\t" \
  "s_waitcnt lgkmcnt(8)\n\t" \
  "v_readfirstlane_b32 s93, v164\n\t" \
  "s_cmp_lg_u32 s93, 7\n\t" \
  "s_cbranch_scc1 .Lb1%=\n\t" \
  "s_waitcnt lgkmcnt(4)\n\t" \
  "v_pk_add_f32 v[128:129], v[128:129], v[130:131]\n\t" \
  "v_pk_add_f32 v[132:133], v[132:133], v[134:135]\n\t" \
  "s_waitcnt lgkmcnt(0)\n\t" \
  "v_pk_add_f32 v[136:137], v[136:137], v[138:139]\n\t" \
  "v_pk_add_f32 v[140:141], v[140:141], v[142:143]\n\t" \
  "v_pk_add_f32 v[128:129], v[128:129], v[132:133]\n\t" \
  "v_pk_add_f32 v[136:137], v[136:137], v[140:141]\n\t" \
  "v_pk_add_f32 v[128:129], v[128:129], v[136:137]\n\t" \
  "v_cvt_pk_bf16_f32 v128, v128, v129\n\t" \
  "s_nop 1\n\t" \
  "v_mov_b32_dpp v129, v128 quad_perm:[1,0,3,2] row_mask:0xf bank_mask:0xf\n\t" \
  "v_perm_b32 v128, v129, v128, v174\n\t" \
  "buffer_store_dword v128, v171, %[rs], %[sh] offen sc1\n\t" \
  "s_memtime s[90:91]\n\t" \
  "s_barrier\n\t" \
  "s_memtime s[76:77]\n\t" \
  "s_memrealtime s[78:79]\n\t" \
  "s_nop 3\n\t" \
  "s_memrealtime s[80:81]\n\t" \
  "s_nop 3\n\t" \
  "s_memrealtime s[82:83]\n\t" \
  "s_nop 3\n\t" \
  "s_memrealtime s[84:85]\n\t" \
  "s_nop 3\n\t" \
  "s_memrealtime s[86:87]\n\t" \
  "s_nop 3\n\t" \
  "s_memrealtime s[88:89]\n\t" \
  "s_mov_b32 s93, 0\n\t" \
  "s_waitcnt lgkmcnt(0)\n\t" \
  "s_cmp_eq_u32 s80, s78\n\t" \
  "s_addc_u32 s93, s93, 0\n\t" \
  "s_cmp_eq_u32 s82, s78\n\t" \
  "s_addc_u32 s93, s93, 0\n\t" \
  "s_cmp_eq_u32 s84, s78\n\t" \
  "s_addc_u32 s93, s93, 0\n\t" \
  "s_cmp_eq_u32 s86, s78\n\t" \
  "s_addc_u32 s93, s93, 0\n\t" \
  "s_cmp_eq_u32 s88, s78\n\t" \
  "s_addc_u32 s93, s93, 0\n\t" \
  "s_mul_i32 s93, s93, 21\n\t" \
  "s_add_u32 s76, s76, 15\n\t" \
  "s_add_u32 s76, s76, s93\n\t" \
  "s_lshl2_add_u32 s78, s78, 16\n\t" \
  "s_mov_b64 exec, 1\n\t" \
  "s_sub_i32 s90, s76, s90\n\t" \
  "s_mul_i32 s90, s90, 11900\n\t" \
  "s_ashr_i32 s90, s90, 16\n\t" \
  "s_sub_u32 s90, s78, s90\n\t" \
  "s_sub_u32 s90, s90, %[base]\n\t" \
  "v_mov_b32 v164, s90\n\t" \
  "ds_write_b32 v168, v164 offset:4108\n\t" \
  "s_mov_b64 exec, -1\n\t" \
  "s_waitcnt vmcnt(1)\n\t" \
  ".Lb2%=:\n\t" \
  "s_setprio 0\n\t" \
  "s_bitcmp0_b64 %[am], %[step]\n\t" \
  "s_cbranch_scc1 .La1%=\n\t" \
  "buffer_store_dwordx4 v[224:227], v228, %[ra], 0 offen sc1\n\t" \
  ".La1%=:\n\t" \
  "buffer_load_dwordx4 v[192:195], v239, %[rt], %[st] offen offset:0 sc1\n\t" \
  "buffer_load_dwordx4 v[196:199], v239, %[rt], %[st] offen offset:1024 sc1\n\t" \
  "v_mfma_f32_16x16x32_bf16 v[128:131], v[0:3], v[176:179], 0\n\t" \
  "v_mfma_f32_16x16x32_bf16 v[136:139], v[8:11], v[176:179], 0\n\t" \
  "ds_read2st64_b32 v[144:145], v231 offset0:0 offset1:8\n\t" \
  "ds_read2st64_b32 v[146:147], v231 offset0:16 offset1:24\n\t" \
  "ds_read2st64_b32 v[148:149], v231 offset0:32 offset1:40\n\t" \
  "ds_read2st64_b32 v[150:151], v231 offset0:48 offset1:56\n\t" \
  "buffer_load_dwordx4 v[200:203], v239, %[rt], %[st4] offen offset:0 sc1\n\t" \
  "buffer_load_dwordx4 v[204:207], v239, %[rt], %[st4] offen offset:1024 sc1\n\t" \
  "v_mfma_f32_16x16x32_bf16 v[132:135], v[4:7], v[180:183], 0\n\t" \
  "v_mfma_f32_16x16x32_bf16 v[140:143], v[12:15], v[180:183], 0\n\t" \
  "s_waitcnt lgkmcnt(0)\n\t" \
  "v_add_f32 v144, v144, v145\n\t" \
  "v_add_f32 v146, v146, v147\n\t" \
  "v_add_f32 v148, v148, v149\n\t" \
  "v_add_f32 v150, v150, v151\n\t" \
  "v_add_f32 v144, v144, v146\n\t" \
  "v_add_f32 v148, v148, v150\n\t" \
  "buffer_load_dwordx4 v[208:211], v240, %[rt], %[st] offen offset:0 sc1\n\t" \
  "buffer_load_dwordx4 v[212:215], v240, %[rt], %[st] offen offset:1024 sc1\n\t" \
  "v_mfma_f32_16x16x32_bf16 v[128:131], v[16:19], v[184:187], v[128:131]\n\t" \
  "v_mfma_f32_16x16x32_bf16 v[136:139], v[24:27], v[184:187], v[136:139]\n\t" \
  "v_add_f32 v144, v144, v148\n\t" \
  "v_cvt_pk_bf16_f32 v144, v144, v144\n\t" \
  "ds_write_b16 v232, v144\n\t" \
  "ds_read_b128 v[224:227], v229\n\t" \
  "v_add_u32 v228, %[ainc], v228\n\t" \
  "v_add_u32 v229, %[rinc], v229\n\t" \
  "v_add_u32 v232, 1024, v232\n\t" \
  "v_xor_b32 v231, 0x4000, v231\n\t" \
  "buffer_load_dwordx4 v[216:219], v240, %[rt], %[st4] offen offset:0 sc1\n\t" \
  "buffer_load_dwordx4 v[220:223], v240, %[rt], %[st4] offen offset:1024 sc1\n\t" \
  "v_mfma_f32_16x16x32_bf16 v[132:135], v[20:23], v[188:191], v[132:135]\n\t" \
  "v_mfma_f32_16x16x32_bf16 v[140:143], v[28:31], v[188:191], v[140:143]\n\t" \
  "buffer_load_dwordx4 v[176:179], v241, %[rt], %[st] offen offset:0 sc1\n\t" \
  "s_waitcnt vmcnt(8)\n\t" \
  "v_mfma_f32_16x16x32_bf16 v[128:131], v[32:35], v[192:195], v[128:131]\n\t" \
  "v_mfma_f32_16x16x32_bf16 v[136:139], v[40:43], v[192:195], v[136:139]\n\t" \
  "buffer_load_dwordx4 v[180:183], v241, %[rt], %[st] offen offset:1024 sc1\n\t" \
  "s_waitcnt vmcnt(8)\n\t" \
  "v_mfma_f32_16x16x32_bf16 v[132:135], v[36:39], v[196:199], v[132:135]\n\t" \
  "v_mfma_f32_16x16x32_bf16 v[140:143], v[44:47], v[196:199], v[140:143]\n\t" \
  "buffer_load_dwordx4 v[184:187], v241, %[rt], %[st4] offen offset:0 sc1\n\t" \
  "s_waitcnt vmcnt(8)\n\t" \
  "v_mfma_f32_16x16x32_bf16 v[128:131], v[48:51], v[200:203], v[128:131]\n\t" \
  "v_mfma_f32_16x16x32_bf16 v[136:139], v[56:59], v[200:203], v[136:139]\n\t" \
  "ds_read_b128 v[160:163], v168 offset:4096\n\t" \
  "buffer_load_dwordx4 v[188:191], v241, %[rt], %[st4] offen offset:1024 sc1\n\t" \
  "s_waitcnt vmcnt(8)\n\t" \
  "v_mfma_f32_16x16x32_bf16 v[132:135], v[52:55], v[204:207], v[132:135]\n\t" \
  "v_mfma_f32_16x16x32_bf16 v[140:143], v[60:63], v[204:207], v[140:143]\n\t" \
  "s_waitcnt lgkmcnt(0)\n\t" \
  "v_readfirstlane_b32 s90, v161\n\t" \
  "v_readfirstlane_b32 s91, v163\n\t" \
  "s_add_u32 s93, s90, %[rl]\n\t" \
  "s_add_u32 s93, s93, %[base]\n\t" \
  "s_add_u32 %[E], s93, %[lk]\n\t" \
  "s_sub_i32 s91, s91, s90\n\t" \
  "s_sub_i32 s91, s91, %[rl]\n\t" \
  "s_min_i32 s93, s91, %[up]\n\t" \
  "s_max_i32 s93, s93, %[dn]\n\t" \
  "s_cmp_gt_i32 s91, 60\n\t" \
  "s_cselect_b32 s93, 2, s93\n\t" \
  "s_add_i32 %[rl], %[rl], s93\n\t" \
  "v_mov_b32 v160, %[rl]\n\t" \
  "buffer_store_dword v160, v175, %[rs], 0 offen sc1\n\t" \
  "s_waitcnt vmcnt(8)\n\t" \
  "v_mfma_f32_16x16x32_bf16 v[128:131], v[64:67], v[208:211], v[128:131]\n\t" \
  "v_mfma_f32_16x16x32_bf16 v[136:139], v[72:75], v[208:211], v[136:139]\n\t" \
  "s_add_u32 %[step], %[step], 1\n\t" \
  "s_lshl_b32 %[sh], %[step], 15\n\t" \
  "s_lshr_b32 s93, %[step], 4\n\t" \
  "s_lshr_b32 s93, %[pm], s93\n\t" \
  "s_and_b32 s93, s93, 4\n\t" \
  "s_lshl_b32 s93, s93, 11\n\t" \
  "s_lshl2_add_u32 %[so], %[sh], s93\n\t" \
  "s_waitcnt vmcnt(7)\n\t" \
  "v_mfma_f32_16x16x32_bf16 v[132:135], v[68:71], v[212:215], v[132:135]\n\t" \
  "v_mfma_f32_16x16x32_bf16 v[140:143], v[76:79], v[212:215], v[140:143]\n\t" \
  "s_sub_i32 s93, %[step], 2\n\t" \
  "s_lshl_b32 %[st], s93, 19\n\t" \
  "s_lshl_b32 s94, s93, 11\n\t" \
  "s_xor_b32 s94, s94, %[s0]\n\t" \
  "s_and_b32 s94, s94, 0x2000\n\t" \
  "s_or_b32 %[st], %[st], s94\n\t" \
  "s_cmp_lt_i32 s93, 0\n\t" \
  "s_cselect_b32 %[st], 0x70000000, %[st]\n\t" \
  "s_add_u32 %[st4], %[st], 4096\n\t" \
  "s_waitcnt vmcnt(6)\n\t" \
  "v_mfma_f32_16x16x32_bf16 v[128:131], v[80:83], v[216:219], v[128:131]\n\t" \
  "v_mfma_f32_16x16x32_bf16 v[136:139], v[88:91], v[216:219], v[136:139]\n\t" \
  "s_waitcnt vmcnt(5)\n\t" \
  "v_mfma_f32_16x16x32_bf16 v[132:135], v[84:87], v[220:223], v[132:135]\n\t" \
  "v_mfma_f32_16x16x32_bf16 v[140:143], v[92:95], v[220:223], v[140:143]\n\t" \
  "s_cmp_eq_u32 %[step], 63\n\t" \
  "s_cbranch_scc1 .Llast%=\n\t" \
  "s_movk_i32 s93, 600\n\t" \
  ".Lpacel%=:\n\t" \
  "s_memrealtime s[90:91]\n\t" \
  "s_waitcnt lgkmcnt(0)\n\t" \
  "s_lshl_b32 s90, s90, 2\n\t" \
  "s_sub_i32 s91, %[E], s90\n\t" \
  "s_cmp_le_i32 s91, 0\n\t" \
  "s_cbranch_scc1 .Lgol%=\n\t" \
  "s_sub_u32 s93, s93, 1\n\t" \
  "s_cmp_lg_u32 s93, 0\n\t" \
  "s_cbranch_scc1 .Lpacel%=\n\t" \
  ".Lgol%=:\n\t" \
  "buffer_load_dwordx2 v[166:167], v173, %[rs], 0 offen sc1\n\t" \
  "s_waitcnt vmcnt(5)\n\t" \
  "v_mfma_f32_16x16x32_bf16 v[128:131], v[96:99], v[176:179], v[128:131]\n\t" \
  "v_mfma_f32_16x16x32_bf16 v[136:139], v[104:107], v[176:179], v[136:139]\n\t" \
  "s_waitcnt vmcnt(4)\n\t" \
  "v_mfma_f32_16x16x32_bf16 v[132:135], v[100:103], v[180:183], v[132:135]\n\t" \
  "v_mfma_f32_16x16x32_bf16 v[140:143], v[108:111], v[180:183], v[140:143]\n\t" \
  "s_waitcnt vmcnt(3)\n\t" \
  "v_mfma_f32_16x16x32_bf16 v[128:131], v[112:115], v[184:187], v[128:131]\n\t" \
  "v_mfma_f32_16x16x32_bf16 v[136:139], v[120:123], v[184:187], v[136:139]\n\t" \
  "s_waitcnt vmcnt(2)\n\t" \
  "v_mfma_f32_16x16x32_bf16 v[132:135], v[116:119], v[188:191], v[132:135]\n\t" \
  "v_mfma_f32_16x16x32_bf16 v[140:143], v[124:127], v[188:191], v[140:143]\n\t" \
  "v_mov_b64 v[144:145], 0\n\t" \
  "v_mov_b64 v[146:147], 0\n\t" \
  "v_mov_b64 v[148:149], 0\n\t" \
  "v_mov_b64 v[150:151], 0\n\t" \
  "v_mov_b64 v[152:153], 0\n\t" \
  "v_mov_b64 v[154:155], 0\n\t" \
  "v_mov_b64 v[156:157], 0\n\t" \
  "v_mov_b64 v[158:159], 0\n\t" \
  "v_pk_add_f32 v[128:129], v[128:129], v[132:133]\n\t" \
  "v_pk_add_f32 v[130:131], v[130:131], v[134:135]\n\t" \
  "ds_write_b128 v230, v[128:131] offset:0\n\t" \
  "v_pk_add_f32 v[136:137], v[136:137], v[140:141]\n\t" \
  "v_pk_add_f32 v[138:139], v[138:139], v[142:143]\n\t" \
  "ds_write_b128 v230, v[136:139] offset:1024\n\t" \
  "v_xor_b32 v230, 0x4000, v230\n\t" \
  "s_cmp_lg_u32 %[step], 63\n\t" \
  "s_cbranch_scc1 .Lstep%=\n\t" \
  ".Lfc%=:\n\t" \
  "s_waitcnt vmcnt(0)\n\t" \
  "v_cmp_eq_u32 vcc, -1, v166\n\t" \
  "s_cbranch_vccnz .Lfw%=\n\t" \
  ".Lfo%=:\n\t" \
  "v_add_u32 v242, 16384, v242\n\t" \
  "buffer_load_dwordx4 v[176:179], v238, %[rt], %[st] offen offset:0 sc1\n\t" \
  "buffer_load_dwordx4 v[180:183], v238, %[rt], %[st] offen offset:1024 sc1\n\t" \
  "buffer_load_dwordx4 v[184:187], v238, %[rt], %[st4] offen offset:0 sc1\n\t" \
  "buffer_load_dwordx4 v[188:191], v238, %[rt], %[st4] offen offset:1024 sc1\n\t" \
  "buffer_load_dwordx4 v[192:195], v239, %[rt], %[st] offen offset:0 sc1\n\t" \
  "buffer_load_dwordx4 v[196:199], v239, %[rt], %[st] offen offset:1024 sc1\n\t" \
  "buffer_load_dwordx4 v[200:203], v239, %[rt], %[st4] offen offset:0 sc1\n\t" \
  "buffer_load_dwordx4 v[204:207], v239, %[rt], %[st4] offen offset:1024 sc1\n\t" \
  "v_subrev_u32 v230, 98304, v230\n\t" \
  ".Lt%=:\n\t" \
  "s_waitcnt vmcnt(7)\n\t" \
  "v_mfma_f32_16x16x32_bf16 v[128:131], v[0:3], v[176:179], 0\n\t" \
  "v_mfma_f32_16x16x32_bf16 v[136:139], v[8:11], v[176:179], 0\n\t" \
  "buffer_load_dwordx4 v[176:179], v240, %[rt], %[st] offen offset:0 sc1\n\t" \
  "buffer_load_dword v166, v242, %[rs], 0 offen sc1\n\t" \
  "s_waitcnt vmcnt(8)\n\t" \
  "v_mfma_f32_16x16x32_bf16 v[132:135], v[4:7], v[180:183], 0\n\t" \
  "v_mfma_f32_16x16x32_bf16 v[140:143], v[12:15], v[180:183], 0\n\t" \
  "buffer_load_dwordx4 v[180:183], v240, %[rt], %[st] offen offset:1024 sc1\n\t" \
  "s_waitcnt vmcnt(8)\n\t" \
  "v_mfma_f32_16x16x32_bf16 v[128:131], v[16:19], v[184:187], v[128:131]\n\t" \
  "v_mfma_f32_16x16x32_bf16 v[136:139], v[24:27], v[184:187], v[136:139]\n\t" \
  "buffer_load_dwordx4 v[184:187], v240, %[rt], %[st4] offen offset:0 sc1\n\t" \
  "s_waitcnt vmcnt(8)\n\t" \
  "v_mfma_f32_16x16x32_bf16 v[132:135], v[20:23], v[188:191], v[132:135]\n\t" \
  "v_mfma_f32_16x16x32_bf16 v[140:143], v[28:31], v[188:191], v[140:143]\n\t" \
  "buffer_load_dwordx4 v[188:191], v240, %[rt], %[st4] offen offset:1024 sc1\n\t" \
  "s_waitcnt vmcnt(8)\n\t" \
  "v_mfma_f32_16x16x32_bf16 v[128:131], v[32:35], v[192:195], v[128:131]\n\t" \
  "v_mfma_f32_16x16x32_bf16 v[136:139], v[40:43], v[192:195], v[136:139]\n\t" \
  "buffer_load_dwordx4 v[192:195], v241, %[rt], %[st] offen offset:0 sc1\n\t" \
  "s_waitcnt vmcnt(8)\n\t" \
  "v_mfma_f32_16x16x32_bf16 v[132:135], v[36:39], v[196:199], v[132:135]\n\t" \
  "v_mfma_f32_16x16x32_bf16 v[140:143], v[44:47], v[196:199], v[140:143]\n\t" \
  "buffer_load_dwordx4 v[196:199], v241, %[rt], %[st] offen offset:1024 sc1\n\t" \
  "s_waitcnt vmcnt(8)\n\t" \
  "v_mfma_f32_16x16x32_bf16 v[128:131], v[48:51], v[200:203], v[128:131]\n\t" \
  "v_mfma_f32_16x16x32_bf16 v[136:139], v[56:59], v[200:203], v[136:139]\n\t" \
  "buffer_load_dwordx4 v[200:203], v241, %[rt], %[st4] offen offset:0 sc1\n\t" \
  "s_waitcnt vmcnt(8)\n\t" \
  "v_mfma_f32_16x16x32_bf16 v[132:135], v[52:55], v[204:207], v[132:135]\n\t" \
  "v_mfma_f32_16x16x32_bf16 v[140:143], v[60:63], v[204:207], v[140:143]\n\t" \
  "buffer_load_dwordx4 v[204:207], v241, %[rt], %[st4] offen offset:1024 sc1\n\t" \
  "s_waitcnt vmcnt(8)\n\t" \
  "v_mfma_f32_16x16x32_bf16 v[128:131], v[64:67], v[176:179], v[128:131]\n\t" \
  "v_mfma_f32_16x16x32_bf16 v[136:139], v[72:75], v[176:179], v[136:139]\n\t" \
  "s_waitcnt vmcnt(7)\n\t" \
  ".Ltc%=:\n\t" \
  "v_cmp_eq_u32 vcc, -1, v166\n\t" \
  "s_cbranch_vccnz .Ltw%=\n\t" \
  ".Lto%=:\n\t" \
  "s_cmp_eq_u32 %[step], 65\n\t" \
  "s_cbranch_scc1 .Ltl%=\n\t" \
  "s_add_u32 %[step], %[step], 1\n\t" \
  "s_sub_i32 s93, %[step], 2\n\t" \
  "s_lshl_b32 %[st], s93, 19\n\t" \
  "s_lshl_b32 s94, s93, 11\n\t" \
  "s_xor_b32 s94, s94, %[s0]\n\t" \
  "s_and_b32 s94, s94, 0x2000\n\t" \
  "s_or_b32 %[st], %[st], s94\n\t" \
  "s_cmp_lt_i32 s93, 0\n\t" \
  "s_cselect_b32 %[st], 0x70000000, %[st]\n\t" \
  "s_add_u32 %[st4], %[st], 4096\n\t" \
  "v_add_u32 v242, 16384, v242\n\t" \
  "s_cmp_eq_u32 %[step], 65\n\t" \
  "s_cbranch_scc0 .Ltn%=\n\t" \
  "v_mov_b32 v242, %[fg]\n\t" \
  ".Ltn%=:\n\t" \
  "buffer_load_dwordx4 v[176:179], v238, %[rt], %[st] offen offset:0 sc1\n\t" \
  "s_waitcnt vmcnt(7)\n\t" \
  "v_mfma_f32_16x16x32_bf16 v[132:135], v[68:71], v[180:183], v[132:135]\n\t" \
  "v_mfma_f32_16x16x32_bf16 v[140:143], v[76:79], v[180:183], v[140:143]\n\t" \
  "buffer_load_dwordx4 v[180:183], v238, %[rt], %[st] offen offset:1024 sc1\n\t" \
  "s_waitcnt vmcnt(7)\n\t" \
  "v_mfma_f32_16x16x32_bf16 v[128:131], v[80:83], v[184:187], v[128:131]\n\t" \
  "v_mfma_f32_16x16x32_bf16 v[136:139], v[88:91], v[184:187], v[136:139]\n\t" \
  "buffer_load_dwordx4 v[184:187], v238, %[rt], %[st4] offen offset:0 sc1\n\t" \
  "s_waitcnt vmcnt(7)\n\t" \
  "v_mfma_f32_16x16x32_bf16 v[132:135], v[84:87], v[188:191], v[132:135]\n\t" \
  "v_mfma_f32_16x16x32_bf16 v[140:143], v[92:95], v[188:191], v[140:143]\n\t" \
  "buffer_load_dwordx4 v[188:191], v238, %[rt], %[st4] offen offset:1024 sc1\n\t" \
  "s_waitcnt vmcnt(7)\n\t" \
  "v_mfma_f32_16x16x32_bf16 v[128:131], v[96:99], v[192:195], v[128:131]\n\t" \
  "v_mfma_f32_16x16x32_bf16 v[136:139], v[104:107], v[192:195], v[136:139]\n\t" \
  "buffer_load_dwordx4 v[192:195], v239, %[rt], %[st] offen offset:0 sc1\n\t" \
  "s_waitcnt vmcnt(7)\n\t" \
  "v_mfma_f32_16x16x32_bf16 v[132:135], v[100:103], v[196:199], v[132:135]\n\t" \
  "v_mfma_f32_16x16x32_bf16 v[140:143], v[108:111], v[196:199], v[140:143]\n\t" \
  "buffer_load_dwordx4 v[196:199], v239, %[rt], %[st] offen offset:1024 sc1\n\t" \
  "s_waitcnt vmcnt(7)\n\t" \
  "v_mfma_f32_16x16x32_bf16 v[128:131], v[112:115], v[200:203], v[128:131]\n\t" \
  "v_mfma_f32_16x16x32_bf16 v[136:139], v[120:123], v[200:203], v[136:139]\n\t" \
  "buffer_load_dwordx4 v[200:203], v239, %[rt], %[st4] offen offset:0 sc1\n\t" \
  "s_waitcnt vmcnt(7)\n\t" \
  "v_mfma_f32_16x16x32_bf16 v[132:135], v[116:119], v[204:207], v[132:135]\n\t" \
  "v_mfma_f32_16x16x32_bf16 v[140:143], v[124:127], v[204:207], v[140:143]\n\t" \
  "buffer_load_dwordx4 v[204:207], v239, %[rt], %[st4] offen offset:1024 sc1\n\t" \
  "s_nop 5\n\t" \
  "v_pk_add_f32 v[128:129], v[128:129], v[132:133]\n\t" \
  "v_pk_add_f32 v[130:131], v[130:131], v[134:135]\n\t" \
  "ds_write_b128 v230, v[128:131] offset:0\n\t" \
  "v_pk_add_f32 v[136:137], v[136:137], v[140:141]\n\t" \
  "v_pk_add_f32 v[138:139], v[138:139], v[142:143]\n\t" \
  "ds_write_b128 v230, v[136:139] offset:1024\n\t" \
  "v_add_u32 v230, 16384, v230\n\t" \
  "s_branch .Lt%=\n\t" \
  ".Ltl%=:\n\t" \
  "buffer_load_dwordx4 v[224:227], v243, %[rg], 0 offen sc1\n\t" \
  "s_waitcnt vmcnt(7)\n\t" \
  "v_mfma_f32_16x16x32_bf16 v[132:135], v[68:71], v[180:183], v[132:135]\n\t" \
  "v_mfma_f32_16x16x32_bf16 v[140:143], v[76:79], v[180:183], v[140:143]\n\t" \
  "s_waitcnt vmcnt(6)\n\t" \
  "v_mfma_f32_16x16x32_bf16 v[128:131], v[80:83], v[184:187], v[128:131]\n\t" \
  "v_mfma_f32_16x16x32_bf16 v[136:139], v[88:91], v[184:187], v[136:139]\n\t" \
  "s_waitcnt vmcnt(5)\n\t" \
  "v_mfma_f32_16x16x32_bf16 v[132:135], v[84:87], v[188:191], v[132:135]\n\t" \
  "v_mfma_f32_16x16x32_bf16 v[140:143], v[92:95], v[188:191], v[140:143]\n\t" \
  "s_waitcnt vmcnt(4)\n\t" \
  "v_mfma_f32_16x16x32_bf16 v[128:131], v[96:99], v[192:195], v[128:131]\n\t" \
  "v_mfma_f32_16x16x32_bf16 v[136:139], v[104:107], v[192:195], v[136:139]\n\t" \
  "s_waitcnt vmcnt(3)\n\t" \
  "v_mfma_f32_16x16x32_bf16 v[132:135], v[100:103], v[196:199], v[132:135]\n\t" \
  "v_mfma_f32_16x16x32_bf16 v[140:143], v[108:111], v[196:199], v[140:143]\n\t" \
  "s_waitcnt vmcnt(2)\n\t" \
  "v_mfma_f32_16x16x32_bf16 v[128:131], v[112:115], v[200:203], v[128:131]\n\t" \
  "v_mfma_f32_16x16x32_bf16 v[136:139], v[120:123], v[200:203], v[136:139]\n\t" \
  "s_waitcnt vmcnt(1)\n\t" \
  "v_mfma_f32_16x16x32_bf16 v[132:135], v[116:119], v[204:207], v[132:135]\n\t" \
  "v_mfma_f32_16x16x32_bf16 v[140:143], v[124:127], v[204:207], v[140:143]\n\t" \
  "s_nop 6\n\t" \
  "v_pk_add_f32 v[128:129], v[128:129], v[132:133]\n\t" \
  "v_pk_add_f32 v[130:131], v[130:131], v[134:135]\n\t" \
  "ds_write_b128 v230, v[128:131] offset:0\n\t" \
  "v_pk_add_f32 v[136:137], v[136:137], v[140:141]\n\t" \
  "v_pk_add_f32 v[138:139], v[138:139], v[142:143]\n\t" \
  "ds_write_b128 v230, v[136:139] offset:1024\n\t" \
  "s_waitcnt vmcnt(0) lgkmcnt(0)\n\t" \
  "s_branch .Lend%=\n\t" \
  ".Ltw%=:\n\t" \
  "s_add_u32 s92, s92, 1\n\t" \
  "s_sleep 2\n\t" \
  "s_and_b32 s93, s92, 63\n\t" \
  "s_cbranch_scc1 .Ltr%=\n\t" \
  "buffer_load_dword v164, off, %[rs], %[sab] sc1\n\t" \
  "s_waitcnt vmcnt(0)\n\t" \
  "v_readfirstlane_b32 s93, v164\n\t" \
  "s_nop 3\n\t" \
  "s_cmp_eq_u32 s93, 1\n\t" \
  "s_cbranch_scc1 .Lto%=\n\t" \
  "s_cmp_lt_u32 s92, 0x80000\n\t" \
  "s_cbranch_scc1 .Ltr%=\n\t" \
  "v_mov_b32 v164, 1\n\t" \
  "buffer_store_dword v164, off, %[rs], %[sab] sc1\n\t" \
  "s_waitcnt vmcnt(0)\n\t" \
  "s_branch .Lto%=\n\t" \
  ".Ltr%=:\n\t" \
  "buffer_load_dword v166, v242, %[rs], 0 offen sc1\n\t" \
  "s_waitcnt vmcnt(0)\n\t" \
  "s_branch .Ltc%=\n\t" \
  ".Llast%=:\n\t" \
  "v_mov_b32 v173, v242\n\t" \
  "s_branch .Lgol%=\n\t" \
  ".Lfw%=:\n\t" \
  "s_add_u32 s92, s92, 1\n\t" \
  "s_sleep 2\n\t" \
  "s_and_b32 s93, s92, 63\n\t" \
  "s_cbranch_scc1 .Lfr%=\n\t" \
  "buffer_load_dword v164, off, %[rs], %[sab] sc1\n\t" \
  "s_waitcnt vmcnt(0)\n\t" \
  "v_readfirstlane_b32 s93, v164\n\t" \
  "s_nop 3\n\t" \
  "s_cmp_eq_u32 s93, 1\n\t" \
  "s_cbranch_scc1 .Lfo%=\n\t" \
  "s_cmp_lt_u32 s92, 0x80000\n\t" \
  "s_cbranch_scc1 .Lfr%=\n\t" \
  "v_mov_b32 v164, 1\n\t" \
  "buffer_store_dword v164, off, %[rs], %[sab] sc1\n\t" \
  "s_waitcnt vmcnt(0)\n\t" \
  "s_branch .Lfo%=\n\t" \
  ".Lfr%=:\n\t" \
  "buffer_load_dwordx2 v[166:167], v173, %[rs], 0 offen sc1\n\t" \
  "s_branch .Lfc%=\n\t" \
  ".Lb1%=:\n\t" \
  "s_waitcnt vmcnt(0) lgkmcnt(0)\n\t" \
  "s_barrier\n\t" \
  "s_branch .Lb2%=\n\t" \
  ".Lbl%=:\n\t" \
  "s_bitset1_b64 %[ps], %[step]\n\t" \
  "s_cmp_lt_u32 s92, 3\n\t" \
  "s_cbranch_scc1 .Lbm%=\n\t" \
  "s_sleep 12\n\t" \
  ".Lbm%=:\n\t" \
  "s_add_u32 s92, s92, 1\n\t" \
  "s_sleep 2\n\t" \
  "s_and_b32 s93, s92, 63\n\t" \
  "s_cbranch_scc1 .Lba%=\n\t" \
  "buffer_load_dword v164, off, %[rs], %[sab] sc1\n\t" \
  "s_waitcnt vmcnt(0)\n\t" \
  "v_readfirstlane_b32 s93, v164\n\t" \
  "s_nop 3\n\t" \
  "s_cmp_eq_u32 s93, 1\n\t" \
  "s_cbranch_scc1 .Lgo%=\n\t" \
  "s_cmp_lt_u32 s92, 0x80000\n\t" \
  "s_cbranch_scc1 .Lba%=\n\t" \
  "v_mov_b32 v164, 1\n\t" \
  "buffer_store_dword v164, off, %[rs], %[sab] sc1\n\t" \
  "s_waitcnt vmcnt(0)\n\t" \
  "s_branch .Lgo%=\n\t" \
  ".Lba%=:\n\t" \
  "buffer_load_dwordx2 v[166:167], v173, %[rs], 0 offen sc1\n\t" \
  "s_branch .Lcal%=\n\t" \
  ".Lslow%=:\n\t" \
  "s_mov_b64 s[94:95], 0\n\t" \
  "v_cmp_eq_u32 vcc, %[sent], v128\n\t" \
  "s_or_b64 s[94:95], s[94:95], vcc\n\t" \
  "v_cmp_eq_u32 vcc, %[sent], v129\n\t" \
  "s_or_b64 s[94:95], s[94:95], vcc\n\t" \
  "v_cmp_eq_u32 vcc, %[sent], v130\n\t" \
  "s_or_b64 s[94:95], s[94:95], vcc\n\t" \
  "v_cmp_eq_u32 vcc, %[sent], v131\n\t" \
  "s_or_b64 s[94:95], s[94:95], vcc\n\t" \
  "v_cmp_eq_u32 vcc, %[sent], v132\n\t" \
  "s_or_b64 s[94:95], s[94:95], vcc\n\t" \
  "v_cmp_eq_u32 vcc, %[sent], v133\n\t" \
  "s_or_b64 s[94:95], s[94:95], vcc\n\t" \
  "v_cmp_eq_u32 vcc, %[sent], v134\n\t" \
  "s_or_b64 s[94:95], s[94:95], vcc\n\t" \
  "v_cmp_eq_u32 vcc, %[sent], v135\n\t" \
  "s_or_b64 s[94:95], s[94:95], vcc\n\t" \
  "v_cmp_eq_u32 vcc, %[sent], v136\n\t" \
  "s_or_b64 s[94:95], s[94:95], vcc\n\t" \
  "v_cmp_eq_u32 vcc, %[sent], v137\n\t" \
  "s_or_b64 s[94:95], s[94:95], vcc\n\t" \
  "v_cmp_eq_u32 vcc, %[sent], v138\n\t" \
  "s_or_b64 s[94:95], s[94:95], vcc\n\t" \
  "v_cmp_eq_u32 vcc, %[sent], v139\n\t" \
  "s_or_b64 s[94:95], s[94:95], vcc\n\t" \
  "v_cmp_eq_u32 vcc, %[sent], v140\n\t" \
  "s_or_b64 s[94:95], s[94:95], vcc\n\t" \
  "v_cmp_eq_u32 vcc, %[sent], v141\n\t" \
  "s_or_b64 s[94:95], s[94:95], vcc\n\t" \
  "v_cmp_eq_u32 vcc, %[sent], v142\n\t" \
  "s_or_b64 s[94:95], s[94:95], vcc\n\t" \
  "v_cmp_eq_u32 vcc, %[sent], v143\n\t" \
  "s_or_b64 s[94:95], s[94:95], vcc\n\t" \
  "s_cbranch_scc0 .Lok%=\n\t" \
  "s_bitset1_b64 %[fm], %[step]\n\t" \
  "s_add_u32 s92, s92, 1\n\t" \
  "s_sleep 1\n\t" \
  "s_and_b32 s93, s92, 63\n\t" \
  "s_cbranch_scc1 .Lre%=\n\t" \
  "buffer_load_dword v164, off, %[rs], %[sab] sc1\n\t" \
  "s_waitcnt vmcnt(0)\n\t" \
  "v_readfirstlane_b32 s93, v164\n\t" \
  "s_nop 3\n\t" \
  "s_cmp_eq_u32 s93, 1\n\t" \
  "s_cbranch_scc1 .Lok%=\n\t" \
  "s_cmp_lt_u32 s92, 0x80000\n\t" \
  "s_cbranch_scc1 .Lre%=\n\t" \
  "v_mov_b32 v164, 1\n\t" \
  "buffer_store_dword v164, off, %[rs], %[sab] sc1\n\t" \
  "s_waitcnt vmcnt(0)\n\t" \
  "s_branch .Lok%=\n\t" \
  ".Lre%=:\n\t" \
  "buffer_load_dwordx4 v[128:131], v234, %[rs], %[so] offen sc1\n\t" \
  "s_nop 15\n\t" \
  "buffer_load_dwordx4 v[132:135], v235, %[rs], %[so] offen sc1\n\t" \
  "s_nop 15\n\t" \
  "buffer_load_dwordx4 v[136:139], v236, %[rs], %[so] offen sc1\n\t" \
  "s_nop 15\n\t" \
  "buffer_load_dwordx4 v[140:143], v237, %[rs], %[so] offen sc1\n\t" \
  "v_mov_b64 v[144:145], 0\n\t" \
  "v_mov_b64 v[146:147], 0\n\t" \
  "v_mov_b64 v[148:149], 0\n\t" \
  "v_mov_b64 v[150:151], 0\n\t" \
  "v_mov_b64 v[152:153], 0\n\t" \
  "v_mov_b64 v[154:155], 0\n\t" \
  "v_mov_b64 v[156:157], 0\n\t" \
  "v_mov_b64 v[158:159], 0\n\t" \
  "s_waitcnt vmcnt(0)\n\t" \
  "s_branch .Lw%=\n\t" \
  ".Lend%=:\n\t"

__device__ __forceinline__ bf16x8 get4(const u32x32& W, int i) {
  return as_bf16x8((u32x4){W[4 * i], W[4 * i + 1], W[4 * i + 2], W[4 * i + 3]});
}
__device__ __forceinline__ int look_prog(__amdgpu_buffer_rsrc_t rs, int off) {
  asm volatile("" ::: "memory");
  const int v = (int)ld_flag(rs, off);
  return min(__builtin_amdgcn_readlane(v, 0), __builtin_amdgcn_readlane(v, 1));
}
__device__ __forceinline__ int wait_prog(__amdgpu_buffer_rsrc_t rs, int off, int need, gu32* abortw) {
  int v;
  for (unsigned spins = 0;;) {
    v = look_prog(rs, off);
    if (v >= need) break;
    __builtin_amdgcn_s_sleep(4);
    if (spin_fail(spins, abortw)) break;
  }
  asm volatile("" ::: "memory");
  return v;
}
#define CLB_S "s90", "s91", "s92", "s93", "s94", "s95", "vcc", "scc", "memory"
#define W_IN "{v[0:31]}"(W[0]), "{v[32:63]}"(W[1]), "{v[64:95]}"(W[2]), "{v[96:127]}"(W[3]), "{v[128:159]}"(W[4]), "{v[160:191]}"(W[5])

#define W_DELAY 0
#define LEAD0 395
#define LEAD_D0 6
#define OX4 8
#define QBJ 3
constexpr int L_MEM = 0, L_KT = 16384, L_QX = 24576, L_KX = 26624, L_VX = 16384, L_MISC = 30720, L_WL = 32768;

#define QK_STEP(TXT)                                                                                                        \
  asm volatile(TXT                                                                                                          \
      : [tout] "=&s"(tout), [spins] "=&s"(spins), [tb1] "=&s"(tb1), [tgt] "=&s"(tgt), "+{v248}"(aL16), "+{v249}"(aMW), "+{v250}"(aWL),                            \
        "+{v251}"(aIDX), "+{v252}"(aQW), "+{v[232:235]}"(QF)                                                                 \
      : W_IN, [rs] "s"(rs4), [som] "s"(o_mem(pm, s) + WV * 1024), [som2] "s"(o_mem(pm, s) + WV * 1024 + 16384), [sent] "s"(DSENT), [sab] "s"(SY_ABORT), \
        [lead] "s"(lead), [step] "s"(s), [sbc] "s"(SY_BA + side(pm, 1) + h * 128), [nh] "s"(0xFF00FF00FF00FF00ull), [up] "s"(0xFFFFFFFF00000000ull), [negc] "s"(-cexp),      \
        [c] "s"(cexp), [so] "s"(o_o(pm, s, h) + WV * 512), [sk] "s"(o_v(pm, s, h) + 2048 + (WV & 3) * 512),                    \
        [sv] "s"(o_v(pm, s, h) + WV * 512), [rt] "s"(rt4), [svt] "s"(t_off(p, T_V + kseg(s, h)) + WV * 2048)                  \
      : CLB8(192, 193, 194, 195, 196, 197, 198, 199), CLB8(200, 201, 202, 203, 204, 205, 206, 207),                          \
        CLB8(208, 209, 210, 211, 212, 213, 214, 215), CLB8(216, 217, 218, 219, 220, 221, 222, 223),                          \
        CLB8(224, 225, 226, 227, 228, 229, 230, 231), CLB8(236, 237, 238, 239, 240, 241, 242, 243), "v244", "v245", "v246", "v247", "v253", "s88", "s89", CLB_S)

template <int WV>
__device__ __forceinline__ void qk_wave(Args& p, int h, char* smem) {
  const int tid = threadIdx.x;
  unsigned pm = 0;
  int have = -1;
  u32x32 W[6];
  {
    int lane = tid & 63;
    asm volatile("" : "+v"(lane));
    const unsigned vo = (WV * 4 + (lane >> 4)) * (HD * 2) + 16 * ((lane & 15) ^ swzB((WV * 4 + (lane >> 4)) & 15));
    auto stage = [&](int j) {
#pragma unroll
      for (int mm = 0; mm < 2; ++mm) {
        const char* src = (const char*)((mm ? p.Wk : p.Wq) + (size_t)j * 128 * HD + h * DH);
        const unsigned dst = lds_off(smem) + (~j & 1) * 65536 + mm * 32768 + WV * 1024;
        glds2(src, 32 * HD * 2, vo, dst);
        if (TH_Q < 63) HOLD_V((h & 3) ^ 1, TH_Q);
        glds2(src + 64 * HD * 2, 32 * HD * 2, vo, dst + 16384);
        if (TH_Q < 63) HOLD_V((h & 3) ^ 1, TH_Q);
      }
    };
    bf16x8 keep[4][4];
    auto place = [&](int g) {
#pragma unroll
      for (int i = 0; i < 4; ++i)
        *reinterpret_cast<bf16x8*>(smem + (g == 0 ? 65536 : g == 1 ? 32768 : g == 2 ? 131072 : 98304) + WV * 4096 + i * 1024 + lane * 16) = keep[g][i];
    };
    stage(0); stage(1);
    Boot bt;
#pragma unroll
    for (int j = 0; j < 8; ++j) {
      if (j < 7) WAIT_V(8); else WAIT_V(0);
      if (WV == 7 && j == QBJ + 1) pm = boot_done(p.ws, bt);
      LBAR();
#pragma unroll
      for (int k = 0; k < 8; ++k) {
        const int P = (QK_MAP[WV] >> (4 * (2 * j + (k >> 2)))) & 15;
        const bf16x8 fr = ld_bfrag(smem + (~j & 1) * 65536 + (WV >> 2) * 32768 + (k >> 2) * 16384, k & 1, 2 * (WV & 3) + ((k >> 1) & 1), lane);
        const int f = P * 4 + (k & 3);
        if (P < 12) put4(W[f >> 3], f & 7, fr);
        else keep[P - 12][k & 3] = fr;
      }
      if (j == (WV >> 1)) place(2);
      LBAR();
      if (WV == 7 && j == QBJ) boot_ask(p.ws, bt);
      if (j + 2 < 8) stage(j + 2);
      if (j == 6) { place(0); place(3); }
    }
    place(1);
    if (WV == 7 && lane < 8) lds_at(smem + L_MISC)[lane] = lane == 7 ? pm : (lane == 1 ? now_q() : 0);
    if (WV == 6) lds_at(smem + 28672)[lane] = ~0u;
    LBAR();
    pm = uni(lds_at(smem + L_MISC)[7]);
    p.s0 = pm >> 31;
    if (WV == 6) RDW(h);
    if (WV == 4 || WV == 5) {
      have = wait_prog(mkrsrc(p.ws + WS_SY, SY_TOTAL), fkq(h, (lane & 1) * 2), 0, (gu32*)(p.ws + WS_SY + SY_ABORT));
      const char* src = p.ws + WS_T + t_off(p, kseg(0, h)) + (WV - 4) * 4096 + lane * 16;
#pragma unroll
      for (int i = 0; i < 4; ++i) glds16_sc1(src + i * 1024, smem + L_KT + (WV - 4) * 4096 + i * 1024);
    }
  }
  const __amdgpu_buffer_rsrc_t rsy = mkrsrc(p.ws + WS_SY, SY_TOTAL);
  const u32x4 rs4 = raw_rsrc(p.ws + WS_SY, SY_TOTAL);
  const u32x4 rt4 = raw_rsrc(p.ws + WS_T, T_BYTES);
  gu32* abortw = (gu32*)(p.ws + WS_SY + SY_ABORT);
  const float cexp = 0.08838834764831845f * 1.4426950408889634f;
  constexpr int m = WV >> 2, b = WV & 3;
  int lead = LEAD0;
  unsigned tb1 = 0;
  unsigned long long fm = 0, ps = 0; (void)fm; (void)ps;
  for (int s = 0; s < NSEG; ++s) {
    int lane = tid & 63;
    asm volatile("" : "+v"(lane));
    const int r = lane & 15, q = lane >> 4;
    unsigned aL16 = lane * 16, aMW = aL16 + WV * 1024, aWL = aL16 + 98304 + WV * 4096;
    unsigned aIDX = (lane & 2) ? 0xEEEE : 0x4444;
    unsigned aQW = (32 * b + r) * 16 + q * 4 + (m ? L_KX : L_QX);
    u32x4 QF;
    asm volatile("" : "={v[232:235]}"(QF));
    if (WV < 4) {
      QF[0] = L_QX + (q * 8 + (r >> 2)) * 16 + (r & 1) * 8;
      QF[1] = ((2 * (q & 1) + (q >> 1)) * 16 + r) * 4;
      QF[2] = ((r >> 2) & 1) * 65536 + ((r >> 1) & 1) * 64 + (r >> 3) * 32 + (r & 1) * 16 + (q >> 1) * 256 + (q & 1) * OX4;
    } else QF[3] = r * 16 + q * 4;
    unsigned tout, spins, tgt;
    if (WV == 0) QK_STEP(QK_ASM_0); else if (WV == 1) QK_STEP(QK_ASM_1); else if (WV == 2) QK_STEP(QK_ASM_2);
    else if (WV == 3) QK_STEP(QK_ASM_3); else if (WV == 4) QK_STEP(QK_ASM_4); else if (WV == 5) QK_STEP(QK_ASM_5);
    else if (WV == 6) QK_STEP(QK_ASM_6); else QK_STEP(QK_ASM_7);
    tout = __builtin_amdgcn_readfirstlane(tout);
    tb1 = __builtin_amdgcn_readfirstlane(tb1);
    lane = tid & 63;
    asm volatile("" : "+v"(lane));
    if (WV < 4) {
      lds_at(smem + L_MISC + 8)[WV] = s + 1;
      if (WV == 3) lds_at(smem + L_MISC)[6] = time_of(tout);
    }
    if (WV == 6) {
      if (s + 1 < NSEG) watch(rsy, s, lane, now_q() + W_DELAY, (const unsigned*)(smem + L_MISC), abortw, side(pm, 1));
    } else if (WV >= 4 && s + 1 < NSEG) {
      unsigned t;
      do {
        __builtin_amdgcn_s_sleep(1);
        t = lds_at(smem + L_MISC + 8)[lane & 3];
      } while (!__all(t == (unsigned)(s + 1)));
      if (WV <= 5) {
        if (s + 2 < NSEG) {
          if (have < s + 1) have = wait_prog(rsy, fkq(h, (lane & 1) * 2), s + 1, abortw);
          const char* src = p.ws + WS_T + t_off(p, kseg(s + 1, h)) + (WV - 4) * 4096 + lane * 16;
#pragma unroll
          for (int i = 0; i < 4; ++i) glds16_sc1(src + i * 1024, smem + L_KT + (WV - 4) * 4096 + i * 1024);
          if (have < s + 2) have = look_prog(rsy, fkq(h, (lane & 1) * 2));
        }
      } else {
        __builtin_amdgcn_s_sleep(6);
        const int x = (int)(__builtin_amdgcn_readfirstlane(lds_at(smem + L_MISC)[6]) - tb1);
        lead = __builtin_amdgcn_readfirstlane(track(lead, x, LEAD_UP, s < 8 ? LEAD_D0 : LEAD_DN, 60));
        SMP(2, s, x); SMP(3, s, lead);
      }
    }
  }
}

__device__ __forceinline__ void qk_role(Args& p, int h, char* smem) {
  switch (__builtin_amdgcn_readfirstlane(threadIdx.x >> 6)) {
    case 0: qk_wave<0>(p, h, smem); break;
    case 1: qk_wave<1>(p, h, smem); break;
    case 2: qk_wave<2>(p, h, smem); break;
    case 3: qk_wave<3>(p, h, smem); break;
    case 4: qk_wave<4>(p, h, smem); break;
    case 5: qk_wave<5>(p, h, smem); break;
    case 6: qk_wave<6>(p, h, smem); break;
    default: qk_wave<7>(p, h, smem); break;
  }
}

#define K_DUE 160
#define T_SL 2
#define O_DUE 400
#define LEAD_V 0

constexpr int V_VM = 16384, V_T = 32768, V_KI = 131072;
__device__ __forceinline__ void vu_role(Args& p, int h, char* smem) {
  const int tid = threadIdx.x, lane = tid & 63;
  const int w = __builtin_amdgcn_readfirstlane(tid >> 6);
  u32x32 W[4];
  {
    const unsigned vo = (tid >> 4) * (HD * 2) + 16 * ((lane & 15) ^ swzB((tid >> 4) & 15));
    auto stage = [&](int j) {
      const char* src = (const char*)(p.Wv + (size_t)j * 128 * HD + h * DH);
      const unsigned dst = lds_off(smem) + L_WL + (j & 3) * 32768 + w * 1024;
      glds2(src, 32 * HD * 2, vo, dst);
      if (TH_V < 63) HOLD_V((h & 3) ^ 1, TH_V);
      glds2(src + 64 * HD * 2, 32 * HD * 2, vo, dst + 16384);
      if (TH_V < 63) HOLD_V((h & 3) ^ 1, TH_V);
    };
    start_sy(p.ws, p.mem0, h, smem);
#pragma unroll
    for (int j = 0; j < 4; ++j) stage(j);
    Boot bt;
#pragma unroll
    for (int j = 0; j < 8; ++j) {
      if (j < 5) WAIT_V(12); else if (j == 5) WAIT_V(8); else if (j == 6) WAIT_V(4); else WAIT_V(0);
      if (j == 7 && w == 7) {
        const unsigned m = boot_done(p.ws, bt);
        if (lane < 8) lds_at(smem + L_MISC)[lane] = lane == 7 ? m : (lane == 1 ? now_q() : 0);
      }
      LBAR();
#pragma unroll
      for (int k = 0; k < 4; ++k) put4(W[j >> 1], (j & 1) * 4 + k, ld_bfrag(smem + L_WL + (j & 3) * 32768 + (k >> 1) * 16384, k & 1, w, lane));
      LBAR();
      if (j + 4 < 8) stage(j + 4);
      if (j == 5 && w == 7) boot_ask(p.ws, bt);
    }
    RDY(32 + h);
  }
  const __amdgpu_buffer_rsrc_t rsy = mkrsrc(p.ws + WS_SY, SY_TOTAL);
  const u32x4 rs4 = raw_rsrc(p.ws + WS_SY, SY_TOTAL);
  gu32* abortw = (gu32*)(p.ws + WS_SY + SY_ABORT);
  const unsigned pm = uni(lds_at(smem + L_MISC)[7]);
  p.s0 = pm >> 31;
  unsigned long long fm = 0; (void)fm;
  int have = -1;
  auto fetch = [&](int sg) {
    if (have < sg) have = wait_prog(rsy, fkq(h, w - 4), sg, abortw);
    const char* src = p.ws + WS_T + t_off(p, (w - 4) * T_Q + kseg(sg, h)) + lane * 16;
#pragma unroll
    for (int i = 0; i < 8; ++i) glds16_sc1(src + i * 1024, smem + V_T + (sg & 3) * 24576 + (w - 4) * 8192 + i * 1024);
  };
  const int fo = SY_FO + ((w & 3) * NH + h) * 128;
  typedef __attribute__((ext_vector_type(8))) float f32x8;
  f32x8 bias;
  u32x4 ta;
  {
    const int r = lane & 15, q = lane >> 4;
#pragma unroll
    for (int j = 0; j < 8; ++j) bias[j] = (j >> 2) * 16 + 4 * q + (j & 3) <= 16 * (w & 1) + r ? INFINITY : -INFINITY;
    ta[0] = V_KI + w * 2048 + lane * 16;
    ta[1] = V_KI + w * 2048 + (q * 8 + (r >> 2)) * 16 + (r & 1) * 8;
    ta[2] = V_VM + (w & 2) * 512 + ((2 * (q & 1) + (q >> 1)) * 16 + r) * 4;
    ta[3] = 0;
  }
#pragma unroll 1
  for (int s = 0; s < NSEG; ++s) {
    unsigned spins, tgt;
    asm volatile(VV_ASM
        : [spins] "=&s"(spins), [tgt] "=&s"(tgt)
        : W4_IN, "{v200}"(lane * 16), "{v201}"(lane * 16 + w * 2048), "{v202}"((lane & 2) ? 0xEEEE : 0x4444), "{v203}"(lane * 4),
          "{v204}"(tid * 4 + V_VM + (s & 1) * 2048), [rs] "s"(rs4), [som] "s"(o_mem(pm, s) + (w >> 2) * 16384 + (w & 3) * 2048), [step] "s"(s),
          [sent] "s"(DSENT), [sab] "s"(SY_ABORT), [sv] "s"(o_v(pm, s, h) + w * 256)
        : CLB32(128), CLB32(160), CLB8(192, 193, 194, 195, 196, 197, 198, 199), CLB_S);
    if (w < 4 || s == NSEG - 1) {
      const int sg = s - (w < 4);
      if (w >= 4) pace_until(now_q() + K_DUE);
      if (sg >= 0) {
        asm volatile(TK_ASM
            :
            : "{v200}"(lane * 16), "{v[208:215]}"(bias), "{v[216:219]}"(ta),
              [rs] "s"(rs4), [rt] "s"(raw_rsrc(p.ws + WS_T, T_BYTES)), [sent] "s"(DSENT), [sab] "s"(SY_ABORT), [sk] "s"(o_v(pm, sg, h) + 2048),
              [tl] "s"(V_T + (sg & 3) * 24576), [vp] "s"((sg & 1) * 2048), [tq] "s"((w & 1) * 4096), [tv] "s"((w & 2) * 2048),
              [so] "s"(t_off_o(p, T_O + (sg * 2 + (w & 1)) * 16 + (h >> 1)) + (h & 1) * 4096 + (w & 2) * 1024),
              [up] "s"(0xFFFFFFFF00000000ull), [c] "s"(1.4426950408889634f), [negc] "s"(-1.4426950408889634f),
              [sc] "s"((unsigned long long)__float_as_uint(0.08838834764831845f)), [od] "s"(s < NSEG - 1 ? O_DUE : 0)
            : CLB32(128), CLB32(160), CLB8(192, 193, 194, 195, 196, 197, 198, 199), CLB8(224, 225, 226, 227, 228, 229, 230, 231),
              "v205", "v206", "v207", "s88", "s89", CLB_S);
        if (sg >= NSEG - 3) {
          static_assert(TLAG == 2, "");
          WAIT_V(0);
          if (lane == 0) __builtin_amdgcn_raw_buffer_store_b32(1u, rsy, fo + (sg - NSEG + 3) * 4 * NH * 128, 0, 16);
        }
      }
    } else if (w == 7) {
      SMP(6, s, (int)(now_q() - tgt));
      watch(rsy, s, lane, now_q() + LEAD_V + W_DELAY, (const unsigned*)(smem + L_MISC), abortw, side(pm, 1));
    } else if (w >= 4) {
      __builtin_amdgcn_s_sleep(T_SL);
      const int s1 = w == 5 ? (s == NSEG - 2 ? s + 1 : s) : s + 1;
#pragma unroll 1
      for (int sg = (w == 5 || s == 0) ? s : s + 1; sg <= s1; ++sg) fetch(sg);
      if (have < s1 + 1 && s1 + 1 < NSEG) have = look_prog(rsy, fkq(h, w - 4));
      if (w == 5 && s + NPRE < NSEG) {
#pragma unroll
        for (int i = 0; i < 4; ++i) st_sc1(rsy, o_v(pm, s + NPRE, h) + i * 1024 + lane * 16, (u32x4){DSENT, DSENT, DSENT, DSENT});
      }
    }
  }
  if (h == 3 && (w & 2) == 0) { LST(351 + (w & 1) + (w >> 2) * 2); }
}

#define RL_UP 6
#define RL_DN 4
#define RED_MIN 40
#define BA_LOOK 360
constexpr int R_SI = 32768, R_PART = 98304;

__device__ __forceinline__ void red_role(Args& p, int j, char* smem) {
  const int tid = threadIdx.x;
  int lane = tid & 63;
  const int w = __builtin_amdgcn_readfirstlane(tid >> 6);
  const int c = (j & 2) * 8 + (j >> 2), rh = j & 1;
  const int hg = (w + (j >> 2)) & 7, rot = (j >> 2) + (j >> 5);
  const __amdgpu_buffer_rsrc_t rsy = mkrsrc(p.ws + WS_SY, SY_TOTAL);
  const u32x4 rs4 = raw_rsrc(p.ws + WS_SY, SY_TOTAL);
  const __amdgpu_buffer_rsrc_t rt = mkrsrc(p.ws + WS_T, T_BYTES);
  gu32* abortw = (gu32*)(p.ws + WS_SY + SY_ABORT);
  u32x32 W[4];
  u32x2 tag = {0u, 0u};
  {
    const u16* Wo = p.Wo + (size_t)(4 * hg * DH) * DM + 32 * c;
    const int ro = (lane >> 2) * 64 + (lane & 3) * 8;
    Boot bt;
    load_blocks<16>(smem + 32768 + w * 16384,
        [&](int i, char* slot) {
          if (i == 12 && w == 0) boot_ask(p.ws, bt);
          if ((i < 2 || i == 4 || i == 8) && (c & 14) != 6 && lane < 10) {
            const int x = i < 2 ? 3 - i : i >> 3, n = min(((c & 15) - ((c & 8) >> 2)) * 10 + lane, 127);
            touch_line((const char*)p.Wo + (size_t)(w * 512 + (x >> 1) * 256 + (x & 1) * 64 + (n & 64) + n) * (DM * 2) + (c & 16) * 64 + 384, smem + w * 256);
          }
          const int x = ((i >> 2) + rot) & 3;
          blk_dma(Wo + (size_t)((x >> 1) * 256 + (x & 1) * 64 + ((i >> 1) & 1) * 128 + (i & 1) * 32) * DM, DM, slot, lane);
          if (TH_R < 63) HOLD_V((c & 14) ^ 6, TH_R);
        },
        [&](int i, char* slot) {
#pragma unroll
          for (int t = 0; t < 2; ++t) {
            s16x4 lo = __builtin_amdgcn_ds_read_tr16_b64_v4i16((lds_s16x4_ptr)(slot + ro + t * 32));
            s16x4 hi = __builtin_amdgcn_ds_read_tr16_b64_v4i16((lds_s16x4_ptr)(slot + ro + t * 32 + 1024));
            put4(W[i >> 2], (i & 2) * 2 + 2 * t + (i & 1), (bf16x8){lo[0], lo[1], lo[2], lo[3], hi[0], hi[1], hi[2], hi[3]});
          }
        });
    if (w == 0) { lds_at(smem + 4128)[0] = boot_done(p.ws, bt); tag = next_tag(bt.e); }
    LBAR();
  }
  const unsigned pm = uni(lds_at(smem + 4128)[0]);
  p.s0 = pm >> 31;
  const int hs = side(pm, 1);
  if (((j >> 2) & 14) != 6 && GPF == 2) {
#pragma unroll
    for (int i = 0; i < 4; ++i)
      touch_line((const char*)(i < 2 ? p.wg : p.wu) + (size_t)(j - (j >= 32 ? 8 : 0)) * 131072 + ((i & 1) * 8 + w) * 8192 + lane * 128, smem + w * 256);
  }
  {
    const u32x4 ds = {DSENT, DSENT, DSENT, DSENT};
#pragma unroll
    for (int i = 0; i < 2; ++i) {
      const int s = (w * 2 + i) * 4 + (lane >> 4);
      if (s >= 2) st_sc1(rsy, o_mem(pm, s) + (c >> 4) * 16384 + (c & 15) * 512 + rh * 128 + ((lane >> 3) & 1) * 256 + (lane & 7) * 16, ds);
    }
  }
  if (tid < 4) { reinterpret_cast<unsigned*>(smem + 4096)[tid] = tid == 3 ? now_q() : 0; reinterpret_cast<unsigned*>(smem + 4112)[tid] = DSENT; }
  if (tid < 256) reinterpret_cast<int*>(smem + 5120)[tid] = ((tid >> 2) - TLAG - 6) >> 2;
  LBAR();
  const unsigned base = __builtin_amdgcn_readfirstlane(lds_at(smem + 4096)[3]);
  RDY(64 + j);
  unsigned long long fm = 0, ps = 0;
  const unsigned aL = lane * 16;
  const int e = ((56 + w) * SEG + rh * 16 + (lane >> 2)) * DM + c * 32 + (lane & 3) * 8;
  u32x4 xv = *reinterpret_cast<const u32x4*>(p.x + e), av = {DSENT, DSENT, DSENT, DSENT};
  asm volatile("" : "+v"(xv));
  {
    const int r = lane & 15, q = lane >> 4;
    const int sm0 = o_mem(pm, 0) + (c >> 4) * 16384 + (c & 15) * 512 + rh * 128;
    u32x4 VO, VT;
    unsigned fo = SY_FO + ((rh + (lane & 4) / 2) * NH + 4 * hg + (lane & 3)) * 128;
    unsigned ao = w == 1 ? (c * S_LEN + rh * 16 - (TLAG + 3) * SEG) * 64 + lane * 16
                         : (w >= 4 ? SY_O + NPRE * 131072 + side(pm, w - 2) + (j >> 3) * 16384 + (j & 7) * 1024 + lane * 16
                                   : SY_FS + (2 * c + rh) * 128 + (lane < 8 ? lane * 16 : 0x70000000));
    unsigned ar = w == 1 ? R_SI - (TLAG + 2) * 1024 + lane * 16 : (w == 2 ? 5120 : 4112);
    const unsigned long long am = w == 1 ? ((1ull << 56) - 1) << (TLAG + 3) : (w == 2 ? 0x11111111111111ull << (TLAG + 7) : (w >= 4 ? (0xFFFFull << (16 * (w - 4))) >> NPRE : 0));
    unsigned pw = R_PART + w * 2048 + lane * 16, pr = R_PART + 16384 + tid * 4;
    unsigned sw = R_SI - (TLAG + 1) * 1024 + (lane >> 2) * 64 + w * 8 + (lane & 3) * 2, stm = 8192 + w * 512;
#pragma unroll
    for (int l = 0; l < 4; ++l) {
      const int x = (l + rot) & 3;
      VO[l] = o_o(0, 0, 4 * hg) + rh * 65536 + x * 1024 + aL;
      VT[l] = (T_O + rh * 16 + 2 * hg + (x >> 1)) * 16384 + (x & 1) * 2048 + aL;
    }
    unsigned ba = lane < 4 ? SY_BA + hs + (4 * hg + lane) * 128 : 0x7F000000u;
    int rl = RL0;
    unsigned step, E, so, st, st4, sh;
    asm volatile(RED_ASM
        : [step] "=&s"(step), [E] "=&s"(E), [so] "=&s"(so), [st] "=&s"(st), [st4] "=&s"(st4), [sh] "=&s"(sh),
          [rl] "+s"(rl), [fm] "+s"(fm), [ps] "+s"(ps),
          "+{v[224:227]}"(av), "+{v228}"(ao), "+{v229}"(ar), "+{v230}"(pw), "+{v231}"(pr), "+{v232}"(sw), "+{v233}"(stm), "+{v242}"(fo), "+{v173}"(ba)
        : W4_IN, "{v168}"(aL), "{v169}"(w * 512 + lane * 8), "{v170}"(lane * 8), "{v174}"((r & 1) ? 0x03020706u : 0x05040100u),
          "{v171}"(sm0 + 32768 + (q >> 1) * 256 + (q & 1) * 64 + ((r >> 1) & 1) * 32 + (r & 1) * 16 + (r >> 2) * 4),
          "{v172}"((lane & 2) ? 0xEEEE : 0x4444),
          "{v175}"(w == 3 && lane < 32 ? SY_BR + hs + j * 128 + lane * 4 : 0x7F000000u), "{v[234:237]}"(VO), "{v[238:241]}"(VT), "{v243}"(e * 2),
          [rg] "s"(raw_rsrc(p.ws + WS_G, S_LEN * DM * 2)), [fg] "s"(SY_FG + ((14 + (w >> 2)) * 8 + (c >> 2)) * 128),
          [rs] "s"(rs4), [ra] "s"(w == 1 ? raw_rsrc(p.ws + WS_S, S_LEN * DM * 2) : rs4), [rt] "s"(raw_rsrc(p.ws + WS_T, T_BYTES)),
          [ds] "n"(DS_RED), [mn] "n"(RED_MIN), [lk] "n"(DS_MEM + BA_LOOK), [up] "n"(RL_UP), [dn] "n"(-RL_DN), [sent] "s"(DSENT), [sab] "s"(SY_ABORT),
          [base] "s"(base), [pm] "s"(pm), [s0] "s"((p.s0 ^ TF_O) << 13), [am] "s"(am), [ainc] "s"(w == 1 ? SEG * 64 : (w >= 4 ? 131072 : 0)),
          [rinc] "s"(w == 1 ? 1024 : (w == 2 ? 16 : 0))
        : CLB32(128), CLB8(160, 161, 162, 163, 164, 165, 166, 167), CLB8(176, 177, 178, 179, 180, 181, 182, 183),
          CLB8(184, 185, 186, 187, 188, 189, 190, 191), CLB8(192, 193, 194, 195, 196, 197, 198, 199), CLB8(200, 201, 202, 203, 204, 205, 206, 207),
          CLB8(208, 209, 210, 211, 212, 213, 214, 215), CLB8(216, 217, 218, 219, 220, 221, 222, 223), CLB_E, "s96", CLB_S);
  }
  if (j == 0 && w == 5) { LST(345); }
  LBAR();
  if (j == 0 && w == 5) { LST(346); }
  u32x4 sv;
  if (w < 6 - TLAG) sv = *reinterpret_cast<const u32x4*>(smem + R_SI + (56 + w) * 1024 + lane * 16);
  else {
    const f32x4* part = reinterpret_cast<const f32x4*>(smem + (w == 6 - TLAG ? R_PART : (w - 6 + TLAG) * 16384)) +
                        (lane & 2) * 32 + (lane & 1) * 32 + (lane >> 2);
    f32x4 x[8][2];
#pragma unroll
    for (int ww = 0; ww < 8; ++ww) { x[ww][0] = part[ww * 128]; x[ww][1] = part[ww * 128 + 16]; }
#pragma unroll
    for (int u = 0; u < 2; ++u) {
      const f32x4 a = ((x[0][u] + x[1][u]) + (x[2][u] + x[3][u])) + ((x[4][u] + x[5][u]) + (x[6][u] + x[7][u]));
      sv[2 * u] = cvtpk(a[0], a[1]); sv[2 * u + 1] = cvtpk(a[2], a[3]);
    }
  }
  st_out(p, e, combine(sv, av, xv));
  if (j == 0 && tid == 0) st2_sc1(rsy, SY_EP, tag);
  if (tid == 128) { LST(400 + j); }
}

__global__ __launch_bounds__(512, 2) void main_kernel(Args p) {
  char* smem = g_smem;
  p.ws = ws_align(p.ws);
  const int bx = blockIdx.x & 7, bk = blockIdx.x >> 3;
  const bool isg = (bx ^ XPROBE) & 4;
  const int b = (isg ? NCHAIN : 0) + bk * 4 + (bx & 3);
  if (b == 64 && threadIdx.x == 0) { LST(500); }
  if (threadIdx.x < 64) {
    unsigned long long pc; unsigned len, keep;
    asm volatile("s_getpc_b64 %0\n.Lpc%=:\n\ts_mov_b32 %1, .Lcode_end-.Lpc%=" : "=s"(pc), "=s"(len));
    asm volatile("s_mov_b32 %0, m0\n\ts_mov_b32 m0, %3\n\ts_nop 0\n\tbuffer_load_dword %1, %2, 0 offen lds\n\ts_mov_b32 m0, %0"
                 : "=&s"(keep) : "v"((bk * 40 + threadIdx.x) * 128), "s"(raw_rsrc((const void*)pc, len)), "s"((b >> 6) == 1 ? 2048 : 163328) : "memory");
  }
  if (b >= NCHAIN) gemm_role(p, b - NCHAIN, smem);
  else if (b < 32) qk_role(p, (b & 3) * 8 + bk, smem);
  else if (b < 64) vu_role(p, (b & 3) * 8 + bk - 8, smem);
  else red_role(p, b - 64, smem);
}

extern "C" void kernel_launch(void* const* d_in, const int* in_sizes, int n_in,
                              void* d_out, int out_size, void* d_ws, size_t ws_size,
                              hipStream_t stream) {
  Args a;
  a.x = (const u16*)d_in[0]; a.mem0 = (const u16*)d_in[1];
  a.Wq = (const u16*)d_in[2]; a.Wk = (const u16*)d_in[3]; a.Wv = (const u16*)d_in[4]; a.Wo = (const u16*)d_in[5];
  a.wg = (const u16*)d_in[8]; a.wu = (const u16*)d_in[9]; a.wd = (const u16*)d_in[10];
  a.ws = (char*)d_ws; a.out = (u16*)d_out; a.s0 = 0; a.pad1 = 0;
  main_kernel<<<256, 512, 0, stream>>>(a);
}
```

```cpp
#include <hip/hip_runtime.h>
#include <hip/hip_bf16.h>
#include <stdint.h>
#include <type_traits>

constexpr int S_LEN = 2048, DM = 1024, NH = 32, DH = 128, NMEM = 8, SEG = 32;
constexpr int NSEG = S_LEN / SEG;
constexpr int HD = NH * DH;
constexpr int FF = 3584;

typedef unsigned short u16;
typedef __attribute__((ext_vector_type(8))) short bf16x8;
typedef __attribute__((ext_vector_type(4))) short s16x4;
typedef __attribute__((ext_vector_type(4))) float f32x4;
typedef __attribute__((ext_vector_type(4))) unsigned int u32x4;
typedef __attribute__((ext_vector_type(2))) unsigned int u32x2;
typedef __attribute__((address_space(3))) s16x4* lds_s16x4_ptr;
typedef __attribute__((address_space(3))) void* lds_void_ptr;
typedef __attribute__((address_space(1))) const void* glb_cvoid_ptr;
typedef __attribute__((address_space(1))) unsigned gu32;
typedef __bf16 bf16x2_t __attribute__((ext_vector_type(2)));
typedef float f32x2_t __attribute__((ext_vector_type(2)));

__device__ __forceinline__ float bf2f(u16 v) { return __uint_as_float(((unsigned)v) << 16); }
__device__ __forceinline__ unsigned cvtpk(float lo, float hi) {
  bf16x2_t r = __builtin_convertvector((f32x2_t){lo, hi}, bf16x2_t);
  return __builtin_bit_cast(unsigned, r);
}
__device__ __forceinline__ float lo_f(unsigned v) { return __uint_as_float(v << 16); }
__device__ __forceinline__ float hi_f(unsigned v) { return __uint_as_float(v & 0xffff0000u); }
__device__ __forceinline__ float rbf(float f) { return lo_f(cvtpk(f, f)); }
__device__ __forceinline__ bf16x8 as_bf16x8(u32x4 v) { return __builtin_bit_cast(bf16x8, v); }
#define MFMA(a, b, c) __builtin_amdgcn_mfma_f32_16x16x32_bf16((a), (b), (c), 0, 0, 0)
typedef __attribute__((ext_vector_type(16))) short bf16x16;
typedef __attribute__((ext_vector_type(8))) __bf16 bfv8;
typedef __attribute__((ext_vector_type(16))) __bf16 bfv16;
#define SMFMA(a, b, c, idx) __builtin_amdgcn_smfmac_f32_16x16x64_bf16(__builtin_bit_cast(bfv8, (a)), __builtin_bit_cast(bfv16, (b)), (c), (idx), 0, 0)
__device__ __forceinline__ bf16x16 cat16(bf16x8 lo, bf16x8 hi) {
  return __builtin_shufflevector(lo, hi, 0, 1, 2, 3, 4, 5, 6, 7, 8, 9, 10, 11, 12, 13, 14, 15);
}

#define KSTR 65
#define XLD 1024
constexpr int PAGE = 2 << 20, NPAGE = 14;
constexpr int T_K = 0, T_Q = 2304, T_V = 4608, T_O = 6912, T_END = T_O + 2048;
constexpr unsigned T_BYTES = T_END * 16384u;
constexpr size_t WS_SY = 0;
constexpr size_t WS_T = (size_t)NPAGE * PAGE;
constexpr size_t WS_XN = WS_T + T_BYTES;
constexpr size_t WS_HF = WS_XN + (size_t)S_LEN * XLD * 2;
constexpr size_t WS_G = WS_HF + (size_t)S_LEN * 16384;
constexpr size_t WS_S = WS_G + (size_t)S_LEN * DM * 2;
constexpr size_t WS_END = WS_S + (size_t)S_LEN * DM * 2;
constexpr unsigned SENT = 0xFFFFFFFFu;
constexpr unsigned DSENT = 0xFFBFFFBFu;
constexpr int SY_MEM = 0;
constexpr int SY_O = 2 * PAGE;
constexpr int SY_V = 6 * PAGE;
constexpr int SY_BR = PAGE, SY_BA = SY_BR + 16384;
constexpr int SY_FKQ = SY_BR + 32768;
constexpr int SY_FG = SY_FKQ + NH * 4 * 128;
constexpr int SY_FO = SY_FG + 16 * 8 * 128;
constexpr int SY_CH = SY_FO + 384 * 128;
constexpr int SY_ABORT = SY_CH + 8 * 128;
constexpr int SY_FS = SY_ABORT + 256;
constexpr int SY_FEND = SY_FS + 64 * 128;
constexpr int SY_EP = SY_FEND, SY_BT = SY_EP + 128;
constexpr int SY_TOTAL = NPAGE * PAGE;
constexpr int PROBE = PAGE - 16384 + 6144;
__device__ __forceinline__ int side(unsigned pm, int page) { return ((pm >> page) & 1u) << 13; }
__device__ __forceinline__ int o_mem(unsigned pm, int s) { return SY_MEM + s * 32768 + side(pm, 0); }
__device__ __forceinline__ int o_o(unsigned pm, int s, int h) {
  return SY_O + s * 131072 + (h >> 3) * 16384 + ((h >> 1) & 3) * 2048 + (h & 1) * 128 + side(pm, 2 + (s >> 4));
}
__device__ __forceinline__ int o_v(unsigned pm, int s, int h) { return SY_V + s * 262144 + (h >> 1) * 16384 + (h & 1) * 4096 + side(pm, 6 + (s >> 3)); }
__device__ __forceinline__ char* ws_align(char* ws) { return ws + ((PAGE - (unsigned)(uintptr_t)ws) & (PAGE - 1)); }
#define NFUSE 12
constexpr int NPRE = 4;
static_assert(WS_END + PAGE <= (size_t)256 << 20 && NH * KSTR <= T_Q && NPAGE % 2 == 0, "");
constexpr int NCHAIN = 128, NGW = 32;
__host__ __device__ constexpr int kseg(int sg, int h) { return h * KSTR + sg; }
__host__ __device__ constexpr int fkq(int h, int kind) { return SY_FKQ + (h * 4 + kind) * 128; }

#define XPROBE 0
struct Args {
  const u16 *x, *mem0, *Wq, *Wk, *Wv, *Wo, *wg, *wu, *wd;
  char* ws;
  u16* out;
  int s0, pad1;
};
__device__ __forceinline__ int h_off(const Args& p, int rblk) { return rblk * PAGE + (((p.s0 ^ rblk ^ 1) & 1) << 13); }
static_assert(WS_HF % (2 * PAGE) == 0, "");
#define TF_KQV 1
#define TF_O 0
__device__ __forceinline__ int t_off(const Args& p, int t) { return t * 16384 + (((p.s0 ^ (t >> 7) ^ (TF_KQV == 2 ? t : TF_KQV)) & 1) << 13); }
__device__ __forceinline__ int t_off_o(const Args& p, int t) { return t * 16384 + (((p.s0 ^ (t >> 7) ^ (TF_O == 2 ? t : TF_O)) & 1) << 13); }

__device__ __forceinline__ float wave_sum(float v) {
#pragma unroll
  for (int i = 0; i < 4; ++i) {
    const int x = (int)__float_as_uint(v);
    v += __uint_as_float(i == 0 ? __builtin_amdgcn_update_dpp(0, x, 0x128, 0xf, 0xf, false) : i == 1 ? __builtin_amdgcn_update_dpp(0, x, 0x124, 0xf, 0xf, false)
                         : i == 2 ? __builtin_amdgcn_update_dpp(0, x, 0x122, 0xf, 0xf, false) : __builtin_amdgcn_update_dpp(0, x, 0x121, 0xf, 0xf, false));
  }
  auto r = __builtin_amdgcn_permlane16_swap(__float_as_uint(v), __float_as_uint(v), false, false);
  v = __uint_as_float(r[0]) + __uint_as_float(r[1]);
  r = __builtin_amdgcn_permlane32_swap(__float_as_uint(v), __float_as_uint(v), false, false);
  return __uint_as_float(r[0]) + __uint_as_float(r[1]);
}

__shared__ __attribute__((aligned(16))) char g_smem[163840];
__device__ __forceinline__ unsigned lds_off(const void* p) {
  return (unsigned)(uintptr_t)(lds_void_ptr)g_smem + (unsigned)((const char*)p - g_smem);
}
__device__ __forceinline__ __amdgpu_buffer_rsrc_t mkrsrc(const void* p, unsigned bytes) {
  return __builtin_amdgcn_make_buffer_rsrc((void*)p, 0, bytes, 0x00020000);
}
__device__ __forceinline__ u32x4 ld_sc1(__amdgpu_buffer_rsrc_t rs, int off) {
  return __builtin_amdgcn_raw_buffer_load_b128(rs, off, 0, 16);
}
__device__ __forceinline__ void st_sc1(__amdgpu_buffer_rsrc_t rs, int off, u32x4 v) {
  __builtin_amdgcn_raw_buffer_store_b128(v, rs, off, 0, 16);
}
__device__ __forceinline__ unsigned ror8(unsigned v) {
  return (unsigned)__builtin_amdgcn_update_dpp(0, (int)v, 0x128, 0xf, 0xf, false);
}
__device__ __forceinline__ float ror8f(float v) { return __uint_as_float(ror8(__float_as_uint(v))); }
__device__ __forceinline__ void swap16(unsigned& x, unsigned& y) {
  auto r = __builtin_amdgcn_permlane16_swap(x, y, false, false);
  x = r[0]; y = r[1];
}
__device__ __forceinline__ void swap32(unsigned& x, unsigned& y) {
  auto r = __builtin_amdgcn_permlane32_swap(x, y, false, false);
  x = r[0]; y = r[1];
}
__device__ __forceinline__ f32x4 fold32(f32x4 x, f32x4 y) {
  f32x4 o;
#pragma unroll
  for (int j = 0; j < 4; ++j) {
    unsigned a = __float_as_uint(x[j]), b = __float_as_uint(y[j]);
    swap32(a, b);
    o[j] = __uint_as_float(a) + __uint_as_float(b);
  }
  return o;
}
__device__ __forceinline__ bf16x8 tr_frag(const char* img_lane) {
  s16x4 lo = __builtin_amdgcn_ds_read_tr16_b64_v4i16((lds_s16x4_ptr)img_lane);
  s16x4 hi = __builtin_amdgcn_ds_read_tr16_b64_v4i16((lds_s16x4_ptr)(img_lane + 64));
  return (bf16x8){lo[0], lo[1], lo[2], lo[3], hi[0], hi[1], hi[2], hi[3]};
}
__device__ __forceinline__ float rowq_max(float v) {
  unsigned a = __float_as_uint(v), b = a;
  swap16(a, b);
  v = __builtin_elementwise_maximum(__uint_as_float(a), __uint_as_float(b));
  a = __float_as_uint(v); b = a;
  swap32(a, b);
  return __builtin_elementwise_maximum(__uint_as_float(a), __uint_as_float(b));
}
__device__ __forceinline__ float rowq_sum(float v) {
  unsigned a = __float_as_uint(v), b = a;
  swap16(a, b);
  v = __uint_as_float(a) + __uint_as_float(b);
  a = __float_as_uint(v); b = a;
  swap32(a, b);
  return __uint_as_float(a) + __uint_as_float(b);
}
__device__ __forceinline__ bool spin_fail(unsigned& spins, gu32* abortw) {
  ++spins;
  if ((spins & 63u) == 0u) {
    const __amdgpu_buffer_rsrc_t ra = mkrsrc((const void*)abortw, 4);
    unsigned z = 0;
    asm volatile("" : "+v"(z));
    if (__builtin_amdgcn_readfirstlane(__builtin_amdgcn_raw_buffer_load_b32(ra, z, 0, 16)) == 1u) return true;
    if (spins > (1u << 19)) {
      __builtin_amdgcn_raw_buffer_store_b32(z + 1u, ra, z, 0, 16);
      return true;
    }
  }
  return false;
}
__device__ __forceinline__ unsigned umax4(u32x4 v) { return max(max(v[0], v[1]), max(v[2], v[3])); }
__device__ __forceinline__ bool has_ds(u32x4 v) { return (v[0] == DSENT) | (v[1] == DSENT) | (v[2] == DSENT) | (v[3] == DSENT); }
__device__ __forceinline__ float fmax2(float a, float b) { return __builtin_elementwise_maximum(a, b); }
__device__ __forceinline__ bf16x8 ldsf(const char* p) { return *reinterpret_cast<const bf16x8*>(p); }
#define SCHED() __builtin_amdgcn_sched_barrier(0)
#define LBAR() do { asm volatile("s_waitcnt lgkmcnt(0)" ::: "memory"); __builtin_amdgcn_s_barrier(); asm volatile("" ::: "memory"); } while (0)
#define WAIT_V(n) asm volatile("s_waitcnt vmcnt(%0)" :: "n"(n) : "memory")

#define HOLD_V(c, n) asm volatile("s_cmp_eq_u32 %0, 0\n\ts_cbranch_scc1 .Lh%=\n\ts_waitcnt vmcnt(%1)\n.Lh%=:" :: "s"((int)(c)), "n"(n) : "memory", "scc")
#define TH_Q 2
#define TH_V 63
#define TH_R 63
#define TH_P 63
__device__ __forceinline__ void wait_flags(__amdgpu_buffer_rsrc_t rs, int off, gu32* abortw) {
  for (unsigned spins = 0;;) {
    asm volatile("" ::: "memory");
    const unsigned v = __builtin_amdgcn_raw_buffer_load_b32(rs, off, 0, 16);
    if (__all(v != SENT)) break;
    __builtin_amdgcn_s_sleep(4);
    if (spin_fail(spins, abortw)) break;
  }
  asm volatile("" ::: "memory");
}

#define LST(i) do {} while (0)
#define RDY(i) do {} while (0)
#define RDW(i) do {} while (0)
#define SMP(k, s, v) do {} while (0)
#define LSTC(i) do {} while (0)
__device__ __forceinline__ unsigned offA(unsigned row, unsigned ch) {
  return row * 128u + 16u * (ch ^ (row & 7u));
}
__device__ __forceinline__ unsigned swzB(unsigned row) { return ((row & 3u) << 2) | ((row >> 2) & 3u); }
__device__ __forceinline__ unsigned offB(unsigned row, unsigned ch) {
  return 256u * row + 16u * (ch ^ swzB(row));
}
__device__ __forceinline__ void glds16(const u16* src, char* dst) {
  const unsigned d = __builtin_amdgcn_readfirstlane(lds_off(dst));
  unsigned keep;
  asm volatile("s_mov_b32 %0, m0\n\ts_mov_b32 m0, %2\n\ts_nop 0\n\tglobal_load_lds_dwordx4 %1, off\n\ts_mov_b32 m0, %0"
               : "=&s"(keep) : "v"(src), "s"(d) : "memory");
}
__device__ __forceinline__ bf16x8 ld_bfrag(const char* sb, int ks, int c, int lane) {
  const unsigned g = lane >> 4, qq = (lane & 15) >> 2, pp = lane & 3;
  s16x4 lo = __builtin_amdgcn_ds_read_tr16_b64_v4i16(
      (lds_s16x4_ptr)(sb + offB(32 * ks + 8 * g + qq, 2 * c + (pp >> 1)) + 8 * (pp & 1)));
  s16x4 hi = __builtin_amdgcn_ds_read_tr16_b64_v4i16(
      (lds_s16x4_ptr)(sb + offB(32 * ks + 8 * g + 4 + qq, 2 * c + (pp >> 1)) + 8 * (pp & 1)));
  return (bf16x8){lo[0], lo[1], lo[2], lo[3], hi[0], hi[1], hi[2], hi[3]};
}

__device__ __forceinline__ void blk_dma(const u16* src, size_t ld, char* slot, int lane) {
  const u16* s = src + (size_t)(lane >> 2) * ld + (lane & 3) * 8;
  glds16(s, slot);
  glds16(s + 16 * ld, slot + 1024);
}
__device__ __forceinline__ bf16x8 blk_frag(const char* slot, int T, int lane) {
  const char* a = slot + ((lane >> 4) * 8 + ((lane & 15) >> 2)) * 64 + (lane & 3) * 16 + T * 8;
  s16x4 lo = __builtin_amdgcn_ds_read_tr16_b64_v4i16((lds_s16x4_ptr)a);
  s16x4 hi = __builtin_amdgcn_ds_read_tr16_b64_v4i16((lds_s16x4_ptr)(a + 256));
  return (bf16x8){lo[0], lo[1], lo[2], lo[3], hi[0], hi[1], hi[2], hi[3]};
}
template <int NB, typename ISSUE, typename SINK>
__device__ __forceinline__ void load_blocks(char* slice, ISSUE&& issue, SINK&& sink) {
#pragma unroll
  for (int i = 0; i < 8; ++i) issue(i, slice + i * 2048);
#pragma unroll
  for (int g = 0; g < NB / 4; ++g) {
    if (g + 1 < NB / 4) WAIT_V(8); else WAIT_V(0);
#pragma unroll
    for (int j = 0; j < 4; ++j) sink(4 * g + j, slice + ((4 * g + j) & 7) * 2048);
    asm volatile("s_waitcnt lgkmcnt(0)" ::: "memory");
    if (g + 2 < NB / 4) {
#pragma unroll
      for (int j = 0; j < 4; ++j) issue(4 * g + 8 + j, slice + ((4 * g + j) & 7) * 2048);
    }
  }
}

__device__ __forceinline__ u32x4 widen32(unsigned a0, unsigned a1, unsigned b0, unsigned b1) {
  swap32(a0, b0); swap16(a0, b0);
  swap32(a1, b1); swap16(a1, b1);
  return (u32x4){a0, a1, b0, b1};
}

__device__ __forceinline__ void publish_flags(__amdgpu_buffer_rsrc_t rs, int off, int stride, int n) {
  WAIT_V(0);
  LBAR();
  int tid = threadIdx.x;
  asm volatile("" : "+v"(tid));
  if (tid < n) __builtin_amdgcn_raw_buffer_store_b32(1u, rs, off + tid * stride, 0, 16);
}

__device__ __forceinline__ void wait_s(const Args& p, __amdgpu_buffer_rsrc_t rsy, int rblk, int c0, int n, bool need) {
  if (need) {
    if (threadIdx.x < 64) {
      for (unsigned spins = 0;;) {
        asm volatile("" ::: "memory");
        const int f = (int)__builtin_amdgcn_raw_buffer_load_b32(rsy, SY_FS + (2 * c0 + (threadIdx.x & (2 * n - 1))) * 128, 0, 16);
        if (__all(f >= rblk)) break;
        __builtin_amdgcn_s_sleep(32);
        if (spin_fail(spins, (gu32*)(p.ws + WS_SY + SY_ABORT))) break;
      }
    }
    LBAR();
  }
}
__device__ __forceinline__ u32x4 combine(u32x4 sv, u32x4 g, u32x4 x) {
  u32x4 o;
#pragma unroll
  for (int j = 0; j < 4; ++j) {
    const unsigned t = cvtpk(lo_f(sv[j]) * lo_f(g[j]), hi_f(sv[j]) * hi_f(g[j]));
    o[j] = cvtpk(lo_f(x[j]) + lo_f(t), hi_f(x[j]) + hi_f(t));
  }
  return o;
}
#define OUT_AUX 0
__device__ __forceinline__ void st_out(const Args& p, int e, u32x4 v) {
  __builtin_amdgcn_raw_buffer_store_b128(v, mkrsrc(p.out, S_LEN * DM * 2), e * 2, 0, OUT_AUX);
}
struct SX { u32x4 s, x; };
__device__ __forceinline__ SX get_sx(const Args& p, int rblk, int e) {
  SX r; r.s = (u32x4){0u, 0u, 0u, 0u}; r.x = r.s;
  if (rblk < NFUSE) {
    r.s = ld_sc1(mkrsrc(p.ws + WS_S, S_LEN * DM * 2), (((e & 1023) >> 5) * S_LEN + (e >> 10)) * 64 + (e & 31) * 2);
    r.x = *reinterpret_cast<const u32x4*>(p.x + e);
  }
  return r;
}
__device__ __forceinline__ void put_g(const Args& p, __amdgpu_buffer_rsrc_t rg, int rblk, int e, u32x4 g, const SX& v) {
  if (rblk < NFUSE) {
    st_out(p, e, combine(v.s, g, v.x));
  } else st_sc1(rg, e * 2, g);
}

__device__ __forceinline__ void job_comb(const Args& p, int rblk, int ct, __amdgpu_buffer_rsrc_t rsy) {
  int tid = threadIdx.x;
  asm volatile("" : "+v"(tid));
  u32x4 xv[8];
#pragma unroll
  for (int i = 0; i < 8; ++i) xv[i] = *reinterpret_cast<const u32x4*>(p.x + (rblk * 128 + i * 16 + (tid >> 5)) * DM + ct * 256 + (tid & 31) * 8);
  if (tid < 64) wait_flags(rsy, SY_FG + (rblk * 8 + 2 * ct + (tid & 1)) * 128, (gu32*)(p.ws + WS_SY + SY_ABORT));
  wait_s(p, rsy, rblk, ct * 8, 8, true);
  u32x4 sv[8], gv[8];
#pragma unroll
  for (int i = 0; i < 8; ++i) {
    const int e = (rblk * 128 + i * 16 + (tid >> 5)) * DM + ct * 256 + (tid & 31) * 8;
    sv[i] = ld_sc1(mkrsrc(p.ws + WS_S, S_LEN * DM * 2), (((e & 1023) >> 5) * S_LEN + (e >> 10)) * 64 + (e & 31) * 2);
    gv[i] = ld_sc1(mkrsrc(p.ws + WS_G, S_LEN * DM * 2), e * 2);
  }
#pragma unroll
  for (int i = 0; i < 8; ++i)
    st_out(p, (rblk * 128 + i * 16 + (tid >> 5)) * DM + ct * 256 + (tid & 31) * 8, combine(sv[i], gv[i], xv[i]));
}

__device__ __forceinline__ u32x4 raw_rsrc(const void* p, unsigned bytes) {
  const unsigned long long a = (unsigned long long)p;
  return (u32x4){(unsigned)a, (unsigned)(a >> 32) & 0xffffu, bytes, 0x00020000u};
}
__device__ __forceinline__ u32x2 next_tag(u32x2 e) { return (u32x2){e[0] + 1u, e[1] + (e[0] == 0xFFFFFFFFu)}; }
struct Boot { u32x2 e; u32x4 c; };
__device__ __forceinline__ void boot_ask(const char* ws, Boot& r) {
  const unsigned o = (threadIdx.x & 63) * 128;
  asm volatile("buffer_load_dwordx2 %0, off, %3, %4 sc1\n\tbuffer_load_dwordx4 %1, %2, %3, %5 offen sc1"
               : "=&v"(r.e), "=&v"(r.c) : "v"(o), "s"(raw_rsrc(ws, SY_TOTAL)), "s"(SY_EP), "s"(SY_BT) : "memory");
}
__device__ __forceinline__ void wait_rows(const char* ws, u32x2 tag) {
  const __amdgpu_buffer_rsrc_t rs = mkrsrc(ws, SY_TOTAL);
  int o = (threadIdx.x & 63) * 128;
  asm volatile("" : "+v"(o));
#pragma unroll 1
  for (int i = 0; i < (1 << 17); ++i) {
    asm volatile("" ::: "memory");
    const u32x2 a = __builtin_amdgcn_raw_buffer_load_b64(rs, o, SY_BT + 32 * 128, 16), b = __builtin_amdgcn_raw_buffer_load_b64(rs, o, SY_BT + 96 * 128, 16);
    if (__all(a[0] == tag[0] && a[1] == tag[1] && b[0] == tag[0] && b[1] == tag[1])) break;
    __builtin_amdgcn_s_sleep(8);
  }
  asm volatile("" ::: "memory");
}
__device__ __forceinline__ unsigned boot_done(const char* ws, Boot& r, bool own = false, u32x2 tag = (u32x2){0u, 0u}) {
#pragma unroll 1
  for (int i = 0; i < (1 << 17); ++i) {
    asm volatile("" : "+v"(r.e), "+v"(r.c));
    if (!own) tag = next_tag(r.e);
    if (__all(r.c[0] == tag[0] && r.c[1] == tag[1])) break;
    __builtin_amdgcn_s_sleep(8);
    boot_ask(ws, r);
    WAIT_V(0);
  }
  unsigned q0[NPAGE], q1[NPAGE], ga = 0xFFFFu, gb = 0xFFFFu;
#pragma unroll
  for (int i = 0; i < NPAGE; ++i) {
    const unsigned x = __builtin_amdgcn_readlane(r.c[2], i), y = __builtin_amdgcn_readlane(r.c[2], i + NPAGE);
    q0[i] = min(x >> 16, y >> 16); q1[i] = min(x & 0xFFFFu, y & 0xFFFFu);
    ga = min(ga, (i & 1) ? q1[i] : q0[i]); gb = min(gb, (i & 1) ? q0[i] : q1[i]);
  }
  unsigned m = gb < ga ? 0x80001555u : 0x2AAAu;
  const unsigned thr = min(ga, gb) + 110;
#pragma unroll
  for (int i = 0; i < NPAGE; ++i) {
    const unsigned lo = min(q0[i], q1[i]), hi = max(q0[i], q1[i]);
    const unsigned keep = (lo < thr ? hi : 0u) > lo + 100 ? ~(1u << i) : ~0u;
    m = (m & keep) | ((q1[i] < q0[i] ? ~0u : 0u) & ~keep);
  }
  return m;
}
typedef __attribute__((address_space(3))) volatile unsigned* lds_vu_ptr;
__device__ __forceinline__ lds_vu_ptr lds_at(const void* p) {
  unsigned a = lds_off(p);
  asm volatile("" : "+v"(a));
  return (lds_vu_ptr)(uintptr_t)a;
}
__device__ __forceinline__ unsigned uni(unsigned v) {
  unsigned r;
  asm volatile("s_nop 1\n\tv_readfirstlane_b32 %0, %1\n\ts_nop 3" : "=s"(r) : "v"(v));
  return r;
}
__device__ __forceinline__ void start_sy(char* ws, const u16* mem0, int i, char* smem) {
  const int tid = threadIdx.x, lane = tid & 63, w = __builtin_amdgcn_readfirstlane(tid >> 6);
  const __amdgpu_buffer_rsrc_t rsy = mkrsrc(ws, SY_TOTAL);
  const __amdgpu_buffer_rsrc_t rs = mkrsrc(ws + (size_t)(i % NPAGE) * PAGE + PROBE - (i / NPAGE) * 2048, 16384);
  const u32x2 e = __builtin_amdgcn_raw_buffer_load_b64(rsy, 0, SY_EP, 16);
  const u32x4 ds = {DSENT, DSENT, DSENT, DSENT};
  if (lane < 32) st_sc1(rs, (lane >> 4) * 8192 + w * 256 + (lane & 15) * 16, ds);
  static_assert(NPRE == 4 && SY_FEND - SY_BR <= 32 * 6144, "");
#pragma unroll
  for (int u = 0; u < 6; ++u)
    st_sc1(rsy, (u < 4 ? SY_V + (i * 4 + u) * 8192 : SY_O + (i * 2 + u - 4) * 8192) + tid * 16, ds);
  if (w == 7) {
    const int n = i * 64 + lane, j = (n >> 10) * 512 + (n & 511);
    const unsigned* m0 = reinterpret_cast<const unsigned*>(mem0 + (2 * ((j >> 2) & 3) + (j & 1)) * DM + (j >> 4) * 16) + ((j >> 1) & 1);
    u32x4 m;
#pragma unroll
    for (int k = 0; k < 4; ++k) { const unsigned t = m0[2 * k]; m[k] = t == DSENT ? 0xFFFFFFFFu : t; }
    st_sc1(rsy, n * 16, m);
  } else if (w == 6) st_sc1(rsy, 32768 + i * 1024 + lane * 16, ds);
  else {
    const int o = SY_BR + i * 6144 + tid * 16;
    st_sc1(rsy, o < SY_FEND ? o : 0x7F000000, (u32x4){SENT, SENT, SENT, SENT});
  }
  unsigned t[4];
#pragma unroll
  for (int k = 0; k < 4; ++k) {
    unsigned long long c0, c1; unsigned v, off = ((k ^ (k >> 1)) & 1) * 8192 + w * 256 + (k >> 1) * 128;
    asm volatile("s_waitcnt vmcnt(0)\n\ts_barrier\n\ts_memtime %0\n\ts_waitcnt lgkmcnt(0)\n\tbuffer_load_dword %2, %3, %4, 0 offen sc1\n\ts_waitcnt vmcnt(0)\n\t"
                 "s_memtime %1\n\ts_waitcnt lgkmcnt(0)" : "=&s"(c0), "=&s"(c1), "=&v"(v) : "v"(off), "s"(rs) : "memory");
    t[k] = (unsigned)(c1 - c0);
  }
  lds_vu_ptr tm = lds_at(smem);
  if (lane == 0) { tm[w] = min(t[0], t[3]); tm[8 + w] = min(t[1], t[2]); }
  int ro = SY_BT + i * 128 + lane * 16;
  asm volatile("" : "+v"(ro));
  LBAR();
  RDY(384 + i);
  if (threadIdx.x < 8) {
    unsigned m0 = tm[0], m1 = tm[8];
#pragma unroll
    for (int k = 1; k < 8; ++k) { m0 = min(m0, tm[k]); m1 = min(m1, tm[8 + k]); }
    const u32x2 tag = next_tag(e);
    st_sc1(rsy, ro, (u32x4){tag[0], tag[1], min(m0, 30000u) << 16 | min(m1, 30000u), 0u});
  }
}
template <int NB, typename F>
__device__ __forceinline__ u32x2 start_rows(const Args& p, int g, unsigned on, F&& between) {
  const int tid = threadIdx.x, lane = tid & 63, row0 = g * 16 + (tid >> 6) * 2;
  u32x4 a[4];
  u32x2 e;
  asm volatile("buffer_load_dwordx4 %0, %5, %8, 0 offen\n\tbuffer_load_dwordx4 %1, %5, %8, 0 offen offset:1024\n\t"
               "buffer_load_dwordx4 %2, %5, %8, 0 offen offset:2048\n\tbuffer_load_dwordx4 %3, %5, %8, 0 offen offset:3072\n\t"
               "buffer_load_dwordx2 %4, off, %6, %7 sc1"
               : "=&v"(a[0]), "=&v"(a[1]), "=&v"(a[2]), "=&v"(a[3]), "=&v"(e)
               : "v"(row0 * (DM * 2) + lane * 16), "s"(raw_rsrc(p.ws, SY_TOTAL)), "s"(SY_EP), "s"(raw_rsrc(p.x, on * (S_LEN * DM * 2))) : "memory");
  between();
  asm volatile("s_waitcnt vmcnt(%5)" : "+v"(a[0]), "+v"(a[1]), "+v"(a[2]), "+v"(a[3]), "+v"(e) : "n"(NB) : "memory");
  const __amdgpu_buffer_rsrc_t ro = mkrsrc(p.ws + WS_XN, on * (S_LEN * XLD * 2));
#pragma unroll
  for (int u = 0; u < 2; ++u) {
    float v[16];
#pragma unroll
    for (int i = 0; i < 8; ++i) { v[2 * i] = lo_f(a[2 * u + (i >> 2)][i & 3]); v[2 * i + 1] = hi_f(a[2 * u + (i >> 2)][i & 3]); }
    float s = 0.f;
#pragma unroll
    for (int i = 0; i < 16; ++i) s += v[i];
    const float mean = wave_sum(s) * (1.0f / DM);
    float q = 0.f;
#pragma unroll
    for (int i = 0; i < 16; ++i) { float d = v[i] - mean; q += d * d; }
    const float var = rbf(wave_sum(q) * (1.0f / DM));
    const float mb = rbf(mean);
    const float inv = 1.0f / rbf(sqrtf(rbf(var + 1e-5f)));
#pragma unroll
    for (int k = 0; k < 2; ++k) {
      u32x4 o;
#pragma unroll
      for (int i = 0; i < 4; ++i) {
        const unsigned t = cvtpk(v[8 * k + 2 * i] - mb, v[8 * k + 2 * i + 1] - mb);
        o[i] = cvtpk(lo_f(t) * inv, hi_f(t) * inv);
      }
      st_sc1(ro, (row0 + u) * (XLD * 2) + k * 1024 + lane * 16, o);
    }
  }
  return next_tag(e);
}
__device__ __forceinline__ void report_rows(const Args& p, int g, u32x2 tag) {
  RDY(256 + g);
  if (threadIdx.x < 8) st_sc1(mkrsrc(p.ws + WS_SY, SY_TOTAL), SY_BT + (32 + g) * 128 + threadIdx.x * 16, (u32x4){tag[0], tag[1], tag[0], tag[1]});
}

typedef __attribute__((ext_vector_type(32))) unsigned u32x32;
__device__ __forceinline__ void put4(u32x32& W, int i, bf16x8 f) {
  const u32x4 v = __builtin_bit_cast(u32x4, f);
#pragma unroll
  for (int j = 0; j < 4; ++j) W[4 * i + j] = v[j];
}
#define CLB8(a, b, c, d, e, f, g, h) "v" #a, "v" #b, "v" #c, "v" #d, "v" #e, "v" #f, "v" #g, "v" #h
#define CLB32(a) CLB32_##a
#define CLB32_128 CLB8(128, 129, 130, 131, 132, 133, 134, 135), CLB8(136, 137, 138, 139, 140, 141, 142, 143), CLB8(144, 145, 146, 147, 148, 149, 150, 151), CLB8(152, 153, 154, 155, 156, 157, 158, 159)
#define CLB32_160 CLB8(160, 161, 162, 163, 164, 165, 166, 167), CLB8(168, 169, 170, 171, 172, 173, 174, 175), CLB8(176, 177, 178, 179, 180, 181, 182, 183), CLB8(184, 185, 186, 187, 188, 189, 190, 191)
#define W4_IN "{v[0:31]}"(W[0]), "{v[32:63]}"(W[1]), "{v[64:95]}"(W[2]), "{v[96:127]}"(W[3])
#define CLB32_192 CLB8(192, 193, 194, 195, 196, 197, 198, 199), CLB8(200, 201, 202, 203, 204, 205, 206, 207), CLB8(208, 209, 210, 211, 212, 213, 214, 215), CLB8(216, 217, 218, 219, 220, 221, 222, 223)
__device__ __forceinline__ unsigned ld_flag0(__amdgpu_buffer_rsrc_t rs, int off) {
  return __builtin_amdgcn_raw_buffer_load_b32(rs, off, 0, 16);
}
__device__ __forceinline__ void glds16_sc1(const char* src, char* dst) {
  const unsigned d = __builtin_amdgcn_readfirstlane(lds_off(dst));
  unsigned keep;
  asm volatile("s_mov_b32 %0, m0\n\ts_mov_b32 m0, %2\n\ts_nop 0\n\tglobal_load_lds_dwordx4 %1, off sc1\n\ts_mov_b32 m0, %0"
               : "=&s"(keep) : "v"(src), "s"(d) : "memory");
}

__device__ __forceinline__ void touch_line(const char* src, char* dst) {
  const unsigned d = __builtin_amdgcn_readfirstlane(lds_off(dst));
  unsigned keep;
  asm volatile("s_mov_b32 %0, m0\n\ts_mov_b32 m0, %2\n\ts_nop 0\n\tglobal_load_lds_dword %1, off sc1\n\ts_mov_b32 m0, %0"
               : "=&s"(keep) : "v"(src), "s"(d) : "memory");
}

#define GPF 1
#define GU_DELAY 0
#define Q_DELAY 0
#define PF_T0 100
#define PF_N 12
#define A_AUX " sc1"
#define LN_M 3
#define LN_X 1
#define PR_G1 8
#define WREV 1
template <bool DEV = false>
__device__ __forceinline__ void glds2(const char* base, int step, unsigned voff, unsigned dst) {
  unsigned keep;
  const char* base2 = base + step;
  if (DEV)
    asm volatile("s_mov_b32 %0, m0\n\ts_mov_b32 m0, %4\n\ts_nop 0\n\tglobal_load_lds_dwordx4 %1, %2 sc1\n\t"
                 "s_add_u32 m0, m0, 0x2000\n\ts_nop 0\n\tglobal_load_lds_dwordx4 %1, %3 sc1\n\ts_mov_b32 m0, %0"
                 : "=&s"(keep) : "v"(voff), "s"(base), "s"(base2), "s"(dst) : "memory", "scc");
  else
  asm volatile("s_mov_b32 %0, m0\n\ts_mov_b32 m0, %4\n\ts_nop 0\n\tglobal_load_lds_dwordx4 %1, %2\n\t"
               "s_add_u32 m0, m0, 0x2000\n\ts_nop 0\n\tglobal_load_lds_dwordx4 %1, %3\n\ts_mov_b32 m0, %0"
               : "=&s"(keep) : "v"(voff), "s"(base), "s"(base2), "s"(dst) : "memory", "scc");
}

#define DN_ASM \
  "s_mov_b32 s94, m0\n\t" \
  "s_mov_b64 s[84:85], %[pa]\n\t" \
  "s_mov_b64 s[86:87], %[pb]\n\t" \
  "s_movk_i32 s88, 0x4000\n\t" \
  "s_mov_b32 s89, 0x10000\n\t" \
  "s_movk_i32 s90, 13\n\t" \
  "v_mov_b64 v[0:1], 0\n\t" \
  "v_mov_b64 v[2:3], 0\n\t" \
  "v_mov_b64 v[4:5], 0\n\t" \
  "v_mov_b64 v[6:7], 0\n\t" \
  "v_mov_b64 v[8:9], 0\n\t" \
  "v_mov_b64 v[10:11], 0\n\t" \
  "v_mov_b64 v[12:13], 0\n\t" \
  "v_mov_b64 v[14:15], 0\n\t" \
  "v_mov_b64 v[16:17], 0\n\t" \
  "v_mov_b64 v[18:19], 0\n\t" \
  "v_mov_b64 v[20:21], 0\n\t" \
  "v_mov_b64 v[22:23], 0\n\t" \
  "v_mov_b64 v[24:25], 0\n\t" \
  "v_mov_b64 v[26:27], 0\n\t" \
  "v_mov_b64 v[28:29], 0\n\t" \
  "v_mov_b64 v[30:31], 0\n\t" \
  "s_cmp_lg_u32 %[hf], 0\n\t" \
  "s_cbranch_scc1 .Lk%=\n\t" \
  "ds_read_b128 v[32:35], v80 offset:0\n\t" \
  "ds_read_b128 v[36:39], v80 offset:1024\n\t" \
  "ds_read_b128 v[40:43], v80 offset:2048\n\t" \
  "ds_read_b128 v[44:47], v80 offset:3072\n\t" \
  "ds_read_b64_tr_b16 v[64:65], v82 offset:0\n\t" \
  "ds_read_b64_tr_b16 v[66:67], v83 offset:0\n\t" \
  "ds_read_b128 v[48:51], v80 offset:8192\n\t" \
  "ds_read_b64_tr_b16 v[68:69], v84 offset:0\n\t" \
  "ds_read_b64_tr_b16 v[70:71], v85 offset:0\n\t" \
  "ds_read_b128 v[52:55], v80 offset:9216\n\t" \
  "ds_read_b128 v[56:59], v80 offset:10240\n\t" \
  "ds_read_b128 v[60:63], v80 offset:11264\n\t" \
  ".Lp0%=:\n\t" \
  "s_waitcnt vmcnt(4)\n\t" \
  "s_barrier\n\t" \
  "s_waitcnt lgkmcnt(3)\n\t" \
  "v_mfma_f32_16x16x32_bf16 v[0:3], v[64:67], v[32:35], v[0:3]\n\t" \
  "ds_read_b64_tr_b16 v[72:73], v82 offset:8192\n\t" \
  "v_mfma_f32_16x16x32_bf16 v[4:7], v[68:71], v[32:35], v[4:7]\n\t" \
  "ds_read_b64_tr_b16 v[74:75], v83 offset:8192\n\t" \
  "ds_read_b128 v[32:35], v80 offset:32768\n\t" \
  "s_add_u32 m0, %[lw], 98304\n\t" \
  "v_mfma_f32_16x16x32_bf16 v[8:11], v[64:67], v[36:39], v[8:11]\n\t" \
  "ds_read_b64_tr_b16 v[76:77], v84 offset:8192\n\t" \
  "global_load_lds_dwordx4 v90, s[84:85] sc1\n\t" \
  "s_add_u32 s84, s84, s88\n\t" \
  "s_addc_u32 s85, s85, 0\n\t" \
  "v_mfma_f32_16x16x32_bf16 v[12:15], v[68:71], v[36:39], v[12:15]\n\t" \
  "ds_read_b64_tr_b16 v[78:79], v85 offset:8192\n\t" \
  "ds_read_b128 v[36:39], v80 offset:33792\n\t" \
  "v_mfma_f32_16x16x32_bf16 v[16:19], v[64:67], v[40:43], v[16:19]\n\t" \
  "v_mfma_f32_16x16x32_bf16 v[20:23], v[68:71], v[40:43], v[20:23]\n\t" \
  "ds_read_b128 v[40:43], v80 offset:34816\n\t" \
  "s_add_u32 m0, %[lw], 114688\n\t" \
  "v_mfma_f32_16x16x32_bf16 v[24:27], v[64:67], v[44:47], v[24:27]\n\t" \
  "global_load_lds_dwordx4 v91, s[86:87]\n\t" \
  "s_add_u32 s86, s86, s89\n\t" \
  "s_addc_u32 s87, s87, 0\n\t" \
  "v_mfma_f32_16x16x32_bf16 v[28:31], v[68:71], v[44:47], v[28:31]\n\t" \
  "ds_read_b128 v[44:47], v80 offset:35840\n\t" \
  "s_waitcnt lgkmcnt(3)\n\t" \
  "v_mfma_f32_16x16x32_bf16 v[0:3], v[72:75], v[48:51], v[0:3]\n\t" \
  "ds_read_b64_tr_b16 v[64:65], v82 offset:32768\n\t" \
  "v_mfma_f32_16x16x32_bf16 v[4:7], v[76:79], v[48:51], v[4:7]\n\t" \
  "ds_read_b64_tr_b16 v[66:67], v83 offset:32768\n\t" \
  "ds_read_b128 v[48:51], v80 offset:40960\n\t" \
  "s_add_u32 m0, %[lw], 106496\n\t" \
  "v_mfma_f32_16x16x32_bf16 v[8:11], v[72:75], v[52:55], v[8:11]\n\t" \
  "ds_read_b64_tr_b16 v[68:69], v84 offset:32768\n\t" \
  "global_load_lds_dwordx4 v90, s[84:85] sc1\n\t" \
  "s_add_u32 s84, s84, s88\n\t" \
  "s_addc_u32 s85, s85, 0\n\t" \
  "v_mfma_f32_16x16x32_bf16 v[12:15], v[76:79], v[52:55], v[12:15]\n\t" \
  "ds_read_b64_tr_b16 v[70:71], v85 offset:32768\n\t" \
  "ds_read_b128 v[52:55], v80 offset:41984\n\t" \
  "v_mfma_f32_16x16x32_bf16 v[16:19], v[72:75], v[56:59], v[16:19]\n\t" \
  "v_mfma_f32_16x16x32_bf16 v[20:23], v[76:79], v[56:59], v[20:23]\n\t" \
  "ds_read_b128 v[56:59], v80 offset:43008\n\t" \
  "s_add_u32 m0, %[lw], 122880\n\t" \
  "v_mfma_f32_16x16x32_bf16 v[24:27], v[72:75], v[60:63], v[24:27]\n\t" \
  "global_load_lds_dwordx4 v91, s[86:87]\n\t" \
  "s_add_u32 s86, s86, s89\n\t" \
  "s_addc_u32 s87, s87, 0\n\t" \
  "v_mfma_f32_16x16x32_bf16 v[28:31], v[76:79], v[60:63], v[28:31]\n\t" \
  "ds_read_b128 v[60:63], v80 offset:44032\n\t" \
  "s_sub_u32 s90, s90, 1\n\t" \
  "s_cselect_b32 s88, 0, s88\n\t" \
  "s_cselect_b32 s89, 0, s89\n\t" \
  "s_cselect_b32 s91, 16384, 0\n\t" \
  "s_sub_u32 s84, s84, s91\n\t" \
  "s_subb_u32 s85, s85, 0\n\t" \
  "s_lshl_b32 s91, s91, 2\n\t" \
  "s_sub_u32 s86, s86, s91\n\t" \
  "s_subb_u32 s87, s87, 0\n\t" \
  "s_waitcnt vmcnt(4)\n\t" \
  "s_barrier\n\t" \
  "s_waitcnt lgkmcnt(3)\n\t" \
  "v_mfma_f32_16x16x32_bf16 v[0:3], v[64:67], v[32:35], v[0:3]\n\t" \
  "ds_read_b64_tr_b16 v[72:73], v82 offset:40960\n\t" \
  "v_mfma_f32_16x16x32_bf16 v[4:7], v[68:71], v[32:35], v[4:7]\n\t" \
  "ds_read_b64_tr_b16 v[74:75], v83 offset:40960\n\t" \
  "ds_read_b128 v[32:35], v81 offset:0\n\t" \
  "s_add_u32 m0, %[lw], 0\n\t" \
  "v_mfma_f32_16x16x32_bf16 v[8:11], v[64:67], v[36:39], v[8:11]\n\t" \
  "ds_read_b64_tr_b16 v[76:77], v84 offset:40960\n\t" \
  "global_load_lds_dwordx4 v90, s[84:85] sc1\n\t" \
  "s_add_u32 s84, s84, s88\n\t" \
  "s_addc_u32 s85, s85, 0\n\t" \
  "v_mfma_f32_16x16x32_bf16 v[12:15], v[68:71], v[36:39], v[12:15]\n\t" \
  "ds_read_b64_tr_b16 v[78:79], v85 offset:40960\n\t" \
  "ds_read_b128 v[36:39], v81 offset:1024\n\t" \
  "v_mfma_f32_16x16x32_bf16 v[16:19], v[64:67], v[40:43], v[16:19]\n\t" \
  "v_mfma_f32_16x16x32_bf16 v[20:23], v[68:71], v[40:43], v[20:23]\n\t" \
  "ds_read_b128 v[40:43], v81 offset:2048\n\t" \
  "s_add_u32 m0, %[lw], 16384\n\t" \
  "v_mfma_f32_16x16x32_bf16 v[24:27], v[64:67], v[44:47], v[24:27]\n\t" \
  "global_load_lds_dwordx4 v91, s[86:87]\n\t" \
  "s_add_u32 s86, s86, s89\n\t" \
  "s_addc_u32 s87, s87, 0\n\t" \
  "v_mfma_f32_16x16x32_bf16 v[28:31], v[68:71], v[44:47], v[28:31]\n\t" \
  "ds_read_b128 v[44:47], v81 offset:3072\n\t" \
  "s_waitcnt lgkmcnt(3)\n\t" \
  "v_mfma_f32_16x16x32_bf16 v[0:3], v[72:75], v[48:51], v[0:3]\n\t" \
  "ds_read_b64_tr_b16 v[64:65], v86 offset:0\n\t" \
  "v_mfma_f32_16x16x32_bf16 v[4:7], v[76:79], v[48:51], v[4:7]\n\t" \
  "ds_read_b64_tr_b16 v[66:67], v87 offset:0\n\t" \
  "ds_read_b128 v[48:51], v81 offset:8192\n\t" \
  "s_add_u32 m0, %[lw], 8192\n\t" \
  "v_mfma_f32_16x16x32_bf16 v[8:11], v[72:75], v[52:55], v[8:11]\n\t" \
  "ds_read_b64_tr_b16 v[68:69], v88 offset:0\n\t" \
  "global_load_lds_dwordx4 v90, s[84:85] sc1\n\t" \
  "s_add_u32 s84, s84, s88\n\t" \
  "s_addc_u32 s85, s85, 0\n\t" \
  "v_mfma_f32_16x16x32_bf16 v[12:15], v[76:79], v[52:55], v[12:15]\n\t" \
  "ds_read_b64_tr_b16 v[70:71], v89 offset:0\n\t" \
  "ds_read_b128 v[52:55], v81 offset:9216\n\t" \
  "v_mfma_f32_16x16x32_bf16 v[16:19], v[72:75], v[56:59], v[16:19]\n\t" \
  "v_mfma_f32_16x16x32_bf16 v[20:23], v[76:79], v[56:59], v[20:23]\n\t" \
  "ds_read_b128 v[56:59], v81 offset:10240\n\t" \
  "s_add_u32 m0, %[lw], 24576\n\t" \
  "v_mfma_f32_16x16x32_bf16 v[24:27], v[72:75], v[60:63], v[24:27]\n\t" \
  "global_load_lds_dwordx4 v91, s[86:87]\n\t" \
  "s_add_u32 s86, s86, s89\n\t" \
  "s_addc_u32 s87, s87, 0\n\t" \
  "v_mfma_f32_16x16x32_bf16 v[28:31], v[76:79], v[60:63], v[28:31]\n\t" \
  "ds_read_b128 v[60:63], v81 offset:11264\n\t" \
  "s_waitcnt vmcnt(4)\n\t" \
  "s_barrier\n\t" \
  "s_waitcnt lgkmcnt(3)\n\t" \
  "v_mfma_f32_16x16x32_bf16 v[0:3], v[64:67], v[32:35], v[0:3]\n\t" \
  "ds_read_b64_tr_b16 v[72:73], v86 offset:8192\n\t" \
  "v_mfma_f32_16x16x32_bf16 v[4:7], v[68:71], v[32:35], v[4:7]\n\t" \
  "ds_read_b64_tr_b16 v[74:75], v87 offset:8192\n\t" \
  "ds_read_b128 v[32:35], v81 offset:32768\n\t" \
  "s_add_u32 m0, %[lw], 32768\n\t" \
  "v_mfma_f32_16x16x32_bf16 v[8:11], v[64:67], v[36:39], v[8:11]\n\t" \
  "ds_read_b64_tr_b16 v[76:77], v88 offset:8192\n\t" \
  "global_load_lds_dwordx4 v90, s[84:85] sc1\n\t" \
  "s_add_u32 s84, s84, s88\n\t" \
  "s_addc_u32 s85, s85, 0\n\t" \
  "v_mfma_f32_16x16x32_bf16 v[12:15], v[68:71], v[36:39], v[12:15]\n\t" \
  "ds_read_b64_tr_b16 v[78:79], v89 offset:8192\n\t" \
  "ds_read_b128 v[36:39], v81 offset:33792\n\t" \
  "v_mfma_f32_16x16x32_bf16 v[16:19], v[64:67], v[40:43], v[16:19]\n\t" \
  "v_mfma_f32_16x16x32_bf16 v[20:23], v[68:71], v[40:43], v[20:23]\n\t" \
  "ds_read_b128 v[40:43], v81 offset:34816\n\t" \
  "s_add_u32 m0, %[lw], 49152\n\t" \
  "v_mfma_f32_16x16x32_bf16 v[24:27], v[64:67], v[44:47], v[24:27]\n\t" \
  "global_load_lds_dwordx4 v91, s[86:87]\n\t" \
  "s_add_u32 s86, s86, s89\n\t" \
  "s_addc_u32 s87, s87, 0\n\t" \
  "v_mfma_f32_16x16x32_bf16 v[28:31], v[68:71], v[44:47], v[28:31]\n\t" \
  "ds_read_b128 v[44:47], v81 offset:35840\n\t" \
  "s_waitcnt lgkmcnt(3)\n\t" \
  "v_mfma_f32_16x16x32_bf16 v[0:3], v[72:75], v[48:51], v[0:3]\n\t" \
  "ds_read_b64_tr_b16 v[64:65], v86 offset:32768\n\t" \
  "v_mfma_f32_16x16x32_bf16 v[4:7], v[76:79], v[48:51], v[4:7]\n\t" \
  "ds_read_b64_tr_b16 v[66:67], v87 offset:32768\n\t" \
  "ds_read_b128 v[48:51], v81 offset:40960\n\t" \
  "s_add_u32 m0, %[lw], 40960\n\t" \
  "v_mfma_f32_16x16x32_bf16 v[8:11], v[72:75], v[52:55], v[8:11]\n\t" \
  "ds_read_b64_tr_b16 v[68:69], v88 offset:32768\n\t" \
  "global_load_lds_dwordx4 v90, s[84:85] sc1\n\t" \
  "s_add_u32 s84, s84, s88\n\t" \
  "s_addc_u32 s85, s85, 0\n\t" \
  "v_mfma_f32_16x16x32_bf16 v[12:15], v[76:79], v[52:55], v[12:15]\n\t" \
  "ds_read_b64_tr_b16 v[70:71], v89 offset:32768\n\t" \
  "ds_read_b128 v[52:55], v81 offset:41984\n\t" \
  "v_mfma_f32_16x16x32_bf16 v[16:19], v[72:75], v[56:59], v[16:19]\n\t" \
  "v_mfma_f32_16x16x32_bf16 v[20:23], v[76:79], v[56:59], v[20:23]\n\t" \
  "ds_read_b128 v[56:59], v81 offset:43008\n\t" \
  "s_add_u32 m0, %[lw], 57344\n\t" \
  "v_mfma_f32_16x16x32_bf16 v[24:27], v[72:75], v[60:63], v[24:27]\n\t" \
  "global_load_lds_dwordx4 v91, s[86:87]\n\t" \
  "s_add_u32 s86, s86, s89\n\t" \
  "s_addc_u32 s87, s87, 0\n\t" \
  "v_mfma_f32_16x16x32_bf16 v[28:31], v[76:79], v[60:63], v[28:31]\n\t" \
  "ds_read_b128 v[60:63], v81 offset:44032\n\t" \
  "s_waitcnt vmcnt(4)\n\t" \
  "s_barrier\n\t" \
  "s_waitcnt lgkmcnt(3)\n\t" \
  "v_mfma_f32_16x16x32_bf16 v[0:3], v[64:67], v[32:35], v[0:3]\n\t" \
  "ds_read_b64_tr_b16 v[72:73], v86 offset:40960\n\t" \
  "v_mfma_f32_16x16x32_bf16 v[4:7], v[68:71], v[32:35], v[4:7]\n\t" \
  "ds_read_b64_tr_b16 v[74:75], v87 offset:40960\n\t" \
  "ds_read_b128 v[32:35], v80 offset:0\n\t" \
  "s_add_u32 m0, %[lw], 65536\n\t" \
  "v_mfma_f32_16x16x32_bf16 v[8:11], v[64:67], v[36:39], v[8:11]\n\t" \
  "ds_read_b64_tr_b16 v[76:77], v88 offset:40960\n\t" \
  "global_load_lds_dwordx4 v90, s[84:85] sc1\n\t" \
  "s_add_u32 s84, s84, s88\n\t" \
  "s_addc_u32 s85, s85, 0\n\t" \
  "v_mfma_f32_16x16x32_bf16 v[12:15], v[68:71], v[36:39], v[12:15]\n\t" \
  "ds_read_b64_tr_b16 v[78:79], v89 offset:40960\n\t" \
  "ds_read_b128 v[36:39], v80 offset:1024\n\t" \
  "v_mfma_f32_16x16x32_bf16 v[16:19], v[64:67], v[40:43], v[16:19]\n\t" \
  "v_mfma_f32_16x16x32_bf16 v[20:23], v[68:71], v[40:43], v[20:23]\n\t" \
  "ds_read_b128 v[40:43], v80 offset:2048\n\t" \
  "s_add_u32 m0, %[lw], 81920\n\t" \
  "v_mfma_f32_16x16x32_bf16 v[24:27], v[64:67], v[44:47], v[24:27]\n\t" \
  "global_load_lds_dwordx4 v91, s[86:87]\n\t" \
  "s_add_u32 s86, s86, s89\n\t" \
  "s_addc_u32 s87, s87, 0\n\t" \
  "v_mfma_f32_16x16x32_bf16 v[28:31], v[68:71], v[44:47], v[28:31]\n\t" \
  "ds_read_b128 v[44:47], v80 offset:3072\n\t" \
  "s_waitcnt lgkmcnt(3)\n\t" \
  "v_mfma_f32_16x16x32_bf16 v[0:3], v[72:75], v[48:51], v[0:3]\n\t" \
  "ds_read_b64_tr_b16 v[64:65], v82 offset:0\n\t" \
  "v_mfma_f32_16x16x32_bf16 v[4:7], v[76:79], v[48:51], v[4:7]\n\t" \
  "ds_read_b64_tr_b16 v[66:67], v83 offset:0\n\t" \
  "ds_read_b128 v[48:51], v80 offset:8192\n\t" \
  "s_add_u32 m0, %[lw], 73728\n\t" \
  "v_mfma_f32_16x16x32_bf16 v[8:11], v[72:75], v[52:55], v[8:11]\n\t" \
  "ds_read_b64_tr_b16 v[68:69], v84 offset:0\n\t" \
  "global_load_lds_dwordx4 v90, s[84:85] sc1\n\t" \
  "s_add_u32 s84, s84, s88\n\t" \
  "s_addc_u32 s85, s85, 0\n\t" \
  "v_mfma_f32_16x16x32_bf16 v[12:15], v[76:79], v[52:55], v[12:15]\n\t" \
  "ds_read_b64_tr_b16 v[70:71], v85 offset:0\n\t" \
  "ds_read_b128 v[52:55], v80 offset:9216\n\t" \
  "v_mfma_f32_16x16x32_bf16 v[16:19], v[72:75], v[56:59], v[16:19]\n\t" \
  "v_mfma_f32_16x16x32_bf16 v[20:23], v[76:79], v[56:59], v[20:23]\n\t" \
  "ds_read_b128 v[56:59], v80 offset:10240\n\t" \
  "s_add_u32 m0, %[lw], 90112\n\t" \
  "v_mfma_f32_16x16x32_bf16 v[24:27], v[72:75], v[60:63], v[24:27]\n\t" \
  "global_load_lds_dwordx4 v91, s[86:87]\n\t" \
  "s_add_u32 s86, s86, s89\n\t" \
  "s_addc_u32 s87, s87, 0\n\t" \
  "v_mfma_f32_16x16x32_bf16 v[28:31], v[76:79], v[60:63], v[28:31]\n\t" \
  "ds_read_b128 v[60:63], v80 offset:11264\n\t" \
  "s_cmp_lg_u32 s88, 0\n\t" \
  "s_cbranch_scc1 .Lp0%=\n\t" \
  "s_branch .Le%=\n\t" \
  ".Lk%=:\n\t" \
  "ds_read_b128 v[32:35], v80 offset:0\n\t" \
  "ds_read_b128 v[36:39], v80 offset:1024\n\t" \
  "ds_read_b128 v[40:43], v80 offset:2048\n\t" \
  "ds_read_b128 v[44:47], v80 offset:3072\n\t" \
  "ds_read_b64_tr_b16 v[64:65], v82 offset:0\n\t" \
  "ds_read_b64_tr_b16 v[66:67], v83 offset:0\n\t" \
  "ds_read_b128 v[48:51], v80 offset:8192\n\t" \
  "ds_read_b64_tr_b16 v[68:69], v84 offset:0\n\t" \
  "ds_read_b64_tr_b16 v[70:71], v85 offset:0\n\t" \
  "ds_read_b128 v[52:55], v80 offset:9216\n\t" \
  "ds_read_b128 v[56:59], v80 offset:10240\n\t" \
  "ds_read_b128 v[60:63], v80 offset:11264\n\t" \
  ".Lp1%=:\n\t" \
  "s_waitcnt vmcnt(4)\n\t" \
  "s_barrier\n\t" \
  "s_waitcnt lgkmcnt(3)\n\t" \
  "v_mfma_f32_16x16x32_bf16 v[0:3], v[64:67], v[32:35], v[0:3]\n\t" \
  "ds_read_b64_tr_b16 v[72:73], v82 offset:8192\n\t" \
  "v_mfma_f32_16x16x32_bf16 v[4:7], v[68:71], v[32:35], v[4:7]\n\t" \
  "ds_read_b64_tr_b16 v[74:75], v83 offset:8192\n\t" \
  "ds_read_b128 v[32:35], v80 offset:32768\n\t" \
  "v_mfma_f32_16x16x32_bf16 v[8:11], v[64:67], v[36:39], v[8:11]\n\t" \
  "ds_read_b64_tr_b16 v[76:77], v84 offset:8192\n\t" \
  "v_mfma_f32_16x16x32_bf16 v[12:15], v[68:71], v[36:39], v[12:15]\n\t" \
  "ds_read_b64_tr_b16 v[78:79], v85 offset:8192\n\t" \
  "ds_read_b128 v[36:39], v80 offset:33792\n\t" \
  "s_add_u32 m0, %[lw], 98304\n\t" \
  "v_mfma_f32_16x16x32_bf16 v[16:19], v[64:67], v[40:43], v[16:19]\n\t" \
  "global_load_lds_dwordx4 v90, s[84:85] sc1\n\t" \
  "s_add_u32 s84, s84, s88\n\t" \
  "s_addc_u32 s85, s85, 0\n\t" \
  "v_mfma_f32_16x16x32_bf16 v[20:23], v[68:71], v[40:43], v[20:23]\n\t" \
  "ds_read_b128 v[40:43], v80 offset:34816\n\t" \
  "v_mfma_f32_16x16x32_bf16 v[24:27], v[64:67], v[44:47], v[24:27]\n\t" \
  "s_add_u32 m0, %[lw], 114688\n\t" \
  "v_mfma_f32_16x16x32_bf16 v[28:31], v[68:71], v[44:47], v[28:31]\n\t" \
  "ds_read_b128 v[44:47], v80 offset:35840\n\t" \
  "global_load_lds_dwordx4 v91, s[86:87]\n\t" \
  "s_add_u32 s86, s86, s89\n\t" \
  "s_addc_u32 s87, s87, 0\n\t" \
  "s_waitcnt lgkmcnt(3)\n\t" \
  "v_mfma_f32_16x16x32_bf16 v[0:3], v[72:75], v[48:51], v[0:3]\n\t" \
  "ds_read_b64_tr_b16 v[64:65], v82 offset:32768\n\t" \
  "v_mfma_f32_16x16x32_bf16 v[4:7], v[76:79], v[48:51], v[4:7]\n\t" \
  "ds_read_b64_tr_b16 v[66:67], v83 offset:32768\n\t" \
  "ds_read_b128 v[48:51], v80 offset:40960\n\t" \
  "v_mfma_f32_16x16x32_bf16 v[8:11], v[72:75], v[52:55], v[8:11]\n\t" \
  "ds_read_b64_tr_b16 v[68:69], v84 offset:32768\n\t" \
  "v_mfma_f32_16x16x32_bf16 v[12:15], v[76:79], v[52:55], v[12:15]\n\t" \
  "ds_read_b64_tr_b16 v[70:71], v85 offset:32768\n\t" \
  "ds_read_b128 v[52:55], v80 offset:41984\n\t" \
  "s_add_u32 m0, %[lw], 106496\n\t" \
  "v_mfma_f32_16x16x32_bf16 v[16:19], v[72:75], v[56:59], v[16:19]\n\t" \
  "global_load_lds_dwordx4 v90, s[84:85] sc1\n\t" \
  "s_add_u32 s84, s84, s88\n\t" \
  "s_addc_u32 s85, s85, 0\n\t" \
  "v_mfma_f32_16x16x32_bf16 v[20:23], v[76:79], v[56:59], v[20:23]\n\t" \
  "ds_read_b128 v[56:59], v80 offset:43008\n\t" \
  "v_mfma_f32_16x16x32_bf16 v[24:27], v[72:75], v[60:63], v[24:27]\n\t" \
  "s_add_u32 m0, %[lw], 122880\n\t" \
  "v_mfma_f32_16x16x32_bf16 v[28:31], v[76:79], v[60:63], v[28:31]\n\t" \
  "ds_read_b128 v[60:63], v80 offset:44032\n\t" \
  "global_load_lds_dwordx4 v91, s[86:87]\n\t" \
  "s_add_u32 s86, s86, s89\n\t" \
  "s_addc_u32 s87, s87, 0\n\t" \
  "s_sub_u32 s90, s90, 1\n\t" \
  "s_cselect_b32 s88, 0, s88\n\t" \
  "s_cselect_b32 s89, 0, s89\n\t" \
  "s_cselect_b32 s91, 16384, 0\n\t" \
  "s_sub_u32 s84, s84, s91\n\t" \
  "s_subb_u32 s85, s85, 0\n\t" \
  "s_lshl_b32 s91, s91, 2\n\t" \
  "s_sub_u32 s86, s86, s91\n\t" \
  "s_subb_u32 s87, s87, 0\n\t" \
  "s_waitcnt vmcnt(4)\n\t" \
  "s_barrier\n\t" \
  "s_waitcnt lgkmcnt(3)\n\t" \
  "v_mfma_f32_16x16x32_bf16 v[0:3], v[64:67], v[32:35], v[0:3]\n\t" \
  "ds_read_b64_tr_b16 v[72:73], v82 offset:40960\n\t" \
  "v_mfma_f32_16x16x32_bf16 v[4:7], v[68:71], v[32:35], v[4:7]\n\t" \
  "ds_read_b64_tr_b16 v[74:75], v83 offset:40960\n\t" \
  "ds_read_b128 v[32:35], v81 offset:0\n\t" \
  "v_mfma_f32_16x16x32_bf16 v[8:11], v[64:67], v[36:39], v[8:11]\n\t" \
  "ds_read_b64_tr_b16 v[76:77], v84 offset:40960\n\t" \
  "v_mfma_f32_16x16x32_bf16 v[12:15], v[68:71], v[36:39], v[12:15]\n\t" \
  "ds_read_b64_tr_b16 v[78:79], v85 offset:40960\n\t" \
  "ds_read_b128 v[36:39], v81 offset:1024\n\t" \
  "s_add_u32 m0, %[lw], 0\n\t" \
  "v_mfma_f32_16x16x32_bf16 v[16:19], v[64:67], v[40:43], v[16:19]\n\t" \
  "global_load_lds_dwordx4 v90, s[84:85] sc1\n\t" \
  "s_add_u32 s84, s84, s88\n\t" \
  "s_addc_u32 s85, s85, 0\n\t" \
  "v_mfma_f32_16x16x32_bf16 v[20:23], v[68:71], v[40:43], v[20:23]\n\t" \
  "ds_read_b128 v[40:43], v81 offset:2048\n\t" \
  "v_mfma_f32_16x16x32_bf16 v[24:27], v[64:67], v[44:47], v[24:27]\n\t" \
  "s_add_u32 m0, %[lw], 16384\n\t" \
  "v_mfma_f32_16x16x32_bf16 v[28:31], v[68:71], v[44:47], v[28:31]\n\t" \
  "ds_read_b128 v[44:47], v81 offset:3072\n\t" \
  "global_load_lds_dwordx4 v91, s[86:87]\n\t" \
  "s_add_u32 s86, s86, s89\n\t" \
  "s_addc_u32 s87, s87, 0\n\t" \
  "s_waitcnt lgkmcnt(3)\n\t" \
  "v_mfma_f32_16x16x32_bf16 v[0:3], v[72:75], v[48:51], v[0:3]\n\t" \
  "ds_read_b64_tr_b16 v[64:65], v86 offset:0\n\t" \
  "v_mfma_f32_16x16x32_bf16 v[4:7], v[76:79], v[48:51], v[4:7]\n\t" \
  "ds_read_b64_tr_b16 v[66:67], v87 offset:0\n\t" \
  "ds_read_b128 v[48:51], v81 offset:8192\n\t" \
  "v_mfma_f32_16x16x32_bf16 v[8:11], v[72:75], v[52:55], v[8:11]\n\t" \
  "ds_read_b64_tr_b16 v[68:69], v88 offset:0\n\t" \
  "v_mfma_f32_16x16x32_bf16 v[12:15], v[76:79], v[52:55], v[12:15]\n\t" \
  "ds_read_b64_tr_b16 v[70:71], v89 offset:0\n\t" \
  "ds_read_b128 v[52:55], v81 offset:9216\n\t" \
  "s_add_u32 m0, %[lw], 8192\n\t" \
  "v_mfma_f32_16x16x32_bf16 v[16:19], v[72:75], v[56:59], v[16:19]\n\t" \
  "global_load_lds_dwordx4 v90, s[84:85] sc1\n\t" \
  "s_add_u32 s84, s84, s88\n\t" \
  "s_addc_u32 s85, s85, 0\n\t" \
  "v_mfma_f32_16x16x32_bf16 v[20:23], v[76:79], v[56:59], v[20:23]\n\t" \
  "ds_read_b128 v[56:59], v81 offset:10240\n\t" \
  "v_mfma_f32_16x16x32_bf16 v[24:27], v[72:75], v[60:63], v[24:27]\n\t" \
  "s_add_u32 m0, %[lw], 24576\n\t" \
  "v_mfma_f32_16x16x32_bf16 v[28:31], v[76:79], v[60:63], v[28:31]\n\t" \
  "ds_read_b128 v[60:63], v81 offset:11264\n\t" \
  "global_load_lds_dwordx4 v91, s[86:87]\n\t" \
  "s_add_u32 s86, s86, s89\n\t" \
  "s_addc_u32 s87, s87, 0\n\t" \
  "s_waitcnt vmcnt(4)\n\t" \
  "s_barrier\n\t" \
  "s_waitcnt lgkmcnt(3)\n\t" \
  "v_mfma_f32_16x16x32_bf16 v[0:3], v[64:67], v[32:35], v[0:3]\n\t" \
  "ds_read_b64_tr_b16 v[72:73], v86 offset:8192\n\t" \
  "v_mfma_f32_16x16x32_bf16 v[4:7], v[68:71], v[32:35], v[4:7]\n\t" \
  "ds_read_b64_tr_b16 v[74:75], v87 offset:8192\n\t" \
  "ds_read_b128 v[32:35], v81 offset:32768\n\t" \
  "v_mfma_f32_16x16x32_bf16 v[8:11], v[64:67], v[36:39], v[8:11]\n\t" \
  "ds_read_b64_tr_b16 v[76:77], v88 offset:8192\n\t" \
  "v_mfma_f32_16x16x32_bf16 v[12:15], v[68:71], v[36:39], v[12:15]\n\t" \
  "ds_read_b64_tr_b16 v[78:79], v89 offset:8192\n\t" \
  "ds_read_b128 v[36:39], v81 offset:33792\n\t" \
  "s_add_u32 m0, %[lw], 32768\n\t" \
  "v_mfma_f32_16x16x32_bf16 v[16:19], v[64:67], v[40:43], v[16:19]\n\t" \
  "global_load_lds_dwordx4 v90, s[84:85] sc1\n\t" \
  "s_add_u32 s84, s84, s88\n\t" \
  "s_addc_u32 s85, s85, 0\n\t" \
  "v_mfma_f32_16x16x32_bf16 v[20:23], v[68:71], v[40:43], v[20:23]\n\t" \
  "ds_read_b128 v[40:43], v81 offset:34816\n\t" \
  "v_mfma_f32_16x16x32_bf16 v[24:27], v[64:67], v[44:47], v[24:27]\n\t" \
  "s_add_u32 m0, %[lw], 49152\n\t" \
  "v_mfma_f32_16x16x32_bf16 v[28:31], v[68:71], v[44:47], v[28:31]\n\t" \
  "ds_read_b128 v[44:47], v81 offset:35840\n\t" \
  "global_load_lds_dwordx4 v91, s[86:87]\n\t" \
  "s_add_u32 s86, s86, s89\n\t" \
  "s_addc_u32 s87, s87, 0\n\t" \
  "s_waitcnt lgkmcnt(3)\n\t" \
  "v_mfma_f32_16x16x32_bf16 v[0:3], v[72:75], v[48:51], v[0:3]\n\t" \
  "ds_read_b64_tr_b16 v[64:65], v86 offset:32768\n\t" \
  "v_mfma_f32_16x16x32_bf16 v[4:7], v[76:79], v[48:51], v[4:7]\n\t" \
  "ds_read_b64_tr_b16 v[66:67], v87 offset:32768\n\t" \
  "ds_read_b128 v[48:51], v81 offset:40960\n\t" \
  "v_mfma_f32_16x16x32_bf16 v[8:11], v[72:75], v[52:55], v[8:11]\n\t" \
  "ds_read_b64_tr_b16 v[68:69], v88 offset:32768\n\t" \
  "v_mfma_f32_16x16x32_bf16 v[12:15], v[76:79], v[52:55], v[12:15]\n\t" \
  "ds_read_b64_tr_b16 v[70:71], v89 offset:32768\n\t" \
  "ds_read_b128 v[52:55], v81 offset:41984\n\t" \
  "s_add_u32 m0, %[lw], 40960\n\t" \
  "v_mfma_f32_16x16x32_bf16 v[16:19], v[72:75], v[56:59], v[16:19]\n\t" \
  "global_load_lds_dwordx4 v90, s[84:85] sc1\n\t" \
  "s_add_u32 s84, s84, s88\n\t" \
  "s_addc_u32 s85, s85, 0\n\t" \
  "v_mfma_f32_16x16x32_bf16 v[20:23], v[76:79], v[56:59], v[20:23]\n\t" \
  "ds_read_b128 v[56:59], v81 offset:43008\n\t" \
  "v_mfma_f32_16x16x32_bf16 v[24:27], v[72:75], v[60:63], v[24:27]\n\t" \
  "s_add_u32 m0, %[lw], 57344\n\t" \
  "v_mfma_f32_16x16x32_bf16 v[28:31], v[76:79], v[60:63], v[28:31]\n\t" \
  "ds_read_b128 v[60:63], v81 offset:44032\n\t" \
  "global_load_lds_dwordx4 v91, s[86:87]\n\t" \
  "s_add_u32 s86, s86, s89\n\t" \
  "s_addc_u32 s87, s87, 0\n\t" \
  "s_waitcnt vmcnt(4)\n\t" \
  "s_barrier\n\t" \
  "s_waitcnt lgkmcnt(3)\n\t" \
  "v_mfma_f32_16x16x32_bf16 v[0:3], v[64:67], v[32:35], v[0:3]\n\t" \
  "ds_read_b64_tr_b16 v[72:73], v86 offset:40960\n\t" \
  "v_mfma_f32_16x16x32_bf16 v[4:7], v[68:71], v[32:35], v[4:7]\n\t" \
  "ds_read_b64_tr_b16 v[74:75], v87 offset:40960\n\t" \
  "ds_read_b128 v[32:35], v80 offset:0\n\t" \
  "v_mfma_f32_16x16x32_bf16 v[8:11], v[64:67], v[36:39], v[8:11]\n\t" \
  "ds_read_b64_tr_b16 v[76:77], v88 offset:40960\n\t" \
  "v_mfma_f32_16x16x32_bf16 v[12:15], v[68:71], v[36:39], v[12:15]\n\t" \
  "ds_read_b64_tr_b16 v[78:79], v89 offset:40960\n\t" \
  "ds_read_b128 v[36:39], v80 offset:1024\n\t" \
  "s_add_u32 m0, %[lw], 65536\n\t" \
  "v_mfma_f32_16x16x32_bf16 v[16:19], v[64:67], v[40:43], v[16:19]\n\t" \
  "global_load_lds_dwordx4 v90, s[84:85] sc1\n\t" \
  "s_add_u32 s84, s84, s88\n\t" \
  "s_addc_u32 s85, s85, 0\n\t" \
  "v_mfma_f32_16x16x32_bf16 v[20:23], v[68:71], v[40:43], v[20:23]\n\t" \
  "ds_read_b128 v[40:43], v80 offset:2048\n\t" \
  "v_mfma_f32_16x16x32_bf16 v[24:27], v[64:67], v[44:47], v[24:27]\n\t" \
  "s_add_u32 m0, %[lw], 81920\n\t" \
  "v_mfma_f32_16x16x32_bf16 v[28:31], v[68:71], v[44:47], v[28:31]\n\t" \
  "ds_read_b128 v[44:47], v80 offset:3072\n\t" \
  "global_load_lds_dwordx4 v91, s[86:87]\n\t" \
  "s_add_u32 s86, s86, s89\n\t" \
  "s_addc_u32 s87, s87, 0\n\t" \
  "s_waitcnt lgkmcnt(3)\n\t" \
  "v_mfma_f32_16x16x32_bf16 v[0:3], v[72:75], v[48:51], v[0:3]\n\t" \
  "ds_read_b64_tr_b16 v[64:65], v82 offset:0\n\t" \
  "v_mfma_f32_16x16x32_bf16 v[4:7], v[76:79], v[48:51], v[4:7]\n\t" \
  "ds_read_b64_tr_b16 v[66:67], v83 offset:0\n\t" \
  "ds_read_b128 v[48:51], v80 offset:8192\n\t" \
  "v_mfma_f32_16x16x32_bf16 v[8:11], v[72:75], v[52:55], v[8:11]\n\t" \
  "ds_read_b64_tr_b16 v[68:69], v84 offset:0\n\t" \
  "v_mfma_f32_16x16x32_bf16 v[12:15], v[76:79], v[52:55], v[12:15]\n\t" \
  "ds_read_b64_tr_b16 v[70:71], v85 offset:0\n\t" \
  "ds_read_b128 v[52:55], v80 offset:9216\n\t" \
  "s_add_u32 m0, %[lw], 73728\n\t" \
  "v_mfma_f32_16x16x32_bf16 v[16:19], v[72:75], v[56:59], v[16:19]\n\t" \
  "global_load_lds_dwordx4 v90, s[84:85] sc1\n\t" \
  "s_add_u32 s84, s84, s88\n\t" \
  "s_addc_u32 s85, s85, 0\n\t" \
  "v_mfma_f32_16x16x32_bf16 v[20:23], v[76:79], v[56:59], v[20:23]\n\t" \
  "ds_read_b128 v[56:59], v80 offset:10240\n\t" \
  "v_mfma_f32_16x16x32_bf16 v[24:27], v[72:75], v[60:63], v[24:27]\n\t" \
  "s_add_u32 m0, %[lw], 90112\n\t" \
  "v_mfma_f32_16x16x32_bf16 v[28:31], v[76:79], v[60:63], v[28:31]\n\t" \
  "ds_read_b128 v[60:63], v80 offset:11264\n\t" \
  "global_load_lds_dwordx4 v91, s[86:87]\n\t" \
  "s_add_u32 s86, s86, s89\n\t" \
  "s_addc_u32 s87, s87, 0\n\t" \
  "s_cmp_lg_u32 s88, 0\n\t" \
  "s_cbranch_scc1 .Lp1%=\n\t" \
  ".Le%=:\n\t" \
  "s_waitcnt vmcnt(0) lgkmcnt(0)\n\t" \
  "s_mov_b32 m0, s94\n\t" \
  "s_setprio 0\n\t" \
  "s_nop 7\n\t"
#define HSL 16
__device__ __forceinline__ void job_down(const Args& p, int rblk, int ct, char* smem, __amdgpu_buffer_rsrc_t rsy) {
  if (threadIdx.x < 64) {
    for (unsigned spins = 0;;) {
      asm volatile("" ::: "memory");
      if (__builtin_amdgcn_readfirstlane((int)ld_flag0(rsy, SY_CH + (rblk >> 1) * 128)) >= 27) break;
      __builtin_amdgcn_s_sleep(HSL);
      if (spin_fail(spins, (gu32*)(p.ws + WS_SY + SY_ABORT))) break;
    }
  }
  int tid = threadIdx.x;
  asm volatile("" : "+v"(tid));
  const int lane = tid & 63, r = lane & 15, q = lane >> 4;
  const int wid = __builtin_amdgcn_readfirstlane(tid >> 6), wr = wid >> 2, wc = wid & 3;
  typedef __attribute__((ext_vector_type(32))) float f32x32;
  typedef __attribute__((ext_vector_type(8))) unsigned u32x8;
  f32x32 acc;
  {
    const char* A = p.ws + WS_HF + h_off(p, rblk);
    const char* B = (const char*)(p.wd + ct * 128);
    const unsigned rb = tid >> 4, va = tid * 16, vb = ((rb & 4) * 4 + (rb >> 3) * 4 + (rb & 3)) * (DM * 2) + 16 * ((tid & 15) ^ swzB(rb & 15));
    u32x8 lb;
#pragma unroll
    for (int i = 0; i < 8; ++i)
      lb[i] = (i >> 2) * 65536 + 16384 + offB(8 * q + 4 * (i & 1) + (r >> 2), 2 * (wc * 2 + ((i >> 1) & 1)) + ((r & 3) >> 1)) + 8 * (r & 1);
    LBAR();
#pragma unroll
    for (int t = 0; t < 3; ++t) {
      glds2<true>(A + t * 32768, 16384, va, wid * 1024 + t * 32768);
      glds2(B + t * 64 * DM * 2, 32 * DM * 2, vb, wid * 1024 + t * 32768 + 16384);
    }
    WAIT_V(8);
    LBAR();
    asm volatile(DN_ASM
        : "={v[0:31]}"(acc)
        : "{v[80:81]}"((u32x2){wr * 4096u + lane * 16u, 65536u + wr * 4096u + lane * 16u}), "{v[82:89]}"(lb), "{v90}"(va), "{v91}"(vb),
          [pa] "s"(A + 3 * 32768), [pb] "s"(B + 3 * 64 * DM * 2), [lw] "s"(wid * 1024), [hf] "s"(wr)
        : CLB8(32, 33, 34, 35, 36, 37, 38, 39), CLB8(40, 41, 42, 43, 44, 45, 46, 47), CLB8(48, 49, 50, 51, 52, 53, 54, 55),
          CLB8(56, 57, 58, 59, 60, 61, 62, 63), CLB8(64, 65, 66, 67, 68, 69, 70, 71), CLB8(72, 73, 74, 75, 76, 77, 78, 79),
          "s84", "s85", "s86", "s87", "s88", "s89", "s90", "s91", "s94", "scc", "memory");
  }
  wait_s(p, rsy, rblk, ct * 4, 4, rblk < NFUSE);
  const int e0 = (rblk * 128 + wr * 64 + r) * DM + ct * 128 + wc * 32 + q * 8;
  SX sx[4];
#pragma unroll
  for (int m = 0; m < 4; ++m) sx[m] = get_sx(p, rblk, e0 + m * 16 * DM);
  const __amdgpu_buffer_rsrc_t ro = mkrsrc(p.ws + WS_G, S_LEN * DM * 2);
#pragma unroll
  for (int m = 0; m < 4; ++m) {
    float rr[8];
#pragma unroll
    for (int j = 0; j < 8; ++j) { float d = rbf(acc[8 * m + j]); rr[j] = d * __builtin_amdgcn_rcpf(1.0f + __expf(-d)); }
    put_g(p, ro, rblk, e0 + m * 16 * DM, widen32(cvtpk(rr[0], rr[1]), cvtpk(rr[2], rr[3]), cvtpk(rr[4], rr[5]), cvtpk(rr[6], rr[7])), sx[m]);
  }
  if (rblk >= NFUSE) publish_flags(rsy, SY_FG + (rblk * 8 + ct) * 128, 0, 1);
}

#define PR_ASM_0 \
  "s_cmp_lg_u32 %[kh], 0\n\t" \
  "s_cbranch_scc1 .Lk%=\n\t" \
  "s_mov_b32 s94, m0\n\t" \
  "s_mov_b64 s[92:93], %[x0]\n\t" \
  "s_mov_b32 s86, %[g0]\n\t" \
  "s_add_u32 s87, s86, %[t0]\n\t" \
  "s_sub_i32 s88, s86, 3\n\t" \
  "ds_read_b128 v[176:179], v208 offset:0\n\t" \
  "ds_read_b128 v[180:183], v208 offset:16384\n\t" \
  "ds_read_b128 v[184:187], v209 offset:0\n\t" \
  "ds_read_b128 v[188:191], v209 offset:16384\n\t" \
  "ds_read_b128 v[192:195], v210 offset:0\n\t" \
  "ds_read_b128 v[196:199], v210 offset:16384\n\t" \
  "ds_read_b128 v[200:203], v211 offset:0\n\t" \
  "ds_read_b128 v[204:207], v211 offset:16384\n\t" \
  ".Lp0%=:\n\t" \
  "s_waitcnt lgkmcnt(6)\n\t" \
  "v_mfma_f32_16x16x32_bf16 v[128:131], v[0:3], v[176:179], 0\n\t" \
  "v_mfma_f32_16x16x32_bf16 v[132:135], v[4:7], v[176:179], 0\n\t" \
  "ds_read_b128 v[176:179], v208 offset:256\n\t" \
  "s_add_u32 m0, %[lw], 67584\n\t" \
  "v_mfma_f32_16x16x32_bf16 v[136:139], v[0:3], v[180:183], 0\n\t" \
  "v_mfma_f32_16x16x32_bf16 v[140:143], v[4:7], v[180:183], 0\n\t" \
  "ds_read_b128 v[180:183], v208 offset:16640\n\t" \
  "global_load_lds_dwordx4 v218, s[92:93] sc1\n\t" \
  "s_waitcnt lgkmcnt(6)\n\t" \
  "v_mfma_f32_16x16x32_bf16 v[128:131], v[8:11], v[184:187], v[128:131]\n\t" \
  "v_mfma_f32_16x16x32_bf16 v[132:135], v[12:15], v[184:187], v[132:135]\n\t" \
  "ds_read_b128 v[184:187], v209 offset:256\n\t" \
  "v_mfma_f32_16x16x32_bf16 v[136:139], v[8:11], v[188:191], v[136:139]\n\t" \
  "v_mfma_f32_16x16x32_bf16 v[140:143], v[12:15], v[188:191], v[140:143]\n\t" \
  "ds_read_b128 v[188:191], v209 offset:16640\n\t" \
  "ds_write_b128 v220, v[152:155] offset:0\n\t" \
  "ds_write_b128 v220, v[156:159] offset:1024\n\t" \
  "s_waitcnt lgkmcnt(8)\n\t" \
  "v_mfma_f32_16x16x32_bf16 v[128:131], v[16:19], v[192:195], v[128:131]\n\t" \
  "v_mfma_f32_16x16x32_bf16 v[132:135], v[20:23], v[192:195], v[132:135]\n\t" \
  "ds_read_b128 v[192:195], v210 offset:256\n\t" \
  "s_add_u32 m0, %[lw], 68608\n\t" \
  "v_mfma_f32_16x16x32_bf16 v[136:139], v[16:19], v[196:199], v[136:139]\n\t" \
  "v_mfma_f32_16x16x32_bf16 v[140:143], v[20:23], v[196:199], v[140:143]\n\t" \
  "ds_read_b128 v[196:199], v210 offset:16640\n\t" \
  "global_load_lds_dwordx4 v219, s[92:93] sc1\n\t" \
  "s_lshr_b32 s90, s87, 7\n\t" \
  "s_xor_b32 s90, s90, %[s0]\n\t" \
  "s_and_b32 s90, s90, 1\n\t" \
  "s_lshl1_add_u32 s90, s87, s90\n\t" \
  "s_lshl_b32 s90, s90, 13\n\t" \
  "s_cmp_lt_i32 s86, 3\n\t" \
  "s_cselect_b32 s90, 0x70000000, s90\n\t" \
  "s_waitcnt lgkmcnt(8)\n\t" \
  "v_mfma_f32_16x16x32_bf16 v[128:131], v[24:27], v[200:203], v[128:131]\n\t" \
  "v_mfma_f32_16x16x32_bf16 v[132:135], v[28:31], v[200:203], v[132:135]\n\t" \
  "ds_read_b128 v[200:203], v211 offset:256\n\t" \
  "v_mfma_f32_16x16x32_bf16 v[136:139], v[24:27], v[204:207], v[136:139]\n\t" \
  "v_mfma_f32_16x16x32_bf16 v[140:143], v[28:31], v[204:207], v[140:143]\n\t" \
  "ds_read_b128 v[204:207], v211 offset:16640\n\t" \
  "s_waitcnt lgkmcnt(4)\n\t" \
  "s_waitcnt vmcnt(4)\n\t" \
  "s_barrier\n\t" \
  ".Lg00%=:\n\t" \
  "s_add_u32 s92, s92, 512\n\t" \
  "s_addc_u32 s93, s93, 0\n\t" \
  "v_mfma_f32_16x16x32_bf16 v[128:131], v[32:35], v[176:179], v[128:131]\n\t" \
  "v_mfma_f32_16x16x32_bf16 v[132:135], v[36:39], v[176:179], v[132:135]\n\t" \
  "ds_read_b128 v[176:179], v208 offset:32768\n\t" \
  "s_add_u32 m0, %[lw], 98304\n\t" \
  "v_mfma_f32_16x16x32_bf16 v[136:139], v[32:35], v[180:183], v[136:139]\n\t" \
  "v_mfma_f32_16x16x32_bf16 v[140:143], v[36:39], v[180:183], v[140:143]\n\t" \
  "ds_read_b128 v[180:183], v208 offset:49152\n\t" \
  "global_load_lds_dwordx4 v216, s[92:93] sc1\n\t" \
  "ds_read_b128 v[160:163], v221 offset:0\n\t" \
  "ds_read_b128 v[164:167], v221 offset:1024\n\t" \
  "v_mfma_f32_16x16x32_bf16 v[128:131], v[40:43], v[184:187], v[128:131]\n\t" \
  "v_mfma_f32_16x16x32_bf16 v[132:135], v[44:47], v[184:187], v[132:135]\n\t" \
  "ds_read_b128 v[184:187], v209 offset:32768\n\t" \
  "v_mfma_f32_16x16x32_bf16 v[136:139], v[40:43], v[188:191], v[136:139]\n\t" \
  "v_mfma_f32_16x16x32_bf16 v[140:143], v[44:47], v[188:191], v[140:143]\n\t" \
  "ds_read_b128 v[188:191], v209 offset:49152\n\t" \
  "s_waitcnt lgkmcnt(8)\n\t" \
  "v_mfma_f32_16x16x32_bf16 v[128:131], v[48:51], v[192:195], v[128:131]\n\t" \
  "v_mfma_f32_16x16x32_bf16 v[132:135], v[52:55], v[192:195], v[132:135]\n\t" \
  "ds_read_b128 v[192:195], v210 offset:32768\n\t" \
  "s_add_u32 m0, %[lw], 99328\n\t" \
  "v_mfma_f32_16x16x32_bf16 v[136:139], v[48:51], v[196:199], v[136:139]\n\t" \
  "v_mfma_f32_16x16x32_bf16 v[140:143], v[52:55], v[196:199], v[140:143]\n\t" \
  "ds_read_b128 v[196:199], v210 offset:49152\n\t" \
  "global_load_lds_dwordx4 v217, s[92:93] sc1\n\t" \
  "s_waitcnt lgkmcnt(4)\n\t" \
  "v_add_f32 v160, v160, v144\n\t" \
  "v_add_f32 v161, v161, v145\n\t" \
  "v_add_f32 v162, v162, v146\n\t" \
  "v_add_f32 v163, v163, v147\n\t" \
  "v_add_f32 v164, v164, v148\n\t" \
  "v_add_f32 v165, v165, v149\n\t" \
  "v_add_f32 v166, v166, v150\n\t" \
  "v_add_f32 v167, v167, v151\n\t" \
  "v_mfma_f32_16x16x32_bf16 v[128:131], v[56:59], v[200:203], v[128:131]\n\t" \
  "v_mfma_f32_16x16x32_bf16 v[132:135], v[60:63], v[200:203], v[132:135]\n\t" \
  "ds_read_b128 v[200:203], v211 offset:32768\n\t" \
  "v_mfma_f32_16x16x32_bf16 v[136:139], v[56:59], v[204:207], v[136:139]\n\t" \
  "v_mfma_f32_16x16x32_bf16 v[140:143], v[60:63], v[204:207], v[140:143]\n\t" \
  "ds_read_b128 v[204:207], v211 offset:49152\n\t" \
  "v_cvt_pk_bf16_f32 v168, v160, v161\n\t" \
  "v_cvt_pk_bf16_f32 v169, v162, v163\n\t" \
  "v_cvt_pk_bf16_f32 v170, v164, v165\n\t" \
  "v_cvt_pk_bf16_f32 v171, v166, v167\n\t" \
  "buffer_store_dwordx4 v[168:171], v172, %[rt], s90 offen sc1\n\t" \
  "v_mfma_f32_16x16x32_bf16 v[128:131], v[64:67], v[176:179], v[128:131]\n\t" \
  "v_mfma_f32_16x16x32_bf16 v[132:135], v[68:71], v[176:179], v[132:135]\n\t" \
  "ds_read_b128 v[176:179], v208 offset:33024\n\t" \
  "s_add_u32 m0, %[lw], 100352\n\t" \
  "v_mfma_f32_16x16x32_bf16 v[136:139], v[64:67], v[180:183], v[136:139]\n\t" \
  "v_mfma_f32_16x16x32_bf16 v[140:143], v[68:71], v[180:183], v[140:143]\n\t" \
  "ds_read_b128 v[180:183], v208 offset:49408\n\t" \
  "global_load_lds_dwordx4 v218, s[92:93] sc1\n\t" \
  "s_waitcnt lgkmcnt(6)\n\t" \
  "v_mfma_f32_16x16x32_bf16 v[128:131], v[72:75], v[184:187], v[128:131]\n\t" \
  "v_mfma_f32_16x16x32_bf16 v[132:135], v[76:79], v[184:187], v[132:135]\n\t" \
  "ds_read_b128 v[184:187], v209 offset:33024\n\t" \
  "v_mfma_f32_16x16x32_bf16 v[136:139], v[72:75], v[188:191], v[136:139]\n\t" \
  "v_mfma_f32_16x16x32_bf16 v[140:143], v[76:79], v[188:191], v[140:143]\n\t" \
  "ds_read_b128 v[188:191], v209 offset:49408\n\t" \
  "s_waitcnt lgkmcnt(6)\n\t" \
  "v_mfma_f32_16x16x32_bf16 v[128:131], v[80:83], v[192:195], v[128:131]\n\t" \
  "v_mfma_f32_16x16x32_bf16 v[132:135], v[84:87], v[192:195], v[132:135]\n\t" \
  "ds_read_b128 v[192:195], v210 offset:33024\n\t" \
  "s_add_u32 m0, %[lw], 101376\n\t" \
  "v_mfma_f32_16x16x32_bf16 v[136:139], v[80:83], v[196:199], v[136:139]\n\t" \
  "v_mfma_f32_16x16x32_bf16 v[140:143], v[84:87], v[196:199], v[140:143]\n\t" \
  "ds_read_b128 v[196:199], v210 offset:49408\n\t" \
  "global_load_lds_dwordx4 v219, s[92:93] sc1\n\t" \
  "s_waitcnt lgkmcnt(6)\n\t" \
  "v_mfma_f32_16x16x32_bf16 v[128:131], v[88:91], v[200:203], v[128:131]\n\t" \
  "v_mfma_f32_16x16x32_bf16 v[132:135], v[92:95], v[200:203], v[132:135]\n\t" \
  "ds_read_b128 v[200:203], v211 offset:33024\n\t" \
  "v_mfma_f32_16x16x32_bf16 v[136:139], v[88:91], v[204:207], v[136:139]\n\t" \
  "v_mfma_f32_16x16x32_bf16 v[140:143], v[92:95], v[204:207], v[140:143]\n\t" \
  "ds_read_b128 v[204:207], v211 offset:49408\n\t" \
  "s_cmp_lt_u32 s86, 6\n\t" \
  "s_cbranch_scc1 .Lw10%=\n\t" \
  "s_waitcnt vmcnt(5)\n\t" \
  "s_barrier\n\t" \
  ".Lg10%=:\n\t" \
  "s_add_u32 s92, s92, 65024\n\t" \
  "s_addc_u32 s93, s93, 0\n\t" \
  "s_waitcnt lgkmcnt(6)\n\t" \
  "v_mfma_f32_16x16x32_bf16 v[128:131], v[96:99], v[176:179], v[128:131]\n\t" \
  "v_mfma_f32_16x16x32_bf16 v[132:135], v[100:103], v[176:179], v[132:135]\n\t" \
  "ds_read_b128 v[176:179], v212 offset:0\n\t" \
  "s_add_u32 m0, %[lw], 0\n\t" \
  "v_mfma_f32_16x16x32_bf16 v[136:139], v[96:99], v[180:183], v[136:139]\n\t" \
  "v_mfma_f32_16x16x32_bf16 v[140:143], v[100:103], v[180:183], v[140:143]\n\t" \
  "ds_read_b128 v[180:183], v212 offset:16384\n\t" \
  "global_load_lds_dwordx4 v216, s[92:93] sc1\n\t" \
  "s_waitcnt lgkmcnt(6)\n\t" \
  "v_mfma_f32_16x16x32_bf16 v[128:131], v[104:107], v[184:187], v[128:131]\n\t" \
  "v_mfma_f32_16x16x32_bf16 v[132:135], v[108:111], v[184:187], v[132:135]\n\t" \
  "ds_read_b128 v[184:187], v213 offset:0\n\t" \
  "v_mfma_f32_16x16x32_bf16 v[136:139], v[104:107], v[188:191], v[136:139]\n\t" \
  "v_mfma_f32_16x16x32_bf16 v[140:143], v[108:111], v[188:191], v[140:143]\n\t" \
  "ds_read_b128 v[188:191], v213 offset:16384\n\t" \
  "s_waitcnt lgkmcnt(6)\n\t" \
  "v_mfma_f32_16x16x32_bf16 v[128:131], v[112:115], v[192:195], v[128:131]\n\t" \
  "v_mfma_f32_16x16x32_bf16 v[132:135], v[116:119], v[192:195], v[132:135]\n\t" \
  "ds_read_b128 v[192:195], v214 offset:0\n\t" \
  "s_add_u32 m0, %[lw], 1024\n\t" \
  "v_mfma_f32_16x16x32_bf16 v[136:139], v[112:115], v[196:199], v[136:139]\n\t" \
  "v_mfma_f32_16x16x32_bf16 v[140:143], v[116:119], v[196:199], v[140:143]\n\t" \
  "ds_read_b128 v[196:199], v214 offset:16384\n\t" \
  "global_load_lds_dwordx4 v217, s[92:93] sc1\n\t" \
  "s_waitcnt lgkmcnt(6)\n\t" \
  "v_mfma_f32_16x16x32_bf16 v[128:131], v[120:123], v[200:203], v[128:131]\n\t" \
  "v_mfma_f32_16x16x32_bf16 v[132:135], v[124:127], v[200:203], v[132:135]\n\t" \
  "ds_read_b128 v[200:203], v215 offset:0\n\t" \
  "v_mfma_f32_16x16x32_bf16 v[136:139], v[120:123], v[204:207], v[136:139]\n\t" \
  "v_mfma_f32_16x16x32_bf16 v[140:143], v[124:127], v[204:207], v[140:143]\n\t" \
  "ds_read_b128 v[204:207], v215 offset:16384\n\t" \
  "s_add_u32 s86, s86, 1\n\t" \
  "s_add_u32 s87, s87, 1\n\t" \
  "s_cmp_eq_u32 s86, 1\n\t" \
  "s_cbranch_scc1 .Lf0%=\n\t" \
  ".Lfb0%=:\n\t" \
  "s_waitcnt lgkmcnt(6)\n\t" \
  "v_mfma_f32_16x16x32_bf16 v[144:147], v[0:3], v[176:179], 0\n\t" \
  "v_mfma_f32_16x16x32_bf16 v[148:151], v[4:7], v[176:179], 0\n\t" \
  "ds_read_b128 v[176:179], v212 offset:256\n\t" \
  "s_add_u32 m0, %[lw], 2048\n\t" \
  "v_mfma_f32_16x16x32_bf16 v[152:155], v[0:3], v[180:183], 0\n\t" \
  "v_mfma_f32_16x16x32_bf16 v[156:159], v[4:7], v[180:183], 0\n\t" \
  "ds_read_b128 v[180:183], v212 offset:16640\n\t" \
  "global_load_lds_dwordx4 v218, s[92:93] sc1\n\t" \
  "s_waitcnt lgkmcnt(6)\n\t" \
  "v_mfma_f32_16x16x32_bf16 v[144:147], v[8:11], v[184:187], v[144:147]\n\t" \
  "v_mfma_f32_16x16x32_bf16 v[148:151], v[12:15], v[184:187], v[148:151]\n\t" \
  "ds_read_b128 v[184:187], v213 offset:256\n\t" \
  "v_mfma_f32_16x16x32_bf16 v[152:155], v[8:11], v[188:191], v[152:155]\n\t" \
  "v_mfma_f32_16x16x32_bf16 v[156:159], v[12:15], v[188:191], v[156:159]\n\t" \
  "ds_read_b128 v[188:191], v213 offset:16640\n\t" \
  "ds_write_b128 v220, v[136:139] offset:0\n\t" \
  "ds_write_b128 v220, v[140:143] offset:1024\n\t" \
  "s_waitcnt lgkmcnt(8)\n\t" \
  "v_mfma_f32_16x16x32_bf16 v[144:147], v[16:19], v[192:195], v[144:147]\n\t" \
  "v_mfma_f32_16x16x32_bf16 v[148:151], v[20:23], v[192:195], v[148:151]\n\t" \
  "ds_read_b128 v[192:195], v214 offset:256\n\t" \
  "s_add_u32 m0, %[lw], 3072\n\t" \
  "v_mfma_f32_16x16x32_bf16 v[152:155], v[16:19], v[196:199], v[152:155]\n\t" \
  "v_mfma_f32_16x16x32_bf16 v[156:159], v[20:23], v[196:199], v[156:159]\n\t" \
  "ds_read_b128 v[196:199], v214 offset:16640\n\t" \
  "global_load_lds_dwordx4 v219, s[92:93] sc1\n\t" \
  "s_lshr_b32 s90, s87, 7\n\t" \
  "s_xor_b32 s90, s90, %[s0]\n\t" \
  "s_and_b32 s90, s90, 1\n\t" \
  "s_lshl1_add_u32 s90, s87, s90\n\t" \
  "s_lshl_b32 s90, s90, 13\n\t" \
  "s_cmp_lt_i32 s86, 3\n\t" \
  "s_cselect_b32 s90, 0x70000000, s90\n\t" \
  "s_waitcnt lgkmcnt(8)\n\t" \
  "v_mfma_f32_16x16x32_bf16 v[144:147], v[24:27], v[200:203], v[144:147]\n\t" \
  "v_mfma_f32_16x16x32_bf16 v[148:151], v[28:31], v[200:203], v[148:151]\n\t" \
  "ds_read_b128 v[200:203], v215 offset:256\n\t" \
  "v_mfma_f32_16x16x32_bf16 v[152:155], v[24:27], v[204:207], v[152:155]\n\t" \
  "v_mfma_f32_16x16x32_bf16 v[156:159], v[28:31], v[204:207], v[156:159]\n\t" \
  "ds_read_b128 v[204:207], v215 offset:16640\n\t" \
  "s_waitcnt lgkmcnt(4)\n\t" \
  "s_waitcnt vmcnt(4)\n\t" \
  "s_barrier\n\t" \
  ".Lg20%=:\n\t" \
  "s_add_u32 s92, s92, 512\n\t" \
  "s_addc_u32 s93, s93, 0\n\t" \
  "v_mfma_f32_16x16x32_bf16 v[144:147], v[32:35], v[176:179], v[144:147]\n\t" \
  "v_mfma_f32_16x16x32_bf16 v[148:151], v[36:39], v[176:179], v[148:151]\n\t" \
  "ds_read_b128 v[176:179], v212 offset:32768\n\t" \
  "s_add_u32 m0, %[lw], 32768\n\t" \
  "v_mfma_f32_16x16x32_bf16 v[152:155], v[32:35], v[180:183], v[152:155]\n\t" \
  "v_mfma_f32_16x16x32_bf16 v[156:159], v[36:39], v[180:183], v[156:159]\n\t" \
  "ds_read_b128 v[180:183], v212 offset:49152\n\t" \
  "global_load_lds_dwordx4 v216, s[92:93] sc1\n\t" \
  "ds_read_b128 v[160:163], v221 offset:0\n\t" \
  "ds_read_b128 v[164:167], v221 offset:1024\n\t" \
  "v_mfma_f32_16x16x32_bf16 v[144:147], v[40:43], v[184:187], v[144:147]\n\t" \
  "v_mfma_f32_16x16x32_bf16 v[148:151], v[44:47], v[184:187], v[148:151]\n\t" \
  "ds_read_b128 v[184:187], v213 offset:32768\n\t" \
  "v_mfma_f32_16x16x32_bf16 v[152:155], v[40:43], v[188:191], v[152:155]\n\t" \
  "v_mfma_f32_16x16x32_bf16 v[156:159], v[44:47], v[188:191], v[156:159]\n\t" \
  "ds_read_b128 v[188:191], v213 offset:49152\n\t" \
  "s_waitcnt lgkmcnt(8)\n\t" \
  "v_mfma_f32_16x16x32_bf16 v[144:147], v[48:51], v[192:195], v[144:147]\n\t" \
  "v_mfma_f32_16x16x32_bf16 v[148:151], v[52:55], v[192:195], v[148:151]\n\t" \
  "ds_read_b128 v[192:195], v214 offset:32768\n\t" \
  "s_add_u32 m0, %[lw], 33792\n\t" \
  "v_mfma_f32_16x16x32_bf16 v[152:155], v[48:51], v[196:199], v[152:155]\n\t" \
  "v_mfma_f32_16x16x32_bf16 v[156:159], v[52:55], v[196:199], v[156:159]\n\t" \
  "ds_read_b128 v[196:199], v214 offset:49152\n\t" \
  "global_load_lds_dwordx4 v217, s[92:93] sc1\n\t" \
  "s_waitcnt lgkmcnt(4)\n\t" \
  "v_add_f32 v160, v160, v128\n\t" \
  "v_add_f32 v161, v161, v129\n\t" \
  "v_add_f32 v162, v162, v130\n\t" \
  "v_add_f32 v163, v163, v131\n\t" \
  "v_add_f32 v164, v164, v132\n\t" \
  "v_add_f32 v165, v165, v133\n\t" \
  "v_add_f32 v166, v166, v134\n\t" \
  "v_add_f32 v167, v167, v135\n\t" \
  "v_mfma_f32_16x16x32_bf16 v[144:147], v[56:59], v[200:203], v[144:147]\n\t" \
  "v_mfma_f32_16x16x32_bf16 v[148:151], v[60:63], v[200:203], v[148:151]\n\t" \
  "ds_read_b128 v[200:203], v215 offset:32768\n\t" \
  "v_mfma_f32_16x16x32_bf16 v[152:155], v[56:59], v[204:207], v[152:155]\n\t" \
  "v_mfma_f32_16x16x32_bf16 v[156:159], v[60:63], v[204:207], v[156:159]\n\t" \
  "ds_read_b128 v[204:207], v215 offset:49152\n\t" \
  "v_cvt_pk_bf16_f32 v168, v160, v161\n\t" \
  "v_cvt_pk_bf16_f32 v169, v162, v163\n\t" \
  "v_cvt_pk_bf16_f32 v170, v164, v165\n\t" \
  "v_cvt_pk_bf16_f32 v171, v166, v167\n\t" \
  "buffer_store_dwordx4 v[168:171], v172, %[rt], s90 offen sc1\n\t" \
  "v_mfma_f32_16x16x32_bf16 v[144:147], v[64:67], v[176:179], v[144:147]\n\t" \
  "v_mfma_f32_16x16x32_bf16 v[148:151], v[68:71], v[176:179], v[148:151]\n\t" \
  "ds_read_b128 v[176:179], v212 offset:33024\n\t" \
  "s_add_u32 m0, %[lw], 34816\n\t" \
  "v_mfma_f32_16x16x32_bf16 v[152:155], v[64:67], v[180:183], v[152:155]\n\t" \
  "v_mfma_f32_16x16x32_bf16 v[156:159], v[68:71], v[180:183], v[156:159]\n\t" \
  "ds_read_b128 v[180:183], v212 offset:49408\n\t" \
  "global_load_lds_dwordx4 v218, s[92:93] sc1\n\t" \
  "s_waitcnt lgkmcnt(6)\n\t" \
  "v_mfma_f32_16x16x32_bf16 v[144:147], v[72:75], v[184:187], v[144:147]\n\t" \
  "v_mfma_f32_16x16x32_bf16 v[148:151], v[76:79], v[184:187], v[148:151]\n\t" \
  "ds_read_b128 v[184:187], v213 offset:33024\n\t" \
  "v_mfma_f32_16x16x32_bf16 v[152:155], v[72:75], v[188:191], v[152:155]\n\t" \
  "v_mfma_f32_16x16x32_bf16 v[156:159], v[76:79], v[188:191], v[156:159]\n\t" \
  "ds_read_b128 v[188:191], v213 offset:49408\n\t" \
  "s_waitcnt lgkmcnt(6)\n\t" \
  "v_mfma_f32_16x16x32_bf16 v[144:147], v[80:83], v[192:195], v[144:147]\n\t" \
  "v_mfma_f32_16x16x32_bf16 v[148:151], v[84:87], v[192:195], v[148:151]\n\t" \
  "ds_read_b128 v[192:195], v214 offset:33024\n\t" \
  "s_add_u32 m0, %[lw], 35840\n\t" \
  "v_mfma_f32_16x16x32_bf16 v[152:155], v[80:83], v[196:199], v[152:155]\n\t" \
  "v_mfma_f32_16x16x32_bf16 v[156:159], v[84:87], v[196:199], v[156:159]\n\t" \
  "ds_read_b128 v[196:199], v214 offset:49408\n\t" \
  "global_load_lds_dwordx4 v219, s[92:93] sc1\n\t" \
  "s_waitcnt lgkmcnt(6)\n\t" \
  "v_mfma_f32_16x16x32_bf16 v[144:147], v[88:91], v[200:203], v[144:147]\n\t" \
  "v_mfma_f32_16x16x32_bf16 v[148:151], v[92:95], v[200:203], v[148:151]\n\t" \
  "ds_read_b128 v[200:203], v215 offset:33024\n\t" \
  "v_mfma_f32_16x16x32_bf16 v[152:155], v[88:91], v[204:207], v[152:155]\n\t" \
  "v_mfma_f32_16x16x32_bf16 v[156:159], v[92:95], v[204:207], v[156:159]\n\t" \
  "ds_read_b128 v[204:207], v215 offset:49408\n\t" \
  "s_cmp_lt_u32 s86, 6\n\t" \
  "s_cbranch_scc1 .Lw30%=\n\t" \
  "s_waitcnt vmcnt(5)\n\t" \
  "s_barrier\n\t" \
  ".Lg30%=:\n\t" \
  "s_add_u32 s92, s92, 65024\n\t" \
  "s_addc_u32 s93, s93, 0\n\t" \
  "s_waitcnt lgkmcnt(6)\n\t" \
  "v_mfma_f32_16x16x32_bf16 v[144:147], v[96:99], v[176:179], v[144:147]\n\t" \
  "v_mfma_f32_16x16x32_bf16 v[148:151], v[100:103], v[176:179], v[148:151]\n\t" \
  "ds_read_b128 v[176:179], v208 offset:0\n\t" \
  "s_add_u32 m0, %[lw], 65536\n\t" \
  "v_mfma_f32_16x16x32_bf16 v[152:155], v[96:99], v[180:183], v[152:155]\n\t" \
  "v_mfma_f32_16x16x32_bf16 v[156:159], v[100:103], v[180:183], v[156:159]\n\t" \
  "ds_read_b128 v[180:183], v208 offset:16384\n\t" \
  "global_load_lds_dwordx4 v216, s[92:93] sc1\n\t" \
  "s_waitcnt lgkmcnt(6)\n\t" \
  "v_mfma_f32_16x16x32_bf16 v[144:147], v[104:107], v[184:187], v[144:147]\n\t" \
  "v_mfma_f32_16x16x32_bf16 v[148:151], v[108:111], v[184:187], v[148:151]\n\t" \
  "ds_read_b128 v[184:187], v209 offset:0\n\t" \
  "v_mfma_f32_16x16x32_bf16 v[152:155], v[104:107], v[188:191], v[152:155]\n\t" \
  "v_mfma_f32_16x16x32_bf16 v[156:159], v[108:111], v[188:191], v[156:159]\n\t" \
  "ds_read_b128 v[188:191], v209 offset:16384\n\t" \
  "s_waitcnt lgkmcnt(6)\n\t" \
  "v_mfma_f32_16x16x32_bf16 v[144:147], v[112:115], v[192:195], v[144:147]\n\t" \
  "v_mfma_f32_16x16x32_bf16 v[148:151], v[116:119], v[192:195], v[148:151]\n\t" \
  "ds_read_b128 v[192:195], v210 offset:0\n\t" \
  "s_add_u32 m0, %[lw], 66560\n\t" \
  "v_mfma_f32_16x16x32_bf16 v[152:155], v[112:115], v[196:199], v[152:155]\n\t" \
  "v_mfma_f32_16x16x32_bf16 v[156:159], v[116:119], v[196:199], v[156:159]\n\t" \
  "ds_read_b128 v[196:199], v210 offset:16384\n\t" \
  "global_load_lds_dwordx4 v217, s[92:93] sc1\n\t" \
  "s_waitcnt lgkmcnt(6)\n\t" \
  "v_mfma_f32_16x16x32_bf16 v[144:147], v[120:123], v[200:203], v[144:147]\n\t" \
  "v_mfma_f32_16x16x32_bf16 v[148:151], v[124:127], v[200:203], v[148:151]\n\t" \
  "ds_read_b128 v[200:203], v211 offset:0\n\t" \
  "v_mfma_f32_16x16x32_bf16 v[152:155], v[120:123], v[204:207], v[152:155]\n\t" \
  "v_mfma_f32_16x16x32_bf16 v[156:159], v[124:127], v[204:207], v[156:159]\n\t" \
  "ds_read_b128 v[204:207], v211 offset:16384\n\t" \
  "s_add_u32 s86, s86, 1\n\t" \
  "s_add_u32 s87, s87, 1\n\t" \
  "s_cmp_eq_u32 s86, 2\n\t" \
  "s_cbranch_scc1 .Ll0%=\n\t" \
  "s_cmp_lt_u32 s86, %[end]\n\t" \
  "s_cbranch_scc1 .Lp0%=\n\t" \
  "s_cmp_lg_u32 s86, 64\n\t" \
  "s_cbranch_scc1 .Lx0%=\n\t" \
  ".Ll0%=:\n\t" \
  "s_nop 7\n\t" \
  "ds_write_b128 v220, v[152:155] offset:0\n\t" \
  "ds_write_b128 v220, v[156:159] offset:1024\n\t" \
  "s_lshr_b32 s90, s87, 7\n\t" \
  "s_xor_b32 s90, s90, %[s0]\n\t" \
  "s_and_b32 s90, s90, 1\n\t" \
  "s_lshl1_add_u32 s90, s87, s90\n\t" \
  "s_lshl_b32 s90, s90, 13\n\t" \
  "s_cmp_lt_i32 s86, 1\n\t" \
  "s_cselect_b32 s90, 0x70000000, s90\n\t" \
  "s_waitcnt lgkmcnt(0)\n\t" \
  "s_barrier\n\t" \
  "ds_read_b128 v[160:163], v221 offset:0\n\t" \
  "ds_read_b128 v[164:167], v221 offset:1024\n\t" \
  "s_waitcnt lgkmcnt(0)\n\t" \
  "v_add_f32 v160, v160, v144\n\t" \
  "v_add_f32 v161, v161, v145\n\t" \
  "v_add_f32 v162, v162, v146\n\t" \
  "v_add_f32 v163, v163, v147\n\t" \
  "v_add_f32 v164, v164, v148\n\t" \
  "v_add_f32 v165, v165, v149\n\t" \
  "v_add_f32 v166, v166, v150\n\t" \
  "v_add_f32 v167, v167, v151\n\t" \
  "v_cvt_pk_bf16_f32 v168, v160, v161\n\t" \
  "v_cvt_pk_bf16_f32 v169, v162, v163\n\t" \
  "v_cvt_pk_bf16_f32 v170, v164, v165\n\t" \
  "v_cvt_pk_bf16_f32 v171, v166, v167\n\t" \
  "buffer_store_dwordx4 v[168:171], v172, %[rt], s90 offen sc1\n\t" \
  "s_waitcnt vmcnt(0)\n\t" \
  "s_barrier\n\t" \
  "s_cmp_eq_u32 s86, 2\n\t" \
  "s_cbranch_scc1 .Lp0%=\n\t" \
  "s_branch .Lx0%=\n\t" \
  ".Lf0%=:\n\t" \
  "s_nop 7\n\t" \
  "ds_write_b128 v220, v[136:139] offset:0\n\t" \
  "ds_write_b128 v220, v[140:143] offset:1024\n\t" \
  "s_lshr_b32 s90, s87, 7\n\t" \
  "s_xor_b32 s90, s90, %[s0]\n\t" \
  "s_and_b32 s90, s90, 1\n\t" \
  "s_lshl1_add_u32 s90, s87, s90\n\t" \
  "s_lshl_b32 s90, s90, 13\n\t" \
  "s_cmp_lt_i32 s86, 1\n\t" \
  "s_cselect_b32 s90, 0x70000000, s90\n\t" \
  "s_waitcnt lgkmcnt(0)\n\t" \
  "s_barrier\n\t" \
  "ds_read_b128 v[160:163], v221 offset:0\n\t" \
  "ds_read_b128 v[164:167], v221 offset:1024\n\t" \
  "s_waitcnt lgkmcnt(0)\n\t" \
  "v_add_f32 v160, v160, v128\n\t" \
  "v_add_f32 v161, v161, v129\n\t" \
  "v_add_f32 v162, v162, v130\n\t" \
  "v_add_f32 v163, v163, v131\n\t" \
  "v_add_f32 v164, v164, v132\n\t" \
  "v_add_f32 v165, v165, v133\n\t" \
  "v_add_f32 v166, v166, v134\n\t" \
  "v_add_f32 v167, v167, v135\n\t" \
  "v_cvt_pk_bf16_f32 v168, v160, v161\n\t" \
  "v_cvt_pk_bf16_f32 v169, v162, v163\n\t" \
  "v_cvt_pk_bf16_f32 v170, v164, v165\n\t" \
  "v_cvt_pk_bf16_f32 v171, v166, v167\n\t" \
  "buffer_store_dwordx4 v[168:171], v172, %[rt], s90 offen sc1\n\t" \
  "s_waitcnt vmcnt(0)\n\t" \
  "s_barrier\n\t" \
  "s_branch .Lfb0%=\n\t" \
  ".Lw10%=:\n\t" \
  "s_waitcnt vmcnt(2)\n\t" \
  "s_barrier\n\t" \
  "s_branch .Lg10%=\n\t" \
  ".Lw30%=:\n\t" \
  "s_waitcnt vmcnt(2)\n\t" \
  "s_barrier\n\t" \
  "s_branch .Lg30%=\n\t" \
  ".Lx0%=:\n\t" \
  "s_waitcnt lgkmcnt(0)\n\t" \
  "s_setprio 0\n\t" \
  "s_mov_b32 m0, s94\n\t" \
  "s_branch .Le%=\n\t" \
  ".Lk%=:\n\t" \
  "s_mov_b32 s94, m0\n\t" \
  "s_mov_b64 s[92:93], %[x0]\n\t" \
  "s_mov_b32 s86, %[g0]\n\t" \
  "s_add_u32 s87, s86, %[t0]\n\t" \
  "s_sub_i32 s88, s86, 3\n\t" \
  "ds_read_b128 v[176:179], v208 offset:0\n\t" \
  "ds_read_b128 v[180:183], v208 offset:16384\n\t" \
  "ds_read_b128 v[184:187], v209 offset:0\n\t" \
  "ds_read_b128 v[188:191], v209 offset:16384\n\t" \
  "ds_read_b128 v[192:195], v210 offset:0\n\t" \
  "ds_read_b128 v[196:199], v210 offset:16384\n\t" \
  "ds_read_b128 v[200:203], v211 offset:0\n\t" \
  "ds_read_b128 v[204:207], v211 offset:16384\n\t" \
  ".Lp1%=:\n\t" \
  "s_waitcnt lgkmcnt(6)\n\t" \
  "v_mfma_f32_16x16x32_bf16 v[128:131], v[0:3], v[176:179], 0\n\t" \
  "v_mfma_f32_16x16x32_bf16 v[132:135], v[4:7], v[176:179], 0\n\t" \
  "ds_read_b128 v[176:179], v208 offset:256\n\t" \
  "v_mfma_f32_16x16x32_bf16 v[136:139], v[0:3], v[180:183], 0\n\t" \
  "v_mfma_f32_16x16x32_bf16 v[140:143], v[4:7], v[180:183], 0\n\t" \
  "ds_read_b128 v[180:183], v208 offset:16640\n\t" \
  "s_waitcnt lgkmcnt(6)\n\t" \
  "v_mfma_f32_16x16x32_bf16 v[128:131], v[8:11], v[184:187], v[128:131]\n\t" \
  "v_mfma_f32_16x16x32_bf16 v[132:135], v[12:15], v[184:187], v[132:135]\n\t" \
  "ds_read_b128 v[184:187], v209 offset:256\n\t" \
  "s_add_u32 m0, %[lw], 67584\n\t" \
  "v_mfma_f32_16x16x32_bf16 v[136:139], v[8:11], v[188:191], v[136:139]\n\t" \
  "v_mfma_f32_16x16x32_bf16 v[140:143], v[12:15], v[188:191], v[140:143]\n\t" \
  "ds_read_b128 v[188:191], v209 offset:16640\n\t" \
  "global_load_lds_dwordx4 v218, s[92:93] sc1\n\t" \
  "ds_write_b128 v220, v[152:155] offset:0\n\t" \
  "ds_write_b128 v220, v[156:159] offset:1024\n\t" \
  "s_waitcnt lgkmcnt(8)\n\t" \
  "v_mfma_f32_16x16x32_bf16 v[128:131], v[16:19], v[192:195], v[128:131]\n\t" \
  "v_mfma_f32_16x16x32_bf16 v[132:135], v[20:23], v[192:195], v[132:135]\n\t" \
  "ds_read_b128 v[192:195], v210 offset:256\n\t" \
  "v_mfma_f32_16x16x32_bf16 v[136:139], v[16:19], v[196:199], v[136:139]\n\t" \
  "v_mfma_f32_16x16x32_bf16 v[140:143], v[20:23], v[196:199], v[140:143]\n\t" \
  "ds_read_b128 v[196:199], v210 offset:16640\n\t" \
  "s_lshr_b32 s90, s87, 7\n\t" \
  "s_xor_b32 s90, s90, %[s0]\n\t" \
  "s_and_b32 s90, s90, 1\n\t" \
  "s_lshl1_add_u32 s90, s87, s90\n\t" \
  "s_lshl_b32 s90, s90, 13\n\t" \
  "s_cmp_lt_i32 s86, 3\n\t" \
  "s_cselect_b32 s90, 0x70000000, s90\n\t" \
  "s_waitcnt lgkmcnt(8)\n\t" \
  "v_mfma_f32_16x16x32_bf16 v[128:131], v[24:27], v[200:203], v[128:131]\n\t" \
  "v_mfma_f32_16x16x32_bf16 v[132:135], v[28:31], v[200:203], v[132:135]\n\t" \
  "ds_read_b128 v[200:203], v211 offset:256\n\t" \
  "s_add_u32 m0, %[lw], 68608\n\t" \
  "v_mfma_f32_16x16x32_bf16 v[136:139], v[24:27], v[204:207], v[136:139]\n\t" \
  "v_mfma_f32_16x16x32_bf16 v[140:143], v[28:31], v[204:207], v[140:143]\n\t" \
  "ds_read_b128 v[204:207], v211 offset:16640\n\t" \
  "global_load_lds_dwordx4 v219, s[92:93] sc1\n\t" \
  "s_waitcnt lgkmcnt(4)\n\t" \
  "s_waitcnt vmcnt(4)\n\t" \
  "s_barrier\n\t" \
  ".Lg01%=:\n\t" \
  "s_add_u32 s92, s92, 512\n\t" \
  "s_addc_u32 s93, s93, 0\n\t" \
  "v_mfma_f32_16x16x32_bf16 v[128:131], v[32:35], v[176:179], v[128:131]\n\t" \
  "v_mfma_f32_16x16x32_bf16 v[132:135], v[36:39], v[176:179], v[132:135]\n\t" \
  "ds_read_b128 v[176:179], v208 offset:32768\n\t" \
  "v_mfma_f32_16x16x32_bf16 v[136:139], v[32:35], v[180:183], v[136:139]\n\t" \
  "v_mfma_f32_16x16x32_bf16 v[140:143], v[36:39], v[180:183], v[140:143]\n\t" \
  "ds_read_b128 v[180:183], v208 offset:49152\n\t" \
  "ds_read_b128 v[160:163], v221 offset:0\n\t" \
  "ds_read_b128 v[164:167], v221 offset:1024\n\t" \
  "s_sub_i32 s91, s86, 2\n\t" \
  "s_max_i32 s88, s88, s91\n\t" \
  "v_mov_b32 v173, s88\n\t" \
  "buffer_store_dword v173, v174, %[rs], 0 offen sc1\n\t" \
  "v_mfma_f32_16x16x32_bf16 v[128:131], v[40:43], v[184:187], v[128:131]\n\t" \
  "v_mfma_f32_16x16x32_bf16 v[132:135], v[44:47], v[184:187], v[132:135]\n\t" \
  "ds_read_b128 v[184:187], v209 offset:32768\n\t" \
  "s_add_u32 m0, %[lw], 98304\n\t" \
  "v_mfma_f32_16x16x32_bf16 v[136:139], v[40:43], v[188:191], v[136:139]\n\t" \
  "v_mfma_f32_16x16x32_bf16 v[140:143], v[44:47], v[188:191], v[140:143]\n\t" \
  "ds_read_b128 v[188:191], v209 offset:49152\n\t" \
  "global_load_lds_dwordx4 v216, s[92:93] sc1\n\t" \
  "s_waitcnt lgkmcnt(8)\n\t" \
  "v_mfma_f32_16x16x32_bf16 v[128:131], v[48:51], v[192:195], v[128:131]\n\t" \
  "v_mfma_f32_16x16x32_bf16 v[132:135], v[52:55], v[192:195], v[132:135]\n\t" \
  "ds_read_b128 v[192:195], v210 offset:32768\n\t" \
  "v_mfma_f32_16x16x32_bf16 v[136:139], v[48:51], v[196:199], v[136:139]\n\t" \
  "v_mfma_f32_16x16x32_bf16 v[140:143], v[52:55], v[196:199], v[140:143]\n\t" \
  "ds_read_b128 v[196:199], v210 offset:49152\n\t" \
  "s_waitcnt lgkmcnt(4)\n\t" \
  "v_add_f32 v160, v160, v144\n\t" \
  "v_add_f32 v161, v161, v145\n\t" \
  "v_add_f32 v162, v162, v146\n\t" \
  "v_add_f32 v163, v163, v147\n\t" \
  "v_add_f32 v164, v164, v148\n\t" \
  "v_add_f32 v165, v165, v149\n\t" \
  "v_add_f32 v166, v166, v150\n\t" \
  "v_add_f32 v167, v167, v151\n\t" \
  "v_mfma_f32_16x16x32_bf16 v[128:131], v[56:59], v[200:203], v[128:131]\n\t" \
  "v_mfma_f32_16x16x32_bf16 v[132:135], v[60:63], v[200:203], v[132:135]\n\t" \
  "ds_read_b128 v[200:203], v211 offset:32768\n\t" \
  "s_add_u32 m0, %[lw], 99328\n\t" \
  "v_mfma_f32_16x16x32_bf16 v[136:139], v[56:59], v[204:207], v[136:139]\n\t" \
  "v_mfma_f32_16x16x32_bf16 v[140:143], v[60:63], v[204:207], v[140:143]\n\t" \
  "ds_read_b128 v[204:207], v211 offset:49152\n\t" \
  "global_load_lds_dwordx4 v217, s[92:93] sc1\n\t" \
  "v_cvt_pk_bf16_f32 v168, v160, v161\n\t" \
  "v_cvt_pk_bf16_f32 v169, v162, v163\n\t" \
  "v_cvt_pk_bf16_f32 v170, v164, v165\n\t" \
  "v_cvt_pk_bf16_f32 v171, v166, v167\n\t" \
  "buffer_store_dwordx4 v[168:171], v172, %[rt], s90 offen sc1\n\t" \
  "v_mfma_f32_16x16x32_bf16 v[128:131], v[64:67], v[176:179], v[128:131]\n\t" \
  "v_mfma_f32_16x16x32_bf16 v[132:135], v[68:71], v[176:179], v[132:135]\n\t" \
  "ds_read_b128 v[176:179], v208 offset:33024\n\t" \
  "v_mfma_f32_16x16x32_bf16 v[136:139], v[64:67], v[180:183], v[136:139]\n\t" \
  "v_mfma_f32_16x16x32_bf16 v[140:143], v[68:71], v[180:183], v[140:143]\n\t" \
  "ds_read_b128 v[180:183], v208 offset:49408\n\t" \
  "s_waitcnt lgkmcnt(6)\n\t" \
  "v_mfma_f32_16x16x32_bf16 v[128:131], v[72:75], v[184:187], v[128:131]\n\t" \
  "v_mfma_f32_16x16x32_bf16 v[132:135], v[76:79], v[184:187], v[132:135]\n\t" \
  "ds_read_b128 v[184:187], v209 offset:33024\n\t" \
  "s_add_u32 m0, %[lw], 100352\n\t" \
  "v_mfma_f32_16x16x32_bf16 v[136:139], v[72:75], v[188:191], v[136:139]\n\t" \
  "v_mfma_f32_16x16x32_bf16 v[140:143], v[76:79], v[188:191], v[140:143]\n\t" \
  "ds_read_b128 v[188:191], v209 offset:49408\n\t" \
  "global_load_lds_dwordx4 v218, s[92:93] sc1\n\t" \
  "s_waitcnt lgkmcnt(6)\n\t" \
  "v_mfma_f32_16x16x32_bf16 v[128:131], v[80:83], v[192:195], v[128:131]\n\t" \
  "v_mfma_f32_16x16x32_bf16 v[132:135], v[84:87], v[192:195], v[132:135]\n\t" \
  "ds_read_b128 v[192:195], v210 offset:33024\n\t" \
  "v_mfma_f32_16x16x32_bf16 v[136:139], v[80:83], v[196:199], v[136:139]\n\t" \
  "v_mfma_f32_16x16x32_bf16 v[140:143], v[84:87], v[196:199], v[140:143]\n\t" \
  "ds_read_b128 v[196:199], v210 offset:49408\n\t" \
  "s_waitcnt lgkmcnt(6)\n\t" \
  "v_mfma_f32_16x16x32_bf16 v[128:131], v[88:91], v[200:203], v[128:131]\n\t" \
  "v_mfma_f32_16x16x32_bf16 v[132:135], v[92:95], v[200:203], v[132:135]\n\t" \
  "ds_read_b128 v[200:203], v211 offset:33024\n\t" \
  "s_add_u32 m0, %[lw], 101376\n\t" \
  "v_mfma_f32_16x16x32_bf16 v[136:139], v[88:91], v[204:207], v[136:139]\n\t" \
  "v_mfma_f32_16x16x32_bf16 v[140:143], v[92:95], v[204:207], v[140:143]\n\t" \
  "ds_read_b128 v[204:207], v211 offset:49408\n\t" \
  "global_load_lds_dwordx4 v219, s[92:93] sc1\n\t" \
  "s_cmp_lt_u32 s86, 6\n\t" \
  "s_cbranch_scc1 .Lw11%=\n\t" \
  "s_waitcnt vmcnt(6)\n\t" \
  "s_barrier\n\t" \
  ".Lg11%=:\n\t" \
  "s_add_u32 s92, s92, 65024\n\t" \
  "s_addc_u32 s93, s93, 0\n\t" \
  "s_waitcnt lgkmcnt(6)\n\t" \
  "v_mfma_f32_16x16x32_bf16 v[128:131], v[96:99], v[176:179], v[128:131]\n\t" \
  "v_mfma_f32_16x16x32_bf16 v[132:135], v[100:103], v[176:179], v[132:135]\n\t" \
  "ds_read_b128 v[176:179], v212 offset:0\n\t" \
  "v_mfma_f32_16x16x32_bf16 v[136:139], v[96:99], v[180:183], v[136:139]\n\t" \
  "v_mfma_f32_16x16x32_bf16 v[140:143], v[100:103], v[180:183], v[140:143]\n\t" \
  "ds_read_b128 v[180:183], v212 offset:16384\n\t" \
  "s_waitcnt lgkmcnt(6)\n\t" \
  "v_mfma_f32_16x16x32_bf16 v[128:131], v[104:107], v[184:187], v[128:131]\n\t" \
  "v_mfma_f32_16x16x32_bf16 v[132:135], v[108:111], v[184:187], v[132:135]\n\t" \
  "ds_read_b128 v[184:187], v213 offset:0\n\t" \
  "s_add_u32 m0, %[lw], 0\n\t" \
  "v_mfma_f32_16x16x32_bf16 v[136:139], v[104:107], v[188:191], v[136:139]\n\t" \
  "v_mfma_f32_16x16x32_bf16 v[140:143], v[108:111], v[188:191], v[140:143]\n\t" \
  "ds_read_b128 v[188:191], v213 offset:16384\n\t" \
  "global_load_lds_dwordx4 v216, s[92:93] sc1\n\t" \
  "s_waitcnt lgkmcnt(6)\n\t" \
  "v_mfma_f32_16x16x32_bf16 v[128:131], v[112:115], v[192:195], v[128:131]\n\t" \
  "v_mfma_f32_16x16x32_bf16 v[132:135], v[116:119], v[192:195], v[132:135]\n\t" \
  "ds_read_b128 v[192:195], v214 offset:0\n\t" \
  "v_mfma_f32_16x16x32_bf16 v[136:139], v[112:115], v[196:199], v[136:139]\n\t" \
  "v_mfma_f32_16x16x32_bf16 v[140:143], v[116:119], v[196:199], v[140:143]\n\t" \
  "ds_read_b128 v[196:199], v214 offset:16384\n\t" \
  "s_waitcnt lgkmcnt(6)\n\t" \
  "v_mfma_f32_16x16x32_bf16 v[128:131], v[120:123], v[200:203], v[128:131]\n\t" \
  "v_mfma_f32_16x16x32_bf16 v[132:135], v[124:127], v[200:203], v[132:135]\n\t" \
  "ds_read_b128 v[200:203], v215 offset:0\n\t" \
  "s_add_u32 m0, %[lw], 1024\n\t" \
  "v_mfma_f32_16x16x32_bf16 v[136:139], v[120:123], v[204:207], v[136:139]\n\t" \
  "v_mfma_f32_16x16x32_bf16 v[140:143], v[124:127], v[204:207], v[140:143]\n\t" \
  "ds_read_b128 v[204:207], v215 offset:16384\n\t" \
  "global_load_lds_dwordx4 v217, s[92:93] sc1\n\t" \
  "s_add_u32 s86, s86, 1\n\t" \
  "s_add_u32 s87, s87, 1\n\t" \
  "s_cmp_eq_u32 s86, 1\n\t" \
  "s_cbranch_scc1 .Lf1%=\n\t" \
  ".Lfb1%=:\n\t" \
  "s_waitcnt lgkmcnt(6)\n\t" \
  "v_mfma_f32_16x16x32_bf16 v[144:147], v[0:3], v[176:179], 0\n\t" \
  "v_mfma_f32_16x16x32_bf16 v[148:151], v[4:7], v[176:179], 0\n\t" \
  "ds_read_b128 v[176:179], v212 offset:256\n\t" \
  "v_mfma_f32_16x16x32_bf16 v[152:155], v[0:3], v[180:183], 0\n\t" \
  "v_mfma_f32_16x16x32_bf16 v[156:159], v[4:7], v[180:183], 0\n\t" \
  "ds_read_b128 v[180:183], v212 offset:16640\n\t" \
  "s_waitcnt lgkmcnt(6)\n\t" \
  "v_mfma_f32_16x16x32_bf16 v[144:147], v[8:11], v[184:187], v[144:147]\n\t" \
  "v_mfma_f32_16x16x32_bf16 v[148:151], v[12:15], v[184:187], v[148:151]\n\t" \
  "ds_read_b128 v[184:187], v213 offset:256\n\t" \
  "s_add_u32 m0, %[lw], 2048\n\t" \
  "v_mfma_f32_16x16x32_bf16 v[152:155], v[8:11], v[188:191], v[152:155]\n\t" \
  "v_mfma_f32_16x16x32_bf16 v[156:159], v[12:15], v[188:191], v[156:159]\n\t" \
  "ds_read_b128 v[188:191], v213 offset:16640\n\t" \
  "global_load_lds_dwordx4 v218, s[92:93] sc1\n\t" \
  "ds_write_b128 v220, v[136:139] offset:0\n\t" \
  "ds_write_b128 v220, v[140:143] offset:1024\n\t" \
  "s_waitcnt lgkmcnt(8)\n\t" \
  "v_mfma_f32_16x16x32_bf16 v[144:147], v[16:19], v[192:195], v[144:147]\n\t" \
  "v_mfma_f32_16x16x32_bf16 v[148:151], v[20:23], v[192:195], v[148:151]\n\t" \
  "ds_read_b128 v[192:195], v214 offset:256\n\t" \
  "v_mfma_f32_16x16x32_bf16 v[152:155], v[16:19], v[196:199], v[152:155]\n\t" \
  "v_mfma_f32_16x16x32_bf16 v[156:159], v[20:23], v[196:199], v[156:159]\n\t" \
  "ds_read_b128 v[196:199], v214 offset:16640\n\t" \
  "s_lshr_b32 s90, s87, 7\n\t" \
  "s_xor_b32 s90, s90, %[s0]\n\t" \
  "s_and_b32 s90, s90, 1\n\t" \
  "s_lshl1_add_u32 s90, s87, s90\n\t" \
  "s_lshl_b32 s90, s90, 13\n\t" \
  "s_cmp_lt_i32 s86, 3\n\t" \
  "s_cselect_b32 s90, 0x70000000, s90\n\t" \
  "s_waitcnt lgkmcnt(8)\n\t" \
  "v_mfma_f32_16x16x32_bf16 v[144:147], v[24:27], v[200:203], v[144:147]\n\t" \
  "v_mfma_f32_16x16x32_bf16 v[148:151], v[28:31], v[200:203], v[148:151]\n\t" \
  "ds_read_b128 v[200:203], v215 offset:256\n\t" \
  "s_add_u32 m0, %[lw], 3072\n\t" \
  "v_mfma_f32_16x16x32_bf16 v[152:155], v[24:27], v[204:207], v[152:155]\n\t" \
  "v_mfma_f32_16x16x32_bf16 v[156:159], v[28:31], v[204:207], v[156:159]\n\t" \
  "ds_read_b128 v[204:207], v215 offset:16640\n\t" \
  "global_load_lds_dwordx4 v219, s[92:93] sc1\n\t" \
  "s_waitcnt lgkmcnt(4)\n\t" \
  "s_waitcnt vmcnt(4)\n\t" \
  "s_barrier\n\t" \
  ".Lg21%=:\n\t" \
  "s_add_u32 s92, s92, 512\n\t" \
  "s_addc_u32 s93, s93, 0\n\t" \
  "v_mfma_f32_16x16x32_bf16 v[144:147], v[32:35], v[176:179], v[144:147]\n\t" \
  "v_mfma_f32_16x16x32_bf16 v[148:151], v[36:39], v[176:179], v[148:151]\n\t" \
  "ds_read_b128 v[176:179], v212 offset:32768\n\t" \
  "v_mfma_f32_16x16x32_bf16 v[152:155], v[32:35], v[180:183], v[152:155]\n\t" \
  "v_mfma_f32_16x16x32_bf16 v[156:159], v[36:39], v[180:183], v[156:159]\n\t" \
  "ds_read_b128 v[180:183], v212 offset:49152\n\t" \
  "ds_read_b128 v[160:163], v221 offset:0\n\t" \
  "ds_read_b128 v[164:167], v221 offset:1024\n\t" \
  "s_sub_i32 s91, s86, 2\n\t" \
  "s_max_i32 s88, s88, s91\n\t" \
  "v_mov_b32 v173, s88\n\t" \
  "buffer_store_dword v173, v174, %[rs], 0 offen sc1\n\t" \
  "v_mfma_f32_16x16x32_bf16 v[144:147], v[40:43], v[184:187], v[144:147]\n\t" \
  "v_mfma_f32_16x16x32_bf16 v[148:151], v[44:47], v[184:187], v[148:151]\n\t" \
  "ds_read_b128 v[184:187], v213 offset:32768\n\t" \
  "s_add_u32 m0, %[lw], 32768\n\t" \
  "v_mfma_f32_16x16x32_bf16 v[152:155], v[40:43], v[188:191], v[152:155]\n\t" \
  "v_mfma_f32_16x16x32_bf16 v[156:159], v[44:47], v[188:191], v[156:159]\n\t" \
  "ds_read_b128 v[188:191], v213 offset:49152\n\t" \
  "global_load_lds_dwordx4 v216, s[92:93] sc1\n\t" \
  "s_waitcnt lgkmcnt(8)\n\t" \
  "v_mfma_f32_16x16x32_bf16 v[144:147], v[48:51], v[192:195], v[144:147]\n\t" \
  "v_mfma_f32_16x16x32_bf16 v[148:151], v[52:55], v[192:195], v[148:151]\n\t" \
  "ds_read_b128 v[192:195], v214 offset:32768\n\t" \
  "v_mfma_f32_16x16x32_bf16 v[152:155], v[48:51], v[196:199], v[152:155]\n\t" \
  "v_mfma_f32_16x16x32_bf16 v[156:159], v[52:55], v[196:199], v[156:159]\n\t" \
  "ds_read_b128 v[196:199], v214 offset:49152\n\t" \
  "s_waitcnt lgkmcnt(4)\n\t" \
  "v_add_f32 v160, v160, v128\n\t" \
  "v_add_f32 v161, v161, v129\n\t" \
  "v_add_f32 v162, v162, v130\n\t" \
  "v_add_f32 v163, v163, v131\n\t" \
  "v_add_f32 v164, v164, v132\n\t" \
  "v_add_f32 v165, v165, v133\n\t" \
  "v_add_f32 v166, v166, v134\n\t" \
  "v_add_f32 v167, v167, v135\n\t" \
  "v_mfma_f32_16x16x32_bf16 v[144:147], v[56:59], v[200:203], v[144:147]\n\t" \
  "v_mfma_f32_16x16x32_bf16 v[148:151], v[60:63], v[200:203], v[148:151]\n\t" \
  "ds_read_b128 v[200:203], v215 offset:32768\n\t" \
  "s_add_u32 m0, %[lw], 33792\n\t" \
  "v_mfma_f32_16x16x32_bf16 v[152:155], v[56:59], v[204:207], v[152:155]\n\t" \
  "v_mfma_f32_16x16x32_bf16 v[156:159], v[60:63], v[204:207], v[156:159]\n\t" \
  "ds_read_b128 v[204:207], v215 offset:49152\n\t" \
  "global_load_lds_dwordx4 v217, s[92:93] sc1\n\t" \
  "v_cvt_pk_bf16_f32 v168, v160, v161\n\t" \
  "v_cvt_pk_bf16_f32 v169, v162, v163\n\t" \
  "v_cvt_pk_bf16_f32 v170, v164, v165\n\t" \
  "v_cvt_pk_bf16_f32 v171, v166, v167\n\t" \
  "buffer_store_dwordx4 v[168:171], v172, %[rt], s90 offen sc1\n\t" \
  "v_mfma_f32_16x16x32_bf16 v[144:147], v[64:67], v[176:179], v[144:147]\n\t" \
  "v_mfma_f32_16x16x32_bf16 v[148:151], v[68:71], v[176:179], v[148:151]\n\t" \
  "ds_read_b128 v[176:179], v212 offset:33024\n\t" \
  "v_mfma_f32_16x16x32_bf16 v[152:155], v[64:67], v[180:183], v[152:155]\n\t" \
  "v_mfma_f32_16x16x32_bf16 v[156:159], v[68:71], v[180:183], v[156:159]\n\t" \
  "ds_read_b128 v[180:183], v212 offset:49408\n\t" \
  "s_waitcnt lgkmcnt(6)\n\t" \
  "v_mfma_f32_16x16x32_bf16 v[144:147], v[72:75], v[184:187], v[144:147]\n\t" \
  "v_mfma_f32_16x16x32_bf16 v[148:151], v[76:79], v[184:187], v[148:151]\n\t" \
  "ds_read_b128 v[184:187], v213 offset:33024\n\t" \
  "s_add_u32 m0, %[lw], 34816\n\t" \
  "v_mfma_f32_16x16x32_bf16 v[152:155], v[72:75], v[188:191], v[152:155]\n\t" \
  "v_mfma_f32_16x16x32_bf16 v[156:159], v[76:79], v[188:191], v[156:159]\n\t" \
  "ds_read_b128 v[188:191], v213 offset:49408\n\t" \
  "global_load_lds_dwordx4 v218, s[92:93] sc1\n\t" \
  "s_waitcnt lgkmcnt(6)\n\t" \
  "v_mfma_f32_16x16x32_bf16 v[144:147], v[80:83], v[192:195], v[144:147]\n\t" \
  "v_mfma_f32_16x16x32_bf16 v[148:151], v[84:87], v[192:195], v[148:151]\n\t" \
  "ds_read_b128 v[192:195], v214 offset:33024\n\t" \
  "v_mfma_f32_16x16x32_bf16 v[152:155], v[80:83], v[196:199], v[152:155]\n\t" \
  "v_mfma_f32_16x16x32_bf16 v[156:159], v[84:87], v[196:199], v[156:159]\n\t" \
  "ds_read_b128 v[196:199], v214 offset:49408\n\t" \
  "s_waitcnt lgkmcnt(6)\n\t" \
  "v_mfma_f32_16x16x32_bf16 v[144:147], v[88:91], v[200:203], v[144:147]\n\t" \
  "v_mfma_f32_16x16x32_bf16 v[148:151], v[92:95], v[200:203], v[148:151]\n\t" \
  "ds_read_b128 v[200:203], v215 offset:33024\n\t" \
  "s_add_u32 m0, %[lw], 35840\n\t" \
  "v_mfma_f32_16x16x32_bf16 v[152:155], v[88:91], v[204:207], v[152:155]\n\t" \
  "v_mfma_f32_16x16x32_bf16 v[156:159], v[92:95], v[204:207], v[156:159]\n\t" \
  "ds_read_b128 v[204:207], v215 offset:49408\n\t" \
  "global_load_lds_dwordx4 v219, s[92:93] sc1\n\t" \
  "s_cmp_lt_u32 s86, 6\n\t" \
  "s_cbranch_scc1 .Lw31%=\n\t" \
  "s_waitcnt vmcnt(6)\n\t" \
  "s_barrier\n\t" \
  ".Lg31%=:\n\t" \
  "s_add_u32 s92, s92, 65024\n\t" \
  "s_addc_u32 s93, s93, 0\n\t" \
  "s_waitcnt lgkmcnt(6)\n\t" \
  "v_mfma_f32_16x16x32_bf16 v[144:147], v[96:99], v[176:179], v[144:147]\n\t" \
  "v_mfma_f32_16x16x32_bf16 v[148:151], v[100:103], v[176:179], v[148:151]\n\t" \
  "ds_read_b128 v[176:179], v208 offset:0\n\t" \
  "v_mfma_f32_16x16x32_bf16 v[152:155], v[96:99], v[180:183], v[152:155]\n\t" \
  "v_mfma_f32_16x16x32_bf16 v[156:159], v[100:103], v[180:183], v[156:159]\n\t" \
  "ds_read_b128 v[180:183], v208 offset:16384\n\t" \
  "s_waitcnt lgkmcnt(6)\n\t" \
  "v_mfma_f32_16x16x32_bf16 v[144:147], v[104:107], v[184:187], v[144:147]\n\t" \
  "v_mfma_f32_16x16x32_bf16 v[148:151], v[108:111], v[184:187], v[148:151]\n\t" \
  "ds_read_b128 v[184:187], v209 offset:0\n\t" \
  "s_add_u32 m0, %[lw], 65536\n\t" \
  "v_mfma_f32_16x16x32_bf16 v[152:155], v[104:107], v[188:191], v[152:155]\n\t" \
  "v_mfma_f32_16x16x32_bf16 v[156:159], v[108:111], v[188:191], v[156:159]\n\t" \
  "ds_read_b128 v[188:191], v209 offset:16384\n\t" \
  "global_load_lds_dwordx4 v216, s[92:93] sc1\n\t" \
  "s_waitcnt lgkmcnt(6)\n\t" \
  "v_mfma_f32_16x16x32_bf16 v[144:147], v[112:115], v[192:195], v[144:147]\n\t" \
  "v_mfma_f32_16x16x32_bf16 v[148:151], v[116:119], v[192:195], v[148:151]\n\t" \
  "ds_read_b128 v[192:195], v210 offset:0\n\t" \
  "v_mfma_f32_16x16x32_bf16 v[152:155], v[112:115], v[196:199], v[152:155]\n\t" \
  "v_mfma_f32_16x16x32_bf16 v[156:159], v[116:119], v[196:199], v[156:159]\n\t" \
  "ds_read_b128 v[196:199], v210 offset:16384\n\t" \
  "s_waitcnt lgkmcnt(6)\n\t" \
  "v_mfma_f32_16x16x32_bf16 v[144:147], v[120:123], v[200:203], v[144:147]\n\t" \
  "v_mfma_f32_16x16x32_bf16 v[148:151], v[124:127], v[200:203], v[148:151]\n\t" \
  "ds_read_b128 v[200:203], v211 offset:0\n\t" \
  "s_add_u32 m0, %[lw], 66560\n\t" \
  "v_mfma_f32_16x16x32_bf16 v[152:155], v[120:123], v[204:207], v[152:155]\n\t" \
  "v_mfma_f32_16x16x32_bf16 v[156:159], v[124:127], v[204:207], v[156:159]\n\t" \
  "ds_read_b128 v[204:207], v211 offset:16384\n\t" \
  "global_load_lds_dwordx4 v217, s[92:93] sc1\n\t" \
  "s_add_u32 s86, s86, 1\n\t" \
  "s_add_u32 s87, s87, 1\n\t" \
  "s_cmp_eq_u32 s86, 2\n\t" \
  "s_cbranch_scc1 .Ll1%=\n\t" \
  "s_cmp_lt_u32 s86, %[end]\n\t" \
  "s_cbranch_scc1 .Lp1%=\n\t" \
  "s_cmp_lg_u32 s86, 64\n\t" \
  "s_cbranch_scc1 .Lx1%=\n\t" \
  ".Ll1%=:\n\t" \
  "s_nop 7\n\t" \
  "ds_write_b128 v220, v[152:155] offset:0\n\t" \
  "ds_write_b128 v220, v[156:159] offset:1024\n\t" \
  "s_lshr_b32 s90, s87, 7\n\t" \
  "s_xor_b32 s90, s90, %[s0]\n\t" \
  "s_and_b32 s90, s90, 1\n\t" \
  "s_lshl1_add_u32 s90, s87, s90\n\t" \
  "s_lshl_b32 s90, s90, 13\n\t" \
  "s_cmp_lt_i32 s86, 1\n\t" \
  "s_cselect_b32 s90, 0x70000000, s90\n\t" \
  "s_waitcnt lgkmcnt(0)\n\t" \
  "s_barrier\n\t" \
  "ds_read_b128 v[160:163], v221 offset:0\n\t" \
  "ds_read_b128 v[164:167], v221 offset:1024\n\t" \
  "s_waitcnt lgkmcnt(0)\n\t" \
  "v_add_f32 v160, v160, v144\n\t" \
  "v_add_f32 v161, v161, v145\n\t" \
  "v_add_f32 v162, v162, v146\n\t" \
  "v_add_f32 v163, v163, v147\n\t" \
  "v_add_f32 v164, v164, v148\n\t" \
  "v_add_f32 v165, v165, v149\n\t" \
  "v_add_f32 v166, v166, v150\n\t" \
  "v_add_f32 v167, v167, v151\n\t" \
  "v_cvt_pk_bf16_f32 v168, v160, v161\n\t" \
  "v_cvt_pk_bf16_f32 v169, v162, v163\n\t" \
  "v_cvt_pk_bf16_f32 v170, v164, v165\n\t" \
  "v_cvt_pk_bf16_f32 v171, v166, v167\n\t" \
  "buffer_store_dwordx4 v[168:171], v172, %[rt], s90 offen sc1\n\t" \
  "s_waitcnt vmcnt(0)\n\t" \
  "s_barrier\n\t" \
  "s_sub_i32 s91, s86, 1\n\t" \
  "s_max_i32 s88, s88, s91\n\t" \
  "v_mov_b32 v173, s88\n\t" \
  "buffer_store_dword v173, v174, %[rs], 0 offen sc1\n\t" \
  "s_cmp_eq_u32 s86, 2\n\t" \
  "s_cbranch_scc1 .Lp1%=\n\t" \
  "s_branch .Lx1%=\n\t" \
  ".Lf1%=:\n\t" \
  "s_nop 7\n\t" \
  "ds_write_b128 v220, v[136:139] offset:0\n\t" \
  "ds_write_b128 v220, v[140:143] offset:1024\n\t" \
  "s_lshr_b32 s90, s87, 7\n\t" \
  "s_xor_b32 s90, s90, %[s0]\n\t" \
  "s_and_b32 s90, s90, 1\n\t" \
  "s_lshl1_add_u32 s90, s87, s90\n\t" \
  "s_lshl_b32 s90, s90, 13\n\t" \
  "s_cmp_lt_i32 s86, 1\n\t" \
  "s_cselect_b32 s90, 0x70000000, s90\n\t" \
  "s_waitcnt lgkmcnt(0)\n\t" \
  "s_barrier\n\t" \
  "ds_read_b128 v[160:163], v221 offset:0\n\t" \
  "ds_read_b128 v[164:167], v221 offset:1024\n\t" \
  "s_waitcnt lgkmcnt(0)\n\t" \
  "v_add_f32 v160, v160, v128\n\t" \
  "v_add_f32 v161, v161, v129\n\t" \
  "v_add_f32 v162, v162, v130\n\t" \
  "v_add_f32 v163, v163, v131\n\t" \
  "v_add_f32 v164, v164, v132\n\t" \
  "v_add_f32 v165, v165, v133\n\t" \
  "v_add_f32 v166, v166, v134\n\t" \
  "v_add_f32 v167, v167, v135\n\t" \
  "v_cvt_pk_bf16_f32 v168, v160, v161\n\t" \
  "v_cvt_pk_bf16_f32 v169, v162, v163\n\t" \
  "v_cvt_pk_bf16_f32 v170, v164, v165\n\t" \
  "v_cvt_pk_bf16_f32 v171, v166, v167\n\t" \
  "buffer_store_dwordx4 v[168:171], v172, %[rt], s90 offen sc1\n\t" \
  "s_waitcnt vmcnt(0)\n\t" \
  "s_barrier\n\t" \
  "s_sub_i32 s91, s86, 1\n\t" \
  "s_max_i32 s88, s88, s91\n\t" \
  "v_mov_b32 v173, s88\n\t" \
  "buffer_store_dword v173, v174, %[rs], 0 offen sc1\n\t" \
  "s_branch .Lfb1%=\n\t" \
  ".Lw11%=:\n\t" \
  "s_waitcnt vmcnt(2)\n\t" \
  "s_barrier\n\t" \
  "s_sub_i32 s91, s86, 1\n\t" \
  "s_max_i32 s88, s88, s91\n\t" \
  "v_mov_b32 v173, s88\n\t" \
  "buffer_store_dword v173, v174, %[rs], 0 offen sc1\n\t" \
  "s_branch .Lg11%=\n\t" \
  ".Lw31%=:\n\t" \
  "s_waitcnt vmcnt(2)\n\t" \
  "s_barrier\n\t" \
  "s_sub_i32 s91, s86, 1\n\t" \
  "s_max_i32 s88, s88, s91\n\t" \
  "v_mov_b32 v173, s88\n\t" \
  "buffer_store_dword v173, v174, %[rs], 0 offen sc1\n\t" \
  "s_branch .Lg31%=\n\t" \
  ".Lx1%=:\n\t" \
  "s_waitcnt lgkmcnt(0)\n\t" \
  "s_setprio 0\n\t" \
  "s_mov_b32 m0, s94\n\t" \
  ".Le%=:\n\t"
#define PR_ASM_1 \
  "s_cmp_lg_u32 %[kh], 0\n\t" \
  "s_cbranch_scc1 .Lk%=\n\t" \
  "s_mov_b32 s94, m0\n\t" \
  "s_mov_b64 s[92:93], %[x0]\n\t" \
  "s_mov_b32 s86, %[g0]\n\t" \
  "s_add_u32 s87, s86, %[t0]\n\t" \
  "s_sub_i32 s88, s86, 3\n\t" \
  "ds_read_b128 v[176:179], v208 offset:0\n\t" \
  "ds_read_b128 v[180:183], v208 offset:16384\n\t" \
  "ds_read_b128 v[184:187], v209 offset:0\n\t" \
  "ds_read_b128 v[188:191], v209 offset:16384\n\t" \
  "ds_read_b128 v[192:195], v210 offset:0\n\t" \
  "ds_read_b128 v[196:199], v210 offset:16384\n\t" \
  "ds_read_b128 v[200:203], v211 offset:0\n\t" \
  "ds_read_b128 v[204:207], v211 offset:16384\n\t" \
  ".Lp0%=:\n\t" \
  "s_waitcnt lgkmcnt(6)\n\t" \
  "v_mfma_f32_16x16x32_bf16 v[128:131], v[176:179], v[0:3], 0\n\t" \
  "v_mfma_f32_16x16x32_bf16 v[136:139], v[176:179], v[4:7], 0\n\t" \
  "ds_read_b128 v[176:179], v208 offset:256\n\t" \
  "s_add_u32 m0, %[lw], 67584\n\t" \
  "v_mfma_f32_16x16x32_bf16 v[132:135], v[180:183], v[0:3], 0\n\t" \
  "v_mfma_f32_16x16x32_bf16 v[140:143], v[180:183], v[4:7], 0\n\t" \
  "ds_read_b128 v[180:183], v208 offset:16640\n\t" \
  "global_load_lds_dwordx4 v218, s[92:93] sc1\n\t" \
  "s_waitcnt lgkmcnt(6)\n\t" \
  "v_mfma_f32_16x16x32_bf16 v[128:131], v[184:187], v[8:11], v[128:131]\n\t" \
  "v_mfma_f32_16x16x32_bf16 v[136:139], v[184:187], v[12:15], v[136:139]\n\t" \
  "ds_read_b128 v[184:187], v209 offset:256\n\t" \
  "v_mfma_f32_16x16x32_bf16 v[132:135], v[188:191], v[8:11], v[132:135]\n\t" \
  "v_mfma_f32_16x16x32_bf16 v[140:143], v[188:191], v[12:15], v[140:143]\n\t" \
  "ds_read_b128 v[188:191], v209 offset:16640\n\t" \
  "ds_write_b128 v220, v[152:155] offset:0\n\t" \
  "ds_write_b128 v220, v[156:159] offset:1024\n\t" \
  "s_waitcnt lgkmcnt(8)\n\t" \
  "v_mfma_f32_16x16x32_bf16 v[128:131], v[192:195], v[16:19], v[128:131]\n\t" \
  "v_mfma_f32_16x16x32_bf16 v[136:139], v[192:195], v[20:23], v[136:139]\n\t" \
  "ds_read_b128 v[192:195], v210 offset:256\n\t" \
  "s_add_u32 m0, %[lw], 68608\n\t" \
  "v_mfma_f32_16x16x32_bf16 v[132:135], v[196:199], v[16:19], v[132:135]\n\t" \
  "v_mfma_f32_16x16x32_bf16 v[140:143], v[196:199], v[20:23], v[140:143]\n\t" \
  "ds_read_b128 v[196:199], v210 offset:16640\n\t" \
  "global_load_lds_dwordx4 v219, s[92:93] sc1\n\t" \
  "s_lshr_b32 s90, s87, 7\n\t" \
  "s_xor_b32 s90, s90, %[s0]\n\t" \
  "s_and_b32 s90, s90, 1\n\t" \
  "s_lshl1_add_u32 s90, s87, s90\n\t" \
  "s_lshl_b32 s90, s90, 13\n\t" \
  "s_cmp_lt_i32 s86, 3\n\t" \
  "s_cselect_b32 s90, 0x70000000, s90\n\t" \
  "s_waitcnt lgkmcnt(8)\n\t" \
  "v_mfma_f32_16x16x32_bf16 v[128:131], v[200:203], v[24:27], v[128:131]\n\t" \
  "v_mfma_f32_16x16x32_bf16 v[136:139], v[200:203], v[28:31], v[136:139]\n\t" \
  "ds_read_b128 v[200:203], v211 offset:256\n\t" \
  "v_mfma_f32_16x16x32_bf16 v[132:135], v[204:207], v[24:27], v[132:135]\n\t" \
  "v_mfma_f32_16x16x32_bf16 v[140:143], v[204:207], v[28:31], v[140:143]\n\t" \
  "ds_read_b128 v[204:207], v211 offset:16640\n\t" \
  "s_waitcnt lgkmcnt(4)\n\t" \
  "s_waitcnt vmcnt(4)\n\t" \
  "s_barrier\n\t" \
  ".Lg00%=:\n\t" \
  "s_add_u32 s92, s92, 512\n\t" \
  "s_addc_u32 s93, s93, 0\n\t" \
  "v_mfma_f32_16x16x32_bf16 v[128:131], v[176:179], v[32:35], v[128:131]\n\t" \
  "v_mfma_f32_16x16x32_bf16 v[136:139], v[176:179], v[36:39], v[136:139]\n\t" \
  "ds_read_b128 v[176:179], v208 offset:32768\n\t" \
  "s_add_u32 m0, %[lw], 98304\n\t" \
  "v_mfma_f32_16x16x32_bf16 v[132:135], v[180:183], v[32:35], v[132:135]\n\t" \
  "v_mfma_f32_16x16x32_bf16 v[140:143], v[180:183], v[36:39], v[140:143]\n\t" \
  "ds_read_b128 v[180:183], v208 offset:49152\n\t" \
  "global_load_lds_dwordx4 v216, s[92:93] sc1\n\t" \
  "ds_read_b128 v[160:163], v221 offset:0\n\t" \
  "ds_read_b128 v[164:167], v221 offset:1024\n\t" \
  "v_mfma_f32_16x16x32_bf16 v[128:131], v[184:187], v[40:43], v[128:131]\n\t" \
  "v_mfma_f32_16x16x32_bf16 v[136:139], v[184:187], v[44:47], v[136:139]\n\t" \
  "ds_read_b128 v[184:187], v209 offset:32768\n\t" \
  "v_mfma_f32_16x16x32_bf16 v[132:135], v[188:191], v[40:43], v[132:135]\n\t" \
  "v_mfma_f32_16x16x32_bf16 v[140:143], v[188:191], v[44:47], v[140:143]\n\t" \
  "ds_read_b128 v[188:191], v209 offset:49152\n\t" \
  "s_waitcnt lgkmcnt(8)\n\t" \
  "v_mfma_f32_16x16x32_bf16 v[128:131], v[192:195], v[48:51], v[128:131]\n\t" \
  "v_mfma_f32_16x16x32_bf16 v[136:139], v[192:195], v[52:55], v[136:139]\n\t" \
  "ds_read_b128 v[192:195], v210 offset:32768\n\t" \
  "s_add_u32 m0, %[lw], 99328\n\t" \
  "v_mfma_f32_16x16x32_bf16 v[132:135], v[196:199], v[48:51], v[132:135]\n\t" \
  "v_mfma_f32_16x16x32_bf16 v[140:143], v[196:199], v[52:55], v[140:143]\n\t" \
  "ds_read_b128 v[196:199], v210 offset:49152\n\t" \
  "global_load_lds_dwordx4 v217, s[92:93] sc1\n\t" \
  "s_waitcnt lgkmcnt(4)\n\t" \
  "v_add_f32 v160, v160, v144\n\t" \
  "v_add_f32 v161, v161, v145\n\t" \
  "v_add_f32 v162, v162, v146\n\t" \
  "v_add_f32 v163, v163, v147\n\t" \
  "v_add_f32 v164, v164, v148\n\t" \
  "v_add_f32 v165, v165, v149\n\t" \
  "v_add_f32 v166, v166, v150\n\t" \
  "v_add_f32 v167, v167, v151\n\t" \
  "v_mfma_f32_16x16x32_bf16 v[128:131], v[200:203], v[56:59], v[128:131]\n\t" \
  "v_mfma_f32_16x16x32_bf16 v[136:139], v[200:203], v[60:63], v[136:139]\n\t" \
  "ds_read_b128 v[200:203], v211 offset:32768\n\t" \
  "v_mfma_f32_16x16x32_bf16 v[132:135], v[204:207], v[56:59], v[132:135]\n\t" \
  "v_mfma_f32_16x16x32_bf16 v[140:143], v[204:207], v[60:63], v[140:143]\n\t" \
  "ds_read_b128 v[204:207], v211 offset:49152\n\t" \
  "v_cvt_pk_bf16_f32 v168, v160, v161\n\t" \
  "v_cvt_pk_bf16_f32 v169, v162, v163\n\t" \
  "v_cvt_pk_bf16_f32 v170, v164, v165\n\t" \
  "v_cvt_pk_bf16_f32 v171, v166, v167\n\t" \
  "buffer_store_dwordx4 v[168:171], v172, %[rt], s90 offen sc1\n\t" \
  "v_mfma_f32_16x16x32_bf16 v[128:131], v[176:179], v[64:67], v[128:131]\n\t" \
  "v_mfma_f32_16x16x32_bf16 v[136:139], v[176:179], v[68:71], v[136:139]\n\t" \
  "ds_read_b128 v[176:179], v208 offset:33024\n\t" \
  "s_add_u32 m0, %[lw], 100352\n\t" \
  "v_mfma_f32_16x16x32_bf16 v[132:135], v[180:183], v[64:67], v[132:135]\n\t" \
  "v_mfma_f32_16x16x32_bf16 v[140:143], v[180:183], v[68:71], v[140:143]\n\t" \
  "ds_read_b128 v[180:183], v208 offset:49408\n\t" \
  "global_load_lds_dwordx4 v218, s[92:93] sc1\n\t" \
  "s_waitcnt lgkmcnt(6)\n\t" \
  "v_mfma_f32_16x16x32_bf16 v[128:131], v[184:187], v[72:75], v[128:131]\n\t" \
  "v_mfma_f32_16x16x32_bf16 v[136:139], v[184:187], v[76:79], v[136:139]\n\t" \
  "ds_read_b128 v[184:187], v209 offset:33024\n\t" \
  "v_mfma_f32_16x16x32_bf16 v[132:135], v[188:191], v[72:75], v[132:135]\n\t" \
  "v_mfma_f32_16x16x32_bf16 v[140:143], v[188:191], v[76:79], v[140:143]\n\t" \
  "ds_read_b128 v[188:191], v209 offset:49408\n\t" \
  "s_waitcnt lgkmcnt(6)\n\t" \
  "v_mfma_f32_16x16x32_bf16 v[128:131], v[192:195], v[80:83], v[128:131]\n\t" \
  "v_mfma_f32_16x16x32_bf16 v[136:139], v[192:195], v[84:87], v[136:139]\n\t" \
  "ds_read_b128 v[192:195], v210 offset:33024\n\t" \
  "s_add_u32 m0, %[lw], 101376\n\t" \
  "v_mfma_f32_16x16x32_bf16 v[132:135], v[196:199], v[80:83], v[132:135]\n\t" \
  "v_mfma_f32_16x16x32_bf16 v[140:143], v[196:199], v[84:87], v[140:143]\n\t" \
  "ds_read_b128 v[196:199], v210 offset:49408\n\t" \
  "global_load_lds_dwordx4 v219, s[92:93] sc1\n\t" \
  "s_waitcnt lgkmcnt(6)\n\t" \
  "v_mfma_f32_16x16x32_bf16 v[128:131], v[200:203], v[88:91], v[128:131]\n\t" \
  "v_mfma_f32_16x16x32_bf16 v[136:139], v[200:203], v[92:95], v[136:139]\n\t" \
  "ds_read_b128 v[200:203], v211 offset:33024\n\t" \
  "v_mfma_f32_16x16x32_bf16 v[132:135], v[204:207], v[88:91], v[132:135]\n\t" \
  "v_mfma_f32_16x16x32_bf16 v[140:143], v[204:207], v[92:95], v[140:143]\n\t" \
  "ds_read_b128 v[204:207], v211 offset:49408\n\t" \
  "s_cmp_lt_u32 s86, 6\n\t" \
  "s_cbranch_scc1 .Lw10%=\n\t" \
  "s_waitcnt vmcnt(5)\n\t" \
  "s_barrier\n\t" \
  ".Lg10%=:\n\t" \
  "s_add_u32 s92, s92, 65024\n\t" \
  "s_addc_u32 s93, s93, 0\n\t" \
  "s_waitcnt lgkmcnt(6)\n\t" \
  "v_mfma_f32_16x16x32_bf16 v[128:131], v[176:179], v[96:99], v[128:131]\n\t" \
  "v_mfma_f32_16x16x32_bf16 v[136:139], v[176:179], v[100:103], v[136:139]\n\t" \
  "ds_read_b128 v[176:179], v212 offset:0\n\t" \
  "s_add_u32 m0, %[lw], 0\n\t" \
  "v_mfma_f32_16x16x32_bf16 v[132:135], v[180:183], v[96:99], v[132:135]\n\t" \
  "v_mfma_f32_16x16x32_bf16 v[140:143], v[180:183], v[100:103], v[140:143]\n\t" \
  "ds_read_b128 v[180:183], v212 offset:16384\n\t" \
  "global_load_lds_dwordx4 v216, s[92:93] sc1\n\t" \
  "s_waitcnt lgkmcnt(6)\n\t" \
  "v_mfma_f32_16x16x32_bf16 v[128:131], v[184:187], v[104:107], v[128:131]\n\t" \
  "v_mfma_f32_16x16x32_bf16 v[136:139], v[184:187], v[108:111], v[136:139]\n\t" \
  "ds_read_b128 v[184:187], v213 offset:0\n\t" \
  "v_mfma_f32_16x16x32_bf16 v[132:135], v[188:191], v[104:107], v[132:135]\n\t" \
  "v_mfma_f32_16x16x32_bf16 v[140:143], v[188:191], v[108:111], v[140:143]\n\t" \
  "ds_read_b128 v[188:191], v213 offset:16384\n\t" \
  "s_waitcnt lgkmcnt(6)\n\t" \
  "v_mfma_f32_16x16x32_bf16 v[128:131], v[192:195], v[112:115], v[128:131]\n\t" \
  "v_mfma_f32_16x16x32_bf16 v[136:139], v[192:195], v[116:119], v[136:139]\n\t" \
  "ds_read_b128 v[192:195], v214 offset:0\n\t" \
  "s_add_u32 m0, %[lw], 1024\n\t" \
  "v_mfma_f32_16x16x32_bf16 v[132:135], v[196:199], v[112:115], v[132:135]\n\t" \
  "v_mfma_f32_16x16x32_bf16 v[140:143], v[196:199], v[116:119], v[140:143]\n\t" \
  "ds_read_b128 v[196:199], v214 offset:16384\n\t" \
  "global_load_lds_dwordx4 v217, s[92:93] sc1\n\t" \
  "s_waitcnt lgkmcnt(6)\n\t" \
  "v_mfma_f32_16x16x32_bf16 v[128:131], v[200:203], v[120:123], v[128:131]\n\t" \
  "v_mfma_f32_16x16x32_bf16 v[136:139], v[200:203], v[124:127], v[136:139]\n\t" \
  "ds_read_b128 v[200:203], v215 offset:0\n\t" \
  "v_mfma_f32_16x16x32_bf16 v[132:135], v[204:207], v[120:123], v[132:135]\n\t" \
  "v_mfma_f32_16x16x32_bf16 v[140:143], v[204:207], v[124:127], v[140:143]\n\t" \
  "ds_read_b128 v[204:207], v215 offset:16384\n\t" \
  "s_add_u32 s86, s86, 1\n\t" \
  "s_add_u32 s87, s87, 1\n\t" \
  "s_cmp_eq_u32 s86, 1\n\t" \
  "s_cbranch_scc1 .Lf0%=\n\t" \
  ".Lfb0%=:\n\t" \
  "s_waitcnt lgkmcnt(6)\n\t" \
  "v_mfma_f32_16x16x32_bf16 v[144:147], v[176:179], v[0:3], 0\n\t" \
  "v_mfma_f32_16x16x32_bf16 v[152:155], v[176:179], v[4:7], 0\n\t" \
  "ds_read_b128 v[176:179], v212 offset:256\n\t" \
  "s_add_u32 m0, %[lw], 2048\n\t" \
  "v_mfma_f32_16x16x32_bf16 v[148:151], v[180:183], v[0:3], 0\n\t" \
  "v_mfma_f32_16x16x32_bf16 v[156:159], v[180:183], v[4:7], 0\n\t" \
  "ds_read_b128 v[180:183], v212 offset:16640\n\t" \
  "global_load_lds_dwordx4 v218, s[92:93] sc1\n\t" \
  "s_waitcnt lgkmcnt(6)\n\t" \
  "v_mfma_f32_16x16x32_bf16 v[144:147], v[184:187], v[8:11], v[144:147]\n\t" \
  "v_mfma_f32_16x16x32_bf16 v[152:155], v[184:187], v[12:15], v[152:155]\n\t" \
  "ds_read_b128 v[184:187], v213 offset:256\n\t" \
  "v_mfma_f32_16x16x32_bf16 v[148:151], v[188:191], v[8:11], v[148:151]\n\t" \
  "v_mfma_f32_16x16x32_bf16 v[156:159], v[188:191], v[12:15], v[156:159]\n\t" \
  "ds_read_b128 v[188:191], v213 offset:16640\n\t" \
  "ds_write_b128 v220, v[136:139] offset:0\n\t" \
  "ds_write_b128 v220, v[140:143] offset:1024\n\t" \
  "s_waitcnt lgkmcnt(8)\n\t" \
  "v_mfma_f32_16x16x32_bf16 v[144:147], v[192:195], v[16:19], v[144:147]\n\t" \
  "v_mfma_f32_16x16x32_bf16 v[152:155], v[192:195], v[20:23], v[152:155]\n\t" \
  "ds_read_b128 v[192:195], v214 offset:256\n\t" \
  "s_add_u32 m0, %[lw], 3072\n\t" \
  "v_mfma_f32_16x16x32_bf16 v[148:151], v[196:199], v[16:19], v[148:151]\n\t" \
  "v_mfma_f32_16x16x32_bf16 v[156:159], v[196:199], v[20:23], v[156:159]\n\t" \
  "ds_read_b128 v[196:199], v214 offset:16640\n\t" \
  "global_load_lds_dwordx4 v219, s[92:93] sc1\n\t" \
  "s_lshr_b32 s90, s87, 7\n\t" \
  "s_xor_b32 s90, s90, %[s0]\n\t" \
  "s_and_b32 s90, s90, 1\n\t" \
  "s_lshl1_add_u32 s90, s87, s90\n\t" \
  "s_lshl_b32 s90, s90, 13\n\t" \
  "s_cmp_lt_i32 s86, 3\n\t" \
  "s_cselect_b32 s90, 0x70000000, s90\n\t" \
  "s_waitcnt lgkmcnt(8)\n\t" \
  "v_mfma_f32_16x16x32_bf16 v[144:147], v[200:203], v[24:27], v[144:147]\n\t" \
  "v_mfma_f32_16x16x32_bf16 v[152:155], v[200:203], v[28:31], v[152:155]\n\t" \
  "ds_read_b128 v[200:203], v215 offset:256\n\t" \
  "v_mfma_f32_16x16x32_bf16 v[148:151], v[204:207], v[24:27], v[148:151]\n\t" \
  "v_mfma_f32_16x16x32_bf16 v[156:159], v[204:207], v[28:31], v[156:159]\n\t" \
  "ds_read_b128 v[204:207], v215 offset:16640\n\t" \
  "s_waitcnt lgkmcnt(4)\n\t" \
  "s_waitcnt vmcnt(4)\n\t" \
  "s_barrier\n\t" \
  ".Lg20%=:\n\t" \
  "s_add_u32 s92, s92, 512\n\t" \
  "s_addc_u32 s93, s93, 0\n\t" \
  "v_mfma_f32_16x16x32_bf16 v[144:147], v[176:179], v[32:35], v[144:147]\n\t" \
  "v_mfma_f32_16x16x32_bf16 v[152:155], v[176:179], v[36:39], v[152:155]\n\t" \
  "ds_read_b128 v[176:179], v212 offset:32768\n\t" \
  "s_add_u32 m0, %[lw], 32768\n\t" \
  "v_mfma_f32_16x16x32_bf16 v[148:151], v[180:183], v[32:35], v[148:151]\n\t" \
  "v_mfma_f32_16x16x32_bf16 v[156:159], v[180:183], v[36:39], v[156:159]\n\t" \
  "ds_read_b128 v[180:183], v212 offset:49152\n\t" \
  "global_load_lds_dwordx4 v216, s[92:93] sc1\n\t" \
  "ds_read_b128 v[160:163], v221 offset:0\n\t" \
  "ds_read_b128 v[164:167], v221 offset:1024\n\t" \
  "v_mfma_f32_16x16x32_bf16 v[144:147], v[184:187], v[40:43], v[144:147]\n\t" \
  "v_mfma_f32_16x16x32_bf16 v[152:155], v[184:187], v[44:47], v[152:155]\n\t" \
  "ds_read_b128 v[184:187], v213 offset:32768\n\t" \
  "v_mfma_f32_16x16x32_bf16 v[148:151], v[188:191], v[40:43], v[148:151]\n\t" \
  "v_mfma_f32_16x16x32_bf16 v[156:159], v[188:191], v[44:47], v[156:159]\n\t" \
  "ds_read_b128 v[188:191], v213 offset:49152\n\t" \
  "s_waitcnt lgkmcnt(8)\n\t" \
  "v_mfma_f32_16x16x32_bf16 v[144:147], v[192:195], v[48:51], v[144:147]\n\t" \
  "v_mfma_f32_16x16x32_bf16 v[152:155], v[192:195], v[52:55], v[152:155]\n\t" \
  "ds_read_b128 v[192:195], v214 offset:32768\n\t" \
  "s_add_u32 m0, %[lw], 33792\n\t" \
  "v_mfma_f32_16x16x32_bf16 v[148:151], v[196:199], v[48:51], v[148:151]\n\t" \
  "v_mfma_f32_16x16x32_bf16 v[156:159], v[196:199], v[52:55], v[156:159]\n\t" \
  "ds_read_b128 v[196:199], v214 offset:49152\n\t" \
  "global_load_lds_dwordx4 v217, s[92:93] sc1\n\t" \
  "s_waitcnt lgkmcnt(4)\n\t" \
  "v_add_f32 v160, v160, v128\n\t" \
  "v_add_f32 v161, v161, v129\n\t" \
  "v_add_f32 v162, v162, v130\n\t" \
  "v_add_f32 v163, v163, v131\n\t" \
  "v_add_f32 v164, v164, v132\n\t" \
  "v_add_f32 v165, v165, v133\n\t" \
  "v_add_f32 v166, v166, v134\n\t" \
  "v_add_f32 v167, v167, v135\n\t" \
  "v_mfma_f32_16x16x32_bf16 v[144:147], v[200:203], v[56:59], v[144:147]\n\t" \
  "v_mfma_f32_16x16x32_bf16 v[152:155], v[200:203], v[60:63], v[152:155]\n\t" \
  "ds_read_b128 v[200:203], v215 offset:32768\n\t" \
  "v_mfma_f32_16x16x32_bf16 v[148:151], v[204:207], v[56:59], v[148:151]\n\t" \
  "v_mfma_f32_16x16x32_bf16 v[156:159], v[204:207], v[60:63], v[156:159]\n\t" \
  "ds_read_b128 v[204:207], v215 offset:49152\n\t" \
  "v_cvt_pk_bf16_f32 v168, v160, v161\n\t" \
  "v_cvt_pk_bf16_f32 v169, v162, v163\n\t" \
  "v_cvt_pk_bf16_f32 v170, v164, v165\n\t" \
  "v_cvt_pk_bf16_f32 v171, v166, v167\n\t" \
  "buffer_store_dwordx4 v[168:171], v172, %[rt], s90 offen sc1\n\t" \
  "v_mfma_f32_16x16x32_bf16 v[144:147], v[176:179], v[64:67], v[144:147]\n\t" \
  "v_mfma_f32_16x16x32_bf16 v[152:155], v[176:179], v[68:71], v[152:155]\n\t" \
  "ds_read_b128 v[176:179], v212 offset:33024\n\t" \
  "s_add_u32 m0, %[lw], 34816\n\t" \
  "v_mfma_f32_16x16x32_bf16 v[148:151], v[180:183], v[64:67], v[148:151]\n\t" \
  "v_mfma_f32_16x16x32_bf16 v[156:159], v[180:183], v[68:71], v[156:159]\n\t" \
  "ds_read_b128 v[180:183], v212 offset:49408\n\t" \
  "global_load_lds_dwordx4 v218, s[92:93] sc1\n\t" \
  "s_waitcnt lgkmcnt(6)\n\t" \
  "v_mfma_f32_16x16x32_bf16 v[144:147], v[184:187], v[72:75], v[144:147]\n\t" \
  "v_mfma_f32_16x16x32_bf16 v[152:155], v[184:187], v[76:79], v[152:155]\n\t" \
  "ds_read_b128 v[184:187], v213 offset:33024\n\t" \
  "v_mfma_f32_16x16x32_bf16 v[148:151], v[188:191], v[72:75], v[148:151]\n\t" \
  "v_mfma_f32_16x16x32_bf16 v[156:159], v[188:191], v[76:79], v[156:159]\n\t" \
  "ds_read_b128 v[188:191], v213 offset:49408\n\t" \
  "s_waitcnt lgkmcnt(6)\n\t" \
  "v_mfma_f32_16x16x32_bf16 v[144:147], v[192:195], v[80:83], v[144:147]\n\t" \
  "v_mfma_f32_16x16x32_bf16 v[152:155], v[192:195], v[84:87], v[152:155]\n\t" \
  "ds_read_b128 v[192:195], v214 offset:33024\n\t" \
  "s_add_u32 m0, %[lw], 35840\n\t" \
  "v_mfma_f32_16x16x32_bf16 v[148:151], v[196:199], v[80:83], v[148:151]\n\t" \
  "v_mfma_f32_16x16x32_bf16 v[156:159], v[196:199], v[84:87], v[156:159]\n\t" \
  "ds_read_b128 v[196:199], v214 offset:49408\n\t" \
  "global_load_lds_dwordx4 v219, s[92:93] sc1\n\t" \
  "s_waitcnt lgkmcnt(6)\n\t" \
  "v_mfma_f32_16x16x32_bf16 v[144:147], v[200:203], v[88:91], v[144:147]\n\t" \
  "v_mfma_f32_16x16x32_bf16 v[152:155], v[200:203], v[92:95], v[152:155]\n\t" \
  "ds_read_b128 v[200:203], v215 offset:33024\n\t" \
  "v_mfma_f32_16x16x32_bf16 v[148:151], v[204:207], v[88:91], v[148:151]\n\t" \
  "v_mfma_f32_16x16x32_bf16 v[156:159], v[204:207], v[92:95], v[156:159]\n\t" \
  "ds_read_b128 v[204:207], v215 offset:49408\n\t" \
  "s_cmp_lt_u32 s86, 6\n\t" \
  "s_cbranch_scc1 .Lw30%=\n\t" \
  "s_waitcnt vmcnt(5)\n\t" \
  "s_barrier\n\t" \
  ".Lg30%=:\n\t" \
  "s_add_u32 s92, s92, 65024\n\t" \
  "s_addc_u32 s93, s93, 0\n\t" \
  "s_waitcnt lgkmcnt(6)\n\t" \
  "v_mfma_f32_16x16x32_bf16 v[144:147], v[176:179], v[96:99], v[144:147]\n\t" \
  "v_mfma_f32_16x16x32_bf16 v[152:155], v[176:179], v[100:103], v[152:155]\n\t" \
  "ds_read_b128 v[176:179], v208 offset:0\n\t" \
  "s_add_u32 m0, %[lw], 65536\n\t" \
  "v_mfma_f32_16x16x32_bf16 v[148:151], v[180:183], v[96:99], v[148:151]\n\t" \
  "v_mfma_f32_16x16x32_bf16 v[156:159], v[180:183], v[100:103], v[156:159]\n\t" \
  "ds_read_b128 v[180:183], v208 offset:16384\n\t" \
  "global_load_lds_dwordx4 v216, s[92:93] sc1\n\t" \
  "s_waitcnt lgkmcnt(6)\n\t" \
  "v_mfma_f32_16x16x32_bf16 v[144:147], v[184:187], v[104:107], v[144:147]\n\t" \
  "v_mfma_f32_16x16x32_bf16 v[152:155], v[184:187], v[108:111], v[152:155]\n\t" \
  "ds_read_b128 v[184:187], v209 offset:0\n\t" \
  "v_mfma_f32_16x16x32_bf16 v[148:151], v[188:191], v[104:107], v[148:151]\n\t" \
  "v_mfma_f32_16x16x32_bf16 v[156:159], v[188:191], v[108:111], v[156:159]\n\t" \
  "ds_read_b128 v[188:191], v209 offset:16384\n\t" \
  "s_waitcnt lgkmcnt(6)\n\t" \
  "v_mfma_f32_16x16x32_bf16 v[144:147], v[192:195], v[112:115], v[144:147]\n\t" \
  "v_mfma_f32_16x16x32_bf16 v[152:155], v[192:195], v[116:119], v[152:155]\n\t" \
  "ds_read_b128 v[192:195], v210 offset:0\n\t" \
  "s_add_u32 m0, %[lw], 66560\n\t" \
  "v_mfma_f32_16x16x32_bf16 v[148:151], v[196:199], v[112:115], v[148:151]\n\t" \
  "v_mfma_f32_16x16x32_bf16 v[156:159], v[196:199], v[116:119], v[156:159]\n\t" \
  "ds_read_b128 v[196:199], v210 offset:16384\n\t" \
  "global_load_lds_dwordx4 v217, s[92:93] sc1\n\t" \
  "s_waitcnt lgkmcnt(6)\n\t" \
  "v_mfma_f32_16x16x32_bf16 v[144:147], v[200:203], v[120:123], v[144:147]\n\t" \
  "v_mfma_f32_16x16x32_bf16 v[152:155], v[200:203], v[124:127], v[152:155]\n\t" \
  "ds_read_b128 v[200:203], v211 offset:0\n\t" \
  "v_mfma_f32_16x16x32_bf16 v[148:151], v[204:207], v[120:123], v[148:151]\n\t" \
  "v_mfma_f32_16x16x32_bf16 v[156:159], v[204:207], v[124:127], v[156:159]\n\t" \
  "ds_read_b128 v[204:207], v211 offset:16384\n\t" \
  "s_add_u32 s86, s86, 1\n\t" \
  "s_add_u32 s87, s87, 1\n\t" \
  "s_cmp_eq_u32 s86, 2\n\t" \
  "s_cbranch_scc1 .Ll0%=\n\t" \
  "s_cmp_lt_u32 s86, %[end]\n\t" \
  "s_cbranch_scc1 .Lp0%=\n\t" \
  "s_cmp_lg_u32 s86, 64\n\t" \
  "s_cbranch_scc1 .Lx0%=\n\t" \
  ".Ll0%=:\n\t" \
  "s_nop 7\n\t" \
  "ds_write_b128 v220, v[152:155] offset:0\n\t" \
  "ds_write_b128 v220, v[156:159] offset:1024\n\t" \
  "s_lshr_b32 s90, s87, 7\n\t" \
  "s_xor_b32 s90, s90, %[s0]\n\t" \
  "s_and_b32 s90, s90, 1\n\t" \
  "s_lshl1_add_u32 s90, s87, s90\n\t" \
  "s_lshl_b32 s90, s90, 13\n\t" \
  "s_cmp_lt_i32 s86, 1\n\t" \
  "s_cselect_b32 s90, 0x70000000, s90\n\t" \
  "s_waitcnt lgkmcnt(0)\n\t" \
  "s_barrier\n\t" \
  "ds_read_b128 v[160:163], v221 offset:0\n\t" \
  "ds_read_b128 v[164:167], v221 offset:1024\n\t" \
  "s_waitcnt lgkmcnt(0)\n\t" \
  "v_add_f32 v160, v160, v144\n\t" \
  "v_add_f32 v161, v161, v145\n\t" \
  "v_add_f32 v162, v162, v146\n\t" \
  "v_add_f32 v163, v163, v147\n\t" \
  "v_add_f32 v164, v164, v148\n\t" \
  "v_add_f32 v165, v165, v149\n\t" \
  "v_add_f32 v166, v166, v150\n\t" \
  "v_add_f32 v167, v167, v151\n\t" \
  "v_cvt_pk_bf16_f32 v168, v160, v161\n\t" \
  "v_cvt_pk_bf16_f32 v169, v162, v163\n\t" \
  "v_cvt_pk_bf16_f32 v170, v164, v165\n\t" \
  "v_cvt_pk_bf16_f32 v171, v166, v167\n\t" \
  "buffer_store_dwordx4 v[168:171], v172, %[rt], s90 offen sc1\n\t" \
  "s_waitcnt vmcnt(0)\n\t" \
  "s_barrier\n\t" \
  "s_cmp_eq_u32 s86, 2\n\t" \
  "s_cbranch_scc1 .Lp0%=\n\t" \
  "s_branch .Lx0%=\n\t" \
  ".Lf0%=:\n\t" \
  "s_nop 7\n\t" \
  "ds_write_b128 v220, v[136:139] offset:0\n\t" \
  "ds_write_b128 v220, v[140:143] offset:1024\n\t" \
  "s_lshr_b32 s90, s87, 7\n\t" \
  "s_xor_b32 s90, s90, %[s0]\n\t" \
  "s_and_b32 s90, s90, 1\n\t" \
  "s_lshl1_add_u32 s90, s87, s90\n\t" \
  "s_lshl_b32 s90, s90, 13\n\t" \
  "s_cmp_lt_i32 s86, 1\n\t" \
  "s_cselect_b32 s90, 0x70000000, s90\n\t" \
  "s_waitcnt lgkmcnt(0)\n\t" \
  "s_barrier\n\t" \
  "ds_read_b128 v[160:163], v221 offset:0\n\t" \
  "ds_read_b128 v[164:167], v221 offset:1024\n\t" \
  "s_waitcnt lgkmcnt(0)\n\t" \
  "v_add_f32 v160, v160, v128\n\t" \
  "v_add_f32 v161, v161, v129\n\t" \
  "v_add_f32 v162, v162, v130\n\t" \
  "v_add_f32 v163, v163, v131\n\t" \
  "v_add_f32 v164, v164, v132\n\t" \
  "v_add_f32 v165, v165, v133\n\t" \
  "v_add_f32 v166, v166, v134\n\t" \
  "v_add_f32 v167, v167, v135\n\t" \
  "v_cvt_pk_bf16_f32 v168, v160, v161\n\t" \
  "v_cvt_pk_bf16_f32 v169, v162, v163\n\t" \
  "v_cvt_pk_bf16_f32 v170, v164, v165\n\t" \
  "v_cvt_pk_bf16_f32 v171, v166, v167\n\t" \
  "buffer_store_dwordx4 v[168:171], v172, %[rt], s90 offen sc1\n\t" \
  "s_waitcnt vmcnt(0)\n\t" \
  "s_barrier\n\t" \
  "s_branch .Lfb0%=\n\t" \
  ".Lw10%=:\n\t" \
  "s_waitcnt vmcnt(2)\n\t" \
  "s_barrier\n\t" \
  "s_branch .Lg10%=\n\t" \
  ".Lw30%=:\n\t" \
  "s_waitcnt vmcnt(2)\n\t" \
  "s_barrier\n\t" \
  "s_branch .Lg30%=\n\t" \
  ".Lx0%=:\n\t" \
  "s_waitcnt lgkmcnt(0)\n\t" \
  "s_setprio 0\n\t" \
  "s_mov_b32 m0, s94\n\t" \
  "s_branch .Le%=\n\t" \
  ".Lk%=:\n\t" \
  "s_mov_b32 s94, m0\n\t" \
  "s_mov_b64 s[92:93], %[x0]\n\t" \
  "s_mov_b32 s86, %[g0]\n\t" \
  "s_add_u32 s87, s86, %[t0]\n\t" \
  "s_sub_i32 s88, s86, 3\n\t" \
  "ds_read_b128 v[176:179], v208 offset:0\n\t" \
  "ds_read_b128 v[180:183], v208 offset:16384\n\t" \
  "ds_read_b128 v[184:187], v209 offset:0\n\t" \
  "ds_read_b128 v[188:191], v209 offset:16384\n\t" \
  "ds_read_b128 v[192:195], v210 offset:0\n\t" \
  "ds_read_b128 v[196:199], v210 offset:16384\n\t" \
  "ds_read_b128 v[200:203], v211 offset:0\n\t" \
  "ds_read_b128 v[204:207], v211 offset:16384\n\t" \
  ".Lp1%=:\n\t" \
  "s_waitcnt lgkmcnt(6)\n\t" \
  "v_mfma_f32_16x16x32_bf16 v[128:131], v[176:179], v[0:3], 0\n\t" \
  "v_mfma_f32_16x16x32_bf16 v[136:139], v[176:179], v[4:7], 0\n\t" \
  "ds_read_b128 v[176:179], v208 offset:256\n\t" \
  "v_mfma_f32_16x16x32_bf16 v[132:135], v[180:183], v[0:3], 0\n\t" \
  "v_mfma_f32_16x16x32_bf16 v[140:143], v[180:183], v[4:7], 0\n\t" \
  "ds_read_b128 v[180:183], v208 offset:16640\n\t" \
  "s_waitcnt lgkmcnt(6)\n\t" \
  "v_mfma_f32_16x16x32_bf16 v[128:131], v[184:187], v[8:11], v[128:131]\n\t" \
  "v_mfma_f32_16x16x32_bf16 v[136:139], v[184:187], v[12:15], v[136:139]\n\t" \
  "ds_read_b128 v[184:187], v209 offset:256\n\t" \
  "s_add_u32 m0, %[lw], 67584\n\t" \
  "v_mfma_f32_16x16x32_bf16 v[132:135], v[188:191], v[8:11], v[132:135]\n\t" \
  "v_mfma_f32_16x16x32_bf16 v[140:143], v[188:191], v[12:15], v[140:143]\n\t" \
  "ds_read_b128 v[188:191], v209 offset:16640\n\t" \
  "global_load_lds_dwordx4 v218, s[92:93] sc1\n\t" \
  "ds_write_b128 v220, v[152:155] offset:0\n\t" \
  "ds_write_b128 v220, v[156:159] offset:1024\n\t" \
  "s_waitcnt lgkmcnt(8)\n\t" \
  "v_mfma_f32_16x16x32_bf16 v[128:131], v[192:195], v[16:19], v[128:131]\n\t" \
  "v_mfma_f32_16x16x32_bf16 v[136:139], v[192:195], v[20:23], v[136:139]\n\t" \
  "ds_read_b128 v[192:195], v210 offset:256\n\t" \
  "v_mfma_f32_16x16x32_bf16 v[132:135], v[196:199], v[16:19], v[132:135]\n\t" \
  "v_mfma_f32_16x16x32_bf16 v[140:143], v[196:199], v[20:23], v[140:143]\n\t" \
  "ds_read_b128 v[196:199], v210 offset:16640\n\t" \
  "s_lshr_b32 s90, s87, 7\n\t" \
  "s_xor_b32 s90, s90, %[s0]\n\t" \
  "s_and_b32 s90, s90, 1\n\t" \
  "s_lshl1_add_u32 s90, s87, s90\n\t" \
  "s_lshl_b32 s90, s90, 13\n\t" \
  "s_cmp_lt_i32 s86, 3\n\t" \
  "s_cselect_b32 s90, 0x70000000, s90\n\t" \
  "s_waitcnt lgkmcnt(8)\n\t" \
  "v_mfma_f32_16x16x32_bf16 v[128:131], v[200:203], v[24:27], v[128:131]\n\t" \
  "v_mfma_f32_16x16x32_bf16 v[136:139], v[200:203], v[28:31], v[136:139]\n\t" \
  "ds_read_b128 v[200:203], v211 offset:256\n\t" \
  "s_add_u32 m0, %[lw], 68608\n\t" \
  "v_mfma_f32_16x16x32_bf16 v[132:135], v[204:207], v[24:27], v[132:135]\n\t" \
  "v_mfma_f32_16x16x32_bf16 v[140:143], v[204:207], v[28:31], v[140:143]\n\t" \
  "ds_read_b128 v[204:207], v211 offset:16640\n\t" \
  "global_load_lds_dwordx4 v219, s[92:93] sc1\n\t" \
  "s_waitcnt lgkmcnt(4)\n\t" \
  "s_waitcnt vmcnt(4)\n\t" \
  "s_barrier\n\t" \
  ".Lg01%=:\n\t" \
  "s_add_u32 s92, s92, 512\n\t" \
  "s_addc_u32 s93, s93, 0\n\t" \
  "v_mfma_f32_16x16x32_bf16 v[128:131], v[176:179], v[32:35], v[128:131]\n\t" \
  "v_mfma_f32_16x16x32_bf16 v[136:139], v[176:179], v[36:39], v[136:139]\n\t" \
  "ds_read_b128 v[176:179], v208 offset:32768\n\t" \
  "v_mfma_f32_16x16x32_bf16 v[132:135], v[180:183], v[32:35], v[132:135]\n\t" \
  "v_mfma_f32_16x16x32_bf16 v[140:143], v[180:183], v[36:39], v[140:143]\n\t" \
  "ds_read_b128 v[180:183], v208 offset:49152\n\t" \
  "ds_read_b128 v[160:163], v221 offset:0\n\t" \
  "ds_read_b128 v[164:167], v221 offset:1024\n\t" \
  "s_sub_i32 s91, s86, 2\n\t" \
  "s_max_i32 s88, s88, s91\n\t" \
  "v_mov_b32 v173, s88\n\t" \
  "buffer_store_dword v173, v174, %[rs], 0 offen sc1\n\t" \
  "v_mfma_f32_16x16x32_bf16 v[128:131], v[184:187], v[40:43], v[128:131]\n\t" \
  "v_mfma_f32_16x16x32_bf16 v[136:139], v[184:187], v[44:47], v[136:139]\n\t" \
  "ds_read_b128 v[184:187], v209 offset:32768\n\t" \
  "s_add_u32 m0, %[lw], 98304\n\t" \
  "v_mfma_f32_16x16x32_bf16 v[132:135], v[188:191], v[40:43], v[132:135]\n\t" \
  "v_mfma_f32_16x16x32_bf16 v[140:143], v[188:191], v[44:47], v[140:143]\n\t" \
  "ds_read_b128 v[188:191], v209 offset:49152\n\t" \
  "global_load_lds_dwordx4 v216, s[92:93] sc1\n\t" \
  "s_waitcnt lgkmcnt(8)\n\t" \
  "v_mfma_f32_16x16x32_bf16 v[128:131], v[192:195], v[48:51], v[128:131]\n\t" \
  "v_mfma_f32_16x16x32_bf16 v[136:139], v[192:195], v[52:55], v[136:139]\n\t" \
  "ds_read_b128 v[192:195], v210 offset:32768\n\t" \
  "v_mfma_f32_16x16x32_bf16 v[132:135], v[196:199], v[48:51], v[132:135]\n\t" \
  "v_mfma_f32_16x16x32_bf16 v[140:143], v[196:199], v[52:55], v[140:143]\n\t" \
  "ds_read_b128 v[196:199], v210 offset:49152\n\t" \
  "s_waitcnt lgkmcnt(4)\n\t" \
  "v_add_f32 v160, v160, v144\n\t" \
  "v_add_f32 v161, v161, v145\n\t" \
  "v_add_f32 v162, v162, v146\n\t" \
  "v_add_f32 v163, v163, v147\n\t" \
  "v_add_f32 v164, v164, v148\n\t" \
  "v_add_f32 v165, v165, v149\n\t" \
  "v_add_f32 v166, v166, v150\n\t" \
  "v_add_f32 v167, v167, v151\n\t" \
  "v_mfma_f32_16x16x32_bf16 v[128:131], v[200:203], v[56:59], v[128:131]\n\t" \
  "v_mfma_f32_16x16x32_bf16 v[136:139], v[200:203], v[60:63], v[136:139]\n\t" \
  "ds_read_b128 v[200:203], v211 offset:32768\n\t" \
  "s_add_u32 m0, %[lw], 99328\n\t" \
  "v_mfma_f32_16x16x32_bf16 v[132:135], v[204:207], v[56:59], v[132:135]\n\t" \
  "v_mfma_f32_16x16x32_bf16 v[140:143], v[204:207], v[60:63], v[140:143]\n\t" \
  "ds_read_b128 v[204:207], v211 offset:49152\n\t" \
  "global_load_lds_dwordx4 v217, s[92:93] sc1\n\t" \
  "v_cvt_pk_bf16_f32 v168, v160, v161\n\t" \
  "v_cvt_pk_bf16_f32 v169, v162, v163\n\t" \
  "v_cvt_pk_bf16_f32 v170, v164, v165\n\t" \
  "v_cvt_pk_bf16_f32 v171, v166, v167\n\t" \
  "buffer_store_dwordx4 v[168:171], v172, %[rt], s90 offen sc1\n\t" \
  "v_mfma_f32_16x16x32_bf16 v[128:131], v[176:179], v[64:67], v[128:131]\n\t" \
  "v_mfma_f32_16x16x32_bf16 v[136:139], v[176:179], v[68:71], v[136:139]\n\t" \
  "ds_read_b128 v[176:179], v208 offset:33024\n\t" \
  "v_mfma_f32_16x16x32_bf16 v[132:135], v[180:183], v[64:67], v[132:135]\n\t" \
  "v_mfma_f32_16x16x32_bf16 v[140:143], v[180:183], v[68:71], v[140:143]\n\t" \
  "ds_read_b128 v[180:183], v208 offset:49408\n\t" \
  "s_waitcnt lgkmcnt(6)\n\t" \
  "v_mfma_f32_16x16x32_bf16 v[128:131], v[184:187], v[72:75], v[128:131]\n\t" \
  "v_mfma_f32_16x16x32_bf16 v[136:139], v[184:187], v[76:79], v[136:139]\n\t" \
  "ds_read_b128 v[184:187], v209 offset:33024\n\t" \
  "s_add_u32 m0, %[lw], 100352\n\t" \
  "v_mfma_f32_16x16x32_bf16 v[132:135], v[188:191], v[72:75], v[132:135]\n\t" \
  "v_mfma_f32_16x16x32_bf16 v[140:143], v[188:191], v[76:79], v[140:143]\n\t" \
  "ds_read_b128 v[188:191], v209 offset:49408\n\t" \
  "global_load_lds_dwordx4 v218, s[92:93] sc1\n\t" \
  "s_waitcnt lgkmcnt(6)\n\t" \
  "v_mfma_f32_16x16x32_bf16 v[128:131], v[192:195], v[80:83], v[128:131]\n\t" \
  "v_mfma_f32_16x16x32_bf16 v[136:139], v[192:195], v[84:87], v[136:139]\n\t" \
  "ds_read_b128 v[192:195], v210 offset:33024\n\t" \
  "v_mfma_f32_16x16x32_bf16 v[132:135], v[196:199], v[80:83], v[132:135]\n\t" \
  "v_mfma_f32_16x16x32_bf16 v[140:143], v[196:199], v[84:87], v[140:143]\n\t" \
  "ds_read_b128 v[196:199], v210 offset:49408\n\t" \
  "s_waitcnt lgkmcnt(6)\n\t" \
  "v_mfma_f32_16x16x32_bf16 v[128:131], v[200:203], v[88:91], v[128:131]\n\t" \
  "v_mfma_f32_16x16x32_bf16 v[136:139], v[200:203], v[92:95], v[136:139]\n\t" \
  "ds_read_b128 v[200:203], v211 offset:33024\n\t" \
  "s_add_u32 m0, %[lw], 101376\n\t" \
  "v_mfma_f32_16x16x32_bf16 v[132:135], v[204:207], v[88:91], v[132:135]\n\t" \
  "v_mfma_f32_16x16x32_bf16 v[140:143], v[204:207], v[92:95], v[140:143]\n\t" \
  "ds_read_b128 v[204:207], v211 offset:49408\n\t" \
  "global_load_lds_dwordx4 v219, s[92:93] sc1\n\t" \
  "s_cmp_lt_u32 s86, 6\n\t" \
  "s_cbranch_scc1 .Lw11%=\n\t" \
  "s_waitcnt vmcnt(6)\n\t" \
  "s_barrier\n\t" \
  ".Lg11%=:\n\t" \
  "s_add_u32 s92, s92, 65024\n\t" \
  "s_addc_u32 s93, s93, 0\n\t" \
  "s_waitcnt lgkmcnt(6)\n\t" \
  "v_mfma_f32_16x16x32_bf16 v[128:131], v[176:179], v[96:99], v[128:131]\n\t" \
  "v_mfma_f32_16x16x32_bf16 v[136:139], v[176:179], v[100:103], v[136:139]\n\t" \
  "ds_read_b128 v[176:179], v212 offset:0\n\t" \
  "v_mfma_f32_16x16x32_bf16 v[132:135], v[180:183], v[96:99], v[132:135]\n\t" \
  "v_mfma_f32_16x16x32_bf16 v[140:143], v[180:183], v[100:103], v[140:143]\n\t" \
  "ds_read_b128 v[180:183], v212 offset:16384\n\t" \
  "s_waitcnt lgkmcnt(6)\n\t" \
  "v_mfma_f32_16x16x32_bf16 v[128:131], v[184:187], v[104:107], v[128:131]\n\t" \
  "v_mfma_f32_16x16x32_bf16 v[136:139], v[184:187], v[108:111], v[136:139]\n\t" \
  "ds_read_b128 v[184:187], v213 offset:0\n\t" \
  "s_add_u32 m0, %[lw], 0\n\t" \
  "v_mfma_f32_16x16x32_bf16 v[132:135], v[188:191], v[104:107], v[132:135]\n\t" \
  "v_mfma_f32_16x16x32_bf16 v[140:143], v[188:191], v[108:111], v[140:143]\n\t" \
  "ds_read_b128 v[188:191], v213 offset:16384\n\t" \
  "global_load_lds_dwordx4 v216, s[92:93] sc1\n\t" \
  "s_waitcnt lgkmcnt(6)\n\t" \
  "v_mfma_f32_16x16x32_bf16 v[128:131], v[192:195], v[112:115], v[128:131]\n\t" \
  "v_mfma_f32_16x16x32_bf16 v[136:139], v[192:195], v[116:119], v[136:139]\n\t" \
  "ds_read_b128 v[192:195], v214 offset:0\n\t" \
  "v_mfma_f32_16x16x32_bf16 v[132:135], v[196:199], v[112:115], v[132:135]\n\t" \
  "v_mfma_f32_16x16x32_bf16 v[140:143], v[196:199], v[116:119], v[140:143]\n\t" \
  "ds_read_b128 v[196:199], v214 offset:16384\n\t" \
  "s_waitcnt lgkmcnt(6)\n\t" \
  "v_mfma_f32_16x16x32_bf16 v[128:131], v[200:203], v[120:123], v[128:131]\n\t" \
  "v_mfma_f32_16x16x32_bf16 v[136:139], v[200:203], v[124:127], v[136:139]\n\t" \
  "ds_read_b128 v[200:203], v215 offset:0\n\t" \
  "s_add_u32 m0, %[lw], 1024\n\t" \
  "v_mfma_f32_16x16x32_bf16 v[132:135], v[204:207], v[120:123], v[132:135]\n\t" \
  "v_mfma_f32_16x16x32_bf16 v[140:143], v[204:207], v[124:127], v[140:143]\n\t" \
  "ds_read_b128 v[204:207], v215 offset:16384\n\t" \
  "global_load_lds_dwordx4 v217, s[92:93] sc1\n\t" \
  "s_add_u32 s86, s86, 1\n\t" \
  "s_add_u32 s87, s87, 1\n\t" \
  "s_cmp_eq_u32 s86, 1\n\t" \
  "s_cbranch_scc1 .Lf1%=\n\t" \
  ".Lfb1%=:\n\t" \
  "s_waitcnt lgkmcnt(6)\n\t" \
  "v_mfma_f32_16x16x32_bf16 v[144:147], v[176:179], v[0:3], 0\n\t" \
  "v_mfma_f32_16x16x32_bf16 v[152:155], v[176:179], v[4:7], 0\n\t" \
  "ds_read_b128 v[176:179], v212 offset:256\n\t" \
  "v_mfma_f32_16x16x32_bf16 v[148:151], v[180:183], v[0:3], 0\n\t" \
  "v_mfma_f32_16x16x32_bf16 v[156:159], v[180:183], v[4:7], 0\n\t" \
  "ds_read_b128 v[180:183], v212 offset:16640\n\t" \
  "s_waitcnt lgkmcnt(6)\n\t" \
  "v_mfma_f32_16x16x32_bf16 v[144:147], v[184:187], v[8:11], v[144:147]\n\t" \
  "v_mfma_f32_16x16x32_bf16 v[152:155], v[184:187], v[12:15], v[152:155]\n\t" \
  "ds_read_b128 v[184:187], v213 offset:256\n\t" \
  "s_add_u32 m0, %[lw], 2048\n\t" \
  "v_mfma_f32_16x16x32_bf16 v[148:151], v[188:191], v[8:11], v[148:151]\n\t" \
  "v_mfma_f32_16x16x32_bf16 v[156:159], v[188:191], v[12:15], v[156:159]\n\t" \
  "ds_read_b128 v[188:191], v213 offset:16640\n\t" \
  "global_load_lds_dwordx4 v218, s[92:93] sc1\n\t" \
  "ds_write_b128 v220, v[136:139] offset:0\n\t" \
  "ds_write_b128 v220, v[140:143] offset:1024\n\t" \
  "s_waitcnt lgkmcnt(8)\n\t" \
  "v_mfma_f32_16x16x32_bf16 v[144:147], v[192:195], v[16:19], v[144:147]\n\t" \
  "v_mfma_f32_16x16x32_bf16 v[152:155], v[192:195], v[20:23], v[152:155]\n\t" \
  "ds_read_b128 v[192:195], v214 offset:256\n\t" \
  "v_mfma_f32_16x16x32_bf16 v[148:151], v[196:199], v[16:19], v[148:151]\n\t" \
  "v_mfma_f32_16x16x32_bf16 v[156:159], v[196:199], v[20:23], v[156:159]\n\t" \
  "ds_read_b128 v[196:199], v214 offset:16640\n\t" \
  "s_lshr_b32 s90, s87, 7\n\t" \
  "s_xor_b32 s90, s90, %[s0]\n\t" \
  "s_and_b32 s90, s90, 1\n\t" \
  "s_lshl1_add_u32 s90, s87, s90\n\t" \
  "s_lshl_b32 s90, s90, 13\n\t" \
  "s_cmp_lt_i32 s86, 3\n\t" \
  "s_cselect_b32 s90, 0x70000000, s90\n\t" \
  "s_waitcnt lgkmcnt(8)\n\t" \
  "v_mfma_f32_16x16x32_bf16 v[144:147], v[200:203], v[24:27], v[144:147]\n\t" \
  "v_mfma_f32_16x16x32_bf16 v[152:155], v[200:203], v[28:31], v[152:155]\n\t" \
  "ds_read_b128 v[200:203], v215 offset:256\n\t" \
  "s_add_u32 m0, %[lw], 3072\n\t" \
  "v_mfma_f32_16x16x32_bf16 v[148:151], v[204:207], v[24:27], v[148:151]\n\t" \
  "v_mfma_f32_16x16x32_bf16 v[156:159], v[204:207], v[28:31], v[156:159]\n\t" \
  "ds_read_b128 v[204:207], v215 offset:16640\n\t" \
  "global_load_lds_dwordx4 v219, s[92:93] sc1\n\t" \
  "s_waitcnt lgkmcnt(4)\n\t" \
  "s_waitcnt vmcnt(4)\n\t" \
  "s_barrier\n\t" \
  ".Lg21%=:\n\t" \
  "s_add_u32 s92, s92, 512\n\t" \
  "s_addc_u32 s93, s93, 0\n\t" \
  "v_mfma_f32_16x16x32_bf16 v[144:147], v[176:179], v[32:35], v[144:147]\n\t" \
  "v_mfma_f32_16x16x32_bf16 v[152:155], v[176:179], v[36:39], v[152:155]\n\t" \
  "ds_read_b128 v[176:179], v212 offset:32768\n\t" \
  "v_mfma_f32_16x16x32_bf16 v[148:151], v[180:183], v[32:35], v[148:151]\n\t" \
  "v_mfma_f32_16x16x32_bf16 v[156:159], v[180:183], v[36:39], v[156:159]\n\t" \
  "ds_read_b128 v[180:183], v212 offset:49152\n\t" \
  "ds_read_b128 v[160:163], v221 offset:0\n\t" \
  "ds_read_b128 v[164:167], v221 offset:1024\n\t" \
  "s_sub_i32 s91, s86, 2\n\t" \
  "s_max_i32 s88, s88, s91\n\t" \
  "v_mov_b32 v173, s88\n\t" \
  "buffer_store_dword v173, v174, %[rs], 0 offen sc1\n\t" \
  "v_mfma_f32_16x16x32_bf16 v[144:147], v[184:187], v[40:43], v[144:147]\n\t" \
  "v_mfma_f32_16x16x32_bf16 v[152:155], v[184:187], v[44:47], v[152:155]\n\t" \
  "ds_read_b128 v[184:187], v213 offset:32768\n\t" \
  "s_add_u32 m0, %[lw], 32768\n\t" \
  "v_mfma_f32_16x16x32_bf16 v[148:151], v[188:191], v[40:43], v[148:151]\n\t" \
  "v_mfma_f32_16x16x32_bf16 v[156:159], v[188:191], v[44:47], v[156:159]\n\t" \
  "ds_read_b128 v[188:191], v213 offset:49152\n\t" \
  "global_load_lds_dwordx4 v216, s[92:93] sc1\n\t" \
  "s_waitcnt lgkmcnt(8)\n\t" \
  "v_mfma_f32_16x16x32_bf16 v[144:147], v[192:195], v[48:51], v[144:147]\n\t" \
  "v_mfma_f32_16x16x32_bf16 v[152:155], v[192:195], v[52:55], v[152:155]\n\t" \
  "ds_read_b128 v[192:195], v214 offset:32768\n\t" \
  "v_mfma_f32_16x16x32_bf16 v[148:151], v[196:199], v[48:51], v[148:151]\n\t" \
  "v_mfma_f32_16x16x32_bf16 v[156:159], v[196:199], v[52:55], v[156:159]\n\t" \
  "ds_read_b128 v[196:199], v214 offset:49152\n\t" \
  "s_waitcnt lgkmcnt(4)\n\t" \
  "v_add_f32 v160, v160, v128\n\t" \
  "v_add_f32 v161, v161, v129\n\t" \
  "v_add_f32 v162, v162, v130\n\t" \
  "v_add_f32 v163, v163, v131\n\t" \
  "v_add_f32 v164, v164, v132\n\t" \
  "v_add_f32 v165, v165, v133\n\t" \
  "v_add_f32 v166, v166, v134\n\t" \
  "v_add_f32 v167, v167, v135\n\t" \
  "v_mfma_f32_16x16x32_bf16 v[144:147], v[200:203], v[56:59], v[144:147]\n\t" \
  "v_mfma_f32_16x16x32_bf16 v[152:155], v[200:203], v[60:63], v[152:155]\n\t" \
  "ds_read_b128 v[200:203], v215 offset:32768\n\t" \
  "s_add_u32 m0, %[lw], 33792\n\t" \
  "v_mfma_f32_16x16x32_bf16 v[148:151], v[204:207], v[56:59], v[148:151]\n\t" \
  "v_mfma_f32_16x16x32_bf16 v[156:159], v[204:207], v[60:63], v[156:159]\n\t" \
  "ds_read_b128 v[204:207], v215 offset:49152\n\t" \
  "global_load_lds_dwordx4 v217, s[92:93] sc1\n\t" \
  "v_cvt_pk_bf16_f32 v168, v160, v161\n\t" \
  "v_cvt_pk_bf16_f32 v169, v162, v163\n\t" \
  "v_cvt_pk_bf16_f32 v170, v164, v165\n\t" \
  "v_cvt_pk_bf16_f32 v171, v166, v167\n\t" \
  "buffer_store_dwordx4 v[168:171], v172, %[rt], s90 offen sc1\n\t" \
  "v_mfma_f32_16x16x32_bf16 v[144:147], v[176:179], v[64:67], v[144:147]\n\t" \
  "v_mfma_f32_16x16x32_bf16 v[152:155], v[176:179], v[68:71], v[152:155]\n\t" \
  "ds_read_b128 v[176:179], v212 offset:33024\n\t" \
  "v_mfma_f32_16x16x32_bf16 v[148:151], v[180:183], v[64:67], v[148:151]\n\t" \
  "v_mfma_f32_16x16x32_bf16 v[156:159], v[180:183], v[68:71], v[156:159]\n\t" \
  "ds_read_b128 v[180:183], v212 offset:49408\n\t" \
  "s_waitcnt lgkmcnt(6)\n\t" \
  "v_mfma_f32_16x16x32_bf16 v[144:147], v[184:187], v[72:75], v[144:147]\n\t" \
  "v_mfma_f32_16x16x32_bf16 v[152:155], v[184:187], v[76:79], v[152:155]\n\t" \
  "ds_read_b128 v[184:187], v213 offset:33024\n\t" \
  "s_add_u32 m0, %[lw], 34816\n\t" \
  "v_mfma_f32_16x16x32_bf16 v[148:151], v[188:191], v[72:75], v[148:151]\n\t" \
  "v_mfma_f32_16x16x32_bf16 v[156:159], v[188:191], v[76:79], v[156:159]\n\t" \
  "ds_read_b128 v[188:191], v213 offset:49408\n\t" \
  "global_load_lds_dwordx4 v218, s[92:93] sc1\n\t" \
  "s_waitcnt lgkmcnt(6)\n\t" \
  "v_mfma_f32_16x16x32_bf16 v[144:147], v[192:195], v[80:83], v[144:147]\n\t" \
  "v_mfma_f32_16x16x32_bf16 v[152:155], v[192:195], v[84:87], v[152:155]\n\t" \
  "ds_read_b128 v[192:195], v214 offset:33024\n\t" \
  "v_mfma_f32_16x16x32_bf16 v[148:151], v[196:199], v[80:83], v[148:151]\n\t" \
  "v_mfma_f32_16x16x32_bf16 v[156:159], v[196:199], v[84:87], v[156:159]\n\t" \
  "ds_read_b128 v[196:199], v214 offset:49408\n\t" \
  "s_waitcnt lgkmcnt(6)\n\t" \
  "v_mfma_f32_16x16x32_bf16 v[144:147], v[200:203], v[88:91], v[144:147]\n\t" \
  "v_mfma_f32_16x16x32_bf16 v[152:155], v[200:203], v[92:95], v[152:155]\n\t" \
  "ds_read_b128 v[200:203], v215 offset:33024\n\t" \
  "s_add_u32 m0, %[lw], 35840\n\t" \
  "v_mfma_f32_16x16x32_bf16 v[148:151], v[204:207], v[88:91], v[148:151]\n\t" \
  "v_mfma_f32_16x16x32_bf16 v[156:159], v[204:207], v[92:95], v[156:159]\n\t" \
  "ds_read_b128 v[204:207], v215 offset:49408\n\t" \
  "global_load_lds_dwordx4 v219, s[92:93] sc1\n\t" \
  "s_cmp_lt_u32 s86, 6\n\t" \
  "s_cbranch_scc1 .Lw31%=\n\t" \
  "s_waitcnt vmcnt(6)\n\t" \
  "s_barrier\n\t" \
  ".Lg31%=:\n\t" \
  "s_add_u32 s92, s92, 65024\n\t" \
  "s_addc_u32 s93, s93, 0\n\t" \
  "s_waitcnt lgkmcnt(6)\n\t" \
  "v_mfma_f32_16x16x32_bf16 v[144:147], v[176:179], v[96:99], v[144:147]\n\t" \
  "v_mfma_f32_16x16x32_bf16 v[152:155], v[176:179], v[100:103], v[152:155]\n\t" \
  "ds_read_b128 v[176:179], v208 offset:0\n\t" \
  "v_mfma_f32_16x16x32_bf16 v[148:151], v[180:183], v[96:99], v[148:151]\n\t" \
  "v_mfma_f32_16x16x32_bf16 v[156:159], v[180:183], v[100:103], v[156:159]\n\t" \
  "ds_read_b128 v[180:183], v208 offset:16384\n\t" \
  "s_waitcnt lgkmcnt(6)\n\t" \
  "v_mfma_f32_16x16x32_bf16 v[144:147], v[184:187], v[104:107], v[144:147]\n\t" \
  "v_mfma_f32_16x16x32_bf16 v[152:155], v[184:187], v[108:111], v[152:155]\n\t" \
  "ds_read_b128 v[184:187], v209 offset:0\n\t" \
  "s_add_u32 m0, %[lw], 65536\n\t" \
  "v_mfma_f32_16x16x32_bf16 v[148:151], v[188:191], v[104:107], v[148:151]\n\t" \
  "v_mfma_f32_16x16x32_bf16 v[156:159], v[188:191], v[108:111], v[156:159]\n\t" \
  "ds_read_b128 v[188:191], v209 offset:16384\n\t" \
  "global_load_lds_dwordx4 v216, s[92:93] sc1\n\t" \
  "s_waitcnt lgkmcnt(6)\n\t" \
  "v_mfma_f32_16x16x32_bf16 v[144:147], v[192:195], v[112:115], v[144:147]\n\t" \
  "v_mfma_f32_16x16x32_bf16 v[152:155], v[192:195], v[116:119], v[152:155]\n\t" \
  "ds_read_b128 v[192:195], v210 offset:0\n\t" \
  "v_mfma_f32_16x16x32_bf16 v[148:151], v[196:199], v[112:115], v[148:151]\n\t" \
  "v_mfma_f32_16x16x32_bf16 v[156:159], v[196:199], v[116:119], v[156:159]\n\t" \
  "ds_read_b128 v[196:199], v210 offset:16384\n\t" \
  "s_waitcnt lgkmcnt(6)\n\t" \
  "v_mfma_f32_16x16x32_bf16 v[144:147], v[200:203], v[120:123], v[144:147]\n\t" \
  "v_mfma_f32_16x16x32_bf16 v[152:155], v[200:203], v[124:127], v[152:155]\n\t" \
  "ds_read_b128 v[200:203], v211 offset:0\n\t" \
  "s_add_u32 m0, %[lw], 66560\n\t" \
  "v_mfma_f32_16x16x32_bf16 v[148:151], v[204:207], v[120:123], v[148:151]\n\t" \
  "v_mfma_f32_16x16x32_bf16 v[156:159], v[204:207], v[124:127], v[156:159]\n\t" \
  "ds_read_b128 v[204:207], v211 offset:16384\n\t" \
  "global_load_lds_dwordx4 v217, s[92:93] sc1\n\t" \
  "s_add_u32 s86, s86, 1\n\t" \
  "s_add_u32 s87, s87, 1\n\t" \
  "s_cmp_eq_u32 s86, 2\n\t" \
  "s_cbranch_scc1 .Ll1%=\n\t" \
  "s_cmp_lt_u32 s86, %[end]\n\t" \
  "s_cbranch_scc1 .Lp1%=\n\t" \
  "s_cmp_lg_u32 s86, 64\n\t" \
  "s_cbranch_scc1 .Lx1%=\n\t" \
  ".Ll1%=:\n\t" \
  "s_nop 7\n\t" \
  "ds_write_b128 v220, v[152:155] offset:0\n\t" \
  "ds_write_b128 v220, v[156:159] offset:1024\n\t" \
  "s_lshr_b32 s90, s87, 7\n\t" \
  "s_xor_b32 s90, s90, %[s0]\n\t" \
  "s_and_b32 s90, s90, 1\n\t" \
  "s_lshl1_add_u32 s90, s87, s90\n\t" \
  "s_lshl_b32 s90, s90, 13\n\t" \
  "s_cmp_lt_i32 s86, 1\n\t" \
  "s_cselect_b32 s90, 0x70000000, s90\n\t" \
  "s_waitcnt lgkmcnt(0)\n\t" \
  "s_barrier\n\t" \
  "ds_read_b128 v[160:163], v221 offset:0\n\t" \
  "ds_read_b128 v[164:167], v221 offset:1024\n\t" \
  "s_waitcnt lgkmcnt(0)\n\t" \
  "v_add_f32 v160, v160, v144\n\t" \
  "v_add_f32 v161, v161, v145\n\t" \
  "v_add_f32 v162, v162, v146\n\t" \
  "v_add_f32 v163, v163, v147\n\t" \
  "v_add_f32 v164, v164, v148\n\t" \
  "v_add_f32 v165, v165, v149\n\t" \
  "v_add_f32 v166, v166, v150\n\t" \
  "v_add_f32 v167, v167, v151\n\t" \
  "v_cvt_pk_bf16_f32 v168, v160, v161\n\t" \
  "v_cvt_pk_bf16_f32 v169, v162, v163\n\t" \
  "v_cvt_pk_bf16_f32 v170, v164, v165\n\t" \
  "v_cvt_pk_bf16_f32 v171, v166, v167\n\t" \
  "buffer_store_dwordx4 v[168:171], v172, %[rt], s90 offen sc1\n\t" \
  "s_waitcnt vmcnt(0)\n\t" \
  "s_barrier\n\t" \
  "s_sub_i32 s91, s86, 1\n\t" \
  "s_max_i32 s88, s88, s91\n\t" \
  "v_mov_b32 v173, s88\n\t" \
  "buffer_store_dword v173, v174, %[rs], 0 offen sc1\n\t" \
  "s_cmp_eq_u32 s86, 2\n\t" \
  "s_cbranch_scc1 .Lp1%=\n\t" \
  "s_branch .Lx1%=\n\t" \
  ".Lf1%=:\n\t" \
  "s_nop 7\n\t" \
  "ds_write_b128 v220, v[136:139] offset:0\n\t" \
  "ds_write_b128 v220, v[140:143] offset:1024\n\t" \
  "s_lshr_b32 s90, s87, 7\n\t" \
  "s_xor_b32 s90, s90, %[s0]\n\t" \
  "s_and_b32 s90, s90, 1\n\t" \
  "s_lshl1_add_u32 s90, s87, s90\n\t" \
  "s_lshl_b32 s90, s90, 13\n\t" \
  "s_cmp_lt_i32 s86, 1\n\t" \
  "s_cselect_b32 s90, 0x70000000, s90\n\t" \
  "s_waitcnt lgkmcnt(0)\n\t" \
  "s_barrier\n\t" \
  "ds_read_b128 v[160:163], v221 offset:0\n\t" \
  "ds_read_b128 v[164:167], v221 offset:1024\n\t" \
  "s_waitcnt lgkmcnt(0)\n\t" \
  "v_add_f32 v160, v160, v128\n\t" \
  "v_add_f32 v161, v161, v129\n\t" \
  "v_add_f32 v162, v162, v130\n\t" \
  "v_add_f32 v163, v163, v131\n\t" \
  "v_add_f32 v164, v164, v132\n\t" \
  "v_add_f32 v165, v165, v133\n\t" \
  "v_add_f32 v166, v166, v134\n\t" \
  "v_add_f32 v167, v167, v135\n\t" \
  "v_cvt_pk_bf16_f32 v168, v160, v161\n\t" \
  "v_cvt_pk_bf16_f32 v169, v162, v163\n\t" \
  "v_cvt_pk_bf16_f32 v170, v164, v165\n\t" \
  "v_cvt_pk_bf16_f32 v171, v166, v167\n\t" \
  "buffer_store_dwordx4 v[168:171], v172, %[rt], s90 offen sc1\n\t" \
  "s_waitcnt vmcnt(0)\n\t" \
  "s_barrier\n\t" \
  "s_sub_i32 s91, s86, 1\n\t" \
  "s_max_i32 s88, s88, s91\n\t" \
  "v_mov_b32 v173, s88\n\t" \
  "buffer_store_dword v173, v174, %[rs], 0 offen sc1\n\t" \
  "s_branch .Lfb1%=\n\t" \
  ".Lw11%=:\n\t" \
  "s_waitcnt vmcnt(2)\n\t" \
  "s_barrier\n\t" \
  "s_sub_i32 s91, s86, 1\n\t" \
  "s_max_i32 s88, s88, s91\n\t" \
  "v_mov_b32 v173, s88\n\t" \
  "buffer_store_dword v173, v174, %[rs], 0 offen sc1\n\t" \
  "s_branch .Lg11%=\n\t" \
  ".Lw31%=:\n\t" \
  "s_waitcnt vmcnt(2)\n\t" \
  "s_barrier\n\t" \
  "s_sub_i32 s91, s86, 1\n\t" \
  "s_max_i32 s88, s88, s91\n\t" \
  "v_mov_b32 v173, s88\n\t" \
  "buffer_store_dword v173, v174, %[rs], 0 offen sc1\n\t" \
  "s_branch .Lg31%=\n\t" \
  ".Lx1%=:\n\t" \
  "s_waitcnt lgkmcnt(0)\n\t" \
  "s_setprio 0\n\t" \
  "s_mov_b32 m0, s94\n\t" \
  ".Le%=:\n\t"
#define PP_ASM_0 \
  "s_cmp_lg_u32 %[kh], 0\n\t" \
  "s_cbranch_scc1 .Lk%=\n\t" \
  "s_mov_b32 s94, m0\n\t" \
  "s_mov_b64 s[92:93], %[x0]\n\t" \
  "s_mov_b32 s86, %[g0]\n\t" \
  "s_add_u32 s87, s86, %[t0]\n\t" \
  "s_add_u32 m0, %[lw], 67584\n\t" \
  "s_nop 0\n\t" \
  "global_load_lds_dwordx4 v250, s[92:93] sc1\n\t" \
  "s_add_u32 m0, %[lw], 68608\n\t" \
  "s_nop 0\n\t" \
  "global_load_lds_dwordx4 v251, s[92:93] sc1\n\t" \
  "s_barrier\n\t" \
  "s_branch .Ls0%=\n\t" \
  ".Lp0%=:\n\t" \
  "s_waitcnt vmcnt(9)\n\t" \
  "s_barrier\n\t" \
  ".Ls0%=:\n\t" \
  "s_add_u32 s92, s92, 512\n\t" \
  "s_addc_u32 s93, s93, 0\n\t" \
  "ds_read_b128 v[176:179], v240 offset:0\n\t" \
  "ds_read_b128 v[180:183], v240 offset:16384\n\t" \
  "ds_read_b128 v[184:187], v241 offset:0\n\t" \
  "ds_read_b128 v[188:191], v241 offset:16384\n\t" \
  "s_add_u32 m0, %[lw], 98304\n\t" \
  "s_nop 0\n\t" \
  "global_load_lds_dwordx4 v248, s[92:93] sc1\n\t" \
  "ds_read_b128 v[192:195], v242 offset:0\n\t" \
  "ds_read_b128 v[196:199], v242 offset:16384\n\t" \
  "ds_read_b128 v[200:203], v243 offset:0\n\t" \
  "ds_read_b128 v[204:207], v243 offset:16384\n\t" \
  "s_add_u32 m0, %[lw], 99328\n\t" \
  "s_nop 0\n\t" \
  "global_load_lds_dwordx4 v249, s[92:93] sc1\n\t" \
  "ds_read_b128 v[208:211], v240 offset:256\n\t" \
  "ds_read_b128 v[212:215], v240 offset:16640\n\t" \
  "ds_read_b128 v[216:219], v241 offset:256\n\t" \
  "ds_read_b128 v[220:223], v241 offset:16640\n\t" \
  "s_add_u32 m0, %[lw], 100352\n\t" \
  "s_nop 0\n\t" \
  "global_load_lds_dwordx4 v250, s[92:93] sc1\n\t" \
  "ds_read_b128 v[224:227], v242 offset:256\n\t" \
  "ds_read_b128 v[228:231], v242 offset:16640\n\t" \
  "ds_read_b128 v[232:235], v243 offset:256\n\t" \
  "ds_read_b128 v[236:239], v243 offset:16640\n\t" \
  "s_add_u32 m0, %[lw], 101376\n\t" \
  "s_nop 0\n\t" \
  "global_load_lds_dwordx4 v251, s[92:93] sc1\n\t" \
  "ds_write_b128 v252, v[152:155] offset:0\n\t" \
  "ds_write_b128 v252, v[156:159] offset:1024\n\t" \
  "s_waitcnt lgkmcnt(0)\n\t" \
  "s_setprio 1\n\t" \
  "v_mfma_f32_16x16x32_bf16 v[128:131], v[0:3], v[176:179], 0\n\t" \
  "v_mfma_f32_16x16x32_bf16 v[132:135], v[4:7], v[176:179], 0\n\t" \
  "v_mfma_f32_16x16x32_bf16 v[136:139], v[0:3], v[180:183], 0\n\t" \
  "v_mfma_f32_16x16x32_bf16 v[140:143], v[4:7], v[180:183], 0\n\t" \
  "v_mfma_f32_16x16x32_bf16 v[128:131], v[8:11], v[184:187], v[128:131]\n\t" \
  "v_mfma_f32_16x16x32_bf16 v[132:135], v[12:15], v[184:187], v[132:135]\n\t" \
  "v_mfma_f32_16x16x32_bf16 v[136:139], v[8:11], v[188:191], v[136:139]\n\t" \
  "v_mfma_f32_16x16x32_bf16 v[140:143], v[12:15], v[188:191], v[140:143]\n\t" \
  "v_mfma_f32_16x16x32_bf16 v[128:131], v[16:19], v[192:195], v[128:131]\n\t" \
  "v_mfma_f32_16x16x32_bf16 v[132:135], v[20:23], v[192:195], v[132:135]\n\t" \
  "v_mfma_f32_16x16x32_bf16 v[136:139], v[16:19], v[196:199], v[136:139]\n\t" \
  "v_mfma_f32_16x16x32_bf16 v[140:143], v[20:23], v[196:199], v[140:143]\n\t" \
  "v_mfma_f32_16x16x32_bf16 v[128:131], v[24:27], v[200:203], v[128:131]\n\t" \
  "v_mfma_f32_16x16x32_bf16 v[132:135], v[28:31], v[200:203], v[132:135]\n\t" \
  "v_mfma_f32_16x16x32_bf16 v[136:139], v[24:27], v[204:207], v[136:139]\n\t" \
  "v_mfma_f32_16x16x32_bf16 v[140:143], v[28:31], v[204:207], v[140:143]\n\t" \
  "v_mfma_f32_16x16x32_bf16 v[128:131], v[32:35], v[208:211], v[128:131]\n\t" \
  "v_mfma_f32_16x16x32_bf16 v[132:135], v[36:39], v[208:211], v[132:135]\n\t" \
  "v_mfma_f32_16x16x32_bf16 v[136:139], v[32:35], v[212:215], v[136:139]\n\t" \
  "v_mfma_f32_16x16x32_bf16 v[140:143], v[36:39], v[212:215], v[140:143]\n\t" \
  "v_mfma_f32_16x16x32_bf16 v[128:131], v[40:43], v[216:219], v[128:131]\n\t" \
  "v_mfma_f32_16x16x32_bf16 v[132:135], v[44:47], v[216:219], v[132:135]\n\t" \
  "v_mfma_f32_16x16x32_bf16 v[136:139], v[40:43], v[220:223], v[136:139]\n\t" \
  "v_mfma_f32_16x16x32_bf16 v[140:143], v[44:47], v[220:223], v[140:143]\n\t" \
  "v_mfma_f32_16x16x32_bf16 v[128:131], v[48:51], v[224:227], v[128:131]\n\t" \
  "v_mfma_f32_16x16x32_bf16 v[132:135], v[52:55], v[224:227], v[132:135]\n\t" \
  "v_mfma_f32_16x16x32_bf16 v[136:139], v[48:51], v[228:231], v[136:139]\n\t" \
  "v_mfma_f32_16x16x32_bf16 v[140:143], v[52:55], v[228:231], v[140:143]\n\t" \
  "v_mfma_f32_16x16x32_bf16 v[128:131], v[56:59], v[232:235], v[128:131]\n\t" \
  "v_mfma_f32_16x16x32_bf16 v[132:135], v[60:63], v[232:235], v[132:135]\n\t" \
  "v_mfma_f32_16x16x32_bf16 v[136:139], v[56:59], v[236:239], v[136:139]\n\t" \
  "v_mfma_f32_16x16x32_bf16 v[140:143], v[60:63], v[236:239], v[140:143]\n\t" \
  "s_setprio 0\n\t" \
  "s_waitcnt vmcnt(4)\n\t" \
  "s_barrier\n\t" \
  "s_add_u32 s92, s92, 65024\n\t" \
  "s_addc_u32 s93, s93, 0\n\t" \
  "ds_read_b128 v[160:163], v253 offset:0\n\t" \
  "ds_read_b128 v[164:167], v253 offset:1024\n\t" \
  "ds_read_b128 v[176:179], v240 offset:32768\n\t" \
  "ds_read_b128 v[180:183], v240 offset:49152\n\t" \
  "ds_read_b128 v[184:187], v241 offset:32768\n\t" \
  "ds_read_b128 v[188:191], v241 offset:49152\n\t" \
  "s_add_u32 m0, %[lw], 0\n\t" \
  "s_nop 0\n\t" \
  "global_load_lds_dwordx4 v248, s[92:93] sc1\n\t" \
  "ds_read_b128 v[192:195], v242 offset:32768\n\t" \
  "ds_read_b128 v[196:199], v242 offset:49152\n\t" \
  "ds_read_b128 v[200:203], v243 offset:32768\n\t" \
  "ds_read_b128 v[204:207], v243 offset:49152\n\t" \
  "s_add_u32 m0, %[lw], 1024\n\t" \
  "s_nop 0\n\t" \
  "global_load_lds_dwordx4 v249, s[92:93] sc1\n\t" \
  "ds_read_b128 v[208:211], v240 offset:33024\n\t" \
  "ds_read_b128 v[212:215], v240 offset:49408\n\t" \
  "ds_read_b128 v[216:219], v241 offset:33024\n\t" \
  "ds_read_b128 v[220:223], v241 offset:49408\n\t" \
  "s_add_u32 m0, %[lw], 2048\n\t" \
  "s_nop 0\n\t" \
  "global_load_lds_dwordx4 v250, s[92:93] sc1\n\t" \
  "ds_read_b128 v[224:227], v242 offset:33024\n\t" \
  "ds_read_b128 v[228:231], v242 offset:49408\n\t" \
  "ds_read_b128 v[232:235], v243 offset:33024\n\t" \
  "ds_read_b128 v[236:239], v243 offset:49408\n\t" \
  "s_add_u32 m0, %[lw], 3072\n\t" \
  "s_nop 0\n\t" \
  "global_load_lds_dwordx4 v251, s[92:93] sc1\n\t" \
  "s_waitcnt lgkmcnt(15)\n\t" \
  "s_waitcnt lgkmcnt(12)\n\t" \
  "s_lshr_b32 s90, s87, 7\n\t" \
  "s_xor_b32 s90, s90, %[s0]\n\t" \
  "s_and_b32 s90, s90, 1\n\t" \
  "s_lshl1_add_u32 s90, s87, s90\n\t" \
  "s_lshl_b32 s90, s90, 13\n\t" \
  "v_add_f32 v160, v160, v144\n\t" \
  "v_add_f32 v161, v161, v145\n\t" \
  "v_add_f32 v162, v162, v146\n\t" \
  "v_add_f32 v163, v163, v147\n\t" \
  "v_add_f32 v164, v164, v148\n\t" \
  "v_add_f32 v165, v165, v149\n\t" \
  "v_add_f32 v166, v166, v150\n\t" \
  "v_add_f32 v167, v167, v151\n\t" \
  "v_cvt_pk_bf16_f32 v168, v160, v161\n\t" \
  "v_cvt_pk_bf16_f32 v169, v162, v163\n\t" \
  "v_cvt_pk_bf16_f32 v170, v164, v165\n\t" \
  "v_cvt_pk_bf16_f32 v171, v166, v167\n\t" \
  "buffer_store_dwordx4 v[168:171], v172, %[rt], s90 offen sc1\n\t" \
  "s_add_u32 s86, s86, 1\n\t" \
  "s_add_u32 s87, s87, 1\n\t" \
  "s_waitcnt lgkmcnt(0)\n\t" \
  "s_setprio 1\n\t" \
  "v_mfma_f32_16x16x32_bf16 v[128:131], v[64:67], v[176:179], v[128:131]\n\t" \
  "v_mfma_f32_16x16x32_bf16 v[132:135], v[68:71], v[176:179], v[132:135]\n\t" \
  "v_mfma_f32_16x16x32_bf16 v[136:139], v[64:67], v[180:183], v[136:139]\n\t" \
  "v_mfma_f32_16x16x32_bf16 v[140:143], v[68:71], v[180:183], v[140:143]\n\t" \
  "v_mfma_f32_16x16x32_bf16 v[128:131], v[72:75], v[184:187], v[128:131]\n\t" \
  "v_mfma_f32_16x16x32_bf16 v[132:135], v[76:79], v[184:187], v[132:135]\n\t" \
  "v_mfma_f32_16x16x32_bf16 v[136:139], v[72:75], v[188:191], v[136:139]\n\t" \
  "v_mfma_f32_16x16x32_bf16 v[140:143], v[76:79], v[188:191], v[140:143]\n\t" \
  "v_mfma_f32_16x16x32_bf16 v[128:131], v[80:83], v[192:195], v[128:131]\n\t" \
  "v_mfma_f32_16x16x32_bf16 v[132:135], v[84:87], v[192:195], v[132:135]\n\t" \
  "v_mfma_f32_16x16x32_bf16 v[136:139], v[80:83], v[196:199], v[136:139]\n\t" \
  "v_mfma_f32_16x16x32_bf16 v[140:143], v[84:87], v[196:199], v[140:143]\n\t" \
  "v_mfma_f32_16x16x32_bf16 v[128:131], v[88:91], v[200:203], v[128:131]\n\t" \
  "v_mfma_f32_16x16x32_bf16 v[132:135], v[92:95], v[200:203], v[132:135]\n\t" \
  "v_mfma_f32_16x16x32_bf16 v[136:139], v[88:91], v[204:207], v[136:139]\n\t" \
  "v_mfma_f32_16x16x32_bf16 v[140:143], v[92:95], v[204:207], v[140:143]\n\t" \
  "v_mfma_f32_16x16x32_bf16 v[128:131], v[96:99], v[208:211], v[128:131]\n\t" \
  "v_mfma_f32_16x16x32_bf16 v[132:135], v[100:103], v[208:211], v[132:135]\n\t" \
  "v_mfma_f32_16x16x32_bf16 v[136:139], v[96:99], v[212:215], v[136:139]\n\t" \
  "v_mfma_f32_16x16x32_bf16 v[140:143], v[100:103], v[212:215], v[140:143]\n\t" \
  "v_mfma_f32_16x16x32_bf16 v[128:131], v[104:107], v[216:219], v[128:131]\n\t" \
  "v_mfma_f32_16x16x32_bf16 v[132:135], v[108:111], v[216:219], v[132:135]\n\t" \
  "v_mfma_f32_16x16x32_bf16 v[136:139], v[104:107], v[220:223], v[136:139]\n\t" \
  "v_mfma_f32_16x16x32_bf16 v[140:143], v[108:111], v[220:223], v[140:143]\n\t" \
  "v_mfma_f32_16x16x32_bf16 v[128:131], v[112:115], v[224:227], v[128:131]\n\t" \
  "v_mfma_f32_16x16x32_bf16 v[132:135], v[116:119], v[224:227], v[132:135]\n\t" \
  "v_mfma_f32_16x16x32_bf16 v[136:139], v[112:115], v[228:231], v[136:139]\n\t" \
  "v_mfma_f32_16x16x32_bf16 v[140:143], v[116:119], v[228:231], v[140:143]\n\t" \
  "v_mfma_f32_16x16x32_bf16 v[128:131], v[120:123], v[232:235], v[128:131]\n\t" \
  "v_mfma_f32_16x16x32_bf16 v[132:135], v[124:127], v[232:235], v[132:135]\n\t" \
  "v_mfma_f32_16x16x32_bf16 v[136:139], v[120:123], v[236:239], v[136:139]\n\t" \
  "v_mfma_f32_16x16x32_bf16 v[140:143], v[124:127], v[236:239], v[140:143]\n\t" \
  "s_setprio 0\n\t" \
  "s_waitcnt vmcnt(9)\n\t" \
  "s_barrier\n\t" \
  "s_add_u32 s92, s92, 512\n\t" \
  "s_addc_u32 s93, s93, 0\n\t" \
  "ds_read_b128 v[176:179], v244 offset:0\n\t" \
  "ds_read_b128 v[180:183], v244 offset:16384\n\t" \
  "ds_read_b128 v[184:187], v245 offset:0\n\t" \
  "ds_read_b128 v[188:191], v245 offset:16384\n\t" \
  "s_add_u32 m0, %[lw], 32768\n\t" \
  "s_nop 0\n\t" \
  "global_load_lds_dwordx4 v248, s[92:93] sc1\n\t" \
  "ds_read_b128 v[192:195], v246 offset:0\n\t" \
  "ds_read_b128 v[196:199], v246 offset:16384\n\t" \
  "ds_read_b128 v[200:203], v247 offset:0\n\t" \
  "ds_read_b128 v[204:207], v247 offset:16384\n\t" \
  "s_add_u32 m0, %[lw], 33792\n\t" \
  "s_nop 0\n\t" \
  "global_load_lds_dwordx4 v249, s[92:93] sc1\n\t" \
  "ds_read_b128 v[208:211], v244 offset:256\n\t" \
  "ds_read_b128 v[212:215], v244 offset:16640\n\t" \
  "ds_read_b128 v[216:219], v245 offset:256\n\t" \
  "ds_read_b128 v[220:223], v245 offset:16640\n\t" \
  "s_add_u32 m0, %[lw], 34816\n\t" \
  "s_nop 0\n\t" \
  "global_load_lds_dwordx4 v250, s[92:93] sc1\n\t" \
  "ds_read_b128 v[224:227], v246 offset:256\n\t" \
  "ds_read_b128 v[228:231], v246 offset:16640\n\t" \
  "ds_read_b128 v[232:235], v247 offset:256\n\t" \
  "ds_read_b128 v[236:239], v247 offset:16640\n\t" \
  "s_add_u32 m0, %[lw], 35840\n\t" \
  "s_nop 0\n\t" \
  "global_load_lds_dwordx4 v251, s[92:93] sc1\n\t" \
  "ds_write_b128 v252, v[136:139] offset:0\n\t" \
  "ds_write_b128 v252, v[140:143] offset:1024\n\t" \
  "s_waitcnt lgkmcnt(0)\n\t" \
  "s_setprio 1\n\t" \
  "v_mfma_f32_16x16x32_bf16 v[144:147], v[0:3], v[176:179], 0\n\t" \
  "v_mfma_f32_16x16x32_bf16 v[148:151], v[4:7], v[176:179], 0\n\t" \
  "v_mfma_f32_16x16x32_bf16 v[152:155], v[0:3], v[180:183], 0\n\t" \
  "v_mfma_f32_16x16x32_bf16 v[156:159], v[4:7], v[180:183], 0\n\t" \
  "v_mfma_f32_16x16x32_bf16 v[144:147], v[8:11], v[184:187], v[144:147]\n\t" \
  "v_mfma_f32_16x16x32_bf16 v[148:151], v[12:15], v[184:187], v[148:151]\n\t" \
  "v_mfma_f32_16x16x32_bf16 v[152:155], v[8:11], v[188:191], v[152:155]\n\t" \
  "v_mfma_f32_16x16x32_bf16 v[156:159], v[12:15], v[188:191], v[156:159]\n\t" \
  "v_mfma_f32_16x16x32_bf16 v[144:147], v[16:19], v[192:195], v[144:147]\n\t" \
  "v_mfma_f32_16x16x32_bf16 v[148:151], v[20:23], v[192:195], v[148:151]\n\t" \
  "v_mfma_f32_16x16x32_bf16 v[152:155], v[16:19], v[196:199], v[152:155]\n\t" \
  "v_mfma_f32_16x16x32_bf16 v[156:159], v[20:23], v[196:199], v[156:159]\n\t" \
  "v_mfma_f32_16x16x32_bf16 v[144:147], v[24:27], v[200:203], v[144:147]\n\t" \
  "v_mfma_f32_16x16x32_bf16 v[148:151], v[28:31], v[200:203], v[148:151]\n\t" \
  "v_mfma_f32_16x16x32_bf16 v[152:155], v[24:27], v[204:207], v[152:155]\n\t" \
  "v_mfma_f32_16x16x32_bf16 v[156:159], v[28:31], v[204:207], v[156:159]\n\t" \
  "v_mfma_f32_16x16x32_bf16 v[144:147], v[32:35], v[208:211], v[144:147]\n\t" \
  "v_mfma_f32_16x16x32_bf16 v[148:151], v[36:39], v[208:211], v[148:151]\n\t" \
  "v_mfma_f32_16x16x32_bf16 v[152:155], v[32:35], v[212:215], v[152:155]\n\t" \
  "v_mfma_f32_16x16x32_bf16 v[156:159], v[36:39], v[212:215], v[156:159]\n\t" \
  "v_mfma_f32_16x16x32_bf16 v[144:147], v[40:43], v[216:219], v[144:147]\n\t" \
  "v_mfma_f32_16x16x32_bf16 v[148:151], v[44:47], v[216:219], v[148:151]\n\t" \
  "v_mfma_f32_16x16x32_bf16 v[152:155], v[40:43], v[220:223], v[152:155]\n\t" \
  "v_mfma_f32_16x16x32_bf16 v[156:159], v[44:47], v[220:223], v[156:159]\n\t" \
  "v_mfma_f32_16x16x32_bf16 v[144:147], v[48:51], v[224:227], v[144:147]\n\t" \
  "v_mfma_f32_16x16x32_bf16 v[148:151], v[52:55], v[224:227], v[148:151]\n\t" \
  "v_mfma_f32_16x16x32_bf16 v[152:155], v[48:51], v[228:231], v[152:155]\n\t" \
  "v_mfma_f32_16x16x32_bf16 v[156:159], v[52:55], v[228:231], v[156:159]\n\t" \
  "v_mfma_f32_16x16x32_bf16 v[144:147], v[56:59], v[232:235], v[144:147]\n\t" \
  "v_mfma_f32_16x16x32_bf16 v[148:151], v[60:63], v[232:235], v[148:151]\n\t" \
  "v_mfma_f32_16x16x32_bf16 v[152:155], v[56:59], v[236:239], v[152:155]\n\t" \
  "v_mfma_f32_16x16x32_bf16 v[156:159], v[60:63], v[236:239], v[156:159]\n\t" \
  "s_setprio 0\n\t" \
  "s_waitcnt vmcnt(4)\n\t" \
  "s_barrier\n\t" \
  "s_add_u32 s92, s92, 65024\n\t" \
  "s_addc_u32 s93, s93, 0\n\t" \
  "ds_read_b128 v[160:163], v253 offset:0\n\t" \
  "ds_read_b128 v[164:167], v253 offset:1024\n\t" \
  "ds_read_b128 v[176:179], v244 offset:32768\n\t" \
  "ds_read_b128 v[180:183], v244 offset:49152\n\t" \
  "ds_read_b128 v[184:187], v245 offset:32768\n\t" \
  "ds_read_b128 v[188:191], v245 offset:49152\n\t" \
  "s_add_u32 m0, %[lw], 65536\n\t" \
  "s_nop 0\n\t" \
  "global_load_lds_dwordx4 v248, s[92:93] sc1\n\t" \
  "ds_read_b128 v[192:195], v246 offset:32768\n\t" \
  "ds_read_b128 v[196:199], v246 offset:49152\n\t" \
  "ds_read_b128 v[200:203], v247 offset:32768\n\t" \
  "ds_read_b128 v[204:207], v247 offset:49152\n\t" \
  "s_add_u32 m0, %[lw], 66560\n\t" \
  "s_nop 0\n\t" \
  "global_load_lds_dwordx4 v249, s[92:93] sc1\n\t" \
  "ds_read_b128 v[208:211], v244 offset:33024\n\t" \
  "ds_read_b128 v[212:215], v244 offset:49408\n\t" \
  "ds_read_b128 v[216:219], v245 offset:33024\n\t" \
  "ds_read_b128 v[220:223], v245 offset:49408\n\t" \
  "s_add_u32 m0, %[lw], 67584\n\t" \
  "s_nop 0\n\t" \
  "global_load_lds_dwordx4 v250, s[92:93] sc1\n\t" \
  "ds_read_b128 v[224:227], v246 offset:33024\n\t" \
  "ds_read_b128 v[228:231], v246 offset:49408\n\t" \
  "ds_read_b128 v[232:235], v247 offset:33024\n\t" \
  "ds_read_b128 v[236:239], v247 offset:49408\n\t" \
  "s_add_u32 m0, %[lw], 68608\n\t" \
  "s_nop 0\n\t" \
  "global_load_lds_dwordx4 v251, s[92:93] sc1\n\t" \
  "s_waitcnt lgkmcnt(15)\n\t" \
  "s_waitcnt lgkmcnt(12)\n\t" \
  "s_lshr_b32 s90, s87, 7\n\t" \
  "s_xor_b32 s90, s90, %[s0]\n\t" \
  "s_and_b32 s90, s90, 1\n\t" \
  "s_lshl1_add_u32 s90, s87, s90\n\t" \
  "s_lshl_b32 s90, s90, 13\n\t" \
  "v_add_f32 v160, v160, v128\n\t" \
  "v_add_f32 v161, v161, v129\n\t" \
  "v_add_f32 v162, v162, v130\n\t" \
  "v_add_f32 v163, v163, v131\n\t" \
  "v_add_f32 v164, v164, v132\n\t" \
  "v_add_f32 v165, v165, v133\n\t" \
  "v_add_f32 v166, v166, v134\n\t" \
  "v_add_f32 v167, v167, v135\n\t" \
  "v_cvt_pk_bf16_f32 v168, v160, v161\n\t" \
  "v_cvt_pk_bf16_f32 v169, v162, v163\n\t" \
  "v_cvt_pk_bf16_f32 v170, v164, v165\n\t" \
  "v_cvt_pk_bf16_f32 v171, v166, v167\n\t" \
  "buffer_store_dwordx4 v[168:171], v172, %[rt], s90 offen sc1\n\t" \
  "s_add_u32 s86, s86, 1\n\t" \
  "s_add_u32 s87, s87, 1\n\t" \
  "s_waitcnt lgkmcnt(0)\n\t" \
  "s_setprio 1\n\t" \
  "v_mfma_f32_16x16x32_bf16 v[144:147], v[64:67], v[176:179], v[144:147]\n\t" \
  "v_mfma_f32_16x16x32_bf16 v[148:151], v[68:71], v[176:179], v[148:151]\n\t" \
  "v_mfma_f32_16x16x32_bf16 v[152:155], v[64:67], v[180:183], v[152:155]\n\t" \
  "v_mfma_f32_16x16x32_bf16 v[156:159], v[68:71], v[180:183], v[156:159]\n\t" \
  "v_mfma_f32_16x16x32_bf16 v[144:147], v[72:75], v[184:187], v[144:147]\n\t" \
  "v_mfma_f32_16x16x32_bf16 v[148:151], v[76:79], v[184:187], v[148:151]\n\t" \
  "v_mfma_f32_16x16x32_bf16 v[152:155], v[72:75], v[188:191], v[152:155]\n\t" \
  "v_mfma_f32_16x16x32_bf16 v[156:159], v[76:79], v[188:191], v[156:159]\n\t" \
  "v_mfma_f32_16x16x32_bf16 v[144:147], v[80:83], v[192:195], v[144:147]\n\t" \
  "v_mfma_f32_16x16x32_bf16 v[148:151], v[84:87], v[192:195], v[148:151]\n\t" \
  "v_mfma_f32_16x16x32_bf16 v[152:155], v[80:83], v[196:199], v[152:155]\n\t" \
  "v_mfma_f32_16x16x32_bf16 v[156:159], v[84:87], v[196:199], v[156:159]\n\t" \
  "v_mfma_f32_16x16x32_bf16 v[144:147], v[88:91], v[200:203], v[144:147]\n\t" \
  "v_mfma_f32_16x16x32_bf16 v[148:151], v[92:95], v[200:203], v[148:151]\n\t" \
  "v_mfma_f32_16x16x32_bf16 v[152:155], v[88:91], v[204:207], v[152:155]\n\t" \
  "v_mfma_f32_16x16x32_bf16 v[156:159], v[92:95], v[204:207], v[156:159]\n\t" \
  "v_mfma_f32_16x16x32_bf16 v[144:147], v[96:99], v[208:211], v[144:147]\n\t" \
  "v_mfma_f32_16x16x32_bf16 v[148:151], v[100:103], v[208:211], v[148:151]\n\t" \
  "v_mfma_f32_16x16x32_bf16 v[152:155], v[96:99], v[212:215], v[152:155]\n\t" \
  "v_mfma_f32_16x16x32_bf16 v[156:159], v[100:103], v[212:215], v[156:159]\n\t" \
  "v_mfma_f32_16x16x32_bf16 v[144:147], v[104:107], v[216:219], v[144:147]\n\t" \
  "v_mfma_f32_16x16x32_bf16 v[148:151], v[108:111], v[216:219], v[148:151]\n\t" \
  "v_mfma_f32_16x16x32_bf16 v[152:155], v[104:107], v[220:223], v[152:155]\n\t" \
  "v_mfma_f32_16x16x32_bf16 v[156:159], v[108:111], v[220:223], v[156:159]\n\t" \
  "v_mfma_f32_16x16x32_bf16 v[144:147], v[112:115], v[224:227], v[144:147]\n\t" \
  "v_mfma_f32_16x16x32_bf16 v[148:151], v[116:119], v[224:227], v[148:151]\n\t" \
  "v_mfma_f32_16x16x32_bf16 v[152:155], v[112:115], v[228:231], v[152:155]\n\t" \
  "v_mfma_f32_16x16x32_bf16 v[156:159], v[116:119], v[228:231], v[156:159]\n\t" \
  "v_mfma_f32_16x16x32_bf16 v[144:147], v[120:123], v[232:235], v[144:147]\n\t" \
  "v_mfma_f32_16x16x32_bf16 v[148:151], v[124:127], v[232:235], v[148:151]\n\t" \
  "v_mfma_f32_16x16x32_bf16 v[152:155], v[120:123], v[236:239], v[152:155]\n\t" \
  "v_mfma_f32_16x16x32_bf16 v[156:159], v[124:127], v[236:239], v[156:159]\n\t" \
  "s_setprio 0\n\t" \
  "s_cmp_lt_u32 s86, 64\n\t" \
  "s_cbranch_scc1 .Lp0%=\n\t" \
  "s_barrier\n\t" \
  "s_nop 2\n\t" \
  "ds_write_b128 v252, v[152:155] offset:0\n\t" \
  "ds_write_b128 v252, v[156:159] offset:1024\n\t" \
  "s_waitcnt lgkmcnt(0)\n\t" \
  "s_barrier\n\t" \
  "ds_read_b128 v[160:163], v253 offset:0\n\t" \
  "ds_read_b128 v[164:167], v253 offset:1024\n\t" \
  "s_waitcnt lgkmcnt(0)\n\t" \
  "s_lshr_b32 s90, s87, 7\n\t" \
  "s_xor_b32 s90, s90, %[s0]\n\t" \
  "s_and_b32 s90, s90, 1\n\t" \
  "s_lshl1_add_u32 s90, s87, s90\n\t" \
  "s_lshl_b32 s90, s90, 13\n\t" \
  "v_add_f32 v160, v160, v144\n\t" \
  "v_add_f32 v161, v161, v145\n\t" \
  "v_add_f32 v162, v162, v146\n\t" \
  "v_add_f32 v163, v163, v147\n\t" \
  "v_add_f32 v164, v164, v148\n\t" \
  "v_add_f32 v165, v165, v149\n\t" \
  "v_add_f32 v166, v166, v150\n\t" \
  "v_add_f32 v167, v167, v151\n\t" \
  "v_cvt_pk_bf16_f32 v168, v160, v161\n\t" \
  "v_cvt_pk_bf16_f32 v169, v162, v163\n\t" \
  "v_cvt_pk_bf16_f32 v170, v164, v165\n\t" \
  "v_cvt_pk_bf16_f32 v171, v166, v167\n\t" \
  "buffer_store_dwordx4 v[168:171], v172, %[rt], s90 offen sc1\n\t" \
  "s_waitcnt vmcnt(0)\n\t" \
  "s_barrier\n\t" \
  "s_mov_b32 m0, s94\n\t" \
  "s_branch .Le%=\n\t" \
  ".Lk%=:\n\t" \
  "s_mov_b32 s94, m0\n\t" \
  "s_mov_b64 s[92:93], %[x0]\n\t" \
  "s_mov_b32 s86, %[g0]\n\t" \
  "s_add_u32 s87, s86, %[t0]\n\t" \
  "s_add_u32 m0, %[lw], 67584\n\t" \
  "s_nop 0\n\t" \
  "global_load_lds_dwordx4 v250, s[92:93] sc1\n\t" \
  "s_add_u32 m0, %[lw], 68608\n\t" \
  "s_nop 0\n\t" \
  "global_load_lds_dwordx4 v251, s[92:93] sc1\n\t" \
  "s_barrier\n\t" \
  "s_branch .Ls1%=\n\t" \
  ".Lp1%=:\n\t" \
  "s_waitcnt vmcnt(10)\n\t" \
  "s_barrier\n\t" \
  "s_setprio 2\n\t" \
  "v_mfma_f32_16x16x32_bf16 v[144:147], v[64:67], v[176:179], v[144:147]\n\t" \
  "v_mfma_f32_16x16x32_bf16 v[148:151], v[68:71], v[176:179], v[148:151]\n\t" \
  "v_mfma_f32_16x16x32_bf16 v[152:155], v[64:67], v[180:183], v[152:155]\n\t" \
  "v_mfma_f32_16x16x32_bf16 v[156:159], v[68:71], v[180:183], v[156:159]\n\t" \
  "v_mfma_f32_16x16x32_bf16 v[144:147], v[72:75], v[184:187], v[144:147]\n\t" \
  "v_mfma_f32_16x16x32_bf16 v[148:151], v[76:79], v[184:187], v[148:151]\n\t" \
  "v_mfma_f32_16x16x32_bf16 v[152:155], v[72:75], v[188:191], v[152:155]\n\t" \
  "v_mfma_f32_16x16x32_bf16 v[156:159], v[76:79], v[188:191], v[156:159]\n\t" \
  "v_mfma_f32_16x16x32_bf16 v[144:147], v[80:83], v[192:195], v[144:147]\n\t" \
  "v_mfma_f32_16x16x32_bf16 v[148:151], v[84:87], v[192:195], v[148:151]\n\t" \
  "v_mfma_f32_16x16x32_bf16 v[152:155], v[80:83], v[196:199], v[152:155]\n\t" \
  "v_mfma_f32_16x16x32_bf16 v[156:159], v[84:87], v[196:199], v[156:159]\n\t" \
  "v_mfma_f32_16x16x32_bf16 v[144:147], v[88:91], v[200:203], v[144:147]\n\t" \
  "v_mfma_f32_16x16x32_bf16 v[148:151], v[92:95], v[200:203], v[148:151]\n\t" \
  "v_mfma_f32_16x16x32_bf16 v[152:155], v[88:91], v[204:207], v[152:155]\n\t" \
  "v_mfma_f32_16x16x32_bf16 v[156:159], v[92:95], v[204:207], v[156:159]\n\t" \
  "v_mfma_f32_16x16x32_bf16 v[144:147], v[96:99], v[208:211], v[144:147]\n\t" \
  "v_mfma_f32_16x16x32_bf16 v[148:151], v[100:103], v[208:211], v[148:151]\n\t" \
  "v_mfma_f32_16x16x32_bf16 v[152:155], v[96:99], v[212:215], v[152:155]\n\t" \
  "v_mfma_f32_16x16x32_bf16 v[156:159], v[100:103], v[212:215], v[156:159]\n\t" \
  "v_mfma_f32_16x16x32_bf16 v[144:147], v[104:107], v[216:219], v[144:147]\n\t" \
  "v_mfma_f32_16x16x32_bf16 v[148:151], v[108:111], v[216:219], v[148:151]\n\t" \
  "v_mfma_f32_16x16x32_bf16 v[152:155], v[104:107], v[220:223], v[152:155]\n\t" \
  "v_mfma_f32_16x16x32_bf16 v[156:159], v[108:111], v[220:223], v[156:159]\n\t" \
  "v_mfma_f32_16x16x32_bf16 v[144:147], v[112:115], v[224:227], v[144:147]\n\t" \
  "v_mfma_f32_16x16x32_bf16 v[148:151], v[116:119], v[224:227], v[148:151]\n\t" \
  "v_mfma_f32_16x16x32_bf16 v[152:155], v[112:115], v[228:231], v[152:155]\n\t" \
  "v_mfma_f32_16x16x32_bf16 v[156:159], v[116:119], v[228:231], v[156:159]\n\t" \
  "v_mfma_f32_16x16x32_bf16 v[144:147], v[120:123], v[232:235], v[144:147]\n\t" \
  "v_mfma_f32_16x16x32_bf16 v[148:151], v[124:127], v[232:235], v[148:151]\n\t" \
  "v_mfma_f32_16x16x32_bf16 v[152:155], v[120:123], v[236:239], v[152:155]\n\t" \
  "v_mfma_f32_16x16x32_bf16 v[156:159], v[124:127], v[236:239], v[156:159]\n\t" \
  "s_setprio 0\n\t" \
  ".Ls1%=:\n\t" \
  "s_add_u32 s92, s92, 512\n\t" \
  "s_addc_u32 s93, s93, 0\n\t" \
  "ds_read_b128 v[176:179], v240 offset:0\n\t" \
  "ds_read_b128 v[180:183], v240 offset:16384\n\t" \
  "ds_read_b128 v[184:187], v241 offset:0\n\t" \
  "ds_read_b128 v[188:191], v241 offset:16384\n\t" \
  "s_add_u32 m0, %[lw], 98304\n\t" \
  "s_nop 0\n\t" \
  "global_load_lds_dwordx4 v248, s[92:93] sc1\n\t" \
  "ds_read_b128 v[192:195], v242 offset:0\n\t" \
  "ds_read_b128 v[196:199], v242 offset:16384\n\t" \
  "ds_read_b128 v[200:203], v243 offset:0\n\t" \
  "ds_read_b128 v[204:207], v243 offset:16384\n\t" \
  "s_add_u32 m0, %[lw], 99328\n\t" \
  "s_nop 0\n\t" \
  "global_load_lds_dwordx4 v249, s[92:93] sc1\n\t" \
  "ds_read_b128 v[208:211], v240 offset:256\n\t" \
  "ds_read_b128 v[212:215], v240 offset:16640\n\t" \
  "ds_read_b128 v[216:219], v241 offset:256\n\t" \
  "ds_read_b128 v[220:223], v241 offset:16640\n\t" \
  "s_add_u32 m0, %[lw], 100352\n\t" \
  "s_nop 0\n\t" \
  "global_load_lds_dwordx4 v250, s[92:93] sc1\n\t" \
  "ds_read_b128 v[224:227], v242 offset:256\n\t" \
  "ds_read_b128 v[228:231], v242 offset:16640\n\t" \
  "ds_read_b128 v[232:235], v243 offset:256\n\t" \
  "ds_read_b128 v[236:239], v243 offset:16640\n\t" \
  "s_add_u32 m0, %[lw], 101376\n\t" \
  "s_nop 0\n\t" \
  "global_load_lds_dwordx4 v251, s[92:93] sc1\n\t" \
  "ds_write_b128 v252, v[152:155] offset:0\n\t" \
  "ds_write_b128 v252, v[156:159] offset:1024\n\t" \
  "s_waitcnt lgkmcnt(0)\n\t" \
  "s_waitcnt vmcnt(4)\n\t" \
  "s_barrier\n\t" \
  "s_setprio 2\n\t" \
  "v_mfma_f32_16x16x32_bf16 v[128:131], v[0:3], v[176:179], 0\n\t" \
  "v_mfma_f32_16x16x32_bf16 v[132:135], v[4:7], v[176:179], 0\n\t" \
  "v_mfma_f32_16x16x32_bf16 v[136:139], v[0:3], v[180:183], 0\n\t" \
  "v_mfma_f32_16x16x32_bf16 v[140:143], v[4:7], v[180:183], 0\n\t" \
  "v_mfma_f32_16x16x32_bf16 v[128:131], v[8:11], v[184:187], v[128:131]\n\t" \
  "v_mfma_f32_16x16x32_bf16 v[132:135], v[12:15], v[184:187], v[132:135]\n\t" \
  "v_mfma_f32_16x16x32_bf16 v[136:139], v[8:11], v[188:191], v[136:139]\n\t" \
  "v_mfma_f32_16x16x32_bf16 v[140:143], v[12:15], v[188:191], v[140:143]\n\t" \
  "v_mfma_f32_16x16x32_bf16 v[128:131], v[16:19], v[192:195], v[128:131]\n\t" \
  "v_mfma_f32_16x16x32_bf16 v[132:135], v[20:23], v[192:195], v[132:135]\n\t" \
  "v_mfma_f32_16x16x32_bf16 v[136:139], v[16:19], v[196:199], v[136:139]\n\t" \
  "v_mfma_f32_16x16x32_bf16 v[140:143], v[20:23], v[196:199], v[140:143]\n\t" \
  "v_mfma_f32_16x16x32_bf16 v[128:131], v[24:27], v[200:203], v[128:131]\n\t" \
  "v_mfma_f32_16x16x32_bf16 v[132:135], v[28:31], v[200:203], v[132:135]\n\t" \
  "v_mfma_f32_16x16x32_bf16 v[136:139], v[24:27], v[204:207], v[136:139]\n\t" \
  "v_mfma_f32_16x16x32_bf16 v[140:143], v[28:31], v[204:207], v[140:143]\n\t" \
  "v_mfma_f32_16x16x32_bf16 v[128:131], v[32:35], v[208:211], v[128:131]\n\t" \
  "v_mfma_f32_16x16x32_bf16 v[132:135], v[36:39], v[208:211], v[132:135]\n\t" \
  "v_mfma_f32_16x16x32_bf16 v[136:139], v[32:35], v[212:215], v[136:139]\n\t" \
  "v_mfma_f32_16x16x32_bf16 v[140:143], v[36:39], v[212:215], v[140:143]\n\t" \
  "v_mfma_f32_16x16x32_bf16 v[128:131], v[40:43], v[216:219], v[128:131]\n\t" \
  "v_mfma_f32_16x16x32_bf16 v[132:135], v[44:47], v[216:219], v[132:135]\n\t" \
  "v_mfma_f32_16x16x32_bf16 v[136:139], v[40:43], v[220:223], v[136:139]\n\t" \
  "v_mfma_f32_16x16x32_bf16 v[140:143], v[44:47], v[220:223], v[140:143]\n\t" \
  "v_mfma_f32_16x16x32_bf16 v[128:131], v[48:51], v[224:227], v[128:131]\n\t" \
  "v_mfma_f32_16x16x32_bf16 v[132:135], v[52:55], v[224:227], v[132:135]\n\t" \
  "v_mfma_f32_16x16x32_bf16 v[136:139], v[48:51], v[228:231], v[136:139]\n\t" \
  "v_mfma_f32_16x16x32_bf16 v[140:143], v[52:55], v[228:231], v[140:143]\n\t" \
  "v_mfma_f32_16x16x32_bf16 v[128:131], v[56:59], v[232:235], v[128:131]\n\t" \
  "v_mfma_f32_16x16x32_bf16 v[132:135], v[60:63], v[232:235], v[132:135]\n\t" \
  "v_mfma_f32_16x16x32_bf16 v[136:139], v[56:59], v[236:239], v[136:139]\n\t" \
  "v_mfma_f32_16x16x32_bf16 v[140:143], v[60:63], v[236:239], v[140:143]\n\t" \
  "s_setprio 0\n\t" \
  "s_add_u32 s92, s92, 65024\n\t" \
  "s_addc_u32 s93, s93, 0\n\t" \
  "ds_read_b128 v[160:163], v253 offset:0\n\t" \
  "ds_read_b128 v[164:167], v253 offset:1024\n\t" \
  "ds_read_b128 v[176:179], v240 offset:32768\n\t" \
  "ds_read_b128 v[180:183], v240 offset:49152\n\t" \
  "ds_read_b128 v[184:187], v241 offset:32768\n\t" \
  "ds_read_b128 v[188:191], v241 offset:49152\n\t" \
  "s_add_u32 m0, %[lw], 0\n\t" \
  "s_nop 0\n\t" \
  "global_load_lds_dwordx4 v248, s[92:93] sc1\n\t" \
  "ds_read_b128 v[192:195], v242 offset:32768\n\t" \
  "ds_read_b128 v[196:199], v242 offset:49152\n\t" \
  "ds_read_b128 v[200:203], v243 offset:32768\n\t" \
  "ds_read_b128 v[204:207], v243 offset:49152\n\t" \
  "s_add_u32 m0, %[lw], 1024\n\t" \
  "s_nop 0\n\t" \
  "global_load_lds_dwordx4 v249, s[92:93] sc1\n\t" \
  "ds_read_b128 v[208:211], v240 offset:33024\n\t" \
  "ds_read_b128 v[212:215], v240 offset:49408\n\t" \
  "ds_read_b128 v[216:219], v241 offset:33024\n\t" \
  "ds_read_b128 v[220:223], v241 offset:49408\n\t" \
  "s_add_u32 m0, %[lw], 2048\n\t" \
  "s_nop 0\n\t" \
  "global_load_lds_dwordx4 v250, s[92:93] sc1\n\t" \
  "ds_read_b128 v[224:227], v242 offset:33024\n\t" \
  "ds_read_b128 v[228:231], v242 offset:49408\n\t" \
  "ds_read_b128 v[232:235], v243 offset:33024\n\t" \
  "ds_read_b128 v[236:239], v243 offset:49408\n\t" \
  "s_add_u32 m0, %[lw], 3072\n\t" \
  "s_nop 0\n\t" \
  "global_load_lds_dwordx4 v251, s[92:93] sc1\n\t" \
  "s_waitcnt lgkmcnt(15)\n\t" \
  "s_waitcnt lgkmcnt(12)\n\t" \
  "s_sub_i32 s91, s86, 2\n\t" \
  "v_mov_b32 v173, s91\n\t" \
  "buffer_store_dword v173, v174, %[rs], 0 offen sc1\n\t" \
  "s_lshr_b32 s90, s87, 7\n\t" \
  "s_xor_b32 s90, s90, %[s0]\n\t" \
  "s_and_b32 s90, s90, 1\n\t" \
  "s_lshl1_add_u32 s90, s87, s90\n\t" \
  "s_lshl_b32 s90, s90, 13\n\t" \
  "v_add_f32 v160, v160, v144\n\t" \
  "v_add_f32 v161, v161, v145\n\t" \
  "v_add_f32 v162, v162, v146\n\t" \
  "v_add_f32 v163, v163, v147\n\t" \
  "v_add_f32 v164, v164, v148\n\t" \
  "v_add_f32 v165, v165, v149\n\t" \
  "v_add_f32 v166, v166, v150\n\t" \
  "v_add_f32 v167, v167, v151\n\t" \
  "v_cvt_pk_bf16_f32 v168, v160, v161\n\t" \
  "v_cvt_pk_bf16_f32 v169, v162, v163\n\t" \
  "v_cvt_pk_bf16_f32 v170, v164, v165\n\t" \
  "v_cvt_pk_bf16_f32 v171, v166, v167\n\t" \
  "buffer_store_dwordx4 v[168:171], v172, %[rt], s90 offen sc1\n\t" \
  "s_add_u32 s86, s86, 1\n\t" \
  "s_add_u32 s87, s87, 1\n\t" \
  "s_waitcnt lgkmcnt(0)\n\t" \
  "s_waitcnt vmcnt(10)\n\t" \
  "s_barrier\n\t" \
  "s_setprio 2\n\t" \
  "v_mfma_f32_16x16x32_bf16 v[128:131], v[64:67], v[176:179], v[128:131]\n\t" \
  "v_mfma_f32_16x16x32_bf16 v[132:135], v[68:71], v[176:179], v[132:135]\n\t" \
  "v_mfma_f32_16x16x32_bf16 v[136:139], v[64:67], v[180:183], v[136:139]\n\t" \
  "v_mfma_f32_16x16x32_bf16 v[140:143], v[68:71], v[180:183], v[140:143]\n\t" \
  "v_mfma_f32_16x16x32_bf16 v[128:131], v[72:75], v[184:187], v[128:131]\n\t" \
  "v_mfma_f32_16x16x32_bf16 v[132:135], v[76:79], v[184:187], v[132:135]\n\t" \
  "v_mfma_f32_16x16x32_bf16 v[136:139], v[72:75], v[188:191], v[136:139]\n\t" \
  "v_mfma_f32_16x16x32_bf16 v[140:143], v[76:79], v[188:191], v[140:143]\n\t" \
  "v_mfma_f32_16x16x32_bf16 v[128:131], v[80:83], v[192:195], v[128:131]\n\t" \
  "v_mfma_f32_16x16x32_bf16 v[132:135], v[84:87], v[192:195], v[132:135]\n\t" \
  "v_mfma_f32_16x16x32_bf16 v[136:139], v[80:83], v[196:199], v[136:139]\n\t" \
  "v_mfma_f32_16x16x32_bf16 v[140:143], v[84:87], v[196:199], v[140:143]\n\t" \
  "v_mfma_f32_16x16x32_bf16 v[128:131], v[88:91], v[200:203], v[128:131]\n\t" \
  "v_mfma_f32_16x16x32_bf16 v[132:135], v[92:95], v[200:203], v[132:135]\n\t" \
  "v_mfma_f32_16x16x32_bf16 v[136:139], v[88:91], v[204:207], v[136:139]\n\t" \
  "v_mfma_f32_16x16x32_bf16 v[140:143], v[92:95], v[204:207], v[140:143]\n\t" \
  "v_mfma_f32_16x16x32_bf16 v[128:131], v[96:99], v[208:211], v[128:131]\n\t" \
  "v_mfma_f32_16x16x32_bf16 v[132:135], v[100:103], v[208:211], v[132:135]\n\t" \
  "v_mfma_f32_16x16x32_bf16 v[136:139], v[96:99], v[212:215], v[136:139]\n\t" \
  "v_mfma_f32_16x16x32_bf16 v[140:143], v[100:103], v[212:215], v[140:143]\n\t" \
  "v_mfma_f32_16x16x32_bf16 v[128:131], v[104:107], v[216:219], v[128:131]\n\t" \
  "v_mfma_f32_16x16x32_bf16 v[132:135], v[108:111], v[216:219], v[132:135]\n\t" \
  "v_mfma_f32_16x16x32_bf16 v[136:139], v[104:107], v[220:223], v[136:139]\n\t" \
  "v_mfma_f32_16x16x32_bf16 v[140:143], v[108:111], v[220:223], v[140:143]\n\t" \
  "v_mfma_f32_16x16x32_bf16 v[128:131], v[112:115], v[224:227], v[128:131]\n\t" \
  "v_mfma_f32_16x16x32_bf16 v[132:135], v[116:119], v[224:227], v[132:135]\n\t" \
  "v_mfma_f32_16x16x32_bf16 v[136:139], v[112:115], v[228:231], v[136:139]\n\t" \
  "v_mfma_f32_16x16x32_bf16 v[140:143], v[116:119], v[228:231], v[140:143]\n\t" \
  "v_mfma_f32_16x16x32_bf16 v[128:131], v[120:123], v[232:235], v[128:131]\n\t" \
  "v_mfma_f32_16x16x32_bf16 v[132:135], v[124:127], v[232:235], v[132:135]\n\t" \
  "v_mfma_f32_16x16x32_bf16 v[136:139], v[120:123], v[236:239], v[136:139]\n\t" \
  "v_mfma_f32_16x16x32_bf16 v[140:143], v[124:127], v[236:239], v[140:143]\n\t" \
  "s_setprio 0\n\t" \
  "s_add_u32 s92, s92, 512\n\t" \
  "s_addc_u32 s93, s93, 0\n\t" \
  "ds_read_b128 v[176:179], v244 offset:0\n\t" \
  "ds_read_b128 v[180:183], v244 offset:16384\n\t" \
  "ds_read_b128 v[184:187], v245 offset:0\n\t" \
  "ds_read_b128 v[188:191], v245 offset:16384\n\t" \
  "s_add_u32 m0, %[lw], 32768\n\t" \
  "s_nop 0\n\t" \
  "global_load_lds_dwordx4 v248, s[92:93] sc1\n\t" \
  "ds_read_b128 v[192:195], v246 offset:0\n\t" \
  "ds_read_b128 v[196:199], v246 offset:16384\n\t" \
  "ds_read_b128 v[200:203], v247 offset:0\n\t" \
  "ds_read_b128 v[204:207], v247 offset:16384\n\t" \
  "s_add_u32 m0, %[lw], 33792\n\t" \
  "s_nop 0\n\t" \
  "global_load_lds_dwordx4 v249, s[92:93] sc1\n\t" \
  "ds_read_b128 v[208:211], v244 offset:256\n\t" \
  "ds_read_b128 v[212:215], v244 offset:16640\n\t" \
  "ds_read_b128 v[216:219], v245 offset:256\n\t" \
  "ds_read_b128 v[220:223], v245 offset:16640\n\t" \
  "s_add_u32 m0, %[lw], 34816\n\t" \
  "s_nop 0\n\t" \
  "global_load_lds_dwordx4 v250, s[92:93] sc1\n\t" \
  "ds_read_b128 v[224:227], v246 offset:256\n\t" \
  "ds_read_b128 v[228:231], v246 offset:16640\n\t" \
  "ds_read_b128 v[232:235], v247 offset:256\n\t" \
  "ds_read_b128 v[236:239], v247 offset:16640\n\t" \
  "s_add_u32 m0, %[lw], 35840\n\t" \
  "s_nop 0\n\t" \
  "global_load_lds_dwordx4 v251, s[92:93] sc1\n\t" \
  "ds_write_b128 v252, v[136:139] offset:0\n\t" \
  "ds_write_b128 v252, v[140:143] offset:1024\n\t" \
  "s_waitcnt lgkmcnt(0)\n\t" \
  "s_waitcnt vmcnt(4)\n\t" \
  "s_barrier\n\t" \
  "s_setprio 2\n\t" \
  "v_mfma_f32_16x16x32_bf16 v[144:147], v[0:3], v[176:179], 0\n\t" \
  "v_mfma_f32_16x16x32_bf16 v[148:151], v[4:7], v[176:179], 0\n\t" \
  "v_mfma_f32_16x16x32_bf16 v[152:155], v[0:3], v[180:183], 0\n\t" \
  "v_mfma_f32_16x16x32_bf16 v[156:159], v[4:7], v[180:183], 0\n\t" \
  "v_mfma_f32_16x16x32_bf16 v[144:147], v[8:11], v[184:187], v[144:147]\n\t" \
  "v_mfma_f32_16x16x32_bf16 v[148:151], v[12:15], v[184:187], v[148:151]\n\t" \
  "v_mfma_f32_16x16x32_bf16 v[152:155], v[8:11], v[188:191], v[152:155]\n\t" \
  "v_mfma_f32_16x16x32_bf16 v[156:159], v[12:15], v[188:191], v[156:159]\n\t" \
  "v_mfma_f32_16x16x32_bf16 v[144:147], v[16:19], v[192:195], v[144:147]\n\t" \
  "v_mfma_f32_16x16x32_bf16 v[148:151], v[20:23], v[192:195], v[148:151]\n\t" \
  "v_mfma_f32_16x16x32_bf16 v[152:155], v[16:19], v[196:199], v[152:155]\n\t" \
  "v_mfma_f32_16x16x32_bf16 v[156:159], v[20:23], v[196:199], v[156:159]\n\t" \
  "v_mfma_f32_16x16x32_bf16 v[144:147], v[24:27], v[200:203], v[144:147]\n\t" \
  "v_mfma_f32_16x16x32_bf16 v[148:151], v[28:31], v[200:203], v[148:151]\n\t" \
  "v_mfma_f32_16x16x32_bf16 v[152:155], v[24:27], v[204:207], v[152:155]\n\t" \
  "v_mfma_f32_16x16x32_bf16 v[156:159], v[28:31], v[204:207], v[156:159]\n\t" \
  "v_mfma_f32_16x16x32_bf16 v[144:147], v[32:35], v[208:211], v[144:147]\n\t" \
  "v_mfma_f32_16x16x32_bf16 v[148:151], v[36:39], v[208:211], v[148:151]\n\t" \
  "v_mfma_f32_16x16x32_bf16 v[152:155], v[32:35], v[212:215], v[152:155]\n\t" \
  "v_mfma_f32_16x16x32_bf16 v[156:159], v[36:39], v[212:215], v[156:159]\n\t" \
  "v_mfma_f32_16x16x32_bf16 v[144:147], v[40:43], v[216:219], v[144:147]\n\t" \
  "v_mfma_f32_16x16x32_bf16 v[148:151], v[44:47], v[216:219], v[148:151]\n\t" \
  "v_mfma_f32_16x16x32_bf16 v[152:155], v[40:43], v[220:223], v[152:155]\n\t" \
  "v_mfma_f32_16x16x32_bf16 v[156:159], v[44:47], v[220:223], v[156:159]\n\t" \
  "v_mfma_f32_16x16x32_bf16 v[144:147], v[48:51], v[224:227], v[144:147]\n\t" \
  "v_mfma_f32_16x16x32_bf16 v[148:151], v[52:55], v[224:227], v[148:151]\n\t" \
  "v_mfma_f32_16x16x32_bf16 v[152:155], v[48:51], v[228:231], v[152:155]\n\t" \
  "v_mfma_f32_16x16x32_bf16 v[156:159], v[52:55], v[228:231], v[156:159]\n\t" \
  "v_mfma_f32_16x16x32_bf16 v[144:147], v[56:59], v[232:235], v[144:147]\n\t" \
  "v_mfma_f32_16x16x32_bf16 v[148:151], v[60:63], v[232:235], v[148:151]\n\t" \
  "v_mfma_f32_16x16x32_bf16 v[152:155], v[56:59], v[236:239], v[152:155]\n\t" \
  "v_mfma_f32_16x16x32_bf16 v[156:159], v[60:63], v[236:239], v[156:159]\n\t" \
  "s_setprio 0\n\t" \
  "s_add_u32 s92, s92, 65024\n\t" \
  "s_addc_u32 s93, s93, 0\n\t" \
  "ds_read_b128 v[160:163], v253 offset:0\n\t" \
  "ds_read_b128 v[164:167], v253 offset:1024\n\t" \
  "ds_read_b128 v[176:179], v244 offset:32768\n\t" \
  "ds_read_b128 v[180:183], v244 offset:49152\n\t" \
  "ds_read_b128 v[184:187], v245 offset:32768\n\t" \
  "ds_read_b128 v[188:191], v245 offset:49152\n\t" \
  "s_add_u32 m0, %[lw], 65536\n\t" \
  "s_nop 0\n\t" \
  "global_load_lds_dwordx4 v248, s[92:93] sc1\n\t" \
  "ds_read_b128 v[192:195], v246 offset:32768\n\t" \
  "ds_read_b128 v[196:199], v246 offset:49152\n\t" \
  "ds_read_b128 v[200:203], v247 offset:32768\n\t" \
  "ds_read_b128 v[204:207], v247 offset:49152\n\t" \
  "s_add_u32 m0, %[lw], 66560\n\t" \
  "s_nop 0\n\t" \
  "global_load_lds_dwordx4 v249, s[92:93] sc1\n\t" \
  "ds_read_b128 v[208:211], v244 offset:33024\n\t" \
  "ds_read_b128 v[212:215], v244 offset:49408\n\t" \
  "ds_read_b128 v[216:219], v245 offset:33024\n\t" \
  "ds_read_b128 v[220:223], v245 offset:49408\n\t" \
  "s_add_u32 m0, %[lw], 67584\n\t" \
  "s_nop 0\n\t" \
  "global_load_lds_dwordx4 v250, s[92:93] sc1\n\t" \
  "ds_read_b128 v[224:227], v246 offset:33024\n\t" \
  "ds_read_b128 v[228:231], v246 offset:49408\n\t" \
  "ds_read_b128 v[232:235], v247 offset:33024\n\t" \
  "ds_read_b128 v[236:239], v247 offset:49408\n\t" \
  "s_add_u32 m0, %[lw], 68608\n\t" \
  "s_nop 0\n\t" \
  "global_load_lds_dwordx4 v251, s[92:93] sc1\n\t" \
  "s_waitcnt lgkmcnt(15)\n\t" \
  "s_waitcnt lgkmcnt(12)\n\t" \
  "s_sub_i32 s91, s86, 2\n\t" \
  "v_mov_b32 v173, s91\n\t" \
  "buffer_store_dword v173, v174, %[rs], 0 offen sc1\n\t" \
  "s_lshr_b32 s90, s87, 7\n\t" \
  "s_xor_b32 s90, s90, %[s0]\n\t" \
  "s_and_b32 s90, s90, 1\n\t" \
  "s_lshl1_add_u32 s90, s87, s90\n\t" \
  "s_lshl_b32 s90, s90, 13\n\t" \
  "v_add_f32 v160, v160, v128\n\t" \
  "v_add_f32 v161, v161, v129\n\t" \
  "v_add_f32 v162, v162, v130\n\t" \
  "v_add_f32 v163, v163, v131\n\t" \
  "v_add_f32 v164, v164, v132\n\t" \
  "v_add_f32 v165, v165, v133\n\t" \
  "v_add_f32 v166, v166, v134\n\t" \
  "v_add_f32 v167, v167, v135\n\t" \
  "v_cvt_pk_bf16_f32 v168, v160, v161\n\t" \
  "v_cvt_pk_bf16_f32 v169, v162, v163\n\t" \
  "v_cvt_pk_bf16_f32 v170, v164, v165\n\t" \
  "v_cvt_pk_bf16_f32 v171, v166, v167\n\t" \
  "buffer_store_dwordx4 v[168:171], v172, %[rt], s90 offen sc1\n\t" \
  "s_add_u32 s86, s86, 1\n\t" \
  "s_add_u32 s87, s87, 1\n\t" \
  "s_waitcnt lgkmcnt(0)\n\t" \
  "s_cmp_lt_u32 s86, 64\n\t" \
  "s_cbranch_scc1 .Lp1%=\n\t" \
  "s_barrier\n\t" \
  "s_setprio 2\n\t" \
  "v_mfma_f32_16x16x32_bf16 v[144:147], v[64:67], v[176:179], v[144:147]\n\t" \
  "v_mfma_f32_16x16x32_bf16 v[148:151], v[68:71], v[176:179], v[148:151]\n\t" \
  "v_mfma_f32_16x16x32_bf16 v[152:155], v[64:67], v[180:183], v[152:155]\n\t" \
  "v_mfma_f32_16x16x32_bf16 v[156:159], v[68:71], v[180:183], v[156:159]\n\t" \
  "v_mfma_f32_16x16x32_bf16 v[144:147], v[72:75], v[184:187], v[144:147]\n\t" \
  "v_mfma_f32_16x16x32_bf16 v[148:151], v[76:79], v[184:187], v[148:151]\n\t" \
  "v_mfma_f32_16x16x32_bf16 v[152:155], v[72:75], v[188:191], v[152:155]\n\t" \
  "v_mfma_f32_16x16x32_bf16 v[156:159], v[76:79], v[188:191], v[156:159]\n\t" \
  "v_mfma_f32_16x16x32_bf16 v[144:147], v[80:83], v[192:195], v[144:147]\n\t" \
  "v_mfma_f32_16x16x32_bf16 v[148:151], v[84:87], v[192:195], v[148:151]\n\t" \
  "v_mfma_f32_16x16x32_bf16 v[152:155], v[80:83], v[196:199], v[152:155]\n\t" \
  "v_mfma_f32_16x16x32_bf16 v[156:159], v[84:87], v[196:199], v[156:159]\n\t" \
  "v_mfma_f32_16x16x32_bf16 v[144:147], v[88:91], v[200:203], v[144:147]\n\t" \
  "v_mfma_f32_16x16x32_bf16 v[148:151], v[92:95], v[200:203], v[148:151]\n\t" \
  "v_mfma_f32_16x16x32_bf16 v[152:155], v[88:91], v[204:207], v[152:155]\n\t" \
  "v_mfma_f32_16x16x32_bf16 v[156:159], v[92:95], v[204:207], v[156:159]\n\t" \
  "v_mfma_f32_16x16x32_bf16 v[144:147], v[96:99], v[208:211], v[144:147]\n\t" \
  "v_mfma_f32_16x16x32_bf16 v[148:151], v[100:103], v[208:211], v[148:151]\n\t" \
  "v_mfma_f32_16x16x32_bf16 v[152:155], v[96:99], v[212:215], v[152:155]\n\t" \
  "v_mfma_f32_16x16x32_bf16 v[156:159], v[100:103], v[212:215], v[156:159]\n\t" \
  "v_mfma_f32_16x16x32_bf16 v[144:147], v[104:107], v[216:219], v[144:147]\n\t" \
  "v_mfma_f32_16x16x32_bf16 v[148:151], v[108:111], v[216:219], v[148:151]\n\t" \
  "v_mfma_f32_16x16x32_bf16 v[152:155], v[104:107], v[220:223], v[152:155]\n\t" \
  "v_mfma_f32_16x16x32_bf16 v[156:159], v[108:111], v[220:223], v[156:159]\n\t" \
  "v_mfma_f32_16x16x32_bf16 v[144:147], v[112:115], v[224:227], v[144:147]\n\t" \
  "v_mfma_f32_16x16x32_bf16 v[148:151], v[116:119], v[224:227], v[148:151]\n\t" \
  "v_mfma_f32_16x16x32_bf16 v[152:155], v[112:115], v[228:231], v[152:155]\n\t" \
  "v_mfma_f32_16x16x32_bf16 v[156:159], v[116:119], v[228:231], v[156:159]\n\t" \
  "v_mfma_f32_16x16x32_bf16 v[144:147], v[120:123], v[232:235], v[144:147]\n\t" \
  "v_mfma_f32_16x16x32_bf16 v[148:151], v[124:127], v[232:235], v[148:151]\n\t" \
  "v_mfma_f32_16x16x32_bf16 v[152:155], v[120:123], v[236:239], v[152:155]\n\t" \
  "v_mfma_f32_16x16x32_bf16 v[156:159], v[124:127], v[236:239], v[156:159]\n\t" \
  "s_setprio 0\n\t" \
  "s_nop 5\n\t" \
  "ds_write_b128 v252, v[152:155] offset:0\n\t" \
  "ds_write_b128 v252, v[156:159] offset:1024\n\t" \
  "s_waitcnt lgkmcnt(0)\n\t" \
  "s_barrier\n\t" \
  "ds_read_b128 v[160:163], v253 offset:0\n\t" \
  "ds_read_b128 v[164:167], v253 offset:1024\n\t" \
  "s_waitcnt lgkmcnt(0)\n\t" \
  "s_lshr_b32 s90, s87, 7\n\t" \
  "s_xor_b32 s90, s90, %[s0]\n\t" \
  "s_and_b32 s90, s90, 1\n\t" \
  "s_lshl1_add_u32 s90, s87, s90\n\t" \
  "s_lshl_b32 s90, s90, 13\n\t" \
  "v_add_f32 v160, v160, v144\n\t" \
  "v_add_f32 v161, v161, v145\n\t" \
  "v_add_f32 v162, v162, v146\n\t" \
  "v_add_f32 v163, v163, v147\n\t" \
  "v_add_f32 v164, v164, v148\n\t" \
  "v_add_f32 v165, v165, v149\n\t" \
  "v_add_f32 v166, v166, v150\n\t" \
  "v_add_f32 v167, v167, v151\n\t" \
  "v_cvt_pk_bf16_f32 v168, v160, v161\n\t" \
  "v_cvt_pk_bf16_f32 v169, v162, v163\n\t" \
  "v_cvt_pk_bf16_f32 v170, v164, v165\n\t" \
  "v_cvt_pk_bf16_f32 v171, v166, v167\n\t" \
  "buffer_store_dwordx4 v[168:171], v172, %[rt], s90 offen sc1\n\t" \
  "s_waitcnt vmcnt(0)\n\t" \
  "s_barrier\n\t" \
  "s_sub_i32 s91, s86, 1\n\t" \
  "v_mov_b32 v173, s91\n\t" \
  "buffer_store_dword v173, v174, %[rs], 0 offen sc1\n\t" \
  "s_mov_b32 m0, s94\n\t" \
  ".Le%=:\n\t"
#define PP_ASM_1 \
  "s_cmp_lg_u32 %[kh], 0\n\t" \
  "s_cbranch_scc1 .Lk%=\n\t" \
  "s_mov_b32 s94, m0\n\t" \
  "s_mov_b64 s[92:93], %[x0]\n\t" \
  "s_mov_b32 s86, %[g0]\n\t" \
  "s_add_u32 s87, s86, %[t0]\n\t" \
  "s_add_u32 m0, %[lw], 67584\n\t" \
  "s_nop 0\n\t" \
  "global_load_lds_dwordx4 v250, s[92:93] sc1\n\t" \
  "s_add_u32 m0, %[lw], 68608\n\t" \
  "s_nop 0\n\t" \
  "global_load_lds_dwordx4 v251, s[92:93] sc1\n\t" \
  "s_barrier\n\t" \
  "s_branch .Ls0%=\n\t" \
  ".Lp0%=:\n\t" \
  "s_waitcnt vmcnt(9)\n\t" \
  "s_barrier\n\t" \
  ".Ls0%=:\n\t" \
  "s_add_u32 s92, s92, 512\n\t" \
  "s_addc_u32 s93, s93, 0\n\t" \
  "ds_read_b128 v[176:179], v240 offset:0\n\t" \
  "ds_read_b128 v[180:183], v240 offset:16384\n\t" \
  "ds_read_b128 v[184:187], v241 offset:0\n\t" \
  "ds_read_b128 v[188:191], v241 offset:16384\n\t" \
  "s_add_u32 m0, %[lw], 98304\n\t" \
  "s_nop 0\n\t" \
  "global_load_lds_dwordx4 v248, s[92:93] sc1\n\t" \
  "ds_read_b128 v[192:195], v242 offset:0\n\t" \
  "ds_read_b128 v[196:199], v242 offset:16384\n\t" \
  "ds_read_b128 v[200:203], v243 offset:0\n\t" \
  "ds_read_b128 v[204:207], v243 offset:16384\n\t" \
  "s_add_u32 m0, %[lw], 99328\n\t" \
  "s_nop 0\n\t" \
  "global_load_lds_dwordx4 v249, s[92:93] sc1\n\t" \
  "ds_read_b128 v[208:211], v240 offset:256\n\t" \
  "ds_read_b128 v[212:215], v240 offset:16640\n\t" \
  "ds_read_b128 v[216:219], v241 offset:256\n\t" \
  "ds_read_b128 v[220:223], v241 offset:16640\n\t" \
  "s_add_u32 m0, %[lw], 100352\n\t" \
  "s_nop 0\n\t" \
  "global_load_lds_dwordx4 v250, s[92:93] sc1\n\t" \
  "ds_read_b128 v[224:227], v242 offset:256\n\t" \
  "ds_read_b128 v[228:231], v242 offset:16640\n\t" \
  "ds_read_b128 v[232:235], v243 offset:256\n\t" \
  "ds_read_b128 v[236:239], v243 offset:16640\n\t" \
  "s_add_u32 m0, %[lw], 101376\n\t" \
  "s_nop 0\n\t" \
  "global_load_lds_dwordx4 v251, s[92:93] sc1\n\t" \
  "ds_write_b128 v252, v[152:155] offset:0\n\t" \
  "ds_write_b128 v252, v[156:159] offset:1024\n\t" \
  "s_waitcnt lgkmcnt(0)\n\t" \
  "s_setprio 1\n\t" \
  "v_mfma_f32_16x16x32_bf16 v[128:131], v[176:179], v[0:3], 0\n\t" \
  "v_mfma_f32_16x16x32_bf16 v[136:139], v[176:179], v[4:7], 0\n\t" \
  "v_mfma_f32_16x16x32_bf16 v[132:135], v[180:183], v[0:3], 0\n\t" \
  "v_mfma_f32_16x16x32_bf16 v[140:143], v[180:183], v[4:7], 0\n\t" \
  "v_mfma_f32_16x16x32_bf16 v[128:131], v[184:187], v[8:11], v[128:131]\n\t" \
  "v_mfma_f32_16x16x32_bf16 v[136:139], v[184:187], v[12:15], v[136:139]\n\t" \
  "v_mfma_f32_16x16x32_bf16 v[132:135], v[188:191], v[8:11], v[132:135]\n\t" \
  "v_mfma_f32_16x16x32_bf16 v[140:143], v[188:191], v[12:15], v[140:143]\n\t" \
  "v_mfma_f32_16x16x32_bf16 v[128:131], v[192:195], v[16:19], v[128:131]\n\t" \
  "v_mfma_f32_16x16x32_bf16 v[136:139], v[192:195], v[20:23], v[136:139]\n\t" \
  "v_mfma_f32_16x16x32_bf16 v[132:135], v[196:199], v[16:19], v[132:135]\n\t" \
  "v_mfma_f32_16x16x32_bf16 v[140:143], v[196:199], v[20:23], v[140:143]\n\t" \
  "v_mfma_f32_16x16x32_bf16 v[128:131], v[200:203], v[24:27], v[128:131]\n\t" \
  "v_mfma_f32_16x16x32_bf16 v[136:139], v[200:203], v[28:31], v[136:139]\n\t" \
  "v_mfma_f32_16x16x32_bf16 v[132:135], v[204:207], v[24:27], v[132:135]\n\t" \
  "v_mfma_f32_16x16x32_bf16 v[140:143], v[204:207], v[28:31], v[140:143]\n\t" \
  "v_mfma_f32_16x16x32_bf16 v[128:131], v[208:211], v[32:35], v[128:131]\n\t" \
  "v_mfma_f32_16x16x32_bf16 v[136:139], v[208:211], v[36:39], v[136:139]\n\t" \
  "v_mfma_f32_16x16x32_bf16 v[132:135], v[212:215], v[32:35], v[132:135]\n\t" \
  "v_mfma_f32_16x16x32_bf16 v[140:143], v[212:215], v[36:39], v[140:143]\n\t" \
  "v_mfma_f32_16x16x32_bf16 v[128:131], v[216:219], v[40:43], v[128:131]\n\t" \
  "v_mfma_f32_16x16x32_bf16 v[136:139], v[216:219], v[44:47], v[136:139]\n\t" \
  "v_mfma_f32_16x16x32_bf16 v[132:135], v[220:223], v[40:43], v[132:135]\n\t" \
  "v_mfma_f32_16x16x32_bf16 v[140:143], v[220:223], v[44:47], v[140:143]\n\t" \
  "v_mfma_f32_16x16x32_bf16 v[128:131], v[224:227], v[48:51], v[128:131]\n\t" \
  "v_mfma_f32_16x16x32_bf16 v[136:139], v[224:227], v[52:55], v[136:139]\n\t" \
  "v_mfma_f32_16x16x32_bf16 v[132:135], v[228:231], v[48:51], v[132:135]\n\t" \
  "v_mfma_f32_16x16x32_bf16 v[140:143], v[228:231], v[52:55], v[140:143]\n\t" \
  "v_mfma_f32_16x16x32_bf16 v[128:131], v[232:235], v[56:59], v[128:131]\n\t" \
  "v_mfma_f32_16x16x32_bf16 v[136:139], v[232:235], v[60:63], v[136:139]\n\t" \
  "v_mfma_f32_16x16x32_bf16 v[132:135], v[236:239], v[56:59], v[132:135]\n\t" \
  "v_mfma_f32_16x16x32_bf16 v[140:143], v[236:239], v[60:63], v[140:143]\n\t" \
  "s_setprio 0\n\t" \
  "s_waitcnt vmcnt(4)\n\t" \
  "s_barrier\n\t" \
  "s_add_u32 s92, s92, 65024\n\t" \
  "s_addc_u32 s93, s93, 0\n\t" \
  "ds_read_b128 v[160:163], v253 offset:0\n\t" \
  "ds_read_b128 v[164:167], v253 offset:1024\n\t" \
  "ds_read_b128 v[176:179], v240 offset:32768\n\t" \
  "ds_read_b128 v[180:183], v240 offset:49152\n\t" \
  "ds_read_b128 v[184:187], v241 offset:32768\n\t" \
  "ds_read_b128 v[188:191], v241 offset:49152\n\t" \
  "s_add_u32 m0, %[lw], 0\n\t" \
  "s_nop 0\n\t" \
  "global_load_lds_dwordx4 v248, s[92:93] sc1\n\t" \
  "ds_read_b128 v[192:195], v242 offset:32768\n\t" \
  "ds_read_b128 v[196:199], v242 offset:49152\n\t" \
  "ds_read_b128 v[200:203], v243 offset:32768\n\t" \
  "ds_read_b128 v[204:207], v243 offset:49152\n\t" \
  "s_add_u32 m0, %[lw], 1024\n\t" \
  "s_nop 0\n\t" \
  "global_load_lds_dwordx4 v249, s[92:93] sc1\n\t" \
  "ds_read_b128 v[208:211], v240 offset:33024\n\t" \
  "ds_read_b128 v[212:215], v240 offset:49408\n\t" \
  "ds_read_b128 v[216:219], v241 offset:33024\n\t" \
  "ds_read_b128 v[220:223], v241 offset:49408\n\t" \
  "s_add_u32 m0, %[lw], 2048\n\t" \
  "s_nop 0\n\t" \
  "global_load_lds_dwordx4 v250, s[92:93] sc1\n\t" \
  "ds_read_b128 v[224:227], v242 offset:33024\n\t" \
  "ds_read_b128 v[228:231], v242 offset:49408\n\t" \
  "ds_read_b128 v[232:235], v243 offset:33024\n\t" \
  "ds_read_b128 v[236:239], v243 offset:49408\n\t" \
  "s_add_u32 m0, %[lw], 3072\n\t" \
  "s_nop 0\n\t" \
  "global_load_lds_dwordx4 v251, s[92:93] sc1\n\t" \
  "s_waitcnt lgkmcnt(15)\n\t" \
  "s_waitcnt lgkmcnt(12)\n\t" \
  "s_lshr_b32 s90, s87, 7\n\t" \
  "s_xor_b32 s90, s90, %[s0]\n\t" \
  "s_and_b32 s90, s90, 1\n\t" \
  "s_lshl1_add_u32 s90, s87, s90\n\t" \
  "s_lshl_b32 s90, s90, 13\n\t" \
  "v_add_f32 v160, v160, v144\n\t" \
  "v_add_f32 v161, v161, v145\n\t" \
  "v_add_f32 v162, v162, v146\n\t" \
  "v_add_f32 v163, v163, v147\n\t" \
  "v_add_f32 v164, v164, v148\n\t" \
  "v_add_f32 v165, v165, v149\n\t" \
  "v_add_f32 v166, v166, v150\n\t" \
  "v_add_f32 v167, v167, v151\n\t" \
  "v_cvt_pk_bf16_f32 v168, v160, v161\n\t" \
  "v_cvt_pk_bf16_f32 v169, v162, v163\n\t" \
  "v_cvt_pk_bf16_f32 v170, v164, v165\n\t" \
  "v_cvt_pk_bf16_f32 v171, v166, v167\n\t" \
  "buffer_store_dwordx4 v[168:171], v172, %[rt], s90 offen sc1\n\t" \
  "s_add_u32 s86, s86, 1\n\t" \
  "s_add_u32 s87, s87, 1\n\t" \
  "s_waitcnt lgkmcnt(0)\n\t" \
  "s_setprio 1\n\t" \
  "v_mfma_f32_16x16x32_bf16 v[128:131], v[176:179], v[64:67], v[128:131]\n\t" \
  "v_mfma_f32_16x16x32_bf16 v[136:139], v[176:179], v[68:71], v[136:139]\n\t" \
  "v_mfma_f32_16x16x32_bf16 v[132:135], v[180:183], v[64:67], v[132:135]\n\t" \
  "v_mfma_f32_16x16x32_bf16 v[140:143], v[180:183], v[68:71], v[140:143]\n\t" \
  "v_mfma_f32_16x16x32_bf16 v[128:131], v[184:187], v[72:75], v[128:131]\n\t" \
  "v_mfma_f32_16x16x32_bf16 v[136:139], v[184:187], v[76:79], v[136:139]\n\t" \
  "v_mfma_f32_16x16x32_bf16 v[132:135], v[188:191], v[72:75], v[132:135]\n\t" \
  "v_mfma_f32_16x16x32_bf16 v[140:143], v[188:191], v[76:79], v[140:143]\n\t" \
  "v_mfma_f32_16x16x32_bf16 v[128:131], v[192:195], v[80:83], v[128:131]\n\t" \
  "v_mfma_f32_16x16x32_bf16 v[136:139], v[192:195], v[84:87], v[136:139]\n\t" \
  "v_mfma_f32_16x16x32_bf16 v[132:135], v[196:199], v[80:83], v[132:135]\n\t" \
  "v_mfma_f32_16x16x32_bf16 v[140:143], v[196:199], v[84:87], v[140:143]\n\t" \
  "v_mfma_f32_16x16x32_bf16 v[128:131], v[200:203], v[88:91], v[128:131]\n\t" \
  "v_mfma_f32_16x16x32_bf16 v[136:139], v[200:203], v[92:95], v[136:139]\n\t" \
  "v_mfma_f32_16x16x32_bf16 v[132:135], v[204:207], v[88:91], v[132:135]\n\t" \
  "v_mfma_f32_16x16x32_bf16 v[140:143], v[204:207], v[92:95], v[140:143]\n\t" \
  "v_mfma_f32_16x16x32_bf16 v[128:131], v[208:211], v[96:99], v[128:131]\n\t" \
  "v_mfma_f32_16x16x32_bf16 v[136:139], v[208:211], v[100:103], v[136:139]\n\t" \
  "v_mfma_f32_16x16x32_bf16 v[132:135], v[212:215], v[96:99], v[132:135]\n\t" \
  "v_mfma_f32_16x16x32_bf16 v[140:143], v[212:215], v[100:103], v[140:143]\n\t" \
  "v_mfma_f32_16x16x32_bf16 v[128:131], v[216:219], v[104:107], v[128:131]\n\t" \
  "v_mfma_f32_16x16x32_bf16 v[136:139], v[216:219], v[108:111], v[136:139]\n\t" \
  "v_mfma_f32_16x16x32_bf16 v[132:135], v[220:223], v[104:107], v[132:135]\n\t" \
  "v_mfma_f32_16x16x32_bf16 v[140:143], v[220:223], v[108:111], v[140:143]\n\t" \
  "v_mfma_f32_16x16x32_bf16 v[128:131], v[224:227], v[112:115], v[128:131]\n\t" \
  "v_mfma_f32_16x16x32_bf16 v[136:139], v[224:227], v[116:119], v[136:139]\n\t" \
  "v_mfma_f32_16x16x32_bf16 v[132:135], v[228:231], v[112:115], v[132:135]\n\t" \
  "v_mfma_f32_16x16x32_bf16 v[140:143], v[228:231], v[116:119], v[140:143]\n\t" \
  "v_mfma_f32_16x16x32_bf16 v[128:131], v[232:235], v[120:123], v[128:131]\n\t" \
  "v_mfma_f32_16x16x32_bf16 v[136:139], v[232:235], v[124:127], v[136:139]\n\t" \
  "v_mfma_f32_16x16x32_bf16 v[132:135], v[236:239], v[120:123], v[132:135]\n\t" \
  "v_mfma_f32_16x16x32_bf16 v[140:143], v[236:239], v[124:127], v[140:143]\n\t" \
  "s_setprio 0\n\t" \
  "s_waitcnt vmcnt(9)\n\t" \
  "s_barrier\n\t" \
  "s_add_u32 s92, s92, 512\n\t" \
  "s_addc_u32 s93, s93, 0\n\t" \
  "ds_read_b128 v[176:179], v244 offset:0\n\t" \
  "ds_read_b128 v[180:183], v244 offset:16384\n\t" \
  "ds_read_b128 v[184:187], v245 offset:0\n\t" \
  "ds_read_b128 v[188:191], v245 offset:16384\n\t" \
  "s_add_u32 m0, %[lw], 32768\n\t" \
  "s_nop 0\n\t" \
  "global_load_lds_dwordx4 v248, s[92:93] sc1\n\t" \
  "ds_read_b128 v[192:195], v246 offset:0\n\t" \
  "ds_read_b128 v[196:199], v246 offset:16384\n\t" \
  "ds_read_b128 v[200:203], v247 offset:0\n\t" \
  "ds_read_b128 v[204:207], v247 offset:16384\n\t" \
  "s_add_u32 m0, %[lw], 33792\n\t" \
  "s_nop 0\n\t" \
  "global_load_lds_dwordx4 v249, s[92:93] sc1\n\t" \
  "ds_read_b128 v[208:211], v244 offset:256\n\t" \
  "ds_read_b128 v[212:215], v244 offset:16640\n\t" \
  "ds_read_b128 v[216:219], v245 offset:256\n\t" \
  "ds_read_b128 v[220:223], v245 offset:16640\n\t" \
  "s_add_u32 m0, %[lw], 34816\n\t" \
  "s_nop 0\n\t" \
  "global_load_lds_dwordx4 v250, s[92:93] sc1\n\t" \
  "ds_read_b128 v[224:227], v246 offset:256\n\t" \
  "ds_read_b128 v[228:231], v246 offset:16640\n\t" \
  "ds_read_b128 v[232:235], v247 offset:256\n\t" \
  "ds_read_b128 v[236:239], v247 offset:16640\n\t" \
  "s_add_u32 m0, %[lw], 35840\n\t" \
  "s_nop 0\n\t" \
  "global_load_lds_dwordx4 v251, s[92:93] sc1\n\t" \
  "ds_write_b128 v252, v[136:139] offset:0\n\t" \
  "ds_write_b128 v252, v[140:143] offset:1024\n\t" \
  "s_waitcnt lgkmcnt(0)\n\t" \
  "s_setprio 1\n\t" \
  "v_mfma_f32_16x16x32_bf16 v[144:147], v[176:179], v[0:3], 0\n\t" \
  "v_mfma_f32_16x16x32_bf16 v[152:155], v[176:179], v[4:7], 0\n\t" \
  "v_mfma_f32_16x16x32_bf16 v[148:151], v[180:183], v[0:3], 0\n\t" \
  "v_mfma_f32_16x16x32_bf16 v[156:159], v[180:183], v[4:7], 0\n\t" \
  "v_mfma_f32_16x16x32_bf16 v[144:147], v[184:187], v[8:11], v[144:147]\n\t" \
  "v_mfma_f32_16x16x32_bf16 v[152:155], v[184:187], v[12:15], v[152:155]\n\t" \
  "v_mfma_f32_16x16x32_bf16 v[148:151], v[188:191], v[8:11], v[148:151]\n\t" \
  "v_mfma_f32_16x16x32_bf16 v[156:159], v[188:191], v[12:15], v[156:159]\n\t" \
  "v_mfma_f32_16x16x32_bf16 v[144:147], v[192:195], v[16:19], v[144:147]\n\t" \
  "v_mfma_f32_16x16x32_bf16 v[152:155], v[192:195], v[20:23], v[152:155]\n\t" \
  "v_mfma_f32_16x16x32_bf16 v[148:151], v[196:199], v[16:19], v[148:151]\n\t" \
  "v_mfma_f32_16x16x32_bf16 v[156:159], v[196:199], v[20:23], v[156:159]\n\t" \
  "v_mfma_f32_16x16x32_bf16 v[144:147], v[200:203], v[24:27], v[144:147]\n\t" \
  "v_mfma_f32_16x16x32_bf16 v[152:155], v[200:203], v[28:31], v[152:155]\n\t" \
  "v_mfma_f32_16x16x32_bf16 v[148:151], v[204:207], v[24:27], v[148:151]\n\t" \
  "v_mfma_f32_16x16x32_bf16 v[156:159], v[204:207], v[28:31], v[156:159]\n\t" \
  "v_mfma_f32_16x16x32_bf16 v[144:147], v[208:211], v[32:35], v[144:147]\n\t" \
  "v_mfma_f32_16x16x32_bf16 v[152:155], v[208:211], v[36:39], v[152:155]\n\t" \
  "v_mfma_f32_16x16x32_bf16 v[148:151], v[212:215], v[32:35], v[148:151]\n\t" \
  "v_mfma_f32_16x16x32_bf16 v[156:159], v[212:215], v[36:39], v[156:159]\n\t" \
  "v_mfma_f32_16x16x32_bf16 v[144:147], v[216:219], v[40:43], v[144:147]\n\t" \
  "v_mfma_f32_16x16x32_bf16 v[152:155], v[216:219], v[44:47], v[152:155]\n\t" \
  "v_mfma_f32_16x16x32_bf16 v[148:151], v[220:223], v[40:43], v[148:151]\n\t" \
  "v_mfma_f32_16x16x32_bf16 v[156:159], v[220:223], v[44:47], v[156:159]\n\t" \
  "v_mfma_f32_16x16x32_bf16 v[144:147], v[224:227], v[48:51], v[144:147]\n\t" \
  "v_mfma_f32_16x16x32_bf16 v[152:155], v[224:227], v[52:55], v[152:155]\n\t" \
  "v_mfma_f32_16x16x32_bf16 v[148:151], v[228:231], v[48:51], v[148:151]\n\t" \
  "v_mfma_f32_16x16x32_bf16 v[156:159], v[228:231], v[52:55], v[156:159]\n\t" \
  "v_mfma_f32_16x16x32_bf16 v[144:147], v[232:235], v[56:59], v[144:147]\n\t" \
  "v_mfma_f32_16x16x32_bf16 v[152:155], v[232:235], v[60:63], v[152:155]\n\t" \
  "v_mfma_f32_16x16x32_bf16 v[148:151], v[236:239], v[56:59], v[148:151]\n\t" \
  "v_mfma_f32_16x16x32_bf16 v[156:159], v[236:239], v[60:63], v[156:159]\n\t" \
  "s_setprio 0\n\t" \
  "s_waitcnt vmcnt(4)\n\t" \
  "s_barrier\n\t" \
  "s_add_u32 s92, s92, 65024\n\t" \
  "s_addc_u32 s93, s93, 0\n\t" \
  "ds_read_b128 v[160:163], v253 offset:0\n\t" \
  "ds_read_b128 v[164:167], v253 offset:1024\n\t" \
  "ds_read_b128 v[176:179], v244 offset:32768\n\t" \
  "ds_read_b128 v[180:183], v244 offset:49152\n\t" \
  "ds_read_b128 v[184:187], v245 offset:32768\n\t" \
  "ds_read_b128 v[188:191], v245 offset:49152\n\t" \
  "s_add_u32 m0, %[lw], 65536\n\t" \
  "s_nop 0\n\t" \
  "global_load_lds_dwordx4 v248, s[92:93] sc1\n\t" \
  "ds_read_b128 v[192:195], v246 offset:32768\n\t" \
  "ds_read_b128 v[196:199], v246 offset:49152\n\t" \
  "ds_read_b128 v[200:203], v247 offset:32768\n\t" \
  "ds_read_b128 v[204:207], v247 offset:49152\n\t" \
  "s_add_u32 m0, %[lw], 66560\n\t" \
  "s_nop 0\n\t" \
  "global_load_lds_dwordx4 v249, s[92:93] sc1\n\t" \
  "ds_read_b128 v[208:211], v244 offset:33024\n\t" \
  "ds_read_b128 v[212:215], v244 offset:49408\n\t" \
  "ds_read_b128 v[216:219], v245 offset:33024\n\t" \
  "ds_read_b128 v[220:223], v245 offset:49408\n\t" \
  "s_add_u32 m0, %[lw], 67584\n\t" \
  "s_nop 0\n\t" \
  "global_load_lds_dwordx4 v250, s[92:93] sc1\n\t" \
  "ds_read_b128 v[224:227], v246 offset:33024\n\t" \
  "ds_read_b128 v[228:231], v246 offset:49408\n\t" \
  "ds_read_b128 v[232:235], v247 offset:33024\n\t" \
  "ds_read_b128 v[236:239], v247 offset:49408\n\t" \
  "s_add_u32 m0, %[lw], 68608\n\t" \
  "s_nop 0\n\t" \
  "global_load_lds_dwordx4 v251, s[92:93] sc1\n\t" \
  "s_waitcnt lgkmcnt(15)\n\t" \
  "s_waitcnt lgkmcnt(12)\n\t" \
  "s_lshr_b32 s90, s87, 7\n\t" \
  "s_xor_b32 s90, s90, %[s0]\n\t" \
  "s_and_b32 s90, s90, 1\n\t" \
  "s_lshl1_add_u32 s90, s87, s90\n\t" \
  "s_lshl_b32 s90, s90, 13\n\t" \
  "v_add_f32 v160, v160, v128\n\t" \
  "v_add_f32 v161, v161, v129\n\t" \
  "v_add_f32 v162, v162, v130\n\t" \
  "v_add_f32 v163, v163, v131\n\t" \
  "v_add_f32 v164, v164, v132\n\t" \
  "v_add_f32 v165, v165, v133\n\t" \
  "v_add_f32 v166, v166, v134\n\t" \
  "v_add_f32 v167, v167, v135\n\t" \
  "v_cvt_pk_bf16_f32 v168, v160, v161\n\t" \
  "v_cvt_pk_bf16_f32 v169, v162, v163\n\t" \
  "v_cvt_pk_bf16_f32 v170, v164, v165\n\t" \
  "v_cvt_pk_bf16_f32 v171, v166, v167\n\t" \
  "buffer_store_dwordx4 v[168:171], v172, %[rt], s90 offen sc1\n\t" \
  "s_add_u32 s86, s86, 1\n\t" \
  "s_add_u32 s87, s87, 1\n\t" \
  "s_waitcnt lgkmcnt(0)\n\t" \
  "s_setprio 1\n\t" \
  "v_mfma_f32_16x16x32_bf16 v[144:147], v[176:179], v[64:67], v[144:147]\n\t" \
  "v_mfma_f32_16x16x32_bf16 v[152:155], v[176:179], v[68:71], v[152:155]\n\t" \
  "v_mfma_f32_16x16x32_bf16 v[148:151], v[180:183], v[64:67], v[148:151]\n\t" \
  "v_mfma_f32_16x16x32_bf16 v[156:159], v[180:183], v[68:71], v[156:159]\n\t" \
  "v_mfma_f32_16x16x32_bf16 v[144:147], v[184:187], v[72:75], v[144:147]\n\t" \
  "v_mfma_f32_16x16x32_bf16 v[152:155], v[184:187], v[76:79], v[152:155]\n\t" \
  "v_mfma_f32_16x16x32_bf16 v[148:151], v[188:191], v[72:75], v[148:151]\n\t" \
  "v_mfma_f32_16x16x32_bf16 v[156:159], v[188:191], v[76:79], v[156:159]\n\t" \
  "v_mfma_f32_16x16x32_bf16 v[144:147], v[192:195], v[80:83], v[144:147]\n\t" \
  "v_mfma_f32_16x16x32_bf16 v[152:155], v[192:195], v[84:87], v[152:155]\n\t" \
  "v_mfma_f32_16x16x32_bf16 v[148:151], v[196:199], v[80:83], v[148:151]\n\t" \
  "v_mfma_f32_16x16x32_bf16 v[156:159], v[196:199], v[84:87], v[156:159]\n\t" \
  "v_mfma_f32_16x16x32_bf16 v[144:147], v[200:203], v[88:91], v[144:147]\n\t" \
  "v_mfma_f32_16x16x32_bf16 v[152:155], v[200:203], v[92:95], v[152:155]\n\t" \
  "v_mfma_f32_16x16x32_bf16 v[148:151], v[204:207], v[88:91], v[148:151]\n\t" \
  "v_mfma_f32_16x16x32_bf16 v[156:159], v[204:207], v[92:95], v[156:159]\n\t" \
  "v_mfma_f32_16x16x32_bf16 v[144:147], v[208:211], v[96:99], v[144:147]\n\t" \
  "v_mfma_f32_16x16x32_bf16 v[152:155], v[208:211], v[100:103], v[152:155]\n\t" \
  "v_mfma_f32_16x16x32_bf16 v[148:151], v[212:215], v[96:99], v[148:151]\n\t" \
  "v_mfma_f32_16x16x32_bf16 v[156:159], v[212:215], v[100:103], v[156:159]\n\t" \
  "v_mfma_f32_16x16x32_bf16 v[144:147], v[216:219], v[104:107], v[144:147]\n\t" \
  "v_mfma_f32_16x16x32_bf16 v[152:155], v[216:219], v[108:111], v[152:155]\n\t" \
  "v_mfma_f32_16x16x32_bf16 v[148:151], v[220:223], v[104:107], v[148:151]\n\t" \
  "v_mfma_f32_16x16x32_bf16 v[156:159], v[220:223], v[108:111], v[156:159]\n\t" \
  "v_mfma_f32_16x16x32_bf16 v[144:147], v[224:227], v[112:115], v[144:147]\n\t" \
  "v_mfma_f32_16x16x32_bf16 v[152:155], v[224:227], v[116:119], v[152:155]\n\t" \
  "v_mfma_f32_16x16x32_bf16 v[148:151], v[228:231], v[112:115], v[148:151]\n\t" \
  "v_mfma_f32_16x16x32_bf16 v[156:159], v[228:231], v[116:119], v[156:159]\n\t" \
  "v_mfma_f32_16x16x32_bf16 v[144:147], v[232:235], v[120:123], v[144:147]\n\t" \
  "v_mfma_f32_16x16x32_bf16 v[152:155], v[232:235], v[124:127], v[152:155]\n\t" \
  "v_mfma_f32_16x16x32_bf16 v[148:151], v[236:239], v[120:123], v[148:151]\n\t" \
  "v_mfma_f32_16x16x32_bf16 v[156:159], v[236:239], v[124:127], v[156:159]\n\t" \
  "s_setprio 0\n\t" \
  "s_cmp_lt_u32 s86, 64\n\t" \
  "s_cbranch_scc1 .Lp0%=\n\t" \
  "s_barrier\n\t" \
  "s_nop 1\n\t" \
  "ds_write_b128 v252, v[152:155] offset:0\n\t" \
  "s_nop 0\n\t" \
  "ds_write_b128 v252, v[156:159] offset:1024\n\t" \
  "s_waitcnt lgkmcnt(0)\n\t" \
  "s_barrier\n\t" \
  "ds_read_b128 v[160:163], v253 offset:0\n\t" \
  "ds_read_b128 v[164:167], v253 offset:1024\n\t" \
  "s_waitcnt lgkmcnt(0)\n\t" \
  "s_lshr_b32 s90, s87, 7\n\t" \
  "s_xor_b32 s90, s90, %[s0]\n\t" \
  "s_and_b32 s90, s90, 1\n\t" \
  "s_lshl1_add_u32 s90, s87, s90\n\t" \
  "s_lshl_b32 s90, s90, 13\n\t" \
  "v_add_f32 v160, v160, v144\n\t" \
  "v_add_f32 v161, v161, v145\n\t" \
  "v_add_f32 v162, v162, v146\n\t" \
  "v_add_f32 v163, v163, v147\n\t" \
  "v_add_f32 v164, v164, v148\n\t" \
  "v_add_f32 v165, v165, v149\n\t" \
  "v_add_f32 v166, v166, v150\n\t" \
  "v_add_f32 v167, v167, v151\n\t" \
  "v_cvt_pk_bf16_f32 v168, v160, v161\n\t" \
  "v_cvt_pk_bf16_f32 v169, v162, v163\n\t" \
  "v_cvt_pk_bf16_f32 v170, v164, v165\n\t" \
  "v_cvt_pk_bf16_f32 v171, v166, v167\n\t" \
  "buffer_store_dwordx4 v[168:171], v172, %[rt], s90 offen sc1\n\t" \
  "s_waitcnt vmcnt(0)\n\t" \
  "s_barrier\n\t" \
  "s_mov_b32 m0, s94\n\t" \
  "s_branch .Le%=\n\t" \
  ".Lk%=:\n\t" \
  "s_mov_b32 s94, m0\n\t" \
  "s_mov_b64 s[92:93], %[x0]\n\t" \
  "s_mov_b32 s86, %[g0]\n\t" \
  "s_add_u32 s87, s86, %[t0]\n\t" \
  "s_add_u32 m0, %[lw], 67584\n\t" \
  "s_nop 0\n\t" \
  "global_load_lds_dwordx4 v250, s[92:93] sc1\n\t" \
  "s_add_u32 m0, %[lw], 68608\n\t" \
  "s_nop 0\n\t" \
  "global_load_lds_dwordx4 v251, s[92:93] sc1\n\t" \
  "s_barrier\n\t" \
  "s_branch .Ls1%=\n\t" \
  ".Lp1%=:\n\t" \
  "s_waitcnt vmcnt(10)\n\t" \
  "s_barrier\n\t" \
  "s_setprio 2\n\t" \
  "v_mfma_f32_16x16x32_bf16 v[144:147], v[176:179], v[64:67], v[144:147]\n\t" \
  "v_mfma_f32_16x16x32_bf16 v[152:155], v[176:179], v[68:71], v[152:155]\n\t" \
  "v_mfma_f32_16x16x32_bf16 v[148:151], v[180:183], v[64:67], v[148:151]\n\t" \
  "v_mfma_f32_16x16x32_bf16 v[156:159], v[180:183], v[68:71], v[156:159]\n\t" \
  "v_mfma_f32_16x16x32_bf16 v[144:147], v[184:187], v[72:75], v[144:147]\n\t" \
  "v_mfma_f32_16x16x32_bf16 v[152:155], v[184:187], v[76:79], v[152:155]\n\t" \
  "v_mfma_f32_16x16x32_bf16 v[148:151], v[188:191], v[72:75], v[148:151]\n\t" \
  "v_mfma_f32_16x16x32_bf16 v[156:159], v[188:191], v[76:79], v[156:159]\n\t" \
  "v_mfma_f32_16x16x32_bf16 v[144:147], v[192:195], v[80:83], v[144:147]\n\t" \
  "v_mfma_f32_16x16x32_bf16 v[152:155], v[192:195], v[84:87], v[152:155]\n\t" \
  "v_mfma_f32_16x16x32_bf16 v[148:151], v[196:199], v[80:83], v[148:151]\n\t" \
  "v_mfma_f32_16x16x32_bf16 v[156:159], v[196:199], v[84:87], v[156:159]\n\t" \
  "v_mfma_f32_16x16x32_bf16 v[144:147], v[200:203], v[88:91], v[144:147]\n\t" \
  "v_mfma_f32_16x16x32_bf16 v[152:155], v[200:203], v[92:95], v[152:155]\n\t" \
  "v_mfma_f32_16x16x32_bf16 v[148:151], v[204:207], v[88:91], v[148:151]\n\t" \
  "v_mfma_f32_16x16x32_bf16 v[156:159], v[204:207], v[92:95], v[156:159]\n\t" \
  "v_mfma_f32_16x16x32_bf16 v[144:147], v[208:211], v[96:99], v[144:147]\n\t" \
  "v_mfma_f32_16x16x32_bf16 v[152:155], v[208:211], v[100:103], v[152:155]\n\t" \
  "v_mfma_f32_16x16x32_bf16 v[148:151], v[212:215], v[96:99], v[148:151]\n\t" \
  "v_mfma_f32_16x16x32_bf16 v[156:159], v[212:215], v[100:103], v[156:159]\n\t" \
  "v_mfma_f32_16x16x32_bf16 v[144:147], v[216:219], v[104:107], v[144:147]\n\t" \
  "v_mfma_f32_16x16x32_bf16 v[152:155], v[216:219], v[108:111], v[152:155]\n\t" \
  "v_mfma_f32_16x16x32_bf16 v[148:151], v[220:223], v[104:107], v[148:151]\n\t" \
  "v_mfma_f32_16x16x32_bf16 v[156:159], v[220:223], v[108:111], v[156:159]\n\t" \
  "v_mfma_f32_16x16x32_bf16 v[144:147], v[224:227], v[112:115], v[144:147]\n\t" \
  "v_mfma_f32_16x16x32_bf16 v[152:155], v[224:227], v[116:119], v[152:155]\n\t" \
  "v_mfma_f32_16x16x32_bf16 v[148:151], v[228:231], v[112:115], v[148:151]\n\t" \
  "v_mfma_f32_16x16x32_bf16 v[156:159], v[228:231], v[116:119], v[156:159]\n\t" \
  "v_mfma_f32_16x16x32_bf16 v[144:147], v[232:235], v[120:123], v[144:147]\n\t" \
  "v_mfma_f32_16x16x32_bf16 v[152:155], v[232:235], v[124:127], v[152:155]\n\t" \
  "v_mfma_f32_16x16x32_bf16 v[148:151], v[236:239], v[120:123], v[148:151]\n\t" \
  "v_mfma_f32_16x16x32_bf16 v[156:159], v[236:239], v[124:127], v[156:159]\n\t" \
  "s_setprio 0\n\t" \
  ".Ls1%=:\n\t" \
  "s_add_u32 s92, s92, 512\n\t" \
  "s_addc_u32 s93, s93, 0\n\t" \
  "ds_read_b128 v[176:179], v240 offset:0\n\t" \
  "ds_read_b128 v[180:183], v240 offset:16384\n\t" \
  "ds_read_b128 v[184:187], v241 offset:0\n\t" \
  "ds_read_b128 v[188:191], v241 offset:16384\n\t" \
  "s_add_u32 m0, %[lw], 98304\n\t" \
  "s_nop 0\n\t" \
  "global_load_lds_dwordx4 v248, s[92:93] sc1\n\t" \
  "ds_read_b128 v[192:195], v242 offset:0\n\t" \
  "ds_read_b128 v[196:199], v242 offset:16384\n\t" \
  "ds_read_b128 v[200:203], v243 offset:0\n\t" \
  "ds_read_b128 v[204:207], v243 offset:16384\n\t" \
  "s_add_u32 m0, %[lw], 99328\n\t" \
  "s_nop 0\n\t" \
  "global_load_lds_dwordx4 v249, s[92:93] sc1\n\t" \
  "ds_read_b128 v[208:211], v240 offset:256\n\t" \
  "ds_read_b128 v[212:215], v240 offset:16640\n\t" \
  "ds_read_b128 v[216:219], v241 offset:256\n\t" \
  "ds_read_b128 v[220:223], v241 offset:16640\n\t" \
  "s_add_u32 m0, %[lw], 100352\n\t" \
  "s_nop 0\n\t" \
  "global_load_lds_dwordx4 v250, s[92:93] sc1\n\t" \
  "ds_read_b128 v[224:227], v242 offset:256\n\t" \
  "ds_read_b128 v[228:231], v242 offset:16640\n\t" \
  "ds_read_b128 v[232:235], v243 offset:256\n\t" \
  "ds_read_b128 v[236:239], v243 offset:16640\n\t" \
  "s_add_u32 m0, %[lw], 101376\n\t" \
  "s_nop 0\n\t" \
  "global_load_lds_dwordx4 v251, s[92:93] sc1\n\t" \
  "ds_write_b128 v252, v[152:155] offset:0\n\t" \
  "ds_write_b128 v252, v[156:159] offset:1024\n\t" \
  "s_waitcnt lgkmcnt(0)\n\t" \
  "s_waitcnt vmcnt(4)\n\t" \
  "s_barrier\n\t" \
  "s_setprio 2\n\t" \
  "v_mfma_f32_16x16x32_bf16 v[128:131], v[176:179], v[0:3], 0\n\t" \
  "v_mfma_f32_16x16x32_bf16 v[136:139], v[176:179], v[4:7], 0\n\t" \
  "v_mfma_f32_16x16x32_bf16 v[132:135], v[180:183], v[0:3], 0\n\t" \
  "v_mfma_f32_16x16x32_bf16 v[140:143], v[180:183], v[4:7], 0\n\t" \
  "v_mfma_f32_16x16x32_bf16 v[128:131], v[184:187], v[8:11], v[128:131]\n\t" \
  "v_mfma_f32_16x16x32_bf16 v[136:139], v[184:187], v[12:15], v[136:139]\n\t" \
  "v_mfma_f32_16x16x32_bf16 v[132:135], v[188:191], v[8:11], v[132:135]\n\t" \
  "v_mfma_f32_16x16x32_bf16 v[140:143], v[188:191], v[12:15], v[140:143]\n\t" \
  "v_mfma_f32_16x16x32_bf16 v[128:131], v[192:195], v[16:19], v[128:131]\n\t" \
  "v_mfma_f32_16x16x32_bf16 v[136:139], v[192:195], v[20:23], v[136:139]\n\t" \
  "v_mfma_f32_16x16x32_bf16 v[132:135], v[196:199], v[16:19], v[132:135]\n\t" \
  "v_mfma_f32_16x16x32_bf16 v[140:143], v[196:199], v[20:23], v[140:143]\n\t" \
  "v_mfma_f32_16x16x32_bf16 v[128:131], v[200:203], v[24:27], v[128:131]\n\t" \
  "v_mfma_f32_16x16x32_bf16 v[136:139], v[200:203], v[28:31], v[136:139]\n\t" \
  "v_mfma_f32_16x16x32_bf16 v[132:135], v[204:207], v[24:27], v[132:135]\n\t" \
  "v_mfma_f32_16x16x32_bf16 v[140:143], v[204:207], v[28:31], v[140:143]\n\t" \
  "v_mfma_f32_16x16x32_bf16 v[128:131], v[208:211], v[32:35], v[128:131]\n\t" \
  "v_mfma_f32_16x16x32_bf16 v[136:139], v[208:211], v[36:39], v[136:139]\n\t" \
  "v_mfma_f32_16x16x32_bf16 v[132:135], v[212:215], v[32:35], v[132:135]\n\t" \
  "v_mfma_f32_16x16x32_bf16 v[140:143], v[212:215], v[36:39], v[140:143]\n\t" \
  "v_mfma_f32_16x16x32_bf16 v[128:131], v[216:219], v[40:43], v[128:131]\n\t" \
  "v_mfma_f32_16x16x32_bf16 v[136:139], v[216:219], v[44:47], v[136:139]\n\t" \
  "v_mfma_f32_16x16x32_bf16 v[132:135], v[220:223], v[40:43], v[132:135]\n\t" \
  "v_mfma_f32_16x16x32_bf16 v[140:143], v[220:223], v[44:47], v[140:143]\n\t" \
  "v_mfma_f32_16x16x32_bf16 v[128:131], v[224:227], v[48:51], v[128:131]\n\t" \
  "v_mfma_f32_16x16x32_bf16 v[136:139], v[224:227], v[52:55], v[136:139]\n\t" \
  "v_mfma_f32_16x16x32_bf16 v[132:135], v[228:231], v[48:51], v[132:135]\n\t" \
  "v_mfma_f32_16x16x32_bf16 v[140:143], v[228:231], v[52:55], v[140:143]\n\t" \
  "v_mfma_f32_16x16x32_bf16 v[128:131], v[232:235], v[56:59], v[128:131]\n\t" \
  "v_mfma_f32_16x16x32_bf16 v[136:139], v[232:235], v[60:63], v[136:139]\n\t" \
  "v_mfma_f32_16x16x32_bf16 v[132:135], v[236:239], v[56:59], v[132:135]\n\t" \
  "v_mfma_f32_16x16x32_bf16 v[140:143], v[236:239], v[60:63], v[140:143]\n\t" \
  "s_setprio 0\n\t" \
  "s_add_u32 s92, s92, 65024\n\t" \
  "s_addc_u32 s93, s93, 0\n\t" \
  "ds_read_b128 v[160:163], v253 offset:0\n\t" \
  "ds_read_b128 v[164:167], v253 offset:1024\n\t" \
  "ds_read_b128 v[176:179], v240 offset:32768\n\t" \
  "ds_read_b128 v[180:183], v240 offset:49152\n\t" \
  "ds_read_b128 v[184:187], v241 offset:32768\n\t" \
  "ds_read_b128 v[188:191], v241 offset:49152\n\t" \
  "s_add_u32 m0, %[lw], 0\n\t" \
  "s_nop 0\n\t" \
  "global_load_lds_dwordx4 v248, s[92:93] sc1\n\t" \
  "ds_read_b128 v[192:195], v242 offset:32768\n\t" \
  "ds_read_b128 v[196:199], v242 offset:49152\n\t" \
  "ds_read_b128 v[200:203], v243 offset:32768\n\t" \
  "ds_read_b128 v[204:207], v243 offset:49152\n\t" \
  "s_add_u32 m0, %[lw], 1024\n\t" \
  "s_nop 0\n\t" \
  "global_load_lds_dwordx4 v249, s[92:93] sc1\n\t" \
  "ds_read_b128 v[208:211], v240 offset:33024\n\t" \
  "ds_read_b128 v[212:215], v240 offset:49408\n\t" \
  "ds_read_b128 v[216:219], v241 offset:33024\n\t" \
  "ds_read_b128 v[220:223], v241 offset:49408\n\t" \
  "s_add_u32 m0, %[lw], 2048\n\t" \
  "s_nop 0\n\t" \
  "global_load_lds_dwordx4 v250, s[92:93] sc1\n\t" \
  "ds_read_b128 v[224:227], v242 offset:33024\n\t" \
  "ds_read_b128 v[228:231], v242 offset:49408\n\t" \
  "ds_read_b128 v[232:235], v243 offset:33024\n\t" \
  "ds_read_b128 v[236:239], v243 offset:49408\n\t" \
  "s_add_u32 m0, %[lw], 3072\n\t" \
  "s_nop 0\n\t" \
  "global_load_lds_dwordx4 v251, s[92:93] sc1\n\t" \
  "s_waitcnt lgkmcnt(15)\n\t" \
  "s_waitcnt lgkmcnt(12)\n\t" \
  "s_sub_i32 s91, s86, 2\n\t" \
  "v_mov_b32 v173, s91\n\t" \
  "buffer_store_dword v173, v174, %[rs], 0 offen sc1\n\t" \
  "s_lshr_b32 s90, s87, 7\n\t" \
  "s_xor_b32 s90, s90, %[s0]\n\t" \
  "s_and_b32 s90, s90, 1\n\t" \
  "s_lshl1_add_u32 s90, s87, s90\n\t" \
  "s_lshl_b32 s90, s90, 13\n\t" \
  "v_add_f32 v160, v160, v144\n\t" \
  "v_add_f32 v161, v161, v145\n\t" \
  "v_add_f32 v162, v162, v146\n\t" \
  "v_add_f32 v163, v163, v147\n\t" \
  "v_add_f32 v164, v164, v148\n\t" \
  "v_add_f32 v165, v165, v149\n\t" \
  "v_add_f32 v166, v166, v150\n\t" \
  "v_add_f32 v167, v167, v151\n\t" \
  "v_cvt_pk_bf16_f32 v168, v160, v161\n\t" \
  "v_cvt_pk_bf16_f32 v169, v162, v163\n\t" \
  "v_cvt_pk_bf16_f32 v170, v164, v165\n\t" \
  "v_cvt_pk_bf16_f32 v171, v166, v167\n\t" \
  "buffer_store_dwordx4 v[168:171], v172, %[rt], s90 offen sc1\n\t" \
  "s_add_u32 s86, s86, 1\n\t" \
  "s_add_u32 s87, s87, 1\n\t" \
  "s_waitcnt lgkmcnt(0)\n\t" \
  "s_waitcnt vmcnt(10)\n\t" \
  "s_barrier\n\t" \
  "s_setprio 2\n\t" \
  "v_mfma_f32_16x16x32_bf16 v[128:131], v[176:179], v[64:67], v[128:131]\n\t" \
  "v_mfma_f32_16x16x32_bf16 v[136:139], v[176:179], v[68:71], v[136:139]\n\t" \
  "v_mfma_f32_16x16x32_bf16 v[132:135], v[180:183], v[64:67], v[132:135]\n\t" \
  "v_mfma_f32_16x16x32_bf16 v[140:143], v[180:183], v[68:71], v[140:143]\n\t" \
  "v_mfma_f32_16x16x32_bf16 v[128:131], v[184:187], v[72:75], v[128:131]\n\t" \
  "v_mfma_f32_16x16x32_bf16 v[136:139], v[184:187], v[76:79], v[136:139]\n\t" \
  "v_mfma_f32_16x16x32_bf16 v[132:135], v[188:191], v[72:75], v[132:135]\n\t" \
  "v_mfma_f32_16x16x32_bf16 v[140:143], v[188:191], v[76:79], v[140:143]\n\t" \
  "v_mfma_f32_16x16x32_bf16 v[128:131], v[192:195], v[80:83], v[128:131]\n\t" \
  "v_mfma_f32_16x16x32_bf16 v[136:139], v[192:195], v[84:87], v[136:139]\n\t" \
  "v_mfma_f32_16x16x32_bf16 v[132:135], v[196:199], v[80:83], v[132:135]\n\t" \
  "v_mfma_f32_16x16x32_bf16 v[140:143], v[196:199], v[84:87], v[140:143]\n\t" \
  "v_mfma_f32_16x16x32_bf16 v[128:131], v[200:203], v[88:91], v[128:131]\n\t" \
  "v_mfma_f32_16x16x32_bf16 v[136:139], v[200:203], v[92:95], v[136:139]\n\t" \
  "v_mfma_f32_16x16x32_bf16 v[132:135], v[204:207], v[88:91], v[132:135]\n\t" \
  "v_mfma_f32_16x16x32_bf16 v[140:143], v[204:207], v[92:95], v[140:143]\n\t" \
  "v_mfma_f32_16x16x32_bf16 v[128:131], v[208:211], v[96:99], v[128:131]\n\t" \
  "v_mfma_f32_16x16x32_bf16 v[136:139], v[208:211], v[100:103], v[136:139]\n\t" \
  "v_mfma_f32_16x16x32_bf16 v[132:135], v[212:215], v[96:99], v[132:135]\n\t" \
  "v_mfma_f32_16x16x32_bf16 v[140:143], v[212:215], v[100:103], v[140:143]\n\t" \
  "v_mfma_f32_16x16x32_bf16 v[128:131], v[216:219], v[104:107], v[128:131]\n\t" \
  "v_mfma_f32_16x16x32_bf16 v[136:139], v[216:219], v[108:111], v[136:139]\n\t" \
  "v_mfma_f32_16x16x32_bf16 v[132:135], v[220:223], v[104:107], v[132:135]\n\t" \
  "v_mfma_f32_16x16x32_bf16 v[140:143], v[220:223], v[108:111], v[140:143]\n\t" \
  "v_mfma_f32_16x16x32_bf16 v[128:131], v[224:227], v[112:115], v[128:131]\n\t" \
  "v_mfma_f32_16x16x32_bf16 v[136:139], v[224:227], v[116:119], v[136:139]\n\t" \
  "v_mfma_f32_16x16x32_bf16 v[132:135], v[228:231], v[112:115], v[132:135]\n\t" \
  "v_mfma_f32_16x16x32_bf16 v[140:143], v[228:231], v[116:119], v[140:143]\n\t" \
  "v_mfma_f32_16x16x32_bf16 v[128:131], v[232:235], v[120:123], v[128:131]\n\t" \
  "v_mfma_f32_16x16x32_bf16 v[136:139], v[232:235], v[124:127], v[136:139]\n\t" \
  "v_mfma_f32_16x16x32_bf16 v[132:135], v[236:239], v[120:123], v[132:135]\n\t" \
  "v_mfma_f32_16x16x32_bf16 v[140:143], v[236:239], v[124:127], v[140:143]\n\t" \
  "s_setprio 0\n\t" \
  "s_add_u32 s92, s92, 512\n\t" \
  "s_addc_u32 s93, s93, 0\n\t" \
  "ds_read_b128 v[176:179], v244 offset:0\n\t" \
  "ds_read_b128 v[180:183], v244 offset:16384\n\t" \
  "ds_read_b128 v[184:187], v245 offset:0\n\t" \
  "ds_read_b128 v[188:191], v245 offset:16384\n\t" \
  "s_add_u32 m0, %[lw], 32768\n\t" \
  "s_nop 0\n\t" \
  "global_load_lds_dwordx4 v248, s[92:93] sc1\n\t" \
  "ds_read_b128 v[192:195], v246 offset:0\n\t" \
  "ds_read_b128 v[196:199], v246 offset:16384\n\t" \
  "ds_read_b128 v[200:203], v247 offset:0\n\t" \
  "ds_read_b128 v[204:207], v247 offset:16384\n\t" \
  "s_add_u32 m0, %[lw], 33792\n\t" \
  "s_nop 0\n\t" \
  "global_load_lds_dwordx4 v249, s[92:93] sc1\n\t" \
  "ds_read_b128 v[208:211], v244 offset:256\n\t" \
  "ds_read_b128 v[212:215], v244 offset:16640\n\t" \
  "ds_read_b128 v[216:219], v245 offset:256\n\t" \
  "ds_read_b128 v[220:223], v245 offset:16640\n\t" \
  "s_add_u32 m0, %[lw], 34816\n\t" \
  "s_nop 0\n\t" \
  "global_load_lds_dwordx4 v250, s[92:93] sc1\n\t" \
  "ds_read_b128 v[224:227], v246 offset:256\n\t" \
  "ds_read_b128 v[228:231], v246 offset:16640\n\t" \
  "ds_read_b128 v[232:235], v247 offset:256\n\t" \
  "ds_read_b128 v[236:239], v247 offset:16640\n\t" \
  "s_add_u32 m0, %[lw], 35840\n\t" \
  "s_nop 0\n\t" \
  "global_load_lds_dwordx4 v251, s[92:93] sc1\n\t" \
  "ds_write_b128 v252, v[136:139] offset:0\n\t" \
  "ds_write_b128 v252, v[140:143] offset:1024\n\t" \
  "s_waitcnt lgkmcnt(0)\n\t" \
  "s_waitcnt vmcnt(4)\n\t" \
  "s_barrier\n\t" \
  "s_setprio 2\n\t" \
  "v_mfma_f32_16x16x32_bf16 v[144:147], v[176:179], v[0:3], 0\n\t" \
  "v_mfma_f32_16x16x32_bf16 v[152:155], v[176:179], v[4:7], 0\n\t" \
  "v_mfma_f32_16x16x32_bf16 v[148:151], v[180:183], v[0:3], 0\n\t" \
  "v_mfma_f32_16x16x32_bf16 v[156:159], v[180:183], v[4:7], 0\n\t" \
  "v_mfma_f32_16x16x32_bf16 v[144:147], v[184:187], v[8:11], v[144:147]\n\t" \
  "v_mfma_f32_16x16x32_bf16 v[152:155], v[184:187], v[12:15], v[152:155]\n\t" \
  "v_mfma_f32_16x16x32_bf16 v[148:151], v[188:191], v[8:11], v[148:151]\n\t" \
  "v_mfma_f32_16x16x32_bf16 v[156:159], v[188:191], v[12:15], v[156:159]\n\t" \
  "v_mfma_f32_16x16x32_bf16 v[144:147], v[192:195], v[16:19], v[144:147]\n\t" \
  "v_mfma_f32_16x16x32_bf16 v[152:155], v[192:195], v[20:23], v[152:155]\n\t" \
  "v_mfma_f32_16x16x32_bf16 v[148:151], v[196:199], v[16:19], v[148:151]\n\t" \
  "v_mfma_f32_16x16x32_bf16 v[156:159], v[196:199], v[20:23], v[156:159]\n\t" \
  "v_mfma_f32_16x16x32_bf16 v[144:147], v[200:203], v[24:27], v[144:147]\n\t" \
  "v_mfma_f32_16x16x32_bf16 v[152:155], v[200:203], v[28:31], v[152:155]\n\t" \
  "v_mfma_f32_16x16x32_bf16 v[148:151], v[204:207], v[24:27], v[148:151]\n\t" \
  "v_mfma_f32_16x16x32_bf16 v[156:159], v[204:207], v[28:31], v[156:159]\n\t" \
  "v_mfma_f32_16x16x32_bf16 v[144:147], v[208:211], v[32:35], v[144:147]\n\t" \
  "v_mfma_f32_16x16x32_bf16 v[152:155], v[208:211], v[36:39], v[152:155]\n\t" \
  "v_mfma_f32_16x16x32_bf16 v[148:151], v[212:215], v[32:35], v[148:151]\n\t" \
  "v_mfma_f32_16x16x32_bf16 v[156:159], v[212:215], v[36:39], v[156:159]\n\t" \
  "v_mfma_f32_16x16x32_bf16 v[144:147], v[216:219], v[40:43], v[144:147]\n\t" \
  "v_mfma_f32_16x16x32_bf16 v[152:155], v[216:219], v[44:47], v[152:155]\n\t" \
  "v_mfma_f32_16x16x32_bf16 v[148:151], v[220:223], v[40:43], v[148:151]\n\t" \
  "v_mfma_f32_16x16x32_bf16 v[156:159], v[220:223], v[44:47], v[156:159]\n\t" \
  "v_mfma_f32_16x16x32_bf16 v[144:147], v[224:227], v[48:51], v[144:147]\n\t" \
  "v_mfma_f32_16x16x32_bf16 v[152:155], v[224:227], v[52:55], v[152:155]\n\t" \
  "v_mfma_f32_16x16x32_bf16 v[148:151], v[228:231], v[48:51], v[148:151]\n\t" \
  "v_mfma_f32_16x16x32_bf16 v[156:159], v[228:231], v[52:55], v[156:159]\n\t" \
  "v_mfma_f32_16x16x32_bf16 v[144:147], v[232:235], v[56:59], v[144:147]\n\t" \
  "v_mfma_f32_16x16x32_bf16 v[152:155], v[232:235], v[60:63], v[152:155]\n\t" \
  "v_mfma_f32_16x16x32_bf16 v[148:151], v[236:239], v[56:59], v[148:151]\n\t" \
  "v_mfma_f32_16x16x32_bf16 v[156:159], v[236:239], v[60:63], v[156:159]\n\t" \
  "s_setprio 0\n\t" \
  "s_add_u32 s92, s92, 65024\n\t" \
  "s_addc_u32 s93, s93, 0\n\t" \
  "ds_read_b128 v[160:163], v253 offset:0\n\t" \
  "ds_read_b128 v[164:167], v253 offset:1024\n\t" \
  "ds_read_b128 v[176:179], v244 offset:32768\n\t" \
  "ds_read_b128 v[180:183], v244 offset:49152\n\t" \
  "ds_read_b128 v[184:187], v245 offset:32768\n\t" \
  "ds_read_b128 v[188:191], v245 offset:49152\n\t" \
  "s_add_u32 m0, %[lw], 65536\n\t" \
  "s_nop 0\n\t" \
  "global_load_lds_dwordx4 v248, s[92:93] sc1\n\t" \
  "ds_read_b128 v[192:195], v246 offset:32768\n\t" \
  "ds_read_b128 v[196:199], v246 offset:49152\n\t" \
  "ds_read_b128 v[200:203], v247 offset:32768\n\t" \
  "ds_read_b128 v[204:207], v247 offset:49152\n\t" \
  "s_add_u32 m0, %[lw], 66560\n\t" \
  "s_nop 0\n\t" \
  "global_load_lds_dwordx4 v249, s[92:93] sc1\n\t" \
  "ds_read_b128 v[208:211], v244 offset:33024\n\t" \
  "ds_read_b128 v[212:215], v244 offset:49408\n\t" \
  "ds_read_b128 v[216:219], v245 offset:33024\n\t" \
  "ds_read_b128 v[220:223], v245 offset:49408\n\t" \
  "s_add_u32 m0, %[lw], 67584\n\t" \
  "s_nop 0\n\t" \
  "global_load_lds_dwordx4 v250, s[92:93] sc1\n\t" \
  "ds_read_b128 v[224:227], v246 offset:33024\n\t" \
  "ds_read_b128 v[228:231], v246 offset:49408\n\t" \
  "ds_read_b128 v[232:235], v247 offset:33024\n\t" \
  "ds_read_b128 v[236:239], v247 offset:49408\n\t" \
  "s_add_u32 m0, %[lw], 68608\n\t" \
  "s_nop 0\n\t" \
  "global_load_lds_dwordx4 v251, s[92:93] sc1\n\t" \
  "s_waitcnt lgkmcnt(15)\n\t" \
  "s_waitcnt lgkmcnt(12)\n\t" \
  "s_sub_i32 s91, s86, 2\n\t" \
  "v_mov_b32 v173, s91\n\t" \
  "buffer_store_dword v173, v174, %[rs], 0 offen sc1\n\t" \
  "s_lshr_b32 s90, s87, 7\n\t" \
  "s_xor_b32 s90, s90, %[s0]\n\t" \
  "s_and_b32 s90, s90, 1\n\t" \
  "s_lshl1_add_u32 s90, s87, s90\n\t" \
  "s_lshl_b32 s90, s90, 13\n\t" \
  "v_add_f32 v160, v160, v128\n\t" \
  "v_add_f32 v161, v161, v129\n\t" \
  "v_add_f32 v162, v162, v130\n\t" \
  "v_add_f32 v163, v163, v131\n\t" \
  "v_add_f32 v164, v164, v132\n\t" \
  "v_add_f32 v165, v165, v133\n\t" \
  "v_add_f32 v166, v166, v134\n\t" \
  "v_add_f32 v167, v167, v135\n\t" \
  "v_cvt_pk_bf16_f32 v168, v160, v161\n\t" \
  "v_cvt_pk_bf16_f32 v169, v162, v163\n\t" \
  "v_cvt_pk_bf16_f32 v170, v164, v165\n\t" \
  "v_cvt_pk_bf16_f32 v171, v166, v167\n\t" \
  "buffer_store_dwordx4 v[168:171], v172, %[rt], s90 offen sc1\n\t" \
  "s_add_u32 s86, s86, 1\n\t" \
  "s_add_u32 s87, s87, 1\n\t" \
  "s_waitcnt lgkmcnt(0)\n\t" \
  "s_cmp_lt_u32 s86, 64\n\t" \
  "s_cbranch_scc1 .Lp1%=\n\t" \
  "s_barrier\n\t" \
  "s_setprio 2\n\t" \
  "v_mfma_f32_16x16x32_bf16 v[144:147], v[176:179], v[64:67], v[144:147]\n\t" \
  "v_mfma_f32_16x16x32_bf16 v[152:155], v[176:179], v[68:71], v[152:155]\n\t" \
  "v_mfma_f32_16x16x32_bf16 v[148:151], v[180:183], v[64:67], v[148:151]\n\t" \
  "v_mfma_f32_16x16x32_bf16 v[156:159], v[180:183], v[68:71], v[156:159]\n\t" \
  "v_mfma_f32_16x16x32_bf16 v[144:147], v[184:187], v[72:75], v[144:147]\n\t" \
  "v_mfma_f32_16x16x32_bf16 v[152:155], v[184:187], v[76:79], v[152:155]\n\t" \
  "v_mfma_f32_16x16x32_bf16 v[148:151], v[188:191], v[72:75], v[148:151]\n\t" \
  "v_mfma_f32_16x16x32_bf16 v[156:159], v[188:191], v[76:79], v[156:159]\n\t" \
  "v_mfma_f32_16x16x32_bf16 v[144:147], v[192:195], v[80:83], v[144:147]\n\t" \
  "v_mfma_f32_16x16x32_bf16 v[152:155], v[192:195], v[84:87], v[152:155]\n\t" \
  "v_mfma_f32_16x16x32_bf16 v[148:151], v[196:199], v[80:83], v[148:151]\n\t" \
  "v_mfma_f32_16x16x32_bf16 v[156:159], v[196:199], v[84:87], v[156:159]\n\t" \
  "v_mfma_f32_16x16x32_bf16 v[144:147], v[200:203], v[88:91], v[144:147]\n\t" \
  "v_mfma_f32_16x16x32_bf16 v[152:155], v[200:203], v[92:95], v[152:155]\n\t" \
  "v_mfma_f32_16x16x32_bf16 v[148:151], v[204:207], v[88:91], v[148:151]\n\t" \
  "v_mfma_f32_16x16x32_bf16 v[156:159], v[204:207], v[92:95], v[156:159]\n\t" \
  "v_mfma_f32_16x16x32_bf16 v[144:147], v[208:211], v[96:99], v[144:147]\n\t" \
  "v_mfma_f32_16x16x32_bf16 v[152:155], v[208:211], v[100:103], v[152:155]\n\t" \
  "v_mfma_f32_16x16x32_bf16 v[148:151], v[212:215], v[96:99], v[148:151]\n\t" \
  "v_mfma_f32_16x16x32_bf16 v[156:159], v[212:215], v[100:103], v[156:159]\n\t" \
  "v_mfma_f32_16x16x32_bf16 v[144:147], v[216:219], v[104:107], v[144:147]\n\t" \
  "v_mfma_f32_16x16x32_bf16 v[152:155], v[216:219], v[108:111], v[152:155]\n\t" \
  "v_mfma_f32_16x16x32_bf16 v[148:151], v[220:223], v[104:107], v[148:151]\n\t" \
  "v_mfma_f32_16x16x32_bf16 v[156:159], v[220:223], v[108:111], v[156:159]\n\t" \
  "v_mfma_f32_16x16x32_bf16 v[144:147], v[224:227], v[112:115], v[144:147]\n\t" \
  "v_mfma_f32_16x16x32_bf16 v[152:155], v[224:227], v[116:119], v[152:155]\n\t" \
  "v_mfma_f32_16x16x32_bf16 v[148:151], v[228:231], v[112:115], v[148:151]\n\t" \
  "v_mfma_f32_16x16x32_bf16 v[156:159], v[228:231], v[116:119], v[156:159]\n\t" \
  "v_mfma_f32_16x16x32_bf16 v[144:147], v[232:235], v[120:123], v[144:147]\n\t" \
  "v_mfma_f32_16x16x32_bf16 v[152:155], v[232:235], v[124:127], v[152:155]\n\t" \
  "v_mfma_f32_16x16x32_bf16 v[148:151], v[236:239], v[120:123], v[148:151]\n\t" \
  "v_mfma_f32_16x16x32_bf16 v[156:159], v[236:239], v[124:127], v[156:159]\n\t" \
  "s_setprio 0\n\t" \
  "s_nop 4\n\t" \
  "ds_write_b128 v252, v[152:155] offset:0\n\t" \
  "s_nop 0\n\t" \
  "ds_write_b128 v252, v[156:159] offset:1024\n\t" \
  "s_waitcnt lgkmcnt(0)\n\t" \
  "s_barrier\n\t" \
  "ds_read_b128 v[160:163], v253 offset:0\n\t" \
  "ds_read_b128 v[164:167], v253 offset:1024\n\t" \
  "s_waitcnt lgkmcnt(0)\n\t" \
  "s_lshr_b32 s90, s87, 7\n\t" \
  "s_xor_b32 s90, s90, %[s0]\n\t" \
  "s_and_b32 s90, s90, 1\n\t" \
  "s_lshl1_add_u32 s90, s87, s90\n\t" \
  "s_lshl_b32 s90, s90, 13\n\t" \
  "v_add_f32 v160, v160, v144\n\t" \
  "v_add_f32 v161, v161, v145\n\t" \
  "v_add_f32 v162, v162, v146\n\t" \
  "v_add_f32 v163, v163, v147\n\t" \
  "v_add_f32 v164, v164, v148\n\t" \
  "v_add_f32 v165, v165, v149\n\t" \
  "v_add_f32 v166, v166, v150\n\t" \
  "v_add_f32 v167, v167, v151\n\t" \
  "v_cvt_pk_bf16_f32 v168, v160, v161\n\t" \
  "v_cvt_pk_bf16_f32 v169, v162, v163\n\t" \
  "v_cvt_pk_bf16_f32 v170, v164, v165\n\t" \
  "v_cvt_pk_bf16_f32 v171, v166, v167\n\t" \
  "buffer_store_dwordx4 v[168:171], v172, %[rt], s90 offen sc1\n\t" \
  "s_waitcnt vmcnt(0)\n\t" \
  "s_barrier\n\t" \
  "s_sub_i32 s91, s86, 1\n\t" \
  "v_mov_b32 v173, s91\n\t" \
  "buffer_store_dword v173, v174, %[rs], 0 offen sc1\n\t" \
  "s_mov_b32 m0, s94\n\t" \
  ".Le%=:\n\t"
constexpr int W_X = 131072, W_PM = W_X + 16384;
template <bool ISV>
__device__ __forceinline__ void job_proj(Args& p, int g, int which, int h, char* smem, __amdgpu_buffer_rsrc_t rsy) {
  const int tid = threadIdx.x, lane = tid & 63, r = lane & 15, q = lane >> 4;
  const int w = __builtin_amdgcn_readfirstlane(tid >> 6), kh = w >> 2, cg = w & 3;
  u32x32 W[4];
  const unsigned lw = lds_off(smem) + w * 4096;
  u32x4 vx;
  u32x2 tag;
  const char* xn = p.ws + WS_XN;
  auto row = [&](int t, int i) {
    const char* src = xn + (size_t)(min(t, 127) >> 1) * (SEG * XLD * 2) + (t & 1) * 512;
    unsigned keep;
    asm volatile("s_mov_b32 %0, m0\n\ts_mov_b32 m0, %3\n\ts_nop 0\n\tglobal_load_lds_dwordx4 %1, %2 sc1\n\ts_mov_b32 m0, %0"
                 : "=&s"(keep) : "v"(vx[i]), "s"(src), "s"(lw + (t & 3) * 32768 + i * 1024) : "memory");
  };
  {
    const u16* Wm = which == 0 ? p.Wk : p.Wq;
    asm volatile("" : "+s"(Wm));
    if (ISV) Wm = p.Wv;
    const unsigned vo = (tid >> 4) * (HD * 2) + 16 * ((lane & 15) ^ swzB((tid >> 4) & 15));
    auto stage = [&](int j) {
      const char* src = (const char*)(Wm + (size_t)(WREV ? 7 - j : j) * 64 * HD + h * DH);
      const unsigned dst = lds_off(smem) + (j & 3) * 32768 + w * 1024;
      glds2(src, 32 * HD * 2, vo, dst);
      if (TH_P < 63) HOLD_V((h & 3) ^ 1, TH_P);
      glds2(src + 512 * HD * 2, 32 * HD * 2, vo, dst + 16384);
      if (TH_P < 63) HOLD_V((h & 3) ^ 1, TH_P);
    };
    if (which == 1) { for (int z = 0; z < Q_DELAY; ++z) __builtin_amdgcn_s_sleep(16); }
    const unsigned own = min((h & LN_M) ^ LN_X, 1);
    tag = start_rows<16>(p, 32 + g, own, [&]() { stage(0); stage(1); stage(2); stage(3); });
#pragma unroll
    for (int i = 0; i < 4; ++i) vx[i] = ((4 * w + i) ^ (ISV ? 0 : (lane >> 1) & 16)) * (XLD * 2) + (lane >> 5) * 1024 + (((lane & 31) ^ ((4 * w + i) & 15)) << 4);
    Boot bt;
#pragma unroll
    for (int j = 0; j < 8; ++j) {
      if (j < 4) WAIT_V(16); else if (j == 4) WAIT_V(12); else if (j == 5) WAIT_V(8); else if (j == 6) WAIT_V(4); else WAIT_V(10);
      if (j == 6 && w == 0) lds_at(smem + W_PM)[0] = boot_done(p.ws, bt, true, tag);
      LBAR();
      if (j == 4 && own) report_rows(p, 32 + g, tag);
      const char* sb = smem + (j & 3) * 32768 + kh * 16384;
#pragma unroll
      for (int k = 0; k < 4; ++k) {
        const int ks = k >> 1, a = k & 1;
        const unsigned ch = ISV ? 2 * (2 * cg + (a ^ kh)) + ((r & 3) >> 1) : 4 * cg + (r & 3), by = ISV ? 8 * (r & 1) : 8 * a;
        s16x4 lo = __builtin_amdgcn_ds_read_tr16_b64_v4i16((lds_s16x4_ptr)(sb + offB(32 * ks + 8 * q + (r >> 2), ch) + by));
        s16x4 hi = __builtin_amdgcn_ds_read_tr16_b64_v4i16((lds_s16x4_ptr)(sb + offB(32 * ks + 8 * q + 4 + (r >> 2), ch) + by));
        const int f = 4 * (WREV ? 7 - j : j) + k;
        put4(W[f >> 3], f & 7, (bf16x8){lo[0], lo[1], lo[2], lo[3], hi[0], hi[1], hi[2], hi[3]});
      }
      LBAR();
      if (j == 3 && w == 0) boot_ask(p.ws, bt);
      if (j + 4 < 8) stage(j + 4);
      if (j == 6) {
#pragma unroll
        for (int i = 0; i < 10; ++i) row(i >> 2, i & 3);
      }
    }
    p.s0 = uni(lds_at(smem + W_PM)[0] >> 31);
  }
  RDY(128 + g);
  typedef __attribute__((ext_vector_type(8))) unsigned u32x8;
  u32x8 la;
#pragma unroll
  for (int i = 0; i < 8; ++i) la[i] = (i >> 2) * 65536 + r * 1024 + kh * 512 + (((4 * (i & 3) + q) ^ r) << 4);
  const unsigned xw = W_X + w * 2048 + lane * 16;
  u32x32 acc;
  asm volatile("" : "={v[128:159]}"(acc));
  WAIT_V(6);
  LBAR();
  {
    const int g0 = 0;
#define PR_LOOP(TXT)                                                                                                              \
    asm volatile(TXT                                                                                                              \
        : "+{v[128:159]}"(acc)                                                                                                    \
        : W4_IN, "{v[208:215]}"(la), "{v[216:219]}"(vx), "{v220}"(xw), "{v221}"(xw ^ 8192),                                       \
          "{v172}"((ISV ? 2 * cg + kh : 4 * kh + cg) * 1024 + lane * 16), "{v174}"(tid == 256 ? fkq(h, which) : 0x7F000000),      \
          [rt] "s"(raw_rsrc(p.ws + WS_T, T_BYTES)), [rs] "s"(raw_rsrc(p.ws + WS_SY, SY_TOTAL)), [s0] "s"(p.s0 ^ TF_KQV),          \
          [lw] "s"(lw), [kh] "s"(kh), [g0] "s"(g0), [end] "s"(g0 ? NSEG : PR_G1), [x0] "s"(xn + (g0 + 1) * (SEG * XLD * 2)),                       \
          [t0] "s"(which * T_Q + kseg(0, h) - 1)                                                                                  \
        : CLB8(160, 161, 162, 163, 164, 165, 166, 167), "v168", "v169", "v170", "v171", "v173", CLB8(176, 177, 178, 179, 180, 181, 182, 183), \
          CLB8(184, 185, 186, 187, 188, 189, 190, 191), CLB8(192, 193, 194, 195, 196, 197, 198, 199),                             \
          CLB8(200, 201, 202, 203, 204, 205, 206, 207), "s86", "s87", "s88", "s90", "s91", "s92", "s93", "s94", "scc", "memory")
    if (ISV) PR_LOOP(PR_ASM_1); else PR_LOOP(PR_ASM_0);
    if (w == 0 && g0 == 0) wait_rows(p.ws, tag);
  }
#define PP_LOOP(TXT)                                                                                                              \
  asm volatile(TXT                                                                                                                \
      : "+{v[128:159]}"(acc)                                                                                                      \
      : W4_IN, "{v[240:247]}"(la), "{v[248:251]}"(vx), "{v252}"(xw), "{v253}"(xw ^ 8192),                                         \
        "{v172}"((ISV ? 2 * cg + kh : 4 * kh + cg) * 1024 + lane * 16), "{v174}"(tid == 256 ? fkq(h, which) : 0x7F000000),        \
        [rt] "s"(raw_rsrc(p.ws + WS_T, T_BYTES)), [rs] "s"(raw_rsrc(p.ws + WS_SY, SY_TOTAL)), [s0] "s"(p.s0 ^ TF_KQV),            \
        [lw] "s"(lw), [kh] "s"(kh), [g0] "n"(PR_G1), [x0] "s"(xn + (PR_G1 + 1) * (SEG * XLD * 2)),                                \
        [t0] "s"(which * T_Q + kseg(0, h) - 1)                                                                                    \
      : CLB8(160, 161, 162, 163, 164, 165, 166, 167), "v168", "v169", "v170", "v171", "v173", CLB8(176, 177, 178, 179, 180, 181, 182, 183), \
        CLB8(184, 185, 186, 187, 188, 189, 190, 191), CLB32(192), CLB8(224, 225, 226, 227, 228, 229, 230, 231),                   \
        CLB8(232, 233, 234, 235, 236, 237, 238, 239), "s86", "s87", "s90", "s91", "s92", "s93", "s94", "scc", "memory")
  if (ISV) PP_LOOP(PP_ASM_1); else PP_LOOP(PP_ASM_0);
}

enum { J_NONE = 0, J_GU = 3, J_PR = 5, J_CB = 8, J_DN = 14 };
struct BigJob { int type, panel, idx; };
struct BigPtr { const char *a, *b0, *b1; };
constexpr int G_LDA = XLD * 2, G_LDB = FF * 2;

__device__ __forceinline__ BigPtr big_ptrs(const Args& p, const BigJob& j) {
  BigPtr r;
  r.a = p.ws + WS_XN + (size_t)j.panel * 128 * G_LDA;
  r.b0 = (const char*)(p.wg + j.idx * 128); r.b1 = (const char*)(p.wu + j.idx * 128);
  return r;
}
__device__ __forceinline__ void big_publish(const Args& p, const BigJob& j) {
  int tid = threadIdx.x;
  asm volatile("" : "+v"(tid));
  if (j.type == J_GU && tid == 0)
    __hip_atomic_fetch_add((gu32*)(p.ws + WS_SY + SY_CH) + j.panel * 16, 1u, __ATOMIC_RELAXED, __HIP_MEMORY_SCOPE_AGENT);
}
typedef f32x4 Acc[2][2][4][2];
__device__ __forceinline__ u32x4 pack32(f32x4 a, f32x4 b) {
  return widen32(cvtpk(a[0], a[1]), cvtpk(a[2], a[3]), cvtpk(b[0], b[1]), cvtpk(b[2], b[3]));
}
#define st_epi(r, o, v) st_sc1(r, o, v)
__device__ __forceinline__ void big_epilogue(const Args& p, const BigJob& j, Acc& acc, char* smem) {
  int tid = threadIdx.x;
  asm volatile("" : "+v"(tid));
  const int lane = tid & 63;
  const int wid = __builtin_amdgcn_readfirstlane(tid >> 6);
  const int wr = wid >> 2, wc = wid & 3;
  {
    const __amdgpu_buffer_rsrc_t ro = mkrsrc(p.ws + WS_HF, S_LEN * 16384);
#pragma unroll
    for (int i = 0; i < 8; ++i) {
      f32x4 hh[2];
#pragma unroll
      for (int n = 0; n < 2; ++n)
#pragma unroll
        for (int jj = 0; jj < 4; ++jj) {
          const float g = acc[i >> 2][0][i & 3][n][jj], u = acc[i >> 2][1][i & 3][n][jj];
          hh[n][jj] = __builtin_amdgcn_rcpf(1.0f + __builtin_amdgcn_exp2f(g * -1.4426950408889634f)) * u;
        }
      st_epi(ro, h_off(p, j.panel + (i >> 2)) + (j.idx * 4 + wc) * 16384 + (wr * 4 + (i & 3)) * 1024 + lane * 16,
             ((u32x4){cvtpk(hh[0][0], hh[0][1]), cvtpk(hh[0][2], hh[0][3]), cvtpk(hh[1][0], hh[1][1]), cvtpk(hh[1][2], hh[1][3])}));
    }
  }
}
#define JST(n, i, v) do {} while (0)
__device__ __forceinline__ void gemm_role(Args& p, int g, char* smem) {
  const __amdgpu_buffer_rsrc_t rsy = mkrsrc(p.ws + WS_SY, SY_TOTAL);
  const int x = g & 3, k = g >> 2;
  volatile unsigned* slot = reinterpret_cast<volatile unsigned*>(smem + 163824);
  int rnd = k < 24 ? 2 : -1;
  int nj = 0; (void)nj;
  auto claim = [&]() {
    ++rnd;
    const int n = k < 24 ? 32 + k : rnd * 8 + k - 24, pn = (n * 37) >> 8;
    BigJob b;
    b.type = rnd < 4 ? J_GU : (rnd == 4 ? J_DN : (rnd == 5 && k >= 24 && k - 24 < 14 - NFUSE ? J_CB : J_NONE));
    b.panel = rnd < 4 ? 14 - 2 * pn : (rnd == 4 ? 4 * ((2 - (k >> 3)) & 3) + x : NFUSE + k - 24);
    b.idx = rnd < 4 ? x + 4 * (n - 7 * pn) : k & 7;
    return b;
  };
  if (g < 4 && threadIdx.x == 0) { LST(480 + 4 * g); LSTC(480 + 4 * g + 1); }
  if (k < 24) {
    JST(nj, 0, J_PR); JST(nj, 1, __builtin_amdgcn_s_memrealtime());
    if (k >= 16) job_proj<true>(p, g, 2, x * 8 + (k & 7), smem, rsy);
    else job_proj<false>(p, g, k >> 3, x * 8 + (k & 7), smem, rsy);
    JST(nj, 3, __builtin_amdgcn_s_memrealtime()); ++nj;
  } else {
    const unsigned t0 = (unsigned)__builtin_amdgcn_s_memrealtime();
    u32x2 tag;
#pragma unroll 1
    for (int u = 0; u < (k < 30 && LN_M ? 2 : 1); ++u) {
      const int rg = u ? 32 + (4 * (k - 24) + 1) * 4 + x : g - 96;
      tag = start_rows<0>(p, rg, 1, []() {});
      WAIT_V(0);
      LBAR();
      report_rows(p, rg, tag);
    }
    while ((unsigned)__builtin_amdgcn_s_memrealtime() - t0 < PF_T0) __builtin_amdgcn_s_sleep(8);
#pragma unroll 1
    for (int i = GPF == 2 ? 2 : 0; i < PF_N; i += GPF == 2 ? 3 : 1) {
      const int m = i % 3, o = ((i / 3) * 32 + g - 96) * 448 + (int)threadIdx.x;
      if (threadIdx.x < 448) touch_line((const char*)(m == 0 ? p.wg : (m == 1 ? p.wu : p.wd)) + (size_t)o * 128, smem + (threadIdx.x >> 6) * 256);
      WAIT_V(1);
    }
    WAIT_V(0);
    if (threadIdx.x < 64) {
      wait_rows(p.ws, tag);
      Boot bt;
      boot_ask(p.ws, bt);
      WAIT_V(0);
      *slot = boot_done(p.ws, bt, true, tag);
      while ((unsigned)__builtin_amdgcn_s_memrealtime() - t0 < GU_DELAY) __builtin_amdgcn_s_sleep(8);
    }
    LBAR();
    p.s0 = *slot >> 31;
    RDY(128 + g);
  }
  p.s0 = uni(p.s0);
  BigJob cur = claim();
  while (cur.type != J_NONE) {
    JST(nj, 0, cur.type | cur.panel << 8); JST(nj, 1, __builtin_amdgcn_s_memrealtime());
    if (cur.type != J_GU) {
      if (cur.type == J_DN) job_down(p, cur.panel, cur.idx, smem, rsy);
      else job_comb(p, cur.panel, x, rsy);
      JST(nj, 3, __builtin_amdgcn_s_memrealtime()); ++nj;
      cur = claim();
      continue;
    }
    int tid = threadIdx.x;
    asm volatile("" : "+v"(tid));
    const int lane = tid & 63;
    const int wid = __builtin_amdgcn_readfirstlane(tid >> 6);
    const int wr = wid >> 2, wc = wid & 3;
    const unsigned ra = tid >> 3, ca = 16 * ((tid & 7) ^ ((tid >> 3) & 7)), rb = tid >> 4, cb = 16 * ((tid & 15) ^ swzB((tid >> 4) & 15));
    const unsigned lw = lds_off(smem) + wid * 1024;
#define B8_SA(b, h) (((b) * 2 + (h)) * 16384)
#define B8_SB(b, h) ((4 + (b) * 2 + (h)) * 16384)
#define B8_STA(b, h, P, kt) glds2<true>((P).a + (size_t)(h) * 128 * G_LDA + (kt) * 128, 64 * G_LDA, ra * G_LDA + ca, lw + B8_SA(b, h))
#define B8_STB(b, h, P, kt) glds2(((h) ? (P).b1 : (P).b0) + (size_t)(kt) * 64 * G_LDB, 32 * G_LDB, rb * G_LDB + cb, lw + B8_SB(b, h))
#define B8_LDA(b, h) do { _Pragma("unroll") for (int m = 0; m < 4; ++m) _Pragma("unroll") for (int kk = 0; kk < 2; ++kk) \
      At[m][kk] = ldsf(smem + B8_SA(b, h) + offA(wr * 64 + m * 16 + (lane & 15), kk * 4 + (lane >> 4))); } while (0)
#define B8_LDB(dst, b, h) do { _Pragma("unroll") for (int n = 0; n < 2; ++n) _Pragma("unroll") for (int kk = 0; kk < 2; ++kk) \
      dst[n][kk] = ld_bfrag(smem + B8_SB(b, h), kk, wc * 2 + n, lane); } while (0)
#define B8_MMA(ai, bj, Bt) do { __builtin_amdgcn_s_setprio(1); _Pragma("unroll") for (int m = 0; m < 4; ++m) _Pragma("unroll") for (int n = 0; n < 2; ++n) \
      _Pragma("unroll") for (int kk = 0; kk < 2; ++kk) acc[ai][bj][m][n] = MFMA(Bt[n][kk], At[m][kk], acc[ai][bj][m][n]); __builtin_amdgcn_s_setprio(0); } while (0)
#define WAIT_L(n) asm volatile("s_waitcnt lgkmcnt(%0)" :: "n"(n) : "memory")
#define BAR() __builtin_amdgcn_s_barrier()
#define WAIT_RX(c) asm volatile("s_waitcnt vmcnt(16)\n\ts_cmp_lg_u32 %0, 0\n\ts_cbranch_scc1 .Lrx%=\n\ts_waitcnt vmcnt(8)\n.Lrx%=:" :: "s"(c) : "memory", "scc")
    Acc acc;
    bf16x8 At[4][2], B0[2][2], B1[2][2];
    BigPtr cp = big_ptrs(p, cur);
    BigJob pend; pend.type = J_NONE; pend.panel = 0; pend.idx = 0;
    LBAR();
    B8_STB(0, 0, cp, 0); B8_STB(0, 1, cp, 0); B8_STA(0, 0, cp, 0); B8_STA(0, 1, cp, 0);
    if (wr == 1) BAR();
    WAIT_V(2); BAR();
    B8_STB(1, 0, cp, 1); B8_STA(1, 0, cp, 1); B8_STB(1, 1, cp, 1);
    WAIT_V(6); BAR();
    BigJob nx = pend;
    for (;;) {
      bool chain = false;
      BigPtr np = cp;
      constexpr int nt = DM / 64;
#pragma unroll
      for (int i = 0; i < 32; ++i) acc[i >> 4][(i >> 3) & 1][(i >> 1) & 3][i & 1] = (f32x4){0.f, 0.f, 0.f, 0.f};
#pragma unroll 1
      for (int t = 0; t < nt; t += 2) {
        const bool last = t == nt - 2;
        if (last) {
          nx = claim();
          chain = nx.type == J_GU;
          if (chain) np = big_ptrs(p, nx);
        }
        const BigPtr sp = np;
        const int t2 = last ? 0 : t + 2;
        if (t == 2) big_publish(p, pend);
        const int rx = __builtin_amdgcn_readfirstlane(t == 0 && pend.type != J_NONE);
        B8_LDB(B0, 0, 0); B8_LDB(B1, 0, 1); SCHED(); B8_LDA(0, 0); B8_STA(1, 1, cp, t + 1);
        WAIT_RX(rx);
        WAIT_L(0); BAR(); B8_MMA(0, 0, B0); B8_MMA(0, 1, B1); BAR(); SCHED();
        B8_LDA(0, 1); B8_STB(0, 0, sp, t2); B8_STB(0, 1, sp, t2); B8_STA(0, 0, sp, t2);
        WAIT_RX(rx);
        WAIT_L(0); BAR(); B8_MMA(1, 0, B0); B8_MMA(1, 1, B1); BAR(); SCHED();
        B8_LDB(B0, 1, 0); B8_LDB(B1, 1, 1); SCHED(); B8_LDA(1, 0); B8_STA(0, 1, sp, t2);
        WAIT_V(8); WAIT_L(0); BAR(); B8_MMA(0, 0, B0); B8_MMA(0, 1, B1); BAR(); SCHED();
        B8_LDA(1, 1); B8_STB(1, 0, sp, t2 + 1); B8_STB(1, 1, sp, t2 + 1); B8_STA(1, 0, sp, t2 + 1);
        WAIT_V(8); WAIT_L(0); BAR(); B8_MMA(1, 0, B0); B8_MMA(1, 1, B1); BAR(); SCHED();
      }
      if (wr == 0) BAR();
      JST(nj, 2, __builtin_amdgcn_s_memrealtime());
      big_epilogue(p, cur, acc, smem);
      if (!chain) break;
      JST(nj, 3, __builtin_amdgcn_s_memrealtime()); ++nj;
      pend = cur; cur = nx; cp = np;
      if (wr == 1) BAR();
      JST(nj, 0, cur.type | cur.panel << 8); JST(nj, 1, __builtin_amdgcn_s_memrealtime());
    }
    WAIT_V(0);
    BAR();
    asm volatile(".Lcode_end:");
    big_publish(p, cur);
    JST(nj, 3, __builtin_amdgcn_s_memrealtime()); ++nj;
    cur = nx;
  }
  if (threadIdx.x == 0) { LST(128 + g); }
  if (g < 4 && threadIdx.x == 0) { LST(480 + 4 * g + 2); LSTC(480 + 4 * g + 3); }
}

__device__ __forceinline__ unsigned now_q() { return (unsigned)__builtin_amdgcn_s_memrealtime() * 4u; }
__device__ __forceinline__ void pace_until(unsigned t) {
#pragma unroll 1
  for (int i = 0; i < 256; ++i) {
    const int d = (int)(t - now_q());
    if (d <= 0) break;
    if (d > 32) __builtin_amdgcn_s_sleep(1);
  }
}
#define CLB_E "s76", "s77", "s78", "s79", "s80", "s81", "s82", "s83", "s84", "s85", "s86", "s87", "s88", "s89"
#define EDGE_ASM \
  "s_memtime s[76:77]\n\t" \
  "s_memrealtime s[78:79]\n\t" \
  "s_nop 3\n\t" \
  "s_memrealtime s[80:81]\n\t" \
  "s_nop 3\n\t" \
  "s_memrealtime s[82:83]\n\t" \
  "s_nop 3\n\t" \
  "s_memrealtime s[84:85]\n\t" \
  "s_nop 3\n\t" \
  "s_memrealtime s[86:87]\n\t" \
  "s_nop 3\n\t" \
  "s_memrealtime s[88:89]\n\t" \
  "s_mov_b32 s93, 0\n\t" \
  "s_waitcnt lgkmcnt(0)\n\t" \
  "s_cmp_eq_u32 s80, s78\n\t" \
  "s_addc_u32 s93, s93, 0\n\t" \
  "s_cmp_eq_u32 s82, s78\n\t" \
  "s_addc_u32 s93, s93, 0\n\t" \
  "s_cmp_eq_u32 s84, s78\n\t" \
  "s_addc_u32 s93, s93, 0\n\t" \
  "s_cmp_eq_u32 s86, s78\n\t" \
  "s_addc_u32 s93, s93, 0\n\t" \
  "s_cmp_eq_u32 s88, s78\n\t" \
  "s_addc_u32 s93, s93, 0\n\t" \
  "s_mul_i32 s93, s93, 21\n\t" \
  "s_add_u32 s76, s76, 15\n\t" \
  "s_add_u32 %[kc], s76, s93\n\t" \
  "s_lshl2_add_u32 %[kq], s78, 16\n\t"
__device__ __forceinline__ unsigned time_of(unsigned cyc) {
  unsigned c, q;
  asm volatile(EDGE_ASM : [kc] "=&s"(c), [kq] "=&s"(q) :: CLB_E, "s93", "scc");
  return q - (((c - cyc) * 11900u) >> 16);
}
#define DS_RED 20
#define DS_MEM 18
#define LEAD_DN 2
#define LEAD_UP 12
__device__ __forceinline__ u32x2 ld2_sc1(__amdgpu_buffer_rsrc_t rs, int off, int soff = 0) {
  return __builtin_amdgcn_raw_buffer_load_b64(rs, off, soff, 16);
}
__device__ __forceinline__ void st2_sc1(__amdgpu_buffer_rsrc_t rs, int off, u32x2 v) {
  __builtin_amdgcn_raw_buffer_store_b64(v, rs, off, 0, 16);
}
__device__ __forceinline__ int row_max(int v) {
  v = max(v, __builtin_amdgcn_update_dpp(0, v, 0x128, 0xf, 0xf, false));
  v = max(v, __builtin_amdgcn_update_dpp(0, v, 0x124, 0xf, 0xf, false));
  v = max(v, __builtin_amdgcn_update_dpp(0, v, 0x122, 0xf, 0xf, false));
  v = max(v, __builtin_amdgcn_update_dpp(0, v, 0x121, 0xf, 0xf, false));
  return v;
}
__device__ __forceinline__ int wave_max(int v) {
  v = row_max(v);
  return max(max(__builtin_amdgcn_readlane(v, 0), __builtin_amdgcn_readlane(v, 16)),
             max(__builtin_amdgcn_readlane(v, 32), __builtin_amdgcn_readlane(v, 48)));
}
__device__ __forceinline__ int track(int est, int x, int up, int dn, int out) {
  const int d = x - est;
  return est + (d > out ? 2 : max(min(d, up), -dn));
}
#define WSL 12
#define PLAN_SLEEP 2
__device__ __forceinline__ unsigned latest(unsigned t) {
  const unsigned t0 = __builtin_amdgcn_readfirstlane(t);
  return t0 + wave_max((int)(t - t0));
}
#define RL0 300
__device__ __forceinline__ void watch(__amdgpu_buffer_rsrc_t rsy, int s, int lane, unsigned t0, const unsigned* tgt_, gu32* abortw, int hs) {
  pace_until(t0);
  const int rl = (int)__builtin_amdgcn_raw_buffer_load_b32(rsy, lane * 128, SY_BR + hs, 16);
  u32x2 ra;
  for (unsigned spins = 0;;) {
    asm volatile("" ::: "memory");
    ra = ld2_sc1(rsy, (lane & 31) * 128, SY_BA + hs);
    const bool all = __all((int)ra[0] >= s);
    SMP(4, s, spins);
    if (all) break;
    if (spins < 3) __builtin_amdgcn_s_sleep(PLAN_SLEEP); else __builtin_amdgcn_s_sleep(WSL);
    if (spin_fail(spins, abortw)) break;
  }
  const unsigned t = latest(ra[0] == (unsigned)s ? ra[1] : t0) + wave_max(rl < 0 ? RL0 : rl) + (DS_RED + DS_MEM);
  lds_vu_ptr tgt = lds_at(tgt_);
  if (lane < 2) tgt[lane] = lane ? t : s + 1;
  SMP(0, s, (int)(t - t0)); SMP(1, s, (int)(t - now_q()));
}
__device__ __forceinline__ unsigned ld_flag(__amdgpu_buffer_rsrc_t rs, int off) {
  return __builtin_amdgcn_raw_buffer_load_b32(rs, off, 0, 16);
}

constexpr unsigned long long QK_MAP[8] = {0xbaf9876d543210ecull, 0xa98f76d543210bceull, 0x98f76d543210ecbaull, 0x876fd543210bcea9ull, 0x76fd543210ecba98ull, 0x65df43210bcea987ull, 0x5df43210ecba9876ull, 0xd43f210bcea98765ull};
#define QK_ASM_0 \
  "s_waitcnt lgkmcnt(0)\n\t" \
  "v_subrev_u32 v244, 0x8000, v250\n\t" \
  "ds_read_b128 v[216:219], v248 offset:32768\n\t" \
  "ds_read_b128 v[220:223], v248 offset:33792\n\t" \
  "ds_read_b128 v[224:227], v248 offset:34816\n\t" \
  "ds_read_b128 v[228:231], v248 offset:35840\n\t" \
  "ds_read_b128 v[208:211], v244 offset:0\n\t" \
  "ds_read_b128 v[212:215], v244 offset:1024\n\t" \
  "ds_read_b128 v[236:239], v244 offset:2048\n\t" \
  "ds_read_b128 v[240:243], v244 offset:3072\n\t" \
  "v_mov_b64 v[192:193], 0\n\t" \
  "v_mov_b64 v[194:195], 0\n\t" \
  "v_mov_b64 v[196:197], 0\n\t" \
  "v_mov_b64 v[198:199], 0\n\t" \
  "s_movk_i32 s92, 0x2000\n\t" \
  ".Lwt%=:\n\t" \
  "ds_read_b64 v[200:201], v248 offset:30720\n\t" \
  "s_memrealtime s[90:91]\n\t" \
  "s_sub_u32 s92, s92, 1\n\t" \
  "s_waitcnt lgkmcnt(0)\n\t" \
  "v_readfirstlane_b32 s93, v200\n\t" \
  "v_readfirstlane_b32 %[tgt], v201\n\t" \
  "s_cbranch_scc1 .Lgo%=\n\t" \
  "s_cmp_eq_u32 s93, %[step]\n\t" \
  "s_cbranch_scc1 .Lwx%=\n\t" \
  "s_sleep 1\n\t" \
  "s_branch .Lwt%=\n\t" \
  ".Lwx%=:\n\t" \
  "s_lshl_b32 s93, s90, 2\n\t" \
  "s_sub_i32 s93, %[tgt], s93\n\t" \
  "s_cmp_le_i32 s93, 0\n\t" \
  "s_cbranch_scc1 .Lgo%=\n\t" \
  "s_min_i32 s93, s93, 8000\n\t" \
  "s_mov_b32 s94, s93\n\t" \
  ".Lsl%=:\n\t" \
  "s_sub_i32 s94, s94, 18\n\t" \
  "s_cmp_lt_i32 s94, 30\n\t" \
  "s_cbranch_scc1 .Lsx%=\n\t" \
  "s_sleep 1\n\t" \
  "s_branch .Lsl%=\n\t" \
  ".Lsx%=:\n\t" \
  "s_sub_i32 s93, s93, 12\n\t" \
  "s_max_i32 s93, s93, 4\n\t" \
  "s_lshr_b32 s93, s93, 2\n\t" \
  "s_add_u32 s93, s93, s90\n\t" \
  "s_memrealtime s[90:91]\n\t" \
  ".Lpace%=:\n\t" \
  "s_memrealtime s[94:95]\n\t" \
  "s_waitcnt lgkmcnt(1)\n\t" \
  "s_sub_i32 s92, s90, s93\n\t" \
  "s_cmp_ge_i32 s92, 0\n\t" \
  "s_cbranch_scc1 .Le%=\n\t" \
  "s_memrealtime s[90:91]\n\t" \
  "s_waitcnt lgkmcnt(1)\n\t" \
  "s_sub_i32 s92, s94, s93\n\t" \
  "s_cmp_lt_i32 s92, 0\n\t" \
  "s_cbranch_scc1 .Lpace%=\n\t" \
  "s_nop 4\n\t" \
  ".Le%=:\n\t" \
  "s_add_u32 s92, s92, s93\n\t" \
  "s_lshl_b32 s92, s92, 2\n\t" \
  "s_sub_i32 s92, %[tgt], s92\n\t" \
  "s_max_i32 s92, s92, 0\n\t" \
  "s_bitcmp0_b32 s92, 3\n\t" \
  "s_cbranch_scc1 .Lf3%=\n\t" \
  "s_nop 15\n\t" \
  ".Lf3%=:\n\t" \
  "s_bitcmp0_b32 s92, 2\n\t" \
  "s_cbranch_scc1 .Lf2%=\n\t" \
  "s_nop 10\n\t" \
  ".Lf2%=:\n\t" \
  ".Lgo%=:\n\t" \
  "s_mov_b32 s92, 0\n\t" \
  ".Lpoll%=:\n\t" \
  "buffer_load_dwordx4 v[200:203], v248, %[rs], %[som] offen sc1\n\t" \
  "buffer_load_dwordx4 v[204:207], v248, %[rs], %[som2] offen sc1\n\t" \
  "s_waitcnt vmcnt(1) lgkmcnt(0)\n\t" \
  "ds_write_b128 v249, v[200:203]\n\t" \
  "v_smfmac_f32_16x16x64_bf16 v[192:195], v[200:203], v[208:215], v251\n\t" \
  "v_max3_u32 v244, v200, v201, v202\n\t" \
  "v_smfmac_f32_16x16x64_bf16 v[196:199], v[200:203], v[236:243], v251\n\t" \
  "s_waitcnt vmcnt(0)\n\t" \
  "ds_write_b128 v249, v[204:207] offset:8192\n\t" \
  "v_smfmac_f32_16x16x64_bf16 v[192:195], v[204:207], v[216:223], v251\n\t" \
  "v_max3_u32 v244, v244, v203, v204\n\t" \
  "v_max3_u32 v244, v244, v205, v206\n\t" \
  "v_smfmac_f32_16x16x64_bf16 v[196:199], v[204:207], v[224:231], v251\n\t" \
  "v_max_u32 v244, v244, v207\n\t" \
  "v_cmp_le_u32 vcc, %[sent], v244\n\t" \
  "s_cbranch_vccnz .Lslow%=\n\t" \
  ".Lok%=:\n\t" \
  "ds_read_b128 v[216:219], v250 offset:32768\n\t" \
  "ds_read_b128 v[220:223], v250 offset:33792\n\t" \
  "ds_read_b128 v[224:227], v250 offset:34816\n\t" \
  "ds_read_b128 v[228:231], v250 offset:35840\n\t" \
  "s_waitcnt lgkmcnt(4)\n\t" \
  "s_barrier\n\t" \
  "ds_read_b128 v[208:211], v248 offset:1024\n\t" \
  "ds_read_b128 v[212:215], v248 offset:2048\n\t" \
  "ds_read_b128 v[236:239], v248 offset:3072\n\t" \
  "ds_read_b128 v[240:243], v248 offset:4096\n\t" \
  "s_waitcnt lgkmcnt(3)\n\t" \
  "ds_read_b128 v[244:247], v248 offset:13312\n\t" \
  "v_smfmac_f32_16x16x64_bf16 v[192:195], v[208:211], v[216:223], v251\n\t" \
  "ds_read_b128 v[216:219], v250 offset:0\n\t" \
  "v_smfmac_f32_16x16x64_bf16 v[196:199], v[208:211], v[224:231], v251\n\t" \
  "s_waitcnt lgkmcnt(4)\n\t" \
  "ds_read_b128 v[220:223], v250 offset:1024\n\t" \
  "v_smfmac_f32_16x16x64_bf16 v[192:195], v[212:215], v[0:7], v251\n\t" \
  "ds_read_b128 v[224:227], v250 offset:2048\n\t" \
  "v_smfmac_f32_16x16x64_bf16 v[196:199], v[212:215], v[8:15], v251\n\t" \
  "s_waitcnt lgkmcnt(5)\n\t" \
  "ds_read_b128 v[228:231], v250 offset:3072\n\t" \
  "v_smfmac_f32_16x16x64_bf16 v[192:195], v[236:239], v[16:23], v251\n\t" \
  "ds_read_b128 v[200:203], v248 offset:5120\n\t" \
  "v_smfmac_f32_16x16x64_bf16 v[196:199], v[236:239], v[24:31], v251\n\t" \
  "s_waitcnt lgkmcnt(6)\n\t" \
  "ds_read_b128 v[204:207], v248 offset:6144\n\t" \
  "v_smfmac_f32_16x16x64_bf16 v[192:195], v[240:243], v[32:39], v251\n\t" \
  "ds_read_b128 v[208:211], v248 offset:7168\n\t" \
  "v_smfmac_f32_16x16x64_bf16 v[196:199], v[240:243], v[40:47], v251\n\t" \
  "s_waitcnt lgkmcnt(5)\n\t" \
  "ds_read_b128 v[212:215], v248 offset:9216\n\t" \
  "v_smfmac_f32_16x16x64_bf16 v[192:195], v[244:247], v[216:223], v251\n\t" \
  "s_waitcnt lgkmcnt(4)\n\t" \
  "v_smfmac_f32_16x16x64_bf16 v[196:199], v[244:247], v[224:231], v251\n\t" \
  "s_waitcnt lgkmcnt(3)\n\t" \
  "ds_read_b128 v[236:239], v248 offset:10240\n\t" \
  "v_smfmac_f32_16x16x64_bf16 v[192:195], v[200:203], v[48:55], v251\n\t" \
  "ds_read_b128 v[240:243], v248 offset:11264\n\t" \
  "v_smfmac_f32_16x16x64_bf16 v[196:199], v[200:203], v[56:63], v251\n\t" \
  "s_waitcnt lgkmcnt(4)\n\t" \
  "v_smfmac_f32_16x16x64_bf16 v[192:195], v[204:207], v[64:71], v251\n\t" \
  "ds_read_b128 v[244:247], v248 offset:12288\n\t" \
  "v_smfmac_f32_16x16x64_bf16 v[196:199], v[204:207], v[72:79], v251\n\t" \
  "s_waitcnt lgkmcnt(4)\n\t" \
  "v_smfmac_f32_16x16x64_bf16 v[192:195], v[208:211], v[80:87], v251\n\t" \
  "ds_read_b128 v[200:203], v248 offset:14336\n\t" \
  "v_smfmac_f32_16x16x64_bf16 v[196:199], v[208:211], v[88:95], v251\n\t" \
  "s_waitcnt lgkmcnt(4)\n\t" \
  "v_smfmac_f32_16x16x64_bf16 v[192:195], v[212:215], v[96:103], v251\n\t" \
  "ds_read_b128 v[204:207], v248 offset:15360\n\t" \
  "v_smfmac_f32_16x16x64_bf16 v[196:199], v[212:215], v[104:111], v251\n\t" \
  "s_waitcnt lgkmcnt(3)\n\t" \
  "v_smfmac_f32_16x16x64_bf16 v[192:195], v[236:239], v[112:119], v251\n\t" \
  "v_smfmac_f32_16x16x64_bf16 v[196:199], v[236:239], v[120:127], v251\n\t" \
  "v_smfmac_f32_16x16x64_bf16 v[192:195], v[240:243], v[128:135], v251\n\t" \
  "v_smfmac_f32_16x16x64_bf16 v[196:199], v[240:243], v[136:143], v251\n\t" \
  "s_waitcnt lgkmcnt(1)\n\t" \
  "v_smfmac_f32_16x16x64_bf16 v[192:195], v[244:247], v[144:151], v251\n\t" \
  "v_smfmac_f32_16x16x64_bf16 v[196:199], v[244:247], v[152:159], v251\n\t" \
  "v_smfmac_f32_16x16x64_bf16 v[192:195], v[200:203], v[160:167], v251\n\t" \
  "v_smfmac_f32_16x16x64_bf16 v[196:199], v[200:203], v[168:175], v251\n\t" \
  "s_waitcnt lgkmcnt(0)\n\t" \
  "v_smfmac_f32_16x16x64_bf16 v[192:195], v[204:207], v[176:183], v251\n\t" \
  "v_smfmac_f32_16x16x64_bf16 v[196:199], v[204:207], v[184:191], v251\n\t" \
  "ds_read_b128 v[200:203], v248 offset:16384\n\t" \
  "ds_read_b128 v[204:207], v248 offset:20480\n\t" \
  "v_mov_b32 v250, v232\n\t" \
  "v_mov_b32 v249, v233\n\t" \
  "s_nop 2\n\t" \
  "v_add_f32 v192, v192, v194\n\t" \
  "v_add_f32 v193, v193, v195\n\t" \
  "v_add_f32 v196, v196, v198\n\t" \
  "v_add_f32 v197, v197, v199\n\t" \
  "v_cvt_pk_bf16_f32 v192, v192, v193\n\t" \
  "v_cvt_pk_bf16_f32 v196, v196, v197\n\t" \
  "ds_write2_b32 v252, v192, v196 offset1:64\n\t" \
  "v_mov_b32 v251, v234\n\t" \
  "ds_read_b64_tr_b16 v[232:233], v250 offset:0\n\t" \
  "ds_read_b64_tr_b16 v[234:235], v250 offset:64\n\t" \
  "v_mov_b32 v212, %[step]\n\t" \
  "s_mov_b64 exec, 1\n\t" \
  "ds_write_b32 v248, v212 offset:28672\n\t" \
  "s_mov_b64 exec, -1\n\t" \
  "buffer_load_dwordx4 v[192:195], v248, %[rt], %[svt] offen offset:0 sc1\n\t" \
  "buffer_load_dwordx4 v[196:199], v248, %[rt], %[svt] offen offset:1024 sc1\n\t" \
  "s_waitcnt lgkmcnt(1)\n\t" \
  "v_mfma_f32_16x16x32_bf16 v[200:203], v[200:203], v[232:235], 0\n\t" \
  "v_mfma_f32_16x16x32_bf16 v[204:207], v[204:207], v[232:235], 0\n\t" \
  "s_nop 7\n\t" \
  "v_cndmask_b32 v216, v200, v204, %[nh]\n\t" \
  "v_cndmask_b32 v217, v201, v205, %[nh]\n\t" \
  "v_cndmask_b32 v218, v202, v206, %[nh]\n\t" \
  "v_cndmask_b32 v219, v203, v207, %[nh]\n\t" \
  "ds_write_b128 v248, v[216:219] offset:16384\n\t" \
  "buffer_load_dword v252, v249, %[rs], %[sv] offen offset:0 sc1\n\t" \
  "buffer_load_dword v253, v249, %[rs], %[sv] offen offset:256 sc1\n\t" \
  "ds_read_b32 v212, v248 offset:28672\n\t" \
  "ds_read_b64_tr_b16 v[236:237], v250 offset:512\n\t" \
  "ds_read_b64_tr_b16 v[238:239], v250 offset:576\n\t" \
  "ds_read_b64_tr_b16 v[240:241], v250 offset:1024\n\t" \
  "ds_read_b64_tr_b16 v[242:243], v250 offset:1088\n\t" \
  "ds_read_b64_tr_b16 v[244:245], v250 offset:1536\n\t" \
  "ds_read_b64_tr_b16 v[246:247], v250 offset:1600\n\t" \
  "s_setprio 3\n\t" \
  "s_waitcnt lgkmcnt(0)\n\t" \
  "v_cmp_ne_u32 vcc, %[step], v212\n\t" \
  "s_barrier\n\t" \
  "s_and_b32 s93, vcc_lo, 0xf\n\t" \
  "s_cbranch_scc1 .Lre%=\n\t" \
  ".Lrb%=:\n\t" \
  "ds_read_b64_tr_b16 v[200:201], v250 offset:2048\n\t" \
  "ds_read_b64_tr_b16 v[202:203], v250 offset:2112\n\t" \
  "ds_read_b64_tr_b16 v[204:205], v250 offset:2560\n\t" \
  "ds_read_b64_tr_b16 v[206:207], v250 offset:2624\n\t" \
  "ds_read_b128 v[220:223], v248 offset:17408\n\t" \
  "ds_read_b128 v[224:227], v248 offset:18432\n\t" \
  "ds_read_b128 v[228:231], v248 offset:19456\n\t" \
  "ds_read_b64_tr_b16 v[208:209], v250 offset:3072\n\t" \
  "ds_read_b64_tr_b16 v[210:211], v250 offset:3136\n\t" \
  "ds_read_b64_tr_b16 v[212:213], v250 offset:3584\n\t" \
  "ds_read_b64_tr_b16 v[214:215], v250 offset:3648\n\t" \
  "s_waitcnt lgkmcnt(9)\n\t" \
  "v_mfma_f32_16x16x32_bf16 v[200:203], v[200:203], v[232:235], 0\n\t" \
  "s_waitcnt lgkmcnt(7)\n\t" \
  "v_mfma_f32_16x16x32_bf16 v[200:203], v[204:207], v[236:239], v[200:203]\n\t" \
  "s_waitcnt lgkmcnt(6)\n\t" \
  "v_pk_add_f32 v[216:217], v[216:217], v[220:221]\n\t" \
  "v_pk_add_f32 v[218:219], v[218:219], v[222:223]\n\t" \
  "s_waitcnt lgkmcnt(4)\n\t" \
  "v_pk_add_f32 v[224:225], v[224:225], v[228:229]\n\t" \
  "v_pk_add_f32 v[226:227], v[226:227], v[230:231]\n\t" \
  "v_pk_add_f32 v[216:217], v[216:217], v[224:225]\n\t" \
  "v_pk_add_f32 v[218:219], v[218:219], v[226:227]\n\t" \
  "s_waitcnt lgkmcnt(2)\n\t" \
  "v_mfma_f32_16x16x32_bf16 v[200:203], v[208:211], v[240:243], v[200:203]\n\t" \
  "v_max3_f32 v224, v216, v217, v218\n\t" \
  "v_max_f32 v224, v224, v219\n\t" \
  "s_waitcnt lgkmcnt(0)\n\t" \
  "v_mfma_f32_16x16x32_bf16 v[200:203], v[212:215], v[244:247], v[200:203]\n\t" \
  "v_max_f32_dpp v225, v224, v224 row_ror:8 row_mask:0xf bank_mask:0xf\n\t" \
  "v_max_f32_dpp v226, v224, v224 row_ror:8 row_mask:0xf bank_mask:0xf\n\t" \
  "s_nop 1\n\t" \
  "v_permlane32_swap_b32 v225, v226\n\t" \
  "v_max_f32 v231, v225, v226\n\t" \
  "v_mov_b32 v220, 0x3f803f80\n\t" \
  "v_mov_b32 v221, 0x3f803f80\n\t" \
  "v_mov_b64 v[222:223], v[220:221]\n\t" \
  "v_mov_b32 v233, 0\n\t" \
  "v_mov_b32 v234, 0\n\t" \
  "v_max3_f32 v204, v200, v201, v231\n\t" \
  "v_max3_f32 v205, v204, v202, v203\n\t" \
  "v_max3_f32 v206, v204, v202, v203\n\t" \
  "v_cndmask_b32 v200, v200, v202, %[up]\n\t" \
  "v_cndmask_b32 v201, v201, v203, %[up]\n\t" \
  "v_permlane16_swap_b32 v205, v206\n\t" \
  "v_max_f32 v204, v205, v206\n\t" \
  "v_mul_f32 v204, %[negc], v204\n\t" \
  "v_fma_f32 v200, v200, %[c], v204\n\t" \
  "v_fma_f32 v201, v201, %[c], v204\n\t" \
  "v_exp_f32 v200, v200\n\t" \
  "v_fma_f32 v216, v216, %[c], v204\n\t" \
  "v_exp_f32 v201, v201\n\t" \
  "v_fma_f32 v217, v217, %[c], v204\n\t" \
  "v_exp_f32 v216, v216\n\t" \
  "v_fma_f32 v218, v218, %[c], v204\n\t" \
  "v_cvt_pk_bf16_f32 v232, v200, v201\n\t" \
  "v_cvt_pk_bf16_f32 v235, v200, v201\n\t" \
  "v_exp_f32 v217, v217\n\t" \
  "v_fma_f32 v219, v219, %[c], v204\n\t" \
  "v_mfma_f32_16x16x16_bf16 v[212:215], v[220:221], v[232:233], 0\n\t" \
  "v_exp_f32 v218, v218\n\t" \
  "s_waitcnt vmcnt(0)\n\t" \
  "v_mfma_f32_16x16x16_bf16 v[224:227], v[252:253], v[232:233], 0\n\t" \
  "v_cvt_pk_bf16_f32 v210, v216, v217\n\t" \
  "v_cvt_pk_bf16_f32 v208, v216, v217\n\t" \
  "v_exp_f32 v219, v219\n\t" \
  "v_mfma_f32_16x16x16_bf16 v[228:231], v[252:253], v[234:235], 0\n\t" \
  "v_cvt_pk_bf16_f32 v211, v218, v219\n\t" \
  "v_cvt_pk_bf16_f32 v209, v218, v219\n\t" \
  "v_mov_b32_dpp v208, v210 row_ror:8 row_mask:0xf bank_mask:0xc\n\t" \
  "v_mov_b32_dpp v210, v210 row_ror:8 row_mask:0xf bank_mask:0x3\n\t" \
  "v_mov_b32_dpp v209, v211 row_ror:8 row_mask:0xf bank_mask:0xc\n\t" \
  "v_mov_b32_dpp v211, v211 row_ror:8 row_mask:0xf bank_mask:0x3\n\t" \
  "v_max_u32 v204, v252, v253\n\t" \
  "v_cmp_le_u32 vcc, %[sent], v204\n\t" \
  "v_mfma_f32_16x16x32_bf16 v[212:215], v[220:223], v[208:211], v[212:215]\n\t" \
  ".Lpv%=:\n\t" \
  "v_mfma_f32_16x16x32_bf16 v[224:227], v[192:195], v[208:211], v[224:227]\n\t" \
  "v_mfma_f32_16x16x32_bf16 v[228:231], v[196:199], v[208:211], v[228:231]\n\t" \
  "s_cbranch_vccnz .Lvs%=\n\t" \
  ".Lvok%=:\n\t" \
  "s_nop 4\n\t" \
  "v_rcp_f32 v204, v212\n\t" \
  "v_cndmask_b32 v224, v224, v226, %[nh]\n\t" \
  "v_cndmask_b32 v225, v225, v227, %[nh]\n\t" \
  "v_cndmask_b32 v228, v228, v230, %[nh]\n\t" \
  "v_cndmask_b32 v229, v229, v231, %[nh]\n\t" \
  "v_pk_mul_f32 v[224:225], v[224:225], v[204:205] op_sel_hi:[1,0]\n\t" \
  "v_pk_mul_f32 v[228:229], v[228:229], v[204:205] op_sel_hi:[1,0]\n\t" \
  "v_cvt_pk_bf16_f32 v224, v224, v225\n\t" \
  "v_cvt_pk_bf16_f32 v225, v228, v229\n\t" \
  "buffer_store_dwordx2 v[224:225], v251, %[rs], %[so] offen sc1\n\t" \
  "s_memtime s[90:91]\n\t" \
  "s_setprio 0\n\t" \
  "s_waitcnt lgkmcnt(0)\n\t" \
  "s_mov_b32 %[tout], s90\n\t" \
  "s_mov_b32 %[spins], s92\n\t" \
  "s_branch .Lend%=\n\t" \
  ".Lre%=:\n\t" \
  "ds_read_b64_tr_b16 v[236:237], v250 offset:512\n\t" \
  "ds_read_b64_tr_b16 v[238:239], v250 offset:576\n\t" \
  "ds_read_b64_tr_b16 v[240:241], v250 offset:1024\n\t" \
  "ds_read_b64_tr_b16 v[242:243], v250 offset:1088\n\t" \
  "ds_read_b64_tr_b16 v[244:245], v250 offset:1536\n\t" \
  "ds_read_b64_tr_b16 v[246:247], v250 offset:1600\n\t" \
  "s_waitcnt lgkmcnt(0)\n\t" \
  "s_branch .Lrb%=\n\t" \
  ".Lvs%=:\n\t" \
  "v_cmp_eq_u32 vcc, %[sent], v252\n\t" \
  "v_cmp_eq_u32 s[94:95], %[sent], v253\n\t" \
  "s_or_b64 s[94:95], s[94:95], vcc\n\t" \
  "s_cbranch_scc0 .Lvok%=\n\t" \
  "s_add_u32 s92, s92, 1\n\t" \
  "s_sleep 2\n\t" \
  "s_and_b32 s93, s92, 63\n\t" \
  "s_cbranch_scc1 .Lvr%=\n\t" \
  "buffer_load_dword v204, off, %[rs], %[sab] sc1\n\t" \
  "s_waitcnt vmcnt(0)\n\t" \
  "v_readfirstlane_b32 s93, v204\n\t" \
  "s_nop 3\n\t" \
  "s_cmp_eq_u32 s93, 1\n\t" \
  "s_cbranch_scc1 .Lvok%=\n\t" \
  "s_cmp_lt_u32 s92, 0x80000\n\t" \
  "s_cbranch_scc1 .Lvr%=\n\t" \
  "v_mov_b32 v204, 1\n\t" \
  "buffer_store_dword v204, off, %[rs], %[sab] sc1\n\t" \
  "s_branch .Lvok%=\n\t" \
  ".Lvr%=:\n\t" \
  "buffer_load_dword v252, v249, %[rs], %[sv] offen offset:0 sc1\n\t" \
  "buffer_load_dword v253, v249, %[rs], %[sv] offen offset:256 sc1\n\t" \
  "s_waitcnt vmcnt(0)\n\t" \
  "v_mfma_f32_16x16x16_bf16 v[224:227], v[252:253], v[232:233], 0\n\t" \
  "v_mfma_f32_16x16x16_bf16 v[228:231], v[252:253], v[234:235], 0\n\t" \
  "v_max_u32 v204, v252, v253\n\t" \
  "v_cmp_le_u32 vcc, %[sent], v204\n\t" \
  "s_nop 4\n\t" \
  "s_branch .Lpv%=\n\t" \
  "s_branch .Lend%=\n\t" \
  ".Lslow%=:\n\t" \
  "s_mov_b64 s[94:95], 0\n\t" \
  "v_cmp_eq_u32 vcc, %[sent], v200\n\t" \
  "s_or_b64 s[94:95], s[94:95], vcc\n\t" \
  "v_cmp_eq_u32 vcc, %[sent], v201\n\t" \
  "s_or_b64 s[94:95], s[94:95], vcc\n\t" \
  "v_cmp_eq_u32 vcc, %[sent], v202\n\t" \
  "s_or_b64 s[94:95], s[94:95], vcc\n\t" \
  "v_cmp_eq_u32 vcc, %[sent], v203\n\t" \
  "s_or_b64 s[94:95], s[94:95], vcc\n\t" \
  "v_cmp_eq_u32 vcc, %[sent], v204\n\t" \
  "s_or_b64 s[94:95], s[94:95], vcc\n\t" \
  "v_cmp_eq_u32 vcc, %[sent], v205\n\t" \
  "s_or_b64 s[94:95], s[94:95], vcc\n\t" \
  "v_cmp_eq_u32 vcc, %[sent], v206\n\t" \
  "s_or_b64 s[94:95], s[94:95], vcc\n\t" \
  "v_cmp_eq_u32 vcc, %[sent], v207\n\t" \
  "s_or_b64 s[94:95], s[94:95], vcc\n\t" \
  "s_cmp_eq_u64 s[94:95], 0\n\t" \
  "s_cbranch_scc1 .Lok%=\n\t" \
  "v_mov_b64 v[192:193], 0\n\t" \
  "v_mov_b64 v[194:195], 0\n\t" \
  "v_mov_b64 v[196:197], 0\n\t" \
  "v_mov_b64 v[198:199], 0\n\t" \
  "s_add_u32 s92, s92, 1\n\t" \
  "s_sleep 6\n\t" \
  "s_cmp_eq_u32 s92, 1\n\t" \
  "s_cbranch_scc1 .Lpoll%=\n\t" \
  "s_sleep 1\n\t" \
  "s_and_b32 s93, s92, 63\n\t" \
  "s_cmp_lg_u32 s93, 0\n\t" \
  "s_cbranch_scc1 .Lpoll%=\n\t" \
  "v_mov_b32 v245, 0\n\t" \
  "buffer_load_dword v244, v245, %[rs], %[sab] offen sc1\n\t" \
  "s_waitcnt vmcnt(0)\n\t" \
  "v_readfirstlane_b32 s93, v244\n\t" \
  "s_nop 3\n\t" \
  "s_cmp_eq_u32 s93, 1\n\t" \
  "s_cbranch_scc1 .Lok%=\n\t" \
  "s_cmp_lt_u32 s92, 0x80000\n\t" \
  "s_cbranch_scc1 .Lpoll%=\n\t" \
  "v_mov_b32 v244, 1\n\t" \
  "buffer_store_dword v244, v245, %[rs], %[sab] offen sc1\n\t" \
  "s_branch .Lok%=\n\t" \
  ".Lend%=:\n\t"
#define QK_ASM_1 \
  "s_waitcnt lgkmcnt(0)\n\t" \
  "v_subrev_u32 v244, 0x8000, v250\n\t" \
  "ds_read_b128 v[216:219], v248 offset:36864\n\t" \
  "ds_read_b128 v[220:223], v248 offset:37888\n\t" \
  "ds_read_b128 v[224:227], v248 offset:38912\n\t" \
  "ds_read_b128 v[228:231], v248 offset:39936\n\t" \
  "ds_read_b128 v[208:211], v244 offset:0\n\t" \
  "ds_read_b128 v[212:215], v244 offset:1024\n\t" \
  "ds_read_b128 v[236:239], v244 offset:2048\n\t" \
  "ds_read_b128 v[240:243], v244 offset:3072\n\t" \
  "v_mov_b64 v[192:193], 0\n\t" \
  "v_mov_b64 v[194:195], 0\n\t" \
  "v_mov_b64 v[196:197], 0\n\t" \
  "v_mov_b64 v[198:199], 0\n\t" \
  "s_movk_i32 s92, 0x2000\n\t" \
  ".Lwt%=:\n\t" \
  "ds_read_b64 v[200:201], v248 offset:30720\n\t" \
  "s_memrealtime s[90:91]\n\t" \
  "s_sub_u32 s92, s92, 1\n\t" \
  "s_waitcnt lgkmcnt(0)\n\t" \
  "v_readfirstlane_b32 s93, v200\n\t" \
  "v_readfirstlane_b32 %[tgt], v201\n\t" \
  "s_cbranch_scc1 .Lgo%=\n\t" \
  "s_cmp_eq_u32 s93, %[step]\n\t" \
  "s_cbranch_scc1 .Lwx%=\n\t" \
  "s_sleep 1\n\t" \
  "s_branch .Lwt%=\n\t" \
  ".Lwx%=:\n\t" \
  "s_lshl_b32 s93, s90, 2\n\t" \
  "s_sub_i32 s93, %[tgt], s93\n\t" \
  "s_cmp_le_i32 s93, 0\n\t" \
  "s_cbranch_scc1 .Lgo%=\n\t" \
  "s_min_i32 s93, s93, 8000\n\t" \
  "s_mov_b32 s94, s93\n\t" \
  ".Lsl%=:\n\t" \
  "s_sub_i32 s94, s94, 18\n\t" \
  "s_cmp_lt_i32 s94, 30\n\t" \
  "s_cbranch_scc1 .Lsx%=\n\t" \
  "s_sleep 1\n\t" \
  "s_branch .Lsl%=\n\t" \
  ".Lsx%=:\n\t" \
  "s_sub_i32 s93, s93, 12\n\t" \
  "s_max_i32 s93, s93, 4\n\t" \
  "s_lshr_b32 s93, s93, 2\n\t" \
  "s_add_u32 s93, s93, s90\n\t" \
  "s_memrealtime s[90:91]\n\t" \
  ".Lpace%=:\n\t" \
  "s_memrealtime s[94:95]\n\t" \
  "s_waitcnt lgkmcnt(1)\n\t" \
  "s_sub_i32 s92, s90, s93\n\t" \
  "s_cmp_ge_i32 s92, 0\n\t" \
  "s_cbranch_scc1 .Le%=\n\t" \
  "s_memrealtime s[90:91]\n\t" \
  "s_waitcnt lgkmcnt(1)\n\t" \
  "s_sub_i32 s92, s94, s93\n\t" \
  "s_cmp_lt_i32 s92, 0\n\t" \
  "s_cbranch_scc1 .Lpace%=\n\t" \
  "s_nop 4\n\t" \
  ".Le%=:\n\t" \
  "s_add_u32 s92, s92, s93\n\t" \
  "s_lshl_b32 s92, s92, 2\n\t" \
  "s_sub_i32 s92, %[tgt], s92\n\t" \
  "s_max_i32 s92, s92, 0\n\t" \
  "s_bitcmp0_b32 s92, 3\n\t" \
  "s_cbranch_scc1 .Lf3%=\n\t" \
  "s_nop 15\n\t" \
  ".Lf3%=:\n\t" \
  "s_bitcmp0_b32 s92, 2\n\t" \
  "s_cbranch_scc1 .Lf2%=\n\t" \
  "s_nop 10\n\t" \
  ".Lf2%=:\n\t" \
  ".Lgo%=:\n\t" \
  "s_mov_b32 s92, 0\n\t" \
  ".Lpoll%=:\n\t" \
  "buffer_load_dwordx4 v[200:203], v248, %[rs], %[som] offen sc1\n\t" \
  "buffer_load_dwordx4 v[204:207], v248, %[rs], %[som2] offen sc1\n\t" \
  "s_waitcnt vmcnt(1) lgkmcnt(0)\n\t" \
  "ds_write_b128 v249, v[200:203]\n\t" \
  "v_smfmac_f32_16x16x64_bf16 v[192:195], v[200:203], v[208:215], v251\n\t" \
  "v_max3_u32 v244, v200, v201, v202\n\t" \
  "v_smfmac_f32_16x16x64_bf16 v[196:199], v[200:203], v[236:243], v251\n\t" \
  "s_waitcnt vmcnt(0)\n\t" \
  "ds_write_b128 v249, v[204:207] offset:8192\n\t" \
  "v_smfmac_f32_16x16x64_bf16 v[192:195], v[204:207], v[216:223], v251\n\t" \
  "v_max3_u32 v244, v244, v203, v204\n\t" \
  "v_max3_u32 v244, v244, v205, v206\n\t" \
  "v_smfmac_f32_16x16x64_bf16 v[196:199], v[204:207], v[224:231], v251\n\t" \
  "v_max_u32 v244, v244, v207\n\t" \
  "v_cmp_le_u32 vcc, %[sent], v244\n\t" \
  "s_cbranch_vccnz .Lslow%=\n\t" \
  ".Lok%=:\n\t" \
  "ds_read_b128 v[216:219], v250 offset:32768\n\t" \
  "ds_read_b128 v[220:223], v250 offset:33792\n\t" \
  "ds_read_b128 v[224:227], v250 offset:34816\n\t" \
  "ds_read_b128 v[228:231], v250 offset:35840\n\t" \
  "s_waitcnt lgkmcnt(4)\n\t" \
  "s_barrier\n\t" \
  "ds_read_b128 v[208:211], v248 offset:0\n\t" \
  "ds_read_b128 v[212:215], v248 offset:3072\n\t" \
  "ds_read_b128 v[236:239], v248 offset:4096\n\t" \
  "ds_read_b128 v[240:243], v248 offset:5120\n\t" \
  "s_waitcnt lgkmcnt(3)\n\t" \
  "ds_read_b128 v[244:247], v248 offset:12288\n\t" \
  "v_smfmac_f32_16x16x64_bf16 v[192:195], v[208:211], v[216:223], v251\n\t" \
  "ds_read_b128 v[216:219], v250 offset:0\n\t" \
  "v_smfmac_f32_16x16x64_bf16 v[196:199], v[208:211], v[224:231], v251\n\t" \
  "s_waitcnt lgkmcnt(4)\n\t" \
  "ds_read_b128 v[220:223], v250 offset:1024\n\t" \
  "v_smfmac_f32_16x16x64_bf16 v[192:195], v[212:215], v[0:7], v251\n\t" \
  "ds_read_b128 v[224:227], v250 offset:2048\n\t" \
  "v_smfmac_f32_16x16x64_bf16 v[196:199], v[212:215], v[8:15], v251\n\t" \
  "s_waitcnt lgkmcnt(5)\n\t" \
  "ds_read_b128 v[228:231], v250 offset:3072\n\t" \
  "v_smfmac_f32_16x16x64_bf16 v[192:195], v[236:239], v[16:23], v251\n\t" \
  "ds_read_b128 v[200:203], v248 offset:6144\n\t" \
  "v_smfmac_f32_16x16x64_bf16 v[196:199], v[236:239], v[24:31], v251\n\t" \
  "s_waitcnt lgkmcnt(6)\n\t" \
  "ds_read_b128 v[204:207], v248 offset:7168\n\t" \
  "v_smfmac_f32_16x16x64_bf16 v[192:195], v[240:243], v[32:39], v251\n\t" \
  "ds_read_b128 v[208:211], v248 offset:8192\n\t" \
  "v_smfmac_f32_16x16x64_bf16 v[196:199], v[240:243], v[40:47], v251\n\t" \
  "s_waitcnt lgkmcnt(5)\n\t" \
  "ds_read_b128 v[212:215], v248 offset:10240\n\t" \
  "v_smfmac_f32_16x16x64_bf16 v[192:195], v[244:247], v[216:223], v251\n\t" \
  "s_waitcnt lgkmcnt(4)\n\t" \
  "v_smfmac_f32_16x16x64_bf16 v[196:199], v[244:247], v[224:231], v251\n\t" \
  "s_waitcnt lgkmcnt(3)\n\t" \
  "ds_read_b128 v[236:239], v248 offset:11264\n\t" \
  "v_smfmac_f32_16x16x64_bf16 v[192:195], v[200:203], v[48:55], v251\n\t" \
  "ds_read_b128 v[240:243], v248 offset:13312\n\t" \
  "v_smfmac_f32_16x16x64_bf16 v[196:199], v[200:203], v[56:63], v251\n\t" \
  "s_waitcnt lgkmcnt(4)\n\t" \
  "v_smfmac_f32_16x16x64_bf16 v[192:195], v[204:207], v[64:71], v251\n\t" \
  "ds_read_b128 v[244:247], v248 offset:14336\n\t" \
  "v_smfmac_f32_16x16x64_bf16 v[196:199], v[204:207], v[72:79], v251\n\t" \
  "s_waitcnt lgkmcnt(4)\n\t" \
  "v_smfmac_f32_16x16x64_bf16 v[192:195], v[208:211], v[80:87], v251\n\t" \
  "ds_read_b128 v[200:203], v248 offset:15360\n\t" \
  "v_smfmac_f32_16x16x64_bf16 v[196:199], v[208:211], v[88:95], v251\n\t" \
  "s_waitcnt lgkmcnt(4)\n\t" \
  "v_smfmac_f32_16x16x64_bf16 v[192:195], v[212:215], v[96:103], v251\n\t" \
  "ds_read_b128 v[204:207], v248 offset:2048\n\t" \
  "v_smfmac_f32_16x16x64_bf16 v[196:199], v[212:215], v[104:111], v251\n\t" \
  "s_waitcnt lgkmcnt(3)\n\t" \
  "v_smfmac_f32_16x16x64_bf16 v[192:195], v[236:239], v[112:119], v251\n\t" \
  "v_smfmac_f32_16x16x64_bf16 v[196:199], v[236:239], v[120:127], v251\n\t" \
  "v_smfmac_f32_16x16x64_bf16 v[192:195], v[240:243], v[128:135], v251\n\t" \
  "v_smfmac_f32_16x16x64_bf16 v[196:199], v[240:243], v[136:143], v251\n\t" \
  "s_waitcnt lgkmcnt(1)\n\t" \
  "v_smfmac_f32_16x16x64_bf16 v[192:195], v[244:247], v[144:151], v251\n\t" \
  "v_smfmac_f32_16x16x64_bf16 v[196:199], v[244:247], v[152:159], v251\n\t" \
  "v_smfmac_f32_16x16x64_bf16 v[192:195], v[200:203], v[160:167], v251\n\t" \
  "v_smfmac_f32_16x16x64_bf16 v[196:199], v[200:203], v[168:175], v251\n\t" \
  "s_waitcnt lgkmcnt(0)\n\t" \
  "v_smfmac_f32_16x16x64_bf16 v[192:195], v[204:207], v[176:183], v251\n\t" \
  "v_smfmac_f32_16x16x64_bf16 v[196:199], v[204:207], v[184:191], v251\n\t" \
  "ds_read_b128 v[200:203], v248 offset:17408\n\t" \
  "ds_read_b128 v[204:207], v248 offset:21504\n\t" \
  "v_mov_b32 v250, v232\n\t" \
  "v_mov_b32 v249, v233\n\t" \
  "s_nop 2\n\t" \
  "v_add_f32 v192, v192, v194\n\t" \
  "v_add_f32 v193, v193, v195\n\t" \
  "v_add_f32 v196, v196, v198\n\t" \
  "v_add_f32 v197, v197, v199\n\t" \
  "v_cvt_pk_bf16_f32 v192, v192, v193\n\t" \
  "v_cvt_pk_bf16_f32 v196, v196, v197\n\t" \
  "ds_write2_b32 v252, v192, v196 offset1:64\n\t" \
  "v_mov_b32 v251, v234\n\t" \
  "ds_read_b64_tr_b16 v[236:237], v250 offset:512\n\t" \
  "ds_read_b64_tr_b16 v[238:239], v250 offset:576\n\t" \
  "v_mov_b32 v212, %[step]\n\t" \
  "s_mov_b64 exec, 1\n\t" \
  "ds_write_b32 v248, v212 offset:28688\n\t" \
  "s_mov_b64 exec, -1\n\t" \
  "buffer_load_dwordx4 v[192:195], v248, %[rt], %[svt] offen offset:0 sc1\n\t" \
  "buffer_load_dwordx4 v[196:199], v248, %[rt], %[svt] offen offset:1024 sc1\n\t" \
  "s_waitcnt lgkmcnt(1)\n\t" \
  "v_mfma_f32_16x16x32_bf16 v[200:203], v[200:203], v[236:239], 0\n\t" \
  "v_mfma_f32_16x16x32_bf16 v[204:207], v[204:207], v[236:239], 0\n\t" \
  "s_nop 7\n\t" \
  "v_cndmask_b32 v216, v200, v204, %[nh]\n\t" \
  "v_cndmask_b32 v217, v201, v205, %[nh]\n\t" \
  "v_cndmask_b32 v218, v202, v206, %[nh]\n\t" \
  "v_cndmask_b32 v219, v203, v207, %[nh]\n\t" \
  "ds_write_b128 v248, v[216:219] offset:17408\n\t" \
  "buffer_load_dword v252, v249, %[rs], %[sv] offen offset:0 sc1\n\t" \
  "buffer_load_dword v253, v249, %[rs], %[sv] offen offset:256 sc1\n\t" \
  "ds_read_b32 v212, v248 offset:28672\n\t" \
  "ds_read_b64_tr_b16 v[240:241], v250 offset:1024\n\t" \
  "ds_read_b64_tr_b16 v[242:243], v250 offset:1088\n\t" \
  "ds_read_b64_tr_b16 v[244:245], v250 offset:1536\n\t" \
  "ds_read_b64_tr_b16 v[246:247], v250 offset:1600\n\t" \
  "ds_read_b64_tr_b16 v[232:233], v250 offset:0\n\t" \
  "ds_read_b64_tr_b16 v[234:235], v250 offset:64\n\t" \
  "s_setprio 3\n\t" \
  "s_waitcnt lgkmcnt(0)\n\t" \
  "v_cmp_ne_u32 vcc, %[step], v212\n\t" \
  "s_barrier\n\t" \
  "s_and_b32 s93, vcc_lo, 0xf\n\t" \
  "s_cbranch_scc1 .Lre%=\n\t" \
  ".Lrb%=:\n\t" \
  "ds_read_b64_tr_b16 v[204:205], v250 offset:2560\n\t" \
  "ds_read_b64_tr_b16 v[206:207], v250 offset:2624\n\t" \
  "ds_read_b64_tr_b16 v[208:209], v250 offset:3072\n\t" \
  "ds_read_b64_tr_b16 v[210:211], v250 offset:3136\n\t" \
  "ds_read_b128 v[220:223], v248 offset:18432\n\t" \
  "ds_read_b128 v[224:227], v248 offset:19456\n\t" \
  "ds_read_b128 v[228:231], v248 offset:16384\n\t" \
  "ds_read_b64_tr_b16 v[212:213], v250 offset:3584\n\t" \
  "ds_read_b64_tr_b16 v[214:215], v250 offset:3648\n\t" \
  "ds_read_b64_tr_b16 v[200:201], v250 offset:2048\n\t" \
  "ds_read_b64_tr_b16 v[202:203], v250 offset:2112\n\t" \
  "s_waitcnt lgkmcnt(9)\n\t" \
  "v_mfma_f32_16x16x32_bf16 v[204:207], v[204:207], v[236:239], 0\n\t" \
  "s_waitcnt lgkmcnt(7)\n\t" \
  "v_mfma_f32_16x16x32_bf16 v[204:207], v[208:211], v[240:243], v[204:207]\n\t" \
  "s_waitcnt lgkmcnt(6)\n\t" \
  "v_pk_add_f32 v[216:217], v[216:217], v[220:221]\n\t" \
  "v_pk_add_f32 v[218:219], v[218:219], v[222:223]\n\t" \
  "s_waitcnt lgkmcnt(4)\n\t" \
  "v_pk_add_f32 v[224:225], v[224:225], v[228:229]\n\t" \
  "v_pk_add_f32 v[226:227], v[226:227], v[230:231]\n\t" \
  "v_pk_add_f32 v[216:217], v[216:217], v[224:225]\n\t" \
  "v_pk_add_f32 v[218:219], v[218:219], v[226:227]\n\t" \
  "s_waitcnt lgkmcnt(2)\n\t" \
  "v_mfma_f32_16x16x32_bf16 v[204:207], v[212:215], v[244:247], v[204:207]\n\t" \
  "v_max3_f32 v224, v216, v217, v218\n\t" \
  "v_max_f32 v224, v224, v219\n\t" \
  "s_waitcnt lgkmcnt(0)\n\t" \
  "v_mfma_f32_16x16x32_bf16 v[204:207], v[200:203], v[232:235], v[204:207]\n\t" \
  "v_max_f32_dpp v225, v224, v224 row_ror:8 row_mask:0xf bank_mask:0xf\n\t" \
  "v_max_f32_dpp v226, v224, v224 row_ror:8 row_mask:0xf bank_mask:0xf\n\t" \
  "s_nop 1\n\t" \
  "v_permlane32_swap_b32 v225, v226\n\t" \
  "v_max_f32 v231, v225, v226\n\t" \
  "v_mov_b32 v220, 0x3f803f80\n\t" \
  "v_mov_b32 v221, 0x3f803f80\n\t" \
  "v_mov_b64 v[222:223], v[220:221]\n\t" \
  "v_mov_b32 v237, 0\n\t" \
  "v_mov_b32 v238, 0\n\t" \
  "v_max3_f32 v208, v204, v205, v231\n\t" \
  "v_max3_f32 v209, v208, v206, v207\n\t" \
  "v_max3_f32 v210, v208, v206, v207\n\t" \
  "v_cndmask_b32 v204, v204, v206, %[up]\n\t" \
  "v_cndmask_b32 v205, v205, v207, %[up]\n\t" \
  "v_permlane16_swap_b32 v209, v210\n\t" \
  "v_max_f32 v208, v209, v210\n\t" \
  "v_mul_f32 v208, %[negc], v208\n\t" \
  "v_fma_f32 v204, v204, %[c], v208\n\t" \
  "v_fma_f32 v205, v205, %[c], v208\n\t" \
  "v_exp_f32 v204, v204\n\t" \
  "v_fma_f32 v216, v216, %[c], v208\n\t" \
  "v_exp_f32 v205, v205\n\t" \
  "v_fma_f32 v217, v217, %[c], v208\n\t" \
  "v_exp_f32 v216, v216\n\t" \
  "v_fma_f32 v218, v218, %[c], v208\n\t" \
  "v_cvt_pk_bf16_f32 v236, v204, v205\n\t" \
  "v_cvt_pk_bf16_f32 v239, v204, v205\n\t" \
  "v_exp_f32 v217, v217\n\t" \
  "v_fma_f32 v219, v219, %[c], v208\n\t" \
  "v_mfma_f32_16x16x16_bf16 v[200:203], v[220:221], v[236:237], 0\n\t" \
  "v_exp_f32 v218, v218\n\t" \
  "s_waitcnt vmcnt(0)\n\t" \
  "v_mfma_f32_16x16x16_bf16 v[224:227], v[252:253], v[236:237], 0\n\t" \
  "v_cvt_pk_bf16_f32 v214, v216, v217\n\t" \
  "v_cvt_pk_bf16_f32 v212, v216, v217\n\t" \
  "v_exp_f32 v219, v219\n\t" \
  "v_mfma_f32_16x16x16_bf16 v[228:231], v[252:253], v[238:239], 0\n\t" \
  "v_cvt_pk_bf16_f32 v215, v218, v219\n\t" \
  "v_cvt_pk_bf16_f32 v213, v218, v219\n\t" \
  "v_mov_b32_dpp v212, v214 row_ror:8 row_mask:0xf bank_mask:0xc\n\t" \
  "v_mov_b32_dpp v214, v214 row_ror:8 row_mask:0xf bank_mask:0x3\n\t" \
  "v_mov_b32_dpp v213, v215 row_ror:8 row_mask:0xf bank_mask:0xc\n\t" \
  "v_mov_b32_dpp v215, v215 row_ror:8 row_mask:0xf bank_mask:0x3\n\t" \
  "v_max_u32 v208, v252, v253\n\t" \
  "v_cmp_le_u32 vcc, %[sent], v208\n\t" \
  "v_mfma_f32_16x16x32_bf16 v[200:203], v[220:223], v[212:215], v[200:203]\n\t" \
  ".Lpv%=:\n\t" \
  "v_mfma_f32_16x16x32_bf16 v[224:227], v[192:195], v[212:215], v[224:227]\n\t" \
  "v_mfma_f32_16x16x32_bf16 v[228:231], v[196:199], v[212:215], v[228:231]\n\t" \
  "s_cbranch_vccnz .Lvs%=\n\t" \
  ".Lvok%=:\n\t" \
  "s_nop 4\n\t" \
  "v_rcp_f32 v208, v200\n\t" \
  "v_cndmask_b32 v224, v224, v226, %[nh]\n\t" \
  "v_cndmask_b32 v225, v225, v227, %[nh]\n\t" \
  "v_cndmask_b32 v228, v228, v230, %[nh]\n\t" \
  "v_cndmask_b32 v229, v229, v231, %[nh]\n\t" \
  "v_pk_mul_f32 v[224:225], v[224:225], v[208:209] op_sel_hi:[1,0]\n\t" \
  "v_pk_mul_f32 v[228:229], v[228:229], v[208:209] op_sel_hi:[1,0]\n\t" \
  "v_cvt_pk_bf16_f32 v224, v224, v225\n\t" \
  "v_cvt_pk_bf16_f32 v225, v228, v229\n\t" \
  "buffer_store_dwordx2 v[224:225], v251, %[rs], %[so] offen sc1\n\t" \
  "s_memtime s[90:91]\n\t" \
  "s_setprio 0\n\t" \
  "s_waitcnt lgkmcnt(0)\n\t" \
  "s_mov_b32 %[tout], s90\n\t" \
  "s_mov_b32 %[spins], s92\n\t" \
  "s_branch .Lend%=\n\t" \
  ".Lre%=:\n\t" \
  "ds_read_b64_tr_b16 v[240:241], v250 offset:1024\n\t" \
  "ds_read_b64_tr_b16 v[242:243], v250 offset:1088\n\t" \
  "ds_read_b64_tr_b16 v[244:245], v250 offset:1536\n\t" \
  "ds_read_b64_tr_b16 v[246:247], v250 offset:1600\n\t" \
  "ds_read_b64_tr_b16 v[232:233], v250 offset:0\n\t" \
  "ds_read_b64_tr_b16 v[234:235], v250 offset:64\n\t" \
  "s_waitcnt lgkmcnt(0)\n\t" \
  "s_branch .Lrb%=\n\t" \
  ".Lvs%=:\n\t" \
  "v_cmp_eq_u32 vcc, %[sent], v252\n\t" \
  "v_cmp_eq_u32 s[94:95], %[sent], v253\n\t" \
  "s_or_b64 s[94:95], s[94:95], vcc\n\t" \
  "s_cbranch_scc0 .Lvok%=\n\t" \
  "s_add_u32 s92, s92, 1\n\t" \
  "s_sleep 2\n\t" \
  "s_and_b32 s93, s92, 63\n\t" \
  "s_cbranch_scc1 .Lvr%=\n\t" \
  "buffer_load_dword v208, off, %[rs], %[sab] sc1\n\t" \
  "s_waitcnt vmcnt(0)\n\t" \
  "v_readfirstlane_b32 s93, v208\n\t" \
  "s_nop 3\n\t" \
  "s_cmp_eq_u32 s93, 1\n\t" \
  "s_cbranch_scc1 .Lvok%=\n\t" \
  "s_cmp_lt_u32 s92, 0x80000\n\t" \
  "s_cbranch_scc1 .Lvr%=\n\t" \
  "v_mov_b32 v208, 1\n\t" \
  "buffer_store_dword v208, off, %[rs], %[sab] sc1\n\t" \
  "s_branch .Lvok%=\n\t" \
  ".Lvr%=:\n\t" \
  "buffer_load_dword v252, v249, %[rs], %[sv] offen offset:0 sc1\n\t" \
  "buffer_load_dword v253, v249, %[rs], %[sv] offen offset:256 sc1\n\t" \
  "s_waitcnt vmcnt(0)\n\t" \
  "v_mfma_f32_16x16x16_bf16 v[224:227], v[252:253], v[236:237], 0\n\t" \
  "v_mfma_f32_16x16x16_bf16 v[228:231], v[252:253], v[238:239], 0\n\t" \
  "v_max_u32 v208, v252, v253\n\t" \
  "v_cmp_le_u32 vcc, %[sent], v208\n\t" \
  "s_nop 4\n\t" \
  "s_branch .Lpv%=\n\t" \
  "s_branch .Lend%=\n\t" \
  ".Lslow%=:\n\t" \
  "s_mov_b64 s[94:95], 0\n\t" \
  "v_cmp_eq_u32 vcc, %[sent], v200\n\t" \
  "s_or_b64 s[94:95], s[94:95], vcc\n\t" \
  "v_cmp_eq_u32 vcc, %[sent], v201\n\t" \
  "s_or_b64 s[94:95], s[94:95], vcc\n\t" \
  "v_cmp_eq_u32 vcc, %[sent], v202\n\t" \
  "s_or_b64 s[94:95], s[94:95], vcc\n\t" \
  "v_cmp_eq_u32 vcc, %[sent], v203\n\t" \
  "s_or_b64 s[94:95], s[94:95], vcc\n\t" \
  "v_cmp_eq_u32 vcc, %[sent], v204\n\t" \
  "s_or_b64 s[94:95], s[94:95], vcc\n\t" \
  "v_cmp_eq_u32 vcc, %[sent], v205\n\t" \
  "s_or_b64 s[94:95], s[94:95], vcc\n\t" \
  "v_cmp_eq_u32 vcc, %[sent], v206\n\t" \
  "s_or_b64 s[94:95], s[94:95], vcc\n\t" \
  "v_cmp_eq_u32 vcc, %[sent], v207\n\t" \
  "s_or_b64 s[94:95], s[94:95], vcc\n\t" \
  "s_cmp_eq_u64 s[94:95], 0\n\t" \
  "s_cbranch_scc1 .Lok%=\n\t" \
  "v_mov_b64 v[192:193], 0\n\t" \
  "v_mov_b64 v[194:195], 0\n\t" \
  "v_mov_b64 v[196:197], 0\n\t" \
  "v_mov_b64 v[198:199], 0\n\t" \
  "s_add_u32 s92, s92, 1\n\t" \
  "s_sleep 6\n\t" \
  "s_cmp_eq_u32 s92, 1\n\t" \
  "s_cbranch_scc1 .Lpoll%=\n\t" \
  "s_sleep 1\n\t" \
  "s_and_b32 s93, s92, 63\n\t" \
  "s_cmp_lg_u32 s93, 0\n\t" \
  "s_cbranch_scc1 .Lpoll%=\n\t" \
  "v_mov_b32 v245, 0\n\t" \
  "buffer_load_dword v244, v245, %[rs], %[sab] offen sc1\n\t" \
  "s_waitcnt vmcnt(0)\n\t" \
  "v_readfirstlane_b32 s93, v244\n\t" \
  "s_nop 3\n\t" \
  "s_cmp_eq_u32 s93, 1\n\t" \
  "s_cbranch_scc1 .Lok%=\n\t" \
  "s_cmp_lt_u32 s92, 0x80000\n\t" \
  "s_cbranch_scc1 .Lpoll%=\n\t" \
  "v_mov_b32 v244, 1\n\t" \
  "buffer_store_dword v244, v245, %[rs], %[sab] offen sc1\n\t" \
  "s_branch .Lok%=\n\t" \
  ".Lend%=:\n\t"
#define QK_ASM_2 \
  "s_waitcnt lgkmcnt(0)\n\t" \
  "v_subrev_u32 v244, 0x8000, v250\n\t" \
  "ds_read_b128 v[216:219], v248 offset:40960\n\t" \
  "ds_read_b128 v[220:223], v248 offset:41984\n\t" \
  "ds_read_b128 v[224:227], v248 offset:43008\n\t" \
  "ds_read_b128 v[228:231], v248 offset:44032\n\t" \
  "ds_read_b128 v[208:211], v244 offset:0\n\t" \
  "ds_read_b128 v[212:215], v244 offset:1024\n\t" \
  "ds_read_b128 v[236:239], v244 offset:2048\n\t" \
  "ds_read_b128 v[240:243], v244 offset:3072\n\t" \
  "v_mov_b64 v[192:193], 0\n\t" \
  "v_mov_b64 v[194:195], 0\n\t" \
  "v_mov_b64 v[196:197], 0\n\t" \
  "v_mov_b64 v[198:199], 0\n\t" \
  "s_movk_i32 s92, 0x2000\n\t" \
  ".Lwt%=:\n\t" \
  "ds_read_b64 v[200:201], v248 offset:30720\n\t" \
  "s_memrealtime s[90:91]\n\t" \
  "s_sub_u32 s92, s92, 1\n\t" \
  "s_waitcnt lgkmcnt(0)\n\t" \
  "v_readfirstlane_b32 s93, v200\n\t" \
  "v_readfirstlane_b32 %[tgt], v201\n\t" \
  "s_cbranch_scc1 .Lgo%=\n\t" \
  "s_cmp_eq_u32 s93, %[step]\n\t" \
  "s_cbranch_scc1 .Lwx%=\n\t" \
  "s_sleep 1\n\t" \
  "s_branch .Lwt%=\n\t" \
  ".Lwx%=:\n\t" \
  "s_lshl_b32 s93, s90, 2\n\t" \
  "s_sub_i32 s93, %[tgt], s93\n\t" \
  "s_cmp_le_i32 s93, 0\n\t" \
  "s_cbranch_scc1 .Lgo%=\n\t" \
  "s_min_i32 s93, s93, 8000\n\t" \
  "s_mov_b32 s94, s93\n\t" \
  ".Lsl%=:\n\t" \
  "s_sub_i32 s94, s94, 18\n\t" \
  "s_cmp_lt_i32 s94, 30\n\t" \
  "s_cbranch_scc1 .Lsx%=\n\t" \
  "s_sleep 1\n\t" \
  "s_branch .Lsl%=\n\t" \
  ".Lsx%=:\n\t" \
  "s_sub_i32 s93, s93, 12\n\t" \
  "s_max_i32 s93, s93, 4\n\t" \
  "s_lshr_b32 s93, s93, 2\n\t" \
  "s_add_u32 s93, s93, s90\n\t" \
  "s_memrealtime s[90:91]\n\t" \
  ".Lpace%=:\n\t" \
  "s_memrealtime s[94:95]\n\t" \
  "s_waitcnt lgkmcnt(1)\n\t" \
  "s_sub_i32 s92, s90, s93\n\t" \
  "s_cmp_ge_i32 s92, 0\n\t" \
  "s_cbranch_scc1 .Le%=\n\t" \
  "s_memrealtime s[90:91]\n\t" \
  "s_waitcnt lgkmcnt(1)\n\t" \
  "s_sub_i32 s92, s94, s93\n\t" \
  "s_cmp_lt_i32 s92, 0\n\t" \
  "s_cbranch_scc1 .Lpace%=\n\t" \
  "s_nop 4\n\t" \
  ".Le%=:\n\t" \
  "s_add_u32 s92, s92, s93\n\t" \
  "s_lshl_b32 s92, s92, 2\n\t" \
  "s_sub_i32 s92, %[tgt], s92\n\t" \
  "s_max_i32 s92, s92, 0\n\t" \
  "s_bitcmp0_b32 s92, 3\n\t" \
  "s_cbranch_scc1 .Lf3%=\n\t" \
  "s_nop 15\n\t" \
  ".Lf3%=:\n\t" \
  "s_bitcmp0_b32 s92, 2\n\t" \
  "s_cbranch_scc1 .Lf2%=\n\t" \
  "s_nop 10\n\t" \
  ".Lf2%=:\n\t" \
  ".Lgo%=:\n\t" \
  "s_mov_b32 s92, 0\n\t" \
  ".Lpoll%=:\n\t" \
  "buffer_load_dwordx4 v[200:203], v248, %[rs], %[som] offen sc1\n\t" \
  "buffer_load_dwordx4 v[204:207], v248, %[rs], %[som2] offen sc1\n\t" \
  "s_waitcnt vmcnt(1) lgkmcnt(0)\n\t" \
  "ds_write_b128 v249, v[200:203]\n\t" \
  "v_smfmac_f32_16x16x64_bf16 v[192:195], v[200:203], v[208:215], v251\n\t" \
  "v_max3_u32 v244, v200, v201, v202\n\t" \
  "v_smfmac_f32_16x16x64_bf16 v[196:199], v[200:203], v[236:243], v251\n\t" \
  "s_waitcnt vmcnt(0)\n\t" \
  "ds_write_b128 v249, v[204:207] offset:8192\n\t" \
  "v_smfmac_f32_16x16x64_bf16 v[192:195], v[204:207], v[216:223], v251\n\t" \
  "v_max3_u32 v244, v244, v203, v204\n\t" \
  "v_max3_u32 v244, v244, v205, v206\n\t" \
  "v_smfmac_f32_16x16x64_bf16 v[196:199], v[204:207], v[224:231], v251\n\t" \
  "v_max_u32 v244, v244, v207\n\t" \
  "v_cmp_le_u32 vcc, %[sent], v244\n\t" \
  "s_cbranch_vccnz .Lslow%=\n\t" \
  ".Lok%=:\n\t" \
  "ds_read_b128 v[216:219], v250 offset:32768\n\t" \
  "ds_read_b128 v[220:223], v250 offset:33792\n\t" \
  "ds_read_b128 v[224:227], v250 offset:34816\n\t" \
  "ds_read_b128 v[228:231], v250 offset:35840\n\t" \
  "s_waitcnt lgkmcnt(4)\n\t" \
  "s_barrier\n\t" \
  "ds_read_b128 v[208:211], v248 offset:3072\n\t" \
  "ds_read_b128 v[212:215], v248 offset:4096\n\t" \
  "ds_read_b128 v[236:239], v248 offset:5120\n\t" \
  "ds_read_b128 v[240:243], v248 offset:6144\n\t" \
  "s_waitcnt lgkmcnt(3)\n\t" \
  "ds_read_b128 v[244:247], v248 offset:13312\n\t" \
  "v_smfmac_f32_16x16x64_bf16 v[192:195], v[208:211], v[216:223], v251\n\t" \
  "ds_read_b128 v[216:219], v250 offset:0\n\t" \
  "v_smfmac_f32_16x16x64_bf16 v[196:199], v[208:211], v[224:231], v251\n\t" \
  "s_waitcnt lgkmcnt(4)\n\t" \
  "ds_read_b128 v[220:223], v250 offset:1024\n\t" \
  "v_smfmac_f32_16x16x64_bf16 v[192:195], v[212:215], v[0:7], v251\n\t" \
  "ds_read_b128 v[224:227], v250 offset:2048\n\t" \
  "v_smfmac_f32_16x16x64_bf16 v[196:199], v[212:215], v[8:15], v251\n\t" \
  "s_waitcnt lgkmcnt(5)\n\t" \
  "ds_read_b128 v[228:231], v250 offset:3072\n\t" \
  "v_smfmac_f32_16x16x64_bf16 v[192:195], v[236:239], v[16:23], v251\n\t" \
  "ds_read_b128 v[200:203], v248 offset:7168\n\t" \
  "v_smfmac_f32_16x16x64_bf16 v[196:199], v[236:239], v[24:31], v251\n\t" \
  "s_waitcnt lgkmcnt(6)\n\t" \
  "ds_read_b128 v[204:207], v248 offset:8192\n\t" \
  "v_smfmac_f32_16x16x64_bf16 v[192:195], v[240:243], v[32:39], v251\n\t" \
  "ds_read_b128 v[208:211], v248 offset:9216\n\t" \
  "v_smfmac_f32_16x16x64_bf16 v[196:199], v[240:243], v[40:47], v251\n\t" \
  "s_waitcnt lgkmcnt(5)\n\t" \
  "ds_read_b128 v[212:215], v248 offset:11264\n\t" \
  "v_smfmac_f32_16x16x64_bf16 v[192:195], v[244:247], v[216:223], v251\n\t" \
  "s_waitcnt lgkmcnt(4)\n\t" \
  "v_smfmac_f32_16x16x64_bf16 v[196:199], v[244:247], v[224:231], v251\n\t" \
  "s_waitcnt lgkmcnt(3)\n\t" \
  "ds_read_b128 v[236:239], v248 offset:12288\n\t" \
  "v_smfmac_f32_16x16x64_bf16 v[192:195], v[200:203], v[48:55], v251\n\t" \
  "ds_read_b128 v[240:243], v248 offset:14336\n\t" \
  "v_smfmac_f32_16x16x64_bf16 v[196:199], v[200:203], v[56:63], v251\n\t" \
  "s_waitcnt lgkmcnt(4)\n\t" \
  "v_smfmac_f32_16x16x64_bf16 v[192:195], v[204:207], v[64:71], v251\n\t" \
  "ds_read_b128 v[244:247], v248 offset:15360\n\t" \
  "v_smfmac_f32_16x16x64_bf16 v[196:199], v[204:207], v[72:79], v251\n\t" \
  "s_waitcnt lgkmcnt(4)\n\t" \
  "v_smfmac_f32_16x16x64_bf16 v[192:195], v[208:211], v[80:87], v251\n\t" \
  "ds_read_b128 v[200:203], v248 offset:0\n\t" \
  "v_smfmac_f32_16x16x64_bf16 v[196:199], v[208:211], v[88:95], v251\n\t" \
  "s_waitcnt lgkmcnt(4)\n\t" \
  "v_smfmac_f32_16x16x64_bf16 v[192:195], v[212:215], v[96:103], v251\n\t" \
  "ds_read_b128 v[204:207], v248 offset:1024\n\t" \
  "v_smfmac_f32_16x16x64_bf16 v[196:199], v[212:215], v[104:111], v251\n\t" \
  "s_waitcnt lgkmcnt(3)\n\t" \
  "v_smfmac_f32_16x16x64_bf16 v[192:195], v[236:239], v[112:119], v251\n\t" \
  "v_smfmac_f32_16x16x64_bf16 v[196:199], v[236:239], v[120:127], v251\n\t" \
  "v_smfmac_f32_16x16x64_bf16 v[192:195], v[240:243], v[128:135], v251\n\t" \
  "v_smfmac_f32_16x16x64_bf16 v[196:199], v[240:243], v[136:143], v251\n\t" \
  "s_waitcnt lgkmcnt(1)\n\t" \
  "v_smfmac_f32_16x16x64_bf16 v[192:195], v[244:247], v[144:151], v251\n\t" \
  "v_smfmac_f32_16x16x64_bf16 v[196:199], v[244:247], v[152:159], v251\n\t" \
  "v_smfmac_f32_16x16x64_bf16 v[192:195], v[200:203], v[160:167], v251\n\t" \
  "v_smfmac_f32_16x16x64_bf16 v[196:199], v[200:203], v[168:175], v251\n\t" \
  "s_waitcnt lgkmcnt(0)\n\t" \
  "v_smfmac_f32_16x16x64_bf16 v[192:195], v[204:207], v[176:183], v251\n\t" \
  "v_smfmac_f32_16x16x64_bf16 v[196:199], v[204:207], v[184:191], v251\n\t" \
  "ds_read_b128 v[200:203], v248 offset:18432\n\t" \
  "ds_read_b128 v[204:207], v248 offset:22528\n\t" \
  "v_mov_b32 v250, v232\n\t" \
  "v_mov_b32 v249, v233\n\t" \
  "s_nop 2\n\t" \
  "v_add_f32 v192, v192, v194\n\t" \
  "v_add_f32 v193, v193, v195\n\t" \
  "v_add_f32 v196, v196, v198\n\t" \
  "v_add_f32 v197, v197, v199\n\t" \
  "v_cvt_pk_bf16_f32 v192, v192, v193\n\t" \
  "v_cvt_pk_bf16_f32 v196, v196, v197\n\t" \
  "ds_write2_b32 v252, v192, v196 offset1:64\n\t" \
  "v_mov_b32 v251, v234\n\t" \
  "ds_read_b64_tr_b16 v[240:241], v250 offset:1024\n\t" \
  "ds_read_b64_tr_b16 v[242:243], v250 offset:1088\n\t" \
  "v_mov_b32 v212, %[step]\n\t" \
  "s_mov_b64 exec, 1\n\t" \
  "ds_write_b32 v248, v212 offset:28704\n\t" \
  "s_mov_b64 exec, -1\n\t" \
  "buffer_load_dwordx4 v[192:195], v248, %[rt], %[svt] offen offset:0 sc1\n\t" \
  "buffer_load_dwordx4 v[196:199], v248, %[rt], %[svt] offen offset:1024 sc1\n\t" \
  "s_waitcnt lgkmcnt(1)\n\t" \
  "v_mfma_f32_16x16x32_bf16 v[200:203], v[200:203], v[240:243], 0\n\t" \
  "v_mfma_f32_16x16x32_bf16 v[204:207], v[204:207], v[240:243], 0\n\t" \
  "s_nop 7\n\t" \
  "v_cndmask_b32 v216, v200, v204, %[nh]\n\t" \
  "v_cndmask_b32 v217, v201, v205, %[nh]\n\t" \
  "v_cndmask_b32 v218, v202, v206, %[nh]\n\t" \
  "v_cndmask_b32 v219, v203, v207, %[nh]\n\t" \
  "ds_write_b128 v248, v[216:219] offset:18432\n\t" \
  "buffer_load_dword v252, v249, %[rs], %[sv] offen offset:0 sc1\n\t" \
  "buffer_load_dword v253, v249, %[rs], %[sv] offen offset:256 sc1\n\t" \
  "ds_read_b32 v212, v248 offset:28672\n\t" \
  "ds_read_b64_tr_b16 v[244:245], v250 offset:1536\n\t" \
  "ds_read_b64_tr_b16 v[246:247], v250 offset:1600\n\t" \
  "ds_read_b64_tr_b16 v[232:233], v250 offset:0\n\t" \
  "ds_read_b64_tr_b16 v[234:235], v250 offset:64\n\t" \
  "ds_read_b64_tr_b16 v[236:237], v250 offset:512\n\t" \
  "ds_read_b64_tr_b16 v[238:239], v250 offset:576\n\t" \
  "s_setprio 3\n\t" \
  "s_waitcnt lgkmcnt(0)\n\t" \
  "v_cmp_ne_u32 vcc, %[step], v212\n\t" \
  "s_barrier\n\t" \
  "s_and_b32 s93, vcc_lo, 0xf\n\t" \
  "s_cbranch_scc1 .Lre%=\n\t" \
  ".Lrb%=:\n\t" \
  "ds_read_b64_tr_b16 v[208:209], v250 offset:3072\n\t" \
  "ds_read_b64_tr_b16 v[210:211], v250 offset:3136\n\t" \
  "ds_read_b64_tr_b16 v[212:213], v250 offset:3584\n\t" \
  "ds_read_b64_tr_b16 v[214:215], v250 offset:3648\n\t" \
  "ds_read_b128 v[220:223], v248 offset:19456\n\t" \
  "ds_read_b128 v[224:227], v248 offset:16384\n\t" \
  "ds_read_b128 v[228:231], v248 offset:17408\n\t" \
  "ds_read_b64_tr_b16 v[200:201], v250 offset:2048\n\t" \
  "ds_read_b64_tr_b16 v[202:203], v250 offset:2112\n\t" \
  "ds_read_b64_tr_b16 v[204:205], v250 offset:2560\n\t" \
  "ds_read_b64_tr_b16 v[206:207], v250 offset:2624\n\t" \
  "s_waitcnt lgkmcnt(9)\n\t" \
  "v_mfma_f32_16x16x32_bf16 v[208:211], v[208:211], v[240:243], 0\n\t" \
  "s_waitcnt lgkmcnt(7)\n\t" \
  "v_mfma_f32_16x16x32_bf16 v[208:211], v[212:215], v[244:247], v[208:211]\n\t" \
  "s_waitcnt lgkmcnt(6)\n\t" \
  "v_pk_add_f32 v[216:217], v[216:217], v[220:221]\n\t" \
  "v_pk_add_f32 v[218:219], v[218:219], v[222:223]\n\t" \
  "s_waitcnt lgkmcnt(4)\n\t" \
  "v_pk_add_f32 v[224:225], v[224:225], v[228:229]\n\t" \
  "v_pk_add_f32 v[226:227], v[226:227], v[230:231]\n\t" \
  "v_pk_add_f32 v[216:217], v[216:217], v[224:225]\n\t" \
  "v_pk_add_f32 v[218:219], v[218:219], v[226:227]\n\t" \
  "s_waitcnt lgkmcnt(2)\n\t" \
  "v_mfma_f32_16x16x32_bf16 v[208:211], v[200:203], v[232:235], v[208:211]\n\t" \
  "v_max3_f32 v224, v216, v217, v218\n\t" \
  "v_max_f32 v224, v224, v219\n\t" \
  "s_waitcnt lgkmcnt(0)\n\t" \
  "v_mfma_f32_16x16x32_bf16 v[208:211], v[204:207], v[236:239], v[208:211]\n\t" \
  "v_max_f32_dpp v225, v224, v224 row_ror:8 row_mask:0xf bank_mask:0xf\n\t" \
  "v_max_f32_dpp v226, v224, v224 row_ror:8 row_mask:0xf bank_mask:0xf\n\t" \
  "s_nop 1\n\t" \
  "v_permlane32_swap_b32 v225, v226\n\t" \
  "v_max_f32 v231, v225, v226\n\t" \
  "v_mov_b32 v220, 0x3f803f80\n\t" \
  "v_mov_b32 v221, 0x3f803f80\n\t" \
  "v_mov_b64 v[222:223], v[220:221]\n\t" \
  "v_mov_b32 v241, 0\n\t" \
  "v_mov_b32 v242, 0\n\t" \
  "v_max3_f32 v212, v208, v209, v231\n\t" \
  "v_max3_f32 v213, v212, v210, v211\n\t" \
  "v_max3_f32 v214, v212, v210, v211\n\t" \
  "v_cndmask_b32 v208, v208, v210, %[up]\n\t" \
  "v_cndmask_b32 v209, v209, v211, %[up]\n\t" \
  "v_permlane16_swap_b32 v213, v214\n\t" \
  "v_max_f32 v212, v213, v214\n\t" \
  "v_mul_f32 v212, %[negc], v212\n\t" \
  "v_fma_f32 v208, v208, %[c], v212\n\t" \
  "v_fma_f32 v209, v209, %[c], v212\n\t" \
  "v_exp_f32 v208, v208\n\t" \
  "v_fma_f32 v216, v216, %[c], v212\n\t" \
  "v_exp_f32 v209, v209\n\t" \
  "v_fma_f32 v217, v217, %[c], v212\n\t" \
  "v_exp_f32 v216, v216\n\t" \
  "v_fma_f32 v218, v218, %[c], v212\n\t" \
  "v_cvt_pk_bf16_f32 v240, v208, v209\n\t" \
  "v_cvt_pk_bf16_f32 v243, v208, v209\n\t" \
  "v_exp_f32 v217, v217\n\t" \
  "v_fma_f32 v219, v219, %[c], v212\n\t" \
  "v_mfma_f32_16x16x16_bf16 v[204:207], v[220:221], v[240:241], 0\n\t" \
  "v_exp_f32 v218, v218\n\t" \
  "s_waitcnt vmcnt(0)\n\t" \
  "v_mfma_f32_16x16x16_bf16 v[224:227], v[252:253], v[240:241], 0\n\t" \
  "v_cvt_pk_bf16_f32 v202, v216, v217\n\t" \
  "v_cvt_pk_bf16_f32 v200, v216, v217\n\t" \
  "v_exp_f32 v219, v219\n\t" \
  "v_mfma_f32_16x16x16_bf16 v[228:231], v[252:253], v[242:243], 0\n\t" \
  "v_cvt_pk_bf16_f32 v203, v218, v219\n\t" \
  "v_cvt_pk_bf16_f32 v201, v218, v219\n\t" \
  "v_mov_b32_dpp v200, v202 row_ror:8 row_mask:0xf bank_mask:0xc\n\t" \
  "v_mov_b32_dpp v202, v202 row_ror:8 row_mask:0xf bank_mask:0x3\n\t" \
  "v_mov_b32_dpp v201, v203 row_ror:8 row_mask:0xf bank_mask:0xc\n\t" \
  "v_mov_b32_dpp v203, v203 row_ror:8 row_mask:0xf bank_mask:0x3\n\t" \
  "v_max_u32 v212, v252, v253\n\t" \
  "v_cmp_le_u32 vcc, %[sent], v212\n\t" \
  "v_mfma_f32_16x16x32_bf16 v[204:207], v[220:223], v[200:203], v[204:207]\n\t" \
  ".Lpv%=:\n\t" \
  "v_mfma_f32_16x16x32_bf16 v[224:227], v[192:195], v[200:203], v[224:227]\n\t" \
  "v_mfma_f32_16x16x32_bf16 v[228:231], v[196:199], v[200:203], v[228:231]\n\t" \
  "s_cbranch_vccnz .Lvs%=\n\t" \
  ".Lvok%=:\n\t" \
  "s_nop 4\n\t" \
  "v_rcp_f32 v212, v204\n\t" \
  "v_cndmask_b32 v224, v224, v226, %[nh]\n\t" \
  "v_cndmask_b32 v225, v225, v227, %[nh]\n\t" \
  "v_cndmask_b32 v228, v228, v230, %[nh]\n\t" \
  "v_cndmask_b32 v229, v229, v231, %[nh]\n\t" \
  "v_pk_mul_f32 v[224:225], v[224:225], v[212:213] op_sel_hi:[1,0]\n\t" \
  "v_pk_mul_f32 v[228:229], v[228:229], v[212:213] op_sel_hi:[1,0]\n\t" \
  "v_cvt_pk_bf16_f32 v224, v224, v225\n\t" \
  "v_cvt_pk_bf16_f32 v225, v228, v229\n\t" \
  "buffer_store_dwordx2 v[224:225], v251, %[rs], %[so] offen sc1\n\t" \
  "s_memtime s[90:91]\n\t" \
  "s_setprio 0\n\t" \
  "s_waitcnt lgkmcnt(0)\n\t" \
  "s_mov_b32 %[tout], s90\n\t" \
  "s_mov_b32 %[spins], s92\n\t" \
  "s_branch .Lend%=\n\t" \
  ".Lre%=:\n\t" \
  "ds_read_b64_tr_b16 v[244:245], v250 offset:1536\n\t" \
  "ds_read_b64_tr_b16 v[246:247], v250 offset:1600\n\t" \
  "ds_read_b64_tr_b16 v[232:233], v250 offset:0\n\t" \
  "ds_read_b64_tr_b16 v[234:235], v250 offset:64\n\t" \
  "ds_read_b64_tr_b16 v[236:237], v250 offset:512\n\t" \
  "ds_read_b64_tr_b16 v[238:239], v250 offset:576\n\t" \
  "s_waitcnt lgkmcnt(0)\n\t" \
  "s_branch .Lrb%=\n\t" \
  ".Lvs%=:\n\t" \
  "v_cmp_eq_u32 vcc, %[sent], v252\n\t" \
  "v_cmp_eq_u32 s[94:95], %[sent], v253\n\t" \
  "s_or_b64 s[94:95], s[94:95], vcc\n\t" \
  "s_cbranch_scc0 .Lvok%=\n\t" \
  "s_add_u32 s92, s92, 1\n\t" \
  "s_sleep 2\n\t" \
  "s_and_b32 s93, s92, 63\n\t" \
  "s_cbranch_scc1 .Lvr%=\n\t" \
  "buffer_load_dword v212, off, %[rs], %[sab] sc1\n\t" \
  "s_waitcnt vmcnt(0)\n\t" \
  "v_readfirstlane_b32 s93, v212\n\t" \
  "s_nop 3\n\t" \
  "s_cmp_eq_u32 s93, 1\n\t" \
  "s_cbranch_scc1 .Lvok%=\n\t" \
  "s_cmp_lt_u32 s92, 0x80000\n\t" \
  "s_cbranch_scc1 .Lvr%=\n\t" \
  "v_mov_b32 v212, 1\n\t" \
  "buffer_store_dword v212, off, %[rs], %[sab] sc1\n\t" \
  "s_branch .Lvok%=\n\t" \
  ".Lvr%=:\n\t" \
  "buffer_load_dword v252, v249, %[rs], %[sv] offen offset:0 sc1\n\t" \
  "buffer_load_dword v253, v249, %[rs], %[sv] offen offset:256 sc1\n\t" \
  "s_waitcnt vmcnt(0)\n\t" \
  "v_mfma_f32_16x16x16_bf16 v[224:227], v[252:253], v[240:241], 0\n\t" \
  "v_mfma_f32_16x16x16_bf16 v[228:231], v[252:253], v[242:243], 0\n\t" \
  "v_max_u32 v212, v252, v253\n\t" \
  "v_cmp_le_u32 vcc, %[sent], v212\n\t" \
  "s_nop 4\n\t" \
  "s_branch .Lpv%=\n\t" \
  "s_branch .Lend%=\n\t" \
  ".Lslow%=:\n\t" \
  "s_mov_b64 s[94:95], 0\n\t" \
  "v_cmp_eq_u32 vcc, %[sent], v200\n\t" \
  "s_or_b64 s[94:95], s[94:95], vcc\n\t" \
  "v_cmp_eq_u32 vcc, %[sent], v201\n\t" \
  "s_or_b64 s[94:95], s[94:95], vcc\n\t" \
  "v_cmp_eq_u32 vcc, %[sent], v202\n\t" \
  "s_or_b64 s[94:95], s[94:95], vcc\n\t" \
  "v_cmp_eq_u32 vcc, %[sent], v203\n\t" \
  "s_or_b64 s[94:95], s[94:95], vcc\n\t" \
  "v_cmp_eq_u32 vcc, %[sent], v204\n\t" \
  "s_or_b64 s[94:95], s[94:95], vcc\n\t" \
  "v_cmp_eq_u32 vcc, %[sent], v205\n\t" \
  "s_or_b64 s[94:95], s[94:95], vcc\n\t" \
  "v_cmp_eq_u32 vcc, %[sent], v206\n\t" \
  "s_or_b64 s[94:95], s[94:95], vcc\n\t" \
  "v_cmp_eq_u32 vcc, %[sent], v207\n\t" \
  "s_or_b64 s[94:95], s[94:95], vcc\n\t" \
  "s_cmp_eq_u64 s[94:95], 0\n\t" \
  "s_cbranch_scc1 .Lok%=\n\t" \
  "v_mov_b64 v[192:193], 0\n\t" \
  "v_mov_b64 v[194:195], 0\n\t" \
  "v_mov_b64 v[196:197], 0\n\t" \
  "v_mov_b64 v[198:199], 0\n\t" \
  "s_add_u32 s92, s92, 1\n\t" \
  "s_sleep 6\n\t" \
  "s_cmp_eq_u32 s92, 1\n\t" \
  "s_cbranch_scc1 .Lpoll%=\n\t" \
  "s_sleep 1\n\t" \
  "s_and_b32 s93, s92, 63\n\t" \
  "s_cmp_lg_u32 s93, 0\n\t" \
  "s_cbranch_scc1 .Lpoll%=\n\t" \
  "v_mov_b32 v245, 0\n\t" \
  "buffer_load_dword v244, v245, %[rs], %[sab] offen sc1\n\t" \
  "s_waitcnt vmcnt(0)\n\t" \
  "v_readfirstlane_b32 s93, v244\n\t" \
  "s_nop 3\n\t" \
  "s_cmp_eq_u32 s93, 1\n\t" \
  "s_cbranch_scc1 .Lok%=\n\t" \
  "s_cmp_lt_u32 s92, 0x80000\n\t" \
  "s_cbranch_scc1 .Lpoll%=\n\t" \
  "v_mov_b32 v244, 1\n\t" \
  "buffer_store_dword v244, v245, %[rs], %[sab] offen sc1\n\t" \
  "s_branch .Lok%=\n\t" \
  ".Lend%=:\n\t"
#define QK_ASM_3 \
  "s_waitcnt lgkmcnt(0)\n\t" \
  "v_subrev_u32 v244, 0x8000, v250\n\t" \
  "ds_read_b128 v[216:219], v248 offset:45056\n\t" \
  "ds_read_b128 v[220:223], v248 offset:46080\n\t" \
  "ds_read_b128 v[224:227], v248 offset:47104\n\t" \
  "ds_read_b128 v[228:231], v248 offset:48128\n\t" \
  "ds_read_b128 v[208:211], v244 offset:0\n\t" \
  "ds_read_b128 v[212:215], v244 offset:1024\n\t" \
  "ds_read_b128 v[236:239], v244 offset:2048\n\t" \
  "ds_read_b128 v[240:243], v244 offset:3072\n\t" \
  "v_mov_b64 v[192:193], 0\n\t" \
  "v_mov_b64 v[194:195], 0\n\t" \
  "v_mov_b64 v[196:197], 0\n\t" \
  "v_mov_b64 v[198:199], 0\n\t" \
  "s_movk_i32 s92, 0x2000\n\t" \
  ".Lwt%=:\n\t" \
  "ds_read_b64 v[200:201], v248 offset:30720\n\t" \
  "s_memrealtime s[90:91]\n\t" \
  "s_sub_u32 s92, s92, 1\n\t" \
  "s_waitcnt lgkmcnt(0)\n\t" \
  "v_readfirstlane_b32 s93, v200\n\t" \
  "v_readfirstlane_b32 %[tgt], v201\n\t" \
  "s_cbranch_scc1 .Lgo%=\n\t" \
  "s_cmp_eq_u32 s93, %[step]\n\t" \
  "s_cbranch_scc1 .Lwx%=\n\t" \
  "s_sleep 1\n\t" \
  "s_branch .Lwt%=\n\t" \
  ".Lwx%=:\n\t" \
  "s_lshl_b32 s93, s90, 2\n\t" \
  "s_sub_i32 s93, %[tgt], s93\n\t" \
  "s_cmp_le_i32 s93, 0\n\t" \
  "s_cbranch_scc1 .Lgo%=\n\t" \
  "s_min_i32 s93, s93, 8000\n\t" \
  "s_mov_b32 s94, s93\n\t" \
  ".Lsl%=:\n\t" \
  "s_sub_i32 s94, s94, 18\n\t" \
  "s_cmp_lt_i32 s94, 30\n\t" \
  "s_cbranch_scc1 .Lsx%=\n\t" \
  "s_sleep 1\n\t" \
  "s_branch .Lsl%=\n\t" \
  ".Lsx%=:\n\t" \
  "s_sub_i32 s93, s93, 12\n\t" \
  "s_max_i32 s93, s93, 4\n\t" \
  "s_lshr_b32 s93, s93, 2\n\t" \
  "s_add_u32 s93, s93, s90\n\t" \
  "s_memrealtime s[90:91]\n\t" \
  ".Lpace%=:\n\t" \
  "s_memrealtime s[94:95]\n\t" \
  "s_waitcnt lgkmcnt(1)\n\t" \
  "s_sub_i32 s92, s90, s93\n\t" \
  "s_cmp_ge_i32 s92, 0\n\t" \
  "s_cbranch_scc1 .Le%=\n\t" \
  "s_memrealtime s[90:91]\n\t" \
  "s_waitcnt lgkmcnt(1)\n\t" \
  "s_sub_i32 s92, s94, s93\n\t" \
  "s_cmp_lt_i32 s92, 0\n\t" \
  "s_cbranch_scc1 .Lpace%=\n\t" \
  "s_nop 4\n\t" \
  ".Le%=:\n\t" \
  "s_add_u32 s92, s92, s93\n\t" \
  "s_lshl_b32 s92, s92, 2\n\t" \
  "s_sub_i32 s92, %[tgt], s92\n\t" \
  "s_max_i32 s92, s92, 0\n\t" \
  "s_bitcmp0_b32 s92, 3\n\t" \
  "s_cbranch_scc1 .Lf3%=\n\t" \
  "s_nop 15\n\t" \
  ".Lf3%=:\n\t" \
  "s_bitcmp0_b32 s92, 2\n\t" \
  "s_cbranch_scc1 .Lf2%=\n\t" \
  "s_nop 10\n\t" \
  ".Lf2%=:\n\t" \
  ".Lgo%=:\n\t" \
  "s_mov_b32 s92, 0\n\t" \
  ".Lpoll%=:\n\t" \
  "buffer_load_dwordx4 v[200:203], v248, %[rs], %[som] offen sc1\n\t" \
  "buffer_load_dwordx4 v[204:207], v248, %[rs], %[som2] offen sc1\n\t" \
  "s_waitcnt vmcnt(1) lgkmcnt(0)\n\t" \
  "ds_write_b128 v249, v[200:203]\n\t" \
  "v_smfmac_f32_16x16x64_bf16 v[192:195], v[200:203], v[208:215], v251\n\t" \
  "v_max3_u32 v244, v200, v201, v202\n\t" \
  "v_smfmac_f32_16x16x64_bf16 v[196:199], v[200:203], v[236:243], v251\n\t" \
  "s_waitcnt vmcnt(0)\n\t" \
  "ds_write_b128 v249, v[204:207] offset:8192\n\t" \
  "v_smfmac_f32_16x16x64_bf16 v[192:195], v[204:207], v[216:223], v251\n\t" \
  "v_max3_u32 v244, v244, v203, v204\n\t" \
  "v_max3_u32 v244, v244, v205, v206\n\t" \
  "v_smfmac_f32_16x16x64_bf16 v[196:199], v[204:207], v[224:231], v251\n\t" \
  "v_max_u32 v244, v244, v207\n\t" \
  "v_cmp_le_u32 vcc, %[sent], v244\n\t" \
  "s_cbranch_vccnz .Lslow%=\n\t" \
  ".Lok%=:\n\t" \
  "ds_read_b128 v[216:219], v250 offset:32768\n\t" \
  "ds_read_b128 v[220:223], v250 offset:33792\n\t" \
  "ds_read_b128 v[224:227], v250 offset:34816\n\t" \
  "ds_read_b128 v[228:231], v250 offset:35840\n\t" \
  "s_waitcnt lgkmcnt(4)\n\t" \
  "s_barrier\n\t" \
  "ds_read_b128 v[208:211], v248 offset:2048\n\t" \
  "ds_read_b128 v[212:215], v248 offset:5120\n\t" \
  "ds_read_b128 v[236:239], v248 offset:6144\n\t" \
  "ds_read_b128 v[240:243], v248 offset:7168\n\t" \
  "s_waitcnt lgkmcnt(3)\n\t" \
  "ds_read_b128 v[244:247], v248 offset:12288\n\t" \
  "v_smfmac_f32_16x16x64_bf16 v[192:195], v[208:211], v[216:223], v251\n\t" \
  "ds_read_b128 v[216:219], v250 offset:0\n\t" \
  "v_smfmac_f32_16x16x64_bf16 v[196:199], v[208:211], v[224:231], v251\n\t" \
  "s_waitcnt lgkmcnt(4)\n\t" \
  "ds_read_b128 v[220:223], v250 offset:1024\n\t" \
  "v_smfmac_f32_16x16x64_bf16 v[192:195], v[212:215], v[0:7], v251\n\t" \
  "ds_read_b128 v[224:227], v250 offset:2048\n\t" \
  "v_smfmac_f32_16x16x64_bf16 v[196:199], v[212:215], v[8:15], v251\n\t" \
  "s_waitcnt lgkmcnt(5)\n\t" \
  "ds_read_b128 v[228:231], v250 offset:3072\n\t" \
  "v_smfmac_f32_16x16x64_bf16 v[192:195], v[236:239], v[16:23], v251\n\t" \
  "ds_read_b128 v[200:203], v248 offset:8192\n\t" \
  "v_smfmac_f32_16x16x64_bf16 v[196:199], v[236:239], v[24:31], v251\n\t" \
  "s_waitcnt lgkmcnt(6)\n\t" \
  "ds_read_b128 v[204:207], v248 offset:9216\n\t" \
  "v_smfmac_f32_16x16x64_bf16 v[192:195], v[240:243], v[32:39], v251\n\t" \
  "ds_read_b128 v[208:211], v248 offset:10240\n\t" \
  "v_smfmac_f32_16x16x64_bf16 v[196:199], v[240:243], v[40:47], v251\n\t" \
  "s_waitcnt lgkmcnt(5)\n\t" \
  "ds_read_b128 v[212:215], v248 offset:13312\n\t" \
  "v_smfmac_f32_16x16x64_bf16 v[192:195], v[244:247], v[216:223], v251\n\t" \
  "s_waitcnt lgkmcnt(4)\n\t" \
  "v_smfmac_f32_16x16x64_bf16 v[196:199], v[244:247], v[224:231], v251\n\t" \
  "s_waitcnt lgkmcnt(3)\n\t" \
  "ds_read_b128 v[236:239], v248 offset:14336\n\t" \
  "v_smfmac_f32_16x16x64_bf16 v[192:195], v[200:203], v[48:55], v251\n\t" \
  "ds_read_b128 v[240:243], v248 offset:15360\n\t" \
  "v_smfmac_f32_16x16x64_bf16 v[196:199], v[200:203], v[56:63], v251\n\t" \
  "s_waitcnt lgkmcnt(4)\n\t" \
  "v_smfmac_f32_16x16x64_bf16 v[192:195], v[204:207], v[64:71], v251\n\t" \
  "ds_read_b128 v[244:247], v248 offset:0\n\t" \
  "v_smfmac_f32_16x16x64_bf16 v[196:199], v[204:207], v[72:79], v251\n\t" \
  "s_waitcnt lgkmcnt(4)\n\t" \
  "v_smfmac_f32_16x16x64_bf16 v[192:195], v[208:211], v[80:87], v251\n\t" \
  "ds_read_b128 v[200:203], v248 offset:1024\n\t" \
  "v_smfmac_f32_16x16x64_bf16 v[196:199], v[208:211], v[88:95], v251\n\t" \
  "s_waitcnt lgkmcnt(4)\n\t" \
  "v_smfmac_f32_16x16x64_bf16 v[192:195], v[212:215], v[96:103], v251\n\t" \
  "ds_read_b128 v[204:207], v248 offset:4096\n\t" \
  "v_smfmac_f32_16x16x64_bf16 v[196:199], v[212:215], v[104:111], v251\n\t" \
  "s_waitcnt lgkmcnt(3)\n\t" \
  "v_smfmac_f32_16x16x64_bf16 v[192:195], v[236:239], v[112:119], v251\n\t" \
  "v_smfmac_f32_16x16x64_bf16 v[196:199], v[236:239], v[120:127], v251\n\t" \
  "v_smfmac_f32_16x16x64_bf16 v[192:195], v[240:243], v[128:135], v251\n\t" \
  "v_smfmac_f32_16x16x64_bf16 v[196:199], v[240:243], v[136:143], v251\n\t" \
  "s_waitcnt lgkmcnt(1)\n\t" \
  "v_smfmac_f32_16x16x64_bf16 v[192:195], v[244:247], v[144:151], v251\n\t" \
  "v_smfmac_f32_16x16x64_bf16 v[196:199], v[244:247], v[152:159], v251\n\t" \
  "v_smfmac_f32_16x16x64_bf16 v[192:195], v[200:203], v[160:167], v251\n\t" \
  "v_smfmac_f32_16x16x64_bf16 v[196:199], v[200:203], v[168:175], v251\n\t" \
  "s_waitcnt lgkmcnt(0)\n\t" \
  "v_smfmac_f32_16x16x64_bf16 v[192:195], v[204:207], v[176:183], v251\n\t" \
  "v_smfmac_f32_16x16x64_bf16 v[196:199], v[204:207], v[184:191], v251\n\t" \
  "ds_read_b128 v[200:203], v248 offset:19456\n\t" \
  "ds_read_b128 v[204:207], v248 offset:23552\n\t" \
  "v_mov_b32 v250, v232\n\t" \
  "v_mov_b32 v249, v233\n\t" \
  "s_nop 2\n\t" \
  "v_add_f32 v192, v192, v194\n\t" \
  "v_add_f32 v193, v193, v195\n\t" \
  "v_add_f32 v196, v196, v198\n\t" \
  "v_add_f32 v197, v197, v199\n\t" \
  "v_cvt_pk_bf16_f32 v192, v192, v193\n\t" \
  "v_cvt_pk_bf16_f32 v196, v196, v197\n\t" \
  "ds_write2_b32 v252, v192, v196 offset1:64\n\t" \
  "v_mov_b32 v251, v234\n\t" \
  "ds_read_b64_tr_b16 v[244:245], v250 offset:1536\n\t" \
  "ds_read_b64_tr_b16 v[246:247], v250 offset:1600\n\t" \
  "v_mov_b32 v212, %[step]\n\t" \
  "s_mov_b64 exec, 1\n\t" \
  "ds_write_b32 v248, v212 offset:28720\n\t" \
  "s_mov_b64 exec, -1\n\t" \
  "buffer_load_dwordx4 v[192:195], v248, %[rt], %[svt] offen offset:0 sc1\n\t" \
  "buffer_load_dwordx4 v[196:199], v248, %[rt], %[svt] offen offset:1024 sc1\n\t" \
  "s_waitcnt lgkmcnt(1)\n\t" \
  "v_mfma_f32_16x16x32_bf16 v[200:203], v[200:203], v[244:247], 0\n\t" \
  "v_mfma_f32_16x16x32_bf16 v[204:207], v[204:207], v[244:247], 0\n\t" \
  "s_nop 7\n\t" \
  "v_cndmask_b32 v216, v200, v204, %[nh]\n\t" \
  "v_cndmask_b32 v217, v201, v205, %[nh]\n\t" \
  "v_cndmask_b32 v218, v202, v206, %[nh]\n\t" \
  "v_cndmask_b32 v219, v203, v207, %[nh]\n\t" \
  "ds_write_b128 v248, v[216:219] offset:19456\n\t" \
  "buffer_load_dword v252, v249, %[rs], %[sv] offen offset:0 sc1\n\t" \
  "buffer_load_dword v253, v249, %[rs], %[sv] offen offset:256 sc1\n\t" \
  "ds_read_b32 v212, v248 offset:28672\n\t" \
  "ds_read_b64_tr_b16 v[232:233], v250 offset:0\n\t" \
  "ds_read_b64_tr_b16 v[234:235], v250 offset:64\n\t" \
  "ds_read_b64_tr_b16 v[236:237], v250 offset:512\n\t" \
  "ds_read_b64_tr_b16 v[238:239], v250 offset:576\n\t" \
  "ds_read_b64_tr_b16 v[240:241], v250 offset:1024\n\t" \
  "ds_read_b64_tr_b16 v[242:243], v250 offset:1088\n\t" \
  "s_setprio 3\n\t" \
  "s_waitcnt lgkmcnt(0)\n\t" \
  "v_cmp_ne_u32 vcc, %[step], v212\n\t" \
  "s_barrier\n\t" \
  "s_and_b32 s93, vcc_lo, 0xf\n\t" \
  "s_cbranch_scc1 .Lre%=\n\t" \
  ".Lrb%=:\n\t" \
  "ds_read_b64_tr_b16 v[212:213], v250 offset:3584\n\t" \
  "ds_read_b64_tr_b16 v[214:215], v250 offset:3648\n\t" \
  "ds_read_b64_tr_b16 v[200:201], v250 offset:2048\n\t" \
  "ds_read_b64_tr_b16 v[202:203], v250 offset:2112\n\t" \
  "ds_read_b128 v[220:223], v248 offset:16384\n\t" \
  "ds_read_b128 v[224:227], v248 offset:17408\n\t" \
  "ds_read_b128 v[228:231], v248 offset:18432\n\t" \
  "ds_read_b64_tr_b16 v[204:205], v250 offset:2560\n\t" \
  "ds_read_b64_tr_b16 v[206:207], v250 offset:2624\n\t" \
  "ds_read_b64_tr_b16 v[208:209], v250 offset:3072\n\t" \
  "ds_read_b64_tr_b16 v[210:211], v250 offset:3136\n\t" \
  "s_waitcnt lgkmcnt(9)\n\t" \
  "v_mfma_f32_16x16x32_bf16 v[212:215], v[212:215], v[244:247], 0\n\t" \
  "s_waitcnt lgkmcnt(7)\n\t" \
  "v_mfma_f32_16x16x32_bf16 v[212:215], v[200:203], v[232:235], v[212:215]\n\t" \
  "s_waitcnt lgkmcnt(6)\n\t" \
  "v_pk_add_f32 v[216:217], v[216:217], v[220:221]\n\t" \
  "v_pk_add_f32 v[218:219], v[218:219], v[222:223]\n\t" \
  "s_waitcnt lgkmcnt(4)\n\t" \
  "v_pk_add_f32 v[224:225], v[224:225], v[228:229]\n\t" \
  "v_pk_add_f32 v[226:227], v[226:227], v[230:231]\n\t" \
  "v_pk_add_f32 v[216:217], v[216:217], v[224:225]\n\t" \
  "v_pk_add_f32 v[218:219], v[218:219], v[226:227]\n\t" \
  "s_waitcnt lgkmcnt(2)\n\t" \
  "v_mfma_f32_16x16x32_bf16 v[212:215], v[204:207], v[236:239], v[212:215]\n\t" \
  "v_max3_f32 v224, v216, v217, v218\n\t" \
  "v_max_f32 v224, v224, v219\n\t" \
  "s_waitcnt lgkmcnt(0)\n\t" \
  "v_mfma_f32_16x16x32_bf16 v[212:215], v[208:211], v[240:243], v[212:215]\n\t" \
  "v_max_f32_dpp v225, v224, v224 row_ror:8 row_mask:0xf bank_mask:0xf\n\t" \
  "v_max_f32_dpp v226, v224, v224 row_ror:8 row_mask:0xf bank_mask:0xf\n\t" \
  "s_nop 1\n\t" \
  "v_permlane32_swap_b32 v225, v226\n\t" \
  "v_max_f32 v231, v225, v226\n\t" \
  "v_mov_b32 v220, 0x3f803f80\n\t" \
  "v_mov_b32 v221, 0x3f803f80\n\t" \
  "v_mov_b64 v[222:223], v[220:221]\n\t" \
  "v_mov_b32 v245, 0\n\t" \
  "v_mov_b32 v246, 0\n\t" \
  "v_max3_f32 v200, v212, v213, v231\n\t" \
  "v_max3_f32 v201, v200, v214, v215\n\t" \
  "v_max3_f32 v202, v200, v214, v215\n\t" \
  "v_cndmask_b32 v212, v212, v214, %[up]\n\t" \
  "v_cndmask_b32 v213, v213, v215, %[up]\n\t" \
  "v_permlane16_swap_b32 v201, v202\n\t" \
  "v_max_f32 v200, v201, v202\n\t" \
  "v_mul_f32 v200, %[negc], v200\n\t" \
  "v_fma_f32 v212, v212, %[c], v200\n\t" \
  "v_fma_f32 v213, v213, %[c], v200\n\t" \
  "v_exp_f32 v212, v212\n\t" \
  "v_fma_f32 v216, v216, %[c], v200\n\t" \
  "v_exp_f32 v213, v213\n\t" \
  "v_fma_f32 v217, v217, %[c], v200\n\t" \
  "v_exp_f32 v216, v216\n\t" \
  "v_fma_f32 v218, v218, %[c], v200\n\t" \
  "v_cvt_pk_bf16_f32 v244, v212, v213\n\t" \
  "v_cvt_pk_bf16_f32 v247, v212, v213\n\t" \
  "v_exp_f32 v217, v217\n\t" \
  "v_fma_f32 v219, v219, %[c], v200\n\t" \
  "v_mfma_f32_16x16x16_bf16 v[208:211], v[220:221], v[244:245], 0\n\t" \
  "v_exp_f32 v218, v218\n\t" \
  "s_waitcnt vmcnt(0)\n\t" \
  "v_mfma_f32_16x16x16_bf16 v[224:227], v[252:253], v[244:245], 0\n\t" \
  "v_cvt_pk_bf16_f32 v206, v216, v217\n\t" \
  "v_cvt_pk_bf16_f32 v204, v216, v217\n\t" \
  "v_exp_f32 v219, v219\n\t" \
  "v_mfma_f32_16x16x16_bf16 v[228:231], v[252:253], v[246:247], 0\n\t" \
  "v_cvt_pk_bf16_f32 v207, v218, v219\n\t" \
  "v_cvt_pk_bf16_f32 v205, v218, v219\n\t" \
  "v_mov_b32_dpp v204, v206 row_ror:8 row_mask:0xf bank_mask:0xc\n\t" \
  "v_mov_b32_dpp v206, v206 row_ror:8 row_mask:0xf bank_mask:0x3\n\t" \
  "v_mov_b32_dpp v205, v207 row_ror:8 row_mask:0xf bank_mask:0xc\n\t" \
  "v_mov_b32_dpp v207, v207 row_ror:8 row_mask:0xf bank_mask:0x3\n\t" \
  "v_max_u32 v200, v252, v253\n\t" \
  "v_cmp_le_u32 vcc, %[sent], v200\n\t" \
  "v_mfma_f32_16x16x32_bf16 v[208:211], v[220:223], v[204:207], v[208:211]\n\t" \
  ".Lpv%=:\n\t" \
  "v_mfma_f32_16x16x32_bf16 v[224:227], v[192:195], v[204:207], v[224:227]\n\t" \
  "v_mfma_f32_16x16x32_bf16 v[228:231], v[196:199], v[204:207], v[228:231]\n\t" \
  "s_cbranch_vccnz .Lvs%=\n\t" \
  ".Lvok%=:\n\t" \
  "s_nop 4\n\t" \
  "v_rcp_f32 v200, v208\n\t" \
  "v_cndmask_b32 v224, v224, v226, %[nh]\n\t" \
  "v_cndmask_b32 v225, v225, v227, %[nh]\n\t" \
  "v_cndmask_b32 v228, v228, v230, %[nh]\n\t" \
  "v_cndmask_b32 v229, v229, v231, %[nh]\n\t" \
  "v_pk_mul_f32 v[224:225], v[224:225], v[200:201] op_sel_hi:[1,0]\n\t" \
  "v_pk_mul_f32 v[228:229], v[228:229], v[200:201] op_sel_hi:[1,0]\n\t" \
  "v_cvt_pk_bf16_f32 v224, v224, v225\n\t" \
  "v_cvt_pk_bf16_f32 v225, v228, v229\n\t" \
  "buffer_store_dwordx2 v[224:225], v251, %[rs], %[so] offen sc1\n\t" \
  "s_memtime s[90:91]\n\t" \
  "s_setprio 0\n\t" \
  "s_waitcnt lgkmcnt(0)\n\t" \
  "s_mov_b32 %[tout], s90\n\t" \
  "s_mov_b32 %[spins], s92\n\t" \
  "s_branch .Lend%=\n\t" \
  ".Lre%=:\n\t" \
  "ds_read_b64_tr_b16 v[232:233], v250 offset:0\n\t" \
  "ds_read_b64_tr_b16 v[234:235], v250 offset:64\n\t" \
  "ds_read_b64_tr_b16 v[236:237], v250 offset:512\n\t" \
  "ds_read_b64_tr_b16 v[238:239], v250 offset:576\n\t" \
  "ds_read_b64_tr_b16 v[240:241], v250 offset:1024\n\t" \
  "ds_read_b64_tr_b16 v[242:243], v250 offset:1088\n\t" \
  "s_waitcnt lgkmcnt(0)\n\t" \
  "s_branch .Lrb%=\n\t" \
  ".Lvs%=:\n\t" \
  "v_cmp_eq_u32 vcc, %[sent], v252\n\t" \
  "v_cmp_eq_u32 s[94:95], %[sent], v253\n\t" \
  "s_or_b64 s[94:95], s[94:95], vcc\n\t" \
  "s_cbranch_scc0 .Lvok%=\n\t" \
  "s_add_u32 s92, s92, 1\n\t" \
  "s_sleep 2\n\t" \
  "s_and_b32 s93, s92, 63\n\t" \
  "s_cbranch_scc1 .Lvr%=\n\t" \
  "buffer_load_dword v200, off, %[rs], %[sab] sc1\n\t" \
  "s_waitcnt vmcnt(0)\n\t" \
  "v_readfirstlane_b32 s93, v200\n\t" \
  "s_nop 3\n\t" \
  "s_cmp_eq_u32 s93, 1\n\t" \
  "s_cbranch_scc1 .Lvok%=\n\t" \
  "s_cmp_lt_u32 s92, 0x80000\n\t" \
  "s_cbranch_scc1 .Lvr%=\n\t" \
  "v_mov_b32 v200, 1\n\t" \
  "buffer_store_dword v200, off, %[rs], %[sab] sc1\n\t" \
  "s_branch .Lvok%=\n\t" \
  ".Lvr%=:\n\t" \
  "buffer_load_dword v252, v249, %[rs], %[sv] offen offset:0 sc1\n\t" \
  "buffer_load_dword v253, v249, %[rs], %[sv] offen offset:256 sc1\n\t" \
  "s_waitcnt vmcnt(0)\n\t" \
  "v_mfma_f32_16x16x16_bf16 v[224:227], v[252:253], v[244:245], 0\n\t" \
  "v_mfma_f32_16x16x16_bf16 v[228:231], v[252:253], v[246:247], 0\n\t" \
  "v_max_u32 v200, v252, v253\n\t" \
  "v_cmp_le_u32 vcc, %[sent], v200\n\t" \
  "s_nop 4\n\t" \
  "s_branch .Lpv%=\n\t" \
  "s_branch .Lend%=\n\t" \
  ".Lslow%=:\n\t" \
  "s_mov_b64 s[94:95], 0\n\t" \
  "v_cmp_eq_u32 vcc, %[sent], v200\n\t" \
  "s_or_b64 s[94:95], s[94:95], vcc\n\t" \
  "v_cmp_eq_u32 vcc, %[sent], v201\n\t" \
  "s_or_b64 s[94:95], s[94:95], vcc\n\t" \
  "v_cmp_eq_u32 vcc, %[sent], v202\n\t" \
  "s_or_b64 s[94:95], s[94:95], vcc\n\t" \
  "v_cmp_eq_u32 vcc, %[sent], v203\n\t" \
  "s_or_b64 s[94:95], s[94:95], vcc\n\t" \
  "v_cmp_eq_u32 vcc, %[sent], v204\n\t" \
  "s_or_b64 s[94:95], s[94:95], vcc\n\t" \
  "v_cmp_eq_u32 vcc, %[sent], v205\n\t" \
  "s_or_b64 s[94:95], s[94:95], vcc\n\t" \
  "v_cmp_eq_u32 vcc, %[sent], v206\n\t" \
  "s_or_b64 s[94:95], s[94:95], vcc\n\t" \
  "v_cmp_eq_u32 vcc, %[sent], v207\n\t" \
  "s_or_b64 s[94:95], s[94:95], vcc\n\t" \
  "s_cmp_eq_u64 s[94:95], 0\n\t" \
  "s_cbranch_scc1 .Lok%=\n\t" \
  "v_mov_b64 v[192:193], 0\n\t" \
  "v_mov_b64 v[194:195], 0\n\t" \
  "v_mov_b64 v[196:197], 0\n\t" \
  "v_mov_b64 v[198:199], 0\n\t" \
  "s_add_u32 s92, s92, 1\n\t" \
  "s_sleep 6\n\t" \
  "s_cmp_eq_u32 s92, 1\n\t" \
  "s_cbranch_scc1 .Lpoll%=\n\t" \
  "s_sleep 1\n\t" \
  "s_and_b32 s93, s92, 63\n\t" \
  "s_cmp_lg_u32 s93, 0\n\t" \
  "s_cbranch_scc1 .Lpoll%=\n\t" \
  "v_mov_b32 v245, 0\n\t" \
  "buffer_load_dword v244, v245, %[rs], %[sab] offen sc1\n\t" \
  "s_waitcnt vmcnt(0)\n\t" \
  "v_readfirstlane_b32 s93, v244\n\t" \
  "s_nop 3\n\t" \
  "s_cmp_eq_u32 s93, 1\n\t" \
  "s_cbranch_scc1 .Lok%=\n\t" \
  "s_cmp_lt_u32 s92, 0x80000\n\t" \
  "s_cbranch_scc1 .Lpoll%=\n\t" \
  "v_mov_b32 v244, 1\n\t" \
  "buffer_store_dword v244, v245, %[rs], %[sab] offen sc1\n\t" \
  "s_branch .Lok%=\n\t" \
  ".Lend%=:\n\t"
#define QK_ASM_4 \
  "s_waitcnt lgkmcnt(0)\n\t" \
  "v_subrev_u32 v244, 0x8000, v250\n\t" \
  "ds_read_b128 v[216:219], v248 offset:49152\n\t" \
  "ds_read_b128 v[220:223], v248 offset:50176\n\t" \
  "ds_read_b128 v[224:227], v248 offset:51200\n\t" \
  "ds_read_b128 v[228:231], v248 offset:52224\n\t" \
  "ds_read_b128 v[208:211], v244 offset:0\n\t" \
  "ds_read_b128 v[212:215], v244 offset:1024\n\t" \
  "ds_read_b128 v[236:239], v244 offset:2048\n\t" \
  "ds_read_b128 v[240:243], v244 offset:3072\n\t" \
  "v_mov_b64 v[192:193], 0\n\t" \
  "v_mov_b64 v[194:195], 0\n\t" \
  "v_mov_b64 v[196:197], 0\n\t" \
  "v_mov_b64 v[198:199], 0\n\t" \
  "s_movk_i32 s92, 0x2000\n\t" \
  ".Lwt%=:\n\t" \
  "ds_read_b64 v[200:201], v248 offset:30720\n\t" \
  "s_memrealtime s[90:91]\n\t" \
  "s_sub_u32 s92, s92, 1\n\t" \
  "s_waitcnt lgkmcnt(0)\n\t" \
  "v_readfirstlane_b32 s93, v200\n\t" \
  "v_readfirstlane_b32 %[tgt], v201\n\t" \
  "s_cbranch_scc1 .Lgo%=\n\t" \
  "s_cmp_eq_u32 s93, %[step]\n\t" \
  "s_cbranch_scc1 .Lwx%=\n\t" \
  "s_sleep 1\n\t" \
  "s_branch .Lwt%=\n\t" \
  ".Lwx%=:\n\t" \
  "s_lshl_b32 s93, s90, 2\n\t" \
  "s_sub_i32 s93, %[tgt], s93\n\t" \
  "s_cmp_le_i32 s93, 0\n\t" \
  "s_cbranch_scc1 .Lgo%=\n\t" \
  "s_min_i32 s93, s93, 8000\n\t" \
  "s_mov_b32 s94, s93\n\t" \
  ".Lsl%=:\n\t" \
  "s_sub_i32 s94, s94, 18\n\t" \
  "s_cmp_lt_i32 s94, 30\n\t" \
  "s_cbranch_scc1 .Lsx%=\n\t" \
  "s_sleep 1\n\t" \
  "s_branch .Lsl%=\n\t" \
  ".Lsx%=:\n\t" \
  "s_sub_i32 s93, s93, 12\n\t" \
  "s_max_i32 s93, s93, 4\n\t" \
  "s_lshr_b32 s93, s93, 2\n\t" \
  "s_add_u32 s93, s93, s90\n\t" \
  "s_memrealtime s[90:91]\n\t" \
  ".Lpace%=:\n\t" \
  "s_memrealtime s[94:95]\n\t" \
  "s_waitcnt lgkmcnt(1)\n\t" \
  "s_sub_i32 s92, s90, s93\n\t" \
  "s_cmp_ge_i32 s92, 0\n\t" \
  "s_cbranch_scc1 .Le%=\n\t" \
  "s_memrealtime s[90:91]\n\t" \
  "s_waitcnt lgkmcnt(1)\n\t" \
  "s_sub_i32 s92, s94, s93\n\t" \
  "s_cmp_lt_i32 s92, 0\n\t" \
  "s_cbranch_scc1 .Lpace%=\n\t" \
  "s_nop 4\n\t" \
  ".Le%=:\n\t" \
  "s_add_u32 s92, s92, s93\n\t" \
  "s_lshl_b32 s92, s92, 2\n\t" \
  "s_sub_i32 s92, %[tgt], s92\n\t" \
  "s_max_i32 s92, s92, 0\n\t" \
  "s_bitcmp0_b32 s92, 3\n\t" \
  "s_cbranch_scc1 .Lf3%=\n\t" \
  "s_nop 15\n\t" \
  ".Lf3%=:\n\t" \
  "s_bitcmp0_b32 s92, 2\n\t" \
  "s_cbranch_scc1 .Lf2%=\n\t" \
  "s_nop 10\n\t" \
  ".Lf2%=:\n\t" \
  ".Lgo%=:\n\t" \
  "s_mov_b32 s92, 0\n\t" \
  ".Lpoll%=:\n\t" \
  "buffer_load_dwordx4 v[200:203], v248, %[rs], %[som] offen sc1\n\t" \
  "buffer_load_dwordx4 v[204:207], v248, %[rs], %[som2] offen sc1\n\t" \
  "s_waitcnt vmcnt(1) lgkmcnt(0)\n\t" \
  "ds_write_b128 v249, v[200:203]\n\t" \
  "v_smfmac_f32_16x16x64_bf16 v[192:195], v[200:203], v[208:215], v251\n\t" \
  "v_max3_u32 v244, v200, v201, v202\n\t" \
  "v_smfmac_f32_16x16x64_bf16 v[196:199], v[200:203], v[236:243], v251\n\t" \
  "s_waitcnt vmcnt(0)\n\t" \
  "ds_write_b128 v249, v[204:207] offset:8192\n\t" \
  "v_smfmac_f32_16x16x64_bf16 v[192:195], v[204:207], v[216:223], v251\n\t" \
  "v_max3_u32 v244, v244, v203, v204\n\t" \
  "v_max3_u32 v244, v244, v205, v206\n\t" \
  "v_smfmac_f32_16x16x64_bf16 v[196:199], v[204:207], v[224:231], v251\n\t" \
  "v_max_u32 v244, v244, v207\n\t" \
  "v_cmp_le_u32 vcc, %[sent], v244\n\t" \
  "s_cbranch_vccnz .Lslow%=\n\t" \
  ".Lok%=:\n\t" \
  "ds_read_b128 v[216:219], v250 offset:32768\n\t" \
  "ds_read_b128 v[220:223], v250 offset:33792\n\t" \
  "ds_read_b128 v[224:227], v250 offset:34816\n\t" \
  "ds_read_b128 v[228:231], v250 offset:35840\n\t" \
  "s_waitcnt lgkmcnt(4)\n\t" \
  "s_barrier\n\t" \
  "ds_read_b128 v[208:211], v248 offset:5120\n\t" \
  "ds_read_b128 v[212:215], v248 offset:6144\n\t" \
  "ds_read_b128 v[236:239], v248 offset:7168\n\t" \
  "ds_read_b128 v[240:243], v248 offset:8192\n\t" \
  "s_waitcnt lgkmcnt(3)\n\t" \
  "ds_read_b128 v[244:247], v248 offset:13312\n\t" \
  "v_smfmac_f32_16x16x64_bf16 v[192:195], v[208:211], v[216:223], v251\n\t" \
  "ds_read_b128 v[216:219], v250 offset:0\n\t" \
  "v_smfmac_f32_16x16x64_bf16 v[196:199], v[208:211], v[224:231], v251\n\t" \
  "s_waitcnt lgkmcnt(4)\n\t" \
  "ds_read_b128 v[220:223], v250 offset:1024\n\t" \
  "v_smfmac_f32_16x16x64_bf16 v[192:195], v[212:215], v[0:7], v251\n\t" \
  "ds_read_b128 v[224:227], v250 offset:2048\n\t" \
  "v_smfmac_f32_16x16x64_bf16 v[196:199], v[212:215], v[8:15], v251\n\t" \
  "s_waitcnt lgkmcnt(5)\n\t" \
  "ds_read_b128 v[228:231], v250 offset:3072\n\t" \
  "v_smfmac_f32_16x16x64_bf16 v[192:195], v[236:239], v[16:23], v251\n\t" \
  "ds_read_b128 v[200:203], v248 offset:9216\n\t" \
  "v_smfmac_f32_16x16x64_bf16 v[196:199], v[236:239], v[24:31], v251\n\t" \
  "s_waitcnt lgkmcnt(6)\n\t" \
  "ds_read_b128 v[204:207], v248 offset:10240\n\t" \
  "v_smfmac_f32_16x16x64_bf16 v[192:195], v[240:243], v[32:39], v251\n\t" \
  "ds_read_b128 v[208:211], v248 offset:11264\n\t" \
  "v_smfmac_f32_16x16x64_bf16 v[196:199], v[240:243], v[40:47], v251\n\t" \
  "s_waitcnt lgkmcnt(5)\n\t" \
  "ds_read_b128 v[212:215], v248 offset:14336\n\t" \
  "v_smfmac_f32_16x16x64_bf16 v[192:195], v[244:247], v[216:223], v251\n\t" \
  "s_waitcnt lgkmcnt(4)\n\t" \
  "v_smfmac_f32_16x16x64_bf16 v[196:199], v[244:247], v[224:231], v251\n\t" \
  "s_waitcnt lgkmcnt(3)\n\t" \
  "ds_read_b128 v[236:239], v248 offset:15360\n\t" \
  "v_smfmac_f32_16x16x64_bf16 v[192:195], v[200:203], v[48:55], v251\n\t" \
  "ds_read_b128 v[240:243], v248 offset:0\n\t" \
  "v_smfmac_f32_16x16x64_bf16 v[196:199], v[200:203], v[56:63], v251\n\t" \
  "s_waitcnt lgkmcnt(4)\n\t" \
  "v_smfmac_f32_16x16x64_bf16 v[192:195], v[204:207], v[64:71], v251\n\t" \
  "ds_read_b128 v[244:247], v248 offset:1024\n\t" \
  "v_smfmac_f32_16x16x64_bf16 v[196:199], v[204:207], v[72:79], v251\n\t" \
  "s_waitcnt lgkmcnt(4)\n\t" \
  "v_smfmac_f32_16x16x64_bf16 v[192:195], v[208:211], v[80:87], v251\n\t" \
  "ds_read_b128 v[200:203], v248 offset:2048\n\t" \
  "v_smfmac_f32_16x16x64_bf16 v[196:199], v[208:211], v[88:95], v251\n\t" \
  "s_waitcnt lgkmcnt(4)\n\t" \
  "v_smfmac_f32_16x16x64_bf16 v[192:195], v[212:215], v[96:103], v251\n\t" \
  "ds_read_b128 v[204:207], v248 offset:3072\n\t" \
  "v_smfmac_f32_16x16x64_bf16 v[196:199], v[212:215], v[104:111], v251\n\t" \
  "s_waitcnt lgkmcnt(3)\n\t" \
  "v_smfmac_f32_16x16x64_bf16 v[192:195], v[236:239], v[112:119], v251\n\t" \
  "v_smfmac_f32_16x16x64_bf16 v[196:199], v[236:239], v[120:127], v251\n\t" \
  "v_smfmac_f32_16x16x64_bf16 v[192:195], v[240:243], v[128:135], v251\n\t" \
  "v_smfmac_f32_16x16x64_bf16 v[196:199], v[240:243], v[136:143], v251\n\t" \
  "s_waitcnt lgkmcnt(1)\n\t" \
  "v_smfmac_f32_16x16x64_bf16 v[192:195], v[244:247], v[144:151], v251\n\t" \
  "v_smfmac_f32_16x16x64_bf16 v[196:199], v[244:247], v[152:159], v251\n\t" \
  "v_smfmac_f32_16x16x64_bf16 v[192:195], v[200:203], v[160:167], v251\n\t" \
  "v_smfmac_f32_16x16x64_bf16 v[196:199], v[200:203], v[168:175], v251\n\t" \
  "s_waitcnt lgkmcnt(0)\n\t" \
  "v_smfmac_f32_16x16x64_bf16 v[192:195], v[204:207], v[176:183], v251\n\t" \
  "v_smfmac_f32_16x16x64_bf16 v[196:199], v[204:207], v[184:191], v251\n\t" \
  "s_nop 6\n\t" \
  "v_add_f32 v192, v192, v194\n\t" \
  "v_add_f32 v193, v193, v195\n\t" \
  "v_add_f32 v196, v196, v198\n\t" \
  "v_add_f32 v197, v197, v199\n\t" \
  "v_cvt_pk_bf16_f32 v192, v192, v193\n\t" \
  "v_cvt_pk_bf16_f32 v196, v196, v197\n\t" \
  "ds_write2_b32 v252, v192, v196 offset1:64\n\t" \
  "s_waitcnt lgkmcnt(0)\n\t" \
  "s_barrier\n\t" \
  "buffer_store_dword v192, v235, %[rs], %[sk] offen sc1\n\t" \
  "buffer_store_dword v196, v235, %[rs], %[sk] offen offset:256 sc1\n\t" \
  "s_mov_b32 %[spins], s92\n\t" \
  "s_branch .Lend%=\n\t" \
  ".Lslow%=:\n\t" \
  "s_mov_b64 s[94:95], 0\n\t" \
  "v_cmp_eq_u32 vcc, %[sent], v200\n\t" \
  "s_or_b64 s[94:95], s[94:95], vcc\n\t" \
  "v_cmp_eq_u32 vcc, %[sent], v201\n\t" \
  "s_or_b64 s[94:95], s[94:95], vcc\n\t" \
  "v_cmp_eq_u32 vcc, %[sent], v202\n\t" \
  "s_or_b64 s[94:95], s[94:95], vcc\n\t" \
  "v_cmp_eq_u32 vcc, %[sent], v203\n\t" \
  "s_or_b64 s[94:95], s[94:95], vcc\n\t" \
  "v_cmp_eq_u32 vcc, %[sent], v204\n\t" \
  "s_or_b64 s[94:95], s[94:95], vcc\n\t" \
  "v_cmp_eq_u32 vcc, %[sent], v205\n\t" \
  "s_or_b64 s[94:95], s[94:95], vcc\n\t" \
  "v_cmp_eq_u32 vcc, %[sent], v206\n\t" \
  "s_or_b64 s[94:95], s[94:95], vcc\n\t" \
  "v_cmp_eq_u32 vcc, %[sent], v207\n\t" \
  "s_or_b64 s[94:95], s[94:95], vcc\n\t" \
  "s_cmp_eq_u64 s[94:95], 0\n\t" \
  "s_cbranch_scc1 .Lok%=\n\t" \
  "v_mov_b64 v[192:193], 0\n\t" \
  "v_mov_b64 v[194:195], 0\n\t" \
  "v_mov_b64 v[196:197], 0\n\t" \
  "v_mov_b64 v[198:199], 0\n\t" \
  "s_add_u32 s92, s92, 1\n\t" \
  "s_sleep 6\n\t" \
  "s_cmp_eq_u32 s92, 1\n\t" \
  "s_cbranch_scc1 .Lpoll%=\n\t" \
  "s_sleep 1\n\t" \
  "s_and_b32 s93, s92, 63\n\t" \
  "s_cmp_lg_u32 s93, 0\n\t" \
  "s_cbranch_scc1 .Lpoll%=\n\t" \
  "v_mov_b32 v245, 0\n\t" \
  "buffer_load_dword v244, v245, %[rs], %[sab] offen sc1\n\t" \
  "s_waitcnt vmcnt(0)\n\t" \
  "v_readfirstlane_b32 s93, v244\n\t" \
  "s_nop 3\n\t" \
  "s_cmp_eq_u32 s93, 1\n\t" \
  "s_cbranch_scc1 .Lok%=\n\t" \
  "s_cmp_lt_u32 s92, 0x80000\n\t" \
  "s_cbranch_scc1 .Lpoll%=\n\t" \
  "v_mov_b32 v244, 1\n\t" \
  "buffer_store_dword v244, v245, %[rs], %[sab] offen sc1\n\t" \
  "s_branch .Lok%=\n\t" \
  ".Lend%=:\n\t"
#define QK_ASM_5 \
  "s_waitcnt lgkmcnt(0)\n\t" \
  "v_subrev_u32 v244, 0x8000, v250\n\t" \
  "ds_read_b128 v[216:219], v248 offset:53248\n\t" \
  "ds_read_b128 v[220:223], v248 offset:54272\n\t" \
  "ds_read_b128 v[224:227], v248 offset:55296\n\t" \
  "ds_read_b128 v[228:231], v248 offset:56320\n\t" \
  "ds_read_b128 v[208:211], v244 offset:0\n\t" \
  "ds_read_b128 v[212:215], v244 offset:1024\n\t" \
  "ds_read_b128 v[236:239], v244 offset:2048\n\t" \
  "ds_read_b128 v[240:243], v244 offset:3072\n\t" \
  "v_mov_b64 v[192:193], 0\n\t" \
  "v_mov_b64 v[194:195], 0\n\t" \
  "v_mov_b64 v[196:197], 0\n\t" \
  "v_mov_b64 v[198:199], 0\n\t" \
  "s_movk_i32 s92, 0x2000\n\t" \
  ".Lwt%=:\n\t" \
  "ds_read_b64 v[200:201], v248 offset:30720\n\t" \
  "s_memrealtime s[90:91]\n\t" \
  "s_sub_u32 s92, s92, 1\n\t" \
  "s_waitcnt lgkmcnt(0)\n\t" \
  "v_readfirstlane_b32 s93, v200\n\t" \
  "v_readfirstlane_b32 %[tgt], v201\n\t" \
  "s_cbranch_scc1 .Lgo%=\n\t" \
  "s_cmp_eq_u32 s93, %[step]\n\t" \
  "s_cbranch_scc1 .Lwx%=\n\t" \
  "s_sleep 1\n\t" \
  "s_branch .Lwt%=\n\t" \
  ".Lwx%=:\n\t" \
  "s_lshl_b32 s93, s90, 2\n\t" \
  "s_sub_i32 s93, %[tgt], s93\n\t" \
  "s_cmp_le_i32 s93, 0\n\t" \
  "s_cbranch_scc1 .Lgo%=\n\t" \
  "s_min_i32 s93, s93, 8000\n\t" \
  "s_mov_b32 s94, s93\n\t" \
  ".Lsl%=:\n\t" \
  "s_sub_i32 s94, s94, 18\n\t" \
  "s_cmp_lt_i32 s94, 30\n\t" \
  "s_cbranch_scc1 .Lsx%=\n\t" \
  "s_sleep 1\n\t" \
  "s_branch .Lsl%=\n\t" \
  ".Lsx%=:\n\t" \
  "s_sub_i32 s93, s93, 12\n\t" \
  "s_max_i32 s93, s93, 4\n\t" \
  "s_lshr_b32 s93, s93, 2\n\t" \
  "s_add_u32 s93, s93, s90\n\t" \
  "s_memrealtime s[90:91]\n\t" \
  ".Lpace%=:\n\t" \
  "s_memrealtime s[94:95]\n\t" \
  "s_waitcnt lgkmcnt(1)\n\t" \
  "s_sub_i32 s92, s90, s93\n\t" \
  "s_cmp_ge_i32 s92, 0\n\t" \
  "s_cbranch_scc1 .Le%=\n\t" \
  "s_memrealtime s[90:91]\n\t" \
  "s_waitcnt lgkmcnt(1)\n\t" \
  "s_sub_i32 s92, s94, s93\n\t" \
  "s_cmp_lt_i32 s92, 0\n\t" \
  "s_cbranch_scc1 .Lpace%=\n\t" \
  "s_nop 4\n\t" \
  ".Le%=:\n\t" \
  "s_add_u32 s92, s92, s93\n\t" \
  "s_lshl_b32 s92, s92, 2\n\t" \
  "s_sub_i32 s92, %[tgt], s92\n\t" \
  "s_max_i32 s92, s92, 0\n\t" \
  "s_bitcmp0_b32 s92, 3\n\t" \
  "s_cbranch_scc1 .Lf3%=\n\t" \
  "s_nop 15\n\t" \
  ".Lf3%=:\n\t" \
  "s_bitcmp0_b32 s92, 2\n\t" \
  "s_cbranch_scc1 .Lf2%=\n\t" \
  "s_nop 10\n\t" \
  ".Lf2%=:\n\t" \
  ".Lgo%=:\n\t" \
  "s_mov_b32 s92, 0\n\t" \
  ".Lpoll%=:\n\t" \
  "buffer_load_dwordx4 v[200:203], v248, %[rs], %[som] offen sc1\n\t" \
  "buffer_load_dwordx4 v[204:207], v248, %[rs], %[som2] offen sc1\n\t" \
  "s_waitcnt vmcnt(1) lgkmcnt(0)\n\t" \
  "ds_write_b128 v249, v[200:203]\n\t" \
  "v_smfmac_f32_16x16x64_bf16 v[192:195], v[200:203], v[208:215], v251\n\t" \
  "v_max3_u32 v244, v200, v201, v202\n\t" \
  "v_smfmac_f32_16x16x64_bf16 v[196:199], v[200:203], v[236:243], v251\n\t" \
  "s_waitcnt vmcnt(0)\n\t" \
  "ds_write_b128 v249, v[204:207] offset:8192\n\t" \
  "v_smfmac_f32_16x16x64_bf16 v[192:195], v[204:207], v[216:223], v251\n\t" \
  "v_max3_u32 v244, v244, v203, v204\n\t" \
  "v_max3_u32 v244, v244, v205, v206\n\t" \
  "v_smfmac_f32_16x16x64_bf16 v[196:199], v[204:207], v[224:231], v251\n\t" \
  "v_max_u32 v244, v244, v207\n\t" \
  "v_cmp_le_u32 vcc, %[sent], v244\n\t" \
  "s_cbranch_vccnz .Lslow%=\n\t" \
  ".Lok%=:\n\t" \
  "ds_read_b128 v[216:219], v250 offset:32768\n\t" \
  "ds_read_b128 v[220:223], v250 offset:33792\n\t" \
  "ds_read_b128 v[224:227], v250 offset:34816\n\t" \
  "ds_read_b128 v[228:231], v250 offset:35840\n\t" \
  "s_waitcnt lgkmcnt(4)\n\t" \
  "s_barrier\n\t" \
  "ds_read_b128 v[208:211], v248 offset:4096\n\t" \
  "ds_read_b128 v[212:215], v248 offset:7168\n\t" \
  "ds_read_b128 v[236:239], v248 offset:8192\n\t" \
  "ds_read_b128 v[240:243], v248 offset:9216\n\t" \
  "s_waitcnt lgkmcnt(3)\n\t" \
  "ds_read_b128 v[244:247], v248 offset:12288\n\t" \
  "v_smfmac_f32_16x16x64_bf16 v[192:195], v[208:211], v[216:223], v251\n\t" \
  "ds_read_b128 v[216:219], v250 offset:0\n\t" \
  "v_smfmac_f32_16x16x64_bf16 v[196:199], v[208:211], v[224:231], v251\n\t" \
  "s_waitcnt lgkmcnt(4)\n\t" \
  "ds_read_b128 v[220:223], v250 offset:1024\n\t" \
  "v_smfmac_f32_16x16x64_bf16 v[192:195], v[212:215], v[0:7], v251\n\t" \
  "ds_read_b128 v[224:227], v250 offset:2048\n\t" \
  "v_smfmac_f32_16x16x64_bf16 v[196:199], v[212:215], v[8:15], v251\n\t" \
  "s_waitcnt lgkmcnt(5)\n\t" \
  "ds_read_b128 v[228:231], v250 offset:3072\n\t" \
  "v_smfmac_f32_16x16x64_bf16 v[192:195], v[236:239], v[16:23], v251\n\t" \
  "ds_read_b128 v[200:203], v248 offset:10240\n\t" \
  "v_smfmac_f32_16x16x64_bf16 v[196:199], v[236:239], v[24:31], v251\n\t" \
  "s_waitcnt lgkmcnt(6)\n\t" \
  "ds_read_b128 v[204:207], v248 offset:11264\n\t" \
  "v_smfmac_f32_16x16x64_bf16 v[192:195], v[240:243], v[32:39], v251\n\t" \
  "ds_read_b128 v[208:211], v248 offset:14336\n\t" \
  "v_smfmac_f32_16x16x64_bf16 v[196:199], v[240:243], v[40:47], v251\n\t" \
  "s_waitcnt lgkmcnt(5)\n\t" \
  "ds_read_b128 v[212:215], v248 offset:15360\n\t" \
  "v_smfmac_f32_16x16x64_bf16 v[192:195], v[244:247], v[216:223], v251\n\t" \
  "s_waitcnt lgkmcnt(4)\n\t" \
  "v_smfmac_f32_16x16x64_bf16 v[196:199], v[244:247], v[224:231], v251\n\t" \
  "s_waitcnt lgkmcnt(3)\n\t" \
  "ds_read_b128 v[236:239], v248 offset:0\n\t" \
  "v_smfmac_f32_16x16x64_bf16 v[192:195], v[200:203], v[48:55], v251\n\t" \
  "ds_read_b128 v[240:243], v248 offset:1024\n\t" \
  "v_smfmac_f32_16x16x64_bf16 v[196:199], v[200:203], v[56:63], v251\n\t" \
  "s_waitcnt lgkmcnt(4)\n\t" \
  "v_smfmac_f32_16x16x64_bf16 v[192:195], v[204:207], v[64:71], v251\n\t" \
  "ds_read_b128 v[244:247], v248 offset:2048\n\t" \
  "v_smfmac_f32_16x16x64_bf16 v[196:199], v[204:207], v[72:79], v251\n\t" \
  "s_waitcnt lgkmcnt(4)\n\t" \
  "v_smfmac_f32_16x16x64_bf16 v[192:195], v[208:211], v[80:87], v251\n\t" \
  "ds_read_b128 v[200:203], v248 offset:3072\n\t" \
  "v_smfmac_f32_16x16x64_bf16 v[196:199], v[208:211], v[88:95], v251\n\t" \
  "s_waitcnt lgkmcnt(4)\n\t" \
  "v_smfmac_f32_16x16x64_bf16 v[192:195], v[212:215], v[96:103], v251\n\t" \
  "ds_read_b128 v[204:207], v248 offset:6144\n\t" \
  "v_smfmac_f32_16x16x64_bf16 v[196:199], v[212:215], v[104:111], v251\n\t" \
  "s_waitcnt lgkmcnt(3)\n\t" \
  "v_smfmac_f32_16x16x64_bf16 v[192:195], v[236:239], v[112:119], v251\n\t" \
  "v_smfmac_f32_16x16x64_bf16 v[196:199], v[236:239], v[120:127], v251\n\t" \
  "v_smfmac_f32_16x16x64_bf16 v[192:195], v[240:243], v[128:135], v251\n\t" \
  "v_smfmac_f32_16x16x64_bf16 v[196:199], v[240:243], v[136:143], v251\n\t" \
  "s_waitcnt lgkmcnt(1)\n\t" \
  "v_smfmac_f32_16x16x64_bf16 v[192:195], v[244:247], v[144:151], v251\n\t" \
  "v_smfmac_f32_16x16x64_bf16 v[196:199], v[244:247], v[152:159], v251\n\t" \
  "v_smfmac_f32_16x16x64_bf16 v[192:195], v[200:203], v[160:167], v251\n\t" \
  "v_smfmac_f32_16x16x64_bf16 v[196:199], v[200:203], v[168:175], v251\n\t" \
  "s_waitcnt lgkmcnt(0)\n\t" \
  "v_smfmac_f32_16x16x64_bf16 v[192:195], v[204:207], v[176:183], v251\n\t" \
  "v_smfmac_f32_16x16x64_bf16 v[196:199], v[204:207], v[184:191], v251\n\t" \
  "s_nop 6\n\t" \
  "v_add_f32 v192, v192, v194\n\t" \
  "v_add_f32 v193, v193, v195\n\t" \
  "v_add_f32 v196, v196, v198\n\t" \
  "v_add_f32 v197, v197, v199\n\t" \
  "v_cvt_pk_bf16_f32 v192, v192, v193\n\t" \
  "v_cvt_pk_bf16_f32 v196, v196, v197\n\t" \
  "ds_write2_b32 v252, v192, v196 offset1:64\n\t" \
  "s_waitcnt lgkmcnt(0)\n\t" \
  "s_barrier\n\t" \
  "buffer_store_dword v192, v235, %[rs], %[sk] offen sc1\n\t" \
  "buffer_store_dword v196, v235, %[rs], %[sk] offen offset:256 sc1\n\t" \
  "s_mov_b32 %[spins], s92\n\t" \
  "s_branch .Lend%=\n\t" \
  ".Lslow%=:\n\t" \
  "s_mov_b64 s[94:95], 0\n\t" \
  "v_cmp_eq_u32 vcc, %[sent], v200\n\t" \
  "s_or_b64 s[94:95], s[94:95], vcc\n\t" \
  "v_cmp_eq_u32 vcc, %[sent], v201\n\t" \
  "s_or_b64 s[94:95], s[94:95], vcc\n\t" \
  "v_cmp_eq_u32 vcc, %[sent], v202\n\t" \
  "s_or_b64 s[94:95], s[94:95], vcc\n\t" \
  "v_cmp_eq_u32 vcc, %[sent], v203\n\t" \
  "s_or_b64 s[94:95], s[94:95], vcc\n\t" \
  "v_cmp_eq_u32 vcc, %[sent], v204\n\t" \
  "s_or_b64 s[94:95], s[94:95], vcc\n\t" \
  "v_cmp_eq_u32 vcc, %[sent], v205\n\t" \
  "s_or_b64 s[94:95], s[94:95], vcc\n\t" \
  "v_cmp_eq_u32 vcc, %[sent], v206\n\t" \
  "s_or_b64 s[94:95], s[94:95], vcc\n\t" \
  "v_cmp_eq_u32 vcc, %[sent], v207\n\t" \
  "s_or_b64 s[94:95], s[94:95], vcc\n\t" \
  "s_cmp_eq_u64 s[94:95], 0\n\t" \
  "s_cbranch_scc1 .Lok%=\n\t" \
  "v_mov_b64 v[192:193], 0\n\t" \
  "v_mov_b64 v[194:195], 0\n\t" \
  "v_mov_b64 v[196:197], 0\n\t" \
  "v_mov_b64 v[198:199], 0\n\t" \
  "s_add_u32 s92, s92, 1\n\t" \
  "s_sleep 6\n\t" \
  "s_cmp_eq_u32 s92, 1\n\t" \
  "s_cbranch_scc1 .Lpoll%=\n\t" \
  "s_sleep 1\n\t" \
  "s_and_b32 s93, s92, 63\n\t" \
  "s_cmp_lg_u32 s93, 0\n\t" \
  "s_cbranch_scc1 .Lpoll%=\n\t" \
  "v_mov_b32 v245, 0\n\t" \
  "buffer_load_dword v244, v245, %[rs], %[sab] offen sc1\n\t" \
  "s_waitcnt vmcnt(0)\n\t" \
  "v_readfirstlane_b32 s93, v244\n\t" \
  "s_nop 3\n\t" \
  "s_cmp_eq_u32 s93, 1\n\t" \
  "s_cbranch_scc1 .Lok%=\n\t" \
  "s_cmp_lt_u32 s92, 0x80000\n\t" \
  "s_cbranch_scc1 .Lpoll%=\n\t" \
  "v_mov_b32 v244, 1\n\t" \
  "buffer_store_dword v244, v245, %[rs], %[sab] offen sc1\n\t" \
  "s_branch .Lok%=\n\t" \
  ".Lend%=:\n\t"
#define QK_ASM_6 \
  "s_waitcnt lgkmcnt(0)\n\t" \
  "v_subrev_u32 v244, 0x8000, v250\n\t" \
  "ds_read_b128 v[216:219], v248 offset:57344\n\t" \
  "ds_read_b128 v[220:223], v248 offset:58368\n\t" \
  "ds_read_b128 v[224:227], v248 offset:59392\n\t" \
  "ds_read_b128 v[228:231], v248 offset:60416\n\t" \
  "ds_read_b128 v[208:211], v244 offset:0\n\t" \
  "ds_read_b128 v[212:215], v244 offset:1024\n\t" \
  "ds_read_b128 v[236:239], v244 offset:2048\n\t" \
  "ds_read_b128 v[240:243], v244 offset:3072\n\t" \
  "v_mov_b64 v[192:193], 0\n\t" \
  "v_mov_b64 v[194:195], 0\n\t" \
  "v_mov_b64 v[196:197], 0\n\t" \
  "v_mov_b64 v[198:199], 0\n\t" \
  "s_movk_i32 s92, 0x2000\n\t" \
  ".Lwt%=:\n\t" \
  "ds_read_b64 v[200:201], v248 offset:30720\n\t" \
  "s_memrealtime s[90:91]\n\t" \
  "s_sub_u32 s92, s92, 1\n\t" \
  "s_waitcnt lgkmcnt(0)\n\t" \
  "v_readfirstlane_b32 s93, v200\n\t" \
  "v_readfirstlane_b32 %[tgt], v201\n\t" \
  "s_cbranch_scc1 .Lgo%=\n\t" \
  "s_cmp_eq_u32 s93, %[step]\n\t" \
  "s_cbranch_scc1 .Lwx%=\n\t" \
  "s_sleep 1\n\t" \
  "s_branch .Lwt%=\n\t" \
  ".Lwx%=:\n\t" \
  "s_lshl_b32 s93, s90, 2\n\t" \
  "s_sub_i32 s93, %[tgt], s93\n\t" \
  "s_cmp_le_i32 s93, 0\n\t" \
  "s_cbranch_scc1 .Lgo%=\n\t" \
  "s_min_i32 s93, s93, 8000\n\t" \
  "s_mov_b32 s94, s93\n\t" \
  ".Lsl%=:\n\t" \
  "s_sub_i32 s94, s94, 18\n\t" \
  "s_cmp_lt_i32 s94, 30\n\t" \
  "s_cbranch_scc1 .Lsx%=\n\t" \
  "s_sleep 1\n\t" \
  "s_branch .Lsl%=\n\t" \
  ".Lsx%=:\n\t" \
  "s_sub_i32 s93, s93, 12\n\t" \
  "s_max_i32 s93, s93, 4\n\t" \
  "s_lshr_b32 s93, s93, 2\n\t" \
  "s_add_u32 s93, s93, s90\n\t" \
  "s_memrealtime s[90:91]\n\t" \
  ".Lpace%=:\n\t" \
  "s_memrealtime s[94:95]\n\t" \
  "s_waitcnt lgkmcnt(1)\n\t" \
  "s_sub_i32 s92, s90, s93\n\t" \
  "s_cmp_ge_i32 s92, 0\n\t" \
  "s_cbranch_scc1 .Le%=\n\t" \
  "s_memrealtime s[90:91]\n\t" \
  "s_waitcnt lgkmcnt(1)\n\t" \
  "s_sub_i32 s92, s94, s93\n\t" \
  "s_cmp_lt_i32 s92, 0\n\t" \
  "s_cbranch_scc1 .Lpace%=\n\t" \
  "s_nop 4\n\t" \
  ".Le%=:\n\t" \
  "s_add_u32 s92, s92, s93\n\t" \
  "s_lshl_b32 s92, s92, 2\n\t" \
  "s_sub_i32 s92, %[tgt], s92\n\t" \
  "s_max_i32 s92, s92, 0\n\t" \
  "s_bitcmp0_b32 s92, 3\n\t" \
  "s_cbranch_scc1 .Lf3%=\n\t" \
  "s_nop 15\n\t" \
  ".Lf3%=:\n\t" \
  "s_bitcmp0_b32 s92, 2\n\t" \
  "s_cbranch_scc1 .Lf2%=\n\t" \
  "s_nop 10\n\t" \
  ".Lf2%=:\n\t" \
  ".Lgo%=:\n\t" \
  "s_mov_b32 s92, 0\n\t" \
  ".Lpoll%=:\n\t" \
  "buffer_load_dwordx4 v[200:203], v248, %[rs], %[som] offen sc1\n\t" \
  "buffer_load_dwordx4 v[204:207], v248, %[rs], %[som2] offen sc1\n\t" \
  "s_waitcnt vmcnt(1) lgkmcnt(0)\n\t" \
  "ds_write_b128 v249, v[200:203]\n\t" \
  "v_smfmac_f32_16x16x64_bf16 v[192:195], v[200:203], v[208:215], v251\n\t" \
  "v_max3_u32 v244, v200, v201, v202\n\t" \
  "v_smfmac_f32_16x16x64_bf16 v[196:199], v[200:203], v[236:243], v251\n\t" \
  "s_waitcnt vmcnt(0)\n\t" \
  "ds_write_b128 v249, v[204:207] offset:8192\n\t" \
  "v_smfmac_f32_16x16x64_bf16 v[192:195], v[204:207], v[216:223], v251\n\t" \
  "v_max3_u32 v244, v244, v203, v204\n\t" \
  "v_max3_u32 v244, v244, v205, v206\n\t" \
  "v_smfmac_f32_16x16x64_bf16 v[196:199], v[204:207], v[224:231], v251\n\t" \
  "v_max_u32 v244, v244, v207\n\t" \
  "v_cmp_le_u32 vcc, %[sent], v244\n\t" \
  "s_cbranch_vccnz .Lslow%=\n\t" \
  ".Lok%=:\n\t" \
  "ds_read_b128 v[216:219], v250 offset:32768\n\t" \
  "ds_read_b128 v[220:223], v250 offset:33792\n\t" \
  "ds_read_b128 v[224:227], v250 offset:34816\n\t" \
  "ds_read_b128 v[228:231], v250 offset:35840\n\t" \
  "s_waitcnt lgkmcnt(4)\n\t" \
  "s_barrier\n\t" \
  "ds_read_b128 v[208:211], v248 offset:7168\n\t" \
  "ds_read_b128 v[212:215], v248 offset:8192\n\t" \
  "ds_read_b128 v[236:239], v248 offset:9216\n\t" \
  "ds_read_b128 v[240:243], v248 offset:10240\n\t" \
  "s_waitcnt lgkmcnt(3)\n\t" \
  "ds_read_b128 v[244:247], v248 offset:13312\n\t" \
  "v_smfmac_f32_16x16x64_bf16 v[192:195], v[208:211], v[216:223], v251\n\t" \
  "ds_read_b128 v[216:219], v250 offset:0\n\t" \
  "v_smfmac_f32_16x16x64_bf16 v[196:199], v[208:211], v[224:231], v251\n\t" \
  "s_waitcnt lgkmcnt(4)\n\t" \
  "ds_read_b128 v[220:223], v250 offset:1024\n\t" \
  "v_smfmac_f32_16x16x64_bf16 v[192:195], v[212:215], v[0:7], v251\n\t" \
  "ds_read_b128 v[224:227], v250 offset:2048\n\t" \
  "v_smfmac_f32_16x16x64_bf16 v[196:199], v[212:215], v[8:15], v251\n\t" \
  "s_waitcnt lgkmcnt(5)\n\t" \
  "ds_read_b128 v[228:231], v250 offset:3072\n\t" \
  "v_smfmac_f32_16x16x64_bf16 v[192:195], v[236:239], v[16:23], v251\n\t" \
  "ds_read_b128 v[200:203], v248 offset:11264\n\t" \
  "v_smfmac_f32_16x16x64_bf16 v[196:199], v[236:239], v[24:31], v251\n\t" \
  "s_waitcnt lgkmcnt(6)\n\t" \
  "ds_read_b128 v[204:207], v248 offset:12288\n\t" \
  "v_smfmac_f32_16x16x64_bf16 v[192:195], v[240:243], v[32:39], v251\n\t" \
  "ds_read_b128 v[208:211], v248 offset:15360\n\t" \
  "v_smfmac_f32_16x16x64_bf16 v[196:199], v[240:243], v[40:47], v251\n\t" \
  "s_waitcnt lgkmcnt(5)\n\t" \
  "ds_read_b128 v[212:215], v248 offset:0\n\t" \
  "v_smfmac_f32_16x16x64_bf16 v[192:195], v[244:247], v[216:223], v251\n\t" \
  "s_waitcnt lgkmcnt(4)\n\t" \
  "v_smfmac_f32_16x16x64_bf16 v[196:199], v[244:247], v[224:231], v251\n\t" \
  "s_waitcnt lgkmcnt(3)\n\t" \
  "ds_read_b128 v[236:239], v248 offset:1024\n\t" \
  "v_smfmac_f32_16x16x64_bf16 v[192:195], v[200:203], v[48:55], v251\n\t" \
  "ds_read_b128 v[240:243], v248 offset:2048\n\t" \
  "v_smfmac_f32_16x16x64_bf16 v[196:199], v[200:203], v[56:63], v251\n\t" \
  "s_waitcnt lgkmcnt(4)\n\t" \
  "v_smfmac_f32_16x16x64_bf16 v[192:195], v[204:207], v[64:71], v251\n\t" \
  "ds_read_b128 v[244:247], v248 offset:3072\n\t" \
  "v_smfmac_f32_16x16x64_bf16 v[196:199], v[204:207], v[72:79], v251\n\t" \
  "s_waitcnt lgkmcnt(4)\n\t" \
  "v_smfmac_f32_16x16x64_bf16 v[192:195], v[208:211], v[80:87], v251\n\t" \
  "ds_read_b128 v[200:203], v248 offset:4096\n\t" \
  "v_smfmac_f32_16x16x64_bf16 v[196:199], v[208:211], v[88:95], v251\n\t" \
  "s_waitcnt lgkmcnt(4)\n\t" \
  "v_smfmac_f32_16x16x64_bf16 v[192:195], v[212:215], v[96:103], v251\n\t" \
  "ds_read_b128 v[204:207], v248 offset:5120\n\t" \
  "v_smfmac_f32_16x16x64_bf16 v[196:199], v[212:215], v[104:111], v251\n\t" \
  "s_waitcnt lgkmcnt(3)\n\t" \
  "v_smfmac_f32_16x16x64_bf16 v[192:195], v[236:239], v[112:119], v251\n\t" \
  "v_smfmac_f32_16x16x64_bf16 v[196:199], v[236:239], v[120:127], v251\n\t" \
  "v_smfmac_f32_16x16x64_bf16 v[192:195], v[240:243], v[128:135], v251\n\t" \
  "v_smfmac_f32_16x16x64_bf16 v[196:199], v[240:243], v[136:143], v251\n\t" \
  "s_waitcnt lgkmcnt(1)\n\t" \
  "v_smfmac_f32_16x16x64_bf16 v[192:195], v[244:247], v[144:151], v251\n\t" \
  "v_smfmac_f32_16x16x64_bf16 v[196:199], v[244:247], v[152:159], v251\n\t" \
  "v_smfmac_f32_16x16x64_bf16 v[192:195], v[200:203], v[160:167], v251\n\t" \
  "v_smfmac_f32_16x16x64_bf16 v[196:199], v[200:203], v[168:175], v251\n\t" \
  "s_waitcnt lgkmcnt(0)\n\t" \
  "v_smfmac_f32_16x16x64_bf16 v[192:195], v[204:207], v[176:183], v251\n\t" \
  "v_smfmac_f32_16x16x64_bf16 v[196:199], v[204:207], v[184:191], v251\n\t" \
  "s_nop 6\n\t" \
  "v_add_f32 v192, v192, v194\n\t" \
  "v_add_f32 v193, v193, v195\n\t" \
  "v_add_f32 v196, v196, v198\n\t" \
  "v_add_f32 v197, v197, v199\n\t" \
  "v_cvt_pk_bf16_f32 v192, v192, v193\n\t" \
  "v_cvt_pk_bf16_f32 v196, v196, v197\n\t" \
  "ds_write2_b32 v252, v192, v196 offset1:64\n\t" \
  "s_waitcnt lgkmcnt(0)\n\t" \
  "s_barrier\n\t" \
  "buffer_store_dword v192, v235, %[rs], %[sk] offen sc1\n\t" \
  "buffer_store_dword v196, v235, %[rs], %[sk] offen offset:256 sc1\n\t" \
  "s_mov_b32 %[spins], s92\n\t" \
  "s_branch .Lend%=\n\t" \
  ".Lslow%=:\n\t" \
  "s_mov_b64 s[94:95], 0\n\t" \
  "v_cmp_eq_u32 vcc, %[sent], v200\n\t" \
  "s_or_b64 s[94:95], s[94:95], vcc\n\t" \
  "v_cmp_eq_u32 vcc, %[sent], v201\n\t" \
  "s_or_b64 s[94:95], s[94:95], vcc\n\t" \
  "v_cmp_eq_u32 vcc, %[sent], v202\n\t" \
  "s_or_b64 s[94:95], s[94:95], vcc\n\t" \
  "v_cmp_eq_u32 vcc, %[sent], v203\n\t" \
  "s_or_b64 s[94:95], s[94:95], vcc\n\t" \
  "v_cmp_eq_u32 vcc, %[sent], v204\n\t" \
  "s_or_b64 s[94:95], s[94:95], vcc\n\t" \
  "v_cmp_eq_u32 vcc, %[sent], v205\n\t" \
  "s_or_b64 s[94:95], s[94:95], vcc\n\t" \
  "v_cmp_eq_u32 vcc, %[sent], v206\n\t" \
  "s_or_b64 s[94:95], s[94:95], vcc\n\t" \
  "v_cmp_eq_u32 vcc, %[sent], v207\n\t" \
  "s_or_b64 s[94:95], s[94:95], vcc\n\t" \
  "s_cmp_eq_u64 s[94:95], 0\n\t" \
  "s_cbranch_scc1 .Lok%=\n\t" \
  "v_mov_b64 v[192:193], 0\n\t" \
  "v_mov_b64 v[194:195], 0\n\t" \
  "v_mov_b64 v[196:197], 0\n\t" \
  "v_mov_b64 v[198:199], 0\n\t" \
  "s_add_u32 s92, s92, 1\n\t" \
  "s_sleep 6\n\t" \
  "s_cmp_eq_u32 s92, 1\n\t" \
  "s_cbranch_scc1 .Lpoll%=\n\t" \
  "s_sleep 1\n\t" \
  "s_and_b32 s93, s92, 63\n\t" \
  "s_cmp_lg_u32 s93, 0\n\t" \
  "s_cbranch_scc1 .Lpoll%=\n\t" \
  "v_mov_b32 v245, 0\n\t" \
  "buffer_load_dword v244, v245, %[rs], %[sab] offen sc1\n\t" \
  "s_waitcnt vmcnt(0)\n\t" \
  "v_readfirstlane_b32 s93, v244\n\t" \
  "s_nop 3\n\t" \
  "s_cmp_eq_u32 s93, 1\n\t" \
  "s_cbranch_scc1 .Lok%=\n\t" \
  "s_cmp_lt_u32 s92, 0x80000\n\t" \
  "s_cbranch_scc1 .Lpoll%=\n\t" \
  "v_mov_b32 v244, 1\n\t" \
  "buffer_store_dword v244, v245, %[rs], %[sab] offen sc1\n\t" \
  "s_branch .Lok%=\n\t" \
  ".Lend%=:\n\t"
#define QK_ASM_7 \
  "s_waitcnt lgkmcnt(0)\n\t" \
  "v_subrev_u32 v244, 0x8000, v250\n\t" \
  "ds_read_b128 v[216:219], v248 offset:61440\n\t" \
  "ds_read_b128 v[220:223], v248 offset:62464\n\t" \
  "ds_read_b128 v[224:227], v248 offset:63488\n\t" \
  "ds_read_b128 v[228:231], v248 offset:64512\n\t" \
  "ds_read_b128 v[208:211], v244 offset:0\n\t" \
  "ds_read_b128 v[212:215], v244 offset:1024\n\t" \
  "ds_read_b128 v[236:239], v244 offset:2048\n\t" \
  "ds_read_b128 v[240:243], v244 offset:3072\n\t" \
  "v_mov_b64 v[192:193], 0\n\t" \
  "v_mov_b64 v[194:195], 0\n\t" \
  "v_mov_b64 v[196:197], 0\n\t" \
  "v_mov_b64 v[198:199], 0\n\t" \
  "s_movk_i32 s92, 0x2000\n\t" \
  ".Lwt%=:\n\t" \
  "ds_read_b64 v[200:201], v248 offset:30720\n\t" \
  "s_memrealtime s[90:91]\n\t" \
  "s_sub_u32 s92, s92, 1\n\t" \
  "s_waitcnt lgkmcnt(0)\n\t" \
  "v_readfirstlane_b32 s93, v200\n\t" \
  "v_readfirstlane_b32 %[tgt], v201\n\t" \
  "s_cbranch_scc1 .Lgo%=\n\t" \
  "s_cmp_eq_u32 s93, %[step]\n\t" \
  "s_cbranch_scc1 .Lwx%=\n\t" \
  "s_sleep 1\n\t" \
  "s_branch .Lwt%=\n\t" \
  ".Lwx%=:\n\t" \
  "s_lshl_b32 s93, s90, 2\n\t" \
  "s_sub_i32 s93, %[tgt], s93\n\t" \
  "s_cmp_le_i32 s93, 0\n\t" \
  "s_cbranch_scc1 .Lgo%=\n\t" \
  "s_min_i32 s93, s93, 8000\n\t" \
  "s_mov_b32 s94, s93\n\t" \
  ".Lsl%=:\n\t" \
  "s_sub_i32 s94, s94, 18\n\t" \
  "s_cmp_lt_i32 s94, 30\n\t" \
  "s_cbranch_scc1 .Lsx%=\n\t" \
  "s_sleep 1\n\t" \
  "s_branch .Lsl%=\n\t" \
  ".Lsx%=:\n\t" \
  "s_sub_i32 s93, s93, 12\n\t" \
  "s_max_i32 s93, s93, 4\n\t" \
  "s_lshr_b32 s93, s93, 2\n\t" \
  "s_add_u32 s93, s93, s90\n\t" \
  "s_memrealtime s[90:91]\n\t" \
  ".Lpace%=:\n\t" \
  "s_memrealtime s[94:95]\n\t" \
  "s_waitcnt lgkmcnt(1)\n\t" \
  "s_sub_i32 s92, s90, s93\n\t" \
  "s_cmp_ge_i32 s92, 0\n\t" \
  "s_cbranch_scc1 .Le%=\n\t" \
  "s_memrealtime s[90:91]\n\t" \
  "s_waitcnt lgkmcnt(1)\n\t" \
  "s_sub_i32 s92, s94, s93\n\t" \
  "s_cmp_lt_i32 s92, 0\n\t" \
  "s_cbranch_scc1 .Lpace%=\n\t" \
  "s_nop 4\n\t" \
  ".Le%=:\n\t" \
  "s_add_u32 s92, s92, s93\n\t" \
  "s_lshl_b32 s92, s92, 2\n\t" \
  "s_sub_i32 s92, %[tgt], s92\n\t" \
  "s_max_i32 s92, s92, 0\n\t" \
  "s_bitcmp0_b32 s92, 3\n\t" \
  "s_cbranch_scc1 .Lf3%=\n\t" \
  "s_nop 15\n\t" \
  ".Lf3%=:\n\t" \
  "s_bitcmp0_b32 s92, 2\n\t" \
  "s_cbranch_scc1 .Lf2%=\n\t" \
  "s_nop 10\n\t" \
  ".Lf2%=:\n\t" \
  ".Lgo%=:\n\t" \
  "s_mov_b32 s92, 0\n\t" \
  "s_memtime s[88:89]\n\t" \
  ".Lpoll%=:\n\t" \
  "buffer_load_dwordx4 v[200:203], v248, %[rs], %[som] offen sc1\n\t" \
  "buffer_load_dwordx4 v[204:207], v248, %[rs], %[som2] offen sc1\n\t" \
  "s_waitcnt vmcnt(1) lgkmcnt(0)\n\t" \
  "ds_write_b128 v249, v[200:203]\n\t" \
  "v_smfmac_f32_16x16x64_bf16 v[192:195], v[200:203], v[208:215], v251\n\t" \
  "v_max3_u32 v244, v200, v201, v202\n\t" \
  "v_smfmac_f32_16x16x64_bf16 v[196:199], v[200:203], v[236:243], v251\n\t" \
  "s_waitcnt vmcnt(0)\n\t" \
  "ds_write_b128 v249, v[204:207] offset:8192\n\t" \
  "v_smfmac_f32_16x16x64_bf16 v[192:195], v[204:207], v[216:223], v251\n\t" \
  "v_max3_u32 v244, v244, v203, v204\n\t" \
  "v_max3_u32 v244, v244, v205, v206\n\t" \
  "v_smfmac_f32_16x16x64_bf16 v[196:199], v[204:207], v[224:231], v251\n\t" \
  "v_max_u32 v244, v244, v207\n\t" \
  "v_cmp_le_u32 vcc, %[sent], v244\n\t" \
  "s_cbranch_vccnz .Lslow%=\n\t" \
  ".Lok%=:\n\t" \
  "ds_read_b128 v[216:219], v250 offset:32768\n\t" \
  "ds_read_b128 v[220:223], v250 offset:33792\n\t" \
  "ds_read_b128 v[224:227], v250 offset:34816\n\t" \
  "ds_read_b128 v[228:231], v250 offset:35840\n\t" \
  "s_waitcnt lgkmcnt(4)\n\t" \
  "s_setprio 3\n\t" \
  "s_barrier\n\t" \
  "s_memrealtime s[90:91]\n\t" \
  "s_memtime s[94:95]\n\t" \
  "v_mov_b32 v232, %[step]\n\t" \
  "v_lshrrev_b32 v234, 1, v248\n\t" \
  "s_mov_b64 exec, 0xffff\n\t" \
  "s_waitcnt lgkmcnt(0)\n\t" \
  "s_lshl_b32 s90, s90, 2\n\t" \
  "s_sub_u32 s94, s94, s88\n\t" \
  "s_mul_i32 s94, s94, 12200\n\t" \
  "s_lshr_b32 s94, s94, 16\n\t" \
  "s_add_u32 s94, s94, %[tgt]\n\t" \
  "s_sub_i32 s93, s90, s94\n\t" \
  "s_sub_i32 s93, s93, 30\n\t" \
  "s_max_i32 s93, s93, 0\n\t" \
  "s_add_u32 s90, s94, s93\n\t" \
  "s_sub_i32 s93, s90, %[tgt]\n\t" \
  "s_cmp_gt_i32 s93, 270\n\t" \
  "s_cselect_b32 s93, 90, 0\n\t" \
  "s_add_u32 s93, s93, s90\n\t" \
  "s_add_u32 s93, s93, %[lead]\n\t" \
  "v_mov_b32 v233, s93\n\t" \
  "buffer_store_dwordx2 v[232:233], v234, %[rs], %[sbc] offen sc1\n\t" \
  "s_mov_b64 exec, -1\n\t" \
  "s_mov_b32 %[tb1], s90\n\t" \
  "s_setprio 0\n\t" \
  "ds_read_b128 v[208:211], v248 offset:6144\n\t" \
  "ds_read_b128 v[212:215], v248 offset:9216\n\t" \
  "ds_read_b128 v[236:239], v248 offset:10240\n\t" \
  "ds_read_b128 v[240:243], v248 offset:11264\n\t" \
  "s_waitcnt lgkmcnt(3)\n\t" \
  "ds_read_b128 v[244:247], v248 offset:12288\n\t" \
  "v_smfmac_f32_16x16x64_bf16 v[192:195], v[208:211], v[216:223], v251\n\t" \
  "ds_read_b128 v[216:219], v250 offset:0\n\t" \
  "v_smfmac_f32_16x16x64_bf16 v[196:199], v[208:211], v[224:231], v251\n\t" \
  "s_waitcnt lgkmcnt(4)\n\t" \
  "ds_read_b128 v[220:223], v250 offset:1024\n\t" \
  "v_smfmac_f32_16x16x64_bf16 v[192:195], v[212:215], v[0:7], v251\n\t" \
  "ds_read_b128 v[224:227], v250 offset:2048\n\t" \
  "v_smfmac_f32_16x16x64_bf16 v[196:199], v[212:215], v[8:15], v251\n\t" \
  "s_waitcnt lgkmcnt(5)\n\t" \
  "ds_read_b128 v[228:231], v250 offset:3072\n\t" \
  "v_smfmac_f32_16x16x64_bf16 v[192:195], v[236:239], v[16:23], v251\n\t" \
  "ds_read_b128 v[200:203], v248 offset:13312\n\t" \
  "v_smfmac_f32_16x16x64_bf16 v[196:199], v[236:239], v[24:31], v251\n\t" \
  "s_waitcnt lgkmcnt(6)\n\t" \
  "ds_read_b128 v[204:207], v248 offset:14336\n\t" \
  "v_smfmac_f32_16x16x64_bf16 v[192:195], v[240:243], v[32:39], v251\n\t" \
  "ds_read_b128 v[208:211], v248 offset:0\n\t" \
  "v_smfmac_f32_16x16x64_bf16 v[196:199], v[240:243], v[40:47], v251\n\t" \
  "s_waitcnt lgkmcnt(5)\n\t" \
  "ds_read_b128 v[212:215], v248 offset:1024\n\t" \
  "v_smfmac_f32_16x16x64_bf16 v[192:195], v[244:247], v[216:223], v251\n\t" \
  "s_waitcnt lgkmcnt(4)\n\t" \
  "v_smfmac_f32_16x16x64_bf16 v[196:199], v[244:247], v[224:231], v251\n\t" \
  "s_waitcnt lgkmcnt(3)\n\t" \
  "ds_read_b128 v[236:239], v248 offset:2048\n\t" \
  "v_smfmac_f32_16x16x64_bf16 v[192:195], v[200:203], v[48:55], v251\n\t" \
  "ds_read_b128 v[240:243], v248 offset:3072\n\t" \
  "v_smfmac_f32_16x16x64_bf16 v[196:199], v[200:203], v[56:63], v251\n\t" \
  "s_waitcnt lgkmcnt(4)\n\t" \
  "v_smfmac_f32_16x16x64_bf16 v[192:195], v[204:207], v[64:71], v251\n\t" \
  "ds_read_b128 v[244:247], v248 offset:4096\n\t" \
  "v_smfmac_f32_16x16x64_bf16 v[196:199], v[204:207], v[72:79], v251\n\t" \
  "s_waitcnt lgkmcnt(4)\n\t" \
  "v_smfmac_f32_16x16x64_bf16 v[192:195], v[208:211], v[80:87], v251\n\t" \
  "ds_read_b128 v[200:203], v248 offset:5120\n\t" \
  "v_smfmac_f32_16x16x64_bf16 v[196:199], v[208:211], v[88:95], v251\n\t" \
  "s_waitcnt lgkmcnt(4)\n\t" \
  "v_smfmac_f32_16x16x64_bf16 v[192:195], v[212:215], v[96:103], v251\n\t" \
  "ds_read_b128 v[204:207], v248 offset:8192\n\t" \
  "v_smfmac_f32_16x16x64_bf16 v[196:199], v[212:215], v[104:111], v251\n\t" \
  "s_waitcnt lgkmcnt(3)\n\t" \
  "v_smfmac_f32_16x16x64_bf16 v[192:195], v[236:239], v[112:119], v251\n\t" \
  "v_smfmac_f32_16x16x64_bf16 v[196:199], v[236:239], v[120:127], v251\n\t" \
  "v_smfmac_f32_16x16x64_bf16 v[192:195], v[240:243], v[128:135], v251\n\t" \
  "v_smfmac_f32_16x16x64_bf16 v[196:199], v[240:243], v[136:143], v251\n\t" \
  "s_waitcnt lgkmcnt(1)\n\t" \
  "v_smfmac_f32_16x16x64_bf16 v[192:195], v[244:247], v[144:151], v251\n\t" \
  "v_smfmac_f32_16x16x64_bf16 v[196:199], v[244:247], v[152:159], v251\n\t" \
  "v_smfmac_f32_16x16x64_bf16 v[192:195], v[200:203], v[160:167], v251\n\t" \
  "v_smfmac_f32_16x16x64_bf16 v[196:199], v[200:203], v[168:175], v251\n\t" \
  "s_waitcnt lgkmcnt(0)\n\t" \
  "v_smfmac_f32_16x16x64_bf16 v[192:195], v[204:207], v[176:183], v251\n\t" \
  "v_smfmac_f32_16x16x64_bf16 v[196:199], v[204:207], v[184:191], v251\n\t" \
  "s_nop 6\n\t" \
  "v_add_f32 v192, v192, v194\n\t" \
  "v_add_f32 v193, v193, v195\n\t" \
  "v_add_f32 v196, v196, v198\n\t" \
  "v_add_f32 v197, v197, v199\n\t" \
  "v_cvt_pk_bf16_f32 v192, v192, v193\n\t" \
  "v_cvt_pk_bf16_f32 v196, v196, v197\n\t" \
  "ds_write2_b32 v252, v192, v196 offset1:64\n\t" \
  "s_waitcnt lgkmcnt(0)\n\t" \
  "s_barrier\n\t" \
  "buffer_store_dword v192, v235, %[rs], %[sk] offen sc1\n\t" \
  "buffer_store_dword v196, v235, %[rs], %[sk] offen offset:256 sc1\n\t" \
  "s_mov_b32 %[spins], s92\n\t" \
  "s_branch .Lend%=\n\t" \
  ".Lslow%=:\n\t" \
  "s_mov_b64 s[94:95], 0\n\t" \
  "v_cmp_eq_u32 vcc, %[sent], v200\n\t" \
  "s_or_b64 s[94:95], s[94:95], vcc\n\t" \
  "v_cmp_eq_u32 vcc, %[sent], v201\n\t" \
  "s_or_b64 s[94:95], s[94:95], vcc\n\t" \
  "v_cmp_eq_u32 vcc, %[sent], v202\n\t" \
  "s_or_b64 s[94:95], s[94:95], vcc\n\t" \
  "v_cmp_eq_u32 vcc, %[sent], v203\n\t" \
  "s_or_b64 s[94:95], s[94:95], vcc\n\t" \
  "v_cmp_eq_u32 vcc, %[sent], v204\n\t" \
  "s_or_b64 s[94:95], s[94:95], vcc\n\t" \
  "v_cmp_eq_u32 vcc, %[sent], v205\n\t" \
  "s_or_b64 s[94:95], s[94:95], vcc\n\t" \
  "v_cmp_eq_u32 vcc, %[sent], v206\n\t" \
  "s_or_b64 s[94:95], s[94:95], vcc\n\t" \
  "v_cmp_eq_u32 vcc, %[sent], v207\n\t" \
  "s_or_b64 s[94:95], s[94:95], vcc\n\t" \
  "s_cmp_eq_u64 s[94:95], 0\n\t" \
  "s_cbranch_scc1 .Lok%=\n\t" \
  "v_mov_b64 v[192:193], 0\n\t" \
  "v_mov_b64 v[194:195], 0\n\t" \
  "v_mov_b64 v[196:197], 0\n\t" \
  "v_mov_b64 v[198:199], 0\n\t" \
  "s_add_u32 s92, s92, 1\n\t" \
  "s_sleep 6\n\t" \
  "s_cmp_eq_u32 s92, 1\n\t" \
  "s_cbranch_scc1 .Lpoll%=\n\t" \
  "s_sleep 1\n\t" \
  "s_and_b32 s93, s92, 63\n\t" \
  "s_cmp_lg_u32 s93, 0\n\t" \
  "s_cbranch_scc1 .Lpoll%=\n\t" \
  "v_mov_b32 v245, 0\n\t" \
  "buffer_load_dword v244, v245, %[rs], %[sab] offen sc1\n\t" \
  "s_waitcnt vmcnt(0)\n\t" \
  "v_readfirstlane_b32 s93, v244\n\t" \
  "s_nop 3\n\t" \
  "s_cmp_eq_u32 s93, 1\n\t" \
  "s_cbranch_scc1 .Lok%=\n\t" \
  "s_cmp_lt_u32 s92, 0x80000\n\t" \
  "s_cbranch_scc1 .Lpoll%=\n\t" \
  "v_mov_b32 v244, 1\n\t" \
  "buffer_store_dword v244, v245, %[rs], %[sab] offen sc1\n\t" \
  "s_branch .Lok%=\n\t" \
  ".Lend%=:\n\t"
#define VV_ASM \
  "s_waitcnt lgkmcnt(0)\n\t" \
  "v_mov_b64 v[128:129], 0\n\t" \
  "v_mov_b64 v[130:131], 0\n\t" \
  "s_movk_i32 s92, 0x2000\n\t" \
  ".Lwt%=:\n\t" \
  "ds_read_b64 v[132:133], v200 offset:30720\n\t" \
  "s_memrealtime s[90:91]\n\t" \
  "s_sub_u32 s92, s92, 1\n\t" \
  "s_waitcnt lgkmcnt(0)\n\t" \
  "v_readfirstlane_b32 s93, v132\n\t" \
  "v_readfirstlane_b32 %[tgt], v133\n\t" \
  "s_cbranch_scc1 .Lgo%=\n\t" \
  "s_cmp_eq_u32 s93, %[step]\n\t" \
  "s_cbranch_scc1 .Lwx%=\n\t" \
  "s_sleep 1\n\t" \
  "s_branch .Lwt%=\n\t" \
  ".Lwx%=:\n\t" \
  "s_lshl_b32 s93, s90, 2\n\t" \
  "s_sub_i32 s93, %[tgt], s93\n\t" \
  "s_cmp_le_i32 s93, 0\n\t" \
  "s_cbranch_scc1 .Lgo%=\n\t" \
  "s_min_i32 s93, s93, 8000\n\t" \
  "s_mov_b32 s94, s93\n\t" \
  ".Lsl%=:\n\t" \
  "s_sub_i32 s94, s94, 18\n\t" \
  "s_cmp_lt_i32 s94, 30\n\t" \
  "s_cbranch_scc1 .Lsx%=\n\t" \
  "s_sleep 1\n\t" \
  "s_branch .Lsl%=\n\t" \
  ".Lsx%=:\n\t" \
  "s_sub_i32 s93, s93, 12\n\t" \
  "s_max_i32 s93, s93, 4\n\t" \
  "s_lshr_b32 s93, s93, 2\n\t" \
  "s_add_u32 s93, s93, s90\n\t" \
  "s_memrealtime s[90:91]\n\t" \
  ".Lpace%=:\n\t" \
  "s_memrealtime s[94:95]\n\t" \
  "s_waitcnt lgkmcnt(1)\n\t" \
  "s_sub_i32 s92, s90, s93\n\t" \
  "s_cmp_ge_i32 s92, 0\n\t" \
  "s_cbranch_scc1 .Le%=\n\t" \
  "s_memrealtime s[90:91]\n\t" \
  "s_waitcnt lgkmcnt(1)\n\t" \
  "s_sub_i32 s92, s94, s93\n\t" \
  "s_cmp_lt_i32 s92, 0\n\t" \
  "s_cbranch_scc1 .Lpace%=\n\t" \
  "s_nop 4\n\t" \
  ".Le%=:\n\t" \
  "s_add_u32 s92, s92, s93\n\t" \
  "s_lshl_b32 s92, s92, 2\n\t" \
  "s_sub_i32 s92, %[tgt], s92\n\t" \
  "s_max_i32 s92, s92, 0\n\t" \
  "s_bitcmp0_b32 s92, 3\n\t" \
  "s_cbranch_scc1 .Lf3%=\n\t" \
  "s_nop 15\n\t" \
  ".Lf3%=:\n\t" \
  "s_bitcmp0_b32 s92, 2\n\t" \
  "s_cbranch_scc1 .Lf2%=\n\t" \
  "s_nop 10\n\t" \
  ".Lf2%=:\n\t" \
  ".Lgo%=:\n\t" \
  "s_mov_b32 s92, 0\n\t" \
  ".Lpoll%=:\n\t" \
  "buffer_load_dwordx4 v[132:135], v200, %[rs], %[som] offen sc1\n\t" \
  "buffer_load_dwordx4 v[136:139], v200, %[rs], %[som2] offen sc1\n\t" \
  "s_waitcnt vmcnt(1) lgkmcnt(0)\n\t" \
  "ds_write_b128 v232, v[132:135]\n\t" \
  "v_smfmac_f32_16x16x64_bf16 v[128:131], v[132:135], v[0:7], v202\n\t" \
  "v_max3_u32 v196, v132, v133, v134\n\t" \
  "s_waitcnt vmcnt(0)\n\t" \
  "ds_write_b128 v232, v[136:139] offset:8192\n\t" \
  "v_smfmac_f32_16x16x64_bf16 v[128:131], v[136:139], v[64:71], v202\n\t" \
  "v_max3_u32 v196, v196, v135, v136\n\t" \
  "v_max3_u32 v196, v196, v137, v138\n\t" \
  "v_max_u32 v196, v196, v139\n\t" \
  "v_cmp_le_u32 vcc, %[sent], v196\n\t" \
  "s_cbranch_vccnz .Lslow%=\n\t" \
  ".Lok%=:\n\t" \
  "s_waitcnt lgkmcnt(0)\n\t" \
  "s_barrier\n\t" \
  "ds_read_b128 v[164:167], v233 offset:0\n\t" \
  "ds_read_b128 v[140:143], v234 offset:0\n\t" \
  "ds_read_b128 v[144:147], v235 offset:0\n\t" \
  "ds_read_b128 v[148:151], v236 offset:0\n\t" \
  "ds_read_b128 v[152:155], v237 offset:0\n\t" \
  "s_waitcnt lgkmcnt(4)\n\t" \
  "ds_read_b128 v[156:159], v238 offset:0\n\t" \
  "v_smfmac_f32_16x16x64_bf16 v[128:131], v[164:167], v[8:15], v202\n\t" \
  "s_waitcnt lgkmcnt(4)\n\t" \
  "ds_read_b128 v[160:163], v239 offset:0\n\t" \
  "v_smfmac_f32_16x16x64_bf16 v[128:131], v[140:143], v[16:23], v202\n\t" \
  "s_waitcnt lgkmcnt(4)\n\t" \
  "ds_read_b128 v[168:171], v233 offset:8192\n\t" \
  "v_smfmac_f32_16x16x64_bf16 v[128:131], v[144:147], v[24:31], v202\n\t" \
  "s_waitcnt lgkmcnt(4)\n\t" \
  "ds_read_b128 v[172:175], v234 offset:8192\n\t" \
  "v_smfmac_f32_16x16x64_bf16 v[128:131], v[148:151], v[32:39], v202\n\t" \
  "s_waitcnt lgkmcnt(4)\n\t" \
  "ds_read_b128 v[176:179], v235 offset:8192\n\t" \
  "v_smfmac_f32_16x16x64_bf16 v[128:131], v[152:155], v[40:47], v202\n\t" \
  "s_waitcnt lgkmcnt(4)\n\t" \
  "ds_read_b128 v[180:183], v236 offset:8192\n\t" \
  "v_smfmac_f32_16x16x64_bf16 v[128:131], v[156:159], v[48:55], v202\n\t" \
  "s_waitcnt lgkmcnt(4)\n\t" \
  "ds_read_b128 v[184:187], v237 offset:8192\n\t" \
  "v_smfmac_f32_16x16x64_bf16 v[128:131], v[160:163], v[56:63], v202\n\t" \
  "s_waitcnt lgkmcnt(4)\n\t" \
  "ds_read_b128 v[188:191], v238 offset:8192\n\t" \
  "v_smfmac_f32_16x16x64_bf16 v[128:131], v[168:171], v[72:79], v202\n\t" \
  "s_waitcnt lgkmcnt(4)\n\t" \
  "ds_read_b128 v[192:195], v239 offset:8192\n\t" \
  "v_smfmac_f32_16x16x64_bf16 v[128:131], v[172:175], v[80:87], v202\n\t" \
  "s_waitcnt lgkmcnt(4)\n\t" \
  "v_smfmac_f32_16x16x64_bf16 v[128:131], v[176:179], v[88:95], v202\n\t" \
  "s_waitcnt lgkmcnt(3)\n\t" \
  "v_smfmac_f32_16x16x64_bf16 v[128:131], v[180:183], v[96:103], v202\n\t" \
  "s_waitcnt lgkmcnt(2)\n\t" \
  "v_smfmac_f32_16x16x64_bf16 v[128:131], v[184:187], v[104:111], v202\n\t" \
  "s_waitcnt lgkmcnt(1)\n\t" \
  "v_smfmac_f32_16x16x64_bf16 v[128:131], v[188:191], v[112:119], v202\n\t" \
  "s_waitcnt lgkmcnt(0)\n\t" \
  "v_smfmac_f32_16x16x64_bf16 v[128:131], v[192:195], v[120:127], v202\n\t" \
  "s_nop 7\n\t" \
  "v_add_f32 v128, v128, v130\n\t" \
  "v_add_f32 v129, v129, v131\n\t" \
  "v_cvt_pk_bf16_f32 v128, v128, v129\n\t" \
  "buffer_store_dword v128, v203, %[rs], %[sv] offen sc1\n\t" \
  "ds_write_b32 v204, v128\n\t" \
  "s_waitcnt lgkmcnt(0)\n\t" \
  "s_barrier\n\t" \
  "s_mov_b32 %[spins], s92\n\t" \
  "s_branch .Lend%=\n\t" \
  ".Lslow%=:\n\t" \
  "s_mov_b64 s[94:95], 0\n\t" \
  "v_cmp_eq_u32 vcc, %[sent], v132\n\t" \
  "s_or_b64 s[94:95], s[94:95], vcc\n\t" \
  "v_cmp_eq_u32 vcc, %[sent], v133\n\t" \
  "s_or_b64 s[94:95], s[94:95], vcc\n\t" \
  "v_cmp_eq_u32 vcc, %[sent], v134\n\t" \
  "s_or_b64 s[94:95], s[94:95], vcc\n\t" \
  "v_cmp_eq_u32 vcc, %[sent], v135\n\t" \
  "s_or_b64 s[94:95], s[94:95], vcc\n\t" \
  "v_cmp_eq_u32 vcc, %[sent], v136\n\t" \
  "s_or_b64 s[94:95], s[94:95], vcc\n\t" \
  "v_cmp_eq_u32 vcc, %[sent], v137\n\t" \
  "s_or_b64 s[94:95], s[94:95], vcc\n\t" \
  "v_cmp_eq_u32 vcc, %[sent], v138\n\t" \
  "s_or_b64 s[94:95], s[94:95], vcc\n\t" \
  "v_cmp_eq_u32 vcc, %[sent], v139\n\t" \
  "s_or_b64 s[94:95], s[94:95], vcc\n\t" \
  "s_cmp_eq_u64 s[94:95], 0\n\t" \
  "s_cbranch_scc1 .Lok%=\n\t" \
  "v_mov_b64 v[128:129], 0\n\t" \
  "v_mov_b64 v[130:131], 0\n\t" \
  "s_add_u32 s92, s92, 1\n\t" \
  "s_sleep 6\n\t" \
  "s_cmp_eq_u32 s92, 1\n\t" \
  "s_cbranch_scc1 .Lpoll%=\n\t" \
  "s_sleep 1\n\t" \
  "s_and_b32 s93, s92, 63\n\t" \
  "s_cmp_lg_u32 s93, 0\n\t" \
  "s_cbranch_scc1 .Lpoll%=\n\t" \
  "v_mov_b32 v197, 0\n\t" \
  "buffer_load_dword v196, v197, %[rs], %[sab] offen sc1\n\t" \
  "s_waitcnt vmcnt(0)\n\t" \
  "v_readfirstlane_b32 s93, v196\n\t" \
  "s_nop 3\n\t" \
  "s_cmp_eq_u32 s93, 1\n\t" \
  "s_cbranch_scc1 .Lok%=\n\t" \
  "s_cmp_lt_u32 s92, 0x80000\n\t" \
  "s_cbranch_scc1 .Lpoll%=\n\t" \
  "v_mov_b32 v196, 1\n\t" \
  "buffer_store_dword v196, v197, %[rs], %[sab] offen sc1\n\t" \
  "s_branch .Lok%=\n\t" \
  ".Lend%=:\n\t"
#define TK_ASM \
  "buffer_load_dwordx4 v[224:227], v200, %[rs], %[sk] offen offset:0 sc1\n\t" \
  "buffer_load_dwordx4 v[228:231], v200, %[rs], %[sk] offen offset:1024 sc1\n\t" \
  "s_memrealtime s[88:89]\n\t" \
  "v_add_u32 v205, %[tl], v200\n\t" \
  "v_add_u32 v206, %[tq], v205\n\t" \
  "v_add_u32 v207, %[tv], v205\n\t" \
  "s_waitcnt lgkmcnt(0)\n\t" \
  "ds_read_b128 v[128:131], v206 offset:8192\n\t" \
  "ds_read_b128 v[144:147], v205 offset:0\n\t" \
  "ds_read_b128 v[160:163], v205 offset:4096\n\t" \
  "ds_read_b128 v[132:135], v206 offset:9216\n\t" \
  "ds_read_b128 v[148:151], v205 offset:1024\n\t" \
  "ds_read_b128 v[164:167], v205 offset:5120\n\t" \
  "ds_read_b128 v[136:139], v206 offset:10240\n\t" \
  "ds_read_b128 v[152:155], v205 offset:2048\n\t" \
  "ds_read_b128 v[168:171], v205 offset:6144\n\t" \
  "ds_read_b128 v[140:143], v206 offset:11264\n\t" \
  "ds_read_b128 v[156:159], v205 offset:3072\n\t" \
  "ds_read_b128 v[172:175], v205 offset:7168\n\t" \
  "v_add_u32 v206, %[vp], v218\n\t" \
  "s_lshl_b32 s88, s88, 2\n\t" \
  "s_add_u32 s89, s88, %[od]\n\t" \
  "s_waitcnt lgkmcnt(10)\n\t" \
  "v_mfma_f32_16x16x32_bf16 v[196:199], v[144:147], v[128:131], 0\n\t" \
  "s_waitcnt lgkmcnt(9)\n\t" \
  "v_mfma_f32_16x16x32_bf16 v[144:147], v[160:163], v[128:131], 0\n\t" \
  "s_waitcnt lgkmcnt(7)\n\t" \
  "v_mfma_f32_16x16x32_bf16 v[196:199], v[148:151], v[132:135], v[196:199]\n\t" \
  "s_waitcnt lgkmcnt(6)\n\t" \
  "v_mfma_f32_16x16x32_bf16 v[144:147], v[164:167], v[132:135], v[144:147]\n\t" \
  "s_waitcnt lgkmcnt(4)\n\t" \
  "v_mfma_f32_16x16x32_bf16 v[196:199], v[152:155], v[136:139], v[196:199]\n\t" \
  "s_waitcnt lgkmcnt(3)\n\t" \
  "v_mfma_f32_16x16x32_bf16 v[144:147], v[168:171], v[136:139], v[144:147]\n\t" \
  "s_waitcnt lgkmcnt(1)\n\t" \
  "v_mfma_f32_16x16x32_bf16 v[196:199], v[156:159], v[140:143], v[196:199]\n\t" \
  "s_waitcnt lgkmcnt(0)\n\t" \
  "v_mfma_f32_16x16x32_bf16 v[144:147], v[172:175], v[140:143], v[144:147]\n\t" \
  "ds_read_b128 v[176:179], v207 offset:16384\n\t" \
  "ds_read_b128 v[180:183], v207 offset:17408\n\t" \
  "ds_read_b128 v[184:187], v207 offset:18432\n\t" \
  "ds_read_b128 v[188:191], v207 offset:19456\n\t" \
  "ds_read2st64_b32 v[192:193], v206 offset0:0 offset1:1\n\t" \
  "ds_read2st64_b32 v[194:195], v206 offset0:2 offset1:3\n\t" \
  "v_cvt_pk_bf16_f32 v197, v196, v197\n\t" \
  "v_cvt_pk_bf16_f32 v199, v198, v199\n\t" \
  "v_cvt_pk_bf16_f32 v145, v144, v145\n\t" \
  "v_cvt_pk_bf16_f32 v147, v146, v147\n\t" \
  "v_lshlrev_b32 v196, 16, v197\n\t" \
  "v_lshlrev_b32 v198, 16, v199\n\t" \
  "v_lshlrev_b32 v144, 16, v145\n\t" \
  "v_lshlrev_b32 v146, 16, v147\n\t" \
  "v_and_b32 v197, 0xffff0000, v197\n\t" \
  "v_and_b32 v199, 0xffff0000, v199\n\t" \
  "v_and_b32 v145, 0xffff0000, v145\n\t" \
  "v_and_b32 v147, 0xffff0000, v147\n\t" \
  "v_pk_mul_f32 v[196:197], v[196:197], %[sc] op_sel_hi:[1,0]\n\t" \
  "v_pk_mul_f32 v[198:199], v[198:199], %[sc] op_sel_hi:[1,0]\n\t" \
  "v_pk_mul_f32 v[144:145], v[144:145], %[sc] op_sel_hi:[1,0]\n\t" \
  "v_pk_mul_f32 v[146:147], v[146:147], %[sc] op_sel_hi:[1,0]\n\t" \
  "v_cvt_pk_bf16_f32 v197, v196, v197\n\t" \
  "v_cvt_pk_bf16_f32 v199, v198, v199\n\t" \
  "v_cvt_pk_bf16_f32 v145, v144, v145\n\t" \
  "v_cvt_pk_bf16_f32 v147, v146, v147\n\t" \
  "v_lshlrev_b32 v196, 16, v197\n\t" \
  "v_lshlrev_b32 v198, 16, v199\n\t" \
  "v_lshlrev_b32 v144, 16, v145\n\t" \
  "v_lshlrev_b32 v146, 16, v147\n\t" \
  "v_and_b32 v197, 0xffff0000, v197\n\t" \
  "v_and_b32 v199, 0xffff0000, v199\n\t" \
  "v_and_b32 v145, 0xffff0000, v145\n\t" \
  "v_and_b32 v147, 0xffff0000, v147\n\t" \
  "v_min_f32 v196, v196, v208\n\t" \
  "v_min_f32 v197, v197, v209\n\t" \
  "v_min_f32 v198, v198, v210\n\t" \
  "v_min_f32 v199, v199, v211\n\t" \
  "v_min_f32 v144, v144, v212\n\t" \
  "v_min_f32 v145, v145, v213\n\t" \
  "v_min_f32 v146, v146, v214\n\t" \
  "v_min_f32 v147, v147, v215\n\t" \
  "v_max3_f32 v148, v196, v197, v198\n\t" \
  "v_max3_f32 v149, v199, v144, v145\n\t" \
  "v_max3_f32 v148, v148, v146, v147\n\t" \
  "v_max_f32 v150, v148, v149\n\t" \
  "v_max_f32 v151, v148, v149\n\t" \
  "s_nop 1\n\t" \
  "v_permlane32_swap_b32 v150, v151\n\t" \
  "v_max_f32 v150, v150, v151\n\t" \
  "s_mov_b32 s92, 0\n\t" \
  ".Ltake%=:\n\t" \
  "s_waitcnt vmcnt(1) lgkmcnt(0)\n\t" \
  "ds_write_b128 v216, v[224:227]\n\t" \
  "v_max3_u32 v148, v224, v225, v226\n\t" \
  "s_waitcnt vmcnt(0)\n\t" \
  "ds_write_b128 v216, v[228:231] offset:1024\n\t" \
  "ds_read_b64_tr_b16 v[160:161], v217 offset:0\n\t" \
  "ds_read_b64_tr_b16 v[162:163], v217 offset:64\n\t" \
  "ds_read_b64_tr_b16 v[164:165], v217 offset:512\n\t" \
  "ds_read_b64_tr_b16 v[166:167], v217 offset:576\n\t" \
  "ds_read_b64_tr_b16 v[168:169], v217 offset:1024\n\t" \
  "ds_read_b64_tr_b16 v[170:171], v217 offset:1088\n\t" \
  "ds_read_b64_tr_b16 v[172:173], v217 offset:1536\n\t" \
  "ds_read_b64_tr_b16 v[174:175], v217 offset:1600\n\t" \
  "v_max3_u32 v148, v148, v227, v228\n\t" \
  "v_max3_u32 v148, v148, v229, v230\n\t" \
  "v_max_u32 v148, v148, v231\n\t" \
  "v_cmp_le_u32 vcc, %[sent], v148\n\t" \
  "s_cbranch_vccnz .Lslow%=\n\t" \
  ".Lok%=:\n\t" \
  "s_waitcnt lgkmcnt(6)\n\t" \
  "v_mfma_f32_16x16x32_bf16 v[152:155], v[160:163], v[128:131], 0\n\t" \
  "s_waitcnt lgkmcnt(4)\n\t" \
  "v_mfma_f32_16x16x32_bf16 v[152:155], v[164:167], v[132:135], v[152:155]\n\t" \
  "s_waitcnt lgkmcnt(2)\n\t" \
  "v_mfma_f32_16x16x32_bf16 v[152:155], v[168:171], v[136:139], v[152:155]\n\t" \
  "s_waitcnt lgkmcnt(0)\n\t" \
  "v_mfma_f32_16x16x32_bf16 v[152:155], v[172:175], v[140:143], v[152:155]\n\t" \
  "v_mov_b32 v157, 0\n\t" \
  "v_mov_b32 v158, 0\n\t" \
  "s_nop 5\n\t" \
  "v_cvt_pk_bf16_f32 v153, v152, v153\n\t" \
  "v_cvt_pk_bf16_f32 v155, v154, v155\n\t" \
  "v_lshlrev_b32 v152, 16, v153\n\t" \
  "v_lshlrev_b32 v154, 16, v155\n\t" \
  "v_and_b32 v153, 0xffff0000, v153\n\t" \
  "v_and_b32 v155, 0xffff0000, v155\n\t" \
  "v_pk_mul_f32 v[152:153], v[152:153], %[sc] op_sel_hi:[1,0]\n\t" \
  "v_pk_mul_f32 v[154:155], v[154:155], %[sc] op_sel_hi:[1,0]\n\t" \
  "v_cvt_pk_bf16_f32 v153, v152, v153\n\t" \
  "v_cvt_pk_bf16_f32 v155, v154, v155\n\t" \
  "v_lshlrev_b32 v152, 16, v153\n\t" \
  "v_lshlrev_b32 v154, 16, v155\n\t" \
  "v_and_b32 v153, 0xffff0000, v153\n\t" \
  "v_and_b32 v155, 0xffff0000, v155\n\t" \
  "v_max3_f32 v148, v152, v153, v150\n\t" \
  "v_max3_f32 v149, v148, v154, v155\n\t" \
  "v_max3_f32 v150, v148, v154, v155\n\t" \
  "v_cndmask_b32 v152, v152, v154, %[up]\n\t" \
  "v_cndmask_b32 v153, v153, v155, %[up]\n\t" \
  "v_permlane16_swap_b32 v149, v150\n\t" \
  "v_max_f32 v148, v149, v150\n\t" \
  "v_mul_f32 v148, %[negc], v148\n\t" \
  "v_fma_f32 v152, v152, %[c], v148\n\t" \
  "v_fma_f32 v153, v153, %[c], v148\n\t" \
  "v_exp_f32 v152, v152\n\t" \
  "v_fma_f32 v196, v196, %[c], v148\n\t" \
  "v_exp_f32 v153, v153\n\t" \
  "v_fma_f32 v197, v197, %[c], v148\n\t" \
  "v_exp_f32 v196, v196\n\t" \
  "v_fma_f32 v198, v198, %[c], v148\n\t" \
  "v_exp_f32 v197, v197\n\t" \
  "v_fma_f32 v199, v199, %[c], v148\n\t" \
  "v_exp_f32 v198, v198\n\t" \
  "v_fma_f32 v144, v144, %[c], v148\n\t" \
  "v_pk_add_f32 v[132:133], v[152:153], v[196:197]\n\t" \
  "v_exp_f32 v199, v199\n\t" \
  "v_fma_f32 v145, v145, %[c], v148\n\t" \
  "v_exp_f32 v144, v144\n\t" \
  "v_fma_f32 v146, v146, %[c], v148\n\t" \
  "v_pk_add_f32 v[132:133], v[132:133], v[198:199]\n\t" \
  "v_exp_f32 v145, v145\n\t" \
  "v_fma_f32 v147, v147, %[c], v148\n\t" \
  "v_exp_f32 v146, v146\n\t" \
  "v_pk_add_f32 v[132:133], v[132:133], v[144:145]\n\t" \
  "v_exp_f32 v147, v147\n\t" \
  "s_nop 0\n\t" \
  "v_pk_add_f32 v[132:133], v[132:133], v[146:147]\n\t" \
  "v_add_f32 v134, v132, v133\n\t" \
  "v_add_f32 v135, v132, v133\n\t" \
  "s_nop 1\n\t" \
  "v_permlane16_swap_b32 v134, v135\n\t" \
  "v_add_f32 v132, v134, v135\n\t" \
  "v_add_f32 v133, v134, v135\n\t" \
  "s_nop 1\n\t" \
  "v_permlane32_swap_b32 v132, v133\n\t" \
  "v_add_f32 v132, v132, v133\n\t" \
  "v_rcp_f32 v148, v132\n\t" \
  "s_nop 0\n\t" \
  "v_pk_mul_f32 v[152:153], v[152:153], v[148:149] op_sel_hi:[1,0]\n\t" \
  "v_pk_mul_f32 v[196:197], v[196:197], v[148:149] op_sel_hi:[1,0]\n\t" \
  "v_pk_mul_f32 v[198:199], v[198:199], v[148:149] op_sel_hi:[1,0]\n\t" \
  "v_pk_mul_f32 v[144:145], v[144:145], v[148:149] op_sel_hi:[1,0]\n\t" \
  "v_pk_mul_f32 v[146:147], v[146:147], v[148:149] op_sel_hi:[1,0]\n\t" \
  "v_cvt_pk_bf16_f32 v156, v152, v153\n\t" \
  "v_cvt_pk_bf16_f32 v159, v152, v153\n\t" \
  "v_cvt_pk_bf16_f32 v128, v196, v197\n\t" \
  "v_cvt_pk_bf16_f32 v129, v198, v199\n\t" \
  "v_mfma_f32_16x16x16_bf16 v[160:163], v[192:193], v[156:157], 0\n\t" \
  "v_cvt_pk_bf16_f32 v130, v144, v145\n\t" \
  "v_mfma_f32_16x16x16_bf16 v[164:167], v[192:193], v[158:159], 0\n\t" \
  "v_cvt_pk_bf16_f32 v131, v146, v147\n\t" \
  "v_mfma_f32_16x16x16_bf16 v[168:171], v[194:195], v[156:157], 0\n\t" \
  "v_mfma_f32_16x16x16_bf16 v[172:175], v[194:195], v[158:159], 0\n\t" \
  "s_nop 0\n\t" \
  "v_mfma_f32_16x16x32_bf16 v[160:163], v[176:179], v[128:131], v[160:163]\n\t" \
  "s_nop 0\n\t" \
  "v_mfma_f32_16x16x32_bf16 v[164:167], v[180:183], v[128:131], v[164:167]\n\t" \
  "s_nop 0\n\t" \
  "v_mfma_f32_16x16x32_bf16 v[168:171], v[184:187], v[128:131], v[168:171]\n\t" \
  "v_mfma_f32_16x16x32_bf16 v[172:175], v[188:191], v[128:131], v[172:175]\n\t" \
  "s_nop 2\n\t" \
  "v_cvt_pk_bf16_f32 v136, v160, v161\n\t" \
  "v_cvt_pk_bf16_f32 v137, v162, v163\n\t" \
  "v_cvt_pk_bf16_f32 v138, v164, v165\n\t" \
  "v_cvt_pk_bf16_f32 v139, v166, v167\n\t" \
  "v_cvt_pk_bf16_f32 v140, v168, v169\n\t" \
  "v_cvt_pk_bf16_f32 v141, v170, v171\n\t" \
  "v_cvt_pk_bf16_f32 v142, v172, v173\n\t" \
  "v_cvt_pk_bf16_f32 v143, v174, v175\n\t" \
  "s_memrealtime s[90:91]\n\t" \
  "s_waitcnt lgkmcnt(0)\n\t" \
  "s_lshl_b32 s93, s90, 2\n\t" \
  "s_sub_i32 s93, s89, s93\n\t" \
  "s_cmp_le_i32 s93, 0\n\t" \
  "s_cbranch_scc1 .Lgoo%=\n\t" \
  "s_min_i32 s93, s93, 8000\n\t" \
  "s_mov_b32 s94, s93\n\t" \
  ".Lslo%=:\n\t" \
  "s_sub_i32 s94, s94, 18\n\t" \
  "s_cmp_lt_i32 s94, 30\n\t" \
  "s_cbranch_scc1 .Lsxo%=\n\t" \
  "s_sleep 1\n\t" \
  "s_branch .Lslo%=\n\t" \
  ".Lsxo%=:\n\t" \
  "s_add_u32 s93, s93, 3\n\t" \
  "s_lshr_b32 s93, s93, 2\n\t" \
  "s_add_u32 s93, s93, s90\n\t" \
  "s_memrealtime s[90:91]\n\t" \
  ".Lpaceo%=:\n\t" \
  "s_memrealtime s[94:95]\n\t" \
  "s_waitcnt lgkmcnt(1)\n\t" \
  "s_sub_i32 s92, s90, s93\n\t" \
  "s_cmp_ge_i32 s92, 0\n\t" \
  "s_cbranch_scc1 .Leo%=\n\t" \
  "s_memrealtime s[90:91]\n\t" \
  "s_waitcnt lgkmcnt(1)\n\t" \
  "s_sub_i32 s92, s94, s93\n\t" \
  "s_cmp_lt_i32 s92, 0\n\t" \
  "s_cbranch_scc1 .Lpaceo%=\n\t" \
  ".Leo%=:\n\t" \
  ".Lgoo%=:\n\t" \
  "buffer_store_dwordx4 v[136:139], v200, %[rt], %[so] offen offset:0 sc1\n\t" \
  "buffer_store_dwordx4 v[140:143], v200, %[rt], %[so] offen offset:1024 sc1\n\t" \
  "s_waitcnt lgkmcnt(0)\n\t" \
  "s_branch .Lend%=\n\t" \
  ".Lpoll%=:\n\t" \
  "buffer_load_dwordx4 v[224:227], v200, %[rs], %[sk] offen offset:0 sc1\n\t" \
  "buffer_load_dwordx4 v[228:231], v200, %[rs], %[sk] offen offset:1024 sc1\n\t" \
  "s_branch .Ltake%=\n\t" \
  "s_branch .Lend%=\n\t" \
  ".Lslow%=:\n\t" \
  "s_mov_b64 s[94:95], 0\n\t" \
  "v_cmp_eq_u32 vcc, %[sent], v224\n\t" \
  "s_or_b64 s[94:95], s[94:95], vcc\n\t" \
  "v_cmp_eq_u32 vcc, %[sent], v225\n\t" \
  "s_or_b64 s[94:95], s[94:95], vcc\n\t" \
  "v_cmp_eq_u32 vcc, %[sent], v226\n\t" \
  "s_or_b64 s[94:95], s[94:95], vcc\n\t" \
  "v_cmp_eq_u32 vcc, %[sent], v227\n\t" \
  "s_or_b64 s[94:95], s[94:95], vcc\n\t" \
  "v_cmp_eq_u32 vcc, %[sent], v228\n\t" \
  "s_or_b64 s[94:95], s[94:95], vcc\n\t" \
  "v_cmp_eq_u32 vcc, %[sent], v229\n\t" \
  "s_or_b64 s[94:95], s[94:95], vcc\n\t" \
  "v_cmp_eq_u32 vcc, %[sent], v230\n\t" \
  "s_or_b64 s[94:95], s[94:95], vcc\n\t" \
  "v_cmp_eq_u32 vcc, %[sent], v231\n\t" \
  "s_or_b64 s[94:95], s[94:95], vcc\n\t" \
  "s_cmp_eq_u64 s[94:95], 0\n\t" \
  "s_cbranch_scc1 .Lok%=\n\t" \
  "s_add_u32 s92, s92, 1\n\t" \
  "s_sleep 6\n\t" \
  "s_cmp_eq_u32 s92, 1\n\t" \
  "s_cbranch_scc1 .Lpoll%=\n\t" \
  "s_sleep 1\n\t" \
  "s_and_b32 s93, s92, 63\n\t" \
  "s_cmp_lg_u32 s93, 0\n\t" \
  "s_cbranch_scc1 .Lpoll%=\n\t" \
  "v_mov_b32 v149, 0\n\t" \
  "buffer_load_dword v148, v149, %[rs], %[sab] offen sc1\n\t" \
  "s_waitcnt vmcnt(0)\n\t" \
  "v_readfirstlane_b32 s93, v148\n\t" \
  "s_nop 3\n\t" \
  "s_cmp_eq_u32 s93, 1\n\t" \
  "s_cbranch_scc1 .Lok%=\n\t" \
  "s_cmp_lt_u32 s92, 0x80000\n\t" \
  "s_cbranch_scc1 .Lpoll%=\n\t" \
  "v_mov_b32 v148, 1\n\t" \
  "buffer_store_dword v148, v149, %[rs], %[sab] offen sc1\n\t" \
  "s_branch .Lok%=\n\t" \
  ".Lend%=:\n\t"
constexpr int TLAG = 2;
#define RED_ASM \
  "s_mov_b32 %[step], 0\n\t" \
  "s_lshl_b32 %[sh], %[step], 15\n\t" \
  "s_lshr_b32 s93, %[step], 4\n\t" \
  "s_lshr_b32 s93, %[pm], s93\n\t" \
  "s_and_b32 s93, s93, 4\n\t" \
  "s_lshl_b32 s93, s93, 11\n\t" \
  "s_lshl2_add_u32 %[so], %[sh], s93\n\t" \
  "s_sub_i32 s93, %[step], 2\n\t" \
  "s_lshl_b32 %[st], s93, 19\n\t" \
  "s_lshl_b32 s94, s93, 11\n\t" \
  "s_xor_b32 s94, s94, %[s0]\n\t" \
  "s_and_b32 s94, s94, 0x2000\n\t" \
  "s_or_b32 %[st], %[st], s94\n\t" \
  "s_cmp_lt_i32 s93, 0\n\t" \
  "s_cselect_b32 %[st], 0x70000000, %[st]\n\t" \
  "s_add_u32 %[st4], %[st], 4096\n\t" \
  "buffer_load_dwordx2 v[166:167], v173, %[rs], 0 offen sc1\n\t" \
  "v_mov_b64 v[144:145], 0\n\t" \
  "v_mov_b64 v[146:147], 0\n\t" \
  "v_mov_b64 v[148:149], 0\n\t" \
  "v_mov_b64 v[150:151], 0\n\t" \
  "v_mov_b64 v[152:153], 0\n\t" \
  "v_mov_b64 v[154:155], 0\n\t" \
  "v_mov_b64 v[156:157], 0\n\t" \
  "v_mov_b64 v[158:159], 0\n\t" \
  ".Lstep%=:\n\t" \
  "s_setprio 3\n\t" \
  "s_mov_b32 s92, 0\n\t" \
  ".Lcal%=:\n\t" \
  "s_waitcnt vmcnt(0)\n\t" \
  "s_memrealtime s[76:77]\n\t" \
  "v_cmp_ne_u32 vcc, %[step], v166\n\t" \
  "v_readlane_b32 s90, v167, 0\n\t" \
  "v_readlane_b32 s91, v167, 1\n\t" \
  "v_readlane_b32 s94, v167, 2\n\t" \
  "v_readlane_b32 s95, v167, 3\n\t" \
  "s_sub_i32 s90, s90, %[base]\n\t" \
  "s_sub_i32 s91, s91, %[base]\n\t" \
  "s_sub_i32 s94, s94, %[base]\n\t" \
  "s_sub_i32 s95, s95, %[base]\n\t" \
  "s_max_i32 s90, s90, s91\n\t" \
  "s_max_i32 s94, s94, s95\n\t" \
  "s_max_i32 s90, s90, s94\n\t" \
  "s_add_i32 s90, s90, %[ds]\n\t" \
  "s_waitcnt lgkmcnt(0)\n\t" \
  "s_lshl2_add_u32 s76, s76, %[mn]\n\t" \
  "s_sub_i32 s76, s76, %[base]\n\t" \
  "s_add_u32 %[E], s90, %[base]\n\t" \
  "s_and_b32 s93, vcc_lo, 0xf\n\t" \
  "s_cbranch_scc1 .Lbl%=\n\t" \
  ".Lgo%=:\n\t" \
  "v_mov_b32 v166, 7\n\t" \
  "s_mov_b64 exec, 1\n\t" \
  "v_mov_b32 v164, s90\n\t" \
  "ds_max_u32 v168, v164 offset:4100\n\t" \
  "s_mov_b64 exec, -1\n\t" \
  "s_memrealtime s[90:91]\n\t" \
  "s_waitcnt lgkmcnt(0)\n\t" \
  "s_lshl_b32 s93, s90, 2\n\t" \
  "s_sub_i32 s93, %[E], s93\n\t" \
  "s_cmp_le_i32 s93, 0\n\t" \
  "s_cbranch_scc1 .Lgop%=\n\t" \
  "s_min_i32 s93, s93, 8000\n\t" \
  "s_mov_b32 s94, s93\n\t" \
  ".Lslp%=:\n\t" \
  "s_sub_i32 s94, s94, 18\n\t" \
  "s_cmp_lt_i32 s94, 30\n\t" \
  "s_cbranch_scc1 .Lsxp%=\n\t" \
  "s_sleep 1\n\t" \
  "s_branch .Lslp%=\n\t" \
  ".Lsxp%=:\n\t" \
  "s_sub_i32 s93, s93, 12\n\t" \
  "s_max_i32 s93, s93, 4\n\t" \
  "s_lshr_b32 s93, s93, 2\n\t" \
  "s_add_u32 s93, s93, s90\n\t" \
  "s_memrealtime s[90:91]\n\t" \
  ".Lpacep%=:\n\t" \
  "s_memrealtime s[94:95]\n\t" \
  "s_waitcnt lgkmcnt(1)\n\t" \
  "s_sub_i32 s92, s90, s93\n\t" \
  "s_cmp_ge_i32 s92, 0\n\t" \
  "s_cbranch_scc1 .Lep%=\n\t" \
  "s_memrealtime s[90:91]\n\t" \
  "s_waitcnt lgkmcnt(1)\n\t" \
  "s_sub_i32 s92, s94, s93\n\t" \
  "s_cmp_lt_i32 s92, 0\n\t" \
  "s_cbranch_scc1 .Lpacep%=\n\t" \
  "s_nop 4\n\t" \
  ".Lep%=:\n\t" \
  "s_add_u32 s92, s92, s93\n\t" \
  "s_lshl_b32 s92, s92, 2\n\t" \
  "s_sub_i32 s92, %[E], s92\n\t" \
  "s_max_i32 s92, s92, 0\n\t" \
  "s_bitcmp0_b32 s92, 3\n\t" \
  "s_cbranch_scc1 .Lf3p%=\n\t" \
  "s_nop 15\n\t" \
  ".Lf3p%=:\n\t" \
  "s_bitcmp0_b32 s92, 2\n\t" \
  "s_cbranch_scc1 .Lf2p%=\n\t" \
  "s_nop 10\n\t" \
  ".Lf2p%=:\n\t" \
  ".Lgop%=:\n\t" \
  "s_mov_b32 s92, 0\n\t" \
  "buffer_load_dwordx4 v[128:131], v234, %[rs], %[so] offen sc1\n\t" \
  "s_nop 15\n\t" \
  "buffer_load_dwordx4 v[132:135], v235, %[rs], %[so] offen sc1\n\t" \
  "s_nop 15\n\t" \
  "buffer_load_dwordx4 v[136:139], v236, %[rs], %[so] offen sc1\n\t" \
  "s_nop 15\n\t" \
  "buffer_load_dwordx4 v[140:143], v237, %[rs], %[so] offen sc1\n\t" \
  "s_waitcnt lgkmcnt(0)\n\t" \
  ".Lw%=:\n\t" \
  "s_waitcnt vmcnt(3)\n\t" \
  "v_smfmac_f32_16x16x64_bf16 v[144:147], v[128:131], v[0:7], v172\n\t" \
  "v_max3_u32 v164, v128, v129, 0\n\t" \
  "v_smfmac_f32_16x16x64_bf16 v[148:151], v[128:131], v[8:15], v172\n\t" \
  "v_max3_u32 v164, v130, v131, v164\n\t" \
  "v_smfmac_f32_16x16x64_bf16 v[152:155], v[128:131], v[16:23], v172\n\t" \
  "v_smfmac_f32_16x16x64_bf16 v[156:159], v[128:131], v[24:31], v172\n\t" \
  "buffer_load_dwordx4 v[176:179], v238, %[rt], %[st] offen offset:0 sc1\n\t" \
  "s_waitcnt vmcnt(3)\n\t" \
  "v_smfmac_f32_16x16x64_bf16 v[144:147], v[132:135], v[32:39], v172\n\t" \
  "v_max3_u32 v164, v132, v133, v164\n\t" \
  "v_smfmac_f32_16x16x64_bf16 v[148:151], v[132:135], v[40:47], v172\n\t" \
  "v_max3_u32 v164, v134, v135, v164\n\t" \
  "v_smfmac_f32_16x16x64_bf16 v[152:155], v[132:135], v[48:55], v172\n\t" \
  "v_smfmac_f32_16x16x64_bf16 v[156:159], v[132:135], v[56:63], v172\n\t" \
  "buffer_load_dwordx4 v[180:183], v238, %[rt], %[st] offen offset:1024 sc1\n\t" \
  "s_waitcnt vmcnt(3)\n\t" \
  "v_smfmac_f32_16x16x64_bf16 v[144:147], v[136:139], v[64:71], v172\n\t" \
  "v_max3_u32 v164, v136, v137, v164\n\t" \
  "v_smfmac_f32_16x16x64_bf16 v[148:151], v[136:139], v[72:79], v172\n\t" \
  "v_max3_u32 v164, v138, v139, v164\n\t" \
  "v_smfmac_f32_16x16x64_bf16 v[152:155], v[136:139], v[80:87], v172\n\t" \
  "v_smfmac_f32_16x16x64_bf16 v[156:159], v[136:139], v[88:95], v172\n\t" \
  "buffer_load_dwordx4 v[184:187], v238, %[rt], %[st4] offen offset:0 sc1\n\t" \
  "s_waitcnt vmcnt(3)\n\t" \
  "v_smfmac_f32_16x16x64_bf16 v[144:147], v[140:143], v[96:103], v172\n\t" \
  "v_max3_u32 v164, v140, v141, v164\n\t" \
  "v_smfmac_f32_16x16x64_bf16 v[148:151], v[140:143], v[104:111], v172\n\t" \
  "v_max3_u32 v164, v142, v143, v164\n\t" \
  "v_smfmac_f32_16x16x64_bf16 v[152:155], v[140:143], v[112:119], v172\n\t" \
  "v_cmp_le_u32 vcc, %[sent], v164\n\t" \
  "v_smfmac_f32_16x16x64_bf16 v[156:159], v[140:143], v[120:127], v172\n\t" \
  "buffer_load_dwordx4 v[188:191], v238, %[rt], %[st4] offen offset:1024 sc1\n\t" \
  "s_cbranch_vccnz .Lslow%=\n\t" \
  ".Lok%=:\n\t" \
  "v_add_f32_dpp v160, v144, v146 quad_perm:[0,1,2,3] row_mask:0x3 bank_mask:0xf\n\t" \
  "v_add_f32_dpp v161, v145, v147 quad_perm:[0,1,2,3] row_mask:0x3 bank_mask:0xf\n\t" \
  "v_add_f32_dpp v162, v148, v150 quad_perm:[0,1,2,3] row_mask:0x3 bank_mask:0xf\n\t" \
  "v_add_f32_dpp v163, v149, v151 quad_perm:[0,1,2,3] row_mask:0x3 bank_mask:0xf\n\t" \
  "v_add_f32_dpp v160, v152, v154 quad_perm:[0,1,2,3] row_mask:0xc bank_mask:0xf\n\t" \
  "v_add_f32_dpp v161, v153, v155 quad_perm:[0,1,2,3] row_mask:0xc bank_mask:0xf\n\t" \
  "v_add_f32_dpp v162, v156, v158 quad_perm:[0,1,2,3] row_mask:0xc bank_mask:0xf\n\t" \
  "v_add_f32_dpp v163, v157, v159 quad_perm:[0,1,2,3] row_mask:0xc bank_mask:0xf\n\t" \
  "s_nop 0\n\t" \
  "v_permlane32_swap_b32 v160, v162\n\t" \
  "v_permlane32_swap_b32 v161, v163\n\t" \
  "v_add_f32 v160, v160, v162\n\t" \
  "v_add_f32 v161, v161, v163\n\t" \
  "ds_write_b64 v169, v[160:161]\n\t" \
  "s_mov_b64 exec, 1\n\t" \
  "ds_inc_rtn_u32 v164, v168, v166 offset:4104\n\t" \
  "s_mov_b64 exec, -1\n\t" \
  "ds_read_b64 v[128:129], v170 offset:0\n\t" \
  "ds_read_b64 v[130:131], v170 offset:512\n\t" \
  "ds_read_b64 v[132:133], v170 offset:1024\n\t" \
  "ds_read_b64 v[134:135], v170 offset:1536\n\t" \
  "ds_read_b64 v[136:137], v170 offset:2048\n\t" \
  "ds_read_b64 v[138:139], v170 offset:2560\n\t" \
  "ds_read_b64 v[140:141], v170 offset:3072\n\t" \
  "ds_read_b64 v[142:143], v170 offset:3584\n\t" \
  "s_waitcnt lgkmcnt(8)\n\t" \
  "v_readfirstlane_b32 s93, v164\n\t" \
  "s_cmp_lg_u32 s93, 7\n\t" \
  "s_cbranch_scc1 .Lb1%=\n\t" \
  "s_waitcnt lgkmcnt(4)\n\t" \
  "v_pk_add_f32 v[128:129], v[128:129], v[130:131]\n\t" \
  "v_pk_add_f32 v[132:133], v[132:133], v[134:135]\n\t" \
  "s_waitcnt lgkmcnt(0)\n\t" \
  "v_pk_add_f32 v[136:137], v[136:137], v[138:139]\n\t" \
  "v_pk_add_f32 v[140:141], v[140:141], v[142:143]\n\t" \
  "v_pk_add_f32 v[128:129], v[128:129], v[132:133]\n\t" \
  "v_pk_add_f32 v[136:137], v[136:137], v[140:141]\n\t" \
  "v_pk_add_f32 v[128:129], v[128:129], v[136:137]\n\t" \
  "v_cvt_pk_bf16_f32 v128, v128, v129\n\t" \
  "s_nop 1\n\t" \
  "v_mov_b32_dpp v129, v128 quad_perm:[1,0,3,2] row_mask:0xf bank_mask:0xf\n\t" \
  "v_perm_b32 v128, v129, v128, v174\n\t" \
  "buffer_store_dword v128, v171, %[rs], %[sh] offen sc1\n\t" \
  "s_memtime s[90:91]\n\t" \
  "s_barrier\n\t" \
  "s_memtime s[76:77]\n\t" \
  "s_memrealtime s[78:79]\n\t" \
  "s_nop 3\n\t" \
  "s_memrealtime s[80:81]\n\t" \
  "s_nop 3\n\t" \
  "s_memrealtime s[82:83]\n\t" \
  "s_nop 3\n\t" \
  "s_memrealtime s[84:85]\n\t" \
  "s_nop 3\n\t" \
  "s_memrealtime s[86:87]\n\t" \
  "s_nop 3\n\t" \
  "s_memrealtime s[88:89]\n\t" \
  "s_mov_b32 s93, 0\n\t" \
  "s_waitcnt lgkmcnt(0)\n\t" \
  "s_cmp_eq_u32 s80, s78\n\t" \
  "s_addc_u32 s93, s93, 0\n\t" \
  "s_cmp_eq_u32 s82, s78\n\t" \
  "s_addc_u32 s93, s93, 0\n\t" \
  "s_cmp_eq_u32 s84, s78\n\t" \
  "s_addc_u32 s93, s93, 0\n\t" \
  "s_cmp_eq_u32 s86, s78\n\t" \
  "s_addc_u32 s93, s93, 0\n\t" \
  "s_cmp_eq_u32 s88, s78\n\t" \
  "s_addc_u32 s93, s93, 0\n\t" \
  "s_mul_i32 s93, s93, 21\n\t" \
  "s_add_u32 s76, s76, 15\n\t" \
  "s_add_u32 s76, s76, s93\n\t" \
  "s_lshl2_add_u32 s78, s78, 16\n\t" \
  "s_mov_b64 exec, 1\n\t" \
  "s_sub_i32 s90, s76, s90\n\t" \
  "s_mul_i32 s90, s90, 11900\n\t" \
  "s_ashr_i32 s90, s90, 16\n\t" \
  "s_sub_u32 s90, s78, s90\n\t" \
  "s_sub_u32 s90, s90, %[base]\n\t" \
  "v_mov_b32 v164, s90\n\t" \
  "ds_write_b32 v168, v164 offset:4108\n\t" \
  "s_mov_b64 exec, -1\n\t" \
  "s_waitcnt vmcnt(1)\n\t" \
  ".Lb2%=:\n\t" \
  "s_setprio 0\n\t" \
  "s_bitcmp0_b64 %[am], %[step]\n\t" \
  "s_cbranch_scc1 .La1%=\n\t" \
  "buffer_store_dwordx4 v[224:227], v228, %[ra], 0 offen sc1\n\t" \
  ".La1%=:\n\t" \
  "buffer_load_dwordx4 v[192:195], v239, %[rt], %[st] offen offset:0 sc1\n\t" \
  "buffer_load_dwordx4 v[196:199], v239, %[rt], %[st] offen offset:1024 sc1\n\t" \
  "v_mfma_f32_16x16x32_bf16 v[128:131], v[0:3], v[176:179], 0\n\t" \
  "v_mfma_f32_16x16x32_bf16 v[136:139], v[8:11], v[176:179], 0\n\t" \
  "ds_read2st64_b32 v[144:145], v231 offset0:0 offset1:8\n\t" \
  "ds_read2st64_b32 v[146:147], v231 offset0:16 offset1:24\n\t" \
  "ds_read2st64_b32 v[148:149], v231 offset0:32 offset1:40\n\t" \
  "ds_read2st64_b32 v[150:151], v231 offset0:48 offset1:56\n\t" \
  "buffer_load_dwordx4 v[200:203], v239, %[rt], %[st4] offen offset:0 sc1\n\t" \
  "buffer_load_dwordx4 v[204:207], v239, %[rt], %[st4] offen offset:1024 sc1\n\t" \
  "v_mfma_f32_16x16x32_bf16 v[132:135], v[4:7], v[180:183], 0\n\t" \
  "v_mfma_f32_16x16x32_bf16 v[140:143], v[12:15], v[180:183], 0\n\t" \
  "s_waitcnt lgkmcnt(0)\n\t" \
  "v_add_f32 v144, v144, v145\n\t" \
  "v_add_f32 v146, v146, v147\n\t" \
  "v_add_f32 v148, v148, v149\n\t" \
  "v_add_f32 v150, v150, v151\n\t" \
  "v_add_f32 v144, v144, v146\n\t" \
  "v_add_f32 v148, v148, v150\n\t" \
  "buffer_load_dwordx4 v[208:211], v240, %[rt], %[st] offen offset:0 sc1\n\t" \
  "buffer_load_dwordx4 v[212:215], v240, %[rt], %[st] offen offset:1024 sc1\n\t" \
  "v_mfma_f32_16x16x32_bf16 v[128:131], v[16:19], v[184:187], v[128:131]\n\t" \
  "v_mfma_f32_16x16x32_bf16 v[136:139], v[24:27], v[184:187], v[136:139]\n\t" \
  "v_add_f32 v144, v144, v148\n\t" \
  "v_cvt_pk_bf16_f32 v144, v144, v144\n\t" \
  "ds_write_b16 v232, v144\n\t" \
  "ds_read_b128 v[224:227], v229\n\t" \
  "v_add_u32 v228, %[ainc], v228\n\t" \
  "v_add_u32 v229, %[rinc], v229\n\t" \
  "v_add_u32 v232, 1024, v232\n\t" \
  "v_xor_b32 v231, 0x4000, v231\n\t" \
  "buffer_load_dwordx4 v[216:219], v240, %[rt], %[st4] offen offset:0 sc1\n\t" \
  "buffer_load_dwordx4 v[220:223], v240, %[rt], %[st4] offen offset:1024 sc1\n\t" \
  "v_mfma_f32_16x16x32_bf16 v[132:135], v[20:23], v[188:191], v[132:135]\n\t" \
  "v_mfma_f32_16x16x32_bf16 v[140:143], v[28:31], v[188:191], v[140:143]\n\t" \
  "buffer_load_dwordx4 v[176:179], v241, %[rt], %[st] offen offset:0 sc1\n\t" \
  "s_waitcnt vmcnt(8)\n\t" \
  "v_mfma_f32_16x16x32_bf16 v[128:131], v[32:35], v[192:195], v[128:131]\n\t" \
  "v_mfma_f32_16x16x32_bf16 v[136:139], v[40:43], v[192:195], v[136:139]\n\t" \
  "buffer_load_dwordx4 v[180:183], v241, %[rt], %[st] offen offset:1024 sc1\n\t" \
  "s_waitcnt vmcnt(8)\n\t" \
  "v_mfma_f32_16x16x32_bf16 v[132:135], v[36:39], v[196:199], v[132:135]\n\t" \
  "v_mfma_f32_16x16x32_bf16 v[140:143], v[44:47], v[196:199], v[140:143]\n\t" \
  "buffer_load_dwordx4 v[184:187], v241, %[rt], %[st4] offen offset:0 sc1\n\t" \
  "s_waitcnt vmcnt(8)\n\t" \
  "v_mfma_f32_16x16x32_bf16 v[128:131], v[48:51], v[200:203], v[128:131]\n\t" \
  "v_mfma_f32_16x16x32_bf16 v[136:139], v[56:59], v[200:203], v[136:139]\n\t" \
  "ds_read_b128 v[160:163], v168 offset:4096\n\t" \
  "buffer_load_dwordx4 v[188:191], v241, %[rt], %[st4] offen offset:1024 sc1\n\t" \
  "s_waitcnt vmcnt(8)\n\t" \
  "v_mfma_f32_16x16x32_bf16 v[132:135], v[52:55], v[204:207], v[132:135]\n\t" \
  "v_mfma_f32_16x16x32_bf16 v[140:143], v[60:63], v[204:207], v[140:143]\n\t" \
  "s_waitcnt lgkmcnt(0)\n\t" \
  "v_readfirstlane_b32 s90, v161\n\t" \
  "v_readfirstlane_b32 s91, v163\n\t" \
  "s_add_u32 s93, s90, %[rl]\n\t" \
  "s_add_u32 s93, s93, %[base]\n\t" \
  "s_add_u32 %[E], s93, %[lk]\n\t" \
  "s_sub_i32 s91, s91, s90\n\t" \
  "s_sub_i32 s91, s91, %[rl]\n\t" \
  "s_min_i32 s93, s91, %[up]\n\t" \
  "s_max_i32 s93, s93, %[dn]\n\t" \
  "s_cmp_gt_i32 s91, 60\n\t" \
  "s_cselect_b32 s93, 2, s93\n\t" \
  "s_add_i32 %[rl], %[rl], s93\n\t" \
  "v_mov_b32 v160, %[rl]\n\t" \
  "buffer_store_dword v160, v175, %[rs], 0 offen sc1\n\t" \
  "s_waitcnt vmcnt(8)\n\t" \
  "v_mfma_f32_16x16x32_bf16 v[128:131], v[64:67], v[208:211], v[128:131]\n\t" \
  "v_mfma_f32_16x16x32_bf16 v[136:139], v[72:75], v[208:211], v[136:139]\n\t" \
  "s_add_u32 %[step], %[step], 1\n\t" \
  "s_lshl_b32 %[sh], %[step], 15\n\t" \
  "s_lshr_b32 s93, %[step], 4\n\t" \
  "s_lshr_b32 s93, %[pm], s93\n\t" \
  "s_and_b32 s93, s93, 4\n\t" \
  "s_lshl_b32 s93, s93, 11\n\t" \
  "s_lshl2_add_u32 %[so], %[sh], s93\n\t" \
  "s_waitcnt vmcnt(7)\n\t" \
  "v_mfma_f32_16x16x32_bf16 v[132:135], v[68:71], v[212:215], v[132:135]\n\t" \
  "v_mfma_f32_16x16x32_bf16 v[140:143], v[76:79], v[212:215], v[140:143]\n\t" \
  "s_sub_i32 s93, %[step], 2\n\t" \
  "s_lshl_b32 %[st], s93, 19\n\t" \
  "s_lshl_b32 s94, s93, 11\n\t" \
  "s_xor_b32 s94, s94, %[s0]\n\t" \
  "s_and_b32 s94, s94, 0x2000\n\t" \
  "s_or_b32 %[st], %[st], s94\n\t" \
  "s_cmp_lt_i32 s93, 0\n\t" \
  "s_cselect_b32 %[st], 0x70000000, %[st]\n\t" \
  "s_add_u32 %[st4], %[st], 4096\n\t" \
  "s_waitcnt vmcnt(6)\n\t" \
  "v_mfma_f32_16x16x32_bf16 v[128:131], v[80:83], v[216:219], v[128:131]\n\t" \
  "v_mfma_f32_16x16x32_bf16 v[136:139], v[88:91], v[216:219], v[136:139]\n\t" \
  "s_waitcnt vmcnt(5)\n\t" \
  "v_mfma_f32_16x16x32_bf16 v[132:135], v[84:87], v[220:223], v[132:135]\n\t" \
  "v_mfma_f32_16x16x32_bf16 v[140:143], v[92:95], v[220:223], v[140:143]\n\t" \
  "s_cmp_eq_u32 %[step], 63\n\t" \
  "s_cbranch_scc1 .Llast%=\n\t" \
  "s_movk_i32 s93, 600\n\t" \
  ".Lpacel%=:\n\t" \
  "s_memrealtime s[90:91]\n\t" \
  "s_waitcnt lgkmcnt(0)\n\t" \
  "s_lshl_b32 s90, s90, 2\n\t" \
  "s_sub_i32 s91, %[E], s90\n\t" \
  "s_cmp_le_i32 s91, 0\n\t" \
  "s_cbranch_scc1 .Lgol%=\n\t" \
  "s_sub_u32 s93, s93, 1\n\t" \
  "s_cmp_lg_u32 s93, 0\n\t" \
  "s_cbranch_scc1 .Lpacel%=\n\t" \
  ".Lgol%=:\n\t" \
  "buffer_load_dwordx2 v[166:167], v173, %[rs], 0 offen sc1\n\t" \
  "s_waitcnt vmcnt(5)\n\t" \
  "v_mfma_f32_16x16x32_bf16 v[128:131], v[96:99], v[176:179], v[128:131]\n\t" \
  "v_mfma_f32_16x16x32_bf16 v[136:139], v[104:107], v[176:179], v[136:139]\n\t" \
  "s_waitcnt vmcnt(4)\n\t" \
  "v_mfma_f32_16x16x32_bf16 v[132:135], v[100:103], v[180:183], v[132:135]\n\t" \
  "v_mfma_f32_16x16x32_bf16 v[140:143], v[108:111], v[180:183], v[140:143]\n\t" \
  "s_waitcnt vmcnt(3)\n\t" \
  "v_mfma_f32_16x16x32_bf16 v[128:131], v[112:115], v[184:187], v[128:131]\n\t" \
  "v_mfma_f32_16x16x32_bf16 v[136:139], v[120:123], v[184:187], v[136:139]\n\t" \
  "s_waitcnt vmcnt(2)\n\t" \
  "v_mfma_f32_16x16x32_bf16 v[132:135], v[116:119], v[188:191], v[132:135]\n\t" \
  "v_mfma_f32_16x16x32_bf16 v[140:143], v[124:127], v[188:191], v[140:143]\n\t" \
  "v_mov_b64 v[144:145], 0\n\t" \
  "v_mov_b64 v[146:147], 0\n\t" \
  "v_mov_b64 v[148:149], 0\n\t" \
  "v_mov_b64 v[150:151], 0\n\t" \
  "v_mov_b64 v[152:153], 0\n\t" \
  "v_mov_b64 v[154:155], 0\n\t" \
  "v_mov_b64 v[156:157], 0\n\t" \
  "v_mov_b64 v[158:159], 0\n\t" \
  "v_pk_add_f32 v[128:129], v[128:129], v[132:133]\n\t" \
  "v_pk_add_f32 v[130:131], v[130:131], v[134:135]\n\t" \
  "ds_write_b128 v230, v[128:131] offset:0\n\t" \
  "v_pk_add_f32 v[136:137], v[136:137], v[140:141]\n\t" \
  "v_pk_add_f32 v[138:139], v[138:139], v[142:143]\n\t" \
  "ds_write_b128 v230, v[136:139] offset:1024\n\t" \
  "v_xor_b32 v230, 0x4000, v230\n\t" \
  "s_cmp_lg_u32 %[step], 63\n\t" \
  "s_cbranch_scc1 .Lstep%=\n\t" \
  ".Lfc%=:\n\t" \
  "s_waitcnt vmcnt(0)\n\t" \
  "v_cmp_eq_u32 vcc, -1, v166\n\t" \
  "s_cbranch_vccnz .Lfw%=\n\t" \
  ".Lfo%=:\n\t" \
  "v_add_u32 v242, 16384, v242\n\t" \
  "buffer_load_dwordx4 v[176:179], v238, %[rt], %[st] offen offset:0 sc1\n\t" \
  "buffer_load_dwordx4 v[180:183], v238, %[rt], %[st] offen offset:1024 sc1\n\t" \
  "buffer_load_dwordx4 v[184:187], v238, %[rt], %[st4] offen offset:0 sc1\n\t" \
  "buffer_load_dwordx4 v[188:191], v238, %[rt], %[st4] offen offset:1024 sc1\n\t" \
  "buffer_load_dwordx4 v[192:195], v239, %[rt], %[st] offen offset:0 sc1\n\t" \
  "buffer_load_dwordx4 v[196:199], v239, %[rt], %[st] offen offset:1024 sc1\n\t" \
  "buffer_load_dwordx4 v[200:203], v239, %[rt], %[st4] offen offset:0 sc1\n\t" \
  "buffer_load_dwordx4 v[204:207], v239, %[rt], %[st4] offen offset:1024 sc1\n\t" \
  "v_subrev_u32 v230, 98304, v230\n\t" \
  ".Lt%=:\n\t" \
  "s_waitcnt vmcnt(7)\n\t" \
  "v_mfma_f32_16x16x32_bf16 v[128:131], v[0:3], v[176:179], 0\n\t" \
  "v_mfma_f32_16x16x32_bf16 v[136:139], v[8:11], v[176:179], 0\n\t" \
  "buffer_load_dwordx4 v[176:179], v240, %[rt], %[st] offen offset:0 sc1\n\t" \
  "buffer_load_dword v166, v242, %[rs], 0 offen sc1\n\t" \
  "s_waitcnt vmcnt(8)\n\t" \
  "v_mfma_f32_16x16x32_bf16 v[132:135], v[4:7], v[180:183], 0\n\t" \
  "v_mfma_f32_16x16x32_bf16 v[140:143], v[12:15], v[180:183], 0\n\t" \
  "buffer_load_dwordx4 v[180:183], v240, %[rt], %[st] offen offset:1024 sc1\n\t" \
  "s_waitcnt vmcnt(8)\n\t" \
  "v_mfma_f32_16x16x32_bf16 v[128:131], v[16:19], v[184:187], v[128:131]\n\t" \
  "v_mfma_f32_16x16x32_bf16 v[136:139], v[24:27], v[184:187], v[136:139]\n\t" \
  "buffer_load_dwordx4 v[184:187], v240, %[rt], %[st4] offen offset:0 sc1\n\t" \
  "s_waitcnt vmcnt(8)\n\t" \
  "v_mfma_f32_16x16x32_bf16 v[132:135], v[20:23], v[188:191], v[132:135]\n\t" \
  "v_mfma_f32_16x16x32_bf16 v[140:143], v[28:31], v[188:191], v[140:143]\n\t" \
  "buffer_load_dwordx4 v[188:191], v240, %[rt], %[st4] offen offset:1024 sc1\n\t" \
  "s_waitcnt vmcnt(8)\n\t" \
  "v_mfma_f32_16x16x32_bf16 v[128:131], v[32:35], v[192:195], v[128:131]\n\t" \
  "v_mfma_f32_16x16x32_bf16 v[136:139], v[40:43], v[192:195], v[136:139]\n\t" \
  "buffer_load_dwordx4 v[192:195], v241, %[rt], %[st] offen offset:0 sc1\n\t" \
  "s_waitcnt vmcnt(8)\n\t" \
  "v_mfma_f32_16x16x32_bf16 v[132:135], v[36:39], v[196:199], v[132:135]\n\t" \
  "v_mfma_f32_16x16x32_bf16 v[140:143], v[44:47], v[196:199], v[140:143]\n\t" \
  "buffer_load_dwordx4 v[196:199], v241, %[rt], %[st] offen offset:1024 sc1\n\t" \
  "s_waitcnt vmcnt(8)\n\t" \
  "v_mfma_f32_16x16x32_bf16 v[128:131], v[48:51], v[200:203], v[128:131]\n\t" \
  "v_mfma_f32_16x16x32_bf16 v[136:139], v[56:59], v[200:203], v[136:139]\n\t" \
  "buffer_load_dwordx4 v[200:203], v241, %[rt], %[st4] offen offset:0 sc1\n\t" \
  "s_waitcnt vmcnt(8)\n\t" \
  "v_mfma_f32_16x16x32_bf16 v[132:135], v[52:55], v[204:207], v[132:135]\n\t" \
  "v_mfma_f32_16x16x32_bf16 v[140:143], v[60:63], v[204:207], v[140:143]\n\t" \
  "buffer_load_dwordx4 v[204:207], v241, %[rt], %[st4] offen offset:1024 sc1\n\t" \
  "s_waitcnt vmcnt(8)\n\t" \
  "v_mfma_f32_16x16x32_bf16 v[128:131], v[64:67], v[176:179], v[128:131]\n\t" \
  "v_mfma_f32_16x16x32_bf16 v[136:139], v[72:75], v[176:179], v[136:139]\n\t" \
  "s_waitcnt vmcnt(7)\n\t" \
  ".Ltc%=:\n\t" \
  "v_cmp_eq_u32 vcc, -1, v166\n\t" \
  "s_cbranch_vccnz .Ltw%=\n\t" \
  ".Lto%=:\n\t" \
  "s_cmp_eq_u32 %[step], 65\n\t" \
  "s_cbranch_scc1 .Ltl%=\n\t" \
  "s_add_u32 %[step], %[step], 1\n\t" \
  "s_sub_i32 s93, %[step], 2\n\t" \
  "s_lshl_b32 %[st], s93, 19\n\t" \
  "s_lshl_b32 s94, s93, 11\n\t" \
  "s_xor_b32 s94, s94, %[s0]\n\t" \
  "s_and_b32 s94, s94, 0x2000\n\t" \
  "s_or_b32 %[st], %[st], s94\n\t" \
  "s_cmp_lt_i32 s93, 0\n\t" \
  "s_cselect_b32 %[st], 0x70000000, %[st]\n\t" \
  "s_add_u32 %[st4], %[st], 4096\n\t" \
  "v_add_u32 v242, 16384, v242\n\t" \
  "s_cmp_eq_u32 %[step], 65\n\t" \
  "s_cbranch_scc0 .Ltn%=\n\t" \
  "v_mov_b32 v242, %[fg]\n\t" \
  ".Ltn%=:\n\t" \
  "buffer_load_dwordx4 v[176:179], v238, %[rt], %[st] offen offset:0 sc1\n\t" \
  "s_waitcnt vmcnt(7)\n\t" \
  "v_mfma_f32_16x16x32_bf16 v[132:135], v[68:71], v[180:183], v[132:135]\n\t" \
  "v_mfma_f32_16x16x32_bf16 v[140:143], v[76:79], v[180:183], v[140:143]\n\t" \
  "buffer_load_dwordx4 v[180:183], v238, %[rt], %[st] offen offset:1024 sc1\n\t" \
  "s_waitcnt vmcnt(7)\n\t" \
  "v_mfma_f32_16x16x32_bf16 v[128:131], v[80:83], v[184:187], v[128:131]\n\t" \
  "v_mfma_f32_16x16x32_bf16 v[136:139], v[88:91], v[184:187], v[136:139]\n\t" \
  "buffer_load_dwordx4 v[184:187], v238, %[rt], %[st4] offen offset:0 sc1\n\t" \
  "s_waitcnt vmcnt(7)\n\t" \
  "v_mfma_f32_16x16x32_bf16 v[132:135], v[84:87], v[188:191], v[132:135]\n\t" \
  "v_mfma_f32_16x16x32_bf16 v[140:143], v[92:95], v[188:191], v[140:143]\n\t" \
  "buffer_load_dwordx4 v[188:191], v238, %[rt], %[st4] offen offset:1024 sc1\n\t" \
  "s_waitcnt vmcnt(7)\n\t" \
  "v_mfma_f32_16x16x32_bf16 v[128:131], v[96:99], v[192:195], v[128:131]\n\t" \
  "v_mfma_f32_16x16x32_bf16 v[136:139], v[104:107], v[192:195], v[136:139]\n\t" \
  "buffer_load_dwordx4 v[192:195], v239, %[rt], %[st] offen offset:0 sc1\n\t" \
  "s_waitcnt vmcnt(7)\n\t" \
  "v_mfma_f32_16x16x32_bf16 v[132:135], v[100:103], v[196:199], v[132:135]\n\t" \
  "v_mfma_f32_16x16x32_bf16 v[140:143], v[108:111], v[196:199], v[140:143]\n\t" \
  "buffer_load_dwordx4 v[196:199], v239, %[rt], %[st] offen offset:1024 sc1\n\t" \
  "s_waitcnt vmcnt(7)\n\t" \
  "v_mfma_f32_16x16x32_bf16 v[128:131], v[112:115], v[200:203], v[128:131]\n\t" \
  "v_mfma_f32_16x16x32_bf16 v[136:139], v[120:123], v[200:203], v[136:139]\n\t" \
  "buffer_load_dwordx4 v[200:203], v239, %[rt], %[st4] offen offset:0 sc1\n\t" \
  "s_waitcnt vmcnt(7)\n\t" \
  "v_mfma_f32_16x16x32_bf16 v[132:135], v[116:119], v[204:207], v[132:135]\n\t" \
  "v_mfma_f32_16x16x32_bf16 v[140:143], v[124:127], v[204:207], v[140:143]\n\t" \
  "buffer_load_dwordx4 v[204:207], v239, %[rt], %[st4] offen offset:1024 sc1\n\t" \
  "s_nop 5\n\t" \
  "v_pk_add_f32 v[128:129], v[128:129], v[132:133]\n\t" \
  "v_pk_add_f32 v[130:131], v[130:131], v[134:135]\n\t" \
  "ds_write_b128 v230, v[128:131] offset:0\n\t" \
  "v_pk_add_f32 v[136:137], v[136:137], v[140:141]\n\t" \
  "v_pk_add_f32 v[138:139], v[138:139], v[142:143]\n\t" \
  "ds_write_b128 v230, v[136:139] offset:1024\n\t" \
  "v_add_u32 v230, 16384, v230\n\t" \
  "s_branch .Lt%=\n\t" \
  ".Ltl%=:\n\t" \
  "buffer_load_dwordx4 v[224:227], v243, %[rg], 0 offen sc1\n\t" \
  "s_waitcnt vmcnt(7)\n\t" \
  "v_mfma_f32_16x16x32_bf16 v[132:135], v[68:71], v[180:183], v[132:135]\n\t" \
  "v_mfma_f32_16x16x32_bf16 v[140:143], v[76:79], v[180:183], v[140:143]\n\t" \
  "s_waitcnt vmcnt(6)\n\t" \
  "v_mfma_f32_16x16x32_bf16 v[128:131], v[80:83], v[184:187], v[128:131]\n\t" \
  "v_mfma_f32_16x16x32_bf16 v[136:139], v[88:91], v[184:187], v[136:139]\n\t" \
  "s_waitcnt vmcnt(5)\n\t" \
  "v_mfma_f32_16x16x32_bf16 v[132:135], v[84:87], v[188:191], v[132:135]\n\t" \
  "v_mfma_f32_16x16x32_bf16 v[140:143], v[92:95], v[188:191], v[140:143]\n\t" \
  "s_waitcnt vmcnt(4)\n\t" \
  "v_mfma_f32_16x16x32_bf16 v[128:131], v[96:99], v[192:195], v[128:131]\n\t" \
  "v_mfma_f32_16x16x32_bf16 v[136:139], v[104:107], v[192:195], v[136:139]\n\t" \
  "s_waitcnt vmcnt(3)\n\t" \
  "v_mfma_f32_16x16x32_bf16 v[132:135], v[100:103], v[196:199], v[132:135]\n\t" \
  "v_mfma_f32_16x16x32_bf16 v[140:143], v[108:111], v[196:199], v[140:143]\n\t" \
  "s_waitcnt vmcnt(2)\n\t" \
  "v_mfma_f32_16x16x32_bf16 v[128:131], v[112:115], v[200:203], v[128:131]\n\t" \
  "v_mfma_f32_16x16x32_bf16 v[136:139], v[120:123], v[200:203], v[136:139]\n\t" \
  "s_waitcnt vmcnt(1)\n\t" \
  "v_mfma_f32_16x16x32_bf16 v[132:135], v[116:119], v[204:207], v[132:135]\n\t" \
  "v_mfma_f32_16x16x32_bf16 v[140:143], v[124:127], v[204:207], v[140:143]\n\t" \
  "s_nop 6\n\t" \
  "v_pk_add_f32 v[128:129], v[128:129], v[132:133]\n\t" \
  "v_pk_add_f32 v[130:131], v[130:131], v[134:135]\n\t" \
  "ds_write_b128 v230, v[128:131] offset:0\n\t" \
  "v_pk_add_f32 v[136:137], v[136:137], v[140:141]\n\t" \
  "v_pk_add_f32 v[138:139], v[138:139], v[142:143]\n\t" \
  "ds_write_b128 v230, v[136:139] offset:1024\n\t" \
  "s_waitcnt vmcnt(0) lgkmcnt(0)\n\t" \
  "s_branch .Lend%=\n\t" \
  ".Ltw%=:\n\t" \
  "s_add_u32 s92, s92, 1\n\t" \
  "s_sleep 2\n\t" \
  "s_and_b32 s93, s92, 63\n\t" \
  "s_cbranch_scc1 .Ltr%=\n\t" \
  "buffer_load_dword v164, off, %[rs], %[sab] sc1\n\t" \
  "s_waitcnt vmcnt(0)\n\t" \
  "v_readfirstlane_b32 s93, v164\n\t" \
  "s_nop 3\n\t" \
  "s_cmp_eq_u32 s93, 1\n\t" \
  "s_cbranch_scc1 .Lto%=\n\t" \
  "s_cmp_lt_u32 s92, 0x80000\n\t" \
  "s_cbranch_scc1 .Ltr%=\n\t" \
  "v_mov_b32 v164, 1\n\t" \
  "buffer_store_dword v164, off, %[rs], %[sab] sc1\n\t" \
  "s_waitcnt vmcnt(0)\n\t" \
  "s_branch .Lto%=\n\t" \
  ".Ltr%=:\n\t" \
  "buffer_load_dword v166, v242, %[rs], 0 offen sc1\n\t" \
  "s_waitcnt vmcnt(0)\n\t" \
  "s_branch .Ltc%=\n\t" \
  ".Llast%=:\n\t" \
  "v_mov_b32 v173, v242\n\t" \
  "s_branch .Lgol%=\n\t" \
  ".Lfw%=:\n\t" \
  "s_add_u32 s92, s92, 1\n\t" \
  "s_sleep 2\n\t" \
  "s_and_b32 s93, s92, 63\n\t" \
  "s_cbranch_scc1 .Lfr%=\n\t" \
  "buffer_load_dword v164, off, %[rs], %[sab] sc1\n\t" \
  "s_waitcnt vmcnt(0)\n\t" \
  "v_readfirstlane_b32 s93, v164\n\t" \
  "s_nop 3\n\t" \
  "s_cmp_eq_u32 s93, 1\n\t" \
  "s_cbranch_scc1 .Lfo%=\n\t" \
  "s_cmp_lt_u32 s92, 0x80000\n\t" \
  "s_cbranch_scc1 .Lfr%=\n\t" \
  "v_mov_b32 v164, 1\n\t" \
  "buffer_store_dword v164, off, %[rs], %[sab] sc1\n\t" \
  "s_waitcnt vmcnt(0)\n\t" \
  "s_branch .Lfo%=\n\t" \
  ".Lfr%=:\n\t" \
  "buffer_load_dwordx2 v[166:167], v173, %[rs], 0 offen sc1\n\t" \
  "s_branch .Lfc%=\n\t" \
  ".Lb1%=:\n\t" \
  "s_waitcnt vmcnt(0) lgkmcnt(0)\n\t" \
  "s_barrier\n\t" \
  "s_branch .Lb2%=\n\t" \
  ".Lbl%=:\n\t" \
  "s_bitset1_b64 %[ps], %[step]\n\t" \
  "s_cmp_lt_u32 s92, 3\n\t" \
  "s_cbranch_scc1 .Lbm%=\n\t" \
  "s_sleep 12\n\t" \
  ".Lbm%=:\n\t" \
  "s_add_u32 s92, s92, 1\n\t" \
  "s_sleep 2\n\t" \
  "s_and_b32 s93, s92, 63\n\t" \
  "s_cbranch_scc1 .Lba%=\n\t" \
  "buffer_load_dword v164, off, %[rs], %[sab] sc1\n\t" \
  "s_waitcnt vmcnt(0)\n\t" \
  "v_readfirstlane_b32 s93, v164\n\t" \
  "s_nop 3\n\t" \
  "s_cmp_eq_u32 s93, 1\n\t" \
  "s_cbranch_scc1 .Lgo%=\n\t" \
  "s_cmp_lt_u32 s92, 0x80000\n\t" \
  "s_cbranch_scc1 .Lba%=\n\t" \
  "v_mov_b32 v164, 1\n\t" \
  "buffer_store_dword v164, off, %[rs], %[sab] sc1\n\t" \
  "s_waitcnt vmcnt(0)\n\t" \
  "s_branch .Lgo%=\n\t" \
  ".Lba%=:\n\t" \
  "buffer_load_dwordx2 v[166:167], v173, %[rs], 0 offen sc1\n\t" \
  "s_branch .Lcal%=\n\t" \
  ".Lslow%=:\n\t" \
  "s_mov_b64 s[94:95], 0\n\t" \
  "v_cmp_eq_u32 vcc, %[sent], v128\n\t" \
  "s_or_b64 s[94:95], s[94:95], vcc\n\t" \
  "v_cmp_eq_u32 vcc, %[sent], v129\n\t" \
  "s_or_b64 s[94:95], s[94:95], vcc\n\t" \
  "v_cmp_eq_u32 vcc, %[sent], v130\n\t" \
  "s_or_b64 s[94:95], s[94:95], vcc\n\t" \
  "v_cmp_eq_u32 vcc, %[sent], v131\n\t" \
  "s_or_b64 s[94:95], s[94:95], vcc\n\t" \
  "v_cmp_eq_u32 vcc, %[sent], v132\n\t" \
  "s_or_b64 s[94:95], s[94:95], vcc\n\t" \
  "v_cmp_eq_u32 vcc, %[sent], v133\n\t" \
  "s_or_b64 s[94:95], s[94:95], vcc\n\t" \
  "v_cmp_eq_u32 vcc, %[sent], v134\n\t" \
  "s_or_b64 s[94:95], s[94:95], vcc\n\t" \
  "v_cmp_eq_u32 vcc, %[sent], v135\n\t" \
  "s_or_b64 s[94:95], s[94:95], vcc\n\t" \
  "v_cmp_eq_u32 vcc, %[sent], v136\n\t" \
  "s_or_b64 s[94:95], s[94:95], vcc\n\t" \
  "v_cmp_eq_u32 vcc, %[sent], v137\n\t" \
  "s_or_b64 s[94:95], s[94:95], vcc\n\t" \
  "v_cmp_eq_u32 vcc, %[sent], v138\n\t" \
  "s_or_b64 s[94:95], s[94:95], vcc\n\t" \
  "v_cmp_eq_u32 vcc, %[sent], v139\n\t" \
  "s_or_b64 s[94:95], s[94:95], vcc\n\t" \
  "v_cmp_eq_u32 vcc, %[sent], v140\n\t" \
  "s_or_b64 s[94:95], s[94:95], vcc\n\t" \
  "v_cmp_eq_u32 vcc, %[sent], v141\n\t" \
  "s_or_b64 s[94:95], s[94:95], vcc\n\t" \
  "v_cmp_eq_u32 vcc, %[sent], v142\n\t" \
  "s_or_b64 s[94:95], s[94:95], vcc\n\t" \
  "v_cmp_eq_u32 vcc, %[sent], v143\n\t" \
  "s_or_b64 s[94:95], s[94:95], vcc\n\t" \
  "s_cbranch_scc0 .Lok%=\n\t" \
  "s_bitset1_b64 %[fm], %[step]\n\t" \
  "s_add_u32 s92, s92, 1\n\t" \
  "s_sleep 1\n\t" \
  "s_and_b32 s93, s92, 63\n\t" \
  "s_cbranch_scc1 .Lre%=\n\t" \
  "buffer_load_dword v164, off, %[rs], %[sab] sc1\n\t" \
  "s_waitcnt vmcnt(0)\n\t" \
  "v_readfirstlane_b32 s93, v164\n\t" \
  "s_nop 3\n\t" \
  "s_cmp_eq_u32 s93, 1\n\t" \
  "s_cbranch_scc1 .Lok%=\n\t" \
  "s_cmp_lt_u32 s92, 0x80000\n\t" \
  "s_cbranch_scc1 .Lre%=\n\t" \
  "v_mov_b32 v164, 1\n\t" \
  "buffer_store_dword v164, off, %[rs], %[sab] sc1\n\t" \
  "s_waitcnt vmcnt(0)\n\t" \
  "s_branch .Lok%=\n\t" \
  ".Lre%=:\n\t" \
  "buffer_load_dwordx4 v[128:131], v234, %[rs], %[so] offen sc1\n\t" \
  "s_nop 15\n\t" \
  "buffer_load_dwordx4 v[132:135], v235, %[rs], %[so] offen sc1\n\t" \
  "s_nop 15\n\t" \
  "buffer_load_dwordx4 v[136:139], v236, %[rs], %[so] offen sc1\n\t" \
  "s_nop 15\n\t" \
  "buffer_load_dwordx4 v[140:143], v237, %[rs], %[so] offen sc1\n\t" \
  "v_mov_b64 v[144:145], 0\n\t" \
  "v_mov_b64 v[146:147], 0\n\t" \
  "v_mov_b64 v[148:149], 0\n\t" \
  "v_mov_b64 v[150:151], 0\n\t" \
  "v_mov_b64 v[152:153], 0\n\t" \
  "v_mov_b64 v[154:155], 0\n\t" \
  "v_mov_b64 v[156:157], 0\n\t" \
  "v_mov_b64 v[158:159], 0\n\t" \
  "s_waitcnt vmcnt(0)\n\t" \
  "s_branch .Lw%=\n\t" \
  ".Lend%=:\n\t"

__device__ __forceinline__ bf16x8 get4(const u32x32& W, int i) {
  return as_bf16x8((u32x4){W[4 * i], W[4 * i + 1], W[4 * i + 2], W[4 * i + 3]});
}
__device__ __forceinline__ int look_prog(__amdgpu_buffer_rsrc_t rs, int off) {
  asm volatile("" ::: "memory");
  const int v = (int)ld_flag(rs, off);
  return min(__builtin_amdgcn_readlane(v, 0), __builtin_amdgcn_readlane(v, 1));
}
__device__ __forceinline__ int wait_prog(__amdgpu_buffer_rsrc_t rs, int off, int need, gu32* abortw) {
  int v;
  for (unsigned spins = 0;;) {
    v = look_prog(rs, off);
    if (v >= need) break;
    __builtin_amdgcn_s_sleep(4);
    if (spin_fail(spins, abortw)) break;
  }
  asm volatile("" ::: "memory");
  return v;
}
#define CLB_S "s90", "s91", "s92", "s93", "s94", "s95", "vcc", "scc", "memory"
#define W_IN "{v[0:31]}"(W[0]), "{v[32:63]}"(W[1]), "{v[64:95]}"(W[2]), "{v[96:127]}"(W[3]), "{v[128:159]}"(W[4]), "{v[160:191]}"(W[5])

#define W_DELAY 0
#define LEAD0 395
#define LEAD_D0 6
#define OX4 8
#define T0_BACK 1000
#define QBJ 3
constexpr int L_MEM = 0, L_KT = 16384, L_QX = 24576, L_KX = 26624, L_VX = 16384, L_MISC = 30720, L_WL = 32768;

#define QK_STEP(TXT)                                                                                                        \
  asm volatile(TXT                                                                                                          \
      : [tout] "=&s"(tout), [spins] "=&s"(spins), [tb1] "=&s"(tb1), [tgt] "=&s"(tgt), "+{v248}"(aL16), "+{v249}"(aMW), "+{v250}"(aWL),                            \
        "+{v251}"(aIDX), "+{v252}"(aQW), "+{v[232:235]}"(QF)                                                                 \
      : W_IN, [rs] "s"(rs4), [som] "s"(o_mem(pm, s) + WV * 1024), [som2] "s"(o_mem(pm, s) + WV * 1024 + 16384), [sent] "s"(DSENT), [sab] "s"(SY_ABORT), \
        [lead] "s"(lead), [step] "s"(s), [sbc] "s"(SY_BA + side(pm, 1) + h * 128), [nh] "s"(0xFF00FF00FF00FF00ull), [up] "s"(0xFFFFFFFF00000000ull), [negc] "s"(-cexp),      \
        [c] "s"(cexp), [so] "s"(o_o(pm, s, h) + WV * 512), [sk] "s"(o_v(pm, s, h) + 2048 + (WV & 3) * 512),                    \
        [sv] "s"(o_v(pm, s, h) + WV * 512), [rt] "s"(rt4), [svt] "s"(t_off(p, T_V + kseg(s, h)) + WV * 2048)                  \
      : CLB8(192, 193, 194, 195, 196, 197, 198, 199), CLB8(200, 201, 202, 203, 204, 205, 206, 207),                          \
        CLB8(208, 209, 210, 211, 212, 213, 214, 215), CLB8(216, 217, 218, 219, 220, 221, 222, 223),                          \
        CLB8(224, 225, 226, 227, 228, 229, 230, 231), CLB8(236, 237, 238, 239, 240, 241, 242, 243), "v244", "v245", "v246", "v247", "v253", "s88", "s89", CLB_S)

template <int WV>
__device__ __forceinline__ void qk_wave(Args& p, int h, char* smem) {
  const int tid = threadIdx.x;
  unsigned pm = 0;
  int have = -1;
  u32x32 W[6];
  {
    int lane = tid & 63;
    asm volatile("" : "+v"(lane));
    const unsigned vo = (WV * 4 + (lane >> 4)) * (HD * 2) + 16 * ((lane & 15) ^ swzB((WV * 4 + (lane >> 4)) & 15));
    auto stage = [&](int j) {
#pragma unroll
      for (int mm = 0; mm < 2; ++mm) {
        const char* src = (const char*)((mm ? p.Wk : p.Wq) + (size_t)j * 128 * HD + h * DH);
        const unsigned dst = lds_off(smem) + (~j & 1) * 65536 + mm * 32768 + WV * 1024;
        glds2(src, 32 * HD * 2, vo, dst);
        if (TH_Q < 63) HOLD_V((h & 3) ^ 1, TH_Q);
        glds2(src + 64 * HD * 2, 32 * HD * 2, vo, dst + 16384);
        if (TH_Q < 63) HOLD_V((h & 3) ^ 1, TH_Q);
      }
    };
    bf16x8 keep[4][4];
    int f0 = -1;
    auto place = [&](int g) {
#pragma unroll
      for (int i = 0; i < 4; ++i)
        *reinterpret_cast<bf16x8*>(smem + (g == 0 ? 65536 : g == 1 ? 32768 : g == 2 ? 131072 : 98304) + WV * 4096 + i * 1024 + lane * 16) = keep[g][i];
    };
    stage(0); stage(1);
    Boot bt;
#pragma unroll
    for (int j = 0; j < 8; ++j) {
      if (j < 7) WAIT_V(8); else WAIT_V(0);
      if (WV == 7 && j == QBJ + 1) pm = boot_done(p.ws, bt);
      if ((WV == 4 || WV == 5) && j == 7) {
        asm volatile("" : "+v"(f0));
        have = min(__builtin_amdgcn_readlane(f0, 0), __builtin_amdgcn_readlane(f0, 1));
      }
      LBAR();
#pragma unroll
      for (int k = 0; k < 8; ++k) {
        const int P = (QK_MAP[WV] >> (4 * (2 * j + (k >> 2)))) & 15;
        const bf16x8 fr = ld_bfrag(smem + (~j & 1) * 65536 + (WV >> 2) * 32768 + (k >> 2) * 16384, k & 1, 2 * (WV & 3) + ((k >> 1) & 1), lane);
        const int f = P * 4 + (k & 3);
        if (P < 12) put4(W[f >> 3], f & 7, fr);
        else keep[P - 12][k & 3] = fr;
      }
      if (j == (WV >> 1)) place(2);
      LBAR();
      if (WV == 7 && j == QBJ) boot_ask(p.ws, bt);
      if (j + 2 < 8) stage(j + 2);
      if (j == 6) {
        if (WV == 4 || WV == 5)
          asm volatile("buffer_load_dword %0, %1, %2, 0 offen sc1" : "=&v"(f0) : "v"(fkq(h, (lane & 1) * 2)), "s"(raw_rsrc(p.ws + WS_SY, SY_TOTAL)) : "memory");
        place(0); place(3);
      }
    }
    place(1);
    if (WV == 7 && lane < 8) lds_at(smem + L_MISC)[lane] = lane == 7 ? pm : (lane == 1 ? now_q() - T0_BACK : 0);
    if (WV == 6) lds_at(smem + 28672)[lane] = ~0u;
    LBAR();
    pm = uni(lds_at(smem + L_MISC)[7]);
    p.s0 = pm >> 31;
    if (WV == 6) RDW(h);
    if (WV == 4 || WV == 5) {
      if (have < 0) have = wait_prog(mkrsrc(p.ws + WS_SY, SY_TOTAL), fkq(h, (lane & 1) * 2), 0, (gu32*)(p.ws + WS_SY + SY_ABORT));
      const char* src = p.ws + WS_T + t_off(p, kseg(0, h)) + (WV - 4) * 4096 + lane * 16;
#pragma unroll
      for (int i = 0; i < 4; ++i) glds16_sc1(src + i * 1024, smem + L_KT + (WV - 4) * 4096 + i * 1024);
    }
  }
  const __amdgpu_buffer_rsrc_t rsy = mkrsrc(p.ws + WS_SY, SY_TOTAL);
  const u32x4 rs4 = raw_rsrc(p.ws + WS_SY, SY_TOTAL);
  const u32x4 rt4 = raw_rsrc(p.ws + WS_T, T_BYTES);
  gu32* abortw = (gu32*)(p.ws + WS_SY + SY_ABORT);
  const float cexp = 0.08838834764831845f * 1.4426950408889634f;
  constexpr int m = WV >> 2, b = WV & 3;
  int lead = LEAD0;
  unsigned tb1 = 0;
  unsigned long long fm = 0, ps = 0; (void)fm; (void)ps;
  for (int s = 0; s < NSEG; ++s) {
    int lane = tid & 63;
    asm volatile("" : "+v"(lane));
    const int r = lane & 15, q = lane >> 4;
    unsigned aL16 = lane * 16, aMW = aL16 + WV * 1024, aWL = aL16 + 98304 + WV * 4096;
    unsigned aIDX = (lane & 2) ? 0xEEEE : 0x4444;
    unsigned aQW = (32 * b + r) * 16 + q * 4 + (m ? L_KX : L_QX);
    u32x4 QF;
    asm volatile("" : "={v[232:235]}"(QF));
    if (WV < 4) {
      QF[0] = L_QX + (q * 8 + (r >> 2)) * 16 + (r & 1) * 8;
      QF[1] = ((2 * (q & 1) + (q >> 1)) * 16 + r) * 4;
      QF[2] = ((r >> 2) & 1) * 65536 + ((r >> 1) & 1) * 64 + (r >> 3) * 32 + (r & 1) * 16 + (q >> 1) * 256 + (q & 1) * OX4;
    } else QF[3] = r * 16 + q * 4;
    unsigned tout, spins, tgt;
    if (WV == 0) QK_STEP(QK_ASM_0); else if (WV == 1) QK_STEP(QK_ASM_1); else if (WV == 2) QK_STEP(QK_ASM_2);
    else if (WV == 3) QK_STEP(QK_ASM_3); else if (WV == 4) QK_STEP(QK_ASM_4); else if (WV == 5) QK_STEP(QK_ASM_5);
    else if (WV == 6) QK_STEP(QK_ASM_6); else QK_STEP(QK_ASM_7);
    tout = __builtin_amdgcn_readfirstlane(tout);
    tb1 = __builtin_amdgcn_readfirstlane(tb1);
    lane = tid & 63;
    asm volatile("" : "+v"(lane));
    if (WV < 4) {
      lds_at(smem + L_MISC + 8)[WV] = s + 1;
      if (WV == 3) lds_at(smem + L_MISC)[6] = time_of(tout);
    }
    if (WV == 6) {
      if (s + 1 < NSEG) watch(rsy, s, lane, now_q() + W_DELAY, (const unsigned*)(smem + L_MISC), abortw, side(pm, 1));
    } else if (WV >= 4 && s + 1 < NSEG) {
      unsigned t;
      do {
        __builtin_amdgcn_s_sleep(1);
        t = lds_at(smem + L_MISC + 8)[lane & 3];
      } while (!__all(t == (unsigned)(s + 1)));
      if (WV <= 5) {
        if (s + 2 < NSEG) {
          if (have < s + 1) have = wait_prog(rsy, fkq(h, (lane & 1) * 2), s + 1, abortw);
          const char* src = p.ws + WS_T + t_off(p, kseg(s + 1, h)) + (WV - 4) * 4096 + lane * 16;
#pragma unroll
          for (int i = 0; i < 4; ++i) glds16_sc1(src + i * 1024, smem + L_KT + (WV - 4) * 4096 + i * 1024);
          if (have < s + 2) have = look_prog(rsy, fkq(h, (lane & 1) * 2));
        }
      } else {
        __builtin_amdgcn_s_sleep(6);
        const int x = (int)(__builtin_amdgcn_readfirstlane(lds_at(smem + L_MISC)[6]) - tb1);
        lead = __builtin_amdgcn_readfirstlane(track(lead, x, LEAD_UP, s < 8 ? LEAD_D0 : LEAD_DN, 60));
        SMP(2, s, x); SMP(3, s, lead);
      }
    }
  }
}

__device__ __forceinline__ void qk_role(Args& p, int h, char* smem) {
  switch (__builtin_amdgcn_readfirstlane(threadIdx.x >> 6)) {
    case 0: qk_wave<0>(p, h, smem); break;
    case 1: qk_wave<1>(p, h, smem); break;
    case 2: qk_wave<2>(p, h, smem); break;
    case 3: qk_wave<3>(p, h, smem); break;
    case 4: qk_wave<4>(p, h, smem); break;
    case 5: qk_wave<5>(p, h, smem); break;
    case 6: qk_wave<6>(p, h, smem); break;
    default: qk_wave<7>(p, h, smem); break;
  }
}

#define K_DUE 160
#define T_SL 2
#define O_DUE 400
#define LEAD_V 0

constexpr int V_VM = 16384, V_T = 32768, V_KI = 131072;
__device__ __forceinline__ void vu_role(Args& p, int h, char* smem) {
  const int tid = threadIdx.x, lane = tid & 63;
  const int w = __builtin_amdgcn_readfirstlane(tid >> 6);
  u32x32 W[4];
  {
    const unsigned vo = (tid >> 4) * (HD * 2) + 16 * ((lane & 15) ^ swzB((tid >> 4) & 15));
    auto stage = [&](int j) {
      const char* src = (const char*)(p.Wv + (size_t)j * 128 * HD + h * DH);
      const unsigned dst = lds_off(smem) + L_WL + (j & 3) * 32768 + w * 1024;
      glds2(src, 32 * HD * 2, vo, dst);
      if (TH_V < 63) HOLD_V((h & 3) ^ 1, TH_V);
      glds2(src + 64 * HD * 2, 32 * HD * 2, vo, dst + 16384);
      if (TH_V < 63) HOLD_V((h & 3) ^ 1, TH_V);
    };
    start_sy(p.ws, p.mem0, h, smem);
    Boot bt;
#pragma unroll
    for (int u = 0; u < 2; ++u) {
#pragma unroll
      for (int j = 0; j < 4; ++j) stage(4 * u + j);
      if (u && w == 7) boot_ask(p.ws, bt);
      WAIT_V(0);
      if (u && w == 7) {
        const unsigned m = boot_done(p.ws, bt);
        if (lane < 8) lds_at(smem + L_MISC)[lane] = lane == 7 ? m : (lane == 1 ? now_q() : 0);
      }
      LBAR();
#pragma unroll
      for (int k = 0; k < 16; ++k) put4(W[2 * u + (k >> 3)], k & 7, ld_bfrag(smem + L_WL + ((k >> 1) ^ w) * 16384, k & 1, w, lane));
      LBAR();
    }
    RDY(32 + h);
  }
  const __amdgpu_buffer_rsrc_t rsy = mkrsrc(p.ws + WS_SY, SY_TOTAL);
  const u32x4 rs4 = raw_rsrc(p.ws + WS_SY, SY_TOTAL);
  gu32* abortw = (gu32*)(p.ws + WS_SY + SY_ABORT);
  const unsigned pm = uni(lds_at(smem + L_MISC)[7]);
  p.s0 = pm >> 31;
  unsigned long long fm = 0; (void)fm;
  int have = -1;
  auto fetch = [&](int sg) {
    if (have < sg) have = wait_prog(rsy, fkq(h, w - 4), sg, abortw);
    const char* src = p.ws + WS_T + t_off(p, (w - 4) * T_Q + kseg(sg, h)) + lane * 16;
#pragma unroll
    for (int i = 0; i < 8; ++i) glds16_sc1(src + i * 1024, smem + V_T + (sg & 3) * 24576 + (w - 4) * 8192 + i * 1024);
  };
  const int fo = SY_FO + ((w & 3) * NH + h) * 128;
  typedef __attribute__((ext_vector_type(8))) float f32x8;
  f32x8 bias;
  typedef __attribute__((ext_vector_type(8))) unsigned u32x8;
  u32x8 ca;
#pragma unroll
  for (int i = 0; i < 8; ++i) ca[i] = lane * 16 + (i ^ w) * 1024;
  u32x4 ta;
  {
    const int r = lane & 15, q = lane >> 4;
#pragma unroll
    for (int j = 0; j < 8; ++j) bias[j] = (j >> 2) * 16 + 4 * q + (j & 3) <= 16 * (w & 1) + r ? INFINITY : -INFINITY;
    ta[0] = V_KI + w * 2048 + lane * 16;
    ta[1] = V_KI + w * 2048 + (q * 8 + (r >> 2)) * 16 + (r & 1) * 8;
    ta[2] = V_VM + (w & 2) * 512 + ((2 * (q & 1) + (q >> 1)) * 16 + r) * 4;
    ta[3] = 0;
  }
#pragma unroll 1
  for (int s = 0; s < NSEG; ++s) {
    unsigned spins, tgt;
    asm volatile(VV_ASM
        : [spins] "=&s"(spins), [tgt] "=&s"(tgt)
        : W4_IN, "{v200}"(lane * 16), "{v[232:239]}"(ca), "{v202}"((lane & 2) ? 0xEEEE : 0x4444), "{v203}"(lane * 4),
          "{v204}"(tid * 4 + V_VM + (s & 1) * 2048), [rs] "s"(rs4), [som] "s"(o_mem(pm, s) + w * 1024), [som2] "s"(o_mem(pm, s) + w * 1024 + 16384), [step] "s"(s),
          [sent] "s"(DSENT), [sab] "s"(SY_ABORT), [sv] "s"(o_v(pm, s, h) + w * 256)
        : CLB32(128), CLB32(160), CLB8(192, 193, 194, 195, 196, 197, 198, 199), CLB_S);
    if (w < 4 || s == NSEG - 1) {
      const int sg = s - (w < 4);
      if (w >= 4) pace_until(now_q() + K_DUE);
      if (sg >= 0) {
        asm volatile(TK_ASM
            :
            : "{v200}"(lane * 16), "{v[208:215]}"(bias), "{v[216:219]}"(ta),
              [rs] "s"(rs4), [rt] "s"(raw_rsrc(p.ws + WS_T, T_BYTES)), [sent] "s"(DSENT), [sab] "s"(SY_ABORT), [sk] "s"(o_v(pm, sg, h) + 2048),
              [tl] "s"(V_T + (sg & 3) * 24576), [vp] "s"((sg & 1) * 2048), [tq] "s"((w & 1) * 4096), [tv] "s"((w & 2) * 2048),
              [so] "s"(t_off_o(p, T_O + (sg * 2 + (w & 1)) * 16 + (h >> 1)) + (h & 1) * 4096 + (w & 2) * 1024),
              [up] "s"(0xFFFFFFFF00000000ull), [c] "s"(1.4426950408889634f), [negc] "s"(-1.4426950408889634f),
              [sc] "s"((unsigned long long)__float_as_uint(0.08838834764831845f)), [od] "s"(s < NSEG - 1 ? O_DUE : 0)
            : CLB32(128), CLB32(160), CLB8(192, 193, 194, 195, 196, 197, 198, 199), CLB8(224, 225, 226, 227, 228, 229, 230, 231),
              "v205", "v206", "v207", "s88", "s89", CLB_S);
        if (sg >= NSEG - 3) {
          static_assert(TLAG == 2, "");
          WAIT_V(0);
          if (lane == 0) __builtin_amdgcn_raw_buffer_store_b32(1u, rsy, fo + (sg - NSEG + 3) * 4 * NH * 128, 0, 16);
        }
      }
    } else if (w == 7) {
      SMP(6, s, (int)(now_q() - tgt));
      watch(rsy, s, lane, now_q() + LEAD_V + W_DELAY, (const unsigned*)(smem + L_MISC), abortw, side(pm, 1));
    } else if (w >= 4) {
      __builtin_amdgcn_s_sleep(T_SL);
      const int s1 = w == 5 ? (s == NSEG - 2 ? s + 1 : s) : s + 1;
#pragma unroll 1
      for (int sg = (w == 5 || s == 0) ? s : s + 1; sg <= s1; ++sg) fetch(sg);
      if (have < s1 + 1 && s1 + 1 < NSEG) have = look_prog(rsy, fkq(h, w - 4));
      if (w == 5 && s + NPRE < NSEG) {
#pragma unroll
        for (int i = 0; i < 4; ++i) st_sc1(rsy, o_v(pm, s + NPRE, h) + i * 1024 + lane * 16, (u32x4){DSENT, DSENT, DSENT, DSENT});
      }
    }
  }
  if (h == 3 && (w & 2) == 0) { LST(351 + (w & 1) + (w >> 2) * 2); }
}

#define RL_UP 6
#define RL_DN 4
#define RED_MIN 40
#define BA_LOOK 360
constexpr int R_SI = 32768, R_PART = 98304;

__device__ __forceinline__ void red_role(Args& p, int j, char* smem) {
  const int tid = threadIdx.x;
  int lane = tid & 63;
  const int w = __builtin_amdgcn_readfirstlane(tid >> 6);
  const int c = (j & 2) * 8 + (j >> 2), rh = j & 1;
  const int hg = (w + (j >> 2)) & 7, rot = (j >> 2) + (j >> 5);
  const __amdgpu_buffer_rsrc_t rsy = mkrsrc(p.ws + WS_SY, SY_TOTAL);
  const u32x4 rs4 = raw_rsrc(p.ws + WS_SY, SY_TOTAL);
  const __amdgpu_buffer_rsrc_t rt = mkrsrc(p.ws + WS_T, T_BYTES);
  gu32* abortw = (gu32*)(p.ws + WS_SY + SY_ABORT);
  u32x32 W[4];
  u32x2 tag = {0u, 0u};
  {
    const u16* Wo = p.Wo + (size_t)(4 * hg * DH) * DM + 32 * c;
    const int ro = (lane >> 2) * 64 + (lane & 3) * 8;
    Boot bt;
    load_blocks<16>(smem + 32768 + w * 16384,
        [&](int i, char* slot) {
          if (i == 12 && w == 0) boot_ask(p.ws, bt);
          if ((i < 2 || i == 4 || i == 8) && (c & 14) != 6 && lane < 10) {
            const int x = i < 2 ? 3 - i : i >> 3, n = min(((c & 15) - ((c & 8) >> 2)) * 10 + lane, 127);
            touch_line((const char*)p.Wo + (size_t)(w * 512 + (x >> 1) * 256 + (x & 1) * 64 + (n & 64) + n) * (DM * 2) + (c & 16) * 64 + 384, smem + w * 256);
          }
          const int x = ((i >> 2) + rot) & 3;
          blk_dma(Wo + (size_t)((x >> 1) * 256 + (x & 1) * 64 + ((i >> 1) & 1) * 128 + (i & 1) * 32) * DM, DM, slot, lane);
          if (TH_R < 63) HOLD_V((c & 14) ^ 6, TH_R);
        },
        [&](int i, char* slot) {
#pragma unroll
          for (int t = 0; t < 2; ++t) {
            s16x4 lo = __builtin_amdgcn_ds_read_tr16_b64_v4i16((lds_s16x4_ptr)(slot + ro + t * 32));
            s16x4 hi = __builtin_amdgcn_ds_read_tr16_b64_v4i16((lds_s16x4_ptr)(slot + ro + t * 32 + 1024));
            put4(W[i >> 2], (i & 2) * 2 + 2 * t + (i & 1), (bf16x8){lo[0], lo[1], lo[2], lo[3], hi[0], hi[1], hi[2], hi[3]});
          }
        });
    if (w == 0) { lds_at(smem + 4128)[0] = boot_done(p.ws, bt); tag = next_tag(bt.e); }
    LBAR();
  }
  const unsigned pm = uni(lds_at(smem + 4128)[0]);
  p.s0 = pm >> 31;
  const int hs = side(pm, 1);
  if ((c & 14) != 6 && GPF == 2) {
#pragma unroll
    for (int i = 0; i < 2; ++i)
      touch_line((const char*)p.wd + (size_t)(j - (j >= 32 ? 8 : 0)) * 131072 + (i * 8 + w) * 8192 + lane * 128, smem + w * 256);
  }
  {
    const u32x4 ds = {DSENT, DSENT, DSENT, DSENT};
#pragma unroll
    for (int i = 0; i < 2; ++i) {
      const int s = (w * 2 + i) * 4 + (lane >> 4);
      if (s >= 2) st_sc1(rsy, o_mem(pm, s) + (c >> 4) * 16384 + (c & 15) * 512 + rh * 128 + ((lane >> 3) & 1) * 256 + (lane & 7) * 16, ds);
    }
  }
  if (tid < 4) { reinterpret_cast<unsigned*>(smem + 4096)[tid] = tid == 3 ? now_q() : 0; reinterpret_cast<unsigned*>(smem + 4112)[tid] = DSENT; }
  if (tid < 256) reinterpret_cast<int*>(smem + 5120)[tid] = ((tid >> 2) - TLAG - 6) >> 2;
  LBAR();
  const unsigned base = __builtin_amdgcn_readfirstlane(lds_at(smem + 4096)[3]);
  RDY(64 + j);
  unsigned long long fm = 0, ps = 0;
  const unsigned aL = lane * 16;
  const int e = ((56 + w) * SEG + rh * 16 + (lane >> 2)) * DM + c * 32 + (lane & 3) * 8;
  u32x4 xv = *reinterpret_cast<const u32x4*>(p.x + e), av = {DSENT, DSENT, DSENT, DSENT};
  asm volatile("" : "+v"(xv));
  {
    const int r = lane & 15, q = lane >> 4;
    const int sm0 = o_mem(pm, 0) + (c >> 4) * 16384 + (c & 15) * 512 + rh * 128;
    u32x4 VO, VT;
    unsigned fo = SY_FO + ((rh + (lane & 4) / 2) * NH + 4 * hg + (lane & 3)) * 128;
    unsigned ao = w == 1 ? (c * S_LEN + rh * 16 - (TLAG + 3) * SEG) * 64 + lane * 16
                         : (w >= 4 ? SY_O + NPRE * 131072 + side(pm, w - 2) + (j >> 3) * 16384 + (j & 7) * 1024 + lane * 16
                                   : SY_FS + (2 * c + rh) * 128 + (lane < 8 ? lane * 16 : 0x70000000));
    unsigned ar = w == 1 ? R_SI - (TLAG + 2) * 1024 + lane * 16 : (w == 2 ? 5120 : 4112);
    const unsigned long long am = w == 1 ? ((1ull << 56) - 1) << (TLAG + 3) : (w == 2 ? 0x11111111111111ull << (TLAG + 7) : (w >= 4 ? (0xFFFFull << (16 * (w - 4))) >> NPRE : 0));
    unsigned pw = R_PART + w * 2048 + lane * 16, pr = R_PART + 16384 + tid * 4;
    unsigned sw = R_SI - (TLAG + 1) * 1024 + (lane >> 2) * 64 + w * 8 + (lane & 3) * 2, stm = 8192 + w * 512;
#pragma unroll
    for (int l = 0; l < 4; ++l) {
      const int x = (l + rot) & 3;
      VO[l] = o_o(0, 0, 4 * hg) + rh * 65536 + x * 1024 + aL;
      VT[l] = (T_O + rh * 16 + 2 * hg + (x >> 1)) * 16384 + (x & 1) * 2048 + aL;
    }
    unsigned ba = lane < 4 ? SY_BA + hs + (4 * hg + lane) * 128 : 0x7F000000u;
    int rl = RL0;
    unsigned step, E, so, st, st4, sh;
    asm volatile(RED_ASM
        : [step] "=&s"(step), [E] "=&s"(E), [so] "=&s"(so), [st] "=&s"(st), [st4] "=&s"(st4), [sh] "=&s"(sh),
          [rl] "+s"(rl), [fm] "+s"(fm), [ps] "+s"(ps),
          "+{v[224:227]}"(av), "+{v228}"(ao), "+{v229}"(ar), "+{v230}"(pw), "+{v231}"(pr), "+{v232}"(sw), "+{v233}"(stm), "+{v242}"(fo), "+{v173}"(ba)
        : W4_IN, "{v168}"(aL), "{v169}"(w * 512 + lane * 8), "{v170}"(lane * 8), "{v174}"((r & 1) ? 0x03020706u : 0x05040100u),
          "{v171}"(sm0 + 32768 + (q >> 1) * 256 + (q & 1) * 64 + ((r >> 1) & 1) * 32 + (r & 1) * 16 + (r >> 2) * 4),
          "{v172}"((lane & 2) ? 0xEEEE : 0x4444),
          "{v175}"(w == 3 && lane < 32 ? SY_BR + hs + j * 128 + lane * 4 : 0x7F000000u), "{v[234:237]}"(VO), "{v[238:241]}"(VT), "{v243}"(e * 2),
          [rg] "s"(raw_rsrc(p.ws + WS_G, S_LEN * DM * 2)), [fg] "s"(SY_FG + ((14 + (w >> 2)) * 8 + (c >> 2)) * 128),
          [rs] "s"(rs4), [ra] "s"(w == 1 ? raw_rsrc(p.ws + WS_S, S_LEN * DM * 2) : rs4), [rt] "s"(raw_rsrc(p.ws + WS_T, T_BYTES)),
          [ds] "n"(DS_RED), [mn] "n"(RED_MIN), [lk] "n"(DS_MEM + BA_LOOK), [up] "n"(RL_UP), [dn] "n"(-RL_DN), [sent] "s"(DSENT), [sab] "s"(SY_ABORT),
          [base] "s"(base), [pm] "s"(pm), [s0] "s"((p.s0 ^ TF_O) << 13), [am] "s"(am), [ainc] "s"(w == 1 ? SEG * 64 : (w >= 4 ? 131072 : 0)),
          [rinc] "s"(w == 1 ? 1024 : (w == 2 ? 16 : 0))
        : CLB32(128), CLB8(160, 161, 162, 163, 164, 165, 166, 167), CLB8(176, 177, 178, 179, 180, 181, 182, 183),
          CLB8(184, 185, 186, 187, 188, 189, 190, 191), CLB8(192, 193, 194, 195, 196, 197, 198, 199), CLB8(200, 201, 202, 203, 204, 205, 206, 207),
          CLB8(208, 209, 210, 211, 212, 213, 214, 215), CLB8(216, 217, 218, 219, 220, 221, 222, 223), CLB_E, "s96", CLB_S);
  }
  if (j == 0 && w == 5) { LST(345); }
  LBAR();
  if (j == 0 && w == 5) { LST(346); }
  u32x4 sv;
  if (w < 6 - TLAG) sv = *reinterpret_cast<const u32x4*>(smem + R_SI + (56 + w) * 1024 + lane * 16);
  else {
    const f32x4* part = reinterpret_cast<const f32x4*>(smem + (w == 6 - TLAG ? R_PART : (w - 6 + TLAG) * 16384)) +
                        (lane & 2) * 32 + (lane & 1) * 32 + (lane >> 2);
    f32x4 x[8][2];
#pragma unroll
    for (int ww = 0; ww < 8; ++ww) { x[ww][0] = part[ww * 128]; x[ww][1] = part[ww * 128 + 16]; }
#pragma unroll
    for (int u = 0; u < 2; ++u) {
      const f32x4 a = ((x[0][u] + x[1][u]) + (x[2][u] + x[3][u])) + ((x[4][u] + x[5][u]) + (x[6][u] + x[7][u]));
      sv[2 * u] = cvtpk(a[0], a[1]); sv[2 * u + 1] = cvtpk(a[2], a[3]);
    }
  }
  st_out(p, e, combine(sv, av, xv));
  if (j == 0 && tid == 0) st2_sc1(rsy, SY_EP, tag);
  if (tid == 128) { LST(400 + j); }
}

__global__ __launch_bounds__(512, 2) void main_kernel(Args p) {
  char* smem = g_smem;
  p.ws = ws_align(p.ws);
  const int bx = blockIdx.x & 7, bk = blockIdx.x >> 3;
  const bool isg = (bx ^ XPROBE) & 4;
  const int b = (isg ? NCHAIN : 0) + bk * 4 + (bx & 3);
  if (b == 64 && threadIdx.x == 0) { LST(500); }
  if (threadIdx.x < 64) {
    unsigned long long pc; unsigned len, keep;
    asm volatile("s_getpc_b64 %0\n.Lpc%=:\n\ts_mov_b32 %1, .Lcode_end-.Lpc%=" : "=s"(pc), "=s"(len));
    asm volatile("s_mov_b32 %0, m0\n\ts_mov_b32 m0, %3\n\ts_nop 0\n\tbuffer_load_dword %1, %2, 0 offen lds\n\ts_mov_b32 m0, %0"
                 : "=&s"(keep) : "v"((bk * 40 + threadIdx.x) * 128), "s"(raw_rsrc((const void*)pc, len)), "s"((b >> 6) == 1 ? 2048 : 163328) : "memory");
  }
  if (b >= NCHAIN) gemm_role(p, b - NCHAIN, smem);
  else if (b < 32) qk_role(p, (b & 3) * 8 + bk, smem);
  else if (b < 64) vu_role(p, (b & 3) * 8 + bk - 8, smem);
  else red_role(p, b - 64, smem);
}

extern "C" void kernel_launch(void* const* d_in, const int* in_sizes, int n_in,
                              void* d_out, int out_size, void* d_ws, size_t ws_size,
                              hipStream_t stream) {
  Args a;
  a.x = (const u16*)d_in[0]; a.mem0 = (const u16*)d_in[1];
  a.Wq = (const u16*)d_in[2]; a.Wk = (const u16*)d_in[3]; a.Wv = (const u16*)d_in[4]; a.Wo = (const u16*)d_in[5];
  a.wg = (const u16*)d_in[8]; a.wu = (const u16*)d_in[9]; a.wd = (const u16*)d_in[10];
  a.ws = (char*)d_ws; a.out = (u16*)d_out; a.s0 = 0; a.pad1 = 0;
  main_kernel<<<256, 512, 0, stream>>>(a);
}
```

```cpp
#include <hip/hip_runtime.h>
#include <hip/hip_bf16.h>
#include <stdint.h>
#include <type_traits>

constexpr int S_LEN = 2048, DM = 1024, NH = 32, DH = 128, NMEM = 8, SEG = 32;
constexpr int NSEG = S_LEN / SEG;
constexpr int HD = NH * DH;
constexpr int FF = 3584;

typedef unsigned short u16;
typedef __attribute__((ext_vector_type(8))) short bf16x8;
typedef __attribute__((ext_vector_type(4))) short s16x4;
typedef __attribute__((ext_vector_type(4))) float f32x4;
typedef __attribute__((ext_vector_type(4))) unsigned int u32x4;
typedef __attribute__((ext_vector_type(2))) unsigned int u32x2;
typedef __attribute__((address_space(3))) s16x4* lds_s16x4_ptr;
typedef __attribute__((address_space(3))) void* lds_void_ptr;
typedef __attribute__((address_space(1))) const void* glb_cvoid_ptr;
typedef __attribute__((address_space(1))) unsigned gu32;
typedef __bf16 bf16x2_t __attribute__((ext_vector_type(2)));
typedef float f32x2_t __attribute__((ext_vector_type(2)));

__device__ __forceinline__ float bf2f(u16 v) { return __uint_as_float(((unsigned)v) << 16); }
__device__ __forceinline__ unsigned cvtpk(float lo, float hi) {
  bf16x2_t r = __builtin_convertvector((f32x2_t){lo, hi}, bf16x2_t);
  return __builtin_bit_cast(unsigned, r);
}
__device__ __forceinline__ float lo_f(unsigned v) { return __uint_as_float(v << 16); }
__device__ __forceinline__ float hi_f(unsigned v) { return __uint_as_float(v & 0xffff0000u); }
__device__ __forceinline__ float rbf(float f) { return lo_f(cvtpk(f, f)); }
__device__ __forceinline__ bf16x8 as_bf16x8(u32x4 v) { return __builtin_bit_cast(bf16x8, v); }
#define MFMA(a, b, c) __builtin_amdgcn_mfma_f32_16x16x32_bf16((a), (b), (c), 0, 0, 0)
typedef __attribute__((ext_vector_type(16))) short bf16x16;
typedef __attribute__((ext_vector_type(8))) __bf16 bfv8;
typedef __attribute__((ext_vector_type(16))) __bf16 bfv16;
#define SMFMA(a, b, c, idx) __builtin_amdgcn_smfmac_f32_16x16x64_bf16(__builtin_bit_cast(bfv8, (a)), __builtin_bit_cast(bfv16, (b)), (c), (idx), 0, 0)
__device__ __forceinline__ bf16x16 cat16(bf16x8 lo, bf16x8 hi) {
  return __builtin_shufflevector(lo, hi, 0, 1, 2, 3, 4, 5, 6, 7, 8, 9, 10, 11, 12, 13, 14, 15);
}

#define KSTR 65
#define XLD 1024
constexpr int PAGE = 2 << 20, NPAGE = 14;
constexpr int T_K = 0, T_Q = 2304, T_V = 4608, T_O = 6912, T_END = T_O + 2048;
constexpr unsigned T_BYTES = T_END * 16384u;
constexpr size_t WS_SY = 0;
constexpr size_t WS_T = (size_t)NPAGE * PAGE;
constexpr size_t WS_XN = WS_T + T_BYTES;
constexpr size_t WS_HF = WS_XN + (size_t)S_LEN * XLD * 2;
constexpr size_t WS_G = WS_HF + (size_t)S_LEN * 16384;
constexpr size_t WS_S = WS_G + (size_t)S_LEN * DM * 2;
constexpr size_t WS_END = WS_S + (size_t)S_LEN * DM * 2;
constexpr unsigned SENT = 0xFFFFFFFFu;
constexpr unsigned DSENT = 0xFFBFFFBFu;
constexpr int SY_MEM = 0;
constexpr int SY_O = 2 * PAGE;
constexpr int SY_V = 6 * PAGE;
constexpr int SY_BR = PAGE, SY_BA = SY_BR + 16384;
constexpr int SY_FKQ = SY_BR + 32768;
constexpr int SY_FG = SY_FKQ + NH * 4 * 128;
constexpr int SY_FO = SY_FG + 16 * 8 * 128;
constexpr int SY_CH = SY_FO + 384 * 128;
constexpr int SY_ABORT = SY_CH + 8 * 128;
constexpr int SY_FS = SY_ABORT + 256;
constexpr int SY_FEND = SY_FS + 64 * 128;
constexpr int SY_EP = SY_FEND, SY_BT = SY_EP + 128;
constexpr int SY_TOTAL = NPAGE * PAGE;
constexpr int PROBE = PAGE - 16384 + 6144;
__device__ __forceinline__ int side(unsigned pm, int page) { return ((pm >> page) & 1u) << 13; }
__device__ __forceinline__ int o_mem(unsigned pm, int s) { return SY_MEM + s * 32768 + side(pm, 0); }
__device__ __forceinline__ int o_o(unsigned pm, int s, int h) {
  return SY_O + s * 131072 + (h >> 3) * 16384 + ((h >> 1) & 3) * 2048 + (h & 1) * 128 + side(pm, 2 + (s >> 4));
}
__device__ __forceinline__ int o_v(unsigned pm, int s, int h) { return SY_V + s * 262144 + (h >> 1) * 16384 + (h & 1) * 4096 + side(pm, 6 + (s >> 3)); }
__device__ __forceinline__ char* ws_align(char* ws) { return ws + ((PAGE - (unsigned)(uintptr_t)ws) & (PAGE - 1)); }
#define NFUSE 12
constexpr int NPRE = 4;
static_assert(WS_END + PAGE <= (size_t)256 << 20 && NH * KSTR <= T_Q && NPAGE % 2 == 0, "");
constexpr int NCHAIN = 128, NGW = 32;
__host__ __device__ constexpr int kseg(int sg, int h) { return h * KSTR + sg; }
__host__ __device__ constexpr int fkq(int h, int kind) { return SY_FKQ + (h * 4 + kind) * 128; }

#define XPROBE 0
struct Args {
  const u16 *x, *mem0, *Wq, *Wk, *Wv, *Wo, *wg, *wu, *wd;
  char* ws;
  u16* out;
  int s0, pad1;
};
__device__ __forceinline__ int h_off(const Args& p, int rblk) { return rblk * PAGE + (((p.s0 ^ rblk ^ 1) & 1) << 13); }
static_assert(WS_HF % (2 * PAGE) == 0, "");
#define TF_KQV 1
#define TF_O 0
__device__ __forceinline__ int t_off(const Args& p, int t) { return t * 16384 + (((p.s0 ^ (t >> 7) ^ (TF_KQV == 2 ? t : TF_KQV)) & 1) << 13); }
__device__ __forceinline__ int t_off_o(const Args& p, int t) { return t * 16384 + (((p.s0 ^ (t >> 7) ^ (TF_O == 2 ? t : TF_O)) & 1) << 13); }

__device__ __forceinline__ float wave_sum(float v) {
#pragma unroll
  for (int i = 0; i < 4; ++i) {
    const int x = (int)__float_as_uint(v);
    v += __uint_as_float(i == 0 ? __builtin_amdgcn_update_dpp(0, x, 0x128, 0xf, 0xf, false) : i == 1 ? __builtin_amdgcn_update_dpp(0, x, 0x124, 0xf, 0xf, false)
                         : i == 2 ? __builtin_amdgcn_update_dpp(0, x, 0x122, 0xf, 0xf, false) : __builtin_amdgcn_update_dpp(0, x, 0x121, 0xf, 0xf, false));
  }
  auto r = __builtin_amdgcn_permlane16_swap(__float_as_uint(v), __float_as_uint(v), false, false);
  v = __uint_as_float(r[0]) + __uint_as_float(r[1]);
  r = __builtin_amdgcn_permlane32_swap(__float_as_uint(v), __float_as_uint(v), false, false);
  return __uint_as_float(r[0]) + __uint_as_float(r[1]);
}

__shared__ __attribute__((aligned(16))) char g_smem[163840];
__device__ __forceinline__ unsigned lds_off(const void* p) {
  return (unsigned)(uintptr_t)(lds_void_ptr)g_smem + (unsigned)((const char*)p - g_smem);
}
__device__ __forceinline__ __amdgpu_buffer_rsrc_t mkrsrc(const void* p, unsigned bytes) {
  return __builtin_amdgcn_make_buffer_rsrc((void*)p, 0, bytes, 0x00020000);
}
__device__ __forceinline__ u32x4 ld_sc1(__amdgpu_buffer_rsrc_t rs, int off) {
  return __builtin_amdgcn_raw_buffer_load_b128(rs, off, 0, 16);
}
__device__ __forceinline__ void st_sc1(__amdgpu_buffer_rsrc_t rs, int off, u32x4 v) {
  __builtin_amdgcn_raw_buffer_store_b128(v, rs, off, 0, 16);
}
__device__ __forceinline__ unsigned ror8(unsigned v) {
  return (unsigned)__builtin_amdgcn_update_dpp(0, (int)v, 0x128, 0xf, 0xf, false);
}
__device__ __forceinline__ float ror8f(float v) { return __uint_as_float(ror8(__float_as_uint(v))); }
__device__ __forceinline__ void swap16(unsigned& x, unsigned& y) {
  auto r = __builtin_amdgcn_permlane16_swap(x, y, false, false);
  x = r[0]; y = r[1];
}
__device__ __forceinline__ void swap32(unsigned& x, unsigned& y) {
  auto r = __builtin_amdgcn_permlane32_swap(x, y, false, false);
  x = r[0]; y = r[1];
}
__device__ __forceinline__ f32x4 fold32(f32x4 x, f32x4 y) {
  f32x4 o;
#pragma unroll
  for (int j = 0; j < 4; ++j) {
    unsigned a = __float_as_uint(x[j]), b = __float_as_uint(y[j]);
    swap32(a, b);
    o[j] = __uint_as_float(a) + __uint_as_float(b);
  }
  return o;
}
__device__ __forceinline__ bf16x8 tr_frag(const char* img_lane) {
  s16x4 lo = __builtin_amdgcn_ds_read_tr16_b64_v4i16((lds_s16x4_ptr)img_lane);
  s16x4 hi = __builtin_amdgcn_ds_read_tr16_b64_v4i16((lds_s16x4_ptr)(img_lane + 64));
  return (bf16x8){lo[0], lo[1], lo[2], lo[3], hi[0], hi[1], hi[2], hi[3]};
}
__device__ __forceinline__ float rowq_max(float v) {
  unsigned a = __float_as_uint(v), b = a;
  swap16(a, b);
  v = __builtin_elementwise_maximum(__uint_as_float(a), __uint_as_float(b));
  a = __float_as_uint(v); b = a;
  swap32(a, b);
  return __builtin_elementwise_maximum(__uint_as_float(a), __uint_as_float(b));
}
__device__ __forceinline__ float rowq_sum(float v) {
  unsigned a = __float_as_uint(v), b = a;
  swap16(a, b);
  v = __uint_as_float(a) + __uint_as_float(b);
  a = __float_as_uint(v); b = a;
  swap32(a, b);
  return __uint_as_float(a) + __uint_as_float(b);
}
__device__ __forceinline__ bool spin_fail(unsigned& spins, gu32* abortw) {
  ++spins;
  if ((spins & 63u) == 0u) {
    const __amdgpu_buffer_rsrc_t ra = mkrsrc((const void*)abortw, 4);
    unsigned z = 0;
    asm volatile("" : "+v"(z));
    if (__builtin_amdgcn_readfirstlane(__builtin_amdgcn_raw_buffer_load_b32(ra, z, 0, 16)) == 1u) return true;
    if (spins > (1u << 19)) {
      __builtin_amdgcn_raw_buffer_store_b32(z + 1u, ra, z, 0, 16);
      return true;
    }
  }
  return false;
}
__device__ __forceinline__ unsigned umax4(u32x4 v) { return max(max(v[0], v[1]), max(v[2], v[3])); }
__device__ __forceinline__ bool has_ds(u32x4 v) { return (v[0] == DSENT) | (v[1] == DSENT) | (v[2] == DSENT) | (v[3] == DSENT); }
__device__ __forceinline__ float fmax2(float a, float b) { return __builtin_elementwise_maximum(a, b); }
__device__ __forceinline__ bf16x8 ldsf(const char* p) { return *reinterpret_cast<const bf16x8*>(p); }
#define SCHED() __builtin_amdgcn_sched_barrier(0)
#define LBAR() do { asm volatile("s_waitcnt lgkmcnt(0)" ::: "memory"); __builtin_amdgcn_s_barrier(); asm volatile("" ::: "memory"); } while (0)
#define WAIT_V(n) asm volatile("s_waitcnt vmcnt(%0)" :: "n"(n) : "memory")

#define HOLD_V(c, n) asm volatile("s_cmp_eq_u32 %0, 0\n\ts_cbranch_scc1 .Lh%=\n\ts_waitcnt vmcnt(%1)\n.Lh%=:" :: "s"((int)(c)), "n"(n) : "memory", "scc")
#define TH_Q 2
#define TH_V 63
#define TH_R 63
#define TH_P 63
__device__ __forceinline__ void wait_flags(__amdgpu_buffer_rsrc_t rs, int off, gu32* abortw) {
  for (unsigned spins = 0;;) {
    asm volatile("" ::: "memory");
    const unsigned v = __builtin_amdgcn_raw_buffer_load_b32(rs, off, 0, 16);
    if (__all(v != SENT)) break;
    __builtin_amdgcn_s_sleep(4);
    if (spin_fail(spins, abortw)) break;
  }
  asm volatile("" ::: "memory");
}

#define LST(i) do {} while (0)
#define RDY(i) do {} while (0)
#define RDW(i) do {} while (0)
#define SMP(k, s, v) do {} while (0)
#define LSTC(i) do {} while (0)
__device__ __forceinline__ unsigned offA(unsigned row, unsigned ch) {
  return row * 128u + 16u * (ch ^ (row & 7u));
}
__device__ __forceinline__ unsigned swzB(unsigned row) { return ((row & 3u) << 2) | ((row >> 2) & 3u); }
__device__ __forceinline__ unsigned offB(unsigned row, unsigned ch) {
  return 256u * row + 16u * (ch ^ swzB(row));
}
__device__ __forceinline__ void glds16(const u16* src, char* dst) {
  const unsigned d = __builtin_amdgcn_readfirstlane(lds_off(dst));
  unsigned keep;
  asm volatile("s_mov_b32 %0, m0\n\ts_mov_b32 m0, %2\n\ts_nop 0\n\tglobal_load_lds_dwordx4 %1, off\n\ts_mov_b32 m0, %0"
               : "=&s"(keep) : "v"(src), "s"(d) : "memory");
}
__device__ __forceinline__ bf16x8 ld_bfrag(const char* sb, int ks, int c, int lane) {
  const unsigned g = lane >> 4, qq = (lane & 15) >> 2, pp = lane & 3;
  s16x4 lo = __builtin_amdgcn_ds_read_tr16_b64_v4i16(
      (lds_s16x4_ptr)(sb + offB(32 * ks + 8 * g + qq, 2 * c + (pp >> 1)) + 8 * (pp & 1)));
  s16x4 hi = __builtin_amdgcn_ds_read_tr16_b64_v4i16(
      (lds_s16x4_ptr)(sb + offB(32 * ks + 8 * g + 4 + qq, 2 * c + (pp >> 1)) + 8 * (pp & 1)));
  return (bf16x8){lo[0], lo[1], lo[2], lo[3], hi[0], hi[1], hi[2], hi[3]};
}

__device__ __forceinline__ void blk_dma(const u16* src, size_t ld, char* slot, int lane) {
  const u16* s = src + (size_t)(lane >> 2) * ld + (lane & 3) * 8;
  glds16(s, slot);
  glds16(s + 16 * ld, slot + 1024);
}
__device__ __forceinline__ bf16x8 blk_frag(const char* slot, int T, int lane) {
  const char* a = slot + ((lane >> 4) * 8 + ((lane & 15) >> 2)) * 64 + (lane & 3) * 16 + T * 8;
  s16x4 lo = __builtin_amdgcn_ds_read_tr16_b64_v4i16((lds_s16x4_ptr)a);
  s16x4 hi = __builtin_amdgcn_ds_read_tr16_b64_v4i16((lds_s16x4_ptr)(a + 256));
  return (bf16x8){lo[0], lo[1], lo[2], lo[3], hi[0], hi[1], hi[2], hi[3]};
}
template <int NB, typename ISSUE, typename SINK>
__device__ __forceinline__ void load_blocks(char* slice, ISSUE&& issue, SINK&& sink) {
#pragma unroll
  for (int i = 0; i < 8; ++i) issue(i, slice + i * 2048);
#pragma unroll
  for (int g = 0; g < NB / 4; ++g) {
    if (g + 1 < NB / 4) WAIT_V(8); else WAIT_V(0);
#pragma unroll
    for (int j = 0; j < 4; ++j) sink(4 * g + j, slice + ((4 * g + j) & 7) * 2048);
    asm volatile("s_waitcnt lgkmcnt(0)" ::: "memory");
    if (g + 2 < NB / 4) {
#pragma unroll
      for (int j = 0; j < 4; ++j) issue(4 * g + 8 + j, slice + ((4 * g + j) & 7) * 2048);
    }
  }
}

__device__ __forceinline__ u32x4 widen32(unsigned a0, unsigned a1, unsigned b0, unsigned b1) {
  swap32(a0, b0); swap16(a0, b0);
  swap32(a1, b1); swap16(a1, b1);
  return (u32x4){a0, a1, b0, b1};
}

__device__ __forceinline__ void publish_flags(__amdgpu_buffer_rsrc_t rs, int off, int stride, int n) {
  WAIT_V(0);
  LBAR();
  int tid = threadIdx.x;
  asm volatile("" : "+v"(tid));
  if (tid < n) __builtin_amdgcn_raw_buffer_store_b32(1u, rs, off + tid * stride, 0, 16);
}

__device__ __forceinline__ void wait_s(const Args& p, __amdgpu_buffer_rsrc_t rsy, int rblk, int c0, int n, bool need) {
  if (need) {
    if (threadIdx.x < 64) {
      for (unsigned spins = 0;;) {
        asm volatile("" ::: "memory");
        const int f = (int)__builtin_amdgcn_raw_buffer_load_b32(rsy, SY_FS + (2 * c0 + (threadIdx.x & (2 * n - 1))) * 128, 0, 16);
        if (__all(f >= rblk)) break;
        __builtin_amdgcn_s_sleep(32);
        if (spin_fail(spins, (gu32*)(p.ws + WS_SY + SY_ABORT))) break;
      }
    }
    LBAR();
  }
}
__device__ __forceinline__ u32x4 combine(u32x4 sv, u32x4 g, u32x4 x) {
  u32x4 o;
#pragma unroll
  for (int j = 0; j < 4; ++j) {
    const unsigned t = cvtpk(lo_f(sv[j]) * lo_f(g[j]), hi_f(sv[j]) * hi_f(g[j]));
    o[j] = cvtpk(lo_f(x[j]) + lo_f(t), hi_f(x[j]) + hi_f(t));
  }
  return o;
}
#define OUT_AUX 0
__device__ __forceinline__ void st_out(const Args& p, int e, u32x4 v) {
  __builtin_amdgcn_raw_buffer_store_b128(v, mkrsrc(p.out, S_LEN * DM * 2), e * 2, 0, OUT_AUX);
}
struct SX { u32x4 s, x; };
__device__ __forceinline__ SX get_sx(const Args& p, int rblk, int e) {
  SX r; r.s = (u32x4){0u, 0u, 0u, 0u}; r.x = r.s;
  if (rblk < NFUSE) {
    r.s = ld_sc1(mkrsrc(p.ws + WS_S, S_LEN * DM * 2), (((e & 1023) >> 5) * S_LEN + (e >> 10)) * 64 + (e & 31) * 2);
    r.x = *reinterpret_cast<const u32x4*>(p.x + e);
  }
  return r;
}
__device__ __forceinline__ void put_g(const Args& p, __amdgpu_buffer_rsrc_t rg, int rblk, int e, u32x4 g, const SX& v) {
  if (rblk < NFUSE) {
    st_out(p, e, combine(v.s, g, v.x));
  } else st_sc1(rg, e * 2, g);
}

__device__ __forceinline__ void job_comb(const Args& p, int rblk, int ct, __amdgpu_buffer_rsrc_t rsy) {
  int tid = threadIdx.x;
  asm volatile("" : "+v"(tid));
  u32x4 xv[8];
#pragma unroll
  for (int i = 0; i < 8; ++i) xv[i] = *reinterpret_cast<const u32x4*>(p.x + (rblk * 128 + i * 16 + (tid >> 5)) * DM + ct * 256 + (tid & 31) * 8);
  if (tid < 64) wait_flags(rsy, SY_FG + (rblk * 8 + 2 * ct + (tid & 1)) * 128, (gu32*)(p.ws + WS_SY + SY_ABORT));
  wait_s(p, rsy, rblk, ct * 8, 8, true);
  u32x4 sv[8], gv[8];
#pragma unroll
  for (int i = 0; i < 8; ++i) {
    const int e = (rblk * 128 + i * 16 + (tid >> 5)) * DM + ct * 256 + (tid & 31) * 8;
    sv[i] = ld_sc1(mkrsrc(p.ws + WS_S, S_LEN * DM * 2), (((e & 1023) >> 5) * S_LEN + (e >> 10)) * 64 + (e & 31) * 2);
    gv[i] = ld_sc1(mkrsrc(p.ws + WS_G, S_LEN * DM * 2), e * 2);
  }
#pragma unroll
  for (int i = 0; i < 8; ++i)
    st_out(p, (rblk * 128 + i * 16 + (tid >> 5)) * DM + ct * 256 + (tid & 31) * 8, combine(sv[i], gv[i], xv[i]));
}

__device__ __forceinline__ u32x4 raw_rsrc(const void* p, unsigned bytes) {
  const unsigned long long a = (unsigned long long)p;
  return (u32x4){(unsigned)a, (unsigned)(a >> 32) & 0xffffu, bytes, 0x00020000u};
}
__device__ __forceinline__ u32x2 next_tag(u32x2 e) { return (u32x2){e[0] + 1u, e[1] + (e[0] == 0xFFFFFFFFu)}; }
struct Boot { u32x2 e; u32x4 c; };
__device__ __forceinline__ void boot_ask(const char* ws, Boot& r) {
  const unsigned o = (threadIdx.x & 63) * 128;
  asm volatile("buffer_load_dwordx2 %0, off, %3, %4 sc1\n\tbuffer_load_dwordx4 %1, %2, %3, %5 offen sc1"
               : "=&v"(r.e), "=&v"(r.c) : "v"(o), "s"(raw_rsrc(ws, SY_TOTAL)), "s"(SY_EP), "s"(SY_BT) : "memory");
}
__device__ __forceinline__ void wait_rows(const char* ws, u32x2 tag) {
  const __amdgpu_buffer_rsrc_t rs = mkrsrc(ws, SY_TOTAL);
  int o = (threadIdx.x & 63) * 128;
  asm volatile("" : "+v"(o));
#pragma unroll 1
  for (int i = 0; i < (1 << 17); ++i) {
    asm volatile("" ::: "memory");
    const u32x2 a = __builtin_amdgcn_raw_buffer_load_b64(rs, o, SY_BT + 32 * 128, 16), b = __builtin_amdgcn_raw_buffer_load_b64(rs, o, SY_BT + 96 * 128, 16);
    if (__all(a[0] == tag[0] && a[1] == tag[1] && b[0] == tag[0] && b[1] == tag[1])) break;
    __builtin_amdgcn_s_sleep(8);
  }
  asm volatile("" ::: "memory");
}
__device__ __forceinline__ unsigned boot_done(const char* ws, Boot& r, bool own = false, u32x2 tag = (u32x2){0u, 0u}) {
#pragma unroll 1
  for (int i = 0; i < (1 << 17); ++i) {
    asm volatile("" : "+v"(r.e), "+v"(r.c));
    if (!own) tag = next_tag(r.e);
    if (__all(r.c[0] == tag[0] && r.c[1] == tag[1])) break;
    __builtin_amdgcn_s_sleep(8);
    boot_ask(ws, r);
    WAIT_V(0);
  }
  unsigned q0[NPAGE], q1[NPAGE], ga = 0xFFFFu, gb = 0xFFFFu;
#pragma unroll
  for (int i = 0; i < NPAGE; ++i) {
    const unsigned x = __builtin_amdgcn_readlane(r.c[2], i), y = __builtin_amdgcn_readlane(r.c[2], i + NPAGE);
    q0[i] = min(x >> 16, y >> 16); q1[i] = min(x & 0xFFFFu, y & 0xFFFFu);
    ga = min(ga, (i & 1) ? q1[i] : q0[i]); gb = min(gb, (i & 1) ? q0[i] : q1[i]);
  }
  unsigned m = gb < ga ? 0x80001555u : 0x2AAAu;
  const unsigned thr = min(ga, gb) + 110;
#pragma unroll
  for (int i = 0; i < NPAGE; ++i) {
    const unsigned lo = min(q0[i], q1[i]), hi = max(q0[i], q1[i]);
    const unsigned keep = (lo < thr ? hi : 0u) > lo + 100 ? ~(1u << i) : ~0u;
    m = (m & keep) | ((q1[i] < q0[i] ? ~0u : 0u) & ~keep);
  }
  return m;
}
typedef __attribute__((address_space(3))) volatile unsigned* lds_vu_ptr;
__device__ __forceinline__ lds_vu_ptr lds_at(const void* p) {
  unsigned a = lds_off(p);
  asm volatile("" : "+v"(a));
  return (lds_vu_ptr)(uintptr_t)a;
}
__device__ __forceinline__ unsigned uni(unsigned v) {
  unsigned r;
  asm volatile("s_nop 1\n\tv_readfirstlane_b32 %0, %1\n\ts_nop 3" : "=s"(r) : "v"(v));
  return r;
}
__device__ __forceinline__ void start_sy(char* ws, const u16* mem0, int i, char* smem) {
  const int tid = threadIdx.x, lane = tid & 63, w = __builtin_amdgcn_readfirstlane(tid >> 6);
  const __amdgpu_buffer_rsrc_t rsy = mkrsrc(ws, SY_TOTAL);
  const __amdgpu_buffer_rsrc_t rs = mkrsrc(ws + (size_t)(i % NPAGE) * PAGE + PROBE - (i / NPAGE) * 2048, 16384);
  const u32x2 e = __builtin_amdgcn_raw_buffer_load_b64(rsy, 0, SY_EP, 16);
  const u32x4 ds = {DSENT, DSENT, DSENT, DSENT};
  if (lane < 32) st_sc1(rs, (lane >> 4) * 8192 + w * 256 + (lane & 15) * 16, ds);
  static_assert(NPRE == 4 && SY_FEND - SY_BR <= 32 * 6144, "");
#pragma unroll
  for (int u = 0; u < 6; ++u)
    st_sc1(rsy, (u < 4 ? SY_V + (i * 4 + u) * 8192 : SY_O + (i * 2 + u - 4) * 8192) + tid * 16, ds);
  if (w == 7) {
    const int n = i * 64 + lane, j = (n >> 10) * 512 + (n & 511);
    const unsigned* m0 = reinterpret_cast<const unsigned*>(mem0 + (2 * ((j >> 2) & 3) + (j & 1)) * DM + (j >> 4) * 16) + ((j >> 1) & 1);
    u32x4 m;
#pragma unroll
    for (int k = 0; k < 4; ++k) { const unsigned t = m0[2 * k]; m[k] = t == DSENT ? 0xFFFFFFFFu : t; }
    st_sc1(rsy, n * 16, m);
  } else if (w == 6) st_sc1(rsy, 32768 + i * 1024 + lane * 16, ds);
  else {
    const int o = SY_BR + i * 6144 + tid * 16;
    st_sc1(rsy, o < SY_FEND ? o : 0x7F000000, (u32x4){SENT, SENT, SENT, SENT});
  }
  unsigned t[4];
#pragma unroll
  for (int k = 0; k < 4; ++k) {
    unsigned long long c0, c1; unsigned v, off = ((k ^ (k >> 1)) & 1) * 8192 + w * 256 + (k >> 1) * 128;
    asm volatile("s_waitcnt vmcnt(0)\n\ts_barrier\n\ts_memtime %0\n\ts_waitcnt lgkmcnt(0)\n\tbuffer_load_dword %2, %3, %4, 0 offen sc1\n\ts_waitcnt vmcnt(0)\n\t"
                 "s_memtime %1\n\ts_waitcnt lgkmcnt(0)" : "=&s"(c0), "=&s"(c1), "=&v"(v) : "v"(off), "s"(rs) : "memory");
    t[k] = (unsigned)(c1 - c0);
  }
  lds_vu_ptr tm = lds_at(smem);
  if (lane == 0) { tm[w] = min(t[0], t[3]); tm[8 + w] = min(t[1], t[2]); }
  int ro = SY_BT + i * 128 + lane * 16;
  asm volatile("" : "+v"(ro));
  LBAR();
  RDY(384 + i);
  if (threadIdx.x < 8) {
    unsigned m0 = tm[0], m1 = tm[8];
#pragma unroll
    for (int k = 1; k < 8; ++k) { m0 = min(m0, tm[k]); m1 = min(m1, tm[8 + k]); }
    const u32x2 tag = next_tag(e);
    st_sc1(rsy, ro, (u32x4){tag[0], tag[1], min(m0, 30000u) << 16 | min(m1, 30000u), 0u});
  }
}
template <int NB, typename F>
__device__ __forceinline__ u32x2 start_rows(const Args& p, int g, unsigned on, F&& between) {
  const int tid = threadIdx.x, lane = tid & 63, row0 = g * 16 + (tid >> 6) * 2;
  u32x4 a[4];
  u32x2 e;
  asm volatile("buffer_load_dwordx4 %0, %5, %8, 0 offen\n\tbuffer_load_dwordx4 %1, %5, %8, 0 offen offset:1024\n\t"
               "buffer_load_dwordx4 %2, %5, %8, 0 offen offset:2048\n\tbuffer_load_dwordx4 %3, %5, %8, 0 offen offset:3072\n\t"
               "buffer_load_dwordx2 %4, off, %6, %7 sc1"
               : "=&v"(a[0]), "=&v"(a[1]), "=&v"(a[2]), "=&v"(a[3]), "=&v"(e)
               : "v"(row0 * (DM * 2) + lane * 16), "s"(raw_rsrc(p.ws, SY_TOTAL)), "s"(SY_EP), "s"(raw_rsrc(p.x, on * (S_LEN * DM * 2))) : "memory");
  between();
  asm volatile("s_waitcnt vmcnt(%5)" : "+v"(a[0]), "+v"(a[1]), "+v"(a[2]), "+v"(a[3]), "+v"(e) : "n"(NB) : "memory");
  const __amdgpu_buffer_rsrc_t ro = mkrsrc(p.ws + WS_XN, on * (S_LEN * XLD * 2));
#pragma unroll
  for (int u = 0; u < 2; ++u) {
    float v[16];
#pragma unroll
    for (int i = 0; i < 8; ++i) { v[2 * i] = lo_f(a[2 * u + (i >> 2)][i & 3]); v[2 * i + 1] = hi_f(a[2 * u + (i >> 2)][i & 3]); }
    float s = 0.f;
#pragma unroll
    for (int i = 0; i < 16; ++i) s += v[i];
    const float mean = wave_sum(s) * (1.0f / DM);
    float q = 0.f;
#pragma unroll
    for (int i = 0; i < 16; ++i) { float d = v[i] - mean; q += d * d; }
    const float var = rbf(wave_sum(q) * (1.0f / DM));
    const float mb = rbf(mean);
    const float inv = 1.0f / rbf(sqrtf(rbf(var + 1e-5f)));
#pragma unroll
    for (int k = 0; k < 2; ++k) {
      u32x4 o;
#pragma unroll
      for (int i = 0; i < 4; ++i) {
        const unsigned t = cvtpk(v[8 * k + 2 * i] - mb, v[8 * k + 2 * i + 1] - mb);
        o[i] = cvtpk(lo_f(t) * inv, hi_f(t) * inv);
      }
      st_sc1(ro, (row0 + u) * (XLD * 2) + k * 1024 + lane * 16, o);
    }
  }
  return next_tag(e);
}
__device__ __forceinline__ void report_rows(const Args& p, int g, u32x2 tag) {
  RDY(256 + g);
  if (threadIdx.x < 8) st_sc1(mkrsrc(p.ws + WS_SY, SY_TOTAL), SY_BT + (32 + g) * 128 + threadIdx.x * 16, (u32x4){tag[0], tag[1], tag[0], tag[1]});
}

typedef __attribute__((ext_vector_type(32))) unsigned u32x32;
__device__ __forceinline__ void put4(u32x32& W, int i, bf16x8 f) {
  const u32x4 v = __builtin_bit_cast(u32x4, f);
#pragma unroll
  for (int j = 0; j < 4; ++j) W[4 * i + j] = v[j];
}
#define CLB8(a, b, c, d, e, f, g, h) "v" #a, "v" #b, "v" #c, "v" #d, "v" #e, "v" #f, "v" #g, "v" #h
#define CLB32(a) CLB32_##a
#define CLB32_128 CLB8(128, 129, 130, 131, 132, 133, 134, 135), CLB8(136, 137, 138, 139, 140, 141, 142, 143), CLB8(144, 145, 146, 147, 148, 149, 150, 151), CLB8(152, 153, 154, 155, 156, 157, 158, 159)
#define CLB32_160 CLB8(160, 161, 162, 163, 164, 165, 166, 167), CLB8(168, 169, 170, 171, 172, 173, 174, 175), CLB8(176, 177, 178, 179, 180, 181, 182, 183), CLB8(184, 185, 186, 187, 188, 189, 190, 191)
#define W4_IN "{v[0:31]}"(W[0]), "{v[32:63]}"(W[1]), "{v[64:95]}"(W[2]), "{v[96:127]}"(W[3])
#define CLB32_192 CLB8(192, 193, 194, 195, 196, 197, 198, 199), CLB8(200, 201, 202, 203, 204, 205, 206, 207), CLB8(208, 209, 210, 211, 212, 213, 214, 215), CLB8(216, 217, 218, 219, 220, 221, 222, 223)
__device__ __forceinline__ unsigned ld_flag0(__amdgpu_buffer_rsrc_t rs, int off) {
  return __builtin_amdgcn_raw_buffer_load_b32(rs, off, 0, 16);
}
__device__ __forceinline__ void glds16_sc1(const char* src, char* dst) {
  const unsigned d = __builtin_amdgcn_readfirstlane(lds_off(dst));
  unsigned keep;
  asm volatile("s_mov_b32 %0, m0\n\ts_mov_b32 m0, %2\n\ts_nop 0\n\tglobal_load_lds_dwordx4 %1, off sc1\n\ts_mov_b32 m0, %0"
               : "=&s"(keep) : "v"(src), "s"(d) : "memory");
}

__device__ __forceinline__ void touch_line(const char* src, char* dst) {
  const unsigned d = __builtin_amdgcn_readfirstlane(lds_off(dst));
  unsigned keep;
  asm volatile("s_mov_b32 %0, m0\n\ts_mov_b32 m0, %2\n\ts_nop 0\n\tglobal_load_lds_dword %1, off sc1\n\ts_mov_b32 m0, %0"
               : "=&s"(keep) : "v"(src), "s"(d) : "memory");
}

#define GPF 1
#define GU_DELAY 0
#define Q_DELAY 0
#define PF_T0 100
#define PF_N 12
#define A_AUX " sc1"
#define LN_M 3
#define LN_X 1
#define PR_G1 8
#define WREV 1
template <bool DEV = false>
__device__ __forceinline__ void glds2(const char* base, int step, unsigned voff, unsigned dst) {
  unsigned keep;
  const char* base2 = base + step;
  if (DEV)
    asm volatile("s_mov_b32 %0, m0\n\ts_mov_b32 m0, %4\n\ts_nop 0\n\tglobal_load_lds_dwordx4 %1, %2 sc1\n\t"
                 "s_add_u32 m0, m0, 0x2000\n\ts_nop 0\n\tglobal_load_lds_dwordx4 %1, %3 sc1\n\ts_mov_b32 m0, %0"
                 : "=&s"(keep) : "v"(voff), "s"(base), "s"(base2), "s"(dst) : "memory", "scc");
  else
  asm volatile("s_mov_b32 %0, m0\n\ts_mov_b32 m0, %4\n\ts_nop 0\n\tglobal_load_lds_dwordx4 %1, %2\n\t"
               "s_add_u32 m0, m0, 0x2000\n\ts_nop 0\n\tglobal_load_lds_dwordx4 %1, %3\n\ts_mov_b32 m0, %0"
               : "=&s"(keep) : "v"(voff), "s"(base), "s"(base2), "s"(dst) : "memory", "scc");
}

#define DN_ASM \
  "s_mov_b32 s94, m0\n\t" \
  "s_mov_b64 s[84:85], %[pa]\n\t" \
  "s_mov_b64 s[86:87], %[pb]\n\t" \
  "s_movk_i32 s88, 0x4000\n\t" \
  "s_mov_b32 s89, 0x10000\n\t" \
  "s_movk_i32 s90, 13\n\t" \
  "v_mov_b64 v[0:1], 0\n\t" \
  "v_mov_b64 v[2:3], 0\n\t" \
  "v_mov_b64 v[4:5], 0\n\t" \
  "v_mov_b64 v[6:7], 0\n\t" \
  "v_mov_b64 v[8:9], 0\n\t" \
  "v_mov_b64 v[10:11], 0\n\t" \
  "v_mov_b64 v[12:13], 0\n\t" \
  "v_mov_b64 v[14:15], 0\n\t" \
  "v_mov_b64 v[16:17], 0\n\t" \
  "v_mov_b64 v[18:19], 0\n\t" \
  "v_mov_b64 v[20:21], 0\n\t" \
  "v_mov_b64 v[22:23], 0\n\t" \
  "v_mov_b64 v[24:25], 0\n\t" \
  "v_mov_b64 v[26:27], 0\n\t" \
  "v_mov_b64 v[28:29], 0\n\t" \
  "v_mov_b64 v[30:31], 0\n\t" \
  "s_cmp_lg_u32 %[hf], 0\n\t" \
  "s_cbranch_scc1 .Lk%=\n\t" \
  "ds_read_b128 v[32:35], v80 offset:0\n\t" \
  "ds_read_b128 v[36:39], v80 offset:1024\n\t" \
  "ds_read_b128 v[40:43], v80 offset:2048\n\t" \
  "ds_read_b128 v[44:47], v80 offset:3072\n\t" \
  "ds_read_b64_tr_b16 v[64:65], v82 offset:0\n\t" \
  "ds_read_b64_tr_b16 v[66:67], v83 offset:0\n\t" \
  "ds_read_b128 v[48:51], v80 offset:8192\n\t" \
  "ds_read_b64_tr_b16 v[68:69], v84 offset:0\n\t" \
  "ds_read_b64_tr_b16 v[70:71], v85 offset:0\n\t" \
  "ds_read_b128 v[52:55], v80 offset:9216\n\t" \
  "ds_read_b128 v[56:59], v80 offset:10240\n\t" \
  "ds_read_b128 v[60:63], v80 offset:11264\n\t" \
  ".Lp0%=:\n\t" \
  "s_waitcnt vmcnt(4)\n\t" \
  "s_barrier\n\t" \
  "s_waitcnt lgkmcnt(3)\n\t" \
  "v_mfma_f32_16x16x32_bf16 v[0:3], v[64:67], v[32:35], v[0:3]\n\t" \
  "ds_read_b64_tr_b16 v[72:73], v82 offset:8192\n\t" \
  "v_mfma_f32_16x16x32_bf16 v[4:7], v[68:71], v[32:35], v[4:7]\n\t" \
  "ds_read_b64_tr_b16 v[74:75], v83 offset:8192\n\t" \
  "ds_read_b128 v[32:35], v80 offset:32768\n\t" \
  "s_add_u32 m0, %[lw], 98304\n\t" \
  "v_mfma_f32_16x16x32_bf16 v[8:11], v[64:67], v[36:39], v[8:11]\n\t" \
  "ds_read_b64_tr_b16 v[76:77], v84 offset:8192\n\t" \
  "global_load_lds_dwordx4 v90, s[84:85] sc1\n\t" \
  "s_add_u32 s84, s84, s88\n\t" \
  "s_addc_u32 s85, s85, 0\n\t" \
  "v_mfma_f32_16x16x32_bf16 v[12:15], v[68:71], v[36:39], v[12:15]\n\t" \
  "ds_read_b64_tr_b16 v[78:79], v85 offset:8192\n\t" \
  "ds_read_b128 v[36:39], v80 offset:33792\n\t" \
  "v_mfma_f32_16x16x32_bf16 v[16:19], v[64:67], v[40:43], v[16:19]\n\t" \
  "v_mfma_f32_16x16x32_bf16 v[20:23], v[68:71], v[40:43], v[20:23]\n\t" \
  "ds_read_b128 v[40:43], v80 offset:34816\n\t" \
  "s_add_u32 m0, %[lw], 114688\n\t" \
  "v_mfma_f32_16x16x32_bf16 v[24:27], v[64:67], v[44:47], v[24:27]\n\t" \
  "global_load_lds_dwordx4 v91, s[86:87]\n\t" \
  "s_add_u32 s86, s86, s89\n\t" \
  "s_addc_u32 s87, s87, 0\n\t" \
  "v_mfma_f32_16x16x32_bf16 v[28:31], v[68:71], v[44:47], v[28:31]\n\t" \
  "ds_read_b128 v[44:47], v80 offset:35840\n\t" \
  "s_waitcnt lgkmcnt(3)\n\t" \
  "v_mfma_f32_16x16x32_bf16 v[0:3], v[72:75], v[48:51], v[0:3]\n\t" \
  "ds_read_b64_tr_b16 v[64:65], v82 offset:32768\n\t" \
  "v_mfma_f32_16x16x32_bf16 v[4:7], v[76:79], v[48:51], v[4:7]\n\t" \
  "ds_read_b64_tr_b16 v[66:67], v83 offset:32768\n\t" \
  "ds_read_b128 v[48:51], v80 offset:40960\n\t" \
  "s_add_u32 m0, %[lw], 106496\n\t" \
  "v_mfma_f32_16x16x32_bf16 v[8:11], v[72:75], v[52:55], v[8:11]\n\t" \
  "ds_read_b64_tr_b16 v[68:69], v84 offset:32768\n\t" \
  "global_load_lds_dwordx4 v90, s[84:85] sc1\n\t" \
  "s_add_u32 s84, s84, s88\n\t" \
  "s_addc_u32 s85, s85, 0\n\t" \
  "v_mfma_f32_16x16x32_bf16 v[12:15], v[76:79], v[52:55], v[12:15]\n\t" \
  "ds_read_b64_tr_b16 v[70:71], v85 offset:32768\n\t" \
  "ds_read_b128 v[52:55], v80 offset:41984\n\t" \
  "v_mfma_f32_16x16x32_bf16 v[16:19], v[72:75], v[56:59], v[16:19]\n\t" \
  "v_mfma_f32_16x16x32_bf16 v[20:23], v[76:79], v[56:59], v[20:23]\n\t" \
  "ds_read_b128 v[56:59], v80 offset:43008\n\t" \
  "s_add_u32 m0, %[lw], 122880\n\t" \
  "v_mfma_f32_16x16x32_bf16 v[24:27], v[72:75], v[60:63], v[24:27]\n\t" \
  "global_load_lds_dwordx4 v91, s[86:87]\n\t" \
  "s_add_u32 s86, s86, s89\n\t" \
  "s_addc_u32 s87, s87, 0\n\t" \
  "v_mfma_f32_16x16x32_bf16 v[28:31], v[76:79], v[60:63], v[28:31]\n\t" \
  "ds_read_b128 v[60:63], v80 offset:44032\n\t" \
  "s_sub_u32 s90, s90, 1\n\t" \
  "s_cselect_b32 s88, 0, s88\n\t" \
  "s_cselect_b32 s89, 0, s89\n\t" \
  "s_cselect_b32 s91, 16384, 0\n\t" \
  "s_sub_u32 s84, s84, s91\n\t" \
  "s_subb_u32 s85, s85, 0\n\t" \
  "s_lshl_b32 s91, s91, 2\n\t" \
  "s_sub_u32 s86, s86, s91\n\t" \
  "s_subb_u32 s87, s87, 0\n\t" \
  "s_waitcnt vmcnt(4)\n\t" \
  "s_barrier\n\t" \
  "s_waitcnt lgkmcnt(3)\n\t" \
  "v_mfma_f32_16x16x32_bf16 v[0:3], v[64:67], v[32:35], v[0:3]\n\t" \
  "ds_read_b64_tr_b16 v[72:73], v82 offset:40960\n\t" \
  "v_mfma_f32_16x16x32_bf16 v[4:7], v[68:71], v[32:35], v[4:7]\n\t" \
  "ds_read_b64_tr_b16 v[74:75], v83 offset:40960\n\t" \
  "ds_read_b128 v[32:35], v81 offset:0\n\t" \
  "s_add_u32 m0, %[lw], 0\n\t" \
  "v_mfma_f32_16x16x32_bf16 v[8:11], v[64:67], v[36:39], v[8:11]\n\t" \
  "ds_read_b64_tr_b16 v[76:77], v84 offset:40960\n\t" \
  "global_load_lds_dwordx4 v90, s[84:85] sc1\n\t" \
  "s_add_u32 s84, s84, s88\n\t" \
  "s_addc_u32 s85, s85, 0\n\t" \
  "v_mfma_f32_16x16x32_bf16 v[12:15], v[68:71], v[36:39], v[12:15]\n\t" \
  "ds_read_b64_tr_b16 v[78:79], v85 offset:40960\n\t" \
  "ds_read_b128 v[36:39], v81 offset:1024\n\t" \
  "v_mfma_f32_16x16x32_bf16 v[16:19], v[64:67], v[40:43], v[16:19]\n\t" \
  "v_mfma_f32_16x16x32_bf16 v[20:23], v[68:71], v[40:43], v[20:23]\n\t" \
  "ds_read_b128 v[40:43], v81 offset:2048\n\t" \
  "s_add_u32 m0, %[lw], 16384\n\t" \
  "v_mfma_f32_16x16x32_bf16 v[24:27], v[64:67], v[44:47], v[24:27]\n\t" \
  "global_load_lds_dwordx4 v91, s[86:87]\n\t" \
  "s_add_u32 s86, s86, s89\n\t" \
  "s_addc_u32 s87, s87, 0\n\t" \
  "v_mfma_f32_16x16x32_bf16 v[28:31], v[68:71], v[44:47], v[28:31]\n\t" \
  "ds_read_b128 v[44:47], v81 offset:3072\n\t" \
  "s_waitcnt lgkmcnt(3)\n\t" \
  "v_mfma_f32_16x16x32_bf16 v[0:3], v[72:75], v[48:51], v[0:3]\n\t" \
  "ds_read_b64_tr_b16 v[64:65], v86 offset:0\n\t" \
  "v_mfma_f32_16x16x32_bf16 v[4:7], v[76:79], v[48:51], v[4:7]\n\t" \
  "ds_read_b64_tr_b16 v[66:67], v87 offset:0\n\t" \
  "ds_read_b128 v[48:51], v81 offset:8192\n\t" \
  "s_add_u32 m0, %[lw], 8192\n\t" \
  "v_mfma_f32_16x16x32_bf16 v[8:11], v[72:75], v[52:55], v[8:11]\n\t" \
  "ds_read_b64_tr_b16 v[68:69], v88 offset:0\n\t" \
  "global_load_lds_dwordx4 v90, s[84:85] sc1\n\t" \
  "s_add_u32 s84, s84, s88\n\t" \
  "s_addc_u32 s85, s85, 0\n\t" \
  "v_mfma_f32_16x16x32_bf16 v[12:15], v[76:79], v[52:55], v[12:15]\n\t" \
  "ds_read_b64_tr_b16 v[70:71], v89 offset:0\n\t" \
  "ds_read_b128 v[52:55], v81 offset:9216\n\t" \
  "v_mfma_f32_16x16x32_bf16 v[16:19], v[72:75], v[56:59], v[16:19]\n\t" \
  "v_mfma_f32_16x16x32_bf16 v[20:23], v[76:79], v[56:59], v[20:23]\n\t" \
  "ds_read_b128 v[56:59], v81 offset:10240\n\t" \
  "s_add_u32 m0, %[lw], 24576\n\t" \
  "v_mfma_f32_16x16x32_bf16 v[24:27], v[72:75], v[60:63], v[24:27]\n\t" \
  "global_load_lds_dwordx4 v91, s[86:87]\n\t" \
  "s_add_u32 s86, s86, s89\n\t" \
  "s_addc_u32 s87, s87, 0\n\t" \
  "v_mfma_f32_16x16x32_bf16 v[28:31], v[76:79], v[60:63], v[28:31]\n\t" \
  "ds_read_b128 v[60:63], v81 offset:11264\n\t" \
  "s_waitcnt vmcnt(4)\n\t" \
  "s_barrier\n\t" \
  "s_waitcnt lgkmcnt(3)\n\t" \
  "v_mfma_f32_16x16x32_bf16 v[0:3], v[64:67], v[32:35], v[0:3]\n\t" \
  "ds_read_b64_tr_b16 v[72:73], v86 offset:8192\n\t" \
  "v_mfma_f32_16x16x32_bf16 v[4:7], v[68:71], v[32:35], v[4:7]\n\t" \
  "ds_read_b64_tr_b16 v[74:75], v87 offset:8192\n\t" \
  "ds_read_b128 v[32:35], v81 offset:32768\n\t" \
  "s_add_u32 m0, %[lw], 32768\n\t" \
  "v_mfma_f32_16x16x32_bf16 v[8:11], v[64:67], v[36:39], v[8:11]\n\t" \
  "ds_read_b64_tr_b16 v[76:77], v88 offset:8192\n\t" \
  "global_load_lds_dwordx4 v90, s[84:85] sc1\n\t" \
  "s_add_u32 s84, s84, s88\n\t" \
  "s_addc_u32 s85, s85, 0\n\t" \
  "v_mfma_f32_16x16x32_bf16 v[12:15], v[68:71], v[36:39], v[12:15]\n\t" \
  "ds_read_b64_tr_b16 v[78:79], v89 offset:8192\n\t" \
  "ds_read_b128 v[36:39], v81 offset:33792\n\t" \
  "v_mfma_f32_16x16x32_bf16 v[16:19], v[64:67], v[40:43], v[16:19]\n\t" \
  "v_mfma_f32_16x16x32_bf16 v[20:23], v[68:71], v[40:43], v[20:23]\n\t" \
  "ds_read_b128 v[40:43], v81 offset:34816\n\t" \
  "s_add_u32 m0, %[lw], 49152\n\t" \
  "v_mfma_f32_16x16x32_bf16 v[24:27], v[64:67], v[44:47], v[24:27]\n\t" \
  "global_load_lds_dwordx4 v91, s[86:87]\n\t" \
  "s_add_u32 s86, s86, s89\n\t" \
  "s_addc_u32 s87, s87, 0\n\t" \
  "v_mfma_f32_16x16x32_bf16 v[28:31], v[68:71], v[44:47], v[28:31]\n\t" \
  "ds_read_b128 v[44:47], v81 offset:35840\n\t" \
  "s_waitcnt lgkmcnt(3)\n\t" \
  "v_mfma_f32_16x16x32_bf16 v[0:3], v[72:75], v[48:51], v[0:3]\n\t" \
  "ds_read_b64_tr_b16 v[64:65], v86 offset:32768\n\t" \
  "v_mfma_f32_16x16x32_bf16 v[4:7], v[76:79], v[48:51], v[4:7]\n\t" \
  "ds_read_b64_tr_b16 v[66:67], v87 offset:32768\n\t" \
  "ds_read_b128 v[48:51], v81 offset:40960\n\t" \
  "s_add_u32 m0, %[lw], 40960\n\t" \
  "v_mfma_f32_16x16x32_bf16 v[8:11], v[72:75], v[52:55], v[8:11]\n\t" \
  "ds_read_b64_tr_b16 v[68:69], v88 offset:32768\n\t" \
  "global_load_lds_dwordx4 v90, s[84:85] sc1\n\t" \
  "s_add_u32 s84, s84, s88\n\t" \
  "s_addc_u32 s85, s85, 0\n\t" \
  "v_mfma_f32_16x16x32_bf16 v[12:15], v[76:79], v[52:55], v[12:15]\n\t" \
  "ds_read_b64_tr_b16 v[70:71], v89 offset:32768\n\t" \
  "ds_read_b128 v[52:55], v81 offset:41984\n\t" \
  "v_mfma_f32_16x16x32_bf16 v[16:19], v[72:75], v[56:59], v[16:19]\n\t" \
  "v_mfma_f32_16x16x32_bf16 v[20:23], v[76:79], v[56:59], v[20:23]\n\t" \
  "ds_read_b128 v[56:59], v81 offset:43008\n\t" \
  "s_add_u32 m0, %[lw], 57344\n\t" \
  "v_mfma_f32_16x16x32_bf16 v[24:27], v[72:75], v[60:63], v[24:27]\n\t" \
  "global_load_lds_dwordx4 v91, s[86:87]\n\t" \
  "s_add_u32 s86, s86, s89\n\t" \
  "s_addc_u32 s87, s87, 0\n\t" \
  "v_mfma_f32_16x16x32_bf16 v[28:31], v[76:79], v[60:63], v[28:31]\n\t" \
  "ds_read_b128 v[60:63], v81 offset:44032\n\t" \
  "s_waitcnt vmcnt(4)\n\t" \
  "s_barrier\n\t" \
  "s_waitcnt lgkmcnt(3)\n\t" \
  "v_mfma_f32_16x16x32_bf16 v[0:3], v[64:67], v[32:35], v[0:3]\n\t" \
  "ds_read_b64_tr_b16 v[72:73], v86 offset:40960\n\t" \
  "v_mfma_f32_16x16x32_bf16 v[4:7], v[68:71], v[32:35], v[4:7]\n\t" \
  "ds_read_b64_tr_b16 v[74:75], v87 offset:40960\n\t" \
  "ds_read_b128 v[32:35], v80 offset:0\n\t" \
  "s_add_u32 m0, %[lw], 65536\n\t" \
  "v_mfma_f32_16x16x32_bf16 v[8:11], v[64:67], v[36:39], v[8:11]\n\t" \
  "ds_read_b64_tr_b16 v[76:77], v88 offset:40960\n\t" \
  "global_load_lds_dwordx4 v90, s[84:85] sc1\n\t" \
  "s_add_u32 s84, s84, s88\n\t" \
  "s_addc_u32 s85, s85, 0\n\t" \
  "v_mfma_f32_16x16x32_bf16 v[12:15], v[68:71], v[36:39], v[12:15]\n\t" \
  "ds_read_b64_tr_b16 v[78:79], v89 offset:40960\n\t" \
  "ds_read_b128 v[36:39], v80 offset:1024\n\t" \
  "v_mfma_f32_16x16x32_bf16 v[16:19], v[64:67], v[40:43], v[16:19]\n\t" \
  "v_mfma_f32_16x16x32_bf16 v[20:23], v[68:71], v[40:43], v[20:23]\n\t" \
  "ds_read_b128 v[40:43], v80 offset:2048\n\t" \
  "s_add_u32 m0, %[lw], 81920\n\t" \
  "v_mfma_f32_16x16x32_bf16 v[24:27], v[64:67], v[44:47], v[24:27]\n\t" \
  "global_load_lds_dwordx4 v91, s[86:87]\n\t" \
  "s_add_u32 s86, s86, s89\n\t" \
  "s_addc_u32 s87, s87, 0\n\t" \
  "v_mfma_f32_16x16x32_bf16 v[28:31], v[68:71], v[44:47], v[28:31]\n\t" \
  "ds_read_b128 v[44:47], v80 offset:3072\n\t" \
  "s_waitcnt lgkmcnt(3)\n\t" \
  "v_mfma_f32_16x16x32_bf16 v[0:3], v[72:75], v[48:51], v[0:3]\n\t" \
  "ds_read_b64_tr_b16 v[64:65], v82 offset:0\n\t" \
  "v_mfma_f32_16x16x32_bf16 v[4:7], v[76:79], v[48:51], v[4:7]\n\t" \
  "ds_read_b64_tr_b16 v[66:67], v83 offset:0\n\t" \
  "ds_read_b128 v[48:51], v80 offset:8192\n\t" \
  "s_add_u32 m0, %[lw], 73728\n\t" \
  "v_mfma_f32_16x16x32_bf16 v[8:11], v[72:75], v[52:55], v[8:11]\n\t" \
  "ds_read_b64_tr_b16 v[68:69], v84 offset:0\n\t" \
  "global_load_lds_dwordx4 v90, s[84:85] sc1\n\t" \
  "s_add_u32 s84, s84, s88\n\t" \
  "s_addc_u32 s85, s85, 0\n\t" \
  "v_mfma_f32_16x16x32_bf16 v[12:15], v[76:79], v[52:55], v[12:15]\n\t" \
  "ds_read_b64_tr_b16 v[70:71], v85 offset:0\n\t" \
  "ds_read_b128 v[52:55], v80 offset:9216\n\t" \
  "v_mfma_f32_16x16x32_bf16 v[16:19], v[72:75], v[56:59], v[16:19]\n\t" \
  "v_mfma_f32_16x16x32_bf16 v[20:23], v[76:79], v[56:59], v[20:23]\n\t" \
  "ds_read_b128 v[56:59], v80 offset:10240\n\t" \
  "s_add_u32 m0, %[lw], 90112\n\t" \
  "v_mfma_f32_16x16x32_bf16 v[24:27], v[72:75], v[60:63], v[24:27]\n\t" \
  "global_load_lds_dwordx4 v91, s[86:87]\n\t" \
  "s_add_u32 s86, s86, s89\n\t" \
  "s_addc_u32 s87, s87, 0\n\t" \
  "v_mfma_f32_16x16x32_bf16 v[28:31], v[76:79], v[60:63], v[28:31]\n\t" \
  "ds_read_b128 v[60:63], v80 offset:11264\n\t" \
  "s_cmp_lg_u32 s88, 0\n\t" \
  "s_cbranch_scc1 .Lp0%=\n\t" \
  "s_branch .Le%=\n\t" \
  ".Lk%=:\n\t" \
  "ds_read_b128 v[32:35], v80 offset:0\n\t" \
  "ds_read_b128 v[36:39], v80 offset:1024\n\t" \
  "ds_read_b128 v[40:43], v80 offset:2048\n\t" \
  "ds_read_b128 v[44:47], v80 offset:3072\n\t" \
  "ds_read_b64_tr_b16 v[64:65], v82 offset:0\n\t" \
  "ds_read_b64_tr_b16 v[66:67], v83 offset:0\n\t" \
  "ds_read_b128 v[48:51], v80 offset:8192\n\t" \
  "ds_read_b64_tr_b16 v[68:69], v84 offset:0\n\t" \
  "ds_read_b64_tr_b16 v[70:71], v85 offset:0\n\t" \
  "ds_read_b128 v[52:55], v80 offset:9216\n\t" \
  "ds_read_b128 v[56:59], v80 offset:10240\n\t" \
  "ds_read_b128 v[60:63], v80 offset:11264\n\t" \
  ".Lp1%=:\n\t" \
  "s_waitcnt vmcnt(4)\n\t" \
  "s_barrier\n\t" \
  "s_waitcnt lgkmcnt(3)\n\t" \
  "v_mfma_f32_16x16x32_bf16 v[0:3], v[64:67], v[32:35], v[0:3]\n\t" \
  "ds_read_b64_tr_b16 v[72:73], v82 offset:8192\n\t" \
  "v_mfma_f32_16x16x32_bf16 v[4:7], v[68:71], v[32:35], v[4:7]\n\t" \
  "ds_read_b64_tr_b16 v[74:75], v83 offset:8192\n\t" \
  "ds_read_b128 v[32:35], v80 offset:32768\n\t" \
  "v_mfma_f32_16x16x32_bf16 v[8:11], v[64:67], v[36:39], v[8:11]\n\t" \
  "ds_read_b64_tr_b16 v[76:77], v84 offset:8192\n\t" \
  "v_mfma_f32_16x16x32_bf16 v[12:15], v[68:71], v[36:39], v[12:15]\n\t" \
  "ds_read_b64_tr_b16 v[78:79], v85 offset:8192\n\t" \
  "ds_read_b128 v[36:39], v80 offset:33792\n\t" \
  "s_add_u32 m0, %[lw], 98304\n\t" \
  "v_mfma_f32_16x16x32_bf16 v[16:19], v[64:67], v[40:43], v[16:19]\n\t" \
  "global_load_lds_dwordx4 v90, s[84:85] sc1\n\t" \
  "s_add_u32 s84, s84, s88\n\t" \
  "s_addc_u32 s85, s85, 0\n\t" \
  "v_mfma_f32_16x16x32_bf16 v[20:23], v[68:71], v[40:43], v[20:23]\n\t" \
  "ds_read_b128 v[40:43], v80 offset:34816\n\t" \
  "v_mfma_f32_16x16x32_bf16 v[24:27], v[64:67], v[44:47], v[24:27]\n\t" \
  "s_add_u32 m0, %[lw], 114688\n\t" \
  "v_mfma_f32_16x16x32_bf16 v[28:31], v[68:71], v[44:47], v[28:31]\n\t" \
  "ds_read_b128 v[44:47], v80 offset:35840\n\t" \
  "global_load_lds_dwordx4 v91, s[86:87]\n\t" \
  "s_add_u32 s86, s86, s89\n\t" \
  "s_addc_u32 s87, s87, 0\n\t" \
  "s_waitcnt lgkmcnt(3)\n\t" \
  "v_mfma_f32_16x16x32_bf16 v[0:3], v[72:75], v[48:51], v[0:3]\n\t" \
  "ds_read_b64_tr_b16 v[64:65], v82 offset:32768\n\t" \
  "v_mfma_f32_16x16x32_bf16 v[4:7], v[76:79], v[48:51], v[4:7]\n\t" \
  "ds_read_b64_tr_b16 v[66:67], v83 offset:32768\n\t" \
  "ds_read_b128 v[48:51], v80 offset:40960\n\t" \
  "v_mfma_f32_16x16x32_bf16 v[8:11], v[72:75], v[52:55], v[8:11]\n\t" \
  "ds_read_b64_tr_b16 v[68:69], v84 offset:32768\n\t" \
  "v_mfma_f32_16x16x32_bf16 v[12:15], v[76:79], v[52:55], v[12:15]\n\t" \
  "ds_read_b64_tr_b16 v[70:71], v85 offset:32768\n\t" \
  "ds_read_b128 v[52:55], v80 offset:41984\n\t" \
  "s_add_u32 m0, %[lw], 106496\n\t" \
  "v_mfma_f32_16x16x32_bf16 v[16:19], v[72:75], v[56:59], v[16:19]\n\t" \
  "global_load_lds_dwordx4 v90, s[84:85] sc1\n\t" \
  "s_add_u32 s84, s84, s88\n\t" \
  "s_addc_u32 s85, s85, 0\n\t" \
  "v_mfma_f32_16x16x32_bf16 v[20:23], v[76:79], v[56:59], v[20:23]\n\t" \
  "ds_read_b128 v[56:59], v80 offset:43008\n\t" \
  "v_mfma_f32_16x16x32_bf16 v[24:27], v[72:75], v[60:63], v[24:27]\n\t" \
  "s_add_u32 m0, %[lw], 122880\n\t" \
  "v_mfma_f32_16x16x32_bf16 v[28:31], v[76:79], v[60:63], v[28:31]\n\t" \
  "ds_read_b128 v[60:63], v80 offset:44032\n\t" \
  "global_load_lds_dwordx4 v91, s[86:87]\n\t" \
  "s_add_u32 s86, s86, s89\n\t" \
  "s_addc_u32 s87, s87, 0\n\t" \
  "s_sub_u32 s90, s90, 1\n\t" \
  "s_cselect_b32 s88, 0, s88\n\t" \
  "s_cselect_b32 s89, 0, s89\n\t" \
  "s_cselect_b32 s91, 16384, 0\n\t" \
  "s_sub_u32 s84, s84, s91\n\t" \
  "s_subb_u32 s85, s85, 0\n\t" \
  "s_lshl_b32 s91, s91, 2\n\t" \
  "s_sub_u32 s86, s86, s91\n\t" \
  "s_subb_u32 s87, s87, 0\n\t" \
  "s_waitcnt vmcnt(4)\n\t" \
  "s_barrier\n\t" \
  "s_waitcnt lgkmcnt(3)\n\t" \
  "v_mfma_f32_16x16x32_bf16 v[0:3], v[64:67], v[32:35], v[0:3]\n\t" \
  "ds_read_b64_tr_b16 v[72:73], v82 offset:40960\n\t" \
  "v_mfma_f32_16x16x32_bf16 v[4:7], v[68:71], v[32:35], v[4:7]\n\t" \
  "ds_read_b64_tr_b16 v[74:75], v83 offset:40960\n\t" \
  "ds_read_b128 v[32:35], v81 offset:0\n\t" \
  "v_mfma_f32_16x16x32_bf16 v[8:11], v[64:67], v[36:39], v[8:11]\n\t" \
  "ds_read_b64_tr_b16 v[76:77], v84 offset:40960\n\t" \
  "v_mfma_f32_16x16x32_bf16 v[12:15], v[68:71], v[36:39], v[12:15]\n\t" \
  "ds_read_b64_tr_b16 v[78:79], v85 offset:40960\n\t" \
  "ds_read_b128 v[36:39], v81 offset:1024\n\t" \
  "s_add_u32 m0, %[lw], 0\n\t" \
  "v_mfma_f32_16x16x32_bf16 v[16:19], v[64:67], v[40:43], v[16:19]\n\t" \
  "global_load_lds_dwordx4 v90, s[84:85] sc1\n\t" \
  "s_add_u32 s84, s84, s88\n\t" \
  "s_addc_u32 s85, s85, 0\n\t" \
  "v_mfma_f32_16x16x32_bf16 v[20:23], v[68:71], v[40:43], v[20:23]\n\t" \
  "ds_read_b128 v[40:43], v81 offset:2048\n\t" \
  "v_mfma_f32_16x16x32_bf16 v[24:27], v[64:67], v[44:47], v[24:27]\n\t" \
  "s_add_u32 m0, %[lw], 16384\n\t" \
  "v_mfma_f32_16x16x32_bf16 v[28:31], v[68:71], v[44:47], v[28:31]\n\t" \
  "ds_read_b128 v[44:47], v81 offset:3072\n\t" \
  "global_load_lds_dwordx4 v91, s[86:87]\n\t" \
  "s_add_u32 s86, s86, s89\n\t" \
  "s_addc_u32 s87, s87, 0\n\t" \
  "s_waitcnt lgkmcnt(3)\n\t" \
  "v_mfma_f32_16x16x32_bf16 v[0:3], v[72:75], v[48:51], v[0:3]\n\t" \
  "ds_read_b64_tr_b16 v[64:65], v86 offset:0\n\t" \
  "v_mfma_f32_16x16x32_bf16 v[4:7], v[76:79], v[48:51], v[4:7]\n\t" \
  "ds_read_b64_tr_b16 v[66:67], v87 offset:0\n\t" \
  "ds_read_b128 v[48:51], v81 offset:8192\n\t" \
  "v_mfma_f32_16x16x32_bf16 v[8:11], v[72:75], v[52:55], v[8:11]\n\t" \
  "ds_read_b64_tr_b16 v[68:69], v88 offset:0\n\t" \
  "v_mfma_f32_16x16x32_bf16 v[12:15], v[76:79], v[52:55], v[12:15]\n\t" \
  "ds_read_b64_tr_b16 v[70:71], v89 offset:0\n\t" \
  "ds_read_b128 v[52:55], v81 offset:9216\n\t" \
  "s_add_u32 m0, %[lw], 8192\n\t" \
  "v_mfma_f32_16x16x32_bf16 v[16:19], v[72:75], v[56:59], v[16:19]\n\t" \
  "global_load_lds_dwordx4 v90, s[84:85] sc1\n\t" \
  "s_add_u32 s84, s84, s88\n\t" \
  "s_addc_u32 s85, s85, 0\n\t" \
  "v_mfma_f32_16x16x32_bf16 v[20:23], v[76:79], v[56:59], v[20:23]\n\t" \
  "ds_read_b128 v[56:59], v81 offset:10240\n\t" \
  "v_mfma_f32_16x16x32_bf16 v[24:27], v[72:75], v[60:63], v[24:27]\n\t" \
  "s_add_u32 m0, %[lw], 24576\n\t" \
  "v_mfma_f32_16x16x32_bf16 v[28:31], v[76:79], v[60:63], v[28:31]\n\t" \
  "ds_read_b128 v[60:63], v81 offset:11264\n\t" \
  "global_load_lds_dwordx4 v91, s[86:87]\n\t" \
  "s_add_u32 s86, s86, s89\n\t" \
  "s_addc_u32 s87, s87, 0\n\t" \
  "s_waitcnt vmcnt(4)\n\t" \
  "s_barrier\n\t" \
  "s_waitcnt lgkmcnt(3)\n\t" \
  "v_mfma_f32_16x16x32_bf16 v[0:3], v[64:67], v[32:35], v[0:3]\n\t" \
  "ds_read_b64_tr_b16 v[72:73], v86 offset:8192\n\t" \
  "v_mfma_f32_16x16x32_bf16 v[4:7], v[68:71], v[32:35], v[4:7]\n\t" \
  "ds_read_b64_tr_b16 v[74:75], v87 offset:8192\n\t" \
  "ds_read_b128 v[32:35], v81 offset:32768\n\t" \
  "v_mfma_f32_16x16x32_bf16 v[8:11], v[64:67], v[36:39], v[8:11]\n\t" \
  "ds_read_b64_tr_b16 v[76:77], v88 offset:8192\n\t" \
  "v_mfma_f32_16x16x32_bf16 v[12:15], v[68:71], v[36:39], v[12:15]\n\t" \
  "ds_read_b64_tr_b16 v[78:79], v89 offset:8192\n\t" \
  "ds_read_b128 v[36:39], v81 offset:33792\n\t" \
  "s_add_u32 m0, %[lw], 32768\n\t" \
  "v_mfma_f32_16x16x32_bf16 v[16:19], v[64:67], v[40:43], v[16:19]\n\t" \
  "global_load_lds_dwordx4 v90, s[84:85] sc1\n\t" \
  "s_add_u32 s84, s84, s88\n\t" \
  "s_addc_u32 s85, s85, 0\n\t" \
  "v_mfma_f32_16x16x32_bf16 v[20:23], v[68:71], v[40:43], v[20:23]\n\t" \
  "ds_read_b128 v[40:43], v81 offset:34816\n\t" \
  "v_mfma_f32_16x16x32_bf16 v[24:27], v[64:67], v[44:47], v[24:27]\n\t" \
  "s_add_u32 m0, %[lw], 49152\n\t" \
  "v_mfma_f32_16x16x32_bf16 v[28:31], v[68:71], v[44:47], v[28:31]\n\t" \
  "ds_read_b128 v[44:47], v81 offset:35840\n\t" \
  "global_load_lds_dwordx4 v91, s[86:87]\n\t" \
  "s_add_u32 s86, s86, s89\n\t" \
  "s_addc_u32 s87, s87, 0\n\t" \
  "s_waitcnt lgkmcnt(3)\n\t" \
  "v_mfma_f32_16x16x32_bf16 v[0:3], v[72:75], v[48:51], v[0:3]\n\t" \
  "ds_read_b64_tr_b16 v[64:65], v86 offset:32768\n\t" \
  "v_mfma_f32_16x16x32_bf16 v[4:7], v[76:79], v[48:51], v[4:7]\n\t" \
  "ds_read_b64_tr_b16 v[66:67], v87 offset:32768\n\t" \
  "ds_read_b128 v[48:51], v81 offset:40960\n\t" \
  "v_mfma_f32_16x16x32_bf16 v[8:11], v[72:75], v[52:55], v[8:11]\n\t" \
  "ds_read_b64_tr_b16 v[68:69], v88 offset:32768\n\t" \
  "v_mfma_f32_16x16x32_bf16 v[12:15], v[76:79], v[52:55], v[12:15]\n\t" \
  "ds_read_b64_tr_b16 v[70:71], v89 offset:32768\n\t" \
  "ds_read_b128 v[52:55], v81 offset:41984\n\t" \
  "s_add_u32 m0, %[lw], 40960\n\t" \
  "v_mfma_f32_16x16x32_bf16 v[16:19], v[72:75], v[56:59], v[16:19]\n\t" \
  "global_load_lds_dwordx4 v90, s[84:85] sc1\n\t" \
  "s_add_u32 s84, s84, s88\n\t" \
  "s_addc_u32 s85, s85, 0\n\t" \
  "v_mfma_f32_16x16x32_bf16 v[20:23], v[76:79], v[56:59], v[20:23]\n\t" \
  "ds_read_b128 v[56:59], v81 offset:43008\n\t" \
  "v_mfma_f32_16x16x32_bf16 v[24:27], v[72:75], v[60:63], v[24:27]\n\t" \
  "s_add_u32 m0, %[lw], 57344\n\t" \
  "v_mfma_f32_16x16x32_bf16 v[28:31], v[76:79], v[60:63], v[28:31]\n\t" \
  "ds_read_b128 v[60:63], v81 offset:44032\n\t" \
  "global_load_lds_dwordx4 v91, s[86:87]\n\t" \
  "s_add_u32 s86, s86, s89\n\t" \
  "s_addc_u32 s87, s87, 0\n\t" \
  "s_waitcnt vmcnt(4)\n\t" \
  "s_barrier\n\t" \
  "s_waitcnt lgkmcnt(3)\n\t" \
  "v_mfma_f32_16x16x32_bf16 v[0:3], v[64:67], v[32:35], v[0:3]\n\t" \
  "ds_read_b64_tr_b16 v[72:73], v86 offset:40960\n\t" \
  "v_mfma_f32_16x16x32_bf16 v[4:7], v[68:71], v[32:35], v[4:7]\n\t" \
  "ds_read_b64_tr_b16 v[74:75], v87 offset:40960\n\t" \
  "ds_read_b128 v[32:35], v80 offset:0\n\t" \
  "v_mfma_f32_16x16x32_bf16 v[8:11], v[64:67], v[36:39], v[8:11]\n\t" \
  "ds_read_b64_tr_b16 v[76:77], v88 offset:40960\n\t" \
  "v_mfma_f32_16x16x32_bf16 v[12:15], v[68:71], v[36:39], v[12:15]\n\t" \
  "ds_read_b64_tr_b16 v[78:79], v89 offset:40960\n\t" \
  "ds_read_b128 v[36:39], v80 offset:1024\n\t" \
  "s_add_u32 m0, %[lw], 65536\n\t" \
  "v_mfma_f32_16x16x32_bf16 v[16:19], v[64:67], v[40:43], v[16:19]\n\t" \
  "global_load_lds_dwordx4 v90, s[84:85] sc1\n\t" \
  "s_add_u32 s84, s84, s88\n\t" \
  "s_addc_u32 s85, s85, 0\n\t" \
  "v_mfma_f32_16x16x32_bf16 v[20:23], v[68:71], v[40:43], v[20:23]\n\t" \
  "ds_read_b128 v[40:43], v80 offset:2048\n\t" \
  "v_mfma_f32_16x16x32_bf16 v[24:27], v[64:67], v[44:47], v[24:27]\n\t" \
  "s_add_u32 m0, %[lw], 81920\n\t" \
  "v_mfma_f32_16x16x32_bf16 v[28:31], v[68:71], v[44:47], v[28:31]\n\t" \
  "ds_read_b128 v[44:47], v80 offset:3072\n\t" \
  "global_load_lds_dwordx4 v91, s[86:87]\n\t" \
  "s_add_u32 s86, s86, s89\n\t" \
  "s_addc_u32 s87, s87, 0\n\t" \
  "s_waitcnt lgkmcnt(3)\n\t" \
  "v_mfma_f32_16x16x32_bf16 v[0:3], v[72:75], v[48:51], v[0:3]\n\t" \
  "ds_read_b64_tr_b16 v[64:65], v82 offset:0\n\t" \
  "v_mfma_f32_16x16x32_bf16 v[4:7], v[76:79], v[48:51], v[4:7]\n\t" \
  "ds_read_b64_tr_b16 v[66:67], v83 offset:0\n\t" \
  "ds_read_b128 v[48:51], v80 offset:8192\n\t" \
  "v_mfma_f32_16x16x32_bf16 v[8:11], v[72:75], v[52:55], v[8:11]\n\t" \
  "ds_read_b64_tr_b16 v[68:69], v84 offset:0\n\t" \
  "v_mfma_f32_16x16x32_bf16 v[12:15], v[76:79], v[52:55], v[12:15]\n\t" \
  "ds_read_b64_tr_b16 v[70:71], v85 offset:0\n\t" \
  "ds_read_b128 v[52:55], v80 offset:9216\n\t" \
  "s_add_u32 m0, %[lw], 73728\n\t" \
  "v_mfma_f32_16x16x32_bf16 v[16:19], v[72:75], v[56:59], v[16:19]\n\t" \
  "global_load_lds_dwordx4 v90, s[84:85] sc1\n\t" \
  "s_add_u32 s84, s84, s88\n\t" \
  "s_addc_u32 s85, s85, 0\n\t" \
  "v_mfma_f32_16x16x32_bf16 v[20:23], v[76:79], v[56:59], v[20:23]\n\t" \
  "ds_read_b128 v[56:59], v80 offset:10240\n\t" \
  "v_mfma_f32_16x16x32_bf16 v[24:27], v[72:75], v[60:63], v[24:27]\n\t" \
  "s_add_u32 m0, %[lw], 90112\n\t" \
  "v_mfma_f32_16x16x32_bf16 v[28:31], v[76:79], v[60:63], v[28:31]\n\t" \
  "ds_read_b128 v[60:63], v80 offset:11264\n\t" \
  "global_load_lds_dwordx4 v91, s[86:87]\n\t" \
  "s_add_u32 s86, s86, s89\n\t" \
  "s_addc_u32 s87, s87, 0\n\t" \
  "s_cmp_lg_u32 s88, 0\n\t" \
  "s_cbranch_scc1 .Lp1%=\n\t" \
  ".Le%=:\n\t" \
  "s_waitcnt vmcnt(0) lgkmcnt(0)\n\t" \
  "s_mov_b32 m0, s94\n\t" \
  "s_setprio 0\n\t" \
  "s_nop 7\n\t"
#define HSL 16
__device__ __forceinline__ void job_down(const Args& p, int rblk, int ct, char* smem, __amdgpu_buffer_rsrc_t rsy) {
  if (threadIdx.x < 64) {
    for (unsigned spins = 0;;) {
      asm volatile("" ::: "memory");
      if (__builtin_amdgcn_readfirstlane((int)ld_flag0(rsy, SY_CH + (rblk >> 1) * 128)) >= 27) break;
      __builtin_amdgcn_s_sleep(HSL);
      if (spin_fail(spins, (gu32*)(p.ws + WS_SY + SY_ABORT))) break;
    }
  }
  int tid = threadIdx.x;
  asm volatile("" : "+v"(tid));
  const int lane = tid & 63, r = lane & 15, q = lane >> 4;
  const int wid = __builtin_amdgcn_readfirstlane(tid >> 6), wr = wid >> 2, wc = wid & 3;
  typedef __attribute__((ext_vector_type(32))) float f32x32;
  typedef __attribute__((ext_vector_type(8))) unsigned u32x8;
  f32x32 acc;
  {
    const char* A = p.ws + WS_HF + h_off(p, rblk);
    const char* B = (const char*)(p.wd + ct * 128);
    const unsigned rb = tid >> 4, va = tid * 16, vb = ((rb & 4) * 4 + (rb >> 3) * 4 + (rb & 3)) * (DM * 2) + 16 * ((tid & 15) ^ swzB(rb & 15));
    u32x8 lb;
#pragma unroll
    for (int i = 0; i < 8; ++i)
      lb[i] = (i >> 2) * 65536 + 16384 + offB(8 * q + 4 * (i & 1) + (r >> 2), 2 * (wc * 2 + ((i >> 1) & 1)) + ((r & 3) >> 1)) + 8 * (r & 1);
    LBAR();
#pragma unroll
    for (int t = 0; t < 3; ++t) {
      glds2<true>(A + t * 32768, 16384, va, wid * 1024 + t * 32768);
      glds2(B + t * 64 * DM * 2, 32 * DM * 2, vb, wid * 1024 + t * 32768 + 16384);
    }
    WAIT_V(8);
    LBAR();
    asm volatile(DN_ASM
        : "={v[0:31]}"(acc)
        : "{v[80:81]}"((u32x2){wr * 4096u + lane * 16u, 65536u + wr * 4096u + lane * 16u}), "{v[82:89]}"(lb), "{v90}"(va), "{v91}"(vb),
          [pa] "s"(A + 3 * 32768), [pb] "s"(B + 3 * 64 * DM * 2), [lw] "s"(wid * 1024), [hf] "s"(wr)
        : CLB8(32, 33, 34, 35, 36, 37, 38, 39), CLB8(40, 41, 42, 43, 44, 45, 46, 47), CLB8(48, 49, 50, 51, 52, 53, 54, 55),
          CLB8(56, 57, 58, 59, 60, 61, 62, 63), CLB8(64, 65, 66, 67, 68, 69, 70, 71), CLB8(72, 73, 74, 75, 76, 77, 78, 79),
          "s84", "s85", "s86", "s87", "s88", "s89", "s90", "s91", "s94", "scc", "memory");
  }
  wait_s(p, rsy, rblk, ct * 4, 4, rblk < NFUSE);
  const int e0 = (rblk * 128 + wr * 64 + r) * DM + ct * 128 + wc * 32 + q * 8;
  SX sx[4];
#pragma unroll
  for (int m = 0; m < 4; ++m) sx[m] = get_sx(p, rblk, e0 + m * 16 * DM);
  const __amdgpu_buffer_rsrc_t ro = mkrsrc(p.ws + WS_G, S_LEN * DM * 2);
#pragma unroll
  for (int m = 0; m < 4; ++m) {
    float rr[8];
#pragma unroll
    for (int j = 0; j < 8; ++j) { float d = rbf(acc[8 * m + j]); rr[j] = d * __builtin_amdgcn_rcpf(1.0f + __expf(-d)); }
    put_g(p, ro, rblk, e0 + m * 16 * DM, widen32(cvtpk(rr[0], rr[1]), cvtpk(rr[2], rr[3]), cvtpk(rr[4], rr[5]), cvtpk(rr[6], rr[7])), sx[m]);
  }
  if (rblk >= NFUSE) publish_flags(rsy, SY_FG + (rblk * 8 + ct) * 128, 0, 1);
}

#define PR_ASM_0 \
  "s_cmp_lg_u32 %[kh], 0\n\t" \
  "s_cbranch_scc1 .Lk%=\n\t" \
  "s_mov_b32 s94, m0\n\t" \
  "s_mov_b64 s[92:93], %[x0]\n\t" \
  "s_mov_b32 s86, %[g0]\n\t" \
  "s_add_u32 s87, s86, %[t0]\n\t" \
  "s_sub_i32 s88, s86, 3\n\t" \
  "ds_read_b128 v[176:179], v208 offset:0\n\t" \
  "ds_read_b128 v[180:183], v208 offset:16384\n\t" \
  "ds_read_b128 v[184:187], v209 offset:0\n\t" \
  "ds_read_b128 v[188:191], v209 offset:16384\n\t" \
  "ds_read_b128 v[192:195], v210 offset:0\n\t" \
  "ds_read_b128 v[196:199], v210 offset:16384\n\t" \
  "ds_read_b128 v[200:203], v211 offset:0\n\t" \
  "ds_read_b128 v[204:207], v211 offset:16384\n\t" \
  "s_branch .Lfb00%=\n\t" \
  ".Lp0%=:\n\t" \
  "s_cmp_le_u32 s86, 5\n\t" \
  "s_cbranch_scc1 .Lf00%=\n\t" \
  ".Lfb00%=:\n\t" \
  "s_waitcnt lgkmcnt(6)\n\t" \
  "v_mfma_f32_16x16x32_bf16 v[128:131], v[0:3], v[176:179], 0\n\t" \
  "v_mfma_f32_16x16x32_bf16 v[132:135], v[4:7], v[176:179], 0\n\t" \
  "ds_read_b128 v[176:179], v208 offset:256\n\t" \
  "s_add_u32 m0, %[lw], 67584\n\t" \
  "v_mfma_f32_16x16x32_bf16 v[136:139], v[0:3], v[180:183], 0\n\t" \
  "v_mfma_f32_16x16x32_bf16 v[140:143], v[4:7], v[180:183], 0\n\t" \
  "ds_read_b128 v[180:183], v208 offset:16640\n\t" \
  "global_load_lds_dwordx4 v218, s[92:93] sc1\n\t" \
  "s_waitcnt lgkmcnt(6)\n\t" \
  "v_mfma_f32_16x16x32_bf16 v[128:131], v[8:11], v[184:187], v[128:131]\n\t" \
  "v_mfma_f32_16x16x32_bf16 v[132:135], v[12:15], v[184:187], v[132:135]\n\t" \
  "ds_read_b128 v[184:187], v209 offset:256\n\t" \
  "v_mfma_f32_16x16x32_bf16 v[136:139], v[8:11], v[188:191], v[136:139]\n\t" \
  "v_mfma_f32_16x16x32_bf16 v[140:143], v[12:15], v[188:191], v[140:143]\n\t" \
  "ds_read_b128 v[188:191], v209 offset:16640\n\t" \
  "ds_write_b128 v220, v[152:155] offset:0\n\t" \
  "ds_write_b128 v220, v[156:159] offset:1024\n\t" \
  "s_waitcnt lgkmcnt(8)\n\t" \
  "v_mfma_f32_16x16x32_bf16 v[128:131], v[16:19], v[192:195], v[128:131]\n\t" \
  "v_mfma_f32_16x16x32_bf16 v[132:135], v[20:23], v[192:195], v[132:135]\n\t" \
  "ds_read_b128 v[192:195], v210 offset:256\n\t" \
  "s_add_u32 m0, %[lw], 68608\n\t" \
  "v_mfma_f32_16x16x32_bf16 v[136:139], v[16:19], v[196:199], v[136:139]\n\t" \
  "v_mfma_f32_16x16x32_bf16 v[140:143], v[20:23], v[196:199], v[140:143]\n\t" \
  "ds_read_b128 v[196:199], v210 offset:16640\n\t" \
  "global_load_lds_dwordx4 v219, s[92:93] sc1\n\t" \
  "s_lshr_b32 s90, s87, 7\n\t" \
  "s_xor_b32 s90, s90, %[s0]\n\t" \
  "s_and_b32 s90, s90, 1\n\t" \
  "s_lshl1_add_u32 s90, s87, s90\n\t" \
  "s_lshl_b32 s90, s90, 13\n\t" \
  "s_cmp_lt_i32 s86, 6\n\t" \
  "s_cselect_b32 s90, 0x70000000, s90\n\t" \
  "s_waitcnt lgkmcnt(8)\n\t" \
  "v_mfma_f32_16x16x32_bf16 v[128:131], v[24:27], v[200:203], v[128:131]\n\t" \
  "v_mfma_f32_16x16x32_bf16 v[132:135], v[28:31], v[200:203], v[132:135]\n\t" \
  "ds_read_b128 v[200:203], v211 offset:256\n\t" \
  "v_mfma_f32_16x16x32_bf16 v[136:139], v[24:27], v[204:207], v[136:139]\n\t" \
  "v_mfma_f32_16x16x32_bf16 v[140:143], v[28:31], v[204:207], v[140:143]\n\t" \
  "ds_read_b128 v[204:207], v211 offset:16640\n\t" \
  "s_waitcnt lgkmcnt(4)\n\t" \
  "s_cmp_lt_u32 s86, 6\n\t" \
  "s_cbranch_scc1 .Lw00%=\n\t" \
  "s_waitcnt vmcnt(4)\n\t" \
  "s_barrier\n\t" \
  ".Lg00%=:\n\t" \
  "s_add_u32 s92, s92, 512\n\t" \
  "s_addc_u32 s93, s93, 0\n\t" \
  "v_mfma_f32_16x16x32_bf16 v[128:131], v[32:35], v[176:179], v[128:131]\n\t" \
  "v_mfma_f32_16x16x32_bf16 v[132:135], v[36:39], v[176:179], v[132:135]\n\t" \
  "ds_read_b128 v[176:179], v208 offset:32768\n\t" \
  "s_add_u32 m0, %[lw], 98304\n\t" \
  "v_mfma_f32_16x16x32_bf16 v[136:139], v[32:35], v[180:183], v[136:139]\n\t" \
  "v_mfma_f32_16x16x32_bf16 v[140:143], v[36:39], v[180:183], v[140:143]\n\t" \
  "ds_read_b128 v[180:183], v208 offset:49152\n\t" \
  "global_load_lds_dwordx4 v216, s[92:93] sc1\n\t" \
  "ds_read_b128 v[160:163], v221 offset:0\n\t" \
  "ds_read_b128 v[164:167], v221 offset:1024\n\t" \
  "v_mfma_f32_16x16x32_bf16 v[128:131], v[40:43], v[184:187], v[128:131]\n\t" \
  "v_mfma_f32_16x16x32_bf16 v[132:135], v[44:47], v[184:187], v[132:135]\n\t" \
  "ds_read_b128 v[184:187], v209 offset:32768\n\t" \
  "v_mfma_f32_16x16x32_bf16 v[136:139], v[40:43], v[188:191], v[136:139]\n\t" \
  "v_mfma_f32_16x16x32_bf16 v[140:143], v[44:47], v[188:191], v[140:143]\n\t" \
  "ds_read_b128 v[188:191], v209 offset:49152\n\t" \
  "s_waitcnt lgkmcnt(8)\n\t" \
  "v_mfma_f32_16x16x32_bf16 v[128:131], v[48:51], v[192:195], v[128:131]\n\t" \
  "v_mfma_f32_16x16x32_bf16 v[132:135], v[52:55], v[192:195], v[132:135]\n\t" \
  "ds_read_b128 v[192:195], v210 offset:32768\n\t" \
  "s_add_u32 m0, %[lw], 99328\n\t" \
  "v_mfma_f32_16x16x32_bf16 v[136:139], v[48:51], v[196:199], v[136:139]\n\t" \
  "v_mfma_f32_16x16x32_bf16 v[140:143], v[52:55], v[196:199], v[140:143]\n\t" \
  "ds_read_b128 v[196:199], v210 offset:49152\n\t" \
  "global_load_lds_dwordx4 v217, s[92:93] sc1\n\t" \
  "s_waitcnt lgkmcnt(4)\n\t" \
  "v_add_f32 v160, v160, v144\n\t" \
  "v_add_f32 v161, v161, v145\n\t" \
  "v_add_f32 v162, v162, v146\n\t" \
  "v_add_f32 v163, v163, v147\n\t" \
  "v_add_f32 v164, v164, v148\n\t" \
  "v_add_f32 v165, v165, v149\n\t" \
  "v_add_f32 v166, v166, v150\n\t" \
  "v_add_f32 v167, v167, v151\n\t" \
  "v_mfma_f32_16x16x32_bf16 v[128:131], v[56:59], v[200:203], v[128:131]\n\t" \
  "v_mfma_f32_16x16x32_bf16 v[132:135], v[60:63], v[200:203], v[132:135]\n\t" \
  "ds_read_b128 v[200:203], v211 offset:32768\n\t" \
  "v_mfma_f32_16x16x32_bf16 v[136:139], v[56:59], v[204:207], v[136:139]\n\t" \
  "v_mfma_f32_16x16x32_bf16 v[140:143], v[60:63], v[204:207], v[140:143]\n\t" \
  "ds_read_b128 v[204:207], v211 offset:49152\n\t" \
  "v_cvt_pk_bf16_f32 v168, v160, v161\n\t" \
  "v_cvt_pk_bf16_f32 v169, v162, v163\n\t" \
  "v_cvt_pk_bf16_f32 v170, v164, v165\n\t" \
  "v_cvt_pk_bf16_f32 v171, v166, v167\n\t" \
  "buffer_store_dwordx4 v[168:171], v172, %[rt], s90 offen sc1\n\t" \
  "v_mfma_f32_16x16x32_bf16 v[128:131], v[64:67], v[176:179], v[128:131]\n\t" \
  "v_mfma_f32_16x16x32_bf16 v[132:135], v[68:71], v[176:179], v[132:135]\n\t" \
  "ds_read_b128 v[176:179], v208 offset:33024\n\t" \
  "s_add_u32 m0, %[lw], 100352\n\t" \
  "v_mfma_f32_16x16x32_bf16 v[136:139], v[64:67], v[180:183], v[136:139]\n\t" \
  "v_mfma_f32_16x16x32_bf16 v[140:143], v[68:71], v[180:183], v[140:143]\n\t" \
  "ds_read_b128 v[180:183], v208 offset:49408\n\t" \
  "global_load_lds_dwordx4 v218, s[92:93] sc1\n\t" \
  "s_waitcnt lgkmcnt(6)\n\t" \
  "v_mfma_f32_16x16x32_bf16 v[128:131], v[72:75], v[184:187], v[128:131]\n\t" \
  "v_mfma_f32_16x16x32_bf16 v[132:135], v[76:79], v[184:187], v[132:135]\n\t" \
  "ds_read_b128 v[184:187], v209 offset:33024\n\t" \
  "v_mfma_f32_16x16x32_bf16 v[136:139], v[72:75], v[188:191], v[136:139]\n\t" \
  "v_mfma_f32_16x16x32_bf16 v[140:143], v[76:79], v[188:191], v[140:143]\n\t" \
  "ds_read_b128 v[188:191], v209 offset:49408\n\t" \
  "s_waitcnt lgkmcnt(6)\n\t" \
  "v_mfma_f32_16x16x32_bf16 v[128:131], v[80:83], v[192:195], v[128:131]\n\t" \
  "v_mfma_f32_16x16x32_bf16 v[132:135], v[84:87], v[192:195], v[132:135]\n\t" \
  "ds_read_b128 v[192:195], v210 offset:33024\n\t" \
  "s_add_u32 m0, %[lw], 101376\n\t" \
  "v_mfma_f32_16x16x32_bf16 v[136:139], v[80:83], v[196:199], v[136:139]\n\t" \
  "v_mfma_f32_16x16x32_bf16 v[140:143], v[84:87], v[196:199], v[140:143]\n\t" \
  "ds_read_b128 v[196:199], v210 offset:49408\n\t" \
  "global_load_lds_dwordx4 v219, s[92:93] sc1\n\t" \
  "s_waitcnt lgkmcnt(6)\n\t" \
  "v_mfma_f32_16x16x32_bf16 v[128:131], v[88:91], v[200:203], v[128:131]\n\t" \
  "v_mfma_f32_16x16x32_bf16 v[132:135], v[92:95], v[200:203], v[132:135]\n\t" \
  "ds_read_b128 v[200:203], v211 offset:33024\n\t" \
  "v_mfma_f32_16x16x32_bf16 v[136:139], v[88:91], v[204:207], v[136:139]\n\t" \
  "v_mfma_f32_16x16x32_bf16 v[140:143], v[92:95], v[204:207], v[140:143]\n\t" \
  "ds_read_b128 v[204:207], v211 offset:49408\n\t" \
  "s_cmp_lt_u32 s86, 8\n\t" \
  "s_cbranch_scc1 .Lw10%=\n\t" \
  "s_waitcnt vmcnt(5)\n\t" \
  "s_barrier\n\t" \
  ".Lg10%=:\n\t" \
  "s_add_u32 s92, s92, 65024\n\t" \
  "s_addc_u32 s93, s93, 0\n\t" \
  "s_waitcnt lgkmcnt(6)\n\t" \
  "v_mfma_f32_16x16x32_bf16 v[128:131], v[96:99], v[176:179], v[128:131]\n\t" \
  "v_mfma_f32_16x16x32_bf16 v[132:135], v[100:103], v[176:179], v[132:135]\n\t" \
  "ds_read_b128 v[176:179], v212 offset:0\n\t" \
  "s_add_u32 m0, %[lw], 0\n\t" \
  "v_mfma_f32_16x16x32_bf16 v[136:139], v[96:99], v[180:183], v[136:139]\n\t" \
  "v_mfma_f32_16x16x32_bf16 v[140:143], v[100:103], v[180:183], v[140:143]\n\t" \
  "ds_read_b128 v[180:183], v212 offset:16384\n\t" \
  "global_load_lds_dwordx4 v216, s[92:93] sc1\n\t" \
  "s_waitcnt lgkmcnt(6)\n\t" \
  "v_mfma_f32_16x16x32_bf16 v[128:131], v[104:107], v[184:187], v[128:131]\n\t" \
  "v_mfma_f32_16x16x32_bf16 v[132:135], v[108:111], v[184:187], v[132:135]\n\t" \
  "ds_read_b128 v[184:187], v213 offset:0\n\t" \
  "v_mfma_f32_16x16x32_bf16 v[136:139], v[104:107], v[188:191], v[136:139]\n\t" \
  "v_mfma_f32_16x16x32_bf16 v[140:143], v[108:111], v[188:191], v[140:143]\n\t" \
  "ds_read_b128 v[188:191], v213 offset:16384\n\t" \
  "s_waitcnt lgkmcnt(6)\n\t" \
  "v_mfma_f32_16x16x32_bf16 v[128:131], v[112:115], v[192:195], v[128:131]\n\t" \
  "v_mfma_f32_16x16x32_bf16 v[132:135], v[116:119], v[192:195], v[132:135]\n\t" \
  "ds_read_b128 v[192:195], v214 offset:0\n\t" \
  "s_add_u32 m0, %[lw], 1024\n\t" \
  "v_mfma_f32_16x16x32_bf16 v[136:139], v[112:115], v[196:199], v[136:139]\n\t" \
  "v_mfma_f32_16x16x32_bf16 v[140:143], v[116:119], v[196:199], v[140:143]\n\t" \
  "ds_read_b128 v[196:199], v214 offset:16384\n\t" \
  "global_load_lds_dwordx4 v217, s[92:93] sc1\n\t" \
  "s_waitcnt lgkmcnt(6)\n\t" \
  "v_mfma_f32_16x16x32_bf16 v[128:131], v[120:123], v[200:203], v[128:131]\n\t" \
  "v_mfma_f32_16x16x32_bf16 v[132:135], v[124:127], v[200:203], v[132:135]\n\t" \
  "ds_read_b128 v[200:203], v215 offset:0\n\t" \
  "v_mfma_f32_16x16x32_bf16 v[136:139], v[120:123], v[204:207], v[136:139]\n\t" \
  "v_mfma_f32_16x16x32_bf16 v[140:143], v[124:127], v[204:207], v[140:143]\n\t" \
  "ds_read_b128 v[204:207], v215 offset:16384\n\t" \
  "s_add_u32 s86, s86, 1\n\t" \
  "s_add_u32 s87, s87, 1\n\t" \
  "s_cmp_le_u32 s86, 5\n\t" \
  "s_cbranch_scc1 .Lf10%=\n\t" \
  ".Lfb10%=:\n\t" \
  "s_waitcnt lgkmcnt(6)\n\t" \
  "v_mfma_f32_16x16x32_bf16 v[144:147], v[0:3], v[176:179], 0\n\t" \
  "v_mfma_f32_16x16x32_bf16 v[148:151], v[4:7], v[176:179], 0\n\t" \
  "ds_read_b128 v[176:179], v212 offset:256\n\t" \
  "s_add_u32 m0, %[lw], 2048\n\t" \
  "v_mfma_f32_16x16x32_bf16 v[152:155], v[0:3], v[180:183], 0\n\t" \
  "v_mfma_f32_16x16x32_bf16 v[156:159], v[4:7], v[180:183], 0\n\t" \
  "ds_read_b128 v[180:183], v212 offset:16640\n\t" \
  "global_load_lds_dwordx4 v218, s[92:93] sc1\n\t" \
  "s_waitcnt lgkmcnt(6)\n\t" \
  "v_mfma_f32_16x16x32_bf16 v[144:147], v[8:11], v[184:187], v[144:147]\n\t" \
  "v_mfma_f32_16x16x32_bf16 v[148:151], v[12:15], v[184:187], v[148:151]\n\t" \
  "ds_read_b128 v[184:187], v213 offset:256\n\t" \
  "v_mfma_f32_16x16x32_bf16 v[152:155], v[8:11], v[188:191], v[152:155]\n\t" \
  "v_mfma_f32_16x16x32_bf16 v[156:159], v[12:15], v[188:191], v[156:159]\n\t" \
  "ds_read_b128 v[188:191], v213 offset:16640\n\t" \
  "ds_write_b128 v220, v[136:139] offset:0\n\t" \
  "ds_write_b128 v220, v[140:143] offset:1024\n\t" \
  "s_waitcnt lgkmcnt(8)\n\t" \
  "v_mfma_f32_16x16x32_bf16 v[144:147], v[16:19], v[192:195], v[144:147]\n\t" \
  "v_mfma_f32_16x16x32_bf16 v[148:151], v[20:23], v[192:195], v[148:151]\n\t" \
  "ds_read_b128 v[192:195], v214 offset:256\n\t" \
  "s_add_u32 m0, %[lw], 3072\n\t" \
  "v_mfma_f32_16x16x32_bf16 v[152:155], v[16:19], v[196:199], v[152:155]\n\t" \
  "v_mfma_f32_16x16x32_bf16 v[156:159], v[20:23], v[196:199], v[156:159]\n\t" \
  "ds_read_b128 v[196:199], v214 offset:16640\n\t" \
  "global_load_lds_dwordx4 v219, s[92:93] sc1\n\t" \
  "s_lshr_b32 s90, s87, 7\n\t" \
  "s_xor_b32 s90, s90, %[s0]\n\t" \
  "s_and_b32 s90, s90, 1\n\t" \
  "s_lshl1_add_u32 s90, s87, s90\n\t" \
  "s_lshl_b32 s90, s90, 13\n\t" \
  "s_cmp_lt_i32 s86, 6\n\t" \
  "s_cselect_b32 s90, 0x70000000, s90\n\t" \
  "s_waitcnt lgkmcnt(8)\n\t" \
  "v_mfma_f32_16x16x32_bf16 v[144:147], v[24:27], v[200:203], v[144:147]\n\t" \
  "v_mfma_f32_16x16x32_bf16 v[148:151], v[28:31], v[200:203], v[148:151]\n\t" \
  "ds_read_b128 v[200:203], v215 offset:256\n\t" \
  "v_mfma_f32_16x16x32_bf16 v[152:155], v[24:27], v[204:207], v[152:155]\n\t" \
  "v_mfma_f32_16x16x32_bf16 v[156:159], v[28:31], v[204:207], v[156:159]\n\t" \
  "ds_read_b128 v[204:207], v215 offset:16640\n\t" \
  "s_waitcnt lgkmcnt(4)\n\t" \
  "s_cmp_lt_u32 s86, 6\n\t" \
  "s_cbranch_scc1 .Lw20%=\n\t" \
  "s_waitcnt vmcnt(4)\n\t" \
  "s_barrier\n\t" \
  ".Lg20%=:\n\t" \
  "s_add_u32 s92, s92, 512\n\t" \
  "s_addc_u32 s93, s93, 0\n\t" \
  "v_mfma_f32_16x16x32_bf16 v[144:147], v[32:35], v[176:179], v[144:147]\n\t" \
  "v_mfma_f32_16x16x32_bf16 v[148:151], v[36:39], v[176:179], v[148:151]\n\t" \
  "ds_read_b128 v[176:179], v212 offset:32768\n\t" \
  "s_add_u32 m0, %[lw], 32768\n\t" \
  "v_mfma_f32_16x16x32_bf16 v[152:155], v[32:35], v[180:183], v[152:155]\n\t" \
  "v_mfma_f32_16x16x32_bf16 v[156:159], v[36:39], v[180:183], v[156:159]\n\t" \
  "ds_read_b128 v[180:183], v212 offset:49152\n\t" \
  "global_load_lds_dwordx4 v216, s[92:93] sc1\n\t" \
  "ds_read_b128 v[160:163], v221 offset:0\n\t" \
  "ds_read_b128 v[164:167], v221 offset:1024\n\t" \
  "v_mfma_f32_16x16x32_bf16 v[144:147], v[40:43], v[184:187], v[144:147]\n\t" \
  "v_mfma_f32_16x16x32_bf16 v[148:151], v[44:47], v[184:187], v[148:151]\n\t" \
  "ds_read_b128 v[184:187], v213 offset:32768\n\t" \
  "v_mfma_f32_16x16x32_bf16 v[152:155], v[40:43], v[188:191], v[152:155]\n\t" \
  "v_mfma_f32_16x16x32_bf16 v[156:159], v[44:47], v[188:191], v[156:159]\n\t" \
  "ds_read_b128 v[188:191], v213 offset:49152\n\t" \
  "s_waitcnt lgkmcnt(8)\n\t" \
  "v_mfma_f32_16x16x32_bf16 v[144:147], v[48:51], v[192:195], v[144:147]\n\t" \
  "v_mfma_f32_16x16x32_bf16 v[148:151], v[52:55], v[192:195], v[148:151]\n\t" \
  "ds_read_b128 v[192:195], v214 offset:32768\n\t" \
  "s_add_u32 m0, %[lw], 33792\n\t" \
  "v_mfma_f32_16x16x32_bf16 v[152:155], v[48:51], v[196:199], v[152:155]\n\t" \
  "v_mfma_f32_16x16x32_bf16 v[156:159], v[52:55], v[196:199], v[156:159]\n\t" \
  "ds_read_b128 v[196:199], v214 offset:49152\n\t" \
  "global_load_lds_dwordx4 v217, s[92:93] sc1\n\t" \
  "s_waitcnt lgkmcnt(4)\n\t" \
  "v_add_f32 v160, v160, v128\n\t" \
  "v_add_f32 v161, v161, v129\n\t" \
  "v_add_f32 v162, v162, v130\n\t" \
  "v_add_f32 v163, v163, v131\n\t" \
  "v_add_f32 v164, v164, v132\n\t" \
  "v_add_f32 v165, v165, v133\n\t" \
  "v_add_f32 v166, v166, v134\n\t" \
  "v_add_f32 v167, v167, v135\n\t" \
  "v_mfma_f32_16x16x32_bf16 v[144:147], v[56:59], v[200:203], v[144:147]\n\t" \
  "v_mfma_f32_16x16x32_bf16 v[148:151], v[60:63], v[200:203], v[148:151]\n\t" \
  "ds_read_b128 v[200:203], v215 offset:32768\n\t" \
  "v_mfma_f32_16x16x32_bf16 v[152:155], v[56:59], v[204:207], v[152:155]\n\t" \
  "v_mfma_f32_16x16x32_bf16 v[156:159], v[60:63], v[204:207], v[156:159]\n\t" \
  "ds_read_b128 v[204:207], v215 offset:49152\n\t" \
  "v_cvt_pk_bf16_f32 v168, v160, v161\n\t" \
  "v_cvt_pk_bf16_f32 v169, v162, v163\n\t" \
  "v_cvt_pk_bf16_f32 v170, v164, v165\n\t" \
  "v_cvt_pk_bf16_f32 v171, v166, v167\n\t" \
  "buffer_store_dwordx4 v[168:171], v172, %[rt], s90 offen sc1\n\t" \
  "v_mfma_f32_16x16x32_bf16 v[144:147], v[64:67], v[176:179], v[144:147]\n\t" \
  "v_mfma_f32_16x16x32_bf16 v[148:151], v[68:71], v[176:179], v[148:151]\n\t" \
  "ds_read_b128 v[176:179], v212 offset:33024\n\t" \
  "s_add_u32 m0, %[lw], 34816\n\t" \
  "v_mfma_f32_16x16x32_bf16 v[152:155], v[64:67], v[180:183], v[152:155]\n\t" \
  "v_mfma_f32_16x16x32_bf16 v[156:159], v[68:71], v[180:183], v[156:159]\n\t" \
  "ds_read_b128 v[180:183], v212 offset:49408\n\t" \
  "global_load_lds_dwordx4 v218, s[92:93] sc1\n\t" \
  "s_waitcnt lgkmcnt(6)\n\t" \
  "v_mfma_f32_16x16x32_bf16 v[144:147], v[72:75], v[184:187], v[144:147]\n\t" \
  "v_mfma_f32_16x16x32_bf16 v[148:151], v[76:79], v[184:187], v[148:151]\n\t" \
  "ds_read_b128 v[184:187], v213 offset:33024\n\t" \
  "v_mfma_f32_16x16x32_bf16 v[152:155], v[72:75], v[188:191], v[152:155]\n\t" \
  "v_mfma_f32_16x16x32_bf16 v[156:159], v[76:79], v[188:191], v[156:159]\n\t" \
  "ds_read_b128 v[188:191], v213 offset:49408\n\t" \
  "s_waitcnt lgkmcnt(6)\n\t" \
  "v_mfma_f32_16x16x32_bf16 v[144:147], v[80:83], v[192:195], v[144:147]\n\t" \
  "v_mfma_f32_16x16x32_bf16 v[148:151], v[84:87], v[192:195], v[148:151]\n\t" \
  "ds_read_b128 v[192:195], v214 offset:33024\n\t" \
  "s_add_u32 m0, %[lw], 35840\n\t" \
  "v_mfma_f32_16x16x32_bf16 v[152:155], v[80:83], v[196:199], v[152:155]\n\t" \
  "v_mfma_f32_16x16x32_bf16 v[156:159], v[84:87], v[196:199], v[156:159]\n\t" \
  "ds_read_b128 v[196:199], v214 offset:49408\n\t" \
  "global_load_lds_dwordx4 v219, s[92:93] sc1\n\t" \
  "s_waitcnt lgkmcnt(6)\n\t" \
  "v_mfma_f32_16x16x32_bf16 v[144:147], v[88:91], v[200:203], v[144:147]\n\t" \
  "v_mfma_f32_16x16x32_bf16 v[148:151], v[92:95], v[200:203], v[148:151]\n\t" \
  "ds_read_b128 v[200:203], v215 offset:33024\n\t" \
  "v_mfma_f32_16x16x32_bf16 v[152:155], v[88:91], v[204:207], v[152:155]\n\t" \
  "v_mfma_f32_16x16x32_bf16 v[156:159], v[92:95], v[204:207], v[156:159]\n\t" \
  "ds_read_b128 v[204:207], v215 offset:49408\n\t" \
  "s_cmp_lt_u32 s86, 8\n\t" \
  "s_cbranch_scc1 .Lw30%=\n\t" \
  "s_waitcnt vmcnt(5)\n\t" \
  "s_barrier\n\t" \
  ".Lg30%=:\n\t" \
  "s_add_u32 s92, s92, 65024\n\t" \
  "s_addc_u32 s93, s93, 0\n\t" \
  "s_waitcnt lgkmcnt(6)\n\t" \
  "v_mfma_f32_16x16x32_bf16 v[144:147], v[96:99], v[176:179], v[144:147]\n\t" \
  "v_mfma_f32_16x16x32_bf16 v[148:151], v[100:103], v[176:179], v[148:151]\n\t" \
  "ds_read_b128 v[176:179], v208 offset:0\n\t" \
  "s_add_u32 m0, %[lw], 65536\n\t" \
  "v_mfma_f32_16x16x32_bf16 v[152:155], v[96:99], v[180:183], v[152:155]\n\t" \
  "v_mfma_f32_16x16x32_bf16 v[156:159], v[100:103], v[180:183], v[156:159]\n\t" \
  "ds_read_b128 v[180:183], v208 offset:16384\n\t" \
  "global_load_lds_dwordx4 v216, s[92:93] sc1\n\t" \
  "s_waitcnt lgkmcnt(6)\n\t" \
  "v_mfma_f32_16x16x32_bf16 v[144:147], v[104:107], v[184:187], v[144:147]\n\t" \
  "v_mfma_f32_16x16x32_bf16 v[148:151], v[108:111], v[184:187], v[148:151]\n\t" \
  "ds_read_b128 v[184:187], v209 offset:0\n\t" \
  "v_mfma_f32_16x16x32_bf16 v[152:155], v[104:107], v[188:191], v[152:155]\n\t" \
  "v_mfma_f32_16x16x32_bf16 v[156:159], v[108:111], v[188:191], v[156:159]\n\t" \
  "ds_read_b128 v[188:191], v209 offset:16384\n\t" \
  "s_waitcnt lgkmcnt(6)\n\t" \
  "v_mfma_f32_16x16x32_bf16 v[144:147], v[112:115], v[192:195], v[144:147]\n\t" \
  "v_mfma_f32_16x16x32_bf16 v[148:151], v[116:119], v[192:195], v[148:151]\n\t" \
  "ds_read_b128 v[192:195], v210 offset:0\n\t" \
  "s_add_u32 m0, %[lw], 66560\n\t" \
  "v_mfma_f32_16x16x32_bf16 v[152:155], v[112:115], v[196:199], v[152:155]\n\t" \
  "v_mfma_f32_16x16x32_bf16 v[156:159], v[116:119], v[196:199], v[156:159]\n\t" \
  "ds_read_b128 v[196:199], v210 offset:16384\n\t" \
  "global_load_lds_dwordx4 v217, s[92:93] sc1\n\t" \
  "s_waitcnt lgkmcnt(6)\n\t" \
  "v_mfma_f32_16x16x32_bf16 v[144:147], v[120:123], v[200:203], v[144:147]\n\t" \
  "v_mfma_f32_16x16x32_bf16 v[148:151], v[124:127], v[200:203], v[148:151]\n\t" \
  "ds_read_b128 v[200:203], v211 offset:0\n\t" \
  "v_mfma_f32_16x16x32_bf16 v[152:155], v[120:123], v[204:207], v[152:155]\n\t" \
  "v_mfma_f32_16x16x32_bf16 v[156:159], v[124:127], v[204:207], v[156:159]\n\t" \
  "ds_read_b128 v[204:207], v211 offset:16384\n\t" \
  "s_add_u32 s86, s86, 1\n\t" \
  "s_add_u32 s87, s87, 1\n\t" \
  "s_cmp_lt_u32 s86, %[end]\n\t" \
  "s_cbranch_scc1 .Lp0%=\n\t" \
  "s_branch .Lx0%=\n\t" \
  ".Lf00%=:\n\t" \
  "s_nop 7\n\t" \
  "ds_write_b128 v220, v[152:155] offset:0\n\t" \
  "ds_write_b128 v220, v[156:159] offset:1024\n\t" \
  "s_lshr_b32 s90, s87, 7\n\t" \
  "s_xor_b32 s90, s90, %[s0]\n\t" \
  "s_and_b32 s90, s90, 1\n\t" \
  "s_lshl1_add_u32 s90, s87, s90\n\t" \
  "s_lshl_b32 s90, s90, 13\n\t" \
  "s_cmp_lt_i32 s86, 1\n\t" \
  "s_cselect_b32 s90, 0x70000000, s90\n\t" \
  "s_waitcnt lgkmcnt(0)\n\t" \
  "s_barrier\n\t" \
  "ds_read_b128 v[160:163], v221 offset:0\n\t" \
  "ds_read_b128 v[164:167], v221 offset:1024\n\t" \
  "s_waitcnt lgkmcnt(0)\n\t" \
  "v_add_f32 v160, v160, v144\n\t" \
  "v_add_f32 v161, v161, v145\n\t" \
  "v_add_f32 v162, v162, v146\n\t" \
  "v_add_f32 v163, v163, v147\n\t" \
  "v_add_f32 v164, v164, v148\n\t" \
  "v_add_f32 v165, v165, v149\n\t" \
  "v_add_f32 v166, v166, v150\n\t" \
  "v_add_f32 v167, v167, v151\n\t" \
  "v_cvt_pk_bf16_f32 v168, v160, v161\n\t" \
  "v_cvt_pk_bf16_f32 v169, v162, v163\n\t" \
  "v_cvt_pk_bf16_f32 v170, v164, v165\n\t" \
  "v_cvt_pk_bf16_f32 v171, v166, v167\n\t" \
  "buffer_store_dwordx4 v[168:171], v172, %[rt], s90 offen sc1\n\t" \
  "s_branch .Lfb00%=\n\t" \
  ".Lf10%=:\n\t" \
  "s_nop 7\n\t" \
  "ds_write_b128 v220, v[136:139] offset:0\n\t" \
  "ds_write_b128 v220, v[140:143] offset:1024\n\t" \
  "s_lshr_b32 s90, s87, 7\n\t" \
  "s_xor_b32 s90, s90, %[s0]\n\t" \
  "s_and_b32 s90, s90, 1\n\t" \
  "s_lshl1_add_u32 s90, s87, s90\n\t" \
  "s_lshl_b32 s90, s90, 13\n\t" \
  "s_cmp_lt_i32 s86, 1\n\t" \
  "s_cselect_b32 s90, 0x70000000, s90\n\t" \
  "s_waitcnt lgkmcnt(0)\n\t" \
  "s_barrier\n\t" \
  "ds_read_b128 v[160:163], v221 offset:0\n\t" \
  "ds_read_b128 v[164:167], v221 offset:1024\n\t" \
  "s_waitcnt lgkmcnt(0)\n\t" \
  "v_add_f32 v160, v160, v128\n\t" \
  "v_add_f32 v161, v161, v129\n\t" \
  "v_add_f32 v162, v162, v130\n\t" \
  "v_add_f32 v163, v163, v131\n\t" \
  "v_add_f32 v164, v164, v132\n\t" \
  "v_add_f32 v165, v165, v133\n\t" \
  "v_add_f32 v166, v166, v134\n\t" \
  "v_add_f32 v167, v167, v135\n\t" \
  "v_cvt_pk_bf16_f32 v168, v160, v161\n\t" \
  "v_cvt_pk_bf16_f32 v169, v162, v163\n\t" \
  "v_cvt_pk_bf16_f32 v170, v164, v165\n\t" \
  "v_cvt_pk_bf16_f32 v171, v166, v167\n\t" \
  "buffer_store_dwordx4 v[168:171], v172, %[rt], s90 offen sc1\n\t" \
  "s_branch .Lfb10%=\n\t" \
  ".Lw00%=:\n\t" \
  "s_waitcnt vmcnt(2)\n\t" \
  "s_barrier\n\t" \
  "s_branch .Lg00%=\n\t" \
  ".Lw10%=:\n\t" \
  "s_waitcnt vmcnt(2)\n\t" \
  "s_barrier\n\t" \
  "s_branch .Lg10%=\n\t" \
  ".Lw20%=:\n\t" \
  "s_waitcnt vmcnt(2)\n\t" \
  "s_barrier\n\t" \
  "s_branch .Lg20%=\n\t" \
  ".Lw30%=:\n\t" \
  "s_waitcnt vmcnt(2)\n\t" \
  "s_barrier\n\t" \
  "s_branch .Lg30%=\n\t" \
  ".Lx0%=:\n\t" \
  "s_waitcnt lgkmcnt(0)\n\t" \
  "s_setprio 0\n\t" \
  "s_mov_b32 m0, s94\n\t" \
  "s_branch .Le%=\n\t" \
  ".Lk%=:\n\t" \
  "s_mov_b32 s94, m0\n\t" \
  "s_mov_b64 s[92:93], %[x0]\n\t" \
  "s_mov_b32 s86, %[g0]\n\t" \
  "s_add_u32 s87, s86, %[t0]\n\t" \
  "s_sub_i32 s88, s86, 3\n\t" \
  "ds_read_b128 v[176:179], v208 offset:0\n\t" \
  "ds_read_b128 v[180:183], v208 offset:16384\n\t" \
  "ds_read_b128 v[184:187], v209 offset:0\n\t" \
  "ds_read_b128 v[188:191], v209 offset:16384\n\t" \
  "ds_read_b128 v[192:195], v210 offset:0\n\t" \
  "ds_read_b128 v[196:199], v210 offset:16384\n\t" \
  "ds_read_b128 v[200:203], v211 offset:0\n\t" \
  "ds_read_b128 v[204:207], v211 offset:16384\n\t" \
  "s_branch .Lfb01%=\n\t" \
  ".Lp1%=:\n\t" \
  "s_cmp_le_u32 s86, 5\n\t" \
  "s_cbranch_scc1 .Lf01%=\n\t" \
  ".Lfb01%=:\n\t" \
  "s_waitcnt lgkmcnt(6)\n\t" \
  "v_mfma_f32_16x16x32_bf16 v[128:131], v[0:3], v[176:179], 0\n\t" \
  "v_mfma_f32_16x16x32_bf16 v[132:135], v[4:7], v[176:179], 0\n\t" \
  "ds_read_b128 v[176:179], v208 offset:256\n\t" \
  "v_mfma_f32_16x16x32_bf16 v[136:139], v[0:3], v[180:183], 0\n\t" \
  "v_mfma_f32_16x16x32_bf16 v[140:143], v[4:7], v[180:183], 0\n\t" \
  "ds_read_b128 v[180:183], v208 offset:16640\n\t" \
  "s_waitcnt lgkmcnt(6)\n\t" \
  "v_mfma_f32_16x16x32_bf16 v[128:131], v[8:11], v[184:187], v[128:131]\n\t" \
  "v_mfma_f32_16x16x32_bf16 v[132:135], v[12:15], v[184:187], v[132:135]\n\t" \
  "ds_read_b128 v[184:187], v209 offset:256\n\t" \
  "s_add_u32 m0, %[lw], 67584\n\t" \
  "v_mfma_f32_16x16x32_bf16 v[136:139], v[8:11], v[188:191], v[136:139]\n\t" \
  "v_mfma_f32_16x16x32_bf16 v[140:143], v[12:15], v[188:191], v[140:143]\n\t" \
  "ds_read_b128 v[188:191], v209 offset:16640\n\t" \
  "global_load_lds_dwordx4 v218, s[92:93] sc1\n\t" \
  "ds_write_b128 v220, v[152:155] offset:0\n\t" \
  "ds_write_b128 v220, v[156:159] offset:1024\n\t" \
  "s_waitcnt lgkmcnt(8)\n\t" \
  "v_mfma_f32_16x16x32_bf16 v[128:131], v[16:19], v[192:195], v[128:131]\n\t" \
  "v_mfma_f32_16x16x32_bf16 v[132:135], v[20:23], v[192:195], v[132:135]\n\t" \
  "ds_read_b128 v[192:195], v210 offset:256\n\t" \
  "v_mfma_f32_16x16x32_bf16 v[136:139], v[16:19], v[196:199], v[136:139]\n\t" \
  "v_mfma_f32_16x16x32_bf16 v[140:143], v[20:23], v[196:199], v[140:143]\n\t" \
  "ds_read_b128 v[196:199], v210 offset:16640\n\t" \
  "s_lshr_b32 s90, s87, 7\n\t" \
  "s_xor_b32 s90, s90, %[s0]\n\t" \
  "s_and_b32 s90, s90, 1\n\t" \
  "s_lshl1_add_u32 s90, s87, s90\n\t" \
  "s_lshl_b32 s90, s90, 13\n\t" \
  "s_cmp_lt_i32 s86, 6\n\t" \
  "s_cselect_b32 s90, 0x70000000, s90\n\t" \
  "s_waitcnt lgkmcnt(8)\n\t" \
  "v_mfma_f32_16x16x32_bf16 v[128:131], v[24:27], v[200:203], v[128:131]\n\t" \
  "v_mfma_f32_16x16x32_bf16 v[132:135], v[28:31], v[200:203], v[132:135]\n\t" \
  "ds_read_b128 v[200:203], v211 offset:256\n\t" \
  "s_add_u32 m0, %[lw], 68608\n\t" \
  "v_mfma_f32_16x16x32_bf16 v[136:139], v[24:27], v[204:207], v[136:139]\n\t" \
  "v_mfma_f32_16x16x32_bf16 v[140:143], v[28:31], v[204:207], v[140:143]\n\t" \
  "ds_read_b128 v[204:207], v211 offset:16640\n\t" \
  "global_load_lds_dwordx4 v219, s[92:93] sc1\n\t" \
  "s_waitcnt lgkmcnt(4)\n\t" \
  "s_cmp_lt_u32 s86, 6\n\t" \
  "s_cbranch_scc1 .Lw01%=\n\t" \
  "s_waitcnt vmcnt(4)\n\t" \
  "s_barrier\n\t" \
  ".Lg01%=:\n\t" \
  "s_add_u32 s92, s92, 512\n\t" \
  "s_addc_u32 s93, s93, 0\n\t" \
  "v_mfma_f32_16x16x32_bf16 v[128:131], v[32:35], v[176:179], v[128:131]\n\t" \
  "v_mfma_f32_16x16x32_bf16 v[132:135], v[36:39], v[176:179], v[132:135]\n\t" \
  "ds_read_b128 v[176:179], v208 offset:32768\n\t" \
  "v_mfma_f32_16x16x32_bf16 v[136:139], v[32:35], v[180:183], v[136:139]\n\t" \
  "v_mfma_f32_16x16x32_bf16 v[140:143], v[36:39], v[180:183], v[140:143]\n\t" \
  "ds_read_b128 v[180:183], v208 offset:49152\n\t" \
  "ds_read_b128 v[160:163], v221 offset:0\n\t" \
  "ds_read_b128 v[164:167], v221 offset:1024\n\t" \
  "s_sub_i32 s91, s86, 2\n\t" \
  "s_max_i32 s88, s88, s91\n\t" \
  "v_mov_b32 v173, s88\n\t" \
  "buffer_store_dword v173, v174, %[rs], 0 offen sc1\n\t" \
  "v_mfma_f32_16x16x32_bf16 v[128:131], v[40:43], v[184:187], v[128:131]\n\t" \
  "v_mfma_f32_16x16x32_bf16 v[132:135], v[44:47], v[184:187], v[132:135]\n\t" \
  "ds_read_b128 v[184:187], v209 offset:32768\n\t" \
  "s_add_u32 m0, %[lw], 98304\n\t" \
  "v_mfma_f32_16x16x32_bf16 v[136:139], v[40:43], v[188:191], v[136:139]\n\t" \
  "v_mfma_f32_16x16x32_bf16 v[140:143], v[44:47], v[188:191], v[140:143]\n\t" \
  "ds_read_b128 v[188:191], v209 offset:49152\n\t" \
  "global_load_lds_dwordx4 v216, s[92:93] sc1\n\t" \
  "s_waitcnt lgkmcnt(8)\n\t" \
  "v_mfma_f32_16x16x32_bf16 v[128:131], v[48:51], v[192:195], v[128:131]\n\t" \
  "v_mfma_f32_16x16x32_bf16 v[132:135], v[52:55], v[192:195], v[132:135]\n\t" \
  "ds_read_b128 v[192:195], v210 offset:32768\n\t" \
  "v_mfma_f32_16x16x32_bf16 v[136:139], v[48:51], v[196:199], v[136:139]\n\t" \
  "v_mfma_f32_16x16x32_bf16 v[140:143], v[52:55], v[196:199], v[140:143]\n\t" \
  "ds_read_b128 v[196:199], v210 offset:49152\n\t" \
  "s_waitcnt lgkmcnt(4)\n\t" \
  "v_add_f32 v160, v160, v144\n\t" \
  "v_add_f32 v161, v161, v145\n\t" \
  "v_add_f32 v162, v162, v146\n\t" \
  "v_add_f32 v163, v163, v147\n\t" \
  "v_add_f32 v164, v164, v148\n\t" \
  "v_add_f32 v165, v165, v149\n\t" \
  "v_add_f32 v166, v166, v150\n\t" \
  "v_add_f32 v167, v167, v151\n\t" \
  "v_mfma_f32_16x16x32_bf16 v[128:131], v[56:59], v[200:203], v[128:131]\n\t" \
  "v_mfma_f32_16x16x32_bf16 v[132:135], v[60:63], v[200:203], v[132:135]\n\t" \
  "ds_read_b128 v[200:203], v211 offset:32768\n\t" \
  "s_add_u32 m0, %[lw], 99328\n\t" \
  "v_mfma_f32_16x16x32_bf16 v[136:139], v[56:59], v[204:207], v[136:139]\n\t" \
  "v_mfma_f32_16x16x32_bf16 v[140:143], v[60:63], v[204:207], v[140:143]\n\t" \
  "ds_read_b128 v[204:207], v211 offset:49152\n\t" \
  "global_load_lds_dwordx4 v217, s[92:93] sc1\n\t" \
  "v_cvt_pk_bf16_f32 v168, v160, v161\n\t" \
  "v_cvt_pk_bf16_f32 v169, v162, v163\n\t" \
  "v_cvt_pk_bf16_f32 v170, v164, v165\n\t" \
  "v_cvt_pk_bf16_f32 v171, v166, v167\n\t" \
  "buffer_store_dwordx4 v[168:171], v172, %[rt], s90 offen sc1\n\t" \
  "v_mfma_f32_16x16x32_bf16 v[128:131], v[64:67], v[176:179], v[128:131]\n\t" \
  "v_mfma_f32_16x16x32_bf16 v[132:135], v[68:71], v[176:179], v[132:135]\n\t" \
  "ds_read_b128 v[176:179], v208 offset:33024\n\t" \
  "v_mfma_f32_16x16x32_bf16 v[136:139], v[64:67], v[180:183], v[136:139]\n\t" \
  "v_mfma_f32_16x16x32_bf16 v[140:143], v[68:71], v[180:183], v[140:143]\n\t" \
  "ds_read_b128 v[180:183], v208 offset:49408\n\t" \
  "s_waitcnt lgkmcnt(6)\n\t" \
  "v_mfma_f32_16x16x32_bf16 v[128:131], v[72:75], v[184:187], v[128:131]\n\t" \
  "v_mfma_f32_16x16x32_bf16 v[132:135], v[76:79], v[184:187], v[132:135]\n\t" \
  "ds_read_b128 v[184:187], v209 offset:33024\n\t" \
  "s_add_u32 m0, %[lw], 100352\n\t" \
  "v_mfma_f32_16x16x32_bf16 v[136:139], v[72:75], v[188:191], v[136:139]\n\t" \
  "v_mfma_f32_16x16x32_bf16 v[140:143], v[76:79], v[188:191], v[140:143]\n\t" \
  "ds_read_b128 v[188:191], v209 offset:49408\n\t" \
  "global_load_lds_dwordx4 v218, s[92:93] sc1\n\t" \
  "s_waitcnt lgkmcnt(6)\n\t" \
  "v_mfma_f32_16x16x32_bf16 v[128:131], v[80:83], v[192:195], v[128:131]\n\t" \
  "v_mfma_f32_16x16x32_bf16 v[132:135], v[84:87], v[192:195], v[132:135]\n\t" \
  "ds_read_b128 v[192:195], v210 offset:33024\n\t" \
  "v_mfma_f32_16x16x32_bf16 v[136:139], v[80:83], v[196:199], v[136:139]\n\t" \
  "v_mfma_f32_16x16x32_bf16 v[140:143], v[84:87], v[196:199], v[140:143]\n\t" \
  "ds_read_b128 v[196:199], v210 offset:49408\n\t" \
  "s_waitcnt lgkmcnt(6)\n\t" \
  "v_mfma_f32_16x16x32_bf16 v[128:131], v[88:91], v[200:203], v[128:131]\n\t" \
  "v_mfma_f32_16x16x32_bf16 v[132:135], v[92:95], v[200:203], v[132:135]\n\t" \
  "ds_read_b128 v[200:203], v211 offset:33024\n\t" \
  "s_add_u32 m0, %[lw], 101376\n\t" \
  "v_mfma_f32_16x16x32_bf16 v[136:139], v[88:91], v[204:207], v[136:139]\n\t" \
  "v_mfma_f32_16x16x32_bf16 v[140:143], v[92:95], v[204:207], v[140:143]\n\t" \
  "ds_read_b128 v[204:207], v211 offset:49408\n\t" \
  "global_load_lds_dwordx4 v219, s[92:93] sc1\n\t" \
  "s_cmp_lt_u32 s86, 8\n\t" \
  "s_cbranch_scc1 .Lw11%=\n\t" \
  "s_waitcnt vmcnt(6)\n\t" \
  "s_barrier\n\t" \
  ".Lg11%=:\n\t" \
  "s_add_u32 s92, s92, 65024\n\t" \
  "s_addc_u32 s93, s93, 0\n\t" \
  "s_waitcnt lgkmcnt(6)\n\t" \
  "v_mfma_f32_16x16x32_bf16 v[128:131], v[96:99], v[176:179], v[128:131]\n\t" \
  "v_mfma_f32_16x16x32_bf16 v[132:135], v[100:103], v[176:179], v[132:135]\n\t" \
  "ds_read_b128 v[176:179], v212 offset:0\n\t" \
  "v_mfma_f32_16x16x32_bf16 v[136:139], v[96:99], v[180:183], v[136:139]\n\t" \
  "v_mfma_f32_16x16x32_bf16 v[140:143], v[100:103], v[180:183], v[140:143]\n\t" \
  "ds_read_b128 v[180:183], v212 offset:16384\n\t" \
  "s_waitcnt lgkmcnt(6)\n\t" \
  "v_mfma_f32_16x16x32_bf16 v[128:131], v[104:107], v[184:187], v[128:131]\n\t" \
  "v_mfma_f32_16x16x32_bf16 v[132:135], v[108:111], v[184:187], v[132:135]\n\t" \
  "ds_read_b128 v[184:187], v213 offset:0\n\t" \
  "s_add_u32 m0, %[lw], 0\n\t" \
  "v_mfma_f32_16x16x32_bf16 v[136:139], v[104:107], v[188:191], v[136:139]\n\t" \
  "v_mfma_f32_16x16x32_bf16 v[140:143], v[108:111], v[188:191], v[140:143]\n\t" \
  "ds_read_b128 v[188:191], v213 offset:16384\n\t" \
  "global_load_lds_dwordx4 v216, s[92:93] sc1\n\t" \
  "s_waitcnt lgkmcnt(6)\n\t" \
  "v_mfma_f32_16x16x32_bf16 v[128:131], v[112:115], v[192:195], v[128:131]\n\t" \
  "v_mfma_f32_16x16x32_bf16 v[132:135], v[116:119], v[192:195], v[132:135]\n\t" \
  "ds_read_b128 v[192:195], v214 offset:0\n\t" \
  "v_mfma_f32_16x16x32_bf16 v[136:139], v[112:115], v[196:199], v[136:139]\n\t" \
  "v_mfma_f32_16x16x32_bf16 v[140:143], v[116:119], v[196:199], v[140:143]\n\t" \
  "ds_read_b128 v[196:199], v214 offset:16384\n\t" \
  "s_waitcnt lgkmcnt(6)\n\t" \
  "v_mfma_f32_16x16x32_bf16 v[128:131], v[120:123], v[200:203], v[128:131]\n\t" \
  "v_mfma_f32_16x16x32_bf16 v[132:135], v[124:127], v[200:203], v[132:135]\n\t" \
  "ds_read_b128 v[200:203], v215 offset:0\n\t" \
  "s_add_u32 m0, %[lw], 1024\n\t" \
  "v_mfma_f32_16x16x32_bf16 v[136:139], v[120:123], v[204:207], v[136:139]\n\t" \
  "v_mfma_f32_16x16x32_bf16 v[140:143], v[124:127], v[204:207], v[140:143]\n\t" \
  "ds_read_b128 v[204:207], v215 offset:16384\n\t" \
  "global_load_lds_dwordx4 v217, s[92:93] sc1\n\t" \
  "s_add_u32 s86, s86, 1\n\t" \
  "s_add_u32 s87, s87, 1\n\t" \
  "s_cmp_le_u32 s86, 5\n\t" \
  "s_cbranch_scc1 .Lf11%=\n\t" \
  ".Lfb11%=:\n\t" \
  "s_waitcnt lgkmcnt(6)\n\t" \
  "v_mfma_f32_16x16x32_bf16 v[144:147], v[0:3], v[176:179], 0\n\t" \
  "v_mfma_f32_16x16x32_bf16 v[148:151], v[4:7], v[176:179], 0\n\t" \
  "ds_read_b128 v[176:179], v212 offset:256\n\t" \
  "v_mfma_f32_16x16x32_bf16 v[152:155], v[0:3], v[180:183], 0\n\t" \
  "v_mfma_f32_16x16x32_bf16 v[156:159], v[4:7], v[180:183], 0\n\t" \
  "ds_read_b128 v[180:183], v212 offset:16640\n\t" \
  "s_waitcnt lgkmcnt(6)\n\t" \
  "v_mfma_f32_16x16x32_bf16 v[144:147], v[8:11], v[184:187], v[144:147]\n\t" \
  "v_mfma_f32_16x16x32_bf16 v[148:151], v[12:15], v[184:187], v[148:151]\n\t" \
  "ds_read_b128 v[184:187], v213 offset:256\n\t" \
  "s_add_u32 m0, %[lw], 2048\n\t" \
  "v_mfma_f32_16x16x32_bf16 v[152:155], v[8:11], v[188:191], v[152:155]\n\t" \
  "v_mfma_f32_16x16x32_bf16 v[156:159], v[12:15], v[188:191], v[156:159]\n\t" \
  "ds_read_b128 v[188:191], v213 offset:16640\n\t" \
  "global_load_lds_dwordx4 v218, s[92:93] sc1\n\t" \
  "ds_write_b128 v220, v[136:139] offset:0\n\t" \
  "ds_write_b128 v220, v[140:143] offset:1024\n\t" \
  "s_waitcnt lgkmcnt(8)\n\t" \
  "v_mfma_f32_16x16x32_bf16 v[144:147], v[16:19], v[192:195], v[144:147]\n\t" \
  "v_mfma_f32_16x16x32_bf16 v[148:151], v[20:23], v[192:195], v[148:151]\n\t" \
  "ds_read_b128 v[192:195], v214 offset:256\n\t" \
  "v_mfma_f32_16x16x32_bf16 v[152:155], v[16:19], v[196:199], v[152:155]\n\t" \
  "v_mfma_f32_16x16x32_bf16 v[156:159], v[20:23], v[196:199], v[156:159]\n\t" \
  "ds_read_b128 v[196:199], v214 offset:16640\n\t" \
  "s_lshr_b32 s90, s87, 7\n\t" \
  "s_xor_b32 s90, s90, %[s0]\n\t" \
  "s_and_b32 s90, s90, 1\n\t" \
  "s_lshl1_add_u32 s90, s87, s90\n\t" \
  "s_lshl_b32 s90, s90, 13\n\t" \
  "s_cmp_lt_i32 s86, 6\n\t" \
  "s_cselect_b32 s90, 0x70000000, s90\n\t" \
  "s_waitcnt lgkmcnt(8)\n\t" \
  "v_mfma_f32_16x16x32_bf16 v[144:147], v[24:27], v[200:203], v[144:147]\n\t" \
  "v_mfma_f32_16x16x32_bf16 v[148:151], v[28:31], v[200:203], v[148:151]\n\t" \
  "ds_read_b128 v[200:203], v215 offset:256\n\t" \
  "s_add_u32 m0, %[lw], 3072\n\t" \
  "v_mfma_f32_16x16x32_bf16 v[152:155], v[24:27], v[204:207], v[152:155]\n\t" \
  "v_mfma_f32_16x16x32_bf16 v[156:159], v[28:31], v[204:207], v[156:159]\n\t" \
  "ds_read_b128 v[204:207], v215 offset:16640\n\t" \
  "global_load_lds_dwordx4 v219, s[92:93] sc1\n\t" \
  "s_waitcnt lgkmcnt(4)\n\t" \
  "s_cmp_lt_u32 s86, 6\n\t" \
  "s_cbranch_scc1 .Lw21%=\n\t" \
  "s_waitcnt vmcnt(4)\n\t" \
  "s_barrier\n\t" \
  ".Lg21%=:\n\t" \
  "s_add_u32 s92, s92, 512\n\t" \
  "s_addc_u32 s93, s93, 0\n\t" \
  "v_mfma_f32_16x16x32_bf16 v[144:147], v[32:35], v[176:179], v[144:147]\n\t" \
  "v_mfma_f32_16x16x32_bf16 v[148:151], v[36:39], v[176:179], v[148:151]\n\t" \
  "ds_read_b128 v[176:179], v212 offset:32768\n\t" \
  "v_mfma_f32_16x16x32_bf16 v[152:155], v[32:35], v[180:183], v[152:155]\n\t" \
  "v_mfma_f32_16x16x32_bf16 v[156:159], v[36:39], v[180:183], v[156:159]\n\t" \
  "ds_read_b128 v[180:183], v212 offset:49152\n\t" \
  "ds_read_b128 v[160:163], v221 offset:0\n\t" \
  "ds_read_b128 v[164:167], v221 offset:1024\n\t" \
  "s_sub_i32 s91, s86, 2\n\t" \
  "s_max_i32 s88, s88, s91\n\t" \
  "v_mov_b32 v173, s88\n\t" \
  "buffer_store_dword v173, v174, %[rs], 0 offen sc1\n\t" \
  "v_mfma_f32_16x16x32_bf16 v[144:147], v[40:43], v[184:187], v[144:147]\n\t" \
  "v_mfma_f32_16x16x32_bf16 v[148:151], v[44:47], v[184:187], v[148:151]\n\t" \
  "ds_read_b128 v[184:187], v213 offset:32768\n\t" \
  "s_add_u32 m0, %[lw], 32768\n\t" \
  "v_mfma_f32_16x16x32_bf16 v[152:155], v[40:43], v[188:191], v[152:155]\n\t" \
  "v_mfma_f32_16x16x32_bf16 v[156:159], v[44:47], v[188:191], v[156:159]\n\t" \
  "ds_read_b128 v[188:191], v213 offset:49152\n\t" \
  "global_load_lds_dwordx4 v216, s[92:93] sc1\n\t" \
  "s_waitcnt lgkmcnt(8)\n\t" \
  "v_mfma_f32_16x16x32_bf16 v[144:147], v[48:51], v[192:195], v[144:147]\n\t" \
  "v_mfma_f32_16x16x32_bf16 v[148:151], v[52:55], v[192:195], v[148:151]\n\t" \
  "ds_read_b128 v[192:195], v214 offset:32768\n\t" \
  "v_mfma_f32_16x16x32_bf16 v[152:155], v[48:51], v[196:199], v[152:155]\n\t" \
  "v_mfma_f32_16x16x32_bf16 v[156:159], v[52:55], v[196:199], v[156:159]\n\t" \
  "ds_read_b128 v[196:199], v214 offset:49152\n\t" \
  "s_waitcnt lgkmcnt(4)\n\t" \
  "v_add_f32 v160, v160, v128\n\t" \
  "v_add_f32 v161, v161, v129\n\t" \
  "v_add_f32 v162, v162, v130\n\t" \
  "v_add_f32 v163, v163, v131\n\t" \
  "v_add_f32 v164, v164, v132\n\t" \
  "v_add_f32 v165, v165, v133\n\t" \
  "v_add_f32 v166, v166, v134\n\t" \
  "v_add_f32 v167, v167, v135\n\t" \
  "v_mfma_f32_16x16x32_bf16 v[144:147], v[56:59], v[200:203], v[144:147]\n\t" \
  "v_mfma_f32_16x16x32_bf16 v[148:151], v[60:63], v[200:203], v[148:151]\n\t" \
  "ds_read_b128 v[200:203], v215 offset:32768\n\t" \
  "s_add_u32 m0, %[lw], 33792\n\t" \
  "v_mfma_f32_16x16x32_bf16 v[152:155], v[56:59], v[204:207], v[152:155]\n\t" \
  "v_mfma_f32_16x16x32_bf16 v[156:159], v[60:63], v[204:207], v[156:159]\n\t" \
  "ds_read_b128 v[204:207], v215 offset:49152\n\t" \
  "global_load_lds_dwordx4 v217, s[92:93] sc1\n\t" \
  "v_cvt_pk_bf16_f32 v168, v160, v161\n\t" \
  "v_cvt_pk_bf16_f32 v169, v162, v163\n\t" \
  "v_cvt_pk_bf16_f32 v170, v164, v165\n\t" \
  "v_cvt_pk_bf16_f32 v171, v166, v167\n\t" \
  "buffer_store_dwordx4 v[168:171], v172, %[rt], s90 offen sc1\n\t" \
  "v_mfma_f32_16x16x32_bf16 v[144:147], v[64:67], v[176:179], v[144:147]\n\t" \
  "v_mfma_f32_16x16x32_bf16 v[148:151], v[68:71], v[176:179], v[148:151]\n\t" \
  "ds_read_b128 v[176:179], v212 offset:33024\n\t" \
  "v_mfma_f32_16x16x32_bf16 v[152:155], v[64:67], v[180:183], v[152:155]\n\t" \
  "v_mfma_f32_16x16x32_bf16 v[156:159], v[68:71], v[180:183], v[156:159]\n\t" \
  "ds_read_b128 v[180:183], v212 offset:49408\n\t" \
  "s_waitcnt lgkmcnt(6)\n\t" \
  "v_mfma_f32_16x16x32_bf16 v[144:147], v[72:75], v[184:187], v[144:147]\n\t" \
  "v_mfma_f32_16x16x32_bf16 v[148:151], v[76:79], v[184:187], v[148:151]\n\t" \
  "ds_read_b128 v[184:187], v213 offset:33024\n\t" \
  "s_add_u32 m0, %[lw], 34816\n\t" \
  "v_mfma_f32_16x16x32_bf16 v[152:155], v[72:75], v[188:191], v[152:155]\n\t" \
  "v_mfma_f32_16x16x32_bf16 v[156:159], v[76:79], v[188:191], v[156:159]\n\t" \
  "ds_read_b128 v[188:191], v213 offset:49408\n\t" \
  "global_load_lds_dwordx4 v218, s[92:93] sc1\n\t" \
  "s_waitcnt lgkmcnt(6)\n\t" \
  "v_mfma_f32_16x16x32_bf16 v[144:147], v[80:83], v[192:195], v[144:147]\n\t" \
  "v_mfma_f32_16x16x32_bf16 v[148:151], v[84:87], v[192:195], v[148:151]\n\t" \
  "ds_read_b128 v[192:195], v214 offset:33024\n\t" \
  "v_mfma_f32_16x16x32_bf16 v[152:155], v[80:83], v[196:199], v[152:155]\n\t" \
  "v_mfma_f32_16x16x32_bf16 v[156:159], v[84:87], v[196:199], v[156:159]\n\t" \
  "ds_read_b128 v[196:199], v214 offset:49408\n\t" \
  "s_waitcnt lgkmcnt(6)\n\t" \
  "v_mfma_f32_16x16x32_bf16 v[144:147], v[88:91], v[200:203], v[144:147]\n\t" \
  "v_mfma_f32_16x16x32_bf16 v[148:151], v[92:95], v[200:203], v[148:151]\n\t" \
  "ds_read_b128 v[200:203], v215 offset:33024\n\t" \
  "s_add_u32 m0, %[lw], 35840\n\t" \
  "v_mfma_f32_16x16x32_bf16 v[152:155], v[88:91], v[204:207], v[152:155]\n\t" \
  "v_mfma_f32_16x16x32_bf16 v[156:159], v[92:95], v[204:207], v[156:159]\n\t" \
  "ds_read_b128 v[204:207], v215 offset:49408\n\t" \
  "global_load_lds_dwordx4 v219, s[92:93] sc1\n\t" \
  "s_cmp_lt_u32 s86, 8\n\t" \
  "s_cbranch_scc1 .Lw31%=\n\t" \
  "s_waitcnt vmcnt(6)\n\t" \
  "s_barrier\n\t" \
  ".Lg31%=:\n\t" \
  "s_add_u32 s92, s92, 65024\n\t" \
  "s_addc_u32 s93, s93, 0\n\t" \
  "s_waitcnt lgkmcnt(6)\n\t" \
  "v_mfma_f32_16x16x32_bf16 v[144:147], v[96:99], v[176:179], v[144:147]\n\t" \
  "v_mfma_f32_16x16x32_bf16 v[148:151], v[100:103], v[176:179], v[148:151]\n\t" \
  "ds_read_b128 v[176:179], v208 offset:0\n\t" \
  "v_mfma_f32_16x16x32_bf16 v[152:155], v[96:99], v[180:183], v[152:155]\n\t" \
  "v_mfma_f32_16x16x32_bf16 v[156:159], v[100:103], v[180:183], v[156:159]\n\t" \
  "ds_read_b128 v[180:183], v208 offset:16384\n\t" \
  "s_waitcnt lgkmcnt(6)\n\t" \
  "v_mfma_f32_16x16x32_bf16 v[144:147], v[104:107], v[184:187], v[144:147]\n\t" \
  "v_mfma_f32_16x16x32_bf16 v[148:151], v[108:111], v[184:187], v[148:151]\n\t" \
  "ds_read_b128 v[184:187], v209 offset:0\n\t" \
  "s_add_u32 m0, %[lw], 65536\n\t" \
  "v_mfma_f32_16x16x32_bf16 v[152:155], v[104:107], v[188:191], v[152:155]\n\t" \
  "v_mfma_f32_16x16x32_bf16 v[156:159], v[108:111], v[188:191], v[156:159]\n\t" \
  "ds_read_b128 v[188:191], v209 offset:16384\n\t" \
  "global_load_lds_dwordx4 v216, s[92:93] sc1\n\t" \
  "s_waitcnt lgkmcnt(6)\n\t" \
  "v_mfma_f32_16x16x32_bf16 v[144:147], v[112:115], v[192:195], v[144:147]\n\t" \
  "v_mfma_f32_16x16x32_bf16 v[148:151], v[116:119], v[192:195], v[148:151]\n\t" \
  "ds_read_b128 v[192:195], v210 offset:0\n\t" \
  "v_mfma_f32_16x16x32_bf16 v[152:155], v[112:115], v[196:199], v[152:155]\n\t" \
  "v_mfma_f32_16x16x32_bf16 v[156:159], v[116:119], v[196:199], v[156:159]\n\t" \
  "ds_read_b128 v[196:199], v210 offset:16384\n\t" \
  "s_waitcnt lgkmcnt(6)\n\t" \
  "v_mfma_f32_16x16x32_bf16 v[144:147], v[120:123], v[200:203], v[144:147]\n\t" \
  "v_mfma_f32_16x16x32_bf16 v[148:151], v[124:127], v[200:203], v[148:151]\n\t" \
  "ds_read_b128 v[200:203], v211 offset:0\n\t" \
  "s_add_u32 m0, %[lw], 66560\n\t" \
  "v_mfma_f32_16x16x32_bf16 v[152:155], v[120:123], v[204:207], v[152:155]\n\t" \
  "v_mfma_f32_16x16x32_bf16 v[156:159], v[124:127], v[204:207], v[156:159]\n\t" \
  "ds_read_b128 v[204:207], v211 offset:16384\n\t" \
  "global_load_lds_dwordx4 v217, s[92:93] sc1\n\t" \
  "s_add_u32 s86, s86, 1\n\t" \
  "s_add_u32 s87, s87, 1\n\t" \
  "s_cmp_lt_u32 s86, %[end]\n\t" \
  "s_cbranch_scc1 .Lp1%=\n\t" \
  "s_branch .Lx1%=\n\t" \
  ".Lf01%=:\n\t" \
  "s_nop 7\n\t" \
  "ds_write_b128 v220, v[152:155] offset:0\n\t" \
  "ds_write_b128 v220, v[156:159] offset:1024\n\t" \
  "s_lshr_b32 s90, s87, 7\n\t" \
  "s_xor_b32 s90, s90, %[s0]\n\t" \
  "s_and_b32 s90, s90, 1\n\t" \
  "s_lshl1_add_u32 s90, s87, s90\n\t" \
  "s_lshl_b32 s90, s90, 13\n\t" \
  "s_cmp_lt_i32 s86, 1\n\t" \
  "s_cselect_b32 s90, 0x70000000, s90\n\t" \
  "s_waitcnt lgkmcnt(0)\n\t" \
  "s_barrier\n\t" \
  "ds_read_b128 v[160:163], v221 offset:0\n\t" \
  "ds_read_b128 v[164:167], v221 offset:1024\n\t" \
  "s_waitcnt lgkmcnt(0)\n\t" \
  "v_add_f32 v160, v160, v144\n\t" \
  "v_add_f32 v161, v161, v145\n\t" \
  "v_add_f32 v162, v162, v146\n\t" \
  "v_add_f32 v163, v163, v147\n\t" \
  "v_add_f32 v164, v164, v148\n\t" \
  "v_add_f32 v165, v165, v149\n\t" \
  "v_add_f32 v166, v166, v150\n\t" \
  "v_add_f32 v167, v167, v151\n\t" \
  "v_cvt_pk_bf16_f32 v168, v160, v161\n\t" \
  "v_cvt_pk_bf16_f32 v169, v162, v163\n\t" \
  "v_cvt_pk_bf16_f32 v170, v164, v165\n\t" \
  "v_cvt_pk_bf16_f32 v171, v166, v167\n\t" \
  "buffer_store_dwordx4 v[168:171], v172, %[rt], s90 offen sc1\n\t" \
  "s_branch .Lfb01%=\n\t" \
  ".Lf11%=:\n\t" \
  "s_nop 7\n\t" \
  "ds_write_b128 v220, v[136:139] offset:0\n\t" \
  "ds_write_b128 v220, v[140:143] offset:1024\n\t" \
  "s_lshr_b32 s90, s87, 7\n\t" \
  "s_xor_b32 s90, s90, %[s0]\n\t" \
  "s_and_b32 s90, s90, 1\n\t" \
  "s_lshl1_add_u32 s90, s87, s90\n\t" \
  "s_lshl_b32 s90, s90, 13\n\t" \
  "s_cmp_lt_i32 s86, 1\n\t" \
  "s_cselect_b32 s90, 0x70000000, s90\n\t" \
  "s_waitcnt lgkmcnt(0)\n\t" \
  "s_barrier\n\t" \
  "ds_read_b128 v[160:163], v221 offset:0\n\t" \
  "ds_read_b128 v[164:167], v221 offset:1024\n\t" \
  "s_waitcnt lgkmcnt(0)\n\t" \
  "v_add_f32 v160, v160, v128\n\t" \
  "v_add_f32 v161, v161, v129\n\t" \
  "v_add_f32 v162, v162, v130\n\t" \
  "v_add_f32 v163, v163, v131\n\t" \
  "v_add_f32 v164, v164, v132\n\t" \
  "v_add_f32 v165, v165, v133\n\t" \
  "v_add_f32 v166, v166, v134\n\t" \
  "v_add_f32 v167, v167, v135\n\t" \
  "v_cvt_pk_bf16_f32 v168, v160, v161\n\t" \
  "v_cvt_pk_bf16_f32 v169, v162, v163\n\t" \
  "v_cvt_pk_bf16_f32 v170, v164, v165\n\t" \
  "v_cvt_pk_bf16_f32 v171, v166, v167\n\t" \
  "buffer_store_dwordx4 v[168:171], v172, %[rt], s90 offen sc1\n\t" \
  "s_branch .Lfb11%=\n\t" \
  ".Lw01%=:\n\t" \
  "s_waitcnt vmcnt(2)\n\t" \
  "s_barrier\n\t" \
  "s_sub_i32 s91, s86, 1\n\t" \
  "s_max_i32 s88, s88, s91\n\t" \
  "v_mov_b32 v173, s88\n\t" \
  "buffer_store_dword v173, v174, %[rs], 0 offen sc1\n\t" \
  "s_branch .Lg01%=\n\t" \
  ".Lw11%=:\n\t" \
  "s_waitcnt vmcnt(2)\n\t" \
  "s_barrier\n\t" \
  "s_sub_i32 s91, s86, 1\n\t" \
  "s_max_i32 s88, s88, s91\n\t" \
  "v_mov_b32 v173, s88\n\t" \
  "buffer_store_dword v173, v174, %[rs], 0 offen sc1\n\t" \
  "s_branch .Lg11%=\n\t" \
  ".Lw21%=:\n\t" \
  "s_waitcnt vmcnt(2)\n\t" \
  "s_barrier\n\t" \
  "s_sub_i32 s91, s86, 1\n\t" \
  "s_max_i32 s88, s88, s91\n\t" \
  "v_mov_b32 v173, s88\n\t" \
  "buffer_store_dword v173, v174, %[rs], 0 offen sc1\n\t" \
  "s_branch .Lg21%=\n\t" \
  ".Lw31%=:\n\t" \
  "s_waitcnt vmcnt(2)\n\t" \
  "s_barrier\n\t" \
  "s_sub_i32 s91, s86, 1\n\t" \
  "s_max_i32 s88, s88, s91\n\t" \
  "v_mov_b32 v173, s88\n\t" \
  "buffer_store_dword v173, v174, %[rs], 0 offen sc1\n\t" \
  "s_branch .Lg31%=\n\t" \
  ".Lx1%=:\n\t" \
  "s_waitcnt lgkmcnt(0)\n\t" \
  "s_setprio 0\n\t" \
  "s_mov_b32 m0, s94\n\t" \
  ".Le%=:\n\t"
#define PR_ASM_1 \
  "s_cmp_lg_u32 %[kh], 0\n\t" \
  "s_cbranch_scc1 .Lk%=\n\t" \
  "s_mov_b32 s94, m0\n\t" \
  "s_mov_b64 s[92:93], %[x0]\n\t" \
  "s_mov_b32 s86, %[g0]\n\t" \
  "s_add_u32 s87, s86, %[t0]\n\t" \
  "s_sub_i32 s88, s86, 3\n\t" \
  "ds_read_b128 v[176:179], v208 offset:0\n\t" \
  "ds_read_b128 v[180:183], v208 offset:16384\n\t" \
  "ds_read_b128 v[184:187], v209 offset:0\n\t" \
  "ds_read_b128 v[188:191], v209 offset:16384\n\t" \
  "ds_read_b128 v[192:195], v210 offset:0\n\t" \
  "ds_read_b128 v[196:199], v210 offset:16384\n\t" \
  "ds_read_b128 v[200:203], v211 offset:0\n\t" \
  "ds_read_b128 v[204:207], v211 offset:16384\n\t" \
  "s_branch .Lfb00%=\n\t" \
  ".Lp0%=:\n\t" \
  "s_cmp_le_u32 s86, 5\n\t" \
  "s_cbranch_scc1 .Lf00%=\n\t" \
  ".Lfb00%=:\n\t" \
  "s_waitcnt lgkmcnt(6)\n\t" \
  "v_mfma_f32_16x16x32_bf16 v[128:131], v[176:179], v[0:3], 0\n\t" \
  "v_mfma_f32_16x16x32_bf16 v[136:139], v[176:179], v[4:7], 0\n\t" \
  "ds_read_b128 v[176:179], v208 offset:256\n\t" \
  "s_add_u32 m0, %[lw], 67584\n\t" \
  "v_mfma_f32_16x16x32_bf16 v[132:135], v[180:183], v[0:3], 0\n\t" \
  "v_mfma_f32_16x16x32_bf16 v[140:143], v[180:183], v[4:7], 0\n\t" \
  "ds_read_b128 v[180:183], v208 offset:16640\n\t" \
  "global_load_lds_dwordx4 v218, s[92:93] sc1\n\t" \
  "s_waitcnt lgkmcnt(6)\n\t" \
  "v_mfma_f32_16x16x32_bf16 v[128:131], v[184:187], v[8:11], v[128:131]\n\t" \
  "v_mfma_f32_16x16x32_bf16 v[136:139], v[184:187], v[12:15], v[136:139]\n\t" \
  "ds_read_b128 v[184:187], v209 offset:256\n\t" \
  "v_mfma_f32_16x16x32_bf16 v[132:135], v[188:191], v[8:11], v[132:135]\n\t" \
  "v_mfma_f32_16x16x32_bf16 v[140:143], v[188:191], v[12:15], v[140:143]\n\t" \
  "ds_read_b128 v[188:191], v209 offset:16640\n\t" \
  "ds_write_b128 v220, v[152:155] offset:0\n\t" \
  "ds_write_b128 v220, v[156:159] offset:1024\n\t" \
  "s_waitcnt lgkmcnt(8)\n\t" \
  "v_mfma_f32_16x16x32_bf16 v[128:131], v[192:195], v[16:19], v[128:131]\n\t" \
  "v_mfma_f32_16x16x32_bf16 v[136:139], v[192:195], v[20:23], v[136:139]\n\t" \
  "ds_read_b128 v[192:195], v210 offset:256\n\t" \
  "s_add_u32 m0, %[lw], 68608\n\t" \
  "v_mfma_f32_16x16x32_bf16 v[132:135], v[196:199], v[16:19], v[132:135]\n\t" \
  "v_mfma_f32_16x16x32_bf16 v[140:143], v[196:199], v[20:23], v[140:143]\n\t" \
  "ds_read_b128 v[196:199], v210 offset:16640\n\t" \
  "global_load_lds_dwordx4 v219, s[92:93] sc1\n\t" \
  "s_lshr_b32 s90, s87, 7\n\t" \
  "s_xor_b32 s90, s90, %[s0]\n\t" \
  "s_and_b32 s90, s90, 1\n\t" \
  "s_lshl1_add_u32 s90, s87, s90\n\t" \
  "s_lshl_b32 s90, s90, 13\n\t" \
  "s_cmp_lt_i32 s86, 6\n\t" \
  "s_cselect_b32 s90, 0x70000000, s90\n\t" \
  "s_waitcnt lgkmcnt(8)\n\t" \
  "v_mfma_f32_16x16x32_bf16 v[128:131], v[200:203], v[24:27], v[128:131]\n\t" \
  "v_mfma_f32_16x16x32_bf16 v[136:139], v[200:203], v[28:31], v[136:139]\n\t" \
  "ds_read_b128 v[200:203], v211 offset:256\n\t" \
  "v_mfma_f32_16x16x32_bf16 v[132:135], v[204:207], v[24:27], v[132:135]\n\t" \
  "v_mfma_f32_16x16x32_bf16 v[140:143], v[204:207], v[28:31], v[140:143]\n\t" \
  "ds_read_b128 v[204:207], v211 offset:16640\n\t" \
  "s_waitcnt lgkmcnt(4)\n\t" \
  "s_cmp_lt_u32 s86, 6\n\t" \
  "s_cbranch_scc1 .Lw00%=\n\t" \
  "s_waitcnt vmcnt(4)\n\t" \
  "s_barrier\n\t" \
  ".Lg00%=:\n\t" \
  "s_add_u32 s92, s92, 512\n\t" \
  "s_addc_u32 s93, s93, 0\n\t" \
  "v_mfma_f32_16x16x32_bf16 v[128:131], v[176:179], v[32:35], v[128:131]\n\t" \
  "v_mfma_f32_16x16x32_bf16 v[136:139], v[176:179], v[36:39], v[136:139]\n\t" \
  "ds_read_b128 v[176:179], v208 offset:32768\n\t" \
  "s_add_u32 m0, %[lw], 98304\n\t" \
  "v_mfma_f32_16x16x32_bf16 v[132:135], v[180:183], v[32:35], v[132:135]\n\t" \
  "v_mfma_f32_16x16x32_bf16 v[140:143], v[180:183], v[36:39], v[140:143]\n\t" \
  "ds_read_b128 v[180:183], v208 offset:49152\n\t" \
  "global_load_lds_dwordx4 v216, s[92:93] sc1\n\t" \
  "ds_read_b128 v[160:163], v221 offset:0\n\t" \
  "ds_read_b128 v[164:167], v221 offset:1024\n\t" \
  "v_mfma_f32_16x16x32_bf16 v[128:131], v[184:187], v[40:43], v[128:131]\n\t" \
  "v_mfma_f32_16x16x32_bf16 v[136:139], v[184:187], v[44:47], v[136:139]\n\t" \
  "ds_read_b128 v[184:187], v209 offset:32768\n\t" \
  "v_mfma_f32_16x16x32_bf16 v[132:135], v[188:191], v[40:43], v[132:135]\n\t" \
  "v_mfma_f32_16x16x32_bf16 v[140:143], v[188:191], v[44:47], v[140:143]\n\t" \
  "ds_read_b128 v[188:191], v209 offset:49152\n\t" \
  "s_waitcnt lgkmcnt(8)\n\t" \
  "v_mfma_f32_16x16x32_bf16 v[128:131], v[192:195], v[48:51], v[128:131]\n\t" \
  "v_mfma_f32_16x16x32_bf16 v[136:139], v[192:195], v[52:55], v[136:139]\n\t" \
  "ds_read_b128 v[192:195], v210 offset:32768\n\t" \
  "s_add_u32 m0, %[lw], 99328\n\t" \
  "v_mfma_f32_16x16x32_bf16 v[132:135], v[196:199], v[48:51], v[132:135]\n\t" \
  "v_mfma_f32_16x16x32_bf16 v[140:143], v[196:199], v[52:55], v[140:143]\n\t" \
  "ds_read_b128 v[196:199], v210 offset:49152\n\t" \
  "global_load_lds_dwordx4 v217, s[92:93] sc1\n\t" \
  "s_waitcnt lgkmcnt(4)\n\t" \
  "v_add_f32 v160, v160, v144\n\t" \
  "v_add_f32 v161, v161, v145\n\t" \
  "v_add_f32 v162, v162, v146\n\t" \
  "v_add_f32 v163, v163, v147\n\t" \
  "v_add_f32 v164, v164, v148\n\t" \
  "v_add_f32 v165, v165, v149\n\t" \
  "v_add_f32 v166, v166, v150\n\t" \
  "v_add_f32 v167, v167, v151\n\t" \
  "v_mfma_f32_16x16x32_bf16 v[128:131], v[200:203], v[56:59], v[128:131]\n\t" \
  "v_mfma_f32_16x16x32_bf16 v[136:139], v[200:203], v[60:63], v[136:139]\n\t" \
  "ds_read_b128 v[200:203], v211 offset:32768\n\t" \
  "v_mfma_f32_16x16x32_bf16 v[132:135], v[204:207], v[56:59], v[132:135]\n\t" \
  "v_mfma_f32_16x16x32_bf16 v[140:143], v[204:207], v[60:63], v[140:143]\n\t" \
  "ds_read_b128 v[204:207], v211 offset:49152\n\t" \
  "v_cvt_pk_bf16_f32 v168, v160, v161\n\t" \
  "v_cvt_pk_bf16_f32 v169, v162, v163\n\t" \
  "v_cvt_pk_bf16_f32 v170, v164, v165\n\t" \
  "v_cvt_pk_bf16_f32 v171, v166, v167\n\t" \
  "buffer_store_dwordx4 v[168:171], v172, %[rt], s90 offen sc1\n\t" \
  "v_mfma_f32_16x16x32_bf16 v[128:131], v[176:179], v[64:67], v[128:131]\n\t" \
  "v_mfma_f32_16x16x32_bf16 v[136:139], v[176:179], v[68:71], v[136:139]\n\t" \
  "ds_read_b128 v[176:179], v208 offset:33024\n\t" \
  "s_add_u32 m0, %[lw], 100352\n\t" \
  "v_mfma_f32_16x16x32_bf16 v[132:135], v[180:183], v[64:67], v[132:135]\n\t" \
  "v_mfma_f32_16x16x32_bf16 v[140:143], v[180:183], v[68:71], v[140:143]\n\t" \
  "ds_read_b128 v[180:183], v208 offset:49408\n\t" \
  "global_load_lds_dwordx4 v218, s[92:93] sc1\n\t" \
  "s_waitcnt lgkmcnt(6)\n\t" \
  "v_mfma_f32_16x16x32_bf16 v[128:131], v[184:187], v[72:75], v[128:131]\n\t" \
  "v_mfma_f32_16x16x32_bf16 v[136:139], v[184:187], v[76:79], v[136:139]\n\t" \
  "ds_read_b128 v[184:187], v209 offset:33024\n\t" \
  "v_mfma_f32_16x16x32_bf16 v[132:135], v[188:191], v[72:75], v[132:135]\n\t" \
  "v_mfma_f32_16x16x32_bf16 v[140:143], v[188:191], v[76:79], v[140:143]\n\t" \
  "ds_read_b128 v[188:191], v209 offset:49408\n\t" \
  "s_waitcnt lgkmcnt(6)\n\t" \
  "v_mfma_f32_16x16x32_bf16 v[128:131], v[192:195], v[80:83], v[128:131]\n\t" \
  "v_mfma_f32_16x16x32_bf16 v[136:139], v[192:195], v[84:87], v[136:139]\n\t" \
  "ds_read_b128 v[192:195], v210 offset:33024\n\t" \
  "s_add_u32 m0, %[lw], 101376\n\t" \
  "v_mfma_f32_16x16x32_bf16 v[132:135], v[196:199], v[80:83], v[132:135]\n\t" \
  "v_mfma_f32_16x16x32_bf16 v[140:143], v[196:199], v[84:87], v[140:143]\n\t" \
  "ds_read_b128 v[196:199], v210 offset:49408\n\t" \
  "global_load_lds_dwordx4 v219, s[92:93] sc1\n\t" \
  "s_waitcnt lgkmcnt(6)\n\t" \
  "v_mfma_f32_16x16x32_bf16 v[128:131], v[200:203], v[88:91], v[128:131]\n\t" \
  "v_mfma_f32_16x16x32_bf16 v[136:139], v[200:203], v[92:95], v[136:139]\n\t" \
  "ds_read_b128 v[200:203], v211 offset:33024\n\t" \
  "v_mfma_f32_16x16x32_bf16 v[132:135], v[204:207], v[88:91], v[132:135]\n\t" \
  "v_mfma_f32_16x16x32_bf16 v[140:143], v[204:207], v[92:95], v[140:143]\n\t" \
  "ds_read_b128 v[204:207], v211 offset:49408\n\t" \
  "s_cmp_lt_u32 s86, 8\n\t" \
  "s_cbranch_scc1 .Lw10%=\n\t" \
  "s_waitcnt vmcnt(5)\n\t" \
  "s_barrier\n\t" \
  ".Lg10%=:\n\t" \
  "s_add_u32 s92, s92, 65024\n\t" \
  "s_addc_u32 s93, s93, 0\n\t" \
  "s_waitcnt lgkmcnt(6)\n\t" \
  "v_mfma_f32_16x16x32_bf16 v[128:131], v[176:179], v[96:99], v[128:131]\n\t" \
  "v_mfma_f32_16x16x32_bf16 v[136:139], v[176:179], v[100:103], v[136:139]\n\t" \
  "ds_read_b128 v[176:179], v212 offset:0\n\t" \
  "s_add_u32 m0, %[lw], 0\n\t" \
  "v_mfma_f32_16x16x32_bf16 v[132:135], v[180:183], v[96:99], v[132:135]\n\t" \
  "v_mfma_f32_16x16x32_bf16 v[140:143], v[180:183], v[100:103], v[140:143]\n\t" \
  "ds_read_b128 v[180:183], v212 offset:16384\n\t" \
  "global_load_lds_dwordx4 v216, s[92:93] sc1\n\t" \
  "s_waitcnt lgkmcnt(6)\n\t" \
  "v_mfma_f32_16x16x32_bf16 v[128:131], v[184:187], v[104:107], v[128:131]\n\t" \
  "v_mfma_f32_16x16x32_bf16 v[136:139], v[184:187], v[108:111], v[136:139]\n\t" \
  "ds_read_b128 v[184:187], v213 offset:0\n\t" \
  "v_mfma_f32_16x16x32_bf16 v[132:135], v[188:191], v[104:107], v[132:135]\n\t" \
  "v_mfma_f32_16x16x32_bf16 v[140:143], v[188:191], v[108:111], v[140:143]\n\t" \
  "ds_read_b128 v[188:191], v213 offset:16384\n\t" \
  "s_waitcnt lgkmcnt(6)\n\t" \
  "v_mfma_f32_16x16x32_bf16 v[128:131], v[192:195], v[112:115], v[128:131]\n\t" \
  "v_mfma_f32_16x16x32_bf16 v[136:139], v[192:195], v[116:119], v[136:139]\n\t" \
  "ds_read_b128 v[192:195], v214 offset:0\n\t" \
  "s_add_u32 m0, %[lw], 1024\n\t" \
  "v_mfma_f32_16x16x32_bf16 v[132:135], v[196:199], v[112:115], v[132:135]\n\t" \
  "v_mfma_f32_16x16x32_bf16 v[140:143], v[196:199], v[116:119], v[140:143]\n\t" \
  "ds_read_b128 v[196:199], v214 offset:16384\n\t" \
  "global_load_lds_dwordx4 v217, s[92:93] sc1\n\t" \
  "s_waitcnt lgkmcnt(6)\n\t" \
  "v_mfma_f32_16x16x32_bf16 v[128:131], v[200:203], v[120:123], v[128:131]\n\t" \
  "v_mfma_f32_16x16x32_bf16 v[136:139], v[200:203], v[124:127], v[136:139]\n\t" \
  "ds_read_b128 v[200:203], v215 offset:0\n\t" \
  "v_mfma_f32_16x16x32_bf16 v[132:135], v[204:207], v[120:123], v[132:135]\n\t" \
  "v_mfma_f32_16x16x32_bf16 v[140:143], v[204:207], v[124:127], v[140:143]\n\t" \
  "ds_read_b128 v[204:207], v215 offset:16384\n\t" \
  "s_add_u32 s86, s86, 1\n\t" \
  "s_add_u32 s87, s87, 1\n\t" \
  "s_cmp_le_u32 s86, 5\n\t" \
  "s_cbranch_scc1 .Lf10%=\n\t" \
  ".Lfb10%=:\n\t" \
  "s_waitcnt lgkmcnt(6)\n\t" \
  "v_mfma_f32_16x16x32_bf16 v[144:147], v[176:179], v[0:3], 0\n\t" \
  "v_mfma_f32_16x16x32_bf16 v[152:155], v[176:179], v[4:7], 0\n\t" \
  "ds_read_b128 v[176:179], v212 offset:256\n\t" \
  "s_add_u32 m0, %[lw], 2048\n\t" \
  "v_mfma_f32_16x16x32_bf16 v[148:151], v[180:183], v[0:3], 0\n\t" \
  "v_mfma_f32_16x16x32_bf16 v[156:159], v[180:183], v[4:7], 0\n\t" \
  "ds_read_b128 v[180:183], v212 offset:16640\n\t" \
  "global_load_lds_dwordx4 v218, s[92:93] sc1\n\t" \
  "s_waitcnt lgkmcnt(6)\n\t" \
  "v_mfma_f32_16x16x32_bf16 v[144:147], v[184:187], v[8:11], v[144:147]\n\t" \
  "v_mfma_f32_16x16x32_bf16 v[152:155], v[184:187], v[12:15], v[152:155]\n\t" \
  "ds_read_b128 v[184:187], v213 offset:256\n\t" \
  "v_mfma_f32_16x16x32_bf16 v[148:151], v[188:191], v[8:11], v[148:151]\n\t" \
  "v_mfma_f32_16x16x32_bf16 v[156:159], v[188:191], v[12:15], v[156:159]\n\t" \
  "ds_read_b128 v[188:191], v213 offset:16640\n\t" \
  "ds_write_b128 v220, v[136:139] offset:0\n\t" \
  "ds_write_b128 v220, v[140:143] offset:1024\n\t" \
  "s_waitcnt lgkmcnt(8)\n\t" \
  "v_mfma_f32_16x16x32_bf16 v[144:147], v[192:195], v[16:19], v[144:147]\n\t" \
  "v_mfma_f32_16x16x32_bf16 v[152:155], v[192:195], v[20:23], v[152:155]\n\t" \
  "ds_read_b128 v[192:195], v214 offset:256\n\t" \
  "s_add_u32 m0, %[lw], 3072\n\t" \
  "v_mfma_f32_16x16x32_bf16 v[148:151], v[196:199], v[16:19], v[148:151]\n\t" \
  "v_mfma_f32_16x16x32_bf16 v[156:159], v[196:199], v[20:23], v[156:159]\n\t" \
  "ds_read_b128 v[196:199], v214 offset:16640\n\t" \
  "global_load_lds_dwordx4 v219, s[92:93] sc1\n\t" \
  "s_lshr_b32 s90, s87, 7\n\t" \
  "s_xor_b32 s90, s90, %[s0]\n\t" \
  "s_and_b32 s90, s90, 1\n\t" \
  "s_lshl1_add_u32 s90, s87, s90\n\t" \
  "s_lshl_b32 s90, s90, 13\n\t" \
  "s_cmp_lt_i32 s86, 6\n\t" \
  "s_cselect_b32 s90, 0x70000000, s90\n\t" \
  "s_waitcnt lgkmcnt(8)\n\t" \
  "v_mfma_f32_16x16x32_bf16 v[144:147], v[200:203], v[24:27], v[144:147]\n\t" \
  "v_mfma_f32_16x16x32_bf16 v[152:155], v[200:203], v[28:31], v[152:155]\n\t" \
  "ds_read_b128 v[200:203], v215 offset:256\n\t" \
  "v_mfma_f32_16x16x32_bf16 v[148:151], v[204:207], v[24:27], v[148:151]\n\t" \
  "v_mfma_f32_16x16x32_bf16 v[156:159], v[204:207], v[28:31], v[156:159]\n\t" \
  "ds_read_b128 v[204:207], v215 offset:16640\n\t" \
  "s_waitcnt lgkmcnt(4)\n\t" \
  "s_cmp_lt_u32 s86, 6\n\t" \
  "s_cbranch_scc1 .Lw20%=\n\t" \
  "s_waitcnt vmcnt(4)\n\t" \
  "s_barrier\n\t" \
  ".Lg20%=:\n\t" \
  "s_add_u32 s92, s92, 512\n\t" \
  "s_addc_u32 s93, s93, 0\n\t" \
  "v_mfma_f32_16x16x32_bf16 v[144:147], v[176:179], v[32:35], v[144:147]\n\t" \
  "v_mfma_f32_16x16x32_bf16 v[152:155], v[176:179], v[36:39], v[152:155]\n\t" \
  "ds_read_b128 v[176:179], v212 offset:32768\n\t" \
  "s_add_u32 m0, %[lw], 32768\n\t" \
  "v_mfma_f32_16x16x32_bf16 v[148:151], v[180:183], v[32:35], v[148:151]\n\t" \
  "v_mfma_f32_16x16x32_bf16 v[156:159], v[180:183], v[36:39], v[156:159]\n\t" \
  "ds_read_b128 v[180:183], v212 offset:49152\n\t" \
  "global_load_lds_dwordx4 v216, s[92:93] sc1\n\t" \
  "ds_read_b128 v[160:163], v221 offset:0\n\t" \
  "ds_read_b128 v[164:167], v221 offset:1024\n\t" \
  "v_mfma_f32_16x16x32_bf16 v[144:147], v[184:187], v[40:43], v[144:147]\n\t" \
  "v_mfma_f32_16x16x32_bf16 v[152:155], v[184:187], v[44:47], v[152:155]\n\t" \
  "ds_read_b128 v[184:187], v213 offset:32768\n\t" \
  "v_mfma_f32_16x16x32_bf16 v[148:151], v[188:191], v[40:43], v[148:151]\n\t" \
  "v_mfma_f32_16x16x32_bf16 v[156:159], v[188:191], v[44:47], v[156:159]\n\t" \
  "ds_read_b128 v[188:191], v213 offset:49152\n\t" \
  "s_waitcnt lgkmcnt(8)\n\t" \
  "v_mfma_f32_16x16x32_bf16 v[144:147], v[192:195], v[48:51], v[144:147]\n\t" \
  "v_mfma_f32_16x16x32_bf16 v[152:155], v[192:195], v[52:55], v[152:155]\n\t" \
  "ds_read_b128 v[192:195], v214 offset:32768\n\t" \
  "s_add_u32 m0, %[lw], 33792\n\t" \
  "v_mfma_f32_16x16x32_bf16 v[148:151], v[196:199], v[48:51], v[148:151]\n\t" \
  "v_mfma_f32_16x16x32_bf16 v[156:159], v[196:199], v[52:55], v[156:159]\n\t" \
  "ds_read_b128 v[196:199], v214 offset:49152\n\t" \
  "global_load_lds_dwordx4 v217, s[92:93] sc1\n\t" \
  "s_waitcnt lgkmcnt(4)\n\t" \
  "v_add_f32 v160, v160, v128\n\t" \
  "v_add_f32 v161, v161, v129\n\t" \
  "v_add_f32 v162, v162, v130\n\t" \
  "v_add_f32 v163, v163, v131\n\t" \
  "v_add_f32 v164, v164, v132\n\t" \
  "v_add_f32 v165, v165, v133\n\t" \
  "v_add_f32 v166, v166, v134\n\t" \
  "v_add_f32 v167, v167, v135\n\t" \
  "v_mfma_f32_16x16x32_bf16 v[144:147], v[200:203], v[56:59], v[144:147]\n\t" \
  "v_mfma_f32_16x16x32_bf16 v[152:155], v[200:203], v[60:63], v[152:155]\n\t" \
  "ds_read_b128 v[200:203], v215 offset:32768\n\t" \
  "v_mfma_f32_16x16x32_bf16 v[148:151], v[204:207], v[56:59], v[148:151]\n\t" \
  "v_mfma_f32_16x16x32_bf16 v[156:159], v[204:207], v[60:63], v[156:159]\n\t" \
  "ds_read_b128 v[204:207], v215 offset:49152\n\t" \
  "v_cvt_pk_bf16_f32 v168, v160, v161\n\t" \
  "v_cvt_pk_bf16_f32 v169, v162, v163\n\t" \
  "v_cvt_pk_bf16_f32 v170, v164, v165\n\t" \
  "v_cvt_pk_bf16_f32 v171, v166, v167\n\t" \
  "buffer_store_dwordx4 v[168:171], v172, %[rt], s90 offen sc1\n\t" \
  "v_mfma_f32_16x16x32_bf16 v[144:147], v[176:179], v[64:67], v[144:147]\n\t" \
  "v_mfma_f32_16x16x32_bf16 v[152:155], v[176:179], v[68:71], v[152:155]\n\t" \
  "ds_read_b128 v[176:179], v212 offset:33024\n\t" \
  "s_add_u32 m0, %[lw], 34816\n\t" \
  "v_mfma_f32_16x16x32_bf16 v[148:151], v[180:183], v[64:67], v[148:151]\n\t" \
  "v_mfma_f32_16x16x32_bf16 v[156:159], v[180:183], v[68:71], v[156:159]\n\t" \
  "ds_read_b128 v[180:183], v212 offset:49408\n\t" \
  "global_load_lds_dwordx4 v218, s[92:93] sc1\n\t" \
  "s_waitcnt lgkmcnt(6)\n\t" \
  "v_mfma_f32_16x16x32_bf16 v[144:147], v[184:187], v[72:75], v[144:147]\n\t" \
  "v_mfma_f32_16x16x32_bf16 v[152:155], v[184:187], v[76:79], v[152:155]\n\t" \
  "ds_read_b128 v[184:187], v213 offset:33024\n\t" \
  "v_mfma_f32_16x16x32_bf16 v[148:151], v[188:191], v[72:75], v[148:151]\n\t" \
  "v_mfma_f32_16x16x32_bf16 v[156:159], v[188:191], v[76:79], v[156:159]\n\t" \
  "ds_read_b128 v[188:191], v213 offset:49408\n\t" \
  "s_waitcnt lgkmcnt(6)\n\t" \
  "v_mfma_f32_16x16x32_bf16 v[144:147], v[192:195], v[80:83], v[144:147]\n\t" \
  "v_mfma_f32_16x16x32_bf16 v[152:155], v[192:195], v[84:87], v[152:155]\n\t" \
  "ds_read_b128 v[192:195], v214 offset:33024\n\t" \
  "s_add_u32 m0, %[lw], 35840\n\t" \
  "v_mfma_f32_16x16x32_bf16 v[148:151], v[196:199], v[80:83], v[148:151]\n\t" \
  "v_mfma_f32_16x16x32_bf16 v[156:159], v[196:199], v[84:87], v[156:159]\n\t" \
  "ds_read_b128 v[196:199], v214 offset:49408\n\t" \
  "global_load_lds_dwordx4 v219, s[92:93] sc1\n\t" \
  "s_waitcnt lgkmcnt(6)\n\t" \
  "v_mfma_f32_16x16x32_bf16 v[144:147], v[200:203], v[88:91], v[144:147]\n\t" \
  "v_mfma_f32_16x16x32_bf16 v[152:155], v[200:203], v[92:95], v[152:155]\n\t" \
  "ds_read_b128 v[200:203], v215 offset:33024\n\t" \
  "v_mfma_f32_16x16x32_bf16 v[148:151], v[204:207], v[88:91], v[148:151]\n\t" \
  "v_mfma_f32_16x16x32_bf16 v[156:159], v[204:207], v[92:95], v[156:159]\n\t" \
  "ds_read_b128 v[204:207], v215 offset:49408\n\t" \
  "s_cmp_lt_u32 s86, 8\n\t" \
  "s_cbranch_scc1 .Lw30%=\n\t" \
  "s_waitcnt vmcnt(5)\n\t" \
  "s_barrier\n\t" \
  ".Lg30%=:\n\t" \
  "s_add_u32 s92, s92, 65024\n\t" \
  "s_addc_u32 s93, s93, 0\n\t" \
  "s_waitcnt lgkmcnt(6)\n\t" \
  "v_mfma_f32_16x16x32_bf16 v[144:147], v[176:179], v[96:99], v[144:147]\n\t" \
  "v_mfma_f32_16x16x32_bf16 v[152:155], v[176:179], v[100:103], v[152:155]\n\t" \
  "ds_read_b128 v[176:179], v208 offset:0\n\t" \
  "s_add_u32 m0, %[lw], 65536\n\t" \
  "v_mfma_f32_16x16x32_bf16 v[148:151], v[180:183], v[96:99], v[148:151]\n\t" \
  "v_mfma_f32_16x16x32_bf16 v[156:159], v[180:183], v[100:103], v[156:159]\n\t" \
  "ds_read_b128 v[180:183], v208 offset:16384\n\t" \
  "global_load_lds_dwordx4 v216, s[92:93] sc1\n\t" \
  "s_waitcnt lgkmcnt(6)\n\t" \
  "v_mfma_f32_16x16x32_bf16 v[144:147], v[184:187], v[104:107], v[144:147]\n\t" \
  "v_mfma_f32_16x16x32_bf16 v[152:155], v[184:187], v[108:111], v[152:155]\n\t" \
  "ds_read_b128 v[184:187], v209 offset:0\n\t" \
  "v_mfma_f32_16x16x32_bf16 v[148:151], v[188:191], v[104:107], v[148:151]\n\t" \
  "v_mfma_f32_16x16x32_bf16 v[156:159], v[188:191], v[108:111], v[156:159]\n\t" \
  "ds_read_b128 v[188:191], v209 offset:16384\n\t" \
  "s_waitcnt lgkmcnt(6)\n\t" \
  "v_mfma_f32_16x16x32_bf16 v[144:147], v[192:195], v[112:115], v[144:147]\n\t" \
  "v_mfma_f32_16x16x32_bf16 v[152:155], v[192:195], v[116:119], v[152:155]\n\t" \
  "ds_read_b128 v[192:195], v210 offset:0\n\t" \
  "s_add_u32 m0, %[lw], 66560\n\t" \
  "v_mfma_f32_16x16x32_bf16 v[148:151], v[196:199], v[112:115], v[148:151]\n\t" \
  "v_mfma_f32_16x16x32_bf16 v[156:159], v[196:199], v[116:119], v[156:159]\n\t" \
  "ds_read_b128 v[196:199], v210 offset:16384\n\t" \
  "global_load_lds_dwordx4 v217, s[92:93] sc1\n\t" \
  "s_waitcnt lgkmcnt(6)\n\t" \
  "v_mfma_f32_16x16x32_bf16 v[144:147], v[200:203], v[120:123], v[144:147]\n\t" \
  "v_mfma_f32_16x16x32_bf16 v[152:155], v[200:203], v[124:127], v[152:155]\n\t" \
  "ds_read_b128 v[200:203], v211 offset:0\n\t" \
  "v_mfma_f32_16x16x32_bf16 v[148:151], v[204:207], v[120:123], v[148:151]\n\t" \
  "v_mfma_f32_16x16x32_bf16 v[156:159], v[204:207], v[124:127], v[156:159]\n\t" \
  "ds_read_b128 v[204:207], v211 offset:16384\n\t" \
  "s_add_u32 s86, s86, 1\n\t" \
  "s_add_u32 s87, s87, 1\n\t" \
  "s_cmp_lt_u32 s86, %[end]\n\t" \
  "s_cbranch_scc1 .Lp0%=\n\t" \
  "s_branch .Lx0%=\n\t" \
  ".Lf00%=:\n\t" \
  "s_nop 7\n\t" \
  "ds_write_b128 v220, v[152:155] offset:0\n\t" \
  "ds_write_b128 v220, v[156:159] offset:1024\n\t" \
  "s_lshr_b32 s90, s87, 7\n\t" \
  "s_xor_b32 s90, s90, %[s0]\n\t" \
  "s_and_b32 s90, s90, 1\n\t" \
  "s_lshl1_add_u32 s90, s87, s90\n\t" \
  "s_lshl_b32 s90, s90, 13\n\t" \
  "s_cmp_lt_i32 s86, 1\n\t" \
  "s_cselect_b32 s90, 0x70000000, s90\n\t" \
  "s_waitcnt lgkmcnt(0)\n\t" \
  "s_barrier\n\t" \
  "ds_read_b128 v[160:163], v221 offset:0\n\t" \
  "ds_read_b128 v[164:167], v221 offset:1024\n\t" \
  "s_waitcnt lgkmcnt(0)\n\t" \
  "v_add_f32 v160, v160, v144\n\t" \
  "v_add_f32 v161, v161, v145\n\t" \
  "v_add_f32 v162, v162, v146\n\t" \
  "v_add_f32 v163, v163, v147\n\t" \
  "v_add_f32 v164, v164, v148\n\t" \
  "v_add_f32 v165, v165, v149\n\t" \
  "v_add_f32 v166, v166, v150\n\t" \
  "v_add_f32 v167, v167, v151\n\t" \
  "v_cvt_pk_bf16_f32 v168, v160, v161\n\t" \
  "v_cvt_pk_bf16_f32 v169, v162, v163\n\t" \
  "v_cvt_pk_bf16_f32 v170, v164, v165\n\t" \
  "v_cvt_pk_bf16_f32 v171, v166, v167\n\t" \
  "buffer_store_dwordx4 v[168:171], v172, %[rt], s90 offen sc1\n\t" \
  "s_branch .Lfb00%=\n\t" \
  ".Lf10%=:\n\t" \
  "s_nop 7\n\t" \
  "ds_write_b128 v220, v[136:139] offset:0\n\t" \
  "ds_write_b128 v220, v[140:143] offset:1024\n\t" \
  "s_lshr_b32 s90, s87, 7\n\t" \
  "s_xor_b32 s90, s90, %[s0]\n\t" \
  "s_and_b32 s90, s90, 1\n\t" \
  "s_lshl1_add_u32 s90, s87, s90\n\t" \
  "s_lshl_b32 s90, s90, 13\n\t" \
  "s_cmp_lt_i32 s86, 1\n\t" \
  "s_cselect_b32 s90, 0x70000000, s90\n\t" \
  "s_waitcnt lgkmcnt(0)\n\t" \
  "s_barrier\n\t" \
  "ds_read_b128 v[160:163], v221 offset:0\n\t" \
  "ds_read_b128 v[164:167], v221 offset:1024\n\t" \
  "s_waitcnt lgkmcnt(0)\n\t" \
  "v_add_f32 v160, v160, v128\n\t" \
  "v_add_f32 v161, v161, v129\n\t" \
  "v_add_f32 v162, v162, v130\n\t" \
  "v_add_f32 v163, v163, v131\n\t" \
  "v_add_f32 v164, v164, v132\n\t" \
  "v_add_f32 v165, v165, v133\n\t" \
  "v_add_f32 v166, v166, v134\n\t" \
  "v_add_f32 v167, v167, v135\n\t" \
  "v_cvt_pk_bf16_f32 v168, v160, v161\n\t" \
  "v_cvt_pk_bf16_f32 v169, v162, v163\n\t" \
  "v_cvt_pk_bf16_f32 v170, v164, v165\n\t" \
  "v_cvt_pk_bf16_f32 v171, v166, v167\n\t" \
  "buffer_store_dwordx4 v[168:171], v172, %[rt], s90 offen sc1\n\t" \
  "s_branch .Lfb10%=\n\t" \
  ".Lw00%=:\n\t" \
  "s_waitcnt vmcnt(2)\n\t" \
  "s_barrier\n\t" \
  "s_branch .Lg00%=\n\t" \
  ".Lw10%=:\n\t" \
  "s_waitcnt vmcnt(2)\n\t" \
  "s_barrier\n\t" \
  "s_branch .Lg10%=\n\t" \
  ".Lw20%=:\n\t" \
  "s_waitcnt vmcnt(2)\n\t" \
  "s_barrier\n\t" \
  "s_branch .Lg20%=\n\t" \
  ".Lw30%=:\n\t" \
  "s_waitcnt vmcnt(2)\n\t" \
  "s_barrier\n\t" \
  "s_branch .Lg30%=\n\t" \
  ".Lx0%=:\n\t" \
  "s_waitcnt lgkmcnt(0)\n\t" \
  "s_setprio 0\n\t" \
  "s_mov_b32 m0, s94\n\t" \
  "s_branch .Le%=\n\t" \
  ".Lk%=:\n\t" \
  "s_mov_b32 s94, m0\n\t" \
  "s_mov_b64 s[92:93], %[x0]\n\t" \
  "s_mov_b32 s86, %[g0]\n\t" \
  "s_add_u32 s87, s86, %[t0]\n\t" \
  "s_sub_i32 s88, s86, 3\n\t" \
  "ds_read_b128 v[176:179], v208 offset:0\n\t" \
  "ds_read_b128 v[180:183], v208 offset:16384\n\t" \
  "ds_read_b128 v[184:187], v209 offset:0\n\t" \
  "ds_read_b128 v[188:191], v209 offset:16384\n\t" \
  "ds_read_b128 v[192:195], v210 offset:0\n\t" \
  "ds_read_b128 v[196:199], v210 offset:16384\n\t" \
  "ds_read_b128 v[200:203], v211 offset:0\n\t" \
  "ds_read_b128 v[204:207], v211 offset:16384\n\t" \
  "s_branch .Lfb01%=\n\t" \
  ".Lp1%=:\n\t" \
  "s_cmp_le_u32 s86, 5\n\t" \
  "s_cbranch_scc1 .Lf01%=\n\t" \
  ".Lfb01%=:\n\t" \
  "s_waitcnt lgkmcnt(6)\n\t" \
  "v_mfma_f32_16x16x32_bf16 v[128:131], v[176:179], v[0:3], 0\n\t" \
  "v_mfma_f32_16x16x32_bf16 v[136:139], v[176:179], v[4:7], 0\n\t" \
  "ds_read_b128 v[176:179], v208 offset:256\n\t" \
  "v_mfma_f32_16x16x32_bf16 v[132:135], v[180:183], v[0:3], 0\n\t" \
  "v_mfma_f32_16x16x32_bf16 v[140:143], v[180:183], v[4:7], 0\n\t" \
  "ds_read_b128 v[180:183], v208 offset:16640\n\t" \
  "s_waitcnt lgkmcnt(6)\n\t" \
  "v_mfma_f32_16x16x32_bf16 v[128:131], v[184:187], v[8:11], v[128:131]\n\t" \
  "v_mfma_f32_16x16x32_bf16 v[136:139], v[184:187], v[12:15], v[136:139]\n\t" \
  "ds_read_b128 v[184:187], v209 offset:256\n\t" \
  "s_add_u32 m0, %[lw], 67584\n\t" \
  "v_mfma_f32_16x16x32_bf16 v[132:135], v[188:191], v[8:11], v[132:135]\n\t" \
  "v_mfma_f32_16x16x32_bf16 v[140:143], v[188:191], v[12:15], v[140:143]\n\t" \
  "ds_read_b128 v[188:191], v209 offset:16640\n\t" \
  "global_load_lds_dwordx4 v218, s[92:93] sc1\n\t" \
  "ds_write_b128 v220, v[152:155] offset:0\n\t" \
  "ds_write_b128 v220, v[156:159] offset:1024\n\t" \
  "s_waitcnt lgkmcnt(8)\n\t" \
  "v_mfma_f32_16x16x32_bf16 v[128:131], v[192:195], v[16:19], v[128:131]\n\t" \
  "v_mfma_f32_16x16x32_bf16 v[136:139], v[192:195], v[20:23], v[136:139]\n\t" \
  "ds_read_b128 v[192:195], v210 offset:256\n\t" \
  "v_mfma_f32_16x16x32_bf16 v[132:135], v[196:199], v[16:19], v[132:135]\n\t" \
  "v_mfma_f32_16x16x32_bf16 v[140:143], v[196:199], v[20:23], v[140:143]\n\t" \
  "ds_read_b128 v[196:199], v210 offset:16640\n\t" \
  "s_lshr_b32 s90, s87, 7\n\t" \
  "s_xor_b32 s90, s90, %[s0]\n\t" \
  "s_and_b32 s90, s90, 1\n\t" \
  "s_lshl1_add_u32 s90, s87, s90\n\t" \
  "s_lshl_b32 s90, s90, 13\n\t" \
  "s_cmp_lt_i32 s86, 6\n\t" \
  "s_cselect_b32 s90, 0x70000000, s90\n\t" \
  "s_waitcnt lgkmcnt(8)\n\t" \
  "v_mfma_f32_16x16x32_bf16 v[128:131], v[200:203], v[24:27], v[128:131]\n\t" \
  "v_mfma_f32_16x16x32_bf16 v[136:139], v[200:203], v[28:31], v[136:139]\n\t" \
  "ds_read_b128 v[200:203], v211 offset:256\n\t" \
  "s_add_u32 m0, %[lw], 68608\n\t" \
  "v_mfma_f32_16x16x32_bf16 v[132:135], v[204:207], v[24:27], v[132:135]\n\t" \
  "v_mfma_f32_16x16x32_bf16 v[140:143], v[204:207], v[28:31], v[140:143]\n\t" \
  "ds_read_b128 v[204:207], v211 offset:16640\n\t" \
  "global_load_lds_dwordx4 v219, s[92:93] sc1\n\t" \
  "s_waitcnt lgkmcnt(4)\n\t" \
  "s_cmp_lt_u32 s86, 6\n\t" \
  "s_cbranch_scc1 .Lw01%=\n\t" \
  "s_waitcnt vmcnt(4)\n\t" \
  "s_barrier\n\t" \
  ".Lg01%=:\n\t" \
  "s_add_u32 s92, s92, 512\n\t" \
  "s_addc_u32 s93, s93, 0\n\t" \
  "v_mfma_f32_16x16x32_bf16 v[128:131], v[176:179], v[32:35], v[128:131]\n\t" \
  "v_mfma_f32_16x16x32_bf16 v[136:139], v[176:179], v[36:39], v[136:139]\n\t" \
  "ds_read_b128 v[176:179], v208 offset:32768\n\t" \
  "v_mfma_f32_16x16x32_bf16 v[132:135], v[180:183], v[32:35], v[132:135]\n\t" \
  "v_mfma_f32_16x16x32_bf16 v[140:143], v[180:183], v[36:39], v[140:143]\n\t" \
  "ds_read_b128 v[180:183], v208 offset:49152\n\t" \
  "ds_read_b128 v[160:163], v221 offset:0\n\t" \
  "ds_read_b128 v[164:167], v221 offset:1024\n\t" \
  "s_sub_i32 s91, s86, 2\n\t" \
  "s_max_i32 s88, s88, s91\n\t" \
  "v_mov_b32 v173, s88\n\t" \
  "buffer_store_dword v173, v174, %[rs], 0 offen sc1\n\t" \
  "v_mfma_f32_16x16x32_bf16 v[128:131], v[184:187], v[40:43], v[128:131]\n\t" \
  "v_mfma_f32_16x16x32_bf16 v[136:139], v[184:187], v[44:47], v[136:139]\n\t" \
  "ds_read_b128 v[184:187], v209 offset:32768\n\t" \
  "s_add_u32 m0, %[lw], 98304\n\t" \
  "v_mfma_f32_16x16x32_bf16 v[132:135], v[188:191], v[40:43], v[132:135]\n\t" \
  "v_mfma_f32_16x16x32_bf16 v[140:143], v[188:191], v[44:47], v[140:143]\n\t" \
  "ds_read_b128 v[188:191], v209 offset:49152\n\t" \
  "global_load_lds_dwordx4 v216, s[92:93] sc1\n\t" \
  "s_waitcnt lgkmcnt(8)\n\t" \
  "v_mfma_f32_16x16x32_bf16 v[128:131], v[192:195], v[48:51], v[128:131]\n\t" \
  "v_mfma_f32_16x16x32_bf16 v[136:139], v[192:195], v[52:55], v[136:139]\n\t" \
  "ds_read_b128 v[192:195], v210 offset:32768\n\t" \
  "v_mfma_f32_16x16x32_bf16 v[132:135], v[196:199], v[48:51], v[132:135]\n\t" \
  "v_mfma_f32_16x16x32_bf16 v[140:143], v[196:199], v[52:55], v[140:143]\n\t" \
  "ds_read_b128 v[196:199], v210 offset:49152\n\t" \
  "s_waitcnt lgkmcnt(4)\n\t" \
  "v_add_f32 v160, v160, v144\n\t" \
  "v_add_f32 v161, v161, v145\n\t" \
  "v_add_f32 v162, v162, v146\n\t" \
  "v_add_f32 v163, v163, v147\n\t" \
  "v_add_f32 v164, v164, v148\n\t" \
  "v_add_f32 v165, v165, v149\n\t" \
  "v_add_f32 v166, v166, v150\n\t" \
  "v_add_f32 v167, v167, v151\n\t" \
  "v_mfma_f32_16x16x32_bf16 v[128:131], v[200:203], v[56:59], v[128:131]\n\t" \
  "v_mfma_f32_16x16x32_bf16 v[136:139], v[200:203], v[60:63], v[136:139]\n\t" \
  "ds_read_b128 v[200:203], v211 offset:32768\n\t" \
  "s_add_u32 m0, %[lw], 99328\n\t" \
  "v_mfma_f32_16x16x32_bf16 v[132:135], v[204:207], v[56:59], v[132:135]\n\t" \
  "v_mfma_f32_16x16x32_bf16 v[140:143], v[204:207], v[60:63], v[140:143]\n\t" \
  "ds_read_b128 v[204:207], v211 offset:49152\n\t" \
  "global_load_lds_dwordx4 v217, s[92:93] sc1\n\t" \
  "v_cvt_pk_bf16_f32 v168, v160, v161\n\t" \
  "v_cvt_pk_bf16_f32 v169, v162, v163\n\t" \
  "v_cvt_pk_bf16_f32 v170, v164, v165\n\t" \
  "v_cvt_pk_bf16_f32 v171, v166, v167\n\t" \
  "buffer_store_dwordx4 v[168:171], v172, %[rt], s90 offen sc1\n\t" \
  "v_mfma_f32_16x16x32_bf16 v[128:131], v[176:179], v[64:67], v[128:131]\n\t" \
  "v_mfma_f32_16x16x32_bf16 v[136:139], v[176:179], v[68:71], v[136:139]\n\t" \
  "ds_read_b128 v[176:179], v208 offset:33024\n\t" \
  "v_mfma_f32_16x16x32_bf16 v[132:135], v[180:183], v[64:67], v[132:135]\n\t" \
  "v_mfma_f32_16x16x32_bf16 v[140:143], v[180:183], v[68:71], v[140:143]\n\t" \
  "ds_read_b128 v[180:183], v208 offset:49408\n\t" \
  "s_waitcnt lgkmcnt(6)\n\t" \
  "v_mfma_f32_16x16x32_bf16 v[128:131], v[184:187], v[72:75], v[128:131]\n\t" \
  "v_mfma_f32_16x16x32_bf16 v[136:139], v[184:187], v[76:79], v[136:139]\n\t" \
  "ds_read_b128 v[184:187], v209 offset:33024\n\t" \
  "s_add_u32 m0, %[lw], 100352\n\t" \
  "v_mfma_f32_16x16x32_bf16 v[132:135], v[188:191], v[72:75], v[132:135]\n\t" \
  "v_mfma_f32_16x16x32_bf16 v[140:143], v[188:191], v[76:79], v[140:143]\n\t" \
  "ds_read_b128 v[188:191], v209 offset:49408\n\t" \
  "global_load_lds_dwordx4 v218, s[92:93] sc1\n\t" \
  "s_waitcnt lgkmcnt(6)\n\t" \
  "v_mfma_f32_16x16x32_bf16 v[128:131], v[192:195], v[80:83], v[128:131]\n\t" \
  "v_mfma_f32_16x16x32_bf16 v[136:139], v[192:195], v[84:87], v[136:139]\n\t" \
  "ds_read_b128 v[192:195], v210 offset:33024\n\t" \
  "v_mfma_f32_16x16x32_bf16 v[132:135], v[196:199], v[80:83], v[132:135]\n\t" \
  "v_mfma_f32_16x16x32_bf16 v[140:143], v[196:199], v[84:87], v[140:143]\n\t" \
  "ds_read_b128 v[196:199], v210 offset:49408\n\t" \
  "s_waitcnt lgkmcnt(6)\n\t" \
  "v_mfma_f32_16x16x32_bf16 v[128:131], v[200:203], v[88:91], v[128:131]\n\t" \
  "v_mfma_f32_16x16x32_bf16 v[136:139], v[200:203], v[92:95], v[136:139]\n\t" \
  "ds_read_b128 v[200:203], v211 offset:33024\n\t" \
  "s_add_u32 m0, %[lw], 101376\n\t" \
  "v_mfma_f32_16x16x32_bf16 v[132:135], v[204:207], v[88:91], v[132:135]\n\t" \
  "v_mfma_f32_16x16x32_bf16 v[140:143], v[204:207], v[92:95], v[140:143]\n\t" \
  "ds_read_b128 v[204:207], v211 offset:49408\n\t" \
  "global_load_lds_dwordx4 v219, s[92:93] sc1\n\t" \
  "s_cmp_lt_u32 s86, 8\n\t" \
  "s_cbranch_scc1 .Lw11%=\n\t" \
  "s_waitcnt vmcnt(6)\n\t" \
  "s_barrier\n\t" \
  ".Lg11%=:\n\t" \
  "s_add_u32 s92, s92, 65024\n\t" \
  "s_addc_u32 s93, s93, 0\n\t" \
  "s_waitcnt lgkmcnt(6)\n\t" \
  "v_mfma_f32_16x16x32_bf16 v[128:131], v[176:179], v[96:99], v[128:131]\n\t" \
  "v_mfma_f32_16x16x32_bf16 v[136:139], v[176:179], v[100:103], v[136:139]\n\t" \
  "ds_read_b128 v[176:179], v212 offset:0\n\t" \
  "v_mfma_f32_16x16x32_bf16 v[132:135], v[180:183], v[96:99], v[132:135]\n\t" \
  "v_mfma_f32_16x16x32_bf16 v[140:143], v[180:183], v[100:103], v[140:143]\n\t" \
  "ds_read_b128 v[180:183], v212 offset:16384\n\t" \
  "s_waitcnt lgkmcnt(6)\n\t" \
  "v_mfma_f32_16x16x32_bf16 v[128:131], v[184:187], v[104:107], v[128:131]\n\t" \
  "v_mfma_f32_16x16x32_bf16 v[136:139], v[184:187], v[108:111], v[136:139]\n\t" \
  "ds_read_b128 v[184:187], v213 offset:0\n\t" \
  "s_add_u32 m0, %[lw], 0\n\t" \
  "v_mfma_f32_16x16x32_bf16 v[132:135], v[188:191], v[104:107], v[132:135]\n\t" \
  "v_mfma_f32_16x16x32_bf16 v[140:143], v[188:191], v[108:111], v[140:143]\n\t" \
  "ds_read_b128 v[188:191], v213 offset:16384\n\t" \
  "global_load_lds_dwordx4 v216, s[92:93] sc1\n\t" \
  "s_waitcnt lgkmcnt(6)\n\t" \
  "v_mfma_f32_16x16x32_bf16 v[128:131], v[192:195], v[112:115], v[128:131]\n\t" \
  "v_mfma_f32_16x16x32_bf16 v[136:139], v[192:195], v[116:119], v[136:139]\n\t" \
  "ds_read_b128 v[192:195], v214 offset:0\n\t" \
  "v_mfma_f32_16x16x32_bf16 v[132:135], v[196:199], v[112:115], v[132:135]\n\t" \
  "v_mfma_f32_16x16x32_bf16 v[140:143], v[196:199], v[116:119], v[140:143]\n\t" \
  "ds_read_b128 v[196:199], v214 offset:16384\n\t" \
  "s_waitcnt lgkmcnt(6)\n\t" \
  "v_mfma_f32_16x16x32_bf16 v[128:131], v[200:203], v[120:123], v[128:131]\n\t" \
  "v_mfma_f32_16x16x32_bf16 v[136:139], v[200:203], v[124:127], v[136:139]\n\t" \
  "ds_read_b128 v[200:203], v215 offset:0\n\t" \
  "s_add_u32 m0, %[lw], 1024\n\t" \
  "v_mfma_f32_16x16x32_bf16 v[132:135], v[204:207], v[120:123], v[132:135]\n\t" \
  "v_mfma_f32_16x16x32_bf16 v[140:143], v[204:207], v[124:127], v[140:143]\n\t" \
  "ds_read_b128 v[204:207], v215 offset:16384\n\t" \
  "global_load_lds_dwordx4 v217, s[92:93] sc1\n\t" \
  "s_add_u32 s86, s86, 1\n\t" \
  "s_add_u32 s87, s87, 1\n\t" \
  "s_cmp_le_u32 s86, 5\n\t" \
  "s_cbranch_scc1 .Lf11%=\n\t" \
  ".Lfb11%=:\n\t" \
  "s_waitcnt lgkmcnt(6)\n\t" \
  "v_mfma_f32_16x16x32_bf16 v[144:147], v[176:179], v[0:3], 0\n\t" \
  "v_mfma_f32_16x16x32_bf16 v[152:155], v[176:179], v[4:7], 0\n\t" \
  "ds_read_b128 v[176:179], v212 offset:256\n\t" \
  "v_mfma_f32_16x16x32_bf16 v[148:151], v[180:183], v[0:3], 0\n\t" \
  "v_mfma_f32_16x16x32_bf16 v[156:159], v[180:183], v[4:7], 0\n\t" \
  "ds_read_b128 v[180:183], v212 offset:16640\n\t" \
  "s_waitcnt lgkmcnt(6)\n\t" \
  "v_mfma_f32_16x16x32_bf16 v[144:147], v[184:187], v[8:11], v[144:147]\n\t" \
  "v_mfma_f32_16x16x32_bf16 v[152:155], v[184:187], v[12:15], v[152:155]\n\t" \
  "ds_read_b128 v[184:187], v213 offset:256\n\t" \
  "s_add_u32 m0, %[lw], 2048\n\t" \
  "v_mfma_f32_16x16x32_bf16 v[148:151], v[188:191], v[8:11], v[148:151]\n\t" \
  "v_mfma_f32_16x16x32_bf16 v[156:159], v[188:191], v[12:15], v[156:159]\n\t" \
  "ds_read_b128 v[188:191], v213 offset:16640\n\t" \
  "global_load_lds_dwordx4 v218, s[92:93] sc1\n\t" \
  "ds_write_b128 v220, v[136:139] offset:0\n\t" \
  "ds_write_b128 v220, v[140:143] offset:1024\n\t" \
  "s_waitcnt lgkmcnt(8)\n\t" \
  "v_mfma_f32_16x16x32_bf16 v[144:147], v[192:195], v[16:19], v[144:147]\n\t" \
  "v_mfma_f32_16x16x32_bf16 v[152:155], v[192:195], v[20:23], v[152:155]\n\t" \
  "ds_read_b128 v[192:195], v214 offset:256\n\t" \
  "v_mfma_f32_16x16x32_bf16 v[148:151], v[196:199], v[16:19], v[148:151]\n\t" \
  "v_mfma_f32_16x16x32_bf16 v[156:159], v[196:199], v[20:23], v[156:159]\n\t" \
  "ds_read_b128 v[196:199], v214 offset:16640\n\t" \
  "s_lshr_b32 s90, s87, 7\n\t" \
  "s_xor_b32 s90, s90, %[s0]\n\t" \
  "s_and_b32 s90, s90, 1\n\t" \
  "s_lshl1_add_u32 s90, s87, s90\n\t" \
  "s_lshl_b32 s90, s90, 13\n\t" \
  "s_cmp_lt_i32 s86, 6\n\t" \
  "s_cselect_b32 s90, 0x70000000, s90\n\t" \
  "s_waitcnt lgkmcnt(8)\n\t" \
  "v_mfma_f32_16x16x32_bf16 v[144:147], v[200:203], v[24:27], v[144:147]\n\t" \
  "v_mfma_f32_16x16x32_bf16 v[152:155], v[200:203], v[28:31], v[152:155]\n\t" \
  "ds_read_b128 v[200:203], v215 offset:256\n\t" \
  "s_add_u32 m0, %[lw], 3072\n\t" \
  "v_mfma_f32_16x16x32_bf16 v[148:151], v[204:207], v[24:27], v[148:151]\n\t" \
  "v_mfma_f32_16x16x32_bf16 v[156:159], v[204:207], v[28:31], v[156:159]\n\t" \
  "ds_read_b128 v[204:207], v215 offset:16640\n\t" \
  "global_load_lds_dwordx4 v219, s[92:93] sc1\n\t" \
  "s_waitcnt lgkmcnt(4)\n\t" \
  "s_cmp_lt_u32 s86, 6\n\t" \
  "s_cbranch_scc1 .Lw21%=\n\t" \
  "s_waitcnt vmcnt(4)\n\t" \
  "s_barrier\n\t" \
  ".Lg21%=:\n\t" \
  "s_add_u32 s92, s92, 512\n\t" \
  "s_addc_u32 s93, s93, 0\n\t" \
  "v_mfma_f32_16x16x32_bf16 v[144:147], v[176:179], v[32:35], v[144:147]\n\t" \
  "v_mfma_f32_16x16x32_bf16 v[152:155], v[176:179], v[36:39], v[152:155]\n\t" \
  "ds_read_b128 v[176:179], v212 offset:32768\n\t" \
  "v_mfma_f32_16x16x32_bf16 v[148:151], v[180:183], v[32:35], v[148:151]\n\t" \
  "v_mfma_f32_16x16x32_bf16 v[156:159], v[180:183], v[36:39], v[156:159]\n\t" \
  "ds_read_b128 v[180:183], v212 offset:49152\n\t" \
  "ds_read_b128 v[160:163], v221 offset:0\n\t" \
  "ds_read_b128 v[164:167], v221 offset:1024\n\t" \
  "s_sub_i32 s91, s86, 2\n\t" \
  "s_max_i32 s88, s88, s91\n\t" \
  "v_mov_b32 v173, s88\n\t" \
  "buffer_store_dword v173, v174, %[rs], 0 offen sc1\n\t" \
  "v_mfma_f32_16x16x32_bf16 v[144:147], v[184:187], v[40:43], v[144:147]\n\t" \
  "v_mfma_f32_16x16x32_bf16 v[152:155], v[184:187], v[44:47], v[152:155]\n\t" \
  "ds_read_b128 v[184:187], v213 offset:32768\n\t" \
  "s_add_u32 m0, %[lw], 32768\n\t" \
  "v_mfma_f32_16x16x32_bf16 v[148:151], v[188:191], v[40:43], v[148:151]\n\t" \
  "v_mfma_f32_16x16x32_bf16 v[156:159], v[188:191], v[44:47], v[156:159]\n\t" \
  "ds_read_b128 v[188:191], v213 offset:49152\n\t" \
  "global_load_lds_dwordx4 v216, s[92:93] sc1\n\t" \
  "s_waitcnt lgkmcnt(8)\n\t" \
  "v_mfma_f32_16x16x32_bf16 v[144:147], v[192:195], v[48:51], v[144:147]\n\t" \
  "v_mfma_f32_16x16x32_bf16 v[152:155], v[192:195], v[52:55], v[152:155]\n\t" \
  "ds_read_b128 v[192:195], v214 offset:32768\n\t" \
  "v_mfma_f32_16x16x32_bf16 v[148:151], v[196:199], v[48:51], v[148:151]\n\t" \
  "v_mfma_f32_16x16x32_bf16 v[156:159], v[196:199], v[52:55], v[156:159]\n\t" \
  "ds_read_b128 v[196:199], v214 offset:49152\n\t" \
  "s_waitcnt lgkmcnt(4)\n\t" \
  "v_add_f32 v160, v160, v128\n\t" \
  "v_add_f32 v161, v161, v129\n\t" \
  "v_add_f32 v162, v162, v130\n\t" \
  "v_add_f32 v163, v163, v131\n\t" \
  "v_add_f32 v164, v164, v132\n\t" \
  "v_add_f32 v165, v165, v133\n\t" \
  "v_add_f32 v166, v166, v134\n\t" \
  "v_add_f32 v167, v167, v135\n\t" \
  "v_mfma_f32_16x16x32_bf16 v[144:147], v[200:203], v[56:59], v[144:147]\n\t" \
  "v_mfma_f32_16x16x32_bf16 v[152:155], v[200:203], v[60:63], v[152:155]\n\t" \
  "ds_read_b128 v[200:203], v215 offset:32768\n\t" \
  "s_add_u32 m0, %[lw], 33792\n\t" \
  "v_mfma_f32_16x16x32_bf16 v[148:151], v[204:207], v[56:59], v[148:151]\n\t" \
  "v_mfma_f32_16x16x32_bf16 v[156:159], v[204:207], v[60:63], v[156:159]\n\t" \
  "ds_read_b128 v[204:207], v215 offset:49152\n\t" \
  "global_load_lds_dwordx4 v217, s[92:93] sc1\n\t" \
  "v_cvt_pk_bf16_f32 v168, v160, v161\n\t" \
  "v_cvt_pk_bf16_f32 v169, v162, v163\n\t" \
  "v_cvt_pk_bf16_f32 v170, v164, v165\n\t" \
  "v_cvt_pk_bf16_f32 v171, v166, v167\n\t" \
  "buffer_store_dwordx4 v[168:171], v172, %[rt], s90 offen sc1\n\t" \
  "v_mfma_f32_16x16x32_bf16 v[144:147], v[176:179], v[64:67], v[144:147]\n\t" \
  "v_mfma_f32_16x16x32_bf16 v[152:155], v[176:179], v[68:71], v[152:155]\n\t" \
  "ds_read_b128 v[176:179], v212 offset:33024\n\t" \
  "v_mfma_f32_16x16x32_bf16 v[148:151], v[180:183], v[64:67], v[148:151]\n\t" \
  "v_mfma_f32_16x16x32_bf16 v[156:159], v[180:183], v[68:71], v[156:159]\n\t" \
  "ds_read_b128 v[180:183], v212 offset:49408\n\t" \
  "s_waitcnt lgkmcnt(6)\n\t" \
  "v_mfma_f32_16x16x32_bf16 v[144:147], v[184:187], v[72:75], v[144:147]\n\t" \
  "v_mfma_f32_16x16x32_bf16 v[152:155], v[184:187], v[76:79], v[152:155]\n\t" \
  "ds_read_b128 v[184:187], v213 offset:33024\n\t" \
  "s_add_u32 m0, %[lw], 34816\n\t" \
  "v_mfma_f32_16x16x32_bf16 v[148:151], v[188:191], v[72:75], v[148:151]\n\t" \
  "v_mfma_f32_16x16x32_bf16 v[156:159], v[188:191], v[76:79], v[156:159]\n\t" \
  "ds_read_b128 v[188:191], v213 offset:49408\n\t" \
  "global_load_lds_dwordx4 v218, s[92:93] sc1\n\t" \
  "s_waitcnt lgkmcnt(6)\n\t" \
  "v_mfma_f32_16x16x32_bf16 v[144:147], v[192:195], v[80:83], v[144:147]\n\t" \
  "v_mfma_f32_16x16x32_bf16 v[152:155], v[192:195], v[84:87], v[152:155]\n\t" \
  "ds_read_b128 v[192:195], v214 offset:33024\n\t" \
  "v_mfma_f32_16x16x32_bf16 v[148:151], v[196:199], v[80:83], v[148:151]\n\t" \
  "v_mfma_f32_16x16x32_bf16 v[156:159], v[196:199], v[84:87], v[156:159]\n\t" \
  "ds_read_b128 v[196:199], v214 offset:49408\n\t" \
  "s_waitcnt lgkmcnt(6)\n\t" \
  "v_mfma_f32_16x16x32_bf16 v[144:147], v[200:203], v[88:91], v[144:147]\n\t" \
  "v_mfma_f32_16x16x32_bf16 v[152:155], v[200:203], v[92:95], v[152:155]\n\t" \
  "ds_read_b128 v[200:203], v215 offset:33024\n\t" \
  "s_add_u32 m0, %[lw], 35840\n\t" \
  "v_mfma_f32_16x16x32_bf16 v[148:151], v[204:207], v[88:91], v[148:151]\n\t" \
  "v_mfma_f32_16x16x32_bf16 v[156:159], v[204:207], v[92:95], v[156:159]\n\t" \
  "ds_read_b128 v[204:207], v215 offset:49408\n\t" \
  "global_load_lds_dwordx4 v219, s[92:93] sc1\n\t" \
  "s_cmp_lt_u32 s86, 8\n\t" \
  "s_cbranch_scc1 .Lw31%=\n\t" \
  "s_waitcnt vmcnt(6)\n\t" \
  "s_barrier\n\t" \
  ".Lg31%=:\n\t" \
  "s_add_u32 s92, s92, 65024\n\t" \
  "s_addc_u32 s93, s93, 0\n\t" \
  "s_waitcnt lgkmcnt(6)\n\t" \
  "v_mfma_f32_16x16x32_bf16 v[144:147], v[176:179], v[96:99], v[144:147]\n\t" \
  "v_mfma_f32_16x16x32_bf16 v[152:155], v[176:179], v[100:103], v[152:155]\n\t" \
  "ds_read_b128 v[176:179], v208 offset:0\n\t" \
  "v_mfma_f32_16x16x32_bf16 v[148:151], v[180:183], v[96:99], v[148:151]\n\t" \
  "v_mfma_f32_16x16x32_bf16 v[156:159], v[180:183], v[100:103], v[156:159]\n\t" \
  "ds_read_b128 v[180:183], v208 offset:16384\n\t" \
  "s_waitcnt lgkmcnt(6)\n\t" \
  "v_mfma_f32_16x16x32_bf16 v[144:147], v[184:187], v[104:107], v[144:147]\n\t" \
  "v_mfma_f32_16x16x32_bf16 v[152:155], v[184:187], v[108:111], v[152:155]\n\t" \
  "ds_read_b128 v[184:187], v209 offset:0\n\t" \
  "s_add_u32 m0, %[lw], 65536\n\t" \
  "v_mfma_f32_16x16x32_bf16 v[148:151], v[188:191], v[104:107], v[148:151]\n\t" \
  "v_mfma_f32_16x16x32_bf16 v[156:159], v[188:191], v[108:111], v[156:159]\n\t" \
  "ds_read_b128 v[188:191], v209 offset:16384\n\t" \
  "global_load_lds_dwordx4 v216, s[92:93] sc1\n\t" \
  "s_waitcnt lgkmcnt(6)\n\t" \
  "v_mfma_f32_16x16x32_bf16 v[144:147], v[192:195], v[112:115], v[144:147]\n\t" \
  "v_mfma_f32_16x16x32_bf16 v[152:155], v[192:195], v[116:119], v[152:155]\n\t" \
  "ds_read_b128 v[192:195], v210 offset:0\n\t" \
  "v_mfma_f32_16x16x32_bf16 v[148:151], v[196:199], v[112:115], v[148:151]\n\t" \
  "v_mfma_f32_16x16x32_bf16 v[156:159], v[196:199], v[116:119], v[156:159]\n\t" \
  "ds_read_b128 v[196:199], v210 offset:16384\n\t" \
  "s_waitcnt lgkmcnt(6)\n\t" \
  "v_mfma_f32_16x16x32_bf16 v[144:147], v[200:203], v[120:123], v[144:147]\n\t" \
  "v_mfma_f32_16x16x32_bf16 v[152:155], v[200:203], v[124:127], v[152:155]\n\t" \
  "ds_read_b128 v[200:203], v211 offset:0\n\t" \
  "s_add_u32 m0, %[lw], 66560\n\t" \
  "v_mfma_f32_16x16x32_bf16 v[148:151], v[204:207], v[120:123], v[148:151]\n\t" \
  "v_mfma_f32_16x16x32_bf16 v[156:159], v[204:207], v[124:127], v[156:159]\n\t" \
  "ds_read_b128 v[204:207], v211 offset:16384\n\t" \
  "global_load_lds_dwordx4 v217, s[92:93] sc1\n\t" \
  "s_add_u32 s86, s86, 1\n\t" \
  "s_add_u32 s87, s87, 1\n\t" \
  "s_cmp_lt_u32 s86, %[end]\n\t" \
  "s_cbranch_scc1 .Lp1%=\n\t" \
  "s_branch .Lx1%=\n\t" \
  ".Lf01%=:\n\t" \
  "s_nop 7\n\t" \
  "ds_write_b128 v220, v[152:155] offset:0\n\t" \
  "ds_write_b128 v220, v[156:159] offset:1024\n\t" \
  "s_lshr_b32 s90, s87, 7\n\t" \
  "s_xor_b32 s90, s90, %[s0]\n\t" \
  "s_and_b32 s90, s90, 1\n\t" \
  "s_lshl1_add_u32 s90, s87, s90\n\t" \
  "s_lshl_b32 s90, s90, 13\n\t" \
  "s_cmp_lt_i32 s86, 1\n\t" \
  "s_cselect_b32 s90, 0x70000000, s90\n\t" \
  "s_waitcnt lgkmcnt(0)\n\t" \
  "s_barrier\n\t" \
  "ds_read_b128 v[160:163], v221 offset:0\n\t" \
  "ds_read_b128 v[164:167], v221 offset:1024\n\t" \
  "s_waitcnt lgkmcnt(0)\n\t" \
  "v_add_f32 v160, v160, v144\n\t" \
  "v_add_f32 v161, v161, v145\n\t" \
  "v_add_f32 v162, v162, v146\n\t" \
  "v_add_f32 v163, v163, v147\n\t" \
  "v_add_f32 v164, v164, v148\n\t" \
  "v_add_f32 v165, v165, v149\n\t" \
  "v_add_f32 v166, v166, v150\n\t" \
  "v_add_f32 v167, v167, v151\n\t" \
  "v_cvt_pk_bf16_f32 v168, v160, v161\n\t" \
  "v_cvt_pk_bf16_f32 v169, v162, v163\n\t" \
  "v_cvt_pk_bf16_f32 v170, v164, v165\n\t" \
  "v_cvt_pk_bf16_f32 v171, v166, v167\n\t" \
  "buffer_store_dwordx4 v[168:171], v172, %[rt], s90 offen sc1\n\t" \
  "s_branch .Lfb01%=\n\t" \
  ".Lf11%=:\n\t" \
  "s_nop 7\n\t" \
  "ds_write_b128 v220, v[136:139] offset:0\n\t" \
  "ds_write_b128 v220, v[140:143] offset:1024\n\t" \
  "s_lshr_b32 s90, s87, 7\n\t" \
  "s_xor_b32 s90, s90, %[s0]\n\t" \
  "s_and_b32 s90, s90, 1\n\t" \
  "s_lshl1_add_u32 s90, s87, s90\n\t" \
  "s_lshl_b32 s90, s90, 13\n\t" \
  "s_cmp_lt_i32 s86, 1\n\t" \
  "s_cselect_b32 s90, 0x70000000, s90\n\t" \
  "s_waitcnt lgkmcnt(0)\n\t" \
  "s_barrier\n\t" \
  "ds_read_b128 v[160:163], v221 offset:0\n\t" \
  "ds_read_b128 v[164:167], v221 offset:1024\n\t" \
  "s_waitcnt lgkmcnt(0)\n\t" \
  "v_add_f32 v160, v160, v128\n\t" \
  "v_add_f32 v161, v161, v129\n\t" \
  "v_add_f32 v162, v162, v130\n\t" \
  "v_add_f32 v163, v163, v131\n\t" \
  "v_add_f32 v164, v164, v132\n\t" \
  "v_add_f32 v165, v165, v133\n\t" \
  "v_add_f32 v166, v166, v134\n\t" \
  "v_add_f32 v167, v167, v135\n\t" \
  "v_cvt_pk_bf16_f32 v168, v160, v161\n\t" \
  "v_cvt_pk_bf16_f32 v169, v162, v163\n\t" \
  "v_cvt_pk_bf16_f32 v170, v164, v165\n\t" \
  "v_cvt_pk_bf16_f32 v171, v166, v167\n\t" \
  "buffer_store_dwordx4 v[168:171], v172, %[rt], s90 offen sc1\n\t" \
  "s_branch .Lfb11%=\n\t" \
  ".Lw01%=:\n\t" \
  "s_waitcnt vmcnt(2)\n\t" \
  "s_barrier\n\t" \
  "s_sub_i32 s91, s86, 1\n\t" \
  "s_max_i32 s88, s88, s91\n\t" \
  "v_mov_b32 v173, s88\n\t" \
  "buffer_store_dword v173, v174, %[rs], 0 offen sc1\n\t" \
  "s_branch .Lg01%=\n\t" \
  ".Lw11%=:\n\t" \
  "s_waitcnt vmcnt(2)\n\t" \
  "s_barrier\n\t" \
  "s_sub_i32 s91, s86, 1\n\t" \
  "s_max_i32 s88, s88, s91\n\t" \
  "v_mov_b32 v173, s88\n\t" \
  "buffer_store_dword v173, v174, %[rs], 0 offen sc1\n\t" \
  "s_branch .Lg11%=\n\t" \
  ".Lw21%=:\n\t" \
  "s_waitcnt vmcnt(2)\n\t" \
  "s_barrier\n\t" \
  "s_sub_i32 s91, s86, 1\n\t" \
  "s_max_i32 s88, s88, s91\n\t" \
  "v_mov_b32 v173, s88\n\t" \
  "buffer_store_dword v173, v174, %[rs], 0 offen sc1\n\t" \
  "s_branch .Lg21%=\n\t" \
  ".Lw31%=:\n\t" \
  "s_waitcnt vmcnt(2)\n\t" \
  "s_barrier\n\t" \
  "s_sub_i32 s91, s86, 1\n\t" \
  "s_max_i32 s88, s88, s91\n\t" \
  "v_mov_b32 v173, s88\n\t" \
  "buffer_store_dword v173, v174, %[rs], 0 offen sc1\n\t" \
  "s_branch .Lg31%=\n\t" \
  ".Lx1%=:\n\t" \
  "s_waitcnt lgkmcnt(0)\n\t" \
  "s_setprio 0\n\t" \
  "s_mov_b32 m0, s94\n\t" \
  ".Le%=:\n\t"
#define PP_ASM_0 \
  "s_cmp_lg_u32 %[kh], 0\n\t" \
  "s_cbranch_scc1 .Lk%=\n\t" \
  "s_mov_b32 s94, m0\n\t" \
  "s_mov_b64 s[92:93], %[x0]\n\t" \
  "s_mov_b32 s86, %[g0]\n\t" \
  "s_add_u32 s87, s86, %[t0]\n\t" \
  "s_add_u32 m0, %[lw], 67584\n\t" \
  "s_nop 0\n\t" \
  "global_load_lds_dwordx4 v250, s[92:93] sc1\n\t" \
  "s_add_u32 m0, %[lw], 68608\n\t" \
  "s_nop 0\n\t" \
  "global_load_lds_dwordx4 v251, s[92:93] sc1\n\t" \
  "s_barrier\n\t" \
  "s_branch .Ls0%=\n\t" \
  ".Lp0%=:\n\t" \
  "s_waitcnt vmcnt(9)\n\t" \
  "s_barrier\n\t" \
  ".Ls0%=:\n\t" \
  "s_add_u32 s92, s92, 512\n\t" \
  "s_addc_u32 s93, s93, 0\n\t" \
  "ds_read_b128 v[176:179], v240 offset:0\n\t" \
  "ds_read_b128 v[180:183], v240 offset:16384\n\t" \
  "ds_read_b128 v[184:187], v241 offset:0\n\t" \
  "ds_read_b128 v[188:191], v241 offset:16384\n\t" \
  "s_add_u32 m0, %[lw], 98304\n\t" \
  "s_nop 0\n\t" \
  "global_load_lds_dwordx4 v248, s[92:93] sc1\n\t" \
  "ds_read_b128 v[192:195], v242 offset:0\n\t" \
  "ds_read_b128 v[196:199], v242 offset:16384\n\t" \
  "ds_read_b128 v[200:203], v243 offset:0\n\t" \
  "ds_read_b128 v[204:207], v243 offset:16384\n\t" \
  "s_add_u32 m0, %[lw], 99328\n\t" \
  "s_nop 0\n\t" \
  "global_load_lds_dwordx4 v249, s[92:93] sc1\n\t" \
  "ds_read_b128 v[208:211], v240 offset:256\n\t" \
  "ds_read_b128 v[212:215], v240 offset:16640\n\t" \
  "ds_read_b128 v[216:219], v241 offset:256\n\t" \
  "ds_read_b128 v[220:223], v241 offset:16640\n\t" \
  "s_add_u32 m0, %[lw], 100352\n\t" \
  "s_nop 0\n\t" \
  "global_load_lds_dwordx4 v250, s[92:93] sc1\n\t" \
  "ds_read_b128 v[224:227], v242 offset:256\n\t" \
  "ds_read_b128 v[228:231], v242 offset:16640\n\t" \
  "ds_read_b128 v[232:235], v243 offset:256\n\t" \
  "ds_read_b128 v[236:239], v243 offset:16640\n\t" \
  "s_add_u32 m0, %[lw], 101376\n\t" \
  "s_nop 0\n\t" \
  "global_load_lds_dwordx4 v251, s[92:93] sc1\n\t" \
  "ds_write_b128 v252, v[152:155] offset:0\n\t" \
  "ds_write_b128 v252, v[156:159] offset:1024\n\t" \
  "s_waitcnt lgkmcnt(0)\n\t" \
  "s_setprio 1\n\t" \
  "v_mfma_f32_16x16x32_bf16 v[128:131], v[0:3], v[176:179], 0\n\t" \
  "v_mfma_f32_16x16x32_bf16 v[132:135], v[4:7], v[176:179], 0\n\t" \
  "v_mfma_f32_16x16x32_bf16 v[136:139], v[0:3], v[180:183], 0\n\t" \
  "v_mfma_f32_16x16x32_bf16 v[140:143], v[4:7], v[180:183], 0\n\t" \
  "v_mfma_f32_16x16x32_bf16 v[128:131], v[8:11], v[184:187], v[128:131]\n\t" \
  "v_mfma_f32_16x16x32_bf16 v[132:135], v[12:15], v[184:187], v[132:135]\n\t" \
  "v_mfma_f32_16x16x32_bf16 v[136:139], v[8:11], v[188:191], v[136:139]\n\t" \
  "v_mfma_f32_16x16x32_bf16 v[140:143], v[12:15], v[188:191], v[140:143]\n\t" \
  "v_mfma_f32_16x16x32_bf16 v[128:131], v[16:19], v[192:195], v[128:131]\n\t" \
  "v_mfma_f32_16x16x32_bf16 v[132:135], v[20:23], v[192:195], v[132:135]\n\t" \
  "v_mfma_f32_16x16x32_bf16 v[136:139], v[16:19], v[196:199], v[136:139]\n\t" \
  "v_mfma_f32_16x16x32_bf16 v[140:143], v[20:23], v[196:199], v[140:143]\n\t" \
  "v_mfma_f32_16x16x32_bf16 v[128:131], v[24:27], v[200:203], v[128:131]\n\t" \
  "v_mfma_f32_16x16x32_bf16 v[132:135], v[28:31], v[200:203], v[132:135]\n\t" \
  "v_mfma_f32_16x16x32_bf16 v[136:139], v[24:27], v[204:207], v[136:139]\n\t" \
  "v_mfma_f32_16x16x32_bf16 v[140:143], v[28:31], v[204:207], v[140:143]\n\t" \
  "v_mfma_f32_16x16x32_bf16 v[128:131], v[32:35], v[208:211], v[128:131]\n\t" \
  "v_mfma_f32_16x16x32_bf16 v[132:135], v[36:39], v[208:211], v[132:135]\n\t" \
  "v_mfma_f32_16x16x32_bf16 v[136:139], v[32:35], v[212:215], v[136:139]\n\t" \
  "v_mfma_f32_16x16x32_bf16 v[140:143], v[36:39], v[212:215], v[140:143]\n\t" \
  "v_mfma_f32_16x16x32_bf16 v[128:131], v[40:43], v[216:219], v[128:131]\n\t" \
  "v_mfma_f32_16x16x32_bf16 v[132:135], v[44:47], v[216:219], v[132:135]\n\t" \
  "v_mfma_f32_16x16x32_bf16 v[136:139], v[40:43], v[220:223], v[136:139]\n\t" \
  "v_mfma_f32_16x16x32_bf16 v[140:143], v[44:47], v[220:223], v[140:143]\n\t" \
  "v_mfma_f32_16x16x32_bf16 v[128:131], v[48:51], v[224:227], v[128:131]\n\t" \
  "v_mfma_f32_16x16x32_bf16 v[132:135], v[52:55], v[224:227], v[132:135]\n\t" \
  "v_mfma_f32_16x16x32_bf16 v[136:139], v[48:51], v[228:231], v[136:139]\n\t" \
  "v_mfma_f32_16x16x32_bf16 v[140:143], v[52:55], v[228:231], v[140:143]\n\t" \
  "v_mfma_f32_16x16x32_bf16 v[128:131], v[56:59], v[232:235], v[128:131]\n\t" \
  "v_mfma_f32_16x16x32_bf16 v[132:135], v[60:63], v[232:235], v[132:135]\n\t" \
  "v_mfma_f32_16x16x32_bf16 v[136:139], v[56:59], v[236:239], v[136:139]\n\t" \
  "v_mfma_f32_16x16x32_bf16 v[140:143], v[60:63], v[236:239], v[140:143]\n\t" \
  "s_setprio 0\n\t" \
  "s_waitcnt vmcnt(4)\n\t" \
  "s_barrier\n\t" \
  "s_add_u32 s92, s92, 65024\n\t" \
  "s_addc_u32 s93, s93, 0\n\t" \
  "ds_read_b128 v[160:163], v253 offset:0\n\t" \
  "ds_read_b128 v[164:167], v253 offset:1024\n\t" \
  "ds_read_b128 v[176:179], v240 offset:32768\n\t" \
  "ds_read_b128 v[180:183], v240 offset:49152\n\t" \
  "ds_read_b128 v[184:187], v241 offset:32768\n\t" \
  "ds_read_b128 v[188:191], v241 offset:49152\n\t" \
  "s_add_u32 m0, %[lw], 0\n\t" \
  "s_nop 0\n\t" \
  "global_load_lds_dwordx4 v248, s[92:93] sc1\n\t" \
  "ds_read_b128 v[192:195], v242 offset:32768\n\t" \
  "ds_read_b128 v[196:199], v242 offset:49152\n\t" \
  "ds_read_b128 v[200:203], v243 offset:32768\n\t" \
  "ds_read_b128 v[204:207], v243 offset:49152\n\t" \
  "s_add_u32 m0, %[lw], 1024\n\t" \
  "s_nop 0\n\t" \
  "global_load_lds_dwordx4 v249, s[92:93] sc1\n\t" \
  "ds_read_b128 v[208:211], v240 offset:33024\n\t" \
  "ds_read_b128 v[212:215], v240 offset:49408\n\t" \
  "ds_read_b128 v[216:219], v241 offset:33024\n\t" \
  "ds_read_b128 v[220:223], v241 offset:49408\n\t" \
  "s_add_u32 m0, %[lw], 2048\n\t" \
  "s_nop 0\n\t" \
  "global_load_lds_dwordx4 v250, s[92:93] sc1\n\t" \
  "ds_read_b128 v[224:227], v242 offset:33024\n\t" \
  "ds_read_b128 v[228:231], v242 offset:49408\n\t" \
  "ds_read_b128 v[232:235], v243 offset:33024\n\t" \
  "ds_read_b128 v[236:239], v243 offset:49408\n\t" \
  "s_add_u32 m0, %[lw], 3072\n\t" \
  "s_nop 0\n\t" \
  "global_load_lds_dwordx4 v251, s[92:93] sc1\n\t" \
  "s_waitcnt lgkmcnt(15)\n\t" \
  "s_waitcnt lgkmcnt(12)\n\t" \
  "s_lshr_b32 s90, s87, 7\n\t" \
  "s_xor_b32 s90, s90, %[s0]\n\t" \
  "s_and_b32 s90, s90, 1\n\t" \
  "s_lshl1_add_u32 s90, s87, s90\n\t" \
  "s_lshl_b32 s90, s90, 13\n\t" \
  "v_add_f32 v160, v160, v144\n\t" \
  "v_add_f32 v161, v161, v145\n\t" \
  "v_add_f32 v162, v162, v146\n\t" \
  "v_add_f32 v163, v163, v147\n\t" \
  "v_add_f32 v164, v164, v148\n\t" \
  "v_add_f32 v165, v165, v149\n\t" \
  "v_add_f32 v166, v166, v150\n\t" \
  "v_add_f32 v167, v167, v151\n\t" \
  "v_cvt_pk_bf16_f32 v168, v160, v161\n\t" \
  "v_cvt_pk_bf16_f32 v169, v162, v163\n\t" \
  "v_cvt_pk_bf16_f32 v170, v164, v165\n\t" \
  "v_cvt_pk_bf16_f32 v171, v166, v167\n\t" \
  "buffer_store_dwordx4 v[168:171], v172, %[rt], s90 offen sc1\n\t" \
  "s_add_u32 s86, s86, 1\n\t" \
  "s_add_u32 s87, s87, 1\n\t" \
  "s_waitcnt lgkmcnt(0)\n\t" \
  "s_setprio 1\n\t" \
  "v_mfma_f32_16x16x32_bf16 v[128:131], v[64:67], v[176:179], v[128:131]\n\t" \
  "v_mfma_f32_16x16x32_bf16 v[132:135], v[68:71], v[176:179], v[132:135]\n\t" \
  "v_mfma_f32_16x16x32_bf16 v[136:139], v[64:67], v[180:183], v[136:139]\n\t" \
  "v_mfma_f32_16x16x32_bf16 v[140:143], v[68:71], v[180:183], v[140:143]\n\t" \
  "v_mfma_f32_16x16x32_bf16 v[128:131], v[72:75], v[184:187], v[128:131]\n\t" \
  "v_mfma_f32_16x16x32_bf16 v[132:135], v[76:79], v[184:187], v[132:135]\n\t" \
  "v_mfma_f32_16x16x32_bf16 v[136:139], v[72:75], v[188:191], v[136:139]\n\t" \
  "v_mfma_f32_16x16x32_bf16 v[140:143], v[76:79], v[188:191], v[140:143]\n\t" \
  "v_mfma_f32_16x16x32_bf16 v[128:131], v[80:83], v[192:195], v[128:131]\n\t" \
  "v_mfma_f32_16x16x32_bf16 v[132:135], v[84:87], v[192:195], v[132:135]\n\t" \
  "v_mfma_f32_16x16x32_bf16 v[136:139], v[80:83], v[196:199], v[136:139]\n\t" \
  "v_mfma_f32_16x16x32_bf16 v[140:143], v[84:87], v[196:199], v[140:143]\n\t" \
  "v_mfma_f32_16x16x32_bf16 v[128:131], v[88:91], v[200:203], v[128:131]\n\t" \
  "v_mfma_f32_16x16x32_bf16 v[132:135], v[92:95], v[200:203], v[132:135]\n\t" \
  "v_mfma_f32_16x16x32_bf16 v[136:139], v[88:91], v[204:207], v[136:139]\n\t" \
  "v_mfma_f32_16x16x32_bf16 v[140:143], v[92:95], v[204:207], v[140:143]\n\t" \
  "v_mfma_f32_16x16x32_bf16 v[128:131], v[96:99], v[208:211], v[128:131]\n\t" \
  "v_mfma_f32_16x16x32_bf16 v[132:135], v[100:103], v[208:211], v[132:135]\n\t" \
  "v_mfma_f32_16x16x32_bf16 v[136:139], v[96:99], v[212:215], v[136:139]\n\t" \
  "v_mfma_f32_16x16x32_bf16 v[140:143], v[100:103], v[212:215], v[140:143]\n\t" \
  "v_mfma_f32_16x16x32_bf16 v[128:131], v[104:107], v[216:219], v[128:131]\n\t" \
  "v_mfma_f32_16x16x32_bf16 v[132:135], v[108:111], v[216:219], v[132:135]\n\t" \
  "v_mfma_f32_16x16x32_bf16 v[136:139], v[104:107], v[220:223], v[136:139]\n\t" \
  "v_mfma_f32_16x16x32_bf16 v[140:143], v[108:111], v[220:223], v[140:143]\n\t" \
  "v_mfma_f32_16x16x32_bf16 v[128:131], v[112:115], v[224:227], v[128:131]\n\t" \
  "v_mfma_f32_16x16x32_bf16 v[132:135], v[116:119], v[224:227], v[132:135]\n\t" \
  "v_mfma_f32_16x16x32_bf16 v[136:139], v[112:115], v[228:231], v[136:139]\n\t" \
  "v_mfma_f32_16x16x32_bf16 v[140:143], v[116:119], v[228:231], v[140:143]\n\t" \
  "v_mfma_f32_16x16x32_bf16 v[128:131], v[120:123], v[232:235], v[128:131]\n\t" \
  "v_mfma_f32_16x16x32_bf16 v[132:135], v[124:127], v[232:235], v[132:135]\n\t" \
  "v_mfma_f32_16x16x32_bf16 v[136:139], v[120:123], v[236:239], v[136:139]\n\t" \
  "v_mfma_f32_16x16x32_bf16 v[140:143], v[124:127], v[236:239], v[140:143]\n\t" \
  "s_setprio 0\n\t" \
  "s_waitcnt vmcnt(9)\n\t" \
  "s_barrier\n\t" \
  "s_add_u32 s92, s92, 512\n\t" \
  "s_addc_u32 s93, s93, 0\n\t" \
  "ds_read_b128 v[176:179], v244 offset:0\n\t" \
  "ds_read_b128 v[180:183], v244 offset:16384\n\t" \
  "ds_read_b128 v[184:187], v245 offset:0\n\t" \
  "ds_read_b128 v[188:191], v245 offset:16384\n\t" \
  "s_add_u32 m0, %[lw], 32768\n\t" \
  "s_nop 0\n\t" \
  "global_load_lds_dwordx4 v248, s[92:93] sc1\n\t" \
  "ds_read_b128 v[192:195], v246 offset:0\n\t" \
  "ds_read_b128 v[196:199], v246 offset:16384\n\t" \
  "ds_read_b128 v[200:203], v247 offset:0\n\t" \
  "ds_read_b128 v[204:207], v247 offset:16384\n\t" \
  "s_add_u32 m0, %[lw], 33792\n\t" \
  "s_nop 0\n\t" \
  "global_load_lds_dwordx4 v249, s[92:93] sc1\n\t" \
  "ds_read_b128 v[208:211], v244 offset:256\n\t" \
  "ds_read_b128 v[212:215], v244 offset:16640\n\t" \
  "ds_read_b128 v[216:219], v245 offset:256\n\t" \
  "ds_read_b128 v[220:223], v245 offset:16640\n\t" \
  "s_add_u32 m0, %[lw], 34816\n\t" \
  "s_nop 0\n\t" \
  "global_load_lds_dwordx4 v250, s[92:93] sc1\n\t" \
  "ds_read_b128 v[224:227], v246 offset:256\n\t" \
  "ds_read_b128 v[228:231], v246 offset:16640\n\t" \
  "ds_read_b128 v[232:235], v247 offset:256\n\t" \
  "ds_read_b128 v[236:239], v247 offset:16640\n\t" \
  "s_add_u32 m0, %[lw], 35840\n\t" \
  "s_nop 0\n\t" \
  "global_load_lds_dwordx4 v251, s[92:93] sc1\n\t" \
  "ds_write_b128 v252, v[136:139] offset:0\n\t" \
  "ds_write_b128 v252, v[140:143] offset:1024\n\t" \
  "s_waitcnt lgkmcnt(0)\n\t" \
  "s_setprio 1\n\t" \
  "v_mfma_f32_16x16x32_bf16 v[144:147], v[0:3], v[176:179], 0\n\t" \
  "v_mfma_f32_16x16x32_bf16 v[148:151], v[4:7], v[176:179], 0\n\t" \
  "v_mfma_f32_16x16x32_bf16 v[152:155], v[0:3], v[180:183], 0\n\t" \
  "v_mfma_f32_16x16x32_bf16 v[156:159], v[4:7], v[180:183], 0\n\t" \
  "v_mfma_f32_16x16x32_bf16 v[144:147], v[8:11], v[184:187], v[144:147]\n\t" \
  "v_mfma_f32_16x16x32_bf16 v[148:151], v[12:15], v[184:187], v[148:151]\n\t" \
  "v_mfma_f32_16x16x32_bf16 v[152:155], v[8:11], v[188:191], v[152:155]\n\t" \
  "v_mfma_f32_16x16x32_bf16 v[156:159], v[12:15], v[188:191], v[156:159]\n\t" \
  "v_mfma_f32_16x16x32_bf16 v[144:147], v[16:19], v[192:195], v[144:147]\n\t" \
  "v_mfma_f32_16x16x32_bf16 v[148:151], v[20:23], v[192:195], v[148:151]\n\t" \
  "v_mfma_f32_16x16x32_bf16 v[152:155], v[16:19], v[196:199], v[152:155]\n\t" \
  "v_mfma_f32_16x16x32_bf16 v[156:159], v[20:23], v[196:199], v[156:159]\n\t" \
  "v_mfma_f32_16x16x32_bf16 v[144:147], v[24:27], v[200:203], v[144:147]\n\t" \
  "v_mfma_f32_16x16x32_bf16 v[148:151], v[28:31], v[200:203], v[148:151]\n\t" \
  "v_mfma_f32_16x16x32_bf16 v[152:155], v[24:27], v[204:207], v[152:155]\n\t" \
  "v_mfma_f32_16x16x32_bf16 v[156:159], v[28:31], v[204:207], v[156:159]\n\t" \
  "v_mfma_f32_16x16x32_bf16 v[144:147], v[32:35], v[208:211], v[144:147]\n\t" \
  "v_mfma_f32_16x16x32_bf16 v[148:151], v[36:39], v[208:211], v[148:151]\n\t" \
  "v_mfma_f32_16x16x32_bf16 v[152:155], v[32:35], v[212:215], v[152:155]\n\t" \
  "v_mfma_f32_16x16x32_bf16 v[156:159], v[36:39], v[212:215], v[156:159]\n\t" \
  "v_mfma_f32_16x16x32_bf16 v[144:147], v[40:43], v[216:219], v[144:147]\n\t" \
  "v_mfma_f32_16x16x32_bf16 v[148:151], v[44:47], v[216:219], v[148:151]\n\t" \
  "v_mfma_f32_16x16x32_bf16 v[152:155], v[40:43], v[220:223], v[152:155]\n\t" \
  "v_mfma_f32_16x16x32_bf16 v[156:159], v[44:47], v[220:223], v[156:159]\n\t" \
  "v_mfma_f32_16x16x32_bf16 v[144:147], v[48:51], v[224:227], v[144:147]\n\t" \
  "v_mfma_f32_16x16x32_bf16 v[148:151], v[52:55], v[224:227], v[148:151]\n\t" \
  "v_mfma_f32_16x16x32_bf16 v[152:155], v[48:51], v[228:231], v[152:155]\n\t" \
  "v_mfma_f32_16x16x32_bf16 v[156:159], v[52:55], v[228:231], v[156:159]\n\t" \
  "v_mfma_f32_16x16x32_bf16 v[144:147], v[56:59], v[232:235], v[144:147]\n\t" \
  "v_mfma_f32_16x16x32_bf16 v[148:151], v[60:63], v[232:235], v[148:151]\n\t" \
  "v_mfma_f32_16x16x32_bf16 v[152:155], v[56:59], v[236:239], v[152:155]\n\t" \
  "v_mfma_f32_16x16x32_bf16 v[156:159], v[60:63], v[236:239], v[156:159]\n\t" \
  "s_setprio 0\n\t" \
  "s_waitcnt vmcnt(4)\n\t" \
  "s_barrier\n\t" \
  "s_add_u32 s92, s92, 65024\n\t" \
  "s_addc_u32 s93, s93, 0\n\t" \
  "ds_read_b128 v[160:163], v253 offset:0\n\t" \
  "ds_read_b128 v[164:167], v253 offset:1024\n\t" \
  "ds_read_b128 v[176:179], v244 offset:32768\n\t" \
  "ds_read_b128 v[180:183], v244 offset:49152\n\t" \
  "ds_read_b128 v[184:187], v245 offset:32768\n\t" \
  "ds_read_b128 v[188:191], v245 offset:49152\n\t" \
  "s_add_u32 m0, %[lw], 65536\n\t" \
  "s_nop 0\n\t" \
  "global_load_lds_dwordx4 v248, s[92:93] sc1\n\t" \
  "ds_read_b128 v[192:195], v246 offset:32768\n\t" \
  "ds_read_b128 v[196:199], v246 offset:49152\n\t" \
  "ds_read_b128 v[200:203], v247 offset:32768\n\t" \
  "ds_read_b128 v[204:207], v247 offset:49152\n\t" \
  "s_add_u32 m0, %[lw], 66560\n\t" \
  "s_nop 0\n\t" \
  "global_load_lds_dwordx4 v249, s[92:93] sc1\n\t" \
  "ds_read_b128 v[208:211], v244 offset:33024\n\t" \
  "ds_read_b128 v[212:215], v244 offset:49408\n\t" \
  "ds_read_b128 v[216:219], v245 offset:33024\n\t" \
  "ds_read_b128 v[220:223], v245 offset:49408\n\t" \
  "s_add_u32 m0, %[lw], 67584\n\t" \
  "s_nop 0\n\t" \
  "global_load_lds_dwordx4 v250, s[92:93] sc1\n\t" \
  "ds_read_b128 v[224:227], v246 offset:33024\n\t" \
  "ds_read_b128 v[228:231], v246 offset:49408\n\t" \
  "ds_read_b128 v[232:235], v247 offset:33024\n\t" \
  "ds_read_b128 v[236:239], v247 offset:49408\n\t" \
  "s_add_u32 m0, %[lw], 68608\n\t" \
  "s_nop 0\n\t" \
  "global_load_lds_dwordx4 v251, s[92:93] sc1\n\t" \
  "s_waitcnt lgkmcnt(15)\n\t" \
  "s_waitcnt lgkmcnt(12)\n\t" \
  "s_lshr_b32 s90, s87, 7\n\t" \
  "s_xor_b32 s90, s90, %[s0]\n\t" \
  "s_and_b32 s90, s90, 1\n\t" \
  "s_lshl1_add_u32 s90, s87, s90\n\t" \
  "s_lshl_b32 s90, s90, 13\n\t" \
  "v_add_f32 v160, v160, v128\n\t" \
  "v_add_f32 v161, v161, v129\n\t" \
  "v_add_f32 v162, v162, v130\n\t" \
  "v_add_f32 v163, v163, v131\n\t" \
  "v_add_f32 v164, v164, v132\n\t" \
  "v_add_f32 v165, v165, v133\n\t" \
  "v_add_f32 v166, v166, v134\n\t" \
  "v_add_f32 v167, v167, v135\n\t" \
  "v_cvt_pk_bf16_f32 v168, v160, v161\n\t" \
  "v_cvt_pk_bf16_f32 v169, v162, v163\n\t" \
  "v_cvt_pk_bf16_f32 v170, v164, v165\n\t" \
  "v_cvt_pk_bf16_f32 v171, v166, v167\n\t" \
  "buffer_store_dwordx4 v[168:171], v172, %[rt], s90 offen sc1\n\t" \
  "s_add_u32 s86, s86, 1\n\t" \
  "s_add_u32 s87, s87, 1\n\t" \
  "s_waitcnt lgkmcnt(0)\n\t" \
  "s_setprio 1\n\t" \
  "v_mfma_f32_16x16x32_bf16 v[144:147], v[64:67], v[176:179], v[144:147]\n\t" \
  "v_mfma_f32_16x16x32_bf16 v[148:151], v[68:71], v[176:179], v[148:151]\n\t" \
  "v_mfma_f32_16x16x32_bf16 v[152:155], v[64:67], v[180:183], v[152:155]\n\t" \
  "v_mfma_f32_16x16x32_bf16 v[156:159], v[68:71], v[180:183], v[156:159]\n\t" \
  "v_mfma_f32_16x16x32_bf16 v[144:147], v[72:75], v[184:187], v[144:147]\n\t" \
  "v_mfma_f32_16x16x32_bf16 v[148:151], v[76:79], v[184:187], v[148:151]\n\t" \
  "v_mfma_f32_16x16x32_bf16 v[152:155], v[72:75], v[188:191], v[152:155]\n\t" \
  "v_mfma_f32_16x16x32_bf16 v[156:159], v[76:79], v[188:191], v[156:159]\n\t" \
  "v_mfma_f32_16x16x32_bf16 v[144:147], v[80:83], v[192:195], v[144:147]\n\t" \
  "v_mfma_f32_16x16x32_bf16 v[148:151], v[84:87], v[192:195], v[148:151]\n\t" \
  "v_mfma_f32_16x16x32_bf16 v[152:155], v[80:83], v[196:199], v[152:155]\n\t" \
  "v_mfma_f32_16x16x32_bf16 v[156:159], v[84:87], v[196:199], v[156:159]\n\t" \
  "v_mfma_f32_16x16x32_bf16 v[144:147], v[88:91], v[200:203], v[144:147]\n\t" \
  "v_mfma_f32_16x16x32_bf16 v[148:151], v[92:95], v[200:203], v[148:151]\n\t" \
  "v_mfma_f32_16x16x32_bf16 v[152:155], v[88:91], v[204:207], v[152:155]\n\t" \
  "v_mfma_f32_16x16x32_bf16 v[156:159], v[92:95], v[204:207], v[156:159]\n\t" \
  "v_mfma_f32_16x16x32_bf16 v[144:147], v[96:99], v[208:211], v[144:147]\n\t" \
  "v_mfma_f32_16x16x32_bf16 v[148:151], v[100:103], v[208:211], v[148:151]\n\t" \
  "v_mfma_f32_16x16x32_bf16 v[152:155], v[96:99], v[212:215], v[152:155]\n\t" \
  "v_mfma_f32_16x16x32_bf16 v[156:159], v[100:103], v[212:215], v[156:159]\n\t" \
  "v_mfma_f32_16x16x32_bf16 v[144:147], v[104:107], v[216:219], v[144:147]\n\t" \
  "v_mfma_f32_16x16x32_bf16 v[148:151], v[108:111], v[216:219], v[148:151]\n\t" \
  "v_mfma_f32_16x16x32_bf16 v[152:155], v[104:107], v[220:223], v[152:155]\n\t" \
  "v_mfma_f32_16x16x32_bf16 v[156:159], v[108:111], v[220:223], v[156:159]\n\t" \
  "v_mfma_f32_16x16x32_bf16 v[144:147], v[112:115], v[224:227], v[144:147]\n\t" \
  "v_mfma_f32_16x16x32_bf16 v[148:151], v[116:119], v[224:227], v[148:151]\n\t" \
  "v_mfma_f32_16x16x32_bf16 v[152:155], v[112:115], v[228:231], v[152:155]\n\t" \
  "v_mfma_f32_16x16x32_bf16 v[156:159], v[116:119], v[228:231], v[156:159]\n\t" \
  "v_mfma_f32_16x16x32_bf16 v[144:147], v[120:123], v[232:235], v[144:147]\n\t" \
  "v_mfma_f32_16x16x32_bf16 v[148:151], v[124:127], v[232:235], v[148:151]\n\t" \
  "v_mfma_f32_16x16x32_bf16 v[152:155], v[120:123], v[236:239], v[152:155]\n\t" \
  "v_mfma_f32_16x16x32_bf16 v[156:159], v[124:127], v[236:239], v[156:159]\n\t" \
  "s_setprio 0\n\t" \
  "s_cmp_lt_u32 s86, 64\n\t" \
  "s_cbranch_scc1 .Lp0%=\n\t" \
  "s_barrier\n\t" \
  "s_nop 2\n\t" \
  "ds_write_b128 v252, v[152:155] offset:0\n\t" \
  "ds_write_b128 v252, v[156:159] offset:1024\n\t" \
  "s_waitcnt lgkmcnt(0)\n\t" \
  "s_barrier\n\t" \
  "ds_read_b128 v[160:163], v253 offset:0\n\t" \
  "ds_read_b128 v[164:167], v253 offset:1024\n\t" \
  "s_waitcnt lgkmcnt(0)\n\t" \
  "s_lshr_b32 s90, s87, 7\n\t" \
  "s_xor_b32 s90, s90, %[s0]\n\t" \
  "s_and_b32 s90, s90, 1\n\t" \
  "s_lshl1_add_u32 s90, s87, s90\n\t" \
  "s_lshl_b32 s90, s90, 13\n\t" \
  "v_add_f32 v160, v160, v144\n\t" \
  "v_add_f32 v161, v161, v145\n\t" \
  "v_add_f32 v162, v162, v146\n\t" \
  "v_add_f32 v163, v163, v147\n\t" \
  "v_add_f32 v164, v164, v148\n\t" \
  "v_add_f32 v165, v165, v149\n\t" \
  "v_add_f32 v166, v166, v150\n\t" \
  "v_add_f32 v167, v167, v151\n\t" \
  "v_cvt_pk_bf16_f32 v168, v160, v161\n\t" \
  "v_cvt_pk_bf16_f32 v169, v162, v163\n\t" \
  "v_cvt_pk_bf16_f32 v170, v164, v165\n\t" \
  "v_cvt_pk_bf16_f32 v171, v166, v167\n\t" \
  "buffer_store_dwordx4 v[168:171], v172, %[rt], s90 offen sc1\n\t" \
  "s_waitcnt vmcnt(0)\n\t" \
  "s_barrier\n\t" \
  "s_mov_b32 m0, s94\n\t" \
  "s_branch .Le%=\n\t" \
  ".Lk%=:\n\t" \
  "s_mov_b32 s94, m0\n\t" \
  "s_mov_b64 s[92:93], %[x0]\n\t" \
  "s_mov_b32 s86, %[g0]\n\t" \
  "s_add_u32 s87, s86, %[t0]\n\t" \
  "s_add_u32 m0, %[lw], 67584\n\t" \
  "s_nop 0\n\t" \
  "global_load_lds_dwordx4 v250, s[92:93] sc1\n\t" \
  "s_add_u32 m0, %[lw], 68608\n\t" \
  "s_nop 0\n\t" \
  "global_load_lds_dwordx4 v251, s[92:93] sc1\n\t" \
  "s_barrier\n\t" \
  "s_branch .Ls1%=\n\t" \
  ".Lp1%=:\n\t" \
  "s_waitcnt vmcnt(10)\n\t" \
  "s_barrier\n\t" \
  "s_setprio 2\n\t" \
  "v_mfma_f32_16x16x32_bf16 v[144:147], v[64:67], v[176:179], v[144:147]\n\t" \
  "v_mfma_f32_16x16x32_bf16 v[148:151], v[68:71], v[176:179], v[148:151]\n\t" \
  "v_mfma_f32_16x16x32_bf16 v[152:155], v[64:67], v[180:183], v[152:155]\n\t" \
  "v_mfma_f32_16x16x32_bf16 v[156:159], v[68:71], v[180:183], v[156:159]\n\t" \
  "v_mfma_f32_16x16x32_bf16 v[144:147], v[72:75], v[184:187], v[144:147]\n\t" \
  "v_mfma_f32_16x16x32_bf16 v[148:151], v[76:79], v[184:187], v[148:151]\n\t" \
  "v_mfma_f32_16x16x32_bf16 v[152:155], v[72:75], v[188:191], v[152:155]\n\t" \
  "v_mfma_f32_16x16x32_bf16 v[156:159], v[76:79], v[188:191], v[156:159]\n\t" \
  "v_mfma_f32_16x16x32_bf16 v[144:147], v[80:83], v[192:195], v[144:147]\n\t" \
  "v_mfma_f32_16x16x32_bf16 v[148:151], v[84:87], v[192:195], v[148:151]\n\t" \
  "v_mfma_f32_16x16x32_bf16 v[152:155], v[80:83], v[196:199], v[152:155]\n\t" \
  "v_mfma_f32_16x16x32_bf16 v[156:159], v[84:87], v[196:199], v[156:159]\n\t" \
  "v_mfma_f32_16x16x32_bf16 v[144:147], v[88:91], v[200:203], v[144:147]\n\t" \
  "v_mfma_f32_16x16x32_bf16 v[148:151], v[92:95], v[200:203], v[148:151]\n\t" \
  "v_mfma_f32_16x16x32_bf16 v[152:155], v[88:91], v[204:207], v[152:155]\n\t" \
  "v_mfma_f32_16x16x32_bf16 v[156:159], v[92:95], v[204:207], v[156:159]\n\t" \
  "v_mfma_f32_16x16x32_bf16 v[144:147], v[96:99], v[208:211], v[144:147]\n\t" \
  "v_mfma_f32_16x16x32_bf16 v[148:151], v[100:103], v[208:211], v[148:151]\n\t" \
  "v_mfma_f32_16x16x32_bf16 v[152:155], v[96:99], v[212:215], v[152:155]\n\t" \
  "v_mfma_f32_16x16x32_bf16 v[156:159], v[100:103], v[212:215], v[156:159]\n\t" \
  "v_mfma_f32_16x16x32_bf16 v[144:147], v[104:107], v[216:219], v[144:147]\n\t" \
  "v_mfma_f32_16x16x32_bf16 v[148:151], v[108:111], v[216:219], v[148:151]\n\t" \
  "v_mfma_f32_16x16x32_bf16 v[152:155], v[104:107], v[220:223], v[152:155]\n\t" \
  "v_mfma_f32_16x16x32_bf16 v[156:159], v[108:111], v[220:223], v[156:159]\n\t" \
  "v_mfma_f32_16x16x32_bf16 v[144:147], v[112:115], v[224:227], v[144:147]\n\t" \
  "v_mfma_f32_16x16x32_bf16 v[148:151], v[116:119], v[224:227], v[148:151]\n\t" \
  "v_mfma_f32_16x16x32_bf16 v[152:155], v[112:115], v[228:231], v[152:155]\n\t" \
  "v_mfma_f32_16x16x32_bf16 v[156:159], v[116:119], v[228:231], v[156:159]\n\t" \
  "v_mfma_f32_16x16x32_bf16 v[144:147], v[120:123], v[232:235], v[144:147]\n\t" \
  "v_mfma_f32_16x16x32_bf16 v[148:151], v[124:127], v[232:235], v[148:151]\n\t" \
  "v_mfma_f32_16x16x32_bf16 v[152:155], v[120:123], v[236:239], v[152:155]\n\t" \
  "v_mfma_f32_16x16x32_bf16 v[156:159], v[124:127], v[236:239], v[156:159]\n\t" \
  "s_setprio 0\n\t" \
  ".Ls1%=:\n\t" \
  "s_add_u32 s92, s92, 512\n\t" \
  "s_addc_u32 s93, s93, 0\n\t" \
  "ds_read_b128 v[176:179], v240 offset:0\n\t" \
  "ds_read_b128 v[180:183], v240 offset:16384\n\t" \
  "ds_read_b128 v[184:187], v241 offset:0\n\t" \
  "ds_read_b128 v[188:191], v241 offset:16384\n\t" \
  "s_add_u32 m0, %[lw], 98304\n\t" \
  "s_nop 0\n\t" \
  "global_load_lds_dwordx4 v248, s[92:93] sc1\n\t" \
  "ds_read_b128 v[192:195], v242 offset:0\n\t" \
  "ds_read_b128 v[196:199], v242 offset:16384\n\t" \
  "ds_read_b128 v[200:203], v243 offset:0\n\t" \
  "ds_read_b128 v[204:207], v243 offset:16384\n\t" \
  "s_add_u32 m0, %[lw], 99328\n\t" \
  "s_nop 0\n\t" \
  "global_load_lds_dwordx4 v249, s[92:93] sc1\n\t" \
  "ds_read_b128 v[208:211], v240 offset:256\n\t" \
  "ds_read_b128 v[212:215], v240 offset:16640\n\t" \
  "ds_read_b128 v[216:219], v241 offset:256\n\t" \
  "ds_read_b128 v[220:223], v241 offset:16640\n\t" \
  "s_add_u32 m0, %[lw], 100352\n\t" \
  "s_nop 0\n\t" \
  "global_load_lds_dwordx4 v250, s[92:93] sc1\n\t" \
  "ds_read_b128 v[224:227], v242 offset:256\n\t" \
  "ds_read_b128 v[228:231], v242 offset:16640\n\t" \
  "ds_read_b128 v[232:235], v243 offset:256\n\t" \
  "ds_read_b128 v[236:239], v243 offset:16640\n\t" \
  "s_add_u32 m0, %[lw], 101376\n\t" \
  "s_nop 0\n\t" \
  "global_load_lds_dwordx4 v251, s[92:93] sc1\n\t" \
  "ds_write_b128 v252, v[152:155] offset:0\n\t" \
  "ds_write_b128 v252, v[156:159] offset:1024\n\t" \
  "s_waitcnt lgkmcnt(0)\n\t" \
  "s_waitcnt vmcnt(4)\n\t" \
  "s_barrier\n\t" \
  "s_setprio 2\n\t" \
  "v_mfma_f32_16x16x32_bf16 v[128:131], v[0:3], v[176:179], 0\n\t" \
  "v_mfma_f32_16x16x32_bf16 v[132:135], v[4:7], v[176:179], 0\n\t" \
  "v_mfma_f32_16x16x32_bf16 v[136:139], v[0:3], v[180:183], 0\n\t" \
  "v_mfma_f32_16x16x32_bf16 v[140:143], v[4:7], v[180:183], 0\n\t" \
  "v_mfma_f32_16x16x32_bf16 v[128:131], v[8:11], v[184:187], v[128:131]\n\t" \
  "v_mfma_f32_16x16x32_bf16 v[132:135], v[12:15], v[184:187], v[132:135]\n\t" \
  "v_mfma_f32_16x16x32_bf16 v[136:139], v[8:11], v[188:191], v[136:139]\n\t" \
  "v_mfma_f32_16x16x32_bf16 v[140:143], v[12:15], v[188:191], v[140:143]\n\t" \
  "v_mfma_f32_16x16x32_bf16 v[128:131], v[16:19], v[192:195], v[128:131]\n\t" \
  "v_mfma_f32_16x16x32_bf16 v[132:135], v[20:23], v[192:195], v[132:135]\n\t" \
  "v_mfma_f32_16x16x32_bf16 v[136:139], v[16:19], v[196:199], v[136:139]\n\t" \
  "v_mfma_f32_16x16x32_bf16 v[140:143], v[20:23], v[196:199], v[140:143]\n\t" \
  "v_mfma_f32_16x16x32_bf16 v[128:131], v[24:27], v[200:203], v[128:131]\n\t" \
  "v_mfma_f32_16x16x32_bf16 v[132:135], v[28:31], v[200:203], v[132:135]\n\t" \
  "v_mfma_f32_16x16x32_bf16 v[136:139], v[24:27], v[204:207], v[136:139]\n\t" \
  "v_mfma_f32_16x16x32_bf16 v[140:143], v[28:31], v[204:207], v[140:143]\n\t" \
  "v_mfma_f32_16x16x32_bf16 v[128:131], v[32:35], v[208:211], v[128:131]\n\t" \
  "v_mfma_f32_16x16x32_bf16 v[132:135], v[36:39], v[208:211], v[132:135]\n\t" \
  "v_mfma_f32_16x16x32_bf16 v[136:139], v[32:35], v[212:215], v[136:139]\n\t" \
  "v_mfma_f32_16x16x32_bf16 v[140:143], v[36:39], v[212:215], v[140:143]\n\t" \
  "v_mfma_f32_16x16x32_bf16 v[128:131], v[40:43], v[216:219], v[128:131]\n\t" \
  "v_mfma_f32_16x16x32_bf16 v[132:135], v[44:47], v[216:219], v[132:135]\n\t" \
  "v_mfma_f32_16x16x32_bf16 v[136:139], v[40:43], v[220:223], v[136:139]\n\t" \
  "v_mfma_f32_16x16x32_bf16 v[140:143], v[44:47], v[220:223], v[140:143]\n\t" \
  "v_mfma_f32_16x16x32_bf16 v[128:131], v[48:51], v[224:227], v[128:131]\n\t" \
  "v_mfma_f32_16x16x32_bf16 v[132:135], v[52:55], v[224:227], v[132:135]\n\t" \
  "v_mfma_f32_16x16x32_bf16 v[136:139], v[48:51], v[228:231], v[136:139]\n\t" \
  "v_mfma_f32_16x16x32_bf16 v[140:143], v[52:55], v[228:231], v[140:143]\n\t" \
  "v_mfma_f32_16x16x32_bf16 v[128:131], v[56:59], v[232:235], v[128:131]\n\t" \
  "v_mfma_f32_16x16x32_bf16 v[132:135], v[60:63], v[232:235], v[132:135]\n\t" \
  "v_mfma_f32_16x16x32_bf16 v[136:139], v[56:59], v[236:239], v[136:139]\n\t" \
  "v_mfma_f32_16x16x32_bf16 v[140:143], v[60:63], v[236:239], v[140:143]\n\t" \
  "s_setprio 0\n\t" \
  "s_add_u32 s92, s92, 65024\n\t" \
  "s_addc_u32 s93, s93, 0\n\t" \
  "ds_read_b128 v[160:163], v253 offset:0\n\t" \
  "ds_read_b128 v[164:167], v253 offset:1024\n\t" \
  "ds_read_b128 v[176:179], v240 offset:32768\n\t" \
  "ds_read_b128 v[180:183], v240 offset:49152\n\t" \
  "ds_read_b128 v[184:187], v241 offset:32768\n\t" \
  "ds_read_b128 v[188:191], v241 offset:49152\n\t" \
  "s_add_u32 m0, %[lw], 0\n\t" \
  "s_nop 0\n\t" \
  "global_load_lds_dwordx4 v248, s[92:93] sc1\n\t" \
  "ds_read_b128 v[192:195], v242 offset:32768\n\t" \
  "ds_read_b128 v[196:199], v242 offset:49152\n\t" \
  "ds_read_b128 v[200:203], v243 offset:32768\n\t" \
  "ds_read_b128 v[204:207], v243 offset:49152\n\t" \
  "s_add_u32 m0, %[lw], 1024\n\t" \
  "s_nop 0\n\t" \
  "global_load_lds_dwordx4 v249, s[92:93] sc1\n\t" \
  "ds_read_b128 v[208:211], v240 offset:33024\n\t" \
  "ds_read_b128 v[212:215], v240 offset:49408\n\t" \
  "ds_read_b128 v[216:219], v241 offset:33024\n\t" \
  "ds_read_b128 v[220:223], v241 offset:49408\n\t" \
  "s_add_u32 m0, %[lw], 2048\n\t" \
  "s_nop 0\n\t" \
  "global_load_lds_dwordx4 v250, s[92:93] sc1\n\t" \
  "ds_read_b128 v[224:227], v242 offset:33024\n\t" \
  "ds_read_b128 v[228:231], v242 offset:49408\n\t" \
  "ds_read_b128 v[232:235], v243 offset:33024\n\t" \
  "ds_read_b128 v[236:239], v243 offset:49408\n\t" \
  "s_add_u32 m0, %[lw], 3072\n\t" \
  "s_nop 0\n\t" \
  "global_load_lds_dwordx4 v251, s[92:93] sc1\n\t" \
  "s_waitcnt lgkmcnt(15)\n\t" \
  "s_waitcnt lgkmcnt(12)\n\t" \
  "s_sub_i32 s91, s86, 2\n\t" \
  "v_mov_b32 v173, s91\n\t" \
  "buffer_store_dword v173, v174, %[rs], 0 offen sc1\n\t" \
  "s_lshr_b32 s90, s87, 7\n\t" \
  "s_xor_b32 s90, s90, %[s0]\n\t" \
  "s_and_b32 s90, s90, 1\n\t" \
  "s_lshl1_add_u32 s90, s87, s90\n\t" \
  "s_lshl_b32 s90, s90, 13\n\t" \
  "v_add_f32 v160, v160, v144\n\t" \
  "v_add_f32 v161, v161, v145\n\t" \
  "v_add_f32 v162, v162, v146\n\t" \
  "v_add_f32 v163, v163, v147\n\t" \
  "v_add_f32 v164, v164, v148\n\t" \
  "v_add_f32 v165, v165, v149\n\t" \
  "v_add_f32 v166, v166, v150\n\t" \
  "v_add_f32 v167, v167, v151\n\t" \
  "v_cvt_pk_bf16_f32 v168, v160, v161\n\t" \
  "v_cvt_pk_bf16_f32 v169, v162, v163\n\t" \
  "v_cvt_pk_bf16_f32 v170, v164, v165\n\t" \
  "v_cvt_pk_bf16_f32 v171, v166, v167\n\t" \
  "buffer_store_dwordx4 v[168:171], v172, %[rt], s90 offen sc1\n\t" \
  "s_add_u32 s86, s86, 1\n\t" \
  "s_add_u32 s87, s87, 1\n\t" \
  "s_waitcnt lgkmcnt(0)\n\t" \
  "s_waitcnt vmcnt(10)\n\t" \
  "s_barrier\n\t" \
  "s_setprio 2\n\t" \
  "v_mfma_f32_16x16x32_bf16 v[128:131], v[64:67], v[176:179], v[128:131]\n\t" \
  "v_mfma_f32_16x16x32_bf16 v[132:135], v[68:71], v[176:179], v[132:135]\n\t" \
  "v_mfma_f32_16x16x32_bf16 v[136:139], v[64:67], v[180:183], v[136:139]\n\t" \
  "v_mfma_f32_16x16x32_bf16 v[140:143], v[68:71], v[180:183], v[140:143]\n\t" \
  "v_mfma_f32_16x16x32_bf16 v[128:131], v[72:75], v[184:187], v[128:131]\n\t" \
  "v_mfma_f32_16x16x32_bf16 v[132:135], v[76:79], v[184:187], v[132:135]\n\t" \
  "v_mfma_f32_16x16x32_bf16 v[136:139], v[72:75], v[188:191], v[136:139]\n\t" \
  "v_mfma_f32_16x16x32_bf16 v[140:143], v[76:79], v[188:191], v[140:143]\n\t" \
  "v_mfma_f32_16x16x32_bf16 v[128:131], v[80:83], v[192:195], v[128:131]\n\t" \
  "v_mfma_f32_16x16x32_bf16 v[132:135], v[84:87], v[192:195], v[132:135]\n\t" \
  "v_mfma_f32_16x16x32_bf16 v[136:139], v[80:83], v[196:199], v[136:139]\n\t" \
  "v_mfma_f32_16x16x32_bf16 v[140:143], v[84:87], v[196:199], v[140:143]\n\t" \
  "v_mfma_f32_16x16x32_bf16 v[128:131], v[88:91], v[200:203], v[128:131]\n\t" \
  "v_mfma_f32_16x16x32_bf16 v[132:135], v[92:95], v[200:203], v[132:135]\n\t" \
  "v_mfma_f32_16x16x32_bf16 v[136:139], v[88:91], v[204:207], v[136:139]\n\t" \
  "v_mfma_f32_16x16x32_bf16 v[140:143], v[92:95], v[204:207], v[140:143]\n\t" \
  "v_mfma_f32_16x16x32_bf16 v[128:131], v[96:99], v[208:211], v[128:131]\n\t" \
  "v_mfma_f32_16x16x32_bf16 v[132:135], v[100:103], v[208:211], v[132:135]\n\t" \
  "v_mfma_f32_16x16x32_bf16 v[136:139], v[96:99], v[212:215], v[136:139]\n\t" \
  "v_mfma_f32_16x16x32_bf16 v[140:143], v[100:103], v[212:215], v[140:143]\n\t" \
  "v_mfma_f32_16x16x32_bf16 v[128:131], v[104:107], v[216:219], v[128:131]\n\t" \
  "v_mfma_f32_16x16x32_bf16 v[132:135], v[108:111], v[216:219], v[132:135]\n\t" \
  "v_mfma_f32_16x16x32_bf16 v[136:139], v[104:107], v[220:223], v[136:139]\n\t" \
  "v_mfma_f32_16x16x32_bf16 v[140:143], v[108:111], v[220:223], v[140:143]\n\t" \
  "v_mfma_f32_16x16x32_bf16 v[128:131], v[112:115], v[224:227], v[128:131]\n\t" \
  "v_mfma_f32_16x16x32_bf16 v[132:135], v[116:119], v[224:227], v[132:135]\n\t" \
  "v_mfma_f32_16x16x32_bf16 v[136:139], v[112:115], v[228:231], v[136:139]\n\t" \
  "v_mfma_f32_16x16x32_bf16 v[140:143], v[116:119], v[228:231], v[140:143]\n\t" \
  "v_mfma_f32_16x16x32_bf16 v[128:131], v[120:123], v[232:235], v[128:131]\n\t" \
  "v_mfma_f32_16x16x32_bf16 v[132:135], v[124:127], v[232:235], v[132:135]\n\t" \
  "v_mfma_f32_16x16x32_bf16 v[136:139], v[120:123], v[236:239], v[136:139]\n\t" \
  "v_mfma_f32_16x16x32_bf16 v[140:143], v[124:127], v[236:239], v[140:143]\n\t" \
  "s_setprio 0\n\t" \
  "s_add_u32 s92, s92, 512\n\t" \
  "s_addc_u32 s93, s93, 0\n\t" \
  "ds_read_b128 v[176:179], v244 offset:0\n\t" \
  "ds_read_b128 v[180:183], v244 offset:16384\n\t" \
  "ds_read_b128 v[184:187], v245 offset:0\n\t" \
  "ds_read_b128 v[188:191], v245 offset:16384\n\t" \
  "s_add_u32 m0, %[lw], 32768\n\t" \
  "s_nop 0\n\t" \
  "global_load_lds_dwordx4 v248, s[92:93] sc1\n\t" \
  "ds_read_b128 v[192:195], v246 offset:0\n\t" \
  "ds_read_b128 v[196:199], v246 offset:16384\n\t" \
  "ds_read_b128 v[200:203], v247 offset:0\n\t" \
  "ds_read_b128 v[204:207], v247 offset:16384\n\t" \
  "s_add_u32 m0, %[lw], 33792\n\t" \
  "s_nop 0\n\t" \
  "global_load_lds_dwordx4 v249, s[92:93] sc1\n\t" \
  "ds_read_b128 v[208:211], v244 offset:256\n\t" \
  "ds_read_b128 v[212:215], v244 offset:16640\n\t" \
  "ds_read_b128 v[216:219], v245 offset:256\n\t" \
  "ds_read_b128 v[220:223], v245 offset:16640\n\t" \
  "s_add_u32 m0, %[lw], 34816\n\t" \
  "s_nop 0\n\t" \
  "global_load_lds_dwordx4 v250, s[92:93] sc1\n\t" \
  "ds_read_b128 v[224:227], v246 offset:256\n\t" \
  "ds_read_b128 v[228:231], v246 offset:16640\n\t" \
  "ds_read_b128 v[232:235], v247 offset:256\n\t" \
  "ds_read_b128 v[236:239], v247 offset:16640\n\t" \
  "s_add_u32 m0, %[lw], 35840\n\t" \
  "s_nop 0\n\t" \
  "global_load_lds_dwordx4 v251, s[92:93] sc1\n\t" \
  "ds_write_b128 v252, v[136:139] offset:0\n\t" \
  "ds_write_b128 v252, v[140:143] offset:1024\n\t" \
  "s_waitcnt lgkmcnt(0)\n\t" \
  "s_waitcnt vmcnt(4)\n\t" \
  "s_barrier\n\t" \
  "s_setprio 2\n\t" \
  "v_mfma_f32_16x16x32_bf16 v[144:147], v[0:3], v[176:179], 0\n\t" \
  "v_mfma_f32_16x16x32_bf16 v[148:151], v[4:7], v[176:179], 0\n\t" \
  "v_mfma_f32_16x16x32_bf16 v[152:155], v[0:3], v[180:183], 0\n\t" \
  "v_mfma_f32_16x16x32_bf16 v[156:159], v[4:7], v[180:183], 0\n\t" \
  "v_mfma_f32_16x16x32_bf16 v[144:147], v[8:11], v[184:187], v[144:147]\n\t" \
  "v_mfma_f32_16x16x32_bf16 v[148:151], v[12:15], v[184:187], v[148:151]\n\t" \
  "v_mfma_f32_16x16x32_bf16 v[152:155], v[8:11], v[188:191], v[152:155]\n\t" \
  "v_mfma_f32_16x16x32_bf16 v[156:159], v[12:15], v[188:191], v[156:159]\n\t" \
  "v_mfma_f32_16x16x32_bf16 v[144:147], v[16:19], v[192:195], v[144:147]\n\t" \
  "v_mfma_f32_16x16x32_bf16 v[148:151], v[20:23], v[192:195], v[148:151]\n\t" \
  "v_mfma_f32_16x16x32_bf16 v[152:155], v[16:19], v[196:199], v[152:155]\n\t" \
  "v_mfma_f32_16x16x32_bf16 v[156:159], v[20:23], v[196:199], v[156:159]\n\t" \
  "v_mfma_f32_16x16x32_bf16 v[144:147], v[24:27], v[200:203], v[144:147]\n\t" \
  "v_mfma_f32_16x16x32_bf16 v[148:151], v[28:31], v[200:203], v[148:151]\n\t" \
  "v_mfma_f32_16x16x32_bf16 v[152:155], v[24:27], v[204:207], v[152:155]\n\t" \
  "v_mfma_f32_16x16x32_bf16 v[156:159], v[28:31], v[204:207], v[156:159]\n\t" \
  "v_mfma_f32_16x16x32_bf16 v[144:147], v[32:35], v[208:211], v[144:147]\n\t" \
  "v_mfma_f32_16x16x32_bf16 v[148:151], v[36:39], v[208:211], v[148:151]\n\t" \
  "v_mfma_f32_16x16x32_bf16 v[152:155], v[32:35], v[212:215], v[152:155]\n\t" \
  "v_mfma_f32_16x16x32_bf16 v[156:159], v[36:39], v[212:215], v[156:159]\n\t" \
  "v_mfma_f32_16x16x32_bf16 v[144:147], v[40:43], v[216:219], v[144:147]\n\t" \
  "v_mfma_f32_16x16x32_bf16 v[148:151], v[44:47], v[216:219], v[148:151]\n\t" \
  "v_mfma_f32_16x16x32_bf16 v[152:155], v[40:43], v[220:223], v[152:155]\n\t" \
  "v_mfma_f32_16x16x32_bf16 v[156:159], v[44:47], v[220:223], v[156:159]\n\t" \
  "v_mfma_f32_16x16x32_bf16 v[144:147], v[48:51], v[224:227], v[144:147]\n\t" \
  "v_mfma_f32_16x16x32_bf16 v[148:151], v[52:55], v[224:227], v[148:151]\n\t" \
  "v_mfma_f32_16x16x32_bf16 v[152:155], v[48:51], v[228:231], v[152:155]\n\t" \
  "v_mfma_f32_16x16x32_bf16 v[156:159], v[52:55], v[228:231], v[156:159]\n\t" \
  "v_mfma_f32_16x16x32_bf16 v[144:147], v[56:59], v[232:235], v[144:147]\n\t" \
  "v_mfma_f32_16x16x32_bf16 v[148:151], v[60:63], v[232:235], v[148:151]\n\t" \
  "v_mfma_f32_16x16x32_bf16 v[152:155], v[56:59], v[236:239], v[152:155]\n\t" \
  "v_mfma_f32_16x16x32_bf16 v[156:159], v[60:63], v[236:239], v[156:159]\n\t" \
  "s_setprio 0\n\t" \
  "s_add_u32 s92, s92, 65024\n\t" \
  "s_addc_u32 s93, s93, 0\n\t" \
  "ds_read_b128 v[160:163], v253 offset:0\n\t" \
  "ds_read_b128 v[164:167], v253 offset:1024\n\t" \
  "ds_read_b128 v[176:179], v244 offset:32768\n\t" \
  "ds_read_b128 v[180:183], v244 offset:49152\n\t" \
  "ds_read_b128 v[184:187], v245 offset:32768\n\t" \
  "ds_read_b128 v[188:191], v245 offset:49152\n\t" \
  "s_add_u32 m0, %[lw], 65536\n\t" \
  "s_nop 0\n\t" \
  "global_load_lds_dwordx4 v248, s[92:93] sc1\n\t" \
  "ds_read_b128 v[192:195], v246 offset:32768\n\t" \
  "ds_read_b128 v[196:199], v246 offset:49152\n\t" \
  "ds_read_b128 v[200:203], v247 offset:32768\n\t" \
  "ds_read_b128 v[204:207], v247 offset:49152\n\t" \
  "s_add_u32 m0, %[lw], 66560\n\t" \
  "s_nop 0\n\t" \
  "global_load_lds_dwordx4 v249, s[92:93] sc1\n\t" \
  "ds_read_b128 v[208:211], v244 offset:33024\n\t" \
  "ds_read_b128 v[212:215], v244 offset:49408\n\t" \
  "ds_read_b128 v[216:219], v245 offset:33024\n\t" \
  "ds_read_b128 v[220:223], v245 offset:49408\n\t" \
  "s_add_u32 m0, %[lw], 67584\n\t" \
  "s_nop 0\n\t" \
  "global_load_lds_dwordx4 v250, s[92:93] sc1\n\t" \
  "ds_read_b128 v[224:227], v246 offset:33024\n\t" \
  "ds_read_b128 v[228:231], v246 offset:49408\n\t" \
  "ds_read_b128 v[232:235], v247 offset:33024\n\t" \
  "ds_read_b128 v[236:239], v247 offset:49408\n\t" \
  "s_add_u32 m0, %[lw], 68608\n\t" \
  "s_nop 0\n\t" \
  "global_load_lds_dwordx4 v251, s[92:93] sc1\n\t" \
  "s_waitcnt lgkmcnt(15)\n\t" \
  "s_waitcnt lgkmcnt(12)\n\t" \
  "s_sub_i32 s91, s86, 2\n\t" \
  "v_mov_b32 v173, s91\n\t" \
  "buffer_store_dword v173, v174, %[rs], 0 offen sc1\n\t" \
  "s_lshr_b32 s90, s87, 7\n\t" \
  "s_xor_b32 s90, s90, %[s0]\n\t" \
  "s_and_b32 s90, s90, 1\n\t" \
  "s_lshl1_add_u32 s90, s87, s90\n\t" \
  "s_lshl_b32 s90, s90, 13\n\t" \
  "v_add_f32 v160, v160, v128\n\t" \
  "v_add_f32 v161, v161, v129\n\t" \
  "v_add_f32 v162, v162, v130\n\t" \
  "v_add_f32 v163, v163, v131\n\t" \
  "v_add_f32 v164, v164, v132\n\t" \
  "v_add_f32 v165, v165, v133\n\t" \
  "v_add_f32 v166, v166, v134\n\t" \
  "v_add_f32 v167, v167, v135\n\t" \
  "v_cvt_pk_bf16_f32 v168, v160, v161\n\t" \
  "v_cvt_pk_bf16_f32 v169, v162, v163\n\t" \
  "v_cvt_pk_bf16_f32 v170, v164, v165\n\t" \
  "v_cvt_pk_bf16_f32 v171, v166, v167\n\t" \
  "buffer_store_dwordx4 v[168:171], v172, %[rt], s90 offen sc1\n\t" \
  "s_add_u32 s86, s86, 1\n\t" \
  "s_add_u32 s87, s87, 1\n\t" \
  "s_waitcnt lgkmcnt(0)\n\t" \
  "s_cmp_lt_u32 s86, 64\n\t" \
  "s_cbranch_scc1 .Lp1%=\n\t" \
  "s_barrier\n\t" \
  "s_setprio 2\n\t" \
  "v_mfma_f32_16x16x32_bf16 v[144:147], v[64:67], v[176:179], v[144:147]\n\t" \
  "v_mfma_f32_16x16x32_bf16 v[148:151], v[68:71], v[176:179], v[148:151]\n\t" \
  "v_mfma_f32_16x16x32_bf16 v[152:155], v[64:67], v[180:183], v[152:155]\n\t" \
  "v_mfma_f32_16x16x32_bf16 v[156:159], v[68:71], v[180:183], v[156:159]\n\t" \
  "v_mfma_f32_16x16x32_bf16 v[144:147], v[72:75], v[184:187], v[144:147]\n\t" \
  "v_mfma_f32_16x16x32_bf16 v[148:151], v[76:79], v[184:187], v[148:151]\n\t" \
  "v_mfma_f32_16x16x32_bf16 v[152:155], v[72:75], v[188:191], v[152:155]\n\t" \
  "v_mfma_f32_16x16x32_bf16 v[156:159], v[76:79], v[188:191], v[156:159]\n\t" \
  "v_mfma_f32_16x16x32_bf16 v[144:147], v[80:83], v[192:195], v[144:147]\n\t" \
  "v_mfma_f32_16x16x32_bf16 v[148:151], v[84:87], v[192:195], v[148:151]\n\t" \
  "v_mfma_f32_16x16x32_bf16 v[152:155], v[80:83], v[196:199], v[152:155]\n\t" \
  "v_mfma_f32_16x16x32_bf16 v[156:159], v[84:87], v[196:199], v[156:159]\n\t" \
  "v_mfma_f32_16x16x32_bf16 v[144:147], v[88:91], v[200:203], v[144:147]\n\t" \
  "v_mfma_f32_16x16x32_bf16 v[148:151], v[92:95], v[200:203], v[148:151]\n\t" \
  "v_mfma_f32_16x16x32_bf16 v[152:155], v[88:91], v[204:207], v[152:155]\n\t" \
  "v_mfma_f32_16x16x32_bf16 v[156:159], v[92:95], v[204:207], v[156:159]\n\t" \
  "v_mfma_f32_16x16x32_bf16 v[144:147], v[96:99], v[208:211], v[144:147]\n\t" \
  "v_mfma_f32_16x16x32_bf16 v[148:151], v[100:103], v[208:211], v[148:151]\n\t" \
  "v_mfma_f32_16x16x32_bf16 v[152:155], v[96:99], v[212:215], v[152:155]\n\t" \
  "v_mfma_f32_16x16x32_bf16 v[156:159], v[100:103], v[212:215], v[156:159]\n\t" \
  "v_mfma_f32_16x16x32_bf16 v[144:147], v[104:107], v[216:219], v[144:147]\n\t" \
  "v_mfma_f32_16x16x32_bf16 v[148:151], v[108:111], v[216:219], v[148:151]\n\t" \
  "v_mfma_f32_16x16x32_bf16 v[152:155], v[104:107], v[220:223], v[152:155]\n\t" \
  "v_mfma_f32_16x16x32_bf16 v[156:159], v[108:111], v[220:223], v[156:159]\n\t" \
  "v_mfma_f32_16x16x32_bf16 v[144:147], v[112:115], v[224:227], v[144:147]\n\t" \
  "v_mfma_f32_16x16x32_bf16 v[148:151], v[116:119], v[224:227], v[148:151]\n\t" \
  "v_mfma_f32_16x16x32_bf16 v[152:155], v[112:115], v[228:231], v[152:155]\n\t" \
  "v_mfma_f32_16x16x32_bf16 v[156:159], v[116:119], v[228:231], v[156:159]\n\t" \
  "v_mfma_f32_16x16x32_bf16 v[144:147], v[120:123], v[232:235], v[144:147]\n\t" \
  "v_mfma_f32_16x16x32_bf16 v[148:151], v[124:127], v[232:235], v[148:151]\n\t" \
  "v_mfma_f32_16x16x32_bf16 v[152:155], v[120:123], v[236:239], v[152:155]\n\t" \
  "v_mfma_f32_16x16x32_bf16 v[156:159], v[124:127], v[236:239], v[156:159]\n\t" \
  "s_setprio 0\n\t" \
  "s_nop 5\n\t" \
  "ds_write_b128 v252, v[152:155] offset:0\n\t" \
  "ds_write_b128 v252, v[156:159] offset:1024\n\t" \
  "s_waitcnt lgkmcnt(0)\n\t" \
  "s_barrier\n\t" \
  "ds_read_b128 v[160:163], v253 offset:0\n\t" \
  "ds_read_b128 v[164:167], v253 offset:1024\n\t" \
  "s_waitcnt lgkmcnt(0)\n\t" \
  "s_lshr_b32 s90, s87, 7\n\t" \
  "s_xor_b32 s90, s90, %[s0]\n\t" \
  "s_and_b32 s90, s90, 1\n\t" \
  "s_lshl1_add_u32 s90, s87, s90\n\t" \
  "s_lshl_b32 s90, s90, 13\n\t" \
  "v_add_f32 v160, v160, v144\n\t" \
  "v_add_f32 v161, v161, v145\n\t" \
  "v_add_f32 v162, v162, v146\n\t" \
  "v_add_f32 v163, v163, v147\n\t" \
  "v_add_f32 v164, v164, v148\n\t" \
  "v_add_f32 v165, v165, v149\n\t" \
  "v_add_f32 v166, v166, v150\n\t" \
  "v_add_f32 v167, v167, v151\n\t" \
  "v_cvt_pk_bf16_f32 v168, v160, v161\n\t" \
  "v_cvt_pk_bf16_f32 v169, v162, v163\n\t" \
  "v_cvt_pk_bf16_f32 v170, v164, v165\n\t" \
  "v_cvt_pk_bf16_f32 v171, v166, v167\n\t" \
  "buffer_store_dwordx4 v[168:171], v172, %[rt], s90 offen sc1\n\t" \
  "s_waitcnt vmcnt(0)\n\t" \
  "s_barrier\n\t" \
  "s_sub_i32 s91, s86, 1\n\t" \
  "v_mov_b32 v173, s91\n\t" \
  "buffer_store_dword v173, v174, %[rs], 0 offen sc1\n\t" \
  "s_mov_b32 m0, s94\n\t" \
  ".Le%=:\n\t"
#define PP_ASM_1 \
  "s_cmp_lg_u32 %[kh], 0\n\t" \
  "s_cbranch_scc1 .Lk%=\n\t" \
  "s_mov_b32 s94, m0\n\t" \
  "s_mov_b64 s[92:93], %[x0]\n\t" \
  "s_mov_b32 s86, %[g0]\n\t" \
  "s_add_u32 s87, s86, %[t0]\n\t" \
  "s_add_u32 m0, %[lw], 67584\n\t" \
  "s_nop 0\n\t" \
  "global_load_lds_dwordx4 v250, s[92:93] sc1\n\t" \
  "s_add_u32 m0, %[lw], 68608\n\t" \
  "s_nop 0\n\t" \
  "global_load_lds_dwordx4 v251, s[92:93] sc1\n\t" \
  "s_barrier\n\t" \
  "s_branch .Ls0%=\n\t" \
  ".Lp0%=:\n\t" \
  "s_waitcnt vmcnt(9)\n\t" \
  "s_barrier\n\t" \
  ".Ls0%=:\n\t" \
  "s_add_u32 s92, s92, 512\n\t" \
  "s_addc_u32 s93, s93, 0\n\t" \
  "ds_read_b128 v[176:179], v240 offset:0\n\t" \
  "ds_read_b128 v[180:183], v240 offset:16384\n\t" \
  "ds_read_b128 v[184:187], v241 offset:0\n\t" \
  "ds_read_b128 v[188:191], v241 offset:16384\n\t" \
  "s_add_u32 m0, %[lw], 98304\n\t" \
  "s_nop 0\n\t" \
  "global_load_lds_dwordx4 v248, s[92:93] sc1\n\t" \
  "ds_read_b128 v[192:195], v242 offset:0\n\t" \
  "ds_read_b128 v[196:199], v242 offset:16384\n\t" \
  "ds_read_b128 v[200:203], v243 offset:0\n\t" \
  "ds_read_b128 v[204:207], v243 offset:16384\n\t" \
  "s_add_u32 m0, %[lw], 99328\n\t" \
  "s_nop 0\n\t" \
  "global_load_lds_dwordx4 v249, s[92:93] sc1\n\t" \
  "ds_read_b128 v[208:211], v240 offset:256\n\t" \
  "ds_read_b128 v[212:215], v240 offset:16640\n\t" \
  "ds_read_b128 v[216:219], v241 offset:256\n\t" \
  "ds_read_b128 v[220:223], v241 offset:16640\n\t" \
  "s_add_u32 m0, %[lw], 100352\n\t" \
  "s_nop 0\n\t" \
  "global_load_lds_dwordx4 v250, s[92:93] sc1\n\t" \
  "ds_read_b128 v[224:227], v242 offset:256\n\t" \
  "ds_read_b128 v[228:231], v242 offset:16640\n\t" \
  "ds_read_b128 v[232:235], v243 offset:256\n\t" \
  "ds_read_b128 v[236:239], v243 offset:16640\n\t" \
  "s_add_u32 m0, %[lw], 101376\n\t" \
  "s_nop 0\n\t" \
  "global_load_lds_dwordx4 v251, s[92:93] sc1\n\t" \
  "ds_write_b128 v252, v[152:155] offset:0\n\t" \
  "ds_write_b128 v252, v[156:159] offset:1024\n\t" \
  "s_waitcnt lgkmcnt(0)\n\t" \
  "s_setprio 1\n\t" \
  "v_mfma_f32_16x16x32_bf16 v[128:131], v[176:179], v[0:3], 0\n\t" \
  "v_mfma_f32_16x16x32_bf16 v[136:139], v[176:179], v[4:7], 0\n\t" \
  "v_mfma_f32_16x16x32_bf16 v[132:135], v[180:183], v[0:3], 0\n\t" \
  "v_mfma_f32_16x16x32_bf16 v[140:143], v[180:183], v[4:7], 0\n\t" \
  "v_mfma_f32_16x16x32_bf16 v[128:131], v[184:187], v[8:11], v[128:131]\n\t" \
  "v_mfma_f32_16x16x32_bf16 v[136:139], v[184:187], v[12:15], v[136:139]\n\t" \
  "v_mfma_f32_16x16x32_bf16 v[132:135], v[188:191], v[8:11], v[132:135]\n\t" \
  "v_mfma_f32_16x16x32_bf16 v[140:143], v[188:191], v[12:15], v[140:143]\n\t" \
  "v_mfma_f32_16x16x32_bf16 v[128:131], v[192:195], v[16:19], v[128:131]\n\t" \
  "v_mfma_f32_16x16x32_bf16 v[136:139], v[192:195], v[20:23], v[136:139]\n\t" \
  "v_mfma_f32_16x16x32_bf16 v[132:135], v[196:199], v[16:19], v[132:135]\n\t" \
  "v_mfma_f32_16x16x32_bf16 v[140:143], v[196:199], v[20:23], v[140:143]\n\t" \
  "v_mfma_f32_16x16x32_bf16 v[128:131], v[200:203], v[24:27], v[128:131]\n\t" \
  "v_mfma_f32_16x16x32_bf16 v[136:139], v[200:203], v[28:31], v[136:139]\n\t" \
  "v_mfma_f32_16x16x32_bf16 v[132:135], v[204:207], v[24:27], v[132:135]\n\t" \
  "v_mfma_f32_16x16x32_bf16 v[140:143], v[204:207], v[28:31], v[140:143]\n\t" \
  "v_mfma_f32_16x16x32_bf16 v[128:131], v[208:211], v[32:35], v[128:131]\n\t" \
  "v_mfma_f32_16x16x32_bf16 v[136:139], v[208:211], v[36:39], v[136:139]\n\t" \
  "v_mfma_f32_16x16x32_bf16 v[132:135], v[212:215], v[32:35], v[132:135]\n\t" \
  "v_mfma_f32_16x16x32_bf16 v[140:143], v[212:215], v[36:39], v[140:143]\n\t" \
  "v_mfma_f32_16x16x32_bf16 v[128:131], v[216:219], v[40:43], v[128:131]\n\t" \
  "v_mfma_f32_16x16x32_bf16 v[136:139], v[216:219], v[44:47], v[136:139]\n\t" \
  "v_mfma_f32_16x16x32_bf16 v[132:135], v[220:223], v[40:43], v[132:135]\n\t" \
  "v_mfma_f32_16x16x32_bf16 v[140:143], v[220:223], v[44:47], v[140:143]\n\t" \
  "v_mfma_f32_16x16x32_bf16 v[128:131], v[224:227], v[48:51], v[128:131]\n\t" \
  "v_mfma_f32_16x16x32_bf16 v[136:139], v[224:227], v[52:55], v[136:139]\n\t" \
  "v_mfma_f32_16x16x32_bf16 v[132:135], v[228:231], v[48:51], v[132:135]\n\t" \
  "v_mfma_f32_16x16x32_bf16 v[140:143], v[228:231], v[52:55], v[140:143]\n\t" \
  "v_mfma_f32_16x16x32_bf16 v[128:131], v[232:235], v[56:59], v[128:131]\n\t" \
  "v_mfma_f32_16x16x32_bf16 v[136:139], v[232:235], v[60:63], v[136:139]\n\t" \
  "v_mfma_f32_16x16x32_bf16 v[132:135], v[236:239], v[56:59], v[132:135]\n\t" \
  "v_mfma_f32_16x16x32_bf16 v[140:143], v[236:239], v[60:63], v[140:143]\n\t" \
  "s_setprio 0\n\t" \
  "s_waitcnt vmcnt(4)\n\t" \
  "s_barrier\n\t" \
  "s_add_u32 s92, s92, 65024\n\t" \
  "s_addc_u32 s93, s93, 0\n\t" \
  "ds_read_b128 v[160:163], v253 offset:0\n\t" \
  "ds_read_b128 v[164:167], v253 offset:1024\n\t" \
  "ds_read_b128 v[176:179], v240 offset:32768\n\t" \
  "ds_read_b128 v[180:183], v240 offset:49152\n\t" \
  "ds_read_b128 v[184:187], v241 offset:32768\n\t" \
  "ds_read_b128 v[188:191], v241 offset:49152\n\t" \
  "s_add_u32 m0, %[lw], 0\n\t" \
  "s_nop 0\n\t" \
  "global_load_lds_dwordx4 v248, s[92:93] sc1\n\t" \
  "ds_read_b128 v[192:195], v242 offset:32768\n\t" \
  "ds_read_b128 v[196:199], v242 offset:49152\n\t" \
  "ds_read_b128 v[200:203], v243 offset:32768\n\t" \
  "ds_read_b128 v[204:207], v243 offset:49152\n\t" \
  "s_add_u32 m0, %[lw], 1024\n\t" \
  "s_nop 0\n\t" \
  "global_load_lds_dwordx4 v249, s[92:93] sc1\n\t" \
  "ds_read_b128 v[208:211], v240 offset:33024\n\t" \
  "ds_read_b128 v[212:215], v240 offset:49408\n\t" \
  "ds_read_b128 v[216:219], v241 offset:33024\n\t" \
  "ds_read_b128 v[220:223], v241 offset:49408\n\t" \
  "s_add_u32 m0, %[lw], 2048\n\t" \
  "s_nop 0\n\t" \
  "global_load_lds_dwordx4 v250, s[92:93] sc1\n\t" \
  "ds_read_b128 v[224:227], v242 offset:33024\n\t" \
  "ds_read_b128 v[228:231], v242 offset:49408\n\t" \
  "ds_read_b128 v[232:235], v243 offset:33024\n\t" \
  "ds_read_b128 v[236:239], v243 offset:49408\n\t" \
  "s_add_u32 m0, %[lw], 3072\n\t" \
  "s_nop 0\n\t" \
  "global_load_lds_dwordx4 v251, s[92:93] sc1\n\t" \
  "s_waitcnt lgkmcnt(15)\n\t" \
  "s_waitcnt lgkmcnt(12)\n\t" \
  "s_lshr_b32 s90, s87, 7\n\t" \
  "s_xor_b32 s90, s90, %[s0]\n\t" \
  "s_and_b32 s90, s90, 1\n\t" \
  "s_lshl1_add_u32 s90, s87, s90\n\t" \
  "s_lshl_b32 s90, s90, 13\n\t" \
  "v_add_f32 v160, v160, v144\n\t" \
  "v_add_f32 v161, v161, v145\n\t" \
  "v_add_f32 v162, v162, v146\n\t" \
  "v_add_f32 v163, v163, v147\n\t" \
  "v_add_f32 v164, v164, v148\n\t" \
  "v_add_f32 v165, v165, v149\n\t" \
  "v_add_f32 v166, v166, v150\n\t" \
  "v_add_f32 v167, v167, v151\n\t" \
  "v_cvt_pk_bf16_f32 v168, v160, v161\n\t" \
  "v_cvt_pk_bf16_f32 v169, v162, v163\n\t" \
  "v_cvt_pk_bf16_f32 v170, v164, v165\n\t" \
  "v_cvt_pk_bf16_f32 v171, v166, v167\n\t" \
  "buffer_store_dwordx4 v[168:171], v172, %[rt], s90 offen sc1\n\t" \
  "s_add_u32 s86, s86, 1\n\t" \
  "s_add_u32 s87, s87, 1\n\t" \
  "s_waitcnt lgkmcnt(0)\n\t" \
  "s_setprio 1\n\t" \
  "v_mfma_f32_16x16x32_bf16 v[128:131], v[176:179], v[64:67], v[128:131]\n\t" \
  "v_mfma_f32_16x16x32_bf16 v[136:139], v[176:179], v[68:71], v[136:139]\n\t" \
  "v_mfma_f32_16x16x32_bf16 v[132:135], v[180:183], v[64:67], v[132:135]\n\t" \
  "v_mfma_f32_16x16x32_bf16 v[140:143], v[180:183], v[68:71], v[140:143]\n\t" \
  "v_mfma_f32_16x16x32_bf16 v[128:131], v[184:187], v[72:75], v[128:131]\n\t" \
  "v_mfma_f32_16x16x32_bf16 v[136:139], v[184:187], v[76:79], v[136:139]\n\t" \
  "v_mfma_f32_16x16x32_bf16 v[132:135], v[188:191], v[72:75], v[132:135]\n\t" \
  "v_mfma_f32_16x16x32_bf16 v[140:143], v[188:191], v[76:79], v[140:143]\n\t" \
  "v_mfma_f32_16x16x32_bf16 v[128:131], v[192:195], v[80:83], v[128:131]\n\t" \
  "v_mfma_f32_16x16x32_bf16 v[136:139], v[192:195], v[84:87], v[136:139]\n\t" \
  "v_mfma_f32_16x16x32_bf16 v[132:135], v[196:199], v[80:83], v[132:135]\n\t" \
  "v_mfma_f32_16x16x32_bf16 v[140:143], v[196:199], v[84:87], v[140:143]\n\t" \
  "v_mfma_f32_16x16x32_bf16 v[128:131], v[200:203], v[88:91], v[128:131]\n\t" \
  "v_mfma_f32_16x16x32_bf16 v[136:139], v[200:203], v[92:95], v[136:139]\n\t" \
  "v_mfma_f32_16x16x32_bf16 v[132:135], v[204:207], v[88:91], v[132:135]\n\t" \
  "v_mfma_f32_16x16x32_bf16 v[140:143], v[204:207], v[92:95], v[140:143]\n\t" \
  "v_mfma_f32_16x16x32_bf16 v[128:131], v[208:211], v[96:99], v[128:131]\n\t" \
  "v_mfma_f32_16x16x32_bf16 v[136:139], v[208:211], v[100:103], v[136:139]\n\t" \
  "v_mfma_f32_16x16x32_bf16 v[132:135], v[212:215], v[96:99], v[132:135]\n\t" \
  "v_mfma_f32_16x16x32_bf16 v[140:143], v[212:215], v[100:103], v[140:143]\n\t" \
  "v_mfma_f32_16x16x32_bf16 v[128:131], v[216:219], v[104:107], v[128:131]\n\t" \
  "v_mfma_f32_16x16x32_bf16 v[136:139], v[216:219], v[108:111], v[136:139]\n\t" \
  "v_mfma_f32_16x16x32_bf16 v[132:135], v[220:223], v[104:107], v[132:135]\n\t" \
  "v_mfma_f32_16x16x32_bf16 v[140:143], v[220:223], v[108:111], v[140:143]\n\t" \
  "v_mfma_f32_16x16x32_bf16 v[128:131], v[224:227], v[112:115], v[128:131]\n\t" \
  "v_mfma_f32_16x16x32_bf16 v[136:139], v[224:227], v[116:119], v[136:139]\n\t" \
  "v_mfma_f32_16x16x32_bf16 v[132:135], v[228:231], v[112:115], v[132:135]\n\t" \
  "v_mfma_f32_16x16x32_bf16 v[140:143], v[228:231], v[116:119], v[140:143]\n\t" \
  "v_mfma_f32_16x16x32_bf16 v[128:131], v[232:235], v[120:123], v[128:131]\n\t" \
  "v_mfma_f32_16x16x32_bf16 v[136:139], v[232:235], v[124:127], v[136:139]\n\t" \
  "v_mfma_f32_16x16x32_bf16 v[132:135], v[236:239], v[120:123], v[132:135]\n\t" \
  "v_mfma_f32_16x16x32_bf16 v[140:143], v[236:239], v[124:127], v[140:143]\n\t" \
  "s_setprio 0\n\t" \
  "s_waitcnt vmcnt(9)\n\t" \
  "s_barrier\n\t" \
  "s_add_u32 s92, s92, 512\n\t" \
  "s_addc_u32 s93, s93, 0\n\t" \
  "ds_read_b128 v[176:179], v244 offset:0\n\t" \
  "ds_read_b128 v[180:183], v244 offset:16384\n\t" \
  "ds_read_b128 v[184:187], v245 offset:0\n\t" \
  "ds_read_b128 v[188:191], v245 offset:16384\n\t" \
  "s_add_u32 m0, %[lw], 32768\n\t" \
  "s_nop 0\n\t" \
  "global_load_lds_dwordx4 v248, s[92:93] sc1\n\t" \
  "ds_read_b128 v[192:195], v246 offset:0\n\t" \
  "ds_read_b128 v[196:199], v246 offset:16384\n\t" \
  "ds_read_b128 v[200:203], v247 offset:0\n\t" \
  "ds_read_b128 v[204:207], v247 offset:16384\n\t" \
  "s_add_u32 m0, %[lw], 33792\n\t" \
  "s_nop 0\n\t" \
  "global_load_lds_dwordx4 v249, s[92:93] sc1\n\t" \
  "ds_read_b128 v[208:211], v244 offset:256\n\t" \
  "ds_read_b128 v[212:215], v244 offset:16640\n\t" \
  "ds_read_b128 v[216:219], v245 offset:256\n\t" \
  "ds_read_b128 v[220:223], v245 offset:16640\n\t" \
  "s_add_u32 m0, %[lw], 34816\n\t" \
  "s_nop 0\n\t" \
  "global_load_lds_dwordx4 v250, s[92:93] sc1\n\t" \
  "ds_read_b128 v[224:227], v246 offset:256\n\t" \
  "ds_read_b128 v[228:231], v246 offset:16640\n\t" \
  "ds_read_b128 v[232:235], v247 offset:256\n\t" \
  "ds_read_b128 v[236:239], v247 offset:16640\n\t" \
  "s_add_u32 m0, %[lw], 35840\n\t" \
  "s_nop 0\n\t" \
  "global_load_lds_dwordx4 v251, s[92:93] sc1\n\t" \
  "ds_write_b128 v252, v[136:139] offset:0\n\t" \
  "ds_write_b128 v252, v[140:143] offset:1024\n\t" \
  "s_waitcnt lgkmcnt(0)\n\t" \
  "s_setprio 1\n\t" \
  "v_mfma_f32_16x16x32_bf16 v[144:147], v[176:179], v[0:3], 0\n\t" \
  "v_mfma_f32_16x16x32_bf16 v[152:155], v[176:179], v[4:7], 0\n\t" \
  "v_mfma_f32_16x16x32_bf16 v[148:151], v[180:183], v[0:3], 0\n\t" \
  "v_mfma_f32_16x16x32_bf16 v[156:159], v[180:183], v[4:7], 0\n\t" \
  "v_mfma_f32_16x16x32_bf16 v[144:147], v[184:187], v[8:11], v[144:147]\n\t" \
  "v_mfma_f32_16x16x32_bf16 v[152:155], v[184:187], v[12:15], v[152:155]\n\t" \
  "v_mfma_f32_16x16x32_bf16 v[148:151], v[188:191], v[8:11], v[148:151]\n\t" \
  "v_mfma_f32_16x16x32_bf16 v[156:159], v[188:191], v[12:15], v[156:159]\n\t" \
  "v_mfma_f32_16x16x32_bf16 v[144:147], v[192:195], v[16:19], v[144:147]\n\t" \
  "v_mfma_f32_16x16x32_bf16 v[152:155], v[192:195], v[20:23], v[152:155]\n\t" \
  "v_mfma_f32_16x16x32_bf16 v[148:151], v[196:199], v[16:19], v[148:151]\n\t" \
  "v_mfma_f32_16x16x32_bf16 v[156:159], v[196:199], v[20:23], v[156:159]\n\t" \
  "v_mfma_f32_16x16x32_bf16 v[144:147], v[200:203], v[24:27], v[144:147]\n\t" \
  "v_mfma_f32_16x16x32_bf16 v[152:155], v[200:203], v[28:31], v[152:155]\n\t" \
  "v_mfma_f32_16x16x32_bf16 v[148:151], v[204:207], v[24:27], v[148:151]\n\t" \
  "v_mfma_f32_16x16x32_bf16 v[156:159], v[204:207], v[28:31], v[156:159]\n\t" \
  "v_mfma_f32_16x16x32_bf16 v[144:147], v[208:211], v[32:35], v[144:147]\n\t" \
  "v_mfma_f32_16x16x32_bf16 v[152:155], v[208:211], v[36:39], v[152:155]\n\t" \
  "v_mfma_f32_16x16x32_bf16 v[148:151], v[212:215], v[32:35], v[148:151]\n\t" \
  "v_mfma_f32_16x16x32_bf16 v[156:159], v[212:215], v[36:39], v[156:159]\n\t" \
  "v_mfma_f32_16x16x32_bf16 v[144:147], v[216:219], v[40:43], v[144:147]\n\t" \
  "v_mfma_f32_16x16x32_bf16 v[152:155], v[216:219], v[44:47], v[152:155]\n\t" \
  "v_mfma_f32_16x16x32_bf16 v[148:151], v[220:223], v[40:43], v[148:151]\n\t" \
  "v_mfma_f32_16x16x32_bf16 v[156:159], v[220:223], v[44:47], v[156:159]\n\t" \
  "v_mfma_f32_16x16x32_bf16 v[144:147], v[224:227], v[48:51], v[144:147]\n\t" \
  "v_mfma_f32_16x16x32_bf16 v[152:155], v[224:227], v[52:55], v[152:155]\n\t" \
  "v_mfma_f32_16x16x32_bf16 v[148:151], v[228:231], v[48:51], v[148:151]\n\t" \
  "v_mfma_f32_16x16x32_bf16 v[156:159], v[228:231], v[52:55], v[156:159]\n\t" \
  "v_mfma_f32_16x16x32_bf16 v[144:147], v[232:235], v[56:59], v[144:147]\n\t" \
  "v_mfma_f32_16x16x32_bf16 v[152:155], v[232:235], v[60:63], v[152:155]\n\t" \
  "v_mfma_f32_16x16x32_bf16 v[148:151], v[236:239], v[56:59], v[148:151]\n\t" \
  "v_mfma_f32_16x16x32_bf16 v[156:159], v[236:239], v[60:63], v[156:159]\n\t" \
  "s_setprio 0\n\t" \
  "s_waitcnt vmcnt(4)\n\t" \
  "s_barrier\n\t" \
  "s_add_u32 s92, s92, 65024\n\t" \
  "s_addc_u32 s93, s93, 0\n\t" \
  "ds_read_b128 v[160:163], v253 offset:0\n\t" \
  "ds_read_b128 v[164:167], v253 offset:1024\n\t" \
  "ds_read_b128 v[176:179], v244 offset:32768\n\t" \
  "ds_read_b128 v[180:183], v244 offset:49152\n\t" \
  "ds_read_b128 v[184:187], v245 offset:32768\n\t" \
  "ds_read_b128 v[188:191], v245 offset:49152\n\t" \
  "s_add_u32 m0, %[lw], 65536\n\t" \
  "s_nop 0\n\t" \
  "global_load_lds_dwordx4 v248, s[92:93] sc1\n\t" \
  "ds_read_b128 v[192:195], v246 offset:32768\n\t" \
  "ds_read_b128 v[196:199], v246 offset:49152\n\t" \
  "ds_read_b128 v[200:203], v247 offset:32768\n\t" \
  "ds_read_b128 v[204:207], v247 offset:49152\n\t" \
  "s_add_u32 m0, %[lw], 66560\n\t" \
  "s_nop 0\n\t" \
  "global_load_lds_dwordx4 v249, s[92:93] sc1\n\t" \
  "ds_read_b128 v[208:211], v244 offset:33024\n\t" \
  "ds_read_b128 v[212:215], v244 offset:49408\n\t" \
  "ds_read_b128 v[216:219], v245 offset:33024\n\t" \
  "ds_read_b128 v[220:223], v245 offset:49408\n\t" \
  "s_add_u32 m0, %[lw], 67584\n\t" \
  "s_nop 0\n\t" \
  "global_load_lds_dwordx4 v250, s[92:93] sc1\n\t" \
  "ds_read_b128 v[224:227], v246 offset:33024\n\t" \
  "ds_read_b128 v[228:231], v246 offset:49408\n\t" \
  "ds_read_b128 v[232:235], v247 offset:33024\n\t" \
  "ds_read_b128 v[236:239], v247 offset:49408\n\t" \
  "s_add_u32 m0, %[lw], 68608\n\t" \
  "s_nop 0\n\t" \
  "global_load_lds_dwordx4 v251, s[92:93] sc1\n\t" \
  "s_waitcnt lgkmcnt(15)\n\t" \
  "s_waitcnt lgkmcnt(12)\n\t" \
  "s_lshr_b32 s90, s87, 7\n\t" \
  "s_xor_b32 s90, s90, %[s0]\n\t" \
  "s_and_b32 s90, s90, 1\n\t" \
  "s_lshl1_add_u32 s90, s87, s90\n\t" \
  "s_lshl_b32 s90, s90, 13\n\t" \
  "v_add_f32 v160, v160, v128\n\t" \
  "v_add_f32 v161, v161, v129\n\t" \
  "v_add_f32 v162, v162, v130\n\t" \
  "v_add_f32 v163, v163, v131\n\t" \
  "v_add_f32 v164, v164, v132\n\t" \
  "v_add_f32 v165, v165, v133\n\t" \
  "v_add_f32 v166, v166, v134\n\t" \
  "v_add_f32 v167, v167, v135\n\t" \
  "v_cvt_pk_bf16_f32 v168, v160, v161\n\t" \
  "v_cvt_pk_bf16_f32 v169, v162, v163\n\t" \
  "v_cvt_pk_bf16_f32 v170, v164, v165\n\t" \
  "v_cvt_pk_bf16_f32 v171, v166, v167\n\t" \
  "buffer_store_dwordx4 v[168:171], v172, %[rt], s90 offen sc1\n\t" \
  "s_add_u32 s86, s86, 1\n\t" \
  "s_add_u32 s87, s87, 1\n\t" \
  "s_waitcnt lgkmcnt(0)\n\t" \
  "s_setprio 1\n\t" \
  "v_mfma_f32_16x16x32_bf16 v[144:147], v[176:179], v[64:67], v[144:147]\n\t" \
  "v_mfma_f32_16x16x32_bf16 v[152:155], v[176:179], v[68:71], v[152:155]\n\t" \
  "v_mfma_f32_16x16x32_bf16 v[148:151], v[180:183], v[64:67], v[148:151]\n\t" \
  "v_mfma_f32_16x16x32_bf16 v[156:159], v[180:183], v[68:71], v[156:159]\n\t" \
  "v_mfma_f32_16x16x32_bf16 v[144:147], v[184:187], v[72:75], v[144:147]\n\t" \
  "v_mfma_f32_16x16x32_bf16 v[152:155], v[184:187], v[76:79], v[152:155]\n\t" \
  "v_mfma_f32_16x16x32_bf16 v[148:151], v[188:191], v[72:75], v[148:151]\n\t" \
  "v_mfma_f32_16x16x32_bf16 v[156:159], v[188:191], v[76:79], v[156:159]\n\t" \
  "v_mfma_f32_16x16x32_bf16 v[144:147], v[192:195], v[80:83], v[144:147]\n\t" \
  "v_mfma_f32_16x16x32_bf16 v[152:155], v[192:195], v[84:87], v[152:155]\n\t" \
  "v_mfma_f32_16x16x32_bf16 v[148:151], v[196:199], v[80:83], v[148:151]\n\t" \
  "v_mfma_f32_16x16x32_bf16 v[156:159], v[196:199], v[84:87], v[156:159]\n\t" \
  "v_mfma_f32_16x16x32_bf16 v[144:147], v[200:203], v[88:91], v[144:147]\n\t" \
  "v_mfma_f32_16x16x32_bf16 v[152:155], v[200:203], v[92:95], v[152:155]\n\t" \
  "v_mfma_f32_16x16x32_bf16 v[148:151], v[204:207], v[88:91], v[148:151]\n\t" \
  "v_mfma_f32_16x16x32_bf16 v[156:159], v[204:207], v[92:95], v[156:159]\n\t" \
  "v_mfma_f32_16x16x32_bf16 v[144:147], v[208:211], v[96:99], v[144:147]\n\t" \
  "v_mfma_f32_16x16x32_bf16 v[152:155], v[208:211], v[100:103], v[152:155]\n\t" \
  "v_mfma_f32_16x16x32_bf16 v[148:151], v[212:215], v[96:99], v[148:151]\n\t" \
  "v_mfma_f32_16x16x32_bf16 v[156:159], v[212:215], v[100:103], v[156:159]\n\t" \
  "v_mfma_f32_16x16x32_bf16 v[144:147], v[216:219], v[104:107], v[144:147]\n\t" \
  "v_mfma_f32_16x16x32_bf16 v[152:155], v[216:219], v[108:111], v[152:155]\n\t" \
  "v_mfma_f32_16x16x32_bf16 v[148:151], v[220:223], v[104:107], v[148:151]\n\t" \
  "v_mfma_f32_16x16x32_bf16 v[156:159], v[220:223], v[108:111], v[156:159]\n\t" \
  "v_mfma_f32_16x16x32_bf16 v[144:147], v[224:227], v[112:115], v[144:147]\n\t" \
  "v_mfma_f32_16x16x32_bf16 v[152:155], v[224:227], v[116:119], v[152:155]\n\t" \
  "v_mfma_f32_16x16x32_bf16 v[148:151], v[228:231], v[112:115], v[148:151]\n\t" \
  "v_mfma_f32_16x16x32_bf16 v[156:159], v[228:231], v[116:119], v[156:159]\n\t" \
  "v_mfma_f32_16x16x32_bf16 v[144:147], v[232:235], v[120:123], v[144:147]\n\t" \
  "v_mfma_f32_16x16x32_bf16 v[152:155], v[232:235], v[124:127], v[152:155]\n\t" \
  "v_mfma_f32_16x16x32_bf16 v[148:151], v[236:239], v[120:123], v[148:151]\n\t" \
  "v_mfma_f32_16x16x32_bf16 v[156:159], v[236:239], v[124:127], v[156:159]\n\t" \
  "s_setprio 0\n\t" \
  "s_cmp_lt_u32 s86, 64\n\t" \
  "s_cbranch_scc1 .Lp0%=\n\t" \
  "s_barrier\n\t" \
  "s_nop 1\n\t" \
  "ds_write_b128 v252, v[152:155] offset:0\n\t" \
  "s_nop 0\n\t" \
  "ds_write_b128 v252, v[156:159] offset:1024\n\t" \
  "s_waitcnt lgkmcnt(0)\n\t" \
  "s_barrier\n\t" \
  "ds_read_b128 v[160:163], v253 offset:0\n\t" \
  "ds_read_b128 v[164:167], v253 offset:1024\n\t" \
  "s_waitcnt lgkmcnt(0)\n\t" \
  "s_lshr_b32 s90, s87, 7\n\t" \
  "s_xor_b32 s90, s90, %[s0]\n\t" \
  "s_and_b32 s90, s90, 1\n\t" \
  "s_lshl1_add_u32 s90, s87, s90\n\t" \
  "s_lshl_b32 s90, s90, 13\n\t" \
  "v_add_f32 v160, v160, v144\n\t" \
  "v_add_f32 v161, v161, v145\n\t" \
  "v_add_f32 v162, v162, v146\n\t" \
  "v_add_f32 v163, v163, v147\n\t" \
  "v_add_f32 v164, v164, v148\n\t" \
  "v_add_f32 v165, v165, v149\n\t" \
  "v_add_f32 v166, v166, v150\n\t" \
  "v_add_f32 v167, v167, v151\n\t" \
  "v_cvt_pk_bf16_f32 v168, v160, v161\n\t" \
  "v_cvt_pk_bf16_f32 v169, v162, v163\n\t" \
  "v_cvt_pk_bf16_f32 v170, v164, v165\n\t" \
  "v_cvt_pk_bf16_f32 v171, v166, v167\n\t" \
  "buffer_store_dwordx4 v[168:171], v172, %[rt], s90 offen sc1\n\t" \
  "s_waitcnt vmcnt(0)\n\t" \
  "s_barrier\n\t" \
  "s_mov_b32 m0, s94\n\t" \
  "s_branch .Le%=\n\t" \
  ".Lk%=:\n\t" \
  "s_mov_b32 s94, m0\n\t" \
  "s_mov_b64 s[92:93], %[x0]\n\t" \
  "s_mov_b32 s86, %[g0]\n\t" \
  "s_add_u32 s87, s86, %[t0]\n\t" \
  "s_add_u32 m0, %[lw], 67584\n\t" \
  "s_nop 0\n\t" \
  "global_load_lds_dwordx4 v250, s[92:93] sc1\n\t" \
  "s_add_u32 m0, %[lw], 68608\n\t" \
  "s_nop 0\n\t" \
  "global_load_lds_dwordx4 v251, s[92:93] sc1\n\t" \
  "s_barrier\n\t" \
  "s_branch .Ls1%=\n\t" \
  ".Lp1%=:\n\t" \
  "s_waitcnt vmcnt(10)\n\t" \
  "s_barrier\n\t" \
  "s_setprio 2\n\t" \
  "v_mfma_f32_16x16x32_bf16 v[144:147], v[176:179], v[64:67], v[144:147]\n\t" \
  "v_mfma_f32_16x16x32_bf16 v[152:155], v[176:179], v[68:71], v[152:155]\n\t" \
  "v_mfma_f32_16x16x32_bf16 v[148:151], v[180:183], v[64:67], v[148:151]\n\t" \
  "v_mfma_f32_16x16x32_bf16 v[156:159], v[180:183], v[68:71], v[156:159]\n\t" \
  "v_mfma_f32_16x16x32_bf16 v[144:147], v[184:187], v[72:75], v[144:147]\n\t" \
  "v_mfma_f32_16x16x32_bf16 v[152:155], v[184:187], v[76:79], v[152:155]\n\t" \
  "v_mfma_f32_16x16x32_bf16 v[148:151], v[188:191], v[72:75], v[148:151]\n\t" \
  "v_mfma_f32_16x16x32_bf16 v[156:159], v[188:191], v[76:79], v[156:159]\n\t" \
  "v_mfma_f32_16x16x32_bf16 v[144:147], v[192:195], v[80:83], v[144:147]\n\t" \
  "v_mfma_f32_16x16x32_bf16 v[152:155], v[192:195], v[84:87], v[152:155]\n\t" \
  "v_mfma_f32_16x16x32_bf16 v[148:151], v[196:199], v[80:83], v[148:151]\n\t" \
  "v_mfma_f32_16x16x32_bf16 v[156:159], v[196:199], v[84:87], v[156:159]\n\t" \
  "v_mfma_f32_16x16x32_bf16 v[144:147], v[200:203], v[88:91], v[144:147]\n\t" \
  "v_mfma_f32_16x16x32_bf16 v[152:155], v[200:203], v[92:95], v[152:155]\n\t" \
  "v_mfma_f32_16x16x32_bf16 v[148:151], v[204:207], v[88:91], v[148:151]\n\t" \
  "v_mfma_f32_16x16x32_bf16 v[156:159], v[204:207], v[92:95], v[156:159]\n\t" \
  "v_mfma_f32_16x16x32_bf16 v[144:147], v[208:211], v[96:99], v[144:147]\n\t" \
  "v_mfma_f32_16x16x32_bf16 v[152:155], v[208:211], v[100:103], v[152:155]\n\t" \
  "v_mfma_f32_16x16x32_bf16 v[148:151], v[212:215], v[96:99], v[148:151]\n\t" \
  "v_mfma_f32_16x16x32_bf16 v[156:159], v[212:215], v[100:103], v[156:159]\n\t" \
  "v_mfma_f32_16x16x32_bf16 v[144:147], v[216:219], v[104:107], v[144:147]\n\t" \
  "v_mfma_f32_16x16x32_bf16 v[152:155], v[216:219], v[108:111], v[152:155]\n\t" \
  "v_mfma_f32_16x16x32_bf16 v[148:151], v[220:223], v[104:107], v[148:151]\n\t" \
  "v_mfma_f32_16x16x32_bf16 v[156:159], v[220:223], v[108:111], v[156:159]\n\t" \
  "v_mfma_f32_16x16x32_bf16 v[144:147], v[224:227], v[112:115], v[144:147]\n\t" \
  "v_mfma_f32_16x16x32_bf16 v[152:155], v[224:227], v[116:119], v[152:155]\n\t" \
  "v_mfma_f32_16x16x32_bf16 v[148:151], v[228:231], v[112:115], v[148:151]\n\t" \
  "v_mfma_f32_16x16x32_bf16 v[156:159], v[228:231], v[116:119], v[156:159]\n\t" \
  "v_mfma_f32_16x16x32_bf16 v[144:147], v[232:235], v[120:123], v[144:147]\n\t" \
  "v_mfma_f32_16x16x32_bf16 v[152:155], v[232:235], v[124:127], v[152:155]\n\t" \
  "v_mfma_f32_16x16x32_bf16 v[148:151], v[236:239], v[120:123], v[148:151]\n\t" \
  "v_mfma_f32_16x16x32_bf16 v[156:159], v[236:239], v[124:127], v[156:159]\n\t" \
  "s_setprio 0\n\t" \
  ".Ls1%=:\n\t" \
  "s_add_u32 s92, s92, 512\n\t" \
  "s_addc_u32 s93, s93, 0\n\t" \
  "ds_read_b128 v[176:179], v240 offset:0\n\t" \
  "ds_read_b128 v[180:183], v240 offset:16384\n\t" \
  "ds_read_b128 v[184:187], v241 offset:0\n\t" \
  "ds_read_b128 v[188:191], v241 offset:16384\n\t" \
  "s_add_u32 m0, %[lw], 98304\n\t" \
  "s_nop 0\n\t" \
  "global_load_lds_dwordx4 v248, s[92:93] sc1\n\t" \
  "ds_read_b128 v[192:195], v242 offset:0\n\t" \
  "ds_read_b128 v[196:199], v242 offset:16384\n\t" \
  "ds_read_b128 v[200:203], v243 offset:0\n\t" \
  "ds_read_b128 v[204:207], v243 offset:16384\n\t" \
  "s_add_u32 m0, %[lw], 99328\n\t" \
  "s_nop 0\n\t" \
  "global_load_lds_dwordx4 v249, s[92:93] sc1\n\t" \
  "ds_read_b128 v[208:211], v240 offset:256\n\t" \
  "ds_read_b128 v[212:215], v240 offset:16640\n\t" \
  "ds_read_b128 v[216:219], v241 offset:256\n\t" \
  "ds_read_b128 v[220:223], v241 offset:16640\n\t" \
  "s_add_u32 m0, %[lw], 100352\n\t" \
  "s_nop 0\n\t" \
  "global_load_lds_dwordx4 v250, s[92:93] sc1\n\t" \
  "ds_read_b128 v[224:227], v242 offset:256\n\t" \
  "ds_read_b128 v[228:231], v242 offset:16640\n\t" \
  "ds_read_b128 v[232:235], v243 offset:256\n\t" \
  "ds_read_b128 v[236:239], v243 offset:16640\n\t" \
  "s_add_u32 m0, %[lw], 101376\n\t" \
  "s_nop 0\n\t" \
  "global_load_lds_dwordx4 v251, s[92:93] sc1\n\t" \
  "ds_write_b128 v252, v[152:155] offset:0\n\t" \
  "ds_write_b128 v252, v[156:159] offset:1024\n\t" \
  "s_waitcnt lgkmcnt(0)\n\t" \
  "s_waitcnt vmcnt(4)\n\t" \
  "s_barrier\n\t" \
  "s_setprio 2\n\t" \
  "v_mfma_f32_16x16x32_bf16 v[128:131], v[176:179], v[0:3], 0\n\t" \
  "v_mfma_f32_16x16x32_bf16 v[136:139], v[176:179], v[4:7], 0\n\t" \
  "v_mfma_f32_16x16x32_bf16 v[132:135], v[180:183], v[0:3], 0\n\t" \
  "v_mfma_f32_16x16x32_bf16 v[140:143], v[180:183], v[4:7], 0\n\t" \
  "v_mfma_f32_16x16x32_bf16 v[128:131], v[184:187], v[8:11], v[128:131]\n\t" \
  "v_mfma_f32_16x16x32_bf16 v[136:139], v[184:187], v[12:15], v[136:139]\n\t" \
  "v_mfma_f32_16x16x32_bf16 v[132:135], v[188:191], v[8:11], v[132:135]\n\t" \
  "v_mfma_f32_16x16x32_bf16 v[140:143], v[188:191], v[12:15], v[140:143]\n\t" \
  "v_mfma_f32_16x16x32_bf16 v[128:131], v[192:195], v[16:19], v[128:131]\n\t" \
  "v_mfma_f32_16x16x32_bf16 v[136:139], v[192:195], v[20:23], v[136:139]\n\t" \
  "v_mfma_f32_16x16x32_bf16 v[132:135], v[196:199], v[16:19], v[132:135]\n\t" \
  "v_mfma_f32_16x16x32_bf16 v[140:143], v[196:199], v[20:23], v[140:143]\n\t" \
  "v_mfma_f32_16x16x32_bf16 v[128:131], v[200:203], v[24:27], v[128:131]\n\t" \
  "v_mfma_f32_16x16x32_bf16 v[136:139], v[200:203], v[28:31], v[136:139]\n\t" \
  "v_mfma_f32_16x16x32_bf16 v[132:135], v[204:207], v[24:27], v[132:135]\n\t" \
  "v_mfma_f32_16x16x32_bf16 v[140:143], v[204:207], v[28:31], v[140:143]\n\t" \
  "v_mfma_f32_16x16x32_bf16 v[128:131], v[208:211], v[32:35], v[128:131]\n\t" \
  "v_mfma_f32_16x16x32_bf16 v[136:139], v[208:211], v[36:39], v[136:139]\n\t" \
  "v_mfma_f32_16x16x32_bf16 v[132:135], v[212:215], v[32:35], v[132:135]\n\t" \
  "v_mfma_f32_16x16x32_bf16 v[140:143], v[212:215], v[36:39], v[140:143]\n\t" \
  "v_mfma_f32_16x16x32_bf16 v[128:131], v[216:219], v[40:43], v[128:131]\n\t" \
  "v_mfma_f32_16x16x32_bf16 v[136:139], v[216:219], v[44:47], v[136:139]\n\t" \
  "v_mfma_f32_16x16x32_bf16 v[132:135], v[220:223], v[40:43], v[132:135]\n\t" \
  "v_mfma_f32_16x16x32_bf16 v[140:143], v[220:223], v[44:47], v[140:143]\n\t" \
  "v_mfma_f32_16x16x32_bf16 v[128:131], v[224:227], v[48:51], v[128:131]\n\t" \
  "v_mfma_f32_16x16x32_bf16 v[136:139], v[224:227], v[52:55], v[136:139]\n\t" \
  "v_mfma_f32_16x16x32_bf16 v[132:135], v[228:231], v[48:51], v[132:135]\n\t" \
  "v_mfma_f32_16x16x32_bf16 v[140:143], v[228:231], v[52:55], v[140:143]\n\t" \
  "v_mfma_f32_16x16x32_bf16 v[128:131], v[232:235], v[56:59], v[128:131]\n\t" \
  "v_mfma_f32_16x16x32_bf16 v[136:139], v[232:235], v[60:63], v[136:139]\n\t" \
  "v_mfma_f32_16x16x32_bf16 v[132:135], v[236:239], v[56:59], v[132:135]\n\t" \
  "v_mfma_f32_16x16x32_bf16 v[140:143], v[236:239], v[60:63], v[140:143]\n\t" \
  "s_setprio 0\n\t" \
  "s_add_u32 s92, s92, 65024\n\t" \
  "s_addc_u32 s93, s93, 0\n\t" \
  "ds_read_b128 v[160:163], v253 offset:0\n\t" \
  "ds_read_b128 v[164:167], v253 offset:1024\n\t" \
  "ds_read_b128 v[176:179], v240 offset:32768\n\t" \
  "ds_read_b128 v[180:183], v240 offset:49152\n\t" \
  "ds_read_b128 v[184:187], v241 offset:32768\n\t" \
  "ds_read_b128 v[188:191], v241 offset:49152\n\t" \
  "s_add_u32 m0, %[lw], 0\n\t" \
  "s_nop 0\n\t" \
  "global_load_lds_dwordx4 v248, s[92:93] sc1\n\t" \
  "ds_read_b128 v[192:195], v242 offset:32768\n\t" \
  "ds_read_b128 v[196:199], v242 offset:49152\n\t" \
  "ds_read_b128 v[200:203], v243 offset:32768\n\t" \
  "ds_read_b128 v[204:207], v243 offset:49152\n\t" \
  "s_add_u32 m0, %[lw], 1024\n\t" \
  "s_nop 0\n\t" \
  "global_load_lds_dwordx4 v249, s[92:93] sc1\n\t" \
  "ds_read_b128 v[208:211], v240 offset:33024\n\t" \
  "ds_read_b128 v[212:215], v240 offset:49408\n\t" \
  "ds_read_b128 v[216:219], v241 offset:33024\n\t" \
  "ds_read_b128 v[220:223], v241 offset:49408\n\t" \
  "s_add_u32 m0, %[lw], 2048\n\t" \
  "s_nop 0\n\t" \
  "global_load_lds_dwordx4 v250, s[92:93] sc1\n\t" \
  "ds_read_b128 v[224:227], v242 offset:33024\n\t" \
  "ds_read_b128 v[228:231], v242 offset:49408\n\t" \
  "ds_read_b128 v[232:235], v243 offset:33024\n\t" \
  "ds_read_b128 v[236:239], v243 offset:49408\n\t" \
  "s_add_u32 m0, %[lw], 3072\n\t" \
  "s_nop 0\n\t" \
  "global_load_lds_dwordx4 v251, s[92:93] sc1\n\t" \
  "s_waitcnt lgkmcnt(15)\n\t" \
  "s_waitcnt lgkmcnt(12)\n\t" \
  "s_sub_i32 s91, s86, 2\n\t" \
  "v_mov_b32 v173, s91\n\t" \
  "buffer_store_dword v173, v174, %[rs], 0 offen sc1\n\t" \
  "s_lshr_b32 s90, s87, 7\n\t" \
  "s_xor_b32 s90, s90, %[s0]\n\t" \
  "s_and_b32 s90, s90, 1\n\t" \
  "s_lshl1_add_u32 s90, s87, s90\n\t" \
  "s_lshl_b32 s90, s90, 13\n\t" \
  "v_add_f32 v160, v160, v144\n\t" \
  "v_add_f32 v161, v161, v145\n\t" \
  "v_add_f32 v162, v162, v146\n\t" \
  "v_add_f32 v163, v163, v147\n\t" \
  "v_add_f32 v164, v164, v148\n\t" \
  "v_add_f32 v165, v165, v149\n\t" \
  "v_add_f32 v166, v166, v150\n\t" \
  "v_add_f32 v167, v167, v151\n\t" \
  "v_cvt_pk_bf16_f32 v168, v160, v161\n\t" \
  "v_cvt_pk_bf16_f32 v169, v162, v163\n\t" \
  "v_cvt_pk_bf16_f32 v170, v164, v165\n\t" \
  "v_cvt_pk_bf16_f32 v171, v166, v167\n\t" \
  "buffer_store_dwordx4 v[168:171], v172, %[rt], s90 offen sc1\n\t" \
  "s_add_u32 s86, s86, 1\n\t" \
  "s_add_u32 s87, s87, 1\n\t" \
  "s_waitcnt lgkmcnt(0)\n\t" \
  "s_waitcnt vmcnt(10)\n\t" \
  "s_barrier\n\t" \
  "s_setprio 2\n\t" \
  "v_mfma_f32_16x16x32_bf16 v[128:131], v[176:179], v[64:67], v[128:131]\n\t" \
  "v_mfma_f32_16x16x32_bf16 v[136:139], v[176:179], v[68:71], v[136:139]\n\t" \
  "v_mfma_f32_16x16x32_bf16 v[132:135], v[180:183], v[64:67], v[132:135]\n\t" \
  "v_mfma_f32_16x16x32_bf16 v[140:143], v[180:183], v[68:71], v[140:143]\n\t" \
  "v_mfma_f32_16x16x32_bf16 v[128:131], v[184:187], v[72:75], v[128:131]\n\t" \
  "v_mfma_f32_16x16x32_bf16 v[136:139], v[184:187], v[76:79], v[136:139]\n\t" \
  "v_mfma_f32_16x16x32_bf16 v[132:135], v[188:191], v[72:75], v[132:135]\n\t" \
  "v_mfma_f32_16x16x32_bf16 v[140:143], v[188:191], v[76:79], v[140:143]\n\t" \
  "v_mfma_f32_16x16x32_bf16 v[128:131], v[192:195], v[80:83], v[128:131]\n\t" \
  "v_mfma_f32_16x16x32_bf16 v[136:139], v[192:195], v[84:87], v[136:139]\n\t" \
  "v_mfma_f32_16x16x32_bf16 v[132:135], v[196:199], v[80:83], v[132:135]\n\t" \
  "v_mfma_f32_16x16x32_bf16 v[140:143], v[196:199], v[84:87], v[140:143]\n\t" \
  "v_mfma_f32_16x16x32_bf16 v[128:131], v[200:203], v[88:91], v[128:131]\n\t" \
  "v_mfma_f32_16x16x32_bf16 v[136:139], v[200:203], v[92:95], v[136:139]\n\t" \
  "v_mfma_f32_16x16x32_bf16 v[132:135], v[204:207], v[88:91], v[132:135]\n\t" \
  "v_mfma_f32_16x16x32_bf16 v[140:143], v[204:207], v[92:95], v[140:143]\n\t" \
  "v_mfma_f32_16x16x32_bf16 v[128:131], v[208:211], v[96:99], v[128:131]\n\t" \
  "v_mfma_f32_16x16x32_bf16 v[136:139], v[208:211], v[100:103], v[136:139]\n\t" \
  "v_mfma_f32_16x16x32_bf16 v[132:135], v[212:215], v[96:99], v[132:135]\n\t" \
  "v_mfma_f32_16x16x32_bf16 v[140:143], v[212:215], v[100:103], v[140:143]\n\t" \
  "v_mfma_f32_16x16x32_bf16 v[128:131], v[216:219], v[104:107], v[128:131]\n\t" \
  "v_mfma_f32_16x16x32_bf16 v[136:139], v[216:219], v[108:111], v[136:139]\n\t" \
  "v_mfma_f32_16x16x32_bf16 v[132:135], v[220:223], v[104:107], v[132:135]\n\t" \
  "v_mfma_f32_16x16x32_bf16 v[140:143], v[220:223], v[108:111], v[140:143]\n\t" \
  "v_mfma_f32_16x16x32_bf16 v[128:131], v[224:227], v[112:115], v[128:131]\n\t" \
  "v_mfma_f32_16x16x32_bf16 v[136:139], v[224:227], v[116:119], v[136:139]\n\t" \
  "v_mfma_f32_16x16x32_bf16 v[132:135], v[228:231], v[112:115], v[132:135]\n\t" \
  "v_mfma_f32_16x16x32_bf16 v[140:143], v[228:231], v[116:119], v[140:143]\n\t" \
  "v_mfma_f32_16x16x32_bf16 v[128:131], v[232:235], v[120:123], v[128:131]\n\t" \
  "v_mfma_f32_16x16x32_bf16 v[136:139], v[232:235], v[124:127], v[136:139]\n\t" \
  "v_mfma_f32_16x16x32_bf16 v[132:135], v[236:239], v[120:123], v[132:135]\n\t" \
  "v_mfma_f32_16x16x32_bf16 v[140:143], v[236:239], v[124:127], v[140:143]\n\t" \
  "s_setprio 0\n\t" \
  "s_add_u32 s92, s92, 512\n\t" \
  "s_addc_u32 s93, s93, 0\n\t" \
  "ds_read_b128 v[176:179], v244 offset:0\n\t" \
  "ds_read_b128 v[180:183], v244 offset:16384\n\t" \
  "ds_read_b128 v[184:187], v245 offset:0\n\t" \
  "ds_read_b128 v[188:191], v245 offset:16384\n\t" \
  "s_add_u32 m0, %[lw], 32768\n\t" \
  "s_nop 0\n\t" \
  "global_load_lds_dwordx4 v248, s[92:93] sc1\n\t" \
  "ds_read_b128 v[192:195], v246 offset:0\n\t" \
  "ds_read_b128 v[196:199], v246 offset:16384\n\t" \
  "ds_read_b128 v[200:203], v247 offset:0\n\t" \
  "ds_read_b128 v[204:207], v247 offset:16384\n\t" \
  "s_add_u32 m0, %[lw], 33792\n\t" \
  "s_nop 0\n\t" \
  "global_load_lds_dwordx4 v249, s[92:93] sc1\n\t" \
  "ds_read_b128 v[208:211], v244 offset:256\n\t" \
  "ds_read_b128 v[212:215], v244 offset:16640\n\t" \
  "ds_read_b128 v[216:219], v245 offset:256\n\t" \
  "ds_read_b128 v[220:223], v245 offset:16640\n\t" \
  "s_add_u32 m0, %[lw], 34816\n\t" \
  "s_nop 0\n\t" \
  "global_load_lds_dwordx4 v250, s[92:93] sc1\n\t" \
  "ds_read_b128 v[224:227], v246 offset:256\n\t" \
  "ds_read_b128 v[228:231], v246 offset:16640\n\t" \
  "ds_read_b128 v[232:235], v247 offset:256\n\t" \
  "ds_read_b128 v[236:239], v247 offset:16640\n\t" \
  "s_add_u32 m0, %[lw], 35840\n\t" \
  "s_nop 0\n\t" \
  "global_load_lds_dwordx4 v251, s[92:93] sc1\n\t" \
  "ds_write_b128 v252, v[136:139] offset:0\n\t" \
  "ds_write_b128 v252, v[140:143] offset:1024\n\t" \
  "s_waitcnt lgkmcnt(0)\n\t" \
  "s_waitcnt vmcnt(4)\n\t" \
  "s_barrier\n\t" \
  "s_setprio 2\n\t" \
  "v_mfma_f32_16x16x32_bf16 v[144:147], v[176:179], v[0:3], 0\n\t" \
  "v_mfma_f32_16x16x32_bf16 v[152:155], v[176:179], v[4:7], 0\n\t" \
  "v_mfma_f32_16x16x32_bf16 v[148:151], v[180:183], v[0:3], 0\n\t" \
  "v_mfma_f32_16x16x32_bf16 v[156:159], v[180:183], v[4:7], 0\n\t" \
  "v_mfma_f32_16x16x32_bf16 v[144:147], v[184:187], v[8:11], v[144:147]\n\t" \
  "v_mfma_f32_16x16x32_bf16 v[152:155], v[184:187], v[12:15], v[152:155]\n\t" \
  "v_mfma_f32_16x16x32_bf16 v[148:151], v[188:191], v[8:11], v[148:151]\n\t" \
  "v_mfma_f32_16x16x32_bf16 v[156:159], v[188:191], v[12:15], v[156:159]\n\t" \
  "v_mfma_f32_16x16x32_bf16 v[144:147], v[192:195], v[16:19], v[144:147]\n\t" \
  "v_mfma_f32_16x16x32_bf16 v[152:155], v[192:195], v[20:23], v[152:155]\n\t" \
  "v_mfma_f32_16x16x32_bf16 v[148:151], v[196:199], v[16:19], v[148:151]\n\t" \
  "v_mfma_f32_16x16x32_bf16 v[156:159], v[196:199], v[20:23], v[156:159]\n\t" \
  "v_mfma_f32_16x16x32_bf16 v[144:147], v[200:203], v[24:27], v[144:147]\n\t" \
  "v_mfma_f32_16x16x32_bf16 v[152:155], v[200:203], v[28:31], v[152:155]\n\t" \
  "v_mfma_f32_16x16x32_bf16 v[148:151], v[204:207], v[24:27], v[148:151]\n\t" \
  "v_mfma_f32_16x16x32_bf16 v[156:159], v[204:207], v[28:31], v[156:159]\n\t" \
  "v_mfma_f32_16x16x32_bf16 v[144:147], v[208:211], v[32:35], v[144:147]\n\t" \
  "v_mfma_f32_16x16x32_bf16 v[152:155], v[208:211], v[36:39], v[152:155]\n\t" \
  "v_mfma_f32_16x16x32_bf16 v[148:151], v[212:215], v[32:35], v[148:151]\n\t" \
  "v_mfma_f32_16x16x32_bf16 v[156:159], v[212:215], v[36:39], v[156:159]\n\t" \
  "v_mfma_f32_16x16x32_bf16 v[144:147], v[216:219], v[40:43], v[144:147]\n\t" \
  "v_mfma_f32_16x16x32_bf16 v[152:155], v[216:219], v[44:47], v[152:155]\n\t" \
  "v_mfma_f32_16x16x32_bf16 v[148:151], v[220:223], v[40:43], v[148:151]\n\t" \
  "v_mfma_f32_16x16x32_bf16 v[156:159], v[220:223], v[44:47], v[156:159]\n\t" \
  "v_mfma_f32_16x16x32_bf16 v[144:147], v[224:227], v[48:51], v[144:147]\n\t" \
  "v_mfma_f32_16x16x32_bf16 v[152:155], v[224:227], v[52:55], v[152:155]\n\t" \
  "v_mfma_f32_16x16x32_bf16 v[148:151], v[228:231], v[48:51], v[148:151]\n\t" \
  "v_mfma_f32_16x16x32_bf16 v[156:159], v[228:231], v[52:55], v[156:159]\n\t" \
  "v_mfma_f32_16x16x32_bf16 v[144:147], v[232:235], v[56:59], v[144:147]\n\t" \
  "v_mfma_f32_16x16x32_bf16 v[152:155], v[232:235], v[60:63], v[152:155]\n\t" \
  "v_mfma_f32_16x16x32_bf16 v[148:151], v[236:239], v[56:59], v[148:151]\n\t" \
  "v_mfma_f32_16x16x32_bf16 v[156:159], v[236:239], v[60:63], v[156:159]\n\t" \
  "s_setprio 0\n\t" \
  "s_add_u32 s92, s92, 65024\n\t" \
  "s_addc_u32 s93, s93, 0\n\t" \
  "ds_read_b128 v[160:163], v253 offset:0\n\t" \
  "ds_read_b128 v[164:167], v253 offset:1024\n\t" \
  "ds_read_b128 v[176:179], v244 offset:32768\n\t" \
  "ds_read_b128 v[180:183], v244 offset:49152\n\t" \
  "ds_read_b128 v[184:187], v245 offset:32768\n\t" \
  "ds_read_b128 v[188:191], v245 offset:49152\n\t" \
  "s_add_u32 m0, %[lw], 65536\n\t" \
  "s_nop 0\n\t" \
  "global_load_lds_dwordx4 v248, s[92:93] sc1\n\t" \
  "ds_read_b128 v[192:195], v246 offset:32768\n\t" \
  "ds_read_b128 v[196:199], v246 offset:49152\n\t" \
  "ds_read_b128 v[200:203], v247 offset:32768\n\t" \
  "ds_read_b128 v[204:207], v247 offset:49152\n\t" \
  "s_add_u32 m0, %[lw], 66560\n\t" \
  "s_nop 0\n\t" \
  "global_load_lds_dwordx4 v249, s[92:93] sc1\n\t" \
  "ds_read_b128 v[208:211], v244 offset:33024\n\t" \
  "ds_read_b128 v[212:215], v244 offset:49408\n\t" \
  "ds_read_b128 v[216:219], v245 offset:33024\n\t" \
  "ds_read_b128 v[220:223], v245 offset:49408\n\t" \
  "s_add_u32 m0, %[lw], 67584\n\t" \
  "s_nop 0\n\t" \
  "global_load_lds_dwordx4 v250, s[92:93] sc1\n\t" \
  "ds_read_b128 v[224:227], v246 offset:33024\n\t" \
  "ds_read_b128 v[228:231], v246 offset:49408\n\t" \
  "ds_read_b128 v[232:235], v247 offset:33024\n\t" \
  "ds_read_b128 v[236:239], v247 offset:49408\n\t" \
  "s_add_u32 m0, %[lw], 68608\n\t" \
  "s_nop 0\n\t" \
  "global_load_lds_dwordx4 v251, s[92:93] sc1\n\t" \
  "s_waitcnt lgkmcnt(15)\n\t" \
  "s_waitcnt lgkmcnt(12)\n\t" \
  "s_sub_i32 s91, s86, 2\n\t" \
  "v_mov_b32 v173, s91\n\t" \
  "buffer_store_dword v173, v174, %[rs], 0 offen sc1\n\t" \
  "s_lshr_b32 s90, s87, 7\n\t" \
  "s_xor_b32 s90, s90, %[s0]\n\t" \
  "s_and_b32 s90, s90, 1\n\t" \
  "s_lshl1_add_u32 s90, s87, s90\n\t" \
  "s_lshl_b32 s90, s90, 13\n\t" \
  "v_add_f32 v160, v160, v128\n\t" \
  "v_add_f32 v161, v161, v129\n\t" \
  "v_add_f32 v162, v162, v130\n\t" \
  "v_add_f32 v163, v163, v131\n\t" \
  "v_add_f32 v164, v164, v132\n\t" \
  "v_add_f32 v165, v165, v133\n\t" \
  "v_add_f32 v166, v166, v134\n\t" \
  "v_add_f32 v167, v167, v135\n\t" \
  "v_cvt_pk_bf16_f32 v168, v160, v161\n\t" \
  "v_cvt_pk_bf16_f32 v169, v162, v163\n\t" \
  "v_cvt_pk_bf16_f32 v170, v164, v165\n\t" \
  "v_cvt_pk_bf16_f32 v171, v166, v167\n\t" \
  "buffer_store_dwordx4 v[168:171], v172, %[rt], s90 offen sc1\n\t" \
  "s_add_u32 s86, s86, 1\n\t" \
  "s_add_u32 s87, s87, 1\n\t" \
  "s_waitcnt lgkmcnt(0)\n\t" \
  "s_cmp_lt_u32 s86, 64\n\t" \
  "s_cbranch_scc1 .Lp1%=\n\t" \
  "s_barrier\n\t" \
  "s_setprio 2\n\t" \
  "v_mfma_f32_16x16x32_bf16 v[144:147], v[176:179], v[64:67], v[144:147]\n\t" \
  "v_mfma_f32_16x16x32_bf16 v[152:155], v[176:179], v[68:71], v[152:155]\n\t" \
  "v_mfma_f32_16x16x32_bf16 v[148:151], v[180:183], v[64:67], v[148:151]\n\t" \
  "v_mfma_f32_16x16x32_bf16 v[156:159], v[180:183], v[68:71], v[156:159]\n\t" \
  "v_mfma_f32_16x16x32_bf16 v[144:147], v[184:187], v[72:75], v[144:147]\n\t" \
  "v_mfma_f32_16x16x32_bf16 v[152:155], v[184:187], v[76:79], v[152:155]\n\t" \
  "v_mfma_f32_16x16x32_bf16 v[148:151], v[188:191], v[72:75], v[148:151]\n\t" \
  "v_mfma_f32_16x16x32_bf16 v[156:159], v[188:191], v[76:79], v[156:159]\n\t" \
  "v_mfma_f32_16x16x32_bf16 v[144:147], v[192:195], v[80:83], v[144:147]\n\t" \
  "v_mfma_f32_16x16x32_bf16 v[152:155], v[192:195], v[84:87], v[152:155]\n\t" \
  "v_mfma_f32_16x16x32_bf16 v[148:151], v[196:199], v[80:83], v[148:151]\n\t" \
  "v_mfma_f32_16x16x32_bf16 v[156:159], v[196:199], v[84:87], v[156:159]\n\t" \
  "v_mfma_f32_16x16x32_bf16 v[144:147], v[200:203], v[88:91], v[144:147]\n\t" \
  "v_mfma_f32_16x16x32_bf16 v[152:155], v[200:203], v[92:95], v[152:155]\n\t" \
  "v_mfma_f32_16x16x32_bf16 v[148:151], v[204:207], v[88:91], v[148:151]\n\t" \
  "v_mfma_f32_16x16x32_bf16 v[156:159], v[204:207], v[92:95], v[156:159]\n\t" \
  "v_mfma_f32_16x16x32_bf16 v[144:147], v[208:211], v[96:99], v[144:147]\n\t" \
  "v_mfma_f32_16x16x32_bf16 v[152:155], v[208:211], v[100:103], v[152:155]\n\t" \
  "v_mfma_f32_16x16x32_bf16 v[148:151], v[212:215], v[96:99], v[148:151]\n\t" \
  "v_mfma_f32_16x16x32_bf16 v[156:159], v[212:215], v[100:103], v[156:159]\n\t" \
  "v_mfma_f32_16x16x32_bf16 v[144:147], v[216:219], v[104:107], v[144:147]\n\t" \
  "v_mfma_f32_16x16x32_bf16 v[152:155], v[216:219], v[108:111], v[152:155]\n\t" \
  "v_mfma_f32_16x16x32_bf16 v[148:151], v[220:223], v[104:107], v[148:151]\n\t" \
  "v_mfma_f32_16x16x32_bf16 v[156:159], v[220:223], v[108:111], v[156:159]\n\t" \
  "v_mfma_f32_16x16x32_bf16 v[144:147], v[224:227], v[112:115], v[144:147]\n\t" \
  "v_mfma_f32_16x16x32_bf16 v[152:155], v[224:227], v[116:119], v[152:155]\n\t" \
  "v_mfma_f32_16x16x32_bf16 v[148:151], v[228:231], v[112:115], v[148:151]\n\t" \
  "v_mfma_f32_16x16x32_bf16 v[156:159], v[228:231], v[116:119], v[156:159]\n\t" \
  "v_mfma_f32_16x16x32_bf16 v[144:147], v[232:235], v[120:123], v[144:147]\n\t" \
  "v_mfma_f32_16x16x32_bf16 v[152:155], v[232:235], v[124:127], v[152:155]\n\t" \
  "v_mfma_f32_16x16x32_bf16 v[148:151], v[236:239], v[120:123], v[148:151]\n\t" \
  "v_mfma_f32_16x16x32_bf16 v[156:159], v[236:239], v[124:127], v[156:159]\n\t" \
  "s_setprio 0\n\t" \
  "s_nop 4\n\t" \
  "ds_write_b128 v252, v[152:155] offset:0\n\t" \
  "s_nop 0\n\t" \
  "ds_write_b128 v252, v[156:159] offset:1024\n\t" \
  "s_waitcnt lgkmcnt(0)\n\t" \
  "s_barrier\n\t" \
  "ds_read_b128 v[160:163], v253 offset:0\n\t" \
  "ds_read_b128 v[164:167], v253 offset:1024\n\t" \
  "s_waitcnt lgkmcnt(0)\n\t" \
  "s_lshr_b32 s90, s87, 7\n\t" \
  "s_xor_b32 s90, s90, %[s0]\n\t" \
  "s_and_b32 s90, s90, 1\n\t" \
  "s_lshl1_add_u32 s90, s87, s90\n\t" \
  "s_lshl_b32 s90, s90, 13\n\t" \
  "v_add_f32 v160, v160, v144\n\t" \
  "v_add_f32 v161, v161, v145\n\t" \
  "v_add_f32 v162, v162, v146\n\t" \
  "v_add_f32 v163, v163, v147\n\t" \
  "v_add_f32 v164, v164, v148\n\t" \
  "v_add_f32 v165, v165, v149\n\t" \
  "v_add_f32 v166, v166, v150\n\t" \
  "v_add_f32 v167, v167, v151\n\t" \
  "v_cvt_pk_bf16_f32 v168, v160, v161\n\t" \
  "v_cvt_pk_bf16_f32 v169, v162, v163\n\t" \
  "v_cvt_pk_bf16_f32 v170, v164, v165\n\t" \
  "v_cvt_pk_bf16_f32 v171, v166, v167\n\t" \
  "buffer_store_dwordx4 v[168:171], v172, %[rt], s90 offen sc1\n\t" \
  "s_waitcnt vmcnt(0)\n\t" \
  "s_barrier\n\t" \
  "s_sub_i32 s91, s86, 1\n\t" \
  "v_mov_b32 v173, s91\n\t" \
  "buffer_store_dword v173, v174, %[rs], 0 offen sc1\n\t" \
  "s_mov_b32 m0, s94\n\t" \
  ".Le%=:\n\t"
constexpr int W_X = 131072, W_PM = W_X + 16384;
template <bool ISV>
__device__ __forceinline__ void job_proj(Args& p, int g, int which, int h, char* smem, __amdgpu_buffer_rsrc_t rsy) {
  const int tid = threadIdx.x, lane = tid & 63, r = lane & 15, q = lane >> 4;
  const int w = __builtin_amdgcn_readfirstlane(tid >> 6), kh = w >> 2, cg = w & 3;
  u32x32 W[4];
  const unsigned lw = lds_off(smem) + w * 4096;
  u32x4 vx;
  u32x2 tag;
  const char* xn = p.ws + WS_XN;
  auto row = [&](int t, int i) {
    const char* src = xn + (size_t)(min(t, 127) >> 1) * (SEG * XLD * 2) + (t & 1) * 512;
    unsigned keep;
    asm volatile("s_mov_b32 %0, m0\n\ts_mov_b32 m0, %3\n\ts_nop 0\n\tglobal_load_lds_dwordx4 %1, %2 sc1\n\ts_mov_b32 m0, %0"
                 : "=&s"(keep) : "v"(vx[i]), "s"(src), "s"(lw + (t & 3) * 32768 + i * 1024) : "memory");
  };
  {
    const u16* Wm = which == 0 ? p.Wk : p.Wq;
    asm volatile("" : "+s"(Wm));
    if (ISV) Wm = p.Wv;
    const unsigned vo = (tid >> 4) * (HD * 2) + 16 * ((lane & 15) ^ swzB((tid >> 4) & 15));
    auto stage = [&](int j) {
      const char* src = (const char*)(Wm + (size_t)(WREV ? 7 - j : j) * 64 * HD + h * DH);
      const unsigned dst = lds_off(smem) + (j & 3) * 32768 + w * 1024;
      glds2(src, 32 * HD * 2, vo, dst);
      if (TH_P < 63) HOLD_V((h & 3) ^ 1, TH_P);
      glds2(src + 512 * HD * 2, 32 * HD * 2, vo, dst + 16384);
      if (TH_P < 63) HOLD_V((h & 3) ^ 1, TH_P);
    };
    if (which == 1) { for (int z = 0; z < Q_DELAY; ++z) __builtin_amdgcn_s_sleep(16); }
    const unsigned own = min((h & LN_M) ^ LN_X, 1);
    tag = start_rows<16>(p, 32 + g, own, [&]() { stage(0); stage(1); stage(2); stage(3); });
#pragma unroll
    for (int i = 0; i < 4; ++i) vx[i] = ((4 * w + i) ^ (ISV ? 0 : (lane >> 1) & 16)) * (XLD * 2) + (lane >> 5) * 1024 + (((lane & 31) ^ ((4 * w + i) & 15)) << 4);
    Boot bt;
#pragma unroll
    for (int j = 0; j < 8; ++j) {
      if (j < 4) WAIT_V(16); else if (j == 4) WAIT_V(12); else if (j == 5) WAIT_V(8); else if (j == 6) WAIT_V(4); else WAIT_V(10);
      if (j == 6 && w == 0) lds_at(smem + W_PM)[0] = boot_done(p.ws, bt, true, tag);
      LBAR();
      if (j == 4 && own) report_rows(p, 32 + g, tag);
      const char* sb = smem + (j & 3) * 32768 + kh * 16384;
#pragma unroll
      for (int k = 0; k < 4; ++k) {
        const int ks = k >> 1, a = k & 1;
        const unsigned ch = ISV ? 2 * (2 * cg + (a ^ kh)) + ((r & 3) >> 1) : 4 * cg + (r & 3), by = ISV ? 8 * (r & 1) : 8 * a;
        s16x4 lo = __builtin_amdgcn_ds_read_tr16_b64_v4i16((lds_s16x4_ptr)(sb + offB(32 * ks + 8 * q + (r >> 2), ch) + by));
        s16x4 hi = __builtin_amdgcn_ds_read_tr16_b64_v4i16((lds_s16x4_ptr)(sb + offB(32 * ks + 8 * q + 4 + (r >> 2), ch) + by));
        const int f = 4 * (WREV ? 7 - j : j) + k;
        put4(W[f >> 3], f & 7, (bf16x8){lo[0], lo[1], lo[2], lo[3], hi[0], hi[1], hi[2], hi[3]});
      }
      LBAR();
      if (j == 3 && w == 0) boot_ask(p.ws, bt);
      if (j + 4 < 8) stage(j + 4);
      if (j == 6) {
#pragma unroll
        for (int i = 0; i < 10; ++i) row(i >> 2, i & 3);
      }
    }
    p.s0 = uni(lds_at(smem + W_PM)[0] >> 31);
  }
  RDY(128 + g);
  typedef __attribute__((ext_vector_type(8))) unsigned u32x8;
  u32x8 la;
#pragma unroll
  for (int i = 0; i < 8; ++i) la[i] = (i >> 2) * 65536 + r * 1024 + kh * 512 + (((4 * (i & 3) + q) ^ r) << 4);
  const unsigned xw = W_X + w * 2048 + lane * 16;
  u32x32 acc;
  asm volatile("" : "={v[128:159]}"(acc));
  WAIT_V(6);
  LBAR();
  {
    const int g0 = 0;
#define PR_LOOP(TXT)                                                                                                              \
    asm volatile(TXT                                                                                                              \
        : "+{v[128:159]}"(acc)                                                                                                    \
        : W4_IN, "{v[208:215]}"(la), "{v[216:219]}"(vx), "{v220}"(xw), "{v221}"(xw ^ 8192),                                       \
          "{v172}"((ISV ? 2 * cg + kh : 4 * kh + cg) * 1024 + lane * 16), "{v174}"(tid == 256 ? fkq(h, which) : 0x7F000000),      \
          [rt] "s"(raw_rsrc(p.ws + WS_T, T_BYTES)), [rs] "s"(raw_rsrc(p.ws + WS_SY, SY_TOTAL)), [s0] "s"(p.s0 ^ TF_KQV),          \
          [lw] "s"(lw), [kh] "s"(kh), [g0] "s"(g0), [end] "s"(g0 ? NSEG : PR_G1), [x0] "s"(xn + (g0 + 1) * (SEG * XLD * 2)),                       \
          [t0] "s"(which * T_Q + kseg(0, h) - 1)                                                                                  \
        : CLB8(160, 161, 162, 163, 164, 165, 166, 167), "v168", "v169", "v170", "v171", "v173", CLB8(176, 177, 178, 179, 180, 181, 182, 183), \
          CLB8(184, 185, 186, 187, 188, 189, 190, 191), CLB8(192, 193, 194, 195, 196, 197, 198, 199),                             \
          CLB8(200, 201, 202, 203, 204, 205, 206, 207), "s86", "s87", "s88", "s90", "s91", "s92", "s93", "s94", "scc", "memory")
    if (ISV) PR_LOOP(PR_ASM_1); else PR_LOOP(PR_ASM_0);
    if (w == 0 && g0 == 0) wait_rows(p.ws, tag);
  }
#define PP_LOOP(TXT)                                                                                                              \
  asm volatile(TXT                                                                                                                \
      : "+{v[128:159]}"(acc)                                                                                                      \
      : W4_IN, "{v[240:247]}"(la), "{v[248:251]}"(vx), "{v252}"(xw), "{v253}"(xw ^ 8192),                                         \
        "{v172}"((ISV ? 2 * cg + kh : 4 * kh + cg) * 1024 + lane * 16), "{v174}"(tid == 256 ? fkq(h, which) : 0x7F000000),        \
        [rt] "s"(raw_rsrc(p.ws + WS_T, T_BYTES)), [rs] "s"(raw_rsrc(p.ws + WS_SY, SY_TOTAL)), [s0] "s"(p.s0 ^ TF_KQV),            \
        [lw] "s"(lw), [kh] "s"(kh), [g0] "n"(PR_G1), [x0] "s"(xn + (PR_G1 + 1) * (SEG * XLD * 2)),                                \
        [t0] "s"(which * T_Q + kseg(0, h) - 1)                                                                                    \
      : CLB8(160, 161, 162, 163, 164, 165, 166, 167), "v168", "v169", "v170", "v171", "v173", CLB8(176, 177, 178, 179, 180, 181, 182, 183), \
        CLB8(184, 185, 186, 187, 188, 189, 190, 191), CLB32(192), CLB8(224, 225, 226, 227, 228, 229, 230, 231),                   \
        CLB8(232, 233, 234, 235, 236, 237, 238, 239), "s86", "s87", "s90", "s91", "s92", "s93", "s94", "scc", "memory")
  if (ISV) PP_LOOP(PP_ASM_1); else PP_LOOP(PP_ASM_0);
}

enum { J_NONE = 0, J_GU = 3, J_PR = 5, J_CB = 8, J_DN = 14 };
struct BigJob { int type, panel, idx; };
struct BigPtr { const char *a, *b0, *b1; };
constexpr int G_LDA = XLD * 2, G_LDB = FF * 2;

__device__ __forceinline__ BigPtr big_ptrs(const Args& p, const BigJob& j) {
  BigPtr r;
  r.a = p.ws + WS_XN + (size_t)j.panel * 128 * G_LDA;
  r.b0 = (const char*)(p.wg + j.idx * 128); r.b1 = (const char*)(p.wu + j.idx * 128);
  return r;
}
__device__ __forceinline__ void big_publish(const Args& p, const BigJob& j) {
  int tid = threadIdx.x;
  asm volatile("" : "+v"(tid));
  if (j.type == J_GU && tid == 0)
    __hip_atomic_fetch_add((gu32*)(p.ws + WS_SY + SY_CH) + j.panel * 16, 1u, __ATOMIC_RELAXED, __HIP_MEMORY_SCOPE_AGENT);
}
typedef f32x4 Acc[2][2][4][2];
__device__ __forceinline__ u32x4 pack32(f32x4 a, f32x4 b) {
  return widen32(cvtpk(a[0], a[1]), cvtpk(a[2], a[3]), cvtpk(b[0], b[1]), cvtpk(b[2], b[3]));
}
#define st_epi(r, o, v) st_sc1(r, o, v)
__device__ __forceinline__ void big_epilogue(const Args& p, const BigJob& j, Acc& acc, char* smem) {
  int tid = threadIdx.x;
  asm volatile("" : "+v"(tid));
  const int lane = tid & 63;
  const int wid = __builtin_amdgcn_readfirstlane(tid >> 6);
  const int wr = wid >> 2, wc = wid & 3;
  {
    const __amdgpu_buffer_rsrc_t ro = mkrsrc(p.ws + WS_HF, S_LEN * 16384);
#pragma unroll
    for (int i = 0; i < 8; ++i) {
      f32x4 hh[2];
#pragma unroll
      for (int n = 0; n < 2; ++n)
#pragma unroll
        for (int jj = 0; jj < 4; ++jj) {
          const float g = acc[i >> 2][0][i & 3][n][jj], u = acc[i >> 2][1][i & 3][n][jj];
          hh[n][jj] = __builtin_amdgcn_rcpf(1.0f + __builtin_amdgcn_exp2f(g * -1.4426950408889634f)) * u;
        }
      st_epi(ro, h_off(p, j.panel + (i >> 2)) + (j.idx * 4 + wc) * 16384 + (wr * 4 + (i & 3)) * 1024 + lane * 16,
             ((u32x4){cvtpk(hh[0][0], hh[0][1]), cvtpk(hh[0][2], hh[0][3]), cvtpk(hh[1][0], hh[1][1]), cvtpk(hh[1][2], hh[1][3])}));
    }
  }
}
#define JST(n, i, v) do {} while (0)
__device__ __forceinline__ void gemm_role(Args& p, int g, char* smem) {
  const __amdgpu_buffer_rsrc_t rsy = mkrsrc(p.ws + WS_SY, SY_TOTAL);
  const int x = g & 3, k = g >> 2;
  volatile unsigned* slot = reinterpret_cast<volatile unsigned*>(smem + 163824);
  int rnd = k < 24 ? 2 : -1;
  int nj = 0; (void)nj;
  auto claim = [&]() {
    ++rnd;
    const int n = k < 24 ? 32 + k : rnd * 8 + k - 24, pn = (n * 37) >> 8;
    BigJob b;
    b.type = rnd < 4 ? J_GU : (rnd == 4 ? J_DN : (rnd == 5 && k >= 24 && k - 24 < 14 - NFUSE ? J_CB : J_NONE));
    b.panel = rnd < 4 ? 14 - 2 * pn : (rnd == 4 ? 4 * ((2 - (k >> 3)) & 3) + x : NFUSE + k - 24);
    b.idx = rnd < 4 ? x + 4 * (n - 7 * pn) : k & 7;
    return b;
  };
  if (g < 4 && threadIdx.x == 0) { LST(480 + 4 * g); LSTC(480 + 4 * g + 1); }
  if (k < 24) {
    JST(nj, 0, J_PR); JST(nj, 1, __builtin_amdgcn_s_memrealtime());
    if (k >= 16) job_proj<true>(p, g, 2, x * 8 + (k & 7), smem, rsy);
    else job_proj<false>(p, g, k >> 3, x * 8 + (k & 7), smem, rsy);
    JST(nj, 3, __builtin_amdgcn_s_memrealtime()); ++nj;
  } else {
    const unsigned t0 = (unsigned)__builtin_amdgcn_s_memrealtime();
    u32x2 tag;
#pragma unroll 1
    for (int u = 0; u < (k < 30 && LN_M ? 2 : 1); ++u) {
      const int rg = u ? 32 + (4 * (k - 24) + 1) * 4 + x : g - 96;
      tag = start_rows<0>(p, rg, 1, []() {});
      WAIT_V(0);
      LBAR();
      report_rows(p, rg, tag);
    }
    while ((unsigned)__builtin_amdgcn_s_memrealtime() - t0 < PF_T0) __builtin_amdgcn_s_sleep(8);
#pragma unroll 1
    for (int i = GPF == 2 ? 2 : 0; i < PF_N; i += GPF == 2 ? 3 : 1) {
      const int m = i % 3, o = ((i / 3) * 32 + g - 96) * 448 + (int)threadIdx.x;
      if (threadIdx.x < 448) touch_line((const char*)(m == 0 ? p.wg : (m == 1 ? p.wu : p.wd)) + (size_t)o * 128, smem + (threadIdx.x >> 6) * 256);
      WAIT_V(1);
    }
    WAIT_V(0);
    if (threadIdx.x < 64) {
      wait_rows(p.ws, tag);
      Boot bt;
      boot_ask(p.ws, bt);
      WAIT_V(0);
      *slot = boot_done(p.ws, bt, true, tag);
      while ((unsigned)__builtin_amdgcn_s_memrealtime() - t0 < GU_DELAY) __builtin_amdgcn_s_sleep(8);
    }
    LBAR();
    p.s0 = *slot >> 31;
    RDY(128 + g);
  }
  p.s0 = uni(p.s0);
  BigJob cur = claim();
  while (cur.type != J_NONE) {
    JST(nj, 0, cur.type | cur.panel << 8); JST(nj, 1, __builtin_amdgcn_s_memrealtime());
    if (cur.type != J_GU) {
      if (cur.type == J_DN) job_down(p, cur.panel, cur.idx, smem, rsy);
      else job_comb(p, cur.panel, x, rsy);
      JST(nj, 3, __builtin_amdgcn_s_memrealtime()); ++nj;
      cur = claim();
      continue;
    }
    int tid = threadIdx.x;
    asm volatile("" : "+v"(tid));
    const int lane = tid & 63;
    const int wid = __builtin_amdgcn_readfirstlane(tid >> 6);
    const int wr = wid >> 2, wc = wid & 3;
    const unsigned ra = tid >> 3, ca = 16 * ((tid & 7) ^ ((tid >> 3) & 7)), rb = tid >> 4, cb = 16 * ((tid & 15) ^ swzB((tid >> 4) & 15));
    const unsigned lw = lds_off(smem) + wid * 1024;
#define B8_SA(b, h) (((b) * 2 + (h)) * 16384)
#define B8_SB(b, h) ((4 + (b) * 2 + (h)) * 16384)
#define B8_STA(b, h, P, kt) glds2<true>((P).a + (size_t)(h) * 128 * G_LDA + (kt) * 128, 64 * G_LDA, ra * G_LDA + ca, lw + B8_SA(b, h))
#define B8_STB(b, h, P, kt) glds2(((h) ? (P).b1 : (P).b0) + (size_t)(kt) * 64 * G_LDB, 32 * G_LDB, rb * G_LDB + cb, lw + B8_SB(b, h))
#define B8_LDA(b, h) do { _Pragma("unroll") for (int m = 0; m < 4; ++m) _Pragma("unroll") for (int kk = 0; kk < 2; ++kk) \
      At[m][kk] = ldsf(smem + B8_SA(b, h) + offA(wr * 64 + m * 16 + (lane & 15), kk * 4 + (lane >> 4))); } while (0)
#define B8_LDB(dst, b, h) do { _Pragma("unroll") for (int n = 0; n < 2; ++n) _Pragma("unroll") for (int kk = 0; kk < 2; ++kk) \
      dst[n][kk] = ld_bfrag(smem + B8_SB(b, h), kk, wc * 2 + n, lane); } while (0)
#define B8_MMA(ai, bj, Bt) do { __builtin_amdgcn_s_setprio(1); _Pragma("unroll") for (int m = 0; m < 4; ++m) _Pragma("unroll") for (int n = 0; n < 2; ++n) \
      _Pragma("unroll") for (int kk = 0; kk < 2; ++kk) acc[ai][bj][m][n] = MFMA(Bt[n][kk], At[m][kk], acc[ai][bj][m][n]); __builtin_amdgcn_s_setprio(0); } while (0)
#define WAIT_L(n) asm volatile("s_waitcnt lgkmcnt(%0)" :: "n"(n) : "memory")
#define BAR() __builtin_amdgcn_s_barrier()
#define WAIT_RX(c) asm volatile("s_waitcnt vmcnt(16)\n\ts_cmp_lg_u32 %0, 0\n\ts_cbranch_scc1 .Lrx%=\n\ts_waitcnt vmcnt(8)\n.Lrx%=:" :: "s"(c) : "memory", "scc")
    Acc acc;
    bf16x8 At[4][2], B0[2][2], B1[2][2];
    BigPtr cp = big_ptrs(p, cur);
    BigJob pend; pend.type = J_NONE; pend.panel = 0; pend.idx = 0;
    LBAR();
    B8_STB(0, 0, cp, 0); B8_STB(0, 1, cp, 0); B8_STA(0, 0, cp, 0); B8_STA(0, 1, cp, 0);
    if (wr == 1) BAR();
    WAIT_V(2); BAR();
    B8_STB(1, 0, cp, 1); B8_STA(1, 0, cp, 1); B8_STB(1, 1, cp, 1);
    WAIT_V(6); BAR();
    BigJob nx = pend;
    for (;;) {
      bool chain = false;
      BigPtr np = cp;
      constexpr int nt = DM / 64;
#pragma unroll
      for (int i = 0; i < 32; ++i) acc[i >> 4][(i >> 3) & 1][(i >> 1) & 3][i & 1] = (f32x4){0.f, 0.f, 0.f, 0.f};
#pragma unroll 1
      for (int t = 0; t < nt; t += 2) {
        const bool last = t == nt - 2;
        if (last) {
          nx = claim();
          chain = nx.type == J_GU;
          if (chain) np = big_ptrs(p, nx);
        }
        const BigPtr sp = np;
        const int t2 = last ? 0 : t + 2;
        if (t == 2) big_publish(p, pend);
        const int rx = __builtin_amdgcn_readfirstlane(t == 0 && pend.type != J_NONE);
        B8_LDB(B0, 0, 0); B8_LDB(B1, 0, 1); SCHED(); B8_LDA(0, 0); B8_STA(1, 1, cp, t + 1);
        WAIT_RX(rx);
        WAIT_L(0); BAR(); B8_MMA(0, 0, B0); B8_MMA(0, 1, B1); BAR(); SCHED();
        B8_LDA(0, 1); B8_STB(0, 0, sp, t2); B8_STB(0, 1, sp, t2); B8_STA(0, 0, sp, t2);
        WAIT_RX(rx);
        WAIT_L(0); BAR(); B8_MMA(1, 0, B0); B8_MMA(1, 1, B1); BAR(); SCHED();
        B8_LDB(B0, 1, 0); B8_LDB(B1, 1, 1); SCHED(); B8_LDA(1, 0); B8_STA(0, 1, sp, t2);
        WAIT_V(8); WAIT_L(0); BAR(); B8_MMA(0, 0, B0); B8_MMA(0, 1, B1); BAR(); SCHED();
        B8_LDA(1, 1); B8_STB(1, 0, sp, t2 + 1); B8_STB(1, 1, sp, t2 + 1); B8_STA(1, 0, sp, t2 + 1);
        WAIT_V(8); WAIT_L(0); BAR(); B8_MMA(1, 0, B0); B8_MMA(1, 1, B1); BAR(); SCHED();
      }
      if (wr == 0) BAR();
      JST(nj, 2, __builtin_amdgcn_s_memrealtime());
      big_epilogue(p, cur, acc, smem);
      if (!chain) break;
      JST(nj, 3, __builtin_amdgcn_s_memrealtime()); ++nj;
      pend = cur; cur = nx; cp = np;
      if (wr == 1) BAR();
      JST(nj, 0, cur.type | cur.panel << 8); JST(nj, 1, __builtin_amdgcn_s_memrealtime());
    }
    WAIT_V(0);
    BAR();
    asm volatile(".Lcode_end:");
    big_publish(p, cur);
    JST(nj, 3, __builtin_amdgcn_s_memrealtime()); ++nj;
    cur = nx;
  }
  if (threadIdx.x == 0) { LST(128 + g); }
  if (g < 4 && threadIdx.x == 0) { LST(480 + 4 * g + 2); LSTC(480 + 4 * g + 3); }
}

__device__ __forceinline__ unsigned now_q() { return (unsigned)__builtin_amdgcn_s_memrealtime() * 4u; }
__device__ __forceinline__ void pace_until(unsigned t) {
#pragma unroll 1
  for (int i = 0; i < 256; ++i) {
    const int d = (int)(t - now_q());
    if (d <= 0) break;
    if (d > 32) __builtin_amdgcn_s_sleep(1);
  }
}
#define CLB_E "s76", "s77", "s78", "s79", "s80", "s81", "s82", "s83", "s84", "s85", "s86", "s87", "s88", "s89"
#define EDGE_ASM \
  "s_memtime s[76:77]\n\t" \
  "s_memrealtime s[78:79]\n\t" \
  "s_nop 3\n\t" \
  "s_memrealtime s[80:81]\n\t" \
  "s_nop 3\n\t" \
  "s_memrealtime s[82:83]\n\t" \
  "s_nop 3\n\t" \
  "s_memrealtime s[84:85]\n\t" \
  "s_nop 3\n\t" \
  "s_memrealtime s[86:87]\n\t" \
  "s_nop 3\n\t" \
  "s_memrealtime s[88:89]\n\t" \
  "s_mov_b32 s93, 0\n\t" \
  "s_waitcnt lgkmcnt(0)\n\t" \
  "s_cmp_eq_u32 s80, s78\n\t" \
  "s_addc_u32 s93, s93, 0\n\t" \
  "s_cmp_eq_u32 s82, s78\n\t" \
  "s_addc_u32 s93, s93, 0\n\t" \
  "s_cmp_eq_u32 s84, s78\n\t" \
  "s_addc_u32 s93, s93, 0\n\t" \
  "s_cmp_eq_u32 s86, s78\n\t" \
  "s_addc_u32 s93, s93, 0\n\t" \
  "s_cmp_eq_u32 s88, s78\n\t" \
  "s_addc_u32 s93, s93, 0\n\t" \
  "s_mul_i32 s93, s93, 21\n\t" \
  "s_add_u32 s76, s76, 15\n\t" \
  "s_add_u32 %[kc], s76, s93\n\t" \
  "s_lshl2_add_u32 %[kq], s78, 16\n\t"
__device__ __forceinline__ unsigned time_of(unsigned cyc) {
  unsigned c, q;
  asm volatile(EDGE_ASM : [kc] "=&s"(c), [kq] "=&s"(q) :: CLB_E, "s93", "scc");
  return q - (((c - cyc) * 11900u) >> 16);
}
#define DS_RED 20
#define DS_MEM 18
#define LEAD_DN 2
#define LEAD_UP 12
__device__ __forceinline__ u32x2 ld2_sc1(__amdgpu_buffer_rsrc_t rs, int off, int soff = 0) {
  return __builtin_amdgcn_raw_buffer_load_b64(rs, off, soff, 16);
}
__device__ __forceinline__ void st2_sc1(__amdgpu_buffer_rsrc_t rs, int off, u32x2 v) {
  __builtin_amdgcn_raw_buffer_store_b64(v, rs, off, 0, 16);
}
__device__ __forceinline__ int row_max(int v) {
  v = max(v, __builtin_amdgcn_update_dpp(0, v, 0x128, 0xf, 0xf, false));
  v = max(v, __builtin_amdgcn_update_dpp(0, v, 0x124, 0xf, 0xf, false));
  v = max(v, __builtin_amdgcn_update_dpp(0, v, 0x122, 0xf, 0xf, false));
  v = max(v, __builtin_amdgcn_update_dpp(0, v, 0x121, 0xf, 0xf, false));
  return v;
}
__device__ __forceinline__ int wave_max(int v) {
  v = row_max(v);
  return max(max(__builtin_amdgcn_readlane(v, 0), __builtin_amdgcn_readlane(v, 16)),
             max(__builtin_amdgcn_readlane(v, 32), __builtin_amdgcn_readlane(v, 48)));
}
__device__ __forceinline__ int track(int est, int x, int up, int dn, int out) {
  const int d = x - est;
  return est + (d > out ? 2 : max(min(d, up), -dn));
}
#define WSL 12
#define PLAN_SLEEP 2
__device__ __forceinline__ unsigned latest(unsigned t) {
  const unsigned t0 = __builtin_amdgcn_readfirstlane(t);
  return t0 + wave_max((int)(t - t0));
}
#define RL00 265
#define RL0 300
__device__ __forceinline__ void watch(__amdgpu_buffer_rsrc_t rsy, int s, int lane, unsigned t0, const unsigned* tgt_, gu32* abortw, int hs) {
  pace_until(t0);
  const int rl = (int)__builtin_amdgcn_raw_buffer_load_b32(rsy, lane * 128, SY_BR + hs, 16);
  u32x2 ra;
  for (unsigned spins = 0;;) {
    asm volatile("" ::: "memory");
    ra = ld2_sc1(rsy, (lane & 31) * 128, SY_BA + hs);
    const bool all = __all((int)ra[0] >= s);
    SMP(4, s, spins);
    if (all) break;
    if (spins < 3) __builtin_amdgcn_s_sleep(PLAN_SLEEP); else __builtin_amdgcn_s_sleep(WSL);
    if (spin_fail(spins, abortw)) break;
  }
  const unsigned t = latest(ra[0] == (unsigned)s ? ra[1] : t0) + wave_max(rl < 0 ? (s ? RL0 : RL00) : rl) + (DS_RED + DS_MEM);
  lds_vu_ptr tgt = lds_at(tgt_);
  if (lane < 2) tgt[lane] = lane ? t : s + 1;
  SMP(0, s, (int)(t - t0)); SMP(1, s, (int)(t - now_q()));
}
__device__ __forceinline__ unsigned ld_flag(__amdgpu_buffer_rsrc_t rs, int off) {
  return __builtin_amdgcn_raw_buffer_load_b32(rs, off, 0, 16);
}

constexpr unsigned long long QK_MAP[8] = {0xbaf9876d543210ecull, 0xa98f76d543210bceull, 0x98f76d543210ecbaull, 0x876fd543210bcea9ull, 0x76fd543210ecba98ull, 0x65df43210bcea987ull, 0x5df43210ecba9876ull, 0xd43f210bcea98765ull};
#define QK_ASM_0 \
  "s_waitcnt lgkmcnt(0)\n\t" \
  "v_subrev_u32 v244, 0x8000, v250\n\t" \
  "ds_read_b128 v[216:219], v248 offset:32768\n\t" \
  "ds_read_b128 v[220:223], v248 offset:33792\n\t" \
  "ds_read_b128 v[224:227], v248 offset:34816\n\t" \
  "ds_read_b128 v[228:231], v248 offset:35840\n\t" \
  "ds_read_b128 v[208:211], v244 offset:0\n\t" \
  "ds_read_b128 v[212:215], v244 offset:1024\n\t" \
  "ds_read_b128 v[236:239], v244 offset:2048\n\t" \
  "ds_read_b128 v[240:243], v244 offset:3072\n\t" \
  "v_mov_b64 v[192:193], 0\n\t" \
  "v_mov_b64 v[194:195], 0\n\t" \
  "v_mov_b64 v[196:197], 0\n\t" \
  "v_mov_b64 v[198:199], 0\n\t" \
  "s_movk_i32 s92, 0x2000\n\t" \
  ".Lwt%=:\n\t" \
  "ds_read_b64 v[200:201], v248 offset:30720\n\t" \
  "s_memrealtime s[90:91]\n\t" \
  "s_sub_u32 s92, s92, 1\n\t" \
  "s_waitcnt lgkmcnt(0)\n\t" \
  "v_readfirstlane_b32 s93, v200\n\t" \
  "v_readfirstlane_b32 %[tgt], v201\n\t" \
  "s_cbranch_scc1 .Lgo%=\n\t" \
  "s_cmp_eq_u32 s93, %[step]\n\t" \
  "s_cbranch_scc1 .Lwx%=\n\t" \
  "s_sleep 1\n\t" \
  "s_branch .Lwt%=\n\t" \
  ".Lwx%=:\n\t" \
  "s_lshl_b32 s93, s90, 2\n\t" \
  "s_sub_i32 s93, %[tgt], s93\n\t" \
  "s_cmp_le_i32 s93, 0\n\t" \
  "s_cbranch_scc1 .Lgo%=\n\t" \
  "s_min_i32 s93, s93, 8000\n\t" \
  "s_mov_b32 s94, s93\n\t" \
  ".Lsl%=:\n\t" \
  "s_sub_i32 s94, s94, 18\n\t" \
  "s_cmp_lt_i32 s94, 30\n\t" \
  "s_cbranch_scc1 .Lsx%=\n\t" \
  "s_sleep 1\n\t" \
  "s_branch .Lsl%=\n\t" \
  ".Lsx%=:\n\t" \
  "s_sub_i32 s93, s93, 12\n\t" \
  "s_max_i32 s93, s93, 4\n\t" \
  "s_lshr_b32 s93, s93, 2\n\t" \
  "s_add_u32 s93, s93, s90\n\t" \
  "s_memrealtime s[90:91]\n\t" \
  ".Lpace%=:\n\t" \
  "s_memrealtime s[94:95]\n\t" \
  "s_waitcnt lgkmcnt(1)\n\t" \
  "s_sub_i32 s92, s90, s93\n\t" \
  "s_cmp_ge_i32 s92, 0\n\t" \
  "s_cbranch_scc1 .Le%=\n\t" \
  "s_memrealtime s[90:91]\n\t" \
  "s_waitcnt lgkmcnt(1)\n\t" \
  "s_sub_i32 s92, s94, s93\n\t" \
  "s_cmp_lt_i32 s92, 0\n\t" \
  "s_cbranch_scc1 .Lpace%=\n\t" \
  "s_nop 4\n\t" \
  ".Le%=:\n\t" \
  "s_add_u32 s92, s92, s93\n\t" \
  "s_lshl_b32 s92, s92, 2\n\t" \
  "s_sub_i32 s92, %[tgt], s92\n\t" \
  "s_max_i32 s92, s92, 0\n\t" \
  "s_bitcmp0_b32 s92, 3\n\t" \
  "s_cbranch_scc1 .Lf3%=\n\t" \
  "s_nop 15\n\t" \
  ".Lf3%=:\n\t" \
  "s_bitcmp0_b32 s92, 2\n\t" \
  "s_cbranch_scc1 .Lf2%=\n\t" \
  "s_nop 10\n\t" \
  ".Lf2%=:\n\t" \
  ".Lgo%=:\n\t" \
  "s_mov_b32 s92, 0\n\t" \
  ".Lpoll%=:\n\t" \
  "buffer_load_dwordx4 v[200:203], v248, %[rs], %[som] offen sc1\n\t" \
  "buffer_load_dwordx4 v[204:207], v248, %[rs], %[som2] offen sc1\n\t" \
  "s_waitcnt vmcnt(1) lgkmcnt(0)\n\t" \
  "ds_write_b128 v249, v[200:203]\n\t" \
  "v_smfmac_f32_16x16x64_bf16 v[192:195], v[200:203], v[208:215], v251\n\t" \
  "v_max3_u32 v244, v200, v201, v202\n\t" \
  "v_smfmac_f32_16x16x64_bf16 v[196:199], v[200:203], v[236:243], v251\n\t" \
  "s_waitcnt vmcnt(0)\n\t" \
  "ds_write_b128 v249, v[204:207] offset:8192\n\t" \
  "v_smfmac_f32_16x16x64_bf16 v[192:195], v[204:207], v[216:223], v251\n\t" \
  "v_max3_u32 v244, v244, v203, v204\n\t" \
  "v_max3_u32 v244, v244, v205, v206\n\t" \
  "v_smfmac_f32_16x16x64_bf16 v[196:199], v[204:207], v[224:231], v251\n\t" \
  "v_max_u32 v244, v244, v207\n\t" \
  "v_cmp_le_u32 vcc, %[sent], v244\n\t" \
  "s_cbranch_vccnz .Lslow%=\n\t" \
  ".Lok%=:\n\t" \
  "ds_read_b128 v[216:219], v250 offset:32768\n\t" \
  "ds_read_b128 v[220:223], v250 offset:33792\n\t" \
  "ds_read_b128 v[224:227], v250 offset:34816\n\t" \
  "ds_read_b128 v[228:231], v250 offset:35840\n\t" \
  "s_waitcnt lgkmcnt(4)\n\t" \
  "s_barrier\n\t" \
  "ds_read_b128 v[208:211], v248 offset:1024\n\t" \
  "ds_read_b128 v[212:215], v248 offset:2048\n\t" \
  "ds_read_b128 v[236:239], v248 offset:3072\n\t" \
  "ds_read_b128 v[240:243], v248 offset:4096\n\t" \
  "s_waitcnt lgkmcnt(3)\n\t" \
  "ds_read_b128 v[244:247], v248 offset:13312\n\t" \
  "v_smfmac_f32_16x16x64_bf16 v[192:195], v[208:211], v[216:223], v251\n\t" \
  "ds_read_b128 v[216:219], v250 offset:0\n\t" \
  "v_smfmac_f32_16x16x64_bf16 v[196:199], v[208:211], v[224:231], v251\n\t" \
  "s_waitcnt lgkmcnt(4)\n\t" \
  "ds_read_b128 v[220:223], v250 offset:1024\n\t" \
  "v_smfmac_f32_16x16x64_bf16 v[192:195], v[212:215], v[0:7], v251\n\t" \
  "ds_read_b128 v[224:227], v250 offset:2048\n\t" \
  "v_smfmac_f32_16x16x64_bf16 v[196:199], v[212:215], v[8:15], v251\n\t" \
  "s_waitcnt lgkmcnt(5)\n\t" \
  "ds_read_b128 v[228:231], v250 offset:3072\n\t" \
  "v_smfmac_f32_16x16x64_bf16 v[192:195], v[236:239], v[16:23], v251\n\t" \
  "ds_read_b128 v[200:203], v248 offset:5120\n\t" \
  "v_smfmac_f32_16x16x64_bf16 v[196:199], v[236:239], v[24:31], v251\n\t" \
  "s_waitcnt lgkmcnt(6)\n\t" \
  "ds_read_b128 v[204:207], v248 offset:6144\n\t" \
  "v_smfmac_f32_16x16x64_bf16 v[192:195], v[240:243], v[32:39], v251\n\t" \
  "ds_read_b128 v[208:211], v248 offset:7168\n\t" \
  "v_smfmac_f32_16x16x64_bf16 v[196:199], v[240:243], v[40:47], v251\n\t" \
  "s_waitcnt lgkmcnt(5)\n\t" \
  "ds_read_b128 v[212:215], v248 offset:9216\n\t" \
  "v_smfmac_f32_16x16x64_bf16 v[192:195], v[244:247], v[216:223], v251\n\t" \
  "s_waitcnt lgkmcnt(4)\n\t" \
  "v_smfmac_f32_16x16x64_bf16 v[196:199], v[244:247], v[224:231], v251\n\t" \
  "s_waitcnt lgkmcnt(3)\n\t" \
  "ds_read_b128 v[236:239], v248 offset:10240\n\t" \
  "v_smfmac_f32_16x16x64_bf16 v[192:195], v[200:203], v[48:55], v251\n\t" \
  "ds_read_b128 v[240:243], v248 offset:11264\n\t" \
  "v_smfmac_f32_16x16x64_bf16 v[196:199], v[200:203], v[56:63], v251\n\t" \
  "s_waitcnt lgkmcnt(4)\n\t" \
  "v_smfmac_f32_16x16x64_bf16 v[192:195], v[204:207], v[64:71], v251\n\t" \
  "ds_read_b128 v[244:247], v248 offset:12288\n\t" \
  "v_smfmac_f32_16x16x64_bf16 v[196:199], v[204:207], v[72:79], v251\n\t" \
  "s_waitcnt lgkmcnt(4)\n\t" \
  "v_smfmac_f32_16x16x64_bf16 v[192:195], v[208:211], v[80:87], v251\n\t" \
  "ds_read_b128 v[200:203], v248 offset:14336\n\t" \
  "v_smfmac_f32_16x16x64_bf16 v[196:199], v[208:211], v[88:95], v251\n\t" \
  "s_waitcnt lgkmcnt(4)\n\t" \
  "v_smfmac_f32_16x16x64_bf16 v[192:195], v[212:215], v[96:103], v251\n\t" \
  "ds_read_b128 v[204:207], v248 offset:15360\n\t" \
  "v_smfmac_f32_16x16x64_bf16 v[196:199], v[212:215], v[104:111], v251\n\t" \
  "s_waitcnt lgkmcnt(3)\n\t" \
  "v_smfmac_f32_16x16x64_bf16 v[192:195], v[236:239], v[112:119], v251\n\t" \
  "v_smfmac_f32_16x16x64_bf16 v[196:199], v[236:239], v[120:127], v251\n\t" \
  "v_smfmac_f32_16x16x64_bf16 v[192:195], v[240:243], v[128:135], v251\n\t" \
  "v_smfmac_f32_16x16x64_bf16 v[196:199], v[240:243], v[136:143], v251\n\t" \
  "s_waitcnt lgkmcnt(1)\n\t" \
  "v_smfmac_f32_16x16x64_bf16 v[192:195], v[244:247], v[144:151], v251\n\t" \
  "v_smfmac_f32_16x16x64_bf16 v[196:199], v[244:247], v[152:159], v251\n\t" \
  "v_smfmac_f32_16x16x64_bf16 v[192:195], v[200:203], v[160:167], v251\n\t" \
  "v_smfmac_f32_16x16x64_bf16 v[196:199], v[200:203], v[168:175], v251\n\t" \
  "s_waitcnt lgkmcnt(0)\n\t" \
  "v_smfmac_f32_16x16x64_bf16 v[192:195], v[204:207], v[176:183], v251\n\t" \
  "v_smfmac_f32_16x16x64_bf16 v[196:199], v[204:207], v[184:191], v251\n\t" \
  "ds_read_b128 v[200:203], v248 offset:16384\n\t" \
  "ds_read_b128 v[204:207], v248 offset:20480\n\t" \
  "v_mov_b32 v250, v232\n\t" \
  "v_mov_b32 v249, v233\n\t" \
  "s_nop 2\n\t" \
  "v_add_f32 v192, v192, v194\n\t" \
  "v_add_f32 v193, v193, v195\n\t" \
  "v_add_f32 v196, v196, v198\n\t" \
  "v_add_f32 v197, v197, v199\n\t" \
  "v_cvt_pk_bf16_f32 v192, v192, v193\n\t" \
  "v_cvt_pk_bf16_f32 v196, v196, v197\n\t" \
  "ds_write2_b32 v252, v192, v196 offset1:64\n\t" \
  "v_mov_b32 v251, v234\n\t" \
  "ds_read_b64_tr_b16 v[232:233], v250 offset:0\n\t" \
  "ds_read_b64_tr_b16 v[234:235], v250 offset:64\n\t" \
  "v_mov_b32 v212, %[step]\n\t" \
  "s_mov_b64 exec, 1\n\t" \
  "ds_write_b32 v248, v212 offset:28672\n\t" \
  "s_mov_b64 exec, -1\n\t" \
  "buffer_load_dwordx4 v[192:195], v248, %[rt], %[svt] offen offset:0 sc1\n\t" \
  "buffer_load_dwordx4 v[196:199], v248, %[rt], %[svt] offen offset:1024 sc1\n\t" \
  "s_waitcnt lgkmcnt(1)\n\t" \
  "v_mfma_f32_16x16x32_bf16 v[200:203], v[200:203], v[232:235], 0\n\t" \
  "v_mfma_f32_16x16x32_bf16 v[204:207], v[204:207], v[232:235], 0\n\t" \
  "s_nop 7\n\t" \
  "v_cndmask_b32 v216, v200, v204, %[nh]\n\t" \
  "v_cndmask_b32 v217, v201, v205, %[nh]\n\t" \
  "v_cndmask_b32 v218, v202, v206, %[nh]\n\t" \
  "v_cndmask_b32 v219, v203, v207, %[nh]\n\t" \
  "ds_write_b128 v248, v[216:219] offset:16384\n\t" \
  "buffer_load_dword v252, v249, %[rs], %[sv] offen offset:0 sc1\n\t" \
  "buffer_load_dword v253, v249, %[rs], %[sv] offen offset:256 sc1\n\t" \
  "ds_read_b32 v212, v248 offset:28672\n\t" \
  "ds_read_b64_tr_b16 v[236:237], v250 offset:512\n\t" \
  "ds_read_b64_tr_b16 v[238:239], v250 offset:576\n\t" \
  "ds_read_b64_tr_b16 v[240:241], v250 offset:1024\n\t" \
  "ds_read_b64_tr_b16 v[242:243], v250 offset:1088\n\t" \
  "ds_read_b64_tr_b16 v[244:245], v250 offset:1536\n\t" \
  "ds_read_b64_tr_b16 v[246:247], v250 offset:1600\n\t" \
  "s_setprio 3\n\t" \
  "s_waitcnt lgkmcnt(0)\n\t" \
  "v_cmp_ne_u32 vcc, %[step], v212\n\t" \
  "s_barrier\n\t" \
  "s_and_b32 s93, vcc_lo, 0xf\n\t" \
  "s_cbranch_scc1 .Lre%=\n\t" \
  ".Lrb%=:\n\t" \
  "ds_read_b64_tr_b16 v[200:201], v250 offset:2048\n\t" \
  "ds_read_b64_tr_b16 v[202:203], v250 offset:2112\n\t" \
  "ds_read_b64_tr_b16 v[204:205], v250 offset:2560\n\t" \
  "ds_read_b64_tr_b16 v[206:207], v250 offset:2624\n\t" \
  "ds_read_b128 v[220:223], v248 offset:17408\n\t" \
  "ds_read_b128 v[224:227], v248 offset:18432\n\t" \
  "ds_read_b128 v[228:231], v248 offset:19456\n\t" \
  "ds_read_b64_tr_b16 v[208:209], v250 offset:3072\n\t" \
  "ds_read_b64_tr_b16 v[210:211], v250 offset:3136\n\t" \
  "ds_read_b64_tr_b16 v[212:213], v250 offset:3584\n\t" \
  "ds_read_b64_tr_b16 v[214:215], v250 offset:3648\n\t" \
  "s_waitcnt lgkmcnt(9)\n\t" \
  "v_mfma_f32_16x16x32_bf16 v[200:203], v[200:203], v[232:235], 0\n\t" \
  "s_waitcnt lgkmcnt(7)\n\t" \
  "v_mfma_f32_16x16x32_bf16 v[200:203], v[204:207], v[236:239], v[200:203]\n\t" \
  "s_waitcnt lgkmcnt(6)\n\t" \
  "v_pk_add_f32 v[216:217], v[216:217], v[220:221]\n\t" \
  "v_pk_add_f32 v[218:219], v[218:219], v[222:223]\n\t" \
  "s_waitcnt lgkmcnt(4)\n\t" \
  "v_pk_add_f32 v[224:225], v[224:225], v[228:229]\n\t" \
  "v_pk_add_f32 v[226:227], v[226:227], v[230:231]\n\t" \
  "v_pk_add_f32 v[216:217], v[216:217], v[224:225]\n\t" \
  "v_pk_add_f32 v[218:219], v[218:219], v[226:227]\n\t" \
  "s_waitcnt lgkmcnt(2)\n\t" \
  "v_mfma_f32_16x16x32_bf16 v[200:203], v[208:211], v[240:243], v[200:203]\n\t" \
  "v_max3_f32 v224, v216, v217, v218\n\t" \
  "v_max_f32 v224, v224, v219\n\t" \
  "s_waitcnt lgkmcnt(0)\n\t" \
  "v_mfma_f32_16x16x32_bf16 v[200:203], v[212:215], v[244:247], v[200:203]\n\t" \
  "v_max_f32_dpp v225, v224, v224 row_ror:8 row_mask:0xf bank_mask:0xf\n\t" \
  "v_max_f32_dpp v226, v224, v224 row_ror:8 row_mask:0xf bank_mask:0xf\n\t" \
  "s_nop 1\n\t" \
  "v_permlane32_swap_b32 v225, v226\n\t" \
  "v_max_f32 v231, v225, v226\n\t" \
  "v_mov_b32 v220, 0x3f803f80\n\t" \
  "v_mov_b32 v221, 0x3f803f80\n\t" \
  "v_mov_b64 v[222:223], v[220:221]\n\t" \
  "v_mov_b32 v233, 0\n\t" \
  "v_mov_b32 v234, 0\n\t" \
  "v_max3_f32 v204, v200, v201, v231\n\t" \
  "v_max3_f32 v205, v204, v202, v203\n\t" \
  "v_max3_f32 v206, v204, v202, v203\n\t" \
  "v_cndmask_b32 v200, v200, v202, %[up]\n\t" \
  "v_cndmask_b32 v201, v201, v203, %[up]\n\t" \
  "v_permlane16_swap_b32 v205, v206\n\t" \
  "v_max_f32 v204, v205, v206\n\t" \
  "v_mul_f32 v204, %[negc], v204\n\t" \
  "v_fma_f32 v200, v200, %[c], v204\n\t" \
  "v_fma_f32 v201, v201, %[c], v204\n\t" \
  "v_exp_f32 v200, v200\n\t" \
  "v_fma_f32 v216, v216, %[c], v204\n\t" \
  "v_exp_f32 v201, v201\n\t" \
  "v_fma_f32 v217, v217, %[c], v204\n\t" \
  "v_exp_f32 v216, v216\n\t" \
  "v_fma_f32 v218, v218, %[c], v204\n\t" \
  "v_cvt_pk_bf16_f32 v232, v200, v201\n\t" \
  "v_cvt_pk_bf16_f32 v235, v200, v201\n\t" \
  "v_exp_f32 v217, v217\n\t" \
  "v_fma_f32 v219, v219, %[c], v204\n\t" \
  "v_mfma_f32_16x16x16_bf16 v[212:215], v[220:221], v[232:233], 0\n\t" \
  "v_exp_f32 v218, v218\n\t" \
  "s_waitcnt vmcnt(0)\n\t" \
  "v_mfma_f32_16x16x16_bf16 v[224:227], v[252:253], v[232:233], 0\n\t" \
  "v_cvt_pk_bf16_f32 v210, v216, v217\n\t" \
  "v_cvt_pk_bf16_f32 v208, v216, v217\n\t" \
  "v_exp_f32 v219, v219\n\t" \
  "v_mfma_f32_16x16x16_bf16 v[228:231], v[252:253], v[234:235], 0\n\t" \
  "v_cvt_pk_bf16_f32 v211, v218, v219\n\t" \
  "v_cvt_pk_bf16_f32 v209, v218, v219\n\t" \
  "v_mov_b32_dpp v208, v210 row_ror:8 row_mask:0xf bank_mask:0xc\n\t" \
  "v_mov_b32_dpp v210, v210 row_ror:8 row_mask:0xf bank_mask:0x3\n\t" \
  "v_mov_b32_dpp v209, v211 row_ror:8 row_mask:0xf bank_mask:0xc\n\t" \
  "v_mov_b32_dpp v211, v211 row_ror:8 row_mask:0xf bank_mask:0x3\n\t" \
  "v_max_u32 v204, v252, v253\n\t" \
  "v_cmp_le_u32 vcc, %[sent], v204\n\t" \
  "v_mfma_f32_16x16x32_bf16 v[212:215], v[220:223], v[208:211], v[212:215]\n\t" \
  ".Lpv%=:\n\t" \
  "v_mfma_f32_16x16x32_bf16 v[224:227], v[192:195], v[208:211], v[224:227]\n\t" \
  "v_mfma_f32_16x16x32_bf16 v[228:231], v[196:199], v[208:211], v[228:231]\n\t" \
  "s_cbranch_vccnz .Lvs%=\n\t" \
  ".Lvok%=:\n\t" \
  "s_nop 4\n\t" \
  "v_rcp_f32 v204, v212\n\t" \
  "v_cndmask_b32 v224, v224, v226, %[nh]\n\t" \
  "v_cndmask_b32 v225, v225, v227, %[nh]\n\t" \
  "v_cndmask_b32 v228, v228, v230, %[nh]\n\t" \
  "v_cndmask_b32 v229, v229, v231, %[nh]\n\t" \
  "v_pk_mul_f32 v[224:225], v[224:225], v[204:205] op_sel_hi:[1,0]\n\t" \
  "v_pk_mul_f32 v[228:229], v[228:229], v[204:205] op_sel_hi:[1,0]\n\t" \
  "v_cvt_pk_bf16_f32 v224, v224, v225\n\t" \
  "v_cvt_pk_bf16_f32 v225, v228, v229\n\t" \
  "buffer_store_dwordx2 v[224:225], v251, %[rs], %[so] offen sc1\n\t" \
  "s_memtime s[90:91]\n\t" \
  "s_setprio 0\n\t" \
  "s_waitcnt lgkmcnt(0)\n\t" \
  "s_mov_b32 %[tout], s90\n\t" \
  "s_mov_b32 %[spins], s92\n\t" \
  "s_branch .Lend%=\n\t" \
  ".Lre%=:\n\t" \
  "ds_read_b64_tr_b16 v[236:237], v250 offset:512\n\t" \
  "ds_read_b64_tr_b16 v[238:239], v250 offset:576\n\t" \
  "ds_read_b64_tr_b16 v[240:241], v250 offset:1024\n\t" \
  "ds_read_b64_tr_b16 v[242:243], v250 offset:1088\n\t" \
  "ds_read_b64_tr_b16 v[244:245], v250 offset:1536\n\t" \
  "ds_read_b64_tr_b16 v[246:247], v250 offset:1600\n\t" \
  "s_waitcnt lgkmcnt(0)\n\t" \
  "s_branch .Lrb%=\n\t" \
  ".Lvs%=:\n\t" \
  "v_cmp_eq_u32 vcc, %[sent], v252\n\t" \
  "v_cmp_eq_u32 s[94:95], %[sent], v253\n\t" \
  "s_or_b64 s[94:95], s[94:95], vcc\n\t" \
  "s_cbranch_scc0 .Lvok%=\n\t" \
  "s_add_u32 s92, s92, 1\n\t" \
  "s_sleep 2\n\t" \
  "s_and_b32 s93, s92, 63\n\t" \
  "s_cbranch_scc1 .Lvr%=\n\t" \
  "buffer_load_dword v204, off, %[rs], %[sab] sc1\n\t" \
  "s_waitcnt vmcnt(0)\n\t" \
  "v_readfirstlane_b32 s93, v204\n\t" \
  "s_nop 3\n\t" \
  "s_cmp_eq_u32 s93, 1\n\t" \
  "s_cbranch_scc1 .Lvok%=\n\t" \
  "s_cmp_lt_u32 s92, 0x80000\n\t" \
  "s_cbranch_scc1 .Lvr%=\n\t" \
  "v_mov_b32 v204, 1\n\t" \
  "buffer_store_dword v204, off, %[rs], %[sab] sc1\n\t" \
  "s_branch .Lvok%=\n\t" \
  ".Lvr%=:\n\t" \
  "buffer_load_dword v252, v249, %[rs], %[sv] offen offset:0 sc1\n\t" \
  "buffer_load_dword v253, v249, %[rs], %[sv] offen offset:256 sc1\n\t" \
  "s_waitcnt vmcnt(0)\n\t" \
  "v_mfma_f32_16x16x16_bf16 v[224:227], v[252:253], v[232:233], 0\n\t" \
  "v_mfma_f32_16x16x16_bf16 v[228:231], v[252:253], v[234:235], 0\n\t" \
  "v_max_u32 v204, v252, v253\n\t" \
  "v_cmp_le_u32 vcc, %[sent], v204\n\t" \
  "s_nop 4\n\t" \
  "s_branch .Lpv%=\n\t" \
  "s_branch .Lend%=\n\t" \
  ".Lslow%=:\n\t" \
  "s_mov_b64 s[94:95], 0\n\t" \
  "v_cmp_eq_u32 vcc, %[sent], v200\n\t" \
  "s_or_b64 s[94:95], s[94:95], vcc\n\t" \
  "v_cmp_eq_u32 vcc, %[sent], v201\n\t" \
  "s_or_b64 s[94:95], s[94:95], vcc\n\t" \
  "v_cmp_eq_u32 vcc, %[sent], v202\n\t" \
  "s_or_b64 s[94:95], s[94:95], vcc\n\t" \
  "v_cmp_eq_u32 vcc, %[sent], v203\n\t" \
  "s_or_b64 s[94:95], s[94:95], vcc\n\t" \
  "v_cmp_eq_u32 vcc, %[sent], v204\n\t" \
  "s_or_b64 s[94:95], s[94:95], vcc\n\t" \
  "v_cmp_eq_u32 vcc, %[sent], v205\n\t" \
  "s_or_b64 s[94:95], s[94:95], vcc\n\t" \
  "v_cmp_eq_u32 vcc, %[sent], v206\n\t" \
  "s_or_b64 s[94:95], s[94:95], vcc\n\t" \
  "v_cmp_eq_u32 vcc, %[sent], v207\n\t" \
  "s_or_b64 s[94:95], s[94:95], vcc\n\t" \
  "s_cmp_eq_u64 s[94:95], 0\n\t" \
  "s_cbranch_scc1 .Lok%=\n\t" \
  "v_mov_b64 v[192:193], 0\n\t" \
  "v_mov_b64 v[194:195], 0\n\t" \
  "v_mov_b64 v[196:197], 0\n\t" \
  "v_mov_b64 v[198:199], 0\n\t" \
  "s_add_u32 s92, s92, 1\n\t" \
  "s_sleep 6\n\t" \
  "s_cmp_eq_u32 s92, 1\n\t" \
  "s_cbranch_scc1 .Lpoll%=\n\t" \
  "s_sleep 1\n\t" \
  "s_and_b32 s93, s92, 63\n\t" \
  "s_cmp_lg_u32 s93, 0\n\t" \
  "s_cbranch_scc1 .Lpoll%=\n\t" \
  "v_mov_b32 v245, 0\n\t" \
  "buffer_load_dword v244, v245, %[rs], %[sab] offen sc1\n\t" \
  "s_waitcnt vmcnt(0)\n\t" \
  "v_readfirstlane_b32 s93, v244\n\t" \
  "s_nop 3\n\t" \
  "s_cmp_eq_u32 s93, 1\n\t" \
  "s_cbranch_scc1 .Lok%=\n\t" \
  "s_cmp_lt_u32 s92, 0x80000\n\t" \
  "s_cbranch_scc1 .Lpoll%=\n\t" \
  "v_mov_b32 v244, 1\n\t" \
  "buffer_store_dword v244, v245, %[rs], %[sab] offen sc1\n\t" \
  "s_branch .Lok%=\n\t" \
  ".Lend%=:\n\t"
#define QK_ASM_1 \
  "s_waitcnt lgkmcnt(0)\n\t" \
  "v_subrev_u32 v244, 0x8000, v250\n\t" \
  "ds_read_b128 v[216:219], v248 offset:36864\n\t" \
  "ds_read_b128 v[220:223], v248 offset:37888\n\t" \
  "ds_read_b128 v[224:227], v248 offset:38912\n\t" \
  "ds_read_b128 v[228:231], v248 offset:39936\n\t" \
  "ds_read_b128 v[208:211], v244 offset:0\n\t" \
  "ds_read_b128 v[212:215], v244 offset:1024\n\t" \
  "ds_read_b128 v[236:239], v244 offset:2048\n\t" \
  "ds_read_b128 v[240:243], v244 offset:3072\n\t" \
  "v_mov_b64 v[192:193], 0\n\t" \
  "v_mov_b64 v[194:195], 0\n\t" \
  "v_mov_b64 v[196:197], 0\n\t" \
  "v_mov_b64 v[198:199], 0\n\t" \
  "s_movk_i32 s92, 0x2000\n\t" \
  ".Lwt%=:\n\t" \
  "ds_read_b64 v[200:201], v248 offset:30720\n\t" \
  "s_memrealtime s[90:91]\n\t" \
  "s_sub_u32 s92, s92, 1\n\t" \
  "s_waitcnt lgkmcnt(0)\n\t" \
  "v_readfirstlane_b32 s93, v200\n\t" \
  "v_readfirstlane_b32 %[tgt], v201\n\t" \
  "s_cbranch_scc1 .Lgo%=\n\t" \
  "s_cmp_eq_u32 s93, %[step]\n\t" \
  "s_cbranch_scc1 .Lwx%=\n\t" \
  "s_sleep 1\n\t" \
  "s_branch .Lwt%=\n\t" \
  ".Lwx%=:\n\t" \
  "s_lshl_b32 s93, s90, 2\n\t" \
  "s_sub_i32 s93, %[tgt], s93\n\t" \
  "s_cmp_le_i32 s93, 0\n\t" \
  "s_cbranch_scc1 .Lgo%=\n\t" \
  "s_min_i32 s93, s93, 8000\n\t" \
  "s_mov_b32 s94, s93\n\t" \
  ".Lsl%=:\n\t" \
  "s_sub_i32 s94, s94, 18\n\t" \
  "s_cmp_lt_i32 s94, 30\n\t" \
  "s_cbranch_scc1 .Lsx%=\n\t" \
  "s_sleep 1\n\t" \
  "s_branch .Lsl%=\n\t" \
  ".Lsx%=:\n\t" \
  "s_sub_i32 s93, s93, 12\n\t" \
  "s_max_i32 s93, s93, 4\n\t" \
  "s_lshr_b32 s93, s93, 2\n\t" \
  "s_add_u32 s93, s93, s90\n\t" \
  "s_memrealtime s[90:91]\n\t" \
  ".Lpace%=:\n\t" \
  "s_memrealtime s[94:95]\n\t" \
  "s_waitcnt lgkmcnt(1)\n\t" \
  "s_sub_i32 s92, s90, s93\n\t" \
  "s_cmp_ge_i32 s92, 0\n\t" \
  "s_cbranch_scc1 .Le%=\n\t" \
  "s_memrealtime s[90:91]\n\t" \
  "s_waitcnt lgkmcnt(1)\n\t" \
  "s_sub_i32 s92, s94, s93\n\t" \
  "s_cmp_lt_i32 s92, 0\n\t" \
  "s_cbranch_scc1 .Lpace%=\n\t" \
  "s_nop 4\n\t" \
  ".Le%=:\n\t" \
  "s_add_u32 s92, s92, s93\n\t" \
  "s_lshl_b32 s92, s92, 2\n\t" \
  "s_sub_i32 s92, %[tgt], s92\n\t" \
  "s_max_i32 s92, s92, 0\n\t" \
  "s_bitcmp0_b32 s92, 3\n\t" \
  "s_cbranch_scc1 .Lf3%=\n\t" \
  "s_nop 15\n\t" \
  ".Lf3%=:\n\t" \
  "s_bitcmp0_b32 s92, 2\n\t" \
  "s_cbranch_scc1 .Lf2%=\n\t" \
  "s_nop 10\n\t" \
  ".Lf2%=:\n\t" \
  ".Lgo%=:\n\t" \
  "s_mov_b32 s92, 0\n\t" \
  ".Lpoll%=:\n\t" \
  "buffer_load_dwordx4 v[200:203], v248, %[rs], %[som] offen sc1\n\t" \
  "buffer_load_dwordx4 v[204:207], v248, %[rs], %[som2] offen sc1\n\t" \
  "s_waitcnt vmcnt(1) lgkmcnt(0)\n\t" \
  "ds_write_b128 v249, v[200:203]\n\t" \
  "v_smfmac_f32_16x16x64_bf16 v[192:195], v[200:203], v[208:215], v251\n\t" \
  "v_max3_u32 v244, v200, v201, v202\n\t" \
  "v_smfmac_f32_16x16x64_bf16 v[196:199], v[200:203], v[236:243], v251\n\t" \
  "s_waitcnt vmcnt(0)\n\t" \
  "ds_write_b128 v249, v[204:207] offset:8192\n\t" \
  "v_smfmac_f32_16x16x64_bf16 v[192:195], v[204:207], v[216:223], v251\n\t" \
  "v_max3_u32 v244, v244, v203, v204\n\t" \
  "v_max3_u32 v244, v244, v205, v206\n\t" \
  "v_smfmac_f32_16x16x64_bf16 v[196:199], v[204:207], v[224:231], v251\n\t" \
  "v_max_u32 v244, v244, v207\n\t" \
  "v_cmp_le_u32 vcc, %[sent], v244\n\t" \
  "s_cbranch_vccnz .Lslow%=\n\t" \
  ".Lok%=:\n\t" \
  "ds_read_b128 v[216:219], v250 offset:32768\n\t" \
  "ds_read_b128 v[220:223], v250 offset:33792\n\t" \
  "ds_read_b128 v[224:227], v250 offset:34816\n\t" \
  "ds_read_b128 v[228:231], v250 offset:35840\n\t" \
  "s_waitcnt lgkmcnt(4)\n\t" \
  "s_barrier\n\t" \
  "ds_read_b128 v[208:211], v248 offset:0\n\t" \
  "ds_read_b128 v[212:215], v248 offset:3072\n\t" \
  "ds_read_b128 v[236:239], v248 offset:4096\n\t" \
  "ds_read_b128 v[240:243], v248 offset:5120\n\t" \
  "s_waitcnt lgkmcnt(3)\n\t" \
  "ds_read_b128 v[244:247], v248 offset:12288\n\t" \
  "v_smfmac_f32_16x16x64_bf16 v[192:195], v[208:211], v[216:223], v251\n\t" \
  "ds_read_b128 v[216:219], v250 offset:0\n\t" \
  "v_smfmac_f32_16x16x64_bf16 v[196:199], v[208:211], v[224:231], v251\n\t" \
  "s_waitcnt lgkmcnt(4)\n\t" \
  "ds_read_b128 v[220:223], v250 offset:1024\n\t" \
  "v_smfmac_f32_16x16x64_bf16 v[192:195], v[212:215], v[0:7], v251\n\t" \
  "ds_read_b128 v[224:227], v250 offset:2048\n\t" \
  "v_smfmac_f32_16x16x64_bf16 v[196:199], v[212:215], v[8:15], v251\n\t" \
  "s_waitcnt lgkmcnt(5)\n\t" \
  "ds_read_b128 v[228:231], v250 offset:3072\n\t" \
  "v_smfmac_f32_16x16x64_bf16 v[192:195], v[236:239], v[16:23], v251\n\t" \
  "ds_read_b128 v[200:203], v248 offset:6144\n\t" \
  "v_smfmac_f32_16x16x64_bf16 v[196:199], v[236:239], v[24:31], v251\n\t" \
  "s_waitcnt lgkmcnt(6)\n\t" \
  "ds_read_b128 v[204:207], v248 offset:7168\n\t" \
  "v_smfmac_f32_16x16x64_bf16 v[192:195], v[240:243], v[32:39], v251\n\t" \
  "ds_read_b128 v[208:211], v248 offset:8192\n\t" \
  "v_smfmac_f32_16x16x64_bf16 v[196:199], v[240:243], v[40:47], v251\n\t" \
  "s_waitcnt lgkmcnt(5)\n\t" \
  "ds_read_b128 v[212:215], v248 offset:10240\n\t" \
  "v_smfmac_f32_16x16x64_bf16 v[192:195], v[244:247], v[216:223], v251\n\t" \
  "s_waitcnt lgkmcnt(4)\n\t" \
  "v_smfmac_f32_16x16x64_bf16 v[196:199], v[244:247], v[224:231], v251\n\t" \
  "s_waitcnt lgkmcnt(3)\n\t" \
  "ds_read_b128 v[236:239], v248 offset:11264\n\t" \
  "v_smfmac_f32_16x16x64_bf16 v[192:195], v[200:203], v[48:55], v251\n\t" \
  "ds_read_b128 v[240:243], v248 offset:13312\n\t" \
  "v_smfmac_f32_16x16x64_bf16 v[196:199], v[200:203], v[56:63], v251\n\t" \
  "s_waitcnt lgkmcnt(4)\n\t" \
  "v_smfmac_f32_16x16x64_bf16 v[192:195], v[204:207], v[64:71], v251\n\t" \
  "ds_read_b128 v[244:247], v248 offset:14336\n\t" \
  "v_smfmac_f32_16x16x64_bf16 v[196:199], v[204:207], v[72:79], v251\n\t" \
  "s_waitcnt lgkmcnt(4)\n\t" \
  "v_smfmac_f32_16x16x64_bf16 v[192:195], v[208:211], v[80:87], v251\n\t" \
  "ds_read_b128 v[200:203], v248 offset:15360\n\t" \
  "v_smfmac_f32_16x16x64_bf16 v[196:199], v[208:211], v[88:95], v251\n\t" \
  "s_waitcnt lgkmcnt(4)\n\t" \
  "v_smfmac_f32_16x16x64_bf16 v[192:195], v[212:215], v[96:103], v251\n\t" \
  "ds_read_b128 v[204:207], v248 offset:2048\n\t" \
  "v_smfmac_f32_16x16x64_bf16 v[196:199], v[212:215], v[104:111], v251\n\t" \
  "s_waitcnt lgkmcnt(3)\n\t" \
  "v_smfmac_f32_16x16x64_bf16 v[192:195], v[236:239], v[112:119], v251\n\t" \
  "v_smfmac_f32_16x16x64_bf16 v[196:199], v[236:239], v[120:127], v251\n\t" \
  "v_smfmac_f32_16x16x64_bf16 v[192:195], v[240:243], v[128:135], v251\n\t" \
  "v_smfmac_f32_16x16x64_bf16 v[196:199], v[240:243], v[136:143], v251\n\t" \
  "s_waitcnt lgkmcnt(1)\n\t" \
  "v_smfmac_f32_16x16x64_bf16 v[192:195], v[244:247], v[144:151], v251\n\t" \
  "v_smfmac_f32_16x16x64_bf16 v[196:199], v[244:247], v[152:159], v251\n\t" \
  "v_smfmac_f32_16x16x64_bf16 v[192:195], v[200:203], v[160:167], v251\n\t" \
  "v_smfmac_f32_16x16x64_bf16 v[196:199], v[200:203], v[168:175], v251\n\t" \
  "s_waitcnt lgkmcnt(0)\n\t" \
  "v_smfmac_f32_16x16x64_bf16 v[192:195], v[204:207], v[176:183], v251\n\t" \
  "v_smfmac_f32_16x16x64_bf16 v[196:199], v[204:207], v[184:191], v251\n\t" \
  "ds_read_b128 v[200:203], v248 offset:17408\n\t" \
  "ds_read_b128 v[204:207], v248 offset:21504\n\t" \
  "v_mov_b32 v250, v232\n\t" \
  "v_mov_b32 v249, v233\n\t" \
  "s_nop 2\n\t" \
  "v_add_f32 v192, v192, v194\n\t" \
  "v_add_f32 v193, v193, v195\n\t" \
  "v_add_f32 v196, v196, v198\n\t" \
  "v_add_f32 v197, v197, v199\n\t" \
  "v_cvt_pk_bf16_f32 v192, v192, v193\n\t" \
  "v_cvt_pk_bf16_f32 v196, v196, v197\n\t" \
  "ds_write2_b32 v252, v192, v196 offset1:64\n\t" \
  "v_mov_b32 v251, v234\n\t" \
  "ds_read_b64_tr_b16 v[236:237], v250 offset:512\n\t" \
  "ds_read_b64_tr_b16 v[238:239], v250 offset:576\n\t" \
  "v_mov_b32 v212, %[step]\n\t" \
  "s_mov_b64 exec, 1\n\t" \
  "ds_write_b32 v248, v212 offset:28688\n\t" \
  "s_mov_b64 exec, -1\n\t" \
  "buffer_load_dwordx4 v[192:195], v248, %[rt], %[svt] offen offset:0 sc1\n\t" \
  "buffer_load_dwordx4 v[196:199], v248, %[rt], %[svt] offen offset:1024 sc1\n\t" \
  "s_waitcnt lgkmcnt(1)\n\t" \
  "v_mfma_f32_16x16x32_bf16 v[200:203], v[200:203], v[236:239], 0\n\t" \
  "v_mfma_f32_16x16x32_bf16 v[204:207], v[204:207], v[236:239], 0\n\t" \
  "s_nop 7\n\t" \
  "v_cndmask_b32 v216, v200, v204, %[nh]\n\t" \
  "v_cndmask_b32 v217, v201, v205, %[nh]\n\t" \
  "v_cndmask_b32 v218, v202, v206, %[nh]\n\t" \
  "v_cndmask_b32 v219, v203, v207, %[nh]\n\t" \
  "ds_write_b128 v248, v[216:219] offset:17408\n\t" \
  "buffer_load_dword v252, v249, %[rs], %[sv] offen offset:0 sc1\n\t" \
  "buffer_load_dword v253, v249, %[rs], %[sv] offen offset:256 sc1\n\t" \
  "ds_read_b32 v212, v248 offset:28672\n\t" \
  "ds_read_b64_tr_b16 v[240:241], v250 offset:1024\n\t" \
  "ds_read_b64_tr_b16 v[242:243], v250 offset:1088\n\t" \
  "ds_read_b64_tr_b16 v[244:245], v250 offset:1536\n\t" \
  "ds_read_b64_tr_b16 v[246:247], v250 offset:1600\n\t" \
  "ds_read_b64_tr_b16 v[232:233], v250 offset:0\n\t" \
  "ds_read_b64_tr_b16 v[234:235], v250 offset:64\n\t" \
  "s_setprio 3\n\t" \
  "s_waitcnt lgkmcnt(0)\n\t" \
  "v_cmp_ne_u32 vcc, %[step], v212\n\t" \
  "s_barrier\n\t" \
  "s_and_b32 s93, vcc_lo, 0xf\n\t" \
  "s_cbranch_scc1 .Lre%=\n\t" \
  ".Lrb%=:\n\t" \
  "ds_read_b64_tr_b16 v[204:205], v250 offset:2560\n\t" \
  "ds_read_b64_tr_b16 v[206:207], v250 offset:2624\n\t" \
  "ds_read_b64_tr_b16 v[208:209], v250 offset:3072\n\t" \
  "ds_read_b64_tr_b16 v[210:211], v250 offset:3136\n\t" \
  "ds_read_b128 v[220:223], v248 offset:18432\n\t" \
  "ds_read_b128 v[224:227], v248 offset:19456\n\t" \
  "ds_read_b128 v[228:231], v248 offset:16384\n\t" \
  "ds_read_b64_tr_b16 v[212:213], v250 offset:3584\n\t" \
  "ds_read_b64_tr_b16 v[214:215], v250 offset:3648\n\t" \
  "ds_read_b64_tr_b16 v[200:201], v250 offset:2048\n\t" \
  "ds_read_b64_tr_b16 v[202:203], v250 offset:2112\n\t" \
  "s_waitcnt lgkmcnt(9)\n\t" \
  "v_mfma_f32_16x16x32_bf16 v[204:207], v[204:207], v[236:239], 0\n\t" \
  "s_waitcnt lgkmcnt(7)\n\t" \
  "v_mfma_f32_16x16x32_bf16 v[204:207], v[208:211], v[240:243], v[204:207]\n\t" \
  "s_waitcnt lgkmcnt(6)\n\t" \
  "v_pk_add_f32 v[216:217], v[216:217], v[220:221]\n\t" \
  "v_pk_add_f32 v[218:219], v[218:219], v[222:223]\n\t" \
  "s_waitcnt lgkmcnt(4)\n\t" \
  "v_pk_add_f32 v[224:225], v[224:225], v[228:229]\n\t" \
  "v_pk_add_f32 v[226:227], v[226:227], v[230:231]\n\t" \
  "v_pk_add_f32 v[216:217], v[216:217], v[224:225]\n\t" \
  "v_pk_add_f32 v[218:219], v[218:219], v[226:227]\n\t" \
  "s_waitcnt lgkmcnt(2)\n\t" \
  "v_mfma_f32_16x16x32_bf16 v[204:207], v[212:215], v[244:247], v[204:207]\n\t" \
  "v_max3_f32 v224, v216, v217, v218\n\t" \
  "v_max_f32 v224, v224, v219\n\t" \
  "s_waitcnt lgkmcnt(0)\n\t" \
  "v_mfma_f32_16x16x32_bf16 v[204:207], v[200:203], v[232:235], v[204:207]\n\t" \
  "v_max_f32_dpp v225, v224, v224 row_ror:8 row_mask:0xf bank_mask:0xf\n\t" \
  "v_max_f32_dpp v226, v224, v224 row_ror:8 row_mask:0xf bank_mask:0xf\n\t" \
  "s_nop 1\n\t" \
  "v_permlane32_swap_b32 v225, v226\n\t" \
  "v_max_f32 v231, v225, v226\n\t" \
  "v_mov_b32 v220, 0x3f803f80\n\t" \
  "v_mov_b32 v221, 0x3f803f80\n\t" \
  "v_mov_b64 v[222:223], v[220:221]\n\t" \
  "v_mov_b32 v237, 0\n\t" \
  "v_mov_b32 v238, 0\n\t" \
  "v_max3_f32 v208, v204, v205, v231\n\t" \
  "v_max3_f32 v209, v208, v206, v207\n\t" \
  "v_max3_f32 v210, v208, v206, v207\n\t" \
  "v_cndmask_b32 v204, v204, v206, %[up]\n\t" \
  "v_cndmask_b32 v205, v205, v207, %[up]\n\t" \
  "v_permlane16_swap_b32 v209, v210\n\t" \
  "v_max_f32 v208, v209, v210\n\t" \
  "v_mul_f32 v208, %[negc], v208\n\t" \
  "v_fma_f32 v204, v204, %[c], v208\n\t" \
  "v_fma_f32 v205, v205, %[c], v208\n\t" \
  "v_exp_f32 v204, v204\n\t" \
  "v_fma_f32 v216, v216, %[c], v208\n\t" \
  "v_exp_f32 v205, v205\n\t" \
  "v_fma_f32 v217, v217, %[c], v208\n\t" \
  "v_exp_f32 v216, v216\n\t" \
  "v_fma_f32 v218, v218, %[c], v208\n\t" \
  "v_cvt_pk_bf16_f32 v236, v204, v205\n\t" \
  "v_cvt_pk_bf16_f32 v239, v204, v205\n\t" \
  "v_exp_f32 v217, v217\n\t" \
  "v_fma_f32 v219, v219, %[c], v208\n\t" \
  "v_mfma_f32_16x16x16_bf16 v[200:203], v[220:221], v[236:237], 0\n\t" \
  "v_exp_f32 v218, v218\n\t" \
  "s_waitcnt vmcnt(0)\n\t" \
  "v_mfma_f32_16x16x16_bf16 v[224:227], v[252:253], v[236:237], 0\n\t" \
  "v_cvt_pk_bf16_f32 v214, v216, v217\n\t" \
  "v_cvt_pk_bf16_f32 v212, v216, v217\n\t" \
  "v_exp_f32 v219, v219\n\t" \
  "v_mfma_f32_16x16x16_bf16 v[228:231], v[252:253], v[238:239], 0\n\t" \
  "v_cvt_pk_bf16_f32 v215, v218, v219\n\t" \
  "v_cvt_pk_bf16_f32 v213, v218, v219\n\t" \
  "v_mov_b32_dpp v212, v214 row_ror:8 row_mask:0xf bank_mask:0xc\n\t" \
  "v_mov_b32_dpp v214, v214 row_ror:8 row_mask:0xf bank_mask:0x3\n\t" \
  "v_mov_b32_dpp v213, v215 row_ror:8 row_mask:0xf bank_mask:0xc\n\t" \
  "v_mov_b32_dpp v215, v215 row_ror:8 row_mask:0xf bank_mask:0x3\n\t" \
  "v_max_u32 v208, v252, v253\n\t" \
  "v_cmp_le_u32 vcc, %[sent], v208\n\t" \
  "v_mfma_f32_16x16x32_bf16 v[200:203], v[220:223], v[212:215], v[200:203]\n\t" \
  ".Lpv%=:\n\t" \
  "v_mfma_f32_16x16x32_bf16 v[224:227], v[192:195], v[212:215], v[224:227]\n\t" \
  "v_mfma_f32_16x16x32_bf16 v[228:231], v[196:199], v[212:215], v[228:231]\n\t" \
  "s_cbranch_vccnz .Lvs%=\n\t" \
  ".Lvok%=:\n\t" \
  "s_nop 4\n\t" \
  "v_rcp_f32 v208, v200\n\t" \
  "v_cndmask_b32 v224, v224, v226, %[nh]\n\t" \
  "v_cndmask_b32 v225, v225, v227, %[nh]\n\t" \
  "v_cndmask_b32 v228, v228, v230, %[nh]\n\t" \
  "v_cndmask_b32 v229, v229, v231, %[nh]\n\t" \
  "v_pk_mul_f32 v[224:225], v[224:225], v[208:209] op_sel_hi:[1,0]\n\t" \
  "v_pk_mul_f32 v[228:229], v[228:229], v[208:209] op_sel_hi:[1,0]\n\t" \
  "v_cvt_pk_bf16_f32 v224, v224, v225\n\t" \
  "v_cvt_pk_bf16_f32 v225, v228, v229\n\t" \
  "buffer_store_dwordx2 v[224:225], v251, %[rs], %[so] offen sc1\n\t" \
  "s_memtime s[90:91]\n\t" \
  "s_setprio 0\n\t" \
  "s_waitcnt lgkmcnt(0)\n\t" \
  "s_mov_b32 %[tout], s90\n\t" \
  "s_mov_b32 %[spins], s92\n\t" \
  "s_branch .Lend%=\n\t" \
  ".Lre%=:\n\t" \
  "ds_read_b64_tr_b16 v[240:241], v250 offset:1024\n\t" \
  "ds_read_b64_tr_b16 v[242:243], v250 offset:1088\n\t" \
  "ds_read_b64_tr_b16 v[244:245], v250 offset:1536\n\t" \
  "ds_read_b64_tr_b16 v[246:247], v250 offset:1600\n\t" \
  "ds_read_b64_tr_b16 v[232:233], v250 offset:0\n\t" \
  "ds_read_b64_tr_b16 v[234:235], v250 offset:64\n\t" \
  "s_waitcnt lgkmcnt(0)\n\t" \
  "s_branch .Lrb%=\n\t" \
  ".Lvs%=:\n\t" \
  "v_cmp_eq_u32 vcc, %[sent], v252\n\t" \
  "v_cmp_eq_u32 s[94:95], %[sent], v253\n\t" \
  "s_or_b64 s[94:95], s[94:95], vcc\n\t" \
  "s_cbranch_scc0 .Lvok%=\n\t" \
  "s_add_u32 s92, s92, 1\n\t" \
  "s_sleep 2\n\t" \
  "s_and_b32 s93, s92, 63\n\t" \
  "s_cbranch_scc1 .Lvr%=\n\t" \
  "buffer_load_dword v208, off, %[rs], %[sab] sc1\n\t" \
  "s_waitcnt vmcnt(0)\n\t" \
  "v_readfirstlane_b32 s93, v208\n\t" \
  "s_nop 3\n\t" \
  "s_cmp_eq_u32 s93, 1\n\t" \
  "s_cbranch_scc1 .Lvok%=\n\t" \
  "s_cmp_lt_u32 s92, 0x80000\n\t" \
  "s_cbranch_scc1 .Lvr%=\n\t" \
  "v_mov_b32 v208, 1\n\t" \
  "buffer_store_dword v208, off, %[rs], %[sab] sc1\n\t" \
  "s_branch .Lvok%=\n\t" \
  ".Lvr%=:\n\t" \
  "buffer_load_dword v252, v249, %[rs], %[sv] offen offset:0 sc1\n\t" \
  "buffer_load_dword v253, v249, %[rs], %[sv] offen offset:256 sc1\n\t" \
  "s_waitcnt vmcnt(0)\n\t" \
  "v_mfma_f32_16x16x16_bf16 v[224:227], v[252:253], v[236:237], 0\n\t" \
  "v_mfma_f32_16x16x16_bf16 v[228:231], v[252:253], v[238:239], 0\n\t" \
  "v_max_u32 v208, v252, v253\n\t" \
  "v_cmp_le_u32 vcc, %[sent], v208\n\t" \
  "s_nop 4\n\t" \
  "s_branch .Lpv%=\n\t" \
  "s_branch .Lend%=\n\t" \
  ".Lslow%=:\n\t" \
  "s_mov_b64 s[94:95], 0\n\t" \
  "v_cmp_eq_u32 vcc, %[sent], v200\n\t" \
  "s_or_b64 s[94:95], s[94:95], vcc\n\t" \
  "v_cmp_eq_u32 vcc, %[sent], v201\n\t" \
  "s_or_b64 s[94:95], s[94:95], vcc\n\t" \
  "v_cmp_eq_u32 vcc, %[sent], v202\n\t" \
  "s_or_b64 s[94:95], s[94:95], vcc\n\t" \
  "v_cmp_eq_u32 vcc, %[sent], v203\n\t" \
  "s_or_b64 s[94:95], s[94:95], vcc\n\t" \
  "v_cmp_eq_u32 vcc, %[sent], v204\n\t" \
  "s_or_b64 s[94:95], s[94:95], vcc\n\t" \
  "v_cmp_eq_u32 vcc, %[sent], v205\n\t" \
  "s_or_b64 s[94:95], s[94:95], vcc\n\t" \
  "v_cmp_eq_u32 vcc, %[sent], v206\n\t" \
  "s_or_b64 s[94:95], s[94:95], vcc\n\t" \
  "v_cmp_eq_u32 vcc, %[sent], v207\n\t" \
  "s_or_b64 s[94:95], s[94:95], vcc\n\t" \
  "s_cmp_eq_u64 s[94:95], 0\n\t" \
  "s_cbranch_scc1 .Lok%=\n\t" \
  "v_mov_b64 v[192:193], 0\n\t" \
  "v_mov_b64 v[194:195], 0\n\t" \
  "v_mov_b64 v[196:197], 0\n\t" \
  "v_mov_b64 v[198:199], 0\n\t" \
  "s_add_u32 s92, s92, 1\n\t" \
  "s_sleep 6\n\t" \
  "s_cmp_eq_u32 s92, 1\n\t" \
  "s_cbranch_scc1 .Lpoll%=\n\t" \
  "s_sleep 1\n\t" \
  "s_and_b32 s93, s92, 63\n\t" \
  "s_cmp_lg_u32 s93, 0\n\t" \
  "s_cbranch_scc1 .Lpoll%=\n\t" \
  "v_mov_b32 v245, 0\n\t" \
  "buffer_load_dword v244, v245, %[rs], %[sab] offen sc1\n\t" \
  "s_waitcnt vmcnt(0)\n\t" \
  "v_readfirstlane_b32 s93, v244\n\t" \
  "s_nop 3\n\t" \
  "s_cmp_eq_u32 s93, 1\n\t" \
  "s_cbranch_scc1 .Lok%=\n\t" \
  "s_cmp_lt_u32 s92, 0x80000\n\t" \
  "s_cbranch_scc1 .Lpoll%=\n\t" \
  "v_mov_b32 v244, 1\n\t" \
  "buffer_store_dword v244, v245, %[rs], %[sab] offen sc1\n\t" \
  "s_branch .Lok%=\n\t" \
  ".Lend%=:\n\t"
#define QK_ASM_2 \
  "s_waitcnt lgkmcnt(0)\n\t" \
  "v_subrev_u32 v244, 0x8000, v250\n\t" \
  "ds_read_b128 v[216:219], v248 offset:40960\n\t" \
  "ds_read_b128 v[220:223], v248 offset:41984\n\t" \
  "ds_read_b128 v[224:227], v248 offset:43008\n\t" \
  "ds_read_b128 v[228:231], v248 offset:44032\n\t" \
  "ds_read_b128 v[208:211], v244 offset:0\n\t" \
  "ds_read_b128 v[212:215], v244 offset:1024\n\t" \
  "ds_read_b128 v[236:239], v244 offset:2048\n\t" \
  "ds_read_b128 v[240:243], v244 offset:3072\n\t" \
  "v_mov_b64 v[192:193], 0\n\t" \
  "v_mov_b64 v[194:195], 0\n\t" \
  "v_mov_b64 v[196:197], 0\n\t" \
  "v_mov_b64 v[198:199], 0\n\t" \
  "s_movk_i32 s92, 0x2000\n\t" \
  ".Lwt%=:\n\t" \
  "ds_read_b64 v[200:201], v248 offset:30720\n\t" \
  "s_memrealtime s[90:91]\n\t" \
  "s_sub_u32 s92, s92, 1\n\t" \
  "s_waitcnt lgkmcnt(0)\n\t" \
  "v_readfirstlane_b32 s93, v200\n\t" \
  "v_readfirstlane_b32 %[tgt], v201\n\t" \
  "s_cbranch_scc1 .Lgo%=\n\t" \
  "s_cmp_eq_u32 s93, %[step]\n\t" \
  "s_cbranch_scc1 .Lwx%=\n\t" \
  "s_sleep 1\n\t" \
  "s_branch .Lwt%=\n\t" \
  ".Lwx%=:\n\t" \
  "s_lshl_b32 s93, s90, 2\n\t" \
  "s_sub_i32 s93, %[tgt], s93\n\t" \
  "s_cmp_le_i32 s93, 0\n\t" \
  "s_cbranch_scc1 .Lgo%=\n\t" \
  "s_min_i32 s93, s93, 8000\n\t" \
  "s_mov_b32 s94, s93\n\t" \
  ".Lsl%=:\n\t" \
  "s_sub_i32 s94, s94, 18\n\t" \
  "s_cmp_lt_i32 s94, 30\n\t" \
  "s_cbranch_scc1 .Lsx%=\n\t" \
  "s_sleep 1\n\t" \
  "s_branch .Lsl%=\n\t" \
  ".Lsx%=:\n\t" \
  "s_sub_i32 s93, s93, 12\n\t" \
  "s_max_i32 s93, s93, 4\n\t" \
  "s_lshr_b32 s93, s93, 2\n\t" \
  "s_add_u32 s93, s93, s90\n\t" \
  "s_memrealtime s[90:91]\n\t" \
  ".Lpace%=:\n\t" \
  "s_memrealtime s[94:95]\n\t" \
  "s_waitcnt lgkmcnt(1)\n\t" \
  "s_sub_i32 s92, s90, s93\n\t" \
  "s_cmp_ge_i32 s92, 0\n\t" \
  "s_cbranch_scc1 .Le%=\n\t" \
  "s_memrealtime s[90:91]\n\t" \
  "s_waitcnt lgkmcnt(1)\n\t" \
  "s_sub_i32 s92, s94, s93\n\t" \
  "s_cmp_lt_i32 s92, 0\n\t" \
  "s_cbranch_scc1 .Lpace%=\n\t" \
  "s_nop 4\n\t" \
  ".Le%=:\n\t" \
  "s_add_u32 s92, s92, s93\n\t" \
  "s_lshl_b32 s92, s92, 2\n\t" \
  "s_sub_i32 s92, %[tgt], s92\n\t" \
  "s_max_i32 s92, s92, 0\n\t" \
  "s_bitcmp0_b32 s92, 3\n\t" \
  "s_cbranch_scc1 .Lf3%=\n\t" \
  "s_nop 15\n\t" \
  ".Lf3%=:\n\t" \
  "s_bitcmp0_b32 s92, 2\n\t" \
  "s_cbranch_scc1 .Lf2%=\n\t" \
  "s_nop 10\n\t" \
  ".Lf2%=:\n\t" \
  ".Lgo%=:\n\t" \
  "s_mov_b32 s92, 0\n\t" \
  ".Lpoll%=:\n\t" \
  "buffer_load_dwordx4 v[200:203], v248, %[rs], %[som] offen sc1\n\t" \
  "buffer_load_dwordx4 v[204:207], v248, %[rs], %[som2] offen sc1\n\t" \
  "s_waitcnt vmcnt(1) lgkmcnt(0)\n\t" \
  "ds_write_b128 v249, v[200:203]\n\t" \
  "v_smfmac_f32_16x16x64_bf16 v[192:195], v[200:203], v[208:215], v251\n\t" \
  "v_max3_u32 v244, v200, v201, v202\n\t" \
  "v_smfmac_f32_16x16x64_bf16 v[196:199], v[200:203], v[236:243], v251\n\t" \
  "s_waitcnt vmcnt(0)\n\t" \
  "ds_write_b128 v249, v[204:207] offset:8192\n\t" \
  "v_smfmac_f32_16x16x64_bf16 v[192:195], v[204:207], v[216:223], v251\n\t" \
  "v_max3_u32 v244, v244, v203, v204\n\t" \
  "v_max3_u32 v244, v244, v205, v206\n\t" \
  "v_smfmac_f32_16x16x64_bf16 v[196:199], v[204:207], v[224:231], v251\n\t" \
  "v_max_u32 v244, v244, v207\n\t" \
  "v_cmp_le_u32 vcc, %[sent], v244\n\t" \
  "s_cbranch_vccnz .Lslow%=\n\t" \
  ".Lok%=:\n\t" \
  "ds_read_b128 v[216:219], v250 offset:32768\n\t" \
  "ds_read_b128 v[220:223], v250 offset:33792\n\t" \
  "ds_read_b128 v[224:227], v250 offset:34816\n\t" \
  "ds_read_b128 v[228:231], v250 offset:35840\n\t" \
  "s_waitcnt lgkmcnt(4)\n\t" \
  "s_barrier\n\t" \
  "ds_read_b128 v[208:211], v248 offset:3072\n\t" \
  "ds_read_b128 v[212:215], v248 offset:4096\n\t" \
  "ds_read_b128 v[236:239], v248 offset:5120\n\t" \
  "ds_read_b128 v[240:243], v248 offset:6144\n\t" \
  "s_waitcnt lgkmcnt(3)\n\t" \
  "ds_read_b128 v[244:247], v248 offset:13312\n\t" \
  "v_smfmac_f32_16x16x64_bf16 v[192:195], v[208:211], v[216:223], v251\n\t" \
  "ds_read_b128 v[216:219], v250 offset:0\n\t" \
  "v_smfmac_f32_16x16x64_bf16 v[196:199], v[208:211], v[224:231], v251\n\t" \
  "s_waitcnt lgkmcnt(4)\n\t" \
  "ds_read_b128 v[220:223], v250 offset:1024\n\t" \
  "v_smfmac_f32_16x16x64_bf16 v[192:195], v[212:215], v[0:7], v251\n\t" \
  "ds_read_b128 v[224:227], v250 offset:2048\n\t" \
  "v_smfmac_f32_16x16x64_bf16 v[196:199], v[212:215], v[8:15], v251\n\t" \
  "s_waitcnt lgkmcnt(5)\n\t" \
  "ds_read_b128 v[228:231], v250 offset:3072\n\t" \
  "v_smfmac_f32_16x16x64_bf16 v[192:195], v[236:239], v[16:23], v251\n\t" \
  "ds_read_b128 v[200:203], v248 offset:7168\n\t" \
  "v_smfmac_f32_16x16x64_bf16 v[196:199], v[236:239], v[24:31], v251\n\t" \
  "s_waitcnt lgkmcnt(6)\n\t" \
  "ds_read_b128 v[204:207], v248 offset:8192\n\t" \
  "v_smfmac_f32_16x16x64_bf16 v[192:195], v[240:243], v[32:39], v251\n\t" \
  "ds_read_b128 v[208:211], v248 offset:9216\n\t" \
  "v_smfmac_f32_16x16x64_bf16 v[196:199], v[240:243], v[40:47], v251\n\t" \
  "s_waitcnt lgkmcnt(5)\n\t" \
  "ds_read_b128 v[212:215], v248 offset:11264\n\t" \
  "v_smfmac_f32_16x16x64_bf16 v[192:195], v[244:247], v[216:223], v251\n\t" \
  "s_waitcnt lgkmcnt(4)\n\t" \
  "v_smfmac_f32_16x16x64_bf16 v[196:199], v[244:247], v[224:231], v251\n\t" \
  "s_waitcnt lgkmcnt(3)\n\t" \
  "ds_read_b128 v[236:239], v248 offset:12288\n\t" \
  "v_smfmac_f32_16x16x64_bf16 v[192:195], v[200:203], v[48:55], v251\n\t" \
  "ds_read_b128 v[240:243], v248 offset:14336\n\t" \
  "v_smfmac_f32_16x16x64_bf16 v[196:199], v[200:203], v[56:63], v251\n\t" \
  "s_waitcnt lgkmcnt(4)\n\t" \
  "v_smfmac_f32_16x16x64_bf16 v[192:195], v[204:207], v[64:71], v251\n\t" \
  "ds_read_b128 v[244:247], v248 offset:15360\n\t" \
  "v_smfmac_f32_16x16x64_bf16 v[196:199], v[204:207], v[72:79], v251\n\t" \
  "s_waitcnt lgkmcnt(4)\n\t" \
  "v_smfmac_f32_16x16x64_bf16 v[192:195], v[208:211], v[80:87], v251\n\t" \
  "ds_read_b128 v[200:203], v248 offset:0\n\t" \
  "v_smfmac_f32_16x16x64_bf16 v[196:199], v[208:211], v[88:95], v251\n\t" \
  "s_waitcnt lgkmcnt(4)\n\t" \
  "v_smfmac_f32_16x16x64_bf16 v[192:195], v[212:215], v[96:103], v251\n\t" \
  "ds_read_b128 v[204:207], v248 offset:1024\n\t" \
  "v_smfmac_f32_16x16x64_bf16 v[196:199], v[212:215], v[104:111], v251\n\t" \
  "s_waitcnt lgkmcnt(3)\n\t" \
  "v_smfmac_f32_16x16x64_bf16 v[192:195], v[236:239], v[112:119], v251\n\t" \
  "v_smfmac_f32_16x16x64_bf16 v[196:199], v[236:239], v[120:127], v251\n\t" \
  "v_smfmac_f32_16x16x64_bf16 v[192:195], v[240:243], v[128:135], v251\n\t" \
  "v_smfmac_f32_16x16x64_bf16 v[196:199], v[240:243], v[136:143], v251\n\t" \
  "s_waitcnt lgkmcnt(1)\n\t" \
  "v_smfmac_f32_16x16x64_bf16 v[192:195], v[244:247], v[144:151], v251\n\t" \
  "v_smfmac_f32_16x16x64_bf16 v[196:199], v[244:247], v[152:159], v251\n\t" \
  "v_smfmac_f32_16x16x64_bf16 v[192:195], v[200:203], v[160:167], v251\n\t" \
  "v_smfmac_f32_16x16x64_bf16 v[196:199], v[200:203], v[168:175], v251\n\t" \
  "s_waitcnt lgkmcnt(0)\n\t" \
  "v_smfmac_f32_16x16x64_bf16 v[192:195], v[204:207], v[176:183], v251\n\t" \
  "v_smfmac_f32_16x16x64_bf16 v[196:199], v[204:207], v[184:191], v251\n\t" \
  "ds_read_b128 v[200:203], v248 offset:18432\n\t" \
  "ds_read_b128 v[204:207], v248 offset:22528\n\t" \
  "v_mov_b32 v250, v232\n\t" \
  "v_mov_b32 v249, v233\n\t" \
  "s_nop 2\n\t" \
  "v_add_f32 v192, v192, v194\n\t" \
  "v_add_f32 v193, v193, v195\n\t" \
  "v_add_f32 v196, v196, v198\n\t" \
  "v_add_f32 v197, v197, v199\n\t" \
  "v_cvt_pk_bf16_f32 v192, v192, v193\n\t" \
  "v_cvt_pk_bf16_f32 v196, v196, v197\n\t" \
  "ds_write2_b32 v252, v192, v196 offset1:64\n\t" \
  "v_mov_b32 v251, v234\n\t" \
  "ds_read_b64_tr_b16 v[240:241], v250 offset:1024\n\t" \
  "ds_read_b64_tr_b16 v[242:243], v250 offset:1088\n\t" \
  "v_mov_b32 v212, %[step]\n\t" \
  "s_mov_b64 exec, 1\n\t" \
  "ds_write_b32 v248, v212 offset:28704\n\t" \
  "s_mov_b64 exec, -1\n\t" \
  "buffer_load_dwordx4 v[192:195], v248, %[rt], %[svt] offen offset:0 sc1\n\t" \
  "buffer_load_dwordx4 v[196:199], v248, %[rt], %[svt] offen offset:1024 sc1\n\t" \
  "s_waitcnt lgkmcnt(1)\n\t" \
  "v_mfma_f32_16x16x32_bf16 v[200:203], v[200:203], v[240:243], 0\n\t" \
  "v_mfma_f32_16x16x32_bf16 v[204:207], v[204:207], v[240:243], 0\n\t" \
  "s_nop 7\n\t" \
  "v_cndmask_b32 v216, v200, v204, %[nh]\n\t" \
  "v_cndmask_b32 v217, v201, v205, %[nh]\n\t" \
  "v_cndmask_b32 v218, v202, v206, %[nh]\n\t" \
  "v_cndmask_b32 v219, v203, v207, %[nh]\n\t" \
  "ds_write_b128 v248, v[216:219] offset:18432\n\t" \
  "buffer_load_dword v252, v249, %[rs], %[sv] offen offset:0 sc1\n\t" \
  "buffer_load_dword v253, v249, %[rs], %[sv] offen offset:256 sc1\n\t" \
  "ds_read_b32 v212, v248 offset:28672\n\t" \
  "ds_read_b64_tr_b16 v[244:245], v250 offset:1536\n\t" \
  "ds_read_b64_tr_b16 v[246:247], v250 offset:1600\n\t" \
  "ds_read_b64_tr_b16 v[232:233], v250 offset:0\n\t" \
  "ds_read_b64_tr_b16 v[234:235], v250 offset:64\n\t" \
  "ds_read_b64_tr_b16 v[236:237], v250 offset:512\n\t" \
  "ds_read_b64_tr_b16 v[238:239], v250 offset:576\n\t" \
  "s_setprio 3\n\t" \
  "s_waitcnt lgkmcnt(0)\n\t" \
  "v_cmp_ne_u32 vcc, %[step], v212\n\t" \
  "s_barrier\n\t" \
  "s_and_b32 s93, vcc_lo, 0xf\n\t" \
  "s_cbranch_scc1 .Lre%=\n\t" \
  ".Lrb%=:\n\t" \
  "ds_read_b64_tr_b16 v[208:209], v250 offset:3072\n\t" \
  "ds_read_b64_tr_b16 v[210:211], v250 offset:3136\n\t" \
  "ds_read_b64_tr_b16 v[212:213], v250 offset:3584\n\t" \
  "ds_read_b64_tr_b16 v[214:215], v250 offset:3648\n\t" \
  "ds_read_b128 v[220:223], v248 offset:19456\n\t" \
  "ds_read_b128 v[224:227], v248 offset:16384\n\t" \
  "ds_read_b128 v[228:231], v248 offset:17408\n\t" \
  "ds_read_b64_tr_b16 v[200:201], v250 offset:2048\n\t" \
  "ds_read_b64_tr_b16 v[202:203], v250 offset:2112\n\t" \
  "ds_read_b64_tr_b16 v[204:205], v250 offset:2560\n\t" \
  "ds_read_b64_tr_b16 v[206:207], v250 offset:2624\n\t" \
  "s_waitcnt lgkmcnt(9)\n\t" \
  "v_mfma_f32_16x16x32_bf16 v[208:211], v[208:211], v[240:243], 0\n\t" \
  "s_waitcnt lgkmcnt(7)\n\t" \
  "v_mfma_f32_16x16x32_bf16 v[208:211], v[212:215], v[244:247], v[208:211]\n\t" \
  "s_waitcnt lgkmcnt(6)\n\t" \
  "v_pk_add_f32 v[216:217], v[216:217], v[220:221]\n\t" \
  "v_pk_add_f32 v[218:219], v[218:219], v[222:223]\n\t" \
  "s_waitcnt lgkmcnt(4)\n\t" \
  "v_pk_add_f32 v[224:225], v[224:225], v[228:229]\n\t" \
  "v_pk_add_f32 v[226:227], v[226:227], v[230:231]\n\t" \
  "v_pk_add_f32 v[216:217], v[216:217], v[224:225]\n\t" \
  "v_pk_add_f32 v[218:219], v[218:219], v[226:227]\n\t" \
  "s_waitcnt lgkmcnt(2)\n\t" \
  "v_mfma_f32_16x16x32_bf16 v[208:211], v[200:203], v[232:235], v[208:211]\n\t" \
  "v_max3_f32 v224, v216, v217, v218\n\t" \
  "v_max_f32 v224, v224, v219\n\t" \
  "s_waitcnt lgkmcnt(0)\n\t" \
  "v_mfma_f32_16x16x32_bf16 v[208:211], v[204:207], v[236:239], v[208:211]\n\t" \
  "v_max_f32_dpp v225, v224, v224 row_ror:8 row_mask:0xf bank_mask:0xf\n\t" \
  "v_max_f32_dpp v226, v224, v224 row_ror:8 row_mask:0xf bank_mask:0xf\n\t" \
  "s_nop 1\n\t" \
  "v_permlane32_swap_b32 v225, v226\n\t" \
  "v_max_f32 v231, v225, v226\n\t" \
  "v_mov_b32 v220, 0x3f803f80\n\t" \
  "v_mov_b32 v221, 0x3f803f80\n\t" \
  "v_mov_b64 v[222:223], v[220:221]\n\t" \
  "v_mov_b32 v241, 0\n\t" \
  "v_mov_b32 v242, 0\n\t" \
  "v_max3_f32 v212, v208, v209, v231\n\t" \
  "v_max3_f32 v213, v212, v210, v211\n\t" \
  "v_max3_f32 v214, v212, v210, v211\n\t" \
  "v_cndmask_b32 v208, v208, v210, %[up]\n\t" \
  "v_cndmask_b32 v209, v209, v211, %[up]\n\t" \
  "v_permlane16_swap_b32 v213, v214\n\t" \
  "v_max_f32 v212, v213, v214\n\t" \
  "v_mul_f32 v212, %[negc], v212\n\t" \
  "v_fma_f32 v208, v208, %[c], v212\n\t" \
  "v_fma_f32 v209, v209, %[c], v212\n\t" \
  "v_exp_f32 v208, v208\n\t" \
  "v_fma_f32 v216, v216, %[c], v212\n\t" \
  "v_exp_f32 v209, v209\n\t" \
  "v_fma_f32 v217, v217, %[c], v212\n\t" \
  "v_exp_f32 v216, v216\n\t" \
  "v_fma_f32 v218, v218, %[c], v212\n\t" \
  "v_cvt_pk_bf16_f32 v240, v208, v209\n\t" \
  "v_cvt_pk_bf16_f32 v243, v208, v209\n\t" \
  "v_exp_f32 v217, v217\n\t" \
  "v_fma_f32 v219, v219, %[c], v212\n\t" \
  "v_mfma_f32_16x16x16_bf16 v[204:207], v[220:221], v[240:241], 0\n\t" \
  "v_exp_f32 v218, v218\n\t" \
  "s_waitcnt vmcnt(0)\n\t" \
  "v_mfma_f32_16x16x16_bf16 v[224:227], v[252:253], v[240:241], 0\n\t" \
  "v_cvt_pk_bf16_f32 v202, v216, v217\n\t" \
  "v_cvt_pk_bf16_f32 v200, v216, v217\n\t" \
  "v_exp_f32 v219, v219\n\t" \
  "v_mfma_f32_16x16x16_bf16 v[228:231], v[252:253], v[242:243], 0\n\t" \
  "v_cvt_pk_bf16_f32 v203, v218, v219\n\t" \
  "v_cvt_pk_bf16_f32 v201, v218, v219\n\t" \
  "v_mov_b32_dpp v200, v202 row_ror:8 row_mask:0xf bank_mask:0xc\n\t" \
  "v_mov_b32_dpp v202, v202 row_ror:8 row_mask:0xf bank_mask:0x3\n\t" \
  "v_mov_b32_dpp v201, v203 row_ror:8 row_mask:0xf bank_mask:0xc\n\t" \
  "v_mov_b32_dpp v203, v203 row_ror:8 row_mask:0xf bank_mask:0x3\n\t" \
  "v_max_u32 v212, v252, v253\n\t" \
  "v_cmp_le_u32 vcc, %[sent], v212\n\t" \
  "v_mfma_f32_16x16x32_bf16 v[204:207], v[220:223], v[200:203], v[204:207]\n\t" \
  ".Lpv%=:\n\t" \
  "v_mfma_f32_16x16x32_bf16 v[224:227], v[192:195], v[200:203], v[224:227]\n\t" \
  "v_mfma_f32_16x16x32_bf16 v[228:231], v[196:199], v[200:203], v[228:231]\n\t" \
  "s_cbranch_vccnz .Lvs%=\n\t" \
  ".Lvok%=:\n\t" \
  "s_nop 4\n\t" \
  "v_rcp_f32 v212, v204\n\t" \
  "v_cndmask_b32 v224, v224, v226, %[nh]\n\t" \
  "v_cndmask_b32 v225, v225, v227, %[nh]\n\t" \
  "v_cndmask_b32 v228, v228, v230, %[nh]\n\t" \
  "v_cndmask_b32 v229, v229, v231, %[nh]\n\t" \
  "v_pk_mul_f32 v[224:225], v[224:225], v[212:213] op_sel_hi:[1,0]\n\t" \
  "v_pk_mul_f32 v[228:229], v[228:229], v[212:213] op_sel_hi:[1,0]\n\t" \
  "v_cvt_pk_bf16_f32 v224, v224, v225\n\t" \
  "v_cvt_pk_bf16_f32 v225, v228, v229\n\t" \
  "buffer_store_dwordx2 v[224:225], v251, %[rs], %[so] offen sc1\n\t" \
  "s_memtime s[90:91]\n\t" \
  "s_setprio 0\n\t" \
  "s_waitcnt lgkmcnt(0)\n\t" \
  "s_mov_b32 %[tout], s90\n\t" \
  "s_mov_b32 %[spins], s92\n\t" \
  "s_branch .Lend%=\n\t" \
  ".Lre%=:\n\t" \
  "ds_read_b64_tr_b16 v[244:245], v250 offset:1536\n\t" \
  "ds_read_b64_tr_b16 v[246:247], v250 offset:1600\n\t" \
  "ds_read_b64_tr_b16 v[232:233], v250 offset:0\n\t" \
  "ds_read_b64_tr_b16 v[234:235], v250 offset:64\n\t" \
  "ds_read_b64_tr_b16 v[236:237], v250 offset:512\n\t" \
  "ds_read_b64_tr_b16 v[238:239], v250 offset:576\n\t" \
  "s_waitcnt lgkmcnt(0)\n\t" \
  "s_branch .Lrb%=\n\t" \
  ".Lvs%=:\n\t" \
  "v_cmp_eq_u32 vcc, %[sent], v252\n\t" \
  "v_cmp_eq_u32 s[94:95], %[sent], v253\n\t" \
  "s_or_b64 s[94:95], s[94:95], vcc\n\t" \
  "s_cbranch_scc0 .Lvok%=\n\t" \
  "s_add_u32 s92, s92, 1\n\t" \
  "s_sleep 2\n\t" \
  "s_and_b32 s93, s92, 63\n\t" \
  "s_cbranch_scc1 .Lvr%=\n\t" \
  "buffer_load_dword v212, off, %[rs], %[sab] sc1\n\t" \
  "s_waitcnt vmcnt(0)\n\t" \
  "v_readfirstlane_b32 s93, v212\n\t" \
  "s_nop 3\n\t" \
  "s_cmp_eq_u32 s93, 1\n\t" \
  "s_cbranch_scc1 .Lvok%=\n\t" \
  "s_cmp_lt_u32 s92, 0x80000\n\t" \
  "s_cbranch_scc1 .Lvr%=\n\t" \
  "v_mov_b32 v212, 1\n\t" \
  "buffer_store_dword v212, off, %[rs], %[sab] sc1\n\t" \
  "s_branch .Lvok%=\n\t" \
  ".Lvr%=:\n\t" \
  "buffer_load_dword v252, v249, %[rs], %[sv] offen offset:0 sc1\n\t" \
  "buffer_load_dword v253, v249, %[rs], %[sv] offen offset:256 sc1\n\t" \
  "s_waitcnt vmcnt(0)\n\t" \
  "v_mfma_f32_16x16x16_bf16 v[224:227], v[252:253], v[240:241], 0\n\t" \
  "v_mfma_f32_16x16x16_bf16 v[228:231], v[252:253], v[242:243], 0\n\t" \
  "v_max_u32 v212, v252, v253\n\t" \
  "v_cmp_le_u32 vcc, %[sent], v212\n\t" \
  "s_nop 4\n\t" \
  "s_branch .Lpv%=\n\t" \
  "s_branch .Lend%=\n\t" \
  ".Lslow%=:\n\t" \
  "s_mov_b64 s[94:95], 0\n\t" \
  "v_cmp_eq_u32 vcc, %[sent], v200\n\t" \
  "s_or_b64 s[94:95], s[94:95], vcc\n\t" \
  "v_cmp_eq_u32 vcc, %[sent], v201\n\t" \
  "s_or_b64 s[94:95], s[94:95], vcc\n\t" \
  "v_cmp_eq_u32 vcc, %[sent], v202\n\t" \
  "s_or_b64 s[94:95], s[94:95], vcc\n\t" \
  "v_cmp_eq_u32 vcc, %[sent], v203\n\t" \
  "s_or_b64 s[94:95], s[94:95], vcc\n\t" \
  "v_cmp_eq_u32 vcc, %[sent], v204\n\t" \
  "s_or_b64 s[94:95], s[94:95], vcc\n\t" \
  "v_cmp_eq_u32 vcc, %[sent], v205\n\t" \
  "s_or_b64 s[94:95], s[94:95], vcc\n\t" \
  "v_cmp_eq_u32 vcc, %[sent], v206\n\t" \
  "s_or_b64 s[94:95], s[94:95], vcc\n\t" \
  "v_cmp_eq_u32 vcc, %[sent], v207\n\t" \
  "s_or_b64 s[94:95], s[94:95], vcc\n\t" \
  "s_cmp_eq_u64 s[94:95], 0\n\t" \
  "s_cbranch_scc1 .Lok%=\n\t" \
  "v_mov_b64 v[192:193], 0\n\t" \
  "v_mov_b64 v[194:195], 0\n\t" \
  "v_mov_b64 v[196:197], 0\n\t" \
  "v_mov_b64 v[198:199], 0\n\t" \
  "s_add_u32 s92, s92, 1\n\t" \
  "s_sleep 6\n\t" \
  "s_cmp_eq_u32 s92, 1\n\t" \
  "s_cbranch_scc1 .Lpoll%=\n\t" \
  "s_sleep 1\n\t" \
  "s_and_b32 s93, s92, 63\n\t" \
  "s_cmp_lg_u32 s93, 0\n\t" \
  "s_cbranch_scc1 .Lpoll%=\n\t" \
  "v_mov_b32 v245, 0\n\t" \
  "buffer_load_dword v244, v245, %[rs], %[sab] offen sc1\n\t" \
  "s_waitcnt vmcnt(0)\n\t" \
  "v_readfirstlane_b32 s93, v244\n\t" \
  "s_nop 3\n\t" \
  "s_cmp_eq_u32 s93, 1\n\t" \
  "s_cbranch_scc1 .Lok%=\n\t" \
  "s_cmp_lt_u32 s92, 0x80000\n\t" \
  "s_cbranch_scc1 .Lpoll%=\n\t" \
  "v_mov_b32 v244, 1\n\t" \
  "buffer_store_dword v244, v245, %[rs], %[sab] offen sc1\n\t" \
  "s_branch .Lok%=\n\t" \
  ".Lend%=:\n\t"
#define QK_ASM_3 \
  "s_waitcnt lgkmcnt(0)\n\t" \
  "v_subrev_u32 v244, 0x8000, v250\n\t" \
  "ds_read_b128 v[216:219], v248 offset:45056\n\t" \
  "ds_read_b128 v[220:223], v248 offset:46080\n\t" \
  "ds_read_b128 v[224:227], v248 offset:47104\n\t" \
  "ds_read_b128 v[228:231], v248 offset:48128\n\t" \
  "ds_read_b128 v[208:211], v244 offset:0\n\t" \
  "ds_read_b128 v[212:215], v244 offset:1024\n\t" \
  "ds_read_b128 v[236:239], v244 offset:2048\n\t" \
  "ds_read_b128 v[240:243], v244 offset:3072\n\t" \
  "v_mov_b64 v[192:193], 0\n\t" \
  "v_mov_b64 v[194:195], 0\n\t" \
  "v_mov_b64 v[196:197], 0\n\t" \
  "v_mov_b64 v[198:199], 0\n\t" \
  "s_movk_i32 s92, 0x2000\n\t" \
  ".Lwt%=:\n\t" \
  "ds_read_b64 v[200:201], v248 offset:30720\n\t" \
  "s_memrealtime s[90:91]\n\t" \
  "s_sub_u32 s92, s92, 1\n\t" \
  "s_waitcnt lgkmcnt(0)\n\t" \
  "v_readfirstlane_b32 s93, v200\n\t" \
  "v_readfirstlane_b32 %[tgt], v201\n\t" \
  "s_cbranch_scc1 .Lgo%=\n\t" \
  "s_cmp_eq_u32 s93, %[step]\n\t" \
  "s_cbranch_scc1 .Lwx%=\n\t" \
  "s_sleep 1\n\t" \
  "s_branch .Lwt%=\n\t" \
  ".Lwx%=:\n\t" \
  "s_lshl_b32 s93, s90, 2\n\t" \
  "s_sub_i32 s93, %[tgt], s93\n\t" \
  "s_cmp_le_i32 s93, 0\n\t" \
  "s_cbranch_scc1 .Lgo%=\n\t" \
  "s_min_i32 s93, s93, 8000\n\t" \
  "s_mov_b32 s94, s93\n\t" \
  ".Lsl%=:\n\t" \
  "s_sub_i32 s94, s94, 18\n\t" \
  "s_cmp_lt_i32 s94, 30\n\t" \
  "s_cbranch_scc1 .Lsx%=\n\t" \
  "s_sleep 1\n\t" \
  "s_branch .Lsl%=\n\t" \
  ".Lsx%=:\n\t" \
  "s_sub_i32 s93, s93, 12\n\t" \
  "s_max_i32 s93, s93, 4\n\t" \
  "s_lshr_b32 s93, s93, 2\n\t" \
  "s_add_u32 s93, s93, s90\n\t" \
  "s_memrealtime s[90:91]\n\t" \
  ".Lpace%=:\n\t" \
  "s_memrealtime s[94:95]\n\t" \
  "s_waitcnt lgkmcnt(1)\n\t" \
  "s_sub_i32 s92, s90, s93\n\t" \
  "s_cmp_ge_i32 s92, 0\n\t" \
  "s_cbranch_scc1 .Le%=\n\t" \
  "s_memrealtime s[90:91]\n\t" \
  "s_waitcnt lgkmcnt(1)\n\t" \
  "s_sub_i32 s92, s94, s93\n\t" \
  "s_cmp_lt_i32 s92, 0\n\t" \
  "s_cbranch_scc1 .Lpace%=\n\t" \
  "s_nop 4\n\t" \
  ".Le%=:\n\t" \
  "s_add_u32 s92, s92, s93\n\t" \
  "s_lshl_b32 s92, s92, 2\n\t" \
  "s_sub_i32 s92, %[tgt], s92\n\t" \
  "s_max_i32 s92, s92, 0\n\t" \
  "s_bitcmp0_b32 s92, 3\n\t" \
  "s_cbranch_scc1 .Lf3%=\n\t" \
  "s_nop 15\n\t" \
  ".Lf3%=:\n\t" \
  "s_bitcmp0_b32 s92, 2\n\t" \
  "s_cbranch_scc1 .Lf2%=\n\t" \
  "s_nop 10\n\t" \
  ".Lf2%=:\n\t" \
  ".Lgo%=:\n\t" \
  "s_mov_b32 s92, 0\n\t" \
  ".Lpoll%=:\n\t" \
  "buffer_load_dwordx4 v[200:203], v248, %[rs], %[som] offen sc1\n\t" \
  "buffer_load_dwordx4 v[204:207], v248, %[rs], %[som2] offen sc1\n\t" \
  "s_waitcnt vmcnt(1) lgkmcnt(0)\n\t" \
  "ds_write_b128 v249, v[200:203]\n\t" \
  "v_smfmac_f32_16x16x64_bf16 v[192:195], v[200:203], v[208:215], v251\n\t" \
  "v_max3_u32 v244, v200, v201, v202\n\t" \
  "v_smfmac_f32_16x16x64_bf16 v[196:199], v[200:203], v[236:243], v251\n\t" \
  "s_waitcnt vmcnt(0)\n\t" \
  "ds_write_b128 v249, v[204:207] offset:8192\n\t" \
  "v_smfmac_f32_16x16x64_bf16 v[192:195], v[204:207], v[216:223], v251\n\t" \
  "v_max3_u32 v244, v244, v203, v204\n\t" \
  "v_max3_u32 v244, v244, v205, v206\n\t" \
  "v_smfmac_f32_16x16x64_bf16 v[196:199], v[204:207], v[224:231], v251\n\t" \
  "v_max_u32 v244, v244, v207\n\t" \
  "v_cmp_le_u32 vcc, %[sent], v244\n\t" \
  "s_cbranch_vccnz .Lslow%=\n\t" \
  ".Lok%=:\n\t" \
  "ds_read_b128 v[216:219], v250 offset:32768\n\t" \
  "ds_read_b128 v[220:223], v250 offset:33792\n\t" \
  "ds_read_b128 v[224:227], v250 offset:34816\n\t" \
  "ds_read_b128 v[228:231], v250 offset:35840\n\t" \
  "s_waitcnt lgkmcnt(4)\n\t" \
  "s_barrier\n\t" \
  "ds_read_b128 v[208:211], v248 offset:2048\n\t" \
  "ds_read_b128 v[212:215], v248 offset:5120\n\t" \
  "ds_read_b128 v[236:239], v248 offset:6144\n\t" \
  "ds_read_b128 v[240:243], v248 offset:7168\n\t" \
  "s_waitcnt lgkmcnt(3)\n\t" \
  "ds_read_b128 v[244:247], v248 offset:12288\n\t" \
  "v_smfmac_f32_16x16x64_bf16 v[192:195], v[208:211], v[216:223], v251\n\t" \
  "ds_read_b128 v[216:219], v250 offset:0\n\t" \
  "v_smfmac_f32_16x16x64_bf16 v[196:199], v[208:211], v[224:231], v251\n\t" \
  "s_waitcnt lgkmcnt(4)\n\t" \
  "ds_read_b128 v[220:223], v250 offset:1024\n\t" \
  "v_smfmac_f32_16x16x64_bf16 v[192:195], v[212:215], v[0:7], v251\n\t" \
  "ds_read_b128 v[224:227], v250 offset:2048\n\t" \
  "v_smfmac_f32_16x16x64_bf16 v[196:199], v[212:215], v[8:15], v251\n\t" \
  "s_waitcnt lgkmcnt(5)\n\t" \
  "ds_read_b128 v[228:231], v250 offset:3072\n\t" \
  "v_smfmac_f32_16x16x64_bf16 v[192:195], v[236:239], v[16:23], v251\n\t" \
  "ds_read_b128 v[200:203], v248 offset:8192\n\t" \
  "v_smfmac_f32_16x16x64_bf16 v[196:199], v[236:239], v[24:31], v251\n\t" \
  "s_waitcnt lgkmcnt(6)\n\t" \
  "ds_read_b128 v[204:207], v248 offset:9216\n\t" \
  "v_smfmac_f32_16x16x64_bf16 v[192:195], v[240:243], v[32:39], v251\n\t" \
  "ds_read_b128 v[208:211], v248 offset:10240\n\t" \
  "v_smfmac_f32_16x16x64_bf16 v[196:199], v[240:243], v[40:47], v251\n\t" \
  "s_waitcnt lgkmcnt(5)\n\t" \
  "ds_read_b128 v[212:215], v248 offset:13312\n\t" \
  "v_smfmac_f32_16x16x64_bf16 v[192:195], v[244:247], v[216:223], v251\n\t" \
  "s_waitcnt lgkmcnt(4)\n\t" \
  "v_smfmac_f32_16x16x64_bf16 v[196:199], v[244:247], v[224:231], v251\n\t" \
  "s_waitcnt lgkmcnt(3)\n\t" \
  "ds_read_b128 v[236:239], v248 offset:14336\n\t" \
  "v_smfmac_f32_16x16x64_bf16 v[192:195], v[200:203], v[48:55], v251\n\t" \
  "ds_read_b128 v[240:243], v248 offset:15360\n\t" \
  "v_smfmac_f32_16x16x64_bf16 v[196:199], v[200:203], v[56:63], v251\n\t" \
  "s_waitcnt lgkmcnt(4)\n\t" \
  "v_smfmac_f32_16x16x64_bf16 v[192:195], v[204:207], v[64:71], v251\n\t" \
  "ds_read_b128 v[244:247], v248 offset:0\n\t" \
  "v_smfmac_f32_16x16x64_bf16 v[196:199], v[204:207], v[72:79], v251\n\t" \
  "s_waitcnt lgkmcnt(4)\n\t" \
  "v_smfmac_f32_16x16x64_bf16 v[192:195], v[208:211], v[80:87], v251\n\t" \
  "ds_read_b128 v[200:203], v248 offset:1024\n\t" \
  "v_smfmac_f32_16x16x64_bf16 v[196:199], v[208:211], v[88:95], v251\n\t" \
  "s_waitcnt lgkmcnt(4)\n\t" \
  "v_smfmac_f32_16x16x64_bf16 v[192:195], v[212:215], v[96:103], v251\n\t" \
  "ds_read_b128 v[204:207], v248 offset:4096\n\t" \
  "v_smfmac_f32_16x16x64_bf16 v[196:199], v[212:215], v[104:111], v251\n\t" \
  "s_waitcnt lgkmcnt(3)\n\t" \
  "v_smfmac_f32_16x16x64_bf16 v[192:195], v[236:239], v[112:119], v251\n\t" \
  "v_smfmac_f32_16x16x64_bf16 v[196:199], v[236:239], v[120:127], v251\n\t" \
  "v_smfmac_f32_16x16x64_bf16 v[192:195], v[240:243], v[128:135], v251\n\t" \
  "v_smfmac_f32_16x16x64_bf16 v[196:199], v[240:243], v[136:143], v251\n\t" \
  "s_waitcnt lgkmcnt(1)\n\t" \
  "v_smfmac_f32_16x16x64_bf16 v[192:195], v[244:247], v[144:151], v251\n\t" \
  "v_smfmac_f32_16x16x64_bf16 v[196:199], v[244:247], v[152:159], v251\n\t" \
  "v_smfmac_f32_16x16x64_bf16 v[192:195], v[200:203], v[160:167], v251\n\t" \
  "v_smfmac_f32_16x16x64_bf16 v[196:199], v[200:203], v[168:175], v251\n\t" \
  "s_waitcnt lgkmcnt(0)\n\t" \
  "v_smfmac_f32_16x16x64_bf16 v[192:195], v[204:207], v[176:183], v251\n\t" \
  "v_smfmac_f32_16x16x64_bf16 v[196:199], v[204:207], v[184:191], v251\n\t" \
  "ds_read_b128 v[200:203], v248 offset:19456\n\t" \
  "ds_read_b128 v[204:207], v248 offset:23552\n\t" \
  "v_mov_b32 v250, v232\n\t" \
  "v_mov_b32 v249, v233\n\t" \
  "s_nop 2\n\t" \
  "v_add_f32 v192, v192, v194\n\t" \
  "v_add_f32 v193, v193, v195\n\t" \
  "v_add_f32 v196, v196, v198\n\t" \
  "v_add_f32 v197, v197, v199\n\t" \
  "v_cvt_pk_bf16_f32 v192, v192, v193\n\t" \
  "v_cvt_pk_bf16_f32 v196, v196, v197\n\t" \
  "ds_write2_b32 v252, v192, v196 offset1:64\n\t" \
  "v_mov_b32 v251, v234\n\t" \
  "ds_read_b64_tr_b16 v[244:245], v250 offset:1536\n\t" \
  "ds_read_b64_tr_b16 v[246:247], v250 offset:1600\n\t" \
  "v_mov_b32 v212, %[step]\n\t" \
  "s_mov_b64 exec, 1\n\t" \
  "ds_write_b32 v248, v212 offset:28720\n\t" \
  "s_mov_b64 exec, -1\n\t" \
  "buffer_load_dwordx4 v[192:195], v248, %[rt], %[svt] offen offset:0 sc1\n\t" \
  "buffer_load_dwordx4 v[196:199], v248, %[rt], %[svt] offen offset:1024 sc1\n\t" \
  "s_waitcnt lgkmcnt(1)\n\t" \
  "v_mfma_f32_16x16x32_bf16 v[200:203], v[200:203], v[244:247], 0\n\t" \
  "v_mfma_f32_16x16x32_bf16 v[204:207], v[204:207], v[244:247], 0\n\t" \
  "s_nop 7\n\t" \
  "v_cndmask_b32 v216, v200, v204, %[nh]\n\t" \
  "v_cndmask_b32 v217, v201, v205, %[nh]\n\t" \
  "v_cndmask_b32 v218, v202, v206, %[nh]\n\t" \
  "v_cndmask_b32 v219, v203, v207, %[nh]\n\t" \
  "ds_write_b128 v248, v[216:219] offset:19456\n\t" \
  "buffer_load_dword v252, v249, %[rs], %[sv] offen offset:0 sc1\n\t" \
  "buffer_load_dword v253, v249, %[rs], %[sv] offen offset:256 sc1\n\t" \
  "ds_read_b32 v212, v248 offset:28672\n\t" \
  "ds_read_b64_tr_b16 v[232:233], v250 offset:0\n\t" \
  "ds_read_b64_tr_b16 v[234:235], v250 offset:64\n\t" \
  "ds_read_b64_tr_b16 v[236:237], v250 offset:512\n\t" \
  "ds_read_b64_tr_b16 v[238:239], v250 offset:576\n\t" \
  "ds_read_b64_tr_b16 v[240:241], v250 offset:1024\n\t" \
  "ds_read_b64_tr_b16 v[242:243], v250 offset:1088\n\t" \
  "s_setprio 3\n\t" \
  "s_waitcnt lgkmcnt(0)\n\t" \
  "v_cmp_ne_u32 vcc, %[step], v212\n\t" \
  "s_barrier\n\t" \
  "s_and_b32 s93, vcc_lo, 0xf\n\t" \
  "s_cbranch_scc1 .Lre%=\n\t" \
  ".Lrb%=:\n\t" \
  "ds_read_b64_tr_b16 v[212:213], v250 offset:3584\n\t" \
  "ds_read_b64_tr_b16 v[214:215], v250 offset:3648\n\t" \
  "ds_read_b64_tr_b16 v[200:201], v250 offset:2048\n\t" \
  "ds_read_b64_tr_b16 v[202:203], v250 offset:2112\n\t" \
  "ds_read_b128 v[220:223], v248 offset:16384\n\t" \
  "ds_read_b128 v[224:227], v248 offset:17408\n\t" \
  "ds_read_b128 v[228:231], v248 offset:18432\n\t" \
  "ds_read_b64_tr_b16 v[204:205], v250 offset:2560\n\t" \
  "ds_read_b64_tr_b16 v[206:207], v250 offset:2624\n\t" \
  "ds_read_b64_tr_b16 v[208:209], v250 offset:3072\n\t" \
  "ds_read_b64_tr_b16 v[210:211], v250 offset:3136\n\t" \
  "s_waitcnt lgkmcnt(9)\n\t" \
  "v_mfma_f32_16x16x32_bf16 v[212:215], v[212:215], v[244:247], 0\n\t" \
  "s_waitcnt lgkmcnt(7)\n\t" \
  "v_mfma_f32_16x16x32_bf16 v[212:215], v[200:203], v[232:235], v[212:215]\n\t" \
  "s_waitcnt lgkmcnt(6)\n\t" \
  "v_pk_add_f32 v[216:217], v[216:217], v[220:221]\n\t" \
  "v_pk_add_f32 v[218:219], v[218:219], v[222:223]\n\t" \
  "s_waitcnt lgkmcnt(4)\n\t" \
  "v_pk_add_f32 v[224:225], v[224:225], v[228:229]\n\t" \
  "v_pk_add_f32 v[226:227], v[226:227], v[230:231]\n\t" \
  "v_pk_add_f32 v[216:217], v[216:217], v[224:225]\n\t" \
  "v_pk_add_f32 v[218:219], v[218:219], v[226:227]\n\t" \
  "s_waitcnt lgkmcnt(2)\n\t" \
  "v_mfma_f32_16x16x32_bf16 v[212:215], v[204:207], v[236:239], v[212:215]\n\t" \
  "v_max3_f32 v224, v216, v217, v218\n\t" \
  "v_max_f32 v224, v224, v219\n\t" \
  "s_waitcnt lgkmcnt(0)\n\t" \
  "v_mfma_f32_16x16x32_bf16 v[212:215], v[208:211], v[240:243], v[212:215]\n\t" \
  "v_max_f32_dpp v225, v224, v224 row_ror:8 row_mask:0xf bank_mask:0xf\n\t" \
  "v_max_f32_dpp v226, v224, v224 row_ror:8 row_mask:0xf bank_mask:0xf\n\t" \
  "s_nop 1\n\t" \
  "v_permlane32_swap_b32 v225, v226\n\t" \
  "v_max_f32 v231, v225, v226\n\t" \
  "v_mov_b32 v220, 0x3f803f80\n\t" \
  "v_mov_b32 v221, 0x3f803f80\n\t" \
  "v_mov_b64 v[222:223], v[220:221]\n\t" \
  "v_mov_b32 v245, 0\n\t" \
  "v_mov_b32 v246, 0\n\t" \
  "v_max3_f32 v200, v212, v213, v231\n\t" \
  "v_max3_f32 v201, v200, v214, v215\n\t" \
  "v_max3_f32 v202, v200, v214, v215\n\t" \
  "v_cndmask_b32 v212, v212, v214, %[up]\n\t" \
  "v_cndmask_b32 v213, v213, v215, %[up]\n\t" \
  "v_permlane16_swap_b32 v201, v202\n\t" \
  "v_max_f32 v200, v201, v202\n\t" \
  "v_mul_f32 v200, %[negc], v200\n\t" \
  "v_fma_f32 v212, v212, %[c], v200\n\t" \
  "v_fma_f32 v213, v213, %[c], v200\n\t" \
  "v_exp_f32 v212, v212\n\t" \
  "v_fma_f32 v216, v216, %[c], v200\n\t" \
  "v_exp_f32 v213, v213\n\t" \
  "v_fma_f32 v217, v217, %[c], v200\n\t" \
  "v_exp_f32 v216, v216\n\t" \
  "v_fma_f32 v218, v218, %[c], v200\n\t" \
  "v_cvt_pk_bf16_f32 v244, v212, v213\n\t" \
  "v_cvt_pk_bf16_f32 v247, v212, v213\n\t" \
  "v_exp_f32 v217, v217\n\t" \
  "v_fma_f32 v219, v219, %[c], v200\n\t" \
  "v_mfma_f32_16x16x16_bf16 v[208:211], v[220:221], v[244:245], 0\n\t" \
  "v_exp_f32 v218, v218\n\t" \
  "s_waitcnt vmcnt(0)\n\t" \
  "v_mfma_f32_16x16x16_bf16 v[224:227], v[252:253], v[244:245], 0\n\t" \
  "v_cvt_pk_bf16_f32 v206, v216, v217\n\t" \
  "v_cvt_pk_bf16_f32 v204, v216, v217\n\t" \
  "v_exp_f32 v219, v219\n\t" \
  "v_mfma_f32_16x16x16_bf16 v[228:231], v[252:253], v[246:247], 0\n\t" \
  "v_cvt_pk_bf16_f32 v207, v218, v219\n\t" \
  "v_cvt_pk_bf16_f32 v205, v218, v219\n\t" \
  "v_mov_b32_dpp v204, v206 row_ror:8 row_mask:0xf bank_mask:0xc\n\t" \
  "v_mov_b32_dpp v206, v206 row_ror:8 row_mask:0xf bank_mask:0x3\n\t" \
  "v_mov_b32_dpp v205, v207 row_ror:8 row_mask:0xf bank_mask:0xc\n\t" \
  "v_mov_b32_dpp v207, v207 row_ror:8 row_mask:0xf bank_mask:0x3\n\t" \
  "v_max_u32 v200, v252, v253\n\t" \
  "v_cmp_le_u32 vcc, %[sent], v200\n\t" \
  "v_mfma_f32_16x16x32_bf16 v[208:211], v[220:223], v[204:207], v[208:211]\n\t" \
  ".Lpv%=:\n\t" \
  "v_mfma_f32_16x16x32_bf16 v[224:227], v[192:195], v[204:207], v[224:227]\n\t" \
  "v_mfma_f32_16x16x32_bf16 v[228:231], v[196:199], v[204:207], v[228:231]\n\t" \
  "s_cbranch_vccnz .Lvs%=\n\t" \
  ".Lvok%=:\n\t" \
  "s_nop 4\n\t" \
  "v_rcp_f32 v200, v208\n\t" \
  "v_cndmask_b32 v224, v224, v226, %[nh]\n\t" \
  "v_cndmask_b32 v225, v225, v227, %[nh]\n\t" \
  "v_cndmask_b32 v228, v228, v230, %[nh]\n\t" \
  "v_cndmask_b32 v229, v229, v231, %[nh]\n\t" \
  "v_pk_mul_f32 v[224:225], v[224:225], v[200:201] op_sel_hi:[1,0]\n\t" \
  "v_pk_mul_f32 v[228:229], v[228:229], v[200:201] op_sel_hi:[1,0]\n\t" \
  "v_cvt_pk_bf16_f32 v224, v224, v225\n\t" \
  "v_cvt_pk_bf16_f32 v225, v228, v229\n\t" \
  "buffer_store_dwordx2 v[224:225], v251, %[rs], %[so] offen sc1\n\t" \
  "s_memtime s[90:91]\n\t" \
  "s_setprio 0\n\t" \
  "s_waitcnt lgkmcnt(0)\n\t" \
  "s_mov_b32 %[tout], s90\n\t" \
  "s_mov_b32 %[spins], s92\n\t" \
  "s_branch .Lend%=\n\t" \
  ".Lre%=:\n\t" \
  "ds_read_b64_tr_b16 v[232:233], v250 offset:0\n\t" \
  "ds_read_b64_tr_b16 v[234:235], v250 offset:64\n\t" \
  "ds_read_b64_tr_b16 v[236:237], v250 offset:512\n\t" \
  "ds_read_b64_tr_b16 v[238:239], v250 offset:576\n\t" \
  "ds_read_b64_tr_b16 v[240:241], v250 offset:1024\n\t" \
  "ds_read_b64_tr_b16 v[242:243], v250 offset:1088\n\t" \
  "s_waitcnt lgkmcnt(0)\n\t" \
  "s_branch .Lrb%=\n\t" \
  ".Lvs%=:\n\t" \
  "v_cmp_eq_u32 vcc, %[sent], v252\n\t" \
  "v_cmp_eq_u32 s[94:95], %[sent], v253\n\t" \
  "s_or_b64 s[94:95], s[94:95], vcc\n\t" \
  "s_cbranch_scc0 .Lvok%=\n\t" \
  "s_add_u32 s92, s92, 1\n\t" \
  "s_sleep 2\n\t" \
  "s_and_b32 s93, s92, 63\n\t" \
  "s_cbranch_scc1 .Lvr%=\n\t" \
  "buffer_load_dword v200, off, %[rs], %[sab] sc1\n\t" \
  "s_waitcnt vmcnt(0)\n\t" \
  "v_readfirstlane_b32 s93, v200\n\t" \
  "s_nop 3\n\t" \
  "s_cmp_eq_u32 s93, 1\n\t" \
  "s_cbranch_scc1 .Lvok%=\n\t" \
  "s_cmp_lt_u32 s92, 0x80000\n\t" \
  "s_cbranch_scc1 .Lvr%=\n\t" \
  "v_mov_b32 v200, 1\n\t" \
  "buffer_store_dword v200, off, %[rs], %[sab] sc1\n\t" \
  "s_branch .Lvok%=\n\t" \
  ".Lvr%=:\n\t" \
  "buffer_load_dword v252, v249, %[rs], %[sv] offen offset:0 sc1\n\t" \
  "buffer_load_dword v253, v249, %[rs], %[sv] offen offset:256 sc1\n\t" \
  "s_waitcnt vmcnt(0)\n\t" \
  "v_mfma_f32_16x16x16_bf16 v[224:227], v[252:253], v[244:245], 0\n\t" \
  "v_mfma_f32_16x16x16_bf16 v[228:231], v[252:253], v[246:247], 0\n\t" \
  "v_max_u32 v200, v252, v253\n\t" \
  "v_cmp_le_u32 vcc, %[sent], v200\n\t" \
  "s_nop 4\n\t" \
  "s_branch .Lpv%=\n\t" \
  "s_branch .Lend%=\n\t" \
  ".Lslow%=:\n\t" \
  "s_mov_b64 s[94:95], 0\n\t" \
  "v_cmp_eq_u32 vcc, %[sent], v200\n\t" \
  "s_or_b64 s[94:95], s[94:95], vcc\n\t" \
  "v_cmp_eq_u32 vcc, %[sent], v201\n\t" \
  "s_or_b64 s[94:95], s[94:95], vcc\n\t" \
  "v_cmp_eq_u32 vcc, %[sent], v202\n\t" \
  "s_or_b64 s[94:95], s[94:95], vcc\n\t" \
  "v_cmp_eq_u32 vcc, %[sent], v203\n\t" \
  "s_or_b64 s[94:95], s[94:95], vcc\n\t" \
  "v_cmp_eq_u32 vcc, %[sent], v204\n\t" \
  "s_or_b64 s[94:95], s[94:95], vcc\n\t" \
  "v_cmp_eq_u32 vcc, %[sent], v205\n\t" \
  "s_or_b64 s[94:95], s[94:95], vcc\n\t" \
  "v_cmp_eq_u32 vcc, %[sent], v206\n\t" \
  "s_or_b64 s[94:95], s[94:95], vcc\n\t" \
  "v_cmp_eq_u32 vcc, %[sent], v207\n\t" \
  "s_or_b64 s[94:95], s[94:95], vcc\n\t" \
  "s_cmp_eq_u64 s[94:95], 0\n\t" \
  "s_cbranch_scc1 .Lok%=\n\t" \
  "v_mov_b64 v[192:193], 0\n\t" \
  "v_mov_b64 v[194:195], 0\n\t" \
  "v_mov_b64 v[196:197], 0\n\t" \
  "v_mov_b64 v[198:199], 0\n\t" \
  "s_add_u32 s92, s92, 1\n\t" \
  "s_sleep 6\n\t" \
  "s_cmp_eq_u32 s92, 1\n\t" \
  "s_cbranch_scc1 .Lpoll%=\n\t" \
  "s_sleep 1\n\t" \
  "s_and_b32 s93, s92, 63\n\t" \
  "s_cmp_lg_u32 s93, 0\n\t" \
  "s_cbranch_scc1 .Lpoll%=\n\t" \
  "v_mov_b32 v245, 0\n\t" \
  "buffer_load_dword v244, v245, %[rs], %[sab] offen sc1\n\t" \
  "s_waitcnt vmcnt(0)\n\t" \
  "v_readfirstlane_b32 s93, v244\n\t" \
  "s_nop 3\n\t" \
  "s_cmp_eq_u32 s93, 1\n\t" \
  "s_cbranch_scc1 .Lok%=\n\t" \
  "s_cmp_lt_u32 s92, 0x80000\n\t" \
  "s_cbranch_scc1 .Lpoll%=\n\t" \
  "v_mov_b32 v244, 1\n\t" \
  "buffer_store_dword v244, v245, %[rs], %[sab] offen sc1\n\t" \
  "s_branch .Lok%=\n\t" \
  ".Lend%=:\n\t"
#define QK_ASM_4 \
  "s_waitcnt lgkmcnt(0)\n\t" \
  "v_subrev_u32 v244, 0x8000, v250\n\t" \
  "ds_read_b128 v[216:219], v248 offset:49152\n\t" \
  "ds_read_b128 v[220:223], v248 offset:50176\n\t" \
  "ds_read_b128 v[224:227], v248 offset:51200\n\t" \
  "ds_read_b128 v[228:231], v248 offset:52224\n\t" \
  "ds_read_b128 v[208:211], v244 offset:0\n\t" \
  "ds_read_b128 v[212:215], v244 offset:1024\n\t" \
  "ds_read_b128 v[236:239], v244 offset:2048\n\t" \
  "ds_read_b128 v[240:243], v244 offset:3072\n\t" \
  "v_mov_b64 v[192:193], 0\n\t" \
  "v_mov_b64 v[194:195], 0\n\t" \
  "v_mov_b64 v[196:197], 0\n\t" \
  "v_mov_b64 v[198:199], 0\n\t" \
  "s_movk_i32 s92, 0x2000\n\t" \
  ".Lwt%=:\n\t" \
  "ds_read_b64 v[200:201], v248 offset:30720\n\t" \
  "s_memrealtime s[90:91]\n\t" \
  "s_sub_u32 s92, s92, 1\n\t" \
  "s_waitcnt lgkmcnt(0)\n\t" \
  "v_readfirstlane_b32 s93, v200\n\t" \
  "v_readfirstlane_b32 %[tgt], v201\n\t" \
  "s_cbranch_scc1 .Lgo%=\n\t" \
  "s_cmp_eq_u32 s93, %[step]\n\t" \
  "s_cbranch_scc1 .Lwx%=\n\t" \
  "s_sleep 1\n\t" \
  "s_branch .Lwt%=\n\t" \
  ".Lwx%=:\n\t" \
  "s_lshl_b32 s93, s90, 2\n\t" \
  "s_sub_i32 s93, %[tgt], s93\n\t" \
  "s_cmp_le_i32 s93, 0\n\t" \
  "s_cbranch_scc1 .Lgo%=\n\t" \
  "s_min_i32 s93, s93, 8000\n\t" \
  "s_mov_b32 s94, s93\n\t" \
  ".Lsl%=:\n\t" \
  "s_sub_i32 s94, s94, 18\n\t" \
  "s_cmp_lt_i32 s94, 30\n\t" \
  "s_cbranch_scc1 .Lsx%=\n\t" \
  "s_sleep 1\n\t" \
  "s_branch .Lsl%=\n\t" \
  ".Lsx%=:\n\t" \
  "s_sub_i32 s93, s93, 12\n\t" \
  "s_max_i32 s93, s93, 4\n\t" \
  "s_lshr_b32 s93, s93, 2\n\t" \
  "s_add_u32 s93, s93, s90\n\t" \
  "s_memrealtime s[90:91]\n\t" \
  ".Lpace%=:\n\t" \
  "s_memrealtime s[94:95]\n\t" \
  "s_waitcnt lgkmcnt(1)\n\t" \
  "s_sub_i32 s92, s90, s93\n\t" \
  "s_cmp_ge_i32 s92, 0\n\t" \
  "s_cbranch_scc1 .Le%=\n\t" \
  "s_memrealtime s[90:91]\n\t" \
  "s_waitcnt lgkmcnt(1)\n\t" \
  "s_sub_i32 s92, s94, s93\n\t" \
  "s_cmp_lt_i32 s92, 0\n\t" \
  "s_cbranch_scc1 .Lpace%=\n\t" \
  "s_nop 4\n\t" \
  ".Le%=:\n\t" \
  "s_add_u32 s92, s92, s93\n\t" \
  "s_lshl_b32 s92, s92, 2\n\t" \
  "s_sub_i32 s92, %[tgt], s92\n\t" \
  "s_max_i32 s92, s92, 0\n\t" \
  "s_bitcmp0_b32 s92, 3\n\t" \
  "s_cbranch_scc1 .Lf3%=\n\t" \
  "s_nop 15\n\t" \
  ".Lf3%=:\n\t" \
  "s_bitcmp0_b32 s92, 2\n\t" \
  "s_cbranch_scc1 .Lf2%=\n\t" \
  "s_nop 10\n\t" \
  ".Lf2%=:\n\t" \
  ".Lgo%=:\n\t" \
  "s_mov_b32 s92, 0\n\t" \
  ".Lpoll%=:\n\t" \
  "buffer_load_dwordx4 v[200:203], v248, %[rs], %[som] offen sc1\n\t" \
  "buffer_load_dwordx4 v[204:207], v248, %[rs], %[som2] offen sc1\n\t" \
  "s_waitcnt vmcnt(1) lgkmcnt(0)\n\t" \
  "ds_write_b128 v249, v[200:203]\n\t" \
  "v_smfmac_f32_16x16x64_bf16 v[192:195], v[200:203], v[208:215], v251\n\t" \
  "v_max3_u32 v244, v200, v201, v202\n\t" \
  "v_smfmac_f32_16x16x64_bf16 v[196:199], v[200:203], v[236:243], v251\n\t" \
  "s_waitcnt vmcnt(0)\n\t" \
  "ds_write_b128 v249, v[204:207] offset:8192\n\t" \
  "v_smfmac_f32_16x16x64_bf16 v[192:195], v[204:207], v[216:223], v251\n\t" \
  "v_max3_u32 v244, v244, v203, v204\n\t" \
  "v_max3_u32 v244, v244, v205, v206\n\t" \
  "v_smfmac_f32_16x16x64_bf16 v[196:199], v[204:207], v[224:231], v251\n\t" \
  "v_max_u32 v244, v244, v207\n\t" \
  "v_cmp_le_u32 vcc, %[sent], v244\n\t" \
  "s_cbranch_vccnz .Lslow%=\n\t" \
  ".Lok%=:\n\t" \
  "ds_read_b128 v[216:219], v250 offset:32768\n\t" \
  "ds_read_b128 v[220:223], v250 offset:33792\n\t" \
  "ds_read_b128 v[224:227], v250 offset:34816\n\t" \
  "ds_read_b128 v[228:231], v250 offset:35840\n\t" \
  "s_waitcnt lgkmcnt(4)\n\t" \
  "s_barrier\n\t" \
  "ds_read_b128 v[208:211], v248 offset:5120\n\t" \
  "ds_read_b128 v[212:215], v248 offset:6144\n\t" \
  "ds_read_b128 v[236:239], v248 offset:7168\n\t" \
  "ds_read_b128 v[240:243], v248 offset:8192\n\t" \
  "s_waitcnt lgkmcnt(3)\n\t" \
  "ds_read_b128 v[244:247], v248 offset:13312\n\t" \
  "v_smfmac_f32_16x16x64_bf16 v[192:195], v[208:211], v[216:223], v251\n\t" \
  "ds_read_b128 v[216:219], v250 offset:0\n\t" \
  "v_smfmac_f32_16x16x64_bf16 v[196:199], v[208:211], v[224:231], v251\n\t" \
  "s_waitcnt lgkmcnt(4)\n\t" \
  "ds_read_b128 v[220:223], v250 offset:1024\n\t" \
  "v_smfmac_f32_16x16x64_bf16 v[192:195], v[212:215], v[0:7], v251\n\t" \
  "ds_read_b128 v[224:227], v250 offset:2048\n\t" \
  "v_smfmac_f32_16x16x64_bf16 v[196:199], v[212:215], v[8:15], v251\n\t" \
  "s_waitcnt lgkmcnt(5)\n\t" \
  "ds_read_b128 v[228:231], v250 offset:3072\n\t" \
  "v_smfmac_f32_16x16x64_bf16 v[192:195], v[236:239], v[16:23], v251\n\t" \
  "ds_read_b128 v[200:203], v248 offset:9216\n\t" \
  "v_smfmac_f32_16x16x64_bf16 v[196:199], v[236:239], v[24:31], v251\n\t" \
  "s_waitcnt lgkmcnt(6)\n\t" \
  "ds_read_b128 v[204:207], v248 offset:10240\n\t" \
  "v_smfmac_f32_16x16x64_bf16 v[192:195], v[240:243], v[32:39], v251\n\t" \
  "ds_read_b128 v[208:211], v248 offset:11264\n\t" \
  "v_smfmac_f32_16x16x64_bf16 v[196:199], v[240:243], v[40:47], v251\n\t" \
  "s_waitcnt lgkmcnt(5)\n\t" \
  "ds_read_b128 v[212:215], v248 offset:14336\n\t" \
  "v_smfmac_f32_16x16x64_bf16 v[192:195], v[244:247], v[216:223], v251\n\t" \
  "s_waitcnt lgkmcnt(4)\n\t" \
  "v_smfmac_f32_16x16x64_bf16 v[196:199], v[244:247], v[224:231], v251\n\t" \
  "s_waitcnt lgkmcnt(3)\n\t" \
  "ds_read_b128 v[236:239], v248 offset:15360\n\t" \
  "v_smfmac_f32_16x16x64_bf16 v[192:195], v[200:203], v[48:55], v251\n\t" \
  "ds_read_b128 v[240:243], v248 offset:0\n\t" \
  "v_smfmac_f32_16x16x64_bf16 v[196:199], v[200:203], v[56:63], v251\n\t" \
  "s_waitcnt lgkmcnt(4)\n\t" \
  "v_smfmac_f32_16x16x64_bf16 v[192:195], v[204:207], v[64:71], v251\n\t" \
  "ds_read_b128 v[244:247], v248 offset:1024\n\t" \
  "v_smfmac_f32_16x16x64_bf16 v[196:199], v[204:207], v[72:79], v251\n\t" \
  "s_waitcnt lgkmcnt(4)\n\t" \
  "v_smfmac_f32_16x16x64_bf16 v[192:195], v[208:211], v[80:87], v251\n\t" \
  "ds_read_b128 v[200:203], v248 offset:2048\n\t" \
  "v_smfmac_f32_16x16x64_bf16 v[196:199], v[208:211], v[88:95], v251\n\t" \
  "s_waitcnt lgkmcnt(4)\n\t" \
  "v_smfmac_f32_16x16x64_bf16 v[192:195], v[212:215], v[96:103], v251\n\t" \
  "ds_read_b128 v[204:207], v248 offset:3072\n\t" \
  "v_smfmac_f32_16x16x64_bf16 v[196:199], v[212:215], v[104:111], v251\n\t" \
  "s_waitcnt lgkmcnt(3)\n\t" \
  "v_smfmac_f32_16x16x64_bf16 v[192:195], v[236:239], v[112:119], v251\n\t" \
  "v_smfmac_f32_16x16x64_bf16 v[196:199], v[236:239], v[120:127], v251\n\t" \
  "v_smfmac_f32_16x16x64_bf16 v[192:195], v[240:243], v[128:135], v251\n\t" \
  "v_smfmac_f32_16x16x64_bf16 v[196:199], v[240:243], v[136:143], v251\n\t" \
  "s_waitcnt lgkmcnt(1)\n\t" \
  "v_smfmac_f32_16x16x64_bf16 v[192:195], v[244:247], v[144:151], v251\n\t" \
  "v_smfmac_f32_16x16x64_bf16 v[196:199], v[244:247], v[152:159], v251\n\t" \
  "v_smfmac_f32_16x16x64_bf16 v[192:195], v[200:203], v[160:167], v251\n\t" \
  "v_smfmac_f32_16x16x64_bf16 v[196:199], v[200:203], v[168:175], v251\n\t" \
  "s_waitcnt lgkmcnt(0)\n\t" \
  "v_smfmac_f32_16x16x64_bf16 v[192:195], v[204:207], v[176:183], v251\n\t" \
  "v_smfmac_f32_16x16x64_bf16 v[196:199], v[204:207], v[184:191], v251\n\t" \
  "s_nop 6\n\t" \
  "v_add_f32 v192, v192, v194\n\t" \
  "v_add_f32 v193, v193, v195\n\t" \
  "v_add_f32 v196, v196, v198\n\t" \
  "v_add_f32 v197, v197, v199\n\t" \
  "v_cvt_pk_bf16_f32 v192, v192, v193\n\t" \
  "v_cvt_pk_bf16_f32 v196, v196, v197\n\t" \
  "ds_write2_b32 v252, v192, v196 offset1:64\n\t" \
  "s_waitcnt lgkmcnt(0)\n\t" \
  "s_barrier\n\t" \
  "buffer_store_dword v192, v235, %[rs], %[sk] offen sc1\n\t" \
  "buffer_store_dword v196, v235, %[rs], %[sk] offen offset:256 sc1\n\t" \
  "s_mov_b32 %[spins], s92\n\t" \
  "s_branch .Lend%=\n\t" \
  ".Lslow%=:\n\t" \
  "s_mov_b64 s[94:95], 0\n\t" \
  "v_cmp_eq_u32 vcc, %[sent], v200\n\t" \
  "s_or_b64 s[94:95], s[94:95], vcc\n\t" \
  "v_cmp_eq_u32 vcc, %[sent], v201\n\t" \
  "s_or_b64 s[94:95], s[94:95], vcc\n\t" \
  "v_cmp_eq_u32 vcc, %[sent], v202\n\t" \
  "s_or_b64 s[94:95], s[94:95], vcc\n\t" \
  "v_cmp_eq_u32 vcc, %[sent], v203\n\t" \
  "s_or_b64 s[94:95], s[94:95], vcc\n\t" \
  "v_cmp_eq_u32 vcc, %[sent], v204\n\t" \
  "s_or_b64 s[94:95], s[94:95], vcc\n\t" \
  "v_cmp_eq_u32 vcc, %[sent], v205\n\t" \
  "s_or_b64 s[94:95], s[94:95], vcc\n\t" \
  "v_cmp_eq_u32 vcc, %[sent], v206\n\t" \
  "s_or_b64 s[94:95], s[94:95], vcc\n\t" \
  "v_cmp_eq_u32 vcc, %[sent], v207\n\t" \
  "s_or_b64 s[94:95], s[94:95], vcc\n\t" \
  "s_cmp_eq_u64 s[94:95], 0\n\t" \
  "s_cbranch_scc1 .Lok%=\n\t" \
  "v_mov_b64 v[192:193], 0\n\t" \
  "v_mov_b64 v[194:195], 0\n\t" \
  "v_mov_b64 v[196:197], 0\n\t" \
  "v_mov_b64 v[198:199], 0\n\t" \
  "s_add_u32 s92, s92, 1\n\t" \
  "s_sleep 6\n\t" \
  "s_cmp_eq_u32 s92, 1\n\t" \
  "s_cbranch_scc1 .Lpoll%=\n\t" \
  "s_sleep 1\n\t" \
  "s_and_b32 s93, s92, 63\n\t" \
  "s_cmp_lg_u32 s93, 0\n\t" \
  "s_cbranch_scc1 .Lpoll%=\n\t" \
  "v_mov_b32 v245, 0\n\t" \
  "buffer_load_dword v244, v245, %[rs], %[sab] offen sc1\n\t" \
  "s_waitcnt vmcnt(0)\n\t" \
  "v_readfirstlane_b32 s93, v244\n\t" \
  "s_nop 3\n\t" \
  "s_cmp_eq_u32 s93, 1\n\t" \
  "s_cbranch_scc1 .Lok%=\n\t" \
  "s_cmp_lt_u32 s92, 0x80000\n\t" \
  "s_cbranch_scc1 .Lpoll%=\n\t" \
  "v_mov_b32 v244, 1\n\t" \
  "buffer_store_dword v244, v245, %[rs], %[sab] offen sc1\n\t" \
  "s_branch .Lok%=\n\t" \
  ".Lend%=:\n\t"
#define QK_ASM_5 \
  "s_waitcnt lgkmcnt(0)\n\t" \
  "v_subrev_u32 v244, 0x8000, v250\n\t" \
  "ds_read_b128 v[216:219], v248 offset:53248\n\t" \
  "ds_read_b128 v[220:223], v248 offset:54272\n\t" \
  "ds_read_b128 v[224:227], v248 offset:55296\n\t" \
  "ds_read_b128 v[228:231], v248 offset:56320\n\t" \
  "ds_read_b128 v[208:211], v244 offset:0\n\t" \
  "ds_read_b128 v[212:215], v244 offset:1024\n\t" \
  "ds_read_b128 v[236:239], v244 offset:2048\n\t" \
  "ds_read_b128 v[240:243], v244 offset:3072\n\t" \
  "v_mov_b64 v[192:193], 0\n\t" \
  "v_mov_b64 v[194:195], 0\n\t" \
  "v_mov_b64 v[196:197], 0\n\t" \
  "v_mov_b64 v[198:199], 0\n\t" \
  "s_movk_i32 s92, 0x2000\n\t" \
  ".Lwt%=:\n\t" \
  "ds_read_b64 v[200:201], v248 offset:30720\n\t" \
  "s_memrealtime s[90:91]\n\t" \
  "s_sub_u32 s92, s92, 1\n\t" \
  "s_waitcnt lgkmcnt(0)\n\t" \
  "v_readfirstlane_b32 s93, v200\n\t" \
  "v_readfirstlane_b32 %[tgt], v201\n\t" \
  "s_cbranch_scc1 .Lgo%=\n\t" \
  "s_cmp_eq_u32 s93, %[step]\n\t" \
  "s_cbranch_scc1 .Lwx%=\n\t" \
  "s_sleep 1\n\t" \
  "s_branch .Lwt%=\n\t" \
  ".Lwx%=:\n\t" \
  "s_lshl_b32 s93, s90, 2\n\t" \
  "s_sub_i32 s93, %[tgt], s93\n\t" \
  "s_cmp_le_i32 s93, 0\n\t" \
  "s_cbranch_scc1 .Lgo%=\n\t" \
  "s_min_i32 s93, s93, 8000\n\t" \
  "s_mov_b32 s94, s93\n\t" \
  ".Lsl%=:\n\t" \
  "s_sub_i32 s94, s94, 18\n\t" \
  "s_cmp_lt_i32 s94, 30\n\t" \
  "s_cbranch_scc1 .Lsx%=\n\t" \
  "s_sleep 1\n\t" \
  "s_branch .Lsl%=\n\t" \
  ".Lsx%=:\n\t" \
  "s_sub_i32 s93, s93, 12\n\t" \
  "s_max_i32 s93, s93, 4\n\t" \
  "s_lshr_b32 s93, s93, 2\n\t" \
  "s_add_u32 s93, s93, s90\n\t" \
  "s_memrealtime s[90:91]\n\t" \
  ".Lpace%=:\n\t" \
  "s_memrealtime s[94:95]\n\t" \
  "s_waitcnt lgkmcnt(1)\n\t" \
  "s_sub_i32 s92, s90, s93\n\t" \
  "s_cmp_ge_i32 s92, 0\n\t" \
  "s_cbranch_scc1 .Le%=\n\t" \
  "s_memrealtime s[90:91]\n\t" \
  "s_waitcnt lgkmcnt(1)\n\t" \
  "s_sub_i32 s92, s94, s93\n\t" \
  "s_cmp_lt_i32 s92, 0\n\t" \
  "s_cbranch_scc1 .Lpace%=\n\t" \
  "s_nop 4\n\t" \
  ".Le%=:\n\t" \
  "s_add_u32 s92, s92, s93\n\t" \
  "s_lshl_b32 s92, s92, 2\n\t" \
  "s_sub_i32 s92, %[tgt], s92\n\t" \
  "s_max_i32 s92, s92, 0\n\t" \
  "s_bitcmp0_b32 s92, 3\n\t" \
  "s_cbranch_scc1 .Lf3%=\n\t" \
  "s_nop 15\n\t" \
  ".Lf3%=:\n\t" \
  "s_bitcmp0_b32 s92, 2\n\t" \
  "s_cbranch_scc1 .Lf2%=\n\t" \
  "s_nop 10\n\t" \
  ".Lf2%=:\n\t" \
  ".Lgo%=:\n\t" \
  "s_mov_b32 s92, 0\n\t" \
  ".Lpoll%=:\n\t" \
  "buffer_load_dwordx4 v[200:203], v248, %[rs], %[som] offen sc1\n\t" \
  "buffer_load_dwordx4 v[204:207], v248, %[rs], %[som2] offen sc1\n\t" \
  "s_waitcnt vmcnt(1) lgkmcnt(0)\n\t" \
  "ds_write_b128 v249, v[200:203]\n\t" \
  "v_smfmac_f32_16x16x64_bf16 v[192:195], v[200:203], v[208:215], v251\n\t" \
  "v_max3_u32 v244, v200, v201, v202\n\t" \
  "v_smfmac_f32_16x16x64_bf16 v[196:199], v[200:203], v[236:243], v251\n\t" \
  "s_waitcnt vmcnt(0)\n\t" \
  "ds_write_b128 v249, v[204:207] offset:8192\n\t" \
  "v_smfmac_f32_16x16x64_bf16 v[192:195], v[204:207], v[216:223], v251\n\t" \
  "v_max3_u32 v244, v244, v203, v204\n\t" \
  "v_max3_u32 v244, v244, v205, v206\n\t" \
  "v_smfmac_f32_16x16x64_bf16 v[196:199], v[204:207], v[224:231], v251\n\t" \
  "v_max_u32 v244, v244, v207\n\t" \
  "v_cmp_le_u32 vcc, %[sent], v244\n\t" \
  "s_cbranch_vccnz .Lslow%=\n\t" \
  ".Lok%=:\n\t" \
  "ds_read_b128 v[216:219], v250 offset:32768\n\t" \
  "ds_read_b128 v[220:223], v250 offset:33792\n\t" \
  "ds_read_b128 v[224:227], v250 offset:34816\n\t" \
  "ds_read_b128 v[228:231], v250 offset:35840\n\t" \
  "s_waitcnt lgkmcnt(4)\n\t" \
  "s_barrier\n\t" \
  "ds_read_b128 v[208:211], v248 offset:4096\n\t" \
  "ds_read_b128 v[212:215], v248 offset:7168\n\t" \
  "ds_read_b128 v[236:239], v248 offset:8192\n\t" \
  "ds_read_b128 v[240:243], v248 offset:9216\n\t" \
  "s_waitcnt lgkmcnt(3)\n\t" \
  "ds_read_b128 v[244:247], v248 offset:12288\n\t" \
  "v_smfmac_f32_16x16x64_bf16 v[192:195], v[208:211], v[216:223], v251\n\t" \
  "ds_read_b128 v[216:219], v250 offset:0\n\t" \
  "v_smfmac_f32_16x16x64_bf16 v[196:199], v[208:211], v[224:231], v251\n\t" \
  "s_waitcnt lgkmcnt(4)\n\t" \
  "ds_read_b128 v[220:223], v250 offset:1024\n\t" \
  "v_smfmac_f32_16x16x64_bf16 v[192:195], v[212:215], v[0:7], v251\n\t" \
  "ds_read_b128 v[224:227], v250 offset:2048\n\t" \
  "v_smfmac_f32_16x16x64_bf16 v[196:199], v[212:215], v[8:15], v251\n\t" \
  "s_waitcnt lgkmcnt(5)\n\t" \
  "ds_read_b128 v[228:231], v250 offset:3072\n\t" \
  "v_smfmac_f32_16x16x64_bf16 v[192:195], v[236:239], v[16:23], v251\n\t" \
  "ds_read_b128 v[200:203], v248 offset:10240\n\t" \
  "v_smfmac_f32_16x16x64_bf16 v[196:199], v[236:239], v[24:31], v251\n\t" \
  "s_waitcnt lgkmcnt(6)\n\t" \
  "ds_read_b128 v[204:207], v248 offset:11264\n\t" \
  "v_smfmac_f32_16x16x64_bf16 v[192:195], v[240:243], v[32:39], v251\n\t" \
  "ds_read_b128 v[208:211], v248 offset:14336\n\t" \
  "v_smfmac_f32_16x16x64_bf16 v[196:199], v[240:243], v[40:47], v251\n\t" \
  "s_waitcnt lgkmcnt(5)\n\t" \
  "ds_read_b128 v[212:215], v248 offset:15360\n\t" \
  "v_smfmac_f32_16x16x64_bf16 v[192:195], v[244:247], v[216:223], v251\n\t" \
  "s_waitcnt lgkmcnt(4)\n\t" \
  "v_smfmac_f32_16x16x64_bf16 v[196:199], v[244:247], v[224:231], v251\n\t" \
  "s_waitcnt lgkmcnt(3)\n\t" \
  "ds_read_b128 v[236:239], v248 offset:0\n\t" \
  "v_smfmac_f32_16x16x64_bf16 v[192:195], v[200:203], v[48:55], v251\n\t" \
  "ds_read_b128 v[240:243], v248 offset:1024\n\t" \
  "v_smfmac_f32_16x16x64_bf16 v[196:199], v[200:203], v[56:63], v251\n\t" \
  "s_waitcnt lgkmcnt(4)\n\t" \
  "v_smfmac_f32_16x16x64_bf16 v[192:195], v[204:207], v[64:71], v251\n\t" \
  "ds_read_b128 v[244:247], v248 offset:2048\n\t" \
  "v_smfmac_f32_16x16x64_bf16 v[196:199], v[204:207], v[72:79], v251\n\t" \
  "s_waitcnt lgkmcnt(4)\n\t" \
  "v_smfmac_f32_16x16x64_bf16 v[192:195], v[208:211], v[80:87], v251\n\t" \
  "ds_read_b128 v[200:203], v248 offset:3072\n\t" \
  "v_smfmac_f32_16x16x64_bf16 v[196:199], v[208:211], v[88:95], v251\n\t" \
  "s_waitcnt lgkmcnt(4)\n\t" \
  "v_smfmac_f32_16x16x64_bf16 v[192:195], v[212:215], v[96:103], v251\n\t" \
  "ds_read_b128 v[204:207], v248 offset:6144\n\t" \
  "v_smfmac_f32_16x16x64_bf16 v[196:199], v[212:215], v[104:111], v251\n\t" \
  "s_waitcnt lgkmcnt(3)\n\t" \
  "v_smfmac_f32_16x16x64_bf16 v[192:195], v[236:239], v[112:119], v251\n\t" \
  "v_smfmac_f32_16x16x64_bf16 v[196:199], v[236:239], v[120:127], v251\n\t" \
  "v_smfmac_f32_16x16x64_bf16 v[192:195], v[240:243], v[128:135], v251\n\t" \
  "v_smfmac_f32_16x16x64_bf16 v[196:199], v[240:243], v[136:143], v251\n\t" \
  "s_waitcnt lgkmcnt(1)\n\t" \
  "v_smfmac_f32_16x16x64_bf16 v[192:195], v[244:247], v[144:151], v251\n\t" \
  "v_smfmac_f32_16x16x64_bf16 v[196:199], v[244:247], v[152:159], v251\n\t" \
  "v_smfmac_f32_16x16x64_bf16 v[192:195], v[200:203], v[160:167], v251\n\t" \
  "v_smfmac_f32_16x16x64_bf16 v[196:199], v[200:203], v[168:175], v251\n\t" \
  "s_waitcnt lgkmcnt(0)\n\t" \
  "v_smfmac_f32_16x16x64_bf16 v[192:195], v[204:207], v[176:183], v251\n\t" \
  "v_smfmac_f32_16x16x64_bf16 v[196:199], v[204:207], v[184:191], v251\n\t" \
  "s_nop 6\n\t" \
  "v_add_f32 v192, v192, v194\n\t" \
  "v_add_f32 v193, v193, v195\n\t" \
  "v_add_f32 v196, v196, v198\n\t" \
  "v_add_f32 v197, v197, v199\n\t" \
  "v_cvt_pk_bf16_f32 v192, v192, v193\n\t" \
  "v_cvt_pk_bf16_f32 v196, v196, v197\n\t" \
  "ds_write2_b32 v252, v192, v196 offset1:64\n\t" \
  "s_waitcnt lgkmcnt(0)\n\t" \
  "s_barrier\n\t" \
  "buffer_store_dword v192, v235, %[rs], %[sk] offen sc1\n\t" \
  "buffer_store_dword v196, v235, %[rs], %[sk] offen offset:256 sc1\n\t" \
  "s_mov_b32 %[spins], s92\n\t" \
  "s_branch .Lend%=\n\t" \
  ".Lslow%=:\n\t" \
  "s_mov_b64 s[94:95], 0\n\t" \
  "v_cmp_eq_u32 vcc, %[sent], v200\n\t" \
  "s_or_b64 s[94:95], s[94:95], vcc\n\t" \
  "v_cmp_eq_u32 vcc, %[sent], v201\n\t" \
  "s_or_b64 s[94:95], s[94:95], vcc\n\t" \
  "v_cmp_eq_u32 vcc, %[sent], v202\n\t" \
  "s_or_b64 s[94:95], s[94:95], vcc\n\t" \
  "v_cmp_eq_u32 vcc, %[sent], v203\n\t" \
  "s_or_b64 s[94:95], s[94:95], vcc\n\t" \
  "v_cmp_eq_u32 vcc, %[sent], v204\n\t" \
  "s_or_b64 s[94:95], s[94:95], vcc\n\t" \
  "v_cmp_eq_u32 vcc, %[sent], v205\n\t" \
  "s_or_b64 s[94:95], s[94:95], vcc\n\t" \
  "v_cmp_eq_u32 vcc, %[sent], v206\n\t" \
  "s_or_b64 s[94:95], s[94:95], vcc\n\t" \
  "v_cmp_eq_u32 vcc, %[sent], v207\n\t" \
  "s_or_b64 s[94:95], s[94:95], vcc\n\t" \
  "s_cmp_eq_u64 s[94:95], 0\n\t" \
  "s_cbranch_scc1 .Lok%=\n\t" \
  "v_mov_b64 v[192:193], 0\n\t" \
  "v_mov_b64 v[194:195], 0\n\t" \
  "v_mov_b64 v[196:197], 0\n\t" \
  "v_mov_b64 v[198:199], 0\n\t" \
  "s_add_u32 s92, s92, 1\n\t" \
  "s_sleep 6\n\t" \
  "s_cmp_eq_u32 s92, 1\n\t" \
  "s_cbranch_scc1 .Lpoll%=\n\t" \
  "s_sleep 1\n\t" \
  "s_and_b32 s93, s92, 63\n\t" \
  "s_cmp_lg_u32 s93, 0\n\t" \
  "s_cbranch_scc1 .Lpoll%=\n\t" \
  "v_mov_b32 v245, 0\n\t" \
  "buffer_load_dword v244, v245, %[rs], %[sab] offen sc1\n\t" \
  "s_waitcnt vmcnt(0)\n\t" \
  "v_readfirstlane_b32 s93, v244\n\t" \
  "s_nop 3\n\t" \
  "s_cmp_eq_u32 s93, 1\n\t" \
  "s_cbranch_scc1 .Lok%=\n\t" \
  "s_cmp_lt_u32 s92, 0x80000\n\t" \
  "s_cbranch_scc1 .Lpoll%=\n\t" \
  "v_mov_b32 v244, 1\n\t" \
  "buffer_store_dword v244, v245, %[rs], %[sab] offen sc1\n\t" \
  "s_branch .Lok%=\n\t" \
  ".Lend%=:\n\t"
#define QK_ASM_6 \
  "s_waitcnt lgkmcnt(0)\n\t" \
  "v_subrev_u32 v244, 0x8000, v250\n\t" \
  "ds_read_b128 v[216:219], v248 offset:57344\n\t" \
  "ds_read_b128 v[220:223], v248 offset:58368\n\t" \
  "ds_read_b128 v[224:227], v248 offset:59392\n\t" \
  "ds_read_b128 v[228:231], v248 offset:60416\n\t" \
  "ds_read_b128 v[208:211], v244 offset:0\n\t" \
  "ds_read_b128 v[212:215], v244 offset:1024\n\t" \
  "ds_read_b128 v[236:239], v244 offset:2048\n\t" \
  "ds_read_b128 v[240:243], v244 offset:3072\n\t" \
  "v_mov_b64 v[192:193], 0\n\t" \
  "v_mov_b64 v[194:195], 0\n\t" \
  "v_mov_b64 v[196:197], 0\n\t" \
  "v_mov_b64 v[198:199], 0\n\t" \
  "s_movk_i32 s92, 0x2000\n\t" \
  ".Lwt%=:\n\t" \
  "ds_read_b64 v[200:201], v248 offset:30720\n\t" \
  "s_memrealtime s[90:91]\n\t" \
  "s_sub_u32 s92, s92, 1\n\t" \
  "s_waitcnt lgkmcnt(0)\n\t" \
  "v_readfirstlane_b32 s93, v200\n\t" \
  "v_readfirstlane_b32 %[tgt], v201\n\t" \
  "s_cbranch_scc1 .Lgo%=\n\t" \
  "s_cmp_eq_u32 s93, %[step]\n\t" \
  "s_cbranch_scc1 .Lwx%=\n\t" \
  "s_sleep 1\n\t" \
  "s_branch .Lwt%=\n\t" \
  ".Lwx%=:\n\t" \
  "s_lshl_b32 s93, s90, 2\n\t" \
  "s_sub_i32 s93, %[tgt], s93\n\t" \
  "s_cmp_le_i32 s93, 0\n\t" \
  "s_cbranch_scc1 .Lgo%=\n\t" \
  "s_min_i32 s93, s93, 8000\n\t" \
  "s_mov_b32 s94, s93\n\t" \
  ".Lsl%=:\n\t" \
  "s_sub_i32 s94, s94, 18\n\t" \
  "s_cmp_lt_i32 s94, 30\n\t" \
  "s_cbranch_scc1 .Lsx%=\n\t" \
  "s_sleep 1\n\t" \
  "s_branch .Lsl%=\n\t" \
  ".Lsx%=:\n\t" \
  "s_sub_i32 s93, s93, 12\n\t" \
  "s_max_i32 s93, s93, 4\n\t" \
  "s_lshr_b32 s93, s93, 2\n\t" \
  "s_add_u32 s93, s93, s90\n\t" \
  "s_memrealtime s[90:91]\n\t" \
  ".Lpace%=:\n\t" \
  "s_memrealtime s[94:95]\n\t" \
  "s_waitcnt lgkmcnt(1)\n\t" \
  "s_sub_i32 s92, s90, s93\n\t" \
  "s_cmp_ge_i32 s92, 0\n\t" \
  "s_cbranch_scc1 .Le%=\n\t" \
  "s_memrealtime s[90:91]\n\t" \
  "s_waitcnt lgkmcnt(1)\n\t" \
  "s_sub_i32 s92, s94, s93\n\t" \
  "s_cmp_lt_i32 s92, 0\n\t" \
  "s_cbranch_scc1 .Lpace%=\n\t" \
  "s_nop 4\n\t" \
  ".Le%=:\n\t" \
  "s_add_u32 s92, s92, s93\n\t" \
  "s_lshl_b32 s92, s92, 2\n\t" \
  "s_sub_i32 s92, %[tgt], s92\n\t" \
  "s_max_i32 s92, s92, 0\n\t" \
  "s_bitcmp0_b32 s92, 3\n\t" \
  "s_cbranch_scc1 .Lf3%=\n\t" \
  "s_nop 15\n\t" \
  ".Lf3%=:\n\t" \
  "s_bitcmp0_b32 s92, 2\n\t" \
  "s_cbranch_scc1 .Lf2%=\n\t" \
  "s_nop 10\n\t" \
  ".Lf2%=:\n\t" \
  ".Lgo%=:\n\t" \
  "s_mov_b32 s92, 0\n\t" \
  ".Lpoll%=:\n\t" \
  "buffer_load_dwordx4 v[200:203], v248, %[rs], %[som] offen sc1\n\t" \
  "buffer_load_dwordx4 v[204:207], v248, %[rs], %[som2] offen sc1\n\t" \
  "s_waitcnt vmcnt(1) lgkmcnt(0)\n\t" \
  "ds_write_b128 v249, v[200:203]\n\t" \
  "v_smfmac_f32_16x16x64_bf16 v[192:195], v[200:203], v[208:215], v251\n\t" \
  "v_max3_u32 v244, v200, v201, v202\n\t" \
  "v_smfmac_f32_16x16x64_bf16 v[196:199], v[200:203], v[236:243], v251\n\t" \
  "s_waitcnt vmcnt(0)\n\t" \
  "ds_write_b128 v249, v[204:207] offset:8192\n\t" \
  "v_smfmac_f32_16x16x64_bf16 v[192:195], v[204:207], v[216:223], v251\n\t" \
  "v_max3_u32 v244, v244, v203, v204\n\t" \
  "v_max3_u32 v244, v244, v205, v206\n\t" \
  "v_smfmac_f32_16x16x64_bf16 v[196:199], v[204:207], v[224:231], v251\n\t" \
  "v_max_u32 v244, v244, v207\n\t" \
  "v_cmp_le_u32 vcc, %[sent], v244\n\t" \
  "s_cbranch_vccnz .Lslow%=\n\t" \
  ".Lok%=:\n\t" \
  "ds_read_b128 v[216:219], v250 offset:32768\n\t" \
  "ds_read_b128 v[220:223], v250 offset:33792\n\t" \
  "ds_read_b128 v[224:227], v250 offset:34816\n\t" \
  "ds_read_b128 v[228:231], v250 offset:35840\n\t" \
  "s_waitcnt lgkmcnt(4)\n\t" \
  "s_barrier\n\t" \
  "ds_read_b128 v[208:211], v248 offset:7168\n\t" \
  "ds_read_b128 v[212:215], v248 offset:8192\n\t" \
  "ds_read_b128 v[236:239], v248 offset:9216\n\t" \
  "ds_read_b128 v[240:243], v248 offset:10240\n\t" \
  "s_waitcnt lgkmcnt(3)\n\t" \
  "ds_read_b128 v[244:247], v248 offset:13312\n\t" \
  "v_smfmac_f32_16x16x64_bf16 v[192:195], v[208:211], v[216:223], v251\n\t" \
  "ds_read_b128 v[216:219], v250 offset:0\n\t" \
  "v_smfmac_f32_16x16x64_bf16 v[196:199], v[208:211], v[224:231], v251\n\t" \
  "s_waitcnt lgkmcnt(4)\n\t" \
  "ds_read_b128 v[220:223], v250 offset:1024\n\t" \
  "v_smfmac_f32_16x16x64_bf16 v[192:195], v[212:215], v[0:7], v251\n\t" \
  "ds_read_b128 v[224:227], v250 offset:2048\n\t" \
  "v_smfmac_f32_16x16x64_bf16 v[196:199], v[212:215], v[8:15], v251\n\t" \
  "s_waitcnt lgkmcnt(5)\n\t" \
  "ds_read_b128 v[228:231], v250 offset:3072\n\t" \
  "v_smfmac_f32_16x16x64_bf16 v[192:195], v[236:239], v[16:23], v251\n\t" \
  "ds_read_b128 v[200:203], v248 offset:11264\n\t" \
  "v_smfmac_f32_16x16x64_bf16 v[196:199], v[236:239], v[24:31], v251\n\t" \
  "s_waitcnt lgkmcnt(6)\n\t" \
  "ds_read_b128 v[204:207], v248 offset:12288\n\t" \
  "v_smfmac_f32_16x16x64_bf16 v[192:195], v[240:243], v[32:39], v251\n\t" \
  "ds_read_b128 v[208:211], v248 offset:15360\n\t" \
  "v_smfmac_f32_16x16x64_bf16 v[196:199], v[240:243], v[40:47], v251\n\t" \
  "s_waitcnt lgkmcnt(5)\n\t" \
  "ds_read_b128 v[212:215], v248 offset:0\n\t" \
  "v_smfmac_f32_16x16x64_bf16 v[192:195], v[244:247], v[216:223], v251\n\t" \
  "s_waitcnt lgkmcnt(4)\n\t" \
  "v_smfmac_f32_16x16x64_bf16 v[196:199], v[244:247], v[224:231], v251\n\t" \
  "s_waitcnt lgkmcnt(3)\n\t" \
  "ds_read_b128 v[236:239], v248 offset:1024\n\t" \
  "v_smfmac_f32_16x16x64_bf16 v[192:195], v[200:203], v[48:55], v251\n\t" \
  "ds_read_b128 v[240:243], v248 offset:2048\n\t" \
  "v_smfmac_f32_16x16x64_bf16 v[196:199], v[200:203], v[56:63], v251\n\t" \
  "s_waitcnt lgkmcnt(4)\n\t" \
  "v_smfmac_f32_16x16x64_bf16 v[192:195], v[204:207], v[64:71], v251\n\t" \
  "ds_read_b128 v[244:247], v248 offset:3072\n\t" \
  "v_smfmac_f32_16x16x64_bf16 v[196:199], v[204:207], v[72:79], v251\n\t" \
  "s_waitcnt lgkmcnt(4)\n\t" \
  "v_smfmac_f32_16x16x64_bf16 v[192:195], v[208:211], v[80:87], v251\n\t" \
  "ds_read_b128 v[200:203], v248 offset:4096\n\t" \
  "v_smfmac_f32_16x16x64_bf16 v[196:199], v[208:211], v[88:95], v251\n\t" \
  "s_waitcnt lgkmcnt(4)\n\t" \
  "v_smfmac_f32_16x16x64_bf16 v[192:195], v[212:215], v[96:103], v251\n\t" \
  "ds_read_b128 v[204:207], v248 offset:5120\n\t" \
  "v_smfmac_f32_16x16x64_bf16 v[196:199], v[212:215], v[104:111], v251\n\t" \
  "s_waitcnt lgkmcnt(3)\n\t" \
  "v_smfmac_f32_16x16x64_bf16 v[192:195], v[236:239], v[112:119], v251\n\t" \
  "v_smfmac_f32_16x16x64_bf16 v[196:199], v[236:239], v[120:127], v251\n\t" \
  "v_smfmac_f32_16x16x64_bf16 v[192:195], v[240:243], v[128:135], v251\n\t" \
  "v_smfmac_f32_16x16x64_bf16 v[196:199], v[240:243], v[136:143], v251\n\t" \
  "s_waitcnt lgkmcnt(1)\n\t" \
  "v_smfmac_f32_16x16x64_bf16 v[192:195], v[244:247], v[144:151], v251\n\t" \
  "v_smfmac_f32_16x16x64_bf16 v[196:199], v[244:247], v[152:159], v251\n\t" \
  "v_smfmac_f32_16x16x64_bf16 v[192:195], v[200:203], v[160:167], v251\n\t" \
  "v_smfmac_f32_16x16x64_bf16 v[196:199], v[200:203], v[168:175], v251\n\t" \
  "s_waitcnt lgkmcnt(0)\n\t" \
  "v_smfmac_f32_16x16x64_bf16 v[192:195], v[204:207], v[176:183], v251\n\t" \
  "v_smfmac_f32_16x16x64_bf16 v[196:199], v[204:207], v[184:191], v251\n\t" \
  "s_nop 6\n\t" \
  "v_add_f32 v192, v192, v194\n\t" \
  "v_add_f32 v193, v193, v195\n\t" \
  "v_add_f32 v196, v196, v198\n\t" \
  "v_add_f32 v197, v197, v199\n\t" \
  "v_cvt_pk_bf16_f32 v192, v192, v193\n\t" \
  "v_cvt_pk_bf16_f32 v196, v196, v197\n\t" \
  "ds_write2_b32 v252, v192, v196 offset1:64\n\t" \
  "s_waitcnt lgkmcnt(0)\n\t" \
  "s_barrier\n\t" \
  "buffer_store_dword v192, v235, %[rs], %[sk] offen sc1\n\t" \
  "buffer_store_dword v196, v235, %[rs], %[sk] offen offset:256 sc1\n\t" \
  "s_mov_b32 %[spins], s92\n\t" \
  "s_branch .Lend%=\n\t" \
  ".Lslow%=:\n\t" \
  "s_mov_b64 s[94:95], 0\n\t" \
  "v_cmp_eq_u32 vcc, %[sent], v200\n\t" \
  "s_or_b64 s[94:95], s[94:95], vcc\n\t" \
  "v_cmp_eq_u32 vcc, %[sent], v201\n\t" \
  "s_or_b64 s[94:95], s[94:95], vcc\n\t" \
  "v_cmp_eq_u32 vcc, %[sent], v202\n\t" \
  "s_or_b64 s[94:95], s[94:95], vcc\n\t" \
  "v_cmp_eq_u32 vcc, %[sent], v203\n\t" \
  "s_or_b64 s[94:95], s[94:95], vcc\n\t" \
  "v_cmp_eq_u32 vcc, %[sent], v204\n\t" \
  "s_or_b64 s[94:95], s[94:95], vcc\n\t" \
  "v_cmp_eq_u32 vcc, %[sent], v205\n\t" \
  "s_or_b64 s[94:95], s[94:95], vcc\n\t" \
  "v_cmp_eq_u32 vcc, %[sent], v206\n\t" \
  "s_or_b64 s[94:95], s[94:95], vcc\n\t" \
  "v_cmp_eq_u32 vcc, %[sent], v207\n\t" \
  "s_or_b64 s[94:95], s[94:95], vcc\n\t" \
  "s_cmp_eq_u64 s[94:95], 0\n\t" \
  "s_cbranch_scc1 .Lok%=\n\t" \
  "v_mov_b64 v[192:193], 0\n\t" \
  "v_mov_b64 v[194:195], 0\n\t" \
  "v_mov_b64 v[196:197], 0\n\t" \
  "v_mov_b64 v[198:199], 0\n\t" \
  "s_add_u32 s92, s92, 1\n\t" \
  "s_sleep 6\n\t" \
  "s_cmp_eq_u32 s92, 1\n\t" \
  "s_cbranch_scc1 .Lpoll%=\n\t" \
  "s_sleep 1\n\t" \
  "s_and_b32 s93, s92, 63\n\t" \
  "s_cmp_lg_u32 s93, 0\n\t" \
  "s_cbranch_scc1 .Lpoll%=\n\t" \
  "v_mov_b32 v245, 0\n\t" \
  "buffer_load_dword v244, v245, %[rs], %[sab] offen sc1\n\t" \
  "s_waitcnt vmcnt(0)\n\t" \
  "v_readfirstlane_b32 s93, v244\n\t" \
  "s_nop 3\n\t" \
  "s_cmp_eq_u32 s93, 1\n\t" \
  "s_cbranch_scc1 .Lok%=\n\t" \
  "s_cmp_lt_u32 s92, 0x80000\n\t" \
  "s_cbranch_scc1 .Lpoll%=\n\t" \
  "v_mov_b32 v244, 1\n\t" \
  "buffer_store_dword v244, v245, %[rs], %[sab] offen sc1\n\t" \
  "s_branch .Lok%=\n\t" \
  ".Lend%=:\n\t"
#define QK_ASM_7 \
  "s_waitcnt lgkmcnt(0)\n\t" \
  "v_subrev_u32 v244, 0x8000, v250\n\t" \
  "ds_read_b128 v[216:219], v248 offset:61440\n\t" \
  "ds_read_b128 v[220:223], v248 offset:62464\n\t" \
  "ds_read_b128 v[224:227], v248 offset:63488\n\t" \
  "ds_read_b128 v[228:231], v248 offset:64512\n\t" \
  "ds_read_b128 v[208:211], v244 offset:0\n\t" \
  "ds_read_b128 v[212:215], v244 offset:1024\n\t" \
  "ds_read_b128 v[236:239], v244 offset:2048\n\t" \
  "ds_read_b128 v[240:243], v244 offset:3072\n\t" \
  "v_mov_b64 v[192:193], 0\n\t" \
  "v_mov_b64 v[194:195], 0\n\t" \
  "v_mov_b64 v[196:197], 0\n\t" \
  "v_mov_b64 v[198:199], 0\n\t" \
  "s_movk_i32 s92, 0x2000\n\t" \
  ".Lwt%=:\n\t" \
  "ds_read_b64 v[200:201], v248 offset:30720\n\t" \
  "s_memrealtime s[90:91]\n\t" \
  "s_sub_u32 s92, s92, 1\n\t" \
  "s_waitcnt lgkmcnt(0)\n\t" \
  "v_readfirstlane_b32 s93, v200\n\t" \
  "v_readfirstlane_b32 %[tgt], v201\n\t" \
  "s_cbranch_scc1 .Lgo%=\n\t" \
  "s_cmp_eq_u32 s93, %[step]\n\t" \
  "s_cbranch_scc1 .Lwx%=\n\t" \
  "s_sleep 1\n\t" \
  "s_branch .Lwt%=\n\t" \
  ".Lwx%=:\n\t" \
  "s_lshl_b32 s93, s90, 2\n\t" \
  "s_sub_i32 s93, %[tgt], s93\n\t" \
  "s_cmp_le_i32 s93, 0\n\t" \
  "s_cbranch_scc1 .Lgo%=\n\t" \
  "s_min_i32 s93, s93, 8000\n\t" \
  "s_mov_b32 s94, s93\n\t" \
  ".Lsl%=:\n\t" \
  "s_sub_i32 s94, s94, 18\n\t" \
  "s_cmp_lt_i32 s94, 30\n\t" \
  "s_cbranch_scc1 .Lsx%=\n\t" \
  "s_sleep 1\n\t" \
  "s_branch .Lsl%=\n\t" \
  ".Lsx%=:\n\t" \
  "s_sub_i32 s93, s93, 12\n\t" \
  "s_max_i32 s93, s93, 4\n\t" \
  "s_lshr_b32 s93, s93, 2\n\t" \
  "s_add_u32 s93, s93, s90\n\t" \
  "s_memrealtime s[90:91]\n\t" \
  ".Lpace%=:\n\t" \
  "s_memrealtime s[94:95]\n\t" \
  "s_waitcnt lgkmcnt(1)\n\t" \
  "s_sub_i32 s92, s90, s93\n\t" \
  "s_cmp_ge_i32 s92, 0\n\t" \
  "s_cbranch_scc1 .Le%=\n\t" \
  "s_memrealtime s[90:91]\n\t" \
  "s_waitcnt lgkmcnt(1)\n\t" \
  "s_sub_i32 s92, s94, s93\n\t" \
  "s_cmp_lt_i32 s92, 0\n\t" \
  "s_cbranch_scc1 .Lpace%=\n\t" \
  "s_nop 4\n\t" \
  ".Le%=:\n\t" \
  "s_add_u32 s92, s92, s93\n\t" \
  "s_lshl_b32 s92, s92, 2\n\t" \
  "s_sub_i32 s92, %[tgt], s92\n\t" \
  "s_max_i32 s92, s92, 0\n\t" \
  "s_bitcmp0_b32 s92, 3\n\t" \
  "s_cbranch_scc1 .Lf3%=\n\t" \
  "s_nop 15\n\t" \
  ".Lf3%=:\n\t" \
  "s_bitcmp0_b32 s92, 2\n\t" \
  "s_cbranch_scc1 .Lf2%=\n\t" \
  "s_nop 10\n\t" \
  ".Lf2%=:\n\t" \
  ".Lgo%=:\n\t" \
  "s_mov_b32 s92, 0\n\t" \
  "s_memtime s[88:89]\n\t" \
  ".Lpoll%=:\n\t" \
  "buffer_load_dwordx4 v[200:203], v248, %[rs], %[som] offen sc1\n\t" \
  "buffer_load_dwordx4 v[204:207], v248, %[rs], %[som2] offen sc1\n\t" \
  "s_waitcnt vmcnt(1) lgkmcnt(0)\n\t" \
  "ds_write_b128 v249, v[200:203]\n\t" \
  "v_smfmac_f32_16x16x64_bf16 v[192:195], v[200:203], v[208:215], v251\n\t" \
  "v_max3_u32 v244, v200, v201, v202\n\t" \
  "v_smfmac_f32_16x16x64_bf16 v[196:199], v[200:203], v[236:243], v251\n\t" \
  "s_waitcnt vmcnt(0)\n\t" \
  "ds_write_b128 v249, v[204:207] offset:8192\n\t" \
  "v_smfmac_f32_16x16x64_bf16 v[192:195], v[204:207], v[216:223], v251\n\t" \
  "v_max3_u32 v244, v244, v203, v204\n\t" \
  "v_max3_u32 v244, v244, v205, v206\n\t" \
  "v_smfmac_f32_16x16x64_bf16 v[196:199], v[204:207], v[224:231], v251\n\t" \
  "v_max_u32 v244, v244, v207\n\t" \
  "v_cmp_le_u32 vcc, %[sent], v244\n\t" \
  "s_cbranch_vccnz .Lslow%=\n\t" \
  ".Lok%=:\n\t" \
  "ds_read_b128 v[216:219], v250 offset:32768\n\t" \
  "ds_read_b128 v[220:223], v250 offset:33792\n\t" \
  "ds_read_b128 v[224:227], v250 offset:34816\n\t" \
  "ds_read_b128 v[228:231], v250 offset:35840\n\t" \
  "s_waitcnt lgkmcnt(4)\n\t" \
  "s_setprio 3\n\t" \
  "s_barrier\n\t" \
  "s_memrealtime s[90:91]\n\t" \
  "s_memtime s[94:95]\n\t" \
  "v_mov_b32 v232, %[step]\n\t" \
  "v_lshrrev_b32 v234, 1, v248\n\t" \
  "s_mov_b64 exec, 0xffff\n\t" \
  "s_waitcnt lgkmcnt(0)\n\t" \
  "s_lshl_b32 s90, s90, 2\n\t" \
  "s_sub_u32 s94, s94, s88\n\t" \
  "s_mul_i32 s94, s94, 12200\n\t" \
  "s_lshr_b32 s94, s94, 16\n\t" \
  "s_add_u32 s94, s94, %[tgt]\n\t" \
  "s_sub_i32 s93, s90, s94\n\t" \
  "s_sub_i32 s93, s93, 30\n\t" \
  "s_max_i32 s93, s93, 0\n\t" \
  "s_add_u32 s90, s94, s93\n\t" \
  "s_sub_i32 s93, s90, %[tgt]\n\t" \
  "s_cmp_gt_i32 s93, 270\n\t" \
  "s_cselect_b32 s93, 90, 0\n\t" \
  "s_add_u32 s93, s93, s90\n\t" \
  "s_add_u32 s93, s93, %[lead]\n\t" \
  "v_mov_b32 v233, s93\n\t" \
  "buffer_store_dwordx2 v[232:233], v234, %[rs], %[sbc] offen sc1\n\t" \
  "s_mov_b64 exec, -1\n\t" \
  "s_mov_b32 %[tb1], s90\n\t" \
  "s_setprio 0\n\t" \
  "ds_read_b128 v[208:211], v248 offset:6144\n\t" \
  "ds_read_b128 v[212:215], v248 offset:9216\n\t" \
  "ds_read_b128 v[236:239], v248 offset:10240\n\t" \
  "ds_read_b128 v[240:243], v248 offset:11264\n\t" \
  "s_waitcnt lgkmcnt(3)\n\t" \
  "ds_read_b128 v[244:247], v248 offset:12288\n\t" \
  "v_smfmac_f32_16x16x64_bf16 v[192:195], v[208:211], v[216:223], v251\n\t" \
  "ds_read_b128 v[216:219], v250 offset:0\n\t" \
  "v_smfmac_f32_16x16x64_bf16 v[196:199], v[208:211], v[224:231], v251\n\t" \
  "s_waitcnt lgkmcnt(4)\n\t" \
  "ds_read_b128 v[220:223], v250 offset:1024\n\t" \
  "v_smfmac_f32_16x16x64_bf16 v[192:195], v[212:215], v[0:7], v251\n\t" \
  "ds_read_b128 v[224:227], v250 offset:2048\n\t" \
  "v_smfmac_f32_16x16x64_bf16 v[196:199], v[212:215], v[8:15], v251\n\t" \
  "s_waitcnt lgkmcnt(5)\n\t" \
  "ds_read_b128 v[228:231], v250 offset:3072\n\t" \
  "v_smfmac_f32_16x16x64_bf16 v[192:195], v[236:239], v[16:23], v251\n\t" \
  "ds_read_b128 v[200:203], v248 offset:13312\n\t" \
  "v_smfmac_f32_16x16x64_bf16 v[196:199], v[236:239], v[24:31], v251\n\t" \
  "s_waitcnt lgkmcnt(6)\n\t" \
  "ds_read_b128 v[204:207], v248 offset:14336\n\t" \
  "v_smfmac_f32_16x16x64_bf16 v[192:195], v[240:243], v[32:39], v251\n\t" \
  "ds_read_b128 v[208:211], v248 offset:0\n\t" \
  "v_smfmac_f32_16x16x64_bf16 v[196:199], v[240:243], v[40:47], v251\n\t" \
  "s_waitcnt lgkmcnt(5)\n\t" \
  "ds_read_b128 v[212:215], v248 offset:1024\n\t" \
  "v_smfmac_f32_16x16x64_bf16 v[192:195], v[244:247], v[216:223], v251\n\t" \
  "s_waitcnt lgkmcnt(4)\n\t" \
  "v_smfmac_f32_16x16x64_bf16 v[196:199], v[244:247], v[224:231], v251\n\t" \
  "s_waitcnt lgkmcnt(3)\n\t" \
  "ds_read_b128 v[236:239], v248 offset:2048\n\t" \
  "v_smfmac_f32_16x16x64_bf16 v[192:195], v[200:203], v[48:55], v251\n\t" \
  "ds_read_b128 v[240:243], v248 offset:3072\n\t" \
  "v_smfmac_f32_16x16x64_bf16 v[196:199], v[200:203], v[56:63], v251\n\t" \
  "s_waitcnt lgkmcnt(4)\n\t" \
  "v_smfmac_f32_16x16x64_bf16 v[192:195], v[204:207], v[64:71], v251\n\t" \
  "ds_read_b128 v[244:247], v248 offset:4096\n\t" \
  "v_smfmac_f32_16x16x64_bf16 v[196:199], v[204:207], v[72:79], v251\n\t" \
  "s_waitcnt lgkmcnt(4)\n\t" \
  "v_smfmac_f32_16x16x64_bf16 v[192:195], v[208:211], v[80:87], v251\n\t" \
  "ds_read_b128 v[200:203], v248 offset:5120\n\t" \
  "v_smfmac_f32_16x16x64_bf16 v[196:199], v[208:211], v[88:95], v251\n\t" \
  "s_waitcnt lgkmcnt(4)\n\t" \
  "v_smfmac_f32_16x16x64_bf16 v[192:195], v[212:215], v[96:103], v251\n\t" \
  "ds_read_b128 v[204:207], v248 offset:8192\n\t" \
  "v_smfmac_f32_16x16x64_bf16 v[196:199], v[212:215], v[104:111], v251\n\t" \
  "s_waitcnt lgkmcnt(3)\n\t" \
  "v_smfmac_f32_16x16x64_bf16 v[192:195], v[236:239], v[112:119], v251\n\t" \
  "v_smfmac_f32_16x16x64_bf16 v[196:199], v[236:239], v[120:127], v251\n\t" \
  "v_smfmac_f32_16x16x64_bf16 v[192:195], v[240:243], v[128:135], v251\n\t" \
  "v_smfmac_f32_16x16x64_bf16 v[196:199], v[240:243], v[136:143], v251\n\t" \
  "s_waitcnt lgkmcnt(1)\n\t" \
  "v_smfmac_f32_16x16x64_bf16 v[192:195], v[244:247], v[144:151], v251\n\t" \
  "v_smfmac_f32_16x16x64_bf16 v[196:199], v[244:247], v[152:159], v251\n\t" \
  "v_smfmac_f32_16x16x64_bf16 v[192:195], v[200:203], v[160:167], v251\n\t" \
  "v_smfmac_f32_16x16x64_bf16 v[196:199], v[200:203], v[168:175], v251\n\t" \
  "s_waitcnt lgkmcnt(0)\n\t" \
  "v_smfmac_f32_16x16x64_bf16 v[192:195], v[204:207], v[176:183], v251\n\t" \
  "v_smfmac_f32_16x16x64_bf16 v[196:199], v[204:207], v[184:191], v251\n\t" \
  "s_nop 6\n\t" \
  "v_add_f32 v192, v192, v194\n\t" \
  "v_add_f32 v193, v193, v195\n\t" \
  "v_add_f32 v196, v196, v198\n\t" \
  "v_add_f32 v197, v197, v199\n\t" \
  "v_cvt_pk_bf16_f32 v192, v192, v193\n\t" \
  "v_cvt_pk_bf16_f32 v196, v196, v197\n\t" \
  "ds_write2_b32 v252, v192, v196 offset1:64\n\t" \
  "s_waitcnt lgkmcnt(0)\n\t" \
  "s_barrier\n\t" \
  "buffer_store_dword v192, v235, %[rs], %[sk] offen sc1\n\t" \
  "buffer_store_dword v196, v235, %[rs], %[sk] offen offset:256 sc1\n\t" \
  "s_mov_b32 %[spins], s92\n\t" \
  "s_branch .Lend%=\n\t" \
  ".Lslow%=:\n\t" \
  "s_mov_b64 s[94:95], 0\n\t" \
  "v_cmp_eq_u32 vcc, %[sent], v200\n\t" \
  "s_or_b64 s[94:95], s[94:95], vcc\n\t" \
  "v_cmp_eq_u32 vcc, %[sent], v201\n\t" \
  "s_or_b64 s[94:95], s[94:95], vcc\n\t" \
  "v_cmp_eq_u32 vcc, %[sent], v202\n\t" \
  "s_or_b64 s[94:95], s[94:95], vcc\n\t" \
  "v_cmp_eq_u32 vcc, %[sent], v203\n\t" \
  "s_or_b64 s[94:95], s[94:95], vcc\n\t" \
  "v_cmp_eq_u32 vcc, %[sent], v204\n\t" \
  "s_or_b64 s[94:95], s[94:95], vcc\n\t" \
  "v_cmp_eq_u32 vcc, %[sent], v205\n\t" \
  "s_or_b64 s[94:95], s[94:95], vcc\n\t" \
  "v_cmp_eq_u32 vcc, %[sent], v206\n\t" \
  "s_or_b64 s[94:95], s[94:95], vcc\n\t" \
  "v_cmp_eq_u32 vcc, %[sent], v207\n\t" \
  "s_or_b64 s[94:95], s[94:95], vcc\n\t" \
  "s_cmp_eq_u64 s[94:95], 0\n\t" \
  "s_cbranch_scc1 .Lok%=\n\t" \
  "v_mov_b64 v[192:193], 0\n\t" \
  "v_mov_b64 v[194:195], 0\n\t" \
  "v_mov_b64 v[196:197], 0\n\t" \
  "v_mov_b64 v[198:199], 0\n\t" \
  "s_add_u32 s92, s92, 1\n\t" \
  "s_sleep 6\n\t" \
  "s_cmp_eq_u32 s92, 1\n\t" \
  "s_cbranch_scc1 .Lpoll%=\n\t" \
  "s_sleep 1\n\t" \
  "s_and_b32 s93, s92, 63\n\t" \
  "s_cmp_lg_u32 s93, 0\n\t" \
  "s_cbranch_scc1 .Lpoll%=\n\t" \
  "v_mov_b32 v245, 0\n\t" \
  "buffer_load_dword v244, v245, %[rs], %[sab] offen sc1\n\t" \
  "s_waitcnt vmcnt(0)\n\t" \
  "v_readfirstlane_b32 s93, v244\n\t" \
  "s_nop 3\n\t" \
  "s_cmp_eq_u32 s93, 1\n\t" \
  "s_cbranch_scc1 .Lok%=\n\t" \
  "s_cmp_lt_u32 s92, 0x80000\n\t" \
  "s_cbranch_scc1 .Lpoll%=\n\t" \
  "v_mov_b32 v244, 1\n\t" \
  "buffer_store_dword v244, v245, %[rs], %[sab] offen sc1\n\t" \
  "s_branch .Lok%=\n\t" \
  ".Lend%=:\n\t"
#define VV_ASM \
  "s_waitcnt lgkmcnt(0)\n\t" \
  "v_mov_b64 v[128:129], 0\n\t" \
  "v_mov_b64 v[130:131], 0\n\t" \
  "s_movk_i32 s92, 0x2000\n\t" \
  ".Lwt%=:\n\t" \
  "ds_read_b64 v[132:133], v200 offset:30720\n\t" \
  "s_memrealtime s[90:91]\n\t" \
  "s_sub_u32 s92, s92, 1\n\t" \
  "s_waitcnt lgkmcnt(0)\n\t" \
  "v_readfirstlane_b32 s93, v132\n\t" \
  "v_readfirstlane_b32 %[tgt], v133\n\t" \
  "s_cbranch_scc1 .Lgo%=\n\t" \
  "s_cmp_eq_u32 s93, %[step]\n\t" \
  "s_cbranch_scc1 .Lwx%=\n\t" \
  "s_sleep 1\n\t" \
  "s_branch .Lwt%=\n\t" \
  ".Lwx%=:\n\t" \
  "s_lshl_b32 s93, s90, 2\n\t" \
  "s_sub_i32 s93, %[tgt], s93\n\t" \
  "s_cmp_le_i32 s93, 0\n\t" \
  "s_cbranch_scc1 .Lgo%=\n\t" \
  "s_min_i32 s93, s93, 8000\n\t" \
  "s_mov_b32 s94, s93\n\t" \
  ".Lsl%=:\n\t" \
  "s_sub_i32 s94, s94, 18\n\t" \
  "s_cmp_lt_i32 s94, 30\n\t" \
  "s_cbranch_scc1 .Lsx%=\n\t" \
  "s_sleep 1\n\t" \
  "s_branch .Lsl%=\n\t" \
  ".Lsx%=:\n\t" \
  "s_sub_i32 s93, s93, 12\n\t" \
  "s_max_i32 s93, s93, 4\n\t" \
  "s_lshr_b32 s93, s93, 2\n\t" \
  "s_add_u32 s93, s93, s90\n\t" \
  "s_memrealtime s[90:91]\n\t" \
  ".Lpace%=:\n\t" \
  "s_memrealtime s[94:95]\n\t" \
  "s_waitcnt lgkmcnt(1)\n\t" \
  "s_sub_i32 s92, s90, s93\n\t" \
  "s_cmp_ge_i32 s92, 0\n\t" \
  "s_cbranch_scc1 .Le%=\n\t" \
  "s_memrealtime s[90:91]\n\t" \
  "s_waitcnt lgkmcnt(1)\n\t" \
  "s_sub_i32 s92, s94, s93\n\t" \
  "s_cmp_lt_i32 s92, 0\n\t" \
  "s_cbranch_scc1 .Lpace%=\n\t" \
  "s_nop 4\n\t" \
  ".Le%=:\n\t" \
  "s_add_u32 s92, s92, s93\n\t" \
  "s_lshl_b32 s92, s92, 2\n\t" \
  "s_sub_i32 s92, %[tgt], s92\n\t" \
  "s_max_i32 s92, s92, 0\n\t" \
  "s_bitcmp0_b32 s92, 3\n\t" \
  "s_cbranch_scc1 .Lf3%=\n\t" \
  "s_nop 15\n\t" \
  ".Lf3%=:\n\t" \
  "s_bitcmp0_b32 s92, 2\n\t" \
  "s_cbranch_scc1 .Lf2%=\n\t" \
  "s_nop 10\n\t" \
  ".Lf2%=:\n\t" \
  ".Lgo%=:\n\t" \
  "s_mov_b32 s92, 0\n\t" \
  ".Lpoll%=:\n\t" \
  "buffer_load_dwordx4 v[132:135], v200, %[rs], %[som] offen sc1\n\t" \
  "buffer_load_dwordx4 v[136:139], v200, %[rs], %[som2] offen sc1\n\t" \
  "s_waitcnt vmcnt(1) lgkmcnt(0)\n\t" \
  "ds_write_b128 v232, v[132:135]\n\t" \
  "v_smfmac_f32_16x16x64_bf16 v[128:131], v[132:135], v[0:7], v202\n\t" \
  "v_max3_u32 v196, v132, v133, v134\n\t" \
  "s_waitcnt vmcnt(0)\n\t" \
  "ds_write_b128 v232, v[136:139] offset:8192\n\t" \
  "v_smfmac_f32_16x16x64_bf16 v[128:131], v[136:139], v[64:71], v202\n\t" \
  "v_max3_u32 v196, v196, v135, v136\n\t" \
  "v_max3_u32 v196, v196, v137, v138\n\t" \
  "v_max_u32 v196, v196, v139\n\t" \
  "v_cmp_le_u32 vcc, %[sent], v196\n\t" \
  "s_cbranch_vccnz .Lslow%=\n\t" \
  ".Lok%=:\n\t" \
  "s_waitcnt lgkmcnt(0)\n\t" \
  "s_barrier\n\t" \
  "ds_read_b128 v[164:167], v233 offset:0\n\t" \
  "ds_read_b128 v[140:143], v234 offset:0\n\t" \
  "ds_read_b128 v[144:147], v235 offset:0\n\t" \
  "ds_read_b128 v[148:151], v236 offset:0\n\t" \
  "ds_read_b128 v[152:155], v237 offset:0\n\t" \
  "s_waitcnt lgkmcnt(4)\n\t" \
  "ds_read_b128 v[156:159], v238 offset:0\n\t" \
  "v_smfmac_f32_16x16x64_bf16 v[128:131], v[164:167], v[8:15], v202\n\t" \
  "s_waitcnt lgkmcnt(4)\n\t" \
  "ds_read_b128 v[160:163], v239 offset:0\n\t" \
  "v_smfmac_f32_16x16x64_bf16 v[128:131], v[140:143], v[16:23], v202\n\t" \
  "s_waitcnt lgkmcnt(4)\n\t" \
  "ds_read_b128 v[168:171], v233 offset:8192\n\t" \
  "v_smfmac_f32_16x16x64_bf16 v[128:131], v[144:147], v[24:31], v202\n\t" \
  "s_waitcnt lgkmcnt(4)\n\t" \
  "ds_read_b128 v[172:175], v234 offset:8192\n\t" \
  "v_smfmac_f32_16x16x64_bf16 v[128:131], v[148:151], v[32:39], v202\n\t" \
  "s_waitcnt lgkmcnt(4)\n\t" \
  "ds_read_b128 v[176:179], v235 offset:8192\n\t" \
  "v_smfmac_f32_16x16x64_bf16 v[128:131], v[152:155], v[40:47], v202\n\t" \
  "s_waitcnt lgkmcnt(4)\n\t" \
  "ds_read_b128 v[180:183], v236 offset:8192\n\t" \
  "v_smfmac_f32_16x16x64_bf16 v[128:131], v[156:159], v[48:55], v202\n\t" \
  "s_waitcnt lgkmcnt(4)\n\t" \
  "ds_read_b128 v[184:187], v237 offset:8192\n\t" \
  "v_smfmac_f32_16x16x64_bf16 v[128:131], v[160:163], v[56:63], v202\n\t" \
  "s_waitcnt lgkmcnt(4)\n\t" \
  "ds_read_b128 v[188:191], v238 offset:8192\n\t" \
  "v_smfmac_f32_16x16x64_bf16 v[128:131], v[168:171], v[72:79], v202\n\t" \
  "s_waitcnt lgkmcnt(4)\n\t" \
  "ds_read_b128 v[192:195], v239 offset:8192\n\t" \
  "v_smfmac_f32_16x16x64_bf16 v[128:131], v[172:175], v[80:87], v202\n\t" \
  "s_waitcnt lgkmcnt(4)\n\t" \
  "v_smfmac_f32_16x16x64_bf16 v[128:131], v[176:179], v[88:95], v202\n\t" \
  "s_waitcnt lgkmcnt(3)\n\t" \
  "v_smfmac_f32_16x16x64_bf16 v[128:131], v[180:183], v[96:103], v202\n\t" \
  "s_waitcnt lgkmcnt(2)\n\t" \
  "v_smfmac_f32_16x16x64_bf16 v[128:131], v[184:187], v[104:111], v202\n\t" \
  "s_waitcnt lgkmcnt(1)\n\t" \
  "v_smfmac_f32_16x16x64_bf16 v[128:131], v[188:191], v[112:119], v202\n\t" \
  "s_waitcnt lgkmcnt(0)\n\t" \
  "v_smfmac_f32_16x16x64_bf16 v[128:131], v[192:195], v[120:127], v202\n\t" \
  "s_nop 7\n\t" \
  "v_add_f32 v128, v128, v130\n\t" \
  "v_add_f32 v129, v129, v131\n\t" \
  "v_cvt_pk_bf16_f32 v128, v128, v129\n\t" \
  "buffer_store_dword v128, v203, %[rs], %[sv] offen sc1\n\t" \
  "ds_write_b32 v204, v128\n\t" \
  "s_waitcnt lgkmcnt(0)\n\t" \
  "s_barrier\n\t" \
  "s_mov_b32 %[spins], s92\n\t" \
  "s_branch .Lend%=\n\t" \
  ".Lslow%=:\n\t" \
  "s_mov_b64 s[94:95], 0\n\t" \
  "v_cmp_eq_u32 vcc, %[sent], v132\n\t" \
  "s_or_b64 s[94:95], s[94:95], vcc\n\t" \
  "v_cmp_eq_u32 vcc, %[sent], v133\n\t" \
  "s_or_b64 s[94:95], s[94:95], vcc\n\t" \
  "v_cmp_eq_u32 vcc, %[sent], v134\n\t" \
  "s_or_b64 s[94:95], s[94:95], vcc\n\t" \
  "v_cmp_eq_u32 vcc, %[sent], v135\n\t" \
  "s_or_b64 s[94:95], s[94:95], vcc\n\t" \
  "v_cmp_eq_u32 vcc, %[sent], v136\n\t" \
  "s_or_b64 s[94:95], s[94:95], vcc\n\t" \
  "v_cmp_eq_u32 vcc, %[sent], v137\n\t" \
  "s_or_b64 s[94:95], s[94:95], vcc\n\t" \
  "v_cmp_eq_u32 vcc, %[sent], v138\n\t" \
  "s_or_b64 s[94:95], s[94:95], vcc\n\t" \
  "v_cmp_eq_u32 vcc, %[sent], v139\n\t" \
  "s_or_b64 s[94:95], s[94:95], vcc\n\t" \
  "s_cmp_eq_u64 s[94:95], 0\n\t" \
  "s_cbranch_scc1 .Lok%=\n\t" \
  "v_mov_b64 v[128:129], 0\n\t" \
  "v_mov_b64 v[130:131], 0\n\t" \
  "s_add_u32 s92, s92, 1\n\t" \
  "s_sleep 6\n\t" \
  "s_cmp_eq_u32 s92, 1\n\t" \
  "s_cbranch_scc1 .Lpoll%=\n\t" \
  "s_sleep 1\n\t" \
  "s_and_b32 s93, s92, 63\n\t" \
  "s_cmp_lg_u32 s93, 0\n\t" \
  "s_cbranch_scc1 .Lpoll%=\n\t" \
  "v_mov_b32 v197, 0\n\t" \
  "buffer_load_dword v196, v197, %[rs], %[sab] offen sc1\n\t" \
  "s_waitcnt vmcnt(0)\n\t" \
  "v_readfirstlane_b32 s93, v196\n\t" \
  "s_nop 3\n\t" \
  "s_cmp_eq_u32 s93, 1\n\t" \
  "s_cbranch_scc1 .Lok%=\n\t" \
  "s_cmp_lt_u32 s92, 0x80000\n\t" \
  "s_cbranch_scc1 .Lpoll%=\n\t" \
  "v_mov_b32 v196, 1\n\t" \
  "buffer_store_dword v196, v197, %[rs], %[sab] offen sc1\n\t" \
  "s_branch .Lok%=\n\t" \
  ".Lend%=:\n\t"
#define TK_ASM \
  "buffer_load_dwordx4 v[224:227], v200, %[rs], %[sk] offen offset:0 sc1\n\t" \
  "buffer_load_dwordx4 v[228:231], v200, %[rs], %[sk] offen offset:1024 sc1\n\t" \
  "s_memrealtime s[88:89]\n\t" \
  "v_add_u32 v205, %[tl], v200\n\t" \
  "v_add_u32 v206, %[tq], v205\n\t" \
  "v_add_u32 v207, %[tv], v205\n\t" \
  "s_waitcnt lgkmcnt(0)\n\t" \
  "ds_read_b128 v[128:131], v206 offset:8192\n\t" \
  "ds_read_b128 v[144:147], v205 offset:0\n\t" \
  "ds_read_b128 v[160:163], v205 offset:4096\n\t" \
  "ds_read_b128 v[132:135], v206 offset:9216\n\t" \
  "ds_read_b128 v[148:151], v205 offset:1024\n\t" \
  "ds_read_b128 v[164:167], v205 offset:5120\n\t" \
  "ds_read_b128 v[136:139], v206 offset:10240\n\t" \
  "ds_read_b128 v[152:155], v205 offset:2048\n\t" \
  "ds_read_b128 v[168:171], v205 offset:6144\n\t" \
  "ds_read_b128 v[140:143], v206 offset:11264\n\t" \
  "ds_read_b128 v[156:159], v205 offset:3072\n\t" \
  "ds_read_b128 v[172:175], v205 offset:7168\n\t" \
  "v_add_u32 v206, %[vp], v218\n\t" \
  "s_lshl_b32 s88, s88, 2\n\t" \
  "s_add_u32 s89, s88, %[od]\n\t" \
  "s_waitcnt lgkmcnt(10)\n\t" \
  "v_mfma_f32_16x16x32_bf16 v[196:199], v[144:147], v[128:131], 0\n\t" \
  "s_waitcnt lgkmcnt(9)\n\t" \
  "v_mfma_f32_16x16x32_bf16 v[144:147], v[160:163], v[128:131], 0\n\t" \
  "s_waitcnt lgkmcnt(7)\n\t" \
  "v_mfma_f32_16x16x32_bf16 v[196:199], v[148:151], v[132:135], v[196:199]\n\t" \
  "s_waitcnt lgkmcnt(6)\n\t" \
  "v_mfma_f32_16x16x32_bf16 v[144:147], v[164:167], v[132:135], v[144:147]\n\t" \
  "s_waitcnt lgkmcnt(4)\n\t" \
  "v_mfma_f32_16x16x32_bf16 v[196:199], v[152:155], v[136:139], v[196:199]\n\t" \
  "s_waitcnt lgkmcnt(3)\n\t" \
  "v_mfma_f32_16x16x32_bf16 v[144:147], v[168:171], v[136:139], v[144:147]\n\t" \
  "s_waitcnt lgkmcnt(1)\n\t" \
  "v_mfma_f32_16x16x32_bf16 v[196:199], v[156:159], v[140:143], v[196:199]\n\t" \
  "s_waitcnt lgkmcnt(0)\n\t" \
  "v_mfma_f32_16x16x32_bf16 v[144:147], v[172:175], v[140:143], v[144:147]\n\t" \
  "ds_read_b128 v[176:179], v207 offset:16384\n\t" \
  "ds_read_b128 v[180:183], v207 offset:17408\n\t" \
  "ds_read_b128 v[184:187], v207 offset:18432\n\t" \
  "ds_read_b128 v[188:191], v207 offset:19456\n\t" \
  "ds_read2st64_b32 v[192:193], v206 offset0:0 offset1:1\n\t" \
  "ds_read2st64_b32 v[194:195], v206 offset0:2 offset1:3\n\t" \
  "v_cvt_pk_bf16_f32 v197, v196, v197\n\t" \
  "v_cvt_pk_bf16_f32 v199, v198, v199\n\t" \
  "v_cvt_pk_bf16_f32 v145, v144, v145\n\t" \
  "v_cvt_pk_bf16_f32 v147, v146, v147\n\t" \
  "v_lshlrev_b32 v196, 16, v197\n\t" \
  "v_lshlrev_b32 v198, 16, v199\n\t" \
  "v_lshlrev_b32 v144, 16, v145\n\t" \
  "v_lshlrev_b32 v146, 16, v147\n\t" \
  "v_and_b32 v197, 0xffff0000, v197\n\t" \
  "v_and_b32 v199, 0xffff0000, v199\n\t" \
  "v_and_b32 v145, 0xffff0000, v145\n\t" \
  "v_and_b32 v147, 0xffff0000, v147\n\t" \
  "v_pk_mul_f32 v[196:197], v[196:197], %[sc] op_sel_hi:[1,0]\n\t" \
  "v_pk_mul_f32 v[198:199], v[198:199], %[sc] op_sel_hi:[1,0]\n\t" \
  "v_pk_mul_f32 v[144:145], v[144:145], %[sc] op_sel_hi:[1,0]\n\t" \
  "v_pk_mul_f32 v[146:147], v[146:147], %[sc] op_sel_hi:[1,0]\n\t" \
  "v_cvt_pk_bf16_f32 v197, v196, v197\n\t" \
  "v_cvt_pk_bf16_f32 v199, v198, v199\n\t" \
  "v_cvt_pk_bf16_f32 v145, v144, v145\n\t" \
  "v_cvt_pk_bf16_f32 v147, v146, v147\n\t" \
  "v_lshlrev_b32 v196, 16, v197\n\t" \
  "v_lshlrev_b32 v198, 16, v199\n\t" \
  "v_lshlrev_b32 v144, 16, v145\n\t" \
  "v_lshlrev_b32 v146, 16, v147\n\t" \
  "v_and_b32 v197, 0xffff0000, v197\n\t" \
  "v_and_b32 v199, 0xffff0000, v199\n\t" \
  "v_and_b32 v145, 0xffff0000, v145\n\t" \
  "v_and_b32 v147, 0xffff0000, v147\n\t" \
  "v_min_f32 v196, v196, v208\n\t" \
  "v_min_f32 v197, v197, v209\n\t" \
  "v_min_f32 v198, v198, v210\n\t" \
  "v_min_f32 v199, v199, v211\n\t" \
  "v_min_f32 v144, v144, v212\n\t" \
  "v_min_f32 v145, v145, v213\n\t" \
  "v_min_f32 v146, v146, v214\n\t" \
  "v_min_f32 v147, v147, v215\n\t" \
  "v_max3_f32 v148, v196, v197, v198\n\t" \
  "v_max3_f32 v149, v199, v144, v145\n\t" \
  "v_max3_f32 v148, v148, v146, v147\n\t" \
  "v_max_f32 v150, v148, v149\n\t" \
  "v_max_f32 v151, v148, v149\n\t" \
  "s_nop 1\n\t" \
  "v_permlane32_swap_b32 v150, v151\n\t" \
  "v_max_f32 v150, v150, v151\n\t" \
  "s_mov_b32 s92, 0\n\t" \
  ".Ltake%=:\n\t" \
  "s_waitcnt vmcnt(1) lgkmcnt(0)\n\t" \
  "ds_write_b128 v216, v[224:227]\n\t" \
  "v_max3_u32 v148, v224, v225, v226\n\t" \
  "s_waitcnt vmcnt(0)\n\t" \
  "ds_write_b128 v216, v[228:231] offset:1024\n\t" \
  "ds_read_b64_tr_b16 v[160:161], v217 offset:0\n\t" \
  "ds_read_b64_tr_b16 v[162:163], v217 offset:64\n\t" \
  "ds_read_b64_tr_b16 v[164:165], v217 offset:512\n\t" \
  "ds_read_b64_tr_b16 v[166:167], v217 offset:576\n\t" \
  "ds_read_b64_tr_b16 v[168:169], v217 offset:1024\n\t" \
  "ds_read_b64_tr_b16 v[170:171], v217 offset:1088\n\t" \
  "ds_read_b64_tr_b16 v[172:173], v217 offset:1536\n\t" \
  "ds_read_b64_tr_b16 v[174:175], v217 offset:1600\n\t" \
  "v_max3_u32 v148, v148, v227, v228\n\t" \
  "v_max3_u32 v148, v148, v229, v230\n\t" \
  "v_max_u32 v148, v148, v231\n\t" \
  "v_cmp_le_u32 vcc, %[sent], v148\n\t" \
  "s_cbranch_vccnz .Lslow%=\n\t" \
  ".Lok%=:\n\t" \
  "s_waitcnt lgkmcnt(6)\n\t" \
  "v_mfma_f32_16x16x32_bf16 v[152:155], v[160:163], v[128:131], 0\n\t" \
  "s_waitcnt lgkmcnt(4)\n\t" \
  "v_mfma_f32_16x16x32_bf16 v[152:155], v[164:167], v[132:135], v[152:155]\n\t" \
  "s_waitcnt lgkmcnt(2)\n\t" \
  "v_mfma_f32_16x16x32_bf16 v[152:155], v[168:171], v[136:139], v[152:155]\n\t" \
  "s_waitcnt lgkmcnt(0)\n\t" \
  "v_mfma_f32_16x16x32_bf16 v[152:155], v[172:175], v[140:143], v[152:155]\n\t" \
  "v_mov_b32 v157, 0\n\t" \
  "v_mov_b32 v158, 0\n\t" \
  "s_nop 5\n\t" \
  "v_cvt_pk_bf16_f32 v153, v152, v153\n\t" \
  "v_cvt_pk_bf16_f32 v155, v154, v155\n\t" \
  "v_lshlrev_b32 v152, 16, v153\n\t" \
  "v_lshlrev_b32 v154, 16, v155\n\t" \
  "v_and_b32 v153, 0xffff0000, v153\n\t" \
  "v_and_b32 v155, 0xffff0000, v155\n\t" \
  "v_pk_mul_f32 v[152:153], v[152:153], %[sc] op_sel_hi:[1,0]\n\t" \
  "v_pk_mul_f32 v[154:155], v[154:155], %[sc] op_sel_hi:[1,0]\n\t" \
  "v_cvt_pk_bf16_f32 v153, v152, v153\n\t" \
  "v_cvt_pk_bf16_f32 v155, v154, v155\n\t" \
  "v_lshlrev_b32 v152, 16, v153\n\t" \
  "v_lshlrev_b32 v154, 16, v155\n\t" \
  "v_and_b32 v153, 0xffff0000, v153\n\t" \
  "v_and_b32 v155, 0xffff0000, v155\n\t" \
  "v_max3_f32 v148, v152, v153, v150\n\t" \
  "v_max3_f32 v149, v148, v154, v155\n\t" \
  "v_max3_f32 v150, v148, v154, v155\n\t" \
  "v_cndmask_b32 v152, v152, v154, %[up]\n\t" \
  "v_cndmask_b32 v153, v153, v155, %[up]\n\t" \
  "v_permlane16_swap_b32 v149, v150\n\t" \
  "v_max_f32 v148, v149, v150\n\t" \
  "v_mul_f32 v148, %[negc], v148\n\t" \
  "v_fma_f32 v152, v152, %[c], v148\n\t" \
  "v_fma_f32 v153, v153, %[c], v148\n\t" \
  "v_exp_f32 v152, v152\n\t" \
  "v_fma_f32 v196, v196, %[c], v148\n\t" \
  "v_exp_f32 v153, v153\n\t" \
  "v_fma_f32 v197, v197, %[c], v148\n\t" \
  "v_exp_f32 v196, v196\n\t" \
  "v_fma_f32 v198, v198, %[c], v148\n\t" \
  "v_exp_f32 v197, v197\n\t" \
  "v_fma_f32 v199, v199, %[c], v148\n\t" \
  "v_exp_f32 v198, v198\n\t" \
  "v_fma_f32 v144, v144, %[c], v148\n\t" \
  "v_pk_add_f32 v[132:133], v[152:153], v[196:197]\n\t" \
  "v_exp_f32 v199, v199\n\t" \
  "v_fma_f32 v145, v145, %[c], v148\n\t" \
  "v_exp_f32 v144, v144\n\t" \
  "v_fma_f32 v146, v146, %[c], v148\n\t" \
  "v_pk_add_f32 v[132:133], v[132:133], v[198:199]\n\t" \
  "v_exp_f32 v145, v145\n\t" \
  "v_fma_f32 v147, v147, %[c], v148\n\t" \
  "v_exp_f32 v146, v146\n\t" \
  "v_pk_add_f32 v[132:133], v[132:133], v[144:145]\n\t" \
  "v_exp_f32 v147, v147\n\t" \
  "s_nop 0\n\t" \
  "v_pk_add_f32 v[132:133], v[132:133], v[146:147]\n\t" \
  "v_add_f32 v134, v132, v133\n\t" \
  "v_add_f32 v135, v132, v133\n\t" \
  "s_nop 1\n\t" \
  "v_permlane16_swap_b32 v134, v135\n\t" \
  "v_add_f32 v132, v134, v135\n\t" \
  "v_add_f32 v133, v134, v135\n\t" \
  "s_nop 1\n\t" \
  "v_permlane32_swap_b32 v132, v133\n\t" \
  "v_add_f32 v132, v132, v133\n\t" \
  "v_rcp_f32 v148, v132\n\t" \
  "s_nop 0\n\t" \
  "v_pk_mul_f32 v[152:153], v[152:153], v[148:149] op_sel_hi:[1,0]\n\t" \
  "v_pk_mul_f32 v[196:197], v[196:197], v[148:149] op_sel_hi:[1,0]\n\t" \
  "v_pk_mul_f32 v[198:199], v[198:199], v[148:149] op_sel_hi:[1,0]\n\t" \
  "v_pk_mul_f32 v[144:145], v[144:145], v[148:149] op_sel_hi:[1,0]\n\t" \
  "v_pk_mul_f32 v[146:147], v[146:147], v[148:149] op_sel_hi:[1,0]\n\t" \
  "v_cvt_pk_bf16_f32 v156, v152, v153\n\t" \
  "v_cvt_pk_bf16_f32 v159, v152, v153\n\t" \
  "v_cvt_pk_bf16_f32 v128, v196, v197\n\t" \
  "v_cvt_pk_bf16_f32 v129, v198, v199\n\t" \
  "v_mfma_f32_16x16x16_bf16 v[160:163], v[192:193], v[156:157], 0\n\t" \
  "v_cvt_pk_bf16_f32 v130, v144, v145\n\t" \
  "v_mfma_f32_16x16x16_bf16 v[164:167], v[192:193], v[158:159], 0\n\t" \
  "v_cvt_pk_bf16_f32 v131, v146, v147\n\t" \
  "v_mfma_f32_16x16x16_bf16 v[168:171], v[194:195], v[156:157], 0\n\t" \
  "v_mfma_f32_16x16x16_bf16 v[172:175], v[194:195], v[158:159], 0\n\t" \
  "s_nop 0\n\t" \
  "v_mfma_f32_16x16x32_bf16 v[160:163], v[176:179], v[128:131], v[160:163]\n\t" \
  "s_nop 0\n\t" \
  "v_mfma_f32_16x16x32_bf16 v[164:167], v[180:183], v[128:131], v[164:167]\n\t" \
  "s_nop 0\n\t" \
  "v_mfma_f32_16x16x32_bf16 v[168:171], v[184:187], v[128:131], v[168:171]\n\t" \
  "v_mfma_f32_16x16x32_bf16 v[172:175], v[188:191], v[128:131], v[172:175]\n\t" \
  "s_nop 2\n\t" \
  "v_cvt_pk_bf16_f32 v136, v160, v161\n\t" \
  "v_cvt_pk_bf16_f32 v137, v162, v163\n\t" \
  "v_cvt_pk_bf16_f32 v138, v164, v165\n\t" \
  "v_cvt_pk_bf16_f32 v139, v166, v167\n\t" \
  "v_cvt_pk_bf16_f32 v140, v168, v169\n\t" \
  "v_cvt_pk_bf16_f32 v141, v170, v171\n\t" \
  "v_cvt_pk_bf16_f32 v142, v172, v173\n\t" \
  "v_cvt_pk_bf16_f32 v143, v174, v175\n\t" \
  "s_memrealtime s[90:91]\n\t" \
  "s_waitcnt lgkmcnt(0)\n\t" \
  "s_lshl_b32 s93, s90, 2\n\t" \
  "s_sub_i32 s93, s89, s93\n\t" \
  "s_cmp_le_i32 s93, 0\n\t" \
  "s_cbranch_scc1 .Lgoo%=\n\t" \
  "s_min_i32 s93, s93, 8000\n\t" \
  "s_mov_b32 s94, s93\n\t" \
  ".Lslo%=:\n\t" \
  "s_sub_i32 s94, s94, 18\n\t" \
  "s_cmp_lt_i32 s94, 30\n\t" \
  "s_cbranch_scc1 .Lsxo%=\n\t" \
  "s_sleep 1\n\t" \
  "s_branch .Lslo%=\n\t" \
  ".Lsxo%=:\n\t" \
  "s_add_u32 s93, s93, 3\n\t" \
  "s_lshr_b32 s93, s93, 2\n\t" \
  "s_add_u32 s93, s93, s90\n\t" \
  "s_memrealtime s[90:91]\n\t" \
  ".Lpaceo%=:\n\t" \
  "s_memrealtime s[94:95]\n\t" \
  "s_waitcnt lgkmcnt(1)\n\t" \
  "s_sub_i32 s92, s90, s93\n\t" \
  "s_cmp_ge_i32 s92, 0\n\t" \
  "s_cbranch_scc1 .Leo%=\n\t" \
  "s_memrealtime s[90:91]\n\t" \
  "s_waitcnt lgkmcnt(1)\n\t" \
  "s_sub_i32 s92, s94, s93\n\t" \
  "s_cmp_lt_i32 s92, 0\n\t" \
  "s_cbranch_scc1 .Lpaceo%=\n\t" \
  ".Leo%=:\n\t" \
  ".Lgoo%=:\n\t" \
  "buffer_store_dwordx4 v[136:139], v200, %[rt], %[so] offen offset:0 sc1\n\t" \
  "buffer_store_dwordx4 v[140:143], v200, %[rt], %[so] offen offset:1024 sc1\n\t" \
  "s_waitcnt lgkmcnt(0)\n\t" \
  "s_branch .Lend%=\n\t" \
  ".Lpoll%=:\n\t" \
  "buffer_load_dwordx4 v[224:227], v200, %[rs], %[sk] offen offset:0 sc1\n\t" \
  "buffer_load_dwordx4 v[228:231], v200, %[rs], %[sk] offen offset:1024 sc1\n\t" \
  "s_branch .Ltake%=\n\t" \
  "s_branch .Lend%=\n\t" \
  ".Lslow%=:\n\t" \
  "s_mov_b64 s[94:95], 0\n\t" \
  "v_cmp_eq_u32 vcc, %[sent], v224\n\t" \
  "s_or_b64 s[94:95], s[94:95], vcc\n\t" \
  "v_cmp_eq_u32 vcc, %[sent], v225\n\t" \
  "s_or_b64 s[94:95], s[94:95], vcc\n\t" \
  "v_cmp_eq_u32 vcc, %[sent], v226\n\t" \
  "s_or_b64 s[94:95], s[94:95], vcc\n\t" \
  "v_cmp_eq_u32 vcc, %[sent], v227\n\t" \
  "s_or_b64 s[94:95], s[94:95], vcc\n\t" \
  "v_cmp_eq_u32 vcc, %[sent], v228\n\t" \
  "s_or_b64 s[94:95], s[94:95], vcc\n\t" \
  "v_cmp_eq_u32 vcc, %[sent], v229\n\t" \
  "s_or_b64 s[94:95], s[94:95], vcc\n\t" \
  "v_cmp_eq_u32 vcc, %[sent], v230\n\t" \
  "s_or_b64 s[94:95], s[94:95], vcc\n\t" \
  "v_cmp_eq_u32 vcc, %[sent], v231\n\t" \
  "s_or_b64 s[94:95], s[94:95], vcc\n\t" \
  "s_cmp_eq_u64 s[94:95], 0\n\t" \
  "s_cbranch_scc1 .Lok%=\n\t" \
  "s_add_u32 s92, s92, 1\n\t" \
  "s_sleep 6\n\t" \
  "s_cmp_eq_u32 s92, 1\n\t" \
  "s_cbranch_scc1 .Lpoll%=\n\t" \
  "s_sleep 1\n\t" \
  "s_and_b32 s93, s92, 63\n\t" \
  "s_cmp_lg_u32 s93, 0\n\t" \
  "s_cbranch_scc1 .Lpoll%=\n\t" \
  "v_mov_b32 v149, 0\n\t" \
  "buffer_load_dword v148, v149, %[rs], %[sab] offen sc1\n\t" \
  "s_waitcnt vmcnt(0)\n\t" \
  "v_readfirstlane_b32 s93, v148\n\t" \
  "s_nop 3\n\t" \
  "s_cmp_eq_u32 s93, 1\n\t" \
  "s_cbranch_scc1 .Lok%=\n\t" \
  "s_cmp_lt_u32 s92, 0x80000\n\t" \
  "s_cbranch_scc1 .Lpoll%=\n\t" \
  "v_mov_b32 v148, 1\n\t" \
  "buffer_store_dword v148, v149, %[rs], %[sab] offen sc1\n\t" \
  "s_branch .Lok%=\n\t" \
  ".Lend%=:\n\t"
constexpr int TLAG = 2;
#define RED_ASM \
  "s_mov_b32 %[step], 0\n\t" \
  "s_lshl_b32 %[sh], %[step], 15\n\t" \
  "s_lshr_b32 s93, %[step], 4\n\t" \
  "s_lshr_b32 s93, %[pm], s93\n\t" \
  "s_and_b32 s93, s93, 4\n\t" \
  "s_lshl_b32 s93, s93, 11\n\t" \
  "s_lshl2_add_u32 %[so], %[sh], s93\n\t" \
  "s_sub_i32 s93, %[step], 2\n\t" \
  "s_lshl_b32 %[st], s93, 19\n\t" \
  "s_lshl_b32 s94, s93, 11\n\t" \
  "s_xor_b32 s94, s94, %[s0]\n\t" \
  "s_and_b32 s94, s94, 0x2000\n\t" \
  "s_or_b32 %[st], %[st], s94\n\t" \
  "s_cmp_lt_i32 s93, 0\n\t" \
  "s_cselect_b32 %[st], 0x70000000, %[st]\n\t" \
  "s_add_u32 %[st4], %[st], 4096\n\t" \
  "buffer_load_dwordx2 v[166:167], v173, %[rs], 0 offen sc1\n\t" \
  "v_mov_b64 v[144:145], 0\n\t" \
  "v_mov_b64 v[146:147], 0\n\t" \
  "v_mov_b64 v[148:149], 0\n\t" \
  "v_mov_b64 v[150:151], 0\n\t" \
  "v_mov_b64 v[152:153], 0\n\t" \
  "v_mov_b64 v[154:155], 0\n\t" \
  "v_mov_b64 v[156:157], 0\n\t" \
  "v_mov_b64 v[158:159], 0\n\t" \
  ".Lstep%=:\n\t" \
  "s_setprio 3\n\t" \
  "s_mov_b32 s92, 0\n\t" \
  ".Lcal%=:\n\t" \
  "s_waitcnt vmcnt(0)\n\t" \
  "s_memrealtime s[76:77]\n\t" \
  "v_cmp_ne_u32 vcc, %[step], v166\n\t" \
  "v_readlane_b32 s90, v167, 0\n\t" \
  "v_readlane_b32 s91, v167, 1\n\t" \
  "v_readlane_b32 s94, v167, 2\n\t" \
  "v_readlane_b32 s95, v167, 3\n\t" \
  "s_sub_i32 s90, s90, %[base]\n\t" \
  "s_sub_i32 s91, s91, %[base]\n\t" \
  "s_sub_i32 s94, s94, %[base]\n\t" \
  "s_sub_i32 s95, s95, %[base]\n\t" \
  "s_max_i32 s90, s90, s91\n\t" \
  "s_max_i32 s94, s94, s95\n\t" \
  "s_max_i32 s90, s90, s94\n\t" \
  "s_add_i32 s90, s90, %[ds]\n\t" \
  "s_waitcnt lgkmcnt(0)\n\t" \
  "s_lshl2_add_u32 s76, s76, %[mn]\n\t" \
  "s_sub_i32 s76, s76, %[base]\n\t" \
  "s_add_u32 %[E], s90, %[base]\n\t" \
  "s_and_b32 s93, vcc_lo, 0xf\n\t" \
  "s_cbranch_scc1 .Lbl%=\n\t" \
  ".Lgo%=:\n\t" \
  "v_mov_b32 v166, 7\n\t" \
  "s_mov_b64 exec, 1\n\t" \
  "v_mov_b32 v164, s90\n\t" \
  "ds_max_u32 v168, v164 offset:4100\n\t" \
  "s_mov_b64 exec, -1\n\t" \
  "s_memrealtime s[90:91]\n\t" \
  "s_waitcnt lgkmcnt(0)\n\t" \
  "s_lshl_b32 s93, s90, 2\n\t" \
  "s_sub_i32 s93, %[E], s93\n\t" \
  "s_cmp_le_i32 s93, 0\n\t" \
  "s_cbranch_scc1 .Lgop%=\n\t" \
  "s_min_i32 s93, s93, 8000\n\t" \
  "s_mov_b32 s94, s93\n\t" \
  ".Lslp%=:\n\t" \
  "s_sub_i32 s94, s94, 18\n\t" \
  "s_cmp_lt_i32 s94, 30\n\t" \
  "s_cbranch_scc1 .Lsxp%=\n\t" \
  "s_sleep 1\n\t" \
  "s_branch .Lslp%=\n\t" \
  ".Lsxp%=:\n\t" \
  "s_sub_i32 s93, s93, 12\n\t" \
  "s_max_i32 s93, s93, 4\n\t" \
  "s_lshr_b32 s93, s93, 2\n\t" \
  "s_add_u32 s93, s93, s90\n\t" \
  "s_memrealtime s[90:91]\n\t" \
  ".Lpacep%=:\n\t" \
  "s_memrealtime s[94:95]\n\t" \
  "s_waitcnt lgkmcnt(1)\n\t" \
  "s_sub_i32 s92, s90, s93\n\t" \
  "s_cmp_ge_i32 s92, 0\n\t" \
  "s_cbranch_scc1 .Lep%=\n\t" \
  "s_memrealtime s[90:91]\n\t" \
  "s_waitcnt lgkmcnt(1)\n\t" \
  "s_sub_i32 s92, s94, s93\n\t" \
  "s_cmp_lt_i32 s92, 0\n\t" \
  "s_cbranch_scc1 .Lpacep%=\n\t" \
  "s_nop 4\n\t" \
  ".Lep%=:\n\t" \
  "s_add_u32 s92, s92, s93\n\t" \
  "s_lshl_b32 s92, s92, 2\n\t" \
  "s_sub_i32 s92, %[E], s92\n\t" \
  "s_max_i32 s92, s92, 0\n\t" \
  "s_bitcmp0_b32 s92, 3\n\t" \
  "s_cbranch_scc1 .Lf3p%=\n\t" \
  "s_nop 15\n\t" \
  ".Lf3p%=:\n\t" \
  "s_bitcmp0_b32 s92, 2\n\t" \
  "s_cbranch_scc1 .Lf2p%=\n\t" \
  "s_nop 10\n\t" \
  ".Lf2p%=:\n\t" \
  ".Lgop%=:\n\t" \
  "s_mov_b32 s92, 0\n\t" \
  "buffer_load_dwordx4 v[128:131], v234, %[rs], %[so] offen sc1\n\t" \
  "s_nop 15\n\t" \
  "buffer_load_dwordx4 v[132:135], v235, %[rs], %[so] offen sc1\n\t" \
  "s_nop 15\n\t" \
  "buffer_load_dwordx4 v[136:139], v236, %[rs], %[so] offen sc1\n\t" \
  "s_nop 15\n\t" \
  "buffer_load_dwordx4 v[140:143], v237, %[rs], %[so] offen sc1\n\t" \
  "s_waitcnt lgkmcnt(0)\n\t" \
  ".Lw%=:\n\t" \
  "s_waitcnt vmcnt(3)\n\t" \
  "v_smfmac_f32_16x16x64_bf16 v[144:147], v[128:131], v[0:7], v172\n\t" \
  "v_max3_u32 v164, v128, v129, 0\n\t" \
  "v_smfmac_f32_16x16x64_bf16 v[148:151], v[128:131], v[8:15], v172\n\t" \
  "v_max3_u32 v164, v130, v131, v164\n\t" \
  "v_smfmac_f32_16x16x64_bf16 v[152:155], v[128:131], v[16:23], v172\n\t" \
  "v_smfmac_f32_16x16x64_bf16 v[156:159], v[128:131], v[24:31], v172\n\t" \
  "buffer_load_dwordx4 v[176:179], v238, %[rt], %[st] offen offset:0 sc1\n\t" \
  "s_waitcnt vmcnt(3)\n\t" \
  "v_smfmac_f32_16x16x64_bf16 v[144:147], v[132:135], v[32:39], v172\n\t" \
  "v_max3_u32 v164, v132, v133, v164\n\t" \
  "v_smfmac_f32_16x16x64_bf16 v[148:151], v[132:135], v[40:47], v172\n\t" \
  "v_max3_u32 v164, v134, v135, v164\n\t" \
  "v_smfmac_f32_16x16x64_bf16 v[152:155], v[132:135], v[48:55], v172\n\t" \
  "v_smfmac_f32_16x16x64_bf16 v[156:159], v[132:135], v[56:63], v172\n\t" \
  "buffer_load_dwordx4 v[180:183], v238, %[rt], %[st] offen offset:1024 sc1\n\t" \
  "s_waitcnt vmcnt(3)\n\t" \
  "v_smfmac_f32_16x16x64_bf16 v[144:147], v[136:139], v[64:71], v172\n\t" \
  "v_max3_u32 v164, v136, v137, v164\n\t" \
  "v_smfmac_f32_16x16x64_bf16 v[148:151], v[136:139], v[72:79], v172\n\t" \
  "v_max3_u32 v164, v138, v139, v164\n\t" \
  "v_smfmac_f32_16x16x64_bf16 v[152:155], v[136:139], v[80:87], v172\n\t" \
  "v_smfmac_f32_16x16x64_bf16 v[156:159], v[136:139], v[88:95], v172\n\t" \
  "buffer_load_dwordx4 v[184:187], v238, %[rt], %[st4] offen offset:0 sc1\n\t" \
  "s_waitcnt vmcnt(3)\n\t" \
  "v_smfmac_f32_16x16x64_bf16 v[144:147], v[140:143], v[96:103], v172\n\t" \
  "v_max3_u32 v164, v140, v141, v164\n\t" \
  "v_smfmac_f32_16x16x64_bf16 v[148:151], v[140:143], v[104:111], v172\n\t" \
  "v_max3_u32 v164, v142, v143, v164\n\t" \
  "v_smfmac_f32_16x16x64_bf16 v[152:155], v[140:143], v[112:119], v172\n\t" \
  "v_cmp_le_u32 vcc, %[sent], v164\n\t" \
  "v_smfmac_f32_16x16x64_bf16 v[156:159], v[140:143], v[120:127], v172\n\t" \
  "buffer_load_dwordx4 v[188:191], v238, %[rt], %[st4] offen offset:1024 sc1\n\t" \
  "s_cbranch_vccnz .Lslow%=\n\t" \
  ".Lok%=:\n\t" \
  "v_add_f32_dpp v160, v144, v146 quad_perm:[0,1,2,3] row_mask:0x3 bank_mask:0xf\n\t" \
  "v_add_f32_dpp v161, v145, v147 quad_perm:[0,1,2,3] row_mask:0x3 bank_mask:0xf\n\t" \
  "v_add_f32_dpp v162, v148, v150 quad_perm:[0,1,2,3] row_mask:0x3 bank_mask:0xf\n\t" \
  "v_add_f32_dpp v163, v149, v151 quad_perm:[0,1,2,3] row_mask:0x3 bank_mask:0xf\n\t" \
  "v_add_f32_dpp v160, v152, v154 quad_perm:[0,1,2,3] row_mask:0xc bank_mask:0xf\n\t" \
  "v_add_f32_dpp v161, v153, v155 quad_perm:[0,1,2,3] row_mask:0xc bank_mask:0xf\n\t" \
  "v_add_f32_dpp v162, v156, v158 quad_perm:[0,1,2,3] row_mask:0xc bank_mask:0xf\n\t" \
  "v_add_f32_dpp v163, v157, v159 quad_perm:[0,1,2,3] row_mask:0xc bank_mask:0xf\n\t" \
  "s_nop 0\n\t" \
  "v_permlane32_swap_b32 v160, v162\n\t" \
  "v_permlane32_swap_b32 v161, v163\n\t" \
  "v_add_f32 v160, v160, v162\n\t" \
  "v_add_f32 v161, v161, v163\n\t" \
  "ds_write_b64 v169, v[160:161]\n\t" \
  "s_mov_b64 exec, 1\n\t" \
  "ds_inc_rtn_u32 v164, v168, v166 offset:4104\n\t" \
  "s_mov_b64 exec, -1\n\t" \
  "ds_read_b64 v[128:129], v170 offset:0\n\t" \
  "ds_read_b64 v[130:131], v170 offset:512\n\t" \
  "ds_read_b64 v[132:133], v170 offset:1024\n\t" \
  "ds_read_b64 v[134:135], v170 offset:1536\n\t" \
  "ds_read_b64 v[136:137], v170 offset:2048\n\t" \
  "ds_read_b64 v[138:139], v170 offset:2560\n\t" \
  "ds_read_b64 v[140:141], v170 offset:3072\n\t" \
  "ds_read_b64 v[142:143], v170 offset:3584\n\t" \
  "s_waitcnt lgkmcnt(8)\n\t" \
  "v_readfirstlane_b32 s93, v164\n\t" \
  "s_cmp_lg_u32 s93, 7\n\t" \
  "s_cbranch_scc1 .Lb1%=\n\t" \
  "s_waitcnt lgkmcnt(4)\n\t" \
  "v_pk_add_f32 v[128:129], v[128:129], v[130:131]\n\t" \
  "v_pk_add_f32 v[132:133], v[132:133], v[134:135]\n\t" \
  "s_waitcnt lgkmcnt(0)\n\t" \
  "v_pk_add_f32 v[136:137], v[136:137], v[138:139]\n\t" \
  "v_pk_add_f32 v[140:141], v[140:141], v[142:143]\n\t" \
  "v_pk_add_f32 v[128:129], v[128:129], v[132:133]\n\t" \
  "v_pk_add_f32 v[136:137], v[136:137], v[140:141]\n\t" \
  "v_pk_add_f32 v[128:129], v[128:129], v[136:137]\n\t" \
  "v_cvt_pk_bf16_f32 v128, v128, v129\n\t" \
  "s_nop 1\n\t" \
  "v_mov_b32_dpp v129, v128 quad_perm:[1,0,3,2] row_mask:0xf bank_mask:0xf\n\t" \
  "v_perm_b32 v128, v129, v128, v174\n\t" \
  "buffer_store_dword v128, v171, %[rs], %[sh] offen sc1\n\t" \
  "s_memtime s[90:91]\n\t" \
  "s_barrier\n\t" \
  "s_memtime s[76:77]\n\t" \
  "s_memrealtime s[78:79]\n\t" \
  "s_nop 3\n\t" \
  "s_memrealtime s[80:81]\n\t" \
  "s_nop 3\n\t" \
  "s_memrealtime s[82:83]\n\t" \
  "s_nop 3\n\t" \
  "s_memrealtime s[84:85]\n\t" \
  "s_nop 3\n\t" \
  "s_memrealtime s[86:87]\n\t" \
  "s_nop 3\n\t" \
  "s_memrealtime s[88:89]\n\t" \
  "s_mov_b32 s93, 0\n\t" \
  "s_waitcnt lgkmcnt(0)\n\t" \
  "s_cmp_eq_u32 s80, s78\n\t" \
  "s_addc_u32 s93, s93, 0\n\t" \
  "s_cmp_eq_u32 s82, s78\n\t" \
  "s_addc_u32 s93, s93, 0\n\t" \
  "s_cmp_eq_u32 s84, s78\n\t" \
  "s_addc_u32 s93, s93, 0\n\t" \
  "s_cmp_eq_u32 s86, s78\n\t" \
  "s_addc_u32 s93, s93, 0\n\t" \
  "s_cmp_eq_u32 s88, s78\n\t" \
  "s_addc_u32 s93, s93, 0\n\t" \
  "s_mul_i32 s93, s93, 21\n\t" \
  "s_add_u32 s76, s76, 15\n\t" \
  "s_add_u32 s76, s76, s93\n\t" \
  "s_lshl2_add_u32 s78, s78, 16\n\t" \
  "s_mov_b64 exec, 1\n\t" \
  "s_sub_i32 s90, s76, s90\n\t" \
  "s_mul_i32 s90, s90, 11900\n\t" \
  "s_ashr_i32 s90, s90, 16\n\t" \
  "s_sub_u32 s90, s78, s90\n\t" \
  "s_sub_u32 s90, s90, %[base]\n\t" \
  "v_mov_b32 v164, s90\n\t" \
  "ds_write_b32 v168, v164 offset:4108\n\t" \
  "s_mov_b64 exec, -1\n\t" \
  "s_waitcnt vmcnt(1)\n\t" \
  ".Lb2%=:\n\t" \
  "s_setprio 0\n\t" \
  "s_bitcmp0_b64 %[am], %[step]\n\t" \
  "s_cbranch_scc1 .La1%=\n\t" \
  "buffer_store_dwordx4 v[224:227], v228, %[ra], 0 offen sc1\n\t" \
  ".La1%=:\n\t" \
  "buffer_load_dwordx4 v[192:195], v239, %[rt], %[st] offen offset:0 sc1\n\t" \
  "buffer_load_dwordx4 v[196:199], v239, %[rt], %[st] offen offset:1024 sc1\n\t" \
  "v_mfma_f32_16x16x32_bf16 v[128:131], v[0:3], v[176:179], 0\n\t" \
  "v_mfma_f32_16x16x32_bf16 v[136:139], v[8:11], v[176:179], 0\n\t" \
  "ds_read2st64_b32 v[144:145], v231 offset0:0 offset1:8\n\t" \
  "ds_read2st64_b32 v[146:147], v231 offset0:16 offset1:24\n\t" \
  "ds_read2st64_b32 v[148:149], v231 offset0:32 offset1:40\n\t" \
  "ds_read2st64_b32 v[150:151], v231 offset0:48 offset1:56\n\t" \
  "buffer_load_dwordx4 v[200:203], v239, %[rt], %[st4] offen offset:0 sc1\n\t" \
  "buffer_load_dwordx4 v[204:207], v239, %[rt], %[st4] offen offset:1024 sc1\n\t" \
  "v_mfma_f32_16x16x32_bf16 v[132:135], v[4:7], v[180:183], 0\n\t" \
  "v_mfma_f32_16x16x32_bf16 v[140:143], v[12:15], v[180:183], 0\n\t" \
  "s_waitcnt lgkmcnt(0)\n\t" \
  "v_add_f32 v144, v144, v145\n\t" \
  "v_add_f32 v146, v146, v147\n\t" \
  "v_add_f32 v148, v148, v149\n\t" \
  "v_add_f32 v150, v150, v151\n\t" \
  "v_add_f32 v144, v144, v146\n\t" \
  "v_add_f32 v148, v148, v150\n\t" \
  "buffer_load_dwordx4 v[208:211], v240, %[rt], %[st] offen offset:0 sc1\n\t" \
  "buffer_load_dwordx4 v[212:215], v240, %[rt], %[st] offen offset:1024 sc1\n\t" \
  "v_mfma_f32_16x16x32_bf16 v[128:131], v[16:19], v[184:187], v[128:131]\n\t" \
  "v_mfma_f32_16x16x32_bf16 v[136:139], v[24:27], v[184:187], v[136:139]\n\t" \
  "v_add_f32 v144, v144, v148\n\t" \
  "v_cvt_pk_bf16_f32 v144, v144, v144\n\t" \
  "ds_write_b16 v232, v144\n\t" \
  "ds_read_b128 v[224:227], v229\n\t" \
  "v_add_u32 v228, %[ainc], v228\n\t" \
  "v_add_u32 v229, %[rinc], v229\n\t" \
  "v_add_u32 v232, 1024, v232\n\t" \
  "v_xor_b32 v231, 0x4000, v231\n\t" \
  "buffer_load_dwordx4 v[216:219], v240, %[rt], %[st4] offen offset:0 sc1\n\t" \
  "buffer_load_dwordx4 v[220:223], v240, %[rt], %[st4] offen offset:1024 sc1\n\t" \
  "v_mfma_f32_16x16x32_bf16 v[132:135], v[20:23], v[188:191], v[132:135]\n\t" \
  "v_mfma_f32_16x16x32_bf16 v[140:143], v[28:31], v[188:191], v[140:143]\n\t" \
  "buffer_load_dwordx4 v[176:179], v241, %[rt], %[st] offen offset:0 sc1\n\t" \
  "s_waitcnt vmcnt(8)\n\t" \
  "v_mfma_f32_16x16x32_bf16 v[128:131], v[32:35], v[192:195], v[128:131]\n\t" \
  "v_mfma_f32_16x16x32_bf16 v[136:139], v[40:43], v[192:195], v[136:139]\n\t" \
  "buffer_load_dwordx4 v[180:183], v241, %[rt], %[st] offen offset:1024 sc1\n\t" \
  "s_waitcnt vmcnt(8)\n\t" \
  "v_mfma_f32_16x16x32_bf16 v[132:135], v[36:39], v[196:199], v[132:135]\n\t" \
  "v_mfma_f32_16x16x32_bf16 v[140:143], v[44:47], v[196:199], v[140:143]\n\t" \
  "buffer_load_dwordx4 v[184:187], v241, %[rt], %[st4] offen offset:0 sc1\n\t" \
  "s_waitcnt vmcnt(8)\n\t" \
  "v_mfma_f32_16x16x32_bf16 v[128:131], v[48:51], v[200:203], v[128:131]\n\t" \
  "v_mfma_f32_16x16x32_bf16 v[136:139], v[56:59], v[200:203], v[136:139]\n\t" \
  "ds_read_b128 v[160:163], v168 offset:4096\n\t" \
  "buffer_load_dwordx4 v[188:191], v241, %[rt], %[st4] offen offset:1024 sc1\n\t" \
  "s_waitcnt vmcnt(8)\n\t" \
  "v_mfma_f32_16x16x32_bf16 v[132:135], v[52:55], v[204:207], v[132:135]\n\t" \
  "v_mfma_f32_16x16x32_bf16 v[140:143], v[60:63], v[204:207], v[140:143]\n\t" \
  "s_waitcnt lgkmcnt(0)\n\t" \
  "v_readfirstlane_b32 s90, v161\n\t" \
  "v_readfirstlane_b32 s91, v163\n\t" \
  "s_add_u32 s93, s90, %[rl]\n\t" \
  "s_add_u32 s93, s93, %[base]\n\t" \
  "s_add_u32 %[E], s93, %[lk]\n\t" \
  "s_sub_i32 s91, s91, s90\n\t" \
  "s_sub_i32 s91, s91, %[rl]\n\t" \
  "s_min_i32 s93, s91, %[up]\n\t" \
  "s_max_i32 s93, s93, %[dn]\n\t" \
  "s_cmp_gt_i32 s91, 60\n\t" \
  "s_cselect_b32 s93, 2, s93\n\t" \
  "s_add_i32 %[rl], %[rl], s93\n\t" \
  "v_mov_b32 v160, %[rl]\n\t" \
  "buffer_store_dword v160, v175, %[rs], 0 offen sc1\n\t" \
  "s_waitcnt vmcnt(8)\n\t" \
  "v_mfma_f32_16x16x32_bf16 v[128:131], v[64:67], v[208:211], v[128:131]\n\t" \
  "v_mfma_f32_16x16x32_bf16 v[136:139], v[72:75], v[208:211], v[136:139]\n\t" \
  "s_add_u32 %[step], %[step], 1\n\t" \
  "s_lshl_b32 %[sh], %[step], 15\n\t" \
  "s_lshr_b32 s93, %[step], 4\n\t" \
  "s_lshr_b32 s93, %[pm], s93\n\t" \
  "s_and_b32 s93, s93, 4\n\t" \
  "s_lshl_b32 s93, s93, 11\n\t" \
  "s_lshl2_add_u32 %[so], %[sh], s93\n\t" \
  "s_waitcnt vmcnt(7)\n\t" \
  "v_mfma_f32_16x16x32_bf16 v[132:135], v[68:71], v[212:215], v[132:135]\n\t" \
  "v_mfma_f32_16x16x32_bf16 v[140:143], v[76:79], v[212:215], v[140:143]\n\t" \
  "s_sub_i32 s93, %[step], 2\n\t" \
  "s_lshl_b32 %[st], s93, 19\n\t" \
  "s_lshl_b32 s94, s93, 11\n\t" \
  "s_xor_b32 s94, s94, %[s0]\n\t" \
  "s_and_b32 s94, s94, 0x2000\n\t" \
  "s_or_b32 %[st], %[st], s94\n\t" \
  "s_cmp_lt_i32 s93, 0\n\t" \
  "s_cselect_b32 %[st], 0x70000000, %[st]\n\t" \
  "s_add_u32 %[st4], %[st], 4096\n\t" \
  "s_waitcnt vmcnt(6)\n\t" \
  "v_mfma_f32_16x16x32_bf16 v[128:131], v[80:83], v[216:219], v[128:131]\n\t" \
  "v_mfma_f32_16x16x32_bf16 v[136:139], v[88:91], v[216:219], v[136:139]\n\t" \
  "s_waitcnt vmcnt(5)\n\t" \
  "v_mfma_f32_16x16x32_bf16 v[132:135], v[84:87], v[220:223], v[132:135]\n\t" \
  "v_mfma_f32_16x16x32_bf16 v[140:143], v[92:95], v[220:223], v[140:143]\n\t" \
  "s_cmp_eq_u32 %[step], 63\n\t" \
  "s_cbranch_scc1 .Llast%=\n\t" \
  "s_movk_i32 s93, 600\n\t" \
  ".Lpacel%=:\n\t" \
  "s_memrealtime s[90:91]\n\t" \
  "s_waitcnt lgkmcnt(0)\n\t" \
  "s_lshl_b32 s90, s90, 2\n\t" \
  "s_sub_i32 s91, %[E], s90\n\t" \
  "s_cmp_le_i32 s91, 0\n\t" \
  "s_cbranch_scc1 .Lgol%=\n\t" \
  "s_sub_u32 s93, s93, 1\n\t" \
  "s_cmp_lg_u32 s93, 0\n\t" \
  "s_cbranch_scc1 .Lpacel%=\n\t" \
  ".Lgol%=:\n\t" \
  "buffer_load_dwordx2 v[166:167], v173, %[rs], 0 offen sc1\n\t" \
  "s_waitcnt vmcnt(5)\n\t" \
  "v_mfma_f32_16x16x32_bf16 v[128:131], v[96:99], v[176:179], v[128:131]\n\t" \
  "v_mfma_f32_16x16x32_bf16 v[136:139], v[104:107], v[176:179], v[136:139]\n\t" \
  "s_waitcnt vmcnt(4)\n\t" \
  "v_mfma_f32_16x16x32_bf16 v[132:135], v[100:103], v[180:183], v[132:135]\n\t" \
  "v_mfma_f32_16x16x32_bf16 v[140:143], v[108:111], v[180:183], v[140:143]\n\t" \
  "s_waitcnt vmcnt(3)\n\t" \
  "v_mfma_f32_16x16x32_bf16 v[128:131], v[112:115], v[184:187], v[128:131]\n\t" \
  "v_mfma_f32_16x16x32_bf16 v[136:139], v[120:123], v[184:187], v[136:139]\n\t" \
  "s_waitcnt vmcnt(2)\n\t" \
  "v_mfma_f32_16x16x32_bf16 v[132:135], v[116:119], v[188:191], v[132:135]\n\t" \
  "v_mfma_f32_16x16x32_bf16 v[140:143], v[124:127], v[188:191], v[140:143]\n\t" \
  "v_mov_b64 v[144:145], 0\n\t" \
  "v_mov_b64 v[146:147], 0\n\t" \
  "v_mov_b64 v[148:149], 0\n\t" \
  "v_mov_b64 v[150:151], 0\n\t" \
  "v_mov_b64 v[152:153], 0\n\t" \
  "v_mov_b64 v[154:155], 0\n\t" \
  "v_mov_b64 v[156:157], 0\n\t" \
  "v_mov_b64 v[158:159], 0\n\t" \
  "v_pk_add_f32 v[128:129], v[128:129], v[132:133]\n\t" \
  "v_pk_add_f32 v[130:131], v[130:131], v[134:135]\n\t" \
  "ds_write_b128 v230, v[128:131] offset:0\n\t" \
  "v_pk_add_f32 v[136:137], v[136:137], v[140:141]\n\t" \
  "v_pk_add_f32 v[138:139], v[138:139], v[142:143]\n\t" \
  "ds_write_b128 v230, v[136:139] offset:1024\n\t" \
  "v_xor_b32 v230, 0x4000, v230\n\t" \
  "s_cmp_lg_u32 %[step], 63\n\t" \
  "s_cbranch_scc1 .Lstep%=\n\t" \
  ".Lfc%=:\n\t" \
  "s_waitcnt vmcnt(0)\n\t" \
  "v_cmp_eq_u32 vcc, -1, v166\n\t" \
  "s_cbranch_vccnz .Lfw%=\n\t" \
  ".Lfo%=:\n\t" \
  "v_add_u32 v242, 16384, v242\n\t" \
  "buffer_load_dwordx4 v[176:179], v238, %[rt], %[st] offen offset:0 sc1\n\t" \
  "buffer_load_dwordx4 v[180:183], v238, %[rt], %[st] offen offset:1024 sc1\n\t" \
  "buffer_load_dwordx4 v[184:187], v238, %[rt], %[st4] offen offset:0 sc1\n\t" \
  "buffer_load_dwordx4 v[188:191], v238, %[rt], %[st4] offen offset:1024 sc1\n\t" \
  "buffer_load_dwordx4 v[192:195], v239, %[rt], %[st] offen offset:0 sc1\n\t" \
  "buffer_load_dwordx4 v[196:199], v239, %[rt], %[st] offen offset:1024 sc1\n\t" \
  "buffer_load_dwordx4 v[200:203], v239, %[rt], %[st4] offen offset:0 sc1\n\t" \
  "buffer_load_dwordx4 v[204:207], v239, %[rt], %[st4] offen offset:1024 sc1\n\t" \
  "v_subrev_u32 v230, 98304, v230\n\t" \
  ".Lt%=:\n\t" \
  "s_waitcnt vmcnt(7)\n\t" \
  "v_mfma_f32_16x16x32_bf16 v[128:131], v[0:3], v[176:179], 0\n\t" \
  "v_mfma_f32_16x16x32_bf16 v[136:139], v[8:11], v[176:179], 0\n\t" \
  "buffer_load_dwordx4 v[176:179], v240, %[rt], %[st] offen offset:0 sc1\n\t" \
  "buffer_load_dword v166, v242, %[rs], 0 offen sc1\n\t" \
  "s_waitcnt vmcnt(8)\n\t" \
  "v_mfma_f32_16x16x32_bf16 v[132:135], v[4:7], v[180:183], 0\n\t" \
  "v_mfma_f32_16x16x32_bf16 v[140:143], v[12:15], v[180:183], 0\n\t" \
  "buffer_load_dwordx4 v[180:183], v240, %[rt], %[st] offen offset:1024 sc1\n\t" \
  "s_waitcnt vmcnt(8)\n\t" \
  "v_mfma_f32_16x16x32_bf16 v[128:131], v[16:19], v[184:187], v[128:131]\n\t" \
  "v_mfma_f32_16x16x32_bf16 v[136:139], v[24:27], v[184:187], v[136:139]\n\t" \
  "buffer_load_dwordx4 v[184:187], v240, %[rt], %[st4] offen offset:0 sc1\n\t" \
  "s_waitcnt vmcnt(8)\n\t" \
  "v_mfma_f32_16x16x32_bf16 v[132:135], v[20:23], v[188:191], v[132:135]\n\t" \
  "v_mfma_f32_16x16x32_bf16 v[140:143], v[28:31], v[188:191], v[140:143]\n\t" \
  "buffer_load_dwordx4 v[188:191], v240, %[rt], %[st4] offen offset:1024 sc1\n\t" \
  "s_waitcnt vmcnt(8)\n\t" \
  "v_mfma_f32_16x16x32_bf16 v[128:131], v[32:35], v[192:195], v[128:131]\n\t" \
  "v_mfma_f32_16x16x32_bf16 v[136:139], v[40:43], v[192:195], v[136:139]\n\t" \
  "buffer_load_dwordx4 v[192:195], v241, %[rt], %[st] offen offset:0 sc1\n\t" \
  "s_waitcnt vmcnt(8)\n\t" \
  "v_mfma_f32_16x16x32_bf16 v[132:135], v[36:39], v[196:199], v[132:135]\n\t" \
  "v_mfma_f32_16x16x32_bf16 v[140:143], v[44:47], v[196:199], v[140:143]\n\t" \
  "buffer_load_dwordx4 v[196:199], v241, %[rt], %[st] offen offset:1024 sc1\n\t" \
  "s_waitcnt vmcnt(8)\n\t" \
  "v_mfma_f32_16x16x32_bf16 v[128:131], v[48:51], v[200:203], v[128:131]\n\t" \
  "v_mfma_f32_16x16x32_bf16 v[136:139], v[56:59], v[200:203], v[136:139]\n\t" \
  "buffer_load_dwordx4 v[200:203], v241, %[rt], %[st4] offen offset:0 sc1\n\t" \
  "s_waitcnt vmcnt(8)\n\t" \
  "v_mfma_f32_16x16x32_bf16 v[132:135], v[52:55], v[204:207], v[132:135]\n\t" \
  "v_mfma_f32_16x16x32_bf16 v[140:143], v[60:63], v[204:207], v[140:143]\n\t" \
  "buffer_load_dwordx4 v[204:207], v241, %[rt], %[st4] offen offset:1024 sc1\n\t" \
  "s_waitcnt vmcnt(8)\n\t" \
  "v_mfma_f32_16x16x32_bf16 v[128:131], v[64:67], v[176:179], v[128:131]\n\t" \
  "v_mfma_f32_16x16x32_bf16 v[136:139], v[72:75], v[176:179], v[136:139]\n\t" \
  "s_waitcnt vmcnt(7)\n\t" \
  ".Ltc%=:\n\t" \
  "v_cmp_eq_u32 vcc, -1, v166\n\t" \
  "s_cbranch_vccnz .Ltw%=\n\t" \
  ".Lto%=:\n\t" \
  "s_cmp_eq_u32 %[step], 65\n\t" \
  "s_cbranch_scc1 .Ltl%=\n\t" \
  "s_add_u32 %[step], %[step], 1\n\t" \
  "s_sub_i32 s93, %[step], 2\n\t" \
  "s_lshl_b32 %[st], s93, 19\n\t" \
  "s_lshl_b32 s94, s93, 11\n\t" \
  "s_xor_b32 s94, s94, %[s0]\n\t" \
  "s_and_b32 s94, s94, 0x2000\n\t" \
  "s_or_b32 %[st], %[st], s94\n\t" \
  "s_cmp_lt_i32 s93, 0\n\t" \
  "s_cselect_b32 %[st], 0x70000000, %[st]\n\t" \
  "s_add_u32 %[st4], %[st], 4096\n\t" \
  "v_add_u32 v242, 16384, v242\n\t" \
  "s_cmp_eq_u32 %[step], 65\n\t" \
  "s_cbranch_scc0 .Ltn%=\n\t" \
  "v_mov_b32 v242, %[fg]\n\t" \
  ".Ltn%=:\n\t" \
  "buffer_load_dwordx4 v[176:179], v238, %[rt], %[st] offen offset:0 sc1\n\t" \
  "s_waitcnt vmcnt(7)\n\t" \
  "v_mfma_f32_16x16x32_bf16 v[132:135], v[68:71], v[180:183], v[132:135]\n\t" \
  "v_mfma_f32_16x16x32_bf16 v[140:143], v[76:79], v[180:183], v[140:143]\n\t" \
  "buffer_load_dwordx4 v[180:183], v238, %[rt], %[st] offen offset:1024 sc1\n\t" \
  "s_waitcnt vmcnt(7)\n\t" \
  "v_mfma_f32_16x16x32_bf16 v[128:131], v[80:83], v[184:187], v[128:131]\n\t" \
  "v_mfma_f32_16x16x32_bf16 v[136:139], v[88:91], v[184:187], v[136:139]\n\t" \
  "buffer_load_dwordx4 v[184:187], v238, %[rt], %[st4] offen offset:0 sc1\n\t" \
  "s_waitcnt vmcnt(7)\n\t" \
  "v_mfma_f32_16x16x32_bf16 v[132:135], v[84:87], v[188:191], v[132:135]\n\t" \
  "v_mfma_f32_16x16x32_bf16 v[140:143], v[92:95], v[188:191], v[140:143]\n\t" \
  "buffer_load_dwordx4 v[188:191], v238, %[rt], %[st4] offen offset:1024 sc1\n\t" \
  "s_waitcnt vmcnt(7)\n\t" \
  "v_mfma_f32_16x16x32_bf16 v[128:131], v[96:99], v[192:195], v[128:131]\n\t" \
  "v_mfma_f32_16x16x32_bf16 v[136:139], v[104:107], v[192:195], v[136:139]\n\t" \
  "buffer_load_dwordx4 v[192:195], v239, %[rt], %[st] offen offset:0 sc1\n\t" \
  "s_waitcnt vmcnt(7)\n\t" \
  "v_mfma_f32_16x16x32_bf16 v[132:135], v[100:103], v[196:199], v[132:135]\n\t" \
  "v_mfma_f32_16x16x32_bf16 v[140:143], v[108:111], v[196:199], v[140:143]\n\t" \
  "buffer_load_dwordx4 v[196:199], v239, %[rt], %[st] offen offset:1024 sc1\n\t" \
  "s_waitcnt vmcnt(7)\n\t" \
  "v_mfma_f32_16x16x32_bf16 v[128:131], v[112:115], v[200:203], v[128:131]\n\t" \
  "v_mfma_f32_16x16x32_bf16 v[136:139], v[120:123], v[200:203], v[136:139]\n\t" \
  "buffer_load_dwordx4 v[200:203], v239, %[rt], %[st4] offen offset:0 sc1\n\t" \
  "s_waitcnt vmcnt(7)\n\t" \
  "v_mfma_f32_16x16x32_bf16 v[132:135], v[116:119], v[204:207], v[132:135]\n\t" \
  "v_mfma_f32_16x16x32_bf16 v[140:143], v[124:127], v[204:207], v[140:143]\n\t" \
  "buffer_load_dwordx4 v[204:207], v239, %[rt], %[st4] offen offset:1024 sc1\n\t" \
  "s_nop 5\n\t" \
  "v_pk_add_f32 v[128:129], v[128:129], v[132:133]\n\t" \
  "v_pk_add_f32 v[130:131], v[130:131], v[134:135]\n\t" \
  "ds_write_b128 v230, v[128:131] offset:0\n\t" \
  "v_pk_add_f32 v[136:137], v[136:137], v[140:141]\n\t" \
  "v_pk_add_f32 v[138:139], v[138:139], v[142:143]\n\t" \
  "ds_write_b128 v230, v[136:139] offset:1024\n\t" \
  "v_add_u32 v230, 16384, v230\n\t" \
  "s_branch .Lt%=\n\t" \
  ".Ltl%=:\n\t" \
  "buffer_load_dwordx4 v[224:227], v243, %[rg], 0 offen sc1\n\t" \
  "s_waitcnt vmcnt(7)\n\t" \
  "v_mfma_f32_16x16x32_bf16 v[132:135], v[68:71], v[180:183], v[132:135]\n\t" \
  "v_mfma_f32_16x16x32_bf16 v[140:143], v[76:79], v[180:183], v[140:143]\n\t" \
  "s_waitcnt vmcnt(6)\n\t" \
  "v_mfma_f32_16x16x32_bf16 v[128:131], v[80:83], v[184:187], v[128:131]\n\t" \
  "v_mfma_f32_16x16x32_bf16 v[136:139], v[88:91], v[184:187], v[136:139]\n\t" \
  "s_waitcnt vmcnt(5)\n\t" \
  "v_mfma_f32_16x16x32_bf16 v[132:135], v[84:87], v[188:191], v[132:135]\n\t" \
  "v_mfma_f32_16x16x32_bf16 v[140:143], v[92:95], v[188:191], v[140:143]\n\t" \
  "s_waitcnt vmcnt(4)\n\t" \
  "v_mfma_f32_16x16x32_bf16 v[128:131], v[96:99], v[192:195], v[128:131]\n\t" \
  "v_mfma_f32_16x16x32_bf16 v[136:139], v[104:107], v[192:195], v[136:139]\n\t" \
  "s_waitcnt vmcnt(3)\n\t" \
  "v_mfma_f32_16x16x32_bf16 v[132:135], v[100:103], v[196:199], v[132:135]\n\t" \
  "v_mfma_f32_16x16x32_bf16 v[140:143], v[108:111], v[196:199], v[140:143]\n\t" \
  "s_waitcnt vmcnt(2)\n\t" \
  "v_mfma_f32_16x16x32_bf16 v[128:131], v[112:115], v[200:203], v[128:131]\n\t" \
  "v_mfma_f32_16x16x32_bf16 v[136:139], v[120:123], v[200:203], v[136:139]\n\t" \
  "s_waitcnt vmcnt(1)\n\t" \
  "v_mfma_f32_16x16x32_bf16 v[132:135], v[116:119], v[204:207], v[132:135]\n\t" \
  "v_mfma_f32_16x16x32_bf16 v[140:143], v[124:127], v[204:207], v[140:143]\n\t" \
  "s_nop 6\n\t" \
  "v_pk_add_f32 v[128:129], v[128:129], v[132:133]\n\t" \
  "v_pk_add_f32 v[130:131], v[130:131], v[134:135]\n\t" \
  "ds_write_b128 v230, v[128:131] offset:0\n\t" \
  "v_pk_add_f32 v[136:137], v[136:137], v[140:141]\n\t" \
  "v_pk_add_f32 v[138:139], v[138:139], v[142:143]\n\t" \
  "ds_write_b128 v230, v[136:139] offset:1024\n\t" \
  "s_waitcnt vmcnt(0) lgkmcnt(0)\n\t" \
  "s_branch .Lend%=\n\t" \
  ".Ltw%=:\n\t" \
  "s_add_u32 s92, s92, 1\n\t" \
  "s_sleep 2\n\t" \
  "s_and_b32 s93, s92, 63\n\t" \
  "s_cbranch_scc1 .Ltr%=\n\t" \
  "buffer_load_dword v164, off, %[rs], %[sab] sc1\n\t" \
  "s_waitcnt vmcnt(0)\n\t" \
  "v_readfirstlane_b32 s93, v164\n\t" \
  "s_nop 3\n\t" \
  "s_cmp_eq_u32 s93, 1\n\t" \
  "s_cbranch_scc1 .Lto%=\n\t" \
  "s_cmp_lt_u32 s92, 0x80000\n\t" \
  "s_cbranch_scc1 .Ltr%=\n\t" \
  "v_mov_b32 v164, 1\n\t" \
  "buffer_store_dword v164, off, %[rs], %[sab] sc1\n\t" \
  "s_waitcnt vmcnt(0)\n\t" \
  "s_branch .Lto%=\n\t" \
  ".Ltr%=:\n\t" \
  "buffer_load_dword v166, v242, %[rs], 0 offen sc1\n\t" \
  "s_waitcnt vmcnt(0)\n\t" \
  "s_branch .Ltc%=\n\t" \
  ".Llast%=:\n\t" \
  "v_mov_b32 v173, v242\n\t" \
  "s_branch .Lgol%=\n\t" \
  ".Lfw%=:\n\t" \
  "s_add_u32 s92, s92, 1\n\t" \
  "s_sleep 2\n\t" \
  "s_and_b32 s93, s92, 63\n\t" \
  "s_cbranch_scc1 .Lfr%=\n\t" \
  "buffer_load_dword v164, off, %[rs], %[sab] sc1\n\t" \
  "s_waitcnt vmcnt(0)\n\t" \
  "v_readfirstlane_b32 s93, v164\n\t" \
  "s_nop 3\n\t" \
  "s_cmp_eq_u32 s93, 1\n\t" \
  "s_cbranch_scc1 .Lfo%=\n\t" \
  "s_cmp_lt_u32 s92, 0x80000\n\t" \
  "s_cbranch_scc1 .Lfr%=\n\t" \
  "v_mov_b32 v164, 1\n\t" \
  "buffer_store_dword v164, off, %[rs], %[sab] sc1\n\t" \
  "s_waitcnt vmcnt(0)\n\t" \
  "s_branch .Lfo%=\n\t" \
  ".Lfr%=:\n\t" \
  "buffer_load_dwordx2 v[166:167], v173, %[rs], 0 offen sc1\n\t" \
  "s_branch .Lfc%=\n\t" \
  ".Lb1%=:\n\t" \
  "s_waitcnt vmcnt(0) lgkmcnt(0)\n\t" \
  "s_barrier\n\t" \
  "s_branch .Lb2%=\n\t" \
  ".Lbl%=:\n\t" \
  "s_bitset1_b64 %[ps], %[step]\n\t" \
  "s_cmp_lt_u32 s92, 3\n\t" \
  "s_cbranch_scc1 .Lbm%=\n\t" \
  "s_sleep 12\n\t" \
  ".Lbm%=:\n\t" \
  "s_add_u32 s92, s92, 1\n\t" \
  "s_sleep 2\n\t" \
  "s_and_b32 s93, s92, 63\n\t" \
  "s_cbranch_scc1 .Lba%=\n\t" \
  "buffer_load_dword v164, off, %[rs], %[sab] sc1\n\t" \
  "s_waitcnt vmcnt(0)\n\t" \
  "v_readfirstlane_b32 s93, v164\n\t" \
  "s_nop 3\n\t" \
  "s_cmp_eq_u32 s93, 1\n\t" \
  "s_cbranch_scc1 .Lgo%=\n\t" \
  "s_cmp_lt_u32 s92, 0x80000\n\t" \
  "s_cbranch_scc1 .Lba%=\n\t" \
  "v_mov_b32 v164, 1\n\t" \
  "buffer_store_dword v164, off, %[rs], %[sab] sc1\n\t" \
  "s_waitcnt vmcnt(0)\n\t" \
  "s_branch .Lgo%=\n\t" \
  ".Lba%=:\n\t" \
  "buffer_load_dwordx2 v[166:167], v173, %[rs], 0 offen sc1\n\t" \
  "s_branch .Lcal%=\n\t" \
  ".Lslow%=:\n\t" \
  "s_mov_b64 s[94:95], 0\n\t" \
  "v_cmp_eq_u32 vcc, %[sent], v128\n\t" \
  "s_or_b64 s[94:95], s[94:95], vcc\n\t" \
  "v_cmp_eq_u32 vcc, %[sent], v129\n\t" \
  "s_or_b64 s[94:95], s[94:95], vcc\n\t" \
  "v_cmp_eq_u32 vcc, %[sent], v130\n\t" \
  "s_or_b64 s[94:95], s[94:95], vcc\n\t" \
  "v_cmp_eq_u32 vcc, %[sent], v131\n\t" \
  "s_or_b64 s[94:95], s[94:95], vcc\n\t" \
  "v_cmp_eq_u32 vcc, %[sent], v132\n\t" \
  "s_or_b64 s[94:95], s[94:95], vcc\n\t" \
  "v_cmp_eq_u32 vcc, %[sent], v133\n\t" \
  "s_or_b64 s[94:95], s[94:95], vcc\n\t" \
  "v_cmp_eq_u32 vcc, %[sent], v134\n\t" \
  "s_or_b64 s[94:95], s[94:95], vcc\n\t" \
  "v_cmp_eq_u32 vcc, %[sent], v135\n\t" \
  "s_or_b64 s[94:95], s[94:95], vcc\n\t" \
  "v_cmp_eq_u32 vcc, %[sent], v136\n\t" \
  "s_or_b64 s[94:95], s[94:95], vcc\n\t" \
  "v_cmp_eq_u32 vcc, %[sent], v137\n\t" \
  "s_or_b64 s[94:95], s[94:95], vcc\n\t" \
  "v_cmp_eq_u32 vcc, %[sent], v138\n\t" \
  "s_or_b64 s[94:95], s[94:95], vcc\n\t" \
  "v_cmp_eq_u32 vcc, %[sent], v139\n\t" \
  "s_or_b64 s[94:95], s[94:95], vcc\n\t" \
  "v_cmp_eq_u32 vcc, %[sent], v140\n\t" \
  "s_or_b64 s[94:95], s[94:95], vcc\n\t" \
  "v_cmp_eq_u32 vcc, %[sent], v141\n\t" \
  "s_or_b64 s[94:95], s[94:95], vcc\n\t" \
  "v_cmp_eq_u32 vcc, %[sent], v142\n\t" \
  "s_or_b64 s[94:95], s[94:95], vcc\n\t" \
  "v_cmp_eq_u32 vcc, %[sent], v143\n\t" \
  "s_or_b64 s[94:95], s[94:95], vcc\n\t" \
  "s_cbranch_scc0 .Lok%=\n\t" \
  "s_bitset1_b64 %[fm], %[step]\n\t" \
  "s_add_u32 s92, s92, 1\n\t" \
  "s_sleep 1\n\t" \
  "s_and_b32 s93, s92, 63\n\t" \
  "s_cbranch_scc1 .Lre%=\n\t" \
  "buffer_load_dword v164, off, %[rs], %[sab] sc1\n\t" \
  "s_waitcnt vmcnt(0)\n\t" \
  "v_readfirstlane_b32 s93, v164\n\t" \
  "s_nop 3\n\t" \
  "s_cmp_eq_u32 s93, 1\n\t" \
  "s_cbranch_scc1 .Lok%=\n\t" \
  "s_cmp_lt_u32 s92, 0x80000\n\t" \
  "s_cbranch_scc1 .Lre%=\n\t" \
  "v_mov_b32 v164, 1\n\t" \
  "buffer_store_dword v164, off, %[rs], %[sab] sc1\n\t" \
  "s_waitcnt vmcnt(0)\n\t" \
  "s_branch .Lok%=\n\t" \
  ".Lre%=:\n\t" \
  "buffer_load_dwordx4 v[128:131], v234, %[rs], %[so] offen sc1\n\t" \
  "s_nop 15\n\t" \
  "buffer_load_dwordx4 v[132:135], v235, %[rs], %[so] offen sc1\n\t" \
  "s_nop 15\n\t" \
  "buffer_load_dwordx4 v[136:139], v236, %[rs], %[so] offen sc1\n\t" \
  "s_nop 15\n\t" \
  "buffer_load_dwordx4 v[140:143], v237, %[rs], %[so] offen sc1\n\t" \
  "v_mov_b64 v[144:145], 0\n\t" \
  "v_mov_b64 v[146:147], 0\n\t" \
  "v_mov_b64 v[148:149], 0\n\t" \
  "v_mov_b64 v[150:151], 0\n\t" \
  "v_mov_b64 v[152:153], 0\n\t" \
  "v_mov_b64 v[154:155], 0\n\t" \
  "v_mov_b64 v[156:157], 0\n\t" \
  "v_mov_b64 v[158:159], 0\n\t" \
  "s_waitcnt vmcnt(0)\n\t" \
  "s_branch .Lw%=\n\t" \
  ".Lend%=:\n\t"

__device__ __forceinline__ bf16x8 get4(const u32x32& W, int i) {
  return as_bf16x8((u32x4){W[4 * i], W[4 * i + 1], W[4 * i + 2], W[4 * i + 3]});
}
__device__ __forceinline__ int look_prog(__amdgpu_buffer_rsrc_t rs, int off) {
  asm volatile("" ::: "memory");
  const int v = (int)ld_flag(rs, off);
  return min(__builtin_amdgcn_readlane(v, 0), __builtin_amdgcn_readlane(v, 1));
}
__device__ __forceinline__ int wait_prog(__amdgpu_buffer_rsrc_t rs, int off, int need, gu32* abortw) {
  int v;
  for (unsigned spins = 0;;) {
    v = look_prog(rs, off);
    if (v >= need) break;
    __builtin_amdgcn_s_sleep(4);
    if (spin_fail(spins, abortw)) break;
  }
  asm volatile("" ::: "memory");
  return v;
}
#define CLB_S "s90", "s91", "s92", "s93", "s94", "s95", "vcc", "scc", "memory"
#define W_IN "{v[0:31]}"(W[0]), "{v[32:63]}"(W[1]), "{v[64:95]}"(W[2]), "{v[96:127]}"(W[3]), "{v[128:159]}"(W[4]), "{v[160:191]}"(W[5])

#define W_DELAY 0
#define LEAD0 395
#define LEAD_D0 6
#define OX4 8
#define T0_BACK 1000
#define QBJ 3
constexpr int L_MEM = 0, L_KT = 16384, L_QX = 24576, L_KX = 26624, L_VX = 16384, L_MISC = 30720, L_WL = 32768;

#define QK_STEP(TXT)                                                                                                        \
  asm volatile(TXT                                                                                                          \
      : [tout] "=&s"(tout), [spins] "=&s"(spins), [tb1] "=&s"(tb1), [tgt] "=&s"(tgt), "+{v248}"(aL16), "+{v249}"(aMW), "+{v250}"(aWL),                            \
        "+{v251}"(aIDX), "+{v252}"(aQW), "+{v[232:235]}"(QF)                                                                 \
      : W_IN, [rs] "s"(rs4), [som] "s"(o_mem(pm, s) + WV * 1024), [som2] "s"(o_mem(pm, s) + WV * 1024 + 16384), [sent] "s"(DSENT), [sab] "s"(SY_ABORT), \
        [lead] "s"(lead), [step] "s"(s), [sbc] "s"(SY_BA + side(pm, 1) + h * 128), [nh] "s"(0xFF00FF00FF00FF00ull), [up] "s"(0xFFFFFFFF00000000ull), [negc] "s"(-cexp),      \
        [c] "s"(cexp), [so] "s"(o_o(pm, s, h) + WV * 512), [sk] "s"(o_v(pm, s, h) + 2048 + (WV & 3) * 512),                    \
        [sv] "s"(o_v(pm, s, h) + WV * 512), [rt] "s"(rt4), [svt] "s"(t_off(p, T_V + kseg(s, h)) + WV * 2048)                  \
      : CLB8(192, 193, 194, 195, 196, 197, 198, 199), CLB8(200, 201, 202, 203, 204, 205, 206, 207),                          \
        CLB8(208, 209, 210, 211, 212, 213, 214, 215), CLB8(216, 217, 218, 219, 220, 221, 222, 223),                          \
        CLB8(224, 225, 226, 227, 228, 229, 230, 231), CLB8(236, 237, 238, 239, 240, 241, 242, 243), "v244", "v245", "v246", "v247", "v253", "s88", "s89", CLB_S)

template <int WV>
__device__ __forceinline__ void qk_wave(Args& p, int h, char* smem) {
  const int tid = threadIdx.x;
  unsigned pm = 0;
  int have = -1;
  u32x32 W[6];
  {
    int lane = tid & 63;
    asm volatile("" : "+v"(lane));
    const unsigned vo = (WV * 4 + (lane >> 4)) * (HD * 2) + 16 * ((lane & 15) ^ swzB((WV * 4 + (lane >> 4)) & 15));
    auto stage = [&](int j) {
#pragma unroll
      for (int mm = 0; mm < 2; ++mm) {
        const char* src = (const char*)((mm ? p.Wk : p.Wq) + (size_t)j * 128 * HD + h * DH);
        const unsigned dst = lds_off(smem) + (~j & 1) * 65536 + mm * 32768 + WV * 1024;
        glds2(src, 32 * HD * 2, vo, dst);
        if (TH_Q < 63) HOLD_V((h & 3) ^ 1, TH_Q);
        glds2(src + 64 * HD * 2, 32 * HD * 2, vo, dst + 16384);
        if (TH_Q < 63) HOLD_V((h & 3) ^ 1, TH_Q);
      }
    };
    bf16x8 keep[4][4];
    int f0 = -1;
    auto place = [&](int g) {
#pragma unroll
      for (int i = 0; i < 4; ++i)
        *reinterpret_cast<bf16x8*>(smem + (g == 0 ? 65536 : g == 1 ? 32768 : g == 2 ? 131072 : 98304) + WV * 4096 + i * 1024 + lane * 16) = keep[g][i];
    };
    stage(0); stage(1);
    Boot bt;
#pragma unroll
    for (int j = 0; j < 8; ++j) {
      if (j < 7) WAIT_V(8); else WAIT_V(0);
      if (WV == 7 && j == QBJ + 1) pm = boot_done(p.ws, bt);
      if ((WV == 4 || WV == 5) && j == 7) {
        asm volatile("" : "+v"(f0));
        have = min(__builtin_amdgcn_readlane(f0, 0), __builtin_amdgcn_readlane(f0, 1));
      }
      LBAR();
#pragma unroll
      for (int k = 0; k < 8; ++k) {
        const int P = (QK_MAP[WV] >> (4 * (2 * j + (k >> 2)))) & 15;
        const bf16x8 fr = ld_bfrag(smem + (~j & 1) * 65536 + (WV >> 2) * 32768 + (k >> 2) * 16384, k & 1, 2 * (WV & 3) + ((k >> 1) & 1), lane);
        const int f = P * 4 + (k & 3);
        if (P < 12) put4(W[f >> 3], f & 7, fr);
        else keep[P - 12][k & 3] = fr;
      }
      if (j == (WV >> 1)) place(2);
      LBAR();
      if (WV == 7 && j == QBJ) boot_ask(p.ws, bt);
      if (j + 2 < 8) stage(j + 2);
      if (j == 6) {
        if (WV == 4 || WV == 5)
          asm volatile("buffer_load_dword %0, %1, %2, 0 offen sc1" : "=&v"(f0) : "v"(fkq(h, (lane & 1) * 2)), "s"(raw_rsrc(p.ws + WS_SY, SY_TOTAL)) : "memory");
        place(0); place(3);
      }
    }
    place(1);
    if (WV == 7 && lane < 8) lds_at(smem + L_MISC)[lane] = lane == 7 ? pm : (lane == 1 ? now_q() - T0_BACK : 0);
    if (WV == 6) lds_at(smem + 28672)[lane] = ~0u;
    LBAR();
    pm = uni(lds_at(smem + L_MISC)[7]);
    p.s0 = pm >> 31;
    if (WV == 6) RDW(h);
    if (WV == 4 || WV == 5) {
      if (have < 0) have = wait_prog(mkrsrc(p.ws + WS_SY, SY_TOTAL), fkq(h, (lane & 1) * 2), 0, (gu32*)(p.ws + WS_SY + SY_ABORT));
      const char* src = p.ws + WS_T + t_off(p, kseg(0, h)) + (WV - 4) * 4096 + lane * 16;
#pragma unroll
      for (int i = 0; i < 4; ++i) glds16_sc1(src + i * 1024, smem + L_KT + (WV - 4) * 4096 + i * 1024);
    }
  }
  const __amdgpu_buffer_rsrc_t rsy = mkrsrc(p.ws + WS_SY, SY_TOTAL);
  const u32x4 rs4 = raw_rsrc(p.ws + WS_SY, SY_TOTAL);
  const u32x4 rt4 = raw_rsrc(p.ws + WS_T, T_BYTES);
  gu32* abortw = (gu32*)(p.ws + WS_SY + SY_ABORT);
  const float cexp = 0.08838834764831845f * 1.4426950408889634f;
  constexpr int m = WV >> 2, b = WV & 3;
  int lead = LEAD0;
  unsigned tb1 = 0;
  unsigned long long fm = 0, ps = 0; (void)fm; (void)ps;
  for (int s = 0; s < NSEG; ++s) {
    int lane = tid & 63;
    asm volatile("" : "+v"(lane));
    const int r = lane & 15, q = lane >> 4;
    unsigned aL16 = lane * 16, aMW = aL16 + WV * 1024, aWL = aL16 + 98304 + WV * 4096;
    unsigned aIDX = (lane & 2) ? 0xEEEE : 0x4444;
    unsigned aQW = (32 * b + r) * 16 + q * 4 + (m ? L_KX : L_QX);
    u32x4 QF;
    asm volatile("" : "={v[232:235]}"(QF));
    if (WV < 4) {
      QF[0] = L_QX + (q * 8 + (r >> 2)) * 16 + (r & 1) * 8;
      QF[1] = ((2 * (q & 1) + (q >> 1)) * 16 + r) * 4;
      QF[2] = ((r >> 2) & 1) * 65536 + ((r >> 1) & 1) * 64 + (r >> 3) * 32 + (r & 1) * 16 + (q >> 1) * 256 + (q & 1) * OX4;
    } else QF[3] = r * 16 + q * 4;
    unsigned tout, spins, tgt;
    if (WV == 0) QK_STEP(QK_ASM_0); else if (WV == 1) QK_STEP(QK_ASM_1); else if (WV == 2) QK_STEP(QK_ASM_2);
    else if (WV == 3) QK_STEP(QK_ASM_3); else if (WV == 4) QK_STEP(QK_ASM_4); else if (WV == 5) QK_STEP(QK_ASM_5);
    else if (WV == 6) QK_STEP(QK_ASM_6); else QK_STEP(QK_ASM_7);
    tout = __builtin_amdgcn_readfirstlane(tout);
    tb1 = __builtin_amdgcn_readfirstlane(tb1);
    lane = tid & 63;
    asm volatile("" : "+v"(lane));
    if (WV < 4) {
      lds_at(smem + L_MISC + 8)[WV] = s + 1;
      if (WV == 3) lds_at(smem + L_MISC)[6] = time_of(tout);
    }
    if (WV == 6) {
      if (s + 1 < NSEG) watch(rsy, s, lane, now_q() + W_DELAY, (const unsigned*)(smem + L_MISC), abortw, side(pm, 1));
    } else if (WV >= 4 && s + 1 < NSEG) {
      unsigned t;
      do {
        __builtin_amdgcn_s_sleep(1);
        t = lds_at(smem + L_MISC + 8)[lane & 3];
      } while (!__all(t == (unsigned)(s + 1)));
      if (WV <= 5) {
        if (s + 2 < NSEG) {
          if (have < s + 1) have = wait_prog(rsy, fkq(h, (lane & 1) * 2), s + 1, abortw);
          const char* src = p.ws + WS_T + t_off(p, kseg(s + 1, h)) + (WV - 4) * 4096 + lane * 16;
#pragma unroll
          for (int i = 0; i < 4; ++i) glds16_sc1(src + i * 1024, smem + L_KT + (WV - 4) * 4096 + i * 1024);
          if (have < s + 2) have = look_prog(rsy, fkq(h, (lane & 1) * 2));
        }
      } else {
        __builtin_amdgcn_s_sleep(6);
        const int x = (int)(__builtin_amdgcn_readfirstlane(lds_at(smem + L_MISC)[6]) - tb1);
        lead = __builtin_amdgcn_readfirstlane(track(lead, x, LEAD_UP, s < 8 ? LEAD_D0 : LEAD_DN, 60));
        SMP(2, s, x); SMP(3, s, lead);
      }
    }
  }
}

__device__ __forceinline__ void qk_role(Args& p, int h, char* smem) {
  switch (__builtin_amdgcn_readfirstlane(threadIdx.x >> 6)) {
    case 0: qk_wave<0>(p, h, smem); break;
    case 1: qk_wave<1>(p, h, smem); break;
    case 2: qk_wave<2>(p, h, smem); break;
    case 3: qk_wave<3>(p, h, smem); break;
    case 4: qk_wave<4>(p, h, smem); break;
    case 5: qk_wave<5>(p, h, smem); break;
    case 6: qk_wave<6>(p, h, smem); break;
    default: qk_wave<7>(p, h, smem); break;
  }
}

#define K_DUE 160
#define T_SL 2
#define O_DUE 400
#define LEAD_V 0

constexpr int V_VM = 16384, V_T = 32768, V_KI = 131072;
__device__ __forceinline__ void vu_role(Args& p, int h, char* smem) {
  const int tid = threadIdx.x, lane = tid & 63;
  const int w = __builtin_amdgcn_readfirstlane(tid >> 6);
  u32x32 W[4];
  {
    const unsigned vo = (tid >> 4) * (HD * 2) + 16 * ((lane & 15) ^ swzB((tid >> 4) & 15));
    auto stage = [&](int j) {
      const char* src = (const char*)(p.Wv + (size_t)j * 128 * HD + h * DH);
      const unsigned dst = lds_off(smem) + L_WL + (j & 3) * 32768 + w * 1024;
      glds2(src, 32 * HD * 2, vo, dst);
      if (TH_V < 63) HOLD_V((h & 3) ^ 1, TH_V);
      glds2(src + 64 * HD * 2, 32 * HD * 2, vo, dst + 16384);
      if (TH_V < 63) HOLD_V((h & 3) ^ 1, TH_V);
    };
    start_sy(p.ws, p.mem0, h, smem);
    Boot bt;
#pragma unroll
    for (int u = 0; u < 2; ++u) {
#pragma unroll
      for (int j = 0; j < 4; ++j) stage(4 * u + j);
      if (u && w == 7) boot_ask(p.ws, bt);
      WAIT_V(0);
      if (u && w == 7) {
        const unsigned m = boot_done(p.ws, bt);
        if (lane < 8) lds_at(smem + L_MISC)[lane] = lane == 7 ? m : (lane == 1 ? now_q() : 0);
      }
      LBAR();
#pragma unroll
      for (int k = 0; k < 16; ++k) put4(W[2 * u + (k >> 3)], k & 7, ld_bfrag(smem + L_WL + ((k >> 1) ^ w) * 16384, k & 1, w, lane));
      LBAR();
    }
    RDY(32 + h);
  }
  const __amdgpu_buffer_rsrc_t rsy = mkrsrc(p.ws + WS_SY, SY_TOTAL);
  const u32x4 rs4 = raw_rsrc(p.ws + WS_SY, SY_TOTAL);
  gu32* abortw = (gu32*)(p.ws + WS_SY + SY_ABORT);
  const unsigned pm = uni(lds_at(smem + L_MISC)[7]);
  p.s0 = pm >> 31;
  unsigned long long fm = 0; (void)fm;
  int have = -1;
  auto fetch = [&](int sg) {
    if (have < sg) have = wait_prog(rsy, fkq(h, w - 4), sg, abortw);
    const char* src = p.ws + WS_T + t_off(p, (w - 4) * T_Q + kseg(sg, h)) + lane * 16;
#pragma unroll
    for (int i = 0; i < 8; ++i) glds16_sc1(src + i * 1024, smem + V_T + (sg & 3) * 24576 + (w - 4) * 8192 + i * 1024);
  };
  const int fo = SY_FO + ((w & 3) * NH + h) * 128;
  typedef __attribute__((ext_vector_type(8))) float f32x8;
  f32x8 bias;
  typedef __attribute__((ext_vector_type(8))) unsigned u32x8;
  u32x8 ca;
#pragma unroll
  for (int i = 0; i < 8; ++i) ca[i] = lane * 16 + (i ^ w) * 1024;
  u32x4 ta;
  {
    const int r = lane & 15, q = lane >> 4;
#pragma unroll
    for (int j = 0; j < 8; ++j) bias[j] = (j >> 2) * 16 + 4 * q + (j & 3) <= 16 * (w & 1) + r ? INFINITY : -INFINITY;
    ta[0] = V_KI + w * 2048 + lane * 16;
    ta[1] = V_KI + w * 2048 + (q * 8 + (r >> 2)) * 16 + (r & 1) * 8;
    ta[2] = V_VM + (w & 2) * 512 + ((2 * (q & 1) + (q >> 1)) * 16 + r) * 4;
    ta[3] = 0;
  }
#pragma unroll 1
  for (int s = 0; s < NSEG; ++s) {
    unsigned spins, tgt;
    asm volatile(VV_ASM
        : [spins] "=&s"(spins), [tgt] "=&s"(tgt)
        : W4_IN, "{v200}"(lane * 16), "{v[232:239]}"(ca), "{v202}"((lane & 2) ? 0xEEEE : 0x4444), "{v203}"(lane * 4),
          "{v204}"(tid * 4 + V_VM + (s & 1) * 2048), [rs] "s"(rs4), [som] "s"(o_mem(pm, s) + w * 1024), [som2] "s"(o_mem(pm, s) + w * 1024 + 16384), [step] "s"(s),
          [sent] "s"(DSENT), [sab] "s"(SY_ABORT), [sv] "s"(o_v(pm, s, h) + w * 256)
        : CLB32(128), CLB32(160), CLB8(192, 193, 194, 195, 196, 197, 198, 199), CLB_S);
    if (w < 4 || s == NSEG - 1) {
      const int sg = s - (w < 4);
      if (w >= 4) pace_until(now_q() + K_DUE);
      if (sg >= 0) {
        asm volatile(TK_ASM
            :
            : "{v200}"(lane * 16), "{v[208:215]}"(bias), "{v[216:219]}"(ta),
              [rs] "s"(rs4), [rt] "s"(raw_rsrc(p.ws + WS_T, T_BYTES)), [sent] "s"(DSENT), [sab] "s"(SY_ABORT), [sk] "s"(o_v(pm, sg, h) + 2048),
              [tl] "s"(V_T + (sg & 3) * 24576), [vp] "s"((sg & 1) * 2048), [tq] "s"((w & 1) * 4096), [tv] "s"((w & 2) * 2048),
              [so] "s"(t_off_o(p, T_O + (sg * 2 + (w & 1)) * 16 + (h >> 1)) + (h & 1) * 4096 + (w & 2) * 1024),
              [up] "s"(0xFFFFFFFF00000000ull), [c] "s"(1.4426950408889634f), [negc] "s"(-1.4426950408889634f),
              [sc] "s"((unsigned long long)__float_as_uint(0.08838834764831845f)), [od] "s"(s < NSEG - 1 ? O_DUE : 0)
            : CLB32(128), CLB32(160), CLB8(192, 193, 194, 195, 196, 197, 198, 199), CLB8(224, 225, 226, 227, 228, 229, 230, 231),
              "v205", "v206", "v207", "s88", "s89", CLB_S);
        if (sg >= NSEG - 3) {
          static_assert(TLAG == 2, "");
          WAIT_V(0);
          if (lane == 0) __builtin_amdgcn_raw_buffer_store_b32(1u, rsy, fo + (sg - NSEG + 3) * 4 * NH * 128, 0, 16);
        }
      }
    } else if (w == 7) {
      SMP(6, s, (int)(now_q() - tgt));
      watch(rsy, s, lane, now_q() + LEAD_V + W_DELAY, (const unsigned*)(smem + L_MISC), abortw, side(pm, 1));
    } else if (w >= 4) {
      __builtin_amdgcn_s_sleep(T_SL);
      const int s1 = w == 5 ? (s == NSEG - 2 ? s + 1 : s) : s + 1;
#pragma unroll 1
      for (int sg = (w == 5 || s == 0) ? s : s + 1; sg <= s1; ++sg) fetch(sg);
      if (have < s1 + 1 && s1 + 1 < NSEG) have = look_prog(rsy, fkq(h, w - 4));
      if (w == 5 && s + NPRE < NSEG) {
#pragma unroll
        for (int i = 0; i < 4; ++i) st_sc1(rsy, o_v(pm, s + NPRE, h) + i * 1024 + lane * 16, (u32x4){DSENT, DSENT, DSENT, DSENT});
      }
    }
  }
  if (h == 3 && (w & 2) == 0) { LST(351 + (w & 1) + (w >> 2) * 2); }
}

#define RL_UP 6
#define RL_DN 4
#define RED_MIN 40
#define BA_LOOK 360
constexpr int R_SI = 32768, R_PART = 98304;

__device__ __forceinline__ void red_role(Args& p, int j, char* smem) {
  const int tid = threadIdx.x;
  int lane = tid & 63;
  const int w = __builtin_amdgcn_readfirstlane(tid >> 6);
  const int c = (j & 2) * 8 + (j >> 2), rh = j & 1;
  const int hg = (w + (j >> 2)) & 7, rot = (j >> 2) + (j >> 5);
  const __amdgpu_buffer_rsrc_t rsy = mkrsrc(p.ws + WS_SY, SY_TOTAL);
  const u32x4 rs4 = raw_rsrc(p.ws + WS_SY, SY_TOTAL);
  const __amdgpu_buffer_rsrc_t rt = mkrsrc(p.ws + WS_T, T_BYTES);
  gu32* abortw = (gu32*)(p.ws + WS_SY + SY_ABORT);
  u32x32 W[4];
  u32x2 tag = {0u, 0u};
  {
    const u16* Wo = p.Wo + (size_t)(4 * hg * DH) * DM + 32 * c;
    const int ro = (lane >> 2) * 64 + (lane & 3) * 8;
    Boot bt;
    load_blocks<16>(smem + 32768 + w * 16384,
        [&](int i, char* slot) {
          if (i == 12 && w == 0) boot_ask(p.ws, bt);
          if ((i < 2 || i == 4 || i == 8) && (c & 14) != 6 && lane < 10) {
            const int x = i < 2 ? 3 - i : i >> 3, n = min(((c & 15) - ((c & 8) >> 2)) * 10 + lane, 127);
            touch_line((const char*)p.Wo + (size_t)(w * 512 + (x >> 1) * 256 + (x & 1) * 64 + (n & 64) + n) * (DM * 2) + (c & 16) * 64 + 384, smem + w * 256);
          }
          const int x = ((i >> 2) + rot) & 3;
          blk_dma(Wo + (size_t)((x >> 1) * 256 + (x & 1) * 64 + ((i >> 1) & 1) * 128 + (i & 1) * 32) * DM, DM, slot, lane);
          if (TH_R < 63) HOLD_V((c & 14) ^ 6, TH_R);
        },
        [&](int i, char* slot) {
#pragma unroll
          for (int t = 0; t < 2; ++t) {
            s16x4 lo = __builtin_amdgcn_ds_read_tr16_b64_v4i16((lds_s16x4_ptr)(slot + ro + t * 32));
            s16x4 hi = __builtin_amdgcn_ds_read_tr16_b64_v4i16((lds_s16x4_ptr)(slot + ro + t * 32 + 1024));
            put4(W[i >> 2], (i & 2) * 2 + 2 * t + (i & 1), (bf16x8){lo[0], lo[1], lo[2], lo[3], hi[0], hi[1], hi[2], hi[3]});
          }
        });
    if (w == 0) { lds_at(smem + 4128)[0] = boot_done(p.ws, bt); tag = next_tag(bt.e); }
    LBAR();
  }
  const unsigned pm = uni(lds_at(smem + 4128)[0]);
  p.s0 = pm >> 31;
  const int hs = side(pm, 1);
  if ((c & 14) != 6 && GPF == 2) {
#pragma unroll
    for (int i = 0; i < 2; ++i)
      touch_line((const char*)p.wd + (size_t)(j - (j >= 32 ? 8 : 0)) * 131072 + (i * 8 + w) * 8192 + lane * 128, smem + w * 256);
  }
  {
    const u32x4 ds = {DSENT, DSENT, DSENT, DSENT};
#pragma unroll
    for (int i = 0; i < 2; ++i) {
      const int s = (w * 2 + i) * 4 + (lane >> 4);
      if (s >= 2) st_sc1(rsy, o_mem(pm, s) + (c >> 4) * 16384 + (c & 15) * 512 + rh * 128 + ((lane >> 3) & 1) * 256 + (lane & 7) * 16, ds);
    }
  }
  if (tid < 4) { reinterpret_cast<unsigned*>(smem + 4096)[tid] = tid == 3 ? now_q() : 0; reinterpret_cast<unsigned*>(smem + 4112)[tid] = DSENT; }
  if (tid < 256) reinterpret_cast<int*>(smem + 5120)[tid] = ((tid >> 2) - TLAG - 6) >> 2;
  LBAR();
  const unsigned base = __builtin_amdgcn_readfirstlane(lds_at(smem + 4096)[3]);
  RDY(64 + j);
  unsigned long long fm = 0, ps = 0;
  const unsigned aL = lane * 16;
  const int e = ((56 + w) * SEG + rh * 16 + (lane >> 2)) * DM + c * 32 + (lane & 3) * 8;
  u32x4 xv = *reinterpret_cast<const u32x4*>(p.x + e), av = {DSENT, DSENT, DSENT, DSENT};
  asm volatile("" : "+v"(xv));
  {
    const int r = lane & 15, q = lane >> 4;
    const int sm0 = o_mem(pm, 0) + (c >> 4) * 16384 + (c & 15) * 512 + rh * 128;
    u32x4 VO, VT;
    unsigned fo = SY_FO + ((rh + (lane & 4) / 2) * NH + 4 * hg + (lane & 3)) * 128;
    unsigned ao = w == 1 ? (c * S_LEN + rh * 16 - (TLAG + 3) * SEG) * 64 + lane * 16
                         : (w >= 4 ? SY_O + NPRE * 131072 + side(pm, w - 2) + (j >> 3) * 16384 + (j & 7) * 1024 + lane * 16
                                   : SY_FS + (2 * c + rh) * 128 + (lane < 8 ? lane * 16 : 0x70000000));
    unsigned ar = w == 1 ? R_SI - (TLAG + 2) * 1024 + lane * 16 : (w == 2 ? 5120 : 4112);
    const unsigned long long am = w == 1 ? ((1ull << 56) - 1) << (TLAG + 3) : (w == 2 ? 0x11111111111111ull << (TLAG + 7) : (w >= 4 ? (0xFFFFull << (16 * (w - 4))) >> NPRE : 0));
    unsigned pw = R_PART + w * 2048 + lane * 16, pr = R_PART + 16384 + tid * 4;
    unsigned sw = R_SI - (TLAG + 1) * 1024 + (lane >> 2) * 64 + w * 8 + (lane & 3) * 2, stm = 8192 + w * 512;
#pragma unroll
    for (int l = 0; l < 4; ++l) {
      const int x = (l + rot) & 3;
      VO[l] = o_o(0, 0, 4 * hg) + rh * 65536 + x * 1024 + aL;
      VT[l] = (T_O + rh * 16 + 2 * hg + (x >> 1)) * 16384 + (x & 1) * 2048 + aL;
    }
    unsigned ba = lane < 4 ? SY_BA + hs + (4 * hg + lane) * 128 : 0x7F000000u;
    int rl = RL0;
    unsigned step, E, so, st, st4, sh;
    asm volatile(RED_ASM
        : [step] "=&s"(step), [E] "=&s"(E), [so] "=&s"(so), [st] "=&s"(st), [st4] "=&s"(st4), [sh] "=&s"(sh),
          [rl] "+s"(rl), [fm] "+s"(fm), [ps] "+s"(ps),
          "+{v[224:227]}"(av), "+{v228}"(ao), "+{v229}"(ar), "+{v230}"(pw), "+{v231}"(pr), "+{v232}"(sw), "+{v233}"(stm), "+{v242}"(fo), "+{v173}"(ba)
        : W4_IN, "{v168}"(aL), "{v169}"(w * 512 + lane * 8), "{v170}"(lane * 8), "{v174}"((r & 1) ? 0x03020706u : 0x05040100u),
          "{v171}"(sm0 + 32768 + (q >> 1) * 256 + (q & 1) * 64 + ((r >> 1) & 1) * 32 + (r & 1) * 16 + (r >> 2) * 4),
          "{v172}"((lane & 2) ? 0xEEEE : 0x4444),
          "{v175}"(w == 3 && lane < 32 ? SY_BR + hs + j * 128 + lane * 4 : 0x7F000000u), "{v[234:237]}"(VO), "{v[238:241]}"(VT), "{v243}"(e * 2),
          [rg] "s"(raw_rsrc(p.ws + WS_G, S_LEN * DM * 2)), [fg] "s"(SY_FG + ((14 + (w >> 2)) * 8 + (c >> 2)) * 128),
          [rs] "s"(rs4), [ra] "s"(w == 1 ? raw_rsrc(p.ws + WS_S, S_LEN * DM * 2) : rs4), [rt] "s"(raw_rsrc(p.ws + WS_T, T_BYTES)),
          [ds] "n"(DS_RED), [mn] "n"(RED_MIN), [lk] "n"(DS_MEM + BA_LOOK), [up] "n"(RL_UP), [dn] "n"(-RL_DN), [sent] "s"(DSENT), [sab] "s"(SY_ABORT),
          [base] "s"(base), [pm] "s"(pm), [s0] "s"((p.s0 ^ TF_O) << 13), [am] "s"(am), [ainc] "s"(w == 1 ? SEG * 64 : (w >= 4 ? 131072 : 0)),
          [rinc] "s"(w == 1 ? 1024 : (w == 2 ? 16 : 0))
        : CLB32(128), CLB8(160, 161, 162, 163, 164, 165, 166, 167), CLB8(176, 177, 178, 179, 180, 181, 182, 183),
          CLB8(184, 185, 186, 187, 188, 189, 190, 191), CLB8(192, 193, 194, 195, 196, 197, 198, 199), CLB8(200, 201, 202, 203, 204, 205, 206, 207),
          CLB8(208, 209, 210, 211, 212, 213, 214, 215), CLB8(216, 217, 218, 219, 220, 221, 222, 223), CLB_E, "s96", CLB_S);
  }
  if (j == 0 && w == 5) { LST(345); }
  LBAR();
  if (j == 0 && w == 5) { LST(346); }
  u32x4 sv;
  if (w < 6 - TLAG) sv = *reinterpret_cast<const u32x4*>(smem + R_SI + (56 + w) * 1024 + lane * 16);
  else {
    const f32x4* part = reinterpret_cast<const f32x4*>(smem + (w == 6 - TLAG ? R_PART : (w - 6 + TLAG) * 16384)) +
                        (lane & 2) * 32 + (lane & 1) * 32 + (lane >> 2);
    f32x4 x[8][2];
#pragma unroll
    for (int ww = 0; ww < 8; ++ww) { x[ww][0] = part[ww * 128]; x[ww][1] = part[ww * 128 + 16]; }
#pragma unroll
    for (int u = 0; u < 2; ++u) {
      const f32x4 a = ((x[0][u] + x[1][u]) + (x[2][u] + x[3][u])) + ((x[4][u] + x[5][u]) + (x[6][u] + x[7][u]));
      sv[2 * u] = cvtpk(a[0], a[1]); sv[2 * u + 1] = cvtpk(a[2], a[3]);
    }
  }
  st_out(p, e, combine(sv, av, xv));
  if (j == 0 && tid == 0) st2_sc1(rsy, SY_EP, tag);
  if (tid == 128) { LST(400 + j); }
}

__global__ __launch_bounds__(512, 2) void main_kernel(Args p) {
  char* smem = g_smem;
  p.ws = ws_align(p.ws);
  const int bx = blockIdx.x & 7, bk = blockIdx.x >> 3;
  const bool isg = (bx ^ XPROBE) & 4;
  const int b = (isg ? NCHAIN : 0) + bk * 4 + (bx & 3);
  if (b == 64 && threadIdx.x == 0) { LST(500); }
  if (threadIdx.x < 64) {
    unsigned long long pc; unsigned len, keep;
    asm volatile("s_getpc_b64 %0\n.Lpc%=:\n\ts_mov_b32 %1, .Lcode_end-.Lpc%=" : "=s"(pc), "=s"(len));
    asm volatile("s_mov_b32 %0, m0\n\ts_mov_b32 m0, %3\n\ts_nop 0\n\tbuffer_load_dword %1, %2, 0 offen lds\n\ts_mov_b32 m0, %0"
                 : "=&s"(keep) : "v"((bk * 40 + threadIdx.x) * 128), "s"(raw_rsrc((const void*)pc, len)), "s"((b >> 6) == 1 ? 2048 : 163328) : "memory");
  }
  if (b >= NCHAIN) gemm_role(p, b - NCHAIN, smem);
  else if (b < 32) qk_role(p, (b & 3) * 8 + bk, smem);
  else if (b < 64) vu_role(p, (b & 3) * 8 + bk - 8, smem);
  else red_role(p, b - 64, smem);
}

extern "C" void kernel_launch(void* const* d_in, const int* in_sizes, int n_in,
                              void* d_out, int out_size, void* d_ws, size_t ws_size,
                              hipStream_t stream) {
  Args a;
  a.x = (const u16*)d_in[0]; a.mem0 = (const u16*)d_in[1];
  a.Wq = (const u16*)d_in[2]; a.Wk = (const u16*)d_in[3]; a.Wv = (const u16*)d_in[4]; a.Wo = (const u16*)d_in[5];
  a.wg = (const u16*)d_in[8]; a.wu = (const u16*)d_in[9]; a.wd = (const u16*)d_in[10];
  a.ws = (char*)d_ws; a.out = (u16*)d_out; a.s0 = 0; a.pad1 = 0;
  main_kernel<<<256, 512, 0, stream>>>(a);
}
```

```cpp
#include <hip/hip_runtime.h>
#include <hip/hip_bf16.h>
#include <stdint.h>
#include <type_traits>

constexpr int S_LEN = 2048, DM = 1024, NH = 32, DH = 128, NMEM = 8, SEG = 32;
constexpr int NSEG = S_LEN / SEG;
constexpr int HD = NH * DH;
constexpr int FF = 3584;

typedef unsigned short u16;
typedef __attribute__((ext_vector_type(8))) short bf16x8;
typedef __attribute__((ext_vector_type(4))) short s16x4;
typedef __attribute__((ext_vector_type(4))) float f32x4;
typedef __attribute__((ext_vector_type(4))) unsigned int u32x4;
typedef __attribute__((ext_vector_type(2))) unsigned int u32x2;
typedef __attribute__((address_space(3))) s16x4* lds_s16x4_ptr;
typedef __attribute__((address_space(3))) void* lds_void_ptr;
typedef __attribute__((address_space(1))) const void* glb_cvoid_ptr;
typedef __attribute__((address_space(1))) unsigned gu32;
typedef __bf16 bf16x2_t __attribute__((ext_vector_type(2)));
typedef float f32x2_t __attribute__((ext_vector_type(2)));

__device__ __forceinline__ float bf2f(u16 v) { return __uint_as_float(((unsigned)v) << 16); }
__device__ __forceinline__ unsigned cvtpk(float lo, float hi) {
  bf16x2_t r = __builtin_convertvector((f32x2_t){lo, hi}, bf16x2_t);
  return __builtin_bit_cast(unsigned, r);
}
__device__ __forceinline__ float lo_f(unsigned v) { return __uint_as_float(v << 16); }
__device__ __forceinline__ float hi_f(unsigned v) { return __uint_as_float(v & 0xffff0000u); }
__device__ __forceinline__ float rbf(float f) { return lo_f(cvtpk(f, f)); }
__device__ __forceinline__ bf16x8 as_bf16x8(u32x4 v) { return __builtin_bit_cast(bf16x8, v); }
#define MFMA(a, b, c) __builtin_amdgcn_mfma_f32_16x16x32_bf16((a), (b), (c), 0, 0, 0)
typedef __attribute__((ext_vector_type(16))) short bf16x16;
typedef __attribute__((ext_vector_type(8))) __bf16 bfv8;
typedef __attribute__((ext_vector_type(16))) __bf16 bfv16;
#define SMFMA(a, b, c, idx) __builtin_amdgcn_smfmac_f32_16x16x64_bf16(__builtin_bit_cast(bfv8, (a)), __builtin_bit_cast(bfv16, (b)), (c), (idx), 0, 0)
__device__ __forceinline__ bf16x16 cat16(bf16x8 lo, bf16x8 hi) {
  return __builtin_shufflevector(lo, hi, 0, 1, 2, 3, 4, 5, 6, 7, 8, 9, 10, 11, 12, 13, 14, 15);
}

#define KSTR 65
#define XLD 1024
constexpr int PAGE = 2 << 20, NPAGE = 14;
constexpr int T_K = 0, T_Q = 2304, T_V = 4608, T_O = 6912, T_END = T_O + 2048;
constexpr unsigned T_BYTES = T_END * 16384u;
constexpr size_t WS_SY = 0;
constexpr size_t WS_T = (size_t)NPAGE * PAGE;
constexpr size_t WS_XN = WS_T + T_BYTES;
constexpr size_t WS_HF = WS_XN + (size_t)S_LEN * XLD * 2;
constexpr size_t WS_G = WS_HF + (size_t)S_LEN * 16384;
constexpr size_t WS_S = WS_G + (size_t)S_LEN * DM * 2;
constexpr size_t WS_END = WS_S + (size_t)S_LEN * DM * 2;
constexpr unsigned SENT = 0xFFFFFFFFu;
constexpr unsigned DSENT = 0xFFBFFFBFu;
constexpr int SY_MEM = 0;
constexpr int SY_O = 2 * PAGE;
constexpr int SY_V = 6 * PAGE;
constexpr int SY_BR = PAGE, SY_BA = SY_BR + 16384;
constexpr int SY_FKQ = SY_BR + 32768;
constexpr int SY_FG = SY_FKQ + NH * 4 * 128;
constexpr int SY_FO = SY_FG + 16 * 8 * 128;
constexpr int SY_CH = SY_FO + 384 * 128;
constexpr int SY_ABORT = SY_CH + 8 * 128;
constexpr int SY_FS = SY_ABORT + 256;
constexpr int SY_FEND = SY_FS + 64 * 128;
constexpr int SY_EP = SY_FEND, SY_BT = SY_EP + 128;
constexpr int SY_TOTAL = NPAGE * PAGE;
constexpr int PROBE = PAGE - 16384 + 6144;
__device__ __forceinline__ int side(unsigned pm, int page) { return ((pm >> page) & 1u) << 13; }
__device__ __forceinline__ int o_mem(unsigned pm, int s) { return SY_MEM + s * 32768 + side(pm, 0); }
__device__ __forceinline__ int o_o(unsigned pm, int s, int h) {
  return SY_O + s * 131072 + (h >> 3) * 16384 + ((h >> 1) & 3) * 2048 + (h & 1) * 128 + side(pm, 2 + (s >> 4));
}
__device__ __forceinline__ int o_v(unsigned pm, int s, int h) { return SY_V + s * 262144 + (h >> 1) * 16384 + (h & 1) * 4096 + side(pm, 6 + (s >> 3)); }
__device__ __forceinline__ char* ws_align(char* ws) { return ws + ((PAGE - (unsigned)(uintptr_t)ws) & (PAGE - 1)); }
#define NFUSE 12
constexpr int NPRE = 4;
static_assert(WS_END + PAGE <= (size_t)256 << 20 && NH * KSTR <= T_Q && NPAGE % 2 == 0, "");
constexpr int NCHAIN = 128, NGW = 32;
__host__ __device__ constexpr int kseg(int sg, int h) { return h * KSTR + sg; }
__host__ __device__ constexpr int fkq(int h, int kind) { return SY_FKQ + (h * 4 + kind) * 128; }

#define XPROBE 0
struct Args {
  const u16 *x, *mem0, *Wq, *Wk, *Wv, *Wo, *wg, *wu, *wd;
  char* ws;
  u16* out;
  int s0, pad1;
};
__device__ __forceinline__ int h_off(const Args& p, int rblk) { return rblk * PAGE + (((p.s0 ^ rblk ^ 1) & 1) << 13); }
static_assert(WS_HF % (2 * PAGE) == 0, "");
#define TF_KQV 1
#define TF_O 0
__device__ __forceinline__ int t_off(const Args& p, int t) { return t * 16384 + (((p.s0 ^ (t >> 7) ^ (TF_KQV == 2 ? t : TF_KQV)) & 1) << 13); }
__device__ __forceinline__ int t_off_o(const Args& p, int t) { return t * 16384 + (((p.s0 ^ (t >> 7) ^ (TF_O == 2 ? t : TF_O)) & 1) << 13); }

__device__ __forceinline__ float wave_sum(float v) {
#pragma unroll
  for (int i = 0; i < 4; ++i) {
    const int x = (int)__float_as_uint(v);
    v += __uint_as_float(i == 0 ? __builtin_amdgcn_update_dpp(0, x, 0x128, 0xf, 0xf, false) : i == 1 ? __builtin_amdgcn_update_dpp(0, x, 0x124, 0xf, 0xf, false)
                         : i == 2 ? __builtin_amdgcn_update_dpp(0, x, 0x122, 0xf, 0xf, false) : __builtin_amdgcn_update_dpp(0, x, 0x121, 0xf, 0xf, false));
  }
  auto r = __builtin_amdgcn_permlane16_swap(__float_as_uint(v), __float_as_uint(v), false, false);
  v = __uint_as_float(r[0]) + __uint_as_float(r[1]);
  r = __builtin_amdgcn_permlane32_swap(__float_as_uint(v), __float_as_uint(v), false, false);
  return __uint_as_float(r[0]) + __uint_as_float(r[1]);
}

__shared__ __attribute__((aligned(16))) char g_smem[163840];
__device__ __forceinline__ unsigned lds_off(const void* p) {
  return (unsigned)(uintptr_t)(lds_void_ptr)g_smem + (unsigned)((const char*)p - g_smem);
}
__device__ __forceinline__ __amdgpu_buffer_rsrc_t mkrsrc(const void* p, unsigned bytes) {
  return __builtin_amdgcn_make_buffer_rsrc((void*)p, 0, bytes, 0x00020000);
}
__device__ __forceinline__ u32x4 ld_sc1(__amdgpu_buffer_rsrc_t rs, int off) {
  return __builtin_amdgcn_raw_buffer_load_b128(rs, off, 0, 16);
}
__device__ __forceinline__ void st_sc1(__amdgpu_buffer_rsrc_t rs, int off, u32x4 v) {
  __builtin_amdgcn_raw_buffer_store_b128(v, rs, off, 0, 16);
}
__device__ __forceinline__ unsigned ror8(unsigned v) {
  return (unsigned)__builtin_amdgcn_update_dpp(0, (int)v, 0x128, 0xf, 0xf, false);
}
__device__ __forceinline__ float ror8f(float v) { return __uint_as_float(ror8(__float_as_uint(v))); }
__device__ __forceinline__ void swap16(unsigned& x, unsigned& y) {
  auto r = __builtin_amdgcn_permlane16_swap(x, y, false, false);
  x = r[0]; y = r[1];
}
__device__ __forceinline__ void swap32(unsigned& x, unsigned& y) {
  auto r = __builtin_amdgcn_permlane32_swap(x, y, false, false);
  x = r[0]; y = r[1];
}
__device__ __forceinline__ f32x4 fold32(f32x4 x, f32x4 y) {
  f32x4 o;
#pragma unroll
  for (int j = 0; j < 4; ++j) {
    unsigned a = __float_as_uint(x[j]), b = __float_as_uint(y[j]);
    swap32(a, b);
    o[j] = __uint_as_float(a) + __uint_as_float(b);
  }
  return o;
}
__device__ __forceinline__ bf16x8 tr_frag(const char* img_lane) {
  s16x4 lo = __builtin_amdgcn_ds_read_tr16_b64_v4i16((lds_s16x4_ptr)img_lane);
  s16x4 hi = __builtin_amdgcn_ds_read_tr16_b64_v4i16((lds_s16x4_ptr)(img_lane + 64));
  return (bf16x8){lo[0], lo[1], lo[2], lo[3], hi[0], hi[1], hi[2], hi[3]};
}
__device__ __forceinline__ float rowq_max(float v) {
  unsigned a = __float_as_uint(v), b = a;
  swap16(a, b);
  v = __builtin_elementwise_maximum(__uint_as_float(a), __uint_as_float(b));
  a = __float_as_uint(v); b = a;
  swap32(a, b);
  return __builtin_elementwise_maximum(__uint_as_float(a), __uint_as_float(b));
}
__device__ __forceinline__ float rowq_sum(float v) {
  unsigned a = __float_as_uint(v), b = a;
  swap16(a, b);
  v = __uint_as_float(a) + __uint_as_float(b);
  a = __float_as_uint(v); b = a;
  swap32(a, b);
  return __uint_as_float(a) + __uint_as_float(b);
}
__device__ __forceinline__ bool spin_fail(unsigned& spins, gu32* abortw) {
  ++spins;
  if ((spins & 63u) == 0u) {
    const __amdgpu_buffer_rsrc_t ra = mkrsrc((const void*)abortw, 4);
    unsigned z = 0;
    asm volatile("" : "+v"(z));
    if (__builtin_amdgcn_readfirstlane(__builtin_amdgcn_raw_buffer_load_b32(ra, z, 0, 16)) == 1u) return true;
    if (spins > (1u << 19)) {
      __builtin_amdgcn_raw_buffer_store_b32(z + 1u, ra, z, 0, 16);
      return true;
    }
  }
  return false;
}
__device__ __forceinline__ unsigned umax4(u32x4 v) { return max(max(v[0], v[1]), max(v[2], v[3])); }
__device__ __forceinline__ bool has_ds(u32x4 v) { return (v[0] == DSENT) | (v[1] == DSENT) | (v[2] == DSENT) | (v[3] == DSENT); }
__device__ __forceinline__ float fmax2(float a, float b) { return __builtin_elementwise_maximum(a, b); }
__device__ __forceinline__ bf16x8 ldsf(const char* p) { return *reinterpret_cast<const bf16x8*>(p); }
#define SCHED() __builtin_amdgcn_sched_barrier(0)
#define LBAR() do { asm volatile("s_waitcnt lgkmcnt(0)" ::: "memory"); __builtin_amdgcn_s_barrier(); asm volatile("" ::: "memory"); } while (0)
#define WAIT_V(n) asm volatile("s_waitcnt vmcnt(%0)" :: "n"(n) : "memory")

#define HOLD_V(c, n) asm volatile("s_cmp_eq_u32 %0, 0\n\ts_cbranch_scc1 .Lh%=\n\ts_waitcnt vmcnt(%1)\n.Lh%=:" :: "s"((int)(c)), "n"(n) : "memory", "scc")
#define TH_Q 2
#define TH_V 63
#define TH_R 63
#define TH_P 63
__device__ __forceinline__ void wait_flags(__amdgpu_buffer_rsrc_t rs, int off, gu32* abortw) {
  for (unsigned spins = 0;;) {
    asm volatile("" ::: "memory");
    const unsigned v = __builtin_amdgcn_raw_buffer_load_b32(rs, off, 0, 16);
    if (__all(v != SENT)) break;
    __builtin_amdgcn_s_sleep(4);
    if (spin_fail(spins, abortw)) break;
  }
  asm volatile("" ::: "memory");
}

#define LST(i) do {} while (0)
#define RDY(i) do {} while (0)
#define RDW(i) do {} while (0)
#define SMP(k, s, v) do {} while (0)
#define LSTC(i) do {} while (0)
__device__ __forceinline__ unsigned offA(unsigned row, unsigned ch) {
  return row * 128u + 16u * (ch ^ (row & 7u));
}
__device__ __forceinline__ unsigned swzB(unsigned row) { return ((row & 3u) << 2) | ((row >> 2) & 3u); }
__device__ __forceinline__ unsigned offB(unsigned row, unsigned ch) {
  return 256u * row + 16u * (ch ^ swzB(row));
}
__device__ __forceinline__ void glds16(const u16* src, char* dst) {
  const unsigned d = __builtin_amdgcn_readfirstlane(lds_off(dst));
  unsigned keep;
  asm volatile("s_mov_b32 %0, m0\n\ts_mov_b32 m0, %2\n\ts_nop 0\n\tglobal_load_lds_dwordx4 %1, off\n\ts_mov_b32 m0, %0"
               : "=&s"(keep) : "v"(src), "s"(d) : "memory");
}
__device__ __forceinline__ bf16x8 ld_bfrag(const char* sb, int ks, int c, int lane) {
  const unsigned g = lane >> 4, qq = (lane & 15) >> 2, pp = lane & 3;
  s16x4 lo = __builtin_amdgcn_ds_read_tr16_b64_v4i16(
      (lds_s16x4_ptr)(sb + offB(32 * ks + 8 * g + qq, 2 * c + (pp >> 1)) + 8 * (pp & 1)));
  s16x4 hi = __builtin_amdgcn_ds_read_tr16_b64_v4i16(
      (lds_s16x4_ptr)(sb + offB(32 * ks + 8 * g + 4 + qq, 2 * c + (pp >> 1)) + 8 * (pp & 1)));
  return (bf16x8){lo[0], lo[1], lo[2], lo[3], hi[0], hi[1], hi[2], hi[3]};
}

__device__ __forceinline__ void blk_dma(const u16* src, size_t ld, char* slot, int lane) {
  const u16* s = src + (size_t)(lane >> 2) * ld + (lane & 3) * 8;
  glds16(s, slot);
  glds16(s + 16 * ld, slot + 1024);
}
__device__ __forceinline__ bf16x8 blk_frag(const char* slot, int T, int lane) {
  const char* a = slot + ((lane >> 4) * 8 + ((lane & 15) >> 2)) * 64 + (lane & 3) * 16 + T * 8;
  s16x4 lo = __builtin_amdgcn_ds_read_tr16_b64_v4i16((lds_s16x4_ptr)a);
  s16x4 hi = __builtin_amdgcn_ds_read_tr16_b64_v4i16((lds_s16x4_ptr)(a + 256));
  return (bf16x8){lo[0], lo[1], lo[2], lo[3], hi[0], hi[1], hi[2], hi[3]};
}
template <int NB, typename ISSUE, typename SINK>
__device__ __forceinline__ void load_blocks(char* slice, ISSUE&& issue, SINK&& sink) {
#pragma unroll
  for (int i = 0; i < 8; ++i) issue(i, slice + i * 2048);
#pragma unroll
  for (int g = 0; g < NB / 4; ++g) {
    if (g + 1 < NB / 4) WAIT_V(8); else WAIT_V(0);
#pragma unroll
    for (int j = 0; j < 4; ++j) sink(4 * g + j, slice + ((4 * g + j) & 7) * 2048);
    asm volatile("s_waitcnt lgkmcnt(0)" ::: "memory");
    if (g + 2 < NB / 4) {
#pragma unroll
      for (int j = 0; j < 4; ++j) issue(4 * g + 8 + j, slice + ((4 * g + j) & 7) * 2048);
    }
  }
}

__device__ __forceinline__ u32x4 widen32(unsigned a0, unsigned a1, unsigned b0, unsigned b1) {
  swap32(a0, b0); swap16(a0, b0);
  swap32(a1, b1); swap16(a1, b1);
  return (u32x4){a0, a1, b0, b1};
}

__device__ __forceinline__ void publish_flags(__amdgpu_buffer_rsrc_t rs, int off, int stride, int n) {
  WAIT_V(0);
  LBAR();
  int tid = threadIdx.x;
  asm volatile("" : "+v"(tid));
  if (tid < n) __builtin_amdgcn_raw_buffer_store_b32(1u, rs, off + tid * stride, 0, 16);
}

__device__ __forceinline__ void wait_s(const Args& p, __amdgpu_buffer_rsrc_t rsy, int rblk, int c0, int n, bool need) {
  if (need) {
    if (threadIdx.x < 64) {
      for (unsigned spins = 0;;) {
        asm volatile("" ::: "memory");
        const int f = (int)__builtin_amdgcn_raw_buffer_load_b32(rsy, SY_FS + (2 * c0 + (threadIdx.x & (2 * n - 1))) * 128, 0, 16);
        if (__all(f >= rblk)) break;
        __builtin_amdgcn_s_sleep(32);
        if (spin_fail(spins, (gu32*)(p.ws + WS_SY + SY_ABORT))) break;
      }
    }
    LBAR();
  }
}
__device__ __forceinline__ u32x4 combine(u32x4 sv, u32x4 g, u32x4 x) {
  u32x4 o;
#pragma unroll
  for (int j = 0; j < 4; ++j) {
    const unsigned t = cvtpk(lo_f(sv[j]) * lo_f(g[j]), hi_f(sv[j]) * hi_f(g[j]));
    o[j] = cvtpk(lo_f(x[j]) + lo_f(t), hi_f(x[j]) + hi_f(t));
  }
  return o;
}
#define OUT_AUX 0
__device__ __forceinline__ void st_out(const Args& p, int e, u32x4 v) {
  __builtin_amdgcn_raw_buffer_store_b128(v, mkrsrc(p.out, S_LEN * DM * 2), e * 2, 0, OUT_AUX);
}
struct SX { u32x4 s, x; };
__device__ __forceinline__ SX get_sx(const Args& p, int rblk, int e) {
  SX r; r.s = (u32x4){0u, 0u, 0u, 0u}; r.x = r.s;
  if (rblk < NFUSE) {
    r.s = ld_sc1(mkrsrc(p.ws + WS_S, S_LEN * DM * 2), (((e & 1023) >> 5) * S_LEN + (e >> 10)) * 64 + (e & 31) * 2);
    r.x = *reinterpret_cast<const u32x4*>(p.x + e);
  }
  return r;
}
__device__ __forceinline__ void put_g(const Args& p, __amdgpu_buffer_rsrc_t rg, int rblk, int e, u32x4 g, const SX& v) {
  if (rblk < NFUSE) {
    st_out(p, e, combine(v.s, g, v.x));
  } else st_sc1(rg, e * 2, g);
}

__device__ __forceinline__ void job_comb(const Args& p, int rblk, int ct, __amdgpu_buffer_rsrc_t rsy) {
  int tid = threadIdx.x;
  asm volatile("" : "+v"(tid));
  u32x4 xv[8];
#pragma unroll
  for (int i = 0; i < 8; ++i) xv[i] = *reinterpret_cast<const u32x4*>(p.x + (rblk * 128 + i * 16 + (tid >> 5)) * DM + ct * 256 + (tid & 31) * 8);
  if (tid < 64) wait_flags(rsy, SY_FG + (rblk * 8 + 2 * ct + (tid & 1)) * 128, (gu32*)(p.ws + WS_SY + SY_ABORT));
  wait_s(p, rsy, rblk, ct * 8, 8, true);
  u32x4 sv[8], gv[8];
#pragma unroll
  for (int i = 0; i < 8; ++i) {
    const int e = (rblk * 128 + i * 16 + (tid >> 5)) * DM + ct * 256 + (tid & 31) * 8;
    sv[i] = ld_sc1(mkrsrc(p.ws + WS_S, S_LEN * DM * 2), (((e & 1023) >> 5) * S_LEN + (e >> 10)) * 64 + (e & 31) * 2);
    gv[i] = ld_sc1(mkrsrc(p.ws + WS_G, S_LEN * DM * 2), e * 2);
  }
#pragma unroll
  for (int i = 0; i < 8; ++i)
    st_out(p, (rblk * 128 + i * 16 + (tid >> 5)) * DM + ct * 256 + (tid & 31) * 8, combine(sv[i], gv[i], xv[i]));
}

__device__ __forceinline__ u32x4 raw_rsrc(const void* p, unsigned bytes) {
  const unsigned long long a = (unsigned long long)p;
  return (u32x4){(unsigned)a, (unsigned)(a >> 32) & 0xffffu, bytes, 0x00020000u};
}
__device__ __forceinline__ u32x2 next_tag(u32x2 e) { return (u32x2){e[0] + 1u, e[1] + (e[0] == 0xFFFFFFFFu)}; }
struct Boot { u32x2 e; u32x4 c; };
__device__ __forceinline__ void boot_ask(const char* ws, Boot& r) {
  const unsigned o = (threadIdx.x & 63) * 128;
  asm volatile("buffer_load_dwordx2 %0, off, %3, %4 sc1\n\tbuffer_load_dwordx4 %1, %2, %3, %5 offen sc1"
               : "=&v"(r.e), "=&v"(r.c) : "v"(o), "s"(raw_rsrc(ws, SY_TOTAL)), "s"(SY_EP), "s"(SY_BT) : "memory");
}
__device__ __forceinline__ void wait_rows(const char* ws, u32x2 tag) {
  const __amdgpu_buffer_rsrc_t rs = mkrsrc(ws, SY_TOTAL);
  int o = (threadIdx.x & 63) * 128;
  asm volatile("" : "+v"(o));
#pragma unroll 1
  for (int i = 0; i < (1 << 17); ++i) {
    asm volatile("" ::: "memory");
    const u32x2 a = __builtin_amdgcn_raw_buffer_load_b64(rs, o, SY_BT + 32 * 128, 16), b = __builtin_amdgcn_raw_buffer_load_b64(rs, o, SY_BT + 96 * 128, 16);
    if (__all(a[0] == tag[0] && a[1] == tag[1] && b[0] == tag[0] && b[1] == tag[1])) break;
    __builtin_amdgcn_s_sleep(8);
  }
  asm volatile("" ::: "memory");
}
__device__ __forceinline__ unsigned boot_done(const char* ws, Boot& r, bool own = false, u32x2 tag = (u32x2){0u, 0u}) {
#pragma unroll 1
  for (int i = 0; i < (1 << 17); ++i) {
    asm volatile("" : "+v"(r.e), "+v"(r.c));
    if (!own) tag = next_tag(r.e);
    if (__all(r.c[0] == tag[0] && r.c[1] == tag[1])) break;
    __builtin_amdgcn_s_sleep(8);
    boot_ask(ws, r);
    WAIT_V(0);
  }
  unsigned q0[NPAGE], q1[NPAGE], ga = 0xFFFFu, gb = 0xFFFFu;
#pragma unroll
  for (int i = 0; i < NPAGE; ++i) {
    const unsigned x = __builtin_amdgcn_readlane(r.c[2], i), y = __builtin_amdgcn_readlane(r.c[2], i + NPAGE);
    q0[i] = min(x >> 16, y >> 16); q1[i] = min(x & 0xFFFFu, y & 0xFFFFu);
    ga = min(ga, (i & 1) ? q1[i] : q0[i]); gb = min(gb, (i & 1) ? q0[i] : q1[i]);
  }
  unsigned m = gb < ga ? 0x80001555u : 0x2AAAu;
  const unsigned thr = min(ga, gb) + 110;
#pragma unroll
  for (int i = 0; i < NPAGE; ++i) {
    const unsigned lo = min(q0[i], q1[i]), hi = max(q0[i], q1[i]);
    const unsigned keep = (lo < thr ? hi : 0u) > lo + 100 ? ~(1u << i) : ~0u;
    m = (m & keep) | ((q1[i] < q0[i] ? ~0u : 0u) & ~keep);
  }
  return m;
}
typedef __attribute__((address_space(3))) volatile unsigned* lds_vu_ptr;
__device__ __forceinline__ lds_vu_ptr lds_at(const void* p) {
  unsigned a = lds_off(p);
  asm volatile("" : "+v"(a));
  return (lds_vu_ptr)(uintptr_t)a;
}
__device__ __forceinline__ unsigned uni(unsigned v) {
  unsigned r;
  asm volatile("s_nop 1\n\tv_readfirstlane_b32 %0, %1\n\ts_nop 3" : "=s"(r) : "v"(v));
  return r;
}
__device__ __forceinline__ void start_sy(char* ws, const u16* mem0, int i, char* smem) {
  const int tid = threadIdx.x, lane = tid & 63, w = __builtin_amdgcn_readfirstlane(tid >> 6);
  const __amdgpu_buffer_rsrc_t rsy = mkrsrc(ws, SY_TOTAL);
  const __amdgpu_buffer_rsrc_t rs = mkrsrc(ws + (size_t)(i % NPAGE) * PAGE + PROBE - (i / NPAGE) * 2048, 16384);
  const u32x2 e = __builtin_amdgcn_raw_buffer_load_b64(rsy, 0, SY_EP, 16);
  const u32x4 ds = {DSENT, DSENT, DSENT, DSENT};
  if (lane < 32) st_sc1(rs, (lane >> 4) * 8192 + w * 256 + (lane & 15) * 16, ds);
  static_assert(NPRE == 4 && SY_FEND - SY_BR <= 32 * 6144, "");
#pragma unroll
  for (int u = 0; u < 6; ++u)
    st_sc1(rsy, (u < 4 ? SY_V + (i * 4 + u) * 8192 : SY_O + (i * 2 + u - 4) * 8192) + tid * 16, ds);
  if (w == 7) {
    const int n = i * 64 + lane, j = (n >> 10) * 512 + (n & 511);
    const unsigned* m0 = reinterpret_cast<const unsigned*>(mem0 + (2 * ((j >> 2) & 3) + (j & 1)) * DM + (j >> 4) * 16) + ((j >> 1) & 1);
    u32x4 m;
#pragma unroll
    for (int k = 0; k < 4; ++k) { const unsigned t = m0[2 * k]; m[k] = t == DSENT ? 0xFFFFFFFFu : t; }
    st_sc1(rsy, n * 16, m);
  } else if (w == 6) st_sc1(rsy, 32768 + i * 1024 + lane * 16, ds);
  else {
    const int o = SY_BR + i * 6144 + tid * 16;
    st_sc1(rsy, o < SY_FEND ? o : 0x7F000000, (u32x4){SENT, SENT, SENT, SENT});
  }
  unsigned t[4];
#pragma unroll
  for (int k = 0; k < 4; ++k) {
    unsigned long long c0, c1; unsigned v, off = ((k ^ (k >> 1)) & 1) * 8192 + w * 256 + (k >> 1) * 128;
    asm volatile("s_waitcnt vmcnt(0)\n\ts_barrier\n\ts_memtime %0\n\ts_waitcnt lgkmcnt(0)\n\tbuffer_load_dword %2, %3, %4, 0 offen sc1\n\ts_waitcnt vmcnt(0)\n\t"
                 "s_memtime %1\n\ts_waitcnt lgkmcnt(0)" : "=&s"(c0), "=&s"(c1), "=&v"(v) : "v"(off), "s"(rs) : "memory");
    t[k] = (unsigned)(c1 - c0);
  }
  lds_vu_ptr tm = lds_at(smem);
  if (lane == 0) { tm[w] = min(t[0], t[3]); tm[8 + w] = min(t[1], t[2]); }
  int ro = SY_BT + i * 128 + lane * 16;
  asm volatile("" : "+v"(ro));
  LBAR();
  RDY(384 + i);
  if (threadIdx.x < 8) {
    unsigned m0 = tm[0], m1 = tm[8];
#pragma unroll
    for (int k = 1; k < 8; ++k) { m0 = min(m0, tm[k]); m1 = min(m1, tm[8 + k]); }
    const u32x2 tag = next_tag(e);
    st_sc1(rsy, ro, (u32x4){tag[0], tag[1], min(m0, 30000u) << 16 | min(m1, 30000u), 0u});
  }
}
template <int NB, typename F>
__device__ __forceinline__ u32x2 start_rows(const Args& p, int g, unsigned on, F&& between) {
  const int tid = threadIdx.x, lane = tid & 63, row0 = g * 16 + (tid >> 6) * 2;
  u32x4 a[4];
  u32x2 e;
  asm volatile("buffer_load_dwordx4 %0, %5, %8, 0 offen\n\tbuffer_load_dwordx4 %1, %5, %8, 0 offen offset:1024\n\t"
               "buffer_load_dwordx4 %2, %5, %8, 0 offen offset:2048\n\tbuffer_load_dwordx4 %3, %5, %8, 0 offen offset:3072\n\t"
               "buffer_load_dwordx2 %4, off, %6, %7 sc1"
               : "=&v"(a[0]), "=&v"(a[1]), "=&v"(a[2]), "=&v"(a[3]), "=&v"(e)
               : "v"(row0 * (DM * 2) + lane * 16), "s"(raw_rsrc(p.ws, SY_TOTAL)), "s"(SY_EP), "s"(raw_rsrc(p.x, on * (S_LEN * DM * 2))) : "memory");
  between();
  asm volatile("s_waitcnt vmcnt(%5)" : "+v"(a[0]), "+v"(a[1]), "+v"(a[2]), "+v"(a[3]), "+v"(e) : "n"(NB) : "memory");
  const __amdgpu_buffer_rsrc_t ro = mkrsrc(p.ws + WS_XN, on * (S_LEN * XLD * 2));
#pragma unroll
  for (int u = 0; u < 2; ++u) {
    float v[16];
#pragma unroll
    for (int i = 0; i < 8; ++i) { v[2 * i] = lo_f(a[2 * u + (i >> 2)][i & 3]); v[2 * i + 1] = hi_f(a[2 * u + (i >> 2)][i & 3]); }
    float s = 0.f;
#pragma unroll
    for (int i = 0; i < 16; ++i) s += v[i];
    const float mean = wave_sum(s) * (1.0f / DM);
    float q = 0.f;
#pragma unroll
    for (int i = 0; i < 16; ++i) { float d = v[i] - mean; q += d * d; }
    const float var = rbf(wave_sum(q) * (1.0f / DM));
    const float mb = rbf(mean);
    const float inv = 1.0f / rbf(sqrtf(rbf(var + 1e-5f)));
#pragma unroll
    for (int k = 0; k < 2; ++k) {
      u32x4 o;
#pragma unroll
      for (int i = 0; i < 4; ++i) {
        const unsigned t = cvtpk(v[8 * k + 2 * i] - mb, v[8 * k + 2 * i + 1] - mb);
        o[i] = cvtpk(lo_f(t) * inv, hi_f(t) * inv);
      }
      st_sc1(ro, (row0 + u) * (XLD * 2) + k * 1024 + lane * 16, o);
    }
  }
  return next_tag(e);
}
__device__ __forceinline__ void report_rows(const Args& p, int g, u32x2 tag) {
  RDY(256 + g);
  if (threadIdx.x < 8) st_sc1(mkrsrc(p.ws + WS_SY, SY_TOTAL), SY_BT + (32 + g) * 128 + threadIdx.x * 16, (u32x4){tag[0], tag[1], tag[0], tag[1]});
}

typedef __attribute__((ext_vector_type(32))) unsigned u32x32;
__device__ __forceinline__ void put4(u32x32& W, int i, bf16x8 f) {
  const u32x4 v = __builtin_bit_cast(u32x4, f);
#pragma unroll
  for (int j = 0; j < 4; ++j) W[4 * i + j] = v[j];
}
#define CLB8(a, b, c, d, e, f, g, h) "v" #a, "v" #b, "v" #c, "v" #d, "v" #e, "v" #f, "v" #g, "v" #h
#define CLB32(a) CLB32_##a
#define CLB32_128 CLB8(128, 129, 130, 131, 132, 133, 134, 135), CLB8(136, 137, 138, 139, 140, 141, 142, 143), CLB8(144, 145, 146, 147, 148, 149, 150, 151), CLB8(152, 153, 154, 155, 156, 157, 158, 159)
#define CLB32_160 CLB8(160, 161, 162, 163, 164, 165, 166, 167), CLB8(168, 169, 170, 171, 172, 173, 174, 175), CLB8(176, 177, 178, 179, 180, 181, 182, 183), CLB8(184, 185, 186, 187, 188, 189, 190, 191)
#define W4_IN "{v[0:31]}"(W[0]), "{v[32:63]}"(W[1]), "{v[64:95]}"(W[2]), "{v[96:127]}"(W[3])
#define CLB32_192 CLB8(192, 193, 194, 195, 196, 197, 198, 199), CLB8(200, 201, 202, 203, 204, 205, 206, 207), CLB8(208, 209, 210, 211, 212, 213, 214, 215), CLB8(216, 217, 218, 219, 220, 221, 222, 223)
__device__ __forceinline__ unsigned ld_flag0(__amdgpu_buffer_rsrc_t rs, int off) {
  return __builtin_amdgcn_raw_buffer_load_b32(rs, off, 0, 16);
}
__device__ __forceinline__ void glds16_sc1(const char* src, char* dst) {
  const unsigned d = __builtin_amdgcn_readfirstlane(lds_off(dst));
  unsigned keep;
  asm volatile("s_mov_b32 %0, m0\n\ts_mov_b32 m0, %2\n\ts_nop 0\n\tglobal_load_lds_dwordx4 %1, off sc1\n\ts_mov_b32 m0, %0"
               : "=&s"(keep) : "v"(src), "s"(d) : "memory");
}

__device__ __forceinline__ void touch_line(const char* src, char* dst) {
  const unsigned d = __builtin_amdgcn_readfirstlane(lds_off(dst));
  unsigned keep;
  asm volatile("s_mov_b32 %0, m0\n\ts_mov_b32 m0, %2\n\ts_nop 0\n\tglobal_load_lds_dword %1, off sc1\n\ts_mov_b32 m0, %0"
               : "=&s"(keep) : "v"(src), "s"(d) : "memory");
}

#define GPF 1
#define GU_DELAY 0
#define Q_DELAY 0
#define PF_T0 100
#define PF_N 12
#define A_AUX " sc1"
#define LN_M 3
#define LN_X 1
#define PR_G1 8
#define WREV 1
template <bool DEV = false>
__device__ __forceinline__ void glds2(const char* base, int step, unsigned voff, unsigned dst) {
  unsigned keep;
  const char* base2 = base + step;
  if (DEV)
    asm volatile("s_mov_b32 %0, m0\n\ts_mov_b32 m0, %4\n\ts_nop 0\n\tglobal_load_lds_dwordx4 %1, %2 sc1\n\t"
                 "s_add_u32 m0, m0, 0x2000\n\ts_nop 0\n\tglobal_load_lds_dwordx4 %1, %3 sc1\n\ts_mov_b32 m0, %0"
                 : "=&s"(keep) : "v"(voff), "s"(base), "s"(base2), "s"(dst) : "memory", "scc");
  else
  asm volatile("s_mov_b32 %0, m0\n\ts_mov_b32 m0, %4\n\ts_nop 0\n\tglobal_load_lds_dwordx4 %1, %2\n\t"
               "s_add_u32 m0, m0, 0x2000\n\ts_nop 0\n\tglobal_load_lds_dwordx4 %1, %3\n\ts_mov_b32 m0, %0"
               : "=&s"(keep) : "v"(voff), "s"(base), "s"(base2), "s"(dst) : "memory", "scc");
}

#define DN_ASM \
  "s_mov_b32 s94, m0\n\t" \
  "s_mov_b64 s[84:85], %[pa]\n\t" \
  "s_mov_b64 s[86:87], %[pb]\n\t" \
  "s_movk_i32 s88, 0x4000\n\t" \
  "s_mov_b32 s89, 0x10000\n\t" \
  "s_movk_i32 s90, 13\n\t" \
  "v_mov_b64 v[0:1], 0\n\t" \
  "v_mov_b64 v[2:3], 0\n\t" \
  "v_mov_b64 v[4:5], 0\n\t" \
  "v_mov_b64 v[6:7], 0\n\t" \
  "v_mov_b64 v[8:9], 0\n\t" \
  "v_mov_b64 v[10:11], 0\n\t" \
  "v_mov_b64 v[12:13], 0\n\t" \
  "v_mov_b64 v[14:15], 0\n\t" \
  "v_mov_b64 v[16:17], 0\n\t" \
  "v_mov_b64 v[18:19], 0\n\t" \
  "v_mov_b64 v[20:21], 0\n\t" \
  "v_mov_b64 v[22:23], 0\n\t" \
  "v_mov_b64 v[24:25], 0\n\t" \
  "v_mov_b64 v[26:27], 0\n\t" \
  "v_mov_b64 v[28:29], 0\n\t" \
  "v_mov_b64 v[30:31], 0\n\t" \
  "s_cmp_lg_u32 %[hf], 0\n\t" \
  "s_cbranch_scc1 .Lk%=\n\t" \
  "ds_read_b128 v[32:35], v80 offset:0\n\t" \
  "ds_read_b128 v[36:39], v80 offset:1024\n\t" \
  "ds_read_b128 v[40:43], v80 offset:2048\n\t" \
  "ds_read_b128 v[44:47], v80 offset:3072\n\t" \
  "ds_read_b64_tr_b16 v[64:65], v82 offset:0\n\t" \
  "ds_read_b64_tr_b16 v[66:67], v83 offset:0\n\t" \
  "ds_read_b128 v[48:51], v80 offset:8192\n\t" \
  "ds_read_b64_tr_b16 v[68:69], v84 offset:0\n\t" \
  "ds_read_b64_tr_b16 v[70:71], v85 offset:0\n\t" \
  "ds_read_b128 v[52:55], v80 offset:9216\n\t" \
  "ds_read_b128 v[56:59], v80 offset:10240\n\t" \
  "ds_read_b128 v[60:63], v80 offset:11264\n\t" \
  ".Lp0%=:\n\t" \
  "s_waitcnt vmcnt(4)\n\t" \
  "s_barrier\n\t" \
  "s_waitcnt lgkmcnt(3)\n\t" \
  "v_mfma_f32_16x16x32_bf16 v[0:3], v[64:67], v[32:35], v[0:3]\n\t" \
  "ds_read_b64_tr_b16 v[72:73], v82 offset:8192\n\t" \
  "v_mfma_f32_16x16x32_bf16 v[4:7], v[68:71], v[32:35], v[4:7]\n\t" \
  "ds_read_b64_tr_b16 v[74:75], v83 offset:8192\n\t" \
  "ds_read_b128 v[32:35], v80 offset:32768\n\t" \
  "s_add_u32 m0, %[lw], 98304\n\t" \
  "v_mfma_f32_16x16x32_bf16 v[8:11], v[64:67], v[36:39], v[8:11]\n\t" \
  "ds_read_b64_tr_b16 v[76:77], v84 offset:8192\n\t" \
  "global_load_lds_dwordx4 v90, s[84:85] sc1\n\t" \
  "s_add_u32 s84, s84, s88\n\t" \
  "s_addc_u32 s85, s85, 0\n\t" \
  "v_mfma_f32_16x16x32_bf16 v[12:15], v[68:71], v[36:39], v[12:15]\n\t" \
  "ds_read_b64_tr_b16 v[78:79], v85 offset:8192\n\t" \
  "ds_read_b128 v[36:39], v80 offset:33792\n\t" \
  "v_mfma_f32_16x16x32_bf16 v[16:19], v[64:67], v[40:43], v[16:19]\n\t" \
  "v_mfma_f32_16x16x32_bf16 v[20:23], v[68:71], v[40:43], v[20:23]\n\t" \
  "ds_read_b128 v[40:43], v80 offset:34816\n\t" \
  "s_add_u32 m0, %[lw], 114688\n\t" \
  "v_mfma_f32_16x16x32_bf16 v[24:27], v[64:67], v[44:47], v[24:27]\n\t" \
  "global_load_lds_dwordx4 v91, s[86:87]\n\t" \
  "s_add_u32 s86, s86, s89\n\t" \
  "s_addc_u32 s87, s87, 0\n\t" \
  "v_mfma_f32_16x16x32_bf16 v[28:31], v[68:71], v[44:47], v[28:31]\n\t" \
  "ds_read_b128 v[44:47], v80 offset:35840\n\t" \
  "s_waitcnt lgkmcnt(3)\n\t" \
  "v_mfma_f32_16x16x32_bf16 v[0:3], v[72:75], v[48:51], v[0:3]\n\t" \
  "ds_read_b64_tr_b16 v[64:65], v82 offset:32768\n\t" \
  "v_mfma_f32_16x16x32_bf16 v[4:7], v[76:79], v[48:51], v[4:7]\n\t" \
  "ds_read_b64_tr_b16 v[66:67], v83 offset:32768\n\t" \
  "ds_read_b128 v[48:51], v80 offset:40960\n\t" \
  "s_add_u32 m0, %[lw], 106496\n\t" \
  "v_mfma_f32_16x16x32_bf16 v[8:11], v[72:75], v[52:55], v[8:11]\n\t" \
  "ds_read_b64_tr_b16 v[68:69], v84 offset:32768\n\t" \
  "global_load_lds_dwordx4 v90, s[84:85] sc1\n\t" \
  "s_add_u32 s84, s84, s88\n\t" \
  "s_addc_u32 s85, s85, 0\n\t" \
  "v_mfma_f32_16x16x32_bf16 v[12:15], v[76:79], v[52:55], v[12:15]\n\t" \
  "ds_read_b64_tr_b16 v[70:71], v85 offset:32768\n\t" \
  "ds_read_b128 v[52:55], v80 offset:41984\n\t" \
  "v_mfma_f32_16x16x32_bf16 v[16:19], v[72:75], v[56:59], v[16:19]\n\t" \
  "v_mfma_f32_16x16x32_bf16 v[20:23], v[76:79], v[56:59], v[20:23]\n\t" \
  "ds_read_b128 v[56:59], v80 offset:43008\n\t" \
  "s_add_u32 m0, %[lw], 122880\n\t" \
  "v_mfma_f32_16x16x32_bf16 v[24:27], v[72:75], v[60:63], v[24:27]\n\t" \
  "global_load_lds_dwordx4 v91, s[86:87]\n\t" \
  "s_add_u32 s86, s86, s89\n\t" \
  "s_addc_u32 s87, s87, 0\n\t" \
  "v_mfma_f32_16x16x32_bf16 v[28:31], v[76:79], v[60:63], v[28:31]\n\t" \
  "ds_read_b128 v[60:63], v80 offset:44032\n\t" \
  "s_sub_u32 s90, s90, 1\n\t" \
  "s_cselect_b32 s88, 0, s88\n\t" \
  "s_cselect_b32 s89, 0, s89\n\t" \
  "s_cselect_b32 s91, 16384, 0\n\t" \
  "s_sub_u32 s84, s84, s91\n\t" \
  "s_subb_u32 s85, s85, 0\n\t" \
  "s_lshl_b32 s91, s91, 2\n\t" \
  "s_sub_u32 s86, s86, s91\n\t" \
  "s_subb_u32 s87, s87, 0\n\t" \
  "s_waitcnt vmcnt(4)\n\t" \
  "s_barrier\n\t" \
  "s_waitcnt lgkmcnt(3)\n\t" \
  "v_mfma_f32_16x16x32_bf16 v[0:3], v[64:67], v[32:35], v[0:3]\n\t" \
  "ds_read_b64_tr_b16 v[72:73], v82 offset:40960\n\t" \
  "v_mfma_f32_16x16x32_bf16 v[4:7], v[68:71], v[32:35], v[4:7]\n\t" \
  "ds_read_b64_tr_b16 v[74:75], v83 offset:40960\n\t" \
  "ds_read_b128 v[32:35], v81 offset:0\n\t" \
  "s_add_u32 m0, %[lw], 0\n\t" \
  "v_mfma_f32_16x16x32_bf16 v[8:11], v[64:67], v[36:39], v[8:11]\n\t" \
  "ds_read_b64_tr_b16 v[76:77], v84 offset:40960\n\t" \
  "global_load_lds_dwordx4 v90, s[84:85] sc1\n\t" \
  "s_add_u32 s84, s84, s88\n\t" \
  "s_addc_u32 s85, s85, 0\n\t" \
  "v_mfma_f32_16x16x32_bf16 v[12:15], v[68:71], v[36:39], v[12:15]\n\t" \
  "ds_read_b64_tr_b16 v[78:79], v85 offset:40960\n\t" \
  "ds_read_b128 v[36:39], v81 offset:1024\n\t" \
  "v_mfma_f32_16x16x32_bf16 v[16:19], v[64:67], v[40:43], v[16:19]\n\t" \
  "v_mfma_f32_16x16x32_bf16 v[20:23], v[68:71], v[40:43], v[20:23]\n\t" \
  "ds_read_b128 v[40:43], v81 offset:2048\n\t" \
  "s_add_u32 m0, %[lw], 16384\n\t" \
  "v_mfma_f32_16x16x32_bf16 v[24:27], v[64:67], v[44:47], v[24:27]\n\t" \
  "global_load_lds_dwordx4 v91, s[86:87]\n\t" \
  "s_add_u32 s86, s86, s89\n\t" \
  "s_addc_u32 s87, s87, 0\n\t" \
  "v_mfma_f32_16x16x32_bf16 v[28:31], v[68:71], v[44:47], v[28:31]\n\t" \
  "ds_read_b128 v[44:47], v81 offset:3072\n\t" \
  "s_waitcnt lgkmcnt(3)\n\t" \
  "v_mfma_f32_16x16x32_bf16 v[0:3], v[72:75], v[48:51], v[0:3]\n\t" \
  "ds_read_b64_tr_b16 v[64:65], v86 offset:0\n\t" \
  "v_mfma_f32_16x16x32_bf16 v[4:7], v[76:79], v[48:51], v[4:7]\n\t" \
  "ds_read_b64_tr_b16 v[66:67], v87 offset:0\n\t" \
  "ds_read_b128 v[48:51], v81 offset:8192\n\t" \
  "s_add_u32 m0, %[lw], 8192\n\t" \
  "v_mfma_f32_16x16x32_bf16 v[8:11], v[72:75], v[52:55], v[8:11]\n\t" \
  "ds_read_b64_tr_b16 v[68:69], v88 offset:0\n\t" \
  "global_load_lds_dwordx4 v90, s[84:85] sc1\n\t" \
  "s_add_u32 s84, s84, s88\n\t" \
  "s_addc_u32 s85, s85, 0\n\t" \
  "v_mfma_f32_16x16x32_bf16 v[12:15], v[76:79], v[52:55], v[12:15]\n\t" \
  "ds_read_b64_tr_b16 v[70:71], v89 offset:0\n\t" \
  "ds_read_b128 v[52:55], v81 offset:9216\n\t" \
  "v_mfma_f32_16x16x32_bf16 v[16:19], v[72:75], v[56:59], v[16:19]\n\t" \
  "v_mfma_f32_16x16x32_bf16 v[20:23], v[76:79], v[56:59], v[20:23]\n\t" \
  "ds_read_b128 v[56:59], v81 offset:10240\n\t" \
  "s_add_u32 m0, %[lw], 24576\n\t" \
  "v_mfma_f32_16x16x32_bf16 v[24:27], v[72:75], v[60:63], v[24:27]\n\t" \
  "global_load_lds_dwordx4 v91, s[86:87]\n\t" \
  "s_add_u32 s86, s86, s89\n\t" \
  "s_addc_u32 s87, s87, 0\n\t" \
  "v_mfma_f32_16x16x32_bf16 v[28:31], v[76:79], v[60:63], v[28:31]\n\t" \
  "ds_read_b128 v[60:63], v81 offset:11264\n\t" \
  "s_waitcnt vmcnt(4)\n\t" \
  "s_barrier\n\t" \
  "s_waitcnt lgkmcnt(3)\n\t" \
  "v_mfma_f32_16x16x32_bf16 v[0:3], v[64:67], v[32:35], v[0:3]\n\t" \
  "ds_read_b64_tr_b16 v[72:73], v86 offset:8192\n\t" \
  "v_mfma_f32_16x16x32_bf16 v[4:7], v[68:71], v[32:35], v[4:7]\n\t" \
  "ds_read_b64_tr_b16 v[74:75], v87 offset:8192\n\t" \
  "ds_read_b128 v[32:35], v81 offset:32768\n\t" \
  "s_add_u32 m0, %[lw], 32768\n\t" \
  "v_mfma_f32_16x16x32_bf16 v[8:11], v[64:67], v[36:39], v[8:11]\n\t" \
  "ds_read_b64_tr_b16 v[76:77], v88 offset:8192\n\t" \
  "global_load_lds_dwordx4 v90, s[84:85] sc1\n\t" \
  "s_add_u32 s84, s84, s88\n\t" \
  "s_addc_u32 s85, s85, 0\n\t" \
  "v_mfma_f32_16x16x32_bf16 v[12:15], v[68:71], v[36:39], v[12:15]\n\t" \
  "ds_read_b64_tr_b16 v[78:79], v89 offset:8192\n\t" \
  "ds_read_b128 v[36:39], v81 offset:33792\n\t" \
  "v_mfma_f32_16x16x32_bf16 v[16:19], v[64:67], v[40:43], v[16:19]\n\t" \
  "v_mfma_f32_16x16x32_bf16 v[20:23], v[68:71], v[40:43], v[20:23]\n\t" \
  "ds_read_b128 v[40:43], v81 offset:34816\n\t" \
  "s_add_u32 m0, %[lw], 49152\n\t" \
  "v_mfma_f32_16x16x32_bf16 v[24:27], v[64:67], v[44:47], v[24:27]\n\t" \
  "global_load_lds_dwordx4 v91, s[86:87]\n\t" \
  "s_add_u32 s86, s86, s89\n\t" \
  "s_addc_u32 s87, s87, 0\n\t" \
  "v_mfma_f32_16x16x32_bf16 v[28:31], v[68:71], v[44:47], v[28:31]\n\t" \
  "ds_read_b128 v[44:47], v81 offset:35840\n\t" \
  "s_waitcnt lgkmcnt(3)\n\t" \
  "v_mfma_f32_16x16x32_bf16 v[0:3], v[72:75], v[48:51], v[0:3]\n\t" \
  "ds_read_b64_tr_b16 v[64:65], v86 offset:32768\n\t" \
  "v_mfma_f32_16x16x32_bf16 v[4:7], v[76:79], v[48:51], v[4:7]\n\t" \
  "ds_read_b64_tr_b16 v[66:67], v87 offset:32768\n\t" \
  "ds_read_b128 v[48:51], v81 offset:40960\n\t" \
  "s_add_u32 m0, %[lw], 40960\n\t" \
  "v_mfma_f32_16x16x32_bf16 v[8:11], v[72:75], v[52:55], v[8:11]\n\t" \
  "ds_read_b64_tr_b16 v[68:69], v88 offset:32768\n\t" \
  "global_load_lds_dwordx4 v90, s[84:85] sc1\n\t" \
  "s_add_u32 s84, s84, s88\n\t" \
  "s_addc_u32 s85, s85, 0\n\t" \
  "v_mfma_f32_16x16x32_bf16 v[12:15], v[76:79], v[52:55], v[12:15]\n\t" \
  "ds_read_b64_tr_b16 v[70:71], v89 offset:32768\n\t" \
  "ds_read_b128 v[52:55], v81 offset:41984\n\t" \
  "v_mfma_f32_16x16x32_bf16 v[16:19], v[72:75], v[56:59], v[16:19]\n\t" \
  "v_mfma_f32_16x16x32_bf16 v[20:23], v[76:79], v[56:59], v[20:23]\n\t" \
  "ds_read_b128 v[56:59], v81 offset:43008\n\t" \
  "s_add_u32 m0, %[lw], 57344\n\t" \
  "v_mfma_f32_16x16x32_bf16 v[24:27], v[72:75], v[60:63], v[24:27]\n\t" \
  "global_load_lds_dwordx4 v91, s[86:87]\n\t" \
  "s_add_u32 s86, s86, s89\n\t" \
  "s_addc_u32 s87, s87, 0\n\t" \
  "v_mfma_f32_16x16x32_bf16 v[28:31], v[76:79], v[60:63], v[28:31]\n\t" \
  "ds_read_b128 v[60:63], v81 offset:44032\n\t" \
  "s_waitcnt vmcnt(4)\n\t" \
  "s_barrier\n\t" \
  "s_waitcnt lgkmcnt(3)\n\t" \
  "v_mfma_f32_16x16x32_bf16 v[0:3], v[64:67], v[32:35], v[0:3]\n\t" \
  "ds_read_b64_tr_b16 v[72:73], v86 offset:40960\n\t" \
  "v_mfma_f32_16x16x32_bf16 v[4:7], v[68:71], v[32:35], v[4:7]\n\t" \
  "ds_read_b64_tr_b16 v[74:75], v87 offset:40960\n\t" \
  "ds_read_b128 v[32:35], v80 offset:0\n\t" \
  "s_add_u32 m0, %[lw], 65536\n\t" \
  "v_mfma_f32_16x16x32_bf16 v[8:11], v[64:67], v[36:39], v[8:11]\n\t" \
  "ds_read_b64_tr_b16 v[76:77], v88 offset:40960\n\t" \
  "global_load_lds_dwordx4 v90, s[84:85] sc1\n\t" \
  "s_add_u32 s84, s84, s88\n\t" \
  "s_addc_u32 s85, s85, 0\n\t" \
  "v_mfma_f32_16x16x32_bf16 v[12:15], v[68:71], v[36:39], v[12:15]\n\t" \
  "ds_read_b64_tr_b16 v[78:79], v89 offset:40960\n\t" \
  "ds_read_b128 v[36:39], v80 offset:1024\n\t" \
  "v_mfma_f32_16x16x32_bf16 v[16:19], v[64:67], v[40:43], v[16:19]\n\t" \
  "v_mfma_f32_16x16x32_bf16 v[20:23], v[68:71], v[40:43], v[20:23]\n\t" \
  "ds_read_b128 v[40:43], v80 offset:2048\n\t" \
  "s_add_u32 m0, %[lw], 81920\n\t" \
  "v_mfma_f32_16x16x32_bf16 v[24:27], v[64:67], v[44:47], v[24:27]\n\t" \
  "global_load_lds_dwordx4 v91, s[86:87]\n\t" \
  "s_add_u32 s86, s86, s89\n\t" \
  "s_addc_u32 s87, s87, 0\n\t" \
  "v_mfma_f32_16x16x32_bf16 v[28:31], v[68:71], v[44:47], v[28:31]\n\t" \
  "ds_read_b128 v[44:47], v80 offset:3072\n\t" \
  "s_waitcnt lgkmcnt(3)\n\t" \
  "v_mfma_f32_16x16x32_bf16 v[0:3], v[72:75], v[48:51], v[0:3]\n\t" \
  "ds_read_b64_tr_b16 v[64:65], v82 offset:0\n\t" \
  "v_mfma_f32_16x16x32_bf16 v[4:7], v[76:79], v[48:51], v[4:7]\n\t" \
  "ds_read_b64_tr_b16 v[66:67], v83 offset:0\n\t" \
  "ds_read_b128 v[48:51], v80 offset:8192\n\t" \
  "s_add_u32 m0, %[lw], 73728\n\t" \
  "v_mfma_f32_16x16x32_bf16 v[8:11], v[72:75], v[52:55], v[8:11]\n\t" \
  "ds_read_b64_tr_b16 v[68:69], v84 offset:0\n\t" \
  "global_load_lds_dwordx4 v90, s[84:85] sc1\n\t" \
  "s_add_u32 s84, s84, s88\n\t" \
  "s_addc_u32 s85, s85, 0\n\t" \
  "v_mfma_f32_16x16x32_bf16 v[12:15], v[76:79], v[52:55], v[12:15]\n\t" \
  "ds_read_b64_tr_b16 v[70:71], v85 offset:0\n\t" \
  "ds_read_b128 v[52:55], v80 offset:9216\n\t" \
  "v_mfma_f32_16x16x32_bf16 v[16:19], v[72:75], v[56:59], v[16:19]\n\t" \
  "v_mfma_f32_16x16x32_bf16 v[20:23], v[76:79], v[56:59], v[20:23]\n\t" \
  "ds_read_b128 v[56:59], v80 offset:10240\n\t" \
  "s_add_u32 m0, %[lw], 90112\n\t" \
  "v_mfma_f32_16x16x32_bf16 v[24:27], v[72:75], v[60:63], v[24:27]\n\t" \
  "global_load_lds_dwordx4 v91, s[86:87]\n\t" \
  "s_add_u32 s86, s86, s89\n\t" \
  "s_addc_u32 s87, s87, 0\n\t" \
  "v_mfma_f32_16x16x32_bf16 v[28:31], v[76:79], v[60:63], v[28:31]\n\t" \
  "ds_read_b128 v[60:63], v80 offset:11264\n\t" \
  "s_cmp_lg_u32 s88, 0\n\t" \
  "s_cbranch_scc1 .Lp0%=\n\t" \
  "s_branch .Le%=\n\t" \
  ".Lk%=:\n\t" \
  "ds_read_b128 v[32:35], v80 offset:0\n\t" \
  "ds_read_b128 v[36:39], v80 offset:1024\n\t" \
  "ds_read_b128 v[40:43], v80 offset:2048\n\t" \
  "ds_read_b128 v[44:47], v80 offset:3072\n\t" \
  "ds_read_b64_tr_b16 v[64:65], v82 offset:0\n\t" \
  "ds_read_b64_tr_b16 v[66:67], v83 offset:0\n\t" \
  "ds_read_b128 v[48:51], v80 offset:8192\n\t" \
  "ds_read_b64_tr_b16 v[68:69], v84 offset:0\n\t" \
  "ds_read_b64_tr_b16 v[70:71], v85 offset:0\n\t" \
  "ds_read_b128 v[52:55], v80 offset:9216\n\t" \
  "ds_read_b128 v[56:59], v80 offset:10240\n\t" \
  "ds_read_b128 v[60:63], v80 offset:11264\n\t" \
  ".Lp1%=:\n\t" \
  "s_waitcnt vmcnt(4)\n\t" \
  "s_barrier\n\t" \
  "s_waitcnt lgkmcnt(3)\n\t" \
  "v_mfma_f32_16x16x32_bf16 v[0:3], v[64:67], v[32:35], v[0:3]\n\t" \
  "ds_read_b64_tr_b16 v[72:73], v82 offset:8192\n\t" \
  "v_mfma_f32_16x16x32_bf16 v[4:7], v[68:71], v[32:35], v[4:7]\n\t" \
  "ds_read_b64_tr_b16 v[74:75], v83 offset:8192\n\t" \
  "ds_read_b128 v[32:35], v80 offset:32768\n\t" \
  "v_mfma_f32_16x16x32_bf16 v[8:11], v[64:67], v[36:39], v[8:11]\n\t" \
  "ds_read_b64_tr_b16 v[76:77], v84 offset:8192\n\t" \
  "v_mfma_f32_16x16x32_bf16 v[12:15], v[68:71], v[36:39], v[12:15]\n\t" \
  "ds_read_b64_tr_b16 v[78:79], v85 offset:8192\n\t" \
  "ds_read_b128 v[36:39], v80 offset:33792\n\t" \
  "s_add_u32 m0, %[lw], 98304\n\t" \
  "v_mfma_f32_16x16x32_bf16 v[16:19], v[64:67], v[40:43], v[16:19]\n\t" \
  "global_load_lds_dwordx4 v90, s[84:85] sc1\n\t" \
  "s_add_u32 s84, s84, s88\n\t" \
  "s_addc_u32 s85, s85, 0\n\t" \
  "v_mfma_f32_16x16x32_bf16 v[20:23], v[68:71], v[40:43], v[20:23]\n\t" \
  "ds_read_b128 v[40:43], v80 offset:34816\n\t" \
  "v_mfma_f32_16x16x32_bf16 v[24:27], v[64:67], v[44:47], v[24:27]\n\t" \
  "s_add_u32 m0, %[lw], 114688\n\t" \
  "v_mfma_f32_16x16x32_bf16 v[28:31], v[68:71], v[44:47], v[28:31]\n\t" \
  "ds_read_b128 v[44:47], v80 offset:35840\n\t" \
  "global_load_lds_dwordx4 v91, s[86:87]\n\t" \
  "s_add_u32 s86, s86, s89\n\t" \
  "s_addc_u32 s87, s87, 0\n\t" \
  "s_waitcnt lgkmcnt(3)\n\t" \
  "v_mfma_f32_16x16x32_bf16 v[0:3], v[72:75], v[48:51], v[0:3]\n\t" \
  "ds_read_b64_tr_b16 v[64:65], v82 offset:32768\n\t" \
  "v_mfma_f32_16x16x32_bf16 v[4:7], v[76:79], v[48:51], v[4:7]\n\t" \
  "ds_read_b64_tr_b16 v[66:67], v83 offset:32768\n\t" \
  "ds_read_b128 v[48:51], v80 offset:40960\n\t" \
  "v_mfma_f32_16x16x32_bf16 v[8:11], v[72:75], v[52:55], v[8:11]\n\t" \
  "ds_read_b64_tr_b16 v[68:69], v84 offset:32768\n\t" \
  "v_mfma_f32_16x16x32_bf16 v[12:15], v[76:79], v[52:55], v[12:15]\n\t" \
  "ds_read_b64_tr_b16 v[70:71], v85 offset:32768\n\t" \
  "ds_read_b128 v[52:55], v80 offset:41984\n\t" \
  "s_add_u32 m0, %[lw], 106496\n\t" \
  "v_mfma_f32_16x16x32_bf16 v[16:19], v[72:75], v[56:59], v[16:19]\n\t" \
  "global_load_lds_dwordx4 v90, s[84:85] sc1\n\t" \
  "s_add_u32 s84, s84, s88\n\t" \
  "s_addc_u32 s85, s85, 0\n\t" \
  "v_mfma_f32_16x16x32_bf16 v[20:23], v[76:79], v[56:59], v[20:23]\n\t" \
  "ds_read_b128 v[56:59], v80 offset:43008\n\t" \
  "v_mfma_f32_16x16x32_bf16 v[24:27], v[72:75], v[60:63], v[24:27]\n\t" \
  "s_add_u32 m0, %[lw], 122880\n\t" \
  "v_mfma_f32_16x16x32_bf16 v[28:31], v[76:79], v[60:63], v[28:31]\n\t" \
  "ds_read_b128 v[60:63], v80 offset:44032\n\t" \
  "global_load_lds_dwordx4 v91, s[86:87]\n\t" \
  "s_add_u32 s86, s86, s89\n\t" \
  "s_addc_u32 s87, s87, 0\n\t" \
  "s_sub_u32 s90, s90, 1\n\t" \
  "s_cselect_b32 s88, 0, s88\n\t" \
  "s_cselect_b32 s89, 0, s89\n\t" \
  "s_cselect_b32 s91, 16384, 0\n\t" \
  "s_sub_u32 s84, s84, s91\n\t" \
  "s_subb_u32 s85, s85, 0\n\t" \
  "s_lshl_b32 s91, s91, 2\n\t" \
  "s_sub_u32 s86, s86, s91\n\t" \
  "s_subb_u32 s87, s87, 0\n\t" \
  "s_waitcnt vmcnt(4)\n\t" \
  "s_barrier\n\t" \
  "s_waitcnt lgkmcnt(3)\n\t" \
  "v_mfma_f32_16x16x32_bf16 v[0:3], v[64:67], v[32:35], v[0:3]\n\t" \
  "ds_read_b64_tr_b16 v[72:73], v82 offset:40960\n\t" \
  "v_mfma_f32_16x16x32_bf16 v[4:7], v[68:71], v[32:35], v[4:7]\n\t" \
  "ds_read_b64_tr_b16 v[74:75], v83 offset:40960\n\t" \
  "ds_read_b128 v[32:35], v81 offset:0\n\t" \
  "v_mfma_f32_16x16x32_bf16 v[8:11], v[64:67], v[36:39], v[8:11]\n\t" \
  "ds_read_b64_tr_b16 v[76:77], v84 offset:40960\n\t" \
  "v_mfma_f32_16x16x32_bf16 v[12:15], v[68:71], v[36:39], v[12:15]\n\t" \
  "ds_read_b64_tr_b16 v[78:79], v85 offset:40960\n\t" \
  "ds_read_b128 v[36:39], v81 offset:1024\n\t" \
  "s_add_u32 m0, %[lw], 0\n\t" \
  "v_mfma_f32_16x16x32_bf16 v[16:19], v[64:67], v[40:43], v[16:19]\n\t" \
  "global_load_lds_dwordx4 v90, s[84:85] sc1\n\t" \
  "s_add_u32 s84, s84, s88\n\t" \
  "s_addc_u32 s85, s85, 0\n\t" \
  "v_mfma_f32_16x16x32_bf16 v[20:23], v[68:71], v[40:43], v[20:23]\n\t" \
  "ds_read_b128 v[40:43], v81 offset:2048\n\t" \
  "v_mfma_f32_16x16x32_bf16 v[24:27], v[64:67], v[44:47], v[24:27]\n\t" \
  "s_add_u32 m0, %[lw], 16384\n\t" \
  "v_mfma_f32_16x16x32_bf16 v[28:31], v[68:71], v[44:47], v[28:31]\n\t" \
  "ds_read_b128 v[44:47], v81 offset:3072\n\t" \
  "global_load_lds_dwordx4 v91, s[86:87]\n\t" \
  "s_add_u32 s86, s86, s89\n\t" \
  "s_addc_u32 s87, s87, 0\n\t" \
  "s_waitcnt lgkmcnt(3)\n\t" \
  "v_mfma_f32_16x16x32_bf16 v[0:3], v[72:75], v[48:51], v[0:3]\n\t" \
  "ds_read_b64_tr_b16 v[64:65], v86 offset:0\n\t" \
  "v_mfma_f32_16x16x32_bf16 v[4:7], v[76:79], v[48:51], v[4:7]\n\t" \
  "ds_read_b64_tr_b16 v[66:67], v87 offset:0\n\t" \
  "ds_read_b128 v[48:51], v81 offset:8192\n\t" \
  "v_mfma_f32_16x16x32_bf16 v[8:11], v[72:75], v[52:55], v[8:11]\n\t" \
  "ds_read_b64_tr_b16 v[68:69], v88 offset:0\n\t" \
  "v_mfma_f32_16x16x32_bf16 v[12:15], v[76:79], v[52:55], v[12:15]\n\t" \
  "ds_read_b64_tr_b16 v[70:71], v89 offset:0\n\t" \
  "ds_read_b128 v[52:55], v81 offset:9216\n\t" \
  "s_add_u32 m0, %[lw], 8192\n\t" \
  "v_mfma_f32_16x16x32_bf16 v[16:19], v[72:75], v[56:59], v[16:19]\n\t" \
  "global_load_lds_dwordx4 v90, s[84:85] sc1\n\t" \
  "s_add_u32 s84, s84, s88\n\t" \
  "s_addc_u32 s85, s85, 0\n\t" \
  "v_mfma_f32_16x16x32_bf16 v[20:23], v[76:79], v[56:59], v[20:23]\n\t" \
  "ds_read_b128 v[56:59], v81 offset:10240\n\t" \
  "v_mfma_f32_16x16x32_bf16 v[24:27], v[72:75], v[60:63], v[24:27]\n\t" \
  "s_add_u32 m0, %[lw], 24576\n\t" \
  "v_mfma_f32_16x16x32_bf16 v[28:31], v[76:79], v[60:63], v[28:31]\n\t" \
  "ds_read_b128 v[60:63], v81 offset:11264\n\t" \
  "global_load_lds_dwordx4 v91, s[86:87]\n\t" \
  "s_add_u32 s86, s86, s89\n\t" \
  "s_addc_u32 s87, s87, 0\n\t" \
  "s_waitcnt vmcnt(4)\n\t" \
  "s_barrier\n\t" \
  "s_waitcnt lgkmcnt(3)\n\t" \
  "v_mfma_f32_16x16x32_bf16 v[0:3], v[64:67], v[32:35], v[0:3]\n\t" \
  "ds_read_b64_tr_b16 v[72:73], v86 offset:8192\n\t" \
  "v_mfma_f32_16x16x32_bf16 v[4:7], v[68:71], v[32:35], v[4:7]\n\t" \
  "ds_read_b64_tr_b16 v[74:75], v87 offset:8192\n\t" \
  "ds_read_b128 v[32:35], v81 offset:32768\n\t" \
  "v_mfma_f32_16x16x32_bf16 v[8:11], v[64:67], v[36:39], v[8:11]\n\t" \
  "ds_read_b64_tr_b16 v[76:77], v88 offset:8192\n\t" \
  "v_mfma_f32_16x16x32_bf16 v[12:15], v[68:71], v[36:39], v[12:15]\n\t" \
  "ds_read_b64_tr_b16 v[78:79], v89 offset:8192\n\t" \
  "ds_read_b128 v[36:39], v81 offset:33792\n\t" \
  "s_add_u32 m0, %[lw], 32768\n\t" \
  "v_mfma_f32_16x16x32_bf16 v[16:19], v[64:67], v[40:43], v[16:19]\n\t" \
  "global_load_lds_dwordx4 v90, s[84:85] sc1\n\t" \
  "s_add_u32 s84, s84, s88\n\t" \
  "s_addc_u32 s85, s85, 0\n\t" \
  "v_mfma_f32_16x16x32_bf16 v[20:23], v[68:71], v[40:43], v[20:23]\n\t" \
  "ds_read_b128 v[40:43], v81 offset:34816\n\t" \
  "v_mfma_f32_16x16x32_bf16 v[24:27], v[64:67], v[44:47], v[24:27]\n\t" \
  "s_add_u32 m0, %[lw], 49152\n\t" \
  "v_mfma_f32_16x16x32_bf16 v[28:31], v[68:71], v[44:47], v[28:31]\n\t" \
  "ds_read_b128 v[44:47], v81 offset:35840\n\t" \
  "global_load_lds_dwordx4 v91, s[86:87]\n\t" \
  "s_add_u32 s86, s86, s89\n\t" \
  "s_addc_u32 s87, s87, 0\n\t" \
  "s_waitcnt lgkmcnt(3)\n\t" \
  "v_mfma_f32_16x16x32_bf16 v[0:3], v[72:75], v[48:51], v[0:3]\n\t" \
  "ds_read_b64_tr_b16 v[64:65], v86 offset:32768\n\t" \
  "v_mfma_f32_16x16x32_bf16 v[4:7], v[76:79], v[48:51], v[4:7]\n\t" \
  "ds_read_b64_tr_b16 v[66:67], v87 offset:32768\n\t" \
  "ds_read_b128 v[48:51], v81 offset:40960\n\t" \
  "v_mfma_f32_16x16x32_bf16 v[8:11], v[72:75], v[52:55], v[8:11]\n\t" \
  "ds_read_b64_tr_b16 v[68:69], v88 offset:32768\n\t" \
  "v_mfma_f32_16x16x32_bf16 v[12:15], v[76:79], v[52:55], v[12:15]\n\t" \
  "ds_read_b64_tr_b16 v[70:71], v89 offset:32768\n\t" \
  "ds_read_b128 v[52:55], v81 offset:41984\n\t" \
  "s_add_u32 m0, %[lw], 40960\n\t" \
  "v_mfma_f32_16x16x32_bf16 v[16:19], v[72:75], v[56:59], v[16:19]\n\t" \
  "global_load_lds_dwordx4 v90, s[84:85] sc1\n\t" \
  "s_add_u32 s84, s84, s88\n\t" \
  "s_addc_u32 s85, s85, 0\n\t" \
  "v_mfma_f32_16x16x32_bf16 v[20:23], v[76:79], v[56:59], v[20:23]\n\t" \
  "ds_read_b128 v[56:59], v81 offset:43008\n\t" \
  "v_mfma_f32_16x16x32_bf16 v[24:27], v[72:75], v[60:63], v[24:27]\n\t" \
  "s_add_u32 m0, %[lw], 57344\n\t" \
  "v_mfma_f32_16x16x32_bf16 v[28:31], v[76:79], v[60:63], v[28:31]\n\t" \
  "ds_read_b128 v[60:63], v81 offset:44032\n\t" \
  "global_load_lds_dwordx4 v91, s[86:87]\n\t" \
  "s_add_u32 s86, s86, s89\n\t" \
  "s_addc_u32 s87, s87, 0\n\t" \
  "s_waitcnt vmcnt(4)\n\t" \
  "s_barrier\n\t" \
  "s_waitcnt lgkmcnt(3)\n\t" \
  "v_mfma_f32_16x16x32_bf16 v[0:3], v[64:67], v[32:35], v[0:3]\n\t" \
  "ds_read_b64_tr_b16 v[72:73], v86 offset:40960\n\t" \
  "v_mfma_f32_16x16x32_bf16 v[4:7], v[68:71], v[32:35], v[4:7]\n\t" \
  "ds_read_b64_tr_b16 v[74:75], v87 offset:40960\n\t" \
  "ds_read_b128 v[32:35], v80 offset:0\n\t" \
  "v_mfma_f32_16x16x32_bf16 v[8:11], v[64:67], v[36:39], v[8:11]\n\t" \
  "ds_read_b64_tr_b16 v[76:77], v88 offset:40960\n\t" \
  "v_mfma_f32_16x16x32_bf16 v[12:15], v[68:71], v[36:39], v[12:15]\n\t" \
  "ds_read_b64_tr_b16 v[78:79], v89 offset:40960\n\t" \
  "ds_read_b128 v[36:39], v80 offset:1024\n\t" \
  "s_add_u32 m0, %[lw], 65536\n\t" \
  "v_mfma_f32_16x16x32_bf16 v[16:19], v[64:67], v[40:43], v[16:19]\n\t" \
  "global_load_lds_dwordx4 v90, s[84:85] sc1\n\t" \
  "s_add_u32 s84, s84, s88\n\t" \
  "s_addc_u32 s85, s85, 0\n\t" \
  "v_mfma_f32_16x16x32_bf16 v[20:23], v[68:71], v[40:43], v[20:23]\n\t" \
  "ds_read_b128 v[40:43], v80 offset:2048\n\t" \
  "v_mfma_f32_16x16x32_bf16 v[24:27], v[64:67], v[44:47], v[24:27]\n\t" \
  "s_add_u32 m0, %[lw], 81920\n\t" \
  "v_mfma_f32_16x16x32_bf16 v[28:31], v[68:71], v[44:47], v[28:31]\n\t" \
  "ds_read_b128 v[44:47], v80 offset:3072\n\t" \
  "global_load_lds_dwordx4 v91, s[86:87]\n\t" \
  "s_add_u32 s86, s86, s89\n\t" \
  "s_addc_u32 s87, s87, 0\n\t" \
  "s_waitcnt lgkmcnt(3)\n\t" \
  "v_mfma_f32_16x16x32_bf16 v[0:3], v[72:75], v[48:51], v[0:3]\n\t" \
  "ds_read_b64_tr_b16 v[64:65], v82 offset:0\n\t" \
  "v_mfma_f32_16x16x32_bf16 v[4:7], v[76:79], v[48:51], v[4:7]\n\t" \
  "ds_read_b64_tr_b16 v[66:67], v83 offset:0\n\t" \
  "ds_read_b128 v[48:51], v80 offset:8192\n\t" \
  "v_mfma_f32_16x16x32_bf16 v[8:11], v[72:75], v[52:55], v[8:11]\n\t" \
  "ds_read_b64_tr_b16 v[68:69], v84 offset:0\n\t" \
  "v_mfma_f32_16x16x32_bf16 v[12:15], v[76:79], v[52:55], v[12:15]\n\t" \
  "ds_read_b64_tr_b16 v[70:71], v85 offset:0\n\t" \
  "ds_read_b128 v[52:55], v80 offset:9216\n\t" \
  "s_add_u32 m0, %[lw], 73728\n\t" \
  "v_mfma_f32_16x16x32_bf16 v[16:19], v[72:75], v[56:59], v[16:19]\n\t" \
  "global_load_lds_dwordx4 v90, s[84:85] sc1\n\t" \
  "s_add_u32 s84, s84, s88\n\t" \
  "s_addc_u32 s85, s85, 0\n\t" \
  "v_mfma_f32_16x16x32_bf16 v[20:23], v[76:79], v[56:59], v[20:23]\n\t" \
  "ds_read_b128 v[56:59], v80 offset:10240\n\t" \
  "v_mfma_f32_16x16x32_bf16 v[24:27], v[72:75], v[60:63], v[24:27]\n\t" \
  "s_add_u32 m0, %[lw], 90112\n\t" \
  "v_mfma_f32_16x16x32_bf16 v[28:31], v[76:79], v[60:63], v[28:31]\n\t" \
  "ds_read_b128 v[60:63], v80 offset:11264\n\t" \
  "global_load_lds_dwordx4 v91, s[86:87]\n\t" \
  "s_add_u32 s86, s86, s89\n\t" \
  "s_addc_u32 s87, s87, 0\n\t" \
  "s_cmp_lg_u32 s88, 0\n\t" \
  "s_cbranch_scc1 .Lp1%=\n\t" \
  ".Le%=:\n\t" \
  "s_waitcnt vmcnt(0) lgkmcnt(0)\n\t" \
  "s_mov_b32 m0, s94\n\t" \
  "s_setprio 0\n\t" \
  "s_nop 7\n\t"
#define HSL 16
__device__ __forceinline__ void job_down(const Args& p, int rblk, int ct, char* smem, __amdgpu_buffer_rsrc_t rsy) {
  if (threadIdx.x < 64) {
    for (unsigned spins = 0;;) {
      asm volatile("" ::: "memory");
      if (__builtin_amdgcn_readfirstlane((int)ld_flag0(rsy, SY_CH + (rblk >> 1) * 128)) >= 27) break;
      __builtin_amdgcn_s_sleep(HSL);
      if (spin_fail(spins, (gu32*)(p.ws + WS_SY + SY_ABORT))) break;
    }
  }
  int tid = threadIdx.x;
  asm volatile("" : "+v"(tid));
  const int lane = tid & 63, r = lane & 15, q = lane >> 4;
  const int wid = __builtin_amdgcn_readfirstlane(tid >> 6), wr = wid >> 2, wc = wid & 3;
  typedef __attribute__((ext_vector_type(32))) float f32x32;
  typedef __attribute__((ext_vector_type(8))) unsigned u32x8;
  f32x32 acc;
  {
    const char* A = p.ws + WS_HF + h_off(p, rblk);
    const char* B = (const char*)(p.wd + ct * 128);
    const unsigned rb = tid >> 4, va = tid * 16, vb = ((rb & 4) * 4 + (rb >> 3) * 4 + (rb & 3)) * (DM * 2) + 16 * ((tid & 15) ^ swzB(rb & 15));
    u32x8 lb;
#pragma unroll
    for (int i = 0; i < 8; ++i)
      lb[i] = (i >> 2) * 65536 + 16384 + offB(8 * q + 4 * (i & 1) + (r >> 2), 2 * (wc * 2 + ((i >> 1) & 1)) + ((r & 3) >> 1)) + 8 * (r & 1);
    LBAR();
#pragma unroll
    for (int t = 0; t < 3; ++t) {
      glds2<true>(A + t * 32768, 16384, va, wid * 1024 + t * 32768);
      glds2(B + t * 64 * DM * 2, 32 * DM * 2, vb, wid * 1024 + t * 32768 + 16384);
    }
    WAIT_V(8);
    LBAR();
    asm volatile(DN_ASM
        : "={v[0:31]}"(acc)
        : "{v[80:81]}"((u32x2){wr * 4096u + lane * 16u, 65536u + wr * 4096u + lane * 16u}), "{v[82:89]}"(lb), "{v90}"(va), "{v91}"(vb),
          [pa] "s"(A + 3 * 32768), [pb] "s"(B + 3 * 64 * DM * 2), [lw] "s"(wid * 1024), [hf] "s"(wr)
        : CLB8(32, 33, 34, 35, 36, 37, 38, 39), CLB8(40, 41, 42, 43, 44, 45, 46, 47), CLB8(48, 49, 50, 51, 52, 53, 54, 55),
          CLB8(56, 57, 58, 59, 60, 61, 62, 63), CLB8(64, 65, 66, 67, 68, 69, 70, 71), CLB8(72, 73, 74, 75, 76, 77, 78, 79),
          "s84", "s85", "s86", "s87", "s88", "s89", "s90", "s91", "s94", "scc", "memory");
  }
  wait_s(p, rsy, rblk, ct * 4, 4, rblk < NFUSE);
  const int e0 = (rblk * 128 + wr * 64 + r) * DM + ct * 128 + wc * 32 + q * 8;
  SX sx[4];
#pragma unroll
  for (int m = 0; m < 4; ++m) sx[m] = get_sx(p, rblk, e0 + m * 16 * DM);
  const __amdgpu_buffer_rsrc_t ro = mkrsrc(p.ws + WS_G, S_LEN * DM * 2);
#pragma unroll
  for (int m = 0; m < 4; ++m) {
    float rr[8];
#pragma unroll
    for (int j = 0; j < 8; ++j) { float d = rbf(acc[8 * m + j]); rr[j] = d * __builtin_amdgcn_rcpf(1.0f + __expf(-d)); }
    put_g(p, ro, rblk, e0 + m * 16 * DM, widen32(cvtpk(rr[0], rr[1]), cvtpk(rr[2], rr[3]), cvtpk(rr[4], rr[5]), cvtpk(rr[6], rr[7])), sx[m]);
  }
  if (rblk >= NFUSE) publish_flags(rsy, SY_FG + (rblk * 8 + ct) * 128, 0, 1);
}

#define PR_ASM_0 \
  "s_cmp_lg_u32 %[kh], 0\n\t" \
  "s_cbranch_scc1 .Lk%=\n\t" \
  "s_mov_b32 s94, m0\n\t" \
  "s_mov_b64 s[92:93], %[x0]\n\t" \
  "s_mov_b32 s86, %[g0]\n\t" \
  "s_add_u32 s87, s86, %[t0]\n\t" \
  "s_sub_i32 s88, s86, 3\n\t" \
  "ds_read_b128 v[176:179], v208 offset:0\n\t" \
  "ds_read_b128 v[180:183], v208 offset:16384\n\t" \
  "ds_read_b128 v[184:187], v209 offset:0\n\t" \
  "ds_read_b128 v[188:191], v209 offset:16384\n\t" \
  "ds_read_b128 v[192:195], v210 offset:0\n\t" \
  "ds_read_b128 v[196:199], v210 offset:16384\n\t" \
  "ds_read_b128 v[200:203], v211 offset:0\n\t" \
  "ds_read_b128 v[204:207], v211 offset:16384\n\t" \
  "s_branch .Lfb00%=\n\t" \
  ".Lp0%=:\n\t" \
  "s_cmp_le_u32 s86, 5\n\t" \
  "s_cbranch_scc1 .Lf00%=\n\t" \
  ".Lfb00%=:\n\t" \
  "s_waitcnt lgkmcnt(6)\n\t" \
  "v_mfma_f32_16x16x32_bf16 v[128:131], v[0:3], v[176:179], 0\n\t" \
  "v_mfma_f32_16x16x32_bf16 v[132:135], v[4:7], v[176:179], 0\n\t" \
  "ds_read_b128 v[176:179], v208 offset:256\n\t" \
  "s_add_u32 m0, %[lw], 67584\n\t" \
  "v_mfma_f32_16x16x32_bf16 v[136:139], v[0:3], v[180:183], 0\n\t" \
  "v_mfma_f32_16x16x32_bf16 v[140:143], v[4:7], v[180:183], 0\n\t" \
  "ds_read_b128 v[180:183], v208 offset:16640\n\t" \
  "global_load_lds_dwordx4 v218, s[92:93] sc1\n\t" \
  "s_waitcnt lgkmcnt(6)\n\t" \
  "v_mfma_f32_16x16x32_bf16 v[128:131], v[8:11], v[184:187], v[128:131]\n\t" \
  "v_mfma_f32_16x16x32_bf16 v[132:135], v[12:15], v[184:187], v[132:135]\n\t" \
  "ds_read_b128 v[184:187], v209 offset:256\n\t" \
  "v_mfma_f32_16x16x32_bf16 v[136:139], v[8:11], v[188:191], v[136:139]\n\t" \
  "v_mfma_f32_16x16x32_bf16 v[140:143], v[12:15], v[188:191], v[140:143]\n\t" \
  "ds_read_b128 v[188:191], v209 offset:16640\n\t" \
  "ds_write_b128 v220, v[152:155] offset:0\n\t" \
  "ds_write_b128 v220, v[156:159] offset:1024\n\t" \
  "s_waitcnt lgkmcnt(8)\n\t" \
  "v_mfma_f32_16x16x32_bf16 v[128:131], v[16:19], v[192:195], v[128:131]\n\t" \
  "v_mfma_f32_16x16x32_bf16 v[132:135], v[20:23], v[192:195], v[132:135]\n\t" \
  "ds_read_b128 v[192:195], v210 offset:256\n\t" \
  "s_add_u32 m0, %[lw], 68608\n\t" \
  "v_mfma_f32_16x16x32_bf16 v[136:139], v[16:19], v[196:199], v[136:139]\n\t" \
  "v_mfma_f32_16x16x32_bf16 v[140:143], v[20:23], v[196:199], v[140:143]\n\t" \
  "ds_read_b128 v[196:199], v210 offset:16640\n\t" \
  "global_load_lds_dwordx4 v219, s[92:93] sc1\n\t" \
  "s_lshr_b32 s90, s87, 7\n\t" \
  "s_xor_b32 s90, s90, %[s0]\n\t" \
  "s_and_b32 s90, s90, 1\n\t" \
  "s_lshl1_add_u32 s90, s87, s90\n\t" \
  "s_lshl_b32 s90, s90, 13\n\t" \
  "s_cmp_lt_i32 s86, 6\n\t" \
  "s_cselect_b32 s90, 0x70000000, s90\n\t" \
  "s_waitcnt lgkmcnt(8)\n\t" \
  "v_mfma_f32_16x16x32_bf16 v[128:131], v[24:27], v[200:203], v[128:131]\n\t" \
  "v_mfma_f32_16x16x32_bf16 v[132:135], v[28:31], v[200:203], v[132:135]\n\t" \
  "ds_read_b128 v[200:203], v211 offset:256\n\t" \
  "v_mfma_f32_16x16x32_bf16 v[136:139], v[24:27], v[204:207], v[136:139]\n\t" \
  "v_mfma_f32_16x16x32_bf16 v[140:143], v[28:31], v[204:207], v[140:143]\n\t" \
  "ds_read_b128 v[204:207], v211 offset:16640\n\t" \
  "s_waitcnt lgkmcnt(4)\n\t" \
  "s_cmp_lt_u32 s86, 6\n\t" \
  "s_cbranch_scc1 .Lw00%=\n\t" \
  "s_waitcnt vmcnt(4)\n\t" \
  "s_barrier\n\t" \
  ".Lg00%=:\n\t" \
  "s_add_u32 s92, s92, 512\n\t" \
  "s_addc_u32 s93, s93, 0\n\t" \
  "v_mfma_f32_16x16x32_bf16 v[128:131], v[32:35], v[176:179], v[128:131]\n\t" \
  "v_mfma_f32_16x16x32_bf16 v[132:135], v[36:39], v[176:179], v[132:135]\n\t" \
  "ds_read_b128 v[176:179], v208 offset:32768\n\t" \
  "s_add_u32 m0, %[lw], 98304\n\t" \
  "v_mfma_f32_16x16x32_bf16 v[136:139], v[32:35], v[180:183], v[136:139]\n\t" \
  "v_mfma_f32_16x16x32_bf16 v[140:143], v[36:39], v[180:183], v[140:143]\n\t" \
  "ds_read_b128 v[180:183], v208 offset:49152\n\t" \
  "global_load_lds_dwordx4 v216, s[92:93] sc1\n\t" \
  "ds_read_b128 v[160:163], v221 offset:0\n\t" \
  "ds_read_b128 v[164:167], v221 offset:1024\n\t" \
  "v_mfma_f32_16x16x32_bf16 v[128:131], v[40:43], v[184:187], v[128:131]\n\t" \
  "v_mfma_f32_16x16x32_bf16 v[132:135], v[44:47], v[184:187], v[132:135]\n\t" \
  "ds_read_b128 v[184:187], v209 offset:32768\n\t" \
  "v_mfma_f32_16x16x32_bf16 v[136:139], v[40:43], v[188:191], v[136:139]\n\t" \
  "v_mfma_f32_16x16x32_bf16 v[140:143], v[44:47], v[188:191], v[140:143]\n\t" \
  "ds_read_b128 v[188:191], v209 offset:49152\n\t" \
  "s_waitcnt lgkmcnt(8)\n\t" \
  "v_mfma_f32_16x16x32_bf16 v[128:131], v[48:51], v[192:195], v[128:131]\n\t" \
  "v_mfma_f32_16x16x32_bf16 v[132:135], v[52:55], v[192:195], v[132:135]\n\t" \
  "ds_read_b128 v[192:195], v210 offset:32768\n\t" \
  "s_add_u32 m0, %[lw], 99328\n\t" \
  "v_mfma_f32_16x16x32_bf16 v[136:139], v[48:51], v[196:199], v[136:139]\n\t" \
  "v_mfma_f32_16x16x32_bf16 v[140:143], v[52:55], v[196:199], v[140:143]\n\t" \
  "ds_read_b128 v[196:199], v210 offset:49152\n\t" \
  "global_load_lds_dwordx4 v217, s[92:93] sc1\n\t" \
  "s_waitcnt lgkmcnt(4)\n\t" \
  "v_add_f32 v160, v160, v144\n\t" \
  "v_add_f32 v161, v161, v145\n\t" \
  "v_add_f32 v162, v162, v146\n\t" \
  "v_add_f32 v163, v163, v147\n\t" \
  "v_add_f32 v164, v164, v148\n\t" \
  "v_add_f32 v165, v165, v149\n\t" \
  "v_add_f32 v166, v166, v150\n\t" \
  "v_add_f32 v167, v167, v151\n\t" \
  "v_mfma_f32_16x16x32_bf16 v[128:131], v[56:59], v[200:203], v[128:131]\n\t" \
  "v_mfma_f32_16x16x32_bf16 v[132:135], v[60:63], v[200:203], v[132:135]\n\t" \
  "ds_read_b128 v[200:203], v211 offset:32768\n\t" \
  "v_mfma_f32_16x16x32_bf16 v[136:139], v[56:59], v[204:207], v[136:139]\n\t" \
  "v_mfma_f32_16x16x32_bf16 v[140:143], v[60:63], v[204:207], v[140:143]\n\t" \
  "ds_read_b128 v[204:207], v211 offset:49152\n\t" \
  "v_cvt_pk_bf16_f32 v168, v160, v161\n\t" \
  "v_cvt_pk_bf16_f32 v169, v162, v163\n\t" \
  "v_cvt_pk_bf16_f32 v170, v164, v165\n\t" \
  "v_cvt_pk_bf16_f32 v171, v166, v167\n\t" \
  "buffer_store_dwordx4 v[168:171], v172, %[rt], s90 offen sc1\n\t" \
  "v_mfma_f32_16x16x32_bf16 v[128:131], v[64:67], v[176:179], v[128:131]\n\t" \
  "v_mfma_f32_16x16x32_bf16 v[132:135], v[68:71], v[176:179], v[132:135]\n\t" \
  "ds_read_b128 v[176:179], v208 offset:33024\n\t" \
  "s_add_u32 m0, %[lw], 100352\n\t" \
  "v_mfma_f32_16x16x32_bf16 v[136:139], v[64:67], v[180:183], v[136:139]\n\t" \
  "v_mfma_f32_16x16x32_bf16 v[140:143], v[68:71], v[180:183], v[140:143]\n\t" \
  "ds_read_b128 v[180:183], v208 offset:49408\n\t" \
  "global_load_lds_dwordx4 v218, s[92:93] sc1\n\t" \
  "s_waitcnt lgkmcnt(6)\n\t" \
  "v_mfma_f32_16x16x32_bf16 v[128:131], v[72:75], v[184:187], v[128:131]\n\t" \
  "v_mfma_f32_16x16x32_bf16 v[132:135], v[76:79], v[184:187], v[132:135]\n\t" \
  "ds_read_b128 v[184:187], v209 offset:33024\n\t" \
  "v_mfma_f32_16x16x32_bf16 v[136:139], v[72:75], v[188:191], v[136:139]\n\t" \
  "v_mfma_f32_16x16x32_bf16 v[140:143], v[76:79], v[188:191], v[140:143]\n\t" \
  "ds_read_b128 v[188:191], v209 offset:49408\n\t" \
  "s_waitcnt lgkmcnt(6)\n\t" \
  "v_mfma_f32_16x16x32_bf16 v[128:131], v[80:83], v[192:195], v[128:131]\n\t" \
  "v_mfma_f32_16x16x32_bf16 v[132:135], v[84:87], v[192:195], v[132:135]\n\t" \
  "ds_read_b128 v[192:195], v210 offset:33024\n\t" \
  "s_add_u32 m0, %[lw], 101376\n\t" \
  "v_mfma_f32_16x16x32_bf16 v[136:139], v[80:83], v[196:199], v[136:139]\n\t" \
  "v_mfma_f32_16x16x32_bf16 v[140:143], v[84:87], v[196:199], v[140:143]\n\t" \
  "ds_read_b128 v[196:199], v210 offset:49408\n\t" \
  "global_load_lds_dwordx4 v219, s[92:93] sc1\n\t" \
  "s_waitcnt lgkmcnt(6)\n\t" \
  "v_mfma_f32_16x16x32_bf16 v[128:131], v[88:91], v[200:203], v[128:131]\n\t" \
  "v_mfma_f32_16x16x32_bf16 v[132:135], v[92:95], v[200:203], v[132:135]\n\t" \
  "ds_read_b128 v[200:203], v211 offset:33024\n\t" \
  "v_mfma_f32_16x16x32_bf16 v[136:139], v[88:91], v[204:207], v[136:139]\n\t" \
  "v_mfma_f32_16x16x32_bf16 v[140:143], v[92:95], v[204:207], v[140:143]\n\t" \
  "ds_read_b128 v[204:207], v211 offset:49408\n\t" \
  "s_cmp_lt_u32 s86, 8\n\t" \
  "s_cbranch_scc1 .Lw10%=\n\t" \
  "s_waitcnt vmcnt(5)\n\t" \
  "s_barrier\n\t" \
  ".Lg10%=:\n\t" \
  "s_add_u32 s92, s92, 65024\n\t" \
  "s_addc_u32 s93, s93, 0\n\t" \
  "s_waitcnt lgkmcnt(6)\n\t" \
  "v_mfma_f32_16x16x32_bf16 v[128:131], v[96:99], v[176:179], v[128:131]\n\t" \
  "v_mfma_f32_16x16x32_bf16 v[132:135], v[100:103], v[176:179], v[132:135]\n\t" \
  "ds_read_b128 v[176:179], v212 offset:0\n\t" \
  "s_add_u32 m0, %[lw], 0\n\t" \
  "v_mfma_f32_16x16x32_bf16 v[136:139], v[96:99], v[180:183], v[136:139]\n\t" \
  "v_mfma_f32_16x16x32_bf16 v[140:143], v[100:103], v[180:183], v[140:143]\n\t" \
  "ds_read_b128 v[180:183], v212 offset:16384\n\t" \
  "global_load_lds_dwordx4 v216, s[92:93] sc1\n\t" \
  "s_waitcnt lgkmcnt(6)\n\t" \
  "v_mfma_f32_16x16x32_bf16 v[128:131], v[104:107], v[184:187], v[128:131]\n\t" \
  "v_mfma_f32_16x16x32_bf16 v[132:135], v[108:111], v[184:187], v[132:135]\n\t" \
  "ds_read_b128 v[184:187], v213 offset:0\n\t" \
  "v_mfma_f32_16x16x32_bf16 v[136:139], v[104:107], v[188:191], v[136:139]\n\t" \
  "v_mfma_f32_16x16x32_bf16 v[140:143], v[108:111], v[188:191], v[140:143]\n\t" \
  "ds_read_b128 v[188:191], v213 offset:16384\n\t" \
  "s_waitcnt lgkmcnt(6)\n\t" \
  "v_mfma_f32_16x16x32_bf16 v[128:131], v[112:115], v[192:195], v[128:131]\n\t" \
  "v_mfma_f32_16x16x32_bf16 v[132:135], v[116:119], v[192:195], v[132:135]\n\t" \
  "ds_read_b128 v[192:195], v214 offset:0\n\t" \
  "s_add_u32 m0, %[lw], 1024\n\t" \
  "v_mfma_f32_16x16x32_bf16 v[136:139], v[112:115], v[196:199], v[136:139]\n\t" \
  "v_mfma_f32_16x16x32_bf16 v[140:143], v[116:119], v[196:199], v[140:143]\n\t" \
  "ds_read_b128 v[196:199], v214 offset:16384\n\t" \
  "global_load_lds_dwordx4 v217, s[92:93] sc1\n\t" \
  "s_waitcnt lgkmcnt(6)\n\t" \
  "v_mfma_f32_16x16x32_bf16 v[128:131], v[120:123], v[200:203], v[128:131]\n\t" \
  "v_mfma_f32_16x16x32_bf16 v[132:135], v[124:127], v[200:203], v[132:135]\n\t" \
  "ds_read_b128 v[200:203], v215 offset:0\n\t" \
  "v_mfma_f32_16x16x32_bf16 v[136:139], v[120:123], v[204:207], v[136:139]\n\t" \
  "v_mfma_f32_16x16x32_bf16 v[140:143], v[124:127], v[204:207], v[140:143]\n\t" \
  "ds_read_b128 v[204:207], v215 offset:16384\n\t" \
  "s_add_u32 s86, s86, 1\n\t" \
  "s_add_u32 s87, s87, 1\n\t" \
  "s_cmp_le_u32 s86, 5\n\t" \
  "s_cbranch_scc1 .Lf10%=\n\t" \
  ".Lfb10%=:\n\t" \
  "s_waitcnt lgkmcnt(6)\n\t" \
  "v_mfma_f32_16x16x32_bf16 v[144:147], v[0:3], v[176:179], 0\n\t" \
  "v_mfma_f32_16x16x32_bf16 v[148:151], v[4:7], v[176:179], 0\n\t" \
  "ds_read_b128 v[176:179], v212 offset:256\n\t" \
  "s_add_u32 m0, %[lw], 2048\n\t" \
  "v_mfma_f32_16x16x32_bf16 v[152:155], v[0:3], v[180:183], 0\n\t" \
  "v_mfma_f32_16x16x32_bf16 v[156:159], v[4:7], v[180:183], 0\n\t" \
  "ds_read_b128 v[180:183], v212 offset:16640\n\t" \
  "global_load_lds_dwordx4 v218, s[92:93] sc1\n\t" \
  "s_waitcnt lgkmcnt(6)\n\t" \
  "v_mfma_f32_16x16x32_bf16 v[144:147], v[8:11], v[184:187], v[144:147]\n\t" \
  "v_mfma_f32_16x16x32_bf16 v[148:151], v[12:15], v[184:187], v[148:151]\n\t" \
  "ds_read_b128 v[184:187], v213 offset:256\n\t" \
  "v_mfma_f32_16x16x32_bf16 v[152:155], v[8:11], v[188:191], v[152:155]\n\t" \
  "v_mfma_f32_16x16x32_bf16 v[156:159], v[12:15], v[188:191], v[156:159]\n\t" \
  "ds_read_b128 v[188:191], v213 offset:16640\n\t" \
  "ds_write_b128 v220, v[136:139] offset:0\n\t" \
  "ds_write_b128 v220, v[140:143] offset:1024\n\t" \
  "s_waitcnt lgkmcnt(8)\n\t" \
  "v_mfma_f32_16x16x32_bf16 v[144:147], v[16:19], v[192:195], v[144:147]\n\t" \
  "v_mfma_f32_16x16x32_bf16 v[148:151], v[20:23], v[192:195], v[148:151]\n\t" \
  "ds_read_b128 v[192:195], v214 offset:256\n\t" \
  "s_add_u32 m0, %[lw], 3072\n\t" \
  "v_mfma_f32_16x16x32_bf16 v[152:155], v[16:19], v[196:199], v[152:155]\n\t" \
  "v_mfma_f32_16x16x32_bf16 v[156:159], v[20:23], v[196:199], v[156:159]\n\t" \
  "ds_read_b128 v[196:199], v214 offset:16640\n\t" \
  "global_load_lds_dwordx4 v219, s[92:93] sc1\n\t" \
  "s_lshr_b32 s90, s87, 7\n\t" \
  "s_xor_b32 s90, s90, %[s0]\n\t" \
  "s_and_b32 s90, s90, 1\n\t" \
  "s_lshl1_add_u32 s90, s87, s90\n\t" \
  "s_lshl_b32 s90, s90, 13\n\t" \
  "s_cmp_lt_i32 s86, 6\n\t" \
  "s_cselect_b32 s90, 0x70000000, s90\n\t" \
  "s_waitcnt lgkmcnt(8)\n\t" \
  "v_mfma_f32_16x16x32_bf16 v[144:147], v[24:27], v[200:203], v[144:147]\n\t" \
  "v_mfma_f32_16x16x32_bf16 v[148:151], v[28:31], v[200:203], v[148:151]\n\t" \
  "ds_read_b128 v[200:203], v215 offset:256\n\t" \
  "v_mfma_f32_16x16x32_bf16 v[152:155], v[24:27], v[204:207], v[152:155]\n\t" \
  "v_mfma_f32_16x16x32_bf16 v[156:159], v[28:31], v[204:207], v[156:159]\n\t" \
  "ds_read_b128 v[204:207], v215 offset:16640\n\t" \
  "s_waitcnt lgkmcnt(4)\n\t" \
  "s_cmp_lt_u32 s86, 6\n\t" \
  "s_cbranch_scc1 .Lw20%=\n\t" \
  "s_waitcnt vmcnt(4)\n\t" \
  "s_barrier\n\t" \
  ".Lg20%=:\n\t" \
  "s_add_u32 s92, s92, 512\n\t" \
  "s_addc_u32 s93, s93, 0\n\t" \
  "v_mfma_f32_16x16x32_bf16 v[144:147], v[32:35], v[176:179], v[144:147]\n\t" \
  "v_mfma_f32_16x16x32_bf16 v[148:151], v[36:39], v[176:179], v[148:151]\n\t" \
  "ds_read_b128 v[176:179], v212 offset:32768\n\t" \
  "s_add_u32 m0, %[lw], 32768\n\t" \
  "v_mfma_f32_16x16x32_bf16 v[152:155], v[32:35], v[180:183], v[152:155]\n\t" \
  "v_mfma_f32_16x16x32_bf16 v[156:159], v[36:39], v[180:183], v[156:159]\n\t" \
  "ds_read_b128 v[180:183], v212 offset:49152\n\t" \
  "global_load_lds_dwordx4 v216, s[92:93] sc1\n\t" \
  "ds_read_b128 v[160:163], v221 offset:0\n\t" \
  "ds_read_b128 v[164:167], v221 offset:1024\n\t" \
  "v_mfma_f32_16x16x32_bf16 v[144:147], v[40:43], v[184:187], v[144:147]\n\t" \
  "v_mfma_f32_16x16x32_bf16 v[148:151], v[44:47], v[184:187], v[148:151]\n\t" \
  "ds_read_b128 v[184:187], v213 offset:32768\n\t" \
  "v_mfma_f32_16x16x32_bf16 v[152:155], v[40:43], v[188:191], v[152:155]\n\t" \
  "v_mfma_f32_16x16x32_bf16 v[156:159], v[44:47], v[188:191], v[156:159]\n\t" \
  "ds_read_b128 v[188:191], v213 offset:49152\n\t" \
  "s_waitcnt lgkmcnt(8)\n\t" \
  "v_mfma_f32_16x16x32_bf16 v[144:147], v[48:51], v[192:195], v[144:147]\n\t" \
  "v_mfma_f32_16x16x32_bf16 v[148:151], v[52:55], v[192:195], v[148:151]\n\t" \
  "ds_read_b128 v[192:195], v214 offset:32768\n\t" \
  "s_add_u32 m0, %[lw], 33792\n\t" \
  "v_mfma_f32_16x16x32_bf16 v[152:155], v[48:51], v[196:199], v[152:155]\n\t" \
  "v_mfma_f32_16x16x32_bf16 v[156:159], v[52:55], v[196:199], v[156:159]\n\t" \
  "ds_read_b128 v[196:199], v214 offset:49152\n\t" \
  "global_load_lds_dwordx4 v217, s[92:93] sc1\n\t" \
  "s_waitcnt lgkmcnt(4)\n\t" \
  "v_add_f32 v160, v160, v128\n\t" \
  "v_add_f32 v161, v161, v129\n\t" \
  "v_add_f32 v162, v162, v130\n\t" \
  "v_add_f32 v163, v163, v131\n\t" \
  "v_add_f32 v164, v164, v132\n\t" \
  "v_add_f32 v165, v165, v133\n\t" \
  "v_add_f32 v166, v166, v134\n\t" \
  "v_add_f32 v167, v167, v135\n\t" \
  "v_mfma_f32_16x16x32_bf16 v[144:147], v[56:59], v[200:203], v[144:147]\n\t" \
  "v_mfma_f32_16x16x32_bf16 v[148:151], v[60:63], v[200:203], v[148:151]\n\t" \
  "ds_read_b128 v[200:203], v215 offset:32768\n\t" \
  "v_mfma_f32_16x16x32_bf16 v[152:155], v[56:59], v[204:207], v[152:155]\n\t" \
  "v_mfma_f32_16x16x32_bf16 v[156:159], v[60:63], v[204:207], v[156:159]\n\t" \
  "ds_read_b128 v[204:207], v215 offset:49152\n\t" \
  "v_cvt_pk_bf16_f32 v168, v160, v161\n\t" \
  "v_cvt_pk_bf16_f32 v169, v162, v163\n\t" \
  "v_cvt_pk_bf16_f32 v170, v164, v165\n\t" \
  "v_cvt_pk_bf16_f32 v171, v166, v167\n\t" \
  "buffer_store_dwordx4 v[168:171], v172, %[rt], s90 offen sc1\n\t" \
  "v_mfma_f32_16x16x32_bf16 v[144:147], v[64:67], v[176:179], v[144:147]\n\t" \
  "v_mfma_f32_16x16x32_bf16 v[148:151], v[68:71], v[176:179], v[148:151]\n\t" \
  "ds_read_b128 v[176:179], v212 offset:33024\n\t" \
  "s_add_u32 m0, %[lw], 34816\n\t" \
  "v_mfma_f32_16x16x32_bf16 v[152:155], v[64:67], v[180:183], v[152:155]\n\t" \
  "v_mfma_f32_16x16x32_bf16 v[156:159], v[68:71], v[180:183], v[156:159]\n\t" \
  "ds_read_b128 v[180:183], v212 offset:49408\n\t" \
  "global_load_lds_dwordx4 v218, s[92:93] sc1\n\t" \
  "s_waitcnt lgkmcnt(6)\n\t" \
  "v_mfma_f32_16x16x32_bf16 v[144:147], v[72:75], v[184:187], v[144:147]\n\t" \
  "v_mfma_f32_16x16x32_bf16 v[148:151], v[76:79], v[184:187], v[148:151]\n\t" \
  "ds_read_b128 v[184:187], v213 offset:33024\n\t" \
  "v_mfma_f32_16x16x32_bf16 v[152:155], v[72:75], v[188:191], v[152:155]\n\t" \
  "v_mfma_f32_16x16x32_bf16 v[156:159], v[76:79], v[188:191], v[156:159]\n\t" \
  "ds_read_b128 v[188:191], v213 offset:49408\n\t" \
  "s_waitcnt lgkmcnt(6)\n\t" \
  "v_mfma_f32_16x16x32_bf16 v[144:147], v[80:83], v[192:195], v[144:147]\n\t" \
  "v_mfma_f32_16x16x32_bf16 v[148:151], v[84:87], v[192:195], v[148:151]\n\t" \
  "ds_read_b128 v[192:195], v214 offset:33024\n\t" \
  "s_add_u32 m0, %[lw], 35840\n\t" \
  "v_mfma_f32_16x16x32_bf16 v[152:155], v[80:83], v[196:199], v[152:155]\n\t" \
  "v_mfma_f32_16x16x32_bf16 v[156:159], v[84:87], v[196:199], v[156:159]\n\t" \
  "ds_read_b128 v[196:199], v214 offset:49408\n\t" \
  "global_load_lds_dwordx4 v219, s[92:93] sc1\n\t" \
  "s_waitcnt lgkmcnt(6)\n\t" \
  "v_mfma_f32_16x16x32_bf16 v[144:147], v[88:91], v[200:203], v[144:147]\n\t" \
  "v_mfma_f32_16x16x32_bf16 v[148:151], v[92:95], v[200:203], v[148:151]\n\t" \
  "ds_read_b128 v[200:203], v215 offset:33024\n\t" \
  "v_mfma_f32_16x16x32_bf16 v[152:155], v[88:91], v[204:207], v[152:155]\n\t" \
  "v_mfma_f32_16x16x32_bf16 v[156:159], v[92:95], v[204:207], v[156:159]\n\t" \
  "ds_read_b128 v[204:207], v215 offset:49408\n\t" \
  "s_cmp_lt_u32 s86, 8\n\t" \
  "s_cbranch_scc1 .Lw30%=\n\t" \
  "s_waitcnt vmcnt(5)\n\t" \
  "s_barrier\n\t" \
  ".Lg30%=:\n\t" \
  "s_add_u32 s92, s92, 65024\n\t" \
  "s_addc_u32 s93, s93, 0\n\t" \
  "s_waitcnt lgkmcnt(6)\n\t" \
  "v_mfma_f32_16x16x32_bf16 v[144:147], v[96:99], v[176:179], v[144:147]\n\t" \
  "v_mfma_f32_16x16x32_bf16 v[148:151], v[100:103], v[176:179], v[148:151]\n\t" \
  "ds_read_b128 v[176:179], v208 offset:0\n\t" \
  "s_add_u32 m0, %[lw], 65536\n\t" \
  "v_mfma_f32_16x16x32_bf16 v[152:155], v[96:99], v[180:183], v[152:155]\n\t" \
  "v_mfma_f32_16x16x32_bf16 v[156:159], v[100:103], v[180:183], v[156:159]\n\t" \
  "ds_read_b128 v[180:183], v208 offset:16384\n\t" \
  "global_load_lds_dwordx4 v216, s[92:93] sc1\n\t" \
  "s_waitcnt lgkmcnt(6)\n\t" \
  "v_mfma_f32_16x16x32_bf16 v[144:147], v[104:107], v[184:187], v[144:147]\n\t" \
  "v_mfma_f32_16x16x32_bf16 v[148:151], v[108:111], v[184:187], v[148:151]\n\t" \
  "ds_read_b128 v[184:187], v209 offset:0\n\t" \
  "v_mfma_f32_16x16x32_bf16 v[152:155], v[104:107], v[188:191], v[152:155]\n\t" \
  "v_mfma_f32_16x16x32_bf16 v[156:159], v[108:111], v[188:191], v[156:159]\n\t" \
  "ds_read_b128 v[188:191], v209 offset:16384\n\t" \
  "s_waitcnt lgkmcnt(6)\n\t" \
  "v_mfma_f32_16x16x32_bf16 v[144:147], v[112:115], v[192:195], v[144:147]\n\t" \
  "v_mfma_f32_16x16x32_bf16 v[148:151], v[116:119], v[192:195], v[148:151]\n\t" \
  "ds_read_b128 v[192:195], v210 offset:0\n\t" \
  "s_add_u32 m0, %[lw], 66560\n\t" \
  "v_mfma_f32_16x16x32_bf16 v[152:155], v[112:115], v[196:199], v[152:155]\n\t" \
  "v_mfma_f32_16x16x32_bf16 v[156:159], v[116:119], v[196:199], v[156:159]\n\t" \
  "ds_read_b128 v[196:199], v210 offset:16384\n\t" \
  "global_load_lds_dwordx4 v217, s[92:93] sc1\n\t" \
  "s_waitcnt lgkmcnt(6)\n\t" \
  "v_mfma_f32_16x16x32_bf16 v[144:147], v[120:123], v[200:203], v[144:147]\n\t" \
  "v_mfma_f32_16x16x32_bf16 v[148:151], v[124:127], v[200:203], v[148:151]\n\t" \
  "ds_read_b128 v[200:203], v211 offset:0\n\t" \
  "v_mfma_f32_16x16x32_bf16 v[152:155], v[120:123], v[204:207], v[152:155]\n\t" \
  "v_mfma_f32_16x16x32_bf16 v[156:159], v[124:127], v[204:207], v[156:159]\n\t" \
  "ds_read_b128 v[204:207], v211 offset:16384\n\t" \
  "s_add_u32 s86, s86, 1\n\t" \
  "s_add_u32 s87, s87, 1\n\t" \
  "s_cmp_lt_u32 s86, %[end]\n\t" \
  "s_cbranch_scc1 .Lp0%=\n\t" \
  "s_branch .Lx0%=\n\t" \
  ".Lf00%=:\n\t" \
  "s_nop 7\n\t" \
  "ds_write_b128 v220, v[152:155] offset:0\n\t" \
  "ds_write_b128 v220, v[156:159] offset:1024\n\t" \
  "s_lshr_b32 s90, s87, 7\n\t" \
  "s_xor_b32 s90, s90, %[s0]\n\t" \
  "s_and_b32 s90, s90, 1\n\t" \
  "s_lshl1_add_u32 s90, s87, s90\n\t" \
  "s_lshl_b32 s90, s90, 13\n\t" \
  "s_cmp_lt_i32 s86, 1\n\t" \
  "s_cselect_b32 s90, 0x70000000, s90\n\t" \
  "s_waitcnt lgkmcnt(0)\n\t" \
  "s_barrier\n\t" \
  "ds_read_b128 v[160:163], v221 offset:0\n\t" \
  "ds_read_b128 v[164:167], v221 offset:1024\n\t" \
  "s_waitcnt lgkmcnt(0)\n\t" \
  "v_add_f32 v160, v160, v144\n\t" \
  "v_add_f32 v161, v161, v145\n\t" \
  "v_add_f32 v162, v162, v146\n\t" \
  "v_add_f32 v163, v163, v147\n\t" \
  "v_add_f32 v164, v164, v148\n\t" \
  "v_add_f32 v165, v165, v149\n\t" \
  "v_add_f32 v166, v166, v150\n\t" \
  "v_add_f32 v167, v167, v151\n\t" \
  "v_cvt_pk_bf16_f32 v168, v160, v161\n\t" \
  "v_cvt_pk_bf16_f32 v169, v162, v163\n\t" \
  "v_cvt_pk_bf16_f32 v170, v164, v165\n\t" \
  "v_cvt_pk_bf16_f32 v171, v166, v167\n\t" \
  "buffer_store_dwordx4 v[168:171], v172, %[rt], s90 offen sc1\n\t" \
  "s_branch .Lfb00%=\n\t" \
  ".Lf10%=:\n\t" \
  "s_nop 7\n\t" \
  "ds_write_b128 v220, v[136:139] offset:0\n\t" \
  "ds_write_b128 v220, v[140:143] offset:1024\n\t" \
  "s_lshr_b32 s90, s87, 7\n\t" \
  "s_xor_b32 s90, s90, %[s0]\n\t" \
  "s_and_b32 s90, s90, 1\n\t" \
  "s_lshl1_add_u32 s90, s87, s90\n\t" \
  "s_lshl_b32 s90, s90, 13\n\t" \
  "s_cmp_lt_i32 s86, 1\n\t" \
  "s_cselect_b32 s90, 0x70000000, s90\n\t" \
  "s_waitcnt lgkmcnt(0)\n\t" \
  "s_barrier\n\t" \
  "ds_read_b128 v[160:163], v221 offset:0\n\t" \
  "ds_read_b128 v[164:167], v221 offset:1024\n\t" \
  "s_waitcnt lgkmcnt(0)\n\t" \
  "v_add_f32 v160, v160, v128\n\t" \
  "v_add_f32 v161, v161, v129\n\t" \
  "v_add_f32 v162, v162, v130\n\t" \
  "v_add_f32 v163, v163, v131\n\t" \
  "v_add_f32 v164, v164, v132\n\t" \
  "v_add_f32 v165, v165, v133\n\t" \
  "v_add_f32 v166, v166, v134\n\t" \
  "v_add_f32 v167, v167, v135\n\t" \
  "v_cvt_pk_bf16_f32 v168, v160, v161\n\t" \
  "v_cvt_pk_bf16_f32 v169, v162, v163\n\t" \
  "v_cvt_pk_bf16_f32 v170, v164, v165\n\t" \
  "v_cvt_pk_bf16_f32 v171, v166, v167\n\t" \
  "buffer_store_dwordx4 v[168:171], v172, %[rt], s90 offen sc1\n\t" \
  "s_branch .Lfb10%=\n\t" \
  ".Lw00%=:\n\t" \
  "s_waitcnt vmcnt(2)\n\t" \
  "s_barrier\n\t" \
  "s_branch .Lg00%=\n\t" \
  ".Lw10%=:\n\t" \
  "s_waitcnt vmcnt(2)\n\t" \
  "s_barrier\n\t" \
  "s_branch .Lg10%=\n\t" \
  ".Lw20%=:\n\t" \
  "s_waitcnt vmcnt(2)\n\t" \
  "s_barrier\n\t" \
  "s_branch .Lg20%=\n\t" \
  ".Lw30%=:\n\t" \
  "s_waitcnt vmcnt(2)\n\t" \
  "s_barrier\n\t" \
  "s_branch .Lg30%=\n\t" \
  ".Lx0%=:\n\t" \
  "s_waitcnt lgkmcnt(0)\n\t" \
  "s_setprio 0\n\t" \
  "s_mov_b32 m0, s94\n\t" \
  "s_branch .Le%=\n\t" \
  ".Lk%=:\n\t" \
  "s_mov_b32 s94, m0\n\t" \
  "s_mov_b64 s[92:93], %[x0]\n\t" \
  "s_mov_b32 s86, %[g0]\n\t" \
  "s_add_u32 s87, s86, %[t0]\n\t" \
  "s_sub_i32 s88, s86, 3\n\t" \
  "ds_read_b128 v[176:179], v208 offset:0\n\t" \
  "ds_read_b128 v[180:183], v208 offset:16384\n\t" \
  "ds_read_b128 v[184:187], v209 offset:0\n\t" \
  "ds_read_b128 v[188:191], v209 offset:16384\n\t" \
  "ds_read_b128 v[192:195], v210 offset:0\n\t" \
  "ds_read_b128 v[196:199], v210 offset:16384\n\t" \
  "ds_read_b128 v[200:203], v211 offset:0\n\t" \
  "ds_read_b128 v[204:207], v211 offset:16384\n\t" \
  "s_branch .Lfb01%=\n\t" \
  ".Lp1%=:\n\t" \
  "s_cmp_le_u32 s86, 5\n\t" \
  "s_cbranch_scc1 .Lf01%=\n\t" \
  ".Lfb01%=:\n\t" \
  "s_waitcnt lgkmcnt(6)\n\t" \
  "v_mfma_f32_16x16x32_bf16 v[128:131], v[0:3], v[176:179], 0\n\t" \
  "v_mfma_f32_16x16x32_bf16 v[132:135], v[4:7], v[176:179], 0\n\t" \
  "ds_read_b128 v[176:179], v208 offset:256\n\t" \
  "v_mfma_f32_16x16x32_bf16 v[136:139], v[0:3], v[180:183], 0\n\t" \
  "v_mfma_f32_16x16x32_bf16 v[140:143], v[4:7], v[180:183], 0\n\t" \
  "ds_read_b128 v[180:183], v208 offset:16640\n\t" \
  "s_waitcnt lgkmcnt(6)\n\t" \
  "v_mfma_f32_16x16x32_bf16 v[128:131], v[8:11], v[184:187], v[128:131]\n\t" \
  "v_mfma_f32_16x16x32_bf16 v[132:135], v[12:15], v[184:187], v[132:135]\n\t" \
  "ds_read_b128 v[184:187], v209 offset:256\n\t" \
  "s_add_u32 m0, %[lw], 67584\n\t" \
  "v_mfma_f32_16x16x32_bf16 v[136:139], v[8:11], v[188:191], v[136:139]\n\t" \
  "v_mfma_f32_16x16x32_bf16 v[140:143], v[12:15], v[188:191], v[140:143]\n\t" \
  "ds_read_b128 v[188:191], v209 offset:16640\n\t" \
  "global_load_lds_dwordx4 v218, s[92:93] sc1\n\t" \
  "ds_write_b128 v220, v[152:155] offset:0\n\t" \
  "ds_write_b128 v220, v[156:159] offset:1024\n\t" \
  "s_waitcnt lgkmcnt(8)\n\t" \
  "v_mfma_f32_16x16x32_bf16 v[128:131], v[16:19], v[192:195], v[128:131]\n\t" \
  "v_mfma_f32_16x16x32_bf16 v[132:135], v[20:23], v[192:195], v[132:135]\n\t" \
  "ds_read_b128 v[192:195], v210 offset:256\n\t" \
  "v_mfma_f32_16x16x32_bf16 v[136:139], v[16:19], v[196:199], v[136:139]\n\t" \
  "v_mfma_f32_16x16x32_bf16 v[140:143], v[20:23], v[196:199], v[140:143]\n\t" \
  "ds_read_b128 v[196:199], v210 offset:16640\n\t" \
  "s_lshr_b32 s90, s87, 7\n\t" \
  "s_xor_b32 s90, s90, %[s0]\n\t" \
  "s_and_b32 s90, s90, 1\n\t" \
  "s_lshl1_add_u32 s90, s87, s90\n\t" \
  "s_lshl_b32 s90, s90, 13\n\t" \
  "s_cmp_lt_i32 s86, 6\n\t" \
  "s_cselect_b32 s90, 0x70000000, s90\n\t" \
  "s_waitcnt lgkmcnt(8)\n\t" \
  "v_mfma_f32_16x16x32_bf16 v[128:131], v[24:27], v[200:203], v[128:131]\n\t" \
  "v_mfma_f32_16x16x32_bf16 v[132:135], v[28:31], v[200:203], v[132:135]\n\t" \
  "ds_read_b128 v[200:203], v211 offset:256\n\t" \
  "s_add_u32 m0, %[lw], 68608\n\t" \
  "v_mfma_f32_16x16x32_bf16 v[136:139], v[24:27], v[204:207], v[136:139]\n\t" \
  "v_mfma_f32_16x16x32_bf16 v[140:143], v[28:31], v[204:207], v[140:143]\n\t" \
  "ds_read_b128 v[204:207], v211 offset:16640\n\t" \
  "global_load_lds_dwordx4 v219, s[92:93] sc1\n\t" \
  "s_waitcnt lgkmcnt(4)\n\t" \
  "s_cmp_lt_u32 s86, 6\n\t" \
  "s_cbranch_scc1 .Lw01%=\n\t" \
  "s_waitcnt vmcnt(4)\n\t" \
  "s_barrier\n\t" \
  ".Lg01%=:\n\t" \
  "s_add_u32 s92, s92, 512\n\t" \
  "s_addc_u32 s93, s93, 0\n\t" \
  "v_mfma_f32_16x16x32_bf16 v[128:131], v[32:35], v[176:179], v[128:131]\n\t" \
  "v_mfma_f32_16x16x32_bf16 v[132:135], v[36:39], v[176:179], v[132:135]\n\t" \
  "ds_read_b128 v[176:179], v208 offset:32768\n\t" \
  "v_mfma_f32_16x16x32_bf16 v[136:139], v[32:35], v[180:183], v[136:139]\n\t" \
  "v_mfma_f32_16x16x32_bf16 v[140:143], v[36:39], v[180:183], v[140:143]\n\t" \
  "ds_read_b128 v[180:183], v208 offset:49152\n\t" \
  "ds_read_b128 v[160:163], v221 offset:0\n\t" \
  "ds_read_b128 v[164:167], v221 offset:1024\n\t" \
  "s_sub_i32 s91, s86, 2\n\t" \
  "s_max_i32 s88, s88, s91\n\t" \
  "v_mov_b32 v173, s88\n\t" \
  "buffer_store_dword v173, v174, %[rs], 0 offen sc1\n\t" \
  "v_mfma_f32_16x16x32_bf16 v[128:131], v[40:43], v[184:187], v[128:131]\n\t" \
  "v_mfma_f32_16x16x32_bf16 v[132:135], v[44:47], v[184:187], v[132:135]\n\t" \
  "ds_read_b128 v[184:187], v209 offset:32768\n\t" \
  "s_add_u32 m0, %[lw], 98304\n\t" \
  "v_mfma_f32_16x16x32_bf16 v[136:139], v[40:43], v[188:191], v[136:139]\n\t" \
  "v_mfma_f32_16x16x32_bf16 v[140:143], v[44:47], v[188:191], v[140:143]\n\t" \
  "ds_read_b128 v[188:191], v209 offset:49152\n\t" \
  "global_load_lds_dwordx4 v216, s[92:93] sc1\n\t" \
  "s_waitcnt lgkmcnt(8)\n\t" \
  "v_mfma_f32_16x16x32_bf16 v[128:131], v[48:51], v[192:195], v[128:131]\n\t" \
  "v_mfma_f32_16x16x32_bf16 v[132:135], v[52:55], v[192:195], v[132:135]\n\t" \
  "ds_read_b128 v[192:195], v210 offset:32768\n\t" \
  "v_mfma_f32_16x16x32_bf16 v[136:139], v[48:51], v[196:199], v[136:139]\n\t" \
  "v_mfma_f32_16x16x32_bf16 v[140:143], v[52:55], v[196:199], v[140:143]\n\t" \
  "ds_read_b128 v[196:199], v210 offset:49152\n\t" \
  "s_waitcnt lgkmcnt(4)\n\t" \
  "v_add_f32 v160, v160, v144\n\t" \
  "v_add_f32 v161, v161, v145\n\t" \
  "v_add_f32 v162, v162, v146\n\t" \
  "v_add_f32 v163, v163, v147\n\t" \
  "v_add_f32 v164, v164, v148\n\t" \
  "v_add_f32 v165, v165, v149\n\t" \
  "v_add_f32 v166, v166, v150\n\t" \
  "v_add_f32 v167, v167, v151\n\t" \
  "v_mfma_f32_16x16x32_bf16 v[128:131], v[56:59], v[200:203], v[128:131]\n\t" \
  "v_mfma_f32_16x16x32_bf16 v[132:135], v[60:63], v[200:203], v[132:135]\n\t" \
  "ds_read_b128 v[200:203], v211 offset:32768\n\t" \
  "s_add_u32 m0, %[lw], 99328\n\t" \
  "v_mfma_f32_16x16x32_bf16 v[136:139], v[56:59], v[204:207], v[136:139]\n\t" \
  "v_mfma_f32_16x16x32_bf16 v[140:143], v[60:63], v[204:207], v[140:143]\n\t" \
  "ds_read_b128 v[204:207], v211 offset:49152\n\t" \
  "global_load_lds_dwordx4 v217, s[92:93] sc1\n\t" \
  "v_cvt_pk_bf16_f32 v168, v160, v161\n\t" \
  "v_cvt_pk_bf16_f32 v169, v162, v163\n\t" \
  "v_cvt_pk_bf16_f32 v170, v164, v165\n\t" \
  "v_cvt_pk_bf16_f32 v171, v166, v167\n\t" \
  "buffer_store_dwordx4 v[168:171], v172, %[rt], s90 offen sc1\n\t" \
  "v_mfma_f32_16x16x32_bf16 v[128:131], v[64:67], v[176:179], v[128:131]\n\t" \
  "v_mfma_f32_16x16x32_bf16 v[132:135], v[68:71], v[176:179], v[132:135]\n\t" \
  "ds_read_b128 v[176:179], v208 offset:33024\n\t" \
  "v_mfma_f32_16x16x32_bf16 v[136:139], v[64:67], v[180:183], v[136:139]\n\t" \
  "v_mfma_f32_16x16x32_bf16 v[140:143], v[68:71], v[180:183], v[140:143]\n\t" \
  "ds_read_b128 v[180:183], v208 offset:49408\n\t" \
  "s_waitcnt lgkmcnt(6)\n\t" \
  "v_mfma_f32_16x16x32_bf16 v[128:131], v[72:75], v[184:187], v[128:131]\n\t" \
  "v_mfma_f32_16x16x32_bf16 v[132:135], v[76:79], v[184:187], v[132:135]\n\t" \
  "ds_read_b128 v[184:187], v209 offset:33024\n\t" \
  "s_add_u32 m0, %[lw], 100352\n\t" \
  "v_mfma_f32_16x16x32_bf16 v[136:139], v[72:75], v[188:191], v[136:139]\n\t" \
  "v_mfma_f32_16x16x32_bf16 v[140:143], v[76:79], v[188:191], v[140:143]\n\t" \
  "ds_read_b128 v[188:191], v209 offset:49408\n\t" \
  "global_load_lds_dwordx4 v218, s[92:93] sc1\n\t" \
  "s_waitcnt lgkmcnt(6)\n\t" \
  "v_mfma_f32_16x16x32_bf16 v[128:131], v[80:83], v[192:195], v[128:131]\n\t" \
  "v_mfma_f32_16x16x32_bf16 v[132:135], v[84:87], v[192:195], v[132:135]\n\t" \
  "ds_read_b128 v[192:195], v210 offset:33024\n\t" \
  "v_mfma_f32_16x16x32_bf16 v[136:139], v[80:83], v[196:199], v[136:139]\n\t" \
  "v_mfma_f32_16x16x32_bf16 v[140:143], v[84:87], v[196:199], v[140:143]\n\t" \
  "ds_read_b128 v[196:199], v210 offset:49408\n\t" \
  "s_waitcnt lgkmcnt(6)\n\t" \
  "v_mfma_f32_16x16x32_bf16 v[128:131], v[88:91], v[200:203], v[128:131]\n\t" \
  "v_mfma_f32_16x16x32_bf16 v[132:135], v[92:95], v[200:203], v[132:135]\n\t" \
  "ds_read_b128 v[200:203], v211 offset:33024\n\t" \
  "s_add_u32 m0, %[lw], 101376\n\t" \
  "v_mfma_f32_16x16x32_bf16 v[136:139], v[88:91], v[204:207], v[136:139]\n\t" \
  "v_mfma_f32_16x16x32_bf16 v[140:143], v[92:95], v[204:207], v[140:143]\n\t" \
  "ds_read_b128 v[204:207], v211 offset:49408\n\t" \
  "global_load_lds_dwordx4 v219, s[92:93] sc1\n\t" \
  "s_cmp_lt_u32 s86, 8\n\t" \
  "s_cbranch_scc1 .Lw11%=\n\t" \
  "s_waitcnt vmcnt(6)\n\t" \
  "s_barrier\n\t" \
  ".Lg11%=:\n\t" \
  "s_add_u32 s92, s92, 65024\n\t" \
  "s_addc_u32 s93, s93, 0\n\t" \
  "s_waitcnt lgkmcnt(6)\n\t" \
  "v_mfma_f32_16x16x32_bf16 v[128:131], v[96:99], v[176:179], v[128:131]\n\t" \
  "v_mfma_f32_16x16x32_bf16 v[132:135], v[100:103], v[176:179], v[132:135]\n\t" \
  "ds_read_b128 v[176:179], v212 offset:0\n\t" \
  "v_mfma_f32_16x16x32_bf16 v[136:139], v[96:99], v[180:183], v[136:139]\n\t" \
  "v_mfma_f32_16x16x32_bf16 v[140:143], v[100:103], v[180:183], v[140:143]\n\t" \
  "ds_read_b128 v[180:183], v212 offset:16384\n\t" \
  "s_waitcnt lgkmcnt(6)\n\t" \
  "v_mfma_f32_16x16x32_bf16 v[128:131], v[104:107], v[184:187], v[128:131]\n\t" \
  "v_mfma_f32_16x16x32_bf16 v[132:135], v[108:111], v[184:187], v[132:135]\n\t" \
  "ds_read_b128 v[184:187], v213 offset:0\n\t" \
  "s_add_u32 m0, %[lw], 0\n\t" \
  "v_mfma_f32_16x16x32_bf16 v[136:139], v[104:107], v[188:191], v[136:139]\n\t" \
  "v_mfma_f32_16x16x32_bf16 v[140:143], v[108:111], v[188:191], v[140:143]\n\t" \
  "ds_read_b128 v[188:191], v213 offset:16384\n\t" \
  "global_load_lds_dwordx4 v216, s[92:93] sc1\n\t" \
  "s_waitcnt lgkmcnt(6)\n\t" \
  "v_mfma_f32_16x16x32_bf16 v[128:131], v[112:115], v[192:195], v[128:131]\n\t" \
  "v_mfma_f32_16x16x32_bf16 v[132:135], v[116:119], v[192:195], v[132:135]\n\t" \
  "ds_read_b128 v[192:195], v214 offset:0\n\t" \
  "v_mfma_f32_16x16x32_bf16 v[136:139], v[112:115], v[196:199], v[136:139]\n\t" \
  "v_mfma_f32_16x16x32_bf16 v[140:143], v[116:119], v[196:199], v[140:143]\n\t" \
  "ds_read_b128 v[196:199], v214 offset:16384\n\t" \
  "s_waitcnt lgkmcnt(6)\n\t" \
  "v_mfma_f32_16x16x32_bf16 v[128:131], v[120:123], v[200:203], v[128:131]\n\t" \
  "v_mfma_f32_16x16x32_bf16 v[132:135], v[124:127], v[200:203], v[132:135]\n\t" \
  "ds_read_b128 v[200:203], v215 offset:0\n\t" \
  "s_add_u32 m0, %[lw], 1024\n\t" \
  "v_mfma_f32_16x16x32_bf16 v[136:139], v[120:123], v[204:207], v[136:139]\n\t" \
  "v_mfma_f32_16x16x32_bf16 v[140:143], v[124:127], v[204:207], v[140:143]\n\t" \
  "ds_read_b128 v[204:207], v215 offset:16384\n\t" \
  "global_load_lds_dwordx4 v217, s[92:93] sc1\n\t" \
  "s_add_u32 s86, s86, 1\n\t" \
  "s_add_u32 s87, s87, 1\n\t" \
  "s_cmp_le_u32 s86, 5\n\t" \
  "s_cbranch_scc1 .Lf11%=\n\t" \
  ".Lfb11%=:\n\t" \
  "s_waitcnt lgkmcnt(6)\n\t" \
  "v_mfma_f32_16x16x32_bf16 v[144:147], v[0:3], v[176:179], 0\n\t" \
  "v_mfma_f32_16x16x32_bf16 v[148:151], v[4:7], v[176:179], 0\n\t" \
  "ds_read_b128 v[176:179], v212 offset:256\n\t" \
  "v_mfma_f32_16x16x32_bf16 v[152:155], v[0:3], v[180:183], 0\n\t" \
  "v_mfma_f32_16x16x32_bf16 v[156:159], v[4:7], v[180:183], 0\n\t" \
  "ds_read_b128 v[180:183], v212 offset:16640\n\t" \
  "s_waitcnt lgkmcnt(6)\n\t" \
  "v_mfma_f32_16x16x32_bf16 v[144:147], v[8:11], v[184:187], v[144:147]\n\t" \
  "v_mfma_f32_16x16x32_bf16 v[148:151], v[12:15], v[184:187], v[148:151]\n\t" \
  "ds_read_b128 v[184:187], v213 offset:256\n\t" \
  "s_add_u32 m0, %[lw], 2048\n\t" \
  "v_mfma_f32_16x16x32_bf16 v[152:155], v[8:11], v[188:191], v[152:155]\n\t" \
  "v_mfma_f32_16x16x32_bf16 v[156:159], v[12:15], v[188:191], v[156:159]\n\t" \
  "ds_read_b128 v[188:191], v213 offset:16640\n\t" \
  "global_load_lds_dwordx4 v218, s[92:93] sc1\n\t" \
  "ds_write_b128 v220, v[136:139] offset:0\n\t" \
  "ds_write_b128 v220, v[140:143] offset:1024\n\t" \
  "s_waitcnt lgkmcnt(8)\n\t" \
  "v_mfma_f32_16x16x32_bf16 v[144:147], v[16:19], v[192:195], v[144:147]\n\t" \
  "v_mfma_f32_16x16x32_bf16 v[148:151], v[20:23], v[192:195], v[148:151]\n\t" \
  "ds_read_b128 v[192:195], v214 offset:256\n\t" \
  "v_mfma_f32_16x16x32_bf16 v[152:155], v[16:19], v[196:199], v[152:155]\n\t" \
  "v_mfma_f32_16x16x32_bf16 v[156:159], v[20:23], v[196:199], v[156:159]\n\t" \
  "ds_read_b128 v[196:199], v214 offset:16640\n\t" \
  "s_lshr_b32 s90, s87, 7\n\t" \
  "s_xor_b32 s90, s90, %[s0]\n\t" \
  "s_and_b32 s90, s90, 1\n\t" \
  "s_lshl1_add_u32 s90, s87, s90\n\t" \
  "s_lshl_b32 s90, s90, 13\n\t" \
  "s_cmp_lt_i32 s86, 6\n\t" \
  "s_cselect_b32 s90, 0x70000000, s90\n\t" \
  "s_waitcnt lgkmcnt(8)\n\t" \
  "v_mfma_f32_16x16x32_bf16 v[144:147], v[24:27], v[200:203], v[144:147]\n\t" \
  "v_mfma_f32_16x16x32_bf16 v[148:151], v[28:31], v[200:203], v[148:151]\n\t" \
  "ds_read_b128 v[200:203], v215 offset:256\n\t" \
  "s_add_u32 m0, %[lw], 3072\n\t" \
  "v_mfma_f32_16x16x32_bf16 v[152:155], v[24:27], v[204:207], v[152:155]\n\t" \
  "v_mfma_f32_16x16x32_bf16 v[156:159], v[28:31], v[204:207], v[156:159]\n\t" \
  "ds_read_b128 v[204:207], v215 offset:16640\n\t" \
  "global_load_lds_dwordx4 v219, s[92:93] sc1\n\t" \
  "s_waitcnt lgkmcnt(4)\n\t" \
  "s_cmp_lt_u32 s86, 6\n\t" \
  "s_cbranch_scc1 .Lw21%=\n\t" \
  "s_waitcnt vmcnt(4)\n\t" \
  "s_barrier\n\t" \
  ".Lg21%=:\n\t" \
  "s_add_u32 s92, s92, 512\n\t" \
  "s_addc_u32 s93, s93, 0\n\t" \
  "v_mfma_f32_16x16x32_bf16 v[144:147], v[32:35], v[176:179], v[144:147]\n\t" \
  "v_mfma_f32_16x16x32_bf16 v[148:151], v[36:39], v[176:179], v[148:151]\n\t" \
  "ds_read_b128 v[176:179], v212 offset:32768\n\t" \
  "v_mfma_f32_16x16x32_bf16 v[152:155], v[32:35], v[180:183], v[152:155]\n\t" \
  "v_mfma_f32_16x16x32_bf16 v[156:159], v[36:39], v[180:183], v[156:159]\n\t" \
  "ds_read_b128 v[180:183], v212 offset:49152\n\t" \
  "ds_read_b128 v[160:163], v221 offset:0\n\t" \
  "ds_read_b128 v[164:167], v221 offset:1024\n\t" \
  "s_sub_i32 s91, s86, 2\n\t" \
  "s_max_i32 s88, s88, s91\n\t" \
  "v_mov_b32 v173, s88\n\t" \
  "buffer_store_dword v173, v174, %[rs], 0 offen sc1\n\t" \
  "v_mfma_f32_16x16x32_bf16 v[144:147], v[40:43], v[184:187], v[144:147]\n\t" \
  "v_mfma_f32_16x16x32_bf16 v[148:151], v[44:47], v[184:187], v[148:151]\n\t" \
  "ds_read_b128 v[184:187], v213 offset:32768\n\t" \
  "s_add_u32 m0, %[lw], 32768\n\t" \
  "v_mfma_f32_16x16x32_bf16 v[152:155], v[40:43], v[188:191], v[152:155]\n\t" \
  "v_mfma_f32_16x16x32_bf16 v[156:159], v[44:47], v[188:191], v[156:159]\n\t" \
  "ds_read_b128 v[188:191], v213 offset:49152\n\t" \
  "global_load_lds_dwordx4 v216, s[92:93] sc1\n\t" \
  "s_waitcnt lgkmcnt(8)\n\t" \
  "v_mfma_f32_16x16x32_bf16 v[144:147], v[48:51], v[192:195], v[144:147]\n\t" \
  "v_mfma_f32_16x16x32_bf16 v[148:151], v[52:55], v[192:195], v[148:151]\n\t" \
  "ds_read_b128 v[192:195], v214 offset:32768\n\t" \
  "v_mfma_f32_16x16x32_bf16 v[152:155], v[48:51], v[196:199], v[152:155]\n\t" \
  "v_mfma_f32_16x16x32_bf16 v[156:159], v[52:55], v[196:199], v[156:159]\n\t" \
  "ds_read_b128 v[196:199], v214 offset:49152\n\t" \
  "s_waitcnt lgkmcnt(4)\n\t" \
  "v_add_f32 v160, v160, v128\n\t" \
  "v_add_f32 v161, v161, v129\n\t" \
  "v_add_f32 v162, v162, v130\n\t" \
  "v_add_f32 v163, v163, v131\n\t" \
  "v_add_f32 v164, v164, v132\n\t" \
  "v_add_f32 v165, v165, v133\n\t" \
  "v_add_f32 v166, v166, v134\n\t" \
  "v_add_f32 v167, v167, v135\n\t" \
  "v_mfma_f32_16x16x32_bf16 v[144:147], v[56:59], v[200:203], v[144:147]\n\t" \
  "v_mfma_f32_16x16x32_bf16 v[148:151], v[60:63], v[200:203], v[148:151]\n\t" \
  "ds_read_b128 v[200:203], v215 offset:32768\n\t" \
  "s_add_u32 m0, %[lw], 33792\n\t" \
  "v_mfma_f32_16x16x32_bf16 v[152:155], v[56:59], v[204:207], v[152:155]\n\t" \
  "v_mfma_f32_16x16x32_bf16 v[156:159], v[60:63], v[204:207], v[156:159]\n\t" \
  "ds_read_b128 v[204:207], v215 offset:49152\n\t" \
  "global_load_lds_dwordx4 v217, s[92:93] sc1\n\t" \
  "v_cvt_pk_bf16_f32 v168, v160, v161\n\t" \
  "v_cvt_pk_bf16_f32 v169, v162, v163\n\t" \
  "v_cvt_pk_bf16_f32 v170, v164, v165\n\t" \
  "v_cvt_pk_bf16_f32 v171, v166, v167\n\t" \
  "buffer_store_dwordx4 v[168:171], v172, %[rt], s90 offen sc1\n\t" \
  "v_mfma_f32_16x16x32_bf16 v[144:147], v[64:67], v[176:179], v[144:147]\n\t" \
  "v_mfma_f32_16x16x32_bf16 v[148:151], v[68:71], v[176:179], v[148:151]\n\t" \
  "ds_read_b128 v[176:179], v212 offset:33024\n\t" \
  "v_mfma_f32_16x16x32_bf16 v[152:155], v[64:67], v[180:183], v[152:155]\n\t" \
  "v_mfma_f32_16x16x32_bf16 v[156:159], v[68:71], v[180:183], v[156:159]\n\t" \
  "ds_read_b128 v[180:183], v212 offset:49408\n\t" \
  "s_waitcnt lgkmcnt(6)\n\t" \
  "v_mfma_f32_16x16x32_bf16 v[144:147], v[72:75], v[184:187], v[144:147]\n\t" \
  "v_mfma_f32_16x16x32_bf16 v[148:151], v[76:79], v[184:187], v[148:151]\n\t" \
  "ds_read_b128 v[184:187], v213 offset:33024\n\t" \
  "s_add_u32 m0, %[lw], 34816\n\t" \
  "v_mfma_f32_16x16x32_bf16 v[152:155], v[72:75], v[188:191], v[152:155]\n\t" \
  "v_mfma_f32_16x16x32_bf16 v[156:159], v[76:79], v[188:191], v[156:159]\n\t" \
  "ds_read_b128 v[188:191], v213 offset:49408\n\t" \
  "global_load_lds_dwordx4 v218, s[92:93] sc1\n\t" \
  "s_waitcnt lgkmcnt(6)\n\t" \
  "v_mfma_f32_16x16x32_bf16 v[144:147], v[80:83], v[192:195], v[144:147]\n\t" \
  "v_mfma_f32_16x16x32_bf16 v[148:151], v[84:87], v[192:195], v[148:151]\n\t" \
  "ds_read_b128 v[192:195], v214 offset:33024\n\t" \
  "v_mfma_f32_16x16x32_bf16 v[152:155], v[80:83], v[196:199], v[152:155]\n\t" \
  "v_mfma_f32_16x16x32_bf16 v[156:159], v[84:87], v[196:199], v[156:159]\n\t" \
  "ds_read_b128 v[196:199], v214 offset:49408\n\t" \
  "s_waitcnt lgkmcnt(6)\n\t" \
  "v_mfma_f32_16x16x32_bf16 v[144:147], v[88:91], v[200:203], v[144:147]\n\t" \
  "v_mfma_f32_16x16x32_bf16 v[148:151], v[92:95], v[200:203], v[148:151]\n\t" \
  "ds_read_b128 v[200:203], v215 offset:33024\n\t" \
  "s_add_u32 m0, %[lw], 35840\n\t" \
  "v_mfma_f32_16x16x32_bf16 v[152:155], v[88:91], v[204:207], v[152:155]\n\t" \
  "v_mfma_f32_16x16x32_bf16 v[156:159], v[92:95], v[204:207], v[156:159]\n\t" \
  "ds_read_b128 v[204:207], v215 offset:49408\n\t" \
  "global_load_lds_dwordx4 v219, s[92:93] sc1\n\t" \
  "s_cmp_lt_u32 s86, 8\n\t" \
  "s_cbranch_scc1 .Lw31%=\n\t" \
  "s_waitcnt vmcnt(6)\n\t" \
  "s_barrier\n\t" \
  ".Lg31%=:\n\t" \
  "s_add_u32 s92, s92, 65024\n\t" \
  "s_addc_u32 s93, s93, 0\n\t" \
  "s_waitcnt lgkmcnt(6)\n\t" \
  "v_mfma_f32_16x16x32_bf16 v[144:147], v[96:99], v[176:179], v[144:147]\n\t" \
  "v_mfma_f32_16x16x32_bf16 v[148:151], v[100:103], v[176:179], v[148:151]\n\t" \
  "ds_read_b128 v[176:179], v208 offset:0\n\t" \
  "v_mfma_f32_16x16x32_bf16 v[152:155], v[96:99], v[180:183], v[152:155]\n\t" \
  "v_mfma_f32_16x16x32_bf16 v[156:159], v[100:103], v[180:183], v[156:159]\n\t" \
  "ds_read_b128 v[180:183], v208 offset:16384\n\t" \
  "s_waitcnt lgkmcnt(6)\n\t" \
  "v_mfma_f32_16x16x32_bf16 v[144:147], v[104:107], v[184:187], v[144:147]\n\t" \
  "v_mfma_f32_16x16x32_bf16 v[148:151], v[108:111], v[184:187], v[148:151]\n\t" \
  "ds_read_b128 v[184:187], v209 offset:0\n\t" \
  "s_add_u32 m0, %[lw], 65536\n\t" \
  "v_mfma_f32_16x16x32_bf16 v[152:155], v[104:107], v[188:191], v[152:155]\n\t" \
  "v_mfma_f32_16x16x32_bf16 v[156:159], v[108:111], v[188:191], v[156:159]\n\t" \
  "ds_read_b128 v[188:191], v209 offset:16384\n\t" \
  "global_load_lds_dwordx4 v216, s[92:93] sc1\n\t" \
  "s_waitcnt lgkmcnt(6)\n\t" \
  "v_mfma_f32_16x16x32_bf16 v[144:147], v[112:115], v[192:195], v[144:147]\n\t" \
  "v_mfma_f32_16x16x32_bf16 v[148:151], v[116:119], v[192:195], v[148:151]\n\t" \
  "ds_read_b128 v[192:195], v210 offset:0\n\t" \
  "v_mfma_f32_16x16x32_bf16 v[152:155], v[112:115], v[196:199], v[152:155]\n\t" \
  "v_mfma_f32_16x16x32_bf16 v[156:159], v[116:119], v[196:199], v[156:159]\n\t" \
  "ds_read_b128 v[196:199], v210 offset:16384\n\t" \
  "s_waitcnt lgkmcnt(6)\n\t" \
  "v_mfma_f32_16x16x32_bf16 v[144:147], v[120:123], v[200:203], v[144:147]\n\t" \
  "v_mfma_f32_16x16x32_bf16 v[148:151], v[124:127], v[200:203], v[148:151]\n\t" \
  "ds_read_b128 v[200:203], v211 offset:0\n\t" \
  "s_add_u32 m0, %[lw], 66560\n\t" \
  "v_mfma_f32_16x16x32_bf16 v[152:155], v[120:123], v[204:207], v[152:155]\n\t" \
  "v_mfma_f32_16x16x32_bf16 v[156:159], v[124:127], v[204:207], v[156:159]\n\t" \
  "ds_read_b128 v[204:207], v211 offset:16384\n\t" \
  "global_load_lds_dwordx4 v217, s[92:93] sc1\n\t" \
  "s_add_u32 s86, s86, 1\n\t" \
  "s_add_u32 s87, s87, 1\n\t" \
  "s_cmp_lt_u32 s86, %[end]\n\t" \
  "s_cbranch_scc1 .Lp1%=\n\t" \
  "s_branch .Lx1%=\n\t" \
  ".Lf01%=:\n\t" \
  "s_nop 7\n\t" \
  "ds_write_b128 v220, v[152:155] offset:0\n\t" \
  "ds_write_b128 v220, v[156:159] offset:1024\n\t" \
  "s_lshr_b32 s90, s87, 7\n\t" \
  "s_xor_b32 s90, s90, %[s0]\n\t" \
  "s_and_b32 s90, s90, 1\n\t" \
  "s_lshl1_add_u32 s90, s87, s90\n\t" \
  "s_lshl_b32 s90, s90, 13\n\t" \
  "s_cmp_lt_i32 s86, 1\n\t" \
  "s_cselect_b32 s90, 0x70000000, s90\n\t" \
  "s_waitcnt lgkmcnt(0)\n\t" \
  "s_barrier\n\t" \
  "ds_read_b128 v[160:163], v221 offset:0\n\t" \
  "ds_read_b128 v[164:167], v221 offset:1024\n\t" \
  "s_waitcnt lgkmcnt(0)\n\t" \
  "v_add_f32 v160, v160, v144\n\t" \
  "v_add_f32 v161, v161, v145\n\t" \
  "v_add_f32 v162, v162, v146\n\t" \
  "v_add_f32 v163, v163, v147\n\t" \
  "v_add_f32 v164, v164, v148\n\t" \
  "v_add_f32 v165, v165, v149\n\t" \
  "v_add_f32 v166, v166, v150\n\t" \
  "v_add_f32 v167, v167, v151\n\t" \
  "v_cvt_pk_bf16_f32 v168, v160, v161\n\t" \
  "v_cvt_pk_bf16_f32 v169, v162, v163\n\t" \
  "v_cvt_pk_bf16_f32 v170, v164, v165\n\t" \
  "v_cvt_pk_bf16_f32 v171, v166, v167\n\t" \
  "buffer_store_dwordx4 v[168:171], v172, %[rt], s90 offen sc1\n\t" \
  "s_branch .Lfb01%=\n\t" \
  ".Lf11%=:\n\t" \
  "s_nop 7\n\t" \
  "ds_write_b128 v220, v[136:139] offset:0\n\t" \
  "ds_write_b128 v220, v[140:143] offset:1024\n\t" \
  "s_lshr_b32 s90, s87, 7\n\t" \
  "s_xor_b32 s90, s90, %[s0]\n\t" \
  "s_and_b32 s90, s90, 1\n\t" \
  "s_lshl1_add_u32 s90, s87, s90\n\t" \
  "s_lshl_b32 s90, s90, 13\n\t" \
  "s_cmp_lt_i32 s86, 1\n\t" \
  "s_cselect_b32 s90, 0x70000000, s90\n\t" \
  "s_waitcnt lgkmcnt(0)\n\t" \
  "s_barrier\n\t" \
  "ds_read_b128 v[160:163], v221 offset:0\n\t" \
  "ds_read_b128 v[164:167], v221 offset:1024\n\t" \
  "s_waitcnt lgkmcnt(0)\n\t" \
  "v_add_f32 v160, v160, v128\n\t" \
  "v_add_f32 v161, v161, v129\n\t" \
  "v_add_f32 v162, v162, v130\n\t" \
  "v_add_f32 v163, v163, v131\n\t" \
  "v_add_f32 v164, v164, v132\n\t" \
  "v_add_f32 v165, v165, v133\n\t" \
  "v_add_f32 v166, v166, v134\n\t" \
  "v_add_f32 v167, v167, v135\n\t" \
  "v_cvt_pk_bf16_f32 v168, v160, v161\n\t" \
  "v_cvt_pk_bf16_f32 v169, v162, v163\n\t" \
  "v_cvt_pk_bf16_f32 v170, v164, v165\n\t" \
  "v_cvt_pk_bf16_f32 v171, v166, v167\n\t" \
  "buffer_store_dwordx4 v[168:171], v172, %[rt], s90 offen sc1\n\t" \
  "s_branch .Lfb11%=\n\t" \
  ".Lw01%=:\n\t" \
  "s_waitcnt vmcnt(2)\n\t" \
  "s_barrier\n\t" \
  "s_sub_i32 s91, s86, 1\n\t" \
  "s_max_i32 s88, s88, s91\n\t" \
  "v_mov_b32 v173, s88\n\t" \
  "buffer_store_dword v173, v174, %[rs], 0 offen sc1\n\t" \
  "s_branch .Lg01%=\n\t" \
  ".Lw11%=:\n\t" \
  "s_waitcnt vmcnt(2)\n\t" \
  "s_barrier\n\t" \
  "s_sub_i32 s91, s86, 1\n\t" \
  "s_max_i32 s88, s88, s91\n\t" \
  "v_mov_b32 v173, s88\n\t" \
  "buffer_store_dword v173, v174, %[rs], 0 offen sc1\n\t" \
  "s_branch .Lg11%=\n\t" \
  ".Lw21%=:\n\t" \
  "s_waitcnt vmcnt(2)\n\t" \
  "s_barrier\n\t" \
  "s_sub_i32 s91, s86, 1\n\t" \
  "s_max_i32 s88, s88, s91\n\t" \
  "v_mov_b32 v173, s88\n\t" \
  "buffer_store_dword v173, v174, %[rs], 0 offen sc1\n\t" \
  "s_branch .Lg21%=\n\t" \
  ".Lw31%=:\n\t" \
  "s_waitcnt vmcnt(2)\n\t" \
  "s_barrier\n\t" \
  "s_sub_i32 s91, s86, 1\n\t" \
  "s_max_i32 s88, s88, s91\n\t" \
  "v_mov_b32 v173, s88\n\t" \
  "buffer_store_dword v173, v174, %[rs], 0 offen sc1\n\t" \
  "s_branch .Lg31%=\n\t" \
  ".Lx1%=:\n\t" \
  "s_waitcnt lgkmcnt(0)\n\t" \
  "s_setprio 0\n\t" \
  "s_mov_b32 m0, s94\n\t" \
  ".Le%=:\n\t"
#define PR_ASM_1 \
  "s_cmp_lg_u32 %[kh], 0\n\t" \
  "s_cbranch_scc1 .Lk%=\n\t" \
  "s_mov_b32 s94, m0\n\t" \
  "s_mov_b64 s[92:93], %[x0]\n\t" \
  "s_mov_b32 s86, %[g0]\n\t" \
  "s_add_u32 s87, s86, %[t0]\n\t" \
  "s_sub_i32 s88, s86, 3\n\t" \
  "ds_read_b128 v[176:179], v208 offset:0\n\t" \
  "ds_read_b128 v[180:183], v208 offset:16384\n\t" \
  "ds_read_b128 v[184:187], v209 offset:0\n\t" \
  "ds_read_b128 v[188:191], v209 offset:16384\n\t" \
  "ds_read_b128 v[192:195], v210 offset:0\n\t" \
  "ds_read_b128 v[196:199], v210 offset:16384\n\t" \
  "ds_read_b128 v[200:203], v211 offset:0\n\t" \
  "ds_read_b128 v[204:207], v211 offset:16384\n\t" \
  "s_branch .Lfb00%=\n\t" \
  ".Lp0%=:\n\t" \
  "s_cmp_le_u32 s86, 5\n\t" \
  "s_cbranch_scc1 .Lf00%=\n\t" \
  ".Lfb00%=:\n\t" \
  "s_waitcnt lgkmcnt(6)\n\t" \
  "v_mfma_f32_16x16x32_bf16 v[128:131], v[176:179], v[0:3], 0\n\t" \
  "v_mfma_f32_16x16x32_bf16 v[136:139], v[176:179], v[4:7], 0\n\t" \
  "ds_read_b128 v[176:179], v208 offset:256\n\t" \
  "s_add_u32 m0, %[lw], 67584\n\t" \
  "v_mfma_f32_16x16x32_bf16 v[132:135], v[180:183], v[0:3], 0\n\t" \
  "v_mfma_f32_16x16x32_bf16 v[140:143], v[180:183], v[4:7], 0\n\t" \
  "ds_read_b128 v[180:183], v208 offset:16640\n\t" \
  "global_load_lds_dwordx4 v218, s[92:93] sc1\n\t" \
  "s_waitcnt lgkmcnt(6)\n\t" \
  "v_mfma_f32_16x16x32_bf16 v[128:131], v[184:187], v[8:11], v[128:131]\n\t" \
  "v_mfma_f32_16x16x32_bf16 v[136:139], v[184:187], v[12:15], v[136:139]\n\t" \
  "ds_read_b128 v[184:187], v209 offset:256\n\t" \
  "v_mfma_f32_16x16x32_bf16 v[132:135], v[188:191], v[8:11], v[132:135]\n\t" \
  "v_mfma_f32_16x16x32_bf16 v[140:143], v[188:191], v[12:15], v[140:143]\n\t" \
  "ds_read_b128 v[188:191], v209 offset:16640\n\t" \
  "ds_write_b128 v220, v[152:155] offset:0\n\t" \
  "ds_write_b128 v220, v[156:159] offset:1024\n\t" \
  "s_waitcnt lgkmcnt(8)\n\t" \
  "v_mfma_f32_16x16x32_bf16 v[128:131], v[192:195], v[16:19], v[128:131]\n\t" \
  "v_mfma_f32_16x16x32_bf16 v[136:139], v[192:195], v[20:23], v[136:139]\n\t" \
  "ds_read_b128 v[192:195], v210 offset:256\n\t" \
  "s_add_u32 m0, %[lw], 68608\n\t" \
  "v_mfma_f32_16x16x32_bf16 v[132:135], v[196:199], v[16:19], v[132:135]\n\t" \
  "v_mfma_f32_16x16x32_bf16 v[140:143], v[196:199], v[20:23], v[140:143]\n\t" \
  "ds_read_b128 v[196:199], v210 offset:16640\n\t" \
  "global_load_lds_dwordx4 v219, s[92:93] sc1\n\t" \
  "s_lshr_b32 s90, s87, 7\n\t" \
  "s_xor_b32 s90, s90, %[s0]\n\t" \
  "s_and_b32 s90, s90, 1\n\t" \
  "s_lshl1_add_u32 s90, s87, s90\n\t" \
  "s_lshl_b32 s90, s90, 13\n\t" \
  "s_cmp_lt_i32 s86, 6\n\t" \
  "s_cselect_b32 s90, 0x70000000, s90\n\t" \
  "s_waitcnt lgkmcnt(8)\n\t" \
  "v_mfma_f32_16x16x32_bf16 v[128:131], v[200:203], v[24:27], v[128:131]\n\t" \
  "v_mfma_f32_16x16x32_bf16 v[136:139], v[200:203], v[28:31], v[136:139]\n\t" \
  "ds_read_b128 v[200:203], v211 offset:256\n\t" \
  "v_mfma_f32_16x16x32_bf16 v[132:135], v[204:207], v[24:27], v[132:135]\n\t" \
  "v_mfma_f32_16x16x32_bf16 v[140:143], v[204:207], v[28:31], v[140:143]\n\t" \
  "ds_read_b128 v[204:207], v211 offset:16640\n\t" \
  "s_waitcnt lgkmcnt(4)\n\t" \
  "s_cmp_lt_u32 s86, 6\n\t" \
  "s_cbranch_scc1 .Lw00%=\n\t" \
  "s_waitcnt vmcnt(4)\n\t" \
  "s_barrier\n\t" \
  ".Lg00%=:\n\t" \
  "s_add_u32 s92, s92, 512\n\t" \
  "s_addc_u32 s93, s93, 0\n\t" \
  "v_mfma_f32_16x16x32_bf16 v[128:131], v[176:179], v[32:35], v[128:131]\n\t" \
  "v_mfma_f32_16x16x32_bf16 v[136:139], v[176:179], v[36:39], v[136:139]\n\t" \
  "ds_read_b128 v[176:179], v208 offset:32768\n\t" \
  "s_add_u32 m0, %[lw], 98304\n\t" \
  "v_mfma_f32_16x16x32_bf16 v[132:135], v[180:183], v[32:35], v[132:135]\n\t" \
  "v_mfma_f32_16x16x32_bf16 v[140:143], v[180:183], v[36:39], v[140:143]\n\t" \
  "ds_read_b128 v[180:183], v208 offset:49152\n\t" \
  "global_load_lds_dwordx4 v216, s[92:93] sc1\n\t" \
  "ds_read_b128 v[160:163], v221 offset:0\n\t" \
  "ds_read_b128 v[164:167], v221 offset:1024\n\t" \
  "v_mfma_f32_16x16x32_bf16 v[128:131], v[184:187], v[40:43], v[128:131]\n\t" \
  "v_mfma_f32_16x16x32_bf16 v[136:139], v[184:187], v[44:47], v[136:139]\n\t" \
  "ds_read_b128 v[184:187], v209 offset:32768\n\t" \
  "v_mfma_f32_16x16x32_bf16 v[132:135], v[188:191], v[40:43], v[132:135]\n\t" \
  "v_mfma_f32_16x16x32_bf16 v[140:143], v[188:191], v[44:47], v[140:143]\n\t" \
  "ds_read_b128 v[188:191], v209 offset:49152\n\t" \
  "s_waitcnt lgkmcnt(8)\n\t" \
  "v_mfma_f32_16x16x32_bf16 v[128:131], v[192:195], v[48:51], v[128:131]\n\t" \
  "v_mfma_f32_16x16x32_bf16 v[136:139], v[192:195], v[52:55], v[136:139]\n\t" \
  "ds_read_b128 v[192:195], v210 offset:32768\n\t" \
  "s_add_u32 m0, %[lw], 99328\n\t" \
  "v_mfma_f32_16x16x32_bf16 v[132:135], v[196:199], v[48:51], v[132:135]\n\t" \
  "v_mfma_f32_16x16x32_bf16 v[140:143], v[196:199], v[52:55], v[140:143]\n\t" \
  "ds_read_b128 v[196:199], v210 offset:49152\n\t" \
  "global_load_lds_dwordx4 v217, s[92:93] sc1\n\t" \
  "s_waitcnt lgkmcnt(4)\n\t" \
  "v_add_f32 v160, v160, v144\n\t" \
  "v_add_f32 v161, v161, v145\n\t" \
  "v_add_f32 v162, v162, v146\n\t" \
  "v_add_f32 v163, v163, v147\n\t" \
  "v_add_f32 v164, v164, v148\n\t" \
  "v_add_f32 v165, v165, v149\n\t" \
  "v_add_f32 v166, v166, v150\n\t" \
  "v_add_f32 v167, v167, v151\n\t" \
  "v_mfma_f32_16x16x32_bf16 v[128:131], v[200:203], v[56:59], v[128:131]\n\t" \
  "v_mfma_f32_16x16x32_bf16 v[136:139], v[200:203], v[60:63], v[136:139]\n\t" \
  "ds_read_b128 v[200:203], v211 offset:32768\n\t" \
  "v_mfma_f32_16x16x32_bf16 v[132:135], v[204:207], v[56:59], v[132:135]\n\t" \
  "v_mfma_f32_16x16x32_bf16 v[140:143], v[204:207], v[60:63], v[140:143]\n\t" \
  "ds_read_b128 v[204:207], v211 offset:49152\n\t" \
  "v_cvt_pk_bf16_f32 v168, v160, v161\n\t" \
  "v_cvt_pk_bf16_f32 v169, v162, v163\n\t" \
  "v_cvt_pk_bf16_f32 v170, v164, v165\n\t" \
  "v_cvt_pk_bf16_f32 v171, v166, v167\n\t" \
  "buffer_store_dwordx4 v[168:171], v172, %[rt], s90 offen sc1\n\t" \
  "v_mfma_f32_16x16x32_bf16 v[128:131], v[176:179], v[64:67], v[128:131]\n\t" \
  "v_mfma_f32_16x16x32_bf16 v[136:139], v[176:179], v[68:71], v[136:139]\n\t" \
  "ds_read_b128 v[176:179], v208 offset:33024\n\t" \
  "s_add_u32 m0, %[lw], 100352\n\t" \
  "v_mfma_f32_16x16x32_bf16 v[132:135], v[180:183], v[64:67], v[132:135]\n\t" \
  "v_mfma_f32_16x16x32_bf16 v[140:143], v[180:183], v[68:71], v[140:143]\n\t" \
  "ds_read_b128 v[180:183], v208 offset:49408\n\t" \
  "global_load_lds_dwordx4 v218, s[92:93] sc1\n\t" \
  "s_waitcnt lgkmcnt(6)\n\t" \
  "v_mfma_f32_16x16x32_bf16 v[128:131], v[184:187], v[72:75], v[128:131]\n\t" \
  "v_mfma_f32_16x16x32_bf16 v[136:139], v[184:187], v[76:79], v[136:139]\n\t" \
  "ds_read_b128 v[184:187], v209 offset:33024\n\t" \
  "v_mfma_f32_16x16x32_bf16 v[132:135], v[188:191], v[72:75], v[132:135]\n\t" \
  "v_mfma_f32_16x16x32_bf16 v[140:143], v[188:191], v[76:79], v[140:143]\n\t" \
  "ds_read_b128 v[188:191], v209 offset:49408\n\t" \
  "s_waitcnt lgkmcnt(6)\n\t" \
  "v_mfma_f32_16x16x32_bf16 v[128:131], v[192:195], v[80:83], v[128:131]\n\t" \
  "v_mfma_f32_16x16x32_bf16 v[136:139], v[192:195], v[84:87], v[136:139]\n\t" \
  "ds_read_b128 v[192:195], v210 offset:33024\n\t" \
  "s_add_u32 m0, %[lw], 101376\n\t" \
  "v_mfma_f32_16x16x32_bf16 v[132:135], v[196:199], v[80:83], v[132:135]\n\t" \
  "v_mfma_f32_16x16x32_bf16 v[140:143], v[196:199], v[84:87], v[140:143]\n\t" \
  "ds_read_b128 v[196:199], v210 offset:49408\n\t" \
  "global_load_lds_dwordx4 v219, s[92:93] sc1\n\t" \
  "s_waitcnt lgkmcnt(6)\n\t" \
  "v_mfma_f32_16x16x32_bf16 v[128:131], v[200:203], v[88:91], v[128:131]\n\t" \
  "v_mfma_f32_16x16x32_bf16 v[136:139], v[200:203], v[92:95], v[136:139]\n\t" \
  "ds_read_b128 v[200:203], v211 offset:33024\n\t" \
  "v_mfma_f32_16x16x32_bf16 v[132:135], v[204:207], v[88:91], v[132:135]\n\t" \
  "v_mfma_f32_16x16x32_bf16 v[140:143], v[204:207], v[92:95], v[140:143]\n\t" \
  "ds_read_b128 v[204:207], v211 offset:49408\n\t" \
  "s_cmp_lt_u32 s86, 8\n\t" \
  "s_cbranch_scc1 .Lw10%=\n\t" \
  "s_waitcnt vmcnt(5)\n\t" \
  "s_barrier\n\t" \
  ".Lg10%=:\n\t" \
  "s_add_u32 s92, s92, 65024\n\t" \
  "s_addc_u32 s93, s93, 0\n\t" \
  "s_waitcnt lgkmcnt(6)\n\t" \
  "v_mfma_f32_16x16x32_bf16 v[128:131], v[176:179], v[96:99], v[128:131]\n\t" \
  "v_mfma_f32_16x16x32_bf16 v[136:139], v[176:179], v[100:103], v[136:139]\n\t" \
  "ds_read_b128 v[176:179], v212 offset:0\n\t" \
  "s_add_u32 m0, %[lw], 0\n\t" \
  "v_mfma_f32_16x16x32_bf16 v[132:135], v[180:183], v[96:99], v[132:135]\n\t" \
  "v_mfma_f32_16x16x32_bf16 v[140:143], v[180:183], v[100:103], v[140:143]\n\t" \
  "ds_read_b128 v[180:183], v212 offset:16384\n\t" \
  "global_load_lds_dwordx4 v216, s[92:93] sc1\n\t" \
  "s_waitcnt lgkmcnt(6)\n\t" \
  "v_mfma_f32_16x16x32_bf16 v[128:131], v[184:187], v[104:107], v[128:131]\n\t" \
  "v_mfma_f32_16x16x32_bf16 v[136:139], v[184:187], v[108:111], v[136:139]\n\t" \
  "ds_read_b128 v[184:187], v213 offset:0\n\t" \
  "v_mfma_f32_16x16x32_bf16 v[132:135], v[188:191], v[104:107], v[132:135]\n\t" \
  "v_mfma_f32_16x16x32_bf16 v[140:143], v[188:191], v[108:111], v[140:143]\n\t" \
  "ds_read_b128 v[188:191], v213 offset:16384\n\t" \
  "s_waitcnt lgkmcnt(6)\n\t" \
  "v_mfma_f32_16x16x32_bf16 v[128:131], v[192:195], v[112:115], v[128:131]\n\t" \
  "v_mfma_f32_16x16x32_bf16 v[136:139], v[192:195], v[116:119], v[136:139]\n\t" \
  "ds_read_b128 v[192:195], v214 offset:0\n\t" \
  "s_add_u32 m0, %[lw], 1024\n\t" \
  "v_mfma_f32_16x16x32_bf16 v[132:135], v[196:199], v[112:115], v[132:135]\n\t" \
  "v_mfma_f32_16x16x32_bf16 v[140:143], v[196:199], v[116:119], v[140:143]\n\t" \
  "ds_read_b128 v[196:199], v214 offset:16384\n\t" \
  "global_load_lds_dwordx4 v217, s[92:93] sc1\n\t" \
  "s_waitcnt lgkmcnt(6)\n\t" \
  "v_mfma_f32_16x16x32_bf16 v[128:131], v[200:203], v[120:123], v[128:131]\n\t" \
  "v_mfma_f32_16x16x32_bf16 v[136:139], v[200:203], v[124:127], v[136:139]\n\t" \
  "ds_read_b128 v[200:203], v215 offset:0\n\t" \
  "v_mfma_f32_16x16x32_bf16 v[132:135], v[204:207], v[120:123], v[132:135]\n\t" \
  "v_mfma_f32_16x16x32_bf16 v[140:143], v[204:207], v[124:127], v[140:143]\n\t" \
  "ds_read_b128 v[204:207], v215 offset:16384\n\t" \
  "s_add_u32 s86, s86, 1\n\t" \
  "s_add_u32 s87, s87, 1\n\t" \
  "s_cmp_le_u32 s86, 5\n\t" \
  "s_cbranch_scc1 .Lf10%=\n\t" \
  ".Lfb10%=:\n\t" \
  "s_waitcnt lgkmcnt(6)\n\t" \
  "v_mfma_f32_16x16x32_bf16 v[144:147], v[176:179], v[0:3], 0\n\t" \
  "v_mfma_f32_16x16x32_bf16 v[152:155], v[176:179], v[4:7], 0\n\t" \
  "ds_read_b128 v[176:179], v212 offset:256\n\t" \
  "s_add_u32 m0, %[lw], 2048\n\t" \
  "v_mfma_f32_16x16x32_bf16 v[148:151], v[180:183], v[0:3], 0\n\t" \
  "v_mfma_f32_16x16x32_bf16 v[156:159], v[180:183], v[4:7], 0\n\t" \
  "ds_read_b128 v[180:183], v212 offset:16640\n\t" \
  "global_load_lds_dwordx4 v218, s[92:93] sc1\n\t" \
  "s_waitcnt lgkmcnt(6)\n\t" \
  "v_mfma_f32_16x16x32_bf16 v[144:147], v[184:187], v[8:11], v[144:147]\n\t" \
  "v_mfma_f32_16x16x32_bf16 v[152:155], v[184:187], v[12:15], v[152:155]\n\t" \
  "ds_read_b128 v[184:187], v213 offset:256\n\t" \
  "v_mfma_f32_16x16x32_bf16 v[148:151], v[188:191], v[8:11], v[148:151]\n\t" \
  "v_mfma_f32_16x16x32_bf16 v[156:159], v[188:191], v[12:15], v[156:159]\n\t" \
  "ds_read_b128 v[188:191], v213 offset:16640\n\t" \
  "ds_write_b128 v220, v[136:139] offset:0\n\t" \
  "ds_write_b128 v220, v[140:143] offset:1024\n\t" \
  "s_waitcnt lgkmcnt(8)\n\t" \
  "v_mfma_f32_16x16x32_bf16 v[144:147], v[192:195], v[16:19], v[144:147]\n\t" \
  "v_mfma_f32_16x16x32_bf16 v[152:155], v[192:195], v[20:23], v[152:155]\n\t" \
  "ds_read_b128 v[192:195], v214 offset:256\n\t" \
  "s_add_u32 m0, %[lw], 3072\n\t" \
  "v_mfma_f32_16x16x32_bf16 v[148:151], v[196:199], v[16:19], v[148:151]\n\t" \
  "v_mfma_f32_16x16x32_bf16 v[156:159], v[196:199], v[20:23], v[156:159]\n\t" \
  "ds_read_b128 v[196:199], v214 offset:16640\n\t" \
  "global_load_lds_dwordx4 v219, s[92:93] sc1\n\t" \
  "s_lshr_b32 s90, s87, 7\n\t" \
  "s_xor_b32 s90, s90, %[s0]\n\t" \
  "s_and_b32 s90, s90, 1\n\t" \
  "s_lshl1_add_u32 s90, s87, s90\n\t" \
  "s_lshl_b32 s90, s90, 13\n\t" \
  "s_cmp_lt_i32 s86, 6\n\t" \
  "s_cselect_b32 s90, 0x70000000, s90\n\t" \
  "s_waitcnt lgkmcnt(8)\n\t" \
  "v_mfma_f32_16x16x32_bf16 v[144:147], v[200:203], v[24:27], v[144:147]\n\t" \
  "v_mfma_f32_16x16x32_bf16 v[152:155], v[200:203], v[28:31], v[152:155]\n\t" \
  "ds_read_b128 v[200:203], v215 offset:256\n\t" \
  "v_mfma_f32_16x16x32_bf16 v[148:151], v[204:207], v[24:27], v[148:151]\n\t" \
  "v_mfma_f32_16x16x32_bf16 v[156:159], v[204:207], v[28:31], v[156:159]\n\t" \
  "ds_read_b128 v[204:207], v215 offset:16640\n\t" \
  "s_waitcnt lgkmcnt(4)\n\t" \
  "s_cmp_lt_u32 s86, 6\n\t" \
  "s_cbranch_scc1 .Lw20%=\n\t" \
  "s_waitcnt vmcnt(4)\n\t" \
  "s_barrier\n\t" \
  ".Lg20%=:\n\t" \
  "s_add_u32 s92, s92, 512\n\t" \
  "s_addc_u32 s93, s93, 0\n\t" \
  "v_mfma_f32_16x16x32_bf16 v[144:147], v[176:179], v[32:35], v[144:147]\n\t" \
  "v_mfma_f32_16x16x32_bf16 v[152:155], v[176:179], v[36:39], v[152:155]\n\t" \
  "ds_read_b128 v[176:179], v212 offset:32768\n\t" \
  "s_add_u32 m0, %[lw], 32768\n\t" \
  "v_mfma_f32_16x16x32_bf16 v[148:151], v[180:183], v[32:35], v[148:151]\n\t" \
  "v_mfma_f32_16x16x32_bf16 v[156:159], v[180:183], v[36:39], v[156:159]\n\t" \
  "ds_read_b128 v[180:183], v212 offset:49152\n\t" \
  "global_load_lds_dwordx4 v216, s[92:93] sc1\n\t" \
  "ds_read_b128 v[160:163], v221 offset:0\n\t" \
  "ds_read_b128 v[164:167], v221 offset:1024\n\t" \
  "v_mfma_f32_16x16x32_bf16 v[144:147], v[184:187], v[40:43], v[144:147]\n\t" \
  "v_mfma_f32_16x16x32_bf16 v[152:155], v[184:187], v[44:47], v[152:155]\n\t" \
  "ds_read_b128 v[184:187], v213 offset:32768\n\t" \
  "v_mfma_f32_16x16x32_bf16 v[148:151], v[188:191], v[40:43], v[148:151]\n\t" \
  "v_mfma_f32_16x16x32_bf16 v[156:159], v[188:191], v[44:47], v[156:159]\n\t" \
  "ds_read_b128 v[188:191], v213 offset:49152\n\t" \
  "s_waitcnt lgkmcnt(8)\n\t" \
  "v_mfma_f32_16x16x32_bf16 v[144:147], v[192:195], v[48:51], v[144:147]\n\t" \
  "v_mfma_f32_16x16x32_bf16 v[152:155], v[192:195], v[52:55], v[152:155]\n\t" \
  "ds_read_b128 v[192:195], v214 offset:32768\n\t" \
  "s_add_u32 m0, %[lw], 33792\n\t" \
  "v_mfma_f32_16x16x32_bf16 v[148:151], v[196:199], v[48:51], v[148:151]\n\t" \
  "v_mfma_f32_16x16x32_bf16 v[156:159], v[196:199], v[52:55], v[156:159]\n\t" \
  "ds_read_b128 v[196:199], v214 offset:49152\n\t" \
  "global_load_lds_dwordx4 v217, s[92:93] sc1\n\t" \
  "s_waitcnt lgkmcnt(4)\n\t" \
  "v_add_f32 v160, v160, v128\n\t" \
  "v_add_f32 v161, v161, v129\n\t" \
  "v_add_f32 v162, v162, v130\n\t" \
  "v_add_f32 v163, v163, v131\n\t" \
  "v_add_f32 v164, v164, v132\n\t" \
  "v_add_f32 v165, v165, v133\n\t" \
  "v_add_f32 v166, v166, v134\n\t" \
  "v_add_f32 v167, v167, v135\n\t" \
  "v_mfma_f32_16x16x32_bf16 v[144:147], v[200:203], v[56:59], v[144:147]\n\t" \
  "v_mfma_f32_16x16x32_bf16 v[152:155], v[200:203], v[60:63], v[152:155]\n\t" \
  "ds_read_b128 v[200:203], v215 offset:32768\n\t" \
  "v_mfma_f32_16x16x32_bf16 v[148:151], v[204:207], v[56:59], v[148:151]\n\t" \
  "v_mfma_f32_16x16x32_bf16 v[156:159], v[204:207], v[60:63], v[156:159]\n\t" \
  "ds_read_b128 v[204:207], v215 offset:49152\n\t" \
  "v_cvt_pk_bf16_f32 v168, v160, v161\n\t" \
  "v_cvt_pk_bf16_f32 v169, v162, v163\n\t" \
  "v_cvt_pk_bf16_f32 v170, v164, v165\n\t" \
  "v_cvt_pk_bf16_f32 v171, v166, v167\n\t" \
  "buffer_store_dwordx4 v[168:171], v172, %[rt], s90 offen sc1\n\t" \
  "v_mfma_f32_16x16x32_bf16 v[144:147], v[176:179], v[64:67], v[144:147]\n\t" \
  "v_mfma_f32_16x16x32_bf16 v[152:155], v[176:179], v[68:71], v[152:155]\n\t" \
  "ds_read_b128 v[176:179], v212 offset:33024\n\t" \
  "s_add_u32 m0, %[lw], 34816\n\t" \
  "v_mfma_f32_16x16x32_bf16 v[148:151], v[180:183], v[64:67], v[148:151]\n\t" \
  "v_mfma_f32_16x16x32_bf16 v[156:159], v[180:183], v[68:71], v[156:159]\n\t" \
  "ds_read_b128 v[180:183], v212 offset:49408\n\t" \
  "global_load_lds_dwordx4 v218, s[92:93] sc1\n\t" \
  "s_waitcnt lgkmcnt(6)\n\t" \
  "v_mfma_f32_16x16x32_bf16 v[144:147], v[184:187], v[72:75], v[144:147]\n\t" \
  "v_mfma_f32_16x16x32_bf16 v[152:155], v[184:187], v[76:79], v[152:155]\n\t" \
  "ds_read_b128 v[184:187], v213 offset:33024\n\t" \
  "v_mfma_f32_16x16x32_bf16 v[148:151], v[188:191], v[72:75], v[148:151]\n\t" \
  "v_mfma_f32_16x16x32_bf16 v[156:159], v[188:191], v[76:79], v[156:159]\n\t" \
  "ds_read_b128 v[188:191], v213 offset:49408\n\t" \
  "s_waitcnt lgkmcnt(6)\n\t" \
  "v_mfma_f32_16x16x32_bf16 v[144:147], v[192:195], v[80:83], v[144:147]\n\t" \
  "v_mfma_f32_16x16x32_bf16 v[152:155], v[192:195], v[84:87], v[152:155]\n\t" \
  "ds_read_b128 v[192:195], v214 offset:33024\n\t" \
  "s_add_u32 m0, %[lw], 35840\n\t" \
  "v_mfma_f32_16x16x32_bf16 v[148:151], v[196:199], v[80:83], v[148:151]\n\t" \
  "v_mfma_f32_16x16x32_bf16 v[156:159], v[196:199], v[84:87], v[156:159]\n\t" \
  "ds_read_b128 v[196:199], v214 offset:49408\n\t" \
  "global_load_lds_dwordx4 v219, s[92:93] sc1\n\t" \
  "s_waitcnt lgkmcnt(6)\n\t" \
  "v_mfma_f32_16x16x32_bf16 v[144:147], v[200:203], v[88:91], v[144:147]\n\t" \
  "v_mfma_f32_16x16x32_bf16 v[152:155], v[200:203], v[92:95], v[152:155]\n\t" \
  "ds_read_b128 v[200:203], v215 offset:33024\n\t" \
  "v_mfma_f32_16x16x32_bf16 v[148:151], v[204:207], v[88:91], v[148:151]\n\t" \
  "v_mfma_f32_16x16x32_bf16 v[156:159], v[204:207], v[92:95], v[156:159]\n\t" \
  "ds_read_b128 v[204:207], v215 offset:49408\n\t" \
  "s_cmp_lt_u32 s86, 8\n\t" \
  "s_cbranch_scc1 .Lw30%=\n\t" \
  "s_waitcnt vmcnt(5)\n\t" \
  "s_barrier\n\t" \
  ".Lg30%=:\n\t" \
  "s_add_u32 s92, s92, 65024\n\t" \
  "s_addc_u32 s93, s93, 0\n\t" \
  "s_waitcnt lgkmcnt(6)\n\t" \
  "v_mfma_f32_16x16x32_bf16 v[144:147], v[176:179], v[96:99], v[144:147]\n\t" \
  "v_mfma_f32_16x16x32_bf16 v[152:155], v[176:179], v[100:103], v[152:155]\n\t" \
  "ds_read_b128 v[176:179], v208 offset:0\n\t" \
  "s_add_u32 m0, %[lw], 65536\n\t" \
  "v_mfma_f32_16x16x32_bf16 v[148:151], v[180:183], v[96:99], v[148:151]\n\t" \
  "v_mfma_f32_16x16x32_bf16 v[156:159], v[180:183], v[100:103], v[156:159]\n\t" \
  "ds_read_b128 v[180:183], v208 offset:16384\n\t" \
  "global_load_lds_dwordx4 v216, s[92:93] sc1\n\t" \
  "s_waitcnt lgkmcnt(6)\n\t" \
  "v_mfma_f32_16x16x32_bf16 v[144:147], v[184:187], v[104:107], v[144:147]\n\t" \
  "v_mfma_f32_16x16x32_bf16 v[152:155], v[184:187], v[108:111], v[152:155]\n\t" \
  "ds_read_b128 v[184:187], v209 offset:0\n\t" \
  "v_mfma_f32_16x16x32_bf16 v[148:151], v[188:191], v[104:107], v[148:151]\n\t" \
  "v_mfma_f32_16x16x32_bf16 v[156:159], v[188:191], v[108:111], v[156:159]\n\t" \
  "ds_read_b128 v[188:191], v209 offset:16384\n\t" \
  "s_waitcnt lgkmcnt(6)\n\t" \
  "v_mfma_f32_16x16x32_bf16 v[144:147], v[192:195], v[112:115], v[144:147]\n\t" \
  "v_mfma_f32_16x16x32_bf16 v[152:155], v[192:195], v[116:119], v[152:155]\n\t" \
  "ds_read_b128 v[192:195], v210 offset:0\n\t" \
  "s_add_u32 m0, %[lw], 66560\n\t" \
  "v_mfma_f32_16x16x32_bf16 v[148:151], v[196:199], v[112:115], v[148:151]\n\t" \
  "v_mfma_f32_16x16x32_bf16 v[156:159], v[196:199], v[116:119], v[156:159]\n\t" \
  "ds_read_b128 v[196:199], v210 offset:16384\n\t" \
  "global_load_lds_dwordx4 v217, s[92:93] sc1\n\t" \
  "s_waitcnt lgkmcnt(6)\n\t" \
  "v_mfma_f32_16x16x32_bf16 v[144:147], v[200:203], v[120:123], v[144:147]\n\t" \
  "v_mfma_f32_16x16x32_bf16 v[152:155], v[200:203], v[124:127], v[152:155]\n\t" \
  "ds_read_b128 v[200:203], v211 offset:0\n\t" \
  "v_mfma_f32_16x16x32_bf16 v[148:151], v[204:207], v[120:123], v[148:151]\n\t" \
  "v_mfma_f32_16x16x32_bf16 v[156:159], v[204:207], v[124:127], v[156:159]\n\t" \
  "ds_read_b128 v[204:207], v211 offset:16384\n\t" \
  "s_add_u32 s86, s86, 1\n\t" \
  "s_add_u32 s87, s87, 1\n\t" \
  "s_cmp_lt_u32 s86, %[end]\n\t" \
  "s_cbranch_scc1 .Lp0%=\n\t" \
  "s_branch .Lx0%=\n\t" \
  ".Lf00%=:\n\t" \
  "s_nop 7\n\t" \
  "ds_write_b128 v220, v[152:155] offset:0\n\t" \
  "ds_write_b128 v220, v[156:159] offset:1024\n\t" \
  "s_lshr_b32 s90, s87, 7\n\t" \
  "s_xor_b32 s90, s90, %[s0]\n\t" \
  "s_and_b32 s90, s90, 1\n\t" \
  "s_lshl1_add_u32 s90, s87, s90\n\t" \
  "s_lshl_b32 s90, s90, 13\n\t" \
  "s_cmp_lt_i32 s86, 1\n\t" \
  "s_cselect_b32 s90, 0x70000000, s90\n\t" \
  "s_waitcnt lgkmcnt(0)\n\t" \
  "s_barrier\n\t" \
  "ds_read_b128 v[160:163], v221 offset:0\n\t" \
  "ds_read_b128 v[164:167], v221 offset:1024\n\t" \
  "s_waitcnt lgkmcnt(0)\n\t" \
  "v_add_f32 v160, v160, v144\n\t" \
  "v_add_f32 v161, v161, v145\n\t" \
  "v_add_f32 v162, v162, v146\n\t" \
  "v_add_f32 v163, v163, v147\n\t" \
  "v_add_f32 v164, v164, v148\n\t" \
  "v_add_f32 v165, v165, v149\n\t" \
  "v_add_f32 v166, v166, v150\n\t" \
  "v_add_f32 v167, v167, v151\n\t" \
  "v_cvt_pk_bf16_f32 v168, v160, v161\n\t" \
  "v_cvt_pk_bf16_f32 v169, v162, v163\n\t" \
  "v_cvt_pk_bf16_f32 v170, v164, v165\n\t" \
  "v_cvt_pk_bf16_f32 v171, v166, v167\n\t" \
  "buffer_store_dwordx4 v[168:171], v172, %[rt], s90 offen sc1\n\t" \
  "s_branch .Lfb00%=\n\t" \
  ".Lf10%=:\n\t" \
  "s_nop 7\n\t" \
  "ds_write_b128 v220, v[136:139] offset:0\n\t" \
  "ds_write_b128 v220, v[140:143] offset:1024\n\t" \
  "s_lshr_b32 s90, s87, 7\n\t" \
  "s_xor_b32 s90, s90, %[s0]\n\t" \
  "s_and_b32 s90, s90, 1\n\t" \
  "s_lshl1_add_u32 s90, s87, s90\n\t" \
  "s_lshl_b32 s90, s90, 13\n\t" \
  "s_cmp_lt_i32 s86, 1\n\t" \
  "s_cselect_b32 s90, 0x70000000, s90\n\t" \
  "s_waitcnt lgkmcnt(0)\n\t" \
  "s_barrier\n\t" \
  "ds_read_b128 v[160:163], v221 offset:0\n\t" \
  "ds_read_b128 v[164:167], v221 offset:1024\n\t" \
  "s_waitcnt lgkmcnt(0)\n\t" \
  "v_add_f32 v160, v160, v128\n\t" \
  "v_add_f32 v161, v161, v129\n\t" \
  "v_add_f32 v162, v162, v130\n\t" \
  "v_add_f32 v163, v163, v131\n\t" \
  "v_add_f32 v164, v164, v132\n\t" \
  "v_add_f32 v165, v165, v133\n\t" \
  "v_add_f32 v166, v166, v134\n\t" \
  "v_add_f32 v167, v167, v135\n\t" \
  "v_cvt_pk_bf16_f32 v168, v160, v161\n\t" \
  "v_cvt_pk_bf16_f32 v169, v162, v163\n\t" \
  "v_cvt_pk_bf16_f32 v170, v164, v165\n\t" \
  "v_cvt_pk_bf16_f32 v171, v166, v167\n\t" \
  "buffer_store_dwordx4 v[168:171], v172, %[rt], s90 offen sc1\n\t" \
  "s_branch .Lfb10%=\n\t" \
  ".Lw00%=:\n\t" \
  "s_waitcnt vmcnt(2)\n\t" \
  "s_barrier\n\t" \
  "s_branch .Lg00%=\n\t" \
  ".Lw10%=:\n\t" \
  "s_waitcnt vmcnt(2)\n\t" \
  "s_barrier\n\t" \
  "s_branch .Lg10%=\n\t" \
  ".Lw20%=:\n\t" \
  "s_waitcnt vmcnt(2)\n\t" \
  "s_barrier\n\t" \
  "s_branch .Lg20%=\n\t" \
  ".Lw30%=:\n\t" \
  "s_waitcnt vmcnt(2)\n\t" \
  "s_barrier\n\t" \
  "s_branch .Lg30%=\n\t" \
  ".Lx0%=:\n\t" \
  "s_waitcnt lgkmcnt(0)\n\t" \
  "s_setprio 0\n\t" \
  "s_mov_b32 m0, s94\n\t" \
  "s_branch .Le%=\n\t" \
  ".Lk%=:\n\t" \
  "s_mov_b32 s94, m0\n\t" \
  "s_mov_b64 s[92:93], %[x0]\n\t" \
  "s_mov_b32 s86, %[g0]\n\t" \
  "s_add_u32 s87, s86, %[t0]\n\t" \
  "s_sub_i32 s88, s86, 3\n\t" \
  "ds_read_b128 v[176:179], v208 offset:0\n\t" \
  "ds_read_b128 v[180:183], v208 offset:16384\n\t" \
  "ds_read_b128 v[184:187], v209 offset:0\n\t" \
  "ds_read_b128 v[188:191], v209 offset:16384\n\t" \
  "ds_read_b128 v[192:195], v210 offset:0\n\t" \
  "ds_read_b128 v[196:199], v210 offset:16384\n\t" \
  "ds_read_b128 v[200:203], v211 offset:0\n\t" \
  "ds_read_b128 v[204:207], v211 offset:16384\n\t" \
  "s_branch .Lfb01%=\n\t" \
  ".Lp1%=:\n\t" \
  "s_cmp_le_u32 s86, 5\n\t" \
  "s_cbranch_scc1 .Lf01%=\n\t" \
  ".Lfb01%=:\n\t" \
  "s_waitcnt lgkmcnt(6)\n\t" \
  "v_mfma_f32_16x16x32_bf16 v[128:131], v[176:179], v[0:3], 0\n\t" \
  "v_mfma_f32_16x16x32_bf16 v[136:139], v[176:179], v[4:7], 0\n\t" \
  "ds_read_b128 v[176:179], v208 offset:256\n\t" \
  "v_mfma_f32_16x16x32_bf16 v[132:135], v[180:183], v[0:3], 0\n\t" \
  "v_mfma_f32_16x16x32_bf16 v[140:143], v[180:183], v[4:7], 0\n\t" \
  "ds_read_b128 v[180:183], v208 offset:16640\n\t" \
  "s_waitcnt lgkmcnt(6)\n\t" \
  "v_mfma_f32_16x16x32_bf16 v[128:131], v[184:187], v[8:11], v[128:131]\n\t" \
  "v_mfma_f32_16x16x32_bf16 v[136:139], v[184:187], v[12:15], v[136:139]\n\t" \
  "ds_read_b128 v[184:187], v209 offset:256\n\t" \
  "s_add_u32 m0, %[lw], 67584\n\t" \
  "v_mfma_f32_16x16x32_bf16 v[132:135], v[188:191], v[8:11], v[132:135]\n\t" \
  "v_mfma_f32_16x16x32_bf16 v[140:143], v[188:191], v[12:15], v[140:143]\n\t" \
  "ds_read_b128 v[188:191], v209 offset:16640\n\t" \
  "global_load_lds_dwordx4 v218, s[92:93] sc1\n\t" \
  "ds_write_b128 v220, v[152:155] offset:0\n\t" \
  "ds_write_b128 v220, v[156:159] offset:1024\n\t" \
  "s_waitcnt lgkmcnt(8)\n\t" \
  "v_mfma_f32_16x16x32_bf16 v[128:131], v[192:195], v[16:19], v[128:131]\n\t" \
  "v_mfma_f32_16x16x32_bf16 v[136:139], v[192:195], v[20:23], v[136:139]\n\t" \
  "ds_read_b128 v[192:195], v210 offset:256\n\t" \
  "v_mfma_f32_16x16x32_bf16 v[132:135], v[196:199], v[16:19], v[132:135]\n\t" \
  "v_mfma_f32_16x16x32_bf16 v[140:143], v[196:199], v[20:23], v[140:143]\n\t" \
  "ds_read_b128 v[196:199], v210 offset:16640\n\t" \
  "s_lshr_b32 s90, s87, 7\n\t" \
  "s_xor_b32 s90, s90, %[s0]\n\t" \
  "s_and_b32 s90, s90, 1\n\t" \
  "s_lshl1_add_u32 s90, s87, s90\n\t" \
  "s_lshl_b32 s90, s90, 13\n\t" \
  "s_cmp_lt_i32 s86, 6\n\t" \
  "s_cselect_b32 s90, 0x70000000, s90\n\t" \
  "s_waitcnt lgkmcnt(8)\n\t" \
  "v_mfma_f32_16x16x32_bf16 v[128:131], v[200:203], v[24:27], v[128:131]\n\t" \
  "v_mfma_f32_16x16x32_bf16 v[136:139], v[200:203], v[28:31], v[136:139]\n\t" \
  "ds_read_b128 v[200:203], v211 offset:256\n\t" \
  "s_add_u32 m0, %[lw], 68608\n\t" \
  "v_mfma_f32_16x16x32_bf16 v[132:135], v[204:207], v[24:27], v[132:135]\n\t" \
  "v_mfma_f32_16x16x32_bf16 v[140:143], v[204:207], v[28:31], v[140:143]\n\t" \
  "ds_read_b128 v[204:207], v211 offset:16640\n\t" \
  "global_load_lds_dwordx4 v219, s[92:93] sc1\n\t" \
  "s_waitcnt lgkmcnt(4)\n\t" \
  "s_cmp_lt_u32 s86, 6\n\t" \
  "s_cbranch_scc1 .Lw01%=\n\t" \
  "s_waitcnt vmcnt(4)\n\t" \
  "s_barrier\n\t" \
  ".Lg01%=:\n\t" \
  "s_add_u32 s92, s92, 512\n\t" \
  "s_addc_u32 s93, s93, 0\n\t" \
  "v_mfma_f32_16x16x32_bf16 v[128:131], v[176:179], v[32:35], v[128:131]\n\t" \
  "v_mfma_f32_16x16x32_bf16 v[136:139], v[176:179], v[36:39], v[136:139]\n\t" \
  "ds_read_b128 v[176:179], v208 offset:32768\n\t" \
  "v_mfma_f32_16x16x32_bf16 v[132:135], v[180:183], v[32:35], v[132:135]\n\t" \
  "v_mfma_f32_16x16x32_bf16 v[140:143], v[180:183], v[36:39], v[140:143]\n\t" \
  "ds_read_b128 v[180:183], v208 offset:49152\n\t" \
  "ds_read_b128 v[160:163], v221 offset:0\n\t" \
  "ds_read_b128 v[164:167], v221 offset:1024\n\t" \
  "s_sub_i32 s91, s86, 2\n\t" \
  "s_max_i32 s88, s88, s91\n\t" \
  "v_mov_b32 v173, s88\n\t" \
  "buffer_store_dword v173, v174, %[rs], 0 offen sc1\n\t" \
  "v_mfma_f32_16x16x32_bf16 v[128:131], v[184:187], v[40:43], v[128:131]\n\t" \
  "v_mfma_f32_16x16x32_bf16 v[136:139], v[184:187], v[44:47], v[136:139]\n\t" \
  "ds_read_b128 v[184:187], v209 offset:32768\n\t" \
  "s_add_u32 m0, %[lw], 98304\n\t" \
  "v_mfma_f32_16x16x32_bf16 v[132:135], v[188:191], v[40:43], v[132:135]\n\t" \
  "v_mfma_f32_16x16x32_bf16 v[140:143], v[188:191], v[44:47], v[140:143]\n\t" \
  "ds_read_b128 v[188:191], v209 offset:49152\n\t" \
  "global_load_lds_dwordx4 v216, s[92:93] sc1\n\t" \
  "s_waitcnt lgkmcnt(8)\n\t" \
  "v_mfma_f32_16x16x32_bf16 v[128:131], v[192:195], v[48:51], v[128:131]\n\t" \
  "v_mfma_f32_16x16x32_bf16 v[136:139], v[192:195], v[52:55], v[136:139]\n\t" \
  "ds_read_b128 v[192:195], v210 offset:32768\n\t" \
  "v_mfma_f32_16x16x32_bf16 v[132:135], v[196:199], v[48:51], v[132:135]\n\t" \
  "v_mfma_f32_16x16x32_bf16 v[140:143], v[196:199], v[52:55], v[140:143]\n\t" \
  "ds_read_b128 v[196:199], v210 offset:49152\n\t" \
  "s_waitcnt lgkmcnt(4)\n\t" \
  "v_add_f32 v160, v160, v144\n\t" \
  "v_add_f32 v161, v161, v145\n\t" \
  "v_add_f32 v162, v162, v146\n\t" \
  "v_add_f32 v163, v163, v147\n\t" \
  "v_add_f32 v164, v164, v148\n\t" \
  "v_add_f32 v165, v165, v149\n\t" \
  "v_add_f32 v166, v166, v150\n\t" \
  "v_add_f32 v167, v167, v151\n\t" \
  "v_mfma_f32_16x16x32_bf16 v[128:131], v[200:203], v[56:59], v[128:131]\n\t" \
  "v_mfma_f32_16x16x32_bf16 v[136:139], v[200:203], v[60:63], v[136:139]\n\t" \
  "ds_read_b128 v[200:203], v211 offset:32768\n\t" \
  "s_add_u32 m0, %[lw], 99328\n\t" \
  "v_mfma_f32_16x16x32_bf16 v[132:135], v[204:207], v[56:59], v[132:135]\n\t" \
  "v_mfma_f32_16x16x32_bf16 v[140:143], v[204:207], v[60:63], v[140:143]\n\t" \
  "ds_read_b128 v[204:207], v211 offset:49152\n\t" \
  "global_load_lds_dwordx4 v217, s[92:93] sc1\n\t" \
  "v_cvt_pk_bf16_f32 v168, v160, v161\n\t" \
  "v_cvt_pk_bf16_f32 v169, v162, v163\n\t" \
  "v_cvt_pk_bf16_f32 v170, v164, v165\n\t" \
  "v_cvt_pk_bf16_f32 v171, v166, v167\n\t" \
  "buffer_store_dwordx4 v[168:171], v172, %[rt], s90 offen sc1\n\t" \
  "v_mfma_f32_16x16x32_bf16 v[128:131], v[176:179], v[64:67], v[128:131]\n\t" \
  "v_mfma_f32_16x16x32_bf16 v[136:139], v[176:179], v[68:71], v[136:139]\n\t" \
  "ds_read_b128 v[176:179], v208 offset:33024\n\t" \
  "v_mfma_f32_16x16x32_bf16 v[132:135], v[180:183], v[64:67], v[132:135]\n\t" \
  "v_mfma_f32_16x16x32_bf16 v[140:143], v[180:183], v[68:71], v[140:143]\n\t" \
  "ds_read_b128 v[180:183], v208 offset:49408\n\t" \
  "s_waitcnt lgkmcnt(6)\n\t" \
  "v_mfma_f32_16x16x32_bf16 v[128:131], v[184:187], v[72:75], v[128:131]\n\t" \
  "v_mfma_f32_16x16x32_bf16 v[136:139], v[184:187], v[76:79], v[136:139]\n\t" \
  "ds_read_b128 v[184:187], v209 offset:33024\n\t" \
  "s_add_u32 m0, %[lw], 100352\n\t" \
  "v_mfma_f32_16x16x32_bf16 v[132:135], v[188:191], v[72:75], v[132:135]\n\t" \
  "v_mfma_f32_16x16x32_bf16 v[140:143], v[188:191], v[76:79], v[140:143]\n\t" \
  "ds_read_b128 v[188:191], v209 offset:49408\n\t" \
  "global_load_lds_dwordx4 v218, s[92:93] sc1\n\t" \
  "s_waitcnt lgkmcnt(6)\n\t" \
  "v_mfma_f32_16x16x32_bf16 v[128:131], v[192:195], v[80:83], v[128:131]\n\t" \
  "v_mfma_f32_16x16x32_bf16 v[136:139], v[192:195], v[84:87], v[136:139]\n\t" \
  "ds_read_b128 v[192:195], v210 offset:33024\n\t" \
  "v_mfma_f32_16x16x32_bf16 v[132:135], v[196:199], v[80:83], v[132:135]\n\t" \
  "v_mfma_f32_16x16x32_bf16 v[140:143], v[196:199], v[84:87], v[140:143]\n\t" \
  "ds_read_b128 v[196:199], v210 offset:49408\n\t" \
  "s_waitcnt lgkmcnt(6)\n\t" \
  "v_mfma_f32_16x16x32_bf16 v[128:131], v[200:203], v[88:91], v[128:131]\n\t" \
  "v_mfma_f32_16x16x32_bf16 v[136:139], v[200:203], v[92:95], v[136:139]\n\t" \
  "ds_read_b128 v[200:203], v211 offset:33024\n\t" \
  "s_add_u32 m0, %[lw], 101376\n\t" \
  "v_mfma_f32_16x16x32_bf16 v[132:135], v[204:207], v[88:91], v[132:135]\n\t" \
  "v_mfma_f32_16x16x32_bf16 v[140:143], v[204:207], v[92:95], v[140:143]\n\t" \
  "ds_read_b128 v[204:207], v211 offset:49408\n\t" \
  "global_load_lds_dwordx4 v219, s[92:93] sc1\n\t" \
  "s_cmp_lt_u32 s86, 8\n\t" \
  "s_cbranch_scc1 .Lw11%=\n\t" \
  "s_waitcnt vmcnt(6)\n\t" \
  "s_barrier\n\t" \
  ".Lg11%=:\n\t" \
  "s_add_u32 s92, s92, 65024\n\t" \
  "s_addc_u32 s93, s93, 0\n\t" \
  "s_waitcnt lgkmcnt(6)\n\t" \
  "v_mfma_f32_16x16x32_bf16 v[128:131], v[176:179], v[96:99], v[128:131]\n\t" \
  "v_mfma_f32_16x16x32_bf16 v[136:139], v[176:179], v[100:103], v[136:139]\n\t" \
  "ds_read_b128 v[176:179], v212 offset:0\n\t" \
  "v_mfma_f32_16x16x32_bf16 v[132:135], v[180:183], v[96:99], v[132:135]\n\t" \
  "v_mfma_f32_16x16x32_bf16 v[140:143], v[180:183], v[100:103], v[140:143]\n\t" \
  "ds_read_b128 v[180:183], v212 offset:16384\n\t" \
  "s_waitcnt lgkmcnt(6)\n\t" \
  "v_mfma_f32_16x16x32_bf16 v[128:131], v[184:187], v[104:107], v[128:131]\n\t" \
  "v_mfma_f32_16x16x32_bf16 v[136:139], v[184:187], v[108:111], v[136:139]\n\t" \
  "ds_read_b128 v[184:187], v213 offset:0\n\t" \
  "s_add_u32 m0, %[lw], 0\n\t" \
  "v_mfma_f32_16x16x32_bf16 v[132:135], v[188:191], v[104:107], v[132:135]\n\t" \
  "v_mfma_f32_16x16x32_bf16 v[140:143], v[188:191], v[108:111], v[140:143]\n\t" \
  "ds_read_b128 v[188:191], v213 offset:16384\n\t" \
  "global_load_lds_dwordx4 v216, s[92:93] sc1\n\t" \
  "s_waitcnt lgkmcnt(6)\n\t" \
  "v_mfma_f32_16x16x32_bf16 v[128:131], v[192:195], v[112:115], v[128:131]\n\t" \
  "v_mfma_f32_16x16x32_bf16 v[136:139], v[192:195], v[116:119], v[136:139]\n\t" \
  "ds_read_b128 v[192:195], v214 offset:0\n\t" \
  "v_mfma_f32_16x16x32_bf16 v[132:135], v[196:199], v[112:115], v[132:135]\n\t" \
  "v_mfma_f32_16x16x32_bf16 v[140:143], v[196:199], v[116:119], v[140:143]\n\t" \
  "ds_read_b128 v[196:199], v214 offset:16384\n\t" \
  "s_waitcnt lgkmcnt(6)\n\t" \
  "v_mfma_f32_16x16x32_bf16 v[128:131], v[200:203], v[120:123], v[128:131]\n\t" \
  "v_mfma_f32_16x16x32_bf16 v[136:139], v[200:203], v[124:127], v[136:139]\n\t" \
  "ds_read_b128 v[200:203], v215 offset:0\n\t" \
  "s_add_u32 m0, %[lw], 1024\n\t" \
  "v_mfma_f32_16x16x32_bf16 v[132:135], v[204:207], v[120:123], v[132:135]\n\t" \
  "v_mfma_f32_16x16x32_bf16 v[140:143], v[204:207], v[124:127], v[140:143]\n\t" \
  "ds_read_b128 v[204:207], v215 offset:16384\n\t" \
  "global_load_lds_dwordx4 v217, s[92:93] sc1\n\t" \
  "s_add_u32 s86, s86, 1\n\t" \
  "s_add_u32 s87, s87, 1\n\t" \
  "s_cmp_le_u32 s86, 5\n\t" \
  "s_cbranch_scc1 .Lf11%=\n\t" \
  ".Lfb11%=:\n\t" \
  "s_waitcnt lgkmcnt(6)\n\t" \
  "v_mfma_f32_16x16x32_bf16 v[144:147], v[176:179], v[0:3], 0\n\t" \
  "v_mfma_f32_16x16x32_bf16 v[152:155], v[176:179], v[4:7], 0\n\t" \
  "ds_read_b128 v[176:179], v212 offset:256\n\t" \
  "v_mfma_f32_16x16x32_bf16 v[148:151], v[180:183], v[0:3], 0\n\t" \
  "v_mfma_f32_16x16x32_bf16 v[156:159], v[180:183], v[4:7], 0\n\t" \
  "ds_read_b128 v[180:183], v212 offset:16640\n\t" \
  "s_waitcnt lgkmcnt(6)\n\t" \
  "v_mfma_f32_16x16x32_bf16 v[144:147], v[184:187], v[8:11], v[144:147]\n\t" \
  "v_mfma_f32_16x16x32_bf16 v[152:155], v[184:187], v[12:15], v[152:155]\n\t" \
  "ds_read_b128 v[184:187], v213 offset:256\n\t" \
  "s_add_u32 m0, %[lw], 2048\n\t" \
  "v_mfma_f32_16x16x32_bf16 v[148:151], v[188:191], v[8:11], v[148:151]\n\t" \
  "v_mfma_f32_16x16x32_bf16 v[156:159], v[188:191], v[12:15], v[156:159]\n\t" \
  "ds_read_b128 v[188:191], v213 offset:16640\n\t" \
  "global_load_lds_dwordx4 v218, s[92:93] sc1\n\t" \
  "ds_write_b128 v220, v[136:139] offset:0\n\t" \
  "ds_write_b128 v220, v[140:143] offset:1024\n\t" \
  "s_waitcnt lgkmcnt(8)\n\t" \
  "v_mfma_f32_16x16x32_bf16 v[144:147], v[192:195], v[16:19], v[144:147]\n\t" \
  "v_mfma_f32_16x16x32_bf16 v[152:155], v[192:195], v[20:23], v[152:155]\n\t" \
  "ds_read_b128 v[192:195], v214 offset:256\n\t" \
  "v_mfma_f32_16x16x32_bf16 v[148:151], v[196:199], v[16:19], v[148:151]\n\t" \
  "v_mfma_f32_16x16x32_bf16 v[156:159], v[196:199], v[20:23], v[156:159]\n\t" \
  "ds_read_b128 v[196:199], v214 offset:16640\n\t" \
  "s_lshr_b32 s90, s87, 7\n\t" \
  "s_xor_b32 s90, s90, %[s0]\n\t" \
  "s_and_b32 s90, s90, 1\n\t" \
  "s_lshl1_add_u32 s90, s87, s90\n\t" \
  "s_lshl_b32 s90, s90, 13\n\t" \
  "s_cmp_lt_i32 s86, 6\n\t" \
  "s_cselect_b32 s90, 0x70000000, s90\n\t" \
  "s_waitcnt lgkmcnt(8)\n\t" \
  "v_mfma_f32_16x16x32_bf16 v[144:147], v[200:203], v[24:27], v[144:147]\n\t" \
  "v_mfma_f32_16x16x32_bf16 v[152:155], v[200:203], v[28:31], v[152:155]\n\t" \
  "ds_read_b128 v[200:203], v215 offset:256\n\t" \
  "s_add_u32 m0, %[lw], 3072\n\t" \
  "v_mfma_f32_16x16x32_bf16 v[148:151], v[204:207], v[24:27], v[148:151]\n\t" \
  "v_mfma_f32_16x16x32_bf16 v[156:159], v[204:207], v[28:31], v[156:159]\n\t" \
  "ds_read_b128 v[204:207], v215 offset:16640\n\t" \
  "global_load_lds_dwordx4 v219, s[92:93] sc1\n\t" \
  "s_waitcnt lgkmcnt(4)\n\t" \
  "s_cmp_lt_u32 s86, 6\n\t" \
  "s_cbranch_scc1 .Lw21%=\n\t" \
  "s_waitcnt vmcnt(4)\n\t" \
  "s_barrier\n\t" \
  ".Lg21%=:\n\t" \
  "s_add_u32 s92, s92, 512\n\t" \
  "s_addc_u32 s93, s93, 0\n\t" \
  "v_mfma_f32_16x16x32_bf16 v[144:147], v[176:179], v[32:35], v[144:147]\n\t" \
  "v_mfma_f32_16x16x32_bf16 v[152:155], v[176:179], v[36:39], v[152:155]\n\t" \
  "ds_read_b128 v[176:179], v212 offset:32768\n\t" \
  "v_mfma_f32_16x16x32_bf16 v[148:151], v[180:183], v[32:35], v[148:151]\n\t" \
  "v_mfma_f32_16x16x32_bf16 v[156:159], v[180:183], v[36:39], v[156:159]\n\t" \
  "ds_read_b128 v[180:183], v212 offset:49152\n\t" \
  "ds_read_b128 v[160:163], v221 offset:0\n\t" \
  "ds_read_b128 v[164:167], v221 offset:1024\n\t" \
  "s_sub_i32 s91, s86, 2\n\t" \
  "s_max_i32 s88, s88, s91\n\t" \
  "v_mov_b32 v173, s88\n\t" \
  "buffer_store_dword v173, v174, %[rs], 0 offen sc1\n\t" \
  "v_mfma_f32_16x16x32_bf16 v[144:147], v[184:187], v[40:43], v[144:147]\n\t" \
  "v_mfma_f32_16x16x32_bf16 v[152:155], v[184:187], v[44:47], v[152:155]\n\t" \
  "ds_read_b128 v[184:187], v213 offset:32768\n\t" \
  "s_add_u32 m0, %[lw], 32768\n\t" \
  "v_mfma_f32_16x16x32_bf16 v[148:151], v[188:191], v[40:43], v[148:151]\n\t" \
  "v_mfma_f32_16x16x32_bf16 v[156:159], v[188:191], v[44:47], v[156:159]\n\t" \
  "ds_read_b128 v[188:191], v213 offset:49152\n\t" \
  "global_load_lds_dwordx4 v216, s[92:93] sc1\n\t" \
  "s_waitcnt lgkmcnt(8)\n\t" \
  "v_mfma_f32_16x16x32_bf16 v[144:147], v[192:195], v[48:51], v[144:147]\n\t" \
  "v_mfma_f32_16x16x32_bf16 v[152:155], v[192:195], v[52:55], v[152:155]\n\t" \
  "ds_read_b128 v[192:195], v214 offset:32768\n\t" \
  "v_mfma_f32_16x16x32_bf16 v[148:151], v[196:199], v[48:51], v[148:151]\n\t" \
  "v_mfma_f32_16x16x32_bf16 v[156:159], v[196:199], v[52:55], v[156:159]\n\t" \
  "ds_read_b128 v[196:199], v214 offset:49152\n\t" \
  "s_waitcnt lgkmcnt(4)\n\t" \
  "v_add_f32 v160, v160, v128\n\t" \
  "v_add_f32 v161, v161, v129\n\t" \
  "v_add_f32 v162, v162, v130\n\t" \
  "v_add_f32 v163, v163, v131\n\t" \
  "v_add_f32 v164, v164, v132\n\t" \
  "v_add_f32 v165, v165, v133\n\t" \
  "v_add_f32 v166, v166, v134\n\t" \
  "v_add_f32 v167, v167, v135\n\t" \
  "v_mfma_f32_16x16x32_bf16 v[144:147], v[200:203], v[56:59], v[144:147]\n\t" \
  "v_mfma_f32_16x16x32_bf16 v[152:155], v[200:203], v[60:63], v[152:155]\n\t" \
  "ds_read_b128 v[200:203], v215 offset:32768\n\t" \
  "s_add_u32 m0, %[lw], 33792\n\t" \
  "v_mfma_f32_16x16x32_bf16 v[148:151], v[204:207], v[56:59], v[148:151]\n\t" \
  "v_mfma_f32_16x16x32_bf16 v[156:159], v[204:207], v[60:63], v[156:159]\n\t" \
  "ds_read_b128 v[204:207], v215 offset:49152\n\t" \
  "global_load_lds_dwordx4 v217, s[92:93] sc1\n\t" \
  "v_cvt_pk_bf16_f32 v168, v160, v161\n\t" \
  "v_cvt_pk_bf16_f32 v169, v162, v163\n\t" \
  "v_cvt_pk_bf16_f32 v170, v164, v165\n\t" \
  "v_cvt_pk_bf16_f32 v171, v166, v167\n\t" \
  "buffer_store_dwordx4 v[168:171], v172, %[rt], s90 offen sc1\n\t" \
  "v_mfma_f32_16x16x32_bf16 v[144:147], v[176:179], v[64:67], v[144:147]\n\t" \
  "v_mfma_f32_16x16x32_bf16 v[152:155], v[176:179], v[68:71], v[152:155]\n\t" \
  "ds_read_b128 v[176:179], v212 offset:33024\n\t" \
  "v_mfma_f32_16x16x32_bf16 v[148:151], v[180:183], v[64:67], v[148:151]\n\t" \
  "v_mfma_f32_16x16x32_bf16 v[156:159], v[180:183], v[68:71], v[156:159]\n\t" \
  "ds_read_b128 v[180:183], v212 offset:49408\n\t" \
  "s_waitcnt lgkmcnt(6)\n\t" \
  "v_mfma_f32_16x16x32_bf16 v[144:147], v[184:187], v[72:75], v[144:147]\n\t" \
  "v_mfma_f32_16x16x32_bf16 v[152:155], v[184:187], v[76:79], v[152:155]\n\t" \
  "ds_read_b128 v[184:187], v213 offset:33024\n\t" \
  "s_add_u32 m0, %[lw], 34816\n\t" \
  "v_mfma_f32_16x16x32_bf16 v[148:151], v[188:191], v[72:75], v[148:151]\n\t" \
  "v_mfma_f32_16x16x32_bf16 v[156:159], v[188:191], v[76:79], v[156:159]\n\t" \
  "ds_read_b128 v[188:191], v213 offset:49408\n\t" \
  "global_load_lds_dwordx4 v218, s[92:93] sc1\n\t" \
  "s_waitcnt lgkmcnt(6)\n\t" \
  "v_mfma_f32_16x16x32_bf16 v[144:147], v[192:195], v[80:83], v[144:147]\n\t" \
  "v_mfma_f32_16x16x32_bf16 v[152:155], v[192:195], v[84:87], v[152:155]\n\t" \
  "ds_read_b128 v[192:195], v214 offset:33024\n\t" \
  "v_mfma_f32_16x16x32_bf16 v[148:151], v[196:199], v[80:83], v[148:151]\n\t" \
  "v_mfma_f32_16x16x32_bf16 v[156:159], v[196:199], v[84:87], v[156:159]\n\t" \
  "ds_read_b128 v[196:199], v214 offset:49408\n\t" \
  "s_waitcnt lgkmcnt(6)\n\t" \
  "v_mfma_f32_16x16x32_bf16 v[144:147], v[200:203], v[88:91], v[144:147]\n\t" \
  "v_mfma_f32_16x16x32_bf16 v[152:155], v[200:203], v[92:95], v[152:155]\n\t" \
  "ds_read_b128 v[200:203], v215 offset:33024\n\t" \
  "s_add_u32 m0, %[lw], 35840\n\t" \
  "v_mfma_f32_16x16x32_bf16 v[148:151], v[204:207], v[88:91], v[148:151]\n\t" \
  "v_mfma_f32_16x16x32_bf16 v[156:159], v[204:207], v[92:95], v[156:159]\n\t" \
  "ds_read_b128 v[204:207], v215 offset:49408\n\t" \
  "global_load_lds_dwordx4 v219, s[92:93] sc1\n\t" \
  "s_cmp_lt_u32 s86, 8\n\t" \
  "s_cbranch_scc1 .Lw31%=\n\t" \
  "s_waitcnt vmcnt(6)\n\t" \
  "s_barrier\n\t" \
  ".Lg31%=:\n\t" \
  "s_add_u32 s92, s92, 65024\n\t" \
  "s_addc_u32 s93, s93, 0\n\t" \
  "s_waitcnt lgkmcnt(6)\n\t" \
  "v_mfma_f32_16x16x32_bf16 v[144:147], v[176:179], v[96:99], v[144:147]\n\t" \
  "v_mfma_f32_16x16x32_bf16 v[152:155], v[176:179], v[100:103], v[152:155]\n\t" \
  "ds_read_b128 v[176:179], v208 offset:0\n\t" \
  "v_mfma_f32_16x16x32_bf16 v[148:151], v[180:183], v[96:99], v[148:151]\n\t" \
  "v_mfma_f32_16x16x32_bf16 v[156:159], v[180:183], v[100:103], v[156:159]\n\t" \
  "ds_read_b128 v[180:183], v208 offset:16384\n\t" \
  "s_waitcnt lgkmcnt(6)\n\t" \
  "v_mfma_f32_16x16x32_bf16 v[144:147], v[184:187], v[104:107], v[144:147]\n\t" \
  "v_mfma_f32_16x16x32_bf16 v[152:155], v[184:187], v[108:111], v[152:155]\n\t" \
  "ds_read_b128 v[184:187], v209 offset:0\n\t" \
  "s_add_u32 m0, %[lw], 65536\n\t" \
  "v_mfma_f32_16x16x32_bf16 v[148:151], v[188:191], v[104:107], v[148:151]\n\t" \
  "v_mfma_f32_16x16x32_bf16 v[156:159], v[188:191], v[108:111], v[156:159]\n\t" \
  "ds_read_b128 v[188:191], v209 offset:16384\n\t" \
  "global_load_lds_dwordx4 v216, s[92:93] sc1\n\t" \
  "s_waitcnt lgkmcnt(6)\n\t" \
  "v_mfma_f32_16x16x32_bf16 v[144:147], v[192:195], v[112:115], v[144:147]\n\t" \
  "v_mfma_f32_16x16x32_bf16 v[152:155], v[192:195], v[116:119], v[152:155]\n\t" \
  "ds_read_b128 v[192:195], v210 offset:0\n\t" \
  "v_mfma_f32_16x16x32_bf16 v[148:151], v[196:199], v[112:115], v[148:151]\n\t" \
  "v_mfma_f32_16x16x32_bf16 v[156:159], v[196:199], v[116:119], v[156:159]\n\t" \
  "ds_read_b128 v[196:199], v210 offset:16384\n\t" \
  "s_waitcnt lgkmcnt(6)\n\t" \
  "v_mfma_f32_16x16x32_bf16 v[144:147], v[200:203], v[120:123], v[144:147]\n\t" \
  "v_mfma_f32_16x16x32_bf16 v[152:155], v[200:203], v[124:127], v[152:155]\n\t" \
  "ds_read_b128 v[200:203], v211 offset:0\n\t" \
  "s_add_u32 m0, %[lw], 66560\n\t" \
  "v_mfma_f32_16x16x32_bf16 v[148:151], v[204:207], v[120:123], v[148:151]\n\t" \
  "v_mfma_f32_16x16x32_bf16 v[156:159], v[204:207], v[124:127], v[156:159]\n\t" \
  "ds_read_b128 v[204:207], v211 offset:16384\n\t" \
  "global_load_lds_dwordx4 v217, s[92:93] sc1\n\t" \
  "s_add_u32 s86, s86, 1\n\t" \
  "s_add_u32 s87, s87, 1\n\t" \
  "s_cmp_lt_u32 s86, %[end]\n\t" \
  "s_cbranch_scc1 .Lp1%=\n\t" \
  "s_branch .Lx1%=\n\t" \
  ".Lf01%=:\n\t" \
  "s_nop 7\n\t" \
  "ds_write_b128 v220, v[152:155] offset:0\n\t" \
  "ds_write_b128 v220, v[156:159] offset:1024\n\t" \
  "s_lshr_b32 s90, s87, 7\n\t" \
  "s_xor_b32 s90, s90, %[s0]\n\t" \
  "s_and_b32 s90, s90, 1\n\t" \
  "s_lshl1_add_u32 s90, s87, s90\n\t" \
  "s_lshl_b32 s90, s90, 13\n\t" \
  "s_cmp_lt_i32 s86, 1\n\t" \
  "s_cselect_b32 s90, 0x70000000, s90\n\t" \
  "s_waitcnt lgkmcnt(0)\n\t" \
  "s_barrier\n\t" \
  "ds_read_b128 v[160:163], v221 offset:0\n\t" \
  "ds_read_b128 v[164:167], v221 offset:1024\n\t" \
  "s_waitcnt lgkmcnt(0)\n\t" \
  "v_add_f32 v160, v160, v144\n\t" \
  "v_add_f32 v161, v161, v145\n\t" \
  "v_add_f32 v162, v162, v146\n\t" \
  "v_add_f32 v163, v163, v147\n\t" \
  "v_add_f32 v164, v164, v148\n\t" \
  "v_add_f32 v165, v165, v149\n\t" \
  "v_add_f32 v166, v166, v150\n\t" \
  "v_add_f32 v167, v167, v151\n\t" \
  "v_cvt_pk_bf16_f32 v168, v160, v161\n\t" \
  "v_cvt_pk_bf16_f32 v169, v162, v163\n\t" \
  "v_cvt_pk_bf16_f32 v170, v164, v165\n\t" \
  "v_cvt_pk_bf16_f32 v171, v166, v167\n\t" \
  "buffer_store_dwordx4 v[168:171], v172, %[rt], s90 offen sc1\n\t" \
  "s_branch .Lfb01%=\n\t" \
  ".Lf11%=:\n\t" \
  "s_nop 7\n\t" \
  "ds_write_b128 v220, v[136:139] offset:0\n\t" \
  "ds_write_b128 v220, v[140:143] offset:1024\n\t" \
  "s_lshr_b32 s90, s87, 7\n\t" \
  "s_xor_b32 s90, s90, %[s0]\n\t" \
  "s_and_b32 s90, s90, 1\n\t" \
  "s_lshl1_add_u32 s90, s87, s90\n\t" \
  "s_lshl_b32 s90, s90, 13\n\t" \
  "s_cmp_lt_i32 s86, 1\n\t" \
  "s_cselect_b32 s90, 0x70000000, s90\n\t" \
  "s_waitcnt lgkmcnt(0)\n\t" \
  "s_barrier\n\t" \
  "ds_read_b128 v[160:163], v221 offset:0\n\t" \
  "ds_read_b128 v[164:167], v221 offset:1024\n\t" \
  "s_waitcnt lgkmcnt(0)\n\t" \
  "v_add_f32 v160, v160, v128\n\t" \
  "v_add_f32 v161, v161, v129\n\t" \
  "v_add_f32 v162, v162, v130\n\t" \
  "v_add_f32 v163, v163, v131\n\t" \
  "v_add_f32 v164, v164, v132\n\t" \
  "v_add_f32 v165, v165, v133\n\t" \
  "v_add_f32 v166, v166, v134\n\t" \
  "v_add_f32 v167, v167, v135\n\t" \
  "v_cvt_pk_bf16_f32 v168, v160, v161\n\t" \
  "v_cvt_pk_bf16_f32 v169, v162, v163\n\t" \
  "v_cvt_pk_bf16_f32 v170, v164, v165\n\t" \
  "v_cvt_pk_bf16_f32 v171, v166, v167\n\t" \
  "buffer_store_dwordx4 v[168:171], v172, %[rt], s90 offen sc1\n\t" \
  "s_branch .Lfb11%=\n\t" \
  ".Lw01%=:\n\t" \
  "s_waitcnt vmcnt(2)\n\t" \
  "s_barrier\n\t" \
  "s_sub_i32 s91, s86, 1\n\t" \
  "s_max_i32 s88, s88, s91\n\t" \
  "v_mov_b32 v173, s88\n\t" \
  "buffer_store_dword v173, v174, %[rs], 0 offen sc1\n\t" \
  "s_branch .Lg01%=\n\t" \
  ".Lw11%=:\n\t" \
  "s_waitcnt vmcnt(2)\n\t" \
  "s_barrier\n\t" \
  "s_sub_i32 s91, s86, 1\n\t" \
  "s_max_i32 s88, s88, s91\n\t" \
  "v_mov_b32 v173, s88\n\t" \
  "buffer_store_dword v173, v174, %[rs], 0 offen sc1\n\t" \
  "s_branch .Lg11%=\n\t" \
  ".Lw21%=:\n\t" \
  "s_waitcnt vmcnt(2)\n\t" \
  "s_barrier\n\t" \
  "s_sub_i32 s91, s86, 1\n\t" \
  "s_max_i32 s88, s88, s91\n\t" \
  "v_mov_b32 v173, s88\n\t" \
  "buffer_store_dword v173, v174, %[rs], 0 offen sc1\n\t" \
  "s_branch .Lg21%=\n\t" \
  ".Lw31%=:\n\t" \
  "s_waitcnt vmcnt(2)\n\t" \
  "s_barrier\n\t" \
  "s_sub_i32 s91, s86, 1\n\t" \
  "s_max_i32 s88, s88, s91\n\t" \
  "v_mov_b32 v173, s88\n\t" \
  "buffer_store_dword v173, v174, %[rs], 0 offen sc1\n\t" \
  "s_branch .Lg31%=\n\t" \
  ".Lx1%=:\n\t" \
  "s_waitcnt lgkmcnt(0)\n\t" \
  "s_setprio 0\n\t" \
  "s_mov_b32 m0, s94\n\t" \
  ".Le%=:\n\t"
#define PP_ASM_0 \
  "s_cmp_lg_u32 %[kh], 0\n\t" \
  "s_cbranch_scc1 .Lk%=\n\t" \
  "s_mov_b32 s94, m0\n\t" \
  "s_mov_b64 s[92:93], %[x0]\n\t" \
  "s_mov_b32 s86, %[g0]\n\t" \
  "s_add_u32 s87, s86, %[t0]\n\t" \
  "s_add_u32 m0, %[lw], 67584\n\t" \
  "s_nop 0\n\t" \
  "global_load_lds_dwordx4 v250, s[92:93] sc1\n\t" \
  "s_add_u32 m0, %[lw], 68608\n\t" \
  "s_nop 0\n\t" \
  "global_load_lds_dwordx4 v251, s[92:93] sc1\n\t" \
  "s_barrier\n\t" \
  "s_branch .Ls0%=\n\t" \
  ".Lp0%=:\n\t" \
  "s_waitcnt vmcnt(9)\n\t" \
  "s_barrier\n\t" \
  ".Ls0%=:\n\t" \
  "s_add_u32 s92, s92, 512\n\t" \
  "s_addc_u32 s93, s93, 0\n\t" \
  "ds_read_b128 v[176:179], v240 offset:0\n\t" \
  "ds_read_b128 v[180:183], v240 offset:16384\n\t" \
  "ds_read_b128 v[184:187], v241 offset:0\n\t" \
  "ds_read_b128 v[188:191], v241 offset:16384\n\t" \
  "s_add_u32 m0, %[lw], 98304\n\t" \
  "s_nop 0\n\t" \
  "global_load_lds_dwordx4 v248, s[92:93] sc1\n\t" \
  "ds_read_b128 v[192:195], v242 offset:0\n\t" \
  "ds_read_b128 v[196:199], v242 offset:16384\n\t" \
  "ds_read_b128 v[200:203], v243 offset:0\n\t" \
  "ds_read_b128 v[204:207], v243 offset:16384\n\t" \
  "s_add_u32 m0, %[lw], 99328\n\t" \
  "s_nop 0\n\t" \
  "global_load_lds_dwordx4 v249, s[92:93] sc1\n\t" \
  "ds_read_b128 v[208:211], v240 offset:256\n\t" \
  "ds_read_b128 v[212:215], v240 offset:16640\n\t" \
  "ds_read_b128 v[216:219], v241 offset:256\n\t" \
  "ds_read_b128 v[220:223], v241 offset:16640\n\t" \
  "s_add_u32 m0, %[lw], 100352\n\t" \
  "s_nop 0\n\t" \
  "global_load_lds_dwordx4 v250, s[92:93] sc1\n\t" \
  "ds_read_b128 v[224:227], v242 offset:256\n\t" \
  "ds_read_b128 v[228:231], v242 offset:16640\n\t" \
  "ds_read_b128 v[232:235], v243 offset:256\n\t" \
  "ds_read_b128 v[236:239], v243 offset:16640\n\t" \
  "s_add_u32 m0, %[lw], 101376\n\t" \
  "s_nop 0\n\t" \
  "global_load_lds_dwordx4 v251, s[92:93] sc1\n\t" \
  "ds_write_b128 v252, v[152:155] offset:0\n\t" \
  "ds_write_b128 v252, v[156:159] offset:1024\n\t" \
  "s_waitcnt lgkmcnt(0)\n\t" \
  "s_setprio 1\n\t" \
  "v_mfma_f32_16x16x32_bf16 v[128:131], v[0:3], v[176:179], 0\n\t" \
  "v_mfma_f32_16x16x32_bf16 v[132:135], v[4:7], v[176:179], 0\n\t" \
  "v_mfma_f32_16x16x32_bf16 v[136:139], v[0:3], v[180:183], 0\n\t" \
  "v_mfma_f32_16x16x32_bf16 v[140:143], v[4:7], v[180:183], 0\n\t" \
  "v_mfma_f32_16x16x32_bf16 v[128:131], v[8:11], v[184:187], v[128:131]\n\t" \
  "v_mfma_f32_16x16x32_bf16 v[132:135], v[12:15], v[184:187], v[132:135]\n\t" \
  "v_mfma_f32_16x16x32_bf16 v[136:139], v[8:11], v[188:191], v[136:139]\n\t" \
  "v_mfma_f32_16x16x32_bf16 v[140:143], v[12:15], v[188:191], v[140:143]\n\t" \
  "v_mfma_f32_16x16x32_bf16 v[128:131], v[16:19], v[192:195], v[128:131]\n\t" \
  "v_mfma_f32_16x16x32_bf16 v[132:135], v[20:23], v[192:195], v[132:135]\n\t" \
  "v_mfma_f32_16x16x32_bf16 v[136:139], v[16:19], v[196:199], v[136:139]\n\t" \
  "v_mfma_f32_16x16x32_bf16 v[140:143], v[20:23], v[196:199], v[140:143]\n\t" \
  "v_mfma_f32_16x16x32_bf16 v[128:131], v[24:27], v[200:203], v[128:131]\n\t" \
  "v_mfma_f32_16x16x32_bf16 v[132:135], v[28:31], v[200:203], v[132:135]\n\t" \
  "v_mfma_f32_16x16x32_bf16 v[136:139], v[24:27], v[204:207], v[136:139]\n\t" \
  "v_mfma_f32_16x16x32_bf16 v[140:143], v[28:31], v[204:207], v[140:143]\n\t" \
  "v_mfma_f32_16x16x32_bf16 v[128:131], v[32:35], v[208:211], v[128:131]\n\t" \
  "v_mfma_f32_16x16x32_bf16 v[132:135], v[36:39], v[208:211], v[132:135]\n\t" \
  "v_mfma_f32_16x16x32_bf16 v[136:139], v[32:35], v[212:215], v[136:139]\n\t" \
  "v_mfma_f32_16x16x32_bf16 v[140:143], v[36:39], v[212:215], v[140:143]\n\t" \
  "v_mfma_f32_16x16x32_bf16 v[128:131], v[40:43], v[216:219], v[128:131]\n\t" \
  "v_mfma_f32_16x16x32_bf16 v[132:135], v[44:47], v[216:219], v[132:135]\n\t" \
  "v_mfma_f32_16x16x32_bf16 v[136:139], v[40:43], v[220:223], v[136:139]\n\t" \
  "v_mfma_f32_16x16x32_bf16 v[140:143], v[44:47], v[220:223], v[140:143]\n\t" \
  "v_mfma_f32_16x16x32_bf16 v[128:131], v[48:51], v[224:227], v[128:131]\n\t" \
  "v_mfma_f32_16x16x32_bf16 v[132:135], v[52:55], v[224:227], v[132:135]\n\t" \
  "v_mfma_f32_16x16x32_bf16 v[136:139], v[48:51], v[228:231], v[136:139]\n\t" \
  "v_mfma_f32_16x16x32_bf16 v[140:143], v[52:55], v[228:231], v[140:143]\n\t" \
  "v_mfma_f32_16x16x32_bf16 v[128:131], v[56:59], v[232:235], v[128:131]\n\t" \
  "v_mfma_f32_16x16x32_bf16 v[132:135], v[60:63], v[232:235], v[132:135]\n\t" \
  "v_mfma_f32_16x16x32_bf16 v[136:139], v[56:59], v[236:239], v[136:139]\n\t" \
  "v_mfma_f32_16x16x32_bf16 v[140:143], v[60:63], v[236:239], v[140:143]\n\t" \
  "s_setprio 0\n\t" \
  "s_waitcnt vmcnt(4)\n\t" \
  "s_barrier\n\t" \
  "s_add_u32 s92, s92, 65024\n\t" \
  "s_addc_u32 s93, s93, 0\n\t" \
  "ds_read_b128 v[160:163], v253 offset:0\n\t" \
  "ds_read_b128 v[164:167], v253 offset:1024\n\t" \
  "ds_read_b128 v[176:179], v240 offset:32768\n\t" \
  "ds_read_b128 v[180:183], v240 offset:49152\n\t" \
  "ds_read_b128 v[184:187], v241 offset:32768\n\t" \
  "ds_read_b128 v[188:191], v241 offset:49152\n\t" \
  "s_add_u32 m0, %[lw], 0\n\t" \
  "s_nop 0\n\t" \
  "global_load_lds_dwordx4 v248, s[92:93] sc1\n\t" \
  "ds_read_b128 v[192:195], v242 offset:32768\n\t" \
  "ds_read_b128 v[196:199], v242 offset:49152\n\t" \
  "ds_read_b128 v[200:203], v243 offset:32768\n\t" \
  "ds_read_b128 v[204:207], v243 offset:49152\n\t" \
  "s_add_u32 m0, %[lw], 1024\n\t" \
  "s_nop 0\n\t" \
  "global_load_lds_dwordx4 v249, s[92:93] sc1\n\t" \
  "ds_read_b128 v[208:211], v240 offset:33024\n\t" \
  "ds_read_b128 v[212:215], v240 offset:49408\n\t" \
  "ds_read_b128 v[216:219], v241 offset:33024\n\t" \
  "ds_read_b128 v[220:223], v241 offset:49408\n\t" \
  "s_add_u32 m0, %[lw], 2048\n\t" \
  "s_nop 0\n\t" \
  "global_load_lds_dwordx4 v250, s[92:93] sc1\n\t" \
  "ds_read_b128 v[224:227], v242 offset:33024\n\t" \
  "ds_read_b128 v[228:231], v242 offset:49408\n\t" \
  "ds_read_b128 v[232:235], v243 offset:33024\n\t" \
  "ds_read_b128 v[236:239], v243 offset:49408\n\t" \
  "s_add_u32 m0, %[lw], 3072\n\t" \
  "s_nop 0\n\t" \
  "global_load_lds_dwordx4 v251, s[92:93] sc1\n\t" \
  "s_waitcnt lgkmcnt(15)\n\t" \
  "s_waitcnt lgkmcnt(12)\n\t" \
  "s_lshr_b32 s90, s87, 7\n\t" \
  "s_xor_b32 s90, s90, %[s0]\n\t" \
  "s_and_b32 s90, s90, 1\n\t" \
  "s_lshl1_add_u32 s90, s87, s90\n\t" \
  "s_lshl_b32 s90, s90, 13\n\t" \
  "v_add_f32 v160, v160, v144\n\t" \
  "v_add_f32 v161, v161, v145\n\t" \
  "v_add_f32 v162, v162, v146\n\t" \
  "v_add_f32 v163, v163, v147\n\t" \
  "v_add_f32 v164, v164, v148\n\t" \
  "v_add_f32 v165, v165, v149\n\t" \
  "v_add_f32 v166, v166, v150\n\t" \
  "v_add_f32 v167, v167, v151\n\t" \
  "v_cvt_pk_bf16_f32 v168, v160, v161\n\t" \
  "v_cvt_pk_bf16_f32 v169, v162, v163\n\t" \
  "v_cvt_pk_bf16_f32 v170, v164, v165\n\t" \
  "v_cvt_pk_bf16_f32 v171, v166, v167\n\t" \
  "buffer_store_dwordx4 v[168:171], v172, %[rt], s90 offen sc1\n\t" \
  "s_add_u32 s86, s86, 1\n\t" \
  "s_add_u32 s87, s87, 1\n\t" \
  "s_waitcnt lgkmcnt(0)\n\t" \
  "s_setprio 1\n\t" \
  "v_mfma_f32_16x16x32_bf16 v[128:131], v[64:67], v[176:179], v[128:131]\n\t" \
  "v_mfma_f32_16x16x32_bf16 v[132:135], v[68:71], v[176:179], v[132:135]\n\t" \
  "v_mfma_f32_16x16x32_bf16 v[136:139], v[64:67], v[180:183], v[136:139]\n\t" \
  "v_mfma_f32_16x16x32_bf16 v[140:143], v[68:71], v[180:183], v[140:143]\n\t" \
  "v_mfma_f32_16x16x32_bf16 v[128:131], v[72:75], v[184:187], v[128:131]\n\t" \
  "v_mfma_f32_16x16x32_bf16 v[132:135], v[76:79], v[184:187], v[132:135]\n\t" \
  "v_mfma_f32_16x16x32_bf16 v[136:139], v[72:75], v[188:191], v[136:139]\n\t" \
  "v_mfma_f32_16x16x32_bf16 v[140:143], v[76:79], v[188:191], v[140:143]\n\t" \
  "v_mfma_f32_16x16x32_bf16 v[128:131], v[80:83], v[192:195], v[128:131]\n\t" \
  "v_mfma_f32_16x16x32_bf16 v[132:135], v[84:87], v[192:195], v[132:135]\n\t" \
  "v_mfma_f32_16x16x32_bf16 v[136:139], v[80:83], v[196:199], v[136:139]\n\t" \
  "v_mfma_f32_16x16x32_bf16 v[140:143], v[84:87], v[196:199], v[140:143]\n\t" \
  "v_mfma_f32_16x16x32_bf16 v[128:131], v[88:91], v[200:203], v[128:131]\n\t" \
  "v_mfma_f32_16x16x32_bf16 v[132:135], v[92:95], v[200:203], v[132:135]\n\t" \
  "v_mfma_f32_16x16x32_bf16 v[136:139], v[88:91], v[204:207], v[136:139]\n\t" \
  "v_mfma_f32_16x16x32_bf16 v[140:143], v[92:95], v[204:207], v[140:143]\n\t" \
  "v_mfma_f32_16x16x32_bf16 v[128:131], v[96:99], v[208:211], v[128:131]\n\t" \
  "v_mfma_f32_16x16x32_bf16 v[132:135], v[100:103], v[208:211], v[132:135]\n\t" \
  "v_mfma_f32_16x16x32_bf16 v[136:139], v[96:99], v[212:215], v[136:139]\n\t" \
  "v_mfma_f32_16x16x32_bf16 v[140:143], v[100:103], v[212:215], v[140:143]\n\t" \
  "v_mfma_f32_16x16x32_bf16 v[128:131], v[104:107], v[216:219], v[128:131]\n\t" \
  "v_mfma_f32_16x16x32_bf16 v[132:135], v[108:111], v[216:219], v[132:135]\n\t" \
  "v_mfma_f32_16x16x32_bf16 v[136:139], v[104:107], v[220:223], v[136:139]\n\t" \
  "v_mfma_f32_16x16x32_bf16 v[140:143], v[108:111], v[220:223], v[140:143]\n\t" \
  "v_mfma_f32_16x16x32_bf16 v[128:131], v[112:115], v[224:227], v[128:131]\n\t" \
  "v_mfma_f32_16x16x32_bf16 v[132:135], v[116:119], v[224:227], v[132:135]\n\t" \
  "v_mfma_f32_16x16x32_bf16 v[136:139], v[112:115], v[228:231], v[136:139]\n\t" \
  "v_mfma_f32_16x16x32_bf16 v[140:143], v[116:119], v[228:231], v[140:143]\n\t" \
  "v_mfma_f32_16x16x32_bf16 v[128:131], v[120:123], v[232:235], v[128:131]\n\t" \
  "v_mfma_f32_16x16x32_bf16 v[132:135], v[124:127], v[232:235], v[132:135]\n\t" \
  "v_mfma_f32_16x16x32_bf16 v[136:139], v[120:123], v[236:239], v[136:139]\n\t" \
  "v_mfma_f32_16x16x32_bf16 v[140:143], v[124:127], v[236:239], v[140:143]\n\t" \
  "s_setprio 0\n\t" \
  "s_waitcnt vmcnt(9)\n\t" \
  "s_barrier\n\t" \
  "s_add_u32 s92, s92, 512\n\t" \
  "s_addc_u32 s93, s93, 0\n\t" \
  "ds_read_b128 v[176:179], v244 offset:0\n\t" \
  "ds_read_b128 v[180:183], v244 offset:16384\n\t" \
  "ds_read_b128 v[184:187], v245 offset:0\n\t" \
  "ds_read_b128 v[188:191], v245 offset:16384\n\t" \
  "s_add_u32 m0, %[lw], 32768\n\t" \
  "s_nop 0\n\t" \
  "global_load_lds_dwordx4 v248, s[92:93] sc1\n\t" \
  "ds_read_b128 v[192:195], v246 offset:0\n\t" \
  "ds_read_b128 v[196:199], v246 offset:16384\n\t" \
  "ds_read_b128 v[200:203], v247 offset:0\n\t" \
  "ds_read_b128 v[204:207], v247 offset:16384\n\t" \
  "s_add_u32 m0, %[lw], 33792\n\t" \
  "s_nop 0\n\t" \
  "global_load_lds_dwordx4 v249, s[92:93] sc1\n\t" \
  "ds_read_b128 v[208:211], v244 offset:256\n\t" \
  "ds_read_b128 v[212:215], v244 offset:16640\n\t" \
  "ds_read_b128 v[216:219], v245 offset:256\n\t" \
  "ds_read_b128 v[220:223], v245 offset:16640\n\t" \
  "s_add_u32 m0, %[lw], 34816\n\t" \
  "s_nop 0\n\t" \
  "global_load_lds_dwordx4 v250, s[92:93] sc1\n\t" \
  "ds_read_b128 v[224:227], v246 offset:256\n\t" \
  "ds_read_b128 v[228:231], v246 offset:16640\n\t" \
  "ds_read_b128 v[232:235], v247 offset:256\n\t" \
  "ds_read_b128 v[236:239], v247 offset:16640\n\t" \
  "s_add_u32 m0, %[lw], 35840\n\t" \
  "s_nop 0\n\t" \
  "global_load_lds_dwordx4 v251, s[92:93] sc1\n\t" \
  "ds_write_b128 v252, v[136:139] offset:0\n\t" \
  "ds_write_b128 v252, v[140:143] offset:1024\n\t" \
  "s_waitcnt lgkmcnt(0)\n\t" \
  "s_setprio 1\n\t" \
  "v_mfma_f32_16x16x32_bf16 v[144:147], v[0:3], v[176:179], 0\n\t" \
  "v_mfma_f32_16x16x32_bf16 v[148:151], v[4:7], v[176:179], 0\n\t" \
  "v_mfma_f32_16x16x32_bf16 v[152:155], v[0:3], v[180:183], 0\n\t" \
  "v_mfma_f32_16x16x32_bf16 v[156:159], v[4:7], v[180:183], 0\n\t" \
  "v_mfma_f32_16x16x32_bf16 v[144:147], v[8:11], v[184:187], v[144:147]\n\t" \
  "v_mfma_f32_16x16x32_bf16 v[148:151], v[12:15], v[184:187], v[148:151]\n\t" \
  "v_mfma_f32_16x16x32_bf16 v[152:155], v[8:11], v[188:191], v[152:155]\n\t" \
  "v_mfma_f32_16x16x32_bf16 v[156:159], v[12:15], v[188:191], v[156:159]\n\t" \
  "v_mfma_f32_16x16x32_bf16 v[144:147], v[16:19], v[192:195], v[144:147]\n\t" \
  "v_mfma_f32_16x16x32_bf16 v[148:151], v[20:23], v[192:195], v[148:151]\n\t" \
  "v_mfma_f32_16x16x32_bf16 v[152:155], v[16:19], v[196:199], v[152:155]\n\t" \
  "v_mfma_f32_16x16x32_bf16 v[156:159], v[20:23], v[196:199], v[156:159]\n\t" \
  "v_mfma_f32_16x16x32_bf16 v[144:147], v[24:27], v[200:203], v[144:147]\n\t" \
  "v_mfma_f32_16x16x32_bf16 v[148:151], v[28:31], v[200:203], v[148:151]\n\t" \
  "v_mfma_f32_16x16x32_bf16 v[152:155], v[24:27], v[204:207], v[152:155]\n\t" \
  "v_mfma_f32_16x16x32_bf16 v[156:159], v[28:31], v[204:207], v[156:159]\n\t" \
  "v_mfma_f32_16x16x32_bf16 v[144:147], v[32:35], v[208:211], v[144:147]\n\t" \
  "v_mfma_f32_16x16x32_bf16 v[148:151], v[36:39], v[208:211], v[148:151]\n\t" \
  "v_mfma_f32_16x16x32_bf16 v[152:155], v[32:35], v[212:215], v[152:155]\n\t" \
  "v_mfma_f32_16x16x32_bf16 v[156:159], v[36:39], v[212:215], v[156:159]\n\t" \
  "v_mfma_f32_16x16x32_bf16 v[144:147], v[40:43], v[216:219], v[144:147]\n\t" \
  "v_mfma_f32_16x16x32_bf16 v[148:151], v[44:47], v[216:219], v[148:151]\n\t" \
  "v_mfma_f32_16x16x32_bf16 v[152:155], v[40:43], v[220:223], v[152:155]\n\t" \
  "v_mfma_f32_16x16x32_bf16 v[156:159], v[44:47], v[220:223], v[156:159]\n\t" \
  "v_mfma_f32_16x16x32_bf16 v[144:147], v[48:51], v[224:227], v[144:147]\n\t" \
  "v_mfma_f32_16x16x32_bf16 v[148:151], v[52:55], v[224:227], v[148:151]\n\t" \
  "v_mfma_f32_16x16x32_bf16 v[152:155], v[48:51], v[228:231], v[152:155]\n\t" \
  "v_mfma_f32_16x16x32_bf16 v[156:159], v[52:55], v[228:231], v[156:159]\n\t" \
  "v_mfma_f32_16x16x32_bf16 v[144:147], v[56:59], v[232:235], v[144:147]\n\t" \
  "v_mfma_f32_16x16x32_bf16 v[148:151], v[60:63], v[232:235], v[148:151]\n\t" \
  "v_mfma_f32_16x16x32_bf16 v[152:155], v[56:59], v[236:239], v[152:155]\n\t" \
  "v_mfma_f32_16x16x32_bf16 v[156:159], v[60:63], v[236:239], v[156:159]\n\t" \
  "s_setprio 0\n\t" \
  "s_waitcnt vmcnt(4)\n\t" \
  "s_barrier\n\t" \
  "s_add_u32 s92, s92, 65024\n\t" \
  "s_addc_u32 s93, s93, 0\n\t" \
  "ds_read_b128 v[160:163], v253 offset:0\n\t" \
  "ds_read_b128 v[164:167], v253 offset:1024\n\t" \
  "ds_read_b128 v[176:179], v244 offset:32768\n\t" \
  "ds_read_b128 v[180:183], v244 offset:49152\n\t" \
  "ds_read_b128 v[184:187], v245 offset:32768\n\t" \
  "ds_read_b128 v[188:191], v245 offset:49152\n\t" \
  "s_add_u32 m0, %[lw], 65536\n\t" \
  "s_nop 0\n\t" \
  "global_load_lds_dwordx4 v248, s[92:93] sc1\n\t" \
  "ds_read_b128 v[192:195], v246 offset:32768\n\t" \
  "ds_read_b128 v[196:199], v246 offset:49152\n\t" \
  "ds_read_b128 v[200:203], v247 offset:32768\n\t" \
  "ds_read_b128 v[204:207], v247 offset:49152\n\t" \
  "s_add_u32 m0, %[lw], 66560\n\t" \
  "s_nop 0\n\t" \
  "global_load_lds_dwordx4 v249, s[92:93] sc1\n\t" \
  "ds_read_b128 v[208:211], v244 offset:33024\n\t" \
  "ds_read_b128 v[212:215], v244 offset:49408\n\t" \
  "ds_read_b128 v[216:219], v245 offset:33024\n\t" \
  "ds_read_b128 v[220:223], v245 offset:49408\n\t" \
  "s_add_u32 m0, %[lw], 67584\n\t" \
  "s_nop 0\n\t" \
  "global_load_lds_dwordx4 v250, s[92:93] sc1\n\t" \
  "ds_read_b128 v[224:227], v246 offset:33024\n\t" \
  "ds_read_b128 v[228:231], v246 offset:49408\n\t" \
  "ds_read_b128 v[232:235], v247 offset:33024\n\t" \
  "ds_read_b128 v[236:239], v247 offset:49408\n\t" \
  "s_add_u32 m0, %[lw], 68608\n\t" \
  "s_nop 0\n\t" \
  "global_load_lds_dwordx4 v251, s[92:93] sc1\n\t" \
  "s_waitcnt lgkmcnt(15)\n\t" \
  "s_waitcnt lgkmcnt(12)\n\t" \
  "s_lshr_b32 s90, s87, 7\n\t" \
  "s_xor_b32 s90, s90, %[s0]\n\t" \
  "s_and_b32 s90, s90, 1\n\t" \
  "s_lshl1_add_u32 s90, s87, s90\n\t" \
  "s_lshl_b32 s90, s90, 13\n\t" \
  "v_add_f32 v160, v160, v128\n\t" \
  "v_add_f32 v161, v161, v129\n\t" \
  "v_add_f32 v162, v162, v130\n\t" \
  "v_add_f32 v163, v163, v131\n\t" \
  "v_add_f32 v164, v164, v132\n\t" \
  "v_add_f32 v165, v165, v133\n\t" \
  "v_add_f32 v166, v166, v134\n\t" \
  "v_add_f32 v167, v167, v135\n\t" \
  "v_cvt_pk_bf16_f32 v168, v160, v161\n\t" \
  "v_cvt_pk_bf16_f32 v169, v162, v163\n\t" \
  "v_cvt_pk_bf16_f32 v170, v164, v165\n\t" \
  "v_cvt_pk_bf16_f32 v171, v166, v167\n\t" \
  "buffer_store_dwordx4 v[168:171], v172, %[rt], s90 offen sc1\n\t" \
  "s_add_u32 s86, s86, 1\n\t" \
  "s_add_u32 s87, s87, 1\n\t" \
  "s_waitcnt lgkmcnt(0)\n\t" \
  "s_setprio 1\n\t" \
  "v_mfma_f32_16x16x32_bf16 v[144:147], v[64:67], v[176:179], v[144:147]\n\t" \
  "v_mfma_f32_16x16x32_bf16 v[148:151], v[68:71], v[176:179], v[148:151]\n\t" \
  "v_mfma_f32_16x16x32_bf16 v[152:155], v[64:67], v[180:183], v[152:155]\n\t" \
  "v_mfma_f32_16x16x32_bf16 v[156:159], v[68:71], v[180:183], v[156:159]\n\t" \
  "v_mfma_f32_16x16x32_bf16 v[144:147], v[72:75], v[184:187], v[144:147]\n\t" \
  "v_mfma_f32_16x16x32_bf16 v[148:151], v[76:79], v[184:187], v[148:151]\n\t" \
  "v_mfma_f32_16x16x32_bf16 v[152:155], v[72:75], v[188:191], v[152:155]\n\t" \
  "v_mfma_f32_16x16x32_bf16 v[156:159], v[76:79], v[188:191], v[156:159]\n\t" \
  "v_mfma_f32_16x16x32_bf16 v[144:147], v[80:83], v[192:195], v[144:147]\n\t" \
  "v_mfma_f32_16x16x32_bf16 v[148:151], v[84:87], v[192:195], v[148:151]\n\t" \
  "v_mfma_f32_16x16x32_bf16 v[152:155], v[80:83], v[196:199], v[152:155]\n\t" \
  "v_mfma_f32_16x16x32_bf16 v[156:159], v[84:87], v[196:199], v[156:159]\n\t" \
  "v_mfma_f32_16x16x32_bf16 v[144:147], v[88:91], v[200:203], v[144:147]\n\t" \
  "v_mfma_f32_16x16x32_bf16 v[148:151], v[92:95], v[200:203], v[148:151]\n\t" \
  "v_mfma_f32_16x16x32_bf16 v[152:155], v[88:91], v[204:207], v[152:155]\n\t" \
  "v_mfma_f32_16x16x32_bf16 v[156:159], v[92:95], v[204:207], v[156:159]\n\t" \
  "v_mfma_f32_16x16x32_bf16 v[144:147], v[96:99], v[208:211], v[144:147]\n\t" \
  "v_mfma_f32_16x16x32_bf16 v[148:151], v[100:103], v[208:211], v[148:151]\n\t" \
  "v_mfma_f32_16x16x32_bf16 v[152:155], v[96:99], v[212:215], v[152:155]\n\t" \
  "v_mfma_f32_16x16x32_bf16 v[156:159], v[100:103], v[212:215], v[156:159]\n\t" \
  "v_mfma_f32_16x16x32_bf16 v[144:147], v[104:107], v[216:219], v[144:147]\n\t" \
  "v_mfma_f32_16x16x32_bf16 v[148:151], v[108:111], v[216:219], v[148:151]\n\t" \
  "v_mfma_f32_16x16x32_bf16 v[152:155], v[104:107], v[220:223], v[152:155]\n\t" \
  "v_mfma_f32_16x16x32_bf16 v[156:159], v[108:111], v[220:223], v[156:159]\n\t" \
  "v_mfma_f32_16x16x32_bf16 v[144:147], v[112:115], v[224:227], v[144:147]\n\t" \
  "v_mfma_f32_16x16x32_bf16 v[148:151], v[116:119], v[224:227], v[148:151]\n\t" \
  "v_mfma_f32_16x16x32_bf16 v[152:155], v[112:115], v[228:231], v[152:155]\n\t" \
  "v_mfma_f32_16x16x32_bf16 v[156:159], v[116:119], v[228:231], v[156:159]\n\t" \
  "v_mfma_f32_16x16x32_bf16 v[144:147], v[120:123], v[232:235], v[144:147]\n\t" \
  "v_mfma_f32_16x16x32_bf16 v[148:151], v[124:127], v[232:235], v[148:151]\n\t" \
  "v_mfma_f32_16x16x32_bf16 v[152:155], v[120:123], v[236:239], v[152:155]\n\t" \
  "v_mfma_f32_16x16x32_bf16 v[156:159], v[124:127], v[236:239], v[156:159]\n\t" \
  "s_setprio 0\n\t" \
  "s_cmp_lt_u32 s86, 64\n\t" \
  "s_cbranch_scc1 .Lp0%=\n\t" \
  "s_barrier\n\t" \
  "s_nop 2\n\t" \
  "ds_write_b128 v252, v[152:155] offset:0\n\t" \
  "ds_write_b128 v252, v[156:159] offset:1024\n\t" \
  "s_waitcnt lgkmcnt(0)\n\t" \
  "s_barrier\n\t" \
  "ds_read_b128 v[160:163], v253 offset:0\n\t" \
  "ds_read_b128 v[164:167], v253 offset:1024\n\t" \
  "s_waitcnt lgkmcnt(0)\n\t" \
  "s_lshr_b32 s90, s87, 7\n\t" \
  "s_xor_b32 s90, s90, %[s0]\n\t" \
  "s_and_b32 s90, s90, 1\n\t" \
  "s_lshl1_add_u32 s90, s87, s90\n\t" \
  "s_lshl_b32 s90, s90, 13\n\t" \
  "v_add_f32 v160, v160, v144\n\t" \
  "v_add_f32 v161, v161, v145\n\t" \
  "v_add_f32 v162, v162, v146\n\t" \
  "v_add_f32 v163, v163, v147\n\t" \
  "v_add_f32 v164, v164, v148\n\t" \
  "v_add_f32 v165, v165, v149\n\t" \
  "v_add_f32 v166, v166, v150\n\t" \
  "v_add_f32 v167, v167, v151\n\t" \
  "v_cvt_pk_bf16_f32 v168, v160, v161\n\t" \
  "v_cvt_pk_bf16_f32 v169, v162, v163\n\t" \
  "v_cvt_pk_bf16_f32 v170, v164, v165\n\t" \
  "v_cvt_pk_bf16_f32 v171, v166, v167\n\t" \
  "buffer_store_dwordx4 v[168:171], v172, %[rt], s90 offen sc1\n\t" \
  "s_waitcnt vmcnt(0)\n\t" \
  "s_barrier\n\t" \
  "s_mov_b32 m0, s94\n\t" \
  "s_branch .Le%=\n\t" \
  ".Lk%=:\n\t" \
  "s_mov_b32 s94, m0\n\t" \
  "s_mov_b64 s[92:93], %[x0]\n\t" \
  "s_mov_b32 s86, %[g0]\n\t" \
  "s_add_u32 s87, s86, %[t0]\n\t" \
  "s_add_u32 m0, %[lw], 67584\n\t" \
  "s_nop 0\n\t" \
  "global_load_lds_dwordx4 v250, s[92:93] sc1\n\t" \
  "s_add_u32 m0, %[lw], 68608\n\t" \
  "s_nop 0\n\t" \
  "global_load_lds_dwordx4 v251, s[92:93] sc1\n\t" \
  "s_barrier\n\t" \
  "s_branch .Ls1%=\n\t" \
  ".Lp1%=:\n\t" \
  "s_waitcnt vmcnt(10)\n\t" \
  "s_barrier\n\t" \
  "s_setprio 2\n\t" \
  "v_mfma_f32_16x16x32_bf16 v[144:147], v[64:67], v[176:179], v[144:147]\n\t" \
  "v_mfma_f32_16x16x32_bf16 v[148:151], v[68:71], v[176:179], v[148:151]\n\t" \
  "v_mfma_f32_16x16x32_bf16 v[152:155], v[64:67], v[180:183], v[152:155]\n\t" \
  "v_mfma_f32_16x16x32_bf16 v[156:159], v[68:71], v[180:183], v[156:159]\n\t" \
  "v_mfma_f32_16x16x32_bf16 v[144:147], v[72:75], v[184:187], v[144:147]\n\t" \
  "v_mfma_f32_16x16x32_bf16 v[148:151], v[76:79], v[184:187], v[148:151]\n\t" \
  "v_mfma_f32_16x16x32_bf16 v[152:155], v[72:75], v[188:191], v[152:155]\n\t" \
  "v_mfma_f32_16x16x32_bf16 v[156:159], v[76:79], v[188:191], v[156:159]\n\t" \
  "v_mfma_f32_16x16x32_bf16 v[144:147], v[80:83], v[192:195], v[144:147]\n\t" \
  "v_mfma_f32_16x16x32_bf16 v[148:151], v[84:87], v[192:195], v[148:151]\n\t" \
  "v_mfma_f32_16x16x32_bf16 v[152:155], v[80:83], v[196:199], v[152:155]\n\t" \
  "v_mfma_f32_16x16x32_bf16 v[156:159], v[84:87], v[196:199], v[156:159]\n\t" \
  "v_mfma_f32_16x16x32_bf16 v[144:147], v[88:91], v[200:203], v[144:147]\n\t" \
  "v_mfma_f32_16x16x32_bf16 v[148:151], v[92:95], v[200:203], v[148:151]\n\t" \
  "v_mfma_f32_16x16x32_bf16 v[152:155], v[88:91], v[204:207], v[152:155]\n\t" \
  "v_mfma_f32_16x16x32_bf16 v[156:159], v[92:95], v[204:207], v[156:159]\n\t" \
  "v_mfma_f32_16x16x32_bf16 v[144:147], v[96:99], v[208:211], v[144:147]\n\t" \
  "v_mfma_f32_16x16x32_bf16 v[148:151], v[100:103], v[208:211], v[148:151]\n\t" \
  "v_mfma_f32_16x16x32_bf16 v[152:155], v[96:99], v[212:215], v[152:155]\n\t" \
  "v_mfma_f32_16x16x32_bf16 v[156:159], v[100:103], v[212:215], v[156:159]\n\t" \
  "v_mfma_f32_16x16x32_bf16 v[144:147], v[104:107], v[216:219], v[144:147]\n\t" \
  "v_mfma_f32_16x16x32_bf16 v[148:151], v[108:111], v[216:219], v[148:151]\n\t" \
  "v_mfma_f32_16x16x32_bf16 v[152:155], v[104:107], v[220:223], v[152:155]\n\t" \
  "v_mfma_f32_16x16x32_bf16 v[156:159], v[108:111], v[220:223], v[156:159]\n\t" \
  "v_mfma_f32_16x16x32_bf16 v[144:147], v[112:115], v[224:227], v[144:147]\n\t" \
  "v_mfma_f32_16x16x32_bf16 v[148:151], v[116:119], v[224:227], v[148:151]\n\t" \
  "v_mfma_f32_16x16x32_bf16 v[152:155], v[112:115], v[228:231], v[152:155]\n\t" \
  "v_mfma_f32_16x16x32_bf16 v[156:159], v[116:119], v[228:231], v[156:159]\n\t" \
  "v_mfma_f32_16x16x32_bf16 v[144:147], v[120:123], v[232:235], v[144:147]\n\t" \
  "v_mfma_f32_16x16x32_bf16 v[148:151], v[124:127], v[232:235], v[148:151]\n\t" \
  "v_mfma_f32_16x16x32_bf16 v[152:155], v[120:123], v[236:239], v[152:155]\n\t" \
  "v_mfma_f32_16x16x32_bf16 v[156:159], v[124:127], v[236:239], v[156:159]\n\t" \
  "s_setprio 0\n\t" \
  ".Ls1%=:\n\t" \
  "s_add_u32 s92, s92, 512\n\t" \
  "s_addc_u32 s93, s93, 0\n\t" \
  "ds_read_b128 v[176:179], v240 offset:0\n\t" \
  "ds_read_b128 v[180:183], v240 offset:16384\n\t" \
  "ds_read_b128 v[184:187], v241 offset:0\n\t" \
  "ds_read_b128 v[188:191], v241 offset:16384\n\t" \
  "s_add_u32 m0, %[lw], 98304\n\t" \
  "s_nop 0\n\t" \
  "global_load_lds_dwordx4 v248, s[92:93] sc1\n\t" \
  "ds_read_b128 v[192:195], v242 offset:0\n\t" \
  "ds_read_b128 v[196:199], v242 offset:16384\n\t" \
  "ds_read_b128 v[200:203], v243 offset:0\n\t" \
  "ds_read_b128 v[204:207], v243 offset:16384\n\t" \
  "s_add_u32 m0, %[lw], 99328\n\t" \
  "s_nop 0\n\t" \
  "global_load_lds_dwordx4 v249, s[92:93] sc1\n\t" \
  "ds_read_b128 v[208:211], v240 offset:256\n\t" \
  "ds_read_b128 v[212:215], v240 offset:16640\n\t" \
  "ds_read_b128 v[216:219], v241 offset:256\n\t" \
  "ds_read_b128 v[220:223], v241 offset:16640\n\t" \
  "s_add_u32 m0, %[lw], 100352\n\t" \
  "s_nop 0\n\t" \
  "global_load_lds_dwordx4 v250, s[92:93] sc1\n\t" \
  "ds_read_b128 v[224:227], v242 offset:256\n\t" \
  "ds_read_b128 v[228:231], v242 offset:16640\n\t" \
  "ds_read_b128 v[232:235], v243 offset:256\n\t" \
  "ds_read_b128 v[236:239], v243 offset:16640\n\t" \
  "s_add_u32 m0, %[lw], 101376\n\t" \
  "s_nop 0\n\t" \
  "global_load_lds_dwordx4 v251, s[92:93] sc1\n\t" \
  "ds_write_b128 v252, v[152:155] offset:0\n\t" \
  "ds_write_b128 v252, v[156:159] offset:1024\n\t" \
  "s_waitcnt lgkmcnt(0)\n\t" \
  "s_waitcnt vmcnt(4)\n\t" \
  "s_barrier\n\t" \
  "s_setprio 2\n\t" \
  "v_mfma_f32_16x16x32_bf16 v[128:131], v[0:3], v[176:179], 0\n\t" \
  "v_mfma_f32_16x16x32_bf16 v[132:135], v[4:7], v[176:179], 0\n\t" \
  "v_mfma_f32_16x16x32_bf16 v[136:139], v[0:3], v[180:183], 0\n\t" \
  "v_mfma_f32_16x16x32_bf16 v[140:143], v[4:7], v[180:183], 0\n\t" \
  "v_mfma_f32_16x16x32_bf16 v[128:131], v[8:11], v[184:187], v[128:131]\n\t" \
  "v_mfma_f32_16x16x32_bf16 v[132:135], v[12:15], v[184:187], v[132:135]\n\t" \
  "v_mfma_f32_16x16x32_bf16 v[136:139], v[8:11], v[188:191], v[136:139]\n\t" \
  "v_mfma_f32_16x16x32_bf16 v[140:143], v[12:15], v[188:191], v[140:143]\n\t" \
  "v_mfma_f32_16x16x32_bf16 v[128:131], v[16:19], v[192:195], v[128:131]\n\t" \
  "v_mfma_f32_16x16x32_bf16 v[132:135], v[20:23], v[192:195], v[132:135]\n\t" \
  "v_mfma_f32_16x16x32_bf16 v[136:139], v[16:19], v[196:199], v[136:139]\n\t" \
  "v_mfma_f32_16x16x32_bf16 v[140:143], v[20:23], v[196:199], v[140:143]\n\t" \
  "v_mfma_f32_16x16x32_bf16 v[128:131], v[24:27], v[200:203], v[128:131]\n\t" \
  "v_mfma_f32_16x16x32_bf16 v[132:135], v[28:31], v[200:203], v[132:135]\n\t" \
  "v_mfma_f32_16x16x32_bf16 v[136:139], v[24:27], v[204:207], v[136:139]\n\t" \
  "v_mfma_f32_16x16x32_bf16 v[140:143], v[28:31], v[204:207], v[140:143]\n\t" \
  "v_mfma_f32_16x16x32_bf16 v[128:131], v[32:35], v[208:211], v[128:131]\n\t" \
  "v_mfma_f32_16x16x32_bf16 v[132:135], v[36:39], v[208:211], v[132:135]\n\t" \
  "v_mfma_f32_16x16x32_bf16 v[136:139], v[32:35], v[212:215], v[136:139]\n\t" \
  "v_mfma_f32_16x16x32_bf16 v[140:143], v[36:39], v[212:215], v[140:143]\n\t" \
  "v_mfma_f32_16x16x32_bf16 v[128:131], v[40:43], v[216:219], v[128:131]\n\t" \
  "v_mfma_f32_16x16x32_bf16 v[132:135], v[44:47], v[216:219], v[132:135]\n\t" \
  "v_mfma_f32_16x16x32_bf16 v[136:139], v[40:43], v[220:223], v[136:139]\n\t" \
  "v_mfma_f32_16x16x32_bf16 v[140:143], v[44:47], v[220:223], v[140:143]\n\t" \
  "v_mfma_f32_16x16x32_bf16 v[128:131], v[48:51], v[224:227], v[128:131]\n\t" \
  "v_mfma_f32_16x16x32_bf16 v[132:135], v[52:55], v[224:227], v[132:135]\n\t" \
  "v_mfma_f32_16x16x32_bf16 v[136:139], v[48:51], v[228:231], v[136:139]\n\t" \
  "v_mfma_f32_16x16x32_bf16 v[140:143], v[52:55], v[228:231], v[140:143]\n\t" \
  "v_mfma_f32_16x16x32_bf16 v[128:131], v[56:59], v[232:235], v[128:131]\n\t" \
  "v_mfma_f32_16x16x32_bf16 v[132:135], v[60:63], v[232:235], v[132:135]\n\t" \
  "v_mfma_f32_16x16x32_bf16 v[136:139], v[56:59], v[236:239], v[136:139]\n\t" \
  "v_mfma_f32_16x16x32_bf16 v[140:143], v[60:63], v[236:239], v[140:143]\n\t" \
  "s_setprio 0\n\t" \
  "s_add_u32 s92, s92, 65024\n\t" \
  "s_addc_u32 s93, s93, 0\n\t" \
  "ds_read_b128 v[160:163], v253 offset:0\n\t" \
  "ds_read_b128 v[164:167], v253 offset:1024\n\t" \
  "ds_read_b128 v[176:179], v240 offset:32768\n\t" \
  "ds_read_b128 v[180:183], v240 offset:49152\n\t" \
  "ds_read_b128 v[184:187], v241 offset:32768\n\t" \
  "ds_read_b128 v[188:191], v241 offset:49152\n\t" \
  "s_add_u32 m0, %[lw], 0\n\t" \
  "s_nop 0\n\t" \
  "global_load_lds_dwordx4 v248, s[92:93] sc1\n\t" \
  "ds_read_b128 v[192:195], v242 offset:32768\n\t" \
  "ds_read_b128 v[196:199], v242 offset:49152\n\t" \
  "ds_read_b128 v[200:203], v243 offset:32768\n\t" \
  "ds_read_b128 v[204:207], v243 offset:49152\n\t" \
  "s_add_u32 m0, %[lw], 1024\n\t" \
  "s_nop 0\n\t" \
  "global_load_lds_dwordx4 v249, s[92:93] sc1\n\t" \
  "ds_read_b128 v[208:211], v240 offset:33024\n\t" \
  "ds_read_b128 v[212:215], v240 offset:49408\n\t" \
  "ds_read_b128 v[216:219], v241 offset:33024\n\t" \
  "ds_read_b128 v[220:223], v241 offset:49408\n\t" \
  "s_add_u32 m0, %[lw], 2048\n\t" \
  "s_nop 0\n\t" \
  "global_load_lds_dwordx4 v250, s[92:93] sc1\n\t" \
  "ds_read_b128 v[224:227], v242 offset:33024\n\t" \
  "ds_read_b128 v[228:231], v242 offset:49408\n\t" \
  "ds_read_b128 v[232:235], v243 offset:33024\n\t" \
  "ds_read_b128 v[236:239], v243 offset:49408\n\t" \
  "s_add_u32 m0, %[lw], 3072\n\t" \
  "s_nop 0\n\t" \
  "global_load_lds_dwordx4 v251, s[92:93] sc1\n\t" \
  "s_waitcnt lgkmcnt(15)\n\t" \
  "s_waitcnt lgkmcnt(12)\n\t" \
  "s_sub_i32 s91, s86, 2\n\t" \
  "v_mov_b32 v173, s91\n\t" \
  "buffer_store_dword v173, v174, %[rs], 0 offen sc1\n\t" \
  "s_lshr_b32 s90, s87, 7\n\t" \
  "s_xor_b32 s90, s90, %[s0]\n\t" \
  "s_and_b32 s90, s90, 1\n\t" \
  "s_lshl1_add_u32 s90, s87, s90\n\t" \
  "s_lshl_b32 s90, s90, 13\n\t" \
  "v_add_f32 v160, v160, v144\n\t" \
  "v_add_f32 v161, v161, v145\n\t" \
  "v_add_f32 v162, v162, v146\n\t" \
  "v_add_f32 v163, v163, v147\n\t" \
  "v_add_f32 v164, v164, v148\n\t" \
  "v_add_f32 v165, v165, v149\n\t" \
  "v_add_f32 v166, v166, v150\n\t" \
  "v_add_f32 v167, v167, v151\n\t" \
  "v_cvt_pk_bf16_f32 v168, v160, v161\n\t" \
  "v_cvt_pk_bf16_f32 v169, v162, v163\n\t" \
  "v_cvt_pk_bf16_f32 v170, v164, v165\n\t" \
  "v_cvt_pk_bf16_f32 v171, v166, v167\n\t" \
  "buffer_store_dwordx4 v[168:171], v172, %[rt], s90 offen sc1\n\t" \
  "s_add_u32 s86, s86, 1\n\t" \
  "s_add_u32 s87, s87, 1\n\t" \
  "s_waitcnt lgkmcnt(0)\n\t" \
  "s_waitcnt vmcnt(10)\n\t" \
  "s_barrier\n\t" \
  "s_setprio 2\n\t" \
  "v_mfma_f32_16x16x32_bf16 v[128:131], v[64:67], v[176:179], v[128:131]\n\t" \
  "v_mfma_f32_16x16x32_bf16 v[132:135], v[68:71], v[176:179], v[132:135]\n\t" \
  "v_mfma_f32_16x16x32_bf16 v[136:139], v[64:67], v[180:183], v[136:139]\n\t" \
  "v_mfma_f32_16x16x32_bf16 v[140:143], v[68:71], v[180:183], v[140:143]\n\t" \
  "v_mfma_f32_16x16x32_bf16 v[128:131], v[72:75], v[184:187], v[128:131]\n\t" \
  "v_mfma_f32_16x16x32_bf16 v[132:135], v[76:79], v[184:187], v[132:135]\n\t" \
  "v_mfma_f32_16x16x32_bf16 v[136:139], v[72:75], v[188:191], v[136:139]\n\t" \
  "v_mfma_f32_16x16x32_bf16 v[140:143], v[76:79], v[188:191], v[140:143]\n\t" \
  "v_mfma_f32_16x16x32_bf16 v[128:131], v[80:83], v[192:195], v[128:131]\n\t" \
  "v_mfma_f32_16x16x32_bf16 v[132:135], v[84:87], v[192:195], v[132:135]\n\t" \
  "v_mfma_f32_16x16x32_bf16 v[136:139], v[80:83], v[196:199], v[136:139]\n\t" \
  "v_mfma_f32_16x16x32_bf16 v[140:143], v[84:87], v[196:199], v[140:143]\n\t" \
  "v_mfma_f32_16x16x32_bf16 v[128:131], v[88:91], v[200:203], v[128:131]\n\t" \
  "v_mfma_f32_16x16x32_bf16 v[132:135], v[92:95], v[200:203], v[132:135]\n\t" \
  "v_mfma_f32_16x16x32_bf16 v[136:139], v[88:91], v[204:207], v[136:139]\n\t" \
  "v_mfma_f32_16x16x32_bf16 v[140:143], v[92:95], v[204:207], v[140:143]\n\t" \
  "v_mfma_f32_16x16x32_bf16 v[128:131], v[96:99], v[208:211], v[128:131]\n\t" \
  "v_mfma_f32_16x16x32_bf16 v[132:135], v[100:103], v[208:211], v[132:135]\n\t" \
  "v_mfma_f32_16x16x32_bf16 v[136:139], v[96:99], v[212:215], v[136:139]\n\t" \
  "v_mfma_f32_16x16x32_bf16 v[140:143], v[100:103], v[212:215], v[140:143]\n\t" \
  "v_mfma_f32_16x16x32_bf16 v[128:131], v[104:107], v[216:219], v[128:131]\n\t" \
  "v_mfma_f32_16x16x32_bf16 v[132:135], v[108:111], v[216:219], v[132:135]\n\t" \
  "v_mfma_f32_16x16x32_bf16 v[136:139], v[104:107], v[220:223], v[136:139]\n\t" \
  "v_mfma_f32_16x16x32_bf16 v[140:143], v[108:111], v[220:223], v[140:143]\n\t" \
  "v_mfma_f32_16x16x32_bf16 v[128:131], v[112:115], v[224:227], v[128:131]\n\t" \
  "v_mfma_f32_16x16x32_bf16 v[132:135], v[116:119], v[224:227], v[132:135]\n\t" \
  "v_mfma_f32_16x16x32_bf16 v[136:139], v[112:115], v[228:231], v[136:139]\n\t" \
  "v_mfma_f32_16x16x32_bf16 v[140:143], v[116:119], v[228:231], v[140:143]\n\t" \
  "v_mfma_f32_16x16x32_bf16 v[128:131], v[120:123], v[232:235], v[128:131]\n\t" \
  "v_mfma_f32_16x16x32_bf16 v[132:135], v[124:127], v[232:235], v[132:135]\n\t" \
  "v_mfma_f32_16x16x32_bf16 v[136:139], v[120:123], v[236:239], v[136:139]\n\t" \
  "v_mfma_f32_16x16x32_bf16 v[140:143], v[124:127], v[236:239], v[140:143]\n\t" \
  "s_setprio 0\n\t" \
  "s_add_u32 s92, s92, 512\n\t" \
  "s_addc_u32 s93, s93, 0\n\t" \
  "ds_read_b128 v[176:179], v244 offset:0\n\t" \
  "ds_read_b128 v[180:183], v244 offset:16384\n\t" \
  "ds_read_b128 v[184:187], v245 offset:0\n\t" \
  "ds_read_b128 v[188:191], v245 offset:16384\n\t" \
  "s_add_u32 m0, %[lw], 32768\n\t" \
  "s_nop 0\n\t" \
  "global_load_lds_dwordx4 v248, s[92:93] sc1\n\t" \
  "ds_read_b128 v[192:195], v246 offset:0\n\t" \
  "ds_read_b128 v[196:199], v246 offset:16384\n\t" \
  "ds_read_b128 v[200:203], v247 offset:0\n\t" \
  "ds_read_b128 v[204:207], v247 offset:16384\n\t" \
  "s_add_u32 m0, %[lw], 33792\n\t" \
  "s_nop 0\n\t" \
  "global_load_lds_dwordx4 v249, s[92:93] sc1\n\t" \
  "ds_read_b128 v[208:211], v244 offset:256\n\t" \
  "ds_read_b128 v[212:215], v244 offset:16640\n\t" \
  "ds_read_b128 v[216:219], v245 offset:256\n\t" \
  "ds_read_b128 v[220:223], v245 offset:16640\n\t" \
  "s_add_u32 m0, %[lw], 34816\n\t" \
  "s_nop 0\n\t" \
  "global_load_lds_dwordx4 v250, s[92:93] sc1\n\t" \
  "ds_read_b128 v[224:227], v246 offset:256\n\t" \
  "ds_read_b128 v[228:231], v246 offset:16640\n\t" \
  "ds_read_b128 v[232:235], v247 offset:256\n\t" \
  "ds_read_b128 v[236:239], v247 offset:16640\n\t" \
  "s_add_u32 m0, %[lw], 35840\n\t" \
  "s_nop 0\n\t" \
  "global_load_lds_dwordx4 v251, s[92:93] sc1\n\t" \
  "ds_write_b128 v252, v[136:139] offset:0\n\t" \
  "ds_write_b128 v252, v[140:143] offset:1024\n\t" \
  "s_waitcnt lgkmcnt(0)\n\t" \
  "s_waitcnt vmcnt(4)\n\t" \
  "s_barrier\n\t" \
  "s_setprio 2\n\t" \
  "v_mfma_f32_16x16x32_bf16 v[144:147], v[0:3], v[176:179], 0\n\t" \
  "v_mfma_f32_16x16x32_bf16 v[148:151], v[4:7], v[176:179], 0\n\t" \
  "v_mfma_f32_16x16x32_bf16 v[152:155], v[0:3], v[180:183], 0\n\t" \
  "v_mfma_f32_16x16x32_bf16 v[156:159], v[4:7], v[180:183], 0\n\t" \
  "v_mfma_f32_16x16x32_bf16 v[144:147], v[8:11], v[184:187], v[144:147]\n\t" \
  "v_mfma_f32_16x16x32_bf16 v[148:151], v[12:15], v[184:187], v[148:151]\n\t" \
  "v_mfma_f32_16x16x32_bf16 v[152:155], v[8:11], v[188:191], v[152:155]\n\t" \
  "v_mfma_f32_16x16x32_bf16 v[156:159], v[12:15], v[188:191], v[156:159]\n\t" \
  "v_mfma_f32_16x16x32_bf16 v[144:147], v[16:19], v[192:195], v[144:147]\n\t" \
  "v_mfma_f32_16x16x32_bf16 v[148:151], v[20:23], v[192:195], v[148:151]\n\t" \
  "v_mfma_f32_16x16x32_bf16 v[152:155], v[16:19], v[196:199], v[152:155]\n\t" \
  "v_mfma_f32_16x16x32_bf16 v[156:159], v[20:23], v[196:199], v[156:159]\n\t" \
  "v_mfma_f32_16x16x32_bf16 v[144:147], v[24:27], v[200:203], v[144:147]\n\t" \
  "v_mfma_f32_16x16x32_bf16 v[148:151], v[28:31], v[200:203], v[148:151]\n\t" \
  "v_mfma_f32_16x16x32_bf16 v[152:155], v[24:27], v[204:207], v[152:155]\n\t" \
  "v_mfma_f32_16x16x32_bf16 v[156:159], v[28:31], v[204:207], v[156:159]\n\t" \
  "v_mfma_f32_16x16x32_bf16 v[144:147], v[32:35], v[208:211], v[144:147]\n\t" \
  "v_mfma_f32_16x16x32_bf16 v[148:151], v[36:39], v[208:211], v[148:151]\n\t" \
  "v_mfma_f32_16x16x32_bf16 v[152:155], v[32:35], v[212:215], v[152:155]\n\t" \
  "v_mfma_f32_16x16x32_bf16 v[156:159], v[36:39], v[212:215], v[156:159]\n\t" \
  "v_mfma_f32_16x16x32_bf16 v[144:147], v[40:43], v[216:219], v[144:147]\n\t" \
  "v_mfma_f32_16x16x32_bf16 v[148:151], v[44:47], v[216:219], v[148:151]\n\t" \
  "v_mfma_f32_16x16x32_bf16 v[152:155], v[40:43], v[220:223], v[152:155]\n\t" \
  "v_mfma_f32_16x16x32_bf16 v[156:159], v[44:47], v[220:223], v[156:159]\n\t" \
  "v_mfma_f32_16x16x32_bf16 v[144:147], v[48:51], v[224:227], v[144:147]\n\t" \
  "v_mfma_f32_16x16x32_bf16 v[148:151], v[52:55], v[224:227], v[148:151]\n\t" \
  "v_mfma_f32_16x16x32_bf16 v[152:155], v[48:51], v[228:231], v[152:155]\n\t" \
  "v_mfma_f32_16x16x32_bf16 v[156:159], v[52:55], v[228:231], v[156:159]\n\t" \
  "v_mfma_f32_16x16x32_bf16 v[144:147], v[56:59], v[232:235], v[144:147]\n\t" \
  "v_mfma_f32_16x16x32_bf16 v[148:151], v[60:63], v[232:235], v[148:151]\n\t" \
  "v_mfma_f32_16x16x32_bf16 v[152:155], v[56:59], v[236:239], v[152:155]\n\t" \
  "v_mfma_f32_16x16x32_bf16 v[156:159], v[60:63], v[236:239], v[156:159]\n\t" \
  "s_setprio 0\n\t" \
  "s_add_u32 s92, s92, 65024\n\t" \
  "s_addc_u32 s93, s93, 0\n\t" \
  "ds_read_b128 v[160:163], v253 offset:0\n\t" \
  "ds_read_b128 v[164:167], v253 offset:1024\n\t" \
  "ds_read_b128 v[176:179], v244 offset:32768\n\t" \
  "ds_read_b128 v[180:183], v244 offset:49152\n\t" \
  "ds_read_b128 v[184:187], v245 offset:32768\n\t" \
  "ds_read_b128 v[188:191], v245 offset:49152\n\t" \
  "s_add_u32 m0, %[lw], 65536\n\t" \
  "s_nop 0\n\t" \
  "global_load_lds_dwordx4 v248, s[92:93] sc1\n\t" \
  "ds_read_b128 v[192:195], v246 offset:32768\n\t" \
  "ds_read_b128 v[196:199], v246 offset:49152\n\t" \
  "ds_read_b128 v[200:203], v247 offset:32768\n\t" \
  "ds_read_b128 v[204:207], v247 offset:49152\n\t" \
  "s_add_u32 m0, %[lw], 66560\n\t" \
  "s_nop 0\n\t" \
  "global_load_lds_dwordx4 v249, s[92:93] sc1\n\t" \
  "ds_read_b128 v[208:211], v244 offset:33024\n\t" \
  "ds_read_b128 v[212:215], v244 offset:49408\n\t" \
  "ds_read_b128 v[216:219], v245 offset:33024\n\t" \
  "ds_read_b128 v[220:223], v245 offset:49408\n\t" \
  "s_add_u32 m0, %[lw], 67584\n\t" \
  "s_nop 0\n\t" \
  "global_load_lds_dwordx4 v250, s[92:93] sc1\n\t" \
  "ds_read_b128 v[224:227], v246 offset:33024\n\t" \
  "ds_read_b128 v[228:231], v246 offset:49408\n\t" \
  "ds_read_b128 v[232:235], v247 offset:33024\n\t" \
  "ds_read_b128 v[236:239], v247 offset:49408\n\t" \
  "s_add_u32 m0, %[lw], 68608\n\t" \
  "s_nop 0\n\t" \
  "global_load_lds_dwordx4 v251, s[92:93] sc1\n\t" \
  "s_waitcnt lgkmcnt(15)\n\t" \
  "s_waitcnt lgkmcnt(12)\n\t" \
  "s_sub_i32 s91, s86, 2\n\t" \
  "v_mov_b32 v173, s91\n\t" \
  "buffer_store_dword v173, v174, %[rs], 0 offen sc1\n\t" \
  "s_lshr_b32 s90, s87, 7\n\t" \
  "s_xor_b32 s90, s90, %[s0]\n\t" \
  "s_and_b32 s90, s90, 1\n\t" \
  "s_lshl1_add_u32 s90, s87, s90\n\t" \
  "s_lshl_b32 s90, s90, 13\n\t" \
  "v_add_f32 v160, v160, v128\n\t" \
  "v_add_f32 v161, v161, v129\n\t" \
  "v_add_f32 v162, v162, v130\n\t" \
  "v_add_f32 v163, v163, v131\n\t" \
  "v_add_f32 v164, v164, v132\n\t" \
  "v_add_f32 v165, v165, v133\n\t" \
  "v_add_f32 v166, v166, v134\n\t" \
  "v_add_f32 v167, v167, v135\n\t" \
  "v_cvt_pk_bf16_f32 v168, v160, v161\n\t" \
  "v_cvt_pk_bf16_f32 v169, v162, v163\n\t" \
  "v_cvt_pk_bf16_f32 v170, v164, v165\n\t" \
  "v_cvt_pk_bf16_f32 v171, v166, v167\n\t" \
  "buffer_store_dwordx4 v[168:171], v172, %[rt], s90 offen sc1\n\t" \
  "s_add_u32 s86, s86, 1\n\t" \
  "s_add_u32 s87, s87, 1\n\t" \
  "s_waitcnt lgkmcnt(0)\n\t" \
  "s_cmp_lt_u32 s86, 64\n\t" \
  "s_cbranch_scc1 .Lp1%=\n\t" \
  "s_barrier\n\t" \
  "s_setprio 2\n\t" \
  "v_mfma_f32_16x16x32_bf16 v[144:147], v[64:67], v[176:179], v[144:147]\n\t" \
  "v_mfma_f32_16x16x32_bf16 v[148:151], v[68:71], v[176:179], v[148:151]\n\t" \
  "v_mfma_f32_16x16x32_bf16 v[152:155], v[64:67], v[180:183], v[152:155]\n\t" \
  "v_mfma_f32_16x16x32_bf16 v[156:159], v[68:71], v[180:183], v[156:159]\n\t" \
  "v_mfma_f32_16x16x32_bf16 v[144:147], v[72:75], v[184:187], v[144:147]\n\t" \
  "v_mfma_f32_16x16x32_bf16 v[148:151], v[76:79], v[184:187], v[148:151]\n\t" \
  "v_mfma_f32_16x16x32_bf16 v[152:155], v[72:75], v[188:191], v[152:155]\n\t" \
  "v_mfma_f32_16x16x32_bf16 v[156:159], v[76:79], v[188:191], v[156:159]\n\t" \
  "v_mfma_f32_16x16x32_bf16 v[144:147], v[80:83], v[192:195], v[144:147]\n\t" \
  "v_mfma_f32_16x16x32_bf16 v[148:151], v[84:87], v[192:195], v[148:151]\n\t" \
  "v_mfma_f32_16x16x32_bf16 v[152:155], v[80:83], v[196:199], v[152:155]\n\t" \
  "v_mfma_f32_16x16x32_bf16 v[156:159], v[84:87], v[196:199], v[156:159]\n\t" \
  "v_mfma_f32_16x16x32_bf16 v[144:147], v[88:91], v[200:203], v[144:147]\n\t" \
  "v_mfma_f32_16x16x32_bf16 v[148:151], v[92:95], v[200:203], v[148:151]\n\t" \
  "v_mfma_f32_16x16x32_bf16 v[152:155], v[88:91], v[204:207], v[152:155]\n\t" \
  "v_mfma_f32_16x16x32_bf16 v[156:159], v[92:95], v[204:207], v[156:159]\n\t" \
  "v_mfma_f32_16x16x32_bf16 v[144:147], v[96:99], v[208:211], v[144:147]\n\t" \
  "v_mfma_f32_16x16x32_bf16 v[148:151], v[100:103], v[208:211], v[148:151]\n\t" \
  "v_mfma_f32_16x16x32_bf16 v[152:155], v[96:99], v[212:215], v[152:155]\n\t" \
  "v_mfma_f32_16x16x32_bf16 v[156:159], v[100:103], v[212:215], v[156:159]\n\t" \
  "v_mfma_f32_16x16x32_bf16 v[144:147], v[104:107], v[216:219], v[144:147]\n\t" \
  "v_mfma_f32_16x16x32_bf16 v[148:151], v[108:111], v[216:219], v[148:151]\n\t" \
  "v_mfma_f32_16x16x32_bf16 v[152:155], v[104:107], v[220:223], v[152:155]\n\t" \
  "v_mfma_f32_16x16x32_bf16 v[156:159], v[108:111], v[220:223], v[156:159]\n\t" \
  "v_mfma_f32_16x16x32_bf16 v[144:147], v[112:115], v[224:227], v[144:147]\n\t" \
  "v_mfma_f32_16x16x32_bf16 v[148:151], v[116:119], v[224:227], v[148:151]\n\t" \
  "v_mfma_f32_16x16x32_bf16 v[152:155], v[112:115], v[228:231], v[152:155]\n\t" \
  "v_mfma_f32_16x16x32_bf16 v[156:159], v[116:119], v[228:231], v[156:159]\n\t" \
  "v_mfma_f32_16x16x32_bf16 v[144:147], v[120:123], v[232:235], v[144:147]\n\t" \
  "v_mfma_f32_16x16x32_bf16 v[148:151], v[124:127], v[232:235], v[148:151]\n\t" \
  "v_mfma_f32_16x16x32_bf16 v[152:155], v[120:123], v[236:239], v[152:155]\n\t" \
  "v_mfma_f32_16x16x32_bf16 v[156:159], v[124:127], v[236:239], v[156:159]\n\t" \
  "s_setprio 0\n\t" \
  "s_nop 5\n\t" \
  "ds_write_b128 v252, v[152:155] offset:0\n\t" \
  "ds_write_b128 v252, v[156:159] offset:1024\n\t" \
  "s_waitcnt lgkmcnt(0)\n\t" \
  "s_barrier\n\t" \
  "ds_read_b128 v[160:163], v253 offset:0\n\t" \
  "ds_read_b128 v[164:167], v253 offset:1024\n\t" \
  "s_waitcnt lgkmcnt(0)\n\t" \
  "s_lshr_b32 s90, s87, 7\n\t" \
  "s_xor_b32 s90, s90, %[s0]\n\t" \
  "s_and_b32 s90, s90, 1\n\t" \
  "s_lshl1_add_u32 s90, s87, s90\n\t" \
  "s_lshl_b32 s90, s90, 13\n\t" \
  "v_add_f32 v160, v160, v144\n\t" \
  "v_add_f32 v161, v161, v145\n\t" \
  "v_add_f32 v162, v162, v146\n\t" \
  "v_add_f32 v163, v163, v147\n\t" \
  "v_add_f32 v164, v164, v148\n\t" \
  "v_add_f32 v165, v165, v149\n\t" \
  "v_add_f32 v166, v166, v150\n\t" \
  "v_add_f32 v167, v167, v151\n\t" \
  "v_cvt_pk_bf16_f32 v168, v160, v161\n\t" \
  "v_cvt_pk_bf16_f32 v169, v162, v163\n\t" \
  "v_cvt_pk_bf16_f32 v170, v164, v165\n\t" \
  "v_cvt_pk_bf16_f32 v171, v166, v167\n\t" \
  "buffer_store_dwordx4 v[168:171], v172, %[rt], s90 offen sc1\n\t" \
  "s_waitcnt vmcnt(0)\n\t" \
  "s_barrier\n\t" \
  "s_sub_i32 s91, s86, 1\n\t" \
  "v_mov_b32 v173, s91\n\t" \
  "buffer_store_dword v173, v174, %[rs], 0 offen sc1\n\t" \
  "s_mov_b32 m0, s94\n\t" \
  ".Le%=:\n\t"
#define PP_ASM_1 \
  "s_cmp_lg_u32 %[kh], 0\n\t" \
  "s_cbranch_scc1 .Lk%=\n\t" \
  "s_mov_b32 s94, m0\n\t" \
  "s_mov_b64 s[92:93], %[x0]\n\t" \
  "s_mov_b32 s86, %[g0]\n\t" \
  "s_add_u32 s87, s86, %[t0]\n\t" \
  "s_add_u32 m0, %[lw], 67584\n\t" \
  "s_nop 0\n\t" \
  "global_load_lds_dwordx4 v250, s[92:93] sc1\n\t" \
  "s_add_u32 m0, %[lw], 68608\n\t" \
  "s_nop 0\n\t" \
  "global_load_lds_dwordx4 v251, s[92:93] sc1\n\t" \
  "s_barrier\n\t" \
  "s_branch .Ls0%=\n\t" \
  ".Lp0%=:\n\t" \
  "s_waitcnt vmcnt(9)\n\t" \
  "s_barrier\n\t" \
  ".Ls0%=:\n\t" \
  "s_add_u32 s92, s92, 512\n\t" \
  "s_addc_u32 s93, s93, 0\n\t" \
  "ds_read_b128 v[176:179], v240 offset:0\n\t" \
  "ds_read_b128 v[180:183], v240 offset:16384\n\t" \
  "ds_read_b128 v[184:187], v241 offset:0\n\t" \
  "ds_read_b128 v[188:191], v241 offset:16384\n\t" \
  "s_add_u32 m0, %[lw], 98304\n\t" \
  "s_nop 0\n\t" \
  "global_load_lds_dwordx4 v248, s[92:93] sc1\n\t" \
  "ds_read_b128 v[192:195], v242 offset:0\n\t" \
  "ds_read_b128 v[196:199], v242 offset:16384\n\t" \
  "ds_read_b128 v[200:203], v243 offset:0\n\t" \
  "ds_read_b128 v[204:207], v243 offset:16384\n\t" \
  "s_add_u32 m0, %[lw], 99328\n\t" \
  "s_nop 0\n\t" \
  "global_load_lds_dwordx4 v249, s[92:93] sc1\n\t" \
  "ds_read_b128 v[208:211], v240 offset:256\n\t" \
  "ds_read_b128 v[212:215], v240 offset:16640\n\t" \
  "ds_read_b128 v[216:219], v241 offset:256\n\t" \
  "ds_read_b128 v[220:223], v241 offset:16640\n\t" \
  "s_add_u32 m0, %[lw], 100352\n\t" \
  "s_nop 0\n\t" \
  "global_load_lds_dwordx4 v250, s[92:93] sc1\n\t" \
  "ds_read_b128 v[224:227], v242 offset:256\n\t" \
  "ds_read_b128 v[228:231], v242 offset:16640\n\t" \
  "ds_read_b128 v[232:235], v243 offset:256\n\t" \
  "ds_read_b128 v[236:239], v243 offset:16640\n\t" \
  "s_add_u32 m0, %[lw], 101376\n\t" \
  "s_nop 0\n\t" \
  "global_load_lds_dwordx4 v251, s[92:93] sc1\n\t" \
  "ds_write_b128 v252, v[152:155] offset:0\n\t" \
  "ds_write_b128 v252, v[156:159] offset:1024\n\t" \
  "s_waitcnt lgkmcnt(0)\n\t" \
  "s_setprio 1\n\t" \
  "v_mfma_f32_16x16x32_bf16 v[128:131], v[176:179], v[0:3], 0\n\t" \
  "v_mfma_f32_16x16x32_bf16 v[136:139], v[176:179], v[4:7], 0\n\t" \
  "v_mfma_f32_16x16x32_bf16 v[132:135], v[180:183], v[0:3], 0\n\t" \
  "v_mfma_f32_16x16x32_bf16 v[140:143], v[180:183], v[4:7], 0\n\t" \
  "v_mfma_f32_16x16x32_bf16 v[128:131], v[184:187], v[8:11], v[128:131]\n\t" \
  "v_mfma_f32_16x16x32_bf16 v[136:139], v[184:187], v[12:15], v[136:139]\n\t" \
  "v_mfma_f32_16x16x32_bf16 v[132:135], v[188:191], v[8:11], v[132:135]\n\t" \
  "v_mfma_f32_16x16x32_bf16 v[140:143], v[188:191], v[12:15], v[140:143]\n\t" \
  "v_mfma_f32_16x16x32_bf16 v[128:131], v[192:195], v[16:19], v[128:131]\n\t" \
  "v_mfma_f32_16x16x32_bf16 v[136:139], v[192:195], v[20:23], v[136:139]\n\t" \
  "v_mfma_f32_16x16x32_bf16 v[132:135], v[196:199], v[16:19], v[132:135]\n\t" \
  "v_mfma_f32_16x16x32_bf16 v[140:143], v[196:199], v[20:23], v[140:143]\n\t" \
  "v_mfma_f32_16x16x32_bf16 v[128:131], v[200:203], v[24:27], v[128:131]\n\t" \
  "v_mfma_f32_16x16x32_bf16 v[136:139], v[200:203], v[28:31], v[136:139]\n\t" \
  "v_mfma_f32_16x16x32_bf16 v[132:135], v[204:207], v[24:27], v[132:135]\n\t" \
  "v_mfma_f32_16x16x32_bf16 v[140:143], v[204:207], v[28:31], v[140:143]\n\t" \
  "v_mfma_f32_16x16x32_bf16 v[128:131], v[208:211], v[32:35], v[128:131]\n\t" \
  "v_mfma_f32_16x16x32_bf16 v[136:139], v[208:211], v[36:39], v[136:139]\n\t" \
  "v_mfma_f32_16x16x32_bf16 v[132:135], v[212:215], v[32:35], v[132:135]\n\t" \
  "v_mfma_f32_16x16x32_bf16 v[140:143], v[212:215], v[36:39], v[140:143]\n\t" \
  "v_mfma_f32_16x16x32_bf16 v[128:131], v[216:219], v[40:43], v[128:131]\n\t" \
  "v_mfma_f32_16x16x32_bf16 v[136:139], v[216:219], v[44:47], v[136:139]\n\t" \
  "v_mfma_f32_16x16x32_bf16 v[132:135], v[220:223], v[40:43], v[132:135]\n\t" \
  "v_mfma_f32_16x16x32_bf16 v[140:143], v[220:223], v[44:47], v[140:143]\n\t" \
  "v_mfma_f32_16x16x32_bf16 v[128:131], v[224:227], v[48:51], v[128:131]\n\t" \
  "v_mfma_f32_16x16x32_bf16 v[136:139], v[224:227], v[52:55], v[136:139]\n\t" \
  "v_mfma_f32_16x16x32_bf16 v[132:135], v[228:231], v[48:51], v[132:135]\n\t" \
  "v_mfma_f32_16x16x32_bf16 v[140:143], v[228:231], v[52:55], v[140:143]\n\t" \
  "v_mfma_f32_16x16x32_bf16 v[128:131], v[232:235], v[56:59], v[128:131]\n\t" \
  "v_mfma_f32_16x16x32_bf16 v[136:139], v[232:235], v[60:63], v[136:139]\n\t" \
  "v_mfma_f32_16x16x32_bf16 v[132:135], v[236:239], v[56:59], v[132:135]\n\t" \
  "v_mfma_f32_16x16x32_bf16 v[140:143], v[236:239], v[60:63], v[140:143]\n\t" \
  "s_setprio 0\n\t" \
  "s_waitcnt vmcnt(4)\n\t" \
  "s_barrier\n\t" \
  "s_add_u32 s92, s92, 65024\n\t" \
  "s_addc_u32 s93, s93, 0\n\t" \
  "ds_read_b128 v[160:163], v253 offset:0\n\t" \
  "ds_read_b128 v[164:167], v253 offset:1024\n\t" \
  "ds_read_b128 v[176:179], v240 offset:32768\n\t" \
  "ds_read_b128 v[180:183], v240 offset:49152\n\t" \
  "ds_read_b128 v[184:187], v241 offset:32768\n\t" \
  "ds_read_b128 v[188:191], v241 offset:49152\n\t" \
  "s_add_u32 m0, %[lw], 0\n\t" \
  "s_nop 0\n\t" \
  "global_load_lds_dwordx4 v248, s[92:93] sc1\n\t" \
  "ds_read_b128 v[192:195], v242 offset:32768\n\t" \
  "ds_read_b128 v[196:199], v242 offset:49152\n\t" \
  "ds_read_b128 v[200:203], v243 offset:32768\n\t" \
  "ds_read_b128 v[204:207], v243 offset:49152\n\t" \
  "s_add_u32 m0, %[lw], 1024\n\t" \
  "s_nop 0\n\t" \
  "global_load_lds_dwordx4 v249, s[92:93] sc1\n\t" \
  "ds_read_b128 v[208:211], v240 offset:33024\n\t" \
  "ds_read_b128 v[212:215], v240 offset:49408\n\t" \
  "ds_read_b128 v[216:219], v241 offset:33024\n\t" \
  "ds_read_b128 v[220:223], v241 offset:49408\n\t" \
  "s_add_u32 m0, %[lw], 2048\n\t" \
  "s_nop 0\n\t" \
  "global_load_lds_dwordx4 v250, s[92:93] sc1\n\t" \
  "ds_read_b128 v[224:227], v242 offset:33024\n\t" \
  "ds_read_b128 v[228:231], v242 offset:49408\n\t" \
  "ds_read_b128 v[232:235], v243 offset:33024\n\t" \
  "ds_read_b128 v[236:239], v243 offset:49408\n\t" \
  "s_add_u32 m0, %[lw], 3072\n\t" \
  "s_nop 0\n\t" \
  "global_load_lds_dwordx4 v251, s[92:93] sc1\n\t" \
  "s_waitcnt lgkmcnt(15)\n\t" \
  "s_waitcnt lgkmcnt(12)\n\t" \
  "s_lshr_b32 s90, s87, 7\n\t" \
  "s_xor_b32 s90, s90, %[s0]\n\t" \
  "s_and_b32 s90, s90, 1\n\t" \
  "s_lshl1_add_u32 s90, s87, s90\n\t" \
  "s_lshl_b32 s90, s90, 13\n\t" \
  "v_add_f32 v160, v160, v144\n\t" \
  "v_add_f32 v161, v161, v145\n\t" \
  "v_add_f32 v162, v162, v146\n\t" \
  "v_add_f32 v163, v163, v147\n\t" \
  "v_add_f32 v164, v164, v148\n\t" \
  "v_add_f32 v165, v165, v149\n\t" \
  "v_add_f32 v166, v166, v150\n\t" \
  "v_add_f32 v167, v167, v151\n\t" \
  "v_cvt_pk_bf16_f32 v168, v160, v161\n\t" \
  "v_cvt_pk_bf16_f32 v169, v162, v163\n\t" \
  "v_cvt_pk_bf16_f32 v170, v164, v165\n\t" \
  "v_cvt_pk_bf16_f32 v171, v166, v167\n\t" \
  "buffer_store_dwordx4 v[168:171], v172, %[rt], s90 offen sc1\n\t" \
  "s_add_u32 s86, s86, 1\n\t" \
  "s_add_u32 s87, s87, 1\n\t" \
  "s_waitcnt lgkmcnt(0)\n\t" \
  "s_setprio 1\n\t" \
  "v_mfma_f32_16x16x32_bf16 v[128:131], v[176:179], v[64:67], v[128:131]\n\t" \
  "v_mfma_f32_16x16x32_bf16 v[136:139], v[176:179], v[68:71], v[136:139]\n\t" \
  "v_mfma_f32_16x16x32_bf16 v[132:135], v[180:183], v[64:67], v[132:135]\n\t" \
  "v_mfma_f32_16x16x32_bf16 v[140:143], v[180:183], v[68:71], v[140:143]\n\t" \
  "v_mfma_f32_16x16x32_bf16 v[128:131], v[184:187], v[72:75], v[128:131]\n\t" \
  "v_mfma_f32_16x16x32_bf16 v[136:139], v[184:187], v[76:79], v[136:139]\n\t" \
  "v_mfma_f32_16x16x32_bf16 v[132:135], v[188:191], v[72:75], v[132:135]\n\t" \
  "v_mfma_f32_16x16x32_bf16 v[140:143], v[188:191], v[76:79], v[140:143]\n\t" \
  "v_mfma_f32_16x16x32_bf16 v[128:131], v[192:195], v[80:83], v[128:131]\n\t" \
  "v_mfma_f32_16x16x32_bf16 v[136:139], v[192:195], v[84:87], v[136:139]\n\t" \
  "v_mfma_f32_16x16x32_bf16 v[132:135], v[196:199], v[80:83], v[132:135]\n\t" \
  "v_mfma_f32_16x16x32_bf16 v[140:143], v[196:199], v[84:87], v[140:143]\n\t" \
  "v_mfma_f32_16x16x32_bf16 v[128:131], v[200:203], v[88:91], v[128:131]\n\t" \
  "v_mfma_f32_16x16x32_bf16 v[136:139], v[200:203], v[92:95], v[136:139]\n\t" \
  "v_mfma_f32_16x16x32_bf16 v[132:135], v[204:207], v[88:91], v[132:135]\n\t" \
  "v_mfma_f32_16x16x32_bf16 v[140:143], v[204:207], v[92:95], v[140:143]\n\t" \
  "v_mfma_f32_16x16x32_bf16 v[128:131], v[208:211], v[96:99], v[128:131]\n\t" \
  "v_mfma_f32_16x16x32_bf16 v[136:139], v[208:211], v[100:103], v[136:139]\n\t" \
  "v_mfma_f32_16x16x32_bf16 v[132:135], v[212:215], v[96:99], v[132:135]\n\t" \
  "v_mfma_f32_16x16x32_bf16 v[140:143], v[212:215], v[100:103], v[140:143]\n\t" \
  "v_mfma_f32_16x16x32_bf16 v[128:131], v[216:219], v[104:107], v[128:131]\n\t" \
  "v_mfma_f32_16x16x32_bf16 v[136:139], v[216:219], v[108:111], v[136:139]\n\t" \
  "v_mfma_f32_16x16x32_bf16 v[132:135], v[220:223], v[104:107], v[132:135]\n\t" \
  "v_mfma_f32_16x16x32_bf16 v[140:143], v[220:223], v[108:111], v[140:143]\n\t" \
  "v_mfma_f32_16x16x32_bf16 v[128:131], v[224:227], v[112:115], v[128:131]\n\t" \
  "v_mfma_f32_16x16x32_bf16 v[136:139], v[224:227], v[116:119], v[136:139]\n\t" \
  "v_mfma_f32_16x16x32_bf16 v[132:135], v[228:231], v[112:115], v[132:135]\n\t" \
  "v_mfma_f32_16x16x32_bf16 v[140:143], v[228:231], v[116:119], v[140:143]\n\t" \
  "v_mfma_f32_16x16x32_bf16 v[128:131], v[232:235], v[120:123], v[128:131]\n\t" \
  "v_mfma_f32_16x16x32_bf16 v[136:139], v[232:235], v[124:127], v[136:139]\n\t" \
  "v_mfma_f32_16x16x32_bf16 v[132:135], v[236:239], v[120:123], v[132:135]\n\t" \
  "v_mfma_f32_16x16x32_bf16 v[140:143], v[236:239], v[124:127], v[140:143]\n\t" \
  "s_setprio 0\n\t" \
  "s_waitcnt vmcnt(9)\n\t" \
  "s_barrier\n\t" \
  "s_add_u32 s92, s92, 512\n\t" \
  "s_addc_u32 s93, s93, 0\n\t" \
  "ds_read_b128 v[176:179], v244 offset:0\n\t" \
  "ds_read_b128 v[180:183], v244 offset:16384\n\t" \
  "ds_read_b128 v[184:187], v245 offset:0\n\t" \
  "ds_read_b128 v[188:191], v245 offset:16384\n\t" \
  "s_add_u32 m0, %[lw], 32768\n\t" \
  "s_nop 0\n\t" \
  "global_load_lds_dwordx4 v248, s[92:93] sc1\n\t" \
  "ds_read_b128 v[192:195], v246 offset:0\n\t" \
  "ds_read_b128 v[196:199], v246 offset:16384\n\t" \
  "ds_read_b128 v[200:203], v247 offset:0\n\t" \
  "ds_read_b128 v[204:207], v247 offset:16384\n\t" \
  "s_add_u32 m0, %[lw], 33792\n\t" \
  "s_nop 0\n\t" \
  "global_load_lds_dwordx4 v249, s[92:93] sc1\n\t" \
  "ds_read_b128 v[208:211], v244 offset:256\n\t" \
  "ds_read_b128 v[212:215], v244 offset:16640\n\t" \
  "ds_read_b128 v[216:219], v245 offset:256\n\t" \
  "ds_read_b128 v[220:223], v245 offset:16640\n\t" \
  "s_add_u32 m0, %[lw], 34816\n\t" \
  "s_nop 0\n\t" \
  "global_load_lds_dwordx4 v250, s[92:93] sc1\n\t" \
  "ds_read_b128 v[224:227], v246 offset:256\n\t" \
  "ds_read_b128 v[228:231], v246 offset:16640\n\t" \
  "ds_read_b128 v[232:235], v247 offset:256\n\t" \
  "ds_read_b128 v[236:239], v247 offset:16640\n\t" \
  "s_add_u32 m0, %[lw], 35840\n\t" \
  "s_nop 0\n\t" \
  "global_load_lds_dwordx4 v251, s[92:93] sc1\n\t" \
  "ds_write_b128 v252, v[136:139] offset:0\n\t" \
  "ds_write_b128 v252, v[140:143] offset:1024\n\t" \
  "s_waitcnt lgkmcnt(0)\n\t" \
  "s_setprio 1\n\t" \
  "v_mfma_f32_16x16x32_bf16 v[144:147], v[176:179], v[0:3], 0\n\t" \
  "v_mfma_f32_16x16x32_bf16 v[152:155], v[176:179], v[4:7], 0\n\t" \
  "v_mfma_f32_16x16x32_bf16 v[148:151], v[180:183], v[0:3], 0\n\t" \
  "v_mfma_f32_16x16x32_bf16 v[156:159], v[180:183], v[4:7], 0\n\t" \
  "v_mfma_f32_16x16x32_bf16 v[144:147], v[184:187], v[8:11], v[144:147]\n\t" \
  "v_mfma_f32_16x16x32_bf16 v[152:155], v[184:187], v[12:15], v[152:155]\n\t" \
  "v_mfma_f32_16x16x32_bf16 v[148:151], v[188:191], v[8:11], v[148:151]\n\t" \
  "v_mfma_f32_16x16x32_bf16 v[156:159], v[188:191], v[12:15], v[156:159]\n\t" \
  "v_mfma_f32_16x16x32_bf16 v[144:147], v[192:195], v[16:19], v[144:147]\n\t" \
  "v_mfma_f32_16x16x32_bf16 v[152:155], v[192:195], v[20:23], v[152:155]\n\t" \
  "v_mfma_f32_16x16x32_bf16 v[148:151], v[196:199], v[16:19], v[148:151]\n\t" \
  "v_mfma_f32_16x16x32_bf16 v[156:159], v[196:199], v[20:23], v[156:159]\n\t" \
  "v_mfma_f32_16x16x32_bf16 v[144:147], v[200:203], v[24:27], v[144:147]\n\t" \
  "v_mfma_f32_16x16x32_bf16 v[152:155], v[200:203], v[28:31], v[152:155]\n\t" \
  "v_mfma_f32_16x16x32_bf16 v[148:151], v[204:207], v[24:27], v[148:151]\n\t" \
  "v_mfma_f32_16x16x32_bf16 v[156:159], v[204:207], v[28:31], v[156:159]\n\t" \
  "v_mfma_f32_16x16x32_bf16 v[144:147], v[208:211], v[32:35], v[144:147]\n\t" \
  "v_mfma_f32_16x16x32_bf16 v[152:155], v[208:211], v[36:39], v[152:155]\n\t" \
  "v_mfma_f32_16x16x32_bf16 v[148:151], v[212:215], v[32:35], v[148:151]\n\t" \
  "v_mfma_f32_16x16x32_bf16 v[156:159], v[212:215], v[36:39], v[156:159]\n\t" \
  "v_mfma_f32_16x16x32_bf16 v[144:147], v[216:219], v[40:43], v[144:147]\n\t" \
  "v_mfma_f32_16x16x32_bf16 v[152:155], v[216:219], v[44:47], v[152:155]\n\t" \
  "v_mfma_f32_16x16x32_bf16 v[148:151], v[220:223], v[40:43], v[148:151]\n\t" \
  "v_mfma_f32_16x16x32_bf16 v[156:159], v[220:223], v[44:47], v[156:159]\n\t" \
  "v_mfma_f32_16x16x32_bf16 v[144:147], v[224:227], v[48:51], v[144:147]\n\t" \
  "v_mfma_f32_16x16x32_bf16 v[152:155], v[224:227], v[52:55], v[152:155]\n\t" \
  "v_mfma_f32_16x16x32_bf16 v[148:151], v[228:231], v[48:51], v[148:151]\n\t" \
  "v_mfma_f32_16x16x32_bf16 v[156:159], v[228:231], v[52:55], v[156:159]\n\t" \
  "v_mfma_f32_16x16x32_bf16 v[144:147], v[232:235], v[56:59], v[144:147]\n\t" \
  "v_mfma_f32_16x16x32_bf16 v[152:155], v[232:235], v[60:63], v[152:155]\n\t" \
  "v_mfma_f32_16x16x32_bf16 v[148:151], v[236:239], v[56:59], v[148:151]\n\t" \
  "v_mfma_f32_16x16x32_bf16 v[156:159], v[236:239], v[60:63], v[156:159]\n\t" \
  "s_setprio 0\n\t" \
  "s_waitcnt vmcnt(4)\n\t" \
  "s_barrier\n\t" \
  "s_add_u32 s92, s92, 65024\n\t" \
  "s_addc_u32 s93, s93, 0\n\t" \
  "ds_read_b128 v[160:163], v253 offset:0\n\t" \
  "ds_read_b128 v[164:167], v253 offset:1024\n\t" \
  "ds_read_b128 v[176:179], v244 offset:32768\n\t" \
  "ds_read_b128 v[180:183], v244 offset:49152\n\t" \
  "ds_read_b128 v[184:187], v245 offset:32768\n\t" \
  "ds_read_b128 v[188:191], v245 offset:49152\n\t" \
  "s_add_u32 m0, %[lw], 65536\n\t" \
  "s_nop 0\n\t" \
  "global_load_lds_dwordx4 v248, s[92:93] sc1\n\t" \
  "ds_read_b128 v[192:195], v246 offset:32768\n\t" \
  "ds_read_b128 v[196:199], v246 offset:49152\n\t" \
  "ds_read_b128 v[200:203], v247 offset:32768\n\t" \
  "ds_read_b128 v[204:207], v247 offset:49152\n\t" \
  "s_add_u32 m0, %[lw], 66560\n\t" \
  "s_nop 0\n\t" \
  "global_load_lds_dwordx4 v249, s[92:93] sc1\n\t" \
  "ds_read_b128 v[208:211], v244 offset:33024\n\t" \
  "ds_read_b128 v[212:215], v244 offset:49408\n\t" \
  "ds_read_b128 v[216:219], v245 offset:33024\n\t" \
  "ds_read_b128 v[220:223], v245 offset:49408\n\t" \
  "s_add_u32 m0, %[lw], 67584\n\t" \
  "s_nop 0\n\t" \
  "global_load_lds_dwordx4 v250, s[92:93] sc1\n\t" \
  "ds_read_b128 v[224:227], v246 offset:33024\n\t" \
  "ds_read_b128 v[228:231], v246 offset:49408\n\t" \
  "ds_read_b128 v[232:235], v247 offset:33024\n\t" \
  "ds_read_b128 v[236:239], v247 offset:49408\n\t" \
  "s_add_u32 m0, %[lw], 68608\n\t" \
  "s_nop 0\n\t" \
  "global_load_lds_dwordx4 v251, s[92:93] sc1\n\t" \
  "s_waitcnt lgkmcnt(15)\n\t" \
  "s_waitcnt lgkmcnt(12)\n\t" \
  "s_lshr_b32 s90, s87, 7\n\t" \
  "s_xor_b32 s90, s90, %[s0]\n\t" \
  "s_and_b32 s90, s90, 1\n\t" \
  "s_lshl1_add_u32 s90, s87, s90\n\t" \
  "s_lshl_b32 s90, s90, 13\n\t" \
  "v_add_f32 v160, v160, v128\n\t" \
  "v_add_f32 v161, v161, v129\n\t" \
  "v_add_f32 v162, v162, v130\n\t" \
  "v_add_f32 v163, v163, v131\n\t" \
  "v_add_f32 v164, v164, v132\n\t" \
  "v_add_f32 v165, v165, v133\n\t" \
  "v_add_f32 v166, v166, v134\n\t" \
  "v_add_f32 v167, v167, v135\n\t" \
  "v_cvt_pk_bf16_f32 v168, v160, v161\n\t" \
  "v_cvt_pk_bf16_f32 v169, v162, v163\n\t" \
  "v_cvt_pk_bf16_f32 v170, v164, v165\n\t" \
  "v_cvt_pk_bf16_f32 v171, v166, v167\n\t" \
  "buffer_store_dwordx4 v[168:171], v172, %[rt], s90 offen sc1\n\t" \
  "s_add_u32 s86, s86, 1\n\t" \
  "s_add_u32 s87, s87, 1\n\t" \
  "s_waitcnt lgkmcnt(0)\n\t" \
  "s_setprio 1\n\t" \
  "v_mfma_f32_16x16x32_bf16 v[144:147], v[176:179], v[64:67], v[144:147]\n\t" \
  "v_mfma_f32_16x16x32_bf16 v[152:155], v[176:179], v[68:71], v[152:155]\n\t" \
  "v_mfma_f32_16x16x32_bf16 v[148:151], v[180:183], v[64:67], v[148:151]\n\t" \
  "v_mfma_f32_16x16x32_bf16 v[156:159], v[180:183], v[68:71], v[156:159]\n\t" \
  "v_mfma_f32_16x16x32_bf16 v[144:147], v[184:187], v[72:75], v[144:147]\n\t" \
  "v_mfma_f32_16x16x32_bf16 v[152:155], v[184:187], v[76:79], v[152:155]\n\t" \
  "v_mfma_f32_16x16x32_bf16 v[148:151], v[188:191], v[72:75], v[148:151]\n\t" \
  "v_mfma_f32_16x16x32_bf16 v[156:159], v[188:191], v[76:79], v[156:159]\n\t" \
  "v_mfma_f32_16x16x32_bf16 v[144:147], v[192:195], v[80:83], v[144:147]\n\t" \
  "v_mfma_f32_16x16x32_bf16 v[152:155], v[192:195], v[84:87], v[152:155]\n\t" \
  "v_mfma_f32_16x16x32_bf16 v[148:151], v[196:199], v[80:83], v[148:151]\n\t" \
  "v_mfma_f32_16x16x32_bf16 v[156:159], v[196:199], v[84:87], v[156:159]\n\t" \
  "v_mfma_f32_16x16x32_bf16 v[144:147], v[200:203], v[88:91], v[144:147]\n\t" \
  "v_mfma_f32_16x16x32_bf16 v[152:155], v[200:203], v[92:95], v[152:155]\n\t" \
  "v_mfma_f32_16x16x32_bf16 v[148:151], v[204:207], v[88:91], v[148:151]\n\t" \
  "v_mfma_f32_16x16x32_bf16 v[156:159], v[204:207], v[92:95], v[156:159]\n\t" \
  "v_mfma_f32_16x16x32_bf16 v[144:147], v[208:211], v[96:99], v[144:147]\n\t" \
  "v_mfma_f32_16x16x32_bf16 v[152:155], v[208:211], v[100:103], v[152:155]\n\t" \
  "v_mfma_f32_16x16x32_bf16 v[148:151], v[212:215], v[96:99], v[148:151]\n\t" \
  "v_mfma_f32_16x16x32_bf16 v[156:159], v[212:215], v[100:103], v[156:159]\n\t" \
  "v_mfma_f32_16x16x32_bf16 v[144:147], v[216:219], v[104:107], v[144:147]\n\t" \
  "v_mfma_f32_16x16x32_bf16 v[152:155], v[216:219], v[108:111], v[152:155]\n\t" \
  "v_mfma_f32_16x16x32_bf16 v[148:151], v[220:223], v[104:107], v[148:151]\n\t" \
  "v_mfma_f32_16x16x32_bf16 v[156:159], v[220:223], v[108:111], v[156:159]\n\t" \
  "v_mfma_f32_16x16x32_bf16 v[144:147], v[224:227], v[112:115], v[144:147]\n\t" \
  "v_mfma_f32_16x16x32_bf16 v[152:155], v[224:227], v[116:119], v[152:155]\n\t" \
  "v_mfma_f32_16x16x32_bf16 v[148:151], v[228:231], v[112:115], v[148:151]\n\t" \
  "v_mfma_f32_16x16x32_bf16 v[156:159], v[228:231], v[116:119], v[156:159]\n\t" \
  "v_mfma_f32_16x16x32_bf16 v[144:147], v[232:235], v[120:123], v[144:147]\n\t" \
  "v_mfma_f32_16x16x32_bf16 v[152:155], v[232:235], v[124:127], v[152:155]\n\t" \
  "v_mfma_f32_16x16x32_bf16 v[148:151], v[236:239], v[120:123], v[148:151]\n\t" \
  "v_mfma_f32_16x16x32_bf16 v[156:159], v[236:239], v[124:127], v[156:159]\n\t" \
  "s_setprio 0\n\t" \
  "s_cmp_lt_u32 s86, 64\n\t" \
  "s_cbranch_scc1 .Lp0%=\n\t" \
  "s_barrier\n\t" \
  "s_nop 1\n\t" \
  "ds_write_b128 v252, v[152:155] offset:0\n\t" \
  "s_nop 0\n\t" \
  "ds_write_b128 v252, v[156:159] offset:1024\n\t" \
  "s_waitcnt lgkmcnt(0)\n\t" \
  "s_barrier\n\t" \
  "ds_read_b128 v[160:163], v253 offset:0\n\t" \
  "ds_read_b128 v[164:167], v253 offset:1024\n\t" \
  "s_waitcnt lgkmcnt(0)\n\t" \
  "s_lshr_b32 s90, s87, 7\n\t" \
  "s_xor_b32 s90, s90, %[s0]\n\t" \
  "s_and_b32 s90, s90, 1\n\t" \
  "s_lshl1_add_u32 s90, s87, s90\n\t" \
  "s_lshl_b32 s90, s90, 13\n\t" \
  "v_add_f32 v160, v160, v144\n\t" \
  "v_add_f32 v161, v161, v145\n\t" \
  "v_add_f32 v162, v162, v146\n\t" \
  "v_add_f32 v163, v163, v147\n\t" \
  "v_add_f32 v164, v164, v148\n\t" \
  "v_add_f32 v165, v165, v149\n\t" \
  "v_add_f32 v166, v166, v150\n\t" \
  "v_add_f32 v167, v167, v151\n\t" \
  "v_cvt_pk_bf16_f32 v168, v160, v161\n\t" \
  "v_cvt_pk_bf16_f32 v169, v162, v163\n\t" \
  "v_cvt_pk_bf16_f32 v170, v164, v165\n\t" \
  "v_cvt_pk_bf16_f32 v171, v166, v167\n\t" \
  "buffer_store_dwordx4 v[168:171], v172, %[rt], s90 offen sc1\n\t" \
  "s_waitcnt vmcnt(0)\n\t" \
  "s_barrier\n\t" \
  "s_mov_b32 m0, s94\n\t" \
  "s_branch .Le%=\n\t" \
  ".Lk%=:\n\t" \
  "s_mov_b32 s94, m0\n\t" \
  "s_mov_b64 s[92:93], %[x0]\n\t" \
  "s_mov_b32 s86, %[g0]\n\t" \
  "s_add_u32 s87, s86, %[t0]\n\t" \
  "s_add_u32 m0, %[lw], 67584\n\t" \
  "s_nop 0\n\t" \
  "global_load_lds_dwordx4 v250, s[92:93] sc1\n\t" \
  "s_add_u32 m0, %[lw], 68608\n\t" \
  "s_nop 0\n\t" \
  "global_load_lds_dwordx4 v251, s[92:93] sc1\n\t" \
  "s_barrier\n\t" \
  "s_branch .Ls1%=\n\t" \
  ".Lp1%=:\n\t" \
  "s_waitcnt vmcnt(10)\n\t" \
  "s_barrier\n\t" \
  "s_setprio 2\n\t" \
  "v_mfma_f32_16x16x32_bf16 v[144:147], v[176:179], v[64:67], v[144:147]\n\t" \
  "v_mfma_f32_16x16x32_bf16 v[152:155], v[176:179], v[68:71], v[152:155]\n\t" \
  "v_mfma_f32_16x16x32_bf16 v[148:151], v[180:183], v[64:67], v[148:151]\n\t" \
  "v_mfma_f32_16x16x32_bf16 v[156:159], v[180:183], v[68:71], v[156:159]\n\t" \
  "v_mfma_f32_16x16x32_bf16 v[144:147], v[184:187], v[72:75], v[144:147]\n\t" \
  "v_mfma_f32_16x16x32_bf16 v[152:155], v[184:187], v[76:79], v[152:155]\n\t" \
  "v_mfma_f32_16x16x32_bf16 v[148:151], v[188:191], v[72:75], v[148:151]\n\t" \
  "v_mfma_f32_16x16x32_bf16 v[156:159], v[188:191], v[76:79], v[156:159]\n\t" \
  "v_mfma_f32_16x16x32_bf16 v[144:147], v[192:195], v[80:83], v[144:147]\n\t" \
  "v_mfma_f32_16x16x32_bf16 v[152:155], v[192:195], v[84:87], v[152:155]\n\t" \
  "v_mfma_f32_16x16x32_bf16 v[148:151], v[196:199], v[80:83], v[148:151]\n\t" \
  "v_mfma_f32_16x16x32_bf16 v[156:159], v[196:199], v[84:87], v[156:159]\n\t" \
  "v_mfma_f32_16x16x32_bf16 v[144:147], v[200:203], v[88:91], v[144:147]\n\t" \
  "v_mfma_f32_16x16x32_bf16 v[152:155], v[200:203], v[92:95], v[152:155]\n\t" \
  "v_mfma_f32_16x16x32_bf16 v[148:151], v[204:207], v[88:91], v[148:151]\n\t" \
  "v_mfma_f32_16x16x32_bf16 v[156:159], v[204:207], v[92:95], v[156:159]\n\t" \
  "v_mfma_f32_16x16x32_bf16 v[144:147], v[208:211], v[96:99], v[144:147]\n\t" \
  "v_mfma_f32_16x16x32_bf16 v[152:155], v[208:211], v[100:103], v[152:155]\n\t" \
  "v_mfma_f32_16x16x32_bf16 v[148:151], v[212:215], v[96:99], v[148:151]\n\t" \
  "v_mfma_f32_16x16x32_bf16 v[156:159], v[212:215], v[100:103], v[156:159]\n\t" \
  "v_mfma_f32_16x16x32_bf16 v[144:147], v[216:219], v[104:107], v[144:147]\n\t" \
  "v_mfma_f32_16x16x32_bf16 v[152:155], v[216:219], v[108:111], v[152:155]\n\t" \
  "v_mfma_f32_16x16x32_bf16 v[148:151], v[220:223], v[104:107], v[148:151]\n\t" \
  "v_mfma_f32_16x16x32_bf16 v[156:159], v[220:223], v[108:111], v[156:159]\n\t" \
  "v_mfma_f32_16x16x32_bf16 v[144:147], v[224:227], v[112:115], v[144:147]\n\t" \
  "v_mfma_f32_16x16x32_bf16 v[152:155], v[224:227], v[116:119], v[152:155]\n\t" \
  "v_mfma_f32_16x16x32_bf16 v[148:151], v[228:231], v[112:115], v[148:151]\n\t" \
  "v_mfma_f32_16x16x32_bf16 v[156:159], v[228:231], v[116:119], v[156:159]\n\t" \
  "v_mfma_f32_16x16x32_bf16 v[144:147], v[232:235], v[120:123], v[144:147]\n\t" \
  "v_mfma_f32_16x16x32_bf16 v[152:155], v[232:235], v[124:127], v[152:155]\n\t" \
  "v_mfma_f32_16x16x32_bf16 v[148:151], v[236:239], v[120:123], v[148:151]\n\t" \
  "v_mfma_f32_16x16x32_bf16 v[156:159], v[236:239], v[124:127], v[156:159]\n\t" \
  "s_setprio 0\n\t" \
  ".Ls1%=:\n\t" \
  "s_add_u32 s92, s92, 512\n\t" \
  "s_addc_u32 s93, s93, 0\n\t" \
  "ds_read_b128 v[176:179], v240 offset:0\n\t" \
  "ds_read_b128 v[180:183], v240 offset:16384\n\t" \
  "ds_read_b128 v[184:187], v241 offset:0\n\t" \
  "ds_read_b128 v[188:191], v241 offset:16384\n\t" \
  "s_add_u32 m0, %[lw], 98304\n\t" \
  "s_nop 0\n\t" \
  "global_load_lds_dwordx4 v248, s[92:93] sc1\n\t" \
  "ds_read_b128 v[192:195], v242 offset:0\n\t" \
  "ds_read_b128 v[196:199], v242 offset:16384\n\t" \
  "ds_read_b128 v[200:203], v243 offset:0\n\t" \
  "ds_read_b128 v[204:207], v243 offset:16384\n\t" \
  "s_add_u32 m0, %[lw], 99328\n\t" \
  "s_nop 0\n\t" \
  "global_load_lds_dwordx4 v249, s[92:93] sc1\n\t" \
  "ds_read_b128 v[208:211], v240 offset:256\n\t" \
  "ds_read_b128 v[212:215], v240 offset:16640\n\t" \
  "ds_read_b128 v[216:219], v241 offset:256\n\t" \
  "ds_read_b128 v[220:223], v241 offset:16640\n\t" \
  "s_add_u32 m0, %[lw], 100352\n\t" \
  "s_nop 0\n\t" \
  "global_load_lds_dwordx4 v250, s[92:93] sc1\n\t" \
  "ds_read_b128 v[224:227], v242 offset:256\n\t" \
  "ds_read_b128 v[228:231], v242 offset:16640\n\t" \
  "ds_read_b128 v[232:235], v243 offset:256\n\t" \
  "ds_read_b128 v[236:239], v243 offset:16640\n\t" \
  "s_add_u32 m0, %[lw], 101376\n\t" \
  "s_nop 0\n\t" \
  "global_load_lds_dwordx4 v251, s[92:93] sc1\n\t" \
  "ds_write_b128 v252, v[152:155] offset:0\n\t" \
  "ds_write_b128 v252, v[156:159] offset:1024\n\t" \
  "s_waitcnt lgkmcnt(0)\n\t" \
  "s_waitcnt vmcnt(4)\n\t" \
  "s_barrier\n\t" \
  "s_setprio 2\n\t" \
  "v_mfma_f32_16x16x32_bf16 v[128:131], v[176:179], v[0:3], 0\n\t" \
  "v_mfma_f32_16x16x32_bf16 v[136:139], v[176:179], v[4:7], 0\n\t" \
  "v_mfma_f32_16x16x32_bf16 v[132:135], v[180:183], v[0:3], 0\n\t" \
  "v_mfma_f32_16x16x32_bf16 v[140:143], v[180:183], v[4:7], 0\n\t" \
  "v_mfma_f32_16x16x32_bf16 v[128:131], v[184:187], v[8:11], v[128:131]\n\t" \
  "v_mfma_f32_16x16x32_bf16 v[136:139], v[184:187], v[12:15], v[136:139]\n\t" \
  "v_mfma_f32_16x16x32_bf16 v[132:135], v[188:191], v[8:11], v[132:135]\n\t" \
  "v_mfma_f32_16x16x32_bf16 v[140:143], v[188:191], v[12:15], v[140:143]\n\t" \
  "v_mfma_f32_16x16x32_bf16 v[128:131], v[192:195], v[16:19], v[128:131]\n\t" \
  "v_mfma_f32_16x16x32_bf16 v[136:139], v[192:195], v[20:23], v[136:139]\n\t" \
  "v_mfma_f32_16x16x32_bf16 v[132:135], v[196:199], v[16:19], v[132:135]\n\t" \
  "v_mfma_f32_16x16x32_bf16 v[140:143], v[196:199], v[20:23], v[140:143]\n\t" \
  "v_mfma_f32_16x16x32_bf16 v[128:131], v[200:203], v[24:27], v[128:131]\n\t" \
  "v_mfma_f32_16x16x32_bf16 v[136:139], v[200:203], v[28:31], v[136:139]\n\t" \
  "v_mfma_f32_16x16x32_bf16 v[132:135], v[204:207], v[24:27], v[132:135]\n\t" \
  "v_mfma_f32_16x16x32_bf16 v[140:143], v[204:207], v[28:31], v[140:143]\n\t" \
  "v_mfma_f32_16x16x32_bf16 v[128:131], v[208:211], v[32:35], v[128:131]\n\t" \
  "v_mfma_f32_16x16x32_bf16 v[136:139], v[208:211], v[36:39], v[136:139]\n\t" \
  "v_mfma_f32_16x16x32_bf16 v[132:135], v[212:215], v[32:35], v[132:135]\n\t" \
  "v_mfma_f32_16x16x32_bf16 v[140:143], v[212:215], v[36:39], v[140:143]\n\t" \
  "v_mfma_f32_16x16x32_bf16 v[128:131], v[216:219], v[40:43], v[128:131]\n\t" \
  "v_mfma_f32_16x16x32_bf16 v[136:139], v[216:219], v[44:47], v[136:139]\n\t" \
  "v_mfma_f32_16x16x32_bf16 v[132:135], v[220:223], v[40:43], v[132:135]\n\t" \
  "v_mfma_f32_16x16x32_bf16 v[140:143], v[220:223], v[44:47], v[140:143]\n\t" \
  "v_mfma_f32_16x16x32_bf16 v[128:131], v[224:227], v[48:51], v[128:131]\n\t" \
  "v_mfma_f32_16x16x32_bf16 v[136:139], v[224:227], v[52:55], v[136:139]\n\t" \
  "v_mfma_f32_16x16x32_bf16 v[132:135], v[228:231], v[48:51], v[132:135]\n\t" \
  "v_mfma_f32_16x16x32_bf16 v[140:143], v[228:231], v[52:55], v[140:143]\n\t" \
  "v_mfma_f32_16x16x32_bf16 v[128:131], v[232:235], v[56:59], v[128:131]\n\t" \
  "v_mfma_f32_16x16x32_bf16 v[136:139], v[232:235], v[60:63], v[136:139]\n\t" \
  "v_mfma_f32_16x16x32_bf16 v[132:135], v[236:239], v[56:59], v[132:135]\n\t" \
  "v_mfma_f32_16x16x32_bf16 v[140:143], v[236:239], v[60:63], v[140:143]\n\t" \
  "s_setprio 0\n\t" \
  "s_add_u32 s92, s92, 65024\n\t" \
  "s_addc_u32 s93, s93, 0\n\t" \
  "ds_read_b128 v[160:163], v253 offset:0\n\t" \
  "ds_read_b128 v[164:167], v253 offset:1024\n\t" \
  "ds_read_b128 v[176:179], v240 offset:32768\n\t" \
  "ds_read_b128 v[180:183], v240 offset:49152\n\t" \
  "ds_read_b128 v[184:187], v241 offset:32768\n\t" \
  "ds_read_b128 v[188:191], v241 offset:49152\n\t" \
  "s_add_u32 m0, %[lw], 0\n\t" \
  "s_nop 0\n\t" \
  "global_load_lds_dwordx4 v248, s[92:93] sc1\n\t" \
  "ds_read_b128 v[192:195], v242 offset:32768\n\t" \
  "ds_read_b128 v[196:199], v242 offset:49152\n\t" \
  "ds_read_b128 v[200:203], v243 offset:32768\n\t" \
  "ds_read_b128 v[204:207], v243 offset:49152\n\t" \
  "s_add_u32 m0, %[lw], 1024\n\t" \
  "s_nop 0\n\t" \
  "global_load_lds_dwordx4 v249, s[92:93] sc1\n\t" \
  "ds_read_b128 v[208:211], v240 offset:33024\n\t" \
  "ds_read_b128 v[212:215], v240 offset:49408\n\t" \
  "ds_read_b128 v[216:219], v241 offset:33024\n\t" \
  "ds_read_b128 v[220:223], v241 offset:49408\n\t" \
  "s_add_u32 m0, %[lw], 2048\n\t" \
  "s_nop 0\n\t" \
  "global_load_lds_dwordx4 v250, s[92:93] sc1\n\t" \
  "ds_read_b128 v[224:227], v242 offset:33024\n\t" \
  "ds_read_b128 v[228:231], v242 offset:49408\n\t" \
  "ds_read_b128 v[232:235], v243 offset:33024\n\t" \
  "ds_read_b128 v[236:239], v243 offset:49408\n\t" \
  "s_add_u32 m0, %[lw], 3072\n\t" \
  "s_nop 0\n\t" \
  "global_load_lds_dwordx4 v251, s[92:93] sc1\n\t" \
  "s_waitcnt lgkmcnt(15)\n\t" \
  "s_waitcnt lgkmcnt(12)\n\t" \
  "s_sub_i32 s91, s86, 2\n\t" \
  "v_mov_b32 v173, s91\n\t" \
  "buffer_store_dword v173, v174, %[rs], 0 offen sc1\n\t" \
  "s_lshr_b32 s90, s87, 7\n\t" \
  "s_xor_b32 s90, s90, %[s0]\n\t" \
  "s_and_b32 s90, s90, 1\n\t" \
  "s_lshl1_add_u32 s90, s87, s90\n\t" \
  "s_lshl_b32 s90, s90, 13\n\t" \
  "v_add_f32 v160, v160, v144\n\t" \
  "v_add_f32 v161, v161, v145\n\t" \
  "v_add_f32 v162, v162, v146\n\t" \
  "v_add_f32 v163, v163, v147\n\t" \
  "v_add_f32 v164, v164, v148\n\t" \
  "v_add_f32 v165, v165, v149\n\t" \
  "v_add_f32 v166, v166, v150\n\t" \
  "v_add_f32 v167, v167, v151\n\t" \
  "v_cvt_pk_bf16_f32 v168, v160, v161\n\t" \
  "v_cvt_pk_bf16_f32 v169, v162, v163\n\t" \
  "v_cvt_pk_bf16_f32 v170, v164, v165\n\t" \
  "v_cvt_pk_bf16_f32 v171, v166, v167\n\t" \
  "buffer_store_dwordx4 v[168:171], v172, %[rt], s90 offen sc1\n\t" \
  "s_add_u32 s86, s86, 1\n\t" \
  "s_add_u32 s87, s87, 1\n\t" \
  "s_waitcnt lgkmcnt(0)\n\t" \
  "s_waitcnt vmcnt(10)\n\t" \
  "s_barrier\n\t" \
  "s_setprio 2\n\t" \
  "v_mfma_f32_16x16x32_bf16 v[128:131], v[176:179], v[64:67], v[128:131]\n\t" \
  "v_mfma_f32_16x16x32_bf16 v[136:139], v[176:179], v[68:71], v[136:139]\n\t" \
  "v_mfma_f32_16x16x32_bf16 v[132:135], v[180:183], v[64:67], v[132:135]\n\t" \
  "v_mfma_f32_16x16x32_bf16 v[140:143], v[180:183], v[68:71], v[140:143]\n\t" \
  "v_mfma_f32_16x16x32_bf16 v[128:131], v[184:187], v[72:75], v[128:131]\n\t" \
  "v_mfma_f32_16x16x32_bf16 v[136:139], v[184:187], v[76:79], v[136:139]\n\t" \
  "v_mfma_f32_16x16x32_bf16 v[132:135], v[188:191], v[72:75], v[132:135]\n\t" \
  "v_mfma_f32_16x16x32_bf16 v[140:143], v[188:191], v[76:79], v[140:143]\n\t" \
  "v_mfma_f32_16x16x32_bf16 v[128:131], v[192:195], v[80:83], v[128:131]\n\t" \
  "v_mfma_f32_16x16x32_bf16 v[136:139], v[192:195], v[84:87], v[136:139]\n\t" \
  "v_mfma_f32_16x16x32_bf16 v[132:135], v[196:199], v[80:83], v[132:135]\n\t" \
  "v_mfma_f32_16x16x32_bf16 v[140:143], v[196:199], v[84:87], v[140:143]\n\t" \
  "v_mfma_f32_16x16x32_bf16 v[128:131], v[200:203], v[88:91], v[128:131]\n\t" \
  "v_mfma_f32_16x16x32_bf16 v[136:139], v[200:203], v[92:95], v[136:139]\n\t" \
  "v_mfma_f32_16x16x32_bf16 v[132:135], v[204:207], v[88:91], v[132:135]\n\t" \
  "v_mfma_f32_16x16x32_bf16 v[140:143], v[204:207], v[92:95], v[140:143]\n\t" \
  "v_mfma_f32_16x16x32_bf16 v[128:131], v[208:211], v[96:99], v[128:131]\n\t" \
  "v_mfma_f32_16x16x32_bf16 v[136:139], v[208:211], v[100:103], v[136:139]\n\t" \
  "v_mfma_f32_16x16x32_bf16 v[132:135], v[212:215], v[96:99], v[132:135]\n\t" \
  "v_mfma_f32_16x16x32_bf16 v[140:143], v[212:215], v[100:103], v[140:143]\n\t" \
  "v_mfma_f32_16x16x32_bf16 v[128:131], v[216:219], v[104:107], v[128:131]\n\t" \
  "v_mfma_f32_16x16x32_bf16 v[136:139], v[216:219], v[108:111], v[136:139]\n\t" \
  "v_mfma_f32_16x16x32_bf16 v[132:135], v[220:223], v[104:107], v[132:135]\n\t" \
  "v_mfma_f32_16x16x32_bf16 v[140:143], v[220:223], v[108:111], v[140:143]\n\t" \
  "v_mfma_f32_16x16x32_bf16 v[128:131], v[224:227], v[112:115], v[128:131]\n\t" \
  "v_mfma_f32_16x16x32_bf16 v[136:139], v[224:227], v[116:119], v[136:139]\n\t" \
  "v_mfma_f32_16x16x32_bf16 v[132:135], v[228:231], v[112:115], v[132:135]\n\t" \
  "v_mfma_f32_16x16x32_bf16 v[140:143], v[228:231], v[116:119], v[140:143]\n\t" \
  "v_mfma_f32_16x16x32_bf16 v[128:131], v[232:235], v[120:123], v[128:131]\n\t" \
  "v_mfma_f32_16x16x32_bf16 v[136:139], v[232:235], v[124:127], v[136:139]\n\t" \
  "v_mfma_f32_16x16x32_bf16 v[132:135], v[236:239], v[120:123], v[132:135]\n\t" \
  "v_mfma_f32_16x16x32_bf16 v[140:143], v[236:239], v[124:127], v[140:143]\n\t" \
  "s_setprio 0\n\t" \
  "s_add_u32 s92, s92, 512\n\t" \
  "s_addc_u32 s93, s93, 0\n\t" \
  "ds_read_b128 v[176:179], v244 offset:0\n\t" \
  "ds_read_b128 v[180:183], v244 offset:16384\n\t" \
  "ds_read_b128 v[184:187], v245 offset:0\n\t" \
  "ds_read_b128 v[188:191], v245 offset:16384\n\t" \
  "s_add_u32 m0, %[lw], 32768\n\t" \
  "s_nop 0\n\t" \
  "global_load_lds_dwordx4 v248, s[92:93] sc1\n\t" \
  "ds_read_b128 v[192:195], v246 offset:0\n\t" \
  "ds_read_b128 v[196:199], v246 offset:16384\n\t" \
  "ds_read_b128 v[200:203], v247 offset:0\n\t" \
  "ds_read_b128 v[204:207], v247 offset:16384\n\t" \
  "s_add_u32 m0, %[lw], 33792\n\t" \
  "s_nop 0\n\t" \
  "global_load_lds_dwordx4 v249, s[92:93] sc1\n\t" \
  "ds_read_b128 v[208:211], v244 offset:256\n\t" \
  "ds_read_b128 v[212:215], v244 offset:16640\n\t" \
  "ds_read_b128 v[216:219], v245 offset:256\n\t" \
  "ds_read_b128 v[220:223], v245 offset:16640\n\t" \
  "s_add_u32 m0, %[lw], 34816\n\t" \
  "s_nop 0\n\t" \
  "global_load_lds_dwordx4 v250, s[92:93] sc1\n\t" \
  "ds_read_b128 v[224:227], v246 offset:256\n\t" \
  "ds_read_b128 v[228:231], v246 offset:16640\n\t" \
  "ds_read_b128 v[232:235], v247 offset:256\n\t" \
  "ds_read_b128 v[236:239], v247 offset:16640\n\t" \
  "s_add_u32 m0, %[lw], 35840\n\t" \
  "s_nop 0\n\t" \
  "global_load_lds_dwordx4 v251, s[92:93] sc1\n\t" \
  "ds_write_b128 v252, v[136:139] offset:0\n\t" \
  "ds_write_b128 v252, v[140:143] offset:1024\n\t" \
  "s_waitcnt lgkmcnt(0)\n\t" \
  "s_waitcnt vmcnt(4)\n\t" \
  "s_barrier\n\t" \
  "s_setprio 2\n\t" \
  "v_mfma_f32_16x16x32_bf16 v[144:147], v[176:179], v[0:3], 0\n\t" \
  "v_mfma_f32_16x16x32_bf16 v[152:155], v[176:179], v[4:7], 0\n\t" \
  "v_mfma_f32_16x16x32_bf16 v[148:151], v[180:183], v[0:3], 0\n\t" \
  "v_mfma_f32_16x16x32_bf16 v[156:159], v[180:183], v[4:7], 0\n\t" \
  "v_mfma_f32_16x16x32_bf16 v[144:147], v[184:187], v[8:11], v[144:147]\n\t" \
  "v_mfma_f32_16x16x32_bf16 v[152:155], v[184:187], v[12:15], v[152:155]\n\t" \
  "v_mfma_f32_16x16x32_bf16 v[148:151], v[188:191], v[8:11], v[148:151]\n\t" \
  "v_mfma_f32_16x16x32_bf16 v[156:159], v[188:191], v[12:15], v[156:159]\n\t" \
  "v_mfma_f32_16x16x32_bf16 v[144:147], v[192:195], v[16:19], v[144:147]\n\t" \
  "v_mfma_f32_16x16x32_bf16 v[152:155], v[192:195], v[20:23], v[152:155]\n\t" \
  "v_mfma_f32_16x16x32_bf16 v[148:151], v[196:199], v[16:19], v[148:151]\n\t" \
  "v_mfma_f32_16x16x32_bf16 v[156:159], v[196:199], v[20:23], v[156:159]\n\t" \
  "v_mfma_f32_16x16x32_bf16 v[144:147], v[200:203], v[24:27], v[144:147]\n\t" \
  "v_mfma_f32_16x16x32_bf16 v[152:155], v[200:203], v[28:31], v[152:155]\n\t" \
  "v_mfma_f32_16x16x32_bf16 v[148:151], v[204:207], v[24:27], v[148:151]\n\t" \
  "v_mfma_f32_16x16x32_bf16 v[156:159], v[204:207], v[28:31], v[156:159]\n\t" \
  "v_mfma_f32_16x16x32_bf16 v[144:147], v[208:211], v[32:35], v[144:147]\n\t" \
  "v_mfma_f32_16x16x32_bf16 v[152:155], v[208:211], v[36:39], v[152:155]\n\t" \
  "v_mfma_f32_16x16x32_bf16 v[148:151], v[212:215], v[32:35], v[148:151]\n\t" \
  "v_mfma_f32_16x16x32_bf16 v[156:159], v[212:215], v[36:39], v[156:159]\n\t" \
  "v_mfma_f32_16x16x32_bf16 v[144:147], v[216:219], v[40:43], v[144:147]\n\t" \
  "v_mfma_f32_16x16x32_bf16 v[152:155], v[216:219], v[44:47], v[152:155]\n\t" \
  "v_mfma_f32_16x16x32_bf16 v[148:151], v[220:223], v[40:43], v[148:151]\n\t" \
  "v_mfma_f32_16x16x32_bf16 v[156:159], v[220:223], v[44:47], v[156:159]\n\t" \
  "v_mfma_f32_16x16x32_bf16 v[144:147], v[224:227], v[48:51], v[144:147]\n\t" \
  "v_mfma_f32_16x16x32_bf16 v[152:155], v[224:227], v[52:55], v[152:155]\n\t" \
  "v_mfma_f32_16x16x32_bf16 v[148:151], v[228:231], v[48:51], v[148:151]\n\t" \
  "v_mfma_f32_16x16x32_bf16 v[156:159], v[228:231], v[52:55], v[156:159]\n\t" \
  "v_mfma_f32_16x16x32_bf16 v[144:147], v[232:235], v[56:59], v[144:147]\n\t" \
  "v_mfma_f32_16x16x32_bf16 v[152:155], v[232:235], v[60:63], v[152:155]\n\t" \
  "v_mfma_f32_16x16x32_bf16 v[148:151], v[236:239], v[56:59], v[148:151]\n\t" \
  "v_mfma_f32_16x16x32_bf16 v[156:159], v[236:239], v[60:63], v[156:159]\n\t" \
  "s_setprio 0\n\t" \
  "s_add_u32 s92, s92, 65024\n\t" \
  "s_addc_u32 s93, s93, 0\n\t" \
  "ds_read_b128 v[160:163], v253 offset:0\n\t" \
  "ds_read_b128 v[164:167], v253 offset:1024\n\t" \
  "ds_read_b128 v[176:179], v244 offset:32768\n\t" \
  "ds_read_b128 v[180:183], v244 offset:49152\n\t" \
  "ds_read_b128 v[184:187], v245 offset:32768\n\t" \
  "ds_read_b128 v[188:191], v245 offset:49152\n\t" \
  "s_add_u32 m0, %[lw], 65536\n\t" \
  "s_nop 0\n\t" \
  "global_load_lds_dwordx4 v248, s[92:93] sc1\n\t" \
  "ds_read_b128 v[192:195], v246 offset:32768\n\t" \
  "ds_read_b128 v[196:199], v246 offset:49152\n\t" \
  "ds_read_b128 v[200:203], v247 offset:32768\n\t" \
  "ds_read_b128 v[204:207], v247 offset:49152\n\t" \
  "s_add_u32 m0, %[lw], 66560\n\t" \
  "s_nop 0\n\t" \
  "global_load_lds_dwordx4 v249, s[92:93] sc1\n\t" \
  "ds_read_b128 v[208:211], v244 offset:33024\n\t" \
  "ds_read_b128 v[212:215], v244 offset:49408\n\t" \
  "ds_read_b128 v[216:219], v245 offset:33024\n\t" \
  "ds_read_b128 v[220:223], v245 offset:49408\n\t" \
  "s_add_u32 m0, %[lw], 67584\n\t" \
  "s_nop 0\n\t" \
  "global_load_lds_dwordx4 v250, s[92:93] sc1\n\t" \
  "ds_read_b128 v[224:227], v246 offset:33024\n\t" \
  "ds_read_b128 v[228:231], v246 offset:49408\n\t" \
  "ds_read_b128 v[232:235], v247 offset:33024\n\t" \
  "ds_read_b128 v[236:239], v247 offset:49408\n\t" \
  "s_add_u32 m0, %[lw], 68608\n\t" \
  "s_nop 0\n\t" \
  "global_load_lds_dwordx4 v251, s[92:93] sc1\n\t" \
  "s_waitcnt lgkmcnt(15)\n\t" \
  "s_waitcnt lgkmcnt(12)\n\t" \
  "s_sub_i32 s91, s86, 2\n\t" \
  "v_mov_b32 v173, s91\n\t" \
  "buffer_store_dword v173, v174, %[rs], 0 offen sc1\n\t" \
  "s_lshr_b32 s90, s87, 7\n\t" \
  "s_xor_b32 s90, s90, %[s0]\n\t" \
  "s_and_b32 s90, s90, 1\n\t" \
  "s_lshl1_add_u32 s90, s87, s90\n\t" \
  "s_lshl_b32 s90, s90, 13\n\t" \
  "v_add_f32 v160, v160, v128\n\t" \
  "v_add_f32 v161, v161, v129\n\t" \
  "v_add_f32 v162, v162, v130\n\t" \
  "v_add_f32 v163, v163, v131\n\t" \
  "v_add_f32 v164, v164, v132\n\t" \
  "v_add_f32 v165, v165, v133\n\t" \
  "v_add_f32 v166, v166, v134\n\t" \
  "v_add_f32 v167, v167, v135\n\t" \
  "v_cvt_pk_bf16_f32 v168, v160, v161\n\t" \
  "v_cvt_pk_bf16_f32 v169, v162, v163\n\t" \
  "v_cvt_pk_bf16_f32 v170, v164, v165\n\t" \
  "v_cvt_pk_bf16_f32 v171, v166, v167\n\t" \
  "buffer_store_dwordx4 v[168:171], v172, %[rt], s90 offen sc1\n\t" \
  "s_add_u32 s86, s86, 1\n\t" \
  "s_add_u32 s87, s87, 1\n\t" \
  "s_waitcnt lgkmcnt(0)\n\t" \
  "s_cmp_lt_u32 s86, 64\n\t" \
  "s_cbranch_scc1 .Lp1%=\n\t" \
  "s_barrier\n\t" \
  "s_setprio 2\n\t" \
  "v_mfma_f32_16x16x32_bf16 v[144:147], v[176:179], v[64:67], v[144:147]\n\t" \
  "v_mfma_f32_16x16x32_bf16 v[152:155], v[176:179], v[68:71], v[152:155]\n\t" \
  "v_mfma_f32_16x16x32_bf16 v[148:151], v[180:183], v[64:67], v[148:151]\n\t" \
  "v_mfma_f32_16x16x32_bf16 v[156:159], v[180:183], v[68:71], v[156:159]\n\t" \
  "v_mfma_f32_16x16x32_bf16 v[144:147], v[184:187], v[72:75], v[144:147]\n\t" \
  "v_mfma_f32_16x16x32_bf16 v[152:155], v[184:187], v[76:79], v[152:155]\n\t" \
  "v_mfma_f32_16x16x32_bf16 v[148:151], v[188:191], v[72:75], v[148:151]\n\t" \
  "v_mfma_f32_16x16x32_bf16 v[156:159], v[188:191], v[76:79], v[156:159]\n\t" \
  "v_mfma_f32_16x16x32_bf16 v[144:147], v[192:195], v[80:83], v[144:147]\n\t" \
  "v_mfma_f32_16x16x32_bf16 v[152:155], v[192:195], v[84:87], v[152:155]\n\t" \
  "v_mfma_f32_16x16x32_bf16 v[148:151], v[196:199], v[80:83], v[148:151]\n\t" \
  "v_mfma_f32_16x16x32_bf16 v[156:159], v[196:199], v[84:87], v[156:159]\n\t" \
  "v_mfma_f32_16x16x32_bf16 v[144:147], v[200:203], v[88:91], v[144:147]\n\t" \
  "v_mfma_f32_16x16x32_bf16 v[152:155], v[200:203], v[92:95], v[152:155]\n\t" \
  "v_mfma_f32_16x16x32_bf16 v[148:151], v[204:207], v[88:91], v[148:151]\n\t" \
  "v_mfma_f32_16x16x32_bf16 v[156:159], v[204:207], v[92:95], v[156:159]\n\t" \
  "v_mfma_f32_16x16x32_bf16 v[144:147], v[208:211], v[96:99], v[144:147]\n\t" \
  "v_mfma_f32_16x16x32_bf16 v[152:155], v[208:211], v[100:103], v[152:155]\n\t" \
  "v_mfma_f32_16x16x32_bf16 v[148:151], v[212:215], v[96:99], v[148:151]\n\t" \
  "v_mfma_f32_16x16x32_bf16 v[156:159], v[212:215], v[100:103], v[156:159]\n\t" \
  "v_mfma_f32_16x16x32_bf16 v[144:147], v[216:219], v[104:107], v[144:147]\n\t" \
  "v_mfma_f32_16x16x32_bf16 v[152:155], v[216:219], v[108:111], v[152:155]\n\t" \
  "v_mfma_f32_16x16x32_bf16 v[148:151], v[220:223], v[104:107], v[148:151]\n\t" \
  "v_mfma_f32_16x16x32_bf16 v[156:159], v[220:223], v[108:111], v[156:159]\n\t" \
  "v_mfma_f32_16x16x32_bf16 v[144:147], v[224:227], v[112:115], v[144:147]\n\t" \
  "v_mfma_f32_16x16x32_bf16 v[152:155], v[224:227], v[116:119], v[152:155]\n\t" \
  "v_mfma_f32_16x16x32_bf16 v[148:151], v[228:231], v[112:115], v[148:151]\n\t" \
  "v_mfma_f32_16x16x32_bf16 v[156:159], v[228:231], v[116:119], v[156:159]\n\t" \
  "v_mfma_f32_16x16x32_bf16 v[144:147], v[232:235], v[120:123], v[144:147]\n\t" \
  "v_mfma_f32_16x16x32_bf16 v[152:155], v[232:235], v[124:127], v[152:155]\n\t" \
  "v_mfma_f32_16x16x32_bf16 v[148:151], v[236:239], v[120:123], v[148:151]\n\t" \
  "v_mfma_f32_16x16x32_bf16 v[156:159], v[236:239], v[124:127], v[156:159]\n\t" \
  "s_setprio 0\n\t" \
  "s_nop 4\n\t" \
  "ds_write_b128 v252, v[152:155] offset:0\n\t" \
  "s_nop 0\n\t" \
  "ds_write_b128 v252, v[156:159] offset:1024\n\t" \
  "s_waitcnt lgkmcnt(0)\n\t" \
  "s_barrier\n\t" \
  "ds_read_b128 v[160:163], v253 offset:0\n\t" \
  "ds_read_b128 v[164:167], v253 offset:1024\n\t" \
  "s_waitcnt lgkmcnt(0)\n\t" \
  "s_lshr_b32 s90, s87, 7\n\t" \
  "s_xor_b32 s90, s90, %[s0]\n\t" \
  "s_and_b32 s90, s90, 1\n\t" \
  "s_lshl1_add_u32 s90, s87, s90\n\t" \
  "s_lshl_b32 s90, s90, 13\n\t" \
  "v_add_f32 v160, v160, v144\n\t" \
  "v_add_f32 v161, v161, v145\n\t" \
  "v_add_f32 v162, v162, v146\n\t" \
  "v_add_f32 v163, v163, v147\n\t" \
  "v_add_f32 v164, v164, v148\n\t" \
  "v_add_f32 v165, v165, v149\n\t" \
  "v_add_f32 v166, v166, v150\n\t" \
  "v_add_f32 v167, v167, v151\n\t" \
  "v_cvt_pk_bf16_f32 v168, v160, v161\n\t" \
  "v_cvt_pk_bf16_f32 v169, v162, v163\n\t" \
  "v_cvt_pk_bf16_f32 v170, v164, v165\n\t" \
  "v_cvt_pk_bf16_f32 v171, v166, v167\n\t" \
  "buffer_store_dwordx4 v[168:171], v172, %[rt], s90 offen sc1\n\t" \
  "s_waitcnt vmcnt(0)\n\t" \
  "s_barrier\n\t" \
  "s_sub_i32 s91, s86, 1\n\t" \
  "v_mov_b32 v173, s91\n\t" \
  "buffer_store_dword v173, v174, %[rs], 0 offen sc1\n\t" \
  "s_mov_b32 m0, s94\n\t" \
  ".Le%=:\n\t"
constexpr int W_X = 131072, W_PM = W_X + 16384;
template <bool ISV>
__device__ __forceinline__ void job_proj(Args& p, int g, int which, int h, char* smem, __amdgpu_buffer_rsrc_t rsy) {
  const int tid = threadIdx.x, lane = tid & 63, r = lane & 15, q = lane >> 4;
  const int w = __builtin_amdgcn_readfirstlane(tid >> 6), kh = w >> 2, cg = w & 3;
  u32x32 W[4];
  const unsigned lw = lds_off(smem) + w * 4096;
  u32x4 vx;
  u32x2 tag;
  const char* xn = p.ws + WS_XN;
  auto row = [&](int t, int i) {
    const char* src = xn + (size_t)(min(t, 127) >> 1) * (SEG * XLD * 2) + (t & 1) * 512;
    unsigned keep;
    asm volatile("s_mov_b32 %0, m0\n\ts_mov_b32 m0, %3\n\ts_nop 0\n\tglobal_load_lds_dwordx4 %1, %2 sc1\n\ts_mov_b32 m0, %0"
                 : "=&s"(keep) : "v"(vx[i]), "s"(src), "s"(lw + (t & 3) * 32768 + i * 1024) : "memory");
  };
  {
    const u16* Wm = which == 0 ? p.Wk : p.Wq;
    asm volatile("" : "+s"(Wm));
    if (ISV) Wm = p.Wv;
    const unsigned vo = (tid >> 4) * (HD * 2) + 16 * ((lane & 15) ^ swzB((tid >> 4) & 15));
    auto stage = [&](int j) {
      const char* src = (const char*)(Wm + (size_t)(WREV ? 7 - j : j) * 64 * HD + h * DH);
      const unsigned dst = lds_off(smem) + (j & 3) * 32768 + w * 1024;
      glds2(src, 32 * HD * 2, vo, dst);
      if (TH_P < 63) HOLD_V((h & 3) ^ 1, TH_P);
      glds2(src + 512 * HD * 2, 32 * HD * 2, vo, dst + 16384);
      if (TH_P < 63) HOLD_V((h & 3) ^ 1, TH_P);
    };
    if (which == 1) { for (int z = 0; z < Q_DELAY; ++z) __builtin_amdgcn_s_sleep(16); }
    const unsigned own = min((h & LN_M) ^ LN_X, 1);
    tag = start_rows<16>(p, 32 + g, own, [&]() { stage(0); stage(1); stage(2); stage(3); });
#pragma unroll
    for (int i = 0; i < 4; ++i) vx[i] = ((4 * w + i) ^ (ISV ? 0 : (lane >> 1) & 16)) * (XLD * 2) + (lane >> 5) * 1024 + (((lane & 31) ^ ((4 * w + i) & 15)) << 4);
    Boot bt;
#pragma unroll
    for (int j = 0; j < 8; ++j) {
      if (j < 4) WAIT_V(16); else if (j == 4) WAIT_V(12); else if (j == 5) WAIT_V(8); else if (j == 6) WAIT_V(4); else WAIT_V(10);
      if (j == 6 && w == 0) lds_at(smem + W_PM)[0] = boot_done(p.ws, bt, true, tag);
      LBAR();
      if (j == 4 && own) report_rows(p, 32 + g, tag);
      const char* sb = smem + (j & 3) * 32768 + kh * 16384;
#pragma unroll
      for (int k = 0; k < 4; ++k) {
        const int ks = k >> 1, a = k & 1;
        const unsigned ch = ISV ? 2 * (2 * cg + (a ^ kh)) + ((r & 3) >> 1) : 4 * cg + (r & 3), by = ISV ? 8 * (r & 1) : 8 * a;
        s16x4 lo = __builtin_amdgcn_ds_read_tr16_b64_v4i16((lds_s16x4_ptr)(sb + offB(32 * ks + 8 * q + (r >> 2), ch) + by));
        s16x4 hi = __builtin_amdgcn_ds_read_tr16_b64_v4i16((lds_s16x4_ptr)(sb + offB(32 * ks + 8 * q + 4 + (r >> 2), ch) + by));
        const int f = 4 * (WREV ? 7 - j : j) + k;
        put4(W[f >> 3], f & 7, (bf16x8){lo[0], lo[1], lo[2], lo[3], hi[0], hi[1], hi[2], hi[3]});
      }
      LBAR();
      if (j == 3 && w == 0) boot_ask(p.ws, bt);
      if (j + 4 < 8) stage(j + 4);
      if (j == 6) {
#pragma unroll
        for (int i = 0; i < 10; ++i) row(i >> 2, i & 3);
      }
    }
    p.s0 = uni(lds_at(smem + W_PM)[0] >> 31);
  }
  RDY(128 + g);
  typedef __attribute__((ext_vector_type(8))) unsigned u32x8;
  u32x8 la;
#pragma unroll
  for (int i = 0; i < 8; ++i) la[i] = (i >> 2) * 65536 + r * 1024 + kh * 512 + (((4 * (i & 3) + q) ^ r) << 4);
  const unsigned xw = W_X + w * 2048 + lane * 16;
  u32x32 acc;
  asm volatile("" : "={v[128:159]}"(acc));
  WAIT_V(6);
  LBAR();
  {
    const int g0 = 0;
#define PR_LOOP(TXT)                                                                                                              \
    asm volatile(TXT                                                                                                              \
        : "+{v[128:159]}"(acc)                                                                                                    \
        : W4_IN, "{v[208:215]}"(la), "{v[216:219]}"(vx), "{v220}"(xw), "{v221}"(xw ^ 8192),                                       \
          "{v172}"((ISV ? 2 * cg + kh : 4 * kh + cg) * 1024 + lane * 16), "{v174}"(tid == 256 ? fkq(h, which) : 0x7F000000),      \
          [rt] "s"(raw_rsrc(p.ws + WS_T, T_BYTES)), [rs] "s"(raw_rsrc(p.ws + WS_SY, SY_TOTAL)), [s0] "s"(p.s0 ^ TF_KQV),          \
          [lw] "s"(lw), [kh] "s"(kh), [g0] "s"(g0), [end] "s"(g0 ? NSEG : PR_G1), [x0] "s"(xn + (g0 + 1) * (SEG * XLD * 2)),                       \
          [t0] "s"(which * T_Q + kseg(0, h) - 1)                                                                                  \
        : CLB8(160, 161, 162, 163, 164, 165, 166, 167), "v168", "v169", "v170", "v171", "v173", CLB8(176, 177, 178, 179, 180, 181, 182, 183), \
          CLB8(184, 185, 186, 187, 188, 189, 190, 191), CLB8(192, 193, 194, 195, 196, 197, 198, 199),                             \
          CLB8(200, 201, 202, 203, 204, 205, 206, 207), "s86", "s87", "s88", "s90", "s91", "s92", "s93", "s94", "scc", "memory")
    if (ISV) PR_LOOP(PR_ASM_1); else PR_LOOP(PR_ASM_0);
    if (w == 0 && g0 == 0) wait_rows(p.ws, tag);
  }
#define PP_LOOP(TXT)                                                                                                              \
  asm volatile(TXT                                                                                                                \
      : "+{v[128:159]}"(acc)                                                                                                      \
      : W4_IN, "{v[240:247]}"(la), "{v[248:251]}"(vx), "{v252}"(xw), "{v253}"(xw ^ 8192),                                         \
        "{v172}"((ISV ? 2 * cg + kh : 4 * kh + cg) * 1024 + lane * 16), "{v174}"(tid == 256 ? fkq(h, which) : 0x7F000000),        \
        [rt] "s"(raw_rsrc(p.ws + WS_T, T_BYTES)), [rs] "s"(raw_rsrc(p.ws + WS_SY, SY_TOTAL)), [s0] "s"(p.s0 ^ TF_KQV),            \
        [lw] "s"(lw), [kh] "s"(kh), [g0] "n"(PR_G1), [x0] "s"(xn + (PR_G1 + 1) * (SEG * XLD * 2)),                                \
        [t0] "s"(which * T_Q + kseg(0, h) - 1)                                                                                    \
      : CLB8(160, 161, 162, 163, 164, 165, 166, 167), "v168", "v169", "v170", "v171", "v173", CLB8(176, 177, 178, 179, 180, 181, 182, 183), \
        CLB8(184, 185, 186, 187, 188, 189, 190, 191), CLB32(192), CLB8(224, 225, 226, 227, 228, 229, 230, 231),                   \
        CLB8(232, 233, 234, 235, 236, 237, 238, 239), "s86", "s87", "s90", "s91", "s92", "s93", "s94", "scc", "memory")
  if (ISV) PP_LOOP(PP_ASM_1); else PP_LOOP(PP_ASM_0);
}

enum { J_NONE = 0, J_GU = 3, J_PR = 5, J_CB = 8, J_DN = 14 };
struct BigJob { int type, panel, idx; };
struct BigPtr { const char *a, *b0, *b1; };
constexpr int G_LDA = XLD * 2, G_LDB = FF * 2;

__device__ __forceinline__ BigPtr big_ptrs(const Args& p, const BigJob& j) {
  BigPtr r;
  r.a = p.ws + WS_XN + (size_t)j.panel * 128 * G_LDA;
  r.b0 = (const char*)(p.wg + j.idx * 128); r.b1 = (const char*)(p.wu + j.idx * 128);
  return r;
}
__device__ __forceinline__ void big_publish(const Args& p, const BigJob& j) {
  int tid = threadIdx.x;
  asm volatile("" : "+v"(tid));
  if (j.type == J_GU && tid == 0)
    __hip_atomic_fetch_add((gu32*)(p.ws + WS_SY + SY_CH) + j.panel * 16, 1u, __ATOMIC_RELAXED, __HIP_MEMORY_SCOPE_AGENT);
}
typedef f32x4 Acc[2][2][4][2];
__device__ __forceinline__ u32x4 pack32(f32x4 a, f32x4 b) {
  return widen32(cvtpk(a[0], a[1]), cvtpk(a[2], a[3]), cvtpk(b[0], b[1]), cvtpk(b[2], b[3]));
}
#define st_epi(r, o, v) st_sc1(r, o, v)
__device__ __forceinline__ void big_epilogue(const Args& p, const BigJob& j, Acc& acc, char* smem) {
  int tid = threadIdx.x;
  asm volatile("" : "+v"(tid));
  const int lane = tid & 63;
  const int wid = __builtin_amdgcn_readfirstlane(tid >> 6);
  const int wr = wid >> 2, wc = wid & 3;
  {
    const __amdgpu_buffer_rsrc_t ro = mkrsrc(p.ws + WS_HF, S_LEN * 16384);
#pragma unroll
    for (int i = 0; i < 8; ++i) {
      f32x4 hh[2];
#pragma unroll
      for (int n = 0; n < 2; ++n)
#pragma unroll
        for (int jj = 0; jj < 4; ++jj) {
          const float g = acc[i >> 2][0][i & 3][n][jj], u = acc[i >> 2][1][i & 3][n][jj];
          hh[n][jj] = __builtin_amdgcn_rcpf(1.0f + __builtin_amdgcn_exp2f(g * -1.4426950408889634f)) * u;
        }
      st_epi(ro, h_off(p, j.panel + (i >> 2)) + (j.idx * 4 + wc) * 16384 + (wr * 4 + (i & 3)) * 1024 + lane * 16,
             ((u32x4){cvtpk(hh[0][0], hh[0][1]), cvtpk(hh[0][2], hh[0][3]), cvtpk(hh[1][0], hh[1][1]), cvtpk(hh[1][2], hh[1][3])}));
    }
  }
}
#define JST(n, i, v) do {} while (0)
__device__ __forceinline__ void gemm_role(Args& p, int g, char* smem) {
  const __amdgpu_buffer_rsrc_t rsy = mkrsrc(p.ws + WS_SY, SY_TOTAL);
  const int x = g & 3, k = g >> 2;
  volatile unsigned* slot = reinterpret_cast<volatile unsigned*>(smem + 163824);
  int rnd = k < 24 ? 2 : -1;
  int nj = 0; (void)nj;
  auto claim = [&]() {
    ++rnd;
    const int n = k < 24 ? 32 + k : rnd * 8 + k - 24, pn = (n * 37) >> 8;
    BigJob b;
    b.type = rnd < 4 ? J_GU : (rnd == 4 ? J_DN : (rnd == 5 && k >= 24 && k - 24 < 14 - NFUSE ? J_CB : J_NONE));
    b.panel = rnd < 4 ? 14 - 2 * pn : (rnd == 4 ? 4 * ((2 - (k >> 3)) & 3) + x : NFUSE + k - 24);
    b.idx = rnd < 4 ? x + 4 * (n - 7 * pn) : k & 7;
    return b;
  };
  if (g < 4 && threadIdx.x == 0) { LST(480 + 4 * g); LSTC(480 + 4 * g + 1); }
  if (k < 24) {
    JST(nj, 0, J_PR); JST(nj, 1, __builtin_amdgcn_s_memrealtime());
    if (k >= 16) job_proj<true>(p, g, 2, x * 8 + (k & 7), smem, rsy);
    else job_proj<false>(p, g, k >> 3, x * 8 + (k & 7), smem, rsy);
    JST(nj, 3, __builtin_amdgcn_s_memrealtime()); ++nj;
  } else {
    const unsigned t0 = (unsigned)__builtin_amdgcn_s_memrealtime();
    u32x2 tag;
#pragma unroll 1
    for (int u = 0; u < (k < 30 && LN_M ? 2 : 1); ++u) {
      const int rg = u ? 32 + (4 * (k - 24) + 1) * 4 + x : g - 96;
      tag = start_rows<0>(p, rg, 1, []() {});
      WAIT_V(0);
      LBAR();
      report_rows(p, rg, tag);
    }
    while ((unsigned)__builtin_amdgcn_s_memrealtime() - t0 < PF_T0) __builtin_amdgcn_s_sleep(8);
#pragma unroll 1
    for (int i = GPF == 2 ? 2 : 0; i < PF_N; i += GPF == 2 ? 3 : 1) {
      const int m = i % 3, o = ((i / 3) * 32 + g - 96) * 448 + (int)threadIdx.x;
      if (threadIdx.x < 448) touch_line((const char*)(m == 0 ? p.wg : (m == 1 ? p.wu : p.wd)) + (size_t)o * 128, smem + (threadIdx.x >> 6) * 256);
      WAIT_V(1);
    }
    WAIT_V(0);
    if (threadIdx.x < 64) {
      wait_rows(p.ws, tag);
      Boot bt;
      boot_ask(p.ws, bt);
      WAIT_V(0);
      *slot = boot_done(p.ws, bt, true, tag);
      while ((unsigned)__builtin_amdgcn_s_memrealtime() - t0 < GU_DELAY) __builtin_amdgcn_s_sleep(8);
    }
    LBAR();
    p.s0 = *slot >> 31;
    RDY(128 + g);
  }
  p.s0 = uni(p.s0);
  BigJob cur = claim();
  while (cur.type != J_NONE) {
    JST(nj, 0, cur.type | cur.panel << 8); JST(nj, 1, __builtin_amdgcn_s_memrealtime());
    if (cur.type != J_GU) {
      if (cur.type == J_DN) job_down(p, cur.panel, cur.idx, smem, rsy);
      else job_comb(p, cur.panel, x, rsy);
      JST(nj, 3, __builtin_amdgcn_s_memrealtime()); ++nj;
      cur = claim();
      continue;
    }
    int tid = threadIdx.x;
    asm volatile("" : "+v"(tid));
    const int lane = tid & 63;
    const int wid = __builtin_amdgcn_readfirstlane(tid >> 6);
    const int wr = wid >> 2, wc = wid & 3;
    const unsigned ra = tid >> 3, ca = 16 * ((tid & 7) ^ ((tid >> 3) & 7)), rb = tid >> 4, cb = 16 * ((tid & 15) ^ swzB((tid >> 4) & 15));
    const unsigned lw = lds_off(smem) + wid * 1024;
#define B8_SA(b, h) (((b) * 2 + (h)) * 16384)
#define B8_SB(b, h) ((4 + (b) * 2 + (h)) * 16384)
#define B8_STA(b, h, P, kt) glds2<true>((P).a + (size_t)(h) * 128 * G_LDA + (kt) * 128, 64 * G_LDA, ra * G_LDA + ca, lw + B8_SA(b, h))
#define B8_STB(b, h, P, kt) glds2(((h) ? (P).b1 : (P).b0) + (size_t)(kt) * 64 * G_LDB, 32 * G_LDB, rb * G_LDB + cb, lw + B8_SB(b, h))
#define B8_LDA(b, h) do { _Pragma("unroll") for (int m = 0; m < 4; ++m) _Pragma("unroll") for (int kk = 0; kk < 2; ++kk) \
      At[m][kk] = ldsf(smem + B8_SA(b, h) + offA(wr * 64 + m * 16 + (lane & 15), kk * 4 + (lane >> 4))); } while (0)
#define B8_LDB(dst, b, h) do { _Pragma("unroll") for (int n = 0; n < 2; ++n) _Pragma("unroll") for (int kk = 0; kk < 2; ++kk) \
      dst[n][kk] = ld_bfrag(smem + B8_SB(b, h), kk, wc * 2 + n, lane); } while (0)
#define B8_MMA(ai, bj, Bt) do { __builtin_amdgcn_s_setprio(1); _Pragma("unroll") for (int m = 0; m < 4; ++m) _Pragma("unroll") for (int n = 0; n < 2; ++n) \
      _Pragma("unroll") for (int kk = 0; kk < 2; ++kk) acc[ai][bj][m][n] = MFMA(Bt[n][kk], At[m][kk], acc[ai][bj][m][n]); __builtin_amdgcn_s_setprio(0); } while (0)
#define WAIT_L(n) asm volatile("s_waitcnt lgkmcnt(%0)" :: "n"(n) : "memory")
#define BAR() __builtin_amdgcn_s_barrier()
#define WAIT_RX(c) asm volatile("s_waitcnt vmcnt(16)\n\ts_cmp_lg_u32 %0, 0\n\ts_cbranch_scc1 .Lrx%=\n\ts_waitcnt vmcnt(8)\n.Lrx%=:" :: "s"(c) : "memory", "scc")
    Acc acc;
    bf16x8 At[4][2], B0[2][2], B1[2][2];
    BigPtr cp = big_ptrs(p, cur);
    BigJob pend; pend.type = J_NONE; pend.panel = 0; pend.idx = 0;
    LBAR();
    B8_STB(0, 0, cp, 0); B8_STB(0, 1, cp, 0); B8_STA(0, 0, cp, 0); B8_STA(0, 1, cp, 0);
    if (wr == 1) BAR();
    WAIT_V(2); BAR();
    B8_STB(1, 0, cp, 1); B8_STA(1, 0, cp, 1); B8_STB(1, 1, cp, 1);
    WAIT_V(6); BAR();
    BigJob nx = pend;
    for (;;) {
      bool chain = false;
      BigPtr np = cp;
      constexpr int nt = DM / 64;
#pragma unroll
      for (int i = 0; i < 32; ++i) acc[i >> 4][(i >> 3) & 1][(i >> 1) & 3][i & 1] = (f32x4){0.f, 0.f, 0.f, 0.f};
#pragma unroll 1
      for (int t = 0; t < nt; t += 2) {
        const bool last = t == nt - 2;
        if (last) {
          nx = claim();
          chain = nx.type == J_GU;
          if (chain) np = big_ptrs(p, nx);
        }
        const BigPtr sp = np;
        const int t2 = last ? 0 : t + 2;
        if (t == 2) big_publish(p, pend);
        const int rx = __builtin_amdgcn_readfirstlane(t == 0 && pend.type != J_NONE);
        B8_LDB(B0, 0, 0); B8_LDB(B1, 0, 1); SCHED(); B8_LDA(0, 0); B8_STA(1, 1, cp, t + 1);
        WAIT_RX(rx);
        WAIT_L(0); BAR(); B8_MMA(0, 0, B0); B8_MMA(0, 1, B1); BAR(); SCHED();
        B8_LDA(0, 1); B8_STB(0, 0, sp, t2); B8_STB(0, 1, sp, t2); B8_STA(0, 0, sp, t2);
        WAIT_RX(rx);
        WAIT_L(0); BAR(); B8_MMA(1, 0, B0); B8_MMA(1, 1, B1); BAR(); SCHED();
        B8_LDB(B0, 1, 0); B8_LDB(B1, 1, 1); SCHED(); B8_LDA(1, 0); B8_STA(0, 1, sp, t2);
        WAIT_V(8); WAIT_L(0); BAR(); B8_MMA(0, 0, B0); B8_MMA(0, 1, B1); BAR(); SCHED();
        B8_LDA(1, 1); B8_STB(1, 0, sp, t2 + 1); B8_STB(1, 1, sp, t2 + 1); B8_STA(1, 0, sp, t2 + 1);
        WAIT_V(8); WAIT_L(0); BAR(); B8_MMA(1, 0, B0); B8_MMA(1, 1, B1); BAR(); SCHED();
      }
      if (wr == 0) BAR();
      JST(nj, 2, __builtin_amdgcn_s_memrealtime());
      big_epilogue(p, cur, acc, smem);
      if (!chain) break;
      JST(nj, 3, __builtin_amdgcn_s_memrealtime()); ++nj;
      pend = cur; cur = nx; cp = np;
      if (wr == 1) BAR();
      JST(nj, 0, cur.type | cur.panel << 8); JST(nj, 1, __builtin_amdgcn_s_memrealtime());
    }
    WAIT_V(0);
    BAR();
    asm volatile(".Lcode_end:");
    big_publish(p, cur);
    JST(nj, 3, __builtin_amdgcn_s_memrealtime()); ++nj;
    cur = nx;
  }
  if (threadIdx.x == 0) { LST(128 + g); }
  if (g < 4 && threadIdx.x == 0) { LST(480 + 4 * g + 2); LSTC(480 + 4 * g + 3); }
}

__device__ __forceinline__ unsigned now_q() { return (unsigned)__builtin_amdgcn_s_memrealtime() * 4u; }
__device__ __forceinline__ void pace_until(unsigned t) {
#pragma unroll 1
  for (int i = 0; i < 256; ++i) {
    const int d = (int)(t - now_q());
    if (d <= 0) break;
    if (d > 32) __builtin_amdgcn_s_sleep(1);
  }
}
#define CLB_E "s76", "s77", "s78", "s79", "s80", "s81", "s82", "s83", "s84", "s85", "s86", "s87", "s88", "s89"
#define EDGE_ASM \
  "s_memtime s[76:77]\n\t" \
  "s_memrealtime s[78:79]\n\t" \
  "s_nop 3\n\t" \
  "s_memrealtime s[80:81]\n\t" \
  "s_nop 3\n\t" \
  "s_memrealtime s[82:83]\n\t" \
  "s_nop 3\n\t" \
  "s_memrealtime s[84:85]\n\t" \
  "s_nop 3\n\t" \
  "s_memrealtime s[86:87]\n\t" \
  "s_nop 3\n\t" \
  "s_memrealtime s[88:89]\n\t" \
  "s_mov_b32 s93, 0\n\t" \
  "s_waitcnt lgkmcnt(0)\n\t" \
  "s_cmp_eq_u32 s80, s78\n\t" \
  "s_addc_u32 s93, s93, 0\n\t" \
  "s_cmp_eq_u32 s82, s78\n\t" \
  "s_addc_u32 s93, s93, 0\n\t" \
  "s_cmp_eq_u32 s84, s78\n\t" \
  "s_addc_u32 s93, s93, 0\n\t" \
  "s_cmp_eq_u32 s86, s78\n\t" \
  "s_addc_u32 s93, s93, 0\n\t" \
  "s_cmp_eq_u32 s88, s78\n\t" \
  "s_addc_u32 s93, s93, 0\n\t" \
  "s_mul_i32 s93, s93, 21\n\t" \
  "s_add_u32 s76, s76, 15\n\t" \
  "s_add_u32 %[kc], s76, s93\n\t" \
  "s_lshl2_add_u32 %[kq], s78, 16\n\t"
__device__ __forceinline__ unsigned time_of(unsigned cyc) {
  unsigned c, q;
  asm volatile(EDGE_ASM : [kc] "=&s"(c), [kq] "=&s"(q) :: CLB_E, "s93", "scc");
  return q - (((c - cyc) * 11900u) >> 16);
}
#define DS_RED 20
#define DS_MEM 18
#define LEAD_DN 2
#define LEAD_UP 12
__device__ __forceinline__ u32x2 ld2_sc1(__amdgpu_buffer_rsrc_t rs, int off, int soff = 0) {
  return __builtin_amdgcn_raw_buffer_load_b64(rs, off, soff, 16);
}
__device__ __forceinline__ void st2_sc1(__amdgpu_buffer_rsrc_t rs, int off, u32x2 v) {
  __builtin_amdgcn_raw_buffer_store_b64(v, rs, off, 0, 16);
}
__device__ __forceinline__ int row_max(int v) {
  v = max(v, __builtin_amdgcn_update_dpp(0, v, 0x128, 0xf, 0xf, false));
  v = max(v, __builtin_amdgcn_update_dpp(0, v, 0x124, 0xf, 0xf, false));
  v = max(v, __builtin_amdgcn_update_dpp(0, v, 0x122, 0xf, 0xf, false));
  v = max(v, __builtin_amdgcn_update_dpp(0, v, 0x121, 0xf, 0xf, false));
  return v;
}
__device__ __forceinline__ int wave_max(int v) {
  v = row_max(v);
  return max(max(__builtin_amdgcn_readlane(v, 0), __builtin_amdgcn_readlane(v, 16)),
             max(__builtin_amdgcn_readlane(v, 32), __builtin_amdgcn_readlane(v, 48)));
}
__device__ __forceinline__ int track(int est, int x, int up, int dn, int out) {
  const int d = x - est;
  return est + (d > out ? 4 : max(min(d, up), -dn));
}
#define WSL 12
#define PLAN_SLEEP 2
__device__ __forceinline__ unsigned latest(unsigned t) {
  const unsigned t0 = __builtin_amdgcn_readfirstlane(t);
  return t0 + wave_max((int)(t - t0));
}
#define RL00 265
#define RL0 306
__device__ __forceinline__ void watch(__amdgpu_buffer_rsrc_t rsy, int s, int lane, unsigned t0, const unsigned* tgt_, gu32* abortw, int hs) {
  pace_until(t0);
  const int rl = (int)__builtin_amdgcn_raw_buffer_load_b32(rsy, lane * 128, SY_BR + hs, 16);
  u32x2 ra;
  for (unsigned spins = 0;;) {
    asm volatile("" ::: "memory");
    ra = ld2_sc1(rsy, (lane & 31) * 128, SY_BA + hs);
    const bool all = __all((int)ra[0] >= s);
    SMP(4, s, spins);
    if (all) break;
    if (spins < 3) __builtin_amdgcn_s_sleep(PLAN_SLEEP); else __builtin_amdgcn_s_sleep(WSL);
    if (spin_fail(spins, abortw)) break;
  }
  const unsigned t = latest(ra[0] == (unsigned)s ? ra[1] : t0) + wave_max(rl < 0 ? (s ? RL0 : RL00) : rl) + (DS_RED + DS_MEM);
  lds_vu_ptr tgt = lds_at(tgt_);
  if (lane < 2) tgt[lane] = lane ? t : s + 1;
  SMP(0, s, (int)(t - t0)); SMP(1, s, (int)(t - now_q()));
}
__device__ __forceinline__ unsigned ld_flag(__amdgpu_buffer_rsrc_t rs, int off) {
  return __builtin_amdgcn_raw_buffer_load_b32(rs, off, 0, 16);
}

constexpr unsigned long long QK_MAP[8] = {0xbaf9876d543210ecull, 0xa98f76d543210bceull, 0x98f76d543210ecbaull, 0x876fd543210bcea9ull, 0x76fd543210ecba98ull, 0x65df43210bcea987ull, 0x5df43210ecba9876ull, 0xd43f210bcea98765ull};
#define QK_ASM_0 \
  "s_waitcnt lgkmcnt(0)\n\t" \
  "v_subrev_u32 v244, 0x8000, v250\n\t" \
  "ds_read_b128 v[216:219], v248 offset:32768\n\t" \
  "ds_read_b128 v[220:223], v248 offset:33792\n\t" \
  "ds_read_b128 v[224:227], v248 offset:34816\n\t" \
  "ds_read_b128 v[228:231], v248 offset:35840\n\t" \
  "ds_read_b128 v[208:211], v244 offset:0\n\t" \
  "ds_read_b128 v[212:215], v244 offset:1024\n\t" \
  "ds_read_b128 v[236:239], v244 offset:2048\n\t" \
  "ds_read_b128 v[240:243], v244 offset:3072\n\t" \
  "v_mov_b64 v[192:193], 0\n\t" \
  "v_mov_b64 v[194:195], 0\n\t" \
  "v_mov_b64 v[196:197], 0\n\t" \
  "v_mov_b64 v[198:199], 0\n\t" \
  "s_movk_i32 s92, 0x2000\n\t" \
  ".Lwt%=:\n\t" \
  "ds_read_b64 v[200:201], v248 offset:30720\n\t" \
  "s_memrealtime s[90:91]\n\t" \
  "s_sub_u32 s92, s92, 1\n\t" \
  "s_waitcnt lgkmcnt(0)\n\t" \
  "v_readfirstlane_b32 s93, v200\n\t" \
  "v_readfirstlane_b32 %[tgt], v201\n\t" \
  "s_cbranch_scc1 .Lgo%=\n\t" \
  "s_cmp_eq_u32 s93, %[step]\n\t" \
  "s_cbranch_scc1 .Lwx%=\n\t" \
  "s_sleep 1\n\t" \
  "s_branch .Lwt%=\n\t" \
  ".Lwx%=:\n\t" \
  "s_lshl_b32 s93, s90, 2\n\t" \
  "s_sub_i32 s93, %[tgt], s93\n\t" \
  "s_cmp_le_i32 s93, 0\n\t" \
  "s_cbranch_scc1 .Lgo%=\n\t" \
  "s_min_i32 s93, s93, 8000\n\t" \
  "s_mov_b32 s94, s93\n\t" \
  ".Lsl%=:\n\t" \
  "s_sub_i32 s94, s94, 18\n\t" \
  "s_cmp_lt_i32 s94, 30\n\t" \
  "s_cbranch_scc1 .Lsx%=\n\t" \
  "s_sleep 1\n\t" \
  "s_branch .Lsl%=\n\t" \
  ".Lsx%=:\n\t" \
  "s_sub_i32 s93, s93, 12\n\t" \
  "s_max_i32 s93, s93, 4\n\t" \
  "s_lshr_b32 s93, s93, 2\n\t" \
  "s_add_u32 s93, s93, s90\n\t" \
  "s_memrealtime s[90:91]\n\t" \
  ".Lpace%=:\n\t" \
  "s_memrealtime s[94:95]\n\t" \
  "s_waitcnt lgkmcnt(1)\n\t" \
  "s_sub_i32 s92, s90, s93\n\t" \
  "s_cmp_ge_i32 s92, 0\n\t" \
  "s_cbranch_scc1 .Le%=\n\t" \
  "s_memrealtime s[90:91]\n\t" \
  "s_waitcnt lgkmcnt(1)\n\t" \
  "s_sub_i32 s92, s94, s93\n\t" \
  "s_cmp_lt_i32 s92, 0\n\t" \
  "s_cbranch_scc1 .Lpace%=\n\t" \
  "s_nop 4\n\t" \
  ".Le%=:\n\t" \
  "s_add_u32 s92, s92, s93\n\t" \
  "s_lshl_b32 s92, s92, 2\n\t" \
  "s_sub_i32 s92, %[tgt], s92\n\t" \
  "s_max_i32 s92, s92, 0\n\t" \
  "s_bitcmp0_b32 s92, 3\n\t" \
  "s_cbranch_scc1 .Lf3%=\n\t" \
  "s_nop 15\n\t" \
  ".Lf3%=:\n\t" \
  "s_bitcmp0_b32 s92, 2\n\t" \
  "s_cbranch_scc1 .Lf2%=\n\t" \
  "s_nop 10\n\t" \
  ".Lf2%=:\n\t" \
  ".Lgo%=:\n\t" \
  "s_mov_b32 s92, 0\n\t" \
  ".Lpoll%=:\n\t" \
  "buffer_load_dwordx4 v[200:203], v248, %[rs], %[som] offen sc1\n\t" \
  "buffer_load_dwordx4 v[204:207], v248, %[rs], %[som2] offen sc1\n\t" \
  "s_waitcnt vmcnt(1) lgkmcnt(0)\n\t" \
  "ds_write_b128 v249, v[200:203]\n\t" \
  "v_smfmac_f32_16x16x64_bf16 v[192:195], v[200:203], v[208:215], v251\n\t" \
  "v_max3_u32 v244, v200, v201, v202\n\t" \
  "v_smfmac_f32_16x16x64_bf16 v[196:199], v[200:203], v[236:243], v251\n\t" \
  "s_waitcnt vmcnt(0)\n\t" \
  "ds_write_b128 v249, v[204:207] offset:8192\n\t" \
  "v_smfmac_f32_16x16x64_bf16 v[192:195], v[204:207], v[216:223], v251\n\t" \
  "v_max3_u32 v244, v244, v203, v204\n\t" \
  "v_max3_u32 v244, v244, v205, v206\n\t" \
  "v_smfmac_f32_16x16x64_bf16 v[196:199], v[204:207], v[224:231], v251\n\t" \
  "v_max_u32 v244, v244, v207\n\t" \
  "v_cmp_le_u32 vcc, %[sent], v244\n\t" \
  "s_cbranch_vccnz .Lslow%=\n\t" \
  ".Lok%=:\n\t" \
  "ds_read_b128 v[216:219], v250 offset:32768\n\t" \
  "ds_read_b128 v[220:223], v250 offset:33792\n\t" \
  "ds_read_b128 v[224:227], v250 offset:34816\n\t" \
  "ds_read_b128 v[228:231], v250 offset:35840\n\t" \
  "s_waitcnt lgkmcnt(4)\n\t" \
  "s_barrier\n\t" \
  "ds_read_b128 v[208:211], v248 offset:1024\n\t" \
  "ds_read_b128 v[212:215], v248 offset:2048\n\t" \
  "ds_read_b128 v[236:239], v248 offset:3072\n\t" \
  "ds_read_b128 v[240:243], v248 offset:4096\n\t" \
  "s_waitcnt lgkmcnt(3)\n\t" \
  "ds_read_b128 v[244:247], v248 offset:13312\n\t" \
  "v_smfmac_f32_16x16x64_bf16 v[192:195], v[208:211], v[216:223], v251\n\t" \
  "ds_read_b128 v[216:219], v250 offset:0\n\t" \
  "v_smfmac_f32_16x16x64_bf16 v[196:199], v[208:211], v[224:231], v251\n\t" \
  "s_waitcnt lgkmcnt(4)\n\t" \
  "ds_read_b128 v[220:223], v250 offset:1024\n\t" \
  "v_smfmac_f32_16x16x64_bf16 v[192:195], v[212:215], v[0:7], v251\n\t" \
  "ds_read_b128 v[224:227], v250 offset:2048\n\t" \
  "v_smfmac_f32_16x16x64_bf16 v[196:199], v[212:215], v[8:15], v251\n\t" \
  "s_waitcnt lgkmcnt(5)\n\t" \
  "ds_read_b128 v[228:231], v250 offset:3072\n\t" \
  "v_smfmac_f32_16x16x64_bf16 v[192:195], v[236:239], v[16:23], v251\n\t" \
  "ds_read_b128 v[200:203], v248 offset:5120\n\t" \
  "v_smfmac_f32_16x16x64_bf16 v[196:199], v[236:239], v[24:31], v251\n\t" \
  "s_waitcnt lgkmcnt(6)\n\t" \
  "ds_read_b128 v[204:207], v248 offset:6144\n\t" \
  "v_smfmac_f32_16x16x64_bf16 v[192:195], v[240:243], v[32:39], v251\n\t" \
  "ds_read_b128 v[208:211], v248 offset:7168\n\t" \
  "v_smfmac_f32_16x16x64_bf16 v[196:199], v[240:243], v[40:47], v251\n\t" \
  "s_waitcnt lgkmcnt(5)\n\t" \
  "ds_read_b128 v[212:215], v248 offset:9216\n\t" \
  "v_smfmac_f32_16x16x64_bf16 v[192:195], v[244:247], v[216:223], v251\n\t" \
  "s_waitcnt lgkmcnt(4)\n\t" \
  "v_smfmac_f32_16x16x64_bf16 v[196:199], v[244:247], v[224:231], v251\n\t" \
  "s_waitcnt lgkmcnt(3)\n\t" \
  "ds_read_b128 v[236:239], v248 offset:10240\n\t" \
  "v_smfmac_f32_16x16x64_bf16 v[192:195], v[200:203], v[48:55], v251\n\t" \
  "ds_read_b128 v[240:243], v248 offset:11264\n\t" \
  "v_smfmac_f32_16x16x64_bf16 v[196:199], v[200:203], v[56:63], v251\n\t" \
  "s_waitcnt lgkmcnt(4)\n\t" \
  "v_smfmac_f32_16x16x64_bf16 v[192:195], v[204:207], v[64:71], v251\n\t" \
  "ds_read_b128 v[244:247], v248 offset:12288\n\t" \
  "v_smfmac_f32_16x16x64_bf16 v[196:199], v[204:207], v[72:79], v251\n\t" \
  "s_waitcnt lgkmcnt(4)\n\t" \
  "v_smfmac_f32_16x16x64_bf16 v[192:195], v[208:211], v[80:87], v251\n\t" \
  "ds_read_b128 v[200:203], v248 offset:14336\n\t" \
  "v_smfmac_f32_16x16x64_bf16 v[196:199], v[208:211], v[88:95], v251\n\t" \
  "s_waitcnt lgkmcnt(4)\n\t" \
  "v_smfmac_f32_16x16x64_bf16 v[192:195], v[212:215], v[96:103], v251\n\t" \
  "ds_read_b128 v[204:207], v248 offset:15360\n\t" \
  "v_smfmac_f32_16x16x64_bf16 v[196:199], v[212:215], v[104:111], v251\n\t" \
  "s_waitcnt lgkmcnt(3)\n\t" \
  "v_smfmac_f32_16x16x64_bf16 v[192:195], v[236:239], v[112:119], v251\n\t" \
  "v_smfmac_f32_16x16x64_bf16 v[196:199], v[236:239], v[120:127], v251\n\t" \
  "v_smfmac_f32_16x16x64_bf16 v[192:195], v[240:243], v[128:135], v251\n\t" \
  "v_smfmac_f32_16x16x64_bf16 v[196:199], v[240:243], v[136:143], v251\n\t" \
  "s_waitcnt lgkmcnt(1)\n\t" \
  "v_smfmac_f32_16x16x64_bf16 v[192:195], v[244:247], v[144:151], v251\n\t" \
  "v_smfmac_f32_16x16x64_bf16 v[196:199], v[244:247], v[152:159], v251\n\t" \
  "v_smfmac_f32_16x16x64_bf16 v[192:195], v[200:203], v[160:167], v251\n\t" \
  "v_smfmac_f32_16x16x64_bf16 v[196:199], v[200:203], v[168:175], v251\n\t" \
  "s_waitcnt lgkmcnt(0)\n\t" \
  "v_smfmac_f32_16x16x64_bf16 v[192:195], v[204:207], v[176:183], v251\n\t" \
  "v_smfmac_f32_16x16x64_bf16 v[196:199], v[204:207], v[184:191], v251\n\t" \
  "ds_read_b128 v[200:203], v248 offset:16384\n\t" \
  "ds_read_b128 v[204:207], v248 offset:20480\n\t" \
  "v_mov_b32 v250, v232\n\t" \
  "v_mov_b32 v249, v233\n\t" \
  "s_nop 2\n\t" \
  "v_add_f32 v192, v192, v194\n\t" \
  "v_add_f32 v193, v193, v195\n\t" \
  "v_add_f32 v196, v196, v198\n\t" \
  "v_add_f32 v197, v197, v199\n\t" \
  "v_cvt_pk_bf16_f32 v192, v192, v193\n\t" \
  "v_cvt_pk_bf16_f32 v196, v196, v197\n\t" \
  "ds_write2_b32 v252, v192, v196 offset1:64\n\t" \
  "v_mov_b32 v251, v234\n\t" \
  "ds_read_b64_tr_b16 v[232:233], v250 offset:0\n\t" \
  "ds_read_b64_tr_b16 v[234:235], v250 offset:64\n\t" \
  "v_mov_b32 v212, %[step]\n\t" \
  "s_mov_b64 exec, 1\n\t" \
  "ds_write_b32 v248, v212 offset:28672\n\t" \
  "s_mov_b64 exec, -1\n\t" \
  "buffer_load_dwordx4 v[192:195], v248, %[rt], %[svt] offen offset:0 sc1\n\t" \
  "buffer_load_dwordx4 v[196:199], v248, %[rt], %[svt] offen offset:1024 sc1\n\t" \
  "s_waitcnt lgkmcnt(1)\n\t" \
  "v_mfma_f32_16x16x32_bf16 v[200:203], v[200:203], v[232:235], 0\n\t" \
  "v_mfma_f32_16x16x32_bf16 v[204:207], v[204:207], v[232:235], 0\n\t" \
  "s_nop 7\n\t" \
  "v_cndmask_b32 v216, v200, v204, %[nh]\n\t" \
  "v_cndmask_b32 v217, v201, v205, %[nh]\n\t" \
  "v_cndmask_b32 v218, v202, v206, %[nh]\n\t" \
  "v_cndmask_b32 v219, v203, v207, %[nh]\n\t" \
  "ds_write_b128 v248, v[216:219] offset:16384\n\t" \
  "buffer_load_dword v252, v249, %[rs], %[sv] offen offset:0 sc1\n\t" \
  "buffer_load_dword v253, v249, %[rs], %[sv] offen offset:256 sc1\n\t" \
  "ds_read_b32 v212, v248 offset:28672\n\t" \
  "ds_read_b64_tr_b16 v[236:237], v250 offset:512\n\t" \
  "ds_read_b64_tr_b16 v[238:239], v250 offset:576\n\t" \
  "ds_read_b64_tr_b16 v[240:241], v250 offset:1024\n\t" \
  "ds_read_b64_tr_b16 v[242:243], v250 offset:1088\n\t" \
  "ds_read_b64_tr_b16 v[244:245], v250 offset:1536\n\t" \
  "ds_read_b64_tr_b16 v[246:247], v250 offset:1600\n\t" \
  "s_setprio 3\n\t" \
  "s_waitcnt lgkmcnt(0)\n\t" \
  "v_cmp_ne_u32 vcc, %[step], v212\n\t" \
  "s_barrier\n\t" \
  "s_and_b32 s93, vcc_lo, 0xf\n\t" \
  "s_cbranch_scc1 .Lre%=\n\t" \
  ".Lrb%=:\n\t" \
  "ds_read_b64_tr_b16 v[200:201], v250 offset:2048\n\t" \
  "ds_read_b64_tr_b16 v[202:203], v250 offset:2112\n\t" \
  "ds_read_b64_tr_b16 v[204:205], v250 offset:2560\n\t" \
  "ds_read_b64_tr_b16 v[206:207], v250 offset:2624\n\t" \
  "ds_read_b128 v[220:223], v248 offset:17408\n\t" \
  "ds_read_b128 v[224:227], v248 offset:18432\n\t" \
  "ds_read_b128 v[228:231], v248 offset:19456\n\t" \
  "ds_read_b64_tr_b16 v[208:209], v250 offset:3072\n\t" \
  "ds_read_b64_tr_b16 v[210:211], v250 offset:3136\n\t" \
  "ds_read_b64_tr_b16 v[212:213], v250 offset:3584\n\t" \
  "ds_read_b64_tr_b16 v[214:215], v250 offset:3648\n\t" \
  "s_waitcnt lgkmcnt(9)\n\t" \
  "v_mfma_f32_16x16x32_bf16 v[200:203], v[200:203], v[232:235], 0\n\t" \
  "s_waitcnt lgkmcnt(7)\n\t" \
  "v_mfma_f32_16x16x32_bf16 v[200:203], v[204:207], v[236:239], v[200:203]\n\t" \
  "s_waitcnt lgkmcnt(6)\n\t" \
  "v_pk_add_f32 v[216:217], v[216:217], v[220:221]\n\t" \
  "v_pk_add_f32 v[218:219], v[218:219], v[222:223]\n\t" \
  "s_waitcnt lgkmcnt(4)\n\t" \
  "v_pk_add_f32 v[224:225], v[224:225], v[228:229]\n\t" \
  "v_pk_add_f32 v[226:227], v[226:227], v[230:231]\n\t" \
  "v_pk_add_f32 v[216:217], v[216:217], v[224:225]\n\t" \
  "v_pk_add_f32 v[218:219], v[218:219], v[226:227]\n\t" \
  "s_waitcnt lgkmcnt(2)\n\t" \
  "v_mfma_f32_16x16x32_bf16 v[200:203], v[208:211], v[240:243], v[200:203]\n\t" \
  "v_max3_f32 v224, v216, v217, v218\n\t" \
  "v_max_f32 v224, v224, v219\n\t" \
  "s_waitcnt lgkmcnt(0)\n\t" \
  "v_mfma_f32_16x16x32_bf16 v[200:203], v[212:215], v[244:247], v[200:203]\n\t" \
  "v_max_f32_dpp v225, v224, v224 row_ror:8 row_mask:0xf bank_mask:0xf\n\t" \
  "v_max_f32_dpp v226, v224, v224 row_ror:8 row_mask:0xf bank_mask:0xf\n\t" \
  "s_nop 1\n\t" \
  "v_permlane32_swap_b32 v225, v226\n\t" \
  "v_max_f32 v231, v225, v226\n\t" \
  "v_mov_b32 v220, 0x3f803f80\n\t" \
  "v_mov_b32 v221, 0x3f803f80\n\t" \
  "v_mov_b64 v[222:223], v[220:221]\n\t" \
  "v_mov_b32 v233, 0\n\t" \
  "v_mov_b32 v234, 0\n\t" \
  "v_max3_f32 v204, v200, v201, v231\n\t" \
  "v_max3_f32 v205, v204, v202, v203\n\t" \
  "v_max3_f32 v206, v204, v202, v203\n\t" \
  "v_cndmask_b32 v200, v200, v202, %[up]\n\t" \
  "v_cndmask_b32 v201, v201, v203, %[up]\n\t" \
  "v_permlane16_swap_b32 v205, v206\n\t" \
  "v_max_f32 v204, v205, v206\n\t" \
  "v_mul_f32 v204, %[negc], v204\n\t" \
  "v_fma_f32 v200, v200, %[c], v204\n\t" \
  "v_fma_f32 v201, v201, %[c], v204\n\t" \
  "v_exp_f32 v200, v200\n\t" \
  "v_fma_f32 v216, v216, %[c], v204\n\t" \
  "v_exp_f32 v201, v201\n\t" \
  "v_fma_f32 v217, v217, %[c], v204\n\t" \
  "v_exp_f32 v216, v216\n\t" \
  "v_fma_f32 v218, v218, %[c], v204\n\t" \
  "v_cvt_pk_bf16_f32 v232, v200, v201\n\t" \
  "v_cvt_pk_bf16_f32 v235, v200, v201\n\t" \
  "v_exp_f32 v217, v217\n\t" \
  "v_fma_f32 v219, v219, %[c], v204\n\t" \
  "v_mfma_f32_16x16x16_bf16 v[212:215], v[220:221], v[232:233], 0\n\t" \
  "v_exp_f32 v218, v218\n\t" \
  "s_waitcnt vmcnt(0)\n\t" \
  "v_mfma_f32_16x16x16_bf16 v[224:227], v[252:253], v[232:233], 0\n\t" \
  "v_cvt_pk_bf16_f32 v210, v216, v217\n\t" \
  "v_cvt_pk_bf16_f32 v208, v216, v217\n\t" \
  "v_exp_f32 v219, v219\n\t" \
  "v_mfma_f32_16x16x16_bf16 v[228:231], v[252:253], v[234:235], 0\n\t" \
  "v_cvt_pk_bf16_f32 v211, v218, v219\n\t" \
  "v_cvt_pk_bf16_f32 v209, v218, v219\n\t" \
  "v_mov_b32_dpp v208, v210 row_ror:8 row_mask:0xf bank_mask:0xc\n\t" \
  "v_mov_b32_dpp v210, v210 row_ror:8 row_mask:0xf bank_mask:0x3\n\t" \
  "v_mov_b32_dpp v209, v211 row_ror:8 row_mask:0xf bank_mask:0xc\n\t" \
  "v_mov_b32_dpp v211, v211 row_ror:8 row_mask:0xf bank_mask:0x3\n\t" \
  "v_max_u32 v204, v252, v253\n\t" \
  "v_cmp_le_u32 vcc, %[sent], v204\n\t" \
  "v_mfma_f32_16x16x32_bf16 v[212:215], v[220:223], v[208:211], v[212:215]\n\t" \
  ".Lpv%=:\n\t" \
  "v_mfma_f32_16x16x32_bf16 v[224:227], v[192:195], v[208:211], v[224:227]\n\t" \
  "v_mfma_f32_16x16x32_bf16 v[228:231], v[196:199], v[208:211], v[228:231]\n\t" \
  "s_cbranch_vccnz .Lvs%=\n\t" \
  ".Lvok%=:\n\t" \
  "s_nop 4\n\t" \
  "v_rcp_f32 v204, v212\n\t" \
  "v_cndmask_b32 v224, v224, v226, %[nh]\n\t" \
  "v_cndmask_b32 v225, v225, v227, %[nh]\n\t" \
  "v_cndmask_b32 v228, v228, v230, %[nh]\n\t" \
  "v_cndmask_b32 v229, v229, v231, %[nh]\n\t" \
  "v_pk_mul_f32 v[224:225], v[224:225], v[204:205] op_sel_hi:[1,0]\n\t" \
  "v_pk_mul_f32 v[228:229], v[228:229], v[204:205] op_sel_hi:[1,0]\n\t" \
  "v_cvt_pk_bf16_f32 v224, v224, v225\n\t" \
  "v_cvt_pk_bf16_f32 v225, v228, v229\n\t" \
  "buffer_store_dwordx2 v[224:225], v251, %[rs], %[so] offen sc1\n\t" \
  "s_memtime s[90:91]\n\t" \
  "s_setprio 0\n\t" \
  "s_waitcnt lgkmcnt(0)\n\t" \
  "s_mov_b32 %[tout], s90\n\t" \
  "s_mov_b32 %[spins], s92\n\t" \
  "s_branch .Lend%=\n\t" \
  ".Lre%=:\n\t" \
  "ds_read_b64_tr_b16 v[236:237], v250 offset:512\n\t" \
  "ds_read_b64_tr_b16 v[238:239], v250 offset:576\n\t" \
  "ds_read_b64_tr_b16 v[240:241], v250 offset:1024\n\t" \
  "ds_read_b64_tr_b16 v[242:243], v250 offset:1088\n\t" \
  "ds_read_b64_tr_b16 v[244:245], v250 offset:1536\n\t" \
  "ds_read_b64_tr_b16 v[246:247], v250 offset:1600\n\t" \
  "s_waitcnt lgkmcnt(0)\n\t" \
  "s_branch .Lrb%=\n\t" \
  ".Lvs%=:\n\t" \
  "v_cmp_eq_u32 vcc, %[sent], v252\n\t" \
  "v_cmp_eq_u32 s[94:95], %[sent], v253\n\t" \
  "s_or_b64 s[94:95], s[94:95], vcc\n\t" \
  "s_cbranch_scc0 .Lvok%=\n\t" \
  "s_add_u32 s92, s92, 1\n\t" \
  "s_sleep 2\n\t" \
  "s_and_b32 s93, s92, 63\n\t" \
  "s_cbranch_scc1 .Lvr%=\n\t" \
  "buffer_load_dword v204, off, %[rs], %[sab] sc1\n\t" \
  "s_waitcnt vmcnt(0)\n\t" \
  "v_readfirstlane_b32 s93, v204\n\t" \
  "s_nop 3\n\t" \
  "s_cmp_eq_u32 s93, 1\n\t" \
  "s_cbranch_scc1 .Lvok%=\n\t" \
  "s_cmp_lt_u32 s92, 0x80000\n\t" \
  "s_cbranch_scc1 .Lvr%=\n\t" \
  "v_mov_b32 v204, 1\n\t" \
  "buffer_store_dword v204, off, %[rs], %[sab] sc1\n\t" \
  "s_branch .Lvok%=\n\t" \
  ".Lvr%=:\n\t" \
  "buffer_load_dword v252, v249, %[rs], %[sv] offen offset:0 sc1\n\t" \
  "buffer_load_dword v253, v249, %[rs], %[sv] offen offset:256 sc1\n\t" \
  "s_waitcnt vmcnt(0)\n\t" \
  "v_mfma_f32_16x16x16_bf16 v[224:227], v[252:253], v[232:233], 0\n\t" \
  "v_mfma_f32_16x16x16_bf16 v[228:231], v[252:253], v[234:235], 0\n\t" \
  "v_max_u32 v204, v252, v253\n\t" \
  "v_cmp_le_u32 vcc, %[sent], v204\n\t" \
  "s_nop 4\n\t" \
  "s_branch .Lpv%=\n\t" \
  "s_branch .Lend%=\n\t" \
  ".Lslow%=:\n\t" \
  "s_mov_b64 s[94:95], 0\n\t" \
  "v_cmp_eq_u32 vcc, %[sent], v200\n\t" \
  "s_or_b64 s[94:95], s[94:95], vcc\n\t" \
  "v_cmp_eq_u32 vcc, %[sent], v201\n\t" \
  "s_or_b64 s[94:95], s[94:95], vcc\n\t" \
  "v_cmp_eq_u32 vcc, %[sent], v202\n\t" \
  "s_or_b64 s[94:95], s[94:95], vcc\n\t" \
  "v_cmp_eq_u32 vcc, %[sent], v203\n\t" \
  "s_or_b64 s[94:95], s[94:95], vcc\n\t" \
  "v_cmp_eq_u32 vcc, %[sent], v204\n\t" \
  "s_or_b64 s[94:95], s[94:95], vcc\n\t" \
  "v_cmp_eq_u32 vcc, %[sent], v205\n\t" \
  "s_or_b64 s[94:95], s[94:95], vcc\n\t" \
  "v_cmp_eq_u32 vcc, %[sent], v206\n\t" \
  "s_or_b64 s[94:95], s[94:95], vcc\n\t" \
  "v_cmp_eq_u32 vcc, %[sent], v207\n\t" \
  "s_or_b64 s[94:95], s[94:95], vcc\n\t" \
  "s_cmp_eq_u64 s[94:95], 0\n\t" \
  "s_cbranch_scc1 .Lok%=\n\t" \
  "v_mov_b64 v[192:193], 0\n\t" \
  "v_mov_b64 v[194:195], 0\n\t" \
  "v_mov_b64 v[196:197], 0\n\t" \
  "v_mov_b64 v[198:199], 0\n\t" \
  "s_add_u32 s92, s92, 1\n\t" \
  "s_sleep 6\n\t" \
  "s_cmp_eq_u32 s92, 1\n\t" \
  "s_cbranch_scc1 .Lpoll%=\n\t" \
  "s_sleep 1\n\t" \
  "s_and_b32 s93, s92, 63\n\t" \
  "s_cmp_lg_u32 s93, 0\n\t" \
  "s_cbranch_scc1 .Lpoll%=\n\t" \
  "v_mov_b32 v245, 0\n\t" \
  "buffer_load_dword v244, v245, %[rs], %[sab] offen sc1\n\t" \
  "s_waitcnt vmcnt(0)\n\t" \
  "v_readfirstlane_b32 s93, v244\n\t" \
  "s_nop 3\n\t" \
  "s_cmp_eq_u32 s93, 1\n\t" \
  "s_cbranch_scc1 .Lok%=\n\t" \
  "s_cmp_lt_u32 s92, 0x80000\n\t" \
  "s_cbranch_scc1 .Lpoll%=\n\t" \
  "v_mov_b32 v244, 1\n\t" \
  "buffer_store_dword v244, v245, %[rs], %[sab] offen sc1\n\t" \
  "s_branch .Lok%=\n\t" \
  ".Lend%=:\n\t"
#define QK_ASM_1 \
  "s_waitcnt lgkmcnt(0)\n\t" \
  "v_subrev_u32 v244, 0x8000, v250\n\t" \
  "ds_read_b128 v[216:219], v248 offset:36864\n\t" \
  "ds_read_b128 v[220:223], v248 offset:37888\n\t" \
  "ds_read_b128 v[224:227], v248 offset:38912\n\t" \
  "ds_read_b128 v[228:231], v248 offset:39936\n\t" \
  "ds_read_b128 v[208:211], v244 offset:0\n\t" \
  "ds_read_b128 v[212:215], v244 offset:1024\n\t" \
  "ds_read_b128 v[236:239], v244 offset:2048\n\t" \
  "ds_read_b128 v[240:243], v244 offset:3072\n\t" \
  "v_mov_b64 v[192:193], 0\n\t" \
  "v_mov_b64 v[194:195], 0\n\t" \
  "v_mov_b64 v[196:197], 0\n\t" \
  "v_mov_b64 v[198:199], 0\n\t" \
  "s_movk_i32 s92, 0x2000\n\t" \
  ".Lwt%=:\n\t" \
  "ds_read_b64 v[200:201], v248 offset:30720\n\t" \
  "s_memrealtime s[90:91]\n\t" \
  "s_sub_u32 s92, s92, 1\n\t" \
  "s_waitcnt lgkmcnt(0)\n\t" \
  "v_readfirstlane_b32 s93, v200\n\t" \
  "v_readfirstlane_b32 %[tgt], v201\n\t" \
  "s_cbranch_scc1 .Lgo%=\n\t" \
  "s_cmp_eq_u32 s93, %[step]\n\t" \
  "s_cbranch_scc1 .Lwx%=\n\t" \
  "s_sleep 1\n\t" \
  "s_branch .Lwt%=\n\t" \
  ".Lwx%=:\n\t" \
  "s_lshl_b32 s93, s90, 2\n\t" \
  "s_sub_i32 s93, %[tgt], s93\n\t" \
  "s_cmp_le_i32 s93, 0\n\t" \
  "s_cbranch_scc1 .Lgo%=\n\t" \
  "s_min_i32 s93, s93, 8000\n\t" \
  "s_mov_b32 s94, s93\n\t" \
  ".Lsl%=:\n\t" \
  "s_sub_i32 s94, s94, 18\n\t" \
  "s_cmp_lt_i32 s94, 30\n\t" \
  "s_cbranch_scc1 .Lsx%=\n\t" \
  "s_sleep 1\n\t" \
  "s_branch .Lsl%=\n\t" \
  ".Lsx%=:\n\t" \
  "s_sub_i32 s93, s93, 12\n\t" \
  "s_max_i32 s93, s93, 4\n\t" \
  "s_lshr_b32 s93, s93, 2\n\t" \
  "s_add_u32 s93, s93, s90\n\t" \
  "s_memrealtime s[90:91]\n\t" \
  ".Lpace%=:\n\t" \
  "s_memrealtime s[94:95]\n\t" \
  "s_waitcnt lgkmcnt(1)\n\t" \
  "s_sub_i32 s92, s90, s93\n\t" \
  "s_cmp_ge_i32 s92, 0\n\t" \
  "s_cbranch_scc1 .Le%=\n\t" \
  "s_memrealtime s[90:91]\n\t" \
  "s_waitcnt lgkmcnt(1)\n\t" \
  "s_sub_i32 s92, s94, s93\n\t" \
  "s_cmp_lt_i32 s92, 0\n\t" \
  "s_cbranch_scc1 .Lpace%=\n\t" \
  "s_nop 4\n\t" \
  ".Le%=:\n\t" \
  "s_add_u32 s92, s92, s93\n\t" \
  "s_lshl_b32 s92, s92, 2\n\t" \
  "s_sub_i32 s92, %[tgt], s92\n\t" \
  "s_max_i32 s92, s92, 0\n\t" \
  "s_bitcmp0_b32 s92, 3\n\t" \
  "s_cbranch_scc1 .Lf3%=\n\t" \
  "s_nop 15\n\t" \
  ".Lf3%=:\n\t" \
  "s_bitcmp0_b32 s92, 2\n\t" \
  "s_cbranch_scc1 .Lf2%=\n\t" \
  "s_nop 10\n\t" \
  ".Lf2%=:\n\t" \
  ".Lgo%=:\n\t" \
  "s_mov_b32 s92, 0\n\t" \
  ".Lpoll%=:\n\t" \
  "buffer_load_dwordx4 v[200:203], v248, %[rs], %[som] offen sc1\n\t" \
  "buffer_load_dwordx4 v[204:207], v248, %[rs], %[som2] offen sc1\n\t" \
  "s_waitcnt vmcnt(1) lgkmcnt(0)\n\t" \
  "ds_write_b128 v249, v[200:203]\n\t" \
  "v_smfmac_f32_16x16x64_bf16 v[192:195], v[200:203], v[208:215], v251\n\t" \
  "v_max3_u32 v244, v200, v201, v202\n\t" \
  "v_smfmac_f32_16x16x64_bf16 v[196:199], v[200:203], v[236:243], v251\n\t" \
  "s_waitcnt vmcnt(0)\n\t" \
  "ds_write_b128 v249, v[204:207] offset:8192\n\t" \
  "v_smfmac_f32_16x16x64_bf16 v[192:195], v[204:207], v[216:223], v251\n\t" \
  "v_max3_u32 v244, v244, v203, v204\n\t" \
  "v_max3_u32 v244, v244, v205, v206\n\t" \
  "v_smfmac_f32_16x16x64_bf16 v[196:199], v[204:207], v[224:231], v251\n\t" \
  "v_max_u32 v244, v244, v207\n\t" \
  "v_cmp_le_u32 vcc, %[sent], v244\n\t" \
  "s_cbranch_vccnz .Lslow%=\n\t" \
  ".Lok%=:\n\t" \
  "ds_read_b128 v[216:219], v250 offset:32768\n\t" \
  "ds_read_b128 v[220:223], v250 offset:33792\n\t" \
  "ds_read_b128 v[224:227], v250 offset:34816\n\t" \
  "ds_read_b128 v[228:231], v250 offset:35840\n\t" \
  "s_waitcnt lgkmcnt(4)\n\t" \
  "s_barrier\n\t" \
  "ds_read_b128 v[208:211], v248 offset:0\n\t" \
  "ds_read_b128 v[212:215], v248 offset:3072\n\t" \
  "ds_read_b128 v[236:239], v248 offset:4096\n\t" \
  "ds_read_b128 v[240:243], v248 offset:5120\n\t" \
  "s_waitcnt lgkmcnt(3)\n\t" \
  "ds_read_b128 v[244:247], v248 offset:12288\n\t" \
  "v_smfmac_f32_16x16x64_bf16 v[192:195], v[208:211], v[216:223], v251\n\t" \
  "ds_read_b128 v[216:219], v250 offset:0\n\t" \
  "v_smfmac_f32_16x16x64_bf16 v[196:199], v[208:211], v[224:231], v251\n\t" \
  "s_waitcnt lgkmcnt(4)\n\t" \
  "ds_read_b128 v[220:223], v250 offset:1024\n\t" \
  "v_smfmac_f32_16x16x64_bf16 v[192:195], v[212:215], v[0:7], v251\n\t" \
  "ds_read_b128 v[224:227], v250 offset:2048\n\t" \
  "v_smfmac_f32_16x16x64_bf16 v[196:199], v[212:215], v[8:15], v251\n\t" \
  "s_waitcnt lgkmcnt(5)\n\t" \
  "ds_read_b128 v[228:231], v250 offset:3072\n\t" \
  "v_smfmac_f32_16x16x64_bf16 v[192:195], v[236:239], v[16:23], v251\n\t" \
  "ds_read_b128 v[200:203], v248 offset:6144\n\t" \
  "v_smfmac_f32_16x16x64_bf16 v[196:199], v[236:239], v[24:31], v251\n\t" \
  "s_waitcnt lgkmcnt(6)\n\t" \
  "ds_read_b128 v[204:207], v248 offset:7168\n\t" \
  "v_smfmac_f32_16x16x64_bf16 v[192:195], v[240:243], v[32:39], v251\n\t" \
  "ds_read_b128 v[208:211], v248 offset:8192\n\t" \
  "v_smfmac_f32_16x16x64_bf16 v[196:199], v[240:243], v[40:47], v251\n\t" \
  "s_waitcnt lgkmcnt(5)\n\t" \
  "ds_read_b128 v[212:215], v248 offset:10240\n\t" \
  "v_smfmac_f32_16x16x64_bf16 v[192:195], v[244:247], v[216:223], v251\n\t" \
  "s_waitcnt lgkmcnt(4)\n\t" \
  "v_smfmac_f32_16x16x64_bf16 v[196:199], v[244:247], v[224:231], v251\n\t" \
  "s_waitcnt lgkmcnt(3)\n\t" \
  "ds_read_b128 v[236:239], v248 offset:11264\n\t" \
  "v_smfmac_f32_16x16x64_bf16 v[192:195], v[200:203], v[48:55], v251\n\t" \
  "ds_read_b128 v[240:243], v248 offset:13312\n\t" \
  "v_smfmac_f32_16x16x64_bf16 v[196:199], v[200:203], v[56:63], v251\n\t" \
  "s_waitcnt lgkmcnt(4)\n\t" \
  "v_smfmac_f32_16x16x64_bf16 v[192:195], v[204:207], v[64:71], v251\n\t" \
  "ds_read_b128 v[244:247], v248 offset:14336\n\t" \
  "v_smfmac_f32_16x16x64_bf16 v[196:199], v[204:207], v[72:79], v251\n\t" \
  "s_waitcnt lgkmcnt(4)\n\t" \
  "v_smfmac_f32_16x16x64_bf16 v[192:195], v[208:211], v[80:87], v251\n\t" \
  "ds_read_b128 v[200:203], v248 offset:15360\n\t" \
  "v_smfmac_f32_16x16x64_bf16 v[196:199], v[208:211], v[88:95], v251\n\t" \
  "s_waitcnt lgkmcnt(4)\n\t" \
  "v_smfmac_f32_16x16x64_bf16 v[192:195], v[212:215], v[96:103], v251\n\t" \
  "ds_read_b128 v[204:207], v248 offset:2048\n\t" \
  "v_smfmac_f32_16x16x64_bf16 v[196:199], v[212:215], v[104:111], v251\n\t" \
  "s_waitcnt lgkmcnt(3)\n\t" \
  "v_smfmac_f32_16x16x64_bf16 v[192:195], v[236:239], v[112:119], v251\n\t" \
  "v_smfmac_f32_16x16x64_bf16 v[196:199], v[236:239], v[120:127], v251\n\t" \
  "v_smfmac_f32_16x16x64_bf16 v[192:195], v[240:243], v[128:135], v251\n\t" \
  "v_smfmac_f32_16x16x64_bf16 v[196:199], v[240:243], v[136:143], v251\n\t" \
  "s_waitcnt lgkmcnt(1)\n\t" \
  "v_smfmac_f32_16x16x64_bf16 v[192:195], v[244:247], v[144:151], v251\n\t" \
  "v_smfmac_f32_16x16x64_bf16 v[196:199], v[244:247], v[152:159], v251\n\t" \
  "v_smfmac_f32_16x16x64_bf16 v[192:195], v[200:203], v[160:167], v251\n\t" \
  "v_smfmac_f32_16x16x64_bf16 v[196:199], v[200:203], v[168:175], v251\n\t" \
  "s_waitcnt lgkmcnt(0)\n\t" \
  "v_smfmac_f32_16x16x64_bf16 v[192:195], v[204:207], v[176:183], v251\n\t" \
  "v_smfmac_f32_16x16x64_bf16 v[196:199], v[204:207], v[184:191], v251\n\t" \
  "ds_read_b128 v[200:203], v248 offset:17408\n\t" \
  "ds_read_b128 v[204:207], v248 offset:21504\n\t" \
  "v_mov_b32 v250, v232\n\t" \
  "v_mov_b32 v249, v233\n\t" \
  "s_nop 2\n\t" \
  "v_add_f32 v192, v192, v194\n\t" \
  "v_add_f32 v193, v193, v195\n\t" \
  "v_add_f32 v196, v196, v198\n\t" \
  "v_add_f32 v197, v197, v199\n\t" \
  "v_cvt_pk_bf16_f32 v192, v192, v193\n\t" \
  "v_cvt_pk_bf16_f32 v196, v196, v197\n\t" \
  "ds_write2_b32 v252, v192, v196 offset1:64\n\t" \
  "v_mov_b32 v251, v234\n\t" \
  "ds_read_b64_tr_b16 v[236:237], v250 offset:512\n\t" \
  "ds_read_b64_tr_b16 v[238:239], v250 offset:576\n\t" \
  "v_mov_b32 v212, %[step]\n\t" \
  "s_mov_b64 exec, 1\n\t" \
  "ds_write_b32 v248, v212 offset:28688\n\t" \
  "s_mov_b64 exec, -1\n\t" \
  "buffer_load_dwordx4 v[192:195], v248, %[rt], %[svt] offen offset:0 sc1\n\t" \
  "buffer_load_dwordx4 v[196:199], v248, %[rt], %[svt] offen offset:1024 sc1\n\t" \
  "s_waitcnt lgkmcnt(1)\n\t" \
  "v_mfma_f32_16x16x32_bf16 v[200:203], v[200:203], v[236:239], 0\n\t" \
  "v_mfma_f32_16x16x32_bf16 v[204:207], v[204:207], v[236:239], 0\n\t" \
  "s_nop 7\n\t" \
  "v_cndmask_b32 v216, v200, v204, %[nh]\n\t" \
  "v_cndmask_b32 v217, v201, v205, %[nh]\n\t" \
  "v_cndmask_b32 v218, v202, v206, %[nh]\n\t" \
  "v_cndmask_b32 v219, v203, v207, %[nh]\n\t" \
  "ds_write_b128 v248, v[216:219] offset:17408\n\t" \
  "buffer_load_dword v252, v249, %[rs], %[sv] offen offset:0 sc1\n\t" \
  "buffer_load_dword v253, v249, %[rs], %[sv] offen offset:256 sc1\n\t" \
  "ds_read_b32 v212, v248 offset:28672\n\t" \
  "ds_read_b64_tr_b16 v[240:241], v250 offset:1024\n\t" \
  "ds_read_b64_tr_b16 v[242:243], v250 offset:1088\n\t" \
  "ds_read_b64_tr_b16 v[244:245], v250 offset:1536\n\t" \
  "ds_read_b64_tr_b16 v[246:247], v250 offset:1600\n\t" \
  "ds_read_b64_tr_b16 v[232:233], v250 offset:0\n\t" \
  "ds_read_b64_tr_b16 v[234:235], v250 offset:64\n\t" \
  "s_setprio 3\n\t" \
  "s_waitcnt lgkmcnt(0)\n\t" \
  "v_cmp_ne_u32 vcc, %[step], v212\n\t" \
  "s_barrier\n\t" \
  "s_and_b32 s93, vcc_lo, 0xf\n\t" \
  "s_cbranch_scc1 .Lre%=\n\t" \
  ".Lrb%=:\n\t" \
  "ds_read_b64_tr_b16 v[204:205], v250 offset:2560\n\t" \
  "ds_read_b64_tr_b16 v[206:207], v250 offset:2624\n\t" \
  "ds_read_b64_tr_b16 v[208:209], v250 offset:3072\n\t" \
  "ds_read_b64_tr_b16 v[210:211], v250 offset:3136\n\t" \
  "ds_read_b128 v[220:223], v248 offset:18432\n\t" \
  "ds_read_b128 v[224:227], v248 offset:19456\n\t" \
  "ds_read_b128 v[228:231], v248 offset:16384\n\t" \
  "ds_read_b64_tr_b16 v[212:213], v250 offset:3584\n\t" \
  "ds_read_b64_tr_b16 v[214:215], v250 offset:3648\n\t" \
  "ds_read_b64_tr_b16 v[200:201], v250 offset:2048\n\t" \
  "ds_read_b64_tr_b16 v[202:203], v250 offset:2112\n\t" \
  "s_waitcnt lgkmcnt(9)\n\t" \
  "v_mfma_f32_16x16x32_bf16 v[204:207], v[204:207], v[236:239], 0\n\t" \
  "s_waitcnt lgkmcnt(7)\n\t" \
  "v_mfma_f32_16x16x32_bf16 v[204:207], v[208:211], v[240:243], v[204:207]\n\t" \
  "s_waitcnt lgkmcnt(6)\n\t" \
  "v_pk_add_f32 v[216:217], v[216:217], v[220:221]\n\t" \
  "v_pk_add_f32 v[218:219], v[218:219], v[222:223]\n\t" \
  "s_waitcnt lgkmcnt(4)\n\t" \
  "v_pk_add_f32 v[224:225], v[224:225], v[228:229]\n\t" \
  "v_pk_add_f32 v[226:227], v[226:227], v[230:231]\n\t" \
  "v_pk_add_f32 v[216:217], v[216:217], v[224:225]\n\t" \
  "v_pk_add_f32 v[218:219], v[218:219], v[226:227]\n\t" \
  "s_waitcnt lgkmcnt(2)\n\t" \
  "v_mfma_f32_16x16x32_bf16 v[204:207], v[212:215], v[244:247], v[204:207]\n\t" \
  "v_max3_f32 v224, v216, v217, v218\n\t" \
  "v_max_f32 v224, v224, v219\n\t" \
  "s_waitcnt lgkmcnt(0)\n\t" \
  "v_mfma_f32_16x16x32_bf16 v[204:207], v[200:203], v[232:235], v[204:207]\n\t" \
  "v_max_f32_dpp v225, v224, v224 row_ror:8 row_mask:0xf bank_mask:0xf\n\t" \
  "v_max_f32_dpp v226, v224, v224 row_ror:8 row_mask:0xf bank_mask:0xf\n\t" \
  "s_nop 1\n\t" \
  "v_permlane32_swap_b32 v225, v226\n\t" \
  "v_max_f32 v231, v225, v226\n\t" \
  "v_mov_b32 v220, 0x3f803f80\n\t" \
  "v_mov_b32 v221, 0x3f803f80\n\t" \
  "v_mov_b64 v[222:223], v[220:221]\n\t" \
  "v_mov_b32 v237, 0\n\t" \
  "v_mov_b32 v238, 0\n\t" \
  "v_max3_f32 v208, v204, v205, v231\n\t" \
  "v_max3_f32 v209, v208, v206, v207\n\t" \
  "v_max3_f32 v210, v208, v206, v207\n\t" \
  "v_cndmask_b32 v204, v204, v206, %[up]\n\t" \
  "v_cndmask_b32 v205, v205, v207, %[up]\n\t" \
  "v_permlane16_swap_b32 v209, v210\n\t" \
  "v_max_f32 v208, v209, v210\n\t" \
  "v_mul_f32 v208, %[negc], v208\n\t" \
  "v_fma_f32 v204, v204, %[c], v208\n\t" \
  "v_fma_f32 v205, v205, %[c], v208\n\t" \
  "v_exp_f32 v204, v204\n\t" \
  "v_fma_f32 v216, v216, %[c], v208\n\t" \
  "v_exp_f32 v205, v205\n\t" \
  "v_fma_f32 v217, v217, %[c], v208\n\t" \
  "v_exp_f32 v216, v216\n\t" \
  "v_fma_f32 v218, v218, %[c], v208\n\t" \
  "v_cvt_pk_bf16_f32 v236, v204, v205\n\t" \
  "v_cvt_pk_bf16_f32 v239, v204, v205\n\t" \
  "v_exp_f32 v217, v217\n\t" \
  "v_fma_f32 v219, v219, %[c], v208\n\t" \
  "v_mfma_f32_16x16x16_bf16 v[200:203], v[220:221], v[236:237], 0\n\t" \
  "v_exp_f32 v218, v218\n\t" \
  "s_waitcnt vmcnt(0)\n\t" \
  "v_mfma_f32_16x16x16_bf16 v[224:227], v[252:253], v[236:237], 0\n\t" \
  "v_cvt_pk_bf16_f32 v214, v216, v217\n\t" \
  "v_cvt_pk_bf16_f32 v212, v216, v217\n\t" \
  "v_exp_f32 v219, v219\n\t" \
  "v_mfma_f32_16x16x16_bf16 v[228:231], v[252:253], v[238:239], 0\n\t" \
  "v_cvt_pk_bf16_f32 v215, v218, v219\n\t" \
  "v_cvt_pk_bf16_f32 v213, v218, v219\n\t" \
  "v_mov_b32_dpp v212, v214 row_ror:8 row_mask:0xf bank_mask:0xc\n\t" \
  "v_mov_b32_dpp v214, v214 row_ror:8 row_mask:0xf bank_mask:0x3\n\t" \
  "v_mov_b32_dpp v213, v215 row_ror:8 row_mask:0xf bank_mask:0xc\n\t" \
  "v_mov_b32_dpp v215, v215 row_ror:8 row_mask:0xf bank_mask:0x3\n\t" \
  "v_max_u32 v208, v252, v253\n\t" \
  "v_cmp_le_u32 vcc, %[sent], v208\n\t" \
  "v_mfma_f32_16x16x32_bf16 v[200:203], v[220:223], v[212:215], v[200:203]\n\t" \
  ".Lpv%=:\n\t" \
  "v_mfma_f32_16x16x32_bf16 v[224:227], v[192:195], v[212:215], v[224:227]\n\t" \
  "v_mfma_f32_16x16x32_bf16 v[228:231], v[196:199], v[212:215], v[228:231]\n\t" \
  "s_cbranch_vccnz .Lvs%=\n\t" \
  ".Lvok%=:\n\t" \
  "s_nop 4\n\t" \
  "v_rcp_f32 v208, v200\n\t" \
  "v_cndmask_b32 v224, v224, v226, %[nh]\n\t" \
  "v_cndmask_b32 v225, v225, v227, %[nh]\n\t" \
  "v_cndmask_b32 v228, v228, v230, %[nh]\n\t" \
  "v_cndmask_b32 v229, v229, v231, %[nh]\n\t" \
  "v_pk_mul_f32 v[224:225], v[224:225], v[208:209] op_sel_hi:[1,0]\n\t" \
  "v_pk_mul_f32 v[228:229], v[228:229], v[208:209] op_sel_hi:[1,0]\n\t" \
  "v_cvt_pk_bf16_f32 v224, v224, v225\n\t" \
  "v_cvt_pk_bf16_f32 v225, v228, v229\n\t" \
  "buffer_store_dwordx2 v[224:225], v251, %[rs], %[so] offen sc1\n\t" \
  "s_memtime s[90:91]\n\t" \
  "s_setprio 0\n\t" \
  "s_waitcnt lgkmcnt(0)\n\t" \
  "s_mov_b32 %[tout], s90\n\t" \
  "s_mov_b32 %[spins], s92\n\t" \
  "s_branch .Lend%=\n\t" \
  ".Lre%=:\n\t" \
  "ds_read_b64_tr_b16 v[240:241], v250 offset:1024\n\t" \
  "ds_read_b64_tr_b16 v[242:243], v250 offset:1088\n\t" \
  "ds_read_b64_tr_b16 v[244:245], v250 offset:1536\n\t" \
  "ds_read_b64_tr_b16 v[246:247], v250 offset:1600\n\t" \
  "ds_read_b64_tr_b16 v[232:233], v250 offset:0\n\t" \
  "ds_read_b64_tr_b16 v[234:235], v250 offset:64\n\t" \
  "s_waitcnt lgkmcnt(0)\n\t" \
  "s_branch .Lrb%=\n\t" \
  ".Lvs%=:\n\t" \
  "v_cmp_eq_u32 vcc, %[sent], v252\n\t" \
  "v_cmp_eq_u32 s[94:95], %[sent], v253\n\t" \
  "s_or_b64 s[94:95], s[94:95], vcc\n\t" \
  "s_cbranch_scc0 .Lvok%=\n\t" \
  "s_add_u32 s92, s92, 1\n\t" \
  "s_sleep 2\n\t" \
  "s_and_b32 s93, s92, 63\n\t" \
  "s_cbranch_scc1 .Lvr%=\n\t" \
  "buffer_load_dword v208, off, %[rs], %[sab] sc1\n\t" \
  "s_waitcnt vmcnt(0)\n\t" \
  "v_readfirstlane_b32 s93, v208\n\t" \
  "s_nop 3\n\t" \
  "s_cmp_eq_u32 s93, 1\n\t" \
  "s_cbranch_scc1 .Lvok%=\n\t" \
  "s_cmp_lt_u32 s92, 0x80000\n\t" \
  "s_cbranch_scc1 .Lvr%=\n\t" \
  "v_mov_b32 v208, 1\n\t" \
  "buffer_store_dword v208, off, %[rs], %[sab] sc1\n\t" \
  "s_branch .Lvok%=\n\t" \
  ".Lvr%=:\n\t" \
  "buffer_load_dword v252, v249, %[rs], %[sv] offen offset:0 sc1\n\t" \
  "buffer_load_dword v253, v249, %[rs], %[sv] offen offset:256 sc1\n\t" \
  "s_waitcnt vmcnt(0)\n\t" \
  "v_mfma_f32_16x16x16_bf16 v[224:227], v[252:253], v[236:237], 0\n\t" \
  "v_mfma_f32_16x16x16_bf16 v[228:231], v[252:253], v[238:239], 0\n\t" \
  "v_max_u32 v208, v252, v253\n\t" \
  "v_cmp_le_u32 vcc, %[sent], v208\n\t" \
  "s_nop 4\n\t" \
  "s_branch .Lpv%=\n\t" \
  "s_branch .Lend%=\n\t" \
  ".Lslow%=:\n\t" \
  "s_mov_b64 s[94:95], 0\n\t" \
  "v_cmp_eq_u32 vcc, %[sent], v200\n\t" \
  "s_or_b64 s[94:95], s[94:95], vcc\n\t" \
  "v_cmp_eq_u32 vcc, %[sent], v201\n\t" \
  "s_or_b64 s[94:95], s[94:95], vcc\n\t" \
  "v_cmp_eq_u32 vcc, %[sent], v202\n\t" \
  "s_or_b64 s[94:95], s[94:95], vcc\n\t" \
  "v_cmp_eq_u32 vcc, %[sent], v203\n\t" \
  "s_or_b64 s[94:95], s[94:95], vcc\n\t" \
  "v_cmp_eq_u32 vcc, %[sent], v204\n\t" \
  "s_or_b64 s[94:95], s[94:95], vcc\n\t" \
  "v_cmp_eq_u32 vcc, %[sent], v205\n\t" \
  "s_or_b64 s[94:95], s[94:95], vcc\n\t" \
  "v_cmp_eq_u32 vcc, %[sent], v206\n\t" \
  "s_or_b64 s[94:95], s[94:95], vcc\n\t" \
  "v_cmp_eq_u32 vcc, %[sent], v207\n\t" \
  "s_or_b64 s[94:95], s[94:95], vcc\n\t" \
  "s_cmp_eq_u64 s[94:95], 0\n\t" \
  "s_cbranch_scc1 .Lok%=\n\t" \
  "v_mov_b64 v[192:193], 0\n\t" \
  "v_mov_b64 v[194:195], 0\n\t" \
  "v_mov_b64 v[196:197], 0\n\t" \
  "v_mov_b64 v[198:199], 0\n\t" \
  "s_add_u32 s92, s92, 1\n\t" \
  "s_sleep 6\n\t" \
  "s_cmp_eq_u32 s92, 1\n\t" \
  "s_cbranch_scc1 .Lpoll%=\n\t" \
  "s_sleep 1\n\t" \
  "s_and_b32 s93, s92, 63\n\t" \
  "s_cmp_lg_u32 s93, 0\n\t" \
  "s_cbranch_scc1 .Lpoll%=\n\t" \
  "v_mov_b32 v245, 0\n\t" \
  "buffer_load_dword v244, v245, %[rs], %[sab] offen sc1\n\t" \
  "s_waitcnt vmcnt(0)\n\t" \
  "v_readfirstlane_b32 s93, v244\n\t" \
  "s_nop 3\n\t" \
  "s_cmp_eq_u32 s93, 1\n\t" \
  "s_cbranch_scc1 .Lok%=\n\t" \
  "s_cmp_lt_u32 s92, 0x80000\n\t" \
  "s_cbranch_scc1 .Lpoll%=\n\t" \
  "v_mov_b32 v244, 1\n\t" \
  "buffer_store_dword v244, v245, %[rs], %[sab] offen sc1\n\t" \
  "s_branch .Lok%=\n\t" \
  ".Lend%=:\n\t"
#define QK_ASM_2 \
  "s_waitcnt lgkmcnt(0)\n\t" \
  "v_subrev_u32 v244, 0x8000, v250\n\t" \
  "ds_read_b128 v[216:219], v248 offset:40960\n\t" \
  "ds_read_b128 v[220:223], v248 offset:41984\n\t" \
  "ds_read_b128 v[224:227], v248 offset:43008\n\t" \
  "ds_read_b128 v[228:231], v248 offset:44032\n\t" \
  "ds_read_b128 v[208:211], v244 offset:0\n\t" \
  "ds_read_b128 v[212:215], v244 offset:1024\n\t" \
  "ds_read_b128 v[236:239], v244 offset:2048\n\t" \
  "ds_read_b128 v[240:243], v244 offset:3072\n\t" \
  "v_mov_b64 v[192:193], 0\n\t" \
  "v_mov_b64 v[194:195], 0\n\t" \
  "v_mov_b64 v[196:197], 0\n\t" \
  "v_mov_b64 v[198:199], 0\n\t" \
  "s_movk_i32 s92, 0x2000\n\t" \
  ".Lwt%=:\n\t" \
  "ds_read_b64 v[200:201], v248 offset:30720\n\t" \
  "s_memrealtime s[90:91]\n\t" \
  "s_sub_u32 s92, s92, 1\n\t" \
  "s_waitcnt lgkmcnt(0)\n\t" \
  "v_readfirstlane_b32 s93, v200\n\t" \
  "v_readfirstlane_b32 %[tgt], v201\n\t" \
  "s_cbranch_scc1 .Lgo%=\n\t" \
  "s_cmp_eq_u32 s93, %[step]\n\t" \
  "s_cbranch_scc1 .Lwx%=\n\t" \
  "s_sleep 1\n\t" \
  "s_branch .Lwt%=\n\t" \
  ".Lwx%=:\n\t" \
  "s_lshl_b32 s93, s90, 2\n\t" \
  "s_sub_i32 s93, %[tgt], s93\n\t" \
  "s_cmp_le_i32 s93, 0\n\t" \
  "s_cbranch_scc1 .Lgo%=\n\t" \
  "s_min_i32 s93, s93, 8000\n\t" \
  "s_mov_b32 s94, s93\n\t" \
  ".Lsl%=:\n\t" \
  "s_sub_i32 s94, s94, 18\n\t" \
  "s_cmp_lt_i32 s94, 30\n\t" \
  "s_cbranch_scc1 .Lsx%=\n\t" \
  "s_sleep 1\n\t" \
  "s_branch .Lsl%=\n\t" \
  ".Lsx%=:\n\t" \
  "s_sub_i32 s93, s93, 12\n\t" \
  "s_max_i32 s93, s93, 4\n\t" \
  "s_lshr_b32 s93, s93, 2\n\t" \
  "s_add_u32 s93, s93, s90\n\t" \
  "s_memrealtime s[90:91]\n\t" \
  ".Lpace%=:\n\t" \
  "s_memrealtime s[94:95]\n\t" \
  "s_waitcnt lgkmcnt(1)\n\t" \
  "s_sub_i32 s92, s90, s93\n\t" \
  "s_cmp_ge_i32 s92, 0\n\t" \
  "s_cbranch_scc1 .Le%=\n\t" \
  "s_memrealtime s[90:91]\n\t" \
  "s_waitcnt lgkmcnt(1)\n\t" \
  "s_sub_i32 s92, s94, s93\n\t" \
  "s_cmp_lt_i32 s92, 0\n\t" \
  "s_cbranch_scc1 .Lpace%=\n\t" \
  "s_nop 4\n\t" \
  ".Le%=:\n\t" \
  "s_add_u32 s92, s92, s93\n\t" \
  "s_lshl_b32 s92, s92, 2\n\t" \
  "s_sub_i32 s92, %[tgt], s92\n\t" \
  "s_max_i32 s92, s92, 0\n\t" \
  "s_bitcmp0_b32 s92, 3\n\t" \
  "s_cbranch_scc1 .Lf3%=\n\t" \
  "s_nop 15\n\t" \
  ".Lf3%=:\n\t" \
  "s_bitcmp0_b32 s92, 2\n\t" \
  "s_cbranch_scc1 .Lf2%=\n\t" \
  "s_nop 10\n\t" \
  ".Lf2%=:\n\t" \
  ".Lgo%=:\n\t" \
  "s_mov_b32 s92, 0\n\t" \
  ".Lpoll%=:\n\t" \
  "buffer_load_dwordx4 v[200:203], v248, %[rs], %[som] offen sc1\n\t" \
  "buffer_load_dwordx4 v[204:207], v248, %[rs], %[som2] offen sc1\n\t" \
  "s_waitcnt vmcnt(1) lgkmcnt(0)\n\t" \
  "ds_write_b128 v249, v[200:203]\n\t" \
  "v_smfmac_f32_16x16x64_bf16 v[192:195], v[200:203], v[208:215], v251\n\t" \
  "v_max3_u32 v244, v200, v201, v202\n\t" \
  "v_smfmac_f32_16x16x64_bf16 v[196:199], v[200:203], v[236:243], v251\n\t" \
  "s_waitcnt vmcnt(0)\n\t" \
  "ds_write_b128 v249, v[204:207] offset:8192\n\t" \
  "v_smfmac_f32_16x16x64_bf16 v[192:195], v[204:207], v[216:223], v251\n\t" \
  "v_max3_u32 v244, v244, v203, v204\n\t" \
  "v_max3_u32 v244, v244, v205, v206\n\t" \
  "v_smfmac_f32_16x16x64_bf16 v[196:199], v[204:207], v[224:231], v251\n\t" \
  "v_max_u32 v244, v244, v207\n\t" \
  "v_cmp_le_u32 vcc, %[sent], v244\n\t" \
  "s_cbranch_vccnz .Lslow%=\n\t" \
  ".Lok%=:\n\t" \
  "ds_read_b128 v[216:219], v250 offset:32768\n\t" \
  "ds_read_b128 v[220:223], v250 offset:33792\n\t" \
  "ds_read_b128 v[224:227], v250 offset:34816\n\t" \
  "ds_read_b128 v[228:231], v250 offset:35840\n\t" \
  "s_waitcnt lgkmcnt(4)\n\t" \
  "s_barrier\n\t" \
  "ds_read_b128 v[208:211], v248 offset:3072\n\t" \
  "ds_read_b128 v[212:215], v248 offset:4096\n\t" \
  "ds_read_b128 v[236:239], v248 offset:5120\n\t" \
  "ds_read_b128 v[240:243], v248 offset:6144\n\t" \
  "s_waitcnt lgkmcnt(3)\n\t" \
  "ds_read_b128 v[244:247], v248 offset:13312\n\t" \
  "v_smfmac_f32_16x16x64_bf16 v[192:195], v[208:211], v[216:223], v251\n\t" \
  "ds_read_b128 v[216:219], v250 offset:0\n\t" \
  "v_smfmac_f32_16x16x64_bf16 v[196:199], v[208:211], v[224:231], v251\n\t" \
  "s_waitcnt lgkmcnt(4)\n\t" \
  "ds_read_b128 v[220:223], v250 offset:1024\n\t" \
  "v_smfmac_f32_16x16x64_bf16 v[192:195], v[212:215], v[0:7], v251\n\t" \
  "ds_read_b128 v[224:227], v250 offset:2048\n\t" \
  "v_smfmac_f32_16x16x64_bf16 v[196:199], v[212:215], v[8:15], v251\n\t" \
  "s_waitcnt lgkmcnt(5)\n\t" \
  "ds_read_b128 v[228:231], v250 offset:3072\n\t" \
  "v_smfmac_f32_16x16x64_bf16 v[192:195], v[236:239], v[16:23], v251\n\t" \
  "ds_read_b128 v[200:203], v248 offset:7168\n\t" \
  "v_smfmac_f32_16x16x64_bf16 v[196:199], v[236:239], v[24:31], v251\n\t" \
  "s_waitcnt lgkmcnt(6)\n\t" \
  "ds_read_b128 v[204:207], v248 offset:8192\n\t" \
  "v_smfmac_f32_16x16x64_bf16 v[192:195], v[240:243], v[32:39], v251\n\t" \
  "ds_read_b128 v[208:211], v248 offset:9216\n\t" \
  "v_smfmac_f32_16x16x64_bf16 v[196:199], v[240:243], v[40:47], v251\n\t" \
  "s_waitcnt lgkmcnt(5)\n\t" \
  "ds_read_b128 v[212:215], v248 offset:11264\n\t" \
  "v_smfmac_f32_16x16x64_bf16 v[192:195], v[244:247], v[216:223], v251\n\t" \
  "s_waitcnt lgkmcnt(4)\n\t" \
  "v_smfmac_f32_16x16x64_bf16 v[196:199], v[244:247], v[224:231], v251\n\t" \
  "s_waitcnt lgkmcnt(3)\n\t" \
  "ds_read_b128 v[236:239], v248 offset:12288\n\t" \
  "v_smfmac_f32_16x16x64_bf16 v[192:195], v[200:203], v[48:55], v251\n\t" \
  "ds_read_b128 v[240:243], v248 offset:14336\n\t" \
  "v_smfmac_f32_16x16x64_bf16 v[196:199], v[200:203], v[56:63], v251\n\t" \
  "s_waitcnt lgkmcnt(4)\n\t" \
  "v_smfmac_f32_16x16x64_bf16 v[192:195], v[204:207], v[64:71], v251\n\t" \
  "ds_read_b128 v[244:247], v248 offset:15360\n\t" \
  "v_smfmac_f32_16x16x64_bf16 v[196:199], v[204:207], v[72:79], v251\n\t" \
  "s_waitcnt lgkmcnt(4)\n\t" \
  "v_smfmac_f32_16x16x64_bf16 v[192:195], v[208:211], v[80:87], v251\n\t" \
  "ds_read_b128 v[200:203], v248 offset:0\n\t" \
  "v_smfmac_f32_16x16x64_bf16 v[196:199], v[208:211], v[88:95], v251\n\t" \
  "s_waitcnt lgkmcnt(4)\n\t" \
  "v_smfmac_f32_16x16x64_bf16 v[192:195], v[212:215], v[96:103], v251\n\t" \
  "ds_read_b128 v[204:207], v248 offset:1024\n\t" \
  "v_smfmac_f32_16x16x64_bf16 v[196:199], v[212:215], v[104:111], v251\n\t" \
  "s_waitcnt lgkmcnt(3)\n\t" \
  "v_smfmac_f32_16x16x64_bf16 v[192:195], v[236:239], v[112:119], v251\n\t" \
  "v_smfmac_f32_16x16x64_bf16 v[196:199], v[236:239], v[120:127], v251\n\t" \
  "v_smfmac_f32_16x16x64_bf16 v[192:195], v[240:243], v[128:135], v251\n\t" \
  "v_smfmac_f32_16x16x64_bf16 v[196:199], v[240:243], v[136:143], v251\n\t" \
  "s_waitcnt lgkmcnt(1)\n\t" \
  "v_smfmac_f32_16x16x64_bf16 v[192:195], v[244:247], v[144:151], v251\n\t" \
  "v_smfmac_f32_16x16x64_bf16 v[196:199], v[244:247], v[152:159], v251\n\t" \
  "v_smfmac_f32_16x16x64_bf16 v[192:195], v[200:203], v[160:167], v251\n\t" \
  "v_smfmac_f32_16x16x64_bf16 v[196:199], v[200:203], v[168:175], v251\n\t" \
  "s_waitcnt lgkmcnt(0)\n\t" \
  "v_smfmac_f32_16x16x64_bf16 v[192:195], v[204:207], v[176:183], v251\n\t" \
  "v_smfmac_f32_16x16x64_bf16 v[196:199], v[204:207], v[184:191], v251\n\t" \
  "ds_read_b128 v[200:203], v248 offset:18432\n\t" \
  "ds_read_b128 v[204:207], v248 offset:22528\n\t" \
  "v_mov_b32 v250, v232\n\t" \
  "v_mov_b32 v249, v233\n\t" \
  "s_nop 2\n\t" \
  "v_add_f32 v192, v192, v194\n\t" \
  "v_add_f32 v193, v193, v195\n\t" \
  "v_add_f32 v196, v196, v198\n\t" \
  "v_add_f32 v197, v197, v199\n\t" \
  "v_cvt_pk_bf16_f32 v192, v192, v193\n\t" \
  "v_cvt_pk_bf16_f32 v196, v196, v197\n\t" \
  "ds_write2_b32 v252, v192, v196 offset1:64\n\t" \
  "v_mov_b32 v251, v234\n\t" \
  "ds_read_b64_tr_b16 v[240:241], v250 offset:1024\n\t" \
  "ds_read_b64_tr_b16 v[242:243], v250 offset:1088\n\t" \
  "v_mov_b32 v212, %[step]\n\t" \
  "s_mov_b64 exec, 1\n\t" \
  "ds_write_b32 v248, v212 offset:28704\n\t" \
  "s_mov_b64 exec, -1\n\t" \
  "buffer_load_dwordx4 v[192:195], v248, %[rt], %[svt] offen offset:0 sc1\n\t" \
  "buffer_load_dwordx4 v[196:199], v248, %[rt], %[svt] offen offset:1024 sc1\n\t" \
  "s_waitcnt lgkmcnt(1)\n\t" \
  "v_mfma_f32_16x16x32_bf16 v[200:203], v[200:203], v[240:243], 0\n\t" \
  "v_mfma_f32_16x16x32_bf16 v[204:207], v[204:207], v[240:243], 0\n\t" \
  "s_nop 7\n\t" \
  "v_cndmask_b32 v216, v200, v204, %[nh]\n\t" \
  "v_cndmask_b32 v217, v201, v205, %[nh]\n\t" \
  "v_cndmask_b32 v218, v202, v206, %[nh]\n\t" \
  "v_cndmask_b32 v219, v203, v207, %[nh]\n\t" \
  "ds_write_b128 v248, v[216:219] offset:18432\n\t" \
  "buffer_load_dword v252, v249, %[rs], %[sv] offen offset:0 sc1\n\t" \
  "buffer_load_dword v253, v249, %[rs], %[sv] offen offset:256 sc1\n\t" \
  "ds_read_b32 v212, v248 offset:28672\n\t" \
  "ds_read_b64_tr_b16 v[244:245], v250 offset:1536\n\t" \
  "ds_read_b64_tr_b16 v[246:247], v250 offset:1600\n\t" \
  "ds_read_b64_tr_b16 v[232:233], v250 offset:0\n\t" \
  "ds_read_b64_tr_b16 v[234:235], v250 offset:64\n\t" \
  "ds_read_b64_tr_b16 v[236:237], v250 offset:512\n\t" \
  "ds_read_b64_tr_b16 v[238:239], v250 offset:576\n\t" \
  "s_setprio 3\n\t" \
  "s_waitcnt lgkmcnt(0)\n\t" \
  "v_cmp_ne_u32 vcc, %[step], v212\n\t" \
  "s_barrier\n\t" \
  "s_and_b32 s93, vcc_lo, 0xf\n\t" \
  "s_cbranch_scc1 .Lre%=\n\t" \
  ".Lrb%=:\n\t" \
  "ds_read_b64_tr_b16 v[208:209], v250 offset:3072\n\t" \
  "ds_read_b64_tr_b16 v[210:211], v250 offset:3136\n\t" \
  "ds_read_b64_tr_b16 v[212:213], v250 offset:3584\n\t" \
  "ds_read_b64_tr_b16 v[214:215], v250 offset:3648\n\t" \
  "ds_read_b128 v[220:223], v248 offset:19456\n\t" \
  "ds_read_b128 v[224:227], v248 offset:16384\n\t" \
  "ds_read_b128 v[228:231], v248 offset:17408\n\t" \
  "ds_read_b64_tr_b16 v[200:201], v250 offset:2048\n\t" \
  "ds_read_b64_tr_b16 v[202:203], v250 offset:2112\n\t" \
  "ds_read_b64_tr_b16 v[204:205], v250 offset:2560\n\t" \
  "ds_read_b64_tr_b16 v[206:207], v250 offset:2624\n\t" \
  "s_waitcnt lgkmcnt(9)\n\t" \
  "v_mfma_f32_16x16x32_bf16 v[208:211], v[208:211], v[240:243], 0\n\t" \
  "s_waitcnt lgkmcnt(7)\n\t" \
  "v_mfma_f32_16x16x32_bf16 v[208:211], v[212:215], v[244:247], v[208:211]\n\t" \
  "s_waitcnt lgkmcnt(6)\n\t" \
  "v_pk_add_f32 v[216:217], v[216:217], v[220:221]\n\t" \
  "v_pk_add_f32 v[218:219], v[218:219], v[222:223]\n\t" \
  "s_waitcnt lgkmcnt(4)\n\t" \
  "v_pk_add_f32 v[224:225], v[224:225], v[228:229]\n\t" \
  "v_pk_add_f32 v[226:227], v[226:227], v[230:231]\n\t" \
  "v_pk_add_f32 v[216:217], v[216:217], v[224:225]\n\t" \
  "v_pk_add_f32 v[218:219], v[218:219], v[226:227]\n\t" \
  "s_waitcnt lgkmcnt(2)\n\t" \
  "v_mfma_f32_16x16x32_bf16 v[208:211], v[200:203], v[232:235], v[208:211]\n\t" \
  "v_max3_f32 v224, v216, v217, v218\n\t" \
  "v_max_f32 v224, v224, v219\n\t" \
  "s_waitcnt lgkmcnt(0)\n\t" \
  "v_mfma_f32_16x16x32_bf16 v[208:211], v[204:207], v[236:239], v[208:211]\n\t" \
  "v_max_f32_dpp v225, v224, v224 row_ror:8 row_mask:0xf bank_mask:0xf\n\t" \
  "v_max_f32_dpp v226, v224, v224 row_ror:8 row_mask:0xf bank_mask:0xf\n\t" \
  "s_nop 1\n\t" \
  "v_permlane32_swap_b32 v225, v226\n\t" \
  "v_max_f32 v231, v225, v226\n\t" \
  "v_mov_b32 v220, 0x3f803f80\n\t" \
  "v_mov_b32 v221, 0x3f803f80\n\t" \
  "v_mov_b64 v[222:223], v[220:221]\n\t" \
  "v_mov_b32 v241, 0\n\t" \
  "v_mov_b32 v242, 0\n\t" \
  "v_max3_f32 v212, v208, v209, v231\n\t" \
  "v_max3_f32 v213, v212, v210, v211\n\t" \
  "v_max3_f32 v214, v212, v210, v211\n\t" \
  "v_cndmask_b32 v208, v208, v210, %[up]\n\t" \
  "v_cndmask_b32 v209, v209, v211, %[up]\n\t" \
  "v_permlane16_swap_b32 v213, v214\n\t" \
  "v_max_f32 v212, v213, v214\n\t" \
  "v_mul_f32 v212, %[negc], v212\n\t" \
  "v_fma_f32 v208, v208, %[c], v212\n\t" \
  "v_fma_f32 v209, v209, %[c], v212\n\t" \
  "v_exp_f32 v208, v208\n\t" \
  "v_fma_f32 v216, v216, %[c], v212\n\t" \
  "v_exp_f32 v209, v209\n\t" \
  "v_fma_f32 v217, v217, %[c], v212\n\t" \
  "v_exp_f32 v216, v216\n\t" \
  "v_fma_f32 v218, v218, %[c], v212\n\t" \
  "v_cvt_pk_bf16_f32 v240, v208, v209\n\t" \
  "v_cvt_pk_bf16_f32 v243, v208, v209\n\t" \
  "v_exp_f32 v217, v217\n\t" \
  "v_fma_f32 v219, v219, %[c], v212\n\t" \
  "v_mfma_f32_16x16x16_bf16 v[204:207], v[220:221], v[240:241], 0\n\t" \
  "v_exp_f32 v218, v218\n\t" \
  "s_waitcnt vmcnt(0)\n\t" \
  "v_mfma_f32_16x16x16_bf16 v[224:227], v[252:253], v[240:241], 0\n\t" \
  "v_cvt_pk_bf16_f32 v202, v216, v217\n\t" \
  "v_cvt_pk_bf16_f32 v200, v216, v217\n\t" \
  "v_exp_f32 v219, v219\n\t" \
  "v_mfma_f32_16x16x16_bf16 v[228:231], v[252:253], v[242:243], 0\n\t" \
  "v_cvt_pk_bf16_f32 v203, v218, v219\n\t" \
  "v_cvt_pk_bf16_f32 v201, v218, v219\n\t" \
  "v_mov_b32_dpp v200, v202 row_ror:8 row_mask:0xf bank_mask:0xc\n\t" \
  "v_mov_b32_dpp v202, v202 row_ror:8 row_mask:0xf bank_mask:0x3\n\t" \
  "v_mov_b32_dpp v201, v203 row_ror:8 row_mask:0xf bank_mask:0xc\n\t" \
  "v_mov_b32_dpp v203, v203 row_ror:8 row_mask:0xf bank_mask:0x3\n\t" \
  "v_max_u32 v212, v252, v253\n\t" \
  "v_cmp_le_u32 vcc, %[sent], v212\n\t" \
  "v_mfma_f32_16x16x32_bf16 v[204:207], v[220:223], v[200:203], v[204:207]\n\t" \
  ".Lpv%=:\n\t" \
  "v_mfma_f32_16x16x32_bf16 v[224:227], v[192:195], v[200:203], v[224:227]\n\t" \
  "v_mfma_f32_16x16x32_bf16 v[228:231], v[196:199], v[200:203], v[228:231]\n\t" \
  "s_cbranch_vccnz .Lvs%=\n\t" \
  ".Lvok%=:\n\t" \
  "s_nop 4\n\t" \
  "v_rcp_f32 v212, v204\n\t" \
  "v_cndmask_b32 v224, v224, v226, %[nh]\n\t" \
  "v_cndmask_b32 v225, v225, v227, %[nh]\n\t" \
  "v_cndmask_b32 v228, v228, v230, %[nh]\n\t" \
  "v_cndmask_b32 v229, v229, v231, %[nh]\n\t" \
  "v_pk_mul_f32 v[224:225], v[224:225], v[212:213] op_sel_hi:[1,0]\n\t" \
  "v_pk_mul_f32 v[228:229], v[228:229], v[212:213] op_sel_hi:[1,0]\n\t" \
  "v_cvt_pk_bf16_f32 v224, v224, v225\n\t" \
  "v_cvt_pk_bf16_f32 v225, v228, v229\n\t" \
  "buffer_store_dwordx2 v[224:225], v251, %[rs], %[so] offen sc1\n\t" \
  "s_memtime s[90:91]\n\t" \
  "s_setprio 0\n\t" \
  "s_waitcnt lgkmcnt(0)\n\t" \
  "s_mov_b32 %[tout], s90\n\t" \
  "s_mov_b32 %[spins], s92\n\t" \
  "s_branch .Lend%=\n\t" \
  ".Lre%=:\n\t" \
  "ds_read_b64_tr_b16 v[244:245], v250 offset:1536\n\t" \
  "ds_read_b64_tr_b16 v[246:247], v250 offset:1600\n\t" \
  "ds_read_b64_tr_b16 v[232:233], v250 offset:0\n\t" \
  "ds_read_b64_tr_b16 v[234:235], v250 offset:64\n\t" \
  "ds_read_b64_tr_b16 v[236:237], v250 offset:512\n\t" \
  "ds_read_b64_tr_b16 v[238:239], v250 offset:576\n\t" \
  "s_waitcnt lgkmcnt(0)\n\t" \
  "s_branch .Lrb%=\n\t" \
  ".Lvs%=:\n\t" \
  "v_cmp_eq_u32 vcc, %[sent], v252\n\t" \
  "v_cmp_eq_u32 s[94:95], %[sent], v253\n\t" \
  "s_or_b64 s[94:95], s[94:95], vcc\n\t" \
  "s_cbranch_scc0 .Lvok%=\n\t" \
  "s_add_u32 s92, s92, 1\n\t" \
  "s_sleep 2\n\t" \
  "s_and_b32 s93, s92, 63\n\t" \
  "s_cbranch_scc1 .Lvr%=\n\t" \
  "buffer_load_dword v212, off, %[rs], %[sab] sc1\n\t" \
  "s_waitcnt vmcnt(0)\n\t" \
  "v_readfirstlane_b32 s93, v212\n\t" \
  "s_nop 3\n\t" \
  "s_cmp_eq_u32 s93, 1\n\t" \
  "s_cbranch_scc1 .Lvok%=\n\t" \
  "s_cmp_lt_u32 s92, 0x80000\n\t" \
  "s_cbranch_scc1 .Lvr%=\n\t" \
  "v_mov_b32 v212, 1\n\t" \
  "buffer_store_dword v212, off, %[rs], %[sab] sc1\n\t" \
  "s_branch .Lvok%=\n\t" \
  ".Lvr%=:\n\t" \
  "buffer_load_dword v252, v249, %[rs], %[sv] offen offset:0 sc1\n\t" \
  "buffer_load_dword v253, v249, %[rs], %[sv] offen offset:256 sc1\n\t" \
  "s_waitcnt vmcnt(0)\n\t" \
  "v_mfma_f32_16x16x16_bf16 v[224:227], v[252:253], v[240:241], 0\n\t" \
  "v_mfma_f32_16x16x16_bf16 v[228:231], v[252:253], v[242:243], 0\n\t" \
  "v_max_u32 v212, v252, v253\n\t" \
  "v_cmp_le_u32 vcc, %[sent], v212\n\t" \
  "s_nop 4\n\t" \
  "s_branch .Lpv%=\n\t" \
  "s_branch .Lend%=\n\t" \
  ".Lslow%=:\n\t" \
  "s_mov_b64 s[94:95], 0\n\t" \
  "v_cmp_eq_u32 vcc, %[sent], v200\n\t" \
  "s_or_b64 s[94:95], s[94:95], vcc\n\t" \
  "v_cmp_eq_u32 vcc, %[sent], v201\n\t" \
  "s_or_b64 s[94:95], s[94:95], vcc\n\t" \
  "v_cmp_eq_u32 vcc, %[sent], v202\n\t" \
  "s_or_b64 s[94:95], s[94:95], vcc\n\t" \
  "v_cmp_eq_u32 vcc, %[sent], v203\n\t" \
  "s_or_b64 s[94:95], s[94:95], vcc\n\t" \
  "v_cmp_eq_u32 vcc, %[sent], v204\n\t" \
  "s_or_b64 s[94:95], s[94:95], vcc\n\t" \
  "v_cmp_eq_u32 vcc, %[sent], v205\n\t" \
  "s_or_b64 s[94:95], s[94:95], vcc\n\t" \
  "v_cmp_eq_u32 vcc, %[sent], v206\n\t" \
  "s_or_b64 s[94:95], s[94:95], vcc\n\t" \
  "v_cmp_eq_u32 vcc, %[sent], v207\n\t" \
  "s_or_b64 s[94:95], s[94:95], vcc\n\t" \
  "s_cmp_eq_u64 s[94:95], 0\n\t" \
  "s_cbranch_scc1 .Lok%=\n\t" \
  "v_mov_b64 v[192:193], 0\n\t" \
  "v_mov_b64 v[194:195], 0\n\t" \
  "v_mov_b64 v[196:197], 0\n\t" \
  "v_mov_b64 v[198:199], 0\n\t" \
  "s_add_u32 s92, s92, 1\n\t" \
  "s_sleep 6\n\t" \
  "s_cmp_eq_u32 s92, 1\n\t" \
  "s_cbranch_scc1 .Lpoll%=\n\t" \
  "s_sleep 1\n\t" \
  "s_and_b32 s93, s92, 63\n\t" \
  "s_cmp_lg_u32 s93, 0\n\t" \
  "s_cbranch_scc1 .Lpoll%=\n\t" \
  "v_mov_b32 v245, 0\n\t" \
  "buffer_load_dword v244, v245, %[rs], %[sab] offen sc1\n\t" \
  "s_waitcnt vmcnt(0)\n\t" \
  "v_readfirstlane_b32 s93, v244\n\t" \
  "s_nop 3\n\t" \
  "s_cmp_eq_u32 s93, 1\n\t" \
  "s_cbranch_scc1 .Lok%=\n\t" \
  "s_cmp_lt_u32 s92, 0x80000\n\t" \
  "s_cbranch_scc1 .Lpoll%=\n\t" \
  "v_mov_b32 v244, 1\n\t" \
  "buffer_store_dword v244, v245, %[rs], %[sab] offen sc1\n\t" \
  "s_branch .Lok%=\n\t" \
  ".Lend%=:\n\t"
#define QK_ASM_3 \
  "s_waitcnt lgkmcnt(0)\n\t" \
  "v_subrev_u32 v244, 0x8000, v250\n\t" \
  "ds_read_b128 v[216:219], v248 offset:45056\n\t" \
  "ds_read_b128 v[220:223], v248 offset:46080\n\t" \
  "ds_read_b128 v[224:227], v248 offset:47104\n\t" \
  "ds_read_b128 v[228:231], v248 offset:48128\n\t" \
  "ds_read_b128 v[208:211], v244 offset:0\n\t" \
  "ds_read_b128 v[212:215], v244 offset:1024\n\t" \
  "ds_read_b128 v[236:239], v244 offset:2048\n\t" \
  "ds_read_b128 v[240:243], v244 offset:3072\n\t" \
  "v_mov_b64 v[192:193], 0\n\t" \
  "v_mov_b64 v[194:195], 0\n\t" \
  "v_mov_b64 v[196:197], 0\n\t" \
  "v_mov_b64 v[198:199], 0\n\t" \
  "s_movk_i32 s92, 0x2000\n\t" \
  ".Lwt%=:\n\t" \
  "ds_read_b64 v[200:201], v248 offset:30720\n\t" \
  "s_memrealtime s[90:91]\n\t" \
  "s_sub_u32 s92, s92, 1\n\t" \
  "s_waitcnt lgkmcnt(0)\n\t" \
  "v_readfirstlane_b32 s93, v200\n\t" \
  "v_readfirstlane_b32 %[tgt], v201\n\t" \
  "s_cbranch_scc1 .Lgo%=\n\t" \
  "s_cmp_eq_u32 s93, %[step]\n\t" \
  "s_cbranch_scc1 .Lwx%=\n\t" \
  "s_sleep 1\n\t" \
  "s_branch .Lwt%=\n\t" \
  ".Lwx%=:\n\t" \
  "s_lshl_b32 s93, s90, 2\n\t" \
  "s_sub_i32 s93, %[tgt], s93\n\t" \
  "s_cmp_le_i32 s93, 0\n\t" \
  "s_cbranch_scc1 .Lgo%=\n\t" \
  "s_min_i32 s93, s93, 8000\n\t" \
  "s_mov_b32 s94, s93\n\t" \
  ".Lsl%=:\n\t" \
  "s_sub_i32 s94, s94, 18\n\t" \
  "s_cmp_lt_i32 s94, 30\n\t" \
  "s_cbranch_scc1 .Lsx%=\n\t" \
  "s_sleep 1\n\t" \
  "s_branch .Lsl%=\n\t" \
  ".Lsx%=:\n\t" \
  "s_sub_i32 s93, s93, 12\n\t" \
  "s_max_i32 s93, s93, 4\n\t" \
  "s_lshr_b32 s93, s93, 2\n\t" \
  "s_add_u32 s93, s93, s90\n\t" \
  "s_memrealtime s[90:91]\n\t" \
  ".Lpace%=:\n\t" \
  "s_memrealtime s[94:95]\n\t" \
  "s_waitcnt lgkmcnt(1)\n\t" \
  "s_sub_i32 s92, s90, s93\n\t" \
  "s_cmp_ge_i32 s92, 0\n\t" \
  "s_cbranch_scc1 .Le%=\n\t" \
  "s_memrealtime s[90:91]\n\t" \
  "s_waitcnt lgkmcnt(1)\n\t" \
  "s_sub_i32 s92, s94, s93\n\t" \
  "s_cmp_lt_i32 s92, 0\n\t" \
  "s_cbranch_scc1 .Lpace%=\n\t" \
  "s_nop 4\n\t" \
  ".Le%=:\n\t" \
  "s_add_u32 s92, s92, s93\n\t" \
  "s_lshl_b32 s92, s92, 2\n\t" \
  "s_sub_i32 s92, %[tgt], s92\n\t" \
  "s_max_i32 s92, s92, 0\n\t" \
  "s_bitcmp0_b32 s92, 3\n\t" \
  "s_cbranch_scc1 .Lf3%=\n\t" \
  "s_nop 15\n\t" \
  ".Lf3%=:\n\t" \
  "s_bitcmp0_b32 s92, 2\n\t" \
  "s_cbranch_scc1 .Lf2%=\n\t" \
  "s_nop 10\n\t" \
  ".Lf2%=:\n\t" \
  ".Lgo%=:\n\t" \
  "s_mov_b32 s92, 0\n\t" \
  ".Lpoll%=:\n\t" \
  "buffer_load_dwordx4 v[200:203], v248, %[rs], %[som] offen sc1\n\t" \
  "buffer_load_dwordx4 v[204:207], v248, %[rs], %[som2] offen sc1\n\t" \
  "s_waitcnt vmcnt(1) lgkmcnt(0)\n\t" \
  "ds_write_b128 v249, v[200:203]\n\t" \
  "v_smfmac_f32_16x16x64_bf16 v[192:195], v[200:203], v[208:215], v251\n\t" \
  "v_max3_u32 v244, v200, v201, v202\n\t" \
  "v_smfmac_f32_16x16x64_bf16 v[196:199], v[200:203], v[236:243], v251\n\t" \
  "s_waitcnt vmcnt(0)\n\t" \
  "ds_write_b128 v249, v[204:207] offset:8192\n\t" \
  "v_smfmac_f32_16x16x64_bf16 v[192:195], v[204:207], v[216:223], v251\n\t" \
  "v_max3_u32 v244, v244, v203, v204\n\t" \
  "v_max3_u32 v244, v244, v205, v206\n\t" \
  "v_smfmac_f32_16x16x64_bf16 v[196:199], v[204:207], v[224:231], v251\n\t" \
  "v_max_u32 v244, v244, v207\n\t" \
  "v_cmp_le_u32 vcc, %[sent], v244\n\t" \
  "s_cbranch_vccnz .Lslow%=\n\t" \
  ".Lok%=:\n\t" \
  "ds_read_b128 v[216:219], v250 offset:32768\n\t" \
  "ds_read_b128 v[220:223], v250 offset:33792\n\t" \
  "ds_read_b128 v[224:227], v250 offset:34816\n\t" \
  "ds_read_b128 v[228:231], v250 offset:35840\n\t" \
  "s_waitcnt lgkmcnt(4)\n\t" \
  "s_barrier\n\t" \
  "ds_read_b128 v[208:211], v248 offset:2048\n\t" \
  "ds_read_b128 v[212:215], v248 offset:5120\n\t" \
  "ds_read_b128 v[236:239], v248 offset:6144\n\t" \
  "ds_read_b128 v[240:243], v248 offset:7168\n\t" \
  "s_waitcnt lgkmcnt(3)\n\t" \
  "ds_read_b128 v[244:247], v248 offset:12288\n\t" \
  "v_smfmac_f32_16x16x64_bf16 v[192:195], v[208:211], v[216:223], v251\n\t" \
  "ds_read_b128 v[216:219], v250 offset:0\n\t" \
  "v_smfmac_f32_16x16x64_bf16 v[196:199], v[208:211], v[224:231], v251\n\t" \
  "s_waitcnt lgkmcnt(4)\n\t" \
  "ds_read_b128 v[220:223], v250 offset:1024\n\t" \
  "v_smfmac_f32_16x16x64_bf16 v[192:195], v[212:215], v[0:7], v251\n\t" \
  "ds_read_b128 v[224:227], v250 offset:2048\n\t" \
  "v_smfmac_f32_16x16x64_bf16 v[196:199], v[212:215], v[8:15], v251\n\t" \
  "s_waitcnt lgkmcnt(5)\n\t" \
  "ds_read_b128 v[228:231], v250 offset:3072\n\t" \
  "v_smfmac_f32_16x16x64_bf16 v[192:195], v[236:239], v[16:23], v251\n\t" \
  "ds_read_b128 v[200:203], v248 offset:8192\n\t" \
  "v_smfmac_f32_16x16x64_bf16 v[196:199], v[236:239], v[24:31], v251\n\t" \
  "s_waitcnt lgkmcnt(6)\n\t" \
  "ds_read_b128 v[204:207], v248 offset:9216\n\t" \
  "v_smfmac_f32_16x16x64_bf16 v[192:195], v[240:243], v[32:39], v251\n\t" \
  "ds_read_b128 v[208:211], v248 offset:10240\n\t" \
  "v_smfmac_f32_16x16x64_bf16 v[196:199], v[240:243], v[40:47], v251\n\t" \
  "s_waitcnt lgkmcnt(5)\n\t" \
  "ds_read_b128 v[212:215], v248 offset:13312\n\t" \
  "v_smfmac_f32_16x16x64_bf16 v[192:195], v[244:247], v[216:223], v251\n\t" \
  "s_waitcnt lgkmcnt(4)\n\t" \
  "v_smfmac_f32_16x16x64_bf16 v[196:199], v[244:247], v[224:231], v251\n\t" \
  "s_waitcnt lgkmcnt(3)\n\t" \
  "ds_read_b128 v[236:239], v248 offset:14336\n\t" \
  "v_smfmac_f32_16x16x64_bf16 v[192:195], v[200:203], v[48:55], v251\n\t" \
  "ds_read_b128 v[240:243], v248 offset:15360\n\t" \
  "v_smfmac_f32_16x16x64_bf16 v[196:199], v[200:203], v[56:63], v251\n\t" \
  "s_waitcnt lgkmcnt(4)\n\t" \
  "v_smfmac_f32_16x16x64_bf16 v[192:195], v[204:207], v[64:71], v251\n\t" \
  "ds_read_b128 v[244:247], v248 offset:0\n\t" \
  "v_smfmac_f32_16x16x64_bf16 v[196:199], v[204:207], v[72:79], v251\n\t" \
  "s_waitcnt lgkmcnt(4)\n\t" \
  "v_smfmac_f32_16x16x64_bf16 v[192:195], v[208:211], v[80:87], v251\n\t" \
  "ds_read_b128 v[200:203], v248 offset:1024\n\t" \
  "v_smfmac_f32_16x16x64_bf16 v[196:199], v[208:211], v[88:95], v251\n\t" \
  "s_waitcnt lgkmcnt(4)\n\t" \
  "v_smfmac_f32_16x16x64_bf16 v[192:195], v[212:215], v[96:103], v251\n\t" \
  "ds_read_b128 v[204:207], v248 offset:4096\n\t" \
  "v_smfmac_f32_16x16x64_bf16 v[196:199], v[212:215], v[104:111], v251\n\t" \
  "s_waitcnt lgkmcnt(3)\n\t" \
  "v_smfmac_f32_16x16x64_bf16 v[192:195], v[236:239], v[112:119], v251\n\t" \
  "v_smfmac_f32_16x16x64_bf16 v[196:199], v[236:239], v[120:127], v251\n\t" \
  "v_smfmac_f32_16x16x64_bf16 v[192:195], v[240:243], v[128:135], v251\n\t" \
  "v_smfmac_f32_16x16x64_bf16 v[196:199], v[240:243], v[136:143], v251\n\t" \
  "s_waitcnt lgkmcnt(1)\n\t" \
  "v_smfmac_f32_16x16x64_bf16 v[192:195], v[244:247], v[144:151], v251\n\t" \
  "v_smfmac_f32_16x16x64_bf16 v[196:199], v[244:247], v[152:159], v251\n\t" \
  "v_smfmac_f32_16x16x64_bf16 v[192:195], v[200:203], v[160:167], v251\n\t" \
  "v_smfmac_f32_16x16x64_bf16 v[196:199], v[200:203], v[168:175], v251\n\t" \
  "s_waitcnt lgkmcnt(0)\n\t" \
  "v_smfmac_f32_16x16x64_bf16 v[192:195], v[204:207], v[176:183], v251\n\t" \
  "v_smfmac_f32_16x16x64_bf16 v[196:199], v[204:207], v[184:191], v251\n\t" \
  "ds_read_b128 v[200:203], v248 offset:19456\n\t" \
  "ds_read_b128 v[204:207], v248 offset:23552\n\t" \
  "v_mov_b32 v250, v232\n\t" \
  "v_mov_b32 v249, v233\n\t" \
  "s_nop 2\n\t" \
  "v_add_f32 v192, v192, v194\n\t" \
  "v_add_f32 v193, v193, v195\n\t" \
  "v_add_f32 v196, v196, v198\n\t" \
  "v_add_f32 v197, v197, v199\n\t" \
  "v_cvt_pk_bf16_f32 v192, v192, v193\n\t" \
  "v_cvt_pk_bf16_f32 v196, v196, v197\n\t" \
  "ds_write2_b32 v252, v192, v196 offset1:64\n\t" \
  "v_mov_b32 v251, v234\n\t" \
  "ds_read_b64_tr_b16 v[244:245], v250 offset:1536\n\t" \
  "ds_read_b64_tr_b16 v[246:247], v250 offset:1600\n\t" \
  "v_mov_b32 v212, %[step]\n\t" \
  "s_mov_b64 exec, 1\n\t" \
  "ds_write_b32 v248, v212 offset:28720\n\t" \
  "s_mov_b64 exec, -1\n\t" \
  "buffer_load_dwordx4 v[192:195], v248, %[rt], %[svt] offen offset:0 sc1\n\t" \
  "buffer_load_dwordx4 v[196:199], v248, %[rt], %[svt] offen offset:1024 sc1\n\t" \
  "s_waitcnt lgkmcnt(1)\n\t" \
  "v_mfma_f32_16x16x32_bf16 v[200:203], v[200:203], v[244:247], 0\n\t" \
  "v_mfma_f32_16x16x32_bf16 v[204:207], v[204:207], v[244:247], 0\n\t" \
  "s_nop 7\n\t" \
  "v_cndmask_b32 v216, v200, v204, %[nh]\n\t" \
  "v_cndmask_b32 v217, v201, v205, %[nh]\n\t" \
  "v_cndmask_b32 v218, v202, v206, %[nh]\n\t" \
  "v_cndmask_b32 v219, v203, v207, %[nh]\n\t" \
  "ds_write_b128 v248, v[216:219] offset:19456\n\t" \
  "buffer_load_dword v252, v249, %[rs], %[sv] offen offset:0 sc1\n\t" \
  "buffer_load_dword v253, v249, %[rs], %[sv] offen offset:256 sc1\n\t" \
  "ds_read_b32 v212, v248 offset:28672\n\t" \
  "ds_read_b64_tr_b16 v[232:233], v250 offset:0\n\t" \
  "ds_read_b64_tr_b16 v[234:235], v250 offset:64\n\t" \
  "ds_read_b64_tr_b16 v[236:237], v250 offset:512\n\t" \
  "ds_read_b64_tr_b16 v[238:239], v250 offset:576\n\t" \
  "ds_read_b64_tr_b16 v[240:241], v250 offset:1024\n\t" \
  "ds_read_b64_tr_b16 v[242:243], v250 offset:1088\n\t" \
  "s_setprio 3\n\t" \
  "s_waitcnt lgkmcnt(0)\n\t" \
  "v_cmp_ne_u32 vcc, %[step], v212\n\t" \
  "s_barrier\n\t" \
  "s_and_b32 s93, vcc_lo, 0xf\n\t" \
  "s_cbranch_scc1 .Lre%=\n\t" \
  ".Lrb%=:\n\t" \
  "ds_read_b64_tr_b16 v[212:213], v250 offset:3584\n\t" \
  "ds_read_b64_tr_b16 v[214:215], v250 offset:3648\n\t" \
  "ds_read_b64_tr_b16 v[200:201], v250 offset:2048\n\t" \
  "ds_read_b64_tr_b16 v[202:203], v250 offset:2112\n\t" \
  "ds_read_b128 v[220:223], v248 offset:16384\n\t" \
  "ds_read_b128 v[224:227], v248 offset:17408\n\t" \
  "ds_read_b128 v[228:231], v248 offset:18432\n\t" \
  "ds_read_b64_tr_b16 v[204:205], v250 offset:2560\n\t" \
  "ds_read_b64_tr_b16 v[206:207], v250 offset:2624\n\t" \
  "ds_read_b64_tr_b16 v[208:209], v250 offset:3072\n\t" \
  "ds_read_b64_tr_b16 v[210:211], v250 offset:3136\n\t" \
  "s_waitcnt lgkmcnt(9)\n\t" \
  "v_mfma_f32_16x16x32_bf16 v[212:215], v[212:215], v[244:247], 0\n\t" \
  "s_waitcnt lgkmcnt(7)\n\t" \
  "v_mfma_f32_16x16x32_bf16 v[212:215], v[200:203], v[232:235], v[212:215]\n\t" \
  "s_waitcnt lgkmcnt(6)\n\t" \
  "v_pk_add_f32 v[216:217], v[216:217], v[220:221]\n\t" \
  "v_pk_add_f32 v[218:219], v[218:219], v[222:223]\n\t" \
  "s_waitcnt lgkmcnt(4)\n\t" \
  "v_pk_add_f32 v[224:225], v[224:225], v[228:229]\n\t" \
  "v_pk_add_f32 v[226:227], v[226:227], v[230:231]\n\t" \
  "v_pk_add_f32 v[216:217], v[216:217], v[224:225]\n\t" \
  "v_pk_add_f32 v[218:219], v[218:219], v[226:227]\n\t" \
  "s_waitcnt lgkmcnt(2)\n\t" \
  "v_mfma_f32_16x16x32_bf16 v[212:215], v[204:207], v[236:239], v[212:215]\n\t" \
  "v_max3_f32 v224, v216, v217, v218\n\t" \
  "v_max_f32 v224, v224, v219\n\t" \
  "s_waitcnt lgkmcnt(0)\n\t" \
  "v_mfma_f32_16x16x32_bf16 v[212:215], v[208:211], v[240:243], v[212:215]\n\t" \
  "v_max_f32_dpp v225, v224, v224 row_ror:8 row_mask:0xf bank_mask:0xf\n\t" \
  "v_max_f32_dpp v226, v224, v224 row_ror:8 row_mask:0xf bank_mask:0xf\n\t" \
  "s_nop 1\n\t" \
  "v_permlane32_swap_b32 v225, v226\n\t" \
  "v_max_f32 v231, v225, v226\n\t" \
  "v_mov_b32 v220, 0x3f803f80\n\t" \
  "v_mov_b32 v221, 0x3f803f80\n\t" \
  "v_mov_b64 v[222:223], v[220:221]\n\t" \
  "v_mov_b32 v245, 0\n\t" \
  "v_mov_b32 v246, 0\n\t" \
  "v_max3_f32 v200, v212, v213, v231\n\t" \
  "v_max3_f32 v201, v200, v214, v215\n\t" \
  "v_max3_f32 v202, v200, v214, v215\n\t" \
  "v_cndmask_b32 v212, v212, v214, %[up]\n\t" \
  "v_cndmask_b32 v213, v213, v215, %[up]\n\t" \
  "v_permlane16_swap_b32 v201, v202\n\t" \
  "v_max_f32 v200, v201, v202\n\t" \
  "v_mul_f32 v200, %[negc], v200\n\t" \
  "v_fma_f32 v212, v212, %[c], v200\n\t" \
  "v_fma_f32 v213, v213, %[c], v200\n\t" \
  "v_exp_f32 v212, v212\n\t" \
  "v_fma_f32 v216, v216, %[c], v200\n\t" \
  "v_exp_f32 v213, v213\n\t" \
  "v_fma_f32 v217, v217, %[c], v200\n\t" \
  "v_exp_f32 v216, v216\n\t" \
  "v_fma_f32 v218, v218, %[c], v200\n\t" \
  "v_cvt_pk_bf16_f32 v244, v212, v213\n\t" \
  "v_cvt_pk_bf16_f32 v247, v212, v213\n\t" \
  "v_exp_f32 v217, v217\n\t" \
  "v_fma_f32 v219, v219, %[c], v200\n\t" \
  "v_mfma_f32_16x16x16_bf16 v[208:211], v[220:221], v[244:245], 0\n\t" \
  "v_exp_f32 v218, v218\n\t" \
  "s_waitcnt vmcnt(0)\n\t" \
  "v_mfma_f32_16x16x16_bf16 v[224:227], v[252:253], v[244:245], 0\n\t" \
  "v_cvt_pk_bf16_f32 v206, v216, v217\n\t" \
  "v_cvt_pk_bf16_f32 v204, v216, v217\n\t" \
  "v_exp_f32 v219, v219\n\t" \
  "v_mfma_f32_16x16x16_bf16 v[228:231], v[252:253], v[246:247], 0\n\t" \
  "v_cvt_pk_bf16_f32 v207, v218, v219\n\t" \
  "v_cvt_pk_bf16_f32 v205, v218, v219\n\t" \
  "v_mov_b32_dpp v204, v206 row_ror:8 row_mask:0xf bank_mask:0xc\n\t" \
  "v_mov_b32_dpp v206, v206 row_ror:8 row_mask:0xf bank_mask:0x3\n\t" \
  "v_mov_b32_dpp v205, v207 row_ror:8 row_mask:0xf bank_mask:0xc\n\t" \
  "v_mov_b32_dpp v207, v207 row_ror:8 row_mask:0xf bank_mask:0x3\n\t" \
  "v_max_u32 v200, v252, v253\n\t" \
  "v_cmp_le_u32 vcc, %[sent], v200\n\t" \
  "v_mfma_f32_16x16x32_bf16 v[208:211], v[220:223], v[204:207], v[208:211]\n\t" \
  ".Lpv%=:\n\t" \
  "v_mfma_f32_16x16x32_bf16 v[224:227], v[192:195], v[204:207], v[224:227]\n\t" \
  "v_mfma_f32_16x16x32_bf16 v[228:231], v[196:199], v[204:207], v[228:231]\n\t" \
  "s_cbranch_vccnz .Lvs%=\n\t" \
  ".Lvok%=:\n\t" \
  "s_nop 4\n\t" \
  "v_rcp_f32 v200, v208\n\t" \
  "v_cndmask_b32 v224, v224, v226, %[nh]\n\t" \
  "v_cndmask_b32 v225, v225, v227, %[nh]\n\t" \
  "v_cndmask_b32 v228, v228, v230, %[nh]\n\t" \
  "v_cndmask_b32 v229, v229, v231, %[nh]\n\t" \
  "v_pk_mul_f32 v[224:225], v[224:225], v[200:201] op_sel_hi:[1,0]\n\t" \
  "v_pk_mul_f32 v[228:229], v[228:229], v[200:201] op_sel_hi:[1,0]\n\t" \
  "v_cvt_pk_bf16_f32 v224, v224, v225\n\t" \
  "v_cvt_pk_bf16_f32 v225, v228, v229\n\t" \
  "buffer_store_dwordx2 v[224:225], v251, %[rs], %[so] offen sc1\n\t" \
  "s_memtime s[90:91]\n\t" \
  "s_setprio 0\n\t" \
  "s_waitcnt lgkmcnt(0)\n\t" \
  "s_mov_b32 %[tout], s90\n\t" \
  "s_mov_b32 %[spins], s92\n\t" \
  "s_branch .Lend%=\n\t" \
  ".Lre%=:\n\t" \
  "ds_read_b64_tr_b16 v[232:233], v250 offset:0\n\t" \
  "ds_read_b64_tr_b16 v[234:235], v250 offset:64\n\t" \
  "ds_read_b64_tr_b16 v[236:237], v250 offset:512\n\t" \
  "ds_read_b64_tr_b16 v[238:239], v250 offset:576\n\t" \
  "ds_read_b64_tr_b16 v[240:241], v250 offset:1024\n\t" \
  "ds_read_b64_tr_b16 v[242:243], v250 offset:1088\n\t" \
  "s_waitcnt lgkmcnt(0)\n\t" \
  "s_branch .Lrb%=\n\t" \
  ".Lvs%=:\n\t" \
  "v_cmp_eq_u32 vcc, %[sent], v252\n\t" \
  "v_cmp_eq_u32 s[94:95], %[sent], v253\n\t" \
  "s_or_b64 s[94:95], s[94:95], vcc\n\t" \
  "s_cbranch_scc0 .Lvok%=\n\t" \
  "s_add_u32 s92, s92, 1\n\t" \
  "s_sleep 2\n\t" \
  "s_and_b32 s93, s92, 63\n\t" \
  "s_cbranch_scc1 .Lvr%=\n\t" \
  "buffer_load_dword v200, off, %[rs], %[sab] sc1\n\t" \
  "s_waitcnt vmcnt(0)\n\t" \
  "v_readfirstlane_b32 s93, v200\n\t" \
  "s_nop 3\n\t" \
  "s_cmp_eq_u32 s93, 1\n\t" \
  "s_cbranch_scc1 .Lvok%=\n\t" \
  "s_cmp_lt_u32 s92, 0x80000\n\t" \
  "s_cbranch_scc1 .Lvr%=\n\t" \
  "v_mov_b32 v200, 1\n\t" \
  "buffer_store_dword v200, off, %[rs], %[sab] sc1\n\t" \
  "s_branch .Lvok%=\n\t" \
  ".Lvr%=:\n\t" \
  "buffer_load_dword v252, v249, %[rs], %[sv] offen offset:0 sc1\n\t" \
  "buffer_load_dword v253, v249, %[rs], %[sv] offen offset:256 sc1\n\t" \
  "s_waitcnt vmcnt(0)\n\t" \
  "v_mfma_f32_16x16x16_bf16 v[224:227], v[252:253], v[244:245], 0\n\t" \
  "v_mfma_f32_16x16x16_bf16 v[228:231], v[252:253], v[246:247], 0\n\t" \
  "v_max_u32 v200, v252, v253\n\t" \
  "v_cmp_le_u32 vcc, %[sent], v200\n\t" \
  "s_nop 4\n\t" \
  "s_branch .Lpv%=\n\t" \
  "s_branch .Lend%=\n\t" \
  ".Lslow%=:\n\t" \
  "s_mov_b64 s[94:95], 0\n\t" \
  "v_cmp_eq_u32 vcc, %[sent], v200\n\t" \
  "s_or_b64 s[94:95], s[94:95], vcc\n\t" \
  "v_cmp_eq_u32 vcc, %[sent], v201\n\t" \
  "s_or_b64 s[94:95], s[94:95], vcc\n\t" \
  "v_cmp_eq_u32 vcc, %[sent], v202\n\t" \
  "s_or_b64 s[94:95], s[94:95], vcc\n\t" \
  "v_cmp_eq_u32 vcc, %[sent], v203\n\t" \
  "s_or_b64 s[94:95], s[94:95], vcc\n\t" \
  "v_cmp_eq_u32 vcc, %[sent], v204\n\t" \
  "s_or_b64 s[94:95], s[94:95], vcc\n\t" \
  "v_cmp_eq_u32 vcc, %[sent], v205\n\t" \
  "s_or_b64 s[94:95], s[94:95], vcc\n\t" \
  "v_cmp_eq_u32 vcc, %[sent], v206\n\t" \
  "s_or_b64 s[94:95], s[94:95], vcc\n\t" \
  "v_cmp_eq_u32 vcc, %[sent], v207\n\t" \
  "s_or_b64 s[94:95], s[94:95], vcc\n\t" \
  "s_cmp_eq_u64 s[94:95], 0\n\t" \
  "s_cbranch_scc1 .Lok%=\n\t" \
  "v_mov_b64 v[192:193], 0\n\t" \
  "v_mov_b64 v[194:195], 0\n\t" \
  "v_mov_b64 v[196:197], 0\n\t" \
  "v_mov_b64 v[198:199], 0\n\t" \
  "s_add_u32 s92, s92, 1\n\t" \
  "s_sleep 6\n\t" \
  "s_cmp_eq_u32 s92, 1\n\t" \
  "s_cbranch_scc1 .Lpoll%=\n\t" \
  "s_sleep 1\n\t" \
  "s_and_b32 s93, s92, 63\n\t" \
  "s_cmp_lg_u32 s93, 0\n\t" \
  "s_cbranch_scc1 .Lpoll%=\n\t" \
  "v_mov_b32 v245, 0\n\t" \
  "buffer_load_dword v244, v245, %[rs], %[sab] offen sc1\n\t" \
  "s_waitcnt vmcnt(0)\n\t" \
  "v_readfirstlane_b32 s93, v244\n\t" \
  "s_nop 3\n\t" \
  "s_cmp_eq_u32 s93, 1\n\t" \
  "s_cbranch_scc1 .Lok%=\n\t" \
  "s_cmp_lt_u32 s92, 0x80000\n\t" \
  "s_cbranch_scc1 .Lpoll%=\n\t" \
  "v_mov_b32 v244, 1\n\t" \
  "buffer_store_dword v244, v245, %[rs], %[sab] offen sc1\n\t" \
  "s_branch .Lok%=\n\t" \
  ".Lend%=:\n\t"
#define QK_ASM_4 \
  "s_waitcnt lgkmcnt(0)\n\t" \
  "v_subrev_u32 v244, 0x8000, v250\n\t" \
  "ds_read_b128 v[216:219], v248 offset:49152\n\t" \
  "ds_read_b128 v[220:223], v248 offset:50176\n\t" \
  "ds_read_b128 v[224:227], v248 offset:51200\n\t" \
  "ds_read_b128 v[228:231], v248 offset:52224\n\t" \
  "ds_read_b128 v[208:211], v244 offset:0\n\t" \
  "ds_read_b128 v[212:215], v244 offset:1024\n\t" \
  "ds_read_b128 v[236:239], v244 offset:2048\n\t" \
  "ds_read_b128 v[240:243], v244 offset:3072\n\t" \
  "v_mov_b64 v[192:193], 0\n\t" \
  "v_mov_b64 v[194:195], 0\n\t" \
  "v_mov_b64 v[196:197], 0\n\t" \
  "v_mov_b64 v[198:199], 0\n\t" \
  "s_movk_i32 s92, 0x2000\n\t" \
  ".Lwt%=:\n\t" \
  "ds_read_b64 v[200:201], v248 offset:30720\n\t" \
  "s_memrealtime s[90:91]\n\t" \
  "s_sub_u32 s92, s92, 1\n\t" \
  "s_waitcnt lgkmcnt(0)\n\t" \
  "v_readfirstlane_b32 s93, v200\n\t" \
  "v_readfirstlane_b32 %[tgt], v201\n\t" \
  "s_cbranch_scc1 .Lgo%=\n\t" \
  "s_cmp_eq_u32 s93, %[step]\n\t" \
  "s_cbranch_scc1 .Lwx%=\n\t" \
  "s_sleep 1\n\t" \
  "s_branch .Lwt%=\n\t" \
  ".Lwx%=:\n\t" \
  "s_lshl_b32 s93, s90, 2\n\t" \
  "s_sub_i32 s93, %[tgt], s93\n\t" \
  "s_cmp_le_i32 s93, 0\n\t" \
  "s_cbranch_scc1 .Lgo%=\n\t" \
  "s_min_i32 s93, s93, 8000\n\t" \
  "s_mov_b32 s94, s93\n\t" \
  ".Lsl%=:\n\t" \
  "s_sub_i32 s94, s94, 18\n\t" \
  "s_cmp_lt_i32 s94, 30\n\t" \
  "s_cbranch_scc1 .Lsx%=\n\t" \
  "s_sleep 1\n\t" \
  "s_branch .Lsl%=\n\t" \
  ".Lsx%=:\n\t" \
  "s_sub_i32 s93, s93, 12\n\t" \
  "s_max_i32 s93, s93, 4\n\t" \
  "s_lshr_b32 s93, s93, 2\n\t" \
  "s_add_u32 s93, s93, s90\n\t" \
  "s_memrealtime s[90:91]\n\t" \
  ".Lpace%=:\n\t" \
  "s_memrealtime s[94:95]\n\t" \
  "s_waitcnt lgkmcnt(1)\n\t" \
  "s_sub_i32 s92, s90, s93\n\t" \
  "s_cmp_ge_i32 s92, 0\n\t" \
  "s_cbranch_scc1 .Le%=\n\t" \
  "s_memrealtime s[90:91]\n\t" \
  "s_waitcnt lgkmcnt(1)\n\t" \
  "s_sub_i32 s92, s94, s93\n\t" \
  "s_cmp_lt_i32 s92, 0\n\t" \
  "s_cbranch_scc1 .Lpace%=\n\t" \
  "s_nop 4\n\t" \
  ".Le%=:\n\t" \
  "s_add_u32 s92, s92, s93\n\t" \
  "s_lshl_b32 s92, s92, 2\n\t" \
  "s_sub_i32 s92, %[tgt], s92\n\t" \
  "s_max_i32 s92, s92, 0\n\t" \
  "s_bitcmp0_b32 s92, 3\n\t" \
  "s_cbranch_scc1 .Lf3%=\n\t" \
  "s_nop 15\n\t" \
  ".Lf3%=:\n\t" \
  "s_bitcmp0_b32 s92, 2\n\t" \
  "s_cbranch_scc1 .Lf2%=\n\t" \
  "s_nop 10\n\t" \
  ".Lf2%=:\n\t" \
  ".Lgo%=:\n\t" \
  "s_mov_b32 s92, 0\n\t" \
  ".Lpoll%=:\n\t" \
  "buffer_load_dwordx4 v[200:203], v248, %[rs], %[som] offen sc1\n\t" \
  "buffer_load_dwordx4 v[204:207], v248, %[rs], %[som2] offen sc1\n\t" \
  "s_waitcnt vmcnt(1) lgkmcnt(0)\n\t" \
  "ds_write_b128 v249, v[200:203]\n\t" \
  "v_smfmac_f32_16x16x64_bf16 v[192:195], v[200:203], v[208:215], v251\n\t" \
  "v_max3_u32 v244, v200, v201, v202\n\t" \
  "v_smfmac_f32_16x16x64_bf16 v[196:199], v[200:203], v[236:243], v251\n\t" \
  "s_waitcnt vmcnt(0)\n\t" \
  "ds_write_b128 v249, v[204:207] offset:8192\n\t" \
  "v_smfmac_f32_16x16x64_bf16 v[192:195], v[204:207], v[216:223], v251\n\t" \
  "v_max3_u32 v244, v244, v203, v204\n\t" \
  "v_max3_u32 v244, v244, v205, v206\n\t" \
  "v_smfmac_f32_16x16x64_bf16 v[196:199], v[204:207], v[224:231], v251\n\t" \
  "v_max_u32 v244, v244, v207\n\t" \
  "v_cmp_le_u32 vcc, %[sent], v244\n\t" \
  "s_cbranch_vccnz .Lslow%=\n\t" \
  ".Lok%=:\n\t" \
  "ds_read_b128 v[216:219], v250 offset:32768\n\t" \
  "ds_read_b128 v[220:223], v250 offset:33792\n\t" \
  "ds_read_b128 v[224:227], v250 offset:34816\n\t" \
  "ds_read_b128 v[228:231], v250 offset:35840\n\t" \
  "s_waitcnt lgkmcnt(4)\n\t" \
  "s_barrier\n\t" \
  "ds_read_b128 v[208:211], v248 offset:5120\n\t" \
  "ds_read_b128 v[212:215], v248 offset:6144\n\t" \
  "ds_read_b128 v[236:239], v248 offset:7168\n\t" \
  "ds_read_b128 v[240:243], v248 offset:8192\n\t" \
  "s_waitcnt lgkmcnt(3)\n\t" \
  "ds_read_b128 v[244:247], v248 offset:13312\n\t" \
  "v_smfmac_f32_16x16x64_bf16 v[192:195], v[208:211], v[216:223], v251\n\t" \
  "ds_read_b128 v[216:219], v250 offset:0\n\t" \
  "v_smfmac_f32_16x16x64_bf16 v[196:199], v[208:211], v[224:231], v251\n\t" \
  "s_waitcnt lgkmcnt(4)\n\t" \
  "ds_read_b128 v[220:223], v250 offset:1024\n\t" \
  "v_smfmac_f32_16x16x64_bf16 v[192:195], v[212:215], v[0:7], v251\n\t" \
  "ds_read_b128 v[224:227], v250 offset:2048\n\t" \
  "v_smfmac_f32_16x16x64_bf16 v[196:199], v[212:215], v[8:15], v251\n\t" \
  "s_waitcnt lgkmcnt(5)\n\t" \
  "ds_read_b128 v[228:231], v250 offset:3072\n\t" \
  "v_smfmac_f32_16x16x64_bf16 v[192:195], v[236:239], v[16:23], v251\n\t" \
  "ds_read_b128 v[200:203], v248 offset:9216\n\t" \
  "v_smfmac_f32_16x16x64_bf16 v[196:199], v[236:239], v[24:31], v251\n\t" \
  "s_waitcnt lgkmcnt(6)\n\t" \
  "ds_read_b128 v[204:207], v248 offset:10240\n\t" \
  "v_smfmac_f32_16x16x64_bf16 v[192:195], v[240:243], v[32:39], v251\n\t" \
  "ds_read_b128 v[208:211], v248 offset:11264\n\t" \
  "v_smfmac_f32_16x16x64_bf16 v[196:199], v[240:243], v[40:47], v251\n\t" \
  "s_waitcnt lgkmcnt(5)\n\t" \
  "ds_read_b128 v[212:215], v248 offset:14336\n\t" \
  "v_smfmac_f32_16x16x64_bf16 v[192:195], v[244:247], v[216:223], v251\n\t" \
  "s_waitcnt lgkmcnt(4)\n\t" \
  "v_smfmac_f32_16x16x64_bf16 v[196:199], v[244:247], v[224:231], v251\n\t" \
  "s_waitcnt lgkmcnt(3)\n\t" \
  "ds_read_b128 v[236:239], v248 offset:15360\n\t" \
  "v_smfmac_f32_16x16x64_bf16 v[192:195], v[200:203], v[48:55], v251\n\t" \
  "ds_read_b128 v[240:243], v248 offset:0\n\t" \
  "v_smfmac_f32_16x16x64_bf16 v[196:199], v[200:203], v[56:63], v251\n\t" \
  "s_waitcnt lgkmcnt(4)\n\t" \
  "v_smfmac_f32_16x16x64_bf16 v[192:195], v[204:207], v[64:71], v251\n\t" \
  "ds_read_b128 v[244:247], v248 offset:1024\n\t" \
  "v_smfmac_f32_16x16x64_bf16 v[196:199], v[204:207], v[72:79], v251\n\t" \
  "s_waitcnt lgkmcnt(4)\n\t" \
  "v_smfmac_f32_16x16x64_bf16 v[192:195], v[208:211], v[80:87], v251\n\t" \
  "ds_read_b128 v[200:203], v248 offset:2048\n\t" \
  "v_smfmac_f32_16x16x64_bf16 v[196:199], v[208:211], v[88:95], v251\n\t" \
  "s_waitcnt lgkmcnt(4)\n\t" \
  "v_smfmac_f32_16x16x64_bf16 v[192:195], v[212:215], v[96:103], v251\n\t" \
  "ds_read_b128 v[204:207], v248 offset:3072\n\t" \
  "v_smfmac_f32_16x16x64_bf16 v[196:199], v[212:215], v[104:111], v251\n\t" \
  "s_waitcnt lgkmcnt(3)\n\t" \
  "v_smfmac_f32_16x16x64_bf16 v[192:195], v[236:239], v[112:119], v251\n\t" \
  "v_smfmac_f32_16x16x64_bf16 v[196:199], v[236:239], v[120:127], v251\n\t" \
  "v_smfmac_f32_16x16x64_bf16 v[192:195], v[240:243], v[128:135], v251\n\t" \
  "v_smfmac_f32_16x16x64_bf16 v[196:199], v[240:243], v[136:143], v251\n\t" \
  "s_waitcnt lgkmcnt(1)\n\t" \
  "v_smfmac_f32_16x16x64_bf16 v[192:195], v[244:247], v[144:151], v251\n\t" \
  "v_smfmac_f32_16x16x64_bf16 v[196:199], v[244:247], v[152:159], v251\n\t" \
  "v_smfmac_f32_16x16x64_bf16 v[192:195], v[200:203], v[160:167], v251\n\t" \
  "v_smfmac_f32_16x16x64_bf16 v[196:199], v[200:203], v[168:175], v251\n\t" \
  "s_waitcnt lgkmcnt(0)\n\t" \
  "v_smfmac_f32_16x16x64_bf16 v[192:195], v[204:207], v[176:183], v251\n\t" \
  "v_smfmac_f32_16x16x64_bf16 v[196:199], v[204:207], v[184:191], v251\n\t" \
  "s_nop 6\n\t" \
  "v_add_f32 v192, v192, v194\n\t" \
  "v_add_f32 v193, v193, v195\n\t" \
  "v_add_f32 v196, v196, v198\n\t" \
  "v_add_f32 v197, v197, v199\n\t" \
  "v_cvt_pk_bf16_f32 v192, v192, v193\n\t" \
  "v_cvt_pk_bf16_f32 v196, v196, v197\n\t" \
  "ds_write2_b32 v252, v192, v196 offset1:64\n\t" \
  "s_waitcnt lgkmcnt(0)\n\t" \
  "s_barrier\n\t" \
  "buffer_store_dword v192, v235, %[rs], %[sk] offen sc1\n\t" \
  "buffer_store_dword v196, v235, %[rs], %[sk] offen offset:256 sc1\n\t" \
  "s_mov_b32 %[spins], s92\n\t" \
  "s_branch .Lend%=\n\t" \
  ".Lslow%=:\n\t" \
  "s_mov_b64 s[94:95], 0\n\t" \
  "v_cmp_eq_u32 vcc, %[sent], v200\n\t" \
  "s_or_b64 s[94:95], s[94:95], vcc\n\t" \
  "v_cmp_eq_u32 vcc, %[sent], v201\n\t" \
  "s_or_b64 s[94:95], s[94:95], vcc\n\t" \
  "v_cmp_eq_u32 vcc, %[sent], v202\n\t" \
  "s_or_b64 s[94:95], s[94:95], vcc\n\t" \
  "v_cmp_eq_u32 vcc, %[sent], v203\n\t" \
  "s_or_b64 s[94:95], s[94:95], vcc\n\t" \
  "v_cmp_eq_u32 vcc, %[sent], v204\n\t" \
  "s_or_b64 s[94:95], s[94:95], vcc\n\t" \
  "v_cmp_eq_u32 vcc, %[sent], v205\n\t" \
  "s_or_b64 s[94:95], s[94:95], vcc\n\t" \
  "v_cmp_eq_u32 vcc, %[sent], v206\n\t" \
  "s_or_b64 s[94:95], s[94:95], vcc\n\t" \
  "v_cmp_eq_u32 vcc, %[sent], v207\n\t" \
  "s_or_b64 s[94:95], s[94:95], vcc\n\t" \
  "s_cmp_eq_u64 s[94:95], 0\n\t" \
  "s_cbranch_scc1 .Lok%=\n\t" \
  "v_mov_b64 v[192:193], 0\n\t" \
  "v_mov_b64 v[194:195], 0\n\t" \
  "v_mov_b64 v[196:197], 0\n\t" \
  "v_mov_b64 v[198:199], 0\n\t" \
  "s_add_u32 s92, s92, 1\n\t" \
  "s_sleep 6\n\t" \
  "s_cmp_eq_u32 s92, 1\n\t" \
  "s_cbranch_scc1 .Lpoll%=\n\t" \
  "s_sleep 1\n\t" \
  "s_and_b32 s93, s92, 63\n\t" \
  "s_cmp_lg_u32 s93, 0\n\t" \
  "s_cbranch_scc1 .Lpoll%=\n\t" \
  "v_mov_b32 v245, 0\n\t" \
  "buffer_load_dword v244, v245, %[rs], %[sab] offen sc1\n\t" \
  "s_waitcnt vmcnt(0)\n\t" \
  "v_readfirstlane_b32 s93, v244\n\t" \
  "s_nop 3\n\t" \
  "s_cmp_eq_u32 s93, 1\n\t" \
  "s_cbranch_scc1 .Lok%=\n\t" \
  "s_cmp_lt_u32 s92, 0x80000\n\t" \
  "s_cbranch_scc1 .Lpoll%=\n\t" \
  "v_mov_b32 v244, 1\n\t" \
  "buffer_store_dword v244, v245, %[rs], %[sab] offen sc1\n\t" \
  "s_branch .Lok%=\n\t" \
  ".Lend%=:\n\t"
#define QK_ASM_5 \
  "s_waitcnt lgkmcnt(0)\n\t" \
  "v_subrev_u32 v244, 0x8000, v250\n\t" \
  "ds_read_b128 v[216:219], v248 offset:53248\n\t" \
  "ds_read_b128 v[220:223], v248 offset:54272\n\t" \
  "ds_read_b128 v[224:227], v248 offset:55296\n\t" \
  "ds_read_b128 v[228:231], v248 offset:56320\n\t" \
  "ds_read_b128 v[208:211], v244 offset:0\n\t" \
  "ds_read_b128 v[212:215], v244 offset:1024\n\t" \
  "ds_read_b128 v[236:239], v244 offset:2048\n\t" \
  "ds_read_b128 v[240:243], v244 offset:3072\n\t" \
  "v_mov_b64 v[192:193], 0\n\t" \
  "v_mov_b64 v[194:195], 0\n\t" \
  "v_mov_b64 v[196:197], 0\n\t" \
  "v_mov_b64 v[198:199], 0\n\t" \
  "s_movk_i32 s92, 0x2000\n\t" \
  ".Lwt%=:\n\t" \
  "ds_read_b64 v[200:201], v248 offset:30720\n\t" \
  "s_memrealtime s[90:91]\n\t" \
  "s_sub_u32 s92, s92, 1\n\t" \
  "s_waitcnt lgkmcnt(0)\n\t" \
  "v_readfirstlane_b32 s93, v200\n\t" \
  "v_readfirstlane_b32 %[tgt], v201\n\t" \
  "s_cbranch_scc1 .Lgo%=\n\t" \
  "s_cmp_eq_u32 s93, %[step]\n\t" \
  "s_cbranch_scc1 .Lwx%=\n\t" \
  "s_sleep 1\n\t" \
  "s_branch .Lwt%=\n\t" \
  ".Lwx%=:\n\t" \
  "s_lshl_b32 s93, s90, 2\n\t" \
  "s_sub_i32 s93, %[tgt], s93\n\t" \
  "s_cmp_le_i32 s93, 0\n\t" \
  "s_cbranch_scc1 .Lgo%=\n\t" \
  "s_min_i32 s93, s93, 8000\n\t" \
  "s_mov_b32 s94, s93\n\t" \
  ".Lsl%=:\n\t" \
  "s_sub_i32 s94, s94, 18\n\t" \
  "s_cmp_lt_i32 s94, 30\n\t" \
  "s_cbranch_scc1 .Lsx%=\n\t" \
  "s_sleep 1\n\t" \
  "s_branch .Lsl%=\n\t" \
  ".Lsx%=:\n\t" \
  "s_sub_i32 s93, s93, 12\n\t" \
  "s_max_i32 s93, s93, 4\n\t" \
  "s_lshr_b32 s93, s93, 2\n\t" \
  "s_add_u32 s93, s93, s90\n\t" \
  "s_memrealtime s[90:91]\n\t" \
  ".Lpace%=:\n\t" \
  "s_memrealtime s[94:95]\n\t" \
  "s_waitcnt lgkmcnt(1)\n\t" \
  "s_sub_i32 s92, s90, s93\n\t" \
  "s_cmp_ge_i32 s92, 0\n\t" \
  "s_cbranch_scc1 .Le%=\n\t" \
  "s_memrealtime s[90:91]\n\t" \
  "s_waitcnt lgkmcnt(1)\n\t" \
  "s_sub_i32 s92, s94, s93\n\t" \
  "s_cmp_lt_i32 s92, 0\n\t" \
  "s_cbranch_scc1 .Lpace%=\n\t" \
  "s_nop 4\n\t" \
  ".Le%=:\n\t" \
  "s_add_u32 s92, s92, s93\n\t" \
  "s_lshl_b32 s92, s92, 2\n\t" \
  "s_sub_i32 s92, %[tgt], s92\n\t" \
  "s_max_i32 s92, s92, 0\n\t" \
  "s_bitcmp0_b32 s92, 3\n\t" \
  "s_cbranch_scc1 .Lf3%=\n\t" \
  "s_nop 15\n\t" \
  ".Lf3%=:\n\t" \
  "s_bitcmp0_b32 s92, 2\n\t" \
  "s_cbranch_scc1 .Lf2%=\n\t" \
  "s_nop 10\n\t" \
  ".Lf2%=:\n\t" \
  ".Lgo%=:\n\t" \
  "s_mov_b32 s92, 0\n\t" \
  ".Lpoll%=:\n\t" \
  "buffer_load_dwordx4 v[200:203], v248, %[rs], %[som] offen sc1\n\t" \
  "buffer_load_dwordx4 v[204:207], v248, %[rs], %[som2] offen sc1\n\t" \
  "s_waitcnt vmcnt(1) lgkmcnt(0)\n\t" \
  "ds_write_b128 v249, v[200:203]\n\t" \
  "v_smfmac_f32_16x16x64_bf16 v[192:195], v[200:203], v[208:215], v251\n\t" \
  "v_max3_u32 v244, v200, v201, v202\n\t" \
  "v_smfmac_f32_16x16x64_bf16 v[196:199], v[200:203], v[236:243], v251\n\t" \
  "s_waitcnt vmcnt(0)\n\t" \
  "ds_write_b128 v249, v[204:207] offset:8192\n\t" \
  "v_smfmac_f32_16x16x64_bf16 v[192:195], v[204:207], v[216:223], v251\n\t" \
  "v_max3_u32 v244, v244, v203, v204\n\t" \
  "v_max3_u32 v244, v244, v205, v206\n\t" \
  "v_smfmac_f32_16x16x64_bf16 v[196:199], v[204:207], v[224:231], v251\n\t" \
  "v_max_u32 v244, v244, v207\n\t" \
  "v_cmp_le_u32 vcc, %[sent], v244\n\t" \
  "s_cbranch_vccnz .Lslow%=\n\t" \
  ".Lok%=:\n\t" \
  "ds_read_b128 v[216:219], v250 offset:32768\n\t" \
  "ds_read_b128 v[220:223], v250 offset:33792\n\t" \
  "ds_read_b128 v[224:227], v250 offset:34816\n\t" \
  "ds_read_b128 v[228:231], v250 offset:35840\n\t" \
  "s_waitcnt lgkmcnt(4)\n\t" \
  "s_barrier\n\t" \
  "ds_read_b128 v[208:211], v248 offset:4096\n\t" \
  "ds_read_b128 v[212:215], v248 offset:7168\n\t" \
  "ds_read_b128 v[236:239], v248 offset:8192\n\t" \
  "ds_read_b128 v[240:243], v248 offset:9216\n\t" \
  "s_waitcnt lgkmcnt(3)\n\t" \
  "ds_read_b128 v[244:247], v248 offset:12288\n\t" \
  "v_smfmac_f32_16x16x64_bf16 v[192:195], v[208:211], v[216:223], v251\n\t" \
  "ds_read_b128 v[216:219], v250 offset:0\n\t" \
  "v_smfmac_f32_16x16x64_bf16 v[196:199], v[208:211], v[224:231], v251\n\t" \
  "s_waitcnt lgkmcnt(4)\n\t" \
  "ds_read_b128 v[220:223], v250 offset:1024\n\t" \
  "v_smfmac_f32_16x16x64_bf16 v[192:195], v[212:215], v[0:7], v251\n\t" \
  "ds_read_b128 v[224:227], v250 offset:2048\n\t" \
  "v_smfmac_f32_16x16x64_bf16 v[196:199], v[212:215], v[8:15], v251\n\t" \
  "s_waitcnt lgkmcnt(5)\n\t" \
  "ds_read_b128 v[228:231], v250 offset:3072\n\t" \
  "v_smfmac_f32_16x16x64_bf16 v[192:195], v[236:239], v[16:23], v251\n\t" \
  "ds_read_b128 v[200:203], v248 offset:10240\n\t" \
  "v_smfmac_f32_16x16x64_bf16 v[196:199], v[236:239], v[24:31], v251\n\t" \
  "s_waitcnt lgkmcnt(6)\n\t" \
  "ds_read_b128 v[204:207], v248 offset:11264\n\t" \
  "v_smfmac_f32_16x16x64_bf16 v[192:195], v[240:243], v[32:39], v251\n\t" \
  "ds_read_b128 v[208:211], v248 offset:14336\n\t" \
  "v_smfmac_f32_16x16x64_bf16 v[196:199], v[240:243], v[40:47], v251\n\t" \
  "s_waitcnt lgkmcnt(5)\n\t" \
  "ds_read_b128 v[212:215], v248 offset:15360\n\t" \
  "v_smfmac_f32_16x16x64_bf16 v[192:195], v[244:247], v[216:223], v251\n\t" \
  "s_waitcnt lgkmcnt(4)\n\t" \
  "v_smfmac_f32_16x16x64_bf16 v[196:199], v[244:247], v[224:231], v251\n\t" \
  "s_waitcnt lgkmcnt(3)\n\t" \
  "ds_read_b128 v[236:239], v248 offset:0\n\t" \
  "v_smfmac_f32_16x16x64_bf16 v[192:195], v[200:203], v[48:55], v251\n\t" \
  "ds_read_b128 v[240:243], v248 offset:1024\n\t" \
  "v_smfmac_f32_16x16x64_bf16 v[196:199], v[200:203], v[56:63], v251\n\t" \
  "s_waitcnt lgkmcnt(4)\n\t" \
  "v_smfmac_f32_16x16x64_bf16 v[192:195], v[204:207], v[64:71], v251\n\t" \
  "ds_read_b128 v[244:247], v248 offset:2048\n\t" \
  "v_smfmac_f32_16x16x64_bf16 v[196:199], v[204:207], v[72:79], v251\n\t" \
  "s_waitcnt lgkmcnt(4)\n\t" \
  "v_smfmac_f32_16x16x64_bf16 v[192:195], v[208:211], v[80:87], v251\n\t" \
  "ds_read_b128 v[200:203], v248 offset:3072\n\t" \
  "v_smfmac_f32_16x16x64_bf16 v[196:199], v[208:211], v[88:95], v251\n\t" \
  "s_waitcnt lgkmcnt(4)\n\t" \
  "v_smfmac_f32_16x16x64_bf16 v[192:195], v[212:215], v[96:103], v251\n\t" \
  "ds_read_b128 v[204:207], v248 offset:6144\n\t" \
  "v_smfmac_f32_16x16x64_bf16 v[196:199], v[212:215], v[104:111], v251\n\t" \
  "s_waitcnt lgkmcnt(3)\n\t" \
  "v_smfmac_f32_16x16x64_bf16 v[192:195], v[236:239], v[112:119], v251\n\t" \
  "v_smfmac_f32_16x16x64_bf16 v[196:199], v[236:239], v[120:127], v251\n\t" \
  "v_smfmac_f32_16x16x64_bf16 v[192:195], v[240:243], v[128:135], v251\n\t" \
  "v_smfmac_f32_16x16x64_bf16 v[196:199], v[240:243], v[136:143], v251\n\t" \
  "s_waitcnt lgkmcnt(1)\n\t" \
  "v_smfmac_f32_16x16x64_bf16 v[192:195], v[244:247], v[144:151], v251\n\t" \
  "v_smfmac_f32_16x16x64_bf16 v[196:199], v[244:247], v[152:159], v251\n\t" \
  "v_smfmac_f32_16x16x64_bf16 v[192:195], v[200:203], v[160:167], v251\n\t" \
  "v_smfmac_f32_16x16x64_bf16 v[196:199], v[200:203], v[168:175], v251\n\t" \
  "s_waitcnt lgkmcnt(0)\n\t" \
  "v_smfmac_f32_16x16x64_bf16 v[192:195], v[204:207], v[176:183], v251\n\t" \
  "v_smfmac_f32_16x16x64_bf16 v[196:199], v[204:207], v[184:191], v251\n\t" \
  "s_nop 6\n\t" \
  "v_add_f32 v192, v192, v194\n\t" \
  "v_add_f32 v193, v193, v195\n\t" \
  "v_add_f32 v196, v196, v198\n\t" \
  "v_add_f32 v197, v197, v199\n\t" \
  "v_cvt_pk_bf16_f32 v192, v192, v193\n\t" \
  "v_cvt_pk_bf16_f32 v196, v196, v197\n\t" \
  "ds_write2_b32 v252, v192, v196 offset1:64\n\t" \
  "s_waitcnt lgkmcnt(0)\n\t" \
  "s_barrier\n\t" \
  "buffer_store_dword v192, v235, %[rs], %[sk] offen sc1\n\t" \
  "buffer_store_dword v196, v235, %[rs], %[sk] offen offset:256 sc1\n\t" \
  "s_mov_b32 %[spins], s92\n\t" \
  "s_branch .Lend%=\n\t" \
  ".Lslow%=:\n\t" \
  "s_mov_b64 s[94:95], 0\n\t" \
  "v_cmp_eq_u32 vcc, %[sent], v200\n\t" \
  "s_or_b64 s[94:95], s[94:95], vcc\n\t" \
  "v_cmp_eq_u32 vcc, %[sent], v201\n\t" \
  "s_or_b64 s[94:95], s[94:95], vcc\n\t" \
  "v_cmp_eq_u32 vcc, %[sent], v202\n\t" \
  "s_or_b64 s[94:95], s[94:95], vcc\n\t" \
  "v_cmp_eq_u32 vcc, %[sent], v203\n\t" \
  "s_or_b64 s[94:95], s[94:95], vcc\n\t" \
  "v_cmp_eq_u32 vcc, %[sent], v204\n\t" \
  "s_or_b64 s[94:95], s[94:95], vcc\n\t" \
  "v_cmp_eq_u32 vcc, %[sent], v205\n\t" \
  "s_or_b64 s[94:95], s[94:95], vcc\n\t" \
  "v_cmp_eq_u32 vcc, %[sent], v206\n\t" \
  "s_or_b64 s[94:95], s[94:95], vcc\n\t" \
  "v_cmp_eq_u32 vcc, %[sent], v207\n\t" \
  "s_or_b64 s[94:95], s[94:95], vcc\n\t" \
  "s_cmp_eq_u64 s[94:95], 0\n\t" \
  "s_cbranch_scc1 .Lok%=\n\t" \
  "v_mov_b64 v[192:193], 0\n\t" \
  "v_mov_b64 v[194:195], 0\n\t" \
  "v_mov_b64 v[196:197], 0\n\t" \
  "v_mov_b64 v[198:199], 0\n\t" \
  "s_add_u32 s92, s92, 1\n\t" \
  "s_sleep 6\n\t" \
  "s_cmp_eq_u32 s92, 1\n\t" \
  "s_cbranch_scc1 .Lpoll%=\n\t" \
  "s_sleep 1\n\t" \
  "s_and_b32 s93, s92, 63\n\t" \
  "s_cmp_lg_u32 s93, 0\n\t" \
  "s_cbranch_scc1 .Lpoll%=\n\t" \
  "v_mov_b32 v245, 0\n\t" \
  "buffer_load_dword v244, v245, %[rs], %[sab] offen sc1\n\t" \
  "s_waitcnt vmcnt(0)\n\t" \
  "v_readfirstlane_b32 s93, v244\n\t" \
  "s_nop 3\n\t" \
  "s_cmp_eq_u32 s93, 1\n\t" \
  "s_cbranch_scc1 .Lok%=\n\t" \
  "s_cmp_lt_u32 s92, 0x80000\n\t" \
  "s_cbranch_scc1 .Lpoll%=\n\t" \
  "v_mov_b32 v244, 1\n\t" \
  "buffer_store_dword v244, v245, %[rs], %[sab] offen sc1\n\t" \
  "s_branch .Lok%=\n\t" \
  ".Lend%=:\n\t"
#define QK_ASM_6 \
  "s_waitcnt lgkmcnt(0)\n\t" \
  "v_subrev_u32 v244, 0x8000, v250\n\t" \
  "ds_read_b128 v[216:219], v248 offset:57344\n\t" \
  "ds_read_b128 v[220:223], v248 offset:58368\n\t" \
  "ds_read_b128 v[224:227], v248 offset:59392\n\t" \
  "ds_read_b128 v[228:231], v248 offset:60416\n\t" \
  "ds_read_b128 v[208:211], v244 offset:0\n\t" \
  "ds_read_b128 v[212:215], v244 offset:1024\n\t" \
  "ds_read_b128 v[236:239], v244 offset:2048\n\t" \
  "ds_read_b128 v[240:243], v244 offset:3072\n\t" \
  "v_mov_b64 v[192:193], 0\n\t" \
  "v_mov_b64 v[194:195], 0\n\t" \
  "v_mov_b64 v[196:197], 0\n\t" \
  "v_mov_b64 v[198:199], 0\n\t" \
  "s_movk_i32 s92, 0x2000\n\t" \
  ".Lwt%=:\n\t" \
  "ds_read_b64 v[200:201], v248 offset:30720\n\t" \
  "s_memrealtime s[90:91]\n\t" \
  "s_sub_u32 s92, s92, 1\n\t" \
  "s_waitcnt lgkmcnt(0)\n\t" \
  "v_readfirstlane_b32 s93, v200\n\t" \
  "v_readfirstlane_b32 %[tgt], v201\n\t" \
  "s_cbranch_scc1 .Lgo%=\n\t" \
  "s_cmp_eq_u32 s93, %[step]\n\t" \
  "s_cbranch_scc1 .Lwx%=\n\t" \
  "s_sleep 1\n\t" \
  "s_branch .Lwt%=\n\t" \
  ".Lwx%=:\n\t" \
  "s_lshl_b32 s93, s90, 2\n\t" \
  "s_sub_i32 s93, %[tgt], s93\n\t" \
  "s_cmp_le_i32 s93, 0\n\t" \
  "s_cbranch_scc1 .Lgo%=\n\t" \
  "s_min_i32 s93, s93, 8000\n\t" \
  "s_mov_b32 s94, s93\n\t" \
  ".Lsl%=:\n\t" \
  "s_sub_i32 s94, s94, 18\n\t" \
  "s_cmp_lt_i32 s94, 30\n\t" \
  "s_cbranch_scc1 .Lsx%=\n\t" \
  "s_sleep 1\n\t" \
  "s_branch .Lsl%=\n\t" \
  ".Lsx%=:\n\t" \
  "s_sub_i32 s93, s93, 12\n\t" \
  "s_max_i32 s93, s93, 4\n\t" \
  "s_lshr_b32 s93, s93, 2\n\t" \
  "s_add_u32 s93, s93, s90\n\t" \
  "s_memrealtime s[90:91]\n\t" \
  ".Lpace%=:\n\t" \
  "s_memrealtime s[94:95]\n\t" \
  "s_waitcnt lgkmcnt(1)\n\t" \
  "s_sub_i32 s92, s90, s93\n\t" \
  "s_cmp_ge_i32 s92, 0\n\t" \
  "s_cbranch_scc1 .Le%=\n\t" \
  "s_memrealtime s[90:91]\n\t" \
  "s_waitcnt lgkmcnt(1)\n\t" \
  "s_sub_i32 s92, s94, s93\n\t" \
  "s_cmp_lt_i32 s92, 0\n\t" \
  "s_cbranch_scc1 .Lpace%=\n\t" \
  "s_nop 4\n\t" \
  ".Le%=:\n\t" \
  "s_add_u32 s92, s92, s93\n\t" \
  "s_lshl_b32 s92, s92, 2\n\t" \
  "s_sub_i32 s92, %[tgt], s92\n\t" \
  "s_max_i32 s92, s92, 0\n\t" \
  "s_bitcmp0_b32 s92, 3\n\t" \
  "s_cbranch_scc1 .Lf3%=\n\t" \
  "s_nop 15\n\t" \
  ".Lf3%=:\n\t" \
  "s_bitcmp0_b32 s92, 2\n\t" \
  "s_cbranch_scc1 .Lf2%=\n\t" \
  "s_nop 10\n\t" \
  ".Lf2%=:\n\t" \
  ".Lgo%=:\n\t" \
  "s_mov_b32 s92, 0\n\t" \
  ".Lpoll%=:\n\t" \
  "buffer_load_dwordx4 v[200:203], v248, %[rs], %[som] offen sc1\n\t" \
  "buffer_load_dwordx4 v[204:207], v248, %[rs], %[som2] offen sc1\n\t" \
  "s_waitcnt vmcnt(1) lgkmcnt(0)\n\t" \
  "ds_write_b128 v249, v[200:203]\n\t" \
  "v_smfmac_f32_16x16x64_bf16 v[192:195], v[200:203], v[208:215], v251\n\t" \
  "v_max3_u32 v244, v200, v201, v202\n\t" \
  "v_smfmac_f32_16x16x64_bf16 v[196:199], v[200:203], v[236:243], v251\n\t" \
  "s_waitcnt vmcnt(0)\n\t" \
  "ds_write_b128 v249, v[204:207] offset:8192\n\t" \
  "v_smfmac_f32_16x16x64_bf16 v[192:195], v[204:207], v[216:223], v251\n\t" \
  "v_max3_u32 v244, v244, v203, v204\n\t" \
  "v_max3_u32 v244, v244, v205, v206\n\t" \
  "v_smfmac_f32_16x16x64_bf16 v[196:199], v[204:207], v[224:231], v251\n\t" \
  "v_max_u32 v244, v244, v207\n\t" \
  "v_cmp_le_u32 vcc, %[sent], v244\n\t" \
  "s_cbranch_vccnz .Lslow%=\n\t" \
  ".Lok%=:\n\t" \
  "ds_read_b128 v[216:219], v250 offset:32768\n\t" \
  "ds_read_b128 v[220:223], v250 offset:33792\n\t" \
  "ds_read_b128 v[224:227], v250 offset:34816\n\t" \
  "ds_read_b128 v[228:231], v250 offset:35840\n\t" \
  "s_waitcnt lgkmcnt(4)\n\t" \
  "s_barrier\n\t" \
  "ds_read_b128 v[208:211], v248 offset:7168\n\t" \
  "ds_read_b128 v[212:215], v248 offset:8192\n\t" \
  "ds_read_b128 v[236:239], v248 offset:9216\n\t" \
  "ds_read_b128 v[240:243], v248 offset:10240\n\t" \
  "s_waitcnt lgkmcnt(3)\n\t" \
  "ds_read_b128 v[244:247], v248 offset:13312\n\t" \
  "v_smfmac_f32_16x16x64_bf16 v[192:195], v[208:211], v[216:223], v251\n\t" \
  "ds_read_b128 v[216:219], v250 offset:0\n\t" \
  "v_smfmac_f32_16x16x64_bf16 v[196:199], v[208:211], v[224:231], v251\n\t" \
  "s_waitcnt lgkmcnt(4)\n\t" \
  "ds_read_b128 v[220:223], v250 offset:1024\n\t" \
  "v_smfmac_f32_16x16x64_bf16 v[192:195], v[212:215], v[0:7], v251\n\t" \
  "ds_read_b128 v[224:227], v250 offset:2048\n\t" \
  "v_smfmac_f32_16x16x64_bf16 v[196:199], v[212:215], v[8:15], v251\n\t" \
  "s_waitcnt lgkmcnt(5)\n\t" \
  "ds_read_b128 v[228:231], v250 offset:3072\n\t" \
  "v_smfmac_f32_16x16x64_bf16 v[192:195], v[236:239], v[16:23], v251\n\t" \
  "ds_read_b128 v[200:203], v248 offset:11264\n\t" \
  "v_smfmac_f32_16x16x64_bf16 v[196:199], v[236:239], v[24:31], v251\n\t" \
  "s_waitcnt lgkmcnt(6)\n\t" \
  "ds_read_b128 v[204:207], v248 offset:12288\n\t" \
  "v_smfmac_f32_16x16x64_bf16 v[192:195], v[240:243], v[32:39], v251\n\t" \
  "ds_read_b128 v[208:211], v248 offset:15360\n\t" \
  "v_smfmac_f32_16x16x64_bf16 v[196:199], v[240:243], v[40:47], v251\n\t" \
  "s_waitcnt lgkmcnt(5)\n\t" \
  "ds_read_b128 v[212:215], v248 offset:0\n\t" \
  "v_smfmac_f32_16x16x64_bf16 v[192:195], v[244:247], v[216:223], v251\n\t" \
  "s_waitcnt lgkmcnt(4)\n\t" \
  "v_smfmac_f32_16x16x64_bf16 v[196:199], v[244:247], v[224:231], v251\n\t" \
  "s_waitcnt lgkmcnt(3)\n\t" \
  "ds_read_b128 v[236:239], v248 offset:1024\n\t" \
  "v_smfmac_f32_16x16x64_bf16 v[192:195], v[200:203], v[48:55], v251\n\t" \
  "ds_read_b128 v[240:243], v248 offset:2048\n\t" \
  "v_smfmac_f32_16x16x64_bf16 v[196:199], v[200:203], v[56:63], v251\n\t" \
  "s_waitcnt lgkmcnt(4)\n\t" \
  "v_smfmac_f32_16x16x64_bf16 v[192:195], v[204:207], v[64:71], v251\n\t" \
  "ds_read_b128 v[244:247], v248 offset:3072\n\t" \
  "v_smfmac_f32_16x16x64_bf16 v[196:199], v[204:207], v[72:79], v251\n\t" \
  "s_waitcnt lgkmcnt(4)\n\t" \
  "v_smfmac_f32_16x16x64_bf16 v[192:195], v[208:211], v[80:87], v251\n\t" \
  "ds_read_b128 v[200:203], v248 offset:4096\n\t" \
  "v_smfmac_f32_16x16x64_bf16 v[196:199], v[208:211], v[88:95], v251\n\t" \
  "s_waitcnt lgkmcnt(4)\n\t" \
  "v_smfmac_f32_16x16x64_bf16 v[192:195], v[212:215], v[96:103], v251\n\t" \
  "ds_read_b128 v[204:207], v248 offset:5120\n\t" \
  "v_smfmac_f32_16x16x64_bf16 v[196:199], v[212:215], v[104:111], v251\n\t" \
  "s_waitcnt lgkmcnt(3)\n\t" \
  "v_smfmac_f32_16x16x64_bf16 v[192:195], v[236:239], v[112:119], v251\n\t" \
  "v_smfmac_f32_16x16x64_bf16 v[196:199], v[236:239], v[120:127], v251\n\t" \
  "v_smfmac_f32_16x16x64_bf16 v[192:195], v[240:243], v[128:135], v251\n\t" \
  "v_smfmac_f32_16x16x64_bf16 v[196:199], v[240:243], v[136:143], v251\n\t" \
  "s_waitcnt lgkmcnt(1)\n\t" \
  "v_smfmac_f32_16x16x64_bf16 v[192:195], v[244:247], v[144:151], v251\n\t" \
  "v_smfmac_f32_16x16x64_bf16 v[196:199], v[244:247], v[152:159], v251\n\t" \
  "v_smfmac_f32_16x16x64_bf16 v[192:195], v[200:203], v[160:167], v251\n\t" \
  "v_smfmac_f32_16x16x64_bf16 v[196:199], v[200:203], v[168:175], v251\n\t" \
  "s_waitcnt lgkmcnt(0)\n\t" \
  "v_smfmac_f32_16x16x64_bf16 v[192:195], v[204:207], v[176:183], v251\n\t" \
  "v_smfmac_f32_16x16x64_bf16 v[196:199], v[204:207], v[184:191], v251\n\t" \
  "s_nop 6\n\t" \
  "v_add_f32 v192, v192, v194\n\t" \
  "v_add_f32 v193, v193, v195\n\t" \
  "v_add_f32 v196, v196, v198\n\t" \
  "v_add_f32 v197, v197, v199\n\t" \
  "v_cvt_pk_bf16_f32 v192, v192, v193\n\t" \
  "v_cvt_pk_bf16_f32 v196, v196, v197\n\t" \
  "ds_write2_b32 v252, v192, v196 offset1:64\n\t" \
  "s_waitcnt lgkmcnt(0)\n\t" \
  "s_barrier\n\t" \
  "buffer_store_dword v192, v235, %[rs], %[sk] offen sc1\n\t" \
  "buffer_store_dword v196, v235, %[rs], %[sk] offen offset:256 sc1\n\t" \
  "s_mov_b32 %[spins], s92\n\t" \
  "s_branch .Lend%=\n\t" \
  ".Lslow%=:\n\t" \
  "s_mov_b64 s[94:95], 0\n\t" \
  "v_cmp_eq_u32 vcc, %[sent], v200\n\t" \
  "s_or_b64 s[94:95], s[94:95], vcc\n\t" \
  "v_cmp_eq_u32 vcc, %[sent], v201\n\t" \
  "s_or_b64 s[94:95], s[94:95], vcc\n\t" \
  "v_cmp_eq_u32 vcc, %[sent], v202\n\t" \
  "s_or_b64 s[94:95], s[94:95], vcc\n\t" \
  "v_cmp_eq_u32 vcc, %[sent], v203\n\t" \
  "s_or_b64 s[94:95], s[94:95], vcc\n\t" \
  "v_cmp_eq_u32 vcc, %[sent], v204\n\t" \
  "s_or_b64 s[94:95], s[94:95], vcc\n\t" \
  "v_cmp_eq_u32 vcc, %[sent], v205\n\t" \
  "s_or_b64 s[94:95], s[94:95], vcc\n\t" \
  "v_cmp_eq_u32 vcc, %[sent], v206\n\t" \
  "s_or_b64 s[94:95], s[94:95], vcc\n\t" \
  "v_cmp_eq_u32 vcc, %[sent], v207\n\t" \
  "s_or_b64 s[94:95], s[94:95], vcc\n\t" \
  "s_cmp_eq_u64 s[94:95], 0\n\t" \
  "s_cbranch_scc1 .Lok%=\n\t" \
  "v_mov_b64 v[192:193], 0\n\t" \
  "v_mov_b64 v[194:195], 0\n\t" \
  "v_mov_b64 v[196:197], 0\n\t" \
  "v_mov_b64 v[198:199], 0\n\t" \
  "s_add_u32 s92, s92, 1\n\t" \
  "s_sleep 6\n\t" \
  "s_cmp_eq_u32 s92, 1\n\t" \
  "s_cbranch_scc1 .Lpoll%=\n\t" \
  "s_sleep 1\n\t" \
  "s_and_b32 s93, s92, 63\n\t" \
  "s_cmp_lg_u32 s93, 0\n\t" \
  "s_cbranch_scc1 .Lpoll%=\n\t" \
  "v_mov_b32 v245, 0\n\t" \
  "buffer_load_dword v244, v245, %[rs], %[sab] offen sc1\n\t" \
  "s_waitcnt vmcnt(0)\n\t" \
  "v_readfirstlane_b32 s93, v244\n\t" \
  "s_nop 3\n\t" \
  "s_cmp_eq_u32 s93, 1\n\t" \
  "s_cbranch_scc1 .Lok%=\n\t" \
  "s_cmp_lt_u32 s92, 0x80000\n\t" \
  "s_cbranch_scc1 .Lpoll%=\n\t" \
  "v_mov_b32 v244, 1\n\t" \
  "buffer_store_dword v244, v245, %[rs], %[sab] offen sc1\n\t" \
  "s_branch .Lok%=\n\t" \
  ".Lend%=:\n\t"
#define QK_ASM_7 \
  "s_waitcnt lgkmcnt(0)\n\t" \
  "v_subrev_u32 v244, 0x8000, v250\n\t" \
  "ds_read_b128 v[216:219], v248 offset:61440\n\t" \
  "ds_read_b128 v[220:223], v248 offset:62464\n\t" \
  "ds_read_b128 v[224:227], v248 offset:63488\n\t" \
  "ds_read_b128 v[228:231], v248 offset:64512\n\t" \
  "ds_read_b128 v[208:211], v244 offset:0\n\t" \
  "ds_read_b128 v[212:215], v244 offset:1024\n\t" \
  "ds_read_b128 v[236:239], v244 offset:2048\n\t" \
  "ds_read_b128 v[240:243], v244 offset:3072\n\t" \
  "v_mov_b64 v[192:193], 0\n\t" \
  "v_mov_b64 v[194:195], 0\n\t" \
  "v_mov_b64 v[196:197], 0\n\t" \
  "v_mov_b64 v[198:199], 0\n\t" \
  "s_movk_i32 s92, 0x2000\n\t" \
  ".Lwt%=:\n\t" \
  "ds_read_b64 v[200:201], v248 offset:30720\n\t" \
  "s_memrealtime s[90:91]\n\t" \
  "s_sub_u32 s92, s92, 1\n\t" \
  "s_waitcnt lgkmcnt(0)\n\t" \
  "v_readfirstlane_b32 s93, v200\n\t" \
  "v_readfirstlane_b32 %[tgt], v201\n\t" \
  "s_cbranch_scc1 .Lgo%=\n\t" \
  "s_cmp_eq_u32 s93, %[step]\n\t" \
  "s_cbranch_scc1 .Lwx%=\n\t" \
  "s_sleep 1\n\t" \
  "s_branch .Lwt%=\n\t" \
  ".Lwx%=:\n\t" \
  "s_lshl_b32 s93, s90, 2\n\t" \
  "s_sub_i32 s93, %[tgt], s93\n\t" \
  "s_cmp_le_i32 s93, 0\n\t" \
  "s_cbranch_scc1 .Lgo%=\n\t" \
  "s_min_i32 s93, s93, 8000\n\t" \
  "s_mov_b32 s94, s93\n\t" \
  ".Lsl%=:\n\t" \
  "s_sub_i32 s94, s94, 18\n\t" \
  "s_cmp_lt_i32 s94, 30\n\t" \
  "s_cbranch_scc1 .Lsx%=\n\t" \
  "s_sleep 1\n\t" \
  "s_branch .Lsl%=\n\t" \
  ".Lsx%=:\n\t" \
  "s_sub_i32 s93, s93, 12\n\t" \
  "s_max_i32 s93, s93, 4\n\t" \
  "s_lshr_b32 s93, s93, 2\n\t" \
  "s_add_u32 s93, s93, s90\n\t" \
  "s_memrealtime s[90:91]\n\t" \
  ".Lpace%=:\n\t" \
  "s_memrealtime s[94:95]\n\t" \
  "s_waitcnt lgkmcnt(1)\n\t" \
  "s_sub_i32 s92, s90, s93\n\t" \
  "s_cmp_ge_i32 s92, 0\n\t" \
  "s_cbranch_scc1 .Le%=\n\t" \
  "s_memrealtime s[90:91]\n\t" \
  "s_waitcnt lgkmcnt(1)\n\t" \
  "s_sub_i32 s92, s94, s93\n\t" \
  "s_cmp_lt_i32 s92, 0\n\t" \
  "s_cbranch_scc1 .Lpace%=\n\t" \
  "s_nop 4\n\t" \
  ".Le%=:\n\t" \
  "s_add_u32 s92, s92, s93\n\t" \
  "s_lshl_b32 s92, s92, 2\n\t" \
  "s_sub_i32 s92, %[tgt], s92\n\t" \
  "s_max_i32 s92, s92, 0\n\t" \
  "s_bitcmp0_b32 s92, 3\n\t" \
  "s_cbranch_scc1 .Lf3%=\n\t" \
  "s_nop 15\n\t" \
  ".Lf3%=:\n\t" \
  "s_bitcmp0_b32 s92, 2\n\t" \
  "s_cbranch_scc1 .Lf2%=\n\t" \
  "s_nop 10\n\t" \
  ".Lf2%=:\n\t" \
  ".Lgo%=:\n\t" \
  "s_mov_b32 s92, 0\n\t" \
  "s_memtime s[88:89]\n\t" \
  ".Lpoll%=:\n\t" \
  "buffer_load_dwordx4 v[200:203], v248, %[rs], %[som] offen sc1\n\t" \
  "buffer_load_dwordx4 v[204:207], v248, %[rs], %[som2] offen sc1\n\t" \
  "s_waitcnt vmcnt(1) lgkmcnt(0)\n\t" \
  "ds_write_b128 v249, v[200:203]\n\t" \
  "v_smfmac_f32_16x16x64_bf16 v[192:195], v[200:203], v[208:215], v251\n\t" \
  "v_max3_u32 v244, v200, v201, v202\n\t" \
  "v_smfmac_f32_16x16x64_bf16 v[196:199], v[200:203], v[236:243], v251\n\t" \
  "s_waitcnt vmcnt(0)\n\t" \
  "ds_write_b128 v249, v[204:207] offset:8192\n\t" \
  "v_smfmac_f32_16x16x64_bf16 v[192:195], v[204:207], v[216:223], v251\n\t" \
  "v_max3_u32 v244, v244, v203, v204\n\t" \
  "v_max3_u32 v244, v244, v205, v206\n\t" \
  "v_smfmac_f32_16x16x64_bf16 v[196:199], v[204:207], v[224:231], v251\n\t" \
  "v_max_u32 v244, v244, v207\n\t" \
  "v_cmp_le_u32 vcc, %[sent], v244\n\t" \
  "s_cbranch_vccnz .Lslow%=\n\t" \
  ".Lok%=:\n\t" \
  "ds_read_b128 v[216:219], v250 offset:32768\n\t" \
  "ds_read_b128 v[220:223], v250 offset:33792\n\t" \
  "ds_read_b128 v[224:227], v250 offset:34816\n\t" \
  "ds_read_b128 v[228:231], v250 offset:35840\n\t" \
  "s_waitcnt lgkmcnt(4)\n\t" \
  "s_setprio 3\n\t" \
  "s_barrier\n\t" \
  "s_memrealtime s[90:91]\n\t" \
  "s_memtime s[94:95]\n\t" \
  "v_mov_b32 v232, %[step]\n\t" \
  "v_lshrrev_b32 v234, 1, v248\n\t" \
  "s_mov_b64 exec, 0xffff\n\t" \
  "s_waitcnt lgkmcnt(0)\n\t" \
  "s_lshl_b32 s90, s90, 2\n\t" \
  "s_sub_u32 s94, s94, s88\n\t" \
  "s_mul_i32 s94, s94, 12200\n\t" \
  "s_lshr_b32 s94, s94, 16\n\t" \
  "s_add_u32 s94, s94, %[tgt]\n\t" \
  "s_sub_i32 s93, s90, s94\n\t" \
  "s_sub_i32 s93, s93, 30\n\t" \
  "s_max_i32 s93, s93, 0\n\t" \
  "s_add_u32 s90, s94, s93\n\t" \
  "s_sub_i32 s93, s90, %[tgt]\n\t" \
  "s_cmp_gt_i32 s93, 270\n\t" \
  "s_cselect_b32 s93, 90, 0\n\t" \
  "s_add_u32 s93, s93, s90\n\t" \
  "s_add_u32 s93, s93, %[lead]\n\t" \
  "v_mov_b32 v233, s93\n\t" \
  "buffer_store_dwordx2 v[232:233], v234, %[rs], %[sbc] offen sc1\n\t" \
  "s_mov_b64 exec, -1\n\t" \
  "s_mov_b32 %[tb1], s90\n\t" \
  "s_setprio 0\n\t" \
  "ds_read_b128 v[208:211], v248 offset:6144\n\t" \
  "ds_read_b128 v[212:215], v248 offset:9216\n\t" \
  "ds_read_b128 v[236:239], v248 offset:10240\n\t" \
  "ds_read_b128 v[240:243], v248 offset:11264\n\t" \
  "s_waitcnt lgkmcnt(3)\n\t" \
  "ds_read_b128 v[244:247], v248 offset:12288\n\t" \
  "v_smfmac_f32_16x16x64_bf16 v[192:195], v[208:211], v[216:223], v251\n\t" \
  "ds_read_b128 v[216:219], v250 offset:0\n\t" \
  "v_smfmac_f32_16x16x64_bf16 v[196:199], v[208:211], v[224:231], v251\n\t" \
  "s_waitcnt lgkmcnt(4)\n\t" \
  "ds_read_b128 v[220:223], v250 offset:1024\n\t" \
  "v_smfmac_f32_16x16x64_bf16 v[192:195], v[212:215], v[0:7], v251\n\t" \
  "ds_read_b128 v[224:227], v250 offset:2048\n\t" \
  "v_smfmac_f32_16x16x64_bf16 v[196:199], v[212:215], v[8:15], v251\n\t" \
  "s_waitcnt lgkmcnt(5)\n\t" \
  "ds_read_b128 v[228:231], v250 offset:3072\n\t" \
  "v_smfmac_f32_16x16x64_bf16 v[192:195], v[236:239], v[16:23], v251\n\t" \
  "ds_read_b128 v[200:203], v248 offset:13312\n\t" \
  "v_smfmac_f32_16x16x64_bf16 v[196:199], v[236:239], v[24:31], v251\n\t" \
  "s_waitcnt lgkmcnt(6)\n\t" \
  "ds_read_b128 v[204:207], v248 offset:14336\n\t" \
  "v_smfmac_f32_16x16x64_bf16 v[192:195], v[240:243], v[32:39], v251\n\t" \
  "ds_read_b128 v[208:211], v248 offset:0\n\t" \
  "v_smfmac_f32_16x16x64_bf16 v[196:199], v[240:243], v[40:47], v251\n\t" \
  "s_waitcnt lgkmcnt(5)\n\t" \
  "ds_read_b128 v[212:215], v248 offset:1024\n\t" \
  "v_smfmac_f32_16x16x64_bf16 v[192:195], v[244:247], v[216:223], v251\n\t" \
  "s_waitcnt lgkmcnt(4)\n\t" \
  "v_smfmac_f32_16x16x64_bf16 v[196:199], v[244:247], v[224:231], v251\n\t" \
  "s_waitcnt lgkmcnt(3)\n\t" \
  "ds_read_b128 v[236:239], v248 offset:2048\n\t" \
  "v_smfmac_f32_16x16x64_bf16 v[192:195], v[200:203], v[48:55], v251\n\t" \
  "ds_read_b128 v[240:243], v248 offset:3072\n\t" \
  "v_smfmac_f32_16x16x64_bf16 v[196:199], v[200:203], v[56:63], v251\n\t" \
  "s_waitcnt lgkmcnt(4)\n\t" \
  "v_smfmac_f32_16x16x64_bf16 v[192:195], v[204:207], v[64:71], v251\n\t" \
  "ds_read_b128 v[244:247], v248 offset:4096\n\t" \
  "v_smfmac_f32_16x16x64_bf16 v[196:199], v[204:207], v[72:79], v251\n\t" \
  "s_waitcnt lgkmcnt(4)\n\t" \
  "v_smfmac_f32_16x16x64_bf16 v[192:195], v[208:211], v[80:87], v251\n\t" \
  "ds_read_b128 v[200:203], v248 offset:5120\n\t" \
  "v_smfmac_f32_16x16x64_bf16 v[196:199], v[208:211], v[88:95], v251\n\t" \
  "s_waitcnt lgkmcnt(4)\n\t" \
  "v_smfmac_f32_16x16x64_bf16 v[192:195], v[212:215], v[96:103], v251\n\t" \
  "ds_read_b128 v[204:207], v248 offset:8192\n\t" \
  "v_smfmac_f32_16x16x64_bf16 v[196:199], v[212:215], v[104:111], v251\n\t" \
  "s_waitcnt lgkmcnt(3)\n\t" \
  "v_smfmac_f32_16x16x64_bf16 v[192:195], v[236:239], v[112:119], v251\n\t" \
  "v_smfmac_f32_16x16x64_bf16 v[196:199], v[236:239], v[120:127], v251\n\t" \
  "v_smfmac_f32_16x16x64_bf16 v[192:195], v[240:243], v[128:135], v251\n\t" \
  "v_smfmac_f32_16x16x64_bf16 v[196:199], v[240:243], v[136:143], v251\n\t" \
  "s_waitcnt lgkmcnt(1)\n\t" \
  "v_smfmac_f32_16x16x64_bf16 v[192:195], v[244:247], v[144:151], v251\n\t" \
  "v_smfmac_f32_16x16x64_bf16 v[196:199], v[244:247], v[152:159], v251\n\t" \
  "v_smfmac_f32_16x16x64_bf16 v[192:195], v[200:203], v[160:167], v251\n\t" \
  "v_smfmac_f32_16x16x64_bf16 v[196:199], v[200:203], v[168:175], v251\n\t" \
  "s_waitcnt lgkmcnt(0)\n\t" \
  "v_smfmac_f32_16x16x64_bf16 v[192:195], v[204:207], v[176:183], v251\n\t" \
  "v_smfmac_f32_16x16x64_bf16 v[196:199], v[204:207], v[184:191], v251\n\t" \
  "s_nop 6\n\t" \
  "v_add_f32 v192, v192, v194\n\t" \
  "v_add_f32 v193, v193, v195\n\t" \
  "v_add_f32 v196, v196, v198\n\t" \
  "v_add_f32 v197, v197, v199\n\t" \
  "v_cvt_pk_bf16_f32 v192, v192, v193\n\t" \
  "v_cvt_pk_bf16_f32 v196, v196, v197\n\t" \
  "ds_write2_b32 v252, v192, v196 offset1:64\n\t" \
  "s_waitcnt lgkmcnt(0)\n\t" \
  "s_barrier\n\t" \
  "buffer_store_dword v192, v235, %[rs], %[sk] offen sc1\n\t" \
  "buffer_store_dword v196, v235, %[rs], %[sk] offen offset:256 sc1\n\t" \
  "s_mov_b32 %[spins], s92\n\t" \
  "s_branch .Lend%=\n\t" \
  ".Lslow%=:\n\t" \
  "s_mov_b64 s[94:95], 0\n\t" \
  "v_cmp_eq_u32 vcc, %[sent], v200\n\t" \
  "s_or_b64 s[94:95], s[94:95], vcc\n\t" \
  "v_cmp_eq_u32 vcc, %[sent], v201\n\t" \
  "s_or_b64 s[94:95], s[94:95], vcc\n\t" \
  "v_cmp_eq_u32 vcc, %[sent], v202\n\t" \
  "s_or_b64 s[94:95], s[94:95], vcc\n\t" \
  "v_cmp_eq_u32 vcc, %[sent], v203\n\t" \
  "s_or_b64 s[94:95], s[94:95], vcc\n\t" \
  "v_cmp_eq_u32 vcc, %[sent], v204\n\t" \
  "s_or_b64 s[94:95], s[94:95], vcc\n\t" \
  "v_cmp_eq_u32 vcc, %[sent], v205\n\t" \
  "s_or_b64 s[94:95], s[94:95], vcc\n\t" \
  "v_cmp_eq_u32 vcc, %[sent], v206\n\t" \
  "s_or_b64 s[94:95], s[94:95], vcc\n\t" \
  "v_cmp_eq_u32 vcc, %[sent], v207\n\t" \
  "s_or_b64 s[94:95], s[94:95], vcc\n\t" \
  "s_cmp_eq_u64 s[94:95], 0\n\t" \
  "s_cbranch_scc1 .Lok%=\n\t" \
  "v_mov_b64 v[192:193], 0\n\t" \
  "v_mov_b64 v[194:195], 0\n\t" \
  "v_mov_b64 v[196:197], 0\n\t" \
  "v_mov_b64 v[198:199], 0\n\t" \
  "s_add_u32 s92, s92, 1\n\t" \
  "s_sleep 6\n\t" \
  "s_cmp_eq_u32 s92, 1\n\t" \
  "s_cbranch_scc1 .Lpoll%=\n\t" \
  "s_sleep 1\n\t" \
  "s_and_b32 s93, s92, 63\n\t" \
  "s_cmp_lg_u32 s93, 0\n\t" \
  "s_cbranch_scc1 .Lpoll%=\n\t" \
  "v_mov_b32 v245, 0\n\t" \
  "buffer_load_dword v244, v245, %[rs], %[sab] offen sc1\n\t" \
  "s_waitcnt vmcnt(0)\n\t" \
  "v_readfirstlane_b32 s93, v244\n\t" \
  "s_nop 3\n\t" \
  "s_cmp_eq_u32 s93, 1\n\t" \
  "s_cbranch_scc1 .Lok%=\n\t" \
  "s_cmp_lt_u32 s92, 0x80000\n\t" \
  "s_cbranch_scc1 .Lpoll%=\n\t" \
  "v_mov_b32 v244, 1\n\t" \
  "buffer_store_dword v244, v245, %[rs], %[sab] offen sc1\n\t" \
  "s_branch .Lok%=\n\t" \
  ".Lend%=:\n\t"
#define VV_ASM \
  "s_waitcnt lgkmcnt(0)\n\t" \
  "v_mov_b64 v[128:129], 0\n\t" \
  "v_mov_b64 v[130:131], 0\n\t" \
  "s_movk_i32 s92, 0x2000\n\t" \
  ".Lwt%=:\n\t" \
  "ds_read_b64 v[132:133], v200 offset:30720\n\t" \
  "s_memrealtime s[90:91]\n\t" \
  "s_sub_u32 s92, s92, 1\n\t" \
  "s_waitcnt lgkmcnt(0)\n\t" \
  "v_readfirstlane_b32 s93, v132\n\t" \
  "v_readfirstlane_b32 %[tgt], v133\n\t" \
  "s_cbranch_scc1 .Lgo%=\n\t" \
  "s_cmp_eq_u32 s93, %[step]\n\t" \
  "s_cbranch_scc1 .Lwx%=\n\t" \
  "s_sleep 1\n\t" \
  "s_branch .Lwt%=\n\t" \
  ".Lwx%=:\n\t" \
  "s_lshl_b32 s93, s90, 2\n\t" \
  "s_sub_i32 s93, %[tgt], s93\n\t" \
  "s_cmp_le_i32 s93, 0\n\t" \
  "s_cbranch_scc1 .Lgo%=\n\t" \
  "s_min_i32 s93, s93, 8000\n\t" \
  "s_mov_b32 s94, s93\n\t" \
  ".Lsl%=:\n\t" \
  "s_sub_i32 s94, s94, 18\n\t" \
  "s_cmp_lt_i32 s94, 30\n\t" \
  "s_cbranch_scc1 .Lsx%=\n\t" \
  "s_sleep 1\n\t" \
  "s_branch .Lsl%=\n\t" \
  ".Lsx%=:\n\t" \
  "s_sub_i32 s93, s93, 12\n\t" \
  "s_max_i32 s93, s93, 4\n\t" \
  "s_lshr_b32 s93, s93, 2\n\t" \
  "s_add_u32 s93, s93, s90\n\t" \
  "s_memrealtime s[90:91]\n\t" \
  ".Lpace%=:\n\t" \
  "s_memrealtime s[94:95]\n\t" \
  "s_waitcnt lgkmcnt(1)\n\t" \
  "s_sub_i32 s92, s90, s93\n\t" \
  "s_cmp_ge_i32 s92, 0\n\t" \
  "s_cbranch_scc1 .Le%=\n\t" \
  "s_memrealtime s[90:91]\n\t" \
  "s_waitcnt lgkmcnt(1)\n\t" \
  "s_sub_i32 s92, s94, s93\n\t" \
  "s_cmp_lt_i32 s92, 0\n\t" \
  "s_cbranch_scc1 .Lpace%=\n\t" \
  "s_nop 4\n\t" \
  ".Le%=:\n\t" \
  "s_add_u32 s92, s92, s93\n\t" \
  "s_lshl_b32 s92, s92, 2\n\t" \
  "s_sub_i32 s92, %[tgt], s92\n\t" \
  "s_max_i32 s92, s92, 0\n\t" \
  "s_bitcmp0_b32 s92, 3\n\t" \
  "s_cbranch_scc1 .Lf3%=\n\t" \
  "s_nop 15\n\t" \
  ".Lf3%=:\n\t" \
  "s_bitcmp0_b32 s92, 2\n\t" \
  "s_cbranch_scc1 .Lf2%=\n\t" \
  "s_nop 10\n\t" \
  ".Lf2%=:\n\t" \
  ".Lgo%=:\n\t" \
  "s_mov_b32 s92, 0\n\t" \
  ".Lpoll%=:\n\t" \
  "buffer_load_dwordx4 v[132:135], v200, %[rs], %[som] offen sc1\n\t" \
  "buffer_load_dwordx4 v[136:139], v200, %[rs], %[som2] offen sc1\n\t" \
  "s_waitcnt vmcnt(1) lgkmcnt(0)\n\t" \
  "ds_write_b128 v232, v[132:135]\n\t" \
  "v_smfmac_f32_16x16x64_bf16 v[128:131], v[132:135], v[0:7], v202\n\t" \
  "v_max3_u32 v196, v132, v133, v134\n\t" \
  "s_waitcnt vmcnt(0)\n\t" \
  "ds_write_b128 v232, v[136:139] offset:8192\n\t" \
  "v_smfmac_f32_16x16x64_bf16 v[128:131], v[136:139], v[64:71], v202\n\t" \
  "v_max3_u32 v196, v196, v135, v136\n\t" \
  "v_max3_u32 v196, v196, v137, v138\n\t" \
  "v_max_u32 v196, v196, v139\n\t" \
  "v_cmp_le_u32 vcc, %[sent], v196\n\t" \
  "s_cbranch_vccnz .Lslow%=\n\t" \
  ".Lok%=:\n\t" \
  "s_waitcnt lgkmcnt(0)\n\t" \
  "s_barrier\n\t" \
  "ds_read_b128 v[164:167], v233 offset:0\n\t" \
  "ds_read_b128 v[140:143], v234 offset:0\n\t" \
  "ds_read_b128 v[144:147], v235 offset:0\n\t" \
  "ds_read_b128 v[148:151], v236 offset:0\n\t" \
  "ds_read_b128 v[152:155], v237 offset:0\n\t" \
  "s_waitcnt lgkmcnt(4)\n\t" \
  "ds_read_b128 v[156:159], v238 offset:0\n\t" \
  "v_smfmac_f32_16x16x64_bf16 v[128:131], v[164:167], v[8:15], v202\n\t" \
  "s_waitcnt lgkmcnt(4)\n\t" \
  "ds_read_b128 v[160:163], v239 offset:0\n\t" \
  "v_smfmac_f32_16x16x64_bf16 v[128:131], v[140:143], v[16:23], v202\n\t" \
  "s_waitcnt lgkmcnt(4)\n\t" \
  "ds_read_b128 v[168:171], v233 offset:8192\n\t" \
  "v_smfmac_f32_16x16x64_bf16 v[128:131], v[144:147], v[24:31], v202\n\t" \
  "s_waitcnt lgkmcnt(4)\n\t" \
  "ds_read_b128 v[172:175], v234 offset:8192\n\t" \
  "v_smfmac_f32_16x16x64_bf16 v[128:131], v[148:151], v[32:39], v202\n\t" \
  "s_waitcnt lgkmcnt(4)\n\t" \
  "ds_read_b128 v[176:179], v235 offset:8192\n\t" \
  "v_smfmac_f32_16x16x64_bf16 v[128:131], v[152:155], v[40:47], v202\n\t" \
  "s_waitcnt lgkmcnt(4)\n\t" \
  "ds_read_b128 v[180:183], v236 offset:8192\n\t" \
  "v_smfmac_f32_16x16x64_bf16 v[128:131], v[156:159], v[48:55], v202\n\t" \
  "s_waitcnt lgkmcnt(4)\n\t" \
  "ds_read_b128 v[184:187], v237 offset:8192\n\t" \
  "v_smfmac_f32_16x16x64_bf16 v[128:131], v[160:163], v[56:63], v202\n\t" \
  "s_waitcnt lgkmcnt(4)\n\t" \
  "ds_read_b128 v[188:191], v238 offset:8192\n\t" \
  "v_smfmac_f32_16x16x64_bf16 v[128:131], v[168:171], v[72:79], v202\n\t" \
  "s_waitcnt lgkmcnt(4)\n\t" \
  "ds_read_b128 v[192:195], v239 offset:8192\n\t" \
  "v_smfmac_f32_16x16x64_bf16 v[128:131], v[172:175], v[80:87], v202\n\t" \
  "s_waitcnt lgkmcnt(4)\n\t" \
  "v_smfmac_f32_16x16x64_bf16 v[128:131], v[176:179], v[88:95], v202\n\t" \
  "s_waitcnt lgkmcnt(3)\n\t" \
  "v_smfmac_f32_16x16x64_bf16 v[128:131], v[180:183], v[96:103], v202\n\t" \
  "s_waitcnt lgkmcnt(2)\n\t" \
  "v_smfmac_f32_16x16x64_bf16 v[128:131], v[184:187], v[104:111], v202\n\t" \
  "s_waitcnt lgkmcnt(1)\n\t" \
  "v_smfmac_f32_16x16x64_bf16 v[128:131], v[188:191], v[112:119], v202\n\t" \
  "s_waitcnt lgkmcnt(0)\n\t" \
  "v_smfmac_f32_16x16x64_bf16 v[128:131], v[192:195], v[120:127], v202\n\t" \
  "s_nop 7\n\t" \
  "v_add_f32 v128, v128, v130\n\t" \
  "v_add_f32 v129, v129, v131\n\t" \
  "v_cvt_pk_bf16_f32 v128, v128, v129\n\t" \
  "buffer_store_dword v128, v203, %[rs], %[sv] offen sc1\n\t" \
  "ds_write_b32 v204, v128\n\t" \
  "s_waitcnt lgkmcnt(0)\n\t" \
  "s_barrier\n\t" \
  "s_mov_b32 %[spins], s92\n\t" \
  "s_branch .Lend%=\n\t" \
  ".Lslow%=:\n\t" \
  "s_mov_b64 s[94:95], 0\n\t" \
  "v_cmp_eq_u32 vcc, %[sent], v132\n\t" \
  "s_or_b64 s[94:95], s[94:95], vcc\n\t" \
  "v_cmp_eq_u32 vcc, %[sent], v133\n\t" \
  "s_or_b64 s[94:95], s[94:95], vcc\n\t" \
  "v_cmp_eq_u32 vcc, %[sent], v134\n\t" \
  "s_or_b64 s[94:95], s[94:95], vcc\n\t" \
  "v_cmp_eq_u32 vcc, %[sent], v135\n\t" \
  "s_or_b64 s[94:95], s[94:95], vcc\n\t" \
  "v_cmp_eq_u32 vcc, %[sent], v136\n\t" \
  "s_or_b64 s[94:95], s[94:95], vcc\n\t" \
  "v_cmp_eq_u32 vcc, %[sent], v137\n\t" \
  "s_or_b64 s[94:95], s[94:95], vcc\n\t" \
  "v_cmp_eq_u32 vcc, %[sent], v138\n\t" \
  "s_or_b64 s[94:95], s[94:95], vcc\n\t" \
  "v_cmp_eq_u32 vcc, %[sent], v139\n\t" \
  "s_or_b64 s[94:95], s[94:95], vcc\n\t" \
  "s_cmp_eq_u64 s[94:95], 0\n\t" \
  "s_cbranch_scc1 .Lok%=\n\t" \
  "v_mov_b64 v[128:129], 0\n\t" \
  "v_mov_b64 v[130:131], 0\n\t" \
  "s_add_u32 s92, s92, 1\n\t" \
  "s_sleep 6\n\t" \
  "s_cmp_eq_u32 s92, 1\n\t" \
  "s_cbranch_scc1 .Lpoll%=\n\t" \
  "s_sleep 1\n\t" \
  "s_and_b32 s93, s92, 63\n\t" \
  "s_cmp_lg_u32 s93, 0\n\t" \
  "s_cbranch_scc1 .Lpoll%=\n\t" \
  "v_mov_b32 v197, 0\n\t" \
  "buffer_load_dword v196, v197, %[rs], %[sab] offen sc1\n\t" \
  "s_waitcnt vmcnt(0)\n\t" \
  "v_readfirstlane_b32 s93, v196\n\t" \
  "s_nop 3\n\t" \
  "s_cmp_eq_u32 s93, 1\n\t" \
  "s_cbranch_scc1 .Lok%=\n\t" \
  "s_cmp_lt_u32 s92, 0x80000\n\t" \
  "s_cbranch_scc1 .Lpoll%=\n\t" \
  "v_mov_b32 v196, 1\n\t" \
  "buffer_store_dword v196, v197, %[rs], %[sab] offen sc1\n\t" \
  "s_branch .Lok%=\n\t" \
  ".Lend%=:\n\t"
#define TK_ASM \
  "buffer_load_dwordx4 v[224:227], v200, %[rs], %[sk] offen offset:0 sc1\n\t" \
  "buffer_load_dwordx4 v[228:231], v200, %[rs], %[sk] offen offset:1024 sc1\n\t" \
  "s_memrealtime s[88:89]\n\t" \
  "v_add_u32 v205, %[tl], v200\n\t" \
  "v_add_u32 v206, %[tq], v205\n\t" \
  "v_add_u32 v207, %[tv], v205\n\t" \
  "s_waitcnt lgkmcnt(0)\n\t" \
  "ds_read_b128 v[128:131], v206 offset:8192\n\t" \
  "ds_read_b128 v[144:147], v205 offset:0\n\t" \
  "ds_read_b128 v[160:163], v205 offset:4096\n\t" \
  "ds_read_b128 v[132:135], v206 offset:9216\n\t" \
  "ds_read_b128 v[148:151], v205 offset:1024\n\t" \
  "ds_read_b128 v[164:167], v205 offset:5120\n\t" \
  "ds_read_b128 v[136:139], v206 offset:10240\n\t" \
  "ds_read_b128 v[152:155], v205 offset:2048\n\t" \
  "ds_read_b128 v[168:171], v205 offset:6144\n\t" \
  "ds_read_b128 v[140:143], v206 offset:11264\n\t" \
  "ds_read_b128 v[156:159], v205 offset:3072\n\t" \
  "ds_read_b128 v[172:175], v205 offset:7168\n\t" \
  "v_add_u32 v206, %[vp], v218\n\t" \
  "s_lshl_b32 s88, s88, 2\n\t" \
  "s_add_u32 s89, s88, %[od]\n\t" \
  "s_waitcnt lgkmcnt(10)\n\t" \
  "v_mfma_f32_16x16x32_bf16 v[196:199], v[144:147], v[128:131], 0\n\t" \
  "s_waitcnt lgkmcnt(9)\n\t" \
  "v_mfma_f32_16x16x32_bf16 v[144:147], v[160:163], v[128:131], 0\n\t" \
  "s_waitcnt lgkmcnt(7)\n\t" \
  "v_mfma_f32_16x16x32_bf16 v[196:199], v[148:151], v[132:135], v[196:199]\n\t" \
  "s_waitcnt lgkmcnt(6)\n\t" \
  "v_mfma_f32_16x16x32_bf16 v[144:147], v[164:167], v[132:135], v[144:147]\n\t" \
  "s_waitcnt lgkmcnt(4)\n\t" \
  "v_mfma_f32_16x16x32_bf16 v[196:199], v[152:155], v[136:139], v[196:199]\n\t" \
  "s_waitcnt lgkmcnt(3)\n\t" \
  "v_mfma_f32_16x16x32_bf16 v[144:147], v[168:171], v[136:139], v[144:147]\n\t" \
  "s_waitcnt lgkmcnt(1)\n\t" \
  "v_mfma_f32_16x16x32_bf16 v[196:199], v[156:159], v[140:143], v[196:199]\n\t" \
  "s_waitcnt lgkmcnt(0)\n\t" \
  "v_mfma_f32_16x16x32_bf16 v[144:147], v[172:175], v[140:143], v[144:147]\n\t" \
  "ds_read_b128 v[176:179], v207 offset:16384\n\t" \
  "ds_read_b128 v[180:183], v207 offset:17408\n\t" \
  "ds_read_b128 v[184:187], v207 offset:18432\n\t" \
  "ds_read_b128 v[188:191], v207 offset:19456\n\t" \
  "ds_read2st64_b32 v[192:193], v206 offset0:0 offset1:1\n\t" \
  "ds_read2st64_b32 v[194:195], v206 offset0:2 offset1:3\n\t" \
  "v_cvt_pk_bf16_f32 v197, v196, v197\n\t" \
  "v_cvt_pk_bf16_f32 v199, v198, v199\n\t" \
  "v_cvt_pk_bf16_f32 v145, v144, v145\n\t" \
  "v_cvt_pk_bf16_f32 v147, v146, v147\n\t" \
  "v_lshlrev_b32 v196, 16, v197\n\t" \
  "v_lshlrev_b32 v198, 16, v199\n\t" \
  "v_lshlrev_b32 v144, 16, v145\n\t" \
  "v_lshlrev_b32 v146, 16, v147\n\t" \
  "v_and_b32 v197, 0xffff0000, v197\n\t" \
  "v_and_b32 v199, 0xffff0000, v199\n\t" \
  "v_and_b32 v145, 0xffff0000, v145\n\t" \
  "v_and_b32 v147, 0xffff0000, v147\n\t" \
  "v_pk_mul_f32 v[196:197], v[196:197], %[sc] op_sel_hi:[1,0]\n\t" \
  "v_pk_mul_f32 v[198:199], v[198:199], %[sc] op_sel_hi:[1,0]\n\t" \
  "v_pk_mul_f32 v[144:145], v[144:145], %[sc] op_sel_hi:[1,0]\n\t" \
  "v_pk_mul_f32 v[146:147], v[146:147], %[sc] op_sel_hi:[1,0]\n\t" \
  "v_cvt_pk_bf16_f32 v197, v196, v197\n\t" \
  "v_cvt_pk_bf16_f32 v199, v198, v199\n\t" \
  "v_cvt_pk_bf16_f32 v145, v144, v145\n\t" \
  "v_cvt_pk_bf16_f32 v147, v146, v147\n\t" \
  "v_lshlrev_b32 v196, 16, v197\n\t" \
  "v_lshlrev_b32 v198, 16, v199\n\t" \
  "v_lshlrev_b32 v144, 16, v145\n\t" \
  "v_lshlrev_b32 v146, 16, v147\n\t" \
  "v_and_b32 v197, 0xffff0000, v197\n\t" \
  "v_and_b32 v199, 0xffff0000, v199\n\t" \
  "v_and_b32 v145, 0xffff0000, v145\n\t" \
  "v_and_b32 v147, 0xffff0000, v147\n\t" \
  "v_min_f32 v196, v196, v208\n\t" \
  "v_min_f32 v197, v197, v209\n\t" \
  "v_min_f32 v198, v198, v210\n\t" \
  "v_min_f32 v199, v199, v211\n\t" \
  "v_min_f32 v144, v144, v212\n\t" \
  "v_min_f32 v145, v145, v213\n\t" \
  "v_min_f32 v146, v146, v214\n\t" \
  "v_min_f32 v147, v147, v215\n\t" \
  "v_max3_f32 v148, v196, v197, v198\n\t" \
  "v_max3_f32 v149, v199, v144, v145\n\t" \
  "v_max3_f32 v148, v148, v146, v147\n\t" \
  "v_max_f32 v150, v148, v149\n\t" \
  "v_max_f32 v151, v148, v149\n\t" \
  "s_nop 1\n\t" \
  "v_permlane32_swap_b32 v150, v151\n\t" \
  "v_max_f32 v150, v150, v151\n\t" \
  "s_mov_b32 s92, 0\n\t" \
  ".Ltake%=:\n\t" \
  "s_waitcnt vmcnt(1) lgkmcnt(0)\n\t" \
  "ds_write_b128 v216, v[224:227]\n\t" \
  "v_max3_u32 v148, v224, v225, v226\n\t" \
  "s_waitcnt vmcnt(0)\n\t" \
  "ds_write_b128 v216, v[228:231] offset:1024\n\t" \
  "ds_read_b64_tr_b16 v[160:161], v217 offset:0\n\t" \
  "ds_read_b64_tr_b16 v[162:163], v217 offset:64\n\t" \
  "ds_read_b64_tr_b16 v[164:165], v217 offset:512\n\t" \
  "ds_read_b64_tr_b16 v[166:167], v217 offset:576\n\t" \
  "ds_read_b64_tr_b16 v[168:169], v217 offset:1024\n\t" \
  "ds_read_b64_tr_b16 v[170:171], v217 offset:1088\n\t" \
  "ds_read_b64_tr_b16 v[172:173], v217 offset:1536\n\t" \
  "ds_read_b64_tr_b16 v[174:175], v217 offset:1600\n\t" \
  "v_max3_u32 v148, v148, v227, v228\n\t" \
  "v_max3_u32 v148, v148, v229, v230\n\t" \
  "v_max_u32 v148, v148, v231\n\t" \
  "v_cmp_le_u32 vcc, %[sent], v148\n\t" \
  "s_cbranch_vccnz .Lslow%=\n\t" \
  ".Lok%=:\n\t" \
  "s_waitcnt lgkmcnt(6)\n\t" \
  "v_mfma_f32_16x16x32_bf16 v[152:155], v[160:163], v[128:131], 0\n\t" \
  "s_waitcnt lgkmcnt(4)\n\t" \
  "v_mfma_f32_16x16x32_bf16 v[152:155], v[164:167], v[132:135], v[152:155]\n\t" \
  "s_waitcnt lgkmcnt(2)\n\t" \
  "v_mfma_f32_16x16x32_bf16 v[152:155], v[168:171], v[136:139], v[152:155]\n\t" \
  "s_waitcnt lgkmcnt(0)\n\t" \
  "v_mfma_f32_16x16x32_bf16 v[152:155], v[172:175], v[140:143], v[152:155]\n\t" \
  "v_mov_b32 v157, 0\n\t" \
  "v_mov_b32 v158, 0\n\t" \
  "s_nop 5\n\t" \
  "v_cvt_pk_bf16_f32 v153, v152, v153\n\t" \
  "v_cvt_pk_bf16_f32 v155, v154, v155\n\t" \
  "v_lshlrev_b32 v152, 16, v153\n\t" \
  "v_lshlrev_b32 v154, 16, v155\n\t" \
  "v_and_b32 v153, 0xffff0000, v153\n\t" \
  "v_and_b32 v155, 0xffff0000, v155\n\t" \
  "v_pk_mul_f32 v[152:153], v[152:153], %[sc] op_sel_hi:[1,0]\n\t" \
  "v_pk_mul_f32 v[154:155], v[154:155], %[sc] op_sel_hi:[1,0]\n\t" \
  "v_cvt_pk_bf16_f32 v153, v152, v153\n\t" \
  "v_cvt_pk_bf16_f32 v155, v154, v155\n\t" \
  "v_lshlrev_b32 v152, 16, v153\n\t" \
  "v_lshlrev_b32 v154, 16, v155\n\t" \
  "v_and_b32 v153, 0xffff0000, v153\n\t" \
  "v_and_b32 v155, 0xffff0000, v155\n\t" \
  "v_max3_f32 v148, v152, v153, v150\n\t" \
  "v_max3_f32 v149, v148, v154, v155\n\t" \
  "v_max3_f32 v150, v148, v154, v155\n\t" \
  "v_cndmask_b32 v152, v152, v154, %[up]\n\t" \
  "v_cndmask_b32 v153, v153, v155, %[up]\n\t" \
  "v_permlane16_swap_b32 v149, v150\n\t" \
  "v_max_f32 v148, v149, v150\n\t" \
  "v_mul_f32 v148, %[negc], v148\n\t" \
  "v_fma_f32 v152, v152, %[c], v148\n\t" \
  "v_fma_f32 v153, v153, %[c], v148\n\t" \
  "v_exp_f32 v152, v152\n\t" \
  "v_fma_f32 v196, v196, %[c], v148\n\t" \
  "v_exp_f32 v153, v153\n\t" \
  "v_fma_f32 v197, v197, %[c], v148\n\t" \
  "v_exp_f32 v196, v196\n\t" \
  "v_fma_f32 v198, v198, %[c], v148\n\t" \
  "v_exp_f32 v197, v197\n\t" \
  "v_fma_f32 v199, v199, %[c], v148\n\t" \
  "v_exp_f32 v198, v198\n\t" \
  "v_fma_f32 v144, v144, %[c], v148\n\t" \
  "v_pk_add_f32 v[132:133], v[152:153], v[196:197]\n\t" \
  "v_exp_f32 v199, v199\n\t" \
  "v_fma_f32 v145, v145, %[c], v148\n\t" \
  "v_exp_f32 v144, v144\n\t" \
  "v_fma_f32 v146, v146, %[c], v148\n\t" \
  "v_pk_add_f32 v[132:133], v[132:133], v[198:199]\n\t" \
  "v_exp_f32 v145, v145\n\t" \
  "v_fma_f32 v147, v147, %[c], v148\n\t" \
  "v_exp_f32 v146, v146\n\t" \
  "v_pk_add_f32 v[132:133], v[132:133], v[144:145]\n\t" \
  "v_exp_f32 v147, v147\n\t" \
  "s_nop 0\n\t" \
  "v_pk_add_f32 v[132:133], v[132:133], v[146:147]\n\t" \
  "v_add_f32 v134, v132, v133\n\t" \
  "v_add_f32 v135, v132, v133\n\t" \
  "s_nop 1\n\t" \
  "v_permlane16_swap_b32 v134, v135\n\t" \
  "v_add_f32 v132, v134, v135\n\t" \
  "v_add_f32 v133, v134, v135\n\t" \
  "s_nop 1\n\t" \
  "v_permlane32_swap_b32 v132, v133\n\t" \
  "v_add_f32 v132, v132, v133\n\t" \
  "v_rcp_f32 v148, v132\n\t" \
  "s_nop 0\n\t" \
  "v_pk_mul_f32 v[152:153], v[152:153], v[148:149] op_sel_hi:[1,0]\n\t" \
  "v_pk_mul_f32 v[196:197], v[196:197], v[148:149] op_sel_hi:[1,0]\n\t" \
  "v_pk_mul_f32 v[198:199], v[198:199], v[148:149] op_sel_hi:[1,0]\n\t" \
  "v_pk_mul_f32 v[144:145], v[144:145], v[148:149] op_sel_hi:[1,0]\n\t" \
  "v_pk_mul_f32 v[146:147], v[146:147], v[148:149] op_sel_hi:[1,0]\n\t" \
  "v_cvt_pk_bf16_f32 v156, v152, v153\n\t" \
  "v_cvt_pk_bf16_f32 v159, v152, v153\n\t" \
  "v_cvt_pk_bf16_f32 v128, v196, v197\n\t" \
  "v_cvt_pk_bf16_f32 v129, v198, v199\n\t" \
  "v_mfma_f32_16x16x16_bf16 v[160:163], v[192:193], v[156:157], 0\n\t" \
  "v_cvt_pk_bf16_f32 v130, v144, v145\n\t" \
  "v_mfma_f32_16x16x16_bf16 v[164:167], v[192:193], v[158:159], 0\n\t" \
  "v_cvt_pk_bf16_f32 v131, v146, v147\n\t" \
  "v_mfma_f32_16x16x16_bf16 v[168:171], v[194:195], v[156:157], 0\n\t" \
  "v_mfma_f32_16x16x16_bf16 v[172:175], v[194:195], v[158:159], 0\n\t" \
  "s_nop 0\n\t" \
  "v_mfma_f32_16x16x32_bf16 v[160:163], v[176:179], v[128:131], v[160:163]\n\t" \
  "s_nop 0\n\t" \
  "v_mfma_f32_16x16x32_bf16 v[164:167], v[180:183], v[128:131], v[164:167]\n\t" \
  "s_nop 0\n\t" \
  "v_mfma_f32_16x16x32_bf16 v[168:171], v[184:187], v[128:131], v[168:171]\n\t" \
  "v_mfma_f32_16x16x32_bf16 v[172:175], v[188:191], v[128:131], v[172:175]\n\t" \
  "s_nop 2\n\t" \
  "v_cvt_pk_bf16_f32 v136, v160, v161\n\t" \
  "v_cvt_pk_bf16_f32 v137, v162, v163\n\t" \
  "v_cvt_pk_bf16_f32 v138, v164, v165\n\t" \
  "v_cvt_pk_bf16_f32 v139, v166, v167\n\t" \
  "v_cvt_pk_bf16_f32 v140, v168, v169\n\t" \
  "v_cvt_pk_bf16_f32 v141, v170, v171\n\t" \
  "v_cvt_pk_bf16_f32 v142, v172, v173\n\t" \
  "v_cvt_pk_bf16_f32 v143, v174, v175\n\t" \
  "s_memrealtime s[90:91]\n\t" \
  "s_waitcnt lgkmcnt(0)\n\t" \
  "s_lshl_b32 s93, s90, 2\n\t" \
  "s_sub_i32 s93, s89, s93\n\t" \
  "s_cmp_le_i32 s93, 0\n\t" \
  "s_cbranch_scc1 .Lgoo%=\n\t" \
  "s_min_i32 s93, s93, 8000\n\t" \
  "s_mov_b32 s94, s93\n\t" \
  ".Lslo%=:\n\t" \
  "s_sub_i32 s94, s94, 18\n\t" \
  "s_cmp_lt_i32 s94, 30\n\t" \
  "s_cbranch_scc1 .Lsxo%=\n\t" \
  "s_sleep 1\n\t" \
  "s_branch .Lslo%=\n\t" \
  ".Lsxo%=:\n\t" \
  "s_add_u32 s93, s93, 3\n\t" \
  "s_lshr_b32 s93, s93, 2\n\t" \
  "s_add_u32 s93, s93, s90\n\t" \
  "s_memrealtime s[90:91]\n\t" \
  ".Lpaceo%=:\n\t" \
  "s_memrealtime s[94:95]\n\t" \
  "s_waitcnt lgkmcnt(1)\n\t" \
  "s_sub_i32 s92, s90, s93\n\t" \
  "s_cmp_ge_i32 s92, 0\n\t" \
  "s_cbranch_scc1 .Leo%=\n\t" \
  "s_memrealtime s[90:91]\n\t" \
  "s_waitcnt lgkmcnt(1)\n\t" \
  "s_sub_i32 s92, s94, s93\n\t" \
  "s_cmp_lt_i32 s92, 0\n\t" \
  "s_cbranch_scc1 .Lpaceo%=\n\t" \
  ".Leo%=:\n\t" \
  ".Lgoo%=:\n\t" \
  "buffer_store_dwordx4 v[136:139], v200, %[rt], %[so] offen offset:0 sc1\n\t" \
  "buffer_store_dwordx4 v[140:143], v200, %[rt], %[so] offen offset:1024 sc1\n\t" \
  "s_waitcnt lgkmcnt(0)\n\t" \
  "s_branch .Lend%=\n\t" \
  ".Lpoll%=:\n\t" \
  "buffer_load_dwordx4 v[224:227], v200, %[rs], %[sk] offen offset:0 sc1\n\t" \
  "buffer_load_dwordx4 v[228:231], v200, %[rs], %[sk] offen offset:1024 sc1\n\t" \
  "s_branch .Ltake%=\n\t" \
  "s_branch .Lend%=\n\t" \
  ".Lslow%=:\n\t" \
  "s_mov_b64 s[94:95], 0\n\t" \
  "v_cmp_eq_u32 vcc, %[sent], v224\n\t" \
  "s_or_b64 s[94:95], s[94:95], vcc\n\t" \
  "v_cmp_eq_u32 vcc, %[sent], v225\n\t" \
  "s_or_b64 s[94:95], s[94:95], vcc\n\t" \
  "v_cmp_eq_u32 vcc, %[sent], v226\n\t" \
  "s_or_b64 s[94:95], s[94:95], vcc\n\t" \
  "v_cmp_eq_u32 vcc, %[sent], v227\n\t" \
  "s_or_b64 s[94:95], s[94:95], vcc\n\t" \
  "v_cmp_eq_u32 vcc, %[sent], v228\n\t" \
  "s_or_b64 s[94:95], s[94:95], vcc\n\t" \
  "v_cmp_eq_u32 vcc, %[sent], v229\n\t" \
  "s_or_b64 s[94:95], s[94:95], vcc\n\t" \
  "v_cmp_eq_u32 vcc, %[sent], v230\n\t" \
  "s_or_b64 s[94:95], s[94:95], vcc\n\t" \
  "v_cmp_eq_u32 vcc, %[sent], v231\n\t" \
  "s_or_b64 s[94:95], s[94:95], vcc\n\t" \
  "s_cmp_eq_u64 s[94:95], 0\n\t" \
  "s_cbranch_scc1 .Lok%=\n\t" \
  "s_add_u32 s92, s92, 1\n\t" \
  "s_sleep 6\n\t" \
  "s_cmp_eq_u32 s92, 1\n\t" \
  "s_cbranch_scc1 .Lpoll%=\n\t" \
  "s_sleep 1\n\t" \
  "s_and_b32 s93, s92, 63\n\t" \
  "s_cmp_lg_u32 s93, 0\n\t" \
  "s_cbranch_scc1 .Lpoll%=\n\t" \
  "v_mov_b32 v149, 0\n\t" \
  "buffer_load_dword v148, v149, %[rs], %[sab] offen sc1\n\t" \
  "s_waitcnt vmcnt(0)\n\t" \
  "v_readfirstlane_b32 s93, v148\n\t" \
  "s_nop 3\n\t" \
  "s_cmp_eq_u32 s93, 1\n\t" \
  "s_cbranch_scc1 .Lok%=\n\t" \
  "s_cmp_lt_u32 s92, 0x80000\n\t" \
  "s_cbranch_scc1 .Lpoll%=\n\t" \
  "v_mov_b32 v148, 1\n\t" \
  "buffer_store_dword v148, v149, %[rs], %[sab] offen sc1\n\t" \
  "s_branch .Lok%=\n\t" \
  ".Lend%=:\n\t"
constexpr int TLAG = 2;
#define RED_ASM \
  "s_mov_b32 %[step], 0\n\t" \
  "s_lshl_b32 %[sh], %[step], 15\n\t" \
  "s_lshr_b32 s93, %[step], 4\n\t" \
  "s_lshr_b32 s93, %[pm], s93\n\t" \
  "s_and_b32 s93, s93, 4\n\t" \
  "s_lshl_b32 s93, s93, 11\n\t" \
  "s_lshl2_add_u32 %[so], %[sh], s93\n\t" \
  "s_sub_i32 s93, %[step], 2\n\t" \
  "s_lshl_b32 %[st], s93, 19\n\t" \
  "s_lshl_b32 s94, s93, 11\n\t" \
  "s_xor_b32 s94, s94, %[s0]\n\t" \
  "s_and_b32 s94, s94, 0x2000\n\t" \
  "s_or_b32 %[st], %[st], s94\n\t" \
  "s_cmp_lt_i32 s93, 0\n\t" \
  "s_cselect_b32 %[st], 0x70000000, %[st]\n\t" \
  "s_add_u32 %[st4], %[st], 4096\n\t" \
  "buffer_load_dwordx2 v[166:167], v173, %[rs], 0 offen sc1\n\t" \
  "v_mov_b64 v[144:145], 0\n\t" \
  "v_mov_b64 v[146:147], 0\n\t" \
  "v_mov_b64 v[148:149], 0\n\t" \
  "v_mov_b64 v[150:151], 0\n\t" \
  "v_mov_b64 v[152:153], 0\n\t" \
  "v_mov_b64 v[154:155], 0\n\t" \
  "v_mov_b64 v[156:157], 0\n\t" \
  "v_mov_b64 v[158:159], 0\n\t" \
  ".Lstep%=:\n\t" \
  "s_setprio 3\n\t" \
  "s_mov_b32 s92, 0\n\t" \
  ".Lcal%=:\n\t" \
  "s_waitcnt vmcnt(0)\n\t" \
  "s_memrealtime s[76:77]\n\t" \
  "v_cmp_ne_u32 vcc, %[step], v166\n\t" \
  "v_readlane_b32 s90, v167, 0\n\t" \
  "v_readlane_b32 s91, v167, 1\n\t" \
  "v_readlane_b32 s94, v167, 2\n\t" \
  "v_readlane_b32 s95, v167, 3\n\t" \
  "s_sub_i32 s90, s90, %[base]\n\t" \
  "s_sub_i32 s91, s91, %[base]\n\t" \
  "s_sub_i32 s94, s94, %[base]\n\t" \
  "s_sub_i32 s95, s95, %[base]\n\t" \
  "s_max_i32 s90, s90, s91\n\t" \
  "s_max_i32 s94, s94, s95\n\t" \
  "s_max_i32 s90, s90, s94\n\t" \
  "s_add_i32 s90, s90, %[ds]\n\t" \
  "s_waitcnt lgkmcnt(0)\n\t" \
  "s_lshl2_add_u32 s76, s76, %[mn]\n\t" \
  "s_sub_i32 s76, s76, %[base]\n\t" \
  "s_add_u32 %[E], s90, %[base]\n\t" \
  "s_and_b32 s93, vcc_lo, 0xf\n\t" \
  "s_cbranch_scc1 .Lbl%=\n\t" \
  ".Lgo%=:\n\t" \
  "v_mov_b32 v166, 7\n\t" \
  "s_mov_b64 exec, 1\n\t" \
  "v_mov_b32 v164, s90\n\t" \
  "ds_max_u32 v168, v164 offset:4100\n\t" \
  "s_mov_b64 exec, -1\n\t" \
  "s_memrealtime s[90:91]\n\t" \
  "s_waitcnt lgkmcnt(0)\n\t" \
  "s_lshl_b32 s93, s90, 2\n\t" \
  "s_sub_i32 s93, %[E], s93\n\t" \
  "s_cmp_le_i32 s93, 0\n\t" \
  "s_cbranch_scc1 .Lgop%=\n\t" \
  "s_min_i32 s93, s93, 8000\n\t" \
  "s_mov_b32 s94, s93\n\t" \
  ".Lslp%=:\n\t" \
  "s_sub_i32 s94, s94, 18\n\t" \
  "s_cmp_lt_i32 s94, 30\n\t" \
  "s_cbranch_scc1 .Lsxp%=\n\t" \
  "s_sleep 1\n\t" \
  "s_branch .Lslp%=\n\t" \
  ".Lsxp%=:\n\t" \
  "s_sub_i32 s93, s93, 12\n\t" \
  "s_max_i32 s93, s93, 4\n\t" \
  "s_lshr_b32 s93, s93, 2\n\t" \
  "s_add_u32 s93, s93, s90\n\t" \
  "s_memrealtime s[90:91]\n\t" \
  ".Lpacep%=:\n\t" \
  "s_memrealtime s[94:95]\n\t" \
  "s_waitcnt lgkmcnt(1)\n\t" \
  "s_sub_i32 s92, s90, s93\n\t" \
  "s_cmp_ge_i32 s92, 0\n\t" \
  "s_cbranch_scc1 .Lep%=\n\t" \
  "s_memrealtime s[90:91]\n\t" \
  "s_waitcnt lgkmcnt(1)\n\t" \
  "s_sub_i32 s92, s94, s93\n\t" \
  "s_cmp_lt_i32 s92, 0\n\t" \
  "s_cbranch_scc1 .Lpacep%=\n\t" \
  "s_nop 4\n\t" \
  ".Lep%=:\n\t" \
  "s_add_u32 s92, s92, s93\n\t" \
  "s_lshl_b32 s92, s92, 2\n\t" \
  "s_sub_i32 s92, %[E], s92\n\t" \
  "s_max_i32 s92, s92, 0\n\t" \
  "s_bitcmp0_b32 s92, 3\n\t" \
  "s_cbranch_scc1 .Lf3p%=\n\t" \
  "s_nop 15\n\t" \
  ".Lf3p%=:\n\t" \
  "s_bitcmp0_b32 s92, 2\n\t" \
  "s_cbranch_scc1 .Lf2p%=\n\t" \
  "s_nop 10\n\t" \
  ".Lf2p%=:\n\t" \
  ".Lgop%=:\n\t" \
  "s_mov_b32 s92, 0\n\t" \
  "buffer_load_dwordx4 v[128:131], v234, %[rs], %[so] offen sc1\n\t" \
  "s_nop 15\n\t" \
  "buffer_load_dwordx4 v[132:135], v235, %[rs], %[so] offen sc1\n\t" \
  "s_nop 15\n\t" \
  "buffer_load_dwordx4 v[136:139], v236, %[rs], %[so] offen sc1\n\t" \
  "s_nop 15\n\t" \
  "buffer_load_dwordx4 v[140:143], v237, %[rs], %[so] offen sc1\n\t" \
  "s_waitcnt lgkmcnt(0)\n\t" \
  ".Lw%=:\n\t" \
  "s_waitcnt vmcnt(3)\n\t" \
  "v_smfmac_f32_16x16x64_bf16 v[144:147], v[128:131], v[0:7], v172\n\t" \
  "v_max3_u32 v164, v128, v129, 0\n\t" \
  "v_smfmac_f32_16x16x64_bf16 v[148:151], v[128:131], v[8:15], v172\n\t" \
  "v_max3_u32 v164, v130, v131, v164\n\t" \
  "v_smfmac_f32_16x16x64_bf16 v[152:155], v[128:131], v[16:23], v172\n\t" \
  "v_smfmac_f32_16x16x64_bf16 v[156:159], v[128:131], v[24:31], v172\n\t" \
  "buffer_load_dwordx4 v[176:179], v238, %[rt], %[st] offen offset:0 sc1\n\t" \
  "s_waitcnt vmcnt(3)\n\t" \
  "v_smfmac_f32_16x16x64_bf16 v[144:147], v[132:135], v[32:39], v172\n\t" \
  "v_max3_u32 v164, v132, v133, v164\n\t" \
  "v_smfmac_f32_16x16x64_bf16 v[148:151], v[132:135], v[40:47], v172\n\t" \
  "v_max3_u32 v164, v134, v135, v164\n\t" \
  "v_smfmac_f32_16x16x64_bf16 v[152:155], v[132:135], v[48:55], v172\n\t" \
  "v_smfmac_f32_16x16x64_bf16 v[156:159], v[132:135], v[56:63], v172\n\t" \
  "buffer_load_dwordx4 v[180:183], v238, %[rt], %[st] offen offset:1024 sc1\n\t" \
  "s_waitcnt vmcnt(3)\n\t" \
  "v_smfmac_f32_16x16x64_bf16 v[144:147], v[136:139], v[64:71], v172\n\t" \
  "v_max3_u32 v164, v136, v137, v164\n\t" \
  "v_smfmac_f32_16x16x64_bf16 v[148:151], v[136:139], v[72:79], v172\n\t" \
  "v_max3_u32 v164, v138, v139, v164\n\t" \
  "v_smfmac_f32_16x16x64_bf16 v[152:155], v[136:139], v[80:87], v172\n\t" \
  "v_smfmac_f32_16x16x64_bf16 v[156:159], v[136:139], v[88:95], v172\n\t" \
  "buffer_load_dwordx4 v[184:187], v238, %[rt], %[st4] offen offset:0 sc1\n\t" \
  "s_waitcnt vmcnt(3)\n\t" \
  "v_smfmac_f32_16x16x64_bf16 v[144:147], v[140:143], v[96:103], v172\n\t" \
  "v_max3_u32 v164, v140, v141, v164\n\t" \
  "v_smfmac_f32_16x16x64_bf16 v[148:151], v[140:143], v[104:111], v172\n\t" \
  "v_max3_u32 v164, v142, v143, v164\n\t" \
  "v_smfmac_f32_16x16x64_bf16 v[152:155], v[140:143], v[112:119], v172\n\t" \
  "v_cmp_le_u32 vcc, %[sent], v164\n\t" \
  "v_smfmac_f32_16x16x64_bf16 v[156:159], v[140:143], v[120:127], v172\n\t" \
  "buffer_load_dwordx4 v[188:191], v238, %[rt], %[st4] offen offset:1024 sc1\n\t" \
  "s_cbranch_vccnz .Lslow%=\n\t" \
  ".Lok%=:\n\t" \
  "v_add_f32_dpp v160, v144, v146 quad_perm:[0,1,2,3] row_mask:0x3 bank_mask:0xf\n\t" \
  "v_add_f32_dpp v161, v145, v147 quad_perm:[0,1,2,3] row_mask:0x3 bank_mask:0xf\n\t" \
  "v_add_f32_dpp v162, v148, v150 quad_perm:[0,1,2,3] row_mask:0x3 bank_mask:0xf\n\t" \
  "v_add_f32_dpp v163, v149, v151 quad_perm:[0,1,2,3] row_mask:0x3 bank_mask:0xf\n\t" \
  "v_add_f32_dpp v160, v152, v154 quad_perm:[0,1,2,3] row_mask:0xc bank_mask:0xf\n\t" \
  "v_add_f32_dpp v161, v153, v155 quad_perm:[0,1,2,3] row_mask:0xc bank_mask:0xf\n\t" \
  "v_add_f32_dpp v162, v156, v158 quad_perm:[0,1,2,3] row_mask:0xc bank_mask:0xf\n\t" \
  "v_add_f32_dpp v163, v157, v159 quad_perm:[0,1,2,3] row_mask:0xc bank_mask:0xf\n\t" \
  "s_nop 0\n\t" \
  "v_permlane32_swap_b32 v160, v162\n\t" \
  "v_permlane32_swap_b32 v161, v163\n\t" \
  "v_add_f32 v160, v160, v162\n\t" \
  "v_add_f32 v161, v161, v163\n\t" \
  "ds_write_b64 v169, v[160:161]\n\t" \
  "s_mov_b64 exec, 1\n\t" \
  "ds_inc_rtn_u32 v164, v168, v166 offset:4104\n\t" \
  "s_mov_b64 exec, -1\n\t" \
  "ds_read_b64 v[128:129], v170 offset:0\n\t" \
  "ds_read_b64 v[130:131], v170 offset:512\n\t" \
  "ds_read_b64 v[132:133], v170 offset:1024\n\t" \
  "ds_read_b64 v[134:135], v170 offset:1536\n\t" \
  "ds_read_b64 v[136:137], v170 offset:2048\n\t" \
  "ds_read_b64 v[138:139], v170 offset:2560\n\t" \
  "ds_read_b64 v[140:141], v170 offset:3072\n\t" \
  "ds_read_b64 v[142:143], v170 offset:3584\n\t" \
  "s_waitcnt lgkmcnt(8)\n\t" \
  "v_readfirstlane_b32 s93, v164\n\t" \
  "s_cmp_lg_u32 s93, 7\n\t" \
  "s_cbranch_scc1 .Lb1%=\n\t" \
  "s_waitcnt lgkmcnt(4)\n\t" \
  "v_pk_add_f32 v[128:129], v[128:129], v[130:131]\n\t" \
  "v_pk_add_f32 v[132:133], v[132:133], v[134:135]\n\t" \
  "s_waitcnt lgkmcnt(0)\n\t" \
  "v_pk_add_f32 v[136:137], v[136:137], v[138:139]\n\t" \
  "v_pk_add_f32 v[140:141], v[140:141], v[142:143]\n\t" \
  "v_pk_add_f32 v[128:129], v[128:129], v[132:133]\n\t" \
  "v_pk_add_f32 v[136:137], v[136:137], v[140:141]\n\t" \
  "v_pk_add_f32 v[128:129], v[128:129], v[136:137]\n\t" \
  "v_cvt_pk_bf16_f32 v128, v128, v129\n\t" \
  "s_nop 1\n\t" \
  "v_mov_b32_dpp v129, v128 quad_perm:[1,0,3,2] row_mask:0xf bank_mask:0xf\n\t" \
  "v_perm_b32 v128, v129, v128, v174\n\t" \
  "buffer_store_dword v128, v171, %[rs], %[sh] offen sc1\n\t" \
  "s_memtime s[90:91]\n\t" \
  "s_barrier\n\t" \
  "s_memtime s[76:77]\n\t" \
  "s_memrealtime s[78:79]\n\t" \
  "s_nop 3\n\t" \
  "s_memrealtime s[80:81]\n\t" \
  "s_nop 3\n\t" \
  "s_memrealtime s[82:83]\n\t" \
  "s_nop 3\n\t" \
  "s_memrealtime s[84:85]\n\t" \
  "s_nop 3\n\t" \
  "s_memrealtime s[86:87]\n\t" \
  "s_nop 3\n\t" \
  "s_memrealtime s[88:89]\n\t" \
  "s_mov_b32 s93, 0\n\t" \
  "s_waitcnt lgkmcnt(0)\n\t" \
  "s_cmp_eq_u32 s80, s78\n\t" \
  "s_addc_u32 s93, s93, 0\n\t" \
  "s_cmp_eq_u32 s82, s78\n\t" \
  "s_addc_u32 s93, s93, 0\n\t" \
  "s_cmp_eq_u32 s84, s78\n\t" \
  "s_addc_u32 s93, s93, 0\n\t" \
  "s_cmp_eq_u32 s86, s78\n\t" \
  "s_addc_u32 s93, s93, 0\n\t" \
  "s_cmp_eq_u32 s88, s78\n\t" \
  "s_addc_u32 s93, s93, 0\n\t" \
  "s_mul_i32 s93, s93, 21\n\t" \
  "s_add_u32 s76, s76, 15\n\t" \
  "s_add_u32 s76, s76, s93\n\t" \
  "s_lshl2_add_u32 s78, s78, 16\n\t" \
  "s_mov_b64 exec, 1\n\t" \
  "s_sub_i32 s90, s76, s90\n\t" \
  "s_mul_i32 s90, s90, 11900\n\t" \
  "s_ashr_i32 s90, s90, 16\n\t" \
  "s_sub_u32 s90, s78, s90\n\t" \
  "s_sub_u32 s90, s90, %[base]\n\t" \
  "v_mov_b32 v164, s90\n\t" \
  "ds_write_b32 v168, v164 offset:4108\n\t" \
  "s_mov_b64 exec, -1\n\t" \
  "s_waitcnt vmcnt(1)\n\t" \
  ".Lb2%=:\n\t" \
  "s_setprio 0\n\t" \
  "s_bitcmp0_b64 %[am], %[step]\n\t" \
  "s_cbranch_scc1 .La1%=\n\t" \
  "buffer_store_dwordx4 v[224:227], v228, %[ra], 0 offen sc1\n\t" \
  ".La1%=:\n\t" \
  "buffer_load_dwordx4 v[192:195], v239, %[rt], %[st] offen offset:0 sc1\n\t" \
  "buffer_load_dwordx4 v[196:199], v239, %[rt], %[st] offen offset:1024 sc1\n\t" \
  "v_mfma_f32_16x16x32_bf16 v[128:131], v[0:3], v[176:179], 0\n\t" \
  "v_mfma_f32_16x16x32_bf16 v[136:139], v[8:11], v[176:179], 0\n\t" \
  "ds_read2st64_b32 v[144:145], v231 offset0:0 offset1:8\n\t" \
  "ds_read2st64_b32 v[146:147], v231 offset0:16 offset1:24\n\t" \
  "ds_read2st64_b32 v[148:149], v231 offset0:32 offset1:40\n\t" \
  "ds_read2st64_b32 v[150:151], v231 offset0:48 offset1:56\n\t" \
  "buffer_load_dwordx4 v[200:203], v239, %[rt], %[st4] offen offset:0 sc1\n\t" \
  "buffer_load_dwordx4 v[204:207], v239, %[rt], %[st4] offen offset:1024 sc1\n\t" \
  "v_mfma_f32_16x16x32_bf16 v[132:135], v[4:7], v[180:183], 0\n\t" \
  "v_mfma_f32_16x16x32_bf16 v[140:143], v[12:15], v[180:183], 0\n\t" \
  "s_waitcnt lgkmcnt(0)\n\t" \
  "v_add_f32 v144, v144, v145\n\t" \
  "v_add_f32 v146, v146, v147\n\t" \
  "v_add_f32 v148, v148, v149\n\t" \
  "v_add_f32 v150, v150, v151\n\t" \
  "v_add_f32 v144, v144, v146\n\t" \
  "v_add_f32 v148, v148, v150\n\t" \
  "buffer_load_dwordx4 v[208:211], v240, %[rt], %[st] offen offset:0 sc1\n\t" \
  "buffer_load_dwordx4 v[212:215], v240, %[rt], %[st] offen offset:1024 sc1\n\t" \
  "v_mfma_f32_16x16x32_bf16 v[128:131], v[16:19], v[184:187], v[128:131]\n\t" \
  "v_mfma_f32_16x16x32_bf16 v[136:139], v[24:27], v[184:187], v[136:139]\n\t" \
  "v_add_f32 v144, v144, v148\n\t" \
  "v_cvt_pk_bf16_f32 v144, v144, v144\n\t" \
  "ds_write_b16 v232, v144\n\t" \
  "ds_read_b128 v[224:227], v229\n\t" \
  "v_add_u32 v228, %[ainc], v228\n\t" \
  "v_add_u32 v229, %[rinc], v229\n\t" \
  "v_add_u32 v232, 1024, v232\n\t" \
  "v_xor_b32 v231, 0x4000, v231\n\t" \
  "buffer_load_dwordx4 v[216:219], v240, %[rt], %[st4] offen offset:0 sc1\n\t" \
  "buffer_load_dwordx4 v[220:223], v240, %[rt], %[st4] offen offset:1024 sc1\n\t" \
  "v_mfma_f32_16x16x32_bf16 v[132:135], v[20:23], v[188:191], v[132:135]\n\t" \
  "v_mfma_f32_16x16x32_bf16 v[140:143], v[28:31], v[188:191], v[140:143]\n\t" \
  "buffer_load_dwordx4 v[176:179], v241, %[rt], %[st] offen offset:0 sc1\n\t" \
  "s_waitcnt vmcnt(8)\n\t" \
  "v_mfma_f32_16x16x32_bf16 v[128:131], v[32:35], v[192:195], v[128:131]\n\t" \
  "v_mfma_f32_16x16x32_bf16 v[136:139], v[40:43], v[192:195], v[136:139]\n\t" \
  "buffer_load_dwordx4 v[180:183], v241, %[rt], %[st] offen offset:1024 sc1\n\t" \
  "s_waitcnt vmcnt(8)\n\t" \
  "v_mfma_f32_16x16x32_bf16 v[132:135], v[36:39], v[196:199], v[132:135]\n\t" \
  "v_mfma_f32_16x16x32_bf16 v[140:143], v[44:47], v[196:199], v[140:143]\n\t" \
  "buffer_load_dwordx4 v[184:187], v241, %[rt], %[st4] offen offset:0 sc1\n\t" \
  "s_waitcnt vmcnt(8)\n\t" \
  "v_mfma_f32_16x16x32_bf16 v[128:131], v[48:51], v[200:203], v[128:131]\n\t" \
  "v_mfma_f32_16x16x32_bf16 v[136:139], v[56:59], v[200:203], v[136:139]\n\t" \
  "ds_read_b128 v[160:163], v168 offset:4096\n\t" \
  "buffer_load_dwordx4 v[188:191], v241, %[rt], %[st4] offen offset:1024 sc1\n\t" \
  "s_waitcnt vmcnt(8)\n\t" \
  "v_mfma_f32_16x16x32_bf16 v[132:135], v[52:55], v[204:207], v[132:135]\n\t" \
  "v_mfma_f32_16x16x32_bf16 v[140:143], v[60:63], v[204:207], v[140:143]\n\t" \
  "s_waitcnt lgkmcnt(0)\n\t" \
  "v_readfirstlane_b32 s90, v161\n\t" \
  "v_readfirstlane_b32 s91, v163\n\t" \
  "s_add_u32 s93, s90, %[rl]\n\t" \
  "s_add_u32 s93, s93, %[base]\n\t" \
  "s_add_u32 %[E], s93, %[lk]\n\t" \
  "s_sub_i32 s91, s91, s90\n\t" \
  "s_sub_i32 s91, s91, %[rl]\n\t" \
  "s_min_i32 s93, s91, %[up]\n\t" \
  "s_max_i32 s93, s93, %[dn]\n\t" \
  "s_cmp_gt_i32 s91, 60\n\t" \
  "s_cselect_b32 s93, 2, s93\n\t" \
  "s_add_i32 %[rl], %[rl], s93\n\t" \
  "v_mov_b32 v160, %[rl]\n\t" \
  "buffer_store_dword v160, v175, %[rs], 0 offen sc1\n\t" \
  "s_waitcnt vmcnt(8)\n\t" \
  "v_mfma_f32_16x16x32_bf16 v[128:131], v[64:67], v[208:211], v[128:131]\n\t" \
  "v_mfma_f32_16x16x32_bf16 v[136:139], v[72:75], v[208:211], v[136:139]\n\t" \
  "s_add_u32 %[step], %[step], 1\n\t" \
  "s_lshl_b32 %[sh], %[step], 15\n\t" \
  "s_lshr_b32 s93, %[step], 4\n\t" \
  "s_lshr_b32 s93, %[pm], s93\n\t" \
  "s_and_b32 s93, s93, 4\n\t" \
  "s_lshl_b32 s93, s93, 11\n\t" \
  "s_lshl2_add_u32 %[so], %[sh], s93\n\t" \
  "s_waitcnt vmcnt(7)\n\t" \
  "v_mfma_f32_16x16x32_bf16 v[132:135], v[68:71], v[212:215], v[132:135]\n\t" \
  "v_mfma_f32_16x16x32_bf16 v[140:143], v[76:79], v[212:215], v[140:143]\n\t" \
  "s_sub_i32 s93, %[step], 2\n\t" \
  "s_lshl_b32 %[st], s93, 19\n\t" \
  "s_lshl_b32 s94, s93, 11\n\t" \
  "s_xor_b32 s94, s94, %[s0]\n\t" \
  "s_and_b32 s94, s94, 0x2000\n\t" \
  "s_or_b32 %[st], %[st], s94\n\t" \
  "s_cmp_lt_i32 s93, 0\n\t" \
  "s_cselect_b32 %[st], 0x70000000, %[st]\n\t" \
  "s_add_u32 %[st4], %[st], 4096\n\t" \
  "s_waitcnt vmcnt(6)\n\t" \
  "v_mfma_f32_16x16x32_bf16 v[128:131], v[80:83], v[216:219], v[128:131]\n\t" \
  "v_mfma_f32_16x16x32_bf16 v[136:139], v[88:91], v[216:219], v[136:139]\n\t" \
  "s_waitcnt vmcnt(5)\n\t" \
  "v_mfma_f32_16x16x32_bf16 v[132:135], v[84:87], v[220:223], v[132:135]\n\t" \
  "v_mfma_f32_16x16x32_bf16 v[140:143], v[92:95], v[220:223], v[140:143]\n\t" \
  "s_cmp_eq_u32 %[step], 63\n\t" \
  "s_cbranch_scc1 .Llast%=\n\t" \
  "s_movk_i32 s93, 600\n\t" \
  ".Lpacel%=:\n\t" \
  "s_memrealtime s[90:91]\n\t" \
  "s_waitcnt lgkmcnt(0)\n\t" \
  "s_lshl_b32 s90, s90, 2\n\t" \
  "s_sub_i32 s91, %[E], s90\n\t" \
  "s_cmp_le_i32 s91, 0\n\t" \
  "s_cbranch_scc1 .Lgol%=\n\t" \
  "s_sub_u32 s93, s93, 1\n\t" \
  "s_cmp_lg_u32 s93, 0\n\t" \
  "s_cbranch_scc1 .Lpacel%=\n\t" \
  ".Lgol%=:\n\t" \
  "buffer_load_dwordx2 v[166:167], v173, %[rs], 0 offen sc1\n\t" \
  "s_waitcnt vmcnt(5)\n\t" \
  "v_mfma_f32_16x16x32_bf16 v[128:131], v[96:99], v[176:179], v[128:131]\n\t" \
  "v_mfma_f32_16x16x32_bf16 v[136:139], v[104:107], v[176:179], v[136:139]\n\t" \
  "s_waitcnt vmcnt(4)\n\t" \
  "v_mfma_f32_16x16x32_bf16 v[132:135], v[100:103], v[180:183], v[132:135]\n\t" \
  "v_mfma_f32_16x16x32_bf16 v[140:143], v[108:111], v[180:183], v[140:143]\n\t" \
  "s_waitcnt vmcnt(3)\n\t" \
  "v_mfma_f32_16x16x32_bf16 v[128:131], v[112:115], v[184:187], v[128:131]\n\t" \
  "v_mfma_f32_16x16x32_bf16 v[136:139], v[120:123], v[184:187], v[136:139]\n\t" \
  "s_waitcnt vmcnt(2)\n\t" \
  "v_mfma_f32_16x16x32_bf16 v[132:135], v[116:119], v[188:191], v[132:135]\n\t" \
  "v_mfma_f32_16x16x32_bf16 v[140:143], v[124:127], v[188:191], v[140:143]\n\t" \
  "v_mov_b64 v[144:145], 0\n\t" \
  "v_mov_b64 v[146:147], 0\n\t" \
  "v_mov_b64 v[148:149], 0\n\t" \
  "v_mov_b64 v[150:151], 0\n\t" \
  "v_mov_b64 v[152:153], 0\n\t" \
  "v_mov_b64 v[154:155], 0\n\t" \
  "v_mov_b64 v[156:157], 0\n\t" \
  "v_mov_b64 v[158:159], 0\n\t" \
  "v_pk_add_f32 v[128:129], v[128:129], v[132:133]\n\t" \
  "v_pk_add_f32 v[130:131], v[130:131], v[134:135]\n\t" \
  "ds_write_b128 v230, v[128:131] offset:0\n\t" \
  "v_pk_add_f32 v[136:137], v[136:137], v[140:141]\n\t" \
  "v_pk_add_f32 v[138:139], v[138:139], v[142:143]\n\t" \
  "ds_write_b128 v230, v[136:139] offset:1024\n\t" \
  "v_xor_b32 v230, 0x4000, v230\n\t" \
  "s_cmp_lg_u32 %[step], 63\n\t" \
  "s_cbranch_scc1 .Lstep%=\n\t" \
  ".Lfc%=:\n\t" \
  "s_waitcnt vmcnt(0)\n\t" \
  "v_cmp_eq_u32 vcc, -1, v166\n\t" \
  "s_cbranch_vccnz .Lfw%=\n\t" \
  ".Lfo%=:\n\t" \
  "v_add_u32 v242, 16384, v242\n\t" \
  "buffer_load_dwordx4 v[176:179], v238, %[rt], %[st] offen offset:0 sc1\n\t" \
  "buffer_load_dwordx4 v[180:183], v238, %[rt], %[st] offen offset:1024 sc1\n\t" \
  "buffer_load_dwordx4 v[184:187], v238, %[rt], %[st4] offen offset:0 sc1\n\t" \
  "buffer_load_dwordx4 v[188:191], v238, %[rt], %[st4] offen offset:1024 sc1\n\t" \
  "buffer_load_dwordx4 v[192:195], v239, %[rt], %[st] offen offset:0 sc1\n\t" \
  "buffer_load_dwordx4 v[196:199], v239, %[rt], %[st] offen offset:1024 sc1\n\t" \
  "buffer_load_dwordx4 v[200:203], v239, %[rt], %[st4] offen offset:0 sc1\n\t" \
  "buffer_load_dwordx4 v[204:207], v239, %[rt], %[st4] offen offset:1024 sc1\n\t" \
  "v_subrev_u32 v230, 98304, v230\n\t" \
  ".Lt%=:\n\t" \
  "s_waitcnt vmcnt(7)\n\t" \
  "v_mfma_f32_16x16x32_bf16 v[128:131], v[0:3], v[176:179], 0\n\t" \
  "v_mfma_f32_16x16x32_bf16 v[136:139], v[8:11], v[176:179], 0\n\t" \
  "buffer_load_dwordx4 v[176:179], v240, %[rt], %[st] offen offset:0 sc1\n\t" \
  "buffer_load_dword v166, v242, %[rs], 0 offen sc1\n\t" \
  "s_waitcnt vmcnt(8)\n\t" \
  "v_mfma_f32_16x16x32_bf16 v[132:135], v[4:7], v[180:183], 0\n\t" \
  "v_mfma_f32_16x16x32_bf16 v[140:143], v[12:15], v[180:183], 0\n\t" \
  "buffer_load_dwordx4 v[180:183], v240, %[rt], %[st] offen offset:1024 sc1\n\t" \
  "s_waitcnt vmcnt(8)\n\t" \
  "v_mfma_f32_16x16x32_bf16 v[128:131], v[16:19], v[184:187], v[128:131]\n\t" \
  "v_mfma_f32_16x16x32_bf16 v[136:139], v[24:27], v[184:187], v[136:139]\n\t" \
  "buffer_load_dwordx4 v[184:187], v240, %[rt], %[st4] offen offset:0 sc1\n\t" \
  "s_waitcnt vmcnt(8)\n\t" \
  "v_mfma_f32_16x16x32_bf16 v[132:135], v[20:23], v[188:191], v[132:135]\n\t" \
  "v_mfma_f32_16x16x32_bf16 v[140:143], v[28:31], v[188:191], v[140:143]\n\t" \
  "buffer_load_dwordx4 v[188:191], v240, %[rt], %[st4] offen offset:1024 sc1\n\t" \
  "s_waitcnt vmcnt(8)\n\t" \
  "v_mfma_f32_16x16x32_bf16 v[128:131], v[32:35], v[192:195], v[128:131]\n\t" \
  "v_mfma_f32_16x16x32_bf16 v[136:139], v[40:43], v[192:195], v[136:139]\n\t" \
  "buffer_load_dwordx4 v[192:195], v241, %[rt], %[st] offen offset:0 sc1\n\t" \
  "s_waitcnt vmcnt(8)\n\t" \
  "v_mfma_f32_16x16x32_bf16 v[132:135], v[36:39], v[196:199], v[132:135]\n\t" \
  "v_mfma_f32_16x16x32_bf16 v[140:143], v[44:47], v[196:199], v[140:143]\n\t" \
  "buffer_load_dwordx4 v[196:199], v241, %[rt], %[st] offen offset:1024 sc1\n\t" \
  "s_waitcnt vmcnt(8)\n\t" \
  "v_mfma_f32_16x16x32_bf16 v[128:131], v[48:51], v[200:203], v[128:131]\n\t" \
  "v_mfma_f32_16x16x32_bf16 v[136:139], v[56:59], v[200:203], v[136:139]\n\t" \
  "buffer_load_dwordx4 v[200:203], v241, %[rt], %[st4] offen offset:0 sc1\n\t" \
  "s_waitcnt vmcnt(8)\n\t" \
  "v_mfma_f32_16x16x32_bf16 v[132:135], v[52:55], v[204:207], v[132:135]\n\t" \
  "v_mfma_f32_16x16x32_bf16 v[140:143], v[60:63], v[204:207], v[140:143]\n\t" \
  "buffer_load_dwordx4 v[204:207], v241, %[rt], %[st4] offen offset:1024 sc1\n\t" \
  "s_waitcnt vmcnt(8)\n\t" \
  "v_mfma_f32_16x16x32_bf16 v[128:131], v[64:67], v[176:179], v[128:131]\n\t" \
  "v_mfma_f32_16x16x32_bf16 v[136:139], v[72:75], v[176:179], v[136:139]\n\t" \
  "s_waitcnt vmcnt(7)\n\t" \
  ".Ltc%=:\n\t" \
  "v_cmp_eq_u32 vcc, -1, v166\n\t" \
  "s_cbranch_vccnz .Ltw%=\n\t" \
  ".Lto%=:\n\t" \
  "s_cmp_eq_u32 %[step], 65\n\t" \
  "s_cbranch_scc1 .Ltl%=\n\t" \
  "s_add_u32 %[step], %[step], 1\n\t" \
  "s_sub_i32 s93, %[step], 2\n\t" \
  "s_lshl_b32 %[st], s93, 19\n\t" \
  "s_lshl_b32 s94, s93, 11\n\t" \
  "s_xor_b32 s94, s94, %[s0]\n\t" \
  "s_and_b32 s94, s94, 0x2000\n\t" \
  "s_or_b32 %[st], %[st], s94\n\t" \
  "s_cmp_lt_i32 s93, 0\n\t" \
  "s_cselect_b32 %[st], 0x70000000, %[st]\n\t" \
  "s_add_u32 %[st4], %[st], 4096\n\t" \
  "v_add_u32 v242, 16384, v242\n\t" \
  "s_cmp_eq_u32 %[step], 65\n\t" \
  "s_cbranch_scc0 .Ltn%=\n\t" \
  "v_mov_b32 v242, %[fg]\n\t" \
  ".Ltn%=:\n\t" \
  "buffer_load_dwordx4 v[176:179], v238, %[rt], %[st] offen offset:0 sc1\n\t" \
  "s_waitcnt vmcnt(7)\n\t" \
  "v_mfma_f32_16x16x32_bf16 v[132:135], v[68:71], v[180:183], v[132:135]\n\t" \
  "v_mfma_f32_16x16x32_bf16 v[140:143], v[76:79], v[180:183], v[140:143]\n\t" \
  "buffer_load_dwordx4 v[180:183], v238, %[rt], %[st] offen offset:1024 sc1\n\t" \
  "s_waitcnt vmcnt(7)\n\t" \
  "v_mfma_f32_16x16x32_bf16 v[128:131], v[80:83], v[184:187], v[128:131]\n\t" \
  "v_mfma_f32_16x16x32_bf16 v[136:139], v[88:91], v[184:187], v[136:139]\n\t" \
  "buffer_load_dwordx4 v[184:187], v238, %[rt], %[st4] offen offset:0 sc1\n\t" \
  "s_waitcnt vmcnt(7)\n\t" \
  "v_mfma_f32_16x16x32_bf16 v[132:135], v[84:87], v[188:191], v[132:135]\n\t" \
  "v_mfma_f32_16x16x32_bf16 v[140:143], v[92:95], v[188:191], v[140:143]\n\t" \
  "buffer_load_dwordx4 v[188:191], v238, %[rt], %[st4] offen offset:1024 sc1\n\t" \
  "s_waitcnt vmcnt(7)\n\t" \
  "v_mfma_f32_16x16x32_bf16 v[128:131], v[96:99], v[192:195], v[128:131]\n\t" \
  "v_mfma_f32_16x16x32_bf16 v[136:139], v[104:107], v[192:195], v[136:139]\n\t" \
  "buffer_load_dwordx4 v[192:195], v239, %[rt], %[st] offen offset:0 sc1\n\t" \
  "s_waitcnt vmcnt(7)\n\t" \
  "v_mfma_f32_16x16x32_bf16 v[132:135], v[100:103], v[196:199], v[132:135]\n\t" \
  "v_mfma_f32_16x16x32_bf16 v[140:143], v[108:111], v[196:199], v[140:143]\n\t" \
  "buffer_load_dwordx4 v[196:199], v239, %[rt], %[st] offen offset:1024 sc1\n\t" \
  "s_waitcnt vmcnt(7)\n\t" \
  "v_mfma_f32_16x16x32_bf16 v[128:131], v[112:115], v[200:203], v[128:131]\n\t" \
  "v_mfma_f32_16x16x32_bf16 v[136:139], v[120:123], v[200:203], v[136:139]\n\t" \
  "buffer_load_dwordx4 v[200:203], v239, %[rt], %[st4] offen offset:0 sc1\n\t" \
  "s_waitcnt vmcnt(7)\n\t" \
  "v_mfma_f32_16x16x32_bf16 v[132:135], v[116:119], v[204:207], v[132:135]\n\t" \
  "v_mfma_f32_16x16x32_bf16 v[140:143], v[124:127], v[204:207], v[140:143]\n\t" \
  "buffer_load_dwordx4 v[204:207], v239, %[rt], %[st4] offen offset:1024 sc1\n\t" \
  "s_nop 5\n\t" \
  "v_pk_add_f32 v[128:129], v[128:129], v[132:133]\n\t" \
  "v_pk_add_f32 v[130:131], v[130:131], v[134:135]\n\t" \
  "ds_write_b128 v230, v[128:131] offset:0\n\t" \
  "v_pk_add_f32 v[136:137], v[136:137], v[140:141]\n\t" \
  "v_pk_add_f32 v[138:139], v[138:139], v[142:143]\n\t" \
  "ds_write_b128 v230, v[136:139] offset:1024\n\t" \
  "v_add_u32 v230, 16384, v230\n\t" \
  "s_branch .Lt%=\n\t" \
  ".Ltl%=:\n\t" \
  "buffer_load_dwordx4 v[224:227], v243, %[rg], 0 offen sc1\n\t" \
  "s_waitcnt vmcnt(7)\n\t" \
  "v_mfma_f32_16x16x32_bf16 v[132:135], v[68:71], v[180:183], v[132:135]\n\t" \
  "v_mfma_f32_16x16x32_bf16 v[140:143], v[76:79], v[180:183], v[140:143]\n\t" \
  "s_waitcnt vmcnt(6)\n\t" \
  "v_mfma_f32_16x16x32_bf16 v[128:131], v[80:83], v[184:187], v[128:131]\n\t" \
  "v_mfma_f32_16x16x32_bf16 v[136:139], v[88:91], v[184:187], v[136:139]\n\t" \
  "s_waitcnt vmcnt(5)\n\t" \
  "v_mfma_f32_16x16x32_bf16 v[132:135], v[84:87], v[188:191], v[132:135]\n\t" \
  "v_mfma_f32_16x16x32_bf16 v[140:143], v[92:95], v[188:191], v[140:143]\n\t" \
  "s_waitcnt vmcnt(4)\n\t" \
  "v_mfma_f32_16x16x32_bf16 v[128:131], v[96:99], v[192:195], v[128:131]\n\t" \
  "v_mfma_f32_16x16x32_bf16 v[136:139], v[104:107], v[192:195], v[136:139]\n\t" \
  "s_waitcnt vmcnt(3)\n\t" \
  "v_mfma_f32_16x16x32_bf16 v[132:135], v[100:103], v[196:199], v[132:135]\n\t" \
  "v_mfma_f32_16x16x32_bf16 v[140:143], v[108:111], v[196:199], v[140:143]\n\t" \
  "s_waitcnt vmcnt(2)\n\t" \
  "v_mfma_f32_16x16x32_bf16 v[128:131], v[112:115], v[200:203], v[128:131]\n\t" \
  "v_mfma_f32_16x16x32_bf16 v[136:139], v[120:123], v[200:203], v[136:139]\n\t" \
  "s_waitcnt vmcnt(1)\n\t" \
  "v_mfma_f32_16x16x32_bf16 v[132:135], v[116:119], v[204:207], v[132:135]\n\t" \
  "v_mfma_f32_16x16x32_bf16 v[140:143], v[124:127], v[204:207], v[140:143]\n\t" \
  "s_nop 6\n\t" \
  "v_pk_add_f32 v[128:129], v[128:129], v[132:133]\n\t" \
  "v_pk_add_f32 v[130:131], v[130:131], v[134:135]\n\t" \
  "ds_write_b128 v230, v[128:131] offset:0\n\t" \
  "v_pk_add_f32 v[136:137], v[136:137], v[140:141]\n\t" \
  "v_pk_add_f32 v[138:139], v[138:139], v[142:143]\n\t" \
  "ds_write_b128 v230, v[136:139] offset:1024\n\t" \
  "s_waitcnt vmcnt(0) lgkmcnt(0)\n\t" \
  "s_branch .Lend%=\n\t" \
  ".Ltw%=:\n\t" \
  "s_add_u32 s92, s92, 1\n\t" \
  "s_sleep 2\n\t" \
  "s_and_b32 s93, s92, 63\n\t" \
  "s_cbranch_scc1 .Ltr%=\n\t" \
  "buffer_load_dword v164, off, %[rs], %[sab] sc1\n\t" \
  "s_waitcnt vmcnt(0)\n\t" \
  "v_readfirstlane_b32 s93, v164\n\t" \
  "s_nop 3\n\t" \
  "s_cmp_eq_u32 s93, 1\n\t" \
  "s_cbranch_scc1 .Lto%=\n\t" \
  "s_cmp_lt_u32 s92, 0x80000\n\t" \
  "s_cbranch_scc1 .Ltr%=\n\t" \
  "v_mov_b32 v164, 1\n\t" \
  "buffer_store_dword v164, off, %[rs], %[sab] sc1\n\t" \
  "s_waitcnt vmcnt(0)\n\t" \
  "s_branch .Lto%=\n\t" \
  ".Ltr%=:\n\t" \
  "buffer_load_dword v166, v242, %[rs], 0 offen sc1\n\t" \
  "s_waitcnt vmcnt(0)\n\t" \
  "s_branch .Ltc%=\n\t" \
  ".Llast%=:\n\t" \
  "v_mov_b32 v173, v242\n\t" \
  "s_branch .Lgol%=\n\t" \
  ".Lfw%=:\n\t" \
  "s_add_u32 s92, s92, 1\n\t" \
  "s_sleep 2\n\t" \
  "s_and_b32 s93, s92, 63\n\t" \
  "s_cbranch_scc1 .Lfr%=\n\t" \
  "buffer_load_dword v164, off, %[rs], %[sab] sc1\n\t" \
  "s_waitcnt vmcnt(0)\n\t" \
  "v_readfirstlane_b32 s93, v164\n\t" \
  "s_nop 3\n\t" \
  "s_cmp_eq_u32 s93, 1\n\t" \
  "s_cbranch_scc1 .Lfo%=\n\t" \
  "s_cmp_lt_u32 s92, 0x80000\n\t" \
  "s_cbranch_scc1 .Lfr%=\n\t" \
  "v_mov_b32 v164, 1\n\t" \
  "buffer_store_dword v164, off, %[rs], %[sab] sc1\n\t" \
  "s_waitcnt vmcnt(0)\n\t" \
  "s_branch .Lfo%=\n\t" \
  ".Lfr%=:\n\t" \
  "buffer_load_dwordx2 v[166:167], v173, %[rs], 0 offen sc1\n\t" \
  "s_branch .Lfc%=\n\t" \
  ".Lb1%=:\n\t" \
  "s_waitcnt vmcnt(0) lgkmcnt(0)\n\t" \
  "s_barrier\n\t" \
  "s_branch .Lb2%=\n\t" \
  ".Lbl%=:\n\t" \
  "s_bitset1_b64 %[ps], %[step]\n\t" \
  "s_cmp_lt_u32 s92, 3\n\t" \
  "s_cbranch_scc1 .Lbm%=\n\t" \
  "s_sleep 12\n\t" \
  ".Lbm%=:\n\t" \
  "s_add_u32 s92, s92, 1\n\t" \
  "s_sleep 2\n\t" \
  "s_and_b32 s93, s92, 63\n\t" \
  "s_cbranch_scc1 .Lba%=\n\t" \
  "buffer_load_dword v164, off, %[rs], %[sab] sc1\n\t" \
  "s_waitcnt vmcnt(0)\n\t" \
  "v_readfirstlane_b32 s93, v164\n\t" \
  "s_nop 3\n\t" \
  "s_cmp_eq_u32 s93, 1\n\t" \
  "s_cbranch_scc1 .Lgo%=\n\t" \
  "s_cmp_lt_u32 s92, 0x80000\n\t" \
  "s_cbranch_scc1 .Lba%=\n\t" \
  "v_mov_b32 v164, 1\n\t" \
  "buffer_store_dword v164, off, %[rs], %[sab] sc1\n\t" \
  "s_waitcnt vmcnt(0)\n\t" \
  "s_branch .Lgo%=\n\t" \
  ".Lba%=:\n\t" \
  "buffer_load_dwordx2 v[166:167], v173, %[rs], 0 offen sc1\n\t" \
  "s_branch .Lcal%=\n\t" \
  ".Lslow%=:\n\t" \
  "s_mov_b64 s[94:95], 0\n\t" \
  "v_cmp_eq_u32 vcc, %[sent], v128\n\t" \
  "s_or_b64 s[94:95], s[94:95], vcc\n\t" \
  "v_cmp_eq_u32 vcc, %[sent], v129\n\t" \
  "s_or_b64 s[94:95], s[94:95], vcc\n\t" \
  "v_cmp_eq_u32 vcc, %[sent], v130\n\t" \
  "s_or_b64 s[94:95], s[94:95], vcc\n\t" \
  "v_cmp_eq_u32 vcc, %[sent], v131\n\t" \
  "s_or_b64 s[94:95], s[94:95], vcc\n\t" \
  "v_cmp_eq_u32 vcc, %[sent], v132\n\t" \
  "s_or_b64 s[94:95], s[94:95], vcc\n\t" \
  "v_cmp_eq_u32 vcc, %[sent], v133\n\t" \
  "s_or_b64 s[94:95], s[94:95], vcc\n\t" \
  "v_cmp_eq_u32 vcc, %[sent], v134\n\t" \
  "s_or_b64 s[94:95], s[94:95], vcc\n\t" \
  "v_cmp_eq_u32 vcc, %[sent], v135\n\t" \
  "s_or_b64 s[94:95], s[94:95], vcc\n\t" \
  "v_cmp_eq_u32 vcc, %[sent], v136\n\t" \
  "s_or_b64 s[94:95], s[94:95], vcc\n\t" \
  "v_cmp_eq_u32 vcc, %[sent], v137\n\t" \
  "s_or_b64 s[94:95], s[94:95], vcc\n\t" \
  "v_cmp_eq_u32 vcc, %[sent], v138\n\t" \
  "s_or_b64 s[94:95], s[94:95], vcc\n\t" \
  "v_cmp_eq_u32 vcc, %[sent], v139\n\t" \
  "s_or_b64 s[94:95], s[94:95], vcc\n\t" \
  "v_cmp_eq_u32 vcc, %[sent], v140\n\t" \
  "s_or_b64 s[94:95], s[94:95], vcc\n\t" \
  "v_cmp_eq_u32 vcc, %[sent], v141\n\t" \
  "s_or_b64 s[94:95], s[94:95], vcc\n\t" \
  "v_cmp_eq_u32 vcc, %[sent], v142\n\t" \
  "s_or_b64 s[94:95], s[94:95], vcc\n\t" \
  "v_cmp_eq_u32 vcc, %[sent], v143\n\t" \
  "s_or_b64 s[94:95], s[94:95], vcc\n\t" \
  "s_cbranch_scc0 .Lok%=\n\t" \
  "s_bitset1_b64 %[fm], %[step]\n\t" \
  "s_add_u32 s92, s92, 1\n\t" \
  "s_sleep 1\n\t" \
  "s_and_b32 s93, s92, 63\n\t" \
  "s_cbranch_scc1 .Lre%=\n\t" \
  "buffer_load_dword v164, off, %[rs], %[sab] sc1\n\t" \
  "s_waitcnt vmcnt(0)\n\t" \
  "v_readfirstlane_b32 s93, v164\n\t" \
  "s_nop 3\n\t" \
  "s_cmp_eq_u32 s93, 1\n\t" \
  "s_cbranch_scc1 .Lok%=\n\t" \
  "s_cmp_lt_u32 s92, 0x80000\n\t" \
  "s_cbranch_scc1 .Lre%=\n\t" \
  "v_mov_b32 v164, 1\n\t" \
  "buffer_store_dword v164, off, %[rs], %[sab] sc1\n\t" \
  "s_waitcnt vmcnt(0)\n\t" \
  "s_branch .Lok%=\n\t" \
  ".Lre%=:\n\t" \
  "buffer_load_dwordx4 v[128:131], v234, %[rs], %[so] offen sc1\n\t" \
  "s_nop 15\n\t" \
  "buffer_load_dwordx4 v[132:135], v235, %[rs], %[so] offen sc1\n\t" \
  "s_nop 15\n\t" \
  "buffer_load_dwordx4 v[136:139], v236, %[rs], %[so] offen sc1\n\t" \
  "s_nop 15\n\t" \
  "buffer_load_dwordx4 v[140:143], v237, %[rs], %[so] offen sc1\n\t" \
  "v_mov_b64 v[144:145], 0\n\t" \
  "v_mov_b64 v[146:147], 0\n\t" \
  "v_mov_b64 v[148:149], 0\n\t" \
  "v_mov_b64 v[150:151], 0\n\t" \
  "v_mov_b64 v[152:153], 0\n\t" \
  "v_mov_b64 v[154:155], 0\n\t" \
  "v_mov_b64 v[156:157], 0\n\t" \
  "v_mov_b64 v[158:159], 0\n\t" \
  "s_waitcnt vmcnt(0)\n\t" \
  "s_branch .Lw%=\n\t" \
  ".Lend%=:\n\t"

__device__ __forceinline__ bf16x8 get4(const u32x32& W, int i) {
  return as_bf16x8((u32x4){W[4 * i], W[4 * i + 1], W[4 * i + 2], W[4 * i + 3]});
}
__device__ __forceinline__ int look_prog(__amdgpu_buffer_rsrc_t rs, int off) {
  asm volatile("" ::: "memory");
  const int v = (int)ld_flag(rs, off);
  return min(__builtin_amdgcn_readlane(v, 0), __builtin_amdgcn_readlane(v, 1));
}
__device__ __forceinline__ int wait_prog(__amdgpu_buffer_rsrc_t rs, int off, int need, gu32* abortw) {
  int v;
  for (unsigned spins = 0;;) {
    v = look_prog(rs, off);
    if (v >= need) break;
    __builtin_amdgcn_s_sleep(4);
    if (spin_fail(spins, abortw)) break;
  }
  asm volatile("" ::: "memory");
  return v;
}
#define CLB_S "s90", "s91", "s92", "s93", "s94", "s95", "vcc", "scc", "memory"
#define W_IN "{v[0:31]}"(W[0]), "{v[32:63]}"(W[1]), "{v[64:95]}"(W[2]), "{v[96:127]}"(W[3]), "{v[128:159]}"(W[4]), "{v[160:191]}"(W[5])

#define W_DELAY 0
#define LEAD0 408
#define LEAD_N0 5
#define LEAD_SH 2
#define LEAD_X0 8
#define LEAD_U0 24
#define LEAD_D0 0
#define OX4 8
#define T0_BACK 1000
#define QBJ 3
constexpr int L_MEM = 0, L_KT = 16384, L_QX = 24576, L_KX = 26624, L_VX = 16384, L_MISC = 30720, L_WL = 32768;

#define QK_STEP(TXT)                                                                                                        \
  asm volatile(TXT                                                                                                          \
      : [tout] "=&s"(tout), [spins] "=&s"(spins), [tb1] "=&s"(tb1), [tgt] "=&s"(tgt), "+{v248}"(aL16), "+{v249}"(aMW), "+{v250}"(aWL),                            \
        "+{v251}"(aIDX), "+{v252}"(aQW), "+{v[232:235]}"(QF)                                                                 \
      : W_IN, [rs] "s"(rs4), [som] "s"(o_mem(pm, s) + WV * 1024), [som2] "s"(o_mem(pm, s) + WV * 1024 + 16384), [sent] "s"(DSENT), [sab] "s"(SY_ABORT), \
        [lead] "s"(lead), [step] "s"(s), [sbc] "s"(SY_BA + side(pm, 1) + h * 128), [nh] "s"(0xFF00FF00FF00FF00ull), [up] "s"(0xFFFFFFFF00000000ull), [negc] "s"(-cexp),      \
        [c] "s"(cexp), [so] "s"(o_o(pm, s, h) + WV * 512), [sk] "s"(o_v(pm, s, h) + 2048 + (WV & 3) * 512),                    \
        [sv] "s"(o_v(pm, s, h) + WV * 512), [rt] "s"(rt4), [svt] "s"(t_off(p, T_V + kseg(s, h)) + WV * 2048)                  \
      : CLB8(192, 193, 194, 195, 196, 197, 198, 199), CLB8(200, 201, 202, 203, 204, 205, 206, 207),                          \
        CLB8(208, 209, 210, 211, 212, 213, 214, 215), CLB8(216, 217, 218, 219, 220, 221, 222, 223),                          \
        CLB8(224, 225, 226, 227, 228, 229, 230, 231), CLB8(236, 237, 238, 239, 240, 241, 242, 243), "v244", "v245", "v246", "v247", "v253", "s88", "s89", CLB_S)

template <int WV>
__device__ __forceinline__ void qk_wave(Args& p, int h, char* smem) {
  const int tid = threadIdx.x;
  unsigned pm = 0;
  int have = -1;
  u32x32 W[6];
  {
    int lane = tid & 63;
    asm volatile("" : "+v"(lane));
    const unsigned vo = (WV * 4 + (lane >> 4)) * (HD * 2) + 16 * ((lane & 15) ^ swzB((WV * 4 + (lane >> 4)) & 15));
    auto stage = [&](int j) {
#pragma unroll
      for (int mm = 0; mm < 2; ++mm) {
        const char* src = (const char*)((mm ? p.Wk : p.Wq) + (size_t)j * 128 * HD + h * DH);
        const unsigned dst = lds_off(smem) + (~j & 1) * 65536 + mm * 32768 + WV * 1024;
        glds2(src, 32 * HD * 2, vo, dst);
        if (TH_Q < 63) HOLD_V((h & 3) ^ 1, TH_Q);
        glds2(src + 64 * HD * 2, 32 * HD * 2, vo, dst + 16384);
        if (TH_Q < 63) HOLD_V((h & 3) ^ 1, TH_Q);
      }
    };
    bf16x8 keep[4][4];
    int f0 = -1;
    auto place = [&](int g) {
#pragma unroll
      for (int i = 0; i < 4; ++i)
        *reinterpret_cast<bf16x8*>(smem + (g == 0 ? 65536 : g == 1 ? 32768 : g == 2 ? 131072 : 98304) + WV * 4096 + i * 1024 + lane * 16) = keep[g][i];
    };
    stage(0); stage(1);
    Boot bt;
#pragma unroll
    for (int j = 0; j < 8; ++j) {
      if (j < 7) WAIT_V(8); else WAIT_V(0);
      if (WV == 7 && j == QBJ + 1) pm = boot_done(p.ws, bt);
      if ((WV == 4 || WV == 5) && j == 7) {
        asm volatile("" : "+v"(f0));
        have = min(__builtin_amdgcn_readlane(f0, 0), __builtin_amdgcn_readlane(f0, 1));
      }
      LBAR();
#pragma unroll
      for (int k = 0; k < 8; ++k) {
        const int P = (QK_MAP[WV] >> (4 * (2 * j + (k >> 2)))) & 15;
        const bf16x8 fr = ld_bfrag(smem + (~j & 1) * 65536 + (WV >> 2) * 32768 + (k >> 2) * 16384, k & 1, 2 * (WV & 3) + ((k >> 1) & 1), lane);
        const int f = P * 4 + (k & 3);
        if (P < 12) put4(W[f >> 3], f & 7, fr);
        else keep[P - 12][k & 3] = fr;
      }
      if (j == (WV >> 1)) place(2);
      LBAR();
      if (WV == 7 && j == QBJ) boot_ask(p.ws, bt);
      if (j + 2 < 8) stage(j + 2);
      if (j == 6) {
        if (WV == 4 || WV == 5)
          asm volatile("buffer_load_dword %0, %1, %2, 0 offen sc1" : "=&v"(f0) : "v"(fkq(h, (lane & 1) * 2)), "s"(raw_rsrc(p.ws + WS_SY, SY_TOTAL)) : "memory");
        place(0); place(3);
      }
    }
    place(1);
    if (WV == 7 && lane < 8) lds_at(smem + L_MISC)[lane] = lane == 7 ? pm : (lane == 1 ? now_q() - T0_BACK : 0);
    if (WV == 6) lds_at(smem + 28672)[lane] = ~0u;
    LBAR();
    pm = uni(lds_at(smem + L_MISC)[7]);
    p.s0 = pm >> 31;
    if (WV == 6) RDW(h);
    if (WV == 4 || WV == 5) {
      if (have < 0) have = wait_prog(mkrsrc(p.ws + WS_SY, SY_TOTAL), fkq(h, (lane & 1) * 2), 0, (gu32*)(p.ws + WS_SY + SY_ABORT));
      const char* src = p.ws + WS_T + t_off(p, kseg(0, h)) + (WV - 4) * 4096 + lane * 16;
#pragma unroll
      for (int i = 0; i < 4; ++i) glds16_sc1(src + i * 1024, smem + L_KT + (WV - 4) * 4096 + i * 1024);
    }
  }
  const __amdgpu_buffer_rsrc_t rsy = mkrsrc(p.ws + WS_SY, SY_TOTAL);
  const u32x4 rs4 = raw_rsrc(p.ws + WS_SY, SY_TOTAL);
  const u32x4 rt4 = raw_rsrc(p.ws + WS_T, T_BYTES);
  gu32* abortw = (gu32*)(p.ws + WS_SY + SY_ABORT);
  const float cexp = 0.08838834764831845f * 1.4426950408889634f;
  constexpr int m = WV >> 2, b = WV & 3;
  int lead = LEAD0;
  unsigned tb1 = 0;
  unsigned long long fm = 0, ps = 0; (void)fm; (void)ps;
  for (int s = 0; s < NSEG; ++s) {
    int lane = tid & 63;
    asm volatile("" : "+v"(lane));
    const int r = lane & 15, q = lane >> 4;
    unsigned aL16 = lane * 16, aMW = aL16 + WV * 1024, aWL = aL16 + 98304 + WV * 4096;
    unsigned aIDX = (lane & 2) ? 0xEEEE : 0x4444;
    unsigned aQW = (32 * b + r) * 16 + q * 4 + (m ? L_KX : L_QX);
    u32x4 QF;
    asm volatile("" : "={v[232:235]}"(QF));
    if (WV < 4) {
      QF[0] = L_QX + (q * 8 + (r >> 2)) * 16 + (r & 1) * 8;
      QF[1] = ((2 * (q & 1) + (q >> 1)) * 16 + r) * 4;
      QF[2] = ((r >> 2) & 1) * 65536 + ((r >> 1) & 1) * 64 + (r >> 3) * 32 + (r & 1) * 16 + (q >> 1) * 256 + (q & 1) * OX4;
    } else QF[3] = r * 16 + q * 4;
    unsigned tout, spins, tgt;
    if (WV == 0) QK_STEP(QK_ASM_0); else if (WV == 1) QK_STEP(QK_ASM_1); else if (WV == 2) QK_STEP(QK_ASM_2);
    else if (WV == 3) QK_STEP(QK_ASM_3); else if (WV == 4) QK_STEP(QK_ASM_4); else if (WV == 5) QK_STEP(QK_ASM_5);
    else if (WV == 6) QK_STEP(QK_ASM_6); else QK_STEP(QK_ASM_7);
    tout = __builtin_amdgcn_readfirstlane(tout);
    tb1 = __builtin_amdgcn_readfirstlane(tb1);
    lane = tid & 63;
    asm volatile("" : "+v"(lane));
    if (WV < 4) {
      lds_at(smem + L_MISC + 8)[WV] = s + 1;
      if (WV == 3) { lds_at(smem + L_MISC)[6] = time_of(tout); lds_at(smem + 28672)[2] = s; }
    }
    if (WV == 6) {
      if (s + 1 < NSEG) watch(rsy, s, lane, now_q() + W_DELAY, (const unsigned*)(smem + L_MISC), abortw, side(pm, 1));
    } else if (WV >= 4 && s + 1 < NSEG) {
      unsigned t;
      do {
        __builtin_amdgcn_s_sleep(1);
        t = lds_at(smem + L_MISC + 8)[lane & 3];
      } while (!__all(t == (unsigned)(s + 1)));
      if (WV <= 5) {
        if (s + 2 < NSEG) {
          if (have < s + 1) have = wait_prog(rsy, fkq(h, (lane & 1) * 2), s + 1, abortw);
          const char* src = p.ws + WS_T + t_off(p, kseg(s + 1, h)) + (WV - 4) * 4096 + lane * 16;
#pragma unroll
          for (int i = 0; i < 4; ++i) glds16_sc1(src + i * 1024, smem + L_KT + (WV - 4) * 4096 + i * 1024);
          if (have < s + 2) have = look_prog(rsy, fkq(h, (lane & 1) * 2));
        }
      } else {
        do __builtin_amdgcn_s_sleep(2); while (__builtin_amdgcn_readfirstlane(lds_at(smem + 28672)[2]) != (unsigned)s);
        const int x = (int)(__builtin_amdgcn_readfirstlane(lds_at(smem + L_MISC)[6]) - tb1);
        lead = __builtin_amdgcn_readfirstlane(track(lead, x + (s < LEAD_N0 ? LEAD_X0 : 0), s < LEAD_N0 ? LEAD_U0 : LEAD_UP, s < LEAD_N0 ? LEAD_D0 : (s < 3 * LEAD_N0 ? min(max(LEAD_DN, (lead - x) >> LEAD_SH), 16) : LEAD_DN), 60));
        SMP(2, s, x); SMP(3, s, lead);
      }
    }
  }
}

__device__ __forceinline__ void qk_role(Args& p, int h, char* smem) {
  switch (__builtin_amdgcn_readfirstlane(threadIdx.x >> 6)) {
    case 0: qk_wave<0>(p, h, smem); break;
    case 1: qk_wave<1>(p, h, smem); break;
    case 2: qk_wave<2>(p, h, smem); break;
    case 3: qk_wave<3>(p, h, smem); break;
    case 4: qk_wave<4>(p, h, smem); break;
    case 5: qk_wave<5>(p, h, smem); break;
    case 6: qk_wave<6>(p, h, smem); break;
    default: qk_wave<7>(p, h, smem); break;
  }
}

#define K_DUE 160
#define T_SL 2
#define O_DUE 400
#define LEAD_V 0

constexpr int V_VM = 16384, V_T = 32768, V_KI = 131072;
__device__ __forceinline__ void vu_role(Args& p, int h, char* smem) {
  const int tid = threadIdx.x, lane = tid & 63;
  const int w = __builtin_amdgcn_readfirstlane(tid >> 6);
  u32x32 W[4];
  {
    const unsigned vo = (tid >> 4) * (HD * 2) + 16 * ((lane & 15) ^ swzB((tid >> 4) & 15));
    auto stage = [&](int j) {
      const char* src = (const char*)(p.Wv + (size_t)j * 128 * HD + h * DH);
      const unsigned dst = lds_off(smem) + L_WL + (j & 3) * 32768 + w * 1024;
      glds2(src, 32 * HD * 2, vo, dst);
      if (TH_V < 63) HOLD_V((h & 3) ^ 1, TH_V);
      glds2(src + 64 * HD * 2, 32 * HD * 2, vo, dst + 16384);
      if (TH_V < 63) HOLD_V((h & 3) ^ 1, TH_V);
    };
    start_sy(p.ws, p.mem0, h, smem);
    Boot bt;
#pragma unroll
    for (int u = 0; u < 2; ++u) {
#pragma unroll
      for (int j = 0; j < 4; ++j) stage(4 * u + j);
      if (u && w == 7) boot_ask(p.ws, bt);
      WAIT_V(0);
      if (u && w == 7) {
        const unsigned m = boot_done(p.ws, bt);
        if (lane < 8) lds_at(smem + L_MISC)[lane] = lane == 7 ? m : (lane == 1 ? now_q() : 0);
      }
      LBAR();
#pragma unroll
      for (int k = 0; k < 16; ++k) put4(W[2 * u + (k >> 3)], k & 7, ld_bfrag(smem + L_WL + ((k >> 1) ^ w) * 16384, k & 1, w, lane));
      LBAR();
    }
    RDY(32 + h);
  }
  const __amdgpu_buffer_rsrc_t rsy = mkrsrc(p.ws + WS_SY, SY_TOTAL);
  const u32x4 rs4 = raw_rsrc(p.ws + WS_SY, SY_TOTAL);
  gu32* abortw = (gu32*)(p.ws + WS_SY + SY_ABORT);
  const unsigned pm = uni(lds_at(smem + L_MISC)[7]);
  p.s0 = pm >> 31;
  unsigned long long fm = 0; (void)fm;
  int have = -1;
  auto fetch = [&](int sg) {
    if (have < sg) have = wait_prog(rsy, fkq(h, w - 4), sg, abortw);
    const char* src = p.ws + WS_T + t_off(p, (w - 4) * T_Q + kseg(sg, h)) + lane * 16;
#pragma unroll
    for (int i = 0; i < 8; ++i) glds16_sc1(src + i * 1024, smem + V_T + (sg & 3) * 24576 + (w - 4) * 8192 + i * 1024);
  };
  const int fo = SY_FO + ((w & 3) * NH + h) * 128;
  typedef __attribute__((ext_vector_type(8))) float f32x8;
  f32x8 bias;
  typedef __attribute__((ext_vector_type(8))) unsigned u32x8;
  u32x8 ca;
#pragma unroll
  for (int i = 0; i < 8; ++i) ca[i] = lane * 16 + (i ^ w) * 1024;
  u32x4 ta;
  {
    const int r = lane & 15, q = lane >> 4;
#pragma unroll
    for (int j = 0; j < 8; ++j) bias[j] = (j >> 2) * 16 + 4 * q + (j & 3) <= 16 * (w & 1) + r ? INFINITY : -INFINITY;
    ta[0] = V_KI + w * 2048 + lane * 16;
    ta[1] = V_KI + w * 2048 + (q * 8 + (r >> 2)) * 16 + (r & 1) * 8;
    ta[2] = V_VM + (w & 2) * 512 + ((2 * (q & 1) + (q >> 1)) * 16 + r) * 4;
    ta[3] = 0;
  }
#pragma unroll 1
  for (int s = 0; s < NSEG; ++s) {
    unsigned spins, tgt;
    asm volatile(VV_ASM
        : [spins] "=&s"(spins), [tgt] "=&s"(tgt)
        : W4_IN, "{v200}"(lane * 16), "{v[232:239]}"(ca), "{v202}"((lane & 2) ? 0xEEEE : 0x4444), "{v203}"(lane * 4),
          "{v204}"(tid * 4 + V_VM + (s & 1) * 2048), [rs] "s"(rs4), [som] "s"(o_mem(pm, s) + w * 1024), [som2] "s"(o_mem(pm, s) + w * 1024 + 16384), [step] "s"(s),
          [sent] "s"(DSENT), [sab] "s"(SY_ABORT), [sv] "s"(o_v(pm, s, h) + w * 256)
        : CLB32(128), CLB32(160), CLB8(192, 193, 194, 195, 196, 197, 198, 199), CLB_S);
    if (w < 4 || s == NSEG - 1) {
      const int sg = s - (w < 4);
      if (w >= 4) pace_until(now_q() + K_DUE);
      if (sg >= 0) {
        asm volatile(TK_ASM
            :
            : "{v200}"(lane * 16), "{v[208:215]}"(bias), "{v[216:219]}"(ta),
              [rs] "s"(rs4), [rt] "s"(raw_rsrc(p.ws + WS_T, T_BYTES)), [sent] "s"(DSENT), [sab] "s"(SY_ABORT), [sk] "s"(o_v(pm, sg, h) + 2048),
              [tl] "s"(V_T + (sg & 3) * 24576), [vp] "s"((sg & 1) * 2048), [tq] "s"((w & 1) * 4096), [tv] "s"((w & 2) * 2048),
              [so] "s"(t_off_o(p, T_O + (sg * 2 + (w & 1)) * 16 + (h >> 1)) + (h & 1) * 4096 + (w & 2) * 1024),
              [up] "s"(0xFFFFFFFF00000000ull), [c] "s"(1.4426950408889634f), [negc] "s"(-1.4426950408889634f),
              [sc] "s"((unsigned long long)__float_as_uint(0.08838834764831845f)), [od] "s"(s < NSEG - 1 ? O_DUE : 0)
            : CLB32(128), CLB32(160), CLB8(192, 193, 194, 195, 196, 197, 198, 199), CLB8(224, 225, 226, 227, 228, 229, 230, 231),
              "v205", "v206", "v207", "s88", "s89", CLB_S);
        if (sg >= NSEG - 3) {
          static_assert(TLAG == 2, "");
          WAIT_V(0);
          if (lane == 0) __builtin_amdgcn_raw_buffer_store_b32(1u, rsy, fo + (sg - NSEG + 3) * 4 * NH * 128, 0, 16);
        }
      }
    } else if (w == 7) {
      SMP(6, s, (int)(now_q() - tgt));
      watch(rsy, s, lane, now_q() + LEAD_V + W_DELAY, (const unsigned*)(smem + L_MISC), abortw, side(pm, 1));
    } else if (w >= 4) {
      __builtin_amdgcn_s_sleep(T_SL);
      const int s1 = w == 5 ? (s == NSEG - 2 ? s + 1 : s) : s + 1;
#pragma unroll 1
      for (int sg = (w == 5 || s == 0) ? s : s + 1; sg <= s1; ++sg) fetch(sg);
      if (have < s1 + 1 && s1 + 1 < NSEG) have = look_prog(rsy, fkq(h, w - 4));
      if (w == 5 && s + NPRE < NSEG) {
#pragma unroll
        for (int i = 0; i < 4; ++i) st_sc1(rsy, o_v(pm, s + NPRE, h) + i * 1024 + lane * 16, (u32x4){DSENT, DSENT, DSENT, DSENT});
      }
    }
  }
  if (h == 3 && (w & 2) == 0) { LST(351 + (w & 1) + (w >> 2) * 2); }
}

#define RL_UP 6
#define RL_DN 4
#define RED_MIN 40
#define BA_LOOK 360
constexpr int R_SI = 32768, R_PART = 98304;

__device__ __forceinline__ void red_role(Args& p, int j, char* smem) {
  const int tid = threadIdx.x;
  int lane = tid & 63;
  const int w = __builtin_amdgcn_readfirstlane(tid >> 6);
  const int c = (j & 2) * 8 + (j >> 2), rh = j & 1;
  const int hg = (w + (j >> 2)) & 7, rot = (j >> 2) + (j >> 5);
  const __amdgpu_buffer_rsrc_t rsy = mkrsrc(p.ws + WS_SY, SY_TOTAL);
  const u32x4 rs4 = raw_rsrc(p.ws + WS_SY, SY_TOTAL);
  const __amdgpu_buffer_rsrc_t rt = mkrsrc(p.ws + WS_T, T_BYTES);
  gu32* abortw = (gu32*)(p.ws + WS_SY + SY_ABORT);
  u32x32 W[4];
  u32x2 tag = {0u, 0u};
  {
    const u16* Wo = p.Wo + (size_t)(4 * hg * DH) * DM + 32 * c;
    const int ro = (lane >> 2) * 64 + (lane & 3) * 8;
    Boot bt;
    load_blocks<16>(smem + 32768 + w * 16384,
        [&](int i, char* slot) {
          if (i == 12 && w == 0) boot_ask(p.ws, bt);
          if ((i < 2 || i == 4 || i == 8) && (c & 14) != 6 && lane < 10) {
            const int x = i < 2 ? 3 - i : i >> 3, n = min(((c & 15) - ((c & 8) >> 2)) * 10 + lane, 127);
            touch_line((const char*)p.Wo + (size_t)(w * 512 + (x >> 1) * 256 + (x & 1) * 64 + (n & 64) + n) * (DM * 2) + (c & 16) * 64 + 384, smem + w * 256);
          }
          const int x = ((i >> 2) + rot) & 3;
          blk_dma(Wo + (size_t)((x >> 1) * 256 + (x & 1) * 64 + ((i >> 1) & 1) * 128 + (i & 1) * 32) * DM, DM, slot, lane);
          if (TH_R < 63) HOLD_V((c & 14) ^ 6, TH_R);
        },
        [&](int i, char* slot) {
#pragma unroll
          for (int t = 0; t < 2; ++t) {
            s16x4 lo = __builtin_amdgcn_ds_read_tr16_b64_v4i16((lds_s16x4_ptr)(slot + ro + t * 32));
            s16x4 hi = __builtin_amdgcn_ds_read_tr16_b64_v4i16((lds_s16x4_ptr)(slot + ro + t * 32 + 1024));
            put4(W[i >> 2], (i & 2) * 2 + 2 * t + (i & 1), (bf16x8){lo[0], lo[1], lo[2], lo[3], hi[0], hi[1], hi[2], hi[3]});
          }
        });
    if (w == 0) { lds_at(smem + 4128)[0] = boot_done(p.ws, bt); tag = next_tag(bt.e); }
    LBAR();
  }
  const unsigned pm = uni(lds_at(smem + 4128)[0]);
  p.s0 = pm >> 31;
  const int hs = side(pm, 1);
  if ((c & 14) != 6 && GPF == 2) {
#pragma unroll
    for (int i = 0; i < 2; ++i)
      touch_line((const char*)p.wd + (size_t)(j - (j >= 32 ? 8 : 0)) * 131072 + (i * 8 + w) * 8192 + lane * 128, smem + w * 256);
  }
  {
    const u32x4 ds = {DSENT, DSENT, DSENT, DSENT};
#pragma unroll
    for (int i = 0; i < 2; ++i) {
      const int s = (w * 2 + i) * 4 + (lane >> 4);
      if (s >= 2) st_sc1(rsy, o_mem(pm, s) + (c >> 4) * 16384 + (c & 15) * 512 + rh * 128 + ((lane >> 3) & 1) * 256 + (lane & 7) * 16, ds);
    }
  }
  if (tid < 4) { reinterpret_cast<unsigned*>(smem + 4096)[tid] = tid == 3 ? now_q() : 0; reinterpret_cast<unsigned*>(smem + 4112)[tid] = DSENT; }
  if (tid < 256) reinterpret_cast<int*>(smem + 5120)[tid] = ((tid >> 2) - TLAG - 6) >> 2;
  LBAR();
  const unsigned base = __builtin_amdgcn_readfirstlane(lds_at(smem + 4096)[3]);
  RDY(64 + j);
  unsigned long long fm = 0, ps = 0;
  const unsigned aL = lane * 16;
  const int e = ((56 + w) * SEG + rh * 16 + (lane >> 2)) * DM + c * 32 + (lane & 3) * 8;
  u32x4 xv = *reinterpret_cast<const u32x4*>(p.x + e), av = {DSENT, DSENT, DSENT, DSENT};
  asm volatile("" : "+v"(xv));
  {
    const int r = lane & 15, q = lane >> 4;
    const int sm0 = o_mem(pm, 0) + (c >> 4) * 16384 + (c & 15) * 512 + rh * 128;
    u32x4 VO, VT;
    unsigned fo = SY_FO + ((rh + (lane & 4) / 2) * NH + 4 * hg + (lane & 3)) * 128;
    unsigned ao = w == 1 ? (c * S_LEN + rh * 16 - (TLAG + 3) * SEG) * 64 + lane * 16
                         : (w >= 4 ? SY_O + NPRE * 131072 + side(pm, w - 2) + (j >> 3) * 16384 + (j & 7) * 1024 + lane * 16
                                   : SY_FS + (2 * c + rh) * 128 + (lane < 8 ? lane * 16 : 0x70000000));
    unsigned ar = w == 1 ? R_SI - (TLAG + 2) * 1024 + lane * 16 : (w == 2 ? 5120 : 4112);
    const unsigned long long am = w == 1 ? ((1ull << 56) - 1) << (TLAG + 3) : (w == 2 ? 0x11111111111111ull << (TLAG + 7) : (w >= 4 ? (0xFFFFull << (16 * (w - 4))) >> NPRE : 0));
    unsigned pw = R_PART + w * 2048 + lane * 16, pr = R_PART + 16384 + tid * 4;
    unsigned sw = R_SI - (TLAG + 1) * 1024 + (lane >> 2) * 64 + w * 8 + (lane & 3) * 2, stm = 8192 + w * 512;
#pragma unroll
    for (int l = 0; l < 4; ++l) {
      const int x = (l + rot) & 3;
      VO[l] = o_o(0, 0, 4 * hg) + rh * 65536 + x * 1024 + aL;
      VT[l] = (T_O + rh * 16 + 2 * hg + (x >> 1)) * 16384 + (x & 1) * 2048 + aL;
    }
    unsigned ba = lane < 4 ? SY_BA + hs + (4 * hg + lane) * 128 : 0x7F000000u;
    int rl = RL0;
    unsigned step, E, so, st, st4, sh;
    asm volatile(RED_ASM
        : [step] "=&s"(step), [E] "=&s"(E), [so] "=&s"(so), [st] "=&s"(st), [st4] "=&s"(st4), [sh] "=&s"(sh),
          [rl] "+s"(rl), [fm] "+s"(fm), [ps] "+s"(ps),
          "+{v[224:227]}"(av), "+{v228}"(ao), "+{v229}"(ar), "+{v230}"(pw), "+{v231}"(pr), "+{v232}"(sw), "+{v233}"(stm), "+{v242}"(fo), "+{v173}"(ba)
        : W4_IN, "{v168}"(aL), "{v169}"(w * 512 + lane * 8), "{v170}"(lane * 8), "{v174}"((r & 1) ? 0x03020706u : 0x05040100u),
          "{v171}"(sm0 + 32768 + (q >> 1) * 256 + (q & 1) * 64 + ((r >> 1) & 1) * 32 + (r & 1) * 16 + (r >> 2) * 4),
          "{v172}"((lane & 2) ? 0xEEEE : 0x4444),
          "{v175}"(w == 3 && lane < 32 ? SY_BR + hs + j * 128 + lane * 4 : 0x7F000000u), "{v[234:237]}"(VO), "{v[238:241]}"(VT), "{v243}"(e * 2),
          [rg] "s"(raw_rsrc(p.ws + WS_G, S_LEN * DM * 2)), [fg] "s"(SY_FG + ((14 + (w >> 2)) * 8 + (c >> 2)) * 128),
          [rs] "s"(rs4), [ra] "s"(w == 1 ? raw_rsrc(p.ws + WS_S, S_LEN * DM * 2) : rs4), [rt] "s"(raw_rsrc(p.ws + WS_T, T_BYTES)),
          [ds] "n"(DS_RED), [mn] "n"(RED_MIN), [lk] "n"(DS_MEM + BA_LOOK), [up] "n"(RL_UP), [dn] "n"(-RL_DN), [sent] "s"(DSENT), [sab] "s"(SY_ABORT),
          [base] "s"(base), [pm] "s"(pm), [s0] "s"((p.s0 ^ TF_O) << 13), [am] "s"(am), [ainc] "s"(w == 1 ? SEG * 64 : (w >= 4 ? 131072 : 0)),
          [rinc] "s"(w == 1 ? 1024 : (w == 2 ? 16 : 0))
        : CLB32(128), CLB8(160, 161, 162, 163, 164, 165, 166, 167), CLB8(176, 177, 178, 179, 180, 181, 182, 183),
          CLB8(184, 185, 186, 187, 188, 189, 190, 191), CLB8(192, 193, 194, 195, 196, 197, 198, 199), CLB8(200, 201, 202, 203, 204, 205, 206, 207),
          CLB8(208, 209, 210, 211, 212, 213, 214, 215), CLB8(216, 217, 218, 219, 220, 221, 222, 223), CLB_E, "s96", CLB_S);
  }
  if (j == 0 && w == 5) { LST(345); }
  LBAR();
  if (j == 0 && w == 5) { LST(346); }
  u32x4 sv;
  if (w < 6 - TLAG) sv = *reinterpret_cast<const u32x4*>(smem + R_SI + (56 + w) * 1024 + lane * 16);
  else {
    const f32x4* part = reinterpret_cast<const f32x4*>(smem + (w == 6 - TLAG ? R_PART : (w - 6 + TLAG) * 16384)) +
                        (lane & 2) * 32 + (lane & 1) * 32 + (lane >> 2);
    f32x4 x[8][2];
#pragma unroll
    for (int ww = 0; ww < 8; ++ww) { x[ww][0] = part[ww * 128]; x[ww][1] = part[ww * 128 + 16]; }
#pragma unroll
    for (int u = 0; u < 2; ++u) {
      const f32x4 a = ((x[0][u] + x[1][u]) + (x[2][u] + x[3][u])) + ((x[4][u] + x[5][u]) + (x[6][u] + x[7][u]));
      sv[2 * u] = cvtpk(a[0], a[1]); sv[2 * u + 1] = cvtpk(a[2], a[3]);
    }
  }
  st_out(p, e, combine(sv, av, xv));
  if (j == 0 && tid == 0) st2_sc1(rsy, SY_EP, tag);
  if (tid == 128) { LST(400 + j); }
}

__global__ __launch_bounds__(512, 2) void main_kernel(Args p) {
  char* smem = g_smem;
  p.ws = ws_align(p.ws);
  const int bx = blockIdx.x & 7, bk = blockIdx.x >> 3;
  const bool isg = (bx ^ XPROBE) & 4;
  const int b = (isg ? NCHAIN : 0) + bk * 4 + (bx & 3);
  if (b == 64 && threadIdx.x == 0) { LST(500); }
  if (threadIdx.x < 64) {
    unsigned long long pc; unsigned len, keep;
    asm volatile("s_getpc_b64 %0\n.Lpc%=:\n\ts_mov_b32 %1, .Lcode_end-.Lpc%=" : "=s"(pc), "=s"(len));
    asm volatile("s_mov_b32 %0, m0\n\ts_mov_b32 m0, %3\n\ts_nop 0\n\tbuffer_load_dword %1, %2, 0 offen lds\n\ts_mov_b32 m0, %0"
                 : "=&s"(keep) : "v"((bk * 40 + threadIdx.x) * 128), "s"(raw_rsrc((const void*)pc, len)), "s"((b >> 6) == 1 ? 2048 : 163328) : "memory");
  }
  if (b >= NCHAIN) gemm_role(p, b - NCHAIN, smem);
  else if (b < 32) qk_role(p, (b & 3) * 8 + bk, smem);
  else if (b < 64) vu_role(p, (b & 3) * 8 + bk - 8, smem);
  else red_role(p, b - 64, smem);
}

extern "C" void kernel_launch(void* const* d_in, const int* in_sizes, int n_in,
                              void* d_out, int out_size, void* d_ws, size_t ws_size,
                              hipStream_t stream) {
  Args a;
  a.x = (const u16*)d_in[0]; a.mem0 = (const u16*)d_in[1];
  a.Wq = (const u16*)d_in[2]; a.Wk = (const u16*)d_in[3]; a.Wv = (const u16*)d_in[4]; a.Wo = (const u16*)d_in[5];
  a.wg = (const u16*)d_in[8]; a.wu = (const u16*)d_in[9]; a.wd = (const u16*)d_in[10];
  a.ws = (char*)d_ws; a.out = (u16*)d_out; a.s0 = 0; a.pad1 = 0;
  main_kernel<<<256, 512, 0, stream>>>(a);
}
```

```cpp
#include <hip/hip_runtime.h>
#include <hip/hip_bf16.h>
#include <stdint.h>
#include <type_traits>

constexpr int S_LEN = 2048, DM = 1024, NH = 32, DH = 128, NMEM = 8, SEG = 32;
constexpr int NSEG = S_LEN / SEG;
constexpr int HD = NH * DH;
constexpr int FF = 3584;

typedef unsigned short u16;
typedef __attribute__((ext_vector_type(8))) short bf16x8;
typedef __attribute__((ext_vector_type(4))) short s16x4;
typedef __attribute__((ext_vector_type(4))) float f32x4;
typedef __attribute__((ext_vector_type(4))) unsigned int u32x4;
typedef __attribute__((ext_vector_type(2))) unsigned int u32x2;
typedef __attribute__((address_space(3))) s16x4* lds_s16x4_ptr;
typedef __attribute__((address_space(3))) void* lds_void_ptr;
typedef __attribute__((address_space(1))) const void* glb_cvoid_ptr;
typedef __attribute__((address_space(1))) unsigned gu32;
typedef __bf16 bf16x2_t __attribute__((ext_vector_type(2)));
typedef float f32x2_t __attribute__((ext_vector_type(2)));

__device__ __forceinline__ float bf2f(u16 v) { return __uint_as_float(((unsigned)v) << 16); }
__device__ __forceinline__ unsigned cvtpk(float lo, float hi) {
  bf16x2_t r = __builtin_convertvector((f32x2_t){lo, hi}, bf16x2_t);
  return __builtin_bit_cast(unsigned, r);
}
__device__ __forceinline__ float lo_f(unsigned v) { return __uint_as_float(v << 16); }
__device__ __forceinline__ float hi_f(unsigned v) { return __uint_as_float(v & 0xffff0000u); }
__device__ __forceinline__ float rbf(float f) { return lo_f(cvtpk(f, f)); }
__device__ __forceinline__ bf16x8 as_bf16x8(u32x4 v) { return __builtin_bit_cast(bf16x8, v); }
#define MFMA(a, b, c) __builtin_amdgcn_mfma_f32_16x16x32_bf16((a), (b), (c), 0, 0, 0)
typedef __attribute__((ext_vector_type(16))) short bf16x16;
typedef __attribute__((ext_vector_type(8))) __bf16 bfv8;
typedef __attribute__((ext_vector_type(16))) __bf16 bfv16;
#define SMFMA(a, b, c, idx) __builtin_amdgcn_smfmac_f32_16x16x64_bf16(__builtin_bit_cast(bfv8, (a)), __builtin_bit_cast(bfv16, (b)), (c), (idx), 0, 0)
__device__ __forceinline__ bf16x16 cat16(bf16x8 lo, bf16x8 hi) {
  return __builtin_shufflevector(lo, hi, 0, 1, 2, 3, 4, 5, 6, 7, 8, 9, 10, 11, 12, 13, 14, 15);
}

#define KSTR 65
#define XLD 1024
constexpr int PAGE = 2 << 20, NPAGE = 14;
constexpr int T_K = 0, T_Q = 2304, T_V = 4608, T_O = 6912, T_END = T_O + 2048;
constexpr unsigned T_BYTES = T_END * 16384u;
constexpr size_t WS_SY = 0;
constexpr size_t WS_T = (size_t)NPAGE * PAGE;
constexpr size_t WS_XN = WS_T + T_BYTES;
constexpr size_t WS_HF = WS_XN + (size_t)S_LEN * XLD * 2;
constexpr size_t WS_G = WS_HF + (size_t)S_LEN * 16384;
constexpr size_t WS_S = WS_G + (size_t)S_LEN * DM * 2;
constexpr size_t WS_END = WS_S + (size_t)S_LEN * DM * 2;
constexpr unsigned SENT = 0xFFFFFFFFu;
constexpr unsigned DSENT = 0xFFBFFFBFu;
constexpr int SY_MEM = 0;
constexpr int SY_O = 2 * PAGE;
constexpr int SY_V = 6 * PAGE;
constexpr int SY_BR = PAGE, SY_BA = SY_BR + 16384;
constexpr int SY_FKQ = SY_BR + 32768;
constexpr int SY_FG = SY_FKQ + NH * 4 * 128;
constexpr int SY_FO = SY_FG + 16 * 8 * 128;
constexpr int SY_CH = SY_FO + 384 * 128;
constexpr int SY_ABORT = SY_CH + 8 * 128;
constexpr int SY_FS = SY_ABORT + 256;
constexpr int SY_FEND = SY_FS + 64 * 128;
constexpr int SY_EP = SY_FEND, SY_BT = SY_EP + 128;
constexpr int SY_TOTAL = NPAGE * PAGE;
constexpr int PROBE = PAGE - 16384 + 6144;
__device__ __forceinline__ int side(unsigned pm, int page) { return ((pm >> page) & 1u) << 13; }
__device__ __forceinline__ int o_mem(unsigned pm, int s) { return SY_MEM + s * 32768 + side(pm, 0); }
__device__ __forceinline__ int o_o(unsigned pm, int s, int h) {
  return SY_O + s * 131072 + (h >> 3) * 16384 + ((h >> 1) & 3) * 2048 + (h & 1) * 128 + side(pm, 2 + (s >> 4));
}
__device__ __forceinline__ int o_v(unsigned pm, int s, int h) { return SY_V + s * 262144 + (h >> 1) * 16384 + (h & 1) * 4096 + side(pm, 6 + (s >> 3)); }
__device__ __forceinline__ char* ws_align(char* ws) { return ws + ((PAGE - (unsigned)(uintptr_t)ws) & (PAGE - 1)); }
#define NFUSE 12
constexpr int NPRE = 4;
static_assert(WS_END + PAGE <= (size_t)256 << 20 && NH * KSTR <= T_Q && NPAGE % 2 == 0, "");
constexpr int NCHAIN = 128, NGW = 32;
__host__ __device__ constexpr int kseg(int sg, int h) { return h * KSTR + sg; }
__host__ __device__ constexpr int fkq(int h, int kind) { return SY_FKQ + (h * 4 + kind) * 128; }

#define XPROBE 0
struct Args {
  const u16 *x, *mem0, *Wq, *Wk, *Wv, *Wo, *wg, *wu, *wd;
  char* ws;
  u16* out;
  int s0, pad1;
};
__device__ __forceinline__ int h_off(const Args& p, int rblk) { return rblk * PAGE + (((p.s0 ^ rblk ^ 1) & 1) << 13); }
static_assert(WS_HF % (2 * PAGE) == 0, "");
#define TF_KQV 1
#define TF_O 0
__device__ __forceinline__ int t_off(const Args& p, int t) { return t * 16384 + (((p.s0 ^ (t >> 7) ^ (TF_KQV == 2 ? t : TF_KQV)) & 1) << 13); }
__device__ __forceinline__ int t_off_o(const Args& p, int t) { return t * 16384 + (((p.s0 ^ (t >> 7) ^ (TF_O == 2 ? t : TF_O)) & 1) << 13); }

__device__ __forceinline__ float wave_sum(float v) {
#pragma unroll
  for (int i = 0; i < 4; ++i) {
    const int x = (int)__float_as_uint(v);
    v += __uint_as_float(i == 0 ? __builtin_amdgcn_update_dpp(0, x, 0x128, 0xf, 0xf, false) : i == 1 ? __builtin_amdgcn_update_dpp(0, x, 0x124, 0xf, 0xf, false)
                         : i == 2 ? __builtin_amdgcn_update_dpp(0, x, 0x122, 0xf, 0xf, false) : __builtin_amdgcn_update_dpp(0, x, 0x121, 0xf, 0xf, false));
  }
  auto r = __builtin_amdgcn_permlane16_swap(__float_as_uint(v), __float_as_uint(v), false, false);
  v = __uint_as_float(r[0]) + __uint_as_float(r[1]);
  r = __builtin_amdgcn_permlane32_swap(__float_as_uint(v), __float_as_uint(v), false, false);
  return __uint_as_float(r[0]) + __uint_as_float(r[1]);
}

__shared__ __attribute__((aligned(16))) char g_smem[163840];
__device__ __forceinline__ unsigned lds_off(const void* p) {
  return (unsigned)(uintptr_t)(lds_void_ptr)g_smem + (unsigned)((const char*)p - g_smem);
}
__device__ __forceinline__ __amdgpu_buffer_rsrc_t mkrsrc(const void* p, unsigned bytes) {
  return __builtin_amdgcn_make_buffer_rsrc((void*)p, 0, bytes, 0x00020000);
}
__device__ __forceinline__ u32x4 ld_sc1(__amdgpu_buffer_rsrc_t rs, int off) {
  return __builtin_amdgcn_raw_buffer_load_b128(rs, off, 0, 16);
}
__device__ __forceinline__ void st_sc1(__amdgpu_buffer_rsrc_t rs, int off, u32x4 v) {
  __builtin_amdgcn_raw_buffer_store_b128(v, rs, off, 0, 16);
}
__device__ __forceinline__ unsigned ror8(unsigned v) {
  return (unsigned)__builtin_amdgcn_update_dpp(0, (int)v, 0x128, 0xf, 0xf, false);
}
__device__ __forceinline__ float ror8f(float v) { return __uint_as_float(ror8(__float_as_uint(v))); }
__device__ __forceinline__ void swap16(unsigned& x, unsigned& y) {
  auto r = __builtin_amdgcn_permlane16_swap(x, y, false, false);
  x = r[0]; y = r[1];
}
__device__ __forceinline__ void swap32(unsigned& x, unsigned& y) {
  auto r = __builtin_amdgcn_permlane32_swap(x, y, false, false);
  x = r[0]; y = r[1];
}
__device__ __forceinline__ f32x4 fold32(f32x4 x, f32x4 y) {
  f32x4 o;
#pragma unroll
  for (int j = 0; j < 4; ++j) {
    unsigned a = __float_as_uint(x[j]), b = __float_as_uint(y[j]);
    swap32(a, b);
    o[j] = __uint_as_float(a) + __uint_as_float(b);
  }
  return o;
}
__device__ __forceinline__ bf16x8 tr_frag(const char* img_lane) {
  s16x4 lo = __builtin_amdgcn_ds_read_tr16_b64_v4i16((lds_s16x4_ptr)img_lane);
  s16x4 hi = __builtin_amdgcn_ds_read_tr16_b64_v4i16((lds_s16x4_ptr)(img_lane + 64));
  return (bf16x8){lo[0], lo[1], lo[2], lo[3], hi[0], hi[1], hi[2], hi[3]};
}
__device__ __forceinline__ float rowq_max(float v) {
  unsigned a = __float_as_uint(v), b = a;
  swap16(a, b);
  v = __builtin_elementwise_maximum(__uint_as_float(a), __uint_as_float(b));
  a = __float_as_uint(v); b = a;
  swap32(a, b);
  return __builtin_elementwise_maximum(__uint_as_float(a), __uint_as_float(b));
}
__device__ __forceinline__ float rowq_sum(float v) {
  unsigned a = __float_as_uint(v), b = a;
  swap16(a, b);
  v = __uint_as_float(a) + __uint_as_float(b);
  a = __float_as_uint(v); b = a;
  swap32(a, b);
  return __uint_as_float(a) + __uint_as_float(b);
}
__device__ __forceinline__ bool spin_fail(unsigned& spins, gu32* abortw) {
  ++spins;
  if ((spins & 63u) == 0u) {
    const __amdgpu_buffer_rsrc_t ra = mkrsrc((const void*)abortw, 4);
    unsigned z = 0;
    asm volatile("" : "+v"(z));
    if (__builtin_amdgcn_readfirstlane(__builtin_amdgcn_raw_buffer_load_b32(ra, z, 0, 16)) == 1u) return true;
    if (spins > (1u << 19)) {
      __builtin_amdgcn_raw_buffer_store_b32(z + 1u, ra, z, 0, 16);
      return true;
    }
  }
  return false;
}
__device__ __forceinline__ unsigned umax4(u32x4 v) { return max(max(v[0], v[1]), max(v[2], v[3])); }
__device__ __forceinline__ bool has_ds(u32x4 v) { return (v[0] == DSENT) | (v[1] == DSENT) | (v[2] == DSENT) | (v[3] == DSENT); }
__device__ __forceinline__ float fmax2(float a, float b) { return __builtin_elementwise_maximum(a, b); }
__device__ __forceinline__ bf16x8 ldsf(const char* p) { return *reinterpret_cast<const bf16x8*>(p); }
#define SCHED() __builtin_amdgcn_sched_barrier(0)
#define LBAR() do { asm volatile("s_waitcnt lgkmcnt(0)" ::: "memory"); __builtin_amdgcn_s_barrier(); asm volatile("" ::: "memory"); } while (0)
#define WAIT_V(n) asm volatile("s_waitcnt vmcnt(%0)" :: "n"(n) : "memory")

#define HOLD_V(c, n) asm volatile("s_cmp_eq_u32 %0, 0\n\ts_cbranch_scc1 .Lh%=\n\ts_waitcnt vmcnt(%1)\n.Lh%=:" :: "s"((int)(c)), "n"(n) : "memory", "scc")
#define TH_Q 2
#define TH_V 63
#define TH_R 63
#define TH_P 63
__device__ __forceinline__ void wait_flags(__amdgpu_buffer_rsrc_t rs, int off, gu32* abortw) {
  for (unsigned spins = 0;;) {
    asm volatile("" ::: "memory");
    const unsigned v = __builtin_amdgcn_raw_buffer_load_b32(rs, off, 0, 16);
    if (__all(v != SENT)) break;
    __builtin_amdgcn_s_sleep(4);
    if (spin_fail(spins, abortw)) break;
  }
  asm volatile("" ::: "memory");
}

#define LST(i) do {} while (0)
#define RDY(i) do {} while (0)
#define RDW(i) do {} while (0)
#define SMP(k, s, v) do {} while (0)
#define LSTC(i) do {} while (0)
__device__ __forceinline__ unsigned offA(unsigned row, unsigned ch) {
  return row * 128u + 16u * (ch ^ (row & 7u));
}
__device__ __forceinline__ unsigned swzB(unsigned row) { return ((row & 3u) << 2) | ((row >> 2) & 3u); }
__device__ __forceinline__ unsigned offB(unsigned row, unsigned ch) {
  return 256u * row + 16u * (ch ^ swzB(row));
}
__device__ __forceinline__ void glds16(const u16* src, char* dst) {
  const unsigned d = __builtin_amdgcn_readfirstlane(lds_off(dst));
  unsigned keep;
  asm volatile("s_mov_b32 %0, m0\n\ts_mov_b32 m0, %2\n\ts_nop 0\n\tglobal_load_lds_dwordx4 %1, off\n\ts_mov_b32 m0, %0"
               : "=&s"(keep) : "v"(src), "s"(d) : "memory");
}
__device__ __forceinline__ bf16x8 ld_bfrag(const char* sb, int ks, int c, int lane) {
  const unsigned g = lane >> 4, qq = (lane & 15) >> 2, pp = lane & 3;
  s16x4 lo = __builtin_amdgcn_ds_read_tr16_b64_v4i16(
      (lds_s16x4_ptr)(sb + offB(32 * ks + 8 * g + qq, 2 * c + (pp >> 1)) + 8 * (pp & 1)));
  s16x4 hi = __builtin_amdgcn_ds_read_tr16_b64_v4i16(
      (lds_s16x4_ptr)(sb + offB(32 * ks + 8 * g + 4 + qq, 2 * c + (pp >> 1)) + 8 * (pp & 1)));
  return (bf16x8){lo[0], lo[1], lo[2], lo[3], hi[0], hi[1], hi[2], hi[3]};
}

__device__ __forceinline__ void blk_dma(const u16* src, size_t ld, char* slot, int lane) {
  const u16* s = src + (size_t)(lane >> 2) * ld + (lane & 3) * 8;
  glds16(s, slot);
  glds16(s + 16 * ld, slot + 1024);
}
__device__ __forceinline__ bf16x8 blk_frag(const char* slot, int T, int lane) {
  const char* a = slot + ((lane >> 4) * 8 + ((lane & 15) >> 2)) * 64 + (lane & 3) * 16 + T * 8;
  s16x4 lo = __builtin_amdgcn_ds_read_tr16_b64_v4i16((lds_s16x4_ptr)a);
  s16x4 hi = __builtin_amdgcn_ds_read_tr16_b64_v4i16((lds_s16x4_ptr)(a + 256));
  return (bf16x8){lo[0], lo[1], lo[2], lo[3], hi[0], hi[1], hi[2], hi[3]};
}
template <int NB, typename ISSUE, typename SINK>
__device__ __forceinline__ void load_blocks(char* slice, ISSUE&& issue, SINK&& sink) {
#pragma unroll
  for (int i = 0; i < 8; ++i) issue(i, slice + i * 2048);
#pragma unroll
  for (int g = 0; g < NB / 4; ++g) {
    if (g + 1 < NB / 4) WAIT_V(8); else WAIT_V(0);
#pragma unroll
    for (int j = 0; j < 4; ++j) sink(4 * g + j, slice + ((4 * g + j) & 7) * 2048);
    asm volatile("s_waitcnt lgkmcnt(0)" ::: "memory");
    if (g + 2 < NB / 4) {
#pragma unroll
      for (int j = 0; j < 4; ++j) issue(4 * g + 8 + j, slice + ((4 * g + j) & 7) * 2048);
    }
  }
}

__device__ __forceinline__ u32x4 widen32(unsigned a0, unsigned a1, unsigned b0, unsigned b1) {
  swap32(a0, b0); swap16(a0, b0);
  swap32(a1, b1); swap16(a1, b1);
  return (u32x4){a0, a1, b0, b1};
}

__device__ __forceinline__ void publish_flags(__amdgpu_buffer_rsrc_t rs, int off, int stride, int n) {
  WAIT_V(0);
  LBAR();
  int tid = threadIdx.x;
  asm volatile("" : "+v"(tid));
  if (tid < n) __builtin_amdgcn_raw_buffer_store_b32(1u, rs, off + tid * stride, 0, 16);
}

__device__ __forceinline__ void wait_s(const Args& p, __amdgpu_buffer_rsrc_t rsy, int rblk, int c0, int n, bool need) {
  if (need) {
    if (threadIdx.x < 64) {
      for (unsigned spins = 0;;) {
        asm volatile("" ::: "memory");
        const int f = (int)__builtin_amdgcn_raw_buffer_load_b32(rsy, SY_FS + (2 * c0 + (threadIdx.x & (2 * n - 1))) * 128, 0, 16);
        if (__all(f >= rblk)) break;
        __builtin_amdgcn_s_sleep(32);
        if (spin_fail(spins, (gu32*)(p.ws + WS_SY + SY_ABORT))) break;
      }
    }
    LBAR();
  }
}
__device__ __forceinline__ u32x4 combine(u32x4 sv, u32x4 g, u32x4 x) {
  u32x4 o;
#pragma unroll
  for (int j = 0; j < 4; ++j) {
    const unsigned t = cvtpk(lo_f(sv[j]) * lo_f(g[j]), hi_f(sv[j]) * hi_f(g[j]));
    o[j] = cvtpk(lo_f(x[j]) + lo_f(t), hi_f(x[j]) + hi_f(t));
  }
  return o;
}
#define OUT_AUX 0
__device__ __forceinline__ void st_out(const Args& p, int e, u32x4 v) {
  __builtin_amdgcn_raw_buffer_store_b128(v, mkrsrc(p.out, S_LEN * DM * 2), e * 2, 0, OUT_AUX);
}
struct SX { u32x4 s, x; };
__device__ __forceinline__ SX get_sx(const Args& p, int rblk, int e) {
  SX r; r.s = (u32x4){0u, 0u, 0u, 0u}; r.x = r.s;
  if (rblk < NFUSE) {
    r.s = ld_sc1(mkrsrc(p.ws + WS_S, S_LEN * DM * 2), (((e & 1023) >> 5) * S_LEN + (e >> 10)) * 64 + (e & 31) * 2);
    r.x = *reinterpret_cast<const u32x4*>(p.x + e);
  }
  return r;
}
__device__ __forceinline__ void put_g(const Args& p, __amdgpu_buffer_rsrc_t rg, int rblk, int e, u32x4 g, const SX& v) {
  if (rblk < NFUSE) {
    st_out(p, e, combine(v.s, g, v.x));
  } else st_sc1(rg, e * 2, g);
}

__device__ __forceinline__ void job_comb(const Args& p, int rblk, int ct, __amdgpu_buffer_rsrc_t rsy) {
  int tid = threadIdx.x;
  asm volatile("" : "+v"(tid));
  u32x4 xv[8];
#pragma unroll
  for (int i = 0; i < 8; ++i) xv[i] = *reinterpret_cast<const u32x4*>(p.x + (rblk * 128 + i * 16 + (tid >> 5)) * DM + ct * 256 + (tid & 31) * 8);
  if (tid < 64) wait_flags(rsy, SY_FG + (rblk * 8 + 2 * ct + (tid & 1)) * 128, (gu32*)(p.ws + WS_SY + SY_ABORT));
  wait_s(p, rsy, rblk, ct * 8, 8, true);
  u32x4 sv[8], gv[8];
#pragma unroll
  for (int i = 0; i < 8; ++i) {
    const int e = (rblk * 128 + i * 16 + (tid >> 5)) * DM + ct * 256 + (tid & 31) * 8;
    sv[i] = ld_sc1(mkrsrc(p.ws + WS_S, S_LEN * DM * 2), (((e & 1023) >> 5) * S_LEN + (e >> 10)) * 64 + (e & 31) * 2);
    gv[i] = ld_sc1(mkrsrc(p.ws + WS_G, S_LEN * DM * 2), e * 2);
  }
#pragma unroll
  for (int i = 0; i < 8; ++i)
    st_out(p, (rblk * 128 + i * 16 + (tid >> 5)) * DM + ct * 256 + (tid & 31) * 8, combine(sv[i], gv[i], xv[i]));
}

__device__ __forceinline__ u32x4 raw_rsrc(const void* p, unsigned bytes) {
  const unsigned long long a = (unsigned long long)p;
  return (u32x4){(unsigned)a, (unsigned)(a >> 32) & 0xffffu, bytes, 0x00020000u};
}
__device__ __forceinline__ u32x2 next_tag(u32x2 e) { return (u32x2){e[0] + 1u, e[1] + (e[0] == 0xFFFFFFFFu)}; }
struct Boot { u32x2 e; u32x4 c; };
__device__ __forceinline__ void boot_ask(const char* ws, Boot& r) {
  const unsigned o = (threadIdx.x & 63) * 128;
  asm volatile("buffer_load_dwordx2 %0, off, %3, %4 sc1\n\tbuffer_load_dwordx4 %1, %2, %3, %5 offen sc1"
               : "=&v"(r.e), "=&v"(r.c) : "v"(o), "s"(raw_rsrc(ws, SY_TOTAL)), "s"(SY_EP), "s"(SY_BT) : "memory");
}
__device__ __forceinline__ void wait_rows(const char* ws, u32x2 tag) {
  const __amdgpu_buffer_rsrc_t rs = mkrsrc(ws, SY_TOTAL);
  int o = (threadIdx.x & 63) * 128;
  asm volatile("" : "+v"(o));
#pragma unroll 1
  for (int i = 0; i < (1 << 17); ++i) {
    asm volatile("" ::: "memory");
    const u32x2 a = __builtin_amdgcn_raw_buffer_load_b64(rs, o, SY_BT + 32 * 128, 16), b = __builtin_amdgcn_raw_buffer_load_b64(rs, o, SY_BT + 96 * 128, 16);
    if (__all(a[0] == tag[0] && a[1] == tag[1] && b[0] == tag[0] && b[1] == tag[1])) break;
    __builtin_amdgcn_s_sleep(8);
  }
  asm volatile("" ::: "memory");
}
__device__ __forceinline__ unsigned boot_done(const char* ws, Boot& r, bool own = false, u32x2 tag = (u32x2){0u, 0u}) {
#pragma unroll 1
  for (int i = 0; i < (1 << 17); ++i) {
    asm volatile("" : "+v"(r.e), "+v"(r.c));
    if (!own) tag = next_tag(r.e);
    if (__all(r.c[0] == tag[0] && r.c[1] == tag[1])) break;
    __builtin_amdgcn_s_sleep(8);
    boot_ask(ws, r);
    WAIT_V(0);
  }
  unsigned q0[NPAGE], q1[NPAGE], ga = 0xFFFFu, gb = 0xFFFFu;
#pragma unroll
  for (int i = 0; i < NPAGE; ++i) {
    const unsigned x = __builtin_amdgcn_readlane(r.c[2], i), y = __builtin_amdgcn_readlane(r.c[2], i + NPAGE);
    q0[i] = min(x >> 16, y >> 16); q1[i] = min(x & 0xFFFFu, y & 0xFFFFu);
    ga = min(ga, (i & 1) ? q1[i] : q0[i]); gb = min(gb, (i & 1) ? q0[i] : q1[i]);
  }
  unsigned m = gb < ga ? 0x80001555u : 0x2AAAu;
  const unsigned thr = min(ga, gb) + 110;
#pragma unroll
  for (int i = 0; i < NPAGE; ++i) {
    const unsigned lo = min(q0[i], q1[i]), hi = max(q0[i], q1[i]);
    const unsigned keep = (lo < thr ? hi : 0u) > lo + 100 ? ~(1u << i) : ~0u;
    m = (m & keep) | ((q1[i] < q0[i] ? ~0u : 0u) & ~keep);
  }
  return m;
}
typedef __attribute__((address_space(3))) volatile unsigned* lds_vu_ptr;
__device__ __forceinline__ lds_vu_ptr lds_at(const void* p) {
  unsigned a = lds_off(p);
  asm volatile("" : "+v"(a));
  return (lds_vu_ptr)(uintptr_t)a;
}
__device__ __forceinline__ unsigned uni(unsigned v) {
  unsigned r;
  asm volatile("s_nop 1\n\tv_readfirstlane_b32 %0, %1\n\ts_nop 3" : "=s"(r) : "v"(v));
  return r;
}
__device__ __forceinline__ void start_sy(char* ws, const u16* mem0, int i, char* smem) {
  const int tid = threadIdx.x, lane = tid & 63, w = __builtin_amdgcn_readfirstlane(tid >> 6);
  const __amdgpu_buffer_rsrc_t rsy = mkrsrc(ws, SY_TOTAL);
  const __amdgpu_buffer_rsrc_t rs = mkrsrc(ws + (size_t)(i % NPAGE) * PAGE + PROBE - (i / NPAGE) * 2048, 16384);
  const u32x2 e = __builtin_amdgcn_raw_buffer_load_b64(rsy, 0, SY_EP, 16);
  const u32x4 ds = {DSENT, DSENT, DSENT, DSENT};
  if (lane < 32) st_sc1(rs, (lane >> 4) * 8192 + w * 256 + (lane & 15) * 16, ds);
  static_assert(NPRE == 4 && SY_FEND - SY_BR <= 32 * 6144, "");
#pragma unroll
  for (int u = 0; u < 6; ++u)
    st_sc1(rsy, (u < 4 ? SY_V + (i * 4 + u) * 8192 : SY_O + (i * 2 + u - 4) * 8192) + tid * 16, ds);
  if (w == 7) {
    const int n = i * 64 + lane, j = (n >> 10) * 512 + (n & 511);
    const unsigned* m0 = reinterpret_cast<const unsigned*>(mem0 + (2 * ((j >> 2) & 3) + (j & 1)) * DM + (j >> 4) * 16) + ((j >> 1) & 1);
    u32x4 m;
#pragma unroll
    for (int k = 0; k < 4; ++k) { const unsigned t = m0[2 * k]; m[k] = t == DSENT ? 0xFFFFFFFFu : t; }
    st_sc1(rsy, n * 16, m);
  } else if (w == 6) st_sc1(rsy, 32768 + i * 1024 + lane * 16, ds);
  else {
    const int o = SY_BR + i * 6144 + tid * 16;
    st_sc1(rsy, o < SY_FEND ? o : 0x7F000000, (u32x4){SENT, SENT, SENT, SENT});
  }
  unsigned t[4];
#pragma unroll
  for (int k = 0; k < 4; ++k) {
    unsigned long long c0, c1; unsigned v, off = ((k ^ (k >> 1)) & 1) * 8192 + w * 256 + (k >> 1) * 128;
    asm volatile("s_waitcnt vmcnt(0)\n\ts_barrier\n\ts_memtime %0\n\ts_waitcnt lgkmcnt(0)\n\tbuffer_load_dword %2, %3, %4, 0 offen sc1\n\ts_waitcnt vmcnt(0)\n\t"
                 "s_memtime %1\n\ts_waitcnt lgkmcnt(0)" : "=&s"(c0), "=&s"(c1), "=&v"(v) : "v"(off), "s"(rs) : "memory");
    t[k] = (unsigned)(c1 - c0);
  }
  lds_vu_ptr tm = lds_at(smem);
  if (lane == 0) { tm[w] = min(t[0], t[3]); tm[8 + w] = min(t[1], t[2]); }
  int ro = SY_BT + i * 128 + lane * 16;
  asm volatile("" : "+v"(ro));
  LBAR();
  RDY(384 + i);
  if (threadIdx.x < 8) {
    unsigned m0 = tm[0], m1 = tm[8];
#pragma unroll
    for (int k = 1; k < 8; ++k) { m0 = min(m0, tm[k]); m1 = min(m1, tm[8 + k]); }
    const u32x2 tag = next_tag(e);
    st_sc1(rsy, ro, (u32x4){tag[0], tag[1], min(m0, 30000u) << 16 | min(m1, 30000u), 0u});
  }
}
template <int NB, typename F>
__device__ __forceinline__ u32x2 start_rows(const Args& p, int g, unsigned on, F&& between) {
  const int tid = threadIdx.x, lane = tid & 63, row0 = g * 16 + (tid >> 6) * 2;
  u32x4 a[4];
  u32x2 e;
  asm volatile("buffer_load_dwordx4 %0, %5, %8, 0 offen\n\tbuffer_load_dwordx4 %1, %5, %8, 0 offen offset:1024\n\t"
               "buffer_load_dwordx4 %2, %5, %8, 0 offen offset:2048\n\tbuffer_load_dwordx4 %3, %5, %8, 0 offen offset:3072\n\t"
               "buffer_load_dwordx2 %4, off, %6, %7 sc1"
               : "=&v"(a[0]), "=&v"(a[1]), "=&v"(a[2]), "=&v"(a[3]), "=&v"(e)
               : "v"(row0 * (DM * 2) + lane * 16), "s"(raw_rsrc(p.ws, SY_TOTAL)), "s"(SY_EP), "s"(raw_rsrc(p.x, on * (S_LEN * DM * 2))) : "memory");
  between();
  asm volatile("s_waitcnt vmcnt(%5)" : "+v"(a[0]), "+v"(a[1]), "+v"(a[2]), "+v"(a[3]), "+v"(e) : "n"(NB) : "memory");
  const __amdgpu_buffer_rsrc_t ro = mkrsrc(p.ws + WS_XN, on * (S_LEN * XLD * 2));
#pragma unroll
  for (int u = 0; u < 2; ++u) {
    float v[16];
#pragma unroll
    for (int i = 0; i < 8; ++i) { v[2 * i] = lo_f(a[2 * u + (i >> 2)][i & 3]); v[2 * i + 1] = hi_f(a[2 * u + (i >> 2)][i & 3]); }
    float s = 0.f;
#pragma unroll
    for (int i = 0; i < 16; ++i) s += v[i];
    const float mean = wave_sum(s) * (1.0f / DM);
    float q = 0.f;
#pragma unroll
    for (int i = 0; i < 16; ++i) { float d = v[i] - mean; q += d * d; }
    const float var = rbf(wave_sum(q) * (1.0f / DM));
    const float mb = rbf(mean);
    const float inv = 1.0f / rbf(sqrtf(rbf(var + 1e-5f)));
#pragma unroll
    for (int k = 0; k < 2; ++k) {
      u32x4 o;
#pragma unroll
      for (int i = 0; i < 4; ++i) {
        const unsigned t = cvtpk(v[8 * k + 2 * i] - mb, v[8 * k + 2 * i + 1] - mb);
        o[i] = cvtpk(lo_f(t) * inv, hi_f(t) * inv);
      }
      st_sc1(ro, (row0 + u) * (XLD * 2) + k * 1024 + lane * 16, o);
    }
  }
  return next_tag(e);
}
__device__ __forceinline__ void report_rows(const Args& p, int g, u32x2 tag) {
  RDY(256 + g);
  if (threadIdx.x < 8) st_sc1(mkrsrc(p.ws + WS_SY, SY_TOTAL), SY_BT + (32 + g) * 128 + threadIdx.x * 16, (u32x4){tag[0], tag[1], tag[0], tag[1]});
}

typedef __attribute__((ext_vector_type(32))) unsigned u32x32;
__device__ __forceinline__ void put4(u32x32& W, int i, bf16x8 f) {
  const u32x4 v = __builtin_bit_cast(u32x4, f);
#pragma unroll
  for (int j = 0; j < 4; ++j) W[4 * i + j] = v[j];
}
#define CLB8(a, b, c, d, e, f, g, h) "v" #a, "v" #b, "v" #c, "v" #d, "v" #e, "v" #f, "v" #g, "v" #h
#define CLB32(a) CLB32_##a
#define CLB32_128 CLB8(128, 129, 130, 131, 132, 133, 134, 135), CLB8(136, 137, 138, 139, 140, 141, 142, 143), CLB8(144, 145, 146, 147, 148, 149, 150, 151), CLB8(152, 153, 154, 155, 156, 157, 158, 159)
#define CLB32_160 CLB8(160, 161, 162, 163, 164, 165, 166, 167), CLB8(168, 169, 170, 171, 172, 173, 174, 175), CLB8(176, 177, 178, 179, 180, 181, 182, 183), CLB8(184, 185, 186, 187, 188, 189, 190, 191)
#define W4_IN "{v[0:31]}"(W[0]), "{v[32:63]}"(W[1]), "{v[64:95]}"(W[2]), "{v[96:127]}"(W[3])
#define CLB32_192 CLB8(192, 193, 194, 195, 196, 197, 198, 199), CLB8(200, 201, 202, 203, 204, 205, 206, 207), CLB8(208, 209, 210, 211, 212, 213, 214, 215), CLB8(216, 217, 218, 219, 220, 221, 222, 223)
__device__ __forceinline__ unsigned ld_flag0(__amdgpu_buffer_rsrc_t rs, int off) {
  return __builtin_amdgcn_raw_buffer_load_b32(rs, off, 0, 16);
}
__device__ __forceinline__ void glds16_sc1(const char* src, char* dst) {
  const unsigned d = __builtin_amdgcn_readfirstlane(lds_off(dst));
  unsigned keep;
  asm volatile("s_mov_b32 %0, m0\n\ts_mov_b32 m0, %2\n\ts_nop 0\n\tglobal_load_lds_dwordx4 %1, off sc1\n\ts_mov_b32 m0, %0"
               : "=&s"(keep) : "v"(src), "s"(d) : "memory");
}

__device__ __forceinline__ void touch_line(const char* src, char* dst) {
  const unsigned d = __builtin_amdgcn_readfirstlane(lds_off(dst));
  unsigned keep;
  asm volatile("s_mov_b32 %0, m0\n\ts_mov_b32 m0, %2\n\ts_nop 0\n\tglobal_load_lds_dword %1, off sc1\n\ts_mov_b32 m0, %0"
               : "=&s"(keep) : "v"(src), "s"(d) : "memory");
}

#define GPF 1
#define GU_DELAY 0
#define Q_DELAY 0
#define PF_T0 100
#define PF_N 12
#define A_AUX " sc1"
#define LN_M 3
#define LN_X 1
#define PR_G1 8
#define WREV 1
template <bool DEV = false>
__device__ __forceinline__ void glds2(const char* base, int step, unsigned voff, unsigned dst) {
  unsigned keep;
  const char* base2 = base + step;
  if (DEV)
    asm volatile("s_mov_b32 %0, m0\n\ts_mov_b32 m0, %4\n\ts_nop 0\n\tglobal_load_lds_dwordx4 %1, %2 sc1\n\t"
                 "s_add_u32 m0, m0, 0x2000\n\ts_nop 0\n\tglobal_load_lds_dwordx4 %1, %3 sc1\n\ts_mov_b32 m0, %0"
                 : "=&s"(keep) : "v"(voff), "s"(base), "s"(base2), "s"(dst) : "memory", "scc");
  else
  asm volatile("s_mov_b32 %0, m0\n\ts_mov_b32 m0, %4\n\ts_nop 0\n\tglobal_load_lds_dwordx4 %1, %2\n\t"
               "s_add_u32 m0, m0, 0x2000\n\ts_nop 0\n\tglobal_load_lds_dwordx4 %1, %3\n\ts_mov_b32 m0, %0"
               : "=&s"(keep) : "v"(voff), "s"(base), "s"(base2), "s"(dst) : "memory", "scc");
}

#define DN_ASM \
  "s_mov_b32 s94, m0\n\t" \
  "s_mov_b64 s[84:85], %[pa]\n\t" \
  "s_mov_b64 s[86:87], %[pb]\n\t" \
  "s_movk_i32 s88, 0x4000\n\t" \
  "s_mov_b32 s89, 0x10000\n\t" \
  "s_movk_i32 s90, 13\n\t" \
  "v_mov_b64 v[0:1], 0\n\t" \
  "v_mov_b64 v[2:3], 0\n\t" \
  "v_mov_b64 v[4:5], 0\n\t" \
  "v_mov_b64 v[6:7], 0\n\t" \
  "v_mov_b64 v[8:9], 0\n\t" \
  "v_mov_b64 v[10:11], 0\n\t" \
  "v_mov_b64 v[12:13], 0\n\t" \
  "v_mov_b64 v[14:15], 0\n\t" \
  "v_mov_b64 v[16:17], 0\n\t" \
  "v_mov_b64 v[18:19], 0\n\t" \
  "v_mov_b64 v[20:21], 0\n\t" \
  "v_mov_b64 v[22:23], 0\n\t" \
  "v_mov_b64 v[24:25], 0\n\t" \
  "v_mov_b64 v[26:27], 0\n\t" \
  "v_mov_b64 v[28:29], 0\n\t" \
  "v_mov_b64 v[30:31], 0\n\t" \
  "s_cmp_lg_u32 %[hf], 0\n\t" \
  "s_cbranch_scc1 .Lk%=\n\t" \
  "ds_read_b128 v[32:35], v80 offset:0\n\t" \
  "ds_read_b128 v[36:39], v80 offset:1024\n\t" \
  "ds_read_b128 v[40:43], v80 offset:2048\n\t" \
  "ds_read_b128 v[44:47], v80 offset:3072\n\t" \
  "ds_read_b64_tr_b16 v[64:65], v82 offset:0\n\t" \
  "ds_read_b64_tr_b16 v[66:67], v83 offset:0\n\t" \
  "ds_read_b128 v[48:51], v80 offset:8192\n\t" \
  "ds_read_b64_tr_b16 v[68:69], v84 offset:0\n\t" \
  "ds_read_b64_tr_b16 v[70:71], v85 offset:0\n\t" \
  "ds_read_b128 v[52:55], v80 offset:9216\n\t" \
  "ds_read_b128 v[56:59], v80 offset:10240\n\t" \
  "ds_read_b128 v[60:63], v80 offset:11264\n\t" \
  ".Lp0%=:\n\t" \
  "s_waitcnt vmcnt(4)\n\t" \
  "s_barrier\n\t" \
  "s_waitcnt lgkmcnt(3)\n\t" \
  "v_mfma_f32_16x16x32_bf16 v[0:3], v[64:67], v[32:35], v[0:3]\n\t" \
  "ds_read_b64_tr_b16 v[72:73], v82 offset:8192\n\t" \
  "v_mfma_f32_16x16x32_bf16 v[4:7], v[68:71], v[32:35], v[4:7]\n\t" \
  "ds_read_b64_tr_b16 v[74:75], v83 offset:8192\n\t" \
  "ds_read_b128 v[32:35], v80 offset:32768\n\t" \
  "s_add_u32 m0, %[lw], 98304\n\t" \
  "v_mfma_f32_16x16x32_bf16 v[8:11], v[64:67], v[36:39], v[8:11]\n\t" \
  "ds_read_b64_tr_b16 v[76:77], v84 offset:8192\n\t" \
  "global_load_lds_dwordx4 v90, s[84:85] sc1\n\t" \
  "s_add_u32 s84, s84, s88\n\t" \
  "s_addc_u32 s85, s85, 0\n\t" \
  "v_mfma_f32_16x16x32_bf16 v[12:15], v[68:71], v[36:39], v[12:15]\n\t" \
  "ds_read_b64_tr_b16 v[78:79], v85 offset:8192\n\t" \
  "ds_read_b128 v[36:39], v80 offset:33792\n\t" \
  "v_mfma_f32_16x16x32_bf16 v[16:19], v[64:67], v[40:43], v[16:19]\n\t" \
  "v_mfma_f32_16x16x32_bf16 v[20:23], v[68:71], v[40:43], v[20:23]\n\t" \
  "ds_read_b128 v[40:43], v80 offset:34816\n\t" \
  "s_add_u32 m0, %[lw], 114688\n\t" \
  "v_mfma_f32_16x16x32_bf16 v[24:27], v[64:67], v[44:47], v[24:27]\n\t" \
  "global_load_lds_dwordx4 v91, s[86:87]\n\t" \
  "s_add_u32 s86, s86, s89\n\t" \
  "s_addc_u32 s87, s87, 0\n\t" \
  "v_mfma_f32_16x16x32_bf16 v[28:31], v[68:71], v[44:47], v[28:31]\n\t" \
  "ds_read_b128 v[44:47], v80 offset:35840\n\t" \
  "s_waitcnt lgkmcnt(3)\n\t" \
  "v_mfma_f32_16x16x32_bf16 v[0:3], v[72:75], v[48:51], v[0:3]\n\t" \
  "ds_read_b64_tr_b16 v[64:65], v82 offset:32768\n\t" \
  "v_mfma_f32_16x16x32_bf16 v[4:7], v[76:79], v[48:51], v[4:7]\n\t" \
  "ds_read_b64_tr_b16 v[66:67], v83 offset:32768\n\t" \
  "ds_read_b128 v[48:51], v80 offset:40960\n\t" \
  "s_add_u32 m0, %[lw], 106496\n\t" \
  "v_mfma_f32_16x16x32_bf16 v[8:11], v[72:75], v[52:55], v[8:11]\n\t" \
  "ds_read_b64_tr_b16 v[68:69], v84 offset:32768\n\t" \
  "global_load_lds_dwordx4 v90, s[84:85] sc1\n\t" \
  "s_add_u32 s84, s84, s88\n\t" \
  "s_addc_u32 s85, s85, 0\n\t" \
  "v_mfma_f32_16x16x32_bf16 v[12:15], v[76:79], v[52:55], v[12:15]\n\t" \
  "ds_read_b64_tr_b16 v[70:71], v85 offset:32768\n\t" \
  "ds_read_b128 v[52:55], v80 offset:41984\n\t" \
  "v_mfma_f32_16x16x32_bf16 v[16:19], v[72:75], v[56:59], v[16:19]\n\t" \
  "v_mfma_f32_16x16x32_bf16 v[20:23], v[76:79], v[56:59], v[20:23]\n\t" \
  "ds_read_b128 v[56:59], v80 offset:43008\n\t" \
  "s_add_u32 m0, %[lw], 122880\n\t" \
  "v_mfma_f32_16x16x32_bf16 v[24:27], v[72:75], v[60:63], v[24:27]\n\t" \
  "global_load_lds_dwordx4 v91, s[86:87]\n\t" \
  "s_add_u32 s86, s86, s89\n\t" \
  "s_addc_u32 s87, s87, 0\n\t" \
  "v_mfma_f32_16x16x32_bf16 v[28:31], v[76:79], v[60:63], v[28:31]\n\t" \
  "ds_read_b128 v[60:63], v80 offset:44032\n\t" \
  "s_sub_u32 s90, s90, 1\n\t" \
  "s_cselect_b32 s88, 0, s88\n\t" \
  "s_cselect_b32 s89, 0, s89\n\t" \
  "s_cselect_b32 s91, 16384, 0\n\t" \
  "s_sub_u32 s84, s84, s91\n\t" \
  "s_subb_u32 s85, s85, 0\n\t" \
  "s_lshl_b32 s91, s91, 2\n\t" \
  "s_sub_u32 s86, s86, s91\n\t" \
  "s_subb_u32 s87, s87, 0\n\t" \
  "s_waitcnt vmcnt(4)\n\t" \
  "s_barrier\n\t" \
  "s_waitcnt lgkmcnt(3)\n\t" \
  "v_mfma_f32_16x16x32_bf16 v[0:3], v[64:67], v[32:35], v[0:3]\n\t" \
  "ds_read_b64_tr_b16 v[72:73], v82 offset:40960\n\t" \
  "v_mfma_f32_16x16x32_bf16 v[4:7], v[68:71], v[32:35], v[4:7]\n\t" \
  "ds_read_b64_tr_b16 v[74:75], v83 offset:40960\n\t" \
  "ds_read_b128 v[32:35], v81 offset:0\n\t" \
  "s_add_u32 m0, %[lw], 0\n\t" \
  "v_mfma_f32_16x16x32_bf16 v[8:11], v[64:67], v[36:39], v[8:11]\n\t" \
  "ds_read_b64_tr_b16 v[76:77], v84 offset:40960\n\t" \
  "global_load_lds_dwordx4 v90, s[84:85] sc1\n\t" \
  "s_add_u32 s84, s84, s88\n\t" \
  "s_addc_u32 s85, s85, 0\n\t" \
  "v_mfma_f32_16x16x32_bf16 v[12:15], v[68:71], v[36:39], v[12:15]\n\t" \
  "ds_read_b64_tr_b16 v[78:79], v85 offset:40960\n\t" \
  "ds_read_b128 v[36:39], v81 offset:1024\n\t" \
  "v_mfma_f32_16x16x32_bf16 v[16:19], v[64:67], v[40:43], v[16:19]\n\t" \
  "v_mfma_f32_16x16x32_bf16 v[20:23], v[68:71], v[40:43], v[20:23]\n\t" \
  "ds_read_b128 v[40:43], v81 offset:2048\n\t" \
  "s_add_u32 m0, %[lw], 16384\n\t" \
  "v_mfma_f32_16x16x32_bf16 v[24:27], v[64:67], v[44:47], v[24:27]\n\t" \
  "global_load_lds_dwordx4 v91, s[86:87]\n\t" \
  "s_add_u32 s86, s86, s89\n\t" \
  "s_addc_u32 s87, s87, 0\n\t" \
  "v_mfma_f32_16x16x32_bf16 v[28:31], v[68:71], v[44:47], v[28:31]\n\t" \
  "ds_read_b128 v[44:47], v81 offset:3072\n\t" \
  "s_waitcnt lgkmcnt(3)\n\t" \
  "v_mfma_f32_16x16x32_bf16 v[0:3], v[72:75], v[48:51], v[0:3]\n\t" \
  "ds_read_b64_tr_b16 v[64:65], v86 offset:0\n\t" \
  "v_mfma_f32_16x16x32_bf16 v[4:7], v[76:79], v[48:51], v[4:7]\n\t" \
  "ds_read_b64_tr_b16 v[66:67], v87 offset:0\n\t" \
  "ds_read_b128 v[48:51], v81 offset:8192\n\t" \
  "s_add_u32 m0, %[lw], 8192\n\t" \
  "v_mfma_f32_16x16x32_bf16 v[8:11], v[72:75], v[52:55], v[8:11]\n\t" \
  "ds_read_b64_tr_b16 v[68:69], v88 offset:0\n\t" \
  "global_load_lds_dwordx4 v90, s[84:85] sc1\n\t" \
  "s_add_u32 s84, s84, s88\n\t" \
  "s_addc_u32 s85, s85, 0\n\t" \
  "v_mfma_f32_16x16x32_bf16 v[12:15], v[76:79], v[52:55], v[12:15]\n\t" \
  "ds_read_b64_tr_b16 v[70:71], v89 offset:0\n\t" \
  "ds_read_b128 v[52:55], v81 offset:9216\n\t" \
  "v_mfma_f32_16x16x32_bf16 v[16:19], v[72:75], v[56:59], v[16:19]\n\t" \
  "v_mfma_f32_16x16x32_bf16 v[20:23], v[76:79], v[56:59], v[20:23]\n\t" \
  "ds_read_b128 v[56:59], v81 offset:10240\n\t" \
  "s_add_u32 m0, %[lw], 24576\n\t" \
  "v_mfma_f32_16x16x32_bf16 v[24:27], v[72:75], v[60:63], v[24:27]\n\t" \
  "global_load_lds_dwordx4 v91, s[86:87]\n\t" \
  "s_add_u32 s86, s86, s89\n\t" \
  "s_addc_u32 s87, s87, 0\n\t" \
  "v_mfma_f32_16x16x32_bf16 v[28:31], v[76:79], v[60:63], v[28:31]\n\t" \
  "ds_read_b128 v[60:63], v81 offset:11264\n\t" \
  "s_waitcnt vmcnt(4)\n\t" \
  "s_barrier\n\t" \
  "s_waitcnt lgkmcnt(3)\n\t" \
  "v_mfma_f32_16x16x32_bf16 v[0:3], v[64:67], v[32:35], v[0:3]\n\t" \
  "ds_read_b64_tr_b16 v[72:73], v86 offset:8192\n\t" \
  "v_mfma_f32_16x16x32_bf16 v[4:7], v[68:71], v[32:35], v[4:7]\n\t" \
  "ds_read_b64_tr_b16 v[74:75], v87 offset:8192\n\t" \
  "ds_read_b128 v[32:35], v81 offset:32768\n\t" \
  "s_add_u32 m0, %[lw], 32768\n\t" \
  "v_mfma_f32_16x16x32_bf16 v[8:11], v[64:67], v[36:39], v[8:11]\n\t" \
  "ds_read_b64_tr_b16 v[76:77], v88 offset:8192\n\t" \
  "global_load_lds_dwordx4 v90, s[84:85] sc1\n\t" \
  "s_add_u32 s84, s84, s88\n\t" \
  "s_addc_u32 s85, s85, 0\n\t" \
  "v_mfma_f32_16x16x32_bf16 v[12:15], v[68:71], v[36:39], v[12:15]\n\t" \
  "ds_read_b64_tr_b16 v[78:79], v89 offset:8192\n\t" \
  "ds_read_b128 v[36:39], v81 offset:33792\n\t" \
  "v_mfma_f32_16x16x32_bf16 v[16:19], v[64:67], v[40:43], v[16:19]\n\t" \
  "v_mfma_f32_16x16x32_bf16 v[20:23], v[68:71], v[40:43], v[20:23]\n\t" \
  "ds_read_b128 v[40:43], v81 offset:34816\n\t" \
  "s_add_u32 m0, %[lw], 49152\n\t" \
  "v_mfma_f32_16x16x32_bf16 v[24:27], v[64:67], v[44:47], v[24:27]\n\t" \
  "global_load_lds_dwordx4 v91, s[86:87]\n\t" \
  "s_add_u32 s86, s86, s89\n\t" \
  "s_addc_u32 s87, s87, 0\n\t" \
  "v_mfma_f32_16x16x32_bf16 v[28:31], v[68:71], v[44:47], v[28:31]\n\t" \
  "ds_read_b128 v[44:47], v81 offset:35840\n\t" \
  "s_waitcnt lgkmcnt(3)\n\t" \
  "v_mfma_f32_16x16x32_bf16 v[0:3], v[72:75], v[48:51], v[0:3]\n\t" \
  "ds_read_b64_tr_b16 v[64:65], v86 offset:32768\n\t" \
  "v_mfma_f32_16x16x32_bf16 v[4:7], v[76:79], v[48:51], v[4:7]\n\t" \
  "ds_read_b64_tr_b16 v[66:67], v87 offset:32768\n\t" \
  "ds_read_b128 v[48:51], v81 offset:40960\n\t" \
  "s_add_u32 m0, %[lw], 40960\n\t" \
  "v_mfma_f32_16x16x32_bf16 v[8:11], v[72:75], v[52:55], v[8:11]\n\t" \
  "ds_read_b64_tr_b16 v[68:69], v88 offset:32768\n\t" \
  "global_load_lds_dwordx4 v90, s[84:85] sc1\n\t" \
  "s_add_u32 s84, s84, s88\n\t" \
  "s_addc_u32 s85, s85, 0\n\t" \
  "v_mfma_f32_16x16x32_bf16 v[12:15], v[76:79], v[52:55], v[12:15]\n\t" \
  "ds_read_b64_tr_b16 v[70:71], v89 offset:32768\n\t" \
  "ds_read_b128 v[52:55], v81 offset:41984\n\t" \
  "v_mfma_f32_16x16x32_bf16 v[16:19], v[72:75], v[56:59], v[16:19]\n\t" \
  "v_mfma_f32_16x16x32_bf16 v[20:23], v[76:79], v[56:59], v[20:23]\n\t" \
  "ds_read_b128 v[56:59], v81 offset:43008\n\t" \
  "s_add_u32 m0, %[lw], 57344\n\t" \
  "v_mfma_f32_16x16x32_bf16 v[24:27], v[72:75], v[60:63], v[24:27]\n\t" \
  "global_load_lds_dwordx4 v91, s[86:87]\n\t" \
  "s_add_u32 s86, s86, s89\n\t" \
  "s_addc_u32 s87, s87, 0\n\t" \
  "v_mfma_f32_16x16x32_bf16 v[28:31], v[76:79], v[60:63], v[28:31]\n\t" \
  "ds_read_b128 v[60:63], v81 offset:44032\n\t" \
  "s_waitcnt vmcnt(4)\n\t" \
  "s_barrier\n\t" \
  "s_waitcnt lgkmcnt(3)\n\t" \
  "v_mfma_f32_16x16x32_bf16 v[0:3], v[64:67], v[32:35], v[0:3]\n\t" \
  "ds_read_b64_tr_b16 v[72:73], v86 offset:40960\n\t" \
  "v_mfma_f32_16x16x32_bf16 v[4:7], v[68:71], v[32:35], v[4:7]\n\t" \
  "ds_read_b64_tr_b16 v[74:75], v87 offset:40960\n\t" \
  "ds_read_b128 v[32:35], v80 offset:0\n\t" \
  "s_add_u32 m0, %[lw], 65536\n\t" \
  "v_mfma_f32_16x16x32_bf16 v[8:11], v[64:67], v[36:39], v[8:11]\n\t" \
  "ds_read_b64_tr_b16 v[76:77], v88 offset:40960\n\t" \
  "global_load_lds_dwordx4 v90, s[84:85] sc1\n\t" \
  "s_add_u32 s84, s84, s88\n\t" \
  "s_addc_u32 s85, s85, 0\n\t" \
  "v_mfma_f32_16x16x32_bf16 v[12:15], v[68:71], v[36:39], v[12:15]\n\t" \
  "ds_read_b64_tr_b16 v[78:79], v89 offset:40960\n\t" \
  "ds_read_b128 v[36:39], v80 offset:1024\n\t" \
  "v_mfma_f32_16x16x32_bf16 v[16:19], v[64:67], v[40:43], v[16:19]\n\t" \
  "v_mfma_f32_16x16x32_bf16 v[20:23], v[68:71], v[40:43], v[20:23]\n\t" \
  "ds_read_b128 v[40:43], v80 offset:2048\n\t" \
  "s_add_u32 m0, %[lw], 81920\n\t" \
  "v_mfma_f32_16x16x32_bf16 v[24:27], v[64:67], v[44:47], v[24:27]\n\t" \
  "global_load_lds_dwordx4 v91, s[86:87]\n\t" \
  "s_add_u32 s86, s86, s89\n\t" \
  "s_addc_u32 s87, s87, 0\n\t" \
  "v_mfma_f32_16x16x32_bf16 v[28:31], v[68:71], v[44:47], v[28:31]\n\t" \
  "ds_read_b128 v[44:47], v80 offset:3072\n\t" \
  "s_waitcnt lgkmcnt(3)\n\t" \
  "v_mfma_f32_16x16x32_bf16 v[0:3], v[72:75], v[48:51], v[0:3]\n\t" \
  "ds_read_b64_tr_b16 v[64:65], v82 offset:0\n\t" \
  "v_mfma_f32_16x16x32_bf16 v[4:7], v[76:79], v[48:51], v[4:7]\n\t" \
  "ds_read_b64_tr_b16 v[66:67], v83 offset:0\n\t" \
  "ds_read_b128 v[48:51], v80 offset:8192\n\t" \
  "s_add_u32 m0, %[lw], 73728\n\t" \
  "v_mfma_f32_16x16x32_bf16 v[8:11], v[72:75], v[52:55], v[8:11]\n\t" \
  "ds_read_b64_tr_b16 v[68:69], v84 offset:0\n\t" \
  "global_load_lds_dwordx4 v90, s[84:85] sc1\n\t" \
  "s_add_u32 s84, s84, s88\n\t" \
  "s_addc_u32 s85, s85, 0\n\t" \
  "v_mfma_f32_16x16x32_bf16 v[12:15], v[76:79], v[52:55], v[12:15]\n\t" \
  "ds_read_b64_tr_b16 v[70:71], v85 offset:0\n\t" \
  "ds_read_b128 v[52:55], v80 offset:9216\n\t" \
  "v_mfma_f32_16x16x32_bf16 v[16:19], v[72:75], v[56:59], v[16:19]\n\t" \
  "v_mfma_f32_16x16x32_bf16 v[20:23], v[76:79], v[56:59], v[20:23]\n\t" \
  "ds_read_b128 v[56:59], v80 offset:10240\n\t" \
  "s_add_u32 m0, %[lw], 90112\n\t" \
  "v_mfma_f32_16x16x32_bf16 v[24:27], v[72:75], v[60:63], v[24:27]\n\t" \
  "global_load_lds_dwordx4 v91, s[86:87]\n\t" \
  "s_add_u32 s86, s86, s89\n\t" \
  "s_addc_u32 s87, s87, 0\n\t" \
  "v_mfma_f32_16x16x32_bf16 v[28:31], v[76:79], v[60:63], v[28:31]\n\t" \
  "ds_read_b128 v[60:63], v80 offset:11264\n\t" \
  "s_cmp_lg_u32 s88, 0\n\t" \
  "s_cbranch_scc1 .Lp0%=\n\t" \
  "s_branch .Le%=\n\t" \
  ".Lk%=:\n\t" \
  "ds_read_b128 v[32:35], v80 offset:0\n\t" \
  "ds_read_b128 v[36:39], v80 offset:1024\n\t" \
  "ds_read_b128 v[40:43], v80 offset:2048\n\t" \
  "ds_read_b128 v[44:47], v80 offset:3072\n\t" \
  "ds_read_b64_tr_b16 v[64:65], v82 offset:0\n\t" \
  "ds_read_b64_tr_b16 v[66:67], v83 offset:0\n\t" \
  "ds_read_b128 v[48:51], v80 offset:8192\n\t" \
  "ds_read_b64_tr_b16 v[68:69], v84 offset:0\n\t" \
  "ds_read_b64_tr_b16 v[70:71], v85 offset:0\n\t" \
  "ds_read_b128 v[52:55], v80 offset:9216\n\t" \
  "ds_read_b128 v[56:59], v80 offset:10240\n\t" \
  "ds_read_b128 v[60:63], v80 offset:11264\n\t" \
  ".Lp1%=:\n\t" \
  "s_waitcnt vmcnt(4)\n\t" \
  "s_barrier\n\t" \
  "s_waitcnt lgkmcnt(3)\n\t" \
  "v_mfma_f32_16x16x32_bf16 v[0:3], v[64:67], v[32:35], v[0:3]\n\t" \
  "ds_read_b64_tr_b16 v[72:73], v82 offset:8192\n\t" \
  "v_mfma_f32_16x16x32_bf16 v[4:7], v[68:71], v[32:35], v[4:7]\n\t" \
  "ds_read_b64_tr_b16 v[74:75], v83 offset:8192\n\t" \
  "ds_read_b128 v[32:35], v80 offset:32768\n\t" \
  "v_mfma_f32_16x16x32_bf16 v[8:11], v[64:67], v[36:39], v[8:11]\n\t" \
  "ds_read_b64_tr_b16 v[76:77], v84 offset:8192\n\t" \
  "v_mfma_f32_16x16x32_bf16 v[12:15], v[68:71], v[36:39], v[12:15]\n\t" \
  "ds_read_b64_tr_b16 v[78:79], v85 offset:8192\n\t" \
  "ds_read_b128 v[36:39], v80 offset:33792\n\t" \
  "s_add_u32 m0, %[lw], 98304\n\t" \
  "v_mfma_f32_16x16x32_bf16 v[16:19], v[64:67], v[40:43], v[16:19]\n\t" \
  "global_load_lds_dwordx4 v90, s[84:85] sc1\n\t" \
  "s_add_u32 s84, s84, s88\n\t" \
  "s_addc_u32 s85, s85, 0\n\t" \
  "v_mfma_f32_16x16x32_bf16 v[20:23], v[68:71], v[40:43], v[20:23]\n\t" \
  "ds_read_b128 v[40:43], v80 offset:34816\n\t" \
  "v_mfma_f32_16x16x32_bf16 v[24:27], v[64:67], v[44:47], v[24:27]\n\t" \
  "s_add_u32 m0, %[lw], 114688\n\t" \
  "v_mfma_f32_16x16x32_bf16 v[28:31], v[68:71], v[44:47], v[28:31]\n\t" \
  "ds_read_b128 v[44:47], v80 offset:35840\n\t" \
  "global_load_lds_dwordx4 v91, s[86:87]\n\t" \
  "s_add_u32 s86, s86, s89\n\t" \
  "s_addc_u32 s87, s87, 0\n\t" \
  "s_waitcnt lgkmcnt(3)\n\t" \
  "v_mfma_f32_16x16x32_bf16 v[0:3], v[72:75], v[48:51], v[0:3]\n\t" \
  "ds_read_b64_tr_b16 v[64:65], v82 offset:32768\n\t" \
  "v_mfma_f32_16x16x32_bf16 v[4:7], v[76:79], v[48:51], v[4:7]\n\t" \
  "ds_read_b64_tr_b16 v[66:67], v83 offset:32768\n\t" \
  "ds_read_b128 v[48:51], v80 offset:40960\n\t" \
  "v_mfma_f32_16x16x32_bf16 v[8:11], v[72:75], v[52:55], v[8:11]\n\t" \
  "ds_read_b64_tr_b16 v[68:69], v84 offset:32768\n\t" \
  "v_mfma_f32_16x16x32_bf16 v[12:15], v[76:79], v[52:55], v[12:15]\n\t" \
  "ds_read_b64_tr_b16 v[70:71], v85 offset:32768\n\t" \
  "ds_read_b128 v[52:55], v80 offset:41984\n\t" \
  "s_add_u32 m0, %[lw], 106496\n\t" \
  "v_mfma_f32_16x16x32_bf16 v[16:19], v[72:75], v[56:59], v[16:19]\n\t" \
  "global_load_lds_dwordx4 v90, s[84:85] sc1\n\t" \
  "s_add_u32 s84, s84, s88\n\t" \
  "s_addc_u32 s85, s85, 0\n\t" \
  "v_mfma_f32_16x16x32_bf16 v[20:23], v[76:79], v[56:59], v[20:23]\n\t" \
  "ds_read_b128 v[56:59], v80 offset:43008\n\t" \
  "v_mfma_f32_16x16x32_bf16 v[24:27], v[72:75], v[60:63], v[24:27]\n\t" \
  "s_add_u32 m0, %[lw], 122880\n\t" \
  "v_mfma_f32_16x16x32_bf16 v[28:31], v[76:79], v[60:63], v[28:31]\n\t" \
  "ds_read_b128 v[60:63], v80 offset:44032\n\t" \
  "global_load_lds_dwordx4 v91, s[86:87]\n\t" \
  "s_add_u32 s86, s86, s89\n\t" \
  "s_addc_u32 s87, s87, 0\n\t" \
  "s_sub_u32 s90, s90, 1\n\t" \
  "s_cselect_b32 s88, 0, s88\n\t" \
  "s_cselect_b32 s89, 0, s89\n\t" \
  "s_cselect_b32 s91, 16384, 0\n\t" \
  "s_sub_u32 s84, s84, s91\n\t" \
  "s_subb_u32 s85, s85, 0\n\t" \
  "s_lshl_b32 s91, s91, 2\n\t" \
  "s_sub_u32 s86, s86, s91\n\t" \
  "s_subb_u32 s87, s87, 0\n\t" \
  "s_waitcnt vmcnt(4)\n\t" \
  "s_barrier\n\t" \
  "s_waitcnt lgkmcnt(3)\n\t" \
  "v_mfma_f32_16x16x32_bf16 v[0:3], v[64:67], v[32:35], v[0:3]\n\t" \
  "ds_read_b64_tr_b16 v[72:73], v82 offset:40960\n\t" \
  "v_mfma_f32_16x16x32_bf16 v[4:7], v[68:71], v[32:35], v[4:7]\n\t" \
  "ds_read_b64_tr_b16 v[74:75], v83 offset:40960\n\t" \
  "ds_read_b128 v[32:35], v81 offset:0\n\t" \
  "v_mfma_f32_16x16x32_bf16 v[8:11], v[64:67], v[36:39], v[8:11]\n\t" \
  "ds_read_b64_tr_b16 v[76:77], v84 offset:40960\n\t" \
  "v_mfma_f32_16x16x32_bf16 v[12:15], v[68:71], v[36:39], v[12:15]\n\t" \
  "ds_read_b64_tr_b16 v[78:79], v85 offset:40960\n\t" \
  "ds_read_b128 v[36:39], v81 offset:1024\n\t" \
  "s_add_u32 m0, %[lw], 0\n\t" \
  "v_mfma_f32_16x16x32_bf16 v[16:19], v[64:67], v[40:43], v[16:19]\n\t" \
  "global_load_lds_dwordx4 v90, s[84:85] sc1\n\t" \
  "s_add_u32 s84, s84, s88\n\t" \
  "s_addc_u32 s85, s85, 0\n\t" \
  "v_mfma_f32_16x16x32_bf16 v[20:23], v[68:71], v[40:43], v[20:23]\n\t" \
  "ds_read_b128 v[40:43], v81 offset:2048\n\t" \
  "v_mfma_f32_16x16x32_bf16 v[24:27], v[64:67], v[44:47], v[24:27]\n\t" \
  "s_add_u32 m0, %[lw], 16384\n\t" \
  "v_mfma_f32_16x16x32_bf16 v[28:31], v[68:71], v[44:47], v[28:31]\n\t" \
  "ds_read_b128 v[44:47], v81 offset:3072\n\t" \
  "global_load_lds_dwordx4 v91, s[86:87]\n\t" \
  "s_add_u32 s86, s86, s89\n\t" \
  "s_addc_u32 s87, s87, 0\n\t" \
  "s_waitcnt lgkmcnt(3)\n\t" \
  "v_mfma_f32_16x16x32_bf16 v[0:3], v[72:75], v[48:51], v[0:3]\n\t" \
  "ds_read_b64_tr_b16 v[64:65], v86 offset:0\n\t" \
  "v_mfma_f32_16x16x32_bf16 v[4:7], v[76:79], v[48:51], v[4:7]\n\t" \
  "ds_read_b64_tr_b16 v[66:67], v87 offset:0\n\t" \
  "ds_read_b128 v[48:51], v81 offset:8192\n\t" \
  "v_mfma_f32_16x16x32_bf16 v[8:11], v[72:75], v[52:55], v[8:11]\n\t" \
  "ds_read_b64_tr_b16 v[68:69], v88 offset:0\n\t" \
  "v_mfma_f32_16x16x32_bf16 v[12:15], v[76:79], v[52:55], v[12:15]\n\t" \
  "ds_read_b64_tr_b16 v[70:71], v89 offset:0\n\t" \
  "ds_read_b128 v[52:55], v81 offset:9216\n\t" \
  "s_add_u32 m0, %[lw], 8192\n\t" \
  "v_mfma_f32_16x16x32_bf16 v[16:19], v[72:75], v[56:59], v[16:19]\n\t" \
  "global_load_lds_dwordx4 v90, s[84:85] sc1\n\t" \
  "s_add_u32 s84, s84, s88\n\t" \
  "s_addc_u32 s85, s85, 0\n\t" \
  "v_mfma_f32_16x16x32_bf16 v[20:23], v[76:79], v[56:59], v[20:23]\n\t" \
  "ds_read_b128 v[56:59], v81 offset:10240\n\t" \
  "v_mfma_f32_16x16x32_bf16 v[24:27], v[72:75], v[60:63], v[24:27]\n\t" \
  "s_add_u32 m0, %[lw], 24576\n\t" \
  "v_mfma_f32_16x16x32_bf16 v[28:31], v[76:79], v[60:63], v[28:31]\n\t" \
  "ds_read_b128 v[60:63], v81 offset:11264\n\t" \
  "global_load_lds_dwordx4 v91, s[86:87]\n\t" \
  "s_add_u32 s86, s86, s89\n\t" \
  "s_addc_u32 s87, s87, 0\n\t" \
  "s_waitcnt vmcnt(4)\n\t" \
  "s_barrier\n\t" \
  "s_waitcnt lgkmcnt(3)\n\t" \
  "v_mfma_f32_16x16x32_bf16 v[0:3], v[64:67], v[32:35], v[0:3]\n\t" \
  "ds_read_b64_tr_b16 v[72:73], v86 offset:8192\n\t" \
  "v_mfma_f32_16x16x32_bf16 v[4:7], v[68:71], v[32:35], v[4:7]\n\t" \
  "ds_read_b64_tr_b16 v[74:75], v87 offset:8192\n\t" \
  "ds_read_b128 v[32:35], v81 offset:32768\n\t" \
  "v_mfma_f32_16x16x32_bf16 v[8:11], v[64:67], v[36:39], v[8:11]\n\t" \
  "ds_read_b64_tr_b16 v[76:77], v88 offset:8192\n\t" \
  "v_mfma_f32_16x16x32_bf16 v[12:15], v[68:71], v[36:39], v[12:15]\n\t" \
  "ds_read_b64_tr_b16 v[78:79], v89 offset:8192\n\t" \
  "ds_read_b128 v[36:39], v81 offset:33792\n\t" \
  "s_add_u32 m0, %[lw], 32768\n\t" \
  "v_mfma_f32_16x16x32_bf16 v[16:19], v[64:67], v[40:43], v[16:19]\n\t" \
  "global_load_lds_dwordx4 v90, s[84:85] sc1\n\t" \
  "s_add_u32 s84, s84, s88\n\t" \
  "s_addc_u32 s85, s85, 0\n\t" \
  "v_mfma_f32_16x16x32_bf16 v[20:23], v[68:71], v[40:43], v[20:23]\n\t" \
  "ds_read_b128 v[40:43], v81 offset:34816\n\t" \
  "v_mfma_f32_16x16x32_bf16 v[24:27], v[64:67], v[44:47], v[24:27]\n\t" \
  "s_add_u32 m0, %[lw], 49152\n\t" \
  "v_mfma_f32_16x16x32_bf16 v[28:31], v[68:71], v[44:47], v[28:31]\n\t" \
  "ds_read_b128 v[44:47], v81 offset:35840\n\t" \
  "global_load_lds_dwordx4 v91, s[86:87]\n\t" \
  "s_add_u32 s86, s86, s89\n\t" \
  "s_addc_u32 s87, s87, 0\n\t" \
  "s_waitcnt lgkmcnt(3)\n\t" \
  "v_mfma_f32_16x16x32_bf16 v[0:3], v[72:75], v[48:51], v[0:3]\n\t" \
  "ds_read_b64_tr_b16 v[64:65], v86 offset:32768\n\t" \
  "v_mfma_f32_16x16x32_bf16 v[4:7], v[76:79], v[48:51], v[4:7]\n\t" \
  "ds_read_b64_tr_b16 v[66:67], v87 offset:32768\n\t" \
  "ds_read_b128 v[48:51], v81 offset:40960\n\t" \
  "v_mfma_f32_16x16x32_bf16 v[8:11], v[72:75], v[52:55], v[8:11]\n\t" \
  "ds_read_b64_tr_b16 v[68:69], v88 offset:32768\n\t" \
  "v_mfma_f32_16x16x32_bf16 v[12:15], v[76:79], v[52:55], v[12:15]\n\t" \
  "ds_read_b64_tr_b16 v[70:71], v89 offset:32768\n\t" \
  "ds_read_b128 v[52:55], v81 offset:41984\n\t" \
  "s_add_u32 m0, %[lw], 40960\n\t" \
  "v_mfma_f32_16x16x32_bf16 v[16:19], v[72:75], v[56:59], v[16:19]\n\t" \
  "global_load_lds_dwordx4 v90, s[84:85] sc1\n\t" \
  "s_add_u32 s84, s84, s88\n\t" \
  "s_addc_u32 s85, s85, 0\n\t" \
  "v_mfma_f32_16x16x32_bf16 v[20:23], v[76:79], v[56:59], v[20:23]\n\t" \
  "ds_read_b128 v[56:59], v81 offset:43008\n\t" \
  "v_mfma_f32_16x16x32_bf16 v[24:27], v[72:75], v[60:63], v[24:27]\n\t" \
  "s_add_u32 m0, %[lw], 57344\n\t" \
  "v_mfma_f32_16x16x32_bf16 v[28:31], v[76:79], v[60:63], v[28:31]\n\t" \
  "ds_read_b128 v[60:63], v81 offset:44032\n\t" \
  "global_load_lds_dwordx4 v91, s[86:87]\n\t" \
  "s_add_u32 s86, s86, s89\n\t" \
  "s_addc_u32 s87, s87, 0\n\t" \
  "s_waitcnt vmcnt(4)\n\t" \
  "s_barrier\n\t" \
  "s_waitcnt lgkmcnt(3)\n\t" \
  "v_mfma_f32_16x16x32_bf16 v[0:3], v[64:67], v[32:35], v[0:3]\n\t" \
  "ds_read_b64_tr_b16 v[72:73], v86 offset:40960\n\t" \
  "v_mfma_f32_16x16x32_bf16 v[4:7], v[68:71], v[32:35], v[4:7]\n\t" \
  "ds_read_b64_tr_b16 v[74:75], v87 offset:40960\n\t" \
  "ds_read_b128 v[32:35], v80 offset:0\n\t" \
  "v_mfma_f32_16x16x32_bf16 v[8:11], v[64:67], v[36:39], v[8:11]\n\t" \
  "ds_read_b64_tr_b16 v[76:77], v88 offset:40960\n\t" \
  "v_mfma_f32_16x16x32_bf16 v[12:15], v[68:71], v[36:39], v[12:15]\n\t" \
  "ds_read_b64_tr_b16 v[78:79], v89 offset:40960\n\t" \
  "ds_read_b128 v[36:39], v80 offset:1024\n\t" \
  "s_add_u32 m0, %[lw], 65536\n\t" \
  "v_mfma_f32_16x16x32_bf16 v[16:19], v[64:67], v[40:43], v[16:19]\n\t" \
  "global_load_lds_dwordx4 v90, s[84:85] sc1\n\t" \
  "s_add_u32 s84, s84, s88\n\t" \
  "s_addc_u32 s85, s85, 0\n\t" \
  "v_mfma_f32_16x16x32_bf16 v[20:23], v[68:71], v[40:43], v[20:23]\n\t" \
  "ds_read_b128 v[40:43], v80 offset:2048\n\t" \
  "v_mfma_f32_16x16x32_bf16 v[24:27], v[64:67], v[44:47], v[24:27]\n\t" \
  "s_add_u32 m0, %[lw], 81920\n\t" \
  "v_mfma_f32_16x16x32_bf16 v[28:31], v[68:71], v[44:47], v[28:31]\n\t" \
  "ds_read_b128 v[44:47], v80 offset:3072\n\t" \
  "global_load_lds_dwordx4 v91, s[86:87]\n\t" \
  "s_add_u32 s86, s86, s89\n\t" \
  "s_addc_u32 s87, s87, 0\n\t" \
  "s_waitcnt lgkmcnt(3)\n\t" \
  "v_mfma_f32_16x16x32_bf16 v[0:3], v[72:75], v[48:51], v[0:3]\n\t" \
  "ds_read_b64_tr_b16 v[64:65], v82 offset:0\n\t" \
  "v_mfma_f32_16x16x32_bf16 v[4:7], v[76:79], v[48:51], v[4:7]\n\t" \
  "ds_read_b64_tr_b16 v[66:67], v83 offset:0\n\t" \
  "ds_read_b128 v[48:51], v80 offset:8192\n\t" \
  "v_mfma_f32_16x16x32_bf16 v[8:11], v[72:75], v[52:55], v[8:11]\n\t" \
  "ds_read_b64_tr_b16 v[68:69], v84 offset:0\n\t" \
  "v_mfma_f32_16x16x32_bf16 v[12:15], v[76:79], v[52:55], v[12:15]\n\t" \
  "ds_read_b64_tr_b16 v[70:71], v85 offset:0\n\t" \
  "ds_read_b128 v[52:55], v80 offset:9216\n\t" \
  "s_add_u32 m0, %[lw], 73728\n\t" \
  "v_mfma_f32_16x16x32_bf16 v[16:19], v[72:75], v[56:59], v[16:19]\n\t" \
  "global_load_lds_dwordx4 v90, s[84:85] sc1\n\t" \
  "s_add_u32 s84, s84, s88\n\t" \
  "s_addc_u32 s85, s85, 0\n\t" \
  "v_mfma_f32_16x16x32_bf16 v[20:23], v[76:79], v[56:59], v[20:23]\n\t" \
  "ds_read_b128 v[56:59], v80 offset:10240\n\t" \
  "v_mfma_f32_16x16x32_bf16 v[24:27], v[72:75], v[60:63], v[24:27]\n\t" \
  "s_add_u32 m0, %[lw], 90112\n\t" \
  "v_mfma_f32_16x16x32_bf16 v[28:31], v[76:79], v[60:63], v[28:31]\n\t" \
  "ds_read_b128 v[60:63], v80 offset:11264\n\t" \
  "global_load_lds_dwordx4 v91, s[86:87]\n\t" \
  "s_add_u32 s86, s86, s89\n\t" \
  "s_addc_u32 s87, s87, 0\n\t" \
  "s_cmp_lg_u32 s88, 0\n\t" \
  "s_cbranch_scc1 .Lp1%=\n\t" \
  ".Le%=:\n\t" \
  "s_waitcnt vmcnt(0) lgkmcnt(0)\n\t" \
  "s_mov_b32 m0, s94\n\t" \
  "s_setprio 0\n\t" \
  "s_nop 7\n\t"
#define HSL 16
__device__ __forceinline__ void job_down(const Args& p, int rblk, int ct, char* smem, __amdgpu_buffer_rsrc_t rsy) {
  if (threadIdx.x < 64) {
    for (unsigned spins = 0;;) {
      asm volatile("" ::: "memory");
      if (__builtin_amdgcn_readfirstlane((int)ld_flag0(rsy, SY_CH + (rblk >> 1) * 128)) >= 27) break;
      __builtin_amdgcn_s_sleep(HSL);
      if (spin_fail(spins, (gu32*)(p.ws + WS_SY + SY_ABORT))) break;
    }
  }
  int tid = threadIdx.x;
  asm volatile("" : "+v"(tid));
  const int lane = tid & 63, r = lane & 15, q = lane >> 4;
  const int wid = __builtin_amdgcn_readfirstlane(tid >> 6), wr = wid >> 2, wc = wid & 3;
  typedef __attribute__((ext_vector_type(32))) float f32x32;
  typedef __attribute__((ext_vector_type(8))) unsigned u32x8;
  f32x32 acc;
  {
    const char* A = p.ws + WS_HF + h_off(p, rblk);
    const char* B = (const char*)(p.wd + ct * 128);
    const unsigned rb = tid >> 4, va = tid * 16, vb = ((rb & 4) * 4 + (rb >> 3) * 4 + (rb & 3)) * (DM * 2) + 16 * ((tid & 15) ^ swzB(rb & 15));
    u32x8 lb;
#pragma unroll
    for (int i = 0; i < 8; ++i)
      lb[i] = (i >> 2) * 65536 + 16384 + offB(8 * q + 4 * (i & 1) + (r >> 2), 2 * (wc * 2 + ((i >> 1) & 1)) + ((r & 3) >> 1)) + 8 * (r & 1);
    LBAR();
#pragma unroll
    for (int t = 0; t < 3; ++t) {
      glds2<true>(A + t * 32768, 16384, va, wid * 1024 + t * 32768);
      glds2(B + t * 64 * DM * 2, 32 * DM * 2, vb, wid * 1024 + t * 32768 + 16384);
    }
    WAIT_V(8);
    LBAR();
    asm volatile(DN_ASM
        : "={v[0:31]}"(acc)
        : "{v[80:81]}"((u32x2){wr * 4096u + lane * 16u, 65536u + wr * 4096u + lane * 16u}), "{v[82:89]}"(lb), "{v90}"(va), "{v91}"(vb),
          [pa] "s"(A + 3 * 32768), [pb] "s"(B + 3 * 64 * DM * 2), [lw] "s"(wid * 1024), [hf] "s"(wr)
        : CLB8(32, 33, 34, 35, 36, 37, 38, 39), CLB8(40, 41, 42, 43, 44, 45, 46, 47), CLB8(48, 49, 50, 51, 52, 53, 54, 55),
          CLB8(56, 57, 58, 59, 60, 61, 62, 63), CLB8(64, 65, 66, 67, 68, 69, 70, 71), CLB8(72, 73, 74, 75, 76, 77, 78, 79),
          "s84", "s85", "s86", "s87", "s88", "s89", "s90", "s91", "s94", "scc", "memory");
  }
  wait_s(p, rsy, rblk, ct * 4, 4, rblk < NFUSE);
  const int e0 = (rblk * 128 + wr * 64 + r) * DM + ct * 128 + wc * 32 + q * 8;
  SX sx[4];
#pragma unroll
  for (int m = 0; m < 4; ++m) sx[m] = get_sx(p, rblk, e0 + m * 16 * DM);
  const __amdgpu_buffer_rsrc_t ro = mkrsrc(p.ws + WS_G, S_LEN * DM * 2);
#pragma unroll
  for (int m = 0; m < 4; ++m) {
    float rr[8];
#pragma unroll
    for (int j = 0; j < 8; ++j) { float d = rbf(acc[8 * m + j]); rr[j] = d * __builtin_amdgcn_rcpf(1.0f + __expf(-d)); }
    put_g(p, ro, rblk, e0 + m * 16 * DM, widen32(cvtpk(rr[0], rr[1]), cvtpk(rr[2], rr[3]), cvtpk(rr[4], rr[5]), cvtpk(rr[6], rr[7])), sx[m]);
  }
  if (rblk >= NFUSE) publish_flags(rsy, SY_FG + (rblk * 8 + ct) * 128, 0, 1);
}

#define PR_ASM_0 \
  "s_cmp_lg_u32 %[kh], 0\n\t" \
  "s_cbranch_scc1 .Lk%=\n\t" \
  "s_mov_b32 s94, m0\n\t" \
  "s_mov_b64 s[92:93], %[x0]\n\t" \
  "s_mov_b32 s86, %[g0]\n\t" \
  "s_add_u32 s87, s86, %[t0]\n\t" \
  "s_sub_i32 s88, s86, 3\n\t" \
  "ds_read_b128 v[176:179], v208 offset:0\n\t" \
  "ds_read_b128 v[180:183], v208 offset:16384\n\t" \
  "ds_read_b128 v[184:187], v209 offset:0\n\t" \
  "ds_read_b128 v[188:191], v209 offset:16384\n\t" \
  "ds_read_b128 v[192:195], v210 offset:0\n\t" \
  "ds_read_b128 v[196:199], v210 offset:16384\n\t" \
  "ds_read_b128 v[200:203], v211 offset:0\n\t" \
  "ds_read_b128 v[204:207], v211 offset:16384\n\t" \
  "s_branch .Lfb00%=\n\t" \
  ".Lp0%=:\n\t" \
  "s_cmp_le_u32 s86, 5\n\t" \
  "s_cbranch_scc1 .Lf00%=\n\t" \
  ".Lfb00%=:\n\t" \
  "s_waitcnt lgkmcnt(6)\n\t" \
  "v_mfma_f32_16x16x32_bf16 v[128:131], v[0:3], v[176:179], 0\n\t" \
  "v_mfma_f32_16x16x32_bf16 v[132:135], v[4:7], v[176:179], 0\n\t" \
  "ds_read_b128 v[176:179], v208 offset:256\n\t" \
  "s_add_u32 m0, %[lw], 67584\n\t" \
  "v_mfma_f32_16x16x32_bf16 v[136:139], v[0:3], v[180:183], 0\n\t" \
  "v_mfma_f32_16x16x32_bf16 v[140:143], v[4:7], v[180:183], 0\n\t" \
  "ds_read_b128 v[180:183], v208 offset:16640\n\t" \
  "global_load_lds_dwordx4 v218, s[92:93] sc1\n\t" \
  "s_waitcnt lgkmcnt(6)\n\t" \
  "v_mfma_f32_16x16x32_bf16 v[128:131], v[8:11], v[184:187], v[128:131]\n\t" \
  "v_mfma_f32_16x16x32_bf16 v[132:135], v[12:15], v[184:187], v[132:135]\n\t" \
  "ds_read_b128 v[184:187], v209 offset:256\n\t" \
  "v_mfma_f32_16x16x32_bf16 v[136:139], v[8:11], v[188:191], v[136:139]\n\t" \
  "v_mfma_f32_16x16x32_bf16 v[140:143], v[12:15], v[188:191], v[140:143]\n\t" \
  "ds_read_b128 v[188:191], v209 offset:16640\n\t" \
  "ds_write_b128 v220, v[152:155] offset:0\n\t" \
  "ds_write_b128 v220, v[156:159] offset:1024\n\t" \
  "s_waitcnt lgkmcnt(8)\n\t" \
  "v_mfma_f32_16x16x32_bf16 v[128:131], v[16:19], v[192:195], v[128:131]\n\t" \
  "v_mfma_f32_16x16x32_bf16 v[132:135], v[20:23], v[192:195], v[132:135]\n\t" \
  "ds_read_b128 v[192:195], v210 offset:256\n\t" \
  "s_add_u32 m0, %[lw], 68608\n\t" \
  "v_mfma_f32_16x16x32_bf16 v[136:139], v[16:19], v[196:199], v[136:139]\n\t" \
  "v_mfma_f32_16x16x32_bf16 v[140:143], v[20:23], v[196:199], v[140:143]\n\t" \
  "ds_read_b128 v[196:199], v210 offset:16640\n\t" \
  "global_load_lds_dwordx4 v219, s[92:93] sc1\n\t" \
  "s_lshr_b32 s90, s87, 7\n\t" \
  "s_xor_b32 s90, s90, %[s0]\n\t" \
  "s_and_b32 s90, s90, 1\n\t" \
  "s_lshl1_add_u32 s90, s87, s90\n\t" \
  "s_lshl_b32 s90, s90, 13\n\t" \
  "s_cmp_lt_i32 s86, 6\n\t" \
  "s_cselect_b32 s90, 0x70000000, s90\n\t" \
  "s_waitcnt lgkmcnt(8)\n\t" \
  "v_mfma_f32_16x16x32_bf16 v[128:131], v[24:27], v[200:203], v[128:131]\n\t" \
  "v_mfma_f32_16x16x32_bf16 v[132:135], v[28:31], v[200:203], v[132:135]\n\t" \
  "ds_read_b128 v[200:203], v211 offset:256\n\t" \
  "v_mfma_f32_16x16x32_bf16 v[136:139], v[24:27], v[204:207], v[136:139]\n\t" \
  "v_mfma_f32_16x16x32_bf16 v[140:143], v[28:31], v[204:207], v[140:143]\n\t" \
  "ds_read_b128 v[204:207], v211 offset:16640\n\t" \
  "s_waitcnt lgkmcnt(4)\n\t" \
  "s_cmp_lt_u32 s86, 6\n\t" \
  "s_cbranch_scc1 .Lw00%=\n\t" \
  "s_waitcnt vmcnt(4)\n\t" \
  "s_barrier\n\t" \
  ".Lg00%=:\n\t" \
  "s_add_u32 s92, s92, 512\n\t" \
  "s_addc_u32 s93, s93, 0\n\t" \
  "v_mfma_f32_16x16x32_bf16 v[128:131], v[32:35], v[176:179], v[128:131]\n\t" \
  "v_mfma_f32_16x16x32_bf16 v[132:135], v[36:39], v[176:179], v[132:135]\n\t" \
  "ds_read_b128 v[176:179], v208 offset:32768\n\t" \
  "s_add_u32 m0, %[lw], 98304\n\t" \
  "v_mfma_f32_16x16x32_bf16 v[136:139], v[32:35], v[180:183], v[136:139]\n\t" \
  "v_mfma_f32_16x16x32_bf16 v[140:143], v[36:39], v[180:183], v[140:143]\n\t" \
  "ds_read_b128 v[180:183], v208 offset:49152\n\t" \
  "global_load_lds_dwordx4 v216, s[92:93] sc1\n\t" \
  "ds_read_b128 v[160:163], v221 offset:0\n\t" \
  "ds_read_b128 v[164:167], v221 offset:1024\n\t" \
  "v_mfma_f32_16x16x32_bf16 v[128:131], v[40:43], v[184:187], v[128:131]\n\t" \
  "v_mfma_f32_16x16x32_bf16 v[132:135], v[44:47], v[184:187], v[132:135]\n\t" \
  "ds_read_b128 v[184:187], v209 offset:32768\n\t" \
  "v_mfma_f32_16x16x32_bf16 v[136:139], v[40:43], v[188:191], v[136:139]\n\t" \
  "v_mfma_f32_16x16x32_bf16 v[140:143], v[44:47], v[188:191], v[140:143]\n\t" \
  "ds_read_b128 v[188:191], v209 offset:49152\n\t" \
  "s_waitcnt lgkmcnt(8)\n\t" \
  "v_mfma_f32_16x16x32_bf16 v[128:131], v[48:51], v[192:195], v[128:131]\n\t" \
  "v_mfma_f32_16x16x32_bf16 v[132:135], v[52:55], v[192:195], v[132:135]\n\t" \
  "ds_read_b128 v[192:195], v210 offset:32768\n\t" \
  "s_add_u32 m0, %[lw], 99328\n\t" \
  "v_mfma_f32_16x16x32_bf16 v[136:139], v[48:51], v[196:199], v[136:139]\n\t" \
  "v_mfma_f32_16x16x32_bf16 v[140:143], v[52:55], v[196:199], v[140:143]\n\t" \
  "ds_read_b128 v[196:199], v210 offset:49152\n\t" \
  "global_load_lds_dwordx4 v217, s[92:93] sc1\n\t" \
  "s_waitcnt lgkmcnt(4)\n\t" \
  "v_add_f32 v160, v160, v144\n\t" \
  "v_add_f32 v161, v161, v145\n\t" \
  "v_add_f32 v162, v162, v146\n\t" \
  "v_add_f32 v163, v163, v147\n\t" \
  "v_add_f32 v164, v164, v148\n\t" \
  "v_add_f32 v165, v165, v149\n\t" \
  "v_add_f32 v166, v166, v150\n\t" \
  "v_add_f32 v167, v167, v151\n\t" \
  "v_mfma_f32_16x16x32_bf16 v[128:131], v[56:59], v[200:203], v[128:131]\n\t" \
  "v_mfma_f32_16x16x32_bf16 v[132:135], v[60:63], v[200:203], v[132:135]\n\t" \
  "ds_read_b128 v[200:203], v211 offset:32768\n\t" \
  "v_mfma_f32_16x16x32_bf16 v[136:139], v[56:59], v[204:207], v[136:139]\n\t" \
  "v_mfma_f32_16x16x32_bf16 v[140:143], v[60:63], v[204:207], v[140:143]\n\t" \
  "ds_read_b128 v[204:207], v211 offset:49152\n\t" \
  "v_cvt_pk_bf16_f32 v168, v160, v161\n\t" \
  "v_cvt_pk_bf16_f32 v169, v162, v163\n\t" \
  "v_cvt_pk_bf16_f32 v170, v164, v165\n\t" \
  "v_cvt_pk_bf16_f32 v171, v166, v167\n\t" \
  "buffer_store_dwordx4 v[168:171], v172, %[rt], s90 offen sc1\n\t" \
  "v_mfma_f32_16x16x32_bf16 v[128:131], v[64:67], v[176:179], v[128:131]\n\t" \
  "v_mfma_f32_16x16x32_bf16 v[132:135], v[68:71], v[176:179], v[132:135]\n\t" \
  "ds_read_b128 v[176:179], v208 offset:33024\n\t" \
  "s_add_u32 m0, %[lw], 100352\n\t" \
  "v_mfma_f32_16x16x32_bf16 v[136:139], v[64:67], v[180:183], v[136:139]\n\t" \
  "v_mfma_f32_16x16x32_bf16 v[140:143], v[68:71], v[180:183], v[140:143]\n\t" \
  "ds_read_b128 v[180:183], v208 offset:49408\n\t" \
  "global_load_lds_dwordx4 v218, s[92:93] sc1\n\t" \
  "s_waitcnt lgkmcnt(6)\n\t" \
  "v_mfma_f32_16x16x32_bf16 v[128:131], v[72:75], v[184:187], v[128:131]\n\t" \
  "v_mfma_f32_16x16x32_bf16 v[132:135], v[76:79], v[184:187], v[132:135]\n\t" \
  "ds_read_b128 v[184:187], v209 offset:33024\n\t" \
  "v_mfma_f32_16x16x32_bf16 v[136:139], v[72:75], v[188:191], v[136:139]\n\t" \
  "v_mfma_f32_16x16x32_bf16 v[140:143], v[76:79], v[188:191], v[140:143]\n\t" \
  "ds_read_b128 v[188:191], v209 offset:49408\n\t" \
  "s_waitcnt lgkmcnt(6)\n\t" \
  "v_mfma_f32_16x16x32_bf16 v[128:131], v[80:83], v[192:195], v[128:131]\n\t" \
  "v_mfma_f32_16x16x32_bf16 v[132:135], v[84:87], v[192:195], v[132:135]\n\t" \
  "ds_read_b128 v[192:195], v210 offset:33024\n\t" \
  "s_add_u32 m0, %[lw], 101376\n\t" \
  "v_mfma_f32_16x16x32_bf16 v[136:139], v[80:83], v[196:199], v[136:139]\n\t" \
  "v_mfma_f32_16x16x32_bf16 v[140:143], v[84:87], v[196:199], v[140:143]\n\t" \
  "ds_read_b128 v[196:199], v210 offset:49408\n\t" \
  "global_load_lds_dwordx4 v219, s[92:93] sc1\n\t" \
  "s_waitcnt lgkmcnt(6)\n\t" \
  "v_mfma_f32_16x16x32_bf16 v[128:131], v[88:91], v[200:203], v[128:131]\n\t" \
  "v_mfma_f32_16x16x32_bf16 v[132:135], v[92:95], v[200:203], v[132:135]\n\t" \
  "ds_read_b128 v[200:203], v211 offset:33024\n\t" \
  "v_mfma_f32_16x16x32_bf16 v[136:139], v[88:91], v[204:207], v[136:139]\n\t" \
  "v_mfma_f32_16x16x32_bf16 v[140:143], v[92:95], v[204:207], v[140:143]\n\t" \
  "ds_read_b128 v[204:207], v211 offset:49408\n\t" \
  "s_cmp_lt_u32 s86, 8\n\t" \
  "s_cbranch_scc1 .Lw10%=\n\t" \
  "s_waitcnt vmcnt(5)\n\t" \
  "s_barrier\n\t" \
  ".Lg10%=:\n\t" \
  "s_add_u32 s92, s92, 65024\n\t" \
  "s_addc_u32 s93, s93, 0\n\t" \
  "s_waitcnt lgkmcnt(6)\n\t" \
  "v_mfma_f32_16x16x32_bf16 v[128:131], v[96:99], v[176:179], v[128:131]\n\t" \
  "v_mfma_f32_16x16x32_bf16 v[132:135], v[100:103], v[176:179], v[132:135]\n\t" \
  "ds_read_b128 v[176:179], v212 offset:0\n\t" \
  "s_add_u32 m0, %[lw], 0\n\t" \
  "v_mfma_f32_16x16x32_bf16 v[136:139], v[96:99], v[180:183], v[136:139]\n\t" \
  "v_mfma_f32_16x16x32_bf16 v[140:143], v[100:103], v[180:183], v[140:143]\n\t" \
  "ds_read_b128 v[180:183], v212 offset:16384\n\t" \
  "global_load_lds_dwordx4 v216, s[92:93] sc1\n\t" \
  "s_waitcnt lgkmcnt(6)\n\t" \
  "v_mfma_f32_16x16x32_bf16 v[128:131], v[104:107], v[184:187], v[128:131]\n\t" \
  "v_mfma_f32_16x16x32_bf16 v[132:135], v[108:111], v[184:187], v[132:135]\n\t" \
  "ds_read_b128 v[184:187], v213 offset:0\n\t" \
  "v_mfma_f32_16x16x32_bf16 v[136:139], v[104:107], v[188:191], v[136:139]\n\t" \
  "v_mfma_f32_16x16x32_bf16 v[140:143], v[108:111], v[188:191], v[140:143]\n\t" \
  "ds_read_b128 v[188:191], v213 offset:16384\n\t" \
  "s_waitcnt lgkmcnt(6)\n\t" \
  "v_mfma_f32_16x16x32_bf16 v[128:131], v[112:115], v[192:195], v[128:131]\n\t" \
  "v_mfma_f32_16x16x32_bf16 v[132:135], v[116:119], v[192:195], v[132:135]\n\t" \
  "ds_read_b128 v[192:195], v214 offset:0\n\t" \
  "s_add_u32 m0, %[lw], 1024\n\t" \
  "v_mfma_f32_16x16x32_bf16 v[136:139], v[112:115], v[196:199], v[136:139]\n\t" \
  "v_mfma_f32_16x16x32_bf16 v[140:143], v[116:119], v[196:199], v[140:143]\n\t" \
  "ds_read_b128 v[196:199], v214 offset:16384\n\t" \
  "global_load_lds_dwordx4 v217, s[92:93] sc1\n\t" \
  "s_waitcnt lgkmcnt(6)\n\t" \
  "v_mfma_f32_16x16x32_bf16 v[128:131], v[120:123], v[200:203], v[128:131]\n\t" \
  "v_mfma_f32_16x16x32_bf16 v[132:135], v[124:127], v[200:203], v[132:135]\n\t" \
  "ds_read_b128 v[200:203], v215 offset:0\n\t" \
  "v_mfma_f32_16x16x32_bf16 v[136:139], v[120:123], v[204:207], v[136:139]\n\t" \
  "v_mfma_f32_16x16x32_bf16 v[140:143], v[124:127], v[204:207], v[140:143]\n\t" \
  "ds_read_b128 v[204:207], v215 offset:16384\n\t" \
  "s_add_u32 s86, s86, 1\n\t" \
  "s_add_u32 s87, s87, 1\n\t" \
  "s_cmp_le_u32 s86, 5\n\t" \
  "s_cbranch_scc1 .Lf10%=\n\t" \
  ".Lfb10%=:\n\t" \
  "s_waitcnt lgkmcnt(6)\n\t" \
  "v_mfma_f32_16x16x32_bf16 v[144:147], v[0:3], v[176:179], 0\n\t" \
  "v_mfma_f32_16x16x32_bf16 v[148:151], v[4:7], v[176:179], 0\n\t" \
  "ds_read_b128 v[176:179], v212 offset:256\n\t" \
  "s_add_u32 m0, %[lw], 2048\n\t" \
  "v_mfma_f32_16x16x32_bf16 v[152:155], v[0:3], v[180:183], 0\n\t" \
  "v_mfma_f32_16x16x32_bf16 v[156:159], v[4:7], v[180:183], 0\n\t" \
  "ds_read_b128 v[180:183], v212 offset:16640\n\t" \
  "global_load_lds_dwordx4 v218, s[92:93] sc1\n\t" \
  "s_waitcnt lgkmcnt(6)\n\t" \
  "v_mfma_f32_16x16x32_bf16 v[144:147], v[8:11], v[184:187], v[144:147]\n\t" \
  "v_mfma_f32_16x16x32_bf16 v[148:151], v[12:15], v[184:187], v[148:151]\n\t" \
  "ds_read_b128 v[184:187], v213 offset:256\n\t" \
  "v_mfma_f32_16x16x32_bf16 v[152:155], v[8:11], v[188:191], v[152:155]\n\t" \
  "v_mfma_f32_16x16x32_bf16 v[156:159], v[12:15], v[188:191], v[156:159]\n\t" \
  "ds_read_b128 v[188:191], v213 offset:16640\n\t" \
  "ds_write_b128 v220, v[136:139] offset:0\n\t" \
  "ds_write_b128 v220, v[140:143] offset:1024\n\t" \
  "s_waitcnt lgkmcnt(8)\n\t" \
  "v_mfma_f32_16x16x32_bf16 v[144:147], v[16:19], v[192:195], v[144:147]\n\t" \
  "v_mfma_f32_16x16x32_bf16 v[148:151], v[20:23], v[192:195], v[148:151]\n\t" \
  "ds_read_b128 v[192:195], v214 offset:256\n\t" \
  "s_add_u32 m0, %[lw], 3072\n\t" \
  "v_mfma_f32_16x16x32_bf16 v[152:155], v[16:19], v[196:199], v[152:155]\n\t" \
  "v_mfma_f32_16x16x32_bf16 v[156:159], v[20:23], v[196:199], v[156:159]\n\t" \
  "ds_read_b128 v[196:199], v214 offset:16640\n\t" \
  "global_load_lds_dwordx4 v219, s[92:93] sc1\n\t" \
  "s_lshr_b32 s90, s87, 7\n\t" \
  "s_xor_b32 s90, s90, %[s0]\n\t" \
  "s_and_b32 s90, s90, 1\n\t" \
  "s_lshl1_add_u32 s90, s87, s90\n\t" \
  "s_lshl_b32 s90, s90, 13\n\t" \
  "s_cmp_lt_i32 s86, 6\n\t" \
  "s_cselect_b32 s90, 0x70000000, s90\n\t" \
  "s_waitcnt lgkmcnt(8)\n\t" \
  "v_mfma_f32_16x16x32_bf16 v[144:147], v[24:27], v[200:203], v[144:147]\n\t" \
  "v_mfma_f32_16x16x32_bf16 v[148:151], v[28:31], v[200:203], v[148:151]\n\t" \
  "ds_read_b128 v[200:203], v215 offset:256\n\t" \
  "v_mfma_f32_16x16x32_bf16 v[152:155], v[24:27], v[204:207], v[152:155]\n\t" \
  "v_mfma_f32_16x16x32_bf16 v[156:159], v[28:31], v[204:207], v[156:159]\n\t" \
  "ds_read_b128 v[204:207], v215 offset:16640\n\t" \
  "s_waitcnt lgkmcnt(4)\n\t" \
  "s_cmp_lt_u32 s86, 6\n\t" \
  "s_cbranch_scc1 .Lw20%=\n\t" \
  "s_waitcnt vmcnt(4)\n\t" \
  "s_barrier\n\t" \
  ".Lg20%=:\n\t" \
  "s_add_u32 s92, s92, 512\n\t" \
  "s_addc_u32 s93, s93, 0\n\t" \
  "v_mfma_f32_16x16x32_bf16 v[144:147], v[32:35], v[176:179], v[144:147]\n\t" \
  "v_mfma_f32_16x16x32_bf16 v[148:151], v[36:39], v[176:179], v[148:151]\n\t" \
  "ds_read_b128 v[176:179], v212 offset:32768\n\t" \
  "s_add_u32 m0, %[lw], 32768\n\t" \
  "v_mfma_f32_16x16x32_bf16 v[152:155], v[32:35], v[180:183], v[152:155]\n\t" \
  "v_mfma_f32_16x16x32_bf16 v[156:159], v[36:39], v[180:183], v[156:159]\n\t" \
  "ds_read_b128 v[180:183], v212 offset:49152\n\t" \
  "global_load_lds_dwordx4 v216, s[92:93] sc1\n\t" \
  "ds_read_b128 v[160:163], v221 offset:0\n\t" \
  "ds_read_b128 v[164:167], v221 offset:1024\n\t" \
  "v_mfma_f32_16x16x32_bf16 v[144:147], v[40:43], v[184:187], v[144:147]\n\t" \
  "v_mfma_f32_16x16x32_bf16 v[148:151], v[44:47], v[184:187], v[148:151]\n\t" \
  "ds_read_b128 v[184:187], v213 offset:32768\n\t" \
  "v_mfma_f32_16x16x32_bf16 v[152:155], v[40:43], v[188:191], v[152:155]\n\t" \
  "v_mfma_f32_16x16x32_bf16 v[156:159], v[44:47], v[188:191], v[156:159]\n\t" \
  "ds_read_b128 v[188:191], v213 offset:49152\n\t" \
  "s_waitcnt lgkmcnt(8)\n\t" \
  "v_mfma_f32_16x16x32_bf16 v[144:147], v[48:51], v[192:195], v[144:147]\n\t" \
  "v_mfma_f32_16x16x32_bf16 v[148:151], v[52:55], v[192:195], v[148:151]\n\t" \
  "ds_read_b128 v[192:195], v214 offset:32768\n\t" \
  "s_add_u32 m0, %[lw], 33792\n\t" \
  "v_mfma_f32_16x16x32_bf16 v[152:155], v[48:51], v[196:199], v[152:155]\n\t" \
  "v_mfma_f32_16x16x32_bf16 v[156:159], v[52:55], v[196:199], v[156:159]\n\t" \
  "ds_read_b128 v[196:199], v214 offset:49152\n\t" \
  "global_load_lds_dwordx4 v217, s[92:93] sc1\n\t" \
  "s_waitcnt lgkmcnt(4)\n\t" \
  "v_add_f32 v160, v160, v128\n\t" \
  "v_add_f32 v161, v161, v129\n\t" \
  "v_add_f32 v162, v162, v130\n\t" \
  "v_add_f32 v163, v163, v131\n\t" \
  "v_add_f32 v164, v164, v132\n\t" \
  "v_add_f32 v165, v165, v133\n\t" \
  "v_add_f32 v166, v166, v134\n\t" \
  "v_add_f32 v167, v167, v135\n\t" \
  "v_mfma_f32_16x16x32_bf16 v[144:147], v[56:59], v[200:203], v[144:147]\n\t" \
  "v_mfma_f32_16x16x32_bf16 v[148:151], v[60:63], v[200:203], v[148:151]\n\t" \
  "ds_read_b128 v[200:203], v215 offset:32768\n\t" \
  "v_mfma_f32_16x16x32_bf16 v[152:155], v[56:59], v[204:207], v[152:155]\n\t" \
  "v_mfma_f32_16x16x32_bf16 v[156:159], v[60:63], v[204:207], v[156:159]\n\t" \
  "ds_read_b128 v[204:207], v215 offset:49152\n\t" \
  "v_cvt_pk_bf16_f32 v168, v160, v161\n\t" \
  "v_cvt_pk_bf16_f32 v169, v162, v163\n\t" \
  "v_cvt_pk_bf16_f32 v170, v164, v165\n\t" \
  "v_cvt_pk_bf16_f32 v171, v166, v167\n\t" \
  "buffer_store_dwordx4 v[168:171], v172, %[rt], s90 offen sc1\n\t" \
  "v_mfma_f32_16x16x32_bf16 v[144:147], v[64:67], v[176:179], v[144:147]\n\t" \
  "v_mfma_f32_16x16x32_bf16 v[148:151], v[68:71], v[176:179], v[148:151]\n\t" \
  "ds_read_b128 v[176:179], v212 offset:33024\n\t" \
  "s_add_u32 m0, %[lw], 34816\n\t" \
  "v_mfma_f32_16x16x32_bf16 v[152:155], v[64:67], v[180:183], v[152:155]\n\t" \
  "v_mfma_f32_16x16x32_bf16 v[156:159], v[68:71], v[180:183], v[156:159]\n\t" \
  "ds_read_b128 v[180:183], v212 offset:49408\n\t" \
  "global_load_lds_dwordx4 v218, s[92:93] sc1\n\t" \
  "s_waitcnt lgkmcnt(6)\n\t" \
  "v_mfma_f32_16x16x32_bf16 v[144:147], v[72:75], v[184:187], v[144:147]\n\t" \
  "v_mfma_f32_16x16x32_bf16 v[148:151], v[76:79], v[184:187], v[148:151]\n\t" \
  "ds_read_b128 v[184:187], v213 offset:33024\n\t" \
  "v_mfma_f32_16x16x32_bf16 v[152:155], v[72:75], v[188:191], v[152:155]\n\t" \
  "v_mfma_f32_16x16x32_bf16 v[156:159], v[76:79], v[188:191], v[156:159]\n\t" \
  "ds_read_b128 v[188:191], v213 offset:49408\n\t" \
  "s_waitcnt lgkmcnt(6)\n\t" \
  "v_mfma_f32_16x16x32_bf16 v[144:147], v[80:83], v[192:195], v[144:147]\n\t" \
  "v_mfma_f32_16x16x32_bf16 v[148:151], v[84:87], v[192:195], v[148:151]\n\t" \
  "ds_read_b128 v[192:195], v214 offset:33024\n\t" \
  "s_add_u32 m0, %[lw], 35840\n\t" \
  "v_mfma_f32_16x16x32_bf16 v[152:155], v[80:83], v[196:199], v[152:155]\n\t" \
  "v_mfma_f32_16x16x32_bf16 v[156:159], v[84:87], v[196:199], v[156:159]\n\t" \
  "ds_read_b128 v[196:199], v214 offset:49408\n\t" \
  "global_load_lds_dwordx4 v219, s[92:93] sc1\n\t" \
  "s_waitcnt lgkmcnt(6)\n\t" \
  "v_mfma_f32_16x16x32_bf16 v[144:147], v[88:91], v[200:203], v[144:147]\n\t" \
  "v_mfma_f32_16x16x32_bf16 v[148:151], v[92:95], v[200:203], v[148:151]\n\t" \
  "ds_read_b128 v[200:203], v215 offset:33024\n\t" \
  "v_mfma_f32_16x16x32_bf16 v[152:155], v[88:91], v[204:207], v[152:155]\n\t" \
  "v_mfma_f32_16x16x32_bf16 v[156:159], v[92:95], v[204:207], v[156:159]\n\t" \
  "ds_read_b128 v[204:207], v215 offset:49408\n\t" \
  "s_cmp_lt_u32 s86, 8\n\t" \
  "s_cbranch_scc1 .Lw30%=\n\t" \
  "s_waitcnt vmcnt(5)\n\t" \
  "s_barrier\n\t" \
  ".Lg30%=:\n\t" \
  "s_add_u32 s92, s92, 65024\n\t" \
  "s_addc_u32 s93, s93, 0\n\t" \
  "s_waitcnt lgkmcnt(6)\n\t" \
  "v_mfma_f32_16x16x32_bf16 v[144:147], v[96:99], v[176:179], v[144:147]\n\t" \
  "v_mfma_f32_16x16x32_bf16 v[148:151], v[100:103], v[176:179], v[148:151]\n\t" \
  "ds_read_b128 v[176:179], v208 offset:0\n\t" \
  "s_add_u32 m0, %[lw], 65536\n\t" \
  "v_mfma_f32_16x16x32_bf16 v[152:155], v[96:99], v[180:183], v[152:155]\n\t" \
  "v_mfma_f32_16x16x32_bf16 v[156:159], v[100:103], v[180:183], v[156:159]\n\t" \
  "ds_read_b128 v[180:183], v208 offset:16384\n\t" \
  "global_load_lds_dwordx4 v216, s[92:93] sc1\n\t" \
  "s_waitcnt lgkmcnt(6)\n\t" \
  "v_mfma_f32_16x16x32_bf16 v[144:147], v[104:107], v[184:187], v[144:147]\n\t" \
  "v_mfma_f32_16x16x32_bf16 v[148:151], v[108:111], v[184:187], v[148:151]\n\t" \
  "ds_read_b128 v[184:187], v209 offset:0\n\t" \
  "v_mfma_f32_16x16x32_bf16 v[152:155], v[104:107], v[188:191], v[152:155]\n\t" \
  "v_mfma_f32_16x16x32_bf16 v[156:159], v[108:111], v[188:191], v[156:159]\n\t" \
  "ds_read_b128 v[188:191], v209 offset:16384\n\t" \
  "s_waitcnt lgkmcnt(6)\n\t" \
  "v_mfma_f32_16x16x32_bf16 v[144:147], v[112:115], v[192:195], v[144:147]\n\t" \
  "v_mfma_f32_16x16x32_bf16 v[148:151], v[116:119], v[192:195], v[148:151]\n\t" \
  "ds_read_b128 v[192:195], v210 offset:0\n\t" \
  "s_add_u32 m0, %[lw], 66560\n\t" \
  "v_mfma_f32_16x16x32_bf16 v[152:155], v[112:115], v[196:199], v[152:155]\n\t" \
  "v_mfma_f32_16x16x32_bf16 v[156:159], v[116:119], v[196:199], v[156:159]\n\t" \
  "ds_read_b128 v[196:199], v210 offset:16384\n\t" \
  "global_load_lds_dwordx4 v217, s[92:93] sc1\n\t" \
  "s_waitcnt lgkmcnt(6)\n\t" \
  "v_mfma_f32_16x16x32_bf16 v[144:147], v[120:123], v[200:203], v[144:147]\n\t" \
  "v_mfma_f32_16x16x32_bf16 v[148:151], v[124:127], v[200:203], v[148:151]\n\t" \
  "ds_read_b128 v[200:203], v211 offset:0\n\t" \
  "v_mfma_f32_16x16x32_bf16 v[152:155], v[120:123], v[204:207], v[152:155]\n\t" \
  "v_mfma_f32_16x16x32_bf16 v[156:159], v[124:127], v[204:207], v[156:159]\n\t" \
  "ds_read_b128 v[204:207], v211 offset:16384\n\t" \
  "s_add_u32 s86, s86, 1\n\t" \
  "s_add_u32 s87, s87, 1\n\t" \
  "s_cmp_lt_u32 s86, %[end]\n\t" \
  "s_cbranch_scc1 .Lp0%=\n\t" \
  "s_branch .Lx0%=\n\t" \
  ".Lf00%=:\n\t" \
  "s_nop 7\n\t" \
  "ds_write_b128 v220, v[152:155] offset:0\n\t" \
  "ds_write_b128 v220, v[156:159] offset:1024\n\t" \
  "s_lshr_b32 s90, s87, 7\n\t" \
  "s_xor_b32 s90, s90, %[s0]\n\t" \
  "s_and_b32 s90, s90, 1\n\t" \
  "s_lshl1_add_u32 s90, s87, s90\n\t" \
  "s_lshl_b32 s90, s90, 13\n\t" \
  "s_cmp_lt_i32 s86, 1\n\t" \
  "s_cselect_b32 s90, 0x70000000, s90\n\t" \
  "s_waitcnt lgkmcnt(0)\n\t" \
  "s_barrier\n\t" \
  "ds_read_b128 v[160:163], v221 offset:0\n\t" \
  "ds_read_b128 v[164:167], v221 offset:1024\n\t" \
  "s_waitcnt lgkmcnt(0)\n\t" \
  "v_add_f32 v160, v160, v144\n\t" \
  "v_add_f32 v161, v161, v145\n\t" \
  "v_add_f32 v162, v162, v146\n\t" \
  "v_add_f32 v163, v163, v147\n\t" \
  "v_add_f32 v164, v164, v148\n\t" \
  "v_add_f32 v165, v165, v149\n\t" \
  "v_add_f32 v166, v166, v150\n\t" \
  "v_add_f32 v167, v167, v151\n\t" \
  "v_cvt_pk_bf16_f32 v168, v160, v161\n\t" \
  "v_cvt_pk_bf16_f32 v169, v162, v163\n\t" \
  "v_cvt_pk_bf16_f32 v170, v164, v165\n\t" \
  "v_cvt_pk_bf16_f32 v171, v166, v167\n\t" \
  "buffer_store_dwordx4 v[168:171], v172, %[rt], s90 offen sc1\n\t" \
  "s_branch .Lfb00%=\n\t" \
  ".Lf10%=:\n\t" \
  "s_nop 7\n\t" \
  "ds_write_b128 v220, v[136:139] offset:0\n\t" \
  "ds_write_b128 v220, v[140:143] offset:1024\n\t" \
  "s_lshr_b32 s90, s87, 7\n\t" \
  "s_xor_b32 s90, s90, %[s0]\n\t" \
  "s_and_b32 s90, s90, 1\n\t" \
  "s_lshl1_add_u32 s90, s87, s90\n\t" \
  "s_lshl_b32 s90, s90, 13\n\t" \
  "s_cmp_lt_i32 s86, 1\n\t" \
  "s_cselect_b32 s90, 0x70000000, s90\n\t" \
  "s_waitcnt lgkmcnt(0)\n\t" \
  "s_barrier\n\t" \
  "ds_read_b128 v[160:163], v221 offset:0\n\t" \
  "ds_read_b128 v[164:167], v221 offset:1024\n\t" \
  "s_waitcnt lgkmcnt(0)\n\t" \
  "v_add_f32 v160, v160, v128\n\t" \
  "v_add_f32 v161, v161, v129\n\t" \
  "v_add_f32 v162, v162, v130\n\t" \
  "v_add_f32 v163, v163, v131\n\t" \
  "v_add_f32 v164, v164, v132\n\t" \
  "v_add_f32 v165, v165, v133\n\t" \
  "v_add_f32 v166, v166, v134\n\t" \
  "v_add_f32 v167, v167, v135\n\t" \
  "v_cvt_pk_bf16_f32 v168, v160, v161\n\t" \
  "v_cvt_pk_bf16_f32 v169, v162, v163\n\t" \
  "v_cvt_pk_bf16_f32 v170, v164, v165\n\t" \
  "v_cvt_pk_bf16_f32 v171, v166, v167\n\t" \
  "buffer_store_dwordx4 v[168:171], v172, %[rt], s90 offen sc1\n\t" \
  "s_branch .Lfb10%=\n\t" \
  ".Lw00%=:\n\t" \
  "s_waitcnt vmcnt(2)\n\t" \
  "s_barrier\n\t" \
  "s_branch .Lg00%=\n\t" \
  ".Lw10%=:\n\t" \
  "s_waitcnt vmcnt(2)\n\t" \
  "s_barrier\n\t" \
  "s_branch .Lg10%=\n\t" \
  ".Lw20%=:\n\t" \
  "s_waitcnt vmcnt(2)\n\t" \
  "s_barrier\n\t" \
  "s_branch .Lg20%=\n\t" \
  ".Lw30%=:\n\t" \
  "s_waitcnt vmcnt(2)\n\t" \
  "s_barrier\n\t" \
  "s_branch .Lg30%=\n\t" \
  ".Lx0%=:\n\t" \
  "s_waitcnt lgkmcnt(0)\n\t" \
  "s_setprio 0\n\t" \
  "s_mov_b32 m0, s94\n\t" \
  "s_branch .Le%=\n\t" \
  ".Lk%=:\n\t" \
  "s_mov_b32 s94, m0\n\t" \
  "s_mov_b64 s[92:93], %[x0]\n\t" \
  "s_mov_b32 s86, %[g0]\n\t" \
  "s_add_u32 s87, s86, %[t0]\n\t" \
  "s_sub_i32 s88, s86, 3\n\t" \
  "ds_read_b128 v[176:179], v208 offset:0\n\t" \
  "ds_read_b128 v[180:183], v208 offset:16384\n\t" \
  "ds_read_b128 v[184:187], v209 offset:0\n\t" \
  "ds_read_b128 v[188:191], v209 offset:16384\n\t" \
  "ds_read_b128 v[192:195], v210 offset:0\n\t" \
  "ds_read_b128 v[196:199], v210 offset:16384\n\t" \
  "ds_read_b128 v[200:203], v211 offset:0\n\t" \
  "ds_read_b128 v[204:207], v211 offset:16384\n\t" \
  "s_branch .Lfb01%=\n\t" \
  ".Lp1%=:\n\t" \
  "s_cmp_le_u32 s86, 5\n\t" \
  "s_cbranch_scc1 .Lf01%=\n\t" \
  ".Lfb01%=:\n\t" \
  "s_waitcnt lgkmcnt(6)\n\t" \
  "v_mfma_f32_16x16x32_bf16 v[128:131], v[0:3], v[176:179], 0\n\t" \
  "v_mfma_f32_16x16x32_bf16 v[132:135], v[4:7], v[176:179], 0\n\t" \
  "ds_read_b128 v[176:179], v208 offset:256\n\t" \
  "v_mfma_f32_16x16x32_bf16 v[136:139], v[0:3], v[180:183], 0\n\t" \
  "v_mfma_f32_16x16x32_bf16 v[140:143], v[4:7], v[180:183], 0\n\t" \
  "ds_read_b128 v[180:183], v208 offset:16640\n\t" \
  "s_waitcnt lgkmcnt(6)\n\t" \
  "v_mfma_f32_16x16x32_bf16 v[128:131], v[8:11], v[184:187], v[128:131]\n\t" \
  "v_mfma_f32_16x16x32_bf16 v[132:135], v[12:15], v[184:187], v[132:135]\n\t" \
  "ds_read_b128 v[184:187], v209 offset:256\n\t" \
  "s_add_u32 m0, %[lw], 67584\n\t" \
  "v_mfma_f32_16x16x32_bf16 v[136:139], v[8:11], v[188:191], v[136:139]\n\t" \
  "v_mfma_f32_16x16x32_bf16 v[140:143], v[12:15], v[188:191], v[140:143]\n\t" \
  "ds_read_b128 v[188:191], v209 offset:16640\n\t" \
  "global_load_lds_dwordx4 v218, s[92:93] sc1\n\t" \
  "ds_write_b128 v220, v[152:155] offset:0\n\t" \
  "ds_write_b128 v220, v[156:159] offset:1024\n\t" \
  "s_waitcnt lgkmcnt(8)\n\t" \
  "v_mfma_f32_16x16x32_bf16 v[128:131], v[16:19], v[192:195], v[128:131]\n\t" \
  "v_mfma_f32_16x16x32_bf16 v[132:135], v[20:23], v[192:195], v[132:135]\n\t" \
  "ds_read_b128 v[192:195], v210 offset:256\n\t" \
  "v_mfma_f32_16x16x32_bf16 v[136:139], v[16:19], v[196:199], v[136:139]\n\t" \
  "v_mfma_f32_16x16x32_bf16 v[140:143], v[20:23], v[196:199], v[140:143]\n\t" \
  "ds_read_b128 v[196:199], v210 offset:16640\n\t" \
  "s_lshr_b32 s90, s87, 7\n\t" \
  "s_xor_b32 s90, s90, %[s0]\n\t" \
  "s_and_b32 s90, s90, 1\n\t" \
  "s_lshl1_add_u32 s90, s87, s90\n\t" \
  "s_lshl_b32 s90, s90, 13\n\t" \
  "s_cmp_lt_i32 s86, 6\n\t" \
  "s_cselect_b32 s90, 0x70000000, s90\n\t" \
  "s_waitcnt lgkmcnt(8)\n\t" \
  "v_mfma_f32_16x16x32_bf16 v[128:131], v[24:27], v[200:203], v[128:131]\n\t" \
  "v_mfma_f32_16x16x32_bf16 v[132:135], v[28:31], v[200:203], v[132:135]\n\t" \
  "ds_read_b128 v[200:203], v211 offset:256\n\t" \
  "s_add_u32 m0, %[lw], 68608\n\t" \
  "v_mfma_f32_16x16x32_bf16 v[136:139], v[24:27], v[204:207], v[136:139]\n\t" \
  "v_mfma_f32_16x16x32_bf16 v[140:143], v[28:31], v[204:207], v[140:143]\n\t" \
  "ds_read_b128 v[204:207], v211 offset:16640\n\t" \
  "global_load_lds_dwordx4 v219, s[92:93] sc1\n\t" \
  "s_waitcnt lgkmcnt(4)\n\t" \
  "s_cmp_lt_u32 s86, 6\n\t" \
  "s_cbranch_scc1 .Lw01%=\n\t" \
  "s_waitcnt vmcnt(4)\n\t" \
  "s_barrier\n\t" \
  ".Lg01%=:\n\t" \
  "s_add_u32 s92, s92, 512\n\t" \
  "s_addc_u32 s93, s93, 0\n\t" \
  "v_mfma_f32_16x16x32_bf16 v[128:131], v[32:35], v[176:179], v[128:131]\n\t" \
  "v_mfma_f32_16x16x32_bf16 v[132:135], v[36:39], v[176:179], v[132:135]\n\t" \
  "ds_read_b128 v[176:179], v208 offset:32768\n\t" \
  "v_mfma_f32_16x16x32_bf16 v[136:139], v[32:35], v[180:183], v[136:139]\n\t" \
  "v_mfma_f32_16x16x32_bf16 v[140:143], v[36:39], v[180:183], v[140:143]\n\t" \
  "ds_read_b128 v[180:183], v208 offset:49152\n\t" \
  "ds_read_b128 v[160:163], v221 offset:0\n\t" \
  "ds_read_b128 v[164:167], v221 offset:1024\n\t" \
  "s_sub_i32 s91, s86, 2\n\t" \
  "s_max_i32 s88, s88, s91\n\t" \
  "v_mov_b32 v173, s88\n\t" \
  "buffer_store_dword v173, v174, %[rs], 0 offen sc1\n\t" \
  "v_mfma_f32_16x16x32_bf16 v[128:131], v[40:43], v[184:187], v[128:131]\n\t" \
  "v_mfma_f32_16x16x32_bf16 v[132:135], v[44:47], v[184:187], v[132:135]\n\t" \
  "ds_read_b128 v[184:187], v209 offset:32768\n\t" \
  "s_add_u32 m0, %[lw], 98304\n\t" \
  "v_mfma_f32_16x16x32_bf16 v[136:139], v[40:43], v[188:191], v[136:139]\n\t" \
  "v_mfma_f32_16x16x32_bf16 v[140:143], v[44:47], v[188:191], v[140:143]\n\t" \
  "ds_read_b128 v[188:191], v209 offset:49152\n\t" \
  "global_load_lds_dwordx4 v216, s[92:93] sc1\n\t" \
  "s_waitcnt lgkmcnt(8)\n\t" \
  "v_mfma_f32_16x16x32_bf16 v[128:131], v[48:51], v[192:195], v[128:131]\n\t" \
  "v_mfma_f32_16x16x32_bf16 v[132:135], v[52:55], v[192:195], v[132:135]\n\t" \
  "ds_read_b128 v[192:195], v210 offset:32768\n\t" \
  "v_mfma_f32_16x16x32_bf16 v[136:139], v[48:51], v[196:199], v[136:139]\n\t" \
  "v_mfma_f32_16x16x32_bf16 v[140:143], v[52:55], v[196:199], v[140:143]\n\t" \
  "ds_read_b128 v[196:199], v210 offset:49152\n\t" \
  "s_waitcnt lgkmcnt(4)\n\t" \
  "v_add_f32 v160, v160, v144\n\t" \
  "v_add_f32 v161, v161, v145\n\t" \
  "v_add_f32 v162, v162, v146\n\t" \
  "v_add_f32 v163, v163, v147\n\t" \
  "v_add_f32 v164, v164, v148\n\t" \
  "v_add_f32 v165, v165, v149\n\t" \
  "v_add_f32 v166, v166, v150\n\t" \
  "v_add_f32 v167, v167, v151\n\t" \
  "v_mfma_f32_16x16x32_bf16 v[128:131], v[56:59], v[200:203], v[128:131]\n\t" \
  "v_mfma_f32_16x16x32_bf16 v[132:135], v[60:63], v[200:203], v[132:135]\n\t" \
  "ds_read_b128 v[200:203], v211 offset:32768\n\t" \
  "s_add_u32 m0, %[lw], 99328\n\t" \
  "v_mfma_f32_16x16x32_bf16 v[136:139], v[56:59], v[204:207], v[136:139]\n\t" \
  "v_mfma_f32_16x16x32_bf16 v[140:143], v[60:63], v[204:207], v[140:143]\n\t" \
  "ds_read_b128 v[204:207], v211 offset:49152\n\t" \
  "global_load_lds_dwordx4 v217, s[92:93] sc1\n\t" \
  "v_cvt_pk_bf16_f32 v168, v160, v161\n\t" \
  "v_cvt_pk_bf16_f32 v169, v162, v163\n\t" \
  "v_cvt_pk_bf16_f32 v170, v164, v165\n\t" \
  "v_cvt_pk_bf16_f32 v171, v166, v167\n\t" \
  "buffer_store_dwordx4 v[168:171], v172, %[rt], s90 offen sc1\n\t" \
  "v_mfma_f32_16x16x32_bf16 v[128:131], v[64:67], v[176:179], v[128:131]\n\t" \
  "v_mfma_f32_16x16x32_bf16 v[132:135], v[68:71], v[176:179], v[132:135]\n\t" \
  "ds_read_b128 v[176:179], v208 offset:33024\n\t" \
  "v_mfma_f32_16x16x32_bf16 v[136:139], v[64:67], v[180:183], v[136:139]\n\t" \
  "v_mfma_f32_16x16x32_bf16 v[140:143], v[68:71], v[180:183], v[140:143]\n\t" \
  "ds_read_b128 v[180:183], v208 offset:49408\n\t" \
  "s_waitcnt lgkmcnt(6)\n\t" \
  "v_mfma_f32_16x16x32_bf16 v[128:131], v[72:75], v[184:187], v[128:131]\n\t" \
  "v_mfma_f32_16x16x32_bf16 v[132:135], v[76:79], v[184:187], v[132:135]\n\t" \
  "ds_read_b128 v[184:187], v209 offset:33024\n\t" \
  "s_add_u32 m0, %[lw], 100352\n\t" \
  "v_mfma_f32_16x16x32_bf16 v[136:139], v[72:75], v[188:191], v[136:139]\n\t" \
  "v_mfma_f32_16x16x32_bf16 v[140:143], v[76:79], v[188:191], v[140:143]\n\t" \
  "ds_read_b128 v[188:191], v209 offset:49408\n\t" \
  "global_load_lds_dwordx4 v218, s[92:93] sc1\n\t" \
  "s_waitcnt lgkmcnt(6)\n\t" \
  "v_mfma_f32_16x16x32_bf16 v[128:131], v[80:83], v[192:195], v[128:131]\n\t" \
  "v_mfma_f32_16x16x32_bf16 v[132:135], v[84:87], v[192:195], v[132:135]\n\t" \
  "ds_read_b128 v[192:195], v210 offset:33024\n\t" \
  "v_mfma_f32_16x16x32_bf16 v[136:139], v[80:83], v[196:199], v[136:139]\n\t" \
  "v_mfma_f32_16x16x32_bf16 v[140:143], v[84:87], v[196:199], v[140:143]\n\t" \
  "ds_read_b128 v[196:199], v210 offset:49408\n\t" \
  "s_waitcnt lgkmcnt(6)\n\t" \
  "v_mfma_f32_16x16x32_bf16 v[128:131], v[88:91], v[200:203], v[128:131]\n\t" \
  "v_mfma_f32_16x16x32_bf16 v[132:135], v[92:95], v[200:203], v[132:135]\n\t" \
  "ds_read_b128 v[200:203], v211 offset:33024\n\t" \
  "s_add_u32 m0, %[lw], 101376\n\t" \
  "v_mfma_f32_16x16x32_bf16 v[136:139], v[88:91], v[204:207], v[136:139]\n\t" \
  "v_mfma_f32_16x16x32_bf16 v[140:143], v[92:95], v[204:207], v[140:143]\n\t" \
  "ds_read_b128 v[204:207], v211 offset:49408\n\t" \
  "global_load_lds_dwordx4 v219, s[92:93] sc1\n\t" \
  "s_cmp_lt_u32 s86, 8\n\t" \
  "s_cbranch_scc1 .Lw11%=\n\t" \
  "s_waitcnt vmcnt(6)\n\t" \
  "s_barrier\n\t" \
  ".Lg11%=:\n\t" \
  "s_add_u32 s92, s92, 65024\n\t" \
  "s_addc_u32 s93, s93, 0\n\t" \
  "s_waitcnt lgkmcnt(6)\n\t" \
  "v_mfma_f32_16x16x32_bf16 v[128:131], v[96:99], v[176:179], v[128:131]\n\t" \
  "v_mfma_f32_16x16x32_bf16 v[132:135], v[100:103], v[176:179], v[132:135]\n\t" \
  "ds_read_b128 v[176:179], v212 offset:0\n\t" \
  "v_mfma_f32_16x16x32_bf16 v[136:139], v[96:99], v[180:183], v[136:139]\n\t" \
  "v_mfma_f32_16x16x32_bf16 v[140:143], v[100:103], v[180:183], v[140:143]\n\t" \
  "ds_read_b128 v[180:183], v212 offset:16384\n\t" \
  "s_waitcnt lgkmcnt(6)\n\t" \
  "v_mfma_f32_16x16x32_bf16 v[128:131], v[104:107], v[184:187], v[128:131]\n\t" \
  "v_mfma_f32_16x16x32_bf16 v[132:135], v[108:111], v[184:187], v[132:135]\n\t" \
  "ds_read_b128 v[184:187], v213 offset:0\n\t" \
  "s_add_u32 m0, %[lw], 0\n\t" \
  "v_mfma_f32_16x16x32_bf16 v[136:139], v[104:107], v[188:191], v[136:139]\n\t" \
  "v_mfma_f32_16x16x32_bf16 v[140:143], v[108:111], v[188:191], v[140:143]\n\t" \
  "ds_read_b128 v[188:191], v213 offset:16384\n\t" \
  "global_load_lds_dwordx4 v216, s[92:93] sc1\n\t" \
  "s_waitcnt lgkmcnt(6)\n\t" \
  "v_mfma_f32_16x16x32_bf16 v[128:131], v[112:115], v[192:195], v[128:131]\n\t" \
  "v_mfma_f32_16x16x32_bf16 v[132:135], v[116:119], v[192:195], v[132:135]\n\t" \
  "ds_read_b128 v[192:195], v214 offset:0\n\t" \
  "v_mfma_f32_16x16x32_bf16 v[136:139], v[112:115], v[196:199], v[136:139]\n\t" \
  "v_mfma_f32_16x16x32_bf16 v[140:143], v[116:119], v[196:199], v[140:143]\n\t" \
  "ds_read_b128 v[196:199], v214 offset:16384\n\t" \
  "s_waitcnt lgkmcnt(6)\n\t" \
  "v_mfma_f32_16x16x32_bf16 v[128:131], v[120:123], v[200:203], v[128:131]\n\t" \
  "v_mfma_f32_16x16x32_bf16 v[132:135], v[124:127], v[200:203], v[132:135]\n\t" \
  "ds_read_b128 v[200:203], v215 offset:0\n\t" \
  "s_add_u32 m0, %[lw], 1024\n\t" \
  "v_mfma_f32_16x16x32_bf16 v[136:139], v[120:123], v[204:207], v[136:139]\n\t" \
  "v_mfma_f32_16x16x32_bf16 v[140:143], v[124:127], v[204:207], v[140:143]\n\t" \
  "ds_read_b128 v[204:207], v215 offset:16384\n\t" \
  "global_load_lds_dwordx4 v217, s[92:93] sc1\n\t" \
  "s_add_u32 s86, s86, 1\n\t" \
  "s_add_u32 s87, s87, 1\n\t" \
  "s_cmp_le_u32 s86, 5\n\t" \
  "s_cbranch_scc1 .Lf11%=\n\t" \
  ".Lfb11%=:\n\t" \
  "s_waitcnt lgkmcnt(6)\n\t" \
  "v_mfma_f32_16x16x32_bf16 v[144:147], v[0:3], v[176:179], 0\n\t" \
  "v_mfma_f32_16x16x32_bf16 v[148:151], v[4:7], v[176:179], 0\n\t" \
  "ds_read_b128 v[176:179], v212 offset:256\n\t" \
  "v_mfma_f32_16x16x32_bf16 v[152:155], v[0:3], v[180:183], 0\n\t" \
  "v_mfma_f32_16x16x32_bf16 v[156:159], v[4:7], v[180:183], 0\n\t" \
  "ds_read_b128 v[180:183], v212 offset:16640\n\t" \
  "s_waitcnt lgkmcnt(6)\n\t" \
  "v_mfma_f32_16x16x32_bf16 v[144:147], v[8:11], v[184:187], v[144:147]\n\t" \
  "v_mfma_f32_16x16x32_bf16 v[148:151], v[12:15], v[184:187], v[148:151]\n\t" \
  "ds_read_b128 v[184:187], v213 offset:256\n\t" \
  "s_add_u32 m0, %[lw], 2048\n\t" \
  "v_mfma_f32_16x16x32_bf16 v[152:155], v[8:11], v[188:191], v[152:155]\n\t" \
  "v_mfma_f32_16x16x32_bf16 v[156:159], v[12:15], v[188:191], v[156:159]\n\t" \
  "ds_read_b128 v[188:191], v213 offset:16640\n\t" \
  "global_load_lds_dwordx4 v218, s[92:93] sc1\n\t" \
  "ds_write_b128 v220, v[136:139] offset:0\n\t" \
  "ds_write_b128 v220, v[140:143] offset:1024\n\t" \
  "s_waitcnt lgkmcnt(8)\n\t" \
  "v_mfma_f32_16x16x32_bf16 v[144:147], v[16:19], v[192:195], v[144:147]\n\t" \
  "v_mfma_f32_16x16x32_bf16 v[148:151], v[20:23], v[192:195], v[148:151]\n\t" \
  "ds_read_b128 v[192:195], v214 offset:256\n\t" \
  "v_mfma_f32_16x16x32_bf16 v[152:155], v[16:19], v[196:199], v[152:155]\n\t" \
  "v_mfma_f32_16x16x32_bf16 v[156:159], v[20:23], v[196:199], v[156:159]\n\t" \
  "ds_read_b128 v[196:199], v214 offset:16640\n\t" \
  "s_lshr_b32 s90, s87, 7\n\t" \
  "s_xor_b32 s90, s90, %[s0]\n\t" \
  "s_and_b32 s90, s90, 1\n\t" \
  "s_lshl1_add_u32 s90, s87, s90\n\t" \
  "s_lshl_b32 s90, s90, 13\n\t" \
  "s_cmp_lt_i32 s86, 6\n\t" \
  "s_cselect_b32 s90, 0x70000000, s90\n\t" \
  "s_waitcnt lgkmcnt(8)\n\t" \
  "v_mfma_f32_16x16x32_bf16 v[144:147], v[24:27], v[200:203], v[144:147]\n\t" \
  "v_mfma_f32_16x16x32_bf16 v[148:151], v[28:31], v[200:203], v[148:151]\n\t" \
  "ds_read_b128 v[200:203], v215 offset:256\n\t" \
  "s_add_u32 m0, %[lw], 3072\n\t" \
  "v_mfma_f32_16x16x32_bf16 v[152:155], v[24:27], v[204:207], v[152:155]\n\t" \
  "v_mfma_f32_16x16x32_bf16 v[156:159], v[28:31], v[204:207], v[156:159]\n\t" \
  "ds_read_b128 v[204:207], v215 offset:16640\n\t" \
  "global_load_lds_dwordx4 v219, s[92:93] sc1\n\t" \
  "s_waitcnt lgkmcnt(4)\n\t" \
  "s_cmp_lt_u32 s86, 6\n\t" \
  "s_cbranch_scc1 .Lw21%=\n\t" \
  "s_waitcnt vmcnt(4)\n\t" \
  "s_barrier\n\t" \
  ".Lg21%=:\n\t" \
  "s_add_u32 s92, s92, 512\n\t" \
  "s_addc_u32 s93, s93, 0\n\t" \
  "v_mfma_f32_16x16x32_bf16 v[144:147], v[32:35], v[176:179], v[144:147]\n\t" \
  "v_mfma_f32_16x16x32_bf16 v[148:151], v[36:39], v[176:179], v[148:151]\n\t" \
  "ds_read_b128 v[176:179], v212 offset:32768\n\t" \
  "v_mfma_f32_16x16x32_bf16 v[152:155], v[32:35], v[180:183], v[152:155]\n\t" \
  "v_mfma_f32_16x16x32_bf16 v[156:159], v[36:39], v[180:183], v[156:159]\n\t" \
  "ds_read_b128 v[180:183], v212 offset:49152\n\t" \
  "ds_read_b128 v[160:163], v221 offset:0\n\t" \
  "ds_read_b128 v[164:167], v221 offset:1024\n\t" \
  "s_sub_i32 s91, s86, 2\n\t" \
  "s_max_i32 s88, s88, s91\n\t" \
  "v_mov_b32 v173, s88\n\t" \
  "buffer_store_dword v173, v174, %[rs], 0 offen sc1\n\t" \
  "v_mfma_f32_16x16x32_bf16 v[144:147], v[40:43], v[184:187], v[144:147]\n\t" \
  "v_mfma_f32_16x16x32_bf16 v[148:151], v[44:47], v[184:187], v[148:151]\n\t" \
  "ds_read_b128 v[184:187], v213 offset:32768\n\t" \
  "s_add_u32 m0, %[lw], 32768\n\t" \
  "v_mfma_f32_16x16x32_bf16 v[152:155], v[40:43], v[188:191], v[152:155]\n\t" \
  "v_mfma_f32_16x16x32_bf16 v[156:159], v[44:47], v[188:191], v[156:159]\n\t" \
  "ds_read_b128 v[188:191], v213 offset:49152\n\t" \
  "global_load_lds_dwordx4 v216, s[92:93] sc1\n\t" \
  "s_waitcnt lgkmcnt(8)\n\t" \
  "v_mfma_f32_16x16x32_bf16 v[144:147], v[48:51], v[192:195], v[144:147]\n\t" \
  "v_mfma_f32_16x16x32_bf16 v[148:151], v[52:55], v[192:195], v[148:151]\n\t" \
  "ds_read_b128 v[192:195], v214 offset:32768\n\t" \
  "v_mfma_f32_16x16x32_bf16 v[152:155], v[48:51], v[196:199], v[152:155]\n\t" \
  "v_mfma_f32_16x16x32_bf16 v[156:159], v[52:55], v[196:199], v[156:159]\n\t" \
  "ds_read_b128 v[196:199], v214 offset:49152\n\t" \
  "s_waitcnt lgkmcnt(4)\n\t" \
  "v_add_f32 v160, v160, v128\n\t" \
  "v_add_f32 v161, v161, v129\n\t" \
  "v_add_f32 v162, v162, v130\n\t" \
  "v_add_f32 v163, v163, v131\n\t" \
  "v_add_f32 v164, v164, v132\n\t" \
  "v_add_f32 v165, v165, v133\n\t" \
  "v_add_f32 v166, v166, v134\n\t" \
  "v_add_f32 v167, v167, v135\n\t" \
  "v_mfma_f32_16x16x32_bf16 v[144:147], v[56:59], v[200:203], v[144:147]\n\t" \
  "v_mfma_f32_16x16x32_bf16 v[148:151], v[60:63], v[200:203], v[148:151]\n\t" \
  "ds_read_b128 v[200:203], v215 offset:32768\n\t" \
  "s_add_u32 m0, %[lw], 33792\n\t" \
  "v_mfma_f32_16x16x32_bf16 v[152:155], v[56:59], v[204:207], v[152:155]\n\t" \
  "v_mfma_f32_16x16x32_bf16 v[156:159], v[60:63], v[204:207], v[156:159]\n\t" \
  "ds_read_b128 v[204:207], v215 offset:49152\n\t" \
  "global_load_lds_dwordx4 v217, s[92:93] sc1\n\t" \
  "v_cvt_pk_bf16_f32 v168, v160, v161\n\t" \
  "v_cvt_pk_bf16_f32 v169, v162, v163\n\t" \
  "v_cvt_pk_bf16_f32 v170, v164, v165\n\t" \
  "v_cvt_pk_bf16_f32 v171, v166, v167\n\t" \
  "buffer_store_dwordx4 v[168:171], v172, %[rt], s90 offen sc1\n\t" \
  "v_mfma_f32_16x16x32_bf16 v[144:147], v[64:67], v[176:179], v[144:147]\n\t" \
  "v_mfma_f32_16x16x32_bf16 v[148:151], v[68:71], v[176:179], v[148:151]\n\t" \
  "ds_read_b128 v[176:179], v212 offset:33024\n\t" \
  "v_mfma_f32_16x16x32_bf16 v[152:155], v[64:67], v[180:183], v[152:155]\n\t" \
  "v_mfma_f32_16x16x32_bf16 v[156:159], v[68:71], v[180:183], v[156:159]\n\t" \
  "ds_read_b128 v[180:183], v212 offset:49408\n\t" \
  "s_waitcnt lgkmcnt(6)\n\t" \
  "v_mfma_f32_16x16x32_bf16 v[144:147], v[72:75], v[184:187], v[144:147]\n\t" \
  "v_mfma_f32_16x16x32_bf16 v[148:151], v[76:79], v[184:187], v[148:151]\n\t" \
  "ds_read_b128 v[184:187], v213 offset:33024\n\t" \
  "s_add_u32 m0, %[lw], 34816\n\t" \
  "v_mfma_f32_16x16x32_bf16 v[152:155], v[72:75], v[188:191], v[152:155]\n\t" \
  "v_mfma_f32_16x16x32_bf16 v[156:159], v[76:79], v[188:191], v[156:159]\n\t" \
  "ds_read_b128 v[188:191], v213 offset:49408\n\t" \
  "global_load_lds_dwordx4 v218, s[92:93] sc1\n\t" \
  "s_waitcnt lgkmcnt(6)\n\t" \
  "v_mfma_f32_16x16x32_bf16 v[144:147], v[80:83], v[192:195], v[144:147]\n\t" \
  "v_mfma_f32_16x16x32_bf16 v[148:151], v[84:87], v[192:195], v[148:151]\n\t" \
  "ds_read_b128 v[192:195], v214 offset:33024\n\t" \
  "v_mfma_f32_16x16x32_bf16 v[152:155], v[80:83], v[196:199], v[152:155]\n\t" \
  "v_mfma_f32_16x16x32_bf16 v[156:159], v[84:87], v[196:199], v[156:159]\n\t" \
  "ds_read_b128 v[196:199], v214 offset:49408\n\t" \
  "s_waitcnt lgkmcnt(6)\n\t" \
  "v_mfma_f32_16x16x32_bf16 v[144:147], v[88:91], v[200:203], v[144:147]\n\t" \
  "v_mfma_f32_16x16x32_bf16 v[148:151], v[92:95], v[200:203], v[148:151]\n\t" \
  "ds_read_b128 v[200:203], v215 offset:33024\n\t" \
  "s_add_u32 m0, %[lw], 35840\n\t" \
  "v_mfma_f32_16x16x32_bf16 v[152:155], v[88:91], v[204:207], v[152:155]\n\t" \
  "v_mfma_f32_16x16x32_bf16 v[156:159], v[92:95], v[204:207], v[156:159]\n\t" \
  "ds_read_b128 v[204:207], v215 offset:49408\n\t" \
  "global_load_lds_dwordx4 v219, s[92:93] sc1\n\t" \
  "s_cmp_lt_u32 s86, 8\n\t" \
  "s_cbranch_scc1 .Lw31%=\n\t" \
  "s_waitcnt vmcnt(6)\n\t" \
  "s_barrier\n\t" \
  ".Lg31%=:\n\t" \
  "s_add_u32 s92, s92, 65024\n\t" \
  "s_addc_u32 s93, s93, 0\n\t" \
  "s_waitcnt lgkmcnt(6)\n\t" \
  "v_mfma_f32_16x16x32_bf16 v[144:147], v[96:99], v[176:179], v[144:147]\n\t" \
  "v_mfma_f32_16x16x32_bf16 v[148:151], v[100:103], v[176:179], v[148:151]\n\t" \
  "ds_read_b128 v[176:179], v208 offset:0\n\t" \
  "v_mfma_f32_16x16x32_bf16 v[152:155], v[96:99], v[180:183], v[152:155]\n\t" \
  "v_mfma_f32_16x16x32_bf16 v[156:159], v[100:103], v[180:183], v[156:159]\n\t" \
  "ds_read_b128 v[180:183], v208 offset:16384\n\t" \
  "s_waitcnt lgkmcnt(6)\n\t" \
  "v_mfma_f32_16x16x32_bf16 v[144:147], v[104:107], v[184:187], v[144:147]\n\t" \
  "v_mfma_f32_16x16x32_bf16 v[148:151], v[108:111], v[184:187], v[148:151]\n\t" \
  "ds_read_b128 v[184:187], v209 offset:0\n\t" \
  "s_add_u32 m0, %[lw], 65536\n\t" \
  "v_mfma_f32_16x16x32_bf16 v[152:155], v[104:107], v[188:191], v[152:155]\n\t" \
  "v_mfma_f32_16x16x32_bf16 v[156:159], v[108:111], v[188:191], v[156:159]\n\t" \
  "ds_read_b128 v[188:191], v209 offset:16384\n\t" \
  "global_load_lds_dwordx4 v216, s[92:93] sc1\n\t" \
  "s_waitcnt lgkmcnt(6)\n\t" \
  "v_mfma_f32_16x16x32_bf16 v[144:147], v[112:115], v[192:195], v[144:147]\n\t" \
  "v_mfma_f32_16x16x32_bf16 v[148:151], v[116:119], v[192:195], v[148:151]\n\t" \
  "ds_read_b128 v[192:195], v210 offset:0\n\t" \
  "v_mfma_f32_16x16x32_bf16 v[152:155], v[112:115], v[196:199], v[152:155]\n\t" \
  "v_mfma_f32_16x16x32_bf16 v[156:159], v[116:119], v[196:199], v[156:159]\n\t" \
  "ds_read_b128 v[196:199], v210 offset:16384\n\t" \
  "s_waitcnt lgkmcnt(6)\n\t" \
  "v_mfma_f32_16x16x32_bf16 v[144:147], v[120:123], v[200:203], v[144:147]\n\t" \
  "v_mfma_f32_16x16x32_bf16 v[148:151], v[124:127], v[200:203], v[148:151]\n\t" \
  "ds_read_b128 v[200:203], v211 offset:0\n\t" \
  "s_add_u32 m0, %[lw], 66560\n\t" \
  "v_mfma_f32_16x16x32_bf16 v[152:155], v[120:123], v[204:207], v[152:155]\n\t" \
  "v_mfma_f32_16x16x32_bf16 v[156:159], v[124:127], v[204:207], v[156:159]\n\t" \
  "ds_read_b128 v[204:207], v211 offset:16384\n\t" \
  "global_load_lds_dwordx4 v217, s[92:93] sc1\n\t" \
  "s_add_u32 s86, s86, 1\n\t" \
  "s_add_u32 s87, s87, 1\n\t" \
  "s_cmp_lt_u32 s86, %[end]\n\t" \
  "s_cbranch_scc1 .Lp1%=\n\t" \
  "s_branch .Lx1%=\n\t" \
  ".Lf01%=:\n\t" \
  "s_nop 7\n\t" \
  "ds_write_b128 v220, v[152:155] offset:0\n\t" \
  "ds_write_b128 v220, v[156:159] offset:1024\n\t" \
  "s_lshr_b32 s90, s87, 7\n\t" \
  "s_xor_b32 s90, s90, %[s0]\n\t" \
  "s_and_b32 s90, s90, 1\n\t" \
  "s_lshl1_add_u32 s90, s87, s90\n\t" \
  "s_lshl_b32 s90, s90, 13\n\t" \
  "s_cmp_lt_i32 s86, 1\n\t" \
  "s_cselect_b32 s90, 0x70000000, s90\n\t" \
  "s_waitcnt lgkmcnt(0)\n\t" \
  "s_barrier\n\t" \
  "ds_read_b128 v[160:163], v221 offset:0\n\t" \
  "ds_read_b128 v[164:167], v221 offset:1024\n\t" \
  "s_waitcnt lgkmcnt(0)\n\t" \
  "v_add_f32 v160, v160, v144\n\t" \
  "v_add_f32 v161, v161, v145\n\t" \
  "v_add_f32 v162, v162, v146\n\t" \
  "v_add_f32 v163, v163, v147\n\t" \
  "v_add_f32 v164, v164, v148\n\t" \
  "v_add_f32 v165, v165, v149\n\t" \
  "v_add_f32 v166, v166, v150\n\t" \
  "v_add_f32 v167, v167, v151\n\t" \
  "v_cvt_pk_bf16_f32 v168, v160, v161\n\t" \
  "v_cvt_pk_bf16_f32 v169, v162, v163\n\t" \
  "v_cvt_pk_bf16_f32 v170, v164, v165\n\t" \
  "v_cvt_pk_bf16_f32 v171, v166, v167\n\t" \
  "buffer_store_dwordx4 v[168:171], v172, %[rt], s90 offen sc1\n\t" \
  "s_branch .Lfb01%=\n\t" \
  ".Lf11%=:\n\t" \
  "s_nop 7\n\t" \
  "ds_write_b128 v220, v[136:139] offset:0\n\t" \
  "ds_write_b128 v220, v[140:143] offset:1024\n\t" \
  "s_lshr_b32 s90, s87, 7\n\t" \
  "s_xor_b32 s90, s90, %[s0]\n\t" \
  "s_and_b32 s90, s90, 1\n\t" \
  "s_lshl1_add_u32 s90, s87, s90\n\t" \
  "s_lshl_b32 s90, s90, 13\n\t" \
  "s_cmp_lt_i32 s86, 1\n\t" \
  "s_cselect_b32 s90, 0x70000000, s90\n\t" \
  "s_waitcnt lgkmcnt(0)\n\t" \
  "s_barrier\n\t" \
  "ds_read_b128 v[160:163], v221 offset:0\n\t" \
  "ds_read_b128 v[164:167], v221 offset:1024\n\t" \
  "s_waitcnt lgkmcnt(0)\n\t" \
  "v_add_f32 v160, v160, v128\n\t" \
  "v_add_f32 v161, v161, v129\n\t" \
  "v_add_f32 v162, v162, v130\n\t" \
  "v_add_f32 v163, v163, v131\n\t" \
  "v_add_f32 v164, v164, v132\n\t" \
  "v_add_f32 v165, v165, v133\n\t" \
  "v_add_f32 v166, v166, v134\n\t" \
  "v_add_f32 v167, v167, v135\n\t" \
  "v_cvt_pk_bf16_f32 v168, v160, v161\n\t" \
  "v_cvt_pk_bf16_f32 v169, v162, v163\n\t" \
  "v_cvt_pk_bf16_f32 v170, v164, v165\n\t" \
  "v_cvt_pk_bf16_f32 v171, v166, v167\n\t" \
  "buffer_store_dwordx4 v[168:171], v172, %[rt], s90 offen sc1\n\t" \
  "s_branch .Lfb11%=\n\t" \
  ".Lw01%=:\n\t" \
  "s_waitcnt vmcnt(2)\n\t" \
  "s_barrier\n\t" \
  "s_sub_i32 s91, s86, 1\n\t" \
  "s_max_i32 s88, s88, s91\n\t" \
  "v_mov_b32 v173, s88\n\t" \
  "buffer_store_dword v173, v174, %[rs], 0 offen sc1\n\t" \
  "s_branch .Lg01%=\n\t" \
  ".Lw11%=:\n\t" \
  "s_waitcnt vmcnt(2)\n\t" \
  "s_barrier\n\t" \
  "s_sub_i32 s91, s86, 1\n\t" \
  "s_max_i32 s88, s88, s91\n\t" \
  "v_mov_b32 v173, s88\n\t" \
  "buffer_store_dword v173, v174, %[rs], 0 offen sc1\n\t" \
  "s_branch .Lg11%=\n\t" \
  ".Lw21%=:\n\t" \
  "s_waitcnt vmcnt(2)\n\t" \
  "s_barrier\n\t" \
  "s_sub_i32 s91, s86, 1\n\t" \
  "s_max_i32 s88, s88, s91\n\t" \
  "v_mov_b32 v173, s88\n\t" \
  "buffer_store_dword v173, v174, %[rs], 0 offen sc1\n\t" \
  "s_branch .Lg21%=\n\t" \
  ".Lw31%=:\n\t" \
  "s_waitcnt vmcnt(2)\n\t" \
  "s_barrier\n\t" \
  "s_sub_i32 s91, s86, 1\n\t" \
  "s_max_i32 s88, s88, s91\n\t" \
  "v_mov_b32 v173, s88\n\t" \
  "buffer_store_dword v173, v174, %[rs], 0 offen sc1\n\t" \
  "s_branch .Lg31%=\n\t" \
  ".Lx1%=:\n\t" \
  "s_waitcnt lgkmcnt(0)\n\t" \
  "s_setprio 0\n\t" \
  "s_mov_b32 m0, s94\n\t" \
  ".Le%=:\n\t"
#define PR_ASM_1 \
  "s_cmp_lg_u32 %[kh], 0\n\t" \
  "s_cbranch_scc1 .Lk%=\n\t" \
  "s_mov_b32 s94, m0\n\t" \
  "s_mov_b64 s[92:93], %[x0]\n\t" \
  "s_mov_b32 s86, %[g0]\n\t" \
  "s_add_u32 s87, s86, %[t0]\n\t" \
  "s_sub_i32 s88, s86, 3\n\t" \
  "ds_read_b128 v[176:179], v208 offset:0\n\t" \
  "ds_read_b128 v[180:183], v208 offset:16384\n\t" \
  "ds_read_b128 v[184:187], v209 offset:0\n\t" \
  "ds_read_b128 v[188:191], v209 offset:16384\n\t" \
  "ds_read_b128 v[192:195], v210 offset:0\n\t" \
  "ds_read_b128 v[196:199], v210 offset:16384\n\t" \
  "ds_read_b128 v[200:203], v211 offset:0\n\t" \
  "ds_read_b128 v[204:207], v211 offset:16384\n\t" \
  "s_branch .Lfb00%=\n\t" \
  ".Lp0%=:\n\t" \
  "s_cmp_le_u32 s86, 5\n\t" \
  "s_cbranch_scc1 .Lf00%=\n\t" \
  ".Lfb00%=:\n\t" \
  "s_waitcnt lgkmcnt(6)\n\t" \
  "v_mfma_f32_16x16x32_bf16 v[128:131], v[176:179], v[0:3], 0\n\t" \
  "v_mfma_f32_16x16x32_bf16 v[136:139], v[176:179], v[4:7], 0\n\t" \
  "ds_read_b128 v[176:179], v208 offset:256\n\t" \
  "s_add_u32 m0, %[lw], 67584\n\t" \
  "v_mfma_f32_16x16x32_bf16 v[132:135], v[180:183], v[0:3], 0\n\t" \
  "v_mfma_f32_16x16x32_bf16 v[140:143], v[180:183], v[4:7], 0\n\t" \
  "ds_read_b128 v[180:183], v208 offset:16640\n\t" \
  "global_load_lds_dwordx4 v218, s[92:93] sc1\n\t" \
  "s_waitcnt lgkmcnt(6)\n\t" \
  "v_mfma_f32_16x16x32_bf16 v[128:131], v[184:187], v[8:11], v[128:131]\n\t" \
  "v_mfma_f32_16x16x32_bf16 v[136:139], v[184:187], v[12:15], v[136:139]\n\t" \
  "ds_read_b128 v[184:187], v209 offset:256\n\t" \
  "v_mfma_f32_16x16x32_bf16 v[132:135], v[188:191], v[8:11], v[132:135]\n\t" \
  "v_mfma_f32_16x16x32_bf16 v[140:143], v[188:191], v[12:15], v[140:143]\n\t" \
  "ds_read_b128 v[188:191], v209 offset:16640\n\t" \
  "ds_write_b128 v220, v[152:155] offset:0\n\t" \
  "ds_write_b128 v220, v[156:159] offset:1024\n\t" \
  "s_waitcnt lgkmcnt(8)\n\t" \
  "v_mfma_f32_16x16x32_bf16 v[128:131], v[192:195], v[16:19], v[128:131]\n\t" \
  "v_mfma_f32_16x16x32_bf16 v[136:139], v[192:195], v[20:23], v[136:139]\n\t" \
  "ds_read_b128 v[192:195], v210 offset:256\n\t" \
  "s_add_u32 m0, %[lw], 68608\n\t" \
  "v_mfma_f32_16x16x32_bf16 v[132:135], v[196:199], v[16:19], v[132:135]\n\t" \
  "v_mfma_f32_16x16x32_bf16 v[140:143], v[196:199], v[20:23], v[140:143]\n\t" \
  "ds_read_b128 v[196:199], v210 offset:16640\n\t" \
  "global_load_lds_dwordx4 v219, s[92:93] sc1\n\t" \
  "s_lshr_b32 s90, s87, 7\n\t" \
  "s_xor_b32 s90, s90, %[s0]\n\t" \
  "s_and_b32 s90, s90, 1\n\t" \
  "s_lshl1_add_u32 s90, s87, s90\n\t" \
  "s_lshl_b32 s90, s90, 13\n\t" \
  "s_cmp_lt_i32 s86, 6\n\t" \
  "s_cselect_b32 s90, 0x70000000, s90\n\t" \
  "s_waitcnt lgkmcnt(8)\n\t" \
  "v_mfma_f32_16x16x32_bf16 v[128:131], v[200:203], v[24:27], v[128:131]\n\t" \
  "v_mfma_f32_16x16x32_bf16 v[136:139], v[200:203], v[28:31], v[136:139]\n\t" \
  "ds_read_b128 v[200:203], v211 offset:256\n\t" \
  "v_mfma_f32_16x16x32_bf16 v[132:135], v[204:207], v[24:27], v[132:135]\n\t" \
  "v_mfma_f32_16x16x32_bf16 v[140:143], v[204:207], v[28:31], v[140:143]\n\t" \
  "ds_read_b128 v[204:207], v211 offset:16640\n\t" \
  "s_waitcnt lgkmcnt(4)\n\t" \
  "s_cmp_lt_u32 s86, 6\n\t" \
  "s_cbranch_scc1 .Lw00%=\n\t" \
  "s_waitcnt vmcnt(4)\n\t" \
  "s_barrier\n\t" \
  ".Lg00%=:\n\t" \
  "s_add_u32 s92, s92, 512\n\t" \
  "s_addc_u32 s93, s93, 0\n\t" \
  "v_mfma_f32_16x16x32_bf16 v[128:131], v[176:179], v[32:35], v[128:131]\n\t" \
  "v_mfma_f32_16x16x32_bf16 v[136:139], v[176:179], v[36:39], v[136:139]\n\t" \
  "ds_read_b128 v[176:179], v208 offset:32768\n\t" \
  "s_add_u32 m0, %[lw], 98304\n\t" \
  "v_mfma_f32_16x16x32_bf16 v[132:135], v[180:183], v[32:35], v[132:135]\n\t" \
  "v_mfma_f32_16x16x32_bf16 v[140:143], v[180:183], v[36:39], v[140:143]\n\t" \
  "ds_read_b128 v[180:183], v208 offset:49152\n\t" \
  "global_load_lds_dwordx4 v216, s[92:93] sc1\n\t" \
  "ds_read_b128 v[160:163], v221 offset:0\n\t" \
  "ds_read_b128 v[164:167], v221 offset:1024\n\t" \
  "v_mfma_f32_16x16x32_bf16 v[128:131], v[184:187], v[40:43], v[128:131]\n\t" \
  "v_mfma_f32_16x16x32_bf16 v[136:139], v[184:187], v[44:47], v[136:139]\n\t" \
  "ds_read_b128 v[184:187], v209 offset:32768\n\t" \
  "v_mfma_f32_16x16x32_bf16 v[132:135], v[188:191], v[40:43], v[132:135]\n\t" \
  "v_mfma_f32_16x16x32_bf16 v[140:143], v[188:191], v[44:47], v[140:143]\n\t" \
  "ds_read_b128 v[188:191], v209 offset:49152\n\t" \
  "s_waitcnt lgkmcnt(8)\n\t" \
  "v_mfma_f32_16x16x32_bf16 v[128:131], v[192:195], v[48:51], v[128:131]\n\t" \
  "v_mfma_f32_16x16x32_bf16 v[136:139], v[192:195], v[52:55], v[136:139]\n\t" \
  "ds_read_b128 v[192:195], v210 offset:32768\n\t" \
  "s_add_u32 m0, %[lw], 99328\n\t" \
  "v_mfma_f32_16x16x32_bf16 v[132:135], v[196:199], v[48:51], v[132:135]\n\t" \
  "v_mfma_f32_16x16x32_bf16 v[140:143], v[196:199], v[52:55], v[140:143]\n\t" \
  "ds_read_b128 v[196:199], v210 offset:49152\n\t" \
  "global_load_lds_dwordx4 v217, s[92:93] sc1\n\t" \
  "s_waitcnt lgkmcnt(4)\n\t" \
  "v_add_f32 v160, v160, v144\n\t" \
  "v_add_f32 v161, v161, v145\n\t" \
  "v_add_f32 v162, v162, v146\n\t" \
  "v_add_f32 v163, v163, v147\n\t" \
  "v_add_f32 v164, v164, v148\n\t" \
  "v_add_f32 v165, v165, v149\n\t" \
  "v_add_f32 v166, v166, v150\n\t" \
  "v_add_f32 v167, v167, v151\n\t" \
  "v_mfma_f32_16x16x32_bf16 v[128:131], v[200:203], v[56:59], v[128:131]\n\t" \
  "v_mfma_f32_16x16x32_bf16 v[136:139], v[200:203], v[60:63], v[136:139]\n\t" \
  "ds_read_b128 v[200:203], v211 offset:32768\n\t" \
  "v_mfma_f32_16x16x32_bf16 v[132:135], v[204:207], v[56:59], v[132:135]\n\t" \
  "v_mfma_f32_16x16x32_bf16 v[140:143], v[204:207], v[60:63], v[140:143]\n\t" \
  "ds_read_b128 v[204:207], v211 offset:49152\n\t" \
  "v_cvt_pk_bf16_f32 v168, v160, v161\n\t" \
  "v_cvt_pk_bf16_f32 v169, v162, v163\n\t" \
  "v_cvt_pk_bf16_f32 v170, v164, v165\n\t" \
  "v_cvt_pk_bf16_f32 v171, v166, v167\n\t" \
  "buffer_store_dwordx4 v[168:171], v172, %[rt], s90 offen sc1\n\t" \
  "v_mfma_f32_16x16x32_bf16 v[128:131], v[176:179], v[64:67], v[128:131]\n\t" \
  "v_mfma_f32_16x16x32_bf16 v[136:139], v[176:179], v[68:71], v[136:139]\n\t" \
  "ds_read_b128 v[176:179], v208 offset:33024\n\t" \
  "s_add_u32 m0, %[lw], 100352\n\t" \
  "v_mfma_f32_16x16x32_bf16 v[132:135], v[180:183], v[64:67], v[132:135]\n\t" \
  "v_mfma_f32_16x16x32_bf16 v[140:143], v[180:183], v[68:71], v[140:143]\n\t" \
  "ds_read_b128 v[180:183], v208 offset:49408\n\t" \
  "global_load_lds_dwordx4 v218, s[92:93] sc1\n\t" \
  "s_waitcnt lgkmcnt(6)\n\t" \
  "v_mfma_f32_16x16x32_bf16 v[128:131], v[184:187], v[72:75], v[128:131]\n\t" \
  "v_mfma_f32_16x16x32_bf16 v[136:139], v[184:187], v[76:79], v[136:139]\n\t" \
  "ds_read_b128 v[184:187], v209 offset:33024\n\t" \
  "v_mfma_f32_16x16x32_bf16 v[132:135], v[188:191], v[72:75], v[132:135]\n\t" \
  "v_mfma_f32_16x16x32_bf16 v[140:143], v[188:191], v[76:79], v[140:143]\n\t" \
  "ds_read_b128 v[188:191], v209 offset:49408\n\t" \
  "s_waitcnt lgkmcnt(6)\n\t" \
  "v_mfma_f32_16x16x32_bf16 v[128:131], v[192:195], v[80:83], v[128:131]\n\t" \
  "v_mfma_f32_16x16x32_bf16 v[136:139], v[192:195], v[84:87], v[136:139]\n\t" \
  "ds_read_b128 v[192:195], v210 offset:33024\n\t" \
  "s_add_u32 m0, %[lw], 101376\n\t" \
  "v_mfma_f32_16x16x32_bf16 v[132:135], v[196:199], v[80:83], v[132:135]\n\t" \
  "v_mfma_f32_16x16x32_bf16 v[140:143], v[196:199], v[84:87], v[140:143]\n\t" \
  "ds_read_b128 v[196:199], v210 offset:49408\n\t" \
  "global_load_lds_dwordx4 v219, s[92:93] sc1\n\t" \
  "s_waitcnt lgkmcnt(6)\n\t" \
  "v_mfma_f32_16x16x32_bf16 v[128:131], v[200:203], v[88:91], v[128:131]\n\t" \
  "v_mfma_f32_16x16x32_bf16 v[136:139], v[200:203], v[92:95], v[136:139]\n\t" \
  "ds_read_b128 v[200:203], v211 offset:33024\n\t" \
  "v_mfma_f32_16x16x32_bf16 v[132:135], v[204:207], v[88:91], v[132:135]\n\t" \
  "v_mfma_f32_16x16x32_bf16 v[140:143], v[204:207], v[92:95], v[140:143]\n\t" \
  "ds_read_b128 v[204:207], v211 offset:49408\n\t" \
  "s_cmp_lt_u32 s86, 8\n\t" \
  "s_cbranch_scc1 .Lw10%=\n\t" \
  "s_waitcnt vmcnt(5)\n\t" \
  "s_barrier\n\t" \
  ".Lg10%=:\n\t" \
  "s_add_u32 s92, s92, 65024\n\t" \
  "s_addc_u32 s93, s93, 0\n\t" \
  "s_waitcnt lgkmcnt(6)\n\t" \
  "v_mfma_f32_16x16x32_bf16 v[128:131], v[176:179], v[96:99], v[128:131]\n\t" \
  "v_mfma_f32_16x16x32_bf16 v[136:139], v[176:179], v[100:103], v[136:139]\n\t" \
  "ds_read_b128 v[176:179], v212 offset:0\n\t" \
  "s_add_u32 m0, %[lw], 0\n\t" \
  "v_mfma_f32_16x16x32_bf16 v[132:135], v[180:183], v[96:99], v[132:135]\n\t" \
  "v_mfma_f32_16x16x32_bf16 v[140:143], v[180:183], v[100:103], v[140:143]\n\t" \
  "ds_read_b128 v[180:183], v212 offset:16384\n\t" \
  "global_load_lds_dwordx4 v216, s[92:93] sc1\n\t" \
  "s_waitcnt lgkmcnt(6)\n\t" \
  "v_mfma_f32_16x16x32_bf16 v[128:131], v[184:187], v[104:107], v[128:131]\n\t" \
  "v_mfma_f32_16x16x32_bf16 v[136:139], v[184:187], v[108:111], v[136:139]\n\t" \
  "ds_read_b128 v[184:187], v213 offset:0\n\t" \
  "v_mfma_f32_16x16x32_bf16 v[132:135], v[188:191], v[104:107], v[132:135]\n\t" \
  "v_mfma_f32_16x16x32_bf16 v[140:143], v[188:191], v[108:111], v[140:143]\n\t" \
  "ds_read_b128 v[188:191], v213 offset:16384\n\t" \
  "s_waitcnt lgkmcnt(6)\n\t" \
  "v_mfma_f32_16x16x32_bf16 v[128:131], v[192:195], v[112:115], v[128:131]\n\t" \
  "v_mfma_f32_16x16x32_bf16 v[136:139], v[192:195], v[116:119], v[136:139]\n\t" \
  "ds_read_b128 v[192:195], v214 offset:0\n\t" \
  "s_add_u32 m0, %[lw], 1024\n\t" \
  "v_mfma_f32_16x16x32_bf16 v[132:135], v[196:199], v[112:115], v[132:135]\n\t" \
  "v_mfma_f32_16x16x32_bf16 v[140:143], v[196:199], v[116:119], v[140:143]\n\t" \
  "ds_read_b128 v[196:199], v214 offset:16384\n\t" \
  "global_load_lds_dwordx4 v217, s[92:93] sc1\n\t" \
  "s_waitcnt lgkmcnt(6)\n\t" \
  "v_mfma_f32_16x16x32_bf16 v[128:131], v[200:203], v[120:123], v[128:131]\n\t" \
  "v_mfma_f32_16x16x32_bf16 v[136:139], v[200:203], v[124:127], v[136:139]\n\t" \
  "ds_read_b128 v[200:203], v215 offset:0\n\t" \
  "v_mfma_f32_16x16x32_bf16 v[132:135], v[204:207], v[120:123], v[132:135]\n\t" \
  "v_mfma_f32_16x16x32_bf16 v[140:143], v[204:207], v[124:127], v[140:143]\n\t" \
  "ds_read_b128 v[204:207], v215 offset:16384\n\t" \
  "s_add_u32 s86, s86, 1\n\t" \
  "s_add_u32 s87, s87, 1\n\t" \
  "s_cmp_le_u32 s86, 5\n\t" \
  "s_cbranch_scc1 .Lf10%=\n\t" \
  ".Lfb10%=:\n\t" \
  "s_waitcnt lgkmcnt(6)\n\t" \
  "v_mfma_f32_16x16x32_bf16 v[144:147], v[176:179], v[0:3], 0\n\t" \
  "v_mfma_f32_16x16x32_bf16 v[152:155], v[176:179], v[4:7], 0\n\t" \
  "ds_read_b128 v[176:179], v212 offset:256\n\t" \
  "s_add_u32 m0, %[lw], 2048\n\t" \
  "v_mfma_f32_16x16x32_bf16 v[148:151], v[180:183], v[0:3], 0\n\t" \
  "v_mfma_f32_16x16x32_bf16 v[156:159], v[180:183], v[4:7], 0\n\t" \
  "ds_read_b128 v[180:183], v212 offset:16640\n\t" \
  "global_load_lds_dwordx4 v218, s[92:93] sc1\n\t" \
  "s_waitcnt lgkmcnt(6)\n\t" \
  "v_mfma_f32_16x16x32_bf16 v[144:147], v[184:187], v[8:11], v[144:147]\n\t" \
  "v_mfma_f32_16x16x32_bf16 v[152:155], v[184:187], v[12:15], v[152:155]\n\t" \
  "ds_read_b128 v[184:187], v213 offset:256\n\t" \
  "v_mfma_f32_16x16x32_bf16 v[148:151], v[188:191], v[8:11], v[148:151]\n\t" \
  "v_mfma_f32_16x16x32_bf16 v[156:159], v[188:191], v[12:15], v[156:159]\n\t" \
  "ds_read_b128 v[188:191], v213 offset:16640\n\t" \
  "ds_write_b128 v220, v[136:139] offset:0\n\t" \
  "ds_write_b128 v220, v[140:143] offset:1024\n\t" \
  "s_waitcnt lgkmcnt(8)\n\t" \
  "v_mfma_f32_16x16x32_bf16 v[144:147], v[192:195], v[16:19], v[144:147]\n\t" \
  "v_mfma_f32_16x16x32_bf16 v[152:155], v[192:195], v[20:23], v[152:155]\n\t" \
  "ds_read_b128 v[192:195], v214 offset:256\n\t" \
  "s_add_u32 m0, %[lw], 3072\n\t" \
  "v_mfma_f32_16x16x32_bf16 v[148:151], v[196:199], v[16:19], v[148:151]\n\t" \
  "v_mfma_f32_16x16x32_bf16 v[156:159], v[196:199], v[20:23], v[156:159]\n\t" \
  "ds_read_b128 v[196:199], v214 offset:16640\n\t" \
  "global_load_lds_dwordx4 v219, s[92:93] sc1\n\t" \
  "s_lshr_b32 s90, s87, 7\n\t" \
  "s_xor_b32 s90, s90, %[s0]\n\t" \
  "s_and_b32 s90, s90, 1\n\t" \
  "s_lshl1_add_u32 s90, s87, s90\n\t" \
  "s_lshl_b32 s90, s90, 13\n\t" \
  "s_cmp_lt_i32 s86, 6\n\t" \
  "s_cselect_b32 s90, 0x70000000, s90\n\t" \
  "s_waitcnt lgkmcnt(8)\n\t" \
  "v_mfma_f32_16x16x32_bf16 v[144:147], v[200:203], v[24:27], v[144:147]\n\t" \
  "v_mfma_f32_16x16x32_bf16 v[152:155], v[200:203], v[28:31], v[152:155]\n\t" \
  "ds_read_b128 v[200:203], v215 offset:256\n\t" \
  "v_mfma_f32_16x16x32_bf16 v[148:151], v[204:207], v[24:27], v[148:151]\n\t" \
  "v_mfma_f32_16x16x32_bf16 v[156:159], v[204:207], v[28:31], v[156:159]\n\t" \
  "ds_read_b128 v[204:207], v215 offset:16640\n\t" \
  "s_waitcnt lgkmcnt(4)\n\t" \
  "s_cmp_lt_u32 s86, 6\n\t" \
  "s_cbranch_scc1 .Lw20%=\n\t" \
  "s_waitcnt vmcnt(4)\n\t" \
  "s_barrier\n\t" \
  ".Lg20%=:\n\t" \
  "s_add_u32 s92, s92, 512\n\t" \
  "s_addc_u32 s93, s93, 0\n\t" \
  "v_mfma_f32_16x16x32_bf16 v[144:147], v[176:179], v[32:35], v[144:147]\n\t" \
  "v_mfma_f32_16x16x32_bf16 v[152:155], v[176:179], v[36:39], v[152:155]\n\t" \
  "ds_read_b128 v[176:179], v212 offset:32768\n\t" \
  "s_add_u32 m0, %[lw], 32768\n\t" \
  "v_mfma_f32_16x16x32_bf16 v[148:151], v[180:183], v[32:35], v[148:151]\n\t" \
  "v_mfma_f32_16x16x32_bf16 v[156:159], v[180:183], v[36:39], v[156:159]\n\t" \
  "ds_read_b128 v[180:183], v212 offset:49152\n\t" \
  "global_load_lds_dwordx4 v216, s[92:93] sc1\n\t" \
  "ds_read_b128 v[160:163], v221 offset:0\n\t" \
  "ds_read_b128 v[164:167], v221 offset:1024\n\t" \
  "v_mfma_f32_16x16x32_bf16 v[144:147], v[184:187], v[40:43], v[144:147]\n\t" \
  "v_mfma_f32_16x16x32_bf16 v[152:155], v[184:187], v[44:47], v[152:155]\n\t" \
  "ds_read_b128 v[184:187], v213 offset:32768\n\t" \
  "v_mfma_f32_16x16x32_bf16 v[148:151], v[188:191], v[40:43], v[148:151]\n\t" \
  "v_mfma_f32_16x16x32_bf16 v[156:159], v[188:191], v[44:47], v[156:159]\n\t" \
  "ds_read_b128 v[188:191], v213 offset:49152\n\t" \
  "s_waitcnt lgkmcnt(8)\n\t" \
  "v_mfma_f32_16x16x32_bf16 v[144:147], v[192:195], v[48:51], v[144:147]\n\t" \
  "v_mfma_f32_16x16x32_bf16 v[152:155], v[192:195], v[52:55], v[152:155]\n\t" \
  "ds_read_b128 v[192:195], v214 offset:32768\n\t" \
  "s_add_u32 m0, %[lw], 33792\n\t" \
  "v_mfma_f32_16x16x32_bf16 v[148:151], v[196:199], v[48:51], v[148:151]\n\t" \
  "v_mfma_f32_16x16x32_bf16 v[156:159], v[196:199], v[52:55], v[156:159]\n\t" \
  "ds_read_b128 v[196:199], v214 offset:49152\n\t" \
  "global_load_lds_dwordx4 v217, s[92:93] sc1\n\t" \
  "s_waitcnt lgkmcnt(4)\n\t" \
  "v_add_f32 v160, v160, v128\n\t" \
  "v_add_f32 v161, v161, v129\n\t" \
  "v_add_f32 v162, v162, v130\n\t" \
  "v_add_f32 v163, v163, v131\n\t" \
  "v_add_f32 v164, v164, v132\n\t" \
  "v_add_f32 v165, v165, v133\n\t" \
  "v_add_f32 v166, v166, v134\n\t" \
  "v_add_f32 v167, v167, v135\n\t" \
  "v_mfma_f32_16x16x32_bf16 v[144:147], v[200:203], v[56:59], v[144:147]\n\t" \
  "v_mfma_f32_16x16x32_bf16 v[152:155], v[200:203], v[60:63], v[152:155]\n\t" \
  "ds_read_b128 v[200:203], v215 offset:32768\n\t" \
  "v_mfma_f32_16x16x32_bf16 v[148:151], v[204:207], v[56:59], v[148:151]\n\t" \
  "v_mfma_f32_16x16x32_bf16 v[156:159], v[204:207], v[60:63], v[156:159]\n\t" \
  "ds_read_b128 v[204:207], v215 offset:49152\n\t" \
  "v_cvt_pk_bf16_f32 v168, v160, v161\n\t" \
  "v_cvt_pk_bf16_f32 v169, v162, v163\n\t" \
  "v_cvt_pk_bf16_f32 v170, v164, v165\n\t" \
  "v_cvt_pk_bf16_f32 v171, v166, v167\n\t" \
  "buffer_store_dwordx4 v[168:171], v172, %[rt], s90 offen sc1\n\t" \
  "v_mfma_f32_16x16x32_bf16 v[144:147], v[176:179], v[64:67], v[144:147]\n\t" \
  "v_mfma_f32_16x16x32_bf16 v[152:155], v[176:179], v[68:71], v[152:155]\n\t" \
  "ds_read_b128 v[176:179], v212 offset:33024\n\t" \
  "s_add_u32 m0, %[lw], 34816\n\t" \
  "v_mfma_f32_16x16x32_bf16 v[148:151], v[180:183], v[64:67], v[148:151]\n\t" \
  "v_mfma_f32_16x16x32_bf16 v[156:159], v[180:183], v[68:71], v[156:159]\n\t" \
  "ds_read_b128 v[180:183], v212 offset:49408\n\t" \
  "global_load_lds_dwordx4 v218, s[92:93] sc1\n\t" \
  "s_waitcnt lgkmcnt(6)\n\t" \
  "v_mfma_f32_16x16x32_bf16 v[144:147], v[184:187], v[72:75], v[144:147]\n\t" \
  "v_mfma_f32_16x16x32_bf16 v[152:155], v[184:187], v[76:79], v[152:155]\n\t" \
  "ds_read_b128 v[184:187], v213 offset:33024\n\t" \
  "v_mfma_f32_16x16x32_bf16 v[148:151], v[188:191], v[72:75], v[148:151]\n\t" \
  "v_mfma_f32_16x16x32_bf16 v[156:159], v[188:191], v[76:79], v[156:159]\n\t" \
  "ds_read_b128 v[188:191], v213 offset:49408\n\t" \
  "s_waitcnt lgkmcnt(6)\n\t" \
  "v_mfma_f32_16x16x32_bf16 v[144:147], v[192:195], v[80:83], v[144:147]\n\t" \
  "v_mfma_f32_16x16x32_bf16 v[152:155], v[192:195], v[84:87], v[152:155]\n\t" \
  "ds_read_b128 v[192:195], v214 offset:33024\n\t" \
  "s_add_u32 m0, %[lw], 35840\n\t" \
  "v_mfma_f32_16x16x32_bf16 v[148:151], v[196:199], v[80:83], v[148:151]\n\t" \
  "v_mfma_f32_16x16x32_bf16 v[156:159], v[196:199], v[84:87], v[156:159]\n\t" \
  "ds_read_b128 v[196:199], v214 offset:49408\n\t" \
  "global_load_lds_dwordx4 v219, s[92:93] sc1\n\t" \
  "s_waitcnt lgkmcnt(6)\n\t" \
  "v_mfma_f32_16x16x32_bf16 v[144:147], v[200:203], v[88:91], v[144:147]\n\t" \
  "v_mfma_f32_16x16x32_bf16 v[152:155], v[200:203], v[92:95], v[152:155]\n\t" \
  "ds_read_b128 v[200:203], v215 offset:33024\n\t" \
  "v_mfma_f32_16x16x32_bf16 v[148:151], v[204:207], v[88:91], v[148:151]\n\t" \
  "v_mfma_f32_16x16x32_bf16 v[156:159], v[204:207], v[92:95], v[156:159]\n\t" \
  "ds_read_b128 v[204:207], v215 offset:49408\n\t" \
  "s_cmp_lt_u32 s86, 8\n\t" \
  "s_cbranch_scc1 .Lw30%=\n\t" \
  "s_waitcnt vmcnt(5)\n\t" \
  "s_barrier\n\t" \
  ".Lg30%=:\n\t" \
  "s_add_u32 s92, s92, 65024\n\t" \
  "s_addc_u32 s93, s93, 0\n\t" \
  "s_waitcnt lgkmcnt(6)\n\t" \
  "v_mfma_f32_16x16x32_bf16 v[144:147], v[176:179], v[96:99], v[144:147]\n\t" \
  "v_mfma_f32_16x16x32_bf16 v[152:155], v[176:179], v[100:103], v[152:155]\n\t" \
  "ds_read_b128 v[176:179], v208 offset:0\n\t" \
  "s_add_u32 m0, %[lw], 65536\n\t" \
  "v_mfma_f32_16x16x32_bf16 v[148:151], v[180:183], v[96:99], v[148:151]\n\t" \
  "v_mfma_f32_16x16x32_bf16 v[156:159], v[180:183], v[100:103], v[156:159]\n\t" \
  "ds_read_b128 v[180:183], v208 offset:16384\n\t" \
  "global_load_lds_dwordx4 v216, s[92:93] sc1\n\t" \
  "s_waitcnt lgkmcnt(6)\n\t" \
  "v_mfma_f32_16x16x32_bf16 v[144:147], v[184:187], v[104:107], v[144:147]\n\t" \
  "v_mfma_f32_16x16x32_bf16 v[152:155], v[184:187], v[108:111], v[152:155]\n\t" \
  "ds_read_b128 v[184:187], v209 offset:0\n\t" \
  "v_mfma_f32_16x16x32_bf16 v[148:151], v[188:191], v[104:107], v[148:151]\n\t" \
  "v_mfma_f32_16x16x32_bf16 v[156:159], v[188:191], v[108:111], v[156:159]\n\t" \
  "ds_read_b128 v[188:191], v209 offset:16384\n\t" \
  "s_waitcnt lgkmcnt(6)\n\t" \
  "v_mfma_f32_16x16x32_bf16 v[144:147], v[192:195], v[112:115], v[144:147]\n\t" \
  "v_mfma_f32_16x16x32_bf16 v[152:155], v[192:195], v[116:119], v[152:155]\n\t" \
  "ds_read_b128 v[192:195], v210 offset:0\n\t" \
  "s_add_u32 m0, %[lw], 66560\n\t" \
  "v_mfma_f32_16x16x32_bf16 v[148:151], v[196:199], v[112:115], v[148:151]\n\t" \
  "v_mfma_f32_16x16x32_bf16 v[156:159], v[196:199], v[116:119], v[156:159]\n\t" \
  "ds_read_b128 v[196:199], v210 offset:16384\n\t" \
  "global_load_lds_dwordx4 v217, s[92:93] sc1\n\t" \
  "s_waitcnt lgkmcnt(6)\n\t" \
  "v_mfma_f32_16x16x32_bf16 v[144:147], v[200:203], v[120:123], v[144:147]\n\t" \
  "v_mfma_f32_16x16x32_bf16 v[152:155], v[200:203], v[124:127], v[152:155]\n\t" \
  "ds_read_b128 v[200:203], v211 offset:0\n\t" \
  "v_mfma_f32_16x16x32_bf16 v[148:151], v[204:207], v[120:123], v[148:151]\n\t" \
  "v_mfma_f32_16x16x32_bf16 v[156:159], v[204:207], v[124:127], v[156:159]\n\t" \
  "ds_read_b128 v[204:207], v211 offset:16384\n\t" \
  "s_add_u32 s86, s86, 1\n\t" \
  "s_add_u32 s87, s87, 1\n\t" \
  "s_cmp_lt_u32 s86, %[end]\n\t" \
  "s_cbranch_scc1 .Lp0%=\n\t" \
  "s_branch .Lx0%=\n\t" \
  ".Lf00%=:\n\t" \
  "s_nop 7\n\t" \
  "ds_write_b128 v220, v[152:155] offset:0\n\t" \
  "ds_write_b128 v220, v[156:159] offset:1024\n\t" \
  "s_lshr_b32 s90, s87, 7\n\t" \
  "s_xor_b32 s90, s90, %[s0]\n\t" \
  "s_and_b32 s90, s90, 1\n\t" \
  "s_lshl1_add_u32 s90, s87, s90\n\t" \
  "s_lshl_b32 s90, s90, 13\n\t" \
  "s_cmp_lt_i32 s86, 1\n\t" \
  "s_cselect_b32 s90, 0x70000000, s90\n\t" \
  "s_waitcnt lgkmcnt(0)\n\t" \
  "s_barrier\n\t" \
  "ds_read_b128 v[160:163], v221 offset:0\n\t" \
  "ds_read_b128 v[164:167], v221 offset:1024\n\t" \
  "s_waitcnt lgkmcnt(0)\n\t" \
  "v_add_f32 v160, v160, v144\n\t" \
  "v_add_f32 v161, v161, v145\n\t" \
  "v_add_f32 v162, v162, v146\n\t" \
  "v_add_f32 v163, v163, v147\n\t" \
  "v_add_f32 v164, v164, v148\n\t" \
  "v_add_f32 v165, v165, v149\n\t" \
  "v_add_f32 v166, v166, v150\n\t" \
  "v_add_f32 v167, v167, v151\n\t" \
  "v_cvt_pk_bf16_f32 v168, v160, v161\n\t" \
  "v_cvt_pk_bf16_f32 v169, v162, v163\n\t" \
  "v_cvt_pk_bf16_f32 v170, v164, v165\n\t" \
  "v_cvt_pk_bf16_f32 v171, v166, v167\n\t" \
  "buffer_store_dwordx4 v[168:171], v172, %[rt], s90 offen sc1\n\t" \
  "s_branch .Lfb00%=\n\t" \
  ".Lf10%=:\n\t" \
  "s_nop 7\n\t" \
  "ds_write_b128 v220, v[136:139] offset:0\n\t" \
  "ds_write_b128 v220, v[140:143] offset:1024\n\t" \
  "s_lshr_b32 s90, s87, 7\n\t" \
  "s_xor_b32 s90, s90, %[s0]\n\t" \
  "s_and_b32 s90, s90, 1\n\t" \
  "s_lshl1_add_u32 s90, s87, s90\n\t" \
  "s_lshl_b32 s90, s90, 13\n\t" \
  "s_cmp_lt_i32 s86, 1\n\t" \
  "s_cselect_b32 s90, 0x70000000, s90\n\t" \
  "s_waitcnt lgkmcnt(0)\n\t" \
  "s_barrier\n\t" \
  "ds_read_b128 v[160:163], v221 offset:0\n\t" \
  "ds_read_b128 v[164:167], v221 offset:1024\n\t" \
  "s_waitcnt lgkmcnt(0)\n\t" \
  "v_add_f32 v160, v160, v128\n\t" \
  "v_add_f32 v161, v161, v129\n\t" \
  "v_add_f32 v162, v162, v130\n\t" \
  "v_add_f32 v163, v163, v131\n\t" \
  "v_add_f32 v164, v164, v132\n\t" \
  "v_add_f32 v165, v165, v133\n\t" \
  "v_add_f32 v166, v166, v134\n\t" \
  "v_add_f32 v167, v167, v135\n\t" \
  "v_cvt_pk_bf16_f32 v168, v160, v161\n\t" \
  "v_cvt_pk_bf16_f32 v169, v162, v163\n\t" \
  "v_cvt_pk_bf16_f32 v170, v164, v165\n\t" \
  "v_cvt_pk_bf16_f32 v171, v166, v167\n\t" \
  "buffer_store_dwordx4 v[168:171], v172, %[rt], s90 offen sc1\n\t" \
  "s_branch .Lfb10%=\n\t" \
  ".Lw00%=:\n\t" \
  "s_waitcnt vmcnt(2)\n\t" \
  "s_barrier\n\t" \
  "s_branch .Lg00%=\n\t" \
  ".Lw10%=:\n\t" \
  "s_waitcnt vmcnt(2)\n\t" \
  "s_barrier\n\t" \
  "s_branch .Lg10%=\n\t" \
  ".Lw20%=:\n\t" \
  "s_waitcnt vmcnt(2)\n\t" \
  "s_barrier\n\t" \
  "s_branch .Lg20%=\n\t" \
  ".Lw30%=:\n\t" \
  "s_waitcnt vmcnt(2)\n\t" \
  "s_barrier\n\t" \
  "s_branch .Lg30%=\n\t" \
  ".Lx0%=:\n\t" \
  "s_waitcnt lgkmcnt(0)\n\t" \
  "s_setprio 0\n\t" \
  "s_mov_b32 m0, s94\n\t" \
  "s_branch .Le%=\n\t" \
  ".Lk%=:\n\t" \
  "s_mov_b32 s94, m0\n\t" \
  "s_mov_b64 s[92:93], %[x0]\n\t" \
  "s_mov_b32 s86, %[g0]\n\t" \
  "s_add_u32 s87, s86, %[t0]\n\t" \
  "s_sub_i32 s88, s86, 3\n\t" \
  "ds_read_b128 v[176:179], v208 offset:0\n\t" \
  "ds_read_b128 v[180:183], v208 offset:16384\n\t" \
  "ds_read_b128 v[184:187], v209 offset:0\n\t" \
  "ds_read_b128 v[188:191], v209 offset:16384\n\t" \
  "ds_read_b128 v[192:195], v210 offset:0\n\t" \
  "ds_read_b128 v[196:199], v210 offset:16384\n\t" \
  "ds_read_b128 v[200:203], v211 offset:0\n\t" \
  "ds_read_b128 v[204:207], v211 offset:16384\n\t" \
  "s_branch .Lfb01%=\n\t" \
  ".Lp1%=:\n\t" \
  "s_cmp_le_u32 s86, 5\n\t" \
  "s_cbranch_scc1 .Lf01%=\n\t" \
  ".Lfb01%=:\n\t" \
  "s_waitcnt lgkmcnt(6)\n\t" \
  "v_mfma_f32_16x16x32_bf16 v[128:131], v[176:179], v[0:3], 0\n\t" \
  "v_mfma_f32_16x16x32_bf16 v[136:139], v[176:179], v[4:7], 0\n\t" \
  "ds_read_b128 v[176:179], v208 offset:256\n\t" \
  "v_mfma_f32_16x16x32_bf16 v[132:135], v[180:183], v[0:3], 0\n\t" \
  "v_mfma_f32_16x16x32_bf16 v[140:143], v[180:183], v[4:7], 0\n\t" \
  "ds_read_b128 v[180:183], v208 offset:16640\n\t" \
  "s_waitcnt lgkmcnt(6)\n\t" \
  "v_mfma_f32_16x16x32_bf16 v[128:131], v[184:187], v[8:11], v[128:131]\n\t" \
  "v_mfma_f32_16x16x32_bf16 v[136:139], v[184:187], v[12:15], v[136:139]\n\t" \
  "ds_read_b128 v[184:187], v209 offset:256\n\t" \
  "s_add_u32 m0, %[lw], 67584\n\t" \
  "v_mfma_f32_16x16x32_bf16 v[132:135], v[188:191], v[8:11], v[132:135]\n\t" \
  "v_mfma_f32_16x16x32_bf16 v[140:143], v[188:191], v[12:15], v[140:143]\n\t" \
  "ds_read_b128 v[188:191], v209 offset:16640\n\t" \
  "global_load_lds_dwordx4 v218, s[92:93] sc1\n\t" \
  "ds_write_b128 v220, v[152:155] offset:0\n\t" \
  "ds_write_b128 v220, v[156:159] offset:1024\n\t" \
  "s_waitcnt lgkmcnt(8)\n\t" \
  "v_mfma_f32_16x16x32_bf16 v[128:131], v[192:195], v[16:19], v[128:131]\n\t" \
  "v_mfma_f32_16x16x32_bf16 v[136:139], v[192:195], v[20:23], v[136:139]\n\t" \
  "ds_read_b128 v[192:195], v210 offset:256\n\t" \
  "v_mfma_f32_16x16x32_bf16 v[132:135], v[196:199], v[16:19], v[132:135]\n\t" \
  "v_mfma_f32_16x16x32_bf16 v[140:143], v[196:199], v[20:23], v[140:143]\n\t" \
  "ds_read_b128 v[196:199], v210 offset:16640\n\t" \
  "s_lshr_b32 s90, s87, 7\n\t" \
  "s_xor_b32 s90, s90, %[s0]\n\t" \
  "s_and_b32 s90, s90, 1\n\t" \
  "s_lshl1_add_u32 s90, s87, s90\n\t" \
  "s_lshl_b32 s90, s90, 13\n\t" \
  "s_cmp_lt_i32 s86, 6\n\t" \
  "s_cselect_b32 s90, 0x70000000, s90\n\t" \
  "s_waitcnt lgkmcnt(8)\n\t" \
  "v_mfma_f32_16x16x32_bf16 v[128:131], v[200:203], v[24:27], v[128:131]\n\t" \
  "v_mfma_f32_16x16x32_bf16 v[136:139], v[200:203], v[28:31], v[136:139]\n\t" \
  "ds_read_b128 v[200:203], v211 offset:256\n\t" \
  "s_add_u32 m0, %[lw], 68608\n\t" \
  "v_mfma_f32_16x16x32_bf16 v[132:135], v[204:207], v[24:27], v[132:135]\n\t" \
  "v_mfma_f32_16x16x32_bf16 v[140:143], v[204:207], v[28:31], v[140:143]\n\t" \
  "ds_read_b128 v[204:207], v211 offset:16640\n\t" \
  "global_load_lds_dwordx4 v219, s[92:93] sc1\n\t" \
  "s_waitcnt lgkmcnt(4)\n\t" \
  "s_cmp_lt_u32 s86, 6\n\t" \
  "s_cbranch_scc1 .Lw01%=\n\t" \
  "s_waitcnt vmcnt(4)\n\t" \
  "s_barrier\n\t" \
  ".Lg01%=:\n\t" \
  "s_add_u32 s92, s92, 512\n\t" \
  "s_addc_u32 s93, s93, 0\n\t" \
  "v_mfma_f32_16x16x32_bf16 v[128:131], v[176:179], v[32:35], v[128:131]\n\t" \
  "v_mfma_f32_16x16x32_bf16 v[136:139], v[176:179], v[36:39], v[136:139]\n\t" \
  "ds_read_b128 v[176:179], v208 offset:32768\n\t" \
  "v_mfma_f32_16x16x32_bf16 v[132:135], v[180:183], v[32:35], v[132:135]\n\t" \
  "v_mfma_f32_16x16x32_bf16 v[140:143], v[180:183], v[36:39], v[140:143]\n\t" \
  "ds_read_b128 v[180:183], v208 offset:49152\n\t" \
  "ds_read_b128 v[160:163], v221 offset:0\n\t" \
  "ds_read_b128 v[164:167], v221 offset:1024\n\t" \
  "s_sub_i32 s91, s86, 2\n\t" \
  "s_max_i32 s88, s88, s91\n\t" \
  "v_mov_b32 v173, s88\n\t" \
  "buffer_store_dword v173, v174, %[rs], 0 offen sc1\n\t" \
  "v_mfma_f32_16x16x32_bf16 v[128:131], v[184:187], v[40:43], v[128:131]\n\t" \
  "v_mfma_f32_16x16x32_bf16 v[136:139], v[184:187], v[44:47], v[136:139]\n\t" \
  "ds_read_b128 v[184:187], v209 offset:32768\n\t" \
  "s_add_u32 m0, %[lw], 98304\n\t" \
  "v_mfma_f32_16x16x32_bf16 v[132:135], v[188:191], v[40:43], v[132:135]\n\t" \
  "v_mfma_f32_16x16x32_bf16 v[140:143], v[188:191], v[44:47], v[140:143]\n\t" \
  "ds_read_b128 v[188:191], v209 offset:49152\n\t" \
  "global_load_lds_dwordx4 v216, s[92:93] sc1\n\t" \
  "s_waitcnt lgkmcnt(8)\n\t" \
  "v_mfma_f32_16x16x32_bf16 v[128:131], v[192:195], v[48:51], v[128:131]\n\t" \
  "v_mfma_f32_16x16x32_bf16 v[136:139], v[192:195], v[52:55], v[136:139]\n\t" \
  "ds_read_b128 v[192:195], v210 offset:32768\n\t" \
  "v_mfma_f32_16x16x32_bf16 v[132:135], v[196:199], v[48:51], v[132:135]\n\t" \
  "v_mfma_f32_16x16x32_bf16 v[140:143], v[196:199], v[52:55], v[140:143]\n\t" \
  "ds_read_b128 v[196:199], v210 offset:49152\n\t" \
  "s_waitcnt lgkmcnt(4)\n\t" \
  "v_add_f32 v160, v160, v144\n\t" \
  "v_add_f32 v161, v161, v145\n\t" \
  "v_add_f32 v162, v162, v146\n\t" \
  "v_add_f32 v163, v163, v147\n\t" \
  "v_add_f32 v164, v164, v148\n\t" \
  "v_add_f32 v165, v165, v149\n\t" \
  "v_add_f32 v166, v166, v150\n\t" \
  "v_add_f32 v167, v167, v151\n\t" \
  "v_mfma_f32_16x16x32_bf16 v[128:131], v[200:203], v[56:59], v[128:131]\n\t" \
  "v_mfma_f32_16x16x32_bf16 v[136:139], v[200:203], v[60:63], v[136:139]\n\t" \
  "ds_read_b128 v[200:203], v211 offset:32768\n\t" \
  "s_add_u32 m0, %[lw], 99328\n\t" \
  "v_mfma_f32_16x16x32_bf16 v[132:135], v[204:207], v[56:59], v[132:135]\n\t" \
  "v_mfma_f32_16x16x32_bf16 v[140:143], v[204:207], v[60:63], v[140:143]\n\t" \
  "ds_read_b128 v[204:207], v211 offset:49152\n\t" \
  "global_load_lds_dwordx4 v217, s[92:93] sc1\n\t" \
  "v_cvt_pk_bf16_f32 v168, v160, v161\n\t" \
  "v_cvt_pk_bf16_f32 v169, v162, v163\n\t" \
  "v_cvt_pk_bf16_f32 v170, v164, v165\n\t" \
  "v_cvt_pk_bf16_f32 v171, v166, v167\n\t" \
  "buffer_store_dwordx4 v[168:171], v172, %[rt], s90 offen sc1\n\t" \
  "v_mfma_f32_16x16x32_bf16 v[128:131], v[176:179], v[64:67], v[128:131]\n\t" \
  "v_mfma_f32_16x16x32_bf16 v[136:139], v[176:179], v[68:71], v[136:139]\n\t" \
  "ds_read_b128 v[176:179], v208 offset:33024\n\t" \
  "v_mfma_f32_16x16x32_bf16 v[132:135], v[180:183], v[64:67], v[132:135]\n\t" \
  "v_mfma_f32_16x16x32_bf16 v[140:143], v[180:183], v[68:71], v[140:143]\n\t" \
  "ds_read_b128 v[180:183], v208 offset:49408\n\t" \
  "s_waitcnt lgkmcnt(6)\n\t" \
  "v_mfma_f32_16x16x32_bf16 v[128:131], v[184:187], v[72:75], v[128:131]\n\t" \
  "v_mfma_f32_16x16x32_bf16 v[136:139], v[184:187], v[76:79], v[136:139]\n\t" \
  "ds_read_b128 v[184:187], v209 offset:33024\n\t" \
  "s_add_u32 m0, %[lw], 100352\n\t" \
  "v_mfma_f32_16x16x32_bf16 v[132:135], v[188:191], v[72:75], v[132:135]\n\t" \
  "v_mfma_f32_16x16x32_bf16 v[140:143], v[188:191], v[76:79], v[140:143]\n\t" \
  "ds_read_b128 v[188:191], v209 offset:49408\n\t" \
  "global_load_lds_dwordx4 v218, s[92:93] sc1\n\t" \
  "s_waitcnt lgkmcnt(6)\n\t" \
  "v_mfma_f32_16x16x32_bf16 v[128:131], v[192:195], v[80:83], v[128:131]\n\t" \
  "v_mfma_f32_16x16x32_bf16 v[136:139], v[192:195], v[84:87], v[136:139]\n\t" \
  "ds_read_b128 v[192:195], v210 offset:33024\n\t" \
  "v_mfma_f32_16x16x32_bf16 v[132:135], v[196:199], v[80:83], v[132:135]\n\t" \
  "v_mfma_f32_16x16x32_bf16 v[140:143], v[196:199], v[84:87], v[140:143]\n\t" \
  "ds_read_b128 v[196:199], v210 offset:49408\n\t" \
  "s_waitcnt lgkmcnt(6)\n\t" \
  "v_mfma_f32_16x16x32_bf16 v[128:131], v[200:203], v[88:91], v[128:131]\n\t" \
  "v_mfma_f32_16x16x32_bf16 v[136:139], v[200:203], v[92:95], v[136:139]\n\t" \
  "ds_read_b128 v[200:203], v211 offset:33024\n\t" \
  "s_add_u32 m0, %[lw], 101376\n\t" \
  "v_mfma_f32_16x16x32_bf16 v[132:135], v[204:207], v[88:91], v[132:135]\n\t" \
  "v_mfma_f32_16x16x32_bf16 v[140:143], v[204:207], v[92:95], v[140:143]\n\t" \
  "ds_read_b128 v[204:207], v211 offset:49408\n\t" \
  "global_load_lds_dwordx4 v219, s[92:93] sc1\n\t" \
  "s_cmp_lt_u32 s86, 8\n\t" \
  "s_cbranch_scc1 .Lw11%=\n\t" \
  "s_waitcnt vmcnt(6)\n\t" \
  "s_barrier\n\t" \
  ".Lg11%=:\n\t" \
  "s_add_u32 s92, s92, 65024\n\t" \
  "s_addc_u32 s93, s93, 0\n\t" \
  "s_waitcnt lgkmcnt(6)\n\t" \
  "v_mfma_f32_16x16x32_bf16 v[128:131], v[176:179], v[96:99], v[128:131]\n\t" \
  "v_mfma_f32_16x16x32_bf16 v[136:139], v[176:179], v[100:103], v[136:139]\n\t" \
  "ds_read_b128 v[176:179], v212 offset:0\n\t" \
  "v_mfma_f32_16x16x32_bf16 v[132:135], v[180:183], v[96:99], v[132:135]\n\t" \
  "v_mfma_f32_16x16x32_bf16 v[140:143], v[180:183], v[100:103], v[140:143]\n\t" \
  "ds_read_b128 v[180:183], v212 offset:16384\n\t" \
  "s_waitcnt lgkmcnt(6)\n\t" \
  "v_mfma_f32_16x16x32_bf16 v[128:131], v[184:187], v[104:107], v[128:131]\n\t" \
  "v_mfma_f32_16x16x32_bf16 v[136:139], v[184:187], v[108:111], v[136:139]\n\t" \
  "ds_read_b128 v[184:187], v213 offset:0\n\t" \
  "s_add_u32 m0, %[lw], 0\n\t" \
  "v_mfma_f32_16x16x32_bf16 v[132:135], v[188:191], v[104:107], v[132:135]\n\t" \
  "v_mfma_f32_16x16x32_bf16 v[140:143], v[188:191], v[108:111], v[140:143]\n\t" \
  "ds_read_b128 v[188:191], v213 offset:16384\n\t" \
  "global_load_lds_dwordx4 v216, s[92:93] sc1\n\t" \
  "s_waitcnt lgkmcnt(6)\n\t" \
  "v_mfma_f32_16x16x32_bf16 v[128:131], v[192:195], v[112:115], v[128:131]\n\t" \
  "v_mfma_f32_16x16x32_bf16 v[136:139], v[192:195], v[116:119], v[136:139]\n\t" \
  "ds_read_b128 v[192:195], v214 offset:0\n\t" \
  "v_mfma_f32_16x16x32_bf16 v[132:135], v[196:199], v[112:115], v[132:135]\n\t" \
  "v_mfma_f32_16x16x32_bf16 v[140:143], v[196:199], v[116:119], v[140:143]\n\t" \
  "ds_read_b128 v[196:199], v214 offset:16384\n\t" \
  "s_waitcnt lgkmcnt(6)\n\t" \
  "v_mfma_f32_16x16x32_bf16 v[128:131], v[200:203], v[120:123], v[128:131]\n\t" \
  "v_mfma_f32_16x16x32_bf16 v[136:139], v[200:203], v[124:127], v[136:139]\n\t" \
  "ds_read_b128 v[200:203], v215 offset:0\n\t" \
  "s_add_u32 m0, %[lw], 1024\n\t" \
  "v_mfma_f32_16x16x32_bf16 v[132:135], v[204:207], v[120:123], v[132:135]\n\t" \
  "v_mfma_f32_16x16x32_bf16 v[140:143], v[204:207], v[124:127], v[140:143]\n\t" \
  "ds_read_b128 v[204:207], v215 offset:16384\n\t" \
  "global_load_lds_dwordx4 v217, s[92:93] sc1\n\t" \
  "s_add_u32 s86, s86, 1\n\t" \
  "s_add_u32 s87, s87, 1\n\t" \
  "s_cmp_le_u32 s86, 5\n\t" \
  "s_cbranch_scc1 .Lf11%=\n\t" \
  ".Lfb11%=:\n\t" \
  "s_waitcnt lgkmcnt(6)\n\t" \
  "v_mfma_f32_16x16x32_bf16 v[144:147], v[176:179], v[0:3], 0\n\t" \
  "v_mfma_f32_16x16x32_bf16 v[152:155], v[176:179], v[4:7], 0\n\t" \
  "ds_read_b128 v[176:179], v212 offset:256\n\t" \
  "v_mfma_f32_16x16x32_bf16 v[148:151], v[180:183], v[0:3], 0\n\t" \
  "v_mfma_f32_16x16x32_bf16 v[156:159], v[180:183], v[4:7], 0\n\t" \
  "ds_read_b128 v[180:183], v212 offset:16640\n\t" \
  "s_waitcnt lgkmcnt(6)\n\t" \
  "v_mfma_f32_16x16x32_bf16 v[144:147], v[184:187], v[8:11], v[144:147]\n\t" \
  "v_mfma_f32_16x16x32_bf16 v[152:155], v[184:187], v[12:15], v[152:155]\n\t" \
  "ds_read_b128 v[184:187], v213 offset:256\n\t" \
  "s_add_u32 m0, %[lw], 2048\n\t" \
  "v_mfma_f32_16x16x32_bf16 v[148:151], v[188:191], v[8:11], v[148:151]\n\t" \
  "v_mfma_f32_16x16x32_bf16 v[156:159], v[188:191], v[12:15], v[156:159]\n\t" \
  "ds_read_b128 v[188:191], v213 offset:16640\n\t" \
  "global_load_lds_dwordx4 v218, s[92:93] sc1\n\t" \
  "ds_write_b128 v220, v[136:139] offset:0\n\t" \
  "ds_write_b128 v220, v[140:143] offset:1024\n\t" \
  "s_waitcnt lgkmcnt(8)\n\t" \
  "v_mfma_f32_16x16x32_bf16 v[144:147], v[192:195], v[16:19], v[144:147]\n\t" \
  "v_mfma_f32_16x16x32_bf16 v[152:155], v[192:195], v[20:23], v[152:155]\n\t" \
  "ds_read_b128 v[192:195], v214 offset:256\n\t" \
  "v_mfma_f32_16x16x32_bf16 v[148:151], v[196:199], v[16:19], v[148:151]\n\t" \
  "v_mfma_f32_16x16x32_bf16 v[156:159], v[196:199], v[20:23], v[156:159]\n\t" \
  "ds_read_b128 v[196:199], v214 offset:16640\n\t" \
  "s_lshr_b32 s90, s87, 7\n\t" \
  "s_xor_b32 s90, s90, %[s0]\n\t" \
  "s_and_b32 s90, s90, 1\n\t" \
  "s_lshl1_add_u32 s90, s87, s90\n\t" \
  "s_lshl_b32 s90, s90, 13\n\t" \
  "s_cmp_lt_i32 s86, 6\n\t" \
  "s_cselect_b32 s90, 0x70000000, s90\n\t" \
  "s_waitcnt lgkmcnt(8)\n\t" \
  "v_mfma_f32_16x16x32_bf16 v[144:147], v[200:203], v[24:27], v[144:147]\n\t" \
  "v_mfma_f32_16x16x32_bf16 v[152:155], v[200:203], v[28:31], v[152:155]\n\t" \
  "ds_read_b128 v[200:203], v215 offset:256\n\t" \
  "s_add_u32 m0, %[lw], 3072\n\t" \
  "v_mfma_f32_16x16x32_bf16 v[148:151], v[204:207], v[24:27], v[148:151]\n\t" \
  "v_mfma_f32_16x16x32_bf16 v[156:159], v[204:207], v[28:31], v[156:159]\n\t" \
  "ds_read_b128 v[204:207], v215 offset:16640\n\t" \
  "global_load_lds_dwordx4 v219, s[92:93] sc1\n\t" \
  "s_waitcnt lgkmcnt(4)\n\t" \
  "s_cmp_lt_u32 s86, 6\n\t" \
  "s_cbranch_scc1 .Lw21%=\n\t" \
  "s_waitcnt vmcnt(4)\n\t" \
  "s_barrier\n\t" \
  ".Lg21%=:\n\t" \
  "s_add_u32 s92, s92, 512\n\t" \
  "s_addc_u32 s93, s93, 0\n\t" \
  "v_mfma_f32_16x16x32_bf16 v[144:147], v[176:179], v[32:35], v[144:147]\n\t" \
  "v_mfma_f32_16x16x32_bf16 v[152:155], v[176:179], v[36:39], v[152:155]\n\t" \
  "ds_read_b128 v[176:179], v212 offset:32768\n\t" \
  "v_mfma_f32_16x16x32_bf16 v[148:151], v[180:183], v[32:35], v[148:151]\n\t" \
  "v_mfma_f32_16x16x32_bf16 v[156:159], v[180:183], v[36:39], v[156:159]\n\t" \
  "ds_read_b128 v[180:183], v212 offset:49152\n\t" \
  "ds_read_b128 v[160:163], v221 offset:0\n\t" \
  "ds_read_b128 v[164:167], v221 offset:1024\n\t" \
  "s_sub_i32 s91, s86, 2\n\t" \
  "s_max_i32 s88, s88, s91\n\t" \
  "v_mov_b32 v173, s88\n\t" \
  "buffer_store_dword v173, v174, %[rs], 0 offen sc1\n\t" \
  "v_mfma_f32_16x16x32_bf16 v[144:147], v[184:187], v[40:43], v[144:147]\n\t" \
  "v_mfma_f32_16x16x32_bf16 v[152:155], v[184:187], v[44:47], v[152:155]\n\t" \
  "ds_read_b128 v[184:187], v213 offset:32768\n\t" \
  "s_add_u32 m0, %[lw], 32768\n\t" \
  "v_mfma_f32_16x16x32_bf16 v[148:151], v[188:191], v[40:43], v[148:151]\n\t" \
  "v_mfma_f32_16x16x32_bf16 v[156:159], v[188:191], v[44:47], v[156:159]\n\t" \
  "ds_read_b128 v[188:191], v213 offset:49152\n\t" \
  "global_load_lds_dwordx4 v216, s[92:93] sc1\n\t" \
  "s_waitcnt lgkmcnt(8)\n\t" \
  "v_mfma_f32_16x16x32_bf16 v[144:147], v[192:195], v[48:51], v[144:147]\n\t" \
  "v_mfma_f32_16x16x32_bf16 v[152:155], v[192:195], v[52:55], v[152:155]\n\t" \
  "ds_read_b128 v[192:195], v214 offset:32768\n\t" \
  "v_mfma_f32_16x16x32_bf16 v[148:151], v[196:199], v[48:51], v[148:151]\n\t" \
  "v_mfma_f32_16x16x32_bf16 v[156:159], v[196:199], v[52:55], v[156:159]\n\t" \
  "ds_read_b128 v[196:199], v214 offset:49152\n\t" \
  "s_waitcnt lgkmcnt(4)\n\t" \
  "v_add_f32 v160, v160, v128\n\t" \
  "v_add_f32 v161, v161, v129\n\t" \
  "v_add_f32 v162, v162, v130\n\t" \
  "v_add_f32 v163, v163, v131\n\t" \
  "v_add_f32 v164, v164, v132\n\t" \
  "v_add_f32 v165, v165, v133\n\t" \
  "v_add_f32 v166, v166, v134\n\t" \
  "v_add_f32 v167, v167, v135\n\t" \
  "v_mfma_f32_16x16x32_bf16 v[144:147], v[200:203], v[56:59], v[144:147]\n\t" \
  "v_mfma_f32_16x16x32_bf16 v[152:155], v[200:203], v[60:63], v[152:155]\n\t" \
  "ds_read_b128 v[200:203], v215 offset:32768\n\t" \
  "s_add_u32 m0, %[lw], 33792\n\t" \
  "v_mfma_f32_16x16x32_bf16 v[148:151], v[204:207], v[56:59], v[148:151]\n\t" \
  "v_mfma_f32_16x16x32_bf16 v[156:159], v[204:207], v[60:63], v[156:159]\n\t" \
  "ds_read_b128 v[204:207], v215 offset:49152\n\t" \
  "global_load_lds_dwordx4 v217, s[92:93] sc1\n\t" \
  "v_cvt_pk_bf16_f32 v168, v160, v161\n\t" \
  "v_cvt_pk_bf16_f32 v169, v162, v163\n\t" \
  "v_cvt_pk_bf16_f32 v170, v164, v165\n\t" \
  "v_cvt_pk_bf16_f32 v171, v166, v167\n\t" \
  "buffer_store_dwordx4 v[168:171], v172, %[rt], s90 offen sc1\n\t" \
  "v_mfma_f32_16x16x32_bf16 v[144:147], v[176:179], v[64:67], v[144:147]\n\t" \
  "v_mfma_f32_16x16x32_bf16 v[152:155], v[176:179], v[68:71], v[152:155]\n\t" \
  "ds_read_b128 v[176:179], v212 offset:33024\n\t" \
  "v_mfma_f32_16x16x32_bf16 v[148:151], v[180:183], v[64:67], v[148:151]\n\t" \
  "v_mfma_f32_16x16x32_bf16 v[156:159], v[180:183], v[68:71], v[156:159]\n\t" \
  "ds_read_b128 v[180:183], v212 offset:49408\n\t" \
  "s_waitcnt lgkmcnt(6)\n\t" \
  "v_mfma_f32_16x16x32_bf16 v[144:147], v[184:187], v[72:75], v[144:147]\n\t" \
  "v_mfma_f32_16x16x32_bf16 v[152:155], v[184:187], v[76:79], v[152:155]\n\t" \
  "ds_read_b128 v[184:187], v213 offset:33024\n\t" \
  "s_add_u32 m0, %[lw], 34816\n\t" \
  "v_mfma_f32_16x16x32_bf16 v[148:151], v[188:191], v[72:75], v[148:151]\n\t" \
  "v_mfma_f32_16x16x32_bf16 v[156:159], v[188:191], v[76:79], v[156:159]\n\t" \
  "ds_read_b128 v[188:191], v213 offset:49408\n\t" \
  "global_load_lds_dwordx4 v218, s[92:93] sc1\n\t" \
  "s_waitcnt lgkmcnt(6)\n\t" \
  "v_mfma_f32_16x16x32_bf16 v[144:147], v[192:195], v[80:83], v[144:147]\n\t" \
  "v_mfma_f32_16x16x32_bf16 v[152:155], v[192:195], v[84:87], v[152:155]\n\t" \
  "ds_read_b128 v[192:195], v214 offset:33024\n\t" \
  "v_mfma_f32_16x16x32_bf16 v[148:151], v[196:199], v[80:83], v[148:151]\n\t" \
  "v_mfma_f32_16x16x32_bf16 v[156:159], v[196:199], v[84:87], v[156:159]\n\t" \
  "ds_read_b128 v[196:199], v214 offset:49408\n\t" \
  "s_waitcnt lgkmcnt(6)\n\t" \
  "v_mfma_f32_16x16x32_bf16 v[144:147], v[200:203], v[88:91], v[144:147]\n\t" \
  "v_mfma_f32_16x16x32_bf16 v[152:155], v[200:203], v[92:95], v[152:155]\n\t" \
  "ds_read_b128 v[200:203], v215 offset:33024\n\t" \
  "s_add_u32 m0, %[lw], 35840\n\t" \
  "v_mfma_f32_16x16x32_bf16 v[148:151], v[204:207], v[88:91], v[148:151]\n\t" \
  "v_mfma_f32_16x16x32_bf16 v[156:159], v[204:207], v[92:95], v[156:159]\n\t" \
  "ds_read_b128 v[204:207], v215 offset:49408\n\t" \
  "global_load_lds_dwordx4 v219, s[92:93] sc1\n\t" \
  "s_cmp_lt_u32 s86, 8\n\t" \
  "s_cbranch_scc1 .Lw31%=\n\t" \
  "s_waitcnt vmcnt(6)\n\t" \
  "s_barrier\n\t" \
  ".Lg31%=:\n\t" \
  "s_add_u32 s92, s92, 65024\n\t" \
  "s_addc_u32 s93, s93, 0\n\t" \
  "s_waitcnt lgkmcnt(6)\n\t" \
  "v_mfma_f32_16x16x32_bf16 v[144:147], v[176:179], v[96:99], v[144:147]\n\t" \
  "v_mfma_f32_16x16x32_bf16 v[152:155], v[176:179], v[100:103], v[152:155]\n\t" \
  "ds_read_b128 v[176:179], v208 offset:0\n\t" \
  "v_mfma_f32_16x16x32_bf16 v[148:151], v[180:183], v[96:99], v[148:151]\n\t" \
  "v_mfma_f32_16x16x32_bf16 v[156:159], v[180:183], v[100:103], v[156:159]\n\t" \
  "ds_read_b128 v[180:183], v208 offset:16384\n\t" \
  "s_waitcnt lgkmcnt(6)\n\t" \
  "v_mfma_f32_16x16x32_bf16 v[144:147], v[184:187], v[104:107], v[144:147]\n\t" \
  "v_mfma_f32_16x16x32_bf16 v[152:155], v[184:187], v[108:111], v[152:155]\n\t" \
  "ds_read_b128 v[184:187], v209 offset:0\n\t" \
  "s_add_u32 m0, %[lw], 65536\n\t" \
  "v_mfma_f32_16x16x32_bf16 v[148:151], v[188:191], v[104:107], v[148:151]\n\t" \
  "v_mfma_f32_16x16x32_bf16 v[156:159], v[188:191], v[108:111], v[156:159]\n\t" \
  "ds_read_b128 v[188:191], v209 offset:16384\n\t" \
  "global_load_lds_dwordx4 v216, s[92:93] sc1\n\t" \
  "s_waitcnt lgkmcnt(6)\n\t" \
  "v_mfma_f32_16x16x32_bf16 v[144:147], v[192:195], v[112:115], v[144:147]\n\t" \
  "v_mfma_f32_16x16x32_bf16 v[152:155], v[192:195], v[116:119], v[152:155]\n\t" \
  "ds_read_b128 v[192:195], v210 offset:0\n\t" \
  "v_mfma_f32_16x16x32_bf16 v[148:151], v[196:199], v[112:115], v[148:151]\n\t" \
  "v_mfma_f32_16x16x32_bf16 v[156:159], v[196:199], v[116:119], v[156:159]\n\t" \
  "ds_read_b128 v[196:199], v210 offset:16384\n\t" \
  "s_waitcnt lgkmcnt(6)\n\t" \
  "v_mfma_f32_16x16x32_bf16 v[144:147], v[200:203], v[120:123], v[144:147]\n\t" \
  "v_mfma_f32_16x16x32_bf16 v[152:155], v[200:203], v[124:127], v[152:155]\n\t" \
  "ds_read_b128 v[200:203], v211 offset:0\n\t" \
  "s_add_u32 m0, %[lw], 66560\n\t" \
  "v_mfma_f32_16x16x32_bf16 v[148:151], v[204:207], v[120:123], v[148:151]\n\t" \
  "v_mfma_f32_16x16x32_bf16 v[156:159], v[204:207], v[124:127], v[156:159]\n\t" \
  "ds_read_b128 v[204:207], v211 offset:16384\n\t" \
  "global_load_lds_dwordx4 v217, s[92:93] sc1\n\t" \
  "s_add_u32 s86, s86, 1\n\t" \
  "s_add_u32 s87, s87, 1\n\t" \
  "s_cmp_lt_u32 s86, %[end]\n\t" \
  "s_cbranch_scc1 .Lp1%=\n\t" \
  "s_branch .Lx1%=\n\t" \
  ".Lf01%=:\n\t" \
  "s_nop 7\n\t" \
  "ds_write_b128 v220, v[152:155] offset:0\n\t" \
  "ds_write_b128 v220, v[156:159] offset:1024\n\t" \
  "s_lshr_b32 s90, s87, 7\n\t" \
  "s_xor_b32 s90, s90, %[s0]\n\t" \
  "s_and_b32 s90, s90, 1\n\t" \
  "s_lshl1_add_u32 s90, s87, s90\n\t" \
  "s_lshl_b32 s90, s90, 13\n\t" \
  "s_cmp_lt_i32 s86, 1\n\t" \
  "s_cselect_b32 s90, 0x70000000, s90\n\t" \
  "s_waitcnt lgkmcnt(0)\n\t" \
  "s_barrier\n\t" \
  "ds_read_b128 v[160:163], v221 offset:0\n\t" \
  "ds_read_b128 v[164:167], v221 offset:1024\n\t" \
  "s_waitcnt lgkmcnt(0)\n\t" \
  "v_add_f32 v160, v160, v144\n\t" \
  "v_add_f32 v161, v161, v145\n\t" \
  "v_add_f32 v162, v162, v146\n\t" \
  "v_add_f32 v163, v163, v147\n\t" \
  "v_add_f32 v164, v164, v148\n\t" \
  "v_add_f32 v165, v165, v149\n\t" \
  "v_add_f32 v166, v166, v150\n\t" \
  "v_add_f32 v167, v167, v151\n\t" \
  "v_cvt_pk_bf16_f32 v168, v160, v161\n\t" \
  "v_cvt_pk_bf16_f32 v169, v162, v163\n\t" \
  "v_cvt_pk_bf16_f32 v170, v164, v165\n\t" \
  "v_cvt_pk_bf16_f32 v171, v166, v167\n\t" \
  "buffer_store_dwordx4 v[168:171], v172, %[rt], s90 offen sc1\n\t" \
  "s_branch .Lfb01%=\n\t" \
  ".Lf11%=:\n\t" \
  "s_nop 7\n\t" \
  "ds_write_b128 v220, v[136:139] offset:0\n\t" \
  "ds_write_b128 v220, v[140:143] offset:1024\n\t" \
  "s_lshr_b32 s90, s87, 7\n\t" \
  "s_xor_b32 s90, s90, %[s0]\n\t" \
  "s_and_b32 s90, s90, 1\n\t" \
  "s_lshl1_add_u32 s90, s87, s90\n\t" \
  "s_lshl_b32 s90, s90, 13\n\t" \
  "s_cmp_lt_i32 s86, 1\n\t" \
  "s_cselect_b32 s90, 0x70000000, s90\n\t" \
  "s_waitcnt lgkmcnt(0)\n\t" \
  "s_barrier\n\t" \
  "ds_read_b128 v[160:163], v221 offset:0\n\t" \
  "ds_read_b128 v[164:167], v221 offset:1024\n\t" \
  "s_waitcnt lgkmcnt(0)\n\t" \
  "v_add_f32 v160, v160, v128\n\t" \
  "v_add_f32 v161, v161, v129\n\t" \
  "v_add_f32 v162, v162, v130\n\t" \
  "v_add_f32 v163, v163, v131\n\t" \
  "v_add_f32 v164, v164, v132\n\t" \
  "v_add_f32 v165, v165, v133\n\t" \
  "v_add_f32 v166, v166, v134\n\t" \
  "v_add_f32 v167, v167, v135\n\t" \
  "v_cvt_pk_bf16_f32 v168, v160, v161\n\t" \
  "v_cvt_pk_bf16_f32 v169, v162, v163\n\t" \
  "v_cvt_pk_bf16_f32 v170, v164, v165\n\t" \
  "v_cvt_pk_bf16_f32 v171, v166, v167\n\t" \
  "buffer_store_dwordx4 v[168:171], v172, %[rt], s90 offen sc1\n\t" \
  "s_branch .Lfb11%=\n\t" \
  ".Lw01%=:\n\t" \
  "s_waitcnt vmcnt(2)\n\t" \
  "s_barrier\n\t" \
  "s_sub_i32 s91, s86, 1\n\t" \
  "s_max_i32 s88, s88, s91\n\t" \
  "v_mov_b32 v173, s88\n\t" \
  "buffer_store_dword v173, v174, %[rs], 0 offen sc1\n\t" \
  "s_branch .Lg01%=\n\t" \
  ".Lw11%=:\n\t" \
  "s_waitcnt vmcnt(2)\n\t" \
  "s_barrier\n\t" \
  "s_sub_i32 s91, s86, 1\n\t" \
  "s_max_i32 s88, s88, s91\n\t" \
  "v_mov_b32 v173, s88\n\t" \
  "buffer_store_dword v173, v174, %[rs], 0 offen sc1\n\t" \
  "s_branch .Lg11%=\n\t" \
  ".Lw21%=:\n\t" \
  "s_waitcnt vmcnt(2)\n\t" \
  "s_barrier\n\t" \
  "s_sub_i32 s91, s86, 1\n\t" \
  "s_max_i32 s88, s88, s91\n\t" \
  "v_mov_b32 v173, s88\n\t" \
  "buffer_store_dword v173, v174, %[rs], 0 offen sc1\n\t" \
  "s_branch .Lg21%=\n\t" \
  ".Lw31%=:\n\t" \
  "s_waitcnt vmcnt(2)\n\t" \
  "s_barrier\n\t" \
  "s_sub_i32 s91, s86, 1\n\t" \
  "s_max_i32 s88, s88, s91\n\t" \
  "v_mov_b32 v173, s88\n\t" \
  "buffer_store_dword v173, v174, %[rs], 0 offen sc1\n\t" \
  "s_branch .Lg31%=\n\t" \
  ".Lx1%=:\n\t" \
  "s_waitcnt lgkmcnt(0)\n\t" \
  "s_setprio 0\n\t" \
  "s_mov_b32 m0, s94\n\t" \
  ".Le%=:\n\t"
#define PP_ASM_0 \
  "s_cmp_lg_u32 %[kh], 0\n\t" \
  "s_cbranch_scc1 .Lk%=\n\t" \
  "s_mov_b32 s94, m0\n\t" \
  "s_mov_b64 s[92:93], %[x0]\n\t" \
  "s_mov_b32 s86, %[g0]\n\t" \
  "s_add_u32 s87, s86, %[t0]\n\t" \
  "s_add_u32 m0, %[lw], 67584\n\t" \
  "s_nop 0\n\t" \
  "global_load_lds_dwordx4 v250, s[92:93] sc1\n\t" \
  "s_add_u32 m0, %[lw], 68608\n\t" \
  "s_nop 0\n\t" \
  "global_load_lds_dwordx4 v251, s[92:93] sc1\n\t" \
  "s_barrier\n\t" \
  "s_branch .Ls0%=\n\t" \
  ".Lp0%=:\n\t" \
  "s_waitcnt vmcnt(9)\n\t" \
  "s_barrier\n\t" \
  ".Ls0%=:\n\t" \
  "s_add_u32 s92, s92, 512\n\t" \
  "s_addc_u32 s93, s93, 0\n\t" \
  "ds_read_b128 v[176:179], v240 offset:0\n\t" \
  "ds_read_b128 v[180:183], v240 offset:16384\n\t" \
  "ds_read_b128 v[184:187], v241 offset:0\n\t" \
  "ds_read_b128 v[188:191], v241 offset:16384\n\t" \
  "s_add_u32 m0, %[lw], 98304\n\t" \
  "s_nop 0\n\t" \
  "global_load_lds_dwordx4 v248, s[92:93] sc1\n\t" \
  "ds_read_b128 v[192:195], v242 offset:0\n\t" \
  "ds_read_b128 v[196:199], v242 offset:16384\n\t" \
  "ds_read_b128 v[200:203], v243 offset:0\n\t" \
  "ds_read_b128 v[204:207], v243 offset:16384\n\t" \
  "s_add_u32 m0, %[lw], 99328\n\t" \
  "s_nop 0\n\t" \
  "global_load_lds_dwordx4 v249, s[92:93] sc1\n\t" \
  "ds_read_b128 v[208:211], v240 offset:256\n\t" \
  "ds_read_b128 v[212:215], v240 offset:16640\n\t" \
  "ds_read_b128 v[216:219], v241 offset:256\n\t" \
  "ds_read_b128 v[220:223], v241 offset:16640\n\t" \
  "s_add_u32 m0, %[lw], 100352\n\t" \
  "s_nop 0\n\t" \
  "global_load_lds_dwordx4 v250, s[92:93] sc1\n\t" \
  "ds_read_b128 v[224:227], v242 offset:256\n\t" \
  "ds_read_b128 v[228:231], v242 offset:16640\n\t" \
  "ds_read_b128 v[232:235], v243 offset:256\n\t" \
  "ds_read_b128 v[236:239], v243 offset:16640\n\t" \
  "s_add_u32 m0, %[lw], 101376\n\t" \
  "s_nop 0\n\t" \
  "global_load_lds_dwordx4 v251, s[92:93] sc1\n\t" \
  "ds_write_b128 v252, v[152:155] offset:0\n\t" \
  "ds_write_b128 v252, v[156:159] offset:1024\n\t" \
  "s_waitcnt lgkmcnt(0)\n\t" \
  "s_setprio 1\n\t" \
  "v_mfma_f32_16x16x32_bf16 v[128:131], v[0:3], v[176:179], 0\n\t" \
  "v_mfma_f32_16x16x32_bf16 v[132:135], v[4:7], v[176:179], 0\n\t" \
  "v_mfma_f32_16x16x32_bf16 v[136:139], v[0:3], v[180:183], 0\n\t" \
  "v_mfma_f32_16x16x32_bf16 v[140:143], v[4:7], v[180:183], 0\n\t" \
  "v_mfma_f32_16x16x32_bf16 v[128:131], v[8:11], v[184:187], v[128:131]\n\t" \
  "v_mfma_f32_16x16x32_bf16 v[132:135], v[12:15], v[184:187], v[132:135]\n\t" \
  "v_mfma_f32_16x16x32_bf16 v[136:139], v[8:11], v[188:191], v[136:139]\n\t" \
  "v_mfma_f32_16x16x32_bf16 v[140:143], v[12:15], v[188:191], v[140:143]\n\t" \
  "v_mfma_f32_16x16x32_bf16 v[128:131], v[16:19], v[192:195], v[128:131]\n\t" \
  "v_mfma_f32_16x16x32_bf16 v[132:135], v[20:23], v[192:195], v[132:135]\n\t" \
  "v_mfma_f32_16x16x32_bf16 v[136:139], v[16:19], v[196:199], v[136:139]\n\t" \
  "v_mfma_f32_16x16x32_bf16 v[140:143], v[20:23], v[196:199], v[140:143]\n\t" \
  "v_mfma_f32_16x16x32_bf16 v[128:131], v[24:27], v[200:203], v[128:131]\n\t" \
  "v_mfma_f32_16x16x32_bf16 v[132:135], v[28:31], v[200:203], v[132:135]\n\t" \
  "v_mfma_f32_16x16x32_bf16 v[136:139], v[24:27], v[204:207], v[136:139]\n\t" \
  "v_mfma_f32_16x16x32_bf16 v[140:143], v[28:31], v[204:207], v[140:143]\n\t" \
  "v_mfma_f32_16x16x32_bf16 v[128:131], v[32:35], v[208:211], v[128:131]\n\t" \
  "v_mfma_f32_16x16x32_bf16 v[132:135], v[36:39], v[208:211], v[132:135]\n\t" \
  "v_mfma_f32_16x16x32_bf16 v[136:139], v[32:35], v[212:215], v[136:139]\n\t" \
  "v_mfma_f32_16x16x32_bf16 v[140:143], v[36:39], v[212:215], v[140:143]\n\t" \
  "v_mfma_f32_16x16x32_bf16 v[128:131], v[40:43], v[216:219], v[128:131]\n\t" \
  "v_mfma_f32_16x16x32_bf16 v[132:135], v[44:47], v[216:219], v[132:135]\n\t" \
  "v_mfma_f32_16x16x32_bf16 v[136:139], v[40:43], v[220:223], v[136:139]\n\t" \
  "v_mfma_f32_16x16x32_bf16 v[140:143], v[44:47], v[220:223], v[140:143]\n\t" \
  "v_mfma_f32_16x16x32_bf16 v[128:131], v[48:51], v[224:227], v[128:131]\n\t" \
  "v_mfma_f32_16x16x32_bf16 v[132:135], v[52:55], v[224:227], v[132:135]\n\t" \
  "v_mfma_f32_16x16x32_bf16 v[136:139], v[48:51], v[228:231], v[136:139]\n\t" \
  "v_mfma_f32_16x16x32_bf16 v[140:143], v[52:55], v[228:231], v[140:143]\n\t" \
  "v_mfma_f32_16x16x32_bf16 v[128:131], v[56:59], v[232:235], v[128:131]\n\t" \
  "v_mfma_f32_16x16x32_bf16 v[132:135], v[60:63], v[232:235], v[132:135]\n\t" \
  "v_mfma_f32_16x16x32_bf16 v[136:139], v[56:59], v[236:239], v[136:139]\n\t" \
  "v_mfma_f32_16x16x32_bf16 v[140:143], v[60:63], v[236:239], v[140:143]\n\t" \
  "s_setprio 0\n\t" \
  "s_waitcnt vmcnt(4)\n\t" \
  "s_barrier\n\t" \
  "s_add_u32 s92, s92, 65024\n\t" \
  "s_addc_u32 s93, s93, 0\n\t" \
  "ds_read_b128 v[160:163], v253 offset:0\n\t" \
  "ds_read_b128 v[164:167], v253 offset:1024\n\t" \
  "ds_read_b128 v[176:179], v240 offset:32768\n\t" \
  "ds_read_b128 v[180:183], v240 offset:49152\n\t" \
  "ds_read_b128 v[184:187], v241 offset:32768\n\t" \
  "ds_read_b128 v[188:191], v241 offset:49152\n\t" \
  "s_add_u32 m0, %[lw], 0\n\t" \
  "s_nop 0\n\t" \
  "global_load_lds_dwordx4 v248, s[92:93] sc1\n\t" \
  "ds_read_b128 v[192:195], v242 offset:32768\n\t" \
  "ds_read_b128 v[196:199], v242 offset:49152\n\t" \
  "ds_read_b128 v[200:203], v243 offset:32768\n\t" \
  "ds_read_b128 v[204:207], v243 offset:49152\n\t" \
  "s_add_u32 m0, %[lw], 1024\n\t" \
  "s_nop 0\n\t" \
  "global_load_lds_dwordx4 v249, s[92:93] sc1\n\t" \
  "ds_read_b128 v[208:211], v240 offset:33024\n\t" \
  "ds_read_b128 v[212:215], v240 offset:49408\n\t" \
  "ds_read_b128 v[216:219], v241 offset:33024\n\t" \
  "ds_read_b128 v[220:223], v241 offset:49408\n\t" \
  "s_add_u32 m0, %[lw], 2048\n\t" \
  "s_nop 0\n\t" \
  "global_load_lds_dwordx4 v250, s[92:93] sc1\n\t" \
  "ds_read_b128 v[224:227], v242 offset:33024\n\t" \
  "ds_read_b128 v[228:231], v242 offset:49408\n\t" \
  "ds_read_b128 v[232:235], v243 offset:33024\n\t" \
  "ds_read_b128 v[236:239], v243 offset:49408\n\t" \
  "s_add_u32 m0, %[lw], 3072\n\t" \
  "s_nop 0\n\t" \
  "global_load_lds_dwordx4 v251, s[92:93] sc1\n\t" \
  "s_waitcnt lgkmcnt(15)\n\t" \
  "s_waitcnt lgkmcnt(12)\n\t" \
  "s_lshr_b32 s90, s87, 7\n\t" \
  "s_xor_b32 s90, s90, %[s0]\n\t" \
  "s_and_b32 s90, s90, 1\n\t" \
  "s_lshl1_add_u32 s90, s87, s90\n\t" \
  "s_lshl_b32 s90, s90, 13\n\t" \
  "v_add_f32 v160, v160, v144\n\t" \
  "v_add_f32 v161, v161, v145\n\t" \
  "v_add_f32 v162, v162, v146\n\t" \
  "v_add_f32 v163, v163, v147\n\t" \
  "v_add_f32 v164, v164, v148\n\t" \
  "v_add_f32 v165, v165, v149\n\t" \
  "v_add_f32 v166, v166, v150\n\t" \
  "v_add_f32 v167, v167, v151\n\t" \
  "v_cvt_pk_bf16_f32 v168, v160, v161\n\t" \
  "v_cvt_pk_bf16_f32 v169, v162, v163\n\t" \
  "v_cvt_pk_bf16_f32 v170, v164, v165\n\t" \
  "v_cvt_pk_bf16_f32 v171, v166, v167\n\t" \
  "buffer_store_dwordx4 v[168:171], v172, %[rt], s90 offen sc1\n\t" \
  "s_add_u32 s86, s86, 1\n\t" \
  "s_add_u32 s87, s87, 1\n\t" \
  "s_waitcnt lgkmcnt(0)\n\t" \
  "s_setprio 1\n\t" \
  "v_mfma_f32_16x16x32_bf16 v[128:131], v[64:67], v[176:179], v[128:131]\n\t" \
  "v_mfma_f32_16x16x32_bf16 v[132:135], v[68:71], v[176:179], v[132:135]\n\t" \
  "v_mfma_f32_16x16x32_bf16 v[136:139], v[64:67], v[180:183], v[136:139]\n\t" \
  "v_mfma_f32_16x16x32_bf16 v[140:143], v[68:71], v[180:183], v[140:143]\n\t" \
  "v_mfma_f32_16x16x32_bf16 v[128:131], v[72:75], v[184:187], v[128:131]\n\t" \
  "v_mfma_f32_16x16x32_bf16 v[132:135], v[76:79], v[184:187], v[132:135]\n\t" \
  "v_mfma_f32_16x16x32_bf16 v[136:139], v[72:75], v[188:191], v[136:139]\n\t" \
  "v_mfma_f32_16x16x32_bf16 v[140:143], v[76:79], v[188:191], v[140:143]\n\t" \
  "v_mfma_f32_16x16x32_bf16 v[128:131], v[80:83], v[192:195], v[128:131]\n\t" \
  "v_mfma_f32_16x16x32_bf16 v[132:135], v[84:87], v[192:195], v[132:135]\n\t" \
  "v_mfma_f32_16x16x32_bf16 v[136:139], v[80:83], v[196:199], v[136:139]\n\t" \
  "v_mfma_f32_16x16x32_bf16 v[140:143], v[84:87], v[196:199], v[140:143]\n\t" \
  "v_mfma_f32_16x16x32_bf16 v[128:131], v[88:91], v[200:203], v[128:131]\n\t" \
  "v_mfma_f32_16x16x32_bf16 v[132:135], v[92:95], v[200:203], v[132:135]\n\t" \
  "v_mfma_f32_16x16x32_bf16 v[136:139], v[88:91], v[204:207], v[136:139]\n\t" \
  "v_mfma_f32_16x16x32_bf16 v[140:143], v[92:95], v[204:207], v[140:143]\n\t" \
  "v_mfma_f32_16x16x32_bf16 v[128:131], v[96:99], v[208:211], v[128:131]\n\t" \
  "v_mfma_f32_16x16x32_bf16 v[132:135], v[100:103], v[208:211], v[132:135]\n\t" \
  "v_mfma_f32_16x16x32_bf16 v[136:139], v[96:99], v[212:215], v[136:139]\n\t" \
  "v_mfma_f32_16x16x32_bf16 v[140:143], v[100:103], v[212:215], v[140:143]\n\t" \
  "v_mfma_f32_16x16x32_bf16 v[128:131], v[104:107], v[216:219], v[128:131]\n\t" \
  "v_mfma_f32_16x16x32_bf16 v[132:135], v[108:111], v[216:219], v[132:135]\n\t" \
  "v_mfma_f32_16x16x32_bf16 v[136:139], v[104:107], v[220:223], v[136:139]\n\t" \
  "v_mfma_f32_16x16x32_bf16 v[140:143], v[108:111], v[220:223], v[140:143]\n\t" \
  "v_mfma_f32_16x16x32_bf16 v[128:131], v[112:115], v[224:227], v[128:131]\n\t" \
  "v_mfma_f32_16x16x32_bf16 v[132:135], v[116:119], v[224:227], v[132:135]\n\t" \
  "v_mfma_f32_16x16x32_bf16 v[136:139], v[112:115], v[228:231], v[136:139]\n\t" \
  "v_mfma_f32_16x16x32_bf16 v[140:143], v[116:119], v[228:231], v[140:143]\n\t" \
  "v_mfma_f32_16x16x32_bf16 v[128:131], v[120:123], v[232:235], v[128:131]\n\t" \
  "v_mfma_f32_16x16x32_bf16 v[132:135], v[124:127], v[232:235], v[132:135]\n\t" \
  "v_mfma_f32_16x16x32_bf16 v[136:139], v[120:123], v[236:239], v[136:139]\n\t" \
  "v_mfma_f32_16x16x32_bf16 v[140:143], v[124:127], v[236:239], v[140:143]\n\t" \
  "s_setprio 0\n\t" \
  "s_waitcnt vmcnt(9)\n\t" \
  "s_barrier\n\t" \
  "s_add_u32 s92, s92, 512\n\t" \
  "s_addc_u32 s93, s93, 0\n\t" \
  "ds_read_b128 v[176:179], v244 offset:0\n\t" \
  "ds_read_b128 v[180:183], v244 offset:16384\n\t" \
  "ds_read_b128 v[184:187], v245 offset:0\n\t" \
  "ds_read_b128 v[188:191], v245 offset:16384\n\t" \
  "s_add_u32 m0, %[lw], 32768\n\t" \
  "s_nop 0\n\t" \
  "global_load_lds_dwordx4 v248, s[92:93] sc1\n\t" \
  "ds_read_b128 v[192:195], v246 offset:0\n\t" \
  "ds_read_b128 v[196:199], v246 offset:16384\n\t" \
  "ds_read_b128 v[200:203], v247 offset:0\n\t" \
  "ds_read_b128 v[204:207], v247 offset:16384\n\t" \
  "s_add_u32 m0, %[lw], 33792\n\t" \
  "s_nop 0\n\t" \
  "global_load_lds_dwordx4 v249, s[92:93] sc1\n\t" \
  "ds_read_b128 v[208:211], v244 offset:256\n\t" \
  "ds_read_b128 v[212:215], v244 offset:16640\n\t" \
  "ds_read_b128 v[216:219], v245 offset:256\n\t" \
  "ds_read_b128 v[220:223], v245 offset:16640\n\t" \
  "s_add_u32 m0, %[lw], 34816\n\t" \
  "s_nop 0\n\t" \
  "global_load_lds_dwordx4 v250, s[92:93] sc1\n\t" \
  "ds_read_b128 v[224:227], v246 offset:256\n\t" \
  "ds_read_b128 v[228:231], v246 offset:16640\n\t" \
  "ds_read_b128 v[232:235], v247 offset:256\n\t" \
  "ds_read_b128 v[236:239], v247 offset:16640\n\t" \
  "s_add_u32 m0, %[lw], 35840\n\t" \
  "s_nop 0\n\t" \
  "global_load_lds_dwordx4 v251, s[92:93] sc1\n\t" \
  "ds_write_b128 v252, v[136:139] offset:0\n\t" \
  "ds_write_b128 v252, v[140:143] offset:1024\n\t" \
  "s_waitcnt lgkmcnt(0)\n\t" \
  "s_setprio 1\n\t" \
  "v_mfma_f32_16x16x32_bf16 v[144:147], v[0:3], v[176:179], 0\n\t" \
  "v_mfma_f32_16x16x32_bf16 v[148:151], v[4:7], v[176:179], 0\n\t" \
  "v_mfma_f32_16x16x32_bf16 v[152:155], v[0:3], v[180:183], 0\n\t" \
  "v_mfma_f32_16x16x32_bf16 v[156:159], v[4:7], v[180:183], 0\n\t" \
  "v_mfma_f32_16x16x32_bf16 v[144:147], v[8:11], v[184:187], v[144:147]\n\t" \
  "v_mfma_f32_16x16x32_bf16 v[148:151], v[12:15], v[184:187], v[148:151]\n\t" \
  "v_mfma_f32_16x16x32_bf16 v[152:155], v[8:11], v[188:191], v[152:155]\n\t" \
  "v_mfma_f32_16x16x32_bf16 v[156:159], v[12:15], v[188:191], v[156:159]\n\t" \
  "v_mfma_f32_16x16x32_bf16 v[144:147], v[16:19], v[192:195], v[144:147]\n\t" \
  "v_mfma_f32_16x16x32_bf16 v[148:151], v[20:23], v[192:195], v[148:151]\n\t" \
  "v_mfma_f32_16x16x32_bf16 v[152:155], v[16:19], v[196:199], v[152:155]\n\t" \
  "v_mfma_f32_16x16x32_bf16 v[156:159], v[20:23], v[196:199], v[156:159]\n\t" \
  "v_mfma_f32_16x16x32_bf16 v[144:147], v[24:27], v[200:203], v[144:147]\n\t" \
  "v_mfma_f32_16x16x32_bf16 v[148:151], v[28:31], v[200:203], v[148:151]\n\t" \
  "v_mfma_f32_16x16x32_bf16 v[152:155], v[24:27], v[204:207], v[152:155]\n\t" \
  "v_mfma_f32_16x16x32_bf16 v[156:159], v[28:31], v[204:207], v[156:159]\n\t" \
  "v_mfma_f32_16x16x32_bf16 v[144:147], v[32:35], v[208:211], v[144:147]\n\t" \
  "v_mfma_f32_16x16x32_bf16 v[148:151], v[36:39], v[208:211], v[148:151]\n\t" \
  "v_mfma_f32_16x16x32_bf16 v[152:155], v[32:35], v[212:215], v[152:155]\n\t" \
  "v_mfma_f32_16x16x32_bf16 v[156:159], v[36:39], v[212:215], v[156:159]\n\t" \
  "v_mfma_f32_16x16x32_bf16 v[144:147], v[40:43], v[216:219], v[144:147]\n\t" \
  "v_mfma_f32_16x16x32_bf16 v[148:151], v[44:47], v[216:219], v[148:151]\n\t" \
  "v_mfma_f32_16x16x32_bf16 v[152:155], v[40:43], v[220:223], v[152:155]\n\t" \
  "v_mfma_f32_16x16x32_bf16 v[156:159], v[44:47], v[220:223], v[156:159]\n\t" \
  "v_mfma_f32_16x16x32_bf16 v[144:147], v[48:51], v[224:227], v[144:147]\n\t" \
  "v_mfma_f32_16x16x32_bf16 v[148:151], v[52:55], v[224:227], v[148:151]\n\t" \
  "v_mfma_f32_16x16x32_bf16 v[152:155], v[48:51], v[228:231], v[152:155]\n\t" \
  "v_mfma_f32_16x16x32_bf16 v[156:159], v[52:55], v[228:231], v[156:159]\n\t" \
  "v_mfma_f32_16x16x32_bf16 v[144:147], v[56:59], v[232:235], v[144:147]\n\t" \
  "v_mfma_f32_16x16x32_bf16 v[148:151], v[60:63], v[232:235], v[148:151]\n\t" \
  "v_mfma_f32_16x16x32_bf16 v[152:155], v[56:59], v[236:239], v[152:155]\n\t" \
  "v_mfma_f32_16x16x32_bf16 v[156:159], v[60:63], v[236:239], v[156:159]\n\t" \
  "s_setprio 0\n\t" \
  "s_waitcnt vmcnt(4)\n\t" \
  "s_barrier\n\t" \
  "s_add_u32 s92, s92, 65024\n\t" \
  "s_addc_u32 s93, s93, 0\n\t" \
  "ds_read_b128 v[160:163], v253 offset:0\n\t" \
  "ds_read_b128 v[164:167], v253 offset:1024\n\t" \
  "ds_read_b128 v[176:179], v244 offset:32768\n\t" \
  "ds_read_b128 v[180:183], v244 offset:49152\n\t" \
  "ds_read_b128 v[184:187], v245 offset:32768\n\t" \
  "ds_read_b128 v[188:191], v245 offset:49152\n\t" \
  "s_add_u32 m0, %[lw], 65536\n\t" \
  "s_nop 0\n\t" \
  "global_load_lds_dwordx4 v248, s[92:93] sc1\n\t" \
  "ds_read_b128 v[192:195], v246 offset:32768\n\t" \
  "ds_read_b128 v[196:199], v246 offset:49152\n\t" \
  "ds_read_b128 v[200:203], v247 offset:32768\n\t" \
  "ds_read_b128 v[204:207], v247 offset:49152\n\t" \
  "s_add_u32 m0, %[lw], 66560\n\t" \
  "s_nop 0\n\t" \
  "global_load_lds_dwordx4 v249, s[92:93] sc1\n\t" \
  "ds_read_b128 v[208:211], v244 offset:33024\n\t" \
  "ds_read_b128 v[212:215], v244 offset:49408\n\t" \
  "ds_read_b128 v[216:219], v245 offset:33024\n\t" \
  "ds_read_b128 v[220:223], v245 offset:49408\n\t" \
  "s_add_u32 m0, %[lw], 67584\n\t" \
  "s_nop 0\n\t" \
  "global_load_lds_dwordx4 v250, s[92:93] sc1\n\t" \
  "ds_read_b128 v[224:227], v246 offset:33024\n\t" \
  "ds_read_b128 v[228:231], v246 offset:49408\n\t" \
  "ds_read_b128 v[232:235], v247 offset:33024\n\t" \
  "ds_read_b128 v[236:239], v247 offset:49408\n\t" \
  "s_add_u32 m0, %[lw], 68608\n\t" \
  "s_nop 0\n\t" \
  "global_load_lds_dwordx4 v251, s[92:93] sc1\n\t" \
  "s_waitcnt lgkmcnt(15)\n\t" \
  "s_waitcnt lgkmcnt(12)\n\t" \
  "s_lshr_b32 s90, s87, 7\n\t" \
  "s_xor_b32 s90, s90, %[s0]\n\t" \
  "s_and_b32 s90, s90, 1\n\t" \
  "s_lshl1_add_u32 s90, s87, s90\n\t" \
  "s_lshl_b32 s90, s90, 13\n\t" \
  "v_add_f32 v160, v160, v128\n\t" \
  "v_add_f32 v161, v161, v129\n\t" \
  "v_add_f32 v162, v162, v130\n\t" \
  "v_add_f32 v163, v163, v131\n\t" \
  "v_add_f32 v164, v164, v132\n\t" \
  "v_add_f32 v165, v165, v133\n\t" \
  "v_add_f32 v166, v166, v134\n\t" \
  "v_add_f32 v167, v167, v135\n\t" \
  "v_cvt_pk_bf16_f32 v168, v160, v161\n\t" \
  "v_cvt_pk_bf16_f32 v169, v162, v163\n\t" \
  "v_cvt_pk_bf16_f32 v170, v164, v165\n\t" \
  "v_cvt_pk_bf16_f32 v171, v166, v167\n\t" \
  "buffer_store_dwordx4 v[168:171], v172, %[rt], s90 offen sc1\n\t" \
  "s_add_u32 s86, s86, 1\n\t" \
  "s_add_u32 s87, s87, 1\n\t" \
  "s_waitcnt lgkmcnt(0)\n\t" \
  "s_setprio 1\n\t" \
  "v_mfma_f32_16x16x32_bf16 v[144:147], v[64:67], v[176:179], v[144:147]\n\t" \
  "v_mfma_f32_16x16x32_bf16 v[148:151], v[68:71], v[176:179], v[148:151]\n\t" \
  "v_mfma_f32_16x16x32_bf16 v[152:155], v[64:67], v[180:183], v[152:155]\n\t" \
  "v_mfma_f32_16x16x32_bf16 v[156:159], v[68:71], v[180:183], v[156:159]\n\t" \
  "v_mfma_f32_16x16x32_bf16 v[144:147], v[72:75], v[184:187], v[144:147]\n\t" \
  "v_mfma_f32_16x16x32_bf16 v[148:151], v[76:79], v[184:187], v[148:151]\n\t" \
  "v_mfma_f32_16x16x32_bf16 v[152:155], v[72:75], v[188:191], v[152:155]\n\t" \
  "v_mfma_f32_16x16x32_bf16 v[156:159], v[76:79], v[188:191], v[156:159]\n\t" \
  "v_mfma_f32_16x16x32_bf16 v[144:147], v[80:83], v[192:195], v[144:147]\n\t" \
  "v_mfma_f32_16x16x32_bf16 v[148:151], v[84:87], v[192:195], v[148:151]\n\t" \
  "v_mfma_f32_16x16x32_bf16 v[152:155], v[80:83], v[196:199], v[152:155]\n\t" \
  "v_mfma_f32_16x16x32_bf16 v[156:159], v[84:87], v[196:199], v[156:159]\n\t" \
  "v_mfma_f32_16x16x32_bf16 v[144:147], v[88:91], v[200:203], v[144:147]\n\t" \
  "v_mfma_f32_16x16x32_bf16 v[148:151], v[92:95], v[200:203], v[148:151]\n\t" \
  "v_mfma_f32_16x16x32_bf16 v[152:155], v[88:91], v[204:207], v[152:155]\n\t" \
  "v_mfma_f32_16x16x32_bf16 v[156:159], v[92:95], v[204:207], v[156:159]\n\t" \
  "v_mfma_f32_16x16x32_bf16 v[144:147], v[96:99], v[208:211], v[144:147]\n\t" \
  "v_mfma_f32_16x16x32_bf16 v[148:151], v[100:103], v[208:211], v[148:151]\n\t" \
  "v_mfma_f32_16x16x32_bf16 v[152:155], v[96:99], v[212:215], v[152:155]\n\t" \
  "v_mfma_f32_16x16x32_bf16 v[156:159], v[100:103], v[212:215], v[156:159]\n\t" \
  "v_mfma_f32_16x16x32_bf16 v[144:147], v[104:107], v[216:219], v[144:147]\n\t" \
  "v_mfma_f32_16x16x32_bf16 v[148:151], v[108:111], v[216:219], v[148:151]\n\t" \
  "v_mfma_f32_16x16x32_bf16 v[152:155], v[104:107], v[220:223], v[152:155]\n\t" \
  "v_mfma_f32_16x16x32_bf16 v[156:159], v[108:111], v[220:223], v[156:159]\n\t" \
  "v_mfma_f32_16x16x32_bf16 v[144:147], v[112:115], v[224:227], v[144:147]\n\t" \
  "v_mfma_f32_16x16x32_bf16 v[148:151], v[116:119], v[224:227], v[148:151]\n\t" \
  "v_mfma_f32_16x16x32_bf16 v[152:155], v[112:115], v[228:231], v[152:155]\n\t" \
  "v_mfma_f32_16x16x32_bf16 v[156:159], v[116:119], v[228:231], v[156:159]\n\t" \
  "v_mfma_f32_16x16x32_bf16 v[144:147], v[120:123], v[232:235], v[144:147]\n\t" \
  "v_mfma_f32_16x16x32_bf16 v[148:151], v[124:127], v[232:235], v[148:151]\n\t" \
  "v_mfma_f32_16x16x32_bf16 v[152:155], v[120:123], v[236:239], v[152:155]\n\t" \
  "v_mfma_f32_16x16x32_bf16 v[156:159], v[124:127], v[236:239], v[156:159]\n\t" \
  "s_setprio 0\n\t" \
  "s_cmp_lt_u32 s86, 64\n\t" \
  "s_cbranch_scc1 .Lp0%=\n\t" \
  "s_barrier\n\t" \
  "s_nop 2\n\t" \
  "ds_write_b128 v252, v[152:155] offset:0\n\t" \
  "ds_write_b128 v252, v[156:159] offset:1024\n\t" \
  "s_waitcnt lgkmcnt(0)\n\t" \
  "s_barrier\n\t" \
  "ds_read_b128 v[160:163], v253 offset:0\n\t" \
  "ds_read_b128 v[164:167], v253 offset:1024\n\t" \
  "s_waitcnt lgkmcnt(0)\n\t" \
  "s_lshr_b32 s90, s87, 7\n\t" \
  "s_xor_b32 s90, s90, %[s0]\n\t" \
  "s_and_b32 s90, s90, 1\n\t" \
  "s_lshl1_add_u32 s90, s87, s90\n\t" \
  "s_lshl_b32 s90, s90, 13\n\t" \
  "v_add_f32 v160, v160, v144\n\t" \
  "v_add_f32 v161, v161, v145\n\t" \
  "v_add_f32 v162, v162, v146\n\t" \
  "v_add_f32 v163, v163, v147\n\t" \
  "v_add_f32 v164, v164, v148\n\t" \
  "v_add_f32 v165, v165, v149\n\t" \
  "v_add_f32 v166, v166, v150\n\t" \
  "v_add_f32 v167, v167, v151\n\t" \
  "v_cvt_pk_bf16_f32 v168, v160, v161\n\t" \
  "v_cvt_pk_bf16_f32 v169, v162, v163\n\t" \
  "v_cvt_pk_bf16_f32 v170, v164, v165\n\t" \
  "v_cvt_pk_bf16_f32 v171, v166, v167\n\t" \
  "buffer_store_dwordx4 v[168:171], v172, %[rt], s90 offen sc1\n\t" \
  "s_waitcnt vmcnt(0)\n\t" \
  "s_barrier\n\t" \
  "s_mov_b32 m0, s94\n\t" \
  "s_branch .Le%=\n\t" \
  ".Lk%=:\n\t" \
  "s_mov_b32 s94, m0\n\t" \
  "s_mov_b64 s[92:93], %[x0]\n\t" \
  "s_mov_b32 s86, %[g0]\n\t" \
  "s_add_u32 s87, s86, %[t0]\n\t" \
  "s_add_u32 m0, %[lw], 67584\n\t" \
  "s_nop 0\n\t" \
  "global_load_lds_dwordx4 v250, s[92:93] sc1\n\t" \
  "s_add_u32 m0, %[lw], 68608\n\t" \
  "s_nop 0\n\t" \
  "global_load_lds_dwordx4 v251, s[92:93] sc1\n\t" \
  "s_barrier\n\t" \
  "s_branch .Ls1%=\n\t" \
  ".Lp1%=:\n\t" \
  "s_waitcnt vmcnt(10)\n\t" \
  "s_barrier\n\t" \
  "s_setprio 2\n\t" \
  "v_mfma_f32_16x16x32_bf16 v[144:147], v[64:67], v[176:179], v[144:147]\n\t" \
  "v_mfma_f32_16x16x32_bf16 v[148:151], v[68:71], v[176:179], v[148:151]\n\t" \
  "v_mfma_f32_16x16x32_bf16 v[152:155], v[64:67], v[180:183], v[152:155]\n\t" \
  "v_mfma_f32_16x16x32_bf16 v[156:159], v[68:71], v[180:183], v[156:159]\n\t" \
  "v_mfma_f32_16x16x32_bf16 v[144:147], v[72:75], v[184:187], v[144:147]\n\t" \
  "v_mfma_f32_16x16x32_bf16 v[148:151], v[76:79], v[184:187], v[148:151]\n\t" \
  "v_mfma_f32_16x16x32_bf16 v[152:155], v[72:75], v[188:191], v[152:155]\n\t" \
  "v_mfma_f32_16x16x32_bf16 v[156:159], v[76:79], v[188:191], v[156:159]\n\t" \
  "v_mfma_f32_16x16x32_bf16 v[144:147], v[80:83], v[192:195], v[144:147]\n\t" \
  "v_mfma_f32_16x16x32_bf16 v[148:151], v[84:87], v[192:195], v[148:151]\n\t" \
  "v_mfma_f32_16x16x32_bf16 v[152:155], v[80:83], v[196:199], v[152:155]\n\t" \
  "v_mfma_f32_16x16x32_bf16 v[156:159], v[84:87], v[196:199], v[156:159]\n\t" \
  "v_mfma_f32_16x16x32_bf16 v[144:147], v[88:91], v[200:203], v[144:147]\n\t" \
  "v_mfma_f32_16x16x32_bf16 v[148:151], v[92:95], v[200:203], v[148:151]\n\t" \
  "v_mfma_f32_16x16x32_bf16 v[152:155], v[88:91], v[204:207], v[152:155]\n\t" \
  "v_mfma_f32_16x16x32_bf16 v[156:159], v[92:95], v[204:207], v[156:159]\n\t" \
  "v_mfma_f32_16x16x32_bf16 v[144:147], v[96:99], v[208:211], v[144:147]\n\t" \
  "v_mfma_f32_16x16x32_bf16 v[148:151], v[100:103], v[208:211], v[148:151]\n\t" \
  "v_mfma_f32_16x16x32_bf16 v[152:155], v[96:99], v[212:215], v[152:155]\n\t" \
  "v_mfma_f32_16x16x32_bf16 v[156:159], v[100:103], v[212:215], v[156:159]\n\t" \
  "v_mfma_f32_16x16x32_bf16 v[144:147], v[104:107], v[216:219], v[144:147]\n\t" \
  "v_mfma_f32_16x16x32_bf16 v[148:151], v[108:111], v[216:219], v[148:151]\n\t" \
  "v_mfma_f32_16x16x32_bf16 v[152:155], v[104:107], v[220:223], v[152:155]\n\t" \
  "v_mfma_f32_16x16x32_bf16 v[156:159], v[108:111], v[220:223], v[156:159]\n\t" \
  "v_mfma_f32_16x16x32_bf16 v[144:147], v[112:115], v[224:227], v[144:147]\n\t" \
  "v_mfma_f32_16x16x32_bf16 v[148:151], v[116:119], v[224:227], v[148:151]\n\t" \
  "v_mfma_f32_16x16x32_bf16 v[152:155], v[112:115], v[228:231], v[152:155]\n\t" \
  "v_mfma_f32_16x16x32_bf16 v[156:159], v[116:119], v[228:231], v[156:159]\n\t" \
  "v_mfma_f32_16x16x32_bf16 v[144:147], v[120:123], v[232:235], v[144:147]\n\t" \
  "v_mfma_f32_16x16x32_bf16 v[148:151], v[124:127], v[232:235], v[148:151]\n\t" \
  "v_mfma_f32_16x16x32_bf16 v[152:155], v[120:123], v[236:239], v[152:155]\n\t" \
  "v_mfma_f32_16x16x32_bf16 v[156:159], v[124:127], v[236:239], v[156:159]\n\t" \
  "s_setprio 0\n\t" \
  ".Ls1%=:\n\t" \
  "s_add_u32 s92, s92, 512\n\t" \
  "s_addc_u32 s93, s93, 0\n\t" \
  "ds_read_b128 v[176:179], v240 offset:0\n\t" \
  "ds_read_b128 v[180:183], v240 offset:16384\n\t" \
  "ds_read_b128 v[184:187], v241 offset:0\n\t" \
  "ds_read_b128 v[188:191], v241 offset:16384\n\t" \
  "s_add_u32 m0, %[lw], 98304\n\t" \
  "s_nop 0\n\t" \
  "global_load_lds_dwordx4 v248, s[92:93] sc1\n\t" \
  "ds_read_b128 v[192:195], v242 offset:0\n\t" \
  "ds_read_b128 v[196:199], v242 offset:16384\n\t" \
  "ds_read_b128 v[200:203], v243 offset:0\n\t" \
  "ds_read_b128 v[204:207], v243 offset:16384\n\t" \
  "s_add_u32 m0, %[lw], 99328\n\t" \
  "s_nop 0\n\t" \
  "global_load_lds_dwordx4 v249, s[92:93] sc1\n\t" \
  "ds_read_b128 v[208:211], v240 offset:256\n\t" \
  "ds_read_b128 v[212:215], v240 offset:16640\n\t" \
  "ds_read_b128 v[216:219], v241 offset:256\n\t" \
  "ds_read_b128 v[220:223], v241 offset:16640\n\t" \
  "s_add_u32 m0, %[lw], 100352\n\t" \
  "s_nop 0\n\t" \
  "global_load_lds_dwordx4 v250, s[92:93] sc1\n\t" \
  "ds_read_b128 v[224:227], v242 offset:256\n\t" \
  "ds_read_b128 v[228:231], v242 offset:16640\n\t" \
  "ds_read_b128 v[232:235], v243 offset:256\n\t" \
  "ds_read_b128 v[236:239], v243 offset:16640\n\t" \
  "s_add_u32 m0, %[lw], 101376\n\t" \
  "s_nop 0\n\t" \
  "global_load_lds_dwordx4 v251, s[92:93] sc1\n\t" \
  "ds_write_b128 v252, v[152:155] offset:0\n\t" \
  "ds_write_b128 v252, v[156:159] offset:1024\n\t" \
  "s_waitcnt lgkmcnt(0)\n\t" \
  "s_waitcnt vmcnt(4)\n\t" \
  "s_barrier\n\t" \
  "s_setprio 2\n\t" \
  "v_mfma_f32_16x16x32_bf16 v[128:131], v[0:3], v[176:179], 0\n\t" \
  "v_mfma_f32_16x16x32_bf16 v[132:135], v[4:7], v[176:179], 0\n\t" \
  "v_mfma_f32_16x16x32_bf16 v[136:139], v[0:3], v[180:183], 0\n\t" \
  "v_mfma_f32_16x16x32_bf16 v[140:143], v[4:7], v[180:183], 0\n\t" \
  "v_mfma_f32_16x16x32_bf16 v[128:131], v[8:11], v[184:187], v[128:131]\n\t" \
  "v_mfma_f32_16x16x32_bf16 v[132:135], v[12:15], v[184:187], v[132:135]\n\t" \
  "v_mfma_f32_16x16x32_bf16 v[136:139], v[8:11], v[188:191], v[136:139]\n\t" \
  "v_mfma_f32_16x16x32_bf16 v[140:143], v[12:15], v[188:191], v[140:143]\n\t" \
  "v_mfma_f32_16x16x32_bf16 v[128:131], v[16:19], v[192:195], v[128:131]\n\t" \
  "v_mfma_f32_16x16x32_bf16 v[132:135], v[20:23], v[192:195], v[132:135]\n\t" \
  "v_mfma_f32_16x16x32_bf16 v[136:139], v[16:19], v[196:199], v[136:139]\n\t" \
  "v_mfma_f32_16x16x32_bf16 v[140:143], v[20:23], v[196:199], v[140:143]\n\t" \
  "v_mfma_f32_16x16x32_bf16 v[128:131], v[24:27], v[200:203], v[128:131]\n\t" \
  "v_mfma_f32_16x16x32_bf16 v[132:135], v[28:31], v[200:203], v[132:135]\n\t" \
  "v_mfma_f32_16x16x32_bf16 v[136:139], v[24:27], v[204:207], v[136:139]\n\t" \
  "v_mfma_f32_16x16x32_bf16 v[140:143], v[28:31], v[204:207], v[140:143]\n\t" \
  "v_mfma_f32_16x16x32_bf16 v[128:131], v[32:35], v[208:211], v[128:131]\n\t" \
  "v_mfma_f32_16x16x32_bf16 v[132:135], v[36:39], v[208:211], v[132:135]\n\t" \
  "v_mfma_f32_16x16x32_bf16 v[136:139], v[32:35], v[212:215], v[136:139]\n\t" \
  "v_mfma_f32_16x16x32_bf16 v[140:143], v[36:39], v[212:215], v[140:143]\n\t" \
  "v_mfma_f32_16x16x32_bf16 v[128:131], v[40:43], v[216:219], v[128:131]\n\t" \
  "v_mfma_f32_16x16x32_bf16 v[132:135], v[44:47], v[216:219], v[132:135]\n\t" \
  "v_mfma_f32_16x16x32_bf16 v[136:139], v[40:43], v[220:223], v[136:139]\n\t" \
  "v_mfma_f32_16x16x32_bf16 v[140:143], v[44:47], v[220:223], v[140:143]\n\t" \
  "v_mfma_f32_16x16x32_bf16 v[128:131], v[48:51], v[224:227], v[128:131]\n\t" \
  "v_mfma_f32_16x16x32_bf16 v[132:135], v[52:55], v[224:227], v[132:135]\n\t" \
  "v_mfma_f32_16x16x32_bf16 v[136:139], v[48:51], v[228:231], v[136:139]\n\t" \
  "v_mfma_f32_16x16x32_bf16 v[140:143], v[52:55], v[228:231], v[140:143]\n\t" \
  "v_mfma_f32_16x16x32_bf16 v[128:131], v[56:59], v[232:235], v[128:131]\n\t" \
  "v_mfma_f32_16x16x32_bf16 v[132:135], v[60:63], v[232:235], v[132:135]\n\t" \
  "v_mfma_f32_16x16x32_bf16 v[136:139], v[56:59], v[236:239], v[136:139]\n\t" \
  "v_mfma_f32_16x16x32_bf16 v[140:143], v[60:63], v[236:239], v[140:143]\n\t" \
  "s_setprio 0\n\t" \
  "s_add_u32 s92, s92, 65024\n\t" \
  "s_addc_u32 s93, s93, 0\n\t" \
  "ds_read_b128 v[160:163], v253 offset:0\n\t" \
  "ds_read_b128 v[164:167], v253 offset:1024\n\t" \
  "ds_read_b128 v[176:179], v240 offset:32768\n\t" \
  "ds_read_b128 v[180:183], v240 offset:49152\n\t" \
  "ds_read_b128 v[184:187], v241 offset:32768\n\t" \
  "ds_read_b128 v[188:191], v241 offset:49152\n\t" \
  "s_add_u32 m0, %[lw], 0\n\t" \
  "s_nop 0\n\t" \
  "global_load_lds_dwordx4 v248, s[92:93] sc1\n\t" \
  "ds_read_b128 v[192:195], v242 offset:32768\n\t" \
  "ds_read_b128 v[196:199], v242 offset:49152\n\t" \
  "ds_read_b128 v[200:203], v243 offset:32768\n\t" \
  "ds_read_b128 v[204:207], v243 offset:49152\n\t" \
  "s_add_u32 m0, %[lw], 1024\n\t" \
  "s_nop 0\n\t" \
  "global_load_lds_dwordx4 v249, s[92:93] sc1\n\t" \
  "ds_read_b128 v[208:211], v240 offset:33024\n\t" \
  "ds_read_b128 v[212:215], v240 offset:49408\n\t" \
  "ds_read_b128 v[216:219], v241 offset:33024\n\t" \
  "ds_read_b128 v[220:223], v241 offset:49408\n\t" \
  "s_add_u32 m0, %[lw], 2048\n\t" \
  "s_nop 0\n\t" \
  "global_load_lds_dwordx4 v250, s[92:93] sc1\n\t" \
  "ds_read_b128 v[224:227], v242 offset:33024\n\t" \
  "ds_read_b128 v[228:231], v242 offset:49408\n\t" \
  "ds_read_b128 v[232:235], v243 offset:33024\n\t" \
  "ds_read_b128 v[236:239], v243 offset:49408\n\t" \
  "s_add_u32 m0, %[lw], 3072\n\t" \
  "s_nop 0\n\t" \
  "global_load_lds_dwordx4 v251, s[92:93] sc1\n\t" \
  "s_waitcnt lgkmcnt(15)\n\t" \
  "s_waitcnt lgkmcnt(12)\n\t" \
  "s_sub_i32 s91, s86, 2\n\t" \
  "v_mov_b32 v173, s91\n\t" \
  "buffer_store_dword v173, v174, %[rs], 0 offen sc1\n\t" \
  "s_lshr_b32 s90, s87, 7\n\t" \
  "s_xor_b32 s90, s90, %[s0]\n\t" \
  "s_and_b32 s90, s90, 1\n\t" \
  "s_lshl1_add_u32 s90, s87, s90\n\t" \
  "s_lshl_b32 s90, s90, 13\n\t" \
  "v_add_f32 v160, v160, v144\n\t" \
  "v_add_f32 v161, v161, v145\n\t" \
  "v_add_f32 v162, v162, v146\n\t" \
  "v_add_f32 v163, v163, v147\n\t" \
  "v_add_f32 v164, v164, v148\n\t" \
  "v_add_f32 v165, v165, v149\n\t" \
  "v_add_f32 v166, v166, v150\n\t" \
  "v_add_f32 v167, v167, v151\n\t" \
  "v_cvt_pk_bf16_f32 v168, v160, v161\n\t" \
  "v_cvt_pk_bf16_f32 v169, v162, v163\n\t" \
  "v_cvt_pk_bf16_f32 v170, v164, v165\n\t" \
  "v_cvt_pk_bf16_f32 v171, v166, v167\n\t" \
  "buffer_store_dwordx4 v[168:171], v172, %[rt], s90 offen sc1\n\t" \
  "s_add_u32 s86, s86, 1\n\t" \
  "s_add_u32 s87, s87, 1\n\t" \
  "s_waitcnt lgkmcnt(0)\n\t" \
  "s_waitcnt vmcnt(10)\n\t" \
  "s_barrier\n\t" \
  "s_setprio 2\n\t" \
  "v_mfma_f32_16x16x32_bf16 v[128:131], v[64:67], v[176:179], v[128:131]\n\t" \
  "v_mfma_f32_16x16x32_bf16 v[132:135], v[68:71], v[176:179], v[132:135]\n\t" \
  "v_mfma_f32_16x16x32_bf16 v[136:139], v[64:67], v[180:183], v[136:139]\n\t" \
  "v_mfma_f32_16x16x32_bf16 v[140:143], v[68:71], v[180:183], v[140:143]\n\t" \
  "v_mfma_f32_16x16x32_bf16 v[128:131], v[72:75], v[184:187], v[128:131]\n\t" \
  "v_mfma_f32_16x16x32_bf16 v[132:135], v[76:79], v[184:187], v[132:135]\n\t" \
  "v_mfma_f32_16x16x32_bf16 v[136:139], v[72:75], v[188:191], v[136:139]\n\t" \
  "v_mfma_f32_16x16x32_bf16 v[140:143], v[76:79], v[188:191], v[140:143]\n\t" \
  "v_mfma_f32_16x16x32_bf16 v[128:131], v[80:83], v[192:195], v[128:131]\n\t" \
  "v_mfma_f32_16x16x32_bf16 v[132:135], v[84:87], v[192:195], v[132:135]\n\t" \
  "v_mfma_f32_16x16x32_bf16 v[136:139], v[80:83], v[196:199], v[136:139]\n\t" \
  "v_mfma_f32_16x16x32_bf16 v[140:143], v[84:87], v[196:199], v[140:143]\n\t" \
  "v_mfma_f32_16x16x32_bf16 v[128:131], v[88:91], v[200:203], v[128:131]\n\t" \
  "v_mfma_f32_16x16x32_bf16 v[132:135], v[92:95], v[200:203], v[132:135]\n\t" \
  "v_mfma_f32_16x16x32_bf16 v[136:139], v[88:91], v[204:207], v[136:139]\n\t" \
  "v_mfma_f32_16x16x32_bf16 v[140:143], v[92:95], v[204:207], v[140:143]\n\t" \
  "v_mfma_f32_16x16x32_bf16 v[128:131], v[96:99], v[208:211], v[128:131]\n\t" \
  "v_mfma_f32_16x16x32_bf16 v[132:135], v[100:103], v[208:211], v[132:135]\n\t" \
  "v_mfma_f32_16x16x32_bf16 v[136:139], v[96:99], v[212:215], v[136:139]\n\t" \
  "v_mfma_f32_16x16x32_bf16 v[140:143], v[100:103], v[212:215], v[140:143]\n\t" \
  "v_mfma_f32_16x16x32_bf16 v[128:131], v[104:107], v[216:219], v[128:131]\n\t" \
  "v_mfma_f32_16x16x32_bf16 v[132:135], v[108:111], v[216:219], v[132:135]\n\t" \
  "v_mfma_f32_16x16x32_bf16 v[136:139], v[104:107], v[220:223], v[136:139]\n\t" \
  "v_mfma_f32_16x16x32_bf16 v[140:143], v[108:111], v[220:223], v[140:143]\n\t" \
  "v_mfma_f32_16x16x32_bf16 v[128:131], v[112:115], v[224:227], v[128:131]\n\t" \
  "v_mfma_f32_16x16x32_bf16 v[132:135], v[116:119], v[224:227], v[132:135]\n\t" \
  "v_mfma_f32_16x16x32_bf16 v[136:139], v[112:115], v[228:231], v[136:139]\n\t" \
  "v_mfma_f32_16x16x32_bf16 v[140:143], v[116:119], v[228:231], v[140:143]\n\t" \
  "v_mfma_f32_16x16x32_bf16 v[128:131], v[120:123], v[232:235], v[128:131]\n\t" \
  "v_mfma_f32_16x16x32_bf16 v[132:135], v[124:127], v[232:235], v[132:135]\n\t" \
  "v_mfma_f32_16x16x32_bf16 v[136:139], v[120:123], v[236:239], v[136:139]\n\t" \
  "v_mfma_f32_16x16x32_bf16 v[140:143], v[124:127], v[236:239], v[140:143]\n\t" \
  "s_setprio 0\n\t" \
  "s_add_u32 s92, s92, 512\n\t" \
  "s_addc_u32 s93, s93, 0\n\t" \
  "ds_read_b128 v[176:179], v244 offset:0\n\t" \
  "ds_read_b128 v[180:183], v244 offset:16384\n\t" \
  "ds_read_b128 v[184:187], v245 offset:0\n\t" \
  "ds_read_b128 v[188:191], v245 offset:16384\n\t" \
  "s_add_u32 m0, %[lw], 32768\n\t" \
  "s_nop 0\n\t" \
  "global_load_lds_dwordx4 v248, s[92:93] sc1\n\t" \
  "ds_read_b128 v[192:195], v246 offset:0\n\t" \
  "ds_read_b128 v[196:199], v246 offset:16384\n\t" \
  "ds_read_b128 v[200:203], v247 offset:0\n\t" \
  "ds_read_b128 v[204:207], v247 offset:16384\n\t" \
  "s_add_u32 m0, %[lw], 33792\n\t" \
  "s_nop 0\n\t" \
  "global_load_lds_dwordx4 v249, s[92:93] sc1\n\t" \
  "ds_read_b128 v[208:211], v244 offset:256\n\t" \
  "ds_read_b128 v[212:215], v244 offset:16640\n\t" \
  "ds_read_b128 v[216:219], v245 offset:256\n\t" \
  "ds_read_b128 v[220:223], v245 offset:16640\n\t" \
  "s_add_u32 m0, %[lw], 34816\n\t" \
  "s_nop 0\n\t" \
  "global_load_lds_dwordx4 v250, s[92:93] sc1\n\t" \
  "ds_read_b128 v[224:227], v246 offset:256\n\t" \
  "ds_read_b128 v[228:231], v246 offset:16640\n\t" \
  "ds_read_b128 v[232:235], v247 offset:256\n\t" \
  "ds_read_b128 v[236:239], v247 offset:16640\n\t" \
  "s_add_u32 m0, %[lw], 35840\n\t" \
  "s_nop 0\n\t" \
  "global_load_lds_dwordx4 v251, s[92:93] sc1\n\t" \
  "ds_write_b128 v252, v[136:139] offset:0\n\t" \
  "ds_write_b128 v252, v[140:143] offset:1024\n\t" \
  "s_waitcnt lgkmcnt(0)\n\t" \
  "s_waitcnt vmcnt(4)\n\t" \
  "s_barrier\n\t" \
  "s_setprio 2\n\t" \
  "v_mfma_f32_16x16x32_bf16 v[144:147], v[0:3], v[176:179], 0\n\t" \
  "v_mfma_f32_16x16x32_bf16 v[148:151], v[4:7], v[176:179], 0\n\t" \
  "v_mfma_f32_16x16x32_bf16 v[152:155], v[0:3], v[180:183], 0\n\t" \
  "v_mfma_f32_16x16x32_bf16 v[156:159], v[4:7], v[180:183], 0\n\t" \
  "v_mfma_f32_16x16x32_bf16 v[144:147], v[8:11], v[184:187], v[144:147]\n\t" \
  "v_mfma_f32_16x16x32_bf16 v[148:151], v[12:15], v[184:187], v[148:151]\n\t" \
  "v_mfma_f32_16x16x32_bf16 v[152:155], v[8:11], v[188:191], v[152:155]\n\t" \
  "v_mfma_f32_16x16x32_bf16 v[156:159], v[12:15], v[188:191], v[156:159]\n\t" \
  "v_mfma_f32_16x16x32_bf16 v[144:147], v[16:19], v[192:195], v[144:147]\n\t" \
  "v_mfma_f32_16x16x32_bf16 v[148:151], v[20:23], v[192:195], v[148:151]\n\t" \
  "v_mfma_f32_16x16x32_bf16 v[152:155], v[16:19], v[196:199], v[152:155]\n\t" \
  "v_mfma_f32_16x16x32_bf16 v[156:159], v[20:23], v[196:199], v[156:159]\n\t" \
  "v_mfma_f32_16x16x32_bf16 v[144:147], v[24:27], v[200:203], v[144:147]\n\t" \
  "v_mfma_f32_16x16x32_bf16 v[148:151], v[28:31], v[200:203], v[148:151]\n\t" \
  "v_mfma_f32_16x16x32_bf16 v[152:155], v[24:27], v[204:207], v[152:155]\n\t" \
  "v_mfma_f32_16x16x32_bf16 v[156:159], v[28:31], v[204:207], v[156:159]\n\t" \
  "v_mfma_f32_16x16x32_bf16 v[144:147], v[32:35], v[208:211], v[144:147]\n\t" \
  "v_mfma_f32_16x16x32_bf16 v[148:151], v[36:39], v[208:211], v[148:151]\n\t" \
  "v_mfma_f32_16x16x32_bf16 v[152:155], v[32:35], v[212:215], v[152:155]\n\t" \
  "v_mfma_f32_16x16x32_bf16 v[156:159], v[36:39], v[212:215], v[156:159]\n\t" \
  "v_mfma_f32_16x16x32_bf16 v[144:147], v[40:43], v[216:219], v[144:147]\n\t" \
  "v_mfma_f32_16x16x32_bf16 v[148:151], v[44:47], v[216:219], v[148:151]\n\t" \
  "v_mfma_f32_16x16x32_bf16 v[152:155], v[40:43], v[220:223], v[152:155]\n\t" \
  "v_mfma_f32_16x16x32_bf16 v[156:159], v[44:47], v[220:223], v[156:159]\n\t" \
  "v_mfma_f32_16x16x32_bf16 v[144:147], v[48:51], v[224:227], v[144:147]\n\t" \
  "v_mfma_f32_16x16x32_bf16 v[148:151], v[52:55], v[224:227], v[148:151]\n\t" \
  "v_mfma_f32_16x16x32_bf16 v[152:155], v[48:51], v[228:231], v[152:155]\n\t" \
  "v_mfma_f32_16x16x32_bf16 v[156:159], v[52:55], v[228:231], v[156:159]\n\t" \
  "v_mfma_f32_16x16x32_bf16 v[144:147], v[56:59], v[232:235], v[144:147]\n\t" \
  "v_mfma_f32_16x16x32_bf16 v[148:151], v[60:63], v[232:235], v[148:151]\n\t" \
  "v_mfma_f32_16x16x32_bf16 v[152:155], v[56:59], v[236:239], v[152:155]\n\t" \
  "v_mfma_f32_16x16x32_bf16 v[156:159], v[60:63], v[236:239], v[156:159]\n\t" \
  "s_setprio 0\n\t" \
  "s_add_u32 s92, s92, 65024\n\t" \
  "s_addc_u32 s93, s93, 0\n\t" \
  "ds_read_b128 v[160:163], v253 offset:0\n\t" \
  "ds_read_b128 v[164:167], v253 offset:1024\n\t" \
  "ds_read_b128 v[176:179], v244 offset:32768\n\t" \
  "ds_read_b128 v[180:183], v244 offset:49152\n\t" \
  "ds_read_b128 v[184:187], v245 offset:32768\n\t" \
  "ds_read_b128 v[188:191], v245 offset:49152\n\t" \
  "s_add_u32 m0, %[lw], 65536\n\t" \
  "s_nop 0\n\t" \
  "global_load_lds_dwordx4 v248, s[92:93] sc1\n\t" \
  "ds_read_b128 v[192:195], v246 offset:32768\n\t" \
  "ds_read_b128 v[196:199], v246 offset:49152\n\t" \
  "ds_read_b128 v[200:203], v247 offset:32768\n\t" \
  "ds_read_b128 v[204:207], v247 offset:49152\n\t" \
  "s_add_u32 m0, %[lw], 66560\n\t" \
  "s_nop 0\n\t" \
  "global_load_lds_dwordx4 v249, s[92:93] sc1\n\t" \
  "ds_read_b128 v[208:211], v244 offset:33024\n\t" \
  "ds_read_b128 v[212:215], v244 offset:49408\n\t" \
  "ds_read_b128 v[216:219], v245 offset:33024\n\t" \
  "ds_read_b128 v[220:223], v245 offset:49408\n\t" \
  "s_add_u32 m0, %[lw], 67584\n\t" \
  "s_nop 0\n\t" \
  "global_load_lds_dwordx4 v250, s[92:93] sc1\n\t" \
  "ds_read_b128 v[224:227], v246 offset:33024\n\t" \
  "ds_read_b128 v[228:231], v246 offset:49408\n\t" \
  "ds_read_b128 v[232:235], v247 offset:33024\n\t" \
  "ds_read_b128 v[236:239], v247 offset:49408\n\t" \
  "s_add_u32 m0, %[lw], 68608\n\t" \
  "s_nop 0\n\t" \
  "global_load_lds_dwordx4 v251, s[92:93] sc1\n\t" \
  "s_waitcnt lgkmcnt(15)\n\t" \
  "s_waitcnt lgkmcnt(12)\n\t" \
  "s_sub_i32 s91, s86, 2\n\t" \
  "v_mov_b32 v173, s91\n\t" \
  "buffer_store_dword v173, v174, %[rs], 0 offen sc1\n\t" \
  "s_lshr_b32 s90, s87, 7\n\t" \
  "s_xor_b32 s90, s90, %[s0]\n\t" \
  "s_and_b32 s90, s90, 1\n\t" \
  "s_lshl1_add_u32 s90, s87, s90\n\t" \
  "s_lshl_b32 s90, s90, 13\n\t" \
  "v_add_f32 v160, v160, v128\n\t" \
  "v_add_f32 v161, v161, v129\n\t" \
  "v_add_f32 v162, v162, v130\n\t" \
  "v_add_f32 v163, v163, v131\n\t" \
  "v_add_f32 v164, v164, v132\n\t" \
  "v_add_f32 v165, v165, v133\n\t" \
  "v_add_f32 v166, v166, v134\n\t" \
  "v_add_f32 v167, v167, v135\n\t" \
  "v_cvt_pk_bf16_f32 v168, v160, v161\n\t" \
  "v_cvt_pk_bf16_f32 v169, v162, v163\n\t" \
  "v_cvt_pk_bf16_f32 v170, v164, v165\n\t" \
  "v_cvt_pk_bf16_f32 v171, v166, v167\n\t" \
  "buffer_store_dwordx4 v[168:171], v172, %[rt], s90 offen sc1\n\t" \
  "s_add_u32 s86, s86, 1\n\t" \
  "s_add_u32 s87, s87, 1\n\t" \
  "s_waitcnt lgkmcnt(0)\n\t" \
  "s_cmp_lt_u32 s86, 64\n\t" \
  "s_cbranch_scc1 .Lp1%=\n\t" \
  "s_barrier\n\t" \
  "s_setprio 2\n\t" \
  "v_mfma_f32_16x16x32_bf16 v[144:147], v[64:67], v[176:179], v[144:147]\n\t" \
  "v_mfma_f32_16x16x32_bf16 v[148:151], v[68:71], v[176:179], v[148:151]\n\t" \
  "v_mfma_f32_16x16x32_bf16 v[152:155], v[64:67], v[180:183], v[152:155]\n\t" \
  "v_mfma_f32_16x16x32_bf16 v[156:159], v[68:71], v[180:183], v[156:159]\n\t" \
  "v_mfma_f32_16x16x32_bf16 v[144:147], v[72:75], v[184:187], v[144:147]\n\t" \
  "v_mfma_f32_16x16x32_bf16 v[148:151], v[76:79], v[184:187], v[148:151]\n\t" \
  "v_mfma_f32_16x16x32_bf16 v[152:155], v[72:75], v[188:191], v[152:155]\n\t" \
  "v_mfma_f32_16x16x32_bf16 v[156:159], v[76:79], v[188:191], v[156:159]\n\t" \
  "v_mfma_f32_16x16x32_bf16 v[144:147], v[80:83], v[192:195], v[144:147]\n\t" \
  "v_mfma_f32_16x16x32_bf16 v[148:151], v[84:87], v[192:195], v[148:151]\n\t" \
  "v_mfma_f32_16x16x32_bf16 v[152:155], v[80:83], v[196:199], v[152:155]\n\t" \
  "v_mfma_f32_16x16x32_bf16 v[156:159], v[84:87], v[196:199], v[156:159]\n\t" \
  "v_mfma_f32_16x16x32_bf16 v[144:147], v[88:91], v[200:203], v[144:147]\n\t" \
  "v_mfma_f32_16x16x32_bf16 v[148:151], v[92:95], v[200:203], v[148:151]\n\t" \
  "v_mfma_f32_16x16x32_bf16 v[152:155], v[88:91], v[204:207], v[152:155]\n\t" \
  "v_mfma_f32_16x16x32_bf16 v[156:159], v[92:95], v[204:207], v[156:159]\n\t" \
  "v_mfma_f32_16x16x32_bf16 v[144:147], v[96:99], v[208:211], v[144:147]\n\t" \
  "v_mfma_f32_16x16x32_bf16 v[148:151], v[100:103], v[208:211], v[148:151]\n\t" \
  "v_mfma_f32_16x16x32_bf16 v[152:155], v[96:99], v[212:215], v[152:155]\n\t" \
  "v_mfma_f32_16x16x32_bf16 v[156:159], v[100:103], v[212:215], v[156:159]\n\t" \
  "v_mfma_f32_16x16x32_bf16 v[144:147], v[104:107], v[216:219], v[144:147]\n\t" \
  "v_mfma_f32_16x16x32_bf16 v[148:151], v[108:111], v[216:219], v[148:151]\n\t" \
  "v_mfma_f32_16x16x32_bf16 v[152:155], v[104:107], v[220:223], v[152:155]\n\t" \
  "v_mfma_f32_16x16x32_bf16 v[156:159], v[108:111], v[220:223], v[156:159]\n\t" \
  "v_mfma_f32_16x16x32_bf16 v[144:147], v[112:115], v[224:227], v[144:147]\n\t" \
  "v_mfma_f32_16x16x32_bf16 v[148:151], v[116:119], v[224:227], v[148:151]\n\t" \
  "v_mfma_f32_16x16x32_bf16 v[152:155], v[112:115], v[228:231], v[152:155]\n\t" \
  "v_mfma_f32_16x16x32_bf16 v[156:159], v[116:119], v[228:231], v[156:159]\n\t" \
  "v_mfma_f32_16x16x32_bf16 v[144:147], v[120:123], v[232:235], v[144:147]\n\t" \
  "v_mfma_f32_16x16x32_bf16 v[148:151], v[124:127], v[232:235], v[148:151]\n\t" \
  "v_mfma_f32_16x16x32_bf16 v[152:155], v[120:123], v[236:239], v[152:155]\n\t" \
  "v_mfma_f32_16x16x32_bf16 v[156:159], v[124:127], v[236:239], v[156:159]\n\t" \
  "s_setprio 0\n\t" \
  "s_nop 5\n\t" \
  "ds_write_b128 v252, v[152:155] offset:0\n\t" \
  "ds_write_b128 v252, v[156:159] offset:1024\n\t" \
  "s_waitcnt lgkmcnt(0)\n\t" \
  "s_barrier\n\t" \
  "ds_read_b128 v[160:163], v253 offset:0\n\t" \
  "ds_read_b128 v[164:167], v253 offset:1024\n\t" \
  "s_waitcnt lgkmcnt(0)\n\t" \
  "s_lshr_b32 s90, s87, 7\n\t" \
  "s_xor_b32 s90, s90, %[s0]\n\t" \
  "s_and_b32 s90, s90, 1\n\t" \
  "s_lshl1_add_u32 s90, s87, s90\n\t" \
  "s_lshl_b32 s90, s90, 13\n\t" \
  "v_add_f32 v160, v160, v144\n\t" \
  "v_add_f32 v161, v161, v145\n\t" \
  "v_add_f32 v162, v162, v146\n\t" \
  "v_add_f32 v163, v163, v147\n\t" \
  "v_add_f32 v164, v164, v148\n\t" \
  "v_add_f32 v165, v165, v149\n\t" \
  "v_add_f32 v166, v166, v150\n\t" \
  "v_add_f32 v167, v167, v151\n\t" \
  "v_cvt_pk_bf16_f32 v168, v160, v161\n\t" \
  "v_cvt_pk_bf16_f32 v169, v162, v163\n\t" \
  "v_cvt_pk_bf16_f32 v170, v164, v165\n\t" \
  "v_cvt_pk_bf16_f32 v171, v166, v167\n\t" \
  "buffer_store_dwordx4 v[168:171], v172, %[rt], s90 offen sc1\n\t" \
  "s_waitcnt vmcnt(0)\n\t" \
  "s_barrier\n\t" \
  "s_sub_i32 s91, s86, 1\n\t" \
  "v_mov_b32 v173, s91\n\t" \
  "buffer_store_dword v173, v174, %[rs], 0 offen sc1\n\t" \
  "s_mov_b32 m0, s94\n\t" \
  ".Le%=:\n\t"
#define PP_ASM_1 \
  "s_cmp_lg_u32 %[kh], 0\n\t" \
  "s_cbranch_scc1 .Lk%=\n\t" \
  "s_mov_b32 s94, m0\n\t" \
  "s_mov_b64 s[92:93], %[x0]\n\t" \
  "s_mov_b32 s86, %[g0]\n\t" \
  "s_add_u32 s87, s86, %[t0]\n\t" \
  "s_add_u32 m0, %[lw], 67584\n\t" \
  "s_nop 0\n\t" \
  "global_load_lds_dwordx4 v250, s[92:93] sc1\n\t" \
  "s_add_u32 m0, %[lw], 68608\n\t" \
  "s_nop 0\n\t" \
  "global_load_lds_dwordx4 v251, s[92:93] sc1\n\t" \
  "s_barrier\n\t" \
  "s_branch .Ls0%=\n\t" \
  ".Lp0%=:\n\t" \
  "s_waitcnt vmcnt(9)\n\t" \
  "s_barrier\n\t" \
  ".Ls0%=:\n\t" \
  "s_add_u32 s92, s92, 512\n\t" \
  "s_addc_u32 s93, s93, 0\n\t" \
  "ds_read_b128 v[176:179], v240 offset:0\n\t" \
  "ds_read_b128 v[180:183], v240 offset:16384\n\t" \
  "ds_read_b128 v[184:187], v241 offset:0\n\t" \
  "ds_read_b128 v[188:191], v241 offset:16384\n\t" \
  "s_add_u32 m0, %[lw], 98304\n\t" \
  "s_nop 0\n\t" \
  "global_load_lds_dwordx4 v248, s[92:93] sc1\n\t" \
  "ds_read_b128 v[192:195], v242 offset:0\n\t" \
  "ds_read_b128 v[196:199], v242 offset:16384\n\t" \
  "ds_read_b128 v[200:203], v243 offset:0\n\t" \
  "ds_read_b128 v[204:207], v243 offset:16384\n\t" \
  "s_add_u32 m0, %[lw], 99328\n\t" \
  "s_nop 0\n\t" \
  "global_load_lds_dwordx4 v249, s[92:93] sc1\n\t" \
  "ds_read_b128 v[208:211], v240 offset:256\n\t" \
  "ds_read_b128 v[212:215], v240 offset:16640\n\t" \
  "ds_read_b128 v[216:219], v241 offset:256\n\t" \
  "ds_read_b128 v[220:223], v241 offset:16640\n\t" \
  "s_add_u32 m0, %[lw], 100352\n\t" \
  "s_nop 0\n\t" \
  "global_load_lds_dwordx4 v250, s[92:93] sc1\n\t" \
  "ds_read_b128 v[224:227], v242 offset:256\n\t" \
  "ds_read_b128 v[228:231], v242 offset:16640\n\t" \
  "ds_read_b128 v[232:235], v243 offset:256\n\t" \
  "ds_read_b128 v[236:239], v243 offset:16640\n\t" \
  "s_add_u32 m0, %[lw], 101376\n\t" \
  "s_nop 0\n\t" \
  "global_load_lds_dwordx4 v251, s[92:93] sc1\n\t" \
  "ds_write_b128 v252, v[152:155] offset:0\n\t" \
  "ds_write_b128 v252, v[156:159] offset:1024\n\t" \
  "s_waitcnt lgkmcnt(0)\n\t" \
  "s_setprio 1\n\t" \
  "v_mfma_f32_16x16x32_bf16 v[128:131], v[176:179], v[0:3], 0\n\t" \
  "v_mfma_f32_16x16x32_bf16 v[136:139], v[176:179], v[4:7], 0\n\t" \
  "v_mfma_f32_16x16x32_bf16 v[132:135], v[180:183], v[0:3], 0\n\t" \
  "v_mfma_f32_16x16x32_bf16 v[140:143], v[180:183], v[4:7], 0\n\t" \
  "v_mfma_f32_16x16x32_bf16 v[128:131], v[184:187], v[8:11], v[128:131]\n\t" \
  "v_mfma_f32_16x16x32_bf16 v[136:139], v[184:187], v[12:15], v[136:139]\n\t" \
  "v_mfma_f32_16x16x32_bf16 v[132:135], v[188:191], v[8:11], v[132:135]\n\t" \
  "v_mfma_f32_16x16x32_bf16 v[140:143], v[188:191], v[12:15], v[140:143]\n\t" \
  "v_mfma_f32_16x16x32_bf16 v[128:131], v[192:195], v[16:19], v[128:131]\n\t" \
  "v_mfma_f32_16x16x32_bf16 v[136:139], v[192:195], v[20:23], v[136:139]\n\t" \
  "v_mfma_f32_16x16x32_bf16 v[132:135], v[196:199], v[16:19], v[132:135]\n\t" \
  "v_mfma_f32_16x16x32_bf16 v[140:143], v[196:199], v[20:23], v[140:143]\n\t" \
  "v_mfma_f32_16x16x32_bf16 v[128:131], v[200:203], v[24:27], v[128:131]\n\t" \
  "v_mfma_f32_16x16x32_bf16 v[136:139], v[200:203], v[28:31], v[136:139]\n\t" \
  "v_mfma_f32_16x16x32_bf16 v[132:135], v[204:207], v[24:27], v[132:135]\n\t" \
  "v_mfma_f32_16x16x32_bf16 v[140:143], v[204:207], v[28:31], v[140:143]\n\t" \
  "v_mfma_f32_16x16x32_bf16 v[128:131], v[208:211], v[32:35], v[128:131]\n\t" \
  "v_mfma_f32_16x16x32_bf16 v[136:139], v[208:211], v[36:39], v[136:139]\n\t" \
  "v_mfma_f32_16x16x32_bf16 v[132:135], v[212:215], v[32:35], v[132:135]\n\t" \
  "v_mfma_f32_16x16x32_bf16 v[140:143], v[212:215], v[36:39], v[140:143]\n\t" \
  "v_mfma_f32_16x16x32_bf16 v[128:131], v[216:219], v[40:43], v[128:131]\n\t" \
  "v_mfma_f32_16x16x32_bf16 v[136:139], v[216:219], v[44:47], v[136:139]\n\t" \
  "v_mfma_f32_16x16x32_bf16 v[132:135], v[220:223], v[40:43], v[132:135]\n\t" \
  "v_mfma_f32_16x16x32_bf16 v[140:143], v[220:223], v[44:47], v[140:143]\n\t" \
  "v_mfma_f32_16x16x32_bf16 v[128:131], v[224:227], v[48:51], v[128:131]\n\t" \
  "v_mfma_f32_16x16x32_bf16 v[136:139], v[224:227], v[52:55], v[136:139]\n\t" \
  "v_mfma_f32_16x16x32_bf16 v[132:135], v[228:231], v[48:51], v[132:135]\n\t" \
  "v_mfma_f32_16x16x32_bf16 v[140:143], v[228:231], v[52:55], v[140:143]\n\t" \
  "v_mfma_f32_16x16x32_bf16 v[128:131], v[232:235], v[56:59], v[128:131]\n\t" \
  "v_mfma_f32_16x16x32_bf16 v[136:139], v[232:235], v[60:63], v[136:139]\n\t" \
  "v_mfma_f32_16x16x32_bf16 v[132:135], v[236:239], v[56:59], v[132:135]\n\t" \
  "v_mfma_f32_16x16x32_bf16 v[140:143], v[236:239], v[60:63], v[140:143]\n\t" \
  "s_setprio 0\n\t" \
  "s_waitcnt vmcnt(4)\n\t" \
  "s_barrier\n\t" \
  "s_add_u32 s92, s92, 65024\n\t" \
  "s_addc_u32 s93, s93, 0\n\t" \
  "ds_read_b128 v[160:163], v253 offset:0\n\t" \
  "ds_read_b128 v[164:167], v253 offset:1024\n\t" \
  "ds_read_b128 v[176:179], v240 offset:32768\n\t" \
  "ds_read_b128 v[180:183], v240 offset:49152\n\t" \
  "ds_read_b128 v[184:187], v241 offset:32768\n\t" \
  "ds_read_b128 v[188:191], v241 offset:49152\n\t" \
  "s_add_u32 m0, %[lw], 0\n\t" \
  "s_nop 0\n\t" \
  "global_load_lds_dwordx4 v248, s[92:93] sc1\n\t" \
  "ds_read_b128 v[192:195], v242 offset:32768\n\t" \
  "ds_read_b128 v[196:199], v242 offset:49152\n\t" \
  "ds_read_b128 v[200:203], v243 offset:32768\n\t" \
  "ds_read_b128 v[204:207], v243 offset:49152\n\t" \
  "s_add_u32 m0, %[lw], 1024\n\t" \
  "s_nop 0\n\t" \
  "global_load_lds_dwordx4 v249, s[92:93] sc1\n\t" \
  "ds_read_b128 v[208:211], v240 offset:33024\n\t" \
  "ds_read_b128 v[212:215], v240 offset:49408\n\t" \
  "ds_read_b128 v[216:219], v241 offset:33024\n\t" \
  "ds_read_b128 v[220:223], v241 offset:49408\n\t" \
  "s_add_u32 m0, %[lw], 2048\n\t" \
  "s_nop 0\n\t" \
  "global_load_lds_dwordx4 v250, s[92:93] sc1\n\t" \
  "ds_read_b128 v[224:227], v242 offset:33024\n\t" \
  "ds_read_b128 v[228:231], v242 offset:49408\n\t" \
  "ds_read_b128 v[232:235], v243 offset:33024\n\t" \
  "ds_read_b128 v[236:239], v243 offset:49408\n\t" \
  "s_add_u32 m0, %[lw], 3072\n\t" \
  "s_nop 0\n\t" \
  "global_load_lds_dwordx4 v251, s[92:93] sc1\n\t" \
  "s_waitcnt lgkmcnt(15)\n\t" \
  "s_waitcnt lgkmcnt(12)\n\t" \
  "s_lshr_b32 s90, s87, 7\n\t" \
  "s_xor_b32 s90, s90, %[s0]\n\t" \
  "s_and_b32 s90, s90, 1\n\t" \
  "s_lshl1_add_u32 s90, s87, s90\n\t" \
  "s_lshl_b32 s90, s90, 13\n\t" \
  "v_add_f32 v160, v160, v144\n\t" \
  "v_add_f32 v161, v161, v145\n\t" \
  "v_add_f32 v162, v162, v146\n\t" \
  "v_add_f32 v163, v163, v147\n\t" \
  "v_add_f32 v164, v164, v148\n\t" \
  "v_add_f32 v165, v165, v149\n\t" \
  "v_add_f32 v166, v166, v150\n\t" \
  "v_add_f32 v167, v167, v151\n\t" \
  "v_cvt_pk_bf16_f32 v168, v160, v161\n\t" \
  "v_cvt_pk_bf16_f32 v169, v162, v163\n\t" \
  "v_cvt_pk_bf16_f32 v170, v164, v165\n\t" \
  "v_cvt_pk_bf16_f32 v171, v166, v167\n\t" \
  "buffer_store_dwordx4 v[168:171], v172, %[rt], s90 offen sc1\n\t" \
  "s_add_u32 s86, s86, 1\n\t" \
  "s_add_u32 s87, s87, 1\n\t" \
  "s_waitcnt lgkmcnt(0)\n\t" \
  "s_setprio 1\n\t" \
  "v_mfma_f32_16x16x32_bf16 v[128:131], v[176:179], v[64:67], v[128:131]\n\t" \
  "v_mfma_f32_16x16x32_bf16 v[136:139], v[176:179], v[68:71], v[136:139]\n\t" \
  "v_mfma_f32_16x16x32_bf16 v[132:135], v[180:183], v[64:67], v[132:135]\n\t" \
  "v_mfma_f32_16x16x32_bf16 v[140:143], v[180:183], v[68:71], v[140:143]\n\t" \
  "v_mfma_f32_16x16x32_bf16 v[128:131], v[184:187], v[72:75], v[128:131]\n\t" \
  "v_mfma_f32_16x16x32_bf16 v[136:139], v[184:187], v[76:79], v[136:139]\n\t" \
  "v_mfma_f32_16x16x32_bf16 v[132:135], v[188:191], v[72:75], v[132:135]\n\t" \
  "v_mfma_f32_16x16x32_bf16 v[140:143], v[188:191], v[76:79], v[140:143]\n\t" \
  "v_mfma_f32_16x16x32_bf16 v[128:131], v[192:195], v[80:83], v[128:131]\n\t" \
  "v_mfma_f32_16x16x32_bf16 v[136:139], v[192:195], v[84:87], v[136:139]\n\t" \
  "v_mfma_f32_16x16x32_bf16 v[132:135], v[196:199], v[80:83], v[132:135]\n\t" \
  "v_mfma_f32_16x16x32_bf16 v[140:143], v[196:199], v[84:87], v[140:143]\n\t" \
  "v_mfma_f32_16x16x32_bf16 v[128:131], v[200:203], v[88:91], v[128:131]\n\t" \
  "v_mfma_f32_16x16x32_bf16 v[136:139], v[200:203], v[92:95], v[136:139]\n\t" \
  "v_mfma_f32_16x16x32_bf16 v[132:135], v[204:207], v[88:91], v[132:135]\n\t" \
  "v_mfma_f32_16x16x32_bf16 v[140:143], v[204:207], v[92:95], v[140:143]\n\t" \
  "v_mfma_f32_16x16x32_bf16 v[128:131], v[208:211], v[96:99], v[128:131]\n\t" \
  "v_mfma_f32_16x16x32_bf16 v[136:139], v[208:211], v[100:103], v[136:139]\n\t" \
  "v_mfma_f32_16x16x32_bf16 v[132:135], v[212:215], v[96:99], v[132:135]\n\t" \
  "v_mfma_f32_16x16x32_bf16 v[140:143], v[212:215], v[100:103], v[140:143]\n\t" \
  "v_mfma_f32_16x16x32_bf16 v[128:131], v[216:219], v[104:107], v[128:131]\n\t" \
  "v_mfma_f32_16x16x32_bf16 v[136:139], v[216:219], v[108:111], v[136:139]\n\t" \
  "v_mfma_f32_16x16x32_bf16 v[132:135], v[220:223], v[104:107], v[132:135]\n\t" \
  "v_mfma_f32_16x16x32_bf16 v[140:143], v[220:223], v[108:111], v[140:143]\n\t" \
  "v_mfma_f32_16x16x32_bf16 v[128:131], v[224:227], v[112:115], v[128:131]\n\t" \
  "v_mfma_f32_16x16x32_bf16 v[136:139], v[224:227], v[116:119], v[136:139]\n\t" \
  "v_mfma_f32_16x16x32_bf16 v[132:135], v[228:231], v[112:115], v[132:135]\n\t" \
  "v_mfma_f32_16x16x32_bf16 v[140:143], v[228:231], v[116:119], v[140:143]\n\t" \
  "v_mfma_f32_16x16x32_bf16 v[128:131], v[232:235], v[120:123], v[128:131]\n\t" \
  "v_mfma_f32_16x16x32_bf16 v[136:139], v[232:235], v[124:127], v[136:139]\n\t" \
  "v_mfma_f32_16x16x32_bf16 v[132:135], v[236:239], v[120:123], v[132:135]\n\t" \
  "v_mfma_f32_16x16x32_bf16 v[140:143], v[236:239], v[124:127], v[140:143]\n\t" \
  "s_setprio 0\n\t" \
  "s_waitcnt vmcnt(9)\n\t" \
  "s_barrier\n\t" \
  "s_add_u32 s92, s92, 512\n\t" \
  "s_addc_u32 s93, s93, 0\n\t" \
  "ds_read_b128 v[176:179], v244 offset:0\n\t" \
  "ds_read_b128 v[180:183], v244 offset:16384\n\t" \
  "ds_read_b128 v[184:187], v245 offset:0\n\t" \
  "ds_read_b128 v[188:191], v245 offset:16384\n\t" \
  "s_add_u32 m0, %[lw], 32768\n\t" \
  "s_nop 0\n\t" \
  "global_load_lds_dwordx4 v248, s[92:93] sc1\n\t" \
  "ds_read_b128 v[192:195], v246 offset:0\n\t" \
  "ds_read_b128 v[196:199], v246 offset:16384\n\t" \
  "ds_read_b128 v[200:203], v247 offset:0\n\t" \
  "ds_read_b128 v[204:207], v247 offset:16384\n\t" \
  "s_add_u32 m0, %[lw], 33792\n\t" \
  "s_nop 0\n\t" \
  "global_load_lds_dwordx4 v249, s[92:93] sc1\n\t" \
  "ds_read_b128 v[208:211], v244 offset:256\n\t" \
  "ds_read_b128 v[212:215], v244 offset:16640\n\t" \
  "ds_read_b128 v[216:219], v245 offset:256\n\t" \
  "ds_read_b128 v[220:223], v245 offset:16640\n\t" \
  "s_add_u32 m0, %[lw], 34816\n\t" \
  "s_nop 0\n\t" \
  "global_load_lds_dwordx4 v250, s[92:93] sc1\n\t" \
  "ds_read_b128 v[224:227], v246 offset:256\n\t" \
  "ds_read_b128 v[228:231], v246 offset:16640\n\t" \
  "ds_read_b128 v[232:235], v247 offset:256\n\t" \
  "ds_read_b128 v[236:239], v247 offset:16640\n\t" \
  "s_add_u32 m0, %[lw], 35840\n\t" \
  "s_nop 0\n\t" \
  "global_load_lds_dwordx4 v251, s[92:93] sc1\n\t" \
  "ds_write_b128 v252, v[136:139] offset:0\n\t" \
  "ds_write_b128 v252, v[140:143] offset:1024\n\t" \
  "s_waitcnt lgkmcnt(0)\n\t" \
  "s_setprio 1\n\t" \
  "v_mfma_f32_16x16x32_bf16 v[144:147], v[176:179], v[0:3], 0\n\t" \
  "v_mfma_f32_16x16x32_bf16 v[152:155], v[176:179], v[4:7], 0\n\t" \
  "v_mfma_f32_16x16x32_bf16 v[148:151], v[180:183], v[0:3], 0\n\t" \
  "v_mfma_f32_16x16x32_bf16 v[156:159], v[180:183], v[4:7], 0\n\t" \
  "v_mfma_f32_16x16x32_bf16 v[144:147], v[184:187], v[8:11], v[144:147]\n\t" \
  "v_mfma_f32_16x16x32_bf16 v[152:155], v[184:187], v[12:15], v[152:155]\n\t" \
  "v_mfma_f32_16x16x32_bf16 v[148:151], v[188:191], v[8:11], v[148:151]\n\t" \
  "v_mfma_f32_16x16x32_bf16 v[156:159], v[188:191], v[12:15], v[156:159]\n\t" \
  "v_mfma_f32_16x16x32_bf16 v[144:147], v[192:195], v[16:19], v[144:147]\n\t" \
  "v_mfma_f32_16x16x32_bf16 v[152:155], v[192:195], v[20:23], v[152:155]\n\t" \
  "v_mfma_f32_16x16x32_bf16 v[148:151], v[196:199], v[16:19], v[148:151]\n\t" \
  "v_mfma_f32_16x16x32_bf16 v[156:159], v[196:199], v[20:23], v[156:159]\n\t" \
  "v_mfma_f32_16x16x32_bf16 v[144:147], v[200:203], v[24:27], v[144:147]\n\t" \
  "v_mfma_f32_16x16x32_bf16 v[152:155], v[200:203], v[28:31], v[152:155]\n\t" \
  "v_mfma_f32_16x16x32_bf16 v[148:151], v[204:207], v[24:27], v[148:151]\n\t" \
  "v_mfma_f32_16x16x32_bf16 v[156:159], v[204:207], v[28:31], v[156:159]\n\t" \
  "v_mfma_f32_16x16x32_bf16 v[144:147], v[208:211], v[32:35], v[144:147]\n\t" \
  "v_mfma_f32_16x16x32_bf16 v[152:155], v[208:211], v[36:39], v[152:155]\n\t" \
  "v_mfma_f32_16x16x32_bf16 v[148:151], v[212:215], v[32:35], v[148:151]\n\t" \
  "v_mfma_f32_16x16x32_bf16 v[156:159], v[212:215], v[36:39], v[156:159]\n\t" \
  "v_mfma_f32_16x16x32_bf16 v[144:147], v[216:219], v[40:43], v[144:147]\n\t" \
  "v_mfma_f32_16x16x32_bf16 v[152:155], v[216:219], v[44:47], v[152:155]\n\t" \
  "v_mfma_f32_16x16x32_bf16 v[148:151], v[220:223], v[40:43], v[148:151]\n\t" \
  "v_mfma_f32_16x16x32_bf16 v[156:159], v[220:223], v[44:47], v[156:159]\n\t" \
  "v_mfma_f32_16x16x32_bf16 v[144:147], v[224:227], v[48:51], v[144:147]\n\t" \
  "v_mfma_f32_16x16x32_bf16 v[152:155], v[224:227], v[52:55], v[152:155]\n\t" \
  "v_mfma_f32_16x16x32_bf16 v[148:151], v[228:231], v[48:51], v[148:151]\n\t" \
  "v_mfma_f32_16x16x32_bf16 v[156:159], v[228:231], v[52:55], v[156:159]\n\t" \
  "v_mfma_f32_16x16x32_bf16 v[144:147], v[232:235], v[56:59], v[144:147]\n\t" \
  "v_mfma_f32_16x16x32_bf16 v[152:155], v[232:235], v[60:63], v[152:155]\n\t" \
  "v_mfma_f32_16x16x32_bf16 v[148:151], v[236:239], v[56:59], v[148:151]\n\t" \
  "v_mfma_f32_16x16x32_bf16 v[156:159], v[236:239], v[60:63], v[156:159]\n\t" \
  "s_setprio 0\n\t" \
  "s_waitcnt vmcnt(4)\n\t" \
  "s_barrier\n\t" \
  "s_add_u32 s92, s92, 65024\n\t" \
  "s_addc_u32 s93, s93, 0\n\t" \
  "ds_read_b128 v[160:163], v253 offset:0\n\t" \
  "ds_read_b128 v[164:167], v253 offset:1024\n\t" \
  "ds_read_b128 v[176:179], v244 offset:32768\n\t" \
  "ds_read_b128 v[180:183], v244 offset:49152\n\t" \
  "ds_read_b128 v[184:187], v245 offset:32768\n\t" \
  "ds_read_b128 v[188:191], v245 offset:49152\n\t" \
  "s_add_u32 m0, %[lw], 65536\n\t" \
  "s_nop 0\n\t" \
  "global_load_lds_dwordx4 v248, s[92:93] sc1\n\t" \
  "ds_read_b128 v[192:195], v246 offset:32768\n\t" \
  "ds_read_b128 v[196:199], v246 offset:49152\n\t" \
  "ds_read_b128 v[200:203], v247 offset:32768\n\t" \
  "ds_read_b128 v[204:207], v247 offset:49152\n\t" \
  "s_add_u32 m0, %[lw], 66560\n\t" \
  "s_nop 0\n\t" \
  "global_load_lds_dwordx4 v249, s[92:93] sc1\n\t" \
  "ds_read_b128 v[208:211], v244 offset:33024\n\t" \
  "ds_read_b128 v[212:215], v244 offset:49408\n\t" \
  "ds_read_b128 v[216:219], v245 offset:33024\n\t" \
  "ds_read_b128 v[220:223], v245 offset:49408\n\t" \
  "s_add_u32 m0, %[lw], 67584\n\t" \
  "s_nop 0\n\t" \
  "global_load_lds_dwordx4 v250, s[92:93] sc1\n\t" \
  "ds_read_b128 v[224:227], v246 offset:33024\n\t" \
  "ds_read_b128 v[228:231], v246 offset:49408\n\t" \
  "ds_read_b128 v[232:235], v247 offset:33024\n\t" \
  "ds_read_b128 v[236:239], v247 offset:49408\n\t" \
  "s_add_u32 m0, %[lw], 68608\n\t" \
  "s_nop 0\n\t" \
  "global_load_lds_dwordx4 v251, s[92:93] sc1\n\t" \
  "s_waitcnt lgkmcnt(15)\n\t" \
  "s_waitcnt lgkmcnt(12)\n\t" \
  "s_lshr_b32 s90, s87, 7\n\t" \
  "s_xor_b32 s90, s90, %[s0]\n\t" \
  "s_and_b32 s90, s90, 1\n\t" \
  "s_lshl1_add_u32 s90, s87, s90\n\t" \
  "s_lshl_b32 s90, s90, 13\n\t" \
  "v_add_f32 v160, v160, v128\n\t" \
  "v_add_f32 v161, v161, v129\n\t" \
  "v_add_f32 v162, v162, v130\n\t" \
  "v_add_f32 v163, v163, v131\n\t" \
  "v_add_f32 v164, v164, v132\n\t" \
  "v_add_f32 v165, v165, v133\n\t" \
  "v_add_f32 v166, v166, v134\n\t" \
  "v_add_f32 v167, v167, v135\n\t" \
  "v_cvt_pk_bf16_f32 v168, v160, v161\n\t" \
  "v_cvt_pk_bf16_f32 v169, v162, v163\n\t" \
  "v_cvt_pk_bf16_f32 v170, v164, v165\n\t" \
  "v_cvt_pk_bf16_f32 v171, v166, v167\n\t" \
  "buffer_store_dwordx4 v[168:171], v172, %[rt], s90 offen sc1\n\t" \
  "s_add_u32 s86, s86, 1\n\t" \
  "s_add_u32 s87, s87, 1\n\t" \
  "s_waitcnt lgkmcnt(0)\n\t" \
  "s_setprio 1\n\t" \
  "v_mfma_f32_16x16x32_bf16 v[144:147], v[176:179], v[64:67], v[144:147]\n\t" \
  "v_mfma_f32_16x16x32_bf16 v[152:155], v[176:179], v[68:71], v[152:155]\n\t" \
  "v_mfma_f32_16x16x32_bf16 v[148:151], v[180:183], v[64:67], v[148:151]\n\t" \
  "v_mfma_f32_16x16x32_bf16 v[156:159], v[180:183], v[68:71], v[156:159]\n\t" \
  "v_mfma_f32_16x16x32_bf16 v[144:147], v[184:187], v[72:75], v[144:147]\n\t" \
  "v_mfma_f32_16x16x32_bf16 v[152:155], v[184:187], v[76:79], v[152:155]\n\t" \
  "v_mfma_f32_16x16x32_bf16 v[148:151], v[188:191], v[72:75], v[148:151]\n\t" \
  "v_mfma_f32_16x16x32_bf16 v[156:159], v[188:191], v[76:79], v[156:159]\n\t" \
  "v_mfma_f32_16x16x32_bf16 v[144:147], v[192:195], v[80:83], v[144:147]\n\t" \
  "v_mfma_f32_16x16x32_bf16 v[152:155], v[192:195], v[84:87], v[152:155]\n\t" \
  "v_mfma_f32_16x16x32_bf16 v[148:151], v[196:199], v[80:83], v[148:151]\n\t" \
  "v_mfma_f32_16x16x32_bf16 v[156:159], v[196:199], v[84:87], v[156:159]\n\t" \
  "v_mfma_f32_16x16x32_bf16 v[144:147], v[200:203], v[88:91], v[144:147]\n\t" \
  "v_mfma_f32_16x16x32_bf16 v[152:155], v[200:203], v[92:95], v[152:155]\n\t" \
  "v_mfma_f32_16x16x32_bf16 v[148:151], v[204:207], v[88:91], v[148:151]\n\t" \
  "v_mfma_f32_16x16x32_bf16 v[156:159], v[204:207], v[92:95], v[156:159]\n\t" \
  "v_mfma_f32_16x16x32_bf16 v[144:147], v[208:211], v[96:99], v[144:147]\n\t" \
  "v_mfma_f32_16x16x32_bf16 v[152:155], v[208:211], v[100:103], v[152:155]\n\t" \
  "v_mfma_f32_16x16x32_bf16 v[148:151], v[212:215], v[96:99], v[148:151]\n\t" \
  "v_mfma_f32_16x16x32_bf16 v[156:159], v[212:215], v[100:103], v[156:159]\n\t" \
  "v_mfma_f32_16x16x32_bf16 v[144:147], v[216:219], v[104:107], v[144:147]\n\t" \
  "v_mfma_f32_16x16x32_bf16 v[152:155], v[216:219], v[108:111], v[152:155]\n\t" \
  "v_mfma_f32_16x16x32_bf16 v[148:151], v[220:223], v[104:107], v[148:151]\n\t" \
  "v_mfma_f32_16x16x32_bf16 v[156:159], v[220:223], v[108:111], v[156:159]\n\t" \
  "v_mfma_f32_16x16x32_bf16 v[144:147], v[224:227], v[112:115], v[144:147]\n\t" \
  "v_mfma_f32_16x16x32_bf16 v[152:155], v[224:227], v[116:119], v[152:155]\n\t" \
  "v_mfma_f32_16x16x32_bf16 v[148:151], v[228:231], v[112:115], v[148:151]\n\t" \
  "v_mfma_f32_16x16x32_bf16 v[156:159], v[228:231], v[116:119], v[156:159]\n\t" \
  "v_mfma_f32_16x16x32_bf16 v[144:147], v[232:235], v[120:123], v[144:147]\n\t" \
  "v_mfma_f32_16x16x32_bf16 v[152:155], v[232:235], v[124:127], v[152:155]\n\t" \
  "v_mfma_f32_16x16x32_bf16 v[148:151], v[236:239], v[120:123], v[148:151]\n\t" \
  "v_mfma_f32_16x16x32_bf16 v[156:159], v[236:239], v[124:127], v[156:159]\n\t" \
  "s_setprio 0\n\t" \
  "s_cmp_lt_u32 s86, 64\n\t" \
  "s_cbranch_scc1 .Lp0%=\n\t" \
  "s_barrier\n\t" \
  "s_nop 1\n\t" \
  "ds_write_b128 v252, v[152:155] offset:0\n\t" \
  "s_nop 0\n\t" \
  "ds_write_b128 v252, v[156:159] offset:1024\n\t" \
  "s_waitcnt lgkmcnt(0)\n\t" \
  "s_barrier\n\t" \
  "ds_read_b128 v[160:163], v253 offset:0\n\t" \
  "ds_read_b128 v[164:167], v253 offset:1024\n\t" \
  "s_waitcnt lgkmcnt(0)\n\t" \
  "s_lshr_b32 s90, s87, 7\n\t" \
  "s_xor_b32 s90, s90, %[s0]\n\t" \
  "s_and_b32 s90, s90, 1\n\t" \
  "s_lshl1_add_u32 s90, s87, s90\n\t" \
  "s_lshl_b32 s90, s90, 13\n\t" \
  "v_add_f32 v160, v160, v144\n\t" \
  "v_add_f32 v161, v161, v145\n\t" \
  "v_add_f32 v162, v162, v146\n\t" \
  "v_add_f32 v163, v163, v147\n\t" \
  "v_add_f32 v164, v164, v148\n\t" \
  "v_add_f32 v165, v165, v149\n\t" \
  "v_add_f32 v166, v166, v150\n\t" \
  "v_add_f32 v167, v167, v151\n\t" \
  "v_cvt_pk_bf16_f32 v168, v160, v161\n\t" \
  "v_cvt_pk_bf16_f32 v169, v162, v163\n\t" \
  "v_cvt_pk_bf16_f32 v170, v164, v165\n\t" \
  "v_cvt_pk_bf16_f32 v171, v166, v167\n\t" \
  "buffer_store_dwordx4 v[168:171], v172, %[rt], s90 offen sc1\n\t" \
  "s_waitcnt vmcnt(0)\n\t" \
  "s_barrier\n\t" \
  "s_mov_b32 m0, s94\n\t" \
  "s_branch .Le%=\n\t" \
  ".Lk%=:\n\t" \
  "s_mov_b32 s94, m0\n\t" \
  "s_mov_b64 s[92:93], %[x0]\n\t" \
  "s_mov_b32 s86, %[g0]\n\t" \
  "s_add_u32 s87, s86, %[t0]\n\t" \
  "s_add_u32 m0, %[lw], 67584\n\t" \
  "s_nop 0\n\t" \
  "global_load_lds_dwordx4 v250, s[92:93] sc1\n\t" \
  "s_add_u32 m0, %[lw], 68608\n\t" \
  "s_nop 0\n\t" \
  "global_load_lds_dwordx4 v251, s[92:93] sc1\n\t" \
  "s_barrier\n\t" \
  "s_branch .Ls1%=\n\t" \
  ".Lp1%=:\n\t" \
  "s_waitcnt vmcnt(10)\n\t" \
  "s_barrier\n\t" \
  "s_setprio 2\n\t" \
  "v_mfma_f32_16x16x32_bf16 v[144:147], v[176:179], v[64:67], v[144:147]\n\t" \
  "v_mfma_f32_16x16x32_bf16 v[152:155], v[176:179], v[68:71], v[152:155]\n\t" \
  "v_mfma_f32_16x16x32_bf16 v[148:151], v[180:183], v[64:67], v[148:151]\n\t" \
  "v_mfma_f32_16x16x32_bf16 v[156:159], v[180:183], v[68:71], v[156:159]\n\t" \
  "v_mfma_f32_16x16x32_bf16 v[144:147], v[184:187], v[72:75], v[144:147]\n\t" \
  "v_mfma_f32_16x16x32_bf16 v[152:155], v[184:187], v[76:79], v[152:155]\n\t" \
  "v_mfma_f32_16x16x32_bf16 v[148:151], v[188:191], v[72:75], v[148:151]\n\t" \
  "v_mfma_f32_16x16x32_bf16 v[156:159], v[188:191], v[76:79], v[156:159]\n\t" \
  "v_mfma_f32_16x16x32_bf16 v[144:147], v[192:195], v[80:83], v[144:147]\n\t" \
  "v_mfma_f32_16x16x32_bf16 v[152:155], v[192:195], v[84:87], v[152:155]\n\t" \
  "v_mfma_f32_16x16x32_bf16 v[148:151], v[196:199], v[80:83], v[148:151]\n\t" \
  "v_mfma_f32_16x16x32_bf16 v[156:159], v[196:199], v[84:87], v[156:159]\n\t" \
  "v_mfma_f32_16x16x32_bf16 v[144:147], v[200:203], v[88:91], v[144:147]\n\t" \
  "v_mfma_f32_16x16x32_bf16 v[152:155], v[200:203], v[92:95], v[152:155]\n\t" \
  "v_mfma_f32_16x16x32_bf16 v[148:151], v[204:207], v[88:91], v[148:151]\n\t" \
  "v_mfma_f32_16x16x32_bf16 v[156:159], v[204:207], v[92:95], v[156:159]\n\t" \
  "v_mfma_f32_16x16x32_bf16 v[144:147], v[208:211], v[96:99], v[144:147]\n\t" \
  "v_mfma_f32_16x16x32_bf16 v[152:155], v[208:211], v[100:103], v[152:155]\n\t" \
  "v_mfma_f32_16x16x32_bf16 v[148:151], v[212:215], v[96:99], v[148:151]\n\t" \
  "v_mfma_f32_16x16x32_bf16 v[156:159], v[212:215], v[100:103], v[156:159]\n\t" \
  "v_mfma_f32_16x16x32_bf16 v[144:147], v[216:219], v[104:107], v[144:147]\n\t" \
  "v_mfma_f32_16x16x32_bf16 v[152:155], v[216:219], v[108:111], v[152:155]\n\t" \
  "v_mfma_f32_16x16x32_bf16 v[148:151], v[220:223], v[104:107], v[148:151]\n\t" \
  "v_mfma_f32_16x16x32_bf16 v[156:159], v[220:223], v[108:111], v[156:159]\n\t" \
  "v_mfma_f32_16x16x32_bf16 v[144:147], v[224:227], v[112:115], v[144:147]\n\t" \
  "v_mfma_f32_16x16x32_bf16 v[152:155], v[224:227], v[116:119], v[152:155]\n\t" \
  "v_mfma_f32_16x16x32_bf16 v[148:151], v[228:231], v[112:115], v[148:151]\n\t" \
  "v_mfma_f32_16x16x32_bf16 v[156:159], v[228:231], v[116:119], v[156:159]\n\t" \
  "v_mfma_f32_16x16x32_bf16 v[144:147], v[232:235], v[120:123], v[144:147]\n\t" \
  "v_mfma_f32_16x16x32_bf16 v[152:155], v[232:235], v[124:127], v[152:155]\n\t" \
  "v_mfma_f32_16x16x32_bf16 v[148:151], v[236:239], v[120:123], v[148:151]\n\t" \
  "v_mfma_f32_16x16x32_bf16 v[156:159], v[236:239], v[124:127], v[156:159]\n\t" \
  "s_setprio 0\n\t" \
  ".Ls1%=:\n\t" \
  "s_add_u32 s92, s92, 512\n\t" \
  "s_addc_u32 s93, s93, 0\n\t" \
  "ds_read_b128 v[176:179], v240 offset:0\n\t" \
  "ds_read_b128 v[180:183], v240 offset:16384\n\t" \
  "ds_read_b128 v[184:187], v241 offset:0\n\t" \
  "ds_read_b128 v[188:191], v241 offset:16384\n\t" \
  "s_add_u32 m0, %[lw], 98304\n\t" \
  "s_nop 0\n\t" \
  "global_load_lds_dwordx4 v248, s[92:93] sc1\n\t" \
  "ds_read_b128 v[192:195], v242 offset:0\n\t" \
  "ds_read_b128 v[196:199], v242 offset:16384\n\t" \
  "ds_read_b128 v[200:203], v243 offset:0\n\t" \
  "ds_read_b128 v[204:207], v243 offset:16384\n\t" \
  "s_add_u32 m0, %[lw], 99328\n\t" \
  "s_nop 0\n\t" \
  "global_load_lds_dwordx4 v249, s[92:93] sc1\n\t" \
  "ds_read_b128 v[208:211], v240 offset:256\n\t" \
  "ds_read_b128 v[212:215], v240 offset:16640\n\t" \
  "ds_read_b128 v[216:219], v241 offset:256\n\t" \
  "ds_read_b128 v[220:223], v241 offset:16640\n\t" \
  "s_add_u32 m0, %[lw], 100352\n\t" \
  "s_nop 0\n\t" \
  "global_load_lds_dwordx4 v250, s[92:93] sc1\n\t" \
  "ds_read_b128 v[224:227], v242 offset:256\n\t" \
  "ds_read_b128 v[228:231], v242 offset:16640\n\t" \
  "ds_read_b128 v[232:235], v243 offset:256\n\t" \
  "ds_read_b128 v[236:239], v243 offset:16640\n\t" \
  "s_add_u32 m0, %[lw], 101376\n\t" \
  "s_nop 0\n\t" \
  "global_load_lds_dwordx4 v251, s[92:93] sc1\n\t" \
  "ds_write_b128 v252, v[152:155] offset:0\n\t" \
  "ds_write_b128 v252, v[156:159] offset:1024\n\t" \
  "s_waitcnt lgkmcnt(0)\n\t" \
  "s_waitcnt vmcnt(4)\n\t" \
  "s_barrier\n\t" \
  "s_setprio 2\n\t" \
  "v_mfma_f32_16x16x32_bf16 v[128:131], v[176:179], v[0:3], 0\n\t" \
  "v_mfma_f32_16x16x32_bf16 v[136:139], v[176:179], v[4:7], 0\n\t" \
  "v_mfma_f32_16x16x32_bf16 v[132:135], v[180:183], v[0:3], 0\n\t" \
  "v_mfma_f32_16x16x32_bf16 v[140:143], v[180:183], v[4:7], 0\n\t" \
  "v_mfma_f32_16x16x32_bf16 v[128:131], v[184:187], v[8:11], v[128:131]\n\t" \
  "v_mfma_f32_16x16x32_bf16 v[136:139], v[184:187], v[12:15], v[136:139]\n\t" \
  "v_mfma_f32_16x16x32_bf16 v[132:135], v[188:191], v[8:11], v[132:135]\n\t" \
  "v_mfma_f32_16x16x32_bf16 v[140:143], v[188:191], v[12:15], v[140:143]\n\t" \
  "v_mfma_f32_16x16x32_bf16 v[128:131], v[192:195], v[16:19], v[128:131]\n\t" \
  "v_mfma_f32_16x16x32_bf16 v[136:139], v[192:195], v[20:23], v[136:139]\n\t" \
  "v_mfma_f32_16x16x32_bf16 v[132:135], v[196:199], v[16:19], v[132:135]\n\t" \
  "v_mfma_f32_16x16x32_bf16 v[140:143], v[196:199], v[20:23], v[140:143]\n\t" \
  "v_mfma_f32_16x16x32_bf16 v[128:131], v[200:203], v[24:27], v[128:131]\n\t" \
  "v_mfma_f32_16x16x32_bf16 v[136:139], v[200:203], v[28:31], v[136:139]\n\t" \
  "v_mfma_f32_16x16x32_bf16 v[132:135], v[204:207], v[24:27], v[132:135]\n\t" \
  "v_mfma_f32_16x16x32_bf16 v[140:143], v[204:207], v[28:31], v[140:143]\n\t" \
  "v_mfma_f32_16x16x32_bf16 v[128:131], v[208:211], v[32:35], v[128:131]\n\t" \
  "v_mfma_f32_16x16x32_bf16 v[136:139], v[208:211], v[36:39], v[136:139]\n\t" \
  "v_mfma_f32_16x16x32_bf16 v[132:135], v[212:215], v[32:35], v[132:135]\n\t" \
  "v_mfma_f32_16x16x32_bf16 v[140:143], v[212:215], v[36:39], v[140:143]\n\t" \
  "v_mfma_f32_16x16x32_bf16 v[128:131], v[216:219], v[40:43], v[128:131]\n\t" \
  "v_mfma_f32_16x16x32_bf16 v[136:139], v[216:219], v[44:47], v[136:139]\n\t" \
  "v_mfma_f32_16x16x32_bf16 v[132:135], v[220:223], v[40:43], v[132:135]\n\t" \
  "v_mfma_f32_16x16x32_bf16 v[140:143], v[220:223], v[44:47], v[140:143]\n\t" \
  "v_mfma_f32_16x16x32_bf16 v[128:131], v[224:227], v[48:51], v[128:131]\n\t" \
  "v_mfma_f32_16x16x32_bf16 v[136:139], v[224:227], v[52:55], v[136:139]\n\t" \
  "v_mfma_f32_16x16x32_bf16 v[132:135], v[228:231], v[48:51], v[132:135]\n\t" \
  "v_mfma_f32_16x16x32_bf16 v[140:143], v[228:231], v[52:55], v[140:143]\n\t" \
  "v_mfma_f32_16x16x32_bf16 v[128:131], v[232:235], v[56:59], v[128:131]\n\t" \
  "v_mfma_f32_16x16x32_bf16 v[136:139], v[232:235], v[60:63], v[136:139]\n\t" \
  "v_mfma_f32_16x16x32_bf16 v[132:135], v[236:239], v[56:59], v[132:135]\n\t" \
  "v_mfma_f32_16x16x32_bf16 v[140:143], v[236:239], v[60:63], v[140:143]\n\t" \
  "s_setprio 0\n\t" \
  "s_add_u32 s92, s92, 65024\n\t" \
  "s_addc_u32 s93, s93, 0\n\t" \
  "ds_read_b128 v[160:163], v253 offset:0\n\t" \
  "ds_read_b128 v[164:167], v253 offset:1024\n\t" \
  "ds_read_b128 v[176:179], v240 offset:32768\n\t" \
  "ds_read_b128 v[180:183], v240 offset:49152\n\t" \
  "ds_read_b128 v[184:187], v241 offset:32768\n\t" \
  "ds_read_b128 v[188:191], v241 offset:49152\n\t" \
  "s_add_u32 m0, %[lw], 0\n\t" \
  "s_nop 0\n\t" \
  "global_load_lds_dwordx4 v248, s[92:93] sc1\n\t" \
  "ds_read_b128 v[192:195], v242 offset:32768\n\t" \
  "ds_read_b128 v[196:199], v242 offset:49152\n\t" \
  "ds_read_b128 v[200:203], v243 offset:32768\n\t" \
  "ds_read_b128 v[204:207], v243 offset:49152\n\t" \
  "s_add_u32 m0, %[lw], 1024\n\t" \
  "s_nop 0\n\t" \
  "global_load_lds_dwordx4 v249, s[92:93] sc1\n\t" \
  "ds_read_b128 v[208:211], v240 offset:33024\n\t" \
  "ds_read_b128 v[212:215], v240 offset:49408\n\t" \
  "ds_read_b128 v[216:219], v241 offset:33024\n\t" \
  "ds_read_b128 v[220:223], v241 offset:49408\n\t" \
  "s_add_u32 m0, %[lw], 2048\n\t" \
  "s_nop 0\n\t" \
  "global_load_lds_dwordx4 v250, s[92:93] sc1\n\t" \
  "ds_read_b128 v[224:227], v242 offset:33024\n\t" \
  "ds_read_b128 v[228:231], v242 offset:49408\n\t" \
  "ds_read_b128 v[232:235], v243 offset:33024\n\t" \
  "ds_read_b128 v[236:239], v243 offset:49408\n\t" \
  "s_add_u32 m0, %[lw], 3072\n\t" \
  "s_nop 0\n\t" \
  "global_load_lds_dwordx4 v251, s[92:93] sc1\n\t" \
  "s_waitcnt lgkmcnt(15)\n\t" \
  "s_waitcnt lgkmcnt(12)\n\t" \
  "s_sub_i32 s91, s86, 2\n\t" \
  "v_mov_b32 v173, s91\n\t" \
  "buffer_store_dword v173, v174, %[rs], 0 offen sc1\n\t" \
  "s_lshr_b32 s90, s87, 7\n\t" \
  "s_xor_b32 s90, s90, %[s0]\n\t" \
  "s_and_b32 s90, s90, 1\n\t" \
  "s_lshl1_add_u32 s90, s87, s90\n\t" \
  "s_lshl_b32 s90, s90, 13\n\t" \
  "v_add_f32 v160, v160, v144\n\t" \
  "v_add_f32 v161, v161, v145\n\t" \
  "v_add_f32 v162, v162, v146\n\t" \
  "v_add_f32 v163, v163, v147\n\t" \
  "v_add_f32 v164, v164, v148\n\t" \
  "v_add_f32 v165, v165, v149\n\t" \
  "v_add_f32 v166, v166, v150\n\t" \
  "v_add_f32 v167, v167, v151\n\t" \
  "v_cvt_pk_bf16_f32 v168, v160, v161\n\t" \
  "v_cvt_pk_bf16_f32 v169, v162, v163\n\t" \
  "v_cvt_pk_bf16_f32 v170, v164, v165\n\t" \
  "v_cvt_pk_bf16_f32 v171, v166, v167\n\t" \
  "buffer_store_dwordx4 v[168:171], v172, %[rt], s90 offen sc1\n\t" \
  "s_add_u32 s86, s86, 1\n\t" \
  "s_add_u32 s87, s87, 1\n\t" \
  "s_waitcnt lgkmcnt(0)\n\t" \
  "s_waitcnt vmcnt(10)\n\t" \
  "s_barrier\n\t" \
  "s_setprio 2\n\t" \
  "v_mfma_f32_16x16x32_bf16 v[128:131], v[176:179], v[64:67], v[128:131]\n\t" \
  "v_mfma_f32_16x16x32_bf16 v[136:139], v[176:179], v[68:71], v[136:139]\n\t" \
  "v_mfma_f32_16x16x32_bf16 v[132:135], v[180:183], v[64:67], v[132:135]\n\t" \
  "v_mfma_f32_16x16x32_bf16 v[140:143], v[180:183], v[68:71], v[140:143]\n\t" \
  "v_mfma_f32_16x16x32_bf16 v[128:131], v[184:187], v[72:75], v[128:131]\n\t" \
  "v_mfma_f32_16x16x32_bf16 v[136:139], v[184:187], v[76:79], v[136:139]\n\t" \
  "v_mfma_f32_16x16x32_bf16 v[132:135], v[188:191], v[72:75], v[132:135]\n\t" \
  "v_mfma_f32_16x16x32_bf16 v[140:143], v[188:191], v[76:79], v[140:143]\n\t" \
  "v_mfma_f32_16x16x32_bf16 v[128:131], v[192:195], v[80:83], v[128:131]\n\t" \
  "v_mfma_f32_16x16x32_bf16 v[136:139], v[192:195], v[84:87], v[136:139]\n\t" \
  "v_mfma_f32_16x16x32_bf16 v[132:135], v[196:199], v[80:83], v[132:135]\n\t" \
  "v_mfma_f32_16x16x32_bf16 v[140:143], v[196:199], v[84:87], v[140:143]\n\t" \
  "v_mfma_f32_16x16x32_bf16 v[128:131], v[200:203], v[88:91], v[128:131]\n\t" \
  "v_mfma_f32_16x16x32_bf16 v[136:139], v[200:203], v[92:95], v[136:139]\n\t" \
  "v_mfma_f32_16x16x32_bf16 v[132:135], v[204:207], v[88:91], v[132:135]\n\t" \
  "v_mfma_f32_16x16x32_bf16 v[140:143], v[204:207], v[92:95], v[140:143]\n\t" \
  "v_mfma_f32_16x16x32_bf16 v[128:131], v[208:211], v[96:99], v[128:131]\n\t" \
  "v_mfma_f32_16x16x32_bf16 v[136:139], v[208:211], v[100:103], v[136:139]\n\t" \
  "v_mfma_f32_16x16x32_bf16 v[132:135], v[212:215], v[96:99], v[132:135]\n\t" \
  "v_mfma_f32_16x16x32_bf16 v[140:143], v[212:215], v[100:103], v[140:143]\n\t" \
  "v_mfma_f32_16x16x32_bf16 v[128:131], v[216:219], v[104:107], v[128:131]\n\t" \
  "v_mfma_f32_16x16x32_bf16 v[136:139], v[216:219], v[108:111], v[136:139]\n\t" \
  "v_mfma_f32_16x16x32_bf16 v[132:135], v[220:223], v[104:107], v[132:135]\n\t" \
  "v_mfma_f32_16x16x32_bf16 v[140:143], v[220:223], v[108:111], v[140:143]\n\t" \
  "v_mfma_f32_16x16x32_bf16 v[128:131], v[224:227], v[112:115], v[128:131]\n\t" \
  "v_mfma_f32_16x16x32_bf16 v[136:139], v[224:227], v[116:119], v[136:139]\n\t" \
  "v_mfma_f32_16x16x32_bf16 v[132:135], v[228:231], v[112:115], v[132:135]\n\t" \
  "v_mfma_f32_16x16x32_bf16 v[140:143], v[228:231], v[116:119], v[140:143]\n\t" \
  "v_mfma_f32_16x16x32_bf16 v[128:131], v[232:235], v[120:123], v[128:131]\n\t" \
  "v_mfma_f32_16x16x32_bf16 v[136:139], v[232:235], v[124:127], v[136:139]\n\t" \
  "v_mfma_f32_16x16x32_bf16 v[132:135], v[236:239], v[120:123], v[132:135]\n\t" \
  "v_mfma_f32_16x16x32_bf16 v[140:143], v[236:239], v[124:127], v[140:143]\n\t" \
  "s_setprio 0\n\t" \
  "s_add_u32 s92, s92, 512\n\t" \
  "s_addc_u32 s93, s93, 0\n\t" \
  "ds_read_b128 v[176:179], v244 offset:0\n\t" \
  "ds_read_b128 v[180:183], v244 offset:16384\n\t" \
  "ds_read_b128 v[184:187], v245 offset:0\n\t" \
  "ds_read_b128 v[188:191], v245 offset:16384\n\t" \
  "s_add_u32 m0, %[lw], 32768\n\t" \
  "s_nop 0\n\t" \
  "global_load_lds_dwordx4 v248, s[92:93] sc1\n\t" \
  "ds_read_b128 v[192:195], v246 offset:0\n\t" \
  "ds_read_b128 v[196:199], v246 offset:16384\n\t" \
  "ds_read_b128 v[200:203], v247 offset:0\n\t" \
  "ds_read_b128 v[204:207], v247 offset:16384\n\t" \
  "s_add_u32 m0, %[lw], 33792\n\t" \
  "s_nop 0\n\t" \
  "global_load_lds_dwordx4 v249, s[92:93] sc1\n\t" \
  "ds_read_b128 v[208:211], v244 offset:256\n\t" \
  "ds_read_b128 v[212:215], v244 offset:16640\n\t" \
  "ds_read_b128 v[216:219], v245 offset:256\n\t" \
  "ds_read_b128 v[220:223], v245 offset:16640\n\t" \
  "s_add_u32 m0, %[lw], 34816\n\t" \
  "s_nop 0\n\t" \
  "global_load_lds_dwordx4 v250, s[92:93] sc1\n\t" \
  "ds_read_b128 v[224:227], v246 offset:256\n\t" \
  "ds_read_b128 v[228:231], v246 offset:16640\n\t" \
  "ds_read_b128 v[232:235], v247 offset:256\n\t" \
  "ds_read_b128 v[236:239], v247 offset:16640\n\t" \
  "s_add_u32 m0, %[lw], 35840\n\t" \
  "s_nop 0\n\t" \
  "global_load_lds_dwordx4 v251, s[92:93] sc1\n\t" \
  "ds_write_b128 v252, v[136:139] offset:0\n\t" \
  "ds_write_b128 v252, v[140:143] offset:1024\n\t" \
  "s_waitcnt lgkmcnt(0)\n\t" \
  "s_waitcnt vmcnt(4)\n\t" \
  "s_barrier\n\t" \
  "s_setprio 2\n\t" \
  "v_mfma_f32_16x16x32_bf16 v[144:147], v[176:179], v[0:3], 0\n\t" \
  "v_mfma_f32_16x16x32_bf16 v[152:155], v[176:179], v[4:7], 0\n\t" \
  "v_mfma_f32_16x16x32_bf16 v[148:151], v[180:183], v[0:3], 0\n\t" \
  "v_mfma_f32_16x16x32_bf16 v[156:159], v[180:183], v[4:7], 0\n\t" \
  "v_mfma_f32_16x16x32_bf16 v[144:147], v[184:187], v[8:11], v[144:147]\n\t" \
  "v_mfma_f32_16x16x32_bf16 v[152:155], v[184:187], v[12:15], v[152:155]\n\t" \
  "v_mfma_f32_16x16x32_bf16 v[148:151], v[188:191], v[8:11], v[148:151]\n\t" \
  "v_mfma_f32_16x16x32_bf16 v[156:159], v[188:191], v[12:15], v[156:159]\n\t" \
  "v_mfma_f32_16x16x32_bf16 v[144:147], v[192:195], v[16:19], v[144:147]\n\t" \
  "v_mfma_f32_16x16x32_bf16 v[152:155], v[192:195], v[20:23], v[152:155]\n\t" \
  "v_mfma_f32_16x16x32_bf16 v[148:151], v[196:199], v[16:19], v[148:151]\n\t" \
  "v_mfma_f32_16x16x32_bf16 v[156:159], v[196:199], v[20:23], v[156:159]\n\t" \
  "v_mfma_f32_16x16x32_bf16 v[144:147], v[200:203], v[24:27], v[144:147]\n\t" \
  "v_mfma_f32_16x16x32_bf16 v[152:155], v[200:203], v[28:31], v[152:155]\n\t" \
  "v_mfma_f32_16x16x32_bf16 v[148:151], v[204:207], v[24:27], v[148:151]\n\t" \
  "v_mfma_f32_16x16x32_bf16 v[156:159], v[204:207], v[28:31], v[156:159]\n\t" \
  "v_mfma_f32_16x16x32_bf16 v[144:147], v[208:211], v[32:35], v[144:147]\n\t" \
  "v_mfma_f32_16x16x32_bf16 v[152:155], v[208:211], v[36:39], v[152:155]\n\t" \
  "v_mfma_f32_16x16x32_bf16 v[148:151], v[212:215], v[32:35], v[148:151]\n\t" \
  "v_mfma_f32_16x16x32_bf16 v[156:159], v[212:215], v[36:39], v[156:159]\n\t" \
  "v_mfma_f32_16x16x32_bf16 v[144:147], v[216:219], v[40:43], v[144:147]\n\t" \
  "v_mfma_f32_16x16x32_bf16 v[152:155], v[216:219], v[44:47], v[152:155]\n\t" \
  "v_mfma_f32_16x16x32_bf16 v[148:151], v[220:223], v[40:43], v[148:151]\n\t" \
  "v_mfma_f32_16x16x32_bf16 v[156:159], v[220:223], v[44:47], v[156:159]\n\t" \
  "v_mfma_f32_16x16x32_bf16 v[144:147], v[224:227], v[48:51], v[144:147]\n\t" \
  "v_mfma_f32_16x16x32_bf16 v[152:155], v[224:227], v[52:55], v[152:155]\n\t" \
  "v_mfma_f32_16x16x32_bf16 v[148:151], v[228:231], v[48:51], v[148:151]\n\t" \
  "v_mfma_f32_16x16x32_bf16 v[156:159], v[228:231], v[52:55], v[156:159]\n\t" \
  "v_mfma_f32_16x16x32_bf16 v[144:147], v[232:235], v[56:59], v[144:147]\n\t" \
  "v_mfma_f32_16x16x32_bf16 v[152:155], v[232:235], v[60:63], v[152:155]\n\t" \
  "v_mfma_f32_16x16x32_bf16 v[148:151], v[236:239], v[56:59], v[148:151]\n\t" \
  "v_mfma_f32_16x16x32_bf16 v[156:159], v[236:239], v[60:63], v[156:159]\n\t" \
  "s_setprio 0\n\t" \
  "s_add_u32 s92, s92, 65024\n\t" \
  "s_addc_u32 s93, s93, 0\n\t" \
  "ds_read_b128 v[160:163], v253 offset:0\n\t" \
  "ds_read_b128 v[164:167], v253 offset:1024\n\t" \
  "ds_read_b128 v[176:179], v244 offset:32768\n\t" \
  "ds_read_b128 v[180:183], v244 offset:49152\n\t" \
  "ds_read_b128 v[184:187], v245 offset:32768\n\t" \
  "ds_read_b128 v[188:191], v245 offset:49152\n\t" \
  "s_add_u32 m0, %[lw], 65536\n\t" \
  "s_nop 0\n\t" \
  "global_load_lds_dwordx4 v248, s[92:93] sc1\n\t" \
  "ds_read_b128 v[192:195], v246 offset:32768\n\t" \
  "ds_read_b128 v[196:199], v246 offset:49152\n\t" \
  "ds_read_b128 v[200:203], v247 offset:32768\n\t" \
  "ds_read_b128 v[204:207], v247 offset:49152\n\t" \
  "s_add_u32 m0, %[lw], 66560\n\t" \
  "s_nop 0\n\t" \
  "global_load_lds_dwordx4 v249, s[92:93] sc1\n\t" \
  "ds_read_b128 v[208:211], v244 offset:33024\n\t" \
  "ds_read_b128 v[212:215], v244 offset:49408\n\t" \
  "ds_read_b128 v[216:219], v245 offset:33024\n\t" \
  "ds_read_b128 v[220:223], v245 offset:49408\n\t" \
  "s_add_u32 m0, %[lw], 67584\n\t" \
  "s_nop 0\n\t" \
  "global_load_lds_dwordx4 v250, s[92:93] sc1\n\t" \
  "ds_read_b128 v[224:227], v246 offset:33024\n\t" \
  "ds_read_b128 v[228:231], v246 offset:49408\n\t" \
  "ds_read_b128 v[232:235], v247 offset:33024\n\t" \
  "ds_read_b128 v[236:239], v247 offset:49408\n\t" \
  "s_add_u32 m0, %[lw], 68608\n\t" \
  "s_nop 0\n\t" \
  "global_load_lds_dwordx4 v251, s[92:93] sc1\n\t" \
  "s_waitcnt lgkmcnt(15)\n\t" \
  "s_waitcnt lgkmcnt(12)\n\t" \
  "s_sub_i32 s91, s86, 2\n\t" \
  "v_mov_b32 v173, s91\n\t" \
  "buffer_store_dword v173, v174, %[rs], 0 offen sc1\n\t" \
  "s_lshr_b32 s90, s87, 7\n\t" \
  "s_xor_b32 s90, s90, %[s0]\n\t" \
  "s_and_b32 s90, s90, 1\n\t" \
  "s_lshl1_add_u32 s90, s87, s90\n\t" \
  "s_lshl_b32 s90, s90, 13\n\t" \
  "v_add_f32 v160, v160, v128\n\t" \
  "v_add_f32 v161, v161, v129\n\t" \
  "v_add_f32 v162, v162, v130\n\t" \
  "v_add_f32 v163, v163, v131\n\t" \
  "v_add_f32 v164, v164, v132\n\t" \
  "v_add_f32 v165, v165, v133\n\t" \
  "v_add_f32 v166, v166, v134\n\t" \
  "v_add_f32 v167, v167, v135\n\t" \
  "v_cvt_pk_bf16_f32 v168, v160, v161\n\t" \
  "v_cvt_pk_bf16_f32 v169, v162, v163\n\t" \
  "v_cvt_pk_bf16_f32 v170, v164, v165\n\t" \
  "v_cvt_pk_bf16_f32 v171, v166, v167\n\t" \
  "buffer_store_dwordx4 v[168:171], v172, %[rt], s90 offen sc1\n\t" \
  "s_add_u32 s86, s86, 1\n\t" \
  "s_add_u32 s87, s87, 1\n\t" \
  "s_waitcnt lgkmcnt(0)\n\t" \
  "s_cmp_lt_u32 s86, 64\n\t" \
  "s_cbranch_scc1 .Lp1%=\n\t" \
  "s_barrier\n\t" \
  "s_setprio 2\n\t" \
  "v_mfma_f32_16x16x32_bf16 v[144:147], v[176:179], v[64:67], v[144:147]\n\t" \
  "v_mfma_f32_16x16x32_bf16 v[152:155], v[176:179], v[68:71], v[152:155]\n\t" \
  "v_mfma_f32_16x16x32_bf16 v[148:151], v[180:183], v[64:67], v[148:151]\n\t" \
  "v_mfma_f32_16x16x32_bf16 v[156:159], v[180:183], v[68:71], v[156:159]\n\t" \
  "v_mfma_f32_16x16x32_bf16 v[144:147], v[184:187], v[72:75], v[144:147]\n\t" \
  "v_mfma_f32_16x16x32_bf16 v[152:155], v[184:187], v[76:79], v[152:155]\n\t" \
  "v_mfma_f32_16x16x32_bf16 v[148:151], v[188:191], v[72:75], v[148:151]\n\t" \
  "v_mfma_f32_16x16x32_bf16 v[156:159], v[188:191], v[76:79], v[156:159]\n\t" \
  "v_mfma_f32_16x16x32_bf16 v[144:147], v[192:195], v[80:83], v[144:147]\n\t" \
  "v_mfma_f32_16x16x32_bf16 v[152:155], v[192:195], v[84:87], v[152:155]\n\t" \
  "v_mfma_f32_16x16x32_bf16 v[148:151], v[196:199], v[80:83], v[148:151]\n\t" \
  "v_mfma_f32_16x16x32_bf16 v[156:159], v[196:199], v[84:87], v[156:159]\n\t" \
  "v_mfma_f32_16x16x32_bf16 v[144:147], v[200:203], v[88:91], v[144:147]\n\t" \
  "v_mfma_f32_16x16x32_bf16 v[152:155], v[200:203], v[92:95], v[152:155]\n\t" \
  "v_mfma_f32_16x16x32_bf16 v[148:151], v[204:207], v[88:91], v[148:151]\n\t" \
  "v_mfma_f32_16x16x32_bf16 v[156:159], v[204:207], v[92:95], v[156:159]\n\t" \
  "v_mfma_f32_16x16x32_bf16 v[144:147], v[208:211], v[96:99], v[144:147]\n\t" \
  "v_mfma_f32_16x16x32_bf16 v[152:155], v[208:211], v[100:103], v[152:155]\n\t" \
  "v_mfma_f32_16x16x32_bf16 v[148:151], v[212:215], v[96:99], v[148:151]\n\t" \
  "v_mfma_f32_16x16x32_bf16 v[156:159], v[212:215], v[100:103], v[156:159]\n\t" \
  "v_mfma_f32_16x16x32_bf16 v[144:147], v[216:219], v[104:107], v[144:147]\n\t" \
  "v_mfma_f32_16x16x32_bf16 v[152:155], v[216:219], v[108:111], v[152:155]\n\t" \
  "v_mfma_f32_16x16x32_bf16 v[148:151], v[220:223], v[104:107], v[148:151]\n\t" \
  "v_mfma_f32_16x16x32_bf16 v[156:159], v[220:223], v[108:111], v[156:159]\n\t" \
  "v_mfma_f32_16x16x32_bf16 v[144:147], v[224:227], v[112:115], v[144:147]\n\t" \
  "v_mfma_f32_16x16x32_bf16 v[152:155], v[224:227], v[116:119], v[152:155]\n\t" \
  "v_mfma_f32_16x16x32_bf16 v[148:151], v[228:231], v[112:115], v[148:151]\n\t" \
  "v_mfma_f32_16x16x32_bf16 v[156:159], v[228:231], v[116:119], v[156:159]\n\t" \
  "v_mfma_f32_16x16x32_bf16 v[144:147], v[232:235], v[120:123], v[144:147]\n\t" \
  "v_mfma_f32_16x16x32_bf16 v[152:155], v[232:235], v[124:127], v[152:155]\n\t" \
  "v_mfma_f32_16x16x32_bf16 v[148:151], v[236:239], v[120:123], v[148:151]\n\t" \
  "v_mfma_f32_16x16x32_bf16 v[156:159], v[236:239], v[124:127], v[156:159]\n\t" \
  "s_setprio 0\n\t" \
  "s_nop 4\n\t" \
  "ds_write_b128 v252, v[152:155] offset:0\n\t" \
  "s_nop 0\n\t" \
  "ds_write_b128 v252, v[156:159] offset:1024\n\t" \
  "s_waitcnt lgkmcnt(0)\n\t" \
  "s_barrier\n\t" \
  "ds_read_b128 v[160:163], v253 offset:0\n\t" \
  "ds_read_b128 v[164:167], v253 offset:1024\n\t" \
  "s_waitcnt lgkmcnt(0)\n\t" \
  "s_lshr_b32 s90, s87, 7\n\t" \
  "s_xor_b32 s90, s90, %[s0]\n\t" \
  "s_and_b32 s90, s90, 1\n\t" \
  "s_lshl1_add_u32 s90, s87, s90\n\t" \
  "s_lshl_b32 s90, s90, 13\n\t" \
  "v_add_f32 v160, v160, v144\n\t" \
  "v_add_f32 v161, v161, v145\n\t" \
  "v_add_f32 v162, v162, v146\n\t" \
  "v_add_f32 v163, v163, v147\n\t" \
  "v_add_f32 v164, v164, v148\n\t" \
  "v_add_f32 v165, v165, v149\n\t" \
  "v_add_f32 v166, v166, v150\n\t" \
  "v_add_f32 v167, v167, v151\n\t" \
  "v_cvt_pk_bf16_f32 v168, v160, v161\n\t" \
  "v_cvt_pk_bf16_f32 v169, v162, v163\n\t" \
  "v_cvt_pk_bf16_f32 v170, v164, v165\n\t" \
  "v_cvt_pk_bf16_f32 v171, v166, v167\n\t" \
  "buffer_store_dwordx4 v[168:171], v172, %[rt], s90 offen sc1\n\t" \
  "s_waitcnt vmcnt(0)\n\t" \
  "s_barrier\n\t" \
  "s_sub_i32 s91, s86, 1\n\t" \
  "v_mov_b32 v173, s91\n\t" \
  "buffer_store_dword v173, v174, %[rs], 0 offen sc1\n\t" \
  "s_mov_b32 m0, s94\n\t" \
  ".Le%=:\n\t"
constexpr int W_X = 131072, W_PM = W_X + 16384;
template <bool ISV>
__device__ __forceinline__ void job_proj(Args& p, int g, int which, int h, char* smem, __amdgpu_buffer_rsrc_t rsy) {
  const int tid = threadIdx.x, lane = tid & 63, r = lane & 15, q = lane >> 4;
  const int w = __builtin_amdgcn_readfirstlane(tid >> 6), kh = w >> 2, cg = w & 3;
  u32x32 W[4];
  const unsigned lw = lds_off(smem) + w * 4096;
  u32x4 vx;
  u32x2 tag;
  const char* xn = p.ws + WS_XN;
  auto row = [&](int t, int i) {
    const char* src = xn + (size_t)(min(t, 127) >> 1) * (SEG * XLD * 2) + (t & 1) * 512;
    unsigned keep;
    asm volatile("s_mov_b32 %0, m0\n\ts_mov_b32 m0, %3\n\ts_nop 0\n\tglobal_load_lds_dwordx4 %1, %2 sc1\n\ts_mov_b32 m0, %0"
                 : "=&s"(keep) : "v"(vx[i]), "s"(src), "s"(lw + (t & 3) * 32768 + i * 1024) : "memory");
  };
  {
    const u16* Wm = which == 0 ? p.Wk : p.Wq;
    asm volatile("" : "+s"(Wm));
    if (ISV) Wm = p.Wv;
    const unsigned vo = (tid >> 4) * (HD * 2) + 16 * ((lane & 15) ^ swzB((tid >> 4) & 15));
    auto stage = [&](int j) {
      const char* src = (const char*)(Wm + (size_t)(WREV ? 7 - j : j) * 64 * HD + h * DH);
      const unsigned dst = lds_off(smem) + (j & 3) * 32768 + w * 1024;
      glds2(src, 32 * HD * 2, vo, dst);
      if (TH_P < 63) HOLD_V((h & 3) ^ 1, TH_P);
      glds2(src + 512 * HD * 2, 32 * HD * 2, vo, dst + 16384);
      if (TH_P < 63) HOLD_V((h & 3) ^ 1, TH_P);
    };
    if (which == 1) { for (int z = 0; z < Q_DELAY; ++z) __builtin_amdgcn_s_sleep(16); }
    const unsigned own = min((h & LN_M) ^ LN_X, 1);
    tag = start_rows<16>(p, 32 + g, own, [&]() { stage(0); stage(1); stage(2); stage(3); });
#pragma unroll
    for (int i = 0; i < 4; ++i) vx[i] = ((4 * w + i) ^ (ISV ? 0 : (lane >> 1) & 16)) * (XLD * 2) + (lane >> 5) * 1024 + (((lane & 31) ^ ((4 * w + i) & 15)) << 4);
    Boot bt;
#pragma unroll
    for (int j = 0; j < 8; ++j) {
      if (j < 4) WAIT_V(16); else if (j == 4) WAIT_V(12); else if (j == 5) WAIT_V(8); else if (j == 6) WAIT_V(4); else WAIT_V(10);
      if (j == 6 && w == 0) lds_at(smem + W_PM)[0] = boot_done(p.ws, bt, true, tag);
      LBAR();
      if (j == 4 && own) report_rows(p, 32 + g, tag);
      const char* sb = smem + (j & 3) * 32768 + kh * 16384;
#pragma unroll
      for (int k = 0; k < 4; ++k) {
        const int ks = k >> 1, a = k & 1;
        const unsigned ch = ISV ? 2 * (2 * cg + (a ^ kh)) + ((r & 3) >> 1) : 4 * cg + (r & 3), by = ISV ? 8 * (r & 1) : 8 * a;
        s16x4 lo = __builtin_amdgcn_ds_read_tr16_b64_v4i16((lds_s16x4_ptr)(sb + offB(32 * ks + 8 * q + (r >> 2), ch) + by));
        s16x4 hi = __builtin_amdgcn_ds_read_tr16_b64_v4i16((lds_s16x4_ptr)(sb + offB(32 * ks + 8 * q + 4 + (r >> 2), ch) + by));
        const int f = 4 * (WREV ? 7 - j : j) + k;
        put4(W[f >> 3], f & 7, (bf16x8){lo[0], lo[1], lo[2], lo[3], hi[0], hi[1], hi[2], hi[3]});
      }
      LBAR();
      if (j == 3 && w == 0) boot_ask(p.ws, bt);
      if (j + 4 < 8) stage(j + 4);
      if (j == 6) {
#pragma unroll
        for (int i = 0; i < 10; ++i) row(i >> 2, i & 3);
      }
    }
    p.s0 = uni(lds_at(smem + W_PM)[0] >> 31);
  }
  RDY(128 + g);
  typedef __attribute__((ext_vector_type(8))) unsigned u32x8;
  u32x8 la;
#pragma unroll
  for (int i = 0; i < 8; ++i) la[i] = (i >> 2) * 65536 + r * 1024 + kh * 512 + (((4 * (i & 3) + q) ^ r) << 4);
  const unsigned xw = W_X + w * 2048 + lane * 16;
  u32x32 acc;
  asm volatile("" : "={v[128:159]}"(acc));
  WAIT_V(6);
  LBAR();
  {
    const int g0 = 0;
#define PR_LOOP(TXT)                                                                                                              \
    asm volatile(TXT                                                                                                              \
        : "+{v[128:159]}"(acc)                                                                                                    \
        : W4_IN, "{v[208:215]}"(la), "{v[216:219]}"(vx), "{v220}"(xw), "{v221}"(xw ^ 8192),                                       \
          "{v172}"((ISV ? 2 * cg + kh : 4 * kh + cg) * 1024 + lane * 16), "{v174}"(tid == 256 ? fkq(h, which) : 0x7F000000),      \
          [rt] "s"(raw_rsrc(p.ws + WS_T, T_BYTES)), [rs] "s"(raw_rsrc(p.ws + WS_SY, SY_TOTAL)), [s0] "s"(p.s0 ^ TF_KQV),          \
          [lw] "s"(lw), [kh] "s"(kh), [g0] "s"(g0), [end] "s"(g0 ? NSEG : PR_G1), [x0] "s"(xn + (g0 + 1) * (SEG * XLD * 2)),                       \
          [t0] "s"(which * T_Q + kseg(0, h) - 1)                                                                                  \
        : CLB8(160, 161, 162, 163, 164, 165, 166, 167), "v168", "v169", "v170", "v171", "v173", CLB8(176, 177, 178, 179, 180, 181, 182, 183), \
          CLB8(184, 185, 186, 187, 188, 189, 190, 191), CLB8(192, 193, 194, 195, 196, 197, 198, 199),                             \
          CLB8(200, 201, 202, 203, 204, 205, 206, 207), "s86", "s87", "s88", "s90", "s91", "s92", "s93", "s94", "scc", "memory")
    if (ISV) PR_LOOP(PR_ASM_1); else PR_LOOP(PR_ASM_0);
    if (w == 0 && g0 == 0) wait_rows(p.ws, tag);
  }
#define PP_LOOP(TXT)                                                                                                              \
  asm volatile(TXT                                                                                                                \
      : "+{v[128:159]}"(acc)                                                                                                      \
      : W4_IN, "{v[240:247]}"(la), "{v[248:251]}"(vx), "{v252}"(xw), "{v253}"(xw ^ 8192),                                         \
        "{v172}"((ISV ? 2 * cg + kh : 4 * kh + cg) * 1024 + lane * 16), "{v174}"(tid == 256 ? fkq(h, which) : 0x7F000000),        \
        [rt] "s"(raw_rsrc(p.ws + WS_T, T_BYTES)), [rs] "s"(raw_rsrc(p.ws + WS_SY, SY_TOTAL)), [s0] "s"(p.s0 ^ TF_KQV),            \
        [lw] "s"(lw), [kh] "s"(kh), [g0] "n"(PR_G1), [x0] "s"(xn + (PR_G1 + 1) * (SEG * XLD * 2)),                                \
        [t0] "s"(which * T_Q + kseg(0, h) - 1)                                                                                    \
      : CLB8(160, 161, 162, 163, 164, 165, 166, 167), "v168", "v169", "v170", "v171", "v173", CLB8(176, 177, 178, 179, 180, 181, 182, 183), \
        CLB8(184, 185, 186, 187, 188, 189, 190, 191), CLB32(192), CLB8(224, 225, 226, 227, 228, 229, 230, 231),                   \
        CLB8(232, 233, 234, 235, 236, 237, 238, 239), "s86", "s87", "s90", "s91", "s92", "s93", "s94", "scc", "memory")
  if (ISV) PP_LOOP(PP_ASM_1); else PP_LOOP(PP_ASM_0);
}

enum { J_NONE = 0, J_GU = 3, J_PR = 5, J_CB = 8, J_DN = 14 };
struct BigJob { int type, panel, idx; };
struct BigPtr { const char *a, *b0, *b1; };
constexpr int G_LDA = XLD * 2, G_LDB = FF * 2;

__device__ __forceinline__ BigPtr big_ptrs(const Args& p, const BigJob& j) {
  BigPtr r;
  r.a = p.ws + WS_XN + (size_t)j.panel * 128 * G_LDA;
  r.b0 = (const char*)(p.wg + j.idx * 128); r.b1 = (const char*)(p.wu + j.idx * 128);
  return r;
}
__device__ __forceinline__ void big_publish(const Args& p, const BigJob& j) {
  int tid = threadIdx.x;
  asm volatile("" : "+v"(tid));
  if (j.type == J_GU && tid == 0)
    __hip_atomic_fetch_add((gu32*)(p.ws + WS_SY + SY_CH) + j.panel * 16, 1u, __ATOMIC_RELAXED, __HIP_MEMORY_SCOPE_AGENT);
}
typedef f32x4 Acc[2][2][4][2];
__device__ __forceinline__ u32x4 pack32(f32x4 a, f32x4 b) {
  return widen32(cvtpk(a[0], a[1]), cvtpk(a[2], a[3]), cvtpk(b[0], b[1]), cvtpk(b[2], b[3]));
}
#define st_epi(r, o, v) st_sc1(r, o, v)
__device__ __forceinline__ void big_epilogue(const Args& p, const BigJob& j, Acc& acc, char* smem) {
  int tid = threadIdx.x;
  asm volatile("" : "+v"(tid));
  const int lane = tid & 63;
  const int wid = __builtin_amdgcn_readfirstlane(tid >> 6);
  const int wr = wid >> 2, wc = wid & 3;
  {
    const __amdgpu_buffer_rsrc_t ro = mkrsrc(p.ws + WS_HF, S_LEN * 16384);
#pragma unroll
    for (int i = 0; i < 8; ++i) {
      f32x4 hh[2];
#pragma unroll
      for (int n = 0; n < 2; ++n)
#pragma unroll
        for (int jj = 0; jj < 4; ++jj) {
          const float g = acc[i >> 2][0][i & 3][n][jj], u = acc[i >> 2][1][i & 3][n][jj];
          hh[n][jj] = __builtin_amdgcn_rcpf(1.0f + __builtin_amdgcn_exp2f(g * -1.4426950408889634f)) * u;
        }
      st_epi(ro, h_off(p, j.panel + (i >> 2)) + (j.idx * 4 + wc) * 16384 + (wr * 4 + (i & 3)) * 1024 + lane * 16,
             ((u32x4){cvtpk(hh[0][0], hh[0][1]), cvtpk(hh[0][2], hh[0][3]), cvtpk(hh[1][0], hh[1][1]), cvtpk(hh[1][2], hh[1][3])}));
    }
  }
}
#define JST(n, i, v) do {} while (0)
__device__ __forceinline__ void gemm_role(Args& p, int g, char* smem) {
  const __amdgpu_buffer_rsrc_t rsy = mkrsrc(p.ws + WS_SY, SY_TOTAL);
  const int x = g & 3, k = g >> 2;
  volatile unsigned* slot = reinterpret_cast<volatile unsigned*>(smem + 163824);
  int rnd = k < 24 ? 2 : -1;
  int nj = 0; (void)nj;
  auto claim = [&]() {
    ++rnd;
    const int n = k < 24 ? 32 + k : rnd * 8 + k - 24, pn = (n * 37) >> 8;
    BigJob b;
    b.type = rnd < 4 ? J_GU : (rnd == 4 ? J_DN : (rnd == 5 && k >= 24 && k - 24 < 14 - NFUSE ? J_CB : J_NONE));
    b.panel = rnd < 4 ? 14 - 2 * pn : (rnd == 4 ? 4 * ((2 - (k >> 3)) & 3) + x : NFUSE + k - 24);
    b.idx = rnd < 4 ? x + 4 * (n - 7 * pn) : k & 7;
    return b;
  };
  if (g < 4 && threadIdx.x == 0) { LST(480 + 4 * g); LSTC(480 + 4 * g + 1); }
  if (k < 24) {
    JST(nj, 0, J_PR); JST(nj, 1, __builtin_amdgcn_s_memrealtime());
    if (k >= 16) job_proj<true>(p, g, 2, x * 8 + (k & 7), smem, rsy);
    else job_proj<false>(p, g, k >> 3, x * 8 + (k & 7), smem, rsy);
    JST(nj, 3, __builtin_amdgcn_s_memrealtime()); ++nj;
  } else {
    const unsigned t0 = (unsigned)__builtin_amdgcn_s_memrealtime();
    u32x2 tag;
#pragma unroll 1
    for (int u = 0; u < (k < 30 && LN_M ? 2 : 1); ++u) {
      const int rg = u ? 32 + (4 * (k - 24) + 1) * 4 + x : g - 96;
      tag = start_rows<0>(p, rg, 1, []() {});
      WAIT_V(0);
      LBAR();
      report_rows(p, rg, tag);
    }
    while ((unsigned)__builtin_amdgcn_s_memrealtime() - t0 < PF_T0) __builtin_amdgcn_s_sleep(8);
#pragma unroll 1
    for (int i = GPF == 2 ? 2 : 0; i < PF_N; i += GPF == 2 ? 3 : 1) {
      const int m = i % 3, o = ((i / 3) * 32 + g - 96) * 448 + (int)threadIdx.x;
      if (threadIdx.x < 448) touch_line((const char*)(m == 0 ? p.wg : (m == 1 ? p.wu : p.wd)) + (size_t)o * 128, smem + (threadIdx.x >> 6) * 256);
      WAIT_V(1);
    }
    WAIT_V(0);
    if (threadIdx.x < 64) {
      wait_rows(p.ws, tag);
      Boot bt;
      boot_ask(p.ws, bt);
      WAIT_V(0);
      *slot = boot_done(p.ws, bt, true, tag);
      while ((unsigned)__builtin_amdgcn_s_memrealtime() - t0 < GU_DELAY) __builtin_amdgcn_s_sleep(8);
    }
    LBAR();
    p.s0 = *slot >> 31;
    RDY(128 + g);
  }
  p.s0 = uni(p.s0);
  BigJob cur = claim();
  while (cur.type != J_NONE) {
    JST(nj, 0, cur.type | cur.panel << 8); JST(nj, 1, __builtin_amdgcn_s_memrealtime());
    if (cur.type != J_GU) {
      if (cur.type == J_DN) job_down(p, cur.panel, cur.idx, smem, rsy);
      else job_comb(p, cur.panel, x, rsy);
      JST(nj, 3, __builtin_amdgcn_s_memrealtime()); ++nj;
      cur = claim();
      continue;
    }
    int tid = threadIdx.x;
    asm volatile("" : "+v"(tid));
    const int lane = tid & 63;
    const int wid = __builtin_amdgcn_readfirstlane(tid >> 6);
    const int wr = wid >> 2, wc = wid & 3;
    const unsigned ra = tid >> 3, ca = 16 * ((tid & 7) ^ ((tid >> 3) & 7)), rb = tid >> 4, cb = 16 * ((tid & 15) ^ swzB((tid >> 4) & 15));
    const unsigned lw = lds_off(smem) + wid * 1024;
#define B8_SA(b, h) (((b) * 2 + (h)) * 16384)
#define B8_SB(b, h) ((4 + (b) * 2 + (h)) * 16384)
#define B8_STA(b, h, P, kt) glds2<true>((P).a + (size_t)(h) * 128 * G_LDA + (kt) * 128, 64 * G_LDA, ra * G_LDA + ca, lw + B8_SA(b, h))
#define B8_STB(b, h, P, kt) glds2(((h) ? (P).b1 : (P).b0) + (size_t)(kt) * 64 * G_LDB, 32 * G_LDB, rb * G_LDB + cb, lw + B8_SB(b, h))
#define B8_LDA(b, h) do { _Pragma("unroll") for (int m = 0; m < 4; ++m) _Pragma("unroll") for (int kk = 0; kk < 2; ++kk) \
      At[m][kk] = ldsf(smem + B8_SA(b, h) + offA(wr * 64 + m * 16 + (lane & 15), kk * 4 + (lane >> 4))); } while (0)
#define B8_LDB(dst, b, h) do { _Pragma("unroll") for (int n = 0; n < 2; ++n) _Pragma("unroll") for (int kk = 0; kk < 2; ++kk) \
      dst[n][kk] = ld_bfrag(smem + B8_SB(b, h), kk, wc * 2 + n, lane); } while (0)
#define B8_MMA(ai, bj, Bt) do { __builtin_amdgcn_s_setprio(1); _Pragma("unroll") for (int m = 0; m < 4; ++m) _Pragma("unroll") for (int n = 0; n < 2; ++n) \
      _Pragma("unroll") for (int kk = 0; kk < 2; ++kk) acc[ai][bj][m][n] = MFMA(Bt[n][kk], At[m][kk], acc[ai][bj][m][n]); __builtin_amdgcn_s_setprio(0); } while (0)
#define WAIT_L(n) asm volatile("s_waitcnt lgkmcnt(%0)" :: "n"(n) : "memory")
#define BAR() __builtin_amdgcn_s_barrier()
#define WAIT_RX(c) asm volatile("s_waitcnt vmcnt(16)\n\ts_cmp_lg_u32 %0, 0\n\ts_cbranch_scc1 .Lrx%=\n\ts_waitcnt vmcnt(8)\n.Lrx%=:" :: "s"(c) : "memory", "scc")
    Acc acc;
    bf16x8 At[4][2], B0[2][2], B1[2][2];
    BigPtr cp = big_ptrs(p, cur);
    BigJob pend; pend.type = J_NONE; pend.panel = 0; pend.idx = 0;
    LBAR();
    B8_STB(0, 0, cp, 0); B8_STB(0, 1, cp, 0); B8_STA(0, 0, cp, 0); B8_STA(0, 1, cp, 0);
    if (wr == 1) BAR();
    WAIT_V(2); BAR();
    B8_STB(1, 0, cp, 1); B8_STA(1, 0, cp, 1); B8_STB(1, 1, cp, 1);
    WAIT_V(6); BAR();
    BigJob nx = pend;
    for (;;) {
      bool chain = false;
      BigPtr np = cp;
      constexpr int nt = DM / 64;
#pragma unroll
      for (int i = 0; i < 32; ++i) acc[i >> 4][(i >> 3) & 1][(i >> 1) & 3][i & 1] = (f32x4){0.f, 0.f, 0.f, 0.f};
#pragma unroll 1
      for (int t = 0; t < nt; t += 2) {
        const bool last = t == nt - 2;
        if (last) {
          nx = claim();
          chain = nx.type == J_GU;
          if (chain) np = big_ptrs(p, nx);
        }
        const BigPtr sp = np;
        const int t2 = last ? 0 : t + 2;
        if (t == 2) big_publish(p, pend);
        const int rx = __builtin_amdgcn_readfirstlane(t == 0 && pend.type != J_NONE);
        B8_LDB(B0, 0, 0); B8_LDB(B1, 0, 1); SCHED(); B8_LDA(0, 0); B8_STA(1, 1, cp, t + 1);
        WAIT_RX(rx);
        WAIT_L(0); BAR(); B8_MMA(0, 0, B0); B8_MMA(0, 1, B1); BAR(); SCHED();
        B8_LDA(0, 1); B8_STB(0, 0, sp, t2); B8_STB(0, 1, sp, t2); B8_STA(0, 0, sp, t2);
        WAIT_RX(rx);
        WAIT_L(0); BAR(); B8_MMA(1, 0, B0); B8_MMA(1, 1, B1); BAR(); SCHED();
        B8_LDB(B0, 1, 0); B8_LDB(B1, 1, 1); SCHED(); B8_LDA(1, 0); B8_STA(0, 1, sp, t2);
        WAIT_V(8); WAIT_L(0); BAR(); B8_MMA(0, 0, B0); B8_MMA(0, 1, B1); BAR(); SCHED();
        B8_LDA(1, 1); B8_STB(1, 0, sp, t2 + 1); B8_STB(1, 1, sp, t2 + 1); B8_STA(1, 0, sp, t2 + 1);
        WAIT_V(8); WAIT_L(0); BAR(); B8_MMA(1, 0, B0); B8_MMA(1, 1, B1); BAR(); SCHED();
      }
      if (wr == 0) BAR();
      JST(nj, 2, __builtin_amdgcn_s_memrealtime());
      big_epilogue(p, cur, acc, smem);
      if (!chain) break;
      JST(nj, 3, __builtin_amdgcn_s_memrealtime()); ++nj;
      pend = cur; cur = nx; cp = np;
      if (wr == 1) BAR();
      JST(nj, 0, cur.type | cur.panel << 8); JST(nj, 1, __builtin_amdgcn_s_memrealtime());
    }
    WAIT_V(0);
    BAR();
    asm volatile(".Lcode_end:");
    big_publish(p, cur);
    JST(nj, 3, __builtin_amdgcn_s_memrealtime()); ++nj;
    cur = nx;
  }
  if (threadIdx.x == 0) { LST(128 + g); }
  if (g < 4 && threadIdx.x == 0) { LST(480 + 4 * g + 2); LSTC(480 + 4 * g + 3); }
}

__device__ __forceinline__ unsigned now_q() { return (unsigned)__builtin_amdgcn_s_memrealtime() * 4u; }
__device__ __forceinline__ void pace_until(unsigned t) {
#pragma unroll 1
  for (int i = 0; i < 256; ++i) {
    const int d = (int)(t - now_q());
    if (d <= 0) break;
    if (d > 32) __builtin_amdgcn_s_sleep(1);
  }
}
#define CLB_E "s76", "s77", "s78", "s79", "s80", "s81", "s82", "s83", "s84", "s85", "s86", "s87", "s88", "s89"
#define EDGE_ASM \
  "s_memtime s[76:77]\n\t" \
  "s_memrealtime s[78:79]\n\t" \
  "s_nop 3\n\t" \
  "s_memrealtime s[80:81]\n\t" \
  "s_nop 3\n\t" \
  "s_memrealtime s[82:83]\n\t" \
  "s_nop 3\n\t" \
  "s_memrealtime s[84:85]\n\t" \
  "s_nop 3\n\t" \
  "s_memrealtime s[86:87]\n\t" \
  "s_nop 3\n\t" \
  "s_memrealtime s[88:89]\n\t" \
  "s_mov_b32 s93, 0\n\t" \
  "s_waitcnt lgkmcnt(0)\n\t" \
  "s_cmp_eq_u32 s80, s78\n\t" \
  "s_addc_u32 s93, s93, 0\n\t" \
  "s_cmp_eq_u32 s82, s78\n\t" \
  "s_addc_u32 s93, s93, 0\n\t" \
  "s_cmp_eq_u32 s84, s78\n\t" \
  "s_addc_u32 s93, s93, 0\n\t" \
  "s_cmp_eq_u32 s86, s78\n\t" \
  "s_addc_u32 s93, s93, 0\n\t" \
  "s_cmp_eq_u32 s88, s78\n\t" \
  "s_addc_u32 s93, s93, 0\n\t" \
  "s_mul_i32 s93, s93, 21\n\t" \
  "s_add_u32 s76, s76, 15\n\t" \
  "s_add_u32 %[kc], s76, s93\n\t" \
  "s_lshl2_add_u32 %[kq], s78, 16\n\t"
__device__ __forceinline__ unsigned time_of(unsigned cyc) {
  unsigned c, q;
  asm volatile(EDGE_ASM : [kc] "=&s"(c), [kq] "=&s"(q) :: CLB_E, "s93", "scc");
  return q - (((c - cyc) * 11900u) >> 16);
}
#define DS_RED 20
#define DS_MEM 13
#define LEAD_DN 2
#define LEAD_UP 6
__device__ __forceinline__ u32x2 ld2_sc1(__amdgpu_buffer_rsrc_t rs, int off, int soff = 0) {
  return __builtin_amdgcn_raw_buffer_load_b64(rs, off, soff, 16);
}
__device__ __forceinline__ void st2_sc1(__amdgpu_buffer_rsrc_t rs, int off, u32x2 v) {
  __builtin_amdgcn_raw_buffer_store_b64(v, rs, off, 0, 16);
}
__device__ __forceinline__ int row_max(int v) {
  v = max(v, __builtin_amdgcn_update_dpp(0, v, 0x128, 0xf, 0xf, false));
  v = max(v, __builtin_amdgcn_update_dpp(0, v, 0x124, 0xf, 0xf, false));
  v = max(v, __builtin_amdgcn_update_dpp(0, v, 0x122, 0xf, 0xf, false));
  v = max(v, __builtin_amdgcn_update_dpp(0, v, 0x121, 0xf, 0xf, false));
  return v;
}
__device__ __forceinline__ int wave_max(int v) {
  v = row_max(v);
  return max(max(__builtin_amdgcn_readlane(v, 0), __builtin_amdgcn_readlane(v, 16)),
             max(__builtin_amdgcn_readlane(v, 32), __builtin_amdgcn_readlane(v, 48)));
}
__device__ __forceinline__ int track(int est, int x, int up, int dn, int out) {
  const int d = x - est;
  return est + (d > out ? 4 : max(min(d, up), -dn));
}
#define WSL 12
#define PLAN_SLEEP 2
__device__ __forceinline__ unsigned latest(unsigned t) {
  const unsigned t0 = __builtin_amdgcn_readfirstlane(t);
  return t0 + wave_max((int)(t - t0));
}
#define REF_W 3
#define REF_G 60
#define RL0 315
__device__ __forceinline__ void watch(__amdgpu_buffer_rsrc_t rsy, int s, int lane, unsigned t0, const unsigned* tgt_, gu32* abortw, int hs) {
  pace_until(t0);
  const int rl = (int)__builtin_amdgcn_raw_buffer_load_b32(rsy, lane * 128, SY_BR + hs, 16);
  u32x2 ra;
  for (unsigned spins = 0;;) {
    asm volatile("" ::: "memory");
    ra = ld2_sc1(rsy, (lane & 31) * 128, SY_BA + hs);
    const bool all = __all((int)ra[0] >= s);
    SMP(4, s, spins);
    if (all) break;
    if (spins < 3) __builtin_amdgcn_s_sleep(PLAN_SLEEP); else __builtin_amdgcn_s_sleep(WSL);
    if (spin_fail(spins, abortw)) break;
  }
  int d = ra[0] == (unsigned)s ? (int)(ra[1] - t0) : 0;
  d = max(d, __builtin_amdgcn_update_dpp(0, d, 0xB1, 0xf, 0xf, false));
  d = max(d, __builtin_amdgcn_update_dpp(0, d, 0x4E, 0xf, 0xf, false));
  int sum = d + __builtin_amdgcn_update_dpp(0, d, 0x128, 0xf, 0xf, false);
  sum += __builtin_amdgcn_update_dpp(0, sum, 0x124, 0xf, 0xf, false);
  const int mx = wave_max(d), mean = (__builtin_amdgcn_readlane(sum, 0) + __builtin_amdgcn_readlane(sum, 16)) >> 3;
  const unsigned t = t0 + mx - min(((mx - mean) * REF_W) >> 2, REF_G) + wave_max(rl < 0 ? RL0 : rl) + (DS_RED + DS_MEM);
  lds_vu_ptr tgt = lds_at(tgt_);
  if (lane < 2) tgt[lane] = lane ? t : s + 1;
  SMP(0, s, (int)(t - t0)); SMP(1, s, (int)(t - now_q()));
}
__device__ __forceinline__ unsigned ld_flag(__amdgpu_buffer_rsrc_t rs, int off) {
  return __builtin_amdgcn_raw_buffer_load_b32(rs, off, 0, 16);
}

constexpr unsigned long long QK_MAP[8] = {0xbaf9876d543210ecull, 0xa98f76d543210bceull, 0x98f76d543210ecbaull, 0x876fd543210bcea9ull, 0x76fd543210ecba98ull, 0x65df43210bcea987ull, 0x5df43210ecba9876ull, 0xd43f210bcea98765ull};
#define QK_ASM_0 \
  "s_waitcnt lgkmcnt(0)\n\t" \
  "v_subrev_u32 v244, 0x8000, v250\n\t" \
  "ds_read_b128 v[216:219], v248 offset:32768\n\t" \
  "ds_read_b128 v[220:223], v248 offset:33792\n\t" \
  "ds_read_b128 v[224:227], v248 offset:34816\n\t" \
  "ds_read_b128 v[228:231], v248 offset:35840\n\t" \
  "ds_read_b128 v[208:211], v244 offset:0\n\t" \
  "ds_read_b128 v[212:215], v244 offset:1024\n\t" \
  "ds_read_b128 v[236:239], v244 offset:2048\n\t" \
  "ds_read_b128 v[240:243], v244 offset:3072\n\t" \
  "v_mov_b64 v[192:193], 0\n\t" \
  "v_mov_b64 v[194:195], 0\n\t" \
  "v_mov_b64 v[196:197], 0\n\t" \
  "v_mov_b64 v[198:199], 0\n\t" \
  "s_movk_i32 s92, 0x2000\n\t" \
  ".Lwt%=:\n\t" \
  "ds_read_b64 v[200:201], v248 offset:30720\n\t" \
  "s_memrealtime s[90:91]\n\t" \
  "s_sub_u32 s92, s92, 1\n\t" \
  "s_waitcnt lgkmcnt(0)\n\t" \
  "v_readfirstlane_b32 s93, v200\n\t" \
  "v_readfirstlane_b32 %[tgt], v201\n\t" \
  "s_cbranch_scc1 .Lgo%=\n\t" \
  "s_cmp_eq_u32 s93, %[step]\n\t" \
  "s_cbranch_scc1 .Lwx%=\n\t" \
  "s_sleep 1\n\t" \
  "s_branch .Lwt%=\n\t" \
  ".Lwx%=:\n\t" \
  "s_lshl_b32 s93, s90, 2\n\t" \
  "s_sub_i32 s93, %[tgt], s93\n\t" \
  "s_cmp_le_i32 s93, 0\n\t" \
  "s_cbranch_scc1 .Lgo%=\n\t" \
  "s_min_i32 s93, s93, 8000\n\t" \
  "s_mov_b32 s94, s93\n\t" \
  ".Lsl%=:\n\t" \
  "s_sub_i32 s94, s94, 18\n\t" \
  "s_cmp_lt_i32 s94, 30\n\t" \
  "s_cbranch_scc1 .Lsx%=\n\t" \
  "s_sleep 1\n\t" \
  "s_branch .Lsl%=\n\t" \
  ".Lsx%=:\n\t" \
  "s_sub_i32 s93, s93, 12\n\t" \
  "s_max_i32 s93, s93, 4\n\t" \
  "s_lshr_b32 s93, s93, 2\n\t" \
  "s_add_u32 s93, s93, s90\n\t" \
  "s_memrealtime s[90:91]\n\t" \
  ".Lpace%=:\n\t" \
  "s_memrealtime s[94:95]\n\t" \
  "s_waitcnt lgkmcnt(1)\n\t" \
  "s_sub_i32 s92, s90, s93\n\t" \
  "s_cmp_ge_i32 s92, 0\n\t" \
  "s_cbranch_scc1 .Le%=\n\t" \
  "s_memrealtime s[90:91]\n\t" \
  "s_waitcnt lgkmcnt(1)\n\t" \
  "s_sub_i32 s92, s94, s93\n\t" \
  "s_cmp_lt_i32 s92, 0\n\t" \
  "s_cbranch_scc1 .Lpace%=\n\t" \
  "s_nop 4\n\t" \
  ".Le%=:\n\t" \
  "s_add_u32 s92, s92, s93\n\t" \
  "s_lshl_b32 s92, s92, 2\n\t" \
  "s_sub_i32 s92, %[tgt], s92\n\t" \
  "s_max_i32 s92, s92, 0\n\t" \
  "s_bitcmp0_b32 s92, 3\n\t" \
  "s_cbranch_scc1 .Lf3%=\n\t" \
  "s_nop 15\n\t" \
  ".Lf3%=:\n\t" \
  "s_bitcmp0_b32 s92, 2\n\t" \
  "s_cbranch_scc1 .Lf2%=\n\t" \
  "s_nop 10\n\t" \
  ".Lf2%=:\n\t" \
  ".Lgo%=:\n\t" \
  "s_mov_b32 s92, 0\n\t" \
  ".Lpoll%=:\n\t" \
  "buffer_load_dwordx4 v[200:203], v248, %[rs], %[som] offen sc1\n\t" \
  "buffer_load_dwordx4 v[204:207], v248, %[rs], %[som2] offen sc1\n\t" \
  "s_waitcnt vmcnt(1) lgkmcnt(0)\n\t" \
  "ds_write_b128 v249, v[200:203]\n\t" \
  "v_smfmac_f32_16x16x64_bf16 v[192:195], v[200:203], v[208:215], v251\n\t" \
  "v_max3_u32 v244, v200, v201, v202\n\t" \
  "v_smfmac_f32_16x16x64_bf16 v[196:199], v[200:203], v[236:243], v251\n\t" \
  "s_waitcnt vmcnt(0)\n\t" \
  "ds_write_b128 v249, v[204:207] offset:8192\n\t" \
  "v_smfmac_f32_16x16x64_bf16 v[192:195], v[204:207], v[216:223], v251\n\t" \
  "v_max3_u32 v244, v244, v203, v204\n\t" \
  "v_max3_u32 v244, v244, v205, v206\n\t" \
  "v_smfmac_f32_16x16x64_bf16 v[196:199], v[204:207], v[224:231], v251\n\t" \
  "v_max_u32 v244, v244, v207\n\t" \
  "v_cmp_le_u32 vcc, %[sent], v244\n\t" \
  "s_cbranch_vccnz .Lslow%=\n\t" \
  ".Lok%=:\n\t" \
  "ds_read_b128 v[216:219], v250 offset:32768\n\t" \
  "ds_read_b128 v[220:223], v250 offset:33792\n\t" \
  "ds_read_b128 v[224:227], v250 offset:34816\n\t" \
  "ds_read_b128 v[228:231], v250 offset:35840\n\t" \
  "s_waitcnt lgkmcnt(4)\n\t" \
  "s_barrier\n\t" \
  "ds_read_b128 v[208:211], v248 offset:1024\n\t" \
  "ds_read_b128 v[212:215], v248 offset:2048\n\t" \
  "ds_read_b128 v[236:239], v248 offset:3072\n\t" \
  "ds_read_b128 v[240:243], v248 offset:4096\n\t" \
  "s_waitcnt lgkmcnt(3)\n\t" \
  "ds_read_b128 v[244:247], v248 offset:13312\n\t" \
  "v_smfmac_f32_16x16x64_bf16 v[192:195], v[208:211], v[216:223], v251\n\t" \
  "ds_read_b128 v[216:219], v250 offset:0\n\t" \
  "v_smfmac_f32_16x16x64_bf16 v[196:199], v[208:211], v[224:231], v251\n\t" \
  "s_waitcnt lgkmcnt(4)\n\t" \
  "ds_read_b128 v[220:223], v250 offset:1024\n\t" \
  "v_smfmac_f32_16x16x64_bf16 v[192:195], v[212:215], v[0:7], v251\n\t" \
  "ds_read_b128 v[224:227], v250 offset:2048\n\t" \
  "v_smfmac_f32_16x16x64_bf16 v[196:199], v[212:215], v[8:15], v251\n\t" \
  "s_waitcnt lgkmcnt(5)\n\t" \
  "ds_read_b128 v[228:231], v250 offset:3072\n\t" \
  "v_smfmac_f32_16x16x64_bf16 v[192:195], v[236:239], v[16:23], v251\n\t" \
  "ds_read_b128 v[200:203], v248 offset:5120\n\t" \
  "v_smfmac_f32_16x16x64_bf16 v[196:199], v[236:239], v[24:31], v251\n\t" \
  "s_waitcnt lgkmcnt(6)\n\t" \
  "ds_read_b128 v[204:207], v248 offset:6144\n\t" \
  "v_smfmac_f32_16x16x64_bf16 v[192:195], v[240:243], v[32:39], v251\n\t" \
  "ds_read_b128 v[208:211], v248 offset:7168\n\t" \
  "v_smfmac_f32_16x16x64_bf16 v[196:199], v[240:243], v[40:47], v251\n\t" \
  "s_waitcnt lgkmcnt(5)\n\t" \
  "ds_read_b128 v[212:215], v248 offset:9216\n\t" \
  "v_smfmac_f32_16x16x64_bf16 v[192:195], v[244:247], v[216:223], v251\n\t" \
  "s_waitcnt lgkmcnt(4)\n\t" \
  "v_smfmac_f32_16x16x64_bf16 v[196:199], v[244:247], v[224:231], v251\n\t" \
  "s_waitcnt lgkmcnt(3)\n\t" \
  "ds_read_b128 v[236:239], v248 offset:10240\n\t" \
  "v_smfmac_f32_16x16x64_bf16 v[192:195], v[200:203], v[48:55], v251\n\t" \
  "ds_read_b128 v[240:243], v248 offset:11264\n\t" \
  "v_smfmac_f32_16x16x64_bf16 v[196:199], v[200:203], v[56:63], v251\n\t" \
  "s_waitcnt lgkmcnt(4)\n\t" \
  "v_smfmac_f32_16x16x64_bf16 v[192:195], v[204:207], v[64:71], v251\n\t" \
  "ds_read_b128 v[244:247], v248 offset:12288\n\t" \
  "v_smfmac_f32_16x16x64_bf16 v[196:199], v[204:207], v[72:79], v251\n\t" \
  "s_waitcnt lgkmcnt(4)\n\t" \
  "v_smfmac_f32_16x16x64_bf16 v[192:195], v[208:211], v[80:87], v251\n\t" \
  "ds_read_b128 v[200:203], v248 offset:14336\n\t" \
  "v_smfmac_f32_16x16x64_bf16 v[196:199], v[208:211], v[88:95], v251\n\t" \
  "s_waitcnt lgkmcnt(4)\n\t" \
  "v_smfmac_f32_16x16x64_bf16 v[192:195], v[212:215], v[96:103], v251\n\t" \
  "ds_read_b128 v[204:207], v248 offset:15360\n\t" \
  "v_smfmac_f32_16x16x64_bf16 v[196:199], v[212:215], v[104:111], v251\n\t" \
  "s_waitcnt lgkmcnt(3)\n\t" \
  "v_smfmac_f32_16x16x64_bf16 v[192:195], v[236:239], v[112:119], v251\n\t" \
  "v_smfmac_f32_16x16x64_bf16 v[196:199], v[236:239], v[120:127], v251\n\t" \
  "v_smfmac_f32_16x16x64_bf16 v[192:195], v[240:243], v[128:135], v251\n\t" \
  "v_smfmac_f32_16x16x64_bf16 v[196:199], v[240:243], v[136:143], v251\n\t" \
  "s_waitcnt lgkmcnt(1)\n\t" \
  "v_smfmac_f32_16x16x64_bf16 v[192:195], v[244:247], v[144:151], v251\n\t" \
  "v_smfmac_f32_16x16x64_bf16 v[196:199], v[244:247], v[152:159], v251\n\t" \
  "v_smfmac_f32_16x16x64_bf16 v[192:195], v[200:203], v[160:167], v251\n\t" \
  "v_smfmac_f32_16x16x64_bf16 v[196:199], v[200:203], v[168:175], v251\n\t" \
  "s_waitcnt lgkmcnt(0)\n\t" \
  "v_smfmac_f32_16x16x64_bf16 v[192:195], v[204:207], v[176:183], v251\n\t" \
  "v_smfmac_f32_16x16x64_bf16 v[196:199], v[204:207], v[184:191], v251\n\t" \
  "ds_read_b128 v[200:203], v248 offset:16384\n\t" \
  "ds_read_b128 v[204:207], v248 offset:20480\n\t" \
  "v_mov_b32 v250, v232\n\t" \
  "v_mov_b32 v249, v233\n\t" \
  "s_nop 2\n\t" \
  "v_add_f32 v192, v192, v194\n\t" \
  "v_add_f32 v193, v193, v195\n\t" \
  "v_add_f32 v196, v196, v198\n\t" \
  "v_add_f32 v197, v197, v199\n\t" \
  "v_cvt_pk_bf16_f32 v192, v192, v193\n\t" \
  "v_cvt_pk_bf16_f32 v196, v196, v197\n\t" \
  "ds_write2_b32 v252, v192, v196 offset1:64\n\t" \
  "v_mov_b32 v251, v234\n\t" \
  "ds_read_b64_tr_b16 v[232:233], v250 offset:0\n\t" \
  "ds_read_b64_tr_b16 v[234:235], v250 offset:64\n\t" \
  "v_mov_b32 v212, %[step]\n\t" \
  "s_mov_b64 exec, 1\n\t" \
  "ds_write_b32 v248, v212 offset:28672\n\t" \
  "s_mov_b64 exec, -1\n\t" \
  "buffer_load_dwordx4 v[192:195], v248, %[rt], %[svt] offen offset:0 sc1\n\t" \
  "buffer_load_dwordx4 v[196:199], v248, %[rt], %[svt] offen offset:1024 sc1\n\t" \
  "s_waitcnt lgkmcnt(1)\n\t" \
  "v_mfma_f32_16x16x32_bf16 v[200:203], v[200:203], v[232:235], 0\n\t" \
  "v_mfma_f32_16x16x32_bf16 v[204:207], v[204:207], v[232:235], 0\n\t" \
  "s_nop 7\n\t" \
  "v_cndmask_b32 v216, v200, v204, %[nh]\n\t" \
  "v_cndmask_b32 v217, v201, v205, %[nh]\n\t" \
  "v_cndmask_b32 v218, v202, v206, %[nh]\n\t" \
  "v_cndmask_b32 v219, v203, v207, %[nh]\n\t" \
  "ds_write_b128 v248, v[216:219] offset:16384\n\t" \
  "buffer_load_dword v252, v249, %[rs], %[sv] offen offset:0 sc1\n\t" \
  "buffer_load_dword v253, v249, %[rs], %[sv] offen offset:256 sc1\n\t" \
  "ds_read_b32 v212, v248 offset:28672\n\t" \
  "ds_read_b64_tr_b16 v[236:237], v250 offset:512\n\t" \
  "ds_read_b64_tr_b16 v[238:239], v250 offset:576\n\t" \
  "ds_read_b64_tr_b16 v[240:241], v250 offset:1024\n\t" \
  "ds_read_b64_tr_b16 v[242:243], v250 offset:1088\n\t" \
  "ds_read_b64_tr_b16 v[244:245], v250 offset:1536\n\t" \
  "ds_read_b64_tr_b16 v[246:247], v250 offset:1600\n\t" \
  "s_setprio 3\n\t" \
  "s_waitcnt lgkmcnt(0)\n\t" \
  "v_cmp_ne_u32 vcc, %[step], v212\n\t" \
  "s_barrier\n\t" \
  "s_and_b32 s93, vcc_lo, 0xf\n\t" \
  "s_cbranch_scc1 .Lre%=\n\t" \
  ".Lrb%=:\n\t" \
  "ds_read_b64_tr_b16 v[200:201], v250 offset:2048\n\t" \
  "ds_read_b64_tr_b16 v[202:203], v250 offset:2112\n\t" \
  "ds_read_b64_tr_b16 v[204:205], v250 offset:2560\n\t" \
  "ds_read_b64_tr_b16 v[206:207], v250 offset:2624\n\t" \
  "ds_read_b128 v[220:223], v248 offset:17408\n\t" \
  "ds_read_b128 v[224:227], v248 offset:18432\n\t" \
  "ds_read_b128 v[228:231], v248 offset:19456\n\t" \
  "ds_read_b64_tr_b16 v[208:209], v250 offset:3072\n\t" \
  "ds_read_b64_tr_b16 v[210:211], v250 offset:3136\n\t" \
  "ds_read_b64_tr_b16 v[212:213], v250 offset:3584\n\t" \
  "ds_read_b64_tr_b16 v[214:215], v250 offset:3648\n\t" \
  "s_waitcnt lgkmcnt(9)\n\t" \
  "v_mfma_f32_16x16x32_bf16 v[200:203], v[200:203], v[232:235], 0\n\t" \
  "s_waitcnt lgkmcnt(7)\n\t" \
  "v_mfma_f32_16x16x32_bf16 v[200:203], v[204:207], v[236:239], v[200:203]\n\t" \
  "s_waitcnt lgkmcnt(6)\n\t" \
  "v_pk_add_f32 v[216:217], v[216:217], v[220:221]\n\t" \
  "v_pk_add_f32 v[218:219], v[218:219], v[222:223]\n\t" \
  "s_waitcnt lgkmcnt(4)\n\t" \
  "v_pk_add_f32 v[224:225], v[224:225], v[228:229]\n\t" \
  "v_pk_add_f32 v[226:227], v[226:227], v[230:231]\n\t" \
  "v_pk_add_f32 v[216:217], v[216:217], v[224:225]\n\t" \
  "v_pk_add_f32 v[218:219], v[218:219], v[226:227]\n\t" \
  "s_waitcnt lgkmcnt(2)\n\t" \
  "v_mfma_f32_16x16x32_bf16 v[200:203], v[208:211], v[240:243], v[200:203]\n\t" \
  "v_max3_f32 v224, v216, v217, v218\n\t" \
  "v_max_f32 v224, v224, v219\n\t" \
  "s_waitcnt lgkmcnt(0)\n\t" \
  "v_mfma_f32_16x16x32_bf16 v[200:203], v[212:215], v[244:247], v[200:203]\n\t" \
  "v_max_f32_dpp v225, v224, v224 row_ror:8 row_mask:0xf bank_mask:0xf\n\t" \
  "v_max_f32_dpp v226, v224, v224 row_ror:8 row_mask:0xf bank_mask:0xf\n\t" \
  "s_nop 1\n\t" \
  "v_permlane32_swap_b32 v225, v226\n\t" \
  "v_max_f32 v231, v225, v226\n\t" \
  "v_mov_b32 v220, 0x3f803f80\n\t" \
  "v_mov_b32 v221, 0x3f803f80\n\t" \
  "v_mov_b64 v[222:223], v[220:221]\n\t" \
  "v_mov_b32 v233, 0\n\t" \
  "v_mov_b32 v234, 0\n\t" \
  "v_max3_f32 v204, v200, v201, v231\n\t" \
  "v_max3_f32 v205, v204, v202, v203\n\t" \
  "v_max3_f32 v206, v204, v202, v203\n\t" \
  "v_cndmask_b32 v200, v200, v202, %[up]\n\t" \
  "v_cndmask_b32 v201, v201, v203, %[up]\n\t" \
  "v_permlane16_swap_b32 v205, v206\n\t" \
  "v_max_f32 v204, v205, v206\n\t" \
  "v_mul_f32 v204, %[negc], v204\n\t" \
  "v_fma_f32 v200, v200, %[c], v204\n\t" \
  "v_fma_f32 v201, v201, %[c], v204\n\t" \
  "v_exp_f32 v200, v200\n\t" \
  "v_fma_f32 v216, v216, %[c], v204\n\t" \
  "v_exp_f32 v201, v201\n\t" \
  "v_fma_f32 v217, v217, %[c], v204\n\t" \
  "v_exp_f32 v216, v216\n\t" \
  "v_fma_f32 v218, v218, %[c], v204\n\t" \
  "v_cvt_pk_bf16_f32 v232, v200, v201\n\t" \
  "v_cvt_pk_bf16_f32 v235, v200, v201\n\t" \
  "v_exp_f32 v217, v217\n\t" \
  "v_fma_f32 v219, v219, %[c], v204\n\t" \
  "v_mfma_f32_16x16x16_bf16 v[212:215], v[220:221], v[232:233], 0\n\t" \
  "v_exp_f32 v218, v218\n\t" \
  "s_waitcnt vmcnt(0)\n\t" \
  "v_mfma_f32_16x16x16_bf16 v[224:227], v[252:253], v[232:233], 0\n\t" \
  "v_cvt_pk_bf16_f32 v210, v216, v217\n\t" \
  "v_cvt_pk_bf16_f32 v208, v216, v217\n\t" \
  "v_exp_f32 v219, v219\n\t" \
  "v_mfma_f32_16x16x16_bf16 v[228:231], v[252:253], v[234:235], 0\n\t" \
  "v_cvt_pk_bf16_f32 v211, v218, v219\n\t" \
  "v_cvt_pk_bf16_f32 v209, v218, v219\n\t" \
  "v_mov_b32_dpp v208, v210 row_ror:8 row_mask:0xf bank_mask:0xc\n\t" \
  "v_mov_b32_dpp v210, v210 row_ror:8 row_mask:0xf bank_mask:0x3\n\t" \
  "v_mov_b32_dpp v209, v211 row_ror:8 row_mask:0xf bank_mask:0xc\n\t" \
  "v_mov_b32_dpp v211, v211 row_ror:8 row_mask:0xf bank_mask:0x3\n\t" \
  "v_max_u32 v204, v252, v253\n\t" \
  "v_cmp_le_u32 vcc, %[sent], v204\n\t" \
  "v_mfma_f32_16x16x32_bf16 v[212:215], v[220:223], v[208:211], v[212:215]\n\t" \
  ".Lpv%=:\n\t" \
  "v_mfma_f32_16x16x32_bf16 v[224:227], v[192:195], v[208:211], v[224:227]\n\t" \
  "v_mfma_f32_16x16x32_bf16 v[228:231], v[196:199], v[208:211], v[228:231]\n\t" \
  "s_cbranch_vccnz .Lvs%=\n\t" \
  ".Lvok%=:\n\t" \
  "s_nop 4\n\t" \
  "v_rcp_f32 v204, v212\n\t" \
  "v_cndmask_b32 v224, v224, v226, %[nh]\n\t" \
  "v_cndmask_b32 v225, v225, v227, %[nh]\n\t" \
  "v_cndmask_b32 v228, v228, v230, %[nh]\n\t" \
  "v_cndmask_b32 v229, v229, v231, %[nh]\n\t" \
  "v_pk_mul_f32 v[224:225], v[224:225], v[204:205] op_sel_hi:[1,0]\n\t" \
  "v_pk_mul_f32 v[228:229], v[228:229], v[204:205] op_sel_hi:[1,0]\n\t" \
  "v_cvt_pk_bf16_f32 v224, v224, v225\n\t" \
  "v_cvt_pk_bf16_f32 v225, v228, v229\n\t" \
  "buffer_store_dwordx2 v[224:225], v251, %[rs], %[so] offen sc1\n\t" \
  "s_memtime s[90:91]\n\t" \
  "s_setprio 0\n\t" \
  "s_waitcnt lgkmcnt(0)\n\t" \
  "s_mov_b32 %[tout], s90\n\t" \
  "s_mov_b32 %[spins], s92\n\t" \
  "s_branch .Lend%=\n\t" \
  ".Lre%=:\n\t" \
  "ds_read_b64_tr_b16 v[236:237], v250 offset:512\n\t" \
  "ds_read_b64_tr_b16 v[238:239], v250 offset:576\n\t" \
  "ds_read_b64_tr_b16 v[240:241], v250 offset:1024\n\t" \
  "ds_read_b64_tr_b16 v[242:243], v250 offset:1088\n\t" \
  "ds_read_b64_tr_b16 v[244:245], v250 offset:1536\n\t" \
  "ds_read_b64_tr_b16 v[246:247], v250 offset:1600\n\t" \
  "s_waitcnt lgkmcnt(0)\n\t" \
  "s_branch .Lrb%=\n\t" \
  ".Lvs%=:\n\t" \
  "v_cmp_eq_u32 vcc, %[sent], v252\n\t" \
  "v_cmp_eq_u32 s[94:95], %[sent], v253\n\t" \
  "s_or_b64 s[94:95], s[94:95], vcc\n\t" \
  "s_cbranch_scc0 .Lvok%=\n\t" \
  "s_add_u32 s92, s92, 1\n\t" \
  "s_sleep 2\n\t" \
  "s_and_b32 s93, s92, 63\n\t" \
  "s_cbranch_scc1 .Lvr%=\n\t" \
  "buffer_load_dword v204, off, %[rs], %[sab] sc1\n\t" \
  "s_waitcnt vmcnt(0)\n\t" \
  "v_readfirstlane_b32 s93, v204\n\t" \
  "s_nop 3\n\t" \
  "s_cmp_eq_u32 s93, 1\n\t" \
  "s_cbranch_scc1 .Lvok%=\n\t" \
  "s_cmp_lt_u32 s92, 0x80000\n\t" \
  "s_cbranch_scc1 .Lvr%=\n\t" \
  "v_mov_b32 v204, 1\n\t" \
  "buffer_store_dword v204, off, %[rs], %[sab] sc1\n\t" \
  "s_branch .Lvok%=\n\t" \
  ".Lvr%=:\n\t" \
  "buffer_load_dword v252, v249, %[rs], %[sv] offen offset:0 sc1\n\t" \
  "buffer_load_dword v253, v249, %[rs], %[sv] offen offset:256 sc1\n\t" \
  "s_waitcnt vmcnt(0)\n\t" \
  "v_mfma_f32_16x16x16_bf16 v[224:227], v[252:253], v[232:233], 0\n\t" \
  "v_mfma_f32_16x16x16_bf16 v[228:231], v[252:253], v[234:235], 0\n\t" \
  "v_max_u32 v204, v252, v253\n\t" \
  "v_cmp_le_u32 vcc, %[sent], v204\n\t" \
  "s_nop 4\n\t" \
  "s_branch .Lpv%=\n\t" \
  "s_branch .Lend%=\n\t" \
  ".Lslow%=:\n\t" \
  "s_mov_b64 s[94:95], 0\n\t" \
  "v_cmp_eq_u32 vcc, %[sent], v200\n\t" \
  "s_or_b64 s[94:95], s[94:95], vcc\n\t" \
  "v_cmp_eq_u32 vcc, %[sent], v201\n\t" \
  "s_or_b64 s[94:95], s[94:95], vcc\n\t" \
  "v_cmp_eq_u32 vcc, %[sent], v202\n\t" \
  "s_or_b64 s[94:95], s[94:95], vcc\n\t" \
  "v_cmp_eq_u32 vcc, %[sent], v203\n\t" \
  "s_or_b64 s[94:95], s[94:95], vcc\n\t" \
  "v_cmp_eq_u32 vcc, %[sent], v204\n\t" \
  "s_or_b64 s[94:95], s[94:95], vcc\n\t" \
  "v_cmp_eq_u32 vcc, %[sent], v205\n\t" \
  "s_or_b64 s[94:95], s[94:95], vcc\n\t" \
  "v_cmp_eq_u32 vcc, %[sent], v206\n\t" \
  "s_or_b64 s[94:95], s[94:95], vcc\n\t" \
  "v_cmp_eq_u32 vcc, %[sent], v207\n\t" \
  "s_or_b64 s[94:95], s[94:95], vcc\n\t" \
  "s_cmp_eq_u64 s[94:95], 0\n\t" \
  "s_cbranch_scc1 .Lok%=\n\t" \
  "v_mov_b64 v[192:193], 0\n\t" \
  "v_mov_b64 v[194:195], 0\n\t" \
  "v_mov_b64 v[196:197], 0\n\t" \
  "v_mov_b64 v[198:199], 0\n\t" \
  "s_add_u32 s92, s92, 1\n\t" \
  "s_cmp_eq_u32 s92, 1\n\t" \
  "s_cbranch_scc1 .Lpoll%=\n\t" \
  "s_sleep 2\n\t" \
  "s_and_b32 s93, s92, 63\n\t" \
  "s_cmp_lg_u32 s93, 0\n\t" \
  "s_cbranch_scc1 .Lpoll%=\n\t" \
  "v_mov_b32 v245, 0\n\t" \
  "buffer_load_dword v244, v245, %[rs], %[sab] offen sc1\n\t" \
  "s_waitcnt vmcnt(0)\n\t" \
  "v_readfirstlane_b32 s93, v244\n\t" \
  "s_nop 3\n\t" \
  "s_cmp_eq_u32 s93, 1\n\t" \
  "s_cbranch_scc1 .Lok%=\n\t" \
  "s_cmp_lt_u32 s92, 0x80000\n\t" \
  "s_cbranch_scc1 .Lpoll%=\n\t" \
  "v_mov_b32 v244, 1\n\t" \
  "buffer_store_dword v244, v245, %[rs], %[sab] offen sc1\n\t" \
  "s_branch .Lok%=\n\t" \
  ".Lend%=:\n\t"
#define QK_ASM_1 \
  "s_waitcnt lgkmcnt(0)\n\t" \
  "v_subrev_u32 v244, 0x8000, v250\n\t" \
  "ds_read_b128 v[216:219], v248 offset:36864\n\t" \
  "ds_read_b128 v[220:223], v248 offset:37888\n\t" \
  "ds_read_b128 v[224:227], v248 offset:38912\n\t" \
  "ds_read_b128 v[228:231], v248 offset:39936\n\t" \
  "ds_read_b128 v[208:211], v244 offset:0\n\t" \
  "ds_read_b128 v[212:215], v244 offset:1024\n\t" \
  "ds_read_b128 v[236:239], v244 offset:2048\n\t" \
  "ds_read_b128 v[240:243], v244 offset:3072\n\t" \
  "v_mov_b64 v[192:193], 0\n\t" \
  "v_mov_b64 v[194:195], 0\n\t" \
  "v_mov_b64 v[196:197], 0\n\t" \
  "v_mov_b64 v[198:199], 0\n\t" \
  "s_movk_i32 s92, 0x2000\n\t" \
  ".Lwt%=:\n\t" \
  "ds_read_b64 v[200:201], v248 offset:30720\n\t" \
  "s_memrealtime s[90:91]\n\t" \
  "s_sub_u32 s92, s92, 1\n\t" \
  "s_waitcnt lgkmcnt(0)\n\t" \
  "v_readfirstlane_b32 s93, v200\n\t" \
  "v_readfirstlane_b32 %[tgt], v201\n\t" \
  "s_cbranch_scc1 .Lgo%=\n\t" \
  "s_cmp_eq_u32 s93, %[step]\n\t" \
  "s_cbranch_scc1 .Lwx%=\n\t" \
  "s_sleep 1\n\t" \
  "s_branch .Lwt%=\n\t" \
  ".Lwx%=:\n\t" \
  "s_lshl_b32 s93, s90, 2\n\t" \
  "s_sub_i32 s93, %[tgt], s93\n\t" \
  "s_cmp_le_i32 s93, 0\n\t" \
  "s_cbranch_scc1 .Lgo%=\n\t" \
  "s_min_i32 s93, s93, 8000\n\t" \
  "s_mov_b32 s94, s93\n\t" \
  ".Lsl%=:\n\t" \
  "s_sub_i32 s94, s94, 18\n\t" \
  "s_cmp_lt_i32 s94, 30\n\t" \
  "s_cbranch_scc1 .Lsx%=\n\t" \
  "s_sleep 1\n\t" \
  "s_branch .Lsl%=\n\t" \
  ".Lsx%=:\n\t" \
  "s_sub_i32 s93, s93, 12\n\t" \
  "s_max_i32 s93, s93, 4\n\t" \
  "s_lshr_b32 s93, s93, 2\n\t" \
  "s_add_u32 s93, s93, s90\n\t" \
  "s_memrealtime s[90:91]\n\t" \
  ".Lpace%=:\n\t" \
  "s_memrealtime s[94:95]\n\t" \
  "s_waitcnt lgkmcnt(1)\n\t" \
  "s_sub_i32 s92, s90, s93\n\t" \
  "s_cmp_ge_i32 s92, 0\n\t" \
  "s_cbranch_scc1 .Le%=\n\t" \
  "s_memrealtime s[90:91]\n\t" \
  "s_waitcnt lgkmcnt(1)\n\t" \
  "s_sub_i32 s92, s94, s93\n\t" \
  "s_cmp_lt_i32 s92, 0\n\t" \
  "s_cbranch_scc1 .Lpace%=\n\t" \
  "s_nop 4\n\t" \
  ".Le%=:\n\t" \
  "s_add_u32 s92, s92, s93\n\t" \
  "s_lshl_b32 s92, s92, 2\n\t" \
  "s_sub_i32 s92, %[tgt], s92\n\t" \
  "s_max_i32 s92, s92, 0\n\t" \
  "s_bitcmp0_b32 s92, 3\n\t" \
  "s_cbranch_scc1 .Lf3%=\n\t" \
  "s_nop 15\n\t" \
  ".Lf3%=:\n\t" \
  "s_bitcmp0_b32 s92, 2\n\t" \
  "s_cbranch_scc1 .Lf2%=\n\t" \
  "s_nop 10\n\t" \
  ".Lf2%=:\n\t" \
  ".Lgo%=:\n\t" \
  "s_mov_b32 s92, 0\n\t" \
  ".Lpoll%=:\n\t" \
  "buffer_load_dwordx4 v[200:203], v248, %[rs], %[som] offen sc1\n\t" \
  "buffer_load_dwordx4 v[204:207], v248, %[rs], %[som2] offen sc1\n\t" \
  "s_waitcnt vmcnt(1) lgkmcnt(0)\n\t" \
  "ds_write_b128 v249, v[200:203]\n\t" \
  "v_smfmac_f32_16x16x64_bf16 v[192:195], v[200:203], v[208:215], v251\n\t" \
  "v_max3_u32 v244, v200, v201, v202\n\t" \
  "v_smfmac_f32_16x16x64_bf16 v[196:199], v[200:203], v[236:243], v251\n\t" \
  "s_waitcnt vmcnt(0)\n\t" \
  "ds_write_b128 v249, v[204:207] offset:8192\n\t" \
  "v_smfmac_f32_16x16x64_bf16 v[192:195], v[204:207], v[216:223], v251\n\t" \
  "v_max3_u32 v244, v244, v203, v204\n\t" \
  "v_max3_u32 v244, v244, v205, v206\n\t" \
  "v_smfmac_f32_16x16x64_bf16 v[196:199], v[204:207], v[224:231], v251\n\t" \
  "v_max_u32 v244, v244, v207\n\t" \
  "v_cmp_le_u32 vcc, %[sent], v244\n\t" \
  "s_cbranch_vccnz .Lslow%=\n\t" \
  ".Lok%=:\n\t" \
  "ds_read_b128 v[216:219], v250 offset:32768\n\t" \
  "ds_read_b128 v[220:223], v250 offset:33792\n\t" \
  "ds_read_b128 v[224:227], v250 offset:34816\n\t" \
  "ds_read_b128 v[228:231], v250 offset:35840\n\t" \
  "s_waitcnt lgkmcnt(4)\n\t" \
  "s_barrier\n\t" \
  "ds_read_b128 v[208:211], v248 offset:0\n\t" \
  "ds_read_b128 v[212:215], v248 offset:3072\n\t" \
  "ds_read_b128 v[236:239], v248 offset:4096\n\t" \
  "ds_read_b128 v[240:243], v248 offset:5120\n\t" \
  "s_waitcnt lgkmcnt(3)\n\t" \
  "ds_read_b128 v[244:247], v248 offset:12288\n\t" \
  "v_smfmac_f32_16x16x64_bf16 v[192:195], v[208:211], v[216:223], v251\n\t" \
  "ds_read_b128 v[216:219], v250 offset:0\n\t" \
  "v_smfmac_f32_16x16x64_bf16 v[196:199], v[208:211], v[224:231], v251\n\t" \
  "s_waitcnt lgkmcnt(4)\n\t" \
  "ds_read_b128 v[220:223], v250 offset:1024\n\t" \
  "v_smfmac_f32_16x16x64_bf16 v[192:195], v[212:215], v[0:7], v251\n\t" \
  "ds_read_b128 v[224:227], v250 offset:2048\n\t" \
  "v_smfmac_f32_16x16x64_bf16 v[196:199], v[212:215], v[8:15], v251\n\t" \
  "s_waitcnt lgkmcnt(5)\n\t" \
  "ds_read_b128 v[228:231], v250 offset:3072\n\t" \
  "v_smfmac_f32_16x16x64_bf16 v[192:195], v[236:239], v[16:23], v251\n\t" \
  "ds_read_b128 v[200:203], v248 offset:6144\n\t" \
  "v_smfmac_f32_16x16x64_bf16 v[196:199], v[236:239], v[24:31], v251\n\t" \
  "s_waitcnt lgkmcnt(6)\n\t" \
  "ds_read_b128 v[204:207], v248 offset:7168\n\t" \
  "v_smfmac_f32_16x16x64_bf16 v[192:195], v[240:243], v[32:39], v251\n\t" \
  "ds_read_b128 v[208:211], v248 offset:8192\n\t" \
  "v_smfmac_f32_16x16x64_bf16 v[196:199], v[240:243], v[40:47], v251\n\t" \
  "s_waitcnt lgkmcnt(5)\n\t" \
  "ds_read_b128 v[212:215], v248 offset:10240\n\t" \
  "v_smfmac_f32_16x16x64_bf16 v[192:195], v[244:247], v[216:223], v251\n\t" \
  "s_waitcnt lgkmcnt(4)\n\t" \
  "v_smfmac_f32_16x16x64_bf16 v[196:199], v[244:247], v[224:231], v251\n\t" \
  "s_waitcnt lgkmcnt(3)\n\t" \
  "ds_read_b128 v[236:239], v248 offset:11264\n\t" \
  "v_smfmac_f32_16x16x64_bf16 v[192:195], v[200:203], v[48:55], v251\n\t" \
  "ds_read_b128 v[240:243], v248 offset:13312\n\t" \
  "v_smfmac_f32_16x16x64_bf16 v[196:199], v[200:203], v[56:63], v251\n\t" \
  "s_waitcnt lgkmcnt(4)\n\t" \
  "v_smfmac_f32_16x16x64_bf16 v[192:195], v[204:207], v[64:71], v251\n\t" \
  "ds_read_b128 v[244:247], v248 offset:14336\n\t" \
  "v_smfmac_f32_16x16x64_bf16 v[196:199], v[204:207], v[72:79], v251\n\t" \
  "s_waitcnt lgkmcnt(4)\n\t" \
  "v_smfmac_f32_16x16x64_bf16 v[192:195], v[208:211], v[80:87], v251\n\t" \
  "ds_read_b128 v[200:203], v248 offset:15360\n\t" \
  "v_smfmac_f32_16x16x64_bf16 v[196:199], v[208:211], v[88:95], v251\n\t" \
  "s_waitcnt lgkmcnt(4)\n\t" \
  "v_smfmac_f32_16x16x64_bf16 v[192:195], v[212:215], v[96:103], v251\n\t" \
  "ds_read_b128 v[204:207], v248 offset:2048\n\t" \
  "v_smfmac_f32_16x16x64_bf16 v[196:199], v[212:215], v[104:111], v251\n\t" \
  "s_waitcnt lgkmcnt(3)\n\t" \
  "v_smfmac_f32_16x16x64_bf16 v[192:195], v[236:239], v[112:119], v251\n\t" \
  "v_smfmac_f32_16x16x64_bf16 v[196:199], v[236:239], v[120:127], v251\n\t" \
  "v_smfmac_f32_16x16x64_bf16 v[192:195], v[240:243], v[128:135], v251\n\t" \
  "v_smfmac_f32_16x16x64_bf16 v[196:199], v[240:243], v[136:143], v251\n\t" \
  "s_waitcnt lgkmcnt(1)\n\t" \
  "v_smfmac_f32_16x16x64_bf16 v[192:195], v[244:247], v[144:151], v251\n\t" \
  "v_smfmac_f32_16x16x64_bf16 v[196:199], v[244:247], v[152:159], v251\n\t" \
  "v_smfmac_f32_16x16x64_bf16 v[192:195], v[200:203], v[160:167], v251\n\t" \
  "v_smfmac_f32_16x16x64_bf16 v[196:199], v[200:203], v[168:175], v251\n\t" \
  "s_waitcnt lgkmcnt(0)\n\t" \
  "v_smfmac_f32_16x16x64_bf16 v[192:195], v[204:207], v[176:183], v251\n\t" \
  "v_smfmac_f32_16x16x64_bf16 v[196:199], v[204:207], v[184:191], v251\n\t" \
  "ds_read_b128 v[200:203], v248 offset:17408\n\t" \
  "ds_read_b128 v[204:207], v248 offset:21504\n\t" \
  "v_mov_b32 v250, v232\n\t" \
  "v_mov_b32 v249, v233\n\t" \
  "s_nop 2\n\t" \
  "v_add_f32 v192, v192, v194\n\t" \
  "v_add_f32 v193, v193, v195\n\t" \
  "v_add_f32 v196, v196, v198\n\t" \
  "v_add_f32 v197, v197, v199\n\t" \
  "v_cvt_pk_bf16_f32 v192, v192, v193\n\t" \
  "v_cvt_pk_bf16_f32 v196, v196, v197\n\t" \
  "ds_write2_b32 v252, v192, v196 offset1:64\n\t" \
  "v_mov_b32 v251, v234\n\t" \
  "ds_read_b64_tr_b16 v[236:237], v250 offset:512\n\t" \
  "ds_read_b64_tr_b16 v[238:239], v250 offset:576\n\t" \
  "v_mov_b32 v212, %[step]\n\t" \
  "s_mov_b64 exec, 1\n\t" \
  "ds_write_b32 v248, v212 offset:28688\n\t" \
  "s_mov_b64 exec, -1\n\t" \
  "buffer_load_dwordx4 v[192:195], v248, %[rt], %[svt] offen offset:0 sc1\n\t" \
  "buffer_load_dwordx4 v[196:199], v248, %[rt], %[svt] offen offset:1024 sc1\n\t" \
  "s_waitcnt lgkmcnt(1)\n\t" \
  "v_mfma_f32_16x16x32_bf16 v[200:203], v[200:203], v[236:239], 0\n\t" \
  "v_mfma_f32_16x16x32_bf16 v[204:207], v[204:207], v[236:239], 0\n\t" \
  "s_nop 7\n\t" \
  "v_cndmask_b32 v216, v200, v204, %[nh]\n\t" \
  "v_cndmask_b32 v217, v201, v205, %[nh]\n\t" \
  "v_cndmask_b32 v218, v202, v206, %[nh]\n\t" \
  "v_cndmask_b32 v219, v203, v207, %[nh]\n\t" \
  "ds_write_b128 v248, v[216:219] offset:17408\n\t" \
  "buffer_load_dword v252, v249, %[rs], %[sv] offen offset:0 sc1\n\t" \
  "buffer_load_dword v253, v249, %[rs], %[sv] offen offset:256 sc1\n\t" \
  "ds_read_b32 v212, v248 offset:28672\n\t" \
  "ds_read_b64_tr_b16 v[240:241], v250 offset:1024\n\t" \
  "ds_read_b64_tr_b16 v[242:243], v250 offset:1088\n\t" \
  "ds_read_b64_tr_b16 v[244:245], v250 offset:1536\n\t" \
  "ds_read_b64_tr_b16 v[246:247], v250 offset:1600\n\t" \
  "ds_read_b64_tr_b16 v[232:233], v250 offset:0\n\t" \
  "ds_read_b64_tr_b16 v[234:235], v250 offset:64\n\t" \
  "s_setprio 3\n\t" \
  "s_waitcnt lgkmcnt(0)\n\t" \
  "v_cmp_ne_u32 vcc, %[step], v212\n\t" \
  "s_barrier\n\t" \
  "s_and_b32 s93, vcc_lo, 0xf\n\t" \
  "s_cbranch_scc1 .Lre%=\n\t" \
  ".Lrb%=:\n\t" \
  "ds_read_b64_tr_b16 v[204:205], v250 offset:2560\n\t" \
  "ds_read_b64_tr_b16 v[206:207], v250 offset:2624\n\t" \
  "ds_read_b64_tr_b16 v[208:209], v250 offset:3072\n\t" \
  "ds_read_b64_tr_b16 v[210:211], v250 offset:3136\n\t" \
  "ds_read_b128 v[220:223], v248 offset:18432\n\t" \
  "ds_read_b128 v[224:227], v248 offset:19456\n\t" \
  "ds_read_b128 v[228:231], v248 offset:16384\n\t" \
  "ds_read_b64_tr_b16 v[212:213], v250 offset:3584\n\t" \
  "ds_read_b64_tr_b16 v[214:215], v250 offset:3648\n\t" \
  "ds_read_b64_tr_b16 v[200:201], v250 offset:2048\n\t" \
  "ds_read_b64_tr_b16 v[202:203], v250 offset:2112\n\t" \
  "s_waitcnt lgkmcnt(9)\n\t" \
  "v_mfma_f32_16x16x32_bf16 v[204:207], v[204:207], v[236:239], 0\n\t" \
  "s_waitcnt lgkmcnt(7)\n\t" \
  "v_mfma_f32_16x16x32_bf16 v[204:207], v[208:211], v[240:243], v[204:207]\n\t" \
  "s_waitcnt lgkmcnt(6)\n\t" \
  "v_pk_add_f32 v[216:217], v[216:217], v[220:221]\n\t" \
  "v_pk_add_f32 v[218:219], v[218:219], v[222:223]\n\t" \
  "s_waitcnt lgkmcnt(4)\n\t" \
  "v_pk_add_f32 v[224:225], v[224:225], v[228:229]\n\t" \
  "v_pk_add_f32 v[226:227], v[226:227], v[230:231]\n\t" \
  "v_pk_add_f32 v[216:217], v[216:217], v[224:225]\n\t" \
  "v_pk_add_f32 v[218:219], v[218:219], v[226:227]\n\t" \
  "s_waitcnt lgkmcnt(2)\n\t" \
  "v_mfma_f32_16x16x32_bf16 v[204:207], v[212:215], v[244:247], v[204:207]\n\t" \
  "v_max3_f32 v224, v216, v217, v218\n\t" \
  "v_max_f32 v224, v224, v219\n\t" \
  "s_waitcnt lgkmcnt(0)\n\t" \
  "v_mfma_f32_16x16x32_bf16 v[204:207], v[200:203], v[232:235], v[204:207]\n\t" \
  "v_max_f32_dpp v225, v224, v224 row_ror:8 row_mask:0xf bank_mask:0xf\n\t" \
  "v_max_f32_dpp v226, v224, v224 row_ror:8 row_mask:0xf bank_mask:0xf\n\t" \
  "s_nop 1\n\t" \
  "v_permlane32_swap_b32 v225, v226\n\t" \
  "v_max_f32 v231, v225, v226\n\t" \
  "v_mov_b32 v220, 0x3f803f80\n\t" \
  "v_mov_b32 v221, 0x3f803f80\n\t" \
  "v_mov_b64 v[222:223], v[220:221]\n\t" \
  "v_mov_b32 v237, 0\n\t" \
  "v_mov_b32 v238, 0\n\t" \
  "v_max3_f32 v208, v204, v205, v231\n\t" \
  "v_max3_f32 v209, v208, v206, v207\n\t" \
  "v_max3_f32 v210, v208, v206, v207\n\t" \
  "v_cndmask_b32 v204, v204, v206, %[up]\n\t" \
  "v_cndmask_b32 v205, v205, v207, %[up]\n\t" \
  "v_permlane16_swap_b32 v209, v210\n\t" \
  "v_max_f32 v208, v209, v210\n\t" \
  "v_mul_f32 v208, %[negc], v208\n\t" \
  "v_fma_f32 v204, v204, %[c], v208\n\t" \
  "v_fma_f32 v205, v205, %[c], v208\n\t" \
  "v_exp_f32 v204, v204\n\t" \
  "v_fma_f32 v216, v216, %[c], v208\n\t" \
  "v_exp_f32 v205, v205\n\t" \
  "v_fma_f32 v217, v217, %[c], v208\n\t" \
  "v_exp_f32 v216, v216\n\t" \
  "v_fma_f32 v218, v218, %[c], v208\n\t" \
  "v_cvt_pk_bf16_f32 v236, v204, v205\n\t" \
  "v_cvt_pk_bf16_f32 v239, v204, v205\n\t" \
  "v_exp_f32 v217, v217\n\t" \
  "v_fma_f32 v219, v219, %[c], v208\n\t" \
  "v_mfma_f32_16x16x16_bf16 v[200:203], v[220:221], v[236:237], 0\n\t" \
  "v_exp_f32 v218, v218\n\t" \
  "s_waitcnt vmcnt(0)\n\t" \
  "v_mfma_f32_16x16x16_bf16 v[224:227], v[252:253], v[236:237], 0\n\t" \
  "v_cvt_pk_bf16_f32 v214, v216, v217\n\t" \
  "v_cvt_pk_bf16_f32 v212, v216, v217\n\t" \
  "v_exp_f32 v219, v219\n\t" \
  "v_mfma_f32_16x16x16_bf16 v[228:231], v[252:253], v[238:239], 0\n\t" \
  "v_cvt_pk_bf16_f32 v215, v218, v219\n\t" \
  "v_cvt_pk_bf16_f32 v213, v218, v219\n\t" \
  "v_mov_b32_dpp v212, v214 row_ror:8 row_mask:0xf bank_mask:0xc\n\t" \
  "v_mov_b32_dpp v214, v214 row_ror:8 row_mask:0xf bank_mask:0x3\n\t" \
  "v_mov_b32_dpp v213, v215 row_ror:8 row_mask:0xf bank_mask:0xc\n\t" \
  "v_mov_b32_dpp v215, v215 row_ror:8 row_mask:0xf bank_mask:0x3\n\t" \
  "v_max_u32 v208, v252, v253\n\t" \
  "v_cmp_le_u32 vcc, %[sent], v208\n\t" \
  "v_mfma_f32_16x16x32_bf16 v[200:203], v[220:223], v[212:215], v[200:203]\n\t" \
  ".Lpv%=:\n\t" \
  "v_mfma_f32_16x16x32_bf16 v[224:227], v[192:195], v[212:215], v[224:227]\n\t" \
  "v_mfma_f32_16x16x32_bf16 v[228:231], v[196:199], v[212:215], v[228:231]\n\t" \
  "s_cbranch_vccnz .Lvs%=\n\t" \
  ".Lvok%=:\n\t" \
  "s_nop 4\n\t" \
  "v_rcp_f32 v208, v200\n\t" \
  "v_cndmask_b32 v224, v224, v226, %[nh]\n\t" \
  "v_cndmask_b32 v225, v225, v227, %[nh]\n\t" \
  "v_cndmask_b32 v228, v228, v230, %[nh]\n\t" \
  "v_cndmask_b32 v229, v229, v231, %[nh]\n\t" \
  "v_pk_mul_f32 v[224:225], v[224:225], v[208:209] op_sel_hi:[1,0]\n\t" \
  "v_pk_mul_f32 v[228:229], v[228:229], v[208:209] op_sel_hi:[1,0]\n\t" \
  "v_cvt_pk_bf16_f32 v224, v224, v225\n\t" \
  "v_cvt_pk_bf16_f32 v225, v228, v229\n\t" \
  "buffer_store_dwordx2 v[224:225], v251, %[rs], %[so] offen sc1\n\t" \
  "s_memtime s[90:91]\n\t" \
  "s_setprio 0\n\t" \
  "s_waitcnt lgkmcnt(0)\n\t" \
  "s_mov_b32 %[tout], s90\n\t" \
  "s_mov_b32 %[spins], s92\n\t" \
  "s_branch .Lend%=\n\t" \
  ".Lre%=:\n\t" \
  "ds_read_b64_tr_b16 v[240:241], v250 offset:1024\n\t" \
  "ds_read_b64_tr_b16 v[242:243], v250 offset:1088\n\t" \
  "ds_read_b64_tr_b16 v[244:245], v250 offset:1536\n\t" \
  "ds_read_b64_tr_b16 v[246:247], v250 offset:1600\n\t" \
  "ds_read_b64_tr_b16 v[232:233], v250 offset:0\n\t" \
  "ds_read_b64_tr_b16 v[234:235], v250 offset:64\n\t" \
  "s_waitcnt lgkmcnt(0)\n\t" \
  "s_branch .Lrb%=\n\t" \
  ".Lvs%=:\n\t" \
  "v_cmp_eq_u32 vcc, %[sent], v252\n\t" \
  "v_cmp_eq_u32 s[94:95], %[sent], v253\n\t" \
  "s_or_b64 s[94:95], s[94:95], vcc\n\t" \
  "s_cbranch_scc0 .Lvok%=\n\t" \
  "s_add_u32 s92, s92, 1\n\t" \
  "s_sleep 2\n\t" \
  "s_and_b32 s93, s92, 63\n\t" \
  "s_cbranch_scc1 .Lvr%=\n\t" \
  "buffer_load_dword v208, off, %[rs], %[sab] sc1\n\t" \
  "s_waitcnt vmcnt(0)\n\t" \
  "v_readfirstlane_b32 s93, v208\n\t" \
  "s_nop 3\n\t" \
  "s_cmp_eq_u32 s93, 1\n\t" \
  "s_cbranch_scc1 .Lvok%=\n\t" \
  "s_cmp_lt_u32 s92, 0x80000\n\t" \
  "s_cbranch_scc1 .Lvr%=\n\t" \
  "v_mov_b32 v208, 1\n\t" \
  "buffer_store_dword v208, off, %[rs], %[sab] sc1\n\t" \
  "s_branch .Lvok%=\n\t" \
  ".Lvr%=:\n\t" \
  "buffer_load_dword v252, v249, %[rs], %[sv] offen offset:0 sc1\n\t" \
  "buffer_load_dword v253, v249, %[rs], %[sv] offen offset:256 sc1\n\t" \
  "s_waitcnt vmcnt(0)\n\t" \
  "v_mfma_f32_16x16x16_bf16 v[224:227], v[252:253], v[236:237], 0\n\t" \
  "v_mfma_f32_16x16x16_bf16 v[228:231], v[252:253], v[238:239], 0\n\t" \
  "v_max_u32 v208, v252, v253\n\t" \
  "v_cmp_le_u32 vcc, %[sent], v208\n\t" \
  "s_nop 4\n\t" \
  "s_branch .Lpv%=\n\t" \
  "s_branch .Lend%=\n\t" \
  ".Lslow%=:\n\t" \
  "s_mov_b64 s[94:95], 0\n\t" \
  "v_cmp_eq_u32 vcc, %[sent], v200\n\t" \
  "s_or_b64 s[94:95], s[94:95], vcc\n\t" \
  "v_cmp_eq_u32 vcc, %[sent], v201\n\t" \
  "s_or_b64 s[94:95], s[94:95], vcc\n\t" \
  "v_cmp_eq_u32 vcc, %[sent], v202\n\t" \
  "s_or_b64 s[94:95], s[94:95], vcc\n\t" \
  "v_cmp_eq_u32 vcc, %[sent], v203\n\t" \
  "s_or_b64 s[94:95], s[94:95], vcc\n\t" \
  "v_cmp_eq_u32 vcc, %[sent], v204\n\t" \
  "s_or_b64 s[94:95], s[94:95], vcc\n\t" \
  "v_cmp_eq_u32 vcc, %[sent], v205\n\t" \
  "s_or_b64 s[94:95], s[94:95], vcc\n\t" \
  "v_cmp_eq_u32 vcc, %[sent], v206\n\t" \
  "s_or_b64 s[94:95], s[94:95], vcc\n\t" \
  "v_cmp_eq_u32 vcc, %[sent], v207\n\t" \
  "s_or_b64 s[94:95], s[94:95], vcc\n\t" \
  "s_cmp_eq_u64 s[94:95], 0\n\t" \
  "s_cbranch_scc1 .Lok%=\n\t" \
  "v_mov_b64 v[192:193], 0\n\t" \
  "v_mov_b64 v[194:195], 0\n\t" \
  "v_mov_b64 v[196:197], 0\n\t" \
  "v_mov_b64 v[198:199], 0\n\t" \
  "s_add_u32 s92, s92, 1\n\t" \
  "s_cmp_eq_u32 s92, 1\n\t" \
  "s_cbranch_scc1 .Lpoll%=\n\t" \
  "s_sleep 2\n\t" \
  "s_and_b32 s93, s92, 63\n\t" \
  "s_cmp_lg_u32 s93, 0\n\t" \
  "s_cbranch_scc1 .Lpoll%=\n\t" \
  "v_mov_b32 v245, 0\n\t" \
  "buffer_load_dword v244, v245, %[rs], %[sab] offen sc1\n\t" \
  "s_waitcnt vmcnt(0)\n\t" \
  "v_readfirstlane_b32 s93, v244\n\t" \
  "s_nop 3\n\t" \
  "s_cmp_eq_u32 s93, 1\n\t" \
  "s_cbranch_scc1 .Lok%=\n\t" \
  "s_cmp_lt_u32 s92, 0x80000\n\t" \
  "s_cbranch_scc1 .Lpoll%=\n\t" \
  "v_mov_b32 v244, 1\n\t" \
  "buffer_store_dword v244, v245, %[rs], %[sab] offen sc1\n\t" \
  "s_branch .Lok%=\n\t" \
  ".Lend%=:\n\t"
#define QK_ASM_2 \
  "s_waitcnt lgkmcnt(0)\n\t" \
  "v_subrev_u32 v244, 0x8000, v250\n\t" \
  "ds_read_b128 v[216:219], v248 offset:40960\n\t" \
  "ds_read_b128 v[220:223], v248 offset:41984\n\t" \
  "ds_read_b128 v[224:227], v248 offset:43008\n\t" \
  "ds_read_b128 v[228:231], v248 offset:44032\n\t" \
  "ds_read_b128 v[208:211], v244 offset:0\n\t" \
  "ds_read_b128 v[212:215], v244 offset:1024\n\t" \
  "ds_read_b128 v[236:239], v244 offset:2048\n\t" \
  "ds_read_b128 v[240:243], v244 offset:3072\n\t" \
  "v_mov_b64 v[192:193], 0\n\t" \
  "v_mov_b64 v[194:195], 0\n\t" \
  "v_mov_b64 v[196:197], 0\n\t" \
  "v_mov_b64 v[198:199], 0\n\t" \
  "s_movk_i32 s92, 0x2000\n\t" \
  ".Lwt%=:\n\t" \
  "ds_read_b64 v[200:201], v248 offset:30720\n\t" \
  "s_memrealtime s[90:91]\n\t" \
  "s_sub_u32 s92, s92, 1\n\t" \
  "s_waitcnt lgkmcnt(0)\n\t" \
  "v_readfirstlane_b32 s93, v200\n\t" \
  "v_readfirstlane_b32 %[tgt], v201\n\t" \
  "s_cbranch_scc1 .Lgo%=\n\t" \
  "s_cmp_eq_u32 s93, %[step]\n\t" \
  "s_cbranch_scc1 .Lwx%=\n\t" \
  "s_sleep 1\n\t" \
  "s_branch .Lwt%=\n\t" \
  ".Lwx%=:\n\t" \
  "s_lshl_b32 s93, s90, 2\n\t" \
  "s_sub_i32 s93, %[tgt], s93\n\t" \
  "s_cmp_le_i32 s93, 0\n\t" \
  "s_cbranch_scc1 .Lgo%=\n\t" \
  "s_min_i32 s93, s93, 8000\n\t" \
  "s_mov_b32 s94, s93\n\t" \
  ".Lsl%=:\n\t" \
  "s_sub_i32 s94, s94, 18\n\t" \
  "s_cmp_lt_i32 s94, 30\n\t" \
  "s_cbranch_scc1 .Lsx%=\n\t" \
  "s_sleep 1\n\t" \
  "s_branch .Lsl%=\n\t" \
  ".Lsx%=:\n\t" \
  "s_sub_i32 s93, s93, 12\n\t" \
  "s_max_i32 s93, s93, 4\n\t" \
  "s_lshr_b32 s93, s93, 2\n\t" \
  "s_add_u32 s93, s93, s90\n\t" \
  "s_memrealtime s[90:91]\n\t" \
  ".Lpace%=:\n\t" \
  "s_memrealtime s[94:95]\n\t" \
  "s_waitcnt lgkmcnt(1)\n\t" \
  "s_sub_i32 s92, s90, s93\n\t" \
  "s_cmp_ge_i32 s92, 0\n\t" \
  "s_cbranch_scc1 .Le%=\n\t" \
  "s_memrealtime s[90:91]\n\t" \
  "s_waitcnt lgkmcnt(1)\n\t" \
  "s_sub_i32 s92, s94, s93\n\t" \
  "s_cmp_lt_i32 s92, 0\n\t" \
  "s_cbranch_scc1 .Lpace%=\n\t" \
  "s_nop 4\n\t" \
  ".Le%=:\n\t" \
  "s_add_u32 s92, s92, s93\n\t" \
  "s_lshl_b32 s92, s92, 2\n\t" \
  "s_sub_i32 s92, %[tgt], s92\n\t" \
  "s_max_i32 s92, s92, 0\n\t" \
  "s_bitcmp0_b32 s92, 3\n\t" \
  "s_cbranch_scc1 .Lf3%=\n\t" \
  "s_nop 15\n\t" \
  ".Lf3%=:\n\t" \
  "s_bitcmp0_b32 s92, 2\n\t" \
  "s_cbranch_scc1 .Lf2%=\n\t" \
  "s_nop 10\n\t" \
  ".Lf2%=:\n\t" \
  ".Lgo%=:\n\t" \
  "s_mov_b32 s92, 0\n\t" \
  ".Lpoll%=:\n\t" \
  "buffer_load_dwordx4 v[200:203], v248, %[rs], %[som] offen sc1\n\t" \
  "buffer_load_dwordx4 v[204:207], v248, %[rs], %[som2] offen sc1\n\t" \
  "s_waitcnt vmcnt(1) lgkmcnt(0)\n\t" \
  "ds_write_b128 v249, v[200:203]\n\t" \
  "v_smfmac_f32_16x16x64_bf16 v[192:195], v[200:203], v[208:215], v251\n\t" \
  "v_max3_u32 v244, v200, v201, v202\n\t" \
  "v_smfmac_f32_16x16x64_bf16 v[196:199], v[200:203], v[236:243], v251\n\t" \
  "s_waitcnt vmcnt(0)\n\t" \
  "ds_write_b128 v249, v[204:207] offset:8192\n\t" \
  "v_smfmac_f32_16x16x64_bf16 v[192:195], v[204:207], v[216:223], v251\n\t" \
  "v_max3_u32 v244, v244, v203, v204\n\t" \
  "v_max3_u32 v244, v244, v205, v206\n\t" \
  "v_smfmac_f32_16x16x64_bf16 v[196:199], v[204:207], v[224:231], v251\n\t" \
  "v_max_u32 v244, v244, v207\n\t" \
  "v_cmp_le_u32 vcc, %[sent], v244\n\t" \
  "s_cbranch_vccnz .Lslow%=\n\t" \
  ".Lok%=:\n\t" \
  "ds_read_b128 v[216:219], v250 offset:32768\n\t" \
  "ds_read_b128 v[220:223], v250 offset:33792\n\t" \
  "ds_read_b128 v[224:227], v250 offset:34816\n\t" \
  "ds_read_b128 v[228:231], v250 offset:35840\n\t" \
  "s_waitcnt lgkmcnt(4)\n\t" \
  "s_barrier\n\t" \
  "ds_read_b128 v[208:211], v248 offset:3072\n\t" \
  "ds_read_b128 v[212:215], v248 offset:4096\n\t" \
  "ds_read_b128 v[236:239], v248 offset:5120\n\t" \
  "ds_read_b128 v[240:243], v248 offset:6144\n\t" \
  "s_waitcnt lgkmcnt(3)\n\t" \
  "ds_read_b128 v[244:247], v248 offset:13312\n\t" \
  "v_smfmac_f32_16x16x64_bf16 v[192:195], v[208:211], v[216:223], v251\n\t" \
  "ds_read_b128 v[216:219], v250 offset:0\n\t" \
  "v_smfmac_f32_16x16x64_bf16 v[196:199], v[208:211], v[224:231], v251\n\t" \
  "s_waitcnt lgkmcnt(4)\n\t" \
  "ds_read_b128 v[220:223], v250 offset:1024\n\t" \
  "v_smfmac_f32_16x16x64_bf16 v[192:195], v[212:215], v[0:7], v251\n\t" \
  "ds_read_b128 v[224:227], v250 offset:2048\n\t" \
  "v_smfmac_f32_16x16x64_bf16 v[196:199], v[212:215], v[8:15], v251\n\t" \
  "s_waitcnt lgkmcnt(5)\n\t" \
  "ds_read_b128 v[228:231], v250 offset:3072\n\t" \
  "v_smfmac_f32_16x16x64_bf16 v[192:195], v[236:239], v[16:23], v251\n\t" \
  "ds_read_b128 v[200:203], v248 offset:7168\n\t" \
  "v_smfmac_f32_16x16x64_bf16 v[196:199], v[236:239], v[24:31], v251\n\t" \
  "s_waitcnt lgkmcnt(6)\n\t" \
  "ds_read_b128 v[204:207], v248 offset:8192\n\t" \
  "v_smfmac_f32_16x16x64_bf16 v[192:195], v[240:243], v[32:39], v251\n\t" \
  "ds_read_b128 v[208:211], v248 offset:9216\n\t" \
  "v_smfmac_f32_16x16x64_bf16 v[196:199], v[240:243], v[40:47], v251\n\t" \
  "s_waitcnt lgkmcnt(5)\n\t" \
  "ds_read_b128 v[212:215], v248 offset:11264\n\t" \
  "v_smfmac_f32_16x16x64_bf16 v[192:195], v[244:247], v[216:223], v251\n\t" \
  "s_waitcnt lgkmcnt(4)\n\t" \
  "v_smfmac_f32_16x16x64_bf16 v[196:199], v[244:247], v[224:231], v251\n\t" \
  "s_waitcnt lgkmcnt(3)\n\t" \
  "ds_read_b128 v[236:239], v248 offset:12288\n\t" \
  "v_smfmac_f32_16x16x64_bf16 v[192:195], v[200:203], v[48:55], v251\n\t" \
  "ds_read_b128 v[240:243], v248 offset:14336\n\t" \
  "v_smfmac_f32_16x16x64_bf16 v[196:199], v[200:203], v[56:63], v251\n\t" \
  "s_waitcnt lgkmcnt(4)\n\t" \
  "v_smfmac_f32_16x16x64_bf16 v[192:195], v[204:207], v[64:71], v251\n\t" \
  "ds_read_b128 v[244:247], v248 offset:15360\n\t" \
  "v_smfmac_f32_16x16x64_bf16 v[196:199], v[204:207], v[72:79], v251\n\t" \
  "s_waitcnt lgkmcnt(4)\n\t" \
  "v_smfmac_f32_16x16x64_bf16 v[192:195], v[208:211], v[80:87], v251\n\t" \
  "ds_read_b128 v[200:203], v248 offset:0\n\t" \
  "v_smfmac_f32_16x16x64_bf16 v[196:199], v[208:211], v[88:95], v251\n\t" \
  "s_waitcnt lgkmcnt(4)\n\t" \
  "v_smfmac_f32_16x16x64_bf16 v[192:195], v[212:215], v[96:103], v251\n\t" \
  "ds_read_b128 v[204:207], v248 offset:1024\n\t" \
  "v_smfmac_f32_16x16x64_bf16 v[196:199], v[212:215], v[104:111], v251\n\t" \
  "s_waitcnt lgkmcnt(3)\n\t" \
  "v_smfmac_f32_16x16x64_bf16 v[192:195], v[236:239], v[112:119], v251\n\t" \
  "v_smfmac_f32_16x16x64_bf16 v[196:199], v[236:239], v[120:127], v251\n\t" \
  "v_smfmac_f32_16x16x64_bf16 v[192:195], v[240:243], v[128:135], v251\n\t" \
  "v_smfmac_f32_16x16x64_bf16 v[196:199], v[240:243], v[136:143], v251\n\t" \
  "s_waitcnt lgkmcnt(1)\n\t" \
  "v_smfmac_f32_16x16x64_bf16 v[192:195], v[244:247], v[144:151], v251\n\t" \
  "v_smfmac_f32_16x16x64_bf16 v[196:199], v[244:247], v[152:159], v251\n\t" \
  "v_smfmac_f32_16x16x64_bf16 v[192:195], v[200:203], v[160:167], v251\n\t" \
  "v_smfmac_f32_16x16x64_bf16 v[196:199], v[200:203], v[168:175], v251\n\t" \
  "s_waitcnt lgkmcnt(0)\n\t" \
  "v_smfmac_f32_16x16x64_bf16 v[192:195], v[204:207], v[176:183], v251\n\t" \
  "v_smfmac_f32_16x16x64_bf16 v[196:199], v[204:207], v[184:191], v251\n\t" \
  "ds_read_b128 v[200:203], v248 offset:18432\n\t" \
  "ds_read_b128 v[204:207], v248 offset:22528\n\t" \
  "v_mov_b32 v250, v232\n\t" \
  "v_mov_b32 v249, v233\n\t" \
  "s_nop 2\n\t" \
  "v_add_f32 v192, v192, v194\n\t" \
  "v_add_f32 v193, v193, v195\n\t" \
  "v_add_f32 v196, v196, v198\n\t" \
  "v_add_f32 v197, v197, v199\n\t" \
  "v_cvt_pk_bf16_f32 v192, v192, v193\n\t" \
  "v_cvt_pk_bf16_f32 v196, v196, v197\n\t" \
  "ds_write2_b32 v252, v192, v196 offset1:64\n\t" \
  "v_mov_b32 v251, v234\n\t" \
  "ds_read_b64_tr_b16 v[240:241], v250 offset:1024\n\t" \
  "ds_read_b64_tr_b16 v[242:243], v250 offset:1088\n\t" \
  "v_mov_b32 v212, %[step]\n\t" \
  "s_mov_b64 exec, 1\n\t" \
  "ds_write_b32 v248, v212 offset:28704\n\t" \
  "s_mov_b64 exec, -1\n\t" \
  "buffer_load_dwordx4 v[192:195], v248, %[rt], %[svt] offen offset:0 sc1\n\t" \
  "buffer_load_dwordx4 v[196:199], v248, %[rt], %[svt] offen offset:1024 sc1\n\t" \
  "s_waitcnt lgkmcnt(1)\n\t" \
  "v_mfma_f32_16x16x32_bf16 v[200:203], v[200:203], v[240:243], 0\n\t" \
  "v_mfma_f32_16x16x32_bf16 v[204:207], v[204:207], v[240:243], 0\n\t" \
  "s_nop 7\n\t" \
  "v_cndmask_b32 v216, v200, v204, %[nh]\n\t" \
  "v_cndmask_b32 v217, v201, v205, %[nh]\n\t" \
  "v_cndmask_b32 v218, v202, v206, %[nh]\n\t" \
  "v_cndmask_b32 v219, v203, v207, %[nh]\n\t" \
  "ds_write_b128 v248, v[216:219] offset:18432\n\t" \
  "buffer_load_dword v252, v249, %[rs], %[sv] offen offset:0 sc1\n\t" \
  "buffer_load_dword v253, v249, %[rs], %[sv] offen offset:256 sc1\n\t" \
  "ds_read_b32 v212, v248 offset:28672\n\t" \
  "ds_read_b64_tr_b16 v[244:245], v250 offset:1536\n\t" \
  "ds_read_b64_tr_b16 v[246:247], v250 offset:1600\n\t" \
  "ds_read_b64_tr_b16 v[232:233], v250 offset:0\n\t" \
  "ds_read_b64_tr_b16 v[234:235], v250 offset:64\n\t" \
  "ds_read_b64_tr_b16 v[236:237], v250 offset:512\n\t" \
  "ds_read_b64_tr_b16 v[238:239], v250 offset:576\n\t" \
  "s_setprio 3\n\t" \
  "s_waitcnt lgkmcnt(0)\n\t" \
  "v_cmp_ne_u32 vcc, %[step], v212\n\t" \
  "s_barrier\n\t" \
  "s_and_b32 s93, vcc_lo, 0xf\n\t" \
  "s_cbranch_scc1 .Lre%=\n\t" \
  ".Lrb%=:\n\t" \
  "ds_read_b64_tr_b16 v[208:209], v250 offset:3072\n\t" \
  "ds_read_b64_tr_b16 v[210:211], v250 offset:3136\n\t" \
  "ds_read_b64_tr_b16 v[212:213], v250 offset:3584\n\t" \
  "ds_read_b64_tr_b16 v[214:215], v250 offset:3648\n\t" \
  "ds_read_b128 v[220:223], v248 offset:19456\n\t" \
  "ds_read_b128 v[224:227], v248 offset:16384\n\t" \
  "ds_read_b128 v[228:231], v248 offset:17408\n\t" \
  "ds_read_b64_tr_b16 v[200:201], v250 offset:2048\n\t" \
  "ds_read_b64_tr_b16 v[202:203], v250 offset:2112\n\t" \
  "ds_read_b64_tr_b16 v[204:205], v250 offset:2560\n\t" \
  "ds_read_b64_tr_b16 v[206:207], v250 offset:2624\n\t" \
  "s_waitcnt lgkmcnt(9)\n\t" \
  "v_mfma_f32_16x16x32_bf16 v[208:211], v[208:211], v[240:243], 0\n\t" \
  "s_waitcnt lgkmcnt(7)\n\t" \
  "v_mfma_f32_16x16x32_bf16 v[208:211], v[212:215], v[244:247], v[208:211]\n\t" \
  "s_waitcnt lgkmcnt(6)\n\t" \
  "v_pk_add_f32 v[216:217], v[216:217], v[220:221]\n\t" \
  "v_pk_add_f32 v[218:219], v[218:219], v[222:223]\n\t" \
  "s_waitcnt lgkmcnt(4)\n\t" \
  "v_pk_add_f32 v[224:225], v[224:225], v[228:229]\n\t" \
  "v_pk_add_f32 v[226:227], v[226:227], v[230:231]\n\t" \
  "v_pk_add_f32 v[216:217], v[216:217], v[224:225]\n\t" \
  "v_pk_add_f32 v[218:219], v[218:219], v[226:227]\n\t" \
  "s_waitcnt lgkmcnt(2)\n\t" \
  "v_mfma_f32_16x16x32_bf16 v[208:211], v[200:203], v[232:235], v[208:211]\n\t" \
  "v_max3_f32 v224, v216, v217, v218\n\t" \
  "v_max_f32 v224, v224, v219\n\t" \
  "s_waitcnt lgkmcnt(0)\n\t" \
  "v_mfma_f32_16x16x32_bf16 v[208:211], v[204:207], v[236:239], v[208:211]\n\t" \
  "v_max_f32_dpp v225, v224, v224 row_ror:8 row_mask:0xf bank_mask:0xf\n\t" \
  "v_max_f32_dpp v226, v224, v224 row_ror:8 row_mask:0xf bank_mask:0xf\n\t" \
  "s_nop 1\n\t" \
  "v_permlane32_swap_b32 v225, v226\n\t" \
  "v_max_f32 v231, v225, v226\n\t" \
  "v_mov_b32 v220, 0x3f803f80\n\t" \
  "v_mov_b32 v221, 0x3f803f80\n\t" \
  "v_mov_b64 v[222:223], v[220:221]\n\t" \
  "v_mov_b32 v241, 0\n\t" \
  "v_mov_b32 v242, 0\n\t" \
  "v_max3_f32 v212, v208, v209, v231\n\t" \
  "v_max3_f32 v213, v212, v210, v211\n\t" \
  "v_max3_f32 v214, v212, v210, v211\n\t" \
  "v_cndmask_b32 v208, v208, v210, %[up]\n\t" \
  "v_cndmask_b32 v209, v209, v211, %[up]\n\t" \
  "v_permlane16_swap_b32 v213, v214\n\t" \
  "v_max_f32 v212, v213, v214\n\t" \
  "v_mul_f32 v212, %[negc], v212\n\t" \
  "v_fma_f32 v208, v208, %[c], v212\n\t" \
  "v_fma_f32 v209, v209, %[c], v212\n\t" \
  "v_exp_f32 v208, v208\n\t" \
  "v_fma_f32 v216, v216, %[c], v212\n\t" \
  "v_exp_f32 v209, v209\n\t" \
  "v_fma_f32 v217, v217, %[c], v212\n\t" \
  "v_exp_f32 v216, v216\n\t" \
  "v_fma_f32 v218, v218, %[c], v212\n\t" \
  "v_cvt_pk_bf16_f32 v240, v208, v209\n\t" \
  "v_cvt_pk_bf16_f32 v243, v208, v209\n\t" \
  "v_exp_f32 v217, v217\n\t" \
  "v_fma_f32 v219, v219, %[c], v212\n\t" \
  "v_mfma_f32_16x16x16_bf16 v[204:207], v[220:221], v[240:241], 0\n\t" \
  "v_exp_f32 v218, v218\n\t" \
  "s_waitcnt vmcnt(0)\n\t" \
  "v_mfma_f32_16x16x16_bf16 v[224:227], v[252:253], v[240:241], 0\n\t" \
  "v_cvt_pk_bf16_f32 v202, v216, v217\n\t" \
  "v_cvt_pk_bf16_f32 v200, v216, v217\n\t" \
  "v_exp_f32 v219, v219\n\t" \
  "v_mfma_f32_16x16x16_bf16 v[228:231], v[252:253], v[242:243], 0\n\t" \
  "v_cvt_pk_bf16_f32 v203, v218, v219\n\t" \
  "v_cvt_pk_bf16_f32 v201, v218, v219\n\t" \
  "v_mov_b32_dpp v200, v202 row_ror:8 row_mask:0xf bank_mask:0xc\n\t" \
  "v_mov_b32_dpp v202, v202 row_ror:8 row_mask:0xf bank_mask:0x3\n\t" \
  "v_mov_b32_dpp v201, v203 row_ror:8 row_mask:0xf bank_mask:0xc\n\t" \
  "v_mov_b32_dpp v203, v203 row_ror:8 row_mask:0xf bank_mask:0x3\n\t" \
  "v_max_u32 v212, v252, v253\n\t" \
  "v_cmp_le_u32 vcc, %[sent], v212\n\t" \
  "v_mfma_f32_16x16x32_bf16 v[204:207], v[220:223], v[200:203], v[204:207]\n\t" \
  ".Lpv%=:\n\t" \
  "v_mfma_f32_16x16x32_bf16 v[224:227], v[192:195], v[200:203], v[224:227]\n\t" \
  "v_mfma_f32_16x16x32_bf16 v[228:231], v[196:199], v[200:203], v[228:231]\n\t" \
  "s_cbranch_vccnz .Lvs%=\n\t" \
  ".Lvok%=:\n\t" \
  "s_nop 4\n\t" \
  "v_rcp_f32 v212, v204\n\t" \
  "v_cndmask_b32 v224, v224, v226, %[nh]\n\t" \
  "v_cndmask_b32 v225, v225, v227, %[nh]\n\t" \
  "v_cndmask_b32 v228, v228, v230, %[nh]\n\t" \
  "v_cndmask_b32 v229, v229, v231, %[nh]\n\t" \
  "v_pk_mul_f32 v[224:225], v[224:225], v[212:213] op_sel_hi:[1,0]\n\t" \
  "v_pk_mul_f32 v[228:229], v[228:229], v[212:213] op_sel_hi:[1,0]\n\t" \
  "v_cvt_pk_bf16_f32 v224, v224, v225\n\t" \
  "v_cvt_pk_bf16_f32 v225, v228, v229\n\t" \
  "buffer_store_dwordx2 v[224:225], v251, %[rs], %[so] offen sc1\n\t" \
  "s_memtime s[90:91]\n\t" \
  "s_setprio 0\n\t" \
  "s_waitcnt lgkmcnt(0)\n\t" \
  "s_mov_b32 %[tout], s90\n\t" \
  "s_mov_b32 %[spins], s92\n\t" \
  "s_branch .Lend%=\n\t" \
  ".Lre%=:\n\t" \
  "ds_read_b64_tr_b16 v[244:245], v250 offset:1536\n\t" \
  "ds_read_b64_tr_b16 v[246:247], v250 offset:1600\n\t" \
  "ds_read_b64_tr_b16 v[232:233], v250 offset:0\n\t" \
  "ds_read_b64_tr_b16 v[234:235], v250 offset:64\n\t" \
  "ds_read_b64_tr_b16 v[236:237], v250 offset:512\n\t" \
  "ds_read_b64_tr_b16 v[238:239], v250 offset:576\n\t" \
  "s_waitcnt lgkmcnt(0)\n\t" \
  "s_branch .Lrb%=\n\t" \
  ".Lvs%=:\n\t" \
  "v_cmp_eq_u32 vcc, %[sent], v252\n\t" \
  "v_cmp_eq_u32 s[94:95], %[sent], v253\n\t" \
  "s_or_b64 s[94:95], s[94:95], vcc\n\t" \
  "s_cbranch_scc0 .Lvok%=\n\t" \
  "s_add_u32 s92, s92, 1\n\t" \
  "s_sleep 2\n\t" \
  "s_and_b32 s93, s92, 63\n\t" \
  "s_cbranch_scc1 .Lvr%=\n\t" \
  "buffer_load_dword v212, off, %[rs], %[sab] sc1\n\t" \
  "s_waitcnt vmcnt(0)\n\t" \
  "v_readfirstlane_b32 s93, v212\n\t" \
  "s_nop 3\n\t" \
  "s_cmp_eq_u32 s93, 1\n\t" \
  "s_cbranch_scc1 .Lvok%=\n\t" \
  "s_cmp_lt_u32 s92, 0x80000\n\t" \
  "s_cbranch_scc1 .Lvr%=\n\t" \
  "v_mov_b32 v212, 1\n\t" \
  "buffer_store_dword v212, off, %[rs], %[sab] sc1\n\t" \
  "s_branch .Lvok%=\n\t" \
  ".Lvr%=:\n\t" \
  "buffer_load_dword v252, v249, %[rs], %[sv] offen offset:0 sc1\n\t" \
  "buffer_load_dword v253, v249, %[rs], %[sv] offen offset:256 sc1\n\t" \
  "s_waitcnt vmcnt(0)\n\t" \
  "v_mfma_f32_16x16x16_bf16 v[224:227], v[252:253], v[240:241], 0\n\t" \
  "v_mfma_f32_16x16x16_bf16 v[228:231], v[252:253], v[242:243], 0\n\t" \
  "v_max_u32 v212, v252, v253\n\t" \
  "v_cmp_le_u32 vcc, %[sent], v212\n\t" \
  "s_nop 4\n\t" \
  "s_branch .Lpv%=\n\t" \
  "s_branch .Lend%=\n\t" \
  ".Lslow%=:\n\t" \
  "s_mov_b64 s[94:95], 0\n\t" \
  "v_cmp_eq_u32 vcc, %[sent], v200\n\t" \
  "s_or_b64 s[94:95], s[94:95], vcc\n\t" \
  "v_cmp_eq_u32 vcc, %[sent], v201\n\t" \
  "s_or_b64 s[94:95], s[94:95], vcc\n\t" \
  "v_cmp_eq_u32 vcc, %[sent], v202\n\t" \
  "s_or_b64 s[94:95], s[94:95], vcc\n\t" \
  "v_cmp_eq_u32 vcc, %[sent], v203\n\t" \
  "s_or_b64 s[94:95], s[94:95], vcc\n\t" \
  "v_cmp_eq_u32 vcc, %[sent], v204\n\t" \
  "s_or_b64 s[94:95], s[94:95], vcc\n\t" \
  "v_cmp_eq_u32 vcc, %[sent], v205\n\t" \
  "s_or_b64 s[94:95], s[94:95], vcc\n\t" \
  "v_cmp_eq_u32 vcc, %[sent], v206\n\t" \
  "s_or_b64 s[94:95], s[94:95], vcc\n\t" \
  "v_cmp_eq_u32 vcc, %[sent], v207\n\t" \
  "s_or_b64 s[94:95], s[94:95], vcc\n\t" \
  "s_cmp_eq_u64 s[94:95], 0\n\t" \
  "s_cbranch_scc1 .Lok%=\n\t" \
  "v_mov_b64 v[192:193], 0\n\t" \
  "v_mov_b64 v[194:195], 0\n\t" \
  "v_mov_b64 v[196:197], 0\n\t" \
  "v_mov_b64 v[198:199], 0\n\t" \
  "s_add_u32 s92, s92, 1\n\t" \
  "s_cmp_eq_u32 s92, 1\n\t" \
  "s_cbranch_scc1 .Lpoll%=\n\t" \
  "s_sleep 2\n\t" \
  "s_and_b32 s93, s92, 63\n\t" \
  "s_cmp_lg_u32 s93, 0\n\t" \
  "s_cbranch_scc1 .Lpoll%=\n\t" \
  "v_mov_b32 v245, 0\n\t" \
  "buffer_load_dword v244, v245, %[rs], %[sab] offen sc1\n\t" \
  "s_waitcnt vmcnt(0)\n\t" \
  "v_readfirstlane_b32 s93, v244\n\t" \
  "s_nop 3\n\t" \
  "s_cmp_eq_u32 s93, 1\n\t" \
  "s_cbranch_scc1 .Lok%=\n\t" \
  "s_cmp_lt_u32 s92, 0x80000\n\t" \
  "s_cbranch_scc1 .Lpoll%=\n\t" \
  "v_mov_b32 v244, 1\n\t" \
  "buffer_store_dword v244, v245, %[rs], %[sab] offen sc1\n\t" \
  "s_branch .Lok%=\n\t" \
  ".Lend%=:\n\t"
#define QK_ASM_3 \
  "s_waitcnt lgkmcnt(0)\n\t" \
  "v_subrev_u32 v244, 0x8000, v250\n\t" \
  "ds_read_b128 v[216:219], v248 offset:45056\n\t" \
  "ds_read_b128 v[220:223], v248 offset:46080\n\t" \
  "ds_read_b128 v[224:227], v248 offset:47104\n\t" \
  "ds_read_b128 v[228:231], v248 offset:48128\n\t" \
  "ds_read_b128 v[208:211], v244 offset:0\n\t" \
  "ds_read_b128 v[212:215], v244 offset:1024\n\t" \
  "ds_read_b128 v[236:239], v244 offset:2048\n\t" \
  "ds_read_b128 v[240:243], v244 offset:3072\n\t" \
  "v_mov_b64 v[192:193], 0\n\t" \
  "v_mov_b64 v[194:195], 0\n\t" \
  "v_mov_b64 v[196:197], 0\n\t" \
  "v_mov_b64 v[198:199], 0\n\t" \
  "s_movk_i32 s92, 0x2000\n\t" \
  ".Lwt%=:\n\t" \
  "ds_read_b64 v[200:201], v248 offset:30720\n\t" \
  "s_memrealtime s[90:91]\n\t" \
  "s_sub_u32 s92, s92, 1\n\t" \
  "s_waitcnt lgkmcnt(0)\n\t" \
  "v_readfirstlane_b32 s93, v200\n\t" \
  "v_readfirstlane_b32 %[tgt], v201\n\t" \
  "s_cbranch_scc1 .Lgo%=\n\t" \
  "s_cmp_eq_u32 s93, %[step]\n\t" \
  "s_cbranch_scc1 .Lwx%=\n\t" \
  "s_sleep 1\n\t" \
  "s_branch .Lwt%=\n\t" \
  ".Lwx%=:\n\t" \
  "s_lshl_b32 s93, s90, 2\n\t" \
  "s_sub_i32 s93, %[tgt], s93\n\t" \
  "s_cmp_le_i32 s93, 0\n\t" \
  "s_cbranch_scc1 .Lgo%=\n\t" \
  "s_min_i32 s93, s93, 8000\n\t" \
  "s_mov_b32 s94, s93\n\t" \
  ".Lsl%=:\n\t" \
  "s_sub_i32 s94, s94, 18\n\t" \
  "s_cmp_lt_i32 s94, 30\n\t" \
  "s_cbranch_scc1 .Lsx%=\n\t" \
  "s_sleep 1\n\t" \
  "s_branch .Lsl%=\n\t" \
  ".Lsx%=:\n\t" \
  "s_sub_i32 s93, s93, 12\n\t" \
  "s_max_i32 s93, s93, 4\n\t" \
  "s_lshr_b32 s93, s93, 2\n\t" \
  "s_add_u32 s93, s93, s90\n\t" \
  "s_memrealtime s[90:91]\n\t" \
  ".Lpace%=:\n\t" \
  "s_memrealtime s[94:95]\n\t" \
  "s_waitcnt lgkmcnt(1)\n\t" \
  "s_sub_i32 s92, s90, s93\n\t" \
  "s_cmp_ge_i32 s92, 0\n\t" \
  "s_cbranch_scc1 .Le%=\n\t" \
  "s_memrealtime s[90:91]\n\t" \
  "s_waitcnt lgkmcnt(1)\n\t" \
  "s_sub_i32 s92, s94, s93\n\t" \
  "s_cmp_lt_i32 s92, 0\n\t" \
  "s_cbranch_scc1 .Lpace%=\n\t" \
  "s_nop 4\n\t" \
  ".Le%=:\n\t" \
  "s_add_u32 s92, s92, s93\n\t" \
  "s_lshl_b32 s92, s92, 2\n\t" \
  "s_sub_i32 s92, %[tgt], s92\n\t" \
  "s_max_i32 s92, s92, 0\n\t" \
  "s_bitcmp0_b32 s92, 3\n\t" \
  "s_cbranch_scc1 .Lf3%=\n\t" \
  "s_nop 15\n\t" \
  ".Lf3%=:\n\t" \
  "s_bitcmp0_b32 s92, 2\n\t" \
  "s_cbranch_scc1 .Lf2%=\n\t" \
  "s_nop 10\n\t" \
  ".Lf2%=:\n\t" \
  ".Lgo%=:\n\t" \
  "s_mov_b32 s92, 0\n\t" \
  ".Lpoll%=:\n\t" \
  "buffer_load_dwordx4 v[200:203], v248, %[rs], %[som] offen sc1\n\t" \
  "buffer_load_dwordx4 v[204:207], v248, %[rs], %[som2] offen sc1\n\t" \
  "s_waitcnt vmcnt(1) lgkmcnt(0)\n\t" \
  "ds_write_b128 v249, v[200:203]\n\t" \
  "v_smfmac_f32_16x16x64_bf16 v[192:195], v[200:203], v[208:215], v251\n\t" \
  "v_max3_u32 v244, v200, v201, v202\n\t" \
  "v_smfmac_f32_16x16x64_bf16 v[196:199], v[200:203], v[236:243], v251\n\t" \
  "s_waitcnt vmcnt(0)\n\t" \
  "ds_write_b128 v249, v[204:207] offset:8192\n\t" \
  "v_smfmac_f32_16x16x64_bf16 v[192:195], v[204:207], v[216:223], v251\n\t" \
  "v_max3_u32 v244, v244, v203, v204\n\t" \
  "v_max3_u32 v244, v244, v205, v206\n\t" \
  "v_smfmac_f32_16x16x64_bf16 v[196:199], v[204:207], v[224:231], v251\n\t" \
  "v_max_u32 v244, v244, v207\n\t" \
  "v_cmp_le_u32 vcc, %[sent], v244\n\t" \
  "s_cbranch_vccnz .Lslow%=\n\t" \
  ".Lok%=:\n\t" \
  "ds_read_b128 v[216:219], v250 offset:32768\n\t" \
  "ds_read_b128 v[220:223], v250 offset:33792\n\t" \
  "ds_read_b128 v[224:227], v250 offset:34816\n\t" \
  "ds_read_b128 v[228:231], v250 offset:35840\n\t" \
  "s_waitcnt lgkmcnt(4)\n\t" \
  "s_barrier\n\t" \
  "ds_read_b128 v[208:211], v248 offset:2048\n\t" \
  "ds_read_b128 v[212:215], v248 offset:5120\n\t" \
  "ds_read_b128 v[236:239], v248 offset:6144\n\t" \
  "ds_read_b128 v[240:243], v248 offset:7168\n\t" \
  "s_waitcnt lgkmcnt(3)\n\t" \
  "ds_read_b128 v[244:247], v248 offset:12288\n\t" \
  "v_smfmac_f32_16x16x64_bf16 v[192:195], v[208:211], v[216:223], v251\n\t" \
  "ds_read_b128 v[216:219], v250 offset:0\n\t" \
  "v_smfmac_f32_16x16x64_bf16 v[196:199], v[208:211], v[224:231], v251\n\t" \
  "s_waitcnt lgkmcnt(4)\n\t" \
  "ds_read_b128 v[220:223], v250 offset:1024\n\t" \
  "v_smfmac_f32_16x16x64_bf16 v[192:195], v[212:215], v[0:7], v251\n\t" \
  "ds_read_b128 v[224:227], v250 offset:2048\n\t" \
  "v_smfmac_f32_16x16x64_bf16 v[196:199], v[212:215], v[8:15], v251\n\t" \
  "s_waitcnt lgkmcnt(5)\n\t" \
  "ds_read_b128 v[228:231], v250 offset:3072\n\t" \
  "v_smfmac_f32_16x16x64_bf16 v[192:195], v[236:239], v[16:23], v251\n\t" \
  "ds_read_b128 v[200:203], v248 offset:8192\n\t" \
  "v_smfmac_f32_16x16x64_bf16 v[196:199], v[236:239], v[24:31], v251\n\t" \
  "s_waitcnt lgkmcnt(6)\n\t" \
  "ds_read_b128 v[204:207], v248 offset:9216\n\t" \
  "v_smfmac_f32_16x16x64_bf16 v[192:195], v[240:243], v[32:39], v251\n\t" \
  "ds_read_b128 v[208:211], v248 offset:10240\n\t" \
  "v_smfmac_f32_16x16x64_bf16 v[196:199], v[240:243], v[40:47], v251\n\t" \
  "s_waitcnt lgkmcnt(5)\n\t" \
  "ds_read_b128 v[212:215], v248 offset:13312\n\t" \
  "v_smfmac_f32_16x16x64_bf16 v[192:195], v[244:247], v[216:223], v251\n\t" \
  "s_waitcnt lgkmcnt(4)\n\t" \
  "v_smfmac_f32_16x16x64_bf16 v[196:199], v[244:247], v[224:231], v251\n\t" \
  "s_waitcnt lgkmcnt(3)\n\t" \
  "ds_read_b128 v[236:239], v248 offset:14336\n\t" \
  "v_smfmac_f32_16x16x64_bf16 v[192:195], v[200:203], v[48:55], v251\n\t" \
  "ds_read_b128 v[240:243], v248 offset:15360\n\t" \
  "v_smfmac_f32_16x16x64_bf16 v[196:199], v[200:203], v[56:63], v251\n\t" \
  "s_waitcnt lgkmcnt(4)\n\t" \
  "v_smfmac_f32_16x16x64_bf16 v[192:195], v[204:207], v[64:71], v251\n\t" \
  "ds_read_b128 v[244:247], v248 offset:0\n\t" \
  "v_smfmac_f32_16x16x64_bf16 v[196:199], v[204:207], v[72:79], v251\n\t" \
  "s_waitcnt lgkmcnt(4)\n\t" \
  "v_smfmac_f32_16x16x64_bf16 v[192:195], v[208:211], v[80:87], v251\n\t" \
  "ds_read_b128 v[200:203], v248 offset:1024\n\t" \
  "v_smfmac_f32_16x16x64_bf16 v[196:199], v[208:211], v[88:95], v251\n\t" \
  "s_waitcnt lgkmcnt(4)\n\t" \
  "v_smfmac_f32_16x16x64_bf16 v[192:195], v[212:215], v[96:103], v251\n\t" \
  "ds_read_b128 v[204:207], v248 offset:4096\n\t" \
  "v_smfmac_f32_16x16x64_bf16 v[196:199], v[212:215], v[104:111], v251\n\t" \
  "s_waitcnt lgkmcnt(3)\n\t" \
  "v_smfmac_f32_16x16x64_bf16 v[192:195], v[236:239], v[112:119], v251\n\t" \
  "v_smfmac_f32_16x16x64_bf16 v[196:199], v[236:239], v[120:127], v251\n\t" \
  "v_smfmac_f32_16x16x64_bf16 v[192:195], v[240:243], v[128:135], v251\n\t" \
  "v_smfmac_f32_16x16x64_bf16 v[196:199], v[240:243], v[136:143], v251\n\t" \
  "s_waitcnt lgkmcnt(1)\n\t" \
  "v_smfmac_f32_16x16x64_bf16 v[192:195], v[244:247], v[144:151], v251\n\t" \
  "v_smfmac_f32_16x16x64_bf16 v[196:199], v[244:247], v[152:159], v251\n\t" \
  "v_smfmac_f32_16x16x64_bf16 v[192:195], v[200:203], v[160:167], v251\n\t" \
  "v_smfmac_f32_16x16x64_bf16 v[196:199], v[200:203], v[168:175], v251\n\t" \
  "s_waitcnt lgkmcnt(0)\n\t" \
  "v_smfmac_f32_16x16x64_bf16 v[192:195], v[204:207], v[176:183], v251\n\t" \
  "v_smfmac_f32_16x16x64_bf16 v[196:199], v[204:207], v[184:191], v251\n\t" \
  "ds_read_b128 v[200:203], v248 offset:19456\n\t" \
  "ds_read_b128 v[204:207], v248 offset:23552\n\t" \
  "v_mov_b32 v250, v232\n\t" \
  "v_mov_b32 v249, v233\n\t" \
  "s_nop 2\n\t" \
  "v_add_f32 v192, v192, v194\n\t" \
  "v_add_f32 v193, v193, v195\n\t" \
  "v_add_f32 v196, v196, v198\n\t" \
  "v_add_f32 v197, v197, v199\n\t" \
  "v_cvt_pk_bf16_f32 v192, v192, v193\n\t" \
  "v_cvt_pk_bf16_f32 v196, v196, v197\n\t" \
  "ds_write2_b32 v252, v192, v196 offset1:64\n\t" \
  "v_mov_b32 v251, v234\n\t" \
  "ds_read_b64_tr_b16 v[244:245], v250 offset:1536\n\t" \
  "ds_read_b64_tr_b16 v[246:247], v250 offset:1600\n\t" \
  "v_mov_b32 v212, %[step]\n\t" \
  "s_mov_b64 exec, 1\n\t" \
  "ds_write_b32 v248, v212 offset:28720\n\t" \
  "s_mov_b64 exec, -1\n\t" \
  "buffer_load_dwordx4 v[192:195], v248, %[rt], %[svt] offen offset:0 sc1\n\t" \
  "buffer_load_dwordx4 v[196:199], v248, %[rt], %[svt] offen offset:1024 sc1\n\t" \
  "s_waitcnt lgkmcnt(1)\n\t" \
  "v_mfma_f32_16x16x32_bf16 v[200:203], v[200:203], v[244:247], 0\n\t" \
  "v_mfma_f32_16x16x32_bf16 v[204:207], v[204:207], v[244:247], 0\n\t" \
  "s_nop 7\n\t" \
  "v_cndmask_b32 v216, v200, v204, %[nh]\n\t" \
  "v_cndmask_b32 v217, v201, v205, %[nh]\n\t" \
  "v_cndmask_b32 v218, v202, v206, %[nh]\n\t" \
  "v_cndmask_b32 v219, v203, v207, %[nh]\n\t" \
  "ds_write_b128 v248, v[216:219] offset:19456\n\t" \
  "buffer_load_dword v252, v249, %[rs], %[sv] offen offset:0 sc1\n\t" \
  "buffer_load_dword v253, v249, %[rs], %[sv] offen offset:256 sc1\n\t" \
  "ds_read_b32 v212, v248 offset:28672\n\t" \
  "ds_read_b64_tr_b16 v[232:233], v250 offset:0\n\t" \
  "ds_read_b64_tr_b16 v[234:235], v250 offset:64\n\t" \
  "ds_read_b64_tr_b16 v[236:237], v250 offset:512\n\t" \
  "ds_read_b64_tr_b16 v[238:239], v250 offset:576\n\t" \
  "ds_read_b64_tr_b16 v[240:241], v250 offset:1024\n\t" \
  "ds_read_b64_tr_b16 v[242:243], v250 offset:1088\n\t" \
  "s_setprio 3\n\t" \
  "s_waitcnt lgkmcnt(0)\n\t" \
  "v_cmp_ne_u32 vcc, %[step], v212\n\t" \
  "s_barrier\n\t" \
  "s_and_b32 s93, vcc_lo, 0xf\n\t" \
  "s_cbranch_scc1 .Lre%=\n\t" \
  ".Lrb%=:\n\t" \
  "ds_read_b64_tr_b16 v[212:213], v250 offset:3584\n\t" \
  "ds_read_b64_tr_b16 v[214:215], v250 offset:3648\n\t" \
  "ds_read_b64_tr_b16 v[200:201], v250 offset:2048\n\t" \
  "ds_read_b64_tr_b16 v[202:203], v250 offset:2112\n\t" \
  "ds_read_b128 v[220:223], v248 offset:16384\n\t" \
  "ds_read_b128 v[224:227], v248 offset:17408\n\t" \
  "ds_read_b128 v[228:231], v248 offset:18432\n\t" \
  "ds_read_b64_tr_b16 v[204:205], v250 offset:2560\n\t" \
  "ds_read_b64_tr_b16 v[206:207], v250 offset:2624\n\t" \
  "ds_read_b64_tr_b16 v[208:209], v250 offset:3072\n\t" \
  "ds_read_b64_tr_b16 v[210:211], v250 offset:3136\n\t" \
  "s_waitcnt lgkmcnt(9)\n\t" \
  "v_mfma_f32_16x16x32_bf16 v[212:215], v[212:215], v[244:247], 0\n\t" \
  "s_waitcnt lgkmcnt(7)\n\t" \
  "v_mfma_f32_16x16x32_bf16 v[212:215], v[200:203], v[232:235], v[212:215]\n\t" \
  "s_waitcnt lgkmcnt(6)\n\t" \
  "v_pk_add_f32 v[216:217], v[216:217], v[220:221]\n\t" \
  "v_pk_add_f32 v[218:219], v[218:219], v[222:223]\n\t" \
  "s_waitcnt lgkmcnt(4)\n\t" \
  "v_pk_add_f32 v[224:225], v[224:225], v[228:229]\n\t" \
  "v_pk_add_f32 v[226:227], v[226:227], v[230:231]\n\t" \
  "v_pk_add_f32 v[216:217], v[216:217], v[224:225]\n\t" \
  "v_pk_add_f32 v[218:219], v[218:219], v[226:227]\n\t" \
  "s_waitcnt lgkmcnt(2)\n\t" \
  "v_mfma_f32_16x16x32_bf16 v[212:215], v[204:207], v[236:239], v[212:215]\n\t" \
  "v_max3_f32 v224, v216, v217, v218\n\t" \
  "v_max_f32 v224, v224, v219\n\t" \
  "s_waitcnt lgkmcnt(0)\n\t" \
  "v_mfma_f32_16x16x32_bf16 v[212:215], v[208:211], v[240:243], v[212:215]\n\t" \
  "v_max_f32_dpp v225, v224, v224 row_ror:8 row_mask:0xf bank_mask:0xf\n\t" \
  "v_max_f32_dpp v226, v224, v224 row_ror:8 row_mask:0xf bank_mask:0xf\n\t" \
  "s_nop 1\n\t" \
  "v_permlane32_swap_b32 v225, v226\n\t" \
  "v_max_f32 v231, v225, v226\n\t" \
  "v_mov_b32 v220, 0x3f803f80\n\t" \
  "v_mov_b32 v221, 0x3f803f80\n\t" \
  "v_mov_b64 v[222:223], v[220:221]\n\t" \
  "v_mov_b32 v245, 0\n\t" \
  "v_mov_b32 v246, 0\n\t" \
  "v_max3_f32 v200, v212, v213, v231\n\t" \
  "v_max3_f32 v201, v200, v214, v215\n\t" \
  "v_max3_f32 v202, v200, v214, v215\n\t" \
  "v_cndmask_b32 v212, v212, v214, %[up]\n\t" \
  "v_cndmask_b32 v213, v213, v215, %[up]\n\t" \
  "v_permlane16_swap_b32 v201, v202\n\t" \
  "v_max_f32 v200, v201, v202\n\t" \
  "v_mul_f32 v200, %[negc], v200\n\t" \
  "v_fma_f32 v212, v212, %[c], v200\n\t" \
  "v_fma_f32 v213, v213, %[c], v200\n\t" \
  "v_exp_f32 v212, v212\n\t" \
  "v_fma_f32 v216, v216, %[c], v200\n\t" \
  "v_exp_f32 v213, v213\n\t" \
  "v_fma_f32 v217, v217, %[c], v200\n\t" \
  "v_exp_f32 v216, v216\n\t" \
  "v_fma_f32 v218, v218, %[c], v200\n\t" \
  "v_cvt_pk_bf16_f32 v244, v212, v213\n\t" \
  "v_cvt_pk_bf16_f32 v247, v212, v213\n\t" \
  "v_exp_f32 v217, v217\n\t" \
  "v_fma_f32 v219, v219, %[c], v200\n\t" \
  "v_mfma_f32_16x16x16_bf16 v[208:211], v[220:221], v[244:245], 0\n\t" \
  "v_exp_f32 v218, v218\n\t" \
  "s_waitcnt vmcnt(0)\n\t" \
  "v_mfma_f32_16x16x16_bf16 v[224:227], v[252:253], v[244:245], 0\n\t" \
  "v_cvt_pk_bf16_f32 v206, v216, v217\n\t" \
  "v_cvt_pk_bf16_f32 v204, v216, v217\n\t" \
  "v_exp_f32 v219, v219\n\t" \
  "v_mfma_f32_16x16x16_bf16 v[228:231], v[252:253], v[246:247], 0\n\t" \
  "v_cvt_pk_bf16_f32 v207, v218, v219\n\t" \
  "v_cvt_pk_bf16_f32 v205, v218, v219\n\t" \
  "v_mov_b32_dpp v204, v206 row_ror:8 row_mask:0xf bank_mask:0xc\n\t" \
  "v_mov_b32_dpp v206, v206 row_ror:8 row_mask:0xf bank_mask:0x3\n\t" \
  "v_mov_b32_dpp v205, v207 row_ror:8 row_mask:0xf bank_mask:0xc\n\t" \
  "v_mov_b32_dpp v207, v207 row_ror:8 row_mask:0xf bank_mask:0x3\n\t" \
  "v_max_u32 v200, v252, v253\n\t" \
  "v_cmp_le_u32 vcc, %[sent], v200\n\t" \
  "v_mfma_f32_16x16x32_bf16 v[208:211], v[220:223], v[204:207], v[208:211]\n\t" \
  ".Lpv%=:\n\t" \
  "v_mfma_f32_16x16x32_bf16 v[224:227], v[192:195], v[204:207], v[224:227]\n\t" \
  "v_mfma_f32_16x16x32_bf16 v[228:231], v[196:199], v[204:207], v[228:231]\n\t" \
  "s_cbranch_vccnz .Lvs%=\n\t" \
  ".Lvok%=:\n\t" \
  "s_nop 4\n\t" \
  "v_rcp_f32 v200, v208\n\t" \
  "v_cndmask_b32 v224, v224, v226, %[nh]\n\t" \
  "v_cndmask_b32 v225, v225, v227, %[nh]\n\t" \
  "v_cndmask_b32 v228, v228, v230, %[nh]\n\t" \
  "v_cndmask_b32 v229, v229, v231, %[nh]\n\t" \
  "v_pk_mul_f32 v[224:225], v[224:225], v[200:201] op_sel_hi:[1,0]\n\t" \
  "v_pk_mul_f32 v[228:229], v[228:229], v[200:201] op_sel_hi:[1,0]\n\t" \
  "v_cvt_pk_bf16_f32 v224, v224, v225\n\t" \
  "v_cvt_pk_bf16_f32 v225, v228, v229\n\t" \
  "buffer_store_dwordx2 v[224:225], v251, %[rs], %[so] offen sc1\n\t" \
  "s_memtime s[90:91]\n\t" \
  "s_setprio 0\n\t" \
  "s_waitcnt lgkmcnt(0)\n\t" \
  "s_mov_b32 %[tout], s90\n\t" \
  "s_mov_b32 %[spins], s92\n\t" \
  "s_branch .Lend%=\n\t" \
  ".Lre%=:\n\t" \
  "ds_read_b64_tr_b16 v[232:233], v250 offset:0\n\t" \
  "ds_read_b64_tr_b16 v[234:235], v250 offset:64\n\t" \
  "ds_read_b64_tr_b16 v[236:237], v250 offset:512\n\t" \
  "ds_read_b64_tr_b16 v[238:239], v250 offset:576\n\t" \
  "ds_read_b64_tr_b16 v[240:241], v250 offset:1024\n\t" \
  "ds_read_b64_tr_b16 v[242:243], v250 offset:1088\n\t" \
  "s_waitcnt lgkmcnt(0)\n\t" \
  "s_branch .Lrb%=\n\t" \
  ".Lvs%=:\n\t" \
  "v_cmp_eq_u32 vcc, %[sent], v252\n\t" \
  "v_cmp_eq_u32 s[94:95], %[sent], v253\n\t" \
  "s_or_b64 s[94:95], s[94:95], vcc\n\t" \
  "s_cbranch_scc0 .Lvok%=\n\t" \
  "s_add_u32 s92, s92, 1\n\t" \
  "s_sleep 2\n\t" \
  "s_and_b32 s93, s92, 63\n\t" \
  "s_cbranch_scc1 .Lvr%=\n\t" \
  "buffer_load_dword v200, off, %[rs], %[sab] sc1\n\t" \
  "s_waitcnt vmcnt(0)\n\t" \
  "v_readfirstlane_b32 s93, v200\n\t" \
  "s_nop 3\n\t" \
  "s_cmp_eq_u32 s93, 1\n\t" \
  "s_cbranch_scc1 .Lvok%=\n\t" \
  "s_cmp_lt_u32 s92, 0x80000\n\t" \
  "s_cbranch_scc1 .Lvr%=\n\t" \
  "v_mov_b32 v200, 1\n\t" \
  "buffer_store_dword v200, off, %[rs], %[sab] sc1\n\t" \
  "s_branch .Lvok%=\n\t" \
  ".Lvr%=:\n\t" \
  "buffer_load_dword v252, v249, %[rs], %[sv] offen offset:0 sc1\n\t" \
  "buffer_load_dword v253, v249, %[rs], %[sv] offen offset:256 sc1\n\t" \
  "s_waitcnt vmcnt(0)\n\t" \
  "v_mfma_f32_16x16x16_bf16 v[224:227], v[252:253], v[244:245], 0\n\t" \
  "v_mfma_f32_16x16x16_bf16 v[228:231], v[252:253], v[246:247], 0\n\t" \
  "v_max_u32 v200, v252, v253\n\t" \
  "v_cmp_le_u32 vcc, %[sent], v200\n\t" \
  "s_nop 4\n\t" \
  "s_branch .Lpv%=\n\t" \
  "s_branch .Lend%=\n\t" \
  ".Lslow%=:\n\t" \
  "s_mov_b64 s[94:95], 0\n\t" \
  "v_cmp_eq_u32 vcc, %[sent], v200\n\t" \
  "s_or_b64 s[94:95], s[94:95], vcc\n\t" \
  "v_cmp_eq_u32 vcc, %[sent], v201\n\t" \
  "s_or_b64 s[94:95], s[94:95], vcc\n\t" \
  "v_cmp_eq_u32 vcc, %[sent], v202\n\t" \
  "s_or_b64 s[94:95], s[94:95], vcc\n\t" \
  "v_cmp_eq_u32 vcc, %[sent], v203\n\t" \
  "s_or_b64 s[94:95], s[94:95], vcc\n\t" \
  "v_cmp_eq_u32 vcc, %[sent], v204\n\t" \
  "s_or_b64 s[94:95], s[94:95], vcc\n\t" \
  "v_cmp_eq_u32 vcc, %[sent], v205\n\t" \
  "s_or_b64 s[94:95], s[94:95], vcc\n\t" \
  "v_cmp_eq_u32 vcc, %[sent], v206\n\t" \
  "s_or_b64 s[94:95], s[94:95], vcc\n\t" \
  "v_cmp_eq_u32 vcc, %[sent], v207\n\t" \
  "s_or_b64 s[94:95], s[94:95], vcc\n\t" \
  "s_cmp_eq_u64 s[94:95], 0\n\t" \
  "s_cbranch_scc1 .Lok%=\n\t" \
  "v_mov_b64 v[192:193], 0\n\t" \
  "v_mov_b64 v[194:195], 0\n\t" \
  "v_mov_b64 v[196:197], 0\n\t" \
  "v_mov_b64 v[198:199], 0\n\t" \
  "s_add_u32 s92, s92, 1\n\t" \
  "s_cmp_eq_u32 s92, 1\n\t" \
  "s_cbranch_scc1 .Lpoll%=\n\t" \
  "s_sleep 2\n\t" \
  "s_and_b32 s93, s92, 63\n\t" \
  "s_cmp_lg_u32 s93, 0\n\t" \
  "s_cbranch_scc1 .Lpoll%=\n\t" \
  "v_mov_b32 v245, 0\n\t" \
  "buffer_load_dword v244, v245, %[rs], %[sab] offen sc1\n\t" \
  "s_waitcnt vmcnt(0)\n\t" \
  "v_readfirstlane_b32 s93, v244\n\t" \
  "s_nop 3\n\t" \
  "s_cmp_eq_u32 s93, 1\n\t" \
  "s_cbranch_scc1 .Lok%=\n\t" \
  "s_cmp_lt_u32 s92, 0x80000\n\t" \
  "s_cbranch_scc1 .Lpoll%=\n\t" \
  "v_mov_b32 v244, 1\n\t" \
  "buffer_store_dword v244, v245, %[rs], %[sab] offen sc1\n\t" \
  "s_branch .Lok%=\n\t" \
  ".Lend%=:\n\t"
#define QK_ASM_4 \
  "s_waitcnt lgkmcnt(0)\n\t" \
  "v_subrev_u32 v244, 0x8000, v250\n\t" \
  "ds_read_b128 v[216:219], v248 offset:49152\n\t" \
  "ds_read_b128 v[220:223], v248 offset:50176\n\t" \
  "ds_read_b128 v[224:227], v248 offset:51200\n\t" \
  "ds_read_b128 v[228:231], v248 offset:52224\n\t" \
  "ds_read_b128 v[208:211], v244 offset:0\n\t" \
  "ds_read_b128 v[212:215], v244 offset:1024\n\t" \
  "ds_read_b128 v[236:239], v244 offset:2048\n\t" \
  "ds_read_b128 v[240:243], v244 offset:3072\n\t" \
  "v_mov_b64 v[192:193], 0\n\t" \
  "v_mov_b64 v[194:195], 0\n\t" \
  "v_mov_b64 v[196:197], 0\n\t" \
  "v_mov_b64 v[198:199], 0\n\t" \
  "s_movk_i32 s92, 0x2000\n\t" \
  ".Lwt%=:\n\t" \
  "ds_read_b64 v[200:201], v248 offset:30720\n\t" \
  "s_memrealtime s[90:91]\n\t" \
  "s_sub_u32 s92, s92, 1\n\t" \
  "s_waitcnt lgkmcnt(0)\n\t" \
  "v_readfirstlane_b32 s93, v200\n\t" \
  "v_readfirstlane_b32 %[tgt], v201\n\t" \
  "s_cbranch_scc1 .Lgo%=\n\t" \
  "s_cmp_eq_u32 s93, %[step]\n\t" \
  "s_cbranch_scc1 .Lwx%=\n\t" \
  "s_sleep 1\n\t" \
  "s_branch .Lwt%=\n\t" \
  ".Lwx%=:\n\t" \
  "s_lshl_b32 s93, s90, 2\n\t" \
  "s_sub_i32 s93, %[tgt], s93\n\t" \
  "s_cmp_le_i32 s93, 0\n\t" \
  "s_cbranch_scc1 .Lgo%=\n\t" \
  "s_min_i32 s93, s93, 8000\n\t" \
  "s_mov_b32 s94, s93\n\t" \
  ".Lsl%=:\n\t" \
  "s_sub_i32 s94, s94, 18\n\t" \
  "s_cmp_lt_i32 s94, 30\n\t" \
  "s_cbranch_scc1 .Lsx%=\n\t" \
  "s_sleep 1\n\t" \
  "s_branch .Lsl%=\n\t" \
  ".Lsx%=:\n\t" \
  "s_sub_i32 s93, s93, 12\n\t" \
  "s_max_i32 s93, s93, 4\n\t" \
  "s_lshr_b32 s93, s93, 2\n\t" \
  "s_add_u32 s93, s93, s90\n\t" \
  "s_memrealtime s[90:91]\n\t" \
  ".Lpace%=:\n\t" \
  "s_memrealtime s[94:95]\n\t" \
  "s_waitcnt lgkmcnt(1)\n\t" \
  "s_sub_i32 s92, s90, s93\n\t" \
  "s_cmp_ge_i32 s92, 0\n\t" \
  "s_cbranch_scc1 .Le%=\n\t" \
  "s_memrealtime s[90:91]\n\t" \
  "s_waitcnt lgkmcnt(1)\n\t" \
  "s_sub_i32 s92, s94, s93\n\t" \
  "s_cmp_lt_i32 s92, 0\n\t" \
  "s_cbranch_scc1 .Lpace%=\n\t" \
  "s_nop 4\n\t" \
  ".Le%=:\n\t" \
  "s_add_u32 s92, s92, s93\n\t" \
  "s_lshl_b32 s92, s92, 2\n\t" \
  "s_sub_i32 s92, %[tgt], s92\n\t" \
  "s_max_i32 s92, s92, 0\n\t" \
  "s_bitcmp0_b32 s92, 3\n\t" \
  "s_cbranch_scc1 .Lf3%=\n\t" \
  "s_nop 15\n\t" \
  ".Lf3%=:\n\t" \
  "s_bitcmp0_b32 s92, 2\n\t" \
  "s_cbranch_scc1 .Lf2%=\n\t" \
  "s_nop 10\n\t" \
  ".Lf2%=:\n\t" \
  ".Lgo%=:\n\t" \
  "s_mov_b32 s92, 0\n\t" \
  ".Lpoll%=:\n\t" \
  "buffer_load_dwordx4 v[200:203], v248, %[rs], %[som] offen sc1\n\t" \
  "buffer_load_dwordx4 v[204:207], v248, %[rs], %[som2] offen sc1\n\t" \
  "s_waitcnt vmcnt(1) lgkmcnt(0)\n\t" \
  "ds_write_b128 v249, v[200:203]\n\t" \
  "v_smfmac_f32_16x16x64_bf16 v[192:195], v[200:203], v[208:215], v251\n\t" \
  "v_max3_u32 v244, v200, v201, v202\n\t" \
  "v_smfmac_f32_16x16x64_bf16 v[196:199], v[200:203], v[236:243], v251\n\t" \
  "s_waitcnt vmcnt(0)\n\t" \
  "ds_write_b128 v249, v[204:207] offset:8192\n\t" \
  "v_smfmac_f32_16x16x64_bf16 v[192:195], v[204:207], v[216:223], v251\n\t" \
  "v_max3_u32 v244, v244, v203, v204\n\t" \
  "v_max3_u32 v244, v244, v205, v206\n\t" \
  "v_smfmac_f32_16x16x64_bf16 v[196:199], v[204:207], v[224:231], v251\n\t" \
  "v_max_u32 v244, v244, v207\n\t" \
  "v_cmp_le_u32 vcc, %[sent], v244\n\t" \
  "s_cbranch_vccnz .Lslow%=\n\t" \
  ".Lok%=:\n\t" \
  "ds_read_b128 v[216:219], v250 offset:32768\n\t" \
  "ds_read_b128 v[220:223], v250 offset:33792\n\t" \
  "ds_read_b128 v[224:227], v250 offset:34816\n\t" \
  "ds_read_b128 v[228:231], v250 offset:35840\n\t" \
  "s_waitcnt lgkmcnt(4)\n\t" \
  "s_barrier\n\t" \
  "ds_read_b128 v[208:211], v248 offset:5120\n\t" \
  "ds_read_b128 v[212:215], v248 offset:6144\n\t" \
  "ds_read_b128 v[236:239], v248 offset:7168\n\t" \
  "ds_read_b128 v[240:243], v248 offset:8192\n\t" \
  "s_waitcnt lgkmcnt(3)\n\t" \
  "ds_read_b128 v[244:247], v248 offset:13312\n\t" \
  "v_smfmac_f32_16x16x64_bf16 v[192:195], v[208:211], v[216:223], v251\n\t" \
  "ds_read_b128 v[216:219], v250 offset:0\n\t" \
  "v_smfmac_f32_16x16x64_bf16 v[196:199], v[208:211], v[224:231], v251\n\t" \
  "s_waitcnt lgkmcnt(4)\n\t" \
  "ds_read_b128 v[220:223], v250 offset:1024\n\t" \
  "v_smfmac_f32_16x16x64_bf16 v[192:195], v[212:215], v[0:7], v251\n\t" \
  "ds_read_b128 v[224:227], v250 offset:2048\n\t" \
  "v_smfmac_f32_16x16x64_bf16 v[196:199], v[212:215], v[8:15], v251\n\t" \
  "s_waitcnt lgkmcnt(5)\n\t" \
  "ds_read_b128 v[228:231], v250 offset:3072\n\t" \
  "v_smfmac_f32_16x16x64_bf16 v[192:195], v[236:239], v[16:23], v251\n\t" \
  "ds_read_b128 v[200:203], v248 offset:9216\n\t" \
  "v_smfmac_f32_16x16x64_bf16 v[196:199], v[236:239], v[24:31], v251\n\t" \
  "s_waitcnt lgkmcnt(6)\n\t" \
  "ds_read_b128 v[204:207], v248 offset:10240\n\t" \
  "v_smfmac_f32_16x16x64_bf16 v[192:195], v[240:243], v[32:39], v251\n\t" \
  "ds_read_b128 v[208:211], v248 offset:11264\n\t" \
  "v_smfmac_f32_16x16x64_bf16 v[196:199], v[240:243], v[40:47], v251\n\t" \
  "s_waitcnt lgkmcnt(5)\n\t" \
  "ds_read_b128 v[212:215], v248 offset:14336\n\t" \
  "v_smfmac_f32_16x16x64_bf16 v[192:195], v[244:247], v[216:223], v251\n\t" \
  "s_waitcnt lgkmcnt(4)\n\t" \
  "v_smfmac_f32_16x16x64_bf16 v[196:199], v[244:247], v[224:231], v251\n\t" \
  "s_waitcnt lgkmcnt(3)\n\t" \
  "ds_read_b128 v[236:239], v248 offset:15360\n\t" \
  "v_smfmac_f32_16x16x64_bf16 v[192:195], v[200:203], v[48:55], v251\n\t" \
  "ds_read_b128 v[240:243], v248 offset:0\n\t" \
  "v_smfmac_f32_16x16x64_bf16 v[196:199], v[200:203], v[56:63], v251\n\t" \
  "s_waitcnt lgkmcnt(4)\n\t" \
  "v_smfmac_f32_16x16x64_bf16 v[192:195], v[204:207], v[64:71], v251\n\t" \
  "ds_read_b128 v[244:247], v248 offset:1024\n\t" \
  "v_smfmac_f32_16x16x64_bf16 v[196:199], v[204:207], v[72:79], v251\n\t" \
  "s_waitcnt lgkmcnt(4)\n\t" \
  "v_smfmac_f32_16x16x64_bf16 v[192:195], v[208:211], v[80:87], v251\n\t" \
  "ds_read_b128 v[200:203], v248 offset:2048\n\t" \
  "v_smfmac_f32_16x16x64_bf16 v[196:199], v[208:211], v[88:95], v251\n\t" \
  "s_waitcnt lgkmcnt(4)\n\t" \
  "v_smfmac_f32_16x16x64_bf16 v[192:195], v[212:215], v[96:103], v251\n\t" \
  "ds_read_b128 v[204:207], v248 offset:3072\n\t" \
  "v_smfmac_f32_16x16x64_bf16 v[196:199], v[212:215], v[104:111], v251\n\t" \
  "s_waitcnt lgkmcnt(3)\n\t" \
  "v_smfmac_f32_16x16x64_bf16 v[192:195], v[236:239], v[112:119], v251\n\t" \
  "v_smfmac_f32_16x16x64_bf16 v[196:199], v[236:239], v[120:127], v251\n\t" \
  "v_smfmac_f32_16x16x64_bf16 v[192:195], v[240:243], v[128:135], v251\n\t" \
  "v_smfmac_f32_16x16x64_bf16 v[196:199], v[240:243], v[136:143], v251\n\t" \
  "s_waitcnt lgkmcnt(1)\n\t" \
  "v_smfmac_f32_16x16x64_bf16 v[192:195], v[244:247], v[144:151], v251\n\t" \
  "v_smfmac_f32_16x16x64_bf16 v[196:199], v[244:247], v[152:159], v251\n\t" \
  "v_smfmac_f32_16x16x64_bf16 v[192:195], v[200:203], v[160:167], v251\n\t" \
  "v_smfmac_f32_16x16x64_bf16 v[196:199], v[200:203], v[168:175], v251\n\t" \
  "s_waitcnt lgkmcnt(0)\n\t" \
  "v_smfmac_f32_16x16x64_bf16 v[192:195], v[204:207], v[176:183], v251\n\t" \
  "v_smfmac_f32_16x16x64_bf16 v[196:199], v[204:207], v[184:191], v251\n\t" \
  "s_nop 6\n\t" \
  "v_add_f32 v192, v192, v194\n\t" \
  "v_add_f32 v193, v193, v195\n\t" \
  "v_add_f32 v196, v196, v198\n\t" \
  "v_add_f32 v197, v197, v199\n\t" \
  "v_cvt_pk_bf16_f32 v192, v192, v193\n\t" \
  "v_cvt_pk_bf16_f32 v196, v196, v197\n\t" \
  "ds_write2_b32 v252, v192, v196 offset1:64\n\t" \
  "s_waitcnt lgkmcnt(0)\n\t" \
  "s_barrier\n\t" \
  "buffer_store_dword v192, v235, %[rs], %[sk] offen sc1\n\t" \
  "buffer_store_dword v196, v235, %[rs], %[sk] offen offset:256 sc1\n\t" \
  "s_mov_b32 %[spins], s92\n\t" \
  "s_branch .Lend%=\n\t" \
  ".Lslow%=:\n\t" \
  "s_mov_b64 s[94:95], 0\n\t" \
  "v_cmp_eq_u32 vcc, %[sent], v200\n\t" \
  "s_or_b64 s[94:95], s[94:95], vcc\n\t" \
  "v_cmp_eq_u32 vcc, %[sent], v201\n\t" \
  "s_or_b64 s[94:95], s[94:95], vcc\n\t" \
  "v_cmp_eq_u32 vcc, %[sent], v202\n\t" \
  "s_or_b64 s[94:95], s[94:95], vcc\n\t" \
  "v_cmp_eq_u32 vcc, %[sent], v203\n\t" \
  "s_or_b64 s[94:95], s[94:95], vcc\n\t" \
  "v_cmp_eq_u32 vcc, %[sent], v204\n\t" \
  "s_or_b64 s[94:95], s[94:95], vcc\n\t" \
  "v_cmp_eq_u32 vcc, %[sent], v205\n\t" \
  "s_or_b64 s[94:95], s[94:95], vcc\n\t" \
  "v_cmp_eq_u32 vcc, %[sent], v206\n\t" \
  "s_or_b64 s[94:95], s[94:95], vcc\n\t" \
  "v_cmp_eq_u32 vcc, %[sent], v207\n\t" \
  "s_or_b64 s[94:95], s[94:95], vcc\n\t" \
  "s_cmp_eq_u64 s[94:95], 0\n\t" \
  "s_cbranch_scc1 .Lok%=\n\t" \
  "v_mov_b64 v[192:193], 0\n\t" \
  "v_mov_b64 v[194:195], 0\n\t" \
  "v_mov_b64 v[196:197], 0\n\t" \
  "v_mov_b64 v[198:199], 0\n\t" \
  "s_add_u32 s92, s92, 1\n\t" \
  "s_cmp_eq_u32 s92, 1\n\t" \
  "s_cbranch_scc1 .Lpoll%=\n\t" \
  "s_sleep 2\n\t" \
  "s_and_b32 s93, s92, 63\n\t" \
  "s_cmp_lg_u32 s93, 0\n\t" \
  "s_cbranch_scc1 .Lpoll%=\n\t" \
  "v_mov_b32 v245, 0\n\t" \
  "buffer_load_dword v244, v245, %[rs], %[sab] offen sc1\n\t" \
  "s_waitcnt vmcnt(0)\n\t" \
  "v_readfirstlane_b32 s93, v244\n\t" \
  "s_nop 3\n\t" \
  "s_cmp_eq_u32 s93, 1\n\t" \
  "s_cbranch_scc1 .Lok%=\n\t" \
  "s_cmp_lt_u32 s92, 0x80000\n\t" \
  "s_cbranch_scc1 .Lpoll%=\n\t" \
  "v_mov_b32 v244, 1\n\t" \
  "buffer_store_dword v244, v245, %[rs], %[sab] offen sc1\n\t" \
  "s_branch .Lok%=\n\t" \
  ".Lend%=:\n\t"
#define QK_ASM_5 \
  "s_waitcnt lgkmcnt(0)\n\t" \
  "v_subrev_u32 v244, 0x8000, v250\n\t" \
  "ds_read_b128 v[216:219], v248 offset:53248\n\t" \
  "ds_read_b128 v[220:223], v248 offset:54272\n\t" \
  "ds_read_b128 v[224:227], v248 offset:55296\n\t" \
  "ds_read_b128 v[228:231], v248 offset:56320\n\t" \
  "ds_read_b128 v[208:211], v244 offset:0\n\t" \
  "ds_read_b128 v[212:215], v244 offset:1024\n\t" \
  "ds_read_b128 v[236:239], v244 offset:2048\n\t" \
  "ds_read_b128 v[240:243], v244 offset:3072\n\t" \
  "v_mov_b64 v[192:193], 0\n\t" \
  "v_mov_b64 v[194:195], 0\n\t" \
  "v_mov_b64 v[196:197], 0\n\t" \
  "v_mov_b64 v[198:199], 0\n\t" \
  "s_movk_i32 s92, 0x2000\n\t" \
  ".Lwt%=:\n\t" \
  "ds_read_b64 v[200:201], v248 offset:30720\n\t" \
  "s_memrealtime s[90:91]\n\t" \
  "s_sub_u32 s92, s92, 1\n\t" \
  "s_waitcnt lgkmcnt(0)\n\t" \
  "v_readfirstlane_b32 s93, v200\n\t" \
  "v_readfirstlane_b32 %[tgt], v201\n\t" \
  "s_cbranch_scc1 .Lgo%=\n\t" \
  "s_cmp_eq_u32 s93, %[step]\n\t" \
  "s_cbranch_scc1 .Lwx%=\n\t" \
  "s_sleep 1\n\t" \
  "s_branch .Lwt%=\n\t" \
  ".Lwx%=:\n\t" \
  "s_lshl_b32 s93, s90, 2\n\t" \
  "s_sub_i32 s93, %[tgt], s93\n\t" \
  "s_cmp_le_i32 s93, 0\n\t" \
  "s_cbranch_scc1 .Lgo%=\n\t" \
  "s_min_i32 s93, s93, 8000\n\t" \
  "s_mov_b32 s94, s93\n\t" \
  ".Lsl%=:\n\t" \
  "s_sub_i32 s94, s94, 18\n\t" \
  "s_cmp_lt_i32 s94, 30\n\t" \
  "s_cbranch_scc1 .Lsx%=\n\t" \
  "s_sleep 1\n\t" \
  "s_branch .Lsl%=\n\t" \
  ".Lsx%=:\n\t" \
  "s_sub_i32 s93, s93, 12\n\t" \
  "s_max_i32 s93, s93, 4\n\t" \
  "s_lshr_b32 s93, s93, 2\n\t" \
  "s_add_u32 s93, s93, s90\n\t" \
  "s_memrealtime s[90:91]\n\t" \
  ".Lpace%=:\n\t" \
  "s_memrealtime s[94:95]\n\t" \
  "s_waitcnt lgkmcnt(1)\n\t" \
  "s_sub_i32 s92, s90, s93\n\t" \
  "s_cmp_ge_i32 s92, 0\n\t" \
  "s_cbranch_scc1 .Le%=\n\t" \
  "s_memrealtime s[90:91]\n\t" \
  "s_waitcnt lgkmcnt(1)\n\t" \
  "s_sub_i32 s92, s94, s93\n\t" \
  "s_cmp_lt_i32 s92, 0\n\t" \
  "s_cbranch_scc1 .Lpace%=\n\t" \
  "s_nop 4\n\t" \
  ".Le%=:\n\t" \
  "s_add_u32 s92, s92, s93\n\t" \
  "s_lshl_b32 s92, s92, 2\n\t" \
  "s_sub_i32 s92, %[tgt], s92\n\t" \
  "s_max_i32 s92, s92, 0\n\t" \
  "s_bitcmp0_b32 s92, 3\n\t" \
  "s_cbranch_scc1 .Lf3%=\n\t" \
  "s_nop 15\n\t" \
  ".Lf3%=:\n\t" \
  "s_bitcmp0_b32 s92, 2\n\t" \
  "s_cbranch_scc1 .Lf2%=\n\t" \
  "s_nop 10\n\t" \
  ".Lf2%=:\n\t" \
  ".Lgo%=:\n\t" \
  "s_mov_b32 s92, 0\n\t" \
  ".Lpoll%=:\n\t" \
  "buffer_load_dwordx4 v[200:203], v248, %[rs], %[som] offen sc1\n\t" \
  "buffer_load_dwordx4 v[204:207], v248, %[rs], %[som2] offen sc1\n\t" \
  "s_waitcnt vmcnt(1) lgkmcnt(0)\n\t" \
  "ds_write_b128 v249, v[200:203]\n\t" \
  "v_smfmac_f32_16x16x64_bf16 v[192:195], v[200:203], v[208:215], v251\n\t" \
  "v_max3_u32 v244, v200, v201, v202\n\t" \
  "v_smfmac_f32_16x16x64_bf16 v[196:199], v[200:203], v[236:243], v251\n\t" \
  "s_waitcnt vmcnt(0)\n\t" \
  "ds_write_b128 v249, v[204:207] offset:8192\n\t" \
  "v_smfmac_f32_16x16x64_bf16 v[192:195], v[204:207], v[216:223], v251\n\t" \
  "v_max3_u32 v244, v244, v203, v204\n\t" \
  "v_max3_u32 v244, v244, v205, v206\n\t" \
  "v_smfmac_f32_16x16x64_bf16 v[196:199], v[204:207], v[224:231], v251\n\t" \
  "v_max_u32 v244, v244, v207\n\t" \
  "v_cmp_le_u32 vcc, %[sent], v244\n\t" \
  "s_cbranch_vccnz .Lslow%=\n\t" \
  ".Lok%=:\n\t" \
  "ds_read_b128 v[216:219], v250 offset:32768\n\t" \
  "ds_read_b128 v[220:223], v250 offset:33792\n\t" \
  "ds_read_b128 v[224:227], v250 offset:34816\n\t" \
  "ds_read_b128 v[228:231], v250 offset:35840\n\t" \
  "s_waitcnt lgkmcnt(4)\n\t" \
  "s_barrier\n\t" \
  "ds_read_b128 v[208:211], v248 offset:4096\n\t" \
  "ds_read_b128 v[212:215], v248 offset:7168\n\t" \
  "ds_read_b128 v[236:239], v248 offset:8192\n\t" \
  "ds_read_b128 v[240:243], v248 offset:9216\n\t" \
  "s_waitcnt lgkmcnt(3)\n\t" \
  "ds_read_b128 v[244:247], v248 offset:12288\n\t" \
  "v_smfmac_f32_16x16x64_bf16 v[192:195], v[208:211], v[216:223], v251\n\t" \
  "ds_read_b128 v[216:219], v250 offset:0\n\t" \
  "v_smfmac_f32_16x16x64_bf16 v[196:199], v[208:211], v[224:231], v251\n\t" \
  "s_waitcnt lgkmcnt(4)\n\t" \
  "ds_read_b128 v[220:223], v250 offset:1024\n\t" \
  "v_smfmac_f32_16x16x64_bf16 v[192:195], v[212:215], v[0:7], v251\n\t" \
  "ds_read_b128 v[224:227], v250 offset:2048\n\t" \
  "v_smfmac_f32_16x16x64_bf16 v[196:199], v[212:215], v[8:15], v251\n\t" \
  "s_waitcnt lgkmcnt(5)\n\t" \
  "ds_read_b128 v[228:231], v250 offset:3072\n\t" \
  "v_smfmac_f32_16x16x64_bf16 v[192:195], v[236:239], v[16:23], v251\n\t" \
  "ds_read_b128 v[200:203], v248 offset:10240\n\t" \
  "v_smfmac_f32_16x16x64_bf16 v[196:199], v[236:239], v[24:31], v251\n\t" \
  "s_waitcnt lgkmcnt(6)\n\t" \
  "ds_read_b128 v[204:207], v248 offset:11264\n\t" \
  "v_smfmac_f32_16x16x64_bf16 v[192:195], v[240:243], v[32:39], v251\n\t" \
  "ds_read_b128 v[208:211], v248 offset:14336\n\t" \
  "v_smfmac_f32_16x16x64_bf16 v[196:199], v[240:243], v[40:47], v251\n\t" \
  "s_waitcnt lgkmcnt(5)\n\t" \
  "ds_read_b128 v[212:215], v248 offset:15360\n\t" \
  "v_smfmac_f32_16x16x64_bf16 v[192:195], v[244:247], v[216:223], v251\n\t" \
  "s_waitcnt lgkmcnt(4)\n\t" \
  "v_smfmac_f32_16x16x64_bf16 v[196:199], v[244:247], v[224:231], v251\n\t" \
  "s_waitcnt lgkmcnt(3)\n\t" \
  "ds_read_b128 v[236:239], v248 offset:0\n\t" \
  "v_smfmac_f32_16x16x64_bf16 v[192:195], v[200:203], v[48:55], v251\n\t" \
  "ds_read_b128 v[240:243], v248 offset:1024\n\t" \
  "v_smfmac_f32_16x16x64_bf16 v[196:199], v[200:203], v[56:63], v251\n\t" \
  "s_waitcnt lgkmcnt(4)\n\t" \
  "v_smfmac_f32_16x16x64_bf16 v[192:195], v[204:207], v[64:71], v251\n\t" \
  "ds_read_b128 v[244:247], v248 offset:2048\n\t" \
  "v_smfmac_f32_16x16x64_bf16 v[196:199], v[204:207], v[72:79], v251\n\t" \
  "s_waitcnt lgkmcnt(4)\n\t" \
  "v_smfmac_f32_16x16x64_bf16 v[192:195], v[208:211], v[80:87], v251\n\t" \
  "ds_read_b128 v[200:203], v248 offset:3072\n\t" \
  "v_smfmac_f32_16x16x64_bf16 v[196:199], v[208:211], v[88:95], v251\n\t" \
  "s_waitcnt lgkmcnt(4)\n\t" \
  "v_smfmac_f32_16x16x64_bf16 v[192:195], v[212:215], v[96:103], v251\n\t" \
  "ds_read_b128 v[204:207], v248 offset:6144\n\t" \
  "v_smfmac_f32_16x16x64_bf16 v[196:199], v[212:215], v[104:111], v251\n\t" \
  "s_waitcnt lgkmcnt(3)\n\t" \
  "v_smfmac_f32_16x16x64_bf16 v[192:195], v[236:239], v[112:119], v251\n\t" \
  "v_smfmac_f32_16x16x64_bf16 v[196:199], v[236:239], v[120:127], v251\n\t" \
  "v_smfmac_f32_16x16x64_bf16 v[192:195], v[240:243], v[128:135], v251\n\t" \
  "v_smfmac_f32_16x16x64_bf16 v[196:199], v[240:243], v[136:143], v251\n\t" \
  "s_waitcnt lgkmcnt(1)\n\t" \
  "v_smfmac_f32_16x16x64_bf16 v[192:195], v[244:247], v[144:151], v251\n\t" \
  "v_smfmac_f32_16x16x64_bf16 v[196:199], v[244:247], v[152:159], v251\n\t" \
  "v_smfmac_f32_16x16x64_bf16 v[192:195], v[200:203], v[160:167], v251\n\t" \
  "v_smfmac_f32_16x16x64_bf16 v[196:199], v[200:203], v[168:175], v251\n\t" \
  "s_waitcnt lgkmcnt(0)\n\t" \
  "v_smfmac_f32_16x16x64_bf16 v[192:195], v[204:207], v[176:183], v251\n\t" \
  "v_smfmac_f32_16x16x64_bf16 v[196:199], v[204:207], v[184:191], v251\n\t" \
  "s_nop 6\n\t" \
  "v_add_f32 v192, v192, v194\n\t" \
  "v_add_f32 v193, v193, v195\n\t" \
  "v_add_f32 v196, v196, v198\n\t" \
  "v_add_f32 v197, v197, v199\n\t" \
  "v_cvt_pk_bf16_f32 v192, v192, v193\n\t" \
  "v_cvt_pk_bf16_f32 v196, v196, v197\n\t" \
  "ds_write2_b32 v252, v192, v196 offset1:64\n\t" \
  "s_waitcnt lgkmcnt(0)\n\t" \
  "s_barrier\n\t" \
  "buffer_store_dword v192, v235, %[rs], %[sk] offen sc1\n\t" \
  "buffer_store_dword v196, v235, %[rs], %[sk] offen offset:256 sc1\n\t" \
  "s_mov_b32 %[spins], s92\n\t" \
  "s_branch .Lend%=\n\t" \
  ".Lslow%=:\n\t" \
  "s_mov_b64 s[94:95], 0\n\t" \
  "v_cmp_eq_u32 vcc, %[sent], v200\n\t" \
  "s_or_b64 s[94:95], s[94:95], vcc\n\t" \
  "v_cmp_eq_u32 vcc, %[sent], v201\n\t" \
  "s_or_b64 s[94:95], s[94:95], vcc\n\t" \
  "v_cmp_eq_u32 vcc, %[sent], v202\n\t" \
  "s_or_b64 s[94:95], s[94:95], vcc\n\t" \
  "v_cmp_eq_u32 vcc, %[sent], v203\n\t" \
  "s_or_b64 s[94:95], s[94:95], vcc\n\t" \
  "v_cmp_eq_u32 vcc, %[sent], v204\n\t" \
  "s_or_b64 s[94:95], s[94:95], vcc\n\t" \
  "v_cmp_eq_u32 vcc, %[sent], v205\n\t" \
  "s_or_b64 s[94:95], s[94:95], vcc\n\t" \
  "v_cmp_eq_u32 vcc, %[sent], v206\n\t" \
  "s_or_b64 s[94:95], s[94:95], vcc\n\t" \
  "v_cmp_eq_u32 vcc, %[sent], v207\n\t" \
  "s_or_b64 s[94:95], s[94:95], vcc\n\t" \
  "s_cmp_eq_u64 s[94:95], 0\n\t" \
  "s_cbranch_scc1 .Lok%=\n\t" \
  "v_mov_b64 v[192:193], 0\n\t" \
  "v_mov_b64 v[194:195], 0\n\t" \
  "v_mov_b64 v[196:197], 0\n\t" \
  "v_mov_b64 v[198:199], 0\n\t" \
  "s_add_u32 s92, s92, 1\n\t" \
  "s_cmp_eq_u32 s92, 1\n\t" \
  "s_cbranch_scc1 .Lpoll%=\n\t" \
  "s_sleep 2\n\t" \
  "s_and_b32 s93, s92, 63\n\t" \
  "s_cmp_lg_u32 s93, 0\n\t" \
  "s_cbranch_scc1 .Lpoll%=\n\t" \
  "v_mov_b32 v245, 0\n\t" \
  "buffer_load_dword v244, v245, %[rs], %[sab] offen sc1\n\t" \
  "s_waitcnt vmcnt(0)\n\t" \
  "v_readfirstlane_b32 s93, v244\n\t" \
  "s_nop 3\n\t" \
  "s_cmp_eq_u32 s93, 1\n\t" \
  "s_cbranch_scc1 .Lok%=\n\t" \
  "s_cmp_lt_u32 s92, 0x80000\n\t" \
  "s_cbranch_scc1 .Lpoll%=\n\t" \
  "v_mov_b32 v244, 1\n\t" \
  "buffer_store_dword v244, v245, %[rs], %[sab] offen sc1\n\t" \
  "s_branch .Lok%=\n\t" \
  ".Lend%=:\n\t"
#define QK_ASM_6 \
  "s_waitcnt lgkmcnt(0)\n\t" \
  "v_subrev_u32 v244, 0x8000, v250\n\t" \
  "ds_read_b128 v[216:219], v248 offset:57344\n\t" \
  "ds_read_b128 v[220:223], v248 offset:58368\n\t" \
  "ds_read_b128 v[224:227], v248 offset:59392\n\t" \
  "ds_read_b128 v[228:231], v248 offset:60416\n\t" \
  "ds_read_b128 v[208:211], v244 offset:0\n\t" \
  "ds_read_b128 v[212:215], v244 offset:1024\n\t" \
  "ds_read_b128 v[236:239], v244 offset:2048\n\t" \
  "ds_read_b128 v[240:243], v244 offset:3072\n\t" \
  "v_mov_b64 v[192:193], 0\n\t" \
  "v_mov_b64 v[194:195], 0\n\t" \
  "v_mov_b64 v[196:197], 0\n\t" \
  "v_mov_b64 v[198:199], 0\n\t" \
  "s_movk_i32 s92, 0x2000\n\t" \
  ".Lwt%=:\n\t" \
  "ds_read_b64 v[200:201], v248 offset:30720\n\t" \
  "s_memrealtime s[90:91]\n\t" \
  "s_sub_u32 s92, s92, 1\n\t" \
  "s_waitcnt lgkmcnt(0)\n\t" \
  "v_readfirstlane_b32 s93, v200\n\t" \
  "v_readfirstlane_b32 %[tgt], v201\n\t" \
  "s_cbranch_scc1 .Lgo%=\n\t" \
  "s_cmp_eq_u32 s93, %[step]\n\t" \
  "s_cbranch_scc1 .Lwx%=\n\t" \
  "s_sleep 1\n\t" \
  "s_branch .Lwt%=\n\t" \
  ".Lwx%=:\n\t" \
  "s_lshl_b32 s93, s90, 2\n\t" \
  "s_sub_i32 s93, %[tgt], s93\n\t" \
  "s_cmp_le_i32 s93, 0\n\t" \
  "s_cbranch_scc1 .Lgo%=\n\t" \
  "s_min_i32 s93, s93, 8000\n\t" \
  "s_mov_b32 s94, s93\n\t" \
  ".Lsl%=:\n\t" \
  "s_sub_i32 s94, s94, 18\n\t" \
  "s_cmp_lt_i32 s94, 30\n\t" \
  "s_cbranch_scc1 .Lsx%=\n\t" \
  "s_sleep 1\n\t" \
  "s_branch .Lsl%=\n\t" \
  ".Lsx%=:\n\t" \
  "s_sub_i32 s93, s93, 12\n\t" \
  "s_max_i32 s93, s93, 4\n\t" \
  "s_lshr_b32 s93, s93, 2\n\t" \
  "s_add_u32 s93, s93, s90\n\t" \
  "s_memrealtime s[90:91]\n\t" \
  ".Lpace%=:\n\t" \
  "s_memrealtime s[94:95]\n\t" \
  "s_waitcnt lgkmcnt(1)\n\t" \
  "s_sub_i32 s92, s90, s93\n\t" \
  "s_cmp_ge_i32 s92, 0\n\t" \
  "s_cbranch_scc1 .Le%=\n\t" \
  "s_memrealtime s[90:91]\n\t" \
  "s_waitcnt lgkmcnt(1)\n\t" \
  "s_sub_i32 s92, s94, s93\n\t" \
  "s_cmp_lt_i32 s92, 0\n\t" \
  "s_cbranch_scc1 .Lpace%=\n\t" \
  "s_nop 4\n\t" \
  ".Le%=:\n\t" \
  "s_add_u32 s92, s92, s93\n\t" \
  "s_lshl_b32 s92, s92, 2\n\t" \
  "s_sub_i32 s92, %[tgt], s92\n\t" \
  "s_max_i32 s92, s92, 0\n\t" \
  "s_bitcmp0_b32 s92, 3\n\t" \
  "s_cbranch_scc1 .Lf3%=\n\t" \
  "s_nop 15\n\t" \
  ".Lf3%=:\n\t" \
  "s_bitcmp0_b32 s92, 2\n\t" \
  "s_cbranch_scc1 .Lf2%=\n\t" \
  "s_nop 10\n\t" \
  ".Lf2%=:\n\t" \
  ".Lgo%=:\n\t" \
  "s_mov_b32 s92, 0\n\t" \
  ".Lpoll%=:\n\t" \
  "buffer_load_dwordx4 v[200:203], v248, %[rs], %[som] offen sc1\n\t" \
  "buffer_load_dwordx4 v[204:207], v248, %[rs], %[som2] offen sc1\n\t" \
  "s_waitcnt vmcnt(1) lgkmcnt(0)\n\t" \
  "ds_write_b128 v249, v[200:203]\n\t" \
  "v_smfmac_f32_16x16x64_bf16 v[192:195], v[200:203], v[208:215], v251\n\t" \
  "v_max3_u32 v244, v200, v201, v202\n\t" \
  "v_smfmac_f32_16x16x64_bf16 v[196:199], v[200:203], v[236:243], v251\n\t" \
  "s_waitcnt vmcnt(0)\n\t" \
  "ds_write_b128 v249, v[204:207] offset:8192\n\t" \
  "v_smfmac_f32_16x16x64_bf16 v[192:195], v[204:207], v[216:223], v251\n\t" \
  "v_max3_u32 v244, v244, v203, v204\n\t" \
  "v_max3_u32 v244, v244, v205, v206\n\t" \
  "v_smfmac_f32_16x16x64_bf16 v[196:199], v[204:207], v[224:231], v251\n\t" \
  "v_max_u32 v244, v244, v207\n\t" \
  "v_cmp_le_u32 vcc, %[sent], v244\n\t" \
  "s_cbranch_vccnz .Lslow%=\n\t" \
  ".Lok%=:\n\t" \
  "ds_read_b128 v[216:219], v250 offset:32768\n\t" \
  "ds_read_b128 v[220:223], v250 offset:33792\n\t" \
  "ds_read_b128 v[224:227], v250 offset:34816\n\t" \
  "ds_read_b128 v[228:231], v250 offset:35840\n\t" \
  "s_waitcnt lgkmcnt(4)\n\t" \
  "s_barrier\n\t" \
  "ds_read_b128 v[208:211], v248 offset:7168\n\t" \
  "ds_read_b128 v[212:215], v248 offset:8192\n\t" \
  "ds_read_b128 v[236:239], v248 offset:9216\n\t" \
  "ds_read_b128 v[240:243], v248 offset:10240\n\t" \
  "s_waitcnt lgkmcnt(3)\n\t" \
  "ds_read_b128 v[244:247], v248 offset:13312\n\t" \
  "v_smfmac_f32_16x16x64_bf16 v[192:195], v[208:211], v[216:223], v251\n\t" \
  "ds_read_b128 v[216:219], v250 offset:0\n\t" \
  "v_smfmac_f32_16x16x64_bf16 v[196:199], v[208:211], v[224:231], v251\n\t" \
  "s_waitcnt lgkmcnt(4)\n\t" \
  "ds_read_b128 v[220:223], v250 offset:1024\n\t" \
  "v_smfmac_f32_16x16x64_bf16 v[192:195], v[212:215], v[0:7], v251\n\t" \
  "ds_read_b128 v[224:227], v250 offset:2048\n\t" \
  "v_smfmac_f32_16x16x64_bf16 v[196:199], v[212:215], v[8:15], v251\n\t" \
  "s_waitcnt lgkmcnt(5)\n\t" \
  "ds_read_b128 v[228:231], v250 offset:3072\n\t" \
  "v_smfmac_f32_16x16x64_bf16 v[192:195], v[236:239], v[16:23], v251\n\t" \
  "ds_read_b128 v[200:203], v248 offset:11264\n\t" \
  "v_smfmac_f32_16x16x64_bf16 v[196:199], v[236:239], v[24:31], v251\n\t" \
  "s_waitcnt lgkmcnt(6)\n\t" \
  "ds_read_b128 v[204:207], v248 offset:12288\n\t" \
  "v_smfmac_f32_16x16x64_bf16 v[192:195], v[240:243], v[32:39], v251\n\t" \
  "ds_read_b128 v[208:211], v248 offset:15360\n\t" \
  "v_smfmac_f32_16x16x64_bf16 v[196:199], v[240:243], v[40:47], v251\n\t" \
  "s_waitcnt lgkmcnt(5)\n\t" \
  "ds_read_b128 v[212:215], v248 offset:0\n\t" \
  "v_smfmac_f32_16x16x64_bf16 v[192:195], v[244:247], v[216:223], v251\n\t" \
  "s_waitcnt lgkmcnt(4)\n\t" \
  "v_smfmac_f32_16x16x64_bf16 v[196:199], v[244:247], v[224:231], v251\n\t" \
  "s_waitcnt lgkmcnt(3)\n\t" \
  "ds_read_b128 v[236:239], v248 offset:1024\n\t" \
  "v_smfmac_f32_16x16x64_bf16 v[192:195], v[200:203], v[48:55], v251\n\t" \
  "ds_read_b128 v[240:243], v248 offset:2048\n\t" \
  "v_smfmac_f32_16x16x64_bf16 v[196:199], v[200:203], v[56:63], v251\n\t" \
  "s_waitcnt lgkmcnt(4)\n\t" \
  "v_smfmac_f32_16x16x64_bf16 v[192:195], v[204:207], v[64:71], v251\n\t" \
  "ds_read_b128 v[244:247], v248 offset:3072\n\t" \
  "v_smfmac_f32_16x16x64_bf16 v[196:199], v[204:207], v[72:79], v251\n\t" \
  "s_waitcnt lgkmcnt(4)\n\t" \
  "v_smfmac_f32_16x16x64_bf16 v[192:195], v[208:211], v[80:87], v251\n\t" \
  "ds_read_b128 v[200:203], v248 offset:4096\n\t" \
  "v_smfmac_f32_16x16x64_bf16 v[196:199], v[208:211], v[88:95], v251\n\t" \
  "s_waitcnt lgkmcnt(4)\n\t" \
  "v_smfmac_f32_16x16x64_bf16 v[192:195], v[212:215], v[96:103], v251\n\t" \
  "ds_read_b128 v[204:207], v248 offset:5120\n\t" \
  "v_smfmac_f32_16x16x64_bf16 v[196:199], v[212:215], v[104:111], v251\n\t" \
  "s_waitcnt lgkmcnt(3)\n\t" \
  "v_smfmac_f32_16x16x64_bf16 v[192:195], v[236:239], v[112:119], v251\n\t" \
  "v_smfmac_f32_16x16x64_bf16 v[196:199], v[236:239], v[120:127], v251\n\t" \
  "v_smfmac_f32_16x16x64_bf16 v[192:195], v[240:243], v[128:135], v251\n\t" \
  "v_smfmac_f32_16x16x64_bf16 v[196:199], v[240:243], v[136:143], v251\n\t" \
  "s_waitcnt lgkmcnt(1)\n\t" \
  "v_smfmac_f32_16x16x64_bf16 v[192:195], v[244:247], v[144:151], v251\n\t" \
  "v_smfmac_f32_16x16x64_bf16 v[196:199], v[244:247], v[152:159], v251\n\t" \
  "v_smfmac_f32_16x16x64_bf16 v[192:195], v[200:203], v[160:167], v251\n\t" \
  "v_smfmac_f32_16x16x64_bf16 v[196:199], v[200:203], v[168:175], v251\n\t" \
  "s_waitcnt lgkmcnt(0)\n\t" \
  "v_smfmac_f32_16x16x64_bf16 v[192:195], v[204:207], v[176:183], v251\n\t" \
  "v_smfmac_f32_16x16x64_bf16 v[196:199], v[204:207], v[184:191], v251\n\t" \
  "s_nop 6\n\t" \
  "v_add_f32 v192, v192, v194\n\t" \
  "v_add_f32 v193, v193, v195\n\t" \
  "v_add_f32 v196, v196, v198\n\t" \
  "v_add_f32 v197, v197, v199\n\t" \
  "v_cvt_pk_bf16_f32 v192, v192, v193\n\t" \
  "v_cvt_pk_bf16_f32 v196, v196, v197\n\t" \
  "ds_write2_b32 v252, v192, v196 offset1:64\n\t" \
  "s_waitcnt lgkmcnt(0)\n\t" \
  "s_barrier\n\t" \
  "buffer_store_dword v192, v235, %[rs], %[sk] offen sc1\n\t" \
  "buffer_store_dword v196, v235, %[rs], %[sk] offen offset:256 sc1\n\t" \
  "s_mov_b32 %[spins], s92\n\t" \
  "s_branch .Lend%=\n\t" \
  ".Lslow%=:\n\t" \
  "s_mov_b64 s[94:95], 0\n\t" \
  "v_cmp_eq_u32 vcc, %[sent], v200\n\t" \
  "s_or_b64 s[94:95], s[94:95], vcc\n\t" \
  "v_cmp_eq_u32 vcc, %[sent], v201\n\t" \
  "s_or_b64 s[94:95], s[94:95], vcc\n\t" \
  "v_cmp_eq_u32 vcc, %[sent], v202\n\t" \
  "s_or_b64 s[94:95], s[94:95], vcc\n\t" \
  "v_cmp_eq_u32 vcc, %[sent], v203\n\t" \
  "s_or_b64 s[94:95], s[94:95], vcc\n\t" \
  "v_cmp_eq_u32 vcc, %[sent], v204\n\t" \
  "s_or_b64 s[94:95], s[94:95], vcc\n\t" \
  "v_cmp_eq_u32 vcc, %[sent], v205\n\t" \
  "s_or_b64 s[94:95], s[94:95], vcc\n\t" \
  "v_cmp_eq_u32 vcc, %[sent], v206\n\t" \
  "s_or_b64 s[94:95], s[94:95], vcc\n\t" \
  "v_cmp_eq_u32 vcc, %[sent], v207\n\t" \
  "s_or_b64 s[94:95], s[94:95], vcc\n\t" \
  "s_cmp_eq_u64 s[94:95], 0\n\t" \
  "s_cbranch_scc1 .Lok%=\n\t" \
  "v_mov_b64 v[192:193], 0\n\t" \
  "v_mov_b64 v[194:195], 0\n\t" \
  "v_mov_b64 v[196:197], 0\n\t" \
  "v_mov_b64 v[198:199], 0\n\t" \
  "s_add_u32 s92, s92, 1\n\t" \
  "s_cmp_eq_u32 s92, 1\n\t" \
  "s_cbranch_scc1 .Lpoll%=\n\t" \
  "s_sleep 2\n\t" \
  "s_and_b32 s93, s92, 63\n\t" \
  "s_cmp_lg_u32 s93, 0\n\t" \
  "s_cbranch_scc1 .Lpoll%=\n\t" \
  "v_mov_b32 v245, 0\n\t" \
  "buffer_load_dword v244, v245, %[rs], %[sab] offen sc1\n\t" \
  "s_waitcnt vmcnt(0)\n\t" \
  "v_readfirstlane_b32 s93, v244\n\t" \
  "s_nop 3\n\t" \
  "s_cmp_eq_u32 s93, 1\n\t" \
  "s_cbranch_scc1 .Lok%=\n\t" \
  "s_cmp_lt_u32 s92, 0x80000\n\t" \
  "s_cbranch_scc1 .Lpoll%=\n\t" \
  "v_mov_b32 v244, 1\n\t" \
  "buffer_store_dword v244, v245, %[rs], %[sab] offen sc1\n\t" \
  "s_branch .Lok%=\n\t" \
  ".Lend%=:\n\t"
#define QK_ASM_7 \
  "s_waitcnt lgkmcnt(0)\n\t" \
  "v_subrev_u32 v244, 0x8000, v250\n\t" \
  "ds_read_b128 v[216:219], v248 offset:61440\n\t" \
  "ds_read_b128 v[220:223], v248 offset:62464\n\t" \
  "ds_read_b128 v[224:227], v248 offset:63488\n\t" \
  "ds_read_b128 v[228:231], v248 offset:64512\n\t" \
  "ds_read_b128 v[208:211], v244 offset:0\n\t" \
  "ds_read_b128 v[212:215], v244 offset:1024\n\t" \
  "ds_read_b128 v[236:239], v244 offset:2048\n\t" \
  "ds_read_b128 v[240:243], v244 offset:3072\n\t" \
  "v_mov_b64 v[192:193], 0\n\t" \
  "v_mov_b64 v[194:195], 0\n\t" \
  "v_mov_b64 v[196:197], 0\n\t" \
  "v_mov_b64 v[198:199], 0\n\t" \
  "s_movk_i32 s92, 0x2000\n\t" \
  ".Lwt%=:\n\t" \
  "ds_read_b64 v[200:201], v248 offset:30720\n\t" \
  "s_memrealtime s[90:91]\n\t" \
  "s_sub_u32 s92, s92, 1\n\t" \
  "s_waitcnt lgkmcnt(0)\n\t" \
  "v_readfirstlane_b32 s93, v200\n\t" \
  "v_readfirstlane_b32 %[tgt], v201\n\t" \
  "s_cbranch_scc1 .Lgo%=\n\t" \
  "s_cmp_eq_u32 s93, %[step]\n\t" \
  "s_cbranch_scc1 .Lwx%=\n\t" \
  "s_sleep 1\n\t" \
  "s_branch .Lwt%=\n\t" \
  ".Lwx%=:\n\t" \
  "s_lshl_b32 s93, s90, 2\n\t" \
  "s_sub_i32 s93, %[tgt], s93\n\t" \
  "s_cmp_le_i32 s93, 0\n\t" \
  "s_cbranch_scc1 .Lgo%=\n\t" \
  "s_min_i32 s93, s93, 8000\n\t" \
  "s_mov_b32 s94, s93\n\t" \
  ".Lsl%=:\n\t" \
  "s_sub_i32 s94, s94, 18\n\t" \
  "s_cmp_lt_i32 s94, 30\n\t" \
  "s_cbranch_scc1 .Lsx%=\n\t" \
  "s_sleep 1\n\t" \
  "s_branch .Lsl%=\n\t" \
  ".Lsx%=:\n\t" \
  "s_sub_i32 s93, s93, 12\n\t" \
  "s_max_i32 s93, s93, 4\n\t" \
  "s_lshr_b32 s93, s93, 2\n\t" \
  "s_add_u32 s93, s93, s90\n\t" \
  "s_memrealtime s[90:91]\n\t" \
  ".Lpace%=:\n\t" \
  "s_memrealtime s[94:95]\n\t" \
  "s_waitcnt lgkmcnt(1)\n\t" \
  "s_sub_i32 s92, s90, s93\n\t" \
  "s_cmp_ge_i32 s92, 0\n\t" \
  "s_cbranch_scc1 .Le%=\n\t" \
  "s_memrealtime s[90:91]\n\t" \
  "s_waitcnt lgkmcnt(1)\n\t" \
  "s_sub_i32 s92, s94, s93\n\t" \
  "s_cmp_lt_i32 s92, 0\n\t" \
  "s_cbranch_scc1 .Lpace%=\n\t" \
  "s_nop 4\n\t" \
  ".Le%=:\n\t" \
  "s_add_u32 s92, s92, s93\n\t" \
  "s_lshl_b32 s92, s92, 2\n\t" \
  "s_sub_i32 s92, %[tgt], s92\n\t" \
  "s_max_i32 s92, s92, 0\n\t" \
  "s_bitcmp0_b32 s92, 3\n\t" \
  "s_cbranch_scc1 .Lf3%=\n\t" \
  "s_nop 15\n\t" \
  ".Lf3%=:\n\t" \
  "s_bitcmp0_b32 s92, 2\n\t" \
  "s_cbranch_scc1 .Lf2%=\n\t" \
  "s_nop 10\n\t" \
  ".Lf2%=:\n\t" \
  ".Lgo%=:\n\t" \
  "s_mov_b32 s92, 0\n\t" \
  "s_memtime s[88:89]\n\t" \
  ".Lpoll%=:\n\t" \
  "buffer_load_dwordx4 v[200:203], v248, %[rs], %[som] offen sc1\n\t" \
  "buffer_load_dwordx4 v[204:207], v248, %[rs], %[som2] offen sc1\n\t" \
  "s_waitcnt vmcnt(1) lgkmcnt(0)\n\t" \
  "ds_write_b128 v249, v[200:203]\n\t" \
  "v_smfmac_f32_16x16x64_bf16 v[192:195], v[200:203], v[208:215], v251\n\t" \
  "v_max3_u32 v244, v200, v201, v202\n\t" \
  "v_smfmac_f32_16x16x64_bf16 v[196:199], v[200:203], v[236:243], v251\n\t" \
  "s_waitcnt vmcnt(0)\n\t" \
  "ds_write_b128 v249, v[204:207] offset:8192\n\t" \
  "v_smfmac_f32_16x16x64_bf16 v[192:195], v[204:207], v[216:223], v251\n\t" \
  "v_max3_u32 v244, v244, v203, v204\n\t" \
  "v_max3_u32 v244, v244, v205, v206\n\t" \
  "v_smfmac_f32_16x16x64_bf16 v[196:199], v[204:207], v[224:231], v251\n\t" \
  "v_max_u32 v244, v244, v207\n\t" \
  "v_cmp_le_u32 vcc, %[sent], v244\n\t" \
  "s_cbranch_vccnz .Lslow%=\n\t" \
  ".Lok%=:\n\t" \
  "ds_read_b128 v[216:219], v250 offset:32768\n\t" \
  "ds_read_b128 v[220:223], v250 offset:33792\n\t" \
  "ds_read_b128 v[224:227], v250 offset:34816\n\t" \
  "ds_read_b128 v[228:231], v250 offset:35840\n\t" \
  "s_waitcnt lgkmcnt(4)\n\t" \
  "s_setprio 3\n\t" \
  "s_barrier\n\t" \
  "s_memrealtime s[90:91]\n\t" \
  "s_memtime s[94:95]\n\t" \
  "v_mov_b32 v232, %[step]\n\t" \
  "v_lshrrev_b32 v234, 1, v248\n\t" \
  "s_mov_b64 exec, 0xffff\n\t" \
  "s_waitcnt lgkmcnt(0)\n\t" \
  "s_lshl_b32 s90, s90, 2\n\t" \
  "s_sub_u32 s94, s94, s88\n\t" \
  "s_mul_i32 s94, s94, 12200\n\t" \
  "s_lshr_b32 s94, s94, 16\n\t" \
  "s_add_u32 s94, s94, %[tgt]\n\t" \
  "s_sub_i32 s93, s90, s94\n\t" \
  "s_sub_i32 s93, s93, 30\n\t" \
  "s_max_i32 s93, s93, 0\n\t" \
  "s_add_u32 s90, s94, s93\n\t" \
  "s_sub_i32 s93, s90, %[tgt]\n\t" \
  "s_cmp_gt_i32 s93, 270\n\t" \
  "s_cselect_b32 s93, 70, 0\n\t" \
  "s_add_u32 s93, s93, s90\n\t" \
  "s_add_u32 s93, s93, %[lead]\n\t" \
  "v_mov_b32 v233, s93\n\t" \
  "buffer_store_dwordx2 v[232:233], v234, %[rs], %[sbc] offen sc1\n\t" \
  "s_mov_b64 exec, -1\n\t" \
  "s_mov_b32 %[tb1], s90\n\t" \
  "s_setprio 0\n\t" \
  "ds_read_b128 v[208:211], v248 offset:6144\n\t" \
  "ds_read_b128 v[212:215], v248 offset:9216\n\t" \
  "ds_read_b128 v[236:239], v248 offset:10240\n\t" \
  "ds_read_b128 v[240:243], v248 offset:11264\n\t" \
  "s_waitcnt lgkmcnt(3)\n\t" \
  "ds_read_b128 v[244:247], v248 offset:12288\n\t" \
  "v_smfmac_f32_16x16x64_bf16 v[192:195], v[208:211], v[216:223], v251\n\t" \
  "ds_read_b128 v[216:219], v250 offset:0\n\t" \
  "v_smfmac_f32_16x16x64_bf16 v[196:199], v[208:211], v[224:231], v251\n\t" \
  "s_waitcnt lgkmcnt(4)\n\t" \
  "ds_read_b128 v[220:223], v250 offset:1024\n\t" \
  "v_smfmac_f32_16x16x64_bf16 v[192:195], v[212:215], v[0:7], v251\n\t" \
  "ds_read_b128 v[224:227], v250 offset:2048\n\t" \
  "v_smfmac_f32_16x16x64_bf16 v[196:199], v[212:215], v[8:15], v251\n\t" \
  "s_waitcnt lgkmcnt(5)\n\t" \
  "ds_read_b128 v[228:231], v250 offset:3072\n\t" \
  "v_smfmac_f32_16x16x64_bf16 v[192:195], v[236:239], v[16:23], v251\n\t" \
  "ds_read_b128 v[200:203], v248 offset:13312\n\t" \
  "v_smfmac_f32_16x16x64_bf16 v[196:199], v[236:239], v[24:31], v251\n\t" \
  "s_waitcnt lgkmcnt(6)\n\t" \
  "ds_read_b128 v[204:207], v248 offset:14336\n\t" \
  "v_smfmac_f32_16x16x64_bf16 v[192:195], v[240:243], v[32:39], v251\n\t" \
  "ds_read_b128 v[208:211], v248 offset:0\n\t" \
  "v_smfmac_f32_16x16x64_bf16 v[196:199], v[240:243], v[40:47], v251\n\t" \
  "s_waitcnt lgkmcnt(5)\n\t" \
  "ds_read_b128 v[212:215], v248 offset:1024\n\t" \
  "v_smfmac_f32_16x16x64_bf16 v[192:195], v[244:247], v[216:223], v251\n\t" \
  "s_waitcnt lgkmcnt(4)\n\t" \
  "v_smfmac_f32_16x16x64_bf16 v[196:199], v[244:247], v[224:231], v251\n\t" \
  "s_waitcnt lgkmcnt(3)\n\t" \
  "ds_read_b128 v[236:239], v248 offset:2048\n\t" \
  "v_smfmac_f32_16x16x64_bf16 v[192:195], v[200:203], v[48:55], v251\n\t" \
  "ds_read_b128 v[240:243], v248 offset:3072\n\t" \
  "v_smfmac_f32_16x16x64_bf16 v[196:199], v[200:203], v[56:63], v251\n\t" \
  "s_waitcnt lgkmcnt(4)\n\t" \
  "v_smfmac_f32_16x16x64_bf16 v[192:195], v[204:207], v[64:71], v251\n\t" \
  "ds_read_b128 v[244:247], v248 offset:4096\n\t" \
  "v_smfmac_f32_16x16x64_bf16 v[196:199], v[204:207], v[72:79], v251\n\t" \
  "s_waitcnt lgkmcnt(4)\n\t" \
  "v_smfmac_f32_16x16x64_bf16 v[192:195], v[208:211], v[80:87], v251\n\t" \
  "ds_read_b128 v[200:203], v248 offset:5120\n\t" \
  "v_smfmac_f32_16x16x64_bf16 v[196:199], v[208:211], v[88:95], v251\n\t" \
  "s_waitcnt lgkmcnt(4)\n\t" \
  "v_smfmac_f32_16x16x64_bf16 v[192:195], v[212:215], v[96:103], v251\n\t" \
  "ds_read_b128 v[204:207], v248 offset:8192\n\t" \
  "v_smfmac_f32_16x16x64_bf16 v[196:199], v[212:215], v[104:111], v251\n\t" \
  "s_waitcnt lgkmcnt(3)\n\t" \
  "v_smfmac_f32_16x16x64_bf16 v[192:195], v[236:239], v[112:119], v251\n\t" \
  "v_smfmac_f32_16x16x64_bf16 v[196:199], v[236:239], v[120:127], v251\n\t" \
  "v_smfmac_f32_16x16x64_bf16 v[192:195], v[240:243], v[128:135], v251\n\t" \
  "v_smfmac_f32_16x16x64_bf16 v[196:199], v[240:243], v[136:143], v251\n\t" \
  "s_waitcnt lgkmcnt(1)\n\t" \
  "v_smfmac_f32_16x16x64_bf16 v[192:195], v[244:247], v[144:151], v251\n\t" \
  "v_smfmac_f32_16x16x64_bf16 v[196:199], v[244:247], v[152:159], v251\n\t" \
  "v_smfmac_f32_16x16x64_bf16 v[192:195], v[200:203], v[160:167], v251\n\t" \
  "v_smfmac_f32_16x16x64_bf16 v[196:199], v[200:203], v[168:175], v251\n\t" \
  "s_waitcnt lgkmcnt(0)\n\t" \
  "v_smfmac_f32_16x16x64_bf16 v[192:195], v[204:207], v[176:183], v251\n\t" \
  "v_smfmac_f32_16x16x64_bf16 v[196:199], v[204:207], v[184:191], v251\n\t" \
  "s_nop 6\n\t" \
  "v_add_f32 v192, v192, v194\n\t" \
  "v_add_f32 v193, v193, v195\n\t" \
  "v_add_f32 v196, v196, v198\n\t" \
  "v_add_f32 v197, v197, v199\n\t" \
  "v_cvt_pk_bf16_f32 v192, v192, v193\n\t" \
  "v_cvt_pk_bf16_f32 v196, v196, v197\n\t" \
  "ds_write2_b32 v252, v192, v196 offset1:64\n\t" \
  "s_waitcnt lgkmcnt(0)\n\t" \
  "s_barrier\n\t" \
  "buffer_store_dword v192, v235, %[rs], %[sk] offen sc1\n\t" \
  "buffer_store_dword v196, v235, %[rs], %[sk] offen offset:256 sc1\n\t" \
  "s_mov_b32 %[spins], s92\n\t" \
  "s_branch .Lend%=\n\t" \
  ".Lslow%=:\n\t" \
  "s_mov_b64 s[94:95], 0\n\t" \
  "v_cmp_eq_u32 vcc, %[sent], v200\n\t" \
  "s_or_b64 s[94:95], s[94:95], vcc\n\t" \
  "v_cmp_eq_u32 vcc, %[sent], v201\n\t" \
  "s_or_b64 s[94:95], s[94:95], vcc\n\t" \
  "v_cmp_eq_u32 vcc, %[sent], v202\n\t" \
  "s_or_b64 s[94:95], s[94:95], vcc\n\t" \
  "v_cmp_eq_u32 vcc, %[sent], v203\n\t" \
  "s_or_b64 s[94:95], s[94:95], vcc\n\t" \
  "v_cmp_eq_u32 vcc, %[sent], v204\n\t" \
  "s_or_b64 s[94:95], s[94:95], vcc\n\t" \
  "v_cmp_eq_u32 vcc, %[sent], v205\n\t" \
  "s_or_b64 s[94:95], s[94:95], vcc\n\t" \
  "v_cmp_eq_u32 vcc, %[sent], v206\n\t" \
  "s_or_b64 s[94:95], s[94:95], vcc\n\t" \
  "v_cmp_eq_u32 vcc, %[sent], v207\n\t" \
  "s_or_b64 s[94:95], s[94:95], vcc\n\t" \
  "s_cmp_eq_u64 s[94:95], 0\n\t" \
  "s_cbranch_scc1 .Lok%=\n\t" \
  "v_mov_b64 v[192:193], 0\n\t" \
  "v_mov_b64 v[194:195], 0\n\t" \
  "v_mov_b64 v[196:197], 0\n\t" \
  "v_mov_b64 v[198:199], 0\n\t" \
  "s_add_u32 s92, s92, 1\n\t" \
  "s_cmp_eq_u32 s92, 1\n\t" \
  "s_cbranch_scc1 .Lpoll%=\n\t" \
  "s_sleep 2\n\t" \
  "s_and_b32 s93, s92, 63\n\t" \
  "s_cmp_lg_u32 s93, 0\n\t" \
  "s_cbranch_scc1 .Lpoll%=\n\t" \
  "v_mov_b32 v245, 0\n\t" \
  "buffer_load_dword v244, v245, %[rs], %[sab] offen sc1\n\t" \
  "s_waitcnt vmcnt(0)\n\t" \
  "v_readfirstlane_b32 s93, v244\n\t" \
  "s_nop 3\n\t" \
  "s_cmp_eq_u32 s93, 1\n\t" \
  "s_cbranch_scc1 .Lok%=\n\t" \
  "s_cmp_lt_u32 s92, 0x80000\n\t" \
  "s_cbranch_scc1 .Lpoll%=\n\t" \
  "v_mov_b32 v244, 1\n\t" \
  "buffer_store_dword v244, v245, %[rs], %[sab] offen sc1\n\t" \
  "s_branch .Lok%=\n\t" \
  ".Lend%=:\n\t"
#define VV_ASM \
  "s_waitcnt lgkmcnt(0)\n\t" \
  "v_mov_b64 v[128:129], 0\n\t" \
  "v_mov_b64 v[130:131], 0\n\t" \
  "s_movk_i32 s92, 0x2000\n\t" \
  ".Lwt%=:\n\t" \
  "ds_read_b64 v[132:133], v200 offset:30720\n\t" \
  "s_memrealtime s[90:91]\n\t" \
  "s_sub_u32 s92, s92, 1\n\t" \
  "s_waitcnt lgkmcnt(0)\n\t" \
  "v_readfirstlane_b32 s93, v132\n\t" \
  "v_readfirstlane_b32 %[tgt], v133\n\t" \
  "s_cbranch_scc1 .Lgo%=\n\t" \
  "s_cmp_eq_u32 s93, %[step]\n\t" \
  "s_cbranch_scc1 .Lwx%=\n\t" \
  "s_sleep 1\n\t" \
  "s_branch .Lwt%=\n\t" \
  ".Lwx%=:\n\t" \
  "s_lshl_b32 s93, s90, 2\n\t" \
  "s_sub_i32 s93, %[tgt], s93\n\t" \
  "s_cmp_le_i32 s93, 0\n\t" \
  "s_cbranch_scc1 .Lgo%=\n\t" \
  "s_min_i32 s93, s93, 8000\n\t" \
  "s_mov_b32 s94, s93\n\t" \
  ".Lsl%=:\n\t" \
  "s_sub_i32 s94, s94, 18\n\t" \
  "s_cmp_lt_i32 s94, 30\n\t" \
  "s_cbranch_scc1 .Lsx%=\n\t" \
  "s_sleep 1\n\t" \
  "s_branch .Lsl%=\n\t" \
  ".Lsx%=:\n\t" \
  "s_sub_i32 s93, s93, 12\n\t" \
  "s_max_i32 s93, s93, 4\n\t" \
  "s_lshr_b32 s93, s93, 2\n\t" \
  "s_add_u32 s93, s93, s90\n\t" \
  "s_memrealtime s[90:91]\n\t" \
  ".Lpace%=:\n\t" \
  "s_memrealtime s[94:95]\n\t" \
  "s_waitcnt lgkmcnt(1)\n\t" \
  "s_sub_i32 s92, s90, s93\n\t" \
  "s_cmp_ge_i32 s92, 0\n\t" \
  "s_cbranch_scc1 .Le%=\n\t" \
  "s_memrealtime s[90:91]\n\t" \
  "s_waitcnt lgkmcnt(1)\n\t" \
  "s_sub_i32 s92, s94, s93\n\t" \
  "s_cmp_lt_i32 s92, 0\n\t" \
  "s_cbranch_scc1 .Lpace%=\n\t" \
  "s_nop 4\n\t" \
  ".Le%=:\n\t" \
  "s_add_u32 s92, s92, s93\n\t" \
  "s_lshl_b32 s92, s92, 2\n\t" \
  "s_sub_i32 s92, %[tgt], s92\n\t" \
  "s_max_i32 s92, s92, 0\n\t" \
  "s_bitcmp0_b32 s92, 3\n\t" \
  "s_cbranch_scc1 .Lf3%=\n\t" \
  "s_nop 15\n\t" \
  ".Lf3%=:\n\t" \
  "s_bitcmp0_b32 s92, 2\n\t" \
  "s_cbranch_scc1 .Lf2%=\n\t" \
  "s_nop 10\n\t" \
  ".Lf2%=:\n\t" \
  ".Lgo%=:\n\t" \
  "s_mov_b32 s92, 0\n\t" \
  ".Lpoll%=:\n\t" \
  "buffer_load_dwordx4 v[132:135], v200, %[rs], %[som] offen sc1\n\t" \
  "buffer_load_dwordx4 v[136:139], v200, %[rs], %[som2] offen sc1\n\t" \
  "s_waitcnt vmcnt(1) lgkmcnt(0)\n\t" \
  "ds_write_b128 v232, v[132:135]\n\t" \
  "v_smfmac_f32_16x16x64_bf16 v[128:131], v[132:135], v[0:7], v202\n\t" \
  "v_max3_u32 v196, v132, v133, v134\n\t" \
  "s_waitcnt vmcnt(0)\n\t" \
  "ds_write_b128 v232, v[136:139] offset:8192\n\t" \
  "v_smfmac_f32_16x16x64_bf16 v[128:131], v[136:139], v[64:71], v202\n\t" \
  "v_max3_u32 v196, v196, v135, v136\n\t" \
  "v_max3_u32 v196, v196, v137, v138\n\t" \
  "v_max_u32 v196, v196, v139\n\t" \
  "v_cmp_le_u32 vcc, %[sent], v196\n\t" \
  "s_cbranch_vccnz .Lslow%=\n\t" \
  ".Lok%=:\n\t" \
  "s_waitcnt lgkmcnt(0)\n\t" \
  "s_barrier\n\t" \
  "ds_read_b128 v[164:167], v233 offset:0\n\t" \
  "ds_read_b128 v[140:143], v234 offset:0\n\t" \
  "ds_read_b128 v[144:147], v235 offset:0\n\t" \
  "ds_read_b128 v[148:151], v236 offset:0\n\t" \
  "ds_read_b128 v[152:155], v237 offset:0\n\t" \
  "s_waitcnt lgkmcnt(4)\n\t" \
  "ds_read_b128 v[156:159], v238 offset:0\n\t" \
  "v_smfmac_f32_16x16x64_bf16 v[128:131], v[164:167], v[8:15], v202\n\t" \
  "s_waitcnt lgkmcnt(4)\n\t" \
  "ds_read_b128 v[160:163], v239 offset:0\n\t" \
  "v_smfmac_f32_16x16x64_bf16 v[128:131], v[140:143], v[16:23], v202\n\t" \
  "s_waitcnt lgkmcnt(4)\n\t" \
  "ds_read_b128 v[168:171], v233 offset:8192\n\t" \
  "v_smfmac_f32_16x16x64_bf16 v[128:131], v[144:147], v[24:31], v202\n\t" \
  "s_waitcnt lgkmcnt(4)\n\t" \
  "ds_read_b128 v[172:175], v234 offset:8192\n\t" \
  "v_smfmac_f32_16x16x64_bf16 v[128:131], v[148:151], v[32:39], v202\n\t" \
  "s_waitcnt lgkmcnt(4)\n\t" \
  "ds_read_b128 v[176:179], v235 offset:8192\n\t" \
  "v_smfmac_f32_16x16x64_bf16 v[128:131], v[152:155], v[40:47], v202\n\t" \
  "s_waitcnt lgkmcnt(4)\n\t" \
  "ds_read_b128 v[180:183], v236 offset:8192\n\t" \
  "v_smfmac_f32_16x16x64_bf16 v[128:131], v[156:159], v[48:55], v202\n\t" \
  "s_waitcnt lgkmcnt(4)\n\t" \
  "ds_read_b128 v[184:187], v237 offset:8192\n\t" \
  "v_smfmac_f32_16x16x64_bf16 v[128:131], v[160:163], v[56:63], v202\n\t" \
  "s_waitcnt lgkmcnt(4)\n\t" \
  "ds_read_b128 v[188:191], v238 offset:8192\n\t" \
  "v_smfmac_f32_16x16x64_bf16 v[128:131], v[168:171], v[72:79], v202\n\t" \
  "s_waitcnt lgkmcnt(4)\n\t" \
  "ds_read_b128 v[192:195], v239 offset:8192\n\t" \
  "v_smfmac_f32_16x16x64_bf16 v[128:131], v[172:175], v[80:87], v202\n\t" \
  "s_waitcnt lgkmcnt(4)\n\t" \
  "v_smfmac_f32_16x16x64_bf16 v[128:131], v[176:179], v[88:95], v202\n\t" \
  "s_waitcnt lgkmcnt(3)\n\t" \
  "v_smfmac_f32_16x16x64_bf16 v[128:131], v[180:183], v[96:103], v202\n\t" \
  "s_waitcnt lgkmcnt(2)\n\t" \
  "v_smfmac_f32_16x16x64_bf16 v[128:131], v[184:187], v[104:111], v202\n\t" \
  "s_waitcnt lgkmcnt(1)\n\t" \
  "v_smfmac_f32_16x16x64_bf16 v[128:131], v[188:191], v[112:119], v202\n\t" \
  "s_waitcnt lgkmcnt(0)\n\t" \
  "v_smfmac_f32_16x16x64_bf16 v[128:131], v[192:195], v[120:127], v202\n\t" \
  "s_nop 7\n\t" \
  "v_add_f32 v128, v128, v130\n\t" \
  "v_add_f32 v129, v129, v131\n\t" \
  "v_cvt_pk_bf16_f32 v128, v128, v129\n\t" \
  "buffer_store_dword v128, v203, %[rs], %[sv] offen sc1\n\t" \
  "ds_write_b32 v204, v128\n\t" \
  "s_waitcnt lgkmcnt(0)\n\t" \
  "s_barrier\n\t" \
  "s_mov_b32 %[spins], s92\n\t" \
  "s_branch .Lend%=\n\t" \
  ".Lslow%=:\n\t" \
  "s_mov_b64 s[94:95], 0\n\t" \
  "v_cmp_eq_u32 vcc, %[sent], v132\n\t" \
  "s_or_b64 s[94:95], s[94:95], vcc\n\t" \
  "v_cmp_eq_u32 vcc, %[sent], v133\n\t" \
  "s_or_b64 s[94:95], s[94:95], vcc\n\t" \
  "v_cmp_eq_u32 vcc, %[sent], v134\n\t" \
  "s_or_b64 s[94:95], s[94:95], vcc\n\t" \
  "v_cmp_eq_u32 vcc, %[sent], v135\n\t" \
  "s_or_b64 s[94:95], s[94:95], vcc\n\t" \
  "v_cmp_eq_u32 vcc, %[sent], v136\n\t" \
  "s_or_b64 s[94:95], s[94:95], vcc\n\t" \
  "v_cmp_eq_u32 vcc, %[sent], v137\n\t" \
  "s_or_b64 s[94:95], s[94:95], vcc\n\t" \
  "v_cmp_eq_u32 vcc, %[sent], v138\n\t" \
  "s_or_b64 s[94:95], s[94:95], vcc\n\t" \
  "v_cmp_eq_u32 vcc, %[sent], v139\n\t" \
  "s_or_b64 s[94:95], s[94:95], vcc\n\t" \
  "s_cmp_eq_u64 s[94:95], 0\n\t" \
  "s_cbranch_scc1 .Lok%=\n\t" \
  "v_mov_b64 v[128:129], 0\n\t" \
  "v_mov_b64 v[130:131], 0\n\t" \
  "s_add_u32 s92, s92, 1\n\t" \
  "s_cmp_eq_u32 s92, 1\n\t" \
  "s_cbranch_scc1 .Lpoll%=\n\t" \
  "s_sleep 2\n\t" \
  "s_and_b32 s93, s92, 63\n\t" \
  "s_cmp_lg_u32 s93, 0\n\t" \
  "s_cbranch_scc1 .Lpoll%=\n\t" \
  "v_mov_b32 v197, 0\n\t" \
  "buffer_load_dword v196, v197, %[rs], %[sab] offen sc1\n\t" \
  "s_waitcnt vmcnt(0)\n\t" \
  "v_readfirstlane_b32 s93, v196\n\t" \
  "s_nop 3\n\t" \
  "s_cmp_eq_u32 s93, 1\n\t" \
  "s_cbranch_scc1 .Lok%=\n\t" \
  "s_cmp_lt_u32 s92, 0x80000\n\t" \
  "s_cbranch_scc1 .Lpoll%=\n\t" \
  "v_mov_b32 v196, 1\n\t" \
  "buffer_store_dword v196, v197, %[rs], %[sab] offen sc1\n\t" \
  "s_branch .Lok%=\n\t" \
  ".Lend%=:\n\t"
#define TK_ASM \
  "buffer_load_dwordx4 v[224:227], v200, %[rs], %[sk] offen offset:0 sc1\n\t" \
  "buffer_load_dwordx4 v[228:231], v200, %[rs], %[sk] offen offset:1024 sc1\n\t" \
  "s_memrealtime s[88:89]\n\t" \
  "v_add_u32 v205, %[tl], v200\n\t" \
  "v_add_u32 v206, %[tq], v205\n\t" \
  "v_add_u32 v207, %[tv], v205\n\t" \
  "s_waitcnt lgkmcnt(0)\n\t" \
  "ds_read_b128 v[128:131], v206 offset:8192\n\t" \
  "ds_read_b128 v[144:147], v205 offset:0\n\t" \
  "ds_read_b128 v[160:163], v205 offset:4096\n\t" \
  "ds_read_b128 v[132:135], v206 offset:9216\n\t" \
  "ds_read_b128 v[148:151], v205 offset:1024\n\t" \
  "ds_read_b128 v[164:167], v205 offset:5120\n\t" \
  "ds_read_b128 v[136:139], v206 offset:10240\n\t" \
  "ds_read_b128 v[152:155], v205 offset:2048\n\t" \
  "ds_read_b128 v[168:171], v205 offset:6144\n\t" \
  "ds_read_b128 v[140:143], v206 offset:11264\n\t" \
  "ds_read_b128 v[156:159], v205 offset:3072\n\t" \
  "ds_read_b128 v[172:175], v205 offset:7168\n\t" \
  "v_add_u32 v206, %[vp], v218\n\t" \
  "s_lshl_b32 s88, s88, 2\n\t" \
  "s_add_u32 s89, s88, %[od]\n\t" \
  "s_waitcnt lgkmcnt(10)\n\t" \
  "v_mfma_f32_16x16x32_bf16 v[196:199], v[144:147], v[128:131], 0\n\t" \
  "s_waitcnt lgkmcnt(9)\n\t" \
  "v_mfma_f32_16x16x32_bf16 v[144:147], v[160:163], v[128:131], 0\n\t" \
  "s_waitcnt lgkmcnt(7)\n\t" \
  "v_mfma_f32_16x16x32_bf16 v[196:199], v[148:151], v[132:135], v[196:199]\n\t" \
  "s_waitcnt lgkmcnt(6)\n\t" \
  "v_mfma_f32_16x16x32_bf16 v[144:147], v[164:167], v[132:135], v[144:147]\n\t" \
  "s_waitcnt lgkmcnt(4)\n\t" \
  "v_mfma_f32_16x16x32_bf16 v[196:199], v[152:155], v[136:139], v[196:199]\n\t" \
  "s_waitcnt lgkmcnt(3)\n\t" \
  "v_mfma_f32_16x16x32_bf16 v[144:147], v[168:171], v[136:139], v[144:147]\n\t" \
  "s_waitcnt lgkmcnt(1)\n\t" \
  "v_mfma_f32_16x16x32_bf16 v[196:199], v[156:159], v[140:143], v[196:199]\n\t" \
  "s_waitcnt lgkmcnt(0)\n\t" \
  "v_mfma_f32_16x16x32_bf16 v[144:147], v[172:175], v[140:143], v[144:147]\n\t" \
  "ds_read_b128 v[176:179], v207 offset:16384\n\t" \
  "ds_read_b128 v[180:183], v207 offset:17408\n\t" \
  "ds_read_b128 v[184:187], v207 offset:18432\n\t" \
  "ds_read_b128 v[188:191], v207 offset:19456\n\t" \
  "ds_read2st64_b32 v[192:193], v206 offset0:0 offset1:1\n\t" \
  "ds_read2st64_b32 v[194:195], v206 offset0:2 offset1:3\n\t" \
  "v_cvt_pk_bf16_f32 v197, v196, v197\n\t" \
  "v_cvt_pk_bf16_f32 v199, v198, v199\n\t" \
  "v_cvt_pk_bf16_f32 v145, v144, v145\n\t" \
  "v_cvt_pk_bf16_f32 v147, v146, v147\n\t" \
  "v_lshlrev_b32 v196, 16, v197\n\t" \
  "v_lshlrev_b32 v198, 16, v199\n\t" \
  "v_lshlrev_b32 v144, 16, v145\n\t" \
  "v_lshlrev_b32 v146, 16, v147\n\t" \
  "v_and_b32 v197, 0xffff0000, v197\n\t" \
  "v_and_b32 v199, 0xffff0000, v199\n\t" \
  "v_and_b32 v145, 0xffff0000, v145\n\t" \
  "v_and_b32 v147, 0xffff0000, v147\n\t" \
  "v_pk_mul_f32 v[196:197], v[196:197], %[sc] op_sel_hi:[1,0]\n\t" \
  "v_pk_mul_f32 v[198:199], v[198:199], %[sc] op_sel_hi:[1,0]\n\t" \
  "v_pk_mul_f32 v[144:145], v[144:145], %[sc] op_sel_hi:[1,0]\n\t" \
  "v_pk_mul_f32 v[146:147], v[146:147], %[sc] op_sel_hi:[1,0]\n\t" \
  "v_cvt_pk_bf16_f32 v197, v196, v197\n\t" \
  "v_cvt_pk_bf16_f32 v199, v198, v199\n\t" \
  "v_cvt_pk_bf16_f32 v145, v144, v145\n\t" \
  "v_cvt_pk_bf16_f32 v147, v146, v147\n\t" \
  "v_lshlrev_b32 v196, 16, v197\n\t" \
  "v_lshlrev_b32 v198, 16, v199\n\t" \
  "v_lshlrev_b32 v144, 16, v145\n\t" \
  "v_lshlrev_b32 v146, 16, v147\n\t" \
  "v_and_b32 v197, 0xffff0000, v197\n\t" \
  "v_and_b32 v199, 0xffff0000, v199\n\t" \
  "v_and_b32 v145, 0xffff0000, v145\n\t" \
  "v_and_b32 v147, 0xffff0000, v147\n\t" \
  "v_min_f32 v196, v196, v208\n\t" \
  "v_min_f32 v197, v197, v209\n\t" \
  "v_min_f32 v198, v198, v210\n\t" \
  "v_min_f32 v199, v199, v211\n\t" \
  "v_min_f32 v144, v144, v212\n\t" \
  "v_min_f32 v145, v145, v213\n\t" \
  "v_min_f32 v146, v146, v214\n\t" \
  "v_min_f32 v147, v147, v215\n\t" \
  "v_max3_f32 v148, v196, v197, v198\n\t" \
  "v_max3_f32 v149, v199, v144, v145\n\t" \
  "v_max3_f32 v148, v148, v146, v147\n\t" \
  "v_max_f32 v150, v148, v149\n\t" \
  "v_max_f32 v151, v148, v149\n\t" \
  "s_nop 1\n\t" \
  "v_permlane32_swap_b32 v150, v151\n\t" \
  "v_max_f32 v150, v150, v151\n\t" \
  "s_mov_b32 s92, 0\n\t" \
  ".Ltake%=:\n\t" \
  "s_waitcnt vmcnt(1) lgkmcnt(0)\n\t" \
  "ds_write_b128 v216, v[224:227]\n\t" \
  "v_max3_u32 v148, v224, v225, v226\n\t" \
  "s_waitcnt vmcnt(0)\n\t" \
  "ds_write_b128 v216, v[228:231] offset:1024\n\t" \
  "ds_read_b64_tr_b16 v[160:161], v217 offset:0\n\t" \
  "ds_read_b64_tr_b16 v[162:163], v217 offset:64\n\t" \
  "ds_read_b64_tr_b16 v[164:165], v217 offset:512\n\t" \
  "ds_read_b64_tr_b16 v[166:167], v217 offset:576\n\t" \
  "ds_read_b64_tr_b16 v[168:169], v217 offset:1024\n\t" \
  "ds_read_b64_tr_b16 v[170:171], v217 offset:1088\n\t" \
  "ds_read_b64_tr_b16 v[172:173], v217 offset:1536\n\t" \
  "ds_read_b64_tr_b16 v[174:175], v217 offset:1600\n\t" \
  "v_max3_u32 v148, v148, v227, v228\n\t" \
  "v_max3_u32 v148, v148, v229, v230\n\t" \
  "v_max_u32 v148, v148, v231\n\t" \
  "v_cmp_le_u32 vcc, %[sent], v148\n\t" \
  "s_cbranch_vccnz .Lslow%=\n\t" \
  ".Lok%=:\n\t" \
  "s_waitcnt lgkmcnt(6)\n\t" \
  "v_mfma_f32_16x16x32_bf16 v[152:155], v[160:163], v[128:131], 0\n\t" \
  "s_waitcnt lgkmcnt(4)\n\t" \
  "v_mfma_f32_16x16x32_bf16 v[152:155], v[164:167], v[132:135], v[152:155]\n\t" \
  "s_waitcnt lgkmcnt(2)\n\t" \
  "v_mfma_f32_16x16x32_bf16 v[152:155], v[168:171], v[136:139], v[152:155]\n\t" \
  "s_waitcnt lgkmcnt(0)\n\t" \
  "v_mfma_f32_16x16x32_bf16 v[152:155], v[172:175], v[140:143], v[152:155]\n\t" \
  "v_mov_b32 v157, 0\n\t" \
  "v_mov_b32 v158, 0\n\t" \
  "s_nop 5\n\t" \
  "v_cvt_pk_bf16_f32 v153, v152, v153\n\t" \
  "v_cvt_pk_bf16_f32 v155, v154, v155\n\t" \
  "v_lshlrev_b32 v152, 16, v153\n\t" \
  "v_lshlrev_b32 v154, 16, v155\n\t" \
  "v_and_b32 v153, 0xffff0000, v153\n\t" \
  "v_and_b32 v155, 0xffff0000, v155\n\t" \
  "v_pk_mul_f32 v[152:153], v[152:153], %[sc] op_sel_hi:[1,0]\n\t" \
  "v_pk_mul_f32 v[154:155], v[154:155], %[sc] op_sel_hi:[1,0]\n\t" \
  "v_cvt_pk_bf16_f32 v153, v152, v153\n\t" \
  "v_cvt_pk_bf16_f32 v155, v154, v155\n\t" \
  "v_lshlrev_b32 v152, 16, v153\n\t" \
  "v_lshlrev_b32 v154, 16, v155\n\t" \
  "v_and_b32 v153, 0xffff0000, v153\n\t" \
  "v_and_b32 v155, 0xffff0000, v155\n\t" \
  "v_max3_f32 v148, v152, v153, v150\n\t" \
  "v_max3_f32 v149, v148, v154, v155\n\t" \
  "v_max3_f32 v150, v148, v154, v155\n\t" \
  "v_cndmask_b32 v152, v152, v154, %[up]\n\t" \
  "v_cndmask_b32 v153, v153, v155, %[up]\n\t" \
  "v_permlane16_swap_b32 v149, v150\n\t" \
  "v_max_f32 v148, v149, v150\n\t" \
  "v_mul_f32 v148, %[negc], v148\n\t" \
  "v_fma_f32 v152, v152, %[c], v148\n\t" \
  "v_fma_f32 v153, v153, %[c], v148\n\t" \
  "v_exp_f32 v152, v152\n\t" \
  "v_fma_f32 v196, v196, %[c], v148\n\t" \
  "v_exp_f32 v153, v153\n\t" \
  "v_fma_f32 v197, v197, %[c], v148\n\t" \
  "v_exp_f32 v196, v196\n\t" \
  "v_fma_f32 v198, v198, %[c], v148\n\t" \
  "v_exp_f32 v197, v197\n\t" \
  "v_fma_f32 v199, v199, %[c], v148\n\t" \
  "v_exp_f32 v198, v198\n\t" \
  "v_fma_f32 v144, v144, %[c], v148\n\t" \
  "v_pk_add_f32 v[132:133], v[152:153], v[196:197]\n\t" \
  "v_exp_f32 v199, v199\n\t" \
  "v_fma_f32 v145, v145, %[c], v148\n\t" \
  "v_exp_f32 v144, v144\n\t" \
  "v_fma_f32 v146, v146, %[c], v148\n\t" \
  "v_pk_add_f32 v[132:133], v[132:133], v[198:199]\n\t" \
  "v_exp_f32 v145, v145\n\t" \
  "v_fma_f32 v147, v147, %[c], v148\n\t" \
  "v_exp_f32 v146, v146\n\t" \
  "v_pk_add_f32 v[132:133], v[132:133], v[144:145]\n\t" \
  "v_exp_f32 v147, v147\n\t" \
  "s_nop 0\n\t" \
  "v_pk_add_f32 v[132:133], v[132:133], v[146:147]\n\t" \
  "v_add_f32 v134, v132, v133\n\t" \
  "v_add_f32 v135, v132, v133\n\t" \
  "s_nop 1\n\t" \
  "v_permlane16_swap_b32 v134, v135\n\t" \
  "v_add_f32 v132, v134, v135\n\t" \
  "v_add_f32 v133, v134, v135\n\t" \
  "s_nop 1\n\t" \
  "v_permlane32_swap_b32 v132, v133\n\t" \
  "v_add_f32 v132, v132, v133\n\t" \
  "v_rcp_f32 v148, v132\n\t" \
  "s_nop 0\n\t" \
  "v_pk_mul_f32 v[152:153], v[152:153], v[148:149] op_sel_hi:[1,0]\n\t" \
  "v_pk_mul_f32 v[196:197], v[196:197], v[148:149] op_sel_hi:[1,0]\n\t" \
  "v_pk_mul_f32 v[198:199], v[198:199], v[148:149] op_sel_hi:[1,0]\n\t" \
  "v_pk_mul_f32 v[144:145], v[144:145], v[148:149] op_sel_hi:[1,0]\n\t" \
  "v_pk_mul_f32 v[146:147], v[146:147], v[148:149] op_sel_hi:[1,0]\n\t" \
  "v_cvt_pk_bf16_f32 v156, v152, v153\n\t" \
  "v_cvt_pk_bf16_f32 v159, v152, v153\n\t" \
  "v_cvt_pk_bf16_f32 v128, v196, v197\n\t" \
  "v_cvt_pk_bf16_f32 v129, v198, v199\n\t" \
  "v_mfma_f32_16x16x16_bf16 v[160:163], v[192:193], v[156:157], 0\n\t" \
  "v_cvt_pk_bf16_f32 v130, v144, v145\n\t" \
  "v_mfma_f32_16x16x16_bf16 v[164:167], v[192:193], v[158:159], 0\n\t" \
  "v_cvt_pk_bf16_f32 v131, v146, v147\n\t" \
  "v_mfma_f32_16x16x16_bf16 v[168:171], v[194:195], v[156:157], 0\n\t" \
  "v_mfma_f32_16x16x16_bf16 v[172:175], v[194:195], v[158:159], 0\n\t" \
  "s_nop 0\n\t" \
  "v_mfma_f32_16x16x32_bf16 v[160:163], v[176:179], v[128:131], v[160:163]\n\t" \
  "s_nop 0\n\t" \
  "v_mfma_f32_16x16x32_bf16 v[164:167], v[180:183], v[128:131], v[164:167]\n\t" \
  "s_nop 0\n\t" \
  "v_mfma_f32_16x16x32_bf16 v[168:171], v[184:187], v[128:131], v[168:171]\n\t" \
  "v_mfma_f32_16x16x32_bf16 v[172:175], v[188:191], v[128:131], v[172:175]\n\t" \
  "s_nop 2\n\t" \
  "v_cvt_pk_bf16_f32 v136, v160, v161\n\t" \
  "v_cvt_pk_bf16_f32 v137, v162, v163\n\t" \
  "v_cvt_pk_bf16_f32 v138, v164, v165\n\t" \
  "v_cvt_pk_bf16_f32 v139, v166, v167\n\t" \
  "v_cvt_pk_bf16_f32 v140, v168, v169\n\t" \
  "v_cvt_pk_bf16_f32 v141, v170, v171\n\t" \
  "v_cvt_pk_bf16_f32 v142, v172, v173\n\t" \
  "v_cvt_pk_bf16_f32 v143, v174, v175\n\t" \
  "s_memrealtime s[90:91]\n\t" \
  "s_waitcnt lgkmcnt(0)\n\t" \
  "s_lshl_b32 s93, s90, 2\n\t" \
  "s_sub_i32 s93, s89, s93\n\t" \
  "s_cmp_le_i32 s93, 0\n\t" \
  "s_cbranch_scc1 .Lgoo%=\n\t" \
  "s_min_i32 s93, s93, 8000\n\t" \
  "s_mov_b32 s94, s93\n\t" \
  ".Lslo%=:\n\t" \
  "s_sub_i32 s94, s94, 18\n\t" \
  "s_cmp_lt_i32 s94, 30\n\t" \
  "s_cbranch_scc1 .Lsxo%=\n\t" \
  "s_sleep 1\n\t" \
  "s_branch .Lslo%=\n\t" \
  ".Lsxo%=:\n\t" \
  "s_add_u32 s93, s93, 3\n\t" \
  "s_lshr_b32 s93, s93, 2\n\t" \
  "s_add_u32 s93, s93, s90\n\t" \
  "s_memrealtime s[90:91]\n\t" \
  ".Lpaceo%=:\n\t" \
  "s_memrealtime s[94:95]\n\t" \
  "s_waitcnt lgkmcnt(1)\n\t" \
  "s_sub_i32 s92, s90, s93\n\t" \
  "s_cmp_ge_i32 s92, 0\n\t" \
  "s_cbranch_scc1 .Leo%=\n\t" \
  "s_memrealtime s[90:91]\n\t" \
  "s_waitcnt lgkmcnt(1)\n\t" \
  "s_sub_i32 s92, s94, s93\n\t" \
  "s_cmp_lt_i32 s92, 0\n\t" \
  "s_cbranch_scc1 .Lpaceo%=\n\t" \
  ".Leo%=:\n\t" \
  ".Lgoo%=:\n\t" \
  "buffer_store_dwordx4 v[136:139], v200, %[rt], %[so] offen offset:0 sc1\n\t" \
  "buffer_store_dwordx4 v[140:143], v200, %[rt], %[so] offen offset:1024 sc1\n\t" \
  "s_waitcnt lgkmcnt(0)\n\t" \
  "s_branch .Lend%=\n\t" \
  ".Lpoll%=:\n\t" \
  "buffer_load_dwordx4 v[224:227], v200, %[rs], %[sk] offen offset:0 sc1\n\t" \
  "buffer_load_dwordx4 v[228:231], v200, %[rs], %[sk] offen offset:1024 sc1\n\t" \
  "s_branch .Ltake%=\n\t" \
  "s_branch .Lend%=\n\t" \
  ".Lslow%=:\n\t" \
  "s_mov_b64 s[94:95], 0\n\t" \
  "v_cmp_eq_u32 vcc, %[sent], v224\n\t" \
  "s_or_b64 s[94:95], s[94:95], vcc\n\t" \
  "v_cmp_eq_u32 vcc, %[sent], v225\n\t" \
  "s_or_b64 s[94:95], s[94:95], vcc\n\t" \
  "v_cmp_eq_u32 vcc, %[sent], v226\n\t" \
  "s_or_b64 s[94:95], s[94:95], vcc\n\t" \
  "v_cmp_eq_u32 vcc, %[sent], v227\n\t" \
  "s_or_b64 s[94:95], s[94:95], vcc\n\t" \
  "v_cmp_eq_u32 vcc, %[sent], v228\n\t" \
  "s_or_b64 s[94:95], s[94:95], vcc\n\t" \
  "v_cmp_eq_u32 vcc, %[sent], v229\n\t" \
  "s_or_b64 s[94:95], s[94:95], vcc\n\t" \
  "v_cmp_eq_u32 vcc, %[sent], v230\n\t" \
  "s_or_b64 s[94:95], s[94:95], vcc\n\t" \
  "v_cmp_eq_u32 vcc, %[sent], v231\n\t" \
  "s_or_b64 s[94:95], s[94:95], vcc\n\t" \
  "s_cmp_eq_u64 s[94:95], 0\n\t" \
  "s_cbranch_scc1 .Lok%=\n\t" \
  "s_add_u32 s92, s92, 1\n\t" \
  "s_cmp_eq_u32 s92, 1\n\t" \
  "s_cbranch_scc1 .Lpoll%=\n\t" \
  "s_sleep 2\n\t" \
  "s_and_b32 s93, s92, 63\n\t" \
  "s_cmp_lg_u32 s93, 0\n\t" \
  "s_cbranch_scc1 .Lpoll%=\n\t" \
  "v_mov_b32 v149, 0\n\t" \
  "buffer_load_dword v148, v149, %[rs], %[sab] offen sc1\n\t" \
  "s_waitcnt vmcnt(0)\n\t" \
  "v_readfirstlane_b32 s93, v148\n\t" \
  "s_nop 3\n\t" \
  "s_cmp_eq_u32 s93, 1\n\t" \
  "s_cbranch_scc1 .Lok%=\n\t" \
  "s_cmp_lt_u32 s92, 0x80000\n\t" \
  "s_cbranch_scc1 .Lpoll%=\n\t" \
  "v_mov_b32 v148, 1\n\t" \
  "buffer_store_dword v148, v149, %[rs], %[sab] offen sc1\n\t" \
  "s_branch .Lok%=\n\t" \
  ".Lend%=:\n\t"
constexpr int TLAG = 2;
#define RED_ASM \
  "s_mov_b32 %[step], 0\n\t" \
  "s_lshl_b32 %[sh], %[step], 15\n\t" \
  "s_lshr_b32 s93, %[step], 4\n\t" \
  "s_lshr_b32 s93, %[pm], s93\n\t" \
  "s_and_b32 s93, s93, 4\n\t" \
  "s_lshl_b32 s93, s93, 11\n\t" \
  "s_lshl2_add_u32 %[so], %[sh], s93\n\t" \
  "s_sub_i32 s93, %[step], 2\n\t" \
  "s_lshl_b32 %[st], s93, 19\n\t" \
  "s_lshl_b32 s94, s93, 11\n\t" \
  "s_xor_b32 s94, s94, %[s0]\n\t" \
  "s_and_b32 s94, s94, 0x2000\n\t" \
  "s_or_b32 %[st], %[st], s94\n\t" \
  "s_cmp_lt_i32 s93, 0\n\t" \
  "s_cselect_b32 %[st], 0x70000000, %[st]\n\t" \
  "s_add_u32 %[st4], %[st], 4096\n\t" \
  "buffer_load_dwordx2 v[166:167], v173, %[rs], 0 offen sc1\n\t" \
  "v_mov_b64 v[144:145], 0\n\t" \
  "v_mov_b64 v[146:147], 0\n\t" \
  "v_mov_b64 v[148:149], 0\n\t" \
  "v_mov_b64 v[150:151], 0\n\t" \
  "v_mov_b64 v[152:153], 0\n\t" \
  "v_mov_b64 v[154:155], 0\n\t" \
  "v_mov_b64 v[156:157], 0\n\t" \
  "v_mov_b64 v[158:159], 0\n\t" \
  ".Lstep%=:\n\t" \
  "s_setprio 3\n\t" \
  "s_mov_b32 s92, 0\n\t" \
  ".Lcal%=:\n\t" \
  "s_waitcnt vmcnt(0)\n\t" \
  "s_memrealtime s[76:77]\n\t" \
  "v_cmp_ne_u32 vcc, %[step], v166\n\t" \
  "v_readlane_b32 s90, v167, 0\n\t" \
  "v_readlane_b32 s91, v167, 1\n\t" \
  "v_readlane_b32 s94, v167, 2\n\t" \
  "v_readlane_b32 s95, v167, 3\n\t" \
  "s_sub_i32 s90, s90, %[base]\n\t" \
  "s_sub_i32 s91, s91, %[base]\n\t" \
  "s_sub_i32 s94, s94, %[base]\n\t" \
  "s_sub_i32 s95, s95, %[base]\n\t" \
  "s_max_i32 s90, s90, s91\n\t" \
  "s_max_i32 s94, s94, s95\n\t" \
  "s_max_i32 s90, s90, s94\n\t" \
  "s_add_i32 s90, s90, %[ds]\n\t" \
  "s_waitcnt lgkmcnt(0)\n\t" \
  "s_lshl2_add_u32 s76, s76, %[mn]\n\t" \
  "s_sub_i32 s76, s76, %[base]\n\t" \
  "s_add_u32 %[E], s90, %[base]\n\t" \
  "s_and_b32 s93, vcc_lo, 0xf\n\t" \
  "s_cbranch_scc1 .Lbl%=\n\t" \
  ".Lgo%=:\n\t" \
  "v_mov_b32 v166, 7\n\t" \
  "s_mov_b64 exec, 1\n\t" \
  "v_mov_b32 v164, s90\n\t" \
  "ds_max_u32 v168, v164 offset:4100\n\t" \
  "ds_add_u32 v168, v164 offset:4096\n\t" \
  "s_mov_b64 exec, -1\n\t" \
  "s_memrealtime s[90:91]\n\t" \
  "s_waitcnt lgkmcnt(0)\n\t" \
  "s_lshl_b32 s93, s90, 2\n\t" \
  "s_sub_i32 s93, %[E], s93\n\t" \
  "s_cmp_le_i32 s93, 0\n\t" \
  "s_cbranch_scc1 .Lgop%=\n\t" \
  "s_min_i32 s93, s93, 8000\n\t" \
  "s_mov_b32 s94, s93\n\t" \
  ".Lslp%=:\n\t" \
  "s_sub_i32 s94, s94, 18\n\t" \
  "s_cmp_lt_i32 s94, 30\n\t" \
  "s_cbranch_scc1 .Lsxp%=\n\t" \
  "s_sleep 1\n\t" \
  "s_branch .Lslp%=\n\t" \
  ".Lsxp%=:\n\t" \
  "s_sub_i32 s93, s93, 12\n\t" \
  "s_max_i32 s93, s93, 4\n\t" \
  "s_lshr_b32 s93, s93, 2\n\t" \
  "s_add_u32 s93, s93, s90\n\t" \
  "s_memrealtime s[90:91]\n\t" \
  ".Lpacep%=:\n\t" \
  "s_memrealtime s[94:95]\n\t" \
  "s_waitcnt lgkmcnt(1)\n\t" \
  "s_sub_i32 s92, s90, s93\n\t" \
  "s_cmp_ge_i32 s92, 0\n\t" \
  "s_cbranch_scc1 .Lep%=\n\t" \
  "s_memrealtime s[90:91]\n\t" \
  "s_waitcnt lgkmcnt(1)\n\t" \
  "s_sub_i32 s92, s94, s93\n\t" \
  "s_cmp_lt_i32 s92, 0\n\t" \
  "s_cbranch_scc1 .Lpacep%=\n\t" \
  "s_nop 4\n\t" \
  ".Lep%=:\n\t" \
  "s_add_u32 s92, s92, s93\n\t" \
  "s_lshl_b32 s92, s92, 2\n\t" \
  "s_sub_i32 s92, %[E], s92\n\t" \
  "s_max_i32 s92, s92, 0\n\t" \
  "s_bitcmp0_b32 s92, 3\n\t" \
  "s_cbranch_scc1 .Lf3p%=\n\t" \
  "s_nop 15\n\t" \
  ".Lf3p%=:\n\t" \
  "s_bitcmp0_b32 s92, 2\n\t" \
  "s_cbranch_scc1 .Lf2p%=\n\t" \
  "s_nop 10\n\t" \
  ".Lf2p%=:\n\t" \
  ".Lgop%=:\n\t" \
  "s_mov_b32 s92, 0\n\t" \
  "buffer_load_dwordx4 v[128:131], v234, %[rs], %[so] offen sc1\n\t" \
  "s_nop 15\n\t" \
  "buffer_load_dwordx4 v[132:135], v235, %[rs], %[so] offen sc1\n\t" \
  "s_nop 15\n\t" \
  "buffer_load_dwordx4 v[136:139], v236, %[rs], %[so] offen sc1\n\t" \
  "s_nop 15\n\t" \
  "buffer_load_dwordx4 v[140:143], v237, %[rs], %[so] offen sc1\n\t" \
  "s_waitcnt lgkmcnt(0)\n\t" \
  ".Lw%=:\n\t" \
  "s_waitcnt vmcnt(3)\n\t" \
  "v_smfmac_f32_16x16x64_bf16 v[144:147], v[128:131], v[0:7], v172\n\t" \
  "v_max3_u32 v164, v128, v129, 0\n\t" \
  "v_smfmac_f32_16x16x64_bf16 v[148:151], v[128:131], v[8:15], v172\n\t" \
  "v_max3_u32 v164, v130, v131, v164\n\t" \
  "v_smfmac_f32_16x16x64_bf16 v[152:155], v[128:131], v[16:23], v172\n\t" \
  "v_smfmac_f32_16x16x64_bf16 v[156:159], v[128:131], v[24:31], v172\n\t" \
  "buffer_load_dwordx4 v[176:179], v238, %[rt], %[st] offen offset:0 sc1\n\t" \
  "s_waitcnt vmcnt(3)\n\t" \
  "v_smfmac_f32_16x16x64_bf16 v[144:147], v[132:135], v[32:39], v172\n\t" \
  "v_max3_u32 v164, v132, v133, v164\n\t" \
  "v_smfmac_f32_16x16x64_bf16 v[148:151], v[132:135], v[40:47], v172\n\t" \
  "v_max3_u32 v164, v134, v135, v164\n\t" \
  "v_smfmac_f32_16x16x64_bf16 v[152:155], v[132:135], v[48:55], v172\n\t" \
  "v_smfmac_f32_16x16x64_bf16 v[156:159], v[132:135], v[56:63], v172\n\t" \
  "buffer_load_dwordx4 v[180:183], v238, %[rt], %[st] offen offset:1024 sc1\n\t" \
  "s_waitcnt vmcnt(3)\n\t" \
  "v_smfmac_f32_16x16x64_bf16 v[144:147], v[136:139], v[64:71], v172\n\t" \
  "v_max3_u32 v164, v136, v137, v164\n\t" \
  "v_smfmac_f32_16x16x64_bf16 v[148:151], v[136:139], v[72:79], v172\n\t" \
  "v_max3_u32 v164, v138, v139, v164\n\t" \
  "v_smfmac_f32_16x16x64_bf16 v[152:155], v[136:139], v[80:87], v172\n\t" \
  "v_smfmac_f32_16x16x64_bf16 v[156:159], v[136:139], v[88:95], v172\n\t" \
  "buffer_load_dwordx4 v[184:187], v238, %[rt], %[st4] offen offset:0 sc1\n\t" \
  "s_waitcnt vmcnt(3)\n\t" \
  "v_smfmac_f32_16x16x64_bf16 v[144:147], v[140:143], v[96:103], v172\n\t" \
  "v_max3_u32 v164, v140, v141, v164\n\t" \
  "v_smfmac_f32_16x16x64_bf16 v[148:151], v[140:143], v[104:111], v172\n\t" \
  "v_max3_u32 v164, v142, v143, v164\n\t" \
  "v_smfmac_f32_16x16x64_bf16 v[152:155], v[140:143], v[112:119], v172\n\t" \
  "v_cmp_le_u32 vcc, %[sent], v164\n\t" \
  "v_smfmac_f32_16x16x64_bf16 v[156:159], v[140:143], v[120:127], v172\n\t" \
  "buffer_load_dwordx4 v[188:191], v238, %[rt], %[st4] offen offset:1024 sc1\n\t" \
  "s_cbranch_vccnz .Lslow%=\n\t" \
  ".Lok%=:\n\t" \
  "v_add_f32_dpp v160, v144, v146 quad_perm:[0,1,2,3] row_mask:0x3 bank_mask:0xf\n\t" \
  "v_add_f32_dpp v161, v145, v147 quad_perm:[0,1,2,3] row_mask:0x3 bank_mask:0xf\n\t" \
  "v_add_f32_dpp v162, v148, v150 quad_perm:[0,1,2,3] row_mask:0x3 bank_mask:0xf\n\t" \
  "v_add_f32_dpp v163, v149, v151 quad_perm:[0,1,2,3] row_mask:0x3 bank_mask:0xf\n\t" \
  "v_add_f32_dpp v160, v152, v154 quad_perm:[0,1,2,3] row_mask:0xc bank_mask:0xf\n\t" \
  "v_add_f32_dpp v161, v153, v155 quad_perm:[0,1,2,3] row_mask:0xc bank_mask:0xf\n\t" \
  "v_add_f32_dpp v162, v156, v158 quad_perm:[0,1,2,3] row_mask:0xc bank_mask:0xf\n\t" \
  "v_add_f32_dpp v163, v157, v159 quad_perm:[0,1,2,3] row_mask:0xc bank_mask:0xf\n\t" \
  "s_nop 0\n\t" \
  "v_permlane32_swap_b32 v160, v162\n\t" \
  "v_permlane32_swap_b32 v161, v163\n\t" \
  "v_add_f32 v160, v160, v162\n\t" \
  "v_add_f32 v161, v161, v163\n\t" \
  "ds_write_b64 v169, v[160:161]\n\t" \
  "s_mov_b64 exec, 1\n\t" \
  "ds_inc_rtn_u32 v164, v168, v166 offset:4104\n\t" \
  "s_mov_b64 exec, -1\n\t" \
  "ds_read_b64 v[128:129], v170 offset:0\n\t" \
  "ds_read_b64 v[130:131], v170 offset:512\n\t" \
  "ds_read_b64 v[132:133], v170 offset:1024\n\t" \
  "ds_read_b64 v[134:135], v170 offset:1536\n\t" \
  "ds_read_b64 v[136:137], v170 offset:2048\n\t" \
  "ds_read_b64 v[138:139], v170 offset:2560\n\t" \
  "ds_read_b64 v[140:141], v170 offset:3072\n\t" \
  "ds_read_b64 v[142:143], v170 offset:3584\n\t" \
  "s_waitcnt lgkmcnt(8)\n\t" \
  "v_readfirstlane_b32 s93, v164\n\t" \
  "s_cmp_lg_u32 s93, 7\n\t" \
  "s_cbranch_scc1 .Lb1%=\n\t" \
  "s_waitcnt lgkmcnt(4)\n\t" \
  "v_pk_add_f32 v[128:129], v[128:129], v[130:131]\n\t" \
  "v_pk_add_f32 v[132:133], v[132:133], v[134:135]\n\t" \
  "s_waitcnt lgkmcnt(0)\n\t" \
  "v_pk_add_f32 v[136:137], v[136:137], v[138:139]\n\t" \
  "v_pk_add_f32 v[140:141], v[140:141], v[142:143]\n\t" \
  "v_pk_add_f32 v[128:129], v[128:129], v[132:133]\n\t" \
  "v_pk_add_f32 v[136:137], v[136:137], v[140:141]\n\t" \
  "v_pk_add_f32 v[128:129], v[128:129], v[136:137]\n\t" \
  "v_cvt_pk_bf16_f32 v128, v128, v129\n\t" \
  "s_nop 1\n\t" \
  "v_mov_b32_dpp v129, v128 quad_perm:[1,0,3,2] row_mask:0xf bank_mask:0xf\n\t" \
  "v_perm_b32 v128, v129, v128, v174\n\t" \
  "buffer_store_dword v128, v171, %[rs], %[sh] offen sc1\n\t" \
  "s_memtime s[90:91]\n\t" \
  "s_barrier\n\t" \
  "s_memtime s[76:77]\n\t" \
  "s_memrealtime s[78:79]\n\t" \
  "s_nop 3\n\t" \
  "s_memrealtime s[80:81]\n\t" \
  "s_nop 3\n\t" \
  "s_memrealtime s[82:83]\n\t" \
  "s_nop 3\n\t" \
  "s_memrealtime s[84:85]\n\t" \
  "s_nop 3\n\t" \
  "s_memrealtime s[86:87]\n\t" \
  "s_nop 3\n\t" \
  "s_memrealtime s[88:89]\n\t" \
  "s_mov_b32 s93, 0\n\t" \
  "s_waitcnt lgkmcnt(0)\n\t" \
  "s_cmp_eq_u32 s80, s78\n\t" \
  "s_addc_u32 s93, s93, 0\n\t" \
  "s_cmp_eq_u32 s82, s78\n\t" \
  "s_addc_u32 s93, s93, 0\n\t" \
  "s_cmp_eq_u32 s84, s78\n\t" \
  "s_addc_u32 s93, s93, 0\n\t" \
  "s_cmp_eq_u32 s86, s78\n\t" \
  "s_addc_u32 s93, s93, 0\n\t" \
  "s_cmp_eq_u32 s88, s78\n\t" \
  "s_addc_u32 s93, s93, 0\n\t" \
  "s_mul_i32 s93, s93, 21\n\t" \
  "s_add_u32 s76, s76, 15\n\t" \
  "s_add_u32 s76, s76, s93\n\t" \
  "s_lshl2_add_u32 s78, s78, 16\n\t" \
  "s_mov_b64 exec, 1\n\t" \
  "s_sub_i32 s90, s76, s90\n\t" \
  "s_mul_i32 s90, s90, 11900\n\t" \
  "s_ashr_i32 s90, s90, 16\n\t" \
  "s_sub_u32 s90, s78, s90\n\t" \
  "s_sub_u32 s90, s90, %[base]\n\t" \
  "v_mov_b32 v164, s90\n\t" \
  "ds_write_b32 v168, v164 offset:4108\n\t" \
  "s_mov_b64 exec, -1\n\t" \
  "s_waitcnt vmcnt(1)\n\t" \
  ".Lb2%=:\n\t" \
  "s_setprio 0\n\t" \
  "s_bitcmp0_b64 %[am], %[step]\n\t" \
  "s_cbranch_scc1 .La1%=\n\t" \
  "buffer_store_dwordx4 v[224:227], v228, %[ra], 0 offen sc1\n\t" \
  ".La1%=:\n\t" \
  "buffer_load_dwordx4 v[192:195], v239, %[rt], %[st] offen offset:0 sc1\n\t" \
  "buffer_load_dwordx4 v[196:199], v239, %[rt], %[st] offen offset:1024 sc1\n\t" \
  "v_mfma_f32_16x16x32_bf16 v[128:131], v[0:3], v[176:179], 0\n\t" \
  "v_mfma_f32_16x16x32_bf16 v[136:139], v[8:11], v[176:179], 0\n\t" \
  "ds_read2st64_b32 v[144:145], v231 offset0:0 offset1:8\n\t" \
  "ds_read2st64_b32 v[146:147], v231 offset0:16 offset1:24\n\t" \
  "ds_read2st64_b32 v[148:149], v231 offset0:32 offset1:40\n\t" \
  "ds_read2st64_b32 v[150:151], v231 offset0:48 offset1:56\n\t" \
  "buffer_load_dwordx4 v[200:203], v239, %[rt], %[st4] offen offset:0 sc1\n\t" \
  "buffer_load_dwordx4 v[204:207], v239, %[rt], %[st4] offen offset:1024 sc1\n\t" \
  "v_mfma_f32_16x16x32_bf16 v[132:135], v[4:7], v[180:183], 0\n\t" \
  "v_mfma_f32_16x16x32_bf16 v[140:143], v[12:15], v[180:183], 0\n\t" \
  "s_waitcnt lgkmcnt(0)\n\t" \
  "v_add_f32 v144, v144, v145\n\t" \
  "v_add_f32 v146, v146, v147\n\t" \
  "v_add_f32 v148, v148, v149\n\t" \
  "v_add_f32 v150, v150, v151\n\t" \
  "v_add_f32 v144, v144, v146\n\t" \
  "v_add_f32 v148, v148, v150\n\t" \
  "buffer_load_dwordx4 v[208:211], v240, %[rt], %[st] offen offset:0 sc1\n\t" \
  "buffer_load_dwordx4 v[212:215], v240, %[rt], %[st] offen offset:1024 sc1\n\t" \
  "v_mfma_f32_16x16x32_bf16 v[128:131], v[16:19], v[184:187], v[128:131]\n\t" \
  "v_mfma_f32_16x16x32_bf16 v[136:139], v[24:27], v[184:187], v[136:139]\n\t" \
  "v_add_f32 v144, v144, v148\n\t" \
  "v_cvt_pk_bf16_f32 v144, v144, v144\n\t" \
  "ds_write_b16 v232, v144\n\t" \
  "ds_read_b128 v[224:227], v229\n\t" \
  "v_add_u32 v228, %[ainc], v228\n\t" \
  "v_add_u32 v229, %[rinc], v229\n\t" \
  "v_add_u32 v232, 1024, v232\n\t" \
  "v_xor_b32 v231, 0x4000, v231\n\t" \
  "buffer_load_dwordx4 v[216:219], v240, %[rt], %[st4] offen offset:0 sc1\n\t" \
  "buffer_load_dwordx4 v[220:223], v240, %[rt], %[st4] offen offset:1024 sc1\n\t" \
  "v_mfma_f32_16x16x32_bf16 v[132:135], v[20:23], v[188:191], v[132:135]\n\t" \
  "v_mfma_f32_16x16x32_bf16 v[140:143], v[28:31], v[188:191], v[140:143]\n\t" \
  "buffer_load_dwordx4 v[176:179], v241, %[rt], %[st] offen offset:0 sc1\n\t" \
  "s_waitcnt vmcnt(8)\n\t" \
  "v_mfma_f32_16x16x32_bf16 v[128:131], v[32:35], v[192:195], v[128:131]\n\t" \
  "v_mfma_f32_16x16x32_bf16 v[136:139], v[40:43], v[192:195], v[136:139]\n\t" \
  "buffer_load_dwordx4 v[180:183], v241, %[rt], %[st] offen offset:1024 sc1\n\t" \
  "s_waitcnt vmcnt(8)\n\t" \
  "v_mfma_f32_16x16x32_bf16 v[132:135], v[36:39], v[196:199], v[132:135]\n\t" \
  "v_mfma_f32_16x16x32_bf16 v[140:143], v[44:47], v[196:199], v[140:143]\n\t" \
  "buffer_load_dwordx4 v[184:187], v241, %[rt], %[st4] offen offset:0 sc1\n\t" \
  "s_waitcnt vmcnt(8)\n\t" \
  "v_mfma_f32_16x16x32_bf16 v[128:131], v[48:51], v[200:203], v[128:131]\n\t" \
  "v_mfma_f32_16x16x32_bf16 v[136:139], v[56:59], v[200:203], v[136:139]\n\t" \
  "ds_read_b128 v[160:163], v168 offset:4096\n\t" \
  "buffer_load_dwordx4 v[188:191], v241, %[rt], %[st4] offen offset:1024 sc1\n\t" \
  "s_waitcnt vmcnt(8)\n\t" \
  "v_mfma_f32_16x16x32_bf16 v[132:135], v[52:55], v[204:207], v[132:135]\n\t" \
  "v_mfma_f32_16x16x32_bf16 v[140:143], v[60:63], v[204:207], v[140:143]\n\t" \
  "s_waitcnt lgkmcnt(0)\n\t" \
  "v_readfirstlane_b32 s90, v161\n\t" \
  "v_readfirstlane_b32 s91, v163\n\t" \
  "v_readfirstlane_b32 s94, v160\n\t" \
  "s_sub_u32 s93, s94, %[sm]\n\t" \
  "s_mov_b32 %[sm], s94\n\t" \
  "s_lshr_b32 s93, s93, 3\n\t" \
  "s_sub_i32 s93, s90, s93\n\t" \
  "s_mul_i32 s93, s93, %[wm]\n\t" \
  "s_ashr_i32 s93, s93, 2\n\t" \
  "s_min_u32 s93, s93, %[gm]\n\t" \
  "s_sub_i32 s90, s90, s93\n\t" \
  "s_add_u32 s93, s90, %[rl]\n\t" \
  "s_add_u32 s93, s93, %[base]\n\t" \
  "s_add_u32 %[E], s93, %[lk]\n\t" \
  "s_sub_i32 s91, s91, s90\n\t" \
  "s_sub_i32 s91, s91, %[rl]\n\t" \
  "s_min_i32 s93, s91, %[up]\n\t" \
  "s_max_i32 s93, s93, %[dn]\n\t" \
  "s_cmp_gt_i32 s91, 60\n\t" \
  "s_cselect_b32 s93, 2, s93\n\t" \
  "s_add_i32 %[rl], %[rl], s93\n\t" \
  "v_mov_b32 v160, %[rl]\n\t" \
  "buffer_store_dword v160, v175, %[rs], 0 offen sc1\n\t" \
  "s_waitcnt vmcnt(8)\n\t" \
  "v_mfma_f32_16x16x32_bf16 v[128:131], v[64:67], v[208:211], v[128:131]\n\t" \
  "v_mfma_f32_16x16x32_bf16 v[136:139], v[72:75], v[208:211], v[136:139]\n\t" \
  "s_add_u32 %[step], %[step], 1\n\t" \
  "s_lshl_b32 %[sh], %[step], 15\n\t" \
  "s_lshr_b32 s93, %[step], 4\n\t" \
  "s_lshr_b32 s93, %[pm], s93\n\t" \
  "s_and_b32 s93, s93, 4\n\t" \
  "s_lshl_b32 s93, s93, 11\n\t" \
  "s_lshl2_add_u32 %[so], %[sh], s93\n\t" \
  "s_waitcnt vmcnt(7)\n\t" \
  "v_mfma_f32_16x16x32_bf16 v[132:135], v[68:71], v[212:215], v[132:135]\n\t" \
  "v_mfma_f32_16x16x32_bf16 v[140:143], v[76:79], v[212:215], v[140:143]\n\t" \
  "s_sub_i32 s93, %[step], 2\n\t" \
  "s_lshl_b32 %[st], s93, 19\n\t" \
  "s_lshl_b32 s94, s93, 11\n\t" \
  "s_xor_b32 s94, s94, %[s0]\n\t" \
  "s_and_b32 s94, s94, 0x2000\n\t" \
  "s_or_b32 %[st], %[st], s94\n\t" \
  "s_cmp_lt_i32 s93, 0\n\t" \
  "s_cselect_b32 %[st], 0x70000000, %[st]\n\t" \
  "s_add_u32 %[st4], %[st], 4096\n\t" \
  "s_waitcnt vmcnt(6)\n\t" \
  "v_mfma_f32_16x16x32_bf16 v[128:131], v[80:83], v[216:219], v[128:131]\n\t" \
  "v_mfma_f32_16x16x32_bf16 v[136:139], v[88:91], v[216:219], v[136:139]\n\t" \
  "s_waitcnt vmcnt(5)\n\t" \
  "v_mfma_f32_16x16x32_bf16 v[132:135], v[84:87], v[220:223], v[132:135]\n\t" \
  "v_mfma_f32_16x16x32_bf16 v[140:143], v[92:95], v[220:223], v[140:143]\n\t" \
  "s_cmp_eq_u32 %[step], 63\n\t" \
  "s_cbranch_scc1 .Llast%=\n\t" \
  "s_movk_i32 s93, 600\n\t" \
  ".Lpacel%=:\n\t" \
  "s_memrealtime s[90:91]\n\t" \
  "s_waitcnt lgkmcnt(0)\n\t" \
  "s_lshl_b32 s90, s90, 2\n\t" \
  "s_sub_i32 s91, %[E], s90\n\t" \
  "s_cmp_le_i32 s91, 0\n\t" \
  "s_cbranch_scc1 .Lgol%=\n\t" \
  "s_sub_u32 s93, s93, 1\n\t" \
  "s_cmp_lg_u32 s93, 0\n\t" \
  "s_cbranch_scc1 .Lpacel%=\n\t" \
  ".Lgol%=:\n\t" \
  "buffer_load_dwordx2 v[166:167], v173, %[rs], 0 offen sc1\n\t" \
  "s_waitcnt vmcnt(5)\n\t" \
  "v_mfma_f32_16x16x32_bf16 v[128:131], v[96:99], v[176:179], v[128:131]\n\t" \
  "v_mfma_f32_16x16x32_bf16 v[136:139], v[104:107], v[176:179], v[136:139]\n\t" \
  "s_waitcnt vmcnt(4)\n\t" \
  "v_mfma_f32_16x16x32_bf16 v[132:135], v[100:103], v[180:183], v[132:135]\n\t" \
  "v_mfma_f32_16x16x32_bf16 v[140:143], v[108:111], v[180:183], v[140:143]\n\t" \
  "s_waitcnt vmcnt(3)\n\t" \
  "v_mfma_f32_16x16x32_bf16 v[128:131], v[112:115], v[184:187], v[128:131]\n\t" \
  "v_mfma_f32_16x16x32_bf16 v[136:139], v[120:123], v[184:187], v[136:139]\n\t" \
  "s_waitcnt vmcnt(2)\n\t" \
  "v_mfma_f32_16x16x32_bf16 v[132:135], v[116:119], v[188:191], v[132:135]\n\t" \
  "v_mfma_f32_16x16x32_bf16 v[140:143], v[124:127], v[188:191], v[140:143]\n\t" \
  "v_mov_b64 v[144:145], 0\n\t" \
  "v_mov_b64 v[146:147], 0\n\t" \
  "v_mov_b64 v[148:149], 0\n\t" \
  "v_mov_b64 v[150:151], 0\n\t" \
  "v_mov_b64 v[152:153], 0\n\t" \
  "v_mov_b64 v[154:155], 0\n\t" \
  "v_mov_b64 v[156:157], 0\n\t" \
  "v_mov_b64 v[158:159], 0\n\t" \
  "v_pk_add_f32 v[128:129], v[128:129], v[132:133]\n\t" \
  "v_pk_add_f32 v[130:131], v[130:131], v[134:135]\n\t" \
  "ds_write_b128 v230, v[128:131] offset:0\n\t" \
  "v_pk_add_f32 v[136:137], v[136:137], v[140:141]\n\t" \
  "v_pk_add_f32 v[138:139], v[138:139], v[142:143]\n\t" \
  "ds_write_b128 v230, v[136:139] offset:1024\n\t" \
  "v_xor_b32 v230, 0x4000, v230\n\t" \
  "s_cmp_lg_u32 %[step], 63\n\t" \
  "s_cbranch_scc1 .Lstep%=\n\t" \
  ".Lfc%=:\n\t" \
  "s_waitcnt vmcnt(0)\n\t" \
  "v_cmp_eq_u32 vcc, -1, v166\n\t" \
  "s_cbranch_vccnz .Lfw%=\n\t" \
  ".Lfo%=:\n\t" \
  "v_add_u32 v242, 16384, v242\n\t" \
  "buffer_load_dwordx4 v[176:179], v238, %[rt], %[st] offen offset:0 sc1\n\t" \
  "buffer_load_dwordx4 v[180:183], v238, %[rt], %[st] offen offset:1024 sc1\n\t" \
  "buffer_load_dwordx4 v[184:187], v238, %[rt], %[st4] offen offset:0 sc1\n\t" \
  "buffer_load_dwordx4 v[188:191], v238, %[rt], %[st4] offen offset:1024 sc1\n\t" \
  "buffer_load_dwordx4 v[192:195], v239, %[rt], %[st] offen offset:0 sc1\n\t" \
  "buffer_load_dwordx4 v[196:199], v239, %[rt], %[st] offen offset:1024 sc1\n\t" \
  "buffer_load_dwordx4 v[200:203], v239, %[rt], %[st4] offen offset:0 sc1\n\t" \
  "buffer_load_dwordx4 v[204:207], v239, %[rt], %[st4] offen offset:1024 sc1\n\t" \
  "v_subrev_u32 v230, 98304, v230\n\t" \
  ".Lt%=:\n\t" \
  "s_waitcnt vmcnt(7)\n\t" \
  "v_mfma_f32_16x16x32_bf16 v[128:131], v[0:3], v[176:179], 0\n\t" \
  "v_mfma_f32_16x16x32_bf16 v[136:139], v[8:11], v[176:179], 0\n\t" \
  "buffer_load_dwordx4 v[176:179], v240, %[rt], %[st] offen offset:0 sc1\n\t" \
  "buffer_load_dword v166, v242, %[rs], 0 offen sc1\n\t" \
  "s_waitcnt vmcnt(8)\n\t" \
  "v_mfma_f32_16x16x32_bf16 v[132:135], v[4:7], v[180:183], 0\n\t" \
  "v_mfma_f32_16x16x32_bf16 v[140:143], v[12:15], v[180:183], 0\n\t" \
  "buffer_load_dwordx4 v[180:183], v240, %[rt], %[st] offen offset:1024 sc1\n\t" \
  "s_waitcnt vmcnt(8)\n\t" \
  "v_mfma_f32_16x16x32_bf16 v[128:131], v[16:19], v[184:187], v[128:131]\n\t" \
  "v_mfma_f32_16x16x32_bf16 v[136:139], v[24:27], v[184:187], v[136:139]\n\t" \
  "buffer_load_dwordx4 v[184:187], v240, %[rt], %[st4] offen offset:0 sc1\n\t" \
  "s_waitcnt vmcnt(8)\n\t" \
  "v_mfma_f32_16x16x32_bf16 v[132:135], v[20:23], v[188:191], v[132:135]\n\t" \
  "v_mfma_f32_16x16x32_bf16 v[140:143], v[28:31], v[188:191], v[140:143]\n\t" \
  "buffer_load_dwordx4 v[188:191], v240, %[rt], %[st4] offen offset:1024 sc1\n\t" \
  "s_waitcnt vmcnt(8)\n\t" \
  "v_mfma_f32_16x16x32_bf16 v[128:131], v[32:35], v[192:195], v[128:131]\n\t" \
  "v_mfma_f32_16x16x32_bf16 v[136:139], v[40:43], v[192:195], v[136:139]\n\t" \
  "buffer_load_dwordx4 v[192:195], v241, %[rt], %[st] offen offset:0 sc1\n\t" \
  "s_waitcnt vmcnt(8)\n\t" \
  "v_mfma_f32_16x16x32_bf16 v[132:135], v[36:39], v[196:199], v[132:135]\n\t" \
  "v_mfma_f32_16x16x32_bf16 v[140:143], v[44:47], v[196:199], v[140:143]\n\t" \
  "buffer_load_dwordx4 v[196:199], v241, %[rt], %[st] offen offset:1024 sc1\n\t" \
  "s_waitcnt vmcnt(8)\n\t" \
  "v_mfma_f32_16x16x32_bf16 v[128:131], v[48:51], v[200:203], v[128:131]\n\t" \
  "v_mfma_f32_16x16x32_bf16 v[136:139], v[56:59], v[200:203], v[136:139]\n\t" \
  "buffer_load_dwordx4 v[200:203], v241, %[rt], %[st4] offen offset:0 sc1\n\t" \
  "s_waitcnt vmcnt(8)\n\t" \
  "v_mfma_f32_16x16x32_bf16 v[132:135], v[52:55], v[204:207], v[132:135]\n\t" \
  "v_mfma_f32_16x16x32_bf16 v[140:143], v[60:63], v[204:207], v[140:143]\n\t" \
  "buffer_load_dwordx4 v[204:207], v241, %[rt], %[st4] offen offset:1024 sc1\n\t" \
  "s_waitcnt vmcnt(8)\n\t" \
  "v_mfma_f32_16x16x32_bf16 v[128:131], v[64:67], v[176:179], v[128:131]\n\t" \
  "v_mfma_f32_16x16x32_bf16 v[136:139], v[72:75], v[176:179], v[136:139]\n\t" \
  "s_waitcnt vmcnt(7)\n\t" \
  ".Ltc%=:\n\t" \
  "v_cmp_eq_u32 vcc, -1, v166\n\t" \
  "s_cbranch_vccnz .Ltw%=\n\t" \
  ".Lto%=:\n\t" \
  "s_cmp_eq_u32 %[step], 65\n\t" \
  "s_cbranch_scc1 .Ltl%=\n\t" \
  "s_add_u32 %[step], %[step], 1\n\t" \
  "s_sub_i32 s93, %[step], 2\n\t" \
  "s_lshl_b32 %[st], s93, 19\n\t" \
  "s_lshl_b32 s94, s93, 11\n\t" \
  "s_xor_b32 s94, s94, %[s0]\n\t" \
  "s_and_b32 s94, s94, 0x2000\n\t" \
  "s_or_b32 %[st], %[st], s94\n\t" \
  "s_cmp_lt_i32 s93, 0\n\t" \
  "s_cselect_b32 %[st], 0x70000000, %[st]\n\t" \
  "s_add_u32 %[st4], %[st], 4096\n\t" \
  "v_add_u32 v242, 16384, v242\n\t" \
  "s_cmp_eq_u32 %[step], 65\n\t" \
  "s_cbranch_scc0 .Ltn%=\n\t" \
  "v_mov_b32 v242, %[fg]\n\t" \
  ".Ltn%=:\n\t" \
  "buffer_load_dwordx4 v[176:179], v238, %[rt], %[st] offen offset:0 sc1\n\t" \
  "s_waitcnt vmcnt(7)\n\t" \
  "v_mfma_f32_16x16x32_bf16 v[132:135], v[68:71], v[180:183], v[132:135]\n\t" \
  "v_mfma_f32_16x16x32_bf16 v[140:143], v[76:79], v[180:183], v[140:143]\n\t" \
  "buffer_load_dwordx4 v[180:183], v238, %[rt], %[st] offen offset:1024 sc1\n\t" \
  "s_waitcnt vmcnt(7)\n\t" \
  "v_mfma_f32_16x16x32_bf16 v[128:131], v[80:83], v[184:187], v[128:131]\n\t" \
  "v_mfma_f32_16x16x32_bf16 v[136:139], v[88:91], v[184:187], v[136:139]\n\t" \
  "buffer_load_dwordx4 v[184:187], v238, %[rt], %[st4] offen offset:0 sc1\n\t" \
  "s_waitcnt vmcnt(7)\n\t" \
  "v_mfma_f32_16x16x32_bf16 v[132:135], v[84:87], v[188:191], v[132:135]\n\t" \
  "v_mfma_f32_16x16x32_bf16 v[140:143], v[92:95], v[188:191], v[140:143]\n\t" \
  "buffer_load_dwordx4 v[188:191], v238, %[rt], %[st4] offen offset:1024 sc1\n\t" \
  "s_waitcnt vmcnt(7)\n\t" \
  "v_mfma_f32_16x16x32_bf16 v[128:131], v[96:99], v[192:195], v[128:131]\n\t" \
  "v_mfma_f32_16x16x32_bf16 v[136:139], v[104:107], v[192:195], v[136:139]\n\t" \
  "buffer_load_dwordx4 v[192:195], v239, %[rt], %[st] offen offset:0 sc1\n\t" \
  "s_waitcnt vmcnt(7)\n\t" \
  "v_mfma_f32_16x16x32_bf16 v[132:135], v[100:103], v[196:199], v[132:135]\n\t" \
  "v_mfma_f32_16x16x32_bf16 v[140:143], v[108:111], v[196:199], v[140:143]\n\t" \
  "buffer_load_dwordx4 v[196:199], v239, %[rt], %[st] offen offset:1024 sc1\n\t" \
  "s_waitcnt vmcnt(7)\n\t" \
  "v_mfma_f32_16x16x32_bf16 v[128:131], v[112:115], v[200:203], v[128:131]\n\t" \
  "v_mfma_f32_16x16x32_bf16 v[136:139], v[120:123], v[200:203], v[136:139]\n\t" \
  "buffer_load_dwordx4 v[200:203], v239, %[rt], %[st4] offen offset:0 sc1\n\t" \
  "s_waitcnt vmcnt(7)\n\t" \
  "v_mfma_f32_16x16x32_bf16 v[132:135], v[116:119], v[204:207], v[132:135]\n\t" \
  "v_mfma_f32_16x16x32_bf16 v[140:143], v[124:127], v[204:207], v[140:143]\n\t" \
  "buffer_load_dwordx4 v[204:207], v239, %[rt], %[st4] offen offset:1024 sc1\n\t" \
  "s_nop 5\n\t" \
  "v_pk_add_f32 v[128:129], v[128:129], v[132:133]\n\t" \
  "v_pk_add_f32 v[130:131], v[130:131], v[134:135]\n\t" \
  "ds_write_b128 v230, v[128:131] offset:0\n\t" \
  "v_pk_add_f32 v[136:137], v[136:137], v[140:141]\n\t" \
  "v_pk_add_f32 v[138:139], v[138:139], v[142:143]\n\t" \
  "ds_write_b128 v230, v[136:139] offset:1024\n\t" \
  "v_add_u32 v230, 16384, v230\n\t" \
  "s_branch .Lt%=\n\t" \
  ".Ltl%=:\n\t" \
  "buffer_load_dwordx4 v[224:227], v243, %[rg], 0 offen sc1\n\t" \
  "s_waitcnt vmcnt(7)\n\t" \
  "v_mfma_f32_16x16x32_bf16 v[132:135], v[68:71], v[180:183], v[132:135]\n\t" \
  "v_mfma_f32_16x16x32_bf16 v[140:143], v[76:79], v[180:183], v[140:143]\n\t" \
  "s_waitcnt vmcnt(6)\n\t" \
  "v_mfma_f32_16x16x32_bf16 v[128:131], v[80:83], v[184:187], v[128:131]\n\t" \
  "v_mfma_f32_16x16x32_bf16 v[136:139], v[88:91], v[184:187], v[136:139]\n\t" \
  "s_waitcnt vmcnt(5)\n\t" \
  "v_mfma_f32_16x16x32_bf16 v[132:135], v[84:87], v[188:191], v[132:135]\n\t" \
  "v_mfma_f32_16x16x32_bf16 v[140:143], v[92:95], v[188:191], v[140:143]\n\t" \
  "s_waitcnt vmcnt(4)\n\t" \
  "v_mfma_f32_16x16x32_bf16 v[128:131], v[96:99], v[192:195], v[128:131]\n\t" \
  "v_mfma_f32_16x16x32_bf16 v[136:139], v[104:107], v[192:195], v[136:139]\n\t" \
  "s_waitcnt vmcnt(3)\n\t" \
  "v_mfma_f32_16x16x32_bf16 v[132:135], v[100:103], v[196:199], v[132:135]\n\t" \
  "v_mfma_f32_16x16x32_bf16 v[140:143], v[108:111], v[196:199], v[140:143]\n\t" \
  "s_waitcnt vmcnt(2)\n\t" \
  "v_mfma_f32_16x16x32_bf16 v[128:131], v[112:115], v[200:203], v[128:131]\n\t" \
  "v_mfma_f32_16x16x32_bf16 v[136:139], v[120:123], v[200:203], v[136:139]\n\t" \
  "s_waitcnt vmcnt(1)\n\t" \
  "v_mfma_f32_16x16x32_bf16 v[132:135], v[116:119], v[204:207], v[132:135]\n\t" \
  "v_mfma_f32_16x16x32_bf16 v[140:143], v[124:127], v[204:207], v[140:143]\n\t" \
  "s_nop 6\n\t" \
  "v_pk_add_f32 v[128:129], v[128:129], v[132:133]\n\t" \
  "v_pk_add_f32 v[130:131], v[130:131], v[134:135]\n\t" \
  "ds_write_b128 v230, v[128:131] offset:0\n\t" \
  "v_pk_add_f32 v[136:137], v[136:137], v[140:141]\n\t" \
  "v_pk_add_f32 v[138:139], v[138:139], v[142:143]\n\t" \
  "ds_write_b128 v230, v[136:139] offset:1024\n\t" \
  "s_waitcnt vmcnt(0) lgkmcnt(0)\n\t" \
  "s_branch .Lend%=\n\t" \
  ".Ltw%=:\n\t" \
  "s_add_u32 s92, s92, 1\n\t" \
  "s_sleep 2\n\t" \
  "s_and_b32 s93, s92, 63\n\t" \
  "s_cbranch_scc1 .Ltr%=\n\t" \
  "buffer_load_dword v164, off, %[rs], %[sab] sc1\n\t" \
  "s_waitcnt vmcnt(0)\n\t" \
  "v_readfirstlane_b32 s93, v164\n\t" \
  "s_nop 3\n\t" \
  "s_cmp_eq_u32 s93, 1\n\t" \
  "s_cbranch_scc1 .Lto%=\n\t" \
  "s_cmp_lt_u32 s92, 0x80000\n\t" \
  "s_cbranch_scc1 .Ltr%=\n\t" \
  "v_mov_b32 v164, 1\n\t" \
  "buffer_store_dword v164, off, %[rs], %[sab] sc1\n\t" \
  "s_waitcnt vmcnt(0)\n\t" \
  "s_branch .Lto%=\n\t" \
  ".Ltr%=:\n\t" \
  "buffer_load_dword v166, v242, %[rs], 0 offen sc1\n\t" \
  "s_waitcnt vmcnt(0)\n\t" \
  "s_branch .Ltc%=\n\t" \
  ".Llast%=:\n\t" \
  "v_mov_b32 v173, v242\n\t" \
  "s_branch .Lgol%=\n\t" \
  ".Lfw%=:\n\t" \
  "s_add_u32 s92, s92, 1\n\t" \
  "s_sleep 2\n\t" \
  "s_and_b32 s93, s92, 63\n\t" \
  "s_cbranch_scc1 .Lfr%=\n\t" \
  "buffer_load_dword v164, off, %[rs], %[sab] sc1\n\t" \
  "s_waitcnt vmcnt(0)\n\t" \
  "v_readfirstlane_b32 s93, v164\n\t" \
  "s_nop 3\n\t" \
  "s_cmp_eq_u32 s93, 1\n\t" \
  "s_cbranch_scc1 .Lfo%=\n\t" \
  "s_cmp_lt_u32 s92, 0x80000\n\t" \
  "s_cbranch_scc1 .Lfr%=\n\t" \
  "v_mov_b32 v164, 1\n\t" \
  "buffer_store_dword v164, off, %[rs], %[sab] sc1\n\t" \
  "s_waitcnt vmcnt(0)\n\t" \
  "s_branch .Lfo%=\n\t" \
  ".Lfr%=:\n\t" \
  "buffer_load_dwordx2 v[166:167], v173, %[rs], 0 offen sc1\n\t" \
  "s_branch .Lfc%=\n\t" \
  ".Lb1%=:\n\t" \
  "s_waitcnt vmcnt(0) lgkmcnt(0)\n\t" \
  "s_barrier\n\t" \
  "s_branch .Lb2%=\n\t" \
  ".Lbl%=:\n\t" \
  "s_bitset1_b64 %[ps], %[step]\n\t" \
  "s_cmp_lt_u32 s92, 3\n\t" \
  "s_cbranch_scc1 .Lbm%=\n\t" \
  "s_sleep 12\n\t" \
  ".Lbm%=:\n\t" \
  "s_add_u32 s92, s92, 1\n\t" \
  "s_sleep 2\n\t" \
  "s_and_b32 s93, s92, 63\n\t" \
  "s_cbranch_scc1 .Lba%=\n\t" \
  "buffer_load_dword v164, off, %[rs], %[sab] sc1\n\t" \
  "s_waitcnt vmcnt(0)\n\t" \
  "v_readfirstlane_b32 s93, v164\n\t" \
  "s_nop 3\n\t" \
  "s_cmp_eq_u32 s93, 1\n\t" \
  "s_cbranch_scc1 .Lgo%=\n\t" \
  "s_cmp_lt_u32 s92, 0x80000\n\t" \
  "s_cbranch_scc1 .Lba%=\n\t" \
  "v_mov_b32 v164, 1\n\t" \
  "buffer_store_dword v164, off, %[rs], %[sab] sc1\n\t" \
  "s_waitcnt vmcnt(0)\n\t" \
  "s_branch .Lgo%=\n\t" \
  ".Lba%=:\n\t" \
  "buffer_load_dwordx2 v[166:167], v173, %[rs], 0 offen sc1\n\t" \
  "s_branch .Lcal%=\n\t" \
  ".Lslow%=:\n\t" \
  "s_mov_b64 s[94:95], 0\n\t" \
  "v_cmp_eq_u32 vcc, %[sent], v128\n\t" \
  "s_or_b64 s[94:95], s[94:95], vcc\n\t" \
  "v_cmp_eq_u32 vcc, %[sent], v129\n\t" \
  "s_or_b64 s[94:95], s[94:95], vcc\n\t" \
  "v_cmp_eq_u32 vcc, %[sent], v130\n\t" \
  "s_or_b64 s[94:95], s[94:95], vcc\n\t" \
  "v_cmp_eq_u32 vcc, %[sent], v131\n\t" \
  "s_or_b64 s[94:95], s[94:95], vcc\n\t" \
  "v_cmp_eq_u32 vcc, %[sent], v132\n\t" \
  "s_or_b64 s[94:95], s[94:95], vcc\n\t" \
  "v_cmp_eq_u32 vcc, %[sent], v133\n\t" \
  "s_or_b64 s[94:95], s[94:95], vcc\n\t" \
  "v_cmp_eq_u32 vcc, %[sent], v134\n\t" \
  "s_or_b64 s[94:95], s[94:95], vcc\n\t" \
  "v_cmp_eq_u32 vcc, %[sent], v135\n\t" \
  "s_or_b64 s[94:95], s[94:95], vcc\n\t" \
  "v_cmp_eq_u32 vcc, %[sent], v136\n\t" \
  "s_or_b64 s[94:95], s[94:95], vcc\n\t" \
  "v_cmp_eq_u32 vcc, %[sent], v137\n\t" \
  "s_or_b64 s[94:95], s[94:95], vcc\n\t" \
  "v_cmp_eq_u32 vcc, %[sent], v138\n\t" \
  "s_or_b64 s[94:95], s[94:95], vcc\n\t" \
  "v_cmp_eq_u32 vcc, %[sent], v139\n\t" \
  "s_or_b64 s[94:95], s[94:95], vcc\n\t" \
  "v_cmp_eq_u32 vcc, %[sent], v140\n\t" \
  "s_or_b64 s[94:95], s[94:95], vcc\n\t" \
  "v_cmp_eq_u32 vcc, %[sent], v141\n\t" \
  "s_or_b64 s[94:95], s[94:95], vcc\n\t" \
  "v_cmp_eq_u32 vcc, %[sent], v142\n\t" \
  "s_or_b64 s[94:95], s[94:95], vcc\n\t" \
  "v_cmp_eq_u32 vcc, %[sent], v143\n\t" \
  "s_or_b64 s[94:95], s[94:95], vcc\n\t" \
  "s_cbranch_scc0 .Lok%=\n\t" \
  "s_bitset1_b64 %[fm], %[step]\n\t" \
  "s_add_u32 s92, s92, 1\n\t" \
  "s_sleep 1\n\t" \
  "s_and_b32 s93, s92, 63\n\t" \
  "s_cbranch_scc1 .Lre%=\n\t" \
  "buffer_load_dword v164, off, %[rs], %[sab] sc1\n\t" \
  "s_waitcnt vmcnt(0)\n\t" \
  "v_readfirstlane_b32 s93, v164\n\t" \
  "s_nop 3\n\t" \
  "s_cmp_eq_u32 s93, 1\n\t" \
  "s_cbranch_scc1 .Lok%=\n\t" \
  "s_cmp_lt_u32 s92, 0x80000\n\t" \
  "s_cbranch_scc1 .Lre%=\n\t" \
  "v_mov_b32 v164, 1\n\t" \
  "buffer_store_dword v164, off, %[rs], %[sab] sc1\n\t" \
  "s_waitcnt vmcnt(0)\n\t" \
  "s_branch .Lok%=\n\t" \
  ".Lre%=:\n\t" \
  "buffer_load_dwordx4 v[128:131], v234, %[rs], %[so] offen sc1\n\t" \
  "s_nop 15\n\t" \
  "buffer_load_dwordx4 v[132:135], v235, %[rs], %[so] offen sc1\n\t" \
  "s_nop 15\n\t" \
  "buffer_load_dwordx4 v[136:139], v236, %[rs], %[so] offen sc1\n\t" \
  "s_nop 15\n\t" \
  "buffer_load_dwordx4 v[140:143], v237, %[rs], %[so] offen sc1\n\t" \
  "v_mov_b64 v[144:145], 0\n\t" \
  "v_mov_b64 v[146:147], 0\n\t" \
  "v_mov_b64 v[148:149], 0\n\t" \
  "v_mov_b64 v[150:151], 0\n\t" \
  "v_mov_b64 v[152:153], 0\n\t" \
  "v_mov_b64 v[154:155], 0\n\t" \
  "v_mov_b64 v[156:157], 0\n\t" \
  "v_mov_b64 v[158:159], 0\n\t" \
  "s_waitcnt vmcnt(0)\n\t" \
  "s_branch .Lw%=\n\t" \
  ".Lend%=:\n\t"

__device__ __forceinline__ bf16x8 get4(const u32x32& W, int i) {
  return as_bf16x8((u32x4){W[4 * i], W[4 * i + 1], W[4 * i + 2], W[4 * i + 3]});
}
__device__ __forceinline__ int look_prog(__amdgpu_buffer_rsrc_t rs, int off) {
  asm volatile("" ::: "memory");
  const int v = (int)ld_flag(rs, off);
  return min(__builtin_amdgcn_readlane(v, 0), __builtin_amdgcn_readlane(v, 1));
}
__device__ __forceinline__ int wait_prog(__amdgpu_buffer_rsrc_t rs, int off, int need, gu32* abortw) {
  int v;
  for (unsigned spins = 0;;) {
    v = look_prog(rs, off);
    if (v >= need) break;
    __builtin_amdgcn_s_sleep(4);
    if (spin_fail(spins, abortw)) break;
  }
  asm volatile("" ::: "memory");
  return v;
}
#define CLB_S "s90", "s91", "s92", "s93", "s94", "s95", "vcc", "scc", "memory"
#define W_IN "{v[0:31]}"(W[0]), "{v[32:63]}"(W[1]), "{v[64:95]}"(W[2]), "{v[96:127]}"(W[3]), "{v[128:159]}"(W[4]), "{v[160:191]}"(W[5])

#define W_DELAY 0
#define LEAD0 408
#define LEAD_N0 5
#define LEAD_SH 2
#define LEAD_X0 8
#define LEAD_U0 24
#define LEAD_D0 0
#define OX4 8
#define T0_BACK 1000
#define QBJ 3
constexpr int L_MEM = 0, L_KT = 16384, L_QX = 24576, L_KX = 26624, L_VX = 16384, L_MISC = 30720, L_WL = 32768;

#define QK_STEP(TXT)                                                                                                        \
  asm volatile(TXT                                                                                                          \
      : [tout] "=&s"(tout), [spins] "=&s"(spins), [tb1] "=&s"(tb1), [tgt] "=&s"(tgt), "+{v248}"(aL16), "+{v249}"(aMW), "+{v250}"(aWL),                            \
        "+{v251}"(aIDX), "+{v252}"(aQW), "+{v[232:235]}"(QF)                                                                 \
      : W_IN, [rs] "s"(rs4), [som] "s"(o_mem(pm, s) + WV * 1024), [som2] "s"(o_mem(pm, s) + WV * 1024 + 16384), [sent] "s"(DSENT), [sab] "s"(SY_ABORT), \
        [lead] "s"(lead), [step] "s"(s), [sbc] "s"(SY_BA + side(pm, 1) + h * 128), [nh] "s"(0xFF00FF00FF00FF00ull), [up] "s"(0xFFFFFFFF00000000ull), [negc] "s"(-cexp),      \
        [c] "s"(cexp), [so] "s"(o_o(pm, s, h) + WV * 512), [sk] "s"(o_v(pm, s, h) + 2048 + (WV & 3) * 512),                    \
        [sv] "s"(o_v(pm, s, h) + WV * 512), [rt] "s"(rt4), [svt] "s"(t_off(p, T_V + kseg(s, h)) + WV * 2048)                  \
      : CLB8(192, 193, 194, 195, 196, 197, 198, 199), CLB8(200, 201, 202, 203, 204, 205, 206, 207),                          \
        CLB8(208, 209, 210, 211, 212, 213, 214, 215), CLB8(216, 217, 218, 219, 220, 221, 222, 223),                          \
        CLB8(224, 225, 226, 227, 228, 229, 230, 231), CLB8(236, 237, 238, 239, 240, 241, 242, 243), "v244", "v245", "v246", "v247", "v253", "s88", "s89", CLB_S)

template <int WV>
__device__ __forceinline__ void qk_wave(Args& p, int h, char* smem) {
  const int tid = threadIdx.x;
  unsigned pm = 0;
  int have = -1;
  u32x32 W[6];
  {
    int lane = tid & 63;
    asm volatile("" : "+v"(lane));
    const unsigned vo = (WV * 4 + (lane >> 4)) * (HD * 2) + 16 * ((lane & 15) ^ swzB((WV * 4 + (lane >> 4)) & 15));
    auto stage = [&](int j) {
#pragma unroll
      for (int mm = 0; mm < 2; ++mm) {
        const char* src = (const char*)((mm ? p.Wk : p.Wq) + (size_t)j * 128 * HD + h * DH);
        const unsigned dst = lds_off(smem) + (~j & 1) * 65536 + mm * 32768 + WV * 1024;
        glds2(src, 32 * HD * 2, vo, dst);
        if (TH_Q < 63) HOLD_V((h & 3) ^ 1, TH_Q);
        glds2(src + 64 * HD * 2, 32 * HD * 2, vo, dst + 16384);
        if (TH_Q < 63) HOLD_V((h & 3) ^ 1, TH_Q);
      }
    };
    bf16x8 keep[4][4];
    int f0 = -1;
    auto place = [&](int g) {
#pragma unroll
      for (int i = 0; i < 4; ++i)
        *reinterpret_cast<bf16x8*>(smem + (g == 0 ? 65536 : g == 1 ? 32768 : g == 2 ? 131072 : 98304) + WV * 4096 + i * 1024 + lane * 16) = keep[g][i];
    };
    stage(0); stage(1);
    Boot bt;
#pragma unroll
    for (int j = 0; j < 8; ++j) {
      if (j < 7) WAIT_V(8); else WAIT_V(0);
      if (WV == 7 && j == QBJ + 1) pm = boot_done(p.ws, bt);
      if ((WV == 4 || WV == 5) && j == 7) {
        asm volatile("" : "+v"(f0));
        have = min(__builtin_amdgcn_readlane(f0, 0), __builtin_amdgcn_readlane(f0, 1));
      }
      LBAR();
#pragma unroll
      for (int k = 0; k < 8; ++k) {
        const int P = (QK_MAP[WV] >> (4 * (2 * j + (k >> 2)))) & 15;
        const bf16x8 fr = ld_bfrag(smem + (~j & 1) * 65536 + (WV >> 2) * 32768 + (k >> 2) * 16384, k & 1, 2 * (WV & 3) + ((k >> 1) & 1), lane);
        const int f = P * 4 + (k & 3);
        if (P < 12) put4(W[f >> 3], f & 7, fr);
        else keep[P - 12][k & 3] = fr;
      }
      if (j == (WV >> 1)) place(2);
      LBAR();
      if (WV == 7 && j == QBJ) boot_ask(p.ws, bt);
      if (j + 2 < 8) stage(j + 2);
      if (j == 6) {
        if (WV == 4 || WV == 5)
          asm volatile("buffer_load_dword %0, %1, %2, 0 offen sc1" : "=&v"(f0) : "v"(fkq(h, (lane & 1) * 2)), "s"(raw_rsrc(p.ws + WS_SY, SY_TOTAL)) : "memory");
        place(0); place(3);
      }
    }
    place(1);
    if (WV == 7 && lane < 8) lds_at(smem + L_MISC)[lane] = lane == 7 ? pm : (lane == 1 ? now_q() - T0_BACK : 0);
    if (WV == 6) lds_at(smem + 28672)[lane] = ~0u;
    LBAR();
    pm = uni(lds_at(smem + L_MISC)[7]);
    p.s0 = pm >> 31;
    if (WV == 6) RDW(h);
    if (WV == 4 || WV == 5) {
      if (have < 0) have = wait_prog(mkrsrc(p.ws + WS_SY, SY_TOTAL), fkq(h, (lane & 1) * 2), 0, (gu32*)(p.ws + WS_SY + SY_ABORT));
      const char* src = p.ws + WS_T + t_off(p, kseg(0, h)) + (WV - 4) * 4096 + lane * 16;
#pragma unroll
      for (int i = 0; i < 4; ++i) glds16_sc1(src + i * 1024, smem + L_KT + (WV - 4) * 4096 + i * 1024);
    }
  }
  const __amdgpu_buffer_rsrc_t rsy = mkrsrc(p.ws + WS_SY, SY_TOTAL);
  const u32x4 rs4 = raw_rsrc(p.ws + WS_SY, SY_TOTAL);
  const u32x4 rt4 = raw_rsrc(p.ws + WS_T, T_BYTES);
  gu32* abortw = (gu32*)(p.ws + WS_SY + SY_ABORT);
  const float cexp = 0.08838834764831845f * 1.4426950408889634f;
  constexpr int m = WV >> 2, b = WV & 3;
  int lead = LEAD0;
  unsigned tb1 = 0;
  unsigned long long fm = 0, ps = 0; (void)fm; (void)ps;
  for (int s = 0; s < NSEG; ++s) {
    int lane = tid & 63;
    asm volatile("" : "+v"(lane));
    const int r = lane & 15, q = lane >> 4;
    unsigned aL16 = lane * 16, aMW = aL16 + WV * 1024, aWL = aL16 + 98304 + WV * 4096;
    unsigned aIDX = (lane & 2) ? 0xEEEE : 0x4444;
    unsigned aQW = (32 * b + r) * 16 + q * 4 + (m ? L_KX : L_QX);
    u32x4 QF;
    asm volatile("" : "={v[232:235]}"(QF));
    if (WV < 4) {
      QF[0] = L_QX + (q * 8 + (r >> 2)) * 16 + (r & 1) * 8;
      QF[1] = ((2 * (q & 1) + (q >> 1)) * 16 + r) * 4;
      QF[2] = ((r >> 2) & 1) * 65536 + ((r >> 1) & 1) * 64 + (r >> 3) * 32 + (r & 1) * 16 + (q >> 1) * 256 + (q & 1) * OX4;
    } else QF[3] = r * 16 + q * 4;
    unsigned tout, spins, tgt;
    if (WV == 0) QK_STEP(QK_ASM_0); else if (WV == 1) QK_STEP(QK_ASM_1); else if (WV == 2) QK_STEP(QK_ASM_2);
    else if (WV == 3) QK_STEP(QK_ASM_3); else if (WV == 4) QK_STEP(QK_ASM_4); else if (WV == 5) QK_STEP(QK_ASM_5);
    else if (WV == 6) QK_STEP(QK_ASM_6); else QK_STEP(QK_ASM_7);
    tout = __builtin_amdgcn_readfirstlane(tout);
    tb1 = __builtin_amdgcn_readfirstlane(tb1);
    lane = tid & 63;
    asm volatile("" : "+v"(lane));
    if (WV < 4) {
      lds_at(smem + L_MISC + 8)[WV] = s + 1;
      if (WV == 3) { lds_at(smem + L_MISC)[6] = time_of(tout); lds_at(smem + 28672)[2] = s; }
    }
    if (WV == 6) {
      if (s + 1 < NSEG) watch(rsy, s, lane, now_q() + W_DELAY, (const unsigned*)(smem + L_MISC), abortw, side(pm, 1));
    } else if (WV >= 4 && s + 1 < NSEG) {
      unsigned t;
      do {
        __builtin_amdgcn_s_sleep(1);
        t = lds_at(smem + L_MISC + 8)[lane & 3];
      } while (!__all(t == (unsigned)(s + 1)));
      if (WV <= 5) {
        if (s + 2 < NSEG) {
          if (have < s + 1) have = wait_prog(rsy, fkq(h, (lane & 1) * 2), s + 1, abortw);
          const char* src = p.ws + WS_T + t_off(p, kseg(s + 1, h)) + (WV - 4) * 4096 + lane * 16;
#pragma unroll
          for (int i = 0; i < 4; ++i) glds16_sc1(src + i * 1024, smem + L_KT + (WV - 4) * 4096 + i * 1024);
          if (have < s + 2) have = look_prog(rsy, fkq(h, (lane & 1) * 2));
        }
      } else {
        do __builtin_amdgcn_s_sleep(2); while (__builtin_amdgcn_readfirstlane(lds_at(smem + 28672)[2]) != (unsigned)s);
        const int x = (int)(__builtin_amdgcn_readfirstlane(lds_at(smem + L_MISC)[6]) - tb1);
        lead = __builtin_amdgcn_readfirstlane(track(lead, x + (s < LEAD_N0 ? LEAD_X0 : 0), s < LEAD_N0 ? LEAD_U0 : LEAD_UP, s < LEAD_N0 ? LEAD_D0 : (s < 3 * LEAD_N0 ? min(max(LEAD_DN, (lead - x) >> LEAD_SH), 16) : LEAD_DN), 60));
        SMP(2, s, x); SMP(3, s, lead);
      }
    }
  }
}

__device__ __forceinline__ void qk_role(Args& p, int h, char* smem) {
  switch (__builtin_amdgcn_readfirstlane(threadIdx.x >> 6)) {
    case 0: qk_wave<0>(p, h, smem); break;
    case 1: qk_wave<1>(p, h, smem); break;
    case 2: qk_wave<2>(p, h, smem); break;
    case 3: qk_wave<3>(p, h, smem); break;
    case 4: qk_wave<4>(p, h, smem); break;
    case 5: qk_wave<5>(p, h, smem); break;
    case 6: qk_wave<6>(p, h, smem); break;
    default: qk_wave<7>(p, h, smem); break;
  }
}

#define K_DUE 160
#define T_SL 2
#define O_DUE 400
#define LEAD_V 0

constexpr int V_VM = 16384, V_T = 32768, V_KI = 131072;
__device__ __forceinline__ void vu_role(Args& p, int h, char* smem) {
  const int tid = threadIdx.x, lane = tid & 63;
  const int w = __builtin_amdgcn_readfirstlane(tid >> 6);
  u32x32 W[4];
  {
    const unsigned vo = (tid >> 4) * (HD * 2) + 16 * ((lane & 15) ^ swzB((tid >> 4) & 15));
    auto stage = [&](int j) {
      const char* src = (const char*)(p.Wv + (size_t)j * 128 * HD + h * DH);
      const unsigned dst = lds_off(smem) + L_WL + (j & 3) * 32768 + w * 1024;
      glds2(src, 32 * HD * 2, vo, dst);
      if (TH_V < 63) HOLD_V((h & 3) ^ 1, TH_V);
      glds2(src + 64 * HD * 2, 32 * HD * 2, vo, dst + 16384);
      if (TH_V < 63) HOLD_V((h & 3) ^ 1, TH_V);
    };
    start_sy(p.ws, p.mem0, h, smem);
    Boot bt;
#pragma unroll
    for (int u = 0; u < 2; ++u) {
#pragma unroll
      for (int j = 0; j < 4; ++j) stage(4 * u + j);
      if (u && w == 7) boot_ask(p.ws, bt);
      WAIT_V(0);
      if (u && w == 7) {
        const unsigned m = boot_done(p.ws, bt);
        if (lane < 8) lds_at(smem + L_MISC)[lane] = lane == 7 ? m : (lane == 1 ? now_q() : 0);
      }
      LBAR();
#pragma unroll
      for (int k = 0; k < 16; ++k) put4(W[2 * u + (k >> 3)], k & 7, ld_bfrag(smem + L_WL + ((k >> 1) ^ w) * 16384, k & 1, w, lane));
      LBAR();
    }
    RDY(32 + h);
  }
  const __amdgpu_buffer_rsrc_t rsy = mkrsrc(p.ws + WS_SY, SY_TOTAL);
  const u32x4 rs4 = raw_rsrc(p.ws + WS_SY, SY_TOTAL);
  gu32* abortw = (gu32*)(p.ws + WS_SY + SY_ABORT);
  const unsigned pm = uni(lds_at(smem + L_MISC)[7]);
  p.s0 = pm >> 31;
  unsigned long long fm = 0; (void)fm;
  int have = -1;
  auto fetch = [&](int sg) {
    if (have < sg) have = wait_prog(rsy, fkq(h, w - 4), sg, abortw);
    const char* src = p.ws + WS_T + t_off(p, (w - 4) * T_Q + kseg(sg, h)) + lane * 16;
#pragma unroll
    for (int i = 0; i < 8; ++i) glds16_sc1(src + i * 1024, smem + V_T + (sg & 3) * 24576 + (w - 4) * 8192 + i * 1024);
  };
  const int fo = SY_FO + ((w & 3) * NH + h) * 128;
  typedef __attribute__((ext_vector_type(8))) float f32x8;
  f32x8 bias;
  typedef __attribute__((ext_vector_type(8))) unsigned u32x8;
  u32x8 ca;
#pragma unroll
  for (int i = 0; i < 8; ++i) ca[i] = lane * 16 + (i ^ w) * 1024;
  u32x4 ta;
  {
    const int r = lane & 15, q = lane >> 4;
#pragma unroll
    for (int j = 0; j < 8; ++j) bias[j] = (j >> 2) * 16 + 4 * q + (j & 3) <= 16 * (w & 1) + r ? INFINITY : -INFINITY;
    ta[0] = V_KI + w * 2048 + lane * 16;
    ta[1] = V_KI + w * 2048 + (q * 8 + (r >> 2)) * 16 + (r & 1) * 8;
    ta[2] = V_VM + (w & 2) * 512 + ((2 * (q & 1) + (q >> 1)) * 16 + r) * 4;
    ta[3] = 0;
  }
#pragma unroll 1
  for (int s = 0; s < NSEG; ++s) {
    unsigned spins, tgt;
    asm volatile(VV_ASM
        : [spins] "=&s"(spins), [tgt] "=&s"(tgt)
        : W4_IN, "{v200}"(lane * 16), "{v[232:239]}"(ca), "{v202}"((lane & 2) ? 0xEEEE : 0x4444), "{v203}"(lane * 4),
          "{v204}"(tid * 4 + V_VM + (s & 1) * 2048), [rs] "s"(rs4), [som] "s"(o_mem(pm, s) + w * 1024), [som2] "s"(o_mem(pm, s) + w * 1024 + 16384), [step] "s"(s),
          [sent] "s"(DSENT), [sab] "s"(SY_ABORT), [sv] "s"(o_v(pm, s, h) + w * 256)
        : CLB32(128), CLB32(160), CLB8(192, 193, 194, 195, 196, 197, 198, 199), CLB_S);
    if (w < 4 || s == NSEG - 1) {
      const int sg = s - (w < 4);
      if (w >= 4) pace_until(now_q() + K_DUE);
      if (sg >= 0) {
        asm volatile(TK_ASM
            :
            : "{v200}"(lane * 16), "{v[208:215]}"(bias), "{v[216:219]}"(ta),
              [rs] "s"(rs4), [rt] "s"(raw_rsrc(p.ws + WS_T, T_BYTES)), [sent] "s"(DSENT), [sab] "s"(SY_ABORT), [sk] "s"(o_v(pm, sg, h) + 2048),
              [tl] "s"(V_T + (sg & 3) * 24576), [vp] "s"((sg & 1) * 2048), [tq] "s"((w & 1) * 4096), [tv] "s"((w & 2) * 2048),
              [so] "s"(t_off_o(p, T_O + (sg * 2 + (w & 1)) * 16 + (h >> 1)) + (h & 1) * 4096 + (w & 2) * 1024),
              [up] "s"(0xFFFFFFFF00000000ull), [c] "s"(1.4426950408889634f), [negc] "s"(-1.4426950408889634f),
              [sc] "s"((unsigned long long)__float_as_uint(0.08838834764831845f)), [od] "s"(s < NSEG - 1 ? O_DUE : 0)
            : CLB32(128), CLB32(160), CLB8(192, 193, 194, 195, 196, 197, 198, 199), CLB8(224, 225, 226, 227, 228, 229, 230, 231),
              "v205", "v206", "v207", "s88", "s89", CLB_S);
        if (sg >= NSEG - 3) {
          static_assert(TLAG == 2, "");
          WAIT_V(0);
          if (lane == 0) __builtin_amdgcn_raw_buffer_store_b32(1u, rsy, fo + (sg - NSEG + 3) * 4 * NH * 128, 0, 16);
        }
      }
    } else if (w == 7) {
      SMP(6, s, (int)(now_q() - tgt));
      watch(rsy, s, lane, now_q() + LEAD_V + W_DELAY, (const unsigned*)(smem + L_MISC), abortw, side(pm, 1));
    } else if (w >= 4) {
      __builtin_amdgcn_s_sleep(T_SL);
      const int s1 = w == 5 ? (s == NSEG - 2 ? s + 1 : s) : s + 1;
#pragma unroll 1
      for (int sg = (w == 5 || s == 0) ? s : s + 1; sg <= s1; ++sg) fetch(sg);
      if (have < s1 + 1 && s1 + 1 < NSEG) have = look_prog(rsy, fkq(h, w - 4));
      if (w == 5 && s + NPRE < NSEG) {
#pragma unroll
        for (int i = 0; i < 4; ++i) st_sc1(rsy, o_v(pm, s + NPRE, h) + i * 1024 + lane * 16, (u32x4){DSENT, DSENT, DSENT, DSENT});
      }
    }
  }
  if (h == 3 && (w & 2) == 0) { LST(351 + (w & 1) + (w >> 2) * 2); }
}

#define RL_UP 6
#define RL_DN 4
#define RED_MIN 40
#define BA_LOOK 360
constexpr int R_SI = 32768, R_PART = 98304;

__device__ __forceinline__ void red_role(Args& p, int j, char* smem) {
  const int tid = threadIdx.x;
  int lane = tid & 63;
  const int w = __builtin_amdgcn_readfirstlane(tid >> 6);
  const int c = (j & 2) * 8 + (j >> 2), rh = j & 1;
  const int hg = (w + (j >> 2)) & 7, rot = (j >> 2) + (j >> 5);
  const __amdgpu_buffer_rsrc_t rsy = mkrsrc(p.ws + WS_SY, SY_TOTAL);
  const u32x4 rs4 = raw_rsrc(p.ws + WS_SY, SY_TOTAL);
  const __amdgpu_buffer_rsrc_t rt = mkrsrc(p.ws + WS_T, T_BYTES);
  gu32* abortw = (gu32*)(p.ws + WS_SY + SY_ABORT);
  u32x32 W[4];
  u32x2 tag = {0u, 0u};
  {
    const u16* Wo = p.Wo + (size_t)(4 * hg * DH) * DM + 32 * c;
    const int ro = (lane >> 2) * 64 + (lane & 3) * 8;
    Boot bt;
    load_blocks<16>(smem + 32768 + w * 16384,
        [&](int i, char* slot) {
          if (i == 12 && w == 0) boot_ask(p.ws, bt);
          if ((i < 2 || i == 4 || i == 8) && (c & 14) != 6 && lane < 10) {
            const int x = i < 2 ? 3 - i : i >> 3, n = min(((c & 15) - ((c & 8) >> 2)) * 10 + lane, 127);
            touch_line((const char*)p.Wo + (size_t)(w * 512 + (x >> 1) * 256 + (x & 1) * 64 + (n & 64) + n) * (DM * 2) + (c & 16) * 64 + 384, smem + w * 256);
          }
          const int x = ((i >> 2) + rot) & 3;
          blk_dma(Wo + (size_t)((x >> 1) * 256 + (x & 1) * 64 + ((i >> 1) & 1) * 128 + (i & 1) * 32) * DM, DM, slot, lane);
          if (TH_R < 63) HOLD_V((c & 14) ^ 6, TH_R);
        },
        [&](int i, char* slot) {
#pragma unroll
          for (int t = 0; t < 2; ++t) {
            s16x4 lo = __builtin_amdgcn_ds_read_tr16_b64_v4i16((lds_s16x4_ptr)(slot + ro + t * 32));
            s16x4 hi = __builtin_amdgcn_ds_read_tr16_b64_v4i16((lds_s16x4_ptr)(slot + ro + t * 32 + 1024));
            put4(W[i >> 2], (i & 2) * 2 + 2 * t + (i & 1), (bf16x8){lo[0], lo[1], lo[2], lo[3], hi[0], hi[1], hi[2], hi[3]});
          }
        });
    if (w == 0) { lds_at(smem + 4128)[0] = boot_done(p.ws, bt); tag = next_tag(bt.e); }
    LBAR();
  }
  const unsigned pm = uni(lds_at(smem + 4128)[0]);
  p.s0 = pm >> 31;
  const int hs = side(pm, 1);
  if ((c & 14) != 6 && GPF == 2) {
#pragma unroll
    for (int i = 0; i < 2; ++i)
      touch_line((const char*)p.wd + (size_t)(j - (j >= 32 ? 8 : 0)) * 131072 + (i * 8 + w) * 8192 + lane * 128, smem + w * 256);
  }
  {
    const u32x4 ds = {DSENT, DSENT, DSENT, DSENT};
#pragma unroll
    for (int i = 0; i < 2; ++i) {
      const int s = (w * 2 + i) * 4 + (lane >> 4);
      if (s >= 2) st_sc1(rsy, o_mem(pm, s) + (c >> 4) * 16384 + (c & 15) * 512 + rh * 128 + ((lane >> 3) & 1) * 256 + (lane & 7) * 16, ds);
    }
  }
  if (tid < 4) { reinterpret_cast<unsigned*>(smem + 4096)[tid] = tid == 3 ? now_q() : 0; reinterpret_cast<unsigned*>(smem + 4112)[tid] = DSENT; }
  if (tid < 256) reinterpret_cast<int*>(smem + 5120)[tid] = ((tid >> 2) - TLAG - 6) >> 2;
  LBAR();
  const unsigned base = __builtin_amdgcn_readfirstlane(lds_at(smem + 4096)[3]);
  RDY(64 + j);
  unsigned long long fm = 0, ps = 0;
  const unsigned aL = lane * 16;
  const int e = ((56 + w) * SEG + rh * 16 + (lane >> 2)) * DM + c * 32 + (lane & 3) * 8;
  u32x4 xv = *reinterpret_cast<const u32x4*>(p.x + e), av = {DSENT, DSENT, DSENT, DSENT};
  asm volatile("" : "+v"(xv));
  {
    const int r = lane & 15, q = lane >> 4;
    const int sm0 = o_mem(pm, 0) + (c >> 4) * 16384 + (c & 15) * 512 + rh * 128;
    u32x4 VO, VT;
    unsigned fo = SY_FO + ((rh + (lane & 4) / 2) * NH + 4 * hg + (lane & 3)) * 128;
    unsigned ao = w == 1 ? (c * S_LEN + rh * 16 - (TLAG + 3) * SEG) * 64 + lane * 16
                         : (w >= 4 ? SY_O + NPRE * 131072 + side(pm, w - 2) + (j >> 3) * 16384 + (j & 7) * 1024 + lane * 16
                                   : SY_FS + (2 * c + rh) * 128 + (lane < 8 ? lane * 16 : 0x70000000));
    unsigned ar = w == 1 ? R_SI - (TLAG + 2) * 1024 + lane * 16 : (w == 2 ? 5120 : 4112);
    const unsigned long long am = w == 1 ? ((1ull << 56) - 1) << (TLAG + 3) : (w == 2 ? 0x11111111111111ull << (TLAG + 7) : (w >= 4 ? (0xFFFFull << (16 * (w - 4))) >> NPRE : 0));
    unsigned pw = R_PART + w * 2048 + lane * 16, pr = R_PART + 16384 + tid * 4;
    unsigned sw = R_SI - (TLAG + 1) * 1024 + (lane >> 2) * 64 + w * 8 + (lane & 3) * 2, stm = 8192 + w * 512;
#pragma unroll
    for (int l = 0; l < 4; ++l) {
      const int x = (l + rot) & 3;
      VO[l] = o_o(0, 0, 4 * hg) + rh * 65536 + x * 1024 + aL;
      VT[l] = (T_O + rh * 16 + 2 * hg + (x >> 1)) * 16384 + (x & 1) * 2048 + aL;
    }
    unsigned ba = lane < 4 ? SY_BA + hs + (4 * hg + lane) * 128 : 0x7F000000u;
    unsigned sm = 0;
    int rl = RL0;
    unsigned step, E, so, st, st4, sh;
    asm volatile(RED_ASM
        : [step] "=&s"(step), [E] "=&s"(E), [so] "=&s"(so), [st] "=&s"(st), [st4] "=&s"(st4), [sh] "=&s"(sh),
          [rl] "+s"(rl), [sm] "+s"(sm), [fm] "+s"(fm), [ps] "+s"(ps),
          "+{v[224:227]}"(av), "+{v228}"(ao), "+{v229}"(ar), "+{v230}"(pw), "+{v231}"(pr), "+{v232}"(sw), "+{v233}"(stm), "+{v242}"(fo), "+{v173}"(ba)
        : W4_IN, "{v168}"(aL), "{v169}"(w * 512 + lane * 8), "{v170}"(lane * 8), "{v174}"((r & 1) ? 0x03020706u : 0x05040100u),
          "{v171}"(sm0 + 32768 + (q >> 1) * 256 + (q & 1) * 64 + ((r >> 1) & 1) * 32 + (r & 1) * 16 + (r >> 2) * 4),
          "{v172}"((lane & 2) ? 0xEEEE : 0x4444),
          "{v175}"(w == 3 && lane < 32 ? SY_BR + hs + j * 128 + lane * 4 : 0x7F000000u), "{v[234:237]}"(VO), "{v[238:241]}"(VT), "{v243}"(e * 2),
          [rg] "s"(raw_rsrc(p.ws + WS_G, S_LEN * DM * 2)), [fg] "s"(SY_FG + ((14 + (w >> 2)) * 8 + (c >> 2)) * 128),
          [rs] "s"(rs4), [ra] "s"(w == 1 ? raw_rsrc(p.ws + WS_S, S_LEN * DM * 2) : rs4), [rt] "s"(raw_rsrc(p.ws + WS_T, T_BYTES)),
          [ds] "n"(DS_RED), [wm] "n"(REF_W), [gm] "n"(REF_G), [mn] "n"(RED_MIN), [lk] "n"(DS_MEM + BA_LOOK), [up] "n"(RL_UP), [dn] "n"(-RL_DN), [sent] "s"(DSENT), [sab] "s"(SY_ABORT),
          [base] "s"(base), [pm] "s"(pm), [s0] "s"((p.s0 ^ TF_O) << 13), [am] "s"(am), [ainc] "s"(w == 1 ? SEG * 64 : (w >= 4 ? 131072 : 0)),
          [rinc] "s"(w == 1 ? 1024 : (w == 2 ? 16 : 0))
        : CLB32(128), CLB8(160, 161, 162, 163, 164, 165, 166, 167), CLB8(176, 177, 178, 179, 180, 181, 182, 183),
          CLB8(184, 185, 186, 187, 188, 189, 190, 191), CLB8(192, 193, 194, 195, 196, 197, 198, 199), CLB8(200, 201, 202, 203, 204, 205, 206, 207),
          CLB8(208, 209, 210, 211, 212, 213, 214, 215), CLB8(216, 217, 218, 219, 220, 221, 222, 223), CLB_E, "s96", CLB_S);
  }
  if (j == 0 && w == 5) { LST(345); }
  LBAR();
  if (j == 0 && w == 5) { LST(346); }
  u32x4 sv;
  if (w < 6 - TLAG) sv = *reinterpret_cast<const u32x4*>(smem + R_SI + (56 + w) * 1024 + lane * 16);
  else {
    const f32x4* part = reinterpret_cast<const f32x4*>(smem + (w == 6 - TLAG ? R_PART : (w - 6 + TLAG) * 16384)) +
                        (lane & 2) * 32 + (lane & 1) * 32 + (lane >> 2);
    f32x4 x[8][2];
#pragma unroll
    for (int ww = 0; ww < 8; ++ww) { x[ww][0] = part[ww * 128]; x[ww][1] = part[ww * 128 + 16]; }
#pragma unroll
    for (int u = 0; u < 2; ++u) {
      const f32x4 a = ((x[0][u] + x[1][u]) + (x[2][u] + x[3][u])) + ((x[4][u] + x[5][u]) + (x[6][u] + x[7][u]));
      sv[2 * u] = cvtpk(a[0], a[1]); sv[2 * u + 1] = cvtpk(a[2], a[3]);
    }
  }
  st_out(p, e, combine(sv, av, xv));
  if (j == 0 && tid == 0) st2_sc1(rsy, SY_EP, tag);
  if (tid == 128) { LST(400 + j); }
}

__global__ __launch_bounds__(512, 2) void main_kernel(Args p) {
  char* smem = g_smem;
  p.ws = ws_align(p.ws);
  const int bx = blockIdx.x & 7, bk = blockIdx.x >> 3;
  const bool isg = (bx ^ XPROBE) & 4;
  const int b = (isg ? NCHAIN : 0) + bk * 4 + (bx & 3);
  if (b == 64 && threadIdx.x == 0) { LST(500); }
  if (threadIdx.x < 64) {
    unsigned long long pc; unsigned len, keep;
    asm volatile("s_getpc_b64 %0\n.Lpc%=:\n\ts_mov_b32 %1, .Lcode_end-.Lpc%=" : "=s"(pc), "=s"(len));
    asm volatile("s_mov_b32 %0, m0\n\ts_mov_b32 m0, %3\n\ts_nop 0\n\tbuffer_load_dword %1, %2, 0 offen lds\n\ts_mov_b32 m0, %0"
                 : "=&s"(keep) : "v"((bk * 40 + threadIdx.x) * 128), "s"(raw_rsrc((const void*)pc, len)), "s"((b >> 6) == 1 ? 2048 : 163328) : "memory");
  }
  if (b >= NCHAIN) gemm_role(p, b - NCHAIN, smem);
  else if (b < 32) qk_role(p, (b & 3) * 8 + bk, smem);
  else if (b < 64) vu_role(p, (b & 3) * 8 + bk - 8, smem);
  else red_role(p, b - 64, smem);
}

extern "C" void kernel_launch(void* const* d_in, const int* in_sizes, int n_in,
                              void* d_out, int out_size, void* d_ws, size_t ws_size,
                              hipStream_t stream) {
  Args a;
  a.x = (const u16*)d_in[0]; a.mem0 = (const u16*)d_in[1];
  a.Wq = (const u16*)d_in[2]; a.Wk = (const u16*)d_in[3]; a.Wv = (const u16*)d_in[4]; a.Wo = (const u16*)d_in[5];
  a.wg = (const u16*)d_in[8]; a.wu = (const u16*)d_in[9]; a.wd = (const u16*)d_in[10];
  a.ws = (char*)d_ws; a.out = (u16*)d_out; a.s0 = 0; a.pad1 = 0;
  main_kernel<<<256, 512, 0, stream>>>(a);
}
```

```cpp
#include <hip/hip_runtime.h>
#include <hip/hip_bf16.h>
#include <stdint.h>

constexpr int S_LEN = 2048, DM = 1024, NH = 32, DH = 128, NMEM = 8, SEG = 32;
constexpr int NSEG = S_LEN / SEG;
constexpr int HD = NH * DH;
constexpr int FF = 3584;

typedef unsigned short u16;
typedef __attribute__((ext_vector_type(8))) short bf16x8;
typedef __attribute__((ext_vector_type(4))) short s16x4;
typedef __attribute__((ext_vector_type(4))) float f32x4;
typedef __attribute__((ext_vector_type(4))) unsigned int u32x4;
typedef __attribute__((ext_vector_type(2))) unsigned int u32x2;
typedef __attribute__((address_space(3))) s16x4* lds_s16x4_ptr;
typedef __attribute__((address_space(3))) void* lds_void_ptr;
typedef __attribute__((address_space(1))) unsigned gu32;
typedef __bf16 bf16x2_t __attribute__((ext_vector_type(2)));
typedef float f32x2_t __attribute__((ext_vector_type(2)));

__device__ __forceinline__ unsigned cvtpk(float lo, float hi) {
  bf16x2_t r = __builtin_convertvector((f32x2_t){lo, hi}, bf16x2_t);
  return __builtin_bit_cast(unsigned, r);
}
__device__ __forceinline__ float lo_f(unsigned v) { return __uint_as_float(v << 16); }
__device__ __forceinline__ float hi_f(unsigned v) { return __uint_as_float(v & 0xffff0000u); }
__device__ __forceinline__ float rbf(float f) { return lo_f(cvtpk(f, f)); }
__device__ __forceinline__ bf16x8 as_bf16x8(u32x4 v) { return __builtin_bit_cast(bf16x8, v); }
#define MFMA(a, b, c) __builtin_amdgcn_mfma_f32_16x16x32_bf16((a), (b), (c), 0, 0, 0)

#define KSTR 65
#define XLD 1024
constexpr int PAGE = 2 << 20, NPAGE = 14;
constexpr int T_K = 0, T_Q = 2304, T_V = 4608, T_O = 6912, T_END = T_O + 2048;
constexpr unsigned T_BYTES = T_END * 16384u;
constexpr size_t WS_SY = 0;
constexpr size_t WS_T = (size_t)NPAGE * PAGE;
constexpr size_t WS_XN = WS_T + T_BYTES;
constexpr size_t WS_HF = WS_XN + (size_t)S_LEN * XLD * 2;
constexpr size_t WS_G = WS_HF + (size_t)S_LEN * 16384;
constexpr size_t WS_S = WS_G + (size_t)S_LEN * DM * 2;
constexpr size_t WS_END = WS_S + (size_t)S_LEN * DM * 2;
constexpr unsigned SENT = 0xFFFFFFFFu;
constexpr unsigned DSENT = 0xFFBFFFBFu;
constexpr int SY_MEM = 0;
constexpr int SY_O = 2 * PAGE;
constexpr int SY_V = 6 * PAGE;
constexpr int SY_BR = PAGE, SY_BA = SY_BR + 16384;
constexpr int SY_FKQ = SY_BR + 32768;
constexpr int SY_FG = SY_FKQ + NH * 4 * 128;
constexpr int SY_FO = SY_FG + 16 * 8 * 128;
constexpr int SY_CH = SY_FO + 384 * 128;
constexpr int SY_ABORT = SY_CH + 8 * 128;
constexpr int SY_FS = SY_ABORT + 256;
constexpr int SY_FEND = SY_FS + 64 * 128;
constexpr int SY_EP = SY_FEND, SY_BT = SY_EP + 128;
constexpr int SY_TOTAL = NPAGE * PAGE;
constexpr int PROBE = PAGE - 16384 + 6144;
__device__ __forceinline__ int side(unsigned pm, int page) { return ((pm >> page) & 1u) << 13; }
__device__ __forceinline__ int o_mem(unsigned pm, int s) { return SY_MEM + s * 32768 + side(pm, 0); }
__device__ __forceinline__ int o_o(unsigned pm, int s, int h) {
  return SY_O + s * 131072 + (h >> 3) * 16384 + ((h >> 1) & 3) * 2048 + (h & 1) * 128 + side(pm, 2 + (s >> 4));
}
__device__ __forceinline__ int o_v(unsigned pm, int s, int h) { return SY_V + s * 262144 + (h >> 1) * 16384 + (h & 1) * 4096 + side(pm, 6 + (s >> 3)); }
__device__ __forceinline__ char* ws_align(char* ws) { return ws + ((PAGE - (unsigned)(uintptr_t)ws) & (PAGE - 1)); }
#define NFUSE 12
constexpr int NPRE = 4;
static_assert(WS_END + PAGE <= (size_t)256 << 20 && NH * KSTR <= T_Q && NPAGE % 2 == 0, "");
constexpr int NCHAIN = 128;
__host__ __device__ constexpr int kseg(int sg, int h) { return h * KSTR + sg; }
__host__ __device__ constexpr int fkq(int h, int kind) { return SY_FKQ + (h * 4 + kind) * 128; }

#define XPROBE 0
struct Args {
  const u16 *x, *mem0, *Wq, *Wk, *Wv, *Wo, *wg, *wu, *wd;
  char* ws;
  u16* out;
  int s0, pad1;
};
__device__ __forceinline__ int h_off(const Args& p, int rblk) { return rblk * PAGE + (((p.s0 ^ rblk ^ 1) & 1) << 13); }
static_assert(WS_HF % (2 * PAGE) == 0, "");
#define TF_KQV 1
#define TF_O 0
__device__ __forceinline__ int t_off(const Args& p, int t) { return t * 16384 + (((p.s0 ^ (t >> 7) ^ (TF_KQV == 2 ? t : TF_KQV)) & 1) << 13); }
__device__ __forceinline__ int t_off_o(const Args& p, int t) { return t * 16384 + (((p.s0 ^ (t >> 7) ^ (TF_O == 2 ? t : TF_O)) & 1) << 13); }

__device__ __forceinline__ float wave_sum(float v) {
#pragma unroll
  for (int i = 0; i < 4; ++i) {
    const int x = (int)__float_as_uint(v);
    v += __uint_as_float(i == 0 ? __builtin_amdgcn_update_dpp(0, x, 0x128, 0xf, 0xf, false) : i == 1 ? __builtin_amdgcn_update_dpp(0, x, 0x124, 0xf, 0xf, false)
                         : i == 2 ? __builtin_amdgcn_update_dpp(0, x, 0x122, 0xf, 0xf, false) : __builtin_amdgcn_update_dpp(0, x, 0x121, 0xf, 0xf, false));
  }
  auto r = __builtin_amdgcn_permlane16_swap(__float_as_uint(v), __float_as_uint(v), false, false);
  v = __uint_as_float(r[0]) + __uint_as_float(r[1]);
  r = __builtin_amdgcn_permlane32_swap(__float_as_uint(v), __float_as_uint(v), false, false);
  return __uint_as_float(r[0]) + __uint_as_float(r[1]);
}

__shared__ __attribute__((aligned(16))) char g_smem[163840];
__device__ __forceinline__ unsigned lds_off(const void* p) {
  return (unsigned)(uintptr_t)(lds_void_ptr)g_smem + (unsigned)((const char*)p - g_smem);
}
__device__ __forceinline__ __amdgpu_buffer_rsrc_t mkrsrc(const void* p, unsigned bytes) {
  return __builtin_amdgcn_make_buffer_rsrc((void*)p, 0, bytes, 0x00020000);
}
__device__ __forceinline__ u32x4 ld_sc1(__amdgpu_buffer_rsrc_t rs, int off) {
  return __builtin_amdgcn_raw_buffer_load_b128(rs, off, 0, 16);
}
__device__ __forceinline__ void st_sc1(__amdgpu_buffer_rsrc_t rs, int off, u32x4 v) {
  __builtin_amdgcn_raw_buffer_store_b128(v, rs, off, 0, 16);
}
__device__ __forceinline__ void swap16(unsigned& x, unsigned& y) {
  auto r = __builtin_amdgcn_permlane16_swap(x, y, false, false);
  x = r[0]; y = r[1];
}
__device__ __forceinline__ void swap32(unsigned& x, unsigned& y) {
  auto r = __builtin_amdgcn_permlane32_swap(x, y, false, false);
  x = r[0]; y = r[1];
}
__device__ __forceinline__ bool spin_fail(unsigned& spins, gu32* abortw) {
  ++spins;
  if ((spins & 63u) == 0u) {
    const __amdgpu_buffer_rsrc_t ra = mkrsrc((const void*)abortw, 4);
    unsigned z = 0;
    asm volatile("" : "+v"(z));
    if (__builtin_amdgcn_readfirstlane(__builtin_amdgcn_raw_buffer_load_b32(ra, z, 0, 16)) == 1u) return true;
    if (spins > (1u << 19)) {
      __builtin_amdgcn_raw_buffer_store_b32(z + 1u, ra, z, 0, 16);
      return true;
    }
  }
  return false;
}
__device__ __forceinline__ bf16x8 ldsf(const char* p) { return *reinterpret_cast<const bf16x8*>(p); }
#define SCHED() __builtin_amdgcn_sched_barrier(0)
#define LBAR() do { asm volatile("s_waitcnt lgkmcnt(0)" ::: "memory"); __builtin_amdgcn_s_barrier(); asm volatile("" ::: "memory"); } while (0)
#define WAIT_V(n) asm volatile("s_waitcnt vmcnt(%0)" :: "n"(n) : "memory")

#define HOLD_V(c, n) asm volatile("s_cmp_eq_u32 %0, 0\n\ts_cbranch_scc1 .Lh%=\n\ts_waitcnt vmcnt(%1)\n.Lh%=:" :: "s"((int)(c)), "n"(n) : "memory", "scc")
#define TH_Q 2
__device__ __forceinline__ void wait_flags(__amdgpu_buffer_rsrc_t rs, int off, gu32* abortw) {
  for (unsigned spins = 0;;) {
    asm volatile("" ::: "memory");
    const unsigned v = __builtin_amdgcn_raw_buffer_load_b32(rs, off, 0, 16);
    if (__all(v != SENT)) break;
    __builtin_amdgcn_s_sleep(4);
    if (spin_fail(spins, abortw)) break;
  }
  asm volatile("" ::: "memory");
}

__device__ __forceinline__ unsigned offA(unsigned row, unsigned ch) {
  return row * 128u + 16u * (ch ^ (row & 7u));
}
__device__ __forceinline__ unsigned swzB(unsigned row) { return ((row & 3u) << 2) | ((row >> 2) & 3u); }
__device__ __forceinline__ unsigned offB(unsigned row, unsigned ch) {
  return 256u * row + 16u * (ch ^ swzB(row));
}
__device__ __forceinline__ void glds16(const u16* src, char* dst) {
  const unsigned d = __builtin_amdgcn_readfirstlane(lds_off(dst));
  unsigned keep;
  asm volatile("s_mov_b32 %0, m0\n\ts_mov_b32 m0, %2\n\ts_nop 0\n\tglobal_load_lds_dwordx4 %1, off\n\ts_mov_b32 m0, %0"
               : "=&s"(keep) : "v"(src), "s"(d) : "memory");
}
__device__ __forceinline__ bf16x8 ld_bfrag(const char* sb, int ks, int c, int lane) {
  const unsigned g = lane >> 4, qq = (lane & 15) >> 2, pp = lane & 3;
  s16x4 lo = __builtin_amdgcn_ds_read_tr16_b64_v4i16(
      (lds_s16x4_ptr)(sb + offB(32 * ks + 8 * g + qq, 2 * c + (pp >> 1)) + 8 * (pp & 1)));
  s16x4 hi = __builtin_amdgcn_ds_read_tr16_b64_v4i16(
      (lds_s16x4_ptr)(sb + offB(32 * ks + 8 * g + 4 + qq, 2 * c + (pp >> 1)) + 8 * (pp & 1)));
  return (bf16x8){lo[0], lo[1], lo[2], lo[3], hi[0], hi[1], hi[2], hi[3]};
}

__device__ __forceinline__ void blk_dma(const u16* src, size_t ld, char* slot, int lane) {
  const u16* s = src + (size_t)(lane >> 2) * ld + (lane & 3) * 8;
  glds16(s, slot);
  glds16(s + 16 * ld, slot + 1024);
}
template <int NB, typename ISSUE, typename SINK>
__device__ __forceinline__ void load_blocks(char* slice, ISSUE&& issue, SINK&& sink) {
#pragma unroll
  for (int i = 0; i < 8; ++i) issue(i, slice + i * 2048);
#pragma unroll
  for (int g = 0; g < NB / 4; ++g) {
    if (g + 1 < NB / 4) WAIT_V(8); else WAIT_V(0);
#pragma unroll
    for (int j = 0; j < 4; ++j) sink(4 * g + j, slice + ((4 * g + j) & 7) * 2048);
    asm volatile("s_waitcnt lgkmcnt(0)" ::: "memory");
    if (g + 2 < NB / 4) {
#pragma unroll
      for (int j = 0; j < 4; ++j) issue(4 * g + 8 + j, slice + ((4 * g + j) & 7) * 2048);
    }
  }
}

__device__ __forceinline__ u32x4 widen32(unsigned a0, unsigned a1, unsigned b0, unsigned b1) {
  swap32(a0, b0); swap16(a0, b0);
  swap32(a1, b1); swap16(a1, b1);
  return (u32x4){a0, a1, b0, b1};
}

__device__ __forceinline__ void publish_flags(__amdgpu_buffer_rsrc_t rs, int off, int stride, int n) {
  WAIT_V(0);
  LBAR();
  int tid = threadIdx.x;
  asm volatile("" : "+v"(tid));
  if (tid < n) __builtin_amdgcn_raw_buffer_store_b32(1u, rs, off + tid * stride, 0, 16);
}

__device__ __forceinline__ void wait_s(const Args& p, __amdgpu_buffer_rsrc_t rsy, int rblk, int c0, int n, bool need) {
  if (need) {
    if (threadIdx.x < 64) {
      for (unsigned spins = 0;;) {
        asm volatile("" ::: "memory");
        const int f = (int)__builtin_amdgcn_raw_buffer_load_b32(rsy, SY_FS + (2 * c0 + (threadIdx.x & (2 * n - 1))) * 128, 0, 16);
        if (__all(f >= rblk)) break;
        __builtin_amdgcn_s_sleep(32);
        if (spin_fail(spins, (gu32*)(p.ws + WS_SY + SY_ABORT))) break;
      }
    }
    LBAR();
  }
}
__device__ __forceinline__ u32x4 combine(u32x4 sv, u32x4 g, u32x4 x) {
  u32x4 o;
#pragma unroll
  for (int j = 0; j < 4; ++j) {
    const unsigned t = cvtpk(lo_f(sv[j]) * lo_f(g[j]), hi_f(sv[j]) * hi_f(g[j]));
    o[j] = cvtpk(lo_f(x[j]) + lo_f(t), hi_f(x[j]) + hi_f(t));
  }
  return o;
}
#define OUT_AUX 0
__device__ __forceinline__ void st_out(const Args& p, int e, u32x4 v) {
  __builtin_amdgcn_raw_buffer_store_b128(v, mkrsrc(p.out, S_LEN * DM * 2), e * 2, 0, OUT_AUX);
}
struct SX { u32x4 s, x; };
__device__ __forceinline__ SX get_sx(const Args& p, int rblk, int e) {
  SX r; r.s = (u32x4){0u, 0u, 0u, 0u}; r.x = r.s;
  if (rblk < NFUSE) {
    r.s = ld_sc1(mkrsrc(p.ws + WS_S, S_LEN * DM * 2), (((e & 1023) >> 5) * S_LEN + (e >> 10)) * 64 + (e & 31) * 2);
    r.x = *reinterpret_cast<const u32x4*>(p.x + e);
  }
  return r;
}
__device__ __forceinline__ void put_g(const Args& p, __amdgpu_buffer_rsrc_t rg, int rblk, int e, u32x4 g, const SX& v) {
  if (rblk < NFUSE) {
    st_out(p, e, combine(v.s, g, v.x));
  } else st_sc1(rg, e * 2, g);
}

__device__ __forceinline__ void job_comb(const Args& p, int rblk, int ct, __amdgpu_buffer_rsrc_t rsy) {
  int tid = threadIdx.x;
  asm volatile("" : "+v"(tid));
  u32x4 xv[8];
#pragma unroll
  for (int i = 0; i < 8; ++i) xv[i] = *reinterpret_cast<const u32x4*>(p.x + (rblk * 128 + i * 16 + (tid >> 5)) * DM + ct * 256 + (tid & 31) * 8);
  if (tid < 64) wait_flags(rsy, SY_FG + (rblk * 8 + 2 * ct + (tid & 1)) * 128, (gu32*)(p.ws + WS_SY + SY_ABORT));
  wait_s(p, rsy, rblk, ct * 8, 8, true);
  u32x4 sv[8], gv[8];
#pragma unroll
  for (int i = 0; i < 8; ++i) {
    const int e = (rblk * 128 + i * 16 + (tid >> 5)) * DM + ct * 256 + (tid & 31) * 8;
    sv[i] = ld_sc1(mkrsrc(p.ws + WS_S, S_LEN * DM * 2), (((e & 1023) >> 5) * S_LEN + (e >> 10)) * 64 + (e & 31) * 2);
    gv[i] = ld_sc1(mkrsrc(p.ws + WS_G, S_LEN * DM * 2), e * 2);
  }
#pragma unroll
  for (int i = 0; i < 8; ++i)
    st_out(p, (rblk * 128 + i * 16 + (tid >> 5)) * DM + ct * 256 + (tid & 31) * 8, combine(sv[i], gv[i], xv[i]));
}

__device__ __forceinline__ u32x4 raw_rsrc(const void* p, unsigned bytes) {
  const unsigned long long a = (unsigned long long)p;
  return (u32x4){(unsigned)a, (unsigned)(a >> 32) & 0xffffu, bytes, 0x00020000u};
}
__device__ __forceinline__ u32x2 next_tag(u32x2 e) { return (u32x2){e[0] + 1u, e[1] + (e[0] == 0xFFFFFFFFu)}; }
struct Boot { u32x2 e; u32x4 c; };
__device__ __forceinline__ void boot_ask(const char* ws, Boot& r) {
  const unsigned o = (threadIdx.x & 63) * 128;
  asm volatile("buffer_load_dwordx2 %0, off, %3, %4 sc1\n\tbuffer_load_dwordx4 %1, %2, %3, %5 offen sc1"
               : "=&v"(r.e), "=&v"(r.c) : "v"(o), "s"(raw_rsrc(ws, SY_TOTAL)), "s"(SY_EP), "s"(SY_BT) : "memory");
}
__device__ __forceinline__ void wait_rows(const char* ws, u32x2 tag) {
  const __amdgpu_buffer_rsrc_t rs = mkrsrc(ws, SY_TOTAL);
  int o = (threadIdx.x & 63) * 128;
  asm volatile("" : "+v"(o));
#pragma unroll 1
  for (int i = 0; i < (1 << 17); ++i) {
    asm volatile("" ::: "memory");
    const u32x2 a = __builtin_amdgcn_raw_buffer_load_b64(rs, o, SY_BT + 32 * 128, 16), b = __builtin_amdgcn_raw_buffer_load_b64(rs, o, SY_BT + 96 * 128, 16);
    if (__all(a[0] == tag[0] && a[1] == tag[1] && b[0] == tag[0] && b[1] == tag[1])) break;
    __builtin_amdgcn_s_sleep(8);
  }
  asm volatile("" ::: "memory");
}
__device__ __forceinline__ unsigned boot_done(const char* ws, Boot& r, bool own = false, u32x2 tag = (u32x2){0u, 0u}) {
#pragma unroll 1
  for (int i = 0; i < (1 << 17); ++i) {
    asm volatile("" : "+v"(r.e), "+v"(r.c));
    if (!own) tag = next_tag(r.e);
    if (__all(r.c[0] == tag[0] && r.c[1] == tag[1] && (r.c[2] ^ r.c[3]) == tag[0])) break;
    __builtin_amdgcn_s_sleep(8);
    boot_ask(ws, r);
    WAIT_V(0);
  }
  unsigned q0[NPAGE], q1[NPAGE], ga = 0xFFFFu, gb = 0xFFFFu;
#pragma unroll
  for (int i = 0; i < NPAGE; ++i) {
    const unsigned x = __builtin_amdgcn_readlane(r.c[2], i), y = __builtin_amdgcn_readlane(r.c[2], i + NPAGE);
    q0[i] = min(x >> 16, y >> 16); q1[i] = min(x & 0xFFFFu, y & 0xFFFFu);
    ga = min(ga, (i & 1) ? q1[i] : q0[i]); gb = min(gb, (i & 1) ? q0[i] : q1[i]);
  }
  unsigned m = gb < ga ? 0x80001555u : 0x2AAAu;
  const unsigned thr = min(ga, gb) + 110;
#pragma unroll
  for (int i = 0; i < NPAGE; ++i) {
    const unsigned lo = min(q0[i], q1[i]), hi = max(q0[i], q1[i]);
    const unsigned keep = (lo < thr ? hi : 0u) > lo + 100 ? ~(1u << i) : ~0u;
    m = (m & keep) | ((q1[i] < q0[i] ? ~0u : 0u) & ~keep);
  }
  return m;
}
typedef __attribute__((address_space(3))) volatile unsigned* lds_vu_ptr;
__device__ __forceinline__ lds_vu_ptr lds_at(const void* p) {
  unsigned a = lds_off(p);
  asm volatile("" : "+v"(a));
  return (lds_vu_ptr)(uintptr_t)a;
}
__device__ __forceinline__ unsigned uni(unsigned v) {
  unsigned r;
  asm volatile("s_nop 1\n\tv_readfirstlane_b32 %0, %1\n\ts_nop 3" : "=s"(r) : "v"(v));
  return r;
}
__device__ __forceinline__ void start_sy(char* ws, const u16* mem0, int i, char* smem) {
  const int tid = threadIdx.x, lane = tid & 63, w = __builtin_amdgcn_readfirstlane(tid >> 6);
  const __amdgpu_buffer_rsrc_t rsy = mkrsrc(ws, SY_TOTAL);
  const __amdgpu_buffer_rsrc_t rs = mkrsrc(ws + (size_t)(i % NPAGE) * PAGE + PROBE - (i / NPAGE) * 2048, 16384);
  const u32x2 e = __builtin_amdgcn_raw_buffer_load_b64(rsy, 0, SY_EP, 16);
  const u32x4 ds = {DSENT, DSENT, DSENT, DSENT};
  if (lane < 32) st_sc1(rs, (lane >> 4) * 8192 + w * 256 + (lane & 15) * 16, ds);
  static_assert(NPRE == 4 && SY_FEND - SY_BR <= 32 * 6144, "");
#pragma unroll
  for (int u = 0; u < 6; ++u)
    st_sc1(rsy, (u < 4 ? SY_V + (i * 4 + u) * 8192 : SY_O + (i * 2 + u - 4) * 8192) + tid * 16, ds);
  if (w == 7) {
    const int n = i * 64 + lane, j = (n >> 10) * 512 + (n & 511);
    const unsigned* m0 = reinterpret_cast<const unsigned*>(mem0 + (2 * ((j >> 2) & 3) + (j & 1)) * DM + (j >> 4) * 16) + ((j >> 1) & 1);
    u32x4 m;
#pragma unroll
    for (int k = 0; k < 4; ++k) { const unsigned t = m0[2 * k]; m[k] = t == DSENT ? 0xFFFFFFFFu : t; }
    st_sc1(rsy, n * 16, m);
  } else if (w == 6) st_sc1(rsy, 32768 + i * 1024 + lane * 16, ds);
  else {
    const int o = SY_BR + i * 6144 + tid * 16;
    st_sc1(rsy, o < SY_FEND ? o : 0x7F000000, (u32x4){SENT, SENT, SENT, SENT});
  }
  unsigned t[4];
#pragma unroll
  for (int k = 0; k < 4; ++k) {
    unsigned long long c0, c1; unsigned v, off = ((k ^ (k >> 1)) & 1) * 8192 + w * 256 + (k >> 1) * 128;
    asm volatile("s_waitcnt vmcnt(0)\n\ts_barrier\n\ts_memtime %0\n\ts_waitcnt lgkmcnt(0)\n\tbuffer_load_dword %2, %3, %4, 0 offen sc1\n\ts_waitcnt vmcnt(0)\n\t"
                 "s_memtime %1\n\ts_waitcnt lgkmcnt(0)" : "=&s"(c0), "=&s"(c1), "=&v"(v) : "v"(off), "s"(rs) : "memory");
    t[k] = (unsigned)(c1 - c0);
  }
  lds_vu_ptr tm = lds_at(smem);
  if (lane == 0) { tm[w] = min(t[0], t[3]); tm[8 + w] = min(t[1], t[2]); }
  int ro = SY_BT + i * 128 + lane * 16;
  asm volatile("" : "+v"(ro));
  LBAR();
  if (threadIdx.x < 8) {
    unsigned m0 = tm[0], m1 = tm[8];
#pragma unroll
    for (int k = 1; k < 8; ++k) { m0 = min(m0, tm[k]); m1 = min(m1, tm[8 + k]); }
    const u32x2 tag = next_tag(e);
    const unsigned v = min(m0, 30000u) << 16 | min(m1, 30000u);
    st_sc1(rsy, ro, (u32x4){tag[0], tag[1], v, v ^ tag[0]});
  }
}
template <int NB, typename F>
__device__ __forceinline__ u32x2 start_rows(const Args& p, int g, unsigned on, F&& between) {
  const int tid = threadIdx.x, lane = tid & 63, row0 = g * 16 + (tid >> 6) * 2;
  u32x4 a[4];
  u32x2 e;
  asm volatile("buffer_load_dwordx4 %0, %5, %8, 0 offen\n\tbuffer_load_dwordx4 %1, %5, %8, 0 offen offset:1024\n\t"
               "buffer_load_dwordx4 %2, %5, %8, 0 offen offset:2048\n\tbuffer_load_dwordx4 %3, %5, %8, 0 offen offset:3072\n\t"
               "buffer_load_dwordx2 %4, off, %6, %7 sc1"
               : "=&v"(a[0]), "=&v"(a[1]), "=&v"(a[2]), "=&v"(a[3]), "=&v"(e)
               : "v"(row0 * (DM * 2) + lane * 16), "s"(raw_rsrc(p.ws, SY_TOTAL)), "s"(SY_EP), "s"(raw_rsrc(p.x, on * (S_LEN * DM * 2))) : "memory");
  between();
  asm volatile("s_waitcnt vmcnt(%5)" : "+v"(a[0]), "+v"(a[1]), "+v"(a[2]), "+v"(a[3]), "+v"(e) : "n"(NB) : "memory");
  const __amdgpu_buffer_rsrc_t ro = mkrsrc(p.ws + WS_XN, on * (S_LEN * XLD * 2));
#pragma unroll
  for (int u = 0; u < 2; ++u) {
    float v[16];
#pragma unroll
    for (int i = 0; i < 8; ++i) { v[2 * i] = lo_f(a[2 * u + (i >> 2)][i & 3]); v[2 * i + 1] = hi_f(a[2 * u + (i >> 2)][i & 3]); }
    float s = 0.f;
#pragma unroll
    for (int i = 0; i < 16; ++i) s += v[i];
    const float mean = wave_sum(s) * (1.0f / DM);
    float q = 0.f;
#pragma unroll
    for (int i = 0; i < 16; ++i) { float d = v[i] - mean; q += d * d; }
    const float var = rbf(wave_sum(q) * (1.0f / DM));
    const float mb = rbf(mean);
    const float inv = 1.0f / rbf(sqrtf(rbf(var + 1e-5f)));
#pragma unroll
    for (int k = 0; k < 2; ++k) {
      u32x4 o;
#pragma unroll
      for (int i = 0; i < 4; ++i) {
        const unsigned t = cvtpk(v[8 * k + 2 * i] - mb, v[8 * k + 2 * i + 1] - mb);
        o[i] = cvtpk(lo_f(t) * inv, hi_f(t) * inv);
      }
      st_sc1(ro, (row0 + u) * (XLD * 2) + k * 1024 + lane * 16, o);
    }
  }
  return next_tag(e);
}
__device__ __forceinline__ void report_rows(const Args& p, int g, u32x2 tag) {
  if (threadIdx.x < 8) st_sc1(mkrsrc(p.ws + WS_SY, SY_TOTAL), SY_BT + (32 + g) * 128 + threadIdx.x * 16, (u32x4){tag[0], tag[1], 0u, tag[0]});
}

typedef __attribute__((ext_vector_type(32))) unsigned u32x32;
__device__ __forceinline__ void put4(u32x32& W, int i, bf16x8 f) {
  const u32x4 v = __builtin_bit_cast(u32x4, f);
#pragma unroll
  for (int j = 0; j < 4; ++j) W[4 * i + j] = v[j];
}
#define CLB8(a, b, c, d, e, f, g, h) "v" #a, "v" #b, "v" #c, "v" #d, "v" #e, "v" #f, "v" #g, "v" #h
#define CLB32(a) CLB32_##a
#define CLB32_128 CLB8(128, 129, 130, 131, 132, 133, 134, 135), CLB8(136, 137, 138, 139, 140, 141, 142, 143), CLB8(144, 145, 146, 147, 148, 149, 150, 151), CLB8(152, 153, 154, 155, 156, 157, 158, 159)
#define CLB32_160 CLB8(160, 161, 162, 163, 164, 165, 166, 167), CLB8(168, 169, 170, 171, 172, 173, 174, 175), CLB8(176, 177, 178, 179, 180, 181, 182, 183), CLB8(184, 185, 186, 187, 188, 189, 190, 191)
#define W4_IN "{v[0:31]}"(W[0]), "{v[32:63]}"(W[1]), "{v[64:95]}"(W[2]), "{v[96:127]}"(W[3])
#define CLB32_192 CLB8(192, 193, 194, 195, 196, 197, 198, 199), CLB8(200, 201, 202, 203, 204, 205, 206, 207), CLB8(208, 209, 210, 211, 212, 213, 214, 215), CLB8(216, 217, 218, 219, 220, 221, 222, 223)
__device__ __forceinline__ unsigned ld_flag0(__amdgpu_buffer_rsrc_t rs, int off) {
  return __builtin_amdgcn_raw_buffer_load_b32(rs, off, 0, 16);
}
__device__ __forceinline__ void glds16_sc1(const char* src, char* dst) {
  const unsigned d = __builtin_amdgcn_readfirstlane(lds_off(dst));
  unsigned keep;
  asm volatile("s_mov_b32 %0, m0\n\ts_mov_b32 m0, %2\n\ts_nop 0\n\tglobal_load_lds_dwordx4 %1, off sc1\n\ts_mov_b32 m0, %0"
               : "=&s"(keep) : "v"(src), "s"(d) : "memory");
}

__device__ __forceinline__ void touch_line(const char* src, char* dst) {
  const unsigned d = __builtin_amdgcn_readfirstlane(lds_off(dst));
  unsigned keep;
  asm volatile("s_mov_b32 %0, m0\n\ts_mov_b32 m0, %2\n\ts_nop 0\n\tglobal_load_lds_dword %1, off sc1\n\ts_mov_b32 m0, %0"
               : "=&s"(keep) : "v"(src), "s"(d) : "memory");
}

#define PF_N 12
#define LN_M 3
#define LN_X 1
#define PR_G1 8
#define WREV 1
template <bool DEV = false>
__device__ __forceinline__ void glds2(const char* base, int step, unsigned voff, unsigned dst) {
  unsigned keep;
  const char* base2 = base + step;
  if (DEV)
    asm volatile("s_mov_b32 %0, m0\n\ts_mov_b32 m0, %4\n\ts_nop 0\n\tglobal_load_lds_dwordx4 %1, %2 sc1\n\t"
                 "s_add_u32 m0, m0, 0x2000\n\ts_nop 0\n\tglobal_load_lds_dwordx4 %1, %3 sc1\n\ts_mov_b32 m0, %0"
                 : "=&s"(keep) : "v"(voff), "s"(base), "s"(base2), "s"(dst) : "memory", "scc");
  else
  asm volatile("s_mov_b32 %0, m0\n\ts_mov_b32 m0, %4\n\ts_nop 0\n\tglobal_load_lds_dwordx4 %1, %2\n\t"
               "s_add_u32 m0, m0, 0x2000\n\ts_nop 0\n\tglobal_load_lds_dwordx4 %1, %3\n\ts_mov_b32 m0, %0"
               : "=&s"(keep) : "v"(voff), "s"(base), "s"(base2), "s"(dst) : "memory", "scc");
}

#define DN_ASM \
  "s_mov_b32 s94, m0\n\t" \
  "s_mov_b64 s[84:85], %[pa]\n\t" \
  "s_mov_b64 s[86:87], %[pb]\n\t" \
  "s_movk_i32 s88, 0x4000\n\t" \
  "s_mov_b32 s89, 0x10000\n\t" \
  "s_movk_i32 s90, 13\n\t" \
  "v_mov_b64 v[0:1], 0\n\t" \
  "v_mov_b64 v[2:3], 0\n\t" \
  "v_mov_b64 v[4:5], 0\n\t" \
  "v_mov_b64 v[6:7], 0\n\t" \
  "v_mov_b64 v[8:9], 0\n\t" \
  "v_mov_b64 v[10:11], 0\n\t" \
  "v_mov_b64 v[12:13], 0\n\t" \
  "v_mov_b64 v[14:15], 0\n\t" \
  "v_mov_b64 v[16:17], 0\n\t" \
  "v_mov_b64 v[18:19], 0\n\t" \
  "v_mov_b64 v[20:21], 0\n\t" \
  "v_mov_b64 v[22:23], 0\n\t" \
  "v_mov_b64 v[24:25], 0\n\t" \
  "v_mov_b64 v[26:27], 0\n\t" \
  "v_mov_b64 v[28:29], 0\n\t" \
  "v_mov_b64 v[30:31], 0\n\t" \
  "s_cmp_lg_u32 %[hf], 0\n\t" \
  "s_cbranch_scc1 .Lk%=\n\t" \
  "ds_read_b128 v[32:35], v80 offset:0\n\t" \
  "ds_read_b128 v[36:39], v80 offset:1024\n\t" \
  "ds_read_b128 v[40:43], v80 offset:2048\n\t" \
  "ds_read_b128 v[44:47], v80 offset:3072\n\t" \
  "ds_read_b64_tr_b16 v[64:65], v82 offset:0\n\t" \
  "ds_read_b64_tr_b16 v[66:67], v83 offset:0\n\t" \
  "ds_read_b128 v[48:51], v80 offset:8192\n\t" \
  "ds_read_b64_tr_b16 v[68:69], v84 offset:0\n\t" \
  "ds_read_b64_tr_b16 v[70:71], v85 offset:0\n\t" \
  "ds_read_b128 v[52:55], v80 offset:9216\n\t" \
  "ds_read_b128 v[56:59], v80 offset:10240\n\t" \
  "ds_read_b128 v[60:63], v80 offset:11264\n\t" \
  ".Lp0%=:\n\t" \
  "s_waitcnt vmcnt(4)\n\t" \
  "s_barrier\n\t" \
  "s_waitcnt lgkmcnt(3)\n\t" \
  "v_mfma_f32_16x16x32_bf16 v[0:3], v[64:67], v[32:35], v[0:3]\n\t" \
  "ds_read_b64_tr_b16 v[72:73], v82 offset:8192\n\t" \
  "v_mfma_f32_16x16x32_bf16 v[4:7], v[68:71], v[32:35], v[4:7]\n\t" \
  "ds_read_b64_tr_b16 v[74:75], v83 offset:8192\n\t" \
  "ds_read_b128 v[32:35], v80 offset:32768\n\t" \
  "s_add_u32 m0, %[lw], 98304\n\t" \
  "v_mfma_f32_16x16x32_bf16 v[8:11], v[64:67], v[36:39], v[8:11]\n\t" \
  "ds_read_b64_tr_b16 v[76:77], v84 offset:8192\n\t" \
  "global_load_lds_dwordx4 v90, s[84:85] sc1\n\t" \
  "s_add_u32 s84, s84, s88\n\t" \
  "s_addc_u32 s85, s85, 0\n\t" \
  "v_mfma_f32_16x16x32_bf16 v[12:15], v[68:71], v[36:39], v[12:15]\n\t" \
  "ds_read_b64_tr_b16 v[78:79], v85 offset:8192\n\t" \
  "ds_read_b128 v[36:39], v80 offset:33792\n\t" \
  "v_mfma_f32_16x16x32_bf16 v[16:19], v[64:67], v[40:43], v[16:19]\n\t" \
  "v_mfma_f32_16x16x32_bf16 v[20:23], v[68:71], v[40:43], v[20:23]\n\t" \
  "ds_read_b128 v[40:43], v80 offset:34816\n\t" \
  "s_add_u32 m0, %[lw], 114688\n\t" \
  "v_mfma_f32_16x16x32_bf16 v[24:27], v[64:67], v[44:47], v[24:27]\n\t" \
  "global_load_lds_dwordx4 v91, s[86:87]\n\t" \
  "s_add_u32 s86, s86, s89\n\t" \
  "s_addc_u32 s87, s87, 0\n\t" \
  "v_mfma_f32_16x16x32_bf16 v[28:31], v[68:71], v[44:47], v[28:31]\n\t" \
  "ds_read_b128 v[44:47], v80 offset:35840\n\t" \
  "s_waitcnt lgkmcnt(3)\n\t" \
  "v_mfma_f32_16x16x32_bf16 v[0:3], v[72:75], v[48:51], v[0:3]\n\t" \
  "ds_read_b64_tr_b16 v[64:65], v82 offset:32768\n\t" \
  "v_mfma_f32_16x16x32_bf16 v[4:7], v[76:79], v[48:51], v[4:7]\n\t" \
  "ds_read_b64_tr_b16 v[66:67], v83 offset:32768\n\t" \
  "ds_read_b128 v[48:51], v80 offset:40960\n\t" \
  "s_add_u32 m0, %[lw], 106496\n\t" \
  "v_mfma_f32_16x16x32_bf16 v[8:11], v[72:75], v[52:55], v[8:11]\n\t" \
  "ds_read_b64_tr_b16 v[68:69], v84 offset:32768\n\t" \
  "global_load_lds_dwordx4 v90, s[84:85] sc1\n\t" \
  "s_add_u32 s84, s84, s88\n\t" \
  "s_addc_u32 s85, s85, 0\n\t" \
  "v_mfma_f32_16x16x32_bf16 v[12:15], v[76:79], v[52:55], v[12:15]\n\t" \
  "ds_read_b64_tr_b16 v[70:71], v85 offset:32768\n\t" \
  "ds_read_b128 v[52:55], v80 offset:41984\n\t" \
  "v_mfma_f32_16x16x32_bf16 v[16:19], v[72:75], v[56:59], v[16:19]\n\t" \
  "v_mfma_f32_16x16x32_bf16 v[20:23], v[76:79], v[56:59], v[20:23]\n\t" \
  "ds_read_b128 v[56:59], v80 offset:43008\n\t" \
  "s_add_u32 m0, %[lw], 122880\n\t" \
  "v_mfma_f32_16x16x32_bf16 v[24:27], v[72:75], v[60:63], v[24:27]\n\t" \
  "global_load_lds_dwordx4 v91, s[86:87]\n\t" \
  "s_add_u32 s86, s86, s89\n\t" \
  "s_addc_u32 s87, s87, 0\n\t" \
  "v_mfma_f32_16x16x32_bf16 v[28:31], v[76:79], v[60:63], v[28:31]\n\t" \
  "ds_read_b128 v[60:63], v80 offset:44032\n\t" \
  "s_sub_u32 s90, s90, 1\n\t" \
  "s_cselect_b32 s88, 0, s88\n\t" \
  "s_cselect_b32 s89, 0, s89\n\t" \
  "s_cselect_b32 s91, 16384, 0\n\t" \
  "s_sub_u32 s84, s84, s91\n\t" \
  "s_subb_u32 s85, s85, 0\n\t" \
  "s_lshl_b32 s91, s91, 2\n\t" \
  "s_sub_u32 s86, s86, s91\n\t" \
  "s_subb_u32 s87, s87, 0\n\t" \
  "s_waitcnt vmcnt(4)\n\t" \
  "s_barrier\n\t" \
  "s_waitcnt lgkmcnt(3)\n\t" \
  "v_mfma_f32_16x16x32_bf16 v[0:3], v[64:67], v[32:35], v[0:3]\n\t" \
  "ds_read_b64_tr_b16 v[72:73], v82 offset:40960\n\t" \
  "v_mfma_f32_16x16x32_bf16 v[4:7], v[68:71], v[32:35], v[4:7]\n\t" \
  "ds_read_b64_tr_b16 v[74:75], v83 offset:40960\n\t" \
  "ds_read_b128 v[32:35], v81 offset:0\n\t" \
  "s_add_u32 m0, %[lw], 0\n\t" \
  "v_mfma_f32_16x16x32_bf16 v[8:11], v[64:67], v[36:39], v[8:11]\n\t" \
  "ds_read_b64_tr_b16 v[76:77], v84 offset:40960\n\t" \
  "global_load_lds_dwordx4 v90, s[84:85] sc1\n\t" \
  "s_add_u32 s84, s84, s88\n\t" \
  "s_addc_u32 s85, s85, 0\n\t" \
  "v_mfma_f32_16x16x32_bf16 v[12:15], v[68:71], v[36:39], v[12:15]\n\t" \
  "ds_read_b64_tr_b16 v[78:79], v85 offset:40960\n\t" \
  "ds_read_b128 v[36:39], v81 offset:1024\n\t" \
  "v_mfma_f32_16x16x32_bf16 v[16:19], v[64:67], v[40:43], v[16:19]\n\t" \
  "v_mfma_f32_16x16x32_bf16 v[20:23], v[68:71], v[40:43], v[20:23]\n\t" \
  "ds_read_b128 v[40:43], v81 offset:2048\n\t" \
  "s_add_u32 m0, %[lw], 16384\n\t" \
  "v_mfma_f32_16x16x32_bf16 v[24:27], v[64:67], v[44:47], v[24:27]\n\t" \
  "global_load_lds_dwordx4 v91, s[86:87]\n\t" \
  "s_add_u32 s86, s86, s89\n\t" \
  "s_addc_u32 s87, s87, 0\n\t" \
  "v_mfma_f32_16x16x32_bf16 v[28:31], v[68:71], v[44:47], v[28:31]\n\t" \
  "ds_read_b128 v[44:47], v81 offset:3072\n\t" \
  "s_waitcnt lgkmcnt(3)\n\t" \
  "v_mfma_f32_16x16x32_bf16 v[0:3], v[72:75], v[48:51], v[0:3]\n\t" \
  "ds_read_b64_tr_b16 v[64:65], v86 offset:0\n\t" \
  "v_mfma_f32_16x16x32_bf16 v[4:7], v[76:79], v[48:51], v[4:7]\n\t" \
  "ds_read_b64_tr_b16 v[66:67], v87 offset:0\n\t" \
  "ds_read_b128 v[48:51], v81 offset:8192\n\t" \
  "s_add_u32 m0, %[lw], 8192\n\t" \
  "v_mfma_f32_16x16x32_bf16 v[8:11], v[72:75], v[52:55], v[8:11]\n\t" \
  "ds_read_b64_tr_b16 v[68:69], v88 offset:0\n\t" \
  "global_load_lds_dwordx4 v90, s[84:85] sc1\n\t" \
  "s_add_u32 s84, s84, s88\n\t" \
  "s_addc_u32 s85, s85, 0\n\t" \
  "v_mfma_f32_16x16x32_bf16 v[12:15], v[76:79], v[52:55], v[12:15]\n\t" \
  "ds_read_b64_tr_b16 v[70:71], v89 offset:0\n\t" \
  "ds_read_b128 v[52:55], v81 offset:9216\n\t" \
  "v_mfma_f32_16x16x32_bf16 v[16:19], v[72:75], v[56:59], v[16:19]\n\t" \
  "v_mfma_f32_16x16x32_bf16 v[20:23], v[76:79], v[56:59], v[20:23]\n\t" \
  "ds_read_b128 v[56:59], v81 offset:10240\n\t" \
  "s_add_u32 m0, %[lw], 24576\n\t" \
  "v_mfma_f32_16x16x32_bf16 v[24:27], v[72:75], v[60:63], v[24:27]\n\t" \
  "global_load_lds_dwordx4 v91, s[86:87]\n\t" \
  "s_add_u32 s86, s86, s89\n\t" \
  "s_addc_u32 s87, s87, 0\n\t" \
  "v_mfma_f32_16x16x32_bf16 v[28:31], v[76:79], v[60:63], v[28:31]\n\t" \
  "ds_read_b128 v[60:63], v81 offset:11264\n\t" \
  "s_waitcnt vmcnt(4)\n\t" \
  "s_barrier\n\t" \
  "s_waitcnt lgkmcnt(3)\n\t" \
  "v_mfma_f32_16x16x32_bf16 v[0:3], v[64:67], v[32:35], v[0:3]\n\t" \
  "ds_read_b64_tr_b16 v[72:73], v86 offset:8192\n\t" \
  "v_mfma_f32_16x16x32_bf16 v[4:7], v[68:71], v[32:35], v[4:7]\n\t" \
  "ds_read_b64_tr_b16 v[74:75], v87 offset:8192\n\t" \
  "ds_read_b128 v[32:35], v81 offset:32768\n\t" \
  "s_add_u32 m0, %[lw], 32768\n\t" \
  "v_mfma_f32_16x16x32_bf16 v[8:11], v[64:67], v[36:39], v[8:11]\n\t" \
  "ds_read_b64_tr_b16 v[76:77], v88 offset:8192\n\t" \
  "global_load_lds_dwordx4 v90, s[84:85] sc1\n\t" \
  "s_add_u32 s84, s84, s88\n\t" \
  "s_addc_u32 s85, s85, 0\n\t" \
  "v_mfma_f32_16x16x32_bf16 v[12:15], v[68:71], v[36:39], v[12:15]\n\t" \
  "ds_read_b64_tr_b16 v[78:79], v89 offset:8192\n\t" \
  "ds_read_b128 v[36:39], v81 offset:33792\n\t" \
  "v_mfma_f32_16x16x32_bf16 v[16:19], v[64:67], v[40:43], v[16:19]\n\t" \
  "v_mfma_f32_16x16x32_bf16 v[20:23], v[68:71], v[40:43], v[20:23]\n\t" \
  "ds_read_b128 v[40:43], v81 offset:34816\n\t" \
  "s_add_u32 m0, %[lw], 49152\n\t" \
  "v_mfma_f32_16x16x32_bf16 v[24:27], v[64:67], v[44:47], v[24:27]\n\t" \
  "global_load_lds_dwordx4 v91, s[86:87]\n\t" \
  "s_add_u32 s86, s86, s89\n\t" \
  "s_addc_u32 s87, s87, 0\n\t" \
  "v_mfma_f32_16x16x32_bf16 v[28:31], v[68:71], v[44:47], v[28:31]\n\t" \
  "ds_read_b128 v[44:47], v81 offset:35840\n\t" \
  "s_waitcnt lgkmcnt(3)\n\t" \
  "v_mfma_f32_16x16x32_bf16 v[0:3], v[72:75], v[48:51], v[0:3]\n\t" \
  "ds_read_b64_tr_b16 v[64:65], v86 offset:32768\n\t" \
  "v_mfma_f32_16x16x32_bf16 v[4:7], v[76:79], v[48:51], v[4:7]\n\t" \
  "ds_read_b64_tr_b16 v[66:67], v87 offset:32768\n\t" \
  "ds_read_b128 v[48:51], v81 offset:40960\n\t" \
  "s_add_u32 m0, %[lw], 40960\n\t" \
  "v_mfma_f32_16x16x32_bf16 v[8:11], v[72:75], v[52:55], v[8:11]\n\t" \
  "ds_read_b64_tr_b16 v[68:69], v88 offset:32768\n\t" \
  "global_load_lds_dwordx4 v90, s[84:85] sc1\n\t" \
  "s_add_u32 s84, s84, s88\n\t" \
  "s_addc_u32 s85, s85, 0\n\t" \
  "v_mfma_f32_16x16x32_bf16 v[12:15], v[76:79], v[52:55], v[12:15]\n\t" \
  "ds_read_b64_tr_b16 v[70:71], v89 offset:32768\n\t" \
  "ds_read_b128 v[52:55], v81 offset:41984\n\t" \
  "v_mfma_f32_16x16x32_bf16 v[16:19], v[72:75], v[56:59], v[16:19]\n\t" \
  "v_mfma_f32_16x16x32_bf16 v[20:23], v[76:79], v[56:59], v[20:23]\n\t" \
  "ds_read_b128 v[56:59], v81 offset:43008\n\t" \
  "s_add_u32 m0, %[lw], 57344\n\t" \
  "v_mfma_f32_16x16x32_bf16 v[24:27], v[72:75], v[60:63], v[24:27]\n\t" \
  "global_load_lds_dwordx4 v91, s[86:87]\n\t" \
  "s_add_u32 s86, s86, s89\n\t" \
  "s_addc_u32 s87, s87, 0\n\t" \
  "v_mfma_f32_16x16x32_bf16 v[28:31], v[76:79], v[60:63], v[28:31]\n\t" \
  "ds_read_b128 v[60:63], v81 offset:44032\n\t" \
  "s_waitcnt vmcnt(4)\n\t" \
  "s_barrier\n\t" \
  "s_waitcnt lgkmcnt(3)\n\t" \
  "v_mfma_f32_16x16x32_bf16 v[0:3], v[64:67], v[32:35], v[0:3]\n\t" \
  "ds_read_b64_tr_b16 v[72:73], v86 offset:40960\n\t" \
  "v_mfma_f32_16x16x32_bf16 v[4:7], v[68:71], v[32:35], v[4:7]\n\t" \
  "ds_read_b64_tr_b16 v[74:75], v87 offset:40960\n\t" \
  "ds_read_b128 v[32:35], v80 offset:0\n\t" \
  "s_add_u32 m0, %[lw], 65536\n\t" \
  "v_mfma_f32_16x16x32_bf16 v[8:11], v[64:67], v[36:39], v[8:11]\n\t" \
  "ds_read_b64_tr_b16 v[76:77], v88 offset:40960\n\t" \
  "global_load_lds_dwordx4 v90, s[84:85] sc1\n\t" \
  "s_add_u32 s84, s84, s88\n\t" \
  "s_addc_u32 s85, s85, 0\n\t" \
  "v_mfma_f32_16x16x32_bf16 v[12:15], v[68:71], v[36:39], v[12:15]\n\t" \
  "ds_read_b64_tr_b16 v[78:79], v89 offset:40960\n\t" \
  "ds_read_b128 v[36:39], v80 offset:1024\n\t" \
  "v_mfma_f32_16x16x32_bf16 v[16:19], v[64:67], v[40:43], v[16:19]\n\t" \
  "v_mfma_f32_16x16x32_bf16 v[20:23], v[68:71], v[40:43], v[20:23]\n\t" \
  "ds_read_b128 v[40:43], v80 offset:2048\n\t" \
  "s_add_u32 m0, %[lw], 81920\n\t" \
  "v_mfma_f32_16x16x32_bf16 v[24:27], v[64:67], v[44:47], v[24:27]\n\t" \
  "global_load_lds_dwordx4 v91, s[86:87]\n\t" \
  "s_add_u32 s86, s86, s89\n\t" \
  "s_addc_u32 s87, s87, 0\n\t" \
  "v_mfma_f32_16x16x32_bf16 v[28:31], v[68:71], v[44:47], v[28:31]\n\t" \
  "ds_read_b128 v[44:47], v80 offset:3072\n\t" \
  "s_waitcnt lgkmcnt(3)\n\t" \
  "v_mfma_f32_16x16x32_bf16 v[0:3], v[72:75], v[48:51], v[0:3]\n\t" \
  "ds_read_b64_tr_b16 v[64:65], v82 offset:0\n\t" \
  "v_mfma_f32_16x16x32_bf16 v[4:7], v[76:79], v[48:51], v[4:7]\n\t" \
  "ds_read_b64_tr_b16 v[66:67], v83 offset:0\n\t" \
  "ds_read_b128 v[48:51], v80 offset:8192\n\t" \
  "s_add_u32 m0, %[lw], 73728\n\t" \
  "v_mfma_f32_16x16x32_bf16 v[8:11], v[72:75], v[52:55], v[8:11]\n\t" \
  "ds_read_b64_tr_b16 v[68:69], v84 offset:0\n\t" \
  "global_load_lds_dwordx4 v90, s[84:85] sc1\n\t" \
  "s_add_u32 s84, s84, s88\n\t" \
  "s_addc_u32 s85, s85, 0\n\t" \
  "v_mfma_f32_16x16x32_bf16 v[12:15], v[76:79], v[52:55], v[12:15]\n\t" \
  "ds_read_b64_tr_b16 v[70:71], v85 offset:0\n\t" \
  "ds_read_b128 v[52:55], v80 offset:9216\n\t" \
  "v_mfma_f32_16x16x32_bf16 v[16:19], v[72:75], v[56:59], v[16:19]\n\t" \
  "v_mfma_f32_16x16x32_bf16 v[20:23], v[76:79], v[56:59], v[20:23]\n\t" \
  "ds_read_b128 v[56:59], v80 offset:10240\n\t" \
  "s_add_u32 m0, %[lw], 90112\n\t" \
  "v_mfma_f32_16x16x32_bf16 v[24:27], v[72:75], v[60:63], v[24:27]\n\t" \
  "global_load_lds_dwordx4 v91, s[86:87]\n\t" \
  "s_add_u32 s86, s86, s89\n\t" \
  "s_addc_u32 s87, s87, 0\n\t" \
  "v_mfma_f32_16x16x32_bf16 v[28:31], v[76:79], v[60:63], v[28:31]\n\t" \
  "ds_read_b128 v[60:63], v80 offset:11264\n\t" \
  "s_cmp_lg_u32 s88, 0\n\t" \
  "s_cbranch_scc1 .Lp0%=\n\t" \
  "s_branch .Le%=\n\t" \
  ".Lk%=:\n\t" \
  "ds_read_b128 v[32:35], v80 offset:0\n\t" \
  "ds_read_b128 v[36:39], v80 offset:1024\n\t" \
  "ds_read_b128 v[40:43], v80 offset:2048\n\t" \
  "ds_read_b128 v[44:47], v80 offset:3072\n\t" \
  "ds_read_b64_tr_b16 v[64:65], v82 offset:0\n\t" \
  "ds_read_b64_tr_b16 v[66:67], v83 offset:0\n\t" \
  "ds_read_b128 v[48:51], v80 offset:8192\n\t" \
  "ds_read_b64_tr_b16 v[68:69], v84 offset:0\n\t" \
  "ds_read_b64_tr_b16 v[70:71], v85 offset:0\n\t" \
  "ds_read_b128 v[52:55], v80 offset:9216\n\t" \
  "ds_read_b128 v[56:59], v80 offset:10240\n\t" \
  "ds_read_b128 v[60:63], v80 offset:11264\n\t" \
  ".Lp1%=:\n\t" \
  "s_waitcnt vmcnt(4)\n\t" \
  "s_barrier\n\t" \
  "s_waitcnt lgkmcnt(3)\n\t" \
  "v_mfma_f32_16x16x32_bf16 v[0:3], v[64:67], v[32:35], v[0:3]\n\t" \
  "ds_read_b64_tr_b16 v[72:73], v82 offset:8192\n\t" \
  "v_mfma_f32_16x16x32_bf16 v[4:7], v[68:71], v[32:35], v[4:7]\n\t" \
  "ds_read_b64_tr_b16 v[74:75], v83 offset:8192\n\t" \
  "ds_read_b128 v[32:35], v80 offset:32768\n\t" \
  "v_mfma_f32_16x16x32_bf16 v[8:11], v[64:67], v[36:39], v[8:11]\n\t" \
  "ds_read_b64_tr_b16 v[76:77], v84 offset:8192\n\t" \
  "v_mfma_f32_16x16x32_bf16 v[12:15], v[68:71], v[36:39], v[12:15]\n\t" \
  "ds_read_b64_tr_b16 v[78:79], v85 offset:8192\n\t" \
  "ds_read_b128 v[36:39], v80 offset:33792\n\t" \
  "s_add_u32 m0, %[lw], 98304\n\t" \
  "v_mfma_f32_16x16x32_bf16 v[16:19], v[64:67], v[40:43], v[16:19]\n\t" \
  "global_load_lds_dwordx4 v90, s[84:85] sc1\n\t" \
  "s_add_u32 s84, s84, s88\n\t" \
  "s_addc_u32 s85, s85, 0\n\t" \
  "v_mfma_f32_16x16x32_bf16 v[20:23], v[68:71], v[40:43], v[20:23]\n\t" \
  "ds_read_b128 v[40:43], v80 offset:34816\n\t" \
  "v_mfma_f32_16x16x32_bf16 v[24:27], v[64:67], v[44:47], v[24:27]\n\t" \
  "s_add_u32 m0, %[lw], 114688\n\t" \
  "v_mfma_f32_16x16x32_bf16 v[28:31], v[68:71], v[44:47], v[28:31]\n\t" \
  "ds_read_b128 v[44:47], v80 offset:35840\n\t" \
  "global_load_lds_dwordx4 v91, s[86:87]\n\t" \
  "s_add_u32 s86, s86, s89\n\t" \
  "s_addc_u32 s87, s87, 0\n\t" \
  "s_waitcnt lgkmcnt(3)\n\t" \
  "v_mfma_f32_16x16x32_bf16 v[0:3], v[72:75], v[48:51], v[0:3]\n\t" \
  "ds_read_b64_tr_b16 v[64:65], v82 offset:32768\n\t" \
  "v_mfma_f32_16x16x32_bf16 v[4:7], v[76:79], v[48:51], v[4:7]\n\t" \
  "ds_read_b64_tr_b16 v[66:67], v83 offset:32768\n\t" \
  "ds_read_b128 v[48:51], v80 offset:40960\n\t" \
  "v_mfma_f32_16x16x32_bf16 v[8:11], v[72:75], v[52:55], v[8:11]\n\t" \
  "ds_read_b64_tr_b16 v[68:69], v84 offset:32768\n\t" \
  "v_mfma_f32_16x16x32_bf16 v[12:15], v[76:79], v[52:55], v[12:15]\n\t" \
  "ds_read_b64_tr_b16 v[70:71], v85 offset:32768\n\t" \
  "ds_read_b128 v[52:55], v80 offset:41984\n\t" \
  "s_add_u32 m0, %[lw], 106496\n\t" \
  "v_mfma_f32_16x16x32_bf16 v[16:19], v[72:75], v[56:59], v[16:19]\n\t" \
  "global_load_lds_dwordx4 v90, s[84:85] sc1\n\t" \
  "s_add_u32 s84, s84, s88\n\t" \
  "s_addc_u32 s85, s85, 0\n\t" \
  "v_mfma_f32_16x16x32_bf16 v[20:23], v[76:79], v[56:59], v[20:23]\n\t" \
  "ds_read_b128 v[56:59], v80 offset:43008\n\t" \
  "v_mfma_f32_16x16x32_bf16 v[24:27], v[72:75], v[60:63], v[24:27]\n\t" \
  "s_add_u32 m0, %[lw], 122880\n\t" \
  "v_mfma_f32_16x16x32_bf16 v[28:31], v[76:79], v[60:63], v[28:31]\n\t" \
  "ds_read_b128 v[60:63], v80 offset:44032\n\t" \
  "global_load_lds_dwordx4 v91, s[86:87]\n\t" \
  "s_add_u32 s86, s86, s89\n\t" \
  "s_addc_u32 s87, s87, 0\n\t" \
  "s_sub_u32 s90, s90, 1\n\t" \
  "s_cselect_b32 s88, 0, s88\n\t" \
  "s_cselect_b32 s89, 0, s89\n\t" \
  "s_cselect_b32 s91, 16384, 0\n\t" \
  "s_sub_u32 s84, s84, s91\n\t" \
  "s_subb_u32 s85, s85, 0\n\t" \
  "s_lshl_b32 s91, s91, 2\n\t" \
  "s_sub_u32 s86, s86, s91\n\t" \
  "s_subb_u32 s87, s87, 0\n\t" \
  "s_waitcnt vmcnt(4)\n\t" \
  "s_barrier\n\t" \
  "s_waitcnt lgkmcnt(3)\n\t" \
  "v_mfma_f32_16x16x32_bf16 v[0:3], v[64:67], v[32:35], v[0:3]\n\t" \
  "ds_read_b64_tr_b16 v[72:73], v82 offset:40960\n\t" \
  "v_mfma_f32_16x16x32_bf16 v[4:7], v[68:71], v[32:35], v[4:7]\n\t" \
  "ds_read_b64_tr_b16 v[74:75], v83 offset:40960\n\t" \
  "ds_read_b128 v[32:35], v81 offset:0\n\t" \
  "v_mfma_f32_16x16x32_bf16 v[8:11], v[64:67], v[36:39], v[8:11]\n\t" \
  "ds_read_b64_tr_b16 v[76:77], v84 offset:40960\n\t" \
  "v_mfma_f32_16x16x32_bf16 v[12:15], v[68:71], v[36:39], v[12:15]\n\t" \
  "ds_read_b64_tr_b16 v[78:79], v85 offset:40960\n\t" \
  "ds_read_b128 v[36:39], v81 offset:1024\n\t" \
  "s_add_u32 m0, %[lw], 0\n\t" \
  "v_mfma_f32_16x16x32_bf16 v[16:19], v[64:67], v[40:43], v[16:19]\n\t" \
  "global_load_lds_dwordx4 v90, s[84:85] sc1\n\t" \
  "s_add_u32 s84, s84, s88\n\t" \
  "s_addc_u32 s85, s85, 0\n\t" \
  "v_mfma_f32_16x16x32_bf16 v[20:23], v[68:71], v[40:43], v[20:23]\n\t" \
  "ds_read_b128 v[40:43], v81 offset:2048\n\t" \
  "v_mfma_f32_16x16x32_bf16 v[24:27], v[64:67], v[44:47], v[24:27]\n\t" \
  "s_add_u32 m0, %[lw], 16384\n\t" \
  "v_mfma_f32_16x16x32_bf16 v[28:31], v[68:71], v[44:47], v[28:31]\n\t" \
  "ds_read_b128 v[44:47], v81 offset:3072\n\t" \
  "global_load_lds_dwordx4 v91, s[86:87]\n\t" \
  "s_add_u32 s86, s86, s89\n\t" \
  "s_addc_u32 s87, s87, 0\n\t" \
  "s_waitcnt lgkmcnt(3)\n\t" \
  "v_mfma_f32_16x16x32_bf16 v[0:3], v[72:75], v[48:51], v[0:3]\n\t" \
  "ds_read_b64_tr_b16 v[64:65], v86 offset:0\n\t" \
  "v_mfma_f32_16x16x32_bf16 v[4:7], v[76:79], v[48:51], v[4:7]\n\t" \
  "ds_read_b64_tr_b16 v[66:67], v87 offset:0\n\t" \
  "ds_read_b128 v[48:51], v81 offset:8192\n\t" \
  "v_mfma_f32_16x16x32_bf16 v[8:11], v[72:75], v[52:55], v[8:11]\n\t" \
  "ds_read_b64_tr_b16 v[68:69], v88 offset:0\n\t" \
  "v_mfma_f32_16x16x32_bf16 v[12:15], v[76:79], v[52:55], v[12:15]\n\t" \
  "ds_read_b64_tr_b16 v[70:71], v89 offset:0\n\t" \
  "ds_read_b128 v[52:55], v81 offset:9216\n\t" \
  "s_add_u32 m0, %[lw], 8192\n\t" \
  "v_mfma_f32_16x16x32_bf16 v[16:19], v[72:75], v[56:59], v[16:19]\n\t" \
  "global_load_lds_dwordx4 v90, s[84:85] sc1\n\t" \
  "s_add_u32 s84, s84, s88\n\t" \
  "s_addc_u32 s85, s85, 0\n\t" \
  "v_mfma_f32_16x16x32_bf16 v[20:23], v[76:79], v[56:59], v[20:23]\n\t" \
  "ds_read_b128 v[56:59], v81 offset:10240\n\t" \
  "v_mfma_f32_16x16x32_bf16 v[24:27], v[72:75], v[60:63], v[24:27]\n\t" \
  "s_add_u32 m0, %[lw], 24576\n\t" \
  "v_mfma_f32_16x16x32_bf16 v[28:31], v[76:79], v[60:63], v[28:31]\n\t" \
  "ds_read_b128 v[60:63], v81 offset:11264\n\t" \
  "global_load_lds_dwordx4 v91, s[86:87]\n\t" \
  "s_add_u32 s86, s86, s89\n\t" \
  "s_addc_u32 s87, s87, 0\n\t" \
  "s_waitcnt vmcnt(4)\n\t" \
  "s_barrier\n\t" \
  "s_waitcnt lgkmcnt(3)\n\t" \
  "v_mfma_f32_16x16x32_bf16 v[0:3], v[64:67], v[32:35], v[0:3]\n\t" \
  "ds_read_b64_tr_b16 v[72:73], v86 offset:8192\n\t" \
  "v_mfma_f32_16x16x32_bf16 v[4:7], v[68:71], v[32:35], v[4:7]\n\t" \
  "ds_read_b64_tr_b16 v[74:75], v87 offset:8192\n\t" \
  "ds_read_b128 v[32:35], v81 offset:32768\n\t" \
  "v_mfma_f32_16x16x32_bf16 v[8:11], v[64:67], v[36:39], v[8:11]\n\t" \
  "ds_read_b64_tr_b16 v[76:77], v88 offset:8192\n\t" \
  "v_mfma_f32_16x16x32_bf16 v[12:15], v[68:71], v[36:39], v[12:15]\n\t" \
  "ds_read_b64_tr_b16 v[78:79], v89 offset:8192\n\t" \
  "ds_read_b128 v[36:39], v81 offset:33792\n\t" \
  "s_add_u32 m0, %[lw], 32768\n\t" \
  "v_mfma_f32_16x16x32_bf16 v[16:19], v[64:67], v[40:43], v[16:19]\n\t" \
  "global_load_lds_dwordx4 v90, s[84:85] sc1\n\t" \
  "s_add_u32 s84, s84, s88\n\t" \
  "s_addc_u32 s85, s85, 0\n\t" \
  "v_mfma_f32_16x16x32_bf16 v[20:23], v[68:71], v[40:43], v[20:23]\n\t" \
  "ds_read_b128 v[40:43], v81 offset:34816\n\t" \
  "v_mfma_f32_16x16x32_bf16 v[24:27], v[64:67], v[44:47], v[24:27]\n\t" \
  "s_add_u32 m0, %[lw], 49152\n\t" \
  "v_mfma_f32_16x16x32_bf16 v[28:31], v[68:71], v[44:47], v[28:31]\n\t" \
  "ds_read_b128 v[44:47], v81 offset:35840\n\t" \
  "global_load_lds_dwordx4 v91, s[86:87]\n\t" \
  "s_add_u32 s86, s86, s89\n\t" \
  "s_addc_u32 s87, s87, 0\n\t" \
  "s_waitcnt lgkmcnt(3)\n\t" \
  "v_mfma_f32_16x16x32_bf16 v[0:3], v[72:75], v[48:51], v[0:3]\n\t" \
  "ds_read_b64_tr_b16 v[64:65], v86 offset:32768\n\t" \
  "v_mfma_f32_16x16x32_bf16 v[4:7], v[76:79], v[48:51], v[4:7]\n\t" \
  "ds_read_b64_tr_b16 v[66:67], v87 offset:32768\n\t" \
  "ds_read_b128 v[48:51], v81 offset:40960\n\t" \
  "v_mfma_f32_16x16x32_bf16 v[8:11], v[72:75], v[52:55], v[8:11]\n\t" \
  "ds_read_b64_tr_b16 v[68:69], v88 offset:32768\n\t" \
  "v_mfma_f32_16x16x32_bf16 v[12:15], v[76:79], v[52:55], v[12:15]\n\t" \
  "ds_read_b64_tr_b16 v[70:71], v89 offset:32768\n\t" \
  "ds_read_b128 v[52:55], v81 offset:41984\n\t" \
  "s_add_u32 m0, %[lw], 40960\n\t" \
  "v_mfma_f32_16x16x32_bf16 v[16:19], v[72:75], v[56:59], v[16:19]\n\t" \
  "global_load_lds_dwordx4 v90, s[84:85] sc1\n\t" \
  "s_add_u32 s84, s84, s88\n\t" \
  "s_addc_u32 s85, s85, 0\n\t" \
  "v_mfma_f32_16x16x32_bf16 v[20:23], v[76:79], v[56:59], v[20:23]\n\t" \
  "ds_read_b128 v[56:59], v81 offset:43008\n\t" \
  "v_mfma_f32_16x16x32_bf16 v[24:27], v[72:75], v[60:63], v[24:27]\n\t" \
  "s_add_u32 m0, %[lw], 57344\n\t" \
  "v_mfma_f32_16x16x32_bf16 v[28:31], v[76:79], v[60:63], v[28:31]\n\t" \
  "ds_read_b128 v[60:63], v81 offset:44032\n\t" \
  "global_load_lds_dwordx4 v91, s[86:87]\n\t" \
  "s_add_u32 s86, s86, s89\n\t" \
  "s_addc_u32 s87, s87, 0\n\t" \
  "s_waitcnt vmcnt(4)\n\t" \
  "s_barrier\n\t" \
  "s_waitcnt lgkmcnt(3)\n\t" \
  "v_mfma_f32_16x16x32_bf16 v[0:3], v[64:67], v[32:35], v[0:3]\n\t" \
  "ds_read_b64_tr_b16 v[72:73], v86 offset:40960\n\t" \
  "v_mfma_f32_16x16x32_bf16 v[4:7], v[68:71], v[32:35], v[4:7]\n\t" \
  "ds_read_b64_tr_b16 v[74:75], v87 offset:40960\n\t" \
  "ds_read_b128 v[32:35], v80 offset:0\n\t" \
  "v_mfma_f32_16x16x32_bf16 v[8:11], v[64:67], v[36:39], v[8:11]\n\t" \
  "ds_read_b64_tr_b16 v[76:77], v88 offset:40960\n\t" \
  "v_mfma_f32_16x16x32_bf16 v[12:15], v[68:71], v[36:39], v[12:15]\n\t" \
  "ds_read_b64_tr_b16 v[78:79], v89 offset:40960\n\t" \
  "ds_read_b128 v[36:39], v80 offset:1024\n\t" \
  "s_add_u32 m0, %[lw], 65536\n\t" \
  "v_mfma_f32_16x16x32_bf16 v[16:19], v[64:67], v[40:43], v[16:19]\n\t" \
  "global_load_lds_dwordx4 v90, s[84:85] sc1\n\t" \
  "s_add_u32 s84, s84, s88\n\t" \
  "s_addc_u32 s85, s85, 0\n\t" \
  "v_mfma_f32_16x16x32_bf16 v[20:23], v[68:71], v[40:43], v[20:23]\n\t" \
  "ds_read_b128 v[40:43], v80 offset:2048\n\t" \
  "v_mfma_f32_16x16x32_bf16 v[24:27], v[64:67], v[44:47], v[24:27]\n\t" \
  "s_add_u32 m0, %[lw], 81920\n\t" \
  "v_mfma_f32_16x16x32_bf16 v[28:31], v[68:71], v[44:47], v[28:31]\n\t" \
  "ds_read_b128 v[44:47], v80 offset:3072\n\t" \
  "global_load_lds_dwordx4 v91, s[86:87]\n\t" \
  "s_add_u32 s86, s86, s89\n\t" \
  "s_addc_u32 s87, s87, 0\n\t" \
  "s_waitcnt lgkmcnt(3)\n\t" \
  "v_mfma_f32_16x16x32_bf16 v[0:3], v[72:75], v[48:51], v[0:3]\n\t" \
  "ds_read_b64_tr_b16 v[64:65], v82 offset:0\n\t" \
  "v_mfma_f32_16x16x32_bf16 v[4:7], v[76:79], v[48:51], v[4:7]\n\t" \
  "ds_read_b64_tr_b16 v[66:67], v83 offset:0\n\t" \
  "ds_read_b128 v[48:51], v80 offset:8192\n\t" \
  "v_mfma_f32_16x16x32_bf16 v[8:11], v[72:75], v[52:55], v[8:11]\n\t" \
  "ds_read_b64_tr_b16 v[68:69], v84 offset:0\n\t" \
  "v_mfma_f32_16x16x32_bf16 v[12:15], v[76:79], v[52:55], v[12:15]\n\t" \
  "ds_read_b64_tr_b16 v[70:71], v85 offset:0\n\t" \
  "ds_read_b128 v[52:55], v80 offset:9216\n\t" \
  "s_add_u32 m0, %[lw], 73728\n\t" \
  "v_mfma_f32_16x16x32_bf16 v[16:19], v[72:75], v[56:59], v[16:19]\n\t" \
  "global_load_lds_dwordx4 v90, s[84:85] sc1\n\t" \
  "s_add_u32 s84, s84, s88\n\t" \
  "s_addc_u32 s85, s85, 0\n\t" \
  "v_mfma_f32_16x16x32_bf16 v[20:23], v[76:79], v[56:59], v[20:23]\n\t" \
  "ds_read_b128 v[56:59], v80 offset:10240\n\t" \
  "v_mfma_f32_16x16x32_bf16 v[24:27], v[72:75], v[60:63], v[24:27]\n\t" \
  "s_add_u32 m0, %[lw], 90112\n\t" \
  "v_mfma_f32_16x16x32_bf16 v[28:31], v[76:79], v[60:63], v[28:31]\n\t" \
  "ds_read_b128 v[60:63], v80 offset:11264\n\t" \
  "global_load_lds_dwordx4 v91, s[86:87]\n\t" \
  "s_add_u32 s86, s86, s89\n\t" \
  "s_addc_u32 s87, s87, 0\n\t" \
  "s_cmp_lg_u32 s88, 0\n\t" \
  "s_cbranch_scc1 .Lp1%=\n\t" \
  ".Le%=:\n\t" \
  "s_waitcnt vmcnt(0) lgkmcnt(0)\n\t" \
  "s_mov_b32 m0, s94\n\t" \
  "s_setprio 0\n\t" \
  "s_nop 7\n\t"
#define HSL 16
__device__ __forceinline__ void job_down(const Args& p, int rblk, int ct, char* smem, __amdgpu_buffer_rsrc_t rsy) {
  if (threadIdx.x < 64) {
    for (unsigned spins = 0;;) {
      asm volatile("" ::: "memory");
      if (__builtin_amdgcn_readfirstlane((int)ld_flag0(rsy, SY_CH + (rblk >> 1) * 128)) >= 27) break;
      __builtin_amdgcn_s_sleep(HSL);
      if (spin_fail(spins, (gu32*)(p.ws + WS_SY + SY_ABORT))) break;
    }
  }
  int tid = threadIdx.x;
  asm volatile("" : "+v"(tid));
  const int lane = tid & 63, r = lane & 15, q = lane >> 4;
  const int wid = __builtin_amdgcn_readfirstlane(tid >> 6), wr = wid >> 2, wc = wid & 3;
  typedef __attribute__((ext_vector_type(32))) float f32x32;
  typedef __attribute__((ext_vector_type(8))) unsigned u32x8;
  f32x32 acc;
  {
    const char* A = p.ws + WS_HF + h_off(p, rblk);
    const char* B = (const char*)(p.wd + ct * 128);
    const unsigned rb = tid >> 4, va = tid * 16, vb = ((rb & 4) * 4 + (rb >> 3) * 4 + (rb & 3)) * (DM * 2) + 16 * ((tid & 15) ^ swzB(rb & 15));
    u32x8 lb;
#pragma unroll
    for (int i = 0; i < 8; ++i)
      lb[i] = (i >> 2) * 65536 + 16384 + offB(8 * q + 4 * (i & 1) + (r >> 2), 2 * (wc * 2 + ((i >> 1) & 1)) + ((r & 3) >> 1)) + 8 * (r & 1);
    LBAR();
#pragma unroll
    for (int t = 0; t < 3; ++t) {
      glds2<true>(A + t * 32768, 16384, va, wid * 1024 + t * 32768);
      glds2(B + t * 64 * DM * 2, 32 * DM * 2, vb, wid * 1024 + t * 32768 + 16384);
    }
    WAIT_V(8);
    LBAR();
    asm volatile(DN_ASM
        : "={v[0:31]}"(acc)
        : "{v[80:81]}"((u32x2){wr * 4096u + lane * 16u, 65536u + wr * 4096u + lane * 16u}), "{v[82:89]}"(lb), "{v90}"(va), "{v91}"(vb),
          [pa] "s"(A + 3 * 32768), [pb] "s"(B + 3 * 64 * DM * 2), [lw] "s"(wid * 1024), [hf] "s"(wr)
        : CLB8(32, 33, 34, 35, 36, 37, 38, 39), CLB8(40, 41, 42, 43, 44, 45, 46, 47), CLB8(48, 49, 50, 51, 52, 53, 54, 55),
          CLB8(56, 57, 58, 59, 60, 61, 62, 63), CLB8(64, 65, 66, 67, 68, 69, 70, 71), CLB8(72, 73, 74, 75, 76, 77, 78, 79),
          "s84", "s85", "s86", "s87", "s88", "s89", "s90", "s91", "s94", "scc", "memory");
  }
  wait_s(p, rsy, rblk, ct * 4, 4, rblk < NFUSE);
  const int e0 = (rblk * 128 + wr * 64 + r) * DM + ct * 128 + wc * 32 + q * 8;
  SX sx[4];
#pragma unroll
  for (int m = 0; m < 4; ++m) sx[m] = get_sx(p, rblk, e0 + m * 16 * DM);
  const __amdgpu_buffer_rsrc_t ro = mkrsrc(p.ws + WS_G, S_LEN * DM * 2);
#pragma unroll
  for (int m = 0; m < 4; ++m) {
    float rr[8];
#pragma unroll
    for (int j = 0; j < 8; ++j) { float d = rbf(acc[8 * m + j]); rr[j] = d * __builtin_amdgcn_rcpf(1.0f + __expf(-d)); }
    put_g(p, ro, rblk, e0 + m * 16 * DM, widen32(cvtpk(rr[0], rr[1]), cvtpk(rr[2], rr[3]), cvtpk(rr[4], rr[5]), cvtpk(rr[6], rr[7])), sx[m]);
  }
  if (rblk >= NFUSE) publish_flags(rsy, SY_FG + (rblk * 8 + ct) * 128, 0, 1);
}

#define PR_ASM_0 \
  "s_cmp_lg_u32 %[kh], 0\n\t" \
  "s_cbranch_scc1 .Lk%=\n\t" \
  "s_mov_b32 s94, m0\n\t" \
  "s_mov_b64 s[92:93], %[x0]\n\t" \
  "s_mov_b32 s86, %[g0]\n\t" \
  "s_add_u32 s87, s86, %[t0]\n\t" \
  "s_sub_i32 s88, s86, 3\n\t" \
  "ds_read_b128 v[176:179], v208 offset:0\n\t" \
  "ds_read_b128 v[180:183], v208 offset:16384\n\t" \
  "ds_read_b128 v[184:187], v209 offset:0\n\t" \
  "ds_read_b128 v[188:191], v209 offset:16384\n\t" \
  "ds_read_b128 v[192:195], v210 offset:0\n\t" \
  "ds_read_b128 v[196:199], v210 offset:16384\n\t" \
  "ds_read_b128 v[200:203], v211 offset:0\n\t" \
  "ds_read_b128 v[204:207], v211 offset:16384\n\t" \
  "s_branch .Lfb00%=\n\t" \
  ".Lp0%=:\n\t" \
  "s_cmp_le_u32 s86, 5\n\t" \
  "s_cbranch_scc1 .Lf00%=\n\t" \
  ".Lfb00%=:\n\t" \
  "s_waitcnt lgkmcnt(6)\n\t" \
  "v_mfma_f32_16x16x32_bf16 v[128:131], v[0:3], v[176:179], 0\n\t" \
  "v_mfma_f32_16x16x32_bf16 v[132:135], v[4:7], v[176:179], 0\n\t" \
  "ds_read_b128 v[176:179], v208 offset:256\n\t" \
  "s_add_u32 m0, %[lw], 67584\n\t" \
  "v_mfma_f32_16x16x32_bf16 v[136:139], v[0:3], v[180:183], 0\n\t" \
  "v_mfma_f32_16x16x32_bf16 v[140:143], v[4:7], v[180:183], 0\n\t" \
  "ds_read_b128 v[180:183], v208 offset:16640\n\t" \
  "global_load_lds_dwordx4 v218, s[92:93] sc1\n\t" \
  "s_waitcnt lgkmcnt(6)\n\t" \
  "v_mfma_f32_16x16x32_bf16 v[128:131], v[8:11], v[184:187], v[128:131]\n\t" \
  "v_mfma_f32_16x16x32_bf16 v[132:135], v[12:15], v[184:187], v[132:135]\n\t" \
  "ds_read_b128 v[184:187], v209 offset:256\n\t" \
  "v_mfma_f32_16x16x32_bf16 v[136:139], v[8:11], v[188:191], v[136:139]\n\t" \
  "v_mfma_f32_16x16x32_bf16 v[140:143], v[12:15], v[188:191], v[140:143]\n\t" \
  "ds_read_b128 v[188:191], v209 offset:16640\n\t" \
  "ds_write_b128 v220, v[152:155] offset:0\n\t" \
  "ds_write_b128 v220, v[156:159] offset:1024\n\t" \
  "s_waitcnt lgkmcnt(8)\n\t" \
  "v_mfma_f32_16x16x32_bf16 v[128:131], v[16:19], v[192:195], v[128:131]\n\t" \
  "v_mfma_f32_16x16x32_bf16 v[132:135], v[20:23], v[192:195], v[132:135]\n\t" \
  "ds_read_b128 v[192:195], v210 offset:256\n\t" \
  "s_add_u32 m0, %[lw], 68608\n\t" \
  "v_mfma_f32_16x16x32_bf16 v[136:139], v[16:19], v[196:199], v[136:139]\n\t" \
  "v_mfma_f32_16x16x32_bf16 v[140:143], v[20:23], v[196:199], v[140:143]\n\t" \
  "ds_read_b128 v[196:199], v210 offset:16640\n\t" \
  "global_load_lds_dwordx4 v219, s[92:93] sc1\n\t" \
  "s_lshr_b32 s90, s87, 7\n\t" \
  "s_xor_b32 s90, s90, %[s0]\n\t" \
  "s_and_b32 s90, s90, 1\n\t" \
  "s_lshl1_add_u32 s90, s87, s90\n\t" \
  "s_lshl_b32 s90, s90, 13\n\t" \
  "s_cmp_lt_i32 s86, 6\n\t" \
  "s_cselect_b32 s90, 0x70000000, s90\n\t" \
  "s_waitcnt lgkmcnt(8)\n\t" \
  "v_mfma_f32_16x16x32_bf16 v[128:131], v[24:27], v[200:203], v[128:131]\n\t" \
  "v_mfma_f32_16x16x32_bf16 v[132:135], v[28:31], v[200:203], v[132:135]\n\t" \
  "ds_read_b128 v[200:203], v211 offset:256\n\t" \
  "v_mfma_f32_16x16x32_bf16 v[136:139], v[24:27], v[204:207], v[136:139]\n\t" \
  "v_mfma_f32_16x16x32_bf16 v[140:143], v[28:31], v[204:207], v[140:143]\n\t" \
  "ds_read_b128 v[204:207], v211 offset:16640\n\t" \
  "s_waitcnt lgkmcnt(4)\n\t" \
  "s_cmp_lt_u32 s86, 6\n\t" \
  "s_cbranch_scc1 .Lw00%=\n\t" \
  "s_waitcnt vmcnt(4)\n\t" \
  "s_barrier\n\t" \
  ".Lg00%=:\n\t" \
  "s_add_u32 s92, s92, 512\n\t" \
  "s_addc_u32 s93, s93, 0\n\t" \
  "v_mfma_f32_16x16x32_bf16 v[128:131], v[32:35], v[176:179], v[128:131]\n\t" \
  "v_mfma_f32_16x16x32_bf16 v[132:135], v[36:39], v[176:179], v[132:135]\n\t" \
  "ds_read_b128 v[176:179], v208 offset:32768\n\t" \
  "s_add_u32 m0, %[lw], 98304\n\t" \
  "v_mfma_f32_16x16x32_bf16 v[136:139], v[32:35], v[180:183], v[136:139]\n\t" \
  "v_mfma_f32_16x16x32_bf16 v[140:143], v[36:39], v[180:183], v[140:143]\n\t" \
  "ds_read_b128 v[180:183], v208 offset:49152\n\t" \
  "global_load_lds_dwordx4 v216, s[92:93] sc1\n\t" \
  "ds_read_b128 v[160:163], v221 offset:0\n\t" \
  "ds_read_b128 v[164:167], v221 offset:1024\n\t" \
  "v_mfma_f32_16x16x32_bf16 v[128:131], v[40:43], v[184:187], v[128:131]\n\t" \
  "v_mfma_f32_16x16x32_bf16 v[132:135], v[44:47], v[184:187], v[132:135]\n\t" \
  "ds_read_b128 v[184:187], v209 offset:32768\n\t" \
  "v_mfma_f32_16x16x32_bf16 v[136:139], v[40:43], v[188:191], v[136:139]\n\t" \
  "v_mfma_f32_16x16x32_bf16 v[140:143], v[44:47], v[188:191], v[140:143]\n\t" \
  "ds_read_b128 v[188:191], v209 offset:49152\n\t" \
  "s_waitcnt lgkmcnt(8)\n\t" \
  "v_mfma_f32_16x16x32_bf16 v[128:131], v[48:51], v[192:195], v[128:131]\n\t" \
  "v_mfma_f32_16x16x32_bf16 v[132:135], v[52:55], v[192:195], v[132:135]\n\t" \
  "ds_read_b128 v[192:195], v210 offset:32768\n\t" \
  "s_add_u32 m0, %[lw], 99328\n\t" \
  "v_mfma_f32_16x16x32_bf16 v[136:139], v[48:51], v[196:199], v[136:139]\n\t" \
  "v_mfma_f32_16x16x32_bf16 v[140:143], v[52:55], v[196:199], v[140:143]\n\t" \
  "ds_read_b128 v[196:199], v210 offset:49152\n\t" \
  "global_load_lds_dwordx4 v217, s[92:93] sc1\n\t" \
  "s_waitcnt lgkmcnt(4)\n\t" \
  "v_add_f32 v160, v160, v144\n\t" \
  "v_add_f32 v161, v161, v145\n\t" \
  "v_add_f32 v162, v162, v146\n\t" \
  "v_add_f32 v163, v163, v147\n\t" \
  "v_add_f32 v164, v164, v148\n\t" \
  "v_add_f32 v165, v165, v149\n\t" \
  "v_add_f32 v166, v166, v150\n\t" \
  "v_add_f32 v167, v167, v151\n\t" \
  "v_mfma_f32_16x16x32_bf16 v[128:131], v[56:59], v[200:203], v[128:131]\n\t" \
  "v_mfma_f32_16x16x32_bf16 v[132:135], v[60:63], v[200:203], v[132:135]\n\t" \
  "ds_read_b128 v[200:203], v211 offset:32768\n\t" \
  "v_mfma_f32_16x16x32_bf16 v[136:139], v[56:59], v[204:207], v[136:139]\n\t" \
  "v_mfma_f32_16x16x32_bf16 v[140:143], v[60:63], v[204:207], v[140:143]\n\t" \
  "ds_read_b128 v[204:207], v211 offset:49152\n\t" \
  "v_cvt_pk_bf16_f32 v168, v160, v161\n\t" \
  "v_cvt_pk_bf16_f32 v169, v162, v163\n\t" \
  "v_cvt_pk_bf16_f32 v170, v164, v165\n\t" \
  "v_cvt_pk_bf16_f32 v171, v166, v167\n\t" \
  "buffer_store_dwordx4 v[168:171], v172, %[rt], s90 offen sc1\n\t" \
  "v_mfma_f32_16x16x32_bf16 v[128:131], v[64:67], v[176:179], v[128:131]\n\t" \
  "v_mfma_f32_16x16x32_bf16 v[132:135], v[68:71], v[176:179], v[132:135]\n\t" \
  "ds_read_b128 v[176:179], v208 offset:33024\n\t" \
  "s_add_u32 m0, %[lw], 100352\n\t" \
  "v_mfma_f32_16x16x32_bf16 v[136:139], v[64:67], v[180:183], v[136:139]\n\t" \
  "v_mfma_f32_16x16x32_bf16 v[140:143], v[68:71], v[180:183], v[140:143]\n\t" \
  "ds_read_b128 v[180:183], v208 offset:49408\n\t" \
  "global_load_lds_dwordx4 v218, s[92:93] sc1\n\t" \
  "s_waitcnt lgkmcnt(6)\n\t" \
  "v_mfma_f32_16x16x32_bf16 v[128:131], v[72:75], v[184:187], v[128:131]\n\t" \
  "v_mfma_f32_16x16x32_bf16 v[132:135], v[76:79], v[184:187], v[132:135]\n\t" \
  "ds_read_b128 v[184:187], v209 offset:33024\n\t" \
  "v_mfma_f32_16x16x32_bf16 v[136:139], v[72:75], v[188:191], v[136:139]\n\t" \
  "v_mfma_f32_16x16x32_bf16 v[140:143], v[76:79], v[188:191], v[140:143]\n\t" \
  "ds_read_b128 v[188:191], v209 offset:49408\n\t" \
  "s_waitcnt lgkmcnt(6)\n\t" \
  "v_mfma_f32_16x16x32_bf16 v[128:131], v[80:83], v[192:195], v[128:131]\n\t" \
  "v_mfma_f32_16x16x32_bf16 v[132:135], v[84:87], v[192:195], v[132:135]\n\t" \
  "ds_read_b128 v[192:195], v210 offset:33024\n\t" \
  "s_add_u32 m0, %[lw], 101376\n\t" \
  "v_mfma_f32_16x16x32_bf16 v[136:139], v[80:83], v[196:199], v[136:139]\n\t" \
  "v_mfma_f32_16x16x32_bf16 v[140:143], v[84:87], v[196:199], v[140:143]\n\t" \
  "ds_read_b128 v[196:199], v210 offset:49408\n\t" \
  "global_load_lds_dwordx4 v219, s[92:93] sc1\n\t" \
  "s_waitcnt lgkmcnt(6)\n\t" \
  "v_mfma_f32_16x16x32_bf16 v[128:131], v[88:91], v[200:203], v[128:131]\n\t" \
  "v_mfma_f32_16x16x32_bf16 v[132:135], v[92:95], v[200:203], v[132:135]\n\t" \
  "ds_read_b128 v[200:203], v211 offset:33024\n\t" \
  "v_mfma_f32_16x16x32_bf16 v[136:139], v[88:91], v[204:207], v[136:139]\n\t" \
  "v_mfma_f32_16x16x32_bf16 v[140:143], v[92:95], v[204:207], v[140:143]\n\t" \
  "ds_read_b128 v[204:207], v211 offset:49408\n\t" \
  "s_cmp_lt_u32 s86, 8\n\t" \
  "s_cbranch_scc1 .Lw10%=\n\t" \
  "s_waitcnt vmcnt(5)\n\t" \
  "s_barrier\n\t" \
  ".Lg10%=:\n\t" \
  "s_add_u32 s92, s92, 65024\n\t" \
  "s_addc_u32 s93, s93, 0\n\t" \
  "s_waitcnt lgkmcnt(6)\n\t" \
  "v_mfma_f32_16x16x32_bf16 v[128:131], v[96:99], v[176:179], v[128:131]\n\t" \
  "v_mfma_f32_16x16x32_bf16 v[132:135], v[100:103], v[176:179], v[132:135]\n\t" \
  "ds_read_b128 v[176:179], v212 offset:0\n\t" \
  "s_add_u32 m0, %[lw], 0\n\t" \
  "v_mfma_f32_16x16x32_bf16 v[136:139], v[96:99], v[180:183], v[136:139]\n\t" \
  "v_mfma_f32_16x16x32_bf16 v[140:143], v[100:103], v[180:183], v[140:143]\n\t" \
  "ds_read_b128 v[180:183], v212 offset:16384\n\t" \
  "global_load_lds_dwordx4 v216, s[92:93] sc1\n\t" \
  "s_waitcnt lgkmcnt(6)\n\t" \
  "v_mfma_f32_16x16x32_bf16 v[128:131], v[104:107], v[184:187], v[128:131]\n\t" \
  "v_mfma_f32_16x16x32_bf16 v[132:135], v[108:111], v[184:187], v[132:135]\n\t" \
  "ds_read_b128 v[184:187], v213 offset:0\n\t" \
  "v_mfma_f32_16x16x32_bf16 v[136:139], v[104:107], v[188:191], v[136:139]\n\t" \
  "v_mfma_f32_16x16x32_bf16 v[140:143], v[108:111], v[188:191], v[140:143]\n\t" \
  "ds_read_b128 v[188:191], v213 offset:16384\n\t" \
  "s_waitcnt lgkmcnt(6)\n\t" \
  "v_mfma_f32_16x16x32_bf16 v[128:131], v[112:115], v[192:195], v[128:131]\n\t" \
  "v_mfma_f32_16x16x32_bf16 v[132:135], v[116:119], v[192:195], v[132:135]\n\t" \
  "ds_read_b128 v[192:195], v214 offset:0\n\t" \
  "s_add_u32 m0, %[lw], 1024\n\t" \
  "v_mfma_f32_16x16x32_bf16 v[136:139], v[112:115], v[196:199], v[136:139]\n\t" \
  "v_mfma_f32_16x16x32_bf16 v[140:143], v[116:119], v[196:199], v[140:143]\n\t" \
  "ds_read_b128 v[196:199], v214 offset:16384\n\t" \
  "global_load_lds_dwordx4 v217, s[92:93] sc1\n\t" \
  "s_waitcnt lgkmcnt(6)\n\t" \
  "v_mfma_f32_16x16x32_bf16 v[128:131], v[120:123], v[200:203], v[128:131]\n\t" \
  "v_mfma_f32_16x16x32_bf16 v[132:135], v[124:127], v[200:203], v[132:135]\n\t" \
  "ds_read_b128 v[200:203], v215 offset:0\n\t" \
  "v_mfma_f32_16x16x32_bf16 v[136:139], v[120:123], v[204:207], v[136:139]\n\t" \
  "v_mfma_f32_16x16x32_bf16 v[140:143], v[124:127], v[204:207], v[140:143]\n\t" \
  "ds_read_b128 v[204:207], v215 offset:16384\n\t" \
  "s_add_u32 s86, s86, 1\n\t" \
  "s_add_u32 s87, s87, 1\n\t" \
  "s_cmp_le_u32 s86, 5\n\t" \
  "s_cbranch_scc1 .Lf10%=\n\t" \
  ".Lfb10%=:\n\t" \
  "s_waitcnt lgkmcnt(6)\n\t" \
  "v_mfma_f32_16x16x32_bf16 v[144:147], v[0:3], v[176:179], 0\n\t" \
  "v_mfma_f32_16x16x32_bf16 v[148:151], v[4:7], v[176:179], 0\n\t" \
  "ds_read_b128 v[176:179], v212 offset:256\n\t" \
  "s_add_u32 m0, %[lw], 2048\n\t" \
  "v_mfma_f32_16x16x32_bf16 v[152:155], v[0:3], v[180:183], 0\n\t" \
  "v_mfma_f32_16x16x32_bf16 v[156:159], v[4:7], v[180:183], 0\n\t" \
  "ds_read_b128 v[180:183], v212 offset:16640\n\t" \
  "global_load_lds_dwordx4 v218, s[92:93] sc1\n\t" \
  "s_waitcnt lgkmcnt(6)\n\t" \
  "v_mfma_f32_16x16x32_bf16 v[144:147], v[8:11], v[184:187], v[144:147]\n\t" \
  "v_mfma_f32_16x16x32_bf16 v[148:151], v[12:15], v[184:187], v[148:151]\n\t" \
  "ds_read_b128 v[184:187], v213 offset:256\n\t" \
  "v_mfma_f32_16x16x32_bf16 v[152:155], v[8:11], v[188:191], v[152:155]\n\t" \
  "v_mfma_f32_16x16x32_bf16 v[156:159], v[12:15], v[188:191], v[156:159]\n\t" \
  "ds_read_b128 v[188:191], v213 offset:16640\n\t" \
  "ds_write_b128 v220, v[136:139] offset:0\n\t" \
  "ds_write_b128 v220, v[140:143] offset:1024\n\t" \
  "s_waitcnt lgkmcnt(8)\n\t" \
  "v_mfma_f32_16x16x32_bf16 v[144:147], v[16:19], v[192:195], v[144:147]\n\t" \
  "v_mfma_f32_16x16x32_bf16 v[148:151], v[20:23], v[192:195], v[148:151]\n\t" \
  "ds_read_b128 v[192:195], v214 offset:256\n\t" \
  "s_add_u32 m0, %[lw], 3072\n\t" \
  "v_mfma_f32_16x16x32_bf16 v[152:155], v[16:19], v[196:199], v[152:155]\n\t" \
  "v_mfma_f32_16x16x32_bf16 v[156:159], v[20:23], v[196:199], v[156:159]\n\t" \
  "ds_read_b128 v[196:199], v214 offset:16640\n\t" \
  "global_load_lds_dwordx4 v219, s[92:93] sc1\n\t" \
  "s_lshr_b32 s90, s87, 7\n\t" \
  "s_xor_b32 s90, s90, %[s0]\n\t" \
  "s_and_b32 s90, s90, 1\n\t" \
  "s_lshl1_add_u32 s90, s87, s90\n\t" \
  "s_lshl_b32 s90, s90, 13\n\t" \
  "s_cmp_lt_i32 s86, 6\n\t" \
  "s_cselect_b32 s90, 0x70000000, s90\n\t" \
  "s_waitcnt lgkmcnt(8)\n\t" \
  "v_mfma_f32_16x16x32_bf16 v[144:147], v[24:27], v[200:203], v[144:147]\n\t" \
  "v_mfma_f32_16x16x32_bf16 v[148:151], v[28:31], v[200:203], v[148:151]\n\t" \
  "ds_read_b128 v[200:203], v215 offset:256\n\t" \
  "v_mfma_f32_16x16x32_bf16 v[152:155], v[24:27], v[204:207], v[152:155]\n\t" \
  "v_mfma_f32_16x16x32_bf16 v[156:159], v[28:31], v[204:207], v[156:159]\n\t" \
  "ds_read_b128 v[204:207], v215 offset:16640\n\t" \
  "s_waitcnt lgkmcnt(4)\n\t" \
  "s_cmp_lt_u32 s86, 6\n\t" \
  "s_cbranch_scc1 .Lw20%=\n\t" \
  "s_waitcnt vmcnt(4)\n\t" \
  "s_barrier\n\t" \
  ".Lg20%=:\n\t" \
  "s_add_u32 s92, s92, 512\n\t" \
  "s_addc_u32 s93, s93, 0\n\t" \
  "v_mfma_f32_16x16x32_bf16 v[144:147], v[32:35], v[176:179], v[144:147]\n\t" \
  "v_mfma_f32_16x16x32_bf16 v[148:151], v[36:39], v[176:179], v[148:151]\n\t" \
  "ds_read_b128 v[176:179], v212 offset:32768\n\t" \
  "s_add_u32 m0, %[lw], 32768\n\t" \
  "v_mfma_f32_16x16x32_bf16 v[152:155], v[32:35], v[180:183], v[152:155]\n\t" \
  "v_mfma_f32_16x16x32_bf16 v[156:159], v[36:39], v[180:183], v[156:159]\n\t" \
  "ds_read_b128 v[180:183], v212 offset:49152\n\t" \
  "global_load_lds_dwordx4 v216, s[92:93] sc1\n\t" \
  "ds_read_b128 v[160:163], v221 offset:0\n\t" \
  "ds_read_b128 v[164:167], v221 offset:1024\n\t" \
  "v_mfma_f32_16x16x32_bf16 v[144:147], v[40:43], v[184:187], v[144:147]\n\t" \
  "v_mfma_f32_16x16x32_bf16 v[148:151], v[44:47], v[184:187], v[148:151]\n\t" \
  "ds_read_b128 v[184:187], v213 offset:32768\n\t" \
  "v_mfma_f32_16x16x32_bf16 v[152:155], v[40:43], v[188:191], v[152:155]\n\t" \
  "v_mfma_f32_16x16x32_bf16 v[156:159], v[44:47], v[188:191], v[156:159]\n\t" \
  "ds_read_b128 v[188:191], v213 offset:49152\n\t" \
  "s_waitcnt lgkmcnt(8)\n\t" \
  "v_mfma_f32_16x16x32_bf16 v[144:147], v[48:51], v[192:195], v[144:147]\n\t" \
  "v_mfma_f32_16x16x32_bf16 v[148:151], v[52:55], v[192:195], v[148:151]\n\t" \
  "ds_read_b128 v[192:195], v214 offset:32768\n\t" \
  "s_add_u32 m0, %[lw], 33792\n\t" \
  "v_mfma_f32_16x16x32_bf16 v[152:155], v[48:51], v[196:199], v[152:155]\n\t" \
  "v_mfma_f32_16x16x32_bf16 v[156:159], v[52:55], v[196:199], v[156:159]\n\t" \
  "ds_read_b128 v[196:199], v214 offset:49152\n\t" \
  "global_load_lds_dwordx4 v217, s[92:93] sc1\n\t" \
  "s_waitcnt lgkmcnt(4)\n\t" \
  "v_add_f32 v160, v160, v128\n\t" \
  "v_add_f32 v161, v161, v129\n\t" \
  "v_add_f32 v162, v162, v130\n\t" \
  "v_add_f32 v163, v163, v131\n\t" \
  "v_add_f32 v164, v164, v132\n\t" \
  "v_add_f32 v165, v165, v133\n\t" \
  "v_add_f32 v166, v166, v134\n\t" \
  "v_add_f32 v167, v167, v135\n\t" \
  "v_mfma_f32_16x16x32_bf16 v[144:147], v[56:59], v[200:203], v[144:147]\n\t" \
  "v_mfma_f32_16x16x32_bf16 v[148:151], v[60:63], v[200:203], v[148:151]\n\t" \
  "ds_read_b128 v[200:203], v215 offset:32768\n\t" \
  "v_mfma_f32_16x16x32_bf16 v[152:155], v[56:59], v[204:207], v[152:155]\n\t" \
  "v_mfma_f32_16x16x32_bf16 v[156:159], v[60:63], v[204:207], v[156:159]\n\t" \
  "ds_read_b128 v[204:207], v215 offset:49152\n\t" \
  "v_cvt_pk_bf16_f32 v168, v160, v161\n\t" \
  "v_cvt_pk_bf16_f32 v169, v162, v163\n\t" \
  "v_cvt_pk_bf16_f32 v170, v164, v165\n\t" \
  "v_cvt_pk_bf16_f32 v171, v166, v167\n\t" \
  "buffer_store_dwordx4 v[168:171], v172, %[rt], s90 offen sc1\n\t" \
  "v_mfma_f32_16x16x32_bf16 v[144:147], v[64:67], v[176:179], v[144:147]\n\t" \
  "v_mfma_f32_16x16x32_bf16 v[148:151], v[68:71], v[176:179], v[148:151]\n\t" \
  "ds_read_b128 v[176:179], v212 offset:33024\n\t" \
  "s_add_u32 m0, %[lw], 34816\n\t" \
  "v_mfma_f32_16x16x32_bf16 v[152:155], v[64:67], v[180:183], v[152:155]\n\t" \
  "v_mfma_f32_16x16x32_bf16 v[156:159], v[68:71], v[180:183], v[156:159]\n\t" \
  "ds_read_b128 v[180:183], v212 offset:49408\n\t" \
  "global_load_lds_dwordx4 v218, s[92:93] sc1\n\t" \
  "s_waitcnt lgkmcnt(6)\n\t" \
  "v_mfma_f32_16x16x32_bf16 v[144:147], v[72:75], v[184:187], v[144:147]\n\t" \
  "v_mfma_f32_16x16x32_bf16 v[148:151], v[76:79], v[184:187], v[148:151]\n\t" \
  "ds_read_b128 v[184:187], v213 offset:33024\n\t" \
  "v_mfma_f32_16x16x32_bf16 v[152:155], v[72:75], v[188:191], v[152:155]\n\t" \
  "v_mfma_f32_16x16x32_bf16 v[156:159], v[76:79], v[188:191], v[156:159]\n\t" \
  "ds_read_b128 v[188:191], v213 offset:49408\n\t" \
  "s_waitcnt lgkmcnt(6)\n\t" \
  "v_mfma_f32_16x16x32_bf16 v[144:147], v[80:83], v[192:195], v[144:147]\n\t" \
  "v_mfma_f32_16x16x32_bf16 v[148:151], v[84:87], v[192:195], v[148:151]\n\t" \
  "ds_read_b128 v[192:195], v214 offset:33024\n\t" \
  "s_add_u32 m0, %[lw], 35840\n\t" \
  "v_mfma_f32_16x16x32_bf16 v[152:155], v[80:83], v[196:199], v[152:155]\n\t" \
  "v_mfma_f32_16x16x32_bf16 v[156:159], v[84:87], v[196:199], v[156:159]\n\t" \
  "ds_read_b128 v[196:199], v214 offset:49408\n\t" \
  "global_load_lds_dwordx4 v219, s[92:93] sc1\n\t" \
  "s_waitcnt lgkmcnt(6)\n\t" \
  "v_mfma_f32_16x16x32_bf16 v[144:147], v[88:91], v[200:203], v[144:147]\n\t" \
  "v_mfma_f32_16x16x32_bf16 v[148:151], v[92:95], v[200:203], v[148:151]\n\t" \
  "ds_read_b128 v[200:203], v215 offset:33024\n\t" \
  "v_mfma_f32_16x16x32_bf16 v[152:155], v[88:91], v[204:207], v[152:155]\n\t" \
  "v_mfma_f32_16x16x32_bf16 v[156:159], v[92:95], v[204:207], v[156:159]\n\t" \
  "ds_read_b128 v[204:207], v215 offset:49408\n\t" \
  "s_cmp_lt_u32 s86, 8\n\t" \
  "s_cbranch_scc1 .Lw30%=\n\t" \
  "s_waitcnt vmcnt(5)\n\t" \
  "s_barrier\n\t" \
  ".Lg30%=:\n\t" \
  "s_add_u32 s92, s92, 65024\n\t" \
  "s_addc_u32 s93, s93, 0\n\t" \
  "s_waitcnt lgkmcnt(6)\n\t" \
  "v_mfma_f32_16x16x32_bf16 v[144:147], v[96:99], v[176:179], v[144:147]\n\t" \
  "v_mfma_f32_16x16x32_bf16 v[148:151], v[100:103], v[176:179], v[148:151]\n\t" \
  "ds_read_b128 v[176:179], v208 offset:0\n\t" \
  "s_add_u32 m0, %[lw], 65536\n\t" \
  "v_mfma_f32_16x16x32_bf16 v[152:155], v[96:99], v[180:183], v[152:155]\n\t" \
  "v_mfma_f32_16x16x32_bf16 v[156:159], v[100:103], v[180:183], v[156:159]\n\t" \
  "ds_read_b128 v[180:183], v208 offset:16384\n\t" \
  "global_load_lds_dwordx4 v216, s[92:93] sc1\n\t" \
  "s_waitcnt lgkmcnt(6)\n\t" \
  "v_mfma_f32_16x16x32_bf16 v[144:147], v[104:107], v[184:187], v[144:147]\n\t" \
  "v_mfma_f32_16x16x32_bf16 v[148:151], v[108:111], v[184:187], v[148:151]\n\t" \
  "ds_read_b128 v[184:187], v209 offset:0\n\t" \
  "v_mfma_f32_16x16x32_bf16 v[152:155], v[104:107], v[188:191], v[152:155]\n\t" \
  "v_mfma_f32_16x16x32_bf16 v[156:159], v[108:111], v[188:191], v[156:159]\n\t" \
  "ds_read_b128 v[188:191], v209 offset:16384\n\t" \
  "s_waitcnt lgkmcnt(6)\n\t" \
  "v_mfma_f32_16x16x32_bf16 v[144:147], v[112:115], v[192:195], v[144:147]\n\t" \
  "v_mfma_f32_16x16x32_bf16 v[148:151], v[116:119], v[192:195], v[148:151]\n\t" \
  "ds_read_b128 v[192:195], v210 offset:0\n\t" \
  "s_add_u32 m0, %[lw], 66560\n\t" \
  "v_mfma_f32_16x16x32_bf16 v[152:155], v[112:115], v[196:199], v[152:155]\n\t" \
  "v_mfma_f32_16x16x32_bf16 v[156:159], v[116:119], v[196:199], v[156:159]\n\t" \
  "ds_read_b128 v[196:199], v210 offset:16384\n\t" \
  "global_load_lds_dwordx4 v217, s[92:93] sc1\n\t" \
  "s_waitcnt lgkmcnt(6)\n\t" \
  "v_mfma_f32_16x16x32_bf16 v[144:147], v[120:123], v[200:203], v[144:147]\n\t" \
  "v_mfma_f32_16x16x32_bf16 v[148:151], v[124:127], v[200:203], v[148:151]\n\t" \
  "ds_read_b128 v[200:203], v211 offset:0\n\t" \
  "v_mfma_f32_16x16x32_bf16 v[152:155], v[120:123], v[204:207], v[152:155]\n\t" \
  "v_mfma_f32_16x16x32_bf16 v[156:159], v[124:127], v[204:207], v[156:159]\n\t" \
  "ds_read_b128 v[204:207], v211 offset:16384\n\t" \
  "s_add_u32 s86, s86, 1\n\t" \
  "s_add_u32 s87, s87, 1\n\t" \
  "s_cmp_lt_u32 s86, %[end]\n\t" \
  "s_cbranch_scc1 .Lp0%=\n\t" \
  "s_branch .Lx0%=\n\t" \
  ".Lf00%=:\n\t" \
  "s_nop 7\n\t" \
  "ds_write_b128 v220, v[152:155] offset:0\n\t" \
  "ds_write_b128 v220, v[156:159] offset:1024\n\t" \
  "s_lshr_b32 s90, s87, 7\n\t" \
  "s_xor_b32 s90, s90, %[s0]\n\t" \
  "s_and_b32 s90, s90, 1\n\t" \
  "s_lshl1_add_u32 s90, s87, s90\n\t" \
  "s_lshl_b32 s90, s90, 13\n\t" \
  "s_cmp_lt_i32 s86, 1\n\t" \
  "s_cselect_b32 s90, 0x70000000, s90\n\t" \
  "s_waitcnt lgkmcnt(0)\n\t" \
  "s_barrier\n\t" \
  "ds_read_b128 v[160:163], v221 offset:0\n\t" \
  "ds_read_b128 v[164:167], v221 offset:1024\n\t" \
  "s_waitcnt lgkmcnt(0)\n\t" \
  "v_add_f32 v160, v160, v144\n\t" \
  "v_add_f32 v161, v161, v145\n\t" \
  "v_add_f32 v162, v162, v146\n\t" \
  "v_add_f32 v163, v163, v147\n\t" \
  "v_add_f32 v164, v164, v148\n\t" \
  "v_add_f32 v165, v165, v149\n\t" \
  "v_add_f32 v166, v166, v150\n\t" \
  "v_add_f32 v167, v167, v151\n\t" \
  "v_cvt_pk_bf16_f32 v168, v160, v161\n\t" \
  "v_cvt_pk_bf16_f32 v169, v162, v163\n\t" \
  "v_cvt_pk_bf16_f32 v170, v164, v165\n\t" \
  "v_cvt_pk_bf16_f32 v171, v166, v167\n\t" \
  "buffer_store_dwordx4 v[168:171], v172, %[rt], s90 offen sc1\n\t" \
  "s_branch .Lfb00%=\n\t" \
  ".Lf10%=:\n\t" \
  "s_nop 7\n\t" \
  "ds_write_b128 v220, v[136:139] offset:0\n\t" \
  "ds_write_b128 v220, v[140:143] offset:1024\n\t" \
  "s_lshr_b32 s90, s87, 7\n\t" \
  "s_xor_b32 s90, s90, %[s0]\n\t" \
  "s_and_b32 s90, s90, 1\n\t" \
  "s_lshl1_add_u32 s90, s87, s90\n\t" \
  "s_lshl_b32 s90, s90, 13\n\t" \
  "s_cmp_lt_i32 s86, 1\n\t" \
  "s_cselect_b32 s90, 0x70000000, s90\n\t" \
  "s_waitcnt lgkmcnt(0)\n\t" \
  "s_barrier\n\t" \
  "ds_read_b128 v[160:163], v221 offset:0\n\t" \
  "ds_read_b128 v[164:167], v221 offset:1024\n\t" \
  "s_waitcnt lgkmcnt(0)\n\t" \
  "v_add_f32 v160, v160, v128\n\t" \
  "v_add_f32 v161, v161, v129\n\t" \
  "v_add_f32 v162, v162, v130\n\t" \
  "v_add_f32 v163, v163, v131\n\t" \
  "v_add_f32 v164, v164, v132\n\t" \
  "v_add_f32 v165, v165, v133\n\t" \
  "v_add_f32 v166, v166, v134\n\t" \
  "v_add_f32 v167, v167, v135\n\t" \
  "v_cvt_pk_bf16_f32 v168, v160, v161\n\t" \
  "v_cvt_pk_bf16_f32 v169, v162, v163\n\t" \
  "v_cvt_pk_bf16_f32 v170, v164, v165\n\t" \
  "v_cvt_pk_bf16_f32 v171, v166, v167\n\t" \
  "buffer_store_dwordx4 v[168:171], v172, %[rt], s90 offen sc1\n\t" \
  "s_branch .Lfb10%=\n\t" \
  ".Lw00%=:\n\t" \
  "s_waitcnt vmcnt(2)\n\t" \
  "s_barrier\n\t" \
  "s_branch .Lg00%=\n\t" \
  ".Lw10%=:\n\t" \
  "s_waitcnt vmcnt(2)\n\t" \
  "s_barrier\n\t" \
  "s_branch .Lg10%=\n\t" \
  ".Lw20%=:\n\t" \
  "s_waitcnt vmcnt(2)\n\t" \
  "s_barrier\n\t" \
  "s_branch .Lg20%=\n\t" \
  ".Lw30%=:\n\t" \
  "s_waitcnt vmcnt(2)\n\t" \
  "s_barrier\n\t" \
  "s_branch .Lg30%=\n\t" \
  ".Lx0%=:\n\t" \
  "s_waitcnt lgkmcnt(0)\n\t" \
  "s_setprio 0\n\t" \
  "s_mov_b32 m0, s94\n\t" \
  "s_branch .Le%=\n\t" \
  ".Lk%=:\n\t" \
  "s_mov_b32 s94, m0\n\t" \
  "s_mov_b64 s[92:93], %[x0]\n\t" \
  "s_mov_b32 s86, %[g0]\n\t" \
  "s_add_u32 s87, s86, %[t0]\n\t" \
  "s_sub_i32 s88, s86, 3\n\t" \
  "ds_read_b128 v[176:179], v208 offset:0\n\t" \
  "ds_read_b128 v[180:183], v208 offset:16384\n\t" \
  "ds_read_b128 v[184:187], v209 offset:0\n\t" \
  "ds_read_b128 v[188:191], v209 offset:16384\n\t" \
  "ds_read_b128 v[192:195], v210 offset:0\n\t" \
  "ds_read_b128 v[196:199], v210 offset:16384\n\t" \
  "ds_read_b128 v[200:203], v211 offset:0\n\t" \
  "ds_read_b128 v[204:207], v211 offset:16384\n\t" \
  "s_branch .Lfb01%=\n\t" \
  ".Lp1%=:\n\t" \
  "s_cmp_le_u32 s86, 5\n\t" \
  "s_cbranch_scc1 .Lf01%=\n\t" \
  ".Lfb01%=:\n\t" \
  "s_waitcnt lgkmcnt(6)\n\t" \
  "v_mfma_f32_16x16x32_bf16 v[128:131], v[0:3], v[176:179], 0\n\t" \
  "v_mfma_f32_16x16x32_bf16 v[132:135], v[4:7], v[176:179], 0\n\t" \
  "ds_read_b128 v[176:179], v208 offset:256\n\t" \
  "v_mfma_f32_16x16x32_bf16 v[136:139], v[0:3], v[180:183], 0\n\t" \
  "v_mfma_f32_16x16x32_bf16 v[140:143], v[4:7], v[180:183], 0\n\t" \
  "ds_read_b128 v[180:183], v208 offset:16640\n\t" \
  "s_waitcnt lgkmcnt(6)\n\t" \
  "v_mfma_f32_16x16x32_bf16 v[128:131], v[8:11], v[184:187], v[128:131]\n\t" \
  "v_mfma_f32_16x16x32_bf16 v[132:135], v[12:15], v[184:187], v[132:135]\n\t" \
  "ds_read_b128 v[184:187], v209 offset:256\n\t" \
  "s_add_u32 m0, %[lw], 67584\n\t" \
  "v_mfma_f32_16x16x32_bf16 v[136:139], v[8:11], v[188:191], v[136:139]\n\t" \
  "v_mfma_f32_16x16x32_bf16 v[140:143], v[12:15], v[188:191], v[140:143]\n\t" \
  "ds_read_b128 v[188:191], v209 offset:16640\n\t" \
  "global_load_lds_dwordx4 v218, s[92:93] sc1\n\t" \
  "ds_write_b128 v220, v[152:155] offset:0\n\t" \
  "ds_write_b128 v220, v[156:159] offset:1024\n\t" \
  "s_waitcnt lgkmcnt(8)\n\t" \
  "v_mfma_f32_16x16x32_bf16 v[128:131], v[16:19], v[192:195], v[128:131]\n\t" \
  "v_mfma_f32_16x16x32_bf16 v[132:135], v[20:23], v[192:195], v[132:135]\n\t" \
  "ds_read_b128 v[192:195], v210 offset:256\n\t" \
  "v_mfma_f32_16x16x32_bf16 v[136:139], v[16:19], v[196:199], v[136:139]\n\t" \
  "v_mfma_f32_16x16x32_bf16 v[140:143], v[20:23], v[196:199], v[140:143]\n\t" \
  "ds_read_b128 v[196:199], v210 offset:16640\n\t" \
  "s_lshr_b32 s90, s87, 7\n\t" \
  "s_xor_b32 s90, s90, %[s0]\n\t" \
  "s_and_b32 s90, s90, 1\n\t" \
  "s_lshl1_add_u32 s90, s87, s90\n\t" \
  "s_lshl_b32 s90, s90, 13\n\t" \
  "s_cmp_lt_i32 s86, 6\n\t" \
  "s_cselect_b32 s90, 0x70000000, s90\n\t" \
  "s_waitcnt lgkmcnt(8)\n\t" \
  "v_mfma_f32_16x16x32_bf16 v[128:131], v[24:27], v[200:203], v[128:131]\n\t" \
  "v_mfma_f32_16x16x32_bf16 v[132:135], v[28:31], v[200:203], v[132:135]\n\t" \
  "ds_read_b128 v[200:203], v211 offset:256\n\t" \
  "s_add_u32 m0, %[lw], 68608\n\t" \
  "v_mfma_f32_16x16x32_bf16 v[136:139], v[24:27], v[204:207], v[136:139]\n\t" \
  "v_mfma_f32_16x16x32_bf16 v[140:143], v[28:31], v[204:207], v[140:143]\n\t" \
  "ds_read_b128 v[204:207], v211 offset:16640\n\t" \
  "global_load_lds_dwordx4 v219, s[92:93] sc1\n\t" \
  "s_waitcnt lgkmcnt(4)\n\t" \
  "s_cmp_lt_u32 s86, 6\n\t" \
  "s_cbranch_scc1 .Lw01%=\n\t" \
  "s_waitcnt vmcnt(4)\n\t" \
  "s_barrier\n\t" \
  ".Lg01%=:\n\t" \
  "s_add_u32 s92, s92, 512\n\t" \
  "s_addc_u32 s93, s93, 0\n\t" \
  "v_mfma_f32_16x16x32_bf16 v[128:131], v[32:35], v[176:179], v[128:131]\n\t" \
  "v_mfma_f32_16x16x32_bf16 v[132:135], v[36:39], v[176:179], v[132:135]\n\t" \
  "ds_read_b128 v[176:179], v208 offset:32768\n\t" \
  "v_mfma_f32_16x16x32_bf16 v[136:139], v[32:35], v[180:183], v[136:139]\n\t" \
  "v_mfma_f32_16x16x32_bf16 v[140:143], v[36:39], v[180:183], v[140:143]\n\t" \
  "ds_read_b128 v[180:183], v208 offset:49152\n\t" \
  "ds_read_b128 v[160:163], v221 offset:0\n\t" \
  "ds_read_b128 v[164:167], v221 offset:1024\n\t" \
  "s_sub_i32 s91, s86, 2\n\t" \
  "s_max_i32 s88, s88, s91\n\t" \
  "v_mov_b32 v173, s88\n\t" \
  "buffer_store_dword v173, v174, %[rs], 0 offen sc1\n\t" \
  "v_mfma_f32_16x16x32_bf16 v[128:131], v[40:43], v[184:187], v[128:131]\n\t" \
  "v_mfma_f32_16x16x32_bf16 v[132:135], v[44:47], v[184:187], v[132:135]\n\t" \
  "ds_read_b128 v[184:187], v209 offset:32768\n\t" \
  "s_add_u32 m0, %[lw], 98304\n\t" \
  "v_mfma_f32_16x16x32_bf16 v[136:139], v[40:43], v[188:191], v[136:139]\n\t" \
  "v_mfma_f32_16x16x32_bf16 v[140:143], v[44:47], v[188:191], v[140:143]\n\t" \
  "ds_read_b128 v[188:191], v209 offset:49152\n\t" \
  "global_load_lds_dwordx4 v216, s[92:93] sc1\n\t" \
  "s_waitcnt lgkmcnt(8)\n\t" \
  "v_mfma_f32_16x16x32_bf16 v[128:131], v[48:51], v[192:195], v[128:131]\n\t" \
  "v_mfma_f32_16x16x32_bf16 v[132:135], v[52:55], v[192:195], v[132:135]\n\t" \
  "ds_read_b128 v[192:195], v210 offset:32768\n\t" \
  "v_mfma_f32_16x16x32_bf16 v[136:139], v[48:51], v[196:199], v[136:139]\n\t" \
  "v_mfma_f32_16x16x32_bf16 v[140:143], v[52:55], v[196:199], v[140:143]\n\t" \
  "ds_read_b128 v[196:199], v210 offset:49152\n\t" \
  "s_waitcnt lgkmcnt(4)\n\t" \
  "v_add_f32 v160, v160, v144\n\t" \
  "v_add_f32 v161, v161, v145\n\t" \
  "v_add_f32 v162, v162, v146\n\t" \
  "v_add_f32 v163, v163, v147\n\t" \
  "v_add_f32 v164, v164, v148\n\t" \
  "v_add_f32 v165, v165, v149\n\t" \
  "v_add_f32 v166, v166, v150\n\t" \
  "v_add_f32 v167, v167, v151\n\t" \
  "v_mfma_f32_16x16x32_bf16 v[128:131], v[56:59], v[200:203], v[128:131]\n\t" \
  "v_mfma_f32_16x16x32_bf16 v[132:135], v[60:63], v[200:203], v[132:135]\n\t" \
  "ds_read_b128 v[200:203], v211 offset:32768\n\t" \
  "s_add_u32 m0, %[lw], 99328\n\t" \
  "v_mfma_f32_16x16x32_bf16 v[136:139], v[56:59], v[204:207], v[136:139]\n\t" \
  "v_mfma_f32_16x16x32_bf16 v[140:143], v[60:63], v[204:207], v[140:143]\n\t" \
  "ds_read_b128 v[204:207], v211 offset:49152\n\t" \
  "global_load_lds_dwordx4 v217, s[92:93] sc1\n\t" \
  "v_cvt_pk_bf16_f32 v168, v160, v161\n\t" \
  "v_cvt_pk_bf16_f32 v169, v162, v163\n\t" \
  "v_cvt_pk_bf16_f32 v170, v164, v165\n\t" \
  "v_cvt_pk_bf16_f32 v171, v166, v167\n\t" \
  "buffer_store_dwordx4 v[168:171], v172, %[rt], s90 offen sc1\n\t" \
  "v_mfma_f32_16x16x32_bf16 v[128:131], v[64:67], v[176:179], v[128:131]\n\t" \
  "v_mfma_f32_16x16x32_bf16 v[132:135], v[68:71], v[176:179], v[132:135]\n\t" \
  "ds_read_b128 v[176:179], v208 offset:33024\n\t" \
  "v_mfma_f32_16x16x32_bf16 v[136:139], v[64:67], v[180:183], v[136:139]\n\t" \
  "v_mfma_f32_16x16x32_bf16 v[140:143], v[68:71], v[180:183], v[140:143]\n\t" \
  "ds_read_b128 v[180:183], v208 offset:49408\n\t" \
  "s_waitcnt lgkmcnt(6)\n\t" \
  "v_mfma_f32_16x16x32_bf16 v[128:131], v[72:75], v[184:187], v[128:131]\n\t" \
  "v_mfma_f32_16x16x32_bf16 v[132:135], v[76:79], v[184:187], v[132:135]\n\t" \
  "ds_read_b128 v[184:187], v209 offset:33024\n\t" \
  "s_add_u32 m0, %[lw], 100352\n\t" \
  "v_mfma_f32_16x16x32_bf16 v[136:139], v[72:75], v[188:191], v[136:139]\n\t" \
  "v_mfma_f32_16x16x32_bf16 v[140:143], v[76:79], v[188:191], v[140:143]\n\t" \
  "ds_read_b128 v[188:191], v209 offset:49408\n\t" \
  "global_load_lds_dwordx4 v218, s[92:93] sc1\n\t" \
  "s_waitcnt lgkmcnt(6)\n\t" \
  "v_mfma_f32_16x16x32_bf16 v[128:131], v[80:83], v[192:195], v[128:131]\n\t" \
  "v_mfma_f32_16x16x32_bf16 v[132:135], v[84:87], v[192:195], v[132:135]\n\t" \
  "ds_read_b128 v[192:195], v210 offset:33024\n\t" \
  "v_mfma_f32_16x16x32_bf16 v[136:139], v[80:83], v[196:199], v[136:139]\n\t" \
  "v_mfma_f32_16x16x32_bf16 v[140:143], v[84:87], v[196:199], v[140:143]\n\t" \
  "ds_read_b128 v[196:199], v210 offset:49408\n\t" \
  "s_waitcnt lgkmcnt(6)\n\t" \
  "v_mfma_f32_16x16x32_bf16 v[128:131], v[88:91], v[200:203], v[128:131]\n\t" \
  "v_mfma_f32_16x16x32_bf16 v[132:135], v[92:95], v[200:203], v[132:135]\n\t" \
  "ds_read_b128 v[200:203], v211 offset:33024\n\t" \
  "s_add_u32 m0, %[lw], 101376\n\t" \
  "v_mfma_f32_16x16x32_bf16 v[136:139], v[88:91], v[204:207], v[136:139]\n\t" \
  "v_mfma_f32_16x16x32_bf16 v[140:143], v[92:95], v[204:207], v[140:143]\n\t" \
  "ds_read_b128 v[204:207], v211 offset:49408\n\t" \
  "global_load_lds_dwordx4 v219, s[92:93] sc1\n\t" \
  "s_cmp_lt_u32 s86, 8\n\t" \
  "s_cbranch_scc1 .Lw11%=\n\t" \
  "s_waitcnt vmcnt(6)\n\t" \
  "s_barrier\n\t" \
  ".Lg11%=:\n\t" \
  "s_add_u32 s92, s92, 65024\n\t" \
  "s_addc_u32 s93, s93, 0\n\t" \
  "s_waitcnt lgkmcnt(6)\n\t" \
  "v_mfma_f32_16x16x32_bf16 v[128:131], v[96:99], v[176:179], v[128:131]\n\t" \
  "v_mfma_f32_16x16x32_bf16 v[132:135], v[100:103], v[176:179], v[132:135]\n\t" \
  "ds_read_b128 v[176:179], v212 offset:0\n\t" \
  "v_mfma_f32_16x16x32_bf16 v[136:139], v[96:99], v[180:183], v[136:139]\n\t" \
  "v_mfma_f32_16x16x32_bf16 v[140:143], v[100:103], v[180:183], v[140:143]\n\t" \
  "ds_read_b128 v[180:183], v212 offset:16384\n\t" \
  "s_waitcnt lgkmcnt(6)\n\t" \
  "v_mfma_f32_16x16x32_bf16 v[128:131], v[104:107], v[184:187], v[128:131]\n\t" \
  "v_mfma_f32_16x16x32_bf16 v[132:135], v[108:111], v[184:187], v[132:135]\n\t" \
  "ds_read_b128 v[184:187], v213 offset:0\n\t" \
  "s_add_u32 m0, %[lw], 0\n\t" \
  "v_mfma_f32_16x16x32_bf16 v[136:139], v[104:107], v[188:191], v[136:139]\n\t" \
  "v_mfma_f32_16x16x32_bf16 v[140:143], v[108:111], v[188:191], v[140:143]\n\t" \
  "ds_read_b128 v[188:191], v213 offset:16384\n\t" \
  "global_load_lds_dwordx4 v216, s[92:93] sc1\n\t" \
  "s_waitcnt lgkmcnt(6)\n\t" \
  "v_mfma_f32_16x16x32_bf16 v[128:131], v[112:115], v[192:195], v[128:131]\n\t" \
  "v_mfma_f32_16x16x32_bf16 v[132:135], v[116:119], v[192:195], v[132:135]\n\t" \
  "ds_read_b128 v[192:195], v214 offset:0\n\t" \
  "v_mfma_f32_16x16x32_bf16 v[136:139], v[112:115], v[196:199], v[136:139]\n\t" \
  "v_mfma_f32_16x16x32_bf16 v[140:143], v[116:119], v[196:199], v[140:143]\n\t" \
  "ds_read_b128 v[196:199], v214 offset:16384\n\t" \
  "s_waitcnt lgkmcnt(6)\n\t" \
  "v_mfma_f32_16x16x32_bf16 v[128:131], v[120:123], v[200:203], v[128:131]\n\t" \
  "v_mfma_f32_16x16x32_bf16 v[132:135], v[124:127], v[200:203], v[132:135]\n\t" \
  "ds_read_b128 v[200:203], v215 offset:0\n\t" \
  "s_add_u32 m0, %[lw], 1024\n\t" \
  "v_mfma_f32_16x16x32_bf16 v[136:139], v[120:123], v[204:207], v[136:139]\n\t" \
  "v_mfma_f32_16x16x32_bf16 v[140:143], v[124:127], v[204:207], v[140:143]\n\t" \
  "ds_read_b128 v[204:207], v215 offset:16384\n\t" \
  "global_load_lds_dwordx4 v217, s[92:93] sc1\n\t" \
  "s_add_u32 s86, s86, 1\n\t" \
  "s_add_u32 s87, s87, 1\n\t" \
  "s_cmp_le_u32 s86, 5\n\t" \
  "s_cbranch_scc1 .Lf11%=\n\t" \
  ".Lfb11%=:\n\t" \
  "s_waitcnt lgkmcnt(6)\n\t" \
  "v_mfma_f32_16x16x32_bf16 v[144:147], v[0:3], v[176:179], 0\n\t" \
  "v_mfma_f32_16x16x32_bf16 v[148:151], v[4:7], v[176:179], 0\n\t" \
  "ds_read_b128 v[176:179], v212 offset:256\n\t" \
  "v_mfma_f32_16x16x32_bf16 v[152:155], v[0:3], v[180:183], 0\n\t" \
  "v_mfma_f32_16x16x32_bf16 v[156:159], v[4:7], v[180:183], 0\n\t" \
  "ds_read_b128 v[180:183], v212 offset:16640\n\t" \
  "s_waitcnt lgkmcnt(6)\n\t" \
  "v_mfma_f32_16x16x32_bf16 v[144:147], v[8:11], v[184:187], v[144:147]\n\t" \
  "v_mfma_f32_16x16x32_bf16 v[148:151], v[12:15], v[184:187], v[148:151]\n\t" \
  "ds_read_b128 v[184:187], v213 offset:256\n\t" \
  "s_add_u32 m0, %[lw], 2048\n\t" \
  "v_mfma_f32_16x16x32_bf16 v[152:155], v[8:11], v[188:191], v[152:155]\n\t" \
  "v_mfma_f32_16x16x32_bf16 v[156:159], v[12:15], v[188:191], v[156:159]\n\t" \
  "ds_read_b128 v[188:191], v213 offset:16640\n\t" \
  "global_load_lds_dwordx4 v218, s[92:93] sc1\n\t" \
  "ds_write_b128 v220, v[136:139] offset:0\n\t" \
  "ds_write_b128 v220, v[140:143] offset:1024\n\t" \
  "s_waitcnt lgkmcnt(8)\n\t" \
  "v_mfma_f32_16x16x32_bf16 v[144:147], v[16:19], v[192:195], v[144:147]\n\t" \
  "v_mfma_f32_16x16x32_bf16 v[148:151], v[20:23], v[192:195], v[148:151]\n\t" \
  "ds_read_b128 v[192:195], v214 offset:256\n\t" \
  "v_mfma_f32_16x16x32_bf16 v[152:155], v[16:19], v[196:199], v[152:155]\n\t" \
  "v_mfma_f32_16x16x32_bf16 v[156:159], v[20:23], v[196:199], v[156:159]\n\t" \
  "ds_read_b128 v[196:199], v214 offset:16640\n\t" \
  "s_lshr_b32 s90, s87, 7\n\t" \
  "s_xor_b32 s90, s90, %[s0]\n\t" \
  "s_and_b32 s90, s90, 1\n\t" \
  "s_lshl1_add_u32 s90, s87, s90\n\t" \
  "s_lshl_b32 s90, s90, 13\n\t" \
  "s_cmp_lt_i32 s86, 6\n\t" \
  "s_cselect_b32 s90, 0x70000000, s90\n\t" \
  "s_waitcnt lgkmcnt(8)\n\t" \
  "v_mfma_f32_16x16x32_bf16 v[144:147], v[24:27], v[200:203], v[144:147]\n\t" \
  "v_mfma_f32_16x16x32_bf16 v[148:151], v[28:31], v[200:203], v[148:151]\n\t" \
  "ds_read_b128 v[200:203], v215 offset:256\n\t" \
  "s_add_u32 m0, %[lw], 3072\n\t" \
  "v_mfma_f32_16x16x32_bf16 v[152:155], v[24:27], v[204:207], v[152:155]\n\t" \
  "v_mfma_f32_16x16x32_bf16 v[156:159], v[28:31], v[204:207], v[156:159]\n\t" \
  "ds_read_b128 v[204:207], v215 offset:16640\n\t" \
  "global_load_lds_dwordx4 v219, s[92:93] sc1\n\t" \
  "s_waitcnt lgkmcnt(4)\n\t" \
  "s_cmp_lt_u32 s86, 6\n\t" \
  "s_cbranch_scc1 .Lw21%=\n\t" \
  "s_waitcnt vmcnt(4)\n\t" \
  "s_barrier\n\t" \
  ".Lg21%=:\n\t" \
  "s_add_u32 s92, s92, 512\n\t" \
  "s_addc_u32 s93, s93, 0\n\t" \
  "v_mfma_f32_16x16x32_bf16 v[144:147], v[32:35], v[176:179], v[144:147]\n\t" \
  "v_mfma_f32_16x16x32_bf16 v[148:151], v[36:39], v[176:179], v[148:151]\n\t" \
  "ds_read_b128 v[176:179], v212 offset:32768\n\t" \
  "v_mfma_f32_16x16x32_bf16 v[152:155], v[32:35], v[180:183], v[152:155]\n\t" \
  "v_mfma_f32_16x16x32_bf16 v[156:159], v[36:39], v[180:183], v[156:159]\n\t" \
  "ds_read_b128 v[180:183], v212 offset:49152\n\t" \
  "ds_read_b128 v[160:163], v221 offset:0\n\t" \
  "ds_read_b128 v[164:167], v221 offset:1024\n\t" \
  "s_sub_i32 s91, s86, 2\n\t" \
  "s_max_i32 s88, s88, s91\n\t" \
  "v_mov_b32 v173, s88\n\t" \
  "buffer_store_dword v173, v174, %[rs], 0 offen sc1\n\t" \
  "v_mfma_f32_16x16x32_bf16 v[144:147], v[40:43], v[184:187], v[144:147]\n\t" \
  "v_mfma_f32_16x16x32_bf16 v[148:151], v[44:47], v[184:187], v[148:151]\n\t" \
  "ds_read_b128 v[184:187], v213 offset:32768\n\t" \
  "s_add_u32 m0, %[lw], 32768\n\t" \
  "v_mfma_f32_16x16x32_bf16 v[152:155], v[40:43], v[188:191], v[152:155]\n\t" \
  "v_mfma_f32_16x16x32_bf16 v[156:159], v[44:47], v[188:191], v[156:159]\n\t" \
  "ds_read_b128 v[188:191], v213 offset:49152\n\t" \
  "global_load_lds_dwordx4 v216, s[92:93] sc1\n\t" \
  "s_waitcnt lgkmcnt(8)\n\t" \
  "v_mfma_f32_16x16x32_bf16 v[144:147], v[48:51], v[192:195], v[144:147]\n\t" \
  "v_mfma_f32_16x16x32_bf16 v[148:151], v[52:55], v[192:195], v[148:151]\n\t" \
  "ds_read_b128 v[192:195], v214 offset:32768\n\t" \
  "v_mfma_f32_16x16x32_bf16 v[152:155], v[48:51], v[196:199], v[152:155]\n\t" \
  "v_mfma_f32_16x16x32_bf16 v[156:159], v[52:55], v[196:199], v[156:159]\n\t" \
  "ds_read_b128 v[196:199], v214 offset:49152\n\t" \
  "s_waitcnt lgkmcnt(4)\n\t" \
  "v_add_f32 v160, v160, v128\n\t" \
  "v_add_f32 v161, v161, v129\n\t" \
  "v_add_f32 v162, v162, v130\n\t" \
  "v_add_f32 v163, v163, v131\n\t" \
  "v_add_f32 v164, v164, v132\n\t" \
  "v_add_f32 v165, v165, v133\n\t" \
  "v_add_f32 v166, v166, v134\n\t" \
  "v_add_f32 v167, v167, v135\n\t" \
  "v_mfma_f32_16x16x32_bf16 v[144:147], v[56:59], v[200:203], v[144:147]\n\t" \
  "v_mfma_f32_16x16x32_bf16 v[148:151], v[60:63], v[200:203], v[148:151]\n\t" \
  "ds_read_b128 v[200:203], v215 offset:32768\n\t" \
  "s_add_u32 m0, %[lw], 33792\n\t" \
  "v_mfma_f32_16x16x32_bf16 v[152:155], v[56:59], v[204:207], v[152:155]\n\t" \
  "v_mfma_f32_16x16x32_bf16 v[156:159], v[60:63], v[204:207], v[156:159]\n\t" \
  "ds_read_b128 v[204:207], v215 offset:49152\n\t" \
  "global_load_lds_dwordx4 v217, s[92:93] sc1\n\t" \
  "v_cvt_pk_bf16_f32 v168, v160, v161\n\t" \
  "v_cvt_pk_bf16_f32 v169, v162, v163\n\t" \
  "v_cvt_pk_bf16_f32 v170, v164, v165\n\t" \
  "v_cvt_pk_bf16_f32 v171, v166, v167\n\t" \
  "buffer_store_dwordx4 v[168:171], v172, %[rt], s90 offen sc1\n\t" \
  "v_mfma_f32_16x16x32_bf16 v[144:147], v[64:67], v[176:179], v[144:147]\n\t" \
  "v_mfma_f32_16x16x32_bf16 v[148:151], v[68:71], v[176:179], v[148:151]\n\t" \
  "ds_read_b128 v[176:179], v212 offset:33024\n\t" \
  "v_mfma_f32_16x16x32_bf16 v[152:155], v[64:67], v[180:183], v[152:155]\n\t" \
  "v_mfma_f32_16x16x32_bf16 v[156:159], v[68:71], v[180:183], v[156:159]\n\t" \
  "ds_read_b128 v[180:183], v212 offset:49408\n\t" \
  "s_waitcnt lgkmcnt(6)\n\t" \
  "v_mfma_f32_16x16x32_bf16 v[144:147], v[72:75], v[184:187], v[144:147]\n\t" \
  "v_mfma_f32_16x16x32_bf16 v[148:151], v[76:79], v[184:187], v[148:151]\n\t" \
  "ds_read_b128 v[184:187], v213 offset:33024\n\t" \
  "s_add_u32 m0, %[lw], 34816\n\t" \
  "v_mfma_f32_16x16x32_bf16 v[152:155], v[72:75], v[188:191], v[152:155]\n\t" \
  "v_mfma_f32_16x16x32_bf16 v[156:159], v[76:79], v[188:191], v[156:159]\n\t" \
  "ds_read_b128 v[188:191], v213 offset:49408\n\t" \
  "global_load_lds_dwordx4 v218, s[92:93] sc1\n\t" \
  "s_waitcnt lgkmcnt(6)\n\t" \
  "v_mfma_f32_16x16x32_bf16 v[144:147], v[80:83], v[192:195], v[144:147]\n\t" \
  "v_mfma_f32_16x16x32_bf16 v[148:151], v[84:87], v[192:195], v[148:151]\n\t" \
  "ds_read_b128 v[192:195], v214 offset:33024\n\t" \
  "v_mfma_f32_16x16x32_bf16 v[152:155], v[80:83], v[196:199], v[152:155]\n\t" \
  "v_mfma_f32_16x16x32_bf16 v[156:159], v[84:87], v[196:199], v[156:159]\n\t" \
  "ds_read_b128 v[196:199], v214 offset:49408\n\t" \
  "s_waitcnt lgkmcnt(6)\n\t" \
  "v_mfma_f32_16x16x32_bf16 v[144:147], v[88:91], v[200:203], v[144:147]\n\t" \
  "v_mfma_f32_16x16x32_bf16 v[148:151], v[92:95], v[200:203], v[148:151]\n\t" \
  "ds_read_b128 v[200:203], v215 offset:33024\n\t" \
  "s_add_u32 m0, %[lw], 35840\n\t" \
  "v_mfma_f32_16x16x32_bf16 v[152:155], v[88:91], v[204:207], v[152:155]\n\t" \
  "v_mfma_f32_16x16x32_bf16 v[156:159], v[92:95], v[204:207], v[156:159]\n\t" \
  "ds_read_b128 v[204:207], v215 offset:49408\n\t" \
  "global_load_lds_dwordx4 v219, s[92:93] sc1\n\t" \
  "s_cmp_lt_u32 s86, 8\n\t" \
  "s_cbranch_scc1 .Lw31%=\n\t" \
  "s_waitcnt vmcnt(6)\n\t" \
  "s_barrier\n\t" \
  ".Lg31%=:\n\t" \
  "s_add_u32 s92, s92, 65024\n\t" \
  "s_addc_u32 s93, s93, 0\n\t" \
  "s_waitcnt lgkmcnt(6)\n\t" \
  "v_mfma_f32_16x16x32_bf16 v[144:147], v[96:99], v[176:179], v[144:147]\n\t" \
  "v_mfma_f32_16x16x32_bf16 v[148:151], v[100:103], v[176:179], v[148:151]\n\t" \
  "ds_read_b128 v[176:179], v208 offset:0\n\t" \
  "v_mfma_f32_16x16x32_bf16 v[152:155], v[96:99], v[180:183], v[152:155]\n\t" \
  "v_mfma_f32_16x16x32_bf16 v[156:159], v[100:103], v[180:183], v[156:159]\n\t" \
  "ds_read_b128 v[180:183], v208 offset:16384\n\t" \
  "s_waitcnt lgkmcnt(6)\n\t" \
  "v_mfma_f32_16x16x32_bf16 v[144:147], v[104:107], v[184:187], v[144:147]\n\t" \
  "v_mfma_f32_16x16x32_bf16 v[148:151], v[108:111], v[184:187], v[148:151]\n\t" \
  "ds_read_b128 v[184:187], v209 offset:0\n\t" \
  "s_add_u32 m0, %[lw], 65536\n\t" \
  "v_mfma_f32_16x16x32_bf16 v[152:155], v[104:107], v[188:191], v[152:155]\n\t" \
  "v_mfma_f32_16x16x32_bf16 v[156:159], v[108:111], v[188:191], v[156:159]\n\t" \
  "ds_read_b128 v[188:191], v209 offset:16384\n\t" \
  "global_load_lds_dwordx4 v216, s[92:93] sc1\n\t" \
  "s_waitcnt lgkmcnt(6)\n\t" \
  "v_mfma_f32_16x16x32_bf16 v[144:147], v[112:115], v[192:195], v[144:147]\n\t" \
  "v_mfma_f32_16x16x32_bf16 v[148:151], v[116:119], v[192:195], v[148:151]\n\t" \
  "ds_read_b128 v[192:195], v210 offset:0\n\t" \
  "v_mfma_f32_16x16x32_bf16 v[152:155], v[112:115], v[196:199], v[152:155]\n\t" \
  "v_mfma_f32_16x16x32_bf16 v[156:159], v[116:119], v[196:199], v[156:159]\n\t" \
  "ds_read_b128 v[196:199], v210 offset:16384\n\t" \
  "s_waitcnt lgkmcnt(6)\n\t" \
  "v_mfma_f32_16x16x32_bf16 v[144:147], v[120:123], v[200:203], v[144:147]\n\t" \
  "v_mfma_f32_16x16x32_bf16 v[148:151], v[124:127], v[200:203], v[148:151]\n\t" \
  "ds_read_b128 v[200:203], v211 offset:0\n\t" \
  "s_add_u32 m0, %[lw], 66560\n\t" \
  "v_mfma_f32_16x16x32_bf16 v[152:155], v[120:123], v[204:207], v[152:155]\n\t" \
  "v_mfma_f32_16x16x32_bf16 v[156:159], v[124:127], v[204:207], v[156:159]\n\t" \
  "ds_read_b128 v[204:207], v211 offset:16384\n\t" \
  "global_load_lds_dwordx4 v217, s[92:93] sc1\n\t" \
  "s_add_u32 s86, s86, 1\n\t" \
  "s_add_u32 s87, s87, 1\n\t" \
  "s_cmp_lt_u32 s86, %[end]\n\t" \
  "s_cbranch_scc1 .Lp1%=\n\t" \
  "s_branch .Lx1%=\n\t" \
  ".Lf01%=:\n\t" \
  "s_nop 7\n\t" \
  "ds_write_b128 v220, v[152:155] offset:0\n\t" \
  "ds_write_b128 v220, v[156:159] offset:1024\n\t" \
  "s_lshr_b32 s90, s87, 7\n\t" \
  "s_xor_b32 s90, s90, %[s0]\n\t" \
  "s_and_b32 s90, s90, 1\n\t" \
  "s_lshl1_add_u32 s90, s87, s90\n\t" \
  "s_lshl_b32 s90, s90, 13\n\t" \
  "s_cmp_lt_i32 s86, 1\n\t" \
  "s_cselect_b32 s90, 0x70000000, s90\n\t" \
  "s_waitcnt lgkmcnt(0)\n\t" \
  "s_barrier\n\t" \
  "ds_read_b128 v[160:163], v221 offset:0\n\t" \
  "ds_read_b128 v[164:167], v221 offset:1024\n\t" \
  "s_waitcnt lgkmcnt(0)\n\t" \
  "v_add_f32 v160, v160, v144\n\t" \
  "v_add_f32 v161, v161, v145\n\t" \
  "v_add_f32 v162, v162, v146\n\t" \
  "v_add_f32 v163, v163, v147\n\t" \
  "v_add_f32 v164, v164, v148\n\t" \
  "v_add_f32 v165, v165, v149\n\t" \
  "v_add_f32 v166, v166, v150\n\t" \
  "v_add_f32 v167, v167, v151\n\t" \
  "v_cvt_pk_bf16_f32 v168, v160, v161\n\t" \
  "v_cvt_pk_bf16_f32 v169, v162, v163\n\t" \
  "v_cvt_pk_bf16_f32 v170, v164, v165\n\t" \
  "v_cvt_pk_bf16_f32 v171, v166, v167\n\t" \
  "buffer_store_dwordx4 v[168:171], v172, %[rt], s90 offen sc1\n\t" \
  "s_branch .Lfb01%=\n\t" \
  ".Lf11%=:\n\t" \
  "s_nop 7\n\t" \
  "ds_write_b128 v220, v[136:139] offset:0\n\t" \
  "ds_write_b128 v220, v[140:143] offset:1024\n\t" \
  "s_lshr_b32 s90, s87, 7\n\t" \
  "s_xor_b32 s90, s90, %[s0]\n\t" \
  "s_and_b32 s90, s90, 1\n\t" \
  "s_lshl1_add_u32 s90, s87, s90\n\t" \
  "s_lshl_b32 s90, s90, 13\n\t" \
  "s_cmp_lt_i32 s86, 1\n\t" \
  "s_cselect_b32 s90, 0x70000000, s90\n\t" \
  "s_waitcnt lgkmcnt(0)\n\t" \
  "s_barrier\n\t" \
  "ds_read_b128 v[160:163], v221 offset:0\n\t" \
  "ds_read_b128 v[164:167], v221 offset:1024\n\t" \
  "s_waitcnt lgkmcnt(0)\n\t" \
  "v_add_f32 v160, v160, v128\n\t" \
  "v_add_f32 v161, v161, v129\n\t" \
  "v_add_f32 v162, v162, v130\n\t" \
  "v_add_f32 v163, v163, v131\n\t" \
  "v_add_f32 v164, v164, v132\n\t" \
  "v_add_f32 v165, v165, v133\n\t" \
  "v_add_f32 v166, v166, v134\n\t" \
  "v_add_f32 v167, v167, v135\n\t" \
  "v_cvt_pk_bf16_f32 v168, v160, v161\n\t" \
  "v_cvt_pk_bf16_f32 v169, v162, v163\n\t" \
  "v_cvt_pk_bf16_f32 v170, v164, v165\n\t" \
  "v_cvt_pk_bf16_f32 v171, v166, v167\n\t" \
  "buffer_store_dwordx4 v[168:171], v172, %[rt], s90 offen sc1\n\t" \
  "s_branch .Lfb11%=\n\t" \
  ".Lw01%=:\n\t" \
  "s_waitcnt vmcnt(2)\n\t" \
  "s_barrier\n\t" \
  "s_sub_i32 s91, s86, 1\n\t" \
  "s_max_i32 s88, s88, s91\n\t" \
  "v_mov_b32 v173, s88\n\t" \
  "buffer_store_dword v173, v174, %[rs], 0 offen sc1\n\t" \
  "s_branch .Lg01%=\n\t" \
  ".Lw11%=:\n\t" \
  "s_waitcnt vmcnt(2)\n\t" \
  "s_barrier\n\t" \
  "s_sub_i32 s91, s86, 1\n\t" \
  "s_max_i32 s88, s88, s91\n\t" \
  "v_mov_b32 v173, s88\n\t" \
  "buffer_store_dword v173, v174, %[rs], 0 offen sc1\n\t" \
  "s_branch .Lg11%=\n\t" \
  ".Lw21%=:\n\t" \
  "s_waitcnt vmcnt(2)\n\t" \
  "s_barrier\n\t" \
  "s_sub_i32 s91, s86, 1\n\t" \
  "s_max_i32 s88, s88, s91\n\t" \
  "v_mov_b32 v173, s88\n\t" \
  "buffer_store_dword v173, v174, %[rs], 0 offen sc1\n\t" \
  "s_branch .Lg21%=\n\t" \
  ".Lw31%=:\n\t" \
  "s_waitcnt vmcnt(2)\n\t" \
  "s_barrier\n\t" \
  "s_sub_i32 s91, s86, 1\n\t" \
  "s_max_i32 s88, s88, s91\n\t" \
  "v_mov_b32 v173, s88\n\t" \
  "buffer_store_dword v173, v174, %[rs], 0 offen sc1\n\t" \
  "s_branch .Lg31%=\n\t" \
  ".Lx1%=:\n\t" \
  "s_waitcnt lgkmcnt(0)\n\t" \
  "s_setprio 0\n\t" \
  "s_mov_b32 m0, s94\n\t" \
  ".Le%=:\n\t"
#define PR_ASM_1 \
  "s_cmp_lg_u32 %[kh], 0\n\t" \
  "s_cbranch_scc1 .Lk%=\n\t" \
  "s_mov_b32 s94, m0\n\t" \
  "s_mov_b64 s[92:93], %[x0]\n\t" \
  "s_mov_b32 s86, %[g0]\n\t" \
  "s_add_u32 s87, s86, %[t0]\n\t" \
  "s_sub_i32 s88, s86, 3\n\t" \
  "ds_read_b128 v[176:179], v208 offset:0\n\t" \
  "ds_read_b128 v[180:183], v208 offset:16384\n\t" \
  "ds_read_b128 v[184:187], v209 offset:0\n\t" \
  "ds_read_b128 v[188:191], v209 offset:16384\n\t" \
  "ds_read_b128 v[192:195], v210 offset:0\n\t" \
  "ds_read_b128 v[196:199], v210 offset:16384\n\t" \
  "ds_read_b128 v[200:203], v211 offset:0\n\t" \
  "ds_read_b128 v[204:207], v211 offset:16384\n\t" \
  "s_branch .Lfb00%=\n\t" \
  ".Lp0%=:\n\t" \
  "s_cmp_le_u32 s86, 5\n\t" \
  "s_cbranch_scc1 .Lf00%=\n\t" \
  ".Lfb00%=:\n\t" \
  "s_waitcnt lgkmcnt(6)\n\t" \
  "v_mfma_f32_16x16x32_bf16 v[128:131], v[176:179], v[0:3], 0\n\t" \
  "v_mfma_f32_16x16x32_bf16 v[136:139], v[176:179], v[4:7], 0\n\t" \
  "ds_read_b128 v[176:179], v208 offset:256\n\t" \
  "s_add_u32 m0, %[lw], 67584\n\t" \
  "v_mfma_f32_16x16x32_bf16 v[132:135], v[180:183], v[0:3], 0\n\t" \
  "v_mfma_f32_16x16x32_bf16 v[140:143], v[180:183], v[4:7], 0\n\t" \
  "ds_read_b128 v[180:183], v208 offset:16640\n\t" \
  "global_load_lds_dwordx4 v218, s[92:93] sc1\n\t" \
  "s_waitcnt lgkmcnt(6)\n\t" \
  "v_mfma_f32_16x16x32_bf16 v[128:131], v[184:187], v[8:11], v[128:131]\n\t" \
  "v_mfma_f32_16x16x32_bf16 v[136:139], v[184:187], v[12:15], v[136:139]\n\t" \
  "ds_read_b128 v[184:187], v209 offset:256\n\t" \
  "v_mfma_f32_16x16x32_bf16 v[132:135], v[188:191], v[8:11], v[132:135]\n\t" \
  "v_mfma_f32_16x16x32_bf16 v[140:143], v[188:191], v[12:15], v[140:143]\n\t" \
  "ds_read_b128 v[188:191], v209 offset:16640\n\t" \
  "ds_write_b128 v220, v[152:155] offset:0\n\t" \
  "ds_write_b128 v220, v[156:159] offset:1024\n\t" \
  "s_waitcnt lgkmcnt(8)\n\t" \
  "v_mfma_f32_16x16x32_bf16 v[128:131], v[192:195], v[16:19], v[128:131]\n\t" \
  "v_mfma_f32_16x16x32_bf16 v[136:139], v[192:195], v[20:23], v[136:139]\n\t" \
  "ds_read_b128 v[192:195], v210 offset:256\n\t" \
  "s_add_u32 m0, %[lw], 68608\n\t" \
  "v_mfma_f32_16x16x32_bf16 v[132:135], v[196:199], v[16:19], v[132:135]\n\t" \
  "v_mfma_f32_16x16x32_bf16 v[140:143], v[196:199], v[20:23], v[140:143]\n\t" \
  "ds_read_b128 v[196:199], v210 offset:16640\n\t" \
  "global_load_lds_dwordx4 v219, s[92:93] sc1\n\t" \
  "s_lshr_b32 s90, s87, 7\n\t" \
  "s_xor_b32 s90, s90, %[s0]\n\t" \
  "s_and_b32 s90, s90, 1\n\t" \
  "s_lshl1_add_u32 s90, s87, s90\n\t" \
  "s_lshl_b32 s90, s90, 13\n\t" \
  "s_cmp_lt_i32 s86, 6\n\t" \
  "s_cselect_b32 s90, 0x70000000, s90\n\t" \
  "s_waitcnt lgkmcnt(8)\n\t" \
  "v_mfma_f32_16x16x32_bf16 v[128:131], v[200:203], v[24:27], v[128:131]\n\t" \
  "v_mfma_f32_16x16x32_bf16 v[136:139], v[200:203], v[28:31], v[136:139]\n\t" \
  "ds_read_b128 v[200:203], v211 offset:256\n\t" \
  "v_mfma_f32_16x16x32_bf16 v[132:135], v[204:207], v[24:27], v[132:135]\n\t" \
  "v_mfma_f32_16x16x32_bf16 v[140:143], v[204:207], v[28:31], v[140:143]\n\t" \
  "ds_read_b128 v[204:207], v211 offset:16640\n\t" \
  "s_waitcnt lgkmcnt(4)\n\t" \
  "s_cmp_lt_u32 s86, 6\n\t" \
  "s_cbranch_scc1 .Lw00%=\n\t" \
  "s_waitcnt vmcnt(4)\n\t" \
  "s_barrier\n\t" \
  ".Lg00%=:\n\t" \
  "s_add_u32 s92, s92, 512\n\t" \
  "s_addc_u32 s93, s93, 0\n\t" \
  "v_mfma_f32_16x16x32_bf16 v[128:131], v[176:179], v[32:35], v[128:131]\n\t" \
  "v_mfma_f32_16x16x32_bf16 v[136:139], v[176:179], v[36:39], v[136:139]\n\t" \
  "ds_read_b128 v[176:179], v208 offset:32768\n\t" \
  "s_add_u32 m0, %[lw], 98304\n\t" \
  "v_mfma_f32_16x16x32_bf16 v[132:135], v[180:183], v[32:35], v[132:135]\n\t" \
  "v_mfma_f32_16x16x32_bf16 v[140:143], v[180:183], v[36:39], v[140:143]\n\t" \
  "ds_read_b128 v[180:183], v208 offset:49152\n\t" \
  "global_load_lds_dwordx4 v216, s[92:93] sc1\n\t" \
  "ds_read_b128 v[160:163], v221 offset:0\n\t" \
  "ds_read_b128 v[164:167], v221 offset:1024\n\t" \
  "v_mfma_f32_16x16x32_bf16 v[128:131], v[184:187], v[40:43], v[128:131]\n\t" \
  "v_mfma_f32_16x16x32_bf16 v[136:139], v[184:187], v[44:47], v[136:139]\n\t" \
  "ds_read_b128 v[184:187], v209 offset:32768\n\t" \
  "v_mfma_f32_16x16x32_bf16 v[132:135], v[188:191], v[40:43], v[132:135]\n\t" \
  "v_mfma_f32_16x16x32_bf16 v[140:143], v[188:191], v[44:47], v[140:143]\n\t" \
  "ds_read_b128 v[188:191], v209 offset:49152\n\t" \
  "s_waitcnt lgkmcnt(8)\n\t" \
  "v_mfma_f32_16x16x32_bf16 v[128:131], v[192:195], v[48:51], v[128:131]\n\t" \
  "v_mfma_f32_16x16x32_bf16 v[136:139], v[192:195], v[52:55], v[136:139]\n\t" \
  "ds_read_b128 v[192:195], v210 offset:32768\n\t" \
  "s_add_u32 m0, %[lw], 99328\n\t" \
  "v_mfma_f32_16x16x32_bf16 v[132:135], v[196:199], v[48:51], v[132:135]\n\t" \
  "v_mfma_f32_16x16x32_bf16 v[140:143], v[196:199], v[52:55], v[140:143]\n\t" \
  "ds_read_b128 v[196:199], v210 offset:49152\n\t" \
  "global_load_lds_dwordx4 v217, s[92:93] sc1\n\t" \
  "s_waitcnt lgkmcnt(4)\n\t" \
  "v_add_f32 v160, v160, v144\n\t" \
  "v_add_f32 v161, v161, v145\n\t" \
  "v_add_f32 v162, v162, v146\n\t" \
  "v_add_f32 v163, v163, v147\n\t" \
  "v_add_f32 v164, v164, v148\n\t" \
  "v_add_f32 v165, v165, v149\n\t" \
  "v_add_f32 v166, v166, v150\n\t" \
  "v_add_f32 v167, v167, v151\n\t" \
  "v_mfma_f32_16x16x32_bf16 v[128:131], v[200:203], v[56:59], v[128:131]\n\t" \
  "v_mfma_f32_16x16x32_bf16 v[136:139], v[200:203], v[60:63], v[136:139]\n\t" \
  "ds_read_b128 v[200:203], v211 offset:32768\n\t" \
  "v_mfma_f32_16x16x32_bf16 v[132:135], v[204:207], v[56:59], v[132:135]\n\t" \
  "v_mfma_f32_16x16x32_bf16 v[140:143], v[204:207], v[60:63], v[140:143]\n\t" \
  "ds_read_b128 v[204:207], v211 offset:49152\n\t" \
  "v_cvt_pk_bf16_f32 v168, v160, v161\n\t" \
  "v_cvt_pk_bf16_f32 v169, v162, v163\n\t" \
  "v_cvt_pk_bf16_f32 v170, v164, v165\n\t" \
  "v_cvt_pk_bf16_f32 v171, v166, v167\n\t" \
  "buffer_store_dwordx4 v[168:171], v172, %[rt], s90 offen sc1\n\t" \
  "v_mfma_f32_16x16x32_bf16 v[128:131], v[176:179], v[64:67], v[128:131]\n\t" \
  "v_mfma_f32_16x16x32_bf16 v[136:139], v[176:179], v[68:71], v[136:139]\n\t" \
  "ds_read_b128 v[176:179], v208 offset:33024\n\t" \
  "s_add_u32 m0, %[lw], 100352\n\t" \
  "v_mfma_f32_16x16x32_bf16 v[132:135], v[180:183], v[64:67], v[132:135]\n\t" \
  "v_mfma_f32_16x16x32_bf16 v[140:143], v[180:183], v[68:71], v[140:143]\n\t" \
  "ds_read_b128 v[180:183], v208 offset:49408\n\t" \
  "global_load_lds_dwordx4 v218, s[92:93] sc1\n\t" \
  "s_waitcnt lgkmcnt(6)\n\t" \
  "v_mfma_f32_16x16x32_bf16 v[128:131], v[184:187], v[72:75], v[128:131]\n\t" \
  "v_mfma_f32_16x16x32_bf16 v[136:139], v[184:187], v[76:79], v[136:139]\n\t" \
  "ds_read_b128 v[184:187], v209 offset:33024\n\t" \
  "v_mfma_f32_16x16x32_bf16 v[132:135], v[188:191], v[72:75], v[132:135]\n\t" \
  "v_mfma_f32_16x16x32_bf16 v[140:143], v[188:191], v[76:79], v[140:143]\n\t" \
  "ds_read_b128 v[188:191], v209 offset:49408\n\t" \
  "s_waitcnt lgkmcnt(6)\n\t" \
  "v_mfma_f32_16x16x32_bf16 v[128:131], v[192:195], v[80:83], v[128:131]\n\t" \
  "v_mfma_f32_16x16x32_bf16 v[136:139], v[192:195], v[84:87], v[136:139]\n\t" \
  "ds_read_b128 v[192:195], v210 offset:33024\n\t" \
  "s_add_u32 m0, %[lw], 101376\n\t" \
  "v_mfma_f32_16x16x32_bf16 v[132:135], v[196:199], v[80:83], v[132:135]\n\t" \
  "v_mfma_f32_16x16x32_bf16 v[140:143], v[196:199], v[84:87], v[140:143]\n\t" \
  "ds_read_b128 v[196:199], v210 offset:49408\n\t" \
  "global_load_lds_dwordx4 v219, s[92:93] sc1\n\t" \
  "s_waitcnt lgkmcnt(6)\n\t" \
  "v_mfma_f32_16x16x32_bf16 v[128:131], v[200:203], v[88:91], v[128:131]\n\t" \
  "v_mfma_f32_16x16x32_bf16 v[136:139], v[200:203], v[92:95], v[136:139]\n\t" \
  "ds_read_b128 v[200:203], v211 offset:33024\n\t" \
  "v_mfma_f32_16x16x32_bf16 v[132:135], v[204:207], v[88:91], v[132:135]\n\t" \
  "v_mfma_f32_16x16x32_bf16 v[140:143], v[204:207], v[92:95], v[140:143]\n\t" \
  "ds_read_b128 v[204:207], v211 offset:49408\n\t" \
  "s_cmp_lt_u32 s86, 8\n\t" \
  "s_cbranch_scc1 .Lw10%=\n\t" \
  "s_waitcnt vmcnt(5)\n\t" \
  "s_barrier\n\t" \
  ".Lg10%=:\n\t" \
  "s_add_u32 s92, s92, 65024\n\t" \
  "s_addc_u32 s93, s93, 0\n\t" \
  "s_waitcnt lgkmcnt(6)\n\t" \
  "v_mfma_f32_16x16x32_bf16 v[128:131], v[176:179], v[96:99], v[128:131]\n\t" \
  "v_mfma_f32_16x16x32_bf16 v[136:139], v[176:179], v[100:103], v[136:139]\n\t" \
  "ds_read_b128 v[176:179], v212 offset:0\n\t" \
  "s_add_u32 m0, %[lw], 0\n\t" \
  "v_mfma_f32_16x16x32_bf16 v[132:135], v[180:183], v[96:99], v[132:135]\n\t" \
  "v_mfma_f32_16x16x32_bf16 v[140:143], v[180:183], v[100:103], v[140:143]\n\t" \
  "ds_read_b128 v[180:183], v212 offset:16384\n\t" \
  "global_load_lds_dwordx4 v216, s[92:93] sc1\n\t" \
  "s_waitcnt lgkmcnt(6)\n\t" \
  "v_mfma_f32_16x16x32_bf16 v[128:131], v[184:187], v[104:107], v[128:131]\n\t" \
  "v_mfma_f32_16x16x32_bf16 v[136:139], v[184:187], v[108:111], v[136:139]\n\t" \
  "ds_read_b128 v[184:187], v213 offset:0\n\t" \
  "v_mfma_f32_16x16x32_bf16 v[132:135], v[188:191], v[104:107], v[132:135]\n\t" \
  "v_mfma_f32_16x16x32_bf16 v[140:143], v[188:191], v[108:111], v[140:143]\n\t" \
  "ds_read_b128 v[188:191], v213 offset:16384\n\t" \
  "s_waitcnt lgkmcnt(6)\n\t" \
  "v_mfma_f32_16x16x32_bf16 v[128:131], v[192:195], v[112:115], v[128:131]\n\t" \
  "v_mfma_f32_16x16x32_bf16 v[136:139], v[192:195], v[116:119], v[136:139]\n\t" \
  "ds_read_b128 v[192:195], v214 offset:0\n\t" \
  "s_add_u32 m0, %[lw], 1024\n\t" \
  "v_mfma_f32_16x16x32_bf16 v[132:135], v[196:199], v[112:115], v[132:135]\n\t" \
  "v_mfma_f32_16x16x32_bf16 v[140:143], v[196:199], v[116:119], v[140:143]\n\t" \
  "ds_read_b128 v[196:199], v214 offset:16384\n\t" \
  "global_load_lds_dwordx4 v217, s[92:93] sc1\n\t" \
  "s_waitcnt lgkmcnt(6)\n\t" \
  "v_mfma_f32_16x16x32_bf16 v[128:131], v[200:203], v[120:123], v[128:131]\n\t" \
  "v_mfma_f32_16x16x32_bf16 v[136:139], v[200:203], v[124:127], v[136:139]\n\t" \
  "ds_read_b128 v[200:203], v215 offset:0\n\t" \
  "v_mfma_f32_16x16x32_bf16 v[132:135], v[204:207], v[120:123], v[132:135]\n\t" \
  "v_mfma_f32_16x16x32_bf16 v[140:143], v[204:207], v[124:127], v[140:143]\n\t" \
  "ds_read_b128 v[204:207], v215 offset:16384\n\t" \
  "s_add_u32 s86, s86, 1\n\t" \
  "s_add_u32 s87, s87, 1\n\t" \
  "s_cmp_le_u32 s86, 5\n\t" \
  "s_cbranch_scc1 .Lf10%=\n\t" \
  ".Lfb10%=:\n\t" \
  "s_waitcnt lgkmcnt(6)\n\t" \
  "v_mfma_f32_16x16x32_bf16 v[144:147], v[176:179], v[0:3], 0\n\t" \
  "v_mfma_f32_16x16x32_bf16 v[152:155], v[176:179], v[4:7], 0\n\t" \
  "ds_read_b128 v[176:179], v212 offset:256\n\t" \
  "s_add_u32 m0, %[lw], 2048\n\t" \
  "v_mfma_f32_16x16x32_bf16 v[148:151], v[180:183], v[0:3], 0\n\t" \
  "v_mfma_f32_16x16x32_bf16 v[156:159], v[180:183], v[4:7], 0\n\t" \
  "ds_read_b128 v[180:183], v212 offset:16640\n\t" \
  "global_load_lds_dwordx4 v218, s[92:93] sc1\n\t" \
  "s_waitcnt lgkmcnt(6)\n\t" \
  "v_mfma_f32_16x16x32_bf16 v[144:147], v[184:187], v[8:11], v[144:147]\n\t" \
  "v_mfma_f32_16x16x32_bf16 v[152:155], v[184:187], v[12:15], v[152:155]\n\t" \
  "ds_read_b128 v[184:187], v213 offset:256\n\t" \
  "v_mfma_f32_16x16x32_bf16 v[148:151], v[188:191], v[8:11], v[148:151]\n\t" \
  "v_mfma_f32_16x16x32_bf16 v[156:159], v[188:191], v[12:15], v[156:159]\n\t" \
  "ds_read_b128 v[188:191], v213 offset:16640\n\t" \
  "ds_write_b128 v220, v[136:139] offset:0\n\t" \
  "ds_write_b128 v220, v[140:143] offset:1024\n\t" \
  "s_waitcnt lgkmcnt(8)\n\t" \
  "v_mfma_f32_16x16x32_bf16 v[144:147], v[192:195], v[16:19], v[144:147]\n\t" \
  "v_mfma_f32_16x16x32_bf16 v[152:155], v[192:195], v[20:23], v[152:155]\n\t" \
  "ds_read_b128 v[192:195], v214 offset:256\n\t" \
  "s_add_u32 m0, %[lw], 3072\n\t" \
  "v_mfma_f32_16x16x32_bf16 v[148:151], v[196:199], v[16:19], v[148:151]\n\t" \
  "v_mfma_f32_16x16x32_bf16 v[156:159], v[196:199], v[20:23], v[156:159]\n\t" \
  "ds_read_b128 v[196:199], v214 offset:16640\n\t" \
  "global_load_lds_dwordx4 v219, s[92:93] sc1\n\t" \
  "s_lshr_b32 s90, s87, 7\n\t" \
  "s_xor_b32 s90, s90, %[s0]\n\t" \
  "s_and_b32 s90, s90, 1\n\t" \
  "s_lshl1_add_u32 s90, s87, s90\n\t" \
  "s_lshl_b32 s90, s90, 13\n\t" \
  "s_cmp_lt_i32 s86, 6\n\t" \
  "s_cselect_b32 s90, 0x70000000, s90\n\t" \
  "s_waitcnt lgkmcnt(8)\n\t" \
  "v_mfma_f32_16x16x32_bf16 v[144:147], v[200:203], v[24:27], v[144:147]\n\t" \
  "v_mfma_f32_16x16x32_bf16 v[152:155], v[200:203], v[28:31], v[152:155]\n\t" \
  "ds_read_b128 v[200:203], v215 offset:256\n\t" \
  "v_mfma_f32_16x16x32_bf16 v[148:151], v[204:207], v[24:27], v[148:151]\n\t" \
  "v_mfma_f32_16x16x32_bf16 v[156:159], v[204:207], v[28:31], v[156:159]\n\t" \
  "ds_read_b128 v[204:207], v215 offset:16640\n\t" \
  "s_waitcnt lgkmcnt(4)\n\t" \
  "s_cmp_lt_u32 s86, 6\n\t" \
  "s_cbranch_scc1 .Lw20%=\n\t" \
  "s_waitcnt vmcnt(4)\n\t" \
  "s_barrier\n\t" \
  ".Lg20%=:\n\t" \
  "s_add_u32 s92, s92, 512\n\t" \
  "s_addc_u32 s93, s93, 0\n\t" \
  "v_mfma_f32_16x16x32_bf16 v[144:147], v[176:179], v[32:35], v[144:147]\n\t" \
  "v_mfma_f32_16x16x32_bf16 v[152:155], v[176:179], v[36:39], v[152:155]\n\t" \
  "ds_read_b128 v[176:179], v212 offset:32768\n\t" \
  "s_add_u32 m0, %[lw], 32768\n\t" \
  "v_mfma_f32_16x16x32_bf16 v[148:151], v[180:183], v[32:35], v[148:151]\n\t" \
  "v_mfma_f32_16x16x32_bf16 v[156:159], v[180:183], v[36:39], v[156:159]\n\t" \
  "ds_read_b128 v[180:183], v212 offset:49152\n\t" \
  "global_load_lds_dwordx4 v216, s[92:93] sc1\n\t" \
  "ds_read_b128 v[160:163], v221 offset:0\n\t" \
  "ds_read_b128 v[164:167], v221 offset:1024\n\t" \
  "v_mfma_f32_16x16x32_bf16 v[144:147], v[184:187], v[40:43], v[144:147]\n\t" \
  "v_mfma_f32_16x16x32_bf16 v[152:155], v[184:187], v[44:47], v[152:155]\n\t" \
  "ds_read_b128 v[184:187], v213 offset:32768\n\t" \
  "v_mfma_f32_16x16x32_bf16 v[148:151], v[188:191], v[40:43], v[148:151]\n\t" \
  "v_mfma_f32_16x16x32_bf16 v[156:159], v[188:191], v[44:47], v[156:159]\n\t" \
  "ds_read_b128 v[188:191], v213 offset:49152\n\t" \
  "s_waitcnt lgkmcnt(8)\n\t" \
  "v_mfma_f32_16x16x32_bf16 v[144:147], v[192:195], v[48:51], v[144:147]\n\t" \
  "v_mfma_f32_16x16x32_bf16 v[152:155], v[192:195], v[52:55], v[152:155]\n\t" \
  "ds_read_b128 v[192:195], v214 offset:32768\n\t" \
  "s_add_u32 m0, %[lw], 33792\n\t" \
  "v_mfma_f32_16x16x32_bf16 v[148:151], v[196:199], v[48:51], v[148:151]\n\t" \
  "v_mfma_f32_16x16x32_bf16 v[156:159], v[196:199], v[52:55], v[156:159]\n\t" \
  "ds_read_b128 v[196:199], v214 offset:49152\n\t" \
  "global_load_lds_dwordx4 v217, s[92:93] sc1\n\t" \
  "s_waitcnt lgkmcnt(4)\n\t" \
  "v_add_f32 v160, v160, v128\n\t" \
  "v_add_f32 v161, v161, v129\n\t" \
  "v_add_f32 v162, v162, v130\n\t" \
  "v_add_f32 v163, v163, v131\n\t" \
  "v_add_f32 v164, v164, v132\n\t" \
  "v_add_f32 v165, v165, v133\n\t" \
  "v_add_f32 v166, v166, v134\n\t" \
  "v_add_f32 v167, v167, v135\n\t" \
  "v_mfma_f32_16x16x32_bf16 v[144:147], v[200:203], v[56:59], v[144:147]\n\t" \
  "v_mfma_f32_16x16x32_bf16 v[152:155], v[200:203], v[60:63], v[152:155]\n\t" \
  "ds_read_b128 v[200:203], v215 offset:32768\n\t" \
  "v_mfma_f32_16x16x32_bf16 v[148:151], v[204:207], v[56:59], v[148:151]\n\t" \
  "v_mfma_f32_16x16x32_bf16 v[156:159], v[204:207], v[60:63], v[156:159]\n\t" \
  "ds_read_b128 v[204:207], v215 offset:49152\n\t" \
  "v_cvt_pk_bf16_f32 v168, v160, v161\n\t" \
  "v_cvt_pk_bf16_f32 v169, v162, v163\n\t" \
  "v_cvt_pk_bf16_f32 v170, v164, v165\n\t" \
  "v_cvt_pk_bf16_f32 v171, v166, v167\n\t" \
  "buffer_store_dwordx4 v[168:171], v172, %[rt], s90 offen sc1\n\t" \
  "v_mfma_f32_16x16x32_bf16 v[144:147], v[176:179], v[64:67], v[144:147]\n\t" \
  "v_mfma_f32_16x16x32_bf16 v[152:155], v[176:179], v[68:71], v[152:155]\n\t" \
  "ds_read_b128 v[176:179], v212 offset:33024\n\t" \
  "s_add_u32 m0, %[lw], 34816\n\t" \
  "v_mfma_f32_16x16x32_bf16 v[148:151], v[180:183], v[64:67], v[148:151]\n\t" \
  "v_mfma_f32_16x16x32_bf16 v[156:159], v[180:183], v[68:71], v[156:159]\n\t" \
  "ds_read_b128 v[180:183], v212 offset:49408\n\t" \
  "global_load_lds_dwordx4 v218, s[92:93] sc1\n\t" \
  "s_waitcnt lgkmcnt(6)\n\t" \
  "v_mfma_f32_16x16x32_bf16 v[144:147], v[184:187], v[72:75], v[144:147]\n\t" \
  "v_mfma_f32_16x16x32_bf16 v[152:155], v[184:187], v[76:79], v[152:155]\n\t" \
  "ds_read_b128 v[184:187], v213 offset:33024\n\t" \
  "v_mfma_f32_16x16x32_bf16 v[148:151], v[188:191], v[72:75], v[148:151]\n\t" \
  "v_mfma_f32_16x16x32_bf16 v[156:159], v[188:191], v[76:79], v[156:159]\n\t" \
  "ds_read_b128 v[188:191], v213 offset:49408\n\t" \
  "s_waitcnt lgkmcnt(6)\n\t" \
  "v_mfma_f32_16x16x32_bf16 v[144:147], v[192:195], v[80:83], v[144:147]\n\t" \
  "v_mfma_f32_16x16x32_bf16 v[152:155], v[192:195], v[84:87], v[152:155]\n\t" \
  "ds_read_b128 v[192:195], v214 offset:33024\n\t" \
  "s_add_u32 m0, %[lw], 35840\n\t" \
  "v_mfma_f32_16x16x32_bf16 v[148:151], v[196:199], v[80:83], v[148:151]\n\t" \
  "v_mfma_f32_16x16x32_bf16 v[156:159], v[196:199], v[84:87], v[156:159]\n\t" \
  "ds_read_b128 v[196:199], v214 offset:49408\n\t" \
  "global_load_lds_dwordx4 v219, s[92:93] sc1\n\t" \
  "s_waitcnt lgkmcnt(6)\n\t" \
  "v_mfma_f32_16x16x32_bf16 v[144:147], v[200:203], v[88:91], v[144:147]\n\t" \
  "v_mfma_f32_16x16x32_bf16 v[152:155], v[200:203], v[92:95], v[152:155]\n\t" \
  "ds_read_b128 v[200:203], v215 offset:33024\n\t" \
  "v_mfma_f32_16x16x32_bf16 v[148:151], v[204:207], v[88:91], v[148:151]\n\t" \
  "v_mfma_f32_16x16x32_bf16 v[156:159], v[204:207], v[92:95], v[156:159]\n\t" \
  "ds_read_b128 v[204:207], v215 offset:49408\n\t" \
  "s_cmp_lt_u32 s86, 8\n\t" \
  "s_cbranch_scc1 .Lw30%=\n\t" \
  "s_waitcnt vmcnt(5)\n\t" \
  "s_barrier\n\t" \
  ".Lg30%=:\n\t" \
  "s_add_u32 s92, s92, 65024\n\t" \
  "s_addc_u32 s93, s93, 0\n\t" \
  "s_waitcnt lgkmcnt(6)\n\t" \
  "v_mfma_f32_16x16x32_bf16 v[144:147], v[176:179], v[96:99], v[144:147]\n\t" \
  "v_mfma_f32_16x16x32_bf16 v[152:155], v[176:179], v[100:103], v[152:155]\n\t" \
  "ds_read_b128 v[176:179], v208 offset:0\n\t" \
  "s_add_u32 m0, %[lw], 65536\n\t" \
  "v_mfma_f32_16x16x32_bf16 v[148:151], v[180:183], v[96:99], v[148:151]\n\t" \
  "v_mfma_f32_16x16x32_bf16 v[156:159], v[180:183], v[100:103], v[156:159]\n\t" \
  "ds_read_b128 v[180:183], v208 offset:16384\n\t" \
  "global_load_lds_dwordx4 v216, s[92:93] sc1\n\t" \
  "s_waitcnt lgkmcnt(6)\n\t" \
  "v_mfma_f32_16x16x32_bf16 v[144:147], v[184:187], v[104:107], v[144:147]\n\t" \
  "v_mfma_f32_16x16x32_bf16 v[152:155], v[184:187], v[108:111], v[152:155]\n\t" \
  "ds_read_b128 v[184:187], v209 offset:0\n\t" \
  "v_mfma_f32_16x16x32_bf16 v[148:151], v[188:191], v[104:107], v[148:151]\n\t" \
  "v_mfma_f32_16x16x32_bf16 v[156:159], v[188:191], v[108:111], v[156:159]\n\t" \
  "ds_read_b128 v[188:191], v209 offset:16384\n\t" \
  "s_waitcnt lgkmcnt(6)\n\t" \
  "v_mfma_f32_16x16x32_bf16 v[144:147], v[192:195], v[112:115], v[144:147]\n\t" \
  "v_mfma_f32_16x16x32_bf16 v[152:155], v[192:195], v[116:119], v[152:155]\n\t" \
  "ds_read_b128 v[192:195], v210 offset:0\n\t" \
  "s_add_u32 m0, %[lw], 66560\n\t" \
  "v_mfma_f32_16x16x32_bf16 v[148:151], v[196:199], v[112:115], v[148:151]\n\t" \
  "v_mfma_f32_16x16x32_bf16 v[156:159], v[196:199], v[116:119], v[156:159]\n\t" \
  "ds_read_b128 v[196:199], v210 offset:16384\n\t" \
  "global_load_lds_dwordx4 v217, s[92:93] sc1\n\t" \
  "s_waitcnt lgkmcnt(6)\n\t" \
  "v_mfma_f32_16x16x32_bf16 v[144:147], v[200:203], v[120:123], v[144:147]\n\t" \
  "v_mfma_f32_16x16x32_bf16 v[152:155], v[200:203], v[124:127], v[152:155]\n\t" \
  "ds_read_b128 v[200:203], v211 offset:0\n\t" \
  "v_mfma_f32_16x16x32_bf16 v[148:151], v[204:207], v[120:123], v[148:151]\n\t" \
  "v_mfma_f32_16x16x32_bf16 v[156:159], v[204:207], v[124:127], v[156:159]\n\t" \
  "ds_read_b128 v[204:207], v211 offset:16384\n\t" \
  "s_add_u32 s86, s86, 1\n\t" \
  "s_add_u32 s87, s87, 1\n\t" \
  "s_cmp_lt_u32 s86, %[end]\n\t" \
  "s_cbranch_scc1 .Lp0%=\n\t" \
  "s_branch .Lx0%=\n\t" \
  ".Lf00%=:\n\t" \
  "s_nop 7\n\t" \
  "ds_write_b128 v220, v[152:155] offset:0\n\t" \
  "ds_write_b128 v220, v[156:159] offset:1024\n\t" \
  "s_lshr_b32 s90, s87, 7\n\t" \
  "s_xor_b32 s90, s90, %[s0]\n\t" \
  "s_and_b32 s90, s90, 1\n\t" \
  "s_lshl1_add_u32 s90, s87, s90\n\t" \
  "s_lshl_b32 s90, s90, 13\n\t" \
  "s_cmp_lt_i32 s86, 1\n\t" \
  "s_cselect_b32 s90, 0x70000000, s90\n\t" \
  "s_waitcnt lgkmcnt(0)\n\t" \
  "s_barrier\n\t" \
  "ds_read_b128 v[160:163], v221 offset:0\n\t" \
  "ds_read_b128 v[164:167], v221 offset:1024\n\t" \
  "s_waitcnt lgkmcnt(0)\n\t" \
  "v_add_f32 v160, v160, v144\n\t" \
  "v_add_f32 v161, v161, v145\n\t" \
  "v_add_f32 v162, v162, v146\n\t" \
  "v_add_f32 v163, v163, v147\n\t" \
  "v_add_f32 v164, v164, v148\n\t" \
  "v_add_f32 v165, v165, v149\n\t" \
  "v_add_f32 v166, v166, v150\n\t" \
  "v_add_f32 v167, v167, v151\n\t" \
  "v_cvt_pk_bf16_f32 v168, v160, v161\n\t" \
  "v_cvt_pk_bf16_f32 v169, v162, v163\n\t" \
  "v_cvt_pk_bf16_f32 v170, v164, v165\n\t" \
  "v_cvt_pk_bf16_f32 v171, v166, v167\n\t" \
  "buffer_store_dwordx4 v[168:171], v172, %[rt], s90 offen sc1\n\t" \
  "s_branch .Lfb00%=\n\t" \
  ".Lf10%=:\n\t" \
  "s_nop 7\n\t" \
  "ds_write_b128 v220, v[136:139] offset:0\n\t" \
  "ds_write_b128 v220, v[140:143] offset:1024\n\t" \
  "s_lshr_b32 s90, s87, 7\n\t" \
  "s_xor_b32 s90, s90, %[s0]\n\t" \
  "s_and_b32 s90, s90, 1\n\t" \
  "s_lshl1_add_u32 s90, s87, s90\n\t" \
  "s_lshl_b32 s90, s90, 13\n\t" \
  "s_cmp_lt_i32 s86, 1\n\t" \
  "s_cselect_b32 s90, 0x70000000, s90\n\t" \
  "s_waitcnt lgkmcnt(0)\n\t" \
  "s_barrier\n\t" \
  "ds_read_b128 v[160:163], v221 offset:0\n\t" \
  "ds_read_b128 v[164:167], v221 offset:1024\n\t" \
  "s_waitcnt lgkmcnt(0)\n\t" \
  "v_add_f32 v160, v160, v128\n\t" \
  "v_add_f32 v161, v161, v129\n\t" \
  "v_add_f32 v162, v162, v130\n\t" \
  "v_add_f32 v163, v163, v131\n\t" \
  "v_add_f32 v164, v164, v132\n\t" \
  "v_add_f32 v165, v165, v133\n\t" \
  "v_add_f32 v166, v166, v134\n\t" \
  "v_add_f32 v167, v167, v135\n\t" \
  "v_cvt_pk_bf16_f32 v168, v160, v161\n\t" \
  "v_cvt_pk_bf16_f32 v169, v162, v163\n\t" \
  "v_cvt_pk_bf16_f32 v170, v164, v165\n\t" \
  "v_cvt_pk_bf16_f32 v171, v166, v167\n\t" \
  "buffer_store_dwordx4 v[168:171], v172, %[rt], s90 offen sc1\n\t" \
  "s_branch .Lfb10%=\n\t" \
  ".Lw00%=:\n\t" \
  "s_waitcnt vmcnt(2)\n\t" \
  "s_barrier\n\t" \
  "s_branch .Lg00%=\n\t" \
  ".Lw10%=:\n\t" \
  "s_waitcnt vmcnt(2)\n\t" \
  "s_barrier\n\t" \
  "s_branch .Lg10%=\n\t" \
  ".Lw20%=:\n\t" \
  "s_waitcnt vmcnt(2)\n\t" \
  "s_barrier\n\t" \
  "s_branch .Lg20%=\n\t" \
  ".Lw30%=:\n\t" \
  "s_waitcnt vmcnt(2)\n\t" \
  "s_barrier\n\t" \
  "s_branch .Lg30%=\n\t" \
  ".Lx0%=:\n\t" \
  "s_waitcnt lgkmcnt(0)\n\t" \
  "s_setprio 0\n\t" \
  "s_mov_b32 m0, s94\n\t" \
  "s_branch .Le%=\n\t" \
  ".Lk%=:\n\t" \
  "s_mov_b32 s94, m0\n\t" \
  "s_mov_b64 s[92:93], %[x0]\n\t" \
  "s_mov_b32 s86, %[g0]\n\t" \
  "s_add_u32 s87, s86, %[t0]\n\t" \
  "s_sub_i32 s88, s86, 3\n\t" \
  "ds_read_b128 v[176:179], v208 offset:0\n\t" \
  "ds_read_b128 v[180:183], v208 offset:16384\n\t" \
  "ds_read_b128 v[184:187], v209 offset:0\n\t" \
  "ds_read_b128 v[188:191], v209 offset:16384\n\t" \
  "ds_read_b128 v[192:195], v210 offset:0\n\t" \
  "ds_read_b128 v[196:199], v210 offset:16384\n\t" \
  "ds_read_b128 v[200:203], v211 offset:0\n\t" \
  "ds_read_b128 v[204:207], v211 offset:16384\n\t" \
  "s_branch .Lfb01%=\n\t" \
  ".Lp1%=:\n\t" \
  "s_cmp_le_u32 s86, 5\n\t" \
  "s_cbranch_scc1 .Lf01%=\n\t" \
  ".Lfb01%=:\n\t" \
  "s_waitcnt lgkmcnt(6)\n\t" \
  "v_mfma_f32_16x16x32_bf16 v[128:131], v[176:179], v[0:3], 0\n\t" \
  "v_mfma_f32_16x16x32_bf16 v[136:139], v[176:179], v[4:7], 0\n\t" \
  "ds_read_b128 v[176:179], v208 offset:256\n\t" \
  "v_mfma_f32_16x16x32_bf16 v[132:135], v[180:183], v[0:3], 0\n\t" \
  "v_mfma_f32_16x16x32_bf16 v[140:143], v[180:183], v[4:7], 0\n\t" \
  "ds_read_b128 v[180:183], v208 offset:16640\n\t" \
  "s_waitcnt lgkmcnt(6)\n\t" \
  "v_mfma_f32_16x16x32_bf16 v[128:131], v[184:187], v[8:11], v[128:131]\n\t" \
  "v_mfma_f32_16x16x32_bf16 v[136:139], v[184:187], v[12:15], v[136:139]\n\t" \
  "ds_read_b128 v[184:187], v209 offset:256\n\t" \
  "s_add_u32 m0, %[lw], 67584\n\t" \
  "v_mfma_f32_16x16x32_bf16 v[132:135], v[188:191], v[8:11], v[132:135]\n\t" \
  "v_mfma_f32_16x16x32_bf16 v[140:143], v[188:191], v[12:15], v[140:143]\n\t" \
  "ds_read_b128 v[188:191], v209 offset:16640\n\t" \
  "global_load_lds_dwordx4 v218, s[92:93] sc1\n\t" \
  "ds_write_b128 v220, v[152:155] offset:0\n\t" \
  "ds_write_b128 v220, v[156:159] offset:1024\n\t" \
  "s_waitcnt lgkmcnt(8)\n\t" \
  "v_mfma_f32_16x16x32_bf16 v[128:131], v[192:195], v[16:19], v[128:131]\n\t" \
  "v_mfma_f32_16x16x32_bf16 v[136:139], v[192:195], v[20:23], v[136:139]\n\t" \
  "ds_read_b128 v[192:195], v210 offset:256\n\t" \
  "v_mfma_f32_16x16x32_bf16 v[132:135], v[196:199], v[16:19], v[132:135]\n\t" \
  "v_mfma_f32_16x16x32_bf16 v[140:143], v[196:199], v[20:23], v[140:143]\n\t" \
  "ds_read_b128 v[196:199], v210 offset:16640\n\t" \
  "s_lshr_b32 s90, s87, 7\n\t" \
  "s_xor_b32 s90, s90, %[s0]\n\t" \
  "s_and_b32 s90, s90, 1\n\t" \
  "s_lshl1_add_u32 s90, s87, s90\n\t" \
  "s_lshl_b32 s90, s90, 13\n\t" \
  "s_cmp_lt_i32 s86, 6\n\t" \
  "s_cselect_b32 s90, 0x70000000, s90\n\t" \
  "s_waitcnt lgkmcnt(8)\n\t" \
  "v_mfma_f32_16x16x32_bf16 v[128:131], v[200:203], v[24:27], v[128:131]\n\t" \
  "v_mfma_f32_16x16x32_bf16 v[136:139], v[200:203], v[28:31], v[136:139]\n\t" \
  "ds_read_b128 v[200:203], v211 offset:256\n\t" \
  "s_add_u32 m0, %[lw], 68608\n\t" \
  "v_mfma_f32_16x16x32_bf16 v[132:135], v[204:207], v[24:27], v[132:135]\n\t" \
  "v_mfma_f32_16x16x32_bf16 v[140:143], v[204:207], v[28:31], v[140:143]\n\t" \
  "ds_read_b128 v[204:207], v211 offset:16640\n\t" \
  "global_load_lds_dwordx4 v219, s[92:93] sc1\n\t" \
  "s_waitcnt lgkmcnt(4)\n\t" \
  "s_cmp_lt_u32 s86, 6\n\t" \
  "s_cbranch_scc1 .Lw01%=\n\t" \
  "s_waitcnt vmcnt(4)\n\t" \
  "s_barrier\n\t" \
  ".Lg01%=:\n\t" \
  "s_add_u32 s92, s92, 512\n\t" \
  "s_addc_u32 s93, s93, 0\n\t" \
  "v_mfma_f32_16x16x32_bf16 v[128:131], v[176:179], v[32:35], v[128:131]\n\t" \
  "v_mfma_f32_16x16x32_bf16 v[136:139], v[176:179], v[36:39], v[136:139]\n\t" \
  "ds_read_b128 v[176:179], v208 offset:32768\n\t" \
  "v_mfma_f32_16x16x32_bf16 v[132:135], v[180:183], v[32:35], v[132:135]\n\t" \
  "v_mfma_f32_16x16x32_bf16 v[140:143], v[180:183], v[36:39], v[140:143]\n\t" \
  "ds_read_b128 v[180:183], v208 offset:49152\n\t" \
  "ds_read_b128 v[160:163], v221 offset:0\n\t" \
  "ds_read_b128 v[164:167], v221 offset:1024\n\t" \
  "s_sub_i32 s91, s86, 2\n\t" \
  "s_max_i32 s88, s88, s91\n\t" \
  "v_mov_b32 v173, s88\n\t" \
  "buffer_store_dword v173, v174, %[rs], 0 offen sc1\n\t" \
  "v_mfma_f32_16x16x32_bf16 v[128:131], v[184:187], v[40:43], v[128:131]\n\t" \
  "v_mfma_f32_16x16x32_bf16 v[136:139], v[184:187], v[44:47], v[136:139]\n\t" \
  "ds_read_b128 v[184:187], v209 offset:32768\n\t" \
  "s_add_u32 m0, %[lw], 98304\n\t" \
  "v_mfma_f32_16x16x32_bf16 v[132:135], v[188:191], v[40:43], v[132:135]\n\t" \
  "v_mfma_f32_16x16x32_bf16 v[140:143], v[188:191], v[44:47], v[140:143]\n\t" \
  "ds_read_b128 v[188:191], v209 offset:49152\n\t" \
  "global_load_lds_dwordx4 v216, s[92:93] sc1\n\t" \
  "s_waitcnt lgkmcnt(8)\n\t" \
  "v_mfma_f32_16x16x32_bf16 v[128:131], v[192:195], v[48:51], v[128:131]\n\t" \
  "v_mfma_f32_16x16x32_bf16 v[136:139], v[192:195], v[52:55], v[136:139]\n\t" \
  "ds_read_b128 v[192:195], v210 offset:32768\n\t" \
  "v_mfma_f32_16x16x32_bf16 v[132:135], v[196:199], v[48:51], v[132:135]\n\t" \
  "v_mfma_f32_16x16x32_bf16 v[140:143], v[196:199], v[52:55], v[140:143]\n\t" \
  "ds_read_b128 v[196:199], v210 offset:49152\n\t" \
  "s_waitcnt lgkmcnt(4)\n\t" \
  "v_add_f32 v160, v160, v144\n\t" \
  "v_add_f32 v161, v161, v145\n\t" \
  "v_add_f32 v162, v162, v146\n\t" \
  "v_add_f32 v163, v163, v147\n\t" \
  "v_add_f32 v164, v164, v148\n\t" \
  "v_add_f32 v165, v165, v149\n\t" \
  "v_add_f32 v166, v166, v150\n\t" \
  "v_add_f32 v167, v167, v151\n\t" \
  "v_mfma_f32_16x16x32_bf16 v[128:131], v[200:203], v[56:59], v[128:131]\n\t" \
  "v_mfma_f32_16x16x32_bf16 v[136:139], v[200:203], v[60:63], v[136:139]\n\t" \
  "ds_read_b128 v[200:203], v211 offset:32768\n\t" \
  "s_add_u32 m0, %[lw], 99328\n\t" \
  "v_mfma_f32_16x16x32_bf16 v[132:135], v[204:207], v[56:59], v[132:135]\n\t" \
  "v_mfma_f32_16x16x32_bf16 v[140:143], v[204:207], v[60:63], v[140:143]\n\t" \
  "ds_read_b128 v[204:207], v211 offset:49152\n\t" \
  "global_load_lds_dwordx4 v217, s[92:93] sc1\n\t" \
  "v_cvt_pk_bf16_f32 v168, v160, v161\n\t" \
  "v_cvt_pk_bf16_f32 v169, v162, v163\n\t" \
  "v_cvt_pk_bf16_f32 v170, v164, v165\n\t" \
  "v_cvt_pk_bf16_f32 v171, v166, v167\n\t" \
  "buffer_store_dwordx4 v[168:171], v172, %[rt], s90 offen sc1\n\t" \
  "v_mfma_f32_16x16x32_bf16 v[128:131], v[176:179], v[64:67], v[128:131]\n\t" \
  "v_mfma_f32_16x16x32_bf16 v[136:139], v[176:179], v[68:71], v[136:139]\n\t" \
  "ds_read_b128 v[176:179], v208 offset:33024\n\t" \
  "v_mfma_f32_16x16x32_bf16 v[132:135], v[180:183], v[64:67], v[132:135]\n\t" \
  "v_mfma_f32_16x16x32_bf16 v[140:143], v[180:183], v[68:71], v[140:143]\n\t" \
  "ds_read_b128 v[180:183], v208 offset:49408\n\t" \
  "s_waitcnt lgkmcnt(6)\n\t" \
  "v_mfma_f32_16x16x32_bf16 v[128:131], v[184:187], v[72:75], v[128:131]\n\t" \
  "v_mfma_f32_16x16x32_bf16 v[136:139], v[184:187], v[76:79], v[136:139]\n\t" \
  "ds_read_b128 v[184:187], v209 offset:33024\n\t" \
  "s_add_u32 m0, %[lw], 100352\n\t" \
  "v_mfma_f32_16x16x32_bf16 v[132:135], v[188:191], v[72:75], v[132:135]\n\t" \
  "v_mfma_f32_16x16x32_bf16 v[140:143], v[188:191], v[76:79], v[140:143]\n\t" \
  "ds_read_b128 v[188:191], v209 offset:49408\n\t" \
  "global_load_lds_dwordx4 v218, s[92:93] sc1\n\t" \
  "s_waitcnt lgkmcnt(6)\n\t" \
  "v_mfma_f32_16x16x32_bf16 v[128:131], v[192:195], v[80:83], v[128:131]\n\t" \
  "v_mfma_f32_16x16x32_bf16 v[136:139], v[192:195], v[84:87], v[136:139]\n\t" \
  "ds_read_b128 v[192:195], v210 offset:33024\n\t" \
  "v_mfma_f32_16x16x32_bf16 v[132:135], v[196:199], v[80:83], v[132:135]\n\t" \
  "v_mfma_f32_16x16x32_bf16 v[140:143], v[196:199], v[84:87], v[140:143]\n\t" \
  "ds_read_b128 v[196:199], v210 offset:49408\n\t" \
  "s_waitcnt lgkmcnt(6)\n\t" \
  "v_mfma_f32_16x16x32_bf16 v[128:131], v[200:203], v[88:91], v[128:131]\n\t" \
  "v_mfma_f32_16x16x32_bf16 v[136:139], v[200:203], v[92:95], v[136:139]\n\t" \
  "ds_read_b128 v[200:203], v211 offset:33024\n\t" \
  "s_add_u32 m0, %[lw], 101376\n\t" \
  "v_mfma_f32_16x16x32_bf16 v[132:135], v[204:207], v[88:91], v[132:135]\n\t" \
  "v_mfma_f32_16x16x32_bf16 v[140:143], v[204:207], v[92:95], v[140:143]\n\t" \
  "ds_read_b128 v[204:207], v211 offset:49408\n\t" \
  "global_load_lds_dwordx4 v219, s[92:93] sc1\n\t" \
  "s_cmp_lt_u32 s86, 8\n\t" \
  "s_cbranch_scc1 .Lw11%=\n\t" \
  "s_waitcnt vmcnt(6)\n\t" \
  "s_barrier\n\t" \
  ".Lg11%=:\n\t" \
  "s_add_u32 s92, s92, 65024\n\t" \
  "s_addc_u32 s93, s93, 0\n\t" \
  "s_waitcnt lgkmcnt(6)\n\t" \
  "v_mfma_f32_16x16x32_bf16 v[128:131], v[176:179], v[96:99], v[128:131]\n\t" \
  "v_mfma_f32_16x16x32_bf16 v[136:139], v[176:179], v[100:103], v[136:139]\n\t" \
  "ds_read_b128 v[176:179], v212 offset:0\n\t" \
  "v_mfma_f32_16x16x32_bf16 v[132:135], v[180:183], v[96:99], v[132:135]\n\t" \
  "v_mfma_f32_16x16x32_bf16 v[140:143], v[180:183], v[100:103], v[140:143]\n\t" \
  "ds_read_b128 v[180:183], v212 offset:16384\n\t" \
  "s_waitcnt lgkmcnt(6)\n\t" \
  "v_mfma_f32_16x16x32_bf16 v[128:131], v[184:187], v[104:107], v[128:131]\n\t" \
  "v_mfma_f32_16x16x32_bf16 v[136:139], v[184:187], v[108:111], v[136:139]\n\t" \
  "ds_read_b128 v[184:187], v213 offset:0\n\t" \
  "s_add_u32 m0, %[lw], 0\n\t" \
  "v_mfma_f32_16x16x32_bf16 v[132:135], v[188:191], v[104:107], v[132:135]\n\t" \
  "v_mfma_f32_16x16x32_bf16 v[140:143], v[188:191], v[108:111], v[140:143]\n\t" \
  "ds_read_b128 v[188:191], v213 offset:16384\n\t" \
  "global_load_lds_dwordx4 v216, s[92:93] sc1\n\t" \
  "s_waitcnt lgkmcnt(6)\n\t" \
  "v_mfma_f32_16x16x32_bf16 v[128:131], v[192:195], v[112:115], v[128:131]\n\t" \
  "v_mfma_f32_16x16x32_bf16 v[136:139], v[192:195], v[116:119], v[136:139]\n\t" \
  "ds_read_b128 v[192:195], v214 offset:0\n\t" \
  "v_mfma_f32_16x16x32_bf16 v[132:135], v[196:199], v[112:115], v[132:135]\n\t" \
  "v_mfma_f32_16x16x32_bf16 v[140:143], v[196:199], v[116:119], v[140:143]\n\t" \
  "ds_read_b128 v[196:199], v214 offset:16384\n\t" \
  "s_waitcnt lgkmcnt(6)\n\t" \
  "v_mfma_f32_16x16x32_bf16 v[128:131], v[200:203], v[120:123], v[128:131]\n\t" \
  "v_mfma_f32_16x16x32_bf16 v[136:139], v[200:203], v[124:127], v[136:139]\n\t" \
  "ds_read_b128 v[200:203], v215 offset:0\n\t" \
  "s_add_u32 m0, %[lw], 1024\n\t" \
  "v_mfma_f32_16x16x32_bf16 v[132:135], v[204:207], v[120:123], v[132:135]\n\t" \
  "v_mfma_f32_16x16x32_bf16 v[140:143], v[204:207], v[124:127], v[140:143]\n\t" \
  "ds_read_b128 v[204:207], v215 offset:16384\n\t" \
  "global_load_lds_dwordx4 v217, s[92:93] sc1\n\t" \
  "s_add_u32 s86, s86, 1\n\t" \
  "s_add_u32 s87, s87, 1\n\t" \
  "s_cmp_le_u32 s86, 5\n\t" \
  "s_cbranch_scc1 .Lf11%=\n\t" \
  ".Lfb11%=:\n\t" \
  "s_waitcnt lgkmcnt(6)\n\t" \
  "v_mfma_f32_16x16x32_bf16 v[144:147], v[176:179], v[0:3], 0\n\t" \
  "v_mfma_f32_16x16x32_bf16 v[152:155], v[176:179], v[4:7], 0\n\t" \
  "ds_read_b128 v[176:179], v212 offset:256\n\t" \
  "v_mfma_f32_16x16x32_bf16 v[148:151], v[180:183], v[0:3], 0\n\t" \
  "v_mfma_f32_16x16x32_bf16 v[156:159], v[180:183], v[4:7], 0\n\t" \
  "ds_read_b128 v[180:183], v212 offset:16640\n\t" \
  "s_waitcnt lgkmcnt(6)\n\t" \
  "v_mfma_f32_16x16x32_bf16 v[144:147], v[184:187], v[8:11], v[144:147]\n\t" \
  "v_mfma_f32_16x16x32_bf16 v[152:155], v[184:187], v[12:15], v[152:155]\n\t" \
  "ds_read_b128 v[184:187], v213 offset:256\n\t" \
  "s_add_u32 m0, %[lw], 2048\n\t" \
  "v_mfma_f32_16x16x32_bf16 v[148:151], v[188:191], v[8:11], v[148:151]\n\t" \
  "v_mfma_f32_16x16x32_bf16 v[156:159], v[188:191], v[12:15], v[156:159]\n\t" \
  "ds_read_b128 v[188:191], v213 offset:16640\n\t" \
  "global_load_lds_dwordx4 v218, s[92:93] sc1\n\t" \
  "ds_write_b128 v220, v[136:139] offset:0\n\t" \
  "ds_write_b128 v220, v[140:143] offset:1024\n\t" \
  "s_waitcnt lgkmcnt(8)\n\t" \
  "v_mfma_f32_16x16x32_bf16 v[144:147], v[192:195], v[16:19], v[144:147]\n\t" \
  "v_mfma_f32_16x16x32_bf16 v[152:155], v[192:195], v[20:23], v[152:155]\n\t" \
  "ds_read_b128 v[192:195], v214 offset:256\n\t" \
  "v_mfma_f32_16x16x32_bf16 v[148:151], v[196:199], v[16:19], v[148:151]\n\t" \
  "v_mfma_f32_16x16x32_bf16 v[156:159], v[196:199], v[20:23], v[156:159]\n\t" \
  "ds_read_b128 v[196:199], v214 offset:16640\n\t" \
  "s_lshr_b32 s90, s87, 7\n\t" \
  "s_xor_b32 s90, s90, %[s0]\n\t" \
  "s_and_b32 s90, s90, 1\n\t" \
  "s_lshl1_add_u32 s90, s87, s90\n\t" \
  "s_lshl_b32 s90, s90, 13\n\t" \
  "s_cmp_lt_i32 s86, 6\n\t" \
  "s_cselect_b32 s90, 0x70000000, s90\n\t" \
  "s_waitcnt lgkmcnt(8)\n\t" \
  "v_mfma_f32_16x16x32_bf16 v[144:147], v[200:203], v[24:27], v[144:147]\n\t" \
  "v_mfma_f32_16x16x32_bf16 v[152:155], v[200:203], v[28:31], v[152:155]\n\t" \
  "ds_read_b128 v[200:203], v215 offset:256\n\t" \
  "s_add_u32 m0, %[lw], 3072\n\t" \
  "v_mfma_f32_16x16x32_bf16 v[148:151], v[204:207], v[24:27], v[148:151]\n\t" \
  "v_mfma_f32_16x16x32_bf16 v[156:159], v[204:207], v[28:31], v[156:159]\n\t" \
  "ds_read_b128 v[204:207], v215 offset:16640\n\t" \
  "global_load_lds_dwordx4 v219, s[92:93] sc1\n\t" \
  "s_waitcnt lgkmcnt(4)\n\t" \
  "s_cmp_lt_u32 s86, 6\n\t" \
  "s_cbranch_scc1 .Lw21%=\n\t" \
  "s_waitcnt vmcnt(4)\n\t" \
  "s_barrier\n\t" \
  ".Lg21%=:\n\t" \
  "s_add_u32 s92, s92, 512\n\t" \
  "s_addc_u32 s93, s93, 0\n\t" \
  "v_mfma_f32_16x16x32_bf16 v[144:147], v[176:179], v[32:35], v[144:147]\n\t" \
  "v_mfma_f32_16x16x32_bf16 v[152:155], v[176:179], v[36:39], v[152:155]\n\t" \
  "ds_read_b128 v[176:179], v212 offset:32768\n\t" \
  "v_mfma_f32_16x16x32_bf16 v[148:151], v[180:183], v[32:35], v[148:151]\n\t" \
  "v_mfma_f32_16x16x32_bf16 v[156:159], v[180:183], v[36:39], v[156:159]\n\t" \
  "ds_read_b128 v[180:183], v212 offset:49152\n\t" \
  "ds_read_b128 v[160:163], v221 offset:0\n\t" \
  "ds_read_b128 v[164:167], v221 offset:1024\n\t" \
  "s_sub_i32 s91, s86, 2\n\t" \
  "s_max_i32 s88, s88, s91\n\t" \
  "v_mov_b32 v173, s88\n\t" \
  "buffer_store_dword v173, v174, %[rs], 0 offen sc1\n\t" \
  "v_mfma_f32_16x16x32_bf16 v[144:147], v[184:187], v[40:43], v[144:147]\n\t" \
  "v_mfma_f32_16x16x32_bf16 v[152:155], v[184:187], v[44:47], v[152:155]\n\t" \
  "ds_read_b128 v[184:187], v213 offset:32768\n\t" \
  "s_add_u32 m0, %[lw], 32768\n\t" \
  "v_mfma_f32_16x16x32_bf16 v[148:151], v[188:191], v[40:43], v[148:151]\n\t" \
  "v_mfma_f32_16x16x32_bf16 v[156:159], v[188:191], v[44:47], v[156:159]\n\t" \
  "ds_read_b128 v[188:191], v213 offset:49152\n\t" \
  "global_load_lds_dwordx4 v216, s[92:93] sc1\n\t" \
  "s_waitcnt lgkmcnt(8)\n\t" \
  "v_mfma_f32_16x16x32_bf16 v[144:147], v[192:195], v[48:51], v[144:147]\n\t" \
  "v_mfma_f32_16x16x32_bf16 v[152:155], v[192:195], v[52:55], v[152:155]\n\t" \
  "ds_read_b128 v[192:195], v214 offset:32768\n\t" \
  "v_mfma_f32_16x16x32_bf16 v[148:151], v[196:199], v[48:51], v[148:151]\n\t" \
  "v_mfma_f32_16x16x32_bf16 v[156:159], v[196:199], v[52:55], v[156:159]\n\t" \
  "ds_read_b128 v[196:199], v214 offset:49152\n\t" \
  "s_waitcnt lgkmcnt(4)\n\t" \
  "v_add_f32 v160, v160, v128\n\t" \
  "v_add_f32 v161, v161, v129\n\t" \
  "v_add_f32 v162, v162, v130\n\t" \
  "v_add_f32 v163, v163, v131\n\t" \
  "v_add_f32 v164, v164, v132\n\t" \
  "v_add_f32 v165, v165, v133\n\t" \
  "v_add_f32 v166, v166, v134\n\t" \
  "v_add_f32 v167, v167, v135\n\t" \
  "v_mfma_f32_16x16x32_bf16 v[144:147], v[200:203], v[56:59], v[144:147]\n\t" \
  "v_mfma_f32_16x16x32_bf16 v[152:155], v[200:203], v[60:63], v[152:155]\n\t" \
  "ds_read_b128 v[200:203], v215 offset:32768\n\t" \
  "s_add_u32 m0, %[lw], 33792\n\t" \
  "v_mfma_f32_16x16x32_bf16 v[148:151], v[204:207], v[56:59], v[148:151]\n\t" \
  "v_mfma_f32_16x16x32_bf16 v[156:159], v[204:207], v[60:63], v[156:159]\n\t" \
  "ds_read_b128 v[204:207], v215 offset:49152\n\t" \
  "global_load_lds_dwordx4 v217, s[92:93] sc1\n\t" \
  "v_cvt_pk_bf16_f32 v168, v160, v161\n\t" \
  "v_cvt_pk_bf16_f32 v169, v162, v163\n\t" \
  "v_cvt_pk_bf16_f32 v170, v164, v165\n\t" \
  "v_cvt_pk_bf16_f32 v171, v166, v167\n\t" \
  "buffer_store_dwordx4 v[168:171], v172, %[rt], s90 offen sc1\n\t" \
  "v_mfma_f32_16x16x32_bf16 v[144:147], v[176:179], v[64:67], v[144:147]\n\t" \
  "v_mfma_f32_16x16x32_bf16 v[152:155], v[176:179], v[68:71], v[152:155]\n\t" \
  "ds_read_b128 v[176:179], v212 offset:33024\n\t" \
  "v_mfma_f32_16x16x32_bf16 v[148:151], v[180:183], v[64:67], v[148:151]\n\t" \
  "v_mfma_f32_16x16x32_bf16 v[156:159], v[180:183], v[68:71], v[156:159]\n\t" \
  "ds_read_b128 v[180:183], v212 offset:49408\n\t" \
  "s_waitcnt lgkmcnt(6)\n\t" \
  "v_mfma_f32_16x16x32_bf16 v[144:147], v[184:187], v[72:75], v[144:147]\n\t" \
  "v_mfma_f32_16x16x32_bf16 v[152:155], v[184:187], v[76:79], v[152:155]\n\t" \
  "ds_read_b128 v[184:187], v213 offset:33024\n\t" \
  "s_add_u32 m0, %[lw], 34816\n\t" \
  "v_mfma_f32_16x16x32_bf16 v[148:151], v[188:191], v[72:75], v[148:151]\n\t" \
  "v_mfma_f32_16x16x32_bf16 v[156:159], v[188:191], v[76:79], v[156:159]\n\t" \
  "ds_read_b128 v[188:191], v213 offset:49408\n\t" \
  "global_load_lds_dwordx4 v218, s[92:93] sc1\n\t" \
  "s_waitcnt lgkmcnt(6)\n\t" \
  "v_mfma_f32_16x16x32_bf16 v[144:147], v[192:195], v[80:83], v[144:147]\n\t" \
  "v_mfma_f32_16x16x32_bf16 v[152:155], v[192:195], v[84:87], v[152:155]\n\t" \
  "ds_read_b128 v[192:195], v214 offset:33024\n\t" \
  "v_mfma_f32_16x16x32_bf16 v[148:151], v[196:199], v[80:83], v[148:151]\n\t" \
  "v_mfma_f32_16x16x32_bf16 v[156:159], v[196:199], v[84:87], v[156:159]\n\t" \
  "ds_read_b128 v[196:199], v214 offset:49408\n\t" \
  "s_waitcnt lgkmcnt(6)\n\t" \
  "v_mfma_f32_16x16x32_bf16 v[144:147], v[200:203], v[88:91], v[144:147]\n\t" \
  "v_mfma_f32_16x16x32_bf16 v[152:155], v[200:203], v[92:95], v[152:155]\n\t" \
  "ds_read_b128 v[200:203], v215 offset:33024\n\t" \
  "s_add_u32 m0, %[lw], 35840\n\t" \
  "v_mfma_f32_16x16x32_bf16 v[148:151], v[204:207], v[88:91], v[148:151]\n\t" \
  "v_mfma_f32_16x16x32_bf16 v[156:159], v[204:207], v[92:95], v[156:159]\n\t" \
  "ds_read_b128 v[204:207], v215 offset:49408\n\t" \
  "global_load_lds_dwordx4 v219, s[92:93] sc1\n\t" \
  "s_cmp_lt_u32 s86, 8\n\t" \
  "s_cbranch_scc1 .Lw31%=\n\t" \
  "s_waitcnt vmcnt(6)\n\t" \
  "s_barrier\n\t" \
  ".Lg31%=:\n\t" \
  "s_add_u32 s92, s92, 65024\n\t" \
  "s_addc_u32 s93, s93, 0\n\t" \
  "s_waitcnt lgkmcnt(6)\n\t" \
  "v_mfma_f32_16x16x32_bf16 v[144:147], v[176:179], v[96:99], v[144:147]\n\t" \
  "v_mfma_f32_16x16x32_bf16 v[152:155], v[176:179], v[100:103], v[152:155]\n\t" \
  "ds_read_b128 v[176:179], v208 offset:0\n\t" \
  "v_mfma_f32_16x16x32_bf16 v[148:151], v[180:183], v[96:99], v[148:151]\n\t" \
  "v_mfma_f32_16x16x32_bf16 v[156:159], v[180:183], v[100:103], v[156:159]\n\t" \
  "ds_read_b128 v[180:183], v208 offset:16384\n\t" \
  "s_waitcnt lgkmcnt(6)\n\t" \
  "v_mfma_f32_16x16x32_bf16 v[144:147], v[184:187], v[104:107], v[144:147]\n\t" \
  "v_mfma_f32_16x16x32_bf16 v[152:155], v[184:187], v[108:111], v[152:155]\n\t" \
  "ds_read_b128 v[184:187], v209 offset:0\n\t" \
  "s_add_u32 m0, %[lw], 65536\n\t" \
  "v_mfma_f32_16x16x32_bf16 v[148:151], v[188:191], v[104:107], v[148:151]\n\t" \
  "v_mfma_f32_16x16x32_bf16 v[156:159], v[188:191], v[108:111], v[156:159]\n\t" \
  "ds_read_b128 v[188:191], v209 offset:16384\n\t" \
  "global_load_lds_dwordx4 v216, s[92:93] sc1\n\t" \
  "s_waitcnt lgkmcnt(6)\n\t" \
  "v_mfma_f32_16x16x32_bf16 v[144:147], v[192:195], v[112:115], v[144:147]\n\t" \
  "v_mfma_f32_16x16x32_bf16 v[152:155], v[192:195], v[116:119], v[152:155]\n\t" \
  "ds_read_b128 v[192:195], v210 offset:0\n\t" \
  "v_mfma_f32_16x16x32_bf16 v[148:151], v[196:199], v[112:115], v[148:151]\n\t" \
  "v_mfma_f32_16x16x32_bf16 v[156:159], v[196:199], v[116:119], v[156:159]\n\t" \
  "ds_read_b128 v[196:199], v210 offset:16384\n\t" \
  "s_waitcnt lgkmcnt(6)\n\t" \
  "v_mfma_f32_16x16x32_bf16 v[144:147], v[200:203], v[120:123], v[144:147]\n\t" \
  "v_mfma_f32_16x16x32_bf16 v[152:155], v[200:203], v[124:127], v[152:155]\n\t" \
  "ds_read_b128 v[200:203], v211 offset:0\n\t" \
  "s_add_u32 m0, %[lw], 66560\n\t" \
  "v_mfma_f32_16x16x32_bf16 v[148:151], v[204:207], v[120:123], v[148:151]\n\t" \
  "v_mfma_f32_16x16x32_bf16 v[156:159], v[204:207], v[124:127], v[156:159]\n\t" \
  "ds_read_b128 v[204:207], v211 offset:16384\n\t" \
  "global_load_lds_dwordx4 v217, s[92:93] sc1\n\t" \
  "s_add_u32 s86, s86, 1\n\t" \
  "s_add_u32 s87, s87, 1\n\t" \
  "s_cmp_lt_u32 s86, %[end]\n\t" \
  "s_cbranch_scc1 .Lp1%=\n\t" \
  "s_branch .Lx1%=\n\t" \
  ".Lf01%=:\n\t" \
  "s_nop 7\n\t" \
  "ds_write_b128 v220, v[152:155] offset:0\n\t" \
  "ds_write_b128 v220, v[156:159] offset:1024\n\t" \
  "s_lshr_b32 s90, s87, 7\n\t" \
  "s_xor_b32 s90, s90, %[s0]\n\t" \
  "s_and_b32 s90, s90, 1\n\t" \
  "s_lshl1_add_u32 s90, s87, s90\n\t" \
  "s_lshl_b32 s90, s90, 13\n\t" \
  "s_cmp_lt_i32 s86, 1\n\t" \
  "s_cselect_b32 s90, 0x70000000, s90\n\t" \
  "s_waitcnt lgkmcnt(0)\n\t" \
  "s_barrier\n\t" \
  "ds_read_b128 v[160:163], v221 offset:0\n\t" \
  "ds_read_b128 v[164:167], v221 offset:1024\n\t" \
  "s_waitcnt lgkmcnt(0)\n\t" \
  "v_add_f32 v160, v160, v144\n\t" \
  "v_add_f32 v161, v161, v145\n\t" \
  "v_add_f32 v162, v162, v146\n\t" \
  "v_add_f32 v163, v163, v147\n\t" \
  "v_add_f32 v164, v164, v148\n\t" \
  "v_add_f32 v165, v165, v149\n\t" \
  "v_add_f32 v166, v166, v150\n\t" \
  "v_add_f32 v167, v167, v151\n\t" \
  "v_cvt_pk_bf16_f32 v168, v160, v161\n\t" \
  "v_cvt_pk_bf16_f32 v169, v162, v163\n\t" \
  "v_cvt_pk_bf16_f32 v170, v164, v165\n\t" \
  "v_cvt_pk_bf16_f32 v171, v166, v167\n\t" \
  "buffer_store_dwordx4 v[168:171], v172, %[rt], s90 offen sc1\n\t" \
  "s_branch .Lfb01%=\n\t" \
  ".Lf11%=:\n\t" \
  "s_nop 7\n\t" \
  "ds_write_b128 v220, v[136:139] offset:0\n\t" \
  "ds_write_b128 v220, v[140:143] offset:1024\n\t" \
  "s_lshr_b32 s90, s87, 7\n\t" \
  "s_xor_b32 s90, s90, %[s0]\n\t" \
  "s_and_b32 s90, s90, 1\n\t" \
  "s_lshl1_add_u32 s90, s87, s90\n\t" \
  "s_lshl_b32 s90, s90, 13\n\t" \
  "s_cmp_lt_i32 s86, 1\n\t" \
  "s_cselect_b32 s90, 0x70000000, s90\n\t" \
  "s_waitcnt lgkmcnt(0)\n\t" \
  "s_barrier\n\t" \
  "ds_read_b128 v[160:163], v221 offset:0\n\t" \
  "ds_read_b128 v[164:167], v221 offset:1024\n\t" \
  "s_waitcnt lgkmcnt(0)\n\t" \
  "v_add_f32 v160, v160, v128\n\t" \
  "v_add_f32 v161, v161, v129\n\t" \
  "v_add_f32 v162, v162, v130\n\t" \
  "v_add_f32 v163, v163, v131\n\t" \
  "v_add_f32 v164, v164, v132\n\t" \
  "v_add_f32 v165, v165, v133\n\t" \
  "v_add_f32 v166, v166, v134\n\t" \
  "v_add_f32 v167, v167, v135\n\t" \
  "v_cvt_pk_bf16_f32 v168, v160, v161\n\t" \
  "v_cvt_pk_bf16_f32 v169, v162, v163\n\t" \
  "v_cvt_pk_bf16_f32 v170, v164, v165\n\t" \
  "v_cvt_pk_bf16_f32 v171, v166, v167\n\t" \
  "buffer_store_dwordx4 v[168:171], v172, %[rt], s90 offen sc1\n\t" \
  "s_branch .Lfb11%=\n\t" \
  ".Lw01%=:\n\t" \
  "s_waitcnt vmcnt(2)\n\t" \
  "s_barrier\n\t" \
  "s_sub_i32 s91, s86, 1\n\t" \
  "s_max_i32 s88, s88, s91\n\t" \
  "v_mov_b32 v173, s88\n\t" \
  "buffer_store_dword v173, v174, %[rs], 0 offen sc1\n\t" \
  "s_branch .Lg01%=\n\t" \
  ".Lw11%=:\n\t" \
  "s_waitcnt vmcnt(2)\n\t" \
  "s_barrier\n\t" \
  "s_sub_i32 s91, s86, 1\n\t" \
  "s_max_i32 s88, s88, s91\n\t" \
  "v_mov_b32 v173, s88\n\t" \
  "buffer_store_dword v173, v174, %[rs], 0 offen sc1\n\t" \
  "s_branch .Lg11%=\n\t" \
  ".Lw21%=:\n\t" \
  "s_waitcnt vmcnt(2)\n\t" \
  "s_barrier\n\t" \
  "s_sub_i32 s91, s86, 1\n\t" \
  "s_max_i32 s88, s88, s91\n\t" \
  "v_mov_b32 v173, s88\n\t" \
  "buffer_store_dword v173, v174, %[rs], 0 offen sc1\n\t" \
  "s_branch .Lg21%=\n\t" \
  ".Lw31%=:\n\t" \
  "s_waitcnt vmcnt(2)\n\t" \
  "s_barrier\n\t" \
  "s_sub_i32 s91, s86, 1\n\t" \
  "s_max_i32 s88, s88, s91\n\t" \
  "v_mov_b32 v173, s88\n\t" \
  "buffer_store_dword v173, v174, %[rs], 0 offen sc1\n\t" \
  "s_branch .Lg31%=\n\t" \
  ".Lx1%=:\n\t" \
  "s_waitcnt lgkmcnt(0)\n\t" \
  "s_setprio 0\n\t" \
  "s_mov_b32 m0, s94\n\t" \
  ".Le%=:\n\t"
#define PP_ASM_0 \
  "s_cmp_lg_u32 %[kh], 0\n\t" \
  "s_cbranch_scc1 .Lk%=\n\t" \
  "s_mov_b32 s94, m0\n\t" \
  "s_mov_b64 s[92:93], %[x0]\n\t" \
  "s_mov_b32 s86, %[g0]\n\t" \
  "s_add_u32 s87, s86, %[t0]\n\t" \
  "s_add_u32 m0, %[lw], 67584\n\t" \
  "s_nop 0\n\t" \
  "global_load_lds_dwordx4 v250, s[92:93] sc1\n\t" \
  "s_add_u32 m0, %[lw], 68608\n\t" \
  "s_nop 0\n\t" \
  "global_load_lds_dwordx4 v251, s[92:93] sc1\n\t" \
  "s_barrier\n\t" \
  "s_branch .Ls0%=\n\t" \
  ".Lp0%=:\n\t" \
  "s_waitcnt vmcnt(9)\n\t" \
  "s_barrier\n\t" \
  ".Ls0%=:\n\t" \
  "s_add_u32 s92, s92, 512\n\t" \
  "s_addc_u32 s93, s93, 0\n\t" \
  "ds_read_b128 v[176:179], v240 offset:0\n\t" \
  "ds_read_b128 v[180:183], v240 offset:16384\n\t" \
  "ds_read_b128 v[184:187], v241 offset:0\n\t" \
  "ds_read_b128 v[188:191], v241 offset:16384\n\t" \
  "s_add_u32 m0, %[lw], 98304\n\t" \
  "s_nop 0\n\t" \
  "global_load_lds_dwordx4 v248, s[92:93] sc1\n\t" \
  "ds_read_b128 v[192:195], v242 offset:0\n\t" \
  "ds_read_b128 v[196:199], v242 offset:16384\n\t" \
  "ds_read_b128 v[200:203], v243 offset:0\n\t" \
  "ds_read_b128 v[204:207], v243 offset:16384\n\t" \
  "s_add_u32 m0, %[lw], 99328\n\t" \
  "s_nop 0\n\t" \
  "global_load_lds_dwordx4 v249, s[92:93] sc1\n\t" \
  "ds_read_b128 v[208:211], v240 offset:256\n\t" \
  "ds_read_b128 v[212:215], v240 offset:16640\n\t" \
  "ds_read_b128 v[216:219], v241 offset:256\n\t" \
  "ds_read_b128 v[220:223], v241 offset:16640\n\t" \
  "s_add_u32 m0, %[lw], 100352\n\t" \
  "s_nop 0\n\t" \
  "global_load_lds_dwordx4 v250, s[92:93] sc1\n\t" \
  "ds_read_b128 v[224:227], v242 offset:256\n\t" \
  "ds_read_b128 v[228:231], v242 offset:16640\n\t" \
  "ds_read_b128 v[232:235], v243 offset:256\n\t" \
  "ds_read_b128 v[236:239], v243 offset:16640\n\t" \
  "s_add_u32 m0, %[lw], 101376\n\t" \
  "s_nop 0\n\t" \
  "global_load_lds_dwordx4 v251, s[92:93] sc1\n\t" \
  "ds_write_b128 v252, v[152:155] offset:0\n\t" \
  "ds_write_b128 v252, v[156:159] offset:1024\n\t" \
  "s_waitcnt lgkmcnt(0)\n\t" \
  "s_setprio 1\n\t" \
  "v_mfma_f32_16x16x32_bf16 v[128:131], v[0:3], v[176:179], 0\n\t" \
  "v_mfma_f32_16x16x32_bf16 v[132:135], v[4:7], v[176:179], 0\n\t" \
  "v_mfma_f32_16x16x32_bf16 v[136:139], v[0:3], v[180:183], 0\n\t" \
  "v_mfma_f32_16x16x32_bf16 v[140:143], v[4:7], v[180:183], 0\n\t" \
  "v_mfma_f32_16x16x32_bf16 v[128:131], v[8:11], v[184:187], v[128:131]\n\t" \
  "v_mfma_f32_16x16x32_bf16 v[132:135], v[12:15], v[184:187], v[132:135]\n\t" \
  "v_mfma_f32_16x16x32_bf16 v[136:139], v[8:11], v[188:191], v[136:139]\n\t" \
  "v_mfma_f32_16x16x32_bf16 v[140:143], v[12:15], v[188:191], v[140:143]\n\t" \
  "v_mfma_f32_16x16x32_bf16 v[128:131], v[16:19], v[192:195], v[128:131]\n\t" \
  "v_mfma_f32_16x16x32_bf16 v[132:135], v[20:23], v[192:195], v[132:135]\n\t" \
  "v_mfma_f32_16x16x32_bf16 v[136:139], v[16:19], v[196:199], v[136:139]\n\t" \
  "v_mfma_f32_16x16x32_bf16 v[140:143], v[20:23], v[196:199], v[140:143]\n\t" \
  "v_mfma_f32_16x16x32_bf16 v[128:131], v[24:27], v[200:203], v[128:131]\n\t" \
  "v_mfma_f32_16x16x32_bf16 v[132:135], v[28:31], v[200:203], v[132:135]\n\t" \
  "v_mfma_f32_16x16x32_bf16 v[136:139], v[24:27], v[204:207], v[136:139]\n\t" \
  "v_mfma_f32_16x16x32_bf16 v[140:143], v[28:31], v[204:207], v[140:143]\n\t" \
  "v_mfma_f32_16x16x32_bf16 v[128:131], v[32:35], v[208:211], v[128:131]\n\t" \
  "v_mfma_f32_16x16x32_bf16 v[132:135], v[36:39], v[208:211], v[132:135]\n\t" \
  "v_mfma_f32_16x16x32_bf16 v[136:139], v[32:35], v[212:215], v[136:139]\n\t" \
  "v_mfma_f32_16x16x32_bf16 v[140:143], v[36:39], v[212:215], v[140:143]\n\t" \
  "v_mfma_f32_16x16x32_bf16 v[128:131], v[40:43], v[216:219], v[128:131]\n\t" \
  "v_mfma_f32_16x16x32_bf16 v[132:135], v[44:47], v[216:219], v[132:135]\n\t" \
  "v_mfma_f32_16x16x32_bf16 v[136:139], v[40:43], v[220:223], v[136:139]\n\t" \
  "v_mfma_f32_16x16x32_bf16 v[140:143], v[44:47], v[220:223], v[140:143]\n\t" \
  "v_mfma_f32_16x16x32_bf16 v[128:131], v[48:51], v[224:227], v[128:131]\n\t" \
  "v_mfma_f32_16x16x32_bf16 v[132:135], v[52:55], v[224:227], v[132:135]\n\t" \
  "v_mfma_f32_16x16x32_bf16 v[136:139], v[48:51], v[228:231], v[136:139]\n\t" \
  "v_mfma_f32_16x16x32_bf16 v[140:143], v[52:55], v[228:231], v[140:143]\n\t" \
  "v_mfma_f32_16x16x32_bf16 v[128:131], v[56:59], v[232:235], v[128:131]\n\t" \
  "v_mfma_f32_16x16x32_bf16 v[132:135], v[60:63], v[232:235], v[132:135]\n\t" \
  "v_mfma_f32_16x16x32_bf16 v[136:139], v[56:59], v[236:239], v[136:139]\n\t" \
  "v_mfma_f32_16x16x32_bf16 v[140:143], v[60:63], v[236:239], v[140:143]\n\t" \
  "s_setprio 0\n\t" \
  "s_waitcnt vmcnt(4)\n\t" \
  "s_barrier\n\t" \
  "s_add_u32 s92, s92, 65024\n\t" \
  "s_addc_u32 s93, s93, 0\n\t" \
  "ds_read_b128 v[160:163], v253 offset:0\n\t" \
  "ds_read_b128 v[164:167], v253 offset:1024\n\t" \
  "ds_read_b128 v[176:179], v240 offset:32768\n\t" \
  "ds_read_b128 v[180:183], v240 offset:49152\n\t" \
  "ds_read_b128 v[184:187], v241 offset:32768\n\t" \
  "ds_read_b128 v[188:191], v241 offset:49152\n\t" \
  "s_add_u32 m0, %[lw], 0\n\t" \
  "s_nop 0\n\t" \
  "global_load_lds_dwordx4 v248, s[92:93] sc1\n\t" \
  "ds_read_b128 v[192:195], v242 offset:32768\n\t" \
  "ds_read_b128 v[196:199], v242 offset:49152\n\t" \
  "ds_read_b128 v[200:203], v243 offset:32768\n\t" \
  "ds_read_b128 v[204:207], v243 offset:49152\n\t" \
  "s_add_u32 m0, %[lw], 1024\n\t" \
  "s_nop 0\n\t" \
  "global_load_lds_dwordx4 v249, s[92:93] sc1\n\t" \
  "ds_read_b128 v[208:211], v240 offset:33024\n\t" \
  "ds_read_b128 v[212:215], v240 offset:49408\n\t" \
  "ds_read_b128 v[216:219], v241 offset:33024\n\t" \
  "ds_read_b128 v[220:223], v241 offset:49408\n\t" \
  "s_add_u32 m0, %[lw], 2048\n\t" \
  "s_nop 0\n\t" \
  "global_load_lds_dwordx4 v250, s[92:93] sc1\n\t" \
  "ds_read_b128 v[224:227], v242 offset:33024\n\t" \
  "ds_read_b128 v[228:231], v242 offset:49408\n\t" \
  "ds_read_b128 v[232:235], v243 offset:33024\n\t" \
  "ds_read_b128 v[236:239], v243 offset:49408\n\t" \
  "s_add_u32 m0, %[lw], 3072\n\t" \
  "s_nop 0\n\t" \
  "global_load_lds_dwordx4 v251, s[92:93] sc1\n\t" \
  "s_waitcnt lgkmcnt(15)\n\t" \
  "s_waitcnt lgkmcnt(12)\n\t" \
  "s_lshr_b32 s90, s87, 7\n\t" \
  "s_xor_b32 s90, s90, %[s0]\n\t" \
  "s_and_b32 s90, s90, 1\n\t" \
  "s_lshl1_add_u32 s90, s87, s90\n\t" \
  "s_lshl_b32 s90, s90, 13\n\t" \
  "v_add_f32 v160, v160, v144\n\t" \
  "v_add_f32 v161, v161, v145\n\t" \
  "v_add_f32 v162, v162, v146\n\t" \
  "v_add_f32 v163, v163, v147\n\t" \
  "v_add_f32 v164, v164, v148\n\t" \
  "v_add_f32 v165, v165, v149\n\t" \
  "v_add_f32 v166, v166, v150\n\t" \
  "v_add_f32 v167, v167, v151\n\t" \
  "v_cvt_pk_bf16_f32 v168, v160, v161\n\t" \
  "v_cvt_pk_bf16_f32 v169, v162, v163\n\t" \
  "v_cvt_pk_bf16_f32 v170, v164, v165\n\t" \
  "v_cvt_pk_bf16_f32 v171, v166, v167\n\t" \
  "buffer_store_dwordx4 v[168:171], v172, %[rt], s90 offen sc1\n\t" \
  "s_add_u32 s86, s86, 1\n\t" \
  "s_add_u32 s87, s87, 1\n\t" \
  "s_waitcnt lgkmcnt(0)\n\t" \
  "s_setprio 1\n\t" \
  "v_mfma_f32_16x16x32_bf16 v[128:131], v[64:67], v[176:179], v[128:131]\n\t" \
  "v_mfma_f32_16x16x32_bf16 v[132:135], v[68:71], v[176:179], v[132:135]\n\t" \
  "v_mfma_f32_16x16x32_bf16 v[136:139], v[64:67], v[180:183], v[136:139]\n\t" \
  "v_mfma_f32_16x16x32_bf16 v[140:143], v[68:71], v[180:183], v[140:143]\n\t" \
  "v_mfma_f32_16x16x32_bf16 v[128:131], v[72:75], v[184:187], v[128:131]\n\t" \
  "v_mfma_f32_16x16x32_bf16 v[132:135], v[76:79], v[184:187], v[132:135]\n\t" \
  "v_mfma_f32_16x16x32_bf16 v[136:139], v[72:75], v[188:191], v[136:139]\n\t" \
  "v_mfma_f32_16x16x32_bf16 v[140:143], v[76:79], v[188:191], v[140:143]\n\t" \
  "v_mfma_f32_16x16x32_bf16 v[128:131], v[80:83], v[192:195], v[128:131]\n\t" \
  "v_mfma_f32_16x16x32_bf16 v[132:135], v[84:87], v[192:195], v[132:135]\n\t" \
  "v_mfma_f32_16x16x32_bf16 v[136:139], v[80:83], v[196:199], v[136:139]\n\t" \
  "v_mfma_f32_16x16x32_bf16 v[140:143], v[84:87], v[196:199], v[140:143]\n\t" \
  "v_mfma_f32_16x16x32_bf16 v[128:131], v[88:91], v[200:203], v[128:131]\n\t" \
  "v_mfma_f32_16x16x32_bf16 v[132:135], v[92:95], v[200:203], v[132:135]\n\t" \
  "v_mfma_f32_16x16x32_bf16 v[136:139], v[88:91], v[204:207], v[136:139]\n\t" \
  "v_mfma_f32_16x16x32_bf16 v[140:143], v[92:95], v[204:207], v[140:143]\n\t" \
  "v_mfma_f32_16x16x32_bf16 v[128:131], v[96:99], v[208:211], v[128:131]\n\t" \
  "v_mfma_f32_16x16x32_bf16 v[132:135], v[100:103], v[208:211], v[132:135]\n\t" \
  "v_mfma_f32_16x16x32_bf16 v[136:139], v[96:99], v[212:215], v[136:139]\n\t" \
  "v_mfma_f32_16x16x32_bf16 v[140:143], v[100:103], v[212:215], v[140:143]\n\t" \
  "v_mfma_f32_16x16x32_bf16 v[128:131], v[104:107], v[216:219], v[128:131]\n\t" \
  "v_mfma_f32_16x16x32_bf16 v[132:135], v[108:111], v[216:219], v[132:135]\n\t" \
  "v_mfma_f32_16x16x32_bf16 v[136:139], v[104:107], v[220:223], v[136:139]\n\t" \
  "v_mfma_f32_16x16x32_bf16 v[140:143], v[108:111], v[220:223], v[140:143]\n\t" \
  "v_mfma_f32_16x16x32_bf16 v[128:131], v[112:115], v[224:227], v[128:131]\n\t" \
  "v_mfma_f32_16x16x32_bf16 v[132:135], v[116:119], v[224:227], v[132:135]\n\t" \
  "v_mfma_f32_16x16x32_bf16 v[136:139], v[112:115], v[228:231], v[136:139]\n\t" \
  "v_mfma_f32_16x16x32_bf16 v[140:143], v[116:119], v[228:231], v[140:143]\n\t" \
  "v_mfma_f32_16x16x32_bf16 v[128:131], v[120:123], v[232:235], v[128:131]\n\t" \
  "v_mfma_f32_16x16x32_bf16 v[132:135], v[124:127], v[232:235], v[132:135]\n\t" \
  "v_mfma_f32_16x16x32_bf16 v[136:139], v[120:123], v[236:239], v[136:139]\n\t" \
  "v_mfma_f32_16x16x32_bf16 v[140:143], v[124:127], v[236:239], v[140:143]\n\t" \
  "s_setprio 0\n\t" \
  "s_waitcnt vmcnt(9)\n\t" \
  "s_barrier\n\t" \
  "s_add_u32 s92, s92, 512\n\t" \
  "s_addc_u32 s93, s93, 0\n\t" \
  "ds_read_b128 v[176:179], v244 offset:0\n\t" \
  "ds_read_b128 v[180:183], v244 offset:16384\n\t" \
  "ds_read_b128 v[184:187], v245 offset:0\n\t" \
  "ds_read_b128 v[188:191], v245 offset:16384\n\t" \
  "s_add_u32 m0, %[lw], 32768\n\t" \
  "s_nop 0\n\t" \
  "global_load_lds_dwordx4 v248, s[92:93] sc1\n\t" \
  "ds_read_b128 v[192:195], v246 offset:0\n\t" \
  "ds_read_b128 v[196:199], v246 offset:16384\n\t" \
  "ds_read_b128 v[200:203], v247 offset:0\n\t" \
  "ds_read_b128 v[204:207], v247 offset:16384\n\t" \
  "s_add_u32 m0, %[lw], 33792\n\t" \
  "s_nop 0\n\t" \
  "global_load_lds_dwordx4 v249, s[92:93] sc1\n\t" \
  "ds_read_b128 v[208:211], v244 offset:256\n\t" \
  "ds_read_b128 v[212:215], v244 offset:16640\n\t" \
  "ds_read_b128 v[216:219], v245 offset:256\n\t" \
  "ds_read_b128 v[220:223], v245 offset:16640\n\t" \
  "s_add_u32 m0, %[lw], 34816\n\t" \
  "s_nop 0\n\t" \
  "global_load_lds_dwordx4 v250, s[92:93] sc1\n\t" \
  "ds_read_b128 v[224:227], v246 offset:256\n\t" \
  "ds_read_b128 v[228:231], v246 offset:16640\n\t" \
  "ds_read_b128 v[232:235], v247 offset:256\n\t" \
  "ds_read_b128 v[236:239], v247 offset:16640\n\t" \
  "s_add_u32 m0, %[lw], 35840\n\t" \
  "s_nop 0\n\t" \
  "global_load_lds_dwordx4 v251, s[92:93] sc1\n\t" \
  "ds_write_b128 v252, v[136:139] offset:0\n\t" \
  "ds_write_b128 v252, v[140:143] offset:1024\n\t" \
  "s_waitcnt lgkmcnt(0)\n\t" \
  "s_setprio 1\n\t" \
  "v_mfma_f32_16x16x32_bf16 v[144:147], v[0:3], v[176:179], 0\n\t" \
  "v_mfma_f32_16x16x32_bf16 v[148:151], v[4:7], v[176:179], 0\n\t" \
  "v_mfma_f32_16x16x32_bf16 v[152:155], v[0:3], v[180:183], 0\n\t" \
  "v_mfma_f32_16x16x32_bf16 v[156:159], v[4:7], v[180:183], 0\n\t" \
  "v_mfma_f32_16x16x32_bf16 v[144:147], v[8:11], v[184:187], v[144:147]\n\t" \
  "v_mfma_f32_16x16x32_bf16 v[148:151], v[12:15], v[184:187], v[148:151]\n\t" \
  "v_mfma_f32_16x16x32_bf16 v[152:155], v[8:11], v[188:191], v[152:155]\n\t" \
  "v_mfma_f32_16x16x32_bf16 v[156:159], v[12:15], v[188:191], v[156:159]\n\t" \
  "v_mfma_f32_16x16x32_bf16 v[144:147], v[16:19], v[192:195], v[144:147]\n\t" \
  "v_mfma_f32_16x16x32_bf16 v[148:151], v[20:23], v[192:195], v[148:151]\n\t" \
  "v_mfma_f32_16x16x32_bf16 v[152:155], v[16:19], v[196:199], v[152:155]\n\t" \
  "v_mfma_f32_16x16x32_bf16 v[156:159], v[20:23], v[196:199], v[156:159]\n\t" \
  "v_mfma_f32_16x16x32_bf16 v[144:147], v[24:27], v[200:203], v[144:147]\n\t" \
  "v_mfma_f32_16x16x32_bf16 v[148:151], v[28:31], v[200:203], v[148:151]\n\t" \
  "v_mfma_f32_16x16x32_bf16 v[152:155], v[24:27], v[204:207], v[152:155]\n\t" \
  "v_mfma_f32_16x16x32_bf16 v[156:159], v[28:31], v[204:207], v[156:159]\n\t" \
  "v_mfma_f32_16x16x32_bf16 v[144:147], v[32:35], v[208:211], v[144:147]\n\t" \
  "v_mfma_f32_16x16x32_bf16 v[148:151], v[36:39], v[208:211], v[148:151]\n\t" \
  "v_mfma_f32_16x16x32_bf16 v[152:155], v[32:35], v[212:215], v[152:155]\n\t" \
  "v_mfma_f32_16x16x32_bf16 v[156:159], v[36:39], v[212:215], v[156:159]\n\t" \
  "v_mfma_f32_16x16x32_bf16 v[144:147], v[40:43], v[216:219], v[144:147]\n\t" \
  "v_mfma_f32_16x16x32_bf16 v[148:151], v[44:47], v[216:219], v[148:151]\n\t" \
  "v_mfma_f32_16x16x32_bf16 v[152:155], v[40:43], v[220:223], v[152:155]\n\t" \
  "v_mfma_f32_16x16x32_bf16 v[156:159], v[44:47], v[220:223], v[156:159]\n\t" \
  "v_mfma_f32_16x16x32_bf16 v[144:147], v[48:51], v[224:227], v[144:147]\n\t" \
  "v_mfma_f32_16x16x32_bf16 v[148:151], v[52:55], v[224:227], v[148:151]\n\t" \
  "v_mfma_f32_16x16x32_bf16 v[152:155], v[48:51], v[228:231], v[152:155]\n\t" \
  "v_mfma_f32_16x16x32_bf16 v[156:159], v[52:55], v[228:231], v[156:159]\n\t" \
  "v_mfma_f32_16x16x32_bf16 v[144:147], v[56:59], v[232:235], v[144:147]\n\t" \
  "v_mfma_f32_16x16x32_bf16 v[148:151], v[60:63], v[232:235], v[148:151]\n\t" \
  "v_mfma_f32_16x16x32_bf16 v[152:155], v[56:59], v[236:239], v[152:155]\n\t" \
  "v_mfma_f32_16x16x32_bf16 v[156:159], v[60:63], v[236:239], v[156:159]\n\t" \
  "s_setprio 0\n\t" \
  "s_waitcnt vmcnt(4)\n\t" \
  "s_barrier\n\t" \
  "s_add_u32 s92, s92, 65024\n\t" \
  "s_addc_u32 s93, s93, 0\n\t" \
  "ds_read_b128 v[160:163], v253 offset:0\n\t" \
  "ds_read_b128 v[164:167], v253 offset:1024\n\t" \
  "ds_read_b128 v[176:179], v244 offset:32768\n\t" \
  "ds_read_b128 v[180:183], v244 offset:49152\n\t" \
  "ds_read_b128 v[184:187], v245 offset:32768\n\t" \
  "ds_read_b128 v[188:191], v245 offset:49152\n\t" \
  "s_add_u32 m0, %[lw], 65536\n\t" \
  "s_nop 0\n\t" \
  "global_load_lds_dwordx4 v248, s[92:93] sc1\n\t" \
  "ds_read_b128 v[192:195], v246 offset:32768\n\t" \
  "ds_read_b128 v[196:199], v246 offset:49152\n\t" \
  "ds_read_b128 v[200:203], v247 offset:32768\n\t" \
  "ds_read_b128 v[204:207], v247 offset:49152\n\t" \
  "s_add_u32 m0, %[lw], 66560\n\t" \
  "s_nop 0\n\t" \
  "global_load_lds_dwordx4 v249, s[92:93] sc1\n\t" \
  "ds_read_b128 v[208:211], v244 offset:33024\n\t" \
  "ds_read_b128 v[212:215], v244 offset:49408\n\t" \
  "ds_read_b128 v[216:219], v245 offset:33024\n\t" \
  "ds_read_b128 v[220:223], v245 offset:49408\n\t" \
  "s_add_u32 m0, %[lw], 67584\n\t" \
  "s_nop 0\n\t" \
  "global_load_lds_dwordx4 v250, s[92:93] sc1\n\t" \
  "ds_read_b128 v[224:227], v246 offset:33024\n\t" \
  "ds_read_b128 v[228:231], v246 offset:49408\n\t" \
  "ds_read_b128 v[232:235], v247 offset:33024\n\t" \
  "ds_read_b128 v[236:239], v247 offset:49408\n\t" \
  "s_add_u32 m0, %[lw], 68608\n\t" \
  "s_nop 0\n\t" \
  "global_load_lds_dwordx4 v251, s[92:93] sc1\n\t" \
  "s_waitcnt lgkmcnt(15)\n\t" \
  "s_waitcnt lgkmcnt(12)\n\t" \
  "s_lshr_b32 s90, s87, 7\n\t" \
  "s_xor_b32 s90, s90, %[s0]\n\t" \
  "s_and_b32 s90, s90, 1\n\t" \
  "s_lshl1_add_u32 s90, s87, s90\n\t" \
  "s_lshl_b32 s90, s90, 13\n\t" \
  "v_add_f32 v160, v160, v128\n\t" \
  "v_add_f32 v161, v161, v129\n\t" \
  "v_add_f32 v162, v162, v130\n\t" \
  "v_add_f32 v163, v163, v131\n\t" \
  "v_add_f32 v164, v164, v132\n\t" \
  "v_add_f32 v165, v165, v133\n\t" \
  "v_add_f32 v166, v166, v134\n\t" \
  "v_add_f32 v167, v167, v135\n\t" \
  "v_cvt_pk_bf16_f32 v168, v160, v161\n\t" \
  "v_cvt_pk_bf16_f32 v169, v162, v163\n\t" \
  "v_cvt_pk_bf16_f32 v170, v164, v165\n\t" \
  "v_cvt_pk_bf16_f32 v171, v166, v167\n\t" \
  "buffer_store_dwordx4 v[168:171], v172, %[rt], s90 offen sc1\n\t" \
  "s_add_u32 s86, s86, 1\n\t" \
  "s_add_u32 s87, s87, 1\n\t" \
  "s_waitcnt lgkmcnt(0)\n\t" \
  "s_setprio 1\n\t" \
  "v_mfma_f32_16x16x32_bf16 v[144:147], v[64:67], v[176:179], v[144:147]\n\t" \
  "v_mfma_f32_16x16x32_bf16 v[148:151], v[68:71], v[176:179], v[148:151]\n\t" \
  "v_mfma_f32_16x16x32_bf16 v[152:155], v[64:67], v[180:183], v[152:155]\n\t" \
  "v_mfma_f32_16x16x32_bf16 v[156:159], v[68:71], v[180:183], v[156:159]\n\t" \
  "v_mfma_f32_16x16x32_bf16 v[144:147], v[72:75], v[184:187], v[144:147]\n\t" \
  "v_mfma_f32_16x16x32_bf16 v[148:151], v[76:79], v[184:187], v[148:151]\n\t" \
  "v_mfma_f32_16x16x32_bf16 v[152:155], v[72:75], v[188:191], v[152:155]\n\t" \
  "v_mfma_f32_16x16x32_bf16 v[156:159], v[76:79], v[188:191], v[156:159]\n\t" \
  "v_mfma_f32_16x16x32_bf16 v[144:147], v[80:83], v[192:195], v[144:147]\n\t" \
  "v_mfma_f32_16x16x32_bf16 v[148:151], v[84:87], v[192:195], v[148:151]\n\t" \
  "v_mfma_f32_16x16x32_bf16 v[152:155], v[80:83], v[196:199], v[152:155]\n\t" \
  "v_mfma_f32_16x16x32_bf16 v[156:159], v[84:87], v[196:199], v[156:159]\n\t" \
  "v_mfma_f32_16x16x32_bf16 v[144:147], v[88:91], v[200:203], v[144:147]\n\t" \
  "v_mfma_f32_16x16x32_bf16 v[148:151], v[92:95], v[200:203], v[148:151]\n\t" \
  "v_mfma_f32_16x16x32_bf16 v[152:155], v[88:91], v[204:207], v[152:155]\n\t" \
  "v_mfma_f32_16x16x32_bf16 v[156:159], v[92:95], v[204:207], v[156:159]\n\t" \
  "v_mfma_f32_16x16x32_bf16 v[144:147], v[96:99], v[208:211], v[144:147]\n\t" \
  "v_mfma_f32_16x16x32_bf16 v[148:151], v[100:103], v[208:211], v[148:151]\n\t" \
  "v_mfma_f32_16x16x32_bf16 v[152:155], v[96:99], v[212:215], v[152:155]\n\t" \
  "v_mfma_f32_16x16x32_bf16 v[156:159], v[100:103], v[212:215], v[156:159]\n\t" \
  "v_mfma_f32_16x16x32_bf16 v[144:147], v[104:107], v[216:219], v[144:147]\n\t" \
  "v_mfma_f32_16x16x32_bf16 v[148:151], v[108:111], v[216:219], v[148:151]\n\t" \
  "v_mfma_f32_16x16x32_bf16 v[152:155], v[104:107], v[220:223], v[152:155]\n\t" \
  "v_mfma_f32_16x16x32_bf16 v[156:159], v[108:111], v[220:223], v[156:159]\n\t" \
  "v_mfma_f32_16x16x32_bf16 v[144:147], v[112:115], v[224:227], v[144:147]\n\t" \
  "v_mfma_f32_16x16x32_bf16 v[148:151], v[116:119], v[224:227], v[148:151]\n\t" \
  "v_mfma_f32_16x16x32_bf16 v[152:155], v[112:115], v[228:231], v[152:155]\n\t" \
  "v_mfma_f32_16x16x32_bf16 v[156:159], v[116:119], v[228:231], v[156:159]\n\t" \
  "v_mfma_f32_16x16x32_bf16 v[144:147], v[120:123], v[232:235], v[144:147]\n\t" \
  "v_mfma_f32_16x16x32_bf16 v[148:151], v[124:127], v[232:235], v[148:151]\n\t" \
  "v_mfma_f32_16x16x32_bf16 v[152:155], v[120:123], v[236:239], v[152:155]\n\t" \
  "v_mfma_f32_16x16x32_bf16 v[156:159], v[124:127], v[236:239], v[156:159]\n\t" \
  "s_setprio 0\n\t" \
  "s_cmp_lt_u32 s86, 64\n\t" \
  "s_cbranch_scc1 .Lp0%=\n\t" \
  "s_barrier\n\t" \
  "s_nop 2\n\t" \
  "ds_write_b128 v252, v[152:155] offset:0\n\t" \
  "ds_write_b128 v252, v[156:159] offset:1024\n\t" \
  "s_waitcnt lgkmcnt(0)\n\t" \
  "s_barrier\n\t" \
  "ds_read_b128 v[160:163], v253 offset:0\n\t" \
  "ds_read_b128 v[164:167], v253 offset:1024\n\t" \
  "s_waitcnt lgkmcnt(0)\n\t" \
  "s_lshr_b32 s90, s87, 7\n\t" \
  "s_xor_b32 s90, s90, %[s0]\n\t" \
  "s_and_b32 s90, s90, 1\n\t" \
  "s_lshl1_add_u32 s90, s87, s90\n\t" \
  "s_lshl_b32 s90, s90, 13\n\t" \
  "v_add_f32 v160, v160, v144\n\t" \
  "v_add_f32 v161, v161, v145\n\t" \
  "v_add_f32 v162, v162, v146\n\t" \
  "v_add_f32 v163, v163, v147\n\t" \
  "v_add_f32 v164, v164, v148\n\t" \
  "v_add_f32 v165, v165, v149\n\t" \
  "v_add_f32 v166, v166, v150\n\t" \
  "v_add_f32 v167, v167, v151\n\t" \
  "v_cvt_pk_bf16_f32 v168, v160, v161\n\t" \
  "v_cvt_pk_bf16_f32 v169, v162, v163\n\t" \
  "v_cvt_pk_bf16_f32 v170, v164, v165\n\t" \
  "v_cvt_pk_bf16_f32 v171, v166, v167\n\t" \
  "buffer_store_dwordx4 v[168:171], v172, %[rt], s90 offen sc1\n\t" \
  "s_waitcnt vmcnt(0)\n\t" \
  "s_barrier\n\t" \
  "s_mov_b32 m0, s94\n\t" \
  "s_branch .Le%=\n\t" \
  ".Lk%=:\n\t" \
  "s_mov_b32 s94, m0\n\t" \
  "s_mov_b64 s[92:93], %[x0]\n\t" \
  "s_mov_b32 s86, %[g0]\n\t" \
  "s_add_u32 s87, s86, %[t0]\n\t" \
  "s_add_u32 m0, %[lw], 67584\n\t" \
  "s_nop 0\n\t" \
  "global_load_lds_dwordx4 v250, s[92:93] sc1\n\t" \
  "s_add_u32 m0, %[lw], 68608\n\t" \
  "s_nop 0\n\t" \
  "global_load_lds_dwordx4 v251, s[92:93] sc1\n\t" \
  "s_barrier\n\t" \
  "s_branch .Ls1%=\n\t" \
  ".Lp1%=:\n\t" \
  "s_waitcnt vmcnt(10)\n\t" \
  "s_barrier\n\t" \
  "s_setprio 2\n\t" \
  "v_mfma_f32_16x16x32_bf16 v[144:147], v[64:67], v[176:179], v[144:147]\n\t" \
  "v_mfma_f32_16x16x32_bf16 v[148:151], v[68:71], v[176:179], v[148:151]\n\t" \
  "v_mfma_f32_16x16x32_bf16 v[152:155], v[64:67], v[180:183], v[152:155]\n\t" \
  "v_mfma_f32_16x16x32_bf16 v[156:159], v[68:71], v[180:183], v[156:159]\n\t" \
  "v_mfma_f32_16x16x32_bf16 v[144:147], v[72:75], v[184:187], v[144:147]\n\t" \
  "v_mfma_f32_16x16x32_bf16 v[148:151], v[76:79], v[184:187], v[148:151]\n\t" \
  "v_mfma_f32_16x16x32_bf16 v[152:155], v[72:75], v[188:191], v[152:155]\n\t" \
  "v_mfma_f32_16x16x32_bf16 v[156:159], v[76:79], v[188:191], v[156:159]\n\t" \
  "v_mfma_f32_16x16x32_bf16 v[144:147], v[80:83], v[192:195], v[144:147]\n\t" \
  "v_mfma_f32_16x16x32_bf16 v[148:151], v[84:87], v[192:195], v[148:151]\n\t" \
  "v_mfma_f32_16x16x32_bf16 v[152:155], v[80:83], v[196:199], v[152:155]\n\t" \
  "v_mfma_f32_16x16x32_bf16 v[156:159], v[84:87], v[196:199], v[156:159]\n\t" \
  "v_mfma_f32_16x16x32_bf16 v[144:147], v[88:91], v[200:203], v[144:147]\n\t" \
  "v_mfma_f32_16x16x32_bf16 v[148:151], v[92:95], v[200:203], v[148:151]\n\t" \
  "v_mfma_f32_16x16x32_bf16 v[152:155], v[88:91], v[204:207], v[152:155]\n\t" \
  "v_mfma_f32_16x16x32_bf16 v[156:159], v[92:95], v[204:207], v[156:159]\n\t" \
  "v_mfma_f32_16x16x32_bf16 v[144:147], v[96:99], v[208:211], v[144:147]\n\t" \
  "v_mfma_f32_16x16x32_bf16 v[148:151], v[100:103], v[208:211], v[148:151]\n\t" \
  "v_mfma_f32_16x16x32_bf16 v[152:155], v[96:99], v[212:215], v[152:155]\n\t" \
  "v_mfma_f32_16x16x32_bf16 v[156:159], v[100:103], v[212:215], v[156:159]\n\t" \
  "v_mfma_f32_16x16x32_bf16 v[144:147], v[104:107], v[216:219], v[144:147]\n\t" \
  "v_mfma_f32_16x16x32_bf16 v[148:151], v[108:111], v[216:219], v[148:151]\n\t" \
  "v_mfma_f32_16x16x32_bf16 v[152:155], v[104:107], v[220:223], v[152:155]\n\t" \
  "v_mfma_f32_16x16x32_bf16 v[156:159], v[108:111], v[220:223], v[156:159]\n\t" \
  "v_mfma_f32_16x16x32_bf16 v[144:147], v[112:115], v[224:227], v[144:147]\n\t" \
  "v_mfma_f32_16x16x32_bf16 v[148:151], v[116:119], v[224:227], v[148:151]\n\t" \
  "v_mfma_f32_16x16x32_bf16 v[152:155], v[112:115], v[228:231], v[152:155]\n\t" \
  "v_mfma_f32_16x16x32_bf16 v[156:159], v[116:119], v[228:231], v[156:159]\n\t" \
  "v_mfma_f32_16x16x32_bf16 v[144:147], v[120:123], v[232:235], v[144:147]\n\t" \
  "v_mfma_f32_16x16x32_bf16 v[148:151], v[124:127], v[232:235], v[148:151]\n\t" \
  "v_mfma_f32_16x16x32_bf16 v[152:155], v[120:123], v[236:239], v[152:155]\n\t" \
  "v_mfma_f32_16x16x32_bf16 v[156:159], v[124:127], v[236:239], v[156:159]\n\t" \
  "s_setprio 0\n\t" \
  ".Ls1%=:\n\t" \
  "s_add_u32 s92, s92, 512\n\t" \
  "s_addc_u32 s93, s93, 0\n\t" \
  "ds_read_b128 v[176:179], v240 offset:0\n\t" \
  "ds_read_b128 v[180:183], v240 offset:16384\n\t" \
  "ds_read_b128 v[184:187], v241 offset:0\n\t" \
  "ds_read_b128 v[188:191], v241 offset:16384\n\t" \
  "s_add_u32 m0, %[lw], 98304\n\t" \
  "s_nop 0\n\t" \
  "global_load_lds_dwordx4 v248, s[92:93] sc1\n\t" \
  "ds_read_b128 v[192:195], v242 offset:0\n\t" \
  "ds_read_b128 v[196:199], v242 offset:16384\n\t" \
  "ds_read_b128 v[200:203], v243 offset:0\n\t" \
  "ds_read_b128 v[204:207], v243 offset:16384\n\t" \
  "s_add_u32 m0, %[lw], 99328\n\t" \
  "s_nop 0\n\t" \
  "global_load_lds_dwordx4 v249, s[92:93] sc1\n\t" \
  "ds_read_b128 v[208:211], v240 offset:256\n\t" \
  "ds_read_b128 v[212:215], v240 offset:16640\n\t" \
  "ds_read_b128 v[216:219], v241 offset:256\n\t" \
  "ds_read_b128 v[220:223], v241 offset:16640\n\t" \
  "s_add_u32 m0, %[lw], 100352\n\t" \
  "s_nop 0\n\t" \
  "global_load_lds_dwordx4 v250, s[92:93] sc1\n\t" \
  "ds_read_b128 v[224:227], v242 offset:256\n\t" \
  "ds_read_b128 v[228:231], v242 offset:16640\n\t" \
  "ds_read_b128 v[232:235], v243 offset:256\n\t" \
  "ds_read_b128 v[236:239], v243 offset:16640\n\t" \
  "s_add_u32 m0, %[lw], 101376\n\t" \
  "s_nop 0\n\t" \
  "global_load_lds_dwordx4 v251, s[92:93] sc1\n\t" \
  "ds_write_b128 v252, v[152:155] offset:0\n\t" \
  "ds_write_b128 v252, v[156:159] offset:1024\n\t" \
  "s_waitcnt lgkmcnt(0)\n\t" \
  "s_waitcnt vmcnt(4)\n\t" \
  "s_barrier\n\t" \
  "s_setprio 2\n\t" \
  "v_mfma_f32_16x16x32_bf16 v[128:131], v[0:3], v[176:179], 0\n\t" \
  "v_mfma_f32_16x16x32_bf16 v[132:135], v[4:7], v[176:179], 0\n\t" \
  "v_mfma_f32_16x16x32_bf16 v[136:139], v[0:3], v[180:183], 0\n\t" \
  "v_mfma_f32_16x16x32_bf16 v[140:143], v[4:7], v[180:183], 0\n\t" \
  "v_mfma_f32_16x16x32_bf16 v[128:131], v[8:11], v[184:187], v[128:131]\n\t" \
  "v_mfma_f32_16x16x32_bf16 v[132:135], v[12:15], v[184:187], v[132:135]\n\t" \
  "v_mfma_f32_16x16x32_bf16 v[136:139], v[8:11], v[188:191], v[136:139]\n\t" \
  "v_mfma_f32_16x16x32_bf16 v[140:143], v[12:15], v[188:191], v[140:143]\n\t" \
  "v_mfma_f32_16x16x32_bf16 v[128:131], v[16:19], v[192:195], v[128:131]\n\t" \
  "v_mfma_f32_16x16x32_bf16 v[132:135], v[20:23], v[192:195], v[132:135]\n\t" \
  "v_mfma_f32_16x16x32_bf16 v[136:139], v[16:19], v[196:199], v[136:139]\n\t" \
  "v_mfma_f32_16x16x32_bf16 v[140:143], v[20:23], v[196:199], v[140:143]\n\t" \
  "v_mfma_f32_16x16x32_bf16 v[128:131], v[24:27], v[200:203], v[128:131]\n\t" \
  "v_mfma_f32_16x16x32_bf16 v[132:135], v[28:31], v[200:203], v[132:135]\n\t" \
  "v_mfma_f32_16x16x32_bf16 v[136:139], v[24:27], v[204:207], v[136:139]\n\t" \
  "v_mfma_f32_16x16x32_bf16 v[140:143], v[28:31], v[204:207], v[140:143]\n\t" \
  "v_mfma_f32_16x16x32_bf16 v[128:131], v[32:35], v[208:211], v[128:131]\n\t" \
  "v_mfma_f32_16x16x32_bf16 v[132:135], v[36:39], v[208:211], v[132:135]\n\t" \
  "v_mfma_f32_16x16x32_bf16 v[136:139], v[32:35], v[212:215], v[136:139]\n\t" \
  "v_mfma_f32_16x16x32_bf16 v[140:143], v[36:39], v[212:215], v[140:143]\n\t" \
  "v_mfma_f32_16x16x32_bf16 v[128:131], v[40:43], v[216:219], v[128:131]\n\t" \
  "v_mfma_f32_16x16x32_bf16 v[132:135], v[44:47], v[216:219], v[132:135]\n\t" \
  "v_mfma_f32_16x16x32_bf16 v[136:139], v[40:43], v[220:223], v[136:139]\n\t" \
  "v_mfma_f32_16x16x32_bf16 v[140:143], v[44:47], v[220:223], v[140:143]\n\t" \
  "v_mfma_f32_16x16x32_bf16 v[128:131], v[48:51], v[224:227], v[128:131]\n\t" \
  "v_mfma_f32_16x16x32_bf16 v[132:135], v[52:55], v[224:227], v[132:135]\n\t" \
  "v_mfma_f32_16x16x32_bf16 v[136:139], v[48:51], v[228:231], v[136:139]\n\t" \
  "v_mfma_f32_16x16x32_bf16 v[140:143], v[52:55], v[228:231], v[140:143]\n\t" \
  "v_mfma_f32_16x16x32_bf16 v[128:131], v[56:59], v[232:235], v[128:131]\n\t" \
  "v_mfma_f32_16x16x32_bf16 v[132:135], v[60:63], v[232:235], v[132:135]\n\t" \
  "v_mfma_f32_16x16x32_bf16 v[136:139], v[56:59], v[236:239], v[136:139]\n\t" \
  "v_mfma_f32_16x16x32_bf16 v[140:143], v[60:63], v[236:239], v[140:143]\n\t" \
  "s_setprio 0\n\t" \
  "s_add_u32 s92, s92, 65024\n\t" \
  "s_addc_u32 s93, s93, 0\n\t" \
  "ds_read_b128 v[160:163], v253 offset:0\n\t" \
  "ds_read_b128 v[164:167], v253 offset:1024\n\t" \
  "ds_read_b128 v[176:179], v240 offset:32768\n\t" \
  "ds_read_b128 v[180:183], v240 offset:49152\n\t" \
  "ds_read_b128 v[184:187], v241 offset:32768\n\t" \
  "ds_read_b128 v[188:191], v241 offset:49152\n\t" \
  "s_add_u32 m0, %[lw], 0\n\t" \
  "s_nop 0\n\t" \
  "global_load_lds_dwordx4 v248, s[92:93] sc1\n\t" \
  "ds_read_b128 v[192:195], v242 offset:32768\n\t" \
  "ds_read_b128 v[196:199], v242 offset:49152\n\t" \
  "ds_read_b128 v[200:203], v243 offset:32768\n\t" \
  "ds_read_b128 v[204:207], v243 offset:49152\n\t" \
  "s_add_u32 m0, %[lw], 1024\n\t" \
  "s_nop 0\n\t" \
  "global_load_lds_dwordx4 v249, s[92:93] sc1\n\t" \
  "ds_read_b128 v[208:211], v240 offset:33024\n\t" \
  "ds_read_b128 v[212:215], v240 offset:49408\n\t" \
  "ds_read_b128 v[216:219], v241 offset:33024\n\t" \
  "ds_read_b128 v[220:223], v241 offset:49408\n\t" \
  "s_add_u32 m0, %[lw], 2048\n\t" \
  "s_nop 0\n\t" \
  "global_load_lds_dwordx4 v250, s[92:93] sc1\n\t" \
  "ds_read_b128 v[224:227], v242 offset:33024\n\t" \
  "ds_read_b128 v[228:231], v242 offset:49408\n\t" \
  "ds_read_b128 v[232:235], v243 offset:33024\n\t" \
  "ds_read_b128 v[236:239], v243 offset:49408\n\t" \
  "s_add_u32 m0, %[lw], 3072\n\t" \
  "s_nop 0\n\t" \
  "global_load_lds_dwordx4 v251, s[92:93] sc1\n\t" \
  "s_waitcnt lgkmcnt(15)\n\t" \
  "s_waitcnt lgkmcnt(12)\n\t" \
  "s_sub_i32 s91, s86, 2\n\t" \
  "v_mov_b32 v173, s91\n\t" \
  "buffer_store_dword v173, v174, %[rs], 0 offen sc1\n\t" \
  "s_lshr_b32 s90, s87, 7\n\t" \
  "s_xor_b32 s90, s90, %[s0]\n\t" \
  "s_and_b32 s90, s90, 1\n\t" \
  "s_lshl1_add_u32 s90, s87, s90\n\t" \
  "s_lshl_b32 s90, s90, 13\n\t" \
  "v_add_f32 v160, v160, v144\n\t" \
  "v_add_f32 v161, v161, v145\n\t" \
  "v_add_f32 v162, v162, v146\n\t" \
  "v_add_f32 v163, v163, v147\n\t" \
  "v_add_f32 v164, v164, v148\n\t" \
  "v_add_f32 v165, v165, v149\n\t" \
  "v_add_f32 v166, v166, v150\n\t" \
  "v_add_f32 v167, v167, v151\n\t" \
  "v_cvt_pk_bf16_f32 v168, v160, v161\n\t" \
  "v_cvt_pk_bf16_f32 v169, v162, v163\n\t" \
  "v_cvt_pk_bf16_f32 v170, v164, v165\n\t" \
  "v_cvt_pk_bf16_f32 v171, v166, v167\n\t" \
  "buffer_store_dwordx4 v[168:171], v172, %[rt], s90 offen sc1\n\t" \
  "s_add_u32 s86, s86, 1\n\t" \
  "s_add_u32 s87, s87, 1\n\t" \
  "s_waitcnt lgkmcnt(0)\n\t" \
  "s_waitcnt vmcnt(10)\n\t" \
  "s_barrier\n\t" \
  "s_setprio 2\n\t" \
  "v_mfma_f32_16x16x32_bf16 v[128:131], v[64:67], v[176:179], v[128:131]\n\t" \
  "v_mfma_f32_16x16x32_bf16 v[132:135], v[68:71], v[176:179], v[132:135]\n\t" \
  "v_mfma_f32_16x16x32_bf16 v[136:139], v[64:67], v[180:183], v[136:139]\n\t" \
  "v_mfma_f32_16x16x32_bf16 v[140:143], v[68:71], v[180:183], v[140:143]\n\t" \
  "v_mfma_f32_16x16x32_bf16 v[128:131], v[72:75], v[184:187], v[128:131]\n\t" \
  "v_mfma_f32_16x16x32_bf16 v[132:135], v[76:79], v[184:187], v[132:135]\n\t" \
  "v_mfma_f32_16x16x32_bf16 v[136:139], v[72:75], v[188:191], v[136:139]\n\t" \
  "v_mfma_f32_16x16x32_bf16 v[140:143], v[76:79], v[188:191], v[140:143]\n\t" \
  "v_mfma_f32_16x16x32_bf16 v[128:131], v[80:83], v[192:195], v[128:131]\n\t" \
  "v_mfma_f32_16x16x32_bf16 v[132:135], v[84:87], v[192:195], v[132:135]\n\t" \
  "v_mfma_f32_16x16x32_bf16 v[136:139], v[80:83], v[196:199], v[136:139]\n\t" \
  "v_mfma_f32_16x16x32_bf16 v[140:143], v[84:87], v[196:199], v[140:143]\n\t" \
  "v_mfma_f32_16x16x32_bf16 v[128:131], v[88:91], v[200:203], v[128:131]\n\t" \
  "v_mfma_f32_16x16x32_bf16 v[132:135], v[92:95], v[200:203], v[132:135]\n\t" \
  "v_mfma_f32_16x16x32_bf16 v[136:139], v[88:91], v[204:207], v[136:139]\n\t" \
  "v_mfma_f32_16x16x32_bf16 v[140:143], v[92:95], v[204:207], v[140:143]\n\t" \
  "v_mfma_f32_16x16x32_bf16 v[128:131], v[96:99], v[208:211], v[128:131]\n\t" \
  "v_mfma_f32_16x16x32_bf16 v[132:135], v[100:103], v[208:211], v[132:135]\n\t" \
  "v_mfma_f32_16x16x32_bf16 v[136:139], v[96:99], v[212:215], v[136:139]\n\t" \
  "v_mfma_f32_16x16x32_bf16 v[140:143], v[100:103], v[212:215], v[140:143]\n\t" \
  "v_mfma_f32_16x16x32_bf16 v[128:131], v[104:107], v[216:219], v[128:131]\n\t" \
  "v_mfma_f32_16x16x32_bf16 v[132:135], v[108:111], v[216:219], v[132:135]\n\t" \
  "v_mfma_f32_16x16x32_bf16 v[136:139], v[104:107], v[220:223], v[136:139]\n\t" \
  "v_mfma_f32_16x16x32_bf16 v[140:143], v[108:111], v[220:223], v[140:143]\n\t" \
  "v_mfma_f32_16x16x32_bf16 v[128:131], v[112:115], v[224:227], v[128:131]\n\t" \
  "v_mfma_f32_16x16x32_bf16 v[132:135], v[116:119], v[224:227], v[132:135]\n\t" \
  "v_mfma_f32_16x16x32_bf16 v[136:139], v[112:115], v[228:231], v[136:139]\n\t" \
  "v_mfma_f32_16x16x32_bf16 v[140:143], v[116:119], v[228:231], v[140:143]\n\t" \
  "v_mfma_f32_16x16x32_bf16 v[128:131], v[120:123], v[232:235], v[128:131]\n\t" \
  "v_mfma_f32_16x16x32_bf16 v[132:135], v[124:127], v[232:235], v[132:135]\n\t" \
  "v_mfma_f32_16x16x32_bf16 v[136:139], v[120:123], v[236:239], v[136:139]\n\t" \
  "v_mfma_f32_16x16x32_bf16 v[140:143], v[124:127], v[236:239], v[140:143]\n\t" \
  "s_setprio 0\n\t" \
  "s_add_u32 s92, s92, 512\n\t" \
  "s_addc_u32 s93, s93, 0\n\t" \
  "ds_read_b128 v[176:179], v244 offset:0\n\t" \
  "ds_read_b128 v[180:183], v244 offset:16384\n\t" \
  "ds_read_b128 v[184:187], v245 offset:0\n\t" \
  "ds_read_b128 v[188:191], v245 offset:16384\n\t" \
  "s_add_u32 m0, %[lw], 32768\n\t" \
  "s_nop 0\n\t" \
  "global_load_lds_dwordx4 v248, s[92:93] sc1\n\t" \
  "ds_read_b128 v[192:195], v246 offset:0\n\t" \
  "ds_read_b128 v[196:199], v246 offset:16384\n\t" \
  "ds_read_b128 v[200:203], v247 offset:0\n\t" \
  "ds_read_b128 v[204:207], v247 offset:16384\n\t" \
  "s_add_u32 m0, %[lw], 33792\n\t" \
  "s_nop 0\n\t" \
  "global_load_lds_dwordx4 v249, s[92:93] sc1\n\t" \
  "ds_read_b128 v[208:211], v244 offset:256\n\t" \
  "ds_read_b128 v[212:215], v244 offset:16640\n\t" \
  "ds_read_b128 v[216:219], v245 offset:256\n\t" \
  "ds_read_b128 v[220:223], v245 offset:16640\n\t" \
  "s_add_u32 m0, %[lw], 34816\n\t" \
  "s_nop 0\n\t" \
  "global_load_lds_dwordx4 v250, s[92:93] sc1\n\t" \
  "ds_read_b128 v[224:227], v246 offset:256\n\t" \
  "ds_read_b128 v[228:231], v246 offset:16640\n\t" \
  "ds_read_b128 v[232:235], v247 offset:256\n\t" \
  "ds_read_b128 v[236:239], v247 offset:16640\n\t" \
  "s_add_u32 m0, %[lw], 35840\n\t" \
  "s_nop 0\n\t" \
  "global_load_lds_dwordx4 v251, s[92:93] sc1\n\t" \
  "ds_write_b128 v252, v[136:139] offset:0\n\t" \
  "ds_write_b128 v252, v[140:143] offset:1024\n\t" \
  "s_waitcnt lgkmcnt(0)\n\t" \
  "s_waitcnt vmcnt(4)\n\t" \
  "s_barrier\n\t" \
  "s_setprio 2\n\t" \
  "v_mfma_f32_16x16x32_bf16 v[144:147], v[0:3], v[176:179], 0\n\t" \
  "v_mfma_f32_16x16x32_bf16 v[148:151], v[4:7], v[176:179], 0\n\t" \
  "v_mfma_f32_16x16x32_bf16 v[152:155], v[0:3], v[180:183], 0\n\t" \
  "v_mfma_f32_16x16x32_bf16 v[156:159], v[4:7], v[180:183], 0\n\t" \
  "v_mfma_f32_16x16x32_bf16 v[144:147], v[8:11], v[184:187], v[144:147]\n\t" \
  "v_mfma_f32_16x16x32_bf16 v[148:151], v[12:15], v[184:187], v[148:151]\n\t" \
  "v_mfma_f32_16x16x32_bf16 v[152:155], v[8:11], v[188:191], v[152:155]\n\t" \
  "v_mfma_f32_16x16x32_bf16 v[156:159], v[12:15], v[188:191], v[156:159]\n\t" \
  "v_mfma_f32_16x16x32_bf16 v[144:147], v[16:19], v[192:195], v[144:147]\n\t" \
  "v_mfma_f32_16x16x32_bf16 v[148:151], v[20:23], v[192:195], v[148:151]\n\t" \
  "v_mfma_f32_16x16x32_bf16 v[152:155], v[16:19], v[196:199], v[152:155]\n\t" \
  "v_mfma_f32_16x16x32_bf16 v[156:159], v[20:23], v[196:199], v[156:159]\n\t" \
  "v_mfma_f32_16x16x32_bf16 v[144:147], v[24:27], v[200:203], v[144:147]\n\t" \
  "v_mfma_f32_16x16x32_bf16 v[148:151], v[28:31], v[200:203], v[148:151]\n\t" \
  "v_mfma_f32_16x16x32_bf16 v[152:155], v[24:27], v[204:207], v[152:155]\n\t" \
  "v_mfma_f32_16x16x32_bf16 v[156:159], v[28:31], v[204:207], v[156:159]\n\t" \
  "v_mfma_f32_16x16x32_bf16 v[144:147], v[32:35], v[208:211], v[144:147]\n\t" \
  "v_mfma_f32_16x16x32_bf16 v[148:151], v[36:39], v[208:211], v[148:151]\n\t" \
  "v_mfma_f32_16x16x32_bf16 v[152:155], v[32:35], v[212:215], v[152:155]\n\t" \
  "v_mfma_f32_16x16x32_bf16 v[156:159], v[36:39], v[212:215], v[156:159]\n\t" \
  "v_mfma_f32_16x16x32_bf16 v[144:147], v[40:43], v[216:219], v[144:147]\n\t" \
  "v_mfma_f32_16x16x32_bf16 v[148:151], v[44:47], v[216:219], v[148:151]\n\t" \
  "v_mfma_f32_16x16x32_bf16 v[152:155], v[40:43], v[220:223], v[152:155]\n\t" \
  "v_mfma_f32_16x16x32_bf16 v[156:159], v[44:47], v[220:223], v[156:159]\n\t" \
  "v_mfma_f32_16x16x32_bf16 v[144:147], v[48:51], v[224:227], v[144:147]\n\t" \
  "v_mfma_f32_16x16x32_bf16 v[148:151], v[52:55], v[224:227], v[148:151]\n\t" \
  "v_mfma_f32_16x16x32_bf16 v[152:155], v[48:51], v[228:231], v[152:155]\n\t" \
  "v_mfma_f32_16x16x32_bf16 v[156:159], v[52:55], v[228:231], v[156:159]\n\t" \
  "v_mfma_f32_16x16x32_bf16 v[144:147], v[56:59], v[232:235], v[144:147]\n\t" \
  "v_mfma_f32_16x16x32_bf16 v[148:151], v[60:63], v[232:235], v[148:151]\n\t" \
  "v_mfma_f32_16x16x32_bf16 v[152:155], v[56:59], v[236:239], v[152:155]\n\t" \
  "v_mfma_f32_16x16x32_bf16 v[156:159], v[60:63], v[236:239], v[156:159]\n\t" \
  "s_setprio 0\n\t" \
  "s_add_u32 s92, s92, 65024\n\t" \
  "s_addc_u32 s93, s93, 0\n\t" \
  "ds_read_b128 v[160:163], v253 offset:0\n\t" \
  "ds_read_b128 v[164:167], v253 offset:1024\n\t" \
  "ds_read_b128 v[176:179], v244 offset:32768\n\t" \
  "ds_read_b128 v[180:183], v244 offset:49152\n\t" \
  "ds_read_b128 v[184:187], v245 offset:32768\n\t" \
  "ds_read_b128 v[188:191], v245 offset:49152\n\t" \
  "s_add_u32 m0, %[lw], 65536\n\t" \
  "s_nop 0\n\t" \
  "global_load_lds_dwordx4 v248, s[92:93] sc1\n\t" \
  "ds_read_b128 v[192:195], v246 offset:32768\n\t" \
  "ds_read_b128 v[196:199], v246 offset:49152\n\t" \
  "ds_read_b128 v[200:203], v247 offset:32768\n\t" \
  "ds_read_b128 v[204:207], v247 offset:49152\n\t" \
  "s_add_u32 m0, %[lw], 66560\n\t" \
  "s_nop 0\n\t" \
  "global_load_lds_dwordx4 v249, s[92:93] sc1\n\t" \
  "ds_read_b128 v[208:211], v244 offset:33024\n\t" \
  "ds_read_b128 v[212:215], v244 offset:49408\n\t" \
  "ds_read_b128 v[216:219], v245 offset:33024\n\t" \
  "ds_read_b128 v[220:223], v245 offset:49408\n\t" \
  "s_add_u32 m0, %[lw], 67584\n\t" \
  "s_nop 0\n\t" \
  "global_load_lds_dwordx4 v250, s[92:93] sc1\n\t" \
  "ds_read_b128 v[224:227], v246 offset:33024\n\t" \
  "ds_read_b128 v[228:231], v246 offset:49408\n\t" \
  "ds_read_b128 v[232:235], v247 offset:33024\n\t" \
  "ds_read_b128 v[236:239], v247 offset:49408\n\t" \
  "s_add_u32 m0, %[lw], 68608\n\t" \
  "s_nop 0\n\t" \
  "global_load_lds_dwordx4 v251, s[92:93] sc1\n\t" \
  "s_waitcnt lgkmcnt(15)\n\t" \
  "s_waitcnt lgkmcnt(12)\n\t" \
  "s_sub_i32 s91, s86, 2\n\t" \
  "v_mov_b32 v173, s91\n\t" \
  "buffer_store_dword v173, v174, %[rs], 0 offen sc1\n\t" \
  "s_lshr_b32 s90, s87, 7\n\t" \
  "s_xor_b32 s90, s90, %[s0]\n\t" \
  "s_and_b32 s90, s90, 1\n\t" \
  "s_lshl1_add_u32 s90, s87, s90\n\t" \
  "s_lshl_b32 s90, s90, 13\n\t" \
  "v_add_f32 v160, v160, v128\n\t" \
  "v_add_f32 v161, v161, v129\n\t" \
  "v_add_f32 v162, v162, v130\n\t" \
  "v_add_f32 v163, v163, v131\n\t" \
  "v_add_f32 v164, v164, v132\n\t" \
  "v_add_f32 v165, v165, v133\n\t" \
  "v_add_f32 v166, v166, v134\n\t" \
  "v_add_f32 v167, v167, v135\n\t" \
  "v_cvt_pk_bf16_f32 v168, v160, v161\n\t" \
  "v_cvt_pk_bf16_f32 v169, v162, v163\n\t" \
  "v_cvt_pk_bf16_f32 v170, v164, v165\n\t" \
  "v_cvt_pk_bf16_f32 v171, v166, v167\n\t" \
  "buffer_store_dwordx4 v[168:171], v172, %[rt], s90 offen sc1\n\t" \
  "s_add_u32 s86, s86, 1\n\t" \
  "s_add_u32 s87, s87, 1\n\t" \
  "s_waitcnt lgkmcnt(0)\n\t" \
  "s_cmp_lt_u32 s86, 64\n\t" \
  "s_cbranch_scc1 .Lp1%=\n\t" \
  "s_barrier\n\t" \
  "s_setprio 2\n\t" \
  "v_mfma_f32_16x16x32_bf16 v[144:147], v[64:67], v[176:179], v[144:147]\n\t" \
  "v_mfma_f32_16x16x32_bf16 v[148:151], v[68:71], v[176:179], v[148:151]\n\t" \
  "v_mfma_f32_16x16x32_bf16 v[152:155], v[64:67], v[180:183], v[152:155]\n\t" \
  "v_mfma_f32_16x16x32_bf16 v[156:159], v[68:71], v[180:183], v[156:159]\n\t" \
  "v_mfma_f32_16x16x32_bf16 v[144:147], v[72:75], v[184:187], v[144:147]\n\t" \
  "v_mfma_f32_16x16x32_bf16 v[148:151], v[76:79], v[184:187], v[148:151]\n\t" \
  "v_mfma_f32_16x16x32_bf16 v[152:155], v[72:75], v[188:191], v[152:155]\n\t" \
  "v_mfma_f32_16x16x32_bf16 v[156:159], v[76:79], v[188:191], v[156:159]\n\t" \
  "v_mfma_f32_16x16x32_bf16 v[144:147], v[80:83], v[192:195], v[144:147]\n\t" \
  "v_mfma_f32_16x16x32_bf16 v[148:151], v[84:87], v[192:195], v[148:151]\n\t" \
  "v_mfma_f32_16x16x32_bf16 v[152:155], v[80:83], v[196:199], v[152:155]\n\t" \
  "v_mfma_f32_16x16x32_bf16 v[156:159], v[84:87], v[196:199], v[156:159]\n\t" \
  "v_mfma_f32_16x16x32_bf16 v[144:147], v[88:91], v[200:203], v[144:147]\n\t" \
  "v_mfma_f32_16x16x32_bf16 v[148:151], v[92:95], v[200:203], v[148:151]\n\t" \
  "v_mfma_f32_16x16x32_bf16 v[152:155], v[88:91], v[204:207], v[152:155]\n\t" \
  "v_mfma_f32_16x16x32_bf16 v[156:159], v[92:95], v[204:207], v[156:159]\n\t" \
  "v_mfma_f32_16x16x32_bf16 v[144:147], v[96:99], v[208:211], v[144:147]\n\t" \
  "v_mfma_f32_16x16x32_bf16 v[148:151], v[100:103], v[208:211], v[148:151]\n\t" \
  "v_mfma_f32_16x16x32_bf16 v[152:155], v[96:99], v[212:215], v[152:155]\n\t" \
  "v_mfma_f32_16x16x32_bf16 v[156:159], v[100:103], v[212:215], v[156:159]\n\t" \
  "v_mfma_f32_16x16x32_bf16 v[144:147], v[104:107], v[216:219], v[144:147]\n\t" \
  "v_mfma_f32_16x16x32_bf16 v[148:151], v[108:111], v[216:219], v[148:151]\n\t" \
  "v_mfma_f32_16x16x32_bf16 v[152:155], v[104:107], v[220:223], v[152:155]\n\t" \
  "v_mfma_f32_16x16x32_bf16 v[156:159], v[108:111], v[220:223], v[156:159]\n\t" \
  "v_mfma_f32_16x16x32_bf16 v[144:147], v[112:115], v[224:227], v[144:147]\n\t" \
  "v_mfma_f32_16x16x32_bf16 v[148:151], v[116:119], v[224:227], v[148:151]\n\t" \
  "v_mfma_f32_16x16x32_bf16 v[152:155], v[112:115], v[228:231], v[152:155]\n\t" \
  "v_mfma_f32_16x16x32_bf16 v[156:159], v[116:119], v[228:231], v[156:159]\n\t" \
  "v_mfma_f32_16x16x32_bf16 v[144:147], v[120:123], v[232:235], v[144:147]\n\t" \
  "v_mfma_f32_16x16x32_bf16 v[148:151], v[124:127], v[232:235], v[148:151]\n\t" \
  "v_mfma_f32_16x16x32_bf16 v[152:155], v[120:123], v[236:239], v[152:155]\n\t" \
  "v_mfma_f32_16x16x32_bf16 v[156:159], v[124:127], v[236:239], v[156:159]\n\t" \
  "s_setprio 0\n\t" \
  "s_nop 5\n\t" \
  "ds_write_b128 v252, v[152:155] offset:0\n\t" \
  "ds_write_b128 v252, v[156:159] offset:1024\n\t" \
  "s_waitcnt lgkmcnt(0)\n\t" \
  "s_barrier\n\t" \
  "ds_read_b128 v[160:163], v253 offset:0\n\t" \
  "ds_read_b128 v[164:167], v253 offset:1024\n\t" \
  "s_waitcnt lgkmcnt(0)\n\t" \
  "s_lshr_b32 s90, s87, 7\n\t" \
  "s_xor_b32 s90, s90, %[s0]\n\t" \
  "s_and_b32 s90, s90, 1\n\t" \
  "s_lshl1_add_u32 s90, s87, s90\n\t" \
  "s_lshl_b32 s90, s90, 13\n\t" \
  "v_add_f32 v160, v160, v144\n\t" \
  "v_add_f32 v161, v161, v145\n\t" \
  "v_add_f32 v162, v162, v146\n\t" \
  "v_add_f32 v163, v163, v147\n\t" \
  "v_add_f32 v164, v164, v148\n\t" \
  "v_add_f32 v165, v165, v149\n\t" \
  "v_add_f32 v166, v166, v150\n\t" \
  "v_add_f32 v167, v167, v151\n\t" \
  "v_cvt_pk_bf16_f32 v168, v160, v161\n\t" \
  "v_cvt_pk_bf16_f32 v169, v162, v163\n\t" \
  "v_cvt_pk_bf16_f32 v170, v164, v165\n\t" \
  "v_cvt_pk_bf16_f32 v171, v166, v167\n\t" \
  "buffer_store_dwordx4 v[168:171], v172, %[rt], s90 offen sc1\n\t" \
  "s_waitcnt vmcnt(0)\n\t" \
  "s_barrier\n\t" \
  "s_sub_i32 s91, s86, 1\n\t" \
  "v_mov_b32 v173, s91\n\t" \
  "buffer_store_dword v173, v174, %[rs], 0 offen sc1\n\t" \
  "s_mov_b32 m0, s94\n\t" \
  ".Le%=:\n\t"
#define PP_ASM_1 \
  "s_cmp_lg_u32 %[kh], 0\n\t" \
  "s_cbranch_scc1 .Lk%=\n\t" \
  "s_mov_b32 s94, m0\n\t" \
  "s_mov_b64 s[92:93], %[x0]\n\t" \
  "s_mov_b32 s86, %[g0]\n\t" \
  "s_add_u32 s87, s86, %[t0]\n\t" \
  "s_add_u32 m0, %[lw], 67584\n\t" \
  "s_nop 0\n\t" \
  "global_load_lds_dwordx4 v250, s[92:93] sc1\n\t" \
  "s_add_u32 m0, %[lw], 68608\n\t" \
  "s_nop 0\n\t" \
  "global_load_lds_dwordx4 v251, s[92:93] sc1\n\t" \
  "s_barrier\n\t" \
  "s_branch .Ls0%=\n\t" \
  ".Lp0%=:\n\t" \
  "s_waitcnt vmcnt(9)\n\t" \
  "s_barrier\n\t" \
  ".Ls0%=:\n\t" \
  "s_add_u32 s92, s92, 512\n\t" \
  "s_addc_u32 s93, s93, 0\n\t" \
  "ds_read_b128 v[176:179], v240 offset:0\n\t" \
  "ds_read_b128 v[180:183], v240 offset:16384\n\t" \
  "ds_read_b128 v[184:187], v241 offset:0\n\t" \
  "ds_read_b128 v[188:191], v241 offset:16384\n\t" \
  "s_add_u32 m0, %[lw], 98304\n\t" \
  "s_nop 0\n\t" \
  "global_load_lds_dwordx4 v248, s[92:93] sc1\n\t" \
  "ds_read_b128 v[192:195], v242 offset:0\n\t" \
  "ds_read_b128 v[196:199], v242 offset:16384\n\t" \
  "ds_read_b128 v[200:203], v243 offset:0\n\t" \
  "ds_read_b128 v[204:207], v243 offset:16384\n\t" \
  "s_add_u32 m0, %[lw], 99328\n\t" \
  "s_nop 0\n\t" \
  "global_load_lds_dwordx4 v249, s[92:93] sc1\n\t" \
  "ds_read_b128 v[208:211], v240 offset:256\n\t" \
  "ds_read_b128 v[212:215], v240 offset:16640\n\t" \
  "ds_read_b128 v[216:219], v241 offset:256\n\t" \
  "ds_read_b128 v[220:223], v241 offset:16640\n\t" \
  "s_add_u32 m0, %[lw], 100352\n\t" \
  "s_nop 0\n\t" \
  "global_load_lds_dwordx4 v250, s[92:93] sc1\n\t" \
  "ds_read_b128 v[224:227], v242 offset:256\n\t" \
  "ds_read_b128 v[228:231], v242 offset:16640\n\t" \
  "ds_read_b128 v[232:235], v243 offset:256\n\t" \
  "ds_read_b128 v[236:239], v243 offset:16640\n\t" \
  "s_add_u32 m0, %[lw], 101376\n\t" \
  "s_nop 0\n\t" \
  "global_load_lds_dwordx4 v251, s[92:93] sc1\n\t" \
  "ds_write_b128 v252, v[152:155] offset:0\n\t" \
  "ds_write_b128 v252, v[156:159] offset:1024\n\t" \
  "s_waitcnt lgkmcnt(0)\n\t" \
  "s_setprio 1\n\t" \
  "v_mfma_f32_16x16x32_bf16 v[128:131], v[176:179], v[0:3], 0\n\t" \
  "v_mfma_f32_16x16x32_bf16 v[136:139], v[176:179], v[4:7], 0\n\t" \
  "v_mfma_f32_16x16x32_bf16 v[132:135], v[180:183], v[0:3], 0\n\t" \
  "v_mfma_f32_16x16x32_bf16 v[140:143], v[180:183], v[4:7], 0\n\t" \
  "v_mfma_f32_16x16x32_bf16 v[128:131], v[184:187], v[8:11], v[128:131]\n\t" \
  "v_mfma_f32_16x16x32_bf16 v[136:139], v[184:187], v[12:15], v[136:139]\n\t" \
  "v_mfma_f32_16x16x32_bf16 v[132:135], v[188:191], v[8:11], v[132:135]\n\t" \
  "v_mfma_f32_16x16x32_bf16 v[140:143], v[188:191], v[12:15], v[140:143]\n\t" \
  "v_mfma_f32_16x16x32_bf16 v[128:131], v[192:195], v[16:19], v[128:131]\n\t" \
  "v_mfma_f32_16x16x32_bf16 v[136:139], v[192:195], v[20:23], v[136:139]\n\t" \
  "v_mfma_f32_16x16x32_bf16 v[132:135], v[196:199], v[16:19], v[132:135]\n\t" \
  "v_mfma_f32_16x16x32_bf16 v[140:143], v[196:199], v[20:23], v[140:143]\n\t" \
  "v_mfma_f32_16x16x32_bf16 v[128:131], v[200:203], v[24:27], v[128:131]\n\t" \
  "v_mfma_f32_16x16x32_bf16 v[136:139], v[200:203], v[28:31], v[136:139]\n\t" \
  "v_mfma_f32_16x16x32_bf16 v[132:135], v[204:207], v[24:27], v[132:135]\n\t" \
  "v_mfma_f32_16x16x32_bf16 v[140:143], v[204:207], v[28:31], v[140:143]\n\t" \
  "v_mfma_f32_16x16x32_bf16 v[128:131], v[208:211], v[32:35], v[128:131]\n\t" \
  "v_mfma_f32_16x16x32_bf16 v[136:139], v[208:211], v[36:39], v[136:139]\n\t" \
  "v_mfma_f32_16x16x32_bf16 v[132:135], v[212:215], v[32:35], v[132:135]\n\t" \
  "v_mfma_f32_16x16x32_bf16 v[140:143], v[212:215], v[36:39], v[140:143]\n\t" \
  "v_mfma_f32_16x16x32_bf16 v[128:131], v[216:219], v[40:43], v[128:131]\n\t" \
  "v_mfma_f32_16x16x32_bf16 v[136:139], v[216:219], v[44:47], v[136:139]\n\t" \
  "v_mfma_f32_16x16x32_bf16 v[132:135], v[220:223], v[40:43], v[132:135]\n\t" \
  "v_mfma_f32_16x16x32_bf16 v[140:143], v[220:223], v[44:47], v[140:143]\n\t" \
  "v_mfma_f32_16x16x32_bf16 v[128:131], v[224:227], v[48:51], v[128:131]\n\t" \
  "v_mfma_f32_16x16x32_bf16 v[136:139], v[224:227], v[52:55], v[136:139]\n\t" \
  "v_mfma_f32_16x16x32_bf16 v[132:135], v[228:231], v[48:51], v[132:135]\n\t" \
  "v_mfma_f32_16x16x32_bf16 v[140:143], v[228:231], v[52:55], v[140:143]\n\t" \
  "v_mfma_f32_16x16x32_bf16 v[128:131], v[232:235], v[56:59], v[128:131]\n\t" \
  "v_mfma_f32_16x16x32_bf16 v[136:139], v[232:235], v[60:63], v[136:139]\n\t" \
  "v_mfma_f32_16x16x32_bf16 v[132:135], v[236:239], v[56:59], v[132:135]\n\t" \
  "v_mfma_f32_16x16x32_bf16 v[140:143], v[236:239], v[60:63], v[140:143]\n\t" \
  "s_setprio 0\n\t" \
  "s_waitcnt vmcnt(4)\n\t" \
  "s_barrier\n\t" \
  "s_add_u32 s92, s92, 65024\n\t" \
  "s_addc_u32 s93, s93, 0\n\t" \
  "ds_read_b128 v[160:163], v253 offset:0\n\t" \
  "ds_read_b128 v[164:167], v253 offset:1024\n\t" \
  "ds_read_b128 v[176:179], v240 offset:32768\n\t" \
  "ds_read_b128 v[180:183], v240 offset:49152\n\t" \
  "ds_read_b128 v[184:187], v241 offset:32768\n\t" \
  "ds_read_b128 v[188:191], v241 offset:49152\n\t" \
  "s_add_u32 m0, %[lw], 0\n\t" \
  "s_nop 0\n\t" \
  "global_load_lds_dwordx4 v248, s[92:93] sc1\n\t" \
  "ds_read_b128 v[192:195], v242 offset:32768\n\t" \
  "ds_read_b128 v[196:199], v242 offset:49152\n\t" \
  "ds_read_b128 v[200:203], v243 offset:32768\n\t" \
  "ds_read_b128 v[204:207], v243 offset:49152\n\t" \
  "s_add_u32 m0, %[lw], 1024\n\t" \
  "s_nop 0\n\t" \
  "global_load_lds_dwordx4 v249, s[92:93] sc1\n\t" \
  "ds_read_b128 v[208:211], v240 offset:33024\n\t" \
  "ds_read_b128 v[212:215], v240 offset:49408\n\t" \
  "ds_read_b128 v[216:219], v241 offset:33024\n\t" \
  "ds_read_b128 v[220:223], v241 offset:49408\n\t" \
  "s_add_u32 m0, %[lw], 2048\n\t" \
  "s_nop 0\n\t" \
  "global_load_lds_dwordx4 v250, s[92:93] sc1\n\t" \
  "ds_read_b128 v[224:227], v242 offset:33024\n\t" \
  "ds_read_b128 v[228:231], v242 offset:49408\n\t" \
  "ds_read_b128 v[232:235], v243 offset:33024\n\t" \
  "ds_read_b128 v[236:239], v243 offset:49408\n\t" \
  "s_add_u32 m0, %[lw], 3072\n\t" \
  "s_nop 0\n\t" \
  "global_load_lds_dwordx4 v251, s[92:93] sc1\n\t" \
  "s_waitcnt lgkmcnt(15)\n\t" \
  "s_waitcnt lgkmcnt(12)\n\t" \
  "s_lshr_b32 s90, s87, 7\n\t" \
  "s_xor_b32 s90, s90, %[s0]\n\t" \
  "s_and_b32 s90, s90, 1\n\t" \
  "s_lshl1_add_u32 s90, s87, s90\n\t" \
  "s_lshl_b32 s90, s90, 13\n\t" \
  "v_add_f32 v160, v160, v144\n\t" \
  "v_add_f32 v161, v161, v145\n\t" \
  "v_add_f32 v162, v162, v146\n\t" \
  "v_add_f32 v163, v163, v147\n\t" \
  "v_add_f32 v164, v164, v148\n\t" \
  "v_add_f32 v165, v165, v149\n\t" \
  "v_add_f32 v166, v166, v150\n\t" \
  "v_add_f32 v167, v167, v151\n\t" \
  "v_cvt_pk_bf16_f32 v168, v160, v161\n\t" \
  "v_cvt_pk_bf16_f32 v169, v162, v163\n\t" \
  "v_cvt_pk_bf16_f32 v170, v164, v165\n\t" \
  "v_cvt_pk_bf16_f32 v171, v166, v167\n\t" \
  "buffer_store_dwordx4 v[168:171], v172, %[rt], s90 offen sc1\n\t" \
  "s_add_u32 s86, s86, 1\n\t" \
  "s_add_u32 s87, s87, 1\n\t" \
  "s_waitcnt lgkmcnt(0)\n\t" \
  "s_setprio 1\n\t" \
  "v_mfma_f32_16x16x32_bf16 v[128:131], v[176:179], v[64:67], v[128:131]\n\t" \
  "v_mfma_f32_16x16x32_bf16 v[136:139], v[176:179], v[68:71], v[136:139]\n\t" \
  "v_mfma_f32_16x16x32_bf16 v[132:135], v[180:183], v[64:67], v[132:135]\n\t" \
  "v_mfma_f32_16x16x32_bf16 v[140:143], v[180:183], v[68:71], v[140:143]\n\t" \
  "v_mfma_f32_16x16x32_bf16 v[128:131], v[184:187], v[72:75], v[128:131]\n\t" \
  "v_mfma_f32_16x16x32_bf16 v[136:139], v[184:187], v[76:79], v[136:139]\n\t" \
  "v_mfma_f32_16x16x32_bf16 v[132:135], v[188:191], v[72:75], v[132:135]\n\t" \
  "v_mfma_f32_16x16x32_bf16 v[140:143], v[188:191], v[76:79], v[140:143]\n\t" \
  "v_mfma_f32_16x16x32_bf16 v[128:131], v[192:195], v[80:83], v[128:131]\n\t" \
  "v_mfma_f32_16x16x32_bf16 v[136:139], v[192:195], v[84:87], v[136:139]\n\t" \
  "v_mfma_f32_16x16x32_bf16 v[132:135], v[196:199], v[80:83], v[132:135]\n\t" \
  "v_mfma_f32_16x16x32_bf16 v[140:143], v[196:199], v[84:87], v[140:143]\n\t" \
  "v_mfma_f32_16x16x32_bf16 v[128:131], v[200:203], v[88:91], v[128:131]\n\t" \
  "v_mfma_f32_16x16x32_bf16 v[136:139], v[200:203], v[92:95], v[136:139]\n\t" \
  "v_mfma_f32_16x16x32_bf16 v[132:135], v[204:207], v[88:91], v[132:135]\n\t" \
  "v_mfma_f32_16x16x32_bf16 v[140:143], v[204:207], v[92:95], v[140:143]\n\t" \
  "v_mfma_f32_16x16x32_bf16 v[128:131], v[208:211], v[96:99], v[128:131]\n\t" \
  "v_mfma_f32_16x16x32_bf16 v[136:139], v[208:211], v[100:103], v[136:139]\n\t" \
  "v_mfma_f32_16x16x32_bf16 v[132:135], v[212:215], v[96:99], v[132:135]\n\t" \
  "v_mfma_f32_16x16x32_bf16 v[140:143], v[212:215], v[100:103], v[140:143]\n\t" \
  "v_mfma_f32_16x16x32_bf16 v[128:131], v[216:219], v[104:107], v[128:131]\n\t" \
  "v_mfma_f32_16x16x32_bf16 v[136:139], v[216:219], v[108:111], v[136:139]\n\t" \
  "v_mfma_f32_16x16x32_bf16 v[132:135], v[220:223], v[104:107], v[132:135]\n\t" \
  "v_mfma_f32_16x16x32_bf16 v[140:143], v[220:223], v[108:111], v[140:143]\n\t" \
  "v_mfma_f32_16x16x32_bf16 v[128:131], v[224:227], v[112:115], v[128:131]\n\t" \
  "v_mfma_f32_16x16x32_bf16 v[136:139], v[224:227], v[116:119], v[136:139]\n\t" \
  "v_mfma_f32_16x16x32_bf16 v[132:135], v[228:231], v[112:115], v[132:135]\n\t" \
  "v_mfma_f32_16x16x32_bf16 v[140:143], v[228:231], v[116:119], v[140:143]\n\t" \
  "v_mfma_f32_16x16x32_bf16 v[128:131], v[232:235], v[120:123], v[128:131]\n\t" \
  "v_mfma_f32_16x16x32_bf16 v[136:139], v[232:235], v[124:127], v[136:139]\n\t" \
  "v_mfma_f32_16x16x32_bf16 v[132:135], v[236:239], v[120:123], v[132:135]\n\t" \
  "v_mfma_f32_16x16x32_bf16 v[140:143], v[236:239], v[124:127], v[140:143]\n\t" \
  "s_setprio 0\n\t" \
  "s_waitcnt vmcnt(9)\n\t" \
  "s_barrier\n\t" \
  "s_add_u32 s92, s92, 512\n\t" \
  "s_addc_u32 s93, s93, 0\n\t" \
  "ds_read_b128 v[176:179], v244 offset:0\n\t" \
  "ds_read_b128 v[180:183], v244 offset:16384\n\t" \
  "ds_read_b128 v[184:187], v245 offset:0\n\t" \
  "ds_read_b128 v[188:191], v245 offset:16384\n\t" \
  "s_add_u32 m0, %[lw], 32768\n\t" \
  "s_nop 0\n\t" \
  "global_load_lds_dwordx4 v248, s[92:93] sc1\n\t" \
  "ds_read_b128 v[192:195], v246 offset:0\n\t" \
  "ds_read_b128 v[196:199], v246 offset:16384\n\t" \
  "ds_read_b128 v[200:203], v247 offset:0\n\t" \
  "ds_read_b128 v[204:207], v247 offset:16384\n\t" \
  "s_add_u32 m0, %[lw], 33792\n\t" \
  "s_nop 0\n\t" \
  "global_load_lds_dwordx4 v249, s[92:93] sc1\n\t" \
  "ds_read_b128 v[208:211], v244 offset:256\n\t" \
  "ds_read_b128 v[212:215], v244 offset:16640\n\t" \
  "ds_read_b128 v[216:219], v245 offset:256\n\t" \
  "ds_read_b128 v[220:223], v245 offset:16640\n\t" \
  "s_add_u32 m0, %[lw], 34816\n\t" \
  "s_nop 0\n\t" \
  "global_load_lds_dwordx4 v250, s[92:93] sc1\n\t" \
  "ds_read_b128 v[224:227], v246 offset:256\n\t" \
  "ds_read_b128 v[228:231], v246 offset:16640\n\t" \
  "ds_read_b128 v[232:235], v247 offset:256\n\t" \
  "ds_read_b128 v[236:239], v247 offset:16640\n\t" \
  "s_add_u32 m0, %[lw], 35840\n\t" \
  "s_nop 0\n\t" \
  "global_load_lds_dwordx4 v251, s[92:93] sc1\n\t" \
  "ds_write_b128 v252, v[136:139] offset:0\n\t" \
  "ds_write_b128 v252, v[140:143] offset:1024\n\t" \
  "s_waitcnt lgkmcnt(0)\n\t" \
  "s_setprio 1\n\t" \
  "v_mfma_f32_16x16x32_bf16 v[144:147], v[176:179], v[0:3], 0\n\t" \
  "v_mfma_f32_16x16x32_bf16 v[152:155], v[176:179], v[4:7], 0\n\t" \
  "v_mfma_f32_16x16x32_bf16 v[148:151], v[180:183], v[0:3], 0\n\t" \
  "v_mfma_f32_16x16x32_bf16 v[156:159], v[180:183], v[4:7], 0\n\t" \
  "v_mfma_f32_16x16x32_bf16 v[144:147], v[184:187], v[8:11], v[144:147]\n\t" \
  "v_mfma_f32_16x16x32_bf16 v[152:155], v[184:187], v[12:15], v[152:155]\n\t" \
  "v_mfma_f32_16x16x32_bf16 v[148:151], v[188:191], v[8:11], v[148:151]\n\t" \
  "v_mfma_f32_16x16x32_bf16 v[156:159], v[188:191], v[12:15], v[156:159]\n\t" \
  "v_mfma_f32_16x16x32_bf16 v[144:147], v[192:195], v[16:19], v[144:147]\n\t" \
  "v_mfma_f32_16x16x32_bf16 v[152:155], v[192:195], v[20:23], v[152:155]\n\t" \
  "v_mfma_f32_16x16x32_bf16 v[148:151], v[196:199], v[16:19], v[148:151]\n\t" \
  "v_mfma_f32_16x16x32_bf16 v[156:159], v[196:199], v[20:23], v[156:159]\n\t" \
  "v_mfma_f32_16x16x32_bf16 v[144:147], v[200:203], v[24:27], v[144:147]\n\t" \
  "v_mfma_f32_16x16x32_bf16 v[152:155], v[200:203], v[28:31], v[152:155]\n\t" \
  "v_mfma_f32_16x16x32_bf16 v[148:151], v[204:207], v[24:27], v[148:151]\n\t" \
  "v_mfma_f32_16x16x32_bf16 v[156:159], v[204:207], v[28:31], v[156:159]\n\t" \
  "v_mfma_f32_16x16x32_bf16 v[144:147], v[208:211], v[32:35], v[144:147]\n\t" \
  "v_mfma_f32_16x16x32_bf16 v[152:155], v[208:211], v[36:39], v[152:155]\n\t" \
  "v_mfma_f32_16x16x32_bf16 v[148:151], v[212:215], v[32:35], v[148:151]\n\t" \
  "v_mfma_f32_16x16x32_bf16 v[156:159], v[212:215], v[36:39], v[156:159]\n\t" \
  "v_mfma_f32_16x16x32_bf16 v[144:147], v[216:219], v[40:43], v[144:147]\n\t" \
  "v_mfma_f32_16x16x32_bf16 v[152:155], v[216:219], v[44:47], v[152:155]\n\t" \
  "v_mfma_f32_16x16x32_bf16 v[148:151], v[220:223], v[40:43], v[148:151]\n\t" \
  "v_mfma_f32_16x16x32_bf16 v[156:159], v[220:223], v[44:47], v[156:159]\n\t" \
  "v_mfma_f32_16x16x32_bf16 v[144:147], v[224:227], v[48:51], v[144:147]\n\t" \
  "v_mfma_f32_16x16x32_bf16 v[152:155], v[224:227], v[52:55], v[152:155]\n\t" \
  "v_mfma_f32_16x16x32_bf16 v[148:151], v[228:231], v[48:51], v[148:151]\n\t" \
  "v_mfma_f32_16x16x32_bf16 v[156:159], v[228:231], v[52:55], v[156:159]\n\t" \
  "v_mfma_f32_16x16x32_bf16 v[144:147], v[232:235], v[56:59], v[144:147]\n\t" \
  "v_mfma_f32_16x16x32_bf16 v[152:155], v[232:235], v[60:63], v[152:155]\n\t" \
  "v_mfma_f32_16x16x32_bf16 v[148:151], v[236:239], v[56:59], v[148:151]\n\t" \
  "v_mfma_f32_16x16x32_bf16 v[156:159], v[236:239], v[60:63], v[156:159]\n\t" \
  "s_setprio 0\n\t" \
  "s_waitcnt vmcnt(4)\n\t" \
  "s_barrier\n\t" \
  "s_add_u32 s92, s92, 65024\n\t" \
  "s_addc_u32 s93, s93, 0\n\t" \
  "ds_read_b128 v[160:163], v253 offset:0\n\t" \
  "ds_read_b128 v[164:167], v253 offset:1024\n\t" \
  "ds_read_b128 v[176:179], v244 offset:32768\n\t" \
  "ds_read_b128 v[180:183], v244 offset:49152\n\t" \
  "ds_read_b128 v[184:187], v245 offset:32768\n\t" \
  "ds_read_b128 v[188:191], v245 offset:49152\n\t" \
  "s_add_u32 m0, %[lw], 65536\n\t" \
  "s_nop 0\n\t" \
  "global_load_lds_dwordx4 v248, s[92:93] sc1\n\t" \
  "ds_read_b128 v[192:195], v246 offset:32768\n\t" \
  "ds_read_b128 v[196:199], v246 offset:49152\n\t" \
  "ds_read_b128 v[200:203], v247 offset:32768\n\t" \
  "ds_read_b128 v[204:207], v247 offset:49152\n\t" \
  "s_add_u32 m0, %[lw], 66560\n\t" \
  "s_nop 0\n\t" \
  "global_load_lds_dwordx4 v249, s[92:93] sc1\n\t" \
  "ds_read_b128 v[208:211], v244 offset:33024\n\t" \
  "ds_read_b128 v[212:215], v244 offset:49408\n\t" \
  "ds_read_b128 v[216:219], v245 offset:33024\n\t" \
  "ds_read_b128 v[220:223], v245 offset:49408\n\t" \
  "s_add_u32 m0, %[lw], 67584\n\t" \
  "s_nop 0\n\t" \
  "global_load_lds_dwordx4 v250, s[92:93] sc1\n\t" \
  "ds_read_b128 v[224:227], v246 offset:33024\n\t" \
  "ds_read_b128 v[228:231], v246 offset:49408\n\t" \
  "ds_read_b128 v[232:235], v247 offset:33024\n\t" \
  "ds_read_b128 v[236:239], v247 offset:49408\n\t" \
  "s_add_u32 m0, %[lw], 68608\n\t" \
  "s_nop 0\n\t" \
  "global_load_lds_dwordx4 v251, s[92:93] sc1\n\t" \
  "s_waitcnt lgkmcnt(15)\n\t" \
  "s_waitcnt lgkmcnt(12)\n\t" \
  "s_lshr_b32 s90, s87, 7\n\t" \
  "s_xor_b32 s90, s90, %[s0]\n\t" \
  "s_and_b32 s90, s90, 1\n\t" \
  "s_lshl1_add_u32 s90, s87, s90\n\t" \
  "s_lshl_b32 s90, s90, 13\n\t" \
  "v_add_f32 v160, v160, v128\n\t" \
  "v_add_f32 v161, v161, v129\n\t" \
  "v_add_f32 v162, v162, v130\n\t" \
  "v_add_f32 v163, v163, v131\n\t" \
  "v_add_f32 v164, v164, v132\n\t" \
  "v_add_f32 v165, v165, v133\n\t" \
  "v_add_f32 v166, v166, v134\n\t" \
  "v_add_f32 v167, v167, v135\n\t" \
  "v_cvt_pk_bf16_f32 v168, v160, v161\n\t" \
  "v_cvt_pk_bf16_f32 v169, v162, v163\n\t" \
  "v_cvt_pk_bf16_f32 v170, v164, v165\n\t" \
  "v_cvt_pk_bf16_f32 v171, v166, v167\n\t" \
  "buffer_store_dwordx4 v[168:171], v172, %[rt], s90 offen sc1\n\t" \
  "s_add_u32 s86, s86, 1\n\t" \
  "s_add_u32 s87, s87, 1\n\t" \
  "s_waitcnt lgkmcnt(0)\n\t" \
  "s_setprio 1\n\t" \
  "v_mfma_f32_16x16x32_bf16 v[144:147], v[176:179], v[64:67], v[144:147]\n\t" \
  "v_mfma_f32_16x16x32_bf16 v[152:155], v[176:179], v[68:71], v[152:155]\n\t" \
  "v_mfma_f32_16x16x32_bf16 v[148:151], v[180:183], v[64:67], v[148:151]\n\t" \
  "v_mfma_f32_16x16x32_bf16 v[156:159], v[180:183], v[68:71], v[156:159]\n\t" \
  "v_mfma_f32_16x16x32_bf16 v[144:147], v[184:187], v[72:75], v[144:147]\n\t" \
  "v_mfma_f32_16x16x32_bf16 v[152:155], v[184:187], v[76:79], v[152:155]\n\t" \
  "v_mfma_f32_16x16x32_bf16 v[148:151], v[188:191], v[72:75], v[148:151]\n\t" \
  "v_mfma_f32_16x16x32_bf16 v[156:159], v[188:191], v[76:79], v[156:159]\n\t" \
  "v_mfma_f32_16x16x32_bf16 v[144:147], v[192:195], v[80:83], v[144:147]\n\t" \
  "v_mfma_f32_16x16x32_bf16 v[152:155], v[192:195], v[84:87], v[152:155]\n\t" \
  "v_mfma_f32_16x16x32_bf16 v[148:151], v[196:199], v[80:83], v[148:151]\n\t" \
  "v_mfma_f32_16x16x32_bf16 v[156:159], v[196:199], v[84:87], v[156:159]\n\t" \
  "v_mfma_f32_16x16x32_bf16 v[144:147], v[200:203], v[88:91], v[144:147]\n\t" \
  "v_mfma_f32_16x16x32_bf16 v[152:155], v[200:203], v[92:95], v[152:155]\n\t" \
  "v_mfma_f32_16x16x32_bf16 v[148:151], v[204:207], v[88:91], v[148:151]\n\t" \
  "v_mfma_f32_16x16x32_bf16 v[156:159], v[204:207], v[92:95], v[156:159]\n\t" \
  "v_mfma_f32_16x16x32_bf16 v[144:147], v[208:211], v[96:99], v[144:147]\n\t" \
  "v_mfma_f32_16x16x32_bf16 v[152:155], v[208:211], v[100:103], v[152:155]\n\t" \
  "v_mfma_f32_16x16x32_bf16 v[148:151], v[212:215], v[96:99], v[148:151]\n\t" \
  "v_mfma_f32_16x16x32_bf16 v[156:159], v[212:215], v[100:103], v[156:159]\n\t" \
  "v_mfma_f32_16x16x32_bf16 v[144:147], v[216:219], v[104:107], v[144:147]\n\t" \
  "v_mfma_f32_16x16x32_bf16 v[152:155], v[216:219], v[108:111], v[152:155]\n\t" \
  "v_mfma_f32_16x16x32_bf16 v[148:151], v[220:223], v[104:107], v[148:151]\n\t" \
  "v_mfma_f32_16x16x32_bf16 v[156:159], v[220:223], v[108:111], v[156:159]\n\t" \
  "v_mfma_f32_16x16x32_bf16 v[144:147], v[224:227], v[112:115], v[144:147]\n\t" \
  "v_mfma_f32_16x16x32_bf16 v[152:155], v[224:227], v[116:119], v[152:155]\n\t" \
  "v_mfma_f32_16x16x32_bf16 v[148:151], v[228:231], v[112:115], v[148:151]\n\t" \
  "v_mfma_f32_16x16x32_bf16 v[156:159], v[228:231], v[116:119], v[156:159]\n\t" \
  "v_mfma_f32_16x16x32_bf16 v[144:147], v[232:235], v[120:123], v[144:147]\n\t" \
  "v_mfma_f32_16x16x32_bf16 v[152:155], v[232:235], v[124:127], v[152:155]\n\t" \
  "v_mfma_f32_16x16x32_bf16 v[148:151], v[236:239], v[120:123], v[148:151]\n\t" \
  "v_mfma_f32_16x16x32_bf16 v[156:159], v[236:239], v[124:127], v[156:159]\n\t" \
  "s_setprio 0\n\t" \
  "s_cmp_lt_u32 s86, 64\n\t" \
  "s_cbranch_scc1 .Lp0%=\n\t" \
  "s_barrier\n\t" \
  "s_nop 1\n\t" \
  "ds_write_b128 v252, v[152:155] offset:0\n\t" \
  "s_nop 0\n\t" \
  "ds_write_b128 v252, v[156:159] offset:1024\n\t" \
  "s_waitcnt lgkmcnt(0)\n\t" \
  "s_barrier\n\t" \
  "ds_read_b128 v[160:163], v253 offset:0\n\t" \
  "ds_read_b128 v[164:167], v253 offset:1024\n\t" \
  "s_waitcnt lgkmcnt(0)\n\t" \
  "s_lshr_b32 s90, s87, 7\n\t" \
  "s_xor_b32 s90, s90, %[s0]\n\t" \
  "s_and_b32 s90, s90, 1\n\t" \
  "s_lshl1_add_u32 s90, s87, s90\n\t" \
  "s_lshl_b32 s90, s90, 13\n\t" \
  "v_add_f32 v160, v160, v144\n\t" \
  "v_add_f32 v161, v161, v145\n\t" \
  "v_add_f32 v162, v162, v146\n\t" \
  "v_add_f32 v163, v163, v147\n\t" \
  "v_add_f32 v164, v164, v148\n\t" \
  "v_add_f32 v165, v165, v149\n\t" \
  "v_add_f32 v166, v166, v150\n\t" \
  "v_add_f32 v167, v167, v151\n\t" \
  "v_cvt_pk_bf16_f32 v168, v160, v161\n\t" \
  "v_cvt_pk_bf16_f32 v169, v162, v163\n\t" \
  "v_cvt_pk_bf16_f32 v170, v164, v165\n\t" \
  "v_cvt_pk_bf16_f32 v171, v166, v167\n\t" \
  "buffer_store_dwordx4 v[168:171], v172, %[rt], s90 offen sc1\n\t" \
  "s_waitcnt vmcnt(0)\n\t" \
  "s_barrier\n\t" \
  "s_mov_b32 m0, s94\n\t" \
  "s_branch .Le%=\n\t" \
  ".Lk%=:\n\t" \
  "s_mov_b32 s94, m0\n\t" \
  "s_mov_b64 s[92:93], %[x0]\n\t" \
  "s_mov_b32 s86, %[g0]\n\t" \
  "s_add_u32 s87, s86, %[t0]\n\t" \
  "s_add_u32 m0, %[lw], 67584\n\t" \
  "s_nop 0\n\t" \
  "global_load_lds_dwordx4 v250, s[92:93] sc1\n\t" \
  "s_add_u32 m0, %[lw], 68608\n\t" \
  "s_nop 0\n\t" \
  "global_load_lds_dwordx4 v251, s[92:93] sc1\n\t" \
  "s_barrier\n\t" \
  "s_branch .Ls1%=\n\t" \
  ".Lp1%=:\n\t" \
  "s_waitcnt vmcnt(10)\n\t" \
  "s_barrier\n\t" \
  "s_setprio 2\n\t" \
  "v_mfma_f32_16x16x32_bf16 v[144:147], v[176:179], v[64:67], v[144:147]\n\t" \
  "v_mfma_f32_16x16x32_bf16 v[152:155], v[176:179], v[68:71], v[152:155]\n\t" \
  "v_mfma_f32_16x16x32_bf16 v[148:151], v[180:183], v[64:67], v[148:151]\n\t" \
  "v_mfma_f32_16x16x32_bf16 v[156:159], v[180:183], v[68:71], v[156:159]\n\t" \
  "v_mfma_f32_16x16x32_bf16 v[144:147], v[184:187], v[72:75], v[144:147]\n\t" \
  "v_mfma_f32_16x16x32_bf16 v[152:155], v[184:187], v[76:79], v[152:155]\n\t" \
  "v_mfma_f32_16x16x32_bf16 v[148:151], v[188:191], v[72:75], v[148:151]\n\t" \
  "v_mfma_f32_16x16x32_bf16 v[156:159], v[188:191], v[76:79], v[156:159]\n\t" \
  "v_mfma_f32_16x16x32_bf16 v[144:147], v[192:195], v[80:83], v[144:147]\n\t" \
  "v_mfma_f32_16x16x32_bf16 v[152:155], v[192:195], v[84:87], v[152:155]\n\t" \
  "v_mfma_f32_16x16x32_bf16 v[148:151], v[196:199], v[80:83], v[148:151]\n\t" \
  "v_mfma_f32_16x16x32_bf16 v[156:159], v[196:199], v[84:87], v[156:159]\n\t" \
  "v_mfma_f32_16x16x32_bf16 v[144:147], v[200:203], v[88:91], v[144:147]\n\t" \
  "v_mfma_f32_16x16x32_bf16 v[152:155], v[200:203], v[92:95], v[152:155]\n\t" \
  "v_mfma_f32_16x16x32_bf16 v[148:151], v[204:207], v[88:91], v[148:151]\n\t" \
  "v_mfma_f32_16x16x32_bf16 v[156:159], v[204:207], v[92:95], v[156:159]\n\t" \
  "v_mfma_f32_16x16x32_bf16 v[144:147], v[208:211], v[96:99], v[144:147]\n\t" \
  "v_mfma_f32_16x16x32_bf16 v[152:155], v[208:211], v[100:103], v[152:155]\n\t" \
  "v_mfma_f32_16x16x32_bf16 v[148:151], v[212:215], v[96:99], v[148:151]\n\t" \
  "v_mfma_f32_16x16x32_bf16 v[156:159], v[212:215], v[100:103], v[156:159]\n\t" \
  "v_mfma_f32_16x16x32_bf16 v[144:147], v[216:219], v[104:107], v[144:147]\n\t" \
  "v_mfma_f32_16x16x32_bf16 v[152:155], v[216:219], v[108:111], v[152:155]\n\t" \
  "v_mfma_f32_16x16x32_bf16 v[148:151], v[220:223], v[104:107], v[148:151]\n\t" \
  "v_mfma_f32_16x16x32_bf16 v[156:159], v[220:223], v[108:111], v[156:159]\n\t" \
  "v_mfma_f32_16x16x32_bf16 v[144:147], v[224:227], v[112:115], v[144:147]\n\t" \
  "v_mfma_f32_16x16x32_bf16 v[152:155], v[224:227], v[116:119], v[152:155]\n\t" \
  "v_mfma_f32_16x16x32_bf16 v[148:151], v[228:231], v[112:115], v[148:151]\n\t" \
  "v_mfma_f32_16x16x32_bf16 v[156:159], v[228:231], v[116:119], v[156:159]\n\t" \
  "v_mfma_f32_16x16x32_bf16 v[144:147], v[232:235], v[120:123], v[144:147]\n\t" \
  "v_mfma_f32_16x16x32_bf16 v[152:155], v[232:235], v[124:127], v[152:155]\n\t" \
  "v_mfma_f32_16x16x32_bf16 v[148:151], v[236:239], v[120:123], v[148:151]\n\t" \
  "v_mfma_f32_16x16x32_bf16 v[156:159], v[236:239], v[124:127], v[156:159]\n\t" \
  "s_setprio 0\n\t" \
  ".Ls1%=:\n\t" \
  "s_add_u32 s92, s92, 512\n\t" \
  "s_addc_u32 s93, s93, 0\n\t" \
  "ds_read_b128 v[176:179], v240 offset:0\n\t" \
  "ds_read_b128 v[180:183], v240 offset:16384\n\t" \
  "ds_read_b128 v[184:187], v241 offset:0\n\t" \
  "ds_read_b128 v[188:191], v241 offset:16384\n\t" \
  "s_add_u32 m0, %[lw], 98304\n\t" \
  "s_nop 0\n\t" \
  "global_load_lds_dwordx4 v248, s[92:93] sc1\n\t" \
  "ds_read_b128 v[192:195], v242 offset:0\n\t" \
  "ds_read_b128 v[196:199], v242 offset:16384\n\t" \
  "ds_read_b128 v[200:203], v243 offset:0\n\t" \
  "ds_read_b128 v[204:207], v243 offset:16384\n\t" \
  "s_add_u32 m0, %[lw], 99328\n\t" \
  "s_nop 0\n\t" \
  "global_load_lds_dwordx4 v249, s[92:93] sc1\n\t" \
  "ds_read_b128 v[208:211], v240 offset:256\n\t" \
  "ds_read_b128 v[212:215], v240 offset:16640\n\t" \
  "ds_read_b128 v[216:219], v241 offset:256\n\t" \
  "ds_read_b128 v[220:223], v241 offset:16640\n\t" \
  "s_add_u32 m0, %[lw], 100352\n\t" \
  "s_nop 0\n\t" \
  "global_load_lds_dwordx4 v250, s[92:93] sc1\n\t" \
  "ds_read_b128 v[224:227], v242 offset:256\n\t" \
  "ds_read_b128 v[228:231], v242 offset:16640\n\t" \
  "ds_read_b128 v[232:235], v243 offset:256\n\t" \
  "ds_read_b128 v[236:239], v243 offset:16640\n\t" \
  "s_add_u32 m0, %[lw], 101376\n\t" \
  "s_nop 0\n\t" \
  "global_load_lds_dwordx4 v251, s[92:93] sc1\n\t" \
  "ds_write_b128 v252, v[152:155] offset:0\n\t" \
  "ds_write_b128 v252, v[156:159] offset:1024\n\t" \
  "s_waitcnt lgkmcnt(0)\n\t" \
  "s_waitcnt vmcnt(4)\n\t" \
  "s_barrier\n\t" \
  "s_setprio 2\n\t" \
  "v_mfma_f32_16x16x32_bf16 v[128:131], v[176:179], v[0:3], 0\n\t" \
  "v_mfma_f32_16x16x32_bf16 v[136:139], v[176:179], v[4:7], 0\n\t" \
  "v_mfma_f32_16x16x32_bf16 v[132:135], v[180:183], v[0:3], 0\n\t" \
  "v_mfma_f32_16x16x32_bf16 v[140:143], v[180:183], v[4:7], 0\n\t" \
  "v_mfma_f32_16x16x32_bf16 v[128:131], v[184:187], v[8:11], v[128:131]\n\t" \
  "v_mfma_f32_16x16x32_bf16 v[136:139], v[184:187], v[12:15], v[136:139]\n\t" \
  "v_mfma_f32_16x16x32_bf16 v[132:135], v[188:191], v[8:11], v[132:135]\n\t" \
  "v_mfma_f32_16x16x32_bf16 v[140:143], v[188:191], v[12:15], v[140:143]\n\t" \
  "v_mfma_f32_16x16x32_bf16 v[128:131], v[192:195], v[16:19], v[128:131]\n\t" \
  "v_mfma_f32_16x16x32_bf16 v[136:139], v[192:195], v[20:23], v[136:139]\n\t" \
  "v_mfma_f32_16x16x32_bf16 v[132:135], v[196:199], v[16:19], v[132:135]\n\t" \
  "v_mfma_f32_16x16x32_bf16 v[140:143], v[196:199], v[20:23], v[140:143]\n\t" \
  "v_mfma_f32_16x16x32_bf16 v[128:131], v[200:203], v[24:27], v[128:131]\n\t" \
  "v_mfma_f32_16x16x32_bf16 v[136:139], v[200:203], v[28:31], v[136:139]\n\t" \
  "v_mfma_f32_16x16x32_bf16 v[132:135], v[204:207], v[24:27], v[132:135]\n\t" \
  "v_mfma_f32_16x16x32_bf16 v[140:143], v[204:207], v[28:31], v[140:143]\n\t" \
  "v_mfma_f32_16x16x32_bf16 v[128:131], v[208:211], v[32:35], v[128:131]\n\t" \
  "v_mfma_f32_16x16x32_bf16 v[136:139], v[208:211], v[36:39], v[136:139]\n\t" \
  "v_mfma_f32_16x16x32_bf16 v[132:135], v[212:215], v[32:35], v[132:135]\n\t" \
  "v_mfma_f32_16x16x32_bf16 v[140:143], v[212:215], v[36:39], v[140:143]\n\t" \
  "v_mfma_f32_16x16x32_bf16 v[128:131], v[216:219], v[40:43], v[128:131]\n\t" \
  "v_mfma_f32_16x16x32_bf16 v[136:139], v[216:219], v[44:47], v[136:139]\n\t" \
  "v_mfma_f32_16x16x32_bf16 v[132:135], v[220:223], v[40:43], v[132:135]\n\t" \
  "v_mfma_f32_16x16x32_bf16 v[140:143], v[220:223], v[44:47], v[140:143]\n\t" \
  "v_mfma_f32_16x16x32_bf16 v[128:131], v[224:227], v[48:51], v[128:131]\n\t" \
  "v_mfma_f32_16x16x32_bf16 v[136:139], v[224:227], v[52:55], v[136:139]\n\t" \
  "v_mfma_f32_16x16x32_bf16 v[132:135], v[228:231], v[48:51], v[132:135]\n\t" \
  "v_mfma_f32_16x16x32_bf16 v[140:143], v[228:231], v[52:55], v[140:143]\n\t" \
  "v_mfma_f32_16x16x32_bf16 v[128:131], v[232:235], v[56:59], v[128:131]\n\t" \
  "v_mfma_f32_16x16x32_bf16 v[136:139], v[232:235], v[60:63], v[136:139]\n\t" \
  "v_mfma_f32_16x16x32_bf16 v[132:135], v[236:239], v[56:59], v[132:135]\n\t" \
  "v_mfma_f32_16x16x32_bf16 v[140:143], v[236:239], v[60:63], v[140:143]\n\t" \
  "s_setprio 0\n\t" \
  "s_add_u32 s92, s92, 65024\n\t" \
  "s_addc_u32 s93, s93, 0\n\t" \
  "ds_read_b128 v[160:163], v253 offset:0\n\t" \
  "ds_read_b128 v[164:167], v253 offset:1024\n\t" \
  "ds_read_b128 v[176:179], v240 offset:32768\n\t" \
  "ds_read_b128 v[180:183], v240 offset:49152\n\t" \
  "ds_read_b128 v[184:187], v241 offset:32768\n\t" \
  "ds_read_b128 v[188:191], v241 offset:49152\n\t" \
  "s_add_u32 m0, %[lw], 0\n\t" \
  "s_nop 0\n\t" \
  "global_load_lds_dwordx4 v248, s[92:93] sc1\n\t" \
  "ds_read_b128 v[192:195], v242 offset:32768\n\t" \
  "ds_read_b128 v[196:199], v242 offset:49152\n\t" \
  "ds_read_b128 v[200:203], v243 offset:32768\n\t" \
  "ds_read_b128 v[204:207], v243 offset:49152\n\t" \
  "s_add_u32 m0, %[lw], 1024\n\t" \
  "s_nop 0\n\t" \
  "global_load_lds_dwordx4 v249, s[92:93] sc1\n\t" \
  "ds_read_b128 v[208:211], v240 offset:33024\n\t" \
  "ds_read_b128 v[212:215], v240 offset:49408\n\t" \
  "ds_read_b128 v[216:219], v241 offset:33024\n\t" \
  "ds_read_b128 v[220:223], v241 offset:49408\n\t" \
  "s_add_u32 m0, %[lw], 2048\n\t" \
  "s_nop 0\n\t" \
  "global_load_lds_dwordx4 v250, s[92:93] sc1\n\t" \
  "ds_read_b128 v[224:227], v242 offset:33024\n\t" \
  "ds_read_b128 v[228:231], v242 offset:49408\n\t" \
  "ds_read_b128 v[232:235], v243 offset:33024\n\t" \
  "ds_read_b128 v[236:239], v243 offset:49408\n\t" \
  "s_add_u32 m0, %[lw], 3072\n\t" \
  "s_nop 0\n\t" \
  "global_load_lds_dwordx4 v251, s[92:93] sc1\n\t" \
  "s_waitcnt lgkmcnt(15)\n\t" \
  "s_waitcnt lgkmcnt(12)\n\t" \
  "s_sub_i32 s91, s86, 2\n\t" \
  "v_mov_b32 v173, s91\n\t" \
  "buffer_store_dword v173, v174, %[rs], 0 offen sc1\n\t" \
  "s_lshr_b32 s90, s87, 7\n\t" \
  "s_xor_b32 s90, s90, %[s0]\n\t" \
  "s_and_b32 s90, s90, 1\n\t" \
  "s_lshl1_add_u32 s90, s87, s90\n\t" \
  "s_lshl_b32 s90, s90, 13\n\t" \
  "v_add_f32 v160, v160, v144\n\t" \
  "v_add_f32 v161, v161, v145\n\t" \
  "v_add_f32 v162, v162, v146\n\t" \
  "v_add_f32 v163, v163, v147\n\t" \
  "v_add_f32 v164, v164, v148\n\t" \
  "v_add_f32 v165, v165, v149\n\t" \
  "v_add_f32 v166, v166, v150\n\t" \
  "v_add_f32 v167, v167, v151\n\t" \
  "v_cvt_pk_bf16_f32 v168, v160, v161\n\t" \
  "v_cvt_pk_bf16_f32 v169, v162, v163\n\t" \
  "v_cvt_pk_bf16_f32 v170, v164, v165\n\t" \
  "v_cvt_pk_bf16_f32 v171, v166, v167\n\t" \
  "buffer_store_dwordx4 v[168:171], v172, %[rt], s90 offen sc1\n\t" \
  "s_add_u32 s86, s86, 1\n\t" \
  "s_add_u32 s87, s87, 1\n\t" \
  "s_waitcnt lgkmcnt(0)\n\t" \
  "s_waitcnt vmcnt(10)\n\t" \
  "s_barrier\n\t" \
  "s_setprio 2\n\t" \
  "v_mfma_f32_16x16x32_bf16 v[128:131], v[176:179], v[64:67], v[128:131]\n\t" \
  "v_mfma_f32_16x16x32_bf16 v[136:139], v[176:179], v[68:71], v[136:139]\n\t" \
  "v_mfma_f32_16x16x32_bf16 v[132:135], v[180:183], v[64:67], v[132:135]\n\t" \
  "v_mfma_f32_16x16x32_bf16 v[140:143], v[180:183], v[68:71], v[140:143]\n\t" \
  "v_mfma_f32_16x16x32_bf16 v[128:131], v[184:187], v[72:75], v[128:131]\n\t" \
  "v_mfma_f32_16x16x32_bf16 v[136:139], v[184:187], v[76:79], v[136:139]\n\t" \
  "v_mfma_f32_16x16x32_bf16 v[132:135], v[188:191], v[72:75], v[132:135]\n\t" \
  "v_mfma_f32_16x16x32_bf16 v[140:143], v[188:191], v[76:79], v[140:143]\n\t" \
  "v_mfma_f32_16x16x32_bf16 v[128:131], v[192:195], v[80:83], v[128:131]\n\t" \
  "v_mfma_f32_16x16x32_bf16 v[136:139], v[192:195], v[84:87], v[136:139]\n\t" \
  "v_mfma_f32_16x16x32_bf16 v[132:135], v[196:199], v[80:83], v[132:135]\n\t" \
  "v_mfma_f32_16x16x32_bf16 v[140:143], v[196:199], v[84:87], v[140:143]\n\t" \
  "v_mfma_f32_16x16x32_bf16 v[128:131], v[200:203], v[88:91], v[128:131]\n\t" \
  "v_mfma_f32_16x16x32_bf16 v[136:139], v[200:203], v[92:95], v[136:139]\n\t" \
  "v_mfma_f32_16x16x32_bf16 v[132:135], v[204:207], v[88:91], v[132:135]\n\t" \
  "v_mfma_f32_16x16x32_bf16 v[140:143], v[204:207], v[92:95], v[140:143]\n\t" \
  "v_mfma_f32_16x16x32_bf16 v[128:131], v[208:211], v[96:99], v[128:131]\n\t" \
  "v_mfma_f32_16x16x32_bf16 v[136:139], v[208:211], v[100:103], v[136:139]\n\t" \
  "v_mfma_f32_16x16x32_bf16 v[132:135], v[212:215], v[96:99], v[132:135]\n\t" \
  "v_mfma_f32_16x16x32_bf16 v[140:143], v[212:215], v[100:103], v[140:143]\n\t" \
  "v_mfma_f32_16x16x32_bf16 v[128:131], v[216:219], v[104:107], v[128:131]\n\t" \
  "v_mfma_f32_16x16x32_bf16 v[136:139], v[216:219], v[108:111], v[136:139]\n\t" \
  "v_mfma_f32_16x16x32_bf16 v[132:135], v[220:223], v[104:107], v[132:135]\n\t" \
  "v_mfma_f32_16x16x32_bf16 v[140:143], v[220:223], v[108:111], v[140:143]\n\t" \
  "v_mfma_f32_16x16x32_bf16 v[128:131], v[224:227], v[112:115], v[128:131]\n\t" \
  "v_mfma_f32_16x16x32_bf16 v[136:139], v[224:227], v[116:119], v[136:139]\n\t" \
  "v_mfma_f32_16x16x32_bf16 v[132:135], v[228:231], v[112:115], v[132:135]\n\t" \
  "v_mfma_f32_16x16x32_bf16 v[140:143], v[228:231], v[116:119], v[140:143]\n\t" \
  "v_mfma_f32_16x16x32_bf16 v[128:131], v[232:235], v[120:123], v[128:131]\n\t" \
  "v_mfma_f32_16x16x32_bf16 v[136:139], v[232:235], v[124:127], v[136:139]\n\t" \
  "v_mfma_f32_16x16x32_bf16 v[132:135], v[236:239], v[120:123], v[132:135]\n\t" \
  "v_mfma_f32_16x16x32_bf16 v[140:143], v[236:239], v[124:127], v[140:143]\n\t" \
  "s_setprio 0\n\t" \
  "s_add_u32 s92, s92, 512\n\t" \
  "s_addc_u32 s93, s93, 0\n\t" \
  "ds_read_b128 v[176:179], v244 offset:0\n\t" \
  "ds_read_b128 v[180:183], v244 offset:16384\n\t" \
  "ds_read_b128 v[184:187], v245 offset:0\n\t" \
  "ds_read_b128 v[188:191], v245 offset:16384\n\t" \
  "s_add_u32 m0, %[lw], 32768\n\t" \
  "s_nop 0\n\t" \
  "global_load_lds_dwordx4 v248, s[92:93] sc1\n\t" \
  "ds_read_b128 v[192:195], v246 offset:0\n\t" \
  "ds_read_b128 v[196:199], v246 offset:16384\n\t" \
  "ds_read_b128 v[200:203], v247 offset:0\n\t" \
  "ds_read_b128 v[204:207], v247 offset:16384\n\t" \
  "s_add_u32 m0, %[lw], 33792\n\t" \
  "s_nop 0\n\t" \
  "global_load_lds_dwordx4 v249, s[92:93] sc1\n\t" \
  "ds_read_b128 v[208:211], v244 offset:256\n\t" \
  "ds_read_b128 v[212:215], v244 offset:16640\n\t" \
  "ds_read_b128 v[216:219], v245 offset:256\n\t" \
  "ds_read_b128 v[220:223], v245 offset:16640\n\t" \
  "s_add_u32 m0, %[lw], 34816\n\t" \
  "s_nop 0\n\t" \
  "global_load_lds_dwordx4 v250, s[92:93] sc1\n\t" \
  "ds_read_b128 v[224:227], v246 offset:256\n\t" \
  "ds_read_b128 v[228:231], v246 offset:16640\n\t" \
  "ds_read_b128 v[232:235], v247 offset:256\n\t" \
  "ds_read_b128 v[236:239], v247 offset:16640\n\t" \
  "s_add_u32 m0, %[lw], 35840\n\t" \
  "s_nop 0\n\t" \
  "global_load_lds_dwordx4 v251, s[92:93] sc1\n\t" \
  "ds_write_b128 v252, v[136:139] offset:0\n\t" \
  "ds_write_b128 v252, v[140:143] offset:1024\n\t" \
  "s_waitcnt lgkmcnt(0)\n\t" \
  "s_waitcnt vmcnt(4)\n\t" \
  "s_barrier\n\t" \
  "s_setprio 2\n\t" \
  "v_mfma_f32_16x16x32_bf16 v[144:147], v[176:179], v[0:3], 0\n\t" \
  "v_mfma_f32_16x16x32_bf16 v[152:155], v[176:179], v[4:7], 0\n\t" \
  "v_mfma_f32_16x16x32_bf16 v[148:151], v[180:183], v[0:3], 0\n\t" \
  "v_mfma_f32_16x16x32_bf16 v[156:159], v[180:183], v[4:7], 0\n\t" \
  "v_mfma_f32_16x16x32_bf16 v[144:147], v[184:187], v[8:11], v[144:147]\n\t" \
  "v_mfma_f32_16x16x32_bf16 v[152:155], v[184:187], v[12:15], v[152:155]\n\t" \
  "v_mfma_f32_16x16x32_bf16 v[148:151], v[188:191], v[8:11], v[148:151]\n\t" \
  "v_mfma_f32_16x16x32_bf16 v[156:159], v[188:191], v[12:15], v[156:159]\n\t" \
  "v_mfma_f32_16x16x32_bf16 v[144:147], v[192:195], v[16:19], v[144:147]\n\t" \
  "v_mfma_f32_16x16x32_bf16 v[152:155], v[192:195], v[20:23], v[152:155]\n\t" \
  "v_mfma_f32_16x16x32_bf16 v[148:151], v[196:199], v[16:19], v[148:151]\n\t" \
  "v_mfma_f32_16x16x32_bf16 v[156:159], v[196:199], v[20:23], v[156:159]\n\t" \
  "v_mfma_f32_16x16x32_bf16 v[144:147], v[200:203], v[24:27], v[144:147]\n\t" \
  "v_mfma_f32_16x16x32_bf16 v[152:155], v[200:203], v[28:31], v[152:155]\n\t" \
  "v_mfma_f32_16x16x32_bf16 v[148:151], v[204:207], v[24:27], v[148:151]\n\t" \
  "v_mfma_f32_16x16x32_bf16 v[156:159], v[204:207], v[28:31], v[156:159]\n\t" \
  "v_mfma_f32_16x16x32_bf16 v[144:147], v[208:211], v[32:35], v[144:147]\n\t" \
  "v_mfma_f32_16x16x32_bf16 v[152:155], v[208:211], v[36:39], v[152:155]\n\t" \
  "v_mfma_f32_16x16x32_bf16 v[148:151], v[212:215], v[32:35], v[148:151]\n\t" \
  "v_mfma_f32_16x16x32_bf16 v[156:159], v[212:215], v[36:39], v[156:159]\n\t" \
  "v_mfma_f32_16x16x32_bf16 v[144:147], v[216:219], v[40:43], v[144:147]\n\t" \
  "v_mfma_f32_16x16x32_bf16 v[152:155], v[216:219], v[44:47], v[152:155]\n\t" \
  "v_mfma_f32_16x16x32_bf16 v[148:151], v[220:223], v[40:43], v[148:151]\n\t" \
  "v_mfma_f32_16x16x32_bf16 v[156:159], v[220:223], v[44:47], v[156:159]\n\t" \
  "v_mfma_f32_16x16x32_bf16 v[144:147], v[224:227], v[48:51], v[144:147]\n\t" \
  "v_mfma_f32_16x16x32_bf16 v[152:155], v[224:227], v[52:55], v[152:155]\n\t" \
  "v_mfma_f32_16x16x32_bf16 v[148:151], v[228:231], v[48:51], v[148:151]\n\t" \
  "v_mfma_f32_16x16x32_bf16 v[156:159], v[228:231], v[52:55], v[156:159]\n\t" \
  "v_mfma_f32_16x16x32_bf16 v[144:147], v[232:235], v[56:59], v[144:147]\n\t" \
  "v_mfma_f32_16x16x32_bf16 v[152:155], v[232:235], v[60:63], v[152:155]\n\t" \
  "v_mfma_f32_16x16x32_bf16 v[148:151], v[236:239], v[56:59], v[148:151]\n\t" \
  "v_mfma_f32_16x16x32_bf16 v[156:159], v[236:239], v[60:63], v[156:159]\n\t" \
  "s_setprio 0\n\t" \
  "s_add_u32 s92, s92, 65024\n\t" \
  "s_addc_u32 s93, s93, 0\n\t" \
  "ds_read_b128 v[160:163], v253 offset:0\n\t" \
  "ds_read_b128 v[164:167], v253 offset:1024\n\t" \
  "ds_read_b128 v[176:179], v244 offset:32768\n\t" \
  "ds_read_b128 v[180:183], v244 offset:49152\n\t" \
  "ds_read_b128 v[184:187], v245 offset:32768\n\t" \
  "ds_read_b128 v[188:191], v245 offset:49152\n\t" \
  "s_add_u32 m0, %[lw], 65536\n\t" \
  "s_nop 0\n\t" \
  "global_load_lds_dwordx4 v248, s[92:93] sc1\n\t" \
  "ds_read_b128 v[192:195], v246 offset:32768\n\t" \
  "ds_read_b128 v[196:199], v246 offset:49152\n\t" \
  "ds_read_b128 v[200:203], v247 offset:32768\n\t" \
  "ds_read_b128 v[204:207], v247 offset:49152\n\t" \
  "s_add_u32 m0, %[lw], 66560\n\t" \
  "s_nop 0\n\t" \
  "global_load_lds_dwordx4 v249, s[92:93] sc1\n\t" \
  "ds_read_b128 v[208:211], v244 offset:33024\n\t" \
  "ds_read_b128 v[212:215], v244 offset:49408\n\t" \
  "ds_read_b128 v[216:219], v245 offset:33024\n\t" \
  "ds_read_b128 v[220:223], v245 offset:49408\n\t" \
  "s_add_u32 m0, %[lw], 67584\n\t" \
  "s_nop 0\n\t" \
  "global_load_lds_dwordx4 v250, s[92:93] sc1\n\t" \
  "ds_read_b128 v[224:227], v246 offset:33024\n\t" \
  "ds_read_b128 v[228:231], v246 offset:49408\n\t" \
  "ds_read_b128 v[232:235], v247 offset:33024\n\t" \
  "ds_read_b128 v[236:239], v247 offset:49408\n\t" \
  "s_add_u32 m0, %[lw], 68608\n\t" \
  "s_nop 0\n\t" \
  "global_load_lds_dwordx4 v251, s[92:93] sc1\n\t" \
  "s_waitcnt lgkmcnt(15)\n\t" \
  "s_waitcnt lgkmcnt(12)\n\t" \
  "s_sub_i32 s91, s86, 2\n\t" \
  "v_mov_b32 v173, s91\n\t" \
  "buffer_store_dword v173, v174, %[rs], 0 offen sc1\n\t" \
  "s_lshr_b32 s90, s87, 7\n\t" \
  "s_xor_b32 s90, s90, %[s0]\n\t" \
  "s_and_b32 s90, s90, 1\n\t" \
  "s_lshl1_add_u32 s90, s87, s90\n\t" \
  "s_lshl_b32 s90, s90, 13\n\t" \
  "v_add_f32 v160, v160, v128\n\t" \
  "v_add_f32 v161, v161, v129\n\t" \
  "v_add_f32 v162, v162, v130\n\t" \
  "v_add_f32 v163, v163, v131\n\t" \
  "v_add_f32 v164, v164, v132\n\t" \
  "v_add_f32 v165, v165, v133\n\t" \
  "v_add_f32 v166, v166, v134\n\t" \
  "v_add_f32 v167, v167, v135\n\t" \
  "v_cvt_pk_bf16_f32 v168, v160, v161\n\t" \
  "v_cvt_pk_bf16_f32 v169, v162, v163\n\t" \
  "v_cvt_pk_bf16_f32 v170, v164, v165\n\t" \
  "v_cvt_pk_bf16_f32 v171, v166, v167\n\t" \
  "buffer_store_dwordx4 v[168:171], v172, %[rt], s90 offen sc1\n\t" \
  "s_add_u32 s86, s86, 1\n\t" \
  "s_add_u32 s87, s87, 1\n\t" \
  "s_waitcnt lgkmcnt(0)\n\t" \
  "s_cmp_lt_u32 s86, 64\n\t" \
  "s_cbranch_scc1 .Lp1%=\n\t" \
  "s_barrier\n\t" \
  "s_setprio 2\n\t" \
  "v_mfma_f32_16x16x32_bf16 v[144:147], v[176:179], v[64:67], v[144:147]\n\t" \
  "v_mfma_f32_16x16x32_bf16 v[152:155], v[176:179], v[68:71], v[152:155]\n\t" \
  "v_mfma_f32_16x16x32_bf16 v[148:151], v[180:183], v[64:67], v[148:151]\n\t" \
  "v_mfma_f32_16x16x32_bf16 v[156:159], v[180:183], v[68:71], v[156:159]\n\t" \
  "v_mfma_f32_16x16x32_bf16 v[144:147], v[184:187], v[72:75], v[144:147]\n\t" \
  "v_mfma_f32_16x16x32_bf16 v[152:155], v[184:187], v[76:79], v[152:155]\n\t" \
  "v_mfma_f32_16x16x32_bf16 v[148:151], v[188:191], v[72:75], v[148:151]\n\t" \
  "v_mfma_f32_16x16x32_bf16 v[156:159], v[188:191], v[76:79], v[156:159]\n\t" \
  "v_mfma_f32_16x16x32_bf16 v[144:147], v[192:195], v[80:83], v[144:147]\n\t" \
  "v_mfma_f32_16x16x32_bf16 v[152:155], v[192:195], v[84:87], v[152:155]\n\t" \
  "v_mfma_f32_16x16x32_bf16 v[148:151], v[196:199], v[80:83], v[148:151]\n\t" \
  "v_mfma_f32_16x16x32_bf16 v[156:159], v[196:199], v[84:87], v[156:159]\n\t" \
  "v_mfma_f32_16x16x32_bf16 v[144:147], v[200:203], v[88:91], v[144:147]\n\t" \
  "v_mfma_f32_16x16x32_bf16 v[152:155], v[200:203], v[92:95], v[152:155]\n\t" \
  "v_mfma_f32_16x16x32_bf16 v[148:151], v[204:207], v[88:91], v[148:151]\n\t" \
  "v_mfma_f32_16x16x32_bf16 v[156:159], v[204:207], v[92:95], v[156:159]\n\t" \
  "v_mfma_f32_16x16x32_bf16 v[144:147], v[208:211], v[96:99], v[144:147]\n\t" \
  "v_mfma_f32_16x16x32_bf16 v[152:155], v[208:211], v[100:103], v[152:155]\n\t" \
  "v_mfma_f32_16x16x32_bf16 v[148:151], v[212:215], v[96:99], v[148:151]\n\t" \
  "v_mfma_f32_16x16x32_bf16 v[156:159], v[212:215], v[100:103], v[156:159]\n\t" \
  "v_mfma_f32_16x16x32_bf16 v[144:147], v[216:219], v[104:107], v[144:147]\n\t" \
  "v_mfma_f32_16x16x32_bf16 v[152:155], v[216:219], v[108:111], v[152:155]\n\t" \
  "v_mfma_f32_16x16x32_bf16 v[148:151], v[220:223], v[104:107], v[148:151]\n\t" \
  "v_mfma_f32_16x16x32_bf16 v[156:159], v[220:223], v[108:111], v[156:159]\n\t" \
  "v_mfma_f32_16x16x32_bf16 v[144:147], v[224:227], v[112:115], v[144:147]\n\t" \
  "v_mfma_f32_16x16x32_bf16 v[152:155], v[224:227], v[116:119], v[152:155]\n\t" \
  "v_mfma_f32_16x16x32_bf16 v[148:151], v[228:231], v[112:115], v[148:151]\n\t" \
  "v_mfma_f32_16x16x32_bf16 v[156:159], v[228:231], v[116:119], v[156:159]\n\t" \
  "v_mfma_f32_16x16x32_bf16 v[144:147], v[232:235], v[120:123], v[144:147]\n\t" \
  "v_mfma_f32_16x16x32_bf16 v[152:155], v[232:235], v[124:127], v[152:155]\n\t" \
  "v_mfma_f32_16x16x32_bf16 v[148:151], v[236:239], v[120:123], v[148:151]\n\t" \
  "v_mfma_f32_16x16x32_bf16 v[156:159], v[236:239], v[124:127], v[156:159]\n\t" \
  "s_setprio 0\n\t" \
  "s_nop 4\n\t" \
  "ds_write_b128 v252, v[152:155] offset:0\n\t" \
  "s_nop 0\n\t" \
  "ds_write_b128 v252, v[156:159] offset:1024\n\t" \
  "s_waitcnt lgkmcnt(0)\n\t" \
  "s_barrier\n\t" \
  "ds_read_b128 v[160:163], v253 offset:0\n\t" \
  "ds_read_b128 v[164:167], v253 offset:1024\n\t" \
  "s_waitcnt lgkmcnt(0)\n\t" \
  "s_lshr_b32 s90, s87, 7\n\t" \
  "s_xor_b32 s90, s90, %[s0]\n\t" \
  "s_and_b32 s90, s90, 1\n\t" \
  "s_lshl1_add_u32 s90, s87, s90\n\t" \
  "s_lshl_b32 s90, s90, 13\n\t" \
  "v_add_f32 v160, v160, v144\n\t" \
  "v_add_f32 v161, v161, v145\n\t" \
  "v_add_f32 v162, v162, v146\n\t" \
  "v_add_f32 v163, v163, v147\n\t" \
  "v_add_f32 v164, v164, v148\n\t" \
  "v_add_f32 v165, v165, v149\n\t" \
  "v_add_f32 v166, v166, v150\n\t" \
  "v_add_f32 v167, v167, v151\n\t" \
  "v_cvt_pk_bf16_f32 v168, v160, v161\n\t" \
  "v_cvt_pk_bf16_f32 v169, v162, v163\n\t" \
  "v_cvt_pk_bf16_f32 v170, v164, v165\n\t" \
  "v_cvt_pk_bf16_f32 v171, v166, v167\n\t" \
  "buffer_store_dwordx4 v[168:171], v172, %[rt], s90 offen sc1\n\t" \
  "s_waitcnt vmcnt(0)\n\t" \
  "s_barrier\n\t" \
  "s_sub_i32 s91, s86, 1\n\t" \
  "v_mov_b32 v173, s91\n\t" \
  "buffer_store_dword v173, v174, %[rs], 0 offen sc1\n\t" \
  "s_mov_b32 m0, s94\n\t" \
  ".Le%=:\n\t"
constexpr int W_X = 131072, W_PM = W_X + 16384;
template <bool ISV>
__device__ __forceinline__ void job_proj(Args& p, int g, int which, int h, char* smem, __amdgpu_buffer_rsrc_t rsy) {
  const int tid = threadIdx.x, lane = tid & 63, r = lane & 15, q = lane >> 4;
  const int w = __builtin_amdgcn_readfirstlane(tid >> 6), kh = w >> 2, cg = w & 3;
  u32x32 W[4];
  const unsigned lw = lds_off(smem) + w * 4096;
  u32x4 vx;
  u32x2 tag;
  const char* xn = p.ws + WS_XN;
  auto row = [&](int t, int i) {
    const char* src = xn + (size_t)(min(t, 127) >> 1) * (SEG * XLD * 2) + (t & 1) * 512;
    unsigned keep;
    asm volatile("s_mov_b32 %0, m0\n\ts_mov_b32 m0, %3\n\ts_nop 0\n\tglobal_load_lds_dwordx4 %1, %2 sc1\n\ts_mov_b32 m0, %0"
                 : "=&s"(keep) : "v"(vx[i]), "s"(src), "s"(lw + (t & 3) * 32768 + i * 1024) : "memory");
  };
  {
    const u16* Wm = which == 0 ? p.Wk : p.Wq;
    asm volatile("" : "+s"(Wm));
    if (ISV) Wm = p.Wv;
    const unsigned vo = (tid >> 4) * (HD * 2) + 16 * ((lane & 15) ^ swzB((tid >> 4) & 15));
    auto stage = [&](int j) {
      const char* src = (const char*)(Wm + (size_t)(WREV ? 7 - j : j) * 64 * HD + h * DH);
      const unsigned dst = lds_off(smem) + (j & 3) * 32768 + w * 1024;
      glds2(src, 32 * HD * 2, vo, dst);
      glds2(src + 512 * HD * 2, 32 * HD * 2, vo, dst + 16384);
    };
    const unsigned own = min((h & LN_M) ^ LN_X, 1);
    tag = start_rows<16>(p, 32 + g, own, [&]() { stage(0); stage(1); stage(2); stage(3); });
#pragma unroll
    for (int i = 0; i < 4; ++i) vx[i] = ((4 * w + i) ^ (ISV ? 0 : (lane >> 1) & 16)) * (XLD * 2) + (lane >> 5) * 1024 + (((lane & 31) ^ ((4 * w + i) & 15)) << 4);
    Boot bt;
#pragma unroll
    for (int j = 0; j < 8; ++j) {
      if (j < 4) WAIT_V(16); else if (j == 4) WAIT_V(12); else if (j == 5) WAIT_V(8); else if (j == 6) WAIT_V(4); else WAIT_V(10);
      if (j == 6 && w == 0) lds_at(smem + W_PM)[0] = boot_done(p.ws, bt, true, tag);
      LBAR();
      if (j == 4 && own) report_rows(p, 32 + g, tag);
      const char* sb = smem + (j & 3) * 32768 + kh * 16384;
#pragma unroll
      for (int k = 0; k < 4; ++k) {
        const int ks = k >> 1, a = k & 1;
        const unsigned ch = ISV ? 2 * (2 * cg + (a ^ kh)) + ((r & 3) >> 1) : 4 * cg + (r & 3), by = ISV ? 8 * (r & 1) : 8 * a;
        s16x4 lo = __builtin_amdgcn_ds_read_tr16_b64_v4i16((lds_s16x4_ptr)(sb + offB(32 * ks + 8 * q + (r >> 2), ch) + by));
        s16x4 hi = __builtin_amdgcn_ds_read_tr16_b64_v4i16((lds_s16x4_ptr)(sb + offB(32 * ks + 8 * q + 4 + (r >> 2), ch) + by));
        const int f = 4 * (WREV ? 7 - j : j) + k;
        put4(W[f >> 3], f & 7, (bf16x8){lo[0], lo[1], lo[2], lo[3], hi[0], hi[1], hi[2], hi[3]});
      }
      LBAR();
      if (j == 3 && w == 0) boot_ask(p.ws, bt);
      if (j + 4 < 8) stage(j + 4);
      if (j == 6) {
#pragma unroll
        for (int i = 0; i < 10; ++i) row(i >> 2, i & 3);
      }
    }
    p.s0 = uni(lds_at(smem + W_PM)[0] >> 31);
  }
  typedef __attribute__((ext_vector_type(8))) unsigned u32x8;
  u32x8 la;
#pragma unroll
  for (int i = 0; i < 8; ++i) la[i] = (i >> 2) * 65536 + r * 1024 + kh * 512 + (((4 * (i & 3) + q) ^ r) << 4);
  const unsigned xw = W_X + w * 2048 + lane * 16;
  u32x32 acc;
  asm volatile("" : "={v[128:159]}"(acc));
  WAIT_V(6);
  LBAR();
  {
    const int g0 = 0;
#define PR_LOOP(TXT)                                                                                                              \
    asm volatile(TXT                                                                                                              \
        : "+{v[128:159]}"(acc)                                                                                                    \
        : W4_IN, "{v[208:215]}"(la), "{v[216:219]}"(vx), "{v220}"(xw), "{v221}"(xw ^ 8192),                                       \
          "{v172}"((ISV ? 2 * cg + kh : 4 * kh + cg) * 1024 + lane * 16), "{v174}"(tid == 256 ? fkq(h, which) : 0x7F000000),      \
          [rt] "s"(raw_rsrc(p.ws + WS_T, T_BYTES)), [rs] "s"(raw_rsrc(p.ws + WS_SY, SY_TOTAL)), [s0] "s"(p.s0 ^ TF_KQV),          \
          [lw] "s"(lw), [kh] "s"(kh), [g0] "s"(g0), [end] "s"(g0 ? NSEG : PR_G1), [x0] "s"(xn + (g0 + 1) * (SEG * XLD * 2)),                       \
          [t0] "s"(which * T_Q + kseg(0, h) - 1)                                                                                  \
        : CLB8(160, 161, 162, 163, 164, 165, 166, 167), "v168", "v169", "v170", "v171", "v173", CLB8(176, 177, 178, 179, 180, 181, 182, 183), \
          CLB8(184, 185, 186, 187, 188, 189, 190, 191), CLB8(192, 193, 194, 195, 196, 197, 198, 199),                             \
          CLB8(200, 201, 202, 203, 204, 205, 206, 207), "s86", "s87", "s88", "s90", "s91", "s92", "s93", "s94", "scc", "memory")
    if (ISV) PR_LOOP(PR_ASM_1); else PR_LOOP(PR_ASM_0);
    if (w == 0 && g0 == 0) wait_rows(p.ws, tag);
  }
#define PP_LOOP(TXT)                                                                                                              \
  asm volatile(TXT                                                                                                                \
      : "+{v[128:159]}"(acc)                                                                                                      \
      : W4_IN, "{v[240:247]}"(la), "{v[248:251]}"(vx), "{v252}"(xw), "{v253}"(xw ^ 8192),                                         \
        "{v172}"((ISV ? 2 * cg + kh : 4 * kh + cg) * 1024 + lane * 16), "{v174}"(tid == 256 ? fkq(h, which) : 0x7F000000),        \
        [rt] "s"(raw_rsrc(p.ws + WS_T, T_BYTES)), [rs] "s"(raw_rsrc(p.ws + WS_SY, SY_TOTAL)), [s0] "s"(p.s0 ^ TF_KQV),            \
        [lw] "s"(lw), [kh] "s"(kh), [g0] "n"(PR_G1), [x0] "s"(xn + (PR_G1 + 1) * (SEG * XLD * 2)),                                \
        [t0] "s"(which * T_Q + kseg(0, h) - 1)                                                                                    \
      : CLB8(160, 161, 162, 163, 164, 165, 166, 167), "v168", "v169", "v170", "v171", "v173", CLB8(176, 177, 178, 179, 180, 181, 182, 183), \
        CLB8(184, 185, 186, 187, 188, 189, 190, 191), CLB32(192), CLB8(224, 225, 226, 227, 228, 229, 230, 231),                   \
        CLB8(232, 233, 234, 235, 236, 237, 238, 239), "s86", "s87", "s90", "s91", "s92", "s93", "s94", "scc", "memory")
  if (ISV) PP_LOOP(PP_ASM_1); else PP_LOOP(PP_ASM_0);
}

enum { J_NONE = 0, J_GU = 3, J_PR = 5, J_CB = 8, J_DN = 14 };
struct BigJob { int type, panel, idx; };
struct BigPtr { const char *a, *b0, *b1; };
constexpr int G_LDA = XLD * 2, G_LDB = FF * 2;

__device__ __forceinline__ BigPtr big_ptrs(const Args& p, const BigJob& j) {
  BigPtr r;
  r.a = p.ws + WS_XN + (size_t)j.panel * 128 * G_LDA;
  r.b0 = (const char*)(p.wg + j.idx * 128); r.b1 = (const char*)(p.wu + j.idx * 128);
  return r;
}
__device__ __forceinline__ void big_publish(const Args& p, const BigJob& j) {
  int tid = threadIdx.x;
  asm volatile("" : "+v"(tid));
  if (j.type == J_GU && tid == 0)
    __hip_atomic_fetch_add((gu32*)(p.ws + WS_SY + SY_CH) + j.panel * 16, 1u, __ATOMIC_RELAXED, __HIP_MEMORY_SCOPE_AGENT);
}
typedef f32x4 Acc[2][2][4][2];
__device__ __forceinline__ void big_epilogue(const Args& p, const BigJob& j, Acc& acc, char* smem) {
  int tid = threadIdx.x;
  asm volatile("" : "+v"(tid));
  const int lane = tid & 63;
  const int wid = __builtin_amdgcn_readfirstlane(tid >> 6);
  const int wr = wid >> 2, wc = wid & 3;
  {
    const __amdgpu_buffer_rsrc_t ro = mkrsrc(p.ws + WS_HF, S_LEN * 16384);
#pragma unroll
    for (int i = 0; i < 8; ++i) {
      f32x4 hh[2];
#pragma unroll
      for (int n = 0; n < 2; ++n)
#pragma unroll
        for (int jj = 0; jj < 4; ++jj) {
          const float g = acc[i >> 2][0][i & 3][n][jj], u = acc[i >> 2][1][i & 3][n][jj];
          hh[n][jj] = __builtin_amdgcn_rcpf(1.0f + __builtin_amdgcn_exp2f(g * -1.4426950408889634f)) * u;
        }
      st_sc1(ro, h_off(p, j.panel + (i >> 2)) + (j.idx * 4 + wc) * 16384 + (wr * 4 + (i & 3)) * 1024 + lane * 16,
             ((u32x4){cvtpk(hh[0][0], hh[0][1]), cvtpk(hh[0][2], hh[0][3]), cvtpk(hh[1][0], hh[1][1]), cvtpk(hh[1][2], hh[1][3])}));
    }
  }
}
__device__ __forceinline__ void gemm_role(Args& p, int g, char* smem) {
  const __amdgpu_buffer_rsrc_t rsy = mkrsrc(p.ws + WS_SY, SY_TOTAL);
  const int x = g & 3, k = g >> 2;
  volatile unsigned* slot = reinterpret_cast<volatile unsigned*>(smem + 163824);
  int rnd = k < 24 ? 2 : -1;
  auto claim = [&]() {
    ++rnd;
    const int n = k < 24 ? 32 + k : rnd * 8 + k - 24, pn = (n * 37) >> 8;
    BigJob b;
    b.type = rnd < 4 ? J_GU : (rnd == 4 ? J_DN : (rnd == 5 && k >= 24 && k - 24 < 14 - NFUSE ? J_CB : J_NONE));
    b.panel = rnd < 4 ? 14 - 2 * pn : (rnd == 4 ? 4 * ((2 - (k >> 3)) & 3) + x : NFUSE + k - 24);
    b.idx = rnd < 4 ? x + 4 * (n - 7 * pn) : k & 7;
    return b;
  };
  if (k < 24) {
    if (k >= 16) job_proj<true>(p, g, 2, x * 8 + (k & 7), smem, rsy);
    else job_proj<false>(p, g, k >> 3, x * 8 + (k & 7), smem, rsy);
  } else {
    u32x2 tag;
#pragma unroll 1
    for (int u = 0; u < (k < 30 && LN_M ? 2 : 1); ++u) {
      const int rg = u ? 32 + (4 * (k - 24) + 1) * 4 + x : g - 96;
      tag = start_rows<0>(p, rg, 1, []() {});
      WAIT_V(0);
      LBAR();
      report_rows(p, rg, tag);
    }
#pragma unroll 1
    for (int i = 0; i < PF_N; ++i) {
      const int m = i % 3, o = ((i / 3) * 32 + g - 96) * 448 + (int)threadIdx.x;
      if (threadIdx.x < 448) touch_line((const char*)(m == 0 ? p.wg : (m == 1 ? p.wu : p.wd)) + (size_t)o * 128, smem + (threadIdx.x >> 6) * 256);
      WAIT_V(1);
    }
    WAIT_V(0);
    if (threadIdx.x < 64) {
      wait_rows(p.ws, tag);
      Boot bt;
      boot_ask(p.ws, bt);
      WAIT_V(0);
      *slot = boot_done(p.ws, bt, true, tag);
    }
    LBAR();
    p.s0 = *slot >> 31;
  }
  p.s0 = uni(p.s0);
  BigJob cur = claim();
  while (cur.type != J_NONE) {
    if (cur.type != J_GU) {
      if (cur.type == J_DN) job_down(p, cur.panel, cur.idx, smem, rsy);
      else job_comb(p, cur.panel, x, rsy);
      cur = claim();
      continue;
    }
    int tid = threadIdx.x;
    asm volatile("" : "+v"(tid));
    const int lane = tid & 63;
    const int wid = __builtin_amdgcn_readfirstlane(tid >> 6);
    const int wr = wid >> 2, wc = wid & 3;
    const unsigned ra = tid >> 3, ca = 16 * ((tid & 7) ^ ((tid >> 3) & 7)), rb = tid >> 4, cb = 16 * ((tid & 15) ^ swzB((tid >> 4) & 15));
    const unsigned lw = lds_off(smem) + wid * 1024;
#define B8_SA(b, h) (((b) * 2 + (h)) * 16384)
#define B8_SB(b, h) ((4 + (b) * 2 + (h)) * 16384)
#define B8_STA(b, h, P, kt) glds2<true>((P).a + (size_t)(h) * 128 * G_LDA + (kt) * 128, 64 * G_LDA, ra * G_LDA + ca, lw + B8_SA(b, h))
#define B8_STB(b, h, P, kt) glds2(((h) ? (P).b1 : (P).b0) + (size_t)(kt) * 64 * G_LDB, 32 * G_LDB, rb * G_LDB + cb, lw + B8_SB(b, h))
#define B8_LDA(b, h) do { _Pragma("unroll") for (int m = 0; m < 4; ++m) _Pragma("unroll") for (int kk = 0; kk < 2; ++kk) \
      At[m][kk] = ldsf(smem + B8_SA(b, h) + offA(wr * 64 + m * 16 + (lane & 15), kk * 4 + (lane >> 4))); } while (0)
#define B8_LDB(dst, b, h) do { _Pragma("unroll") for (int n = 0; n < 2; ++n) _Pragma("unroll") for (int kk = 0; kk < 2; ++kk) \
      dst[n][kk] = ld_bfrag(smem + B8_SB(b, h), kk, wc * 2 + n, lane); } while (0)
#define B8_MMA(ai, bj, Bt) do { __builtin_amdgcn_s_setprio(1); _Pragma("unroll") for (int m = 0; m < 4; ++m) _Pragma("unroll") for (int n = 0; n < 2; ++n) \
      _Pragma("unroll") for (int kk = 0; kk < 2; ++kk) acc[ai][bj][m][n] = MFMA(Bt[n][kk], At[m][kk], acc[ai][bj][m][n]); __builtin_amdgcn_s_setprio(0); } while (0)
#define WAIT_L(n) asm volatile("s_waitcnt lgkmcnt(%0)" :: "n"(n) : "memory")
#define BAR() __builtin_amdgcn_s_barrier()
#define WAIT_RX(c) asm volatile("s_waitcnt vmcnt(16)\n\ts_cmp_lg_u32 %0, 0\n\ts_cbranch_scc1 .Lrx%=\n\ts_waitcnt vmcnt(8)\n.Lrx%=:" :: "s"(c) : "memory", "scc")
    Acc acc;
    bf16x8 At[4][2], B0[2][2], B1[2][2];
    BigPtr cp = big_ptrs(p, cur);
    BigJob pend; pend.type = J_NONE; pend.panel = 0; pend.idx = 0;
    LBAR();
    B8_STB(0, 0, cp, 0); B8_STB(0, 1, cp, 0); B8_STA(0, 0, cp, 0); B8_STA(0, 1, cp, 0);
    if (wr == 1) BAR();
    WAIT_V(2); BAR();
    B8_STB(1, 0, cp, 1); B8_STA(1, 0, cp, 1); B8_STB(1, 1, cp, 1);
    WAIT_V(6); BAR();
    BigJob nx = pend;
    for (;;) {
      bool chain = false;
      BigPtr np = cp;
      constexpr int nt = DM / 64;
#pragma unroll
      for (int i = 0; i < 32; ++i) acc[i >> 4][(i >> 3) & 1][(i >> 1) & 3][i & 1] = (f32x4){0.f, 0.f, 0.f, 0.f};
#pragma unroll 1
      for (int t = 0; t < nt; t += 2) {
        const bool last = t == nt - 2;
        if (last) {
          nx = claim();
          chain = nx.type == J_GU;
          if (chain) np = big_ptrs(p, nx);
        }
        const BigPtr sp = np;
        const int t2 = last ? 0 : t + 2;
        if (t == 2) big_publish(p, pend);
        const int rx = __builtin_amdgcn_readfirstlane(t == 0 && pend.type != J_NONE);
        B8_LDB(B0, 0, 0); B8_LDB(B1, 0, 1); SCHED(); B8_LDA(0, 0); B8_STA(1, 1, cp, t + 1);
        WAIT_RX(rx);
        WAIT_L(0); BAR(); B8_MMA(0, 0, B0); B8_MMA(0, 1, B1); BAR(); SCHED();
        B8_LDA(0, 1); B8_STB(0, 0, sp, t2); B8_STB(0, 1, sp, t2); B8_STA(0, 0, sp, t2);
        WAIT_RX(rx);
        WAIT_L(0); BAR(); B8_MMA(1, 0, B0); B8_MMA(1, 1, B1); BAR(); SCHED();
        B8_LDB(B0, 1, 0); B8_LDB(B1, 1, 1); SCHED(); B8_LDA(1, 0); B8_STA(0, 1, sp, t2);
        WAIT_V(8); WAIT_L(0); BAR(); B8_MMA(0, 0, B0); B8_MMA(0, 1, B1); BAR(); SCHED();
        B8_LDA(1, 1); B8_STB(1, 0, sp, t2 + 1); B8_STB(1, 1, sp, t2 + 1); B8_STA(1, 0, sp, t2 + 1);
        WAIT_V(8); WAIT_L(0); BAR(); B8_MMA(1, 0, B0); B8_MMA(1, 1, B1); BAR(); SCHED();
      }
      if (wr == 0) BAR();
      big_epilogue(p, cur, acc, smem);
      if (!chain) break;
      pend = cur; cur = nx; cp = np;
      if (wr == 1) BAR();
    }
    WAIT_V(0);
    BAR();
    asm volatile(".Lcode_end:");
    big_publish(p, cur);
    cur = nx;
  }
}


__device__ __forceinline__ unsigned now_q() { return (unsigned)__builtin_amdgcn_s_memrealtime() * 4u; }
__device__ __forceinline__ void pace_until(unsigned t) {
#pragma unroll 1
  for (int i = 0; i < 256; ++i) {
    const int d = (int)(t - now_q());
    if (d <= 0) break;
    if (d > 32) __builtin_amdgcn_s_sleep(1);
  }
}
#define CLB_E "s76", "s77", "s78", "s79", "s80", "s81", "s82", "s83", "s84", "s85", "s86", "s87", "s88", "s89"
#define EDGE_ASM \
  "s_memtime s[76:77]\n\t" \
  "s_memrealtime s[78:79]\n\t" \
  "s_nop 3\n\t" \
  "s_memrealtime s[80:81]\n\t" \
  "s_nop 3\n\t" \
  "s_memrealtime s[82:83]\n\t" \
  "s_nop 3\n\t" \
  "s_memrealtime s[84:85]\n\t" \
  "s_nop 3\n\t" \
  "s_memrealtime s[86:87]\n\t" \
  "s_nop 3\n\t" \
  "s_memrealtime s[88:89]\n\t" \
  "s_mov_b32 s93, 0\n\t" \
  "s_waitcnt lgkmcnt(0)\n\t" \
  "s_cmp_eq_u32 s80, s78\n\t" \
  "s_addc_u32 s93, s93, 0\n\t" \
  "s_cmp_eq_u32 s82, s78\n\t" \
  "s_addc_u32 s93, s93, 0\n\t" \
  "s_cmp_eq_u32 s84, s78\n\t" \
  "s_addc_u32 s93, s93, 0\n\t" \
  "s_cmp_eq_u32 s86, s78\n\t" \
  "s_addc_u32 s93, s93, 0\n\t" \
  "s_cmp_eq_u32 s88, s78\n\t" \
  "s_addc_u32 s93, s93, 0\n\t" \
  "s_mul_i32 s93, s93, 21\n\t" \
  "s_add_u32 s76, s76, 15\n\t" \
  "s_add_u32 %[kc], s76, s93\n\t" \
  "s_lshl2_add_u32 %[kq], s78, 16\n\t"
__device__ __forceinline__ unsigned time_of(unsigned cyc) {
  unsigned c, q;
  asm volatile(EDGE_ASM : [kc] "=&s"(c), [kq] "=&s"(q) :: CLB_E, "s93", "scc");
  return q - (((c - cyc) * 11900u) >> 16);
}
#define DS_RED 20
#define DS_MEM 13
#define LEAD_DN 2
#define LEAD_UP 6
__device__ __forceinline__ u32x2 ld2_sc1(__amdgpu_buffer_rsrc_t rs, int off, int soff = 0) {
  return __builtin_amdgcn_raw_buffer_load_b64(rs, off, soff, 16);
}
__device__ __forceinline__ void st2_sc1(__amdgpu_buffer_rsrc_t rs, int off, u32x2 v) {
  __builtin_amdgcn_raw_buffer_store_b64(v, rs, off, 0, 16);
}
__device__ __forceinline__ int row_max(int v) {
  v = max(v, __builtin_amdgcn_update_dpp(0, v, 0x128, 0xf, 0xf, false));
  v = max(v, __builtin_amdgcn_update_dpp(0, v, 0x124, 0xf, 0xf, false));
  v = max(v, __builtin_amdgcn_update_dpp(0, v, 0x122, 0xf, 0xf, false));
  v = max(v, __builtin_amdgcn_update_dpp(0, v, 0x121, 0xf, 0xf, false));
  return v;
}
__device__ __forceinline__ int wave_max(int v) {
  v = row_max(v);
  return max(max(__builtin_amdgcn_readlane(v, 0), __builtin_amdgcn_readlane(v, 16)),
             max(__builtin_amdgcn_readlane(v, 32), __builtin_amdgcn_readlane(v, 48)));
}
__device__ __forceinline__ int track(int est, int x, int up, int dn, int out) {
  const int d = x - est;
  return est + (d > out ? 4 : max(min(d, up), -dn));
}
#define WSL 12
#define PLAN_SLEEP 2
#define REF_W 3
#define REF_G 60
#define RL0 315
__device__ __forceinline__ void watch(__amdgpu_buffer_rsrc_t rsy, int s, int lane, unsigned t0, const unsigned* tgt_, gu32* abortw, int hs) {
  const int rl = (int)__builtin_amdgcn_raw_buffer_load_b32(rsy, lane * 128, SY_BR + hs, 16);
  u32x2 ra;
  for (unsigned spins = 0;;) {
    asm volatile("" ::: "memory");
    ra = ld2_sc1(rsy, (lane & 31) * 128, SY_BA + hs);
    const bool all = __all((int)ra[0] >= s);
    if (all) break;
    if (spins < 3) __builtin_amdgcn_s_sleep(PLAN_SLEEP); else __builtin_amdgcn_s_sleep(WSL);
    if (spin_fail(spins, abortw)) break;
  }
  int d = ra[0] == (unsigned)s ? (int)(ra[1] - t0) : 0;
  d = max(d, __builtin_amdgcn_update_dpp(0, d, 0xB1, 0xf, 0xf, false));
  d = max(d, __builtin_amdgcn_update_dpp(0, d, 0x4E, 0xf, 0xf, false));
  int sum = d + __builtin_amdgcn_update_dpp(0, d, 0x128, 0xf, 0xf, false);
  sum += __builtin_amdgcn_update_dpp(0, sum, 0x124, 0xf, 0xf, false);
  const int mx = wave_max(d), mean = (__builtin_amdgcn_readlane(sum, 0) + __builtin_amdgcn_readlane(sum, 16)) >> 3;
  const unsigned t = t0 + mx - min(((mx - mean) * REF_W) >> 2, REF_G) + wave_max(rl < 0 ? RL0 : rl) + (DS_RED + DS_MEM);
  lds_vu_ptr tgt = lds_at(tgt_);
  if (lane < 2) tgt[lane] = lane ? t : s + 1;
}
__device__ __forceinline__ unsigned ld_flag(__amdgpu_buffer_rsrc_t rs, int off) {
  return __builtin_amdgcn_raw_buffer_load_b32(rs, off, 0, 16);
}

constexpr unsigned long long QK_MAP[8] = {0xbaf9876d543210ecull, 0xa98f76d543210bceull, 0x98f76d543210ecbaull, 0x876fd543210bcea9ull, 0x76fd543210ecba98ull, 0x65df43210bcea987ull, 0x5df43210ecba9876ull, 0xd43f210bcea98765ull};
#define QK_ASM_0 \
  "s_waitcnt lgkmcnt(0)\n\t" \
  "v_subrev_u32 v244, 0x8000, v250\n\t" \
  "ds_read_b128 v[216:219], v248 offset:32768\n\t" \
  "ds_read_b128 v[220:223], v248 offset:33792\n\t" \
  "ds_read_b128 v[224:227], v248 offset:34816\n\t" \
  "ds_read_b128 v[228:231], v248 offset:35840\n\t" \
  "ds_read_b128 v[208:211], v244 offset:0\n\t" \
  "ds_read_b128 v[212:215], v244 offset:1024\n\t" \
  "ds_read_b128 v[236:239], v244 offset:2048\n\t" \
  "ds_read_b128 v[240:243], v244 offset:3072\n\t" \
  "v_mov_b64 v[192:193], 0\n\t" \
  "v_mov_b64 v[194:195], 0\n\t" \
  "v_mov_b64 v[196:197], 0\n\t" \
  "v_mov_b64 v[198:199], 0\n\t" \
  "s_movk_i32 s92, 0x2000\n\t" \
  ".Lwt%=:\n\t" \
  "ds_read_b64 v[200:201], v248 offset:30720\n\t" \
  "s_memrealtime s[90:91]\n\t" \
  "s_sub_u32 s92, s92, 1\n\t" \
  "s_waitcnt lgkmcnt(0)\n\t" \
  "v_readfirstlane_b32 s93, v200\n\t" \
  "v_readfirstlane_b32 %[tgt], v201\n\t" \
  "s_cbranch_scc1 .Lgo%=\n\t" \
  "s_cmp_eq_u32 s93, %[step]\n\t" \
  "s_cbranch_scc1 .Lwx%=\n\t" \
  "s_sleep 1\n\t" \
  "s_branch .Lwt%=\n\t" \
  ".Lwx%=:\n\t" \
  "s_lshl_b32 s93, s90, 2\n\t" \
  "s_sub_i32 s93, %[tgt], s93\n\t" \
  "s_cmp_le_i32 s93, 0\n\t" \
  "s_cbranch_scc1 .Lgo%=\n\t" \
  "s_min_i32 s93, s93, 8000\n\t" \
  "s_mov_b32 s94, s93\n\t" \
  ".Lsl%=:\n\t" \
  "s_sub_i32 s94, s94, 18\n\t" \
  "s_cmp_lt_i32 s94, 30\n\t" \
  "s_cbranch_scc1 .Lsx%=\n\t" \
  "s_sleep 1\n\t" \
  "s_branch .Lsl%=\n\t" \
  ".Lsx%=:\n\t" \
  "s_sub_i32 s93, s93, 12\n\t" \
  "s_max_i32 s93, s93, 4\n\t" \
  "s_lshr_b32 s93, s93, 2\n\t" \
  "s_add_u32 s93, s93, s90\n\t" \
  "s_memrealtime s[90:91]\n\t" \
  ".Lpace%=:\n\t" \
  "s_memrealtime s[94:95]\n\t" \
  "s_waitcnt lgkmcnt(1)\n\t" \
  "s_sub_i32 s92, s90, s93\n\t" \
  "s_cmp_ge_i32 s92, 0\n\t" \
  "s_cbranch_scc1 .Le%=\n\t" \
  "s_memrealtime s[90:91]\n\t" \
  "s_waitcnt lgkmcnt(1)\n\t" \
  "s_sub_i32 s92, s94, s93\n\t" \
  "s_cmp_lt_i32 s92, 0\n\t" \
  "s_cbranch_scc1 .Lpace%=\n\t" \
  "s_nop 4\n\t" \
  ".Le%=:\n\t" \
  "s_add_u32 s92, s92, s93\n\t" \
  "s_lshl_b32 s92, s92, 2\n\t" \
  "s_sub_i32 s92, %[tgt], s92\n\t" \
  "s_max_i32 s92, s92, 0\n\t" \
  "s_bitcmp0_b32 s92, 3\n\t" \
  "s_cbranch_scc1 .Lf3%=\n\t" \
  "s_nop 15\n\t" \
  ".Lf3%=:\n\t" \
  "s_bitcmp0_b32 s92, 2\n\t" \
  "s_cbranch_scc1 .Lf2%=\n\t" \
  "s_nop 10\n\t" \
  ".Lf2%=:\n\t" \
  ".Lgo%=:\n\t" \
  "s_mov_b32 s92, 0\n\t" \
  ".Lpoll%=:\n\t" \
  "buffer_load_dwordx4 v[200:203], v248, %[rs], %[som] offen sc1\n\t" \
  "buffer_load_dwordx4 v[204:207], v248, %[rs], %[som2] offen sc1\n\t" \
  "s_waitcnt vmcnt(1) lgkmcnt(0)\n\t" \
  "ds_write_b128 v249, v[200:203]\n\t" \
  "v_smfmac_f32_16x16x64_bf16 v[192:195], v[200:203], v[208:215], v251\n\t" \
  "v_max3_u32 v244, v200, v201, v202\n\t" \
  "v_smfmac_f32_16x16x64_bf16 v[196:199], v[200:203], v[236:243], v251\n\t" \
  "s_waitcnt vmcnt(0)\n\t" \
  "ds_write_b128 v249, v[204:207] offset:8192\n\t" \
  "v_smfmac_f32_16x16x64_bf16 v[192:195], v[204:207], v[216:223], v251\n\t" \
  "v_max3_u32 v244, v244, v203, v204\n\t" \
  "v_max3_u32 v244, v244, v205, v206\n\t" \
  "v_smfmac_f32_16x16x64_bf16 v[196:199], v[204:207], v[224:231], v251\n\t" \
  "v_max_u32 v244, v244, v207\n\t" \
  "v_cmp_le_u32 vcc, %[sent], v244\n\t" \
  "s_cbranch_vccnz .Lslow%=\n\t" \
  ".Lok%=:\n\t" \
  "ds_read_b128 v[216:219], v250 offset:32768\n\t" \
  "ds_read_b128 v[220:223], v250 offset:33792\n\t" \
  "ds_read_b128 v[224:227], v250 offset:34816\n\t" \
  "ds_read_b128 v[228:231], v250 offset:35840\n\t" \
  "s_waitcnt lgkmcnt(4)\n\t" \
  "s_barrier\n\t" \
  "ds_read_b128 v[208:211], v248 offset:1024\n\t" \
  "ds_read_b128 v[212:215], v248 offset:2048\n\t" \
  "ds_read_b128 v[236:239], v248 offset:3072\n\t" \
  "ds_read_b128 v[240:243], v248 offset:4096\n\t" \
  "s_waitcnt lgkmcnt(3)\n\t" \
  "ds_read_b128 v[244:247], v248 offset:13312\n\t" \
  "v_smfmac_f32_16x16x64_bf16 v[192:195], v[208:211], v[216:223], v251\n\t" \
  "ds_read_b128 v[216:219], v250 offset:0\n\t" \
  "v_smfmac_f32_16x16x64_bf16 v[196:199], v[208:211], v[224:231], v251\n\t" \
  "s_waitcnt lgkmcnt(4)\n\t" \
  "ds_read_b128 v[220:223], v250 offset:1024\n\t" \
  "v_smfmac_f32_16x16x64_bf16 v[192:195], v[212:215], v[0:7], v251\n\t" \
  "ds_read_b128 v[224:227], v250 offset:2048\n\t" \
  "v_smfmac_f32_16x16x64_bf16 v[196:199], v[212:215], v[8:15], v251\n\t" \
  "s_waitcnt lgkmcnt(5)\n\t" \
  "ds_read_b128 v[228:231], v250 offset:3072\n\t" \
  "v_smfmac_f32_16x16x64_bf16 v[192:195], v[236:239], v[16:23], v251\n\t" \
  "ds_read_b128 v[200:203], v248 offset:5120\n\t" \
  "v_smfmac_f32_16x16x64_bf16 v[196:199], v[236:239], v[24:31], v251\n\t" \
  "s_waitcnt lgkmcnt(6)\n\t" \
  "ds_read_b128 v[204:207], v248 offset:6144\n\t" \
  "v_smfmac_f32_16x16x64_bf16 v[192:195], v[240:243], v[32:39], v251\n\t" \
  "ds_read_b128 v[208:211], v248 offset:7168\n\t" \
  "v_smfmac_f32_16x16x64_bf16 v[196:199], v[240:243], v[40:47], v251\n\t" \
  "s_waitcnt lgkmcnt(5)\n\t" \
  "ds_read_b128 v[212:215], v248 offset:9216\n\t" \
  "v_smfmac_f32_16x16x64_bf16 v[192:195], v[244:247], v[216:223], v251\n\t" \
  "s_waitcnt lgkmcnt(4)\n\t" \
  "v_smfmac_f32_16x16x64_bf16 v[196:199], v[244:247], v[224:231], v251\n\t" \
  "s_waitcnt lgkmcnt(3)\n\t" \
  "ds_read_b128 v[236:239], v248 offset:10240\n\t" \
  "v_smfmac_f32_16x16x64_bf16 v[192:195], v[200:203], v[48:55], v251\n\t" \
  "ds_read_b128 v[240:243], v248 offset:11264\n\t" \
  "v_smfmac_f32_16x16x64_bf16 v[196:199], v[200:203], v[56:63], v251\n\t" \
  "s_waitcnt lgkmcnt(4)\n\t" \
  "v_smfmac_f32_16x16x64_bf16 v[192:195], v[204:207], v[64:71], v251\n\t" \
  "ds_read_b128 v[244:247], v248 offset:12288\n\t" \
  "v_smfmac_f32_16x16x64_bf16 v[196:199], v[204:207], v[72:79], v251\n\t" \
  "s_waitcnt lgkmcnt(4)\n\t" \
  "v_smfmac_f32_16x16x64_bf16 v[192:195], v[208:211], v[80:87], v251\n\t" \
  "ds_read_b128 v[200:203], v248 offset:14336\n\t" \
  "v_smfmac_f32_16x16x64_bf16 v[196:199], v[208:211], v[88:95], v251\n\t" \
  "s_waitcnt lgkmcnt(4)\n\t" \
  "v_smfmac_f32_16x16x64_bf16 v[192:195], v[212:215], v[96:103], v251\n\t" \
  "ds_read_b128 v[204:207], v248 offset:15360\n\t" \
  "v_smfmac_f32_16x16x64_bf16 v[196:199], v[212:215], v[104:111], v251\n\t" \
  "s_waitcnt lgkmcnt(3)\n\t" \
  "v_smfmac_f32_16x16x64_bf16 v[192:195], v[236:239], v[112:119], v251\n\t" \
  "v_smfmac_f32_16x16x64_bf16 v[196:199], v[236:239], v[120:127], v251\n\t" \
  "v_smfmac_f32_16x16x64_bf16 v[192:195], v[240:243], v[128:135], v251\n\t" \
  "v_smfmac_f32_16x16x64_bf16 v[196:199], v[240:243], v[136:143], v251\n\t" \
  "s_waitcnt lgkmcnt(1)\n\t" \
  "v_smfmac_f32_16x16x64_bf16 v[192:195], v[244:247], v[144:151], v251\n\t" \
  "v_smfmac_f32_16x16x64_bf16 v[196:199], v[244:247], v[152:159], v251\n\t" \
  "v_smfmac_f32_16x16x64_bf16 v[192:195], v[200:203], v[160:167], v251\n\t" \
  "v_smfmac_f32_16x16x64_bf16 v[196:199], v[200:203], v[168:175], v251\n\t" \
  "s_waitcnt lgkmcnt(0)\n\t" \
  "v_smfmac_f32_16x16x64_bf16 v[192:195], v[204:207], v[176:183], v251\n\t" \
  "v_smfmac_f32_16x16x64_bf16 v[196:199], v[204:207], v[184:191], v251\n\t" \
  "ds_read_b128 v[200:203], v248 offset:16384\n\t" \
  "ds_read_b128 v[204:207], v248 offset:20480\n\t" \
  "v_mov_b32 v250, v232\n\t" \
  "v_mov_b32 v249, v233\n\t" \
  "s_nop 2\n\t" \
  "v_add_f32 v192, v192, v194\n\t" \
  "v_add_f32 v193, v193, v195\n\t" \
  "v_add_f32 v196, v196, v198\n\t" \
  "v_add_f32 v197, v197, v199\n\t" \
  "v_cvt_pk_bf16_f32 v192, v192, v193\n\t" \
  "v_cvt_pk_bf16_f32 v196, v196, v197\n\t" \
  "ds_write2_b32 v252, v192, v196 offset1:64\n\t" \
  "v_mov_b32 v251, v234\n\t" \
  "ds_read_b64_tr_b16 v[232:233], v250 offset:0\n\t" \
  "ds_read_b64_tr_b16 v[234:235], v250 offset:64\n\t" \
  "v_mov_b32 v212, %[step]\n\t" \
  "s_mov_b64 exec, 1\n\t" \
  "ds_write_b32 v248, v212 offset:28672\n\t" \
  "s_mov_b64 exec, -1\n\t" \
  "buffer_load_dwordx4 v[192:195], v248, %[rt], %[svt] offen offset:0 sc1\n\t" \
  "buffer_load_dwordx4 v[196:199], v248, %[rt], %[svt] offen offset:1024 sc1\n\t" \
  "s_waitcnt lgkmcnt(1)\n\t" \
  "v_mfma_f32_16x16x32_bf16 v[200:203], v[200:203], v[232:235], 0\n\t" \
  "v_mfma_f32_16x16x32_bf16 v[204:207], v[204:207], v[232:235], 0\n\t" \
  "s_nop 7\n\t" \
  "v_cndmask_b32 v216, v200, v204, %[nh]\n\t" \
  "v_cndmask_b32 v217, v201, v205, %[nh]\n\t" \
  "v_cndmask_b32 v218, v202, v206, %[nh]\n\t" \
  "v_cndmask_b32 v219, v203, v207, %[nh]\n\t" \
  "ds_write_b128 v248, v[216:219] offset:16384\n\t" \
  "buffer_load_dword v252, v249, %[rs], %[sv] offen offset:0 sc1\n\t" \
  "buffer_load_dword v253, v249, %[rs], %[sv] offen offset:256 sc1\n\t" \
  "ds_read_b32 v212, v248 offset:28672\n\t" \
  "ds_read_b64_tr_b16 v[236:237], v250 offset:512\n\t" \
  "ds_read_b64_tr_b16 v[238:239], v250 offset:576\n\t" \
  "ds_read_b64_tr_b16 v[240:241], v250 offset:1024\n\t" \
  "ds_read_b64_tr_b16 v[242:243], v250 offset:1088\n\t" \
  "ds_read_b64_tr_b16 v[244:245], v250 offset:1536\n\t" \
  "ds_read_b64_tr_b16 v[246:247], v250 offset:1600\n\t" \
  "s_setprio 3\n\t" \
  "s_waitcnt lgkmcnt(0)\n\t" \
  "v_cmp_ne_u32 vcc, %[step], v212\n\t" \
  "s_barrier\n\t" \
  "s_and_b32 s93, vcc_lo, 0xf\n\t" \
  "s_cbranch_scc1 .Lre%=\n\t" \
  ".Lrb%=:\n\t" \
  "ds_read_b64_tr_b16 v[200:201], v250 offset:2048\n\t" \
  "ds_read_b64_tr_b16 v[202:203], v250 offset:2112\n\t" \
  "ds_read_b64_tr_b16 v[204:205], v250 offset:2560\n\t" \
  "ds_read_b64_tr_b16 v[206:207], v250 offset:2624\n\t" \
  "ds_read_b128 v[220:223], v248 offset:17408\n\t" \
  "ds_read_b128 v[224:227], v248 offset:18432\n\t" \
  "ds_read_b128 v[228:231], v248 offset:19456\n\t" \
  "ds_read_b64_tr_b16 v[208:209], v250 offset:3072\n\t" \
  "ds_read_b64_tr_b16 v[210:211], v250 offset:3136\n\t" \
  "ds_read_b64_tr_b16 v[212:213], v250 offset:3584\n\t" \
  "ds_read_b64_tr_b16 v[214:215], v250 offset:3648\n\t" \
  "s_waitcnt lgkmcnt(9)\n\t" \
  "v_mfma_f32_16x16x32_bf16 v[200:203], v[200:203], v[232:235], 0\n\t" \
  "s_waitcnt lgkmcnt(7)\n\t" \
  "v_mfma_f32_16x16x32_bf16 v[200:203], v[204:207], v[236:239], v[200:203]\n\t" \
  "s_waitcnt lgkmcnt(6)\n\t" \
  "v_pk_add_f32 v[216:217], v[216:217], v[220:221]\n\t" \
  "v_pk_add_f32 v[218:219], v[218:219], v[222:223]\n\t" \
  "s_waitcnt lgkmcnt(4)\n\t" \
  "v_pk_add_f32 v[224:225], v[224:225], v[228:229]\n\t" \
  "v_pk_add_f32 v[226:227], v[226:227], v[230:231]\n\t" \
  "v_pk_add_f32 v[216:217], v[216:217], v[224:225]\n\t" \
  "v_pk_add_f32 v[218:219], v[218:219], v[226:227]\n\t" \
  "s_waitcnt lgkmcnt(2)\n\t" \
  "v_mfma_f32_16x16x32_bf16 v[200:203], v[208:211], v[240:243], v[200:203]\n\t" \
  "v_max3_f32 v224, v216, v217, v218\n\t" \
  "v_max_f32 v224, v224, v219\n\t" \
  "s_waitcnt lgkmcnt(0)\n\t" \
  "v_mfma_f32_16x16x32_bf16 v[200:203], v[212:215], v[244:247], v[200:203]\n\t" \
  "v_max_f32_dpp v225, v224, v224 row_ror:8 row_mask:0xf bank_mask:0xf\n\t" \
  "v_max_f32_dpp v226, v224, v224 row_ror:8 row_mask:0xf bank_mask:0xf\n\t" \
  "s_nop 1\n\t" \
  "v_permlane32_swap_b32 v225, v226\n\t" \
  "v_max_f32 v231, v225, v226\n\t" \
  "v_mov_b32 v220, 0x3f803f80\n\t" \
  "v_mov_b32 v221, 0x3f803f80\n\t" \
  "v_mov_b64 v[222:223], v[220:221]\n\t" \
  "v_mov_b32 v233, 0\n\t" \
  "v_mov_b32 v234, 0\n\t" \
  "v_max3_f32 v204, v200, v201, v231\n\t" \
  "v_max3_f32 v205, v204, v202, v203\n\t" \
  "v_max3_f32 v206, v204, v202, v203\n\t" \
  "v_cndmask_b32 v200, v200, v202, %[up]\n\t" \
  "v_cndmask_b32 v201, v201, v203, %[up]\n\t" \
  "v_permlane16_swap_b32 v205, v206\n\t" \
  "v_max_f32 v204, v205, v206\n\t" \
  "v_mul_f32 v204, %[negc], v204\n\t" \
  "v_fma_f32 v200, v200, %[c], v204\n\t" \
  "v_fma_f32 v201, v201, %[c], v204\n\t" \
  "v_exp_f32 v200, v200\n\t" \
  "v_fma_f32 v216, v216, %[c], v204\n\t" \
  "v_exp_f32 v201, v201\n\t" \
  "v_fma_f32 v217, v217, %[c], v204\n\t" \
  "v_exp_f32 v216, v216\n\t" \
  "v_fma_f32 v218, v218, %[c], v204\n\t" \
  "v_cvt_pk_bf16_f32 v232, v200, v201\n\t" \
  "v_cvt_pk_bf16_f32 v235, v200, v201\n\t" \
  "v_exp_f32 v217, v217\n\t" \
  "v_fma_f32 v219, v219, %[c], v204\n\t" \
  "v_mfma_f32_16x16x16_bf16 v[212:215], v[220:221], v[232:233], 0\n\t" \
  "v_exp_f32 v218, v218\n\t" \
  "s_waitcnt vmcnt(0)\n\t" \
  "v_mfma_f32_16x16x16_bf16 v[224:227], v[252:253], v[232:233], 0\n\t" \
  "v_cvt_pk_bf16_f32 v210, v216, v217\n\t" \
  "v_cvt_pk_bf16_f32 v208, v216, v217\n\t" \
  "v_exp_f32 v219, v219\n\t" \
  "v_mfma_f32_16x16x16_bf16 v[228:231], v[252:253], v[234:235], 0\n\t" \
  "v_cvt_pk_bf16_f32 v211, v218, v219\n\t" \
  "v_cvt_pk_bf16_f32 v209, v218, v219\n\t" \
  "v_mov_b32_dpp v208, v210 row_ror:8 row_mask:0xf bank_mask:0xc\n\t" \
  "v_mov_b32_dpp v210, v210 row_ror:8 row_mask:0xf bank_mask:0x3\n\t" \
  "v_mov_b32_dpp v209, v211 row_ror:8 row_mask:0xf bank_mask:0xc\n\t" \
  "v_mov_b32_dpp v211, v211 row_ror:8 row_mask:0xf bank_mask:0x3\n\t" \
  "v_max_u32 v204, v252, v253\n\t" \
  "v_cmp_le_u32 vcc, %[sent], v204\n\t" \
  "v_mfma_f32_16x16x32_bf16 v[212:215], v[220:223], v[208:211], v[212:215]\n\t" \
  ".Lpv%=:\n\t" \
  "v_mfma_f32_16x16x32_bf16 v[224:227], v[192:195], v[208:211], v[224:227]\n\t" \
  "v_mfma_f32_16x16x32_bf16 v[228:231], v[196:199], v[208:211], v[228:231]\n\t" \
  "s_cbranch_vccnz .Lvs%=\n\t" \
  ".Lvok%=:\n\t" \
  "s_nop 4\n\t" \
  "v_rcp_f32 v204, v212\n\t" \
  "v_cndmask_b32 v224, v224, v226, %[nh]\n\t" \
  "v_cndmask_b32 v225, v225, v227, %[nh]\n\t" \
  "v_cndmask_b32 v228, v228, v230, %[nh]\n\t" \
  "v_cndmask_b32 v229, v229, v231, %[nh]\n\t" \
  "v_pk_mul_f32 v[224:225], v[224:225], v[204:205] op_sel_hi:[1,0]\n\t" \
  "v_pk_mul_f32 v[228:229], v[228:229], v[204:205] op_sel_hi:[1,0]\n\t" \
  "v_cvt_pk_bf16_f32 v224, v224, v225\n\t" \
  "v_cvt_pk_bf16_f32 v225, v228, v229\n\t" \
  "buffer_store_dwordx2 v[224:225], v251, %[rs], %[so] offen sc1\n\t" \
  "s_memtime s[90:91]\n\t" \
  "s_setprio 0\n\t" \
  "s_waitcnt lgkmcnt(0)\n\t" \
  "s_mov_b32 %[tout], s90\n\t" \
  "s_mov_b32 %[spins], s92\n\t" \
  "s_branch .Lend%=\n\t" \
  ".Lre%=:\n\t" \
  "ds_read_b64_tr_b16 v[236:237], v250 offset:512\n\t" \
  "ds_read_b64_tr_b16 v[238:239], v250 offset:576\n\t" \
  "ds_read_b64_tr_b16 v[240:241], v250 offset:1024\n\t" \
  "ds_read_b64_tr_b16 v[242:243], v250 offset:1088\n\t" \
  "ds_read_b64_tr_b16 v[244:245], v250 offset:1536\n\t" \
  "ds_read_b64_tr_b16 v[246:247], v250 offset:1600\n\t" \
  "s_waitcnt lgkmcnt(0)\n\t" \
  "s_branch .Lrb%=\n\t" \
  ".Lvs%=:\n\t" \
  "v_cmp_eq_u32 vcc, %[sent], v252\n\t" \
  "v_cmp_eq_u32 s[94:95], %[sent], v253\n\t" \
  "s_or_b64 s[94:95], s[94:95], vcc\n\t" \
  "s_cbranch_scc0 .Lvok%=\n\t" \
  "s_add_u32 s92, s92, 1\n\t" \
  "s_sleep 2\n\t" \
  "s_and_b32 s93, s92, 63\n\t" \
  "s_cbranch_scc1 .Lvr%=\n\t" \
  "buffer_load_dword v204, off, %[rs], %[sab] sc1\n\t" \
  "s_waitcnt vmcnt(0)\n\t" \
  "v_readfirstlane_b32 s93, v204\n\t" \
  "s_nop 3\n\t" \
  "s_cmp_eq_u32 s93, 1\n\t" \
  "s_cbranch_scc1 .Lvok%=\n\t" \
  "s_cmp_lt_u32 s92, 0x80000\n\t" \
  "s_cbranch_scc1 .Lvr%=\n\t" \
  "v_mov_b32 v204, 1\n\t" \
  "buffer_store_dword v204, off, %[rs], %[sab] sc1\n\t" \
  "s_branch .Lvok%=\n\t" \
  ".Lvr%=:\n\t" \
  "buffer_load_dword v252, v249, %[rs], %[sv] offen offset:0 sc1\n\t" \
  "buffer_load_dword v253, v249, %[rs], %[sv] offen offset:256 sc1\n\t" \
  "s_waitcnt vmcnt(0)\n\t" \
  "v_mfma_f32_16x16x16_bf16 v[224:227], v[252:253], v[232:233], 0\n\t" \
  "v_mfma_f32_16x16x16_bf16 v[228:231], v[252:253], v[234:235], 0\n\t" \
  "v_max_u32 v204, v252, v253\n\t" \
  "v_cmp_le_u32 vcc, %[sent], v204\n\t" \
  "s_nop 4\n\t" \
  "s_branch .Lpv%=\n\t" \
  "s_branch .Lend%=\n\t" \
  ".Lslow%=:\n\t" \
  "s_mov_b64 s[94:95], 0\n\t" \
  "v_cmp_eq_u32 vcc, %[sent], v200\n\t" \
  "s_or_b64 s[94:95], s[94:95], vcc\n\t" \
  "v_cmp_eq_u32 vcc, %[sent], v201\n\t" \
  "s_or_b64 s[94:95], s[94:95], vcc\n\t" \
  "v_cmp_eq_u32 vcc, %[sent], v202\n\t" \
  "s_or_b64 s[94:95], s[94:95], vcc\n\t" \
  "v_cmp_eq_u32 vcc, %[sent], v203\n\t" \
  "s_or_b64 s[94:95], s[94:95], vcc\n\t" \
  "v_cmp_eq_u32 vcc, %[sent], v204\n\t" \
  "s_or_b64 s[94:95], s[94:95], vcc\n\t" \
  "v_cmp_eq_u32 vcc, %[sent], v205\n\t" \
  "s_or_b64 s[94:95], s[94:95], vcc\n\t" \
  "v_cmp_eq_u32 vcc, %[sent], v206\n\t" \
  "s_or_b64 s[94:95], s[94:95], vcc\n\t" \
  "v_cmp_eq_u32 vcc, %[sent], v207\n\t" \
  "s_or_b64 s[94:95], s[94:95], vcc\n\t" \
  "s_cmp_eq_u64 s[94:95], 0\n\t" \
  "s_cbranch_scc1 .Lok%=\n\t" \
  "v_mov_b64 v[192:193], 0\n\t" \
  "v_mov_b64 v[194:195], 0\n\t" \
  "v_mov_b64 v[196:197], 0\n\t" \
  "v_mov_b64 v[198:199], 0\n\t" \
  "s_add_u32 s92, s92, 1\n\t" \
  "s_cmp_eq_u32 s92, 1\n\t" \
  "s_cbranch_scc1 .Lpoll%=\n\t" \
  "s_sleep 2\n\t" \
  "s_and_b32 s93, s92, 63\n\t" \
  "s_cmp_lg_u32 s93, 0\n\t" \
  "s_cbranch_scc1 .Lpoll%=\n\t" \
  "v_mov_b32 v245, 0\n\t" \
  "buffer_load_dword v244, v245, %[rs], %[sab] offen sc1\n\t" \
  "s_waitcnt vmcnt(0)\n\t" \
  "v_readfirstlane_b32 s93, v244\n\t" \
  "s_nop 3\n\t" \
  "s_cmp_eq_u32 s93, 1\n\t" \
  "s_cbranch_scc1 .Lok%=\n\t" \
  "s_cmp_lt_u32 s92, 0x80000\n\t" \
  "s_cbranch_scc1 .Lpoll%=\n\t" \
  "v_mov_b32 v244, 1\n\t" \
  "buffer_store_dword v244, v245, %[rs], %[sab] offen sc1\n\t" \
  "s_branch .Lok%=\n\t" \
  ".Lend%=:\n\t"
#define QK_ASM_1 \
  "s_waitcnt lgkmcnt(0)\n\t" \
  "v_subrev_u32 v244, 0x8000, v250\n\t" \
  "ds_read_b128 v[216:219], v248 offset:36864\n\t" \
  "ds_read_b128 v[220:223], v248 offset:37888\n\t" \
  "ds_read_b128 v[224:227], v248 offset:38912\n\t" \
  "ds_read_b128 v[228:231], v248 offset:39936\n\t" \
  "ds_read_b128 v[208:211], v244 offset:0\n\t" \
  "ds_read_b128 v[212:215], v244 offset:1024\n\t" \
  "ds_read_b128 v[236:239], v244 offset:2048\n\t" \
  "ds_read_b128 v[240:243], v244 offset:3072\n\t" \
  "v_mov_b64 v[192:193], 0\n\t" \
  "v_mov_b64 v[194:195], 0\n\t" \
  "v_mov_b64 v[196:197], 0\n\t" \
  "v_mov_b64 v[198:199], 0\n\t" \
  "s_movk_i32 s92, 0x2000\n\t" \
  ".Lwt%=:\n\t" \
  "ds_read_b64 v[200:201], v248 offset:30720\n\t" \
  "s_memrealtime s[90:91]\n\t" \
  "s_sub_u32 s92, s92, 1\n\t" \
  "s_waitcnt lgkmcnt(0)\n\t" \
  "v_readfirstlane_b32 s93, v200\n\t" \
  "v_readfirstlane_b32 %[tgt], v201\n\t" \
  "s_cbranch_scc1 .Lgo%=\n\t" \
  "s_cmp_eq_u32 s93, %[step]\n\t" \
  "s_cbranch_scc1 .Lwx%=\n\t" \
  "s_sleep 1\n\t" \
  "s_branch .Lwt%=\n\t" \
  ".Lwx%=:\n\t" \
  "s_lshl_b32 s93, s90, 2\n\t" \
  "s_sub_i32 s93, %[tgt], s93\n\t" \
  "s_cmp_le_i32 s93, 0\n\t" \
  "s_cbranch_scc1 .Lgo%=\n\t" \
  "s_min_i32 s93, s93, 8000\n\t" \
  "s_mov_b32 s94, s93\n\t" \
  ".Lsl%=:\n\t" \
  "s_sub_i32 s94, s94, 18\n\t" \
  "s_cmp_lt_i32 s94, 30\n\t" \
  "s_cbranch_scc1 .Lsx%=\n\t" \
  "s_sleep 1\n\t" \
  "s_branch .Lsl%=\n\t" \
  ".Lsx%=:\n\t" \
  "s_sub_i32 s93, s93, 12\n\t" \
  "s_max_i32 s93, s93, 4\n\t" \
  "s_lshr_b32 s93, s93, 2\n\t" \
  "s_add_u32 s93, s93, s90\n\t" \
  "s_memrealtime s[90:91]\n\t" \
  ".Lpace%=:\n\t" \
  "s_memrealtime s[94:95]\n\t" \
  "s_waitcnt lgkmcnt(1)\n\t" \
  "s_sub_i32 s92, s90, s93\n\t" \
  "s_cmp_ge_i32 s92, 0\n\t" \
  "s_cbranch_scc1 .Le%=\n\t" \
  "s_memrealtime s[90:91]\n\t" \
  "s_waitcnt lgkmcnt(1)\n\t" \
  "s_sub_i32 s92, s94, s93\n\t" \
  "s_cmp_lt_i32 s92, 0\n\t" \
  "s_cbranch_scc1 .Lpace%=\n\t" \
  "s_nop 4\n\t" \
  ".Le%=:\n\t" \
  "s_add_u32 s92, s92, s93\n\t" \
  "s_lshl_b32 s92, s92, 2\n\t" \
  "s_sub_i32 s92, %[tgt], s92\n\t" \
  "s_max_i32 s92, s92, 0\n\t" \
  "s_bitcmp0_b32 s92, 3\n\t" \
  "s_cbranch_scc1 .Lf3%=\n\t" \
  "s_nop 15\n\t" \
  ".Lf3%=:\n\t" \
  "s_bitcmp0_b32 s92, 2\n\t" \
  "s_cbranch_scc1 .Lf2%=\n\t" \
  "s_nop 10\n\t" \
  ".Lf2%=:\n\t" \
  ".Lgo%=:\n\t" \
  "s_mov_b32 s92, 0\n\t" \
  ".Lpoll%=:\n\t" \
  "buffer_load_dwordx4 v[200:203], v248, %[rs], %[som] offen sc1\n\t" \
  "buffer_load_dwordx4 v[204:207], v248, %[rs], %[som2] offen sc1\n\t" \
  "s_waitcnt vmcnt(1) lgkmcnt(0)\n\t" \
  "ds_write_b128 v249, v[200:203]\n\t" \
  "v_smfmac_f32_16x16x64_bf16 v[192:195], v[200:203], v[208:215], v251\n\t" \
  "v_max3_u32 v244, v200, v201, v202\n\t" \
  "v_smfmac_f32_16x16x64_bf16 v[196:199], v[200:203], v[236:243], v251\n\t" \
  "s_waitcnt vmcnt(0)\n\t" \
  "ds_write_b128 v249, v[204:207] offset:8192\n\t" \
  "v_smfmac_f32_16x16x64_bf16 v[192:195], v[204:207], v[216:223], v251\n\t" \
  "v_max3_u32 v244, v244, v203, v204\n\t" \
  "v_max3_u32 v244, v244, v205, v206\n\t" \
  "v_smfmac_f32_16x16x64_bf16 v[196:199], v[204:207], v[224:231], v251\n\t" \
  "v_max_u32 v244, v244, v207\n\t" \
  "v_cmp_le_u32 vcc, %[sent], v244\n\t" \
  "s_cbranch_vccnz .Lslow%=\n\t" \
  ".Lok%=:\n\t" \
  "ds_read_b128 v[216:219], v250 offset:32768\n\t" \
  "ds_read_b128 v[220:223], v250 offset:33792\n\t" \
  "ds_read_b128 v[224:227], v250 offset:34816\n\t" \
  "ds_read_b128 v[228:231], v250 offset:35840\n\t" \
  "s_waitcnt lgkmcnt(4)\n\t" \
  "s_barrier\n\t" \
  "ds_read_b128 v[208:211], v248 offset:0\n\t" \
  "ds_read_b128 v[212:215], v248 offset:3072\n\t" \
  "ds_read_b128 v[236:239], v248 offset:4096\n\t" \
  "ds_read_b128 v[240:243], v248 offset:5120\n\t" \
  "s_waitcnt lgkmcnt(3)\n\t" \
  "ds_read_b128 v[244:247], v248 offset:12288\n\t" \
  "v_smfmac_f32_16x16x64_bf16 v[192:195], v[208:211], v[216:223], v251\n\t" \
  "ds_read_b128 v[216:219], v250 offset:0\n\t" \
  "v_smfmac_f32_16x16x64_bf16 v[196:199], v[208:211], v[224:231], v251\n\t" \
  "s_waitcnt lgkmcnt(4)\n\t" \
  "ds_read_b128 v[220:223], v250 offset:1024\n\t" \
  "v_smfmac_f32_16x16x64_bf16 v[192:195], v[212:215], v[0:7], v251\n\t" \
  "ds_read_b128 v[224:227], v250 offset:2048\n\t" \
  "v_smfmac_f32_16x16x64_bf16 v[196:199], v[212:215], v[8:15], v251\n\t" \
  "s_waitcnt lgkmcnt(5)\n\t" \
  "ds_read_b128 v[228:231], v250 offset:3072\n\t" \
  "v_smfmac_f32_16x16x64_bf16 v[192:195], v[236:239], v[16:23], v251\n\t" \
  "ds_read_b128 v[200:203], v248 offset:6144\n\t" \
  "v_smfmac_f32_16x16x64_bf16 v[196:199], v[236:239], v[24:31], v251\n\t" \
  "s_waitcnt lgkmcnt(6)\n\t" \
  "ds_read_b128 v[204:207], v248 offset:7168\n\t" \
  "v_smfmac_f32_16x16x64_bf16 v[192:195], v[240:243], v[32:39], v251\n\t" \
  "ds_read_b128 v[208:211], v248 offset:8192\n\t" \
  "v_smfmac_f32_16x16x64_bf16 v[196:199], v[240:243], v[40:47], v251\n\t" \
  "s_waitcnt lgkmcnt(5)\n\t" \
  "ds_read_b128 v[212:215], v248 offset:10240\n\t" \
  "v_smfmac_f32_16x16x64_bf16 v[192:195], v[244:247], v[216:223], v251\n\t" \
  "s_waitcnt lgkmcnt(4)\n\t" \
  "v_smfmac_f32_16x16x64_bf16 v[196:199], v[244:247], v[224:231], v251\n\t" \
  "s_waitcnt lgkmcnt(3)\n\t" \
  "ds_read_b128 v[236:239], v248 offset:11264\n\t" \
  "v_smfmac_f32_16x16x64_bf16 v[192:195], v[200:203], v[48:55], v251\n\t" \
  "ds_read_b128 v[240:243], v248 offset:13312\n\t" \
  "v_smfmac_f32_16x16x64_bf16 v[196:199], v[200:203], v[56:63], v251\n\t" \
  "s_waitcnt lgkmcnt(4)\n\t" \
  "v_smfmac_f32_16x16x64_bf16 v[192:195], v[204:207], v[64:71], v251\n\t" \
  "ds_read_b128 v[244:247], v248 offset:14336\n\t" \
  "v_smfmac_f32_16x16x64_bf16 v[196:199], v[204:207], v[72:79], v251\n\t" \
  "s_waitcnt lgkmcnt(4)\n\t" \
  "v_smfmac_f32_16x16x64_bf16 v[192:195], v[208:211], v[80:87], v251\n\t" \
  "ds_read_b128 v[200:203], v248 offset:15360\n\t" \
  "v_smfmac_f32_16x16x64_bf16 v[196:199], v[208:211], v[88:95], v251\n\t" \
  "s_waitcnt lgkmcnt(4)\n\t" \
  "v_smfmac_f32_16x16x64_bf16 v[192:195], v[212:215], v[96:103], v251\n\t" \
  "ds_read_b128 v[204:207], v248 offset:2048\n\t" \
  "v_smfmac_f32_16x16x64_bf16 v[196:199], v[212:215], v[104:111], v251\n\t" \
  "s_waitcnt lgkmcnt(3)\n\t" \
  "v_smfmac_f32_16x16x64_bf16 v[192:195], v[236:239], v[112:119], v251\n\t" \
  "v_smfmac_f32_16x16x64_bf16 v[196:199], v[236:239], v[120:127], v251\n\t" \
  "v_smfmac_f32_16x16x64_bf16 v[192:195], v[240:243], v[128:135], v251\n\t" \
  "v_smfmac_f32_16x16x64_bf16 v[196:199], v[240:243], v[136:143], v251\n\t" \
  "s_waitcnt lgkmcnt(1)\n\t" \
  "v_smfmac_f32_16x16x64_bf16 v[192:195], v[244:247], v[144:151], v251\n\t" \
  "v_smfmac_f32_16x16x64_bf16 v[196:199], v[244:247], v[152:159], v251\n\t" \
  "v_smfmac_f32_16x16x64_bf16 v[192:195], v[200:203], v[160:167], v251\n\t" \
  "v_smfmac_f32_16x16x64_bf16 v[196:199], v[200:203], v[168:175], v251\n\t" \
  "s_waitcnt lgkmcnt(0)\n\t" \
  "v_smfmac_f32_16x16x64_bf16 v[192:195], v[204:207], v[176:183], v251\n\t" \
  "v_smfmac_f32_16x16x64_bf16 v[196:199], v[204:207], v[184:191], v251\n\t" \
  "ds_read_b128 v[200:203], v248 offset:17408\n\t" \
  "ds_read_b128 v[204:207], v248 offset:21504\n\t" \
  "v_mov_b32 v250, v232\n\t" \
  "v_mov_b32 v249, v233\n\t" \
  "s_nop 2\n\t" \
  "v_add_f32 v192, v192, v194\n\t" \
  "v_add_f32 v193, v193, v195\n\t" \
  "v_add_f32 v196, v196, v198\n\t" \
  "v_add_f32 v197, v197, v199\n\t" \
  "v_cvt_pk_bf16_f32 v192, v192, v193\n\t" \
  "v_cvt_pk_bf16_f32 v196, v196, v197\n\t" \
  "ds_write2_b32 v252, v192, v196 offset1:64\n\t" \
  "v_mov_b32 v251, v234\n\t" \
  "ds_read_b64_tr_b16 v[236:237], v250 offset:512\n\t" \
  "ds_read_b64_tr_b16 v[238:239], v250 offset:576\n\t" \
  "v_mov_b32 v212, %[step]\n\t" \
  "s_mov_b64 exec, 1\n\t" \
  "ds_write_b32 v248, v212 offset:28688\n\t" \
  "s_mov_b64 exec, -1\n\t" \
  "buffer_load_dwordx4 v[192:195], v248, %[rt], %[svt] offen offset:0 sc1\n\t" \
  "buffer_load_dwordx4 v[196:199], v248, %[rt], %[svt] offen offset:1024 sc1\n\t" \
  "s_waitcnt lgkmcnt(1)\n\t" \
  "v_mfma_f32_16x16x32_bf16 v[200:203], v[200:203], v[236:239], 0\n\t" \
  "v_mfma_f32_16x16x32_bf16 v[204:207], v[204:207], v[236:239], 0\n\t" \
  "s_nop 7\n\t" \
  "v_cndmask_b32 v216, v200, v204, %[nh]\n\t" \
  "v_cndmask_b32 v217, v201, v205, %[nh]\n\t" \
  "v_cndmask_b32 v218, v202, v206, %[nh]\n\t" \
  "v_cndmask_b32 v219, v203, v207, %[nh]\n\t" \
  "ds_write_b128 v248, v[216:219] offset:17408\n\t" \
  "buffer_load_dword v252, v249, %[rs], %[sv] offen offset:0 sc1\n\t" \
  "buffer_load_dword v253, v249, %[rs], %[sv] offen offset:256 sc1\n\t" \
  "ds_read_b32 v212, v248 offset:28672\n\t" \
  "ds_read_b64_tr_b16 v[240:241], v250 offset:1024\n\t" \
  "ds_read_b64_tr_b16 v[242:243], v250 offset:1088\n\t" \
  "ds_read_b64_tr_b16 v[244:245], v250 offset:1536\n\t" \
  "ds_read_b64_tr_b16 v[246:247], v250 offset:1600\n\t" \
  "ds_read_b64_tr_b16 v[232:233], v250 offset:0\n\t" \
  "ds_read_b64_tr_b16 v[234:235], v250 offset:64\n\t" \
  "s_setprio 3\n\t" \
  "s_waitcnt lgkmcnt(0)\n\t" \
  "v_cmp_ne_u32 vcc, %[step], v212\n\t" \
  "s_barrier\n\t" \
  "s_and_b32 s93, vcc_lo, 0xf\n\t" \
  "s_cbranch_scc1 .Lre%=\n\t" \
  ".Lrb%=:\n\t" \
  "ds_read_b64_tr_b16 v[204:205], v250 offset:2560\n\t" \
  "ds_read_b64_tr_b16 v[206:207], v250 offset:2624\n\t" \
  "ds_read_b64_tr_b16 v[208:209], v250 offset:3072\n\t" \
  "ds_read_b64_tr_b16 v[210:211], v250 offset:3136\n\t" \
  "ds_read_b128 v[220:223], v248 offset:18432\n\t" \
  "ds_read_b128 v[224:227], v248 offset:19456\n\t" \
  "ds_read_b128 v[228:231], v248 offset:16384\n\t" \
  "ds_read_b64_tr_b16 v[212:213], v250 offset:3584\n\t" \
  "ds_read_b64_tr_b16 v[214:215], v250 offset:3648\n\t" \
  "ds_read_b64_tr_b16 v[200:201], v250 offset:2048\n\t" \
  "ds_read_b64_tr_b16 v[202:203], v250 offset:2112\n\t" \
  "s_waitcnt lgkmcnt(9)\n\t" \
  "v_mfma_f32_16x16x32_bf16 v[204:207], v[204:207], v[236:239], 0\n\t" \
  "s_waitcnt lgkmcnt(7)\n\t" \
  "v_mfma_f32_16x16x32_bf16 v[204:207], v[208:211], v[240:243], v[204:207]\n\t" \
  "s_waitcnt lgkmcnt(6)\n\t" \
  "v_pk_add_f32 v[216:217], v[216:217], v[220:221]\n\t" \
  "v_pk_add_f32 v[218:219], v[218:219], v[222:223]\n\t" \
  "s_waitcnt lgkmcnt(4)\n\t" \
  "v_pk_add_f32 v[224:225], v[224:225], v[228:229]\n\t" \
  "v_pk_add_f32 v[226:227], v[226:227], v[230:231]\n\t" \
  "v_pk_add_f32 v[216:217], v[216:217], v[224:225]\n\t" \
  "v_pk_add_f32 v[218:219], v[218:219], v[226:227]\n\t" \
  "s_waitcnt lgkmcnt(2)\n\t" \
  "v_mfma_f32_16x16x32_bf16 v[204:207], v[212:215], v[244:247], v[204:207]\n\t" \
  "v_max3_f32 v224, v216, v217, v218\n\t" \
  "v_max_f32 v224, v224, v219\n\t" \
  "s_waitcnt lgkmcnt(0)\n\t" \
  "v_mfma_f32_16x16x32_bf16 v[204:207], v[200:203], v[232:235], v[204:207]\n\t" \
  "v_max_f32_dpp v225, v224, v224 row_ror:8 row_mask:0xf bank_mask:0xf\n\t" \
  "v_max_f32_dpp v226, v224, v224 row_ror:8 row_mask:0xf bank_mask:0xf\n\t" \
  "s_nop 1\n\t" \
  "v_permlane32_swap_b32 v225, v226\n\t" \
  "v_max_f32 v231, v225, v226\n\t" \
  "v_mov_b32 v220, 0x3f803f80\n\t" \
  "v_mov_b32 v221, 0x3f803f80\n\t" \
  "v_mov_b64 v[222:223], v[220:221]\n\t" \
  "v_mov_b32 v237, 0\n\t" \
  "v_mov_b32 v238, 0\n\t" \
  "v_max3_f32 v208, v204, v205, v231\n\t" \
  "v_max3_f32 v209, v208, v206, v207\n\t" \
  "v_max3_f32 v210, v208, v206, v207\n\t" \
  "v_cndmask_b32 v204, v204, v206, %[up]\n\t" \
  "v_cndmask_b32 v205, v205, v207, %[up]\n\t" \
  "v_permlane16_swap_b32 v209, v210\n\t" \
  "v_max_f32 v208, v209, v210\n\t" \
  "v_mul_f32 v208, %[negc], v208\n\t" \
  "v_fma_f32 v204, v204, %[c], v208\n\t" \
  "v_fma_f32 v205, v205, %[c], v208\n\t" \
  "v_exp_f32 v204, v204\n\t" \
  "v_fma_f32 v216, v216, %[c], v208\n\t" \
  "v_exp_f32 v205, v205\n\t" \
  "v_fma_f32 v217, v217, %[c], v208\n\t" \
  "v_exp_f32 v216, v216\n\t" \
  "v_fma_f32 v218, v218, %[c], v208\n\t" \
  "v_cvt_pk_bf16_f32 v236, v204, v205\n\t" \
  "v_cvt_pk_bf16_f32 v239, v204, v205\n\t" \
  "v_exp_f32 v217, v217\n\t" \
  "v_fma_f32 v219, v219, %[c], v208\n\t" \
  "v_mfma_f32_16x16x16_bf16 v[200:203], v[220:221], v[236:237], 0\n\t" \
  "v_exp_f32 v218, v218\n\t" \
  "s_waitcnt vmcnt(0)\n\t" \
  "v_mfma_f32_16x16x16_bf16 v[224:227], v[252:253], v[236:237], 0\n\t" \
  "v_cvt_pk_bf16_f32 v214, v216, v217\n\t" \
  "v_cvt_pk_bf16_f32 v212, v216, v217\n\t" \
  "v_exp_f32 v219, v219\n\t" \
  "v_mfma_f32_16x16x16_bf16 v[228:231], v[252:253], v[238:239], 0\n\t" \
  "v_cvt_pk_bf16_f32 v215, v218, v219\n\t" \
  "v_cvt_pk_bf16_f32 v213, v218, v219\n\t" \
  "v_mov_b32_dpp v212, v214 row_ror:8 row_mask:0xf bank_mask:0xc\n\t" \
  "v_mov_b32_dpp v214, v214 row_ror:8 row_mask:0xf bank_mask:0x3\n\t" \
  "v_mov_b32_dpp v213, v215 row_ror:8 row_mask:0xf bank_mask:0xc\n\t" \
  "v_mov_b32_dpp v215, v215 row_ror:8 row_mask:0xf bank_mask:0x3\n\t" \
  "v_max_u32 v208, v252, v253\n\t" \
  "v_cmp_le_u32 vcc, %[sent], v208\n\t" \
  "v_mfma_f32_16x16x32_bf16 v[200:203], v[220:223], v[212:215], v[200:203]\n\t" \
  ".Lpv%=:\n\t" \
  "v_mfma_f32_16x16x32_bf16 v[224:227], v[192:195], v[212:215], v[224:227]\n\t" \
  "v_mfma_f32_16x16x32_bf16 v[228:231], v[196:199], v[212:215], v[228:231]\n\t" \
  "s_cbranch_vccnz .Lvs%=\n\t" \
  ".Lvok%=:\n\t" \
  "s_nop 4\n\t" \
  "v_rcp_f32 v208, v200\n\t" \
  "v_cndmask_b32 v224, v224, v226, %[nh]\n\t" \
  "v_cndmask_b32 v225, v225, v227, %[nh]\n\t" \
  "v_cndmask_b32 v228, v228, v230, %[nh]\n\t" \
  "v_cndmask_b32 v229, v229, v231, %[nh]\n\t" \
  "v_pk_mul_f32 v[224:225], v[224:225], v[208:209] op_sel_hi:[1,0]\n\t" \
  "v_pk_mul_f32 v[228:229], v[228:229], v[208:209] op_sel_hi:[1,0]\n\t" \
  "v_cvt_pk_bf16_f32 v224, v224, v225\n\t" \
  "v_cvt_pk_bf16_f32 v225, v228, v229\n\t" \
  "buffer_store_dwordx2 v[224:225], v251, %[rs], %[so] offen sc1\n\t" \
  "s_memtime s[90:91]\n\t" \
  "s_setprio 0\n\t" \
  "s_waitcnt lgkmcnt(0)\n\t" \
  "s_mov_b32 %[tout], s90\n\t" \
  "s_mov_b32 %[spins], s92\n\t" \
  "s_branch .Lend%=\n\t" \
  ".Lre%=:\n\t" \
  "ds_read_b64_tr_b16 v[240:241], v250 offset:1024\n\t" \
  "ds_read_b64_tr_b16 v[242:243], v250 offset:1088\n\t" \
  "ds_read_b64_tr_b16 v[244:245], v250 offset:1536\n\t" \
  "ds_read_b64_tr_b16 v[246:247], v250 offset:1600\n\t" \
  "ds_read_b64_tr_b16 v[232:233], v250 offset:0\n\t" \
  "ds_read_b64_tr_b16 v[234:235], v250 offset:64\n\t" \
  "s_waitcnt lgkmcnt(0)\n\t" \
  "s_branch .Lrb%=\n\t" \
  ".Lvs%=:\n\t" \
  "v_cmp_eq_u32 vcc, %[sent], v252\n\t" \
  "v_cmp_eq_u32 s[94:95], %[sent], v253\n\t" \
  "s_or_b64 s[94:95], s[94:95], vcc\n\t" \
  "s_cbranch_scc0 .Lvok%=\n\t" \
  "s_add_u32 s92, s92, 1\n\t" \
  "s_sleep 2\n\t" \
  "s_and_b32 s93, s92, 63\n\t" \
  "s_cbranch_scc1 .Lvr%=\n\t" \
  "buffer_load_dword v208, off, %[rs], %[sab] sc1\n\t" \
  "s_waitcnt vmcnt(0)\n\t" \
  "v_readfirstlane_b32 s93, v208\n\t" \
  "s_nop 3\n\t" \
  "s_cmp_eq_u32 s93, 1\n\t" \
  "s_cbranch_scc1 .Lvok%=\n\t" \
  "s_cmp_lt_u32 s92, 0x80000\n\t" \
  "s_cbranch_scc1 .Lvr%=\n\t" \
  "v_mov_b32 v208, 1\n\t" \
  "buffer_store_dword v208, off, %[rs], %[sab] sc1\n\t" \
  "s_branch .Lvok%=\n\t" \
  ".Lvr%=:\n\t" \
  "buffer_load_dword v252, v249, %[rs], %[sv] offen offset:0 sc1\n\t" \
  "buffer_load_dword v253, v249, %[rs], %[sv] offen offset:256 sc1\n\t" \
  "s_waitcnt vmcnt(0)\n\t" \
  "v_mfma_f32_16x16x16_bf16 v[224:227], v[252:253], v[236:237], 0\n\t" \
  "v_mfma_f32_16x16x16_bf16 v[228:231], v[252:253], v[238:239], 0\n\t" \
  "v_max_u32 v208, v252, v253\n\t" \
  "v_cmp_le_u32 vcc, %[sent], v208\n\t" \
  "s_nop 4\n\t" \
  "s_branch .Lpv%=\n\t" \
  "s_branch .Lend%=\n\t" \
  ".Lslow%=:\n\t" \
  "s_mov_b64 s[94:95], 0\n\t" \
  "v_cmp_eq_u32 vcc, %[sent], v200\n\t" \
  "s_or_b64 s[94:95], s[94:95], vcc\n\t" \
  "v_cmp_eq_u32 vcc, %[sent], v201\n\t" \
  "s_or_b64 s[94:95], s[94:95], vcc\n\t" \
  "v_cmp_eq_u32 vcc, %[sent], v202\n\t" \
  "s_or_b64 s[94:95], s[94:95], vcc\n\t" \
  "v_cmp_eq_u32 vcc, %[sent], v203\n\t" \
  "s_or_b64 s[94:95], s[94:95], vcc\n\t" \
  "v_cmp_eq_u32 vcc, %[sent], v204\n\t" \
  "s_or_b64 s[94:95], s[94:95], vcc\n\t" \
  "v_cmp_eq_u32 vcc, %[sent], v205\n\t" \
  "s_or_b64 s[94:95], s[94:95], vcc\n\t" \
  "v_cmp_eq_u32 vcc, %[sent], v206\n\t" \
  "s_or_b64 s[94:95], s[94:95], vcc\n\t" \
  "v_cmp_eq_u32 vcc, %[sent], v207\n\t" \
  "s_or_b64 s[94:95], s[94:95], vcc\n\t" \
  "s_cmp_eq_u64 s[94:95], 0\n\t" \
  "s_cbranch_scc1 .Lok%=\n\t" \
  "v_mov_b64 v[192:193], 0\n\t" \
  "v_mov_b64 v[194:195], 0\n\t" \
  "v_mov_b64 v[196:197], 0\n\t" \
  "v_mov_b64 v[198:199], 0\n\t" \
  "s_add_u32 s92, s92, 1\n\t" \
  "s_cmp_eq_u32 s92, 1\n\t" \
  "s_cbranch_scc1 .Lpoll%=\n\t" \
  "s_sleep 2\n\t" \
  "s_and_b32 s93, s92, 63\n\t" \
  "s_cmp_lg_u32 s93, 0\n\t" \
  "s_cbranch_scc1 .Lpoll%=\n\t" \
  "v_mov_b32 v245, 0\n\t" \
  "buffer_load_dword v244, v245, %[rs], %[sab] offen sc1\n\t" \
  "s_waitcnt vmcnt(0)\n\t" \
  "v_readfirstlane_b32 s93, v244\n\t" \
  "s_nop 3\n\t" \
  "s_cmp_eq_u32 s93, 1\n\t" \
  "s_cbranch_scc1 .Lok%=\n\t" \
  "s_cmp_lt_u32 s92, 0x80000\n\t" \
  "s_cbranch_scc1 .Lpoll%=\n\t" \
  "v_mov_b32 v244, 1\n\t" \
  "buffer_store_dword v244, v245, %[rs], %[sab] offen sc1\n\t" \
  "s_branch .Lok%=\n\t" \
  ".Lend%=:\n\t"
#define QK_ASM_2 \
  "s_waitcnt lgkmcnt(0)\n\t" \
  "v_subrev_u32 v244, 0x8000, v250\n\t" \
  "ds_read_b128 v[216:219], v248 offset:40960\n\t" \
  "ds_read_b128 v[220:223], v248 offset:41984\n\t" \
  "ds_read_b128 v[224:227], v248 offset:43008\n\t" \
  "ds_read_b128 v[228:231], v248 offset:44032\n\t" \
  "ds_read_b128 v[208:211], v244 offset:0\n\t" \
  "ds_read_b128 v[212:215], v244 offset:1024\n\t" \
  "ds_read_b128 v[236:239], v244 offset:2048\n\t" \
  "ds_read_b128 v[240:243], v244 offset:3072\n\t" \
  "v_mov_b64 v[192:193], 0\n\t" \
  "v_mov_b64 v[194:195], 0\n\t" \
  "v_mov_b64 v[196:197], 0\n\t" \
  "v_mov_b64 v[198:199], 0\n\t" \
  "s_movk_i32 s92, 0x2000\n\t" \
  ".Lwt%=:\n\t" \
  "ds_read_b64 v[200:201], v248 offset:30720\n\t" \
  "s_memrealtime s[90:91]\n\t" \
  "s_sub_u32 s92, s92, 1\n\t" \
  "s_waitcnt lgkmcnt(0)\n\t" \
  "v_readfirstlane_b32 s93, v200\n\t" \
  "v_readfirstlane_b32 %[tgt], v201\n\t" \
  "s_cbranch_scc1 .Lgo%=\n\t" \
  "s_cmp_eq_u32 s93, %[step]\n\t" \
  "s_cbranch_scc1 .Lwx%=\n\t" \
  "s_sleep 1\n\t" \
  "s_branch .Lwt%=\n\t" \
  ".Lwx%=:\n\t" \
  "s_lshl_b32 s93, s90, 2\n\t" \
  "s_sub_i32 s93, %[tgt], s93\n\t" \
  "s_cmp_le_i32 s93, 0\n\t" \
  "s_cbranch_scc1 .Lgo%=\n\t" \
  "s_min_i32 s93, s93, 8000\n\t" \
  "s_mov_b32 s94, s93\n\t" \
  ".Lsl%=:\n\t" \
  "s_sub_i32 s94, s94, 18\n\t" \
  "s_cmp_lt_i32 s94, 30\n\t" \
  "s_cbranch_scc1 .Lsx%=\n\t" \
  "s_sleep 1\n\t" \
  "s_branch .Lsl%=\n\t" \
  ".Lsx%=:\n\t" \
  "s_sub_i32 s93, s93, 12\n\t" \
  "s_max_i32 s93, s93, 4\n\t" \
  "s_lshr_b32 s93, s93, 2\n\t" \
  "s_add_u32 s93, s93, s90\n\t" \
  "s_memrealtime s[90:91]\n\t" \
  ".Lpace%=:\n\t" \
  "s_memrealtime s[94:95]\n\t" \
  "s_waitcnt lgkmcnt(1)\n\t" \
  "s_sub_i32 s92, s90, s93\n\t" \
  "s_cmp_ge_i32 s92, 0\n\t" \
  "s_cbranch_scc1 .Le%=\n\t" \
  "s_memrealtime s[90:91]\n\t" \
  "s_waitcnt lgkmcnt(1)\n\t" \
  "s_sub_i32 s92, s94, s93\n\t" \
  "s_cmp_lt_i32 s92, 0\n\t" \
  "s_cbranch_scc1 .Lpace%=\n\t" \
  "s_nop 4\n\t" \
  ".Le%=:\n\t" \
  "s_add_u32 s92, s92, s93\n\t" \
  "s_lshl_b32 s92, s92, 2\n\t" \
  "s_sub_i32 s92, %[tgt], s92\n\t" \
  "s_max_i32 s92, s92, 0\n\t" \
  "s_bitcmp0_b32 s92, 3\n\t" \
  "s_cbranch_scc1 .Lf3%=\n\t" \
  "s_nop 15\n\t" \
  ".Lf3%=:\n\t" \
  "s_bitcmp0_b32 s92, 2\n\t" \
  "s_cbranch_scc1 .Lf2%=\n\t" \
  "s_nop 10\n\t" \
  ".Lf2%=:\n\t" \
  ".Lgo%=:\n\t" \
  "s_mov_b32 s92, 0\n\t" \
  ".Lpoll%=:\n\t" \
  "buffer_load_dwordx4 v[200:203], v248, %[rs], %[som] offen sc1\n\t" \
  "buffer_load_dwordx4 v[204:207], v248, %[rs], %[som2] offen sc1\n\t" \
  "s_waitcnt vmcnt(1) lgkmcnt(0)\n\t" \
  "ds_write_b128 v249, v[200:203]\n\t" \
  "v_smfmac_f32_16x16x64_bf16 v[192:195], v[200:203], v[208:215], v251\n\t" \
  "v_max3_u32 v244, v200, v201, v202\n\t" \
  "v_smfmac_f32_16x16x64_bf16 v[196:199], v[200:203], v[236:243], v251\n\t" \
  "s_waitcnt vmcnt(0)\n\t" \
  "ds_write_b128 v249, v[204:207] offset:8192\n\t" \
  "v_smfmac_f32_16x16x64_bf16 v[192:195], v[204:207], v[216:223], v251\n\t" \
  "v_max3_u32 v244, v244, v203, v204\n\t" \
  "v_max3_u32 v244, v244, v205, v206\n\t" \
  "v_smfmac_f32_16x16x64_bf16 v[196:199], v[204:207], v[224:231], v251\n\t" \
  "v_max_u32 v244, v244, v207\n\t" \
  "v_cmp_le_u32 vcc, %[sent], v244\n\t" \
  "s_cbranch_vccnz .Lslow%=\n\t" \
  ".Lok%=:\n\t" \
  "ds_read_b128 v[216:219], v250 offset:32768\n\t" \
  "ds_read_b128 v[220:223], v250 offset:33792\n\t" \
  "ds_read_b128 v[224:227], v250 offset:34816\n\t" \
  "ds_read_b128 v[228:231], v250 offset:35840\n\t" \
  "s_waitcnt lgkmcnt(4)\n\t" \
  "s_barrier\n\t" \
  "ds_read_b128 v[208:211], v248 offset:3072\n\t" \
  "ds_read_b128 v[212:215], v248 offset:4096\n\t" \
  "ds_read_b128 v[236:239], v248 offset:5120\n\t" \
  "ds_read_b128 v[240:243], v248 offset:6144\n\t" \
  "s_waitcnt lgkmcnt(3)\n\t" \
  "ds_read_b128 v[244:247], v248 offset:13312\n\t" \
  "v_smfmac_f32_16x16x64_bf16 v[192:195], v[208:211], v[216:223], v251\n\t" \
  "ds_read_b128 v[216:219], v250 offset:0\n\t" \
  "v_smfmac_f32_16x16x64_bf16 v[196:199], v[208:211], v[224:231], v251\n\t" \
  "s_waitcnt lgkmcnt(4)\n\t" \
  "ds_read_b128 v[220:223], v250 offset:1024\n\t" \
  "v_smfmac_f32_16x16x64_bf16 v[192:195], v[212:215], v[0:7], v251\n\t" \
  "ds_read_b128 v[224:227], v250 offset:2048\n\t" \
  "v_smfmac_f32_16x16x64_bf16 v[196:199], v[212:215], v[8:15], v251\n\t" \
  "s_waitcnt lgkmcnt(5)\n\t" \
  "ds_read_b128 v[228:231], v250 offset:3072\n\t" \
  "v_smfmac_f32_16x16x64_bf16 v[192:195], v[236:239], v[16:23], v251\n\t" \
  "ds_read_b128 v[200:203], v248 offset:7168\n\t" \
  "v_smfmac_f32_16x16x64_bf16 v[196:199], v[236:239], v[24:31], v251\n\t" \
  "s_waitcnt lgkmcnt(6)\n\t" \
  "ds_read_b128 v[204:207], v248 offset:8192\n\t" \
  "v_smfmac_f32_16x16x64_bf16 v[192:195], v[240:243], v[32:39], v251\n\t" \
  "ds_read_b128 v[208:211], v248 offset:9216\n\t" \
  "v_smfmac_f32_16x16x64_bf16 v[196:199], v[240:243], v[40:47], v251\n\t" \
  "s_waitcnt lgkmcnt(5)\n\t" \
  "ds_read_b128 v[212:215], v248 offset:11264\n\t" \
  "v_smfmac_f32_16x16x64_bf16 v[192:195], v[244:247], v[216:223], v251\n\t" \
  "s_waitcnt lgkmcnt(4)\n\t" \
  "v_smfmac_f32_16x16x64_bf16 v[196:199], v[244:247], v[224:231], v251\n\t" \
  "s_waitcnt lgkmcnt(3)\n\t" \
  "ds_read_b128 v[236:239], v248 offset:12288\n\t" \
  "v_smfmac_f32_16x16x64_bf16 v[192:195], v[200:203], v[48:55], v251\n\t" \
  "ds_read_b128 v[240:243], v248 offset:14336\n\t" \
  "v_smfmac_f32_16x16x64_bf16 v[196:199], v[200:203], v[56:63], v251\n\t" \
  "s_waitcnt lgkmcnt(4)\n\t" \
  "v_smfmac_f32_16x16x64_bf16 v[192:195], v[204:207], v[64:71], v251\n\t" \
  "ds_read_b128 v[244:247], v248 offset:15360\n\t" \
  "v_smfmac_f32_16x16x64_bf16 v[196:199], v[204:207], v[72:79], v251\n\t" \
  "s_waitcnt lgkmcnt(4)\n\t" \
  "v_smfmac_f32_16x16x64_bf16 v[192:195], v[208:211], v[80:87], v251\n\t" \
  "ds_read_b128 v[200:203], v248 offset:0\n\t" \
  "v_smfmac_f32_16x16x64_bf16 v[196:199], v[208:211], v[88:95], v251\n\t" \
  "s_waitcnt lgkmcnt(4)\n\t" \
  "v_smfmac_f32_16x16x64_bf16 v[192:195], v[212:215], v[96:103], v251\n\t" \
  "ds_read_b128 v[204:207], v248 offset:1024\n\t" \
  "v_smfmac_f32_16x16x64_bf16 v[196:199], v[212:215], v[104:111], v251\n\t" \
  "s_waitcnt lgkmcnt(3)\n\t" \
  "v_smfmac_f32_16x16x64_bf16 v[192:195], v[236:239], v[112:119], v251\n\t" \
  "v_smfmac_f32_16x16x64_bf16 v[196:199], v[236:239], v[120:127], v251\n\t" \
  "v_smfmac_f32_16x16x64_bf16 v[192:195], v[240:243], v[128:135], v251\n\t" \
  "v_smfmac_f32_16x16x64_bf16 v[196:199], v[240:243], v[136:143], v251\n\t" \
  "s_waitcnt lgkmcnt(1)\n\t" \
  "v_smfmac_f32_16x16x64_bf16 v[192:195], v[244:247], v[144:151], v251\n\t" \
  "v_smfmac_f32_16x16x64_bf16 v[196:199], v[244:247], v[152:159], v251\n\t" \
  "v_smfmac_f32_16x16x64_bf16 v[192:195], v[200:203], v[160:167], v251\n\t" \
  "v_smfmac_f32_16x16x64_bf16 v[196:199], v[200:203], v[168:175], v251\n\t" \
  "s_waitcnt lgkmcnt(0)\n\t" \
  "v_smfmac_f32_16x16x64_bf16 v[192:195], v[204:207], v[176:183], v251\n\t" \
  "v_smfmac_f32_16x16x64_bf16 v[196:199], v[204:207], v[184:191], v251\n\t" \
  "ds_read_b128 v[200:203], v248 offset:18432\n\t" \
  "ds_read_b128 v[204:207], v248 offset:22528\n\t" \
  "v_mov_b32 v250, v232\n\t" \
  "v_mov_b32 v249, v233\n\t" \
  "s_nop 2\n\t" \
  "v_add_f32 v192, v192, v194\n\t" \
  "v_add_f32 v193, v193, v195\n\t" \
  "v_add_f32 v196, v196, v198\n\t" \
  "v_add_f32 v197, v197, v199\n\t" \
  "v_cvt_pk_bf16_f32 v192, v192, v193\n\t" \
  "v_cvt_pk_bf16_f32 v196, v196, v197\n\t" \
  "ds_write2_b32 v252, v192, v196 offset1:64\n\t" \
  "v_mov_b32 v251, v234\n\t" \
  "ds_read_b64_tr_b16 v[240:241], v250 offset:1024\n\t" \
  "ds_read_b64_tr_b16 v[242:243], v250 offset:1088\n\t" \
  "v_mov_b32 v212, %[step]\n\t" \
  "s_mov_b64 exec, 1\n\t" \
  "ds_write_b32 v248, v212 offset:28704\n\t" \
  "s_mov_b64 exec, -1\n\t" \
  "buffer_load_dwordx4 v[192:195], v248, %[rt], %[svt] offen offset:0 sc1\n\t" \
  "buffer_load_dwordx4 v[196:199], v248, %[rt], %[svt] offen offset:1024 sc1\n\t" \
  "s_waitcnt lgkmcnt(1)\n\t" \
  "v_mfma_f32_16x16x32_bf16 v[200:203], v[200:203], v[240:243], 0\n\t" \
  "v_mfma_f32_16x16x32_bf16 v[204:207], v[204:207], v[240:243], 0\n\t" \
  "s_nop 7\n\t" \
  "v_cndmask_b32 v216, v200, v204, %[nh]\n\t" \
  "v_cndmask_b32 v217, v201, v205, %[nh]\n\t" \
  "v_cndmask_b32 v218, v202, v206, %[nh]\n\t" \
  "v_cndmask_b32 v219, v203, v207, %[nh]\n\t" \
  "ds_write_b128 v248, v[216:219] offset:18432\n\t" \
  "buffer_load_dword v252, v249, %[rs], %[sv] offen offset:0 sc1\n\t" \
  "buffer_load_dword v253, v249, %[rs], %[sv] offen offset:256 sc1\n\t" \
  "ds_read_b32 v212, v248 offset:28672\n\t" \
  "ds_read_b64_tr_b16 v[244:245], v250 offset:1536\n\t" \
  "ds_read_b64_tr_b16 v[246:247], v250 offset:1600\n\t" \
  "ds_read_b64_tr_b16 v[232:233], v250 offset:0\n\t" \
  "ds_read_b64_tr_b16 v[234:235], v250 offset:64\n\t" \
  "ds_read_b64_tr_b16 v[236:237], v250 offset:512\n\t" \
  "ds_read_b64_tr_b16 v[238:239], v250 offset:576\n\t" \
  "s_setprio 3\n\t" \
  "s_waitcnt lgkmcnt(0)\n\t" \
  "v_cmp_ne_u32 vcc, %[step], v212\n\t" \
  "s_barrier\n\t" \
  "s_and_b32 s93, vcc_lo, 0xf\n\t" \
  "s_cbranch_scc1 .Lre%=\n\t" \
  ".Lrb%=:\n\t" \
  "ds_read_b64_tr_b16 v[208:209], v250 offset:3072\n\t" \
  "ds_read_b64_tr_b16 v[210:211], v250 offset:3136\n\t" \
  "ds_read_b64_tr_b16 v[212:213], v250 offset:3584\n\t" \
  "ds_read_b64_tr_b16 v[214:215], v250 offset:3648\n\t" \
  "ds_read_b128 v[220:223], v248 offset:19456\n\t" \
  "ds_read_b128 v[224:227], v248 offset:16384\n\t" \
  "ds_read_b128 v[228:231], v248 offset:17408\n\t" \
  "ds_read_b64_tr_b16 v[200:201], v250 offset:2048\n\t" \
  "ds_read_b64_tr_b16 v[202:203], v250 offset:2112\n\t" \
  "ds_read_b64_tr_b16 v[204:205], v250 offset:2560\n\t" \
  "ds_read_b64_tr_b16 v[206:207], v250 offset:2624\n\t" \
  "s_waitcnt lgkmcnt(9)\n\t" \
  "v_mfma_f32_16x16x32_bf16 v[208:211], v[208:211], v[240:243], 0\n\t" \
  "s_waitcnt lgkmcnt(7)\n\t" \
  "v_mfma_f32_16x16x32_bf16 v[208:211], v[212:215], v[244:247], v[208:211]\n\t" \
  "s_waitcnt lgkmcnt(6)\n\t" \
  "v_pk_add_f32 v[216:217], v[216:217], v[220:221]\n\t" \
  "v_pk_add_f32 v[218:219], v[218:219], v[222:223]\n\t" \
  "s_waitcnt lgkmcnt(4)\n\t" \
  "v_pk_add_f32 v[224:225], v[224:225], v[228:229]\n\t" \
  "v_pk_add_f32 v[226:227], v[226:227], v[230:231]\n\t" \
  "v_pk_add_f32 v[216:217], v[216:217], v[224:225]\n\t" \
  "v_pk_add_f32 v[218:219], v[218:219], v[226:227]\n\t" \
  "s_waitcnt lgkmcnt(2)\n\t" \
  "v_mfma_f32_16x16x32_bf16 v[208:211], v[200:203], v[232:235], v[208:211]\n\t" \
  "v_max3_f32 v224, v216, v217, v218\n\t" \
  "v_max_f32 v224, v224, v219\n\t" \
  "s_waitcnt lgkmcnt(0)\n\t" \
  "v_mfma_f32_16x16x32_bf16 v[208:211], v[204:207], v[236:239], v[208:211]\n\t" \
  "v_max_f32_dpp v225, v224, v224 row_ror:8 row_mask:0xf bank_mask:0xf\n\t" \
  "v_max_f32_dpp v226, v224, v224 row_ror:8 row_mask:0xf bank_mask:0xf\n\t" \
  "s_nop 1\n\t" \
  "v_permlane32_swap_b32 v225, v226\n\t" \
  "v_max_f32 v231, v225, v226\n\t" \
  "v_mov_b32 v220, 0x3f803f80\n\t" \
  "v_mov_b32 v221, 0x3f803f80\n\t" \
  "v_mov_b64 v[222:223], v[220:221]\n\t" \
  "v_mov_b32 v241, 0\n\t" \
  "v_mov_b32 v242, 0\n\t" \
  "v_max3_f32 v212, v208, v209, v231\n\t" \
  "v_max3_f32 v213, v212, v210, v211\n\t" \
  "v_max3_f32 v214, v212, v210, v211\n\t" \
  "v_cndmask_b32 v208, v208, v210, %[up]\n\t" \
  "v_cndmask_b32 v209, v209, v211, %[up]\n\t" \
  "v_permlane16_swap_b32 v213, v214\n\t" \
  "v_max_f32 v212, v213, v214\n\t" \
  "v_mul_f32 v212, %[negc], v212\n\t" \
  "v_fma_f32 v208, v208, %[c], v212\n\t" \
  "v_fma_f32 v209, v209, %[c], v212\n\t" \
  "v_exp_f32 v208, v208\n\t" \
  "v_fma_f32 v216, v216, %[c], v212\n\t" \
  "v_exp_f32 v209, v209\n\t" \
  "v_fma_f32 v217, v217, %[c], v212\n\t" \
  "v_exp_f32 v216, v216\n\t" \
  "v_fma_f32 v218, v218, %[c], v212\n\t" \
  "v_cvt_pk_bf16_f32 v240, v208, v209\n\t" \
  "v_cvt_pk_bf16_f32 v243, v208, v209\n\t" \
  "v_exp_f32 v217, v217\n\t" \
  "v_fma_f32 v219, v219, %[c], v212\n\t" \
  "v_mfma_f32_16x16x16_bf16 v[204:207], v[220:221], v[240:241], 0\n\t" \
  "v_exp_f32 v218, v218\n\t" \
  "s_waitcnt vmcnt(0)\n\t" \
  "v_mfma_f32_16x16x16_bf16 v[224:227], v[252:253], v[240:241], 0\n\t" \
  "v_cvt_pk_bf16_f32 v202, v216, v217\n\t" \
  "v_cvt_pk_bf16_f32 v200, v216, v217\n\t" \
  "v_exp_f32 v219, v219\n\t" \
  "v_mfma_f32_16x16x16_bf16 v[228:231], v[252:253], v[242:243], 0\n\t" \
  "v_cvt_pk_bf16_f32 v203, v218, v219\n\t" \
  "v_cvt_pk_bf16_f32 v201, v218, v219\n\t" \
  "v_mov_b32_dpp v200, v202 row_ror:8 row_mask:0xf bank_mask:0xc\n\t" \
  "v_mov_b32_dpp v202, v202 row_ror:8 row_mask:0xf bank_mask:0x3\n\t" \
  "v_mov_b32_dpp v201, v203 row_ror:8 row_mask:0xf bank_mask:0xc\n\t" \
  "v_mov_b32_dpp v203, v203 row_ror:8 row_mask:0xf bank_mask:0x3\n\t" \
  "v_max_u32 v212, v252, v253\n\t" \
  "v_cmp_le_u32 vcc, %[sent], v212\n\t" \
  "v_mfma_f32_16x16x32_bf16 v[204:207], v[220:223], v[200:203], v[204:207]\n\t" \
  ".Lpv%=:\n\t" \
  "v_mfma_f32_16x16x32_bf16 v[224:227], v[192:195], v[200:203], v[224:227]\n\t" \
  "v_mfma_f32_16x16x32_bf16 v[228:231], v[196:199], v[200:203], v[228:231]\n\t" \
  "s_cbranch_vccnz .Lvs%=\n\t" \
  ".Lvok%=:\n\t" \
  "s_nop 4\n\t" \
  "v_rcp_f32 v212, v204\n\t" \
  "v_cndmask_b32 v224, v224, v226, %[nh]\n\t" \
  "v_cndmask_b32 v225, v225, v227, %[nh]\n\t" \
  "v_cndmask_b32 v228, v228, v230, %[nh]\n\t" \
  "v_cndmask_b32 v229, v229, v231, %[nh]\n\t" \
  "v_pk_mul_f32 v[224:225], v[224:225], v[212:213] op_sel_hi:[1,0]\n\t" \
  "v_pk_mul_f32 v[228:229], v[228:229], v[212:213] op_sel_hi:[1,0]\n\t" \
  "v_cvt_pk_bf16_f32 v224, v224, v225\n\t" \
  "v_cvt_pk_bf16_f32 v225, v228, v229\n\t" \
  "buffer_store_dwordx2 v[224:225], v251, %[rs], %[so] offen sc1\n\t" \
  "s_memtime s[90:91]\n\t" \
  "s_setprio 0\n\t" \
  "s_waitcnt lgkmcnt(0)\n\t" \
  "s_mov_b32 %[tout], s90\n\t" \
  "s_mov_b32 %[spins], s92\n\t" \
  "s_branch .Lend%=\n\t" \
  ".Lre%=:\n\t" \
  "ds_read_b64_tr_b16 v[244:245], v250 offset:1536\n\t" \
  "ds_read_b64_tr_b16 v[246:247], v250 offset:1600\n\t" \
  "ds_read_b64_tr_b16 v[232:233], v250 offset:0\n\t" \
  "ds_read_b64_tr_b16 v[234:235], v250 offset:64\n\t" \
  "ds_read_b64_tr_b16 v[236:237], v250 offset:512\n\t" \
  "ds_read_b64_tr_b16 v[238:239], v250 offset:576\n\t" \
  "s_waitcnt lgkmcnt(0)\n\t" \
  "s_branch .Lrb%=\n\t" \
  ".Lvs%=:\n\t" \
  "v_cmp_eq_u32 vcc, %[sent], v252\n\t" \
  "v_cmp_eq_u32 s[94:95], %[sent], v253\n\t" \
  "s_or_b64 s[94:95], s[94:95], vcc\n\t" \
  "s_cbranch_scc0 .Lvok%=\n\t" \
  "s_add_u32 s92, s92, 1\n\t" \
  "s_sleep 2\n\t" \
  "s_and_b32 s93, s92, 63\n\t" \
  "s_cbranch_scc1 .Lvr%=\n\t" \
  "buffer_load_dword v212, off, %[rs], %[sab] sc1\n\t" \
  "s_waitcnt vmcnt(0)\n\t" \
  "v_readfirstlane_b32 s93, v212\n\t" \
  "s_nop 3\n\t" \
  "s_cmp_eq_u32 s93, 1\n\t" \
  "s_cbranch_scc1 .Lvok%=\n\t" \
  "s_cmp_lt_u32 s92, 0x80000\n\t" \
  "s_cbranch_scc1 .Lvr%=\n\t" \
  "v_mov_b32 v212, 1\n\t" \
  "buffer_store_dword v212, off, %[rs], %[sab] sc1\n\t" \
  "s_branch .Lvok%=\n\t" \
  ".Lvr%=:\n\t" \
  "buffer_load_dword v252, v249, %[rs], %[sv] offen offset:0 sc1\n\t" \
  "buffer_load_dword v253, v249, %[rs], %[sv] offen offset:256 sc1\n\t" \
  "s_waitcnt vmcnt(0)\n\t" \
  "v_mfma_f32_16x16x16_bf16 v[224:227], v[252:253], v[240:241], 0\n\t" \
  "v_mfma_f32_16x16x16_bf16 v[228:231], v[252:253], v[242:243], 0\n\t" \
  "v_max_u32 v212, v252, v253\n\t" \
  "v_cmp_le_u32 vcc, %[sent], v212\n\t" \
  "s_nop 4\n\t" \
  "s_branch .Lpv%=\n\t" \
  "s_branch .Lend%=\n\t" \
  ".Lslow%=:\n\t" \
  "s_mov_b64 s[94:95], 0\n\t" \
  "v_cmp_eq_u32 vcc, %[sent], v200\n\t" \
  "s_or_b64 s[94:95], s[94:95], vcc\n\t" \
  "v_cmp_eq_u32 vcc, %[sent], v201\n\t" \
  "s_or_b64 s[94:95], s[94:95], vcc\n\t" \
  "v_cmp_eq_u32 vcc, %[sent], v202\n\t" \
  "s_or_b64 s[94:95], s[94:95], vcc\n\t" \
  "v_cmp_eq_u32 vcc, %[sent], v203\n\t" \
  "s_or_b64 s[94:95], s[94:95], vcc\n\t" \
  "v_cmp_eq_u32 vcc, %[sent], v204\n\t" \
  "s_or_b64 s[94:95], s[94:95], vcc\n\t" \
  "v_cmp_eq_u32 vcc, %[sent], v205\n\t" \
  "s_or_b64 s[94:95], s[94:95], vcc\n\t" \
  "v_cmp_eq_u32 vcc, %[sent], v206\n\t" \
  "s_or_b64 s[94:95], s[94:95], vcc\n\t" \
  "v_cmp_eq_u32 vcc, %[sent], v207\n\t" \
  "s_or_b64 s[94:95], s[94:95], vcc\n\t" \
  "s_cmp_eq_u64 s[94:95], 0\n\t" \
  "s_cbranch_scc1 .Lok%=\n\t" \
  "v_mov_b64 v[192:193], 0\n\t" \
  "v_mov_b64 v[194:195], 0\n\t" \
  "v_mov_b64 v[196:197], 0\n\t" \
  "v_mov_b64 v[198:199], 0\n\t" \
  "s_add_u32 s92, s92, 1\n\t" \
  "s_cmp_eq_u32 s92, 1\n\t" \
  "s_cbranch_scc1 .Lpoll%=\n\t" \
  "s_sleep 2\n\t" \
  "s_and_b32 s93, s92, 63\n\t" \
  "s_cmp_lg_u32 s93, 0\n\t" \
  "s_cbranch_scc1 .Lpoll%=\n\t" \
  "v_mov_b32 v245, 0\n\t" \
  "buffer_load_dword v244, v245, %[rs], %[sab] offen sc1\n\t" \
  "s_waitcnt vmcnt(0)\n\t" \
  "v_readfirstlane_b32 s93, v244\n\t" \
  "s_nop 3\n\t" \
  "s_cmp_eq_u32 s93, 1\n\t" \
  "s_cbranch_scc1 .Lok%=\n\t" \
  "s_cmp_lt_u32 s92, 0x80000\n\t" \
  "s_cbranch_scc1 .Lpoll%=\n\t" \
  "v_mov_b32 v244, 1\n\t" \
  "buffer_store_dword v244, v245, %[rs], %[sab] offen sc1\n\t" \
  "s_branch .Lok%=\n\t" \
  ".Lend%=:\n\t"
#define QK_ASM_3 \
  "s_waitcnt lgkmcnt(0)\n\t" \
  "v_subrev_u32 v244, 0x8000, v250\n\t" \
  "ds_read_b128 v[216:219], v248 offset:45056\n\t" \
  "ds_read_b128 v[220:223], v248 offset:46080\n\t" \
  "ds_read_b128 v[224:227], v248 offset:47104\n\t" \
  "ds_read_b128 v[228:231], v248 offset:48128\n\t" \
  "ds_read_b128 v[208:211], v244 offset:0\n\t" \
  "ds_read_b128 v[212:215], v244 offset:1024\n\t" \
  "ds_read_b128 v[236:239], v244 offset:2048\n\t" \
  "ds_read_b128 v[240:243], v244 offset:3072\n\t" \
  "v_mov_b64 v[192:193], 0\n\t" \
  "v_mov_b64 v[194:195], 0\n\t" \
  "v_mov_b64 v[196:197], 0\n\t" \
  "v_mov_b64 v[198:199], 0\n\t" \
  "s_movk_i32 s92, 0x2000\n\t" \
  ".Lwt%=:\n\t" \
  "ds_read_b64 v[200:201], v248 offset:30720\n\t" \
  "s_memrealtime s[90:91]\n\t" \
  "s_sub_u32 s92, s92, 1\n\t" \
  "s_waitcnt lgkmcnt(0)\n\t" \
  "v_readfirstlane_b32 s93, v200\n\t" \
  "v_readfirstlane_b32 %[tgt], v201\n\t" \
  "s_cbranch_scc1 .Lgo%=\n\t" \
  "s_cmp_eq_u32 s93, %[step]\n\t" \
  "s_cbranch_scc1 .Lwx%=\n\t" \
  "s_sleep 1\n\t" \
  "s_branch .Lwt%=\n\t" \
  ".Lwx%=:\n\t" \
  "s_lshl_b32 s93, s90, 2\n\t" \
  "s_sub_i32 s93, %[tgt], s93\n\t" \
  "s_cmp_le_i32 s93, 0\n\t" \
  "s_cbranch_scc1 .Lgo%=\n\t" \
  "s_min_i32 s93, s93, 8000\n\t" \
  "s_mov_b32 s94, s93\n\t" \
  ".Lsl%=:\n\t" \
  "s_sub_i32 s94, s94, 18\n\t" \
  "s_cmp_lt_i32 s94, 30\n\t" \
  "s_cbranch_scc1 .Lsx%=\n\t" \
  "s_sleep 1\n\t" \
  "s_branch .Lsl%=\n\t" \
  ".Lsx%=:\n\t" \
  "s_sub_i32 s93, s93, 12\n\t" \
  "s_max_i32 s93, s93, 4\n\t" \
  "s_lshr_b32 s93, s93, 2\n\t" \
  "s_add_u32 s93, s93, s90\n\t" \
  "s_memrealtime s[90:91]\n\t" \
  ".Lpace%=:\n\t" \
  "s_memrealtime s[94:95]\n\t" \
  "s_waitcnt lgkmcnt(1)\n\t" \
  "s_sub_i32 s92, s90, s93\n\t" \
  "s_cmp_ge_i32 s92, 0\n\t" \
  "s_cbranch_scc1 .Le%=\n\t" \
  "s_memrealtime s[90:91]\n\t" \
  "s_waitcnt lgkmcnt(1)\n\t" \
  "s_sub_i32 s92, s94, s93\n\t" \
  "s_cmp_lt_i32 s92, 0\n\t" \
  "s_cbranch_scc1 .Lpace%=\n\t" \
  "s_nop 4\n\t" \
  ".Le%=:\n\t" \
  "s_add_u32 s92, s92, s93\n\t" \
  "s_lshl_b32 s92, s92, 2\n\t" \
  "s_sub_i32 s92, %[tgt], s92\n\t" \
  "s_max_i32 s92, s92, 0\n\t" \
  "s_bitcmp0_b32 s92, 3\n\t" \
  "s_cbranch_scc1 .Lf3%=\n\t" \
  "s_nop 15\n\t" \
  ".Lf3%=:\n\t" \
  "s_bitcmp0_b32 s92, 2\n\t" \
  "s_cbranch_scc1 .Lf2%=\n\t" \
  "s_nop 10\n\t" \
  ".Lf2%=:\n\t" \
  ".Lgo%=:\n\t" \
  "s_mov_b32 s92, 0\n\t" \
  ".Lpoll%=:\n\t" \
  "buffer_load_dwordx4 v[200:203], v248, %[rs], %[som] offen sc1\n\t" \
  "buffer_load_dwordx4 v[204:207], v248, %[rs], %[som2] offen sc1\n\t" \
  "s_waitcnt vmcnt(1) lgkmcnt(0)\n\t" \
  "ds_write_b128 v249, v[200:203]\n\t" \
  "v_smfmac_f32_16x16x64_bf16 v[192:195], v[200:203], v[208:215], v251\n\t" \
  "v_max3_u32 v244, v200, v201, v202\n\t" \
  "v_smfmac_f32_16x16x64_bf16 v[196:199], v[200:203], v[236:243], v251\n\t" \
  "s_waitcnt vmcnt(0)\n\t" \
  "ds_write_b128 v249, v[204:207] offset:8192\n\t" \
  "v_smfmac_f32_16x16x64_bf16 v[192:195], v[204:207], v[216:223], v251\n\t" \
  "v_max3_u32 v244, v244, v203, v204\n\t" \
  "v_max3_u32 v244, v244, v205, v206\n\t" \
  "v_smfmac_f32_16x16x64_bf16 v[196:199], v[204:207], v[224:231], v251\n\t" \
  "v_max_u32 v244, v244, v207\n\t" \
  "v_cmp_le_u32 vcc, %[sent], v244\n\t" \
  "s_cbranch_vccnz .Lslow%=\n\t" \
  ".Lok%=:\n\t" \
  "ds_read_b128 v[216:219], v250 offset:32768\n\t" \
  "ds_read_b128 v[220:223], v250 offset:33792\n\t" \
  "ds_read_b128 v[224:227], v250 offset:34816\n\t" \
  "ds_read_b128 v[228:231], v250 offset:35840\n\t" \
  "s_waitcnt lgkmcnt(4)\n\t" \
  "s_barrier\n\t" \
  "ds_read_b128 v[208:211], v248 offset:2048\n\t" \
  "ds_read_b128 v[212:215], v248 offset:5120\n\t" \
  "ds_read_b128 v[236:239], v248 offset:6144\n\t" \
  "ds_read_b128 v[240:243], v248 offset:7168\n\t" \
  "s_waitcnt lgkmcnt(3)\n\t" \
  "ds_read_b128 v[244:247], v248 offset:12288\n\t" \
  "v_smfmac_f32_16x16x64_bf16 v[192:195], v[208:211], v[216:223], v251\n\t" \
  "ds_read_b128 v[216:219], v250 offset:0\n\t" \
  "v_smfmac_f32_16x16x64_bf16 v[196:199], v[208:211], v[224:231], v251\n\t" \
  "s_waitcnt lgkmcnt(4)\n\t" \
  "ds_read_b128 v[220:223], v250 offset:1024\n\t" \
  "v_smfmac_f32_16x16x64_bf16 v[192:195], v[212:215], v[0:7], v251\n\t" \
  "ds_read_b128 v[224:227], v250 offset:2048\n\t" \
  "v_smfmac_f32_16x16x64_bf16 v[196:199], v[212:215], v[8:15], v251\n\t" \
  "s_waitcnt lgkmcnt(5)\n\t" \
  "ds_read_b128 v[228:231], v250 offset:3072\n\t" \
  "v_smfmac_f32_16x16x64_bf16 v[192:195], v[236:239], v[16:23], v251\n\t" \
  "ds_read_b128 v[200:203], v248 offset:8192\n\t" \
  "v_smfmac_f32_16x16x64_bf16 v[196:199], v[236:239], v[24:31], v251\n\t" \
  "s_waitcnt lgkmcnt(6)\n\t" \
  "ds_read_b128 v[204:207], v248 offset:9216\n\t" \
  "v_smfmac_f32_16x16x64_bf16 v[192:195], v[240:243], v[32:39], v251\n\t" \
  "ds_read_b128 v[208:211], v248 offset:10240\n\t" \
  "v_smfmac_f32_16x16x64_bf16 v[196:199], v[240:243], v[40:47], v251\n\t" \
  "s_waitcnt lgkmcnt(5)\n\t" \
  "ds_read_b128 v[212:215], v248 offset:13312\n\t" \
  "v_smfmac_f32_16x16x64_bf16 v[192:195], v[244:247], v[216:223], v251\n\t" \
  "s_waitcnt lgkmcnt(4)\n\t" \
  "v_smfmac_f32_16x16x64_bf16 v[196:199], v[244:247], v[224:231], v251\n\t" \
  "s_waitcnt lgkmcnt(3)\n\t" \
  "ds_read_b128 v[236:239], v248 offset:14336\n\t" \
  "v_smfmac_f32_16x16x64_bf16 v[192:195], v[200:203], v[48:55], v251\n\t" \
  "ds_read_b128 v[240:243], v248 offset:15360\n\t" \
  "v_smfmac_f32_16x16x64_bf16 v[196:199], v[200:203], v[56:63], v251\n\t" \
  "s_waitcnt lgkmcnt(4)\n\t" \
  "v_smfmac_f32_16x16x64_bf16 v[192:195], v[204:207], v[64:71], v251\n\t" \
  "ds_read_b128 v[244:247], v248 offset:0\n\t" \
  "v_smfmac_f32_16x16x64_bf16 v[196:199], v[204:207], v[72:79], v251\n\t" \
  "s_waitcnt lgkmcnt(4)\n\t" \
  "v_smfmac_f32_16x16x64_bf16 v[192:195], v[208:211], v[80:87], v251\n\t" \
  "ds_read_b128 v[200:203], v248 offset:1024\n\t" \
  "v_smfmac_f32_16x16x64_bf16 v[196:199], v[208:211], v[88:95], v251\n\t" \
  "s_waitcnt lgkmcnt(4)\n\t" \
  "v_smfmac_f32_16x16x64_bf16 v[192:195], v[212:215], v[96:103], v251\n\t" \
  "ds_read_b128 v[204:207], v248 offset:4096\n\t" \
  "v_smfmac_f32_16x16x64_bf16 v[196:199], v[212:215], v[104:111], v251\n\t" \
  "s_waitcnt lgkmcnt(3)\n\t" \
  "v_smfmac_f32_16x16x64_bf16 v[192:195], v[236:239], v[112:119], v251\n\t" \
  "v_smfmac_f32_16x16x64_bf16 v[196:199], v[236:239], v[120:127], v251\n\t" \
  "v_smfmac_f32_16x16x64_bf16 v[192:195], v[240:243], v[128:135], v251\n\t" \
  "v_smfmac_f32_16x16x64_bf16 v[196:199], v[240:243], v[136:143], v251\n\t" \
  "s_waitcnt lgkmcnt(1)\n\t" \
  "v_smfmac_f32_16x16x64_bf16 v[192:195], v[244:247], v[144:151], v251\n\t" \
  "v_smfmac_f32_16x16x64_bf16 v[196:199], v[244:247], v[152:159], v251\n\t" \
  "v_smfmac_f32_16x16x64_bf16 v[192:195], v[200:203], v[160:167], v251\n\t" \
  "v_smfmac_f32_16x16x64_bf16 v[196:199], v[200:203], v[168:175], v251\n\t" \
  "s_waitcnt lgkmcnt(0)\n\t" \
  "v_smfmac_f32_16x16x64_bf16 v[192:195], v[204:207], v[176:183], v251\n\t" \
  "v_smfmac_f32_16x16x64_bf16 v[196:199], v[204:207], v[184:191], v251\n\t" \
  "ds_read_b128 v[200:203], v248 offset:19456\n\t" \
  "ds_read_b128 v[204:207], v248 offset:23552\n\t" \
  "v_mov_b32 v250, v232\n\t" \
  "v_mov_b32 v249, v233\n\t" \
  "s_nop 2\n\t" \
  "v_add_f32 v192, v192, v194\n\t" \
  "v_add_f32 v193, v193, v195\n\t" \
  "v_add_f32 v196, v196, v198\n\t" \
  "v_add_f32 v197, v197, v199\n\t" \
  "v_cvt_pk_bf16_f32 v192, v192, v193\n\t" \
  "v_cvt_pk_bf16_f32 v196, v196, v197\n\t" \
  "ds_write2_b32 v252, v192, v196 offset1:64\n\t" \
  "v_mov_b32 v251, v234\n\t" \
  "ds_read_b64_tr_b16 v[244:245], v250 offset:1536\n\t" \
  "ds_read_b64_tr_b16 v[246:247], v250 offset:1600\n\t" \
  "v_mov_b32 v212, %[step]\n\t" \
  "s_mov_b64 exec, 1\n\t" \
  "ds_write_b32 v248, v212 offset:28720\n\t" \
  "s_mov_b64 exec, -1\n\t" \
  "buffer_load_dwordx4 v[192:195], v248, %[rt], %[svt] offen offset:0 sc1\n\t" \
  "buffer_load_dwordx4 v[196:199], v248, %[rt], %[svt] offen offset:1024 sc1\n\t" \
  "s_waitcnt lgkmcnt(1)\n\t" \
  "v_mfma_f32_16x16x32_bf16 v[200:203], v[200:203], v[244:247], 0\n\t" \
  "v_mfma_f32_16x16x32_bf16 v[204:207], v[204:207], v[244:247], 0\n\t" \
  "s_nop 7\n\t" \
  "v_cndmask_b32 v216, v200, v204, %[nh]\n\t" \
  "v_cndmask_b32 v217, v201, v205, %[nh]\n\t" \
  "v_cndmask_b32 v218, v202, v206, %[nh]\n\t" \
  "v_cndmask_b32 v219, v203, v207, %[nh]\n\t" \
  "ds_write_b128 v248, v[216:219] offset:19456\n\t" \
  "buffer_load_dword v252, v249, %[rs], %[sv] offen offset:0 sc1\n\t" \
  "buffer_load_dword v253, v249, %[rs], %[sv] offen offset:256 sc1\n\t" \
  "ds_read_b32 v212, v248 offset:28672\n\t" \
  "ds_read_b64_tr_b16 v[232:233], v250 offset:0\n\t" \
  "ds_read_b64_tr_b16 v[234:235], v250 offset:64\n\t" \
  "ds_read_b64_tr_b16 v[236:237], v250 offset:512\n\t" \
  "ds_read_b64_tr_b16 v[238:239], v250 offset:576\n\t" \
  "ds_read_b64_tr_b16 v[240:241], v250 offset:1024\n\t" \
  "ds_read_b64_tr_b16 v[242:243], v250 offset:1088\n\t" \
  "s_setprio 3\n\t" \
  "s_waitcnt lgkmcnt(0)\n\t" \
  "v_cmp_ne_u32 vcc, %[step], v212\n\t" \
  "s_barrier\n\t" \
  "s_and_b32 s93, vcc_lo, 0xf\n\t" \
  "s_cbranch_scc1 .Lre%=\n\t" \
  ".Lrb%=:\n\t" \
  "ds_read_b64_tr_b16 v[212:213], v250 offset:3584\n\t" \
  "ds_read_b64_tr_b16 v[214:215], v250 offset:3648\n\t" \
  "ds_read_b64_tr_b16 v[200:201], v250 offset:2048\n\t" \
  "ds_read_b64_tr_b16 v[202:203], v250 offset:2112\n\t" \
  "ds_read_b128 v[220:223], v248 offset:16384\n\t" \
  "ds_read_b128 v[224:227], v248 offset:17408\n\t" \
  "ds_read_b128 v[228:231], v248 offset:18432\n\t" \
  "ds_read_b64_tr_b16 v[204:205], v250 offset:2560\n\t" \
  "ds_read_b64_tr_b16 v[206:207], v250 offset:2624\n\t" \
  "ds_read_b64_tr_b16 v[208:209], v250 offset:3072\n\t" \
  "ds_read_b64_tr_b16 v[210:211], v250 offset:3136\n\t" \
  "s_waitcnt lgkmcnt(9)\n\t" \
  "v_mfma_f32_16x16x32_bf16 v[212:215], v[212:215], v[244:247], 0\n\t" \
  "s_waitcnt lgkmcnt(7)\n\t" \
  "v_mfma_f32_16x16x32_bf16 v[212:215], v[200:203], v[232:235], v[212:215]\n\t" \
  "s_waitcnt lgkmcnt(6)\n\t" \
  "v_pk_add_f32 v[216:217], v[216:217], v[220:221]\n\t" \
  "v_pk_add_f32 v[218:219], v[218:219], v[222:223]\n\t" \
  "s_waitcnt lgkmcnt(4)\n\t" \
  "v_pk_add_f32 v[224:225], v[224:225], v[228:229]\n\t" \
  "v_pk_add_f32 v[226:227], v[226:227], v[230:231]\n\t" \
  "v_pk_add_f32 v[216:217], v[216:217], v[224:225]\n\t" \
  "v_pk_add_f32 v[218:219], v[218:219], v[226:227]\n\t" \
  "s_waitcnt lgkmcnt(2)\n\t" \
  "v_mfma_f32_16x16x32_bf16 v[212:215], v[204:207], v[236:239], v[212:215]\n\t" \
  "v_max3_f32 v224, v216, v217, v218\n\t" \
  "v_max_f32 v224, v224, v219\n\t" \
  "s_waitcnt lgkmcnt(0)\n\t" \
  "v_mfma_f32_16x16x32_bf16 v[212:215], v[208:211], v[240:243], v[212:215]\n\t" \
  "v_max_f32_dpp v225, v224, v224 row_ror:8 row_mask:0xf bank_mask:0xf\n\t" \
  "v_max_f32_dpp v226, v224, v224 row_ror:8 row_mask:0xf bank_mask:0xf\n\t" \
  "s_nop 1\n\t" \
  "v_permlane32_swap_b32 v225, v226\n\t" \
  "v_max_f32 v231, v225, v226\n\t" \
  "v_mov_b32 v220, 0x3f803f80\n\t" \
  "v_mov_b32 v221, 0x3f803f80\n\t" \
  "v_mov_b64 v[222:223], v[220:221]\n\t" \
  "v_mov_b32 v245, 0\n\t" \
  "v_mov_b32 v246, 0\n\t" \
  "v_max3_f32 v200, v212, v213, v231\n\t" \
  "v_max3_f32 v201, v200, v214, v215\n\t" \
  "v_max3_f32 v202, v200, v214, v215\n\t" \
  "v_cndmask_b32 v212, v212, v214, %[up]\n\t" \
  "v_cndmask_b32 v213, v213, v215, %[up]\n\t" \
  "v_permlane16_swap_b32 v201, v202\n\t" \
  "v_max_f32 v200, v201, v202\n\t" \
  "v_mul_f32 v200, %[negc], v200\n\t" \
  "v_fma_f32 v212, v212, %[c], v200\n\t" \
  "v_fma_f32 v213, v213, %[c], v200\n\t" \
  "v_exp_f32 v212, v212\n\t" \
  "v_fma_f32 v216, v216, %[c], v200\n\t" \
  "v_exp_f32 v213, v213\n\t" \
  "v_fma_f32 v217, v217, %[c], v200\n\t" \
  "v_exp_f32 v216, v216\n\t" \
  "v_fma_f32 v218, v218, %[c], v200\n\t" \
  "v_cvt_pk_bf16_f32 v244, v212, v213\n\t" \
  "v_cvt_pk_bf16_f32 v247, v212, v213\n\t" \
  "v_exp_f32 v217, v217\n\t" \
  "v_fma_f32 v219, v219, %[c], v200\n\t" \
  "v_mfma_f32_16x16x16_bf16 v[208:211], v[220:221], v[244:245], 0\n\t" \
  "v_exp_f32 v218, v218\n\t" \
  "s_waitcnt vmcnt(0)\n\t" \
  "v_mfma_f32_16x16x16_bf16 v[224:227], v[252:253], v[244:245], 0\n\t" \
  "v_cvt_pk_bf16_f32 v206, v216, v217\n\t" \
  "v_cvt_pk_bf16_f32 v204, v216, v217\n\t" \
  "v_exp_f32 v219, v219\n\t" \
  "v_mfma_f32_16x16x16_bf16 v[228:231], v[252:253], v[246:247], 0\n\t" \
  "v_cvt_pk_bf16_f32 v207, v218, v219\n\t" \
  "v_cvt_pk_bf16_f32 v205, v218, v219\n\t" \
  "v_mov_b32_dpp v204, v206 row_ror:8 row_mask:0xf bank_mask:0xc\n\t" \
  "v_mov_b32_dpp v206, v206 row_ror:8 row_mask:0xf bank_mask:0x3\n\t" \
  "v_mov_b32_dpp v205, v207 row_ror:8 row_mask:0xf bank_mask:0xc\n\t" \
  "v_mov_b32_dpp v207, v207 row_ror:8 row_mask:0xf bank_mask:0x3\n\t" \
  "v_max_u32 v200, v252, v253\n\t" \
  "v_cmp_le_u32 vcc, %[sent], v200\n\t" \
  "v_mfma_f32_16x16x32_bf16 v[208:211], v[220:223], v[204:207], v[208:211]\n\t" \
  ".Lpv%=:\n\t" \
  "v_mfma_f32_16x16x32_bf16 v[224:227], v[192:195], v[204:207], v[224:227]\n\t" \
  "v_mfma_f32_16x16x32_bf16 v[228:231], v[196:199], v[204:207], v[228:231]\n\t" \
  "s_cbranch_vccnz .Lvs%=\n\t" \
  ".Lvok%=:\n\t" \
  "s_nop 4\n\t" \
  "v_rcp_f32 v200, v208\n\t" \
  "v_cndmask_b32 v224, v224, v226, %[nh]\n\t" \
  "v_cndmask_b32 v225, v225, v227, %[nh]\n\t" \
  "v_cndmask_b32 v228, v228, v230, %[nh]\n\t" \
  "v_cndmask_b32 v229, v229, v231, %[nh]\n\t" \
  "v_pk_mul_f32 v[224:225], v[224:225], v[200:201] op_sel_hi:[1,0]\n\t" \
  "v_pk_mul_f32 v[228:229], v[228:229], v[200:201] op_sel_hi:[1,0]\n\t" \
  "v_cvt_pk_bf16_f32 v224, v224, v225\n\t" \
  "v_cvt_pk_bf16_f32 v225, v228, v229\n\t" \
  "buffer_store_dwordx2 v[224:225], v251, %[rs], %[so] offen sc1\n\t" \
  "s_memtime s[90:91]\n\t" \
  "s_setprio 0\n\t" \
  "s_waitcnt lgkmcnt(0)\n\t" \
  "s_mov_b32 %[tout], s90\n\t" \
  "s_mov_b32 %[spins], s92\n\t" \
  "s_branch .Lend%=\n\t" \
  ".Lre%=:\n\t" \
  "ds_read_b64_tr_b16 v[232:233], v250 offset:0\n\t" \
  "ds_read_b64_tr_b16 v[234:235], v250 offset:64\n\t" \
  "ds_read_b64_tr_b16 v[236:237], v250 offset:512\n\t" \
  "ds_read_b64_tr_b16 v[238:239], v250 offset:576\n\t" \
  "ds_read_b64_tr_b16 v[240:241], v250 offset:1024\n\t" \
  "ds_read_b64_tr_b16 v[242:243], v250 offset:1088\n\t" \
  "s_waitcnt lgkmcnt(0)\n\t" \
  "s_branch .Lrb%=\n\t" \
  ".Lvs%=:\n\t" \
  "v_cmp_eq_u32 vcc, %[sent], v252\n\t" \
  "v_cmp_eq_u32 s[94:95], %[sent], v253\n\t" \
  "s_or_b64 s[94:95], s[94:95], vcc\n\t" \
  "s_cbranch_scc0 .Lvok%=\n\t" \
  "s_add_u32 s92, s92, 1\n\t" \
  "s_sleep 2\n\t" \
  "s_and_b32 s93, s92, 63\n\t" \
  "s_cbranch_scc1 .Lvr%=\n\t" \
  "buffer_load_dword v200, off, %[rs], %[sab] sc1\n\t" \
  "s_waitcnt vmcnt(0)\n\t" \
  "v_readfirstlane_b32 s93, v200\n\t" \
  "s_nop 3\n\t" \
  "s_cmp_eq_u32 s93, 1\n\t" \
  "s_cbranch_scc1 .Lvok%=\n\t" \
  "s_cmp_lt_u32 s92, 0x80000\n\t" \
  "s_cbranch_scc1 .Lvr%=\n\t" \
  "v_mov_b32 v200, 1\n\t" \
  "buffer_store_dword v200, off, %[rs], %[sab] sc1\n\t" \
  "s_branch .Lvok%=\n\t" \
  ".Lvr%=:\n\t" \
  "buffer_load_dword v252, v249, %[rs], %[sv] offen offset:0 sc1\n\t" \
  "buffer_load_dword v253, v249, %[rs], %[sv] offen offset:256 sc1\n\t" \
  "s_waitcnt vmcnt(0)\n\t" \
  "v_mfma_f32_16x16x16_bf16 v[224:227], v[252:253], v[244:245], 0\n\t" \
  "v_mfma_f32_16x16x16_bf16 v[228:231], v[252:253], v[246:247], 0\n\t" \
  "v_max_u32 v200, v252, v253\n\t" \
  "v_cmp_le_u32 vcc, %[sent], v200\n\t" \
  "s_nop 4\n\t" \
  "s_branch .Lpv%=\n\t" \
  "s_branch .Lend%=\n\t" \
  ".Lslow%=:\n\t" \
  "s_mov_b64 s[94:95], 0\n\t" \
  "v_cmp_eq_u32 vcc, %[sent], v200\n\t" \
  "s_or_b64 s[94:95], s[94:95], vcc\n\t" \
  "v_cmp_eq_u32 vcc, %[sent], v201\n\t" \
  "s_or_b64 s[94:95], s[94:95], vcc\n\t" \
  "v_cmp_eq_u32 vcc, %[sent], v202\n\t" \
  "s_or_b64 s[94:95], s[94:95], vcc\n\t" \
  "v_cmp_eq_u32 vcc, %[sent], v203\n\t" \
  "s_or_b64 s[94:95], s[94:95], vcc\n\t" \
  "v_cmp_eq_u32 vcc, %[sent], v204\n\t" \
  "s_or_b64 s[94:95], s[94:95], vcc\n\t" \
  "v_cmp_eq_u32 vcc, %[sent], v205\n\t" \
  "s_or_b64 s[94:95], s[94:95], vcc\n\t" \
  "v_cmp_eq_u32 vcc, %[sent], v206\n\t" \
  "s_or_b64 s[94:95], s[94:95], vcc\n\t" \
  "v_cmp_eq_u32 vcc, %[sent], v207\n\t" \
  "s_or_b64 s[94:95], s[94:95], vcc\n\t" \
  "s_cmp_eq_u64 s[94:95], 0\n\t" \
  "s_cbranch_scc1 .Lok%=\n\t" \
  "v_mov_b64 v[192:193], 0\n\t" \
  "v_mov_b64 v[194:195], 0\n\t" \
  "v_mov_b64 v[196:197], 0\n\t" \
  "v_mov_b64 v[198:199], 0\n\t" \
  "s_add_u32 s92, s92, 1\n\t" \
  "s_cmp_eq_u32 s92, 1\n\t" \
  "s_cbranch_scc1 .Lpoll%=\n\t" \
  "s_sleep 2\n\t" \
  "s_and_b32 s93, s92, 63\n\t" \
  "s_cmp_lg_u32 s93, 0\n\t" \
  "s_cbranch_scc1 .Lpoll%=\n\t" \
  "v_mov_b32 v245, 0\n\t" \
  "buffer_load_dword v244, v245, %[rs], %[sab] offen sc1\n\t" \
  "s_waitcnt vmcnt(0)\n\t" \
  "v_readfirstlane_b32 s93, v244\n\t" \
  "s_nop 3\n\t" \
  "s_cmp_eq_u32 s93, 1\n\t" \
  "s_cbranch_scc1 .Lok%=\n\t" \
  "s_cmp_lt_u32 s92, 0x80000\n\t" \
  "s_cbranch_scc1 .Lpoll%=\n\t" \
  "v_mov_b32 v244, 1\n\t" \
  "buffer_store_dword v244, v245, %[rs], %[sab] offen sc1\n\t" \
  "s_branch .Lok%=\n\t" \
  ".Lend%=:\n\t"
#define QK_ASM_4 \
  "s_waitcnt lgkmcnt(0)\n\t" \
  "v_subrev_u32 v244, 0x8000, v250\n\t" \
  "ds_read_b128 v[216:219], v248 offset:49152\n\t" \
  "ds_read_b128 v[220:223], v248 offset:50176\n\t" \
  "ds_read_b128 v[224:227], v248 offset:51200\n\t" \
  "ds_read_b128 v[228:231], v248 offset:52224\n\t" \
  "ds_read_b128 v[208:211], v244 offset:0\n\t" \
  "ds_read_b128 v[212:215], v244 offset:1024\n\t" \
  "ds_read_b128 v[236:239], v244 offset:2048\n\t" \
  "ds_read_b128 v[240:243], v244 offset:3072\n\t" \
  "v_mov_b64 v[192:193], 0\n\t" \
  "v_mov_b64 v[194:195], 0\n\t" \
  "v_mov_b64 v[196:197], 0\n\t" \
  "v_mov_b64 v[198:199], 0\n\t" \
  "s_movk_i32 s92, 0x2000\n\t" \
  ".Lwt%=:\n\t" \
  "ds_read_b64 v[200:201], v248 offset:30720\n\t" \
  "s_memrealtime s[90:91]\n\t" \
  "s_sub_u32 s92, s92, 1\n\t" \
  "s_waitcnt lgkmcnt(0)\n\t" \
  "v_readfirstlane_b32 s93, v200\n\t" \
  "v_readfirstlane_b32 %[tgt], v201\n\t" \
  "s_cbranch_scc1 .Lgo%=\n\t" \
  "s_cmp_eq_u32 s93, %[step]\n\t" \
  "s_cbranch_scc1 .Lwx%=\n\t" \
  "s_sleep 1\n\t" \
  "s_branch .Lwt%=\n\t" \
  ".Lwx%=:\n\t" \
  "s_lshl_b32 s93, s90, 2\n\t" \
  "s_sub_i32 s93, %[tgt], s93\n\t" \
  "s_cmp_le_i32 s93, 0\n\t" \
  "s_cbranch_scc1 .Lgo%=\n\t" \
  "s_min_i32 s93, s93, 8000\n\t" \
  "s_mov_b32 s94, s93\n\t" \
  ".Lsl%=:\n\t" \
  "s_sub_i32 s94, s94, 18\n\t" \
  "s_cmp_lt_i32 s94, 30\n\t" \
  "s_cbranch_scc1 .Lsx%=\n\t" \
  "s_sleep 1\n\t" \
  "s_branch .Lsl%=\n\t" \
  ".Lsx%=:\n\t" \
  "s_sub_i32 s93, s93, 12\n\t" \
  "s_max_i32 s93, s93, 4\n\t" \
  "s_lshr_b32 s93, s93, 2\n\t" \
  "s_add_u32 s93, s93, s90\n\t" \
  "s_memrealtime s[90:91]\n\t" \
  ".Lpace%=:\n\t" \
  "s_memrealtime s[94:95]\n\t" \
  "s_waitcnt lgkmcnt(1)\n\t" \
  "s_sub_i32 s92, s90, s93\n\t" \
  "s_cmp_ge_i32 s92, 0\n\t" \
  "s_cbranch_scc1 .Le%=\n\t" \
  "s_memrealtime s[90:91]\n\t" \
  "s_waitcnt lgkmcnt(1)\n\t" \
  "s_sub_i32 s92, s94, s93\n\t" \
  "s_cmp_lt_i32 s92, 0\n\t" \
  "s_cbranch_scc1 .Lpace%=\n\t" \
  "s_nop 4\n\t" \
  ".Le%=:\n\t" \
  "s_add_u32 s92, s92, s93\n\t" \
  "s_lshl_b32 s92, s92, 2\n\t" \
  "s_sub_i32 s92, %[tgt], s92\n\t" \
  "s_max_i32 s92, s92, 0\n\t" \
  "s_bitcmp0_b32 s92, 3\n\t" \
  "s_cbranch_scc1 .Lf3%=\n\t" \
  "s_nop 15\n\t" \
  ".Lf3%=:\n\t" \
  "s_bitcmp0_b32 s92, 2\n\t" \
  "s_cbranch_scc1 .Lf2%=\n\t" \
  "s_nop 10\n\t" \
  ".Lf2%=:\n\t" \
  ".Lgo%=:\n\t" \
  "s_mov_b32 s92, 0\n\t" \
  ".Lpoll%=:\n\t" \
  "buffer_load_dwordx4 v[200:203], v248, %[rs], %[som] offen sc1\n\t" \
  "buffer_load_dwordx4 v[204:207], v248, %[rs], %[som2] offen sc1\n\t" \
  "s_waitcnt vmcnt(1) lgkmcnt(0)\n\t" \
  "ds_write_b128 v249, v[200:203]\n\t" \
  "v_smfmac_f32_16x16x64_bf16 v[192:195], v[200:203], v[208:215], v251\n\t" \
  "v_max3_u32 v244, v200, v201, v202\n\t" \
  "v_smfmac_f32_16x16x64_bf16 v[196:199], v[200:203], v[236:243], v251\n\t" \
  "s_waitcnt vmcnt(0)\n\t" \
  "ds_write_b128 v249, v[204:207] offset:8192\n\t" \
  "v_smfmac_f32_16x16x64_bf16 v[192:195], v[204:207], v[216:223], v251\n\t" \
  "v_max3_u32 v244, v244, v203, v204\n\t" \
  "v_max3_u32 v244, v244, v205, v206\n\t" \
  "v_smfmac_f32_16x16x64_bf16 v[196:199], v[204:207], v[224:231], v251\n\t" \
  "v_max_u32 v244, v244, v207\n\t" \
  "v_cmp_le_u32 vcc, %[sent], v244\n\t" \
  "s_cbranch_vccnz .Lslow%=\n\t" \
  ".Lok%=:\n\t" \
  "ds_read_b128 v[216:219], v250 offset:32768\n\t" \
  "ds_read_b128 v[220:223], v250 offset:33792\n\t" \
  "ds_read_b128 v[224:227], v250 offset:34816\n\t" \
  "ds_read_b128 v[228:231], v250 offset:35840\n\t" \
  "s_waitcnt lgkmcnt(4)\n\t" \
  "s_barrier\n\t" \
  "ds_read_b128 v[208:211], v248 offset:5120\n\t" \
  "ds_read_b128 v[212:215], v248 offset:6144\n\t" \
  "ds_read_b128 v[236:239], v248 offset:7168\n\t" \
  "ds_read_b128 v[240:243], v248 offset:8192\n\t" \
  "s_waitcnt lgkmcnt(3)\n\t" \
  "ds_read_b128 v[244:247], v248 offset:13312\n\t" \
  "v_smfmac_f32_16x16x64_bf16 v[192:195], v[208:211], v[216:223], v251\n\t" \
  "ds_read_b128 v[216:219], v250 offset:0\n\t" \
  "v_smfmac_f32_16x16x64_bf16 v[196:199], v[208:211], v[224:231], v251\n\t" \
  "s_waitcnt lgkmcnt(4)\n\t" \
  "ds_read_b128 v[220:223], v250 offset:1024\n\t" \
  "v_smfmac_f32_16x16x64_bf16 v[192:195], v[212:215], v[0:7], v251\n\t" \
  "ds_read_b128 v[224:227], v250 offset:2048\n\t" \
  "v_smfmac_f32_16x16x64_bf16 v[196:199], v[212:215], v[8:15], v251\n\t" \
  "s_waitcnt lgkmcnt(5)\n\t" \
  "ds_read_b128 v[228:231], v250 offset:3072\n\t" \
  "v_smfmac_f32_16x16x64_bf16 v[192:195], v[236:239], v[16:23], v251\n\t" \
  "ds_read_b128 v[200:203], v248 offset:9216\n\t" \
  "v_smfmac_f32_16x16x64_bf16 v[196:199], v[236:239], v[24:31], v251\n\t" \
  "s_waitcnt lgkmcnt(6)\n\t" \
  "ds_read_b128 v[204:207], v248 offset:10240\n\t" \
  "v_smfmac_f32_16x16x64_bf16 v[192:195], v[240:243], v[32:39], v251\n\t" \
  "ds_read_b128 v[208:211], v248 offset:11264\n\t" \
  "v_smfmac_f32_16x16x64_bf16 v[196:199], v[240:243], v[40:47], v251\n\t" \
  "s_waitcnt lgkmcnt(5)\n\t" \
  "ds_read_b128 v[212:215], v248 offset:14336\n\t" \
  "v_smfmac_f32_16x16x64_bf16 v[192:195], v[244:247], v[216:223], v251\n\t" \
  "s_waitcnt lgkmcnt(4)\n\t" \
  "v_smfmac_f32_16x16x64_bf16 v[196:199], v[244:247], v[224:231], v251\n\t" \
  "s_waitcnt lgkmcnt(3)\n\t" \
  "ds_read_b128 v[236:239], v248 offset:15360\n\t" \
  "v_smfmac_f32_16x16x64_bf16 v[192:195], v[200:203], v[48:55], v251\n\t" \
  "ds_read_b128 v[240:243], v248 offset:0\n\t" \
  "v_smfmac_f32_16x16x64_bf16 v[196:199], v[200:203], v[56:63], v251\n\t" \
  "s_waitcnt lgkmcnt(4)\n\t" \
  "v_smfmac_f32_16x16x64_bf16 v[192:195], v[204:207], v[64:71], v251\n\t" \
  "ds_read_b128 v[244:247], v248 offset:1024\n\t" \
  "v_smfmac_f32_16x16x64_bf16 v[196:199], v[204:207], v[72:79], v251\n\t" \
  "s_waitcnt lgkmcnt(4)\n\t" \
  "v_smfmac_f32_16x16x64_bf16 v[192:195], v[208:211], v[80:87], v251\n\t" \
  "ds_read_b128 v[200:203], v248 offset:2048\n\t" \
  "v_smfmac_f32_16x16x64_bf16 v[196:199], v[208:211], v[88:95], v251\n\t" \
  "s_waitcnt lgkmcnt(4)\n\t" \
  "v_smfmac_f32_16x16x64_bf16 v[192:195], v[212:215], v[96:103], v251\n\t" \
  "ds_read_b128 v[204:207], v248 offset:3072\n\t" \
  "v_smfmac_f32_16x16x64_bf16 v[196:199], v[212:215], v[104:111], v251\n\t" \
  "s_waitcnt lgkmcnt(3)\n\t" \
  "v_smfmac_f32_16x16x64_bf16 v[192:195], v[236:239], v[112:119], v251\n\t" \
  "v_smfmac_f32_16x16x64_bf16 v[196:199], v[236:239], v[120:127], v251\n\t" \
  "v_smfmac_f32_16x16x64_bf16 v[192:195], v[240:243], v[128:135], v251\n\t" \
  "v_smfmac_f32_16x16x64_bf16 v[196:199], v[240:243], v[136:143], v251\n\t" \
  "s_waitcnt lgkmcnt(1)\n\t" \
  "v_smfmac_f32_16x16x64_bf16 v[192:195], v[244:247], v[144:151], v251\n\t" \
  "v_smfmac_f32_16x16x64_bf16 v[196:199], v[244:247], v[152:159], v251\n\t" \
  "v_smfmac_f32_16x16x64_bf16 v[192:195], v[200:203], v[160:167], v251\n\t" \
  "v_smfmac_f32_16x16x64_bf16 v[196:199], v[200:203], v[168:175], v251\n\t" \
  "s_waitcnt lgkmcnt(0)\n\t" \
  "v_smfmac_f32_16x16x64_bf16 v[192:195], v[204:207], v[176:183], v251\n\t" \
  "v_smfmac_f32_16x16x64_bf16 v[196:199], v[204:207], v[184:191], v251\n\t" \
  "s_nop 6\n\t" \
  "v_add_f32 v192, v192, v194\n\t" \
  "v_add_f32 v193, v193, v195\n\t" \
  "v_add_f32 v196, v196, v198\n\t" \
  "v_add_f32 v197, v197, v199\n\t" \
  "v_cvt_pk_bf16_f32 v192, v192, v193\n\t" \
  "v_cvt_pk_bf16_f32 v196, v196, v197\n\t" \
  "ds_write2_b32 v252, v192, v196 offset1:64\n\t" \
  "s_waitcnt lgkmcnt(0)\n\t" \
  "s_barrier\n\t" \
  "buffer_store_dword v192, v235, %[rs], %[sk] offen sc1\n\t" \
  "buffer_store_dword v196, v235, %[rs], %[sk] offen offset:256 sc1\n\t" \
  "s_mov_b32 %[spins], s92\n\t" \
  "s_branch .Lend%=\n\t" \
  ".Lslow%=:\n\t" \
  "s_mov_b64 s[94:95], 0\n\t" \
  "v_cmp_eq_u32 vcc, %[sent], v200\n\t" \
  "s_or_b64 s[94:95], s[94:95], vcc\n\t" \
  "v_cmp_eq_u32 vcc, %[sent], v201\n\t" \
  "s_or_b64 s[94:95], s[94:95], vcc\n\t" \
  "v_cmp_eq_u32 vcc, %[sent], v202\n\t" \
  "s_or_b64 s[94:95], s[94:95], vcc\n\t" \
  "v_cmp_eq_u32 vcc, %[sent], v203\n\t" \
  "s_or_b64 s[94:95], s[94:95], vcc\n\t" \
  "v_cmp_eq_u32 vcc, %[sent], v204\n\t" \
  "s_or_b64 s[94:95], s[94:95], vcc\n\t" \
  "v_cmp_eq_u32 vcc, %[sent], v205\n\t" \
  "s_or_b64 s[94:95], s[94:95], vcc\n\t" \
  "v_cmp_eq_u32 vcc, %[sent], v206\n\t" \
  "s_or_b64 s[94:95], s[94:95], vcc\n\t" \
  "v_cmp_eq_u32 vcc, %[sent], v207\n\t" \
  "s_or_b64 s[94:95], s[94:95], vcc\n\t" \
  "s_cmp_eq_u64 s[94:95], 0\n\t" \
  "s_cbranch_scc1 .Lok%=\n\t" \
  "v_mov_b64 v[192:193], 0\n\t" \
  "v_mov_b64 v[194:195], 0\n\t" \
  "v_mov_b64 v[196:197], 0\n\t" \
  "v_mov_b64 v[198:199], 0\n\t" \
  "s_add_u32 s92, s92, 1\n\t" \
  "s_cmp_eq_u32 s92, 1\n\t" \
  "s_cbranch_scc1 .Lpoll%=\n\t" \
  "s_sleep 2\n\t" \
  "s_and_b32 s93, s92, 63\n\t" \
  "s_cmp_lg_u32 s93, 0\n\t" \
  "s_cbranch_scc1 .Lpoll%=\n\t" \
  "v_mov_b32 v245, 0\n\t" \
  "buffer_load_dword v244, v245, %[rs], %[sab] offen sc1\n\t" \
  "s_waitcnt vmcnt(0)\n\t" \
  "v_readfirstlane_b32 s93, v244\n\t" \
  "s_nop 3\n\t" \
  "s_cmp_eq_u32 s93, 1\n\t" \
  "s_cbranch_scc1 .Lok%=\n\t" \
  "s_cmp_lt_u32 s92, 0x80000\n\t" \
  "s_cbranch_scc1 .Lpoll%=\n\t" \
  "v_mov_b32 v244, 1\n\t" \
  "buffer_store_dword v244, v245, %[rs], %[sab] offen sc1\n\t" \
  "s_branch .Lok%=\n\t" \
  ".Lend%=:\n\t"
#define QK_ASM_5 \
  "s_waitcnt lgkmcnt(0)\n\t" \
  "v_subrev_u32 v244, 0x8000, v250\n\t" \
  "ds_read_b128 v[216:219], v248 offset:53248\n\t" \
  "ds_read_b128 v[220:223], v248 offset:54272\n\t" \
  "ds_read_b128 v[224:227], v248 offset:55296\n\t" \
  "ds_read_b128 v[228:231], v248 offset:56320\n\t" \
  "ds_read_b128 v[208:211], v244 offset:0\n\t" \
  "ds_read_b128 v[212:215], v244 offset:1024\n\t" \
  "ds_read_b128 v[236:239], v244 offset:2048\n\t" \
  "ds_read_b128 v[240:243], v244 offset:3072\n\t" \
  "v_mov_b64 v[192:193], 0\n\t" \
  "v_mov_b64 v[194:195], 0\n\t" \
  "v_mov_b64 v[196:197], 0\n\t" \
  "v_mov_b64 v[198:199], 0\n\t" \
  "s_movk_i32 s92, 0x2000\n\t" \
  ".Lwt%=:\n\t" \
  "ds_read_b64 v[200:201], v248 offset:30720\n\t" \
  "s_memrealtime s[90:91]\n\t" \
  "s_sub_u32 s92, s92, 1\n\t" \
  "s_waitcnt lgkmcnt(0)\n\t" \
  "v_readfirstlane_b32 s93, v200\n\t" \
  "v_readfirstlane_b32 %[tgt], v201\n\t" \
  "s_cbranch_scc1 .Lgo%=\n\t" \
  "s_cmp_eq_u32 s93, %[step]\n\t" \
  "s_cbranch_scc1 .Lwx%=\n\t" \
  "s_sleep 1\n\t" \
  "s_branch .Lwt%=\n\t" \
  ".Lwx%=:\n\t" \
  "s_lshl_b32 s93, s90, 2\n\t" \
  "s_sub_i32 s93, %[tgt], s93\n\t" \
  "s_cmp_le_i32 s93, 0\n\t" \
  "s_cbranch_scc1 .Lgo%=\n\t" \
  "s_min_i32 s93, s93, 8000\n\t" \
  "s_mov_b32 s94, s93\n\t" \
  ".Lsl%=:\n\t" \
  "s_sub_i32 s94, s94, 18\n\t" \
  "s_cmp_lt_i32 s94, 30\n\t" \
  "s_cbranch_scc1 .Lsx%=\n\t" \
  "s_sleep 1\n\t" \
  "s_branch .Lsl%=\n\t" \
  ".Lsx%=:\n\t" \
  "s_sub_i32 s93, s93, 12\n\t" \
  "s_max_i32 s93, s93, 4\n\t" \
  "s_lshr_b32 s93, s93, 2\n\t" \
  "s_add_u32 s93, s93, s90\n\t" \
  "s_memrealtime s[90:91]\n\t" \
  ".Lpace%=:\n\t" \
  "s_memrealtime s[94:95]\n\t" \
  "s_waitcnt lgkmcnt(1)\n\t" \
  "s_sub_i32 s92, s90, s93\n\t" \
  "s_cmp_ge_i32 s92, 0\n\t" \
  "s_cbranch_scc1 .Le%=\n\t" \
  "s_memrealtime s[90:91]\n\t" \
  "s_waitcnt lgkmcnt(1)\n\t" \
  "s_sub_i32 s92, s94, s93\n\t" \
  "s_cmp_lt_i32 s92, 0\n\t" \
  "s_cbranch_scc1 .Lpace%=\n\t" \
  "s_nop 4\n\t" \
  ".Le%=:\n\t" \
  "s_add_u32 s92, s92, s93\n\t" \
  "s_lshl_b32 s92, s92, 2\n\t" \
  "s_sub_i32 s92, %[tgt], s92\n\t" \
  "s_max_i32 s92, s92, 0\n\t" \
  "s_bitcmp0_b32 s92, 3\n\t" \
  "s_cbranch_scc1 .Lf3%=\n\t" \
  "s_nop 15\n\t" \
  ".Lf3%=:\n\t" \
  "s_bitcmp0_b32 s92, 2\n\t" \
  "s_cbranch_scc1 .Lf2%=\n\t" \
  "s_nop 10\n\t" \
  ".Lf2%=:\n\t" \
  ".Lgo%=:\n\t" \
  "s_mov_b32 s92, 0\n\t" \
  ".Lpoll%=:\n\t" \
  "buffer_load_dwordx4 v[200:203], v248, %[rs], %[som] offen sc1\n\t" \
  "buffer_load_dwordx4 v[204:207], v248, %[rs], %[som2] offen sc1\n\t" \
  "s_waitcnt vmcnt(1) lgkmcnt(0)\n\t" \
  "ds_write_b128 v249, v[200:203]\n\t" \
  "v_smfmac_f32_16x16x64_bf16 v[192:195], v[200:203], v[208:215], v251\n\t" \
  "v_max3_u32 v244, v200, v201, v202\n\t" \
  "v_smfmac_f32_16x16x64_bf16 v[196:199], v[200:203], v[236:243], v251\n\t" \
  "s_waitcnt vmcnt(0)\n\t" \
  "ds_write_b128 v249, v[204:207] offset:8192\n\t" \
  "v_smfmac_f32_16x16x64_bf16 v[192:195], v[204:207], v[216:223], v251\n\t" \
  "v_max3_u32 v244, v244, v203, v204\n\t" \
  "v_max3_u32 v244, v244, v205, v206\n\t" \
  "v_smfmac_f32_16x16x64_bf16 v[196:199], v[204:207], v[224:231], v251\n\t" \
  "v_max_u32 v244, v244, v207\n\t" \
  "v_cmp_le_u32 vcc, %[sent], v244\n\t" \
  "s_cbranch_vccnz .Lslow%=\n\t" \
  ".Lok%=:\n\t" \
  "ds_read_b128 v[216:219], v250 offset:32768\n\t" \
  "ds_read_b128 v[220:223], v250 offset:33792\n\t" \
  "ds_read_b128 v[224:227], v250 offset:34816\n\t" \
  "ds_read_b128 v[228:231], v250 offset:35840\n\t" \
  "s_waitcnt lgkmcnt(4)\n\t" \
  "s_barrier\n\t" \
  "ds_read_b128 v[208:211], v248 offset:4096\n\t" \
  "ds_read_b128 v[212:215], v248 offset:7168\n\t" \
  "ds_read_b128 v[236:239], v248 offset:8192\n\t" \
  "ds_read_b128 v[240:243], v248 offset:9216\n\t" \
  "s_waitcnt lgkmcnt(3)\n\t" \
  "ds_read_b128 v[244:247], v248 offset:12288\n\t" \
  "v_smfmac_f32_16x16x64_bf16 v[192:195], v[208:211], v[216:223], v251\n\t" \
  "ds_read_b128 v[216:219], v250 offset:0\n\t" \
  "v_smfmac_f32_16x16x64_bf16 v[196:199], v[208:211], v[224:231], v251\n\t" \
  "s_waitcnt lgkmcnt(4)\n\t" \
  "ds_read_b128 v[220:223], v250 offset:1024\n\t" \
  "v_smfmac_f32_16x16x64_bf16 v[192:195], v[212:215], v[0:7], v251\n\t" \
  "ds_read_b128 v[224:227], v250 offset:2048\n\t" \
  "v_smfmac_f32_16x16x64_bf16 v[196:199], v[212:215], v[8:15], v251\n\t" \
  "s_waitcnt lgkmcnt(5)\n\t" \
  "ds_read_b128 v[228:231], v250 offset:3072\n\t" \
  "v_smfmac_f32_16x16x64_bf16 v[192:195], v[236:239], v[16:23], v251\n\t" \
  "ds_read_b128 v[200:203], v248 offset:10240\n\t" \
  "v_smfmac_f32_16x16x64_bf16 v[196:199], v[236:239], v[24:31], v251\n\t" \
  "s_waitcnt lgkmcnt(6)\n\t" \
  "ds_read_b128 v[204:207], v248 offset:11264\n\t" \
  "v_smfmac_f32_16x16x64_bf16 v[192:195], v[240:243], v[32:39], v251\n\t" \
  "ds_read_b128 v[208:211], v248 offset:14336\n\t" \
  "v_smfmac_f32_16x16x64_bf16 v[196:199], v[240:243], v[40:47], v251\n\t" \
  "s_waitcnt lgkmcnt(5)\n\t" \
  "ds_read_b128 v[212:215], v248 offset:15360\n\t" \
  "v_smfmac_f32_16x16x64_bf16 v[192:195], v[244:247], v[216:223], v251\n\t" \
  "s_waitcnt lgkmcnt(4)\n\t" \
  "v_smfmac_f32_16x16x64_bf16 v[196:199], v[244:247], v[224:231], v251\n\t" \
  "s_waitcnt lgkmcnt(3)\n\t" \
  "ds_read_b128 v[236:239], v248 offset:0\n\t" \
  "v_smfmac_f32_16x16x64_bf16 v[192:195], v[200:203], v[48:55], v251\n\t" \
  "ds_read_b128 v[240:243], v248 offset:1024\n\t" \
  "v_smfmac_f32_16x16x64_bf16 v[196:199], v[200:203], v[56:63], v251\n\t" \
  "s_waitcnt lgkmcnt(4)\n\t" \
  "v_smfmac_f32_16x16x64_bf16 v[192:195], v[204:207], v[64:71], v251\n\t" \
  "ds_read_b128 v[244:247], v248 offset:2048\n\t" \
  "v_smfmac_f32_16x16x64_bf16 v[196:199], v[204:207], v[72:79], v251\n\t" \
  "s_waitcnt lgkmcnt(4)\n\t" \
  "v_smfmac_f32_16x16x64_bf16 v[192:195], v[208:211], v[80:87], v251\n\t" \
  "ds_read_b128 v[200:203], v248 offset:3072\n\t" \
  "v_smfmac_f32_16x16x64_bf16 v[196:199], v[208:211], v[88:95], v251\n\t" \
  "s_waitcnt lgkmcnt(4)\n\t" \
  "v_smfmac_f32_16x16x64_bf16 v[192:195], v[212:215], v[96:103], v251\n\t" \
  "ds_read_b128 v[204:207], v248 offset:6144\n\t" \
  "v_smfmac_f32_16x16x64_bf16 v[196:199], v[212:215], v[104:111], v251\n\t" \
  "s_waitcnt lgkmcnt(3)\n\t" \
  "v_smfmac_f32_16x16x64_bf16 v[192:195], v[236:239], v[112:119], v251\n\t" \
  "v_smfmac_f32_16x16x64_bf16 v[196:199], v[236:239], v[120:127], v251\n\t" \
  "v_smfmac_f32_16x16x64_bf16 v[192:195], v[240:243], v[128:135], v251\n\t" \
  "v_smfmac_f32_16x16x64_bf16 v[196:199], v[240:243], v[136:143], v251\n\t" \
  "s_waitcnt lgkmcnt(1)\n\t" \
  "v_smfmac_f32_16x16x64_bf16 v[192:195], v[244:247], v[144:151], v251\n\t" \
  "v_smfmac_f32_16x16x64_bf16 v[196:199], v[244:247], v[152:159], v251\n\t" \
  "v_smfmac_f32_16x16x64_bf16 v[192:195], v[200:203], v[160:167], v251\n\t" \
  "v_smfmac_f32_16x16x64_bf16 v[196:199], v[200:203], v[168:175], v251\n\t" \
  "s_waitcnt lgkmcnt(0)\n\t" \
  "v_smfmac_f32_16x16x64_bf16 v[192:195], v[204:207], v[176:183], v251\n\t" \
  "v_smfmac_f32_16x16x64_bf16 v[196:199], v[204:207], v[184:191], v251\n\t" \
  "s_nop 6\n\t" \
  "v_add_f32 v192, v192, v194\n\t" \
  "v_add_f32 v193, v193, v195\n\t" \
  "v_add_f32 v196, v196, v198\n\t" \
  "v_add_f32 v197, v197, v199\n\t" \
  "v_cvt_pk_bf16_f32 v192, v192, v193\n\t" \
  "v_cvt_pk_bf16_f32 v196, v196, v197\n\t" \
  "ds_write2_b32 v252, v192, v196 offset1:64\n\t" \
  "s_waitcnt lgkmcnt(0)\n\t" \
  "s_barrier\n\t" \
  "buffer_store_dword v192, v235, %[rs], %[sk] offen sc1\n\t" \
  "buffer_store_dword v196, v235, %[rs], %[sk] offen offset:256 sc1\n\t" \
  "s_mov_b32 %[spins], s92\n\t" \
  "s_branch .Lend%=\n\t" \
  ".Lslow%=:\n\t" \
  "s_mov_b64 s[94:95], 0\n\t" \
  "v_cmp_eq_u32 vcc, %[sent], v200\n\t" \
  "s_or_b64 s[94:95], s[94:95], vcc\n\t" \
  "v_cmp_eq_u32 vcc, %[sent], v201\n\t" \
  "s_or_b64 s[94:95], s[94:95], vcc\n\t" \
  "v_cmp_eq_u32 vcc, %[sent], v202\n\t" \
  "s_or_b64 s[94:95], s[94:95], vcc\n\t" \
  "v_cmp_eq_u32 vcc, %[sent], v203\n\t" \
  "s_or_b64 s[94:95], s[94:95], vcc\n\t" \
  "v_cmp_eq_u32 vcc, %[sent], v204\n\t" \
  "s_or_b64 s[94:95], s[94:95], vcc\n\t" \
  "v_cmp_eq_u32 vcc, %[sent], v205\n\t" \
  "s_or_b64 s[94:95], s[94:95], vcc\n\t" \
  "v_cmp_eq_u32 vcc, %[sent], v206\n\t" \
  "s_or_b64 s[94:95], s[94:95], vcc\n\t" \
  "v_cmp_eq_u32 vcc, %[sent], v207\n\t" \
  "s_or_b64 s[94:95], s[94:95], vcc\n\t" \
  "s_cmp_eq_u64 s[94:95], 0\n\t" \
  "s_cbranch_scc1 .Lok%=\n\t" \
  "v_mov_b64 v[192:193], 0\n\t" \
  "v_mov_b64 v[194:195], 0\n\t" \
  "v_mov_b64 v[196:197], 0\n\t" \
  "v_mov_b64 v[198:199], 0\n\t" \
  "s_add_u32 s92, s92, 1\n\t" \
  "s_cmp_eq_u32 s92, 1\n\t" \
  "s_cbranch_scc1 .Lpoll%=\n\t" \
  "s_sleep 2\n\t" \
  "s_and_b32 s93, s92, 63\n\t" \
  "s_cmp_lg_u32 s93, 0\n\t" \
  "s_cbranch_scc1 .Lpoll%=\n\t" \
  "v_mov_b32 v245, 0\n\t" \
  "buffer_load_dword v244, v245, %[rs], %[sab] offen sc1\n\t" \
  "s_waitcnt vmcnt(0)\n\t" \
  "v_readfirstlane_b32 s93, v244\n\t" \
  "s_nop 3\n\t" \
  "s_cmp_eq_u32 s93, 1\n\t" \
  "s_cbranch_scc1 .Lok%=\n\t" \
  "s_cmp_lt_u32 s92, 0x80000\n\t" \
  "s_cbranch_scc1 .Lpoll%=\n\t" \
  "v_mov_b32 v244, 1\n\t" \
  "buffer_store_dword v244, v245, %[rs], %[sab] offen sc1\n\t" \
  "s_branch .Lok%=\n\t" \
  ".Lend%=:\n\t"
#define QK_ASM_6 \
  "s_waitcnt lgkmcnt(0)\n\t" \
  "v_subrev_u32 v244, 0x8000, v250\n\t" \
  "ds_read_b128 v[216:219], v248 offset:57344\n\t" \
  "ds_read_b128 v[220:223], v248 offset:58368\n\t" \
  "ds_read_b128 v[224:227], v248 offset:59392\n\t" \
  "ds_read_b128 v[228:231], v248 offset:60416\n\t" \
  "ds_read_b128 v[208:211], v244 offset:0\n\t" \
  "ds_read_b128 v[212:215], v244 offset:1024\n\t" \
  "ds_read_b128 v[236:239], v244 offset:2048\n\t" \
  "ds_read_b128 v[240:243], v244 offset:3072\n\t" \
  "v_mov_b64 v[192:193], 0\n\t" \
  "v_mov_b64 v[194:195], 0\n\t" \
  "v_mov_b64 v[196:197], 0\n\t" \
  "v_mov_b64 v[198:199], 0\n\t" \
  "s_movk_i32 s92, 0x2000\n\t" \
  ".Lwt%=:\n\t" \
  "ds_read_b64 v[200:201], v248 offset:30720\n\t" \
  "s_memrealtime s[90:91]\n\t" \
  "s_sub_u32 s92, s92, 1\n\t" \
  "s_waitcnt lgkmcnt(0)\n\t" \
  "v_readfirstlane_b32 s93, v200\n\t" \
  "v_readfirstlane_b32 %[tgt], v201\n\t" \
  "s_cbranch_scc1 .Lgo%=\n\t" \
  "s_cmp_eq_u32 s93, %[step]\n\t" \
  "s_cbranch_scc1 .Lwx%=\n\t" \
  "s_sleep 1\n\t" \
  "s_branch .Lwt%=\n\t" \
  ".Lwx%=:\n\t" \
  "s_lshl_b32 s93, s90, 2\n\t" \
  "s_sub_i32 s93, %[tgt], s93\n\t" \
  "s_cmp_le_i32 s93, 0\n\t" \
  "s_cbranch_scc1 .Lgo%=\n\t" \
  "s_min_i32 s93, s93, 8000\n\t" \
  "s_mov_b32 s94, s93\n\t" \
  ".Lsl%=:\n\t" \
  "s_sub_i32 s94, s94, 18\n\t" \
  "s_cmp_lt_i32 s94, 30\n\t" \
  "s_cbranch_scc1 .Lsx%=\n\t" \
  "s_sleep 1\n\t" \
  "s_branch .Lsl%=\n\t" \
  ".Lsx%=:\n\t" \
  "s_sub_i32 s93, s93, 12\n\t" \
  "s_max_i32 s93, s93, 4\n\t" \
  "s_lshr_b32 s93, s93, 2\n\t" \
  "s_add_u32 s93, s93, s90\n\t" \
  "s_memrealtime s[90:91]\n\t" \
  ".Lpace%=:\n\t" \
  "s_memrealtime s[94:95]\n\t" \
  "s_waitcnt lgkmcnt(1)\n\t" \
  "s_sub_i32 s92, s90, s93\n\t" \
  "s_cmp_ge_i32 s92, 0\n\t" \
  "s_cbranch_scc1 .Le%=\n\t" \
  "s_memrealtime s[90:91]\n\t" \
  "s_waitcnt lgkmcnt(1)\n\t" \
  "s_sub_i32 s92, s94, s93\n\t" \
  "s_cmp_lt_i32 s92, 0\n\t" \
  "s_cbranch_scc1 .Lpace%=\n\t" \
  "s_nop 4\n\t" \
  ".Le%=:\n\t" \
  "s_add_u32 s92, s92, s93\n\t" \
  "s_lshl_b32 s92, s92, 2\n\t" \
  "s_sub_i32 s92, %[tgt], s92\n\t" \
  "s_max_i32 s92, s92, 0\n\t" \
  "s_bitcmp0_b32 s92, 3\n\t" \
  "s_cbranch_scc1 .Lf3%=\n\t" \
  "s_nop 15\n\t" \
  ".Lf3%=:\n\t" \
  "s_bitcmp0_b32 s92, 2\n\t" \
  "s_cbranch_scc1 .Lf2%=\n\t" \
  "s_nop 10\n\t" \
  ".Lf2%=:\n\t" \
  ".Lgo%=:\n\t" \
  "s_mov_b32 s92, 0\n\t" \
  ".Lpoll%=:\n\t" \
  "buffer_load_dwordx4 v[200:203], v248, %[rs], %[som] offen sc1\n\t" \
  "buffer_load_dwordx4 v[204:207], v248, %[rs], %[som2] offen sc1\n\t" \
  "s_waitcnt vmcnt(1) lgkmcnt(0)\n\t" \
  "ds_write_b128 v249, v[200:203]\n\t" \
  "v_smfmac_f32_16x16x64_bf16 v[192:195], v[200:203], v[208:215], v251\n\t" \
  "v_max3_u32 v244, v200, v201, v202\n\t" \
  "v_smfmac_f32_16x16x64_bf16 v[196:199], v[200:203], v[236:243], v251\n\t" \
  "s_waitcnt vmcnt(0)\n\t" \
  "ds_write_b128 v249, v[204:207] offset:8192\n\t" \
  "v_smfmac_f32_16x16x64_bf16 v[192:195], v[204:207], v[216:223], v251\n\t" \
  "v_max3_u32 v244, v244, v203, v204\n\t" \
  "v_max3_u32 v244, v244, v205, v206\n\t" \
  "v_smfmac_f32_16x16x64_bf16 v[196:199], v[204:207], v[224:231], v251\n\t" \
  "v_max_u32 v244, v244, v207\n\t" \
  "v_cmp_le_u32 vcc, %[sent], v244\n\t" \
  "s_cbranch_vccnz .Lslow%=\n\t" \
  ".Lok%=:\n\t" \
  "ds_read_b128 v[216:219], v250 offset:32768\n\t" \
  "ds_read_b128 v[220:223], v250 offset:33792\n\t" \
  "ds_read_b128 v[224:227], v250 offset:34816\n\t" \
  "ds_read_b128 v[228:231], v250 offset:35840\n\t" \
  "s_waitcnt lgkmcnt(4)\n\t" \
  "s_barrier\n\t" \
  "ds_read_b128 v[208:211], v248 offset:7168\n\t" \
  "ds_read_b128 v[212:215], v248 offset:8192\n\t" \
  "ds_read_b128 v[236:239], v248 offset:9216\n\t" \
  "ds_read_b128 v[240:243], v248 offset:10240\n\t" \
  "s_waitcnt lgkmcnt(3)\n\t" \
  "ds_read_b128 v[244:247], v248 offset:13312\n\t" \
  "v_smfmac_f32_16x16x64_bf16 v[192:195], v[208:211], v[216:223], v251\n\t" \
  "ds_read_b128 v[216:219], v250 offset:0\n\t" \
  "v_smfmac_f32_16x16x64_bf16 v[196:199], v[208:211], v[224:231], v251\n\t" \
  "s_waitcnt lgkmcnt(4)\n\t" \
  "ds_read_b128 v[220:223], v250 offset:1024\n\t" \
  "v_smfmac_f32_16x16x64_bf16 v[192:195], v[212:215], v[0:7], v251\n\t" \
  "ds_read_b128 v[224:227], v250 offset:2048\n\t" \
  "v_smfmac_f32_16x16x64_bf16 v[196:199], v[212:215], v[8:15], v251\n\t" \
  "s_waitcnt lgkmcnt(5)\n\t" \
  "ds_read_b128 v[228:231], v250 offset:3072\n\t" \
  "v_smfmac_f32_16x16x64_bf16 v[192:195], v[236:239], v[16:23], v251\n\t" \
  "ds_read_b128 v[200:203], v248 offset:11264\n\t" \
  "v_smfmac_f32_16x16x64_bf16 v[196:199], v[236:239], v[24:31], v251\n\t" \
  "s_waitcnt lgkmcnt(6)\n\t" \
  "ds_read_b128 v[204:207], v248 offset:12288\n\t" \
  "v_smfmac_f32_16x16x64_bf16 v[192:195], v[240:243], v[32:39], v251\n\t" \
  "ds_read_b128 v[208:211], v248 offset:15360\n\t" \
  "v_smfmac_f32_16x16x64_bf16 v[196:199], v[240:243], v[40:47], v251\n\t" \
  "s_waitcnt lgkmcnt(5)\n\t" \
  "ds_read_b128 v[212:215], v248 offset:0\n\t" \
  "v_smfmac_f32_16x16x64_bf16 v[192:195], v[244:247], v[216:223], v251\n\t" \
  "s_waitcnt lgkmcnt(4)\n\t" \
  "v_smfmac_f32_16x16x64_bf16 v[196:199], v[244:247], v[224:231], v251\n\t" \
  "s_waitcnt lgkmcnt(3)\n\t" \
  "ds_read_b128 v[236:239], v248 offset:1024\n\t" \
  "v_smfmac_f32_16x16x64_bf16 v[192:195], v[200:203], v[48:55], v251\n\t" \
  "ds_read_b128 v[240:243], v248 offset:2048\n\t" \
  "v_smfmac_f32_16x16x64_bf16 v[196:199], v[200:203], v[56:63], v251\n\t" \
  "s_waitcnt lgkmcnt(4)\n\t" \
  "v_smfmac_f32_16x16x64_bf16 v[192:195], v[204:207], v[64:71], v251\n\t" \
  "ds_read_b128 v[244:247], v248 offset:3072\n\t" \
  "v_smfmac_f32_16x16x64_bf16 v[196:199], v[204:207], v[72:79], v251\n\t" \
  "s_waitcnt lgkmcnt(4)\n\t" \
  "v_smfmac_f32_16x16x64_bf16 v[192:195], v[208:211], v[80:87], v251\n\t" \
  "ds_read_b128 v[200:203], v248 offset:4096\n\t" \
  "v_smfmac_f32_16x16x64_bf16 v[196:199], v[208:211], v[88:95], v251\n\t" \
  "s_waitcnt lgkmcnt(4)\n\t" \
  "v_smfmac_f32_16x16x64_bf16 v[192:195], v[212:215], v[96:103], v251\n\t" \
  "ds_read_b128 v[204:207], v248 offset:5120\n\t" \
  "v_smfmac_f32_16x16x64_bf16 v[196:199], v[212:215], v[104:111], v251\n\t" \
  "s_waitcnt lgkmcnt(3)\n\t" \
  "v_smfmac_f32_16x16x64_bf16 v[192:195], v[236:239], v[112:119], v251\n\t" \
  "v_smfmac_f32_16x16x64_bf16 v[196:199], v[236:239], v[120:127], v251\n\t" \
  "v_smfmac_f32_16x16x64_bf16 v[192:195], v[240:243], v[128:135], v251\n\t" \
  "v_smfmac_f32_16x16x64_bf16 v[196:199], v[240:243], v[136:143], v251\n\t" \
  "s_waitcnt lgkmcnt(1)\n\t" \
  "v_smfmac_f32_16x16x64_bf16 v[192:195], v[244:247], v[144:151], v251\n\t" \
  "v_smfmac_f32_16x16x64_bf16 v[196:199], v[244:247], v[152:159], v251\n\t" \
  "v_smfmac_f32_16x16x64_bf16 v[192:195], v[200:203], v[160:167], v251\n\t" \
  "v_smfmac_f32_16x16x64_bf16 v[196:199], v[200:203], v[168:175], v251\n\t" \
  "s_waitcnt lgkmcnt(0)\n\t" \
  "v_smfmac_f32_16x16x64_bf16 v[192:195], v[204:207], v[176:183], v251\n\t" \
  "v_smfmac_f32_16x16x64_bf16 v[196:199], v[204:207], v[184:191], v251\n\t" \
  "s_nop 6\n\t" \
  "v_add_f32 v192, v192, v194\n\t" \
  "v_add_f32 v193, v193, v195\n\t" \
  "v_add_f32 v196, v196, v198\n\t" \
  "v_add_f32 v197, v197, v199\n\t" \
  "v_cvt_pk_bf16_f32 v192, v192, v193\n\t" \
  "v_cvt_pk_bf16_f32 v196, v196, v197\n\t" \
  "ds_write2_b32 v252, v192, v196 offset1:64\n\t" \
  "s_waitcnt lgkmcnt(0)\n\t" \
  "s_barrier\n\t" \
  "buffer_store_dword v192, v235, %[rs], %[sk] offen sc1\n\t" \
  "buffer_store_dword v196, v235, %[rs], %[sk] offen offset:256 sc1\n\t" \
  "s_mov_b32 %[spins], s92\n\t" \
  "s_branch .Lend%=\n\t" \
  ".Lslow%=:\n\t" \
  "s_mov_b64 s[94:95], 0\n\t" \
  "v_cmp_eq_u32 vcc, %[sent], v200\n\t" \
  "s_or_b64 s[94:95], s[94:95], vcc\n\t" \
  "v_cmp_eq_u32 vcc, %[sent], v201\n\t" \
  "s_or_b64 s[94:95], s[94:95], vcc\n\t" \
  "v_cmp_eq_u32 vcc, %[sent], v202\n\t" \
  "s_or_b64 s[94:95], s[94:95], vcc\n\t" \
  "v_cmp_eq_u32 vcc, %[sent], v203\n\t" \
  "s_or_b64 s[94:95], s[94:95], vcc\n\t" \
  "v_cmp_eq_u32 vcc, %[sent], v204\n\t" \
  "s_or_b64 s[94:95], s[94:95], vcc\n\t" \
  "v_cmp_eq_u32 vcc, %[sent], v205\n\t" \
  "s_or_b64 s[94:95], s[94:95], vcc\n\t" \
  "v_cmp_eq_u32 vcc, %[sent], v206\n\t" \
  "s_or_b64 s[94:95], s[94:95], vcc\n\t" \
  "v_cmp_eq_u32 vcc, %[sent], v207\n\t" \
  "s_or_b64 s[94:95], s[94:95], vcc\n\t" \
  "s_cmp_eq_u64 s[94:95], 0\n\t" \
  "s_cbranch_scc1 .Lok%=\n\t" \
  "v_mov_b64 v[192:193], 0\n\t" \
  "v_mov_b64 v[194:195], 0\n\t" \
  "v_mov_b64 v[196:197], 0\n\t" \
  "v_mov_b64 v[198:199], 0\n\t" \
  "s_add_u32 s92, s92, 1\n\t" \
  "s_cmp_eq_u32 s92, 1\n\t" \
  "s_cbranch_scc1 .Lpoll%=\n\t" \
  "s_sleep 2\n\t" \
  "s_and_b32 s93, s92, 63\n\t" \
  "s_cmp_lg_u32 s93, 0\n\t" \
  "s_cbranch_scc1 .Lpoll%=\n\t" \
  "v_mov_b32 v245, 0\n\t" \
  "buffer_load_dword v244, v245, %[rs], %[sab] offen sc1\n\t" \
  "s_waitcnt vmcnt(0)\n\t" \
  "v_readfirstlane_b32 s93, v244\n\t" \
  "s_nop 3\n\t" \
  "s_cmp_eq_u32 s93, 1\n\t" \
  "s_cbranch_scc1 .Lok%=\n\t" \
  "s_cmp_lt_u32 s92, 0x80000\n\t" \
  "s_cbranch_scc1 .Lpoll%=\n\t" \
  "v_mov_b32 v244, 1\n\t" \
  "buffer_store_dword v244, v245, %[rs], %[sab] offen sc1\n\t" \
  "s_branch .Lok%=\n\t" \
  ".Lend%=:\n\t"
#define QK_ASM_7 \
  "s_waitcnt lgkmcnt(0)\n\t" \
  "v_subrev_u32 v244, 0x8000, v250\n\t" \
  "ds_read_b128 v[216:219], v248 offset:61440\n\t" \
  "ds_read_b128 v[220:223], v248 offset:62464\n\t" \
  "ds_read_b128 v[224:227], v248 offset:63488\n\t" \
  "ds_read_b128 v[228:231], v248 offset:64512\n\t" \
  "ds_read_b128 v[208:211], v244 offset:0\n\t" \
  "ds_read_b128 v[212:215], v244 offset:1024\n\t" \
  "ds_read_b128 v[236:239], v244 offset:2048\n\t" \
  "ds_read_b128 v[240:243], v244 offset:3072\n\t" \
  "v_mov_b64 v[192:193], 0\n\t" \
  "v_mov_b64 v[194:195], 0\n\t" \
  "v_mov_b64 v[196:197], 0\n\t" \
  "v_mov_b64 v[198:199], 0\n\t" \
  "s_movk_i32 s92, 0x2000\n\t" \
  ".Lwt%=:\n\t" \
  "ds_read_b64 v[200:201], v248 offset:30720\n\t" \
  "s_memrealtime s[90:91]\n\t" \
  "s_sub_u32 s92, s92, 1\n\t" \
  "s_waitcnt lgkmcnt(0)\n\t" \
  "v_readfirstlane_b32 s93, v200\n\t" \
  "v_readfirstlane_b32 %[tgt], v201\n\t" \
  "s_cbranch_scc1 .Lgo%=\n\t" \
  "s_cmp_eq_u32 s93, %[step]\n\t" \
  "s_cbranch_scc1 .Lwx%=\n\t" \
  "s_sleep 1\n\t" \
  "s_branch .Lwt%=\n\t" \
  ".Lwx%=:\n\t" \
  "s_lshl_b32 s93, s90, 2\n\t" \
  "s_sub_i32 s93, %[tgt], s93\n\t" \
  "s_cmp_le_i32 s93, 0\n\t" \
  "s_cbranch_scc1 .Lgo%=\n\t" \
  "s_min_i32 s93, s93, 8000\n\t" \
  "s_mov_b32 s94, s93\n\t" \
  ".Lsl%=:\n\t" \
  "s_sub_i32 s94, s94, 18\n\t" \
  "s_cmp_lt_i32 s94, 30\n\t" \
  "s_cbranch_scc1 .Lsx%=\n\t" \
  "s_sleep 1\n\t" \
  "s_branch .Lsl%=\n\t" \
  ".Lsx%=:\n\t" \
  "s_sub_i32 s93, s93, 12\n\t" \
  "s_max_i32 s93, s93, 4\n\t" \
  "s_lshr_b32 s93, s93, 2\n\t" \
  "s_add_u32 s93, s93, s90\n\t" \
  "s_memrealtime s[90:91]\n\t" \
  ".Lpace%=:\n\t" \
  "s_memrealtime s[94:95]\n\t" \
  "s_waitcnt lgkmcnt(1)\n\t" \
  "s_sub_i32 s92, s90, s93\n\t" \
  "s_cmp_ge_i32 s92, 0\n\t" \
  "s_cbranch_scc1 .Le%=\n\t" \
  "s_memrealtime s[90:91]\n\t" \
  "s_waitcnt lgkmcnt(1)\n\t" \
  "s_sub_i32 s92, s94, s93\n\t" \
  "s_cmp_lt_i32 s92, 0\n\t" \
  "s_cbranch_scc1 .Lpace%=\n\t" \
  "s_nop 4\n\t" \
  ".Le%=:\n\t" \
  "s_add_u32 s92, s92, s93\n\t" \
  "s_lshl_b32 s92, s92, 2\n\t" \
  "s_sub_i32 s92, %[tgt], s92\n\t" \
  "s_max_i32 s92, s92, 0\n\t" \
  "s_bitcmp0_b32 s92, 3\n\t" \
  "s_cbranch_scc1 .Lf3%=\n\t" \
  "s_nop 15\n\t" \
  ".Lf3%=:\n\t" \
  "s_bitcmp0_b32 s92, 2\n\t" \
  "s_cbranch_scc1 .Lf2%=\n\t" \
  "s_nop 10\n\t" \
  ".Lf2%=:\n\t" \
  ".Lgo%=:\n\t" \
  "s_mov_b32 s92, 0\n\t" \
  "s_memtime s[88:89]\n\t" \
  ".Lpoll%=:\n\t" \
  "buffer_load_dwordx4 v[200:203], v248, %[rs], %[som] offen sc1\n\t" \
  "buffer_load_dwordx4 v[204:207], v248, %[rs], %[som2] offen sc1\n\t" \
  "s_waitcnt vmcnt(1) lgkmcnt(0)\n\t" \
  "ds_write_b128 v249, v[200:203]\n\t" \
  "v_smfmac_f32_16x16x64_bf16 v[192:195], v[200:203], v[208:215], v251\n\t" \
  "v_max3_u32 v244, v200, v201, v202\n\t" \
  "v_smfmac_f32_16x16x64_bf16 v[196:199], v[200:203], v[236:243], v251\n\t" \
  "s_waitcnt vmcnt(0)\n\t" \
  "ds_write_b128 v249, v[204:207] offset:8192\n\t" \
  "v_smfmac_f32_16x16x64_bf16 v[192:195], v[204:207], v[216:223], v251\n\t" \
  "v_max3_u32 v244, v244, v203, v204\n\t" \
  "v_max3_u32 v244, v244, v205, v206\n\t" \
  "v_smfmac_f32_16x16x64_bf16 v[196:199], v[204:207], v[224:231], v251\n\t" \
  "v_max_u32 v244, v244, v207\n\t" \
  "v_cmp_le_u32 vcc, %[sent], v244\n\t" \
  "s_cbranch_vccnz .Lslow%=\n\t" \
  ".Lok%=:\n\t" \
  "ds_read_b128 v[216:219], v250 offset:32768\n\t" \
  "ds_read_b128 v[220:223], v250 offset:33792\n\t" \
  "ds_read_b128 v[224:227], v250 offset:34816\n\t" \
  "ds_read_b128 v[228:231], v250 offset:35840\n\t" \
  "s_waitcnt lgkmcnt(4)\n\t" \
  "s_setprio 3\n\t" \
  "s_barrier\n\t" \
  "s_memrealtime s[90:91]\n\t" \
  "s_memtime s[94:95]\n\t" \
  "v_mov_b32 v232, %[step]\n\t" \
  "v_lshrrev_b32 v234, 1, v248\n\t" \
  "s_mov_b64 exec, 0xffff\n\t" \
  "s_waitcnt lgkmcnt(0)\n\t" \
  "s_lshl_b32 s90, s90, 2\n\t" \
  "s_sub_u32 s94, s94, s88\n\t" \
  "s_mul_i32 s94, s94, 12200\n\t" \
  "s_lshr_b32 s94, s94, 16\n\t" \
  "s_add_u32 s94, s94, %[tgt]\n\t" \
  "s_sub_i32 s93, s90, s94\n\t" \
  "s_sub_i32 s93, s93, 30\n\t" \
  "s_max_i32 s93, s93, 0\n\t" \
  "s_add_u32 s90, s94, s93\n\t" \
  "s_sub_i32 s93, s90, %[tgt]\n\t" \
  "s_cmp_gt_i32 s93, 270\n\t" \
  "s_cselect_b32 s93, 70, 0\n\t" \
  "s_add_u32 s93, s93, s90\n\t" \
  "s_add_u32 s93, s93, %[lead]\n\t" \
  "v_mov_b32 v233, s93\n\t" \
  "buffer_store_dwordx2 v[232:233], v234, %[rs], %[sbc] offen sc1\n\t" \
  "s_mov_b64 exec, -1\n\t" \
  "s_mov_b32 %[tb1], s90\n\t" \
  "s_setprio 0\n\t" \
  "ds_read_b128 v[208:211], v248 offset:6144\n\t" \
  "ds_read_b128 v[212:215], v248 offset:9216\n\t" \
  "ds_read_b128 v[236:239], v248 offset:10240\n\t" \
  "ds_read_b128 v[240:243], v248 offset:11264\n\t" \
  "s_waitcnt lgkmcnt(3)\n\t" \
  "ds_read_b128 v[244:247], v248 offset:12288\n\t" \
  "v_smfmac_f32_16x16x64_bf16 v[192:195], v[208:211], v[216:223], v251\n\t" \
  "ds_read_b128 v[216:219], v250 offset:0\n\t" \
  "v_smfmac_f32_16x16x64_bf16 v[196:199], v[208:211], v[224:231], v251\n\t" \
  "s_waitcnt lgkmcnt(4)\n\t" \
  "ds_read_b128 v[220:223], v250 offset:1024\n\t" \
  "v_smfmac_f32_16x16x64_bf16 v[192:195], v[212:215], v[0:7], v251\n\t" \
  "ds_read_b128 v[224:227], v250 offset:2048\n\t" \
  "v_smfmac_f32_16x16x64_bf16 v[196:199], v[212:215], v[8:15], v251\n\t" \
  "s_waitcnt lgkmcnt(5)\n\t" \
  "ds_read_b128 v[228:231], v250 offset:3072\n\t" \
  "v_smfmac_f32_16x16x64_bf16 v[192:195], v[236:239], v[16:23], v251\n\t" \
  "ds_read_b128 v[200:203], v248 offset:13312\n\t" \
  "v_smfmac_f32_16x16x64_bf16 v[196:199], v[236:239], v[24:31], v251\n\t" \
  "s_waitcnt lgkmcnt(6)\n\t" \
  "ds_read_b128 v[204:207], v248 offset:14336\n\t" \
  "v_smfmac_f32_16x16x64_bf16 v[192:195], v[240:243], v[32:39], v251\n\t" \
  "ds_read_b128 v[208:211], v248 offset:0\n\t" \
  "v_smfmac_f32_16x16x64_bf16 v[196:199], v[240:243], v[40:47], v251\n\t" \
  "s_waitcnt lgkmcnt(5)\n\t" \
  "ds_read_b128 v[212:215], v248 offset:1024\n\t" \
  "v_smfmac_f32_16x16x64_bf16 v[192:195], v[244:247], v[216:223], v251\n\t" \
  "s_waitcnt lgkmcnt(4)\n\t" \
  "v_smfmac_f32_16x16x64_bf16 v[196:199], v[244:247], v[224:231], v251\n\t" \
  "s_waitcnt lgkmcnt(3)\n\t" \
  "ds_read_b128 v[236:239], v248 offset:2048\n\t" \
  "v_smfmac_f32_16x16x64_bf16 v[192:195], v[200:203], v[48:55], v251\n\t" \
  "ds_read_b128 v[240:243], v248 offset:3072\n\t" \
  "v_smfmac_f32_16x16x64_bf16 v[196:199], v[200:203], v[56:63], v251\n\t" \
  "s_waitcnt lgkmcnt(4)\n\t" \
  "v_smfmac_f32_16x16x64_bf16 v[192:195], v[204:207], v[64:71], v251\n\t" \
  "ds_read_b128 v[244:247], v248 offset:4096\n\t" \
  "v_smfmac_f32_16x16x64_bf16 v[196:199], v[204:207], v[72:79], v251\n\t" \
  "s_waitcnt lgkmcnt(4)\n\t" \
  "v_smfmac_f32_16x16x64_bf16 v[192:195], v[208:211], v[80:87], v251\n\t" \
  "ds_read_b128 v[200:203], v248 offset:5120\n\t" \
  "v_smfmac_f32_16x16x64_bf16 v[196:199], v[208:211], v[88:95], v251\n\t" \
  "s_waitcnt lgkmcnt(4)\n\t" \
  "v_smfmac_f32_16x16x64_bf16 v[192:195], v[212:215], v[96:103], v251\n\t" \
  "ds_read_b128 v[204:207], v248 offset:8192\n\t" \
  "v_smfmac_f32_16x16x64_bf16 v[196:199], v[212:215], v[104:111], v251\n\t" \
  "s_waitcnt lgkmcnt(3)\n\t" \
  "v_smfmac_f32_16x16x64_bf16 v[192:195], v[236:239], v[112:119], v251\n\t" \
  "v_smfmac_f32_16x16x64_bf16 v[196:199], v[236:239], v[120:127], v251\n\t" \
  "v_smfmac_f32_16x16x64_bf16 v[192:195], v[240:243], v[128:135], v251\n\t" \
  "v_smfmac_f32_16x16x64_bf16 v[196:199], v[240:243], v[136:143], v251\n\t" \
  "s_waitcnt lgkmcnt(1)\n\t" \
  "v_smfmac_f32_16x16x64_bf16 v[192:195], v[244:247], v[144:151], v251\n\t" \
  "v_smfmac_f32_16x16x64_bf16 v[196:199], v[244:247], v[152:159], v251\n\t" \
  "v_smfmac_f32_16x16x64_bf16 v[192:195], v[200:203], v[160:167], v251\n\t" \
  "v_smfmac_f32_16x16x64_bf16 v[196:199], v[200:203], v[168:175], v251\n\t" \
  "s_waitcnt lgkmcnt(0)\n\t" \
  "v_smfmac_f32_16x16x64_bf16 v[192:195], v[204:207], v[176:183], v251\n\t" \
  "v_smfmac_f32_16x16x64_bf16 v[196:199], v[204:207], v[184:191], v251\n\t" \
  "s_nop 6\n\t" \
  "v_add_f32 v192, v192, v194\n\t" \
  "v_add_f32 v193, v193, v195\n\t" \
  "v_add_f32 v196, v196, v198\n\t" \
  "v_add_f32 v197, v197, v199\n\t" \
  "v_cvt_pk_bf16_f32 v192, v192, v193\n\t" \
  "v_cvt_pk_bf16_f32 v196, v196, v197\n\t" \
  "ds_write2_b32 v252, v192, v196 offset1:64\n\t" \
  "s_waitcnt lgkmcnt(0)\n\t" \
  "s_barrier\n\t" \
  "buffer_store_dword v192, v235, %[rs], %[sk] offen sc1\n\t" \
  "buffer_store_dword v196, v235, %[rs], %[sk] offen offset:256 sc1\n\t" \
  "s_mov_b32 %[spins], s92\n\t" \
  "s_branch .Lend%=\n\t" \
  ".Lslow%=:\n\t" \
  "s_mov_b64 s[94:95], 0\n\t" \
  "v_cmp_eq_u32 vcc, %[sent], v200\n\t" \
  "s_or_b64 s[94:95], s[94:95], vcc\n\t" \
  "v_cmp_eq_u32 vcc, %[sent], v201\n\t" \
  "s_or_b64 s[94:95], s[94:95], vcc\n\t" \
  "v_cmp_eq_u32 vcc, %[sent], v202\n\t" \
  "s_or_b64 s[94:95], s[94:95], vcc\n\t" \
  "v_cmp_eq_u32 vcc, %[sent], v203\n\t" \
  "s_or_b64 s[94:95], s[94:95], vcc\n\t" \
  "v_cmp_eq_u32 vcc, %[sent], v204\n\t" \
  "s_or_b64 s[94:95], s[94:95], vcc\n\t" \
  "v_cmp_eq_u32 vcc, %[sent], v205\n\t" \
  "s_or_b64 s[94:95], s[94:95], vcc\n\t" \
  "v_cmp_eq_u32 vcc, %[sent], v206\n\t" \
  "s_or_b64 s[94:95], s[94:95], vcc\n\t" \
  "v_cmp_eq_u32 vcc, %[sent], v207\n\t" \
  "s_or_b64 s[94:95], s[94:95], vcc\n\t" \
  "s_cmp_eq_u64 s[94:95], 0\n\t" \
  "s_cbranch_scc1 .Lok%=\n\t" \
  "v_mov_b64 v[192:193], 0\n\t" \
  "v_mov_b64 v[194:195], 0\n\t" \
  "v_mov_b64 v[196:197], 0\n\t" \
  "v_mov_b64 v[198:199], 0\n\t" \
  "s_add_u32 s92, s92, 1\n\t" \
  "s_cmp_eq_u32 s92, 1\n\t" \
  "s_cbranch_scc1 .Lpoll%=\n\t" \
  "s_sleep 2\n\t" \
  "s_and_b32 s93, s92, 63\n\t" \
  "s_cmp_lg_u32 s93, 0\n\t" \
  "s_cbranch_scc1 .Lpoll%=\n\t" \
  "v_mov_b32 v245, 0\n\t" \
  "buffer_load_dword v244, v245, %[rs], %[sab] offen sc1\n\t" \
  "s_waitcnt vmcnt(0)\n\t" \
  "v_readfirstlane_b32 s93, v244\n\t" \
  "s_nop 3\n\t" \
  "s_cmp_eq_u32 s93, 1\n\t" \
  "s_cbranch_scc1 .Lok%=\n\t" \
  "s_cmp_lt_u32 s92, 0x80000\n\t" \
  "s_cbranch_scc1 .Lpoll%=\n\t" \
  "v_mov_b32 v244, 1\n\t" \
  "buffer_store_dword v244, v245, %[rs], %[sab] offen sc1\n\t" \
  "s_branch .Lok%=\n\t" \
  ".Lend%=:\n\t"
#define VV_ASM \
  "s_waitcnt lgkmcnt(0)\n\t" \
  "v_mov_b64 v[128:129], 0\n\t" \
  "v_mov_b64 v[130:131], 0\n\t" \
  "s_movk_i32 s92, 0x2000\n\t" \
  ".Lwt%=:\n\t" \
  "ds_read_b64 v[132:133], v200 offset:30720\n\t" \
  "s_memrealtime s[90:91]\n\t" \
  "s_sub_u32 s92, s92, 1\n\t" \
  "s_waitcnt lgkmcnt(0)\n\t" \
  "v_readfirstlane_b32 s93, v132\n\t" \
  "v_readfirstlane_b32 %[tgt], v133\n\t" \
  "s_cbranch_scc1 .Lgo%=\n\t" \
  "s_cmp_eq_u32 s93, %[step]\n\t" \
  "s_cbranch_scc1 .Lwx%=\n\t" \
  "s_sleep 1\n\t" \
  "s_branch .Lwt%=\n\t" \
  ".Lwx%=:\n\t" \
  "s_lshl_b32 s93, s90, 2\n\t" \
  "s_sub_i32 s93, %[tgt], s93\n\t" \
  "s_cmp_le_i32 s93, 0\n\t" \
  "s_cbranch_scc1 .Lgo%=\n\t" \
  "s_min_i32 s93, s93, 8000\n\t" \
  "s_mov_b32 s94, s93\n\t" \
  ".Lsl%=:\n\t" \
  "s_sub_i32 s94, s94, 18\n\t" \
  "s_cmp_lt_i32 s94, 30\n\t" \
  "s_cbranch_scc1 .Lsx%=\n\t" \
  "s_sleep 1\n\t" \
  "s_branch .Lsl%=\n\t" \
  ".Lsx%=:\n\t" \
  "s_sub_i32 s93, s93, 12\n\t" \
  "s_max_i32 s93, s93, 4\n\t" \
  "s_lshr_b32 s93, s93, 2\n\t" \
  "s_add_u32 s93, s93, s90\n\t" \
  "s_memrealtime s[90:91]\n\t" \
  ".Lpace%=:\n\t" \
  "s_memrealtime s[94:95]\n\t" \
  "s_waitcnt lgkmcnt(1)\n\t" \
  "s_sub_i32 s92, s90, s93\n\t" \
  "s_cmp_ge_i32 s92, 0\n\t" \
  "s_cbranch_scc1 .Le%=\n\t" \
  "s_memrealtime s[90:91]\n\t" \
  "s_waitcnt lgkmcnt(1)\n\t" \
  "s_sub_i32 s92, s94, s93\n\t" \
  "s_cmp_lt_i32 s92, 0\n\t" \
  "s_cbranch_scc1 .Lpace%=\n\t" \
  "s_nop 4\n\t" \
  ".Le%=:\n\t" \
  "s_add_u32 s92, s92, s93\n\t" \
  "s_lshl_b32 s92, s92, 2\n\t" \
  "s_sub_i32 s92, %[tgt], s92\n\t" \
  "s_max_i32 s92, s92, 0\n\t" \
  "s_bitcmp0_b32 s92, 3\n\t" \
  "s_cbranch_scc1 .Lf3%=\n\t" \
  "s_nop 15\n\t" \
  ".Lf3%=:\n\t" \
  "s_bitcmp0_b32 s92, 2\n\t" \
  "s_cbranch_scc1 .Lf2%=\n\t" \
  "s_nop 10\n\t" \
  ".Lf2%=:\n\t" \
  ".Lgo%=:\n\t" \
  "s_mov_b32 s92, 0\n\t" \
  ".Lpoll%=:\n\t" \
  "buffer_load_dwordx4 v[132:135], v200, %[rs], %[som] offen sc1\n\t" \
  "buffer_load_dwordx4 v[136:139], v200, %[rs], %[som2] offen sc1\n\t" \
  "s_waitcnt vmcnt(1) lgkmcnt(0)\n\t" \
  "ds_write_b128 v232, v[132:135]\n\t" \
  "v_smfmac_f32_16x16x64_bf16 v[128:131], v[132:135], v[0:7], v202\n\t" \
  "v_max3_u32 v196, v132, v133, v134\n\t" \
  "s_waitcnt vmcnt(0)\n\t" \
  "ds_write_b128 v232, v[136:139] offset:8192\n\t" \
  "v_smfmac_f32_16x16x64_bf16 v[128:131], v[136:139], v[64:71], v202\n\t" \
  "v_max3_u32 v196, v196, v135, v136\n\t" \
  "v_max3_u32 v196, v196, v137, v138\n\t" \
  "v_max_u32 v196, v196, v139\n\t" \
  "v_cmp_le_u32 vcc, %[sent], v196\n\t" \
  "s_cbranch_vccnz .Lslow%=\n\t" \
  ".Lok%=:\n\t" \
  "s_waitcnt lgkmcnt(0)\n\t" \
  "s_barrier\n\t" \
  "ds_read_b128 v[164:167], v233 offset:0\n\t" \
  "ds_read_b128 v[140:143], v234 offset:0\n\t" \
  "ds_read_b128 v[144:147], v235 offset:0\n\t" \
  "ds_read_b128 v[148:151], v236 offset:0\n\t" \
  "ds_read_b128 v[152:155], v237 offset:0\n\t" \
  "s_waitcnt lgkmcnt(4)\n\t" \
  "ds_read_b128 v[156:159], v238 offset:0\n\t" \
  "v_smfmac_f32_16x16x64_bf16 v[128:131], v[164:167], v[8:15], v202\n\t" \
  "s_waitcnt lgkmcnt(4)\n\t" \
  "ds_read_b128 v[160:163], v239 offset:0\n\t" \
  "v_smfmac_f32_16x16x64_bf16 v[128:131], v[140:143], v[16:23], v202\n\t" \
  "s_waitcnt lgkmcnt(4)\n\t" \
  "ds_read_b128 v[168:171], v233 offset:8192\n\t" \
  "v_smfmac_f32_16x16x64_bf16 v[128:131], v[144:147], v[24:31], v202\n\t" \
  "s_waitcnt lgkmcnt(4)\n\t" \
  "ds_read_b128 v[172:175], v234 offset:8192\n\t" \
  "v_smfmac_f32_16x16x64_bf16 v[128:131], v[148:151], v[32:39], v202\n\t" \
  "s_waitcnt lgkmcnt(4)\n\t" \
  "ds_read_b128 v[176:179], v235 offset:8192\n\t" \
  "v_smfmac_f32_16x16x64_bf16 v[128:131], v[152:155], v[40:47], v202\n\t" \
  "s_waitcnt lgkmcnt(4)\n\t" \
  "ds_read_b128 v[180:183], v236 offset:8192\n\t" \
  "v_smfmac_f32_16x16x64_bf16 v[128:131], v[156:159], v[48:55], v202\n\t" \
  "s_waitcnt lgkmcnt(4)\n\t" \
  "ds_read_b128 v[184:187], v237 offset:8192\n\t" \
  "v_smfmac_f32_16x16x64_bf16 v[128:131], v[160:163], v[56:63], v202\n\t" \
  "s_waitcnt lgkmcnt(4)\n\t" \
  "ds_read_b128 v[188:191], v238 offset:8192\n\t" \
  "v_smfmac_f32_16x16x64_bf16 v[128:131], v[168:171], v[72:79], v202\n\t" \
  "s_waitcnt lgkmcnt(4)\n\t" \
  "ds_read_b128 v[192:195], v239 offset:8192\n\t" \
  "v_smfmac_f32_16x16x64_bf16 v[128:131], v[172:175], v[80:87], v202\n\t" \
  "s_waitcnt lgkmcnt(4)\n\t" \
  "v_smfmac_f32_16x16x64_bf16 v[128:131], v[176:179], v[88:95], v202\n\t" \
  "s_waitcnt lgkmcnt(3)\n\t" \
  "v_smfmac_f32_16x16x64_bf16 v[128:131], v[180:183], v[96:103], v202\n\t" \
  "s_waitcnt lgkmcnt(2)\n\t" \
  "v_smfmac_f32_16x16x64_bf16 v[128:131], v[184:187], v[104:111], v202\n\t" \
  "s_waitcnt lgkmcnt(1)\n\t" \
  "v_smfmac_f32_16x16x64_bf16 v[128:131], v[188:191], v[112:119], v202\n\t" \
  "s_waitcnt lgkmcnt(0)\n\t" \
  "v_smfmac_f32_16x16x64_bf16 v[128:131], v[192:195], v[120:127], v202\n\t" \
  "s_nop 7\n\t" \
  "v_add_f32 v128, v128, v130\n\t" \
  "v_add_f32 v129, v129, v131\n\t" \
  "v_cvt_pk_bf16_f32 v128, v128, v129\n\t" \
  "buffer_store_dword v128, v203, %[rs], %[sv] offen sc1\n\t" \
  "ds_write_b32 v204, v128\n\t" \
  "s_waitcnt lgkmcnt(0)\n\t" \
  "s_barrier\n\t" \
  "s_mov_b32 %[spins], s92\n\t" \
  "s_branch .Lend%=\n\t" \
  ".Lslow%=:\n\t" \
  "s_mov_b64 s[94:95], 0\n\t" \
  "v_cmp_eq_u32 vcc, %[sent], v132\n\t" \
  "s_or_b64 s[94:95], s[94:95], vcc\n\t" \
  "v_cmp_eq_u32 vcc, %[sent], v133\n\t" \
  "s_or_b64 s[94:95], s[94:95], vcc\n\t" \
  "v_cmp_eq_u32 vcc, %[sent], v134\n\t" \
  "s_or_b64 s[94:95], s[94:95], vcc\n\t" \
  "v_cmp_eq_u32 vcc, %[sent], v135\n\t" \
  "s_or_b64 s[94:95], s[94:95], vcc\n\t" \
  "v_cmp_eq_u32 vcc, %[sent], v136\n\t" \
  "s_or_b64 s[94:95], s[94:95], vcc\n\t" \
  "v_cmp_eq_u32 vcc, %[sent], v137\n\t" \
  "s_or_b64 s[94:95], s[94:95], vcc\n\t" \
  "v_cmp_eq_u32 vcc, %[sent], v138\n\t" \
  "s_or_b64 s[94:95], s[94:95], vcc\n\t" \
  "v_cmp_eq_u32 vcc, %[sent], v139\n\t" \
  "s_or_b64 s[94:95], s[94:95], vcc\n\t" \
  "s_cmp_eq_u64 s[94:95], 0\n\t" \
  "s_cbranch_scc1 .Lok%=\n\t" \
  "v_mov_b64 v[128:129], 0\n\t" \
  "v_mov_b64 v[130:131], 0\n\t" \
  "s_add_u32 s92, s92, 1\n\t" \
  "s_cmp_eq_u32 s92, 1\n\t" \
  "s_cbranch_scc1 .Lpoll%=\n\t" \
  "s_sleep 2\n\t" \
  "s_and_b32 s93, s92, 63\n\t" \
  "s_cmp_lg_u32 s93, 0\n\t" \
  "s_cbranch_scc1 .Lpoll%=\n\t" \
  "v_mov_b32 v197, 0\n\t" \
  "buffer_load_dword v196, v197, %[rs], %[sab] offen sc1\n\t" \
  "s_waitcnt vmcnt(0)\n\t" \
  "v_readfirstlane_b32 s93, v196\n\t" \
  "s_nop 3\n\t" \
  "s_cmp_eq_u32 s93, 1\n\t" \
  "s_cbranch_scc1 .Lok%=\n\t" \
  "s_cmp_lt_u32 s92, 0x80000\n\t" \
  "s_cbranch_scc1 .Lpoll%=\n\t" \
  "v_mov_b32 v196, 1\n\t" \
  "buffer_store_dword v196, v197, %[rs], %[sab] offen sc1\n\t" \
  "s_branch .Lok%=\n\t" \
  ".Lend%=:\n\t"
#define TK_ASM \
  "buffer_load_dwordx4 v[224:227], v200, %[rs], %[sk] offen offset:0 sc1\n\t" \
  "buffer_load_dwordx4 v[228:231], v200, %[rs], %[sk] offen offset:1024 sc1\n\t" \
  "s_memrealtime s[88:89]\n\t" \
  "v_add_u32 v205, %[tl], v200\n\t" \
  "v_add_u32 v206, %[tq], v205\n\t" \
  "v_add_u32 v207, %[tv], v205\n\t" \
  "s_waitcnt lgkmcnt(0)\n\t" \
  "ds_read_b128 v[128:131], v206 offset:8192\n\t" \
  "ds_read_b128 v[144:147], v205 offset:0\n\t" \
  "ds_read_b128 v[160:163], v205 offset:4096\n\t" \
  "ds_read_b128 v[132:135], v206 offset:9216\n\t" \
  "ds_read_b128 v[148:151], v205 offset:1024\n\t" \
  "ds_read_b128 v[164:167], v205 offset:5120\n\t" \
  "ds_read_b128 v[136:139], v206 offset:10240\n\t" \
  "ds_read_b128 v[152:155], v205 offset:2048\n\t" \
  "ds_read_b128 v[168:171], v205 offset:6144\n\t" \
  "ds_read_b128 v[140:143], v206 offset:11264\n\t" \
  "ds_read_b128 v[156:159], v205 offset:3072\n\t" \
  "ds_read_b128 v[172:175], v205 offset:7168\n\t" \
  "v_add_u32 v206, %[vp], v218\n\t" \
  "s_lshl_b32 s88, s88, 2\n\t" \
  "s_add_u32 s89, s88, %[od]\n\t" \
  "s_waitcnt lgkmcnt(10)\n\t" \
  "v_mfma_f32_16x16x32_bf16 v[196:199], v[144:147], v[128:131], 0\n\t" \
  "s_waitcnt lgkmcnt(9)\n\t" \
  "v_mfma_f32_16x16x32_bf16 v[144:147], v[160:163], v[128:131], 0\n\t" \
  "s_waitcnt lgkmcnt(7)\n\t" \
  "v_mfma_f32_16x16x32_bf16 v[196:199], v[148:151], v[132:135], v[196:199]\n\t" \
  "s_waitcnt lgkmcnt(6)\n\t" \
  "v_mfma_f32_16x16x32_bf16 v[144:147], v[164:167], v[132:135], v[144:147]\n\t" \
  "s_waitcnt lgkmcnt(4)\n\t" \
  "v_mfma_f32_16x16x32_bf16 v[196:199], v[152:155], v[136:139], v[196:199]\n\t" \
  "s_waitcnt lgkmcnt(3)\n\t" \
  "v_mfma_f32_16x16x32_bf16 v[144:147], v[168:171], v[136:139], v[144:147]\n\t" \
  "s_waitcnt lgkmcnt(1)\n\t" \
  "v_mfma_f32_16x16x32_bf16 v[196:199], v[156:159], v[140:143], v[196:199]\n\t" \
  "s_waitcnt lgkmcnt(0)\n\t" \
  "v_mfma_f32_16x16x32_bf16 v[144:147], v[172:175], v[140:143], v[144:147]\n\t" \
  "ds_read_b128 v[176:179], v207 offset:16384\n\t" \
  "ds_read_b128 v[180:183], v207 offset:17408\n\t" \
  "ds_read_b128 v[184:187], v207 offset:18432\n\t" \
  "ds_read_b128 v[188:191], v207 offset:19456\n\t" \
  "ds_read2st64_b32 v[192:193], v206 offset0:0 offset1:1\n\t" \
  "ds_read2st64_b32 v[194:195], v206 offset0:2 offset1:3\n\t" \
  "v_cvt_pk_bf16_f32 v197, v196, v197\n\t" \
  "v_cvt_pk_bf16_f32 v199, v198, v199\n\t" \
  "v_cvt_pk_bf16_f32 v145, v144, v145\n\t" \
  "v_cvt_pk_bf16_f32 v147, v146, v147\n\t" \
  "v_lshlrev_b32 v196, 16, v197\n\t" \
  "v_lshlrev_b32 v198, 16, v199\n\t" \
  "v_lshlrev_b32 v144, 16, v145\n\t" \
  "v_lshlrev_b32 v146, 16, v147\n\t" \
  "v_and_b32 v197, 0xffff0000, v197\n\t" \
  "v_and_b32 v199, 0xffff0000, v199\n\t" \
  "v_and_b32 v145, 0xffff0000, v145\n\t" \
  "v_and_b32 v147, 0xffff0000, v147\n\t" \
  "v_pk_mul_f32 v[196:197], v[196:197], %[sc] op_sel_hi:[1,0]\n\t" \
  "v_pk_mul_f32 v[198:199], v[198:199], %[sc] op_sel_hi:[1,0]\n\t" \
  "v_pk_mul_f32 v[144:145], v[144:145], %[sc] op_sel_hi:[1,0]\n\t" \
  "v_pk_mul_f32 v[146:147], v[146:147], %[sc] op_sel_hi:[1,0]\n\t" \
  "v_cvt_pk_bf16_f32 v197, v196, v197\n\t" \
  "v_cvt_pk_bf16_f32 v199, v198, v199\n\t" \
  "v_cvt_pk_bf16_f32 v145, v144, v145\n\t" \
  "v_cvt_pk_bf16_f32 v147, v146, v147\n\t" \
  "v_lshlrev_b32 v196, 16, v197\n\t" \
  "v_lshlrev_b32 v198, 16, v199\n\t" \
  "v_lshlrev_b32 v144, 16, v145\n\t" \
  "v_lshlrev_b32 v146, 16, v147\n\t" \
  "v_and_b32 v197, 0xffff0000, v197\n\t" \
  "v_and_b32 v199, 0xffff0000, v199\n\t" \
  "v_and_b32 v145, 0xffff0000, v145\n\t" \
  "v_and_b32 v147, 0xffff0000, v147\n\t" \
  "v_min_f32 v196, v196, v208\n\t" \
  "v_min_f32 v197, v197, v209\n\t" \
  "v_min_f32 v198, v198, v210\n\t" \
  "v_min_f32 v199, v199, v211\n\t" \
  "v_min_f32 v144, v144, v212\n\t" \
  "v_min_f32 v145, v145, v213\n\t" \
  "v_min_f32 v146, v146, v214\n\t" \
  "v_min_f32 v147, v147, v215\n\t" \
  "v_max3_f32 v148, v196, v197, v198\n\t" \
  "v_max3_f32 v149, v199, v144, v145\n\t" \
  "v_max3_f32 v148, v148, v146, v147\n\t" \
  "v_max_f32 v150, v148, v149\n\t" \
  "v_max_f32 v151, v148, v149\n\t" \
  "s_nop 1\n\t" \
  "v_permlane32_swap_b32 v150, v151\n\t" \
  "v_max_f32 v150, v150, v151\n\t" \
  "s_mov_b32 s92, 0\n\t" \
  ".Ltake%=:\n\t" \
  "s_waitcnt vmcnt(1) lgkmcnt(0)\n\t" \
  "ds_write_b128 v216, v[224:227]\n\t" \
  "v_max3_u32 v148, v224, v225, v226\n\t" \
  "s_waitcnt vmcnt(0)\n\t" \
  "ds_write_b128 v216, v[228:231] offset:1024\n\t" \
  "ds_read_b64_tr_b16 v[160:161], v217 offset:0\n\t" \
  "ds_read_b64_tr_b16 v[162:163], v217 offset:64\n\t" \
  "ds_read_b64_tr_b16 v[164:165], v217 offset:512\n\t" \
  "ds_read_b64_tr_b16 v[166:167], v217 offset:576\n\t" \
  "ds_read_b64_tr_b16 v[168:169], v217 offset:1024\n\t" \
  "ds_read_b64_tr_b16 v[170:171], v217 offset:1088\n\t" \
  "ds_read_b64_tr_b16 v[172:173], v217 offset:1536\n\t" \
  "ds_read_b64_tr_b16 v[174:175], v217 offset:1600\n\t" \
  "v_max3_u32 v148, v148, v227, v228\n\t" \
  "v_max3_u32 v148, v148, v229, v230\n\t" \
  "v_max_u32 v148, v148, v231\n\t" \
  "v_cmp_le_u32 vcc, %[sent], v148\n\t" \
  "s_cbranch_vccnz .Lslow%=\n\t" \
  ".Lok%=:\n\t" \
  "s_waitcnt lgkmcnt(6)\n\t" \
  "v_mfma_f32_16x16x32_bf16 v[152:155], v[160:163], v[128:131], 0\n\t" \
  "s_waitcnt lgkmcnt(4)\n\t" \
  "v_mfma_f32_16x16x32_bf16 v[152:155], v[164:167], v[132:135], v[152:155]\n\t" \
  "s_waitcnt lgkmcnt(2)\n\t" \
  "v_mfma_f32_16x16x32_bf16 v[152:155], v[168:171], v[136:139], v[152:155]\n\t" \
  "s_waitcnt lgkmcnt(0)\n\t" \
  "v_mfma_f32_16x16x32_bf16 v[152:155], v[172:175], v[140:143], v[152:155]\n\t" \
  "v_mov_b32 v157, 0\n\t" \
  "v_mov_b32 v158, 0\n\t" \
  "s_nop 5\n\t" \
  "v_cvt_pk_bf16_f32 v153, v152, v153\n\t" \
  "v_cvt_pk_bf16_f32 v155, v154, v155\n\t" \
  "v_lshlrev_b32 v152, 16, v153\n\t" \
  "v_lshlrev_b32 v154, 16, v155\n\t" \
  "v_and_b32 v153, 0xffff0000, v153\n\t" \
  "v_and_b32 v155, 0xffff0000, v155\n\t" \
  "v_pk_mul_f32 v[152:153], v[152:153], %[sc] op_sel_hi:[1,0]\n\t" \
  "v_pk_mul_f32 v[154:155], v[154:155], %[sc] op_sel_hi:[1,0]\n\t" \
  "v_cvt_pk_bf16_f32 v153, v152, v153\n\t" \
  "v_cvt_pk_bf16_f32 v155, v154, v155\n\t" \
  "v_lshlrev_b32 v152, 16, v153\n\t" \
  "v_lshlrev_b32 v154, 16, v155\n\t" \
  "v_and_b32 v153, 0xffff0000, v153\n\t" \
  "v_and_b32 v155, 0xffff0000, v155\n\t" \
  "v_max3_f32 v148, v152, v153, v150\n\t" \
  "v_max3_f32 v149, v148, v154, v155\n\t" \
  "v_max3_f32 v150, v148, v154, v155\n\t" \
  "v_cndmask_b32 v152, v152, v154, %[up]\n\t" \
  "v_cndmask_b32 v153, v153, v155, %[up]\n\t" \
  "v_permlane16_swap_b32 v149, v150\n\t" \
  "v_max_f32 v148, v149, v150\n\t" \
  "v_mul_f32 v148, %[negc], v148\n\t" \
  "v_fma_f32 v152, v152, %[c], v148\n\t" \
  "v_fma_f32 v153, v153, %[c], v148\n\t" \
  "v_exp_f32 v152, v152\n\t" \
  "v_fma_f32 v196, v196, %[c], v148\n\t" \
  "v_exp_f32 v153, v153\n\t" \
  "v_fma_f32 v197, v197, %[c], v148\n\t" \
  "v_exp_f32 v196, v196\n\t" \
  "v_fma_f32 v198, v198, %[c], v148\n\t" \
  "v_exp_f32 v197, v197\n\t" \
  "v_fma_f32 v199, v199, %[c], v148\n\t" \
  "v_exp_f32 v198, v198\n\t" \
  "v_fma_f32 v144, v144, %[c], v148\n\t" \
  "v_pk_add_f32 v[132:133], v[152:153], v[196:197]\n\t" \
  "v_exp_f32 v199, v199\n\t" \
  "v_fma_f32 v145, v145, %[c], v148\n\t" \
  "v_exp_f32 v144, v144\n\t" \
  "v_fma_f32 v146, v146, %[c], v148\n\t" \
  "v_pk_add_f32 v[132:133], v[132:133], v[198:199]\n\t" \
  "v_exp_f32 v145, v145\n\t" \
  "v_fma_f32 v147, v147, %[c], v148\n\t" \
  "v_exp_f32 v146, v146\n\t" \
  "v_pk_add_f32 v[132:133], v[132:133], v[144:145]\n\t" \
  "v_exp_f32 v147, v147\n\t" \
  "s_nop 0\n\t" \
  "v_pk_add_f32 v[132:133], v[132:133], v[146:147]\n\t" \
  "v_add_f32 v134, v132, v133\n\t" \
  "v_add_f32 v135, v132, v133\n\t" \
  "s_nop 1\n\t" \
  "v_permlane16_swap_b32 v134, v135\n\t" \
  "v_add_f32 v132, v134, v135\n\t" \
  "v_add_f32 v133, v134, v135\n\t" \
  "s_nop 1\n\t" \
  "v_permlane32_swap_b32 v132, v133\n\t" \
  "v_add_f32 v132, v132, v133\n\t" \
  "v_rcp_f32 v148, v132\n\t" \
  "s_nop 0\n\t" \
  "v_pk_mul_f32 v[152:153], v[152:153], v[148:149] op_sel_hi:[1,0]\n\t" \
  "v_pk_mul_f32 v[196:197], v[196:197], v[148:149] op_sel_hi:[1,0]\n\t" \
  "v_pk_mul_f32 v[198:199], v[198:199], v[148:149] op_sel_hi:[1,0]\n\t" \
  "v_pk_mul_f32 v[144:145], v[144:145], v[148:149] op_sel_hi:[1,0]\n\t" \
  "v_pk_mul_f32 v[146:147], v[146:147], v[148:149] op_sel_hi:[1,0]\n\t" \
  "v_cvt_pk_bf16_f32 v156, v152, v153\n\t" \
  "v_cvt_pk_bf16_f32 v159, v152, v153\n\t" \
  "v_cvt_pk_bf16_f32 v128, v196, v197\n\t" \
  "v_cvt_pk_bf16_f32 v129, v198, v199\n\t" \
  "v_mfma_f32_16x16x16_bf16 v[160:163], v[192:193], v[156:157], 0\n\t" \
  "v_cvt_pk_bf16_f32 v130, v144, v145\n\t" \
  "v_mfma_f32_16x16x16_bf16 v[164:167], v[192:193], v[158:159], 0\n\t" \
  "v_cvt_pk_bf16_f32 v131, v146, v147\n\t" \
  "v_mfma_f32_16x16x16_bf16 v[168:171], v[194:195], v[156:157], 0\n\t" \
  "v_mfma_f32_16x16x16_bf16 v[172:175], v[194:195], v[158:159], 0\n\t" \
  "s_nop 0\n\t" \
  "v_mfma_f32_16x16x32_bf16 v[160:163], v[176:179], v[128:131], v[160:163]\n\t" \
  "s_nop 0\n\t" \
  "v_mfma_f32_16x16x32_bf16 v[164:167], v[180:183], v[128:131], v[164:167]\n\t" \
  "s_nop 0\n\t" \
  "v_mfma_f32_16x16x32_bf16 v[168:171], v[184:187], v[128:131], v[168:171]\n\t" \
  "v_mfma_f32_16x16x32_bf16 v[172:175], v[188:191], v[128:131], v[172:175]\n\t" \
  "s_nop 2\n\t" \
  "v_cvt_pk_bf16_f32 v136, v160, v161\n\t" \
  "v_cvt_pk_bf16_f32 v137, v162, v163\n\t" \
  "v_cvt_pk_bf16_f32 v138, v164, v165\n\t" \
  "v_cvt_pk_bf16_f32 v139, v166, v167\n\t" \
  "v_cvt_pk_bf16_f32 v140, v168, v169\n\t" \
  "v_cvt_pk_bf16_f32 v141, v170, v171\n\t" \
  "v_cvt_pk_bf16_f32 v142, v172, v173\n\t" \
  "v_cvt_pk_bf16_f32 v143, v174, v175\n\t" \
  "s_memrealtime s[90:91]\n\t" \
  "s_waitcnt lgkmcnt(0)\n\t" \
  "s_lshl_b32 s93, s90, 2\n\t" \
  "s_sub_i32 s93, s89, s93\n\t" \
  "s_cmp_le_i32 s93, 0\n\t" \
  "s_cbranch_scc1 .Lgoo%=\n\t" \
  "s_min_i32 s93, s93, 8000\n\t" \
  "s_mov_b32 s94, s93\n\t" \
  ".Lslo%=:\n\t" \
  "s_sub_i32 s94, s94, 18\n\t" \
  "s_cmp_lt_i32 s94, 30\n\t" \
  "s_cbranch_scc1 .Lsxo%=\n\t" \
  "s_sleep 1\n\t" \
  "s_branch .Lslo%=\n\t" \
  ".Lsxo%=:\n\t" \
  "s_add_u32 s93, s93, 3\n\t" \
  "s_lshr_b32 s93, s93, 2\n\t" \
  "s_add_u32 s93, s93, s90\n\t" \
  "s_memrealtime s[90:91]\n\t" \
  ".Lpaceo%=:\n\t" \
  "s_memrealtime s[94:95]\n\t" \
  "s_waitcnt lgkmcnt(1)\n\t" \
  "s_sub_i32 s92, s90, s93\n\t" \
  "s_cmp_ge_i32 s92, 0\n\t" \
  "s_cbranch_scc1 .Leo%=\n\t" \
  "s_memrealtime s[90:91]\n\t" \
  "s_waitcnt lgkmcnt(1)\n\t" \
  "s_sub_i32 s92, s94, s93\n\t" \
  "s_cmp_lt_i32 s92, 0\n\t" \
  "s_cbranch_scc1 .Lpaceo%=\n\t" \
  ".Leo%=:\n\t" \
  ".Lgoo%=:\n\t" \
  "buffer_store_dwordx4 v[136:139], v200, %[rt], %[so] offen offset:0 sc1\n\t" \
  "buffer_store_dwordx4 v[140:143], v200, %[rt], %[so] offen offset:1024 sc1\n\t" \
  "s_waitcnt lgkmcnt(0)\n\t" \
  "s_branch .Lend%=\n\t" \
  ".Lpoll%=:\n\t" \
  "buffer_load_dwordx4 v[224:227], v200, %[rs], %[sk] offen offset:0 sc1\n\t" \
  "buffer_load_dwordx4 v[228:231], v200, %[rs], %[sk] offen offset:1024 sc1\n\t" \
  "s_branch .Ltake%=\n\t" \
  "s_branch .Lend%=\n\t" \
  ".Lslow%=:\n\t" \
  "s_mov_b64 s[94:95], 0\n\t" \
  "v_cmp_eq_u32 vcc, %[sent], v224\n\t" \
  "s_or_b64 s[94:95], s[94:95], vcc\n\t" \
  "v_cmp_eq_u32 vcc, %[sent], v225\n\t" \
  "s_or_b64 s[94:95], s[94:95], vcc\n\t" \
  "v_cmp_eq_u32 vcc, %[sent], v226\n\t" \
  "s_or_b64 s[94:95], s[94:95], vcc\n\t" \
  "v_cmp_eq_u32 vcc, %[sent], v227\n\t" \
  "s_or_b64 s[94:95], s[94:95], vcc\n\t" \
  "v_cmp_eq_u32 vcc, %[sent], v228\n\t" \
  "s_or_b64 s[94:95], s[94:95], vcc\n\t" \
  "v_cmp_eq_u32 vcc, %[sent], v229\n\t" \
  "s_or_b64 s[94:95], s[94:95], vcc\n\t" \
  "v_cmp_eq_u32 vcc, %[sent], v230\n\t" \
  "s_or_b64 s[94:95], s[94:95], vcc\n\t" \
  "v_cmp_eq_u32 vcc, %[sent], v231\n\t" \
  "s_or_b64 s[94:95], s[94:95], vcc\n\t" \
  "s_cmp_eq_u64 s[94:95], 0\n\t" \
  "s_cbranch_scc1 .Lok%=\n\t" \
  "s_add_u32 s92, s92, 1\n\t" \
  "s_cmp_eq_u32 s92, 1\n\t" \
  "s_cbranch_scc1 .Lpoll%=\n\t" \
  "s_sleep 2\n\t" \
  "s_and_b32 s93, s92, 63\n\t" \
  "s_cmp_lg_u32 s93, 0\n\t" \
  "s_cbranch_scc1 .Lpoll%=\n\t" \
  "v_mov_b32 v149, 0\n\t" \
  "buffer_load_dword v148, v149, %[rs], %[sab] offen sc1\n\t" \
  "s_waitcnt vmcnt(0)\n\t" \
  "v_readfirstlane_b32 s93, v148\n\t" \
  "s_nop 3\n\t" \
  "s_cmp_eq_u32 s93, 1\n\t" \
  "s_cbranch_scc1 .Lok%=\n\t" \
  "s_cmp_lt_u32 s92, 0x80000\n\t" \
  "s_cbranch_scc1 .Lpoll%=\n\t" \
  "v_mov_b32 v148, 1\n\t" \
  "buffer_store_dword v148, v149, %[rs], %[sab] offen sc1\n\t" \
  "s_branch .Lok%=\n\t" \
  ".Lend%=:\n\t"
constexpr int TLAG = 2;
#define RED_ASM \
  "s_mov_b32 %[step], 0\n\t" \
  "s_lshl_b32 %[sh], %[step], 15\n\t" \
  "s_lshr_b32 s93, %[step], 4\n\t" \
  "s_lshr_b32 s93, %[pm], s93\n\t" \
  "s_and_b32 s93, s93, 4\n\t" \
  "s_lshl_b32 s93, s93, 11\n\t" \
  "s_lshl2_add_u32 %[so], %[sh], s93\n\t" \
  "s_sub_i32 s93, %[step], 2\n\t" \
  "s_lshl_b32 %[st], s93, 19\n\t" \
  "s_lshl_b32 s94, s93, 11\n\t" \
  "s_xor_b32 s94, s94, %[s0]\n\t" \
  "s_and_b32 s94, s94, 0x2000\n\t" \
  "s_or_b32 %[st], %[st], s94\n\t" \
  "s_cmp_lt_i32 s93, 0\n\t" \
  "s_cselect_b32 %[st], 0x70000000, %[st]\n\t" \
  "s_add_u32 %[st4], %[st], 4096\n\t" \
  "buffer_load_dwordx2 v[166:167], v173, %[rs], 0 offen sc1\n\t" \
  "v_mov_b64 v[144:145], 0\n\t" \
  "v_mov_b64 v[146:147], 0\n\t" \
  "v_mov_b64 v[148:149], 0\n\t" \
  "v_mov_b64 v[150:151], 0\n\t" \
  "v_mov_b64 v[152:153], 0\n\t" \
  "v_mov_b64 v[154:155], 0\n\t" \
  "v_mov_b64 v[156:157], 0\n\t" \
  "v_mov_b64 v[158:159], 0\n\t" \
  ".Lstep%=:\n\t" \
  "s_setprio 3\n\t" \
  "s_mov_b32 s92, 0\n\t" \
  ".Lcal%=:\n\t" \
  "s_waitcnt vmcnt(0)\n\t" \
  "s_memrealtime s[76:77]\n\t" \
  "v_cmp_ne_u32 vcc, %[step], v166\n\t" \
  "v_readlane_b32 s90, v167, 0\n\t" \
  "v_readlane_b32 s91, v167, 1\n\t" \
  "v_readlane_b32 s94, v167, 2\n\t" \
  "v_readlane_b32 s95, v167, 3\n\t" \
  "s_sub_i32 s90, s90, %[base]\n\t" \
  "s_sub_i32 s91, s91, %[base]\n\t" \
  "s_sub_i32 s94, s94, %[base]\n\t" \
  "s_sub_i32 s95, s95, %[base]\n\t" \
  "s_max_i32 s90, s90, s91\n\t" \
  "s_max_i32 s94, s94, s95\n\t" \
  "s_max_i32 s90, s90, s94\n\t" \
  "s_add_i32 s90, s90, %[ds]\n\t" \
  "s_waitcnt lgkmcnt(0)\n\t" \
  "s_lshl2_add_u32 s76, s76, %[mn]\n\t" \
  "s_sub_i32 s76, s76, %[base]\n\t" \
  "s_add_u32 %[E], s90, %[base]\n\t" \
  "s_and_b32 s93, vcc_lo, 0xf\n\t" \
  "s_cbranch_scc1 .Lbl%=\n\t" \
  ".Lgo%=:\n\t" \
  "v_mov_b32 v166, 7\n\t" \
  "s_mov_b64 exec, 1\n\t" \
  "v_mov_b32 v164, s90\n\t" \
  "ds_max_u32 v168, v164 offset:4100\n\t" \
  "ds_add_u32 v168, v164 offset:4096\n\t" \
  "s_mov_b64 exec, -1\n\t" \
  "s_memrealtime s[90:91]\n\t" \
  "s_waitcnt lgkmcnt(0)\n\t" \
  "s_lshl_b32 s93, s90, 2\n\t" \
  "s_sub_i32 s93, %[E], s93\n\t" \
  "s_cmp_le_i32 s93, 0\n\t" \
  "s_cbranch_scc1 .Lgop%=\n\t" \
  "s_min_i32 s93, s93, 8000\n\t" \
  "s_mov_b32 s94, s93\n\t" \
  ".Lslp%=:\n\t" \
  "s_sub_i32 s94, s94, 18\n\t" \
  "s_cmp_lt_i32 s94, 30\n\t" \
  "s_cbranch_scc1 .Lsxp%=\n\t" \
  "s_sleep 1\n\t" \
  "s_branch .Lslp%=\n\t" \
  ".Lsxp%=:\n\t" \
  "s_sub_i32 s93, s93, 12\n\t" \
  "s_max_i32 s93, s93, 4\n\t" \
  "s_lshr_b32 s93, s93, 2\n\t" \
  "s_add_u32 s93, s93, s90\n\t" \
  "s_memrealtime s[90:91]\n\t" \
  ".Lpacep%=:\n\t" \
  "s_memrealtime s[94:95]\n\t" \
  "s_waitcnt lgkmcnt(1)\n\t" \
  "s_sub_i32 s92, s90, s93\n\t" \
  "s_cmp_ge_i32 s92, 0\n\t" \
  "s_cbranch_scc1 .Lep%=\n\t" \
  "s_memrealtime s[90:91]\n\t" \
  "s_waitcnt lgkmcnt(1)\n\t" \
  "s_sub_i32 s92, s94, s93\n\t" \
  "s_cmp_lt_i32 s92, 0\n\t" \
  "s_cbranch_scc1 .Lpacep%=\n\t" \
  "s_nop 4\n\t" \
  ".Lep%=:\n\t" \
  "s_add_u32 s92, s92, s93\n\t" \
  "s_lshl_b32 s92, s92, 2\n\t" \
  "s_sub_i32 s92, %[E], s92\n\t" \
  "s_max_i32 s92, s92, 0\n\t" \
  "s_bitcmp0_b32 s92, 3\n\t" \
  "s_cbranch_scc1 .Lf3p%=\n\t" \
  "s_nop 15\n\t" \
  ".Lf3p%=:\n\t" \
  "s_bitcmp0_b32 s92, 2\n\t" \
  "s_cbranch_scc1 .Lf2p%=\n\t" \
  "s_nop 10\n\t" \
  ".Lf2p%=:\n\t" \
  ".Lgop%=:\n\t" \
  "s_mov_b32 s92, 0\n\t" \
  "buffer_load_dwordx4 v[128:131], v234, %[rs], %[so] offen sc1\n\t" \
  "s_nop 15\n\t" \
  "buffer_load_dwordx4 v[132:135], v235, %[rs], %[so] offen sc1\n\t" \
  "s_nop 15\n\t" \
  "buffer_load_dwordx4 v[136:139], v236, %[rs], %[so] offen sc1\n\t" \
  "s_nop 15\n\t" \
  "buffer_load_dwordx4 v[140:143], v237, %[rs], %[so] offen sc1\n\t" \
  "s_waitcnt lgkmcnt(0)\n\t" \
  ".Lw%=:\n\t" \
  "s_waitcnt vmcnt(3)\n\t" \
  "v_smfmac_f32_16x16x64_bf16 v[144:147], v[128:131], v[0:7], v172\n\t" \
  "v_max3_u32 v164, v128, v129, 0\n\t" \
  "v_smfmac_f32_16x16x64_bf16 v[148:151], v[128:131], v[8:15], v172\n\t" \
  "v_max3_u32 v164, v130, v131, v164\n\t" \
  "v_smfmac_f32_16x16x64_bf16 v[152:155], v[128:131], v[16:23], v172\n\t" \
  "v_smfmac_f32_16x16x64_bf16 v[156:159], v[128:131], v[24:31], v172\n\t" \
  "buffer_load_dwordx4 v[176:179], v238, %[rt], %[st] offen offset:0 sc1\n\t" \
  "s_waitcnt vmcnt(3)\n\t" \
  "v_smfmac_f32_16x16x64_bf16 v[144:147], v[132:135], v[32:39], v172\n\t" \
  "v_max3_u32 v164, v132, v133, v164\n\t" \
  "v_smfmac_f32_16x16x64_bf16 v[148:151], v[132:135], v[40:47], v172\n\t" \
  "v_max3_u32 v164, v134, v135, v164\n\t" \
  "v_smfmac_f32_16x16x64_bf16 v[152:155], v[132:135], v[48:55], v172\n\t" \
  "v_smfmac_f32_16x16x64_bf16 v[156:159], v[132:135], v[56:63], v172\n\t" \
  "buffer_load_dwordx4 v[180:183], v238, %[rt], %[st] offen offset:1024 sc1\n\t" \
  "s_waitcnt vmcnt(3)\n\t" \
  "v_smfmac_f32_16x16x64_bf16 v[144:147], v[136:139], v[64:71], v172\n\t" \
  "v_max3_u32 v164, v136, v137, v164\n\t" \
  "v_smfmac_f32_16x16x64_bf16 v[148:151], v[136:139], v[72:79], v172\n\t" \
  "v_max3_u32 v164, v138, v139, v164\n\t" \
  "v_smfmac_f32_16x16x64_bf16 v[152:155], v[136:139], v[80:87], v172\n\t" \
  "v_smfmac_f32_16x16x64_bf16 v[156:159], v[136:139], v[88:95], v172\n\t" \
  "buffer_load_dwordx4 v[184:187], v238, %[rt], %[st4] offen offset:0 sc1\n\t" \
  "s_waitcnt vmcnt(3)\n\t" \
  "v_smfmac_f32_16x16x64_bf16 v[144:147], v[140:143], v[96:103], v172\n\t" \
  "v_max3_u32 v164, v140, v141, v164\n\t" \
  "v_smfmac_f32_16x16x64_bf16 v[148:151], v[140:143], v[104:111], v172\n\t" \
  "v_max3_u32 v164, v142, v143, v164\n\t" \
  "v_smfmac_f32_16x16x64_bf16 v[152:155], v[140:143], v[112:119], v172\n\t" \
  "v_cmp_le_u32 vcc, %[sent], v164\n\t" \
  "v_smfmac_f32_16x16x64_bf16 v[156:159], v[140:143], v[120:127], v172\n\t" \
  "buffer_load_dwordx4 v[188:191], v238, %[rt], %[st4] offen offset:1024 sc1\n\t" \
  "s_cbranch_vccnz .Lslow%=\n\t" \
  ".Lok%=:\n\t" \
  "v_add_f32_dpp v160, v144, v146 quad_perm:[0,1,2,3] row_mask:0x3 bank_mask:0xf\n\t" \
  "v_add_f32_dpp v161, v145, v147 quad_perm:[0,1,2,3] row_mask:0x3 bank_mask:0xf\n\t" \
  "v_add_f32_dpp v162, v148, v150 quad_perm:[0,1,2,3] row_mask:0x3 bank_mask:0xf\n\t" \
  "v_add_f32_dpp v163, v149, v151 quad_perm:[0,1,2,3] row_mask:0x3 bank_mask:0xf\n\t" \
  "v_add_f32_dpp v160, v152, v154 quad_perm:[0,1,2,3] row_mask:0xc bank_mask:0xf\n\t" \
  "v_add_f32_dpp v161, v153, v155 quad_perm:[0,1,2,3] row_mask:0xc bank_mask:0xf\n\t" \
  "v_add_f32_dpp v162, v156, v158 quad_perm:[0,1,2,3] row_mask:0xc bank_mask:0xf\n\t" \
  "v_add_f32_dpp v163, v157, v159 quad_perm:[0,1,2,3] row_mask:0xc bank_mask:0xf\n\t" \
  "s_nop 0\n\t" \
  "v_permlane32_swap_b32 v160, v162\n\t" \
  "v_permlane32_swap_b32 v161, v163\n\t" \
  "v_add_f32 v160, v160, v162\n\t" \
  "v_add_f32 v161, v161, v163\n\t" \
  "ds_write_b64 v169, v[160:161]\n\t" \
  "s_mov_b64 exec, 1\n\t" \
  "ds_inc_rtn_u32 v164, v168, v166 offset:4104\n\t" \
  "s_mov_b64 exec, -1\n\t" \
  "ds_read_b64 v[128:129], v170 offset:0\n\t" \
  "ds_read_b64 v[130:131], v170 offset:512\n\t" \
  "ds_read_b64 v[132:133], v170 offset:1024\n\t" \
  "ds_read_b64 v[134:135], v170 offset:1536\n\t" \
  "ds_read_b64 v[136:137], v170 offset:2048\n\t" \
  "ds_read_b64 v[138:139], v170 offset:2560\n\t" \
  "ds_read_b64 v[140:141], v170 offset:3072\n\t" \
  "ds_read_b64 v[142:143], v170 offset:3584\n\t" \
  "s_waitcnt lgkmcnt(8)\n\t" \
  "v_readfirstlane_b32 s93, v164\n\t" \
  "s_cmp_lg_u32 s93, 7\n\t" \
  "s_cbranch_scc1 .Lb1%=\n\t" \
  "s_waitcnt lgkmcnt(4)\n\t" \
  "v_pk_add_f32 v[128:129], v[128:129], v[130:131]\n\t" \
  "v_pk_add_f32 v[132:133], v[132:133], v[134:135]\n\t" \
  "s_waitcnt lgkmcnt(0)\n\t" \
  "v_pk_add_f32 v[136:137], v[136:137], v[138:139]\n\t" \
  "v_pk_add_f32 v[140:141], v[140:141], v[142:143]\n\t" \
  "v_pk_add_f32 v[128:129], v[128:129], v[132:133]\n\t" \
  "v_pk_add_f32 v[136:137], v[136:137], v[140:141]\n\t" \
  "v_pk_add_f32 v[128:129], v[128:129], v[136:137]\n\t" \
  "v_cvt_pk_bf16_f32 v128, v128, v129\n\t" \
  "s_nop 1\n\t" \
  "v_mov_b32_dpp v129, v128 quad_perm:[1,0,3,2] row_mask:0xf bank_mask:0xf\n\t" \
  "v_perm_b32 v128, v129, v128, v174\n\t" \
  "buffer_store_dword v128, v171, %[rs], %[sh] offen sc1\n\t" \
  "s_memtime s[90:91]\n\t" \
  "s_barrier\n\t" \
  "s_memtime s[76:77]\n\t" \
  "s_memrealtime s[78:79]\n\t" \
  "s_nop 3\n\t" \
  "s_memrealtime s[80:81]\n\t" \
  "s_nop 3\n\t" \
  "s_memrealtime s[82:83]\n\t" \
  "s_nop 3\n\t" \
  "s_memrealtime s[84:85]\n\t" \
  "s_nop 3\n\t" \
  "s_memrealtime s[86:87]\n\t" \
  "s_nop 3\n\t" \
  "s_memrealtime s[88:89]\n\t" \
  "s_mov_b32 s93, 0\n\t" \
  "s_waitcnt lgkmcnt(0)\n\t" \
  "s_cmp_eq_u32 s80, s78\n\t" \
  "s_addc_u32 s93, s93, 0\n\t" \
  "s_cmp_eq_u32 s82, s78\n\t" \
  "s_addc_u32 s93, s93, 0\n\t" \
  "s_cmp_eq_u32 s84, s78\n\t" \
  "s_addc_u32 s93, s93, 0\n\t" \
  "s_cmp_eq_u32 s86, s78\n\t" \
  "s_addc_u32 s93, s93, 0\n\t" \
  "s_cmp_eq_u32 s88, s78\n\t" \
  "s_addc_u32 s93, s93, 0\n\t" \
  "s_mul_i32 s93, s93, 21\n\t" \
  "s_add_u32 s76, s76, 15\n\t" \
  "s_add_u32 s76, s76, s93\n\t" \
  "s_lshl2_add_u32 s78, s78, 16\n\t" \
  "s_mov_b64 exec, 1\n\t" \
  "s_sub_i32 s90, s76, s90\n\t" \
  "s_mul_i32 s90, s90, 11900\n\t" \
  "s_ashr_i32 s90, s90, 16\n\t" \
  "s_sub_u32 s90, s78, s90\n\t" \
  "s_sub_u32 s90, s90, %[base]\n\t" \
  "v_mov_b32 v164, s90\n\t" \
  "ds_write_b32 v168, v164 offset:4108\n\t" \
  "s_mov_b64 exec, -1\n\t" \
  "s_waitcnt vmcnt(1)\n\t" \
  ".Lb2%=:\n\t" \
  "s_setprio 0\n\t" \
  "s_bitcmp0_b64 %[am], %[step]\n\t" \
  "s_cbranch_scc1 .La1%=\n\t" \
  "buffer_store_dwordx4 v[224:227], v228, %[ra], 0 offen sc1\n\t" \
  ".La1%=:\n\t" \
  "buffer_load_dwordx4 v[192:195], v239, %[rt], %[st] offen offset:0 sc1\n\t" \
  "buffer_load_dwordx4 v[196:199], v239, %[rt], %[st] offen offset:1024 sc1\n\t" \
  "v_mfma_f32_16x16x32_bf16 v[128:131], v[0:3], v[176:179], 0\n\t" \
  "v_mfma_f32_16x16x32_bf16 v[136:139], v[8:11], v[176:179], 0\n\t" \
  "ds_read2st64_b32 v[144:145], v231 offset0:0 offset1:8\n\t" \
  "ds_read2st64_b32 v[146:147], v231 offset0:16 offset1:24\n\t" \
  "ds_read2st64_b32 v[148:149], v231 offset0:32 offset1:40\n\t" \
  "ds_read2st64_b32 v[150:151], v231 offset0:48 offset1:56\n\t" \
  "buffer_load_dwordx4 v[200:203], v239, %[rt], %[st4] offen offset:0 sc1\n\t" \
  "buffer_load_dwordx4 v[204:207], v239, %[rt], %[st4] offen offset:1024 sc1\n\t" \
  "v_mfma_f32_16x16x32_bf16 v[132:135], v[4:7], v[180:183], 0\n\t" \
  "v_mfma_f32_16x16x32_bf16 v[140:143], v[12:15], v[180:183], 0\n\t" \
  "s_waitcnt lgkmcnt(0)\n\t" \
  "v_add_f32 v144, v144, v145\n\t" \
  "v_add_f32 v146, v146, v147\n\t" \
  "v_add_f32 v148, v148, v149\n\t" \
  "v_add_f32 v150, v150, v151\n\t" \
  "v_add_f32 v144, v144, v146\n\t" \
  "v_add_f32 v148, v148, v150\n\t" \
  "buffer_load_dwordx4 v[208:211], v240, %[rt], %[st] offen offset:0 sc1\n\t" \
  "buffer_load_dwordx4 v[212:215], v240, %[rt], %[st] offen offset:1024 sc1\n\t" \
  "v_mfma_f32_16x16x32_bf16 v[128:131], v[16:19], v[184:187], v[128:131]\n\t" \
  "v_mfma_f32_16x16x32_bf16 v[136:139], v[24:27], v[184:187], v[136:139]\n\t" \
  "v_add_f32 v144, v144, v148\n\t" \
  "v_cvt_pk_bf16_f32 v144, v144, v144\n\t" \
  "ds_write_b16 v232, v144\n\t" \
  "ds_read_b128 v[224:227], v229\n\t" \
  "v_add_u32 v228, %[ainc], v228\n\t" \
  "v_add_u32 v229, %[rinc], v229\n\t" \
  "v_add_u32 v232, 1024, v232\n\t" \
  "v_xor_b32 v231, 0x4000, v231\n\t" \
  "buffer_load_dwordx4 v[216:219], v240, %[rt], %[st4] offen offset:0 sc1\n\t" \
  "buffer_load_dwordx4 v[220:223], v240, %[rt], %[st4] offen offset:1024 sc1\n\t" \
  "v_mfma_f32_16x16x32_bf16 v[132:135], v[20:23], v[188:191], v[132:135]\n\t" \
  "v_mfma_f32_16x16x32_bf16 v[140:143], v[28:31], v[188:191], v[140:143]\n\t" \
  "buffer_load_dwordx4 v[176:179], v241, %[rt], %[st] offen offset:0 sc1\n\t" \
  "s_waitcnt vmcnt(8)\n\t" \
  "v_mfma_f32_16x16x32_bf16 v[128:131], v[32:35], v[192:195], v[128:131]\n\t" \
  "v_mfma_f32_16x16x32_bf16 v[136:139], v[40:43], v[192:195], v[136:139]\n\t" \
  "buffer_load_dwordx4 v[180:183], v241, %[rt], %[st] offen offset:1024 sc1\n\t" \
  "s_waitcnt vmcnt(8)\n\t" \
  "v_mfma_f32_16x16x32_bf16 v[132:135], v[36:39], v[196:199], v[132:135]\n\t" \
  "v_mfma_f32_16x16x32_bf16 v[140:143], v[44:47], v[196:199], v[140:143]\n\t" \
  "buffer_load_dwordx4 v[184:187], v241, %[rt], %[st4] offen offset:0 sc1\n\t" \
  "s_waitcnt vmcnt(8)\n\t" \
  "v_mfma_f32_16x16x32_bf16 v[128:131], v[48:51], v[200:203], v[128:131]\n\t" \
  "v_mfma_f32_16x16x32_bf16 v[136:139], v[56:59], v[200:203], v[136:139]\n\t" \
  "ds_read_b128 v[160:163], v168 offset:4096\n\t" \
  "buffer_load_dwordx4 v[188:191], v241, %[rt], %[st4] offen offset:1024 sc1\n\t" \
  "s_waitcnt vmcnt(8)\n\t" \
  "v_mfma_f32_16x16x32_bf16 v[132:135], v[52:55], v[204:207], v[132:135]\n\t" \
  "v_mfma_f32_16x16x32_bf16 v[140:143], v[60:63], v[204:207], v[140:143]\n\t" \
  "s_waitcnt lgkmcnt(0)\n\t" \
  "v_readfirstlane_b32 s90, v161\n\t" \
  "v_readfirstlane_b32 s91, v163\n\t" \
  "v_readfirstlane_b32 s94, v160\n\t" \
  "s_sub_u32 s93, s94, %[sm]\n\t" \
  "s_mov_b32 %[sm], s94\n\t" \
  "s_lshr_b32 s93, s93, 3\n\t" \
  "s_sub_i32 s93, s90, s93\n\t" \
  "s_mul_i32 s93, s93, %[wm]\n\t" \
  "s_ashr_i32 s93, s93, 2\n\t" \
  "s_min_u32 s93, s93, %[gm]\n\t" \
  "s_sub_i32 s90, s90, s93\n\t" \
  "s_add_u32 s93, s90, %[rl]\n\t" \
  "s_add_u32 s93, s93, %[base]\n\t" \
  "s_add_u32 %[E], s93, %[lk]\n\t" \
  "s_sub_i32 s91, s91, s90\n\t" \
  "s_sub_i32 s91, s91, %[rl]\n\t" \
  "s_min_i32 s93, s91, %[up]\n\t" \
  "s_max_i32 s93, s93, %[dn]\n\t" \
  "s_cmp_gt_i32 s91, 60\n\t" \
  "s_cselect_b32 s93, 2, s93\n\t" \
  "s_add_i32 %[rl], %[rl], s93\n\t" \
  "v_mov_b32 v160, %[rl]\n\t" \
  "buffer_store_dword v160, v175, %[rs], 0 offen sc1\n\t" \
  "s_waitcnt vmcnt(8)\n\t" \
  "v_mfma_f32_16x16x32_bf16 v[128:131], v[64:67], v[208:211], v[128:131]\n\t" \
  "v_mfma_f32_16x16x32_bf16 v[136:139], v[72:75], v[208:211], v[136:139]\n\t" \
  "s_add_u32 %[step], %[step], 1\n\t" \
  "s_lshl_b32 %[sh], %[step], 15\n\t" \
  "s_lshr_b32 s93, %[step], 4\n\t" \
  "s_lshr_b32 s93, %[pm], s93\n\t" \
  "s_and_b32 s93, s93, 4\n\t" \
  "s_lshl_b32 s93, s93, 11\n\t" \
  "s_lshl2_add_u32 %[so], %[sh], s93\n\t" \
  "s_waitcnt vmcnt(7)\n\t" \
  "v_mfma_f32_16x16x32_bf16 v[132:135], v[68:71], v[212:215], v[132:135]\n\t" \
  "v_mfma_f32_16x16x32_bf16 v[140:143], v[76:79], v[212:215], v[140:143]\n\t" \
  "s_sub_i32 s93, %[step], 2\n\t" \
  "s_lshl_b32 %[st], s93, 19\n\t" \
  "s_lshl_b32 s94, s93, 11\n\t" \
  "s_xor_b32 s94, s94, %[s0]\n\t" \
  "s_and_b32 s94, s94, 0x2000\n\t" \
  "s_or_b32 %[st], %[st], s94\n\t" \
  "s_cmp_lt_i32 s93, 0\n\t" \
  "s_cselect_b32 %[st], 0x70000000, %[st]\n\t" \
  "s_add_u32 %[st4], %[st], 4096\n\t" \
  "s_waitcnt vmcnt(6)\n\t" \
  "v_mfma_f32_16x16x32_bf16 v[128:131], v[80:83], v[216:219], v[128:131]\n\t" \
  "v_mfma_f32_16x16x32_bf16 v[136:139], v[88:91], v[216:219], v[136:139]\n\t" \
  "s_waitcnt vmcnt(5)\n\t" \
  "v_mfma_f32_16x16x32_bf16 v[132:135], v[84:87], v[220:223], v[132:135]\n\t" \
  "v_mfma_f32_16x16x32_bf16 v[140:143], v[92:95], v[220:223], v[140:143]\n\t" \
  "s_cmp_eq_u32 %[step], 63\n\t" \
  "s_cbranch_scc1 .Llast%=\n\t" \
  "s_movk_i32 s93, 600\n\t" \
  ".Lpacel%=:\n\t" \
  "s_memrealtime s[90:91]\n\t" \
  "s_waitcnt lgkmcnt(0)\n\t" \
  "s_lshl_b32 s90, s90, 2\n\t" \
  "s_sub_i32 s91, %[E], s90\n\t" \
  "s_cmp_le_i32 s91, 0\n\t" \
  "s_cbranch_scc1 .Lgol%=\n\t" \
  "s_sub_u32 s93, s93, 1\n\t" \
  "s_cmp_lg_u32 s93, 0\n\t" \
  "s_cbranch_scc1 .Lpacel%=\n\t" \
  ".Lgol%=:\n\t" \
  "buffer_load_dwordx2 v[166:167], v173, %[rs], 0 offen sc1\n\t" \
  "s_waitcnt vmcnt(5)\n\t" \
  "v_mfma_f32_16x16x32_bf16 v[128:131], v[96:99], v[176:179], v[128:131]\n\t" \
  "v_mfma_f32_16x16x32_bf16 v[136:139], v[104:107], v[176:179], v[136:139]\n\t" \
  "s_waitcnt vmcnt(4)\n\t" \
  "v_mfma_f32_16x16x32_bf16 v[132:135], v[100:103], v[180:183], v[132:135]\n\t" \
  "v_mfma_f32_16x16x32_bf16 v[140:143], v[108:111], v[180:183], v[140:143]\n\t" \
  "s_waitcnt vmcnt(3)\n\t" \
  "v_mfma_f32_16x16x32_bf16 v[128:131], v[112:115], v[184:187], v[128:131]\n\t" \
  "v_mfma_f32_16x16x32_bf16 v[136:139], v[120:123], v[184:187], v[136:139]\n\t" \
  "s_waitcnt vmcnt(2)\n\t" \
  "v_mfma_f32_16x16x32_bf16 v[132:135], v[116:119], v[188:191], v[132:135]\n\t" \
  "v_mfma_f32_16x16x32_bf16 v[140:143], v[124:127], v[188:191], v[140:143]\n\t" \
  "v_mov_b64 v[144:145], 0\n\t" \
  "v_mov_b64 v[146:147], 0\n\t" \
  "v_mov_b64 v[148:149], 0\n\t" \
  "v_mov_b64 v[150:151], 0\n\t" \
  "v_mov_b64 v[152:153], 0\n\t" \
  "v_mov_b64 v[154:155], 0\n\t" \
  "v_mov_b64 v[156:157], 0\n\t" \
  "v_mov_b64 v[158:159], 0\n\t" \
  "v_pk_add_f32 v[128:129], v[128:129], v[132:133]\n\t" \
  "v_pk_add_f32 v[130:131], v[130:131], v[134:135]\n\t" \
  "ds_write_b128 v230, v[128:131] offset:0\n\t" \
  "v_pk_add_f32 v[136:137], v[136:137], v[140:141]\n\t" \
  "v_pk_add_f32 v[138:139], v[138:139], v[142:143]\n\t" \
  "ds_write_b128 v230, v[136:139] offset:1024\n\t" \
  "v_xor_b32 v230, 0x4000, v230\n\t" \
  "s_cmp_lg_u32 %[step], 63\n\t" \
  "s_cbranch_scc1 .Lstep%=\n\t" \
  ".Lfc%=:\n\t" \
  "s_waitcnt vmcnt(0)\n\t" \
  "v_cmp_eq_u32 vcc, -1, v166\n\t" \
  "s_cbranch_vccnz .Lfw%=\n\t" \
  ".Lfo%=:\n\t" \
  "v_add_u32 v242, 16384, v242\n\t" \
  "buffer_load_dwordx4 v[176:179], v238, %[rt], %[st] offen offset:0 sc1\n\t" \
  "buffer_load_dwordx4 v[180:183], v238, %[rt], %[st] offen offset:1024 sc1\n\t" \
  "buffer_load_dwordx4 v[184:187], v238, %[rt], %[st4] offen offset:0 sc1\n\t" \
  "buffer_load_dwordx4 v[188:191], v238, %[rt], %[st4] offen offset:1024 sc1\n\t" \
  "buffer_load_dwordx4 v[192:195], v239, %[rt], %[st] offen offset:0 sc1\n\t" \
  "buffer_load_dwordx4 v[196:199], v239, %[rt], %[st] offen offset:1024 sc1\n\t" \
  "buffer_load_dwordx4 v[200:203], v239, %[rt], %[st4] offen offset:0 sc1\n\t" \
  "buffer_load_dwordx4 v[204:207], v239, %[rt], %[st4] offen offset:1024 sc1\n\t" \
  "v_subrev_u32 v230, 98304, v230\n\t" \
  ".Lt%=:\n\t" \
  "s_waitcnt vmcnt(7)\n\t" \
  "v_mfma_f32_16x16x32_bf16 v[128:131], v[0:3], v[176:179], 0\n\t" \
  "v_mfma_f32_16x16x32_bf16 v[136:139], v[8:11], v[176:179], 0\n\t" \
  "buffer_load_dwordx4 v[176:179], v240, %[rt], %[st] offen offset:0 sc1\n\t" \
  "buffer_load_dword v166, v242, %[rs], 0 offen sc1\n\t" \
  "s_waitcnt vmcnt(8)\n\t" \
  "v_mfma_f32_16x16x32_bf16 v[132:135], v[4:7], v[180:183], 0\n\t" \
  "v_mfma_f32_16x16x32_bf16 v[140:143], v[12:15], v[180:183], 0\n\t" \
  "buffer_load_dwordx4 v[180:183], v240, %[rt], %[st] offen offset:1024 sc1\n\t" \
  "s_waitcnt vmcnt(8)\n\t" \
  "v_mfma_f32_16x16x32_bf16 v[128:131], v[16:19], v[184:187], v[128:131]\n\t" \
  "v_mfma_f32_16x16x32_bf16 v[136:139], v[24:27], v[184:187], v[136:139]\n\t" \
  "buffer_load_dwordx4 v[184:187], v240, %[rt], %[st4] offen offset:0 sc1\n\t" \
  "s_waitcnt vmcnt(8)\n\t" \
  "v_mfma_f32_16x16x32_bf16 v[132:135], v[20:23], v[188:191], v[132:135]\n\t" \
  "v_mfma_f32_16x16x32_bf16 v[140:143], v[28:31], v[188:191], v[140:143]\n\t" \
  "buffer_load_dwordx4 v[188:191], v240, %[rt], %[st4] offen offset:1024 sc1\n\t" \
  "s_waitcnt vmcnt(8)\n\t" \
  "v_mfma_f32_16x16x32_bf16 v[128:131], v[32:35], v[192:195], v[128:131]\n\t" \
  "v_mfma_f32_16x16x32_bf16 v[136:139], v[40:43], v[192:195], v[136:139]\n\t" \
  "buffer_load_dwordx4 v[192:195], v241, %[rt], %[st] offen offset:0 sc1\n\t" \
  "s_waitcnt vmcnt(8)\n\t" \
  "v_mfma_f32_16x16x32_bf16 v[132:135], v[36:39], v[196:199], v[132:135]\n\t" \
  "v_mfma_f32_16x16x32_bf16 v[140:143], v[44:47], v[196:199], v[140:143]\n\t" \
  "buffer_load_dwordx4 v[196:199], v241, %[rt], %[st] offen offset:1024 sc1\n\t" \
  "s_waitcnt vmcnt(8)\n\t" \
  "v_mfma_f32_16x16x32_bf16 v[128:131], v[48:51], v[200:203], v[128:131]\n\t" \
  "v_mfma_f32_16x16x32_bf16 v[136:139], v[56:59], v[200:203], v[136:139]\n\t" \
  "buffer_load_dwordx4 v[200:203], v241, %[rt], %[st4] offen offset:0 sc1\n\t" \
  "s_waitcnt vmcnt(8)\n\t" \
  "v_mfma_f32_16x16x32_bf16 v[132:135], v[52:55], v[204:207], v[132:135]\n\t" \
  "v_mfma_f32_16x16x32_bf16 v[140:143], v[60:63], v[204:207], v[140:143]\n\t" \
  "buffer_load_dwordx4 v[204:207], v241, %[rt], %[st4] offen offset:1024 sc1\n\t" \
  "s_waitcnt vmcnt(8)\n\t" \
  "v_mfma_f32_16x16x32_bf16 v[128:131], v[64:67], v[176:179], v[128:131]\n\t" \
  "v_mfma_f32_16x16x32_bf16 v[136:139], v[72:75], v[176:179], v[136:139]\n\t" \
  "s_waitcnt vmcnt(7)\n\t" \
  ".Ltc%=:\n\t" \
  "v_cmp_eq_u32 vcc, -1, v166\n\t" \
  "s_cbranch_vccnz .Ltw%=\n\t" \
  ".Lto%=:\n\t" \
  "s_cmp_eq_u32 %[step], 65\n\t" \
  "s_cbranch_scc1 .Ltl%=\n\t" \
  "s_add_u32 %[step], %[step], 1\n\t" \
  "s_sub_i32 s93, %[step], 2\n\t" \
  "s_lshl_b32 %[st], s93, 19\n\t" \
  "s_lshl_b32 s94, s93, 11\n\t" \
  "s_xor_b32 s94, s94, %[s0]\n\t" \
  "s_and_b32 s94, s94, 0x2000\n\t" \
  "s_or_b32 %[st], %[st], s94\n\t" \
  "s_cmp_lt_i32 s93, 0\n\t" \
  "s_cselect_b32 %[st], 0x70000000, %[st]\n\t" \
  "s_add_u32 %[st4], %[st], 4096\n\t" \
  "v_add_u32 v242, 16384, v242\n\t" \
  "s_cmp_eq_u32 %[step], 65\n\t" \
  "s_cbranch_scc0 .Ltn%=\n\t" \
  "v_mov_b32 v242, %[fg]\n\t" \
  ".Ltn%=:\n\t" \
  "buffer_load_dwordx4 v[176:179], v238, %[rt], %[st] offen offset:0 sc1\n\t" \
  "s_waitcnt vmcnt(7)\n\t" \
  "v_mfma_f32_16x16x32_bf16 v[132:135], v[68:71], v[180:183], v[132:135]\n\t" \
  "v_mfma_f32_16x16x32_bf16 v[140:143], v[76:79], v[180:183], v[140:143]\n\t" \
  "buffer_load_dwordx4 v[180:183], v238, %[rt], %[st] offen offset:1024 sc1\n\t" \
  "s_waitcnt vmcnt(7)\n\t" \
  "v_mfma_f32_16x16x32_bf16 v[128:131], v[80:83], v[184:187], v[128:131]\n\t" \
  "v_mfma_f32_16x16x32_bf16 v[136:139], v[88:91], v[184:187], v[136:139]\n\t" \
  "buffer_load_dwordx4 v[184:187], v238, %[rt], %[st4] offen offset:0 sc1\n\t" \
  "s_waitcnt vmcnt(7)\n\t" \
  "v_mfma_f32_16x16x32_bf16 v[132:135], v[84:87], v[188:191], v[132:135]\n\t" \
  "v_mfma_f32_16x16x32_bf16 v[140:143], v[92:95], v[188:191], v[140:143]\n\t" \
  "buffer_load_dwordx4 v[188:191], v238, %[rt], %[st4] offen offset:1024 sc1\n\t" \
  "s_waitcnt vmcnt(7)\n\t" \
  "v_mfma_f32_16x16x32_bf16 v[128:131], v[96:99], v[192:195], v[128:131]\n\t" \
  "v_mfma_f32_16x16x32_bf16 v[136:139], v[104:107], v[192:195], v[136:139]\n\t" \
  "buffer_load_dwordx4 v[192:195], v239, %[rt], %[st] offen offset:0 sc1\n\t" \
  "s_waitcnt vmcnt(7)\n\t" \
  "v_mfma_f32_16x16x32_bf16 v[132:135], v[100:103], v[196:199], v[132:135]\n\t" \
  "v_mfma_f32_16x16x32_bf16 v[140:143], v[108:111], v[196:199], v[140:143]\n\t" \
  "buffer_load_dwordx4 v[196:199], v239, %[rt], %[st] offen offset:1024 sc1\n\t" \
  "s_waitcnt vmcnt(7)\n\t" \
  "v_mfma_f32_16x16x32_bf16 v[128:131], v[112:115], v[200:203], v[128:131]\n\t" \
  "v_mfma_f32_16x16x32_bf16 v[136:139], v[120:123], v[200:203], v[136:139]\n\t" \
  "buffer_load_dwordx4 v[200:203], v239, %[rt], %[st4] offen offset:0 sc1\n\t" \
  "s_waitcnt vmcnt(7)\n\t" \
  "v_mfma_f32_16x16x32_bf16 v[132:135], v[116:119], v[204:207], v[132:135]\n\t" \
  "v_mfma_f32_16x16x32_bf16 v[140:143], v[124:127], v[204:207], v[140:143]\n\t" \
  "buffer_load_dwordx4 v[204:207], v239, %[rt], %[st4] offen offset:1024 sc1\n\t" \
  "s_nop 5\n\t" \
  "v_pk_add_f32 v[128:129], v[128:129], v[132:133]\n\t" \
  "v_pk_add_f32 v[130:131], v[130:131], v[134:135]\n\t" \
  "ds_write_b128 v230, v[128:131] offset:0\n\t" \
  "v_pk_add_f32 v[136:137], v[136:137], v[140:141]\n\t" \
  "v_pk_add_f32 v[138:139], v[138:139], v[142:143]\n\t" \
  "ds_write_b128 v230, v[136:139] offset:1024\n\t" \
  "v_add_u32 v230, 16384, v230\n\t" \
  "s_branch .Lt%=\n\t" \
  ".Ltl%=:\n\t" \
  "buffer_load_dwordx4 v[224:227], v243, %[rg], 0 offen sc1\n\t" \
  "s_waitcnt vmcnt(7)\n\t" \
  "v_mfma_f32_16x16x32_bf16 v[132:135], v[68:71], v[180:183], v[132:135]\n\t" \
  "v_mfma_f32_16x16x32_bf16 v[140:143], v[76:79], v[180:183], v[140:143]\n\t" \
  "s_waitcnt vmcnt(6)\n\t" \
  "v_mfma_f32_16x16x32_bf16 v[128:131], v[80:83], v[184:187], v[128:131]\n\t" \
  "v_mfma_f32_16x16x32_bf16 v[136:139], v[88:91], v[184:187], v[136:139]\n\t" \
  "s_waitcnt vmcnt(5)\n\t" \
  "v_mfma_f32_16x16x32_bf16 v[132:135], v[84:87], v[188:191], v[132:135]\n\t" \
  "v_mfma_f32_16x16x32_bf16 v[140:143], v[92:95], v[188:191], v[140:143]\n\t" \
  "s_waitcnt vmcnt(4)\n\t" \
  "v_mfma_f32_16x16x32_bf16 v[128:131], v[96:99], v[192:195], v[128:131]\n\t" \
  "v_mfma_f32_16x16x32_bf16 v[136:139], v[104:107], v[192:195], v[136:139]\n\t" \
  "s_waitcnt vmcnt(3)\n\t" \
  "v_mfma_f32_16x16x32_bf16 v[132:135], v[100:103], v[196:199], v[132:135]\n\t" \
  "v_mfma_f32_16x16x32_bf16 v[140:143], v[108:111], v[196:199], v[140:143]\n\t" \
  "s_waitcnt vmcnt(2)\n\t" \
  "v_mfma_f32_16x16x32_bf16 v[128:131], v[112:115], v[200:203], v[128:131]\n\t" \
  "v_mfma_f32_16x16x32_bf16 v[136:139], v[120:123], v[200:203], v[136:139]\n\t" \
  "s_waitcnt vmcnt(1)\n\t" \
  "v_mfma_f32_16x16x32_bf16 v[132:135], v[116:119], v[204:207], v[132:135]\n\t" \
  "v_mfma_f32_16x16x32_bf16 v[140:143], v[124:127], v[204:207], v[140:143]\n\t" \
  "s_nop 6\n\t" \
  "v_pk_add_f32 v[128:129], v[128:129], v[132:133]\n\t" \
  "v_pk_add_f32 v[130:131], v[130:131], v[134:135]\n\t" \
  "ds_write_b128 v230, v[128:131] offset:0\n\t" \
  "v_pk_add_f32 v[136:137], v[136:137], v[140:141]\n\t" \
  "v_pk_add_f32 v[138:139], v[138:139], v[142:143]\n\t" \
  "ds_write_b128 v230, v[136:139] offset:1024\n\t" \
  "s_waitcnt vmcnt(0) lgkmcnt(0)\n\t" \
  "s_branch .Lend%=\n\t" \
  ".Ltw%=:\n\t" \
  "s_add_u32 s92, s92, 1\n\t" \
  "s_sleep 2\n\t" \
  "s_and_b32 s93, s92, 63\n\t" \
  "s_cbranch_scc1 .Ltr%=\n\t" \
  "buffer_load_dword v164, off, %[rs], %[sab] sc1\n\t" \
  "s_waitcnt vmcnt(0)\n\t" \
  "v_readfirstlane_b32 s93, v164\n\t" \
  "s_nop 3\n\t" \
  "s_cmp_eq_u32 s93, 1\n\t" \
  "s_cbranch_scc1 .Lto%=\n\t" \
  "s_cmp_lt_u32 s92, 0x80000\n\t" \
  "s_cbranch_scc1 .Ltr%=\n\t" \
  "v_mov_b32 v164, 1\n\t" \
  "buffer_store_dword v164, off, %[rs], %[sab] sc1\n\t" \
  "s_waitcnt vmcnt(0)\n\t" \
  "s_branch .Lto%=\n\t" \
  ".Ltr%=:\n\t" \
  "buffer_load_dword v166, v242, %[rs], 0 offen sc1\n\t" \
  "s_waitcnt vmcnt(0)\n\t" \
  "s_branch .Ltc%=\n\t" \
  ".Llast%=:\n\t" \
  "v_mov_b32 v173, v242\n\t" \
  "s_branch .Lgol%=\n\t" \
  ".Lfw%=:\n\t" \
  "s_add_u32 s92, s92, 1\n\t" \
  "s_sleep 2\n\t" \
  "s_and_b32 s93, s92, 63\n\t" \
  "s_cbranch_scc1 .Lfr%=\n\t" \
  "buffer_load_dword v164, off, %[rs], %[sab] sc1\n\t" \
  "s_waitcnt vmcnt(0)\n\t" \
  "v_readfirstlane_b32 s93, v164\n\t" \
  "s_nop 3\n\t" \
  "s_cmp_eq_u32 s93, 1\n\t" \
  "s_cbranch_scc1 .Lfo%=\n\t" \
  "s_cmp_lt_u32 s92, 0x80000\n\t" \
  "s_cbranch_scc1 .Lfr%=\n\t" \
  "v_mov_b32 v164, 1\n\t" \
  "buffer_store_dword v164, off, %[rs], %[sab] sc1\n\t" \
  "s_waitcnt vmcnt(0)\n\t" \
  "s_branch .Lfo%=\n\t" \
  ".Lfr%=:\n\t" \
  "buffer_load_dwordx2 v[166:167], v173, %[rs], 0 offen sc1\n\t" \
  "s_branch .Lfc%=\n\t" \
  ".Lb1%=:\n\t" \
  "s_waitcnt vmcnt(0) lgkmcnt(0)\n\t" \
  "s_barrier\n\t" \
  "s_branch .Lb2%=\n\t" \
  ".Lbl%=:\n\t" \
  "s_bitset1_b64 %[ps], %[step]\n\t" \
  "s_cmp_lt_u32 s92, 3\n\t" \
  "s_cbranch_scc1 .Lbm%=\n\t" \
  "s_sleep 12\n\t" \
  ".Lbm%=:\n\t" \
  "s_add_u32 s92, s92, 1\n\t" \
  "s_sleep 2\n\t" \
  "s_and_b32 s93, s92, 63\n\t" \
  "s_cbranch_scc1 .Lba%=\n\t" \
  "buffer_load_dword v164, off, %[rs], %[sab] sc1\n\t" \
  "s_waitcnt vmcnt(0)\n\t" \
  "v_readfirstlane_b32 s93, v164\n\t" \
  "s_nop 3\n\t" \
  "s_cmp_eq_u32 s93, 1\n\t" \
  "s_cbranch_scc1 .Lgo%=\n\t" \
  "s_cmp_lt_u32 s92, 0x80000\n\t" \
  "s_cbranch_scc1 .Lba%=\n\t" \
  "v_mov_b32 v164, 1\n\t" \
  "buffer_store_dword v164, off, %[rs], %[sab] sc1\n\t" \
  "s_waitcnt vmcnt(0)\n\t" \
  "s_branch .Lgo%=\n\t" \
  ".Lba%=:\n\t" \
  "buffer_load_dwordx2 v[166:167], v173, %[rs], 0 offen sc1\n\t" \
  "s_branch .Lcal%=\n\t" \
  ".Lslow%=:\n\t" \
  "s_mov_b64 s[94:95], 0\n\t" \
  "v_cmp_eq_u32 vcc, %[sent], v128\n\t" \
  "s_or_b64 s[94:95], s[94:95], vcc\n\t" \
  "v_cmp_eq_u32 vcc, %[sent], v129\n\t" \
  "s_or_b64 s[94:95], s[94:95], vcc\n\t" \
  "v_cmp_eq_u32 vcc, %[sent], v130\n\t" \
  "s_or_b64 s[94:95], s[94:95], vcc\n\t" \
  "v_cmp_eq_u32 vcc, %[sent], v131\n\t" \
  "s_or_b64 s[94:95], s[94:95], vcc\n\t" \
  "v_cmp_eq_u32 vcc, %[sent], v132\n\t" \
  "s_or_b64 s[94:95], s[94:95], vcc\n\t" \
  "v_cmp_eq_u32 vcc, %[sent], v133\n\t" \
  "s_or_b64 s[94:95], s[94:95], vcc\n\t" \
  "v_cmp_eq_u32 vcc, %[sent], v134\n\t" \
  "s_or_b64 s[94:95], s[94:95], vcc\n\t" \
  "v_cmp_eq_u32 vcc, %[sent], v135\n\t" \
  "s_or_b64 s[94:95], s[94:95], vcc\n\t" \
  "v_cmp_eq_u32 vcc, %[sent], v136\n\t" \
  "s_or_b64 s[94:95], s[94:95], vcc\n\t" \
  "v_cmp_eq_u32 vcc, %[sent], v137\n\t" \
  "s_or_b64 s[94:95], s[94:95], vcc\n\t" \
  "v_cmp_eq_u32 vcc, %[sent], v138\n\t" \
  "s_or_b64 s[94:95], s[94:95], vcc\n\t" \
  "v_cmp_eq_u32 vcc, %[sent], v139\n\t" \
  "s_or_b64 s[94:95], s[94:95], vcc\n\t" \
  "v_cmp_eq_u32 vcc, %[sent], v140\n\t" \
  "s_or_b64 s[94:95], s[94:95], vcc\n\t" \
  "v_cmp_eq_u32 vcc, %[sent], v141\n\t" \
  "s_or_b64 s[94:95], s[94:95], vcc\n\t" \
  "v_cmp_eq_u32 vcc, %[sent], v142\n\t" \
  "s_or_b64 s[94:95], s[94:95], vcc\n\t" \
  "v_cmp_eq_u32 vcc, %[sent], v143\n\t" \
  "s_or_b64 s[94:95], s[94:95], vcc\n\t" \
  "s_cbranch_scc0 .Lok%=\n\t" \
  "s_bitset1_b64 %[fm], %[step]\n\t" \
  "s_add_u32 s92, s92, 1\n\t" \
  "s_sleep 1\n\t" \
  "s_and_b32 s93, s92, 63\n\t" \
  "s_cbranch_scc1 .Lre%=\n\t" \
  "buffer_load_dword v164, off, %[rs], %[sab] sc1\n\t" \
  "s_waitcnt vmcnt(0)\n\t" \
  "v_readfirstlane_b32 s93, v164\n\t" \
  "s_nop 3\n\t" \
  "s_cmp_eq_u32 s93, 1\n\t" \
  "s_cbranch_scc1 .Lok%=\n\t" \
  "s_cmp_lt_u32 s92, 0x80000\n\t" \
  "s_cbranch_scc1 .Lre%=\n\t" \
  "v_mov_b32 v164, 1\n\t" \
  "buffer_store_dword v164, off, %[rs], %[sab] sc1\n\t" \
  "s_waitcnt vmcnt(0)\n\t" \
  "s_branch .Lok%=\n\t" \
  ".Lre%=:\n\t" \
  "buffer_load_dwordx4 v[128:131], v234, %[rs], %[so] offen sc1\n\t" \
  "s_nop 15\n\t" \
  "buffer_load_dwordx4 v[132:135], v235, %[rs], %[so] offen sc1\n\t" \
  "s_nop 15\n\t" \
  "buffer_load_dwordx4 v[136:139], v236, %[rs], %[so] offen sc1\n\t" \
  "s_nop 15\n\t" \
  "buffer_load_dwordx4 v[140:143], v237, %[rs], %[so] offen sc1\n\t" \
  "v_mov_b64 v[144:145], 0\n\t" \
  "v_mov_b64 v[146:147], 0\n\t" \
  "v_mov_b64 v[148:149], 0\n\t" \
  "v_mov_b64 v[150:151], 0\n\t" \
  "v_mov_b64 v[152:153], 0\n\t" \
  "v_mov_b64 v[154:155], 0\n\t" \
  "v_mov_b64 v[156:157], 0\n\t" \
  "v_mov_b64 v[158:159], 0\n\t" \
  "s_waitcnt vmcnt(0)\n\t" \
  "s_branch .Lw%=\n\t" \
  ".Lend%=:\n\t"


__device__ __forceinline__ int look_prog(__amdgpu_buffer_rsrc_t rs, int off) {
  asm volatile("" ::: "memory");
  const int v = (int)ld_flag(rs, off);
  return min(__builtin_amdgcn_readlane(v, 0), __builtin_amdgcn_readlane(v, 1));
}
__device__ __forceinline__ int wait_prog(__amdgpu_buffer_rsrc_t rs, int off, int need, gu32* abortw) {
  int v;
  for (unsigned spins = 0;;) {
    v = look_prog(rs, off);
    if (v >= need) break;
    __builtin_amdgcn_s_sleep(4);
    if (spin_fail(spins, abortw)) break;
  }
  asm volatile("" ::: "memory");
  return v;
}
#define CLB_S "s90", "s91", "s92", "s93", "s94", "s95", "vcc", "scc", "memory"
#define W_IN "{v[0:31]}"(W[0]), "{v[32:63]}"(W[1]), "{v[64:95]}"(W[2]), "{v[96:127]}"(W[3]), "{v[128:159]}"(W[4]), "{v[160:191]}"(W[5])

#define LEAD0 408
#define LEAD_N0 5
#define LEAD_SH 2
#define LEAD_X0 8
#define LEAD_U0 24
#define LEAD_D0 0
#define T0_BACK 1000
#define QBJ 3
constexpr int L_MEM = 0, L_KT = 16384, L_QX = 24576, L_KX = 26624, L_VX = 16384, L_MISC = 30720, L_WL = 32768;

#define QK_STEP(TXT)                                                                                                        \
  asm volatile(TXT                                                                                                          \
      : [tout] "=&s"(tout), [spins] "=&s"(spins), [tb1] "=&s"(tb1), [tgt] "=&s"(tgt), "+{v248}"(aL16), "+{v249}"(aMW), "+{v250}"(aWL),                            \
        "+{v251}"(aIDX), "+{v252}"(aQW), "+{v[232:235]}"(QF)                                                                 \
      : W_IN, [rs] "s"(rs4), [som] "s"(o_mem(pm, s) + WV * 1024), [som2] "s"(o_mem(pm, s) + WV * 1024 + 16384), [sent] "s"(DSENT), [sab] "s"(SY_ABORT), \
        [lead] "s"(lead), [step] "s"(s), [sbc] "s"(SY_BA + side(pm, 1) + h * 128), [nh] "s"(0xFF00FF00FF00FF00ull), [up] "s"(0xFFFFFFFF00000000ull), [negc] "s"(-cexp),      \
        [c] "s"(cexp), [so] "s"(o_o(pm, s, h) + WV * 512), [sk] "s"(o_v(pm, s, h) + 2048 + (WV & 3) * 512),                    \
        [sv] "s"(o_v(pm, s, h) + WV * 512), [rt] "s"(rt4), [svt] "s"(t_off(p, T_V + kseg(s, h)) + WV * 2048)                  \
      : CLB8(192, 193, 194, 195, 196, 197, 198, 199), CLB8(200, 201, 202, 203, 204, 205, 206, 207),                          \
        CLB8(208, 209, 210, 211, 212, 213, 214, 215), CLB8(216, 217, 218, 219, 220, 221, 222, 223),                          \
        CLB8(224, 225, 226, 227, 228, 229, 230, 231), CLB8(236, 237, 238, 239, 240, 241, 242, 243), "v244", "v245", "v246", "v247", "v253", "s88", "s89", CLB_S)

template <int WV>
__device__ __forceinline__ void qk_wave(Args& p, int h, char* smem) {
  const int tid = threadIdx.x;
  unsigned pm = 0;
  int have = -1;
  u32x32 W[6];
  {
    int lane = tid & 63;
    asm volatile("" : "+v"(lane));
    const unsigned vo = (WV * 4 + (lane >> 4)) * (HD * 2) + 16 * ((lane & 15) ^ swzB((WV * 4 + (lane >> 4)) & 15));
    auto stage = [&](int j) {
#pragma unroll
      for (int mm = 0; mm < 2; ++mm) {
        const char* src = (const char*)((mm ? p.Wk : p.Wq) + (size_t)j * 128 * HD + h * DH);
        const unsigned dst = lds_off(smem) + (~j & 1) * 65536 + mm * 32768 + WV * 1024;
        glds2(src, 32 * HD * 2, vo, dst);
        if (TH_Q < 63) HOLD_V((h & 3) ^ 1, TH_Q);
        glds2(src + 64 * HD * 2, 32 * HD * 2, vo, dst + 16384);
        if (TH_Q < 63) HOLD_V((h & 3) ^ 1, TH_Q);
      }
    };
    bf16x8 keep[4][4];
    int f0 = -1;
    auto place = [&](int g) {
#pragma unroll
      for (int i = 0; i < 4; ++i)
        *reinterpret_cast<bf16x8*>(smem + (g == 0 ? 65536 : g == 1 ? 32768 : g == 2 ? 131072 : 98304) + WV * 4096 + i * 1024 + lane * 16) = keep[g][i];
    };
    stage(0); stage(1);
    Boot bt;
#pragma unroll
    for (int j = 0; j < 8; ++j) {
      if (j < 7) WAIT_V(8); else WAIT_V(0);
      if (WV == 7 && j == QBJ + 1) pm = boot_done(p.ws, bt);
      if ((WV == 4 || WV == 5) && j == 7) {
        asm volatile("" : "+v"(f0));
        have = min(__builtin_amdgcn_readlane(f0, 0), __builtin_amdgcn_readlane(f0, 1));
      }
      LBAR();
#pragma unroll
      for (int k = 0; k < 8; ++k) {
        const int P = (QK_MAP[WV] >> (4 * (2 * j + (k >> 2)))) & 15;
        const bf16x8 fr = ld_bfrag(smem + (~j & 1) * 65536 + (WV >> 2) * 32768 + (k >> 2) * 16384, k & 1, 2 * (WV & 3) + ((k >> 1) & 1), lane);
        const int f = P * 4 + (k & 3);
        if (P < 12) put4(W[f >> 3], f & 7, fr);
        else keep[P - 12][k & 3] = fr;
      }
      if (j == (WV >> 1)) place(2);
      LBAR();
      if (WV == 7 && j == QBJ) boot_ask(p.ws, bt);
      if (j + 2 < 8) stage(j + 2);
      if (j == 6) {
        if (WV == 4 || WV == 5)
          asm volatile("buffer_load_dword %0, %1, %2, 0 offen sc1" : "=&v"(f0) : "v"(fkq(h, (lane & 1) * 2)), "s"(raw_rsrc(p.ws + WS_SY, SY_TOTAL)) : "memory");
        place(0); place(3);
      }
    }
    place(1);
    if (WV == 7 && lane < 8) lds_at(smem + L_MISC)[lane] = lane == 7 ? pm : (lane == 1 ? now_q() - T0_BACK : 0);
    if (WV == 6) lds_at(smem + 28672)[lane] = ~0u;
    LBAR();
    pm = uni(lds_at(smem + L_MISC)[7]);
    p.s0 = pm >> 31;
    if (WV == 4 || WV == 5) {
      if (have < 0) have = wait_prog(mkrsrc(p.ws + WS_SY, SY_TOTAL), fkq(h, (lane & 1) * 2), 0, (gu32*)(p.ws + WS_SY + SY_ABORT));
      const char* src = p.ws + WS_T + t_off(p, kseg(0, h)) + (WV - 4) * 4096 + lane * 16;
#pragma unroll
      for (int i = 0; i < 4; ++i) glds16_sc1(src + i * 1024, smem + L_KT + (WV - 4) * 4096 + i * 1024);
    }
  }
  const __amdgpu_buffer_rsrc_t rsy = mkrsrc(p.ws + WS_SY, SY_TOTAL);
  const u32x4 rs4 = raw_rsrc(p.ws + WS_SY, SY_TOTAL);
  const u32x4 rt4 = raw_rsrc(p.ws + WS_T, T_BYTES);
  gu32* abortw = (gu32*)(p.ws + WS_SY + SY_ABORT);
  const float cexp = 0.08838834764831845f * 1.4426950408889634f;
  constexpr int m = WV >> 2, b = WV & 3;
  int lead = LEAD0;
  unsigned tb1 = 0;
  unsigned long long fm = 0, ps = 0; (void)fm; (void)ps;
  for (int s = 0; s < NSEG; ++s) {
    int lane = tid & 63;
    asm volatile("" : "+v"(lane));
    const int r = lane & 15, q = lane >> 4;
    unsigned aL16 = lane * 16, aMW = aL16 + WV * 1024, aWL = aL16 + 98304 + WV * 4096;
    unsigned aIDX = (lane & 2) ? 0xEEEE : 0x4444;
    unsigned aQW = (32 * b + r) * 16 + q * 4 + (m ? L_KX : L_QX);
    u32x4 QF;
    asm volatile("" : "={v[232:235]}"(QF));
    if (WV < 4) {
      QF[0] = L_QX + (q * 8 + (r >> 2)) * 16 + (r & 1) * 8;
      QF[1] = ((2 * (q & 1) + (q >> 1)) * 16 + r) * 4;
      QF[2] = ((r >> 2) & 1) * 65536 + ((r >> 1) & 1) * 64 + (r >> 3) * 32 + (r & 1) * 16 + (q >> 1) * 256 + (q & 1) * 8;
    } else QF[3] = r * 16 + q * 4;
    unsigned tout, spins, tgt;
    if (WV == 0) QK_STEP(QK_ASM_0); else if (WV == 1) QK_STEP(QK_ASM_1); else if (WV == 2) QK_STEP(QK_ASM_2);
    else if (WV == 3) QK_STEP(QK_ASM_3); else if (WV == 4) QK_STEP(QK_ASM_4); else if (WV == 5) QK_STEP(QK_ASM_5);
    else if (WV == 6) QK_STEP(QK_ASM_6); else QK_STEP(QK_ASM_7);
    tout = __builtin_amdgcn_readfirstlane(tout);
    tb1 = __builtin_amdgcn_readfirstlane(tb1);
    lane = tid & 63;
    asm volatile("" : "+v"(lane));
    if (WV < 4) {
      lds_at(smem + L_MISC + 8)[WV] = s + 1;
      if (WV == 3) { lds_at(smem + L_MISC)[6] = time_of(tout); lds_at(smem + 28672)[2] = s; }
    }
    if (WV == 6) {
      if (s + 1 < NSEG) watch(rsy, s, lane, now_q(), (const unsigned*)(smem + L_MISC), abortw, side(pm, 1));
    } else if (WV >= 4 && s + 1 < NSEG) {
      unsigned t;
      do {
        __builtin_amdgcn_s_sleep(1);
        t = lds_at(smem + L_MISC + 8)[lane & 3];
      } while (!__all(t == (unsigned)(s + 1)));
      if (WV <= 5) {
        if (s + 2 < NSEG) {
          if (have < s + 1) have = wait_prog(rsy, fkq(h, (lane & 1) * 2), s + 1, abortw);
          const char* src = p.ws + WS_T + t_off(p, kseg(s + 1, h)) + (WV - 4) * 4096 + lane * 16;
#pragma unroll
          for (int i = 0; i < 4; ++i) glds16_sc1(src + i * 1024, smem + L_KT + (WV - 4) * 4096 + i * 1024);
          if (have < s + 2) have = look_prog(rsy, fkq(h, (lane & 1) * 2));
        }
      } else {
        do __builtin_amdgcn_s_sleep(2); while (__builtin_amdgcn_readfirstlane(lds_at(smem + 28672)[2]) != (unsigned)s);
        const int x = (int)(__builtin_amdgcn_readfirstlane(lds_at(smem + L_MISC)[6]) - tb1);
        lead = __builtin_amdgcn_readfirstlane(track(lead, x + (s < LEAD_N0 ? LEAD_X0 : 0), s < LEAD_N0 ? LEAD_U0 : LEAD_UP, s < LEAD_N0 ? LEAD_D0 : (s < 3 * LEAD_N0 ? min(max(LEAD_DN, (lead - x) >> LEAD_SH), 16) : LEAD_DN), 60));
      }
    }
  }
}

__device__ __forceinline__ void qk_role(Args& p, int h, char* smem) {
  switch (__builtin_amdgcn_readfirstlane(threadIdx.x >> 6)) {
    case 0: qk_wave<0>(p, h, smem); break;
    case 1: qk_wave<1>(p, h, smem); break;
    case 2: qk_wave<2>(p, h, smem); break;
    case 3: qk_wave<3>(p, h, smem); break;
    case 4: qk_wave<4>(p, h, smem); break;
    case 5: qk_wave<5>(p, h, smem); break;
    case 6: qk_wave<6>(p, h, smem); break;
    default: qk_wave<7>(p, h, smem); break;
  }
}

#define K_DUE 160
#define T_SL 2
#define O_DUE 400

constexpr int V_VM = 16384, V_T = 32768, V_KI = 131072;
__device__ __forceinline__ void vu_role(Args& p, int h, char* smem) {
  const int tid = threadIdx.x, lane = tid & 63;
  const int w = __builtin_amdgcn_readfirstlane(tid >> 6);
  u32x32 W[4];
  {
    const unsigned vo = (tid >> 4) * (HD * 2) + 16 * ((lane & 15) ^ swzB((tid >> 4) & 15));
    auto stage = [&](int j) {
      const char* src = (const char*)(p.Wv + (size_t)j * 128 * HD + h * DH);
      const unsigned dst = lds_off(smem) + L_WL + (j & 3) * 32768 + w * 1024;
      glds2(src, 32 * HD * 2, vo, dst);
      glds2(src + 64 * HD * 2, 32 * HD * 2, vo, dst + 16384);
    };
    start_sy(p.ws, p.mem0, h, smem);
    Boot bt;
#pragma unroll
    for (int u = 0; u < 2; ++u) {
#pragma unroll
      for (int j = 0; j < 4; ++j) stage(4 * u + j);
      if (u && w == 7) boot_ask(p.ws, bt);
      WAIT_V(0);
      if (u && w == 7) {
        const unsigned m = boot_done(p.ws, bt);
        if (lane < 8) lds_at(smem + L_MISC)[lane] = lane == 7 ? m : (lane == 1 ? now_q() : 0);
      }
      LBAR();
#pragma unroll
      for (int k = 0; k < 16; ++k) put4(W[2 * u + (k >> 3)], k & 7, ld_bfrag(smem + L_WL + ((k >> 1) ^ w) * 16384, k & 1, w, lane));
      LBAR();
    }
  }
  const __amdgpu_buffer_rsrc_t rsy = mkrsrc(p.ws + WS_SY, SY_TOTAL);
  const u32x4 rs4 = raw_rsrc(p.ws + WS_SY, SY_TOTAL);
  gu32* abortw = (gu32*)(p.ws + WS_SY + SY_ABORT);
  const unsigned pm = uni(lds_at(smem + L_MISC)[7]);
  p.s0 = pm >> 31;
  unsigned long long fm = 0; (void)fm;
  int have = -1;
  auto fetch = [&](int sg) {
    if (have < sg) have = wait_prog(rsy, fkq(h, w - 4), sg, abortw);
    const char* src = p.ws + WS_T + t_off(p, (w - 4) * T_Q + kseg(sg, h)) + lane * 16;
#pragma unroll
    for (int i = 0; i < 8; ++i) glds16_sc1(src + i * 1024, smem + V_T + (sg & 3) * 24576 + (w - 4) * 8192 + i * 1024);
  };
  const int fo = SY_FO + ((w & 3) * NH + h) * 128;
  typedef __attribute__((ext_vector_type(8))) float f32x8;
  f32x8 bias;
  typedef __attribute__((ext_vector_type(8))) unsigned u32x8;
  u32x8 ca;
#pragma unroll
  for (int i = 0; i < 8; ++i) ca[i] = lane * 16 + (i ^ w) * 1024;
  u32x4 ta;
  {
    const int r = lane & 15, q = lane >> 4;
#pragma unroll
    for (int j = 0; j < 8; ++j) bias[j] = (j >> 2) * 16 + 4 * q + (j & 3) <= 16 * (w & 1) + r ? INFINITY : -INFINITY;
    ta[0] = V_KI + w * 2048 + lane * 16;
    ta[1] = V_KI + w * 2048 + (q * 8 + (r >> 2)) * 16 + (r & 1) * 8;
    ta[2] = V_VM + (w & 2) * 512 + ((2 * (q & 1) + (q >> 1)) * 16 + r) * 4;
    ta[3] = 0;
  }
#pragma unroll 1
  for (int s = 0; s < NSEG; ++s) {
    unsigned spins, tgt;
    asm volatile(VV_ASM
        : [spins] "=&s"(spins), [tgt] "=&s"(tgt)
        : W4_IN, "{v200}"(lane * 16), "{v[232:239]}"(ca), "{v202}"((lane & 2) ? 0xEEEE : 0x4444), "{v203}"(lane * 4),
          "{v204}"(tid * 4 + V_VM + (s & 1) * 2048), [rs] "s"(rs4), [som] "s"(o_mem(pm, s) + w * 1024), [som2] "s"(o_mem(pm, s) + w * 1024 + 16384), [step] "s"(s),
          [sent] "s"(DSENT), [sab] "s"(SY_ABORT), [sv] "s"(o_v(pm, s, h) + w * 256)
        : CLB32(128), CLB32(160), CLB8(192, 193, 194, 195, 196, 197, 198, 199), CLB_S);
    if (w < 4 || s == NSEG - 1) {
      const int sg = s - (w < 4);
      if (w >= 4) pace_until(now_q() + K_DUE);
      if (sg >= 0) {
        asm volatile(TK_ASM
            :
            : "{v200}"(lane * 16), "{v[208:215]}"(bias), "{v[216:219]}"(ta),
              [rs] "s"(rs4), [rt] "s"(raw_rsrc(p.ws + WS_T, T_BYTES)), [sent] "s"(DSENT), [sab] "s"(SY_ABORT), [sk] "s"(o_v(pm, sg, h) + 2048),
              [tl] "s"(V_T + (sg & 3) * 24576), [vp] "s"((sg & 1) * 2048), [tq] "s"((w & 1) * 4096), [tv] "s"((w & 2) * 2048),
              [so] "s"(t_off_o(p, T_O + (sg * 2 + (w & 1)) * 16 + (h >> 1)) + (h & 1) * 4096 + (w & 2) * 1024),
              [up] "s"(0xFFFFFFFF00000000ull), [c] "s"(1.4426950408889634f), [negc] "s"(-1.4426950408889634f),
              [sc] "s"((unsigned long long)__float_as_uint(0.08838834764831845f)), [od] "s"(s < NSEG - 1 ? O_DUE : 0)
            : CLB32(128), CLB32(160), CLB8(192, 193, 194, 195, 196, 197, 198, 199), CLB8(224, 225, 226, 227, 228, 229, 230, 231),
              "v205", "v206", "v207", "s88", "s89", CLB_S);
        if (sg >= NSEG - 3) {
          static_assert(TLAG == 2, "");
          WAIT_V(0);
          if (lane == 0) __builtin_amdgcn_raw_buffer_store_b32(1u, rsy, fo + (sg - NSEG + 3) * 4 * NH * 128, 0, 16);
        }
      }
    } else if (w == 7) {
      watch(rsy, s, lane, now_q(), (const unsigned*)(smem + L_MISC), abortw, side(pm, 1));
    } else if (w >= 4) {
      __builtin_amdgcn_s_sleep(T_SL);
      const int s1 = w == 5 ? (s == NSEG - 2 ? s + 1 : s) : s + 1;
#pragma unroll 1
      for (int sg = (w == 5 || s == 0) ? s : s + 1; sg <= s1; ++sg) fetch(sg);
      if (have < s1 + 1 && s1 + 1 < NSEG) have = look_prog(rsy, fkq(h, w - 4));
      if (w == 5 && s + NPRE < NSEG) {
#pragma unroll
        for (int i = 0; i < 4; ++i) st_sc1(rsy, o_v(pm, s + NPRE, h) + i * 1024 + lane * 16, (u32x4){DSENT, DSENT, DSENT, DSENT});
      }
    }
  }
}

#define RL_UP 6
#define RL_DN 4
#define RED_MIN 40
#define BA_LOOK 360
constexpr int R_SI = 32768, R_PART = 98304;

__device__ __forceinline__ void red_role(Args& p, int j, char* smem) {
  const int tid = threadIdx.x;
  int lane = tid & 63;
  const int w = __builtin_amdgcn_readfirstlane(tid >> 6);
  const int c = (j & 2) * 8 + (j >> 2), rh = j & 1;
  const int hg = (w + (j >> 2)) & 7, rot = (j >> 2) + (j >> 5);
  const __amdgpu_buffer_rsrc_t rsy = mkrsrc(p.ws + WS_SY, SY_TOTAL);
  const u32x4 rs4 = raw_rsrc(p.ws + WS_SY, SY_TOTAL);
  const __amdgpu_buffer_rsrc_t rt = mkrsrc(p.ws + WS_T, T_BYTES);
  gu32* abortw = (gu32*)(p.ws + WS_SY + SY_ABORT);
  u32x32 W[4];
  u32x2 tag = {0u, 0u};
  {
    const u16* Wo = p.Wo + (size_t)(4 * hg * DH) * DM + 32 * c;
    const int ro = (lane >> 2) * 64 + (lane & 3) * 8;
    Boot bt;
    load_blocks<16>(smem + 32768 + w * 16384,
        [&](int i, char* slot) {
          if (i == 12 && w == 0) boot_ask(p.ws, bt);
          if ((i < 2 || i == 4 || i == 8) && (c & 14) != 6 && lane < 10) {
            const int x = i < 2 ? 3 - i : i >> 3, n = min(((c & 15) - ((c & 8) >> 2)) * 10 + lane, 127);
            touch_line((const char*)p.Wo + (size_t)(w * 512 + (x >> 1) * 256 + (x & 1) * 64 + (n & 64) + n) * (DM * 2) + (c & 16) * 64 + 384, smem + w * 256);
          }
          const int x = ((i >> 2) + rot) & 3;
          blk_dma(Wo + (size_t)((x >> 1) * 256 + (x & 1) * 64 + ((i >> 1) & 1) * 128 + (i & 1) * 32) * DM, DM, slot, lane);
        },
        [&](int i, char* slot) {
#pragma unroll
          for (int t = 0; t < 2; ++t) {
            s16x4 lo = __builtin_amdgcn_ds_read_tr16_b64_v4i16((lds_s16x4_ptr)(slot + ro + t * 32));
            s16x4 hi = __builtin_amdgcn_ds_read_tr16_b64_v4i16((lds_s16x4_ptr)(slot + ro + t * 32 + 1024));
            put4(W[i >> 2], (i & 2) * 2 + 2 * t + (i & 1), (bf16x8){lo[0], lo[1], lo[2], lo[3], hi[0], hi[1], hi[2], hi[3]});
          }
        });
    if (w == 0) { lds_at(smem + 4128)[0] = boot_done(p.ws, bt); tag = next_tag(bt.e); }
    LBAR();
  }
  const unsigned pm = uni(lds_at(smem + 4128)[0]);
  p.s0 = pm >> 31;
  const int hs = side(pm, 1);
  {
    const u32x4 ds = {DSENT, DSENT, DSENT, DSENT};
#pragma unroll
    for (int i = 0; i < 2; ++i) {
      const int s = (w * 2 + i) * 4 + (lane >> 4);
      if (s >= 2) st_sc1(rsy, o_mem(pm, s) + (c >> 4) * 16384 + (c & 15) * 512 + rh * 128 + ((lane >> 3) & 1) * 256 + (lane & 7) * 16, ds);
    }
  }
  if (tid < 4) { reinterpret_cast<unsigned*>(smem + 4096)[tid] = tid == 3 ? now_q() : 0; reinterpret_cast<unsigned*>(smem + 4112)[tid] = DSENT; }
  if (tid < 256) reinterpret_cast<int*>(smem + 5120)[tid] = ((tid >> 2) - TLAG - 6) >> 2;
  LBAR();
  const unsigned base = __builtin_amdgcn_readfirstlane(lds_at(smem + 4096)[3]);
  unsigned long long fm = 0, ps = 0;
  const unsigned aL = lane * 16;
  const int e = ((56 + w) * SEG + rh * 16 + (lane >> 2)) * DM + c * 32 + (lane & 3) * 8;
  u32x4 xv = *reinterpret_cast<const u32x4*>(p.x + e), av = {DSENT, DSENT, DSENT, DSENT};
  asm volatile("" : "+v"(xv));
  {
    const int r = lane & 15, q = lane >> 4;
    const int sm0 = o_mem(pm, 0) + (c >> 4) * 16384 + (c & 15) * 512 + rh * 128;
    u32x4 VO, VT;
    unsigned fo = SY_FO + ((rh + (lane & 4) / 2) * NH + 4 * hg + (lane & 3)) * 128;
    unsigned ao = w == 1 ? (c * S_LEN + rh * 16 - (TLAG + 3) * SEG) * 64 + lane * 16
                         : (w >= 4 ? SY_O + NPRE * 131072 + side(pm, w - 2) + (j >> 3) * 16384 + (j & 7) * 1024 + lane * 16
                                   : SY_FS + (2 * c + rh) * 128 + (lane < 8 ? lane * 16 : 0x70000000));
    unsigned ar = w == 1 ? R_SI - (TLAG + 2) * 1024 + lane * 16 : (w == 2 ? 5120 : 4112);
    const unsigned long long am = w == 1 ? ((1ull << 56) - 1) << (TLAG + 3) : (w == 2 ? 0x11111111111111ull << (TLAG + 7) : (w >= 4 ? (0xFFFFull << (16 * (w - 4))) >> NPRE : 0));
    unsigned pw = R_PART + w * 2048 + lane * 16, pr = R_PART + 16384 + tid * 4;
    unsigned sw = R_SI - (TLAG + 1) * 1024 + (lane >> 2) * 64 + w * 8 + (lane & 3) * 2, stm = 8192 + w * 512;
#pragma unroll
    for (int l = 0; l < 4; ++l) {
      const int x = (l + rot) & 3;
      VO[l] = o_o(0, 0, 4 * hg) + rh * 65536 + x * 1024 + aL;
      VT[l] = (T_O + rh * 16 + 2 * hg + (x >> 1)) * 16384 + (x & 1) * 2048 + aL;
    }
    unsigned ba = lane < 4 ? SY_BA + hs + (4 * hg + lane) * 128 : 0x7F000000u;
    unsigned sm = 0;
    int rl = RL0;
    unsigned step, E, so, st, st4, sh;
    asm volatile(RED_ASM
        : [step] "=&s"(step), [E] "=&s"(E), [so] "=&s"(so), [st] "=&s"(st), [st4] "=&s"(st4), [sh] "=&s"(sh),
          [rl] "+s"(rl), [sm] "+s"(sm), [fm] "+s"(fm), [ps] "+s"(ps),
          "+{v[224:227]}"(av), "+{v228}"(ao), "+{v229}"(ar), "+{v230}"(pw), "+{v231}"(pr), "+{v232}"(sw), "+{v233}"(stm), "+{v242}"(fo), "+{v173}"(ba)
        : W4_IN, "{v168}"(aL), "{v169}"(w * 512 + lane * 8), "{v170}"(lane * 8), "{v174}"((r & 1) ? 0x03020706u : 0x05040100u),
          "{v171}"(sm0 + 32768 + (q >> 1) * 256 + (q & 1) * 64 + ((r >> 1) & 1) * 32 + (r & 1) * 16 + (r >> 2) * 4),
          "{v172}"((lane & 2) ? 0xEEEE : 0x4444),
          "{v175}"(w == 3 && lane < 32 ? SY_BR + hs + j * 128 + lane * 4 : 0x7F000000u), "{v[234:237]}"(VO), "{v[238:241]}"(VT), "{v243}"(e * 2),
          [rg] "s"(raw_rsrc(p.ws + WS_G, S_LEN * DM * 2)), [fg] "s"(SY_FG + ((14 + (w >> 2)) * 8 + (c >> 2)) * 128),
          [rs] "s"(rs4), [ra] "s"(w == 1 ? raw_rsrc(p.ws + WS_S, S_LEN * DM * 2) : rs4), [rt] "s"(raw_rsrc(p.ws + WS_T, T_BYTES)),
          [ds] "n"(DS_RED), [wm] "n"(REF_W), [gm] "n"(REF_G), [mn] "n"(RED_MIN), [lk] "n"(DS_MEM + BA_LOOK), [up] "n"(RL_UP), [dn] "n"(-RL_DN), [sent] "s"(DSENT), [sab] "s"(SY_ABORT),
          [base] "s"(base), [pm] "s"(pm), [s0] "s"((p.s0 ^ TF_O) << 13), [am] "s"(am), [ainc] "s"(w == 1 ? SEG * 64 : (w >= 4 ? 131072 : 0)),
          [rinc] "s"(w == 1 ? 1024 : (w == 2 ? 16 : 0))
        : CLB32(128), CLB8(160, 161, 162, 163, 164, 165, 166, 167), CLB8(176, 177, 178, 179, 180, 181, 182, 183),
          CLB8(184, 185, 186, 187, 188, 189, 190, 191), CLB8(192, 193, 194, 195, 196, 197, 198, 199), CLB8(200, 201, 202, 203, 204, 205, 206, 207),
          CLB8(208, 209, 210, 211, 212, 213, 214, 215), CLB8(216, 217, 218, 219, 220, 221, 222, 223), CLB_E, "s96", CLB_S);
  }
  LBAR();
  u32x4 sv;
  if (w < 6 - TLAG) sv = *reinterpret_cast<const u32x4*>(smem + R_SI + (56 + w) * 1024 + lane * 16);
  else {
    const f32x4* part = reinterpret_cast<const f32x4*>(smem + (w == 6 - TLAG ? R_PART : (w - 6 + TLAG) * 16384)) +
                        (lane & 2) * 32 + (lane & 1) * 32 + (lane >> 2);
    f32x4 x[8][2];
#pragma unroll
    for (int ww = 0; ww < 8; ++ww) { x[ww][0] = part[ww * 128]; x[ww][1] = part[ww * 128 + 16]; }
#pragma unroll
    for (int u = 0; u < 2; ++u) {
      const f32x4 a = ((x[0][u] + x[1][u]) + (x[2][u] + x[3][u])) + ((x[4][u] + x[5][u]) + (x[6][u] + x[7][u]));
      sv[2 * u] = cvtpk(a[0], a[1]); sv[2 * u + 1] = cvtpk(a[2], a[3]);
    }
  }
  st_out(p, e, combine(sv, av, xv));
  if (j == 0 && tid == 0) st2_sc1(rsy, SY_EP, tag);
}


__global__ __launch_bounds__(512, 2) void main_kernel(Args p) {
  char* smem = g_smem;
  p.ws = ws_align(p.ws);
  const int bx = blockIdx.x & 7, bk = blockIdx.x >> 3;
  const bool isg = (bx ^ XPROBE) & 4;
  const int b = (isg ? NCHAIN : 0) + bk * 4 + (bx & 3);
  if (threadIdx.x < 64) {
    unsigned long long pc; unsigned len, keep;
    asm volatile("s_getpc_b64 %0\n.Lpc%=:\n\ts_mov_b32 %1, .Lcode_end-.Lpc%=" : "=s"(pc), "=s"(len));
    asm volatile("s_mov_b32 %0, m0\n\ts_mov_b32 m0, %3\n\ts_nop 0\n\tbuffer_load_dword %1, %2, 0 offen lds\n\ts_mov_b32 m0, %0"
                 : "=&s"(keep) : "v"((bk * 40 + threadIdx.x) * 128), "s"(raw_rsrc((const void*)pc, len)), "s"((b >> 6) == 1 ? 2048 : 163328) : "memory");
  }
  if (b >= NCHAIN) gemm_role(p, b - NCHAIN, smem);
  else if (b < 32) qk_role(p, (b & 3) * 8 + bk, smem);
  else if (b < 64) vu_role(p, (b & 3) * 8 + bk - 8, smem);
  else red_role(p, b - 64, smem);
}

extern "C" void kernel_launch(void* const* d_in, const int* in_sizes, int n_in,
                              void* d_out, int out_size, void* d_ws, size_t ws_size,
                              hipStream_t stream) {
  Args a;
  a.x = (const u16*)d_in[0]; a.mem0 = (const u16*)d_in[1];
  a.Wq = (const u16*)d_in[2]; a.Wk = (const u16*)d_in[3]; a.Wv = (const u16*)d_in[4]; a.Wo = (const u16*)d_in[5];
  a.wg = (const u16*)d_in[8]; a.wu = (const u16*)d_in[9]; a.wd = (const u16*)d_in[10];
  a.ws = (char*)d_ws; a.out = (u16*)d_out; a.s0 = 0; a.pad1 = 0;
  main_kernel<<<256, 512, 0, stream>>>(a);
}
```

```cpp
#include <hip/hip_runtime.h>
#include <hip/hip_bf16.h>
#include <stdint.h>

constexpr int S_LEN = 2048, DM = 1024, NH = 32, DH = 128, NMEM = 8, SEG = 32;
constexpr int NSEG = S_LEN / SEG;
constexpr int HD = NH * DH;
constexpr int FF = 3584;

typedef unsigned short u16;
typedef __attribute__((ext_vector_type(8))) short bf16x8;
typedef __attribute__((ext_vector_type(4))) short s16x4;
typedef __attribute__((ext_vector_type(4))) float f32x4;
typedef __attribute__((ext_vector_type(4))) unsigned int u32x4;
typedef __attribute__((ext_vector_type(2))) unsigned int u32x2;
typedef __attribute__((address_space(3))) s16x4* lds_s16x4_ptr;
typedef __attribute__((address_space(3))) void* lds_void_ptr;
typedef __attribute__((address_space(1))) unsigned gu32;
typedef __bf16 bf16x2_t __attribute__((ext_vector_type(2)));
typedef float f32x2_t __attribute__((ext_vector_type(2)));

__device__ __forceinline__ unsigned cvtpk(float lo, float hi) {
  bf16x2_t r = __builtin_convertvector((f32x2_t){lo, hi}, bf16x2_t);
  return __builtin_bit_cast(unsigned, r);
}
__device__ __forceinline__ float lo_f(unsigned v) { return __uint_as_float(v << 16); }
__device__ __forceinline__ float hi_f(unsigned v) { return __uint_as_float(v & 0xffff0000u); }
__device__ __forceinline__ float rbf(float f) { return lo_f(cvtpk(f, f)); }
__device__ __forceinline__ bf16x8 as_bf16x8(u32x4 v) { return __builtin_bit_cast(bf16x8, v); }
#define MFMA(a, b, c) __builtin_amdgcn_mfma_f32_16x16x32_bf16((a), (b), (c), 0, 0, 0)

#define KSTR 65
#define XLD 1024
constexpr int PAGE = 2 << 20, NPAGE = 14;
constexpr int T_K = 0, T_Q = 2304, T_V = 4608, T_O = 6912, T_END = T_O + 2048;
constexpr unsigned T_BYTES = T_END * 16384u;
constexpr size_t WS_SY = 0;
constexpr size_t WS_T = (size_t)NPAGE * PAGE;
constexpr size_t WS_XN = WS_T + T_BYTES;
constexpr size_t WS_HF = WS_XN + (size_t)S_LEN * XLD * 2;
constexpr size_t WS_G = WS_HF + (size_t)S_LEN * 16384;
constexpr size_t WS_S = WS_G + (size_t)S_LEN * DM * 2;
constexpr size_t WS_END = WS_S + (size_t)S_LEN * DM * 2;
constexpr unsigned SENT = 0xFFFFFFFFu;
constexpr unsigned DSENT = 0xFFBFFFBFu;
constexpr int SY_MEM = 0;
constexpr int SY_O = 2 * PAGE;
constexpr int SY_V = 6 * PAGE;
constexpr int SY_BR = PAGE, SY_BA = SY_BR + 16384;
constexpr int SY_FKQ = SY_BR + 32768;
constexpr int SY_FG = SY_FKQ + NH * 4 * 128;
constexpr int SY_FO = SY_FG + 16 * 8 * 128;
constexpr int SY_CH = SY_FO + 384 * 128;
constexpr int SY_ABORT = SY_CH + 8 * 128;
constexpr int SY_FS = SY_ABORT + 256;
constexpr int SY_FEND = SY_FS + 64 * 128;
constexpr int SY_EP = SY_FEND, SY_BT = SY_EP + 128;
constexpr int SY_TOTAL = NPAGE * PAGE;
constexpr int PROBE = PAGE - 16384 + 6144;
__device__ __forceinline__ int side(unsigned pm, int page) { return ((pm >> page) & 1u) << 13; }
__device__ __forceinline__ int o_mem(unsigned pm, int s) { return SY_MEM + s * 32768 + side(pm, 0); }
__device__ __forceinline__ int o_o(unsigned pm, int s, int h) {
  return SY_O + s * 131072 + (h >> 3) * 16384 + ((h >> 1) & 3) * 2048 + (h & 1) * 128 + side(pm, 2 + (s >> 4));
}
__device__ __forceinline__ int o_v(unsigned pm, int s, int h) { return SY_V + s * 262144 + (h >> 1) * 16384 + (h & 1) * 4096 + side(pm, 6 + (s >> 3)); }
__device__ __forceinline__ char* ws_align(char* ws) { return ws + ((PAGE - (unsigned)(uintptr_t)ws) & (PAGE - 1)); }
#define NFUSE 12
constexpr int NPRE = 4;
static_assert(WS_END + PAGE <= (size_t)256 << 20 && NH * KSTR <= T_Q && NPAGE % 2 == 0, "");
constexpr int NCHAIN = 128;
__host__ __device__ constexpr int kseg(int sg, int h) { return h * KSTR + sg; }
__host__ __device__ constexpr int fkq(int h, int kind) { return SY_FKQ + (h * 4 + kind) * 128; }

#define TLB_SH 8
#define XPROBE 0
struct Args {
  const u16 *x, *mem0, *Wq, *Wk, *Wv, *Wo, *wg, *wu, *wd;
  char* ws;
  u16* out;
  int s0, pad1;
};
__device__ __forceinline__ int h_off(const Args& p, int rblk) { return rblk * PAGE + (((p.s0 ^ rblk ^ 1) & 1) << 13); }
static_assert(WS_HF % (2 * PAGE) == 0, "");
#define TF_KQV 1
#define TF_O 0
__device__ __forceinline__ int t_off(const Args& p, int t) { return t * 16384 + (((p.s0 ^ (t >> 7) ^ (TF_KQV == 2 ? t : TF_KQV)) & 1) << 13); }
__device__ __forceinline__ int t_off_o(const Args& p, int t) { return t * 16384 + (((p.s0 ^ (t >> 7) ^ (TF_O == 2 ? t : TF_O)) & 1) << 13); }

__device__ __forceinline__ float wave_sum(float v) {
#pragma unroll
  for (int i = 0; i < 4; ++i) {
    const int x = (int)__float_as_uint(v);
    v += __uint_as_float(i == 0 ? __builtin_amdgcn_update_dpp(0, x, 0x128, 0xf, 0xf, false) : i == 1 ? __builtin_amdgcn_update_dpp(0, x, 0x124, 0xf, 0xf, false)
                         : i == 2 ? __builtin_amdgcn_update_dpp(0, x, 0x122, 0xf, 0xf, false) : __builtin_amdgcn_update_dpp(0, x, 0x121, 0xf, 0xf, false));
  }
  auto r = __builtin_amdgcn_permlane16_swap(__float_as_uint(v), __float_as_uint(v), false, false);
  v = __uint_as_float(r[0]) + __uint_as_float(r[1]);
  r = __builtin_amdgcn_permlane32_swap(__float_as_uint(v), __float_as_uint(v), false, false);
  return __uint_as_float(r[0]) + __uint_as_float(r[1]);
}

__shared__ __attribute__((aligned(16))) char g_smem[163840];
__device__ __forceinline__ unsigned lds_off(const void* p) {
  return (unsigned)(uintptr_t)(lds_void_ptr)g_smem + (unsigned)((const char*)p - g_smem);
}
__device__ __forceinline__ __amdgpu_buffer_rsrc_t mkrsrc(const void* p, unsigned bytes) {
  return __builtin_amdgcn_make_buffer_rsrc((void*)p, 0, bytes, 0x00020000);
}
__device__ __forceinline__ u32x4 ld_sc1(__amdgpu_buffer_rsrc_t rs, int off) {
  return __builtin_amdgcn_raw_buffer_load_b128(rs, off, 0, 16);
}
__device__ __forceinline__ void st_sc1(__amdgpu_buffer_rsrc_t rs, int off, u32x4 v) {
  __builtin_amdgcn_raw_buffer_store_b128(v, rs, off, 0, 16);
}
__device__ __forceinline__ void swap16(unsigned& x, unsigned& y) {
  auto r = __builtin_amdgcn_permlane16_swap(x, y, false, false);
  x = r[0]; y = r[1];
}
__device__ __forceinline__ void swap32(unsigned& x, unsigned& y) {
  auto r = __builtin_amdgcn_permlane32_swap(x, y, false, false);
  x = r[0]; y = r[1];
}
__device__ __forceinline__ bool spin_fail(unsigned& spins, gu32* abortw) {
  ++spins;
  if ((spins & 63u) == 0u) {
    const __amdgpu_buffer_rsrc_t ra = mkrsrc((const void*)abortw, 4);
    unsigned z = 0;
    asm volatile("" : "+v"(z));
    if (__builtin_amdgcn_readfirstlane(__builtin_amdgcn_raw_buffer_load_b32(ra, z, 0, 16)) == 1u) return true;
    if (spins > (1u << 19)) {
      __builtin_amdgcn_raw_buffer_store_b32(z + 1u, ra, z, 0, 16);
      return true;
    }
  }
  return false;
}
__device__ __forceinline__ bf16x8 ldsf(const char* p) { return *reinterpret_cast<const bf16x8*>(p); }
#define SCHED() __builtin_amdgcn_sched_barrier(0)
#define LBAR() do { asm volatile("s_waitcnt lgkmcnt(0)" ::: "memory"); __builtin_amdgcn_s_barrier(); asm volatile("" ::: "memory"); } while (0)
#define WAIT_V(n) asm volatile("s_waitcnt vmcnt(%0)" :: "n"(n) : "memory")

#define HOLD_V(c, n) asm volatile("s_cmp_eq_u32 %0, 0\n\ts_cbranch_scc1 .Lh%=\n\ts_waitcnt vmcnt(%1)\n.Lh%=:" :: "s"((int)(c)), "n"(n) : "memory", "scc")
#define TH_Q 2
__device__ __forceinline__ void wait_flags(__amdgpu_buffer_rsrc_t rs, int off, gu32* abortw) {
  for (unsigned spins = 0;;) {
    asm volatile("" ::: "memory");
    const unsigned v = __builtin_amdgcn_raw_buffer_load_b32(rs, off, 0, 16);
    if (__all(v != SENT)) break;
    __builtin_amdgcn_s_sleep(4);
    if (spin_fail(spins, abortw)) break;
  }
  asm volatile("" ::: "memory");
}

__device__ __forceinline__ unsigned offA(unsigned row, unsigned ch) {
  return row * 128u + 16u * (ch ^ (row & 7u));
}
__device__ __forceinline__ unsigned swzB(unsigned row) { return ((row & 3u) << 2) | ((row >> 2) & 3u); }
__device__ __forceinline__ unsigned offB(unsigned row, unsigned ch) {
  return 256u * row + 16u * (ch ^ swzB(row));
}
__device__ __forceinline__ void glds16(const u16* src, char* dst) {
  const unsigned d = __builtin_amdgcn_readfirstlane(lds_off(dst));
  unsigned keep;
  asm volatile("s_mov_b32 %0, m0\n\ts_mov_b32 m0, %2\n\ts_nop 0\n\tglobal_load_lds_dwordx4 %1, off\n\ts_mov_b32 m0, %0"
               : "=&s"(keep) : "v"(src), "s"(d) : "memory");
}
__device__ __forceinline__ bf16x8 ld_bfrag(const char* sb, int ks, int c, int lane) {
  const unsigned g = lane >> 4, qq = (lane & 15) >> 2, pp = lane & 3;
  s16x4 lo = __builtin_amdgcn_ds_read_tr16_b64_v4i16(
      (lds_s16x4_ptr)(sb + offB(32 * ks + 8 * g + qq, 2 * c + (pp >> 1)) + 8 * (pp & 1)));
  s16x4 hi = __builtin_amdgcn_ds_read_tr16_b64_v4i16(
      (lds_s16x4_ptr)(sb + offB(32 * ks + 8 * g + 4 + qq, 2 * c + (pp >> 1)) + 8 * (pp & 1)));
  return (bf16x8){lo[0], lo[1], lo[2], lo[3], hi[0], hi[1], hi[2], hi[3]};
}

__device__ __forceinline__ void blk_dma(const u16* src, size_t ld, char* slot, int lane) {
  const u16* s = src + (size_t)(lane >> 2) * ld + (lane & 3) * 8;
  glds16(s, slot);
  glds16(s + 16 * ld, slot + 1024);
}
template <int NB, typename ISSUE, typename SINK>
__device__ __forceinline__ void load_blocks(char* slice, ISSUE&& issue, SINK&& sink) {
#pragma unroll
  for (int i = 0; i < 8; ++i) issue(i, slice + i * 2048);
#pragma unroll
  for (int g = 0; g < NB / 4; ++g) {
    if (g + 1 < NB / 4) WAIT_V(8); else WAIT_V(0);
#pragma unroll
    for (int j = 0; j < 4; ++j) sink(4 * g + j, slice + ((4 * g + j) & 7) * 2048);
    asm volatile("s_waitcnt lgkmcnt(0)" ::: "memory");
    if (g + 2 < NB / 4) {
#pragma unroll
      for (int j = 0; j < 4; ++j) issue(4 * g + 8 + j, slice + ((4 * g + j) & 7) * 2048);
    }
  }
}

__device__ __forceinline__ u32x4 widen32(unsigned a0, unsigned a1, unsigned b0, unsigned b1) {
  swap32(a0, b0); swap16(a0, b0);
  swap32(a1, b1); swap16(a1, b1);
  return (u32x4){a0, a1, b0, b1};
}

__device__ __forceinline__ void publish_flags(__amdgpu_buffer_rsrc_t rs, int off, int stride, int n) {
  WAIT_V(0);
  LBAR();
  int tid = threadIdx.x;
  asm volatile("" : "+v"(tid));
  if (tid < n) __builtin_amdgcn_raw_buffer_store_b32(1u, rs, off + tid * stride, 0, 16);
}

__device__ __forceinline__ void wait_s(const Args& p, __amdgpu_buffer_rsrc_t rsy, int rblk, int c0, int n, bool need) {
  if (need) {
    if (threadIdx.x < 64) {
      for (unsigned spins = 0;;) {
        asm volatile("" ::: "memory");
        const int f = (int)__builtin_amdgcn_raw_buffer_load_b32(rsy, SY_FS + (2 * c0 + (threadIdx.x & (2 * n - 1))) * 128, 0, 16);
        if (__all(f >= rblk)) break;
        __builtin_amdgcn_s_sleep(32);
        if (spin_fail(spins, (gu32*)(p.ws + WS_SY + SY_ABORT))) break;
      }
    }
    LBAR();
  }
}
__device__ __forceinline__ u32x4 combine(u32x4 sv, u32x4 g, u32x4 x) {
  u32x4 o;
#pragma unroll
  for (int j = 0; j < 4; ++j) {
    const unsigned t = cvtpk(lo_f(sv[j]) * lo_f(g[j]), hi_f(sv[j]) * hi_f(g[j]));
    o[j] = cvtpk(lo_f(x[j]) + lo_f(t), hi_f(x[j]) + hi_f(t));
  }
  return o;
}
#define OUT_AUX 0
__device__ __forceinline__ void st_out(const Args& p, int e, u32x4 v) {
  __builtin_amdgcn_raw_buffer_store_b128(v, mkrsrc(p.out, S_LEN * DM * 2), e * 2, 0, OUT_AUX);
}
struct SX { u32x4 s, x; };
__device__ __forceinline__ SX get_sx(const Args& p, int rblk, int e) {
  SX r; r.s = (u32x4){0u, 0u, 0u, 0u}; r.x = r.s;
  if (rblk < NFUSE) {
    r.s = ld_sc1(mkrsrc(p.ws + WS_S, S_LEN * DM * 2), (((e & 1023) >> 5) * S_LEN + (e >> 10)) * 64 + (e & 31) * 2);
    r.x = *reinterpret_cast<const u32x4*>(p.x + e);
  }
  return r;
}
__device__ __forceinline__ void put_g(const Args& p, __amdgpu_buffer_rsrc_t rg, int rblk, int e, u32x4 g, const SX& v) {
  if (rblk < NFUSE) {
    st_out(p, e, combine(v.s, g, v.x));
  } else st_sc1(rg, e * 2, g);
}

__device__ __forceinline__ void job_comb(const Args& p, int rblk, int ct, __amdgpu_buffer_rsrc_t rsy) {
  int tid = threadIdx.x;
  asm volatile("" : "+v"(tid));
  u32x4 xv[8];
#pragma unroll
  for (int i = 0; i < 8; ++i) xv[i] = *reinterpret_cast<const u32x4*>(p.x + (rblk * 128 + i * 16 + (tid >> 5)) * DM + ct * 256 + (tid & 31) * 8);
  if (tid < 64) wait_flags(rsy, SY_FG + (rblk * 8 + 2 * ct + (tid & 1)) * 128, (gu32*)(p.ws + WS_SY + SY_ABORT));
  wait_s(p, rsy, rblk, ct * 8, 8, true);
  u32x4 sv[8], gv[8];
#pragma unroll
  for (int i = 0; i < 8; ++i) {
    const int e = (rblk * 128 + i * 16 + (tid >> 5)) * DM + ct * 256 + (tid & 31) * 8;
    sv[i] = ld_sc1(mkrsrc(p.ws + WS_S, S_LEN * DM * 2), (((e & 1023) >> 5) * S_LEN + (e >> 10)) * 64 + (e & 31) * 2);
    gv[i] = ld_sc1(mkrsrc(p.ws + WS_G, S_LEN * DM * 2), e * 2);
  }
#pragma unroll
  for (int i = 0; i < 8; ++i)
    st_out(p, (rblk * 128 + i * 16 + (tid >> 5)) * DM + ct * 256 + (tid & 31) * 8, combine(sv[i], gv[i], xv[i]));
}

__device__ __forceinline__ u32x4 raw_rsrc(const void* p, unsigned bytes) {
  const unsigned long long a = (unsigned long long)p;
  return (u32x4){(unsigned)a, (unsigned)(a >> 32) & 0xffffu, bytes, 0x00020000u};
}
__device__ __forceinline__ u32x2 next_tag(u32x2 e) { return (u32x2){e[0] + 1u, e[1] + (e[0] == 0xFFFFFFFFu)}; }
struct Boot { u32x2 e; u32x4 c; };
__device__ __forceinline__ void boot_ask(const char* ws, Boot& r) {
  const unsigned o = (threadIdx.x & 63) * 128;
  asm volatile("buffer_load_dwordx2 %0, off, %3, %4 sc1\n\tbuffer_load_dwordx4 %1, %2, %3, %5 offen sc1"
               : "=&v"(r.e), "=&v"(r.c) : "v"(o), "s"(raw_rsrc(ws, SY_TOTAL)), "s"(SY_EP), "s"(SY_BT) : "memory");
}
__device__ __forceinline__ void wait_rows(const char* ws, u32x2 tag) {
  const __amdgpu_buffer_rsrc_t rs = mkrsrc(ws, SY_TOTAL);
  int o = (threadIdx.x & 63) * 128;
  asm volatile("" : "+v"(o));
#pragma unroll 1
  for (int i = 0; i < (1 << 17); ++i) {
    asm volatile("" ::: "memory");
    const u32x2 a = __builtin_amdgcn_raw_buffer_load_b64(rs, o, SY_BT + 32 * 128, 16), b = __builtin_amdgcn_raw_buffer_load_b64(rs, o, SY_BT + 96 * 128, 16);
    if (__all(a[0] == tag[0] && a[1] == tag[1] && b[0] == tag[0] && b[1] == tag[1])) break;
    __builtin_amdgcn_s_sleep(8);
  }
  asm volatile("" ::: "memory");
}
__device__ __forceinline__ unsigned boot_done(const char* ws, Boot& r, bool own = false, u32x2 tag = (u32x2){0u, 0u}) {
#pragma unroll 1
  for (int i = 0; i < (1 << 17); ++i) {
    asm volatile("" : "+v"(r.e), "+v"(r.c));
    if (!own) tag = next_tag(r.e);
    if (__all(r.c[0] == tag[0] && r.c[1] == tag[1] && (r.c[2] ^ r.c[3]) == tag[0])) break;
    __builtin_amdgcn_s_sleep(8);
    boot_ask(ws, r);
    WAIT_V(0);
  }
  unsigned q0[NPAGE], q1[NPAGE], ga = 0xFFFFu, gb = 0xFFFFu;
#pragma unroll
  for (int i = 0; i < NPAGE; ++i) {
    const unsigned x = __builtin_amdgcn_readlane(r.c[2], i), y = __builtin_amdgcn_readlane(r.c[2], i + NPAGE);
    q0[i] = min(x >> 16, y >> 16); q1[i] = min(x & 0xFFFFu, y & 0xFFFFu);
    ga = min(ga, (i & 1) ? q1[i] : q0[i]); gb = min(gb, (i & 1) ? q0[i] : q1[i]);
  }
  unsigned m = gb < ga ? 0x80001555u : 0x2AAAu;
  const unsigned thr = min(ga, gb) + 110;
#pragma unroll
  for (int i = 0; i < NPAGE; ++i) {
    const unsigned lo = min(q0[i], q1[i]), hi = max(q0[i], q1[i]);
    const unsigned keep = (lo < thr ? hi : 0u) > lo + 100 ? ~(1u << i) : ~0u;
    m = (m & keep) | ((q1[i] < q0[i] ? ~0u : 0u) & ~keep);
  }
  return m;
}
typedef __attribute__((address_space(3))) volatile unsigned* lds_vu_ptr;
__device__ __forceinline__ lds_vu_ptr lds_at(const void* p) {
  unsigned a = lds_off(p);
  asm volatile("" : "+v"(a));
  return (lds_vu_ptr)(uintptr_t)a;
}
__device__ __forceinline__ unsigned uni(unsigned v) {
  unsigned r;
  asm volatile("s_nop 1\n\tv_readfirstlane_b32 %0, %1\n\ts_nop 3" : "=s"(r) : "v"(v));
  return r;
}
__device__ __forceinline__ void start_sy(char* ws, const u16* mem0, int i, char* smem) {
  const int tid = threadIdx.x, lane = tid & 63, w = __builtin_amdgcn_readfirstlane(tid >> 6);
  const __amdgpu_buffer_rsrc_t rsy = mkrsrc(ws, SY_TOTAL);
  const __amdgpu_buffer_rsrc_t rs = mkrsrc(ws + (size_t)(i % NPAGE) * PAGE + PROBE - (i / NPAGE) * 2048, 16384);
  const u32x2 e = __builtin_amdgcn_raw_buffer_load_b64(rsy, 0, SY_EP, 16);
  const u32x4 ds = {DSENT, DSENT, DSENT, DSENT};
  if (lane < 32) st_sc1(rs, (lane >> 4) * 8192 + w * 256 + (lane & 15) * 16, ds);
  static_assert(NPRE == 4 && SY_FEND - SY_BR <= 32 * 6144, "");
#pragma unroll
  for (int u = 0; u < 6; ++u)
    st_sc1(rsy, (u < 4 ? SY_V + (i * 4 + u) * 8192 : SY_O + (i * 2 + u - 4) * 8192) + tid * 16, ds);
  if (w == 7) {
    const int n = i * 64 + lane, j = (n >> 10) * 512 + (n & 511);
    const unsigned* m0 = reinterpret_cast<const unsigned*>(mem0 + (2 * ((j >> 2) & 3) + (j & 1)) * DM + (j >> 4) * 16) + ((j >> 1) & 1);
    u32x4 m;
#pragma unroll
    for (int k = 0; k < 4; ++k) { const unsigned t = m0[2 * k]; m[k] = t == DSENT ? 0xFFFFFFFFu : t; }
    st_sc1(rsy, n * 16, m);
  } else if (w == 6) st_sc1(rsy, 32768 + i * 1024 + lane * 16, ds);
  else {
    const int o = SY_BR + i * 6144 + tid * 16;
    st_sc1(rsy, o < SY_FEND ? o : 0x7F000000, (u32x4){SENT, SENT, SENT, SENT});
  }
  unsigned t[4];
#pragma unroll
  for (int k = 0; k < 4; ++k) {
    unsigned long long c0, c1; unsigned v, off = ((k ^ (k >> 1)) & 1) * 8192 + w * 256 + (k >> 1) * 128;
    asm volatile("s_waitcnt vmcnt(0)\n\ts_barrier\n\ts_memtime %0\n\ts_waitcnt lgkmcnt(0)\n\tbuffer_load_dword %2, %3, %4, 0 offen sc1\n\ts_waitcnt vmcnt(0)\n\t"
                 "s_memtime %1\n\ts_waitcnt lgkmcnt(0)" : "=&s"(c0), "=&s"(c1), "=&v"(v) : "v"(off), "s"(rs) : "memory");
    t[k] = (unsigned)(c1 - c0);
  }
  lds_vu_ptr tm = lds_at(smem);
  if (lane == 0) { tm[w] = min(t[0], t[3]); tm[8 + w] = min(t[1], t[2]); }
  int ro = SY_BT + i * 128 + lane * 16;
  asm volatile("" : "+v"(ro));
  LBAR();
  if (threadIdx.x < 8) {
    unsigned m0 = tm[0], m1 = tm[8];
#pragma unroll
    for (int k = 1; k < 8; ++k) { m0 = min(m0, tm[k]); m1 = min(m1, tm[8 + k]); }
    const u32x2 tag = next_tag(e);
    const unsigned v = min(m0, 30000u) << 16 | min(m1, 30000u);
    st_sc1(rsy, ro, (u32x4){tag[0], tag[1], v, v ^ tag[0]});
  }
}
template <int NB, typename F>
__device__ __forceinline__ u32x2 start_rows(const Args& p, int g, unsigned on, F&& between) {
  const int tid = threadIdx.x, lane = tid & 63, row0 = g * 16 + (tid >> 6) * 2;
  u32x4 a[4];
  u32x2 e;
  asm volatile("buffer_load_dwordx4 %0, %5, %8, 0 offen\n\tbuffer_load_dwordx4 %1, %5, %8, 0 offen offset:1024\n\t"
               "buffer_load_dwordx4 %2, %5, %8, 0 offen offset:2048\n\tbuffer_load_dwordx4 %3, %5, %8, 0 offen offset:3072\n\t"
               "buffer_load_dwordx2 %4, off, %6, %7 sc1"
               : "=&v"(a[0]), "=&v"(a[1]), "=&v"(a[2]), "=&v"(a[3]), "=&v"(e)
               : "v"(row0 * (DM * 2) + lane * 16), "s"(raw_rsrc(p.ws, SY_TOTAL)), "s"(SY_EP), "s"(raw_rsrc(p.x, on * (S_LEN * DM * 2))) : "memory");
  between();
  asm volatile("s_waitcnt vmcnt(%5)" : "+v"(a[0]), "+v"(a[1]), "+v"(a[2]), "+v"(a[3]), "+v"(e) : "n"(NB) : "memory");
  const __amdgpu_buffer_rsrc_t ro = mkrsrc(p.ws + WS_XN, on * (S_LEN * XLD * 2));
#pragma unroll
  for (int u = 0; u < 2; ++u) {
    float v[16];
#pragma unroll
    for (int i = 0; i < 8; ++i) { v[2 * i] = lo_f(a[2 * u + (i >> 2)][i & 3]); v[2 * i + 1] = hi_f(a[2 * u + (i >> 2)][i & 3]); }
    float s = 0.f;
#pragma unroll
    for (int i = 0; i < 16; ++i) s += v[i];
    const float mean = wave_sum(s) * (1.0f / DM);
    float q = 0.f;
#pragma unroll
    for (int i = 0; i < 16; ++i) { float d = v[i] - mean; q += d * d; }
    const float var = rbf(wave_sum(q) * (1.0f / DM));
    const float mb = rbf(mean);
    const float inv = 1.0f / rbf(sqrtf(rbf(var + 1e-5f)));
#pragma unroll
    for (int k = 0; k < 2; ++k) {
      u32x4 o;
#pragma unroll
      for (int i = 0; i < 4; ++i) {
        const unsigned t = cvtpk(v[8 * k + 2 * i] - mb, v[8 * k + 2 * i + 1] - mb);
        o[i] = cvtpk(lo_f(t) * inv, hi_f(t) * inv);
      }
      st_sc1(ro, (row0 + u) * (XLD * 2) + k * 1024 + lane * 16, o);
    }
  }
  return next_tag(e);
}
__device__ __forceinline__ void report_rows(const Args& p, int g, u32x2 tag) {
  if (threadIdx.x < 8) st_sc1(mkrsrc(p.ws + WS_SY, SY_TOTAL), SY_BT + (32 + g) * 128 + threadIdx.x * 16, (u32x4){tag[0], tag[1], 0u, tag[0]});
}

typedef __attribute__((ext_vector_type(32))) unsigned u32x32;
__device__ __forceinline__ void put4(u32x32& W, int i, bf16x8 f) {
  const u32x4 v = __builtin_bit_cast(u32x4, f);
#pragma unroll
  for (int j = 0; j < 4; ++j) W[4 * i + j] = v[j];
}
#define CLB8(a, b, c, d, e, f, g, h) "v" #a, "v" #b, "v" #c, "v" #d, "v" #e, "v" #f, "v" #g, "v" #h
#define CLB32(a) CLB32_##a
#define CLB32_128 CLB8(128, 129, 130, 131, 132, 133, 134, 135), CLB8(136, 137, 138, 139, 140, 141, 142, 143), CLB8(144, 145, 146, 147, 148, 149, 150, 151), CLB8(152, 153, 154, 155, 156, 157, 158, 159)
#define CLB32_160 CLB8(160, 161, 162, 163, 164, 165, 166, 167), CLB8(168, 169, 170, 171, 172, 173, 174, 175), CLB8(176, 177, 178, 179, 180, 181, 182, 183), CLB8(184, 185, 186, 187, 188, 189, 190, 191)
#define W4_IN "{v[0:31]}"(W[0]), "{v[32:63]}"(W[1]), "{v[64:95]}"(W[2]), "{v[96:127]}"(W[3])
#define CLB32_192 CLB8(192, 193, 194, 195, 196, 197, 198, 199), CLB8(200, 201, 202, 203, 204, 205, 206, 207), CLB8(208, 209, 210, 211, 212, 213, 214, 215), CLB8(216, 217, 218, 219, 220, 221, 222, 223)
__device__ __forceinline__ unsigned ld_flag0(__amdgpu_buffer_rsrc_t rs, int off) {
  return __builtin_amdgcn_raw_buffer_load_b32(rs, off, 0, 16);
}
__device__ __forceinline__ void glds16_sc1(const char* src, char* dst) {
  const unsigned d = __builtin_amdgcn_readfirstlane(lds_off(dst));
  unsigned keep;
  asm volatile("s_mov_b32 %0, m0\n\ts_mov_b32 m0, %2\n\ts_nop 0\n\tglobal_load_lds_dwordx4 %1, off sc1\n\ts_mov_b32 m0, %0"
               : "=&s"(keep) : "v"(src), "s"(d) : "memory");
}

__device__ __forceinline__ void touch_line(const char* src, char* dst) {
  const unsigned d = __builtin_amdgcn_readfirstlane(lds_off(dst));
  unsigned keep;
  asm volatile("s_mov_b32 %0, m0\n\ts_mov_b32 m0, %2\n\ts_nop 0\n\tglobal_load_lds_dword %1, off sc1\n\ts_mov_b32 m0, %0"
               : "=&s"(keep) : "v"(src), "s"(d) : "memory");
}

#define PF_N 12
#define LN_M 3
#define LN_X 1
#define PR_G1 8
#define WREV 1
template <bool DEV = false>
__device__ __forceinline__ void glds2(const char* base, int step, unsigned voff, unsigned dst) {
  unsigned keep;
  const char* base2 = base + step;
  if (DEV)
    asm volatile("s_mov_b32 %0, m0\n\ts_mov_b32 m0, %4\n\ts_nop 0\n\tglobal_load_lds_dwordx4 %1, %2 sc1\n\t"
                 "s_add_u32 m0, m0, 0x2000\n\ts_nop 0\n\tglobal_load_lds_dwordx4 %1, %3 sc1\n\ts_mov_b32 m0, %0"
                 : "=&s"(keep) : "v"(voff), "s"(base), "s"(base2), "s"(dst) : "memory", "scc");
  else
  asm volatile("s_mov_b32 %0, m0\n\ts_mov_b32 m0, %4\n\ts_nop 0\n\tglobal_load_lds_dwordx4 %1, %2\n\t"
               "s_add_u32 m0, m0, 0x2000\n\ts_nop 0\n\tglobal_load_lds_dwordx4 %1, %3\n\ts_mov_b32 m0, %0"
               : "=&s"(keep) : "v"(voff), "s"(base), "s"(base2), "s"(dst) : "memory", "scc");
}

#define DN_ASM \
  "s_mov_b32 s94, m0\n\t" \
  "s_mov_b64 s[84:85], %[pa]\n\t" \
  "s_mov_b64 s[86:87], %[pb]\n\t" \
  "s_movk_i32 s88, 0x4000\n\t" \
  "s_mov_b32 s89, 0x10000\n\t" \
  "s_movk_i32 s90, 13\n\t" \
  "v_mov_b64 v[0:1], 0\n\t" \
  "v_mov_b64 v[2:3], 0\n\t" \
  "v_mov_b64 v[4:5], 0\n\t" \
  "v_mov_b64 v[6:7], 0\n\t" \
  "v_mov_b64 v[8:9], 0\n\t" \
  "v_mov_b64 v[10:11], 0\n\t" \
  "v_mov_b64 v[12:13], 0\n\t" \
  "v_mov_b64 v[14:15], 0\n\t" \
  "v_mov_b64 v[16:17], 0\n\t" \
  "v_mov_b64 v[18:19], 0\n\t" \
  "v_mov_b64 v[20:21], 0\n\t" \
  "v_mov_b64 v[22:23], 0\n\t" \
  "v_mov_b64 v[24:25], 0\n\t" \
  "v_mov_b64 v[26:27], 0\n\t" \
  "v_mov_b64 v[28:29], 0\n\t" \
  "v_mov_b64 v[30:31], 0\n\t" \
  "s_cmp_lg_u32 %[hf], 0\n\t" \
  "s_cbranch_scc1 .Lk%=\n\t" \
  "ds_read_b128 v[32:35], v80 offset:0\n\t" \
  "ds_read_b128 v[36:39], v80 offset:1024\n\t" \
  "ds_read_b128 v[40:43], v80 offset:2048\n\t" \
  "ds_read_b128 v[44:47], v80 offset:3072\n\t" \
  "ds_read_b64_tr_b16 v[64:65], v82 offset:0\n\t" \
  "ds_read_b64_tr_b16 v[66:67], v83 offset:0\n\t" \
  "ds_read_b128 v[48:51], v80 offset:8192\n\t" \
  "ds_read_b64_tr_b16 v[68:69], v84 offset:0\n\t" \
  "ds_read_b64_tr_b16 v[70:71], v85 offset:0\n\t" \
  "ds_read_b128 v[52:55], v80 offset:9216\n\t" \
  "ds_read_b128 v[56:59], v80 offset:10240\n\t" \
  "ds_read_b128 v[60:63], v80 offset:11264\n\t" \
  ".Lp0%=:\n\t" \
  "s_waitcnt vmcnt(4)\n\t" \
  "s_barrier\n\t" \
  "s_waitcnt lgkmcnt(3)\n\t" \
  "v_mfma_f32_16x16x32_bf16 v[0:3], v[64:67], v[32:35], v[0:3]\n\t" \
  "ds_read_b64_tr_b16 v[72:73], v82 offset:8192\n\t" \
  "v_mfma_f32_16x16x32_bf16 v[4:7], v[68:71], v[32:35], v[4:7]\n\t" \
  "ds_read_b64_tr_b16 v[74:75], v83 offset:8192\n\t" \
  "ds_read_b128 v[32:35], v80 offset:32768\n\t" \
  "s_add_u32 m0, %[lw], 98304\n\t" \
  "v_mfma_f32_16x16x32_bf16 v[8:11], v[64:67], v[36:39], v[8:11]\n\t" \
  "ds_read_b64_tr_b16 v[76:77], v84 offset:8192\n\t" \
  "global_load_lds_dwordx4 v90, s[84:85] sc1\n\t" \
  "s_add_u32 s84, s84, s88\n\t" \
  "s_addc_u32 s85, s85, 0\n\t" \
  "v_mfma_f32_16x16x32_bf16 v[12:15], v[68:71], v[36:39], v[12:15]\n\t" \
  "ds_read_b64_tr_b16 v[78:79], v85 offset:8192\n\t" \
  "ds_read_b128 v[36:39], v80 offset:33792\n\t" \
  "v_mfma_f32_16x16x32_bf16 v[16:19], v[64:67], v[40:43], v[16:19]\n\t" \
  "v_mfma_f32_16x16x32_bf16 v[20:23], v[68:71], v[40:43], v[20:23]\n\t" \
  "ds_read_b128 v[40:43], v80 offset:34816\n\t" \
  "s_add_u32 m0, %[lw], 114688\n\t" \
  "v_mfma_f32_16x16x32_bf16 v[24:27], v[64:67], v[44:47], v[24:27]\n\t" \
  "global_load_lds_dwordx4 v91, s[86:87]\n\t" \
  "s_add_u32 s86, s86, s89\n\t" \
  "s_addc_u32 s87, s87, 0\n\t" \
  "v_mfma_f32_16x16x32_bf16 v[28:31], v[68:71], v[44:47], v[28:31]\n\t" \
  "ds_read_b128 v[44:47], v80 offset:35840\n\t" \
  "s_waitcnt lgkmcnt(3)\n\t" \
  "v_mfma_f32_16x16x32_bf16 v[0:3], v[72:75], v[48:51], v[0:3]\n\t" \
  "ds_read_b64_tr_b16 v[64:65], v82 offset:32768\n\t" \
  "v_mfma_f32_16x16x32_bf16 v[4:7], v[76:79], v[48:51], v[4:7]\n\t" \
  "ds_read_b64_tr_b16 v[66:67], v83 offset:32768\n\t" \
  "ds_read_b128 v[48:51], v80 offset:40960\n\t" \
  "s_add_u32 m0, %[lw], 106496\n\t" \
  "v_mfma_f32_16x16x32_bf16 v[8:11], v[72:75], v[52:55], v[8:11]\n\t" \
  "ds_read_b64_tr_b16 v[68:69], v84 offset:32768\n\t" \
  "global_load_lds_dwordx4 v90, s[84:85] sc1\n\t" \
  "s_add_u32 s84, s84, s88\n\t" \
  "s_addc_u32 s85, s85, 0\n\t" \
  "v_mfma_f32_16x16x32_bf16 v[12:15], v[76:79], v[52:55], v[12:15]\n\t" \
  "ds_read_b64_tr_b16 v[70:71], v85 offset:32768\n\t" \
  "ds_read_b128 v[52:55], v80 offset:41984\n\t" \
  "v_mfma_f32_16x16x32_bf16 v[16:19], v[72:75], v[56:59], v[16:19]\n\t" \
  "v_mfma_f32_16x16x32_bf16 v[20:23], v[76:79], v[56:59], v[20:23]\n\t" \
  "ds_read_b128 v[56:59], v80 offset:43008\n\t" \
  "s_add_u32 m0, %[lw], 122880\n\t" \
  "v_mfma_f32_16x16x32_bf16 v[24:27], v[72:75], v[60:63], v[24:27]\n\t" \
  "global_load_lds_dwordx4 v91, s[86:87]\n\t" \
  "s_add_u32 s86, s86, s89\n\t" \
  "s_addc_u32 s87, s87, 0\n\t" \
  "v_mfma_f32_16x16x32_bf16 v[28:31], v[76:79], v[60:63], v[28:31]\n\t" \
  "ds_read_b128 v[60:63], v80 offset:44032\n\t" \
  "s_sub_u32 s90, s90, 1\n\t" \
  "s_cselect_b32 s88, 0, s88\n\t" \
  "s_cselect_b32 s89, 0, s89\n\t" \
  "s_cselect_b32 s91, 16384, 0\n\t" \
  "s_sub_u32 s84, s84, s91\n\t" \
  "s_subb_u32 s85, s85, 0\n\t" \
  "s_lshl_b32 s91, s91, 2\n\t" \
  "s_sub_u32 s86, s86, s91\n\t" \
  "s_subb_u32 s87, s87, 0\n\t" \
  "s_waitcnt vmcnt(4)\n\t" \
  "s_barrier\n\t" \
  "s_waitcnt lgkmcnt(3)\n\t" \
  "v_mfma_f32_16x16x32_bf16 v[0:3], v[64:67], v[32:35], v[0:3]\n\t" \
  "ds_read_b64_tr_b16 v[72:73], v82 offset:40960\n\t" \
  "v_mfma_f32_16x16x32_bf16 v[4:7], v[68:71], v[32:35], v[4:7]\n\t" \
  "ds_read_b64_tr_b16 v[74:75], v83 offset:40960\n\t" \
  "ds_read_b128 v[32:35], v81 offset:0\n\t" \
  "s_add_u32 m0, %[lw], 0\n\t" \
  "v_mfma_f32_16x16x32_bf16 v[8:11], v[64:67], v[36:39], v[8:11]\n\t" \
  "ds_read_b64_tr_b16 v[76:77], v84 offset:40960\n\t" \
  "global_load_lds_dwordx4 v90, s[84:85] sc1\n\t" \
  "s_add_u32 s84, s84, s88\n\t" \
  "s_addc_u32 s85, s85, 0\n\t" \
  "v_mfma_f32_16x16x32_bf16 v[12:15], v[68:71], v[36:39], v[12:15]\n\t" \
  "ds_read_b64_tr_b16 v[78:79], v85 offset:40960\n\t" \
  "ds_read_b128 v[36:39], v81 offset:1024\n\t" \
  "v_mfma_f32_16x16x32_bf16 v[16:19], v[64:67], v[40:43], v[16:19]\n\t" \
  "v_mfma_f32_16x16x32_bf16 v[20:23], v[68:71], v[40:43], v[20:23]\n\t" \
  "ds_read_b128 v[40:43], v81 offset:2048\n\t" \
  "s_add_u32 m0, %[lw], 16384\n\t" \
  "v_mfma_f32_16x16x32_bf16 v[24:27], v[64:67], v[44:47], v[24:27]\n\t" \
  "global_load_lds_dwordx4 v91, s[86:87]\n\t" \
  "s_add_u32 s86, s86, s89\n\t" \
  "s_addc_u32 s87, s87, 0\n\t" \
  "v_mfma_f32_16x16x32_bf16 v[28:31], v[68:71], v[44:47], v[28:31]\n\t" \
  "ds_read_b128 v[44:47], v81 offset:3072\n\t" \
  "s_waitcnt lgkmcnt(3)\n\t" \
  "v_mfma_f32_16x16x32_bf16 v[0:3], v[72:75], v[48:51], v[0:3]\n\t" \
  "ds_read_b64_tr_b16 v[64:65], v86 offset:0\n\t" \
  "v_mfma_f32_16x16x32_bf16 v[4:7], v[76:79], v[48:51], v[4:7]\n\t" \
  "ds_read_b64_tr_b16 v[66:67], v87 offset:0\n\t" \
  "ds_read_b128 v[48:51], v81 offset:8192\n\t" \
  "s_add_u32 m0, %[lw], 8192\n\t" \
  "v_mfma_f32_16x16x32_bf16 v[8:11], v[72:75], v[52:55], v[8:11]\n\t" \
  "ds_read_b64_tr_b16 v[68:69], v88 offset:0\n\t" \
  "global_load_lds_dwordx4 v90, s[84:85] sc1\n\t" \
  "s_add_u32 s84, s84, s88\n\t" \
  "s_addc_u32 s85, s85, 0\n\t" \
  "v_mfma_f32_16x16x32_bf16 v[12:15], v[76:79], v[52:55], v[12:15]\n\t" \
  "ds_read_b64_tr_b16 v[70:71], v89 offset:0\n\t" \
  "ds_read_b128 v[52:55], v81 offset:9216\n\t" \
  "v_mfma_f32_16x16x32_bf16 v[16:19], v[72:75], v[56:59], v[16:19]\n\t" \
  "v_mfma_f32_16x16x32_bf16 v[20:23], v[76:79], v[56:59], v[20:23]\n\t" \
  "ds_read_b128 v[56:59], v81 offset:10240\n\t" \
  "s_add_u32 m0, %[lw], 24576\n\t" \
  "v_mfma_f32_16x16x32_bf16 v[24:27], v[72:75], v[60:63], v[24:27]\n\t" \
  "global_load_lds_dwordx4 v91, s[86:87]\n\t" \
  "s_add_u32 s86, s86, s89\n\t" \
  "s_addc_u32 s87, s87, 0\n\t" \
  "v_mfma_f32_16x16x32_bf16 v[28:31], v[76:79], v[60:63], v[28:31]\n\t" \
  "ds_read_b128 v[60:63], v81 offset:11264\n\t" \
  "s_waitcnt vmcnt(4)\n\t" \
  "s_barrier\n\t" \
  "s_waitcnt lgkmcnt(3)\n\t" \
  "v_mfma_f32_16x16x32_bf16 v[0:3], v[64:67], v[32:35], v[0:3]\n\t" \
  "ds_read_b64_tr_b16 v[72:73], v86 offset:8192\n\t" \
  "v_mfma_f32_16x16x32_bf16 v[4:7], v[68:71], v[32:35], v[4:7]\n\t" \
  "ds_read_b64_tr_b16 v[74:75], v87 offset:8192\n\t" \
  "ds_read_b128 v[32:35], v81 offset:32768\n\t" \
  "s_add_u32 m0, %[lw], 32768\n\t" \
  "v_mfma_f32_16x16x32_bf16 v[8:11], v[64:67], v[36:39], v[8:11]\n\t" \
  "ds_read_b64_tr_b16 v[76:77], v88 offset:8192\n\t" \
  "global_load_lds_dwordx4 v90, s[84:85] sc1\n\t" \
  "s_add_u32 s84, s84, s88\n\t" \
  "s_addc_u32 s85, s85, 0\n\t" \
  "v_mfma_f32_16x16x32_bf16 v[12:15], v[68:71], v[36:39], v[12:15]\n\t" \
  "ds_read_b64_tr_b16 v[78:79], v89 offset:8192\n\t" \
  "ds_read_b128 v[36:39], v81 offset:33792\n\t" \
  "v_mfma_f32_16x16x32_bf16 v[16:19], v[64:67], v[40:43], v[16:19]\n\t" \
  "v_mfma_f32_16x16x32_bf16 v[20:23], v[68:71], v[40:43], v[20:23]\n\t" \
  "ds_read_b128 v[40:43], v81 offset:34816\n\t" \
  "s_add_u32 m0, %[lw], 49152\n\t" \
  "v_mfma_f32_16x16x32_bf16 v[24:27], v[64:67], v[44:47], v[24:27]\n\t" \
  "global_load_lds_dwordx4 v91, s[86:87]\n\t" \
  "s_add_u32 s86, s86, s89\n\t" \
  "s_addc_u32 s87, s87, 0\n\t" \
  "v_mfma_f32_16x16x32_bf16 v[28:31], v[68:71], v[44:47], v[28:31]\n\t" \
  "ds_read_b128 v[44:47], v81 offset:35840\n\t" \
  "s_waitcnt lgkmcnt(3)\n\t" \
  "v_mfma_f32_16x16x32_bf16 v[0:3], v[72:75], v[48:51], v[0:3]\n\t" \
  "ds_read_b64_tr_b16 v[64:65], v86 offset:32768\n\t" \
  "v_mfma_f32_16x16x32_bf16 v[4:7], v[76:79], v[48:51], v[4:7]\n\t" \
  "ds_read_b64_tr_b16 v[66:67], v87 offset:32768\n\t" \
  "ds_read_b128 v[48:51], v81 offset:40960\n\t" \
  "s_add_u32 m0, %[lw], 40960\n\t" \
  "v_mfma_f32_16x16x32_bf16 v[8:11], v[72:75], v[52:55], v[8:11]\n\t" \
  "ds_read_b64_tr_b16 v[68:69], v88 offset:32768\n\t" \
  "global_load_lds_dwordx4 v90, s[84:85] sc1\n\t" \
  "s_add_u32 s84, s84, s88\n\t" \
  "s_addc_u32 s85, s85, 0\n\t" \
  "v_mfma_f32_16x16x32_bf16 v[12:15], v[76:79], v[52:55], v[12:15]\n\t" \
  "ds_read_b64_tr_b16 v[70:71], v89 offset:32768\n\t" \
  "ds_read_b128 v[52:55], v81 offset:41984\n\t" \
  "v_mfma_f32_16x16x32_bf16 v[16:19], v[72:75], v[56:59], v[16:19]\n\t" \
  "v_mfma_f32_16x16x32_bf16 v[20:23], v[76:79], v[56:59], v[20:23]\n\t" \
  "ds_read_b128 v[56:59], v81 offset:43008\n\t" \
  "s_add_u32 m0, %[lw], 57344\n\t" \
  "v_mfma_f32_16x16x32_bf16 v[24:27], v[72:75], v[60:63], v[24:27]\n\t" \
  "global_load_lds_dwordx4 v91, s[86:87]\n\t" \
  "s_add_u32 s86, s86, s89\n\t" \
  "s_addc_u32 s87, s87, 0\n\t" \
  "v_mfma_f32_16x16x32_bf16 v[28:31], v[76:79], v[60:63], v[28:31]\n\t" \
  "ds_read_b128 v[60:63], v81 offset:44032\n\t" \
  "s_waitcnt vmcnt(4)\n\t" \
  "s_barrier\n\t" \
  "s_waitcnt lgkmcnt(3)\n\t" \
  "v_mfma_f32_16x16x32_bf16 v[0:3], v[64:67], v[32:35], v[0:3]\n\t" \
  "ds_read_b64_tr_b16 v[72:73], v86 offset:40960\n\t" \
  "v_mfma_f32_16x16x32_bf16 v[4:7], v[68:71], v[32:35], v[4:7]\n\t" \
  "ds_read_b64_tr_b16 v[74:75], v87 offset:40960\n\t" \
  "ds_read_b128 v[32:35], v80 offset:0\n\t" \
  "s_add_u32 m0, %[lw], 65536\n\t" \
  "v_mfma_f32_16x16x32_bf16 v[8:11], v[64:67], v[36:39], v[8:11]\n\t" \
  "ds_read_b64_tr_b16 v[76:77], v88 offset:40960\n\t" \
  "global_load_lds_dwordx4 v90, s[84:85] sc1\n\t" \
  "s_add_u32 s84, s84, s88\n\t" \
  "s_addc_u32 s85, s85, 0\n\t" \
  "v_mfma_f32_16x16x32_bf16 v[12:15], v[68:71], v[36:39], v[12:15]\n\t" \
  "ds_read_b64_tr_b16 v[78:79], v89 offset:40960\n\t" \
  "ds_read_b128 v[36:39], v80 offset:1024\n\t" \
  "v_mfma_f32_16x16x32_bf16 v[16:19], v[64:67], v[40:43], v[16:19]\n\t" \
  "v_mfma_f32_16x16x32_bf16 v[20:23], v[68:71], v[40:43], v[20:23]\n\t" \
  "ds_read_b128 v[40:43], v80 offset:2048\n\t" \
  "s_add_u32 m0, %[lw], 81920\n\t" \
  "v_mfma_f32_16x16x32_bf16 v[24:27], v[64:67], v[44:47], v[24:27]\n\t" \
  "global_load_lds_dwordx4 v91, s[86:87]\n\t" \
  "s_add_u32 s86, s86, s89\n\t" \
  "s_addc_u32 s87, s87, 0\n\t" \
  "v_mfma_f32_16x16x32_bf16 v[28:31], v[68:71], v[44:47], v[28:31]\n\t" \
  "ds_read_b128 v[44:47], v80 offset:3072\n\t" \
  "s_waitcnt lgkmcnt(3)\n\t" \
  "v_mfma_f32_16x16x32_bf16 v[0:3], v[72:75], v[48:51], v[0:3]\n\t" \
  "ds_read_b64_tr_b16 v[64:65], v82 offset:0\n\t" \
  "v_mfma_f32_16x16x32_bf16 v[4:7], v[76:79], v[48:51], v[4:7]\n\t" \
  "ds_read_b64_tr_b16 v[66:67], v83 offset:0\n\t" \
  "ds_read_b128 v[48:51], v80 offset:8192\n\t" \
  "s_add_u32 m0, %[lw], 73728\n\t" \
  "v_mfma_f32_16x16x32_bf16 v[8:11], v[72:75], v[52:55], v[8:11]\n\t" \
  "ds_read_b64_tr_b16 v[68:69], v84 offset:0\n\t" \
  "global_load_lds_dwordx4 v90, s[84:85] sc1\n\t" \
  "s_add_u32 s84, s84, s88\n\t" \
  "s_addc_u32 s85, s85, 0\n\t" \
  "v_mfma_f32_16x16x32_bf16 v[12:15], v[76:79], v[52:55], v[12:15]\n\t" \
  "ds_read_b64_tr_b16 v[70:71], v85 offset:0\n\t" \
  "ds_read_b128 v[52:55], v80 offset:9216\n\t" \
  "v_mfma_f32_16x16x32_bf16 v[16:19], v[72:75], v[56:59], v[16:19]\n\t" \
  "v_mfma_f32_16x16x32_bf16 v[20:23], v[76:79], v[56:59], v[20:23]\n\t" \
  "ds_read_b128 v[56:59], v80 offset:10240\n\t" \
  "s_add_u32 m0, %[lw], 90112\n\t" \
  "v_mfma_f32_16x16x32_bf16 v[24:27], v[72:75], v[60:63], v[24:27]\n\t" \
  "global_load_lds_dwordx4 v91, s[86:87]\n\t" \
  "s_add_u32 s86, s86, s89\n\t" \
  "s_addc_u32 s87, s87, 0\n\t" \
  "v_mfma_f32_16x16x32_bf16 v[28:31], v[76:79], v[60:63], v[28:31]\n\t" \
  "ds_read_b128 v[60:63], v80 offset:11264\n\t" \
  "s_cmp_lg_u32 s88, 0\n\t" \
  "s_cbranch_scc1 .Lp0%=\n\t" \
  "s_branch .Le%=\n\t" \
  ".Lk%=:\n\t" \
  "ds_read_b128 v[32:35], v80 offset:0\n\t" \
  "ds_read_b128 v[36:39], v80 offset:1024\n\t" \
  "ds_read_b128 v[40:43], v80 offset:2048\n\t" \
  "ds_read_b128 v[44:47], v80 offset:3072\n\t" \
  "ds_read_b64_tr_b16 v[64:65], v82 offset:0\n\t" \
  "ds_read_b64_tr_b16 v[66:67], v83 offset:0\n\t" \
  "ds_read_b128 v[48:51], v80 offset:8192\n\t" \
  "ds_read_b64_tr_b16 v[68:69], v84 offset:0\n\t" \
  "ds_read_b64_tr_b16 v[70:71], v85 offset:0\n\t" \
  "ds_read_b128 v[52:55], v80 offset:9216\n\t" \
  "ds_read_b128 v[56:59], v80 offset:10240\n\t" \
  "ds_read_b128 v[60:63], v80 offset:11264\n\t" \
  ".Lp1%=:\n\t" \
  "s_waitcnt vmcnt(4)\n\t" \
  "s_barrier\n\t" \
  "s_waitcnt lgkmcnt(3)\n\t" \
  "v_mfma_f32_16x16x32_bf16 v[0:3], v[64:67], v[32:35], v[0:3]\n\t" \
  "ds_read_b64_tr_b16 v[72:73], v82 offset:8192\n\t" \
  "v_mfma_f32_16x16x32_bf16 v[4:7], v[68:71], v[32:35], v[4:7]\n\t" \
  "ds_read_b64_tr_b16 v[74:75], v83 offset:8192\n\t" \
  "ds_read_b128 v[32:35], v80 offset:32768\n\t" \
  "v_mfma_f32_16x16x32_bf16 v[8:11], v[64:67], v[36:39], v[8:11]\n\t" \
  "ds_read_b64_tr_b16 v[76:77], v84 offset:8192\n\t" \
  "v_mfma_f32_16x16x32_bf16 v[12:15], v[68:71], v[36:39], v[12:15]\n\t" \
  "ds_read_b64_tr_b16 v[78:79], v85 offset:8192\n\t" \
  "ds_read_b128 v[36:39], v80 offset:33792\n\t" \
  "s_add_u32 m0, %[lw], 98304\n\t" \
  "v_mfma_f32_16x16x32_bf16 v[16:19], v[64:67], v[40:43], v[16:19]\n\t" \
  "global_load_lds_dwordx4 v90, s[84:85] sc1\n\t" \
  "s_add_u32 s84, s84, s88\n\t" \
  "s_addc_u32 s85, s85, 0\n\t" \
  "v_mfma_f32_16x16x32_bf16 v[20:23], v[68:71], v[40:43], v[20:23]\n\t" \
  "ds_read_b128 v[40:43], v80 offset:34816\n\t" \
  "v_mfma_f32_16x16x32_bf16 v[24:27], v[64:67], v[44:47], v[24:27]\n\t" \
  "s_add_u32 m0, %[lw], 114688\n\t" \
  "v_mfma_f32_16x16x32_bf16 v[28:31], v[68:71], v[44:47], v[28:31]\n\t" \
  "ds_read_b128 v[44:47], v80 offset:35840\n\t" \
  "global_load_lds_dwordx4 v91, s[86:87]\n\t" \
  "s_add_u32 s86, s86, s89\n\t" \
  "s_addc_u32 s87, s87, 0\n\t" \
  "s_waitcnt lgkmcnt(3)\n\t" \
  "v_mfma_f32_16x16x32_bf16 v[0:3], v[72:75], v[48:51], v[0:3]\n\t" \
  "ds_read_b64_tr_b16 v[64:65], v82 offset:32768\n\t" \
  "v_mfma_f32_16x16x32_bf16 v[4:7], v[76:79], v[48:51], v[4:7]\n\t" \
  "ds_read_b64_tr_b16 v[66:67], v83 offset:32768\n\t" \
  "ds_read_b128 v[48:51], v80 offset:40960\n\t" \
  "v_mfma_f32_16x16x32_bf16 v[8:11], v[72:75], v[52:55], v[8:11]\n\t" \
  "ds_read_b64_tr_b16 v[68:69], v84 offset:32768\n\t" \
  "v_mfma_f32_16x16x32_bf16 v[12:15], v[76:79], v[52:55], v[12:15]\n\t" \
  "ds_read_b64_tr_b16 v[70:71], v85 offset:32768\n\t" \
  "ds_read_b128 v[52:55], v80 offset:41984\n\t" \
  "s_add_u32 m0, %[lw], 106496\n\t" \
  "v_mfma_f32_16x16x32_bf16 v[16:19], v[72:75], v[56:59], v[16:19]\n\t" \
  "global_load_lds_dwordx4 v90, s[84:85] sc1\n\t" \
  "s_add_u32 s84, s84, s88\n\t" \
  "s_addc_u32 s85, s85, 0\n\t" \
  "v_mfma_f32_16x16x32_bf16 v[20:23], v[76:79], v[56:59], v[20:23]\n\t" \
  "ds_read_b128 v[56:59], v80 offset:43008\n\t" \
  "v_mfma_f32_16x16x32_bf16 v[24:27], v[72:75], v[60:63], v[24:27]\n\t" \
  "s_add_u32 m0, %[lw], 122880\n\t" \
  "v_mfma_f32_16x16x32_bf16 v[28:31], v[76:79], v[60:63], v[28:31]\n\t" \
  "ds_read_b128 v[60:63], v80 offset:44032\n\t" \
  "global_load_lds_dwordx4 v91, s[86:87]\n\t" \
  "s_add_u32 s86, s86, s89\n\t" \
  "s_addc_u32 s87, s87, 0\n\t" \
  "s_sub_u32 s90, s90, 1\n\t" \
  "s_cselect_b32 s88, 0, s88\n\t" \
  "s_cselect_b32 s89, 0, s89\n\t" \
  "s_cselect_b32 s91, 16384, 0\n\t" \
  "s_sub_u32 s84, s84, s91\n\t" \
  "s_subb_u32 s85, s85, 0\n\t" \
  "s_lshl_b32 s91, s91, 2\n\t" \
  "s_sub_u32 s86, s86, s91\n\t" \
  "s_subb_u32 s87, s87, 0\n\t" \
  "s_waitcnt vmcnt(4)\n\t" \
  "s_barrier\n\t" \
  "s_waitcnt lgkmcnt(3)\n\t" \
  "v_mfma_f32_16x16x32_bf16 v[0:3], v[64:67], v[32:35], v[0:3]\n\t" \
  "ds_read_b64_tr_b16 v[72:73], v82 offset:40960\n\t" \
  "v_mfma_f32_16x16x32_bf16 v[4:7], v[68:71], v[32:35], v[4:7]\n\t" \
  "ds_read_b64_tr_b16 v[74:75], v83 offset:40960\n\t" \
  "ds_read_b128 v[32:35], v81 offset:0\n\t" \
  "v_mfma_f32_16x16x32_bf16 v[8:11], v[64:67], v[36:39], v[8:11]\n\t" \
  "ds_read_b64_tr_b16 v[76:77], v84 offset:40960\n\t" \
  "v_mfma_f32_16x16x32_bf16 v[12:15], v[68:71], v[36:39], v[12:15]\n\t" \
  "ds_read_b64_tr_b16 v[78:79], v85 offset:40960\n\t" \
  "ds_read_b128 v[36:39], v81 offset:1024\n\t" \
  "s_add_u32 m0, %[lw], 0\n\t" \
  "v_mfma_f32_16x16x32_bf16 v[16:19], v[64:67], v[40:43], v[16:19]\n\t" \
  "global_load_lds_dwordx4 v90, s[84:85] sc1\n\t" \
  "s_add_u32 s84, s84, s88\n\t" \
  "s_addc_u32 s85, s85, 0\n\t" \
  "v_mfma_f32_16x16x32_bf16 v[20:23], v[68:71], v[40:43], v[20:23]\n\t" \
  "ds_read_b128 v[40:43], v81 offset:2048\n\t" \
  "v_mfma_f32_16x16x32_bf16 v[24:27], v[64:67], v[44:47], v[24:27]\n\t" \
  "s_add_u32 m0, %[lw], 16384\n\t" \
  "v_mfma_f32_16x16x32_bf16 v[28:31], v[68:71], v[44:47], v[28:31]\n\t" \
  "ds_read_b128 v[44:47], v81 offset:3072\n\t" \
  "global_load_lds_dwordx4 v91, s[86:87]\n\t" \
  "s_add_u32 s86, s86, s89\n\t" \
  "s_addc_u32 s87, s87, 0\n\t" \
  "s_waitcnt lgkmcnt(3)\n\t" \
  "v_mfma_f32_16x16x32_bf16 v[0:3], v[72:75], v[48:51], v[0:3]\n\t" \
  "ds_read_b64_tr_b16 v[64:65], v86 offset:0\n\t" \
  "v_mfma_f32_16x16x32_bf16 v[4:7], v[76:79], v[48:51], v[4:7]\n\t" \
  "ds_read_b64_tr_b16 v[66:67], v87 offset:0\n\t" \
  "ds_read_b128 v[48:51], v81 offset:8192\n\t" \
  "v_mfma_f32_16x16x32_bf16 v[8:11], v[72:75], v[52:55], v[8:11]\n\t" \
  "ds_read_b64_tr_b16 v[68:69], v88 offset:0\n\t" \
  "v_mfma_f32_16x16x32_bf16 v[12:15], v[76:79], v[52:55], v[12:15]\n\t" \
  "ds_read_b64_tr_b16 v[70:71], v89 offset:0\n\t" \
  "ds_read_b128 v[52:55], v81 offset:9216\n\t" \
  "s_add_u32 m0, %[lw], 8192\n\t" \
  "v_mfma_f32_16x16x32_bf16 v[16:19], v[72:75], v[56:59], v[16:19]\n\t" \
  "global_load_lds_dwordx4 v90, s[84:85] sc1\n\t" \
  "s_add_u32 s84, s84, s88\n\t" \
  "s_addc_u32 s85, s85, 0\n\t" \
  "v_mfma_f32_16x16x32_bf16 v[20:23], v[76:79], v[56:59], v[20:23]\n\t" \
  "ds_read_b128 v[56:59], v81 offset:10240\n\t" \
  "v_mfma_f32_16x16x32_bf16 v[24:27], v[72:75], v[60:63], v[24:27]\n\t" \
  "s_add_u32 m0, %[lw], 24576\n\t" \
  "v_mfma_f32_16x16x32_bf16 v[28:31], v[76:79], v[60:63], v[28:31]\n\t" \
  "ds_read_b128 v[60:63], v81 offset:11264\n\t" \
  "global_load_lds_dwordx4 v91, s[86:87]\n\t" \
  "s_add_u32 s86, s86, s89\n\t" \
  "s_addc_u32 s87, s87, 0\n\t" \
  "s_waitcnt vmcnt(4)\n\t" \
  "s_barrier\n\t" \
  "s_waitcnt lgkmcnt(3)\n\t" \
  "v_mfma_f32_16x16x32_bf16 v[0:3], v[64:67], v[32:35], v[0:3]\n\t" \
  "ds_read_b64_tr_b16 v[72:73], v86 offset:8192\n\t" \
  "v_mfma_f32_16x16x32_bf16 v[4:7], v[68:71], v[32:35], v[4:7]\n\t" \
  "ds_read_b64_tr_b16 v[74:75], v87 offset:8192\n\t" \
  "ds_read_b128 v[32:35], v81 offset:32768\n\t" \
  "v_mfma_f32_16x16x32_bf16 v[8:11], v[64:67], v[36:39], v[8:11]\n\t" \
  "ds_read_b64_tr_b16 v[76:77], v88 offset:8192\n\t" \
  "v_mfma_f32_16x16x32_bf16 v[12:15], v[68:71], v[36:39], v[12:15]\n\t" \
  "ds_read_b64_tr_b16 v[78:79], v89 offset:8192\n\t" \
  "ds_read_b128 v[36:39], v81 offset:33792\n\t" \
  "s_add_u32 m0, %[lw], 32768\n\t" \
  "v_mfma_f32_16x16x32_bf16 v[16:19], v[64:67], v[40:43], v[16:19]\n\t" \
  "global_load_lds_dwordx4 v90, s[84:85] sc1\n\t" \
  "s_add_u32 s84, s84, s88\n\t" \
  "s_addc_u32 s85, s85, 0\n\t" \
  "v_mfma_f32_16x16x32_bf16 v[20:23], v[68:71], v[40:43], v[20:23]\n\t" \
  "ds_read_b128 v[40:43], v81 offset:34816\n\t" \
  "v_mfma_f32_16x16x32_bf16 v[24:27], v[64:67], v[44:47], v[24:27]\n\t" \
  "s_add_u32 m0, %[lw], 49152\n\t" \
  "v_mfma_f32_16x16x32_bf16 v[28:31], v[68:71], v[44:47], v[28:31]\n\t" \
  "ds_read_b128 v[44:47], v81 offset:35840\n\t" \
  "global_load_lds_dwordx4 v91, s[86:87]\n\t" \
  "s_add_u32 s86, s86, s89\n\t" \
  "s_addc_u32 s87, s87, 0\n\t" \
  "s_waitcnt lgkmcnt(3)\n\t" \
  "v_mfma_f32_16x16x32_bf16 v[0:3], v[72:75], v[48:51], v[0:3]\n\t" \
  "ds_read_b64_tr_b16 v[64:65], v86 offset:32768\n\t" \
  "v_mfma_f32_16x16x32_bf16 v[4:7], v[76:79], v[48:51], v[4:7]\n\t" \
  "ds_read_b64_tr_b16 v[66:67], v87 offset:32768\n\t" \
  "ds_read_b128 v[48:51], v81 offset:40960\n\t" \
  "v_mfma_f32_16x16x32_bf16 v[8:11], v[72:75], v[52:55], v[8:11]\n\t" \
  "ds_read_b64_tr_b16 v[68:69], v88 offset:32768\n\t" \
  "v_mfma_f32_16x16x32_bf16 v[12:15], v[76:79], v[52:55], v[12:15]\n\t" \
  "ds_read_b64_tr_b16 v[70:71], v89 offset:32768\n\t" \
  "ds_read_b128 v[52:55], v81 offset:41984\n\t" \
  "s_add_u32 m0, %[lw], 40960\n\t" \
  "v_mfma_f32_16x16x32_bf16 v[16:19], v[72:75], v[56:59], v[16:19]\n\t" \
  "global_load_lds_dwordx4 v90, s[84:85] sc1\n\t" \
  "s_add_u32 s84, s84, s88\n\t" \
  "s_addc_u32 s85, s85, 0\n\t" \
  "v_mfma_f32_16x16x32_bf16 v[20:23], v[76:79], v[56:59], v[20:23]\n\t" \
  "ds_read_b128 v[56:59], v81 offset:43008\n\t" \
  "v_mfma_f32_16x16x32_bf16 v[24:27], v[72:75], v[60:63], v[24:27]\n\t" \
  "s_add_u32 m0, %[lw], 57344\n\t" \
  "v_mfma_f32_16x16x32_bf16 v[28:31], v[76:79], v[60:63], v[28:31]\n\t" \
  "ds_read_b128 v[60:63], v81 offset:44032\n\t" \
  "global_load_lds_dwordx4 v91, s[86:87]\n\t" \
  "s_add_u32 s86, s86, s89\n\t" \
  "s_addc_u32 s87, s87, 0\n\t" \
  "s_waitcnt vmcnt(4)\n\t" \
  "s_barrier\n\t" \
  "s_waitcnt lgkmcnt(3)\n\t" \
  "v_mfma_f32_16x16x32_bf16 v[0:3], v[64:67], v[32:35], v[0:3]\n\t" \
  "ds_read_b64_tr_b16 v[72:73], v86 offset:40960\n\t" \
  "v_mfma_f32_16x16x32_bf16 v[4:7], v[68:71], v[32:35], v[4:7]\n\t" \
  "ds_read_b64_tr_b16 v[74:75], v87 offset:40960\n\t" \
  "ds_read_b128 v[32:35], v80 offset:0\n\t" \
  "v_mfma_f32_16x16x32_bf16 v[8:11], v[64:67], v[36:39], v[8:11]\n\t" \
  "ds_read_b64_tr_b16 v[76:77], v88 offset:40960\n\t" \
  "v_mfma_f32_16x16x32_bf16 v[12:15], v[68:71], v[36:39], v[12:15]\n\t" \
  "ds_read_b64_tr_b16 v[78:79], v89 offset:40960\n\t" \
  "ds_read_b128 v[36:39], v80 offset:1024\n\t" \
  "s_add_u32 m0, %[lw], 65536\n\t" \
  "v_mfma_f32_16x16x32_bf16 v[16:19], v[64:67], v[40:43], v[16:19]\n\t" \
  "global_load_lds_dwordx4 v90, s[84:85] sc1\n\t" \
  "s_add_u32 s84, s84, s88\n\t" \
  "s_addc_u32 s85, s85, 0\n\t" \
  "v_mfma_f32_16x16x32_bf16 v[20:23], v[68:71], v[40:43], v[20:23]\n\t" \
  "ds_read_b128 v[40:43], v80 offset:2048\n\t" \
  "v_mfma_f32_16x16x32_bf16 v[24:27], v[64:67], v[44:47], v[24:27]\n\t" \
  "s_add_u32 m0, %[lw], 81920\n\t" \
  "v_mfma_f32_16x16x32_bf16 v[28:31], v[68:71], v[44:47], v[28:31]\n\t" \
  "ds_read_b128 v[44:47], v80 offset:3072\n\t" \
  "global_load_lds_dwordx4 v91, s[86:87]\n\t" \
  "s_add_u32 s86, s86, s89\n\t" \
  "s_addc_u32 s87, s87, 0\n\t" \
  "s_waitcnt lgkmcnt(3)\n\t" \
  "v_mfma_f32_16x16x32_bf16 v[0:3], v[72:75], v[48:51], v[0:3]\n\t" \
  "ds_read_b64_tr_b16 v[64:65], v82 offset:0\n\t" \
  "v_mfma_f32_16x16x32_bf16 v[4:7], v[76:79], v[48:51], v[4:7]\n\t" \
  "ds_read_b64_tr_b16 v[66:67], v83 offset:0\n\t" \
  "ds_read_b128 v[48:51], v80 offset:8192\n\t" \
  "v_mfma_f32_16x16x32_bf16 v[8:11], v[72:75], v[52:55], v[8:11]\n\t" \
  "ds_read_b64_tr_b16 v[68:69], v84 offset:0\n\t" \
  "v_mfma_f32_16x16x32_bf16 v[12:15], v[76:79], v[52:55], v[12:15]\n\t" \
  "ds_read_b64_tr_b16 v[70:71], v85 offset:0\n\t" \
  "ds_read_b128 v[52:55], v80 offset:9216\n\t" \
  "s_add_u32 m0, %[lw], 73728\n\t" \
  "v_mfma_f32_16x16x32_bf16 v[16:19], v[72:75], v[56:59], v[16:19]\n\t" \
  "global_load_lds_dwordx4 v90, s[84:85] sc1\n\t" \
  "s_add_u32 s84, s84, s88\n\t" \
  "s_addc_u32 s85, s85, 0\n\t" \
  "v_mfma_f32_16x16x32_bf16 v[20:23], v[76:79], v[56:59], v[20:23]\n\t" \
  "ds_read_b128 v[56:59], v80 offset:10240\n\t" \
  "v_mfma_f32_16x16x32_bf16 v[24:27], v[72:75], v[60:63], v[24:27]\n\t" \
  "s_add_u32 m0, %[lw], 90112\n\t" \
  "v_mfma_f32_16x16x32_bf16 v[28:31], v[76:79], v[60:63], v[28:31]\n\t" \
  "ds_read_b128 v[60:63], v80 offset:11264\n\t" \
  "global_load_lds_dwordx4 v91, s[86:87]\n\t" \
  "s_add_u32 s86, s86, s89\n\t" \
  "s_addc_u32 s87, s87, 0\n\t" \
  "s_cmp_lg_u32 s88, 0\n\t" \
  "s_cbranch_scc1 .Lp1%=\n\t" \
  ".Le%=:\n\t" \
  "s_waitcnt vmcnt(0) lgkmcnt(0)\n\t" \
  "s_mov_b32 m0, s94\n\t" \
  "s_setprio 0\n\t" \
  "s_nop 7\n\t"
#define HSL 16
__device__ __forceinline__ void job_down(const Args& p, int rblk, int ct, char* smem, __amdgpu_buffer_rsrc_t rsy) {
  if (threadIdx.x < 64) {
    for (unsigned spins = 0;;) {
      asm volatile("" ::: "memory");
      if (__builtin_amdgcn_readfirstlane((int)ld_flag0(rsy, SY_CH + (rblk >> 1) * 128)) >= 27) break;
      __builtin_amdgcn_s_sleep(HSL);
      if (spin_fail(spins, (gu32*)(p.ws + WS_SY + SY_ABORT))) break;
    }
  }
  int tid = threadIdx.x;
  asm volatile("" : "+v"(tid));
  const int lane = tid & 63, r = lane & 15, q = lane >> 4;
  const int wid = __builtin_amdgcn_readfirstlane(tid >> 6), wr = wid >> 2, wc = wid & 3;
  typedef __attribute__((ext_vector_type(32))) float f32x32;
  typedef __attribute__((ext_vector_type(8))) unsigned u32x8;
  f32x32 acc;
  {
    const char* A = p.ws + WS_HF + h_off(p, rblk);
    const char* B = (const char*)(p.wd + ct * 128);
    const unsigned rb = tid >> 4, va = tid * 16, vb = ((rb & 4) * 4 + (rb >> 3) * 4 + (rb & 3)) * (DM * 2) + 16 * ((tid & 15) ^ swzB(rb & 15));
    u32x8 lb;
#pragma unroll
    for (int i = 0; i < 8; ++i)
      lb[i] = (i >> 2) * 65536 + 16384 + offB(8 * q + 4 * (i & 1) + (r >> 2), 2 * (wc * 2 + ((i >> 1) & 1)) + ((r & 3) >> 1)) + 8 * (r & 1);
    LBAR();
#pragma unroll
    for (int t = 0; t < 3; ++t) {
      glds2<true>(A + t * 32768, 16384, va, wid * 1024 + t * 32768);
      glds2(B + t * 64 * DM * 2, 32 * DM * 2, vb, wid * 1024 + t * 32768 + 16384);
    }
    WAIT_V(8);
    LBAR();
    asm volatile(DN_ASM
        : "={v[0:31]}"(acc)
        : "{v[80:81]}"((u32x2){wr * 4096u + lane * 16u, 65536u + wr * 4096u + lane * 16u}), "{v[82:89]}"(lb), "{v90}"(va), "{v91}"(vb),
          [pa] "s"(A + 3 * 32768), [pb] "s"(B + 3 * 64 * DM * 2), [lw] "s"(wid * 1024), [hf] "s"(wr)
        : CLB8(32, 33, 34, 35, 36, 37, 38, 39), CLB8(40, 41, 42, 43, 44, 45, 46, 47), CLB8(48, 49, 50, 51, 52, 53, 54, 55),
          CLB8(56, 57, 58, 59, 60, 61, 62, 63), CLB8(64, 65, 66, 67, 68, 69, 70, 71), CLB8(72, 73, 74, 75, 76, 77, 78, 79),
          "s84", "s85", "s86", "s87", "s88", "s89", "s90", "s91", "s94", "scc", "memory");
  }
  wait_s(p, rsy, rblk, ct * 4, 4, rblk < NFUSE);
  const int e0 = (rblk * 128 + wr * 64 + r) * DM + ct * 128 + wc * 32 + q * 8;
  SX sx[4];
#pragma unroll
  for (int m = 0; m < 4; ++m) sx[m] = get_sx(p, rblk, e0 + m * 16 * DM);
  const __amdgpu_buffer_rsrc_t ro = mkrsrc(p.ws + WS_G, S_LEN * DM * 2);
#pragma unroll
  for (int m = 0; m < 4; ++m) {
    float rr[8];
#pragma unroll
    for (int j = 0; j < 8; ++j) { float d = rbf(acc[8 * m + j]); rr[j] = d * __builtin_amdgcn_rcpf(1.0f + __expf(-d)); }
    put_g(p, ro, rblk, e0 + m * 16 * DM, widen32(cvtpk(rr[0], rr[1]), cvtpk(rr[2], rr[3]), cvtpk(rr[4], rr[5]), cvtpk(rr[6], rr[7])), sx[m]);
  }
  if (rblk >= NFUSE) publish_flags(rsy, SY_FG + (rblk * 8 + ct) * 128, 0, 1);
}

#define PR_ASM_0 \
  "s_cmp_lg_u32 %[kh], 0\n\t" \
  "s_cbranch_scc1 .Lk%=\n\t" \
  "s_mov_b32 s94, m0\n\t" \
  "s_mov_b64 s[92:93], %[x0]\n\t" \
  "s_mov_b32 s86, %[g0]\n\t" \
  "s_add_u32 s87, s86, %[t0]\n\t" \
  "s_sub_i32 s88, s86, 3\n\t" \
  "ds_read_b128 v[176:179], v208 offset:0\n\t" \
  "ds_read_b128 v[180:183], v208 offset:16384\n\t" \
  "ds_read_b128 v[184:187], v209 offset:0\n\t" \
  "ds_read_b128 v[188:191], v209 offset:16384\n\t" \
  "ds_read_b128 v[192:195], v210 offset:0\n\t" \
  "ds_read_b128 v[196:199], v210 offset:16384\n\t" \
  "ds_read_b128 v[200:203], v211 offset:0\n\t" \
  "ds_read_b128 v[204:207], v211 offset:16384\n\t" \
  "s_branch .Lfb00%=\n\t" \
  ".Lp0%=:\n\t" \
  "s_cmp_le_u32 s86, 5\n\t" \
  "s_cbranch_scc1 .Lf00%=\n\t" \
  ".Lfb00%=:\n\t" \
  "s_waitcnt lgkmcnt(6)\n\t" \
  "v_mfma_f32_16x16x32_bf16 v[128:131], v[0:3], v[176:179], 0\n\t" \
  "v_mfma_f32_16x16x32_bf16 v[132:135], v[4:7], v[176:179], 0\n\t" \
  "ds_read_b128 v[176:179], v208 offset:256\n\t" \
  "s_add_u32 m0, %[lw], 67584\n\t" \
  "v_mfma_f32_16x16x32_bf16 v[136:139], v[0:3], v[180:183], 0\n\t" \
  "v_mfma_f32_16x16x32_bf16 v[140:143], v[4:7], v[180:183], 0\n\t" \
  "ds_read_b128 v[180:183], v208 offset:16640\n\t" \
  "global_load_lds_dwordx4 v218, s[92:93] sc1\n\t" \
  "s_waitcnt lgkmcnt(6)\n\t" \
  "v_mfma_f32_16x16x32_bf16 v[128:131], v[8:11], v[184:187], v[128:131]\n\t" \
  "v_mfma_f32_16x16x32_bf16 v[132:135], v[12:15], v[184:187], v[132:135]\n\t" \
  "ds_read_b128 v[184:187], v209 offset:256\n\t" \
  "v_mfma_f32_16x16x32_bf16 v[136:139], v[8:11], v[188:191], v[136:139]\n\t" \
  "v_mfma_f32_16x16x32_bf16 v[140:143], v[12:15], v[188:191], v[140:143]\n\t" \
  "ds_read_b128 v[188:191], v209 offset:16640\n\t" \
  "ds_write_b128 v220, v[152:155] offset:0\n\t" \
  "ds_write_b128 v220, v[156:159] offset:1024\n\t" \
  "s_waitcnt lgkmcnt(8)\n\t" \
  "v_mfma_f32_16x16x32_bf16 v[128:131], v[16:19], v[192:195], v[128:131]\n\t" \
  "v_mfma_f32_16x16x32_bf16 v[132:135], v[20:23], v[192:195], v[132:135]\n\t" \
  "ds_read_b128 v[192:195], v210 offset:256\n\t" \
  "s_add_u32 m0, %[lw], 68608\n\t" \
  "v_mfma_f32_16x16x32_bf16 v[136:139], v[16:19], v[196:199], v[136:139]\n\t" \
  "v_mfma_f32_16x16x32_bf16 v[140:143], v[20:23], v[196:199], v[140:143]\n\t" \
  "ds_read_b128 v[196:199], v210 offset:16640\n\t" \
  "global_load_lds_dwordx4 v219, s[92:93] sc1\n\t" \
  "s_lshr_b32 s90, s87, 7\n\t" \
  "s_xor_b32 s90, s90, %[s0]\n\t" \
  "s_and_b32 s90, s90, 1\n\t" \
  "s_lshl1_add_u32 s90, s87, s90\n\t" \
  "s_lshl_b32 s90, s90, 13\n\t" \
  "s_cmp_lt_i32 s86, 6\n\t" \
  "s_cselect_b32 s90, 0x70000000, s90\n\t" \
  "s_waitcnt lgkmcnt(8)\n\t" \
  "v_mfma_f32_16x16x32_bf16 v[128:131], v[24:27], v[200:203], v[128:131]\n\t" \
  "v_mfma_f32_16x16x32_bf16 v[132:135], v[28:31], v[200:203], v[132:135]\n\t" \
  "ds_read_b128 v[200:203], v211 offset:256\n\t" \
  "v_mfma_f32_16x16x32_bf16 v[136:139], v[24:27], v[204:207], v[136:139]\n\t" \
  "v_mfma_f32_16x16x32_bf16 v[140:143], v[28:31], v[204:207], v[140:143]\n\t" \
  "ds_read_b128 v[204:207], v211 offset:16640\n\t" \
  "s_waitcnt lgkmcnt(4)\n\t" \
  "s_cmp_lt_u32 s86, 6\n\t" \
  "s_cbranch_scc1 .Lw00%=\n\t" \
  "s_waitcnt vmcnt(4)\n\t" \
  "s_barrier\n\t" \
  ".Lg00%=:\n\t" \
  "s_add_u32 s92, s92, 512\n\t" \
  "s_addc_u32 s93, s93, 0\n\t" \
  "v_mfma_f32_16x16x32_bf16 v[128:131], v[32:35], v[176:179], v[128:131]\n\t" \
  "v_mfma_f32_16x16x32_bf16 v[132:135], v[36:39], v[176:179], v[132:135]\n\t" \
  "ds_read_b128 v[176:179], v208 offset:32768\n\t" \
  "s_add_u32 m0, %[lw], 98304\n\t" \
  "v_mfma_f32_16x16x32_bf16 v[136:139], v[32:35], v[180:183], v[136:139]\n\t" \
  "v_mfma_f32_16x16x32_bf16 v[140:143], v[36:39], v[180:183], v[140:143]\n\t" \
  "ds_read_b128 v[180:183], v208 offset:49152\n\t" \
  "global_load_lds_dwordx4 v216, s[92:93] sc1\n\t" \
  "ds_read_b128 v[160:163], v221 offset:0\n\t" \
  "ds_read_b128 v[164:167], v221 offset:1024\n\t" \
  "v_mfma_f32_16x16x32_bf16 v[128:131], v[40:43], v[184:187], v[128:131]\n\t" \
  "v_mfma_f32_16x16x32_bf16 v[132:135], v[44:47], v[184:187], v[132:135]\n\t" \
  "ds_read_b128 v[184:187], v209 offset:32768\n\t" \
  "v_mfma_f32_16x16x32_bf16 v[136:139], v[40:43], v[188:191], v[136:139]\n\t" \
  "v_mfma_f32_16x16x32_bf16 v[140:143], v[44:47], v[188:191], v[140:143]\n\t" \
  "ds_read_b128 v[188:191], v209 offset:49152\n\t" \
  "s_waitcnt lgkmcnt(8)\n\t" \
  "v_mfma_f32_16x16x32_bf16 v[128:131], v[48:51], v[192:195], v[128:131]\n\t" \
  "v_mfma_f32_16x16x32_bf16 v[132:135], v[52:55], v[192:195], v[132:135]\n\t" \
  "ds_read_b128 v[192:195], v210 offset:32768\n\t" \
  "s_add_u32 m0, %[lw], 99328\n\t" \
  "v_mfma_f32_16x16x32_bf16 v[136:139], v[48:51], v[196:199], v[136:139]\n\t" \
  "v_mfma_f32_16x16x32_bf16 v[140:143], v[52:55], v[196:199], v[140:143]\n\t" \
  "ds_read_b128 v[196:199], v210 offset:49152\n\t" \
  "global_load_lds_dwordx4 v217, s[92:93] sc1\n\t" \
  "s_waitcnt lgkmcnt(4)\n\t" \
  "v_add_f32 v160, v160, v144\n\t" \
  "v_add_f32 v161, v161, v145\n\t" \
  "v_add_f32 v162, v162, v146\n\t" \
  "v_add_f32 v163, v163, v147\n\t" \
  "v_add_f32 v164, v164, v148\n\t" \
  "v_add_f32 v165, v165, v149\n\t" \
  "v_add_f32 v166, v166, v150\n\t" \
  "v_add_f32 v167, v167, v151\n\t" \
  "v_mfma_f32_16x16x32_bf16 v[128:131], v[56:59], v[200:203], v[128:131]\n\t" \
  "v_mfma_f32_16x16x32_bf16 v[132:135], v[60:63], v[200:203], v[132:135]\n\t" \
  "ds_read_b128 v[200:203], v211 offset:32768\n\t" \
  "v_mfma_f32_16x16x32_bf16 v[136:139], v[56:59], v[204:207], v[136:139]\n\t" \
  "v_mfma_f32_16x16x32_bf16 v[140:143], v[60:63], v[204:207], v[140:143]\n\t" \
  "ds_read_b128 v[204:207], v211 offset:49152\n\t" \
  "v_cvt_pk_bf16_f32 v168, v160, v161\n\t" \
  "v_cvt_pk_bf16_f32 v169, v162, v163\n\t" \
  "v_cvt_pk_bf16_f32 v170, v164, v165\n\t" \
  "v_cvt_pk_bf16_f32 v171, v166, v167\n\t" \
  "buffer_store_dwordx4 v[168:171], v172, %[rt], s90 offen sc1\n\t" \
  "v_mfma_f32_16x16x32_bf16 v[128:131], v[64:67], v[176:179], v[128:131]\n\t" \
  "v_mfma_f32_16x16x32_bf16 v[132:135], v[68:71], v[176:179], v[132:135]\n\t" \
  "ds_read_b128 v[176:179], v208 offset:33024\n\t" \
  "s_add_u32 m0, %[lw], 100352\n\t" \
  "v_mfma_f32_16x16x32_bf16 v[136:139], v[64:67], v[180:183], v[136:139]\n\t" \
  "v_mfma_f32_16x16x32_bf16 v[140:143], v[68:71], v[180:183], v[140:143]\n\t" \
  "ds_read_b128 v[180:183], v208 offset:49408\n\t" \
  "global_load_lds_dwordx4 v218, s[92:93] sc1\n\t" \
  "s_waitcnt lgkmcnt(6)\n\t" \
  "v_mfma_f32_16x16x32_bf16 v[128:131], v[72:75], v[184:187], v[128:131]\n\t" \
  "v_mfma_f32_16x16x32_bf16 v[132:135], v[76:79], v[184:187], v[132:135]\n\t" \
  "ds_read_b128 v[184:187], v209 offset:33024\n\t" \
  "v_mfma_f32_16x16x32_bf16 v[136:139], v[72:75], v[188:191], v[136:139]\n\t" \
  "v_mfma_f32_16x16x32_bf16 v[140:143], v[76:79], v[188:191], v[140:143]\n\t" \
  "ds_read_b128 v[188:191], v209 offset:49408\n\t" \
  "s_waitcnt lgkmcnt(6)\n\t" \
  "v_mfma_f32_16x16x32_bf16 v[128:131], v[80:83], v[192:195], v[128:131]\n\t" \
  "v_mfma_f32_16x16x32_bf16 v[132:135], v[84:87], v[192:195], v[132:135]\n\t" \
  "ds_read_b128 v[192:195], v210 offset:33024\n\t" \
  "s_add_u32 m0, %[lw], 101376\n\t" \
  "v_mfma_f32_16x16x32_bf16 v[136:139], v[80:83], v[196:199], v[136:139]\n\t" \
  "v_mfma_f32_16x16x32_bf16 v[140:143], v[84:87], v[196:199], v[140:143]\n\t" \
  "ds_read_b128 v[196:199], v210 offset:49408\n\t" \
  "global_load_lds_dwordx4 v219, s[92:93] sc1\n\t" \
  "s_waitcnt lgkmcnt(6)\n\t" \
  "v_mfma_f32_16x16x32_bf16 v[128:131], v[88:91], v[200:203], v[128:131]\n\t" \
  "v_mfma_f32_16x16x32_bf16 v[132:135], v[92:95], v[200:203], v[132:135]\n\t" \
  "ds_read_b128 v[200:203], v211 offset:33024\n\t" \
  "v_mfma_f32_16x16x32_bf16 v[136:139], v[88:91], v[204:207], v[136:139]\n\t" \
  "v_mfma_f32_16x16x32_bf16 v[140:143], v[92:95], v[204:207], v[140:143]\n\t" \
  "ds_read_b128 v[204:207], v211 offset:49408\n\t" \
  "s_cmp_lt_u32 s86, 8\n\t" \
  "s_cbranch_scc1 .Lw10%=\n\t" \
  "s_waitcnt vmcnt(5)\n\t" \
  "s_barrier\n\t" \
  ".Lg10%=:\n\t" \
  "s_add_u32 s92, s92, 65024\n\t" \
  "s_addc_u32 s93, s93, 0\n\t" \
  "s_waitcnt lgkmcnt(6)\n\t" \
  "v_mfma_f32_16x16x32_bf16 v[128:131], v[96:99], v[176:179], v[128:131]\n\t" \
  "v_mfma_f32_16x16x32_bf16 v[132:135], v[100:103], v[176:179], v[132:135]\n\t" \
  "ds_read_b128 v[176:179], v212 offset:0\n\t" \
  "s_add_u32 m0, %[lw], 0\n\t" \
  "v_mfma_f32_16x16x32_bf16 v[136:139], v[96:99], v[180:183], v[136:139]\n\t" \
  "v_mfma_f32_16x16x32_bf16 v[140:143], v[100:103], v[180:183], v[140:143]\n\t" \
  "ds_read_b128 v[180:183], v212 offset:16384\n\t" \
  "global_load_lds_dwordx4 v216, s[92:93] sc1\n\t" \
  "s_waitcnt lgkmcnt(6)\n\t" \
  "v_mfma_f32_16x16x32_bf16 v[128:131], v[104:107], v[184:187], v[128:131]\n\t" \
  "v_mfma_f32_16x16x32_bf16 v[132:135], v[108:111], v[184:187], v[132:135]\n\t" \
  "ds_read_b128 v[184:187], v213 offset:0\n\t" \
  "v_mfma_f32_16x16x32_bf16 v[136:139], v[104:107], v[188:191], v[136:139]\n\t" \
  "v_mfma_f32_16x16x32_bf16 v[140:143], v[108:111], v[188:191], v[140:143]\n\t" \
  "ds_read_b128 v[188:191], v213 offset:16384\n\t" \
  "s_waitcnt lgkmcnt(6)\n\t" \
  "v_mfma_f32_16x16x32_bf16 v[128:131], v[112:115], v[192:195], v[128:131]\n\t" \
  "v_mfma_f32_16x16x32_bf16 v[132:135], v[116:119], v[192:195], v[132:135]\n\t" \
  "ds_read_b128 v[192:195], v214 offset:0\n\t" \
  "s_add_u32 m0, %[lw], 1024\n\t" \
  "v_mfma_f32_16x16x32_bf16 v[136:139], v[112:115], v[196:199], v[136:139]\n\t" \
  "v_mfma_f32_16x16x32_bf16 v[140:143], v[116:119], v[196:199], v[140:143]\n\t" \
  "ds_read_b128 v[196:199], v214 offset:16384\n\t" \
  "global_load_lds_dwordx4 v217, s[92:93] sc1\n\t" \
  "s_waitcnt lgkmcnt(6)\n\t" \
  "v_mfma_f32_16x16x32_bf16 v[128:131], v[120:123], v[200:203], v[128:131]\n\t" \
  "v_mfma_f32_16x16x32_bf16 v[132:135], v[124:127], v[200:203], v[132:135]\n\t" \
  "ds_read_b128 v[200:203], v215 offset:0\n\t" \
  "v_mfma_f32_16x16x32_bf16 v[136:139], v[120:123], v[204:207], v[136:139]\n\t" \
  "v_mfma_f32_16x16x32_bf16 v[140:143], v[124:127], v[204:207], v[140:143]\n\t" \
  "ds_read_b128 v[204:207], v215 offset:16384\n\t" \
  "s_add_u32 s86, s86, 1\n\t" \
  "s_add_u32 s87, s87, 1\n\t" \
  "s_cmp_le_u32 s86, 5\n\t" \
  "s_cbranch_scc1 .Lf10%=\n\t" \
  ".Lfb10%=:\n\t" \
  "s_waitcnt lgkmcnt(6)\n\t" \
  "v_mfma_f32_16x16x32_bf16 v[144:147], v[0:3], v[176:179], 0\n\t" \
  "v_mfma_f32_16x16x32_bf16 v[148:151], v[4:7], v[176:179], 0\n\t" \
  "ds_read_b128 v[176:179], v212 offset:256\n\t" \
  "s_add_u32 m0, %[lw], 2048\n\t" \
  "v_mfma_f32_16x16x32_bf16 v[152:155], v[0:3], v[180:183], 0\n\t" \
  "v_mfma_f32_16x16x32_bf16 v[156:159], v[4:7], v[180:183], 0\n\t" \
  "ds_read_b128 v[180:183], v212 offset:16640\n\t" \
  "global_load_lds_dwordx4 v218, s[92:93] sc1\n\t" \
  "s_waitcnt lgkmcnt(6)\n\t" \
  "v_mfma_f32_16x16x32_bf16 v[144:147], v[8:11], v[184:187], v[144:147]\n\t" \
  "v_mfma_f32_16x16x32_bf16 v[148:151], v[12:15], v[184:187], v[148:151]\n\t" \
  "ds_read_b128 v[184:187], v213 offset:256\n\t" \
  "v_mfma_f32_16x16x32_bf16 v[152:155], v[8:11], v[188:191], v[152:155]\n\t" \
  "v_mfma_f32_16x16x32_bf16 v[156:159], v[12:15], v[188:191], v[156:159]\n\t" \
  "ds_read_b128 v[188:191], v213 offset:16640\n\t" \
  "ds_write_b128 v220, v[136:139] offset:0\n\t" \
  "ds_write_b128 v220, v[140:143] offset:1024\n\t" \
  "s_waitcnt lgkmcnt(8)\n\t" \
  "v_mfma_f32_16x16x32_bf16 v[144:147], v[16:19], v[192:195], v[144:147]\n\t" \
  "v_mfma_f32_16x16x32_bf16 v[148:151], v[20:23], v[192:195], v[148:151]\n\t" \
  "ds_read_b128 v[192:195], v214 offset:256\n\t" \
  "s_add_u32 m0, %[lw], 3072\n\t" \
  "v_mfma_f32_16x16x32_bf16 v[152:155], v[16:19], v[196:199], v[152:155]\n\t" \
  "v_mfma_f32_16x16x32_bf16 v[156:159], v[20:23], v[196:199], v[156:159]\n\t" \
  "ds_read_b128 v[196:199], v214 offset:16640\n\t" \
  "global_load_lds_dwordx4 v219, s[92:93] sc1\n\t" \
  "s_lshr_b32 s90, s87, 7\n\t" \
  "s_xor_b32 s90, s90, %[s0]\n\t" \
  "s_and_b32 s90, s90, 1\n\t" \
  "s_lshl1_add_u32 s90, s87, s90\n\t" \
  "s_lshl_b32 s90, s90, 13\n\t" \
  "s_cmp_lt_i32 s86, 6\n\t" \
  "s_cselect_b32 s90, 0x70000000, s90\n\t" \
  "s_waitcnt lgkmcnt(8)\n\t" \
  "v_mfma_f32_16x16x32_bf16 v[144:147], v[24:27], v[200:203], v[144:147]\n\t" \
  "v_mfma_f32_16x16x32_bf16 v[148:151], v[28:31], v[200:203], v[148:151]\n\t" \
  "ds_read_b128 v[200:203], v215 offset:256\n\t" \
  "v_mfma_f32_16x16x32_bf16 v[152:155], v[24:27], v[204:207], v[152:155]\n\t" \
  "v_mfma_f32_16x16x32_bf16 v[156:159], v[28:31], v[204:207], v[156:159]\n\t" \
  "ds_read_b128 v[204:207], v215 offset:16640\n\t" \
  "s_waitcnt lgkmcnt(4)\n\t" \
  "s_cmp_lt_u32 s86, 6\n\t" \
  "s_cbranch_scc1 .Lw20%=\n\t" \
  "s_waitcnt vmcnt(4)\n\t" \
  "s_barrier\n\t" \
  ".Lg20%=:\n\t" \
  "s_add_u32 s92, s92, 512\n\t" \
  "s_addc_u32 s93, s93, 0\n\t" \
  "v_mfma_f32_16x16x32_bf16 v[144:147], v[32:35], v[176:179], v[144:147]\n\t" \
  "v_mfma_f32_16x16x32_bf16 v[148:151], v[36:39], v[176:179], v[148:151]\n\t" \
  "ds_read_b128 v[176:179], v212 offset:32768\n\t" \
  "s_add_u32 m0, %[lw], 32768\n\t" \
  "v_mfma_f32_16x16x32_bf16 v[152:155], v[32:35], v[180:183], v[152:155]\n\t" \
  "v_mfma_f32_16x16x32_bf16 v[156:159], v[36:39], v[180:183], v[156:159]\n\t" \
  "ds_read_b128 v[180:183], v212 offset:49152\n\t" \
  "global_load_lds_dwordx4 v216, s[92:93] sc1\n\t" \
  "ds_read_b128 v[160:163], v221 offset:0\n\t" \
  "ds_read_b128 v[164:167], v221 offset:1024\n\t" \
  "v_mfma_f32_16x16x32_bf16 v[144:147], v[40:43], v[184:187], v[144:147]\n\t" \
  "v_mfma_f32_16x16x32_bf16 v[148:151], v[44:47], v[184:187], v[148:151]\n\t" \
  "ds_read_b128 v[184:187], v213 offset:32768\n\t" \
  "v_mfma_f32_16x16x32_bf16 v[152:155], v[40:43], v[188:191], v[152:155]\n\t" \
  "v_mfma_f32_16x16x32_bf16 v[156:159], v[44:47], v[188:191], v[156:159]\n\t" \
  "ds_read_b128 v[188:191], v213 offset:49152\n\t" \
  "s_waitcnt lgkmcnt(8)\n\t" \
  "v_mfma_f32_16x16x32_bf16 v[144:147], v[48:51], v[192:195], v[144:147]\n\t" \
  "v_mfma_f32_16x16x32_bf16 v[148:151], v[52:55], v[192:195], v[148:151]\n\t" \
  "ds_read_b128 v[192:195], v214 offset:32768\n\t" \
  "s_add_u32 m0, %[lw], 33792\n\t" \
  "v_mfma_f32_16x16x32_bf16 v[152:155], v[48:51], v[196:199], v[152:155]\n\t" \
  "v_mfma_f32_16x16x32_bf16 v[156:159], v[52:55], v[196:199], v[156:159]\n\t" \
  "ds_read_b128 v[196:199], v214 offset:49152\n\t" \
  "global_load_lds_dwordx4 v217, s[92:93] sc1\n\t" \
  "s_waitcnt lgkmcnt(4)\n\t" \
  "v_add_f32 v160, v160, v128\n\t" \
  "v_add_f32 v161, v161, v129\n\t" \
  "v_add_f32 v162, v162, v130\n\t" \
  "v_add_f32 v163, v163, v131\n\t" \
  "v_add_f32 v164, v164, v132\n\t" \
  "v_add_f32 v165, v165, v133\n\t" \
  "v_add_f32 v166, v166, v134\n\t" \
  "v_add_f32 v167, v167, v135\n\t" \
  "v_mfma_f32_16x16x32_bf16 v[144:147], v[56:59], v[200:203], v[144:147]\n\t" \
  "v_mfma_f32_16x16x32_bf16 v[148:151], v[60:63], v[200:203], v[148:151]\n\t" \
  "ds_read_b128 v[200:203], v215 offset:32768\n\t" \
  "v_mfma_f32_16x16x32_bf16 v[152:155], v[56:59], v[204:207], v[152:155]\n\t" \
  "v_mfma_f32_16x16x32_bf16 v[156:159], v[60:63], v[204:207], v[156:159]\n\t" \
  "ds_read_b128 v[204:207], v215 offset:49152\n\t" \
  "v_cvt_pk_bf16_f32 v168, v160, v161\n\t" \
  "v_cvt_pk_bf16_f32 v169, v162, v163\n\t" \
  "v_cvt_pk_bf16_f32 v170, v164, v165\n\t" \
  "v_cvt_pk_bf16_f32 v171, v166, v167\n\t" \
  "buffer_store_dwordx4 v[168:171], v172, %[rt], s90 offen sc1\n\t" \
  "v_mfma_f32_16x16x32_bf16 v[144:147], v[64:67], v[176:179], v[144:147]\n\t" \
  "v_mfma_f32_16x16x32_bf16 v[148:151], v[68:71], v[176:179], v[148:151]\n\t" \
  "ds_read_b128 v[176:179], v212 offset:33024\n\t" \
  "s_add_u32 m0, %[lw], 34816\n\t" \
  "v_mfma_f32_16x16x32_bf16 v[152:155], v[64:67], v[180:183], v[152:155]\n\t" \
  "v_mfma_f32_16x16x32_bf16 v[156:159], v[68:71], v[180:183], v[156:159]\n\t" \
  "ds_read_b128 v[180:183], v212 offset:49408\n\t" \
  "global_load_lds_dwordx4 v218, s[92:93] sc1\n\t" \
  "s_waitcnt lgkmcnt(6)\n\t" \
  "v_mfma_f32_16x16x32_bf16 v[144:147], v[72:75], v[184:187], v[144:147]\n\t" \
  "v_mfma_f32_16x16x32_bf16 v[148:151], v[76:79], v[184:187], v[148:151]\n\t" \
  "ds_read_b128 v[184:187], v213 offset:33024\n\t" \
  "v_mfma_f32_16x16x32_bf16 v[152:155], v[72:75], v[188:191], v[152:155]\n\t" \
  "v_mfma_f32_16x16x32_bf16 v[156:159], v[76:79], v[188:191], v[156:159]\n\t" \
  "ds_read_b128 v[188:191], v213 offset:49408\n\t" \
  "s_waitcnt lgkmcnt(6)\n\t" \
  "v_mfma_f32_16x16x32_bf16 v[144:147], v[80:83], v[192:195], v[144:147]\n\t" \
  "v_mfma_f32_16x16x32_bf16 v[148:151], v[84:87], v[192:195], v[148:151]\n\t" \
  "ds_read_b128 v[192:195], v214 offset:33024\n\t" \
  "s_add_u32 m0, %[lw], 35840\n\t" \
  "v_mfma_f32_16x16x32_bf16 v[152:155], v[80:83], v[196:199], v[152:155]\n\t" \
  "v_mfma_f32_16x16x32_bf16 v[156:159], v[84:87], v[196:199], v[156:159]\n\t" \
  "ds_read_b128 v[196:199], v214 offset:49408\n\t" \
  "global_load_lds_dwordx4 v219, s[92:93] sc1\n\t" \
  "s_waitcnt lgkmcnt(6)\n\t" \
  "v_mfma_f32_16x16x32_bf16 v[144:147], v[88:91], v[200:203], v[144:147]\n\t" \
  "v_mfma_f32_16x16x32_bf16 v[148:151], v[92:95], v[200:203], v[148:151]\n\t" \
  "ds_read_b128 v[200:203], v215 offset:33024\n\t" \
  "v_mfma_f32_16x16x32_bf16 v[152:155], v[88:91], v[204:207], v[152:155]\n\t" \
  "v_mfma_f32_16x16x32_bf16 v[156:159], v[92:95], v[204:207], v[156:159]\n\t" \
  "ds_read_b128 v[204:207], v215 offset:49408\n\t" \
  "s_cmp_lt_u32 s86, 8\n\t" \
  "s_cbranch_scc1 .Lw30%=\n\t" \
  "s_waitcnt vmcnt(5)\n\t" \
  "s_barrier\n\t" \
  ".Lg30%=:\n\t" \
  "s_add_u32 s92, s92, 65024\n\t" \
  "s_addc_u32 s93, s93, 0\n\t" \
  "s_waitcnt lgkmcnt(6)\n\t" \
  "v_mfma_f32_16x16x32_bf16 v[144:147], v[96:99], v[176:179], v[144:147]\n\t" \
  "v_mfma_f32_16x16x32_bf16 v[148:151], v[100:103], v[176:179], v[148:151]\n\t" \
  "ds_read_b128 v[176:179], v208 offset:0\n\t" \
  "s_add_u32 m0, %[lw], 65536\n\t" \
  "v_mfma_f32_16x16x32_bf16 v[152:155], v[96:99], v[180:183], v[152:155]\n\t" \
  "v_mfma_f32_16x16x32_bf16 v[156:159], v[100:103], v[180:183], v[156:159]\n\t" \
  "ds_read_b128 v[180:183], v208 offset:16384\n\t" \
  "global_load_lds_dwordx4 v216, s[92:93] sc1\n\t" \
  "s_waitcnt lgkmcnt(6)\n\t" \
  "v_mfma_f32_16x16x32_bf16 v[144:147], v[104:107], v[184:187], v[144:147]\n\t" \
  "v_mfma_f32_16x16x32_bf16 v[148:151], v[108:111], v[184:187], v[148:151]\n\t" \
  "ds_read_b128 v[184:187], v209 offset:0\n\t" \
  "v_mfma_f32_16x16x32_bf16 v[152:155], v[104:107], v[188:191], v[152:155]\n\t" \
  "v_mfma_f32_16x16x32_bf16 v[156:159], v[108:111], v[188:191], v[156:159]\n\t" \
  "ds_read_b128 v[188:191], v209 offset:16384\n\t" \
  "s_waitcnt lgkmcnt(6)\n\t" \
  "v_mfma_f32_16x16x32_bf16 v[144:147], v[112:115], v[192:195], v[144:147]\n\t" \
  "v_mfma_f32_16x16x32_bf16 v[148:151], v[116:119], v[192:195], v[148:151]\n\t" \
  "ds_read_b128 v[192:195], v210 offset:0\n\t" \
  "s_add_u32 m0, %[lw], 66560\n\t" \
  "v_mfma_f32_16x16x32_bf16 v[152:155], v[112:115], v[196:199], v[152:155]\n\t" \
  "v_mfma_f32_16x16x32_bf16 v[156:159], v[116:119], v[196:199], v[156:159]\n\t" \
  "ds_read_b128 v[196:199], v210 offset:16384\n\t" \
  "global_load_lds_dwordx4 v217, s[92:93] sc1\n\t" \
  "s_waitcnt lgkmcnt(6)\n\t" \
  "v_mfma_f32_16x16x32_bf16 v[144:147], v[120:123], v[200:203], v[144:147]\n\t" \
  "v_mfma_f32_16x16x32_bf16 v[148:151], v[124:127], v[200:203], v[148:151]\n\t" \
  "ds_read_b128 v[200:203], v211 offset:0\n\t" \
  "v_mfma_f32_16x16x32_bf16 v[152:155], v[120:123], v[204:207], v[152:155]\n\t" \
  "v_mfma_f32_16x16x32_bf16 v[156:159], v[124:127], v[204:207], v[156:159]\n\t" \
  "ds_read_b128 v[204:207], v211 offset:16384\n\t" \
  "s_add_u32 s86, s86, 1\n\t" \
  "s_add_u32 s87, s87, 1\n\t" \
  "s_cmp_lt_u32 s86, %[end]\n\t" \
  "s_cbranch_scc1 .Lp0%=\n\t" \
  "s_branch .Lx0%=\n\t" \
  ".Lf00%=:\n\t" \
  "s_nop 7\n\t" \
  "ds_write_b128 v220, v[152:155] offset:0\n\t" \
  "ds_write_b128 v220, v[156:159] offset:1024\n\t" \
  "s_lshr_b32 s90, s87, 7\n\t" \
  "s_xor_b32 s90, s90, %[s0]\n\t" \
  "s_and_b32 s90, s90, 1\n\t" \
  "s_lshl1_add_u32 s90, s87, s90\n\t" \
  "s_lshl_b32 s90, s90, 13\n\t" \
  "s_cmp_lt_i32 s86, 1\n\t" \
  "s_cselect_b32 s90, 0x70000000, s90\n\t" \
  "s_waitcnt lgkmcnt(0)\n\t" \
  "s_barrier\n\t" \
  "ds_read_b128 v[160:163], v221 offset:0\n\t" \
  "ds_read_b128 v[164:167], v221 offset:1024\n\t" \
  "s_waitcnt lgkmcnt(0)\n\t" \
  "v_add_f32 v160, v160, v144\n\t" \
  "v_add_f32 v161, v161, v145\n\t" \
  "v_add_f32 v162, v162, v146\n\t" \
  "v_add_f32 v163, v163, v147\n\t" \
  "v_add_f32 v164, v164, v148\n\t" \
  "v_add_f32 v165, v165, v149\n\t" \
  "v_add_f32 v166, v166, v150\n\t" \
  "v_add_f32 v167, v167, v151\n\t" \
  "v_cvt_pk_bf16_f32 v168, v160, v161\n\t" \
  "v_cvt_pk_bf16_f32 v169, v162, v163\n\t" \
  "v_cvt_pk_bf16_f32 v170, v164, v165\n\t" \
  "v_cvt_pk_bf16_f32 v171, v166, v167\n\t" \
  "buffer_store_dwordx4 v[168:171], v172, %[rt], s90 offen sc1\n\t" \
  "s_branch .Lfb00%=\n\t" \
  ".Lf10%=:\n\t" \
  "s_nop 7\n\t" \
  "ds_write_b128 v220, v[136:139] offset:0\n\t" \
  "ds_write_b128 v220, v[140:143] offset:1024\n\t" \
  "s_lshr_b32 s90, s87, 7\n\t" \
  "s_xor_b32 s90, s90, %[s0]\n\t" \
  "s_and_b32 s90, s90, 1\n\t" \
  "s_lshl1_add_u32 s90, s87, s90\n\t" \
  "s_lshl_b32 s90, s90, 13\n\t" \
  "s_cmp_lt_i32 s86, 1\n\t" \
  "s_cselect_b32 s90, 0x70000000, s90\n\t" \
  "s_waitcnt lgkmcnt(0)\n\t" \
  "s_barrier\n\t" \
  "ds_read_b128 v[160:163], v221 offset:0\n\t" \
  "ds_read_b128 v[164:167], v221 offset:1024\n\t" \
  "s_waitcnt lgkmcnt(0)\n\t" \
  "v_add_f32 v160, v160, v128\n\t" \
  "v_add_f32 v161, v161, v129\n\t" \
  "v_add_f32 v162, v162, v130\n\t" \
  "v_add_f32 v163, v163, v131\n\t" \
  "v_add_f32 v164, v164, v132\n\t" \
  "v_add_f32 v165, v165, v133\n\t" \
  "v_add_f32 v166, v166, v134\n\t" \
  "v_add_f32 v167, v167, v135\n\t" \
  "v_cvt_pk_bf16_f32 v168, v160, v161\n\t" \
  "v_cvt_pk_bf16_f32 v169, v162, v163\n\t" \
  "v_cvt_pk_bf16_f32 v170, v164, v165\n\t" \
  "v_cvt_pk_bf16_f32 v171, v166, v167\n\t" \
  "buffer_store_dwordx4 v[168:171], v172, %[rt], s90 offen sc1\n\t" \
  "s_branch .Lfb10%=\n\t" \
  ".Lw00%=:\n\t" \
  "s_waitcnt vmcnt(2)\n\t" \
  "s_barrier\n\t" \
  "s_branch .Lg00%=\n\t" \
  ".Lw10%=:\n\t" \
  "s_waitcnt vmcnt(2)\n\t" \
  "s_barrier\n\t" \
  "s_branch .Lg10%=\n\t" \
  ".Lw20%=:\n\t" \
  "s_waitcnt vmcnt(2)\n\t" \
  "s_barrier\n\t" \
  "s_branch .Lg20%=\n\t" \
  ".Lw30%=:\n\t" \
  "s_waitcnt vmcnt(2)\n\t" \
  "s_barrier\n\t" \
  "s_branch .Lg30%=\n\t" \
  ".Lx0%=:\n\t" \
  "s_waitcnt lgkmcnt(0)\n\t" \
  "s_setprio 0\n\t" \
  "s_mov_b32 m0, s94\n\t" \
  "s_branch .Le%=\n\t" \
  ".Lk%=:\n\t" \
  "s_mov_b32 s94, m0\n\t" \
  "s_mov_b64 s[92:93], %[x0]\n\t" \
  "s_mov_b32 s86, %[g0]\n\t" \
  "s_add_u32 s87, s86, %[t0]\n\t" \
  "s_sub_i32 s88, s86, 3\n\t" \
  "ds_read_b128 v[176:179], v208 offset:0\n\t" \
  "ds_read_b128 v[180:183], v208 offset:16384\n\t" \
  "ds_read_b128 v[184:187], v209 offset:0\n\t" \
  "ds_read_b128 v[188:191], v209 offset:16384\n\t" \
  "ds_read_b128 v[192:195], v210 offset:0\n\t" \
  "ds_read_b128 v[196:199], v210 offset:16384\n\t" \
  "ds_read_b128 v[200:203], v211 offset:0\n\t" \
  "ds_read_b128 v[204:207], v211 offset:16384\n\t" \
  "s_branch .Lfb01%=\n\t" \
  ".Lp1%=:\n\t" \
  "s_cmp_le_u32 s86, 5\n\t" \
  "s_cbranch_scc1 .Lf01%=\n\t" \
  ".Lfb01%=:\n\t" \
  "s_waitcnt lgkmcnt(6)\n\t" \
  "v_mfma_f32_16x16x32_bf16 v[128:131], v[0:3], v[176:179], 0\n\t" \
  "v_mfma_f32_16x16x32_bf16 v[132:135], v[4:7], v[176:179], 0\n\t" \
  "ds_read_b128 v[176:179], v208 offset:256\n\t" \
  "v_mfma_f32_16x16x32_bf16 v[136:139], v[0:3], v[180:183], 0\n\t" \
  "v_mfma_f32_16x16x32_bf16 v[140:143], v[4:7], v[180:183], 0\n\t" \
  "ds_read_b128 v[180:183], v208 offset:16640\n\t" \
  "s_waitcnt lgkmcnt(6)\n\t" \
  "v_mfma_f32_16x16x32_bf16 v[128:131], v[8:11], v[184:187], v[128:131]\n\t" \
  "v_mfma_f32_16x16x32_bf16 v[132:135], v[12:15], v[184:187], v[132:135]\n\t" \
  "ds_read_b128 v[184:187], v209 offset:256\n\t" \
  "s_add_u32 m0, %[lw], 67584\n\t" \
  "v_mfma_f32_16x16x32_bf16 v[136:139], v[8:11], v[188:191], v[136:139]\n\t" \
  "v_mfma_f32_16x16x32_bf16 v[140:143], v[12:15], v[188:191], v[140:143]\n\t" \
  "ds_read_b128 v[188:191], v209 offset:16640\n\t" \
  "global_load_lds_dwordx4 v218, s[92:93] sc1\n\t" \
  "ds_write_b128 v220, v[152:155] offset:0\n\t" \
  "ds_write_b128 v220, v[156:159] offset:1024\n\t" \
  "s_waitcnt lgkmcnt(8)\n\t" \
  "v_mfma_f32_16x16x32_bf16 v[128:131], v[16:19], v[192:195], v[128:131]\n\t" \
  "v_mfma_f32_16x16x32_bf16 v[132:135], v[20:23], v[192:195], v[132:135]\n\t" \
  "ds_read_b128 v[192:195], v210 offset:256\n\t" \
  "v_mfma_f32_16x16x32_bf16 v[136:139], v[16:19], v[196:199], v[136:139]\n\t" \
  "v_mfma_f32_16x16x32_bf16 v[140:143], v[20:23], v[196:199], v[140:143]\n\t" \
  "ds_read_b128 v[196:199], v210 offset:16640\n\t" \
  "s_lshr_b32 s90, s87, 7\n\t" \
  "s_xor_b32 s90, s90, %[s0]\n\t" \
  "s_and_b32 s90, s90, 1\n\t" \
  "s_lshl1_add_u32 s90, s87, s90\n\t" \
  "s_lshl_b32 s90, s90, 13\n\t" \
  "s_cmp_lt_i32 s86, 6\n\t" \
  "s_cselect_b32 s90, 0x70000000, s90\n\t" \
  "s_waitcnt lgkmcnt(8)\n\t" \
  "v_mfma_f32_16x16x32_bf16 v[128:131], v[24:27], v[200:203], v[128:131]\n\t" \
  "v_mfma_f32_16x16x32_bf16 v[132:135], v[28:31], v[200:203], v[132:135]\n\t" \
  "ds_read_b128 v[200:203], v211 offset:256\n\t" \
  "s_add_u32 m0, %[lw], 68608\n\t" \
  "v_mfma_f32_16x16x32_bf16 v[136:139], v[24:27], v[204:207], v[136:139]\n\t" \
  "v_mfma_f32_16x16x32_bf16 v[140:143], v[28:31], v[204:207], v[140:143]\n\t" \
  "ds_read_b128 v[204:207], v211 offset:16640\n\t" \
  "global_load_lds_dwordx4 v219, s[92:93] sc1\n\t" \
  "s_waitcnt lgkmcnt(4)\n\t" \
  "s_cmp_lt_u32 s86, 6\n\t" \
  "s_cbranch_scc1 .Lw01%=\n\t" \
  "s_waitcnt vmcnt(4)\n\t" \
  "s_barrier\n\t" \
  ".Lg01%=:\n\t" \
  "s_add_u32 s92, s92, 512\n\t" \
  "s_addc_u32 s93, s93, 0\n\t" \
  "v_mfma_f32_16x16x32_bf16 v[128:131], v[32:35], v[176:179], v[128:131]\n\t" \
  "v_mfma_f32_16x16x32_bf16 v[132:135], v[36:39], v[176:179], v[132:135]\n\t" \
  "ds_read_b128 v[176:179], v208 offset:32768\n\t" \
  "v_mfma_f32_16x16x32_bf16 v[136:139], v[32:35], v[180:183], v[136:139]\n\t" \
  "v_mfma_f32_16x16x32_bf16 v[140:143], v[36:39], v[180:183], v[140:143]\n\t" \
  "ds_read_b128 v[180:183], v208 offset:49152\n\t" \
  "ds_read_b128 v[160:163], v221 offset:0\n\t" \
  "ds_read_b128 v[164:167], v221 offset:1024\n\t" \
  "s_sub_i32 s91, s86, 2\n\t" \
  "s_max_i32 s88, s88, s91\n\t" \
  "v_mov_b32 v173, s88\n\t" \
  "buffer_store_dword v173, v174, %[rs], 0 offen sc1\n\t" \
  "v_mfma_f32_16x16x32_bf16 v[128:131], v[40:43], v[184:187], v[128:131]\n\t" \
  "v_mfma_f32_16x16x32_bf16 v[132:135], v[44:47], v[184:187], v[132:135]\n\t" \
  "ds_read_b128 v[184:187], v209 offset:32768\n\t" \
  "s_add_u32 m0, %[lw], 98304\n\t" \
  "v_mfma_f32_16x16x32_bf16 v[136:139], v[40:43], v[188:191], v[136:139]\n\t" \
  "v_mfma_f32_16x16x32_bf16 v[140:143], v[44:47], v[188:191], v[140:143]\n\t" \
  "ds_read_b128 v[188:191], v209 offset:49152\n\t" \
  "global_load_lds_dwordx4 v216, s[92:93] sc1\n\t" \
  "s_waitcnt lgkmcnt(8)\n\t" \
  "v_mfma_f32_16x16x32_bf16 v[128:131], v[48:51], v[192:195], v[128:131]\n\t" \
  "v_mfma_f32_16x16x32_bf16 v[132:135], v[52:55], v[192:195], v[132:135]\n\t" \
  "ds_read_b128 v[192:195], v210 offset:32768\n\t" \
  "v_mfma_f32_16x16x32_bf16 v[136:139], v[48:51], v[196:199], v[136:139]\n\t" \
  "v_mfma_f32_16x16x32_bf16 v[140:143], v[52:55], v[196:199], v[140:143]\n\t" \
  "ds_read_b128 v[196:199], v210 offset:49152\n\t" \
  "s_waitcnt lgkmcnt(4)\n\t" \
  "v_add_f32 v160, v160, v144\n\t" \
  "v_add_f32 v161, v161, v145\n\t" \
  "v_add_f32 v162, v162, v146\n\t" \
  "v_add_f32 v163, v163, v147\n\t" \
  "v_add_f32 v164, v164, v148\n\t" \
  "v_add_f32 v165, v165, v149\n\t" \
  "v_add_f32 v166, v166, v150\n\t" \
  "v_add_f32 v167, v167, v151\n\t" \
  "v_mfma_f32_16x16x32_bf16 v[128:131], v[56:59], v[200:203], v[128:131]\n\t" \
  "v_mfma_f32_16x16x32_bf16 v[132:135], v[60:63], v[200:203], v[132:135]\n\t" \
  "ds_read_b128 v[200:203], v211 offset:32768\n\t" \
  "s_add_u32 m0, %[lw], 99328\n\t" \
  "v_mfma_f32_16x16x32_bf16 v[136:139], v[56:59], v[204:207], v[136:139]\n\t" \
  "v_mfma_f32_16x16x32_bf16 v[140:143], v[60:63], v[204:207], v[140:143]\n\t" \
  "ds_read_b128 v[204:207], v211 offset:49152\n\t" \
  "global_load_lds_dwordx4 v217, s[92:93] sc1\n\t" \
  "v_cvt_pk_bf16_f32 v168, v160, v161\n\t" \
  "v_cvt_pk_bf16_f32 v169, v162, v163\n\t" \
  "v_cvt_pk_bf16_f32 v170, v164, v165\n\t" \
  "v_cvt_pk_bf16_f32 v171, v166, v167\n\t" \
  "buffer_store_dwordx4 v[168:171], v172, %[rt], s90 offen sc1\n\t" \
  "v_mfma_f32_16x16x32_bf16 v[128:131], v[64:67], v[176:179], v[128:131]\n\t" \
  "v_mfma_f32_16x16x32_bf16 v[132:135], v[68:71], v[176:179], v[132:135]\n\t" \
  "ds_read_b128 v[176:179], v208 offset:33024\n\t" \
  "v_mfma_f32_16x16x32_bf16 v[136:139], v[64:67], v[180:183], v[136:139]\n\t" \
  "v_mfma_f32_16x16x32_bf16 v[140:143], v[68:71], v[180:183], v[140:143]\n\t" \
  "ds_read_b128 v[180:183], v208 offset:49408\n\t" \
  "s_waitcnt lgkmcnt(6)\n\t" \
  "v_mfma_f32_16x16x32_bf16 v[128:131], v[72:75], v[184:187], v[128:131]\n\t" \
  "v_mfma_f32_16x16x32_bf16 v[132:135], v[76:79], v[184:187], v[132:135]\n\t" \
  "ds_read_b128 v[184:187], v209 offset:33024\n\t" \
  "s_add_u32 m0, %[lw], 100352\n\t" \
  "v_mfma_f32_16x16x32_bf16 v[136:139], v[72:75], v[188:191], v[136:139]\n\t" \
  "v_mfma_f32_16x16x32_bf16 v[140:143], v[76:79], v[188:191], v[140:143]\n\t" \
  "ds_read_b128 v[188:191], v209 offset:49408\n\t" \
  "global_load_lds_dwordx4 v218, s[92:93] sc1\n\t" \
  "s_waitcnt lgkmcnt(6)\n\t" \
  "v_mfma_f32_16x16x32_bf16 v[128:131], v[80:83], v[192:195], v[128:131]\n\t" \
  "v_mfma_f32_16x16x32_bf16 v[132:135], v[84:87], v[192:195], v[132:135]\n\t" \
  "ds_read_b128 v[192:195], v210 offset:33024\n\t" \
  "v_mfma_f32_16x16x32_bf16 v[136:139], v[80:83], v[196:199], v[136:139]\n\t" \
  "v_mfma_f32_16x16x32_bf16 v[140:143], v[84:87], v[196:199], v[140:143]\n\t" \
  "ds_read_b128 v[196:199], v210 offset:49408\n\t" \
  "s_waitcnt lgkmcnt(6)\n\t" \
  "v_mfma_f32_16x16x32_bf16 v[128:131], v[88:91], v[200:203], v[128:131]\n\t" \
  "v_mfma_f32_16x16x32_bf16 v[132:135], v[92:95], v[200:203], v[132:135]\n\t" \
  "ds_read_b128 v[200:203], v211 offset:33024\n\t" \
  "s_add_u32 m0, %[lw], 101376\n\t" \
  "v_mfma_f32_16x16x32_bf16 v[136:139], v[88:91], v[204:207], v[136:139]\n\t" \
  "v_mfma_f32_16x16x32_bf16 v[140:143], v[92:95], v[204:207], v[140:143]\n\t" \
  "ds_read_b128 v[204:207], v211 offset:49408\n\t" \
  "global_load_lds_dwordx4 v219, s[92:93] sc1\n\t" \
  "s_cmp_lt_u32 s86, 8\n\t" \
  "s_cbranch_scc1 .Lw11%=\n\t" \
  "s_waitcnt vmcnt(6)\n\t" \
  "s_barrier\n\t" \
  ".Lg11%=:\n\t" \
  "s_add_u32 s92, s92, 65024\n\t" \
  "s_addc_u32 s93, s93, 0\n\t" \
  "s_waitcnt lgkmcnt(6)\n\t" \
  "v_mfma_f32_16x16x32_bf16 v[128:131], v[96:99], v[176:179], v[128:131]\n\t" \
  "v_mfma_f32_16x16x32_bf16 v[132:135], v[100:103], v[176:179], v[132:135]\n\t" \
  "ds_read_b128 v[176:179], v212 offset:0\n\t" \
  "v_mfma_f32_16x16x32_bf16 v[136:139], v[96:99], v[180:183], v[136:139]\n\t" \
  "v_mfma_f32_16x16x32_bf16 v[140:143], v[100:103], v[180:183], v[140:143]\n\t" \
  "ds_read_b128 v[180:183], v212 offset:16384\n\t" \
  "s_waitcnt lgkmcnt(6)\n\t" \
  "v_mfma_f32_16x16x32_bf16 v[128:131], v[104:107], v[184:187], v[128:131]\n\t" \
  "v_mfma_f32_16x16x32_bf16 v[132:135], v[108:111], v[184:187], v[132:135]\n\t" \
  "ds_read_b128 v[184:187], v213 offset:0\n\t" \
  "s_add_u32 m0, %[lw], 0\n\t" \
  "v_mfma_f32_16x16x32_bf16 v[136:139], v[104:107], v[188:191], v[136:139]\n\t" \
  "v_mfma_f32_16x16x32_bf16 v[140:143], v[108:111], v[188:191], v[140:143]\n\t" \
  "ds_read_b128 v[188:191], v213 offset:16384\n\t" \
  "global_load_lds_dwordx4 v216, s[92:93] sc1\n\t" \
  "s_waitcnt lgkmcnt(6)\n\t" \
  "v_mfma_f32_16x16x32_bf16 v[128:131], v[112:115], v[192:195], v[128:131]\n\t" \
  "v_mfma_f32_16x16x32_bf16 v[132:135], v[116:119], v[192:195], v[132:135]\n\t" \
  "ds_read_b128 v[192:195], v214 offset:0\n\t" \
  "v_mfma_f32_16x16x32_bf16 v[136:139], v[112:115], v[196:199], v[136:139]\n\t" \
  "v_mfma_f32_16x16x32_bf16 v[140:143], v[116:119], v[196:199], v[140:143]\n\t" \
  "ds_read_b128 v[196:199], v214 offset:16384\n\t" \
  "s_waitcnt lgkmcnt(6)\n\t" \
  "v_mfma_f32_16x16x32_bf16 v[128:131], v[120:123], v[200:203], v[128:131]\n\t" \
  "v_mfma_f32_16x16x32_bf16 v[132:135], v[124:127], v[200:203], v[132:135]\n\t" \
  "ds_read_b128 v[200:203], v215 offset:0\n\t" \
  "s_add_u32 m0, %[lw], 1024\n\t" \
  "v_mfma_f32_16x16x32_bf16 v[136:139], v[120:123], v[204:207], v[136:139]\n\t" \
  "v_mfma_f32_16x16x32_bf16 v[140:143], v[124:127], v[204:207], v[140:143]\n\t" \
  "ds_read_b128 v[204:207], v215 offset:16384\n\t" \
  "global_load_lds_dwordx4 v217, s[92:93] sc1\n\t" \
  "s_add_u32 s86, s86, 1\n\t" \
  "s_add_u32 s87, s87, 1\n\t" \
  "s_cmp_le_u32 s86, 5\n\t" \
  "s_cbranch_scc1 .Lf11%=\n\t" \
  ".Lfb11%=:\n\t" \
  "s_waitcnt lgkmcnt(6)\n\t" \
  "v_mfma_f32_16x16x32_bf16 v[144:147], v[0:3], v[176:179], 0\n\t" \
  "v_mfma_f32_16x16x32_bf16 v[148:151], v[4:7], v[176:179], 0\n\t" \
  "ds_read_b128 v[176:179], v212 offset:256\n\t" \
  "v_mfma_f32_16x16x32_bf16 v[152:155], v[0:3], v[180:183], 0\n\t" \
  "v_mfma_f32_16x16x32_bf16 v[156:159], v[4:7], v[180:183], 0\n\t" \
  "ds_read_b128 v[180:183], v212 offset:16640\n\t" \
  "s_waitcnt lgkmcnt(6)\n\t" \
  "v_mfma_f32_16x16x32_bf16 v[144:147], v[8:11], v[184:187], v[144:147]\n\t" \
  "v_mfma_f32_16x16x32_bf16 v[148:151], v[12:15], v[184:187], v[148:151]\n\t" \
  "ds_read_b128 v[184:187], v213 offset:256\n\t" \
  "s_add_u32 m0, %[lw], 2048\n\t" \
  "v_mfma_f32_16x16x32_bf16 v[152:155], v[8:11], v[188:191], v[152:155]\n\t" \
  "v_mfma_f32_16x16x32_bf16 v[156:159], v[12:15], v[188:191], v[156:159]\n\t" \
  "ds_read_b128 v[188:191], v213 offset:16640\n\t" \
  "global_load_lds_dwordx4 v218, s[92:93] sc1\n\t" \
  "ds_write_b128 v220, v[136:139] offset:0\n\t" \
  "ds_write_b128 v220, v[140:143] offset:1024\n\t" \
  "s_waitcnt lgkmcnt(8)\n\t" \
  "v_mfma_f32_16x16x32_bf16 v[144:147], v[16:19], v[192:195], v[144:147]\n\t" \
  "v_mfma_f32_16x16x32_bf16 v[148:151], v[20:23], v[192:195], v[148:151]\n\t" \
  "ds_read_b128 v[192:195], v214 offset:256\n\t" \
  "v_mfma_f32_16x16x32_bf16 v[152:155], v[16:19], v[196:199], v[152:155]\n\t" \
  "v_mfma_f32_16x16x32_bf16 v[156:159], v[20:23], v[196:199], v[156:159]\n\t" \
  "ds_read_b128 v[196:199], v214 offset:16640\n\t" \
  "s_lshr_b32 s90, s87, 7\n\t" \
  "s_xor_b32 s90, s90, %[s0]\n\t" \
  "s_and_b32 s90, s90, 1\n\t" \
  "s_lshl1_add_u32 s90, s87, s90\n\t" \
  "s_lshl_b32 s90, s90, 13\n\t" \
  "s_cmp_lt_i32 s86, 6\n\t" \
  "s_cselect_b32 s90, 0x70000000, s90\n\t" \
  "s_waitcnt lgkmcnt(8)\n\t" \
  "v_mfma_f32_16x16x32_bf16 v[144:147], v[24:27], v[200:203], v[144:147]\n\t" \
  "v_mfma_f32_16x16x32_bf16 v[148:151], v[28:31], v[200:203], v[148:151]\n\t" \
  "ds_read_b128 v[200:203], v215 offset:256\n\t" \
  "s_add_u32 m0, %[lw], 3072\n\t" \
  "v_mfma_f32_16x16x32_bf16 v[152:155], v[24:27], v[204:207], v[152:155]\n\t" \
  "v_mfma_f32_16x16x32_bf16 v[156:159], v[28:31], v[204:207], v[156:159]\n\t" \
  "ds_read_b128 v[204:207], v215 offset:16640\n\t" \
  "global_load_lds_dwordx4 v219, s[92:93] sc1\n\t" \
  "s_waitcnt lgkmcnt(4)\n\t" \
  "s_cmp_lt_u32 s86, 6\n\t" \
  "s_cbranch_scc1 .Lw21%=\n\t" \
  "s_waitcnt vmcnt(4)\n\t" \
  "s_barrier\n\t" \
  ".Lg21%=:\n\t" \
  "s_add_u32 s92, s92, 512\n\t" \
  "s_addc_u32 s93, s93, 0\n\t" \
  "v_mfma_f32_16x16x32_bf16 v[144:147], v[32:35], v[176:179], v[144:147]\n\t" \
  "v_mfma_f32_16x16x32_bf16 v[148:151], v[36:39], v[176:179], v[148:151]\n\t" \
  "ds_read_b128 v[176:179], v212 offset:32768\n\t" \
  "v_mfma_f32_16x16x32_bf16 v[152:155], v[32:35], v[180:183], v[152:155]\n\t" \
  "v_mfma_f32_16x16x32_bf16 v[156:159], v[36:39], v[180:183], v[156:159]\n\t" \
  "ds_read_b128 v[180:183], v212 offset:49152\n\t" \
  "ds_read_b128 v[160:163], v221 offset:0\n\t" \
  "ds_read_b128 v[164:167], v221 offset:1024\n\t" \
  "s_sub_i32 s91, s86, 2\n\t" \
  "s_max_i32 s88, s88, s91\n\t" \
  "v_mov_b32 v173, s88\n\t" \
  "buffer_store_dword v173, v174, %[rs], 0 offen sc1\n\t" \
  "v_mfma_f32_16x16x32_bf16 v[144:147], v[40:43], v[184:187], v[144:147]\n\t" \
  "v_mfma_f32_16x16x32_bf16 v[148:151], v[44:47], v[184:187], v[148:151]\n\t" \
  "ds_read_b128 v[184:187], v213 offset:32768\n\t" \
  "s_add_u32 m0, %[lw], 32768\n\t" \
  "v_mfma_f32_16x16x32_bf16 v[152:155], v[40:43], v[188:191], v[152:155]\n\t" \
  "v_mfma_f32_16x16x32_bf16 v[156:159], v[44:47], v[188:191], v[156:159]\n\t" \
  "ds_read_b128 v[188:191], v213 offset:49152\n\t" \
  "global_load_lds_dwordx4 v216, s[92:93] sc1\n\t" \
  "s_waitcnt lgkmcnt(8)\n\t" \
  "v_mfma_f32_16x16x32_bf16 v[144:147], v[48:51], v[192:195], v[144:147]\n\t" \
  "v_mfma_f32_16x16x32_bf16 v[148:151], v[52:55], v[192:195], v[148:151]\n\t" \
  "ds_read_b128 v[192:195], v214 offset:32768\n\t" \
  "v_mfma_f32_16x16x32_bf16 v[152:155], v[48:51], v[196:199], v[152:155]\n\t" \
  "v_mfma_f32_16x16x32_bf16 v[156:159], v[52:55], v[196:199], v[156:159]\n\t" \
  "ds_read_b128 v[196:199], v214 offset:49152\n\t" \
  "s_waitcnt lgkmcnt(4)\n\t" \
  "v_add_f32 v160, v160, v128\n\t" \
  "v_add_f32 v161, v161, v129\n\t" \
  "v_add_f32 v162, v162, v130\n\t" \
  "v_add_f32 v163, v163, v131\n\t" \
  "v_add_f32 v164, v164, v132\n\t" \
  "v_add_f32 v165, v165, v133\n\t" \
  "v_add_f32 v166, v166, v134\n\t" \
  "v_add_f32 v167, v167, v135\n\t" \
  "v_mfma_f32_16x16x32_bf16 v[144:147], v[56:59], v[200:203], v[144:147]\n\t" \
  "v_mfma_f32_16x16x32_bf16 v[148:151], v[60:63], v[200:203], v[148:151]\n\t" \
  "ds_read_b128 v[200:203], v215 offset:32768\n\t" \
  "s_add_u32 m0, %[lw], 33792\n\t" \
  "v_mfma_f32_16x16x32_bf16 v[152:155], v[56:59], v[204:207], v[152:155]\n\t" \
  "v_mfma_f32_16x16x32_bf16 v[156:159], v[60:63], v[204:207], v[156:159]\n\t" \
  "ds_read_b128 v[204:207], v215 offset:49152\n\t" \
  "global_load_lds_dwordx4 v217, s[92:93] sc1\n\t" \
  "v_cvt_pk_bf16_f32 v168, v160, v161\n\t" \
  "v_cvt_pk_bf16_f32 v169, v162, v163\n\t" \
  "v_cvt_pk_bf16_f32 v170, v164, v165\n\t" \
  "v_cvt_pk_bf16_f32 v171, v166, v167\n\t" \
  "buffer_store_dwordx4 v[168:171], v172, %[rt], s90 offen sc1\n\t" \
  "v_mfma_f32_16x16x32_bf16 v[144:147], v[64:67], v[176:179], v[144:147]\n\t" \
  "v_mfma_f32_16x16x32_bf16 v[148:151], v[68:71], v[176:179], v[148:151]\n\t" \
  "ds_read_b128 v[176:179], v212 offset:33024\n\t" \
  "v_mfma_f32_16x16x32_bf16 v[152:155], v[64:67], v[180:183], v[152:155]\n\t" \
  "v_mfma_f32_16x16x32_bf16 v[156:159], v[68:71], v[180:183], v[156:159]\n\t" \
  "ds_read_b128 v[180:183], v212 offset:49408\n\t" \
  "s_waitcnt lgkmcnt(6)\n\t" \
  "v_mfma_f32_16x16x32_bf16 v[144:147], v[72:75], v[184:187], v[144:147]\n\t" \
  "v_mfma_f32_16x16x32_bf16 v[148:151], v[76:79], v[184:187], v[148:151]\n\t" \
  "ds_read_b128 v[184:187], v213 offset:33024\n\t" \
  "s_add_u32 m0, %[lw], 34816\n\t" \
  "v_mfma_f32_16x16x32_bf16 v[152:155], v[72:75], v[188:191], v[152:155]\n\t" \
  "v_mfma_f32_16x16x32_bf16 v[156:159], v[76:79], v[188:191], v[156:159]\n\t" \
  "ds_read_b128 v[188:191], v213 offset:49408\n\t" \
  "global_load_lds_dwordx4 v218, s[92:93] sc1\n\t" \
  "s_waitcnt lgkmcnt(6)\n\t" \
  "v_mfma_f32_16x16x32_bf16 v[144:147], v[80:83], v[192:195], v[144:147]\n\t" \
  "v_mfma_f32_16x16x32_bf16 v[148:151], v[84:87], v[192:195], v[148:151]\n\t" \
  "ds_read_b128 v[192:195], v214 offset:33024\n\t" \
  "v_mfma_f32_16x16x32_bf16 v[152:155], v[80:83], v[196:199], v[152:155]\n\t" \
  "v_mfma_f32_16x16x32_bf16 v[156:159], v[84:87], v[196:199], v[156:159]\n\t" \
  "ds_read_b128 v[196:199], v214 offset:49408\n\t" \
  "s_waitcnt lgkmcnt(6)\n\t" \
  "v_mfma_f32_16x16x32_bf16 v[144:147], v[88:91], v[200:203], v[144:147]\n\t" \
  "v_mfma_f32_16x16x32_bf16 v[148:151], v[92:95], v[200:203], v[148:151]\n\t" \
  "ds_read_b128 v[200:203], v215 offset:33024\n\t" \
  "s_add_u32 m0, %[lw], 35840\n\t" \
  "v_mfma_f32_16x16x32_bf16 v[152:155], v[88:91], v[204:207], v[152:155]\n\t" \
  "v_mfma_f32_16x16x32_bf16 v[156:159], v[92:95], v[204:207], v[156:159]\n\t" \
  "ds_read_b128 v[204:207], v215 offset:49408\n\t" \
  "global_load_lds_dwordx4 v219, s[92:93] sc1\n\t" \
  "s_cmp_lt_u32 s86, 8\n\t" \
  "s_cbranch_scc1 .Lw31%=\n\t" \
  "s_waitcnt vmcnt(6)\n\t" \
  "s_barrier\n\t" \
  ".Lg31%=:\n\t" \
  "s_add_u32 s92, s92, 65024\n\t" \
  "s_addc_u32 s93, s93, 0\n\t" \
  "s_waitcnt lgkmcnt(6)\n\t" \
  "v_mfma_f32_16x16x32_bf16 v[144:147], v[96:99], v[176:179], v[144:147]\n\t" \
  "v_mfma_f32_16x16x32_bf16 v[148:151], v[100:103], v[176:179], v[148:151]\n\t" \
  "ds_read_b128 v[176:179], v208 offset:0\n\t" \
  "v_mfma_f32_16x16x32_bf16 v[152:155], v[96:99], v[180:183], v[152:155]\n\t" \
  "v_mfma_f32_16x16x32_bf16 v[156:159], v[100:103], v[180:183], v[156:159]\n\t" \
  "ds_read_b128 v[180:183], v208 offset:16384\n\t" \
  "s_waitcnt lgkmcnt(6)\n\t" \
  "v_mfma_f32_16x16x32_bf16 v[144:147], v[104:107], v[184:187], v[144:147]\n\t" \
  "v_mfma_f32_16x16x32_bf16 v[148:151], v[108:111], v[184:187], v[148:151]\n\t" \
  "ds_read_b128 v[184:187], v209 offset:0\n\t" \
  "s_add_u32 m0, %[lw], 65536\n\t" \
  "v_mfma_f32_16x16x32_bf16 v[152:155], v[104:107], v[188:191], v[152:155]\n\t" \
  "v_mfma_f32_16x16x32_bf16 v[156:159], v[108:111], v[188:191], v[156:159]\n\t" \
  "ds_read_b128 v[188:191], v209 offset:16384\n\t" \
  "global_load_lds_dwordx4 v216, s[92:93] sc1\n\t" \
  "s_waitcnt lgkmcnt(6)\n\t" \
  "v_mfma_f32_16x16x32_bf16 v[144:147], v[112:115], v[192:195], v[144:147]\n\t" \
  "v_mfma_f32_16x16x32_bf16 v[148:151], v[116:119], v[192:195], v[148:151]\n\t" \
  "ds_read_b128 v[192:195], v210 offset:0\n\t" \
  "v_mfma_f32_16x16x32_bf16 v[152:155], v[112:115], v[196:199], v[152:155]\n\t" \
  "v_mfma_f32_16x16x32_bf16 v[156:159], v[116:119], v[196:199], v[156:159]\n\t" \
  "ds_read_b128 v[196:199], v210 offset:16384\n\t" \
  "s_waitcnt lgkmcnt(6)\n\t" \
  "v_mfma_f32_16x16x32_bf16 v[144:147], v[120:123], v[200:203], v[144:147]\n\t" \
  "v_mfma_f32_16x16x32_bf16 v[148:151], v[124:127], v[200:203], v[148:151]\n\t" \
  "ds_read_b128 v[200:203], v211 offset:0\n\t" \
  "s_add_u32 m0, %[lw], 66560\n\t" \
  "v_mfma_f32_16x16x32_bf16 v[152:155], v[120:123], v[204:207], v[152:155]\n\t" \
  "v_mfma_f32_16x16x32_bf16 v[156:159], v[124:127], v[204:207], v[156:159]\n\t" \
  "ds_read_b128 v[204:207], v211 offset:16384\n\t" \
  "global_load_lds_dwordx4 v217, s[92:93] sc1\n\t" \
  "s_add_u32 s86, s86, 1\n\t" \
  "s_add_u32 s87, s87, 1\n\t" \
  "s_cmp_lt_u32 s86, %[end]\n\t" \
  "s_cbranch_scc1 .Lp1%=\n\t" \
  "s_branch .Lx1%=\n\t" \
  ".Lf01%=:\n\t" \
  "s_nop 7\n\t" \
  "ds_write_b128 v220, v[152:155] offset:0\n\t" \
  "ds_write_b128 v220, v[156:159] offset:1024\n\t" \
  "s_lshr_b32 s90, s87, 7\n\t" \
  "s_xor_b32 s90, s90, %[s0]\n\t" \
  "s_and_b32 s90, s90, 1\n\t" \
  "s_lshl1_add_u32 s90, s87, s90\n\t" \
  "s_lshl_b32 s90, s90, 13\n\t" \
  "s_cmp_lt_i32 s86, 1\n\t" \
  "s_cselect_b32 s90, 0x70000000, s90\n\t" \
  "s_waitcnt lgkmcnt(0)\n\t" \
  "s_barrier\n\t" \
  "ds_read_b128 v[160:163], v221 offset:0\n\t" \
  "ds_read_b128 v[164:167], v221 offset:1024\n\t" \
  "s_waitcnt lgkmcnt(0)\n\t" \
  "v_add_f32 v160, v160, v144\n\t" \
  "v_add_f32 v161, v161, v145\n\t" \
  "v_add_f32 v162, v162, v146\n\t" \
  "v_add_f32 v163, v163, v147\n\t" \
  "v_add_f32 v164, v164, v148\n\t" \
  "v_add_f32 v165, v165, v149\n\t" \
  "v_add_f32 v166, v166, v150\n\t" \
  "v_add_f32 v167, v167, v151\n\t" \
  "v_cvt_pk_bf16_f32 v168, v160, v161\n\t" \
  "v_cvt_pk_bf16_f32 v169, v162, v163\n\t" \
  "v_cvt_pk_bf16_f32 v170, v164, v165\n\t" \
  "v_cvt_pk_bf16_f32 v171, v166, v167\n\t" \
  "buffer_store_dwordx4 v[168:171], v172, %[rt], s90 offen sc1\n\t" \
  "s_branch .Lfb01%=\n\t" \
  ".Lf11%=:\n\t" \
  "s_nop 7\n\t" \
  "ds_write_b128 v220, v[136:139] offset:0\n\t" \
  "ds_write_b128 v220, v[140:143] offset:1024\n\t" \
  "s_lshr_b32 s90, s87, 7\n\t" \
  "s_xor_b32 s90, s90, %[s0]\n\t" \
  "s_and_b32 s90, s90, 1\n\t" \
  "s_lshl1_add_u32 s90, s87, s90\n\t" \
  "s_lshl_b32 s90, s90, 13\n\t" \
  "s_cmp_lt_i32 s86, 1\n\t" \
  "s_cselect_b32 s90, 0x70000000, s90\n\t" \
  "s_waitcnt lgkmcnt(0)\n\t" \
  "s_barrier\n\t" \
  "ds_read_b128 v[160:163], v221 offset:0\n\t" \
  "ds_read_b128 v[164:167], v221 offset:1024\n\t" \
  "s_waitcnt lgkmcnt(0)\n\t" \
  "v_add_f32 v160, v160, v128\n\t" \
  "v_add_f32 v161, v161, v129\n\t" \
  "v_add_f32 v162, v162, v130\n\t" \
  "v_add_f32 v163, v163, v131\n\t" \
  "v_add_f32 v164, v164, v132\n\t" \
  "v_add_f32 v165, v165, v133\n\t" \
  "v_add_f32 v166, v166, v134\n\t" \
  "v_add_f32 v167, v167, v135\n\t" \
  "v_cvt_pk_bf16_f32 v168, v160, v161\n\t" \
  "v_cvt_pk_bf16_f32 v169, v162, v163\n\t" \
  "v_cvt_pk_bf16_f32 v170, v164, v165\n\t" \
  "v_cvt_pk_bf16_f32 v171, v166, v167\n\t" \
  "buffer_store_dwordx4 v[168:171], v172, %[rt], s90 offen sc1\n\t" \
  "s_branch .Lfb11%=\n\t" \
  ".Lw01%=:\n\t" \
  "s_waitcnt vmcnt(2)\n\t" \
  "s_barrier\n\t" \
  "s_sub_i32 s91, s86, 1\n\t" \
  "s_max_i32 s88, s88, s91\n\t" \
  "v_mov_b32 v173, s88\n\t" \
  "buffer_store_dword v173, v174, %[rs], 0 offen sc1\n\t" \
  "s_branch .Lg01%=\n\t" \
  ".Lw11%=:\n\t" \
  "s_waitcnt vmcnt(2)\n\t" \
  "s_barrier\n\t" \
  "s_sub_i32 s91, s86, 1\n\t" \
  "s_max_i32 s88, s88, s91\n\t" \
  "v_mov_b32 v173, s88\n\t" \
  "buffer_store_dword v173, v174, %[rs], 0 offen sc1\n\t" \
  "s_branch .Lg11%=\n\t" \
  ".Lw21%=:\n\t" \
  "s_waitcnt vmcnt(2)\n\t" \
  "s_barrier\n\t" \
  "s_sub_i32 s91, s86, 1\n\t" \
  "s_max_i32 s88, s88, s91\n\t" \
  "v_mov_b32 v173, s88\n\t" \
  "buffer_store_dword v173, v174, %[rs], 0 offen sc1\n\t" \
  "s_branch .Lg21%=\n\t" \
  ".Lw31%=:\n\t" \
  "s_waitcnt vmcnt(2)\n\t" \
  "s_barrier\n\t" \
  "s_sub_i32 s91, s86, 1\n\t" \
  "s_max_i32 s88, s88, s91\n\t" \
  "v_mov_b32 v173, s88\n\t" \
  "buffer_store_dword v173, v174, %[rs], 0 offen sc1\n\t" \
  "s_branch .Lg31%=\n\t" \
  ".Lx1%=:\n\t" \
  "s_waitcnt lgkmcnt(0)\n\t" \
  "s_setprio 0\n\t" \
  "s_mov_b32 m0, s94\n\t" \
  ".Le%=:\n\t"
#define PR_ASM_1 \
  "s_cmp_lg_u32 %[kh], 0\n\t" \
  "s_cbranch_scc1 .Lk%=\n\t" \
  "s_mov_b32 s94, m0\n\t" \
  "s_mov_b64 s[92:93], %[x0]\n\t" \
  "s_mov_b32 s86, %[g0]\n\t" \
  "s_add_u32 s87, s86, %[t0]\n\t" \
  "s_sub_i32 s88, s86, 3\n\t" \
  "ds_read_b128 v[176:179], v208 offset:0\n\t" \
  "ds_read_b128 v[180:183], v208 offset:16384\n\t" \
  "ds_read_b128 v[184:187], v209 offset:0\n\t" \
  "ds_read_b128 v[188:191], v209 offset:16384\n\t" \
  "ds_read_b128 v[192:195], v210 offset:0\n\t" \
  "ds_read_b128 v[196:199], v210 offset:16384\n\t" \
  "ds_read_b128 v[200:203], v211 offset:0\n\t" \
  "ds_read_b128 v[204:207], v211 offset:16384\n\t" \
  "s_branch .Lfb00%=\n\t" \
  ".Lp0%=:\n\t" \
  "s_cmp_le_u32 s86, 5\n\t" \
  "s_cbranch_scc1 .Lf00%=\n\t" \
  ".Lfb00%=:\n\t" \
  "s_waitcnt lgkmcnt(6)\n\t" \
  "v_mfma_f32_16x16x32_bf16 v[128:131], v[176:179], v[0:3], 0\n\t" \
  "v_mfma_f32_16x16x32_bf16 v[136:139], v[176:179], v[4:7], 0\n\t" \
  "ds_read_b128 v[176:179], v208 offset:256\n\t" \
  "s_add_u32 m0, %[lw], 67584\n\t" \
  "v_mfma_f32_16x16x32_bf16 v[132:135], v[180:183], v[0:3], 0\n\t" \
  "v_mfma_f32_16x16x32_bf16 v[140:143], v[180:183], v[4:7], 0\n\t" \
  "ds_read_b128 v[180:183], v208 offset:16640\n\t" \
  "global_load_lds_dwordx4 v218, s[92:93] sc1\n\t" \
  "s_waitcnt lgkmcnt(6)\n\t" \
  "v_mfma_f32_16x16x32_bf16 v[128:131], v[184:187], v[8:11], v[128:131]\n\t" \
  "v_mfma_f32_16x16x32_bf16 v[136:139], v[184:187], v[12:15], v[136:139]\n\t" \
  "ds_read_b128 v[184:187], v209 offset:256\n\t" \
  "v_mfma_f32_16x16x32_bf16 v[132:135], v[188:191], v[8:11], v[132:135]\n\t" \
  "v_mfma_f32_16x16x32_bf16 v[140:143], v[188:191], v[12:15], v[140:143]\n\t" \
  "ds_read_b128 v[188:191], v209 offset:16640\n\t" \
  "ds_write_b128 v220, v[152:155] offset:0\n\t" \
  "ds_write_b128 v220, v[156:159] offset:1024\n\t" \
  "s_waitcnt lgkmcnt(8)\n\t" \
  "v_mfma_f32_16x16x32_bf16 v[128:131], v[192:195], v[16:19], v[128:131]\n\t" \
  "v_mfma_f32_16x16x32_bf16 v[136:139], v[192:195], v[20:23], v[136:139]\n\t" \
  "ds_read_b128 v[192:195], v210 offset:256\n\t" \
  "s_add_u32 m0, %[lw], 68608\n\t" \
  "v_mfma_f32_16x16x32_bf16 v[132:135], v[196:199], v[16:19], v[132:135]\n\t" \
  "v_mfma_f32_16x16x32_bf16 v[140:143], v[196:199], v[20:23], v[140:143]\n\t" \
  "ds_read_b128 v[196:199], v210 offset:16640\n\t" \
  "global_load_lds_dwordx4 v219, s[92:93] sc1\n\t" \
  "s_lshr_b32 s90, s87, 7\n\t" \
  "s_xor_b32 s90, s90, %[s0]\n\t" \
  "s_and_b32 s90, s90, 1\n\t" \
  "s_lshl1_add_u32 s90, s87, s90\n\t" \
  "s_lshl_b32 s90, s90, 13\n\t" \
  "s_cmp_lt_i32 s86, 6\n\t" \
  "s_cselect_b32 s90, 0x70000000, s90\n\t" \
  "s_waitcnt lgkmcnt(8)\n\t" \
  "v_mfma_f32_16x16x32_bf16 v[128:131], v[200:203], v[24:27], v[128:131]\n\t" \
  "v_mfma_f32_16x16x32_bf16 v[136:139], v[200:203], v[28:31], v[136:139]\n\t" \
  "ds_read_b128 v[200:203], v211 offset:256\n\t" \
  "v_mfma_f32_16x16x32_bf16 v[132:135], v[204:207], v[24:27], v[132:135]\n\t" \
  "v_mfma_f32_16x16x32_bf16 v[140:143], v[204:207], v[28:31], v[140:143]\n\t" \
  "ds_read_b128 v[204:207], v211 offset:16640\n\t" \
  "s_waitcnt lgkmcnt(4)\n\t" \
  "s_cmp_lt_u32 s86, 6\n\t" \
  "s_cbranch_scc1 .Lw00%=\n\t" \
  "s_waitcnt vmcnt(4)\n\t" \
  "s_barrier\n\t" \
  ".Lg00%=:\n\t" \
  "s_add_u32 s92, s92, 512\n\t" \
  "s_addc_u32 s93, s93, 0\n\t" \
  "v_mfma_f32_16x16x32_bf16 v[128:131], v[176:179], v[32:35], v[128:131]\n\t" \
  "v_mfma_f32_16x16x32_bf16 v[136:139], v[176:179], v[36:39], v[136:139]\n\t" \
  "ds_read_b128 v[176:179], v208 offset:32768\n\t" \
  "s_add_u32 m0, %[lw], 98304\n\t" \
  "v_mfma_f32_16x16x32_bf16 v[132:135], v[180:183], v[32:35], v[132:135]\n\t" \
  "v_mfma_f32_16x16x32_bf16 v[140:143], v[180:183], v[36:39], v[140:143]\n\t" \
  "ds_read_b128 v[180:183], v208 offset:49152\n\t" \
  "global_load_lds_dwordx4 v216, s[92:93] sc1\n\t" \
  "ds_read_b128 v[160:163], v221 offset:0\n\t" \
  "ds_read_b128 v[164:167], v221 offset:1024\n\t" \
  "v_mfma_f32_16x16x32_bf16 v[128:131], v[184:187], v[40:43], v[128:131]\n\t" \
  "v_mfma_f32_16x16x32_bf16 v[136:139], v[184:187], v[44:47], v[136:139]\n\t" \
  "ds_read_b128 v[184:187], v209 offset:32768\n\t" \
  "v_mfma_f32_16x16x32_bf16 v[132:135], v[188:191], v[40:43], v[132:135]\n\t" \
  "v_mfma_f32_16x16x32_bf16 v[140:143], v[188:191], v[44:47], v[140:143]\n\t" \
  "ds_read_b128 v[188:191], v209 offset:49152\n\t" \
  "s_waitcnt lgkmcnt(8)\n\t" \
  "v_mfma_f32_16x16x32_bf16 v[128:131], v[192:195], v[48:51], v[128:131]\n\t" \
  "v_mfma_f32_16x16x32_bf16 v[136:139], v[192:195], v[52:55], v[136:139]\n\t" \
  "ds_read_b128 v[192:195], v210 offset:32768\n\t" \
  "s_add_u32 m0, %[lw], 99328\n\t" \
  "v_mfma_f32_16x16x32_bf16 v[132:135], v[196:199], v[48:51], v[132:135]\n\t" \
  "v_mfma_f32_16x16x32_bf16 v[140:143], v[196:199], v[52:55], v[140:143]\n\t" \
  "ds_read_b128 v[196:199], v210 offset:49152\n\t" \
  "global_load_lds_dwordx4 v217, s[92:93] sc1\n\t" \
  "s_waitcnt lgkmcnt(4)\n\t" \
  "v_add_f32 v160, v160, v144\n\t" \
  "v_add_f32 v161, v161, v145\n\t" \
  "v_add_f32 v162, v162, v146\n\t" \
  "v_add_f32 v163, v163, v147\n\t" \
  "v_add_f32 v164, v164, v148\n\t" \
  "v_add_f32 v165, v165, v149\n\t" \
  "v_add_f32 v166, v166, v150\n\t" \
  "v_add_f32 v167, v167, v151\n\t" \
  "v_mfma_f32_16x16x32_bf16 v[128:131], v[200:203], v[56:59], v[128:131]\n\t" \
  "v_mfma_f32_16x16x32_bf16 v[136:139], v[200:203], v[60:63], v[136:139]\n\t" \
  "ds_read_b128 v[200:203], v211 offset:32768\n\t" \
  "v_mfma_f32_16x16x32_bf16 v[132:135], v[204:207], v[56:59], v[132:135]\n\t" \
  "v_mfma_f32_16x16x32_bf16 v[140:143], v[204:207], v[60:63], v[140:143]\n\t" \
  "ds_read_b128 v[204:207], v211 offset:49152\n\t" \
  "v_cvt_pk_bf16_f32 v168, v160, v161\n\t" \
  "v_cvt_pk_bf16_f32 v169, v162, v163\n\t" \
  "v_cvt_pk_bf16_f32 v170, v164, v165\n\t" \
  "v_cvt_pk_bf16_f32 v171, v166, v167\n\t" \
  "buffer_store_dwordx4 v[168:171], v172, %[rt], s90 offen sc1\n\t" \
  "v_mfma_f32_16x16x32_bf16 v[128:131], v[176:179], v[64:67], v[128:131]\n\t" \
  "v_mfma_f32_16x16x32_bf16 v[136:139], v[176:179], v[68:71], v[136:139]\n\t" \
  "ds_read_b128 v[176:179], v208 offset:33024\n\t" \
  "s_add_u32 m0, %[lw], 100352\n\t" \
  "v_mfma_f32_16x16x32_bf16 v[132:135], v[180:183], v[64:67], v[132:135]\n\t" \
  "v_mfma_f32_16x16x32_bf16 v[140:143], v[180:183], v[68:71], v[140:143]\n\t" \
  "ds_read_b128 v[180:183], v208 offset:49408\n\t" \
  "global_load_lds_dwordx4 v218, s[92:93] sc1\n\t" \
  "s_waitcnt lgkmcnt(6)\n\t" \
  "v_mfma_f32_16x16x32_bf16 v[128:131], v[184:187], v[72:75], v[128:131]\n\t" \
  "v_mfma_f32_16x16x32_bf16 v[136:139], v[184:187], v[76:79], v[136:139]\n\t" \
  "ds_read_b128 v[184:187], v209 offset:33024\n\t" \
  "v_mfma_f32_16x16x32_bf16 v[132:135], v[188:191], v[72:75], v[132:135]\n\t" \
  "v_mfma_f32_16x16x32_bf16 v[140:143], v[188:191], v[76:79], v[140:143]\n\t" \
  "ds_read_b128 v[188:191], v209 offset:49408\n\t" \
  "s_waitcnt lgkmcnt(6)\n\t" \
  "v_mfma_f32_16x16x32_bf16 v[128:131], v[192:195], v[80:83], v[128:131]\n\t" \
  "v_mfma_f32_16x16x32_bf16 v[136:139], v[192:195], v[84:87], v[136:139]\n\t" \
  "ds_read_b128 v[192:195], v210 offset:33024\n\t" \
  "s_add_u32 m0, %[lw], 101376\n\t" \
  "v_mfma_f32_16x16x32_bf16 v[132:135], v[196:199], v[80:83], v[132:135]\n\t" \
  "v_mfma_f32_16x16x32_bf16 v[140:143], v[196:199], v[84:87], v[140:143]\n\t" \
  "ds_read_b128 v[196:199], v210 offset:49408\n\t" \
  "global_load_lds_dwordx4 v219, s[92:93] sc1\n\t" \
  "s_waitcnt lgkmcnt(6)\n\t" \
  "v_mfma_f32_16x16x32_bf16 v[128:131], v[200:203], v[88:91], v[128:131]\n\t" \
  "v_mfma_f32_16x16x32_bf16 v[136:139], v[200:203], v[92:95], v[136:139]\n\t" \
  "ds_read_b128 v[200:203], v211 offset:33024\n\t" \
  "v_mfma_f32_16x16x32_bf16 v[132:135], v[204:207], v[88:91], v[132:135]\n\t" \
  "v_mfma_f32_16x16x32_bf16 v[140:143], v[204:207], v[92:95], v[140:143]\n\t" \
  "ds_read_b128 v[204:207], v211 offset:49408\n\t" \
  "s_cmp_lt_u32 s86, 8\n\t" \
  "s_cbranch_scc1 .Lw10%=\n\t" \
  "s_waitcnt vmcnt(5)\n\t" \
  "s_barrier\n\t" \
  ".Lg10%=:\n\t" \
  "s_add_u32 s92, s92, 65024\n\t" \
  "s_addc_u32 s93, s93, 0\n\t" \
  "s_waitcnt lgkmcnt(6)\n\t" \
  "v_mfma_f32_16x16x32_bf16 v[128:131], v[176:179], v[96:99], v[128:131]\n\t" \
  "v_mfma_f32_16x16x32_bf16 v[136:139], v[176:179], v[100:103], v[136:139]\n\t" \
  "ds_read_b128 v[176:179], v212 offset:0\n\t" \
  "s_add_u32 m0, %[lw], 0\n\t" \
  "v_mfma_f32_16x16x32_bf16 v[132:135], v[180:183], v[96:99], v[132:135]\n\t" \
  "v_mfma_f32_16x16x32_bf16 v[140:143], v[180:183], v[100:103], v[140:143]\n\t" \
  "ds_read_b128 v[180:183], v212 offset:16384\n\t" \
  "global_load_lds_dwordx4 v216, s[92:93] sc1\n\t" \
  "s_waitcnt lgkmcnt(6)\n\t" \
  "v_mfma_f32_16x16x32_bf16 v[128:131], v[184:187], v[104:107], v[128:131]\n\t" \
  "v_mfma_f32_16x16x32_bf16 v[136:139], v[184:187], v[108:111], v[136:139]\n\t" \
  "ds_read_b128 v[184:187], v213 offset:0\n\t" \
  "v_mfma_f32_16x16x32_bf16 v[132:135], v[188:191], v[104:107], v[132:135]\n\t" \
  "v_mfma_f32_16x16x32_bf16 v[140:143], v[188:191], v[108:111], v[140:143]\n\t" \
  "ds_read_b128 v[188:191], v213 offset:16384\n\t" \
  "s_waitcnt lgkmcnt(6)\n\t" \
  "v_mfma_f32_16x16x32_bf16 v[128:131], v[192:195], v[112:115], v[128:131]\n\t" \
  "v_mfma_f32_16x16x32_bf16 v[136:139], v[192:195], v[116:119], v[136:139]\n\t" \
  "ds_read_b128 v[192:195], v214 offset:0\n\t" \
  "s_add_u32 m0, %[lw], 1024\n\t" \
  "v_mfma_f32_16x16x32_bf16 v[132:135], v[196:199], v[112:115], v[132:135]\n\t" \
  "v_mfma_f32_16x16x32_bf16 v[140:143], v[196:199], v[116:119], v[140:143]\n\t" \
  "ds_read_b128 v[196:199], v214 offset:16384\n\t" \
  "global_load_lds_dwordx4 v217, s[92:93] sc1\n\t" \
  "s_waitcnt lgkmcnt(6)\n\t" \
  "v_mfma_f32_16x16x32_bf16 v[128:131], v[200:203], v[120:123], v[128:131]\n\t" \
  "v_mfma_f32_16x16x32_bf16 v[136:139], v[200:203], v[124:127], v[136:139]\n\t" \
  "ds_read_b128 v[200:203], v215 offset:0\n\t" \
  "v_mfma_f32_16x16x32_bf16 v[132:135], v[204:207], v[120:123], v[132:135]\n\t" \
  "v_mfma_f32_16x16x32_bf16 v[140:143], v[204:207], v[124:127], v[140:143]\n\t" \
  "ds_read_b128 v[204:207], v215 offset:16384\n\t" \
  "s_add_u32 s86, s86, 1\n\t" \
  "s_add_u32 s87, s87, 1\n\t" \
  "s_cmp_le_u32 s86, 5\n\t" \
  "s_cbranch_scc1 .Lf10%=\n\t" \
  ".Lfb10%=:\n\t" \
  "s_waitcnt lgkmcnt(6)\n\t" \
  "v_mfma_f32_16x16x32_bf16 v[144:147], v[176:179], v[0:3], 0\n\t" \
  "v_mfma_f32_16x16x32_bf16 v[152:155], v[176:179], v[4:7], 0\n\t" \
  "ds_read_b128 v[176:179], v212 offset:256\n\t" \
  "s_add_u32 m0, %[lw], 2048\n\t" \
  "v_mfma_f32_16x16x32_bf16 v[148:151], v[180:183], v[0:3], 0\n\t" \
  "v_mfma_f32_16x16x32_bf16 v[156:159], v[180:183], v[4:7], 0\n\t" \
  "ds_read_b128 v[180:183], v212 offset:16640\n\t" \
  "global_load_lds_dwordx4 v218, s[92:93] sc1\n\t" \
  "s_waitcnt lgkmcnt(6)\n\t" \
  "v_mfma_f32_16x16x32_bf16 v[144:147], v[184:187], v[8:11], v[144:147]\n\t" \
  "v_mfma_f32_16x16x32_bf16 v[152:155], v[184:187], v[12:15], v[152:155]\n\t" \
  "ds_read_b128 v[184:187], v213 offset:256\n\t" \
  "v_mfma_f32_16x16x32_bf16 v[148:151], v[188:191], v[8:11], v[148:151]\n\t" \
  "v_mfma_f32_16x16x32_bf16 v[156:159], v[188:191], v[12:15], v[156:159]\n\t" \
  "ds_read_b128 v[188:191], v213 offset:16640\n\t" \
  "ds_write_b128 v220, v[136:139] offset:0\n\t" \
  "ds_write_b128 v220, v[140:143] offset:1024\n\t" \
  "s_waitcnt lgkmcnt(8)\n\t" \
  "v_mfma_f32_16x16x32_bf16 v[144:147], v[192:195], v[16:19], v[144:147]\n\t" \
  "v_mfma_f32_16x16x32_bf16 v[152:155], v[192:195], v[20:23], v[152:155]\n\t" \
  "ds_read_b128 v[192:195], v214 offset:256\n\t" \
  "s_add_u32 m0, %[lw], 3072\n\t" \
  "v_mfma_f32_16x16x32_bf16 v[148:151], v[196:199], v[16:19], v[148:151]\n\t" \
  "v_mfma_f32_16x16x32_bf16 v[156:159], v[196:199], v[20:23], v[156:159]\n\t" \
  "ds_read_b128 v[196:199], v214 offset:16640\n\t" \
  "global_load_lds_dwordx4 v219, s[92:93] sc1\n\t" \
  "s_lshr_b32 s90, s87, 7\n\t" \
  "s_xor_b32 s90, s90, %[s0]\n\t" \
  "s_and_b32 s90, s90, 1\n\t" \
  "s_lshl1_add_u32 s90, s87, s90\n\t" \
  "s_lshl_b32 s90, s90, 13\n\t" \
  "s_cmp_lt_i32 s86, 6\n\t" \
  "s_cselect_b32 s90, 0x70000000, s90\n\t" \
  "s_waitcnt lgkmcnt(8)\n\t" \
  "v_mfma_f32_16x16x32_bf16 v[144:147], v[200:203], v[24:27], v[144:147]\n\t" \
  "v_mfma_f32_16x16x32_bf16 v[152:155], v[200:203], v[28:31], v[152:155]\n\t" \
  "ds_read_b128 v[200:203], v215 offset:256\n\t" \
  "v_mfma_f32_16x16x32_bf16 v[148:151], v[204:207], v[24:27], v[148:151]\n\t" \
  "v_mfma_f32_16x16x32_bf16 v[156:159], v[204:207], v[28:31], v[156:159]\n\t" \
  "ds_read_b128 v[204:207], v215 offset:16640\n\t" \
  "s_waitcnt lgkmcnt(4)\n\t" \
  "s_cmp_lt_u32 s86, 6\n\t" \
  "s_cbranch_scc1 .Lw20%=\n\t" \
  "s_waitcnt vmcnt(4)\n\t" \
  "s_barrier\n\t" \
  ".Lg20%=:\n\t" \
  "s_add_u32 s92, s92, 512\n\t" \
  "s_addc_u32 s93, s93, 0\n\t" \
  "v_mfma_f32_16x16x32_bf16 v[144:147], v[176:179], v[32:35], v[144:147]\n\t" \
  "v_mfma_f32_16x16x32_bf16 v[152:155], v[176:179], v[36:39], v[152:155]\n\t" \
  "ds_read_b128 v[176:179], v212 offset:32768\n\t" \
  "s_add_u32 m0, %[lw], 32768\n\t" \
  "v_mfma_f32_16x16x32_bf16 v[148:151], v[180:183], v[32:35], v[148:151]\n\t" \
  "v_mfma_f32_16x16x32_bf16 v[156:159], v[180:183], v[36:39], v[156:159]\n\t" \
  "ds_read_b128 v[180:183], v212 offset:49152\n\t" \
  "global_load_lds_dwordx4 v216, s[92:93] sc1\n\t" \
  "ds_read_b128 v[160:163], v221 offset:0\n\t" \
  "ds_read_b128 v[164:167], v221 offset:1024\n\t" \
  "v_mfma_f32_16x16x32_bf16 v[144:147], v[184:187], v[40:43], v[144:147]\n\t" \
  "v_mfma_f32_16x16x32_bf16 v[152:155], v[184:187], v[44:47], v[152:155]\n\t" \
  "ds_read_b128 v[184:187], v213 offset:32768\n\t" \
  "v_mfma_f32_16x16x32_bf16 v[148:151], v[188:191], v[40:43], v[148:151]\n\t" \
  "v_mfma_f32_16x16x32_bf16 v[156:159], v[188:191], v[44:47], v[156:159]\n\t" \
  "ds_read_b128 v[188:191], v213 offset:49152\n\t" \
  "s_waitcnt lgkmcnt(8)\n\t" \
  "v_mfma_f32_16x16x32_bf16 v[144:147], v[192:195], v[48:51], v[144:147]\n\t" \
  "v_mfma_f32_16x16x32_bf16 v[152:155], v[192:195], v[52:55], v[152:155]\n\t" \
  "ds_read_b128 v[192:195], v214 offset:32768\n\t" \
  "s_add_u32 m0, %[lw], 33792\n\t" \
  "v_mfma_f32_16x16x32_bf16 v[148:151], v[196:199], v[48:51], v[148:151]\n\t" \
  "v_mfma_f32_16x16x32_bf16 v[156:159], v[196:199], v[52:55], v[156:159]\n\t" \
  "ds_read_b128 v[196:199], v214 offset:49152\n\t" \
  "global_load_lds_dwordx4 v217, s[92:93] sc1\n\t" \
  "s_waitcnt lgkmcnt(4)\n\t" \
  "v_add_f32 v160, v160, v128\n\t" \
  "v_add_f32 v161, v161, v129\n\t" \
  "v_add_f32 v162, v162, v130\n\t" \
  "v_add_f32 v163, v163, v131\n\t" \
  "v_add_f32 v164, v164, v132\n\t" \
  "v_add_f32 v165, v165, v133\n\t" \
  "v_add_f32 v166, v166, v134\n\t" \
  "v_add_f32 v167, v167, v135\n\t" \
  "v_mfma_f32_16x16x32_bf16 v[144:147], v[200:203], v[56:59], v[144:147]\n\t" \
  "v_mfma_f32_16x16x32_bf16 v[152:155], v[200:203], v[60:63], v[152:155]\n\t" \
  "ds_read_b128 v[200:203], v215 offset:32768\n\t" \
  "v_mfma_f32_16x16x32_bf16 v[148:151], v[204:207], v[56:59], v[148:151]\n\t" \
  "v_mfma_f32_16x16x32_bf16 v[156:159], v[204:207], v[60:63], v[156:159]\n\t" \
  "ds_read_b128 v[204:207], v215 offset:49152\n\t" \
  "v_cvt_pk_bf16_f32 v168, v160, v161\n\t" \
  "v_cvt_pk_bf16_f32 v169, v162, v163\n\t" \
  "v_cvt_pk_bf16_f32 v170, v164, v165\n\t" \
  "v_cvt_pk_bf16_f32 v171, v166, v167\n\t" \
  "buffer_store_dwordx4 v[168:171], v172, %[rt], s90 offen sc1\n\t" \
  "v_mfma_f32_16x16x32_bf16 v[144:147], v[176:179], v[64:67], v[144:147]\n\t" \
  "v_mfma_f32_16x16x32_bf16 v[152:155], v[176:179], v[68:71], v[152:155]\n\t" \
  "ds_read_b128 v[176:179], v212 offset:33024\n\t" \
  "s_add_u32 m0, %[lw], 34816\n\t" \
  "v_mfma_f32_16x16x32_bf16 v[148:151], v[180:183], v[64:67], v[148:151]\n\t" \
  "v_mfma_f32_16x16x32_bf16 v[156:159], v[180:183], v[68:71], v[156:159]\n\t" \
  "ds_read_b128 v[180:183], v212 offset:49408\n\t" \
  "global_load_lds_dwordx4 v218, s[92:93] sc1\n\t" \
  "s_waitcnt lgkmcnt(6)\n\t" \
  "v_mfma_f32_16x16x32_bf16 v[144:147], v[184:187], v[72:75], v[144:147]\n\t" \
  "v_mfma_f32_16x16x32_bf16 v[152:155], v[184:187], v[76:79], v[152:155]\n\t" \
  "ds_read_b128 v[184:187], v213 offset:33024\n\t" \
  "v_mfma_f32_16x16x32_bf16 v[148:151], v[188:191], v[72:75], v[148:151]\n\t" \
  "v_mfma_f32_16x16x32_bf16 v[156:159], v[188:191], v[76:79], v[156:159]\n\t" \
  "ds_read_b128 v[188:191], v213 offset:49408\n\t" \
  "s_waitcnt lgkmcnt(6)\n\t" \
  "v_mfma_f32_16x16x32_bf16 v[144:147], v[192:195], v[80:83], v[144:147]\n\t" \
  "v_mfma_f32_16x16x32_bf16 v[152:155], v[192:195], v[84:87], v[152:155]\n\t" \
  "ds_read_b128 v[192:195], v214 offset:33024\n\t" \
  "s_add_u32 m0, %[lw], 35840\n\t" \
  "v_mfma_f32_16x16x32_bf16 v[148:151], v[196:199], v[80:83], v[148:151]\n\t" \
  "v_mfma_f32_16x16x32_bf16 v[156:159], v[196:199], v[84:87], v[156:159]\n\t" \
  "ds_read_b128 v[196:199], v214 offset:49408\n\t" \
  "global_load_lds_dwordx4 v219, s[92:93] sc1\n\t" \
  "s_waitcnt lgkmcnt(6)\n\t" \
  "v_mfma_f32_16x16x32_bf16 v[144:147], v[200:203], v[88:91], v[144:147]\n\t" \
  "v_mfma_f32_16x16x32_bf16 v[152:155], v[200:203], v[92:95], v[152:155]\n\t" \
  "ds_read_b128 v[200:203], v215 offset:33024\n\t" \
  "v_mfma_f32_16x16x32_bf16 v[148:151], v[204:207], v[88:91], v[148:151]\n\t" \
  "v_mfma_f32_16x16x32_bf16 v[156:159], v[204:207], v[92:95], v[156:159]\n\t" \
  "ds_read_b128 v[204:207], v215 offset:49408\n\t" \
  "s_cmp_lt_u32 s86, 8\n\t" \
  "s_cbranch_scc1 .Lw30%=\n\t" \
  "s_waitcnt vmcnt(5)\n\t" \
  "s_barrier\n\t" \
  ".Lg30%=:\n\t" \
  "s_add_u32 s92, s92, 65024\n\t" \
  "s_addc_u32 s93, s93, 0\n\t" \
  "s_waitcnt lgkmcnt(6)\n\t" \
  "v_mfma_f32_16x16x32_bf16 v[144:147], v[176:179], v[96:99], v[144:147]\n\t" \
  "v_mfma_f32_16x16x32_bf16 v[152:155], v[176:179], v[100:103], v[152:155]\n\t" \
  "ds_read_b128 v[176:179], v208 offset:0\n\t" \
  "s_add_u32 m0, %[lw], 65536\n\t" \
  "v_mfma_f32_16x16x32_bf16 v[148:151], v[180:183], v[96:99], v[148:151]\n\t" \
  "v_mfma_f32_16x16x32_bf16 v[156:159], v[180:183], v[100:103], v[156:159]\n\t" \
  "ds_read_b128 v[180:183], v208 offset:16384\n\t" \
  "global_load_lds_dwordx4 v216, s[92:93] sc1\n\t" \
  "s_waitcnt lgkmcnt(6)\n\t" \
  "v_mfma_f32_16x16x32_bf16 v[144:147], v[184:187], v[104:107], v[144:147]\n\t" \
  "v_mfma_f32_16x16x32_bf16 v[152:155], v[184:187], v[108:111], v[152:155]\n\t" \
  "ds_read_b128 v[184:187], v209 offset:0\n\t" \
  "v_mfma_f32_16x16x32_bf16 v[148:151], v[188:191], v[104:107], v[148:151]\n\t" \
  "v_mfma_f32_16x16x32_bf16 v[156:159], v[188:191], v[108:111], v[156:159]\n\t" \
  "ds_read_b128 v[188:191], v209 offset:16384\n\t" \
  "s_waitcnt lgkmcnt(6)\n\t" \
  "v_mfma_f32_16x16x32_bf16 v[144:147], v[192:195], v[112:115], v[144:147]\n\t" \
  "v_mfma_f32_16x16x32_bf16 v[152:155], v[192:195], v[116:119], v[152:155]\n\t" \
  "ds_read_b128 v[192:195], v210 offset:0\n\t" \
  "s_add_u32 m0, %[lw], 66560\n\t" \
  "v_mfma_f32_16x16x32_bf16 v[148:151], v[196:199], v[112:115], v[148:151]\n\t" \
  "v_mfma_f32_16x16x32_bf16 v[156:159], v[196:199], v[116:119], v[156:159]\n\t" \
  "ds_read_b128 v[196:199], v210 offset:16384\n\t" \
  "global_load_lds_dwordx4 v217, s[92:93] sc1\n\t" \
  "s_waitcnt lgkmcnt(6)\n\t" \
  "v_mfma_f32_16x16x32_bf16 v[144:147], v[200:203], v[120:123], v[144:147]\n\t" \
  "v_mfma_f32_16x16x32_bf16 v[152:155], v[200:203], v[124:127], v[152:155]\n\t" \
  "ds_read_b128 v[200:203], v211 offset:0\n\t" \
  "v_mfma_f32_16x16x32_bf16 v[148:151], v[204:207], v[120:123], v[148:151]\n\t" \
  "v_mfma_f32_16x16x32_bf16 v[156:159], v[204:207], v[124:127], v[156:159]\n\t" \
  "ds_read_b128 v[204:207], v211 offset:16384\n\t" \
  "s_add_u32 s86, s86, 1\n\t" \
  "s_add_u32 s87, s87, 1\n\t" \
  "s_cmp_lt_u32 s86, %[end]\n\t" \
  "s_cbranch_scc1 .Lp0%=\n\t" \
  "s_branch .Lx0%=\n\t" \
  ".Lf00%=:\n\t" \
  "s_nop 7\n\t" \
  "ds_write_b128 v220, v[152:155] offset:0\n\t" \
  "ds_write_b128 v220, v[156:159] offset:1024\n\t" \
  "s_lshr_b32 s90, s87, 7\n\t" \
  "s_xor_b32 s90, s90, %[s0]\n\t" \
  "s_and_b32 s90, s90, 1\n\t" \
  "s_lshl1_add_u32 s90, s87, s90\n\t" \
  "s_lshl_b32 s90, s90, 13\n\t" \
  "s_cmp_lt_i32 s86, 1\n\t" \
  "s_cselect_b32 s90, 0x70000000, s90\n\t" \
  "s_waitcnt lgkmcnt(0)\n\t" \
  "s_barrier\n\t" \
  "ds_read_b128 v[160:163], v221 offset:0\n\t" \
  "ds_read_b128 v[164:167], v221 offset:1024\n\t" \
  "s_waitcnt lgkmcnt(0)\n\t" \
  "v_add_f32 v160, v160, v144\n\t" \
  "v_add_f32 v161, v161, v145\n\t" \
  "v_add_f32 v162, v162, v146\n\t" \
  "v_add_f32 v163, v163, v147\n\t" \
  "v_add_f32 v164, v164, v148\n\t" \
  "v_add_f32 v165, v165, v149\n\t" \
  "v_add_f32 v166, v166, v150\n\t" \
  "v_add_f32 v167, v167, v151\n\t" \
  "v_cvt_pk_bf16_f32 v168, v160, v161\n\t" \
  "v_cvt_pk_bf16_f32 v169, v162, v163\n\t" \
  "v_cvt_pk_bf16_f32 v170, v164, v165\n\t" \
  "v_cvt_pk_bf16_f32 v171, v166, v167\n\t" \
  "buffer_store_dwordx4 v[168:171], v172, %[rt], s90 offen sc1\n\t" \
  "s_branch .Lfb00%=\n\t" \
  ".Lf10%=:\n\t" \
  "s_nop 7\n\t" \
  "ds_write_b128 v220, v[136:139] offset:0\n\t" \
  "ds_write_b128 v220, v[140:143] offset:1024\n\t" \
  "s_lshr_b32 s90, s87, 7\n\t" \
  "s_xor_b32 s90, s90, %[s0]\n\t" \
  "s_and_b32 s90, s90, 1\n\t" \
  "s_lshl1_add_u32 s90, s87, s90\n\t" \
  "s_lshl_b32 s90, s90, 13\n\t" \
  "s_cmp_lt_i32 s86, 1\n\t" \
  "s_cselect_b32 s90, 0x70000000, s90\n\t" \
  "s_waitcnt lgkmcnt(0)\n\t" \
  "s_barrier\n\t" \
  "ds_read_b128 v[160:163], v221 offset:0\n\t" \
  "ds_read_b128 v[164:167], v221 offset:1024\n\t" \
  "s_waitcnt lgkmcnt(0)\n\t" \
  "v_add_f32 v160, v160, v128\n\t" \
  "v_add_f32 v161, v161, v129\n\t" \
  "v_add_f32 v162, v162, v130\n\t" \
  "v_add_f32 v163, v163, v131\n\t" \
  "v_add_f32 v164, v164, v132\n\t" \
  "v_add_f32 v165, v165, v133\n\t" \
  "v_add_f32 v166, v166, v134\n\t" \
  "v_add_f32 v167, v167, v135\n\t" \
  "v_cvt_pk_bf16_f32 v168, v160, v161\n\t" \
  "v_cvt_pk_bf16_f32 v169, v162, v163\n\t" \
  "v_cvt_pk_bf16_f32 v170, v164, v165\n\t" \
  "v_cvt_pk_bf16_f32 v171, v166, v167\n\t" \
  "buffer_store_dwordx4 v[168:171], v172, %[rt], s90 offen sc1\n\t" \
  "s_branch .Lfb10%=\n\t" \
  ".Lw00%=:\n\t" \
  "s_waitcnt vmcnt(2)\n\t" \
  "s_barrier\n\t" \
  "s_branch .Lg00%=\n\t" \
  ".Lw10%=:\n\t" \
  "s_waitcnt vmcnt(2)\n\t" \
  "s_barrier\n\t" \
  "s_branch .Lg10%=\n\t" \
  ".Lw20%=:\n\t" \
  "s_waitcnt vmcnt(2)\n\t" \
  "s_barrier\n\t" \
  "s_branch .Lg20%=\n\t" \
  ".Lw30%=:\n\t" \
  "s_waitcnt vmcnt(2)\n\t" \
  "s_barrier\n\t" \
  "s_branch .Lg30%=\n\t" \
  ".Lx0%=:\n\t" \
  "s_waitcnt lgkmcnt(0)\n\t" \
  "s_setprio 0\n\t" \
  "s_mov_b32 m0, s94\n\t" \
  "s_branch .Le%=\n\t" \
  ".Lk%=:\n\t" \
  "s_mov_b32 s94, m0\n\t" \
  "s_mov_b64 s[92:93], %[x0]\n\t" \
  "s_mov_b32 s86, %[g0]\n\t" \
  "s_add_u32 s87, s86, %[t0]\n\t" \
  "s_sub_i32 s88, s86, 3\n\t" \
  "ds_read_b128 v[176:179], v208 offset:0\n\t" \
  "ds_read_b128 v[180:183], v208 offset:16384\n\t" \
  "ds_read_b128 v[184:187], v209 offset:0\n\t" \
  "ds_read_b128 v[188:191], v209 offset:16384\n\t" \
  "ds_read_b128 v[192:195], v210 offset:0\n\t" \
  "ds_read_b128 v[196:199], v210 offset:16384\n\t" \
  "ds_read_b128 v[200:203], v211 offset:0\n\t" \
  "ds_read_b128 v[204:207], v211 offset:16384\n\t" \
  "s_branch .Lfb01%=\n\t" \
  ".Lp1%=:\n\t" \
  "s_cmp_le_u32 s86, 5\n\t" \
  "s_cbranch_scc1 .Lf01%=\n\t" \
  ".Lfb01%=:\n\t" \
  "s_waitcnt lgkmcnt(6)\n\t" \
  "v_mfma_f32_16x16x32_bf16 v[128:131], v[176:179], v[0:3], 0\n\t" \
  "v_mfma_f32_16x16x32_bf16 v[136:139], v[176:179], v[4:7], 0\n\t" \
  "ds_read_b128 v[176:179], v208 offset:256\n\t" \
  "v_mfma_f32_16x16x32_bf16 v[132:135], v[180:183], v[0:3], 0\n\t" \
  "v_mfma_f32_16x16x32_bf16 v[140:143], v[180:183], v[4:7], 0\n\t" \
  "ds_read_b128 v[180:183], v208 offset:16640\n\t" \
  "s_waitcnt lgkmcnt(6)\n\t" \
  "v_mfma_f32_16x16x32_bf16 v[128:131], v[184:187], v[8:11], v[128:131]\n\t" \
  "v_mfma_f32_16x16x32_bf16 v[136:139], v[184:187], v[12:15], v[136:139]\n\t" \
  "ds_read_b128 v[184:187], v209 offset:256\n\t" \
  "s_add_u32 m0, %[lw], 67584\n\t" \
  "v_mfma_f32_16x16x32_bf16 v[132:135], v[188:191], v[8:11], v[132:135]\n\t" \
  "v_mfma_f32_16x16x32_bf16 v[140:143], v[188:191], v[12:15], v[140:143]\n\t" \
  "ds_read_b128 v[188:191], v209 offset:16640\n\t" \
  "global_load_lds_dwordx4 v218, s[92:93] sc1\n\t" \
  "ds_write_b128 v220, v[152:155] offset:0\n\t" \
  "ds_write_b128 v220, v[156:159] offset:1024\n\t" \
  "s_waitcnt lgkmcnt(8)\n\t" \
  "v_mfma_f32_16x16x32_bf16 v[128:131], v[192:195], v[16:19], v[128:131]\n\t" \
  "v_mfma_f32_16x16x32_bf16 v[136:139], v[192:195], v[20:23], v[136:139]\n\t" \
  "ds_read_b128 v[192:195], v210 offset:256\n\t" \
  "v_mfma_f32_16x16x32_bf16 v[132:135], v[196:199], v[16:19], v[132:135]\n\t" \
  "v_mfma_f32_16x16x32_bf16 v[140:143], v[196:199], v[20:23], v[140:143]\n\t" \
  "ds_read_b128 v[196:199], v210 offset:16640\n\t" \
  "s_lshr_b32 s90, s87, 7\n\t" \
  "s_xor_b32 s90, s90, %[s0]\n\t" \
  "s_and_b32 s90, s90, 1\n\t" \
  "s_lshl1_add_u32 s90, s87, s90\n\t" \
  "s_lshl_b32 s90, s90, 13\n\t" \
  "s_cmp_lt_i32 s86, 6\n\t" \
  "s_cselect_b32 s90, 0x70000000, s90\n\t" \
  "s_waitcnt lgkmcnt(8)\n\t" \
  "v_mfma_f32_16x16x32_bf16 v[128:131], v[200:203], v[24:27], v[128:131]\n\t" \
  "v_mfma_f32_16x16x32_bf16 v[136:139], v[200:203], v[28:31], v[136:139]\n\t" \
  "ds_read_b128 v[200:203], v211 offset:256\n\t" \
  "s_add_u32 m0, %[lw], 68608\n\t" \
  "v_mfma_f32_16x16x32_bf16 v[132:135], v[204:207], v[24:27], v[132:135]\n\t" \
  "v_mfma_f32_16x16x32_bf16 v[140:143], v[204:207], v[28:31], v[140:143]\n\t" \
  "ds_read_b128 v[204:207], v211 offset:16640\n\t" \
  "global_load_lds_dwordx4 v219, s[92:93] sc1\n\t" \
  "s_waitcnt lgkmcnt(4)\n\t" \
  "s_cmp_lt_u32 s86, 6\n\t" \
  "s_cbranch_scc1 .Lw01%=\n\t" \
  "s_waitcnt vmcnt(4)\n\t" \
  "s_barrier\n\t" \
  ".Lg01%=:\n\t" \
  "s_add_u32 s92, s92, 512\n\t" \
  "s_addc_u32 s93, s93, 0\n\t" \
  "v_mfma_f32_16x16x32_bf16 v[128:131], v[176:179], v[32:35], v[128:131]\n\t" \
  "v_mfma_f32_16x16x32_bf16 v[136:139], v[176:179], v[36:39], v[136:139]\n\t" \
  "ds_read_b128 v[176:179], v208 offset:32768\n\t" \
  "v_mfma_f32_16x16x32_bf16 v[132:135], v[180:183], v[32:35], v[132:135]\n\t" \
  "v_mfma_f32_16x16x32_bf16 v[140:143], v[180:183], v[36:39], v[140:143]\n\t" \
  "ds_read_b128 v[180:183], v208 offset:49152\n\t" \
  "ds_read_b128 v[160:163], v221 offset:0\n\t" \
  "ds_read_b128 v[164:167], v221 offset:1024\n\t" \
  "s_sub_i32 s91, s86, 2\n\t" \
  "s_max_i32 s88, s88, s91\n\t" \
  "v_mov_b32 v173, s88\n\t" \
  "buffer_store_dword v173, v174, %[rs], 0 offen sc1\n\t" \
  "v_mfma_f32_16x16x32_bf16 v[128:131], v[184:187], v[40:43], v[128:131]\n\t" \
  "v_mfma_f32_16x16x32_bf16 v[136:139], v[184:187], v[44:47], v[136:139]\n\t" \
  "ds_read_b128 v[184:187], v209 offset:32768\n\t" \
  "s_add_u32 m0, %[lw], 98304\n\t" \
  "v_mfma_f32_16x16x32_bf16 v[132:135], v[188:191], v[40:43], v[132:135]\n\t" \
  "v_mfma_f32_16x16x32_bf16 v[140:143], v[188:191], v[44:47], v[140:143]\n\t" \
  "ds_read_b128 v[188:191], v209 offset:49152\n\t" \
  "global_load_lds_dwordx4 v216, s[92:93] sc1\n\t" \
  "s_waitcnt lgkmcnt(8)\n\t" \
  "v_mfma_f32_16x16x32_bf16 v[128:131], v[192:195], v[48:51], v[128:131]\n\t" \
  "v_mfma_f32_16x16x32_bf16 v[136:139], v[192:195], v[52:55], v[136:139]\n\t" \
  "ds_read_b128 v[192:195], v210 offset:32768\n\t" \
  "v_mfma_f32_16x16x32_bf16 v[132:135], v[196:199], v[48:51], v[132:135]\n\t" \
  "v_mfma_f32_16x16x32_bf16 v[140:143], v[196:199], v[52:55], v[140:143]\n\t" \
  "ds_read_b128 v[196:199], v210 offset:49152\n\t" \
  "s_waitcnt lgkmcnt(4)\n\t" \
  "v_add_f32 v160, v160, v144\n\t" \
  "v_add_f32 v161, v161, v145\n\t" \
  "v_add_f32 v162, v162, v146\n\t" \
  "v_add_f32 v163, v163, v147\n\t" \
  "v_add_f32 v164, v164, v148\n\t" \
  "v_add_f32 v165, v165, v149\n\t" \
  "v_add_f32 v166, v166, v150\n\t" \
  "v_add_f32 v167, v167, v151\n\t" \
  "v_mfma_f32_16x16x32_bf16 v[128:131], v[200:203], v[56:59], v[128:131]\n\t" \
  "v_mfma_f32_16x16x32_bf16 v[136:139], v[200:203], v[60:63], v[136:139]\n\t" \
  "ds_read_b128 v[200:203], v211 offset:32768\n\t" \
  "s_add_u32 m0, %[lw], 99328\n\t" \
  "v_mfma_f32_16x16x32_bf16 v[132:135], v[204:207], v[56:59], v[132:135]\n\t" \
  "v_mfma_f32_16x16x32_bf16 v[140:143], v[204:207], v[60:63], v[140:143]\n\t" \
  "ds_read_b128 v[204:207], v211 offset:49152\n\t" \
  "global_load_lds_dwordx4 v217, s[92:93] sc1\n\t" \
  "v_cvt_pk_bf16_f32 v168, v160, v161\n\t" \
  "v_cvt_pk_bf16_f32 v169, v162, v163\n\t" \
  "v_cvt_pk_bf16_f32 v170, v164, v165\n\t" \
  "v_cvt_pk_bf16_f32 v171, v166, v167\n\t" \
  "buffer_store_dwordx4 v[168:171], v172, %[rt], s90 offen sc1\n\t" \
  "v_mfma_f32_16x16x32_bf16 v[128:131], v[176:179], v[64:67], v[128:131]\n\t" \
  "v_mfma_f32_16x16x32_bf16 v[136:139], v[176:179], v[68:71], v[136:139]\n\t" \
  "ds_read_b128 v[176:179], v208 offset:33024\n\t" \
  "v_mfma_f32_16x16x32_bf16 v[132:135], v[180:183], v[64:67], v[132:135]\n\t" \
  "v_mfma_f32_16x16x32_bf16 v[140:143], v[180:183], v[68:71], v[140:143]\n\t" \
  "ds_read_b128 v[180:183], v208 offset:49408\n\t" \
  "s_waitcnt lgkmcnt(6)\n\t" \
  "v_mfma_f32_16x16x32_bf16 v[128:131], v[184:187], v[72:75], v[128:131]\n\t" \
  "v_mfma_f32_16x16x32_bf16 v[136:139], v[184:187], v[76:79], v[136:139]\n\t" \
  "ds_read_b128 v[184:187], v209 offset:33024\n\t" \
  "s_add_u32 m0, %[lw], 100352\n\t" \
  "v_mfma_f32_16x16x32_bf16 v[132:135], v[188:191], v[72:75], v[132:135]\n\t" \
  "v_mfma_f32_16x16x32_bf16 v[140:143], v[188:191], v[76:79], v[140:143]\n\t" \
  "ds_read_b128 v[188:191], v209 offset:49408\n\t" \
  "global_load_lds_dwordx4 v218, s[92:93] sc1\n\t" \
  "s_waitcnt lgkmcnt(6)\n\t" \
  "v_mfma_f32_16x16x32_bf16 v[128:131], v[192:195], v[80:83], v[128:131]\n\t" \
  "v_mfma_f32_16x16x32_bf16 v[136:139], v[192:195], v[84:87], v[136:139]\n\t" \
  "ds_read_b128 v[192:195], v210 offset:33024\n\t" \
  "v_mfma_f32_16x16x32_bf16 v[132:135], v[196:199], v[80:83], v[132:135]\n\t" \
  "v_mfma_f32_16x16x32_bf16 v[140:143], v[196:199], v[84:87], v[140:143]\n\t" \
  "ds_read_b128 v[196:199], v210 offset:49408\n\t" \
  "s_waitcnt lgkmcnt(6)\n\t" \
  "v_mfma_f32_16x16x32_bf16 v[128:131], v[200:203], v[88:91], v[128:131]\n\t" \
  "v_mfma_f32_16x16x32_bf16 v[136:139], v[200:203], v[92:95], v[136:139]\n\t" \
  "ds_read_b128 v[200:203], v211 offset:33024\n\t" \
  "s_add_u32 m0, %[lw], 101376\n\t" \
  "v_mfma_f32_16x16x32_bf16 v[132:135], v[204:207], v[88:91], v[132:135]\n\t" \
  "v_mfma_f32_16x16x32_bf16 v[140:143], v[204:207], v[92:95], v[140:143]\n\t" \
  "ds_read_b128 v[204:207], v211 offset:49408\n\t" \
  "global_load_lds_dwordx4 v219, s[92:93] sc1\n\t" \
  "s_cmp_lt_u32 s86, 8\n\t" \
  "s_cbranch_scc1 .Lw11%=\n\t" \
  "s_waitcnt vmcnt(6)\n\t" \
  "s_barrier\n\t" \
  ".Lg11%=:\n\t" \
  "s_add_u32 s92, s92, 65024\n\t" \
  "s_addc_u32 s93, s93, 0\n\t" \
  "s_waitcnt lgkmcnt(6)\n\t" \
  "v_mfma_f32_16x16x32_bf16 v[128:131], v[176:179], v[96:99], v[128:131]\n\t" \
  "v_mfma_f32_16x16x32_bf16 v[136:139], v[176:179], v[100:103], v[136:139]\n\t" \
  "ds_read_b128 v[176:179], v212 offset:0\n\t" \
  "v_mfma_f32_16x16x32_bf16 v[132:135], v[180:183], v[96:99], v[132:135]\n\t" \
  "v_mfma_f32_16x16x32_bf16 v[140:143], v[180:183], v[100:103], v[140:143]\n\t" \
  "ds_read_b128 v[180:183], v212 offset:16384\n\t" \
  "s_waitcnt lgkmcnt(6)\n\t" \
  "v_mfma_f32_16x16x32_bf16 v[128:131], v[184:187], v[104:107], v[128:131]\n\t" \
  "v_mfma_f32_16x16x32_bf16 v[136:139], v[184:187], v[108:111], v[136:139]\n\t" \
  "ds_read_b128 v[184:187], v213 offset:0\n\t" \
  "s_add_u32 m0, %[lw], 0\n\t" \
  "v_mfma_f32_16x16x32_bf16 v[132:135], v[188:191], v[104:107], v[132:135]\n\t" \
  "v_mfma_f32_16x16x32_bf16 v[140:143], v[188:191], v[108:111], v[140:143]\n\t" \
  "ds_read_b128 v[188:191], v213 offset:16384\n\t" \
  "global_load_lds_dwordx4 v216, s[92:93] sc1\n\t" \
  "s_waitcnt lgkmcnt(6)\n\t" \
  "v_mfma_f32_16x16x32_bf16 v[128:131], v[192:195], v[112:115], v[128:131]\n\t" \
  "v_mfma_f32_16x16x32_bf16 v[136:139], v[192:195], v[116:119], v[136:139]\n\t" \
  "ds_read_b128 v[192:195], v214 offset:0\n\t" \
  "v_mfma_f32_16x16x32_bf16 v[132:135], v[196:199], v[112:115], v[132:135]\n\t" \
  "v_mfma_f32_16x16x32_bf16 v[140:143], v[196:199], v[116:119], v[140:143]\n\t" \
  "ds_read_b128 v[196:199], v214 offset:16384\n\t" \
  "s_waitcnt lgkmcnt(6)\n\t" \
  "v_mfma_f32_16x16x32_bf16 v[128:131], v[200:203], v[120:123], v[128:131]\n\t" \
  "v_mfma_f32_16x16x32_bf16 v[136:139], v[200:203], v[124:127], v[136:139]\n\t" \
  "ds_read_b128 v[200:203], v215 offset:0\n\t" \
  "s_add_u32 m0, %[lw], 1024\n\t" \
  "v_mfma_f32_16x16x32_bf16 v[132:135], v[204:207], v[120:123], v[132:135]\n\t" \
  "v_mfma_f32_16x16x32_bf16 v[140:143], v[204:207], v[124:127], v[140:143]\n\t" \
  "ds_read_b128 v[204:207], v215 offset:16384\n\t" \
  "global_load_lds_dwordx4 v217, s[92:93] sc1\n\t" \
  "s_add_u32 s86, s86, 1\n\t" \
  "s_add_u32 s87, s87, 1\n\t" \
  "s_cmp_le_u32 s86, 5\n\t" \
  "s_cbranch_scc1 .Lf11%=\n\t" \
  ".Lfb11%=:\n\t" \
  "s_waitcnt lgkmcnt(6)\n\t" \
  "v_mfma_f32_16x16x32_bf16 v[144:147], v[176:179], v[0:3], 0\n\t" \
  "v_mfma_f32_16x16x32_bf16 v[152:155], v[176:179], v[4:7], 0\n\t" \
  "ds_read_b128 v[176:179], v212 offset:256\n\t" \
  "v_mfma_f32_16x16x32_bf16 v[148:151], v[180:183], v[0:3], 0\n\t" \
  "v_mfma_f32_16x16x32_bf16 v[156:159], v[180:183], v[4:7], 0\n\t" \
  "ds_read_b128 v[180:183], v212 offset:16640\n\t" \
  "s_waitcnt lgkmcnt(6)\n\t" \
  "v_mfma_f32_16x16x32_bf16 v[144:147], v[184:187], v[8:11], v[144:147]\n\t" \
  "v_mfma_f32_16x16x32_bf16 v[152:155], v[184:187], v[12:15], v[152:155]\n\t" \
  "ds_read_b128 v[184:187], v213 offset:256\n\t" \
  "s_add_u32 m0, %[lw], 2048\n\t" \
  "v_mfma_f32_16x16x32_bf16 v[148:151], v[188:191], v[8:11], v[148:151]\n\t" \
  "v_mfma_f32_16x16x32_bf16 v[156:159], v[188:191], v[12:15], v[156:159]\n\t" \
  "ds_read_b128 v[188:191], v213 offset:16640\n\t" \
  "global_load_lds_dwordx4 v218, s[92:93] sc1\n\t" \
  "ds_write_b128 v220, v[136:139] offset:0\n\t" \
  "ds_write_b128 v220, v[140:143] offset:1024\n\t" \
  "s_waitcnt lgkmcnt(8)\n\t" \
  "v_mfma_f32_16x16x32_bf16 v[144:147], v[192:195], v[16:19], v[144:147]\n\t" \
  "v_mfma_f32_16x16x32_bf16 v[152:155], v[192:195], v[20:23], v[152:155]\n\t" \
  "ds_read_b128 v[192:195], v214 offset:256\n\t" \
  "v_mfma_f32_16x16x32_bf16 v[148:151], v[196:199], v[16:19], v[148:151]\n\t" \
  "v_mfma_f32_16x16x32_bf16 v[156:159], v[196:199], v[20:23], v[156:159]\n\t" \
  "ds_read_b128 v[196:199], v214 offset:16640\n\t" \
  "s_lshr_b32 s90, s87, 7\n\t" \
  "s_xor_b32 s90, s90, %[s0]\n\t" \
  "s_and_b32 s90, s90, 1\n\t" \
  "s_lshl1_add_u32 s90, s87, s90\n\t" \
  "s_lshl_b32 s90, s90, 13\n\t" \
  "s_cmp_lt_i32 s86, 6\n\t" \
  "s_cselect_b32 s90, 0x70000000, s90\n\t" \
  "s_waitcnt lgkmcnt(8)\n\t" \
  "v_mfma_f32_16x16x32_bf16 v[144:147], v[200:203], v[24:27], v[144:147]\n\t" \
  "v_mfma_f32_16x16x32_bf16 v[152:155], v[200:203], v[28:31], v[152:155]\n\t" \
  "ds_read_b128 v[200:203], v215 offset:256\n\t" \
  "s_add_u32 m0, %[lw], 3072\n\t" \
  "v_mfma_f32_16x16x32_bf16 v[148:151], v[204:207], v[24:27], v[148:151]\n\t" \
  "v_mfma_f32_16x16x32_bf16 v[156:159], v[204:207], v[28:31], v[156:159]\n\t" \
  "ds_read_b128 v[204:207], v215 offset:16640\n\t" \
  "global_load_lds_dwordx4 v219, s[92:93] sc1\n\t" \
  "s_waitcnt lgkmcnt(4)\n\t" \
  "s_cmp_lt_u32 s86, 6\n\t" \
  "s_cbranch_scc1 .Lw21%=\n\t" \
  "s_waitcnt vmcnt(4)\n\t" \
  "s_barrier\n\t" \
  ".Lg21%=:\n\t" \
  "s_add_u32 s92, s92, 512\n\t" \
  "s_addc_u32 s93, s93, 0\n\t" \
  "v_mfma_f32_16x16x32_bf16 v[144:147], v[176:179], v[32:35], v[144:147]\n\t" \
  "v_mfma_f32_16x16x32_bf16 v[152:155], v[176:179], v[36:39], v[152:155]\n\t" \
  "ds_read_b128 v[176:179], v212 offset:32768\n\t" \
  "v_mfma_f32_16x16x32_bf16 v[148:151], v[180:183], v[32:35], v[148:151]\n\t" \
  "v_mfma_f32_16x16x32_bf16 v[156:159], v[180:183], v[36:39], v[156:159]\n\t" \
  "ds_read_b128 v[180:183], v212 offset:49152\n\t" \
  "ds_read_b128 v[160:163], v221 offset:0\n\t" \
  "ds_read_b128 v[164:167], v221 offset:1024\n\t" \
  "s_sub_i32 s91, s86, 2\n\t" \
  "s_max_i32 s88, s88, s91\n\t" \
  "v_mov_b32 v173, s88\n\t" \
  "buffer_store_dword v173, v174, %[rs], 0 offen sc1\n\t" \
  "v_mfma_f32_16x16x32_bf16 v[144:147], v[184:187], v[40:43], v[144:147]\n\t" \
  "v_mfma_f32_16x16x32_bf16 v[152:155], v[184:187], v[44:47], v[152:155]\n\t" \
  "ds_read_b128 v[184:187], v213 offset:32768\n\t" \
  "s_add_u32 m0, %[lw], 32768\n\t" \
  "v_mfma_f32_16x16x32_bf16 v[148:151], v[188:191], v[40:43], v[148:151]\n\t" \
  "v_mfma_f32_16x16x32_bf16 v[156:159], v[188:191], v[44:47], v[156:159]\n\t" \
  "ds_read_b128 v[188:191], v213 offset:49152\n\t" \
  "global_load_lds_dwordx4 v216, s[92:93] sc1\n\t" \
  "s_waitcnt lgkmcnt(8)\n\t" \
  "v_mfma_f32_16x16x32_bf16 v[144:147], v[192:195], v[48:51], v[144:147]\n\t" \
  "v_mfma_f32_16x16x32_bf16 v[152:155], v[192:195], v[52:55], v[152:155]\n\t" \
  "ds_read_b128 v[192:195], v214 offset:32768\n\t" \
  "v_mfma_f32_16x16x32_bf16 v[148:151], v[196:199], v[48:51], v[148:151]\n\t" \
  "v_mfma_f32_16x16x32_bf16 v[156:159], v[196:199], v[52:55], v[156:159]\n\t" \
  "ds_read_b128 v[196:199], v214 offset:49152\n\t" \
  "s_waitcnt lgkmcnt(4)\n\t" \
  "v_add_f32 v160, v160, v128\n\t" \
  "v_add_f32 v161, v161, v129\n\t" \
  "v_add_f32 v162, v162, v130\n\t" \
  "v_add_f32 v163, v163, v131\n\t" \
  "v_add_f32 v164, v164, v132\n\t" \
  "v_add_f32 v165, v165, v133\n\t" \
  "v_add_f32 v166, v166, v134\n\t" \
  "v_add_f32 v167, v167, v135\n\t" \
  "v_mfma_f32_16x16x32_bf16 v[144:147], v[200:203], v[56:59], v[144:147]\n\t" \
  "v_mfma_f32_16x16x32_bf16 v[152:155], v[200:203], v[60:63], v[152:155]\n\t" \
  "ds_read_b128 v[200:203], v215 offset:32768\n\t" \
  "s_add_u32 m0, %[lw], 33792\n\t" \
  "v_mfma_f32_16x16x32_bf16 v[148:151], v[204:207], v[56:59], v[148:151]\n\t" \
  "v_mfma_f32_16x16x32_bf16 v[156:159], v[204:207], v[60:63], v[156:159]\n\t" \
  "ds_read_b128 v[204:207], v215 offset:49152\n\t" \
  "global_load_lds_dwordx4 v217, s[92:93] sc1\n\t" \
  "v_cvt_pk_bf16_f32 v168, v160, v161\n\t" \
  "v_cvt_pk_bf16_f32 v169, v162, v163\n\t" \
  "v_cvt_pk_bf16_f32 v170, v164, v165\n\t" \
  "v_cvt_pk_bf16_f32 v171, v166, v167\n\t" \
  "buffer_store_dwordx4 v[168:171], v172, %[rt], s90 offen sc1\n\t" \
  "v_mfma_f32_16x16x32_bf16 v[144:147], v[176:179], v[64:67], v[144:147]\n\t" \
  "v_mfma_f32_16x16x32_bf16 v[152:155], v[176:179], v[68:71], v[152:155]\n\t" \
  "ds_read_b128 v[176:179], v212 offset:33024\n\t" \
  "v_mfma_f32_16x16x32_bf16 v[148:151], v[180:183], v[64:67], v[148:151]\n\t" \
  "v_mfma_f32_16x16x32_bf16 v[156:159], v[180:183], v[68:71], v[156:159]\n\t" \
  "ds_read_b128 v[180:183], v212 offset:49408\n\t" \
  "s_waitcnt lgkmcnt(6)\n\t" \
  "v_mfma_f32_16x16x32_bf16 v[144:147], v[184:187], v[72:75], v[144:147]\n\t" \
  "v_mfma_f32_16x16x32_bf16 v[152:155], v[184:187], v[76:79], v[152:155]\n\t" \
  "ds_read_b128 v[184:187], v213 offset:33024\n\t" \
  "s_add_u32 m0, %[lw], 34816\n\t" \
  "v_mfma_f32_16x16x32_bf16 v[148:151], v[188:191], v[72:75], v[148:151]\n\t" \
  "v_mfma_f32_16x16x32_bf16 v[156:159], v[188:191], v[76:79], v[156:159]\n\t" \
  "ds_read_b128 v[188:191], v213 offset:49408\n\t" \
  "global_load_lds_dwordx4 v218, s[92:93] sc1\n\t" \
  "s_waitcnt lgkmcnt(6)\n\t" \
  "v_mfma_f32_16x16x32_bf16 v[144:147], v[192:195], v[80:83], v[144:147]\n\t" \
  "v_mfma_f32_16x16x32_bf16 v[152:155], v[192:195], v[84:87], v[152:155]\n\t" \
  "ds_read_b128 v[192:195], v214 offset:33024\n\t" \
  "v_mfma_f32_16x16x32_bf16 v[148:151], v[196:199], v[80:83], v[148:151]\n\t" \
  "v_mfma_f32_16x16x32_bf16 v[156:159], v[196:199], v[84:87], v[156:159]\n\t" \
  "ds_read_b128 v[196:199], v214 offset:49408\n\t" \
  "s_waitcnt lgkmcnt(6)\n\t" \
  "v_mfma_f32_16x16x32_bf16 v[144:147], v[200:203], v[88:91], v[144:147]\n\t" \
  "v_mfma_f32_16x16x32_bf16 v[152:155], v[200:203], v[92:95], v[152:155]\n\t" \
  "ds_read_b128 v[200:203], v215 offset:33024\n\t" \
  "s_add_u32 m0, %[lw], 35840\n\t" \
  "v_mfma_f32_16x16x32_bf16 v[148:151], v[204:207], v[88:91], v[148:151]\n\t" \
  "v_mfma_f32_16x16x32_bf16 v[156:159], v[204:207], v[92:95], v[156:159]\n\t" \
  "ds_read_b128 v[204:207], v215 offset:49408\n\t" \
  "global_load_lds_dwordx4 v219, s[92:93] sc1\n\t" \
  "s_cmp_lt_u32 s86, 8\n\t" \
  "s_cbranch_scc1 .Lw31%=\n\t" \
  "s_waitcnt vmcnt(6)\n\t" \
  "s_barrier\n\t" \
  ".Lg31%=:\n\t" \
  "s_add_u32 s92, s92, 65024\n\t" \
  "s_addc_u32 s93, s93, 0\n\t" \
  "s_waitcnt lgkmcnt(6)\n\t" \
  "v_mfma_f32_16x16x32_bf16 v[144:147], v[176:179], v[96:99], v[144:147]\n\t" \
  "v_mfma_f32_16x16x32_bf16 v[152:155], v[176:179], v[100:103], v[152:155]\n\t" \
  "ds_read_b128 v[176:179], v208 offset:0\n\t" \
  "v_mfma_f32_16x16x32_bf16 v[148:151], v[180:183], v[96:99], v[148:151]\n\t" \
  "v_mfma_f32_16x16x32_bf16 v[156:159], v[180:183], v[100:103], v[156:159]\n\t" \
  "ds_read_b128 v[180:183], v208 offset:16384\n\t" \
  "s_waitcnt lgkmcnt(6)\n\t" \
  "v_mfma_f32_16x16x32_bf16 v[144:147], v[184:187], v[104:107], v[144:147]\n\t" \
  "v_mfma_f32_16x16x32_bf16 v[152:155], v[184:187], v[108:111], v[152:155]\n\t" \
  "ds_read_b128 v[184:187], v209 offset:0\n\t" \
  "s_add_u32 m0, %[lw], 65536\n\t" \
  "v_mfma_f32_16x16x32_bf16 v[148:151], v[188:191], v[104:107], v[148:151]\n\t" \
  "v_mfma_f32_16x16x32_bf16 v[156:159], v[188:191], v[108:111], v[156:159]\n\t" \
  "ds_read_b128 v[188:191], v209 offset:16384\n\t" \
  "global_load_lds_dwordx4 v216, s[92:93] sc1\n\t" \
  "s_waitcnt lgkmcnt(6)\n\t" \
  "v_mfma_f32_16x16x32_bf16 v[144:147], v[192:195], v[112:115], v[144:147]\n\t" \
  "v_mfma_f32_16x16x32_bf16 v[152:155], v[192:195], v[116:119], v[152:155]\n\t" \
  "ds_read_b128 v[192:195], v210 offset:0\n\t" \
  "v_mfma_f32_16x16x32_bf16 v[148:151], v[196:199], v[112:115], v[148:151]\n\t" \
  "v_mfma_f32_16x16x32_bf16 v[156:159], v[196:199], v[116:119], v[156:159]\n\t" \
  "ds_read_b128 v[196:199], v210 offset:16384\n\t" \
  "s_waitcnt lgkmcnt(6)\n\t" \
  "v_mfma_f32_16x16x32_bf16 v[144:147], v[200:203], v[120:123], v[144:147]\n\t" \
  "v_mfma_f32_16x16x32_bf16 v[152:155], v[200:203], v[124:127], v[152:155]\n\t" \
  "ds_read_b128 v[200:203], v211 offset:0\n\t" \
  "s_add_u32 m0, %[lw], 66560\n\t" \
  "v_mfma_f32_16x16x32_bf16 v[148:151], v[204:207], v[120:123], v[148:151]\n\t" \
  "v_mfma_f32_16x16x32_bf16 v[156:159], v[204:207], v[124:127], v[156:159]\n\t" \
  "ds_read_b128 v[204:207], v211 offset:16384\n\t" \
  "global_load_lds_dwordx4 v217, s[92:93] sc1\n\t" \
  "s_add_u32 s86, s86, 1\n\t" \
  "s_add_u32 s87, s87, 1\n\t" \
  "s_cmp_lt_u32 s86, %[end]\n\t" \
  "s_cbranch_scc1 .Lp1%=\n\t" \
  "s_branch .Lx1%=\n\t" \
  ".Lf01%=:\n\t" \
  "s_nop 7\n\t" \
  "ds_write_b128 v220, v[152:155] offset:0\n\t" \
  "ds_write_b128 v220, v[156:159] offset:1024\n\t" \
  "s_lshr_b32 s90, s87, 7\n\t" \
  "s_xor_b32 s90, s90, %[s0]\n\t" \
  "s_and_b32 s90, s90, 1\n\t" \
  "s_lshl1_add_u32 s90, s87, s90\n\t" \
  "s_lshl_b32 s90, s90, 13\n\t" \
  "s_cmp_lt_i32 s86, 1\n\t" \
  "s_cselect_b32 s90, 0x70000000, s90\n\t" \
  "s_waitcnt lgkmcnt(0)\n\t" \
  "s_barrier\n\t" \
  "ds_read_b128 v[160:163], v221 offset:0\n\t" \
  "ds_read_b128 v[164:167], v221 offset:1024\n\t" \
  "s_waitcnt lgkmcnt(0)\n\t" \
  "v_add_f32 v160, v160, v144\n\t" \
  "v_add_f32 v161, v161, v145\n\t" \
  "v_add_f32 v162, v162, v146\n\t" \
  "v_add_f32 v163, v163, v147\n\t" \
  "v_add_f32 v164, v164, v148\n\t" \
  "v_add_f32 v165, v165, v149\n\t" \
  "v_add_f32 v166, v166, v150\n\t" \
  "v_add_f32 v167, v167, v151\n\t" \
  "v_cvt_pk_bf16_f32 v168, v160, v161\n\t" \
  "v_cvt_pk_bf16_f32 v169, v162, v163\n\t" \
  "v_cvt_pk_bf16_f32 v170, v164, v165\n\t" \
  "v_cvt_pk_bf16_f32 v171, v166, v167\n\t" \
  "buffer_store_dwordx4 v[168:171], v172, %[rt], s90 offen sc1\n\t" \
  "s_branch .Lfb01%=\n\t" \
  ".Lf11%=:\n\t" \
  "s_nop 7\n\t" \
  "ds_write_b128 v220, v[136:139] offset:0\n\t" \
  "ds_write_b128 v220, v[140:143] offset:1024\n\t" \
  "s_lshr_b32 s90, s87, 7\n\t" \
  "s_xor_b32 s90, s90, %[s0]\n\t" \
  "s_and_b32 s90, s90, 1\n\t" \
  "s_lshl1_add_u32 s90, s87, s90\n\t" \
  "s_lshl_b32 s90, s90, 13\n\t" \
  "s_cmp_lt_i32 s86, 1\n\t" \
  "s_cselect_b32 s90, 0x70000000, s90\n\t" \
  "s_waitcnt lgkmcnt(0)\n\t" \
  "s_barrier\n\t" \
  "ds_read_b128 v[160:163], v221 offset:0\n\t" \
  "ds_read_b128 v[164:167], v221 offset:1024\n\t" \
  "s_waitcnt lgkmcnt(0)\n\t" \
  "v_add_f32 v160, v160, v128\n\t" \
  "v_add_f32 v161, v161, v129\n\t" \
  "v_add_f32 v162, v162, v130\n\t" \
  "v_add_f32 v163, v163, v131\n\t" \
  "v_add_f32 v164, v164, v132\n\t" \
  "v_add_f32 v165, v165, v133\n\t" \
  "v_add_f32 v166, v166, v134\n\t" \
  "v_add_f32 v167, v167, v135\n\t" \
  "v_cvt_pk_bf16_f32 v168, v160, v161\n\t" \
  "v_cvt_pk_bf16_f32 v169, v162, v163\n\t" \
  "v_cvt_pk_bf16_f32 v170, v164, v165\n\t" \
  "v_cvt_pk_bf16_f32 v171, v166, v167\n\t" \
  "buffer_store_dwordx4 v[168:171], v172, %[rt], s90 offen sc1\n\t" \
  "s_branch .Lfb11%=\n\t" \
  ".Lw01%=:\n\t" \
  "s_waitcnt vmcnt(2)\n\t" \
  "s_barrier\n\t" \
  "s_sub_i32 s91, s86, 1\n\t" \
  "s_max_i32 s88, s88, s91\n\t" \
  "v_mov_b32 v173, s88\n\t" \
  "buffer_store_dword v173, v174, %[rs], 0 offen sc1\n\t" \
  "s_branch .Lg01%=\n\t" \
  ".Lw11%=:\n\t" \
  "s_waitcnt vmcnt(2)\n\t" \
  "s_barrier\n\t" \
  "s_sub_i32 s91, s86, 1\n\t" \
  "s_max_i32 s88, s88, s91\n\t" \
  "v_mov_b32 v173, s88\n\t" \
  "buffer_store_dword v173, v174, %[rs], 0 offen sc1\n\t" \
  "s_branch .Lg11%=\n\t" \
  ".Lw21%=:\n\t" \
  "s_waitcnt vmcnt(2)\n\t" \
  "s_barrier\n\t" \
  "s_sub_i32 s91, s86, 1\n\t" \
  "s_max_i32 s88, s88, s91\n\t" \
  "v_mov_b32 v173, s88\n\t" \
  "buffer_store_dword v173, v174, %[rs], 0 offen sc1\n\t" \
  "s_branch .Lg21%=\n\t" \
  ".Lw31%=:\n\t" \
  "s_waitcnt vmcnt(2)\n\t" \
  "s_barrier\n\t" \
  "s_sub_i32 s91, s86, 1\n\t" \
  "s_max_i32 s88, s88, s91\n\t" \
  "v_mov_b32 v173, s88\n\t" \
  "buffer_store_dword v173, v174, %[rs], 0 offen sc1\n\t" \
  "s_branch .Lg31%=\n\t" \
  ".Lx1%=:\n\t" \
  "s_waitcnt lgkmcnt(0)\n\t" \
  "s_setprio 0\n\t" \
  "s_mov_b32 m0, s94\n\t" \
  ".Le%=:\n\t"
#define PP_ASM_0 \
  "s_cmp_lg_u32 %[kh], 0\n\t" \
  "s_cbranch_scc1 .Lk%=\n\t" \
  "s_mov_b32 s94, m0\n\t" \
  "s_mov_b64 s[92:93], %[x0]\n\t" \
  "s_mov_b32 s86, %[g0]\n\t" \
  "s_add_u32 s87, s86, %[t0]\n\t" \
  "s_add_u32 m0, %[lw], 67584\n\t" \
  "s_nop 0\n\t" \
  "global_load_lds_dwordx4 v250, s[92:93] sc1\n\t" \
  "s_add_u32 m0, %[lw], 68608\n\t" \
  "s_nop 0\n\t" \
  "global_load_lds_dwordx4 v251, s[92:93] sc1\n\t" \
  "s_barrier\n\t" \
  "s_branch .Ls0%=\n\t" \
  ".Lp0%=:\n\t" \
  "s_waitcnt vmcnt(9)\n\t" \
  "s_barrier\n\t" \
  ".Ls0%=:\n\t" \
  "s_add_u32 s92, s92, 512\n\t" \
  "s_addc_u32 s93, s93, 0\n\t" \
  "ds_read_b128 v[176:179], v240 offset:0\n\t" \
  "ds_read_b128 v[180:183], v240 offset:16384\n\t" \
  "ds_read_b128 v[184:187], v241 offset:0\n\t" \
  "ds_read_b128 v[188:191], v241 offset:16384\n\t" \
  "s_add_u32 m0, %[lw], 98304\n\t" \
  "s_nop 0\n\t" \
  "global_load_lds_dwordx4 v248, s[92:93] sc1\n\t" \
  "ds_read_b128 v[192:195], v242 offset:0\n\t" \
  "ds_read_b128 v[196:199], v242 offset:16384\n\t" \
  "ds_read_b128 v[200:203], v243 offset:0\n\t" \
  "ds_read_b128 v[204:207], v243 offset:16384\n\t" \
  "s_add_u32 m0, %[lw], 99328\n\t" \
  "s_nop 0\n\t" \
  "global_load_lds_dwordx4 v249, s[92:93] sc1\n\t" \
  "ds_read_b128 v[208:211], v240 offset:256\n\t" \
  "ds_read_b128 v[212:215], v240 offset:16640\n\t" \
  "ds_read_b128 v[216:219], v241 offset:256\n\t" \
  "ds_read_b128 v[220:223], v241 offset:16640\n\t" \
  "s_add_u32 m0, %[lw], 100352\n\t" \
  "s_nop 0\n\t" \
  "global_load_lds_dwordx4 v250, s[92:93] sc1\n\t" \
  "ds_read_b128 v[224:227], v242 offset:256\n\t" \
  "ds_read_b128 v[228:231], v242 offset:16640\n\t" \
  "ds_read_b128 v[232:235], v243 offset:256\n\t" \
  "ds_read_b128 v[236:239], v243 offset:16640\n\t" \
  "s_add_u32 m0, %[lw], 101376\n\t" \
  "s_nop 0\n\t" \
  "global_load_lds_dwordx4 v251, s[92:93] sc1\n\t" \
  "ds_write_b128 v252, v[152:155] offset:0\n\t" \
  "ds_write_b128 v252, v[156:159] offset:1024\n\t" \
  "s_waitcnt lgkmcnt(0)\n\t" \
  "s_setprio 1\n\t" \
  "v_mfma_f32_16x16x32_bf16 v[128:131], v[0:3], v[176:179], 0\n\t" \
  "v_mfma_f32_16x16x32_bf16 v[132:135], v[4:7], v[176:179], 0\n\t" \
  "v_mfma_f32_16x16x32_bf16 v[136:139], v[0:3], v[180:183], 0\n\t" \
  "v_mfma_f32_16x16x32_bf16 v[140:143], v[4:7], v[180:183], 0\n\t" \
  "v_mfma_f32_16x16x32_bf16 v[128:131], v[8:11], v[184:187], v[128:131]\n\t" \
  "v_mfma_f32_16x16x32_bf16 v[132:135], v[12:15], v[184:187], v[132:135]\n\t" \
  "v_mfma_f32_16x16x32_bf16 v[136:139], v[8:11], v[188:191], v[136:139]\n\t" \
  "v_mfma_f32_16x16x32_bf16 v[140:143], v[12:15], v[188:191], v[140:143]\n\t" \
  "v_mfma_f32_16x16x32_bf16 v[128:131], v[16:19], v[192:195], v[128:131]\n\t" \
  "v_mfma_f32_16x16x32_bf16 v[132:135], v[20:23], v[192:195], v[132:135]\n\t" \
  "v_mfma_f32_16x16x32_bf16 v[136:139], v[16:19], v[196:199], v[136:139]\n\t" \
  "v_mfma_f32_16x16x32_bf16 v[140:143], v[20:23], v[196:199], v[140:143]\n\t" \
  "v_mfma_f32_16x16x32_bf16 v[128:131], v[24:27], v[200:203], v[128:131]\n\t" \
  "v_mfma_f32_16x16x32_bf16 v[132:135], v[28:31], v[200:203], v[132:135]\n\t" \
  "v_mfma_f32_16x16x32_bf16 v[136:139], v[24:27], v[204:207], v[136:139]\n\t" \
  "v_mfma_f32_16x16x32_bf16 v[140:143], v[28:31], v[204:207], v[140:143]\n\t" \
  "v_mfma_f32_16x16x32_bf16 v[128:131], v[32:35], v[208:211], v[128:131]\n\t" \
  "v_mfma_f32_16x16x32_bf16 v[132:135], v[36:39], v[208:211], v[132:135]\n\t" \
  "v_mfma_f32_16x16x32_bf16 v[136:139], v[32:35], v[212:215], v[136:139]\n\t" \
  "v_mfma_f32_16x16x32_bf16 v[140:143], v[36:39], v[212:215], v[140:143]\n\t" \
  "v_mfma_f32_16x16x32_bf16 v[128:131], v[40:43], v[216:219], v[128:131]\n\t" \
  "v_mfma_f32_16x16x32_bf16 v[132:135], v[44:47], v[216:219], v[132:135]\n\t" \
  "v_mfma_f32_16x16x32_bf16 v[136:139], v[40:43], v[220:223], v[136:139]\n\t" \
  "v_mfma_f32_16x16x32_bf16 v[140:143], v[44:47], v[220:223], v[140:143]\n\t" \
  "v_mfma_f32_16x16x32_bf16 v[128:131], v[48:51], v[224:227], v[128:131]\n\t" \
  "v_mfma_f32_16x16x32_bf16 v[132:135], v[52:55], v[224:227], v[132:135]\n\t" \
  "v_mfma_f32_16x16x32_bf16 v[136:139], v[48:51], v[228:231], v[136:139]\n\t" \
  "v_mfma_f32_16x16x32_bf16 v[140:143], v[52:55], v[228:231], v[140:143]\n\t" \
  "v_mfma_f32_16x16x32_bf16 v[128:131], v[56:59], v[232:235], v[128:131]\n\t" \
  "v_mfma_f32_16x16x32_bf16 v[132:135], v[60:63], v[232:235], v[132:135]\n\t" \
  "v_mfma_f32_16x16x32_bf16 v[136:139], v[56:59], v[236:239], v[136:139]\n\t" \
  "v_mfma_f32_16x16x32_bf16 v[140:143], v[60:63], v[236:239], v[140:143]\n\t" \
  "s_setprio 0\n\t" \
  "s_waitcnt vmcnt(4)\n\t" \
  "s_barrier\n\t" \
  "s_add_u32 s92, s92, 65024\n\t" \
  "s_addc_u32 s93, s93, 0\n\t" \
  "ds_read_b128 v[160:163], v253 offset:0\n\t" \
  "ds_read_b128 v[164:167], v253 offset:1024\n\t" \
  "ds_read_b128 v[176:179], v240 offset:32768\n\t" \
  "ds_read_b128 v[180:183], v240 offset:49152\n\t" \
  "ds_read_b128 v[184:187], v241 offset:32768\n\t" \
  "ds_read_b128 v[188:191], v241 offset:49152\n\t" \
  "s_add_u32 m0, %[lw], 0\n\t" \
  "s_nop 0\n\t" \
  "global_load_lds_dwordx4 v248, s[92:93] sc1\n\t" \
  "ds_read_b128 v[192:195], v242 offset:32768\n\t" \
  "ds_read_b128 v[196:199], v242 offset:49152\n\t" \
  "ds_read_b128 v[200:203], v243 offset:32768\n\t" \
  "ds_read_b128 v[204:207], v243 offset:49152\n\t" \
  "s_add_u32 m0, %[lw], 1024\n\t" \
  "s_nop 0\n\t" \
  "global_load_lds_dwordx4 v249, s[92:93] sc1\n\t" \
  "ds_read_b128 v[208:211], v240 offset:33024\n\t" \
  "ds_read_b128 v[212:215], v240 offset:49408\n\t" \
  "ds_read_b128 v[216:219], v241 offset:33024\n\t" \
  "ds_read_b128 v[220:223], v241 offset:49408\n\t" \
  "s_add_u32 m0, %[lw], 2048\n\t" \
  "s_nop 0\n\t" \
  "global_load_lds_dwordx4 v250, s[92:93] sc1\n\t" \
  "ds_read_b128 v[224:227], v242 offset:33024\n\t" \
  "ds_read_b128 v[228:231], v242 offset:49408\n\t" \
  "ds_read_b128 v[232:235], v243 offset:33024\n\t" \
  "ds_read_b128 v[236:239], v243 offset:49408\n\t" \
  "s_add_u32 m0, %[lw], 3072\n\t" \
  "s_nop 0\n\t" \
  "global_load_lds_dwordx4 v251, s[92:93] sc1\n\t" \
  "s_waitcnt lgkmcnt(15)\n\t" \
  "s_waitcnt lgkmcnt(12)\n\t" \
  "s_lshr_b32 s90, s87, 7\n\t" \
  "s_xor_b32 s90, s90, %[s0]\n\t" \
  "s_and_b32 s90, s90, 1\n\t" \
  "s_lshl1_add_u32 s90, s87, s90\n\t" \
  "s_lshl_b32 s90, s90, 13\n\t" \
  "v_add_f32 v160, v160, v144\n\t" \
  "v_add_f32 v161, v161, v145\n\t" \
  "v_add_f32 v162, v162, v146\n\t" \
  "v_add_f32 v163, v163, v147\n\t" \
  "v_add_f32 v164, v164, v148\n\t" \
  "v_add_f32 v165, v165, v149\n\t" \
  "v_add_f32 v166, v166, v150\n\t" \
  "v_add_f32 v167, v167, v151\n\t" \
  "v_cvt_pk_bf16_f32 v168, v160, v161\n\t" \
  "v_cvt_pk_bf16_f32 v169, v162, v163\n\t" \
  "v_cvt_pk_bf16_f32 v170, v164, v165\n\t" \
  "v_cvt_pk_bf16_f32 v171, v166, v167\n\t" \
  "buffer_store_dwordx4 v[168:171], v172, %[rt], s90 offen sc1\n\t" \
  "s_add_u32 s86, s86, 1\n\t" \
  "s_add_u32 s87, s87, 1\n\t" \
  "s_waitcnt lgkmcnt(0)\n\t" \
  "s_setprio 1\n\t" \
  "v_mfma_f32_16x16x32_bf16 v[128:131], v[64:67], v[176:179], v[128:131]\n\t" \
  "v_mfma_f32_16x16x32_bf16 v[132:135], v[68:71], v[176:179], v[132:135]\n\t" \
  "v_mfma_f32_16x16x32_bf16 v[136:139], v[64:67], v[180:183], v[136:139]\n\t" \
  "v_mfma_f32_16x16x32_bf16 v[140:143], v[68:71], v[180:183], v[140:143]\n\t" \
  "v_mfma_f32_16x16x32_bf16 v[128:131], v[72:75], v[184:187], v[128:131]\n\t" \
  "v_mfma_f32_16x16x32_bf16 v[132:135], v[76:79], v[184:187], v[132:135]\n\t" \
  "v_mfma_f32_16x16x32_bf16 v[136:139], v[72:75], v[188:191], v[136:139]\n\t" \
  "v_mfma_f32_16x16x32_bf16 v[140:143], v[76:79], v[188:191], v[140:143]\n\t" \
  "v_mfma_f32_16x16x32_bf16 v[128:131], v[80:83], v[192:195], v[128:131]\n\t" \
  "v_mfma_f32_16x16x32_bf16 v[132:135], v[84:87], v[192:195], v[132:135]\n\t" \
  "v_mfma_f32_16x16x32_bf16 v[136:139], v[80:83], v[196:199], v[136:139]\n\t" \
  "v_mfma_f32_16x16x32_bf16 v[140:143], v[84:87], v[196:199], v[140:143]\n\t" \
  "v_mfma_f32_16x16x32_bf16 v[128:131], v[88:91], v[200:203], v[128:131]\n\t" \
  "v_mfma_f32_16x16x32_bf16 v[132:135], v[92:95], v[200:203], v[132:135]\n\t" \
  "v_mfma_f32_16x16x32_bf16 v[136:139], v[88:91], v[204:207], v[136:139]\n\t" \
  "v_mfma_f32_16x16x32_bf16 v[140:143], v[92:95], v[204:207], v[140:143]\n\t" \
  "v_mfma_f32_16x16x32_bf16 v[128:131], v[96:99], v[208:211], v[128:131]\n\t" \
  "v_mfma_f32_16x16x32_bf16 v[132:135], v[100:103], v[208:211], v[132:135]\n\t" \
  "v_mfma_f32_16x16x32_bf16 v[136:139], v[96:99], v[212:215], v[136:139]\n\t" \
  "v_mfma_f32_16x16x32_bf16 v[140:143], v[100:103], v[212:215], v[140:143]\n\t" \
  "v_mfma_f32_16x16x32_bf16 v[128:131], v[104:107], v[216:219], v[128:131]\n\t" \
  "v_mfma_f32_16x16x32_bf16 v[132:135], v[108:111], v[216:219], v[132:135]\n\t" \
  "v_mfma_f32_16x16x32_bf16 v[136:139], v[104:107], v[220:223], v[136:139]\n\t" \
  "v_mfma_f32_16x16x32_bf16 v[140:143], v[108:111], v[220:223], v[140:143]\n\t" \
  "v_mfma_f32_16x16x32_bf16 v[128:131], v[112:115], v[224:227], v[128:131]\n\t" \
  "v_mfma_f32_16x16x32_bf16 v[132:135], v[116:119], v[224:227], v[132:135]\n\t" \
  "v_mfma_f32_16x16x32_bf16 v[136:139], v[112:115], v[228:231], v[136:139]\n\t" \
  "v_mfma_f32_16x16x32_bf16 v[140:143], v[116:119], v[228:231], v[140:143]\n\t" \
  "v_mfma_f32_16x16x32_bf16 v[128:131], v[120:123], v[232:235], v[128:131]\n\t" \
  "v_mfma_f32_16x16x32_bf16 v[132:135], v[124:127], v[232:235], v[132:135]\n\t" \
  "v_mfma_f32_16x16x32_bf16 v[136:139], v[120:123], v[236:239], v[136:139]\n\t" \
  "v_mfma_f32_16x16x32_bf16 v[140:143], v[124:127], v[236:239], v[140:143]\n\t" \
  "s_setprio 0\n\t" \
  "s_waitcnt vmcnt(9)\n\t" \
  "s_barrier\n\t" \
  "s_add_u32 s92, s92, 512\n\t" \
  "s_addc_u32 s93, s93, 0\n\t" \
  "ds_read_b128 v[176:179], v244 offset:0\n\t" \
  "ds_read_b128 v[180:183], v244 offset:16384\n\t" \
  "ds_read_b128 v[184:187], v245 offset:0\n\t" \
  "ds_read_b128 v[188:191], v245 offset:16384\n\t" \
  "s_add_u32 m0, %[lw], 32768\n\t" \
  "s_nop 0\n\t" \
  "global_load_lds_dwordx4 v248, s[92:93] sc1\n\t" \
  "ds_read_b128 v[192:195], v246 offset:0\n\t" \
  "ds_read_b128 v[196:199], v246 offset:16384\n\t" \
  "ds_read_b128 v[200:203], v247 offset:0\n\t" \
  "ds_read_b128 v[204:207], v247 offset:16384\n\t" \
  "s_add_u32 m0, %[lw], 33792\n\t" \
  "s_nop 0\n\t" \
  "global_load_lds_dwordx4 v249, s[92:93] sc1\n\t" \
  "ds_read_b128 v[208:211], v244 offset:256\n\t" \
  "ds_read_b128 v[212:215], v244 offset:16640\n\t" \
  "ds_read_b128 v[216:219], v245 offset:256\n\t" \
  "ds_read_b128 v[220:223], v245 offset:16640\n\t" \
  "s_add_u32 m0, %[lw], 34816\n\t" \
  "s_nop 0\n\t" \
  "global_load_lds_dwordx4 v250, s[92:93] sc1\n\t" \
  "ds_read_b128 v[224:227], v246 offset:256\n\t" \
  "ds_read_b128 v[228:231], v246 offset:16640\n\t" \
  "ds_read_b128 v[232:235], v247 offset:256\n\t" \
  "ds_read_b128 v[236:239], v247 offset:16640\n\t" \
  "s_add_u32 m0, %[lw], 35840\n\t" \
  "s_nop 0\n\t" \
  "global_load_lds_dwordx4 v251, s[92:93] sc1\n\t" \
  "ds_write_b128 v252, v[136:139] offset:0\n\t" \
  "ds_write_b128 v252, v[140:143] offset:1024\n\t" \
  "s_waitcnt lgkmcnt(0)\n\t" \
  "s_setprio 1\n\t" \
  "v_mfma_f32_16x16x32_bf16 v[144:147], v[0:3], v[176:179], 0\n\t" \
  "v_mfma_f32_16x16x32_bf16 v[148:151], v[4:7], v[176:179], 0\n\t" \
  "v_mfma_f32_16x16x32_bf16 v[152:155], v[0:3], v[180:183], 0\n\t" \
  "v_mfma_f32_16x16x32_bf16 v[156:159], v[4:7], v[180:183], 0\n\t" \
  "v_mfma_f32_16x16x32_bf16 v[144:147], v[8:11], v[184:187], v[144:147]\n\t" \
  "v_mfma_f32_16x16x32_bf16 v[148:151], v[12:15], v[184:187], v[148:151]\n\t" \
  "v_mfma_f32_16x16x32_bf16 v[152:155], v[8:11], v[188:191], v[152:155]\n\t" \
  "v_mfma_f32_16x16x32_bf16 v[156:159], v[12:15], v[188:191], v[156:159]\n\t" \
  "v_mfma_f32_16x16x32_bf16 v[144:147], v[16:19], v[192:195], v[144:147]\n\t" \
  "v_mfma_f32_16x16x32_bf16 v[148:151], v[20:23], v[192:195], v[148:151]\n\t" \
  "v_mfma_f32_16x16x32_bf16 v[152:155], v[16:19], v[196:199], v[152:155]\n\t" \
  "v_mfma_f32_16x16x32_bf16 v[156:159], v[20:23], v[196:199], v[156:159]\n\t" \
  "v_mfma_f32_16x16x32_bf16 v[144:147], v[24:27], v[200:203], v[144:147]\n\t" \
  "v_mfma_f32_16x16x32_bf16 v[148:151], v[28:31], v[200:203], v[148:151]\n\t" \
  "v_mfma_f32_16x16x32_bf16 v[152:155], v[24:27], v[204:207], v[152:155]\n\t" \
  "v_mfma_f32_16x16x32_bf16 v[156:159], v[28:31], v[204:207], v[156:159]\n\t" \
  "v_mfma_f32_16x16x32_bf16 v[144:147], v[32:35], v[208:211], v[144:147]\n\t" \
  "v_mfma_f32_16x16x32_bf16 v[148:151], v[36:39], v[208:211], v[148:151]\n\t" \
  "v_mfma_f32_16x16x32_bf16 v[152:155], v[32:35], v[212:215], v[152:155]\n\t" \
  "v_mfma_f32_16x16x32_bf16 v[156:159], v[36:39], v[212:215], v[156:159]\n\t" \
  "v_mfma_f32_16x16x32_bf16 v[144:147], v[40:43], v[216:219], v[144:147]\n\t" \
  "v_mfma_f32_16x16x32_bf16 v[148:151], v[44:47], v[216:219], v[148:151]\n\t" \
  "v_mfma_f32_16x16x32_bf16 v[152:155], v[40:43], v[220:223], v[152:155]\n\t" \
  "v_mfma_f32_16x16x32_bf16 v[156:159], v[44:47], v[220:223], v[156:159]\n\t" \
  "v_mfma_f32_16x16x32_bf16 v[144:147], v[48:51], v[224:227], v[144:147]\n\t" \
  "v_mfma_f32_16x16x32_bf16 v[148:151], v[52:55], v[224:227], v[148:151]\n\t" \
  "v_mfma_f32_16x16x32_bf16 v[152:155], v[48:51], v[228:231], v[152:155]\n\t" \
  "v_mfma_f32_16x16x32_bf16 v[156:159], v[52:55], v[228:231], v[156:159]\n\t" \
  "v_mfma_f32_16x16x32_bf16 v[144:147], v[56:59], v[232:235], v[144:147]\n\t" \
  "v_mfma_f32_16x16x32_bf16 v[148:151], v[60:63], v[232:235], v[148:151]\n\t" \
  "v_mfma_f32_16x16x32_bf16 v[152:155], v[56:59], v[236:239], v[152:155]\n\t" \
  "v_mfma_f32_16x16x32_bf16 v[156:159], v[60:63], v[236:239], v[156:159]\n\t" \
  "s_setprio 0\n\t" \
  "s_waitcnt vmcnt(4)\n\t" \
  "s_barrier\n\t" \
  "s_add_u32 s92, s92, 65024\n\t" \
  "s_addc_u32 s93, s93, 0\n\t" \
  "ds_read_b128 v[160:163], v253 offset:0\n\t" \
  "ds_read_b128 v[164:167], v253 offset:1024\n\t" \
  "ds_read_b128 v[176:179], v244 offset:32768\n\t" \
  "ds_read_b128 v[180:183], v244 offset:49152\n\t" \
  "ds_read_b128 v[184:187], v245 offset:32768\n\t" \
  "ds_read_b128 v[188:191], v245 offset:49152\n\t" \
  "s_add_u32 m0, %[lw], 65536\n\t" \
  "s_nop 0\n\t" \
  "global_load_lds_dwordx4 v248, s[92:93] sc1\n\t" \
  "ds_read_b128 v[192:195], v246 offset:32768\n\t" \
  "ds_read_b128 v[196:199], v246 offset:49152\n\t" \
  "ds_read_b128 v[200:203], v247 offset:32768\n\t" \
  "ds_read_b128 v[204:207], v247 offset:49152\n\t" \
  "s_add_u32 m0, %[lw], 66560\n\t" \
  "s_nop 0\n\t" \
  "global_load_lds_dwordx4 v249, s[92:93] sc1\n\t" \
  "ds_read_b128 v[208:211], v244 offset:33024\n\t" \
  "ds_read_b128 v[212:215], v244 offset:49408\n\t" \
  "ds_read_b128 v[216:219], v245 offset:33024\n\t" \
  "ds_read_b128 v[220:223], v245 offset:49408\n\t" \
  "s_add_u32 m0, %[lw], 67584\n\t" \
  "s_nop 0\n\t" \
  "global_load_lds_dwordx4 v250, s[92:93] sc1\n\t" \
  "ds_read_b128 v[224:227], v246 offset:33024\n\t" \
  "ds_read_b128 v[228:231], v246 offset:49408\n\t" \
  "ds_read_b128 v[232:235], v247 offset:33024\n\t" \
  "ds_read_b128 v[236:239], v247 offset:49408\n\t" \
  "s_add_u32 m0, %[lw], 68608\n\t" \
  "s_nop 0\n\t" \
  "global_load_lds_dwordx4 v251, s[92:93] sc1\n\t" \
  "s_waitcnt lgkmcnt(15)\n\t" \
  "s_waitcnt lgkmcnt(12)\n\t" \
  "s_lshr_b32 s90, s87, 7\n\t" \
  "s_xor_b32 s90, s90, %[s0]\n\t" \
  "s_and_b32 s90, s90, 1\n\t" \
  "s_lshl1_add_u32 s90, s87, s90\n\t" \
  "s_lshl_b32 s90, s90, 13\n\t" \
  "v_add_f32 v160, v160, v128\n\t" \
  "v_add_f32 v161, v161, v129\n\t" \
  "v_add_f32 v162, v162, v130\n\t" \
  "v_add_f32 v163, v163, v131\n\t" \
  "v_add_f32 v164, v164, v132\n\t" \
  "v_add_f32 v165, v165, v133\n\t" \
  "v_add_f32 v166, v166, v134\n\t" \
  "v_add_f32 v167, v167, v135\n\t" \
  "v_cvt_pk_bf16_f32 v168, v160, v161\n\t" \
  "v_cvt_pk_bf16_f32 v169, v162, v163\n\t" \
  "v_cvt_pk_bf16_f32 v170, v164, v165\n\t" \
  "v_cvt_pk_bf16_f32 v171, v166, v167\n\t" \
  "buffer_store_dwordx4 v[168:171], v172, %[rt], s90 offen sc1\n\t" \
  "s_add_u32 s86, s86, 1\n\t" \
  "s_add_u32 s87, s87, 1\n\t" \
  "s_waitcnt lgkmcnt(0)\n\t" \
  "s_setprio 1\n\t" \
  "v_mfma_f32_16x16x32_bf16 v[144:147], v[64:67], v[176:179], v[144:147]\n\t" \
  "v_mfma_f32_16x16x32_bf16 v[148:151], v[68:71], v[176:179], v[148:151]\n\t" \
  "v_mfma_f32_16x16x32_bf16 v[152:155], v[64:67], v[180:183], v[152:155]\n\t" \
  "v_mfma_f32_16x16x32_bf16 v[156:159], v[68:71], v[180:183], v[156:159]\n\t" \
  "v_mfma_f32_16x16x32_bf16 v[144:147], v[72:75], v[184:187], v[144:147]\n\t" \
  "v_mfma_f32_16x16x32_bf16 v[148:151], v[76:79], v[184:187], v[148:151]\n\t" \
  "v_mfma_f32_16x16x32_bf16 v[152:155], v[72:75], v[188:191], v[152:155]\n\t" \
  "v_mfma_f32_16x16x32_bf16 v[156:159], v[76:79], v[188:191], v[156:159]\n\t" \
  "v_mfma_f32_16x16x32_bf16 v[144:147], v[80:83], v[192:195], v[144:147]\n\t" \
  "v_mfma_f32_16x16x32_bf16 v[148:151], v[84:87], v[192:195], v[148:151]\n\t" \
  "v_mfma_f32_16x16x32_bf16 v[152:155], v[80:83], v[196:199], v[152:155]\n\t" \
  "v_mfma_f32_16x16x32_bf16 v[156:159], v[84:87], v[196:199], v[156:159]\n\t" \
  "v_mfma_f32_16x16x32_bf16 v[144:147], v[88:91], v[200:203], v[144:147]\n\t" \
  "v_mfma_f32_16x16x32_bf16 v[148:151], v[92:95], v[200:203], v[148:151]\n\t" \
  "v_mfma_f32_16x16x32_bf16 v[152:155], v[88:91], v[204:207], v[152:155]\n\t" \
  "v_mfma_f32_16x16x32_bf16 v[156:159], v[92:95], v[204:207], v[156:159]\n\t" \
  "v_mfma_f32_16x16x32_bf16 v[144:147], v[96:99], v[208:211], v[144:147]\n\t" \
  "v_mfma_f32_16x16x32_bf16 v[148:151], v[100:103], v[208:211], v[148:151]\n\t" \
  "v_mfma_f32_16x16x32_bf16 v[152:155], v[96:99], v[212:215], v[152:155]\n\t" \
  "v_mfma_f32_16x16x32_bf16 v[156:159], v[100:103], v[212:215], v[156:159]\n\t" \
  "v_mfma_f32_16x16x32_bf16 v[144:147], v[104:107], v[216:219], v[144:147]\n\t" \
  "v_mfma_f32_16x16x32_bf16 v[148:151], v[108:111], v[216:219], v[148:151]\n\t" \
  "v_mfma_f32_16x16x32_bf16 v[152:155], v[104:107], v[220:223], v[152:155]\n\t" \
  "v_mfma_f32_16x16x32_bf16 v[156:159], v[108:111], v[220:223], v[156:159]\n\t" \
  "v_mfma_f32_16x16x32_bf16 v[144:147], v[112:115], v[224:227], v[144:147]\n\t" \
  "v_mfma_f32_16x16x32_bf16 v[148:151], v[116:119], v[224:227], v[148:151]\n\t" \
  "v_mfma_f32_16x16x32_bf16 v[152:155], v[112:115], v[228:231], v[152:155]\n\t" \
  "v_mfma_f32_16x16x32_bf16 v[156:159], v[116:119], v[228:231], v[156:159]\n\t" \
  "v_mfma_f32_16x16x32_bf16 v[144:147], v[120:123], v[232:235], v[144:147]\n\t" \
  "v_mfma_f32_16x16x32_bf16 v[148:151], v[124:127], v[232:235], v[148:151]\n\t" \
  "v_mfma_f32_16x16x32_bf16 v[152:155], v[120:123], v[236:239], v[152:155]\n\t" \
  "v_mfma_f32_16x16x32_bf16 v[156:159], v[124:127], v[236:239], v[156:159]\n\t" \
  "s_setprio 0\n\t" \
  "s_cmp_lt_u32 s86, 64\n\t" \
  "s_cbranch_scc1 .Lp0%=\n\t" \
  "s_barrier\n\t" \
  "s_nop 2\n\t" \
  "ds_write_b128 v252, v[152:155] offset:0\n\t" \
  "ds_write_b128 v252, v[156:159] offset:1024\n\t" \
  "s_waitcnt lgkmcnt(0)\n\t" \
  "s_barrier\n\t" \
  "ds_read_b128 v[160:163], v253 offset:0\n\t" \
  "ds_read_b128 v[164:167], v253 offset:1024\n\t" \
  "s_waitcnt lgkmcnt(0)\n\t" \
  "s_lshr_b32 s90, s87, 7\n\t" \
  "s_xor_b32 s90, s90, %[s0]\n\t" \
  "s_and_b32 s90, s90, 1\n\t" \
  "s_lshl1_add_u32 s90, s87, s90\n\t" \
  "s_lshl_b32 s90, s90, 13\n\t" \
  "v_add_f32 v160, v160, v144\n\t" \
  "v_add_f32 v161, v161, v145\n\t" \
  "v_add_f32 v162, v162, v146\n\t" \
  "v_add_f32 v163, v163, v147\n\t" \
  "v_add_f32 v164, v164, v148\n\t" \
  "v_add_f32 v165, v165, v149\n\t" \
  "v_add_f32 v166, v166, v150\n\t" \
  "v_add_f32 v167, v167, v151\n\t" \
  "v_cvt_pk_bf16_f32 v168, v160, v161\n\t" \
  "v_cvt_pk_bf16_f32 v169, v162, v163\n\t" \
  "v_cvt_pk_bf16_f32 v170, v164, v165\n\t" \
  "v_cvt_pk_bf16_f32 v171, v166, v167\n\t" \
  "buffer_store_dwordx4 v[168:171], v172, %[rt], s90 offen sc1\n\t" \
  "s_waitcnt vmcnt(0)\n\t" \
  "s_barrier\n\t" \
  "s_mov_b32 m0, s94\n\t" \
  "s_branch .Le%=\n\t" \
  ".Lk%=:\n\t" \
  "s_mov_b32 s94, m0\n\t" \
  "s_mov_b64 s[92:93], %[x0]\n\t" \
  "s_mov_b32 s86, %[g0]\n\t" \
  "s_add_u32 s87, s86, %[t0]\n\t" \
  "s_add_u32 m0, %[lw], 67584\n\t" \
  "s_nop 0\n\t" \
  "global_load_lds_dwordx4 v250, s[92:93] sc1\n\t" \
  "s_add_u32 m0, %[lw], 68608\n\t" \
  "s_nop 0\n\t" \
  "global_load_lds_dwordx4 v251, s[92:93] sc1\n\t" \
  "s_barrier\n\t" \
  "s_branch .Ls1%=\n\t" \
  ".Lp1%=:\n\t" \
  "s_waitcnt vmcnt(10)\n\t" \
  "s_barrier\n\t" \
  "s_setprio 2\n\t" \
  "v_mfma_f32_16x16x32_bf16 v[144:147], v[64:67], v[176:179], v[144:147]\n\t" \
  "v_mfma_f32_16x16x32_bf16 v[148:151], v[68:71], v[176:179], v[148:151]\n\t" \
  "v_mfma_f32_16x16x32_bf16 v[152:155], v[64:67], v[180:183], v[152:155]\n\t" \
  "v_mfma_f32_16x16x32_bf16 v[156:159], v[68:71], v[180:183], v[156:159]\n\t" \
  "v_mfma_f32_16x16x32_bf16 v[144:147], v[72:75], v[184:187], v[144:147]\n\t" \
  "v_mfma_f32_16x16x32_bf16 v[148:151], v[76:79], v[184:187], v[148:151]\n\t" \
  "v_mfma_f32_16x16x32_bf16 v[152:155], v[72:75], v[188:191], v[152:155]\n\t" \
  "v_mfma_f32_16x16x32_bf16 v[156:159], v[76:79], v[188:191], v[156:159]\n\t" \
  "v_mfma_f32_16x16x32_bf16 v[144:147], v[80:83], v[192:195], v[144:147]\n\t" \
  "v_mfma_f32_16x16x32_bf16 v[148:151], v[84:87], v[192:195], v[148:151]\n\t" \
  "v_mfma_f32_16x16x32_bf16 v[152:155], v[80:83], v[196:199], v[152:155]\n\t" \
  "v_mfma_f32_16x16x32_bf16 v[156:159], v[84:87], v[196:199], v[156:159]\n\t" \
  "v_mfma_f32_16x16x32_bf16 v[144:147], v[88:91], v[200:203], v[144:147]\n\t" \
  "v_mfma_f32_16x16x32_bf16 v[148:151], v[92:95], v[200:203], v[148:151]\n\t" \
  "v_mfma_f32_16x16x32_bf16 v[152:155], v[88:91], v[204:207], v[152:155]\n\t" \
  "v_mfma_f32_16x16x32_bf16 v[156:159], v[92:95], v[204:207], v[156:159]\n\t" \
  "v_mfma_f32_16x16x32_bf16 v[144:147], v[96:99], v[208:211], v[144:147]\n\t" \
  "v_mfma_f32_16x16x32_bf16 v[148:151], v[100:103], v[208:211], v[148:151]\n\t" \
  "v_mfma_f32_16x16x32_bf16 v[152:155], v[96:99], v[212:215], v[152:155]\n\t" \
  "v_mfma_f32_16x16x32_bf16 v[156:159], v[100:103], v[212:215], v[156:159]\n\t" \
  "v_mfma_f32_16x16x32_bf16 v[144:147], v[104:107], v[216:219], v[144:147]\n\t" \
  "v_mfma_f32_16x16x32_bf16 v[148:151], v[108:111], v[216:219], v[148:151]\n\t" \
  "v_mfma_f32_16x16x32_bf16 v[152:155], v[104:107], v[220:223], v[152:155]\n\t" \
  "v_mfma_f32_16x16x32_bf16 v[156:159], v[108:111], v[220:223], v[156:159]\n\t" \
  "v_mfma_f32_16x16x32_bf16 v[144:147], v[112:115], v[224:227], v[144:147]\n\t" \
  "v_mfma_f32_16x16x32_bf16 v[148:151], v[116:119], v[224:227], v[148:151]\n\t" \
  "v_mfma_f32_16x16x32_bf16 v[152:155], v[112:115], v[228:231], v[152:155]\n\t" \
  "v_mfma_f32_16x16x32_bf16 v[156:159], v[116:119], v[228:231], v[156:159]\n\t" \
  "v_mfma_f32_16x16x32_bf16 v[144:147], v[120:123], v[232:235], v[144:147]\n\t" \
  "v_mfma_f32_16x16x32_bf16 v[148:151], v[124:127], v[232:235], v[148:151]\n\t" \
  "v_mfma_f32_16x16x32_bf16 v[152:155], v[120:123], v[236:239], v[152:155]\n\t" \
  "v_mfma_f32_16x16x32_bf16 v[156:159], v[124:127], v[236:239], v[156:159]\n\t" \
  "s_setprio 0\n\t" \
  ".Ls1%=:\n\t" \
  "s_add_u32 s92, s92, 512\n\t" \
  "s_addc_u32 s93, s93, 0\n\t" \
  "ds_read_b128 v[176:179], v240 offset:0\n\t" \
  "ds_read_b128 v[180:183], v240 offset:16384\n\t" \
  "ds_read_b128 v[184:187], v241 offset:0\n\t" \
  "ds_read_b128 v[188:191], v241 offset:16384\n\t" \
  "s_add_u32 m0, %[lw], 98304\n\t" \
  "s_nop 0\n\t" \
  "global_load_lds_dwordx4 v248, s[92:93] sc1\n\t" \
  "ds_read_b128 v[192:195], v242 offset:0\n\t" \
  "ds_read_b128 v[196:199], v242 offset:16384\n\t" \
  "ds_read_b128 v[200:203], v243 offset:0\n\t" \
  "ds_read_b128 v[204:207], v243 offset:16384\n\t" \
  "s_add_u32 m0, %[lw], 99328\n\t" \
  "s_nop 0\n\t" \
  "global_load_lds_dwordx4 v249, s[92:93] sc1\n\t" \
  "ds_read_b128 v[208:211], v240 offset:256\n\t" \
  "ds_read_b128 v[212:215], v240 offset:16640\n\t" \
  "ds_read_b128 v[216:219], v241 offset:256\n\t" \
  "ds_read_b128 v[220:223], v241 offset:16640\n\t" \
  "s_add_u32 m0, %[lw], 100352\n\t" \
  "s_nop 0\n\t" \
  "global_load_lds_dwordx4 v250, s[92:93] sc1\n\t" \
  "ds_read_b128 v[224:227], v242 offset:256\n\t" \
  "ds_read_b128 v[228:231], v242 offset:16640\n\t" \
  "ds_read_b128 v[232:235], v243 offset:256\n\t" \
  "ds_read_b128 v[236:239], v243 offset:16640\n\t" \
  "s_add_u32 m0, %[lw], 101376\n\t" \
  "s_nop 0\n\t" \
  "global_load_lds_dwordx4 v251, s[92:93] sc1\n\t" \
  "ds_write_b128 v252, v[152:155] offset:0\n\t" \
  "ds_write_b128 v252, v[156:159] offset:1024\n\t" \
  "s_waitcnt lgkmcnt(0)\n\t" \
  "s_waitcnt vmcnt(4)\n\t" \
  "s_barrier\n\t" \
  "s_setprio 2\n\t" \
  "v_mfma_f32_16x16x32_bf16 v[128:131], v[0:3], v[176:179], 0\n\t" \
  "v_mfma_f32_16x16x32_bf16 v[132:135], v[4:7], v[176:179], 0\n\t" \
  "v_mfma_f32_16x16x32_bf16 v[136:139], v[0:3], v[180:183], 0\n\t" \
  "v_mfma_f32_16x16x32_bf16 v[140:143], v[4:7], v[180:183], 0\n\t" \
  "v_mfma_f32_16x16x32_bf16 v[128:131], v[8:11], v[184:187], v[128:131]\n\t" \
  "v_mfma_f32_16x16x32_bf16 v[132:135], v[12:15], v[184:187], v[132:135]\n\t" \
  "v_mfma_f32_16x16x32_bf16 v[136:139], v[8:11], v[188:191], v[136:139]\n\t" \
  "v_mfma_f32_16x16x32_bf16 v[140:143], v[12:15], v[188:191], v[140:143]\n\t" \
  "v_mfma_f32_16x16x32_bf16 v[128:131], v[16:19], v[192:195], v[128:131]\n\t" \
  "v_mfma_f32_16x16x32_bf16 v[132:135], v[20:23], v[192:195], v[132:135]\n\t" \
  "v_mfma_f32_16x16x32_bf16 v[136:139], v[16:19], v[196:199], v[136:139]\n\t" \
  "v_mfma_f32_16x16x32_bf16 v[140:143], v[20:23], v[196:199], v[140:143]\n\t" \
  "v_mfma_f32_16x16x32_bf16 v[128:131], v[24:27], v[200:203], v[128:131]\n\t" \
  "v_mfma_f32_16x16x32_bf16 v[132:135], v[28:31], v[200:203], v[132:135]\n\t" \
  "v_mfma_f32_16x16x32_bf16 v[136:139], v[24:27], v[204:207], v[136:139]\n\t" \
  "v_mfma_f32_16x16x32_bf16 v[140:143], v[28:31], v[204:207], v[140:143]\n\t" \
  "v_mfma_f32_16x16x32_bf16 v[128:131], v[32:35], v[208:211], v[128:131]\n\t" \
  "v_mfma_f32_16x16x32_bf16 v[132:135], v[36:39], v[208:211], v[132:135]\n\t" \
  "v_mfma_f32_16x16x32_bf16 v[136:139], v[32:35], v[212:215], v[136:139]\n\t" \
  "v_mfma_f32_16x16x32_bf16 v[140:143], v[36:39], v[212:215], v[140:143]\n\t" \
  "v_mfma_f32_16x16x32_bf16 v[128:131], v[40:43], v[216:219], v[128:131]\n\t" \
  "v_mfma_f32_16x16x32_bf16 v[132:135], v[44:47], v[216:219], v[132:135]\n\t" \
  "v_mfma_f32_16x16x32_bf16 v[136:139], v[40:43], v[220:223], v[136:139]\n\t" \
  "v_mfma_f32_16x16x32_bf16 v[140:143], v[44:47], v[220:223], v[140:143]\n\t" \
  "v_mfma_f32_16x16x32_bf16 v[128:131], v[48:51], v[224:227], v[128:131]\n\t" \
  "v_mfma_f32_16x16x32_bf16 v[132:135], v[52:55], v[224:227], v[132:135]\n\t" \
  "v_mfma_f32_16x16x32_bf16 v[136:139], v[48:51], v[228:231], v[136:139]\n\t" \
  "v_mfma_f32_16x16x32_bf16 v[140:143], v[52:55], v[228:231], v[140:143]\n\t" \
  "v_mfma_f32_16x16x32_bf16 v[128:131], v[56:59], v[232:235], v[128:131]\n\t" \
  "v_mfma_f32_16x16x32_bf16 v[132:135], v[60:63], v[232:235], v[132:135]\n\t" \
  "v_mfma_f32_16x16x32_bf16 v[136:139], v[56:59], v[236:239], v[136:139]\n\t" \
  "v_mfma_f32_16x16x32_bf16 v[140:143], v[60:63], v[236:239], v[140:143]\n\t" \
  "s_setprio 0\n\t" \
  "s_add_u32 s92, s92, 65024\n\t" \
  "s_addc_u32 s93, s93, 0\n\t" \
  "ds_read_b128 v[160:163], v253 offset:0\n\t" \
  "ds_read_b128 v[164:167], v253 offset:1024\n\t" \
  "ds_read_b128 v[176:179], v240 offset:32768\n\t" \
  "ds_read_b128 v[180:183], v240 offset:49152\n\t" \
  "ds_read_b128 v[184:187], v241 offset:32768\n\t" \
  "ds_read_b128 v[188:191], v241 offset:49152\n\t" \
  "s_add_u32 m0, %[lw], 0\n\t" \
  "s_nop 0\n\t" \
  "global_load_lds_dwordx4 v248, s[92:93] sc1\n\t" \
  "ds_read_b128 v[192:195], v242 offset:32768\n\t" \
  "ds_read_b128 v[196:199], v242 offset:49152\n\t" \
  "ds_read_b128 v[200:203], v243 offset:32768\n\t" \
  "ds_read_b128 v[204:207], v243 offset:49152\n\t" \
  "s_add_u32 m0, %[lw], 1024\n\t" \
  "s_nop 0\n\t" \
  "global_load_lds_dwordx4 v249, s[92:93] sc1\n\t" \
  "ds_read_b128 v[208:211], v240 offset:33024\n\t" \
  "ds_read_b128 v[212:215], v240 offset:49408\n\t" \
  "ds_read_b128 v[216:219], v241 offset:33024\n\t" \
  "ds_read_b128 v[220:223], v241 offset:49408\n\t" \
  "s_add_u32 m0, %[lw], 2048\n\t" \
  "s_nop 0\n\t" \
  "global_load_lds_dwordx4 v250, s[92:93] sc1\n\t" \
  "ds_read_b128 v[224:227], v242 offset:33024\n\t" \
  "ds_read_b128 v[228:231], v242 offset:49408\n\t" \
  "ds_read_b128 v[232:235], v243 offset:33024\n\t" \
  "ds_read_b128 v[236:239], v243 offset:49408\n\t" \
  "s_add_u32 m0, %[lw], 3072\n\t" \
  "s_nop 0\n\t" \
  "global_load_lds_dwordx4 v251, s[92:93] sc1\n\t" \
  "s_waitcnt lgkmcnt(15)\n\t" \
  "s_waitcnt lgkmcnt(12)\n\t" \
  "s_sub_i32 s91, s86, 2\n\t" \
  "v_mov_b32 v173, s91\n\t" \
  "buffer_store_dword v173, v174, %[rs], 0 offen sc1\n\t" \
  "s_lshr_b32 s90, s87, 7\n\t" \
  "s_xor_b32 s90, s90, %[s0]\n\t" \
  "s_and_b32 s90, s90, 1\n\t" \
  "s_lshl1_add_u32 s90, s87, s90\n\t" \
  "s_lshl_b32 s90, s90, 13\n\t" \
  "v_add_f32 v160, v160, v144\n\t" \
  "v_add_f32 v161, v161, v145\n\t" \
  "v_add_f32 v162, v162, v146\n\t" \
  "v_add_f32 v163, v163, v147\n\t" \
  "v_add_f32 v164, v164, v148\n\t" \
  "v_add_f32 v165, v165, v149\n\t" \
  "v_add_f32 v166, v166, v150\n\t" \
  "v_add_f32 v167, v167, v151\n\t" \
  "v_cvt_pk_bf16_f32 v168, v160, v161\n\t" \
  "v_cvt_pk_bf16_f32 v169, v162, v163\n\t" \
  "v_cvt_pk_bf16_f32 v170, v164, v165\n\t" \
  "v_cvt_pk_bf16_f32 v171, v166, v167\n\t" \
  "buffer_store_dwordx4 v[168:171], v172, %[rt], s90 offen sc1\n\t" \
  "s_add_u32 s86, s86, 1\n\t" \
  "s_add_u32 s87, s87, 1\n\t" \
  "s_waitcnt lgkmcnt(0)\n\t" \
  "s_waitcnt vmcnt(10)\n\t" \
  "s_barrier\n\t" \
  "s_setprio 2\n\t" \
  "v_mfma_f32_16x16x32_bf16 v[128:131], v[64:67], v[176:179], v[128:131]\n\t" \
  "v_mfma_f32_16x16x32_bf16 v[132:135], v[68:71], v[176:179], v[132:135]\n\t" \
  "v_mfma_f32_16x16x32_bf16 v[136:139], v[64:67], v[180:183], v[136:139]\n\t" \
  "v_mfma_f32_16x16x32_bf16 v[140:143], v[68:71], v[180:183], v[140:143]\n\t" \
  "v_mfma_f32_16x16x32_bf16 v[128:131], v[72:75], v[184:187], v[128:131]\n\t" \
  "v_mfma_f32_16x16x32_bf16 v[132:135], v[76:79], v[184:187], v[132:135]\n\t" \
  "v_mfma_f32_16x16x32_bf16 v[136:139], v[72:75], v[188:191], v[136:139]\n\t" \
  "v_mfma_f32_16x16x32_bf16 v[140:143], v[76:79], v[188:191], v[140:143]\n\t" \
  "v_mfma_f32_16x16x32_bf16 v[128:131], v[80:83], v[192:195], v[128:131]\n\t" \
  "v_mfma_f32_16x16x32_bf16 v[132:135], v[84:87], v[192:195], v[132:135]\n\t" \
  "v_mfma_f32_16x16x32_bf16 v[136:139], v[80:83], v[196:199], v[136:139]\n\t" \
  "v_mfma_f32_16x16x32_bf16 v[140:143], v[84:87], v[196:199], v[140:143]\n\t" \
  "v_mfma_f32_16x16x32_bf16 v[128:131], v[88:91], v[200:203], v[128:131]\n\t" \
  "v_mfma_f32_16x16x32_bf16 v[132:135], v[92:95], v[200:203], v[132:135]\n\t" \
  "v_mfma_f32_16x16x32_bf16 v[136:139], v[88:91], v[204:207], v[136:139]\n\t" \
  "v_mfma_f32_16x16x32_bf16 v[140:143], v[92:95], v[204:207], v[140:143]\n\t" \
  "v_mfma_f32_16x16x32_bf16 v[128:131], v[96:99], v[208:211], v[128:131]\n\t" \
  "v_mfma_f32_16x16x32_bf16 v[132:135], v[100:103], v[208:211], v[132:135]\n\t" \
  "v_mfma_f32_16x16x32_bf16 v[136:139], v[96:99], v[212:215], v[136:139]\n\t" \
  "v_mfma_f32_16x16x32_bf16 v[140:143], v[100:103], v[212:215], v[140:143]\n\t" \
  "v_mfma_f32_16x16x32_bf16 v[128:131], v[104:107], v[216:219], v[128:131]\n\t" \
  "v_mfma_f32_16x16x32_bf16 v[132:135], v[108:111], v[216:219], v[132:135]\n\t" \
  "v_mfma_f32_16x16x32_bf16 v[136:139], v[104:107], v[220:223], v[136:139]\n\t" \
  "v_mfma_f32_16x16x32_bf16 v[140:143], v[108:111], v[220:223], v[140:143]\n\t" \
  "v_mfma_f32_16x16x32_bf16 v[128:131], v[112:115], v[224:227], v[128:131]\n\t" \
  "v_mfma_f32_16x16x32_bf16 v[132:135], v[116:119], v[224:227], v[132:135]\n\t" \
  "v_mfma_f32_16x16x32_bf16 v[136:139], v[112:115], v[228:231], v[136:139]\n\t" \
  "v_mfma_f32_16x16x32_bf16 v[140:143], v[116:119], v[228:231], v[140:143]\n\t" \
  "v_mfma_f32_16x16x32_bf16 v[128:131], v[120:123], v[232:235], v[128:131]\n\t" \
  "v_mfma_f32_16x16x32_bf16 v[132:135], v[124:127], v[232:235], v[132:135]\n\t" \
  "v_mfma_f32_16x16x32_bf16 v[136:139], v[120:123], v[236:239], v[136:139]\n\t" \
  "v_mfma_f32_16x16x32_bf16 v[140:143], v[124:127], v[236:239], v[140:143]\n\t" \
  "s_setprio 0\n\t" \
  "s_add_u32 s92, s92, 512\n\t" \
  "s_addc_u32 s93, s93, 0\n\t" \
  "ds_read_b128 v[176:179], v244 offset:0\n\t" \
  "ds_read_b128 v[180:183], v244 offset:16384\n\t" \
  "ds_read_b128 v[184:187], v245 offset:0\n\t" \
  "ds_read_b128 v[188:191], v245 offset:16384\n\t" \
  "s_add_u32 m0, %[lw], 32768\n\t" \
  "s_nop 0\n\t" \
  "global_load_lds_dwordx4 v248, s[92:93] sc1\n\t" \
  "ds_read_b128 v[192:195], v246 offset:0\n\t" \
  "ds_read_b128 v[196:199], v246 offset:16384\n\t" \
  "ds_read_b128 v[200:203], v247 offset:0\n\t" \
  "ds_read_b128 v[204:207], v247 offset:16384\n\t" \
  "s_add_u32 m0, %[lw], 33792\n\t" \
  "s_nop 0\n\t" \
  "global_load_lds_dwordx4 v249, s[92:93] sc1\n\t" \
  "ds_read_b128 v[208:211], v244 offset:256\n\t" \
  "ds_read_b128 v[212:215], v244 offset:16640\n\t" \
  "ds_read_b128 v[216:219], v245 offset:256\n\t" \
  "ds_read_b128 v[220:223], v245 offset:16640\n\t" \
  "s_add_u32 m0, %[lw], 34816\n\t" \
  "s_nop 0\n\t" \
  "global_load_lds_dwordx4 v250, s[92:93] sc1\n\t" \
  "ds_read_b128 v[224:227], v246 offset:256\n\t" \
  "ds_read_b128 v[228:231], v246 offset:16640\n\t" \
  "ds_read_b128 v[232:235], v247 offset:256\n\t" \
  "ds_read_b128 v[236:239], v247 offset:16640\n\t" \
  "s_add_u32 m0, %[lw], 35840\n\t" \
  "s_nop 0\n\t" \
  "global_load_lds_dwordx4 v251, s[92:93] sc1\n\t" \
  "ds_write_b128 v252, v[136:139] offset:0\n\t" \
  "ds_write_b128 v252, v[140:143] offset:1024\n\t" \
  "s_waitcnt lgkmcnt(0)\n\t" \
  "s_waitcnt vmcnt(4)\n\t" \
  "s_barrier\n\t" \
  "s_setprio 2\n\t" \
  "v_mfma_f32_16x16x32_bf16 v[144:147], v[0:3], v[176:179], 0\n\t" \
  "v_mfma_f32_16x16x32_bf16 v[148:151], v[4:7], v[176:179], 0\n\t" \
  "v_mfma_f32_16x16x32_bf16 v[152:155], v[0:3], v[180:183], 0\n\t" \
  "v_mfma_f32_16x16x32_bf16 v[156:159], v[4:7], v[180:183], 0\n\t" \
  "v_mfma_f32_16x16x32_bf16 v[144:147], v[8:11], v[184:187], v[144:147]\n\t" \
  "v_mfma_f32_16x16x32_bf16 v[148:151], v[12:15], v[184:187], v[148:151]\n\t" \
  "v_mfma_f32_16x16x32_bf16 v[152:155], v[8:11], v[188:191], v[152:155]\n\t" \
  "v_mfma_f32_16x16x32_bf16 v[156:159], v[12:15], v[188:191], v[156:159]\n\t" \
  "v_mfma_f32_16x16x32_bf16 v[144:147], v[16:19], v[192:195], v[144:147]\n\t" \
  "v_mfma_f32_16x16x32_bf16 v[148:151], v[20:23], v[192:195], v[148:151]\n\t" \
  "v_mfma_f32_16x16x32_bf16 v[152:155], v[16:19], v[196:199], v[152:155]\n\t" \
  "v_mfma_f32_16x16x32_bf16 v[156:159], v[20:23], v[196:199], v[156:159]\n\t" \
  "v_mfma_f32_16x16x32_bf16 v[144:147], v[24:27], v[200:203], v[144:147]\n\t" \
  "v_mfma_f32_16x16x32_bf16 v[148:151], v[28:31], v[200:203], v[148:151]\n\t" \
  "v_mfma_f32_16x16x32_bf16 v[152:155], v[24:27], v[204:207], v[152:155]\n\t" \
  "v_mfma_f32_16x16x32_bf16 v[156:159], v[28:31], v[204:207], v[156:159]\n\t" \
  "v_mfma_f32_16x16x32_bf16 v[144:147], v[32:35], v[208:211], v[144:147]\n\t" \
  "v_mfma_f32_16x16x32_bf16 v[148:151], v[36:39], v[208:211], v[148:151]\n\t" \
  "v_mfma_f32_16x16x32_bf16 v[152:155], v[32:35], v[212:215], v[152:155]\n\t" \
  "v_mfma_f32_16x16x32_bf16 v[156:159], v[36:39], v[212:215], v[156:159]\n\t" \
  "v_mfma_f32_16x16x32_bf16 v[144:147], v[40:43], v[216:219], v[144:147]\n\t" \
  "v_mfma_f32_16x16x32_bf16 v[148:151], v[44:47], v[216:219], v[148:151]\n\t" \
  "v_mfma_f32_16x16x32_bf16 v[152:155], v[40:43], v[220:223], v[152:155]\n\t" \
  "v_mfma_f32_16x16x32_bf16 v[156:159], v[44:47], v[220:223], v[156:159]\n\t" \
  "v_mfma_f32_16x16x32_bf16 v[144:147], v[48:51], v[224:227], v[144:147]\n\t" \
  "v_mfma_f32_16x16x32_bf16 v[148:151], v[52:55], v[224:227], v[148:151]\n\t" \
  "v_mfma_f32_16x16x32_bf16 v[152:155], v[48:51], v[228:231], v[152:155]\n\t" \
  "v_mfma_f32_16x16x32_bf16 v[156:159], v[52:55], v[228:231], v[156:159]\n\t" \
  "v_mfma_f32_16x16x32_bf16 v[144:147], v[56:59], v[232:235], v[144:147]\n\t" \
  "v_mfma_f32_16x16x32_bf16 v[148:151], v[60:63], v[232:235], v[148:151]\n\t" \
  "v_mfma_f32_16x16x32_bf16 v[152:155], v[56:59], v[236:239], v[152:155]\n\t" \
  "v_mfma_f32_16x16x32_bf16 v[156:159], v[60:63], v[236:239], v[156:159]\n\t" \
  "s_setprio 0\n\t" \
  "s_add_u32 s92, s92, 65024\n\t" \
  "s_addc_u32 s93, s93, 0\n\t" \
  "ds_read_b128 v[160:163], v253 offset:0\n\t" \
  "ds_read_b128 v[164:167], v253 offset:1024\n\t" \
  "ds_read_b128 v[176:179], v244 offset:32768\n\t" \
  "ds_read_b128 v[180:183], v244 offset:49152\n\t" \
  "ds_read_b128 v[184:187], v245 offset:32768\n\t" \
  "ds_read_b128 v[188:191], v245 offset:49152\n\t" \
  "s_add_u32 m0, %[lw], 65536\n\t" \
  "s_nop 0\n\t" \
  "global_load_lds_dwordx4 v248, s[92:93] sc1\n\t" \
  "ds_read_b128 v[192:195], v246 offset:32768\n\t" \
  "ds_read_b128 v[196:199], v246 offset:49152\n\t" \
  "ds_read_b128 v[200:203], v247 offset:32768\n\t" \
  "ds_read_b128 v[204:207], v247 offset:49152\n\t" \
  "s_add_u32 m0, %[lw], 66560\n\t" \
  "s_nop 0\n\t" \
  "global_load_lds_dwordx4 v249, s[92:93] sc1\n\t" \
  "ds_read_b128 v[208:211], v244 offset:33024\n\t" \
  "ds_read_b128 v[212:215], v244 offset:49408\n\t" \
  "ds_read_b128 v[216:219], v245 offset:33024\n\t" \
  "ds_read_b128 v[220:223], v245 offset:49408\n\t" \
  "s_add_u32 m0, %[lw], 67584\n\t" \
  "s_nop 0\n\t" \
  "global_load_lds_dwordx4 v250, s[92:93] sc1\n\t" \
  "ds_read_b128 v[224:227], v246 offset:33024\n\t" \
  "ds_read_b128 v[228:231], v246 offset:49408\n\t" \
  "ds_read_b128 v[232:235], v247 offset:33024\n\t" \
  "ds_read_b128 v[236:239], v247 offset:49408\n\t" \
  "s_add_u32 m0, %[lw], 68608\n\t" \
  "s_nop 0\n\t" \
  "global_load_lds_dwordx4 v251, s[92:93] sc1\n\t" \
  "s_waitcnt lgkmcnt(15)\n\t" \
  "s_waitcnt lgkmcnt(12)\n\t" \
  "s_sub_i32 s91, s86, 2\n\t" \
  "v_mov_b32 v173, s91\n\t" \
  "buffer_store_dword v173, v174, %[rs], 0 offen sc1\n\t" \
  "s_lshr_b32 s90, s87, 7\n\t" \
  "s_xor_b32 s90, s90, %[s0]\n\t" \
  "s_and_b32 s90, s90, 1\n\t" \
  "s_lshl1_add_u32 s90, s87, s90\n\t" \
  "s_lshl_b32 s90, s90, 13\n\t" \
  "v_add_f32 v160, v160, v128\n\t" \
  "v_add_f32 v161, v161, v129\n\t" \
  "v_add_f32 v162, v162, v130\n\t" \
  "v_add_f32 v163, v163, v131\n\t" \
  "v_add_f32 v164, v164, v132\n\t" \
  "v_add_f32 v165, v165, v133\n\t" \
  "v_add_f32 v166, v166, v134\n\t" \
  "v_add_f32 v167, v167, v135\n\t" \
  "v_cvt_pk_bf16_f32 v168, v160, v161\n\t" \
  "v_cvt_pk_bf16_f32 v169, v162, v163\n\t" \
  "v_cvt_pk_bf16_f32 v170, v164, v165\n\t" \
  "v_cvt_pk_bf16_f32 v171, v166, v167\n\t" \
  "buffer_store_dwordx4 v[168:171], v172, %[rt], s90 offen sc1\n\t" \
  "s_add_u32 s86, s86, 1\n\t" \
  "s_add_u32 s87, s87, 1\n\t" \
  "s_waitcnt lgkmcnt(0)\n\t" \
  "s_cmp_lt_u32 s86, 64\n\t" \
  "s_cbranch_scc1 .Lp1%=\n\t" \
  "s_barrier\n\t" \
  "s_setprio 2\n\t" \
  "v_mfma_f32_16x16x32_bf16 v[144:147], v[64:67], v[176:179], v[144:147]\n\t" \
  "v_mfma_f32_16x16x32_bf16 v[148:151], v[68:71], v[176:179], v[148:151]\n\t" \
  "v_mfma_f32_16x16x32_bf16 v[152:155], v[64:67], v[180:183], v[152:155]\n\t" \
  "v_mfma_f32_16x16x32_bf16 v[156:159], v[68:71], v[180:183], v[156:159]\n\t" \
  "v_mfma_f32_16x16x32_bf16 v[144:147], v[72:75], v[184:187], v[144:147]\n\t" \
  "v_mfma_f32_16x16x32_bf16 v[148:151], v[76:79], v[184:187], v[148:151]\n\t" \
  "v_mfma_f32_16x16x32_bf16 v[152:155], v[72:75], v[188:191], v[152:155]\n\t" \
  "v_mfma_f32_16x16x32_bf16 v[156:159], v[76:79], v[188:191], v[156:159]\n\t" \
  "v_mfma_f32_16x16x32_bf16 v[144:147], v[80:83], v[192:195], v[144:147]\n\t" \
  "v_mfma_f32_16x16x32_bf16 v[148:151], v[84:87], v[192:195], v[148:151]\n\t" \
  "v_mfma_f32_16x16x32_bf16 v[152:155], v[80:83], v[196:199], v[152:155]\n\t" \
  "v_mfma_f32_16x16x32_bf16 v[156:159], v[84:87], v[196:199], v[156:159]\n\t" \
  "v_mfma_f32_16x16x32_bf16 v[144:147], v[88:91], v[200:203], v[144:147]\n\t" \
  "v_mfma_f32_16x16x32_bf16 v[148:151], v[92:95], v[200:203], v[148:151]\n\t" \
  "v_mfma_f32_16x16x32_bf16 v[152:155], v[88:91], v[204:207], v[152:155]\n\t" \
  "v_mfma_f32_16x16x32_bf16 v[156:159], v[92:95], v[204:207], v[156:159]\n\t" \
  "v_mfma_f32_16x16x32_bf16 v[144:147], v[96:99], v[208:211], v[144:147]\n\t" \
  "v_mfma_f32_16x16x32_bf16 v[148:151], v[100:103], v[208:211], v[148:151]\n\t" \
  "v_mfma_f32_16x16x32_bf16 v[152:155], v[96:99], v[212:215], v[152:155]\n\t" \
  "v_mfma_f32_16x16x32_bf16 v[156:159], v[100:103], v[212:215], v[156:159]\n\t" \
  "v_mfma_f32_16x16x32_bf16 v[144:147], v[104:107], v[216:219], v[144:147]\n\t" \
  "v_mfma_f32_16x16x32_bf16 v[148:151], v[108:111], v[216:219], v[148:151]\n\t" \
  "v_mfma_f32_16x16x32_bf16 v[152:155], v[104:107], v[220:223], v[152:155]\n\t" \
  "v_mfma_f32_16x16x32_bf16 v[156:159], v[108:111], v[220:223], v[156:159]\n\t" \
  "v_mfma_f32_16x16x32_bf16 v[144:147], v[112:115], v[224:227], v[144:147]\n\t" \
  "v_mfma_f32_16x16x32_bf16 v[148:151], v[116:119], v[224:227], v[148:151]\n\t" \
  "v_mfma_f32_16x16x32_bf16 v[152:155], v[112:115], v[228:231], v[152:155]\n\t" \
  "v_mfma_f32_16x16x32_bf16 v[156:159], v[116:119], v[228:231], v[156:159]\n\t" \
  "v_mfma_f32_16x16x32_bf16 v[144:147], v[120:123], v[232:235], v[144:147]\n\t" \
  "v_mfma_f32_16x16x32_bf16 v[148:151], v[124:127], v[232:235], v[148:151]\n\t" \
  "v_mfma_f32_16x16x32_bf16 v[152:155], v[120:123], v[236:239], v[152:155]\n\t" \
  "v_mfma_f32_16x16x32_bf16 v[156:159], v[124:127], v[236:239], v[156:159]\n\t" \
  "s_setprio 0\n\t" \
  "s_nop 5\n\t" \
  "ds_write_b128 v252, v[152:155] offset:0\n\t" \
  "ds_write_b128 v252, v[156:159] offset:1024\n\t" \
  "s_waitcnt lgkmcnt(0)\n\t" \
  "s_barrier\n\t" \
  "ds_read_b128 v[160:163], v253 offset:0\n\t" \
  "ds_read_b128 v[164:167], v253 offset:1024\n\t" \
  "s_waitcnt lgkmcnt(0)\n\t" \
  "s_lshr_b32 s90, s87, 7\n\t" \
  "s_xor_b32 s90, s90, %[s0]\n\t" \
  "s_and_b32 s90, s90, 1\n\t" \
  "s_lshl1_add_u32 s90, s87, s90\n\t" \
  "s_lshl_b32 s90, s90, 13\n\t" \
  "v_add_f32 v160, v160, v144\n\t" \
  "v_add_f32 v161, v161, v145\n\t" \
  "v_add_f32 v162, v162, v146\n\t" \
  "v_add_f32 v163, v163, v147\n\t" \
  "v_add_f32 v164, v164, v148\n\t" \
  "v_add_f32 v165, v165, v149\n\t" \
  "v_add_f32 v166, v166, v150\n\t" \
  "v_add_f32 v167, v167, v151\n\t" \
  "v_cvt_pk_bf16_f32 v168, v160, v161\n\t" \
  "v_cvt_pk_bf16_f32 v169, v162, v163\n\t" \
  "v_cvt_pk_bf16_f32 v170, v164, v165\n\t" \
  "v_cvt_pk_bf16_f32 v171, v166, v167\n\t" \
  "buffer_store_dwordx4 v[168:171], v172, %[rt], s90 offen sc1\n\t" \
  "s_waitcnt vmcnt(0)\n\t" \
  "s_barrier\n\t" \
  "s_sub_i32 s91, s86, 1\n\t" \
  "v_mov_b32 v173, s91\n\t" \
  "buffer_store_dword v173, v174, %[rs], 0 offen sc1\n\t" \
  "s_mov_b32 m0, s94\n\t" \
  ".Le%=:\n\t"
#define PP_ASM_1 \
  "s_cmp_lg_u32 %[kh], 0\n\t" \
  "s_cbranch_scc1 .Lk%=\n\t" \
  "s_mov_b32 s94, m0\n\t" \
  "s_mov_b64 s[92:93], %[x0]\n\t" \
  "s_mov_b32 s86, %[g0]\n\t" \
  "s_add_u32 s87, s86, %[t0]\n\t" \
  "s_add_u32 m0, %[lw], 67584\n\t" \
  "s_nop 0\n\t" \
  "global_load_lds_dwordx4 v250, s[92:93] sc1\n\t" \
  "s_add_u32 m0, %[lw], 68608\n\t" \
  "s_nop 0\n\t" \
  "global_load_lds_dwordx4 v251, s[92:93] sc1\n\t" \
  "s_barrier\n\t" \
  "s_branch .Ls0%=\n\t" \
  ".Lp0%=:\n\t" \
  "s_waitcnt vmcnt(9)\n\t" \
  "s_barrier\n\t" \
  ".Ls0%=:\n\t" \
  "s_add_u32 s92, s92, 512\n\t" \
  "s_addc_u32 s93, s93, 0\n\t" \
  "ds_read_b128 v[176:179], v240 offset:0\n\t" \
  "ds_read_b128 v[180:183], v240 offset:16384\n\t" \
  "ds_read_b128 v[184:187], v241 offset:0\n\t" \
  "ds_read_b128 v[188:191], v241 offset:16384\n\t" \
  "s_add_u32 m0, %[lw], 98304\n\t" \
  "s_nop 0\n\t" \
  "global_load_lds_dwordx4 v248, s[92:93] sc1\n\t" \
  "ds_read_b128 v[192:195], v242 offset:0\n\t" \
  "ds_read_b128 v[196:199], v242 offset:16384\n\t" \
  "ds_read_b128 v[200:203], v243 offset:0\n\t" \
  "ds_read_b128 v[204:207], v243 offset:16384\n\t" \
  "s_add_u32 m0, %[lw], 99328\n\t" \
  "s_nop 0\n\t" \
  "global_load_lds_dwordx4 v249, s[92:93] sc1\n\t" \
  "ds_read_b128 v[208:211], v240 offset:256\n\t" \
  "ds_read_b128 v[212:215], v240 offset:16640\n\t" \
  "ds_read_b128 v[216:219], v241 offset:256\n\t" \
  "ds_read_b128 v[220:223], v241 offset:16640\n\t" \
  "s_add_u32 m0, %[lw], 100352\n\t" \
  "s_nop 0\n\t" \
  "global_load_lds_dwordx4 v250, s[92:93] sc1\n\t" \
  "ds_read_b128 v[224:227], v242 offset:256\n\t" \
  "ds_read_b128 v[228:231], v242 offset:16640\n\t" \
  "ds_read_b128 v[232:235], v243 offset:256\n\t" \
  "ds_read_b128 v[236:239], v243 offset:16640\n\t" \
  "s_add_u32 m0, %[lw], 101376\n\t" \
  "s_nop 0\n\t" \
  "global_load_lds_dwordx4 v251, s[92:93] sc1\n\t" \
  "ds_write_b128 v252, v[152:155] offset:0\n\t" \
  "ds_write_b128 v252, v[156:159] offset:1024\n\t" \
  "s_waitcnt lgkmcnt(0)\n\t" \
  "s_setprio 1\n\t" \
  "v_mfma_f32_16x16x32_bf16 v[128:131], v[176:179], v[0:3], 0\n\t" \
  "v_mfma_f32_16x16x32_bf16 v[136:139], v[176:179], v[4:7], 0\n\t" \
  "v_mfma_f32_16x16x32_bf16 v[132:135], v[180:183], v[0:3], 0\n\t" \
  "v_mfma_f32_16x16x32_bf16 v[140:143], v[180:183], v[4:7], 0\n\t" \
  "v_mfma_f32_16x16x32_bf16 v[128:131], v[184:187], v[8:11], v[128:131]\n\t" \
  "v_mfma_f32_16x16x32_bf16 v[136:139], v[184:187], v[12:15], v[136:139]\n\t" \
  "v_mfma_f32_16x16x32_bf16 v[132:135], v[188:191], v[8:11], v[132:135]\n\t" \
  "v_mfma_f32_16x16x32_bf16 v[140:143], v[188:191], v[12:15], v[140:143]\n\t" \
  "v_mfma_f32_16x16x32_bf16 v[128:131], v[192:195], v[16:19], v[128:131]\n\t" \
  "v_mfma_f32_16x16x32_bf16 v[136:139], v[192:195], v[20:23], v[136:139]\n\t" \
  "v_mfma_f32_16x16x32_bf16 v[132:135], v[196:199], v[16:19], v[132:135]\n\t" \
  "v_mfma_f32_16x16x32_bf16 v[140:143], v[196:199], v[20:23], v[140:143]\n\t" \
  "v_mfma_f32_16x16x32_bf16 v[128:131], v[200:203], v[24:27], v[128:131]\n\t" \
  "v_mfma_f32_16x16x32_bf16 v[136:139], v[200:203], v[28:31], v[136:139]\n\t" \
  "v_mfma_f32_16x16x32_bf16 v[132:135], v[204:207], v[24:27], v[132:135]\n\t" \
  "v_mfma_f32_16x16x32_bf16 v[140:143], v[204:207], v[28:31], v[140:143]\n\t" \
  "v_mfma_f32_16x16x32_bf16 v[128:131], v[208:211], v[32:35], v[128:131]\n\t" \
  "v_mfma_f32_16x16x32_bf16 v[136:139], v[208:211], v[36:39], v[136:139]\n\t" \
  "v_mfma_f32_16x16x32_bf16 v[132:135], v[212:215], v[32:35], v[132:135]\n\t" \
  "v_mfma_f32_16x16x32_bf16 v[140:143], v[212:215], v[36:39], v[140:143]\n\t" \
  "v_mfma_f32_16x16x32_bf16 v[128:131], v[216:219], v[40:43], v[128:131]\n\t" \
  "v_mfma_f32_16x16x32_bf16 v[136:139], v[216:219], v[44:47], v[136:139]\n\t" \
  "v_mfma_f32_16x16x32_bf16 v[132:135], v[220:223], v[40:43], v[132:135]\n\t" \
  "v_mfma_f32_16x16x32_bf16 v[140:143], v[220:223], v[44:47], v[140:143]\n\t" \
  "v_mfma_f32_16x16x32_bf16 v[128:131], v[224:227], v[48:51], v[128:131]\n\t" \
  "v_mfma_f32_16x16x32_bf16 v[136:139], v[224:227], v[52:55], v[136:139]\n\t" \
  "v_mfma_f32_16x16x32_bf16 v[132:135], v[228:231], v[48:51], v[132:135]\n\t" \
  "v_mfma_f32_16x16x32_bf16 v[140:143], v[228:231], v[52:55], v[140:143]\n\t" \
  "v_mfma_f32_16x16x32_bf16 v[128:131], v[232:235], v[56:59], v[128:131]\n\t" \
  "v_mfma_f32_16x16x32_bf16 v[136:139], v[232:235], v[60:63], v[136:139]\n\t" \
  "v_mfma_f32_16x16x32_bf16 v[132:135], v[236:239], v[56:59], v[132:135]\n\t" \
  "v_mfma_f32_16x16x32_bf16 v[140:143], v[236:239], v[60:63], v[140:143]\n\t" \
  "s_setprio 0\n\t" \
  "s_waitcnt vmcnt(4)\n\t" \
  "s_barrier\n\t" \
  "s_add_u32 s92, s92, 65024\n\t" \
  "s_addc_u32 s93, s93, 0\n\t" \
  "ds_read_b128 v[160:163], v253 offset:0\n\t" \
  "ds_read_b128 v[164:167], v253 offset:1024\n\t" \
  "ds_read_b128 v[176:179], v240 offset:32768\n\t" \
  "ds_read_b128 v[180:183], v240 offset:49152\n\t" \
  "ds_read_b128 v[184:187], v241 offset:32768\n\t" \
  "ds_read_b128 v[188:191], v241 offset:49152\n\t" \
  "s_add_u32 m0, %[lw], 0\n\t" \
  "s_nop 0\n\t" \
  "global_load_lds_dwordx4 v248, s[92:93] sc1\n\t" \
  "ds_read_b128 v[192:195], v242 offset:32768\n\t" \
  "ds_read_b128 v[196:199], v242 offset:49152\n\t" \
  "ds_read_b128 v[200:203], v243 offset:32768\n\t" \
  "ds_read_b128 v[204:207], v243 offset:49152\n\t" \
  "s_add_u32 m0, %[lw], 1024\n\t" \
  "s_nop 0\n\t" \
  "global_load_lds_dwordx4 v249, s[92:93] sc1\n\t" \
  "ds_read_b128 v[208:211], v240 offset:33024\n\t" \
  "ds_read_b128 v[212:215], v240 offset:49408\n\t" \
  "ds_read_b128 v[216:219], v241 offset:33024\n\t" \
  "ds_read_b128 v[220:223], v241 offset:49408\n\t" \
  "s_add_u32 m0, %[lw], 2048\n\t" \
  "s_nop 0\n\t" \
  "global_load_lds_dwordx4 v250, s[92:93] sc1\n\t" \
  "ds_read_b128 v[224:227], v242 offset:33024\n\t" \
  "ds_read_b128 v[228:231], v242 offset:49408\n\t" \
  "ds_read_b128 v[232:235], v243 offset:33024\n\t" \
  "ds_read_b128 v[236:239], v243 offset:49408\n\t" \
  "s_add_u32 m0, %[lw], 3072\n\t" \
  "s_nop 0\n\t" \
  "global_load_lds_dwordx4 v251, s[92:93] sc1\n\t" \
  "s_waitcnt lgkmcnt(15)\n\t" \
  "s_waitcnt lgkmcnt(12)\n\t" \
  "s_lshr_b32 s90, s87, 7\n\t" \
  "s_xor_b32 s90, s90, %[s0]\n\t" \
  "s_and_b32 s90, s90, 1\n\t" \
  "s_lshl1_add_u32 s90, s87, s90\n\t" \
  "s_lshl_b32 s90, s90, 13\n\t" \
  "v_add_f32 v160, v160, v144\n\t" \
  "v_add_f32 v161, v161, v145\n\t" \
  "v_add_f32 v162, v162, v146\n\t" \
  "v_add_f32 v163, v163, v147\n\t" \
  "v_add_f32 v164, v164, v148\n\t" \
  "v_add_f32 v165, v165, v149\n\t" \
  "v_add_f32 v166, v166, v150\n\t" \
  "v_add_f32 v167, v167, v151\n\t" \
  "v_cvt_pk_bf16_f32 v168, v160, v161\n\t" \
  "v_cvt_pk_bf16_f32 v169, v162, v163\n\t" \
  "v_cvt_pk_bf16_f32 v170, v164, v165\n\t" \
  "v_cvt_pk_bf16_f32 v171, v166, v167\n\t" \
  "buffer_store_dwordx4 v[168:171], v172, %[rt], s90 offen sc1\n\t" \
  "s_add_u32 s86, s86, 1\n\t" \
  "s_add_u32 s87, s87, 1\n\t" \
  "s_waitcnt lgkmcnt(0)\n\t" \
  "s_setprio 1\n\t" \
  "v_mfma_f32_16x16x32_bf16 v[128:131], v[176:179], v[64:67], v[128:131]\n\t" \
  "v_mfma_f32_16x16x32_bf16 v[136:139], v[176:179], v[68:71], v[136:139]\n\t" \
  "v_mfma_f32_16x16x32_bf16 v[132:135], v[180:183], v[64:67], v[132:135]\n\t" \
  "v_mfma_f32_16x16x32_bf16 v[140:143], v[180:183], v[68:71], v[140:143]\n\t" \
  "v_mfma_f32_16x16x32_bf16 v[128:131], v[184:187], v[72:75], v[128:131]\n\t" \
  "v_mfma_f32_16x16x32_bf16 v[136:139], v[184:187], v[76:79], v[136:139]\n\t" \
  "v_mfma_f32_16x16x32_bf16 v[132:135], v[188:191], v[72:75], v[132:135]\n\t" \
  "v_mfma_f32_16x16x32_bf16 v[140:143], v[188:191], v[76:79], v[140:143]\n\t" \
  "v_mfma_f32_16x16x32_bf16 v[128:131], v[192:195], v[80:83], v[128:131]\n\t" \
  "v_mfma_f32_16x16x32_bf16 v[136:139], v[192:195], v[84:87], v[136:139]\n\t" \
  "v_mfma_f32_16x16x32_bf16 v[132:135], v[196:199], v[80:83], v[132:135]\n\t" \
  "v_mfma_f32_16x16x32_bf16 v[140:143], v[196:199], v[84:87], v[140:143]\n\t" \
  "v_mfma_f32_16x16x32_bf16 v[128:131], v[200:203], v[88:91], v[128:131]\n\t" \
  "v_mfma_f32_16x16x32_bf16 v[136:139], v[200:203], v[92:95], v[136:139]\n\t" \
  "v_mfma_f32_16x16x32_bf16 v[132:135], v[204:207], v[88:91], v[132:135]\n\t" \
  "v_mfma_f32_16x16x32_bf16 v[140:143], v[204:207], v[92:95], v[140:143]\n\t" \
  "v_mfma_f32_16x16x32_bf16 v[128:131], v[208:211], v[96:99], v[128:131]\n\t" \
  "v_mfma_f32_16x16x32_bf16 v[136:139], v[208:211], v[100:103], v[136:139]\n\t" \
  "v_mfma_f32_16x16x32_bf16 v[132:135], v[212:215], v[96:99], v[132:135]\n\t" \
  "v_mfma_f32_16x16x32_bf16 v[140:143], v[212:215], v[100:103], v[140:143]\n\t" \
  "v_mfma_f32_16x16x32_bf16 v[128:131], v[216:219], v[104:107], v[128:131]\n\t" \
  "v_mfma_f32_16x16x32_bf16 v[136:139], v[216:219], v[108:111], v[136:139]\n\t" \
  "v_mfma_f32_16x16x32_bf16 v[132:135], v[220:223], v[104:107], v[132:135]\n\t" \
  "v_mfma_f32_16x16x32_bf16 v[140:143], v[220:223], v[108:111], v[140:143]\n\t" \
  "v_mfma_f32_16x16x32_bf16 v[128:131], v[224:227], v[112:115], v[128:131]\n\t" \
  "v_mfma_f32_16x16x32_bf16 v[136:139], v[224:227], v[116:119], v[136:139]\n\t" \
  "v_mfma_f32_16x16x32_bf16 v[132:135], v[228:231], v[112:115], v[132:135]\n\t" \
  "v_mfma_f32_16x16x32_bf16 v[140:143], v[228:231], v[116:119], v[140:143]\n\t" \
  "v_mfma_f32_16x16x32_bf16 v[128:131], v[232:235], v[120:123], v[128:131]\n\t" \
  "v_mfma_f32_16x16x32_bf16 v[136:139], v[232:235], v[124:127], v[136:139]\n\t" \
  "v_mfma_f32_16x16x32_bf16 v[132:135], v[236:239], v[120:123], v[132:135]\n\t" \
  "v_mfma_f32_16x16x32_bf16 v[140:143], v[236:239], v[124:127], v[140:143]\n\t" \
  "s_setprio 0\n\t" \
  "s_waitcnt vmcnt(9)\n\t" \
  "s_barrier\n\t" \
  "s_add_u32 s92, s92, 512\n\t" \
  "s_addc_u32 s93, s93, 0\n\t" \
  "ds_read_b128 v[176:179], v244 offset:0\n\t" \
  "ds_read_b128 v[180:183], v244 offset:16384\n\t" \
  "ds_read_b128 v[184:187], v245 offset:0\n\t" \
  "ds_read_b128 v[188:191], v245 offset:16384\n\t" \
  "s_add_u32 m0, %[lw], 32768\n\t" \
  "s_nop 0\n\t" \
  "global_load_lds_dwordx4 v248, s[92:93] sc1\n\t" \
  "ds_read_b128 v[192:195], v246 offset:0\n\t" \
  "ds_read_b128 v[196:199], v246 offset:16384\n\t" \
  "ds_read_b128 v[200:203], v247 offset:0\n\t" \
  "ds_read_b128 v[204:207], v247 offset:16384\n\t" \
  "s_add_u32 m0, %[lw], 33792\n\t" \
  "s_nop 0\n\t" \
  "global_load_lds_dwordx4 v249, s[92:93] sc1\n\t" \
  "ds_read_b128 v[208:211], v244 offset:256\n\t" \
  "ds_read_b128 v[212:215], v244 offset:16640\n\t" \
  "ds_read_b128 v[216:219], v245 offset:256\n\t" \
  "ds_read_b128 v[220:223], v245 offset:16640\n\t" \
  "s_add_u32 m0, %[lw], 34816\n\t" \
  "s_nop 0\n\t" \
  "global_load_lds_dwordx4 v250, s[92:93] sc1\n\t" \
  "ds_read_b128 v[224:227], v246 offset:256\n\t" \
  "ds_read_b128 v[228:231], v246 offset:16640\n\t" \
  "ds_read_b128 v[232:235], v247 offset:256\n\t" \
  "ds_read_b128 v[236:239], v247 offset:16640\n\t" \
  "s_add_u32 m0, %[lw], 35840\n\t" \
  "s_nop 0\n\t" \
  "global_load_lds_dwordx4 v251, s[92:93] sc1\n\t" \
  "ds_write_b128 v252, v[136:139] offset:0\n\t" \
  "ds_write_b128 v252, v[140:143] offset:1024\n\t" \
  "s_waitcnt lgkmcnt(0)\n\t" \
  "s_setprio 1\n\t" \
  "v_mfma_f32_16x16x32_bf16 v[144:147], v[176:179], v[0:3], 0\n\t" \
  "v_mfma_f32_16x16x32_bf16 v[152:155], v[176:179], v[4:7], 0\n\t" \
  "v_mfma_f32_16x16x32_bf16 v[148:151], v[180:183], v[0:3], 0\n\t" \
  "v_mfma_f32_16x16x32_bf16 v[156:159], v[180:183], v[4:7], 0\n\t" \
  "v_mfma_f32_16x16x32_bf16 v[144:147], v[184:187], v[8:11], v[144:147]\n\t" \
  "v_mfma_f32_16x16x32_bf16 v[152:155], v[184:187], v[12:15], v[152:155]\n\t" \
  "v_mfma_f32_16x16x32_bf16 v[148:151], v[188:191], v[8:11], v[148:151]\n\t" \
  "v_mfma_f32_16x16x32_bf16 v[156:159], v[188:191], v[12:15], v[156:159]\n\t" \
  "v_mfma_f32_16x16x32_bf16 v[144:147], v[192:195], v[16:19], v[144:147]\n\t" \
  "v_mfma_f32_16x16x32_bf16 v[152:155], v[192:195], v[20:23], v[152:155]\n\t" \
  "v_mfma_f32_16x16x32_bf16 v[148:151], v[196:199], v[16:19], v[148:151]\n\t" \
  "v_mfma_f32_16x16x32_bf16 v[156:159], v[196:199], v[20:23], v[156:159]\n\t" \
  "v_mfma_f32_16x16x32_bf16 v[144:147], v[200:203], v[24:27], v[144:147]\n\t" \
  "v_mfma_f32_16x16x32_bf16 v[152:155], v[200:203], v[28:31], v[152:155]\n\t" \
  "v_mfma_f32_16x16x32_bf16 v[148:151], v[204:207], v[24:27], v[148:151]\n\t" \
  "v_mfma_f32_16x16x32_bf16 v[156:159], v[204:207], v[28:31], v[156:159]\n\t" \
  "v_mfma_f32_16x16x32_bf16 v[144:147], v[208:211], v[32:35], v[144:147]\n\t" \
  "v_mfma_f32_16x16x32_bf16 v[152:155], v[208:211], v[36:39], v[152:155]\n\t" \
  "v_mfma_f32_16x16x32_bf16 v[148:151], v[212:215], v[32:35], v[148:151]\n\t" \
  "v_mfma_f32_16x16x32_bf16 v[156:159], v[212:215], v[36:39], v[156:159]\n\t" \
  "v_mfma_f32_16x16x32_bf16 v[144:147], v[216:219], v[40:43], v[144:147]\n\t" \
  "v_mfma_f32_16x16x32_bf16 v[152:155], v[216:219], v[44:47], v[152:155]\n\t" \
  "v_mfma_f32_16x16x32_bf16 v[148:151], v[220:223], v[40:43], v[148:151]\n\t" \
  "v_mfma_f32_16x16x32_bf16 v[156:159], v[220:223], v[44:47], v[156:159]\n\t" \
  "v_mfma_f32_16x16x32_bf16 v[144:147], v[224:227], v[48:51], v[144:147]\n\t" \
  "v_mfma_f32_16x16x32_bf16 v[152:155], v[224:227], v[52:55], v[152:155]\n\t" \
  "v_mfma_f32_16x16x32_bf16 v[148:151], v[228:231], v[48:51], v[148:151]\n\t" \
  "v_mfma_f32_16x16x32_bf16 v[156:159], v[228:231], v[52:55], v[156:159]\n\t" \
  "v_mfma_f32_16x16x32_bf16 v[144:147], v[232:235], v[56:59], v[144:147]\n\t" \
  "v_mfma_f32_16x16x32_bf16 v[152:155], v[232:235], v[60:63], v[152:155]\n\t" \
  "v_mfma_f32_16x16x32_bf16 v[148:151], v[236:239], v[56:59], v[148:151]\n\t" \
  "v_mfma_f32_16x16x32_bf16 v[156:159], v[236:239], v[60:63], v[156:159]\n\t" \
  "s_setprio 0\n\t" \
  "s_waitcnt vmcnt(4)\n\t" \
  "s_barrier\n\t" \
  "s_add_u32 s92, s92, 65024\n\t" \
  "s_addc_u32 s93, s93, 0\n\t" \
  "ds_read_b128 v[160:163], v253 offset:0\n\t" \
  "ds_read_b128 v[164:167], v253 offset:1024\n\t" \
  "ds_read_b128 v[176:179], v244 offset:32768\n\t" \
  "ds_read_b128 v[180:183], v244 offset:49152\n\t" \
  "ds_read_b128 v[184:187], v245 offset:32768\n\t" \
  "ds_read_b128 v[188:191], v245 offset:49152\n\t" \
  "s_add_u32 m0, %[lw], 65536\n\t" \
  "s_nop 0\n\t" \
  "global_load_lds_dwordx4 v248, s[92:93] sc1\n\t" \
  "ds_read_b128 v[192:195], v246 offset:32768\n\t" \
  "ds_read_b128 v[196:199], v246 offset:49152\n\t" \
  "ds_read_b128 v[200:203], v247 offset:32768\n\t" \
  "ds_read_b128 v[204:207], v247 offset:49152\n\t" \
  "s_add_u32 m0, %[lw], 66560\n\t" \
  "s_nop 0\n\t" \
  "global_load_lds_dwordx4 v249, s[92:93] sc1\n\t" \
  "ds_read_b128 v[208:211], v244 offset:33024\n\t" \
  "ds_read_b128 v[212:215], v244 offset:49408\n\t" \
  "ds_read_b128 v[216:219], v245 offset:33024\n\t" \
  "ds_read_b128 v[220:223], v245 offset:49408\n\t" \
  "s_add_u32 m0, %[lw], 67584\n\t" \
  "s_nop 0\n\t" \
  "global_load_lds_dwordx4 v250, s[92:93] sc1\n\t" \
  "ds_read_b128 v[224:227], v246 offset:33024\n\t" \
  "ds_read_b128 v[228:231], v246 offset:49408\n\t" \
  "ds_read_b128 v[232:235], v247 offset:33024\n\t" \
  "ds_read_b128 v[236:239], v247 offset:49408\n\t" \
  "s_add_u32 m0, %[lw], 68608\n\t" \
  "s_nop 0\n\t" \
  "global_load_lds_dwordx4 v251, s[92:93] sc1\n\t" \
  "s_waitcnt lgkmcnt(15)\n\t" \
  "s_waitcnt lgkmcnt(12)\n\t" \
  "s_lshr_b32 s90, s87, 7\n\t" \
  "s_xor_b32 s90, s90, %[s0]\n\t" \
  "s_and_b32 s90, s90, 1\n\t" \
  "s_lshl1_add_u32 s90, s87, s90\n\t" \
  "s_lshl_b32 s90, s90, 13\n\t" \
  "v_add_f32 v160, v160, v128\n\t" \
  "v_add_f32 v161, v161, v129\n\t" \
  "v_add_f32 v162, v162, v130\n\t" \
  "v_add_f32 v163, v163, v131\n\t" \
  "v_add_f32 v164, v164, v132\n\t" \
  "v_add_f32 v165, v165, v133\n\t" \
  "v_add_f32 v166, v166, v134\n\t" \
  "v_add_f32 v167, v167, v135\n\t" \
  "v_cvt_pk_bf16_f32 v168, v160, v161\n\t" \
  "v_cvt_pk_bf16_f32 v169, v162, v163\n\t" \
  "v_cvt_pk_bf16_f32 v170, v164, v165\n\t" \
  "v_cvt_pk_bf16_f32 v171, v166, v167\n\t" \
  "buffer_store_dwordx4 v[168:171], v172, %[rt], s90 offen sc1\n\t" \
  "s_add_u32 s86, s86, 1\n\t" \
  "s_add_u32 s87, s87, 1\n\t" \
  "s_waitcnt lgkmcnt(0)\n\t" \
  "s_setprio 1\n\t" \
  "v_mfma_f32_16x16x32_bf16 v[144:147], v[176:179], v[64:67], v[144:147]\n\t" \
  "v_mfma_f32_16x16x32_bf16 v[152:155], v[176:179], v[68:71], v[152:155]\n\t" \
  "v_mfma_f32_16x16x32_bf16 v[148:151], v[180:183], v[64:67], v[148:151]\n\t" \
  "v_mfma_f32_16x16x32_bf16 v[156:159], v[180:183], v[68:71], v[156:159]\n\t" \
  "v_mfma_f32_16x16x32_bf16 v[144:147], v[184:187], v[72:75], v[144:147]\n\t" \
  "v_mfma_f32_16x16x32_bf16 v[152:155], v[184:187], v[76:79], v[152:155]\n\t" \
  "v_mfma_f32_16x16x32_bf16 v[148:151], v[188:191], v[72:75], v[148:151]\n\t" \
  "v_mfma_f32_16x16x32_bf16 v[156:159], v[188:191], v[76:79], v[156:159]\n\t" \
  "v_mfma_f32_16x16x32_bf16 v[144:147], v[192:195], v[80:83], v[144:147]\n\t" \
  "v_mfma_f32_16x16x32_bf16 v[152:155], v[192:195], v[84:87], v[152:155]\n\t" \
  "v_mfma_f32_16x16x32_bf16 v[148:151], v[196:199], v[80:83], v[148:151]\n\t" \
  "v_mfma_f32_16x16x32_bf16 v[156:159], v[196:199], v[84:87], v[156:159]\n\t" \
  "v_mfma_f32_16x16x32_bf16 v[144:147], v[200:203], v[88:91], v[144:147]\n\t" \
  "v_mfma_f32_16x16x32_bf16 v[152:155], v[200:203], v[92:95], v[152:155]\n\t" \
  "v_mfma_f32_16x16x32_bf16 v[148:151], v[204:207], v[88:91], v[148:151]\n\t" \
  "v_mfma_f32_16x16x32_bf16 v[156:159], v[204:207], v[92:95], v[156:159]\n\t" \
  "v_mfma_f32_16x16x32_bf16 v[144:147], v[208:211], v[96:99], v[144:147]\n\t" \
  "v_mfma_f32_16x16x32_bf16 v[152:155], v[208:211], v[100:103], v[152:155]\n\t" \
  "v_mfma_f32_16x16x32_bf16 v[148:151], v[212:215], v[96:99], v[148:151]\n\t" \
  "v_mfma_f32_16x16x32_bf16 v[156:159], v[212:215], v[100:103], v[156:159]\n\t" \
  "v_mfma_f32_16x16x32_bf16 v[144:147], v[216:219], v[104:107], v[144:147]\n\t" \
  "v_mfma_f32_16x16x32_bf16 v[152:155], v[216:219], v[108:111], v[152:155]\n\t" \
  "v_mfma_f32_16x16x32_bf16 v[148:151], v[220:223], v[104:107], v[148:151]\n\t" \
  "v_mfma_f32_16x16x32_bf16 v[156:159], v[220:223], v[108:111], v[156:159]\n\t" \
  "v_mfma_f32_16x16x32_bf16 v[144:147], v[224:227], v[112:115], v[144:147]\n\t" \
  "v_mfma_f32_16x16x32_bf16 v[152:155], v[224:227], v[116:119], v[152:155]\n\t" \
  "v_mfma_f32_16x16x32_bf16 v[148:151], v[228:231], v[112:115], v[148:151]\n\t" \
  "v_mfma_f32_16x16x32_bf16 v[156:159], v[228:231], v[116:119], v[156:159]\n\t" \
  "v_mfma_f32_16x16x32_bf16 v[144:147], v[232:235], v[120:123], v[144:147]\n\t" \
  "v_mfma_f32_16x16x32_bf16 v[152:155], v[232:235], v[124:127], v[152:155]\n\t" \
  "v_mfma_f32_16x16x32_bf16 v[148:151], v[236:239], v[120:123], v[148:151]\n\t" \
  "v_mfma_f32_16x16x32_bf16 v[156:159], v[236:239], v[124:127], v[156:159]\n\t" \
  "s_setprio 0\n\t" \
  "s_cmp_lt_u32 s86, 64\n\t" \
  "s_cbranch_scc1 .Lp0%=\n\t" \
  "s_barrier\n\t" \
  "s_nop 1\n\t" \
  "ds_write_b128 v252, v[152:155] offset:0\n\t" \
  "s_nop 0\n\t" \
  "ds_write_b128 v252, v[156:159] offset:1024\n\t" \
  "s_waitcnt lgkmcnt(0)\n\t" \
  "s_barrier\n\t" \
  "ds_read_b128 v[160:163], v253 offset:0\n\t" \
  "ds_read_b128 v[164:167], v253 offset:1024\n\t" \
  "s_waitcnt lgkmcnt(0)\n\t" \
  "s_lshr_b32 s90, s87, 7\n\t" \
  "s_xor_b32 s90, s90, %[s0]\n\t" \
  "s_and_b32 s90, s90, 1\n\t" \
  "s_lshl1_add_u32 s90, s87, s90\n\t" \
  "s_lshl_b32 s90, s90, 13\n\t" \
  "v_add_f32 v160, v160, v144\n\t" \
  "v_add_f32 v161, v161, v145\n\t" \
  "v_add_f32 v162, v162, v146\n\t" \
  "v_add_f32 v163, v163, v147\n\t" \
  "v_add_f32 v164, v164, v148\n\t" \
  "v_add_f32 v165, v165, v149\n\t" \
  "v_add_f32 v166, v166, v150\n\t" \
  "v_add_f32 v167, v167, v151\n\t" \
  "v_cvt_pk_bf16_f32 v168, v160, v161\n\t" \
  "v_cvt_pk_bf16_f32 v169, v162, v163\n\t" \
  "v_cvt_pk_bf16_f32 v170, v164, v165\n\t" \
  "v_cvt_pk_bf16_f32 v171, v166, v167\n\t" \
  "buffer_store_dwordx4 v[168:171], v172, %[rt], s90 offen sc1\n\t" \
  "s_waitcnt vmcnt(0)\n\t" \
  "s_barrier\n\t" \
  "s_mov_b32 m0, s94\n\t" \
  "s_branch .Le%=\n\t" \
  ".Lk%=:\n\t" \
  "s_mov_b32 s94, m0\n\t" \
  "s_mov_b64 s[92:93], %[x0]\n\t" \
  "s_mov_b32 s86, %[g0]\n\t" \
  "s_add_u32 s87, s86, %[t0]\n\t" \
  "s_add_u32 m0, %[lw], 67584\n\t" \
  "s_nop 0\n\t" \
  "global_load_lds_dwordx4 v250, s[92:93] sc1\n\t" \
  "s_add_u32 m0, %[lw], 68608\n\t" \
  "s_nop 0\n\t" \
  "global_load_lds_dwordx4 v251, s[92:93] sc1\n\t" \
  "s_barrier\n\t" \
  "s_branch .Ls1%=\n\t" \
  ".Lp1%=:\n\t" \
  "s_waitcnt vmcnt(10)\n\t" \
  "s_barrier\n\t" \
  "s_setprio 2\n\t" \
  "v_mfma_f32_16x16x32_bf16 v[144:147], v[176:179], v[64:67], v[144:147]\n\t" \
  "v_mfma_f32_16x16x32_bf16 v[152:155], v[176:179], v[68:71], v[152:155]\n\t" \
  "v_mfma_f32_16x16x32_bf16 v[148:151], v[180:183], v[64:67], v[148:151]\n\t" \
  "v_mfma_f32_16x16x32_bf16 v[156:159], v[180:183], v[68:71], v[156:159]\n\t" \
  "v_mfma_f32_16x16x32_bf16 v[144:147], v[184:187], v[72:75], v[144:147]\n\t" \
  "v_mfma_f32_16x16x32_bf16 v[152:155], v[184:187], v[76:79], v[152:155]\n\t" \
  "v_mfma_f32_16x16x32_bf16 v[148:151], v[188:191], v[72:75], v[148:151]\n\t" \
  "v_mfma_f32_16x16x32_bf16 v[156:159], v[188:191], v[76:79], v[156:159]\n\t" \
  "v_mfma_f32_16x16x32_bf16 v[144:147], v[192:195], v[80:83], v[144:147]\n\t" \
  "v_mfma_f32_16x16x32_bf16 v[152:155], v[192:195], v[84:87], v[152:155]\n\t" \
  "v_mfma_f32_16x16x32_bf16 v[148:151], v[196:199], v[80:83], v[148:151]\n\t" \
  "v_mfma_f32_16x16x32_bf16 v[156:159], v[196:199], v[84:87], v[156:159]\n\t" \
  "v_mfma_f32_16x16x32_bf16 v[144:147], v[200:203], v[88:91], v[144:147]\n\t" \
  "v_mfma_f32_16x16x32_bf16 v[152:155], v[200:203], v[92:95], v[152:155]\n\t" \
  "v_mfma_f32_16x16x32_bf16 v[148:151], v[204:207], v[88:91], v[148:151]\n\t" \
  "v_mfma_f32_16x16x32_bf16 v[156:159], v[204:207], v[92:95], v[156:159]\n\t" \
  "v_mfma_f32_16x16x32_bf16 v[144:147], v[208:211], v[96:99], v[144:147]\n\t" \
  "v_mfma_f32_16x16x32_bf16 v[152:155], v[208:211], v[100:103], v[152:155]\n\t" \
  "v_mfma_f32_16x16x32_bf16 v[148:151], v[212:215], v[96:99], v[148:151]\n\t" \
  "v_mfma_f32_16x16x32_bf16 v[156:159], v[212:215], v[100:103], v[156:159]\n\t" \
  "v_mfma_f32_16x16x32_bf16 v[144:147], v[216:219], v[104:107], v[144:147]\n\t" \
  "v_mfma_f32_16x16x32_bf16 v[152:155], v[216:219], v[108:111], v[152:155]\n\t" \
  "v_mfma_f32_16x16x32_bf16 v[148:151], v[220:223], v[104:107], v[148:151]\n\t" \
  "v_mfma_f32_16x16x32_bf16 v[156:159], v[220:223], v[108:111], v[156:159]\n\t" \
  "v_mfma_f32_16x16x32_bf16 v[144:147], v[224:227], v[112:115], v[144:147]\n\t" \
  "v_mfma_f32_16x16x32_bf16 v[152:155], v[224:227], v[116:119], v[152:155]\n\t" \
  "v_mfma_f32_16x16x32_bf16 v[148:151], v[228:231], v[112:115], v[148:151]\n\t" \
  "v_mfma_f32_16x16x32_bf16 v[156:159], v[228:231], v[116:119], v[156:159]\n\t" \
  "v_mfma_f32_16x16x32_bf16 v[144:147], v[232:235], v[120:123], v[144:147]\n\t" \
  "v_mfma_f32_16x16x32_bf16 v[152:155], v[232:235], v[124:127], v[152:155]\n\t" \
  "v_mfma_f32_16x16x32_bf16 v[148:151], v[236:239], v[120:123], v[148:151]\n\t" \
  "v_mfma_f32_16x16x32_bf16 v[156:159], v[236:239], v[124:127], v[156:159]\n\t" \
  "s_setprio 0\n\t" \
  ".Ls1%=:\n\t" \
  "s_add_u32 s92, s92, 512\n\t" \
  "s_addc_u32 s93, s93, 0\n\t" \
  "ds_read_b128 v[176:179], v240 offset:0\n\t" \
  "ds_read_b128 v[180:183], v240 offset:16384\n\t" \
  "ds_read_b128 v[184:187], v241 offset:0\n\t" \
  "ds_read_b128 v[188:191], v241 offset:16384\n\t" \
  "s_add_u32 m0, %[lw], 98304\n\t" \
  "s_nop 0\n\t" \
  "global_load_lds_dwordx4 v248, s[92:93] sc1\n\t" \
  "ds_read_b128 v[192:195], v242 offset:0\n\t" \
  "ds_read_b128 v[196:199], v242 offset:16384\n\t" \
  "ds_read_b128 v[200:203], v243 offset:0\n\t" \
  "ds_read_b128 v[204:207], v243 offset:16384\n\t" \
  "s_add_u32 m0, %[lw], 99328\n\t" \
  "s_nop 0\n\t" \
  "global_load_lds_dwordx4 v249, s[92:93] sc1\n\t" \
  "ds_read_b128 v[208:211], v240 offset:256\n\t" \
  "ds_read_b128 v[212:215], v240 offset:16640\n\t" \
  "ds_read_b128 v[216:219], v241 offset:256\n\t" \
  "ds_read_b128 v[220:223], v241 offset:16640\n\t" \
  "s_add_u32 m0, %[lw], 100352\n\t" \
  "s_nop 0\n\t" \
  "global_load_lds_dwordx4 v250, s[92:93] sc1\n\t" \
  "ds_read_b128 v[224:227], v242 offset:256\n\t" \
  "ds_read_b128 v[228:231], v242 offset:16640\n\t" \
  "ds_read_b128 v[232:235], v243 offset:256\n\t" \
  "ds_read_b128 v[236:239], v243 offset:16640\n\t" \
  "s_add_u32 m0, %[lw], 101376\n\t" \
  "s_nop 0\n\t" \
  "global_load_lds_dwordx4 v251, s[92:93] sc1\n\t" \
  "ds_write_b128 v252, v[152:155] offset:0\n\t" \
  "ds_write_b128 v252, v[156:159] offset:1024\n\t" \
  "s_waitcnt lgkmcnt(0)\n\t" \
  "s_waitcnt vmcnt(4)\n\t" \
  "s_barrier\n\t" \
  "s_setprio 2\n\t" \
  "v_mfma_f32_16x16x32_bf16 v[128:131], v[176:179], v[0:3], 0\n\t" \
  "v_mfma_f32_16x16x32_bf16 v[136:139], v[176:179], v[4:7], 0\n\t" \
  "v_mfma_f32_16x16x32_bf16 v[132:135], v[180:183], v[0:3], 0\n\t" \
  "v_mfma_f32_16x16x32_bf16 v[140:143], v[180:183], v[4:7], 0\n\t" \
  "v_mfma_f32_16x16x32_bf16 v[128:131], v[184:187], v[8:11], v[128:131]\n\t" \
  "v_mfma_f32_16x16x32_bf16 v[136:139], v[184:187], v[12:15], v[136:139]\n\t" \
  "v_mfma_f32_16x16x32_bf16 v[132:135], v[188:191], v[8:11], v[132:135]\n\t" \
  "v_mfma_f32_16x16x32_bf16 v[140:143], v[188:191], v[12:15], v[140:143]\n\t" \
  "v_mfma_f32_16x16x32_bf16 v[128:131], v[192:195], v[16:19], v[128:131]\n\t" \
  "v_mfma_f32_16x16x32_bf16 v[136:139], v[192:195], v[20:23], v[136:139]\n\t" \
  "v_mfma_f32_16x16x32_bf16 v[132:135], v[196:199], v[16:19], v[132:135]\n\t" \
  "v_mfma_f32_16x16x32_bf16 v[140:143], v[196:199], v[20:23], v[140:143]\n\t" \
  "v_mfma_f32_16x16x32_bf16 v[128:131], v[200:203], v[24:27], v[128:131]\n\t" \
  "v_mfma_f32_16x16x32_bf16 v[136:139], v[200:203], v[28:31], v[136:139]\n\t" \
  "v_mfma_f32_16x16x32_bf16 v[132:135], v[204:207], v[24:27], v[132:135]\n\t" \
  "v_mfma_f32_16x16x32_bf16 v[140:143], v[204:207], v[28:31], v[140:143]\n\t" \
  "v_mfma_f32_16x16x32_bf16 v[128:131], v[208:211], v[32:35], v[128:131]\n\t" \
  "v_mfma_f32_16x16x32_bf16 v[136:139], v[208:211], v[36:39], v[136:139]\n\t" \
  "v_mfma_f32_16x16x32_bf16 v[132:135], v[212:215], v[32:35], v[132:135]\n\t" \
  "v_mfma_f32_16x16x32_bf16 v[140:143], v[212:215], v[36:39], v[140:143]\n\t" \
  "v_mfma_f32_16x16x32_bf16 v[128:131], v[216:219], v[40:43], v[128:131]\n\t" \
  "v_mfma_f32_16x16x32_bf16 v[136:139], v[216:219], v[44:47], v[136:139]\n\t" \
  "v_mfma_f32_16x16x32_bf16 v[132:135], v[220:223], v[40:43], v[132:135]\n\t" \
  "v_mfma_f32_16x16x32_bf16 v[140:143], v[220:223], v[44:47], v[140:143]\n\t" \
  "v_mfma_f32_16x16x32_bf16 v[128:131], v[224:227], v[48:51], v[128:131]\n\t" \
  "v_mfma_f32_16x16x32_bf16 v[136:139], v[224:227], v[52:55], v[136:139]\n\t" \
  "v_mfma_f32_16x16x32_bf16 v[132:135], v[228:231], v[48:51], v[132:135]\n\t" \
  "v_mfma_f32_16x16x32_bf16 v[140:143], v[228:231], v[52:55], v[140:143]\n\t" \
  "v_mfma_f32_16x16x32_bf16 v[128:131], v[232:235], v[56:59], v[128:131]\n\t" \
  "v_mfma_f32_16x16x32_bf16 v[136:139], v[232:235], v[60:63], v[136:139]\n\t" \
  "v_mfma_f32_16x16x32_bf16 v[132:135], v[236:239], v[56:59], v[132:135]\n\t" \
  "v_mfma_f32_16x16x32_bf16 v[140:143], v[236:239], v[60:63], v[140:143]\n\t" \
  "s_setprio 0\n\t" \
  "s_add_u32 s92, s92, 65024\n\t" \
  "s_addc_u32 s93, s93, 0\n\t" \
  "ds_read_b128 v[160:163], v253 offset:0\n\t" \
  "ds_read_b128 v[164:167], v253 offset:1024\n\t" \
  "ds_read_b128 v[176:179], v240 offset:32768\n\t" \
  "ds_read_b128 v[180:183], v240 offset:49152\n\t" \
  "ds_read_b128 v[184:187], v241 offset:32768\n\t" \
  "ds_read_b128 v[188:191], v241 offset:49152\n\t" \
  "s_add_u32 m0, %[lw], 0\n\t" \
  "s_nop 0\n\t" \
  "global_load_lds_dwordx4 v248, s[92:93] sc1\n\t" \
  "ds_read_b128 v[192:195], v242 offset:32768\n\t" \
  "ds_read_b128 v[196:199], v242 offset:49152\n\t" \
  "ds_read_b128 v[200:203], v243 offset:32768\n\t" \
  "ds_read_b128 v[204:207], v243 offset:49152\n\t" \
  "s_add_u32 m0, %[lw], 1024\n\t" \
  "s_nop 0\n\t" \
  "global_load_lds_dwordx4 v249, s[92:93] sc1\n\t" \
  "ds_read_b128 v[208:211], v240 offset:33024\n\t" \
  "ds_read_b128 v[212:215], v240 offset:49408\n\t" \
  "ds_read_b128 v[216:219], v241 offset:33024\n\t" \
  "ds_read_b128 v[220:223], v241 offset:49408\n\t" \
  "s_add_u32 m0, %[lw], 2048\n\t" \
  "s_nop 0\n\t" \
  "global_load_lds_dwordx4 v250, s[92:93] sc1\n\t" \
  "ds_read_b128 v[224:227], v242 offset:33024\n\t" \
  "ds_read_b128 v[228:231], v242 offset:49408\n\t" \
  "ds_read_b128 v[232:235], v243 offset:33024\n\t" \
  "ds_read_b128 v[236:239], v243 offset:49408\n\t" \
  "s_add_u32 m0, %[lw], 3072\n\t" \
  "s_nop 0\n\t" \
  "global_load_lds_dwordx4 v251, s[92:93] sc1\n\t" \
  "s_waitcnt lgkmcnt(15)\n\t" \
  "s_waitcnt lgkmcnt(12)\n\t" \
  "s_sub_i32 s91, s86, 2\n\t" \
  "v_mov_b32 v173, s91\n\t" \
  "buffer_store_dword v173, v174, %[rs], 0 offen sc1\n\t" \
  "s_lshr_b32 s90, s87, 7\n\t" \
  "s_xor_b32 s90, s90, %[s0]\n\t" \
  "s_and_b32 s90, s90, 1\n\t" \
  "s_lshl1_add_u32 s90, s87, s90\n\t" \
  "s_lshl_b32 s90, s90, 13\n\t" \
  "v_add_f32 v160, v160, v144\n\t" \
  "v_add_f32 v161, v161, v145\n\t" \
  "v_add_f32 v162, v162, v146\n\t" \
  "v_add_f32 v163, v163, v147\n\t" \
  "v_add_f32 v164, v164, v148\n\t" \
  "v_add_f32 v165, v165, v149\n\t" \
  "v_add_f32 v166, v166, v150\n\t" \
  "v_add_f32 v167, v167, v151\n\t" \
  "v_cvt_pk_bf16_f32 v168, v160, v161\n\t" \
  "v_cvt_pk_bf16_f32 v169, v162, v163\n\t" \
  "v_cvt_pk_bf16_f32 v170, v164, v165\n\t" \
  "v_cvt_pk_bf16_f32 v171, v166, v167\n\t" \
  "buffer_store_dwordx4 v[168:171], v172, %[rt], s90 offen sc1\n\t" \
  "s_add_u32 s86, s86, 1\n\t" \
  "s_add_u32 s87, s87, 1\n\t" \
  "s_waitcnt lgkmcnt(0)\n\t" \
  "s_waitcnt vmcnt(10)\n\t" \
  "s_barrier\n\t" \
  "s_setprio 2\n\t" \
  "v_mfma_f32_16x16x32_bf16 v[128:131], v[176:179], v[64:67], v[128:131]\n\t" \
  "v_mfma_f32_16x16x32_bf16 v[136:139], v[176:179], v[68:71], v[136:139]\n\t" \
  "v_mfma_f32_16x16x32_bf16 v[132:135], v[180:183], v[64:67], v[132:135]\n\t" \
  "v_mfma_f32_16x16x32_bf16 v[140:143], v[180:183], v[68:71], v[140:143]\n\t" \
  "v_mfma_f32_16x16x32_bf16 v[128:131], v[184:187], v[72:75], v[128:131]\n\t" \
  "v_mfma_f32_16x16x32_bf16 v[136:139], v[184:187], v[76:79], v[136:139]\n\t" \
  "v_mfma_f32_16x16x32_bf16 v[132:135], v[188:191], v[72:75], v[132:135]\n\t" \
  "v_mfma_f32_16x16x32_bf16 v[140:143], v[188:191], v[76:79], v[140:143]\n\t" \
  "v_mfma_f32_16x16x32_bf16 v[128:131], v[192:195], v[80:83], v[128:131]\n\t" \
  "v_mfma_f32_16x16x32_bf16 v[136:139], v[192:195], v[84:87], v[136:139]\n\t" \
  "v_mfma_f32_16x16x32_bf16 v[132:135], v[196:199], v[80:83], v[132:135]\n\t" \
  "v_mfma_f32_16x16x32_bf16 v[140:143], v[196:199], v[84:87], v[140:143]\n\t" \
  "v_mfma_f32_16x16x32_bf16 v[128:131], v[200:203], v[88:91], v[128:131]\n\t" \
  "v_mfma_f32_16x16x32_bf16 v[136:139], v[200:203], v[92:95], v[136:139]\n\t" \
  "v_mfma_f32_16x16x32_bf16 v[132:135], v[204:207], v[88:91], v[132:135]\n\t" \
  "v_mfma_f32_16x16x32_bf16 v[140:143], v[204:207], v[92:95], v[140:143]\n\t" \
  "v_mfma_f32_16x16x32_bf16 v[128:131], v[208:211], v[96:99], v[128:131]\n\t" \
  "v_mfma_f32_16x16x32_bf16 v[136:139], v[208:211], v[100:103], v[136:139]\n\t" \
  "v_mfma_f32_16x16x32_bf16 v[132:135], v[212:215], v[96:99], v[132:135]\n\t" \
  "v_mfma_f32_16x16x32_bf16 v[140:143], v[212:215], v[100:103], v[140:143]\n\t" \
  "v_mfma_f32_16x16x32_bf16 v[128:131], v[216:219], v[104:107], v[128:131]\n\t" \
  "v_mfma_f32_16x16x32_bf16 v[136:139], v[216:219], v[108:111], v[136:139]\n\t" \
  "v_mfma_f32_16x16x32_bf16 v[132:135], v[220:223], v[104:107], v[132:135]\n\t" \
  "v_mfma_f32_16x16x32_bf16 v[140:143], v[220:223], v[108:111], v[140:143]\n\t" \
  "v_mfma_f32_16x16x32_bf16 v[128:131], v[224:227], v[112:115], v[128:131]\n\t" \
  "v_mfma_f32_16x16x32_bf16 v[136:139], v[224:227], v[116:119], v[136:139]\n\t" \
  "v_mfma_f32_16x16x32_bf16 v[132:135], v[228:231], v[112:115], v[132:135]\n\t" \
  "v_mfma_f32_16x16x32_bf16 v[140:143], v[228:231], v[116:119], v[140:143]\n\t" \
  "v_mfma_f32_16x16x32_bf16 v[128:131], v[232:235], v[120:123], v[128:131]\n\t" \
  "v_mfma_f32_16x16x32_bf16 v[136:139], v[232:235], v[124:127], v[136:139]\n\t" \
  "v_mfma_f32_16x16x32_bf16 v[132:135], v[236:239], v[120:123], v[132:135]\n\t" \
  "v_mfma_f32_16x16x32_bf16 v[140:143], v[236:239], v[124:127], v[140:143]\n\t" \
  "s_setprio 0\n\t" \
  "s_add_u32 s92, s92, 512\n\t" \
  "s_addc_u32 s93, s93, 0\n\t" \
  "ds_read_b128 v[176:179], v244 offset:0\n\t" \
  "ds_read_b128 v[180:183], v244 offset:16384\n\t" \
  "ds_read_b128 v[184:187], v245 offset:0\n\t" \
  "ds_read_b128 v[188:191], v245 offset:16384\n\t" \
  "s_add_u32 m0, %[lw], 32768\n\t" \
  "s_nop 0\n\t" \
  "global_load_lds_dwordx4 v248, s[92:93] sc1\n\t" \
  "ds_read_b128 v[192:195], v246 offset:0\n\t" \
  "ds_read_b128 v[196:199], v246 offset:16384\n\t" \
  "ds_read_b128 v[200:203], v247 offset:0\n\t" \
  "ds_read_b128 v[204:207], v247 offset:16384\n\t" \
  "s_add_u32 m0, %[lw], 33792\n\t" \
  "s_nop 0\n\t" \
  "global_load_lds_dwordx4 v249, s[92:93] sc1\n\t" \
  "ds_read_b128 v[208:211], v244 offset:256\n\t" \
  "ds_read_b128 v[212:215], v244 offset:16640\n\t" \
  "ds_read_b128 v[216:219], v245 offset:256\n\t" \
  "ds_read_b128 v[220:223], v245 offset:16640\n\t" \
  "s_add_u32 m0, %[lw], 34816\n\t" \
  "s_nop 0\n\t" \
  "global_load_lds_dwordx4 v250, s[92:93] sc1\n\t" \
  "ds_read_b128 v[224:227], v246 offset:256\n\t" \
  "ds_read_b128 v[228:231], v246 offset:16640\n\t" \
  "ds_read_b128 v[232:235], v247 offset:256\n\t" \
  "ds_read_b128 v[236:239], v247 offset:16640\n\t" \
  "s_add_u32 m0, %[lw], 35840\n\t" \
  "s_nop 0\n\t" \
  "global_load_lds_dwordx4 v251, s[92:93] sc1\n\t" \
  "ds_write_b128 v252, v[136:139] offset:0\n\t" \
  "ds_write_b128 v252, v[140:143] offset:1024\n\t" \
  "s_waitcnt lgkmcnt(0)\n\t" \
  "s_waitcnt vmcnt(4)\n\t" \
  "s_barrier\n\t" \
  "s_setprio 2\n\t" \
  "v_mfma_f32_16x16x32_bf16 v[144:147], v[176:179], v[0:3], 0\n\t" \
  "v_mfma_f32_16x16x32_bf16 v[152:155], v[176:179], v[4:7], 0\n\t" \
  "v_mfma_f32_16x16x32_bf16 v[148:151], v[180:183], v[0:3], 0\n\t" \
  "v_mfma_f32_16x16x32_bf16 v[156:159], v[180:183], v[4:7], 0\n\t" \
  "v_mfma_f32_16x16x32_bf16 v[144:147], v[184:187], v[8:11], v[144:147]\n\t" \
  "v_mfma_f32_16x16x32_bf16 v[152:155], v[184:187], v[12:15], v[152:155]\n\t" \
  "v_mfma_f32_16x16x32_bf16 v[148:151], v[188:191], v[8:11], v[148:151]\n\t" \
  "v_mfma_f32_16x16x32_bf16 v[156:159], v[188:191], v[12:15], v[156:159]\n\t" \
  "v_mfma_f32_16x16x32_bf16 v[144:147], v[192:195], v[16:19], v[144:147]\n\t" \
  "v_mfma_f32_16x16x32_bf16 v[152:155], v[192:195], v[20:23], v[152:155]\n\t" \
  "v_mfma_f32_16x16x32_bf16 v[148:151], v[196:199], v[16:19], v[148:151]\n\t" \
  "v_mfma_f32_16x16x32_bf16 v[156:159], v[196:199], v[20:23], v[156:159]\n\t" \
  "v_mfma_f32_16x16x32_bf16 v[144:147], v[200:203], v[24:27], v[144:147]\n\t" \
  "v_mfma_f32_16x16x32_bf16 v[152:155], v[200:203], v[28:31], v[152:155]\n\t" \
  "v_mfma_f32_16x16x32_bf16 v[148:151], v[204:207], v[24:27], v[148:151]\n\t" \
  "v_mfma_f32_16x16x32_bf16 v[156:159], v[204:207], v[28:31], v[156:159]\n\t" \
  "v_mfma_f32_16x16x32_bf16 v[144:147], v[208:211], v[32:35], v[144:147]\n\t" \
  "v_mfma_f32_16x16x32_bf16 v[152:155], v[208:211], v[36:39], v[152:155]\n\t" \
  "v_mfma_f32_16x16x32_bf16 v[148:151], v[212:215], v[32:35], v[148:151]\n\t" \
  "v_mfma_f32_16x16x32_bf16 v[156:159], v[212:215], v[36:39], v[156:159]\n\t" \
  "v_mfma_f32_16x16x32_bf16 v[144:147], v[216:219], v[40:43], v[144:147]\n\t" \
  "v_mfma_f32_16x16x32_bf16 v[152:155], v[216:219], v[44:47], v[152:155]\n\t" \
  "v_mfma_f32_16x16x32_bf16 v[148:151], v[220:223], v[40:43], v[148:151]\n\t" \
  "v_mfma_f32_16x16x32_bf16 v[156:159], v[220:223], v[44:47], v[156:159]\n\t" \
  "v_mfma_f32_16x16x32_bf16 v[144:147], v[224:227], v[48:51], v[144:147]\n\t" \
  "v_mfma_f32_16x16x32_bf16 v[152:155], v[224:227], v[52:55], v[152:155]\n\t" \
  "v_mfma_f32_16x16x32_bf16 v[148:151], v[228:231], v[48:51], v[148:151]\n\t" \
  "v_mfma_f32_16x16x32_bf16 v[156:159], v[228:231], v[52:55], v[156:159]\n\t" \
  "v_mfma_f32_16x16x32_bf16 v[144:147], v[232:235], v[56:59], v[144:147]\n\t" \
  "v_mfma_f32_16x16x32_bf16 v[152:155], v[232:235], v[60:63], v[152:155]\n\t" \
  "v_mfma_f32_16x16x32_bf16 v[148:151], v[236:239], v[56:59], v[148:151]\n\t" \
  "v_mfma_f32_16x16x32_bf16 v[156:159], v[236:239], v[60:63], v[156:159]\n\t" \
  "s_setprio 0\n\t" \
  "s_add_u32 s92, s92, 65024\n\t" \
  "s_addc_u32 s93, s93, 0\n\t" \
  "ds_read_b128 v[160:163], v253 offset:0\n\t" \
  "ds_read_b128 v[164:167], v253 offset:1024\n\t" \
  "ds_read_b128 v[176:179], v244 offset:32768\n\t" \
  "ds_read_b128 v[180:183], v244 offset:49152\n\t" \
  "ds_read_b128 v[184:187], v245 offset:32768\n\t" \
  "ds_read_b128 v[188:191], v245 offset:49152\n\t" \
  "s_add_u32 m0, %[lw], 65536\n\t" \
  "s_nop 0\n\t" \
  "global_load_lds_dwordx4 v248, s[92:93] sc1\n\t" \
  "ds_read_b128 v[192:195], v246 offset:32768\n\t" \
  "ds_read_b128 v[196:199], v246 offset:49152\n\t" \
  "ds_read_b128 v[200:203], v247 offset:32768\n\t" \
  "ds_read_b128 v[204:207], v247 offset:49152\n\t" \
  "s_add_u32 m0, %[lw], 66560\n\t" \
  "s_nop 0\n\t" \
  "global_load_lds_dwordx4 v249, s[92:93] sc1\n\t" \
  "ds_read_b128 v[208:211], v244 offset:33024\n\t" \
  "ds_read_b128 v[212:215], v244 offset:49408\n\t" \
  "ds_read_b128 v[216:219], v245 offset:33024\n\t" \
  "ds_read_b128 v[220:223], v245 offset:49408\n\t" \
  "s_add_u32 m0, %[lw], 67584\n\t" \
  "s_nop 0\n\t" \
  "global_load_lds_dwordx4 v250, s[92:93] sc1\n\t" \
  "ds_read_b128 v[224:227], v246 offset:33024\n\t" \
  "ds_read_b128 v[228:231], v246 offset:49408\n\t" \
  "ds_read_b128 v[232:235], v247 offset:33024\n\t" \
  "ds_read_b128 v[236:239], v247 offset:49408\n\t" \
  "s_add_u32 m0, %[lw], 68608\n\t" \
  "s_nop 0\n\t" \
  "global_load_lds_dwordx4 v251, s[92:93] sc1\n\t" \
  "s_waitcnt lgkmcnt(15)\n\t" \
  "s_waitcnt lgkmcnt(12)\n\t" \
  "s_sub_i32 s91, s86, 2\n\t" \
  "v_mov_b32 v173, s91\n\t" \
  "buffer_store_dword v173, v174, %[rs], 0 offen sc1\n\t" \
  "s_lshr_b32 s90, s87, 7\n\t" \
  "s_xor_b32 s90, s90, %[s0]\n\t" \
  "s_and_b32 s90, s90, 1\n\t" \
  "s_lshl1_add_u32 s90, s87, s90\n\t" \
  "s_lshl_b32 s90, s90, 13\n\t" \
  "v_add_f32 v160, v160, v128\n\t" \
  "v_add_f32 v161, v161, v129\n\t" \
  "v_add_f32 v162, v162, v130\n\t" \
  "v_add_f32 v163, v163, v131\n\t" \
  "v_add_f32 v164, v164, v132\n\t" \
  "v_add_f32 v165, v165, v133\n\t" \
  "v_add_f32 v166, v166, v134\n\t" \
  "v_add_f32 v167, v167, v135\n\t" \
  "v_cvt_pk_bf16_f32 v168, v160, v161\n\t" \
  "v_cvt_pk_bf16_f32 v169, v162, v163\n\t" \
  "v_cvt_pk_bf16_f32 v170, v164, v165\n\t" \
  "v_cvt_pk_bf16_f32 v171, v166, v167\n\t" \
  "buffer_store_dwordx4 v[168:171], v172, %[rt], s90 offen sc1\n\t" \
  "s_add_u32 s86, s86, 1\n\t" \
  "s_add_u32 s87, s87, 1\n\t" \
  "s_waitcnt lgkmcnt(0)\n\t" \
  "s_cmp_lt_u32 s86, 64\n\t" \
  "s_cbranch_scc1 .Lp1%=\n\t" \
  "s_barrier\n\t" \
  "s_setprio 2\n\t" \
  "v_mfma_f32_16x16x32_bf16 v[144:147], v[176:179], v[64:67], v[144:147]\n\t" \
  "v_mfma_f32_16x16x32_bf16 v[152:155], v[176:179], v[68:71], v[152:155]\n\t" \
  "v_mfma_f32_16x16x32_bf16 v[148:151], v[180:183], v[64:67], v[148:151]\n\t" \
  "v_mfma_f32_16x16x32_bf16 v[156:159], v[180:183], v[68:71], v[156:159]\n\t" \
  "v_mfma_f32_16x16x32_bf16 v[144:147], v[184:187], v[72:75], v[144:147]\n\t" \
  "v_mfma_f32_16x16x32_bf16 v[152:155], v[184:187], v[76:79], v[152:155]\n\t" \
  "v_mfma_f32_16x16x32_bf16 v[148:151], v[188:191], v[72:75], v[148:151]\n\t" \
  "v_mfma_f32_16x16x32_bf16 v[156:159], v[188:191], v[76:79], v[156:159]\n\t" \
  "v_mfma_f32_16x16x32_bf16 v[144:147], v[192:195], v[80:83], v[144:147]\n\t" \
  "v_mfma_f32_16x16x32_bf16 v[152:155], v[192:195], v[84:87], v[152:155]\n\t" \
  "v_mfma_f32_16x16x32_bf16 v[148:151], v[196:199], v[80:83], v[148:151]\n\t" \
  "v_mfma_f32_16x16x32_bf16 v[156:159], v[196:199], v[84:87], v[156:159]\n\t" \
  "v_mfma_f32_16x16x32_bf16 v[144:147], v[200:203], v[88:91], v[144:147]\n\t" \
  "v_mfma_f32_16x16x32_bf16 v[152:155], v[200:203], v[92:95], v[152:155]\n\t" \
  "v_mfma_f32_16x16x32_bf16 v[148:151], v[204:207], v[88:91], v[148:151]\n\t" \
  "v_mfma_f32_16x16x32_bf16 v[156:159], v[204:207], v[92:95], v[156:159]\n\t" \
  "v_mfma_f32_16x16x32_bf16 v[144:147], v[208:211], v[96:99], v[144:147]\n\t" \
  "v_mfma_f32_16x16x32_bf16 v[152:155], v[208:211], v[100:103], v[152:155]\n\t" \
  "v_mfma_f32_16x16x32_bf16 v[148:151], v[212:215], v[96:99], v[148:151]\n\t" \
  "v_mfma_f32_16x16x32_bf16 v[156:159], v[212:215], v[100:103], v[156:159]\n\t" \
  "v_mfma_f32_16x16x32_bf16 v[144:147], v[216:219], v[104:107], v[144:147]\n\t" \
  "v_mfma_f32_16x16x32_bf16 v[152:155], v[216:219], v[108:111], v[152:155]\n\t" \
  "v_mfma_f32_16x16x32_bf16 v[148:151], v[220:223], v[104:107], v[148:151]\n\t" \
  "v_mfma_f32_16x16x32_bf16 v[156:159], v[220:223], v[108:111], v[156:159]\n\t" \
  "v_mfma_f32_16x16x32_bf16 v[144:147], v[224:227], v[112:115], v[144:147]\n\t" \
  "v_mfma_f32_16x16x32_bf16 v[152:155], v[224:227], v[116:119], v[152:155]\n\t" \
  "v_mfma_f32_16x16x32_bf16 v[148:151], v[228:231], v[112:115], v[148:151]\n\t" \
  "v_mfma_f32_16x16x32_bf16 v[156:159], v[228:231], v[116:119], v[156:159]\n\t" \
  "v_mfma_f32_16x16x32_bf16 v[144:147], v[232:235], v[120:123], v[144:147]\n\t" \
  "v_mfma_f32_16x16x32_bf16 v[152:155], v[232:235], v[124:127], v[152:155]\n\t" \
  "v_mfma_f32_16x16x32_bf16 v[148:151], v[236:239], v[120:123], v[148:151]\n\t" \
  "v_mfma_f32_16x16x32_bf16 v[156:159], v[236:239], v[124:127], v[156:159]\n\t" \
  "s_setprio 0\n\t" \
  "s_nop 4\n\t" \
  "ds_write_b128 v252, v[152:155] offset:0\n\t" \
  "s_nop 0\n\t" \
  "ds_write_b128 v252, v[156:159] offset:1024\n\t" \
  "s_waitcnt lgkmcnt(0)\n\t" \
  "s_barrier\n\t" \
  "ds_read_b128 v[160:163], v253 offset:0\n\t" \
  "ds_read_b128 v[164:167], v253 offset:1024\n\t" \
  "s_waitcnt lgkmcnt(0)\n\t" \
  "s_lshr_b32 s90, s87, 7\n\t" \
  "s_xor_b32 s90, s90, %[s0]\n\t" \
  "s_and_b32 s90, s90, 1\n\t" \
  "s_lshl1_add_u32 s90, s87, s90\n\t" \
  "s_lshl_b32 s90, s90, 13\n\t" \
  "v_add_f32 v160, v160, v144\n\t" \
  "v_add_f32 v161, v161, v145\n\t" \
  "v_add_f32 v162, v162, v146\n\t" \
  "v_add_f32 v163, v163, v147\n\t" \
  "v_add_f32 v164, v164, v148\n\t" \
  "v_add_f32 v165, v165, v149\n\t" \
  "v_add_f32 v166, v166, v150\n\t" \
  "v_add_f32 v167, v167, v151\n\t" \
  "v_cvt_pk_bf16_f32 v168, v160, v161\n\t" \
  "v_cvt_pk_bf16_f32 v169, v162, v163\n\t" \
  "v_cvt_pk_bf16_f32 v170, v164, v165\n\t" \
  "v_cvt_pk_bf16_f32 v171, v166, v167\n\t" \
  "buffer_store_dwordx4 v[168:171], v172, %[rt], s90 offen sc1\n\t" \
  "s_waitcnt vmcnt(0)\n\t" \
  "s_barrier\n\t" \
  "s_sub_i32 s91, s86, 1\n\t" \
  "v_mov_b32 v173, s91\n\t" \
  "buffer_store_dword v173, v174, %[rs], 0 offen sc1\n\t" \
  "s_mov_b32 m0, s94\n\t" \
  ".Le%=:\n\t"
constexpr int W_X = 131072, W_PM = W_X + 16384;
template <bool ISV>
__device__ __forceinline__ void job_proj(Args& p, int g, int which, int h, char* smem, __amdgpu_buffer_rsrc_t rsy) {
  const int tid = threadIdx.x, lane = tid & 63, r = lane & 15, q = lane >> 4;
  const int w = __builtin_amdgcn_readfirstlane(tid >> 6), kh = w >> 2, cg = w & 3;
  u32x32 W[4];
  const unsigned lw = lds_off(smem) + w * 4096;
  u32x4 vx;
  u32x2 tag;
  const char* xn = p.ws + WS_XN;
  auto row = [&](int t, int i) {
    const char* src = xn + (size_t)(min(t, 127) >> 1) * (SEG * XLD * 2) + (t & 1) * 512;
    unsigned keep;
    asm volatile("s_mov_b32 %0, m0\n\ts_mov_b32 m0, %3\n\ts_nop 0\n\tglobal_load_lds_dwordx4 %1, %2 sc1\n\ts_mov_b32 m0, %0"
                 : "=&s"(keep) : "v"(vx[i]), "s"(src), "s"(lw + (t & 3) * 32768 + i * 1024) : "memory");
  };
  {
    const u16* Wm = which == 0 ? p.Wk : p.Wq;
    asm volatile("" : "+s"(Wm));
    if (ISV) Wm = p.Wv;
    const unsigned vo = (tid >> 4) * (HD * 2) + 16 * ((lane & 15) ^ swzB((tid >> 4) & 15));
    auto stage = [&](int j) {
      const char* src = (const char*)(Wm + (size_t)(WREV ? 7 - j : j) * 64 * HD + h * DH);
      const unsigned dst = lds_off(smem) + (j & 3) * 32768 + w * 1024;
      glds2(src, 32 * HD * 2, vo, dst);
      glds2(src + 512 * HD * 2, 32 * HD * 2, vo, dst + 16384);
    };
    const unsigned own = min((h & LN_M) ^ LN_X, 1);
    tag = start_rows<16>(p, 32 + g, own, [&]() { stage(0); stage(1); stage(2); stage(3); });
#pragma unroll
    for (int i = 0; i < 4; ++i) vx[i] = ((4 * w + i) ^ (ISV ? 0 : (lane >> 1) & 16)) * (XLD * 2) + (lane >> 5) * 1024 + (((lane & 31) ^ ((4 * w + i) & 15)) << 4);
    Boot bt;
#pragma unroll
    for (int j = 0; j < 8; ++j) {
      if (j < 4) WAIT_V(16); else if (j == 4) WAIT_V(12); else if (j == 5) WAIT_V(8); else if (j == 6) WAIT_V(4); else WAIT_V(10);
      if (j == 6 && w == 0) lds_at(smem + W_PM)[0] = boot_done(p.ws, bt, true, tag);
      LBAR();
      if (j == 4 && own) report_rows(p, 32 + g, tag);
      const char* sb = smem + (j & 3) * 32768 + kh * 16384;
#pragma unroll
      for (int k = 0; k < 4; ++k) {
        const int ks = k >> 1, a = k & 1;
        const unsigned ch = ISV ? 2 * (2 * cg + (a ^ kh)) + ((r & 3) >> 1) : 4 * cg + (r & 3), by = ISV ? 8 * (r & 1) : 8 * a;
        s16x4 lo = __builtin_amdgcn_ds_read_tr16_b64_v4i16((lds_s16x4_ptr)(sb + offB(32 * ks + 8 * q + (r >> 2), ch) + by));
        s16x4 hi = __builtin_amdgcn_ds_read_tr16_b64_v4i16((lds_s16x4_ptr)(sb + offB(32 * ks + 8 * q + 4 + (r >> 2), ch) + by));
        const int f = 4 * (WREV ? 7 - j : j) + k;
        put4(W[f >> 3], f & 7, (bf16x8){lo[0], lo[1], lo[2], lo[3], hi[0], hi[1], hi[2], hi[3]});
      }
      LBAR();
      if (j == 3 && w == 0) boot_ask(p.ws, bt);
      if (j + 4 < 8) stage(j + 4);
      if (j == 6) {
#pragma unroll
        for (int i = 0; i < 10; ++i) row(i >> 2, i & 3);
      }
    }
    p.s0 = uni(lds_at(smem + W_PM)[0] >> 31);
  }
  typedef __attribute__((ext_vector_type(8))) unsigned u32x8;
  u32x8 la;
#pragma unroll
  for (int i = 0; i < 8; ++i) la[i] = (i >> 2) * 65536 + r * 1024 + kh * 512 + (((4 * (i & 3) + q) ^ r) << 4);
  const unsigned xw = W_X + w * 2048 + lane * 16;
  u32x32 acc;
  asm volatile("" : "={v[128:159]}"(acc));
  WAIT_V(6);
  LBAR();
  {
    const int g0 = 0;
#define PR_LOOP(TXT)                                                                                                              \
    asm volatile(TXT                                                                                                              \
        : "+{v[128:159]}"(acc)                                                                                                    \
        : W4_IN, "{v[208:215]}"(la), "{v[216:219]}"(vx), "{v220}"(xw), "{v221}"(xw ^ 8192),                                       \
          "{v172}"((ISV ? 2 * cg + kh : 4 * kh + cg) * 1024 + lane * 16), "{v174}"(tid == 256 ? fkq(h, which) : 0x7F000000),      \
          [rt] "s"(raw_rsrc(p.ws + WS_T, T_BYTES)), [rs] "s"(raw_rsrc(p.ws + WS_SY, SY_TOTAL)), [s0] "s"(p.s0 ^ TF_KQV),          \
          [lw] "s"(lw), [kh] "s"(kh), [g0] "s"(g0), [end] "s"(g0 ? NSEG : PR_G1), [x0] "s"(xn + (g0 + 1) * (SEG * XLD * 2)),                       \
          [t0] "s"(which * T_Q + kseg(0, h) - 1)                                                                                  \
        : CLB8(160, 161, 162, 163, 164, 165, 166, 167), "v168", "v169", "v170", "v171", "v173", CLB8(176, 177, 178, 179, 180, 181, 182, 183), \
          CLB8(184, 185, 186, 187, 188, 189, 190, 191), CLB8(192, 193, 194, 195, 196, 197, 198, 199),                             \
          CLB8(200, 201, 202, 203, 204, 205, 206, 207), "s86", "s87", "s88", "s90", "s91", "s92", "s93", "s94", "scc", "memory")
    if (ISV) PR_LOOP(PR_ASM_1); else PR_LOOP(PR_ASM_0);
    if (w == 0 && g0 == 0) wait_rows(p.ws, tag);
  }
#define PP_LOOP(TXT)                                                                                                              \
  asm volatile(TXT                                                                                                                \
      : "+{v[128:159]}"(acc)                                                                                                      \
      : W4_IN, "{v[240:247]}"(la), "{v[248:251]}"(vx), "{v252}"(xw), "{v253}"(xw ^ 8192),                                         \
        "{v172}"((ISV ? 2 * cg + kh : 4 * kh + cg) * 1024 + lane * 16), "{v174}"(tid == 256 ? fkq(h, which) : 0x7F000000),        \
        [rt] "s"(raw_rsrc(p.ws + WS_T, T_BYTES)), [rs] "s"(raw_rsrc(p.ws + WS_SY, SY_TOTAL)), [s0] "s"(p.s0 ^ TF_KQV),            \
        [lw] "s"(lw), [kh] "s"(kh), [g0] "n"(PR_G1), [x0] "s"(xn + (PR_G1 + 1) * (SEG * XLD * 2)),                                \
        [t0] "s"(which * T_Q + kseg(0, h) - 1)                                                                                    \
      : CLB8(160, 161, 162, 163, 164, 165, 166, 167), "v168", "v169", "v170", "v171", "v173", CLB8(176, 177, 178, 179, 180, 181, 182, 183), \
        CLB8(184, 185, 186, 187, 188, 189, 190, 191), CLB32(192), CLB8(224, 225, 226, 227, 228, 229, 230, 231),                   \
        CLB8(232, 233, 234, 235, 236, 237, 238, 239), "s86", "s87", "s90", "s91", "s92", "s93", "s94", "scc", "memory")
  if (ISV) PP_LOOP(PP_ASM_1); else PP_LOOP(PP_ASM_0);
}

enum { J_NONE = 0, J_GU = 3, J_PR = 5, J_CB = 8, J_DN = 14 };
struct BigJob { int type, panel, idx; };
struct BigPtr { const char *a, *b0, *b1; };
constexpr int G_LDA = XLD * 2, G_LDB = FF * 2;

__device__ __forceinline__ BigPtr big_ptrs(const Args& p, const BigJob& j) {
  BigPtr r;
  r.a = p.ws + WS_XN + (size_t)j.panel * 128 * G_LDA;
  r.b0 = (const char*)(p.wg + j.idx * 128); r.b1 = (const char*)(p.wu + j.idx * 128);
  return r;
}
__device__ __forceinline__ void big_publish(const Args& p, const BigJob& j) {
  int tid = threadIdx.x;
  asm volatile("" : "+v"(tid));
  if (j.type == J_GU && tid == 0)
    __hip_atomic_fetch_add((gu32*)(p.ws + WS_SY + SY_CH) + j.panel * 16, 1u, __ATOMIC_RELAXED, __HIP_MEMORY_SCOPE_AGENT);
}
typedef f32x4 Acc[2][2][4][2];
__device__ __forceinline__ void big_epilogue(const Args& p, const BigJob& j, Acc& acc, char* smem) {
  int tid = threadIdx.x;
  asm volatile("" : "+v"(tid));
  const int lane = tid & 63;
  const int wid = __builtin_amdgcn_readfirstlane(tid >> 6);
  const int wr = wid >> 2, wc = wid & 3;
  {
    const __amdgpu_buffer_rsrc_t ro = mkrsrc(p.ws + WS_HF, S_LEN * 16384);
#pragma unroll
    for (int i = 0; i < 8; ++i) {
      f32x4 hh[2];
#pragma unroll
      for (int n = 0; n < 2; ++n)
#pragma unroll
        for (int jj = 0; jj < 4; ++jj) {
          const float g = acc[i >> 2][0][i & 3][n][jj], u = acc[i >> 2][1][i & 3][n][jj];
          hh[n][jj] = __builtin_amdgcn_rcpf(1.0f + __builtin_amdgcn_exp2f(g * -1.4426950408889634f)) * u;
        }
      st_sc1(ro, h_off(p, j.panel + (i >> 2)) + (j.idx * 4 + wc) * 16384 + (wr * 4 + (i & 3)) * 1024 + lane * 16,
             ((u32x4){cvtpk(hh[0][0], hh[0][1]), cvtpk(hh[0][2], hh[0][3]), cvtpk(hh[1][0], hh[1][1]), cvtpk(hh[1][2], hh[1][3])}));
    }
  }
}
__device__ __forceinline__ void gemm_role(Args& p, int g, char* smem) {
  const __amdgpu_buffer_rsrc_t rsy = mkrsrc(p.ws + WS_SY, SY_TOTAL);
  const int x = g & 3, k = g >> 2;
  volatile unsigned* slot = reinterpret_cast<volatile unsigned*>(smem + 163824);
  int rnd = k < 24 ? 2 : -1;
  auto claim = [&]() {
    ++rnd;
    const int n = k < 24 ? 32 + k : rnd * 8 + k - 24, pn = (n * 37) >> 8;
    BigJob b;
    b.type = rnd < 4 ? J_GU : (rnd == 4 ? J_DN : (rnd == 5 && k >= 24 && k - 24 < 14 - NFUSE ? J_CB : J_NONE));
    b.panel = rnd < 4 ? 14 - 2 * pn : (rnd == 4 ? 4 * ((2 - (k >> 3)) & 3) + x : NFUSE + k - 24);
    b.idx = rnd < 4 ? x + 4 * (n - 7 * pn) : k & 7;
    return b;
  };
  if (k < 24) {
    if (k >= 16) job_proj<true>(p, g, 2, x * 8 + (k & 7), smem, rsy);
    else job_proj<false>(p, g, k >> 3, x * 8 + (k & 7), smem, rsy);
  } else {
    u32x2 tag;
#pragma unroll 1
    for (int u = 0; u < (k < 30 && LN_M ? 2 : 1); ++u) {
      const int rg = u ? 32 + (4 * (k - 24) + 1) * 4 + x : g - 96;
      tag = start_rows<0>(p, rg, 1, []() {});
      WAIT_V(0);
      LBAR();
      report_rows(p, rg, tag);
    }
#pragma unroll 1
    for (int i = 0; i < PF_N; ++i) {
      const int m = i % 3, o = ((i / 3) * 32 + g - 96) * 448 + (int)threadIdx.x;
      if (threadIdx.x < 448) touch_line((const char*)(m == 0 ? p.wg : (m == 1 ? p.wu : p.wd)) + (size_t)o * 128, smem + (threadIdx.x >> 6) * 256);
      WAIT_V(1);
    }
    WAIT_V(0);
    if (threadIdx.x < 64) {
      wait_rows(p.ws, tag);
      Boot bt;
      boot_ask(p.ws, bt);
      WAIT_V(0);
      *slot = boot_done(p.ws, bt, true, tag);
    }
    LBAR();
    p.s0 = *slot >> 31;
  }
  p.s0 = uni(p.s0);
  BigJob cur = claim();
  while (cur.type != J_NONE) {
    if (cur.type != J_GU) {
      if (cur.type == J_DN) job_down(p, cur.panel, cur.idx, smem, rsy);
      else job_comb(p, cur.panel, x, rsy);
      cur = claim();
      continue;
    }
    int tid = threadIdx.x;
    asm volatile("" : "+v"(tid));
    const int lane = tid & 63;
    const int wid = __builtin_amdgcn_readfirstlane(tid >> 6);
    const int wr = wid >> 2, wc = wid & 3;
    const unsigned ra = tid >> 3, ca = 16 * ((tid & 7) ^ ((tid >> 3) & 7)), rb = tid >> 4, cb = 16 * ((tid & 15) ^ swzB((tid >> 4) & 15));
    const unsigned lw = lds_off(smem) + wid * 1024;
#define B8_SA(b, h) (((b) * 2 + (h)) * 16384)
#define B8_SB(b, h) ((4 + (b) * 2 + (h)) * 16384)
#define B8_STA(b, h, P, kt) glds2<true>((P).a + (size_t)(h) * 128 * G_LDA + (kt) * 128, 64 * G_LDA, ra * G_LDA + ca, lw + B8_SA(b, h))
#define B8_STB(b, h, P, kt) glds2(((h) ? (P).b1 : (P).b0) + (size_t)(kt) * 64 * G_LDB, 32 * G_LDB, rb * G_LDB + cb, lw + B8_SB(b, h))
#define B8_LDA(b, h) do { _Pragma("unroll") for (int m = 0; m < 4; ++m) _Pragma("unroll") for (int kk = 0; kk < 2; ++kk) \
      At[m][kk] = ldsf(smem + B8_SA(b, h) + offA(wr * 64 + m * 16 + (lane & 15), kk * 4 + (lane >> 4))); } while (0)
#define B8_LDB(dst, b, h) do { _Pragma("unroll") for (int n = 0; n < 2; ++n) _Pragma("unroll") for (int kk = 0; kk < 2; ++kk) \
      dst[n][kk] = ld_bfrag(smem + B8_SB(b, h), kk, wc * 2 + n, lane); } while (0)
#define B8_MMA(ai, bj, Bt) do { __builtin_amdgcn_s_setprio(1); _Pragma("unroll") for (int m = 0; m < 4; ++m) _Pragma("unroll") for (int n = 0; n < 2; ++n) \
      _Pragma("unroll") for (int kk = 0; kk < 2; ++kk) acc[ai][bj][m][n] = MFMA(Bt[n][kk], At[m][kk], acc[ai][bj][m][n]); __builtin_amdgcn_s_setprio(0); } while (0)
#define WAIT_L(n) asm volatile("s_waitcnt lgkmcnt(%0)" :: "n"(n) : "memory")
#define BAR() __builtin_amdgcn_s_barrier()
#define WAIT_RX(c) asm volatile("s_waitcnt vmcnt(16)\n\ts_cmp_lg_u32 %0, 0\n\ts_cbranch_scc1 .Lrx%=\n\ts_waitcnt vmcnt(8)\n.Lrx%=:" :: "s"(c) : "memory", "scc")
    Acc acc;
    bf16x8 At[4][2], B0[2][2], B1[2][2];
    BigPtr cp = big_ptrs(p, cur);
    BigJob pend; pend.type = J_NONE; pend.panel = 0; pend.idx = 0;
    LBAR();
    B8_STB(0, 0, cp, 0); B8_STB(0, 1, cp, 0); B8_STA(0, 0, cp, 0); B8_STA(0, 1, cp, 0);
    if (wr == 1) BAR();
    WAIT_V(2); BAR();
    B8_STB(1, 0, cp, 1); B8_STA(1, 0, cp, 1); B8_STB(1, 1, cp, 1);
    WAIT_V(6); BAR();
    BigJob nx = pend;
    for (;;) {
      bool chain = false;
      BigPtr np = cp;
      constexpr int nt = DM / 64;
#pragma unroll
      for (int i = 0; i < 32; ++i) acc[i >> 4][(i >> 3) & 1][(i >> 1) & 3][i & 1] = (f32x4){0.f, 0.f, 0.f, 0.f};
#pragma unroll 1
      for (int t = 0; t < nt; t += 2) {
        const bool last = t == nt - 2;
        if (last) {
          nx = claim();
          chain = nx.type == J_GU;
          if (chain) np = big_ptrs(p, nx);
        }
        const BigPtr sp = np;
        const int t2 = last ? 0 : t + 2;
        if (t == 2) big_publish(p, pend);
        const int rx = __builtin_amdgcn_readfirstlane(t == 0 && pend.type != J_NONE);
        B8_LDB(B0, 0, 0); B8_LDB(B1, 0, 1); SCHED(); B8_LDA(0, 0); B8_STA(1, 1, cp, t + 1);
        WAIT_RX(rx);
        WAIT_L(0); BAR(); B8_MMA(0, 0, B0); B8_MMA(0, 1, B1); BAR(); SCHED();
        B8_LDA(0, 1); B8_STB(0, 0, sp, t2); B8_STB(0, 1, sp, t2); B8_STA(0, 0, sp, t2);
        WAIT_RX(rx);
        WAIT_L(0); BAR(); B8_MMA(1, 0, B0); B8_MMA(1, 1, B1); BAR(); SCHED();
        B8_LDB(B0, 1, 0); B8_LDB(B1, 1, 1); SCHED(); B8_LDA(1, 0); B8_STA(0, 1, sp, t2);
        WAIT_V(8); WAIT_L(0); BAR(); B8_MMA(0, 0, B0); B8_MMA(0, 1, B1); BAR(); SCHED();
        B8_LDA(1, 1); B8_STB(1, 0, sp, t2 + 1); B8_STB(1, 1, sp, t2 + 1); B8_STA(1, 0, sp, t2 + 1);
        WAIT_V(8); WAIT_L(0); BAR(); B8_MMA(1, 0, B0); B8_MMA(1, 1, B1); BAR(); SCHED();
      }
      if (wr == 0) BAR();
      big_epilogue(p, cur, acc, smem);
      if (!chain) break;
      pend = cur; cur = nx; cp = np;
      if (wr == 1) BAR();
    }
    WAIT_V(0);
    BAR();
    asm volatile(".Lcode_end:");
    big_publish(p, cur);
    cur = nx;
  }
}


__device__ __forceinline__ unsigned now_q() { return (unsigned)__builtin_amdgcn_s_memrealtime() * 4u; }
__device__ __forceinline__ void pace_until(unsigned t) {
#pragma unroll 1
  for (int i = 0; i < 256; ++i) {
    const int d = (int)(t - now_q());
    if (d <= 0) break;
    if (d > 32) __builtin_amdgcn_s_sleep(1);
  }
}
#define CLB_E "s76", "s77", "s78", "s79", "s80", "s81", "s82", "s83", "s84", "s85", "s86", "s87", "s88", "s89"
#define EDGE_ASM \
  "s_memtime s[76:77]\n\t" \
  "s_memrealtime s[78:79]\n\t" \
  "s_nop 3\n\t" \
  "s_memrealtime s[80:81]\n\t" \
  "s_nop 3\n\t" \
  "s_memrealtime s[82:83]\n\t" \
  "s_nop 3\n\t" \
  "s_memrealtime s[84:85]\n\t" \
  "s_nop 3\n\t" \
  "s_memrealtime s[86:87]\n\t" \
  "s_nop 3\n\t" \
  "s_memrealtime s[88:89]\n\t" \
  "s_mov_b32 s93, 0\n\t" \
  "s_waitcnt lgkmcnt(0)\n\t" \
  "s_cmp_eq_u32 s80, s78\n\t" \
  "s_addc_u32 s93, s93, 0\n\t" \
  "s_cmp_eq_u32 s82, s78\n\t" \
  "s_addc_u32 s93, s93, 0\n\t" \
  "s_cmp_eq_u32 s84, s78\n\t" \
  "s_addc_u32 s93, s93, 0\n\t" \
  "s_cmp_eq_u32 s86, s78\n\t" \
  "s_addc_u32 s93, s93, 0\n\t" \
  "s_cmp_eq_u32 s88, s78\n\t" \
  "s_addc_u32 s93, s93, 0\n\t" \
  "s_mul_i32 s93, s93, 21\n\t" \
  "s_add_u32 s76, s76, 15\n\t" \
  "s_add_u32 %[kc], s76, s93\n\t" \
  "s_lshl2_add_u32 %[kq], s78, 16\n\t"
__device__ __forceinline__ unsigned time_of(unsigned cyc) {
  unsigned c, q;
  asm volatile(EDGE_ASM : [kc] "=&s"(c), [kq] "=&s"(q) :: CLB_E, "s93", "scc");
  return q - (((c - cyc) * 11900u) >> 16);
}
#define DS_RED 20
#define DS_MEM 13
#define LEAD_DN 2
#define LEAD_UP 6
__device__ __forceinline__ u32x2 ld2_sc1(__amdgpu_buffer_rsrc_t rs, int off, int soff = 0) {
  return __builtin_amdgcn_raw_buffer_load_b64(rs, off, soff, 16);
}
__device__ __forceinline__ void st2_sc1(__amdgpu_buffer_rsrc_t rs, int off, u32x2 v) {
  __builtin_amdgcn_raw_buffer_store_b64(v, rs, off, 0, 16);
}
__device__ __forceinline__ int row_max(int v) {
  v = max(v, __builtin_amdgcn_update_dpp(0, v, 0x128, 0xf, 0xf, false));
  v = max(v, __builtin_amdgcn_update_dpp(0, v, 0x124, 0xf, 0xf, false));
  v = max(v, __builtin_amdgcn_update_dpp(0, v, 0x122, 0xf, 0xf, false));
  v = max(v, __builtin_amdgcn_update_dpp(0, v, 0x121, 0xf, 0xf, false));
  return v;
}
__device__ __forceinline__ int wave_max(int v) {
  v = row_max(v);
  return max(max(__builtin_amdgcn_readlane(v, 0), __builtin_amdgcn_readlane(v, 16)),
             max(__builtin_amdgcn_readlane(v, 32), __builtin_amdgcn_readlane(v, 48)));
}
__device__ __forceinline__ int track(int est, int x, int up, int dn, int out) {
  const int d = x - est;
  return est + (d > out ? 4 : max(min(d, up), -dn));
}
#define WSL 12
#define PLAN_SLEEP 2
#define REF_W 3
#define REF_G 60
#define RL0 315
__device__ __forceinline__ void watch(__amdgpu_buffer_rsrc_t rsy, int s, int lane, unsigned t0, const unsigned* tgt_, gu32* abortw, int hs) {
  const int rl = (int)__builtin_amdgcn_raw_buffer_load_b32(rsy, lane * 128, SY_BR + hs, 16);
  u32x2 ra;
  for (unsigned spins = 0;;) {
    asm volatile("" ::: "memory");
    ra = ld2_sc1(rsy, (lane & 31) * 128, SY_BA + hs);
    const bool all = __all((int)ra[0] >= s);
    if (all) break;
    if (spins < 3) __builtin_amdgcn_s_sleep(PLAN_SLEEP); else __builtin_amdgcn_s_sleep(WSL);
    if (spin_fail(spins, abortw)) break;
  }
  int d = ra[0] == (unsigned)s ? (int)(ra[1] - t0) : 0;
  d = max(d, __builtin_amdgcn_update_dpp(0, d, 0xB1, 0xf, 0xf, false));
  d = max(d, __builtin_amdgcn_update_dpp(0, d, 0x4E, 0xf, 0xf, false));
  int sum = d + __builtin_amdgcn_update_dpp(0, d, 0x128, 0xf, 0xf, false);
  sum += __builtin_amdgcn_update_dpp(0, sum, 0x124, 0xf, 0xf, false);
  const int mx = wave_max(d), mean = (__builtin_amdgcn_readlane(sum, 0) + __builtin_amdgcn_readlane(sum, 16)) >> 3;
  const unsigned t = t0 + mx - min(((mx - mean) * REF_W) >> 2, REF_G) + wave_max(rl < 0 ? RL0 : rl) + (DS_RED + DS_MEM);
  lds_vu_ptr tgt = lds_at(tgt_);
  if (lane < 2) tgt[lane] = lane ? t : s + 1;
}
__device__ __forceinline__ unsigned ld_flag(__amdgpu_buffer_rsrc_t rs, int off) {
  return __builtin_amdgcn_raw_buffer_load_b32(rs, off, 0, 16);
}

constexpr unsigned long long QK_MAP[8] = {0xbaf9876d543210ecull, 0xa98f76d543210bceull, 0x98f76d543210ecbaull, 0x876fd543210bcea9ull, 0x76fd543210ecba98ull, 0x65df43210bcea987ull, 0x5df43210ecba9876ull, 0xd43f210bcea98765ull};
#define QK_ASM_0 \
  "s_waitcnt lgkmcnt(0)\n\t" \
  "v_subrev_u32 v244, 0x8000, v250\n\t" \
  "ds_read_b128 v[216:219], v248 offset:32768\n\t" \
  "ds_read_b128 v[220:223], v248 offset:33792\n\t" \
  "ds_read_b128 v[224:227], v248 offset:34816\n\t" \
  "ds_read_b128 v[228:231], v248 offset:35840\n\t" \
  "ds_read_b128 v[208:211], v244 offset:0\n\t" \
  "ds_read_b128 v[212:215], v244 offset:1024\n\t" \
  "ds_read_b128 v[236:239], v244 offset:2048\n\t" \
  "ds_read_b128 v[240:243], v244 offset:3072\n\t" \
  "v_mov_b64 v[192:193], 0\n\t" \
  "v_mov_b64 v[194:195], 0\n\t" \
  "v_mov_b64 v[196:197], 0\n\t" \
  "v_mov_b64 v[198:199], 0\n\t" \
  "s_movk_i32 s92, 0x2000\n\t" \
  ".Lwt%=:\n\t" \
  "ds_read_b64 v[200:201], v248 offset:30720\n\t" \
  "s_memrealtime s[90:91]\n\t" \
  "s_sub_u32 s92, s92, 1\n\t" \
  "s_waitcnt lgkmcnt(0)\n\t" \
  "v_readfirstlane_b32 s93, v200\n\t" \
  "v_readfirstlane_b32 %[tgt], v201\n\t" \
  "s_cbranch_scc1 .Lgo%=\n\t" \
  "s_cmp_eq_u32 s93, %[step]\n\t" \
  "s_cbranch_scc1 .Lwx%=\n\t" \
  "s_sleep 1\n\t" \
  "s_branch .Lwt%=\n\t" \
  ".Lwx%=:\n\t" \
  "s_lshl_b32 s93, s90, 2\n\t" \
  "s_sub_i32 s93, %[tgt], s93\n\t" \
  "s_cmp_le_i32 s93, 0\n\t" \
  "s_cbranch_scc1 .Lgo%=\n\t" \
  "s_min_i32 s93, s93, 8000\n\t" \
  "s_mov_b32 s94, s93\n\t" \
  ".Lsl%=:\n\t" \
  "s_sub_i32 s94, s94, 18\n\t" \
  "s_cmp_lt_i32 s94, 30\n\t" \
  "s_cbranch_scc1 .Lsx%=\n\t" \
  "s_sleep 1\n\t" \
  "s_branch .Lsl%=\n\t" \
  ".Lsx%=:\n\t" \
  "s_sub_i32 s93, s93, 12\n\t" \
  "s_max_i32 s93, s93, 4\n\t" \
  "s_lshr_b32 s93, s93, 2\n\t" \
  "s_add_u32 s93, s93, s90\n\t" \
  "s_memrealtime s[90:91]\n\t" \
  ".Lpace%=:\n\t" \
  "s_memrealtime s[94:95]\n\t" \
  "s_waitcnt lgkmcnt(1)\n\t" \
  "s_sub_i32 s92, s90, s93\n\t" \
  "s_cmp_ge_i32 s92, 0\n\t" \
  "s_cbranch_scc1 .Le%=\n\t" \
  "s_memrealtime s[90:91]\n\t" \
  "s_waitcnt lgkmcnt(1)\n\t" \
  "s_sub_i32 s92, s94, s93\n\t" \
  "s_cmp_lt_i32 s92, 0\n\t" \
  "s_cbranch_scc1 .Lpace%=\n\t" \
  "s_nop 4\n\t" \
  ".Le%=:\n\t" \
  "s_add_u32 s92, s92, s93\n\t" \
  "s_lshl_b32 s92, s92, 2\n\t" \
  "s_sub_i32 s92, %[tgt], s92\n\t" \
  "s_max_i32 s92, s92, 0\n\t" \
  "s_bitcmp0_b32 s92, 3\n\t" \
  "s_cbranch_scc1 .Lf3%=\n\t" \
  "s_nop 15\n\t" \
  ".Lf3%=:\n\t" \
  "s_bitcmp0_b32 s92, 2\n\t" \
  "s_cbranch_scc1 .Lf2%=\n\t" \
  "s_nop 10\n\t" \
  ".Lf2%=:\n\t" \
  ".Lgo%=:\n\t" \
  "s_mov_b32 s92, 0\n\t" \
  ".Lpoll%=:\n\t" \
  "buffer_load_dwordx4 v[200:203], v248, %[rs], %[som] offen sc1\n\t" \
  "buffer_load_dwordx4 v[204:207], v248, %[rs], %[som2] offen sc1\n\t" \
  "s_waitcnt vmcnt(1) lgkmcnt(0)\n\t" \
  "ds_write_b128 v249, v[200:203]\n\t" \
  "v_smfmac_f32_16x16x64_bf16 v[192:195], v[200:203], v[208:215], v251\n\t" \
  "v_max3_u32 v244, v200, v201, v202\n\t" \
  "v_smfmac_f32_16x16x64_bf16 v[196:199], v[200:203], v[236:243], v251\n\t" \
  "s_waitcnt vmcnt(0)\n\t" \
  "ds_write_b128 v249, v[204:207] offset:8192\n\t" \
  "v_smfmac_f32_16x16x64_bf16 v[192:195], v[204:207], v[216:223], v251\n\t" \
  "v_max3_u32 v244, v244, v203, v204\n\t" \
  "v_max3_u32 v244, v244, v205, v206\n\t" \
  "v_smfmac_f32_16x16x64_bf16 v[196:199], v[204:207], v[224:231], v251\n\t" \
  "v_max_u32 v244, v244, v207\n\t" \
  "v_cmp_le_u32 vcc, %[sent], v244\n\t" \
  "s_cbranch_vccnz .Lslow%=\n\t" \
  ".Lok%=:\n\t" \
  "ds_read_b128 v[216:219], v250 offset:32768\n\t" \
  "ds_read_b128 v[220:223], v250 offset:33792\n\t" \
  "ds_read_b128 v[224:227], v250 offset:34816\n\t" \
  "ds_read_b128 v[228:231], v250 offset:35840\n\t" \
  "s_waitcnt lgkmcnt(4)\n\t" \
  "s_barrier\n\t" \
  "ds_read_b128 v[208:211], v248 offset:1024\n\t" \
  "ds_read_b128 v[212:215], v248 offset:2048\n\t" \
  "ds_read_b128 v[236:239], v248 offset:3072\n\t" \
  "ds_read_b128 v[240:243], v248 offset:4096\n\t" \
  "s_waitcnt lgkmcnt(3)\n\t" \
  "ds_read_b128 v[244:247], v248 offset:13312\n\t" \
  "v_smfmac_f32_16x16x64_bf16 v[192:195], v[208:211], v[216:223], v251\n\t" \
  "ds_read_b128 v[216:219], v250 offset:0\n\t" \
  "v_smfmac_f32_16x16x64_bf16 v[196:199], v[208:211], v[224:231], v251\n\t" \
  "s_waitcnt lgkmcnt(4)\n\t" \
  "ds_read_b128 v[220:223], v250 offset:1024\n\t" \
  "v_smfmac_f32_16x16x64_bf16 v[192:195], v[212:215], v[0:7], v251\n\t" \
  "ds_read_b128 v[224:227], v250 offset:2048\n\t" \
  "v_smfmac_f32_16x16x64_bf16 v[196:199], v[212:215], v[8:15], v251\n\t" \
  "s_waitcnt lgkmcnt(5)\n\t" \
  "ds_read_b128 v[228:231], v250 offset:3072\n\t" \
  "v_smfmac_f32_16x16x64_bf16 v[192:195], v[236:239], v[16:23], v251\n\t" \
  "ds_read_b128 v[200:203], v248 offset:5120\n\t" \
  "v_smfmac_f32_16x16x64_bf16 v[196:199], v[236:239], v[24:31], v251\n\t" \
  "s_waitcnt lgkmcnt(6)\n\t" \
  "ds_read_b128 v[204:207], v248 offset:6144\n\t" \
  "v_smfmac_f32_16x16x64_bf16 v[192:195], v[240:243], v[32:39], v251\n\t" \
  "ds_read_b128 v[208:211], v248 offset:7168\n\t" \
  "v_smfmac_f32_16x16x64_bf16 v[196:199], v[240:243], v[40:47], v251\n\t" \
  "s_waitcnt lgkmcnt(5)\n\t" \
  "ds_read_b128 v[212:215], v248 offset:9216\n\t" \
  "v_smfmac_f32_16x16x64_bf16 v[192:195], v[244:247], v[216:223], v251\n\t" \
  "s_waitcnt lgkmcnt(4)\n\t" \
  "v_smfmac_f32_16x16x64_bf16 v[196:199], v[244:247], v[224:231], v251\n\t" \
  "s_waitcnt lgkmcnt(3)\n\t" \
  "ds_read_b128 v[236:239], v248 offset:10240\n\t" \
  "v_smfmac_f32_16x16x64_bf16 v[192:195], v[200:203], v[48:55], v251\n\t" \
  "ds_read_b128 v[240:243], v248 offset:11264\n\t" \
  "v_smfmac_f32_16x16x64_bf16 v[196:199], v[200:203], v[56:63], v251\n\t" \
  "s_waitcnt lgkmcnt(4)\n\t" \
  "v_smfmac_f32_16x16x64_bf16 v[192:195], v[204:207], v[64:71], v251\n\t" \
  "ds_read_b128 v[244:247], v248 offset:12288\n\t" \
  "v_smfmac_f32_16x16x64_bf16 v[196:199], v[204:207], v[72:79], v251\n\t" \
  "s_waitcnt lgkmcnt(4)\n\t" \
  "v_smfmac_f32_16x16x64_bf16 v[192:195], v[208:211], v[80:87], v251\n\t" \
  "ds_read_b128 v[200:203], v248 offset:14336\n\t" \
  "v_smfmac_f32_16x16x64_bf16 v[196:199], v[208:211], v[88:95], v251\n\t" \
  "s_waitcnt lgkmcnt(4)\n\t" \
  "v_smfmac_f32_16x16x64_bf16 v[192:195], v[212:215], v[96:103], v251\n\t" \
  "ds_read_b128 v[204:207], v248 offset:15360\n\t" \
  "v_smfmac_f32_16x16x64_bf16 v[196:199], v[212:215], v[104:111], v251\n\t" \
  "s_waitcnt lgkmcnt(3)\n\t" \
  "v_smfmac_f32_16x16x64_bf16 v[192:195], v[236:239], v[112:119], v251\n\t" \
  "v_smfmac_f32_16x16x64_bf16 v[196:199], v[236:239], v[120:127], v251\n\t" \
  "v_smfmac_f32_16x16x64_bf16 v[192:195], v[240:243], v[128:135], v251\n\t" \
  "v_smfmac_f32_16x16x64_bf16 v[196:199], v[240:243], v[136:143], v251\n\t" \
  "s_waitcnt lgkmcnt(1)\n\t" \
  "v_smfmac_f32_16x16x64_bf16 v[192:195], v[244:247], v[144:151], v251\n\t" \
  "v_smfmac_f32_16x16x64_bf16 v[196:199], v[244:247], v[152:159], v251\n\t" \
  "v_smfmac_f32_16x16x64_bf16 v[192:195], v[200:203], v[160:167], v251\n\t" \
  "v_smfmac_f32_16x16x64_bf16 v[196:199], v[200:203], v[168:175], v251\n\t" \
  "s_waitcnt lgkmcnt(0)\n\t" \
  "v_smfmac_f32_16x16x64_bf16 v[192:195], v[204:207], v[176:183], v251\n\t" \
  "v_smfmac_f32_16x16x64_bf16 v[196:199], v[204:207], v[184:191], v251\n\t" \
  "ds_read_b128 v[200:203], v248 offset:16384\n\t" \
  "ds_read_b128 v[204:207], v248 offset:20480\n\t" \
  "v_mov_b32 v250, v232\n\t" \
  "v_mov_b32 v249, v233\n\t" \
  "s_nop 2\n\t" \
  "v_add_f32 v192, v192, v194\n\t" \
  "v_add_f32 v193, v193, v195\n\t" \
  "v_add_f32 v196, v196, v198\n\t" \
  "v_add_f32 v197, v197, v199\n\t" \
  "v_cvt_pk_bf16_f32 v192, v192, v193\n\t" \
  "v_cvt_pk_bf16_f32 v196, v196, v197\n\t" \
  "ds_write2_b32 v252, v192, v196 offset1:64\n\t" \
  "v_mov_b32 v251, v234\n\t" \
  "ds_read_b64_tr_b16 v[232:233], v250 offset:0\n\t" \
  "ds_read_b64_tr_b16 v[234:235], v250 offset:64\n\t" \
  "v_mov_b32 v212, %[step]\n\t" \
  "s_mov_b64 exec, 1\n\t" \
  "ds_write_b32 v248, v212 offset:28672\n\t" \
  "s_mov_b64 exec, -1\n\t" \
  "buffer_load_dwordx4 v[192:195], v248, %[rt], %[svt] offen offset:0 sc1\n\t" \
  "buffer_load_dwordx4 v[196:199], v248, %[rt], %[svt] offen offset:1024 sc1\n\t" \
  "s_waitcnt lgkmcnt(1)\n\t" \
  "v_mfma_f32_16x16x32_bf16 v[200:203], v[200:203], v[232:235], 0\n\t" \
  "v_mfma_f32_16x16x32_bf16 v[204:207], v[204:207], v[232:235], 0\n\t" \
  "s_nop 7\n\t" \
  "v_cndmask_b32 v216, v200, v204, %[nh]\n\t" \
  "v_cndmask_b32 v217, v201, v205, %[nh]\n\t" \
  "v_cndmask_b32 v218, v202, v206, %[nh]\n\t" \
  "v_cndmask_b32 v219, v203, v207, %[nh]\n\t" \
  "ds_write_b128 v248, v[216:219] offset:16384\n\t" \
  "buffer_load_dword v252, v249, %[rs], %[sv] offen offset:0 sc1\n\t" \
  "buffer_load_dword v253, v249, %[rs], %[sv] offen offset:256 sc1\n\t" \
  "ds_read_b32 v212, v248 offset:28672\n\t" \
  "ds_read_b64_tr_b16 v[236:237], v250 offset:512\n\t" \
  "ds_read_b64_tr_b16 v[238:239], v250 offset:576\n\t" \
  "ds_read_b64_tr_b16 v[240:241], v250 offset:1024\n\t" \
  "ds_read_b64_tr_b16 v[242:243], v250 offset:1088\n\t" \
  "ds_read_b64_tr_b16 v[244:245], v250 offset:1536\n\t" \
  "ds_read_b64_tr_b16 v[246:247], v250 offset:1600\n\t" \
  "s_setprio 3\n\t" \
  "s_waitcnt lgkmcnt(0)\n\t" \
  "v_cmp_ne_u32 vcc, %[step], v212\n\t" \
  "s_barrier\n\t" \
  "s_and_b32 s93, vcc_lo, 0xf\n\t" \
  "s_cbranch_scc1 .Lre%=\n\t" \
  ".Lrb%=:\n\t" \
  "ds_read_b64_tr_b16 v[200:201], v250 offset:2048\n\t" \
  "ds_read_b64_tr_b16 v[202:203], v250 offset:2112\n\t" \
  "ds_read_b64_tr_b16 v[204:205], v250 offset:2560\n\t" \
  "ds_read_b64_tr_b16 v[206:207], v250 offset:2624\n\t" \
  "ds_read_b128 v[220:223], v248 offset:17408\n\t" \
  "ds_read_b128 v[224:227], v248 offset:18432\n\t" \
  "ds_read_b128 v[228:231], v248 offset:19456\n\t" \
  "ds_read_b64_tr_b16 v[208:209], v250 offset:3072\n\t" \
  "ds_read_b64_tr_b16 v[210:211], v250 offset:3136\n\t" \
  "ds_read_b64_tr_b16 v[212:213], v250 offset:3584\n\t" \
  "ds_read_b64_tr_b16 v[214:215], v250 offset:3648\n\t" \
  "s_waitcnt lgkmcnt(9)\n\t" \
  "v_mfma_f32_16x16x32_bf16 v[200:203], v[200:203], v[232:235], 0\n\t" \
  "s_waitcnt lgkmcnt(7)\n\t" \
  "v_mfma_f32_16x16x32_bf16 v[200:203], v[204:207], v[236:239], v[200:203]\n\t" \
  "s_waitcnt lgkmcnt(6)\n\t" \
  "v_pk_add_f32 v[216:217], v[216:217], v[220:221]\n\t" \
  "v_pk_add_f32 v[218:219], v[218:219], v[222:223]\n\t" \
  "s_waitcnt lgkmcnt(4)\n\t" \
  "v_pk_add_f32 v[224:225], v[224:225], v[228:229]\n\t" \
  "v_pk_add_f32 v[226:227], v[226:227], v[230:231]\n\t" \
  "v_pk_add_f32 v[216:217], v[216:217], v[224:225]\n\t" \
  "v_pk_add_f32 v[218:219], v[218:219], v[226:227]\n\t" \
  "s_waitcnt lgkmcnt(2)\n\t" \
  "v_mfma_f32_16x16x32_bf16 v[200:203], v[208:211], v[240:243], v[200:203]\n\t" \
  "v_max3_f32 v224, v216, v217, v218\n\t" \
  "v_max_f32 v224, v224, v219\n\t" \
  "s_waitcnt lgkmcnt(0)\n\t" \
  "v_mfma_f32_16x16x32_bf16 v[200:203], v[212:215], v[244:247], v[200:203]\n\t" \
  "v_max_f32_dpp v225, v224, v224 row_ror:8 row_mask:0xf bank_mask:0xf\n\t" \
  "v_max_f32_dpp v226, v224, v224 row_ror:8 row_mask:0xf bank_mask:0xf\n\t" \
  "s_nop 1\n\t" \
  "v_permlane32_swap_b32 v225, v226\n\t" \
  "v_max_f32 v231, v225, v226\n\t" \
  "v_mov_b32 v220, 0x3f803f80\n\t" \
  "v_mov_b32 v221, 0x3f803f80\n\t" \
  "v_mov_b64 v[222:223], v[220:221]\n\t" \
  "v_mov_b32 v233, 0\n\t" \
  "v_mov_b32 v234, 0\n\t" \
  "v_max3_f32 v204, v200, v201, v231\n\t" \
  "v_max3_f32 v205, v204, v202, v203\n\t" \
  "v_max3_f32 v206, v204, v202, v203\n\t" \
  "v_cndmask_b32 v200, v200, v202, %[up]\n\t" \
  "v_cndmask_b32 v201, v201, v203, %[up]\n\t" \
  "v_permlane16_swap_b32 v205, v206\n\t" \
  "v_max_f32 v204, v205, v206\n\t" \
  "v_mul_f32 v204, %[negc], v204\n\t" \
  "v_fma_f32 v200, v200, %[c], v204\n\t" \
  "v_fma_f32 v201, v201, %[c], v204\n\t" \
  "v_exp_f32 v200, v200\n\t" \
  "v_fma_f32 v216, v216, %[c], v204\n\t" \
  "v_exp_f32 v201, v201\n\t" \
  "v_fma_f32 v217, v217, %[c], v204\n\t" \
  "v_exp_f32 v216, v216\n\t" \
  "v_fma_f32 v218, v218, %[c], v204\n\t" \
  "v_cvt_pk_bf16_f32 v232, v200, v201\n\t" \
  "v_cvt_pk_bf16_f32 v235, v200, v201\n\t" \
  "v_exp_f32 v217, v217\n\t" \
  "v_fma_f32 v219, v219, %[c], v204\n\t" \
  "v_mfma_f32_16x16x16_bf16 v[212:215], v[220:221], v[232:233], 0\n\t" \
  "v_exp_f32 v218, v218\n\t" \
  "s_waitcnt vmcnt(0)\n\t" \
  "v_mfma_f32_16x16x16_bf16 v[224:227], v[252:253], v[232:233], 0\n\t" \
  "v_cvt_pk_bf16_f32 v210, v216, v217\n\t" \
  "v_cvt_pk_bf16_f32 v208, v216, v217\n\t" \
  "v_exp_f32 v219, v219\n\t" \
  "v_mfma_f32_16x16x16_bf16 v[228:231], v[252:253], v[234:235], 0\n\t" \
  "v_cvt_pk_bf16_f32 v211, v218, v219\n\t" \
  "v_cvt_pk_bf16_f32 v209, v218, v219\n\t" \
  "v_mov_b32_dpp v208, v210 row_ror:8 row_mask:0xf bank_mask:0xc\n\t" \
  "v_mov_b32_dpp v210, v210 row_ror:8 row_mask:0xf bank_mask:0x3\n\t" \
  "v_mov_b32_dpp v209, v211 row_ror:8 row_mask:0xf bank_mask:0xc\n\t" \
  "v_mov_b32_dpp v211, v211 row_ror:8 row_mask:0xf bank_mask:0x3\n\t" \
  "v_max_u32 v204, v252, v253\n\t" \
  "v_cmp_le_u32 vcc, %[sent], v204\n\t" \
  "v_mfma_f32_16x16x32_bf16 v[212:215], v[220:223], v[208:211], v[212:215]\n\t" \
  ".Lpv%=:\n\t" \
  "v_mfma_f32_16x16x32_bf16 v[224:227], v[192:195], v[208:211], v[224:227]\n\t" \
  "v_mfma_f32_16x16x32_bf16 v[228:231], v[196:199], v[208:211], v[228:231]\n\t" \
  "s_cbranch_vccnz .Lvs%=\n\t" \
  ".Lvok%=:\n\t" \
  "s_nop 4\n\t" \
  "v_rcp_f32 v204, v212\n\t" \
  "v_cndmask_b32 v224, v224, v226, %[nh]\n\t" \
  "v_cndmask_b32 v225, v225, v227, %[nh]\n\t" \
  "v_cndmask_b32 v228, v228, v230, %[nh]\n\t" \
  "v_cndmask_b32 v229, v229, v231, %[nh]\n\t" \
  "v_pk_mul_f32 v[224:225], v[224:225], v[204:205] op_sel_hi:[1,0]\n\t" \
  "v_pk_mul_f32 v[228:229], v[228:229], v[204:205] op_sel_hi:[1,0]\n\t" \
  "v_cvt_pk_bf16_f32 v224, v224, v225\n\t" \
  "v_cvt_pk_bf16_f32 v225, v228, v229\n\t" \
  "buffer_store_dwordx2 v[224:225], v251, %[rs], %[so] offen sc1\n\t" \
  "s_memtime s[90:91]\n\t" \
  "s_setprio 0\n\t" \
  "s_waitcnt lgkmcnt(0)\n\t" \
  "s_mov_b32 %[tout], s90\n\t" \
  "s_mov_b32 %[spins], s92\n\t" \
  "s_branch .Lend%=\n\t" \
  ".Lre%=:\n\t" \
  "ds_read_b64_tr_b16 v[236:237], v250 offset:512\n\t" \
  "ds_read_b64_tr_b16 v[238:239], v250 offset:576\n\t" \
  "ds_read_b64_tr_b16 v[240:241], v250 offset:1024\n\t" \
  "ds_read_b64_tr_b16 v[242:243], v250 offset:1088\n\t" \
  "ds_read_b64_tr_b16 v[244:245], v250 offset:1536\n\t" \
  "ds_read_b64_tr_b16 v[246:247], v250 offset:1600\n\t" \
  "s_waitcnt lgkmcnt(0)\n\t" \
  "s_branch .Lrb%=\n\t" \
  ".Lvs%=:\n\t" \
  "v_cmp_eq_u32 vcc, %[sent], v252\n\t" \
  "v_cmp_eq_u32 s[94:95], %[sent], v253\n\t" \
  "s_or_b64 s[94:95], s[94:95], vcc\n\t" \
  "s_cbranch_scc0 .Lvok%=\n\t" \
  "s_add_u32 s92, s92, 1\n\t" \
  "s_sleep 2\n\t" \
  "s_and_b32 s93, s92, 63\n\t" \
  "s_cbranch_scc1 .Lvr%=\n\t" \
  "buffer_load_dword v204, off, %[rs], %[sab] sc1\n\t" \
  "s_waitcnt vmcnt(0)\n\t" \
  "v_readfirstlane_b32 s93, v204\n\t" \
  "s_nop 3\n\t" \
  "s_cmp_eq_u32 s93, 1\n\t" \
  "s_cbranch_scc1 .Lvok%=\n\t" \
  "s_cmp_lt_u32 s92, 0x80000\n\t" \
  "s_cbranch_scc1 .Lvr%=\n\t" \
  "v_mov_b32 v204, 1\n\t" \
  "buffer_store_dword v204, off, %[rs], %[sab] sc1\n\t" \
  "s_branch .Lvok%=\n\t" \
  ".Lvr%=:\n\t" \
  "buffer_load_dword v252, v249, %[rs], %[sv] offen offset:0 sc1\n\t" \
  "buffer_load_dword v253, v249, %[rs], %[sv] offen offset:256 sc1\n\t" \
  "s_waitcnt vmcnt(0)\n\t" \
  "v_mfma_f32_16x16x16_bf16 v[224:227], v[252:253], v[232:233], 0\n\t" \
  "v_mfma_f32_16x16x16_bf16 v[228:231], v[252:253], v[234:235], 0\n\t" \
  "v_max_u32 v204, v252, v253\n\t" \
  "v_cmp_le_u32 vcc, %[sent], v204\n\t" \
  "s_nop 4\n\t" \
  "s_branch .Lpv%=\n\t" \
  "s_branch .Lend%=\n\t" \
  ".Lslow%=:\n\t" \
  "s_mov_b64 s[94:95], 0\n\t" \
  "v_cmp_eq_u32 vcc, %[sent], v200\n\t" \
  "s_or_b64 s[94:95], s[94:95], vcc\n\t" \
  "v_cmp_eq_u32 vcc, %[sent], v201\n\t" \
  "s_or_b64 s[94:95], s[94:95], vcc\n\t" \
  "v_cmp_eq_u32 vcc, %[sent], v202\n\t" \
  "s_or_b64 s[94:95], s[94:95], vcc\n\t" \
  "v_cmp_eq_u32 vcc, %[sent], v203\n\t" \
  "s_or_b64 s[94:95], s[94:95], vcc\n\t" \
  "v_cmp_eq_u32 vcc, %[sent], v204\n\t" \
  "s_or_b64 s[94:95], s[94:95], vcc\n\t" \
  "v_cmp_eq_u32 vcc, %[sent], v205\n\t" \
  "s_or_b64 s[94:95], s[94:95], vcc\n\t" \
  "v_cmp_eq_u32 vcc, %[sent], v206\n\t" \
  "s_or_b64 s[94:95], s[94:95], vcc\n\t" \
  "v_cmp_eq_u32 vcc, %[sent], v207\n\t" \
  "s_or_b64 s[94:95], s[94:95], vcc\n\t" \
  "s_cmp_eq_u64 s[94:95], 0\n\t" \
  "s_cbranch_scc1 .Lok%=\n\t" \
  "v_mov_b64 v[192:193], 0\n\t" \
  "v_mov_b64 v[194:195], 0\n\t" \
  "v_mov_b64 v[196:197], 0\n\t" \
  "v_mov_b64 v[198:199], 0\n\t" \
  "s_add_u32 s92, s92, 1\n\t" \
  "s_cmp_eq_u32 s92, 1\n\t" \
  "s_cbranch_scc1 .Lpoll%=\n\t" \
  "s_sleep 2\n\t" \
  "s_and_b32 s93, s92, 63\n\t" \
  "s_cmp_lg_u32 s93, 0\n\t" \
  "s_cbranch_scc1 .Lpoll%=\n\t" \
  "v_mov_b32 v245, 0\n\t" \
  "buffer_load_dword v244, v245, %[rs], %[sab] offen sc1\n\t" \
  "s_waitcnt vmcnt(0)\n\t" \
  "v_readfirstlane_b32 s93, v244\n\t" \
  "s_nop 3\n\t" \
  "s_cmp_eq_u32 s93, 1\n\t" \
  "s_cbranch_scc1 .Lok%=\n\t" \
  "s_cmp_lt_u32 s92, 0x80000\n\t" \
  "s_cbranch_scc1 .Lpoll%=\n\t" \
  "v_mov_b32 v244, 1\n\t" \
  "buffer_store_dword v244, v245, %[rs], %[sab] offen sc1\n\t" \
  "s_branch .Lok%=\n\t" \
  ".Lend%=:\n\t"
#define QK_ASM_1 \
  "s_waitcnt lgkmcnt(0)\n\t" \
  "v_subrev_u32 v244, 0x8000, v250\n\t" \
  "ds_read_b128 v[216:219], v248 offset:36864\n\t" \
  "ds_read_b128 v[220:223], v248 offset:37888\n\t" \
  "ds_read_b128 v[224:227], v248 offset:38912\n\t" \
  "ds_read_b128 v[228:231], v248 offset:39936\n\t" \
  "ds_read_b128 v[208:211], v244 offset:0\n\t" \
  "ds_read_b128 v[212:215], v244 offset:1024\n\t" \
  "ds_read_b128 v[236:239], v244 offset:2048\n\t" \
  "ds_read_b128 v[240:243], v244 offset:3072\n\t" \
  "v_mov_b64 v[192:193], 0\n\t" \
  "v_mov_b64 v[194:195], 0\n\t" \
  "v_mov_b64 v[196:197], 0\n\t" \
  "v_mov_b64 v[198:199], 0\n\t" \
  "s_movk_i32 s92, 0x2000\n\t" \
  ".Lwt%=:\n\t" \
  "ds_read_b64 v[200:201], v248 offset:30720\n\t" \
  "s_memrealtime s[90:91]\n\t" \
  "s_sub_u32 s92, s92, 1\n\t" \
  "s_waitcnt lgkmcnt(0)\n\t" \
  "v_readfirstlane_b32 s93, v200\n\t" \
  "v_readfirstlane_b32 %[tgt], v201\n\t" \
  "s_cbranch_scc1 .Lgo%=\n\t" \
  "s_cmp_eq_u32 s93, %[step]\n\t" \
  "s_cbranch_scc1 .Lwx%=\n\t" \
  "s_sleep 1\n\t" \
  "s_branch .Lwt%=\n\t" \
  ".Lwx%=:\n\t" \
  "s_lshl_b32 s93, s90, 2\n\t" \
  "s_sub_i32 s93, %[tgt], s93\n\t" \
  "s_cmp_le_i32 s93, 0\n\t" \
  "s_cbranch_scc1 .Lgo%=\n\t" \
  "s_min_i32 s93, s93, 8000\n\t" \
  "s_mov_b32 s94, s93\n\t" \
  ".Lsl%=:\n\t" \
  "s_sub_i32 s94, s94, 18\n\t" \
  "s_cmp_lt_i32 s94, 30\n\t" \
  "s_cbranch_scc1 .Lsx%=\n\t" \
  "s_sleep 1\n\t" \
  "s_branch .Lsl%=\n\t" \
  ".Lsx%=:\n\t" \
  "s_sub_i32 s93, s93, 12\n\t" \
  "s_max_i32 s93, s93, 4\n\t" \
  "s_lshr_b32 s93, s93, 2\n\t" \
  "s_add_u32 s93, s93, s90\n\t" \
  "s_memrealtime s[90:91]\n\t" \
  ".Lpace%=:\n\t" \
  "s_memrealtime s[94:95]\n\t" \
  "s_waitcnt lgkmcnt(1)\n\t" \
  "s_sub_i32 s92, s90, s93\n\t" \
  "s_cmp_ge_i32 s92, 0\n\t" \
  "s_cbranch_scc1 .Le%=\n\t" \
  "s_memrealtime s[90:91]\n\t" \
  "s_waitcnt lgkmcnt(1)\n\t" \
  "s_sub_i32 s92, s94, s93\n\t" \
  "s_cmp_lt_i32 s92, 0\n\t" \
  "s_cbranch_scc1 .Lpace%=\n\t" \
  "s_nop 4\n\t" \
  ".Le%=:\n\t" \
  "s_add_u32 s92, s92, s93\n\t" \
  "s_lshl_b32 s92, s92, 2\n\t" \
  "s_sub_i32 s92, %[tgt], s92\n\t" \
  "s_max_i32 s92, s92, 0\n\t" \
  "s_bitcmp0_b32 s92, 3\n\t" \
  "s_cbranch_scc1 .Lf3%=\n\t" \
  "s_nop 15\n\t" \
  ".Lf3%=:\n\t" \
  "s_bitcmp0_b32 s92, 2\n\t" \
  "s_cbranch_scc1 .Lf2%=\n\t" \
  "s_nop 10\n\t" \
  ".Lf2%=:\n\t" \
  ".Lgo%=:\n\t" \
  "s_mov_b32 s92, 0\n\t" \
  ".Lpoll%=:\n\t" \
  "buffer_load_dwordx4 v[200:203], v248, %[rs], %[som] offen sc1\n\t" \
  "buffer_load_dwordx4 v[204:207], v248, %[rs], %[som2] offen sc1\n\t" \
  "s_waitcnt vmcnt(1) lgkmcnt(0)\n\t" \
  "ds_write_b128 v249, v[200:203]\n\t" \
  "v_smfmac_f32_16x16x64_bf16 v[192:195], v[200:203], v[208:215], v251\n\t" \
  "v_max3_u32 v244, v200, v201, v202\n\t" \
  "v_smfmac_f32_16x16x64_bf16 v[196:199], v[200:203], v[236:243], v251\n\t" \
  "s_waitcnt vmcnt(0)\n\t" \
  "ds_write_b128 v249, v[204:207] offset:8192\n\t" \
  "v_smfmac_f32_16x16x64_bf16 v[192:195], v[204:207], v[216:223], v251\n\t" \
  "v_max3_u32 v244, v244, v203, v204\n\t" \
  "v_max3_u32 v244, v244, v205, v206\n\t" \
  "v_smfmac_f32_16x16x64_bf16 v[196:199], v[204:207], v[224:231], v251\n\t" \
  "v_max_u32 v244, v244, v207\n\t" \
  "v_cmp_le_u32 vcc, %[sent], v244\n\t" \
  "s_cbranch_vccnz .Lslow%=\n\t" \
  ".Lok%=:\n\t" \
  "ds_read_b128 v[216:219], v250 offset:32768\n\t" \
  "ds_read_b128 v[220:223], v250 offset:33792\n\t" \
  "ds_read_b128 v[224:227], v250 offset:34816\n\t" \
  "ds_read_b128 v[228:231], v250 offset:35840\n\t" \
  "s_waitcnt lgkmcnt(4)\n\t" \
  "s_barrier\n\t" \
  "ds_read_b128 v[208:211], v248 offset:0\n\t" \
  "ds_read_b128 v[212:215], v248 offset:3072\n\t" \
  "ds_read_b128 v[236:239], v248 offset:4096\n\t" \
  "ds_read_b128 v[240:243], v248 offset:5120\n\t" \
  "s_waitcnt lgkmcnt(3)\n\t" \
  "ds_read_b128 v[244:247], v248 offset:12288\n\t" \
  "v_smfmac_f32_16x16x64_bf16 v[192:195], v[208:211], v[216:223], v251\n\t" \
  "ds_read_b128 v[216:219], v250 offset:0\n\t" \
  "v_smfmac_f32_16x16x64_bf16 v[196:199], v[208:211], v[224:231], v251\n\t" \
  "s_waitcnt lgkmcnt(4)\n\t" \
  "ds_read_b128 v[220:223], v250 offset:1024\n\t" \
  "v_smfmac_f32_16x16x64_bf16 v[192:195], v[212:215], v[0:7], v251\n\t" \
  "ds_read_b128 v[224:227], v250 offset:2048\n\t" \
  "v_smfmac_f32_16x16x64_bf16 v[196:199], v[212:215], v[8:15], v251\n\t" \
  "s_waitcnt lgkmcnt(5)\n\t" \
  "ds_read_b128 v[228:231], v250 offset:3072\n\t" \
  "v_smfmac_f32_16x16x64_bf16 v[192:195], v[236:239], v[16:23], v251\n\t" \
  "ds_read_b128 v[200:203], v248 offset:6144\n\t" \
  "v_smfmac_f32_16x16x64_bf16 v[196:199], v[236:239], v[24:31], v251\n\t" \
  "s_waitcnt lgkmcnt(6)\n\t" \
  "ds_read_b128 v[204:207], v248 offset:7168\n\t" \
  "v_smfmac_f32_16x16x64_bf16 v[192:195], v[240:243], v[32:39], v251\n\t" \
  "ds_read_b128 v[208:211], v248 offset:8192\n\t" \
  "v_smfmac_f32_16x16x64_bf16 v[196:199], v[240:243], v[40:47], v251\n\t" \
  "s_waitcnt lgkmcnt(5)\n\t" \
  "ds_read_b128 v[212:215], v248 offset:10240\n\t" \
  "v_smfmac_f32_16x16x64_bf16 v[192:195], v[244:247], v[216:223], v251\n\t" \
  "s_waitcnt lgkmcnt(4)\n\t" \
  "v_smfmac_f32_16x16x64_bf16 v[196:199], v[244:247], v[224:231], v251\n\t" \
  "s_waitcnt lgkmcnt(3)\n\t" \
  "ds_read_b128 v[236:239], v248 offset:11264\n\t" \
  "v_smfmac_f32_16x16x64_bf16 v[192:195], v[200:203], v[48:55], v251\n\t" \
  "ds_read_b128 v[240:243], v248 offset:13312\n\t" \
  "v_smfmac_f32_16x16x64_bf16 v[196:199], v[200:203], v[56:63], v251\n\t" \
  "s_waitcnt lgkmcnt(4)\n\t" \
  "v_smfmac_f32_16x16x64_bf16 v[192:195], v[204:207], v[64:71], v251\n\t" \
  "ds_read_b128 v[244:247], v248 offset:14336\n\t" \
  "v_smfmac_f32_16x16x64_bf16 v[196:199], v[204:207], v[72:79], v251\n\t" \
  "s_waitcnt lgkmcnt(4)\n\t" \
  "v_smfmac_f32_16x16x64_bf16 v[192:195], v[208:211], v[80:87], v251\n\t" \
  "ds_read_b128 v[200:203], v248 offset:15360\n\t" \
  "v_smfmac_f32_16x16x64_bf16 v[196:199], v[208:211], v[88:95], v251\n\t" \
  "s_waitcnt lgkmcnt(4)\n\t" \
  "v_smfmac_f32_16x16x64_bf16 v[192:195], v[212:215], v[96:103], v251\n\t" \
  "ds_read_b128 v[204:207], v248 offset:2048\n\t" \
  "v_smfmac_f32_16x16x64_bf16 v[196:199], v[212:215], v[104:111], v251\n\t" \
  "s_waitcnt lgkmcnt(3)\n\t" \
  "v_smfmac_f32_16x16x64_bf16 v[192:195], v[236:239], v[112:119], v251\n\t" \
  "v_smfmac_f32_16x16x64_bf16 v[196:199], v[236:239], v[120:127], v251\n\t" \
  "v_smfmac_f32_16x16x64_bf16 v[192:195], v[240:243], v[128:135], v251\n\t" \
  "v_smfmac_f32_16x16x64_bf16 v[196:199], v[240:243], v[136:143], v251\n\t" \
  "s_waitcnt lgkmcnt(1)\n\t" \
  "v_smfmac_f32_16x16x64_bf16 v[192:195], v[244:247], v[144:151], v251\n\t" \
  "v_smfmac_f32_16x16x64_bf16 v[196:199], v[244:247], v[152:159], v251\n\t" \
  "v_smfmac_f32_16x16x64_bf16 v[192:195], v[200:203], v[160:167], v251\n\t" \
  "v_smfmac_f32_16x16x64_bf16 v[196:199], v[200:203], v[168:175], v251\n\t" \
  "s_waitcnt lgkmcnt(0)\n\t" \
  "v_smfmac_f32_16x16x64_bf16 v[192:195], v[204:207], v[176:183], v251\n\t" \
  "v_smfmac_f32_16x16x64_bf16 v[196:199], v[204:207], v[184:191], v251\n\t" \
  "ds_read_b128 v[200:203], v248 offset:17408\n\t" \
  "ds_read_b128 v[204:207], v248 offset:21504\n\t" \
  "v_mov_b32 v250, v232\n\t" \
  "v_mov_b32 v249, v233\n\t" \
  "s_nop 2\n\t" \
  "v_add_f32 v192, v192, v194\n\t" \
  "v_add_f32 v193, v193, v195\n\t" \
  "v_add_f32 v196, v196, v198\n\t" \
  "v_add_f32 v197, v197, v199\n\t" \
  "v_cvt_pk_bf16_f32 v192, v192, v193\n\t" \
  "v_cvt_pk_bf16_f32 v196, v196, v197\n\t" \
  "ds_write2_b32 v252, v192, v196 offset1:64\n\t" \
  "v_mov_b32 v251, v234\n\t" \
  "ds_read_b64_tr_b16 v[236:237], v250 offset:512\n\t" \
  "ds_read_b64_tr_b16 v[238:239], v250 offset:576\n\t" \
  "v_mov_b32 v212, %[step]\n\t" \
  "s_mov_b64 exec, 1\n\t" \
  "ds_write_b32 v248, v212 offset:28688\n\t" \
  "s_mov_b64 exec, -1\n\t" \
  "buffer_load_dwordx4 v[192:195], v248, %[rt], %[svt] offen offset:0 sc1\n\t" \
  "buffer_load_dwordx4 v[196:199], v248, %[rt], %[svt] offen offset:1024 sc1\n\t" \
  "s_waitcnt lgkmcnt(1)\n\t" \
  "v_mfma_f32_16x16x32_bf16 v[200:203], v[200:203], v[236:239], 0\n\t" \
  "v_mfma_f32_16x16x32_bf16 v[204:207], v[204:207], v[236:239], 0\n\t" \
  "s_nop 7\n\t" \
  "v_cndmask_b32 v216, v200, v204, %[nh]\n\t" \
  "v_cndmask_b32 v217, v201, v205, %[nh]\n\t" \
  "v_cndmask_b32 v218, v202, v206, %[nh]\n\t" \
  "v_cndmask_b32 v219, v203, v207, %[nh]\n\t" \
  "ds_write_b128 v248, v[216:219] offset:17408\n\t" \
  "buffer_load_dword v252, v249, %[rs], %[sv] offen offset:0 sc1\n\t" \
  "buffer_load_dword v253, v249, %[rs], %[sv] offen offset:256 sc1\n\t" \
  "ds_read_b32 v212, v248 offset:28672\n\t" \
  "ds_read_b64_tr_b16 v[240:241], v250 offset:1024\n\t" \
  "ds_read_b64_tr_b16 v[242:243], v250 offset:1088\n\t" \
  "ds_read_b64_tr_b16 v[244:245], v250 offset:1536\n\t" \
  "ds_read_b64_tr_b16 v[246:247], v250 offset:1600\n\t" \
  "ds_read_b64_tr_b16 v[232:233], v250 offset:0\n\t" \
  "ds_read_b64_tr_b16 v[234:235], v250 offset:64\n\t" \
  "s_setprio 3\n\t" \
  "s_waitcnt lgkmcnt(0)\n\t" \
  "v_cmp_ne_u32 vcc, %[step], v212\n\t" \
  "s_barrier\n\t" \
  "s_and_b32 s93, vcc_lo, 0xf\n\t" \
  "s_cbranch_scc1 .Lre%=\n\t" \
  ".Lrb%=:\n\t" \
  "ds_read_b64_tr_b16 v[204:205], v250 offset:2560\n\t" \
  "ds_read_b64_tr_b16 v[206:207], v250 offset:2624\n\t" \
  "ds_read_b64_tr_b16 v[208:209], v250 offset:3072\n\t" \
  "ds_read_b64_tr_b16 v[210:211], v250 offset:3136\n\t" \
  "ds_read_b128 v[220:223], v248 offset:18432\n\t" \
  "ds_read_b128 v[224:227], v248 offset:19456\n\t" \
  "ds_read_b128 v[228:231], v248 offset:16384\n\t" \
  "ds_read_b64_tr_b16 v[212:213], v250 offset:3584\n\t" \
  "ds_read_b64_tr_b16 v[214:215], v250 offset:3648\n\t" \
  "ds_read_b64_tr_b16 v[200:201], v250 offset:2048\n\t" \
  "ds_read_b64_tr_b16 v[202:203], v250 offset:2112\n\t" \
  "s_waitcnt lgkmcnt(9)\n\t" \
  "v_mfma_f32_16x16x32_bf16 v[204:207], v[204:207], v[236:239], 0\n\t" \
  "s_waitcnt lgkmcnt(7)\n\t" \
  "v_mfma_f32_16x16x32_bf16 v[204:207], v[208:211], v[240:243], v[204:207]\n\t" \
  "s_waitcnt lgkmcnt(6)\n\t" \
  "v_pk_add_f32 v[216:217], v[216:217], v[220:221]\n\t" \
  "v_pk_add_f32 v[218:219], v[218:219], v[222:223]\n\t" \
  "s_waitcnt lgkmcnt(4)\n\t" \
  "v_pk_add_f32 v[224:225], v[224:225], v[228:229]\n\t" \
  "v_pk_add_f32 v[226:227], v[226:227], v[230:231]\n\t" \
  "v_pk_add_f32 v[216:217], v[216:217], v[224:225]\n\t" \
  "v_pk_add_f32 v[218:219], v[218:219], v[226:227]\n\t" \
  "s_waitcnt lgkmcnt(2)\n\t" \
  "v_mfma_f32_16x16x32_bf16 v[204:207], v[212:215], v[244:247], v[204:207]\n\t" \
  "v_max3_f32 v224, v216, v217, v218\n\t" \
  "v_max_f32 v224, v224, v219\n\t" \
  "s_waitcnt lgkmcnt(0)\n\t" \
  "v_mfma_f32_16x16x32_bf16 v[204:207], v[200:203], v[232:235], v[204:207]\n\t" \
  "v_max_f32_dpp v225, v224, v224 row_ror:8 row_mask:0xf bank_mask:0xf\n\t" \
  "v_max_f32_dpp v226, v224, v224 row_ror:8 row_mask:0xf bank_mask:0xf\n\t" \
  "s_nop 1\n\t" \
  "v_permlane32_swap_b32 v225, v226\n\t" \
  "v_max_f32 v231, v225, v226\n\t" \
  "v_mov_b32 v220, 0x3f803f80\n\t" \
  "v_mov_b32 v221, 0x3f803f80\n\t" \
  "v_mov_b64 v[222:223], v[220:221]\n\t" \
  "v_mov_b32 v237, 0\n\t" \
  "v_mov_b32 v238, 0\n\t" \
  "v_max3_f32 v208, v204, v205, v231\n\t" \
  "v_max3_f32 v209, v208, v206, v207\n\t" \
  "v_max3_f32 v210, v208, v206, v207\n\t" \
  "v_cndmask_b32 v204, v204, v206, %[up]\n\t" \
  "v_cndmask_b32 v205, v205, v207, %[up]\n\t" \
  "v_permlane16_swap_b32 v209, v210\n\t" \
  "v_max_f32 v208, v209, v210\n\t" \
  "v_mul_f32 v208, %[negc], v208\n\t" \
  "v_fma_f32 v204, v204, %[c], v208\n\t" \
  "v_fma_f32 v205, v205, %[c], v208\n\t" \
  "v_exp_f32 v204, v204\n\t" \
  "v_fma_f32 v216, v216, %[c], v208\n\t" \
  "v_exp_f32 v205, v205\n\t" \
  "v_fma_f32 v217, v217, %[c], v208\n\t" \
  "v_exp_f32 v216, v216\n\t" \
  "v_fma_f32 v218, v218, %[c], v208\n\t" \
  "v_cvt_pk_bf16_f32 v236, v204, v205\n\t" \
  "v_cvt_pk_bf16_f32 v239, v204, v205\n\t" \
  "v_exp_f32 v217, v217\n\t" \
  "v_fma_f32 v219, v219, %[c], v208\n\t" \
  "v_mfma_f32_16x16x16_bf16 v[200:203], v[220:221], v[236:237], 0\n\t" \
  "v_exp_f32 v218, v218\n\t" \
  "s_waitcnt vmcnt(0)\n\t" \
  "v_mfma_f32_16x16x16_bf16 v[224:227], v[252:253], v[236:237], 0\n\t" \
  "v_cvt_pk_bf16_f32 v214, v216, v217\n\t" \
  "v_cvt_pk_bf16_f32 v212, v216, v217\n\t" \
  "v_exp_f32 v219, v219\n\t" \
  "v_mfma_f32_16x16x16_bf16 v[228:231], v[252:253], v[238:239], 0\n\t" \
  "v_cvt_pk_bf16_f32 v215, v218, v219\n\t" \
  "v_cvt_pk_bf16_f32 v213, v218, v219\n\t" \
  "v_mov_b32_dpp v212, v214 row_ror:8 row_mask:0xf bank_mask:0xc\n\t" \
  "v_mov_b32_dpp v214, v214 row_ror:8 row_mask:0xf bank_mask:0x3\n\t" \
  "v_mov_b32_dpp v213, v215 row_ror:8 row_mask:0xf bank_mask:0xc\n\t" \
  "v_mov_b32_dpp v215, v215 row_ror:8 row_mask:0xf bank_mask:0x3\n\t" \
  "v_max_u32 v208, v252, v253\n\t" \
  "v_cmp_le_u32 vcc, %[sent], v208\n\t" \
  "v_mfma_f32_16x16x32_bf16 v[200:203], v[220:223], v[212:215], v[200:203]\n\t" \
  ".Lpv%=:\n\t" \
  "v_mfma_f32_16x16x32_bf16 v[224:227], v[192:195], v[212:215], v[224:227]\n\t" \
  "v_mfma_f32_16x16x32_bf16 v[228:231], v[196:199], v[212:215], v[228:231]\n\t" \
  "s_cbranch_vccnz .Lvs%=\n\t" \
  ".Lvok%=:\n\t" \
  "s_nop 4\n\t" \
  "v_rcp_f32 v208, v200\n\t" \
  "v_cndmask_b32 v224, v224, v226, %[nh]\n\t" \
  "v_cndmask_b32 v225, v225, v227, %[nh]\n\t" \
  "v_cndmask_b32 v228, v228, v230, %[nh]\n\t" \
  "v_cndmask_b32 v229, v229, v231, %[nh]\n\t" \
  "v_pk_mul_f32 v[224:225], v[224:225], v[208:209] op_sel_hi:[1,0]\n\t" \
  "v_pk_mul_f32 v[228:229], v[228:229], v[208:209] op_sel_hi:[1,0]\n\t" \
  "v_cvt_pk_bf16_f32 v224, v224, v225\n\t" \
  "v_cvt_pk_bf16_f32 v225, v228, v229\n\t" \
  "buffer_store_dwordx2 v[224:225], v251, %[rs], %[so] offen sc1\n\t" \
  "s_memtime s[90:91]\n\t" \
  "s_setprio 0\n\t" \
  "s_waitcnt lgkmcnt(0)\n\t" \
  "s_mov_b32 %[tout], s90\n\t" \
  "s_mov_b32 %[spins], s92\n\t" \
  "s_branch .Lend%=\n\t" \
  ".Lre%=:\n\t" \
  "ds_read_b64_tr_b16 v[240:241], v250 offset:1024\n\t" \
  "ds_read_b64_tr_b16 v[242:243], v250 offset:1088\n\t" \
  "ds_read_b64_tr_b16 v[244:245], v250 offset:1536\n\t" \
  "ds_read_b64_tr_b16 v[246:247], v250 offset:1600\n\t" \
  "ds_read_b64_tr_b16 v[232:233], v250 offset:0\n\t" \
  "ds_read_b64_tr_b16 v[234:235], v250 offset:64\n\t" \
  "s_waitcnt lgkmcnt(0)\n\t" \
  "s_branch .Lrb%=\n\t" \
  ".Lvs%=:\n\t" \
  "v_cmp_eq_u32 vcc, %[sent], v252\n\t" \
  "v_cmp_eq_u32 s[94:95], %[sent], v253\n\t" \
  "s_or_b64 s[94:95], s[94:95], vcc\n\t" \
  "s_cbranch_scc0 .Lvok%=\n\t" \
  "s_add_u32 s92, s92, 1\n\t" \
  "s_sleep 2\n\t" \
  "s_and_b32 s93, s92, 63\n\t" \
  "s_cbranch_scc1 .Lvr%=\n\t" \
  "buffer_load_dword v208, off, %[rs], %[sab] sc1\n\t" \
  "s_waitcnt vmcnt(0)\n\t" \
  "v_readfirstlane_b32 s93, v208\n\t" \
  "s_nop 3\n\t" \
  "s_cmp_eq_u32 s93, 1\n\t" \
  "s_cbranch_scc1 .Lvok%=\n\t" \
  "s_cmp_lt_u32 s92, 0x80000\n\t" \
  "s_cbranch_scc1 .Lvr%=\n\t" \
  "v_mov_b32 v208, 1\n\t" \
  "buffer_store_dword v208, off, %[rs], %[sab] sc1\n\t" \
  "s_branch .Lvok%=\n\t" \
  ".Lvr%=:\n\t" \
  "buffer_load_dword v252, v249, %[rs], %[sv] offen offset:0 sc1\n\t" \
  "buffer_load_dword v253, v249, %[rs], %[sv] offen offset:256 sc1\n\t" \
  "s_waitcnt vmcnt(0)\n\t" \
  "v_mfma_f32_16x16x16_bf16 v[224:227], v[252:253], v[236:237], 0\n\t" \
  "v_mfma_f32_16x16x16_bf16 v[228:231], v[252:253], v[238:239], 0\n\t" \
  "v_max_u32 v208, v252, v253\n\t" \
  "v_cmp_le_u32 vcc, %[sent], v208\n\t" \
  "s_nop 4\n\t" \
  "s_branch .Lpv%=\n\t" \
  "s_branch .Lend%=\n\t" \
  ".Lslow%=:\n\t" \
  "s_mov_b64 s[94:95], 0\n\t" \
  "v_cmp_eq_u32 vcc, %[sent], v200\n\t" \
  "s_or_b64 s[94:95], s[94:95], vcc\n\t" \
  "v_cmp_eq_u32 vcc, %[sent], v201\n\t" \
  "s_or_b64 s[94:95], s[94:95], vcc\n\t" \
  "v_cmp_eq_u32 vcc, %[sent], v202\n\t" \
  "s_or_b64 s[94:95], s[94:95], vcc\n\t" \
  "v_cmp_eq_u32 vcc, %[sent], v203\n\t" \
  "s_or_b64 s[94:95], s[94:95], vcc\n\t" \
  "v_cmp_eq_u32 vcc, %[sent], v204\n\t" \
  "s_or_b64 s[94:95], s[94:95], vcc\n\t" \
  "v_cmp_eq_u32 vcc, %[sent], v205\n\t" \
  "s_or_b64 s[94:95], s[94:95], vcc\n\t" \
  "v_cmp_eq_u32 vcc, %[sent], v206\n\t" \
  "s_or_b64 s[94:95], s[94:95], vcc\n\t" \
  "v_cmp_eq_u32 vcc, %[sent], v207\n\t" \
  "s_or_b64 s[94:95], s[94:95], vcc\n\t" \
  "s_cmp_eq_u64 s[94:95], 0\n\t" \
  "s_cbranch_scc1 .Lok%=\n\t" \
  "v_mov_b64 v[192:193], 0\n\t" \
  "v_mov_b64 v[194:195], 0\n\t" \
  "v_mov_b64 v[196:197], 0\n\t" \
  "v_mov_b64 v[198:199], 0\n\t" \
  "s_add_u32 s92, s92, 1\n\t" \
  "s_cmp_eq_u32 s92, 1\n\t" \
  "s_cbranch_scc1 .Lpoll%=\n\t" \
  "s_sleep 2\n\t" \
  "s_and_b32 s93, s92, 63\n\t" \
  "s_cmp_lg_u32 s93, 0\n\t" \
  "s_cbranch_scc1 .Lpoll%=\n\t" \
  "v_mov_b32 v245, 0\n\t" \
  "buffer_load_dword v244, v245, %[rs], %[sab] offen sc1\n\t" \
  "s_waitcnt vmcnt(0)\n\t" \
  "v_readfirstlane_b32 s93, v244\n\t" \
  "s_nop 3\n\t" \
  "s_cmp_eq_u32 s93, 1\n\t" \
  "s_cbranch_scc1 .Lok%=\n\t" \
  "s_cmp_lt_u32 s92, 0x80000\n\t" \
  "s_cbranch_scc1 .Lpoll%=\n\t" \
  "v_mov_b32 v244, 1\n\t" \
  "buffer_store_dword v244, v245, %[rs], %[sab] offen sc1\n\t" \
  "s_branch .Lok%=\n\t" \
  ".Lend%=:\n\t"
#define QK_ASM_2 \
  "s_waitcnt lgkmcnt(0)\n\t" \
  "v_subrev_u32 v244, 0x8000, v250\n\t" \
  "ds_read_b128 v[216:219], v248 offset:40960\n\t" \
  "ds_read_b128 v[220:223], v248 offset:41984\n\t" \
  "ds_read_b128 v[224:227], v248 offset:43008\n\t" \
  "ds_read_b128 v[228:231], v248 offset:44032\n\t" \
  "ds_read_b128 v[208:211], v244 offset:0\n\t" \
  "ds_read_b128 v[212:215], v244 offset:1024\n\t" \
  "ds_read_b128 v[236:239], v244 offset:2048\n\t" \
  "ds_read_b128 v[240:243], v244 offset:3072\n\t" \
  "v_mov_b64 v[192:193], 0\n\t" \
  "v_mov_b64 v[194:195], 0\n\t" \
  "v_mov_b64 v[196:197], 0\n\t" \
  "v_mov_b64 v[198:199], 0\n\t" \
  "s_movk_i32 s92, 0x2000\n\t" \
  ".Lwt%=:\n\t" \
  "ds_read_b64 v[200:201], v248 offset:30720\n\t" \
  "s_memrealtime s[90:91]\n\t" \
  "s_sub_u32 s92, s92, 1\n\t" \
  "s_waitcnt lgkmcnt(0)\n\t" \
  "v_readfirstlane_b32 s93, v200\n\t" \
  "v_readfirstlane_b32 %[tgt], v201\n\t" \
  "s_cbranch_scc1 .Lgo%=\n\t" \
  "s_cmp_eq_u32 s93, %[step]\n\t" \
  "s_cbranch_scc1 .Lwx%=\n\t" \
  "s_sleep 1\n\t" \
  "s_branch .Lwt%=\n\t" \
  ".Lwx%=:\n\t" \
  "s_lshl_b32 s93, s90, 2\n\t" \
  "s_sub_i32 s93, %[tgt], s93\n\t" \
  "s_cmp_le_i32 s93, 0\n\t" \
  "s_cbranch_scc1 .Lgo%=\n\t" \
  "s_min_i32 s93, s93, 8000\n\t" \
  "s_mov_b32 s94, s93\n\t" \
  ".Lsl%=:\n\t" \
  "s_sub_i32 s94, s94, 18\n\t" \
  "s_cmp_lt_i32 s94, 30\n\t" \
  "s_cbranch_scc1 .Lsx%=\n\t" \
  "s_sleep 1\n\t" \
  "s_branch .Lsl%=\n\t" \
  ".Lsx%=:\n\t" \
  "s_sub_i32 s93, s93, 12\n\t" \
  "s_max_i32 s93, s93, 4\n\t" \
  "s_lshr_b32 s93, s93, 2\n\t" \
  "s_add_u32 s93, s93, s90\n\t" \
  "s_memrealtime s[90:91]\n\t" \
  ".Lpace%=:\n\t" \
  "s_memrealtime s[94:95]\n\t" \
  "s_waitcnt lgkmcnt(1)\n\t" \
  "s_sub_i32 s92, s90, s93\n\t" \
  "s_cmp_ge_i32 s92, 0\n\t" \
  "s_cbranch_scc1 .Le%=\n\t" \
  "s_memrealtime s[90:91]\n\t" \
  "s_waitcnt lgkmcnt(1)\n\t" \
  "s_sub_i32 s92, s94, s93\n\t" \
  "s_cmp_lt_i32 s92, 0\n\t" \
  "s_cbranch_scc1 .Lpace%=\n\t" \
  "s_nop 4\n\t" \
  ".Le%=:\n\t" \
  "s_add_u32 s92, s92, s93\n\t" \
  "s_lshl_b32 s92, s92, 2\n\t" \
  "s_sub_i32 s92, %[tgt], s92\n\t" \
  "s_max_i32 s92, s92, 0\n\t" \
  "s_bitcmp0_b32 s92, 3\n\t" \
  "s_cbranch_scc1 .Lf3%=\n\t" \
  "s_nop 15\n\t" \
  ".Lf3%=:\n\t" \
  "s_bitcmp0_b32 s92, 2\n\t" \
  "s_cbranch_scc1 .Lf2%=\n\t" \
  "s_nop 10\n\t" \
  ".Lf2%=:\n\t" \
  ".Lgo%=:\n\t" \
  "s_mov_b32 s92, 0\n\t" \
  ".Lpoll%=:\n\t" \
  "buffer_load_dwordx4 v[200:203], v248, %[rs], %[som] offen sc1\n\t" \
  "buffer_load_dwordx4 v[204:207], v248, %[rs], %[som2] offen sc1\n\t" \
  "s_waitcnt vmcnt(1) lgkmcnt(0)\n\t" \
  "ds_write_b128 v249, v[200:203]\n\t" \
  "v_smfmac_f32_16x16x64_bf16 v[192:195], v[200:203], v[208:215], v251\n\t" \
  "v_max3_u32 v244, v200, v201, v202\n\t" \
  "v_smfmac_f32_16x16x64_bf16 v[196:199], v[200:203], v[236:243], v251\n\t" \
  "s_waitcnt vmcnt(0)\n\t" \
  "ds_write_b128 v249, v[204:207] offset:8192\n\t" \
  "v_smfmac_f32_16x16x64_bf16 v[192:195], v[204:207], v[216:223], v251\n\t" \
  "v_max3_u32 v244, v244, v203, v204\n\t" \
  "v_max3_u32 v244, v244, v205, v206\n\t" \
  "v_smfmac_f32_16x16x64_bf16 v[196:199], v[204:207], v[224:231], v251\n\t" \
  "v_max_u32 v244, v244, v207\n\t" \
  "v_cmp_le_u32 vcc, %[sent], v244\n\t" \
  "s_cbranch_vccnz .Lslow%=\n\t" \
  ".Lok%=:\n\t" \
  "ds_read_b128 v[216:219], v250 offset:32768\n\t" \
  "ds_read_b128 v[220:223], v250 offset:33792\n\t" \
  "ds_read_b128 v[224:227], v250 offset:34816\n\t" \
  "ds_read_b128 v[228:231], v250 offset:35840\n\t" \
  "s_waitcnt lgkmcnt(4)\n\t" \
  "s_barrier\n\t" \
  "ds_read_b128 v[208:211], v248 offset:3072\n\t" \
  "ds_read_b128 v[212:215], v248 offset:4096\n\t" \
  "ds_read_b128 v[236:239], v248 offset:5120\n\t" \
  "ds_read_b128 v[240:243], v248 offset:6144\n\t" \
  "s_waitcnt lgkmcnt(3)\n\t" \
  "ds_read_b128 v[244:247], v248 offset:13312\n\t" \
  "v_smfmac_f32_16x16x64_bf16 v[192:195], v[208:211], v[216:223], v251\n\t" \
  "ds_read_b128 v[216:219], v250 offset:0\n\t" \
  "v_smfmac_f32_16x16x64_bf16 v[196:199], v[208:211], v[224:231], v251\n\t" \
  "s_waitcnt lgkmcnt(4)\n\t" \
  "ds_read_b128 v[220:223], v250 offset:1024\n\t" \
  "v_smfmac_f32_16x16x64_bf16 v[192:195], v[212:215], v[0:7], v251\n\t" \
  "ds_read_b128 v[224:227], v250 offset:2048\n\t" \
  "v_smfmac_f32_16x16x64_bf16 v[196:199], v[212:215], v[8:15], v251\n\t" \
  "s_waitcnt lgkmcnt(5)\n\t" \
  "ds_read_b128 v[228:231], v250 offset:3072\n\t" \
  "v_smfmac_f32_16x16x64_bf16 v[192:195], v[236:239], v[16:23], v251\n\t" \
  "ds_read_b128 v[200:203], v248 offset:7168\n\t" \
  "v_smfmac_f32_16x16x64_bf16 v[196:199], v[236:239], v[24:31], v251\n\t" \
  "s_waitcnt lgkmcnt(6)\n\t" \
  "ds_read_b128 v[204:207], v248 offset:8192\n\t" \
  "v_smfmac_f32_16x16x64_bf16 v[192:195], v[240:243], v[32:39], v251\n\t" \
  "ds_read_b128 v[208:211], v248 offset:9216\n\t" \
  "v_smfmac_f32_16x16x64_bf16 v[196:199], v[240:243], v[40:47], v251\n\t" \
  "s_waitcnt lgkmcnt(5)\n\t" \
  "ds_read_b128 v[212:215], v248 offset:11264\n\t" \
  "v_smfmac_f32_16x16x64_bf16 v[192:195], v[244:247], v[216:223], v251\n\t" \
  "s_waitcnt lgkmcnt(4)\n\t" \
  "v_smfmac_f32_16x16x64_bf16 v[196:199], v[244:247], v[224:231], v251\n\t" \
  "s_waitcnt lgkmcnt(3)\n\t" \
  "ds_read_b128 v[236:239], v248 offset:12288\n\t" \
  "v_smfmac_f32_16x16x64_bf16 v[192:195], v[200:203], v[48:55], v251\n\t" \
  "ds_read_b128 v[240:243], v248 offset:14336\n\t" \
  "v_smfmac_f32_16x16x64_bf16 v[196:199], v[200:203], v[56:63], v251\n\t" \
  "s_waitcnt lgkmcnt(4)\n\t" \
  "v_smfmac_f32_16x16x64_bf16 v[192:195], v[204:207], v[64:71], v251\n\t" \
  "ds_read_b128 v[244:247], v248 offset:15360\n\t" \
  "v_smfmac_f32_16x16x64_bf16 v[196:199], v[204:207], v[72:79], v251\n\t" \
  "s_waitcnt lgkmcnt(4)\n\t" \
  "v_smfmac_f32_16x16x64_bf16 v[192:195], v[208:211], v[80:87], v251\n\t" \
  "ds_read_b128 v[200:203], v248 offset:0\n\t" \
  "v_smfmac_f32_16x16x64_bf16 v[196:199], v[208:211], v[88:95], v251\n\t" \
  "s_waitcnt lgkmcnt(4)\n\t" \
  "v_smfmac_f32_16x16x64_bf16 v[192:195], v[212:215], v[96:103], v251\n\t" \
  "ds_read_b128 v[204:207], v248 offset:1024\n\t" \
  "v_smfmac_f32_16x16x64_bf16 v[196:199], v[212:215], v[104:111], v251\n\t" \
  "s_waitcnt lgkmcnt(3)\n\t" \
  "v_smfmac_f32_16x16x64_bf16 v[192:195], v[236:239], v[112:119], v251\n\t" \
  "v_smfmac_f32_16x16x64_bf16 v[196:199], v[236:239], v[120:127], v251\n\t" \
  "v_smfmac_f32_16x16x64_bf16 v[192:195], v[240:243], v[128:135], v251\n\t" \
  "v_smfmac_f32_16x16x64_bf16 v[196:199], v[240:243], v[136:143], v251\n\t" \
  "s_waitcnt lgkmcnt(1)\n\t" \
  "v_smfmac_f32_16x16x64_bf16 v[192:195], v[244:247], v[144:151], v251\n\t" \
  "v_smfmac_f32_16x16x64_bf16 v[196:199], v[244:247], v[152:159], v251\n\t" \
  "v_smfmac_f32_16x16x64_bf16 v[192:195], v[200:203], v[160:167], v251\n\t" \
  "v_smfmac_f32_16x16x64_bf16 v[196:199], v[200:203], v[168:175], v251\n\t" \
  "s_waitcnt lgkmcnt(0)\n\t" \
  "v_smfmac_f32_16x16x64_bf16 v[192:195], v[204:207], v[176:183], v251\n\t" \
  "v_smfmac_f32_16x16x64_bf16 v[196:199], v[204:207], v[184:191], v251\n\t" \
  "ds_read_b128 v[200:203], v248 offset:18432\n\t" \
  "ds_read_b128 v[204:207], v248 offset:22528\n\t" \
  "v_mov_b32 v250, v232\n\t" \
  "v_mov_b32 v249, v233\n\t" \
  "s_nop 2\n\t" \
  "v_add_f32 v192, v192, v194\n\t" \
  "v_add_f32 v193, v193, v195\n\t" \
  "v_add_f32 v196, v196, v198\n\t" \
  "v_add_f32 v197, v197, v199\n\t" \
  "v_cvt_pk_bf16_f32 v192, v192, v193\n\t" \
  "v_cvt_pk_bf16_f32 v196, v196, v197\n\t" \
  "ds_write2_b32 v252, v192, v196 offset1:64\n\t" \
  "v_mov_b32 v251, v234\n\t" \
  "ds_read_b64_tr_b16 v[240:241], v250 offset:1024\n\t" \
  "ds_read_b64_tr_b16 v[242:243], v250 offset:1088\n\t" \
  "v_mov_b32 v212, %[step]\n\t" \
  "s_mov_b64 exec, 1\n\t" \
  "ds_write_b32 v248, v212 offset:28704\n\t" \
  "s_mov_b64 exec, -1\n\t" \
  "buffer_load_dwordx4 v[192:195], v248, %[rt], %[svt] offen offset:0 sc1\n\t" \
  "buffer_load_dwordx4 v[196:199], v248, %[rt], %[svt] offen offset:1024 sc1\n\t" \
  "s_waitcnt lgkmcnt(1)\n\t" \
  "v_mfma_f32_16x16x32_bf16 v[200:203], v[200:203], v[240:243], 0\n\t" \
  "v_mfma_f32_16x16x32_bf16 v[204:207], v[204:207], v[240:243], 0\n\t" \
  "s_nop 7\n\t" \
  "v_cndmask_b32 v216, v200, v204, %[nh]\n\t" \
  "v_cndmask_b32 v217, v201, v205, %[nh]\n\t" \
  "v_cndmask_b32 v218, v202, v206, %[nh]\n\t" \
  "v_cndmask_b32 v219, v203, v207, %[nh]\n\t" \
  "ds_write_b128 v248, v[216:219] offset:18432\n\t" \
  "buffer_load_dword v252, v249, %[rs], %[sv] offen offset:0 sc1\n\t" \
  "buffer_load_dword v253, v249, %[rs], %[sv] offen offset:256 sc1\n\t" \
  "ds_read_b32 v212, v248 offset:28672\n\t" \
  "ds_read_b64_tr_b16 v[244:245], v250 offset:1536\n\t" \
  "ds_read_b64_tr_b16 v[246:247], v250 offset:1600\n\t" \
  "ds_read_b64_tr_b16 v[232:233], v250 offset:0\n\t" \
  "ds_read_b64_tr_b16 v[234:235], v250 offset:64\n\t" \
  "ds_read_b64_tr_b16 v[236:237], v250 offset:512\n\t" \
  "ds_read_b64_tr_b16 v[238:239], v250 offset:576\n\t" \
  "s_setprio 3\n\t" \
  "s_waitcnt lgkmcnt(0)\n\t" \
  "v_cmp_ne_u32 vcc, %[step], v212\n\t" \
  "s_barrier\n\t" \
  "s_and_b32 s93, vcc_lo, 0xf\n\t" \
  "s_cbranch_scc1 .Lre%=\n\t" \
  ".Lrb%=:\n\t" \
  "ds_read_b64_tr_b16 v[208:209], v250 offset:3072\n\t" \
  "ds_read_b64_tr_b16 v[210:211], v250 offset:3136\n\t" \
  "ds_read_b64_tr_b16 v[212:213], v250 offset:3584\n\t" \
  "ds_read_b64_tr_b16 v[214:215], v250 offset:3648\n\t" \
  "ds_read_b128 v[220:223], v248 offset:19456\n\t" \
  "ds_read_b128 v[224:227], v248 offset:16384\n\t" \
  "ds_read_b128 v[228:231], v248 offset:17408\n\t" \
  "ds_read_b64_tr_b16 v[200:201], v250 offset:2048\n\t" \
  "ds_read_b64_tr_b16 v[202:203], v250 offset:2112\n\t" \
  "ds_read_b64_tr_b16 v[204:205], v250 offset:2560\n\t" \
  "ds_read_b64_tr_b16 v[206:207], v250 offset:2624\n\t" \
  "s_waitcnt lgkmcnt(9)\n\t" \
  "v_mfma_f32_16x16x32_bf16 v[208:211], v[208:211], v[240:243], 0\n\t" \
  "s_waitcnt lgkmcnt(7)\n\t" \
  "v_mfma_f32_16x16x32_bf16 v[208:211], v[212:215], v[244:247], v[208:211]\n\t" \
  "s_waitcnt lgkmcnt(6)\n\t" \
  "v_pk_add_f32 v[216:217], v[216:217], v[220:221]\n\t" \
  "v_pk_add_f32 v[218:219], v[218:219], v[222:223]\n\t" \
  "s_waitcnt lgkmcnt(4)\n\t" \
  "v_pk_add_f32 v[224:225], v[224:225], v[228:229]\n\t" \
  "v_pk_add_f32 v[226:227], v[226:227], v[230:231]\n\t" \
  "v_pk_add_f32 v[216:217], v[216:217], v[224:225]\n\t" \
  "v_pk_add_f32 v[218:219], v[218:219], v[226:227]\n\t" \
  "s_waitcnt lgkmcnt(2)\n\t" \
  "v_mfma_f32_16x16x32_bf16 v[208:211], v[200:203], v[232:235], v[208:211]\n\t" \
  "v_max3_f32 v224, v216, v217, v218\n\t" \
  "v_max_f32 v224, v224, v219\n\t" \
  "s_waitcnt lgkmcnt(0)\n\t" \
  "v_mfma_f32_16x16x32_bf16 v[208:211], v[204:207], v[236:239], v[208:211]\n\t" \
  "v_max_f32_dpp v225, v224, v224 row_ror:8 row_mask:0xf bank_mask:0xf\n\t" \
  "v_max_f32_dpp v226, v224, v224 row_ror:8 row_mask:0xf bank_mask:0xf\n\t" \
  "s_nop 1\n\t" \
  "v_permlane32_swap_b32 v225, v226\n\t" \
  "v_max_f32 v231, v225, v226\n\t" \
  "v_mov_b32 v220, 0x3f803f80\n\t" \
  "v_mov_b32 v221, 0x3f803f80\n\t" \
  "v_mov_b64 v[222:223], v[220:221]\n\t" \
  "v_mov_b32 v241, 0\n\t" \
  "v_mov_b32 v242, 0\n\t" \
  "v_max3_f32 v212, v208, v209, v231\n\t" \
  "v_max3_f32 v213, v212, v210, v211\n\t" \
  "v_max3_f32 v214, v212, v210, v211\n\t" \
  "v_cndmask_b32 v208, v208, v210, %[up]\n\t" \
  "v_cndmask_b32 v209, v209, v211, %[up]\n\t" \
  "v_permlane16_swap_b32 v213, v214\n\t" \
  "v_max_f32 v212, v213, v214\n\t" \
  "v_mul_f32 v212, %[negc], v212\n\t" \
  "v_fma_f32 v208, v208, %[c], v212\n\t" \
  "v_fma_f32 v209, v209, %[c], v212\n\t" \
  "v_exp_f32 v208, v208\n\t" \
  "v_fma_f32 v216, v216, %[c], v212\n\t" \
  "v_exp_f32 v209, v209\n\t" \
  "v_fma_f32 v217, v217, %[c], v212\n\t" \
  "v_exp_f32 v216, v216\n\t" \
  "v_fma_f32 v218, v218, %[c], v212\n\t" \
  "v_cvt_pk_bf16_f32 v240, v208, v209\n\t" \
  "v_cvt_pk_bf16_f32 v243, v208, v209\n\t" \
  "v_exp_f32 v217, v217\n\t" \
  "v_fma_f32 v219, v219, %[c], v212\n\t" \
  "v_mfma_f32_16x16x16_bf16 v[204:207], v[220:221], v[240:241], 0\n\t" \
  "v_exp_f32 v218, v218\n\t" \
  "s_waitcnt vmcnt(0)\n\t" \
  "v_mfma_f32_16x16x16_bf16 v[224:227], v[252:253], v[240:241], 0\n\t" \
  "v_cvt_pk_bf16_f32 v202, v216, v217\n\t" \
  "v_cvt_pk_bf16_f32 v200, v216, v217\n\t" \
  "v_exp_f32 v219, v219\n\t" \
  "v_mfma_f32_16x16x16_bf16 v[228:231], v[252:253], v[242:243], 0\n\t" \
  "v_cvt_pk_bf16_f32 v203, v218, v219\n\t" \
  "v_cvt_pk_bf16_f32 v201, v218, v219\n\t" \
  "v_mov_b32_dpp v200, v202 row_ror:8 row_mask:0xf bank_mask:0xc\n\t" \
  "v_mov_b32_dpp v202, v202 row_ror:8 row_mask:0xf bank_mask:0x3\n\t" \
  "v_mov_b32_dpp v201, v203 row_ror:8 row_mask:0xf bank_mask:0xc\n\t" \
  "v_mov_b32_dpp v203, v203 row_ror:8 row_mask:0xf bank_mask:0x3\n\t" \
  "v_max_u32 v212, v252, v253\n\t" \
  "v_cmp_le_u32 vcc, %[sent], v212\n\t" \
  "v_mfma_f32_16x16x32_bf16 v[204:207], v[220:223], v[200:203], v[204:207]\n\t" \
  ".Lpv%=:\n\t" \
  "v_mfma_f32_16x16x32_bf16 v[224:227], v[192:195], v[200:203], v[224:227]\n\t" \
  "v_mfma_f32_16x16x32_bf16 v[228:231], v[196:199], v[200:203], v[228:231]\n\t" \
  "s_cbranch_vccnz .Lvs%=\n\t" \
  ".Lvok%=:\n\t" \
  "s_nop 4\n\t" \
  "v_rcp_f32 v212, v204\n\t" \
  "v_cndmask_b32 v224, v224, v226, %[nh]\n\t" \
  "v_cndmask_b32 v225, v225, v227, %[nh]\n\t" \
  "v_cndmask_b32 v228, v228, v230, %[nh]\n\t" \
  "v_cndmask_b32 v229, v229, v231, %[nh]\n\t" \
  "v_pk_mul_f32 v[224:225], v[224:225], v[212:213] op_sel_hi:[1,0]\n\t" \
  "v_pk_mul_f32 v[228:229], v[228:229], v[212:213] op_sel_hi:[1,0]\n\t" \
  "v_cvt_pk_bf16_f32 v224, v224, v225\n\t" \
  "v_cvt_pk_bf16_f32 v225, v228, v229\n\t" \
  "buffer_store_dwordx2 v[224:225], v251, %[rs], %[so] offen sc1\n\t" \
  "s_memtime s[90:91]\n\t" \
  "s_setprio 0\n\t" \
  "s_waitcnt lgkmcnt(0)\n\t" \
  "s_mov_b32 %[tout], s90\n\t" \
  "s_mov_b32 %[spins], s92\n\t" \
  "s_branch .Lend%=\n\t" \
  ".Lre%=:\n\t" \
  "ds_read_b64_tr_b16 v[244:245], v250 offset:1536\n\t" \
  "ds_read_b64_tr_b16 v[246:247], v250 offset:1600\n\t" \
  "ds_read_b64_tr_b16 v[232:233], v250 offset:0\n\t" \
  "ds_read_b64_tr_b16 v[234:235], v250 offset:64\n\t" \
  "ds_read_b64_tr_b16 v[236:237], v250 offset:512\n\t" \
  "ds_read_b64_tr_b16 v[238:239], v250 offset:576\n\t" \
  "s_waitcnt lgkmcnt(0)\n\t" \
  "s_branch .Lrb%=\n\t" \
  ".Lvs%=:\n\t" \
  "v_cmp_eq_u32 vcc, %[sent], v252\n\t" \
  "v_cmp_eq_u32 s[94:95], %[sent], v253\n\t" \
  "s_or_b64 s[94:95], s[94:95], vcc\n\t" \
  "s_cbranch_scc0 .Lvok%=\n\t" \
  "s_add_u32 s92, s92, 1\n\t" \
  "s_sleep 2\n\t" \
  "s_and_b32 s93, s92, 63\n\t" \
  "s_cbranch_scc1 .Lvr%=\n\t" \
  "buffer_load_dword v212, off, %[rs], %[sab] sc1\n\t" \
  "s_waitcnt vmcnt(0)\n\t" \
  "v_readfirstlane_b32 s93, v212\n\t" \
  "s_nop 3\n\t" \
  "s_cmp_eq_u32 s93, 1\n\t" \
  "s_cbranch_scc1 .Lvok%=\n\t" \
  "s_cmp_lt_u32 s92, 0x80000\n\t" \
  "s_cbranch_scc1 .Lvr%=\n\t" \
  "v_mov_b32 v212, 1\n\t" \
  "buffer_store_dword v212, off, %[rs], %[sab] sc1\n\t" \
  "s_branch .Lvok%=\n\t" \
  ".Lvr%=:\n\t" \
  "buffer_load_dword v252, v249, %[rs], %[sv] offen offset:0 sc1\n\t" \
  "buffer_load_dword v253, v249, %[rs], %[sv] offen offset:256 sc1\n\t" \
  "s_waitcnt vmcnt(0)\n\t" \
  "v_mfma_f32_16x16x16_bf16 v[224:227], v[252:253], v[240:241], 0\n\t" \
  "v_mfma_f32_16x16x16_bf16 v[228:231], v[252:253], v[242:243], 0\n\t" \
  "v_max_u32 v212, v252, v253\n\t" \
  "v_cmp_le_u32 vcc, %[sent], v212\n\t" \
  "s_nop 4\n\t" \
  "s_branch .Lpv%=\n\t" \
  "s_branch .Lend%=\n\t" \
  ".Lslow%=:\n\t" \
  "s_mov_b64 s[94:95], 0\n\t" \
  "v_cmp_eq_u32 vcc, %[sent], v200\n\t" \
  "s_or_b64 s[94:95], s[94:95], vcc\n\t" \
  "v_cmp_eq_u32 vcc, %[sent], v201\n\t" \
  "s_or_b64 s[94:95], s[94:95], vcc\n\t" \
  "v_cmp_eq_u32 vcc, %[sent], v202\n\t" \
  "s_or_b64 s[94:95], s[94:95], vcc\n\t" \
  "v_cmp_eq_u32 vcc, %[sent], v203\n\t" \
  "s_or_b64 s[94:95], s[94:95], vcc\n\t" \
  "v_cmp_eq_u32 vcc, %[sent], v204\n\t" \
  "s_or_b64 s[94:95], s[94:95], vcc\n\t" \
  "v_cmp_eq_u32 vcc, %[sent], v205\n\t" \
  "s_or_b64 s[94:95], s[94:95], vcc\n\t" \
  "v_cmp_eq_u32 vcc, %[sent], v206\n\t" \
  "s_or_b64 s[94:95], s[94:95], vcc\n\t" \
  "v_cmp_eq_u32 vcc, %[sent], v207\n\t" \
  "s_or_b64 s[94:95], s[94:95], vcc\n\t" \
  "s_cmp_eq_u64 s[94:95], 0\n\t" \
  "s_cbranch_scc1 .Lok%=\n\t" \
  "v_mov_b64 v[192:193], 0\n\t" \
  "v_mov_b64 v[194:195], 0\n\t" \
  "v_mov_b64 v[196:197], 0\n\t" \
  "v_mov_b64 v[198:199], 0\n\t" \
  "s_add_u32 s92, s92, 1\n\t" \
  "s_cmp_eq_u32 s92, 1\n\t" \
  "s_cbranch_scc1 .Lpoll%=\n\t" \
  "s_sleep 2\n\t" \
  "s_and_b32 s93, s92, 63\n\t" \
  "s_cmp_lg_u32 s93, 0\n\t" \
  "s_cbranch_scc1 .Lpoll%=\n\t" \
  "v_mov_b32 v245, 0\n\t" \
  "buffer_load_dword v244, v245, %[rs], %[sab] offen sc1\n\t" \
  "s_waitcnt vmcnt(0)\n\t" \
  "v_readfirstlane_b32 s93, v244\n\t" \
  "s_nop 3\n\t" \
  "s_cmp_eq_u32 s93, 1\n\t" \
  "s_cbranch_scc1 .Lok%=\n\t" \
  "s_cmp_lt_u32 s92, 0x80000\n\t" \
  "s_cbranch_scc1 .Lpoll%=\n\t" \
  "v_mov_b32 v244, 1\n\t" \
  "buffer_store_dword v244, v245, %[rs], %[sab] offen sc1\n\t" \
  "s_branch .Lok%=\n\t" \
  ".Lend%=:\n\t"
#define QK_ASM_3 \
  "s_waitcnt lgkmcnt(0)\n\t" \
  "v_subrev_u32 v244, 0x8000, v250\n\t" \
  "ds_read_b128 v[216:219], v248 offset:45056\n\t" \
  "ds_read_b128 v[220:223], v248 offset:46080\n\t" \
  "ds_read_b128 v[224:227], v248 offset:47104\n\t" \
  "ds_read_b128 v[228:231], v248 offset:48128\n\t" \
  "ds_read_b128 v[208:211], v244 offset:0\n\t" \
  "ds_read_b128 v[212:215], v244 offset:1024\n\t" \
  "ds_read_b128 v[236:239], v244 offset:2048\n\t" \
  "ds_read_b128 v[240:243], v244 offset:3072\n\t" \
  "v_mov_b64 v[192:193], 0\n\t" \
  "v_mov_b64 v[194:195], 0\n\t" \
  "v_mov_b64 v[196:197], 0\n\t" \
  "v_mov_b64 v[198:199], 0\n\t" \
  "s_movk_i32 s92, 0x2000\n\t" \
  ".Lwt%=:\n\t" \
  "ds_read_b64 v[200:201], v248 offset:30720\n\t" \
  "s_memrealtime s[90:91]\n\t" \
  "s_sub_u32 s92, s92, 1\n\t" \
  "s_waitcnt lgkmcnt(0)\n\t" \
  "v_readfirstlane_b32 s93, v200\n\t" \
  "v_readfirstlane_b32 %[tgt], v201\n\t" \
  "s_cbranch_scc1 .Lgo%=\n\t" \
  "s_cmp_eq_u32 s93, %[step]\n\t" \
  "s_cbranch_scc1 .Lwx%=\n\t" \
  "s_sleep 1\n\t" \
  "s_branch .Lwt%=\n\t" \
  ".Lwx%=:\n\t" \
  "s_lshl_b32 s93, s90, 2\n\t" \
  "s_sub_i32 s93, %[tgt], s93\n\t" \
  "s_cmp_le_i32 s93, 0\n\t" \
  "s_cbranch_scc1 .Lgo%=\n\t" \
  "s_min_i32 s93, s93, 8000\n\t" \
  "s_mov_b32 s94, s93\n\t" \
  ".Lsl%=:\n\t" \
  "s_sub_i32 s94, s94, 18\n\t" \
  "s_cmp_lt_i32 s94, 30\n\t" \
  "s_cbranch_scc1 .Lsx%=\n\t" \
  "s_sleep 1\n\t" \
  "s_branch .Lsl%=\n\t" \
  ".Lsx%=:\n\t" \
  "s_sub_i32 s93, s93, 12\n\t" \
  "s_max_i32 s93, s93, 4\n\t" \
  "s_lshr_b32 s93, s93, 2\n\t" \
  "s_add_u32 s93, s93, s90\n\t" \
  "s_memrealtime s[90:91]\n\t" \
  ".Lpace%=:\n\t" \
  "s_memrealtime s[94:95]\n\t" \
  "s_waitcnt lgkmcnt(1)\n\t" \
  "s_sub_i32 s92, s90, s93\n\t" \
  "s_cmp_ge_i32 s92, 0\n\t" \
  "s_cbranch_scc1 .Le%=\n\t" \
  "s_memrealtime s[90:91]\n\t" \
  "s_waitcnt lgkmcnt(1)\n\t" \
  "s_sub_i32 s92, s94, s93\n\t" \
  "s_cmp_lt_i32 s92, 0\n\t" \
  "s_cbranch_scc1 .Lpace%=\n\t" \
  "s_nop 4\n\t" \
  ".Le%=:\n\t" \
  "s_add_u32 s92, s92, s93\n\t" \
  "s_lshl_b32 s92, s92, 2\n\t" \
  "s_sub_i32 s92, %[tgt], s92\n\t" \
  "s_max_i32 s92, s92, 0\n\t" \
  "s_bitcmp0_b32 s92, 3\n\t" \
  "s_cbranch_scc1 .Lf3%=\n\t" \
  "s_nop 15\n\t" \
  ".Lf3%=:\n\t" \
  "s_bitcmp0_b32 s92, 2\n\t" \
  "s_cbranch_scc1 .Lf2%=\n\t" \
  "s_nop 10\n\t" \
  ".Lf2%=:\n\t" \
  ".Lgo%=:\n\t" \
  "s_mov_b32 s92, 0\n\t" \
  ".Lpoll%=:\n\t" \
  "buffer_load_dwordx4 v[200:203], v248, %[rs], %[som] offen sc1\n\t" \
  "buffer_load_dwordx4 v[204:207], v248, %[rs], %[som2] offen sc1\n\t" \
  "s_waitcnt vmcnt(1) lgkmcnt(0)\n\t" \
  "ds_write_b128 v249, v[200:203]\n\t" \
  "v_smfmac_f32_16x16x64_bf16 v[192:195], v[200:203], v[208:215], v251\n\t" \
  "v_max3_u32 v244, v200, v201, v202\n\t" \
  "v_smfmac_f32_16x16x64_bf16 v[196:199], v[200:203], v[236:243], v251\n\t" \
  "s_waitcnt vmcnt(0)\n\t" \
  "ds_write_b128 v249, v[204:207] offset:8192\n\t" \
  "v_smfmac_f32_16x16x64_bf16 v[192:195], v[204:207], v[216:223], v251\n\t" \
  "v_max3_u32 v244, v244, v203, v204\n\t" \
  "v_max3_u32 v244, v244, v205, v206\n\t" \
  "v_smfmac_f32_16x16x64_bf16 v[196:199], v[204:207], v[224:231], v251\n\t" \
  "v_max_u32 v244, v244, v207\n\t" \
  "v_cmp_le_u32 vcc, %[sent], v244\n\t" \
  "s_cbranch_vccnz .Lslow%=\n\t" \
  ".Lok%=:\n\t" \
  "ds_read_b128 v[216:219], v250 offset:32768\n\t" \
  "ds_read_b128 v[220:223], v250 offset:33792\n\t" \
  "ds_read_b128 v[224:227], v250 offset:34816\n\t" \
  "ds_read_b128 v[228:231], v250 offset:35840\n\t" \
  "s_waitcnt lgkmcnt(4)\n\t" \
  "s_barrier\n\t" \
  "ds_read_b128 v[208:211], v248 offset:2048\n\t" \
  "ds_read_b128 v[212:215], v248 offset:5120\n\t" \
  "ds_read_b128 v[236:239], v248 offset:6144\n\t" \
  "ds_read_b128 v[240:243], v248 offset:7168\n\t" \
  "s_waitcnt lgkmcnt(3)\n\t" \
  "ds_read_b128 v[244:247], v248 offset:12288\n\t" \
  "v_smfmac_f32_16x16x64_bf16 v[192:195], v[208:211], v[216:223], v251\n\t" \
  "ds_read_b128 v[216:219], v250 offset:0\n\t" \
  "v_smfmac_f32_16x16x64_bf16 v[196:199], v[208:211], v[224:231], v251\n\t" \
  "s_waitcnt lgkmcnt(4)\n\t" \
  "ds_read_b128 v[220:223], v250 offset:1024\n\t" \
  "v_smfmac_f32_16x16x64_bf16 v[192:195], v[212:215], v[0:7], v251\n\t" \
  "ds_read_b128 v[224:227], v250 offset:2048\n\t" \
  "v_smfmac_f32_16x16x64_bf16 v[196:199], v[212:215], v[8:15], v251\n\t" \
  "s_waitcnt lgkmcnt(5)\n\t" \
  "ds_read_b128 v[228:231], v250 offset:3072\n\t" \
  "v_smfmac_f32_16x16x64_bf16 v[192:195], v[236:239], v[16:23], v251\n\t" \
  "ds_read_b128 v[200:203], v248 offset:8192\n\t" \
  "v_smfmac_f32_16x16x64_bf16 v[196:199], v[236:239], v[24:31], v251\n\t" \
  "s_waitcnt lgkmcnt(6)\n\t" \
  "ds_read_b128 v[204:207], v248 offset:9216\n\t" \
  "v_smfmac_f32_16x16x64_bf16 v[192:195], v[240:243], v[32:39], v251\n\t" \
  "ds_read_b128 v[208:211], v248 offset:10240\n\t" \
  "v_smfmac_f32_16x16x64_bf16 v[196:199], v[240:243], v[40:47], v251\n\t" \
  "s_waitcnt lgkmcnt(5)\n\t" \
  "ds_read_b128 v[212:215], v248 offset:13312\n\t" \
  "v_smfmac_f32_16x16x64_bf16 v[192:195], v[244:247], v[216:223], v251\n\t" \
  "s_waitcnt lgkmcnt(4)\n\t" \
  "v_smfmac_f32_16x16x64_bf16 v[196:199], v[244:247], v[224:231], v251\n\t" \
  "s_waitcnt lgkmcnt(3)\n\t" \
  "ds_read_b128 v[236:239], v248 offset:14336\n\t" \
  "v_smfmac_f32_16x16x64_bf16 v[192:195], v[200:203], v[48:55], v251\n\t" \
  "ds_read_b128 v[240:243], v248 offset:15360\n\t" \
  "v_smfmac_f32_16x16x64_bf16 v[196:199], v[200:203], v[56:63], v251\n\t" \
  "s_waitcnt lgkmcnt(4)\n\t" \
  "v_smfmac_f32_16x16x64_bf16 v[192:195], v[204:207], v[64:71], v251\n\t" \
  "ds_read_b128 v[244:247], v248 offset:0\n\t" \
  "v_smfmac_f32_16x16x64_bf16 v[196:199], v[204:207], v[72:79], v251\n\t" \
  "s_waitcnt lgkmcnt(4)\n\t" \
  "v_smfmac_f32_16x16x64_bf16 v[192:195], v[208:211], v[80:87], v251\n\t" \
  "ds_read_b128 v[200:203], v248 offset:1024\n\t" \
  "v_smfmac_f32_16x16x64_bf16 v[196:199], v[208:211], v[88:95], v251\n\t" \
  "s_waitcnt lgkmcnt(4)\n\t" \
  "v_smfmac_f32_16x16x64_bf16 v[192:195], v[212:215], v[96:103], v251\n\t" \
  "ds_read_b128 v[204:207], v248 offset:4096\n\t" \
  "v_smfmac_f32_16x16x64_bf16 v[196:199], v[212:215], v[104:111], v251\n\t" \
  "s_waitcnt lgkmcnt(3)\n\t" \
  "v_smfmac_f32_16x16x64_bf16 v[192:195], v[236:239], v[112:119], v251\n\t" \
  "v_smfmac_f32_16x16x64_bf16 v[196:199], v[236:239], v[120:127], v251\n\t" \
  "v_smfmac_f32_16x16x64_bf16 v[192:195], v[240:243], v[128:135], v251\n\t" \
  "v_smfmac_f32_16x16x64_bf16 v[196:199], v[240:243], v[136:143], v251\n\t" \
  "s_waitcnt lgkmcnt(1)\n\t" \
  "v_smfmac_f32_16x16x64_bf16 v[192:195], v[244:247], v[144:151], v251\n\t" \
  "v_smfmac_f32_16x16x64_bf16 v[196:199], v[244:247], v[152:159], v251\n\t" \
  "v_smfmac_f32_16x16x64_bf16 v[192:195], v[200:203], v[160:167], v251\n\t" \
  "v_smfmac_f32_16x16x64_bf16 v[196:199], v[200:203], v[168:175], v251\n\t" \
  "s_waitcnt lgkmcnt(0)\n\t" \
  "v_smfmac_f32_16x16x64_bf16 v[192:195], v[204:207], v[176:183], v251\n\t" \
  "v_smfmac_f32_16x16x64_bf16 v[196:199], v[204:207], v[184:191], v251\n\t" \
  "ds_read_b128 v[200:203], v248 offset:19456\n\t" \
  "ds_read_b128 v[204:207], v248 offset:23552\n\t" \
  "v_mov_b32 v250, v232\n\t" \
  "v_mov_b32 v249, v233\n\t" \
  "s_nop 2\n\t" \
  "v_add_f32 v192, v192, v194\n\t" \
  "v_add_f32 v193, v193, v195\n\t" \
  "v_add_f32 v196, v196, v198\n\t" \
  "v_add_f32 v197, v197, v199\n\t" \
  "v_cvt_pk_bf16_f32 v192, v192, v193\n\t" \
  "v_cvt_pk_bf16_f32 v196, v196, v197\n\t" \
  "ds_write2_b32 v252, v192, v196 offset1:64\n\t" \
  "v_mov_b32 v251, v234\n\t" \
  "ds_read_b64_tr_b16 v[244:245], v250 offset:1536\n\t" \
  "ds_read_b64_tr_b16 v[246:247], v250 offset:1600\n\t" \
  "v_mov_b32 v212, %[step]\n\t" \
  "s_mov_b64 exec, 1\n\t" \
  "ds_write_b32 v248, v212 offset:28720\n\t" \
  "s_mov_b64 exec, -1\n\t" \
  "buffer_load_dwordx4 v[192:195], v248, %[rt], %[svt] offen offset:0 sc1\n\t" \
  "buffer_load_dwordx4 v[196:199], v248, %[rt], %[svt] offen offset:1024 sc1\n\t" \
  "s_waitcnt lgkmcnt(1)\n\t" \
  "v_mfma_f32_16x16x32_bf16 v[200:203], v[200:203], v[244:247], 0\n\t" \
  "v_mfma_f32_16x16x32_bf16 v[204:207], v[204:207], v[244:247], 0\n\t" \
  "s_nop 7\n\t" \
  "v_cndmask_b32 v216, v200, v204, %[nh]\n\t" \
  "v_cndmask_b32 v217, v201, v205, %[nh]\n\t" \
  "v_cndmask_b32 v218, v202, v206, %[nh]\n\t" \
  "v_cndmask_b32 v219, v203, v207, %[nh]\n\t" \
  "ds_write_b128 v248, v[216:219] offset:19456\n\t" \
  "buffer_load_dword v252, v249, %[rs], %[sv] offen offset:0 sc1\n\t" \
  "buffer_load_dword v253, v249, %[rs], %[sv] offen offset:256 sc1\n\t" \
  "ds_read_b32 v212, v248 offset:28672\n\t" \
  "ds_read_b64_tr_b16 v[232:233], v250 offset:0\n\t" \
  "ds_read_b64_tr_b16 v[234:235], v250 offset:64\n\t" \
  "ds_read_b64_tr_b16 v[236:237], v250 offset:512\n\t" \
  "ds_read_b64_tr_b16 v[238:239], v250 offset:576\n\t" \
  "ds_read_b64_tr_b16 v[240:241], v250 offset:1024\n\t" \
  "ds_read_b64_tr_b16 v[242:243], v250 offset:1088\n\t" \
  "s_setprio 3\n\t" \
  "s_waitcnt lgkmcnt(0)\n\t" \
  "v_cmp_ne_u32 vcc, %[step], v212\n\t" \
  "s_barrier\n\t" \
  "s_and_b32 s93, vcc_lo, 0xf\n\t" \
  "s_cbranch_scc1 .Lre%=\n\t" \
  ".Lrb%=:\n\t" \
  "ds_read_b64_tr_b16 v[212:213], v250 offset:3584\n\t" \
  "ds_read_b64_tr_b16 v[214:215], v250 offset:3648\n\t" \
  "ds_read_b64_tr_b16 v[200:201], v250 offset:2048\n\t" \
  "ds_read_b64_tr_b16 v[202:203], v250 offset:2112\n\t" \
  "ds_read_b128 v[220:223], v248 offset:16384\n\t" \
  "ds_read_b128 v[224:227], v248 offset:17408\n\t" \
  "ds_read_b128 v[228:231], v248 offset:18432\n\t" \
  "ds_read_b64_tr_b16 v[204:205], v250 offset:2560\n\t" \
  "ds_read_b64_tr_b16 v[206:207], v250 offset:2624\n\t" \
  "ds_read_b64_tr_b16 v[208:209], v250 offset:3072\n\t" \
  "ds_read_b64_tr_b16 v[210:211], v250 offset:3136\n\t" \
  "s_waitcnt lgkmcnt(9)\n\t" \
  "v_mfma_f32_16x16x32_bf16 v[212:215], v[212:215], v[244:247], 0\n\t" \
  "s_waitcnt lgkmcnt(7)\n\t" \
  "v_mfma_f32_16x16x32_bf16 v[212:215], v[200:203], v[232:235], v[212:215]\n\t" \
  "s_waitcnt lgkmcnt(6)\n\t" \
  "v_pk_add_f32 v[216:217], v[216:217], v[220:221]\n\t" \
  "v_pk_add_f32 v[218:219], v[218:219], v[222:223]\n\t" \
  "s_waitcnt lgkmcnt(4)\n\t" \
  "v_pk_add_f32 v[224:225], v[224:225], v[228:229]\n\t" \
  "v_pk_add_f32 v[226:227], v[226:227], v[230:231]\n\t" \
  "v_pk_add_f32 v[216:217], v[216:217], v[224:225]\n\t" \
  "v_pk_add_f32 v[218:219], v[218:219], v[226:227]\n\t" \
  "s_waitcnt lgkmcnt(2)\n\t" \
  "v_mfma_f32_16x16x32_bf16 v[212:215], v[204:207], v[236:239], v[212:215]\n\t" \
  "v_max3_f32 v224, v216, v217, v218\n\t" \
  "v_max_f32 v224, v224, v219\n\t" \
  "s_waitcnt lgkmcnt(0)\n\t" \
  "v_mfma_f32_16x16x32_bf16 v[212:215], v[208:211], v[240:243], v[212:215]\n\t" \
  "v_max_f32_dpp v225, v224, v224 row_ror:8 row_mask:0xf bank_mask:0xf\n\t" \
  "v_max_f32_dpp v226, v224, v224 row_ror:8 row_mask:0xf bank_mask:0xf\n\t" \
  "s_nop 1\n\t" \
  "v_permlane32_swap_b32 v225, v226\n\t" \
  "v_max_f32 v231, v225, v226\n\t" \
  "v_mov_b32 v220, 0x3f803f80\n\t" \
  "v_mov_b32 v221, 0x3f803f80\n\t" \
  "v_mov_b64 v[222:223], v[220:221]\n\t" \
  "v_mov_b32 v245, 0\n\t" \
  "v_mov_b32 v246, 0\n\t" \
  "v_max3_f32 v200, v212, v213, v231\n\t" \
  "v_max3_f32 v201, v200, v214, v215\n\t" \
  "v_max3_f32 v202, v200, v214, v215\n\t" \
  "v_cndmask_b32 v212, v212, v214, %[up]\n\t" \
  "v_cndmask_b32 v213, v213, v215, %[up]\n\t" \
  "v_permlane16_swap_b32 v201, v202\n\t" \
  "v_max_f32 v200, v201, v202\n\t" \
  "v_mul_f32 v200, %[negc], v200\n\t" \
  "v_fma_f32 v212, v212, %[c], v200\n\t" \
  "v_fma_f32 v213, v213, %[c], v200\n\t" \
  "v_exp_f32 v212, v212\n\t" \
  "v_fma_f32 v216, v216, %[c], v200\n\t" \
  "v_exp_f32 v213, v213\n\t" \
  "v_fma_f32 v217, v217, %[c], v200\n\t" \
  "v_exp_f32 v216, v216\n\t" \
  "v_fma_f32 v218, v218, %[c], v200\n\t" \
  "v_cvt_pk_bf16_f32 v244, v212, v213\n\t" \
  "v_cvt_pk_bf16_f32 v247, v212, v213\n\t" \
  "v_exp_f32 v217, v217\n\t" \
  "v_fma_f32 v219, v219, %[c], v200\n\t" \
  "v_mfma_f32_16x16x16_bf16 v[208:211], v[220:221], v[244:245], 0\n\t" \
  "v_exp_f32 v218, v218\n\t" \
  "s_waitcnt vmcnt(0)\n\t" \
  "v_mfma_f32_16x16x16_bf16 v[224:227], v[252:253], v[244:245], 0\n\t" \
  "v_cvt_pk_bf16_f32 v206, v216, v217\n\t" \
  "v_cvt_pk_bf16_f32 v204, v216, v217\n\t" \
  "v_exp_f32 v219, v219\n\t" \
  "v_mfma_f32_16x16x16_bf16 v[228:231], v[252:253], v[246:247], 0\n\t" \
  "v_cvt_pk_bf16_f32 v207, v218, v219\n\t" \
  "v_cvt_pk_bf16_f32 v205, v218, v219\n\t" \
  "v_mov_b32_dpp v204, v206 row_ror:8 row_mask:0xf bank_mask:0xc\n\t" \
  "v_mov_b32_dpp v206, v206 row_ror:8 row_mask:0xf bank_mask:0x3\n\t" \
  "v_mov_b32_dpp v205, v207 row_ror:8 row_mask:0xf bank_mask:0xc\n\t" \
  "v_mov_b32_dpp v207, v207 row_ror:8 row_mask:0xf bank_mask:0x3\n\t" \
  "v_max_u32 v200, v252, v253\n\t" \
  "v_cmp_le_u32 vcc, %[sent], v200\n\t" \
  "v_mfma_f32_16x16x32_bf16 v[208:211], v[220:223], v[204:207], v[208:211]\n\t" \
  ".Lpv%=:\n\t" \
  "v_mfma_f32_16x16x32_bf16 v[224:227], v[192:195], v[204:207], v[224:227]\n\t" \
  "v_mfma_f32_16x16x32_bf16 v[228:231], v[196:199], v[204:207], v[228:231]\n\t" \
  "s_cbranch_vccnz .Lvs%=\n\t" \
  ".Lvok%=:\n\t" \
  "s_nop 4\n\t" \
  "v_rcp_f32 v200, v208\n\t" \
  "v_cndmask_b32 v224, v224, v226, %[nh]\n\t" \
  "v_cndmask_b32 v225, v225, v227, %[nh]\n\t" \
  "v_cndmask_b32 v228, v228, v230, %[nh]\n\t" \
  "v_cndmask_b32 v229, v229, v231, %[nh]\n\t" \
  "v_pk_mul_f32 v[224:225], v[224:225], v[200:201] op_sel_hi:[1,0]\n\t" \
  "v_pk_mul_f32 v[228:229], v[228:229], v[200:201] op_sel_hi:[1,0]\n\t" \
  "v_cvt_pk_bf16_f32 v224, v224, v225\n\t" \
  "v_cvt_pk_bf16_f32 v225, v228, v229\n\t" \
  "buffer_store_dwordx2 v[224:225], v251, %[rs], %[so] offen sc1\n\t" \
  "s_memtime s[90:91]\n\t" \
  "s_setprio 0\n\t" \
  "s_waitcnt lgkmcnt(0)\n\t" \
  "s_mov_b32 %[tout], s90\n\t" \
  "s_mov_b32 %[spins], s92\n\t" \
  "s_branch .Lend%=\n\t" \
  ".Lre%=:\n\t" \
  "ds_read_b64_tr_b16 v[232:233], v250 offset:0\n\t" \
  "ds_read_b64_tr_b16 v[234:235], v250 offset:64\n\t" \
  "ds_read_b64_tr_b16 v[236:237], v250 offset:512\n\t" \
  "ds_read_b64_tr_b16 v[238:239], v250 offset:576\n\t" \
  "ds_read_b64_tr_b16 v[240:241], v250 offset:1024\n\t" \
  "ds_read_b64_tr_b16 v[242:243], v250 offset:1088\n\t" \
  "s_waitcnt lgkmcnt(0)\n\t" \
  "s_branch .Lrb%=\n\t" \
  ".Lvs%=:\n\t" \
  "v_cmp_eq_u32 vcc, %[sent], v252\n\t" \
  "v_cmp_eq_u32 s[94:95], %[sent], v253\n\t" \
  "s_or_b64 s[94:95], s[94:95], vcc\n\t" \
  "s_cbranch_scc0 .Lvok%=\n\t" \
  "s_add_u32 s92, s92, 1\n\t" \
  "s_sleep 2\n\t" \
  "s_and_b32 s93, s92, 63\n\t" \
  "s_cbranch_scc1 .Lvr%=\n\t" \
  "buffer_load_dword v200, off, %[rs], %[sab] sc1\n\t" \
  "s_waitcnt vmcnt(0)\n\t" \
  "v_readfirstlane_b32 s93, v200\n\t" \
  "s_nop 3\n\t" \
  "s_cmp_eq_u32 s93, 1\n\t" \
  "s_cbranch_scc1 .Lvok%=\n\t" \
  "s_cmp_lt_u32 s92, 0x80000\n\t" \
  "s_cbranch_scc1 .Lvr%=\n\t" \
  "v_mov_b32 v200, 1\n\t" \
  "buffer_store_dword v200, off, %[rs], %[sab] sc1\n\t" \
  "s_branch .Lvok%=\n\t" \
  ".Lvr%=:\n\t" \
  "buffer_load_dword v252, v249, %[rs], %[sv] offen offset:0 sc1\n\t" \
  "buffer_load_dword v253, v249, %[rs], %[sv] offen offset:256 sc1\n\t" \
  "s_waitcnt vmcnt(0)\n\t" \
  "v_mfma_f32_16x16x16_bf16 v[224:227], v[252:253], v[244:245], 0\n\t" \
  "v_mfma_f32_16x16x16_bf16 v[228:231], v[252:253], v[246:247], 0\n\t" \
  "v_max_u32 v200, v252, v253\n\t" \
  "v_cmp_le_u32 vcc, %[sent], v200\n\t" \
  "s_nop 4\n\t" \
  "s_branch .Lpv%=\n\t" \
  "s_branch .Lend%=\n\t" \
  ".Lslow%=:\n\t" \
  "s_mov_b64 s[94:95], 0\n\t" \
  "v_cmp_eq_u32 vcc, %[sent], v200\n\t" \
  "s_or_b64 s[94:95], s[94:95], vcc\n\t" \
  "v_cmp_eq_u32 vcc, %[sent], v201\n\t" \
  "s_or_b64 s[94:95], s[94:95], vcc\n\t" \
  "v_cmp_eq_u32 vcc, %[sent], v202\n\t" \
  "s_or_b64 s[94:95], s[94:95], vcc\n\t" \
  "v_cmp_eq_u32 vcc, %[sent], v203\n\t" \
  "s_or_b64 s[94:95], s[94:95], vcc\n\t" \
  "v_cmp_eq_u32 vcc, %[sent], v204\n\t" \
  "s_or_b64 s[94:95], s[94:95], vcc\n\t" \
  "v_cmp_eq_u32 vcc, %[sent], v205\n\t" \
  "s_or_b64 s[94:95], s[94:95], vcc\n\t" \
  "v_cmp_eq_u32 vcc, %[sent], v206\n\t" \
  "s_or_b64 s[94:95], s[94:95], vcc\n\t" \
  "v_cmp_eq_u32 vcc, %[sent], v207\n\t" \
  "s_or_b64 s[94:95], s[94:95], vcc\n\t" \
  "s_cmp_eq_u64 s[94:95], 0\n\t" \
  "s_cbranch_scc1 .Lok%=\n\t" \
  "v_mov_b64 v[192:193], 0\n\t" \
  "v_mov_b64 v[194:195], 0\n\t" \
  "v_mov_b64 v[196:197], 0\n\t" \
  "v_mov_b64 v[198:199], 0\n\t" \
  "s_add_u32 s92, s92, 1\n\t" \
  "s_cmp_eq_u32 s92, 1\n\t" \
  "s_cbranch_scc1 .Lpoll%=\n\t" \
  "s_sleep 2\n\t" \
  "s_and_b32 s93, s92, 63\n\t" \
  "s_cmp_lg_u32 s93, 0\n\t" \
  "s_cbranch_scc1 .Lpoll%=\n\t" \
  "v_mov_b32 v245, 0\n\t" \
  "buffer_load_dword v244, v245, %[rs], %[sab] offen sc1\n\t" \
  "s_waitcnt vmcnt(0)\n\t" \
  "v_readfirstlane_b32 s93, v244\n\t" \
  "s_nop 3\n\t" \
  "s_cmp_eq_u32 s93, 1\n\t" \
  "s_cbranch_scc1 .Lok%=\n\t" \
  "s_cmp_lt_u32 s92, 0x80000\n\t" \
  "s_cbranch_scc1 .Lpoll%=\n\t" \
  "v_mov_b32 v244, 1\n\t" \
  "buffer_store_dword v244, v245, %[rs], %[sab] offen sc1\n\t" \
  "s_branch .Lok%=\n\t" \
  ".Lend%=:\n\t"
#define QK_ASM_4 \
  "s_waitcnt lgkmcnt(0)\n\t" \
  "v_subrev_u32 v244, 0x8000, v250\n\t" \
  "ds_read_b128 v[216:219], v248 offset:49152\n\t" \
  "ds_read_b128 v[220:223], v248 offset:50176\n\t" \
  "ds_read_b128 v[224:227], v248 offset:51200\n\t" \
  "ds_read_b128 v[228:231], v248 offset:52224\n\t" \
  "ds_read_b128 v[208:211], v244 offset:0\n\t" \
  "ds_read_b128 v[212:215], v244 offset:1024\n\t" \
  "ds_read_b128 v[236:239], v244 offset:2048\n\t" \
  "ds_read_b128 v[240:243], v244 offset:3072\n\t" \
  "v_mov_b64 v[192:193], 0\n\t" \
  "v_mov_b64 v[194:195], 0\n\t" \
  "v_mov_b64 v[196:197], 0\n\t" \
  "v_mov_b64 v[198:199], 0\n\t" \
  "s_movk_i32 s92, 0x2000\n\t" \
  ".Lwt%=:\n\t" \
  "ds_read_b64 v[200:201], v248 offset:30720\n\t" \
  "s_memrealtime s[90:91]\n\t" \
  "s_sub_u32 s92, s92, 1\n\t" \
  "s_waitcnt lgkmcnt(0)\n\t" \
  "v_readfirstlane_b32 s93, v200\n\t" \
  "v_readfirstlane_b32 %[tgt], v201\n\t" \
  "s_cbranch_scc1 .Lgo%=\n\t" \
  "s_cmp_eq_u32 s93, %[step]\n\t" \
  "s_cbranch_scc1 .Lwx%=\n\t" \
  "s_sleep 1\n\t" \
  "s_branch .Lwt%=\n\t" \
  ".Lwx%=:\n\t" \
  "s_lshl_b32 s93, s90, 2\n\t" \
  "s_sub_i32 s93, %[tgt], s93\n\t" \
  "s_cmp_le_i32 s93, 0\n\t" \
  "s_cbranch_scc1 .Lgo%=\n\t" \
  "s_min_i32 s93, s93, 8000\n\t" \
  "s_mov_b32 s94, s93\n\t" \
  ".Lsl%=:\n\t" \
  "s_sub_i32 s94, s94, 18\n\t" \
  "s_cmp_lt_i32 s94, 30\n\t" \
  "s_cbranch_scc1 .Lsx%=\n\t" \
  "s_sleep 1\n\t" \
  "s_branch .Lsl%=\n\t" \
  ".Lsx%=:\n\t" \
  "s_sub_i32 s93, s93, 12\n\t" \
  "s_max_i32 s93, s93, 4\n\t" \
  "s_lshr_b32 s93, s93, 2\n\t" \
  "s_add_u32 s93, s93, s90\n\t" \
  "s_memrealtime s[90:91]\n\t" \
  ".Lpace%=:\n\t" \
  "s_memrealtime s[94:95]\n\t" \
  "s_waitcnt lgkmcnt(1)\n\t" \
  "s_sub_i32 s92, s90, s93\n\t" \
  "s_cmp_ge_i32 s92, 0\n\t" \
  "s_cbranch_scc1 .Le%=\n\t" \
  "s_memrealtime s[90:91]\n\t" \
  "s_waitcnt lgkmcnt(1)\n\t" \
  "s_sub_i32 s92, s94, s93\n\t" \
  "s_cmp_lt_i32 s92, 0\n\t" \
  "s_cbranch_scc1 .Lpace%=\n\t" \
  "s_nop 4\n\t" \
  ".Le%=:\n\t" \
  "s_add_u32 s92, s92, s93\n\t" \
  "s_lshl_b32 s92, s92, 2\n\t" \
  "s_sub_i32 s92, %[tgt], s92\n\t" \
  "s_max_i32 s92, s92, 0\n\t" \
  "s_bitcmp0_b32 s92, 3\n\t" \
  "s_cbranch_scc1 .Lf3%=\n\t" \
  "s_nop 15\n\t" \
  ".Lf3%=:\n\t" \
  "s_bitcmp0_b32 s92, 2\n\t" \
  "s_cbranch_scc1 .Lf2%=\n\t" \
  "s_nop 10\n\t" \
  ".Lf2%=:\n\t" \
  ".Lgo%=:\n\t" \
  "s_mov_b32 s92, 0\n\t" \
  ".Lpoll%=:\n\t" \
  "buffer_load_dwordx4 v[200:203], v248, %[rs], %[som] offen sc1\n\t" \
  "buffer_load_dwordx4 v[204:207], v248, %[rs], %[som2] offen sc1\n\t" \
  "s_waitcnt vmcnt(1) lgkmcnt(0)\n\t" \
  "ds_write_b128 v249, v[200:203]\n\t" \
  "v_smfmac_f32_16x16x64_bf16 v[192:195], v[200:203], v[208:215], v251\n\t" \
  "v_max3_u32 v244, v200, v201, v202\n\t" \
  "v_smfmac_f32_16x16x64_bf16 v[196:199], v[200:203], v[236:243], v251\n\t" \
  "s_waitcnt vmcnt(0)\n\t" \
  "ds_write_b128 v249, v[204:207] offset:8192\n\t" \
  "v_smfmac_f32_16x16x64_bf16 v[192:195], v[204:207], v[216:223], v251\n\t" \
  "v_max3_u32 v244, v244, v203, v204\n\t" \
  "v_max3_u32 v244, v244, v205, v206\n\t" \
  "v_smfmac_f32_16x16x64_bf16 v[196:199], v[204:207], v[224:231], v251\n\t" \
  "v_max_u32 v244, v244, v207\n\t" \
  "v_cmp_le_u32 vcc, %[sent], v244\n\t" \
  "s_cbranch_vccnz .Lslow%=\n\t" \
  ".Lok%=:\n\t" \
  "ds_read_b128 v[216:219], v250 offset:32768\n\t" \
  "ds_read_b128 v[220:223], v250 offset:33792\n\t" \
  "ds_read_b128 v[224:227], v250 offset:34816\n\t" \
  "ds_read_b128 v[228:231], v250 offset:35840\n\t" \
  "s_waitcnt lgkmcnt(4)\n\t" \
  "s_barrier\n\t" \
  "ds_read_b128 v[208:211], v248 offset:5120\n\t" \
  "ds_read_b128 v[212:215], v248 offset:6144\n\t" \
  "ds_read_b128 v[236:239], v248 offset:7168\n\t" \
  "ds_read_b128 v[240:243], v248 offset:8192\n\t" \
  "s_waitcnt lgkmcnt(3)\n\t" \
  "ds_read_b128 v[244:247], v248 offset:13312\n\t" \
  "v_smfmac_f32_16x16x64_bf16 v[192:195], v[208:211], v[216:223], v251\n\t" \
  "ds_read_b128 v[216:219], v250 offset:0\n\t" \
  "v_smfmac_f32_16x16x64_bf16 v[196:199], v[208:211], v[224:231], v251\n\t" \
  "s_waitcnt lgkmcnt(4)\n\t" \
  "ds_read_b128 v[220:223], v250 offset:1024\n\t" \
  "v_smfmac_f32_16x16x64_bf16 v[192:195], v[212:215], v[0:7], v251\n\t" \
  "ds_read_b128 v[224:227], v250 offset:2048\n\t" \
  "v_smfmac_f32_16x16x64_bf16 v[196:199], v[212:215], v[8:15], v251\n\t" \
  "s_waitcnt lgkmcnt(5)\n\t" \
  "ds_read_b128 v[228:231], v250 offset:3072\n\t" \
  "v_smfmac_f32_16x16x64_bf16 v[192:195], v[236:239], v[16:23], v251\n\t" \
  "ds_read_b128 v[200:203], v248 offset:9216\n\t" \
  "v_smfmac_f32_16x16x64_bf16 v[196:199], v[236:239], v[24:31], v251\n\t" \
  "s_waitcnt lgkmcnt(6)\n\t" \
  "ds_read_b128 v[204:207], v248 offset:10240\n\t" \
  "v_smfmac_f32_16x16x64_bf16 v[192:195], v[240:243], v[32:39], v251\n\t" \
  "ds_read_b128 v[208:211], v248 offset:11264\n\t" \
  "v_smfmac_f32_16x16x64_bf16 v[196:199], v[240:243], v[40:47], v251\n\t" \
  "s_waitcnt lgkmcnt(5)\n\t" \
  "ds_read_b128 v[212:215], v248 offset:14336\n\t" \
  "v_smfmac_f32_16x16x64_bf16 v[192:195], v[244:247], v[216:223], v251\n\t" \
  "s_waitcnt lgkmcnt(4)\n\t" \
  "v_smfmac_f32_16x16x64_bf16 v[196:199], v[244:247], v[224:231], v251\n\t" \
  "s_waitcnt lgkmcnt(3)\n\t" \
  "ds_read_b128 v[236:239], v248 offset:15360\n\t" \
  "v_smfmac_f32_16x16x64_bf16 v[192:195], v[200:203], v[48:55], v251\n\t" \
  "ds_read_b128 v[240:243], v248 offset:0\n\t" \
  "v_smfmac_f32_16x16x64_bf16 v[196:199], v[200:203], v[56:63], v251\n\t" \
  "s_waitcnt lgkmcnt(4)\n\t" \
  "v_smfmac_f32_16x16x64_bf16 v[192:195], v[204:207], v[64:71], v251\n\t" \
  "ds_read_b128 v[244:247], v248 offset:1024\n\t" \
  "v_smfmac_f32_16x16x64_bf16 v[196:199], v[204:207], v[72:79], v251\n\t" \
  "s_waitcnt lgkmcnt(4)\n\t" \
  "v_smfmac_f32_16x16x64_bf16 v[192:195], v[208:211], v[80:87], v251\n\t" \
  "ds_read_b128 v[200:203], v248 offset:2048\n\t" \
  "v_smfmac_f32_16x16x64_bf16 v[196:199], v[208:211], v[88:95], v251\n\t" \
  "s_waitcnt lgkmcnt(4)\n\t" \
  "v_smfmac_f32_16x16x64_bf16 v[192:195], v[212:215], v[96:103], v251\n\t" \
  "ds_read_b128 v[204:207], v248 offset:3072\n\t" \
  "v_smfmac_f32_16x16x64_bf16 v[196:199], v[212:215], v[104:111], v251\n\t" \
  "s_waitcnt lgkmcnt(3)\n\t" \
  "v_smfmac_f32_16x16x64_bf16 v[192:195], v[236:239], v[112:119], v251\n\t" \
  "v_smfmac_f32_16x16x64_bf16 v[196:199], v[236:239], v[120:127], v251\n\t" \
  "v_smfmac_f32_16x16x64_bf16 v[192:195], v[240:243], v[128:135], v251\n\t" \
  "v_smfmac_f32_16x16x64_bf16 v[196:199], v[240:243], v[136:143], v251\n\t" \
  "s_waitcnt lgkmcnt(1)\n\t" \
  "v_smfmac_f32_16x16x64_bf16 v[192:195], v[244:247], v[144:151], v251\n\t" \
  "v_smfmac_f32_16x16x64_bf16 v[196:199], v[244:247], v[152:159], v251\n\t" \
  "v_smfmac_f32_16x16x64_bf16 v[192:195], v[200:203], v[160:167], v251\n\t" \
  "v_smfmac_f32_16x16x64_bf16 v[196:199], v[200:203], v[168:175], v251\n\t" \
  "s_waitcnt lgkmcnt(0)\n\t" \
  "v_smfmac_f32_16x16x64_bf16 v[192:195], v[204:207], v[176:183], v251\n\t" \
  "v_smfmac_f32_16x16x64_bf16 v[196:199], v[204:207], v[184:191], v251\n\t" \
  "s_nop 6\n\t" \
  "v_add_f32 v192, v192, v194\n\t" \
  "v_add_f32 v193, v193, v195\n\t" \
  "v_add_f32 v196, v196, v198\n\t" \
  "v_add_f32 v197, v197, v199\n\t" \
  "v_cvt_pk_bf16_f32 v192, v192, v193\n\t" \
  "v_cvt_pk_bf16_f32 v196, v196, v197\n\t" \
  "ds_write2_b32 v252, v192, v196 offset1:64\n\t" \
  "s_waitcnt lgkmcnt(0)\n\t" \
  "s_barrier\n\t" \
  "buffer_store_dword v192, v235, %[rs], %[sk] offen sc1\n\t" \
  "buffer_store_dword v196, v235, %[rs], %[sk] offen offset:256 sc1\n\t" \
  "s_mov_b32 %[spins], s92\n\t" \
  "s_branch .Lend%=\n\t" \
  ".Lslow%=:\n\t" \
  "s_mov_b64 s[94:95], 0\n\t" \
  "v_cmp_eq_u32 vcc, %[sent], v200\n\t" \
  "s_or_b64 s[94:95], s[94:95], vcc\n\t" \
  "v_cmp_eq_u32 vcc, %[sent], v201\n\t" \
  "s_or_b64 s[94:95], s[94:95], vcc\n\t" \
  "v_cmp_eq_u32 vcc, %[sent], v202\n\t" \
  "s_or_b64 s[94:95], s[94:95], vcc\n\t" \
  "v_cmp_eq_u32 vcc, %[sent], v203\n\t" \
  "s_or_b64 s[94:95], s[94:95], vcc\n\t" \
  "v_cmp_eq_u32 vcc, %[sent], v204\n\t" \
  "s_or_b64 s[94:95], s[94:95], vcc\n\t" \
  "v_cmp_eq_u32 vcc, %[sent], v205\n\t" \
  "s_or_b64 s[94:95], s[94:95], vcc\n\t" \
  "v_cmp_eq_u32 vcc, %[sent], v206\n\t" \
  "s_or_b64 s[94:95], s[94:95], vcc\n\t" \
  "v_cmp_eq_u32 vcc, %[sent], v207\n\t" \
  "s_or_b64 s[94:95], s[94:95], vcc\n\t" \
  "s_cmp_eq_u64 s[94:95], 0\n\t" \
  "s_cbranch_scc1 .Lok%=\n\t" \
  "v_mov_b64 v[192:193], 0\n\t" \
  "v_mov_b64 v[194:195], 0\n\t" \
  "v_mov_b64 v[196:197], 0\n\t" \
  "v_mov_b64 v[198:199], 0\n\t" \
  "s_add_u32 s92, s92, 1\n\t" \
  "s_cmp_eq_u32 s92, 1\n\t" \
  "s_cbranch_scc1 .Lpoll%=\n\t" \
  "s_sleep 2\n\t" \
  "s_and_b32 s93, s92, 63\n\t" \
  "s_cmp_lg_u32 s93, 0\n\t" \
  "s_cbranch_scc1 .Lpoll%=\n\t" \
  "v_mov_b32 v245, 0\n\t" \
  "buffer_load_dword v244, v245, %[rs], %[sab] offen sc1\n\t" \
  "s_waitcnt vmcnt(0)\n\t" \
  "v_readfirstlane_b32 s93, v244\n\t" \
  "s_nop 3\n\t" \
  "s_cmp_eq_u32 s93, 1\n\t" \
  "s_cbranch_scc1 .Lok%=\n\t" \
  "s_cmp_lt_u32 s92, 0x80000\n\t" \
  "s_cbranch_scc1 .Lpoll%=\n\t" \
  "v_mov_b32 v244, 1\n\t" \
  "buffer_store_dword v244, v245, %[rs], %[sab] offen sc1\n\t" \
  "s_branch .Lok%=\n\t" \
  ".Lend%=:\n\t"
#define QK_ASM_5 \
  "s_waitcnt lgkmcnt(0)\n\t" \
  "v_subrev_u32 v244, 0x8000, v250\n\t" \
  "ds_read_b128 v[216:219], v248 offset:53248\n\t" \
  "ds_read_b128 v[220:223], v248 offset:54272\n\t" \
  "ds_read_b128 v[224:227], v248 offset:55296\n\t" \
  "ds_read_b128 v[228:231], v248 offset:56320\n\t" \
  "ds_read_b128 v[208:211], v244 offset:0\n\t" \
  "ds_read_b128 v[212:215], v244 offset:1024\n\t" \
  "ds_read_b128 v[236:239], v244 offset:2048\n\t" \
  "ds_read_b128 v[240:243], v244 offset:3072\n\t" \
  "v_mov_b64 v[192:193], 0\n\t" \
  "v_mov_b64 v[194:195], 0\n\t" \
  "v_mov_b64 v[196:197], 0\n\t" \
  "v_mov_b64 v[198:199], 0\n\t" \
  "s_movk_i32 s92, 0x2000\n\t" \
  ".Lwt%=:\n\t" \
  "ds_read_b64 v[200:201], v248 offset:30720\n\t" \
  "s_memrealtime s[90:91]\n\t" \
  "s_sub_u32 s92, s92, 1\n\t" \
  "s_waitcnt lgkmcnt(0)\n\t" \
  "v_readfirstlane_b32 s93, v200\n\t" \
  "v_readfirstlane_b32 %[tgt], v201\n\t" \
  "s_cbranch_scc1 .Lgo%=\n\t" \
  "s_cmp_eq_u32 s93, %[step]\n\t" \
  "s_cbranch_scc1 .Lwx%=\n\t" \
  "s_sleep 1\n\t" \
  "s_branch .Lwt%=\n\t" \
  ".Lwx%=:\n\t" \
  "s_lshl_b32 s93, s90, 2\n\t" \
  "s_sub_i32 s93, %[tgt], s93\n\t" \
  "s_cmp_le_i32 s93, 0\n\t" \
  "s_cbranch_scc1 .Lgo%=\n\t" \
  "s_min_i32 s93, s93, 8000\n\t" \
  "s_mov_b32 s94, s93\n\t" \
  ".Lsl%=:\n\t" \
  "s_sub_i32 s94, s94, 18\n\t" \
  "s_cmp_lt_i32 s94, 30\n\t" \
  "s_cbranch_scc1 .Lsx%=\n\t" \
  "s_sleep 1\n\t" \
  "s_branch .Lsl%=\n\t" \
  ".Lsx%=:\n\t" \
  "s_sub_i32 s93, s93, 12\n\t" \
  "s_max_i32 s93, s93, 4\n\t" \
  "s_lshr_b32 s93, s93, 2\n\t" \
  "s_add_u32 s93, s93, s90\n\t" \
  "s_memrealtime s[90:91]\n\t" \
  ".Lpace%=:\n\t" \
  "s_memrealtime s[94:95]\n\t" \
  "s_waitcnt lgkmcnt(1)\n\t" \
  "s_sub_i32 s92, s90, s93\n\t" \
  "s_cmp_ge_i32 s92, 0\n\t" \
  "s_cbranch_scc1 .Le%=\n\t" \
  "s_memrealtime s[90:91]\n\t" \
  "s_waitcnt lgkmcnt(1)\n\t" \
  "s_sub_i32 s92, s94, s93\n\t" \
  "s_cmp_lt_i32 s92, 0\n\t" \
  "s_cbranch_scc1 .Lpace%=\n\t" \
  "s_nop 4\n\t" \
  ".Le%=:\n\t" \
  "s_add_u32 s92, s92, s93\n\t" \
  "s_lshl_b32 s92, s92, 2\n\t" \
  "s_sub_i32 s92, %[tgt], s92\n\t" \
  "s_max_i32 s92, s92, 0\n\t" \
  "s_bitcmp0_b32 s92, 3\n\t" \
  "s_cbranch_scc1 .Lf3%=\n\t" \
  "s_nop 15\n\t" \
  ".Lf3%=:\n\t" \
  "s_bitcmp0_b32 s92, 2\n\t" \
  "s_cbranch_scc1 .Lf2%=\n\t" \
  "s_nop 10\n\t" \
  ".Lf2%=:\n\t" \
  ".Lgo%=:\n\t" \
  "s_mov_b32 s92, 0\n\t" \
  ".Lpoll%=:\n\t" \
  "buffer_load_dwordx4 v[200:203], v248, %[rs], %[som] offen sc1\n\t" \
  "buffer_load_dwordx4 v[204:207], v248, %[rs], %[som2] offen sc1\n\t" \
  "s_waitcnt vmcnt(1) lgkmcnt(0)\n\t" \
  "ds_write_b128 v249, v[200:203]\n\t" \
  "v_smfmac_f32_16x16x64_bf16 v[192:195], v[200:203], v[208:215], v251\n\t" \
  "v_max3_u32 v244, v200, v201, v202\n\t" \
  "v_smfmac_f32_16x16x64_bf16 v[196:199], v[200:203], v[236:243], v251\n\t" \
  "s_waitcnt vmcnt(0)\n\t" \
  "ds_write_b128 v249, v[204:207] offset:8192\n\t" \
  "v_smfmac_f32_16x16x64_bf16 v[192:195], v[204:207], v[216:223], v251\n\t" \
  "v_max3_u32 v244, v244, v203, v204\n\t" \
  "v_max3_u32 v244, v244, v205, v206\n\t" \
  "v_smfmac_f32_16x16x64_bf16 v[196:199], v[204:207], v[224:231], v251\n\t" \
  "v_max_u32 v244, v244, v207\n\t" \
  "v_cmp_le_u32 vcc, %[sent], v244\n\t" \
  "s_cbranch_vccnz .Lslow%=\n\t" \
  ".Lok%=:\n\t" \
  "ds_read_b128 v[216:219], v250 offset:32768\n\t" \
  "ds_read_b128 v[220:223], v250 offset:33792\n\t" \
  "ds_read_b128 v[224:227], v250 offset:34816\n\t" \
  "ds_read_b128 v[228:231], v250 offset:35840\n\t" \
  "s_waitcnt lgkmcnt(4)\n\t" \
  "s_barrier\n\t" \
  "ds_read_b128 v[208:211], v248 offset:4096\n\t" \
  "ds_read_b128 v[212:215], v248 offset:7168\n\t" \
  "ds_read_b128 v[236:239], v248 offset:8192\n\t" \
  "ds_read_b128 v[240:243], v248 offset:9216\n\t" \
  "s_waitcnt lgkmcnt(3)\n\t" \
  "ds_read_b128 v[244:247], v248 offset:12288\n\t" \
  "v_smfmac_f32_16x16x64_bf16 v[192:195], v[208:211], v[216:223], v251\n\t" \
  "ds_read_b128 v[216:219], v250 offset:0\n\t" \
  "v_smfmac_f32_16x16x64_bf16 v[196:199], v[208:211], v[224:231], v251\n\t" \
  "s_waitcnt lgkmcnt(4)\n\t" \
  "ds_read_b128 v[220:223], v250 offset:1024\n\t" \
  "v_smfmac_f32_16x16x64_bf16 v[192:195], v[212:215], v[0:7], v251\n\t" \
  "ds_read_b128 v[224:227], v250 offset:2048\n\t" \
  "v_smfmac_f32_16x16x64_bf16 v[196:199], v[212:215], v[8:15], v251\n\t" \
  "s_waitcnt lgkmcnt(5)\n\t" \
  "ds_read_b128 v[228:231], v250 offset:3072\n\t" \
  "v_smfmac_f32_16x16x64_bf16 v[192:195], v[236:239], v[16:23], v251\n\t" \
  "ds_read_b128 v[200:203], v248 offset:10240\n\t" \
  "v_smfmac_f32_16x16x64_bf16 v[196:199], v[236:239], v[24:31], v251\n\t" \
  "s_waitcnt lgkmcnt(6)\n\t" \
  "ds_read_b128 v[204:207], v248 offset:11264\n\t" \
  "v_smfmac_f32_16x16x64_bf16 v[192:195], v[240:243], v[32:39], v251\n\t" \
  "ds_read_b128 v[208:211], v248 offset:14336\n\t" \
  "v_smfmac_f32_16x16x64_bf16 v[196:199], v[240:243], v[40:47], v251\n\t" \
  "s_waitcnt lgkmcnt(5)\n\t" \
  "ds_read_b128 v[212:215], v248 offset:15360\n\t" \
  "v_smfmac_f32_16x16x64_bf16 v[192:195], v[244:247], v[216:223], v251\n\t" \
  "s_waitcnt lgkmcnt(4)\n\t" \
  "v_smfmac_f32_16x16x64_bf16 v[196:199], v[244:247], v[224:231], v251\n\t" \
  "s_waitcnt lgkmcnt(3)\n\t" \
  "ds_read_b128 v[236:239], v248 offset:0\n\t" \
  "v_smfmac_f32_16x16x64_bf16 v[192:195], v[200:203], v[48:55], v251\n\t" \
  "ds_read_b128 v[240:243], v248 offset:1024\n\t" \
  "v_smfmac_f32_16x16x64_bf16 v[196:199], v[200:203], v[56:63], v251\n\t" \
  "s_waitcnt lgkmcnt(4)\n\t" \
  "v_smfmac_f32_16x16x64_bf16 v[192:195], v[204:207], v[64:71], v251\n\t" \
  "ds_read_b128 v[244:247], v248 offset:2048\n\t" \
  "v_smfmac_f32_16x16x64_bf16 v[196:199], v[204:207], v[72:79], v251\n\t" \
  "s_waitcnt lgkmcnt(4)\n\t" \
  "v_smfmac_f32_16x16x64_bf16 v[192:195], v[208:211], v[80:87], v251\n\t" \
  "ds_read_b128 v[200:203], v248 offset:3072\n\t" \
  "v_smfmac_f32_16x16x64_bf16 v[196:199], v[208:211], v[88:95], v251\n\t" \
  "s_waitcnt lgkmcnt(4)\n\t" \
  "v_smfmac_f32_16x16x64_bf16 v[192:195], v[212:215], v[96:103], v251\n\t" \
  "ds_read_b128 v[204:207], v248 offset:6144\n\t" \
  "v_smfmac_f32_16x16x64_bf16 v[196:199], v[212:215], v[104:111], v251\n\t" \
  "s_waitcnt lgkmcnt(3)\n\t" \
  "v_smfmac_f32_16x16x64_bf16 v[192:195], v[236:239], v[112:119], v251\n\t" \
  "v_smfmac_f32_16x16x64_bf16 v[196:199], v[236:239], v[120:127], v251\n\t" \
  "v_smfmac_f32_16x16x64_bf16 v[192:195], v[240:243], v[128:135], v251\n\t" \
  "v_smfmac_f32_16x16x64_bf16 v[196:199], v[240:243], v[136:143], v251\n\t" \
  "s_waitcnt lgkmcnt(1)\n\t" \
  "v_smfmac_f32_16x16x64_bf16 v[192:195], v[244:247], v[144:151], v251\n\t" \
  "v_smfmac_f32_16x16x64_bf16 v[196:199], v[244:247], v[152:159], v251\n\t" \
  "v_smfmac_f32_16x16x64_bf16 v[192:195], v[200:203], v[160:167], v251\n\t" \
  "v_smfmac_f32_16x16x64_bf16 v[196:199], v[200:203], v[168:175], v251\n\t" \
  "s_waitcnt lgkmcnt(0)\n\t" \
  "v_smfmac_f32_16x16x64_bf16 v[192:195], v[204:207], v[176:183], v251\n\t" \
  "v_smfmac_f32_16x16x64_bf16 v[196:199], v[204:207], v[184:191], v251\n\t" \
  "s_nop 6\n\t" \
  "v_add_f32 v192, v192, v194\n\t" \
  "v_add_f32 v193, v193, v195\n\t" \
  "v_add_f32 v196, v196, v198\n\t" \
  "v_add_f32 v197, v197, v199\n\t" \
  "v_cvt_pk_bf16_f32 v192, v192, v193\n\t" \
  "v_cvt_pk_bf16_f32 v196, v196, v197\n\t" \
  "ds_write2_b32 v252, v192, v196 offset1:64\n\t" \
  "s_waitcnt lgkmcnt(0)\n\t" \
  "s_barrier\n\t" \
  "buffer_store_dword v192, v235, %[rs], %[sk] offen sc1\n\t" \
  "buffer_store_dword v196, v235, %[rs], %[sk] offen offset:256 sc1\n\t" \
  "s_mov_b32 %[spins], s92\n\t" \
  "s_branch .Lend%=\n\t" \
  ".Lslow%=:\n\t" \
  "s_mov_b64 s[94:95], 0\n\t" \
  "v_cmp_eq_u32 vcc, %[sent], v200\n\t" \
  "s_or_b64 s[94:95], s[94:95], vcc\n\t" \
  "v_cmp_eq_u32 vcc, %[sent], v201\n\t" \
  "s_or_b64 s[94:95], s[94:95], vcc\n\t" \
  "v_cmp_eq_u32 vcc, %[sent], v202\n\t" \
  "s_or_b64 s[94:95], s[94:95], vcc\n\t" \
  "v_cmp_eq_u32 vcc, %[sent], v203\n\t" \
  "s_or_b64 s[94:95], s[94:95], vcc\n\t" \
  "v_cmp_eq_u32 vcc, %[sent], v204\n\t" \
  "s_or_b64 s[94:95], s[94:95], vcc\n\t" \
  "v_cmp_eq_u32 vcc, %[sent], v205\n\t" \
  "s_or_b64 s[94:95], s[94:95], vcc\n\t" \
  "v_cmp_eq_u32 vcc, %[sent], v206\n\t" \
  "s_or_b64 s[94:95], s[94:95], vcc\n\t" \
  "v_cmp_eq_u32 vcc, %[sent], v207\n\t" \
  "s_or_b64 s[94:95], s[94:95], vcc\n\t" \
  "s_cmp_eq_u64 s[94:95], 0\n\t" \
  "s_cbranch_scc1 .Lok%=\n\t" \
  "v_mov_b64 v[192:193], 0\n\t" \
  "v_mov_b64 v[194:195], 0\n\t" \
  "v_mov_b64 v[196:197], 0\n\t" \
  "v_mov_b64 v[198:199], 0\n\t" \
  "s_add_u32 s92, s92, 1\n\t" \
  "s_cmp_eq_u32 s92, 1\n\t" \
  "s_cbranch_scc1 .Lpoll%=\n\t" \
  "s_sleep 2\n\t" \
  "s_and_b32 s93, s92, 63\n\t" \
  "s_cmp_lg_u32 s93, 0\n\t" \
  "s_cbranch_scc1 .Lpoll%=\n\t" \
  "v_mov_b32 v245, 0\n\t" \
  "buffer_load_dword v244, v245, %[rs], %[sab] offen sc1\n\t" \
  "s_waitcnt vmcnt(0)\n\t" \
  "v_readfirstlane_b32 s93, v244\n\t" \
  "s_nop 3\n\t" \
  "s_cmp_eq_u32 s93, 1\n\t" \
  "s_cbranch_scc1 .Lok%=\n\t" \
  "s_cmp_lt_u32 s92, 0x80000\n\t" \
  "s_cbranch_scc1 .Lpoll%=\n\t" \
  "v_mov_b32 v244, 1\n\t" \
  "buffer_store_dword v244, v245, %[rs], %[sab] offen sc1\n\t" \
  "s_branch .Lok%=\n\t" \
  ".Lend%=:\n\t"
#define QK_ASM_6 \
  "s_waitcnt lgkmcnt(0)\n\t" \
  "v_subrev_u32 v244, 0x8000, v250\n\t" \
  "ds_read_b128 v[216:219], v248 offset:57344\n\t" \
  "ds_read_b128 v[220:223], v248 offset:58368\n\t" \
  "ds_read_b128 v[224:227], v248 offset:59392\n\t" \
  "ds_read_b128 v[228:231], v248 offset:60416\n\t" \
  "ds_read_b128 v[208:211], v244 offset:0\n\t" \
  "ds_read_b128 v[212:215], v244 offset:1024\n\t" \
  "ds_read_b128 v[236:239], v244 offset:2048\n\t" \
  "ds_read_b128 v[240:243], v244 offset:3072\n\t" \
  "v_mov_b64 v[192:193], 0\n\t" \
  "v_mov_b64 v[194:195], 0\n\t" \
  "v_mov_b64 v[196:197], 0\n\t" \
  "v_mov_b64 v[198:199], 0\n\t" \
  "s_movk_i32 s92, 0x2000\n\t" \
  ".Lwt%=:\n\t" \
  "ds_read_b64 v[200:201], v248 offset:30720\n\t" \
  "s_memrealtime s[90:91]\n\t" \
  "s_sub_u32 s92, s92, 1\n\t" \
  "s_waitcnt lgkmcnt(0)\n\t" \
  "v_readfirstlane_b32 s93, v200\n\t" \
  "v_readfirstlane_b32 %[tgt], v201\n\t" \
  "s_cbranch_scc1 .Lgo%=\n\t" \
  "s_cmp_eq_u32 s93, %[step]\n\t" \
  "s_cbranch_scc1 .Lwx%=\n\t" \
  "s_sleep 1\n\t" \
  "s_branch .Lwt%=\n\t" \
  ".Lwx%=:\n\t" \
  "s_lshl_b32 s93, s90, 2\n\t" \
  "s_sub_i32 s93, %[tgt], s93\n\t" \
  "s_cmp_le_i32 s93, 0\n\t" \
  "s_cbranch_scc1 .Lgo%=\n\t" \
  "s_min_i32 s93, s93, 8000\n\t" \
  "s_mov_b32 s94, s93\n\t" \
  ".Lsl%=:\n\t" \
  "s_sub_i32 s94, s94, 18\n\t" \
  "s_cmp_lt_i32 s94, 30\n\t" \
  "s_cbranch_scc1 .Lsx%=\n\t" \
  "s_sleep 1\n\t" \
  "s_branch .Lsl%=\n\t" \
  ".Lsx%=:\n\t" \
  "s_sub_i32 s93, s93, 12\n\t" \
  "s_max_i32 s93, s93, 4\n\t" \
  "s_lshr_b32 s93, s93, 2\n\t" \
  "s_add_u32 s93, s93, s90\n\t" \
  "s_memrealtime s[90:91]\n\t" \
  ".Lpace%=:\n\t" \
  "s_memrealtime s[94:95]\n\t" \
  "s_waitcnt lgkmcnt(1)\n\t" \
  "s_sub_i32 s92, s90, s93\n\t" \
  "s_cmp_ge_i32 s92, 0\n\t" \
  "s_cbranch_scc1 .Le%=\n\t" \
  "s_memrealtime s[90:91]\n\t" \
  "s_waitcnt lgkmcnt(1)\n\t" \
  "s_sub_i32 s92, s94, s93\n\t" \
  "s_cmp_lt_i32 s92, 0\n\t" \
  "s_cbranch_scc1 .Lpace%=\n\t" \
  "s_nop 4\n\t" \
  ".Le%=:\n\t" \
  "s_add_u32 s92, s92, s93\n\t" \
  "s_lshl_b32 s92, s92, 2\n\t" \
  "s_sub_i32 s92, %[tgt], s92\n\t" \
  "s_max_i32 s92, s92, 0\n\t" \
  "s_bitcmp0_b32 s92, 3\n\t" \
  "s_cbranch_scc1 .Lf3%=\n\t" \
  "s_nop 15\n\t" \
  ".Lf3%=:\n\t" \
  "s_bitcmp0_b32 s92, 2\n\t" \
  "s_cbranch_scc1 .Lf2%=\n\t" \
  "s_nop 10\n\t" \
  ".Lf2%=:\n\t" \
  ".Lgo%=:\n\t" \
  "s_mov_b32 s92, 0\n\t" \
  ".Lpoll%=:\n\t" \
  "buffer_load_dwordx4 v[200:203], v248, %[rs], %[som] offen sc1\n\t" \
  "buffer_load_dwordx4 v[204:207], v248, %[rs], %[som2] offen sc1\n\t" \
  "s_waitcnt vmcnt(1) lgkmcnt(0)\n\t" \
  "ds_write_b128 v249, v[200:203]\n\t" \
  "v_smfmac_f32_16x16x64_bf16 v[192:195], v[200:203], v[208:215], v251\n\t" \
  "v_max3_u32 v244, v200, v201, v202\n\t" \
  "v_smfmac_f32_16x16x64_bf16 v[196:199], v[200:203], v[236:243], v251\n\t" \
  "s_waitcnt vmcnt(0)\n\t" \
  "ds_write_b128 v249, v[204:207] offset:8192\n\t" \
  "v_smfmac_f32_16x16x64_bf16 v[192:195], v[204:207], v[216:223], v251\n\t" \
  "v_max3_u32 v244, v244, v203, v204\n\t" \
  "v_max3_u32 v244, v244, v205, v206\n\t" \
  "v_smfmac_f32_16x16x64_bf16 v[196:199], v[204:207], v[224:231], v251\n\t" \
  "v_max_u32 v244, v244, v207\n\t" \
  "v_cmp_le_u32 vcc, %[sent], v244\n\t" \
  "s_cbranch_vccnz .Lslow%=\n\t" \
  ".Lok%=:\n\t" \
  "ds_read_b128 v[216:219], v250 offset:32768\n\t" \
  "ds_read_b128 v[220:223], v250 offset:33792\n\t" \
  "ds_read_b128 v[224:227], v250 offset:34816\n\t" \
  "ds_read_b128 v[228:231], v250 offset:35840\n\t" \
  "s_waitcnt lgkmcnt(4)\n\t" \
  "s_barrier\n\t" \
  "ds_read_b128 v[208:211], v248 offset:7168\n\t" \
  "ds_read_b128 v[212:215], v248 offset:8192\n\t" \
  "ds_read_b128 v[236:239], v248 offset:9216\n\t" \
  "ds_read_b128 v[240:243], v248 offset:10240\n\t" \
  "s_waitcnt lgkmcnt(3)\n\t" \
  "ds_read_b128 v[244:247], v248 offset:13312\n\t" \
  "v_smfmac_f32_16x16x64_bf16 v[192:195], v[208:211], v[216:223], v251\n\t" \
  "ds_read_b128 v[216:219], v250 offset:0\n\t" \
  "v_smfmac_f32_16x16x64_bf16 v[196:199], v[208:211], v[224:231], v251\n\t" \
  "s_waitcnt lgkmcnt(4)\n\t" \
  "ds_read_b128 v[220:223], v250 offset:1024\n\t" \
  "v_smfmac_f32_16x16x64_bf16 v[192:195], v[212:215], v[0:7], v251\n\t" \
  "ds_read_b128 v[224:227], v250 offset:2048\n\t" \
  "v_smfmac_f32_16x16x64_bf16 v[196:199], v[212:215], v[8:15], v251\n\t" \
  "s_waitcnt lgkmcnt(5)\n\t" \
  "ds_read_b128 v[228:231], v250 offset:3072\n\t" \
  "v_smfmac_f32_16x16x64_bf16 v[192:195], v[236:239], v[16:23], v251\n\t" \
  "ds_read_b128 v[200:203], v248 offset:11264\n\t" \
  "v_smfmac_f32_16x16x64_bf16 v[196:199], v[236:239], v[24:31], v251\n\t" \
  "s_waitcnt lgkmcnt(6)\n\t" \
  "ds_read_b128 v[204:207], v248 offset:12288\n\t" \
  "v_smfmac_f32_16x16x64_bf16 v[192:195], v[240:243], v[32:39], v251\n\t" \
  "ds_read_b128 v[208:211], v248 offset:15360\n\t" \
  "v_smfmac_f32_16x16x64_bf16 v[196:199], v[240:243], v[40:47], v251\n\t" \
  "s_waitcnt lgkmcnt(5)\n\t" \
  "ds_read_b128 v[212:215], v248 offset:0\n\t" \
  "v_smfmac_f32_16x16x64_bf16 v[192:195], v[244:247], v[216:223], v251\n\t" \
  "s_waitcnt lgkmcnt(4)\n\t" \
  "v_smfmac_f32_16x16x64_bf16 v[196:199], v[244:247], v[224:231], v251\n\t" \
  "s_waitcnt lgkmcnt(3)\n\t" \
  "ds_read_b128 v[236:239], v248 offset:1024\n\t" \
  "v_smfmac_f32_16x16x64_bf16 v[192:195], v[200:203], v[48:55], v251\n\t" \
  "ds_read_b128 v[240:243], v248 offset:2048\n\t" \
  "v_smfmac_f32_16x16x64_bf16 v[196:199], v[200:203], v[56:63], v251\n\t" \
  "s_waitcnt lgkmcnt(4)\n\t" \
  "v_smfmac_f32_16x16x64_bf16 v[192:195], v[204:207], v[64:71], v251\n\t" \
  "ds_read_b128 v[244:247], v248 offset:3072\n\t" \
  "v_smfmac_f32_16x16x64_bf16 v[196:199], v[204:207], v[72:79], v251\n\t" \
  "s_waitcnt lgkmcnt(4)\n\t" \
  "v_smfmac_f32_16x16x64_bf16 v[192:195], v[208:211], v[80:87], v251\n\t" \
  "ds_read_b128 v[200:203], v248 offset:4096\n\t" \
  "v_smfmac_f32_16x16x64_bf16 v[196:199], v[208:211], v[88:95], v251\n\t" \
  "s_waitcnt lgkmcnt(4)\n\t" \
  "v_smfmac_f32_16x16x64_bf16 v[192:195], v[212:215], v[96:103], v251\n\t" \
  "ds_read_b128 v[204:207], v248 offset:5120\n\t" \
  "v_smfmac_f32_16x16x64_bf16 v[196:199], v[212:215], v[104:111], v251\n\t" \
  "s_waitcnt lgkmcnt(3)\n\t" \
  "v_smfmac_f32_16x16x64_bf16 v[192:195], v[236:239], v[112:119], v251\n\t" \
  "v_smfmac_f32_16x16x64_bf16 v[196:199], v[236:239], v[120:127], v251\n\t" \
  "v_smfmac_f32_16x16x64_bf16 v[192:195], v[240:243], v[128:135], v251\n\t" \
  "v_smfmac_f32_16x16x64_bf16 v[196:199], v[240:243], v[136:143], v251\n\t" \
  "s_waitcnt lgkmcnt(1)\n\t" \
  "v_smfmac_f32_16x16x64_bf16 v[192:195], v[244:247], v[144:151], v251\n\t" \
  "v_smfmac_f32_16x16x64_bf16 v[196:199], v[244:247], v[152:159], v251\n\t" \
  "v_smfmac_f32_16x16x64_bf16 v[192:195], v[200:203], v[160:167], v251\n\t" \
  "v_smfmac_f32_16x16x64_bf16 v[196:199], v[200:203], v[168:175], v251\n\t" \
  "s_waitcnt lgkmcnt(0)\n\t" \
  "v_smfmac_f32_16x16x64_bf16 v[192:195], v[204:207], v[176:183], v251\n\t" \
  "v_smfmac_f32_16x16x64_bf16 v[196:199], v[204:207], v[184:191], v251\n\t" \
  "s_nop 6\n\t" \
  "v_add_f32 v192, v192, v194\n\t" \
  "v_add_f32 v193, v193, v195\n\t" \
  "v_add_f32 v196, v196, v198\n\t" \
  "v_add_f32 v197, v197, v199\n\t" \
  "v_cvt_pk_bf16_f32 v192, v192, v193\n\t" \
  "v_cvt_pk_bf16_f32 v196, v196, v197\n\t" \
  "ds_write2_b32 v252, v192, v196 offset1:64\n\t" \
  "s_waitcnt lgkmcnt(0)\n\t" \
  "s_barrier\n\t" \
  "buffer_store_dword v192, v235, %[rs], %[sk] offen sc1\n\t" \
  "buffer_store_dword v196, v235, %[rs], %[sk] offen offset:256 sc1\n\t" \
  "s_mov_b32 %[spins], s92\n\t" \
  "s_branch .Lend%=\n\t" \
  ".Lslow%=:\n\t" \
  "s_mov_b64 s[94:95], 0\n\t" \
  "v_cmp_eq_u32 vcc, %[sent], v200\n\t" \
  "s_or_b64 s[94:95], s[94:95], vcc\n\t" \
  "v_cmp_eq_u32 vcc, %[sent], v201\n\t" \
  "s_or_b64 s[94:95], s[94:95], vcc\n\t" \
  "v_cmp_eq_u32 vcc, %[sent], v202\n\t" \
  "s_or_b64 s[94:95], s[94:95], vcc\n\t" \
  "v_cmp_eq_u32 vcc, %[sent], v203\n\t" \
  "s_or_b64 s[94:95], s[94:95], vcc\n\t" \
  "v_cmp_eq_u32 vcc, %[sent], v204\n\t" \
  "s_or_b64 s[94:95], s[94:95], vcc\n\t" \
  "v_cmp_eq_u32 vcc, %[sent], v205\n\t" \
  "s_or_b64 s[94:95], s[94:95], vcc\n\t" \
  "v_cmp_eq_u32 vcc, %[sent], v206\n\t" \
  "s_or_b64 s[94:95], s[94:95], vcc\n\t" \
  "v_cmp_eq_u32 vcc, %[sent], v207\n\t" \
  "s_or_b64 s[94:95], s[94:95], vcc\n\t" \
  "s_cmp_eq_u64 s[94:95], 0\n\t" \
  "s_cbranch_scc1 .Lok%=\n\t" \
  "v_mov_b64 v[192:193], 0\n\t" \
  "v_mov_b64 v[194:195], 0\n\t" \
  "v_mov_b64 v[196:197], 0\n\t" \
  "v_mov_b64 v[198:199], 0\n\t" \
  "s_add_u32 s92, s92, 1\n\t" \
  "s_cmp_eq_u32 s92, 1\n\t" \
  "s_cbranch_scc1 .Lpoll%=\n\t" \
  "s_sleep 2\n\t" \
  "s_and_b32 s93, s92, 63\n\t" \
  "s_cmp_lg_u32 s93, 0\n\t" \
  "s_cbranch_scc1 .Lpoll%=\n\t" \
  "v_mov_b32 v245, 0\n\t" \
  "buffer_load_dword v244, v245, %[rs], %[sab] offen sc1\n\t" \
  "s_waitcnt vmcnt(0)\n\t" \
  "v_readfirstlane_b32 s93, v244\n\t" \
  "s_nop 3\n\t" \
  "s_cmp_eq_u32 s93, 1\n\t" \
  "s_cbranch_scc1 .Lok%=\n\t" \
  "s_cmp_lt_u32 s92, 0x80000\n\t" \
  "s_cbranch_scc1 .Lpoll%=\n\t" \
  "v_mov_b32 v244, 1\n\t" \
  "buffer_store_dword v244, v245, %[rs], %[sab] offen sc1\n\t" \
  "s_branch .Lok%=\n\t" \
  ".Lend%=:\n\t"
#define QK_ASM_7 \
  "s_waitcnt lgkmcnt(0)\n\t" \
  "v_subrev_u32 v244, 0x8000, v250\n\t" \
  "ds_read_b128 v[216:219], v248 offset:61440\n\t" \
  "ds_read_b128 v[220:223], v248 offset:62464\n\t" \
  "ds_read_b128 v[224:227], v248 offset:63488\n\t" \
  "ds_read_b128 v[228:231], v248 offset:64512\n\t" \
  "ds_read_b128 v[208:211], v244 offset:0\n\t" \
  "ds_read_b128 v[212:215], v244 offset:1024\n\t" \
  "ds_read_b128 v[236:239], v244 offset:2048\n\t" \
  "ds_read_b128 v[240:243], v244 offset:3072\n\t" \
  "v_mov_b64 v[192:193], 0\n\t" \
  "v_mov_b64 v[194:195], 0\n\t" \
  "v_mov_b64 v[196:197], 0\n\t" \
  "v_mov_b64 v[198:199], 0\n\t" \
  "s_movk_i32 s92, 0x2000\n\t" \
  ".Lwt%=:\n\t" \
  "ds_read_b64 v[200:201], v248 offset:30720\n\t" \
  "s_memrealtime s[90:91]\n\t" \
  "s_sub_u32 s92, s92, 1\n\t" \
  "s_waitcnt lgkmcnt(0)\n\t" \
  "v_readfirstlane_b32 s93, v200\n\t" \
  "v_readfirstlane_b32 %[tgt], v201\n\t" \
  "s_cbranch_scc1 .Lgo%=\n\t" \
  "s_cmp_eq_u32 s93, %[step]\n\t" \
  "s_cbranch_scc1 .Lwx%=\n\t" \
  "s_sleep 1\n\t" \
  "s_branch .Lwt%=\n\t" \
  ".Lwx%=:\n\t" \
  "s_lshl_b32 s93, s90, 2\n\t" \
  "s_sub_i32 s93, %[tgt], s93\n\t" \
  "s_cmp_le_i32 s93, 0\n\t" \
  "s_cbranch_scc1 .Lgo%=\n\t" \
  "s_min_i32 s93, s93, 8000\n\t" \
  "s_mov_b32 s94, s93\n\t" \
  ".Lsl%=:\n\t" \
  "s_sub_i32 s94, s94, 18\n\t" \
  "s_cmp_lt_i32 s94, 30\n\t" \
  "s_cbranch_scc1 .Lsx%=\n\t" \
  "s_sleep 1\n\t" \
  "s_branch .Lsl%=\n\t" \
  ".Lsx%=:\n\t" \
  "s_sub_i32 s93, s93, 12\n\t" \
  "s_max_i32 s93, s93, 4\n\t" \
  "s_lshr_b32 s93, s93, 2\n\t" \
  "s_add_u32 s93, s93, s90\n\t" \
  "s_memrealtime s[90:91]\n\t" \
  ".Lpace%=:\n\t" \
  "s_memrealtime s[94:95]\n\t" \
  "s_waitcnt lgkmcnt(1)\n\t" \
  "s_sub_i32 s92, s90, s93\n\t" \
  "s_cmp_ge_i32 s92, 0\n\t" \
  "s_cbranch_scc1 .Le%=\n\t" \
  "s_memrealtime s[90:91]\n\t" \
  "s_waitcnt lgkmcnt(1)\n\t" \
  "s_sub_i32 s92, s94, s93\n\t" \
  "s_cmp_lt_i32 s92, 0\n\t" \
  "s_cbranch_scc1 .Lpace%=\n\t" \
  "s_nop 4\n\t" \
  ".Le%=:\n\t" \
  "s_add_u32 s92, s92, s93\n\t" \
  "s_lshl_b32 s92, s92, 2\n\t" \
  "s_sub_i32 s92, %[tgt], s92\n\t" \
  "s_max_i32 s92, s92, 0\n\t" \
  "s_bitcmp0_b32 s92, 3\n\t" \
  "s_cbranch_scc1 .Lf3%=\n\t" \
  "s_nop 15\n\t" \
  ".Lf3%=:\n\t" \
  "s_bitcmp0_b32 s92, 2\n\t" \
  "s_cbranch_scc1 .Lf2%=\n\t" \
  "s_nop 10\n\t" \
  ".Lf2%=:\n\t" \
  ".Lgo%=:\n\t" \
  "s_mov_b32 s92, 0\n\t" \
  "s_memtime s[88:89]\n\t" \
  ".Lpoll%=:\n\t" \
  "buffer_load_dwordx4 v[200:203], v248, %[rs], %[som] offen sc1\n\t" \
  "buffer_load_dwordx4 v[204:207], v248, %[rs], %[som2] offen sc1\n\t" \
  "s_waitcnt vmcnt(1) lgkmcnt(0)\n\t" \
  "ds_write_b128 v249, v[200:203]\n\t" \
  "v_smfmac_f32_16x16x64_bf16 v[192:195], v[200:203], v[208:215], v251\n\t" \
  "v_max3_u32 v244, v200, v201, v202\n\t" \
  "v_smfmac_f32_16x16x64_bf16 v[196:199], v[200:203], v[236:243], v251\n\t" \
  "s_waitcnt vmcnt(0)\n\t" \
  "ds_write_b128 v249, v[204:207] offset:8192\n\t" \
  "v_smfmac_f32_16x16x64_bf16 v[192:195], v[204:207], v[216:223], v251\n\t" \
  "v_max3_u32 v244, v244, v203, v204\n\t" \
  "v_max3_u32 v244, v244, v205, v206\n\t" \
  "v_smfmac_f32_16x16x64_bf16 v[196:199], v[204:207], v[224:231], v251\n\t" \
  "v_max_u32 v244, v244, v207\n\t" \
  "v_cmp_le_u32 vcc, %[sent], v244\n\t" \
  "s_cbranch_vccnz .Lslow%=\n\t" \
  ".Lok%=:\n\t" \
  "ds_read_b128 v[216:219], v250 offset:32768\n\t" \
  "ds_read_b128 v[220:223], v250 offset:33792\n\t" \
  "ds_read_b128 v[224:227], v250 offset:34816\n\t" \
  "ds_read_b128 v[228:231], v250 offset:35840\n\t" \
  "s_waitcnt lgkmcnt(4)\n\t" \
  "s_setprio 3\n\t" \
  "s_barrier\n\t" \
  "s_memrealtime s[90:91]\n\t" \
  "s_memtime s[94:95]\n\t" \
  "v_mov_b32 v232, %[step]\n\t" \
  "v_lshrrev_b32 v234, 1, v248\n\t" \
  "s_mov_b64 exec, 0xffff\n\t" \
  "s_waitcnt lgkmcnt(0)\n\t" \
  "s_lshl_b32 s90, s90, 2\n\t" \
  "s_sub_u32 s94, s94, s88\n\t" \
  "s_mul_i32 s94, s94, 12200\n\t" \
  "s_lshr_b32 s94, s94, 16\n\t" \
  "s_add_u32 s94, s94, %[tgt]\n\t" \
  "s_sub_i32 s93, s90, s94\n\t" \
  "s_sub_i32 s93, s93, 30\n\t" \
  "s_max_i32 s93, s93, 0\n\t" \
  "s_add_u32 s90, s94, s93\n\t" \
  "s_sub_i32 s93, s90, %[tgt]\n\t" \
  "s_cmp_gt_i32 s93, 270\n\t" \
  "s_cselect_b32 s93, 70, 0\n\t" \
  "s_add_u32 s93, s93, s90\n\t" \
  "s_add_u32 s93, s93, %[lead]\n\t" \
  "v_mov_b32 v233, s93\n\t" \
  "buffer_store_dwordx2 v[232:233], v234, %[rs], %[sbc] offen sc1\n\t" \
  "s_mov_b64 exec, -1\n\t" \
  "s_mov_b32 %[tb1], s90\n\t" \
  "s_setprio 0\n\t" \
  "ds_read_b128 v[208:211], v248 offset:6144\n\t" \
  "ds_read_b128 v[212:215], v248 offset:9216\n\t" \
  "ds_read_b128 v[236:239], v248 offset:10240\n\t" \
  "ds_read_b128 v[240:243], v248 offset:11264\n\t" \
  "s_waitcnt lgkmcnt(3)\n\t" \
  "ds_read_b128 v[244:247], v248 offset:12288\n\t" \
  "v_smfmac_f32_16x16x64_bf16 v[192:195], v[208:211], v[216:223], v251\n\t" \
  "ds_read_b128 v[216:219], v250 offset:0\n\t" \
  "v_smfmac_f32_16x16x64_bf16 v[196:199], v[208:211], v[224:231], v251\n\t" \
  "s_waitcnt lgkmcnt(4)\n\t" \
  "ds_read_b128 v[220:223], v250 offset:1024\n\t" \
  "v_smfmac_f32_16x16x64_bf16 v[192:195], v[212:215], v[0:7], v251\n\t" \
  "ds_read_b128 v[224:227], v250 offset:2048\n\t" \
  "v_smfmac_f32_16x16x64_bf16 v[196:199], v[212:215], v[8:15], v251\n\t" \
  "s_waitcnt lgkmcnt(5)\n\t" \
  "ds_read_b128 v[228:231], v250 offset:3072\n\t" \
  "v_smfmac_f32_16x16x64_bf16 v[192:195], v[236:239], v[16:23], v251\n\t" \
  "ds_read_b128 v[200:203], v248 offset:13312\n\t" \
  "v_smfmac_f32_16x16x64_bf16 v[196:199], v[236:239], v[24:31], v251\n\t" \
  "s_waitcnt lgkmcnt(6)\n\t" \
  "ds_read_b128 v[204:207], v248 offset:14336\n\t" \
  "v_smfmac_f32_16x16x64_bf16 v[192:195], v[240:243], v[32:39], v251\n\t" \
  "ds_read_b128 v[208:211], v248 offset:0\n\t" \
  "v_smfmac_f32_16x16x64_bf16 v[196:199], v[240:243], v[40:47], v251\n\t" \
  "s_waitcnt lgkmcnt(5)\n\t" \
  "ds_read_b128 v[212:215], v248 offset:1024\n\t" \
  "v_smfmac_f32_16x16x64_bf16 v[192:195], v[244:247], v[216:223], v251\n\t" \
  "s_waitcnt lgkmcnt(4)\n\t" \
  "v_smfmac_f32_16x16x64_bf16 v[196:199], v[244:247], v[224:231], v251\n\t" \
  "s_waitcnt lgkmcnt(3)\n\t" \
  "ds_read_b128 v[236:239], v248 offset:2048\n\t" \
  "v_smfmac_f32_16x16x64_bf16 v[192:195], v[200:203], v[48:55], v251\n\t" \
  "ds_read_b128 v[240:243], v248 offset:3072\n\t" \
  "v_smfmac_f32_16x16x64_bf16 v[196:199], v[200:203], v[56:63], v251\n\t" \
  "s_waitcnt lgkmcnt(4)\n\t" \
  "v_smfmac_f32_16x16x64_bf16 v[192:195], v[204:207], v[64:71], v251\n\t" \
  "ds_read_b128 v[244:247], v248 offset:4096\n\t" \
  "v_smfmac_f32_16x16x64_bf16 v[196:199], v[204:207], v[72:79], v251\n\t" \
  "s_waitcnt lgkmcnt(4)\n\t" \
  "v_smfmac_f32_16x16x64_bf16 v[192:195], v[208:211], v[80:87], v251\n\t" \
  "ds_read_b128 v[200:203], v248 offset:5120\n\t" \
  "v_smfmac_f32_16x16x64_bf16 v[196:199], v[208:211], v[88:95], v251\n\t" \
  "s_waitcnt lgkmcnt(4)\n\t" \
  "v_smfmac_f32_16x16x64_bf16 v[192:195], v[212:215], v[96:103], v251\n\t" \
  "ds_read_b128 v[204:207], v248 offset:8192\n\t" \
  "v_smfmac_f32_16x16x64_bf16 v[196:199], v[212:215], v[104:111], v251\n\t" \
  "s_waitcnt lgkmcnt(3)\n\t" \
  "v_smfmac_f32_16x16x64_bf16 v[192:195], v[236:239], v[112:119], v251\n\t" \
  "v_smfmac_f32_16x16x64_bf16 v[196:199], v[236:239], v[120:127], v251\n\t" \
  "v_smfmac_f32_16x16x64_bf16 v[192:195], v[240:243], v[128:135], v251\n\t" \
  "v_smfmac_f32_16x16x64_bf16 v[196:199], v[240:243], v[136:143], v251\n\t" \
  "s_waitcnt lgkmcnt(1)\n\t" \
  "v_smfmac_f32_16x16x64_bf16 v[192:195], v[244:247], v[144:151], v251\n\t" \
  "v_smfmac_f32_16x16x64_bf16 v[196:199], v[244:247], v[152:159], v251\n\t" \
  "v_smfmac_f32_16x16x64_bf16 v[192:195], v[200:203], v[160:167], v251\n\t" \
  "v_smfmac_f32_16x16x64_bf16 v[196:199], v[200:203], v[168:175], v251\n\t" \
  "s_waitcnt lgkmcnt(0)\n\t" \
  "v_smfmac_f32_16x16x64_bf16 v[192:195], v[204:207], v[176:183], v251\n\t" \
  "v_smfmac_f32_16x16x64_bf16 v[196:199], v[204:207], v[184:191], v251\n\t" \
  "s_nop 6\n\t" \
  "v_add_f32 v192, v192, v194\n\t" \
  "v_add_f32 v193, v193, v195\n\t" \
  "v_add_f32 v196, v196, v198\n\t" \
  "v_add_f32 v197, v197, v199\n\t" \
  "v_cvt_pk_bf16_f32 v192, v192, v193\n\t" \
  "v_cvt_pk_bf16_f32 v196, v196, v197\n\t" \
  "ds_write2_b32 v252, v192, v196 offset1:64\n\t" \
  "s_waitcnt lgkmcnt(0)\n\t" \
  "s_barrier\n\t" \
  "buffer_store_dword v192, v235, %[rs], %[sk] offen sc1\n\t" \
  "buffer_store_dword v196, v235, %[rs], %[sk] offen offset:256 sc1\n\t" \
  "s_mov_b32 %[spins], s92\n\t" \
  "s_branch .Lend%=\n\t" \
  ".Lslow%=:\n\t" \
  "s_mov_b64 s[94:95], 0\n\t" \
  "v_cmp_eq_u32 vcc, %[sent], v200\n\t" \
  "s_or_b64 s[94:95], s[94:95], vcc\n\t" \
  "v_cmp_eq_u32 vcc, %[sent], v201\n\t" \
  "s_or_b64 s[94:95], s[94:95], vcc\n\t" \
  "v_cmp_eq_u32 vcc, %[sent], v202\n\t" \
  "s_or_b64 s[94:95], s[94:95], vcc\n\t" \
  "v_cmp_eq_u32 vcc, %[sent], v203\n\t" \
  "s_or_b64 s[94:95], s[94:95], vcc\n\t" \
  "v_cmp_eq_u32 vcc, %[sent], v204\n\t" \
  "s_or_b64 s[94:95], s[94:95], vcc\n\t" \
  "v_cmp_eq_u32 vcc, %[sent], v205\n\t" \
  "s_or_b64 s[94:95], s[94:95], vcc\n\t" \
  "v_cmp_eq_u32 vcc, %[sent], v206\n\t" \
  "s_or_b64 s[94:95], s[94:95], vcc\n\t" \
  "v_cmp_eq_u32 vcc, %[sent], v207\n\t" \
  "s_or_b64 s[94:95], s[94:95], vcc\n\t" \
  "s_cmp_eq_u64 s[94:95], 0\n\t" \
  "s_cbranch_scc1 .Lok%=\n\t" \
  "v_mov_b64 v[192:193], 0\n\t" \
  "v_mov_b64 v[194:195], 0\n\t" \
  "v_mov_b64 v[196:197], 0\n\t" \
  "v_mov_b64 v[198:199], 0\n\t" \
  "s_add_u32 s92, s92, 1\n\t" \
  "s_cmp_eq_u32 s92, 1\n\t" \
  "s_cbranch_scc1 .Lpoll%=\n\t" \
  "s_sleep 2\n\t" \
  "s_and_b32 s93, s92, 63\n\t" \
  "s_cmp_lg_u32 s93, 0\n\t" \
  "s_cbranch_scc1 .Lpoll%=\n\t" \
  "v_mov_b32 v245, 0\n\t" \
  "buffer_load_dword v244, v245, %[rs], %[sab] offen sc1\n\t" \
  "s_waitcnt vmcnt(0)\n\t" \
  "v_readfirstlane_b32 s93, v244\n\t" \
  "s_nop 3\n\t" \
  "s_cmp_eq_u32 s93, 1\n\t" \
  "s_cbranch_scc1 .Lok%=\n\t" \
  "s_cmp_lt_u32 s92, 0x80000\n\t" \
  "s_cbranch_scc1 .Lpoll%=\n\t" \
  "v_mov_b32 v244, 1\n\t" \
  "buffer_store_dword v244, v245, %[rs], %[sab] offen sc1\n\t" \
  "s_branch .Lok%=\n\t" \
  ".Lend%=:\n\t"
#define VV_ASM \
  "s_waitcnt lgkmcnt(0)\n\t" \
  "v_mov_b64 v[128:129], 0\n\t" \
  "v_mov_b64 v[130:131], 0\n\t" \
  "s_movk_i32 s92, 0x2000\n\t" \
  ".Lwt%=:\n\t" \
  "ds_read_b64 v[132:133], v200 offset:30720\n\t" \
  "s_memrealtime s[90:91]\n\t" \
  "s_sub_u32 s92, s92, 1\n\t" \
  "s_waitcnt lgkmcnt(0)\n\t" \
  "v_readfirstlane_b32 s93, v132\n\t" \
  "v_readfirstlane_b32 %[tgt], v133\n\t" \
  "s_cbranch_scc1 .Lgo%=\n\t" \
  "s_cmp_eq_u32 s93, %[step]\n\t" \
  "s_cbranch_scc1 .Lwx%=\n\t" \
  "s_sleep 1\n\t" \
  "s_branch .Lwt%=\n\t" \
  ".Lwx%=:\n\t" \
  "s_lshl_b32 s93, s90, 2\n\t" \
  "s_sub_i32 s93, %[tgt], s93\n\t" \
  "s_cmp_le_i32 s93, 0\n\t" \
  "s_cbranch_scc1 .Lgo%=\n\t" \
  "s_min_i32 s93, s93, 8000\n\t" \
  "s_mov_b32 s94, s93\n\t" \
  ".Lsl%=:\n\t" \
  "s_sub_i32 s94, s94, 18\n\t" \
  "s_cmp_lt_i32 s94, 30\n\t" \
  "s_cbranch_scc1 .Lsx%=\n\t" \
  "s_sleep 1\n\t" \
  "s_branch .Lsl%=\n\t" \
  ".Lsx%=:\n\t" \
  "s_sub_i32 s93, s93, 12\n\t" \
  "s_max_i32 s93, s93, 4\n\t" \
  "s_lshr_b32 s93, s93, 2\n\t" \
  "s_add_u32 s93, s93, s90\n\t" \
  "s_memrealtime s[90:91]\n\t" \
  ".Lpace%=:\n\t" \
  "s_memrealtime s[94:95]\n\t" \
  "s_waitcnt lgkmcnt(1)\n\t" \
  "s_sub_i32 s92, s90, s93\n\t" \
  "s_cmp_ge_i32 s92, 0\n\t" \
  "s_cbranch_scc1 .Le%=\n\t" \
  "s_memrealtime s[90:91]\n\t" \
  "s_waitcnt lgkmcnt(1)\n\t" \
  "s_sub_i32 s92, s94, s93\n\t" \
  "s_cmp_lt_i32 s92, 0\n\t" \
  "s_cbranch_scc1 .Lpace%=\n\t" \
  "s_nop 4\n\t" \
  ".Le%=:\n\t" \
  "s_add_u32 s92, s92, s93\n\t" \
  "s_lshl_b32 s92, s92, 2\n\t" \
  "s_sub_i32 s92, %[tgt], s92\n\t" \
  "s_max_i32 s92, s92, 0\n\t" \
  "s_bitcmp0_b32 s92, 3\n\t" \
  "s_cbranch_scc1 .Lf3%=\n\t" \
  "s_nop 15\n\t" \
  ".Lf3%=:\n\t" \
  "s_bitcmp0_b32 s92, 2\n\t" \
  "s_cbranch_scc1 .Lf2%=\n\t" \
  "s_nop 10\n\t" \
  ".Lf2%=:\n\t" \
  ".Lgo%=:\n\t" \
  "s_mov_b32 s92, 0\n\t" \
  ".Lpoll%=:\n\t" \
  "buffer_load_dwordx4 v[132:135], v200, %[rs], %[som] offen sc1\n\t" \
  "buffer_load_dwordx4 v[136:139], v200, %[rs], %[som2] offen sc1\n\t" \
  "s_waitcnt vmcnt(1) lgkmcnt(0)\n\t" \
  "ds_write_b128 v232, v[132:135]\n\t" \
  "v_smfmac_f32_16x16x64_bf16 v[128:131], v[132:135], v[0:7], v202\n\t" \
  "v_max3_u32 v196, v132, v133, v134\n\t" \
  "s_waitcnt vmcnt(0)\n\t" \
  "ds_write_b128 v232, v[136:139] offset:8192\n\t" \
  "v_smfmac_f32_16x16x64_bf16 v[128:131], v[136:139], v[64:71], v202\n\t" \
  "v_max3_u32 v196, v196, v135, v136\n\t" \
  "v_max3_u32 v196, v196, v137, v138\n\t" \
  "v_max_u32 v196, v196, v139\n\t" \
  "v_cmp_le_u32 vcc, %[sent], v196\n\t" \
  "s_cbranch_vccnz .Lslow%=\n\t" \
  ".Lok%=:\n\t" \
  "s_waitcnt lgkmcnt(0)\n\t" \
  "s_barrier\n\t" \
  "ds_read_b128 v[164:167], v233 offset:0\n\t" \
  "ds_read_b128 v[140:143], v234 offset:0\n\t" \
  "ds_read_b128 v[144:147], v235 offset:0\n\t" \
  "ds_read_b128 v[148:151], v236 offset:0\n\t" \
  "ds_read_b128 v[152:155], v237 offset:0\n\t" \
  "s_waitcnt lgkmcnt(4)\n\t" \
  "ds_read_b128 v[156:159], v238 offset:0\n\t" \
  "v_smfmac_f32_16x16x64_bf16 v[128:131], v[164:167], v[8:15], v202\n\t" \
  "s_waitcnt lgkmcnt(4)\n\t" \
  "ds_read_b128 v[160:163], v239 offset:0\n\t" \
  "v_smfmac_f32_16x16x64_bf16 v[128:131], v[140:143], v[16:23], v202\n\t" \
  "s_waitcnt lgkmcnt(4)\n\t" \
  "ds_read_b128 v[168:171], v233 offset:8192\n\t" \
  "v_smfmac_f32_16x16x64_bf16 v[128:131], v[144:147], v[24:31], v202\n\t" \
  "s_waitcnt lgkmcnt(4)\n\t" \
  "ds_read_b128 v[172:175], v234 offset:8192\n\t" \
  "v_smfmac_f32_16x16x64_bf16 v[128:131], v[148:151], v[32:39], v202\n\t" \
  "s_waitcnt lgkmcnt(4)\n\t" \
  "ds_read_b128 v[176:179], v235 offset:8192\n\t" \
  "v_smfmac_f32_16x16x64_bf16 v[128:131], v[152:155], v[40:47], v202\n\t" \
  "s_waitcnt lgkmcnt(4)\n\t" \
  "ds_read_b128 v[180:183], v236 offset:8192\n\t" \
  "v_smfmac_f32_16x16x64_bf16 v[128:131], v[156:159], v[48:55], v202\n\t" \
  "s_waitcnt lgkmcnt(4)\n\t" \
  "ds_read_b128 v[184:187], v237 offset:8192\n\t" \
  "v_smfmac_f32_16x16x64_bf16 v[128:131], v[160:163], v[56:63], v202\n\t" \
  "s_waitcnt lgkmcnt(4)\n\t" \
  "ds_read_b128 v[188:191], v238 offset:8192\n\t" \
  "v_smfmac_f32_16x16x64_bf16 v[128:131], v[168:171], v[72:79], v202\n\t" \
  "s_waitcnt lgkmcnt(4)\n\t" \
  "ds_read_b128 v[192:195], v239 offset:8192\n\t" \
  "v_smfmac_f32_16x16x64_bf16 v[128:131], v[172:175], v[80:87], v202\n\t" \
  "s_waitcnt lgkmcnt(4)\n\t" \
  "v_smfmac_f32_16x16x64_bf16 v[128:131], v[176:179], v[88:95], v202\n\t" \
  "s_waitcnt lgkmcnt(3)\n\t" \
  "v_smfmac_f32_16x16x64_bf16 v[128:131], v[180:183], v[96:103], v202\n\t" \
  "s_waitcnt lgkmcnt(2)\n\t" \
  "v_smfmac_f32_16x16x64_bf16 v[128:131], v[184:187], v[104:111], v202\n\t" \
  "s_waitcnt lgkmcnt(1)\n\t" \
  "v_smfmac_f32_16x16x64_bf16 v[128:131], v[188:191], v[112:119], v202\n\t" \
  "s_waitcnt lgkmcnt(0)\n\t" \
  "v_smfmac_f32_16x16x64_bf16 v[128:131], v[192:195], v[120:127], v202\n\t" \
  "s_nop 7\n\t" \
  "v_add_f32 v128, v128, v130\n\t" \
  "v_add_f32 v129, v129, v131\n\t" \
  "v_cvt_pk_bf16_f32 v128, v128, v129\n\t" \
  "buffer_store_dword v128, v203, %[rs], %[sv] offen sc1\n\t" \
  "ds_write_b32 v204, v128\n\t" \
  "s_waitcnt lgkmcnt(0)\n\t" \
  "s_barrier\n\t" \
  "s_mov_b32 %[spins], s92\n\t" \
  "s_branch .Lend%=\n\t" \
  ".Lslow%=:\n\t" \
  "s_mov_b64 s[94:95], 0\n\t" \
  "v_cmp_eq_u32 vcc, %[sent], v132\n\t" \
  "s_or_b64 s[94:95], s[94:95], vcc\n\t" \
  "v_cmp_eq_u32 vcc, %[sent], v133\n\t" \
  "s_or_b64 s[94:95], s[94:95], vcc\n\t" \
  "v_cmp_eq_u32 vcc, %[sent], v134\n\t" \
  "s_or_b64 s[94:95], s[94:95], vcc\n\t" \
  "v_cmp_eq_u32 vcc, %[sent], v135\n\t" \
  "s_or_b64 s[94:95], s[94:95], vcc\n\t" \
  "v_cmp_eq_u32 vcc, %[sent], v136\n\t" \
  "s_or_b64 s[94:95], s[94:95], vcc\n\t" \
  "v_cmp_eq_u32 vcc, %[sent], v137\n\t" \
  "s_or_b64 s[94:95], s[94:95], vcc\n\t" \
  "v_cmp_eq_u32 vcc, %[sent], v138\n\t" \
  "s_or_b64 s[94:95], s[94:95], vcc\n\t" \
  "v_cmp_eq_u32 vcc, %[sent], v139\n\t" \
  "s_or_b64 s[94:95], s[94:95], vcc\n\t" \
  "s_cmp_eq_u64 s[94:95], 0\n\t" \
  "s_cbranch_scc1 .Lok%=\n\t" \
  "v_mov_b64 v[128:129], 0\n\t" \
  "v_mov_b64 v[130:131], 0\n\t" \
  "s_add_u32 s92, s92, 1\n\t" \
  "s_cmp_eq_u32 s92, 1\n\t" \
  "s_cbranch_scc1 .Lpoll%=\n\t" \
  "s_sleep 2\n\t" \
  "s_and_b32 s93, s92, 63\n\t" \
  "s_cmp_lg_u32 s93, 0\n\t" \
  "s_cbranch_scc1 .Lpoll%=\n\t" \
  "v_mov_b32 v197, 0\n\t" \
  "buffer_load_dword v196, v197, %[rs], %[sab] offen sc1\n\t" \
  "s_waitcnt vmcnt(0)\n\t" \
  "v_readfirstlane_b32 s93, v196\n\t" \
  "s_nop 3\n\t" \
  "s_cmp_eq_u32 s93, 1\n\t" \
  "s_cbranch_scc1 .Lok%=\n\t" \
  "s_cmp_lt_u32 s92, 0x80000\n\t" \
  "s_cbranch_scc1 .Lpoll%=\n\t" \
  "v_mov_b32 v196, 1\n\t" \
  "buffer_store_dword v196, v197, %[rs], %[sab] offen sc1\n\t" \
  "s_branch .Lok%=\n\t" \
  ".Lend%=:\n\t"
#define TK_ASM \
  "buffer_load_dwordx4 v[224:227], v200, %[rs], %[sk] offen offset:0 sc1\n\t" \
  "buffer_load_dwordx4 v[228:231], v200, %[rs], %[sk] offen offset:1024 sc1\n\t" \
  "s_memrealtime s[88:89]\n\t" \
  "v_add_u32 v205, %[tl], v200\n\t" \
  "v_add_u32 v206, %[tq], v205\n\t" \
  "v_add_u32 v207, %[tv], v205\n\t" \
  "s_waitcnt lgkmcnt(0)\n\t" \
  "ds_read_b128 v[128:131], v206 offset:8192\n\t" \
  "ds_read_b128 v[144:147], v205 offset:0\n\t" \
  "ds_read_b128 v[160:163], v205 offset:4096\n\t" \
  "ds_read_b128 v[132:135], v206 offset:9216\n\t" \
  "ds_read_b128 v[148:151], v205 offset:1024\n\t" \
  "ds_read_b128 v[164:167], v205 offset:5120\n\t" \
  "ds_read_b128 v[136:139], v206 offset:10240\n\t" \
  "ds_read_b128 v[152:155], v205 offset:2048\n\t" \
  "ds_read_b128 v[168:171], v205 offset:6144\n\t" \
  "ds_read_b128 v[140:143], v206 offset:11264\n\t" \
  "ds_read_b128 v[156:159], v205 offset:3072\n\t" \
  "ds_read_b128 v[172:175], v205 offset:7168\n\t" \
  "v_add_u32 v206, %[vp], v218\n\t" \
  "s_lshl_b32 s88, s88, 2\n\t" \
  "s_add_u32 s89, s88, %[od]\n\t" \
  "s_waitcnt lgkmcnt(10)\n\t" \
  "v_mfma_f32_16x16x32_bf16 v[196:199], v[144:147], v[128:131], 0\n\t" \
  "s_waitcnt lgkmcnt(9)\n\t" \
  "v_mfma_f32_16x16x32_bf16 v[144:147], v[160:163], v[128:131], 0\n\t" \
  "s_waitcnt lgkmcnt(7)\n\t" \
  "v_mfma_f32_16x16x32_bf16 v[196:199], v[148:151], v[132:135], v[196:199]\n\t" \
  "s_waitcnt lgkmcnt(6)\n\t" \
  "v_mfma_f32_16x16x32_bf16 v[144:147], v[164:167], v[132:135], v[144:147]\n\t" \
  "s_waitcnt lgkmcnt(4)\n\t" \
  "v_mfma_f32_16x16x32_bf16 v[196:199], v[152:155], v[136:139], v[196:199]\n\t" \
  "s_waitcnt lgkmcnt(3)\n\t" \
  "v_mfma_f32_16x16x32_bf16 v[144:147], v[168:171], v[136:139], v[144:147]\n\t" \
  "s_waitcnt lgkmcnt(1)\n\t" \
  "v_mfma_f32_16x16x32_bf16 v[196:199], v[156:159], v[140:143], v[196:199]\n\t" \
  "s_waitcnt lgkmcnt(0)\n\t" \
  "v_mfma_f32_16x16x32_bf16 v[144:147], v[172:175], v[140:143], v[144:147]\n\t" \
  "ds_read_b128 v[176:179], v207 offset:16384\n\t" \
  "ds_read_b128 v[180:183], v207 offset:17408\n\t" \
  "ds_read_b128 v[184:187], v207 offset:18432\n\t" \
  "ds_read_b128 v[188:191], v207 offset:19456\n\t" \
  "ds_read2st64_b32 v[192:193], v206 offset0:0 offset1:1\n\t" \
  "ds_read2st64_b32 v[194:195], v206 offset0:2 offset1:3\n\t" \
  "v_cvt_pk_bf16_f32 v197, v196, v197\n\t" \
  "v_cvt_pk_bf16_f32 v199, v198, v199\n\t" \
  "v_cvt_pk_bf16_f32 v145, v144, v145\n\t" \
  "v_cvt_pk_bf16_f32 v147, v146, v147\n\t" \
  "v_lshlrev_b32 v196, 16, v197\n\t" \
  "v_lshlrev_b32 v198, 16, v199\n\t" \
  "v_lshlrev_b32 v144, 16, v145\n\t" \
  "v_lshlrev_b32 v146, 16, v147\n\t" \
  "v_and_b32 v197, 0xffff0000, v197\n\t" \
  "v_and_b32 v199, 0xffff0000, v199\n\t" \
  "v_and_b32 v145, 0xffff0000, v145\n\t" \
  "v_and_b32 v147, 0xffff0000, v147\n\t" \
  "v_pk_mul_f32 v[196:197], v[196:197], %[sc] op_sel_hi:[1,0]\n\t" \
  "v_pk_mul_f32 v[198:199], v[198:199], %[sc] op_sel_hi:[1,0]\n\t" \
  "v_pk_mul_f32 v[144:145], v[144:145], %[sc] op_sel_hi:[1,0]\n\t" \
  "v_pk_mul_f32 v[146:147], v[146:147], %[sc] op_sel_hi:[1,0]\n\t" \
  "v_cvt_pk_bf16_f32 v197, v196, v197\n\t" \
  "v_cvt_pk_bf16_f32 v199, v198, v199\n\t" \
  "v_cvt_pk_bf16_f32 v145, v144, v145\n\t" \
  "v_cvt_pk_bf16_f32 v147, v146, v147\n\t" \
  "v_lshlrev_b32 v196, 16, v197\n\t" \
  "v_lshlrev_b32 v198, 16, v199\n\t" \
  "v_lshlrev_b32 v144, 16, v145\n\t" \
  "v_lshlrev_b32 v146, 16, v147\n\t" \
  "v_and_b32 v197, 0xffff0000, v197\n\t" \
  "v_and_b32 v199, 0xffff0000, v199\n\t" \
  "v_and_b32 v145, 0xffff0000, v145\n\t" \
  "v_and_b32 v147, 0xffff0000, v147\n\t" \
  "v_min_f32 v196, v196, v208\n\t" \
  "v_min_f32 v197, v197, v209\n\t" \
  "v_min_f32 v198, v198, v210\n\t" \
  "v_min_f32 v199, v199, v211\n\t" \
  "v_min_f32 v144, v144, v212\n\t" \
  "v_min_f32 v145, v145, v213\n\t" \
  "v_min_f32 v146, v146, v214\n\t" \
  "v_min_f32 v147, v147, v215\n\t" \
  "v_max3_f32 v148, v196, v197, v198\n\t" \
  "v_max3_f32 v149, v199, v144, v145\n\t" \
  "v_max3_f32 v148, v148, v146, v147\n\t" \
  "v_max_f32 v150, v148, v149\n\t" \
  "v_max_f32 v151, v148, v149\n\t" \
  "s_nop 1\n\t" \
  "v_permlane32_swap_b32 v150, v151\n\t" \
  "v_max_f32 v150, v150, v151\n\t" \
  "s_mov_b32 s92, 0\n\t" \
  ".Ltake%=:\n\t" \
  "s_waitcnt vmcnt(1) lgkmcnt(0)\n\t" \
  "ds_write_b128 v216, v[224:227]\n\t" \
  "v_max3_u32 v148, v224, v225, v226\n\t" \
  "s_waitcnt vmcnt(0)\n\t" \
  "ds_write_b128 v216, v[228:231] offset:1024\n\t" \
  "ds_read_b64_tr_b16 v[160:161], v217 offset:0\n\t" \
  "ds_read_b64_tr_b16 v[162:163], v217 offset:64\n\t" \
  "ds_read_b64_tr_b16 v[164:165], v217 offset:512\n\t" \
  "ds_read_b64_tr_b16 v[166:167], v217 offset:576\n\t" \
  "ds_read_b64_tr_b16 v[168:169], v217 offset:1024\n\t" \
  "ds_read_b64_tr_b16 v[170:171], v217 offset:1088\n\t" \
  "ds_read_b64_tr_b16 v[172:173], v217 offset:1536\n\t" \
  "ds_read_b64_tr_b16 v[174:175], v217 offset:1600\n\t" \
  "v_max3_u32 v148, v148, v227, v228\n\t" \
  "v_max3_u32 v148, v148, v229, v230\n\t" \
  "v_max_u32 v148, v148, v231\n\t" \
  "v_cmp_le_u32 vcc, %[sent], v148\n\t" \
  "s_cbranch_vccnz .Lslow%=\n\t" \
  ".Lok%=:\n\t" \
  "s_waitcnt lgkmcnt(6)\n\t" \
  "v_mfma_f32_16x16x32_bf16 v[152:155], v[160:163], v[128:131], 0\n\t" \
  "s_waitcnt lgkmcnt(4)\n\t" \
  "v_mfma_f32_16x16x32_bf16 v[152:155], v[164:167], v[132:135], v[152:155]\n\t" \
  "s_waitcnt lgkmcnt(2)\n\t" \
  "v_mfma_f32_16x16x32_bf16 v[152:155], v[168:171], v[136:139], v[152:155]\n\t" \
  "s_waitcnt lgkmcnt(0)\n\t" \
  "v_mfma_f32_16x16x32_bf16 v[152:155], v[172:175], v[140:143], v[152:155]\n\t" \
  "v_mov_b32 v157, 0\n\t" \
  "v_mov_b32 v158, 0\n\t" \
  "s_nop 5\n\t" \
  "v_cvt_pk_bf16_f32 v153, v152, v153\n\t" \
  "v_cvt_pk_bf16_f32 v155, v154, v155\n\t" \
  "v_lshlrev_b32 v152, 16, v153\n\t" \
  "v_lshlrev_b32 v154, 16, v155\n\t" \
  "v_and_b32 v153, 0xffff0000, v153\n\t" \
  "v_and_b32 v155, 0xffff0000, v155\n\t" \
  "v_pk_mul_f32 v[152:153], v[152:153], %[sc] op_sel_hi:[1,0]\n\t" \
  "v_pk_mul_f32 v[154:155], v[154:155], %[sc] op_sel_hi:[1,0]\n\t" \
  "v_cvt_pk_bf16_f32 v153, v152, v153\n\t" \
  "v_cvt_pk_bf16_f32 v155, v154, v155\n\t" \
  "v_lshlrev_b32 v152, 16, v153\n\t" \
  "v_lshlrev_b32 v154, 16, v155\n\t" \
  "v_and_b32 v153, 0xffff0000, v153\n\t" \
  "v_and_b32 v155, 0xffff0000, v155\n\t" \
  "v_max3_f32 v148, v152, v153, v150\n\t" \
  "v_max3_f32 v149, v148, v154, v155\n\t" \
  "v_max3_f32 v150, v148, v154, v155\n\t" \
  "v_cndmask_b32 v152, v152, v154, %[up]\n\t" \
  "v_cndmask_b32 v153, v153, v155, %[up]\n\t" \
  "v_permlane16_swap_b32 v149, v150\n\t" \
  "v_max_f32 v148, v149, v150\n\t" \
  "v_mul_f32 v148, %[negc], v148\n\t" \
  "v_fma_f32 v152, v152, %[c], v148\n\t" \
  "v_fma_f32 v153, v153, %[c], v148\n\t" \
  "v_exp_f32 v152, v152\n\t" \
  "v_fma_f32 v196, v196, %[c], v148\n\t" \
  "v_exp_f32 v153, v153\n\t" \
  "v_fma_f32 v197, v197, %[c], v148\n\t" \
  "v_exp_f32 v196, v196\n\t" \
  "v_fma_f32 v198, v198, %[c], v148\n\t" \
  "v_exp_f32 v197, v197\n\t" \
  "v_fma_f32 v199, v199, %[c], v148\n\t" \
  "v_exp_f32 v198, v198\n\t" \
  "v_fma_f32 v144, v144, %[c], v148\n\t" \
  "v_pk_add_f32 v[132:133], v[152:153], v[196:197]\n\t" \
  "v_exp_f32 v199, v199\n\t" \
  "v_fma_f32 v145, v145, %[c], v148\n\t" \
  "v_exp_f32 v144, v144\n\t" \
  "v_fma_f32 v146, v146, %[c], v148\n\t" \
  "v_pk_add_f32 v[132:133], v[132:133], v[198:199]\n\t" \
  "v_exp_f32 v145, v145\n\t" \
  "v_fma_f32 v147, v147, %[c], v148\n\t" \
  "v_exp_f32 v146, v146\n\t" \
  "v_pk_add_f32 v[132:133], v[132:133], v[144:145]\n\t" \
  "v_exp_f32 v147, v147\n\t" \
  "s_nop 0\n\t" \
  "v_pk_add_f32 v[132:133], v[132:133], v[146:147]\n\t" \
  "v_add_f32 v134, v132, v133\n\t" \
  "v_add_f32 v135, v132, v133\n\t" \
  "s_nop 1\n\t" \
  "v_permlane16_swap_b32 v134, v135\n\t" \
  "v_add_f32 v132, v134, v135\n\t" \
  "v_add_f32 v133, v134, v135\n\t" \
  "s_nop 1\n\t" \
  "v_permlane32_swap_b32 v132, v133\n\t" \
  "v_add_f32 v132, v132, v133\n\t" \
  "v_rcp_f32 v148, v132\n\t" \
  "s_nop 0\n\t" \
  "v_pk_mul_f32 v[152:153], v[152:153], v[148:149] op_sel_hi:[1,0]\n\t" \
  "v_pk_mul_f32 v[196:197], v[196:197], v[148:149] op_sel_hi:[1,0]\n\t" \
  "v_pk_mul_f32 v[198:199], v[198:199], v[148:149] op_sel_hi:[1,0]\n\t" \
  "v_pk_mul_f32 v[144:145], v[144:145], v[148:149] op_sel_hi:[1,0]\n\t" \
  "v_pk_mul_f32 v[146:147], v[146:147], v[148:149] op_sel_hi:[1,0]\n\t" \
  "v_cvt_pk_bf16_f32 v156, v152, v153\n\t" \
  "v_cvt_pk_bf16_f32 v159, v152, v153\n\t" \
  "v_cvt_pk_bf16_f32 v128, v196, v197\n\t" \
  "v_cvt_pk_bf16_f32 v129, v198, v199\n\t" \
  "v_mfma_f32_16x16x16_bf16 v[160:163], v[192:193], v[156:157], 0\n\t" \
  "v_cvt_pk_bf16_f32 v130, v144, v145\n\t" \
  "v_mfma_f32_16x16x16_bf16 v[164:167], v[192:193], v[158:159], 0\n\t" \
  "v_cvt_pk_bf16_f32 v131, v146, v147\n\t" \
  "v_mfma_f32_16x16x16_bf16 v[168:171], v[194:195], v[156:157], 0\n\t" \
  "v_mfma_f32_16x16x16_bf16 v[172:175], v[194:195], v[158:159], 0\n\t" \
  "s_nop 0\n\t" \
  "v_mfma_f32_16x16x32_bf16 v[160:163], v[176:179], v[128:131], v[160:163]\n\t" \
  "s_nop 0\n\t" \
  "v_mfma_f32_16x16x32_bf16 v[164:167], v[180:183], v[128:131], v[164:167]\n\t" \
  "s_nop 0\n\t" \
  "v_mfma_f32_16x16x32_bf16 v[168:171], v[184:187], v[128:131], v[168:171]\n\t" \
  "v_mfma_f32_16x16x32_bf16 v[172:175], v[188:191], v[128:131], v[172:175]\n\t" \
  "s_nop 2\n\t" \
  "v_cvt_pk_bf16_f32 v136, v160, v161\n\t" \
  "v_cvt_pk_bf16_f32 v137, v162, v163\n\t" \
  "v_cvt_pk_bf16_f32 v138, v164, v165\n\t" \
  "v_cvt_pk_bf16_f32 v139, v166, v167\n\t" \
  "v_cvt_pk_bf16_f32 v140, v168, v169\n\t" \
  "v_cvt_pk_bf16_f32 v141, v170, v171\n\t" \
  "v_cvt_pk_bf16_f32 v142, v172, v173\n\t" \
  "v_cvt_pk_bf16_f32 v143, v174, v175\n\t" \
  "s_memrealtime s[90:91]\n\t" \
  "s_waitcnt lgkmcnt(0)\n\t" \
  "s_lshl_b32 s93, s90, 2\n\t" \
  "s_sub_i32 s93, s89, s93\n\t" \
  "s_cmp_le_i32 s93, 0\n\t" \
  "s_cbranch_scc1 .Lgoo%=\n\t" \
  "s_min_i32 s93, s93, 8000\n\t" \
  "s_mov_b32 s94, s93\n\t" \
  ".Lslo%=:\n\t" \
  "s_sub_i32 s94, s94, 18\n\t" \
  "s_cmp_lt_i32 s94, 30\n\t" \
  "s_cbranch_scc1 .Lsxo%=\n\t" \
  "s_sleep 1\n\t" \
  "s_branch .Lslo%=\n\t" \
  ".Lsxo%=:\n\t" \
  "s_add_u32 s93, s93, 3\n\t" \
  "s_lshr_b32 s93, s93, 2\n\t" \
  "s_add_u32 s93, s93, s90\n\t" \
  "s_memrealtime s[90:91]\n\t" \
  ".Lpaceo%=:\n\t" \
  "s_memrealtime s[94:95]\n\t" \
  "s_waitcnt lgkmcnt(1)\n\t" \
  "s_sub_i32 s92, s90, s93\n\t" \
  "s_cmp_ge_i32 s92, 0\n\t" \
  "s_cbranch_scc1 .Leo%=\n\t" \
  "s_memrealtime s[90:91]\n\t" \
  "s_waitcnt lgkmcnt(1)\n\t" \
  "s_sub_i32 s92, s94, s93\n\t" \
  "s_cmp_lt_i32 s92, 0\n\t" \
  "s_cbranch_scc1 .Lpaceo%=\n\t" \
  ".Leo%=:\n\t" \
  ".Lgoo%=:\n\t" \
  "buffer_store_dwordx4 v[136:139], v200, %[rt], %[so] offen offset:0 sc1\n\t" \
  "buffer_store_dwordx4 v[140:143], v200, %[rt], %[so] offen offset:1024 sc1\n\t" \
  "s_waitcnt lgkmcnt(0)\n\t" \
  "s_branch .Lend%=\n\t" \
  ".Lpoll%=:\n\t" \
  "buffer_load_dwordx4 v[224:227], v200, %[rs], %[sk] offen offset:0 sc1\n\t" \
  "buffer_load_dwordx4 v[228:231], v200, %[rs], %[sk] offen offset:1024 sc1\n\t" \
  "s_branch .Ltake%=\n\t" \
  "s_branch .Lend%=\n\t" \
  ".Lslow%=:\n\t" \
  "s_mov_b64 s[94:95], 0\n\t" \
  "v_cmp_eq_u32 vcc, %[sent], v224\n\t" \
  "s_or_b64 s[94:95], s[94:95], vcc\n\t" \
  "v_cmp_eq_u32 vcc, %[sent], v225\n\t" \
  "s_or_b64 s[94:95], s[94:95], vcc\n\t" \
  "v_cmp_eq_u32 vcc, %[sent], v226\n\t" \
  "s_or_b64 s[94:95], s[94:95], vcc\n\t" \
  "v_cmp_eq_u32 vcc, %[sent], v227\n\t" \
  "s_or_b64 s[94:95], s[94:95], vcc\n\t" \
  "v_cmp_eq_u32 vcc, %[sent], v228\n\t" \
  "s_or_b64 s[94:95], s[94:95], vcc\n\t" \
  "v_cmp_eq_u32 vcc, %[sent], v229\n\t" \
  "s_or_b64 s[94:95], s[94:95], vcc\n\t" \
  "v_cmp_eq_u32 vcc, %[sent], v230\n\t" \
  "s_or_b64 s[94:95], s[94:95], vcc\n\t" \
  "v_cmp_eq_u32 vcc, %[sent], v231\n\t" \
  "s_or_b64 s[94:95], s[94:95], vcc\n\t" \
  "s_cmp_eq_u64 s[94:95], 0\n\t" \
  "s_cbranch_scc1 .Lok%=\n\t" \
  "s_add_u32 s92, s92, 1\n\t" \
  "s_cmp_eq_u32 s92, 1\n\t" \
  "s_cbranch_scc1 .Lpoll%=\n\t" \
  "s_sleep 2\n\t" \
  "s_and_b32 s93, s92, 63\n\t" \
  "s_cmp_lg_u32 s93, 0\n\t" \
  "s_cbranch_scc1 .Lpoll%=\n\t" \
  "v_mov_b32 v149, 0\n\t" \
  "buffer_load_dword v148, v149, %[rs], %[sab] offen sc1\n\t" \
  "s_waitcnt vmcnt(0)\n\t" \
  "v_readfirstlane_b32 s93, v148\n\t" \
  "s_nop 3\n\t" \
  "s_cmp_eq_u32 s93, 1\n\t" \
  "s_cbranch_scc1 .Lok%=\n\t" \
  "s_cmp_lt_u32 s92, 0x80000\n\t" \
  "s_cbranch_scc1 .Lpoll%=\n\t" \
  "v_mov_b32 v148, 1\n\t" \
  "buffer_store_dword v148, v149, %[rs], %[sab] offen sc1\n\t" \
  "s_branch .Lok%=\n\t" \
  ".Lend%=:\n\t"
constexpr int TLAG = 2;
#define RED_ASM \
  "s_mov_b32 %[step], 0\n\t" \
  "s_lshl_b32 %[sh], %[step], 15\n\t" \
  "s_lshr_b32 s93, %[step], 4\n\t" \
  "s_lshr_b32 s93, %[pm], s93\n\t" \
  "s_and_b32 s93, s93, 4\n\t" \
  "s_lshl_b32 s93, s93, 11\n\t" \
  "s_lshl2_add_u32 %[so], %[sh], s93\n\t" \
  "s_sub_i32 s93, %[step], 2\n\t" \
  "s_lshl_b32 %[st], s93, 19\n\t" \
  "s_lshl_b32 s94, s93, 11\n\t" \
  "s_xor_b32 s94, s94, %[s0]\n\t" \
  "s_and_b32 s94, s94, 0x2000\n\t" \
  "s_or_b32 %[st], %[st], s94\n\t" \
  "s_cmp_lt_i32 s93, 0\n\t" \
  "s_cselect_b32 %[st], 0x70000000, %[st]\n\t" \
  "s_add_u32 %[st4], %[st], 4096\n\t" \
  "buffer_load_dwordx2 v[166:167], v173, %[rs], 0 offen sc1\n\t" \
  "v_mov_b64 v[144:145], 0\n\t" \
  "v_mov_b64 v[146:147], 0\n\t" \
  "v_mov_b64 v[148:149], 0\n\t" \
  "v_mov_b64 v[150:151], 0\n\t" \
  "v_mov_b64 v[152:153], 0\n\t" \
  "v_mov_b64 v[154:155], 0\n\t" \
  "v_mov_b64 v[156:157], 0\n\t" \
  "v_mov_b64 v[158:159], 0\n\t" \
  ".Lstep%=:\n\t" \
  "s_setprio 3\n\t" \
  "s_mov_b32 s92, 0\n\t" \
  ".Lcal%=:\n\t" \
  "s_waitcnt vmcnt(0)\n\t" \
  "s_memrealtime s[76:77]\n\t" \
  "v_cmp_ne_u32 vcc, %[step], v166\n\t" \
  "v_readlane_b32 s90, v167, 0\n\t" \
  "v_readlane_b32 s91, v167, 1\n\t" \
  "v_readlane_b32 s94, v167, 2\n\t" \
  "v_readlane_b32 s95, v167, 3\n\t" \
  "s_sub_i32 s90, s90, %[base]\n\t" \
  "s_sub_i32 s91, s91, %[base]\n\t" \
  "s_sub_i32 s94, s94, %[base]\n\t" \
  "s_sub_i32 s95, s95, %[base]\n\t" \
  "s_max_i32 s90, s90, s91\n\t" \
  "s_max_i32 s94, s94, s95\n\t" \
  "s_max_i32 s90, s90, s94\n\t" \
  "s_add_i32 s90, s90, %[ds]\n\t" \
  "s_waitcnt lgkmcnt(0)\n\t" \
  "s_lshl2_add_u32 s76, s76, %[mn]\n\t" \
  "s_sub_i32 s76, s76, %[base]\n\t" \
  "s_add_u32 %[E], s90, %[base]\n\t" \
  "s_and_b32 s93, vcc_lo, 0xf\n\t" \
  "s_cbranch_scc1 .Lbl%=\n\t" \
  ".Lgo%=:\n\t" \
  "v_mov_b32 v166, 7\n\t" \
  "s_mov_b64 exec, 1\n\t" \
  "v_mov_b32 v164, s90\n\t" \
  "ds_max_u32 v168, v164 offset:4100\n\t" \
  "ds_add_u32 v168, v164 offset:4096\n\t" \
  "s_mov_b64 exec, -1\n\t" \
  "s_memrealtime s[90:91]\n\t" \
  "s_waitcnt lgkmcnt(0)\n\t" \
  "s_lshl_b32 s93, s90, 2\n\t" \
  "s_sub_i32 s93, %[E], s93\n\t" \
  "s_cmp_le_i32 s93, 0\n\t" \
  "s_cbranch_scc1 .Lgop%=\n\t" \
  "s_min_i32 s93, s93, 8000\n\t" \
  "s_mov_b32 s94, s93\n\t" \
  ".Lslp%=:\n\t" \
  "s_sub_i32 s94, s94, 18\n\t" \
  "s_cmp_lt_i32 s94, 30\n\t" \
  "s_cbranch_scc1 .Lsxp%=\n\t" \
  "s_sleep 1\n\t" \
  "s_branch .Lslp%=\n\t" \
  ".Lsxp%=:\n\t" \
  "s_sub_i32 s93, s93, 12\n\t" \
  "s_max_i32 s93, s93, 4\n\t" \
  "s_lshr_b32 s93, s93, 2\n\t" \
  "s_add_u32 s93, s93, s90\n\t" \
  "s_memrealtime s[90:91]\n\t" \
  ".Lpacep%=:\n\t" \
  "s_memrealtime s[94:95]\n\t" \
  "s_waitcnt lgkmcnt(1)\n\t" \
  "s_sub_i32 s92, s90, s93\n\t" \
  "s_cmp_ge_i32 s92, 0\n\t" \
  "s_cbranch_scc1 .Lep%=\n\t" \
  "s_memrealtime s[90:91]\n\t" \
  "s_waitcnt lgkmcnt(1)\n\t" \
  "s_sub_i32 s92, s94, s93\n\t" \
  "s_cmp_lt_i32 s92, 0\n\t" \
  "s_cbranch_scc1 .Lpacep%=\n\t" \
  "s_nop 4\n\t" \
  ".Lep%=:\n\t" \
  "s_add_u32 s92, s92, s93\n\t" \
  "s_lshl_b32 s92, s92, 2\n\t" \
  "s_sub_i32 s92, %[E], s92\n\t" \
  "s_max_i32 s92, s92, 0\n\t" \
  "s_bitcmp0_b32 s92, 3\n\t" \
  "s_cbranch_scc1 .Lf3p%=\n\t" \
  "s_nop 15\n\t" \
  ".Lf3p%=:\n\t" \
  "s_bitcmp0_b32 s92, 2\n\t" \
  "s_cbranch_scc1 .Lf2p%=\n\t" \
  "s_nop 10\n\t" \
  ".Lf2p%=:\n\t" \
  ".Lgop%=:\n\t" \
  "s_mov_b32 s92, 0\n\t" \
  "buffer_load_dwordx4 v[128:131], v234, %[rs], %[so] offen sc1\n\t" \
  "s_nop 15\n\t" \
  "buffer_load_dwordx4 v[132:135], v235, %[rs], %[so] offen sc1\n\t" \
  "s_nop 15\n\t" \
  "buffer_load_dwordx4 v[136:139], v236, %[rs], %[so] offen sc1\n\t" \
  "s_nop 15\n\t" \
  "buffer_load_dwordx4 v[140:143], v237, %[rs], %[so] offen sc1\n\t" \
  "s_waitcnt lgkmcnt(0)\n\t" \
  ".Lw%=:\n\t" \
  "s_waitcnt vmcnt(3)\n\t" \
  "v_smfmac_f32_16x16x64_bf16 v[144:147], v[128:131], v[0:7], v172\n\t" \
  "v_max3_u32 v164, v128, v129, 0\n\t" \
  "v_smfmac_f32_16x16x64_bf16 v[148:151], v[128:131], v[8:15], v172\n\t" \
  "v_max3_u32 v164, v130, v131, v164\n\t" \
  "v_smfmac_f32_16x16x64_bf16 v[152:155], v[128:131], v[16:23], v172\n\t" \
  "v_smfmac_f32_16x16x64_bf16 v[156:159], v[128:131], v[24:31], v172\n\t" \
  "buffer_load_dwordx4 v[176:179], v238, %[rt], %[st] offen offset:0 sc1\n\t" \
  "s_waitcnt vmcnt(3)\n\t" \
  "v_smfmac_f32_16x16x64_bf16 v[144:147], v[132:135], v[32:39], v172\n\t" \
  "v_max3_u32 v164, v132, v133, v164\n\t" \
  "v_smfmac_f32_16x16x64_bf16 v[148:151], v[132:135], v[40:47], v172\n\t" \
  "v_max3_u32 v164, v134, v135, v164\n\t" \
  "v_smfmac_f32_16x16x64_bf16 v[152:155], v[132:135], v[48:55], v172\n\t" \
  "v_smfmac_f32_16x16x64_bf16 v[156:159], v[132:135], v[56:63], v172\n\t" \
  "buffer_load_dwordx4 v[180:183], v238, %[rt], %[st] offen offset:1024 sc1\n\t" \
  "s_waitcnt vmcnt(3)\n\t" \
  "v_smfmac_f32_16x16x64_bf16 v[144:147], v[136:139], v[64:71], v172\n\t" \
  "v_max3_u32 v164, v136, v137, v164\n\t" \
  "v_smfmac_f32_16x16x64_bf16 v[148:151], v[136:139], v[72:79], v172\n\t" \
  "v_max3_u32 v164, v138, v139, v164\n\t" \
  "v_smfmac_f32_16x16x64_bf16 v[152:155], v[136:139], v[80:87], v172\n\t" \
  "v_smfmac_f32_16x16x64_bf16 v[156:159], v[136:139], v[88:95], v172\n\t" \
  "buffer_load_dwordx4 v[184:187], v238, %[rt], %[st4] offen offset:0 sc1\n\t" \
  "s_waitcnt vmcnt(3)\n\t" \
  "v_smfmac_f32_16x16x64_bf16 v[144:147], v[140:143], v[96:103], v172\n\t" \
  "v_max3_u32 v164, v140, v141, v164\n\t" \
  "v_smfmac_f32_16x16x64_bf16 v[148:151], v[140:143], v[104:111], v172\n\t" \
  "v_max3_u32 v164, v142, v143, v164\n\t" \
  "v_smfmac_f32_16x16x64_bf16 v[152:155], v[140:143], v[112:119], v172\n\t" \
  "v_cmp_le_u32 vcc, %[sent], v164\n\t" \
  "v_smfmac_f32_16x16x64_bf16 v[156:159], v[140:143], v[120:127], v172\n\t" \
  "buffer_load_dwordx4 v[188:191], v238, %[rt], %[st4] offen offset:1024 sc1\n\t" \
  "s_cbranch_vccnz .Lslow%=\n\t" \
  ".Lok%=:\n\t" \
  "v_add_f32_dpp v160, v144, v146 quad_perm:[0,1,2,3] row_mask:0x3 bank_mask:0xf\n\t" \
  "v_add_f32_dpp v161, v145, v147 quad_perm:[0,1,2,3] row_mask:0x3 bank_mask:0xf\n\t" \
  "v_add_f32_dpp v162, v148, v150 quad_perm:[0,1,2,3] row_mask:0x3 bank_mask:0xf\n\t" \
  "v_add_f32_dpp v163, v149, v151 quad_perm:[0,1,2,3] row_mask:0x3 bank_mask:0xf\n\t" \
  "v_add_f32_dpp v160, v152, v154 quad_perm:[0,1,2,3] row_mask:0xc bank_mask:0xf\n\t" \
  "v_add_f32_dpp v161, v153, v155 quad_perm:[0,1,2,3] row_mask:0xc bank_mask:0xf\n\t" \
  "v_add_f32_dpp v162, v156, v158 quad_perm:[0,1,2,3] row_mask:0xc bank_mask:0xf\n\t" \
  "v_add_f32_dpp v163, v157, v159 quad_perm:[0,1,2,3] row_mask:0xc bank_mask:0xf\n\t" \
  "s_nop 0\n\t" \
  "v_permlane32_swap_b32 v160, v162\n\t" \
  "v_permlane32_swap_b32 v161, v163\n\t" \
  "v_add_f32 v160, v160, v162\n\t" \
  "v_add_f32 v161, v161, v163\n\t" \
  "ds_write_b64 v169, v[160:161]\n\t" \
  "s_mov_b64 exec, 1\n\t" \
  "ds_inc_rtn_u32 v164, v168, v166 offset:4104\n\t" \
  "s_mov_b64 exec, -1\n\t" \
  "ds_read_b64 v[128:129], v170 offset:0\n\t" \
  "ds_read_b64 v[130:131], v170 offset:512\n\t" \
  "ds_read_b64 v[132:133], v170 offset:1024\n\t" \
  "ds_read_b64 v[134:135], v170 offset:1536\n\t" \
  "ds_read_b64 v[136:137], v170 offset:2048\n\t" \
  "ds_read_b64 v[138:139], v170 offset:2560\n\t" \
  "ds_read_b64 v[140:141], v170 offset:3072\n\t" \
  "ds_read_b64 v[142:143], v170 offset:3584\n\t" \
  "s_waitcnt lgkmcnt(8)\n\t" \
  "v_readfirstlane_b32 s93, v164\n\t" \
  "s_cmp_lg_u32 s93, 7\n\t" \
  "s_cbranch_scc1 .Lb1%=\n\t" \
  "s_waitcnt lgkmcnt(4)\n\t" \
  "v_pk_add_f32 v[128:129], v[128:129], v[130:131]\n\t" \
  "v_pk_add_f32 v[132:133], v[132:133], v[134:135]\n\t" \
  "s_waitcnt lgkmcnt(0)\n\t" \
  "v_pk_add_f32 v[136:137], v[136:137], v[138:139]\n\t" \
  "v_pk_add_f32 v[140:141], v[140:141], v[142:143]\n\t" \
  "v_pk_add_f32 v[128:129], v[128:129], v[132:133]\n\t" \
  "v_pk_add_f32 v[136:137], v[136:137], v[140:141]\n\t" \
  "v_pk_add_f32 v[128:129], v[128:129], v[136:137]\n\t" \
  "v_cvt_pk_bf16_f32 v128, v128, v129\n\t" \
  "s_nop 1\n\t" \
  "v_mov_b32_dpp v129, v128 quad_perm:[1,0,3,2] row_mask:0xf bank_mask:0xf\n\t" \
  "v_perm_b32 v128, v129, v128, v174\n\t" \
  "buffer_store_dword v128, v171, %[rs], %[sh] offen sc1\n\t" \
  "s_memtime s[90:91]\n\t" \
  "s_barrier\n\t" \
  "s_memtime s[76:77]\n\t" \
  "s_memrealtime s[78:79]\n\t" \
  "s_nop 3\n\t" \
  "s_memrealtime s[80:81]\n\t" \
  "s_nop 3\n\t" \
  "s_memrealtime s[82:83]\n\t" \
  "s_nop 3\n\t" \
  "s_memrealtime s[84:85]\n\t" \
  "s_nop 3\n\t" \
  "s_memrealtime s[86:87]\n\t" \
  "s_nop 3\n\t" \
  "s_memrealtime s[88:89]\n\t" \
  "s_mov_b32 s93, 0\n\t" \
  "s_waitcnt lgkmcnt(0)\n\t" \
  "s_cmp_eq_u32 s80, s78\n\t" \
  "s_addc_u32 s93, s93, 0\n\t" \
  "s_cmp_eq_u32 s82, s78\n\t" \
  "s_addc_u32 s93, s93, 0\n\t" \
  "s_cmp_eq_u32 s84, s78\n\t" \
  "s_addc_u32 s93, s93, 0\n\t" \
  "s_cmp_eq_u32 s86, s78\n\t" \
  "s_addc_u32 s93, s93, 0\n\t" \
  "s_cmp_eq_u32 s88, s78\n\t" \
  "s_addc_u32 s93, s93, 0\n\t" \
  "s_mul_i32 s93, s93, 21\n\t" \
  "s_add_u32 s76, s76, 15\n\t" \
  "s_add_u32 s76, s76, s93\n\t" \
  "s_lshl2_add_u32 s78, s78, 16\n\t" \
  "s_mov_b64 exec, 1\n\t" \
  "s_sub_i32 s90, s76, s90\n\t" \
  "s_mul_i32 s90, s90, 11900\n\t" \
  "s_ashr_i32 s90, s90, 16\n\t" \
  "s_sub_u32 s90, s78, s90\n\t" \
  "s_sub_u32 s90, s90, %[base]\n\t" \
  "v_mov_b32 v164, s90\n\t" \
  "ds_write_b32 v168, v164 offset:4108\n\t" \
  "s_mov_b64 exec, -1\n\t" \
  "s_waitcnt vmcnt(1)\n\t" \
  ".Lb2%=:\n\t" \
  "s_setprio 0\n\t" \
  "s_bitcmp0_b64 %[am], %[step]\n\t" \
  "s_cbranch_scc1 .La1%=\n\t" \
  "buffer_store_dwordx4 v[224:227], v228, %[ra], 0 offen sc1\n\t" \
  ".La1%=:\n\t" \
  "buffer_load_dwordx4 v[192:195], v239, %[rt], %[st] offen offset:0 sc1\n\t" \
  "buffer_load_dwordx4 v[196:199], v239, %[rt], %[st] offen offset:1024 sc1\n\t" \
  "v_mfma_f32_16x16x32_bf16 v[128:131], v[0:3], v[176:179], 0\n\t" \
  "v_mfma_f32_16x16x32_bf16 v[136:139], v[8:11], v[176:179], 0\n\t" \
  "ds_read2st64_b32 v[144:145], v231 offset0:0 offset1:8\n\t" \
  "ds_read2st64_b32 v[146:147], v231 offset0:16 offset1:24\n\t" \
  "ds_read2st64_b32 v[148:149], v231 offset0:32 offset1:40\n\t" \
  "ds_read2st64_b32 v[150:151], v231 offset0:48 offset1:56\n\t" \
  "buffer_load_dwordx4 v[200:203], v239, %[rt], %[st4] offen offset:0 sc1\n\t" \
  "buffer_load_dwordx4 v[204:207], v239, %[rt], %[st4] offen offset:1024 sc1\n\t" \
  "v_mfma_f32_16x16x32_bf16 v[132:135], v[4:7], v[180:183], 0\n\t" \
  "v_mfma_f32_16x16x32_bf16 v[140:143], v[12:15], v[180:183], 0\n\t" \
  "s_waitcnt lgkmcnt(0)\n\t" \
  "v_add_f32 v144, v144, v145\n\t" \
  "v_add_f32 v146, v146, v147\n\t" \
  "v_add_f32 v148, v148, v149\n\t" \
  "v_add_f32 v150, v150, v151\n\t" \
  "v_add_f32 v144, v144, v146\n\t" \
  "v_add_f32 v148, v148, v150\n\t" \
  "buffer_load_dwordx4 v[208:211], v240, %[rt], %[st] offen offset:0 sc1\n\t" \
  "buffer_load_dwordx4 v[212:215], v240, %[rt], %[st] offen offset:1024 sc1\n\t" \
  "v_mfma_f32_16x16x32_bf16 v[128:131], v[16:19], v[184:187], v[128:131]\n\t" \
  "v_mfma_f32_16x16x32_bf16 v[136:139], v[24:27], v[184:187], v[136:139]\n\t" \
  "v_add_f32 v144, v144, v148\n\t" \
  "v_cvt_pk_bf16_f32 v144, v144, v144\n\t" \
  "ds_write_b16 v232, v144\n\t" \
  "ds_read_b128 v[224:227], v229\n\t" \
  "v_add_u32 v228, %[ainc], v228\n\t" \
  "v_add_u32 v229, %[rinc], v229\n\t" \
  "v_add_u32 v232, 1024, v232\n\t" \
  "v_xor_b32 v231, 0x4000, v231\n\t" \
  "buffer_load_dwordx4 v[216:219], v240, %[rt], %[st4] offen offset:0 sc1\n\t" \
  "buffer_load_dwordx4 v[220:223], v240, %[rt], %[st4] offen offset:1024 sc1\n\t" \
  "v_mfma_f32_16x16x32_bf16 v[132:135], v[20:23], v[188:191], v[132:135]\n\t" \
  "v_mfma_f32_16x16x32_bf16 v[140:143], v[28:31], v[188:191], v[140:143]\n\t" \
  "buffer_load_dwordx4 v[176:179], v241, %[rt], %[st] offen offset:0 sc1\n\t" \
  "s_waitcnt vmcnt(8)\n\t" \
  "v_mfma_f32_16x16x32_bf16 v[128:131], v[32:35], v[192:195], v[128:131]\n\t" \
  "v_mfma_f32_16x16x32_bf16 v[136:139], v[40:43], v[192:195], v[136:139]\n\t" \
  "buffer_load_dwordx4 v[180:183], v241, %[rt], %[st] offen offset:1024 sc1\n\t" \
  "s_waitcnt vmcnt(8)\n\t" \
  "v_mfma_f32_16x16x32_bf16 v[132:135], v[36:39], v[196:199], v[132:135]\n\t" \
  "v_mfma_f32_16x16x32_bf16 v[140:143], v[44:47], v[196:199], v[140:143]\n\t" \
  "buffer_load_dwordx4 v[184:187], v241, %[rt], %[st4] offen offset:0 sc1\n\t" \
  "s_waitcnt vmcnt(8)\n\t" \
  "v_mfma_f32_16x16x32_bf16 v[128:131], v[48:51], v[200:203], v[128:131]\n\t" \
  "v_mfma_f32_16x16x32_bf16 v[136:139], v[56:59], v[200:203], v[136:139]\n\t" \
  "ds_read_b128 v[160:163], v168 offset:4096\n\t" \
  "buffer_load_dwordx4 v[188:191], v241, %[rt], %[st4] offen offset:1024 sc1\n\t" \
  "s_waitcnt vmcnt(8)\n\t" \
  "v_mfma_f32_16x16x32_bf16 v[132:135], v[52:55], v[204:207], v[132:135]\n\t" \
  "v_mfma_f32_16x16x32_bf16 v[140:143], v[60:63], v[204:207], v[140:143]\n\t" \
  "s_waitcnt lgkmcnt(0)\n\t" \
  "v_readfirstlane_b32 s90, v161\n\t" \
  "v_readfirstlane_b32 s91, v163\n\t" \
  "v_readfirstlane_b32 s94, v160\n\t" \
  "s_sub_u32 s93, s94, %[sm]\n\t" \
  "s_mov_b32 %[sm], s94\n\t" \
  "s_lshr_b32 s93, s93, 3\n\t" \
  "s_sub_i32 s93, s90, s93\n\t" \
  "s_mul_i32 s93, s93, %[wm]\n\t" \
  "s_ashr_i32 s93, s93, 2\n\t" \
  "s_min_u32 s93, s93, %[gm]\n\t" \
  "s_sub_i32 s90, s90, s93\n\t" \
  "s_add_u32 s93, s90, %[rl]\n\t" \
  "s_add_u32 s93, s93, %[base]\n\t" \
  "s_add_u32 %[E], s93, %[lk]\n\t" \
  "s_sub_i32 s91, s91, s90\n\t" \
  "s_sub_i32 s91, s91, %[rl]\n\t" \
  "s_min_i32 s93, s91, %[up]\n\t" \
  "s_max_i32 s93, s93, %[dn]\n\t" \
  "s_cmp_gt_i32 s91, 60\n\t" \
  "s_cselect_b32 s93, 2, s93\n\t" \
  "s_add_i32 %[rl], %[rl], s93\n\t" \
  "v_mov_b32 v160, %[rl]\n\t" \
  "buffer_store_dword v160, v175, %[rs], 0 offen sc1\n\t" \
  "s_waitcnt vmcnt(8)\n\t" \
  "v_mfma_f32_16x16x32_bf16 v[128:131], v[64:67], v[208:211], v[128:131]\n\t" \
  "v_mfma_f32_16x16x32_bf16 v[136:139], v[72:75], v[208:211], v[136:139]\n\t" \
  "s_add_u32 %[step], %[step], 1\n\t" \
  "s_lshl_b32 %[sh], %[step], 15\n\t" \
  "s_lshr_b32 s93, %[step], 4\n\t" \
  "s_lshr_b32 s93, %[pm], s93\n\t" \
  "s_and_b32 s93, s93, 4\n\t" \
  "s_lshl_b32 s93, s93, 11\n\t" \
  "s_lshl2_add_u32 %[so], %[sh], s93\n\t" \
  "s_waitcnt vmcnt(7)\n\t" \
  "v_mfma_f32_16x16x32_bf16 v[132:135], v[68:71], v[212:215], v[132:135]\n\t" \
  "v_mfma_f32_16x16x32_bf16 v[140:143], v[76:79], v[212:215], v[140:143]\n\t" \
  "s_sub_i32 s93, %[step], 2\n\t" \
  "s_lshl_b32 %[st], s93, 19\n\t" \
  "s_lshl_b32 s94, s93, 11\n\t" \
  "s_xor_b32 s94, s94, %[s0]\n\t" \
  "s_and_b32 s94, s94, 0x2000\n\t" \
  "s_or_b32 %[st], %[st], s94\n\t" \
  "s_cmp_lt_i32 s93, 0\n\t" \
  "s_cselect_b32 %[st], 0x70000000, %[st]\n\t" \
  "s_add_u32 %[st4], %[st], 4096\n\t" \
  "s_waitcnt vmcnt(6)\n\t" \
  "v_mfma_f32_16x16x32_bf16 v[128:131], v[80:83], v[216:219], v[128:131]\n\t" \
  "v_mfma_f32_16x16x32_bf16 v[136:139], v[88:91], v[216:219], v[136:139]\n\t" \
  "s_waitcnt vmcnt(5)\n\t" \
  "v_mfma_f32_16x16x32_bf16 v[132:135], v[84:87], v[220:223], v[132:135]\n\t" \
  "v_mfma_f32_16x16x32_bf16 v[140:143], v[92:95], v[220:223], v[140:143]\n\t" \
  "s_cmp_eq_u32 %[step], 63\n\t" \
  "s_cbranch_scc1 .Llast%=\n\t" \
  "s_movk_i32 s93, 600\n\t" \
  ".Lpacel%=:\n\t" \
  "s_memrealtime s[90:91]\n\t" \
  "s_waitcnt lgkmcnt(0)\n\t" \
  "s_lshl_b32 s90, s90, 2\n\t" \
  "s_sub_i32 s91, %[E], s90\n\t" \
  "s_cmp_le_i32 s91, 0\n\t" \
  "s_cbranch_scc1 .Lgol%=\n\t" \
  "s_sub_u32 s93, s93, 1\n\t" \
  "s_cmp_lg_u32 s93, 0\n\t" \
  "s_cbranch_scc1 .Lpacel%=\n\t" \
  ".Lgol%=:\n\t" \
  "buffer_load_dwordx2 v[166:167], v173, %[rs], 0 offen sc1\n\t" \
  "s_waitcnt vmcnt(5)\n\t" \
  "v_mfma_f32_16x16x32_bf16 v[128:131], v[96:99], v[176:179], v[128:131]\n\t" \
  "v_mfma_f32_16x16x32_bf16 v[136:139], v[104:107], v[176:179], v[136:139]\n\t" \
  "s_waitcnt vmcnt(4)\n\t" \
  "v_mfma_f32_16x16x32_bf16 v[132:135], v[100:103], v[180:183], v[132:135]\n\t" \
  "v_mfma_f32_16x16x32_bf16 v[140:143], v[108:111], v[180:183], v[140:143]\n\t" \
  "s_waitcnt vmcnt(3)\n\t" \
  "v_mfma_f32_16x16x32_bf16 v[128:131], v[112:115], v[184:187], v[128:131]\n\t" \
  "v_mfma_f32_16x16x32_bf16 v[136:139], v[120:123], v[184:187], v[136:139]\n\t" \
  "s_waitcnt vmcnt(2)\n\t" \
  "v_mfma_f32_16x16x32_bf16 v[132:135], v[116:119], v[188:191], v[132:135]\n\t" \
  "v_mfma_f32_16x16x32_bf16 v[140:143], v[124:127], v[188:191], v[140:143]\n\t" \
  "v_mov_b64 v[144:145], 0\n\t" \
  "v_mov_b64 v[146:147], 0\n\t" \
  "v_mov_b64 v[148:149], 0\n\t" \
  "v_mov_b64 v[150:151], 0\n\t" \
  "v_mov_b64 v[152:153], 0\n\t" \
  "v_mov_b64 v[154:155], 0\n\t" \
  "v_mov_b64 v[156:157], 0\n\t" \
  "v_mov_b64 v[158:159], 0\n\t" \
  "v_pk_add_f32 v[128:129], v[128:129], v[132:133]\n\t" \
  "v_pk_add_f32 v[130:131], v[130:131], v[134:135]\n\t" \
  "ds_write_b128 v230, v[128:131] offset:0\n\t" \
  "v_pk_add_f32 v[136:137], v[136:137], v[140:141]\n\t" \
  "v_pk_add_f32 v[138:139], v[138:139], v[142:143]\n\t" \
  "ds_write_b128 v230, v[136:139] offset:1024\n\t" \
  "v_xor_b32 v230, 0x4000, v230\n\t" \
  "s_cmp_lg_u32 %[step], 63\n\t" \
  "s_cbranch_scc1 .Lstep%=\n\t" \
  ".Lfc%=:\n\t" \
  "s_waitcnt vmcnt(0)\n\t" \
  "v_cmp_eq_u32 vcc, -1, v166\n\t" \
  "s_cbranch_vccnz .Lfw%=\n\t" \
  ".Lfo%=:\n\t" \
  "v_add_u32 v242, 16384, v242\n\t" \
  "buffer_load_dwordx4 v[176:179], v238, %[rt], %[st] offen offset:0 sc1\n\t" \
  "buffer_load_dwordx4 v[180:183], v238, %[rt], %[st] offen offset:1024 sc1\n\t" \
  "buffer_load_dwordx4 v[184:187], v238, %[rt], %[st4] offen offset:0 sc1\n\t" \
  "buffer_load_dwordx4 v[188:191], v238, %[rt], %[st4] offen offset:1024 sc1\n\t" \
  "buffer_load_dwordx4 v[192:195], v239, %[rt], %[st] offen offset:0 sc1\n\t" \
  "buffer_load_dwordx4 v[196:199], v239, %[rt], %[st] offen offset:1024 sc1\n\t" \
  "buffer_load_dwordx4 v[200:203], v239, %[rt], %[st4] offen offset:0 sc1\n\t" \
  "buffer_load_dwordx4 v[204:207], v239, %[rt], %[st4] offen offset:1024 sc1\n\t" \
  "v_subrev_u32 v230, 98304, v230\n\t" \
  ".Lt%=:\n\t" \
  "s_waitcnt vmcnt(7)\n\t" \
  "v_mfma_f32_16x16x32_bf16 v[128:131], v[0:3], v[176:179], 0\n\t" \
  "v_mfma_f32_16x16x32_bf16 v[136:139], v[8:11], v[176:179], 0\n\t" \
  "buffer_load_dwordx4 v[176:179], v240, %[rt], %[st] offen offset:0 sc1\n\t" \
  "buffer_load_dword v166, v242, %[rs], 0 offen sc1\n\t" \
  "s_waitcnt vmcnt(8)\n\t" \
  "v_mfma_f32_16x16x32_bf16 v[132:135], v[4:7], v[180:183], 0\n\t" \
  "v_mfma_f32_16x16x32_bf16 v[140:143], v[12:15], v[180:183], 0\n\t" \
  "buffer_load_dwordx4 v[180:183], v240, %[rt], %[st] offen offset:1024 sc1\n\t" \
  "s_waitcnt vmcnt(8)\n\t" \
  "v_mfma_f32_16x16x32_bf16 v[128:131], v[16:19], v[184:187], v[128:131]\n\t" \
  "v_mfma_f32_16x16x32_bf16 v[136:139], v[24:27], v[184:187], v[136:139]\n\t" \
  "buffer_load_dwordx4 v[184:187], v240, %[rt], %[st4] offen offset:0 sc1\n\t" \
  "s_waitcnt vmcnt(8)\n\t" \
  "v_mfma_f32_16x16x32_bf16 v[132:135], v[20:23], v[188:191], v[132:135]\n\t" \
  "v_mfma_f32_16x16x32_bf16 v[140:143], v[28:31], v[188:191], v[140:143]\n\t" \
  "buffer_load_dwordx4 v[188:191], v240, %[rt], %[st4] offen offset:1024 sc1\n\t" \
  "s_waitcnt vmcnt(8)\n\t" \
  "v_mfma_f32_16x16x32_bf16 v[128:131], v[32:35], v[192:195], v[128:131]\n\t" \
  "v_mfma_f32_16x16x32_bf16 v[136:139], v[40:43], v[192:195], v[136:139]\n\t" \
  "buffer_load_dwordx4 v[192:195], v241, %[rt], %[st] offen offset:0 sc1\n\t" \
  "s_waitcnt vmcnt(8)\n\t" \
  "v_mfma_f32_16x16x32_bf16 v[132:135], v[36:39], v[196:199], v[132:135]\n\t" \
  "v_mfma_f32_16x16x32_bf16 v[140:143], v[44:47], v[196:199], v[140:143]\n\t" \
  "buffer_load_dwordx4 v[196:199], v241, %[rt], %[st] offen offset:1024 sc1\n\t" \
  "s_waitcnt vmcnt(8)\n\t" \
  "v_mfma_f32_16x16x32_bf16 v[128:131], v[48:51], v[200:203], v[128:131]\n\t" \
  "v_mfma_f32_16x16x32_bf16 v[136:139], v[56:59], v[200:203], v[136:139]\n\t" \
  "buffer_load_dwordx4 v[200:203], v241, %[rt], %[st4] offen offset:0 sc1\n\t" \
  "s_waitcnt vmcnt(8)\n\t" \
  "v_mfma_f32_16x16x32_bf16 v[132:135], v[52:55], v[204:207], v[132:135]\n\t" \
  "v_mfma_f32_16x16x32_bf16 v[140:143], v[60:63], v[204:207], v[140:143]\n\t" \
  "buffer_load_dwordx4 v[204:207], v241, %[rt], %[st4] offen offset:1024 sc1\n\t" \
  "s_waitcnt vmcnt(8)\n\t" \
  "v_mfma_f32_16x16x32_bf16 v[128:131], v[64:67], v[176:179], v[128:131]\n\t" \
  "v_mfma_f32_16x16x32_bf16 v[136:139], v[72:75], v[176:179], v[136:139]\n\t" \
  "s_waitcnt vmcnt(7)\n\t" \
  ".Ltc%=:\n\t" \
  "v_cmp_eq_u32 vcc, -1, v166\n\t" \
  "s_cbranch_vccnz .Ltw%=\n\t" \
  ".Lto%=:\n\t" \
  "s_cmp_eq_u32 %[step], 65\n\t" \
  "s_cbranch_scc1 .Ltl%=\n\t" \
  "s_add_u32 %[step], %[step], 1\n\t" \
  "s_sub_i32 s93, %[step], 2\n\t" \
  "s_lshl_b32 %[st], s93, 19\n\t" \
  "s_lshl_b32 s94, s93, 11\n\t" \
  "s_xor_b32 s94, s94, %[s0]\n\t" \
  "s_and_b32 s94, s94, 0x2000\n\t" \
  "s_or_b32 %[st], %[st], s94\n\t" \
  "s_cmp_lt_i32 s93, 0\n\t" \
  "s_cselect_b32 %[st], 0x70000000, %[st]\n\t" \
  "s_add_u32 %[st4], %[st], 4096\n\t" \
  "v_add_u32 v242, 16384, v242\n\t" \
  "s_cmp_eq_u32 %[step], 65\n\t" \
  "s_cbranch_scc0 .Ltn%=\n\t" \
  "v_mov_b32 v242, %[fg]\n\t" \
  ".Ltn%=:\n\t" \
  "buffer_load_dwordx4 v[176:179], v238, %[rt], %[st] offen offset:0 sc1\n\t" \
  "s_waitcnt vmcnt(7)\n\t" \
  "v_mfma_f32_16x16x32_bf16 v[132:135], v[68:71], v[180:183], v[132:135]\n\t" \
  "v_mfma_f32_16x16x32_bf16 v[140:143], v[76:79], v[180:183], v[140:143]\n\t" \
  "buffer_load_dwordx4 v[180:183], v238, %[rt], %[st] offen offset:1024 sc1\n\t" \
  "s_waitcnt vmcnt(7)\n\t" \
  "v_mfma_f32_16x16x32_bf16 v[128:131], v[80:83], v[184:187], v[128:131]\n\t" \
  "v_mfma_f32_16x16x32_bf16 v[136:139], v[88:91], v[184:187], v[136:139]\n\t" \
  "buffer_load_dwordx4 v[184:187], v238, %[rt], %[st4] offen offset:0 sc1\n\t" \
  "s_waitcnt vmcnt(7)\n\t" \
  "v_mfma_f32_16x16x32_bf16 v[132:135], v[84:87], v[188:191], v[132:135]\n\t" \
  "v_mfma_f32_16x16x32_bf16 v[140:143], v[92:95], v[188:191], v[140:143]\n\t" \
  "buffer_load_dwordx4 v[188:191], v238, %[rt], %[st4] offen offset:1024 sc1\n\t" \
  "s_waitcnt vmcnt(7)\n\t" \
  "v_mfma_f32_16x16x32_bf16 v[128:131], v[96:99], v[192:195], v[128:131]\n\t" \
  "v_mfma_f32_16x16x32_bf16 v[136:139], v[104:107], v[192:195], v[136:139]\n\t" \
  "buffer_load_dwordx4 v[192:195], v239, %[rt], %[st] offen offset:0 sc1\n\t" \
  "s_waitcnt vmcnt(7)\n\t" \
  "v_mfma_f32_16x16x32_bf16 v[132:135], v[100:103], v[196:199], v[132:135]\n\t" \
  "v_mfma_f32_16x16x32_bf16 v[140:143], v[108:111], v[196:199], v[140:143]\n\t" \
  "buffer_load_dwordx4 v[196:199], v239, %[rt], %[st] offen offset:1024 sc1\n\t" \
  "s_waitcnt vmcnt(7)\n\t" \
  "v_mfma_f32_16x16x32_bf16 v[128:131], v[112:115], v[200:203], v[128:131]\n\t" \
  "v_mfma_f32_16x16x32_bf16 v[136:139], v[120:123], v[200:203], v[136:139]\n\t" \
  "buffer_load_dwordx4 v[200:203], v239, %[rt], %[st4] offen offset:0 sc1\n\t" \
  "s_waitcnt vmcnt(7)\n\t" \
  "v_mfma_f32_16x16x32_bf16 v[132:135], v[116:119], v[204:207], v[132:135]\n\t" \
  "v_mfma_f32_16x16x32_bf16 v[140:143], v[124:127], v[204:207], v[140:143]\n\t" \
  "buffer_load_dwordx4 v[204:207], v239, %[rt], %[st4] offen offset:1024 sc1\n\t" \
  "s_nop 5\n\t" \
  "v_pk_add_f32 v[128:129], v[128:129], v[132:133]\n\t" \
  "v_pk_add_f32 v[130:131], v[130:131], v[134:135]\n\t" \
  "ds_write_b128 v230, v[128:131] offset:0\n\t" \
  "v_pk_add_f32 v[136:137], v[136:137], v[140:141]\n\t" \
  "v_pk_add_f32 v[138:139], v[138:139], v[142:143]\n\t" \
  "ds_write_b128 v230, v[136:139] offset:1024\n\t" \
  "v_add_u32 v230, 16384, v230\n\t" \
  "s_branch .Lt%=\n\t" \
  ".Ltl%=:\n\t" \
  "buffer_load_dwordx4 v[224:227], v243, %[rg], 0 offen sc1\n\t" \
  "s_waitcnt vmcnt(7)\n\t" \
  "v_mfma_f32_16x16x32_bf16 v[132:135], v[68:71], v[180:183], v[132:135]\n\t" \
  "v_mfma_f32_16x16x32_bf16 v[140:143], v[76:79], v[180:183], v[140:143]\n\t" \
  "s_waitcnt vmcnt(6)\n\t" \
  "v_mfma_f32_16x16x32_bf16 v[128:131], v[80:83], v[184:187], v[128:131]\n\t" \
  "v_mfma_f32_16x16x32_bf16 v[136:139], v[88:91], v[184:187], v[136:139]\n\t" \
  "s_waitcnt vmcnt(5)\n\t" \
  "v_mfma_f32_16x16x32_bf16 v[132:135], v[84:87], v[188:191], v[132:135]\n\t" \
  "v_mfma_f32_16x16x32_bf16 v[140:143], v[92:95], v[188:191], v[140:143]\n\t" \
  "s_waitcnt vmcnt(4)\n\t" \
  "v_mfma_f32_16x16x32_bf16 v[128:131], v[96:99], v[192:195], v[128:131]\n\t" \
  "v_mfma_f32_16x16x32_bf16 v[136:139], v[104:107], v[192:195], v[136:139]\n\t" \
  "s_waitcnt vmcnt(3)\n\t" \
  "v_mfma_f32_16x16x32_bf16 v[132:135], v[100:103], v[196:199], v[132:135]\n\t" \
  "v_mfma_f32_16x16x32_bf16 v[140:143], v[108:111], v[196:199], v[140:143]\n\t" \
  "s_waitcnt vmcnt(2)\n\t" \
  "v_mfma_f32_16x16x32_bf16 v[128:131], v[112:115], v[200:203], v[128:131]\n\t" \
  "v_mfma_f32_16x16x32_bf16 v[136:139], v[120:123], v[200:203], v[136:139]\n\t" \
  "s_waitcnt vmcnt(1)\n\t" \
  "v_mfma_f32_16x16x32_bf16 v[132:135], v[116:119], v[204:207], v[132:135]\n\t" \
  "v_mfma_f32_16x16x32_bf16 v[140:143], v[124:127], v[204:207], v[140:143]\n\t" \
  "s_nop 6\n\t" \
  "v_pk_add_f32 v[128:129], v[128:129], v[132:133]\n\t" \
  "v_pk_add_f32 v[130:131], v[130:131], v[134:135]\n\t" \
  "ds_write_b128 v230, v[128:131] offset:0\n\t" \
  "v_pk_add_f32 v[136:137], v[136:137], v[140:141]\n\t" \
  "v_pk_add_f32 v[138:139], v[138:139], v[142:143]\n\t" \
  "ds_write_b128 v230, v[136:139] offset:1024\n\t" \
  "s_waitcnt vmcnt(0) lgkmcnt(0)\n\t" \
  "s_branch .Lend%=\n\t" \
  ".Ltw%=:\n\t" \
  "s_add_u32 s92, s92, 1\n\t" \
  "s_sleep 2\n\t" \
  "s_and_b32 s93, s92, 63\n\t" \
  "s_cbranch_scc1 .Ltr%=\n\t" \
  "buffer_load_dword v164, off, %[rs], %[sab] sc1\n\t" \
  "s_waitcnt vmcnt(0)\n\t" \
  "v_readfirstlane_b32 s93, v164\n\t" \
  "s_nop 3\n\t" \
  "s_cmp_eq_u32 s93, 1\n\t" \
  "s_cbranch_scc1 .Lto%=\n\t" \
  "s_cmp_lt_u32 s92, 0x80000\n\t" \
  "s_cbranch_scc1 .Ltr%=\n\t" \
  "v_mov_b32 v164, 1\n\t" \
  "buffer_store_dword v164, off, %[rs], %[sab] sc1\n\t" \
  "s_waitcnt vmcnt(0)\n\t" \
  "s_branch .Lto%=\n\t" \
  ".Ltr%=:\n\t" \
  "buffer_load_dword v166, v242, %[rs], 0 offen sc1\n\t" \
  "s_waitcnt vmcnt(0)\n\t" \
  "s_branch .Ltc%=\n\t" \
  ".Llast%=:\n\t" \
  "v_mov_b32 v173, v242\n\t" \
  "s_branch .Lgol%=\n\t" \
  ".Lfw%=:\n\t" \
  "s_add_u32 s92, s92, 1\n\t" \
  "s_sleep 2\n\t" \
  "s_and_b32 s93, s92, 63\n\t" \
  "s_cbranch_scc1 .Lfr%=\n\t" \
  "buffer_load_dword v164, off, %[rs], %[sab] sc1\n\t" \
  "s_waitcnt vmcnt(0)\n\t" \
  "v_readfirstlane_b32 s93, v164\n\t" \
  "s_nop 3\n\t" \
  "s_cmp_eq_u32 s93, 1\n\t" \
  "s_cbranch_scc1 .Lfo%=\n\t" \
  "s_cmp_lt_u32 s92, 0x80000\n\t" \
  "s_cbranch_scc1 .Lfr%=\n\t" \
  "v_mov_b32 v164, 1\n\t" \
  "buffer_store_dword v164, off, %[rs], %[sab] sc1\n\t" \
  "s_waitcnt vmcnt(0)\n\t" \
  "s_branch .Lfo%=\n\t" \
  ".Lfr%=:\n\t" \
  "buffer_load_dwordx2 v[166:167], v173, %[rs], 0 offen sc1\n\t" \
  "s_branch .Lfc%=\n\t" \
  ".Lb1%=:\n\t" \
  "s_waitcnt vmcnt(0) lgkmcnt(0)\n\t" \
  "s_barrier\n\t" \
  "s_branch .Lb2%=\n\t" \
  ".Lbl%=:\n\t" \
  "s_bitset1_b64 %[ps], %[step]\n\t" \
  "s_cmp_lt_u32 s92, 3\n\t" \
  "s_cbranch_scc1 .Lbm%=\n\t" \
  "s_sleep 12\n\t" \
  ".Lbm%=:\n\t" \
  "s_add_u32 s92, s92, 1\n\t" \
  "s_sleep 2\n\t" \
  "s_and_b32 s93, s92, 63\n\t" \
  "s_cbranch_scc1 .Lba%=\n\t" \
  "buffer_load_dword v164, off, %[rs], %[sab] sc1\n\t" \
  "s_waitcnt vmcnt(0)\n\t" \
  "v_readfirstlane_b32 s93, v164\n\t" \
  "s_nop 3\n\t" \
  "s_cmp_eq_u32 s93, 1\n\t" \
  "s_cbranch_scc1 .Lgo%=\n\t" \
  "s_cmp_lt_u32 s92, 0x80000\n\t" \
  "s_cbranch_scc1 .Lba%=\n\t" \
  "v_mov_b32 v164, 1\n\t" \
  "buffer_store_dword v164, off, %[rs], %[sab] sc1\n\t" \
  "s_waitcnt vmcnt(0)\n\t" \
  "s_branch .Lgo%=\n\t" \
  ".Lba%=:\n\t" \
  "buffer_load_dwordx2 v[166:167], v173, %[rs], 0 offen sc1\n\t" \
  "s_branch .Lcal%=\n\t" \
  ".Lslow%=:\n\t" \
  "s_mov_b64 s[94:95], 0\n\t" \
  "v_cmp_eq_u32 vcc, %[sent], v128\n\t" \
  "s_or_b64 s[94:95], s[94:95], vcc\n\t" \
  "v_cmp_eq_u32 vcc, %[sent], v129\n\t" \
  "s_or_b64 s[94:95], s[94:95], vcc\n\t" \
  "v_cmp_eq_u32 vcc, %[sent], v130\n\t" \
  "s_or_b64 s[94:95], s[94:95], vcc\n\t" \
  "v_cmp_eq_u32 vcc, %[sent], v131\n\t" \
  "s_or_b64 s[94:95], s[94:95], vcc\n\t" \
  "v_cmp_eq_u32 vcc, %[sent], v132\n\t" \
  "s_or_b64 s[94:95], s[94:95], vcc\n\t" \
  "v_cmp_eq_u32 vcc, %[sent], v133\n\t" \
  "s_or_b64 s[94:95], s[94:95], vcc\n\t" \
  "v_cmp_eq_u32 vcc, %[sent], v134\n\t" \
  "s_or_b64 s[94:95], s[94:95], vcc\n\t" \
  "v_cmp_eq_u32 vcc, %[sent], v135\n\t" \
  "s_or_b64 s[94:95], s[94:95], vcc\n\t" \
  "v_cmp_eq_u32 vcc, %[sent], v136\n\t" \
  "s_or_b64 s[94:95], s[94:95], vcc\n\t" \
  "v_cmp_eq_u32 vcc, %[sent], v137\n\t" \
  "s_or_b64 s[94:95], s[94:95], vcc\n\t" \
  "v_cmp_eq_u32 vcc, %[sent], v138\n\t" \
  "s_or_b64 s[94:95], s[94:95], vcc\n\t" \
  "v_cmp_eq_u32 vcc, %[sent], v139\n\t" \
  "s_or_b64 s[94:95], s[94:95], vcc\n\t" \
  "v_cmp_eq_u32 vcc, %[sent], v140\n\t" \
  "s_or_b64 s[94:95], s[94:95], vcc\n\t" \
  "v_cmp_eq_u32 vcc, %[sent], v141\n\t" \
  "s_or_b64 s[94:95], s[94:95], vcc\n\t" \
  "v_cmp_eq_u32 vcc, %[sent], v142\n\t" \
  "s_or_b64 s[94:95], s[94:95], vcc\n\t" \
  "v_cmp_eq_u32 vcc, %[sent], v143\n\t" \
  "s_or_b64 s[94:95], s[94:95], vcc\n\t" \
  "s_cbranch_scc0 .Lok%=\n\t" \
  "s_bitset1_b64 %[fm], %[step]\n\t" \
  "s_add_u32 s92, s92, 1\n\t" \
  "s_sleep 1\n\t" \
  "s_and_b32 s93, s92, 63\n\t" \
  "s_cbranch_scc1 .Lre%=\n\t" \
  "buffer_load_dword v164, off, %[rs], %[sab] sc1\n\t" \
  "s_waitcnt vmcnt(0)\n\t" \
  "v_readfirstlane_b32 s93, v164\n\t" \
  "s_nop 3\n\t" \
  "s_cmp_eq_u32 s93, 1\n\t" \
  "s_cbranch_scc1 .Lok%=\n\t" \
  "s_cmp_lt_u32 s92, 0x80000\n\t" \
  "s_cbranch_scc1 .Lre%=\n\t" \
  "v_mov_b32 v164, 1\n\t" \
  "buffer_store_dword v164, off, %[rs], %[sab] sc1\n\t" \
  "s_waitcnt vmcnt(0)\n\t" \
  "s_branch .Lok%=\n\t" \
  ".Lre%=:\n\t" \
  "buffer_load_dwordx4 v[128:131], v234, %[rs], %[so] offen sc1\n\t" \
  "s_nop 15\n\t" \
  "buffer_load_dwordx4 v[132:135], v235, %[rs], %[so] offen sc1\n\t" \
  "s_nop 15\n\t" \
  "buffer_load_dwordx4 v[136:139], v236, %[rs], %[so] offen sc1\n\t" \
  "s_nop 15\n\t" \
  "buffer_load_dwordx4 v[140:143], v237, %[rs], %[so] offen sc1\n\t" \
  "v_mov_b64 v[144:145], 0\n\t" \
  "v_mov_b64 v[146:147], 0\n\t" \
  "v_mov_b64 v[148:149], 0\n\t" \
  "v_mov_b64 v[150:151], 0\n\t" \
  "v_mov_b64 v[152:153], 0\n\t" \
  "v_mov_b64 v[154:155], 0\n\t" \
  "v_mov_b64 v[156:157], 0\n\t" \
  "v_mov_b64 v[158:159], 0\n\t" \
  "s_waitcnt vmcnt(0)\n\t" \
  "s_branch .Lw%=\n\t" \
  ".Lend%=:\n\t"


__device__ __forceinline__ int look_prog(__amdgpu_buffer_rsrc_t rs, int off) {
  asm volatile("" ::: "memory");
  const int v = (int)ld_flag(rs, off);
  return min(__builtin_amdgcn_readlane(v, 0), __builtin_amdgcn_readlane(v, 1));
}
__device__ __forceinline__ int wait_prog(__amdgpu_buffer_rsrc_t rs, int off, int need, gu32* abortw) {
  int v;
  for (unsigned spins = 0;;) {
    v = look_prog(rs, off);
    if (v >= need) break;
    __builtin_amdgcn_s_sleep(4);
    if (spin_fail(spins, abortw)) break;
  }
  asm volatile("" ::: "memory");
  return v;
}
#define CLB_S "s90", "s91", "s92", "s93", "s94", "s95", "vcc", "scc", "memory"
#define W_IN "{v[0:31]}"(W[0]), "{v[32:63]}"(W[1]), "{v[64:95]}"(W[2]), "{v[96:127]}"(W[3]), "{v[128:159]}"(W[4]), "{v[160:191]}"(W[5])

#define LEAD0 408
#define LEAD_N0 5
#define LEAD_SH 2
#define LEAD_X0 8
#define LEAD_U0 24
#define LEAD_D0 0
#define T0_BACK 1000
#define QBJ 3
constexpr int L_MEM = 0, L_KT = 16384, L_QX = 24576, L_KX = 26624, L_VX = 16384, L_MISC = 30720, L_WL = 32768;

#define QK_STEP(TXT)                                                                                                        \
  asm volatile(TXT                                                                                                          \
      : [tout] "=&s"(tout), [spins] "=&s"(spins), [tb1] "=&s"(tb1), [tgt] "=&s"(tgt), "+{v248}"(aL16), "+{v249}"(aMW), "+{v250}"(aWL),                            \
        "+{v251}"(aIDX), "+{v252}"(aQW), "+{v[232:235]}"(QF)                                                                 \
      : W_IN, [rs] "s"(rs4), [som] "s"(o_mem(pm, s) + WV * 1024), [som2] "s"(o_mem(pm, s) + WV * 1024 + 16384), [sent] "s"(DSENT), [sab] "s"(SY_ABORT), \
        [lead] "s"(lead), [step] "s"(s), [sbc] "s"(SY_BA + side(pm, 1) + h * 128), [nh] "s"(0xFF00FF00FF00FF00ull), [up] "s"(0xFFFFFFFF00000000ull), [negc] "s"(-cexp),      \
        [c] "s"(cexp), [so] "s"(o_o(pm, s, h) + WV * 512), [sk] "s"(o_v(pm, s, h) + 2048 + (WV & 3) * 512),                    \
        [sv] "s"(o_v(pm, s, h) + WV * 512), [rt] "s"(rt4), [svt] "s"(t_off(p, T_V + kseg(s, h)) + WV * 2048)                  \
      : CLB8(192, 193, 194, 195, 196, 197, 198, 199), CLB8(200, 201, 202, 203, 204, 205, 206, 207),                          \
        CLB8(208, 209, 210, 211, 212, 213, 214, 215), CLB8(216, 217, 218, 219, 220, 221, 222, 223),                          \
        CLB8(224, 225, 226, 227, 228, 229, 230, 231), CLB8(236, 237, 238, 239, 240, 241, 242, 243), "v244", "v245", "v246", "v247", "v253", "s88", "s89", CLB_S)

template <int WV>
__device__ __forceinline__ void qk_wave(Args& p, int h, char* smem) {
  const int tid = threadIdx.x;
  unsigned pm = 0;
  int have = -1;
  u32x32 W[6];
  {
    int lane = tid & 63;
    asm volatile("" : "+v"(lane));
    const unsigned vo = (WV * 4 + (lane >> 4)) * (HD * 2) + 16 * ((lane & 15) ^ swzB((WV * 4 + (lane >> 4)) & 15));
    auto stage = [&](int j) {
#pragma unroll
      for (int mm = 0; mm < 2; ++mm) {
        const char* src = (const char*)((mm ? p.Wk : p.Wq) + (size_t)j * 128 * HD + h * DH);
        const unsigned dst = lds_off(smem) + (~j & 1) * 65536 + mm * 32768 + WV * 1024;
        glds2(src, 32 * HD * 2, vo, dst);
        if (TH_Q < 63) HOLD_V((h & 3) ^ 1, TH_Q);
        glds2(src + 64 * HD * 2, 32 * HD * 2, vo, dst + 16384);
        if (TH_Q < 63) HOLD_V((h & 3) ^ 1, TH_Q);
      }
    };
    bf16x8 keep[4][4];
    int f0 = -1;
    auto place = [&](int g) {
#pragma unroll
      for (int i = 0; i < 4; ++i)
        *reinterpret_cast<bf16x8*>(smem + (g == 0 ? 65536 : g == 1 ? 32768 : g == 2 ? 131072 : 98304) + WV * 4096 + i * 1024 + lane * 16) = keep[g][i];
    };
    stage(0); stage(1);
    Boot bt;
#pragma unroll
    for (int j = 0; j < 8; ++j) {
      if (j < 7) WAIT_V(8); else WAIT_V(0);
      if (WV == 7 && j == QBJ + 1) pm = boot_done(p.ws, bt);
      if ((WV == 4 || WV == 5) && j == 7) {
        asm volatile("" : "+v"(f0));
        have = min(__builtin_amdgcn_readlane(f0, 0), __builtin_amdgcn_readlane(f0, 1));
      }
      LBAR();
#pragma unroll
      for (int k = 0; k < 8; ++k) {
        const int P = (QK_MAP[WV] >> (4 * (2 * j + (k >> 2)))) & 15;
        const bf16x8 fr = ld_bfrag(smem + (~j & 1) * 65536 + (WV >> 2) * 32768 + (k >> 2) * 16384, k & 1, 2 * (WV & 3) + ((k >> 1) & 1), lane);
        const int f = P * 4 + (k & 3);
        if (P < 12) put4(W[f >> 3], f & 7, fr);
        else keep[P - 12][k & 3] = fr;
      }
      if (j == (WV >> 1)) place(2);
      LBAR();
      if (WV == 7 && j == QBJ) boot_ask(p.ws, bt);
      if (j + 2 < 8) stage(j + 2);
      if (j == 6) {
        if (WV == 4 || WV == 5)
          asm volatile("buffer_load_dword %0, %1, %2, 0 offen sc1" : "=&v"(f0) : "v"(fkq(h, (lane & 1) * 2)), "s"(raw_rsrc(p.ws + WS_SY, SY_TOTAL)) : "memory");
        place(0); place(3);
      }
    }
    place(1);
    if (WV == 7 && lane < 8) lds_at(smem + L_MISC)[lane] = lane == 7 ? pm : (lane == 1 ? now_q() - T0_BACK : 0);
    if (WV == 6) lds_at(smem + 28672)[lane] = ~0u;
    LBAR();
    pm = uni(lds_at(smem + L_MISC)[7]);
    p.s0 = pm >> 31;
    if (WV == 4 || WV == 5) {
      if (have < 0) have = wait_prog(mkrsrc(p.ws + WS_SY, SY_TOTAL), fkq(h, (lane & 1) * 2), 0, (gu32*)(p.ws + WS_SY + SY_ABORT));
      const char* src = p.ws + WS_T + t_off(p, kseg(0, h)) + (WV - 4) * 4096 + lane * 16;
#pragma unroll
      for (int i = 0; i < 4; ++i) glds16_sc1(src + i * 1024, smem + L_KT + (WV - 4) * 4096 + i * 1024);
    }
  }
  const __amdgpu_buffer_rsrc_t rsy = mkrsrc(p.ws + WS_SY, SY_TOTAL);
  const u32x4 rs4 = raw_rsrc(p.ws + WS_SY, SY_TOTAL);
  const u32x4 rt4 = raw_rsrc(p.ws + WS_T, T_BYTES);
  gu32* abortw = (gu32*)(p.ws + WS_SY + SY_ABORT);
  const float cexp = 0.08838834764831845f * 1.4426950408889634f;
  constexpr int m = WV >> 2, b = WV & 3;
  int lead = LEAD0;
  unsigned tb1 = 0;
  unsigned long long fm = 0, ps = 0; (void)fm; (void)ps;
  for (int s = 0; s < NSEG; ++s) {
    int lane = tid & 63;
    asm volatile("" : "+v"(lane));
    const int r = lane & 15, q = lane >> 4;
    unsigned aL16 = lane * 16, aMW = aL16 + WV * 1024, aWL = aL16 + 98304 + WV * 4096;
    unsigned aIDX = (lane & 2) ? 0xEEEE : 0x4444;
    unsigned aQW = (32 * b + r) * 16 + q * 4 + (m ? L_KX : L_QX);
    u32x4 QF;
    asm volatile("" : "={v[232:235]}"(QF));
    if (WV < 4) {
      QF[0] = L_QX + (q * 8 + (r >> 2)) * 16 + (r & 1) * 8;
      QF[1] = ((2 * (q & 1) + (q >> 1)) * 16 + r) * 4;
      QF[2] = ((r >> 2) & 1) * 65536 + ((r >> 1) & 1) * 64 + (r >> 3) * 32 + (r & 1) * 16 + (q >> 1) * 256 + (q & 1) * 8;
    } else QF[3] = r * 16 + q * 4;
    unsigned tout, spins, tgt;
    if (WV == 0) QK_STEP(QK_ASM_0); else if (WV == 1) QK_STEP(QK_ASM_1); else if (WV == 2) QK_STEP(QK_ASM_2);
    else if (WV == 3) QK_STEP(QK_ASM_3); else if (WV == 4) QK_STEP(QK_ASM_4); else if (WV == 5) QK_STEP(QK_ASM_5);
    else if (WV == 6) QK_STEP(QK_ASM_6); else QK_STEP(QK_ASM_7);
    tout = __builtin_amdgcn_readfirstlane(tout);
    tb1 = __builtin_amdgcn_readfirstlane(tb1);
    lane = tid & 63;
    asm volatile("" : "+v"(lane));
    if (WV < 4) {
      lds_at(smem + L_MISC + 8)[WV] = s + 1;
      if (WV == 3) { lds_at(smem + L_MISC)[6] = time_of(tout); lds_at(smem + 28672)[2] = s; }
    }
    if (WV == 6) {
      if (s + 1 < NSEG) watch(rsy, s, lane, now_q(), (const unsigned*)(smem + L_MISC), abortw, side(pm, 1));
    } else if (WV >= 4 && s + 1 < NSEG) {
      unsigned t;
      do {
        __builtin_amdgcn_s_sleep(1);
        t = lds_at(smem + L_MISC + 8)[lane & 3];
      } while (!__all(t == (unsigned)(s + 1)));
      if (WV <= 5) {
        if (s + 2 < NSEG) {
          if (have < s + 1) have = wait_prog(rsy, fkq(h, (lane & 1) * 2), s + 1, abortw);
          const char* src = p.ws + WS_T + t_off(p, kseg(s + 1, h)) + (WV - 4) * 4096 + lane * 16;
#pragma unroll
          for (int i = 0; i < 4; ++i) glds16_sc1(src + i * 1024, smem + L_KT + (WV - 4) * 4096 + i * 1024);
          if (have < s + 2) have = look_prog(rsy, fkq(h, (lane & 1) * 2));
        }
      } else {
        do __builtin_amdgcn_s_sleep(2); while (__builtin_amdgcn_readfirstlane(lds_at(smem + 28672)[2]) != (unsigned)s);
        const int x = (int)(__builtin_amdgcn_readfirstlane(lds_at(smem + L_MISC)[6]) - tb1);
        lead = __builtin_amdgcn_readfirstlane(track(lead, x + (s < LEAD_N0 ? LEAD_X0 : 0), s < LEAD_N0 ? LEAD_U0 : LEAD_UP, s < LEAD_N0 ? LEAD_D0 : (s < 3 * LEAD_N0 ? min(max(LEAD_DN, (lead - x) >> LEAD_SH), 16) : LEAD_DN), 60));
      }
    }
  }
}

__device__ __forceinline__ void qk_role(Args& p, int h, char* smem) {
  switch (__builtin_amdgcn_readfirstlane(threadIdx.x >> 6)) {
    case 0: qk_wave<0>(p, h, smem); break;
    case 1: qk_wave<1>(p, h, smem); break;
    case 2: qk_wave<2>(p, h, smem); break;
    case 3: qk_wave<3>(p, h, smem); break;
    case 4: qk_wave<4>(p, h, smem); break;
    case 5: qk_wave<5>(p, h, smem); break;
    case 6: qk_wave<6>(p, h, smem); break;
    default: qk_wave<7>(p, h, smem); break;
  }
}

#define K_DUE 160
#define T_SL 2
#define O_DUE 400

constexpr int V_VM = 16384, V_T = 32768, V_KI = 131072;
__device__ __forceinline__ void vu_role(Args& p, int h, char* smem) {
  const int tid = threadIdx.x, lane = tid & 63;
  const int w = __builtin_amdgcn_readfirstlane(tid >> 6);
  u32x32 W[4];
  {
    const unsigned vo = (tid >> 4) * (HD * 2) + 16 * ((lane & 15) ^ swzB((tid >> 4) & 15));
    auto stage = [&](int j) {
      const char* src = (const char*)(p.Wv + (size_t)j * 128 * HD + h * DH);
      const unsigned dst = lds_off(smem) + L_WL + (j & 3) * 32768 + w * 1024;
      glds2(src, 32 * HD * 2, vo, dst);
      glds2(src + 64 * HD * 2, 32 * HD * 2, vo, dst + 16384);
    };
    start_sy(p.ws, p.mem0, h, smem);
    Boot bt;
#pragma unroll
    for (int u = 0; u < 2; ++u) {
#pragma unroll
      for (int j = 0; j < 4; ++j) stage(4 * u + j);
      if (u && w == 7) boot_ask(p.ws, bt);
      WAIT_V(0);
      if (u && w == 7) {
        const unsigned m = boot_done(p.ws, bt);
        if (lane < 8) lds_at(smem + L_MISC)[lane] = lane == 7 ? m : (lane == 1 ? now_q() : 0);
      }
      LBAR();
#pragma unroll
      for (int k = 0; k < 16; ++k) put4(W[2 * u + (k >> 3)], k & 7, ld_bfrag(smem + L_WL + ((k >> 1) ^ w) * 16384, k & 1, w, lane));
      LBAR();
    }
  }
  const __amdgpu_buffer_rsrc_t rsy = mkrsrc(p.ws + WS_SY, SY_TOTAL);
  const u32x4 rs4 = raw_rsrc(p.ws + WS_SY, SY_TOTAL);
  gu32* abortw = (gu32*)(p.ws + WS_SY + SY_ABORT);
  const unsigned pm = uni(lds_at(smem + L_MISC)[7]);
  p.s0 = pm >> 31;
  unsigned long long fm = 0; (void)fm;
  int have = -1;
  auto fetch = [&](int sg) {
    if (have < sg) have = wait_prog(rsy, fkq(h, w - 4), sg, abortw);
    const char* src = p.ws + WS_T + t_off(p, (w - 4) * T_Q + kseg(sg, h)) + lane * 16;
#pragma unroll
    for (int i = 0; i < 8; ++i) glds16_sc1(src + i * 1024, smem + V_T + (sg & 3) * 24576 + (w - 4) * 8192 + i * 1024);
  };
  const int fo = SY_FO + ((w & 3) * NH + h) * 128;
  typedef __attribute__((ext_vector_type(8))) float f32x8;
  f32x8 bias;
  typedef __attribute__((ext_vector_type(8))) unsigned u32x8;
  u32x8 ca;
#pragma unroll
  for (int i = 0; i < 8; ++i) ca[i] = lane * 16 + (i ^ w) * 1024;
  u32x4 ta;
  {
    const int r = lane & 15, q = lane >> 4;
#pragma unroll
    for (int j = 0; j < 8; ++j) bias[j] = (j >> 2) * 16 + 4 * q + (j & 3) <= 16 * (w & 1) + r ? INFINITY : -INFINITY;
    ta[0] = V_KI + w * 2048 + lane * 16;
    ta[1] = V_KI + w * 2048 + (q * 8 + (r >> 2)) * 16 + (r & 1) * 8;
    ta[2] = V_VM + (w & 2) * 512 + ((2 * (q & 1) + (q >> 1)) * 16 + r) * 4;
    ta[3] = 0;
  }
#pragma unroll 1
  for (int s = 0; s < NSEG; ++s) {
    unsigned spins, tgt;
    asm volatile(VV_ASM
        : [spins] "=&s"(spins), [tgt] "=&s"(tgt)
        : W4_IN, "{v200}"(lane * 16), "{v[232:239]}"(ca), "{v202}"((lane & 2) ? 0xEEEE : 0x4444), "{v203}"(lane * 4),
          "{v204}"(tid * 4 + V_VM + (s & 1) * 2048), [rs] "s"(rs4), [som] "s"(o_mem(pm, s) + w * 1024), [som2] "s"(o_mem(pm, s) + w * 1024 + 16384), [step] "s"(s),
          [sent] "s"(DSENT), [sab] "s"(SY_ABORT), [sv] "s"(o_v(pm, s, h) + w * 256)
        : CLB32(128), CLB32(160), CLB8(192, 193, 194, 195, 196, 197, 198, 199), CLB_S);
    if (w < 4 || s == NSEG - 1) {
      const int sg = s - (w < 4);
      if (w >= 4) pace_until(now_q() + K_DUE);
      if (sg >= 0) {
        asm volatile(TK_ASM
            :
            : "{v200}"(lane * 16), "{v[208:215]}"(bias), "{v[216:219]}"(ta),
              [rs] "s"(rs4), [rt] "s"(raw_rsrc(p.ws + WS_T, T_BYTES)), [sent] "s"(DSENT), [sab] "s"(SY_ABORT), [sk] "s"(o_v(pm, sg, h) + 2048),
              [tl] "s"(V_T + (sg & 3) * 24576), [vp] "s"((sg & 1) * 2048), [tq] "s"((w & 1) * 4096), [tv] "s"((w & 2) * 2048),
              [so] "s"(t_off_o(p, T_O + (sg * 2 + (w & 1)) * 16 + (h >> 1)) + (h & 1) * 4096 + (w & 2) * 1024),
              [up] "s"(0xFFFFFFFF00000000ull), [c] "s"(1.4426950408889634f), [negc] "s"(-1.4426950408889634f),
              [sc] "s"((unsigned long long)__float_as_uint(0.08838834764831845f)), [od] "s"(s < NSEG - 1 ? O_DUE : 0)
            : CLB32(128), CLB32(160), CLB8(192, 193, 194, 195, 196, 197, 198, 199), CLB8(224, 225, 226, 227, 228, 229, 230, 231),
              "v205", "v206", "v207", "s88", "s89", CLB_S);
        if (sg >= NSEG - 3) {
          static_assert(TLAG == 2, "");
          WAIT_V(0);
          if (lane == 0) __builtin_amdgcn_raw_buffer_store_b32(1u, rsy, fo + (sg - NSEG + 3) * 4 * NH * 128, 0, 16);
        }
      }
    } else if (w == 7) {
      watch(rsy, s, lane, now_q(), (const unsigned*)(smem + L_MISC), abortw, side(pm, 1));
    } else if (w >= 4) {
      __builtin_amdgcn_s_sleep(T_SL);
      const int s1 = w == 5 ? (s == NSEG - 2 ? s + 1 : s) : s + 1;
#pragma unroll 1
      for (int sg = (w == 5 || s == 0) ? s : s + 1; sg <= s1; ++sg) fetch(sg);
      if (have < s1 + 1 && s1 + 1 < NSEG) have = look_prog(rsy, fkq(h, w - 4));
      if (w == 5 && s + NPRE < NSEG) {
#pragma unroll
        for (int i = 0; i < 4; ++i) st_sc1(rsy, o_v(pm, s + NPRE, h) + i * 1024 + lane * 16, (u32x4){DSENT, DSENT, DSENT, DSENT});
      }
    }
  }
}

#define RL_UP 6
#define RL_DN 4
#define RED_MIN 40
#define BA_LOOK 360
constexpr int R_SI = 32768, R_PART = 98304;

__device__ __forceinline__ void red_role(Args& p, int j, char* smem) {
  const int tid = threadIdx.x;
  int lane = tid & 63;
  const int w = __builtin_amdgcn_readfirstlane(tid >> 6);
  const int c = (j & 2) * 8 + (j >> 2), rh = j & 1;
  const int hg = (w + (j >> 2)) & 7, rot = (j >> 2) + (j >> 5);
  const __amdgpu_buffer_rsrc_t rsy = mkrsrc(p.ws + WS_SY, SY_TOTAL);
  const u32x4 rs4 = raw_rsrc(p.ws + WS_SY, SY_TOTAL);
  const __amdgpu_buffer_rsrc_t rt = mkrsrc(p.ws + WS_T, T_BYTES);
  gu32* abortw = (gu32*)(p.ws + WS_SY + SY_ABORT);
  u32x32 W[4];
  u32x2 tag = {0u, 0u};
  {
    const u16* Wo = p.Wo + (size_t)(4 * hg * DH) * DM + 32 * c;
    const int ro = (lane >> 2) * 64 + (lane & 3) * 8;
    Boot bt;
    load_blocks<16>(smem + 32768 + w * 16384,
        [&](int i, char* slot) {
          if (i == 12 && w == 0) boot_ask(p.ws, bt);
          if ((i < 2 || i == 4 || i == 8) && (c & 14) != 6 && lane < 10) {
            const int x = i < 2 ? 3 - i : i >> 3, n = min(((c & 15) - ((c & 8) >> 2)) * 10 + lane, 127);
            touch_line((const char*)p.Wo + (size_t)(w * 512 + (x >> 1) * 256 + (x & 1) * 64 + (n & 64) + n) * (DM * 2) + (c & 16) * 64 + 384, smem + w * 256);
          }
          const int x = ((i >> 2) + rot) & 3;
          blk_dma(Wo + (size_t)((x >> 1) * 256 + (x & 1) * 64 + ((i >> 1) & 1) * 128 + (i & 1) * 32) * DM, DM, slot, lane);
        },
        [&](int i, char* slot) {
#pragma unroll
          for (int t = 0; t < 2; ++t) {
            s16x4 lo = __builtin_amdgcn_ds_read_tr16_b64_v4i16((lds_s16x4_ptr)(slot + ro + t * 32));
            s16x4 hi = __builtin_amdgcn_ds_read_tr16_b64_v4i16((lds_s16x4_ptr)(slot + ro + t * 32 + 1024));
            put4(W[i >> 2], (i & 2) * 2 + 2 * t + (i & 1), (bf16x8){lo[0], lo[1], lo[2], lo[3], hi[0], hi[1], hi[2], hi[3]});
          }
        });
    if (w == 0) { lds_at(smem + 4128)[0] = boot_done(p.ws, bt); tag = next_tag(bt.e); }
    LBAR();
  }
  const unsigned pm = uni(lds_at(smem + 4128)[0]);
  p.s0 = pm >> 31;
  const int hs = side(pm, 1);
  {
    const u32x4 ds = {DSENT, DSENT, DSENT, DSENT};
#pragma unroll
    for (int i = 0; i < 2; ++i) {
      const int s = (w * 2 + i) * 4 + (lane >> 4);
      if (s >= 2) st_sc1(rsy, o_mem(pm, s) + (c >> 4) * 16384 + (c & 15) * 512 + rh * 128 + ((lane >> 3) & 1) * 256 + (lane & 7) * 16, ds);
    }
  }
  if (tid < 4) { reinterpret_cast<unsigned*>(smem + 4096)[tid] = tid == 3 ? now_q() : 0; reinterpret_cast<unsigned*>(smem + 4112)[tid] = DSENT; }
  if (tid < 256) reinterpret_cast<int*>(smem + 5120)[tid] = ((tid >> 2) - TLAG - 6) >> 2;
  LBAR();
  const unsigned base = __builtin_amdgcn_readfirstlane(lds_at(smem + 4096)[3]);
  unsigned long long fm = 0, ps = 0;
  const unsigned aL = lane * 16;
  const int e = ((56 + w) * SEG + rh * 16 + (lane >> 2)) * DM + c * 32 + (lane & 3) * 8;
  u32x4 xv = *reinterpret_cast<const u32x4*>(p.x + e), av = {DSENT, DSENT, DSENT, DSENT};
  asm volatile("" : "+v"(xv));
  {
    const int r = lane & 15, q = lane >> 4;
    const int sm0 = o_mem(pm, 0) + (c >> 4) * 16384 + (c & 15) * 512 + rh * 128;
    u32x4 VO, VT;
    unsigned fo = SY_FO + ((rh + (lane & 4) / 2) * NH + 4 * hg + (lane & 3)) * 128;
    unsigned ao = w == 1 ? (c * S_LEN + rh * 16 - (TLAG + 3) * SEG) * 64 + lane * 16
                         : (w >= 4 ? SY_O + NPRE * 131072 + side(pm, w - 2) + (j >> 3) * 16384 + (j & 7) * 1024 + lane * 16
                                   : SY_FS + (2 * c + rh) * 128 + (lane < 8 ? lane * 16 : 0x70000000));
    unsigned ar = w == 1 ? R_SI - (TLAG + 2) * 1024 + lane * 16 : (w == 2 ? 5120 : 4112);
    const unsigned long long am = w == 1 ? ((1ull << 56) - 1) << (TLAG + 3) : (w == 2 ? 0x11111111111111ull << (TLAG + 7) : (w >= 4 ? (0xFFFFull << (16 * (w - 4))) >> NPRE : 0));
    unsigned pw = R_PART + w * 2048 + lane * 16, pr = R_PART + 16384 + tid * 4;
    unsigned sw = R_SI - (TLAG + 1) * 1024 + (lane >> 2) * 64 + w * 8 + (lane & 3) * 2, stm = 8192 + w * 512;
#pragma unroll
    for (int l = 0; l < 4; ++l) {
      const int x = (l + rot) & 3;
      VO[l] = o_o(0, 0, 4 * hg) + rh * 65536 + x * 1024 + aL;
      VT[l] = (T_O + rh * 16 + 2 * hg + (x >> 1)) * 16384 + (x & 1) * 2048 + aL;
    }
    unsigned ba = lane < 4 ? SY_BA + hs + (4 * hg + lane) * 128 : 0x7F000000u;
    unsigned sm = 0;
    int rl = RL0;
    unsigned step, E, so, st, st4, sh;
    asm volatile(RED_ASM
        : [step] "=&s"(step), [E] "=&s"(E), [so] "=&s"(so), [st] "=&s"(st), [st4] "=&s"(st4), [sh] "=&s"(sh),
          [rl] "+s"(rl), [sm] "+s"(sm), [fm] "+s"(fm), [ps] "+s"(ps),
          "+{v[224:227]}"(av), "+{v228}"(ao), "+{v229}"(ar), "+{v230}"(pw), "+{v231}"(pr), "+{v232}"(sw), "+{v233}"(stm), "+{v242}"(fo), "+{v173}"(ba)
        : W4_IN, "{v168}"(aL), "{v169}"(w * 512 + lane * 8), "{v170}"(lane * 8), "{v174}"((r & 1) ? 0x03020706u : 0x05040100u),
          "{v171}"(sm0 + 32768 + (q >> 1) * 256 + (q & 1) * 64 + ((r >> 1) & 1) * 32 + (r & 1) * 16 + (r >> 2) * 4),
          "{v172}"((lane & 2) ? 0xEEEE : 0x4444),
          "{v175}"(w == 3 && lane < 32 ? SY_BR + hs + j * 128 + lane * 4 : 0x7F000000u), "{v[234:237]}"(VO), "{v[238:241]}"(VT), "{v243}"(e * 2),
          [rg] "s"(raw_rsrc(p.ws + WS_G, S_LEN * DM * 2)), [fg] "s"(SY_FG + ((14 + (w >> 2)) * 8 + (c >> 2)) * 128),
          [rs] "s"(rs4), [ra] "s"(w == 1 ? raw_rsrc(p.ws + WS_S, S_LEN * DM * 2) : rs4), [rt] "s"(raw_rsrc(p.ws + WS_T, T_BYTES)),
          [ds] "n"(DS_RED), [wm] "n"(REF_W), [gm] "n"(REF_G), [mn] "n"(RED_MIN), [lk] "n"(DS_MEM + BA_LOOK), [up] "n"(RL_UP), [dn] "n"(-RL_DN), [sent] "s"(DSENT), [sab] "s"(SY_ABORT),
          [base] "s"(base), [pm] "s"(pm), [s0] "s"((p.s0 ^ TF_O) << 13), [am] "s"(am), [ainc] "s"(w == 1 ? SEG * 64 : (w >= 4 ? 131072 : 0)),
          [rinc] "s"(w == 1 ? 1024 : (w == 2 ? 16 : 0))
        : CLB32(128), CLB8(160, 161, 162, 163, 164, 165, 166, 167), CLB8(176, 177, 178, 179, 180, 181, 182, 183),
          CLB8(184, 185, 186, 187, 188, 189, 190, 191), CLB8(192, 193, 194, 195, 196, 197, 198, 199), CLB8(200, 201, 202, 203, 204, 205, 206, 207),
          CLB8(208, 209, 210, 211, 212, 213, 214, 215), CLB8(216, 217, 218, 219, 220, 221, 222, 223), CLB_E, "s96", CLB_S);
  }
  LBAR();
  u32x4 sv;
  if (w < 6 - TLAG) sv = *reinterpret_cast<const u32x4*>(smem + R_SI + (56 + w) * 1024 + lane * 16);
  else {
    const f32x4* part = reinterpret_cast<const f32x4*>(smem + (w == 6 - TLAG ? R_PART : (w - 6 + TLAG) * 16384)) +
                        (lane & 2) * 32 + (lane & 1) * 32 + (lane >> 2);
    f32x4 x[8][2];
#pragma unroll
    for (int ww = 0; ww < 8; ++ww) { x[ww][0] = part[ww * 128]; x[ww][1] = part[ww * 128 + 16]; }
#pragma unroll
    for (int u = 0; u < 2; ++u) {
      const f32x4 a = ((x[0][u] + x[1][u]) + (x[2][u] + x[3][u])) + ((x[4][u] + x[5][u]) + (x[6][u] + x[7][u]));
      sv[2 * u] = cvtpk(a[0], a[1]); sv[2 * u + 1] = cvtpk(a[2], a[3]);
    }
  }
  st_out(p, e, combine(sv, av, xv));
  if (j == 0 && tid == 0) st2_sc1(rsy, SY_EP, tag);
}


__global__ __launch_bounds__(512, 2) void main_kernel(Args p) {
  char* smem = g_smem;
  p.ws = ws_align(p.ws);
  const int bx = blockIdx.x & 7, bk = blockIdx.x >> 3;
  const bool isg = (bx ^ XPROBE) & 4;
  const int b = (isg ? NCHAIN : 0) + bk * 4 + (bx & 3);
  if (threadIdx.x < 64) {
    unsigned long long pc; unsigned len, keep;
    asm volatile("s_getpc_b64 %0\n.Lpc%=:\n\ts_mov_b32 %1, .Lcode_end-.Lpc%=" : "=s"(pc), "=s"(len));
    asm volatile("s_mov_b32 %0, m0\n\ts_mov_b32 m0, %3\n\ts_nop 0\n\tbuffer_load_dword %1, %2, 0 offen lds\n\ts_mov_b32 m0, %0"
                 : "=&s"(keep) : "v"((bk * 40 + threadIdx.x) * 128), "s"(raw_rsrc((const void*)pc, len)), "s"((b >> 6) == 1 ? 2048 : 163328) : "memory");
  }
  if ((bk >> 3) == (isg ? 3 : 1) && (threadIdx.x >> 6) == 1) {
    const int n = (threadIdx.x & 63) * TLB_SH + (bk & (TLB_SH - 1)), m = n >> 2;
    const u16* q = m == 0 ? p.Wq : m == 1 ? p.Wk : m == 2 ? p.Wv : m == 3 ? p.Wo : m == 4 ? p.wg : m == 5 ? p.wu : m == 6 ? p.wd : m == 7 ? p.x : p.out;
    if (n < 36 + (int)(WS_END / PAGE))
      touch_line(n < 36 ? (const char*)q + (n & 3) * (m < 7 ? PAGE : PAGE / 2) : p.ws + (size_t)(n - 35) * PAGE - 16384, smem + 163072);
  }
  if (b >= NCHAIN) gemm_role(p, b - NCHAIN, smem);
  else if (b < 32) qk_role(p, (b & 3) * 8 + bk, smem);
  else if (b < 64) vu_role(p, (b & 3) * 8 + bk - 8, smem);
  else red_role(p, b - 64, smem);
}

extern "C" void kernel_launch(void* const* d_in, const int* in_sizes, int n_in,
                              void* d_out, int out_size, void* d_ws, size_t ws_size,
                              hipStream_t stream) {
  Args a;
  a.x = (const u16*)d_in[0]; a.mem0 = (const u16*)d_in[1];
  a.Wq = (const u16*)d_in[2]; a.Wk = (const u16*)d_in[3]; a.Wv = (const u16*)d_in[4]; a.Wo = (const u16*)d_in[5];
  a.wg = (const u16*)d_in[8]; a.wu = (const u16*)d_in[9]; a.wd = (const u16*)d_in[10];
  a.ws = (char*)d_ws; a.out = (u16*)d_out; a.s0 = 0; a.pad1 = 0;
  main_kernel<<<256, 512, 0, stream>>>(a);
}
```

```cpp
#include <hip/hip_runtime.h>
#include <hip/hip_bf16.h>
#include <stdint.h>

constexpr int S_LEN = 2048, DM = 1024, NH = 32, DH = 128, NMEM = 8, SEG = 32;
constexpr int NSEG = S_LEN / SEG;
constexpr int HD = NH * DH;
constexpr int FF = 3584;

typedef unsigned short u16;
typedef __attribute__((ext_vector_type(8))) short bf16x8;
typedef __attribute__((ext_vector_type(4))) short s16x4;
typedef __attribute__((ext_vector_type(4))) float f32x4;
typedef __attribute__((ext_vector_type(4))) unsigned int u32x4;
typedef __attribute__((ext_vector_type(2))) unsigned int u32x2;
typedef __attribute__((address_space(3))) s16x4* lds_s16x4_ptr;
typedef __attribute__((address_space(3))) void* lds_void_ptr;
typedef __attribute__((address_space(1))) unsigned gu32;
typedef __bf16 bf16x2_t __attribute__((ext_vector_type(2)));
typedef float f32x2_t __attribute__((ext_vector_type(2)));

__device__ __forceinline__ unsigned cvtpk(float lo, float hi) {
  bf16x2_t r = __builtin_convertvector((f32x2_t){lo, hi}, bf16x2_t);
  return __builtin_bit_cast(unsigned, r);
}
__device__ __forceinline__ float lo_f(unsigned v) { return __uint_as_float(v << 16); }
__device__ __forceinline__ float hi_f(unsigned v) { return __uint_as_float(v & 0xffff0000u); }
__device__ __forceinline__ float rbf(float f) { return lo_f(cvtpk(f, f)); }
__device__ __forceinline__ bf16x8 as_bf16x8(u32x4 v) { return __builtin_bit_cast(bf16x8, v); }
#define MFMA(a, b, c) __builtin_amdgcn_mfma_f32_16x16x32_bf16((a), (b), (c), 0, 0, 0)

#define KSTR 65
#define XLD 1024
constexpr int PAGE = 2 << 20, NPAGE = 14;
constexpr int T_K = 0, T_Q = 2304, T_V = 4608, T_O = 6912, T_END = T_O + 2048;
constexpr unsigned T_BYTES = T_END * 16384u;
constexpr size_t WS_SY = 0;
constexpr size_t WS_T = (size_t)NPAGE * PAGE;
constexpr size_t WS_XN = WS_T + T_BYTES;
constexpr size_t WS_HF = WS_XN + (size_t)S_LEN * XLD * 2;
constexpr size_t WS_G = WS_HF + (size_t)S_LEN * 16384;
constexpr size_t WS_S = WS_G + (size_t)S_LEN * DM * 2;
constexpr size_t WS_END = WS_S + (size_t)S_LEN * DM * 2;
constexpr unsigned SENT = 0xFFFFFFFFu;
constexpr unsigned DSENT = 0xFFBFFFBFu;
constexpr int SY_MEM = 0;
constexpr int SY_O = 2 * PAGE;
constexpr int SY_V = 6 * PAGE;
constexpr int SY_BR = PAGE, SY_BA = SY_BR + 16384;
constexpr int SY_FKQ = SY_BR + 32768;
constexpr int SY_FG = SY_FKQ + NH * 4 * 128;
constexpr int SY_FO = SY_FG + 16 * 8 * 128;
constexpr int SY_CH = SY_FO + 384 * 128;
constexpr int SY_ABORT = SY_CH + 8 * 128;
constexpr int SY_FS = SY_ABORT + 256;
constexpr int SY_FEND = SY_FS + 64 * 128;
constexpr int SY_EP = SY_FEND, SY_BT = SY_EP + 128;
constexpr int SY_TOTAL = NPAGE * PAGE;
constexpr int PROBE = PAGE - 16384 + 6144;
__device__ __forceinline__ int side(unsigned pm, int page) { return ((pm >> page) & 1u) << 13; }
__device__ __forceinline__ int o_mem(unsigned pm, int s) { return SY_MEM + s * 32768 + side(pm, 0); }
__device__ __forceinline__ int o_o(unsigned pm, int s, int h) {
  return SY_O + s * 131072 + (h >> 3) * 16384 + ((h >> 1) & 3) * 2048 + (h & 1) * 128 + side(pm, 2 + (s >> 4));
}
__device__ __forceinline__ int o_v(unsigned pm, int s, int h) { return SY_V + s * 262144 + (h >> 1) * 16384 + (h & 1) * 4096 + side(pm, 6 + (s >> 3)); }
__device__ __forceinline__ char* ws_align(char* ws) { return ws + ((PAGE - (unsigned)(uintptr_t)ws) & (PAGE - 1)); }
#define NFUSE 12
constexpr int NPRE = 4;
static_assert(WS_END + PAGE <= (size_t)256 << 20 && NH * KSTR <= T_Q && NPAGE % 2 == 0, "");
constexpr int NCHAIN = 128;
__host__ __device__ constexpr int kseg(int sg, int h) { return h * KSTR + sg; }
__host__ __device__ constexpr int fkq(int h, int kind) { return SY_FKQ + (h * 4 + kind) * 128; }

#define TLB_NI 16
#define TLB_WS (2 << 20)
#define XPROBE 0
struct Args {
  const u16 *x, *mem0, *Wq, *Wk, *Wv, *Wo, *wg, *wu, *wd;
  char* ws;
  u16* out;
  int s0, pad1;
};
__device__ __forceinline__ int h_off(const Args& p, int rblk) { return rblk * PAGE + (((p.s0 ^ rblk ^ 1) & 1) << 13); }
static_assert(WS_HF % (2 * PAGE) == 0, "");
#define TF_KQV 1
#define TF_O 0
__device__ __forceinline__ int t_off(const Args& p, int t) { return t * 16384 + (((p.s0 ^ (t >> 7) ^ (TF_KQV == 2 ? t : TF_KQV)) & 1) << 13); }
__device__ __forceinline__ int t_off_o(const Args& p, int t) { return t * 16384 + (((p.s0 ^ (t >> 7) ^ (TF_O == 2 ? t : TF_O)) & 1) << 13); }

__device__ __forceinline__ float wave_sum(float v) {
#pragma unroll
  for (int i = 0; i < 4; ++i) {
    const int x = (int)__float_as_uint(v);
    v += __uint_as_float(i == 0 ? __builtin_amdgcn_update_dpp(0, x, 0x128, 0xf, 0xf, false) : i == 1 ? __builtin_amdgcn_update_dpp(0, x, 0x124, 0xf, 0xf, false)
                         : i == 2 ? __builtin_amdgcn_update_dpp(0, x, 0x122, 0xf, 0xf, false) : __builtin_amdgcn_update_dpp(0, x, 0x121, 0xf, 0xf, false));
  }
  auto r = __builtin_amdgcn_permlane16_swap(__float_as_uint(v), __float_as_uint(v), false, false);
  v = __uint_as_float(r[0]) + __uint_as_float(r[1]);
  r = __builtin_amdgcn_permlane32_swap(__float_as_uint(v), __float_as_uint(v), false, false);
  return __uint_as_float(r[0]) + __uint_as_float(r[1]);
}

__shared__ __attribute__((aligned(16))) char g_smem[163840];
__device__ __forceinline__ unsigned lds_off(const void* p) {
  return (unsigned)(uintptr_t)(lds_void_ptr)g_smem + (unsigned)((const char*)p - g_smem);
}
__device__ __forceinline__ __amdgpu_buffer_rsrc_t mkrsrc(const void* p, unsigned bytes) {
  return __builtin_amdgcn_make_buffer_rsrc((void*)p, 0, bytes, 0x00020000);
}
__device__ __forceinline__ u32x4 ld_sc1(__amdgpu_buffer_rsrc_t rs, int off) {
  return __builtin_amdgcn_raw_buffer_load_b128(rs, off, 0, 16);
}
__device__ __forceinline__ void st_sc1(__amdgpu_buffer_rsrc_t rs, int off, u32x4 v) {
  __builtin_amdgcn_raw_buffer_store_b128(v, rs, off, 0, 16);
}
__device__ __forceinline__ void swap16(unsigned& x, unsigned& y) {
  auto r = __builtin_amdgcn_permlane16_swap(x, y, false, false);
  x = r[0]; y = r[1];
}
__device__ __forceinline__ void swap32(unsigned& x, unsigned& y) {
  auto r = __builtin_amdgcn_permlane32_swap(x, y, false, false);
  x = r[0]; y = r[1];
}
__device__ __forceinline__ bool spin_fail(unsigned& spins, gu32* abortw) {
  ++spins;
  if ((spins & 63u) == 0u) {
    const __amdgpu_buffer_rsrc_t ra = mkrsrc((const void*)abortw, 4);
    unsigned z = 0;
    asm volatile("" : "+v"(z));
    if (__builtin_amdgcn_readfirstlane(__builtin_amdgcn_raw_buffer_load_b32(ra, z, 0, 16)) == 1u) return true;
    if (spins > (1u << 19)) {
      __builtin_amdgcn_raw_buffer_store_b32(z + 1u, ra, z, 0, 16);
      return true;
    }
  }
  return false;
}
__device__ __forceinline__ bf16x8 ldsf(const char* p) { return *reinterpret_cast<const bf16x8*>(p); }
#define SCHED() __builtin_amdgcn_sched_barrier(0)
#define LBAR() do { asm volatile("s_waitcnt lgkmcnt(0)" ::: "memory"); __builtin_amdgcn_s_barrier(); asm volatile("" ::: "memory"); } while (0)
#define WAIT_V(n) asm volatile("s_waitcnt vmcnt(%0)" :: "n"(n) : "memory")

#define HOLD_V(c, n) asm volatile("s_cmp_eq_u32 %0, 0\n\ts_cbranch_scc1 .Lh%=\n\ts_waitcnt vmcnt(%1)\n.Lh%=:" :: "s"((int)(c)), "n"(n) : "memory", "scc")
#define TH_Q 2
__device__ __forceinline__ void wait_flags(__amdgpu_buffer_rsrc_t rs, int off, gu32* abortw) {
  for (unsigned spins = 0;;) {
    asm volatile("" ::: "memory");
    const unsigned v = __builtin_amdgcn_raw_buffer_load_b32(rs, off, 0, 16);
    if (__all(v != SENT)) break;
    __builtin_amdgcn_s_sleep(4);
    if (spin_fail(spins, abortw)) break;
  }
  asm volatile("" ::: "memory");
}

__device__ __forceinline__ unsigned offA(unsigned row, unsigned ch) {
  return row * 128u + 16u * (ch ^ (row & 7u));
}
__device__ __forceinline__ unsigned swzB(unsigned row) { return ((row & 3u) << 2) | ((row >> 2) & 3u); }
__device__ __forceinline__ unsigned offB(unsigned row, unsigned ch) {
  return 256u * row + 16u * (ch ^ swzB(row));
}
__device__ __forceinline__ void glds16(const u16* src, char* dst) {
  const unsigned d = __builtin_amdgcn_readfirstlane(lds_off(dst));
  unsigned keep;
  asm volatile("s_mov_b32 %0, m0\n\ts_mov_b32 m0, %2\n\ts_nop 0\n\tglobal_load_lds_dwordx4 %1, off\n\ts_mov_b32 m0, %0"
               : "=&s"(keep) : "v"(src), "s"(d) : "memory");
}
__device__ __forceinline__ bf16x8 ld_bfrag(const char* sb, int ks, int c, int lane) {
  const unsigned g = lane >> 4, qq = (lane & 15) >> 2, pp = lane & 3;
  s16x4 lo = __builtin_amdgcn_ds_read_tr16_b64_v4i16(
      (lds_s16x4_ptr)(sb + offB(32 * ks + 8 * g + qq, 2 * c + (pp >> 1)) + 8 * (pp & 1)));
  s16x4 hi = __builtin_amdgcn_ds_read_tr16_b64_v4i16(
      (lds_s16x4_ptr)(sb + offB(32 * ks + 8 * g + 4 + qq, 2 * c + (pp >> 1)) + 8 * (pp & 1)));
  return (bf16x8){lo[0], lo[1], lo[2], lo[3], hi[0], hi[1], hi[2], hi[3]};
}

__device__ __forceinline__ void blk_dma(const u16* src, size_t ld, char* slot, int lane) {
  const u16* s = src + (size_t)(lane >> 2) * ld + (lane & 3) * 8;
  glds16(s, slot);
  glds16(s + 16 * ld, slot + 1024);
}
template <int NB, typename ISSUE, typename SINK>
__device__ __forceinline__ void load_blocks(char* slice, ISSUE&& issue, SINK&& sink) {
#pragma unroll
  for (int i = 0; i < 8; ++i) issue(i, slice + i * 2048);
#pragma unroll
  for (int g = 0; g < NB / 4; ++g) {
    if (g + 1 < NB / 4) WAIT_V(8); else WAIT_V(0);
#pragma unroll
    for (int j = 0; j < 4; ++j) sink(4 * g + j, slice + ((4 * g + j) & 7) * 2048);
    asm volatile("s_waitcnt lgkmcnt(0)" ::: "memory");
    if (g + 2 < NB / 4) {
#pragma unroll
      for (int j = 0; j < 4; ++j) issue(4 * g + 8 + j, slice + ((4 * g + j) & 7) * 2048);
    }
  }
}

__device__ __forceinline__ u32x4 widen32(unsigned a0, unsigned a1, unsigned b0, unsigned b1) {
  swap32(a0, b0); swap16(a0, b0);
  swap32(a1, b1); swap16(a1, b1);
  return (u32x4){a0, a1, b0, b1};
}

__device__ __forceinline__ void publish_flags(__amdgpu_buffer_rsrc_t rs, int off, int stride, int n) {
  WAIT_V(0);
  LBAR();
  int tid = threadIdx.x;
  asm volatile("" : "+v"(tid));
  if (tid < n) __builtin_amdgcn_raw_buffer_store_b32(1u, rs, off + tid * stride, 0, 16);
}

__device__ __forceinline__ void wait_s(const Args& p, __amdgpu_buffer_rsrc_t rsy, int rblk, int c0, int n, bool need) {
  if (need) {
    if (threadIdx.x < 64) {
      for (unsigned spins = 0;;) {
        asm volatile("" ::: "memory");
        const int f = (int)__builtin_amdgcn_raw_buffer_load_b32(rsy, SY_FS + (2 * c0 + (threadIdx.x & (2 * n - 1))) * 128, 0, 16);
        if (__all(f >= rblk)) break;
        __builtin_amdgcn_s_sleep(32);
        if (spin_fail(spins, (gu32*)(p.ws + WS_SY + SY_ABORT))) break;
      }
    }
    LBAR();
  }
}
__device__ __forceinline__ u32x4 combine(u32x4 sv, u32x4 g, u32x4 x) {
  u32x4 o;
#pragma unroll
  for (int j = 0; j < 4; ++j) {
    const unsigned t = cvtpk(lo_f(sv[j]) * lo_f(g[j]), hi_f(sv[j]) * hi_f(g[j]));
    o[j] = cvtpk(lo_f(x[j]) + lo_f(t), hi_f(x[j]) + hi_f(t));
  }
  return o;
}
#define OUT_AUX 0
__device__ __forceinline__ void st_out(const Args& p, int e, u32x4 v) {
  __builtin_amdgcn_raw_buffer_store_b128(v, mkrsrc(p.out, S_LEN * DM * 2), e * 2, 0, OUT_AUX);
}
struct SX { u32x4 s, x; };
__device__ __forceinline__ SX get_sx(const Args& p, int rblk, int e) {
  SX r; r.s = (u32x4){0u, 0u, 0u, 0u}; r.x = r.s;
  if (rblk < NFUSE) {
    r.s = ld_sc1(mkrsrc(p.ws + WS_S, S_LEN * DM * 2), (((e & 1023) >> 5) * S_LEN + (e >> 10)) * 64 + (e & 31) * 2);
    r.x = *reinterpret_cast<const u32x4*>(p.x + e);
  }
  return r;
}
__device__ __forceinline__ void put_g(const Args& p, __amdgpu_buffer_rsrc_t rg, int rblk, int e, u32x4 g, const SX& v) {
  if (rblk < NFUSE) {
    st_out(p, e, combine(v.s, g, v.x));
  } else st_sc1(rg, e * 2, g);
}

__device__ __forceinline__ void job_comb(const Args& p, int rblk, int ct, __amdgpu_buffer_rsrc_t rsy) {
  int tid = threadIdx.x;
  asm volatile("" : "+v"(tid));
  u32x4 xv[8];
#pragma unroll
  for (int i = 0; i < 8; ++i) xv[i] = *reinterpret_cast<const u32x4*>(p.x + (rblk * 128 + i * 16 + (tid >> 5)) * DM + ct * 256 + (tid & 31) * 8);
  if (tid < 64) wait_flags(rsy, SY_FG + (rblk * 8 + 2 * ct + (tid & 1)) * 128, (gu32*)(p.ws + WS_SY + SY_ABORT));
  wait_s(p, rsy, rblk, ct * 8, 8, true);
  u32x4 sv[8], gv[8];
#pragma unroll
  for (int i = 0; i < 8; ++i) {
    const int e = (rblk * 128 + i * 16 + (tid >> 5)) * DM + ct * 256 + (tid & 31) * 8;
    sv[i] = ld_sc1(mkrsrc(p.ws + WS_S, S_LEN * DM * 2), (((e & 1023) >> 5) * S_LEN + (e >> 10)) * 64 + (e & 31) * 2);
    gv[i] = ld_sc1(mkrsrc(p.ws + WS_G, S_LEN * DM * 2), e * 2);
  }
#pragma unroll
  for (int i = 0; i < 8; ++i)
    st_out(p, (rblk * 128 + i * 16 + (tid >> 5)) * DM + ct * 256 + (tid & 31) * 8, combine(sv[i], gv[i], xv[i]));
}

__device__ __forceinline__ u32x4 raw_rsrc(const void* p, unsigned bytes) {
  const unsigned long long a = (unsigned long long)p;
  return (u32x4){(unsigned)a, (unsigned)(a >> 32) & 0xffffu, bytes, 0x00020000u};
}
__device__ __forceinline__ u32x2 next_tag(u32x2 e) { return (u32x2){e[0] + 1u, e[1] + (e[0] == 0xFFFFFFFFu)}; }
struct Boot { u32x2 e; u32x4 c; };
__device__ __forceinline__ void boot_ask(const char* ws, Boot& r) {
  const unsigned o = (threadIdx.x & 63) * 128;
  asm volatile("buffer_load_dwordx2 %0, off, %3, %4 sc1\n\tbuffer_load_dwordx4 %1, %2, %3, %5 offen sc1"
               : "=&v"(r.e), "=&v"(r.c) : "v"(o), "s"(raw_rsrc(ws, SY_TOTAL)), "s"(SY_EP), "s"(SY_BT) : "memory");
}
__device__ __forceinline__ void wait_rows(const char* ws, u32x2 tag) {
  const __amdgpu_buffer_rsrc_t rs = mkrsrc(ws, SY_TOTAL);
  int o = (threadIdx.x & 63) * 128;
  asm volatile("" : "+v"(o));
#pragma unroll 1
  for (int i = 0; i < (1 << 17); ++i) {
    asm volatile("" ::: "memory");
    const u32x2 a = __builtin_amdgcn_raw_buffer_load_b64(rs, o, SY_BT + 32 * 128, 16), b = __builtin_amdgcn_raw_buffer_load_b64(rs, o, SY_BT + 96 * 128, 16);
    if (__all(a[0] == tag[0] && a[1] == tag[1] && b[0] == tag[0] && b[1] == tag[1])) break;
    __builtin_amdgcn_s_sleep(8);
  }
  asm volatile("" ::: "memory");
}
__device__ __forceinline__ unsigned boot_done(const char* ws, Boot& r, bool own = false, u32x2 tag = (u32x2){0u, 0u}) {
#pragma unroll 1
  for (int i = 0; i < (1 << 17); ++i) {
    asm volatile("" : "+v"(r.e), "+v"(r.c));
    if (!own) tag = next_tag(r.e);
    if (__all(r.c[0] == tag[0] && r.c[1] == tag[1] && (r.c[2] ^ r.c[3]) == tag[0])) break;
    __builtin_amdgcn_s_sleep(8);
    boot_ask(ws, r);
    WAIT_V(0);
  }
  unsigned q0[NPAGE], q1[NPAGE], ga = 0xFFFFu, gb = 0xFFFFu;
#pragma unroll
  for (int i = 0; i < NPAGE; ++i) {
    const unsigned x = __builtin_amdgcn_readlane(r.c[2], i), y = __builtin_amdgcn_readlane(r.c[2], i + NPAGE);
    q0[i] = min(x >> 16, y >> 16); q1[i] = min(x & 0xFFFFu, y & 0xFFFFu);
    ga = min(ga, (i & 1) ? q1[i] : q0[i]); gb = min(gb, (i & 1) ? q0[i] : q1[i]);
  }
  unsigned m = gb < ga ? 0x80001555u : 0x2AAAu;
  const unsigned thr = min(ga, gb) + 110;
#pragma unroll
  for (int i = 0; i < NPAGE; ++i) {
    const unsigned lo = min(q0[i], q1[i]), hi = max(q0[i], q1[i]);
    const unsigned keep = (lo < thr ? hi : 0u) > lo + 100 ? ~(1u << i) : ~0u;
    m = (m & keep) | ((q1[i] < q0[i] ? ~0u : 0u) & ~keep);
  }
  return m;
}
typedef __attribute__((address_space(3))) volatile unsigned* lds_vu_ptr;
__device__ __forceinline__ lds_vu_ptr lds_at(const void* p) {
  unsigned a = lds_off(p);
  asm volatile("" : "+v"(a));
  return (lds_vu_ptr)(uintptr_t)a;
}
__device__ __forceinline__ unsigned uni(unsigned v) {
  unsigned r;
  asm volatile("s_nop 1\n\tv_readfirstlane_b32 %0, %1\n\ts_nop 3" : "=s"(r) : "v"(v));
  return r;
}
__device__ __forceinline__ void start_sy(char* ws, const u16* mem0, int i, char* smem) {
  const int tid = threadIdx.x, lane = tid & 63, w = __builtin_amdgcn_readfirstlane(tid >> 6);
  const __amdgpu_buffer_rsrc_t rsy = mkrsrc(ws, SY_TOTAL);
  const __amdgpu_buffer_rsrc_t rs = mkrsrc(ws + (size_t)(i % NPAGE) * PAGE + PROBE - (i / NPAGE) * 2048, 16384);
  const u32x2 e = __builtin_amdgcn_raw_buffer_load_b64(rsy, 0, SY_EP, 16);
  const u32x4 ds = {DSENT, DSENT, DSENT, DSENT};
  if (lane < 32) st_sc1(rs, (lane >> 4) * 8192 + w * 256 + (lane & 15) * 16, ds);
  static_assert(NPRE == 4 && SY_FEND - SY_BR <= 32 * 6144, "");
#pragma unroll
  for (int u = 0; u < 6; ++u)
    st_sc1(rsy, (u < 4 ? SY_V + (i * 4 + u) * 8192 : SY_O + (i * 2 + u - 4) * 8192) + tid * 16, ds);
  if (w == 7) {
    const int n = i * 64 + lane, j = (n >> 10) * 512 + (n & 511);
    const unsigned* m0 = reinterpret_cast<const unsigned*>(mem0 + (2 * ((j >> 2) & 3) + (j & 1)) * DM + (j >> 4) * 16) + ((j >> 1) & 1);
    u32x4 m;
#pragma unroll
    for (int k = 0; k < 4; ++k) { const unsigned t = m0[2 * k]; m[k] = t == DSENT ? 0xFFFFFFFFu : t; }
    st_sc1(rsy, n * 16, m);
  } else if (w == 6) st_sc1(rsy, 32768 + i * 1024 + lane * 16, ds);
  else {
    const int o = SY_BR + i * 6144 + tid * 16;
    st_sc1(rsy, o < SY_FEND ? o : 0x7F000000, (u32x4){SENT, SENT, SENT, SENT});
  }
  unsigned t[4];
#pragma unroll
  for (int k = 0; k < 4; ++k) {
    unsigned long long c0, c1; unsigned v, off = ((k ^ (k >> 1)) & 1) * 8192 + w * 256 + (k >> 1) * 128;
    asm volatile("s_waitcnt vmcnt(0)\n\ts_barrier\n\ts_memtime %0\n\ts_waitcnt lgkmcnt(0)\n\tbuffer_load_dword %2, %3, %4, 0 offen sc1\n\ts_waitcnt vmcnt(0)\n\t"
                 "s_memtime %1\n\ts_waitcnt lgkmcnt(0)" : "=&s"(c0), "=&s"(c1), "=&v"(v) : "v"(off), "s"(rs) : "memory");
    t[k] = (unsigned)(c1 - c0);
  }
  lds_vu_ptr tm = lds_at(smem);
  if (lane == 0) { tm[w] = min(t[0], t[3]); tm[8 + w] = min(t[1], t[2]); }
  int ro = SY_BT + i * 128 + lane * 16;
  asm volatile("" : "+v"(ro));
  LBAR();
  if (threadIdx.x < 8) {
    unsigned m0 = tm[0], m1 = tm[8];
#pragma unroll
    for (int k = 1; k < 8; ++k) { m0 = min(m0, tm[k]); m1 = min(m1, tm[8 + k]); }
    const u32x2 tag = next_tag(e);
    const unsigned v = min(m0, 30000u) << 16 | min(m1, 30000u);
    st_sc1(rsy, ro, (u32x4){tag[0], tag[1], v, v ^ tag[0]});
  }
}
template <int NB, typename F>
__device__ __forceinline__ u32x2 start_rows(const Args& p, int g, unsigned on, F&& between) {
  const int tid = threadIdx.x, lane = tid & 63, row0 = g * 16 + (tid >> 6) * 2;
  u32x4 a[4];
  u32x2 e;
  asm volatile("buffer_load_dwordx4 %0, %5, %8, 0 offen\n\tbuffer_load_dwordx4 %1, %5, %8, 0 offen offset:1024\n\t"
               "buffer_load_dwordx4 %2, %5, %8, 0 offen offset:2048\n\tbuffer_load_dwordx4 %3, %5, %8, 0 offen offset:3072\n\t"
               "buffer_load_dwordx2 %4, off, %6, %7 sc1"
               : "=&v"(a[0]), "=&v"(a[1]), "=&v"(a[2]), "=&v"(a[3]), "=&v"(e)
               : "v"(row0 * (DM * 2) + lane * 16), "s"(raw_rsrc(p.ws, SY_TOTAL)), "s"(SY_EP), "s"(raw_rsrc(p.x, on * (S_LEN * DM * 2))) : "memory");
  between();
  asm volatile("s_waitcnt vmcnt(%5)" : "+v"(a[0]), "+v"(a[1]), "+v"(a[2]), "+v"(a[3]), "+v"(e) : "n"(NB) : "memory");
  const __amdgpu_buffer_rsrc_t ro = mkrsrc(p.ws + WS_XN, on * (S_LEN * XLD * 2));
#pragma unroll
  for (int u = 0; u < 2; ++u) {
    float v[16];
#pragma unroll
    for (int i = 0; i < 8; ++i) { v[2 * i] = lo_f(a[2 * u + (i >> 2)][i & 3]); v[2 * i + 1] = hi_f(a[2 * u + (i >> 2)][i & 3]); }
    float s = 0.f;
#pragma unroll
    for (int i = 0; i < 16; ++i) s += v[i];
    const float mean = wave_sum(s) * (1.0f / DM);
    float q = 0.f;
#pragma unroll
    for (int i = 0; i < 16; ++i) { float d = v[i] - mean; q += d * d; }
    const float var = rbf(wave_sum(q) * (1.0f / DM));
    const float mb = rbf(mean);
    const float inv = 1.0f / rbf(sqrtf(rbf(var + 1e-5f)));
#pragma unroll
    for (int k = 0; k < 2; ++k) {
      u32x4 o;
#pragma unroll
      for (int i = 0; i < 4; ++i) {
        const unsigned t = cvtpk(v[8 * k + 2 * i] - mb, v[8 * k + 2 * i + 1] - mb);
        o[i] = cvtpk(lo_f(t) * inv, hi_f(t) * inv);
      }
      st_sc1(ro, (row0 + u) * (XLD * 2) + k * 1024 + lane * 16, o);
    }
  }
  return next_tag(e);
}
__device__ __forceinline__ void report_rows(const Args& p, int g, u32x2 tag) {
  if (threadIdx.x < 8) st_sc1(mkrsrc(p.ws + WS_SY, SY_TOTAL), SY_BT + (32 + g) * 128 + threadIdx.x * 16, (u32x4){tag[0], tag[1], 0u, tag[0]});
}

typedef __attribute__((ext_vector_type(32))) unsigned u32x32;
__device__ __forceinline__ void put4(u32x32& W, int i, bf16x8 f) {
  const u32x4 v = __builtin_bit_cast(u32x4, f);
#pragma unroll
  for (int j = 0; j < 4; ++j) W[4 * i + j] = v[j];
}
#define CLB8(a, b, c, d, e, f, g, h) "v" #a, "v" #b, "v" #c, "v" #d, "v" #e, "v" #f, "v" #g, "v" #h
#define CLB32(a) CLB32_##a
#define CLB32_128 CLB8(128, 129, 130, 131, 132, 133, 134, 135), CLB8(136, 137, 138, 139, 140, 141, 142, 143), CLB8(144, 145, 146, 147, 148, 149, 150, 151), CLB8(152, 153, 154, 155, 156, 157, 158, 159)
#define CLB32_160 CLB8(160, 161, 162, 163, 164, 165, 166, 167), CLB8(168, 169, 170, 171, 172, 173, 174, 175), CLB8(176, 177, 178, 179, 180, 181, 182, 183), CLB8(184, 185, 186, 187, 188, 189, 190, 191)
#define W4_IN "{v[0:31]}"(W[0]), "{v[32:63]}"(W[1]), "{v[64:95]}"(W[2]), "{v[96:127]}"(W[3])
#define CLB32_192 CLB8(192, 193, 194, 195, 196, 197, 198, 199), CLB8(200, 201, 202, 203, 204, 205, 206, 207), CLB8(208, 209, 210, 211, 212, 213, 214, 215), CLB8(216, 217, 218, 219, 220, 221, 222, 223)
__device__ __forceinline__ unsigned ld_flag0(__amdgpu_buffer_rsrc_t rs, int off) {
  return __builtin_amdgcn_raw_buffer_load_b32(rs, off, 0, 16);
}
__device__ __forceinline__ void glds16_sc1(const char* src, char* dst) {
  const unsigned d = __builtin_amdgcn_readfirstlane(lds_off(dst));
  unsigned keep;
  asm volatile("s_mov_b32 %0, m0\n\ts_mov_b32 m0, %2\n\ts_nop 0\n\tglobal_load_lds_dwordx4 %1, off sc1\n\ts_mov_b32 m0, %0"
               : "=&s"(keep) : "v"(src), "s"(d) : "memory");
}

__device__ __forceinline__ void touch_line(const char* src, char* dst) {
  const unsigned d = __builtin_amdgcn_readfirstlane(lds_off(dst));
  unsigned keep;
  asm volatile("s_mov_b32 %0, m0\n\ts_mov_b32 m0, %2\n\ts_nop 0\n\tglobal_load_lds_dword %1, off sc1\n\ts_mov_b32 m0, %0"
               : "=&s"(keep) : "v"(src), "s"(d) : "memory");
}

#define PF_N 12
#define LN_M 3
#define LN_X 1
#define PR_G1 8
#define WREV 1
template <bool DEV = false>
__device__ __forceinline__ void glds2(const char* base, int step, unsigned voff, unsigned dst) {
  unsigned keep;
  const char* base2 = base + step;
  if (DEV)
    asm volatile("s_mov_b32 %0, m0\n\ts_mov_b32 m0, %4\n\ts_nop 0\n\tglobal_load_lds_dwordx4 %1, %2 sc1\n\t"
                 "s_add_u32 m0, m0, 0x2000\n\ts_nop 0\n\tglobal_load_lds_dwordx4 %1, %3 sc1\n\ts_mov_b32 m0, %0"
                 : "=&s"(keep) : "v"(voff), "s"(base), "s"(base2), "s"(dst) : "memory", "scc");
  else
  asm volatile("s_mov_b32 %0, m0\n\ts_mov_b32 m0, %4\n\ts_nop 0\n\tglobal_load_lds_dwordx4 %1, %2\n\t"
               "s_add_u32 m0, m0, 0x2000\n\ts_nop 0\n\tglobal_load_lds_dwordx4 %1, %3\n\ts_mov_b32 m0, %0"
               : "=&s"(keep) : "v"(voff), "s"(base), "s"(base2), "s"(dst) : "memory", "scc");
}

#define DN_ASM \
  "s_mov_b32 s94, m0\n\t" \
  "s_mov_b64 s[84:85], %[pa]\n\t" \
  "s_mov_b64 s[86:87], %[pb]\n\t" \
  "s_movk_i32 s88, 0x4000\n\t" \
  "s_mov_b32 s89, 0x10000\n\t" \
  "s_movk_i32 s90, 13\n\t" \
  "v_mov_b64 v[0:1], 0\n\t" \
  "v_mov_b64 v[2:3], 0\n\t" \
  "v_mov_b64 v[4:5], 0\n\t" \
  "v_mov_b64 v[6:7], 0\n\t" \
  "v_mov_b64 v[8:9], 0\n\t" \
  "v_mov_b64 v[10:11], 0\n\t" \
  "v_mov_b64 v[12:13], 0\n\t" \
  "v_mov_b64 v[14:15], 0\n\t" \
  "v_mov_b64 v[16:17], 0\n\t" \
  "v_mov_b64 v[18:19], 0\n\t" \
  "v_mov_b64 v[20:21], 0\n\t" \
  "v_mov_b64 v[22:23], 0\n\t" \
  "v_mov_b64 v[24:25], 0\n\t" \
  "v_mov_b64 v[26:27], 0\n\t" \
  "v_mov_b64 v[28:29], 0\n\t" \
  "v_mov_b64 v[30:31], 0\n\t" \
  "s_cmp_lg_u32 %[hf], 0\n\t" \
  "s_cbranch_scc1 .Lk%=\n\t" \
  "ds_read_b128 v[32:35], v80 offset:0\n\t" \
  "ds_read_b128 v[36:39], v80 offset:1024\n\t" \
  "ds_read_b128 v[40:43], v80 offset:2048\n\t" \
  "ds_read_b128 v[44:47], v80 offset:3072\n\t" \
  "ds_read_b64_tr_b16 v[64:65], v82 offset:0\n\t" \
  "ds_read_b64_tr_b16 v[66:67], v83 offset:0\n\t" \
  "ds_read_b128 v[48:51], v80 offset:8192\n\t" \
  "ds_read_b64_tr_b16 v[68:69], v84 offset:0\n\t" \
  "ds_read_b64_tr_b16 v[70:71], v85 offset:0\n\t" \
  "ds_read_b128 v[52:55], v80 offset:9216\n\t" \
  "ds_read_b128 v[56:59], v80 offset:10240\n\t" \
  "ds_read_b128 v[60:63], v80 offset:11264\n\t" \
  ".Lp0%=:\n\t" \
  "s_waitcnt vmcnt(4)\n\t" \
  "s_barrier\n\t" \
  "s_waitcnt lgkmcnt(3)\n\t" \
  "v_mfma_f32_16x16x32_bf16 v[0:3], v[64:67], v[32:35], v[0:3]\n\t" \
  "ds_read_b64_tr_b16 v[72:73], v82 offset:8192\n\t" \
  "v_mfma_f32_16x16x32_bf16 v[4:7], v[68:71], v[32:35], v[4:7]\n\t" \
  "ds_read_b64_tr_b16 v[74:75], v83 offset:8192\n\t" \
  "ds_read_b128 v[32:35], v80 offset:32768\n\t" \
  "s_add_u32 m0, %[lw], 98304\n\t" \
  "v_mfma_f32_16x16x32_bf16 v[8:11], v[64:67], v[36:39], v[8:11]\n\t" \
  "ds_read_b64_tr_b16 v[76:77], v84 offset:8192\n\t" \
  "global_load_lds_dwordx4 v90, s[84:85] sc1\n\t" \
  "s_add_u32 s84, s84, s88\n\t" \
  "s_addc_u32 s85, s85, 0\n\t" \
  "v_mfma_f32_16x16x32_bf16 v[12:15], v[68:71], v[36:39], v[12:15]\n\t" \
  "ds_read_b64_tr_b16 v[78:79], v85 offset:8192\n\t" \
  "ds_read_b128 v[36:39], v80 offset:33792\n\t" \
  "v_mfma_f32_16x16x32_bf16 v[16:19], v[64:67], v[40:43], v[16:19]\n\t" \
  "v_mfma_f32_16x16x32_bf16 v[20:23], v[68:71], v[40:43], v[20:23]\n\t" \
  "ds_read_b128 v[40:43], v80 offset:34816\n\t" \
  "s_add_u32 m0, %[lw], 114688\n\t" \
  "v_mfma_f32_16x16x32_bf16 v[24:27], v[64:67], v[44:47], v[24:27]\n\t" \
  "global_load_lds_dwordx4 v91, s[86:87]\n\t" \
  "s_add_u32 s86, s86, s89\n\t" \
  "s_addc_u32 s87, s87, 0\n\t" \
  "v_mfma_f32_16x16x32_bf16 v[28:31], v[68:71], v[44:47], v[28:31]\n\t" \
  "ds_read_b128 v[44:47], v80 offset:35840\n\t" \
  "s_waitcnt lgkmcnt(3)\n\t" \
  "v_mfma_f32_16x16x32_bf16 v[0:3], v[72:75], v[48:51], v[0:3]\n\t" \
  "ds_read_b64_tr_b16 v[64:65], v82 offset:32768\n\t" \
  "v_mfma_f32_16x16x32_bf16 v[4:7], v[76:79], v[48:51], v[4:7]\n\t" \
  "ds_read_b64_tr_b16 v[66:67], v83 offset:32768\n\t" \
  "ds_read_b128 v[48:51], v80 offset:40960\n\t" \
  "s_add_u32 m0, %[lw], 106496\n\t" \
  "v_mfma_f32_16x16x32_bf16 v[8:11], v[72:75], v[52:55], v[8:11]\n\t" \
  "ds_read_b64_tr_b16 v[68:69], v84 offset:32768\n\t" \
  "global_load_lds_dwordx4 v90, s[84:85] sc1\n\t" \
  "s_add_u32 s84, s84, s88\n\t" \
  "s_addc_u32 s85, s85, 0\n\t" \
  "v_mfma_f32_16x16x32_bf16 v[12:15], v[76:79], v[52:55], v[12:15]\n\t" \
  "ds_read_b64_tr_b16 v[70:71], v85 offset:32768\n\t" \
  "ds_read_b128 v[52:55], v80 offset:41984\n\t" \
  "v_mfma_f32_16x16x32_bf16 v[16:19], v[72:75], v[56:59], v[16:19]\n\t" \
  "v_mfma_f32_16x16x32_bf16 v[20:23], v[76:79], v[56:59], v[20:23]\n\t" \
  "ds_read_b128 v[56:59], v80 offset:43008\n\t" \
  "s_add_u32 m0, %[lw], 122880\n\t" \
  "v_mfma_f32_16x16x32_bf16 v[24:27], v[72:75], v[60:63], v[24:27]\n\t" \
  "global_load_lds_dwordx4 v91, s[86:87]\n\t" \
  "s_add_u32 s86, s86, s89\n\t" \
  "s_addc_u32 s87, s87, 0\n\t" \
  "v_mfma_f32_16x16x32_bf16 v[28:31], v[76:79], v[60:63], v[28:31]\n\t" \
  "ds_read_b128 v[60:63], v80 offset:44032\n\t" \
  "s_sub_u32 s90, s90, 1\n\t" \
  "s_cselect_b32 s88, 0, s88\n\t" \
  "s_cselect_b32 s89, 0, s89\n\t" \
  "s_cselect_b32 s91, 16384, 0\n\t" \
  "s_sub_u32 s84, s84, s91\n\t" \
  "s_subb_u32 s85, s85, 0\n\t" \
  "s_lshl_b32 s91, s91, 2\n\t" \
  "s_sub_u32 s86, s86, s91\n\t" \
  "s_subb_u32 s87, s87, 0\n\t" \
  "s_waitcnt vmcnt(4)\n\t" \
  "s_barrier\n\t" \
  "s_waitcnt lgkmcnt(3)\n\t" \
  "v_mfma_f32_16x16x32_bf16 v[0:3], v[64:67], v[32:35], v[0:3]\n\t" \
  "ds_read_b64_tr_b16 v[72:73], v82 offset:40960\n\t" \
  "v_mfma_f32_16x16x32_bf16 v[4:7], v[68:71], v[32:35], v[4:7]\n\t" \
  "ds_read_b64_tr_b16 v[74:75], v83 offset:40960\n\t" \
  "ds_read_b128 v[32:35], v81 offset:0\n\t" \
  "s_add_u32 m0, %[lw], 0\n\t" \
  "v_mfma_f32_16x16x32_bf16 v[8:11], v[64:67], v[36:39], v[8:11]\n\t" \
  "ds_read_b64_tr_b16 v[76:77], v84 offset:40960\n\t" \
  "global_load_lds_dwordx4 v90, s[84:85] sc1\n\t" \
  "s_add_u32 s84, s84, s88\n\t" \
  "s_addc_u32 s85, s85, 0\n\t" \
  "v_mfma_f32_16x16x32_bf16 v[12:15], v[68:71], v[36:39], v[12:15]\n\t" \
  "ds_read_b64_tr_b16 v[78:79], v85 offset:40960\n\t" \
  "ds_read_b128 v[36:39], v81 offset:1024\n\t" \
  "v_mfma_f32_16x16x32_bf16 v[16:19], v[64:67], v[40:43], v[16:19]\n\t" \
  "v_mfma_f32_16x16x32_bf16 v[20:23], v[68:71], v[40:43], v[20:23]\n\t" \
  "ds_read_b128 v[40:43], v81 offset:2048\n\t" \
  "s_add_u32 m0, %[lw], 16384\n\t" \
  "v_mfma_f32_16x16x32_bf16 v[24:27], v[64:67], v[44:47], v[24:27]\n\t" \
  "global_load_lds_dwordx4 v91, s[86:87]\n\t" \
  "s_add_u32 s86, s86, s89\n\t" \
  "s_addc_u32 s87, s87, 0\n\t" \
  "v_mfma_f32_16x16x32_bf16 v[28:31], v[68:71], v[44:47], v[28:31]\n\t" \
  "ds_read_b128 v[44:47], v81 offset:3072\n\t" \
  "s_waitcnt lgkmcnt(3)\n\t" \
  "v_mfma_f32_16x16x32_bf16 v[0:3], v[72:75], v[48:51], v[0:3]\n\t" \
  "ds_read_b64_tr_b16 v[64:65], v86 offset:0\n\t" \
  "v_mfma_f32_16x16x32_bf16 v[4:7], v[76:79], v[48:51], v[4:7]\n\t" \
  "ds_read_b64_tr_b16 v[66:67], v87 offset:0\n\t" \
  "ds_read_b128 v[48:51], v81 offset:8192\n\t" \
  "s_add_u32 m0, %[lw], 8192\n\t" \
  "v_mfma_f32_16x16x32_bf16 v[8:11], v[72:75], v[52:55], v[8:11]\n\t" \
  "ds_read_b64_tr_b16 v[68:69], v88 offset:0\n\t" \
  "global_load_lds_dwordx4 v90, s[84:85] sc1\n\t" \
  "s_add_u32 s84, s84, s88\n\t" \
  "s_addc_u32 s85, s85, 0\n\t" \
  "v_mfma_f32_16x16x32_bf16 v[12:15], v[76:79], v[52:55], v[12:15]\n\t" \
  "ds_read_b64_tr_b16 v[70:71], v89 offset:0\n\t" \
  "ds_read_b128 v[52:55], v81 offset:9216\n\t" \
  "v_mfma_f32_16x16x32_bf16 v[16:19], v[72:75], v[56:59], v[16:19]\n\t" \
  "v_mfma_f32_16x16x32_bf16 v[20:23], v[76:79], v[56:59], v[20:23]\n\t" \
  "ds_read_b128 v[56:59], v81 offset:10240\n\t" \
  "s_add_u32 m0, %[lw], 24576\n\t" \
  "v_mfma_f32_16x16x32_bf16 v[24:27], v[72:75], v[60:63], v[24:27]\n\t" \
  "global_load_lds_dwordx4 v91, s[86:87]\n\t" \
  "s_add_u32 s86, s86, s89\n\t" \
  "s_addc_u32 s87, s87, 0\n\t" \
  "v_mfma_f32_16x16x32_bf16 v[28:31], v[76:79], v[60:63], v[28:31]\n\t" \
  "ds_read_b128 v[60:63], v81 offset:11264\n\t" \
  "s_waitcnt vmcnt(4)\n\t" \
  "s_barrier\n\t" \
  "s_waitcnt lgkmcnt(3)\n\t" \
  "v_mfma_f32_16x16x32_bf16 v[0:3], v[64:67], v[32:35], v[0:3]\n\t" \
  "ds_read_b64_tr_b16 v[72:73], v86 offset:8192\n\t" \
  "v_mfma_f32_16x16x32_bf16 v[4:7], v[68:71], v[32:35], v[4:7]\n\t" \
  "ds_read_b64_tr_b16 v[74:75], v87 offset:8192\n\t" \
  "ds_read_b128 v[32:35], v81 offset:32768\n\t" \
  "s_add_u32 m0, %[lw], 32768\n\t" \
  "v_mfma_f32_16x16x32_bf16 v[8:11], v[64:67], v[36:39], v[8:11]\n\t" \
  "ds_read_b64_tr_b16 v[76:77], v88 offset:8192\n\t" \
  "global_load_lds_dwordx4 v90, s[84:85] sc1\n\t" \
  "s_add_u32 s84, s84, s88\n\t" \
  "s_addc_u32 s85, s85, 0\n\t" \
  "v_mfma_f32_16x16x32_bf16 v[12:15], v[68:71], v[36:39], v[12:15]\n\t" \
  "ds_read_b64_tr_b16 v[78:79], v89 offset:8192\n\t" \
  "ds_read_b128 v[36:39], v81 offset:33792\n\t" \
  "v_mfma_f32_16x16x32_bf16 v[16:19], v[64:67], v[40:43], v[16:19]\n\t" \
  "v_mfma_f32_16x16x32_bf16 v[20:23], v[68:71], v[40:43], v[20:23]\n\t" \
  "ds_read_b128 v[40:43], v81 offset:34816\n\t" \
  "s_add_u32 m0, %[lw], 49152\n\t" \
  "v_mfma_f32_16x16x32_bf16 v[24:27], v[64:67], v[44:47], v[24:27]\n\t" \
  "global_load_lds_dwordx4 v91, s[86:87]\n\t" \
  "s_add_u32 s86, s86, s89\n\t" \
  "s_addc_u32 s87, s87, 0\n\t" \
  "v_mfma_f32_16x16x32_bf16 v[28:31], v[68:71], v[44:47], v[28:31]\n\t" \
  "ds_read_b128 v[44:47], v81 offset:35840\n\t" \
  "s_waitcnt lgkmcnt(3)\n\t" \
  "v_mfma_f32_16x16x32_bf16 v[0:3], v[72:75], v[48:51], v[0:3]\n\t" \
  "ds_read_b64_tr_b16 v[64:65], v86 offset:32768\n\t" \
  "v_mfma_f32_16x16x32_bf16 v[4:7], v[76:79], v[48:51], v[4:7]\n\t" \
  "ds_read_b64_tr_b16 v[66:67], v87 offset:32768\n\t" \
  "ds_read_b128 v[48:51], v81 offset:40960\n\t" \
  "s_add_u32 m0, %[lw], 40960\n\t" \
  "v_mfma_f32_16x16x32_bf16 v[8:11], v[72:75], v[52:55], v[8:11]\n\t" \
  "ds_read_b64_tr_b16 v[68:69], v88 offset:32768\n\t" \
  "global_load_lds_dwordx4 v90, s[84:85] sc1\n\t" \
  "s_add_u32 s84, s84, s88\n\t" \
  "s_addc_u32 s85, s85, 0\n\t" \
  "v_mfma_f32_16x16x32_bf16 v[12:15], v[76:79], v[52:55], v[12:15]\n\t" \
  "ds_read_b64_tr_b16 v[70:71], v89 offset:32768\n\t" \
  "ds_read_b128 v[52:55], v81 offset:41984\n\t" \
  "v_mfma_f32_16x16x32_bf16 v[16:19], v[72:75], v[56:59], v[16:19]\n\t" \
  "v_mfma_f32_16x16x32_bf16 v[20:23], v[76:79], v[56:59], v[20:23]\n\t" \
  "ds_read_b128 v[56:59], v81 offset:43008\n\t" \
  "s_add_u32 m0, %[lw], 57344\n\t" \
  "v_mfma_f32_16x16x32_bf16 v[24:27], v[72:75], v[60:63], v[24:27]\n\t" \
  "global_load_lds_dwordx4 v91, s[86:87]\n\t" \
  "s_add_u32 s86, s86, s89\n\t" \
  "s_addc_u32 s87, s87, 0\n\t" \
  "v_mfma_f32_16x16x32_bf16 v[28:31], v[76:79], v[60:63], v[28:31]\n\t" \
  "ds_read_b128 v[60:63], v81 offset:44032\n\t" \
  "s_waitcnt vmcnt(4)\n\t" \
  "s_barrier\n\t" \
  "s_waitcnt lgkmcnt(3)\n\t" \
  "v_mfma_f32_16x16x32_bf16 v[0:3], v[64:67], v[32:35], v[0:3]\n\t" \
  "ds_read_b64_tr_b16 v[72:73], v86 offset:40960\n\t" \
  "v_mfma_f32_16x16x32_bf16 v[4:7], v[68:71], v[32:35], v[4:7]\n\t" \
  "ds_read_b64_tr_b16 v[74:75], v87 offset:40960\n\t" \
  "ds_read_b128 v[32:35], v80 offset:0\n\t" \
  "s_add_u32 m0, %[lw], 65536\n\t" \
  "v_mfma_f32_16x16x32_bf16 v[8:11], v[64:67], v[36:39], v[8:11]\n\t" \
  "ds_read_b64_tr_b16 v[76:77], v88 offset:40960\n\t" \
  "global_load_lds_dwordx4 v90, s[84:85] sc1\n\t" \
  "s_add_u32 s84, s84, s88\n\t" \
  "s_addc_u32 s85, s85, 0\n\t" \
  "v_mfma_f32_16x16x32_bf16 v[12:15], v[68:71], v[36:39], v[12:15]\n\t" \
  "ds_read_b64_tr_b16 v[78:79], v89 offset:40960\n\t" \
  "ds_read_b128 v[36:39], v80 offset:1024\n\t" \
  "v_mfma_f32_16x16x32_bf16 v[16:19], v[64:67], v[40:43], v[16:19]\n\t" \
  "v_mfma_f32_16x16x32_bf16 v[20:23], v[68:71], v[40:43], v[20:23]\n\t" \
  "ds_read_b128 v[40:43], v80 offset:2048\n\t" \
  "s_add_u32 m0, %[lw], 81920\n\t" \
  "v_mfma_f32_16x16x32_bf16 v[24:27], v[64:67], v[44:47], v[24:27]\n\t" \
  "global_load_lds_dwordx4 v91, s[86:87]\n\t" \
  "s_add_u32 s86, s86, s89\n\t" \
  "s_addc_u32 s87, s87, 0\n\t" \
  "v_mfma_f32_16x16x32_bf16 v[28:31], v[68:71], v[44:47], v[28:31]\n\t" \
  "ds_read_b128 v[44:47], v80 offset:3072\n\t" \
  "s_waitcnt lgkmcnt(3)\n\t" \
  "v_mfma_f32_16x16x32_bf16 v[0:3], v[72:75], v[48:51], v[0:3]\n\t" \
  "ds_read_b64_tr_b16 v[64:65], v82 offset:0\n\t" \
  "v_mfma_f32_16x16x32_bf16 v[4:7], v[76:79], v[48:51], v[4:7]\n\t" \
  "ds_read_b64_tr_b16 v[66:67], v83 offset:0\n\t" \
  "ds_read_b128 v[48:51], v80 offset:8192\n\t" \
  "s_add_u32 m0, %[lw], 73728\n\t" \
  "v_mfma_f32_16x16x32_bf16 v[8:11], v[72:75], v[52:55], v[8:11]\n\t" \
  "ds_read_b64_tr_b16 v[68:69], v84 offset:0\n\t" \
  "global_load_lds_dwordx4 v90, s[84:85] sc1\n\t" \
  "s_add_u32 s84, s84, s88\n\t" \
  "s_addc_u32 s85, s85, 0\n\t" \
  "v_mfma_f32_16x16x32_bf16 v[12:15], v[76:79], v[52:55], v[12:15]\n\t" \
  "ds_read_b64_tr_b16 v[70:71], v85 offset:0\n\t" \
  "ds_read_b128 v[52:55], v80 offset:9216\n\t" \
  "v_mfma_f32_16x16x32_bf16 v[16:19], v[72:75], v[56:59], v[16:19]\n\t" \
  "v_mfma_f32_16x16x32_bf16 v[20:23], v[76:79], v[56:59], v[20:23]\n\t" \
  "ds_read_b128 v[56:59], v80 offset:10240\n\t" \
  "s_add_u32 m0, %[lw], 90112\n\t" \
  "v_mfma_f32_16x16x32_bf16 v[24:27], v[72:75], v[60:63], v[24:27]\n\t" \
  "global_load_lds_dwordx4 v91, s[86:87]\n\t" \
  "s_add_u32 s86, s86, s89\n\t" \
  "s_addc_u32 s87, s87, 0\n\t" \
  "v_mfma_f32_16x16x32_bf16 v[28:31], v[76:79], v[60:63], v[28:31]\n\t" \
  "ds_read_b128 v[60:63], v80 offset:11264\n\t" \
  "s_cmp_lg_u32 s88, 0\n\t" \
  "s_cbranch_scc1 .Lp0%=\n\t" \
  "s_branch .Le%=\n\t" \
  ".Lk%=:\n\t" \
  "ds_read_b128 v[32:35], v80 offset:0\n\t" \
  "ds_read_b128 v[36:39], v80 offset:1024\n\t" \
  "ds_read_b128 v[40:43], v80 offset:2048\n\t" \
  "ds_read_b128 v[44:47], v80 offset:3072\n\t" \
  "ds_read_b64_tr_b16 v[64:65], v82 offset:0\n\t" \
  "ds_read_b64_tr_b16 v[66:67], v83 offset:0\n\t" \
  "ds_read_b128 v[48:51], v80 offset:8192\n\t" \
  "ds_read_b64_tr_b16 v[68:69], v84 offset:0\n\t" \
  "ds_read_b64_tr_b16 v[70:71], v85 offset:0\n\t" \
  "ds_read_b128 v[52:55], v80 offset:9216\n\t" \
  "ds_read_b128 v[56:59], v80 offset:10240\n\t" \
  "ds_read_b128 v[60:63], v80 offset:11264\n\t" \
  ".Lp1%=:\n\t" \
  "s_waitcnt vmcnt(4)\n\t" \
  "s_barrier\n\t" \
  "s_waitcnt lgkmcnt(3)\n\t" \
  "v_mfma_f32_16x16x32_bf16 v[0:3], v[64:67], v[32:35], v[0:3]\n\t" \
  "ds_read_b64_tr_b16 v[72:73], v82 offset:8192\n\t" \
  "v_mfma_f32_16x16x32_bf16 v[4:7], v[68:71], v[32:35], v[4:7]\n\t" \
  "ds_read_b64_tr_b16 v[74:75], v83 offset:8192\n\t" \
  "ds_read_b128 v[32:35], v80 offset:32768\n\t" \
  "v_mfma_f32_16x16x32_bf16 v[8:11], v[64:67], v[36:39], v[8:11]\n\t" \
  "ds_read_b64_tr_b16 v[76:77], v84 offset:8192\n\t" \
  "v_mfma_f32_16x16x32_bf16 v[12:15], v[68:71], v[36:39], v[12:15]\n\t" \
  "ds_read_b64_tr_b16 v[78:79], v85 offset:8192\n\t" \
  "ds_read_b128 v[36:39], v80 offset:33792\n\t" \
  "s_add_u32 m0, %[lw], 98304\n\t" \
  "v_mfma_f32_16x16x32_bf16 v[16:19], v[64:67], v[40:43], v[16:19]\n\t" \
  "global_load_lds_dwordx4 v90, s[84:85] sc1\n\t" \
  "s_add_u32 s84, s84, s88\n\t" \
  "s_addc_u32 s85, s85, 0\n\t" \
  "v_mfma_f32_16x16x32_bf16 v[20:23], v[68:71], v[40:43], v[20:23]\n\t" \
  "ds_read_b128 v[40:43], v80 offset:34816\n\t" \
  "v_mfma_f32_16x16x32_bf16 v[24:27], v[64:67], v[44:47], v[24:27]\n\t" \
  "s_add_u32 m0, %[lw], 114688\n\t" \
  "v_mfma_f32_16x16x32_bf16 v[28:31], v[68:71], v[44:47], v[28:31]\n\t" \
  "ds_read_b128 v[44:47], v80 offset:35840\n\t" \
  "global_load_lds_dwordx4 v91, s[86:87]\n\t" \
  "s_add_u32 s86, s86, s89\n\t" \
  "s_addc_u32 s87, s87, 0\n\t" \
  "s_waitcnt lgkmcnt(3)\n\t" \
  "v_mfma_f32_16x16x32_bf16 v[0:3], v[72:75], v[48:51], v[0:3]\n\t" \
  "ds_read_b64_tr_b16 v[64:65], v82 offset:32768\n\t" \
  "v_mfma_f32_16x16x32_bf16 v[4:7], v[76:79], v[48:51], v[4:7]\n\t" \
  "ds_read_b64_tr_b16 v[66:67], v83 offset:32768\n\t" \
  "ds_read_b128 v[48:51], v80 offset:40960\n\t" \
  "v_mfma_f32_16x16x32_bf16 v[8:11], v[72:75], v[52:55], v[8:11]\n\t" \
  "ds_read_b64_tr_b16 v[68:69], v84 offset:32768\n\t" \
  "v_mfma_f32_16x16x32_bf16 v[12:15], v[76:79], v[52:55], v[12:15]\n\t" \
  "ds_read_b64_tr_b16 v[70:71], v85 offset:32768\n\t" \
  "ds_read_b128 v[52:55], v80 offset:41984\n\t" \
  "s_add_u32 m0, %[lw], 106496\n\t" \
  "v_mfma_f32_16x16x32_bf16 v[16:19], v[72:75], v[56:59], v[16:19]\n\t" \
  "global_load_lds_dwordx4 v90, s[84:85] sc1\n\t" \
  "s_add_u32 s84, s84, s88\n\t" \
  "s_addc_u32 s85, s85, 0\n\t" \
  "v_mfma_f32_16x16x32_bf16 v[20:23], v[76:79], v[56:59], v[20:23]\n\t" \
  "ds_read_b128 v[56:59], v80 offset:43008\n\t" \
  "v_mfma_f32_16x16x32_bf16 v[24:27], v[72:75], v[60:63], v[24:27]\n\t" \
  "s_add_u32 m0, %[lw], 122880\n\t" \
  "v_mfma_f32_16x16x32_bf16 v[28:31], v[76:79], v[60:63], v[28:31]\n\t" \
  "ds_read_b128 v[60:63], v80 offset:44032\n\t" \
  "global_load_lds_dwordx4 v91, s[86:87]\n\t" \
  "s_add_u32 s86, s86, s89\n\t" \
  "s_addc_u32 s87, s87, 0\n\t" \
  "s_sub_u32 s90, s90, 1\n\t" \
  "s_cselect_b32 s88, 0, s88\n\t" \
  "s_cselect_b32 s89, 0, s89\n\t" \
  "s_cselect_b32 s91, 16384, 0\n\t" \
  "s_sub_u32 s84, s84, s91\n\t" \
  "s_subb_u32 s85, s85, 0\n\t" \
  "s_lshl_b32 s91, s91, 2\n\t" \
  "s_sub_u32 s86, s86, s91\n\t" \
  "s_subb_u32 s87, s87, 0\n\t" \
  "s_waitcnt vmcnt(4)\n\t" \
  "s_barrier\n\t" \
  "s_waitcnt lgkmcnt(3)\n\t" \
  "v_mfma_f32_16x16x32_bf16 v[0:3], v[64:67], v[32:35], v[0:3]\n\t" \
  "ds_read_b64_tr_b16 v[72:73], v82 offset:40960\n\t" \
  "v_mfma_f32_16x16x32_bf16 v[4:7], v[68:71], v[32:35], v[4:7]\n\t" \
  "ds_read_b64_tr_b16 v[74:75], v83 offset:40960\n\t" \
  "ds_read_b128 v[32:35], v81 offset:0\n\t" \
  "v_mfma_f32_16x16x32_bf16 v[8:11], v[64:67], v[36:39], v[8:11]\n\t" \
  "ds_read_b64_tr_b16 v[76:77], v84 offset:40960\n\t" \
  "v_mfma_f32_16x16x32_bf16 v[12:15], v[68:71], v[36:39], v[12:15]\n\t" \
  "ds_read_b64_tr_b16 v[78:79], v85 offset:40960\n\t" \
  "ds_read_b128 v[36:39], v81 offset:1024\n\t" \
  "s_add_u32 m0, %[lw], 0\n\t" \
  "v_mfma_f32_16x16x32_bf16 v[16:19], v[64:67], v[40:43], v[16:19]\n\t" \
  "global_load_lds_dwordx4 v90, s[84:85] sc1\n\t" \
  "s_add_u32 s84, s84, s88\n\t" \
  "s_addc_u32 s85, s85, 0\n\t" \
  "v_mfma_f32_16x16x32_bf16 v[20:23], v[68:71], v[40:43], v[20:23]\n\t" \
  "ds_read_b128 v[40:43], v81 offset:2048\n\t" \
  "v_mfma_f32_16x16x32_bf16 v[24:27], v[64:67], v[44:47], v[24:27]\n\t" \
  "s_add_u32 m0, %[lw], 16384\n\t" \
  "v_mfma_f32_16x16x32_bf16 v[28:31], v[68:71], v[44:47], v[28:31]\n\t" \
  "ds_read_b128 v[44:47], v81 offset:3072\n\t" \
  "global_load_lds_dwordx4 v91, s[86:87]\n\t" \
  "s_add_u32 s86, s86, s89\n\t" \
  "s_addc_u32 s87, s87, 0\n\t" \
  "s_waitcnt lgkmcnt(3)\n\t" \
  "v_mfma_f32_16x16x32_bf16 v[0:3], v[72:75], v[48:51], v[0:3]\n\t" \
  "ds_read_b64_tr_b16 v[64:65], v86 offset:0\n\t" \
  "v_mfma_f32_16x16x32_bf16 v[4:7], v[76:79], v[48:51], v[4:7]\n\t" \
  "ds_read_b64_tr_b16 v[66:67], v87 offset:0\n\t" \
  "ds_read_b128 v[48:51], v81 offset:8192\n\t" \
  "v_mfma_f32_16x16x32_bf16 v[8:11], v[72:75], v[52:55], v[8:11]\n\t" \
  "ds_read_b64_tr_b16 v[68:69], v88 offset:0\n\t" \
  "v_mfma_f32_16x16x32_bf16 v[12:15], v[76:79], v[52:55], v[12:15]\n\t" \
  "ds_read_b64_tr_b16 v[70:71], v89 offset:0\n\t" \
  "ds_read_b128 v[52:55], v81 offset:9216\n\t" \
  "s_add_u32 m0, %[lw], 8192\n\t" \
  "v_mfma_f32_16x16x32_bf16 v[16:19], v[72:75], v[56:59], v[16:19]\n\t" \
  "global_load_lds_dwordx4 v90, s[84:85] sc1\n\t" \
  "s_add_u32 s84, s84, s88\n\t" \
  "s_addc_u32 s85, s85, 0\n\t" \
  "v_mfma_f32_16x16x32_bf16 v[20:23], v[76:79], v[56:59], v[20:23]\n\t" \
  "ds_read_b128 v[56:59], v81 offset:10240\n\t" \
  "v_mfma_f32_16x16x32_bf16 v[24:27], v[72:75], v[60:63], v[24:27]\n\t" \
  "s_add_u32 m0, %[lw], 24576\n\t" \
  "v_mfma_f32_16x16x32_bf16 v[28:31], v[76:79], v[60:63], v[28:31]\n\t" \
  "ds_read_b128 v[60:63], v81 offset:11264\n\t" \
  "global_load_lds_dwordx4 v91, s[86:87]\n\t" \
  "s_add_u32 s86, s86, s89\n\t" \
  "s_addc_u32 s87, s87, 0\n\t" \
  "s_waitcnt vmcnt(4)\n\t" \
  "s_barrier\n\t" \
  "s_waitcnt lgkmcnt(3)\n\t" \
  "v_mfma_f32_16x16x32_bf16 v[0:3], v[64:67], v[32:35], v[0:3]\n\t" \
  "ds_read_b64_tr_b16 v[72:73], v86 offset:8192\n\t" \
  "v_mfma_f32_16x16x32_bf16 v[4:7], v[68:71], v[32:35], v[4:7]\n\t" \
  "ds_read_b64_tr_b16 v[74:75], v87 offset:8192\n\t" \
  "ds_read_b128 v[32:35], v81 offset:32768\n\t" \
  "v_mfma_f32_16x16x32_bf16 v[8:11], v[64:67], v[36:39], v[8:11]\n\t" \
  "ds_read_b64_tr_b16 v[76:77], v88 offset:8192\n\t" \
  "v_mfma_f32_16x16x32_bf16 v[12:15], v[68:71], v[36:39], v[12:15]\n\t" \
  "ds_read_b64_tr_b16 v[78:79], v89 offset:8192\n\t" \
  "ds_read_b128 v[36:39], v81 offset:33792\n\t" \
  "s_add_u32 m0, %[lw], 32768\n\t" \
  "v_mfma_f32_16x16x32_bf16 v[16:19], v[64:67], v[40:43], v[16:19]\n\t" \
  "global_load_lds_dwordx4 v90, s[84:85] sc1\n\t" \
  "s_add_u32 s84, s84, s88\n\t" \
  "s_addc_u32 s85, s85, 0\n\t" \
  "v_mfma_f32_16x16x32_bf16 v[20:23], v[68:71], v[40:43], v[20:23]\n\t" \
  "ds_read_b128 v[40:43], v81 offset:34816\n\t" \
  "v_mfma_f32_16x16x32_bf16 v[24:27], v[64:67], v[44:47], v[24:27]\n\t" \
  "s_add_u32 m0, %[lw], 49152\n\t" \
  "v_mfma_f32_16x16x32_bf16 v[28:31], v[68:71], v[44:47], v[28:31]\n\t" \
  "ds_read_b128 v[44:47], v81 offset:35840\n\t" \
  "global_load_lds_dwordx4 v91, s[86:87]\n\t" \
  "s_add_u32 s86, s86, s89\n\t" \
  "s_addc_u32 s87, s87, 0\n\t" \
  "s_waitcnt lgkmcnt(3)\n\t" \
  "v_mfma_f32_16x16x32_bf16 v[0:3], v[72:75], v[48:51], v[0:3]\n\t" \
  "ds_read_b64_tr_b16 v[64:65], v86 offset:32768\n\t" \
  "v_mfma_f32_16x16x32_bf16 v[4:7], v[76:79], v[48:51], v[4:7]\n\t" \
  "ds_read_b64_tr_b16 v[66:67], v87 offset:32768\n\t" \
  "ds_read_b128 v[48:51], v81 offset:40960\n\t" \
  "v_mfma_f32_16x16x32_bf16 v[8:11], v[72:75], v[52:55], v[8:11]\n\t" \
  "ds_read_b64_tr_b16 v[68:69], v88 offset:32768\n\t" \
  "v_mfma_f32_16x16x32_bf16 v[12:15], v[76:79], v[52:55], v[12:15]\n\t" \
  "ds_read_b64_tr_b16 v[70:71], v89 offset:32768\n\t" \
  "ds_read_b128 v[52:55], v81 offset:41984\n\t" \
  "s_add_u32 m0, %[lw], 40960\n\t" \
  "v_mfma_f32_16x16x32_bf16 v[16:19], v[72:75], v[56:59], v[16:19]\n\t" \
  "global_load_lds_dwordx4 v90, s[84:85] sc1\n\t" \
  "s_add_u32 s84, s84, s88\n\t" \
  "s_addc_u32 s85, s85, 0\n\t" \
  "v_mfma_f32_16x16x32_bf16 v[20:23], v[76:79], v[56:59], v[20:23]\n\t" \
  "ds_read_b128 v[56:59], v81 offset:43008\n\t" \
  "v_mfma_f32_16x16x32_bf16 v[24:27], v[72:75], v[60:63], v[24:27]\n\t" \
  "s_add_u32 m0, %[lw], 57344\n\t" \
  "v_mfma_f32_16x16x32_bf16 v[28:31], v[76:79], v[60:63], v[28:31]\n\t" \
  "ds_read_b128 v[60:63], v81 offset:44032\n\t" \
  "global_load_lds_dwordx4 v91, s[86:87]\n\t" \
  "s_add_u32 s86, s86, s89\n\t" \
  "s_addc_u32 s87, s87, 0\n\t" \
  "s_waitcnt vmcnt(4)\n\t" \
  "s_barrier\n\t" \
  "s_waitcnt lgkmcnt(3)\n\t" \
  "v_mfma_f32_16x16x32_bf16 v[0:3], v[64:67], v[32:35], v[0:3]\n\t" \
  "ds_read_b64_tr_b16 v[72:73], v86 offset:40960\n\t" \
  "v_mfma_f32_16x16x32_bf16 v[4:7], v[68:71], v[32:35], v[4:7]\n\t" \
  "ds_read_b64_tr_b16 v[74:75], v87 offset:40960\n\t" \
  "ds_read_b128 v[32:35], v80 offset:0\n\t" \
  "v_mfma_f32_16x16x32_bf16 v[8:11], v[64:67], v[36:39], v[8:11]\n\t" \
  "ds_read_b64_tr_b16 v[76:77], v88 offset:40960\n\t" \
  "v_mfma_f32_16x16x32_bf16 v[12:15], v[68:71], v[36:39], v[12:15]\n\t" \
  "ds_read_b64_tr_b16 v[78:79], v89 offset:40960\n\t" \
  "ds_read_b128 v[36:39], v80 offset:1024\n\t" \
  "s_add_u32 m0, %[lw], 65536\n\t" \
  "v_mfma_f32_16x16x32_bf16 v[16:19], v[64:67], v[40:43], v[16:19]\n\t" \
  "global_load_lds_dwordx4 v90, s[84:85] sc1\n\t" \
  "s_add_u32 s84, s84, s88\n\t" \
  "s_addc_u32 s85, s85, 0\n\t" \
  "v_mfma_f32_16x16x32_bf16 v[20:23], v[68:71], v[40:43], v[20:23]\n\t" \
  "ds_read_b128 v[40:43], v80 offset:2048\n\t" \
  "v_mfma_f32_16x16x32_bf16 v[24:27], v[64:67], v[44:47], v[24:27]\n\t" \
  "s_add_u32 m0, %[lw], 81920\n\t" \
  "v_mfma_f32_16x16x32_bf16 v[28:31], v[68:71], v[44:47], v[28:31]\n\t" \
  "ds_read_b128 v[44:47], v80 offset:3072\n\t" \
  "global_load_lds_dwordx4 v91, s[86:87]\n\t" \
  "s_add_u32 s86, s86, s89\n\t" \
  "s_addc_u32 s87, s87, 0\n\t" \
  "s_waitcnt lgkmcnt(3)\n\t" \
  "v_mfma_f32_16x16x32_bf16 v[0:3], v[72:75], v[48:51], v[0:3]\n\t" \
  "ds_read_b64_tr_b16 v[64:65], v82 offset:0\n\t" \
  "v_mfma_f32_16x16x32_bf16 v[4:7], v[76:79], v[48:51], v[4:7]\n\t" \
  "ds_read_b64_tr_b16 v[66:67], v83 offset:0\n\t" \
  "ds_read_b128 v[48:51], v80 offset:8192\n\t" \
  "v_mfma_f32_16x16x32_bf16 v[8:11], v[72:75], v[52:55], v[8:11]\n\t" \
  "ds_read_b64_tr_b16 v[68:69], v84 offset:0\n\t" \
  "v_mfma_f32_16x16x32_bf16 v[12:15], v[76:79], v[52:55], v[12:15]\n\t" \
  "ds_read_b64_tr_b16 v[70:71], v85 offset:0\n\t" \
  "ds_read_b128 v[52:55], v80 offset:9216\n\t" \
  "s_add_u32 m0, %[lw], 73728\n\t" \
  "v_mfma_f32_16x16x32_bf16 v[16:19], v[72:75], v[56:59], v[16:19]\n\t" \
  "global_load_lds_dwordx4 v90, s[84:85] sc1\n\t" \
  "s_add_u32 s84, s84, s88\n\t" \
  "s_addc_u32 s85, s85, 0\n\t" \
  "v_mfma_f32_16x16x32_bf16 v[20:23], v[76:79], v[56:59], v[20:23]\n\t" \
  "ds_read_b128 v[56:59], v80 offset:10240\n\t" \
  "v_mfma_f32_16x16x32_bf16 v[24:27], v[72:75], v[60:63], v[24:27]\n\t" \
  "s_add_u32 m0, %[lw], 90112\n\t" \
  "v_mfma_f32_16x16x32_bf16 v[28:31], v[76:79], v[60:63], v[28:31]\n\t" \
  "ds_read_b128 v[60:63], v80 offset:11264\n\t" \
  "global_load_lds_dwordx4 v91, s[86:87]\n\t" \
  "s_add_u32 s86, s86, s89\n\t" \
  "s_addc_u32 s87, s87, 0\n\t" \
  "s_cmp_lg_u32 s88, 0\n\t" \
  "s_cbranch_scc1 .Lp1%=\n\t" \
  ".Le%=:\n\t" \
  "s_waitcnt vmcnt(0) lgkmcnt(0)\n\t" \
  "s_mov_b32 m0, s94\n\t" \
  "s_setprio 0\n\t" \
  "s_nop 7\n\t"
#define HSL 16
__device__ __forceinline__ void job_down(const Args& p, int rblk, int ct, char* smem, __amdgpu_buffer_rsrc_t rsy) {
  if (threadIdx.x < 64) {
    for (unsigned spins = 0;;) {
      asm volatile("" ::: "memory");
      if (__builtin_amdgcn_readfirstlane((int)ld_flag0(rsy, SY_CH + (rblk >> 1) * 128)) >= 27) break;
      __builtin_amdgcn_s_sleep(HSL);
      if (spin_fail(spins, (gu32*)(p.ws + WS_SY + SY_ABORT))) break;
    }
  }
  int tid = threadIdx.x;
  asm volatile("" : "+v"(tid));
  const int lane = tid & 63, r = lane & 15, q = lane >> 4;
  const int wid = __builtin_amdgcn_readfirstlane(tid >> 6), wr = wid >> 2, wc = wid & 3;
  typedef __attribute__((ext_vector_type(32))) float f32x32;
  typedef __attribute__((ext_vector_type(8))) unsigned u32x8;
  f32x32 acc;
  {
    const char* A = p.ws + WS_HF + h_off(p, rblk);
    const char* B = (const char*)(p.wd + ct * 128);
    const unsigned rb = tid >> 4, va = tid * 16, vb = ((rb & 4) * 4 + (rb >> 3) * 4 + (rb & 3)) * (DM * 2) + 16 * ((tid & 15) ^ swzB(rb & 15));
    u32x8 lb;
#pragma unroll
    for (int i = 0; i < 8; ++i)
      lb[i] = (i >> 2) * 65536 + 16384 + offB(8 * q + 4 * (i & 1) + (r >> 2), 2 * (wc * 2 + ((i >> 1) & 1)) + ((r & 3) >> 1)) + 8 * (r & 1);
    LBAR();
#pragma unroll
    for (int t = 0; t < 3; ++t) {
      glds2<true>(A + t * 32768, 16384, va, wid * 1024 + t * 32768);
      glds2(B + t * 64 * DM * 2, 32 * DM * 2, vb, wid * 1024 + t * 32768 + 16384);
    }
    WAIT_V(8);
    LBAR();
    asm volatile(DN_ASM
        : "={v[0:31]}"(acc)
        : "{v[80:81]}"((u32x2){wr * 4096u + lane * 16u, 65536u + wr * 4096u + lane * 16u}), "{v[82:89]}"(lb), "{v90}"(va), "{v91}"(vb),
          [pa] "s"(A + 3 * 32768), [pb] "s"(B + 3 * 64 * DM * 2), [lw] "s"(wid * 1024), [hf] "s"(wr)
        : CLB8(32, 33, 34, 35, 36, 37, 38, 39), CLB8(40, 41, 42, 43, 44, 45, 46, 47), CLB8(48, 49, 50, 51, 52, 53, 54, 55),
          CLB8(56, 57, 58, 59, 60, 61, 62, 63), CLB8(64, 65, 66, 67, 68, 69, 70, 71), CLB8(72, 73, 74, 75, 76, 77, 78, 79),
          "s84", "s85", "s86", "s87", "s88", "s89", "s90", "s91", "s94", "scc", "memory");
  }
  wait_s(p, rsy, rblk, ct * 4, 4, rblk < NFUSE);
  const int e0 = (rblk * 128 + wr * 64 + r) * DM + ct * 128 + wc * 32 + q * 8;
  SX sx[4];
#pragma unroll
  for (int m = 0; m < 4; ++m) sx[m] = get_sx(p, rblk, e0 + m * 16 * DM);
  const __amdgpu_buffer_rsrc_t ro = mkrsrc(p.ws + WS_G, S_LEN * DM * 2);
#pragma unroll
  for (int m = 0; m < 4; ++m) {
    float rr[8];
#pragma unroll
    for (int j = 0; j < 8; ++j) { float d = rbf(acc[8 * m + j]); rr[j] = d * __builtin_amdgcn_rcpf(1.0f + __expf(-d)); }
    put_g(p, ro, rblk, e0 + m * 16 * DM, widen32(cvtpk(rr[0], rr[1]), cvtpk(rr[2], rr[3]), cvtpk(rr[4], rr[5]), cvtpk(rr[6], rr[7])), sx[m]);
  }
  if (rblk >= NFUSE) publish_flags(rsy, SY_FG + (rblk * 8 + ct) * 128, 0, 1);
}

#define PR_ASM_0 \
  "s_cmp_lg_u32 %[kh], 0\n\t" \
  "s_cbranch_scc1 .Lk%=\n\t" \
  "s_mov_b32 s94, m0\n\t" \
  "s_mov_b64 s[92:93], %[x0]\n\t" \
  "s_mov_b32 s86, %[g0]\n\t" \
  "s_add_u32 s87, s86, %[t0]\n\t" \
  "s_sub_i32 s88, s86, 3\n\t" \
  "ds_read_b128 v[176:179], v208 offset:0\n\t" \
  "ds_read_b128 v[180:183], v208 offset:16384\n\t" \
  "ds_read_b128 v[184:187], v209 offset:0\n\t" \
  "ds_read_b128 v[188:191], v209 offset:16384\n\t" \
  "ds_read_b128 v[192:195], v210 offset:0\n\t" \
  "ds_read_b128 v[196:199], v210 offset:16384\n\t" \
  "ds_read_b128 v[200:203], v211 offset:0\n\t" \
  "ds_read_b128 v[204:207], v211 offset:16384\n\t" \
  "s_branch .Lfb00%=\n\t" \
  ".Lp0%=:\n\t" \
  "s_cmp_le_u32 s86, 5\n\t" \
  "s_cbranch_scc1 .Lf00%=\n\t" \
  ".Lfb00%=:\n\t" \
  "s_waitcnt lgkmcnt(6)\n\t" \
  "v_mfma_f32_16x16x32_bf16 v[128:131], v[0:3], v[176:179], 0\n\t" \
  "v_mfma_f32_16x16x32_bf16 v[132:135], v[4:7], v[176:179], 0\n\t" \
  "ds_read_b128 v[176:179], v208 offset:256\n\t" \
  "s_add_u32 m0, %[lw], 67584\n\t" \
  "v_mfma_f32_16x16x32_bf16 v[136:139], v[0:3], v[180:183], 0\n\t" \
  "v_mfma_f32_16x16x32_bf16 v[140:143], v[4:7], v[180:183], 0\n\t" \
  "ds_read_b128 v[180:183], v208 offset:16640\n\t" \
  "global_load_lds_dwordx4 v218, s[92:93] sc1\n\t" \
  "s_waitcnt lgkmcnt(6)\n\t" \
  "v_mfma_f32_16x16x32_bf16 v[128:131], v[8:11], v[184:187], v[128:131]\n\t" \
  "v_mfma_f32_16x16x32_bf16 v[132:135], v[12:15], v[184:187], v[132:135]\n\t" \
  "ds_read_b128 v[184:187], v209 offset:256\n\t" \
  "v_mfma_f32_16x16x32_bf16 v[136:139], v[8:11], v[188:191], v[136:139]\n\t" \
  "v_mfma_f32_16x16x32_bf16 v[140:143], v[12:15], v[188:191], v[140:143]\n\t" \
  "ds_read_b128 v[188:191], v209 offset:16640\n\t" \
  "ds_write_b128 v220, v[152:155] offset:0\n\t" \
  "ds_write_b128 v220, v[156:159] offset:1024\n\t" \
  "s_waitcnt lgkmcnt(8)\n\t" \
  "v_mfma_f32_16x16x32_bf16 v[128:131], v[16:19], v[192:195], v[128:131]\n\t" \
  "v_mfma_f32_16x16x32_bf16 v[132:135], v[20:23], v[192:195], v[132:135]\n\t" \
  "ds_read_b128 v[192:195], v210 offset:256\n\t" \
  "s_add_u32 m0, %[lw], 68608\n\t" \
  "v_mfma_f32_16x16x32_bf16 v[136:139], v[16:19], v[196:199], v[136:139]\n\t" \
  "v_mfma_f32_16x16x32_bf16 v[140:143], v[20:23], v[196:199], v[140:143]\n\t" \
  "ds_read_b128 v[196:199], v210 offset:16640\n\t" \
  "global_load_lds_dwordx4 v219, s[92:93] sc1\n\t" \
  "s_lshr_b32 s90, s87, 7\n\t" \
  "s_xor_b32 s90, s90, %[s0]\n\t" \
  "s_and_b32 s90, s90, 1\n\t" \
  "s_lshl1_add_u32 s90, s87, s90\n\t" \
  "s_lshl_b32 s90, s90, 13\n\t" \
  "s_cmp_lt_i32 s86, 6\n\t" \
  "s_cselect_b32 s90, 0x70000000, s90\n\t" \
  "s_waitcnt lgkmcnt(8)\n\t" \
  "v_mfma_f32_16x16x32_bf16 v[128:131], v[24:27], v[200:203], v[128:131]\n\t" \
  "v_mfma_f32_16x16x32_bf16 v[132:135], v[28:31], v[200:203], v[132:135]\n\t" \
  "ds_read_b128 v[200:203], v211 offset:256\n\t" \
  "v_mfma_f32_16x16x32_bf16 v[136:139], v[24:27], v[204:207], v[136:139]\n\t" \
  "v_mfma_f32_16x16x32_bf16 v[140:143], v[28:31], v[204:207], v[140:143]\n\t" \
  "ds_read_b128 v[204:207], v211 offset:16640\n\t" \
  "s_waitcnt lgkmcnt(4)\n\t" \
  "s_cmp_lt_u32 s86, 6\n\t" \
  "s_cbranch_scc1 .Lw00%=\n\t" \
  "s_waitcnt vmcnt(4)\n\t" \
  "s_barrier\n\t" \
  ".Lg00%=:\n\t" \
  "s_add_u32 s92, s92, 512\n\t" \
  "s_addc_u32 s93, s93, 0\n\t" \
  "v_mfma_f32_16x16x32_bf16 v[128:131], v[32:35], v[176:179], v[128:131]\n\t" \
  "v_mfma_f32_16x16x32_bf16 v[132:135], v[36:39], v[176:179], v[132:135]\n\t" \
  "ds_read_b128 v[176:179], v208 offset:32768\n\t" \
  "s_add_u32 m0, %[lw], 98304\n\t" \
  "v_mfma_f32_16x16x32_bf16 v[136:139], v[32:35], v[180:183], v[136:139]\n\t" \
  "v_mfma_f32_16x16x32_bf16 v[140:143], v[36:39], v[180:183], v[140:143]\n\t" \
  "ds_read_b128 v[180:183], v208 offset:49152\n\t" \
  "global_load_lds_dwordx4 v216, s[92:93] sc1\n\t" \
  "ds_read_b128 v[160:163], v221 offset:0\n\t" \
  "ds_read_b128 v[164:167], v221 offset:1024\n\t" \
  "v_mfma_f32_16x16x32_bf16 v[128:131], v[40:43], v[184:187], v[128:131]\n\t" \
  "v_mfma_f32_16x16x32_bf16 v[132:135], v[44:47], v[184:187], v[132:135]\n\t" \
  "ds_read_b128 v[184:187], v209 offset:32768\n\t" \
  "v_mfma_f32_16x16x32_bf16 v[136:139], v[40:43], v[188:191], v[136:139]\n\t" \
  "v_mfma_f32_16x16x32_bf16 v[140:143], v[44:47], v[188:191], v[140:143]\n\t" \
  "ds_read_b128 v[188:191], v209 offset:49152\n\t" \
  "s_waitcnt lgkmcnt(8)\n\t" \
  "v_mfma_f32_16x16x32_bf16 v[128:131], v[48:51], v[192:195], v[128:131]\n\t" \
  "v_mfma_f32_16x16x32_bf16 v[132:135], v[52:55], v[192:195], v[132:135]\n\t" \
  "ds_read_b128 v[192:195], v210 offset:32768\n\t" \
  "s_add_u32 m0, %[lw], 99328\n\t" \
  "v_mfma_f32_16x16x32_bf16 v[136:139], v[48:51], v[196:199], v[136:139]\n\t" \
  "v_mfma_f32_16x16x32_bf16 v[140:143], v[52:55], v[196:199], v[140:143]\n\t" \
  "ds_read_b128 v[196:199], v210 offset:49152\n\t" \
  "global_load_lds_dwordx4 v217, s[92:93] sc1\n\t" \
  "s_waitcnt lgkmcnt(4)\n\t" \
  "v_add_f32 v160, v160, v144\n\t" \
  "v_add_f32 v161, v161, v145\n\t" \
  "v_add_f32 v162, v162, v146\n\t" \
  "v_add_f32 v163, v163, v147\n\t" \
  "v_add_f32 v164, v164, v148\n\t" \
  "v_add_f32 v165, v165, v149\n\t" \
  "v_add_f32 v166, v166, v150\n\t" \
  "v_add_f32 v167, v167, v151\n\t" \
  "v_mfma_f32_16x16x32_bf16 v[128:131], v[56:59], v[200:203], v[128:131]\n\t" \
  "v_mfma_f32_16x16x32_bf16 v[132:135], v[60:63], v[200:203], v[132:135]\n\t" \
  "ds_read_b128 v[200:203], v211 offset:32768\n\t" \
  "v_mfma_f32_16x16x32_bf16 v[136:139], v[56:59], v[204:207], v[136:139]\n\t" \
  "v_mfma_f32_16x16x32_bf16 v[140:143], v[60:63], v[204:207], v[140:143]\n\t" \
  "ds_read_b128 v[204:207], v211 offset:49152\n\t" \
  "v_cvt_pk_bf16_f32 v168, v160, v161\n\t" \
  "v_cvt_pk_bf16_f32 v169, v162, v163\n\t" \
  "v_cvt_pk_bf16_f32 v170, v164, v165\n\t" \
  "v_cvt_pk_bf16_f32 v171, v166, v167\n\t" \
  "buffer_store_dwordx4 v[168:171], v172, %[rt], s90 offen sc1\n\t" \
  "v_mfma_f32_16x16x32_bf16 v[128:131], v[64:67], v[176:179], v[128:131]\n\t" \
  "v_mfma_f32_16x16x32_bf16 v[132:135], v[68:71], v[176:179], v[132:135]\n\t" \
  "ds_read_b128 v[176:179], v208 offset:33024\n\t" \
  "s_add_u32 m0, %[lw], 100352\n\t" \
  "v_mfma_f32_16x16x32_bf16 v[136:139], v[64:67], v[180:183], v[136:139]\n\t" \
  "v_mfma_f32_16x16x32_bf16 v[140:143], v[68:71], v[180:183], v[140:143]\n\t" \
  "ds_read_b128 v[180:183], v208 offset:49408\n\t" \
  "global_load_lds_dwordx4 v218, s[92:93] sc1\n\t" \
  "s_waitcnt lgkmcnt(6)\n\t" \
  "v_mfma_f32_16x16x32_bf16 v[128:131], v[72:75], v[184:187], v[128:131]\n\t" \
  "v_mfma_f32_16x16x32_bf16 v[132:135], v[76:79], v[184:187], v[132:135]\n\t" \
  "ds_read_b128 v[184:187], v209 offset:33024\n\t" \
  "v_mfma_f32_16x16x32_bf16 v[136:139], v[72:75], v[188:191], v[136:139]\n\t" \
  "v_mfma_f32_16x16x32_bf16 v[140:143], v[76:79], v[188:191], v[140:143]\n\t" \
  "ds_read_b128 v[188:191], v209 offset:49408\n\t" \
  "s_waitcnt lgkmcnt(6)\n\t" \
  "v_mfma_f32_16x16x32_bf16 v[128:131], v[80:83], v[192:195], v[128:131]\n\t" \
  "v_mfma_f32_16x16x32_bf16 v[132:135], v[84:87], v[192:195], v[132:135]\n\t" \
  "ds_read_b128 v[192:195], v210 offset:33024\n\t" \
  "s_add_u32 m0, %[lw], 101376\n\t" \
  "v_mfma_f32_16x16x32_bf16 v[136:139], v[80:83], v[196:199], v[136:139]\n\t" \
  "v_mfma_f32_16x16x32_bf16 v[140:143], v[84:87], v[196:199], v[140:143]\n\t" \
  "ds_read_b128 v[196:199], v210 offset:49408\n\t" \
  "global_load_lds_dwordx4 v219, s[92:93] sc1\n\t" \
  "s_waitcnt lgkmcnt(6)\n\t" \
  "v_mfma_f32_16x16x32_bf16 v[128:131], v[88:91], v[200:203], v[128:131]\n\t" \
  "v_mfma_f32_16x16x32_bf16 v[132:135], v[92:95], v[200:203], v[132:135]\n\t" \
  "ds_read_b128 v[200:203], v211 offset:33024\n\t" \
  "v_mfma_f32_16x16x32_bf16 v[136:139], v[88:91], v[204:207], v[136:139]\n\t" \
  "v_mfma_f32_16x16x32_bf16 v[140:143], v[92:95], v[204:207], v[140:143]\n\t" \
  "ds_read_b128 v[204:207], v211 offset:49408\n\t" \
  "s_cmp_lt_u32 s86, 8\n\t" \
  "s_cbranch_scc1 .Lw10%=\n\t" \
  "s_waitcnt vmcnt(5)\n\t" \
  "s_barrier\n\t" \
  ".Lg10%=:\n\t" \
  "s_add_u32 s92, s92, 65024\n\t" \
  "s_addc_u32 s93, s93, 0\n\t" \
  "s_waitcnt lgkmcnt(6)\n\t" \
  "v_mfma_f32_16x16x32_bf16 v[128:131], v[96:99], v[176:179], v[128:131]\n\t" \
  "v_mfma_f32_16x16x32_bf16 v[132:135], v[100:103], v[176:179], v[132:135]\n\t" \
  "ds_read_b128 v[176:179], v212 offset:0\n\t" \
  "s_add_u32 m0, %[lw], 0\n\t" \
  "v_mfma_f32_16x16x32_bf16 v[136:139], v[96:99], v[180:183], v[136:139]\n\t" \
  "v_mfma_f32_16x16x32_bf16 v[140:143], v[100:103], v[180:183], v[140:143]\n\t" \
  "ds_read_b128 v[180:183], v212 offset:16384\n\t" \
  "global_load_lds_dwordx4 v216, s[92:93] sc1\n\t" \
  "s_waitcnt lgkmcnt(6)\n\t" \
  "v_mfma_f32_16x16x32_bf16 v[128:131], v[104:107], v[184:187], v[128:131]\n\t" \
  "v_mfma_f32_16x16x32_bf16 v[132:135], v[108:111], v[184:187], v[132:135]\n\t" \
  "ds_read_b128 v[184:187], v213 offset:0\n\t" \
  "v_mfma_f32_16x16x32_bf16 v[136:139], v[104:107], v[188:191], v[136:139]\n\t" \
  "v_mfma_f32_16x16x32_bf16 v[140:143], v[108:111], v[188:191], v[140:143]\n\t" \
  "ds_read_b128 v[188:191], v213 offset:16384\n\t" \
  "s_waitcnt lgkmcnt(6)\n\t" \
  "v_mfma_f32_16x16x32_bf16 v[128:131], v[112:115], v[192:195], v[128:131]\n\t" \
  "v_mfma_f32_16x16x32_bf16 v[132:135], v[116:119], v[192:195], v[132:135]\n\t" \
  "ds_read_b128 v[192:195], v214 offset:0\n\t" \
  "s_add_u32 m0, %[lw], 1024\n\t" \
  "v_mfma_f32_16x16x32_bf16 v[136:139], v[112:115], v[196:199], v[136:139]\n\t" \
  "v_mfma_f32_16x16x32_bf16 v[140:143], v[116:119], v[196:199], v[140:143]\n\t" \
  "ds_read_b128 v[196:199], v214 offset:16384\n\t" \
  "global_load_lds_dwordx4 v217, s[92:93] sc1\n\t" \
  "s_waitcnt lgkmcnt(6)\n\t" \
  "v_mfma_f32_16x16x32_bf16 v[128:131], v[120:123], v[200:203], v[128:131]\n\t" \
  "v_mfma_f32_16x16x32_bf16 v[132:135], v[124:127], v[200:203], v[132:135]\n\t" \
  "ds_read_b128 v[200:203], v215 offset:0\n\t" \
  "v_mfma_f32_16x16x32_bf16 v[136:139], v[120:123], v[204:207], v[136:139]\n\t" \
  "v_mfma_f32_16x16x32_bf16 v[140:143], v[124:127], v[204:207], v[140:143]\n\t" \
  "ds_read_b128 v[204:207], v215 offset:16384\n\t" \
  "s_add_u32 s86, s86, 1\n\t" \
  "s_add_u32 s87, s87, 1\n\t" \
  "s_cmp_le_u32 s86, 5\n\t" \
  "s_cbranch_scc1 .Lf10%=\n\t" \
  ".Lfb10%=:\n\t" \
  "s_waitcnt lgkmcnt(6)\n\t" \
  "v_mfma_f32_16x16x32_bf16 v[144:147], v[0:3], v[176:179], 0\n\t" \
  "v_mfma_f32_16x16x32_bf16 v[148:151], v[4:7], v[176:179], 0\n\t" \
  "ds_read_b128 v[176:179], v212 offset:256\n\t" \
  "s_add_u32 m0, %[lw], 2048\n\t" \
  "v_mfma_f32_16x16x32_bf16 v[152:155], v[0:3], v[180:183], 0\n\t" \
  "v_mfma_f32_16x16x32_bf16 v[156:159], v[4:7], v[180:183], 0\n\t" \
  "ds_read_b128 v[180:183], v212 offset:16640\n\t" \
  "global_load_lds_dwordx4 v218, s[92:93] sc1\n\t" \
  "s_waitcnt lgkmcnt(6)\n\t" \
  "v_mfma_f32_16x16x32_bf16 v[144:147], v[8:11], v[184:187], v[144:147]\n\t" \
  "v_mfma_f32_16x16x32_bf16 v[148:151], v[12:15], v[184:187], v[148:151]\n\t" \
  "ds_read_b128 v[184:187], v213 offset:256\n\t" \
  "v_mfma_f32_16x16x32_bf16 v[152:155], v[8:11], v[188:191], v[152:155]\n\t" \
  "v_mfma_f32_16x16x32_bf16 v[156:159], v[12:15], v[188:191], v[156:159]\n\t" \
  "ds_read_b128 v[188:191], v213 offset:16640\n\t" \
  "ds_write_b128 v220, v[136:139] offset:0\n\t" \
  "ds_write_b128 v220, v[140:143] offset:1024\n\t" \
  "s_waitcnt lgkmcnt(8)\n\t" \
  "v_mfma_f32_16x16x32_bf16 v[144:147], v[16:19], v[192:195], v[144:147]\n\t" \
  "v_mfma_f32_16x16x32_bf16 v[148:151], v[20:23], v[192:195], v[148:151]\n\t" \
  "ds_read_b128 v[192:195], v214 offset:256\n\t" \
  "s_add_u32 m0, %[lw], 3072\n\t" \
  "v_mfma_f32_16x16x32_bf16 v[152:155], v[16:19], v[196:199], v[152:155]\n\t" \
  "v_mfma_f32_16x16x32_bf16 v[156:159], v[20:23], v[196:199], v[156:159]\n\t" \
  "ds_read_b128 v[196:199], v214 offset:16640\n\t" \
  "global_load_lds_dwordx4 v219, s[92:93] sc1\n\t" \
  "s_lshr_b32 s90, s87, 7\n\t" \
  "s_xor_b32 s90, s90, %[s0]\n\t" \
  "s_and_b32 s90, s90, 1\n\t" \
  "s_lshl1_add_u32 s90, s87, s90\n\t" \
  "s_lshl_b32 s90, s90, 13\n\t" \
  "s_cmp_lt_i32 s86, 6\n\t" \
  "s_cselect_b32 s90, 0x70000000, s90\n\t" \
  "s_waitcnt lgkmcnt(8)\n\t" \
  "v_mfma_f32_16x16x32_bf16 v[144:147], v[24:27], v[200:203], v[144:147]\n\t" \
  "v_mfma_f32_16x16x32_bf16 v[148:151], v[28:31], v[200:203], v[148:151]\n\t" \
  "ds_read_b128 v[200:203], v215 offset:256\n\t" \
  "v_mfma_f32_16x16x32_bf16 v[152:155], v[24:27], v[204:207], v[152:155]\n\t" \
  "v_mfma_f32_16x16x32_bf16 v[156:159], v[28:31], v[204:207], v[156:159]\n\t" \
  "ds_read_b128 v[204:207], v215 offset:16640\n\t" \
  "s_waitcnt lgkmcnt(4)\n\t" \
  "s_cmp_lt_u32 s86, 6\n\t" \
  "s_cbranch_scc1 .Lw20%=\n\t" \
  "s_waitcnt vmcnt(4)\n\t" \
  "s_barrier\n\t" \
  ".Lg20%=:\n\t" \
  "s_add_u32 s92, s92, 512\n\t" \
  "s_addc_u32 s93, s93, 0\n\t" \
  "v_mfma_f32_16x16x32_bf16 v[144:147], v[32:35], v[176:179], v[144:147]\n\t" \
  "v_mfma_f32_16x16x32_bf16 v[148:151], v[36:39], v[176:179], v[148:151]\n\t" \
  "ds_read_b128 v[176:179], v212 offset:32768\n\t" \
  "s_add_u32 m0, %[lw], 32768\n\t" \
  "v_mfma_f32_16x16x32_bf16 v[152:155], v[32:35], v[180:183], v[152:155]\n\t" \
  "v_mfma_f32_16x16x32_bf16 v[156:159], v[36:39], v[180:183], v[156:159]\n\t" \
  "ds_read_b128 v[180:183], v212 offset:49152\n\t" \
  "global_load_lds_dwordx4 v216, s[92:93] sc1\n\t" \
  "ds_read_b128 v[160:163], v221 offset:0\n\t" \
  "ds_read_b128 v[164:167], v221 offset:1024\n\t" \
  "v_mfma_f32_16x16x32_bf16 v[144:147], v[40:43], v[184:187], v[144:147]\n\t" \
  "v_mfma_f32_16x16x32_bf16 v[148:151], v[44:47], v[184:187], v[148:151]\n\t" \
  "ds_read_b128 v[184:187], v213 offset:32768\n\t" \
  "v_mfma_f32_16x16x32_bf16 v[152:155], v[40:43], v[188:191], v[152:155]\n\t" \
  "v_mfma_f32_16x16x32_bf16 v[156:159], v[44:47], v[188:191], v[156:159]\n\t" \
  "ds_read_b128 v[188:191], v213 offset:49152\n\t" \
  "s_waitcnt lgkmcnt(8)\n\t" \
  "v_mfma_f32_16x16x32_bf16 v[144:147], v[48:51], v[192:195], v[144:147]\n\t" \
  "v_mfma_f32_16x16x32_bf16 v[148:151], v[52:55], v[192:195], v[148:151]\n\t" \
  "ds_read_b128 v[192:195], v214 offset:32768\n\t" \
  "s_add_u32 m0, %[lw], 33792\n\t" \
  "v_mfma_f32_16x16x32_bf16 v[152:155], v[48:51], v[196:199], v[152:155]\n\t" \
  "v_mfma_f32_16x16x32_bf16 v[156:159], v[52:55], v[196:199], v[156:159]\n\t" \
  "ds_read_b128 v[196:199], v214 offset:49152\n\t" \
  "global_load_lds_dwordx4 v217, s[92:93] sc1\n\t" \
  "s_waitcnt lgkmcnt(4)\n\t" \
  "v_add_f32 v160, v160, v128\n\t" \
  "v_add_f32 v161, v161, v129\n\t" \
  "v_add_f32 v162, v162, v130\n\t" \
  "v_add_f32 v163, v163, v131\n\t" \
  "v_add_f32 v164, v164, v132\n\t" \
  "v_add_f32 v165, v165, v133\n\t" \
  "v_add_f32 v166, v166, v134\n\t" \
  "v_add_f32 v167, v167, v135\n\t" \
  "v_mfma_f32_16x16x32_bf16 v[144:147], v[56:59], v[200:203], v[144:147]\n\t" \
  "v_mfma_f32_16x16x32_bf16 v[148:151], v[60:63], v[200:203], v[148:151]\n\t" \
  "ds_read_b128 v[200:203], v215 offset:32768\n\t" \
  "v_mfma_f32_16x16x32_bf16 v[152:155], v[56:59], v[204:207], v[152:155]\n\t" \
  "v_mfma_f32_16x16x32_bf16 v[156:159], v[60:63], v[204:207], v[156:159]\n\t" \
  "ds_read_b128 v[204:207], v215 offset:49152\n\t" \
  "v_cvt_pk_bf16_f32 v168, v160, v161\n\t" \
  "v_cvt_pk_bf16_f32 v169, v162, v163\n\t" \
  "v_cvt_pk_bf16_f32 v170, v164, v165\n\t" \
  "v_cvt_pk_bf16_f32 v171, v166, v167\n\t" \
  "buffer_store_dwordx4 v[168:171], v172, %[rt], s90 offen sc1\n\t" \
  "v_mfma_f32_16x16x32_bf16 v[144:147], v[64:67], v[176:179], v[144:147]\n\t" \
  "v_mfma_f32_16x16x32_bf16 v[148:151], v[68:71], v[176:179], v[148:151]\n\t" \
  "ds_read_b128 v[176:179], v212 offset:33024\n\t" \
  "s_add_u32 m0, %[lw], 34816\n\t" \
  "v_mfma_f32_16x16x32_bf16 v[152:155], v[64:67], v[180:183], v[152:155]\n\t" \
  "v_mfma_f32_16x16x32_bf16 v[156:159], v[68:71], v[180:183], v[156:159]\n\t" \
  "ds_read_b128 v[180:183], v212 offset:49408\n\t" \
  "global_load_lds_dwordx4 v218, s[92:93] sc1\n\t" \
  "s_waitcnt lgkmcnt(6)\n\t" \
  "v_mfma_f32_16x16x32_bf16 v[144:147], v[72:75], v[184:187], v[144:147]\n\t" \
  "v_mfma_f32_16x16x32_bf16 v[148:151], v[76:79], v[184:187], v[148:151]\n\t" \
  "ds_read_b128 v[184:187], v213 offset:33024\n\t" \
  "v_mfma_f32_16x16x32_bf16 v[152:155], v[72:75], v[188:191], v[152:155]\n\t" \
  "v_mfma_f32_16x16x32_bf16 v[156:159], v[76:79], v[188:191], v[156:159]\n\t" \
  "ds_read_b128 v[188:191], v213 offset:49408\n\t" \
  "s_waitcnt lgkmcnt(6)\n\t" \
  "v_mfma_f32_16x16x32_bf16 v[144:147], v[80:83], v[192:195], v[144:147]\n\t" \
  "v_mfma_f32_16x16x32_bf16 v[148:151], v[84:87], v[192:195], v[148:151]\n\t" \
  "ds_read_b128 v[192:195], v214 offset:33024\n\t" \
  "s_add_u32 m0, %[lw], 35840\n\t" \
  "v_mfma_f32_16x16x32_bf16 v[152:155], v[80:83], v[196:199], v[152:155]\n\t" \
  "v_mfma_f32_16x16x32_bf16 v[156:159], v[84:87], v[196:199], v[156:159]\n\t" \
  "ds_read_b128 v[196:199], v214 offset:49408\n\t" \
  "global_load_lds_dwordx4 v219, s[92:93] sc1\n\t" \
  "s_waitcnt lgkmcnt(6)\n\t" \
  "v_mfma_f32_16x16x32_bf16 v[144:147], v[88:91], v[200:203], v[144:147]\n\t" \
  "v_mfma_f32_16x16x32_bf16 v[148:151], v[92:95], v[200:203], v[148:151]\n\t" \
  "ds_read_b128 v[200:203], v215 offset:33024\n\t" \
  "v_mfma_f32_16x16x32_bf16 v[152:155], v[88:91], v[204:207], v[152:155]\n\t" \
  "v_mfma_f32_16x16x32_bf16 v[156:159], v[92:95], v[204:207], v[156:159]\n\t" \
  "ds_read_b128 v[204:207], v215 offset:49408\n\t" \
  "s_cmp_lt_u32 s86, 8\n\t" \
  "s_cbranch_scc1 .Lw30%=\n\t" \
  "s_waitcnt vmcnt(5)\n\t" \
  "s_barrier\n\t" \
  ".Lg30%=:\n\t" \
  "s_add_u32 s92, s92, 65024\n\t" \
  "s_addc_u32 s93, s93, 0\n\t" \
  "s_waitcnt lgkmcnt(6)\n\t" \
  "v_mfma_f32_16x16x32_bf16 v[144:147], v[96:99], v[176:179], v[144:147]\n\t" \
  "v_mfma_f32_16x16x32_bf16 v[148:151], v[100:103], v[176:179], v[148:151]\n\t" \
  "ds_read_b128 v[176:179], v208 offset:0\n\t" \
  "s_add_u32 m0, %[lw], 65536\n\t" \
  "v_mfma_f32_16x16x32_bf16 v[152:155], v[96:99], v[180:183], v[152:155]\n\t" \
  "v_mfma_f32_16x16x32_bf16 v[156:159], v[100:103], v[180:183], v[156:159]\n\t" \
  "ds_read_b128 v[180:183], v208 offset:16384\n\t" \
  "global_load_lds_dwordx4 v216, s[92:93] sc1\n\t" \
  "s_waitcnt lgkmcnt(6)\n\t" \
  "v_mfma_f32_16x16x32_bf16 v[144:147], v[104:107], v[184:187], v[144:147]\n\t" \
  "v_mfma_f32_16x16x32_bf16 v[148:151], v[108:111], v[184:187], v[148:151]\n\t" \
  "ds_read_b128 v[184:187], v209 offset:0\n\t" \
  "v_mfma_f32_16x16x32_bf16 v[152:155], v[104:107], v[188:191], v[152:155]\n\t" \
  "v_mfma_f32_16x16x32_bf16 v[156:159], v[108:111], v[188:191], v[156:159]\n\t" \
  "ds_read_b128 v[188:191], v209 offset:16384\n\t" \
  "s_waitcnt lgkmcnt(6)\n\t" \
  "v_mfma_f32_16x16x32_bf16 v[144:147], v[112:115], v[192:195], v[144:147]\n\t" \
  "v_mfma_f32_16x16x32_bf16 v[148:151], v[116:119], v[192:195], v[148:151]\n\t" \
  "ds_read_b128 v[192:195], v210 offset:0\n\t" \
  "s_add_u32 m0, %[lw], 66560\n\t" \
  "v_mfma_f32_16x16x32_bf16 v[152:155], v[112:115], v[196:199], v[152:155]\n\t" \
  "v_mfma_f32_16x16x32_bf16 v[156:159], v[116:119], v[196:199], v[156:159]\n\t" \
  "ds_read_b128 v[196:199], v210 offset:16384\n\t" \
  "global_load_lds_dwordx4 v217, s[92:93] sc1\n\t" \
  "s_waitcnt lgkmcnt(6)\n\t" \
  "v_mfma_f32_16x16x32_bf16 v[144:147], v[120:123], v[200:203], v[144:147]\n\t" \
  "v_mfma_f32_16x16x32_bf16 v[148:151], v[124:127], v[200:203], v[148:151]\n\t" \
  "ds_read_b128 v[200:203], v211 offset:0\n\t" \
  "v_mfma_f32_16x16x32_bf16 v[152:155], v[120:123], v[204:207], v[152:155]\n\t" \
  "v_mfma_f32_16x16x32_bf16 v[156:159], v[124:127], v[204:207], v[156:159]\n\t" \
  "ds_read_b128 v[204:207], v211 offset:16384\n\t" \
  "s_add_u32 s86, s86, 1\n\t" \
  "s_add_u32 s87, s87, 1\n\t" \
  "s_cmp_lt_u32 s86, %[end]\n\t" \
  "s_cbranch_scc1 .Lp0%=\n\t" \
  "s_branch .Lx0%=\n\t" \
  ".Lf00%=:\n\t" \
  "s_nop 7\n\t" \
  "ds_write_b128 v220, v[152:155] offset:0\n\t" \
  "ds_write_b128 v220, v[156:159] offset:1024\n\t" \
  "s_lshr_b32 s90, s87, 7\n\t" \
  "s_xor_b32 s90, s90, %[s0]\n\t" \
  "s_and_b32 s90, s90, 1\n\t" \
  "s_lshl1_add_u32 s90, s87, s90\n\t" \
  "s_lshl_b32 s90, s90, 13\n\t" \
  "s_cmp_lt_i32 s86, 1\n\t" \
  "s_cselect_b32 s90, 0x70000000, s90\n\t" \
  "s_waitcnt lgkmcnt(0)\n\t" \
  "s_barrier\n\t" \
  "ds_read_b128 v[160:163], v221 offset:0\n\t" \
  "ds_read_b128 v[164:167], v221 offset:1024\n\t" \
  "s_waitcnt lgkmcnt(0)\n\t" \
  "v_add_f32 v160, v160, v144\n\t" \
  "v_add_f32 v161, v161, v145\n\t" \
  "v_add_f32 v162, v162, v146\n\t" \
  "v_add_f32 v163, v163, v147\n\t" \
  "v_add_f32 v164, v164, v148\n\t" \
  "v_add_f32 v165, v165, v149\n\t" \
  "v_add_f32 v166, v166, v150\n\t" \
  "v_add_f32 v167, v167, v151\n\t" \
  "v_cvt_pk_bf16_f32 v168, v160, v161\n\t" \
  "v_cvt_pk_bf16_f32 v169, v162, v163\n\t" \
  "v_cvt_pk_bf16_f32 v170, v164, v165\n\t" \
  "v_cvt_pk_bf16_f32 v171, v166, v167\n\t" \
  "buffer_store_dwordx4 v[168:171], v172, %[rt], s90 offen sc1\n\t" \
  "s_branch .Lfb00%=\n\t" \
  ".Lf10%=:\n\t" \
  "s_nop 7\n\t" \
  "ds_write_b128 v220, v[136:139] offset:0\n\t" \
  "ds_write_b128 v220, v[140:143] offset:1024\n\t" \
  "s_lshr_b32 s90, s87, 7\n\t" \
  "s_xor_b32 s90, s90, %[s0]\n\t" \
  "s_and_b32 s90, s90, 1\n\t" \
  "s_lshl1_add_u32 s90, s87, s90\n\t" \
  "s_lshl_b32 s90, s90, 13\n\t" \
  "s_cmp_lt_i32 s86, 1\n\t" \
  "s_cselect_b32 s90, 0x70000000, s90\n\t" \
  "s_waitcnt lgkmcnt(0)\n\t" \
  "s_barrier\n\t" \
  "ds_read_b128 v[160:163], v221 offset:0\n\t" \
  "ds_read_b128 v[164:167], v221 offset:1024\n\t" \
  "s_waitcnt lgkmcnt(0)\n\t" \
  "v_add_f32 v160, v160, v128\n\t" \
  "v_add_f32 v161, v161, v129\n\t" \
  "v_add_f32 v162, v162, v130\n\t" \
  "v_add_f32 v163, v163, v131\n\t" \
  "v_add_f32 v164, v164, v132\n\t" \
  "v_add_f32 v165, v165, v133\n\t" \
  "v_add_f32 v166, v166, v134\n\t" \
  "v_add_f32 v167, v167, v135\n\t" \
  "v_cvt_pk_bf16_f32 v168, v160, v161\n\t" \
  "v_cvt_pk_bf16_f32 v169, v162, v163\n\t" \
  "v_cvt_pk_bf16_f32 v170, v164, v165\n\t" \
  "v_cvt_pk_bf16_f32 v171, v166, v167\n\t" \
  "buffer_store_dwordx4 v[168:171], v172, %[rt], s90 offen sc1\n\t" \
  "s_branch .Lfb10%=\n\t" \
  ".Lw00%=:\n\t" \
  "s_waitcnt vmcnt(2)\n\t" \
  "s_barrier\n\t" \
  "s_branch .Lg00%=\n\t" \
  ".Lw10%=:\n\t" \
  "s_waitcnt vmcnt(2)\n\t" \
  "s_barrier\n\t" \
  "s_branch .Lg10%=\n\t" \
  ".Lw20%=:\n\t" \
  "s_waitcnt vmcnt(2)\n\t" \
  "s_barrier\n\t" \
  "s_branch .Lg20%=\n\t" \
  ".Lw30%=:\n\t" \
  "s_waitcnt vmcnt(2)\n\t" \
  "s_barrier\n\t" \
  "s_branch .Lg30%=\n\t" \
  ".Lx0%=:\n\t" \
  "s_waitcnt lgkmcnt(0)\n\t" \
  "s_setprio 0\n\t" \
  "s_mov_b32 m0, s94\n\t" \
  "s_branch .Le%=\n\t" \
  ".Lk%=:\n\t" \
  "s_mov_b32 s94, m0\n\t" \
  "s_mov_b64 s[92:93], %[x0]\n\t" \
  "s_mov_b32 s86, %[g0]\n\t" \
  "s_add_u32 s87, s86, %[t0]\n\t" \
  "s_sub_i32 s88, s86, 3\n\t" \
  "ds_read_b128 v[176:179], v208 offset:0\n\t" \
  "ds_read_b128 v[180:183], v208 offset:16384\n\t" \
  "ds_read_b128 v[184:187], v209 offset:0\n\t" \
  "ds_read_b128 v[188:191], v209 offset:16384\n\t" \
  "ds_read_b128 v[192:195], v210 offset:0\n\t" \
  "ds_read_b128 v[196:199], v210 offset:16384\n\t" \
  "ds_read_b128 v[200:203], v211 offset:0\n\t" \
  "ds_read_b128 v[204:207], v211 offset:16384\n\t" \
  "s_branch .Lfb01%=\n\t" \
  ".Lp1%=:\n\t" \
  "s_cmp_le_u32 s86, 5\n\t" \
  "s_cbranch_scc1 .Lf01%=\n\t" \
  ".Lfb01%=:\n\t" \
  "s_waitcnt lgkmcnt(6)\n\t" \
  "v_mfma_f32_16x16x32_bf16 v[128:131], v[0:3], v[176:179], 0\n\t" \
  "v_mfma_f32_16x16x32_bf16 v[132:135], v[4:7], v[176:179], 0\n\t" \
  "ds_read_b128 v[176:179], v208 offset:256\n\t" \
  "v_mfma_f32_16x16x32_bf16 v[136:139], v[0:3], v[180:183], 0\n\t" \
  "v_mfma_f32_16x16x32_bf16 v[140:143], v[4:7], v[180:183], 0\n\t" \
  "ds_read_b128 v[180:183], v208 offset:16640\n\t" \
  "s_waitcnt lgkmcnt(6)\n\t" \
  "v_mfma_f32_16x16x32_bf16 v[128:131], v[8:11], v[184:187], v[128:131]\n\t" \
  "v_mfma_f32_16x16x32_bf16 v[132:135], v[12:15], v[184:187], v[132:135]\n\t" \
  "ds_read_b128 v[184:187], v209 offset:256\n\t" \
  "s_add_u32 m0, %[lw], 67584\n\t" \
  "v_mfma_f32_16x16x32_bf16 v[136:139], v[8:11], v[188:191], v[136:139]\n\t" \
  "v_mfma_f32_16x16x32_bf16 v[140:143], v[12:15], v[188:191], v[140:143]\n\t" \
  "ds_read_b128 v[188:191], v209 offset:16640\n\t" \
  "global_load_lds_dwordx4 v218, s[92:93] sc1\n\t" \
  "ds_write_b128 v220, v[152:155] offset:0\n\t" \
  "ds_write_b128 v220, v[156:159] offset:1024\n\t" \
  "s_waitcnt lgkmcnt(8)\n\t" \
  "v_mfma_f32_16x16x32_bf16 v[128:131], v[16:19], v[192:195], v[128:131]\n\t" \
  "v_mfma_f32_16x16x32_bf16 v[132:135], v[20:23], v[192:195], v[132:135]\n\t" \
  "ds_read_b128 v[192:195], v210 offset:256\n\t" \
  "v_mfma_f32_16x16x32_bf16 v[136:139], v[16:19], v[196:199], v[136:139]\n\t" \
  "v_mfma_f32_16x16x32_bf16 v[140:143], v[20:23], v[196:199], v[140:143]\n\t" \
  "ds_read_b128 v[196:199], v210 offset:16640\n\t" \
  "s_lshr_b32 s90, s87, 7\n\t" \
  "s_xor_b32 s90, s90, %[s0]\n\t" \
  "s_and_b32 s90, s90, 1\n\t" \
  "s_lshl1_add_u32 s90, s87, s90\n\t" \
  "s_lshl_b32 s90, s90, 13\n\t" \
  "s_cmp_lt_i32 s86, 6\n\t" \
  "s_cselect_b32 s90, 0x70000000, s90\n\t" \
  "s_waitcnt lgkmcnt(8)\n\t" \
  "v_mfma_f32_16x16x32_bf16 v[128:131], v[24:27], v[200:203], v[128:131]\n\t" \
  "v_mfma_f32_16x16x32_bf16 v[132:135], v[28:31], v[200:203], v[132:135]\n\t" \
  "ds_read_b128 v[200:203], v211 offset:256\n\t" \
  "s_add_u32 m0, %[lw], 68608\n\t" \
  "v_mfma_f32_16x16x32_bf16 v[136:139], v[24:27], v[204:207], v[136:139]\n\t" \
  "v_mfma_f32_16x16x32_bf16 v[140:143], v[28:31], v[204:207], v[140:143]\n\t" \
  "ds_read_b128 v[204:207], v211 offset:16640\n\t" \
  "global_load_lds_dwordx4 v219, s[92:93] sc1\n\t" \
  "s_waitcnt lgkmcnt(4)\n\t" \
  "s_cmp_lt_u32 s86, 6\n\t" \
  "s_cbranch_scc1 .Lw01%=\n\t" \
  "s_waitcnt vmcnt(4)\n\t" \
  "s_barrier\n\t" \
  ".Lg01%=:\n\t" \
  "s_add_u32 s92, s92, 512\n\t" \
  "s_addc_u32 s93, s93, 0\n\t" \
  "v_mfma_f32_16x16x32_bf16 v[128:131], v[32:35], v[176:179], v[128:131]\n\t" \
  "v_mfma_f32_16x16x32_bf16 v[132:135], v[36:39], v[176:179], v[132:135]\n\t" \
  "ds_read_b128 v[176:179], v208 offset:32768\n\t" \
  "v_mfma_f32_16x16x32_bf16 v[136:139], v[32:35], v[180:183], v[136:139]\n\t" \
  "v_mfma_f32_16x16x32_bf16 v[140:143], v[36:39], v[180:183], v[140:143]\n\t" \
  "ds_read_b128 v[180:183], v208 offset:49152\n\t" \
  "ds_read_b128 v[160:163], v221 offset:0\n\t" \
  "ds_read_b128 v[164:167], v221 offset:1024\n\t" \
  "s_sub_i32 s91, s86, 2\n\t" \
  "s_max_i32 s88, s88, s91\n\t" \
  "v_mov_b32 v173, s88\n\t" \
  "buffer_store_dword v173, v174, %[rs], 0 offen sc1\n\t" \
  "v_mfma_f32_16x16x32_bf16 v[128:131], v[40:43], v[184:187], v[128:131]\n\t" \
  "v_mfma_f32_16x16x32_bf16 v[132:135], v[44:47], v[184:187], v[132:135]\n\t" \
  "ds_read_b128 v[184:187], v209 offset:32768\n\t" \
  "s_add_u32 m0, %[lw], 98304\n\t" \
  "v_mfma_f32_16x16x32_bf16 v[136:139], v[40:43], v[188:191], v[136:139]\n\t" \
  "v_mfma_f32_16x16x32_bf16 v[140:143], v[44:47], v[188:191], v[140:143]\n\t" \
  "ds_read_b128 v[188:191], v209 offset:49152\n\t" \
  "global_load_lds_dwordx4 v216, s[92:93] sc1\n\t" \
  "s_waitcnt lgkmcnt(8)\n\t" \
  "v_mfma_f32_16x16x32_bf16 v[128:131], v[48:51], v[192:195], v[128:131]\n\t" \
  "v_mfma_f32_16x16x32_bf16 v[132:135], v[52:55], v[192:195], v[132:135]\n\t" \
  "ds_read_b128 v[192:195], v210 offset:32768\n\t" \
  "v_mfma_f32_16x16x32_bf16 v[136:139], v[48:51], v[196:199], v[136:139]\n\t" \
  "v_mfma_f32_16x16x32_bf16 v[140:143], v[52:55], v[196:199], v[140:143]\n\t" \
  "ds_read_b128 v[196:199], v210 offset:49152\n\t" \
  "s_waitcnt lgkmcnt(4)\n\t" \
  "v_add_f32 v160, v160, v144\n\t" \
  "v_add_f32 v161, v161, v145\n\t" \
  "v_add_f32 v162, v162, v146\n\t" \
  "v_add_f32 v163, v163, v147\n\t" \
  "v_add_f32 v164, v164, v148\n\t" \
  "v_add_f32 v165, v165, v149\n\t" \
  "v_add_f32 v166, v166, v150\n\t" \
  "v_add_f32 v167, v167, v151\n\t" \
  "v_mfma_f32_16x16x32_bf16 v[128:131], v[56:59], v[200:203], v[128:131]\n\t" \
  "v_mfma_f32_16x16x32_bf16 v[132:135], v[60:63], v[200:203], v[132:135]\n\t" \
  "ds_read_b128 v[200:203], v211 offset:32768\n\t" \
  "s_add_u32 m0, %[lw], 99328\n\t" \
  "v_mfma_f32_16x16x32_bf16 v[136:139], v[56:59], v[204:207], v[136:139]\n\t" \
  "v_mfma_f32_16x16x32_bf16 v[140:143], v[60:63], v[204:207], v[140:143]\n\t" \
  "ds_read_b128 v[204:207], v211 offset:49152\n\t" \
  "global_load_lds_dwordx4 v217, s[92:93] sc1\n\t" \
  "v_cvt_pk_bf16_f32 v168, v160, v161\n\t" \
  "v_cvt_pk_bf16_f32 v169, v162, v163\n\t" \
  "v_cvt_pk_bf16_f32 v170, v164, v165\n\t" \
  "v_cvt_pk_bf16_f32 v171, v166, v167\n\t" \
  "buffer_store_dwordx4 v[168:171], v172, %[rt], s90 offen sc1\n\t" \
  "v_mfma_f32_16x16x32_bf16 v[128:131], v[64:67], v[176:179], v[128:131]\n\t" \
  "v_mfma_f32_16x16x32_bf16 v[132:135], v[68:71], v[176:179], v[132:135]\n\t" \
  "ds_read_b128 v[176:179], v208 offset:33024\n\t" \
  "v_mfma_f32_16x16x32_bf16 v[136:139], v[64:67], v[180:183], v[136:139]\n\t" \
  "v_mfma_f32_16x16x32_bf16 v[140:143], v[68:71], v[180:183], v[140:143]\n\t" \
  "ds_read_b128 v[180:183], v208 offset:49408\n\t" \
  "s_waitcnt lgkmcnt(6)\n\t" \
  "v_mfma_f32_16x16x32_bf16 v[128:131], v[72:75], v[184:187], v[128:131]\n\t" \
  "v_mfma_f32_16x16x32_bf16 v[132:135], v[76:79], v[184:187], v[132:135]\n\t" \
  "ds_read_b128 v[184:187], v209 offset:33024\n\t" \
  "s_add_u32 m0, %[lw], 100352\n\t" \
  "v_mfma_f32_16x16x32_bf16 v[136:139], v[72:75], v[188:191], v[136:139]\n\t" \
  "v_mfma_f32_16x16x32_bf16 v[140:143], v[76:79], v[188:191], v[140:143]\n\t" \
  "ds_read_b128 v[188:191], v209 offset:49408\n\t" \
  "global_load_lds_dwordx4 v218, s[92:93] sc1\n\t" \
  "s_waitcnt lgkmcnt(6)\n\t" \
  "v_mfma_f32_16x16x32_bf16 v[128:131], v[80:83], v[192:195], v[128:131]\n\t" \
  "v_mfma_f32_16x16x32_bf16 v[132:135], v[84:87], v[192:195], v[132:135]\n\t" \
  "ds_read_b128 v[192:195], v210 offset:33024\n\t" \
  "v_mfma_f32_16x16x32_bf16 v[136:139], v[80:83], v[196:199], v[136:139]\n\t" \
  "v_mfma_f32_16x16x32_bf16 v[140:143], v[84:87], v[196:199], v[140:143]\n\t" \
  "ds_read_b128 v[196:199], v210 offset:49408\n\t" \
  "s_waitcnt lgkmcnt(6)\n\t" \
  "v_mfma_f32_16x16x32_bf16 v[128:131], v[88:91], v[200:203], v[128:131]\n\t" \
  "v_mfma_f32_16x16x32_bf16 v[132:135], v[92:95], v[200:203], v[132:135]\n\t" \
  "ds_read_b128 v[200:203], v211 offset:33024\n\t" \
  "s_add_u32 m0, %[lw], 101376\n\t" \
  "v_mfma_f32_16x16x32_bf16 v[136:139], v[88:91], v[204:207], v[136:139]\n\t" \
  "v_mfma_f32_16x16x32_bf16 v[140:143], v[92:95], v[204:207], v[140:143]\n\t" \
  "ds_read_b128 v[204:207], v211 offset:49408\n\t" \
  "global_load_lds_dwordx4 v219, s[92:93] sc1\n\t" \
  "s_cmp_lt_u32 s86, 8\n\t" \
  "s_cbranch_scc1 .Lw11%=\n\t" \
  "s_waitcnt vmcnt(6)\n\t" \
  "s_barrier\n\t" \
  ".Lg11%=:\n\t" \
  "s_add_u32 s92, s92, 65024\n\t" \
  "s_addc_u32 s93, s93, 0\n\t" \
  "s_waitcnt lgkmcnt(6)\n\t" \
  "v_mfma_f32_16x16x32_bf16 v[128:131], v[96:99], v[176:179], v[128:131]\n\t" \
  "v_mfma_f32_16x16x32_bf16 v[132:135], v[100:103], v[176:179], v[132:135]\n\t" \
  "ds_read_b128 v[176:179], v212 offset:0\n\t" \
  "v_mfma_f32_16x16x32_bf16 v[136:139], v[96:99], v[180:183], v[136:139]\n\t" \
  "v_mfma_f32_16x16x32_bf16 v[140:143], v[100:103], v[180:183], v[140:143]\n\t" \
  "ds_read_b128 v[180:183], v212 offset:16384\n\t" \
  "s_waitcnt lgkmcnt(6)\n\t" \
  "v_mfma_f32_16x16x32_bf16 v[128:131], v[104:107], v[184:187], v[128:131]\n\t" \
  "v_mfma_f32_16x16x32_bf16 v[132:135], v[108:111], v[184:187], v[132:135]\n\t" \
  "ds_read_b128 v[184:187], v213 offset:0\n\t" \
  "s_add_u32 m0, %[lw], 0\n\t" \
  "v_mfma_f32_16x16x32_bf16 v[136:139], v[104:107], v[188:191], v[136:139]\n\t" \
  "v_mfma_f32_16x16x32_bf16 v[140:143], v[108:111], v[188:191], v[140:143]\n\t" \
  "ds_read_b128 v[188:191], v213 offset:16384\n\t" \
  "global_load_lds_dwordx4 v216, s[92:93] sc1\n\t" \
  "s_waitcnt lgkmcnt(6)\n\t" \
  "v_mfma_f32_16x16x32_bf16 v[128:131], v[112:115], v[192:195], v[128:131]\n\t" \
  "v_mfma_f32_16x16x32_bf16 v[132:135], v[116:119], v[192:195], v[132:135]\n\t" \
  "ds_read_b128 v[192:195], v214 offset:0\n\t" \
  "v_mfma_f32_16x16x32_bf16 v[136:139], v[112:115], v[196:199], v[136:139]\n\t" \
  "v_mfma_f32_16x16x32_bf16 v[140:143], v[116:119], v[196:199], v[140:143]\n\t" \
  "ds_read_b128 v[196:199], v214 offset:16384\n\t" \
  "s_waitcnt lgkmcnt(6)\n\t" \
  "v_mfma_f32_16x16x32_bf16 v[128:131], v[120:123], v[200:203], v[128:131]\n\t" \
  "v_mfma_f32_16x16x32_bf16 v[132:135], v[124:127], v[200:203], v[132:135]\n\t" \
  "ds_read_b128 v[200:203], v215 offset:0\n\t" \
  "s_add_u32 m0, %[lw], 1024\n\t" \
  "v_mfma_f32_16x16x32_bf16 v[136:139], v[120:123], v[204:207], v[136:139]\n\t" \
  "v_mfma_f32_16x16x32_bf16 v[140:143], v[124:127], v[204:207], v[140:143]\n\t" \
  "ds_read_b128 v[204:207], v215 offset:16384\n\t" \
  "global_load_lds_dwordx4 v217, s[92:93] sc1\n\t" \
  "s_add_u32 s86, s86, 1\n\t" \
  "s_add_u32 s87, s87, 1\n\t" \
  "s_cmp_le_u32 s86, 5\n\t" \
  "s_cbranch_scc1 .Lf11%=\n\t" \
  ".Lfb11%=:\n\t" \
  "s_waitcnt lgkmcnt(6)\n\t" \
  "v_mfma_f32_16x16x32_bf16 v[144:147], v[0:3], v[176:179], 0\n\t" \
  "v_mfma_f32_16x16x32_bf16 v[148:151], v[4:7], v[176:179], 0\n\t" \
  "ds_read_b128 v[176:179], v212 offset:256\n\t" \
  "v_mfma_f32_16x16x32_bf16 v[152:155], v[0:3], v[180:183], 0\n\t" \
  "v_mfma_f32_16x16x32_bf16 v[156:159], v[4:7], v[180:183], 0\n\t" \
  "ds_read_b128 v[180:183], v212 offset:16640\n\t" \
  "s_waitcnt lgkmcnt(6)\n\t" \
  "v_mfma_f32_16x16x32_bf16 v[144:147], v[8:11], v[184:187], v[144:147]\n\t" \
  "v_mfma_f32_16x16x32_bf16 v[148:151], v[12:15], v[184:187], v[148:151]\n\t" \
  "ds_read_b128 v[184:187], v213 offset:256\n\t" \
  "s_add_u32 m0, %[lw], 2048\n\t" \
  "v_mfma_f32_16x16x32_bf16 v[152:155], v[8:11], v[188:191], v[152:155]\n\t" \
  "v_mfma_f32_16x16x32_bf16 v[156:159], v[12:15], v[188:191], v[156:159]\n\t" \
  "ds_read_b128 v[188:191], v213 offset:16640\n\t" \
  "global_load_lds_dwordx4 v218, s[92:93] sc1\n\t" \
  "ds_write_b128 v220, v[136:139] offset:0\n\t" \
  "ds_write_b128 v220, v[140:143] offset:1024\n\t" \
  "s_waitcnt lgkmcnt(8)\n\t" \
  "v_mfma_f32_16x16x32_bf16 v[144:147], v[16:19], v[192:195], v[144:147]\n\t" \
  "v_mfma_f32_16x16x32_bf16 v[148:151], v[20:23], v[192:195], v[148:151]\n\t" \
  "ds_read_b128 v[192:195], v214 offset:256\n\t" \
  "v_mfma_f32_16x16x32_bf16 v[152:155], v[16:19], v[196:199], v[152:155]\n\t" \
  "v_mfma_f32_16x16x32_bf16 v[156:159], v[20:23], v[196:199], v[156:159]\n\t" \
  "ds_read_b128 v[196:199], v214 offset:16640\n\t" \
  "s_lshr_b32 s90, s87, 7\n\t" \
  "s_xor_b32 s90, s90, %[s0]\n\t" \
  "s_and_b32 s90, s90, 1\n\t" \
  "s_lshl1_add_u32 s90, s87, s90\n\t" \
  "s_lshl_b32 s90, s90, 13\n\t" \
  "s_cmp_lt_i32 s86, 6\n\t" \
  "s_cselect_b32 s90, 0x70000000, s90\n\t" \
  "s_waitcnt lgkmcnt(8)\n\t" \
  "v_mfma_f32_16x16x32_bf16 v[144:147], v[24:27], v[200:203], v[144:147]\n\t" \
  "v_mfma_f32_16x16x32_bf16 v[148:151], v[28:31], v[200:203], v[148:151]\n\t" \
  "ds_read_b128 v[200:203], v215 offset:256\n\t" \
  "s_add_u32 m0, %[lw], 3072\n\t" \
  "v_mfma_f32_16x16x32_bf16 v[152:155], v[24:27], v[204:207], v[152:155]\n\t" \
  "v_mfma_f32_16x16x32_bf16 v[156:159], v[28:31], v[204:207], v[156:159]\n\t" \
  "ds_read_b128 v[204:207], v215 offset:16640\n\t" \
  "global_load_lds_dwordx4 v219, s[92:93] sc1\n\t" \
  "s_waitcnt lgkmcnt(4)\n\t" \
  "s_cmp_lt_u32 s86, 6\n\t" \
  "s_cbranch_scc1 .Lw21%=\n\t" \
  "s_waitcnt vmcnt(4)\n\t" \
  "s_barrier\n\t" \
  ".Lg21%=:\n\t" \
  "s_add_u32 s92, s92, 512\n\t" \
  "s_addc_u32 s93, s93, 0\n\t" \
  "v_mfma_f32_16x16x32_bf16 v[144:147], v[32:35], v[176:179], v[144:147]\n\t" \
  "v_mfma_f32_16x16x32_bf16 v[148:151], v[36:39], v[176:179], v[148:151]\n\t" \
  "ds_read_b128 v[176:179], v212 offset:32768\n\t" \
  "v_mfma_f32_16x16x32_bf16 v[152:155], v[32:35], v[180:183], v[152:155]\n\t" \
  "v_mfma_f32_16x16x32_bf16 v[156:159], v[36:39], v[180:183], v[156:159]\n\t" \
  "ds_read_b128 v[180:183], v212 offset:49152\n\t" \
  "ds_read_b128 v[160:163], v221 offset:0\n\t" \
  "ds_read_b128 v[164:167], v221 offset:1024\n\t" \
  "s_sub_i32 s91, s86, 2\n\t" \
  "s_max_i32 s88, s88, s91\n\t" \
  "v_mov_b32 v173, s88\n\t" \
  "buffer_store_dword v173, v174, %[rs], 0 offen sc1\n\t" \
  "v_mfma_f32_16x16x32_bf16 v[144:147], v[40:43], v[184:187], v[144:147]\n\t" \
  "v_mfma_f32_16x16x32_bf16 v[148:151], v[44:47], v[184:187], v[148:151]\n\t" \
  "ds_read_b128 v[184:187], v213 offset:32768\n\t" \
  "s_add_u32 m0, %[lw], 32768\n\t" \
  "v_mfma_f32_16x16x32_bf16 v[152:155], v[40:43], v[188:191], v[152:155]\n\t" \
  "v_mfma_f32_16x16x32_bf16 v[156:159], v[44:47], v[188:191], v[156:159]\n\t" \
  "ds_read_b128 v[188:191], v213 offset:49152\n\t" \
  "global_load_lds_dwordx4 v216, s[92:93] sc1\n\t" \
  "s_waitcnt lgkmcnt(8)\n\t" \
  "v_mfma_f32_16x16x32_bf16 v[144:147], v[48:51], v[192:195], v[144:147]\n\t" \
  "v_mfma_f32_16x16x32_bf16 v[148:151], v[52:55], v[192:195], v[148:151]\n\t" \
  "ds_read_b128 v[192:195], v214 offset:32768\n\t" \
  "v_mfma_f32_16x16x32_bf16 v[152:155], v[48:51], v[196:199], v[152:155]\n\t" \
  "v_mfma_f32_16x16x32_bf16 v[156:159], v[52:55], v[196:199], v[156:159]\n\t" \
  "ds_read_b128 v[196:199], v214 offset:49152\n\t" \
  "s_waitcnt lgkmcnt(4)\n\t" \
  "v_add_f32 v160, v160, v128\n\t" \
  "v_add_f32 v161, v161, v129\n\t" \
  "v_add_f32 v162, v162, v130\n\t" \
  "v_add_f32 v163, v163, v131\n\t" \
  "v_add_f32 v164, v164, v132\n\t" \
  "v_add_f32 v165, v165, v133\n\t" \
  "v_add_f32 v166, v166, v134\n\t" \
  "v_add_f32 v167, v167, v135\n\t" \
  "v_mfma_f32_16x16x32_bf16 v[144:147], v[56:59], v[200:203], v[144:147]\n\t" \
  "v_mfma_f32_16x16x32_bf16 v[148:151], v[60:63], v[200:203], v[148:151]\n\t" \
  "ds_read_b128 v[200:203], v215 offset:32768\n\t" \
  "s_add_u32 m0, %[lw], 33792\n\t" \
  "v_mfma_f32_16x16x32_bf16 v[152:155], v[56:59], v[204:207], v[152:155]\n\t" \
  "v_mfma_f32_16x16x32_bf16 v[156:159], v[60:63], v[204:207], v[156:159]\n\t" \
  "ds_read_b128 v[204:207], v215 offset:49152\n\t" \
  "global_load_lds_dwordx4 v217, s[92:93] sc1\n\t" \
  "v_cvt_pk_bf16_f32 v168, v160, v161\n\t" \
  "v_cvt_pk_bf16_f32 v169, v162, v163\n\t" \
  "v_cvt_pk_bf16_f32 v170, v164, v165\n\t" \
  "v_cvt_pk_bf16_f32 v171, v166, v167\n\t" \
  "buffer_store_dwordx4 v[168:171], v172, %[rt], s90 offen sc1\n\t" \
  "v_mfma_f32_16x16x32_bf16 v[144:147], v[64:67], v[176:179], v[144:147]\n\t" \
  "v_mfma_f32_16x16x32_bf16 v[148:151], v[68:71], v[176:179], v[148:151]\n\t" \
  "ds_read_b128 v[176:179], v212 offset:33024\n\t" \
  "v_mfma_f32_16x16x32_bf16 v[152:155], v[64:67], v[180:183], v[152:155]\n\t" \
  "v_mfma_f32_16x16x32_bf16 v[156:159], v[68:71], v[180:183], v[156:159]\n\t" \
  "ds_read_b128 v[180:183], v212 offset:49408\n\t" \
  "s_waitcnt lgkmcnt(6)\n\t" \
  "v_mfma_f32_16x16x32_bf16 v[144:147], v[72:75], v[184:187], v[144:147]\n\t" \
  "v_mfma_f32_16x16x32_bf16 v[148:151], v[76:79], v[184:187], v[148:151]\n\t" \
  "ds_read_b128 v[184:187], v213 offset:33024\n\t" \
  "s_add_u32 m0, %[lw], 34816\n\t" \
  "v_mfma_f32_16x16x32_bf16 v[152:155], v[72:75], v[188:191], v[152:155]\n\t" \
  "v_mfma_f32_16x16x32_bf16 v[156:159], v[76:79], v[188:191], v[156:159]\n\t" \
  "ds_read_b128 v[188:191], v213 offset:49408\n\t" \
  "global_load_lds_dwordx4 v218, s[92:93] sc1\n\t" \
  "s_waitcnt lgkmcnt(6)\n\t" \
  "v_mfma_f32_16x16x32_bf16 v[144:147], v[80:83], v[192:195], v[144:147]\n\t" \
  "v_mfma_f32_16x16x32_bf16 v[148:151], v[84:87], v[192:195], v[148:151]\n\t" \
  "ds_read_b128 v[192:195], v214 offset:33024\n\t" \
  "v_mfma_f32_16x16x32_bf16 v[152:155], v[80:83], v[196:199], v[152:155]\n\t" \
  "v_mfma_f32_16x16x32_bf16 v[156:159], v[84:87], v[196:199], v[156:159]\n\t" \
  "ds_read_b128 v[196:199], v214 offset:49408\n\t" \
  "s_waitcnt lgkmcnt(6)\n\t" \
  "v_mfma_f32_16x16x32_bf16 v[144:147], v[88:91], v[200:203], v[144:147]\n\t" \
  "v_mfma_f32_16x16x32_bf16 v[148:151], v[92:95], v[200:203], v[148:151]\n\t" \
  "ds_read_b128 v[200:203], v215 offset:33024\n\t" \
  "s_add_u32 m0, %[lw], 35840\n\t" \
  "v_mfma_f32_16x16x32_bf16 v[152:155], v[88:91], v[204:207], v[152:155]\n\t" \
  "v_mfma_f32_16x16x32_bf16 v[156:159], v[92:95], v[204:207], v[156:159]\n\t" \
  "ds_read_b128 v[204:207], v215 offset:49408\n\t" \
  "global_load_lds_dwordx4 v219, s[92:93] sc1\n\t" \
  "s_cmp_lt_u32 s86, 8\n\t" \
  "s_cbranch_scc1 .Lw31%=\n\t" \
  "s_waitcnt vmcnt(6)\n\t" \
  "s_barrier\n\t" \
  ".Lg31%=:\n\t" \
  "s_add_u32 s92, s92, 65024\n\t" \
  "s_addc_u32 s93, s93, 0\n\t" \
  "s_waitcnt lgkmcnt(6)\n\t" \
  "v_mfma_f32_16x16x32_bf16 v[144:147], v[96:99], v[176:179], v[144:147]\n\t" \
  "v_mfma_f32_16x16x32_bf16 v[148:151], v[100:103], v[176:179], v[148:151]\n\t" \
  "ds_read_b128 v[176:179], v208 offset:0\n\t" \
  "v_mfma_f32_16x16x32_bf16 v[152:155], v[96:99], v[180:183], v[152:155]\n\t" \
  "v_mfma_f32_16x16x32_bf16 v[156:159], v[100:103], v[180:183], v[156:159]\n\t" \
  "ds_read_b128 v[180:183], v208 offset:16384\n\t" \
  "s_waitcnt lgkmcnt(6)\n\t" \
  "v_mfma_f32_16x16x32_bf16 v[144:147], v[104:107], v[184:187], v[144:147]\n\t" \
  "v_mfma_f32_16x16x32_bf16 v[148:151], v[108:111], v[184:187], v[148:151]\n\t" \
  "ds_read_b128 v[184:187], v209 offset:0\n\t" \
  "s_add_u32 m0, %[lw], 65536\n\t" \
  "v_mfma_f32_16x16x32_bf16 v[152:155], v[104:107], v[188:191], v[152:155]\n\t" \
  "v_mfma_f32_16x16x32_bf16 v[156:159], v[108:111], v[188:191], v[156:159]\n\t" \
  "ds_read_b128 v[188:191], v209 offset:16384\n\t" \
  "global_load_lds_dwordx4 v216, s[92:93] sc1\n\t" \
  "s_waitcnt lgkmcnt(6)\n\t" \
  "v_mfma_f32_16x16x32_bf16 v[144:147], v[112:115], v[192:195], v[144:147]\n\t" \
  "v_mfma_f32_16x16x32_bf16 v[148:151], v[116:119], v[192:195], v[148:151]\n\t" \
  "ds_read_b128 v[192:195], v210 offset:0\n\t" \
  "v_mfma_f32_16x16x32_bf16 v[152:155], v[112:115], v[196:199], v[152:155]\n\t" \
  "v_mfma_f32_16x16x32_bf16 v[156:159], v[116:119], v[196:199], v[156:159]\n\t" \
  "ds_read_b128 v[196:199], v210 offset:16384\n\t" \
  "s_waitcnt lgkmcnt(6)\n\t" \
  "v_mfma_f32_16x16x32_bf16 v[144:147], v[120:123], v[200:203], v[144:147]\n\t" \
  "v_mfma_f32_16x16x32_bf16 v[148:151], v[124:127], v[200:203], v[148:151]\n\t" \
  "ds_read_b128 v[200:203], v211 offset:0\n\t" \
  "s_add_u32 m0, %[lw], 66560\n\t" \
  "v_mfma_f32_16x16x32_bf16 v[152:155], v[120:123], v[204:207], v[152:155]\n\t" \
  "v_mfma_f32_16x16x32_bf16 v[156:159], v[124:127], v[204:207], v[156:159]\n\t" \
  "ds_read_b128 v[204:207], v211 offset:16384\n\t" \
  "global_load_lds_dwordx4 v217, s[92:93] sc1\n\t" \
  "s_add_u32 s86, s86, 1\n\t" \
  "s_add_u32 s87, s87, 1\n\t" \
  "s_cmp_lt_u32 s86, %[end]\n\t" \
  "s_cbranch_scc1 .Lp1%=\n\t" \
  "s_branch .Lx1%=\n\t" \
  ".Lf01%=:\n\t" \
  "s_nop 7\n\t" \
  "ds_write_b128 v220, v[152:155] offset:0\n\t" \
  "ds_write_b128 v220, v[156:159] offset:1024\n\t" \
  "s_lshr_b32 s90, s87, 7\n\t" \
  "s_xor_b32 s90, s90, %[s0]\n\t" \
  "s_and_b32 s90, s90, 1\n\t" \
  "s_lshl1_add_u32 s90, s87, s90\n\t" \
  "s_lshl_b32 s90, s90, 13\n\t" \
  "s_cmp_lt_i32 s86, 1\n\t" \
  "s_cselect_b32 s90, 0x70000000, s90\n\t" \
  "s_waitcnt lgkmcnt(0)\n\t" \
  "s_barrier\n\t" \
  "ds_read_b128 v[160:163], v221 offset:0\n\t" \
  "ds_read_b128 v[164:167], v221 offset:1024\n\t" \
  "s_waitcnt lgkmcnt(0)\n\t" \
  "v_add_f32 v160, v160, v144\n\t" \
  "v_add_f32 v161, v161, v145\n\t" \
  "v_add_f32 v162, v162, v146\n\t" \
  "v_add_f32 v163, v163, v147\n\t" \
  "v_add_f32 v164, v164, v148\n\t" \
  "v_add_f32 v165, v165, v149\n\t" \
  "v_add_f32 v166, v166, v150\n\t" \
  "v_add_f32 v167, v167, v151\n\t" \
  "v_cvt_pk_bf16_f32 v168, v160, v161\n\t" \
  "v_cvt_pk_bf16_f32 v169, v162, v163\n\t" \
  "v_cvt_pk_bf16_f32 v170, v164, v165\n\t" \
  "v_cvt_pk_bf16_f32 v171, v166, v167\n\t" \
  "buffer_store_dwordx4 v[168:171], v172, %[rt], s90 offen sc1\n\t" \
  "s_branch .Lfb01%=\n\t" \
  ".Lf11%=:\n\t" \
  "s_nop 7\n\t" \
  "ds_write_b128 v220, v[136:139] offset:0\n\t" \
  "ds_write_b128 v220, v[140:143] offset:1024\n\t" \
  "s_lshr_b32 s90, s87, 7\n\t" \
  "s_xor_b32 s90, s90, %[s0]\n\t" \
  "s_and_b32 s90, s90, 1\n\t" \
  "s_lshl1_add_u32 s90, s87, s90\n\t" \
  "s_lshl_b32 s90, s90, 13\n\t" \
  "s_cmp_lt_i32 s86, 1\n\t" \
  "s_cselect_b32 s90, 0x70000000, s90\n\t" \
  "s_waitcnt lgkmcnt(0)\n\t" \
  "s_barrier\n\t" \
  "ds_read_b128 v[160:163], v221 offset:0\n\t" \
  "ds_read_b128 v[164:167], v221 offset:1024\n\t" \
  "s_waitcnt lgkmcnt(0)\n\t" \
  "v_add_f32 v160, v160, v128\n\t" \
  "v_add_f32 v161, v161, v129\n\t" \
  "v_add_f32 v162, v162, v130\n\t" \
  "v_add_f32 v163, v163, v131\n\t" \
  "v_add_f32 v164, v164, v132\n\t" \
  "v_add_f32 v165, v165, v133\n\t" \
  "v_add_f32 v166, v166, v134\n\t" \
  "v_add_f32 v167, v167, v135\n\t" \
  "v_cvt_pk_bf16_f32 v168, v160, v161\n\t" \
  "v_cvt_pk_bf16_f32 v169, v162, v163\n\t" \
  "v_cvt_pk_bf16_f32 v170, v164, v165\n\t" \
  "v_cvt_pk_bf16_f32 v171, v166, v167\n\t" \
  "buffer_store_dwordx4 v[168:171], v172, %[rt], s90 offen sc1\n\t" \
  "s_branch .Lfb11%=\n\t" \
  ".Lw01%=:\n\t" \
  "s_waitcnt vmcnt(2)\n\t" \
  "s_barrier\n\t" \
  "s_sub_i32 s91, s86, 1\n\t" \
  "s_max_i32 s88, s88, s91\n\t" \
  "v_mov_b32 v173, s88\n\t" \
  "buffer_store_dword v173, v174, %[rs], 0 offen sc1\n\t" \
  "s_branch .Lg01%=\n\t" \
  ".Lw11%=:\n\t" \
  "s_waitcnt vmcnt(2)\n\t" \
  "s_barrier\n\t" \
  "s_sub_i32 s91, s86, 1\n\t" \
  "s_max_i32 s88, s88, s91\n\t" \
  "v_mov_b32 v173, s88\n\t" \
  "buffer_store_dword v173, v174, %[rs], 0 offen sc1\n\t" \
  "s_branch .Lg11%=\n\t" \
  ".Lw21%=:\n\t" \
  "s_waitcnt vmcnt(2)\n\t" \
  "s_barrier\n\t" \
  "s_sub_i32 s91, s86, 1\n\t" \
  "s_max_i32 s88, s88, s91\n\t" \
  "v_mov_b32 v173, s88\n\t" \
  "buffer_store_dword v173, v174, %[rs], 0 offen sc1\n\t" \
  "s_branch .Lg21%=\n\t" \
  ".Lw31%=:\n\t" \
  "s_waitcnt vmcnt(2)\n\t" \
  "s_barrier\n\t" \
  "s_sub_i32 s91, s86, 1\n\t" \
  "s_max_i32 s88, s88, s91\n\t" \
  "v_mov_b32 v173, s88\n\t" \
  "buffer_store_dword v173, v174, %[rs], 0 offen sc1\n\t" \
  "s_branch .Lg31%=\n\t" \
  ".Lx1%=:\n\t" \
  "s_waitcnt lgkmcnt(0)\n\t" \
  "s_setprio 0\n\t" \
  "s_mov_b32 m0, s94\n\t" \
  ".Le%=:\n\t"
#define PR_ASM_1 \
  "s_cmp_lg_u32 %[kh], 0\n\t" \
  "s_cbranch_scc1 .Lk%=\n\t" \
  "s_mov_b32 s94, m0\n\t" \
  "s_mov_b64 s[92:93], %[x0]\n\t" \
  "s_mov_b32 s86, %[g0]\n\t" \
  "s_add_u32 s87, s86, %[t0]\n\t" \
  "s_sub_i32 s88, s86, 3\n\t" \
  "ds_read_b128 v[176:179], v208 offset:0\n\t" \
  "ds_read_b128 v[180:183], v208 offset:16384\n\t" \
  "ds_read_b128 v[184:187], v209 offset:0\n\t" \
  "ds_read_b128 v[188:191], v209 offset:16384\n\t" \
  "ds_read_b128 v[192:195], v210 offset:0\n\t" \
  "ds_read_b128 v[196:199], v210 offset:16384\n\t" \
  "ds_read_b128 v[200:203], v211 offset:0\n\t" \
  "ds_read_b128 v[204:207], v211 offset:16384\n\t" \
  "s_branch .Lfb00%=\n\t" \
  ".Lp0%=:\n\t" \
  "s_cmp_le_u32 s86, 5\n\t" \
  "s_cbranch_scc1 .Lf00%=\n\t" \
  ".Lfb00%=:\n\t" \
  "s_waitcnt lgkmcnt(6)\n\t" \
  "v_mfma_f32_16x16x32_bf16 v[128:131], v[176:179], v[0:3], 0\n\t" \
  "v_mfma_f32_16x16x32_bf16 v[136:139], v[176:179], v[4:7], 0\n\t" \
  "ds_read_b128 v[176:179], v208 offset:256\n\t" \
  "s_add_u32 m0, %[lw], 67584\n\t" \
  "v_mfma_f32_16x16x32_bf16 v[132:135], v[180:183], v[0:3], 0\n\t" \
  "v_mfma_f32_16x16x32_bf16 v[140:143], v[180:183], v[4:7], 0\n\t" \
  "ds_read_b128 v[180:183], v208 offset:16640\n\t" \
  "global_load_lds_dwordx4 v218, s[92:93] sc1\n\t" \
  "s_waitcnt lgkmcnt(6)\n\t" \
  "v_mfma_f32_16x16x32_bf16 v[128:131], v[184:187], v[8:11], v[128:131]\n\t" \
  "v_mfma_f32_16x16x32_bf16 v[136:139], v[184:187], v[12:15], v[136:139]\n\t" \
  "ds_read_b128 v[184:187], v209 offset:256\n\t" \
  "v_mfma_f32_16x16x32_bf16 v[132:135], v[188:191], v[8:11], v[132:135]\n\t" \
  "v_mfma_f32_16x16x32_bf16 v[140:143], v[188:191], v[12:15], v[140:143]\n\t" \
  "ds_read_b128 v[188:191], v209 offset:16640\n\t" \
  "ds_write_b128 v220, v[152:155] offset:0\n\t" \
  "ds_write_b128 v220, v[156:159] offset:1024\n\t" \
  "s_waitcnt lgkmcnt(8)\n\t" \
  "v_mfma_f32_16x16x32_bf16 v[128:131], v[192:195], v[16:19], v[128:131]\n\t" \
  "v_mfma_f32_16x16x32_bf16 v[136:139], v[192:195], v[20:23], v[136:139]\n\t" \
  "ds_read_b128 v[192:195], v210 offset:256\n\t" \
  "s_add_u32 m0, %[lw], 68608\n\t" \
  "v_mfma_f32_16x16x32_bf16 v[132:135], v[196:199], v[16:19], v[132:135]\n\t" \
  "v_mfma_f32_16x16x32_bf16 v[140:143], v[196:199], v[20:23], v[140:143]\n\t" \
  "ds_read_b128 v[196:199], v210 offset:16640\n\t" \
  "global_load_lds_dwordx4 v219, s[92:93] sc1\n\t" \
  "s_lshr_b32 s90, s87, 7\n\t" \
  "s_xor_b32 s90, s90, %[s0]\n\t" \
  "s_and_b32 s90, s90, 1\n\t" \
  "s_lshl1_add_u32 s90, s87, s90\n\t" \
  "s_lshl_b32 s90, s90, 13\n\t" \
  "s_cmp_lt_i32 s86, 6\n\t" \
  "s_cselect_b32 s90, 0x70000000, s90\n\t" \
  "s_waitcnt lgkmcnt(8)\n\t" \
  "v_mfma_f32_16x16x32_bf16 v[128:131], v[200:203], v[24:27], v[128:131]\n\t" \
  "v_mfma_f32_16x16x32_bf16 v[136:139], v[200:203], v[28:31], v[136:139]\n\t" \
  "ds_read_b128 v[200:203], v211 offset:256\n\t" \
  "v_mfma_f32_16x16x32_bf16 v[132:135], v[204:207], v[24:27], v[132:135]\n\t" \
  "v_mfma_f32_16x16x32_bf16 v[140:143], v[204:207], v[28:31], v[140:143]\n\t" \
  "ds_read_b128 v[204:207], v211 offset:16640\n\t" \
  "s_waitcnt lgkmcnt(4)\n\t" \
  "s_cmp_lt_u32 s86, 6\n\t" \
  "s_cbranch_scc1 .Lw00%=\n\t" \
  "s_waitcnt vmcnt(4)\n\t" \
  "s_barrier\n\t" \
  ".Lg00%=:\n\t" \
  "s_add_u32 s92, s92, 512\n\t" \
  "s_addc_u32 s93, s93, 0\n\t" \
  "v_mfma_f32_16x16x32_bf16 v[128:131], v[176:179], v[32:35], v[128:131]\n\t" \
  "v_mfma_f32_16x16x32_bf16 v[136:139], v[176:179], v[36:39], v[136:139]\n\t" \
  "ds_read_b128 v[176:179], v208 offset:32768\n\t" \
  "s_add_u32 m0, %[lw], 98304\n\t" \
  "v_mfma_f32_16x16x32_bf16 v[132:135], v[180:183], v[32:35], v[132:135]\n\t" \
  "v_mfma_f32_16x16x32_bf16 v[140:143], v[180:183], v[36:39], v[140:143]\n\t" \
  "ds_read_b128 v[180:183], v208 offset:49152\n\t" \
  "global_load_lds_dwordx4 v216, s[92:93] sc1\n\t" \
  "ds_read_b128 v[160:163], v221 offset:0\n\t" \
  "ds_read_b128 v[164:167], v221 offset:1024\n\t" \
  "v_mfma_f32_16x16x32_bf16 v[128:131], v[184:187], v[40:43], v[128:131]\n\t" \
  "v_mfma_f32_16x16x32_bf16 v[136:139], v[184:187], v[44:47], v[136:139]\n\t" \
  "ds_read_b128 v[184:187], v209 offset:32768\n\t" \
  "v_mfma_f32_16x16x32_bf16 v[132:135], v[188:191], v[40:43], v[132:135]\n\t" \
  "v_mfma_f32_16x16x32_bf16 v[140:143], v[188:191], v[44:47], v[140:143]\n\t" \
  "ds_read_b128 v[188:191], v209 offset:49152\n\t" \
  "s_waitcnt lgkmcnt(8)\n\t" \
  "v_mfma_f32_16x16x32_bf16 v[128:131], v[192:195], v[48:51], v[128:131]\n\t" \
  "v_mfma_f32_16x16x32_bf16 v[136:139], v[192:195], v[52:55], v[136:139]\n\t" \
  "ds_read_b128 v[192:195], v210 offset:32768\n\t" \
  "s_add_u32 m0, %[lw], 99328\n\t" \
  "v_mfma_f32_16x16x32_bf16 v[132:135], v[196:199], v[48:51], v[132:135]\n\t" \
  "v_mfma_f32_16x16x32_bf16 v[140:143], v[196:199], v[52:55], v[140:143]\n\t" \
  "ds_read_b128 v[196:199], v210 offset:49152\n\t" \
  "global_load_lds_dwordx4 v217, s[92:93] sc1\n\t" \
  "s_waitcnt lgkmcnt(4)\n\t" \
  "v_add_f32 v160, v160, v144\n\t" \
  "v_add_f32 v161, v161, v145\n\t" \
  "v_add_f32 v162, v162, v146\n\t" \
  "v_add_f32 v163, v163, v147\n\t" \
  "v_add_f32 v164, v164, v148\n\t" \
  "v_add_f32 v165, v165, v149\n\t" \
  "v_add_f32 v166, v166, v150\n\t" \
  "v_add_f32 v167, v167, v151\n\t" \
  "v_mfma_f32_16x16x32_bf16 v[128:131], v[200:203], v[56:59], v[128:131]\n\t" \
  "v_mfma_f32_16x16x32_bf16 v[136:139], v[200:203], v[60:63], v[136:139]\n\t" \
  "ds_read_b128 v[200:203], v211 offset:32768\n\t" \
  "v_mfma_f32_16x16x32_bf16 v[132:135], v[204:207], v[56:59], v[132:135]\n\t" \
  "v_mfma_f32_16x16x32_bf16 v[140:143], v[204:207], v[60:63], v[140:143]\n\t" \
  "ds_read_b128 v[204:207], v211 offset:49152\n\t" \
  "v_cvt_pk_bf16_f32 v168, v160, v161\n\t" \
  "v_cvt_pk_bf16_f32 v169, v162, v163\n\t" \
  "v_cvt_pk_bf16_f32 v170, v164, v165\n\t" \
  "v_cvt_pk_bf16_f32 v171, v166, v167\n\t" \
  "buffer_store_dwordx4 v[168:171], v172, %[rt], s90 offen sc1\n\t" \
  "v_mfma_f32_16x16x32_bf16 v[128:131], v[176:179], v[64:67], v[128:131]\n\t" \
  "v_mfma_f32_16x16x32_bf16 v[136:139], v[176:179], v[68:71], v[136:139]\n\t" \
  "ds_read_b128 v[176:179], v208 offset:33024\n\t" \
  "s_add_u32 m0, %[lw], 100352\n\t" \
  "v_mfma_f32_16x16x32_bf16 v[132:135], v[180:183], v[64:67], v[132:135]\n\t" \
  "v_mfma_f32_16x16x32_bf16 v[140:143], v[180:183], v[68:71], v[140:143]\n\t" \
  "ds_read_b128 v[180:183], v208 offset:49408\n\t" \
  "global_load_lds_dwordx4 v218, s[92:93] sc1\n\t" \
  "s_waitcnt lgkmcnt(6)\n\t" \
  "v_mfma_f32_16x16x32_bf16 v[128:131], v[184:187], v[72:75], v[128:131]\n\t" \
  "v_mfma_f32_16x16x32_bf16 v[136:139], v[184:187], v[76:79], v[136:139]\n\t" \
  "ds_read_b128 v[184:187], v209 offset:33024\n\t" \
  "v_mfma_f32_16x16x32_bf16 v[132:135], v[188:191], v[72:75], v[132:135]\n\t" \
  "v_mfma_f32_16x16x32_bf16 v[140:143], v[188:191], v[76:79], v[140:143]\n\t" \
  "ds_read_b128 v[188:191], v209 offset:49408\n\t" \
  "s_waitcnt lgkmcnt(6)\n\t" \
  "v_mfma_f32_16x16x32_bf16 v[128:131], v[192:195], v[80:83], v[128:131]\n\t" \
  "v_mfma_f32_16x16x32_bf16 v[136:139], v[192:195], v[84:87], v[136:139]\n\t" \
  "ds_read_b128 v[192:195], v210 offset:33024\n\t" \
  "s_add_u32 m0, %[lw], 101376\n\t" \
  "v_mfma_f32_16x16x32_bf16 v[132:135], v[196:199], v[80:83], v[132:135]\n\t" \
  "v_mfma_f32_16x16x32_bf16 v[140:143], v[196:199], v[84:87], v[140:143]\n\t" \
  "ds_read_b128 v[196:199], v210 offset:49408\n\t" \
  "global_load_lds_dwordx4 v219, s[92:93] sc1\n\t" \
  "s_waitcnt lgkmcnt(6)\n\t" \
  "v_mfma_f32_16x16x32_bf16 v[128:131], v[200:203], v[88:91], v[128:131]\n\t" \
  "v_mfma_f32_16x16x32_bf16 v[136:139], v[200:203], v[92:95], v[136:139]\n\t" \
  "ds_read_b128 v[200:203], v211 offset:33024\n\t" \
  "v_mfma_f32_16x16x32_bf16 v[132:135], v[204:207], v[88:91], v[132:135]\n\t" \
  "v_mfma_f32_16x16x32_bf16 v[140:143], v[204:207], v[92:95], v[140:143]\n\t" \
  "ds_read_b128 v[204:207], v211 offset:49408\n\t" \
  "s_cmp_lt_u32 s86, 8\n\t" \
  "s_cbranch_scc1 .Lw10%=\n\t" \
  "s_waitcnt vmcnt(5)\n\t" \
  "s_barrier\n\t" \
  ".Lg10%=:\n\t" \
  "s_add_u32 s92, s92, 65024\n\t" \
  "s_addc_u32 s93, s93, 0\n\t" \
  "s_waitcnt lgkmcnt(6)\n\t" \
  "v_mfma_f32_16x16x32_bf16 v[128:131], v[176:179], v[96:99], v[128:131]\n\t" \
  "v_mfma_f32_16x16x32_bf16 v[136:139], v[176:179], v[100:103], v[136:139]\n\t" \
  "ds_read_b128 v[176:179], v212 offset:0\n\t" \
  "s_add_u32 m0, %[lw], 0\n\t" \
  "v_mfma_f32_16x16x32_bf16 v[132:135], v[180:183], v[96:99], v[132:135]\n\t" \
  "v_mfma_f32_16x16x32_bf16 v[140:143], v[180:183], v[100:103], v[140:143]\n\t" \
  "ds_read_b128 v[180:183], v212 offset:16384\n\t" \
  "global_load_lds_dwordx4 v216, s[92:93] sc1\n\t" \
  "s_waitcnt lgkmcnt(6)\n\t" \
  "v_mfma_f32_16x16x32_bf16 v[128:131], v[184:187], v[104:107], v[128:131]\n\t" \
  "v_mfma_f32_16x16x32_bf16 v[136:139], v[184:187], v[108:111], v[136:139]\n\t" \
  "ds_read_b128 v[184:187], v213 offset:0\n\t" \
  "v_mfma_f32_16x16x32_bf16 v[132:135], v[188:191], v[104:107], v[132:135]\n\t" \
  "v_mfma_f32_16x16x32_bf16 v[140:143], v[188:191], v[108:111], v[140:143]\n\t" \
  "ds_read_b128 v[188:191], v213 offset:16384\n\t" \
  "s_waitcnt lgkmcnt(6)\n\t" \
  "v_mfma_f32_16x16x32_bf16 v[128:131], v[192:195], v[112:115], v[128:131]\n\t" \
  "v_mfma_f32_16x16x32_bf16 v[136:139], v[192:195], v[116:119], v[136:139]\n\t" \
  "ds_read_b128 v[192:195], v214 offset:0\n\t" \
  "s_add_u32 m0, %[lw], 1024\n\t" \
  "v_mfma_f32_16x16x32_bf16 v[132:135], v[196:199], v[112:115], v[132:135]\n\t" \
  "v_mfma_f32_16x16x32_bf16 v[140:143], v[196:199], v[116:119], v[140:143]\n\t" \
  "ds_read_b128 v[196:199], v214 offset:16384\n\t" \
  "global_load_lds_dwordx4 v217, s[92:93] sc1\n\t" \
  "s_waitcnt lgkmcnt(6)\n\t" \
  "v_mfma_f32_16x16x32_bf16 v[128:131], v[200:203], v[120:123], v[128:131]\n\t" \
  "v_mfma_f32_16x16x32_bf16 v[136:139], v[200:203], v[124:127], v[136:139]\n\t" \
  "ds_read_b128 v[200:203], v215 offset:0\n\t" \
  "v_mfma_f32_16x16x32_bf16 v[132:135], v[204:207], v[120:123], v[132:135]\n\t" \
  "v_mfma_f32_16x16x32_bf16 v[140:143], v[204:207], v[124:127], v[140:143]\n\t" \
  "ds_read_b128 v[204:207], v215 offset:16384\n\t" \
  "s_add_u32 s86, s86, 1\n\t" \
  "s_add_u32 s87, s87, 1\n\t" \
  "s_cmp_le_u32 s86, 5\n\t" \
  "s_cbranch_scc1 .Lf10%=\n\t" \
  ".Lfb10%=:\n\t" \
  "s_waitcnt lgkmcnt(6)\n\t" \
  "v_mfma_f32_16x16x32_bf16 v[144:147], v[176:179], v[0:3], 0\n\t" \
  "v_mfma_f32_16x16x32_bf16 v[152:155], v[176:179], v[4:7], 0\n\t" \
  "ds_read_b128 v[176:179], v212 offset:256\n\t" \
  "s_add_u32 m0, %[lw], 2048\n\t" \
  "v_mfma_f32_16x16x32_bf16 v[148:151], v[180:183], v[0:3], 0\n\t" \
  "v_mfma_f32_16x16x32_bf16 v[156:159], v[180:183], v[4:7], 0\n\t" \
  "ds_read_b128 v[180:183], v212 offset:16640\n\t" \
  "global_load_lds_dwordx4 v218, s[92:93] sc1\n\t" \
  "s_waitcnt lgkmcnt(6)\n\t" \
  "v_mfma_f32_16x16x32_bf16 v[144:147], v[184:187], v[8:11], v[144:147]\n\t" \
  "v_mfma_f32_16x16x32_bf16 v[152:155], v[184:187], v[12:15], v[152:155]\n\t" \
  "ds_read_b128 v[184:187], v213 offset:256\n\t" \
  "v_mfma_f32_16x16x32_bf16 v[148:151], v[188:191], v[8:11], v[148:151]\n\t" \
  "v_mfma_f32_16x16x32_bf16 v[156:159], v[188:191], v[12:15], v[156:159]\n\t" \
  "ds_read_b128 v[188:191], v213 offset:16640\n\t" \
  "ds_write_b128 v220, v[136:139] offset:0\n\t" \
  "ds_write_b128 v220, v[140:143] offset:1024\n\t" \
  "s_waitcnt lgkmcnt(8)\n\t" \
  "v_mfma_f32_16x16x32_bf16 v[144:147], v[192:195], v[16:19], v[144:147]\n\t" \
  "v_mfma_f32_16x16x32_bf16 v[152:155], v[192:195], v[20:23], v[152:155]\n\t" \
  "ds_read_b128 v[192:195], v214 offset:256\n\t" \
  "s_add_u32 m0, %[lw], 3072\n\t" \
  "v_mfma_f32_16x16x32_bf16 v[148:151], v[196:199], v[16:19], v[148:151]\n\t" \
  "v_mfma_f32_16x16x32_bf16 v[156:159], v[196:199], v[20:23], v[156:159]\n\t" \
  "ds_read_b128 v[196:199], v214 offset:16640\n\t" \
  "global_load_lds_dwordx4 v219, s[92:93] sc1\n\t" \
  "s_lshr_b32 s90, s87, 7\n\t" \
  "s_xor_b32 s90, s90, %[s0]\n\t" \
  "s_and_b32 s90, s90, 1\n\t" \
  "s_lshl1_add_u32 s90, s87, s90\n\t" \
  "s_lshl_b32 s90, s90, 13\n\t" \
  "s_cmp_lt_i32 s86, 6\n\t" \
  "s_cselect_b32 s90, 0x70000000, s90\n\t" \
  "s_waitcnt lgkmcnt(8)\n\t" \
  "v_mfma_f32_16x16x32_bf16 v[144:147], v[200:203], v[24:27], v[144:147]\n\t" \
  "v_mfma_f32_16x16x32_bf16 v[152:155], v[200:203], v[28:31], v[152:155]\n\t" \
  "ds_read_b128 v[200:203], v215 offset:256\n\t" \
  "v_mfma_f32_16x16x32_bf16 v[148:151], v[204:207], v[24:27], v[148:151]\n\t" \
  "v_mfma_f32_16x16x32_bf16 v[156:159], v[204:207], v[28:31], v[156:159]\n\t" \
  "ds_read_b128 v[204:207], v215 offset:16640\n\t" \
  "s_waitcnt lgkmcnt(4)\n\t" \
  "s_cmp_lt_u32 s86, 6\n\t" \
  "s_cbranch_scc1 .Lw20%=\n\t" \
  "s_waitcnt vmcnt(4)\n\t" \
  "s_barrier\n\t" \
  ".Lg20%=:\n\t" \
  "s_add_u32 s92, s92, 512\n\t" \
  "s_addc_u32 s93, s93, 0\n\t" \
  "v_mfma_f32_16x16x32_bf16 v[144:147], v[176:179], v[32:35], v[144:147]\n\t" \
  "v_mfma_f32_16x16x32_bf16 v[152:155], v[176:179], v[36:39], v[152:155]\n\t" \
  "ds_read_b128 v[176:179], v212 offset:32768\n\t" \
  "s_add_u32 m0, %[lw], 32768\n\t" \
  "v_mfma_f32_16x16x32_bf16 v[148:151], v[180:183], v[32:35], v[148:151]\n\t" \
  "v_mfma_f32_16x16x32_bf16 v[156:159], v[180:183], v[36:39], v[156:159]\n\t" \
  "ds_read_b128 v[180:183], v212 offset:49152\n\t" \
  "global_load_lds_dwordx4 v216, s[92:93] sc1\n\t" \
  "ds_read_b128 v[160:163], v221 offset:0\n\t" \
  "ds_read_b128 v[164:167], v221 offset:1024\n\t" \
  "v_mfma_f32_16x16x32_bf16 v[144:147], v[184:187], v[40:43], v[144:147]\n\t" \
  "v_mfma_f32_16x16x32_bf16 v[152:155], v[184:187], v[44:47], v[152:155]\n\t" \
  "ds_read_b128 v[184:187], v213 offset:32768\n\t" \
  "v_mfma_f32_16x16x32_bf16 v[148:151], v[188:191], v[40:43], v[148:151]\n\t" \
  "v_mfma_f32_16x16x32_bf16 v[156:159], v[188:191], v[44:47], v[156:159]\n\t" \
  "ds_read_b128 v[188:191], v213 offset:49152\n\t" \
  "s_waitcnt lgkmcnt(8)\n\t" \
  "v_mfma_f32_16x16x32_bf16 v[144:147], v[192:195], v[48:51], v[144:147]\n\t" \
  "v_mfma_f32_16x16x32_bf16 v[152:155], v[192:195], v[52:55], v[152:155]\n\t" \
  "ds_read_b128 v[192:195], v214 offset:32768\n\t" \
  "s_add_u32 m0, %[lw], 33792\n\t" \
  "v_mfma_f32_16x16x32_bf16 v[148:151], v[196:199], v[48:51], v[148:151]\n\t" \
  "v_mfma_f32_16x16x32_bf16 v[156:159], v[196:199], v[52:55], v[156:159]\n\t" \
  "ds_read_b128 v[196:199], v214 offset:49152\n\t" \
  "global_load_lds_dwordx4 v217, s[92:93] sc1\n\t" \
  "s_waitcnt lgkmcnt(4)\n\t" \
  "v_add_f32 v160, v160, v128\n\t" \
  "v_add_f32 v161, v161, v129\n\t" \
  "v_add_f32 v162, v162, v130\n\t" \
  "v_add_f32 v163, v163, v131\n\t" \
  "v_add_f32 v164, v164, v132\n\t" \
  "v_add_f32 v165, v165, v133\n\t" \
  "v_add_f32 v166, v166, v134\n\t" \
  "v_add_f32 v167, v167, v135\n\t" \
  "v_mfma_f32_16x16x32_bf16 v[144:147], v[200:203], v[56:59], v[144:147]\n\t" \
  "v_mfma_f32_16x16x32_bf16 v[152:155], v[200:203], v[60:63], v[152:155]\n\t" \
  "ds_read_b128 v[200:203], v215 offset:32768\n\t" \
  "v_mfma_f32_16x16x32_bf16 v[148:151], v[204:207], v[56:59], v[148:151]\n\t" \
  "v_mfma_f32_16x16x32_bf16 v[156:159], v[204:207], v[60:63], v[156:159]\n\t" \
  "ds_read_b128 v[204:207], v215 offset:49152\n\t" \
  "v_cvt_pk_bf16_f32 v168, v160, v161\n\t" \
  "v_cvt_pk_bf16_f32 v169, v162, v163\n\t" \
  "v_cvt_pk_bf16_f32 v170, v164, v165\n\t" \
  "v_cvt_pk_bf16_f32 v171, v166, v167\n\t" \
  "buffer_store_dwordx4 v[168:171], v172, %[rt], s90 offen sc1\n\t" \
  "v_mfma_f32_16x16x32_bf16 v[144:147], v[176:179], v[64:67], v[144:147]\n\t" \
  "v_mfma_f32_16x16x32_bf16 v[152:155], v[176:179], v[68:71], v[152:155]\n\t" \
  "ds_read_b128 v[176:179], v212 offset:33024\n\t" \
  "s_add_u32 m0, %[lw], 34816\n\t" \
  "v_mfma_f32_16x16x32_bf16 v[148:151], v[180:183], v[64:67], v[148:151]\n\t" \
  "v_mfma_f32_16x16x32_bf16 v[156:159], v[180:183], v[68:71], v[156:159]\n\t" \
  "ds_read_b128 v[180:183], v212 offset:49408\n\t" \
  "global_load_lds_dwordx4 v218, s[92:93] sc1\n\t" \
  "s_waitcnt lgkmcnt(6)\n\t" \
  "v_mfma_f32_16x16x32_bf16 v[144:147], v[184:187], v[72:75], v[144:147]\n\t" \
  "v_mfma_f32_16x16x32_bf16 v[152:155], v[184:187], v[76:79], v[152:155]\n\t" \
  "ds_read_b128 v[184:187], v213 offset:33024\n\t" \
  "v_mfma_f32_16x16x32_bf16 v[148:151], v[188:191], v[72:75], v[148:151]\n\t" \
  "v_mfma_f32_16x16x32_bf16 v[156:159], v[188:191], v[76:79], v[156:159]\n\t" \
  "ds_read_b128 v[188:191], v213 offset:49408\n\t" \
  "s_waitcnt lgkmcnt(6)\n\t" \
  "v_mfma_f32_16x16x32_bf16 v[144:147], v[192:195], v[80:83], v[144:147]\n\t" \
  "v_mfma_f32_16x16x32_bf16 v[152:155], v[192:195], v[84:87], v[152:155]\n\t" \
  "ds_read_b128 v[192:195], v214 offset:33024\n\t" \
  "s_add_u32 m0, %[lw], 35840\n\t" \
  "v_mfma_f32_16x16x32_bf16 v[148:151], v[196:199], v[80:83], v[148:151]\n\t" \
  "v_mfma_f32_16x16x32_bf16 v[156:159], v[196:199], v[84:87], v[156:159]\n\t" \
  "ds_read_b128 v[196:199], v214 offset:49408\n\t" \
  "global_load_lds_dwordx4 v219, s[92:93] sc1\n\t" \
  "s_waitcnt lgkmcnt(6)\n\t" \
  "v_mfma_f32_16x16x32_bf16 v[144:147], v[200:203], v[88:91], v[144:147]\n\t" \
  "v_mfma_f32_16x16x32_bf16 v[152:155], v[200:203], v[92:95], v[152:155]\n\t" \
  "ds_read_b128 v[200:203], v215 offset:33024\n\t" \
  "v_mfma_f32_16x16x32_bf16 v[148:151], v[204:207], v[88:91], v[148:151]\n\t" \
  "v_mfma_f32_16x16x32_bf16 v[156:159], v[204:207], v[92:95], v[156:159]\n\t" \
  "ds_read_b128 v[204:207], v215 offset:49408\n\t" \
  "s_cmp_lt_u32 s86, 8\n\t" \
  "s_cbranch_scc1 .Lw30%=\n\t" \
  "s_waitcnt vmcnt(5)\n\t" \
  "s_barrier\n\t" \
  ".Lg30%=:\n\t" \
  "s_add_u32 s92, s92, 65024\n\t" \
  "s_addc_u32 s93, s93, 0\n\t" \
  "s_waitcnt lgkmcnt(6)\n\t" \
  "v_mfma_f32_16x16x32_bf16 v[144:147], v[176:179], v[96:99], v[144:147]\n\t" \
  "v_mfma_f32_16x16x32_bf16 v[152:155], v[176:179], v[100:103], v[152:155]\n\t" \
  "ds_read_b128 v[176:179], v208 offset:0\n\t" \
  "s_add_u32 m0, %[lw], 65536\n\t" \
  "v_mfma_f32_16x16x32_bf16 v[148:151], v[180:183], v[96:99], v[148:151]\n\t" \
  "v_mfma_f32_16x16x32_bf16 v[156:159], v[180:183], v[100:103], v[156:159]\n\t" \
  "ds_read_b128 v[180:183], v208 offset:16384\n\t" \
  "global_load_lds_dwordx4 v216, s[92:93] sc1\n\t" \
  "s_waitcnt lgkmcnt(6)\n\t" \
  "v_mfma_f32_16x16x32_bf16 v[144:147], v[184:187], v[104:107], v[144:147]\n\t" \
  "v_mfma_f32_16x16x32_bf16 v[152:155], v[184:187], v[108:111], v[152:155]\n\t" \
  "ds_read_b128 v[184:187], v209 offset:0\n\t" \
  "v_mfma_f32_16x16x32_bf16 v[148:151], v[188:191], v[104:107], v[148:151]\n\t" \
  "v_mfma_f32_16x16x32_bf16 v[156:159], v[188:191], v[108:111], v[156:159]\n\t" \
  "ds_read_b128 v[188:191], v209 offset:16384\n\t" \
  "s_waitcnt lgkmcnt(6)\n\t" \
  "v_mfma_f32_16x16x32_bf16 v[144:147], v[192:195], v[112:115], v[144:147]\n\t" \
  "v_mfma_f32_16x16x32_bf16 v[152:155], v[192:195], v[116:119], v[152:155]\n\t" \
  "ds_read_b128 v[192:195], v210 offset:0\n\t" \
  "s_add_u32 m0, %[lw], 66560\n\t" \
  "v_mfma_f32_16x16x32_bf16 v[148:151], v[196:199], v[112:115], v[148:151]\n\t" \
  "v_mfma_f32_16x16x32_bf16 v[156:159], v[196:199], v[116:119], v[156:159]\n\t" \
  "ds_read_b128 v[196:199], v210 offset:16384\n\t" \
  "global_load_lds_dwordx4 v217, s[92:93] sc1\n\t" \
  "s_waitcnt lgkmcnt(6)\n\t" \
  "v_mfma_f32_16x16x32_bf16 v[144:147], v[200:203], v[120:123], v[144:147]\n\t" \
  "v_mfma_f32_16x16x32_bf16 v[152:155], v[200:203], v[124:127], v[152:155]\n\t" \
  "ds_read_b128 v[200:203], v211 offset:0\n\t" \
  "v_mfma_f32_16x16x32_bf16 v[148:151], v[204:207], v[120:123], v[148:151]\n\t" \
  "v_mfma_f32_16x16x32_bf16 v[156:159], v[204:207], v[124:127], v[156:159]\n\t" \
  "ds_read_b128 v[204:207], v211 offset:16384\n\t" \
  "s_add_u32 s86, s86, 1\n\t" \
  "s_add_u32 s87, s87, 1\n\t" \
  "s_cmp_lt_u32 s86, %[end]\n\t" \
  "s_cbranch_scc1 .Lp0%=\n\t" \
  "s_branch .Lx0%=\n\t" \
  ".Lf00%=:\n\t" \
  "s_nop 7\n\t" \
  "ds_write_b128 v220, v[152:155] offset:0\n\t" \
  "ds_write_b128 v220, v[156:159] offset:1024\n\t" \
  "s_lshr_b32 s90, s87, 7\n\t" \
  "s_xor_b32 s90, s90, %[s0]\n\t" \
  "s_and_b32 s90, s90, 1\n\t" \
  "s_lshl1_add_u32 s90, s87, s90\n\t" \
  "s_lshl_b32 s90, s90, 13\n\t" \
  "s_cmp_lt_i32 s86, 1\n\t" \
  "s_cselect_b32 s90, 0x70000000, s90\n\t" \
  "s_waitcnt lgkmcnt(0)\n\t" \
  "s_barrier\n\t" \
  "ds_read_b128 v[160:163], v221 offset:0\n\t" \
  "ds_read_b128 v[164:167], v221 offset:1024\n\t" \
  "s_waitcnt lgkmcnt(0)\n\t" \
  "v_add_f32 v160, v160, v144\n\t" \
  "v_add_f32 v161, v161, v145\n\t" \
  "v_add_f32 v162, v162, v146\n\t" \
  "v_add_f32 v163, v163, v147\n\t" \
  "v_add_f32 v164, v164, v148\n\t" \
  "v_add_f32 v165, v165, v149\n\t" \
  "v_add_f32 v166, v166, v150\n\t" \
  "v_add_f32 v167, v167, v151\n\t" \
  "v_cvt_pk_bf16_f32 v168, v160, v161\n\t" \
  "v_cvt_pk_bf16_f32 v169, v162, v163\n\t" \
  "v_cvt_pk_bf16_f32 v170, v164, v165\n\t" \
  "v_cvt_pk_bf16_f32 v171, v166, v167\n\t" \
  "buffer_store_dwordx4 v[168:171], v172, %[rt], s90 offen sc1\n\t" \
  "s_branch .Lfb00%=\n\t" \
  ".Lf10%=:\n\t" \
  "s_nop 7\n\t" \
  "ds_write_b128 v220, v[136:139] offset:0\n\t" \
  "ds_write_b128 v220, v[140:143] offset:1024\n\t" \
  "s_lshr_b32 s90, s87, 7\n\t" \
  "s_xor_b32 s90, s90, %[s0]\n\t" \
  "s_and_b32 s90, s90, 1\n\t" \
  "s_lshl1_add_u32 s90, s87, s90\n\t" \
  "s_lshl_b32 s90, s90, 13\n\t" \
  "s_cmp_lt_i32 s86, 1\n\t" \
  "s_cselect_b32 s90, 0x70000000, s90\n\t" \
  "s_waitcnt lgkmcnt(0)\n\t" \
  "s_barrier\n\t" \
  "ds_read_b128 v[160:163], v221 offset:0\n\t" \
  "ds_read_b128 v[164:167], v221 offset:1024\n\t" \
  "s_waitcnt lgkmcnt(0)\n\t" \
  "v_add_f32 v160, v160, v128\n\t" \
  "v_add_f32 v161, v161, v129\n\t" \
  "v_add_f32 v162, v162, v130\n\t" \
  "v_add_f32 v163, v163, v131\n\t" \
  "v_add_f32 v164, v164, v132\n\t" \
  "v_add_f32 v165, v165, v133\n\t" \
  "v_add_f32 v166, v166, v134\n\t" \
  "v_add_f32 v167, v167, v135\n\t" \
  "v_cvt_pk_bf16_f32 v168, v160, v161\n\t" \
  "v_cvt_pk_bf16_f32 v169, v162, v163\n\t" \
  "v_cvt_pk_bf16_f32 v170, v164, v165\n\t" \
  "v_cvt_pk_bf16_f32 v171, v166, v167\n\t" \
  "buffer_store_dwordx4 v[168:171], v172, %[rt], s90 offen sc1\n\t" \
  "s_branch .Lfb10%=\n\t" \
  ".Lw00%=:\n\t" \
  "s_waitcnt vmcnt(2)\n\t" \
  "s_barrier\n\t" \
  "s_branch .Lg00%=\n\t" \
  ".Lw10%=:\n\t" \
  "s_waitcnt vmcnt(2)\n\t" \
  "s_barrier\n\t" \
  "s_branch .Lg10%=\n\t" \
  ".Lw20%=:\n\t" \
  "s_waitcnt vmcnt(2)\n\t" \
  "s_barrier\n\t" \
  "s_branch .Lg20%=\n\t" \
  ".Lw30%=:\n\t" \
  "s_waitcnt vmcnt(2)\n\t" \
  "s_barrier\n\t" \
  "s_branch .Lg30%=\n\t" \
  ".Lx0%=:\n\t" \
  "s_waitcnt lgkmcnt(0)\n\t" \
  "s_setprio 0\n\t" \
  "s_mov_b32 m0, s94\n\t" \
  "s_branch .Le%=\n\t" \
  ".Lk%=:\n\t" \
  "s_mov_b32 s94, m0\n\t" \
  "s_mov_b64 s[92:93], %[x0]\n\t" \
  "s_mov_b32 s86, %[g0]\n\t" \
  "s_add_u32 s87, s86, %[t0]\n\t" \
  "s_sub_i32 s88, s86, 3\n\t" \
  "ds_read_b128 v[176:179], v208 offset:0\n\t" \
  "ds_read_b128 v[180:183], v208 offset:16384\n\t" \
  "ds_read_b128 v[184:187], v209 offset:0\n\t" \
  "ds_read_b128 v[188:191], v209 offset:16384\n\t" \
  "ds_read_b128 v[192:195], v210 offset:0\n\t" \
  "ds_read_b128 v[196:199], v210 offset:16384\n\t" \
  "ds_read_b128 v[200:203], v211 offset:0\n\t" \
  "ds_read_b128 v[204:207], v211 offset:16384\n\t" \
  "s_branch .Lfb01%=\n\t" \
  ".Lp1%=:\n\t" \
  "s_cmp_le_u32 s86, 5\n\t" \
  "s_cbranch_scc1 .Lf01%=\n\t" \
  ".Lfb01%=:\n\t" \
  "s_waitcnt lgkmcnt(6)\n\t" \
  "v_mfma_f32_16x16x32_bf16 v[128:131], v[176:179], v[0:3], 0\n\t" \
  "v_mfma_f32_16x16x32_bf16 v[136:139], v[176:179], v[4:7], 0\n\t" \
  "ds_read_b128 v[176:179], v208 offset:256\n\t" \
  "v_mfma_f32_16x16x32_bf16 v[132:135], v[180:183], v[0:3], 0\n\t" \
  "v_mfma_f32_16x16x32_bf16 v[140:143], v[180:183], v[4:7], 0\n\t" \
  "ds_read_b128 v[180:183], v208 offset:16640\n\t" \
  "s_waitcnt lgkmcnt(6)\n\t" \
  "v_mfma_f32_16x16x32_bf16 v[128:131], v[184:187], v[8:11], v[128:131]\n\t" \
  "v_mfma_f32_16x16x32_bf16 v[136:139], v[184:187], v[12:15], v[136:139]\n\t" \
  "ds_read_b128 v[184:187], v209 offset:256\n\t" \
  "s_add_u32 m0, %[lw], 67584\n\t" \
  "v_mfma_f32_16x16x32_bf16 v[132:135], v[188:191], v[8:11], v[132:135]\n\t" \
  "v_mfma_f32_16x16x32_bf16 v[140:143], v[188:191], v[12:15], v[140:143]\n\t" \
  "ds_read_b128 v[188:191], v209 offset:16640\n\t" \
  "global_load_lds_dwordx4 v218, s[92:93] sc1\n\t" \
  "ds_write_b128 v220, v[152:155] offset:0\n\t" \
  "ds_write_b128 v220, v[156:159] offset:1024\n\t" \
  "s_waitcnt lgkmcnt(8)\n\t" \
  "v_mfma_f32_16x16x32_bf16 v[128:131], v[192:195], v[16:19], v[128:131]\n\t" \
  "v_mfma_f32_16x16x32_bf16 v[136:139], v[192:195], v[20:23], v[136:139]\n\t" \
  "ds_read_b128 v[192:195], v210 offset:256\n\t" \
  "v_mfma_f32_16x16x32_bf16 v[132:135], v[196:199], v[16:19], v[132:135]\n\t" \
  "v_mfma_f32_16x16x32_bf16 v[140:143], v[196:199], v[20:23], v[140:143]\n\t" \
  "ds_read_b128 v[196:199], v210 offset:16640\n\t" \
  "s_lshr_b32 s90, s87, 7\n\t" \
  "s_xor_b32 s90, s90, %[s0]\n\t" \
  "s_and_b32 s90, s90, 1\n\t" \
  "s_lshl1_add_u32 s90, s87, s90\n\t" \
  "s_lshl_b32 s90, s90, 13\n\t" \
  "s_cmp_lt_i32 s86, 6\n\t" \
  "s_cselect_b32 s90, 0x70000000, s90\n\t" \
  "s_waitcnt lgkmcnt(8)\n\t" \
  "v_mfma_f32_16x16x32_bf16 v[128:131], v[200:203], v[24:27], v[128:131]\n\t" \
  "v_mfma_f32_16x16x32_bf16 v[136:139], v[200:203], v[28:31], v[136:139]\n\t" \
  "ds_read_b128 v[200:203], v211 offset:256\n\t" \
  "s_add_u32 m0, %[lw], 68608\n\t" \
  "v_mfma_f32_16x16x32_bf16 v[132:135], v[204:207], v[24:27], v[132:135]\n\t" \
  "v_mfma_f32_16x16x32_bf16 v[140:143], v[204:207], v[28:31], v[140:143]\n\t" \
  "ds_read_b128 v[204:207], v211 offset:16640\n\t" \
  "global_load_lds_dwordx4 v219, s[92:93] sc1\n\t" \
  "s_waitcnt lgkmcnt(4)\n\t" \
  "s_cmp_lt_u32 s86, 6\n\t" \
  "s_cbranch_scc1 .Lw01%=\n\t" \
  "s_waitcnt vmcnt(4)\n\t" \
  "s_barrier\n\t" \
  ".Lg01%=:\n\t" \
  "s_add_u32 s92, s92, 512\n\t" \
  "s_addc_u32 s93, s93, 0\n\t" \
  "v_mfma_f32_16x16x32_bf16 v[128:131], v[176:179], v[32:35], v[128:131]\n\t" \
  "v_mfma_f32_16x16x32_bf16 v[136:139], v[176:179], v[36:39], v[136:139]\n\t" \
  "ds_read_b128 v[176:179], v208 offset:32768\n\t" \
  "v_mfma_f32_16x16x32_bf16 v[132:135], v[180:183], v[32:35], v[132:135]\n\t" \
  "v_mfma_f32_16x16x32_bf16 v[140:143], v[180:183], v[36:39], v[140:143]\n\t" \
  "ds_read_b128 v[180:183], v208 offset:49152\n\t" \
  "ds_read_b128 v[160:163], v221 offset:0\n\t" \
  "ds_read_b128 v[164:167], v221 offset:1024\n\t" \
  "s_sub_i32 s91, s86, 2\n\t" \
  "s_max_i32 s88, s88, s91\n\t" \
  "v_mov_b32 v173, s88\n\t" \
  "buffer_store_dword v173, v174, %[rs], 0 offen sc1\n\t" \
  "v_mfma_f32_16x16x32_bf16 v[128:131], v[184:187], v[40:43], v[128:131]\n\t" \
  "v_mfma_f32_16x16x32_bf16 v[136:139], v[184:187], v[44:47], v[136:139]\n\t" \
  "ds_read_b128 v[184:187], v209 offset:32768\n\t" \
  "s_add_u32 m0, %[lw], 98304\n\t" \
  "v_mfma_f32_16x16x32_bf16 v[132:135], v[188:191], v[40:43], v[132:135]\n\t" \
  "v_mfma_f32_16x16x32_bf16 v[140:143], v[188:191], v[44:47], v[140:143]\n\t" \
  "ds_read_b128 v[188:191], v209 offset:49152\n\t" \
  "global_load_lds_dwordx4 v216, s[92:93] sc1\n\t" \
  "s_waitcnt lgkmcnt(8)\n\t" \
  "v_mfma_f32_16x16x32_bf16 v[128:131], v[192:195], v[48:51], v[128:131]\n\t" \
  "v_mfma_f32_16x16x32_bf16 v[136:139], v[192:195], v[52:55], v[136:139]\n\t" \
  "ds_read_b128 v[192:195], v210 offset:32768\n\t" \
  "v_mfma_f32_16x16x32_bf16 v[132:135], v[196:199], v[48:51], v[132:135]\n\t" \
  "v_mfma_f32_16x16x32_bf16 v[140:143], v[196:199], v[52:55], v[140:143]\n\t" \
  "ds_read_b128 v[196:199], v210 offset:49152\n\t" \
  "s_waitcnt lgkmcnt(4)\n\t" \
  "v_add_f32 v160, v160, v144\n\t" \
  "v_add_f32 v161, v161, v145\n\t" \
  "v_add_f32 v162, v162, v146\n\t" \
  "v_add_f32 v163, v163, v147\n\t" \
  "v_add_f32 v164, v164, v148\n\t" \
  "v_add_f32 v165, v165, v149\n\t" \
  "v_add_f32 v166, v166, v150\n\t" \
  "v_add_f32 v167, v167, v151\n\t" \
  "v_mfma_f32_16x16x32_bf16 v[128:131], v[200:203], v[56:59], v[128:131]\n\t" \
  "v_mfma_f32_16x16x32_bf16 v[136:139], v[200:203], v[60:63], v[136:139]\n\t" \
  "ds_read_b128 v[200:203], v211 offset:32768\n\t" \
  "s_add_u32 m0, %[lw], 99328\n\t" \
  "v_mfma_f32_16x16x32_bf16 v[132:135], v[204:207], v[56:59], v[132:135]\n\t" \
  "v_mfma_f32_16x16x32_bf16 v[140:143], v[204:207], v[60:63], v[140:143]\n\t" \
  "ds_read_b128 v[204:207], v211 offset:49152\n\t" \
  "global_load_lds_dwordx4 v217, s[92:93] sc1\n\t" \
  "v_cvt_pk_bf16_f32 v168, v160, v161\n\t" \
  "v_cvt_pk_bf16_f32 v169, v162, v163\n\t" \
  "v_cvt_pk_bf16_f32 v170, v164, v165\n\t" \
  "v_cvt_pk_bf16_f32 v171, v166, v167\n\t" \
  "buffer_store_dwordx4 v[168:171], v172, %[rt], s90 offen sc1\n\t" \
  "v_mfma_f32_16x16x32_bf16 v[128:131], v[176:179], v[64:67], v[128:131]\n\t" \
  "v_mfma_f32_16x16x32_bf16 v[136:139], v[176:179], v[68:71], v[136:139]\n\t" \
  "ds_read_b128 v[176:179], v208 offset:33024\n\t" \
  "v_mfma_f32_16x16x32_bf16 v[132:135], v[180:183], v[64:67], v[132:135]\n\t" \
  "v_mfma_f32_16x16x32_bf16 v[140:143], v[180:183], v[68:71], v[140:143]\n\t" \
  "ds_read_b128 v[180:183], v208 offset:49408\n\t" \
  "s_waitcnt lgkmcnt(6)\n\t" \
  "v_mfma_f32_16x16x32_bf16 v[128:131], v[184:187], v[72:75], v[128:131]\n\t" \
  "v_mfma_f32_16x16x32_bf16 v[136:139], v[184:187], v[76:79], v[136:139]\n\t" \
  "ds_read_b128 v[184:187], v209 offset:33024\n\t" \
  "s_add_u32 m0, %[lw], 100352\n\t" \
  "v_mfma_f32_16x16x32_bf16 v[132:135], v[188:191], v[72:75], v[132:135]\n\t" \
  "v_mfma_f32_16x16x32_bf16 v[140:143], v[188:191], v[76:79], v[140:143]\n\t" \
  "ds_read_b128 v[188:191], v209 offset:49408\n\t" \
  "global_load_lds_dwordx4 v218, s[92:93] sc1\n\t" \
  "s_waitcnt lgkmcnt(6)\n\t" \
  "v_mfma_f32_16x16x32_bf16 v[128:131], v[192:195], v[80:83], v[128:131]\n\t" \
  "v_mfma_f32_16x16x32_bf16 v[136:139], v[192:195], v[84:87], v[136:139]\n\t" \
  "ds_read_b128 v[192:195], v210 offset:33024\n\t" \
  "v_mfma_f32_16x16x32_bf16 v[132:135], v[196:199], v[80:83], v[132:135]\n\t" \
  "v_mfma_f32_16x16x32_bf16 v[140:143], v[196:199], v[84:87], v[140:143]\n\t" \
  "ds_read_b128 v[196:199], v210 offset:49408\n\t" \
  "s_waitcnt lgkmcnt(6)\n\t" \
  "v_mfma_f32_16x16x32_bf16 v[128:131], v[200:203], v[88:91], v[128:131]\n\t" \
  "v_mfma_f32_16x16x32_bf16 v[136:139], v[200:203], v[92:95], v[136:139]\n\t" \
  "ds_read_b128 v[200:203], v211 offset:33024\n\t" \
  "s_add_u32 m0, %[lw], 101376\n\t" \
  "v_mfma_f32_16x16x32_bf16 v[132:135], v[204:207], v[88:91], v[132:135]\n\t" \
  "v_mfma_f32_16x16x32_bf16 v[140:143], v[204:207], v[92:95], v[140:143]\n\t" \
  "ds_read_b128 v[204:207], v211 offset:49408\n\t" \
  "global_load_lds_dwordx4 v219, s[92:93] sc1\n\t" \
  "s_cmp_lt_u32 s86, 8\n\t" \
  "s_cbranch_scc1 .Lw11%=\n\t" \
  "s_waitcnt vmcnt(6)\n\t" \
  "s_barrier\n\t" \
  ".Lg11%=:\n\t" \
  "s_add_u32 s92, s92, 65024\n\t" \
  "s_addc_u32 s93, s93, 0\n\t" \
  "s_waitcnt lgkmcnt(6)\n\t" \
  "v_mfma_f32_16x16x32_bf16 v[128:131], v[176:179], v[96:99], v[128:131]\n\t" \
  "v_mfma_f32_16x16x32_bf16 v[136:139], v[176:179], v[100:103], v[136:139]\n\t" \
  "ds_read_b128 v[176:179], v212 offset:0\n\t" \
  "v_mfma_f32_16x16x32_bf16 v[132:135], v[180:183], v[96:99], v[132:135]\n\t" \
  "v_mfma_f32_16x16x32_bf16 v[140:143], v[180:183], v[100:103], v[140:143]\n\t" \
  "ds_read_b128 v[180:183], v212 offset:16384\n\t" \
  "s_waitcnt lgkmcnt(6)\n\t" \
  "v_mfma_f32_16x16x32_bf16 v[128:131], v[184:187], v[104:107], v[128:131]\n\t" \
  "v_mfma_f32_16x16x32_bf16 v[136:139], v[184:187], v[108:111], v[136:139]\n\t" \
  "ds_read_b128 v[184:187], v213 offset:0\n\t" \
  "s_add_u32 m0, %[lw], 0\n\t" \
  "v_mfma_f32_16x16x32_bf16 v[132:135], v[188:191], v[104:107], v[132:135]\n\t" \
  "v_mfma_f32_16x16x32_bf16 v[140:143], v[188:191], v[108:111], v[140:143]\n\t" \
  "ds_read_b128 v[188:191], v213 offset:16384\n\t" \
  "global_load_lds_dwordx4 v216, s[92:93] sc1\n\t" \
  "s_waitcnt lgkmcnt(6)\n\t" \
  "v_mfma_f32_16x16x32_bf16 v[128:131], v[192:195], v[112:115], v[128:131]\n\t" \
  "v_mfma_f32_16x16x32_bf16 v[136:139], v[192:195], v[116:119], v[136:139]\n\t" \
  "ds_read_b128 v[192:195], v214 offset:0\n\t" \
  "v_mfma_f32_16x16x32_bf16 v[132:135], v[196:199], v[112:115], v[132:135]\n\t" \
  "v_mfma_f32_16x16x32_bf16 v[140:143], v[196:199], v[116:119], v[140:143]\n\t" \
  "ds_read_b128 v[196:199], v214 offset:16384\n\t" \
  "s_waitcnt lgkmcnt(6)\n\t" \
  "v_mfma_f32_16x16x32_bf16 v[128:131], v[200:203], v[120:123], v[128:131]\n\t" \
  "v_mfma_f32_16x16x32_bf16 v[136:139], v[200:203], v[124:127], v[136:139]\n\t" \
  "ds_read_b128 v[200:203], v215 offset:0\n\t" \
  "s_add_u32 m0, %[lw], 1024\n\t" \
  "v_mfma_f32_16x16x32_bf16 v[132:135], v[204:207], v[120:123], v[132:135]\n\t" \
  "v_mfma_f32_16x16x32_bf16 v[140:143], v[204:207], v[124:127], v[140:143]\n\t" \
  "ds_read_b128 v[204:207], v215 offset:16384\n\t" \
  "global_load_lds_dwordx4 v217, s[92:93] sc1\n\t" \
  "s_add_u32 s86, s86, 1\n\t" \
  "s_add_u32 s87, s87, 1\n\t" \
  "s_cmp_le_u32 s86, 5\n\t" \
  "s_cbranch_scc1 .Lf11%=\n\t" \
  ".Lfb11%=:\n\t" \
  "s_waitcnt lgkmcnt(6)\n\t" \
  "v_mfma_f32_16x16x32_bf16 v[144:147], v[176:179], v[0:3], 0\n\t" \
  "v_mfma_f32_16x16x32_bf16 v[152:155], v[176:179], v[4:7], 0\n\t" \
  "ds_read_b128 v[176:179], v212 offset:256\n\t" \
  "v_mfma_f32_16x16x32_bf16 v[148:151], v[180:183], v[0:3], 0\n\t" \
  "v_mfma_f32_16x16x32_bf16 v[156:159], v[180:183], v[4:7], 0\n\t" \
  "ds_read_b128 v[180:183], v212 offset:16640\n\t" \
  "s_waitcnt lgkmcnt(6)\n\t" \
  "v_mfma_f32_16x16x32_bf16 v[144:147], v[184:187], v[8:11], v[144:147]\n\t" \
  "v_mfma_f32_16x16x32_bf16 v[152:155], v[184:187], v[12:15], v[152:155]\n\t" \
  "ds_read_b128 v[184:187], v213 offset:256\n\t" \
  "s_add_u32 m0, %[lw], 2048\n\t" \
  "v_mfma_f32_16x16x32_bf16 v[148:151], v[188:191], v[8:11], v[148:151]\n\t" \
  "v_mfma_f32_16x16x32_bf16 v[156:159], v[188:191], v[12:15], v[156:159]\n\t" \
  "ds_read_b128 v[188:191], v213 offset:16640\n\t" \
  "global_load_lds_dwordx4 v218, s[92:93] sc1\n\t" \
  "ds_write_b128 v220, v[136:139] offset:0\n\t" \
  "ds_write_b128 v220, v[140:143] offset:1024\n\t" \
  "s_waitcnt lgkmcnt(8)\n\t" \
  "v_mfma_f32_16x16x32_bf16 v[144:147], v[192:195], v[16:19], v[144:147]\n\t" \
  "v_mfma_f32_16x16x32_bf16 v[152:155], v[192:195], v[20:23], v[152:155]\n\t" \
  "ds_read_b128 v[192:195], v214 offset:256\n\t" \
  "v_mfma_f32_16x16x32_bf16 v[148:151], v[196:199], v[16:19], v[148:151]\n\t" \
  "v_mfma_f32_16x16x32_bf16 v[156:159], v[196:199], v[20:23], v[156:159]\n\t" \
  "ds_read_b128 v[196:199], v214 offset:16640\n\t" \
  "s_lshr_b32 s90, s87, 7\n\t" \
  "s_xor_b32 s90, s90, %[s0]\n\t" \
  "s_and_b32 s90, s90, 1\n\t" \
  "s_lshl1_add_u32 s90, s87, s90\n\t" \
  "s_lshl_b32 s90, s90, 13\n\t" \
  "s_cmp_lt_i32 s86, 6\n\t" \
  "s_cselect_b32 s90, 0x70000000, s90\n\t" \
  "s_waitcnt lgkmcnt(8)\n\t" \
  "v_mfma_f32_16x16x32_bf16 v[144:147], v[200:203], v[24:27], v[144:147]\n\t" \
  "v_mfma_f32_16x16x32_bf16 v[152:155], v[200:203], v[28:31], v[152:155]\n\t" \
  "ds_read_b128 v[200:203], v215 offset:256\n\t" \
  "s_add_u32 m0, %[lw], 3072\n\t" \
  "v_mfma_f32_16x16x32_bf16 v[148:151], v[204:207], v[24:27], v[148:151]\n\t" \
  "v_mfma_f32_16x16x32_bf16 v[156:159], v[204:207], v[28:31], v[156:159]\n\t" \
  "ds_read_b128 v[204:207], v215 offset:16640\n\t" \
  "global_load_lds_dwordx4 v219, s[92:93] sc1\n\t" \
  "s_waitcnt lgkmcnt(4)\n\t" \
  "s_cmp_lt_u32 s86, 6\n\t" \
  "s_cbranch_scc1 .Lw21%=\n\t" \
  "s_waitcnt vmcnt(4)\n\t" \
  "s_barrier\n\t" \
  ".Lg21%=:\n\t" \
  "s_add_u32 s92, s92, 512\n\t" \
  "s_addc_u32 s93, s93, 0\n\t" \
  "v_mfma_f32_16x16x32_bf16 v[144:147], v[176:179], v[32:35], v[144:147]\n\t" \
  "v_mfma_f32_16x16x32_bf16 v[152:155], v[176:179], v[36:39], v[152:155]\n\t" \
  "ds_read_b128 v[176:179], v212 offset:32768\n\t" \
  "v_mfma_f32_16x16x32_bf16 v[148:151], v[180:183], v[32:35], v[148:151]\n\t" \
  "v_mfma_f32_16x16x32_bf16 v[156:159], v[180:183], v[36:39], v[156:159]\n\t" \
  "ds_read_b128 v[180:183], v212 offset:49152\n\t" \
  "ds_read_b128 v[160:163], v221 offset:0\n\t" \
  "ds_read_b128 v[164:167], v221 offset:1024\n\t" \
  "s_sub_i32 s91, s86, 2\n\t" \
  "s_max_i32 s88, s88, s91\n\t" \
  "v_mov_b32 v173, s88\n\t" \
  "buffer_store_dword v173, v174, %[rs], 0 offen sc1\n\t" \
  "v_mfma_f32_16x16x32_bf16 v[144:147], v[184:187], v[40:43], v[144:147]\n\t" \
  "v_mfma_f32_16x16x32_bf16 v[152:155], v[184:187], v[44:47], v[152:155]\n\t" \
  "ds_read_b128 v[184:187], v213 offset:32768\n\t" \
  "s_add_u32 m0, %[lw], 32768\n\t" \
  "v_mfma_f32_16x16x32_bf16 v[148:151], v[188:191], v[40:43], v[148:151]\n\t" \
  "v_mfma_f32_16x16x32_bf16 v[156:159], v[188:191], v[44:47], v[156:159]\n\t" \
  "ds_read_b128 v[188:191], v213 offset:49152\n\t" \
  "global_load_lds_dwordx4 v216, s[92:93] sc1\n\t" \
  "s_waitcnt lgkmcnt(8)\n\t" \
  "v_mfma_f32_16x16x32_bf16 v[144:147], v[192:195], v[48:51], v[144:147]\n\t" \
  "v_mfma_f32_16x16x32_bf16 v[152:155], v[192:195], v[52:55], v[152:155]\n\t" \
  "ds_read_b128 v[192:195], v214 offset:32768\n\t" \
  "v_mfma_f32_16x16x32_bf16 v[148:151], v[196:199], v[48:51], v[148:151]\n\t" \
  "v_mfma_f32_16x16x32_bf16 v[156:159], v[196:199], v[52:55], v[156:159]\n\t" \
  "ds_read_b128 v[196:199], v214 offset:49152\n\t" \
  "s_waitcnt lgkmcnt(4)\n\t" \
  "v_add_f32 v160, v160, v128\n\t" \
  "v_add_f32 v161, v161, v129\n\t" \
  "v_add_f32 v162, v162, v130\n\t" \
  "v_add_f32 v163, v163, v131\n\t" \
  "v_add_f32 v164, v164, v132\n\t" \
  "v_add_f32 v165, v165, v133\n\t" \
  "v_add_f32 v166, v166, v134\n\t" \
  "v_add_f32 v167, v167, v135\n\t" \
  "v_mfma_f32_16x16x32_bf16 v[144:147], v[200:203], v[56:59], v[144:147]\n\t" \
  "v_mfma_f32_16x16x32_bf16 v[152:155], v[200:203], v[60:63], v[152:155]\n\t" \
  "ds_read_b128 v[200:203], v215 offset:32768\n\t" \
  "s_add_u32 m0, %[lw], 33792\n\t" \
  "v_mfma_f32_16x16x32_bf16 v[148:151], v[204:207], v[56:59], v[148:151]\n\t" \
  "v_mfma_f32_16x16x32_bf16 v[156:159], v[204:207], v[60:63], v[156:159]\n\t" \
  "ds_read_b128 v[204:207], v215 offset:49152\n\t" \
  "global_load_lds_dwordx4 v217, s[92:93] sc1\n\t" \
  "v_cvt_pk_bf16_f32 v168, v160, v161\n\t" \
  "v_cvt_pk_bf16_f32 v169, v162, v163\n\t" \
  "v_cvt_pk_bf16_f32 v170, v164, v165\n\t" \
  "v_cvt_pk_bf16_f32 v171, v166, v167\n\t" \
  "buffer_store_dwordx4 v[168:171], v172, %[rt], s90 offen sc1\n\t" \
  "v_mfma_f32_16x16x32_bf16 v[144:147], v[176:179], v[64:67], v[144:147]\n\t" \
  "v_mfma_f32_16x16x32_bf16 v[152:155], v[176:179], v[68:71], v[152:155]\n\t" \
  "ds_read_b128 v[176:179], v212 offset:33024\n\t" \
  "v_mfma_f32_16x16x32_bf16 v[148:151], v[180:183], v[64:67], v[148:151]\n\t" \
  "v_mfma_f32_16x16x32_bf16 v[156:159], v[180:183], v[68:71], v[156:159]\n\t" \
  "ds_read_b128 v[180:183], v212 offset:49408\n\t" \
  "s_waitcnt lgkmcnt(6)\n\t" \
  "v_mfma_f32_16x16x32_bf16 v[144:147], v[184:187], v[72:75], v[144:147]\n\t" \
  "v_mfma_f32_16x16x32_bf16 v[152:155], v[184:187], v[76:79], v[152:155]\n\t" \
  "ds_read_b128 v[184:187], v213 offset:33024\n\t" \
  "s_add_u32 m0, %[lw], 34816\n\t" \
  "v_mfma_f32_16x16x32_bf16 v[148:151], v[188:191], v[72:75], v[148:151]\n\t" \
  "v_mfma_f32_16x16x32_bf16 v[156:159], v[188:191], v[76:79], v[156:159]\n\t" \
  "ds_read_b128 v[188:191], v213 offset:49408\n\t" \
  "global_load_lds_dwordx4 v218, s[92:93] sc1\n\t" \
  "s_waitcnt lgkmcnt(6)\n\t" \
  "v_mfma_f32_16x16x32_bf16 v[144:147], v[192:195], v[80:83], v[144:147]\n\t" \
  "v_mfma_f32_16x16x32_bf16 v[152:155], v[192:195], v[84:87], v[152:155]\n\t" \
  "ds_read_b128 v[192:195], v214 offset:33024\n\t" \
  "v_mfma_f32_16x16x32_bf16 v[148:151], v[196:199], v[80:83], v[148:151]\n\t" \
  "v_mfma_f32_16x16x32_bf16 v[156:159], v[196:199], v[84:87], v[156:159]\n\t" \
  "ds_read_b128 v[196:199], v214 offset:49408\n\t" \
  "s_waitcnt lgkmcnt(6)\n\t" \
  "v_mfma_f32_16x16x32_bf16 v[144:147], v[200:203], v[88:91], v[144:147]\n\t" \
  "v_mfma_f32_16x16x32_bf16 v[152:155], v[200:203], v[92:95], v[152:155]\n\t" \
  "ds_read_b128 v[200:203], v215 offset:33024\n\t" \
  "s_add_u32 m0, %[lw], 35840\n\t" \
  "v_mfma_f32_16x16x32_bf16 v[148:151], v[204:207], v[88:91], v[148:151]\n\t" \
  "v_mfma_f32_16x16x32_bf16 v[156:159], v[204:207], v[92:95], v[156:159]\n\t" \
  "ds_read_b128 v[204:207], v215 offset:49408\n\t" \
  "global_load_lds_dwordx4 v219, s[92:93] sc1\n\t" \
  "s_cmp_lt_u32 s86, 8\n\t" \
  "s_cbranch_scc1 .Lw31%=\n\t" \
  "s_waitcnt vmcnt(6)\n\t" \
  "s_barrier\n\t" \
  ".Lg31%=:\n\t" \
  "s_add_u32 s92, s92, 65024\n\t" \
  "s_addc_u32 s93, s93, 0\n\t" \
  "s_waitcnt lgkmcnt(6)\n\t" \
  "v_mfma_f32_16x16x32_bf16 v[144:147], v[176:179], v[96:99], v[144:147]\n\t" \
  "v_mfma_f32_16x16x32_bf16 v[152:155], v[176:179], v[100:103], v[152:155]\n\t" \
  "ds_read_b128 v[176:179], v208 offset:0\n\t" \
  "v_mfma_f32_16x16x32_bf16 v[148:151], v[180:183], v[96:99], v[148:151]\n\t" \
  "v_mfma_f32_16x16x32_bf16 v[156:159], v[180:183], v[100:103], v[156:159]\n\t" \
  "ds_read_b128 v[180:183], v208 offset:16384\n\t" \
  "s_waitcnt lgkmcnt(6)\n\t" \
  "v_mfma_f32_16x16x32_bf16 v[144:147], v[184:187], v[104:107], v[144:147]\n\t" \
  "v_mfma_f32_16x16x32_bf16 v[152:155], v[184:187], v[108:111], v[152:155]\n\t" \
  "ds_read_b128 v[184:187], v209 offset:0\n\t" \
  "s_add_u32 m0, %[lw], 65536\n\t" \
  "v_mfma_f32_16x16x32_bf16 v[148:151], v[188:191], v[104:107], v[148:151]\n\t" \
  "v_mfma_f32_16x16x32_bf16 v[156:159], v[188:191], v[108:111], v[156:159]\n\t" \
  "ds_read_b128 v[188:191], v209 offset:16384\n\t" \
  "global_load_lds_dwordx4 v216, s[92:93] sc1\n\t" \
  "s_waitcnt lgkmcnt(6)\n\t" \
  "v_mfma_f32_16x16x32_bf16 v[144:147], v[192:195], v[112:115], v[144:147]\n\t" \
  "v_mfma_f32_16x16x32_bf16 v[152:155], v[192:195], v[116:119], v[152:155]\n\t" \
  "ds_read_b128 v[192:195], v210 offset:0\n\t" \
  "v_mfma_f32_16x16x32_bf16 v[148:151], v[196:199], v[112:115], v[148:151]\n\t" \
  "v_mfma_f32_16x16x32_bf16 v[156:159], v[196:199], v[116:119], v[156:159]\n\t" \
  "ds_read_b128 v[196:199], v210 offset:16384\n\t" \
  "s_waitcnt lgkmcnt(6)\n\t" \
  "v_mfma_f32_16x16x32_bf16 v[144:147], v[200:203], v[120:123], v[144:147]\n\t" \
  "v_mfma_f32_16x16x32_bf16 v[152:155], v[200:203], v[124:127], v[152:155]\n\t" \
  "ds_read_b128 v[200:203], v211 offset:0\n\t" \
  "s_add_u32 m0, %[lw], 66560\n\t" \
  "v_mfma_f32_16x16x32_bf16 v[148:151], v[204:207], v[120:123], v[148:151]\n\t" \
  "v_mfma_f32_16x16x32_bf16 v[156:159], v[204:207], v[124:127], v[156:159]\n\t" \
  "ds_read_b128 v[204:207], v211 offset:16384\n\t" \
  "global_load_lds_dwordx4 v217, s[92:93] sc1\n\t" \
  "s_add_u32 s86, s86, 1\n\t" \
  "s_add_u32 s87, s87, 1\n\t" \
  "s_cmp_lt_u32 s86, %[end]\n\t" \
  "s_cbranch_scc1 .Lp1%=\n\t" \
  "s_branch .Lx1%=\n\t" \
  ".Lf01%=:\n\t" \
  "s_nop 7\n\t" \
  "ds_write_b128 v220, v[152:155] offset:0\n\t" \
  "ds_write_b128 v220, v[156:159] offset:1024\n\t" \
  "s_lshr_b32 s90, s87, 7\n\t" \
  "s_xor_b32 s90, s90, %[s0]\n\t" \
  "s_and_b32 s90, s90, 1\n\t" \
  "s_lshl1_add_u32 s90, s87, s90\n\t" \
  "s_lshl_b32 s90, s90, 13\n\t" \
  "s_cmp_lt_i32 s86, 1\n\t" \
  "s_cselect_b32 s90, 0x70000000, s90\n\t" \
  "s_waitcnt lgkmcnt(0)\n\t" \
  "s_barrier\n\t" \
  "ds_read_b128 v[160:163], v221 offset:0\n\t" \
  "ds_read_b128 v[164:167], v221 offset:1024\n\t" \
  "s_waitcnt lgkmcnt(0)\n\t" \
  "v_add_f32 v160, v160, v144\n\t" \
  "v_add_f32 v161, v161, v145\n\t" \
  "v_add_f32 v162, v162, v146\n\t" \
  "v_add_f32 v163, v163, v147\n\t" \
  "v_add_f32 v164, v164, v148\n\t" \
  "v_add_f32 v165, v165, v149\n\t" \
  "v_add_f32 v166, v166, v150\n\t" \
  "v_add_f32 v167, v167, v151\n\t" \
  "v_cvt_pk_bf16_f32 v168, v160, v161\n\t" \
  "v_cvt_pk_bf16_f32 v169, v162, v163\n\t" \
  "v_cvt_pk_bf16_f32 v170, v164, v165\n\t" \
  "v_cvt_pk_bf16_f32 v171, v166, v167\n\t" \
  "buffer_store_dwordx4 v[168:171], v172, %[rt], s90 offen sc1\n\t" \
  "s_branch .Lfb01%=\n\t" \
  ".Lf11%=:\n\t" \
  "s_nop 7\n\t" \
  "ds_write_b128 v220, v[136:139] offset:0\n\t" \
  "ds_write_b128 v220, v[140:143] offset:1024\n\t" \
  "s_lshr_b32 s90, s87, 7\n\t" \
  "s_xor_b32 s90, s90, %[s0]\n\t" \
  "s_and_b32 s90, s90, 1\n\t" \
  "s_lshl1_add_u32 s90, s87, s90\n\t" \
  "s_lshl_b32 s90, s90, 13\n\t" \
  "s_cmp_lt_i32 s86, 1\n\t" \
  "s_cselect_b32 s90, 0x70000000, s90\n\t" \
  "s_waitcnt lgkmcnt(0)\n\t" \
  "s_barrier\n\t" \
  "ds_read_b128 v[160:163], v221 offset:0\n\t" \
  "ds_read_b128 v[164:167], v221 offset:1024\n\t" \
  "s_waitcnt lgkmcnt(0)\n\t" \
  "v_add_f32 v160, v160, v128\n\t" \
  "v_add_f32 v161, v161, v129\n\t" \
  "v_add_f32 v162, v162, v130\n\t" \
  "v_add_f32 v163, v163, v131\n\t" \
  "v_add_f32 v164, v164, v132\n\t" \
  "v_add_f32 v165, v165, v133\n\t" \
  "v_add_f32 v166, v166, v134\n\t" \
  "v_add_f32 v167, v167, v135\n\t" \
  "v_cvt_pk_bf16_f32 v168, v160, v161\n\t" \
  "v_cvt_pk_bf16_f32 v169, v162, v163\n\t" \
  "v_cvt_pk_bf16_f32 v170, v164, v165\n\t" \
  "v_cvt_pk_bf16_f32 v171, v166, v167\n\t" \
  "buffer_store_dwordx4 v[168:171], v172, %[rt], s90 offen sc1\n\t" \
  "s_branch .Lfb11%=\n\t" \
  ".Lw01%=:\n\t" \
  "s_waitcnt vmcnt(2)\n\t" \
  "s_barrier\n\t" \
  "s_sub_i32 s91, s86, 1\n\t" \
  "s_max_i32 s88, s88, s91\n\t" \
  "v_mov_b32 v173, s88\n\t" \
  "buffer_store_dword v173, v174, %[rs], 0 offen sc1\n\t" \
  "s_branch .Lg01%=\n\t" \
  ".Lw11%=:\n\t" \
  "s_waitcnt vmcnt(2)\n\t" \
  "s_barrier\n\t" \
  "s_sub_i32 s91, s86, 1\n\t" \
  "s_max_i32 s88, s88, s91\n\t" \
  "v_mov_b32 v173, s88\n\t" \
  "buffer_store_dword v173, v174, %[rs], 0 offen sc1\n\t" \
  "s_branch .Lg11%=\n\t" \
  ".Lw21%=:\n\t" \
  "s_waitcnt vmcnt(2)\n\t" \
  "s_barrier\n\t" \
  "s_sub_i32 s91, s86, 1\n\t" \
  "s_max_i32 s88, s88, s91\n\t" \
  "v_mov_b32 v173, s88\n\t" \
  "buffer_store_dword v173, v174, %[rs], 0 offen sc1\n\t" \
  "s_branch .Lg21%=\n\t" \
  ".Lw31%=:\n\t" \
  "s_waitcnt vmcnt(2)\n\t" \
  "s_barrier\n\t" \
  "s_sub_i32 s91, s86, 1\n\t" \
  "s_max_i32 s88, s88, s91\n\t" \
  "v_mov_b32 v173, s88\n\t" \
  "buffer_store_dword v173, v174, %[rs], 0 offen sc1\n\t" \
  "s_branch .Lg31%=\n\t" \
  ".Lx1%=:\n\t" \
  "s_waitcnt lgkmcnt(0)\n\t" \
  "s_setprio 0\n\t" \
  "s_mov_b32 m0, s94\n\t" \
  ".Le%=:\n\t"
#define PP_ASM_0 \
  "s_cmp_lg_u32 %[kh], 0\n\t" \
  "s_cbranch_scc1 .Lk%=\n\t" \
  "s_mov_b32 s94, m0\n\t" \
  "s_mov_b64 s[92:93], %[x0]\n\t" \
  "s_mov_b32 s86, %[g0]\n\t" \
  "s_add_u32 s87, s86, %[t0]\n\t" \
  "s_add_u32 m0, %[lw], 67584\n\t" \
  "s_nop 0\n\t" \
  "global_load_lds_dwordx4 v250, s[92:93] sc1\n\t" \
  "s_add_u32 m0, %[lw], 68608\n\t" \
  "s_nop 0\n\t" \
  "global_load_lds_dwordx4 v251, s[92:93] sc1\n\t" \
  "s_barrier\n\t" \
  "s_branch .Ls0%=\n\t" \
  ".Lp0%=:\n\t" \
  "s_waitcnt vmcnt(9)\n\t" \
  "s_barrier\n\t" \
  ".Ls0%=:\n\t" \
  "s_add_u32 s92, s92, 512\n\t" \
  "s_addc_u32 s93, s93, 0\n\t" \
  "ds_read_b128 v[176:179], v240 offset:0\n\t" \
  "ds_read_b128 v[180:183], v240 offset:16384\n\t" \
  "ds_read_b128 v[184:187], v241 offset:0\n\t" \
  "ds_read_b128 v[188:191], v241 offset:16384\n\t" \
  "s_add_u32 m0, %[lw], 98304\n\t" \
  "s_nop 0\n\t" \
  "global_load_lds_dwordx4 v248, s[92:93] sc1\n\t" \
  "ds_read_b128 v[192:195], v242 offset:0\n\t" \
  "ds_read_b128 v[196:199], v242 offset:16384\n\t" \
  "ds_read_b128 v[200:203], v243 offset:0\n\t" \
  "ds_read_b128 v[204:207], v243 offset:16384\n\t" \
  "s_add_u32 m0, %[lw], 99328\n\t" \
  "s_nop 0\n\t" \
  "global_load_lds_dwordx4 v249, s[92:93] sc1\n\t" \
  "ds_read_b128 v[208:211], v240 offset:256\n\t" \
  "ds_read_b128 v[212:215], v240 offset:16640\n\t" \
  "ds_read_b128 v[216:219], v241 offset:256\n\t" \
  "ds_read_b128 v[220:223], v241 offset:16640\n\t" \
  "s_add_u32 m0, %[lw], 100352\n\t" \
  "s_nop 0\n\t" \
  "global_load_lds_dwordx4 v250, s[92:93] sc1\n\t" \
  "ds_read_b128 v[224:227], v242 offset:256\n\t" \
  "ds_read_b128 v[228:231], v242 offset:16640\n\t" \
  "ds_read_b128 v[232:235], v243 offset:256\n\t" \
  "ds_read_b128 v[236:239], v243 offset:16640\n\t" \
  "s_add_u32 m0, %[lw], 101376\n\t" \
  "s_nop 0\n\t" \
  "global_load_lds_dwordx4 v251, s[92:93] sc1\n\t" \
  "ds_write_b128 v252, v[152:155] offset:0\n\t" \
  "ds_write_b128 v252, v[156:159] offset:1024\n\t" \
  "s_waitcnt lgkmcnt(0)\n\t" \
  "s_setprio 1\n\t" \
  "v_mfma_f32_16x16x32_bf16 v[128:131], v[0:3], v[176:179], 0\n\t" \
  "v_mfma_f32_16x16x32_bf16 v[132:135], v[4:7], v[176:179], 0\n\t" \
  "v_mfma_f32_16x16x32_bf16 v[136:139], v[0:3], v[180:183], 0\n\t" \
  "v_mfma_f32_16x16x32_bf16 v[140:143], v[4:7], v[180:183], 0\n\t" \
  "v_mfma_f32_16x16x32_bf16 v[128:131], v[8:11], v[184:187], v[128:131]\n\t" \
  "v_mfma_f32_16x16x32_bf16 v[132:135], v[12:15], v[184:187], v[132:135]\n\t" \
  "v_mfma_f32_16x16x32_bf16 v[136:139], v[8:11], v[188:191], v[136:139]\n\t" \
  "v_mfma_f32_16x16x32_bf16 v[140:143], v[12:15], v[188:191], v[140:143]\n\t" \
  "v_mfma_f32_16x16x32_bf16 v[128:131], v[16:19], v[192:195], v[128:131]\n\t" \
  "v_mfma_f32_16x16x32_bf16 v[132:135], v[20:23], v[192:195], v[132:135]\n\t" \
  "v_mfma_f32_16x16x32_bf16 v[136:139], v[16:19], v[196:199], v[136:139]\n\t" \
  "v_mfma_f32_16x16x32_bf16 v[140:143], v[20:23], v[196:199], v[140:143]\n\t" \
  "v_mfma_f32_16x16x32_bf16 v[128:131], v[24:27], v[200:203], v[128:131]\n\t" \
  "v_mfma_f32_16x16x32_bf16 v[132:135], v[28:31], v[200:203], v[132:135]\n\t" \
  "v_mfma_f32_16x16x32_bf16 v[136:139], v[24:27], v[204:207], v[136:139]\n\t" \
  "v_mfma_f32_16x16x32_bf16 v[140:143], v[28:31], v[204:207], v[140:143]\n\t" \
  "v_mfma_f32_16x16x32_bf16 v[128:131], v[32:35], v[208:211], v[128:131]\n\t" \
  "v_mfma_f32_16x16x32_bf16 v[132:135], v[36:39], v[208:211], v[132:135]\n\t" \
  "v_mfma_f32_16x16x32_bf16 v[136:139], v[32:35], v[212:215], v[136:139]\n\t" \
  "v_mfma_f32_16x16x32_bf16 v[140:143], v[36:39], v[212:215], v[140:143]\n\t" \
  "v_mfma_f32_16x16x32_bf16 v[128:131], v[40:43], v[216:219], v[128:131]\n\t" \
  "v_mfma_f32_16x16x32_bf16 v[132:135], v[44:47], v[216:219], v[132:135]\n\t" \
  "v_mfma_f32_16x16x32_bf16 v[136:139], v[40:43], v[220:223], v[136:139]\n\t" \
  "v_mfma_f32_16x16x32_bf16 v[140:143], v[44:47], v[220:223], v[140:143]\n\t" \
  "v_mfma_f32_16x16x32_bf16 v[128:131], v[48:51], v[224:227], v[128:131]\n\t" \
  "v_mfma_f32_16x16x32_bf16 v[132:135], v[52:55], v[224:227], v[132:135]\n\t" \
  "v_mfma_f32_16x16x32_bf16 v[136:139], v[48:51], v[228:231], v[136:139]\n\t" \
  "v_mfma_f32_16x16x32_bf16 v[140:143], v[52:55], v[228:231], v[140:143]\n\t" \
  "v_mfma_f32_16x16x32_bf16 v[128:131], v[56:59], v[232:235], v[128:131]\n\t" \
  "v_mfma_f32_16x16x32_bf16 v[132:135], v[60:63], v[232:235], v[132:135]\n\t" \
  "v_mfma_f32_16x16x32_bf16 v[136:139], v[56:59], v[236:239], v[136:139]\n\t" \
  "v_mfma_f32_16x16x32_bf16 v[140:143], v[60:63], v[236:239], v[140:143]\n\t" \
  "s_setprio 0\n\t" \
  "s_waitcnt vmcnt(4)\n\t" \
  "s_barrier\n\t" \
  "s_add_u32 s92, s92, 65024\n\t" \
  "s_addc_u32 s93, s93, 0\n\t" \
  "ds_read_b128 v[160:163], v253 offset:0\n\t" \
  "ds_read_b128 v[164:167], v253 offset:1024\n\t" \
  "ds_read_b128 v[176:179], v240 offset:32768\n\t" \
  "ds_read_b128 v[180:183], v240 offset:49152\n\t" \
  "ds_read_b128 v[184:187], v241 offset:32768\n\t" \
  "ds_read_b128 v[188:191], v241 offset:49152\n\t" \
  "s_add_u32 m0, %[lw], 0\n\t" \
  "s_nop 0\n\t" \
  "global_load_lds_dwordx4 v248, s[92:93] sc1\n\t" \
  "ds_read_b128 v[192:195], v242 offset:32768\n\t" \
  "ds_read_b128 v[196:199], v242 offset:49152\n\t" \
  "ds_read_b128 v[200:203], v243 offset:32768\n\t" \
  "ds_read_b128 v[204:207], v243 offset:49152\n\t" \
  "s_add_u32 m0, %[lw], 1024\n\t" \
  "s_nop 0\n\t" \
  "global_load_lds_dwordx4 v249, s[92:93] sc1\n\t" \
  "ds_read_b128 v[208:211], v240 offset:33024\n\t" \
  "ds_read_b128 v[212:215], v240 offset:49408\n\t" \
  "ds_read_b128 v[216:219], v241 offset:33024\n\t" \
  "ds_read_b128 v[220:223], v241 offset:49408\n\t" \
  "s_add_u32 m0, %[lw], 2048\n\t" \
  "s_nop 0\n\t" \
  "global_load_lds_dwordx4 v250, s[92:93] sc1\n\t" \
  "ds_read_b128 v[224:227], v242 offset:33024\n\t" \
  "ds_read_b128 v[228:231], v242 offset:49408\n\t" \
  "ds_read_b128 v[232:235], v243 offset:33024\n\t" \
  "ds_read_b128 v[236:239], v243 offset:49408\n\t" \
  "s_add_u32 m0, %[lw], 3072\n\t" \
  "s_nop 0\n\t" \
  "global_load_lds_dwordx4 v251, s[92:93] sc1\n\t" \
  "s_waitcnt lgkmcnt(15)\n\t" \
  "s_waitcnt lgkmcnt(12)\n\t" \
  "s_lshr_b32 s90, s87, 7\n\t" \
  "s_xor_b32 s90, s90, %[s0]\n\t" \
  "s_and_b32 s90, s90, 1\n\t" \
  "s_lshl1_add_u32 s90, s87, s90\n\t" \
  "s_lshl_b32 s90, s90, 13\n\t" \
  "v_add_f32 v160, v160, v144\n\t" \
  "v_add_f32 v161, v161, v145\n\t" \
  "v_add_f32 v162, v162, v146\n\t" \
  "v_add_f32 v163, v163, v147\n\t" \
  "v_add_f32 v164, v164, v148\n\t" \
  "v_add_f32 v165, v165, v149\n\t" \
  "v_add_f32 v166, v166, v150\n\t" \
  "v_add_f32 v167, v167, v151\n\t" \
  "v_cvt_pk_bf16_f32 v168, v160, v161\n\t" \
  "v_cvt_pk_bf16_f32 v169, v162, v163\n\t" \
  "v_cvt_pk_bf16_f32 v170, v164, v165\n\t" \
  "v_cvt_pk_bf16_f32 v171, v166, v167\n\t" \
  "buffer_store_dwordx4 v[168:171], v172, %[rt], s90 offen sc1\n\t" \
  "s_add_u32 s86, s86, 1\n\t" \
  "s_add_u32 s87, s87, 1\n\t" \
  "s_waitcnt lgkmcnt(0)\n\t" \
  "s_setprio 1\n\t" \
  "v_mfma_f32_16x16x32_bf16 v[128:131], v[64:67], v[176:179], v[128:131]\n\t" \
  "v_mfma_f32_16x16x32_bf16 v[132:135], v[68:71], v[176:179], v[132:135]\n\t" \
  "v_mfma_f32_16x16x32_bf16 v[136:139], v[64:67], v[180:183], v[136:139]\n\t" \
  "v_mfma_f32_16x16x32_bf16 v[140:143], v[68:71], v[180:183], v[140:143]\n\t" \
  "v_mfma_f32_16x16x32_bf16 v[128:131], v[72:75], v[184:187], v[128:131]\n\t" \
  "v_mfma_f32_16x16x32_bf16 v[132:135], v[76:79], v[184:187], v[132:135]\n\t" \
  "v_mfma_f32_16x16x32_bf16 v[136:139], v[72:75], v[188:191], v[136:139]\n\t" \
  "v_mfma_f32_16x16x32_bf16 v[140:143], v[76:79], v[188:191], v[140:143]\n\t" \
  "v_mfma_f32_16x16x32_bf16 v[128:131], v[80:83], v[192:195], v[128:131]\n\t" \
  "v_mfma_f32_16x16x32_bf16 v[132:135], v[84:87], v[192:195], v[132:135]\n\t" \
  "v_mfma_f32_16x16x32_bf16 v[136:139], v[80:83], v[196:199], v[136:139]\n\t" \
  "v_mfma_f32_16x16x32_bf16 v[140:143], v[84:87], v[196:199], v[140:143]\n\t" \
  "v_mfma_f32_16x16x32_bf16 v[128:131], v[88:91], v[200:203], v[128:131]\n\t" \
  "v_mfma_f32_16x16x32_bf16 v[132:135], v[92:95], v[200:203], v[132:135]\n\t" \
  "v_mfma_f32_16x16x32_bf16 v[136:139], v[88:91], v[204:207], v[136:139]\n\t" \
  "v_mfma_f32_16x16x32_bf16 v[140:143], v[92:95], v[204:207], v[140:143]\n\t" \
  "v_mfma_f32_16x16x32_bf16 v[128:131], v[96:99], v[208:211], v[128:131]\n\t" \
  "v_mfma_f32_16x16x32_bf16 v[132:135], v[100:103], v[208:211], v[132:135]\n\t" \
  "v_mfma_f32_16x16x32_bf16 v[136:139], v[96:99], v[212:215], v[136:139]\n\t" \
  "v_mfma_f32_16x16x32_bf16 v[140:143], v[100:103], v[212:215], v[140:143]\n\t" \
  "v_mfma_f32_16x16x32_bf16 v[128:131], v[104:107], v[216:219], v[128:131]\n\t" \
  "v_mfma_f32_16x16x32_bf16 v[132:135], v[108:111], v[216:219], v[132:135]\n\t" \
  "v_mfma_f32_16x16x32_bf16 v[136:139], v[104:107], v[220:223], v[136:139]\n\t" \
  "v_mfma_f32_16x16x32_bf16 v[140:143], v[108:111], v[220:223], v[140:143]\n\t" \
  "v_mfma_f32_16x16x32_bf16 v[128:131], v[112:115], v[224:227], v[128:131]\n\t" \
  "v_mfma_f32_16x16x32_bf16 v[132:135], v[116:119], v[224:227], v[132:135]\n\t" \
  "v_mfma_f32_16x16x32_bf16 v[136:139], v[112:115], v[228:231], v[136:139]\n\t" \
  "v_mfma_f32_16x16x32_bf16 v[140:143], v[116:119], v[228:231], v[140:143]\n\t" \
  "v_mfma_f32_16x16x32_bf16 v[128:131], v[120:123], v[232:235], v[128:131]\n\t" \
  "v_mfma_f32_16x16x32_bf16 v[132:135], v[124:127], v[232:235], v[132:135]\n\t" \
  "v_mfma_f32_16x16x32_bf16 v[136:139], v[120:123], v[236:239], v[136:139]\n\t" \
  "v_mfma_f32_16x16x32_bf16 v[140:143], v[124:127], v[236:239], v[140:143]\n\t" \
  "s_setprio 0\n\t" \
  "s_waitcnt vmcnt(9)\n\t" \
  "s_barrier\n\t" \
  "s_add_u32 s92, s92, 512\n\t" \
  "s_addc_u32 s93, s93, 0\n\t" \
  "ds_read_b128 v[176:179], v244 offset:0\n\t" \
  "ds_read_b128 v[180:183], v244 offset:16384\n\t" \
  "ds_read_b128 v[184:187], v245 offset:0\n\t" \
  "ds_read_b128 v[188:191], v245 offset:16384\n\t" \
  "s_add_u32 m0, %[lw], 32768\n\t" \
  "s_nop 0\n\t" \
  "global_load_lds_dwordx4 v248, s[92:93] sc1\n\t" \
  "ds_read_b128 v[192:195], v246 offset:0\n\t" \
  "ds_read_b128 v[196:199], v246 offset:16384\n\t" \
  "ds_read_b128 v[200:203], v247 offset:0\n\t" \
  "ds_read_b128 v[204:207], v247 offset:16384\n\t" \
  "s_add_u32 m0, %[lw], 33792\n\t" \
  "s_nop 0\n\t" \
  "global_load_lds_dwordx4 v249, s[92:93] sc1\n\t" \
  "ds_read_b128 v[208:211], v244 offset:256\n\t" \
  "ds_read_b128 v[212:215], v244 offset:16640\n\t" \
  "ds_read_b128 v[216:219], v245 offset:256\n\t" \
  "ds_read_b128 v[220:223], v245 offset:16640\n\t" \
  "s_add_u32 m0, %[lw], 34816\n\t" \
  "s_nop 0\n\t" \
  "global_load_lds_dwordx4 v250, s[92:93] sc1\n\t" \
  "ds_read_b128 v[224:227], v246 offset:256\n\t" \
  "ds_read_b128 v[228:231], v246 offset:16640\n\t" \
  "ds_read_b128 v[232:235], v247 offset:256\n\t" \
  "ds_read_b128 v[236:239], v247 offset:16640\n\t" \
  "s_add_u32 m0, %[lw], 35840\n\t" \
  "s_nop 0\n\t" \
  "global_load_lds_dwordx4 v251, s[92:93] sc1\n\t" \
  "ds_write_b128 v252, v[136:139] offset:0\n\t" \
  "ds_write_b128 v252, v[140:143] offset:1024\n\t" \
  "s_waitcnt lgkmcnt(0)\n\t" \
  "s_setprio 1\n\t" \
  "v_mfma_f32_16x16x32_bf16 v[144:147], v[0:3], v[176:179], 0\n\t" \
  "v_mfma_f32_16x16x32_bf16 v[148:151], v[4:7], v[176:179], 0\n\t" \
  "v_mfma_f32_16x16x32_bf16 v[152:155], v[0:3], v[180:183], 0\n\t" \
  "v_mfma_f32_16x16x32_bf16 v[156:159], v[4:7], v[180:183], 0\n\t" \
  "v_mfma_f32_16x16x32_bf16 v[144:147], v[8:11], v[184:187], v[144:147]\n\t" \
  "v_mfma_f32_16x16x32_bf16 v[148:151], v[12:15], v[184:187], v[148:151]\n\t" \
  "v_mfma_f32_16x16x32_bf16 v[152:155], v[8:11], v[188:191], v[152:155]\n\t" \
  "v_mfma_f32_16x16x32_bf16 v[156:159], v[12:15], v[188:191], v[156:159]\n\t" \
  "v_mfma_f32_16x16x32_bf16 v[144:147], v[16:19], v[192:195], v[144:147]\n\t" \
  "v_mfma_f32_16x16x32_bf16 v[148:151], v[20:23], v[192:195], v[148:151]\n\t" \
  "v_mfma_f32_16x16x32_bf16 v[152:155], v[16:19], v[196:199], v[152:155]\n\t" \
  "v_mfma_f32_16x16x32_bf16 v[156:159], v[20:23], v[196:199], v[156:159]\n\t" \
  "v_mfma_f32_16x16x32_bf16 v[144:147], v[24:27], v[200:203], v[144:147]\n\t" \
  "v_mfma_f32_16x16x32_bf16 v[148:151], v[28:31], v[200:203], v[148:151]\n\t" \
  "v_mfma_f32_16x16x32_bf16 v[152:155], v[24:27], v[204:207], v[152:155]\n\t" \
  "v_mfma_f32_16x16x32_bf16 v[156:159], v[28:31], v[204:207], v[156:159]\n\t" \
  "v_mfma_f32_16x16x32_bf16 v[144:147], v[32:35], v[208:211], v[144:147]\n\t" \
  "v_mfma_f32_16x16x32_bf16 v[148:151], v[36:39], v[208:211], v[148:151]\n\t" \
  "v_mfma_f32_16x16x32_bf16 v[152:155], v[32:35], v[212:215], v[152:155]\n\t" \
  "v_mfma_f32_16x16x32_bf16 v[156:159], v[36:39], v[212:215], v[156:159]\n\t" \
  "v_mfma_f32_16x16x32_bf16 v[144:147], v[40:43], v[216:219], v[144:147]\n\t" \
  "v_mfma_f32_16x16x32_bf16 v[148:151], v[44:47], v[216:219], v[148:151]\n\t" \
  "v_mfma_f32_16x16x32_bf16 v[152:155], v[40:43], v[220:223], v[152:155]\n\t" \
  "v_mfma_f32_16x16x32_bf16 v[156:159], v[44:47], v[220:223], v[156:159]\n\t" \
  "v_mfma_f32_16x16x32_bf16 v[144:147], v[48:51], v[224:227], v[144:147]\n\t" \
  "v_mfma_f32_16x16x32_bf16 v[148:151], v[52:55], v[224:227], v[148:151]\n\t" \
  "v_mfma_f32_16x16x32_bf16 v[152:155], v[48:51], v[228:231], v[152:155]\n\t" \
  "v_mfma_f32_16x16x32_bf16 v[156:159], v[52:55], v[228:231], v[156:159]\n\t" \
  "v_mfma_f32_16x16x32_bf16 v[144:147], v[56:59], v[232:235], v[144:147]\n\t" \
  "v_mfma_f32_16x16x32_bf16 v[148:151], v[60:63], v[232:235], v[148:151]\n\t" \
  "v_mfma_f32_16x16x32_bf16 v[152:155], v[56:59], v[236:239], v[152:155]\n\t" \
  "v_mfma_f32_16x16x32_bf16 v[156:159], v[60:63], v[236:239], v[156:159]\n\t" \
  "s_setprio 0\n\t" \
  "s_waitcnt vmcnt(4)\n\t" \
  "s_barrier\n\t" \
  "s_add_u32 s92, s92, 65024\n\t" \
  "s_addc_u32 s93, s93, 0\n\t" \
  "ds_read_b128 v[160:163], v253 offset:0\n\t" \
  "ds_read_b128 v[164:167], v253 offset:1024\n\t" \
  "ds_read_b128 v[176:179], v244 offset:32768\n\t" \
  "ds_read_b128 v[180:183], v244 offset:49152\n\t" \
  "ds_read_b128 v[184:187], v245 offset:32768\n\t" \
  "ds_read_b128 v[188:191], v245 offset:49152\n\t" \
  "s_add_u32 m0, %[lw], 65536\n\t" \
  "s_nop 0\n\t" \
  "global_load_lds_dwordx4 v248, s[92:93] sc1\n\t" \
  "ds_read_b128 v[192:195], v246 offset:32768\n\t" \
  "ds_read_b128 v[196:199], v246 offset:49152\n\t" \
  "ds_read_b128 v[200:203], v247 offset:32768\n\t" \
  "ds_read_b128 v[204:207], v247 offset:49152\n\t" \
  "s_add_u32 m0, %[lw], 66560\n\t" \
  "s_nop 0\n\t" \
  "global_load_lds_dwordx4 v249, s[92:93] sc1\n\t" \
  "ds_read_b128 v[208:211], v244 offset:33024\n\t" \
  "ds_read_b128 v[212:215], v244 offset:49408\n\t" \
  "ds_read_b128 v[216:219], v245 offset:33024\n\t" \
  "ds_read_b128 v[220:223], v245 offset:49408\n\t" \
  "s_add_u32 m0, %[lw], 67584\n\t" \
  "s_nop 0\n\t" \
  "global_load_lds_dwordx4 v250, s[92:93] sc1\n\t" \
  "ds_read_b128 v[224:227], v246 offset:33024\n\t" \
  "ds_read_b128 v[228:231], v246 offset:49408\n\t" \
  "ds_read_b128 v[232:235], v247 offset:33024\n\t" \
  "ds_read_b128 v[236:239], v247 offset:49408\n\t" \
  "s_add_u32 m0, %[lw], 68608\n\t" \
  "s_nop 0\n\t" \
  "global_load_lds_dwordx4 v251, s[92:93] sc1\n\t" \
  "s_waitcnt lgkmcnt(15)\n\t" \
  "s_waitcnt lgkmcnt(12)\n\t" \
  "s_lshr_b32 s90, s87, 7\n\t" \
  "s_xor_b32 s90, s90, %[s0]\n\t" \
  "s_and_b32 s90, s90, 1\n\t" \
  "s_lshl1_add_u32 s90, s87, s90\n\t" \
  "s_lshl_b32 s90, s90, 13\n\t" \
  "v_add_f32 v160, v160, v128\n\t" \
  "v_add_f32 v161, v161, v129\n\t" \
  "v_add_f32 v162, v162, v130\n\t" \
  "v_add_f32 v163, v163, v131\n\t" \
  "v_add_f32 v164, v164, v132\n\t" \
  "v_add_f32 v165, v165, v133\n\t" \
  "v_add_f32 v166, v166, v134\n\t" \
  "v_add_f32 v167, v167, v135\n\t" \
  "v_cvt_pk_bf16_f32 v168, v160, v161\n\t" \
  "v_cvt_pk_bf16_f32 v169, v162, v163\n\t" \
  "v_cvt_pk_bf16_f32 v170, v164, v165\n\t" \
  "v_cvt_pk_bf16_f32 v171, v166, v167\n\t" \
  "buffer_store_dwordx4 v[168:171], v172, %[rt], s90 offen sc1\n\t" \
  "s_add_u32 s86, s86, 1\n\t" \
  "s_add_u32 s87, s87, 1\n\t" \
  "s_waitcnt lgkmcnt(0)\n\t" \
  "s_setprio 1\n\t" \
  "v_mfma_f32_16x16x32_bf16 v[144:147], v[64:67], v[176:179], v[144:147]\n\t" \
  "v_mfma_f32_16x16x32_bf16 v[148:151], v[68:71], v[176:179], v[148:151]\n\t" \
  "v_mfma_f32_16x16x32_bf16 v[152:155], v[64:67], v[180:183], v[152:155]\n\t" \
  "v_mfma_f32_16x16x32_bf16 v[156:159], v[68:71], v[180:183], v[156:159]\n\t" \
  "v_mfma_f32_16x16x32_bf16 v[144:147], v[72:75], v[184:187], v[144:147]\n\t" \
  "v_mfma_f32_16x16x32_bf16 v[148:151], v[76:79], v[184:187], v[148:151]\n\t" \
  "v_mfma_f32_16x16x32_bf16 v[152:155], v[72:75], v[188:191], v[152:155]\n\t" \
  "v_mfma_f32_16x16x32_bf16 v[156:159], v[76:79], v[188:191], v[156:159]\n\t" \
  "v_mfma_f32_16x16x32_bf16 v[144:147], v[80:83], v[192:195], v[144:147]\n\t" \
  "v_mfma_f32_16x16x32_bf16 v[148:151], v[84:87], v[192:195], v[148:151]\n\t" \
  "v_mfma_f32_16x16x32_bf16 v[152:155], v[80:83], v[196:199], v[152:155]\n\t" \
  "v_mfma_f32_16x16x32_bf16 v[156:159], v[84:87], v[196:199], v[156:159]\n\t" \
  "v_mfma_f32_16x16x32_bf16 v[144:147], v[88:91], v[200:203], v[144:147]\n\t" \
  "v_mfma_f32_16x16x32_bf16 v[148:151], v[92:95], v[200:203], v[148:151]\n\t" \
  "v_mfma_f32_16x16x32_bf16 v[152:155], v[88:91], v[204:207], v[152:155]\n\t" \
  "v_mfma_f32_16x16x32_bf16 v[156:159], v[92:95], v[204:207], v[156:159]\n\t" \
  "v_mfma_f32_16x16x32_bf16 v[144:147], v[96:99], v[208:211], v[144:147]\n\t" \
  "v_mfma_f32_16x16x32_bf16 v[148:151], v[100:103], v[208:211], v[148:151]\n\t" \
  "v_mfma_f32_16x16x32_bf16 v[152:155], v[96:99], v[212:215], v[152:155]\n\t" \
  "v_mfma_f32_16x16x32_bf16 v[156:159], v[100:103], v[212:215], v[156:159]\n\t" \
  "v_mfma_f32_16x16x32_bf16 v[144:147], v[104:107], v[216:219], v[144:147]\n\t" \
  "v_mfma_f32_16x16x32_bf16 v[148:151], v[108:111], v[216:219], v[148:151]\n\t" \
  "v_mfma_f32_16x16x32_bf16 v[152:155], v[104:107], v[220:223], v[152:155]\n\t" \
  "v_mfma_f32_16x16x32_bf16 v[156:159], v[108:111], v[220:223], v[156:159]\n\t" \
  "v_mfma_f32_16x16x32_bf16 v[144:147], v[112:115], v[224:227], v[144:147]\n\t" \
  "v_mfma_f32_16x16x32_bf16 v[148:151], v[116:119], v[224:227], v[148:151]\n\t" \
  "v_mfma_f32_16x16x32_bf16 v[152:155], v[112:115], v[228:231], v[152:155]\n\t" \
  "v_mfma_f32_16x16x32_bf16 v[156:159], v[116:119], v[228:231], v[156:159]\n\t" \
  "v_mfma_f32_16x16x32_bf16 v[144:147], v[120:123], v[232:235], v[144:147]\n\t" \
  "v_mfma_f32_16x16x32_bf16 v[148:151], v[124:127], v[232:235], v[148:151]\n\t" \
  "v_mfma_f32_16x16x32_bf16 v[152:155], v[120:123], v[236:239], v[152:155]\n\t" \
  "v_mfma_f32_16x16x32_bf16 v[156:159], v[124:127], v[236:239], v[156:159]\n\t" \
  "s_setprio 0\n\t" \
  "s_cmp_lt_u32 s86, 64\n\t" \
  "s_cbranch_scc1 .Lp0%=\n\t" \
  "s_barrier\n\t" \
  "s_nop 2\n\t" \
  "ds_write_b128 v252, v[152:155] offset:0\n\t" \
  "ds_write_b128 v252, v[156:159] offset:1024\n\t" \
  "s_waitcnt lgkmcnt(0)\n\t" \
  "s_barrier\n\t" \
  "ds_read_b128 v[160:163], v253 offset:0\n\t" \
  "ds_read_b128 v[164:167], v253 offset:1024\n\t" \
  "s_waitcnt lgkmcnt(0)\n\t" \
  "s_lshr_b32 s90, s87, 7\n\t" \
  "s_xor_b32 s90, s90, %[s0]\n\t" \
  "s_and_b32 s90, s90, 1\n\t" \
  "s_lshl1_add_u32 s90, s87, s90\n\t" \
  "s_lshl_b32 s90, s90, 13\n\t" \
  "v_add_f32 v160, v160, v144\n\t" \
  "v_add_f32 v161, v161, v145\n\t" \
  "v_add_f32 v162, v162, v146\n\t" \
  "v_add_f32 v163, v163, v147\n\t" \
  "v_add_f32 v164, v164, v148\n\t" \
  "v_add_f32 v165, v165, v149\n\t" \
  "v_add_f32 v166, v166, v150\n\t" \
  "v_add_f32 v167, v167, v151\n\t" \
  "v_cvt_pk_bf16_f32 v168, v160, v161\n\t" \
  "v_cvt_pk_bf16_f32 v169, v162, v163\n\t" \
  "v_cvt_pk_bf16_f32 v170, v164, v165\n\t" \
  "v_cvt_pk_bf16_f32 v171, v166, v167\n\t" \
  "buffer_store_dwordx4 v[168:171], v172, %[rt], s90 offen sc1\n\t" \
  "s_waitcnt vmcnt(0)\n\t" \
  "s_barrier\n\t" \
  "s_mov_b32 m0, s94\n\t" \
  "s_branch .Le%=\n\t" \
  ".Lk%=:\n\t" \
  "s_mov_b32 s94, m0\n\t" \
  "s_mov_b64 s[92:93], %[x0]\n\t" \
  "s_mov_b32 s86, %[g0]\n\t" \
  "s_add_u32 s87, s86, %[t0]\n\t" \
  "s_add_u32 m0, %[lw], 67584\n\t" \
  "s_nop 0\n\t" \
  "global_load_lds_dwordx4 v250, s[92:93] sc1\n\t" \
  "s_add_u32 m0, %[lw], 68608\n\t" \
  "s_nop 0\n\t" \
  "global_load_lds_dwordx4 v251, s[92:93] sc1\n\t" \
  "s_barrier\n\t" \
  "s_branch .Ls1%=\n\t" \
  ".Lp1%=:\n\t" \
  "s_waitcnt vmcnt(10)\n\t" \
  "s_barrier\n\t" \
  "s_setprio 2\n\t" \
  "v_mfma_f32_16x16x32_bf16 v[144:147], v[64:67], v[176:179], v[144:147]\n\t" \
  "v_mfma_f32_16x16x32_bf16 v[148:151], v[68:71], v[176:179], v[148:151]\n\t" \
  "v_mfma_f32_16x16x32_bf16 v[152:155], v[64:67], v[180:183], v[152:155]\n\t" \
  "v_mfma_f32_16x16x32_bf16 v[156:159], v[68:71], v[180:183], v[156:159]\n\t" \
  "v_mfma_f32_16x16x32_bf16 v[144:147], v[72:75], v[184:187], v[144:147]\n\t" \
  "v_mfma_f32_16x16x32_bf16 v[148:151], v[76:79], v[184:187], v[148:151]\n\t" \
  "v_mfma_f32_16x16x32_bf16 v[152:155], v[72:75], v[188:191], v[152:155]\n\t" \
  "v_mfma_f32_16x16x32_bf16 v[156:159], v[76:79], v[188:191], v[156:159]\n\t" \
  "v_mfma_f32_16x16x32_bf16 v[144:147], v[80:83], v[192:195], v[144:147]\n\t" \
  "v_mfma_f32_16x16x32_bf16 v[148:151], v[84:87], v[192:195], v[148:151]\n\t" \
  "v_mfma_f32_16x16x32_bf16 v[152:155], v[80:83], v[196:199], v[152:155]\n\t" \
  "v_mfma_f32_16x16x32_bf16 v[156:159], v[84:87], v[196:199], v[156:159]\n\t" \
  "v_mfma_f32_16x16x32_bf16 v[144:147], v[88:91], v[200:203], v[144:147]\n\t" \
  "v_mfma_f32_16x16x32_bf16 v[148:151], v[92:95], v[200:203], v[148:151]\n\t" \
  "v_mfma_f32_16x16x32_bf16 v[152:155], v[88:91], v[204:207], v[152:155]\n\t" \
  "v_mfma_f32_16x16x32_bf16 v[156:159], v[92:95], v[204:207], v[156:159]\n\t" \
  "v_mfma_f32_16x16x32_bf16 v[144:147], v[96:99], v[208:211], v[144:147]\n\t" \
  "v_mfma_f32_16x16x32_bf16 v[148:151], v[100:103], v[208:211], v[148:151]\n\t" \
  "v_mfma_f32_16x16x32_bf16 v[152:155], v[96:99], v[212:215], v[152:155]\n\t" \
  "v_mfma_f32_16x16x32_bf16 v[156:159], v[100:103], v[212:215], v[156:159]\n\t" \
  "v_mfma_f32_16x16x32_bf16 v[144:147], v[104:107], v[216:219], v[144:147]\n\t" \
  "v_mfma_f32_16x16x32_bf16 v[148:151], v[108:111], v[216:219], v[148:151]\n\t" \
  "v_mfma_f32_16x16x32_bf16 v[152:155], v[104:107], v[220:223], v[152:155]\n\t" \
  "v_mfma_f32_16x16x32_bf16 v[156:159], v[108:111], v[220:223], v[156:159]\n\t" \
  "v_mfma_f32_16x16x32_bf16 v[144:147], v[112:115], v[224:227], v[144:147]\n\t" \
  "v_mfma_f32_16x16x32_bf16 v[148:151], v[116:119], v[224:227], v[148:151]\n\t" \
  "v_mfma_f32_16x16x32_bf16 v[152:155], v[112:115], v[228:231], v[152:155]\n\t" \
  "v_mfma_f32_16x16x32_bf16 v[156:159], v[116:119], v[228:231], v[156:159]\n\t" \
  "v_mfma_f32_16x16x32_bf16 v[144:147], v[120:123], v[232:235], v[144:147]\n\t" \
  "v_mfma_f32_16x16x32_bf16 v[148:151], v[124:127], v[232:235], v[148:151]\n\t" \
  "v_mfma_f32_16x16x32_bf16 v[152:155], v[120:123], v[236:239], v[152:155]\n\t" \
  "v_mfma_f32_16x16x32_bf16 v[156:159], v[124:127], v[236:239], v[156:159]\n\t" \
  "s_setprio 0\n\t" \
  ".Ls1%=:\n\t" \
  "s_add_u32 s92, s92, 512\n\t" \
  "s_addc_u32 s93, s93, 0\n\t" \
  "ds_read_b128 v[176:179], v240 offset:0\n\t" \
  "ds_read_b128 v[180:183], v240 offset:16384\n\t" \
  "ds_read_b128 v[184:187], v241 offset:0\n\t" \
  "ds_read_b128 v[188:191], v241 offset:16384\n\t" \
  "s_add_u32 m0, %[lw], 98304\n\t" \
  "s_nop 0\n\t" \
  "global_load_lds_dwordx4 v248, s[92:93] sc1\n\t" \
  "ds_read_b128 v[192:195], v242 offset:0\n\t" \
  "ds_read_b128 v[196:199], v242 offset:16384\n\t" \
  "ds_read_b128 v[200:203], v243 offset:0\n\t" \
  "ds_read_b128 v[204:207], v243 offset:16384\n\t" \
  "s_add_u32 m0, %[lw], 99328\n\t" \
  "s_nop 0\n\t" \
  "global_load_lds_dwordx4 v249, s[92:93] sc1\n\t" \
  "ds_read_b128 v[208:211], v240 offset:256\n\t" \
  "ds_read_b128 v[212:215], v240 offset:16640\n\t" \
  "ds_read_b128 v[216:219], v241 offset:256\n\t" \
  "ds_read_b128 v[220:223], v241 offset:16640\n\t" \
  "s_add_u32 m0, %[lw], 100352\n\t" \
  "s_nop 0\n\t" \
  "global_load_lds_dwordx4 v250, s[92:93] sc1\n\t" \
  "ds_read_b128 v[224:227], v242 offset:256\n\t" \
  "ds_read_b128 v[228:231], v242 offset:16640\n\t" \
  "ds_read_b128 v[232:235], v243 offset:256\n\t" \
  "ds_read_b128 v[236:239], v243 offset:16640\n\t" \
  "s_add_u32 m0, %[lw], 101376\n\t" \
  "s_nop 0\n\t" \
  "global_load_lds_dwordx4 v251, s[92:93] sc1\n\t" \
  "ds_write_b128 v252, v[152:155] offset:0\n\t" \
  "ds_write_b128 v252, v[156:159] offset:1024\n\t" \
  "s_waitcnt lgkmcnt(0)\n\t" \
  "s_waitcnt vmcnt(4)\n\t" \
  "s_barrier\n\t" \
  "s_setprio 2\n\t" \
  "v_mfma_f32_16x16x32_bf16 v[128:131], v[0:3], v[176:179], 0\n\t" \
  "v_mfma_f32_16x16x32_bf16 v[132:135], v[4:7], v[176:179], 0\n\t" \
  "v_mfma_f32_16x16x32_bf16 v[136:139], v[0:3], v[180:183], 0\n\t" \
  "v_mfma_f32_16x16x32_bf16 v[140:143], v[4:7], v[180:183], 0\n\t" \
  "v_mfma_f32_16x16x32_bf16 v[128:131], v[8:11], v[184:187], v[128:131]\n\t" \
  "v_mfma_f32_16x16x32_bf16 v[132:135], v[12:15], v[184:187], v[132:135]\n\t" \
  "v_mfma_f32_16x16x32_bf16 v[136:139], v[8:11], v[188:191], v[136:139]\n\t" \
  "v_mfma_f32_16x16x32_bf16 v[140:143], v[12:15], v[188:191], v[140:143]\n\t" \
  "v_mfma_f32_16x16x32_bf16 v[128:131], v[16:19], v[192:195], v[128:131]\n\t" \
  "v_mfma_f32_16x16x32_bf16 v[132:135], v[20:23], v[192:195], v[132:135]\n\t" \
  "v_mfma_f32_16x16x32_bf16 v[136:139], v[16:19], v[196:199], v[136:139]\n\t" \
  "v_mfma_f32_16x16x32_bf16 v[140:143], v[20:23], v[196:199], v[140:143]\n\t" \
  "v_mfma_f32_16x16x32_bf16 v[128:131], v[24:27], v[200:203], v[128:131]\n\t" \
  "v_mfma_f32_16x16x32_bf16 v[132:135], v[28:31], v[200:203], v[132:135]\n\t" \
  "v_mfma_f32_16x16x32_bf16 v[136:139], v[24:27], v[204:207], v[136:139]\n\t" \
  "v_mfma_f32_16x16x32_bf16 v[140:143], v[28:31], v[204:207], v[140:143]\n\t" \
  "v_mfma_f32_16x16x32_bf16 v[128:131], v[32:35], v[208:211], v[128:131]\n\t" \
  "v_mfma_f32_16x16x32_bf16 v[132:135], v[36:39], v[208:211], v[132:135]\n\t" \
  "v_mfma_f32_16x16x32_bf16 v[136:139], v[32:35], v[212:215], v[136:139]\n\t" \
  "v_mfma_f32_16x16x32_bf16 v[140:143], v[36:39], v[212:215], v[140:143]\n\t" \
  "v_mfma_f32_16x16x32_bf16 v[128:131], v[40:43], v[216:219], v[128:131]\n\t" \
  "v_mfma_f32_16x16x32_bf16 v[132:135], v[44:47], v[216:219], v[132:135]\n\t" \
  "v_mfma_f32_16x16x32_bf16 v[136:139], v[40:43], v[220:223], v[136:139]\n\t" \
  "v_mfma_f32_16x16x32_bf16 v[140:143], v[44:47], v[220:223], v[140:143]\n\t" \
  "v_mfma_f32_16x16x32_bf16 v[128:131], v[48:51], v[224:227], v[128:131]\n\t" \
  "v_mfma_f32_16x16x32_bf16 v[132:135], v[52:55], v[224:227], v[132:135]\n\t" \
  "v_mfma_f32_16x16x32_bf16 v[136:139], v[48:51], v[228:231], v[136:139]\n\t" \
  "v_mfma_f32_16x16x32_bf16 v[140:143], v[52:55], v[228:231], v[140:143]\n\t" \
  "v_mfma_f32_16x16x32_bf16 v[128:131], v[56:59], v[232:235], v[128:131]\n\t" \
  "v_mfma_f32_16x16x32_bf16 v[132:135], v[60:63], v[232:235], v[132:135]\n\t" \
  "v_mfma_f32_16x16x32_bf16 v[136:139], v[56:59], v[236:239], v[136:139]\n\t" \
  "v_mfma_f32_16x16x32_bf16 v[140:143], v[60:63], v[236:239], v[140:143]\n\t" \
  "s_setprio 0\n\t" \
  "s_add_u32 s92, s92, 65024\n\t" \
  "s_addc_u32 s93, s93, 0\n\t" \
  "ds_read_b128 v[160:163], v253 offset:0\n\t" \
  "ds_read_b128 v[164:167], v253 offset:1024\n\t" \
  "ds_read_b128 v[176:179], v240 offset:32768\n\t" \
  "ds_read_b128 v[180:183], v240 offset:49152\n\t" \
  "ds_read_b128 v[184:187], v241 offset:32768\n\t" \
  "ds_read_b128 v[188:191], v241 offset:49152\n\t" \
  "s_add_u32 m0, %[lw], 0\n\t" \
  "s_nop 0\n\t" \
  "global_load_lds_dwordx4 v248, s[92:93] sc1\n\t" \
  "ds_read_b128 v[192:195], v242 offset:32768\n\t" \
  "ds_read_b128 v[196:199], v242 offset:49152\n\t" \
  "ds_read_b128 v[200:203], v243 offset:32768\n\t" \
  "ds_read_b128 v[204:207], v243 offset:49152\n\t" \
  "s_add_u32 m0, %[lw], 1024\n\t" \
  "s_nop 0\n\t" \
  "global_load_lds_dwordx4 v249, s[92:93] sc1\n\t" \
  "ds_read_b128 v[208:211], v240 offset:33024\n\t" \
  "ds_read_b128 v[212:215], v240 offset:49408\n\t" \
  "ds_read_b128 v[216:219], v241 offset:33024\n\t" \
  "ds_read_b128 v[220:223], v241 offset:49408\n\t" \
  "s_add_u32 m0, %[lw], 2048\n\t" \
  "s_nop 0\n\t" \
  "global_load_lds_dwordx4 v250, s[92:93] sc1\n\t" \
  "ds_read_b128 v[224:227], v242 offset:33024\n\t" \
  "ds_read_b128 v[228:231], v242 offset:49408\n\t" \
  "ds_read_b128 v[232:235], v243 offset:33024\n\t" \
  "ds_read_b128 v[236:239], v243 offset:49408\n\t" \
  "s_add_u32 m0, %[lw], 3072\n\t" \
  "s_nop 0\n\t" \
  "global_load_lds_dwordx4 v251, s[92:93] sc1\n\t" \
  "s_waitcnt lgkmcnt(15)\n\t" \
  "s_waitcnt lgkmcnt(12)\n\t" \
  "s_sub_i32 s91, s86, 2\n\t" \
  "v_mov_b32 v173, s91\n\t" \
  "buffer_store_dword v173, v174, %[rs], 0 offen sc1\n\t" \
  "s_lshr_b32 s90, s87, 7\n\t" \
  "s_xor_b32 s90, s90, %[s0]\n\t" \
  "s_and_b32 s90, s90, 1\n\t" \
  "s_lshl1_add_u32 s90, s87, s90\n\t" \
  "s_lshl_b32 s90, s90, 13\n\t" \
  "v_add_f32 v160, v160, v144\n\t" \
  "v_add_f32 v161, v161, v145\n\t" \
  "v_add_f32 v162, v162, v146\n\t" \
  "v_add_f32 v163, v163, v147\n\t" \
  "v_add_f32 v164, v164, v148\n\t" \
  "v_add_f32 v165, v165, v149\n\t" \
  "v_add_f32 v166, v166, v150\n\t" \
  "v_add_f32 v167, v167, v151\n\t" \
  "v_cvt_pk_bf16_f32 v168, v160, v161\n\t" \
  "v_cvt_pk_bf16_f32 v169, v162, v163\n\t" \
  "v_cvt_pk_bf16_f32 v170, v164, v165\n\t" \
  "v_cvt_pk_bf16_f32 v171, v166, v167\n\t" \
  "buffer_store_dwordx4 v[168:171], v172, %[rt], s90 offen sc1\n\t" \
  "s_add_u32 s86, s86, 1\n\t" \
  "s_add_u32 s87, s87, 1\n\t" \
  "s_waitcnt lgkmcnt(0)\n\t" \
  "s_waitcnt vmcnt(10)\n\t" \
  "s_barrier\n\t" \
  "s_setprio 2\n\t" \
  "v_mfma_f32_16x16x32_bf16 v[128:131], v[64:67], v[176:179], v[128:131]\n\t" \
  "v_mfma_f32_16x16x32_bf16 v[132:135], v[68:71], v[176:179], v[132:135]\n\t" \
  "v_mfma_f32_16x16x32_bf16 v[136:139], v[64:67], v[180:183], v[136:139]\n\t" \
  "v_mfma_f32_16x16x32_bf16 v[140:143], v[68:71], v[180:183], v[140:143]\n\t" \
  "v_mfma_f32_16x16x32_bf16 v[128:131], v[72:75], v[184:187], v[128:131]\n\t" \
  "v_mfma_f32_16x16x32_bf16 v[132:135], v[76:79], v[184:187], v[132:135]\n\t" \
  "v_mfma_f32_16x16x32_bf16 v[136:139], v[72:75], v[188:191], v[136:139]\n\t" \
  "v_mfma_f32_16x16x32_bf16 v[140:143], v[76:79], v[188:191], v[140:143]\n\t" \
  "v_mfma_f32_16x16x32_bf16 v[128:131], v[80:83], v[192:195], v[128:131]\n\t" \
  "v_mfma_f32_16x16x32_bf16 v[132:135], v[84:87], v[192:195], v[132:135]\n\t" \
  "v_mfma_f32_16x16x32_bf16 v[136:139], v[80:83], v[196:199], v[136:139]\n\t" \
  "v_mfma_f32_16x16x32_bf16 v[140:143], v[84:87], v[196:199], v[140:143]\n\t" \
  "v_mfma_f32_16x16x32_bf16 v[128:131], v[88:91], v[200:203], v[128:131]\n\t" \
  "v_mfma_f32_16x16x32_bf16 v[132:135], v[92:95], v[200:203], v[132:135]\n\t" \
  "v_mfma_f32_16x16x32_bf16 v[136:139], v[88:91], v[204:207], v[136:139]\n\t" \
  "v_mfma_f32_16x16x32_bf16 v[140:143], v[92:95], v[204:207], v[140:143]\n\t" \
  "v_mfma_f32_16x16x32_bf16 v[128:131], v[96:99], v[208:211], v[128:131]\n\t" \
  "v_mfma_f32_16x16x32_bf16 v[132:135], v[100:103], v[208:211], v[132:135]\n\t" \
  "v_mfma_f32_16x16x32_bf16 v[136:139], v[96:99], v[212:215], v[136:139]\n\t" \
  "v_mfma_f32_16x16x32_bf16 v[140:143], v[100:103], v[212:215], v[140:143]\n\t" \
  "v_mfma_f32_16x16x32_bf16 v[128:131], v[104:107], v[216:219], v[128:131]\n\t" \
  "v_mfma_f32_16x16x32_bf16 v[132:135], v[108:111], v[216:219], v[132:135]\n\t" \
  "v_mfma_f32_16x16x32_bf16 v[136:139], v[104:107], v[220:223], v[136:139]\n\t" \
  "v_mfma_f32_16x16x32_bf16 v[140:143], v[108:111], v[220:223], v[140:143]\n\t" \
  "v_mfma_f32_16x16x32_bf16 v[128:131], v[112:115], v[224:227], v[128:131]\n\t" \
  "v_mfma_f32_16x16x32_bf16 v[132:135], v[116:119], v[224:227], v[132:135]\n\t" \
  "v_mfma_f32_16x16x32_bf16 v[136:139], v[112:115], v[228:231], v[136:139]\n\t" \
  "v_mfma_f32_16x16x32_bf16 v[140:143], v[116:119], v[228:231], v[140:143]\n\t" \
  "v_mfma_f32_16x16x32_bf16 v[128:131], v[120:123], v[232:235], v[128:131]\n\t" \
  "v_mfma_f32_16x16x32_bf16 v[132:135], v[124:127], v[232:235], v[132:135]\n\t" \
  "v_mfma_f32_16x16x32_bf16 v[136:139], v[120:123], v[236:239], v[136:139]\n\t" \
  "v_mfma_f32_16x16x32_bf16 v[140:143], v[124:127], v[236:239], v[140:143]\n\t" \
  "s_setprio 0\n\t" \
  "s_add_u32 s92, s92, 512\n\t" \
  "s_addc_u32 s93, s93, 0\n\t" \
  "ds_read_b128 v[176:179], v244 offset:0\n\t" \
  "ds_read_b128 v[180:183], v244 offset:16384\n\t" \
  "ds_read_b128 v[184:187], v245 offset:0\n\t" \
  "ds_read_b128 v[188:191], v245 offset:16384\n\t" \
  "s_add_u32 m0, %[lw], 32768\n\t" \
  "s_nop 0\n\t" \
  "global_load_lds_dwordx4 v248, s[92:93] sc1\n\t" \
  "ds_read_b128 v[192:195], v246 offset:0\n\t" \
  "ds_read_b128 v[196:199], v246 offset:16384\n\t" \
  "ds_read_b128 v[200:203], v247 offset:0\n\t" \
  "ds_read_b128 v[204:207], v247 offset:16384\n\t" \
  "s_add_u32 m0, %[lw], 33792\n\t" \
  "s_nop 0\n\t" \
  "global_load_lds_dwordx4 v249, s[92:93] sc1\n\t" \
  "ds_read_b128 v[208:211], v244 offset:256\n\t" \
  "ds_read_b128 v[212:215], v244 offset:16640\n\t" \
  "ds_read_b128 v[216:219], v245 offset:256\n\t" \
  "ds_read_b128 v[220:223], v245 offset:16640\n\t" \
  "s_add_u32 m0, %[lw], 34816\n\t" \
  "s_nop 0\n\t" \
  "global_load_lds_dwordx4 v250, s[92:93] sc1\n\t" \
  "ds_read_b128 v[224:227], v246 offset:256\n\t" \
  "ds_read_b128 v[228:231], v246 offset:16640\n\t" \
  "ds_read_b128 v[232:235], v247 offset:256\n\t" \
  "ds_read_b128 v[236:239], v247 offset:16640\n\t" \
  "s_add_u32 m0, %[lw], 35840\n\t" \
  "s_nop 0\n\t" \
  "global_load_lds_dwordx4 v251, s[92:93] sc1\n\t" \
  "ds_write_b128 v252, v[136:139] offset:0\n\t" \
  "ds_write_b128 v252, v[140:143] offset:1024\n\t" \
  "s_waitcnt lgkmcnt(0)\n\t" \
  "s_waitcnt vmcnt(4)\n\t" \
  "s_barrier\n\t" \
  "s_setprio 2\n\t" \
  "v_mfma_f32_16x16x32_bf16 v[144:147], v[0:3], v[176:179], 0\n\t" \
  "v_mfma_f32_16x16x32_bf16 v[148:151], v[4:7], v[176:179], 0\n\t" \
  "v_mfma_f32_16x16x32_bf16 v[152:155], v[0:3], v[180:183], 0\n\t" \
  "v_mfma_f32_16x16x32_bf16 v[156:159], v[4:7], v[180:183], 0\n\t" \
  "v_mfma_f32_16x16x32_bf16 v[144:147], v[8:11], v[184:187], v[144:147]\n\t" \
  "v_mfma_f32_16x16x32_bf16 v[148:151], v[12:15], v[184:187], v[148:151]\n\t" \
  "v_mfma_f32_16x16x32_bf16 v[152:155], v[8:11], v[188:191], v[152:155]\n\t" \
  "v_mfma_f32_16x16x32_bf16 v[156:159], v[12:15], v[188:191], v[156:159]\n\t" \
  "v_mfma_f32_16x16x32_bf16 v[144:147], v[16:19], v[192:195], v[144:147]\n\t" \
  "v_mfma_f32_16x16x32_bf16 v[148:151], v[20:23], v[192:195], v[148:151]\n\t" \
  "v_mfma_f32_16x16x32_bf16 v[152:155], v[16:19], v[196:199], v[152:155]\n\t" \
  "v_mfma_f32_16x16x32_bf16 v[156:159], v[20:23], v[196:199], v[156:159]\n\t" \
  "v_mfma_f32_16x16x32_bf16 v[144:147], v[24:27], v[200:203], v[144:147]\n\t" \
  "v_mfma_f32_16x16x32_bf16 v[148:151], v[28:31], v[200:203], v[148:151]\n\t" \
  "v_mfma_f32_16x16x32_bf16 v[152:155], v[24:27], v[204:207], v[152:155]\n\t" \
  "v_mfma_f32_16x16x32_bf16 v[156:159], v[28:31], v[204:207], v[156:159]\n\t" \
  "v_mfma_f32_16x16x32_bf16 v[144:147], v[32:35], v[208:211], v[144:147]\n\t" \
  "v_mfma_f32_16x16x32_bf16 v[148:151], v[36:39], v[208:211], v[148:151]\n\t" \
  "v_mfma_f32_16x16x32_bf16 v[152:155], v[32:35], v[212:215], v[152:155]\n\t" \
  "v_mfma_f32_16x16x32_bf16 v[156:159], v[36:39], v[212:215], v[156:159]\n\t" \
  "v_mfma_f32_16x16x32_bf16 v[144:147], v[40:43], v[216:219], v[144:147]\n\t" \
  "v_mfma_f32_16x16x32_bf16 v[148:151], v[44:47], v[216:219], v[148:151]\n\t" \
  "v_mfma_f32_16x16x32_bf16 v[152:155], v[40:43], v[220:223], v[152:155]\n\t" \
  "v_mfma_f32_16x16x32_bf16 v[156:159], v[44:47], v[220:223], v[156:159]\n\t" \
  "v_mfma_f32_16x16x32_bf16 v[144:147], v[48:51], v[224:227], v[144:147]\n\t" \
  "v_mfma_f32_16x16x32_bf16 v[148:151], v[52:55], v[224:227], v[148:151]\n\t" \
  "v_mfma_f32_16x16x32_bf16 v[152:155], v[48:51], v[228:231], v[152:155]\n\t" \
  "v_mfma_f32_16x16x32_bf16 v[156:159], v[52:55], v[228:231], v[156:159]\n\t" \
  "v_mfma_f32_16x16x32_bf16 v[144:147], v[56:59], v[232:235], v[144:147]\n\t" \
  "v_mfma_f32_16x16x32_bf16 v[148:151], v[60:63], v[232:235], v[148:151]\n\t" \
  "v_mfma_f32_16x16x32_bf16 v[152:155], v[56:59], v[236:239], v[152:155]\n\t" \
  "v_mfma_f32_16x16x32_bf16 v[156:159], v[60:63], v[236:239], v[156:159]\n\t" \
  "s_setprio 0\n\t" \
  "s_add_u32 s92, s92, 65024\n\t" \
  "s_addc_u32 s93, s93, 0\n\t" \
  "ds_read_b128 v[160:163], v253 offset:0\n\t" \
  "ds_read_b128 v[164:167], v253 offset:1024\n\t" \
  "ds_read_b128 v[176:179], v244 offset:32768\n\t" \
  "ds_read_b128 v[180:183], v244 offset:49152\n\t" \
  "ds_read_b128 v[184:187], v245 offset:32768\n\t" \
  "ds_read_b128 v[188:191], v245 offset:49152\n\t" \
  "s_add_u32 m0, %[lw], 65536\n\t" \
  "s_nop 0\n\t" \
  "global_load_lds_dwordx4 v248, s[92:93] sc1\n\t" \
  "ds_read_b128 v[192:195], v246 offset:32768\n\t" \
  "ds_read_b128 v[196:199], v246 offset:49152\n\t" \
  "ds_read_b128 v[200:203], v247 offset:32768\n\t" \
  "ds_read_b128 v[204:207], v247 offset:49152\n\t" \
  "s_add_u32 m0, %[lw], 66560\n\t" \
  "s_nop 0\n\t" \
  "global_load_lds_dwordx4 v249, s[92:93] sc1\n\t" \
  "ds_read_b128 v[208:211], v244 offset:33024\n\t" \
  "ds_read_b128 v[212:215], v244 offset:49408\n\t" \
  "ds_read_b128 v[216:219], v245 offset:33024\n\t" \
  "ds_read_b128 v[220:223], v245 offset:49408\n\t" \
  "s_add_u32 m0, %[lw], 67584\n\t" \
  "s_nop 0\n\t" \
  "global_load_lds_dwordx4 v250, s[92:93] sc1\n\t" \
  "ds_read_b128 v[224:227], v246 offset:33024\n\t" \
  "ds_read_b128 v[228:231], v246 offset:49408\n\t" \
  "ds_read_b128 v[232:235], v247 offset:33024\n\t" \
  "ds_read_b128 v[236:239], v247 offset:49408\n\t" \
  "s_add_u32 m0, %[lw], 68608\n\t" \
  "s_nop 0\n\t" \
  "global_load_lds_dwordx4 v251, s[92:93] sc1\n\t" \
  "s_waitcnt lgkmcnt(15)\n\t" \
  "s_waitcnt lgkmcnt(12)\n\t" \
  "s_sub_i32 s91, s86, 2\n\t" \
  "v_mov_b32 v173, s91\n\t" \
  "buffer_store_dword v173, v174, %[rs], 0 offen sc1\n\t" \
  "s_lshr_b32 s90, s87, 7\n\t" \
  "s_xor_b32 s90, s90, %[s0]\n\t" \
  "s_and_b32 s90, s90, 1\n\t" \
  "s_lshl1_add_u32 s90, s87, s90\n\t" \
  "s_lshl_b32 s90, s90, 13\n\t" \
  "v_add_f32 v160, v160, v128\n\t" \
  "v_add_f32 v161, v161, v129\n\t" \
  "v_add_f32 v162, v162, v130\n\t" \
  "v_add_f32 v163, v163, v131\n\t" \
  "v_add_f32 v164, v164, v132\n\t" \
  "v_add_f32 v165, v165, v133\n\t" \
  "v_add_f32 v166, v166, v134\n\t" \
  "v_add_f32 v167, v167, v135\n\t" \
  "v_cvt_pk_bf16_f32 v168, v160, v161\n\t" \
  "v_cvt_pk_bf16_f32 v169, v162, v163\n\t" \
  "v_cvt_pk_bf16_f32 v170, v164, v165\n\t" \
  "v_cvt_pk_bf16_f32 v171, v166, v167\n\t" \
  "buffer_store_dwordx4 v[168:171], v172, %[rt], s90 offen sc1\n\t" \
  "s_add_u32 s86, s86, 1\n\t" \
  "s_add_u32 s87, s87, 1\n\t" \
  "s_waitcnt lgkmcnt(0)\n\t" \
  "s_cmp_lt_u32 s86, 64\n\t" \
  "s_cbranch_scc1 .Lp1%=\n\t" \
  "s_barrier\n\t" \
  "s_setprio 2\n\t" \
  "v_mfma_f32_16x16x32_bf16 v[144:147], v[64:67], v[176:179], v[144:147]\n\t" \
  "v_mfma_f32_16x16x32_bf16 v[148:151], v[68:71], v[176:179], v[148:151]\n\t" \
  "v_mfma_f32_16x16x32_bf16 v[152:155], v[64:67], v[180:183], v[152:155]\n\t" \
  "v_mfma_f32_16x16x32_bf16 v[156:159], v[68:71], v[180:183], v[156:159]\n\t" \
  "v_mfma_f32_16x16x32_bf16 v[144:147], v[72:75], v[184:187], v[144:147]\n\t" \
  "v_mfma_f32_16x16x32_bf16 v[148:151], v[76:79], v[184:187], v[148:151]\n\t" \
  "v_mfma_f32_16x16x32_bf16 v[152:155], v[72:75], v[188:191], v[152:155]\n\t" \
  "v_mfma_f32_16x16x32_bf16 v[156:159], v[76:79], v[188:191], v[156:159]\n\t" \
  "v_mfma_f32_16x16x32_bf16 v[144:147], v[80:83], v[192:195], v[144:147]\n\t" \
  "v_mfma_f32_16x16x32_bf16 v[148:151], v[84:87], v[192:195], v[148:151]\n\t" \
  "v_mfma_f32_16x16x32_bf16 v[152:155], v[80:83], v[196:199], v[152:155]\n\t" \
  "v_mfma_f32_16x16x32_bf16 v[156:159], v[84:87], v[196:199], v[156:159]\n\t" \
  "v_mfma_f32_16x16x32_bf16 v[144:147], v[88:91], v[200:203], v[144:147]\n\t" \
  "v_mfma_f32_16x16x32_bf16 v[148:151], v[92:95], v[200:203], v[148:151]\n\t" \
  "v_mfma_f32_16x16x32_bf16 v[152:155], v[88:91], v[204:207], v[152:155]\n\t" \
  "v_mfma_f32_16x16x32_bf16 v[156:159], v[92:95], v[204:207], v[156:159]\n\t" \
  "v_mfma_f32_16x16x32_bf16 v[144:147], v[96:99], v[208:211], v[144:147]\n\t" \
  "v_mfma_f32_16x16x32_bf16 v[148:151], v[100:103], v[208:211], v[148:151]\n\t" \
  "v_mfma_f32_16x16x32_bf16 v[152:155], v[96:99], v[212:215], v[152:155]\n\t" \
  "v_mfma_f32_16x16x32_bf16 v[156:159], v[100:103], v[212:215], v[156:159]\n\t" \
  "v_mfma_f32_16x16x32_bf16 v[144:147], v[104:107], v[216:219], v[144:147]\n\t" \
  "v_mfma_f32_16x16x32_bf16 v[148:151], v[108:111], v[216:219], v[148:151]\n\t" \
  "v_mfma_f32_16x16x32_bf16 v[152:155], v[104:107], v[220:223], v[152:155]\n\t" \
  "v_mfma_f32_16x16x32_bf16 v[156:159], v[108:111], v[220:223], v[156:159]\n\t" \
  "v_mfma_f32_16x16x32_bf16 v[144:147], v[112:115], v[224:227], v[144:147]\n\t" \
  "v_mfma_f32_16x16x32_bf16 v[148:151], v[116:119], v[224:227], v[148:151]\n\t" \
  "v_mfma_f32_16x16x32_bf16 v[152:155], v[112:115], v[228:231], v[152:155]\n\t" \
  "v_mfma_f32_16x16x32_bf16 v[156:159], v[116:119], v[228:231], v[156:159]\n\t" \
  "v_mfma_f32_16x16x32_bf16 v[144:147], v[120:123], v[232:235], v[144:147]\n\t" \
  "v_mfma_f32_16x16x32_bf16 v[148:151], v[124:127], v[232:235], v[148:151]\n\t" \
  "v_mfma_f32_16x16x32_bf16 v[152:155], v[120:123], v[236:239], v[152:155]\n\t" \
  "v_mfma_f32_16x16x32_bf16 v[156:159], v[124:127], v[236:239], v[156:159]\n\t" \
  "s_setprio 0\n\t" \
  "s_nop 5\n\t" \
  "ds_write_b128 v252, v[152:155] offset:0\n\t" \
  "ds_write_b128 v252, v[156:159] offset:1024\n\t" \
  "s_waitcnt lgkmcnt(0)\n\t" \
  "s_barrier\n\t" \
  "ds_read_b128 v[160:163], v253 offset:0\n\t" \
  "ds_read_b128 v[164:167], v253 offset:1024\n\t" \
  "s_waitcnt lgkmcnt(0)\n\t" \
  "s_lshr_b32 s90, s87, 7\n\t" \
  "s_xor_b32 s90, s90, %[s0]\n\t" \
  "s_and_b32 s90, s90, 1\n\t" \
  "s_lshl1_add_u32 s90, s87, s90\n\t" \
  "s_lshl_b32 s90, s90, 13\n\t" \
  "v_add_f32 v160, v160, v144\n\t" \
  "v_add_f32 v161, v161, v145\n\t" \
  "v_add_f32 v162, v162, v146\n\t" \
  "v_add_f32 v163, v163, v147\n\t" \
  "v_add_f32 v164, v164, v148\n\t" \
  "v_add_f32 v165, v165, v149\n\t" \
  "v_add_f32 v166, v166, v150\n\t" \
  "v_add_f32 v167, v167, v151\n\t" \
  "v_cvt_pk_bf16_f32 v168, v160, v161\n\t" \
  "v_cvt_pk_bf16_f32 v169, v162, v163\n\t" \
  "v_cvt_pk_bf16_f32 v170, v164, v165\n\t" \
  "v_cvt_pk_bf16_f32 v171, v166, v167\n\t" \
  "buffer_store_dwordx4 v[168:171], v172, %[rt], s90 offen sc1\n\t" \
  "s_waitcnt vmcnt(0)\n\t" \
  "s_barrier\n\t" \
  "s_sub_i32 s91, s86, 1\n\t" \
  "v_mov_b32 v173, s91\n\t" \
  "buffer_store_dword v173, v174, %[rs], 0 offen sc1\n\t" \
  "s_mov_b32 m0, s94\n\t" \
  ".Le%=:\n\t"
#define PP_ASM_1 \
  "s_cmp_lg_u32 %[kh], 0\n\t" \
  "s_cbranch_scc1 .Lk%=\n\t" \
  "s_mov_b32 s94, m0\n\t" \
  "s_mov_b64 s[92:93], %[x0]\n\t" \
  "s_mov_b32 s86, %[g0]\n\t" \
  "s_add_u32 s87, s86, %[t0]\n\t" \
  "s_add_u32 m0, %[lw], 67584\n\t" \
  "s_nop 0\n\t" \
  "global_load_lds_dwordx4 v250, s[92:93] sc1\n\t" \
  "s_add_u32 m0, %[lw], 68608\n\t" \
  "s_nop 0\n\t" \
  "global_load_lds_dwordx4 v251, s[92:93] sc1\n\t" \
  "s_barrier\n\t" \
  "s_branch .Ls0%=\n\t" \
  ".Lp0%=:\n\t" \
  "s_waitcnt vmcnt(9)\n\t" \
  "s_barrier\n\t" \
  ".Ls0%=:\n\t" \
  "s_add_u32 s92, s92, 512\n\t" \
  "s_addc_u32 s93, s93, 0\n\t" \
  "ds_read_b128 v[176:179], v240 offset:0\n\t" \
  "ds_read_b128 v[180:183], v240 offset:16384\n\t" \
  "ds_read_b128 v[184:187], v241 offset:0\n\t" \
  "ds_read_b128 v[188:191], v241 offset:16384\n\t" \
  "s_add_u32 m0, %[lw], 98304\n\t" \
  "s_nop 0\n\t" \
  "global_load_lds_dwordx4 v248, s[92:93] sc1\n\t" \
  "ds_read_b128 v[192:195], v242 offset:0\n\t" \
  "ds_read_b128 v[196:199], v242 offset:16384\n\t" \
  "ds_read_b128 v[200:203], v243 offset:0\n\t" \
  "ds_read_b128 v[204:207], v243 offset:16384\n\t" \
  "s_add_u32 m0, %[lw], 99328\n\t" \
  "s_nop 0\n\t" \
  "global_load_lds_dwordx4 v249, s[92:93] sc1\n\t" \
  "ds_read_b128 v[208:211], v240 offset:256\n\t" \
  "ds_read_b128 v[212:215], v240 offset:16640\n\t" \
  "ds_read_b128 v[216:219], v241 offset:256\n\t" \
  "ds_read_b128 v[220:223], v241 offset:16640\n\t" \
  "s_add_u32 m0, %[lw], 100352\n\t" \
  "s_nop 0\n\t" \
  "global_load_lds_dwordx4 v250, s[92:93] sc1\n\t" \
  "ds_read_b128 v[224:227], v242 offset:256\n\t" \
  "ds_read_b128 v[228:231], v242 offset:16640\n\t" \
  "ds_read_b128 v[232:235], v243 offset:256\n\t" \
  "ds_read_b128 v[236:239], v243 offset:16640\n\t" \
  "s_add_u32 m0, %[lw], 101376\n\t" \
  "s_nop 0\n\t" \
  "global_load_lds_dwordx4 v251, s[92:93] sc1\n\t" \
  "ds_write_b128 v252, v[152:155] offset:0\n\t" \
  "ds_write_b128 v252, v[156:159] offset:1024\n\t" \
  "s_waitcnt lgkmcnt(0)\n\t" \
  "s_setprio 1\n\t" \
  "v_mfma_f32_16x16x32_bf16 v[128:131], v[176:179], v[0:3], 0\n\t" \
  "v_mfma_f32_16x16x32_bf16 v[136:139], v[176:179], v[4:7], 0\n\t" \
  "v_mfma_f32_16x16x32_bf16 v[132:135], v[180:183], v[0:3], 0\n\t" \
  "v_mfma_f32_16x16x32_bf16 v[140:143], v[180:183], v[4:7], 0\n\t" \
  "v_mfma_f32_16x16x32_bf16 v[128:131], v[184:187], v[8:11], v[128:131]\n\t" \
  "v_mfma_f32_16x16x32_bf16 v[136:139], v[184:187], v[12:15], v[136:139]\n\t" \
  "v_mfma_f32_16x16x32_bf16 v[132:135], v[188:191], v[8:11], v[132:135]\n\t" \
  "v_mfma_f32_16x16x32_bf16 v[140:143], v[188:191], v[12:15], v[140:143]\n\t" \
  "v_mfma_f32_16x16x32_bf16 v[128:131], v[192:195], v[16:19], v[128:131]\n\t" \
  "v_mfma_f32_16x16x32_bf16 v[136:139], v[192:195], v[20:23], v[136:139]\n\t" \
  "v_mfma_f32_16x16x32_bf16 v[132:135], v[196:199], v[16:19], v[132:135]\n\t" \
  "v_mfma_f32_16x16x32_bf16 v[140:143], v[196:199], v[20:23], v[140:143]\n\t" \
  "v_mfma_f32_16x16x32_bf16 v[128:131], v[200:203], v[24:27], v[128:131]\n\t" \
  "v_mfma_f32_16x16x32_bf16 v[136:139], v[200:203], v[28:31], v[136:139]\n\t" \
  "v_mfma_f32_16x16x32_bf16 v[132:135], v[204:207], v[24:27], v[132:135]\n\t" \
  "v_mfma_f32_16x16x32_bf16 v[140:143], v[204:207], v[28:31], v[140:143]\n\t" \
  "v_mfma_f32_16x16x32_bf16 v[128:131], v[208:211], v[32:35], v[128:131]\n\t" \
  "v_mfma_f32_16x16x32_bf16 v[136:139], v[208:211], v[36:39], v[136:139]\n\t" \
  "v_mfma_f32_16x16x32_bf16 v[132:135], v[212:215], v[32:35], v[132:135]\n\t" \
  "v_mfma_f32_16x16x32_bf16 v[140:143], v[212:215], v[36:39], v[140:143]\n\t" \
  "v_mfma_f32_16x16x32_bf16 v[128:131], v[216:219], v[40:43], v[128:131]\n\t" \
  "v_mfma_f32_16x16x32_bf16 v[136:139], v[216:219], v[44:47], v[136:139]\n\t" \
  "v_mfma_f32_16x16x32_bf16 v[132:135], v[220:223], v[40:43], v[132:135]\n\t" \
  "v_mfma_f32_16x16x32_bf16 v[140:143], v[220:223], v[44:47], v[140:143]\n\t" \
  "v_mfma_f32_16x16x32_bf16 v[128:131], v[224:227], v[48:51], v[128:131]\n\t" \
  "v_mfma_f32_16x16x32_bf16 v[136:139], v[224:227], v[52:55], v[136:139]\n\t" \
  "v_mfma_f32_16x16x32_bf16 v[132:135], v[228:231], v[48:51], v[132:135]\n\t" \
  "v_mfma_f32_16x16x32_bf16 v[140:143], v[228:231], v[52:55], v[140:143]\n\t" \
  "v_mfma_f32_16x16x32_bf16 v[128:131], v[232:235], v[56:59], v[128:131]\n\t" \
  "v_mfma_f32_16x16x32_bf16 v[136:139], v[232:235], v[60:63], v[136:139]\n\t" \
  "v_mfma_f32_16x16x32_bf16 v[132:135], v[236:239], v[56:59], v[132:135]\n\t" \
  "v_mfma_f32_16x16x32_bf16 v[140:143], v[236:239], v[60:63], v[140:143]\n\t" \
  "s_setprio 0\n\t" \
  "s_waitcnt vmcnt(4)\n\t" \
  "s_barrier\n\t" \
  "s_add_u32 s92, s92, 65024\n\t" \
  "s_addc_u32 s93, s93, 0\n\t" \
  "ds_read_b128 v[160:163], v253 offset:0\n\t" \
  "ds_read_b128 v[164:167], v253 offset:1024\n\t" \
  "ds_read_b128 v[176:179], v240 offset:32768\n\t" \
  "ds_read_b128 v[180:183], v240 offset:49152\n\t" \
  "ds_read_b128 v[184:187], v241 offset:32768\n\t" \
  "ds_read_b128 v[188:191], v241 offset:49152\n\t" \
  "s_add_u32 m0, %[lw], 0\n\t" \
  "s_nop 0\n\t" \
  "global_load_lds_dwordx4 v248, s[92:93] sc1\n\t" \
  "ds_read_b128 v[192:195], v242 offset:32768\n\t" \
  "ds_read_b128 v[196:199], v242 offset:49152\n\t" \
  "ds_read_b128 v[200:203], v243 offset:32768\n\t" \
  "ds_read_b128 v[204:207], v243 offset:49152\n\t" \
  "s_add_u32 m0, %[lw], 1024\n\t" \
  "s_nop 0\n\t" \
  "global_load_lds_dwordx4 v249, s[92:93] sc1\n\t" \
  "ds_read_b128 v[208:211], v240 offset:33024\n\t" \
  "ds_read_b128 v[212:215], v240 offset:49408\n\t" \
  "ds_read_b128 v[216:219], v241 offset:33024\n\t" \
  "ds_read_b128 v[220:223], v241 offset:49408\n\t" \
  "s_add_u32 m0, %[lw], 2048\n\t" \
  "s_nop 0\n\t" \
  "global_load_lds_dwordx4 v250, s[92:93] sc1\n\t" \
  "ds_read_b128 v[224:227], v242 offset:33024\n\t" \
  "ds_read_b128 v[228:231], v242 offset:49408\n\t" \
  "ds_read_b128 v[232:235], v243 offset:33024\n\t" \
  "ds_read_b128 v[236:239], v243 offset:49408\n\t" \
  "s_add_u32 m0, %[lw], 3072\n\t" \
  "s_nop 0\n\t" \
  "global_load_lds_dwordx4 v251, s[92:93] sc1\n\t" \
  "s_waitcnt lgkmcnt(15)\n\t" \
  "s_waitcnt lgkmcnt(12)\n\t" \
  "s_lshr_b32 s90, s87, 7\n\t" \
  "s_xor_b32 s90, s90, %[s0]\n\t" \
  "s_and_b32 s90, s90, 1\n\t" \
  "s_lshl1_add_u32 s90, s87, s90\n\t" \
  "s_lshl_b32 s90, s90, 13\n\t" \
  "v_add_f32 v160, v160, v144\n\t" \
  "v_add_f32 v161, v161, v145\n\t" \
  "v_add_f32 v162, v162, v146\n\t" \
  "v_add_f32 v163, v163, v147\n\t" \
  "v_add_f32 v164, v164, v148\n\t" \
  "v_add_f32 v165, v165, v149\n\t" \
  "v_add_f32 v166, v166, v150\n\t" \
  "v_add_f32 v167, v167, v151\n\t" \
  "v_cvt_pk_bf16_f32 v168, v160, v161\n\t" \
  "v_cvt_pk_bf16_f32 v169, v162, v163\n\t" \
  "v_cvt_pk_bf16_f32 v170, v164, v165\n\t" \
  "v_cvt_pk_bf16_f32 v171, v166, v167\n\t" \
  "buffer_store_dwordx4 v[168:171], v172, %[rt], s90 offen sc1\n\t" \
  "s_add_u32 s86, s86, 1\n\t" \
  "s_add_u32 s87, s87, 1\n\t" \
  "s_waitcnt lgkmcnt(0)\n\t" \
  "s_setprio 1\n\t" \
  "v_mfma_f32_16x16x32_bf16 v[128:131], v[176:179], v[64:67], v[128:131]\n\t" \
  "v_mfma_f32_16x16x32_bf16 v[136:139], v[176:179], v[68:71], v[136:139]\n\t" \
  "v_mfma_f32_16x16x32_bf16 v[132:135], v[180:183], v[64:67], v[132:135]\n\t" \
  "v_mfma_f32_16x16x32_bf16 v[140:143], v[180:183], v[68:71], v[140:143]\n\t" \
  "v_mfma_f32_16x16x32_bf16 v[128:131], v[184:187], v[72:75], v[128:131]\n\t" \
  "v_mfma_f32_16x16x32_bf16 v[136:139], v[184:187], v[76:79], v[136:139]\n\t" \
  "v_mfma_f32_16x16x32_bf16 v[132:135], v[188:191], v[72:75], v[132:135]\n\t" \
  "v_mfma_f32_16x16x32_bf16 v[140:143], v[188:191], v[76:79], v[140:143]\n\t" \
  "v_mfma_f32_16x16x32_bf16 v[128:131], v[192:195], v[80:83], v[128:131]\n\t" \
  "v_mfma_f32_16x16x32_bf16 v[136:139], v[192:195], v[84:87], v[136:139]\n\t" \
  "v_mfma_f32_16x16x32_bf16 v[132:135], v[196:199], v[80:83], v[132:135]\n\t" \
  "v_mfma_f32_16x16x32_bf16 v[140:143], v[196:199], v[84:87], v[140:143]\n\t" \
  "v_mfma_f32_16x16x32_bf16 v[128:131], v[200:203], v[88:91], v[128:131]\n\t" \
  "v_mfma_f32_16x16x32_bf16 v[136:139], v[200:203], v[92:95], v[136:139]\n\t" \
  "v_mfma_f32_16x16x32_bf16 v[132:135], v[204:207], v[88:91], v[132:135]\n\t" \
  "v_mfma_f32_16x16x32_bf16 v[140:143], v[204:207], v[92:95], v[140:143]\n\t" \
  "v_mfma_f32_16x16x32_bf16 v[128:131], v[208:211], v[96:99], v[128:131]\n\t" \
  "v_mfma_f32_16x16x32_bf16 v[136:139], v[208:211], v[100:103], v[136:139]\n\t" \
  "v_mfma_f32_16x16x32_bf16 v[132:135], v[212:215], v[96:99], v[132:135]\n\t" \
  "v_mfma_f32_16x16x32_bf16 v[140:143], v[212:215], v[100:103], v[140:143]\n\t" \
  "v_mfma_f32_16x16x32_bf16 v[128:131], v[216:219], v[104:107], v[128:131]\n\t" \
  "v_mfma_f32_16x16x32_bf16 v[136:139], v[216:219], v[108:111], v[136:139]\n\t" \
  "v_mfma_f32_16x16x32_bf16 v[132:135], v[220:223], v[104:107], v[132:135]\n\t" \
  "v_mfma_f32_16x16x32_bf16 v[140:143], v[220:223], v[108:111], v[140:143]\n\t" \
  "v_mfma_f32_16x16x32_bf16 v[128:131], v[224:227], v[112:115], v[128:131]\n\t" \
  "v_mfma_f32_16x16x32_bf16 v[136:139], v[224:227], v[116:119], v[136:139]\n\t" \
  "v_mfma_f32_16x16x32_bf16 v[132:135], v[228:231], v[112:115], v[132:135]\n\t" \
  "v_mfma_f32_16x16x32_bf16 v[140:143], v[228:231], v[116:119], v[140:143]\n\t" \
  "v_mfma_f32_16x16x32_bf16 v[128:131], v[232:235], v[120:123], v[128:131]\n\t" \
  "v_mfma_f32_16x16x32_bf16 v[136:139], v[232:235], v[124:127], v[136:139]\n\t" \
  "v_mfma_f32_16x16x32_bf16 v[132:135], v[236:239], v[120:123], v[132:135]\n\t" \
  "v_mfma_f32_16x16x32_bf16 v[140:143], v[236:239], v[124:127], v[140:143]\n\t" \
  "s_setprio 0\n\t" \
  "s_waitcnt vmcnt(9)\n\t" \
  "s_barrier\n\t" \
  "s_add_u32 s92, s92, 512\n\t" \
  "s_addc_u32 s93, s93, 0\n\t" \
  "ds_read_b128 v[176:179], v244 offset:0\n\t" \
  "ds_read_b128 v[180:183], v244 offset:16384\n\t" \
  "ds_read_b128 v[184:187], v245 offset:0\n\t" \
  "ds_read_b128 v[188:191], v245 offset:16384\n\t" \
  "s_add_u32 m0, %[lw], 32768\n\t" \
  "s_nop 0\n\t" \
  "global_load_lds_dwordx4 v248, s[92:93] sc1\n\t" \
  "ds_read_b128 v[192:195], v246 offset:0\n\t" \
  "ds_read_b128 v[196:199], v246 offset:16384\n\t" \
  "ds_read_b128 v[200:203], v247 offset:0\n\t" \
  "ds_read_b128 v[204:207], v247 offset:16384\n\t" \
  "s_add_u32 m0, %[lw], 33792\n\t" \
  "s_nop 0\n\t" \
  "global_load_lds_dwordx4 v249, s[92:93] sc1\n\t" \
  "ds_read_b128 v[208:211], v244 offset:256\n\t" \
  "ds_read_b128 v[212:215], v244 offset:16640\n\t" \
  "ds_read_b128 v[216:219], v245 offset:256\n\t" \
  "ds_read_b128 v[220:223], v245 offset:16640\n\t" \
  "s_add_u32 m0, %[lw], 34816\n\t" \
  "s_nop 0\n\t" \
  "global_load_lds_dwordx4 v250, s[92:93] sc1\n\t" \
  "ds_read_b128 v[224:227], v246 offset:256\n\t" \
  "ds_read_b128 v[228:231], v246 offset:16640\n\t" \
  "ds_read_b128 v[232:235], v247 offset:256\n\t" \
  "ds_read_b128 v[236:239], v247 offset:16640\n\t" \
  "s_add_u32 m0, %[lw], 35840\n\t" \
  "s_nop 0\n\t" \
  "global_load_lds_dwordx4 v251, s[92:93] sc1\n\t" \
  "ds_write_b128 v252, v[136:139] offset:0\n\t" \
  "ds_write_b128 v252, v[140:143] offset:1024\n\t" \
  "s_waitcnt lgkmcnt(0)\n\t" \
  "s_setprio 1\n\t" \
  "v_mfma_f32_16x16x32_bf16 v[144:147], v[176:179], v[0:3], 0\n\t" \
  "v_mfma_f32_16x16x32_bf16 v[152:155], v[176:179], v[4:7], 0\n\t" \
  "v_mfma_f32_16x16x32_bf16 v[148:151], v[180:183], v[0:3], 0\n\t" \
  "v_mfma_f32_16x16x32_bf16 v[156:159], v[180:183], v[4:7], 0\n\t" \
  "v_mfma_f32_16x16x32_bf16 v[144:147], v[184:187], v[8:11], v[144:147]\n\t" \
  "v_mfma_f32_16x16x32_bf16 v[152:155], v[184:187], v[12:15], v[152:155]\n\t" \
  "v_mfma_f32_16x16x32_bf16 v[148:151], v[188:191], v[8:11], v[148:151]\n\t" \
  "v_mfma_f32_16x16x32_bf16 v[156:159], v[188:191], v[12:15], v[156:159]\n\t" \
  "v_mfma_f32_16x16x32_bf16 v[144:147], v[192:195], v[16:19], v[144:147]\n\t" \
  "v_mfma_f32_16x16x32_bf16 v[152:155], v[192:195], v[20:23], v[152:155]\n\t" \
  "v_mfma_f32_16x16x32_bf16 v[148:151], v[196:199], v[16:19], v[148:151]\n\t" \
  "v_mfma_f32_16x16x32_bf16 v[156:159], v[196:199], v[20:23], v[156:159]\n\t" \
  "v_mfma_f32_16x16x32_bf16 v[144:147], v[200:203], v[24:27], v[144:147]\n\t" \
  "v_mfma_f32_16x16x32_bf16 v[152:155], v[200:203], v[28:31], v[152:155]\n\t" \
  "v_mfma_f32_16x16x32_bf16 v[148:151], v[204:207], v[24:27], v[148:151]\n\t" \
  "v_mfma_f32_16x16x32_bf16 v[156:159], v[204:207], v[28:31], v[156:159]\n\t" \
  "v_mfma_f32_16x16x32_bf16 v[144:147], v[208:211], v[32:35], v[144:147]\n\t" \
  "v_mfma_f32_16x16x32_bf16 v[152:155], v[208:211], v[36:39], v[152:155]\n\t" \
  "v_mfma_f32_16x16x32_bf16 v[148:151], v[212:215], v[32:35], v[148:151]\n\t" \
  "v_mfma_f32_16x16x32_bf16 v[156:159], v[212:215], v[36:39], v[156:159]\n\t" \
  "v_mfma_f32_16x16x32_bf16 v[144:147], v[216:219], v[40:43], v[144:147]\n\t" \
  "v_mfma_f32_16x16x32_bf16 v[152:155], v[216:219], v[44:47], v[152:155]\n\t" \
  "v_mfma_f32_16x16x32_bf16 v[148:151], v[220:223], v[40:43], v[148:151]\n\t" \
  "v_mfma_f32_16x16x32_bf16 v[156:159], v[220:223], v[44:47], v[156:159]\n\t" \
  "v_mfma_f32_16x16x32_bf16 v[144:147], v[224:227], v[48:51], v[144:147]\n\t" \
  "v_mfma_f32_16x16x32_bf16 v[152:155], v[224:227], v[52:55], v[152:155]\n\t" \
  "v_mfma_f32_16x16x32_bf16 v[148:151], v[228:231], v[48:51], v[148:151]\n\t" \
  "v_mfma_f32_16x16x32_bf16 v[156:159], v[228:231], v[52:55], v[156:159]\n\t" \
  "v_mfma_f32_16x16x32_bf16 v[144:147], v[232:235], v[56:59], v[144:147]\n\t" \
  "v_mfma_f32_16x16x32_bf16 v[152:155], v[232:235], v[60:63], v[152:155]\n\t" \
  "v_mfma_f32_16x16x32_bf16 v[148:151], v[236:239], v[56:59], v[148:151]\n\t" \
  "v_mfma_f32_16x16x32_bf16 v[156:159], v[236:239], v[60:63], v[156:159]\n\t" \
  "s_setprio 0\n\t" \
  "s_waitcnt vmcnt(4)\n\t" \
  "s_barrier\n\t" \
  "s_add_u32 s92, s92, 65024\n\t" \
  "s_addc_u32 s93, s93, 0\n\t" \
  "ds_read_b128 v[160:163], v253 offset:0\n\t" \
  "ds_read_b128 v[164:167], v253 offset:1024\n\t" \
  "ds_read_b128 v[176:179], v244 offset:32768\n\t" \
  "ds_read_b128 v[180:183], v244 offset:49152\n\t" \
  "ds_read_b128 v[184:187], v245 offset:32768\n\t" \
  "ds_read_b128 v[188:191], v245 offset:49152\n\t" \
  "s_add_u32 m0, %[lw], 65536\n\t" \
  "s_nop 0\n\t" \
  "global_load_lds_dwordx4 v248, s[92:93] sc1\n\t" \
  "ds_read_b128 v[192:195], v246 offset:32768\n\t" \
  "ds_read_b128 v[196:199], v246 offset:49152\n\t" \
  "ds_read_b128 v[200:203], v247 offset:32768\n\t" \
  "ds_read_b128 v[204:207], v247 offset:49152\n\t" \
  "s_add_u32 m0, %[lw], 66560\n\t" \
  "s_nop 0\n\t" \
  "global_load_lds_dwordx4 v249, s[92:93] sc1\n\t" \
  "ds_read_b128 v[208:211], v244 offset:33024\n\t" \
  "ds_read_b128 v[212:215], v244 offset:49408\n\t" \
  "ds_read_b128 v[216:219], v245 offset:33024\n\t" \
  "ds_read_b128 v[220:223], v245 offset:49408\n\t" \
  "s_add_u32 m0, %[lw], 67584\n\t" \
  "s_nop 0\n\t" \
  "global_load_lds_dwordx4 v250, s[92:93] sc1\n\t" \
  "ds_read_b128 v[224:227], v246 offset:33024\n\t" \
  "ds_read_b128 v[228:231], v246 offset:49408\n\t" \
  "ds_read_b128 v[232:235], v247 offset:33024\n\t" \
  "ds_read_b128 v[236:239], v247 offset:49408\n\t" \
  "s_add_u32 m0, %[lw], 68608\n\t" \
  "s_nop 0\n\t" \
  "global_load_lds_dwordx4 v251, s[92:93] sc1\n\t" \
  "s_waitcnt lgkmcnt(15)\n\t" \
  "s_waitcnt lgkmcnt(12)\n\t" \
  "s_lshr_b32 s90, s87, 7\n\t" \
  "s_xor_b32 s90, s90, %[s0]\n\t" \
  "s_and_b32 s90, s90, 1\n\t" \
  "s_lshl1_add_u32 s90, s87, s90\n\t" \
  "s_lshl_b32 s90, s90, 13\n\t" \
  "v_add_f32 v160, v160, v128\n\t" \
  "v_add_f32 v161, v161, v129\n\t" \
  "v_add_f32 v162, v162, v130\n\t" \
  "v_add_f32 v163, v163, v131\n\t" \
  "v_add_f32 v164, v164, v132\n\t" \
  "v_add_f32 v165, v165, v133\n\t" \
  "v_add_f32 v166, v166, v134\n\t" \
  "v_add_f32 v167, v167, v135\n\t" \
  "v_cvt_pk_bf16_f32 v168, v160, v161\n\t" \
  "v_cvt_pk_bf16_f32 v169, v162, v163\n\t" \
  "v_cvt_pk_bf16_f32 v170, v164, v165\n\t" \
  "v_cvt_pk_bf16_f32 v171, v166, v167\n\t" \
  "buffer_store_dwordx4 v[168:171], v172, %[rt], s90 offen sc1\n\t" \
  "s_add_u32 s86, s86, 1\n\t" \
  "s_add_u32 s87, s87, 1\n\t" \
  "s_waitcnt lgkmcnt(0)\n\t" \
  "s_setprio 1\n\t" \
  "v_mfma_f32_16x16x32_bf16 v[144:147], v[176:179], v[64:67], v[144:147]\n\t" \
  "v_mfma_f32_16x16x32_bf16 v[152:155], v[176:179], v[68:71], v[152:155]\n\t" \
  "v_mfma_f32_16x16x32_bf16 v[148:151], v[180:183], v[64:67], v[148:151]\n\t" \
  "v_mfma_f32_16x16x32_bf16 v[156:159], v[180:183], v[68:71], v[156:159]\n\t" \
  "v_mfma_f32_16x16x32_bf16 v[144:147], v[184:187], v[72:75], v[144:147]\n\t" \
  "v_mfma_f32_16x16x32_bf16 v[152:155], v[184:187], v[76:79], v[152:155]\n\t" \
  "v_mfma_f32_16x16x32_bf16 v[148:151], v[188:191], v[72:75], v[148:151]\n\t" \
  "v_mfma_f32_16x16x32_bf16 v[156:159], v[188:191], v[76:79], v[156:159]\n\t" \
  "v_mfma_f32_16x16x32_bf16 v[144:147], v[192:195], v[80:83], v[144:147]\n\t" \
  "v_mfma_f32_16x16x32_bf16 v[152:155], v[192:195], v[84:87], v[152:155]\n\t" \
  "v_mfma_f32_16x16x32_bf16 v[148:151], v[196:199], v[80:83], v[148:151]\n\t" \
  "v_mfma_f32_16x16x32_bf16 v[156:159], v[196:199], v[84:87], v[156:159]\n\t" \
  "v_mfma_f32_16x16x32_bf16 v[144:147], v[200:203], v[88:91], v[144:147]\n\t" \
  "v_mfma_f32_16x16x32_bf16 v[152:155], v[200:203], v[92:95], v[152:155]\n\t" \
  "v_mfma_f32_16x16x32_bf16 v[148:151], v[204:207], v[88:91], v[148:151]\n\t" \
  "v_mfma_f32_16x16x32_bf16 v[156:159], v[204:207], v[92:95], v[156:159]\n\t" \
  "v_mfma_f32_16x16x32_bf16 v[144:147], v[208:211], v[96:99], v[144:147]\n\t" \
  "v_mfma_f32_16x16x32_bf16 v[152:155], v[208:211], v[100:103], v[152:155]\n\t" \
  "v_mfma_f32_16x16x32_bf16 v[148:151], v[212:215], v[96:99], v[148:151]\n\t" \
  "v_mfma_f32_16x16x32_bf16 v[156:159], v[212:215], v[100:103], v[156:159]\n\t" \
  "v_mfma_f32_16x16x32_bf16 v[144:147], v[216:219], v[104:107], v[144:147]\n\t" \
  "v_mfma_f32_16x16x32_bf16 v[152:155], v[216:219], v[108:111], v[152:155]\n\t" \
  "v_mfma_f32_16x16x32_bf16 v[148:151], v[220:223], v[104:107], v[148:151]\n\t" \
  "v_mfma_f32_16x16x32_bf16 v[156:159], v[220:223], v[108:111], v[156:159]\n\t" \
  "v_mfma_f32_16x16x32_bf16 v[144:147], v[224:227], v[112:115], v[144:147]\n\t" \
  "v_mfma_f32_16x16x32_bf16 v[152:155], v[224:227], v[116:119], v[152:155]\n\t" \
  "v_mfma_f32_16x16x32_bf16 v[148:151], v[228:231], v[112:115], v[148:151]\n\t" \
  "v_mfma_f32_16x16x32_bf16 v[156:159], v[228:231], v[116:119], v[156:159]\n\t" \
  "v_mfma_f32_16x16x32_bf16 v[144:147], v[232:235], v[120:123], v[144:147]\n\t" \
  "v_mfma_f32_16x16x32_bf16 v[152:155], v[232:235], v[124:127], v[152:155]\n\t" \
  "v_mfma_f32_16x16x32_bf16 v[148:151], v[236:239], v[120:123], v[148:151]\n\t" \
  "v_mfma_f32_16x16x32_bf16 v[156:159], v[236:239], v[124:127], v[156:159]\n\t" \
  "s_setprio 0\n\t" \
  "s_cmp_lt_u32 s86, 64\n\t" \
  "s_cbranch_scc1 .Lp0%=\n\t" \
  "s_barrier\n\t" \
  "s_nop 1\n\t" \
  "ds_write_b128 v252, v[152:155] offset:0\n\t" \
  "s_nop 0\n\t" \
  "ds_write_b128 v252, v[156:159] offset:1024\n\t" \
  "s_waitcnt lgkmcnt(0)\n\t" \
  "s_barrier\n\t" \
  "ds_read_b128 v[160:163], v253 offset:0\n\t" \
  "ds_read_b128 v[164:167], v253 offset:1024\n\t" \
  "s_waitcnt lgkmcnt(0)\n\t" \
  "s_lshr_b32 s90, s87, 7\n\t" \
  "s_xor_b32 s90, s90, %[s0]\n\t" \
  "s_and_b32 s90, s90, 1\n\t" \
  "s_lshl1_add_u32 s90, s87, s90\n\t" \
  "s_lshl_b32 s90, s90, 13\n\t" \
  "v_add_f32 v160, v160, v144\n\t" \
  "v_add_f32 v161, v161, v145\n\t" \
  "v_add_f32 v162, v162, v146\n\t" \
  "v_add_f32 v163, v163, v147\n\t" \
  "v_add_f32 v164, v164, v148\n\t" \
  "v_add_f32 v165, v165, v149\n\t" \
  "v_add_f32 v166, v166, v150\n\t" \
  "v_add_f32 v167, v167, v151\n\t" \
  "v_cvt_pk_bf16_f32 v168, v160, v161\n\t" \
  "v_cvt_pk_bf16_f32 v169, v162, v163\n\t" \
  "v_cvt_pk_bf16_f32 v170, v164, v165\n\t" \
  "v_cvt_pk_bf16_f32 v171, v166, v167\n\t" \
  "buffer_store_dwordx4 v[168:171], v172, %[rt], s90 offen sc1\n\t" \
  "s_waitcnt vmcnt(0)\n\t" \
  "s_barrier\n\t" \
  "s_mov_b32 m0, s94\n\t" \
  "s_branch .Le%=\n\t" \
  ".Lk%=:\n\t" \
  "s_mov_b32 s94, m0\n\t" \
  "s_mov_b64 s[92:93], %[x0]\n\t" \
  "s_mov_b32 s86, %[g0]\n\t" \
  "s_add_u32 s87, s86, %[t0]\n\t" \
  "s_add_u32 m0, %[lw], 67584\n\t" \
  "s_nop 0\n\t" \
  "global_load_lds_dwordx4 v250, s[92:93] sc1\n\t" \
  "s_add_u32 m0, %[lw], 68608\n\t" \
  "s_nop 0\n\t" \
  "global_load_lds_dwordx4 v251, s[92:93] sc1\n\t" \
  "s_barrier\n\t" \
  "s_branch .Ls1%=\n\t" \
  ".Lp1%=:\n\t" \
  "s_waitcnt vmcnt(10)\n\t" \
  "s_barrier\n\t" \
  "s_setprio 2\n\t" \
  "v_mfma_f32_16x16x32_bf16 v[144:147], v[176:179], v[64:67], v[144:147]\n\t" \
  "v_mfma_f32_16x16x32_bf16 v[152:155], v[176:179], v[68:71], v[152:155]\n\t" \
  "v_mfma_f32_16x16x32_bf16 v[148:151], v[180:183], v[64:67], v[148:151]\n\t" \
  "v_mfma_f32_16x16x32_bf16 v[156:159], v[180:183], v[68:71], v[156:159]\n\t" \
  "v_mfma_f32_16x16x32_bf16 v[144:147], v[184:187], v[72:75], v[144:147]\n\t" \
  "v_mfma_f32_16x16x32_bf16 v[152:155], v[184:187], v[76:79], v[152:155]\n\t" \
  "v_mfma_f32_16x16x32_bf16 v[148:151], v[188:191], v[72:75], v[148:151]\n\t" \
  "v_mfma_f32_16x16x32_bf16 v[156:159], v[188:191], v[76:79], v[156:159]\n\t" \
  "v_mfma_f32_16x16x32_bf16 v[144:147], v[192:195], v[80:83], v[144:147]\n\t" \
  "v_mfma_f32_16x16x32_bf16 v[152:155], v[192:195], v[84:87], v[152:155]\n\t" \
  "v_mfma_f32_16x16x32_bf16 v[148:151], v[196:199], v[80:83], v[148:151]\n\t" \
  "v_mfma_f32_16x16x32_bf16 v[156:159], v[196:199], v[84:87], v[156:159]\n\t" \
  "v_mfma_f32_16x16x32_bf16 v[144:147], v[200:203], v[88:91], v[144:147]\n\t" \
  "v_mfma_f32_16x16x32_bf16 v[152:155], v[200:203], v[92:95], v[152:155]\n\t" \
  "v_mfma_f32_16x16x32_bf16 v[148:151], v[204:207], v[88:91], v[148:151]\n\t" \
  "v_mfma_f32_16x16x32_bf16 v[156:159], v[204:207], v[92:95], v[156:159]\n\t" \
  "v_mfma_f32_16x16x32_bf16 v[144:147], v[208:211], v[96:99], v[144:147]\n\t" \
  "v_mfma_f32_16x16x32_bf16 v[152:155], v[208:211], v[100:103], v[152:155]\n\t" \
  "v_mfma_f32_16x16x32_bf16 v[148:151], v[212:215], v[96:99], v[148:151]\n\t" \
  "v_mfma_f32_16x16x32_bf16 v[156:159], v[212:215], v[100:103], v[156:159]\n\t" \
  "v_mfma_f32_16x16x32_bf16 v[144:147], v[216:219], v[104:107], v[144:147]\n\t" \
  "v_mfma_f32_16x16x32_bf16 v[152:155], v[216:219], v[108:111], v[152:155]\n\t" \
  "v_mfma_f32_16x16x32_bf16 v[148:151], v[220:223], v[104:107], v[148:151]\n\t" \
  "v_mfma_f32_16x16x32_bf16 v[156:159], v[220:223], v[108:111], v[156:159]\n\t" \
  "v_mfma_f32_16x16x32_bf16 v[144:147], v[224:227], v[112:115], v[144:147]\n\t" \
  "v_mfma_f32_16x16x32_bf16 v[152:155], v[224:227], v[116:119], v[152:155]\n\t" \
  "v_mfma_f32_16x16x32_bf16 v[148:151], v[228:231], v[112:115], v[148:151]\n\t" \
  "v_mfma_f32_16x16x32_bf16 v[156:159], v[228:231], v[116:119], v[156:159]\n\t" \
  "v_mfma_f32_16x16x32_bf16 v[144:147], v[232:235], v[120:123], v[144:147]\n\t" \
  "v_mfma_f32_16x16x32_bf16 v[152:155], v[232:235], v[124:127], v[152:155]\n\t" \
  "v_mfma_f32_16x16x32_bf16 v[148:151], v[236:239], v[120:123], v[148:151]\n\t" \
  "v_mfma_f32_16x16x32_bf16 v[156:159], v[236:239], v[124:127], v[156:159]\n\t" \
  "s_setprio 0\n\t" \
  ".Ls1%=:\n\t" \
  "s_add_u32 s92, s92, 512\n\t" \
  "s_addc_u32 s93, s93, 0\n\t" \
  "ds_read_b128 v[176:179], v240 offset:0\n\t" \
  "ds_read_b128 v[180:183], v240 offset:16384\n\t" \
  "ds_read_b128 v[184:187], v241 offset:0\n\t" \
  "ds_read_b128 v[188:191], v241 offset:16384\n\t" \
  "s_add_u32 m0, %[lw], 98304\n\t" \
  "s_nop 0\n\t" \
  "global_load_lds_dwordx4 v248, s[92:93] sc1\n\t" \
  "ds_read_b128 v[192:195], v242 offset:0\n\t" \
  "ds_read_b128 v[196:199], v242 offset:16384\n\t" \
  "ds_read_b128 v[200:203], v243 offset:0\n\t" \
  "ds_read_b128 v[204:207], v243 offset:16384\n\t" \
  "s_add_u32 m0, %[lw], 99328\n\t" \
  "s_nop 0\n\t" \
  "global_load_lds_dwordx4 v249, s[92:93] sc1\n\t" \
  "ds_read_b128 v[208:211], v240 offset:256\n\t" \
  "ds_read_b128 v[212:215], v240 offset:16640\n\t" \
  "ds_read_b128 v[216:219], v241 offset:256\n\t" \
  "ds_read_b128 v[220:223], v241 offset:16640\n\t" \
  "s_add_u32 m0, %[lw], 100352\n\t" \
  "s_nop 0\n\t" \
  "global_load_lds_dwordx4 v250, s[92:93] sc1\n\t" \
  "ds_read_b128 v[224:227], v242 offset:256\n\t" \
  "ds_read_b128 v[228:231], v242 offset:16640\n\t" \
  "ds_read_b128 v[232:235], v243 offset:256\n\t" \
  "ds_read_b128 v[236:239], v243 offset:16640\n\t" \
  "s_add_u32 m0, %[lw], 101376\n\t" \
  "s_nop 0\n\t" \
  "global_load_lds_dwordx4 v251, s[92:93] sc1\n\t" \
  "ds_write_b128 v252, v[152:155] offset:0\n\t" \
  "ds_write_b128 v252, v[156:159] offset:1024\n\t" \
  "s_waitcnt lgkmcnt(0)\n\t" \
  "s_waitcnt vmcnt(4)\n\t" \
  "s_barrier\n\t" \
  "s_setprio 2\n\t" \
  "v_mfma_f32_16x16x32_bf16 v[128:131], v[176:179], v[0:3], 0\n\t" \
  "v_mfma_f32_16x16x32_bf16 v[136:139], v[176:179], v[4:7], 0\n\t" \
  "v_mfma_f32_16x16x32_bf16 v[132:135], v[180:183], v[0:3], 0\n\t" \
  "v_mfma_f32_16x16x32_bf16 v[140:143], v[180:183], v[4:7], 0\n\t" \
  "v_mfma_f32_16x16x32_bf16 v[128:131], v[184:187], v[8:11], v[128:131]\n\t" \
  "v_mfma_f32_16x16x32_bf16 v[136:139], v[184:187], v[12:15], v[136:139]\n\t" \
  "v_mfma_f32_16x16x32_bf16 v[132:135], v[188:191], v[8:11], v[132:135]\n\t" \
  "v_mfma_f32_16x16x32_bf16 v[140:143], v[188:191], v[12:15], v[140:143]\n\t" \
  "v_mfma_f32_16x16x32_bf16 v[128:131], v[192:195], v[16:19], v[128:131]\n\t" \
  "v_mfma_f32_16x16x32_bf16 v[136:139], v[192:195], v[20:23], v[136:139]\n\t" \
  "v_mfma_f32_16x16x32_bf16 v[132:135], v[196:199], v[16:19], v[132:135]\n\t" \
  "v_mfma_f32_16x16x32_bf16 v[140:143], v[196:199], v[20:23], v[140:143]\n\t" \
  "v_mfma_f32_16x16x32_bf16 v[128:131], v[200:203], v[24:27], v[128:131]\n\t" \
  "v_mfma_f32_16x16x32_bf16 v[136:139], v[200:203], v[28:31], v[136:139]\n\t" \
  "v_mfma_f32_16x16x32_bf16 v[132:135], v[204:207], v[24:27], v[132:135]\n\t" \
  "v_mfma_f32_16x16x32_bf16 v[140:143], v[204:207], v[28:31], v[140:143]\n\t" \
  "v_mfma_f32_16x16x32_bf16 v[128:131], v[208:211], v[32:35], v[128:131]\n\t" \
  "v_mfma_f32_16x16x32_bf16 v[136:139], v[208:211], v[36:39], v[136:139]\n\t" \
  "v_mfma_f32_16x16x32_bf16 v[132:135], v[212:215], v[32:35], v[132:135]\n\t" \
  "v_mfma_f32_16x16x32_bf16 v[140:143], v[212:215], v[36:39], v[140:143]\n\t" \
  "v_mfma_f32_16x16x32_bf16 v[128:131], v[216:219], v[40:43], v[128:131]\n\t" \
  "v_mfma_f32_16x16x32_bf16 v[136:139], v[216:219], v[44:47], v[136:139]\n\t" \
  "v_mfma_f32_16x16x32_bf16 v[132:135], v[220:223], v[40:43], v[132:135]\n\t" \
  "v_mfma_f32_16x16x32_bf16 v[140:143], v[220:223], v[44:47], v[140:143]\n\t" \
  "v_mfma_f32_16x16x32_bf16 v[128:131], v[224:227], v[48:51], v[128:131]\n\t" \
  "v_mfma_f32_16x16x32_bf16 v[136:139], v[224:227], v[52:55], v[136:139]\n\t" \
  "v_mfma_f32_16x16x32_bf16 v[132:135], v[228:231], v[48:51], v[132:135]\n\t" \
  "v_mfma_f32_16x16x32_bf16 v[140:143], v[228:231], v[52:55], v[140:143]\n\t" \
  "v_mfma_f32_16x16x32_bf16 v[128:131], v[232:235], v[56:59], v[128:131]\n\t" \
  "v_mfma_f32_16x16x32_bf16 v[136:139], v[232:235], v[60:63], v[136:139]\n\t" \
  "v_mfma_f32_16x16x32_bf16 v[132:135], v[236:239], v[56:59], v[132:135]\n\t" \
  "v_mfma_f32_16x16x32_bf16 v[140:143], v[236:239], v[60:63], v[140:143]\n\t" \
  "s_setprio 0\n\t" \
  "s_add_u32 s92, s92, 65024\n\t" \
  "s_addc_u32 s93, s93, 0\n\t" \
  "ds_read_b128 v[160:163], v253 offset:0\n\t" \
  "ds_read_b128 v[164:167], v253 offset:1024\n\t" \
  "ds_read_b128 v[176:179], v240 offset:32768\n\t" \
  "ds_read_b128 v[180:183], v240 offset:49152\n\t" \
  "ds_read_b128 v[184:187], v241 offset:32768\n\t" \
  "ds_read_b128 v[188:191], v241 offset:49152\n\t" \
  "s_add_u32 m0, %[lw], 0\n\t" \
  "s_nop 0\n\t" \
  "global_load_lds_dwordx4 v248, s[92:93] sc1\n\t" \
  "ds_read_b128 v[192:195], v242 offset:32768\n\t" \
  "ds_read_b128 v[196:199], v242 offset:49152\n\t" \
  "ds_read_b128 v[200:203], v243 offset:32768\n\t" \
  "ds_read_b128 v[204:207], v243 offset:49152\n\t" \
  "s_add_u32 m0, %[lw], 1024\n\t" \
  "s_nop 0\n\t" \
  "global_load_lds_dwordx4 v249, s[92:93] sc1\n\t" \
  "ds_read_b128 v[208:211], v240 offset:33024\n\t" \
  "ds_read_b128 v[212:215], v240 offset:49408\n\t" \
  "ds_read_b128 v[216:219], v241 offset:33024\n\t" \
  "ds_read_b128 v[220:223], v241 offset:49408\n\t" \
  "s_add_u32 m0, %[lw], 2048\n\t" \
  "s_nop 0\n\t" \
  "global_load_lds_dwordx4 v250, s[92:93] sc1\n\t" \
  "ds_read_b128 v[224:227], v242 offset:33024\n\t" \
  "ds_read_b128 v[228:231], v242 offset:49408\n\t" \
  "ds_read_b128 v[232:235], v243 offset:33024\n\t" \
  "ds_read_b128 v[236:239], v243 offset:49408\n\t" \
  "s_add_u32 m0, %[lw], 3072\n\t" \
  "s_nop 0\n\t" \
  "global_load_lds_dwordx4 v251, s[92:93] sc1\n\t" \
  "s_waitcnt lgkmcnt(15)\n\t" \
  "s_waitcnt lgkmcnt(12)\n\t" \
  "s_sub_i32 s91, s86, 2\n\t" \
  "v_mov_b32 v173, s91\n\t" \
  "buffer_store_dword v173, v174, %[rs], 0 offen sc1\n\t" \
  "s_lshr_b32 s90, s87, 7\n\t" \
  "s_xor_b32 s90, s90, %[s0]\n\t" \
  "s_and_b32 s90, s90, 1\n\t" \
  "s_lshl1_add_u32 s90, s87, s90\n\t" \
  "s_lshl_b32 s90, s90, 13\n\t" \
  "v_add_f32 v160, v160, v144\n\t" \
  "v_add_f32 v161, v161, v145\n\t" \
  "v_add_f32 v162, v162, v146\n\t" \
  "v_add_f32 v163, v163, v147\n\t" \
  "v_add_f32 v164, v164, v148\n\t" \
  "v_add_f32 v165, v165, v149\n\t" \
  "v_add_f32 v166, v166, v150\n\t" \
  "v_add_f32 v167, v167, v151\n\t" \
  "v_cvt_pk_bf16_f32 v168, v160, v161\n\t" \
  "v_cvt_pk_bf16_f32 v169, v162, v163\n\t" \
  "v_cvt_pk_bf16_f32 v170, v164, v165\n\t" \
  "v_cvt_pk_bf16_f32 v171, v166, v167\n\t" \
  "buffer_store_dwordx4 v[168:171], v172, %[rt], s90 offen sc1\n\t" \
  "s_add_u32 s86, s86, 1\n\t" \
  "s_add_u32 s87, s87, 1\n\t" \
  "s_waitcnt lgkmcnt(0)\n\t" \
  "s_waitcnt vmcnt(10)\n\t" \
  "s_barrier\n\t" \
  "s_setprio 2\n\t" \
  "v_mfma_f32_16x16x32_bf16 v[128:131], v[176:179], v[64:67], v[128:131]\n\t" \
  "v_mfma_f32_16x16x32_bf16 v[136:139], v[176:179], v[68:71], v[136:139]\n\t" \
  "v_mfma_f32_16x16x32_bf16 v[132:135], v[180:183], v[64:67], v[132:135]\n\t" \
  "v_mfma_f32_16x16x32_bf16 v[140:143], v[180:183], v[68:71], v[140:143]\n\t" \
  "v_mfma_f32_16x16x32_bf16 v[128:131], v[184:187], v[72:75], v[128:131]\n\t" \
  "v_mfma_f32_16x16x32_bf16 v[136:139], v[184:187], v[76:79], v[136:139]\n\t" \
  "v_mfma_f32_16x16x32_bf16 v[132:135], v[188:191], v[72:75], v[132:135]\n\t" \
  "v_mfma_f32_16x16x32_bf16 v[140:143], v[188:191], v[76:79], v[140:143]\n\t" \
  "v_mfma_f32_16x16x32_bf16 v[128:131], v[192:195], v[80:83], v[128:131]\n\t" \
  "v_mfma_f32_16x16x32_bf16 v[136:139], v[192:195], v[84:87], v[136:139]\n\t" \
  "v_mfma_f32_16x16x32_bf16 v[132:135], v[196:199], v[80:83], v[132:135]\n\t" \
  "v_mfma_f32_16x16x32_bf16 v[140:143], v[196:199], v[84:87], v[140:143]\n\t" \
  "v_mfma_f32_16x16x32_bf16 v[128:131], v[200:203], v[88:91], v[128:131]\n\t" \
  "v_mfma_f32_16x16x32_bf16 v[136:139], v[200:203], v[92:95], v[136:139]\n\t" \
  "v_mfma_f32_16x16x32_bf16 v[132:135], v[204:207], v[88:91], v[132:135]\n\t" \
  "v_mfma_f32_16x16x32_bf16 v[140:143], v[204:207], v[92:95], v[140:143]\n\t" \
  "v_mfma_f32_16x16x32_bf16 v[128:131], v[208:211], v[96:99], v[128:131]\n\t" \
  "v_mfma_f32_16x16x32_bf16 v[136:139], v[208:211], v[100:103], v[136:139]\n\t" \
  "v_mfma_f32_16x16x32_bf16 v[132:135], v[212:215], v[96:99], v[132:135]\n\t" \
  "v_mfma_f32_16x16x32_bf16 v[140:143], v[212:215], v[100:103], v[140:143]\n\t" \
  "v_mfma_f32_16x16x32_bf16 v[128:131], v[216:219], v[104:107], v[128:131]\n\t" \
  "v_mfma_f32_16x16x32_bf16 v[136:139], v[216:219], v[108:111], v[136:139]\n\t" \
  "v_mfma_f32_16x16x32_bf16 v[132:135], v[220:223], v[104:107], v[132:135]\n\t" \
  "v_mfma_f32_16x16x32_bf16 v[140:143], v[220:223], v[108:111], v[140:143]\n\t" \
  "v_mfma_f32_16x16x32_bf16 v[128:131], v[224:227], v[112:115], v[128:131]\n\t" \
  "v_mfma_f32_16x16x32_bf16 v[136:139], v[224:227], v[116:119], v[136:139]\n\t" \
  "v_mfma_f32_16x16x32_bf16 v[132:135], v[228:231], v[112:115], v[132:135]\n\t" \
  "v_mfma_f32_16x16x32_bf16 v[140:143], v[228:231], v[116:119], v[140:143]\n\t" \
  "v_mfma_f32_16x16x32_bf16 v[128:131], v[232:235], v[120:123], v[128:131]\n\t" \
  "v_mfma_f32_16x16x32_bf16 v[136:139], v[232:235], v[124:127], v[136:139]\n\t" \
  "v_mfma_f32_16x16x32_bf16 v[132:135], v[236:239], v[120:123], v[132:135]\n\t" \
  "v_mfma_f32_16x16x32_bf16 v[140:143], v[236:239], v[124:127], v[140:143]\n\t" \
  "s_setprio 0\n\t" \
  "s_add_u32 s92, s92, 512\n\t" \
  "s_addc_u32 s93, s93, 0\n\t" \
  "ds_read_b128 v[176:179], v244 offset:0\n\t" \
  "ds_read_b128 v[180:183], v244 offset:16384\n\t" \
  "ds_read_b128 v[184:187], v245 offset:0\n\t" \
  "ds_read_b128 v[188:191], v245 offset:16384\n\t" \
  "s_add_u32 m0, %[lw], 32768\n\t" \
  "s_nop 0\n\t" \
  "global_load_lds_dwordx4 v248, s[92:93] sc1\n\t" \
  "ds_read_b128 v[192:195], v246 offset:0\n\t" \
  "ds_read_b128 v[196:199], v246 offset:16384\n\t" \
  "ds_read_b128 v[200:203], v247 offset:0\n\t" \
  "ds_read_b128 v[204:207], v247 offset:16384\n\t" \
  "s_add_u32 m0, %[lw], 33792\n\t" \
  "s_nop 0\n\t" \
  "global_load_lds_dwordx4 v249, s[92:93] sc1\n\t" \
  "ds_read_b128 v[208:211], v244 offset:256\n\t" \
  "ds_read_b128 v[212:215], v244 offset:16640\n\t" \
  "ds_read_b128 v[216:219], v245 offset:256\n\t" \
  "ds_read_b128 v[220:223], v245 offset:16640\n\t" \
  "s_add_u32 m0, %[lw], 34816\n\t" \
  "s_nop 0\n\t" \
  "global_load_lds_dwordx4 v250, s[92:93] sc1\n\t" \
  "ds_read_b128 v[224:227], v246 offset:256\n\t" \
  "ds_read_b128 v[228:231], v246 offset:16640\n\t" \
  "ds_read_b128 v[232:235], v247 offset:256\n\t" \
  "ds_read_b128 v[236:239], v247 offset:16640\n\t" \
  "s_add_u32 m0, %[lw], 35840\n\t" \
  "s_nop 0\n\t" \
  "global_load_lds_dwordx4 v251, s[92:93] sc1\n\t" \
  "ds_write_b128 v252, v[136:139] offset:0\n\t" \
  "ds_write_b128 v252, v[140:143] offset:1024\n\t" \
  "s_waitcnt lgkmcnt(0)\n\t" \
  "s_waitcnt vmcnt(4)\n\t" \
  "s_barrier\n\t" \
  "s_setprio 2\n\t" \
  "v_mfma_f32_16x16x32_bf16 v[144:147], v[176:179], v[0:3], 0\n\t" \
  "v_mfma_f32_16x16x32_bf16 v[152:155], v[176:179], v[4:7], 0\n\t" \
  "v_mfma_f32_16x16x32_bf16 v[148:151], v[180:183], v[0:3], 0\n\t" \
  "v_mfma_f32_16x16x32_bf16 v[156:159], v[180:183], v[4:7], 0\n\t" \
  "v_mfma_f32_16x16x32_bf16 v[144:147], v[184:187], v[8:11], v[144:147]\n\t" \
  "v_mfma_f32_16x16x32_bf16 v[152:155], v[184:187], v[12:15], v[152:155]\n\t" \
  "v_mfma_f32_16x16x32_bf16 v[148:151], v[188:191], v[8:11], v[148:151]\n\t" \
  "v_mfma_f32_16x16x32_bf16 v[156:159], v[188:191], v[12:15], v[156:159]\n\t" \
  "v_mfma_f32_16x16x32_bf16 v[144:147], v[192:195], v[16:19], v[144:147]\n\t" \
  "v_mfma_f32_16x16x32_bf16 v[152:155], v[192:195], v[20:23], v[152:155]\n\t" \
  "v_mfma_f32_16x16x32_bf16 v[148:151], v[196:199], v[16:19], v[148:151]\n\t" \
  "v_mfma_f32_16x16x32_bf16 v[156:159], v[196:199], v[20:23], v[156:159]\n\t" \
  "v_mfma_f32_16x16x32_bf16 v[144:147], v[200:203], v[24:27], v[144:147]\n\t" \
  "v_mfma_f32_16x16x32_bf16 v[152:155], v[200:203], v[28:31], v[152:155]\n\t" \
  "v_mfma_f32_16x16x32_bf16 v[148:151], v[204:207], v[24:27], v[148:151]\n\t" \
  "v_mfma_f32_16x16x32_bf16 v[156:159], v[204:207], v[28:31], v[156:159]\n\t" \
  "v_mfma_f32_16x16x32_bf16 v[144:147], v[208:211], v[32:35], v[144:147]\n\t" \
  "v_mfma_f32_16x16x32_bf16 v[152:155], v[208:211], v[36:39], v[152:155]\n\t" \
  "v_mfma_f32_16x16x32_bf16 v[148:151], v[212:215], v[32:35], v[148:151]\n\t" \
  "v_mfma_f32_16x16x32_bf16 v[156:159], v[212:215], v[36:39], v[156:159]\n\t" \
  "v_mfma_f32_16x16x32_bf16 v[144:147], v[216:219], v[40:43], v[144:147]\n\t" \
  "v_mfma_f32_16x16x32_bf16 v[152:155], v[216:219], v[44:47], v[152:155]\n\t" \
  "v_mfma_f32_16x16x32_bf16 v[148:151], v[220:223], v[40:43], v[148:151]\n\t" \
  "v_mfma_f32_16x16x32_bf16 v[156:159], v[220:223], v[44:47], v[156:159]\n\t" \
  "v_mfma_f32_16x16x32_bf16 v[144:147], v[224:227], v[48:51], v[144:147]\n\t" \
  "v_mfma_f32_16x16x32_bf16 v[152:155], v[224:227], v[52:55], v[152:155]\n\t" \
  "v_mfma_f32_16x16x32_bf16 v[148:151], v[228:231], v[48:51], v[148:151]\n\t" \
  "v_mfma_f32_16x16x32_bf16 v[156:159], v[228:231], v[52:55], v[156:159]\n\t" \
  "v_mfma_f32_16x16x32_bf16 v[144:147], v[232:235], v[56:59], v[144:147]\n\t" \
  "v_mfma_f32_16x16x32_bf16 v[152:155], v[232:235], v[60:63], v[152:155]\n\t" \
  "v_mfma_f32_16x16x32_bf16 v[148:151], v[236:239], v[56:59], v[148:151]\n\t" \
  "v_mfma_f32_16x16x32_bf16 v[156:159], v[236:239], v[60:63], v[156:159]\n\t" \
  "s_setprio 0\n\t" \
  "s_add_u32 s92, s92, 65024\n\t" \
  "s_addc_u32 s93, s93, 0\n\t" \
  "ds_read_b128 v[160:163], v253 offset:0\n\t" \
  "ds_read_b128 v[164:167], v253 offset:1024\n\t" \
  "ds_read_b128 v[176:179], v244 offset:32768\n\t" \
  "ds_read_b128 v[180:183], v244 offset:49152\n\t" \
  "ds_read_b128 v[184:187], v245 offset:32768\n\t" \
  "ds_read_b128 v[188:191], v245 offset:49152\n\t" \
  "s_add_u32 m0, %[lw], 65536\n\t" \
  "s_nop 0\n\t" \
  "global_load_lds_dwordx4 v248, s[92:93] sc1\n\t" \
  "ds_read_b128 v[192:195], v246 offset:32768\n\t" \
  "ds_read_b128 v[196:199], v246 offset:49152\n\t" \
  "ds_read_b128 v[200:203], v247 offset:32768\n\t" \
  "ds_read_b128 v[204:207], v247 offset:49152\n\t" \
  "s_add_u32 m0, %[lw], 66560\n\t" \
  "s_nop 0\n\t" \
  "global_load_lds_dwordx4 v249, s[92:93] sc1\n\t" \
  "ds_read_b128 v[208:211], v244 offset:33024\n\t" \
  "ds_read_b128 v[212:215], v244 offset:49408\n\t" \
  "ds_read_b128 v[216:219], v245 offset:33024\n\t" \
  "ds_read_b128 v[220:223], v245 offset:49408\n\t" \
  "s_add_u32 m0, %[lw], 67584\n\t" \
  "s_nop 0\n\t" \
  "global_load_lds_dwordx4 v250, s[92:93] sc1\n\t" \
  "ds_read_b128 v[224:227], v246 offset:33024\n\t" \
  "ds_read_b128 v[228:231], v246 offset:49408\n\t" \
  "ds_read_b128 v[232:235], v247 offset:33024\n\t" \
  "ds_read_b128 v[236:239], v247 offset:49408\n\t" \
  "s_add_u32 m0, %[lw], 68608\n\t" \
  "s_nop 0\n\t" \
  "global_load_lds_dwordx4 v251, s[92:93] sc1\n\t" \
  "s_waitcnt lgkmcnt(15)\n\t" \
  "s_waitcnt lgkmcnt(12)\n\t" \
  "s_sub_i32 s91, s86, 2\n\t" \
  "v_mov_b32 v173, s91\n\t" \
  "buffer_store_dword v173, v174, %[rs], 0 offen sc1\n\t" \
  "s_lshr_b32 s90, s87, 7\n\t" \
  "s_xor_b32 s90, s90, %[s0]\n\t" \
  "s_and_b32 s90, s90, 1\n\t" \
  "s_lshl1_add_u32 s90, s87, s90\n\t" \
  "s_lshl_b32 s90, s90, 13\n\t" \
  "v_add_f32 v160, v160, v128\n\t" \
  "v_add_f32 v161, v161, v129\n\t" \
  "v_add_f32 v162, v162, v130\n\t" \
  "v_add_f32 v163, v163, v131\n\t" \
  "v_add_f32 v164, v164, v132\n\t" \
  "v_add_f32 v165, v165, v133\n\t" \
  "v_add_f32 v166, v166, v134\n\t" \
  "v_add_f32 v167, v167, v135\n\t" \
  "v_cvt_pk_bf16_f32 v168, v160, v161\n\t" \
  "v_cvt_pk_bf16_f32 v169, v162, v163\n\t" \
  "v_cvt_pk_bf16_f32 v170, v164, v165\n\t" \
  "v_cvt_pk_bf16_f32 v171, v166, v167\n\t" \
  "buffer_store_dwordx4 v[168:171], v172, %[rt], s90 offen sc1\n\t" \
  "s_add_u32 s86, s86, 1\n\t" \
  "s_add_u32 s87, s87, 1\n\t" \
  "s_waitcnt lgkmcnt(0)\n\t" \
  "s_cmp_lt_u32 s86, 64\n\t" \
  "s_cbranch_scc1 .Lp1%=\n\t" \
  "s_barrier\n\t" \
  "s_setprio 2\n\t" \
  "v_mfma_f32_16x16x32_bf16 v[144:147], v[176:179], v[64:67], v[144:147]\n\t" \
  "v_mfma_f32_16x16x32_bf16 v[152:155], v[176:179], v[68:71], v[152:155]\n\t" \
  "v_mfma_f32_16x16x32_bf16 v[148:151], v[180:183], v[64:67], v[148:151]\n\t" \
  "v_mfma_f32_16x16x32_bf16 v[156:159], v[180:183], v[68:71], v[156:159]\n\t" \
  "v_mfma_f32_16x16x32_bf16 v[144:147], v[184:187], v[72:75], v[144:147]\n\t" \
  "v_mfma_f32_16x16x32_bf16 v[152:155], v[184:187], v[76:79], v[152:155]\n\t" \
  "v_mfma_f32_16x16x32_bf16 v[148:151], v[188:191], v[72:75], v[148:151]\n\t" \
  "v_mfma_f32_16x16x32_bf16 v[156:159], v[188:191], v[76:79], v[156:159]\n\t" \
  "v_mfma_f32_16x16x32_bf16 v[144:147], v[192:195], v[80:83], v[144:147]\n\t" \
  "v_mfma_f32_16x16x32_bf16 v[152:155], v[192:195], v[84:87], v[152:155]\n\t" \
  "v_mfma_f32_16x16x32_bf16 v[148:151], v[196:199], v[80:83], v[148:151]\n\t" \
  "v_mfma_f32_16x16x32_bf16 v[156:159], v[196:199], v[84:87], v[156:159]\n\t" \
  "v_mfma_f32_16x16x32_bf16 v[144:147], v[200:203], v[88:91], v[144:147]\n\t" \
  "v_mfma_f32_16x16x32_bf16 v[152:155], v[200:203], v[92:95], v[152:155]\n\t" \
  "v_mfma_f32_16x16x32_bf16 v[148:151], v[204:207], v[88:91], v[148:151]\n\t" \
  "v_mfma_f32_16x16x32_bf16 v[156:159], v[204:207], v[92:95], v[156:159]\n\t" \
  "v_mfma_f32_16x16x32_bf16 v[144:147], v[208:211], v[96:99], v[144:147]\n\t" \
  "v_mfma_f32_16x16x32_bf16 v[152:155], v[208:211], v[100:103], v[152:155]\n\t" \
  "v_mfma_f32_16x16x32_bf16 v[148:151], v[212:215], v[96:99], v[148:151]\n\t" \
  "v_mfma_f32_16x16x32_bf16 v[156:159], v[212:215], v[100:103], v[156:159]\n\t" \
  "v_mfma_f32_16x16x32_bf16 v[144:147], v[216:219], v[104:107], v[144:147]\n\t" \
  "v_mfma_f32_16x16x32_bf16 v[152:155], v[216:219], v[108:111], v[152:155]\n\t" \
  "v_mfma_f32_16x16x32_bf16 v[148:151], v[220:223], v[104:107], v[148:151]\n\t" \
  "v_mfma_f32_16x16x32_bf16 v[156:159], v[220:223], v[108:111], v[156:159]\n\t" \
  "v_mfma_f32_16x16x32_bf16 v[144:147], v[224:227], v[112:115], v[144:147]\n\t" \
  "v_mfma_f32_16x16x32_bf16 v[152:155], v[224:227], v[116:119], v[152:155]\n\t" \
  "v_mfma_f32_16x16x32_bf16 v[148:151], v[228:231], v[112:115], v[148:151]\n\t" \
  "v_mfma_f32_16x16x32_bf16 v[156:159], v[228:231], v[116:119], v[156:159]\n\t" \
  "v_mfma_f32_16x16x32_bf16 v[144:147], v[232:235], v[120:123], v[144:147]\n\t" \
  "v_mfma_f32_16x16x32_bf16 v[152:155], v[232:235], v[124:127], v[152:155]\n\t" \
  "v_mfma_f32_16x16x32_bf16 v[148:151], v[236:239], v[120:123], v[148:151]\n\t" \
  "v_mfma_f32_16x16x32_bf16 v[156:159], v[236:239], v[124:127], v[156:159]\n\t" \
  "s_setprio 0\n\t" \
  "s_nop 4\n\t" \
  "ds_write_b128 v252, v[152:155] offset:0\n\t" \
  "s_nop 0\n\t" \
  "ds_write_b128 v252, v[156:159] offset:1024\n\t" \
  "s_waitcnt lgkmcnt(0)\n\t" \
  "s_barrier\n\t" \
  "ds_read_b128 v[160:163], v253 offset:0\n\t" \
  "ds_read_b128 v[164:167], v253 offset:1024\n\t" \
  "s_waitcnt lgkmcnt(0)\n\t" \
  "s_lshr_b32 s90, s87, 7\n\t" \
  "s_xor_b32 s90, s90, %[s0]\n\t" \
  "s_and_b32 s90, s90, 1\n\t" \
  "s_lshl1_add_u32 s90, s87, s90\n\t" \
  "s_lshl_b32 s90, s90, 13\n\t" \
  "v_add_f32 v160, v160, v144\n\t" \
  "v_add_f32 v161, v161, v145\n\t" \
  "v_add_f32 v162, v162, v146\n\t" \
  "v_add_f32 v163, v163, v147\n\t" \
  "v_add_f32 v164, v164, v148\n\t" \
  "v_add_f32 v165, v165, v149\n\t" \
  "v_add_f32 v166, v166, v150\n\t" \
  "v_add_f32 v167, v167, v151\n\t" \
  "v_cvt_pk_bf16_f32 v168, v160, v161\n\t" \
  "v_cvt_pk_bf16_f32 v169, v162, v163\n\t" \
  "v_cvt_pk_bf16_f32 v170, v164, v165\n\t" \
  "v_cvt_pk_bf16_f32 v171, v166, v167\n\t" \
  "buffer_store_dwordx4 v[168:171], v172, %[rt], s90 offen sc1\n\t" \
  "s_waitcnt vmcnt(0)\n\t" \
  "s_barrier\n\t" \
  "s_sub_i32 s91, s86, 1\n\t" \
  "v_mov_b32 v173, s91\n\t" \
  "buffer_store_dword v173, v174, %[rs], 0 offen sc1\n\t" \
  "s_mov_b32 m0, s94\n\t" \
  ".Le%=:\n\t"
constexpr int W_X = 131072, W_PM = W_X + 16384;
template <bool ISV>
__device__ __forceinline__ void job_proj(Args& p, int g, int which, int h, char* smem, __amdgpu_buffer_rsrc_t rsy) {
  const int tid = threadIdx.x, lane = tid & 63, r = lane & 15, q = lane >> 4;
  const int w = __builtin_amdgcn_readfirstlane(tid >> 6), kh = w >> 2, cg = w & 3;
  u32x32 W[4];
  const unsigned lw = lds_off(smem) + w * 4096;
  u32x4 vx;
  u32x2 tag;
  const char* xn = p.ws + WS_XN;
  auto row = [&](int t, int i) {
    const char* src = xn + (size_t)(min(t, 127) >> 1) * (SEG * XLD * 2) + (t & 1) * 512;
    unsigned keep;
    asm volatile("s_mov_b32 %0, m0\n\ts_mov_b32 m0, %3\n\ts_nop 0\n\tglobal_load_lds_dwordx4 %1, %2 sc1\n\ts_mov_b32 m0, %0"
                 : "=&s"(keep) : "v"(vx[i]), "s"(src), "s"(lw + (t & 3) * 32768 + i * 1024) : "memory");
  };
  {
    const u16* Wm = which == 0 ? p.Wk : p.Wq;
    asm volatile("" : "+s"(Wm));
    if (ISV) Wm = p.Wv;
    const unsigned vo = (tid >> 4) * (HD * 2) + 16 * ((lane & 15) ^ swzB((tid >> 4) & 15));
    auto stage = [&](int j) {
      const char* src = (const char*)(Wm + (size_t)(WREV ? 7 - j : j) * 64 * HD + h * DH);
      const unsigned dst = lds_off(smem) + (j & 3) * 32768 + w * 1024;
      glds2(src, 32 * HD * 2, vo, dst);
      glds2(src + 512 * HD * 2, 32 * HD * 2, vo, dst + 16384);
    };
    const unsigned own = min((h & LN_M) ^ LN_X, 1);
    tag = start_rows<16>(p, 32 + g, own, [&]() { stage(0); stage(1); stage(2); stage(3); });
#pragma unroll
    for (int i = 0; i < 4; ++i) vx[i] = ((4 * w + i) ^ (ISV ? 0 : (lane >> 1) & 16)) * (XLD * 2) + (lane >> 5) * 1024 + (((lane & 31) ^ ((4 * w + i) & 15)) << 4);
    Boot bt;
#pragma unroll
    for (int j = 0; j < 8; ++j) {
      if (j < 4) WAIT_V(16); else if (j == 4) WAIT_V(12); else if (j == 5) WAIT_V(8); else if (j == 6) WAIT_V(4); else WAIT_V(10);
      if (j == 6 && w == 0) lds_at(smem + W_PM)[0] = boot_done(p.ws, bt, true, tag);
      LBAR();
      if (j == 4 && own) report_rows(p, 32 + g, tag);
      const char* sb = smem + (j & 3) * 32768 + kh * 16384;
#pragma unroll
      for (int k = 0; k < 4; ++k) {
        const int ks = k >> 1, a = k & 1;
        const unsigned ch = ISV ? 2 * (2 * cg + (a ^ kh)) + ((r & 3) >> 1) : 4 * cg + (r & 3), by = ISV ? 8 * (r & 1) : 8 * a;
        s16x4 lo = __builtin_amdgcn_ds_read_tr16_b64_v4i16((lds_s16x4_ptr)(sb + offB(32 * ks + 8 * q + (r >> 2), ch) + by));
        s16x4 hi = __builtin_amdgcn_ds_read_tr16_b64_v4i16((lds_s16x4_ptr)(sb + offB(32 * ks + 8 * q + 4 + (r >> 2), ch) + by));
        const int f = 4 * (WREV ? 7 - j : j) + k;
        put4(W[f >> 3], f & 7, (bf16x8){lo[0], lo[1], lo[2], lo[3], hi[0], hi[1], hi[2], hi[3]});
      }
      LBAR();
      if (j == 3 && w == 0) boot_ask(p.ws, bt);
      if (j + 4 < 8) stage(j + 4);
      if (j == 6) {
#pragma unroll
        for (int i = 0; i < 10; ++i) row(i >> 2, i & 3);
      }
    }
    p.s0 = uni(lds_at(smem + W_PM)[0] >> 31);
  }
  typedef __attribute__((ext_vector_type(8))) unsigned u32x8;
  u32x8 la;
#pragma unroll
  for (int i = 0; i < 8; ++i) la[i] = (i >> 2) * 65536 + r * 1024 + kh * 512 + (((4 * (i & 3) + q) ^ r) << 4);
  const unsigned xw = W_X + w * 2048 + lane * 16;
  u32x32 acc;
  asm volatile("" : "={v[128:159]}"(acc));
  WAIT_V(6);
  LBAR();
  {
    const int g0 = 0;
#define PR_LOOP(TXT)                                                                                                              \
    asm volatile(TXT                                                                                                              \
        : "+{v[128:159]}"(acc)                                                                                                    \
        : W4_IN, "{v[208:215]}"(la), "{v[216:219]}"(vx), "{v220}"(xw), "{v221}"(xw ^ 8192),                                       \
          "{v172}"((ISV ? 2 * cg + kh : 4 * kh + cg) * 1024 + lane * 16), "{v174}"(tid == 256 ? fkq(h, which) : 0x7F000000),      \
          [rt] "s"(raw_rsrc(p.ws + WS_T, T_BYTES)), [rs] "s"(raw_rsrc(p.ws + WS_SY, SY_TOTAL)), [s0] "s"(p.s0 ^ TF_KQV),          \
          [lw] "s"(lw), [kh] "s"(kh), [g0] "s"(g0), [end] "s"(g0 ? NSEG : PR_G1), [x0] "s"(xn + (g0 + 1) * (SEG * XLD * 2)),                       \
          [t0] "s"(which * T_Q + kseg(0, h) - 1)                                                                                  \
        : CLB8(160, 161, 162, 163, 164, 165, 166, 167), "v168", "v169", "v170", "v171", "v173", CLB8(176, 177, 178, 179, 180, 181, 182, 183), \
          CLB8(184, 185, 186, 187, 188, 189, 190, 191), CLB8(192, 193, 194, 195, 196, 197, 198, 199),                             \
          CLB8(200, 201, 202, 203, 204, 205, 206, 207), "s86", "s87", "s88", "s90", "s91", "s92", "s93", "s94", "scc", "memory")
    if (ISV) PR_LOOP(PR_ASM_1); else PR_LOOP(PR_ASM_0);
    if (w == 0 && g0 == 0) wait_rows(p.ws, tag);
  }
#define PP_LOOP(TXT)                                                                                                              \
  asm volatile(TXT                                                                                                                \
      : "+{v[128:159]}"(acc)                                                                                                      \
      : W4_IN, "{v[240:247]}"(la), "{v[248:251]}"(vx), "{v252}"(xw), "{v253}"(xw ^ 8192),                                         \
        "{v172}"((ISV ? 2 * cg + kh : 4 * kh + cg) * 1024 + lane * 16), "{v174}"(tid == 256 ? fkq(h, which) : 0x7F000000),        \
        [rt] "s"(raw_rsrc(p.ws + WS_T, T_BYTES)), [rs] "s"(raw_rsrc(p.ws + WS_SY, SY_TOTAL)), [s0] "s"(p.s0 ^ TF_KQV),            \
        [lw] "s"(lw), [kh] "s"(kh), [g0] "n"(PR_G1), [x0] "s"(xn + (PR_G1 + 1) * (SEG * XLD * 2)),                                \
        [t0] "s"(which * T_Q + kseg(0, h) - 1)                                                                                    \
      : CLB8(160, 161, 162, 163, 164, 165, 166, 167), "v168", "v169", "v170", "v171", "v173", CLB8(176, 177, 178, 179, 180, 181, 182, 183), \
        CLB8(184, 185, 186, 187, 188, 189, 190, 191), CLB32(192), CLB8(224, 225, 226, 227, 228, 229, 230, 231),                   \
        CLB8(232, 233, 234, 235, 236, 237, 238, 239), "s86", "s87", "s90", "s91", "s92", "s93", "s94", "scc", "memory")
  if (ISV) PP_LOOP(PP_ASM_1); else PP_LOOP(PP_ASM_0);
}

enum { J_NONE = 0, J_GU = 3, J_PR = 5, J_CB = 8, J_DN = 14 };
struct BigJob { int type, panel, idx; };
struct BigPtr { const char *a, *b0, *b1; };
constexpr int G_LDA = XLD * 2, G_LDB = FF * 2;

__device__ __forceinline__ BigPtr big_ptrs(const Args& p, const BigJob& j) {
  BigPtr r;
  r.a = p.ws + WS_XN + (size_t)j.panel * 128 * G_LDA;
  r.b0 = (const char*)(p.wg + j.idx * 128); r.b1 = (const char*)(p.wu + j.idx * 128);
  return r;
}
__device__ __forceinline__ void big_publish(const Args& p, const BigJob& j) {
  int tid = threadIdx.x;
  asm volatile("" : "+v"(tid));
  if (j.type == J_GU && tid == 0)
    __hip_atomic_fetch_add((gu32*)(p.ws + WS_SY + SY_CH) + j.panel * 16, 1u, __ATOMIC_RELAXED, __HIP_MEMORY_SCOPE_AGENT);
}
typedef f32x4 Acc[2][2][4][2];
__device__ __forceinline__ void big_epilogue(const Args& p, const BigJob& j, Acc& acc, char* smem) {
  int tid = threadIdx.x;
  asm volatile("" : "+v"(tid));
  const int lane = tid & 63;
  const int wid = __builtin_amdgcn_readfirstlane(tid >> 6);
  const int wr = wid >> 2, wc = wid & 3;
  {
    const __amdgpu_buffer_rsrc_t ro = mkrsrc(p.ws + WS_HF, S_LEN * 16384);
#pragma unroll
    for (int i = 0; i < 8; ++i) {
      f32x4 hh[2];
#pragma unroll
      for (int n = 0; n < 2; ++n)
#pragma unroll
        for (int jj = 0; jj < 4; ++jj) {
          const float g = acc[i >> 2][0][i & 3][n][jj], u = acc[i >> 2][1][i & 3][n][jj];
          hh[n][jj] = __builtin_amdgcn_rcpf(1.0f + __builtin_amdgcn_exp2f(g * -1.4426950408889634f)) * u;
        }
      st_sc1(ro, h_off(p, j.panel + (i >> 2)) + (j.idx * 4 + wc) * 16384 + (wr * 4 + (i & 3)) * 1024 + lane * 16,
             ((u32x4){cvtpk(hh[0][0], hh[0][1]), cvtpk(hh[0][2], hh[0][3]), cvtpk(hh[1][0], hh[1][1]), cvtpk(hh[1][2], hh[1][3])}));
    }
  }
}
__device__ __forceinline__ void gemm_role(Args& p, int g, char* smem) {
  const __amdgpu_buffer_rsrc_t rsy = mkrsrc(p.ws + WS_SY, SY_TOTAL);
  const int x = g & 3, k = g >> 2;
  volatile unsigned* slot = reinterpret_cast<volatile unsigned*>(smem + 163824);
  int rnd = k < 24 ? 2 : -1;
  auto claim = [&]() {
    ++rnd;
    const int n = k < 24 ? 32 + k : rnd * 8 + k - 24, pn = (n * 37) >> 8;
    BigJob b;
    b.type = rnd < 4 ? J_GU : (rnd == 4 ? J_DN : (rnd == 5 && k >= 24 && k - 24 < 14 - NFUSE ? J_CB : J_NONE));
    b.panel = rnd < 4 ? 14 - 2 * pn : (rnd == 4 ? 4 * ((2 - (k >> 3)) & 3) + x : NFUSE + k - 24);
    b.idx = rnd < 4 ? x + 4 * (n - 7 * pn) : k & 7;
    return b;
  };
  if (k < 24) {
    if (k >= 16) job_proj<true>(p, g, 2, x * 8 + (k & 7), smem, rsy);
    else job_proj<false>(p, g, k >> 3, x * 8 + (k & 7), smem, rsy);
  } else {
    u32x2 tag;
#pragma unroll 1
    for (int u = 0; u < (k < 30 && LN_M ? 2 : 1); ++u) {
      const int rg = u ? 32 + (4 * (k - 24) + 1) * 4 + x : g - 96;
      tag = start_rows<0>(p, rg, 1, []() {});
      WAIT_V(0);
      LBAR();
      report_rows(p, rg, tag);
    }
#pragma unroll 1
    for (int i = 0; i < PF_N; ++i) {
      const int m = i % 3, o = ((i / 3) * 32 + g - 96) * 448 + (int)threadIdx.x;
      if (threadIdx.x < 448) touch_line((const char*)(m == 0 ? p.wg : (m == 1 ? p.wu : p.wd)) + (size_t)o * 128, smem + (threadIdx.x >> 6) * 256);
      WAIT_V(1);
    }
    WAIT_V(0);
    if (threadIdx.x < 64) {
      wait_rows(p.ws, tag);
      Boot bt;
      boot_ask(p.ws, bt);
      WAIT_V(0);
      *slot = boot_done(p.ws, bt, true, tag);
    }
    LBAR();
    p.s0 = *slot >> 31;
  }
  p.s0 = uni(p.s0);
  BigJob cur = claim();
  while (cur.type != J_NONE) {
    if (cur.type != J_GU) {
      if (cur.type == J_DN) job_down(p, cur.panel, cur.idx, smem, rsy);
      else job_comb(p, cur.panel, x, rsy);
      cur = claim();
      continue;
    }
    int tid = threadIdx.x;
    asm volatile("" : "+v"(tid));
    const int lane = tid & 63;
    const int wid = __builtin_amdgcn_readfirstlane(tid >> 6);
    const int wr = wid >> 2, wc = wid & 3;
    const unsigned ra = tid >> 3, ca = 16 * ((tid & 7) ^ ((tid >> 3) & 7)), rb = tid >> 4, cb = 16 * ((tid & 15) ^ swzB((tid >> 4) & 15));
    const unsigned lw = lds_off(smem) + wid * 1024;
#define B8_SA(b, h) (((b) * 2 + (h)) * 16384)
#define B8_SB(b, h) ((4 + (b) * 2 + (h)) * 16384)
#define B8_STA(b, h, P, kt) glds2<true>((P).a + (size_t)(h) * 128 * G_LDA + (kt) * 128, 64 * G_LDA, ra * G_LDA + ca, lw + B8_SA(b, h))
#define B8_STB(b, h, P, kt) glds2(((h) ? (P).b1 : (P).b0) + (size_t)(kt) * 64 * G_LDB, 32 * G_LDB, rb * G_LDB + cb, lw + B8_SB(b, h))
#define B8_LDA(b, h) do { _Pragma("unroll") for (int m = 0; m < 4; ++m) _Pragma("unroll") for (int kk = 0; kk < 2; ++kk) \
      At[m][kk] = ldsf(smem + B8_SA(b, h) + offA(wr * 64 + m * 16 + (lane & 15), kk * 4 + (lane >> 4))); } while (0)
#define B8_LDB(dst, b, h) do { _Pragma("unroll") for (int n = 0; n < 2; ++n) _Pragma("unroll") for (int kk = 0; kk < 2; ++kk) \
      dst[n][kk] = ld_bfrag(smem + B8_SB(b, h), kk, wc * 2 + n, lane); } while (0)
#define B8_MMA(ai, bj, Bt) do { __builtin_amdgcn_s_setprio(1); _Pragma("unroll") for (int m = 0; m < 4; ++m) _Pragma("unroll") for (int n = 0; n < 2; ++n) \
      _Pragma("unroll") for (int kk = 0; kk < 2; ++kk) acc[ai][bj][m][n] = MFMA(Bt[n][kk], At[m][kk], acc[ai][bj][m][n]); __builtin_amdgcn_s_setprio(0); } while (0)
#define WAIT_L(n) asm volatile("s_waitcnt lgkmcnt(%0)" :: "n"(n) : "memory")
#define BAR() __builtin_amdgcn_s_barrier()
#define WAIT_RX(c) asm volatile("s_waitcnt vmcnt(16)\n\ts_cmp_lg_u32 %0, 0\n\ts_cbranch_scc1 .Lrx%=\n\ts_waitcnt vmcnt(8)\n.Lrx%=:" :: "s"(c) : "memory", "scc")
    Acc acc;
    bf16x8 At[4][2], B0[2][2], B1[2][2];
    BigPtr cp = big_ptrs(p, cur);
    BigJob pend; pend.type = J_NONE; pend.panel = 0; pend.idx = 0;
    LBAR();
    B8_STB(0, 0, cp, 0); B8_STB(0, 1, cp, 0); B8_STA(0, 0, cp, 0); B8_STA(0, 1, cp, 0);
    if (wr == 1) BAR();
    WAIT_V(2); BAR();
    B8_STB(1, 0, cp, 1); B8_STA(1, 0, cp, 1); B8_STB(1, 1, cp, 1);
    WAIT_V(6); BAR();
    BigJob nx = pend;
    for (;;) {
      bool chain = false;
      BigPtr np = cp;
      constexpr int nt = DM / 64;
#pragma unroll
      for (int i = 0; i < 32; ++i) acc[i >> 4][(i >> 3) & 1][(i >> 1) & 3][i & 1] = (f32x4){0.f, 0.f, 0.f, 0.f};
#pragma unroll 1
      for (int t = 0; t < nt; t += 2) {
        const bool last = t == nt - 2;
        if (last) {
          nx = claim();
          chain = nx.type == J_GU;
          if (chain) np = big_ptrs(p, nx);
        }
        const BigPtr sp = np;
        const int t2 = last ? 0 : t + 2;
        if (t == 2) big_publish(p, pend);
        const int rx = __builtin_amdgcn_readfirstlane(t == 0 && pend.type != J_NONE);
        B8_LDB(B0, 0, 0); B8_LDB(B1, 0, 1); SCHED(); B8_LDA(0, 0); B8_STA(1, 1, cp, t + 1);
        WAIT_RX(rx);
        WAIT_L(0); BAR(); B8_MMA(0, 0, B0); B8_MMA(0, 1, B1); BAR(); SCHED();
        B8_LDA(0, 1); B8_STB(0, 0, sp, t2); B8_STB(0, 1, sp, t2); B8_STA(0, 0, sp, t2);
        WAIT_RX(rx);
        WAIT_L(0); BAR(); B8_MMA(1, 0, B0); B8_MMA(1, 1, B1); BAR(); SCHED();
        B8_LDB(B0, 1, 0); B8_LDB(B1, 1, 1); SCHED(); B8_LDA(1, 0); B8_STA(0, 1, sp, t2);
        WAIT_V(8); WAIT_L(0); BAR(); B8_MMA(0, 0, B0); B8_MMA(0, 1, B1); BAR(); SCHED();
        B8_LDA(1, 1); B8_STB(1, 0, sp, t2 + 1); B8_STB(1, 1, sp, t2 + 1); B8_STA(1, 0, sp, t2 + 1);
        WAIT_V(8); WAIT_L(0); BAR(); B8_MMA(1, 0, B0); B8_MMA(1, 1, B1); BAR(); SCHED();
      }
      if (wr == 0) BAR();
      big_epilogue(p, cur, acc, smem);
      if (!chain) break;
      pend = cur; cur = nx; cp = np;
      if (wr == 1) BAR();
    }
    WAIT_V(0);
    BAR();
    asm volatile(".Lcode_end:");
    big_publish(p, cur);
    cur = nx;
  }
}


__device__ __forceinline__ unsigned now_q() { return (unsigned)__builtin_amdgcn_s_memrealtime() * 4u; }
__device__ __forceinline__ void pace_until(unsigned t) {
#pragma unroll 1
  for (int i = 0; i < 256; ++i) {
    const int d = (int)(t - now_q());
    if (d <= 0) break;
    if (d > 32) __builtin_amdgcn_s_sleep(1);
  }
}
#define CLB_E "s76", "s77", "s78", "s79", "s80", "s81", "s82", "s83", "s84", "s85", "s86", "s87", "s88", "s89"
#define EDGE_ASM \
  "s_memtime s[76:77]\n\t" \
  "s_memrealtime s[78:79]\n\t" \
  "s_nop 3\n\t" \
  "s_memrealtime s[80:81]\n\t" \
  "s_nop 3\n\t" \
  "s_memrealtime s[82:83]\n\t" \
  "s_nop 3\n\t" \
  "s_memrealtime s[84:85]\n\t" \
  "s_nop 3\n\t" \
  "s_memrealtime s[86:87]\n\t" \
  "s_nop 3\n\t" \
  "s_memrealtime s[88:89]\n\t" \
  "s_mov_b32 s93, 0\n\t" \
  "s_waitcnt lgkmcnt(0)\n\t" \
  "s_cmp_eq_u32 s80, s78\n\t" \
  "s_addc_u32 s93, s93, 0\n\t" \
  "s_cmp_eq_u32 s82, s78\n\t" \
  "s_addc_u32 s93, s93, 0\n\t" \
  "s_cmp_eq_u32 s84, s78\n\t" \
  "s_addc_u32 s93, s93, 0\n\t" \
  "s_cmp_eq_u32 s86, s78\n\t" \
  "s_addc_u32 s93, s93, 0\n\t" \
  "s_cmp_eq_u32 s88, s78\n\t" \
  "s_addc_u32 s93, s93, 0\n\t" \
  "s_mul_i32 s93, s93, 21\n\t" \
  "s_add_u32 s76, s76, 15\n\t" \
  "s_add_u32 %[kc], s76, s93\n\t" \
  "s_lshl2_add_u32 %[kq], s78, 16\n\t"
__device__ __forceinline__ unsigned time_of(unsigned cyc) {
  unsigned c, q;
  asm volatile(EDGE_ASM : [kc] "=&s"(c), [kq] "=&s"(q) :: CLB_E, "s93", "scc");
  return q - (((c - cyc) * 11900u) >> 16);
}
#define DS_RED 20
#define DS_MEM 13
#define LEAD_DN 2
#define LEAD_UP 6
__device__ __forceinline__ u32x2 ld2_sc1(__amdgpu_buffer_rsrc_t rs, int off, int soff = 0) {
  return __builtin_amdgcn_raw_buffer_load_b64(rs, off, soff, 16);
}
__device__ __forceinline__ void st2_sc1(__amdgpu_buffer_rsrc_t rs, int off, u32x2 v) {
  __builtin_amdgcn_raw_buffer_store_b64(v, rs, off, 0, 16);
}
__device__ __forceinline__ int row_max(int v) {
  v = max(v, __builtin_amdgcn_update_dpp(0, v, 0x128, 0xf, 0xf, false));
  v = max(v, __builtin_amdgcn_update_dpp(0, v, 0x124, 0xf, 0xf, false));
  v = max(v, __builtin_amdgcn_update_dpp(0, v, 0x122, 0xf, 0xf, false));
  v = max(v, __builtin_amdgcn_update_dpp(0, v, 0x121, 0xf, 0xf, false));
  return v;
}
__device__ __forceinline__ int wave_max(int v) {
  v = row_max(v);
  return max(max(__builtin_amdgcn_readlane(v, 0), __builtin_amdgcn_readlane(v, 16)),
             max(__builtin_amdgcn_readlane(v, 32), __builtin_amdgcn_readlane(v, 48)));
}
__device__ __forceinline__ int track(int est, int x, int up, int dn, int out) {
  const int d = x - est;
  return est + (d > out ? 4 : max(min(d, up), -dn));
}
#define WSL 12
#define PLAN_SLEEP 2
#define REF_W 3
#define REF_G 60
#define RL0 315
__device__ __forceinline__ void watch(__amdgpu_buffer_rsrc_t rsy, int s, int lane, unsigned t0, const unsigned* tgt_, gu32* abortw, int hs) {
  const int rl = (int)__builtin_amdgcn_raw_buffer_load_b32(rsy, lane * 128, SY_BR + hs, 16);
  u32x2 ra;
  for (unsigned spins = 0;;) {
    asm volatile("" ::: "memory");
    ra = ld2_sc1(rsy, (lane & 31) * 128, SY_BA + hs);
    const bool all = __all((int)ra[0] >= s);
    if (all) break;
    if (spins < 3) __builtin_amdgcn_s_sleep(PLAN_SLEEP); else __builtin_amdgcn_s_sleep(WSL);
    if (spin_fail(spins, abortw)) break;
  }
  int d = ra[0] == (unsigned)s ? (int)(ra[1] - t0) : 0;
  d = max(d, __builtin_amdgcn_update_dpp(0, d, 0xB1, 0xf, 0xf, false));
  d = max(d, __builtin_amdgcn_update_dpp(0, d, 0x4E, 0xf, 0xf, false));
  int sum = d + __builtin_amdgcn_update_dpp(0, d, 0x128, 0xf, 0xf, false);
  sum += __builtin_amdgcn_update_dpp(0, sum, 0x124, 0xf, 0xf, false);
  const int mx = wave_max(d), mean = (__builtin_amdgcn_readlane(sum, 0) + __builtin_amdgcn_readlane(sum, 16)) >> 3;
  const unsigned t = t0 + mx - min(((mx - mean) * REF_W) >> 2, REF_G) + wave_max(rl < 0 ? RL0 : rl) + (DS_RED + DS_MEM);
  lds_vu_ptr tgt = lds_at(tgt_);
  if (lane < 2) tgt[lane] = lane ? t : s + 1;
}
__device__ __forceinline__ unsigned ld_flag(__amdgpu_buffer_rsrc_t rs, int off) {
  return __builtin_amdgcn_raw_buffer_load_b32(rs, off, 0, 16);
}

constexpr unsigned long long QK_MAP[8] = {0xbaf9876d543210ecull, 0xa98f76d543210bceull, 0x98f76d543210ecbaull, 0x876fd543210bcea9ull, 0x76fd543210ecba98ull, 0x65df43210bcea987ull, 0x5df43210ecba9876ull, 0xd43f210bcea98765ull};
#define QK_ASM_0 \
  "s_waitcnt lgkmcnt(0)\n\t" \
  "v_subrev_u32 v244, 0x8000, v250\n\t" \
  "ds_read_b128 v[216:219], v248 offset:32768\n\t" \
  "ds_read_b128 v[220:223], v248 offset:33792\n\t" \
  "ds_read_b128 v[224:227], v248 offset:34816\n\t" \
  "ds_read_b128 v[228:231], v248 offset:35840\n\t" \
  "ds_read_b128 v[208:211], v244 offset:0\n\t" \
  "ds_read_b128 v[212:215], v244 offset:1024\n\t" \
  "ds_read_b128 v[236:239], v244 offset:2048\n\t" \
  "ds_read_b128 v[240:243], v244 offset:3072\n\t" \
  "v_mov_b64 v[192:193], 0\n\t" \
  "v_mov_b64 v[194:195], 0\n\t" \
  "v_mov_b64 v[196:197], 0\n\t" \
  "v_mov_b64 v[198:199], 0\n\t" \
  "s_movk_i32 s92, 0x2000\n\t" \
  ".Lwt%=:\n\t" \
  "ds_read_b64 v[200:201], v248 offset:30720\n\t" \
  "s_memrealtime s[90:91]\n\t" \
  "s_sub_u32 s92, s92, 1\n\t" \
  "s_waitcnt lgkmcnt(0)\n\t" \
  "v_readfirstlane_b32 s93, v200\n\t" \
  "v_readfirstlane_b32 %[tgt], v201\n\t" \
  "s_cbranch_scc1 .Lgo%=\n\t" \
  "s_cmp_eq_u32 s93, %[step]\n\t" \
  "s_cbranch_scc1 .Lwx%=\n\t" \
  "s_sleep 1\n\t" \
  "s_branch .Lwt%=\n\t" \
  ".Lwx%=:\n\t" \
  "s_lshl_b32 s93, s90, 2\n\t" \
  "s_sub_i32 s93, %[tgt], s93\n\t" \
  "s_cmp_le_i32 s93, 0\n\t" \
  "s_cbranch_scc1 .Lgo%=\n\t" \
  "s_min_i32 s93, s93, 8000\n\t" \
  "s_mov_b32 s94, s93\n\t" \
  ".Lsl%=:\n\t" \
  "s_sub_i32 s94, s94, 18\n\t" \
  "s_cmp_lt_i32 s94, 30\n\t" \
  "s_cbranch_scc1 .Lsx%=\n\t" \
  "s_sleep 1\n\t" \
  "s_branch .Lsl%=\n\t" \
  ".Lsx%=:\n\t" \
  "s_sub_i32 s93, s93, 12\n\t" \
  "s_max_i32 s93, s93, 4\n\t" \
  "s_lshr_b32 s93, s93, 2\n\t" \
  "s_add_u32 s93, s93, s90\n\t" \
  "s_memrealtime s[90:91]\n\t" \
  ".Lpace%=:\n\t" \
  "s_memrealtime s[94:95]\n\t" \
  "s_waitcnt lgkmcnt(1)\n\t" \
  "s_sub_i32 s92, s90, s93\n\t" \
  "s_cmp_ge_i32 s92, 0\n\t" \
  "s_cbranch_scc1 .Le%=\n\t" \
  "s_memrealtime s[90:91]\n\t" \
  "s_waitcnt lgkmcnt(1)\n\t" \
  "s_sub_i32 s92, s94, s93\n\t" \
  "s_cmp_lt_i32 s92, 0\n\t" \
  "s_cbranch_scc1 .Lpace%=\n\t" \
  "s_nop 4\n\t" \
  ".Le%=:\n\t" \
  "s_add_u32 s92, s92, s93\n\t" \
  "s_lshl_b32 s92, s92, 2\n\t" \
  "s_sub_i32 s92, %[tgt], s92\n\t" \
  "s_max_i32 s92, s92, 0\n\t" \
  "s_bitcmp0_b32 s92, 3\n\t" \
  "s_cbranch_scc1 .Lf3%=\n\t" \
  "s_nop 15\n\t" \
  ".Lf3%=:\n\t" \
  "s_bitcmp0_b32 s92, 2\n\t" \
  "s_cbranch_scc1 .Lf2%=\n\t" \
  "s_nop 10\n\t" \
  ".Lf2%=:\n\t" \
  ".Lgo%=:\n\t" \
  "s_mov_b32 s92, 0\n\t" \
  ".Lpoll%=:\n\t" \
  "buffer_load_dwordx4 v[200:203], v248, %[rs], %[som] offen sc1\n\t" \
  "buffer_load_dwordx4 v[204:207], v248, %[rs], %[som2] offen sc1\n\t" \
  "s_waitcnt vmcnt(1) lgkmcnt(0)\n\t" \
  "ds_write_b128 v249, v[200:203]\n\t" \
  "v_smfmac_f32_16x16x64_bf16 v[192:195], v[200:203], v[208:215], v251\n\t" \
  "v_max3_u32 v244, v200, v201, v202\n\t" \
  "v_smfmac_f32_16x16x64_bf16 v[196:199], v[200:203], v[236:243], v251\n\t" \
  "s_waitcnt vmcnt(0)\n\t" \
  "ds_write_b128 v249, v[204:207] offset:8192\n\t" \
  "v_smfmac_f32_16x16x64_bf16 v[192:195], v[204:207], v[216:223], v251\n\t" \
  "v_max3_u32 v244, v244, v203, v204\n\t" \
  "v_max3_u32 v244, v244, v205, v206\n\t" \
  "v_smfmac_f32_16x16x64_bf16 v[196:199], v[204:207], v[224:231], v251\n\t" \
  "v_max_u32 v244, v244, v207\n\t" \
  "v_cmp_le_u32 vcc, %[sent], v244\n\t" \
  "s_cbranch_vccnz .Lslow%=\n\t" \
  ".Lok%=:\n\t" \
  "ds_read_b128 v[216:219], v250 offset:32768\n\t" \
  "ds_read_b128 v[220:223], v250 offset:33792\n\t" \
  "ds_read_b128 v[224:227], v250 offset:34816\n\t" \
  "ds_read_b128 v[228:231], v250 offset:35840\n\t" \
  "s_waitcnt lgkmcnt(4)\n\t" \
  "s_barrier\n\t" \
  "ds_read_b128 v[208:211], v248 offset:1024\n\t" \
  "ds_read_b128 v[212:215], v248 offset:2048\n\t" \
  "ds_read_b128 v[236:239], v248 offset:3072\n\t" \
  "ds_read_b128 v[240:243], v248 offset:4096\n\t" \
  "s_cmp_eq_u32 %[step], 63\n\t" \
  "s_cbranch_scc1 .Lls%=\n\t" \
  "s_waitcnt lgkmcnt(3)\n\t" \
  "ds_read_b128 v[244:247], v248 offset:13312\n\t" \
  "v_smfmac_f32_16x16x64_bf16 v[192:195], v[208:211], v[216:223], v251\n\t" \
  "ds_read_b128 v[216:219], v250 offset:0\n\t" \
  "v_smfmac_f32_16x16x64_bf16 v[196:199], v[208:211], v[224:231], v251\n\t" \
  "s_waitcnt lgkmcnt(4)\n\t" \
  "ds_read_b128 v[220:223], v250 offset:1024\n\t" \
  "v_smfmac_f32_16x16x64_bf16 v[192:195], v[212:215], v[0:7], v251\n\t" \
  "ds_read_b128 v[224:227], v250 offset:2048\n\t" \
  "v_smfmac_f32_16x16x64_bf16 v[196:199], v[212:215], v[8:15], v251\n\t" \
  "s_waitcnt lgkmcnt(5)\n\t" \
  "ds_read_b128 v[228:231], v250 offset:3072\n\t" \
  "v_smfmac_f32_16x16x64_bf16 v[192:195], v[236:239], v[16:23], v251\n\t" \
  "ds_read_b128 v[200:203], v248 offset:5120\n\t" \
  "v_smfmac_f32_16x16x64_bf16 v[196:199], v[236:239], v[24:31], v251\n\t" \
  "s_waitcnt lgkmcnt(6)\n\t" \
  "ds_read_b128 v[204:207], v248 offset:6144\n\t" \
  "v_smfmac_f32_16x16x64_bf16 v[192:195], v[240:243], v[32:39], v251\n\t" \
  "ds_read_b128 v[208:211], v248 offset:7168\n\t" \
  "v_smfmac_f32_16x16x64_bf16 v[196:199], v[240:243], v[40:47], v251\n\t" \
  "s_waitcnt lgkmcnt(5)\n\t" \
  "ds_read_b128 v[212:215], v248 offset:9216\n\t" \
  "v_smfmac_f32_16x16x64_bf16 v[192:195], v[244:247], v[216:223], v251\n\t" \
  "s_waitcnt lgkmcnt(4)\n\t" \
  "v_smfmac_f32_16x16x64_bf16 v[196:199], v[244:247], v[224:231], v251\n\t" \
  "s_waitcnt lgkmcnt(3)\n\t" \
  "ds_read_b128 v[236:239], v248 offset:10240\n\t" \
  "v_smfmac_f32_16x16x64_bf16 v[192:195], v[200:203], v[48:55], v251\n\t" \
  "ds_read_b128 v[240:243], v248 offset:11264\n\t" \
  "v_smfmac_f32_16x16x64_bf16 v[196:199], v[200:203], v[56:63], v251\n\t" \
  "s_waitcnt lgkmcnt(4)\n\t" \
  "v_smfmac_f32_16x16x64_bf16 v[192:195], v[204:207], v[64:71], v251\n\t" \
  "ds_read_b128 v[244:247], v248 offset:12288\n\t" \
  "v_smfmac_f32_16x16x64_bf16 v[196:199], v[204:207], v[72:79], v251\n\t" \
  "s_waitcnt lgkmcnt(4)\n\t" \
  "v_smfmac_f32_16x16x64_bf16 v[192:195], v[208:211], v[80:87], v251\n\t" \
  "ds_read_b128 v[200:203], v248 offset:14336\n\t" \
  "v_smfmac_f32_16x16x64_bf16 v[196:199], v[208:211], v[88:95], v251\n\t" \
  "s_waitcnt lgkmcnt(4)\n\t" \
  "v_smfmac_f32_16x16x64_bf16 v[192:195], v[212:215], v[96:103], v251\n\t" \
  "ds_read_b128 v[204:207], v248 offset:15360\n\t" \
  "v_smfmac_f32_16x16x64_bf16 v[196:199], v[212:215], v[104:111], v251\n\t" \
  "s_waitcnt lgkmcnt(3)\n\t" \
  "v_smfmac_f32_16x16x64_bf16 v[192:195], v[236:239], v[112:119], v251\n\t" \
  "v_smfmac_f32_16x16x64_bf16 v[196:199], v[236:239], v[120:127], v251\n\t" \
  "v_smfmac_f32_16x16x64_bf16 v[192:195], v[240:243], v[128:135], v251\n\t" \
  "v_smfmac_f32_16x16x64_bf16 v[196:199], v[240:243], v[136:143], v251\n\t" \
  "s_waitcnt lgkmcnt(1)\n\t" \
  "v_smfmac_f32_16x16x64_bf16 v[192:195], v[244:247], v[144:151], v251\n\t" \
  "v_smfmac_f32_16x16x64_bf16 v[196:199], v[244:247], v[152:159], v251\n\t" \
  "v_smfmac_f32_16x16x64_bf16 v[192:195], v[200:203], v[160:167], v251\n\t" \
  "v_smfmac_f32_16x16x64_bf16 v[196:199], v[200:203], v[168:175], v251\n\t" \
  "s_waitcnt lgkmcnt(0)\n\t" \
  "v_smfmac_f32_16x16x64_bf16 v[192:195], v[204:207], v[176:183], v251\n\t" \
  "v_smfmac_f32_16x16x64_bf16 v[196:199], v[204:207], v[184:191], v251\n\t" \
  "ds_read_b128 v[200:203], v248 offset:16384\n\t" \
  "ds_read_b128 v[204:207], v248 offset:20480\n\t" \
  "v_mov_b32 v250, v232\n\t" \
  "v_mov_b32 v249, v233\n\t" \
  "s_nop 2\n\t" \
  "v_add_f32 v192, v192, v194\n\t" \
  "v_add_f32 v193, v193, v195\n\t" \
  "v_add_f32 v196, v196, v198\n\t" \
  "v_add_f32 v197, v197, v199\n\t" \
  "v_cvt_pk_bf16_f32 v192, v192, v193\n\t" \
  "v_cvt_pk_bf16_f32 v196, v196, v197\n\t" \
  "ds_write2_b32 v252, v192, v196 offset1:64\n\t" \
  "v_mov_b32 v251, v234\n\t" \
  "ds_read_b64_tr_b16 v[232:233], v250 offset:0\n\t" \
  "ds_read_b64_tr_b16 v[234:235], v250 offset:64\n\t" \
  "v_mov_b32 v212, %[step]\n\t" \
  "s_mov_b64 exec, 1\n\t" \
  "ds_write_b32 v248, v212 offset:28672\n\t" \
  "s_mov_b64 exec, -1\n\t" \
  "buffer_load_dwordx4 v[192:195], v248, %[rt], %[svt] offen offset:0 sc1\n\t" \
  "buffer_load_dwordx4 v[196:199], v248, %[rt], %[svt] offen offset:1024 sc1\n\t" \
  "s_waitcnt lgkmcnt(1)\n\t" \
  "v_mfma_f32_16x16x32_bf16 v[200:203], v[200:203], v[232:235], 0\n\t" \
  "v_mfma_f32_16x16x32_bf16 v[204:207], v[204:207], v[232:235], 0\n\t" \
  "s_nop 7\n\t" \
  "v_cndmask_b32 v216, v200, v204, %[nh]\n\t" \
  "v_cndmask_b32 v217, v201, v205, %[nh]\n\t" \
  "v_cndmask_b32 v218, v202, v206, %[nh]\n\t" \
  "v_cndmask_b32 v219, v203, v207, %[nh]\n\t" \
  "ds_write_b128 v248, v[216:219] offset:16384\n\t" \
  "buffer_load_dword v252, v249, %[rs], %[sv] offen offset:0 sc1\n\t" \
  "buffer_load_dword v253, v249, %[rs], %[sv] offen offset:256 sc1\n\t" \
  "ds_read_b32 v212, v248 offset:28672\n\t" \
  "ds_read_b64_tr_b16 v[236:237], v250 offset:512\n\t" \
  "ds_read_b64_tr_b16 v[238:239], v250 offset:576\n\t" \
  "ds_read_b64_tr_b16 v[240:241], v250 offset:1024\n\t" \
  "ds_read_b64_tr_b16 v[242:243], v250 offset:1088\n\t" \
  "ds_read_b64_tr_b16 v[244:245], v250 offset:1536\n\t" \
  "ds_read_b64_tr_b16 v[246:247], v250 offset:1600\n\t" \
  "s_setprio 3\n\t" \
  "s_waitcnt lgkmcnt(0)\n\t" \
  "v_cmp_ne_u32 vcc, %[step], v212\n\t" \
  "s_barrier\n\t" \
  "s_and_b32 s93, vcc_lo, 0xf\n\t" \
  "s_cbranch_scc1 .Lre%=\n\t" \
  ".Lrb%=:\n\t" \
  "ds_read_b64_tr_b16 v[200:201], v250 offset:2048\n\t" \
  "ds_read_b64_tr_b16 v[202:203], v250 offset:2112\n\t" \
  "ds_read_b64_tr_b16 v[204:205], v250 offset:2560\n\t" \
  "ds_read_b64_tr_b16 v[206:207], v250 offset:2624\n\t" \
  "ds_read_b128 v[220:223], v248 offset:17408\n\t" \
  "ds_read_b128 v[224:227], v248 offset:18432\n\t" \
  "ds_read_b128 v[228:231], v248 offset:19456\n\t" \
  "ds_read_b64_tr_b16 v[208:209], v250 offset:3072\n\t" \
  "ds_read_b64_tr_b16 v[210:211], v250 offset:3136\n\t" \
  "ds_read_b64_tr_b16 v[212:213], v250 offset:3584\n\t" \
  "ds_read_b64_tr_b16 v[214:215], v250 offset:3648\n\t" \
  "s_waitcnt lgkmcnt(9)\n\t" \
  "v_mfma_f32_16x16x32_bf16 v[200:203], v[200:203], v[232:235], 0\n\t" \
  "s_waitcnt lgkmcnt(7)\n\t" \
  "v_mfma_f32_16x16x32_bf16 v[200:203], v[204:207], v[236:239], v[200:203]\n\t" \
  "s_waitcnt lgkmcnt(6)\n\t" \
  "v_pk_add_f32 v[216:217], v[216:217], v[220:221]\n\t" \
  "v_pk_add_f32 v[218:219], v[218:219], v[222:223]\n\t" \
  "s_waitcnt lgkmcnt(4)\n\t" \
  "v_pk_add_f32 v[224:225], v[224:225], v[228:229]\n\t" \
  "v_pk_add_f32 v[226:227], v[226:227], v[230:231]\n\t" \
  "v_pk_add_f32 v[216:217], v[216:217], v[224:225]\n\t" \
  "v_pk_add_f32 v[218:219], v[218:219], v[226:227]\n\t" \
  "s_waitcnt lgkmcnt(2)\n\t" \
  "v_mfma_f32_16x16x32_bf16 v[200:203], v[208:211], v[240:243], v[200:203]\n\t" \
  "v_max3_f32 v224, v216, v217, v218\n\t" \
  "v_max_f32 v224, v224, v219\n\t" \
  "s_waitcnt lgkmcnt(0)\n\t" \
  "v_mfma_f32_16x16x32_bf16 v[200:203], v[212:215], v[244:247], v[200:203]\n\t" \
  "v_max_f32_dpp v225, v224, v224 row_ror:8 row_mask:0xf bank_mask:0xf\n\t" \
  "v_max_f32_dpp v226, v224, v224 row_ror:8 row_mask:0xf bank_mask:0xf\n\t" \
  "s_nop 1\n\t" \
  "v_permlane32_swap_b32 v225, v226\n\t" \
  "v_max_f32 v231, v225, v226\n\t" \
  "v_mov_b32 v220, 0x3f803f80\n\t" \
  "v_mov_b32 v221, 0x3f803f80\n\t" \
  "v_mov_b64 v[222:223], v[220:221]\n\t" \
  "v_mov_b32 v233, 0\n\t" \
  "v_mov_b32 v234, 0\n\t" \
  "v_max3_f32 v204, v200, v201, v231\n\t" \
  "v_max3_f32 v205, v204, v202, v203\n\t" \
  "v_max3_f32 v206, v204, v202, v203\n\t" \
  "v_cndmask_b32 v200, v200, v202, %[up]\n\t" \
  "v_cndmask_b32 v201, v201, v203, %[up]\n\t" \
  "v_permlane16_swap_b32 v205, v206\n\t" \
  "v_max_f32 v204, v205, v206\n\t" \
  "v_mul_f32 v204, %[negc], v204\n\t" \
  "v_fma_f32 v200, v200, %[c], v204\n\t" \
  "v_fma_f32 v201, v201, %[c], v204\n\t" \
  "v_exp_f32 v200, v200\n\t" \
  "v_fma_f32 v216, v216, %[c], v204\n\t" \
  "v_exp_f32 v201, v201\n\t" \
  "v_fma_f32 v217, v217, %[c], v204\n\t" \
  "v_exp_f32 v216, v216\n\t" \
  "v_fma_f32 v218, v218, %[c], v204\n\t" \
  "v_cvt_pk_bf16_f32 v232, v200, v201\n\t" \
  "v_cvt_pk_bf16_f32 v235, v200, v201\n\t" \
  "v_exp_f32 v217, v217\n\t" \
  "v_fma_f32 v219, v219, %[c], v204\n\t" \
  "v_mfma_f32_16x16x16_bf16 v[212:215], v[220:221], v[232:233], 0\n\t" \
  "v_exp_f32 v218, v218\n\t" \
  "s_waitcnt vmcnt(0)\n\t" \
  "v_mfma_f32_16x16x16_bf16 v[224:227], v[252:253], v[232:233], 0\n\t" \
  "v_cvt_pk_bf16_f32 v210, v216, v217\n\t" \
  "v_cvt_pk_bf16_f32 v208, v216, v217\n\t" \
  "v_exp_f32 v219, v219\n\t" \
  "v_mfma_f32_16x16x16_bf16 v[228:231], v[252:253], v[234:235], 0\n\t" \
  "v_cvt_pk_bf16_f32 v211, v218, v219\n\t" \
  "v_cvt_pk_bf16_f32 v209, v218, v219\n\t" \
  "v_mov_b32_dpp v208, v210 row_ror:8 row_mask:0xf bank_mask:0xc\n\t" \
  "v_mov_b32_dpp v210, v210 row_ror:8 row_mask:0xf bank_mask:0x3\n\t" \
  "v_mov_b32_dpp v209, v211 row_ror:8 row_mask:0xf bank_mask:0xc\n\t" \
  "v_mov_b32_dpp v211, v211 row_ror:8 row_mask:0xf bank_mask:0x3\n\t" \
  "v_max_u32 v204, v252, v253\n\t" \
  "v_cmp_le_u32 vcc, %[sent], v204\n\t" \
  "v_mfma_f32_16x16x32_bf16 v[212:215], v[220:223], v[208:211], v[212:215]\n\t" \
  ".Lpv%=:\n\t" \
  "v_mfma_f32_16x16x32_bf16 v[224:227], v[192:195], v[208:211], v[224:227]\n\t" \
  "v_mfma_f32_16x16x32_bf16 v[228:231], v[196:199], v[208:211], v[228:231]\n\t" \
  "s_cbranch_vccnz .Lvs%=\n\t" \
  ".Lvok%=:\n\t" \
  "s_nop 4\n\t" \
  "v_rcp_f32 v204, v212\n\t" \
  "v_cndmask_b32 v224, v224, v226, %[nh]\n\t" \
  "v_cndmask_b32 v225, v225, v227, %[nh]\n\t" \
  "v_cndmask_b32 v228, v228, v230, %[nh]\n\t" \
  "v_cndmask_b32 v229, v229, v231, %[nh]\n\t" \
  "v_pk_mul_f32 v[224:225], v[224:225], v[204:205] op_sel_hi:[1,0]\n\t" \
  "v_pk_mul_f32 v[228:229], v[228:229], v[204:205] op_sel_hi:[1,0]\n\t" \
  "v_cvt_pk_bf16_f32 v224, v224, v225\n\t" \
  "v_cvt_pk_bf16_f32 v225, v228, v229\n\t" \
  "buffer_store_dwordx2 v[224:225], v251, %[rs], %[so] offen sc1\n\t" \
  "s_memtime s[90:91]\n\t" \
  "s_setprio 0\n\t" \
  "s_waitcnt lgkmcnt(0)\n\t" \
  "s_mov_b32 %[tout], s90\n\t" \
  "s_mov_b32 %[spins], s92\n\t" \
  "s_branch .Lend%=\n\t" \
  ".Lls%=:\n\t" \
  "s_waitcnt lgkmcnt(0)\n\t" \
  "s_barrier\n\t" \
  "s_mov_b32 %[spins], 0\n\t" \
  "s_branch .Lend%=\n\t" \
  ".Lre%=:\n\t" \
  "ds_read_b64_tr_b16 v[236:237], v250 offset:512\n\t" \
  "ds_read_b64_tr_b16 v[238:239], v250 offset:576\n\t" \
  "ds_read_b64_tr_b16 v[240:241], v250 offset:1024\n\t" \
  "ds_read_b64_tr_b16 v[242:243], v250 offset:1088\n\t" \
  "ds_read_b64_tr_b16 v[244:245], v250 offset:1536\n\t" \
  "ds_read_b64_tr_b16 v[246:247], v250 offset:1600\n\t" \
  "s_waitcnt lgkmcnt(0)\n\t" \
  "s_branch .Lrb%=\n\t" \
  ".Lvs%=:\n\t" \
  "v_cmp_eq_u32 vcc, %[sent], v252\n\t" \
  "v_cmp_eq_u32 s[94:95], %[sent], v253\n\t" \
  "s_or_b64 s[94:95], s[94:95], vcc\n\t" \
  "s_cbranch_scc0 .Lvok%=\n\t" \
  "s_add_u32 s92, s92, 1\n\t" \
  "s_sleep 2\n\t" \
  "s_and_b32 s93, s92, 63\n\t" \
  "s_cbranch_scc1 .Lvr%=\n\t" \
  "buffer_load_dword v204, off, %[rs], %[sab] sc1\n\t" \
  "s_waitcnt vmcnt(0)\n\t" \
  "v_readfirstlane_b32 s93, v204\n\t" \
  "s_nop 3\n\t" \
  "s_cmp_eq_u32 s93, 1\n\t" \
  "s_cbranch_scc1 .Lvok%=\n\t" \
  "s_cmp_lt_u32 s92, 0x80000\n\t" \
  "s_cbranch_scc1 .Lvr%=\n\t" \
  "v_mov_b32 v204, 1\n\t" \
  "buffer_store_dword v204, off, %[rs], %[sab] sc1\n\t" \
  "s_branch .Lvok%=\n\t" \
  ".Lvr%=:\n\t" \
  "buffer_load_dword v252, v249, %[rs], %[sv] offen offset:0 sc1\n\t" \
  "buffer_load_dword v253, v249, %[rs], %[sv] offen offset:256 sc1\n\t" \
  "s_waitcnt vmcnt(0)\n\t" \
  "v_mfma_f32_16x16x16_bf16 v[224:227], v[252:253], v[232:233], 0\n\t" \
  "v_mfma_f32_16x16x16_bf16 v[228:231], v[252:253], v[234:235], 0\n\t" \
  "v_max_u32 v204, v252, v253\n\t" \
  "v_cmp_le_u32 vcc, %[sent], v204\n\t" \
  "s_nop 4\n\t" \
  "s_branch .Lpv%=\n\t" \
  "s_branch .Lend%=\n\t" \
  ".Lslow%=:\n\t" \
  "s_mov_b64 s[94:95], 0\n\t" \
  "v_cmp_eq_u32 vcc, %[sent], v200\n\t" \
  "s_or_b64 s[94:95], s[94:95], vcc\n\t" \
  "v_cmp_eq_u32 vcc, %[sent], v201\n\t" \
  "s_or_b64 s[94:95], s[94:95], vcc\n\t" \
  "v_cmp_eq_u32 vcc, %[sent], v202\n\t" \
  "s_or_b64 s[94:95], s[94:95], vcc\n\t" \
  "v_cmp_eq_u32 vcc, %[sent], v203\n\t" \
  "s_or_b64 s[94:95], s[94:95], vcc\n\t" \
  "v_cmp_eq_u32 vcc, %[sent], v204\n\t" \
  "s_or_b64 s[94:95], s[94:95], vcc\n\t" \
  "v_cmp_eq_u32 vcc, %[sent], v205\n\t" \
  "s_or_b64 s[94:95], s[94:95], vcc\n\t" \
  "v_cmp_eq_u32 vcc, %[sent], v206\n\t" \
  "s_or_b64 s[94:95], s[94:95], vcc\n\t" \
  "v_cmp_eq_u32 vcc, %[sent], v207\n\t" \
  "s_or_b64 s[94:95], s[94:95], vcc\n\t" \
  "s_cmp_eq_u64 s[94:95], 0\n\t" \
  "s_cbranch_scc1 .Lok%=\n\t" \
  "v_mov_b64 v[192:193], 0\n\t" \
  "v_mov_b64 v[194:195], 0\n\t" \
  "v_mov_b64 v[196:197], 0\n\t" \
  "v_mov_b64 v[198:199], 0\n\t" \
  "s_add_u32 s92, s92, 1\n\t" \
  "s_cmp_eq_u32 s92, 1\n\t" \
  "s_cbranch_scc1 .Lpoll%=\n\t" \
  "s_sleep 2\n\t" \
  "s_and_b32 s93, s92, 63\n\t" \
  "s_cmp_lg_u32 s93, 0\n\t" \
  "s_cbranch_scc1 .Lpoll%=\n\t" \
  "v_mov_b32 v245, 0\n\t" \
  "buffer_load_dword v244, v245, %[rs], %[sab] offen sc1\n\t" \
  "s_waitcnt vmcnt(0)\n\t" \
  "v_readfirstlane_b32 s93, v244\n\t" \
  "s_nop 3\n\t" \
  "s_cmp_eq_u32 s93, 1\n\t" \
  "s_cbranch_scc1 .Lok%=\n\t" \
  "s_cmp_lt_u32 s92, 0x80000\n\t" \
  "s_cbranch_scc1 .Lpoll%=\n\t" \
  "v_mov_b32 v244, 1\n\t" \
  "buffer_store_dword v244, v245, %[rs], %[sab] offen sc1\n\t" \
  "s_branch .Lok%=\n\t" \
  ".Lend%=:\n\t"
#define QK_ASM_1 \
  "s_waitcnt lgkmcnt(0)\n\t" \
  "v_subrev_u32 v244, 0x8000, v250\n\t" \
  "ds_read_b128 v[216:219], v248 offset:36864\n\t" \
  "ds_read_b128 v[220:223], v248 offset:37888\n\t" \
  "ds_read_b128 v[224:227], v248 offset:38912\n\t" \
  "ds_read_b128 v[228:231], v248 offset:39936\n\t" \
  "ds_read_b128 v[208:211], v244 offset:0\n\t" \
  "ds_read_b128 v[212:215], v244 offset:1024\n\t" \
  "ds_read_b128 v[236:239], v244 offset:2048\n\t" \
  "ds_read_b128 v[240:243], v244 offset:3072\n\t" \
  "v_mov_b64 v[192:193], 0\n\t" \
  "v_mov_b64 v[194:195], 0\n\t" \
  "v_mov_b64 v[196:197], 0\n\t" \
  "v_mov_b64 v[198:199], 0\n\t" \
  "s_movk_i32 s92, 0x2000\n\t" \
  ".Lwt%=:\n\t" \
  "ds_read_b64 v[200:201], v248 offset:30720\n\t" \
  "s_memrealtime s[90:91]\n\t" \
  "s_sub_u32 s92, s92, 1\n\t" \
  "s_waitcnt lgkmcnt(0)\n\t" \
  "v_readfirstlane_b32 s93, v200\n\t" \
  "v_readfirstlane_b32 %[tgt], v201\n\t" \
  "s_cbranch_scc1 .Lgo%=\n\t" \
  "s_cmp_eq_u32 s93, %[step]\n\t" \
  "s_cbranch_scc1 .Lwx%=\n\t" \
  "s_sleep 1\n\t" \
  "s_branch .Lwt%=\n\t" \
  ".Lwx%=:\n\t" \
  "s_lshl_b32 s93, s90, 2\n\t" \
  "s_sub_i32 s93, %[tgt], s93\n\t" \
  "s_cmp_le_i32 s93, 0\n\t" \
  "s_cbranch_scc1 .Lgo%=\n\t" \
  "s_min_i32 s93, s93, 8000\n\t" \
  "s_mov_b32 s94, s93\n\t" \
  ".Lsl%=:\n\t" \
  "s_sub_i32 s94, s94, 18\n\t" \
  "s_cmp_lt_i32 s94, 30\n\t" \
  "s_cbranch_scc1 .Lsx%=\n\t" \
  "s_sleep 1\n\t" \
  "s_branch .Lsl%=\n\t" \
  ".Lsx%=:\n\t" \
  "s_sub_i32 s93, s93, 12\n\t" \
  "s_max_i32 s93, s93, 4\n\t" \
  "s_lshr_b32 s93, s93, 2\n\t" \
  "s_add_u32 s93, s93, s90\n\t" \
  "s_memrealtime s[90:91]\n\t" \
  ".Lpace%=:\n\t" \
  "s_memrealtime s[94:95]\n\t" \
  "s_waitcnt lgkmcnt(1)\n\t" \
  "s_sub_i32 s92, s90, s93\n\t" \
  "s_cmp_ge_i32 s92, 0\n\t" \
  "s_cbranch_scc1 .Le%=\n\t" \
  "s_memrealtime s[90:91]\n\t" \
  "s_waitcnt lgkmcnt(1)\n\t" \
  "s_sub_i32 s92, s94, s93\n\t" \
  "s_cmp_lt_i32 s92, 0\n\t" \
  "s_cbranch_scc1 .Lpace%=\n\t" \
  "s_nop 4\n\t" \
  ".Le%=:\n\t" \
  "s_add_u32 s92, s92, s93\n\t" \
  "s_lshl_b32 s92, s92, 2\n\t" \
  "s_sub_i32 s92, %[tgt], s92\n\t" \
  "s_max_i32 s92, s92, 0\n\t" \
  "s_bitcmp0_b32 s92, 3\n\t" \
  "s_cbranch_scc1 .Lf3%=\n\t" \
  "s_nop 15\n\t" \
  ".Lf3%=:\n\t" \
  "s_bitcmp0_b32 s92, 2\n\t" \
  "s_cbranch_scc1 .Lf2%=\n\t" \
  "s_nop 10\n\t" \
  ".Lf2%=:\n\t" \
  ".Lgo%=:\n\t" \
  "s_mov_b32 s92, 0\n\t" \
  ".Lpoll%=:\n\t" \
  "buffer_load_dwordx4 v[200:203], v248, %[rs], %[som] offen sc1\n\t" \
  "buffer_load_dwordx4 v[204:207], v248, %[rs], %[som2] offen sc1\n\t" \
  "s_waitcnt vmcnt(1) lgkmcnt(0)\n\t" \
  "ds_write_b128 v249, v[200:203]\n\t" \
  "v_smfmac_f32_16x16x64_bf16 v[192:195], v[200:203], v[208:215], v251\n\t" \
  "v_max3_u32 v244, v200, v201, v202\n\t" \
  "v_smfmac_f32_16x16x64_bf16 v[196:199], v[200:203], v[236:243], v251\n\t" \
  "s_waitcnt vmcnt(0)\n\t" \
  "ds_write_b128 v249, v[204:207] offset:8192\n\t" \
  "v_smfmac_f32_16x16x64_bf16 v[192:195], v[204:207], v[216:223], v251\n\t" \
  "v_max3_u32 v244, v244, v203, v204\n\t" \
  "v_max3_u32 v244, v244, v205, v206\n\t" \
  "v_smfmac_f32_16x16x64_bf16 v[196:199], v[204:207], v[224:231], v251\n\t" \
  "v_max_u32 v244, v244, v207\n\t" \
  "v_cmp_le_u32 vcc, %[sent], v244\n\t" \
  "s_cbranch_vccnz .Lslow%=\n\t" \
  ".Lok%=:\n\t" \
  "ds_read_b128 v[216:219], v250 offset:32768\n\t" \
  "ds_read_b128 v[220:223], v250 offset:33792\n\t" \
  "ds_read_b128 v[224:227], v250 offset:34816\n\t" \
  "ds_read_b128 v[228:231], v250 offset:35840\n\t" \
  "s_waitcnt lgkmcnt(4)\n\t" \
  "s_barrier\n\t" \
  "ds_read_b128 v[208:211], v248 offset:0\n\t" \
  "ds_read_b128 v[212:215], v248 offset:3072\n\t" \
  "ds_read_b128 v[236:239], v248 offset:4096\n\t" \
  "ds_read_b128 v[240:243], v248 offset:5120\n\t" \
  "s_cmp_eq_u32 %[step], 63\n\t" \
  "s_cbranch_scc1 .Lls%=\n\t" \
  "s_waitcnt lgkmcnt(3)\n\t" \
  "ds_read_b128 v[244:247], v248 offset:12288\n\t" \
  "v_smfmac_f32_16x16x64_bf16 v[192:195], v[208:211], v[216:223], v251\n\t" \
  "ds_read_b128 v[216:219], v250 offset:0\n\t" \
  "v_smfmac_f32_16x16x64_bf16 v[196:199], v[208:211], v[224:231], v251\n\t" \
  "s_waitcnt lgkmcnt(4)\n\t" \
  "ds_read_b128 v[220:223], v250 offset:1024\n\t" \
  "v_smfmac_f32_16x16x64_bf16 v[192:195], v[212:215], v[0:7], v251\n\t" \
  "ds_read_b128 v[224:227], v250 offset:2048\n\t" \
  "v_smfmac_f32_16x16x64_bf16 v[196:199], v[212:215], v[8:15], v251\n\t" \
  "s_waitcnt lgkmcnt(5)\n\t" \
  "ds_read_b128 v[228:231], v250 offset:3072\n\t" \
  "v_smfmac_f32_16x16x64_bf16 v[192:195], v[236:239], v[16:23], v251\n\t" \
  "ds_read_b128 v[200:203], v248 offset:6144\n\t" \
  "v_smfmac_f32_16x16x64_bf16 v[196:199], v[236:239], v[24:31], v251\n\t" \
  "s_waitcnt lgkmcnt(6)\n\t" \
  "ds_read_b128 v[204:207], v248 offset:7168\n\t" \
  "v_smfmac_f32_16x16x64_bf16 v[192:195], v[240:243], v[32:39], v251\n\t" \
  "ds_read_b128 v[208:211], v248 offset:8192\n\t" \
  "v_smfmac_f32_16x16x64_bf16 v[196:199], v[240:243], v[40:47], v251\n\t" \
  "s_waitcnt lgkmcnt(5)\n\t" \
  "ds_read_b128 v[212:215], v248 offset:10240\n\t" \
  "v_smfmac_f32_16x16x64_bf16 v[192:195], v[244:247], v[216:223], v251\n\t" \
  "s_waitcnt lgkmcnt(4)\n\t" \
  "v_smfmac_f32_16x16x64_bf16 v[196:199], v[244:247], v[224:231], v251\n\t" \
  "s_waitcnt lgkmcnt(3)\n\t" \
  "ds_read_b128 v[236:239], v248 offset:11264\n\t" \
  "v_smfmac_f32_16x16x64_bf16 v[192:195], v[200:203], v[48:55], v251\n\t" \
  "ds_read_b128 v[240:243], v248 offset:13312\n\t" \
  "v_smfmac_f32_16x16x64_bf16 v[196:199], v[200:203], v[56:63], v251\n\t" \
  "s_waitcnt lgkmcnt(4)\n\t" \
  "v_smfmac_f32_16x16x64_bf16 v[192:195], v[204:207], v[64:71], v251\n\t" \
  "ds_read_b128 v[244:247], v248 offset:14336\n\t" \
  "v_smfmac_f32_16x16x64_bf16 v[196:199], v[204:207], v[72:79], v251\n\t" \
  "s_waitcnt lgkmcnt(4)\n\t" \
  "v_smfmac_f32_16x16x64_bf16 v[192:195], v[208:211], v[80:87], v251\n\t" \
  "ds_read_b128 v[200:203], v248 offset:15360\n\t" \
  "v_smfmac_f32_16x16x64_bf16 v[196:199], v[208:211], v[88:95], v251\n\t" \
  "s_waitcnt lgkmcnt(4)\n\t" \
  "v_smfmac_f32_16x16x64_bf16 v[192:195], v[212:215], v[96:103], v251\n\t" \
  "ds_read_b128 v[204:207], v248 offset:2048\n\t" \
  "v_smfmac_f32_16x16x64_bf16 v[196:199], v[212:215], v[104:111], v251\n\t" \
  "s_waitcnt lgkmcnt(3)\n\t" \
  "v_smfmac_f32_16x16x64_bf16 v[192:195], v[236:239], v[112:119], v251\n\t" \
  "v_smfmac_f32_16x16x64_bf16 v[196:199], v[236:239], v[120:127], v251\n\t" \
  "v_smfmac_f32_16x16x64_bf16 v[192:195], v[240:243], v[128:135], v251\n\t" \
  "v_smfmac_f32_16x16x64_bf16 v[196:199], v[240:243], v[136:143], v251\n\t" \
  "s_waitcnt lgkmcnt(1)\n\t" \
  "v_smfmac_f32_16x16x64_bf16 v[192:195], v[244:247], v[144:151], v251\n\t" \
  "v_smfmac_f32_16x16x64_bf16 v[196:199], v[244:247], v[152:159], v251\n\t" \
  "v_smfmac_f32_16x16x64_bf16 v[192:195], v[200:203], v[160:167], v251\n\t" \
  "v_smfmac_f32_16x16x64_bf16 v[196:199], v[200:203], v[168:175], v251\n\t" \
  "s_waitcnt lgkmcnt(0)\n\t" \
  "v_smfmac_f32_16x16x64_bf16 v[192:195], v[204:207], v[176:183], v251\n\t" \
  "v_smfmac_f32_16x16x64_bf16 v[196:199], v[204:207], v[184:191], v251\n\t" \
  "ds_read_b128 v[200:203], v248 offset:17408\n\t" \
  "ds_read_b128 v[204:207], v248 offset:21504\n\t" \
  "v_mov_b32 v250, v232\n\t" \
  "v_mov_b32 v249, v233\n\t" \
  "s_nop 2\n\t" \
  "v_add_f32 v192, v192, v194\n\t" \
  "v_add_f32 v193, v193, v195\n\t" \
  "v_add_f32 v196, v196, v198\n\t" \
  "v_add_f32 v197, v197, v199\n\t" \
  "v_cvt_pk_bf16_f32 v192, v192, v193\n\t" \
  "v_cvt_pk_bf16_f32 v196, v196, v197\n\t" \
  "ds_write2_b32 v252, v192, v196 offset1:64\n\t" \
  "v_mov_b32 v251, v234\n\t" \
  "ds_read_b64_tr_b16 v[236:237], v250 offset:512\n\t" \
  "ds_read_b64_tr_b16 v[238:239], v250 offset:576\n\t" \
  "v_mov_b32 v212, %[step]\n\t" \
  "s_mov_b64 exec, 1\n\t" \
  "ds_write_b32 v248, v212 offset:28688\n\t" \
  "s_mov_b64 exec, -1\n\t" \
  "buffer_load_dwordx4 v[192:195], v248, %[rt], %[svt] offen offset:0 sc1\n\t" \
  "buffer_load_dwordx4 v[196:199], v248, %[rt], %[svt] offen offset:1024 sc1\n\t" \
  "s_waitcnt lgkmcnt(1)\n\t" \
  "v_mfma_f32_16x16x32_bf16 v[200:203], v[200:203], v[236:239], 0\n\t" \
  "v_mfma_f32_16x16x32_bf16 v[204:207], v[204:207], v[236:239], 0\n\t" \
  "s_nop 7\n\t" \
  "v_cndmask_b32 v216, v200, v204, %[nh]\n\t" \
  "v_cndmask_b32 v217, v201, v205, %[nh]\n\t" \
  "v_cndmask_b32 v218, v202, v206, %[nh]\n\t" \
  "v_cndmask_b32 v219, v203, v207, %[nh]\n\t" \
  "ds_write_b128 v248, v[216:219] offset:17408\n\t" \
  "buffer_load_dword v252, v249, %[rs], %[sv] offen offset:0 sc1\n\t" \
  "buffer_load_dword v253, v249, %[rs], %[sv] offen offset:256 sc1\n\t" \
  "ds_read_b32 v212, v248 offset:28672\n\t" \
  "ds_read_b64_tr_b16 v[240:241], v250 offset:1024\n\t" \
  "ds_read_b64_tr_b16 v[242:243], v250 offset:1088\n\t" \
  "ds_read_b64_tr_b16 v[244:245], v250 offset:1536\n\t" \
  "ds_read_b64_tr_b16 v[246:247], v250 offset:1600\n\t" \
  "ds_read_b64_tr_b16 v[232:233], v250 offset:0\n\t" \
  "ds_read_b64_tr_b16 v[234:235], v250 offset:64\n\t" \
  "s_setprio 3\n\t" \
  "s_waitcnt lgkmcnt(0)\n\t" \
  "v_cmp_ne_u32 vcc, %[step], v212\n\t" \
  "s_barrier\n\t" \
  "s_and_b32 s93, vcc_lo, 0xf\n\t" \
  "s_cbranch_scc1 .Lre%=\n\t" \
  ".Lrb%=:\n\t" \
  "ds_read_b64_tr_b16 v[204:205], v250 offset:2560\n\t" \
  "ds_read_b64_tr_b16 v[206:207], v250 offset:2624\n\t" \
  "ds_read_b64_tr_b16 v[208:209], v250 offset:3072\n\t" \
  "ds_read_b64_tr_b16 v[210:211], v250 offset:3136\n\t" \
  "ds_read_b128 v[220:223], v248 offset:18432\n\t" \
  "ds_read_b128 v[224:227], v248 offset:19456\n\t" \
  "ds_read_b128 v[228:231], v248 offset:16384\n\t" \
  "ds_read_b64_tr_b16 v[212:213], v250 offset:3584\n\t" \
  "ds_read_b64_tr_b16 v[214:215], v250 offset:3648\n\t" \
  "ds_read_b64_tr_b16 v[200:201], v250 offset:2048\n\t" \
  "ds_read_b64_tr_b16 v[202:203], v250 offset:2112\n\t" \
  "s_waitcnt lgkmcnt(9)\n\t" \
  "v_mfma_f32_16x16x32_bf16 v[204:207], v[204:207], v[236:239], 0\n\t" \
  "s_waitcnt lgkmcnt(7)\n\t" \
  "v_mfma_f32_16x16x32_bf16 v[204:207], v[208:211], v[240:243], v[204:207]\n\t" \
  "s_waitcnt lgkmcnt(6)\n\t" \
  "v_pk_add_f32 v[216:217], v[216:217], v[220:221]\n\t" \
  "v_pk_add_f32 v[218:219], v[218:219], v[222:223]\n\t" \
  "s_waitcnt lgkmcnt(4)\n\t" \
  "v_pk_add_f32 v[224:225], v[224:225], v[228:229]\n\t" \
  "v_pk_add_f32 v[226:227], v[226:227], v[230:231]\n\t" \
  "v_pk_add_f32 v[216:217], v[216:217], v[224:225]\n\t" \
  "v_pk_add_f32 v[218:219], v[218:219], v[226:227]\n\t" \
  "s_waitcnt lgkmcnt(2)\n\t" \
  "v_mfma_f32_16x16x32_bf16 v[204:207], v[212:215], v[244:247], v[204:207]\n\t" \
  "v_max3_f32 v224, v216, v217, v218\n\t" \
  "v_max_f32 v224, v224, v219\n\t" \
  "s_waitcnt lgkmcnt(0)\n\t" \
  "v_mfma_f32_16x16x32_bf16 v[204:207], v[200:203], v[232:235], v[204:207]\n\t" \
  "v_max_f32_dpp v225, v224, v224 row_ror:8 row_mask:0xf bank_mask:0xf\n\t" \
  "v_max_f32_dpp v226, v224, v224 row_ror:8 row_mask:0xf bank_mask:0xf\n\t" \
  "s_nop 1\n\t" \
  "v_permlane32_swap_b32 v225, v226\n\t" \
  "v_max_f32 v231, v225, v226\n\t" \
  "v_mov_b32 v220, 0x3f803f80\n\t" \
  "v_mov_b32 v221, 0x3f803f80\n\t" \
  "v_mov_b64 v[222:223], v[220:221]\n\t" \
  "v_mov_b32 v237, 0\n\t" \
  "v_mov_b32 v238, 0\n\t" \
  "v_max3_f32 v208, v204, v205, v231\n\t" \
  "v_max3_f32 v209, v208, v206, v207\n\t" \
  "v_max3_f32 v210, v208, v206, v207\n\t" \
  "v_cndmask_b32 v204, v204, v206, %[up]\n\t" \
  "v_cndmask_b32 v205, v205, v207, %[up]\n\t" \
  "v_permlane16_swap_b32 v209, v210\n\t" \
  "v_max_f32 v208, v209, v210\n\t" \
  "v_mul_f32 v208, %[negc], v208\n\t" \
  "v_fma_f32 v204, v204, %[c], v208\n\t" \
  "v_fma_f32 v205, v205, %[c], v208\n\t" \
  "v_exp_f32 v204, v204\n\t" \
  "v_fma_f32 v216, v216, %[c], v208\n\t" \
  "v_exp_f32 v205, v205\n\t" \
  "v_fma_f32 v217, v217, %[c], v208\n\t" \
  "v_exp_f32 v216, v216\n\t" \
  "v_fma_f32 v218, v218, %[c], v208\n\t" \
  "v_cvt_pk_bf16_f32 v236, v204, v205\n\t" \
  "v_cvt_pk_bf16_f32 v239, v204, v205\n\t" \
  "v_exp_f32 v217, v217\n\t" \
  "v_fma_f32 v219, v219, %[c], v208\n\t" \
  "v_mfma_f32_16x16x16_bf16 v[200:203], v[220:221], v[236:237], 0\n\t" \
  "v_exp_f32 v218, v218\n\t" \
  "s_waitcnt vmcnt(0)\n\t" \
  "v_mfma_f32_16x16x16_bf16 v[224:227], v[252:253], v[236:237], 0\n\t" \
  "v_cvt_pk_bf16_f32 v214, v216, v217\n\t" \
  "v_cvt_pk_bf16_f32 v212, v216, v217\n\t" \
  "v_exp_f32 v219, v219\n\t" \
  "v_mfma_f32_16x16x16_bf16 v[228:231], v[252:253], v[238:239], 0\n\t" \
  "v_cvt_pk_bf16_f32 v215, v218, v219\n\t" \
  "v_cvt_pk_bf16_f32 v213, v218, v219\n\t" \
  "v_mov_b32_dpp v212, v214 row_ror:8 row_mask:0xf bank_mask:0xc\n\t" \
  "v_mov_b32_dpp v214, v214 row_ror:8 row_mask:0xf bank_mask:0x3\n\t" \
  "v_mov_b32_dpp v213, v215 row_ror:8 row_mask:0xf bank_mask:0xc\n\t" \
  "v_mov_b32_dpp v215, v215 row_ror:8 row_mask:0xf bank_mask:0x3\n\t" \
  "v_max_u32 v208, v252, v253\n\t" \
  "v_cmp_le_u32 vcc, %[sent], v208\n\t" \
  "v_mfma_f32_16x16x32_bf16 v[200:203], v[220:223], v[212:215], v[200:203]\n\t" \
  ".Lpv%=:\n\t" \
  "v_mfma_f32_16x16x32_bf16 v[224:227], v[192:195], v[212:215], v[224:227]\n\t" \
  "v_mfma_f32_16x16x32_bf16 v[228:231], v[196:199], v[212:215], v[228:231]\n\t" \
  "s_cbranch_vccnz .Lvs%=\n\t" \
  ".Lvok%=:\n\t" \
  "s_nop 4\n\t" \
  "v_rcp_f32 v208, v200\n\t" \
  "v_cndmask_b32 v224, v224, v226, %[nh]\n\t" \
  "v_cndmask_b32 v225, v225, v227, %[nh]\n\t" \
  "v_cndmask_b32 v228, v228, v230, %[nh]\n\t" \
  "v_cndmask_b32 v229, v229, v231, %[nh]\n\t" \
  "v_pk_mul_f32 v[224:225], v[224:225], v[208:209] op_sel_hi:[1,0]\n\t" \
  "v_pk_mul_f32 v[228:229], v[228:229], v[208:209] op_sel_hi:[1,0]\n\t" \
  "v_cvt_pk_bf16_f32 v224, v224, v225\n\t" \
  "v_cvt_pk_bf16_f32 v225, v228, v229\n\t" \
  "buffer_store_dwordx2 v[224:225], v251, %[rs], %[so] offen sc1\n\t" \
  "s_memtime s[90:91]\n\t" \
  "s_setprio 0\n\t" \
  "s_waitcnt lgkmcnt(0)\n\t" \
  "s_mov_b32 %[tout], s90\n\t" \
  "s_mov_b32 %[spins], s92\n\t" \
  "s_branch .Lend%=\n\t" \
  ".Lls%=:\n\t" \
  "s_waitcnt lgkmcnt(0)\n\t" \
  "s_barrier\n\t" \
  "s_mov_b32 %[spins], 0\n\t" \
  "s_branch .Lend%=\n\t" \
  ".Lre%=:\n\t" \
  "ds_read_b64_tr_b16 v[240:241], v250 offset:1024\n\t" \
  "ds_read_b64_tr_b16 v[242:243], v250 offset:1088\n\t" \
  "ds_read_b64_tr_b16 v[244:245], v250 offset:1536\n\t" \
  "ds_read_b64_tr_b16 v[246:247], v250 offset:1600\n\t" \
  "ds_read_b64_tr_b16 v[232:233], v250 offset:0\n\t" \
  "ds_read_b64_tr_b16 v[234:235], v250 offset:64\n\t" \
  "s_waitcnt lgkmcnt(0)\n\t" \
  "s_branch .Lrb%=\n\t" \
  ".Lvs%=:\n\t" \
  "v_cmp_eq_u32 vcc, %[sent], v252\n\t" \
  "v_cmp_eq_u32 s[94:95], %[sent], v253\n\t" \
  "s_or_b64 s[94:95], s[94:95], vcc\n\t" \
  "s_cbranch_scc0 .Lvok%=\n\t" \
  "s_add_u32 s92, s92, 1\n\t" \
  "s_sleep 2\n\t" \
  "s_and_b32 s93, s92, 63\n\t" \
  "s_cbranch_scc1 .Lvr%=\n\t" \
  "buffer_load_dword v208, off, %[rs], %[sab] sc1\n\t" \
  "s_waitcnt vmcnt(0)\n\t" \
  "v_readfirstlane_b32 s93, v208\n\t" \
  "s_nop 3\n\t" \
  "s_cmp_eq_u32 s93, 1\n\t" \
  "s_cbranch_scc1 .Lvok%=\n\t" \
  "s_cmp_lt_u32 s92, 0x80000\n\t" \
  "s_cbranch_scc1 .Lvr%=\n\t" \
  "v_mov_b32 v208, 1\n\t" \
  "buffer_store_dword v208, off, %[rs], %[sab] sc1\n\t" \
  "s_branch .Lvok%=\n\t" \
  ".Lvr%=:\n\t" \
  "buffer_load_dword v252, v249, %[rs], %[sv] offen offset:0 sc1\n\t" \
  "buffer_load_dword v253, v249, %[rs], %[sv] offen offset:256 sc1\n\t" \
  "s_waitcnt vmcnt(0)\n\t" \
  "v_mfma_f32_16x16x16_bf16 v[224:227], v[252:253], v[236:237], 0\n\t" \
  "v_mfma_f32_16x16x16_bf16 v[228:231], v[252:253], v[238:239], 0\n\t" \
  "v_max_u32 v208, v252, v253\n\t" \
  "v_cmp_le_u32 vcc, %[sent], v208\n\t" \
  "s_nop 4\n\t" \
  "s_branch .Lpv%=\n\t" \
  "s_branch .Lend%=\n\t" \
  ".Lslow%=:\n\t" \
  "s_mov_b64 s[94:95], 0\n\t" \
  "v_cmp_eq_u32 vcc, %[sent], v200\n\t" \
  "s_or_b64 s[94:95], s[94:95], vcc\n\t" \
  "v_cmp_eq_u32 vcc, %[sent], v201\n\t" \
  "s_or_b64 s[94:95], s[94:95], vcc\n\t" \
  "v_cmp_eq_u32 vcc, %[sent], v202\n\t" \
  "s_or_b64 s[94:95], s[94:95], vcc\n\t" \
  "v_cmp_eq_u32 vcc, %[sent], v203\n\t" \
  "s_or_b64 s[94:95], s[94:95], vcc\n\t" \
  "v_cmp_eq_u32 vcc, %[sent], v204\n\t" \
  "s_or_b64 s[94:95], s[94:95], vcc\n\t" \
  "v_cmp_eq_u32 vcc, %[sent], v205\n\t" \
  "s_or_b64 s[94:95], s[94:95], vcc\n\t" \
  "v_cmp_eq_u32 vcc, %[sent], v206\n\t" \
  "s_or_b64 s[94:95], s[94:95], vcc\n\t" \
  "v_cmp_eq_u32 vcc, %[sent], v207\n\t" \
  "s_or_b64 s[94:95], s[94:95], vcc\n\t" \
  "s_cmp_eq_u64 s[94:95], 0\n\t" \
  "s_cbranch_scc1 .Lok%=\n\t" \
  "v_mov_b64 v[192:193], 0\n\t" \
  "v_mov_b64 v[194:195], 0\n\t" \
  "v_mov_b64 v[196:197], 0\n\t" \
  "v_mov_b64 v[198:199], 0\n\t" \
  "s_add_u32 s92, s92, 1\n\t" \
  "s_cmp_eq_u32 s92, 1\n\t" \
  "s_cbranch_scc1 .Lpoll%=\n\t" \
  "s_sleep 2\n\t" \
  "s_and_b32 s93, s92, 63\n\t" \
  "s_cmp_lg_u32 s93, 0\n\t" \
  "s_cbranch_scc1 .Lpoll%=\n\t" \
  "v_mov_b32 v245, 0\n\t" \
  "buffer_load_dword v244, v245, %[rs], %[sab] offen sc1\n\t" \
  "s_waitcnt vmcnt(0)\n\t" \
  "v_readfirstlane_b32 s93, v244\n\t" \
  "s_nop 3\n\t" \
  "s_cmp_eq_u32 s93, 1\n\t" \
  "s_cbranch_scc1 .Lok%=\n\t" \
  "s_cmp_lt_u32 s92, 0x80000\n\t" \
  "s_cbranch_scc1 .Lpoll%=\n\t" \
  "v_mov_b32 v244, 1\n\t" \
  "buffer_store_dword v244, v245, %[rs], %[sab] offen sc1\n\t" \
  "s_branch .Lok%=\n\t" \
  ".Lend%=:\n\t"
#define QK_ASM_2 \
  "s_waitcnt lgkmcnt(0)\n\t" \
  "v_subrev_u32 v244, 0x8000, v250\n\t" \
  "ds_read_b128 v[216:219], v248 offset:40960\n\t" \
  "ds_read_b128 v[220:223], v248 offset:41984\n\t" \
  "ds_read_b128 v[224:227], v248 offset:43008\n\t" \
  "ds_read_b128 v[228:231], v248 offset:44032\n\t" \
  "ds_read_b128 v[208:211], v244 offset:0\n\t" \
  "ds_read_b128 v[212:215], v244 offset:1024\n\t" \
  "ds_read_b128 v[236:239], v244 offset:2048\n\t" \
  "ds_read_b128 v[240:243], v244 offset:3072\n\t" \
  "v_mov_b64 v[192:193], 0\n\t" \
  "v_mov_b64 v[194:195], 0\n\t" \
  "v_mov_b64 v[196:197], 0\n\t" \
  "v_mov_b64 v[198:199], 0\n\t" \
  "s_movk_i32 s92, 0x2000\n\t" \
  ".Lwt%=:\n\t" \
  "ds_read_b64 v[200:201], v248 offset:30720\n\t" \
  "s_memrealtime s[90:91]\n\t" \
  "s_sub_u32 s92, s92, 1\n\t" \
  "s_waitcnt lgkmcnt(0)\n\t" \
  "v_readfirstlane_b32 s93, v200\n\t" \
  "v_readfirstlane_b32 %[tgt], v201\n\t" \
  "s_cbranch_scc1 .Lgo%=\n\t" \
  "s_cmp_eq_u32 s93, %[step]\n\t" \
  "s_cbranch_scc1 .Lwx%=\n\t" \
  "s_sleep 1\n\t" \
  "s_branch .Lwt%=\n\t" \
  ".Lwx%=:\n\t" \
  "s_lshl_b32 s93, s90, 2\n\t" \
  "s_sub_i32 s93, %[tgt], s93\n\t" \
  "s_cmp_le_i32 s93, 0\n\t" \
  "s_cbranch_scc1 .Lgo%=\n\t" \
  "s_min_i32 s93, s93, 8000\n\t" \
  "s_mov_b32 s94, s93\n\t" \
  ".Lsl%=:\n\t" \
  "s_sub_i32 s94, s94, 18\n\t" \
  "s_cmp_lt_i32 s94, 30\n\t" \
  "s_cbranch_scc1 .Lsx%=\n\t" \
  "s_sleep 1\n\t" \
  "s_branch .Lsl%=\n\t" \
  ".Lsx%=:\n\t" \
  "s_sub_i32 s93, s93, 12\n\t" \
  "s_max_i32 s93, s93, 4\n\t" \
  "s_lshr_b32 s93, s93, 2\n\t" \
  "s_add_u32 s93, s93, s90\n\t" \
  "s_memrealtime s[90:91]\n\t" \
  ".Lpace%=:\n\t" \
  "s_memrealtime s[94:95]\n\t" \
  "s_waitcnt lgkmcnt(1)\n\t" \
  "s_sub_i32 s92, s90, s93\n\t" \
  "s_cmp_ge_i32 s92, 0\n\t" \
  "s_cbranch_scc1 .Le%=\n\t" \
  "s_memrealtime s[90:91]\n\t" \
  "s_waitcnt lgkmcnt(1)\n\t" \
  "s_sub_i32 s92, s94, s93\n\t" \
  "s_cmp_lt_i32 s92, 0\n\t" \
  "s_cbranch_scc1 .Lpace%=\n\t" \
  "s_nop 4\n\t" \
  ".Le%=:\n\t" \
  "s_add_u32 s92, s92, s93\n\t" \
  "s_lshl_b32 s92, s92, 2\n\t" \
  "s_sub_i32 s92, %[tgt], s92\n\t" \
  "s_max_i32 s92, s92, 0\n\t" \
  "s_bitcmp0_b32 s92, 3\n\t" \
  "s_cbranch_scc1 .Lf3%=\n\t" \
  "s_nop 15\n\t" \
  ".Lf3%=:\n\t" \
  "s_bitcmp0_b32 s92, 2\n\t" \
  "s_cbranch_scc1 .Lf2%=\n\t" \
  "s_nop 10\n\t" \
  ".Lf2%=:\n\t" \
  ".Lgo%=:\n\t" \
  "s_mov_b32 s92, 0\n\t" \
  ".Lpoll%=:\n\t" \
  "buffer_load_dwordx4 v[200:203], v248, %[rs], %[som] offen sc1\n\t" \
  "buffer_load_dwordx4 v[204:207], v248, %[rs], %[som2] offen sc1\n\t" \
  "s_waitcnt vmcnt(1) lgkmcnt(0)\n\t" \
  "ds_write_b128 v249, v[200:203]\n\t" \
  "v_smfmac_f32_16x16x64_bf16 v[192:195], v[200:203], v[208:215], v251\n\t" \
  "v_max3_u32 v244, v200, v201, v202\n\t" \
  "v_smfmac_f32_16x16x64_bf16 v[196:199], v[200:203], v[236:243], v251\n\t" \
  "s_waitcnt vmcnt(0)\n\t" \
  "ds_write_b128 v249, v[204:207] offset:8192\n\t" \
  "v_smfmac_f32_16x16x64_bf16 v[192:195], v[204:207], v[216:223], v251\n\t" \
  "v_max3_u32 v244, v244, v203, v204\n\t" \
  "v_max3_u32 v244, v244, v205, v206\n\t" \
  "v_smfmac_f32_16x16x64_bf16 v[196:199], v[204:207], v[224:231], v251\n\t" \
  "v_max_u32 v244, v244, v207\n\t" \
  "v_cmp_le_u32 vcc, %[sent], v244\n\t" \
  "s_cbranch_vccnz .Lslow%=\n\t" \
  ".Lok%=:\n\t" \
  "ds_read_b128 v[216:219], v250 offset:32768\n\t" \
  "ds_read_b128 v[220:223], v250 offset:33792\n\t" \
  "ds_read_b128 v[224:227], v250 offset:34816\n\t" \
  "ds_read_b128 v[228:231], v250 offset:35840\n\t" \
  "s_waitcnt lgkmcnt(4)\n\t" \
  "s_barrier\n\t" \
  "ds_read_b128 v[208:211], v248 offset:3072\n\t" \
  "ds_read_b128 v[212:215], v248 offset:4096\n\t" \
  "ds_read_b128 v[236:239], v248 offset:5120\n\t" \
  "ds_read_b128 v[240:243], v248 offset:6144\n\t" \
  "s_cmp_eq_u32 %[step], 63\n\t" \
  "s_cbranch_scc1 .Lls%=\n\t" \
  "s_waitcnt lgkmcnt(3)\n\t" \
  "ds_read_b128 v[244:247], v248 offset:13312\n\t" \
  "v_smfmac_f32_16x16x64_bf16 v[192:195], v[208:211], v[216:223], v251\n\t" \
  "ds_read_b128 v[216:219], v250 offset:0\n\t" \
  "v_smfmac_f32_16x16x64_bf16 v[196:199], v[208:211], v[224:231], v251\n\t" \
  "s_waitcnt lgkmcnt(4)\n\t" \
  "ds_read_b128 v[220:223], v250 offset:1024\n\t" \
  "v_smfmac_f32_16x16x64_bf16 v[192:195], v[212:215], v[0:7], v251\n\t" \
  "ds_read_b128 v[224:227], v250 offset:2048\n\t" \
  "v_smfmac_f32_16x16x64_bf16 v[196:199], v[212:215], v[8:15], v251\n\t" \
  "s_waitcnt lgkmcnt(5)\n\t" \
  "ds_read_b128 v[228:231], v250 offset:3072\n\t" \
  "v_smfmac_f32_16x16x64_bf16 v[192:195], v[236:239], v[16:23], v251\n\t" \
  "ds_read_b128 v[200:203], v248 offset:7168\n\t" \
  "v_smfmac_f32_16x16x64_bf16 v[196:199], v[236:239], v[24:31], v251\n\t" \
  "s_waitcnt lgkmcnt(6)\n\t" \
  "ds_read_b128 v[204:207], v248 offset:8192\n\t" \
  "v_smfmac_f32_16x16x64_bf16 v[192:195], v[240:243], v[32:39], v251\n\t" \
  "ds_read_b128 v[208:211], v248 offset:9216\n\t" \
  "v_smfmac_f32_16x16x64_bf16 v[196:199], v[240:243], v[40:47], v251\n\t" \
  "s_waitcnt lgkmcnt(5)\n\t" \
  "ds_read_b128 v[212:215], v248 offset:11264\n\t" \
  "v_smfmac_f32_16x16x64_bf16 v[192:195], v[244:247], v[216:223], v251\n\t" \
  "s_waitcnt lgkmcnt(4)\n\t" \
  "v_smfmac_f32_16x16x64_bf16 v[196:199], v[244:247], v[224:231], v251\n\t" \
  "s_waitcnt lgkmcnt(3)\n\t" \
  "ds_read_b128 v[236:239], v248 offset:12288\n\t" \
  "v_smfmac_f32_16x16x64_bf16 v[192:195], v[200:203], v[48:55], v251\n\t" \
  "ds_read_b128 v[240:243], v248 offset:14336\n\t" \
  "v_smfmac_f32_16x16x64_bf16 v[196:199], v[200:203], v[56:63], v251\n\t" \
  "s_waitcnt lgkmcnt(4)\n\t" \
  "v_smfmac_f32_16x16x64_bf16 v[192:195], v[204:207], v[64:71], v251\n\t" \
  "ds_read_b128 v[244:247], v248 offset:15360\n\t" \
  "v_smfmac_f32_16x16x64_bf16 v[196:199], v[204:207], v[72:79], v251\n\t" \
  "s_waitcnt lgkmcnt(4)\n\t" \
  "v_smfmac_f32_16x16x64_bf16 v[192:195], v[208:211], v[80:87], v251\n\t" \
  "ds_read_b128 v[200:203], v248 offset:0\n\t" \
  "v_smfmac_f32_16x16x64_bf16 v[196:199], v[208:211], v[88:95], v251\n\t" \
  "s_waitcnt lgkmcnt(4)\n\t" \
  "v_smfmac_f32_16x16x64_bf16 v[192:195], v[212:215], v[96:103], v251\n\t" \
  "ds_read_b128 v[204:207], v248 offset:1024\n\t" \
  "v_smfmac_f32_16x16x64_bf16 v[196:199], v[212:215], v[104:111], v251\n\t" \
  "s_waitcnt lgkmcnt(3)\n\t" \
  "v_smfmac_f32_16x16x64_bf16 v[192:195], v[236:239], v[112:119], v251\n\t" \
  "v_smfmac_f32_16x16x64_bf16 v[196:199], v[236:239], v[120:127], v251\n\t" \
  "v_smfmac_f32_16x16x64_bf16 v[192:195], v[240:243], v[128:135], v251\n\t" \
  "v_smfmac_f32_16x16x64_bf16 v[196:199], v[240:243], v[136:143], v251\n\t" \
  "s_waitcnt lgkmcnt(1)\n\t" \
  "v_smfmac_f32_16x16x64_bf16 v[192:195], v[244:247], v[144:151], v251\n\t" \
  "v_smfmac_f32_16x16x64_bf16 v[196:199], v[244:247], v[152:159], v251\n\t" \
  "v_smfmac_f32_16x16x64_bf16 v[192:195], v[200:203], v[160:167], v251\n\t" \
  "v_smfmac_f32_16x16x64_bf16 v[196:199], v[200:203], v[168:175], v251\n\t" \
  "s_waitcnt lgkmcnt(0)\n\t" \
  "v_smfmac_f32_16x16x64_bf16 v[192:195], v[204:207], v[176:183], v251\n\t" \
  "v_smfmac_f32_16x16x64_bf16 v[196:199], v[204:207], v[184:191], v251\n\t" \
  "ds_read_b128 v[200:203], v248 offset:18432\n\t" \
  "ds_read_b128 v[204:207], v248 offset:22528\n\t" \
  "v_mov_b32 v250, v232\n\t" \
  "v_mov_b32 v249, v233\n\t" \
  "s_nop 2\n\t" \
  "v_add_f32 v192, v192, v194\n\t" \
  "v_add_f32 v193, v193, v195\n\t" \
  "v_add_f32 v196, v196, v198\n\t" \
  "v_add_f32 v197, v197, v199\n\t" \
  "v_cvt_pk_bf16_f32 v192, v192, v193\n\t" \
  "v_cvt_pk_bf16_f32 v196, v196, v197\n\t" \
  "ds_write2_b32 v252, v192, v196 offset1:64\n\t" \
  "v_mov_b32 v251, v234\n\t" \
  "ds_read_b64_tr_b16 v[240:241], v250 offset:1024\n\t" \
  "ds_read_b64_tr_b16 v[242:243], v250 offset:1088\n\t" \
  "v_mov_b32 v212, %[step]\n\t" \
  "s_mov_b64 exec, 1\n\t" \
  "ds_write_b32 v248, v212 offset:28704\n\t" \
  "s_mov_b64 exec, -1\n\t" \
  "buffer_load_dwordx4 v[192:195], v248, %[rt], %[svt] offen offset:0 sc1\n\t" \
  "buffer_load_dwordx4 v[196:199], v248, %[rt], %[svt] offen offset:1024 sc1\n\t" \
  "s_waitcnt lgkmcnt(1)\n\t" \
  "v_mfma_f32_16x16x32_bf16 v[200:203], v[200:203], v[240:243], 0\n\t" \
  "v_mfma_f32_16x16x32_bf16 v[204:207], v[204:207], v[240:243], 0\n\t" \
  "s_nop 7\n\t" \
  "v_cndmask_b32 v216, v200, v204, %[nh]\n\t" \
  "v_cndmask_b32 v217, v201, v205, %[nh]\n\t" \
  "v_cndmask_b32 v218, v202, v206, %[nh]\n\t" \
  "v_cndmask_b32 v219, v203, v207, %[nh]\n\t" \
  "ds_write_b128 v248, v[216:219] offset:18432\n\t" \
  "buffer_load_dword v252, v249, %[rs], %[sv] offen offset:0 sc1\n\t" \
  "buffer_load_dword v253, v249, %[rs], %[sv] offen offset:256 sc1\n\t" \
  "ds_read_b32 v212, v248 offset:28672\n\t" \
  "ds_read_b64_tr_b16 v[244:245], v250 offset:1536\n\t" \
  "ds_read_b64_tr_b16 v[246:247], v250 offset:1600\n\t" \
  "ds_read_b64_tr_b16 v[232:233], v250 offset:0\n\t" \
  "ds_read_b64_tr_b16 v[234:235], v250 offset:64\n\t" \
  "ds_read_b64_tr_b16 v[236:237], v250 offset:512\n\t" \
  "ds_read_b64_tr_b16 v[238:239], v250 offset:576\n\t" \
  "s_setprio 3\n\t" \
  "s_waitcnt lgkmcnt(0)\n\t" \
  "v_cmp_ne_u32 vcc, %[step], v212\n\t" \
  "s_barrier\n\t" \
  "s_and_b32 s93, vcc_lo, 0xf\n\t" \
  "s_cbranch_scc1 .Lre%=\n\t" \
  ".Lrb%=:\n\t" \
  "ds_read_b64_tr_b16 v[208:209], v250 offset:3072\n\t" \
  "ds_read_b64_tr_b16 v[210:211], v250 offset:3136\n\t" \
  "ds_read_b64_tr_b16 v[212:213], v250 offset:3584\n\t" \
  "ds_read_b64_tr_b16 v[214:215], v250 offset:3648\n\t" \
  "ds_read_b128 v[220:223], v248 offset:19456\n\t" \
  "ds_read_b128 v[224:227], v248 offset:16384\n\t" \
  "ds_read_b128 v[228:231], v248 offset:17408\n\t" \
  "ds_read_b64_tr_b16 v[200:201], v250 offset:2048\n\t" \
  "ds_read_b64_tr_b16 v[202:203], v250 offset:2112\n\t" \
  "ds_read_b64_tr_b16 v[204:205], v250 offset:2560\n\t" \
  "ds_read_b64_tr_b16 v[206:207], v250 offset:2624\n\t" \
  "s_waitcnt lgkmcnt(9)\n\t" \
  "v_mfma_f32_16x16x32_bf16 v[208:211], v[208:211], v[240:243], 0\n\t" \
  "s_waitcnt lgkmcnt(7)\n\t" \
  "v_mfma_f32_16x16x32_bf16 v[208:211], v[212:215], v[244:247], v[208:211]\n\t" \
  "s_waitcnt lgkmcnt(6)\n\t" \
  "v_pk_add_f32 v[216:217], v[216:217], v[220:221]\n\t" \
  "v_pk_add_f32 v[218:219], v[218:219], v[222:223]\n\t" \
  "s_waitcnt lgkmcnt(4)\n\t" \
  "v_pk_add_f32 v[224:225], v[224:225], v[228:229]\n\t" \
  "v_pk_add_f32 v[226:227], v[226:227], v[230:231]\n\t" \
  "v_pk_add_f32 v[216:217], v[216:217], v[224:225]\n\t" \
  "v_pk_add_f32 v[218:219], v[218:219], v[226:227]\n\t" \
  "s_waitcnt lgkmcnt(2)\n\t" \
  "v_mfma_f32_16x16x32_bf16 v[208:211], v[200:203], v[232:235], v[208:211]\n\t" \
  "v_max3_f32 v224, v216, v217, v218\n\t" \
  "v_max_f32 v224, v224, v219\n\t" \
  "s_waitcnt lgkmcnt(0)\n\t" \
  "v_mfma_f32_16x16x32_bf16 v[208:211], v[204:207], v[236:239], v[208:211]\n\t" \
  "v_max_f32_dpp v225, v224, v224 row_ror:8 row_mask:0xf bank_mask:0xf\n\t" \
  "v_max_f32_dpp v226, v224, v224 row_ror:8 row_mask:0xf bank_mask:0xf\n\t" \
  "s_nop 1\n\t" \
  "v_permlane32_swap_b32 v225, v226\n\t" \
  "v_max_f32 v231, v225, v226\n\t" \
  "v_mov_b32 v220, 0x3f803f80\n\t" \
  "v_mov_b32 v221, 0x3f803f80\n\t" \
  "v_mov_b64 v[222:223], v[220:221]\n\t" \
  "v_mov_b32 v241, 0\n\t" \
  "v_mov_b32 v242, 0\n\t" \
  "v_max3_f32 v212, v208, v209, v231\n\t" \
  "v_max3_f32 v213, v212, v210, v211\n\t" \
  "v_max3_f32 v214, v212, v210, v211\n\t" \
  "v_cndmask_b32 v208, v208, v210, %[up]\n\t" \
  "v_cndmask_b32 v209, v209, v211, %[up]\n\t" \
  "v_permlane16_swap_b32 v213, v214\n\t" \
  "v_max_f32 v212, v213, v214\n\t" \
  "v_mul_f32 v212, %[negc], v212\n\t" \
  "v_fma_f32 v208, v208, %[c], v212\n\t" \
  "v_fma_f32 v209, v209, %[c], v212\n\t" \
  "v_exp_f32 v208, v208\n\t" \
  "v_fma_f32 v216, v216, %[c], v212\n\t" \
  "v_exp_f32 v209, v209\n\t" \
  "v_fma_f32 v217, v217, %[c], v212\n\t" \
  "v_exp_f32 v216, v216\n\t" \
  "v_fma_f32 v218, v218, %[c], v212\n\t" \
  "v_cvt_pk_bf16_f32 v240, v208, v209\n\t" \
  "v_cvt_pk_bf16_f32 v243, v208, v209\n\t" \
  "v_exp_f32 v217, v217\n\t" \
  "v_fma_f32 v219, v219, %[c], v212\n\t" \
  "v_mfma_f32_16x16x16_bf16 v[204:207], v[220:221], v[240:241], 0\n\t" \
  "v_exp_f32 v218, v218\n\t" \
  "s_waitcnt vmcnt(0)\n\t" \
  "v_mfma_f32_16x16x16_bf16 v[224:227], v[252:253], v[240:241], 0\n\t" \
  "v_cvt_pk_bf16_f32 v202, v216, v217\n\t" \
  "v_cvt_pk_bf16_f32 v200, v216, v217\n\t" \
  "v_exp_f32 v219, v219\n\t" \
  "v_mfma_f32_16x16x16_bf16 v[228:231], v[252:253], v[242:243], 0\n\t" \
  "v_cvt_pk_bf16_f32 v203, v218, v219\n\t" \
  "v_cvt_pk_bf16_f32 v201, v218, v219\n\t" \
  "v_mov_b32_dpp v200, v202 row_ror:8 row_mask:0xf bank_mask:0xc\n\t" \
  "v_mov_b32_dpp v202, v202 row_ror:8 row_mask:0xf bank_mask:0x3\n\t" \
  "v_mov_b32_dpp v201, v203 row_ror:8 row_mask:0xf bank_mask:0xc\n\t" \
  "v_mov_b32_dpp v203, v203 row_ror:8 row_mask:0xf bank_mask:0x3\n\t" \
  "v_max_u32 v212, v252, v253\n\t" \
  "v_cmp_le_u32 vcc, %[sent], v212\n\t" \
  "v_mfma_f32_16x16x32_bf16 v[204:207], v[220:223], v[200:203], v[204:207]\n\t" \
  ".Lpv%=:\n\t" \
  "v_mfma_f32_16x16x32_bf16 v[224:227], v[192:195], v[200:203], v[224:227]\n\t" \
  "v_mfma_f32_16x16x32_bf16 v[228:231], v[196:199], v[200:203], v[228:231]\n\t" \
  "s_cbranch_vccnz .Lvs%=\n\t" \
  ".Lvok%=:\n\t" \
  "s_nop 4\n\t" \
  "v_rcp_f32 v212, v204\n\t" \
  "v_cndmask_b32 v224, v224, v226, %[nh]\n\t" \
  "v_cndmask_b32 v225, v225, v227, %[nh]\n\t" \
  "v_cndmask_b32 v228, v228, v230, %[nh]\n\t" \
  "v_cndmask_b32 v229, v229, v231, %[nh]\n\t" \
  "v_pk_mul_f32 v[224:225], v[224:225], v[212:213] op_sel_hi:[1,0]\n\t" \
  "v_pk_mul_f32 v[228:229], v[228:229], v[212:213] op_sel_hi:[1,0]\n\t" \
  "v_cvt_pk_bf16_f32 v224, v224, v225\n\t" \
  "v_cvt_pk_bf16_f32 v225, v228, v229\n\t" \
  "buffer_store_dwordx2 v[224:225], v251, %[rs], %[so] offen sc1\n\t" \
  "s_memtime s[90:91]\n\t" \
  "s_setprio 0\n\t" \
  "s_waitcnt lgkmcnt(0)\n\t" \
  "s_mov_b32 %[tout], s90\n\t" \
  "s_mov_b32 %[spins], s92\n\t" \
  "s_branch .Lend%=\n\t" \
  ".Lls%=:\n\t" \
  "s_waitcnt lgkmcnt(0)\n\t" \
  "s_barrier\n\t" \
  "s_mov_b32 %[spins], 0\n\t" \
  "s_branch .Lend%=\n\t" \
  ".Lre%=:\n\t" \
  "ds_read_b64_tr_b16 v[244:245], v250 offset:1536\n\t" \
  "ds_read_b64_tr_b16 v[246:247], v250 offset:1600\n\t" \
  "ds_read_b64_tr_b16 v[232:233], v250 offset:0\n\t" \
  "ds_read_b64_tr_b16 v[234:235], v250 offset:64\n\t" \
  "ds_read_b64_tr_b16 v[236:237], v250 offset:512\n\t" \
  "ds_read_b64_tr_b16 v[238:239], v250 offset:576\n\t" \
  "s_waitcnt lgkmcnt(0)\n\t" \
  "s_branch .Lrb%=\n\t" \
  ".Lvs%=:\n\t" \
  "v_cmp_eq_u32 vcc, %[sent], v252\n\t" \
  "v_cmp_eq_u32 s[94:95], %[sent], v253\n\t" \
  "s_or_b64 s[94:95], s[94:95], vcc\n\t" \
  "s_cbranch_scc0 .Lvok%=\n\t" \
  "s_add_u32 s92, s92, 1\n\t" \
  "s_sleep 2\n\t" \
  "s_and_b32 s93, s92, 63\n\t" \
  "s_cbranch_scc1 .Lvr%=\n\t" \
  "buffer_load_dword v212, off, %[rs], %[sab] sc1\n\t" \
  "s_waitcnt vmcnt(0)\n\t" \
  "v_readfirstlane_b32 s93, v212\n\t" \
  "s_nop 3\n\t" \
  "s_cmp_eq_u32 s93, 1\n\t" \
  "s_cbranch_scc1 .Lvok%=\n\t" \
  "s_cmp_lt_u32 s92, 0x80000\n\t" \
  "s_cbranch_scc1 .Lvr%=\n\t" \
  "v_mov_b32 v212, 1\n\t" \
  "buffer_store_dword v212, off, %[rs], %[sab] sc1\n\t" \
  "s_branch .Lvok%=\n\t" \
  ".Lvr%=:\n\t" \
  "buffer_load_dword v252, v249, %[rs], %[sv] offen offset:0 sc1\n\t" \
  "buffer_load_dword v253, v249, %[rs], %[sv] offen offset:256 sc1\n\t" \
  "s_waitcnt vmcnt(0)\n\t" \
  "v_mfma_f32_16x16x16_bf16 v[224:227], v[252:253], v[240:241], 0\n\t" \
  "v_mfma_f32_16x16x16_bf16 v[228:231], v[252:253], v[242:243], 0\n\t" \
  "v_max_u32 v212, v252, v253\n\t" \
  "v_cmp_le_u32 vcc, %[sent], v212\n\t" \
  "s_nop 4\n\t" \
  "s_branch .Lpv%=\n\t" \
  "s_branch .Lend%=\n\t" \
  ".Lslow%=:\n\t" \
  "s_mov_b64 s[94:95], 0\n\t" \
  "v_cmp_eq_u32 vcc, %[sent], v200\n\t" \
  "s_or_b64 s[94:95], s[94:95], vcc\n\t" \
  "v_cmp_eq_u32 vcc, %[sent], v201\n\t" \
  "s_or_b64 s[94:95], s[94:95], vcc\n\t" \
  "v_cmp_eq_u32 vcc, %[sent], v202\n\t" \
  "s_or_b64 s[94:95], s[94:95], vcc\n\t" \
  "v_cmp_eq_u32 vcc, %[sent], v203\n\t" \
  "s_or_b64 s[94:95], s[94:95], vcc\n\t" \
  "v_cmp_eq_u32 vcc, %[sent], v204\n\t" \
  "s_or_b64 s[94:95], s[94:95], vcc\n\t" \
  "v_cmp_eq_u32 vcc, %[sent], v205\n\t" \
  "s_or_b64 s[94:95], s[94:95], vcc\n\t" \
  "v_cmp_eq_u32 vcc, %[sent], v206\n\t" \
  "s_or_b64 s[94:95], s[94:95], vcc\n\t" \
  "v_cmp_eq_u32 vcc, %[sent], v207\n\t" \
  "s_or_b64 s[94:95], s[94:95], vcc\n\t" \
  "s_cmp_eq_u64 s[94:95], 0\n\t" \
  "s_cbranch_scc1 .Lok%=\n\t" \
  "v_mov_b64 v[192:193], 0\n\t" \
  "v_mov_b64 v[194:195], 0\n\t" \
  "v_mov_b64 v[196:197], 0\n\t" \
  "v_mov_b64 v[198:199], 0\n\t" \
  "s_add_u32 s92, s92, 1\n\t" \
  "s_cmp_eq_u32 s92, 1\n\t" \
  "s_cbranch_scc1 .Lpoll%=\n\t" \
  "s_sleep 2\n\t" \
  "s_and_b32 s93, s92, 63\n\t" \
  "s_cmp_lg_u32 s93, 0\n\t" \
  "s_cbranch_scc1 .Lpoll%=\n\t" \
  "v_mov_b32 v245, 0\n\t" \
  "buffer_load_dword v244, v245, %[rs], %[sab] offen sc1\n\t" \
  "s_waitcnt vmcnt(0)\n\t" \
  "v_readfirstlane_b32 s93, v244\n\t" \
  "s_nop 3\n\t" \
  "s_cmp_eq_u32 s93, 1\n\t" \
  "s_cbranch_scc1 .Lok%=\n\t" \
  "s_cmp_lt_u32 s92, 0x80000\n\t" \
  "s_cbranch_scc1 .Lpoll%=\n\t" \
  "v_mov_b32 v244, 1\n\t" \
  "buffer_store_dword v244, v245, %[rs], %[sab] offen sc1\n\t" \
  "s_branch .Lok%=\n\t" \
  ".Lend%=:\n\t"
#define QK_ASM_3 \
  "s_waitcnt lgkmcnt(0)\n\t" \
  "v_subrev_u32 v244, 0x8000, v250\n\t" \
  "ds_read_b128 v[216:219], v248 offset:45056\n\t" \
  "ds_read_b128 v[220:223], v248 offset:46080\n\t" \
  "ds_read_b128 v[224:227], v248 offset:47104\n\t" \
  "ds_read_b128 v[228:231], v248 offset:48128\n\t" \
  "ds_read_b128 v[208:211], v244 offset:0\n\t" \
  "ds_read_b128 v[212:215], v244 offset:1024\n\t" \
  "ds_read_b128 v[236:239], v244 offset:2048\n\t" \
  "ds_read_b128 v[240:243], v244 offset:3072\n\t" \
  "v_mov_b64 v[192:193], 0\n\t" \
  "v_mov_b64 v[194:195], 0\n\t" \
  "v_mov_b64 v[196:197], 0\n\t" \
  "v_mov_b64 v[198:199], 0\n\t" \
  "s_movk_i32 s92, 0x2000\n\t" \
  ".Lwt%=:\n\t" \
  "ds_read_b64 v[200:201], v248 offset:30720\n\t" \
  "s_memrealtime s[90:91]\n\t" \
  "s_sub_u32 s92, s92, 1\n\t" \
  "s_waitcnt lgkmcnt(0)\n\t" \
  "v_readfirstlane_b32 s93, v200\n\t" \
  "v_readfirstlane_b32 %[tgt], v201\n\t" \
  "s_cbranch_scc1 .Lgo%=\n\t" \
  "s_cmp_eq_u32 s93, %[step]\n\t" \
  "s_cbranch_scc1 .Lwx%=\n\t" \
  "s_sleep 1\n\t" \
  "s_branch .Lwt%=\n\t" \
  ".Lwx%=:\n\t" \
  "s_lshl_b32 s93, s90, 2\n\t" \
  "s_sub_i32 s93, %[tgt], s93\n\t" \
  "s_cmp_le_i32 s93, 0\n\t" \
  "s_cbranch_scc1 .Lgo%=\n\t" \
  "s_min_i32 s93, s93, 8000\n\t" \
  "s_mov_b32 s94, s93\n\t" \
  ".Lsl%=:\n\t" \
  "s_sub_i32 s94, s94, 18\n\t" \
  "s_cmp_lt_i32 s94, 30\n\t" \
  "s_cbranch_scc1 .Lsx%=\n\t" \
  "s_sleep 1\n\t" \
  "s_branch .Lsl%=\n\t" \
  ".Lsx%=:\n\t" \
  "s_sub_i32 s93, s93, 12\n\t" \
  "s_max_i32 s93, s93, 4\n\t" \
  "s_lshr_b32 s93, s93, 2\n\t" \
  "s_add_u32 s93, s93, s90\n\t" \
  "s_memrealtime s[90:91]\n\t" \
  ".Lpace%=:\n\t" \
  "s_memrealtime s[94:95]\n\t" \
  "s_waitcnt lgkmcnt(1)\n\t" \
  "s_sub_i32 s92, s90, s93\n\t" \
  "s_cmp_ge_i32 s92, 0\n\t" \
  "s_cbranch_scc1 .Le%=\n\t" \
  "s_memrealtime s[90:91]\n\t" \
  "s_waitcnt lgkmcnt(1)\n\t" \
  "s_sub_i32 s92, s94, s93\n\t" \
  "s_cmp_lt_i32 s92, 0\n\t" \
  "s_cbranch_scc1 .Lpace%=\n\t" \
  "s_nop 4\n\t" \
  ".Le%=:\n\t" \
  "s_add_u32 s92, s92, s93\n\t" \
  "s_lshl_b32 s92, s92, 2\n\t" \
  "s_sub_i32 s92, %[tgt], s92\n\t" \
  "s_max_i32 s92, s92, 0\n\t" \
  "s_bitcmp0_b32 s92, 3\n\t" \
  "s_cbranch_scc1 .Lf3%=\n\t" \
  "s_nop 15\n\t" \
  ".Lf3%=:\n\t" \
  "s_bitcmp0_b32 s92, 2\n\t" \
  "s_cbranch_scc1 .Lf2%=\n\t" \
  "s_nop 10\n\t" \
  ".Lf2%=:\n\t" \
  ".Lgo%=:\n\t" \
  "s_mov_b32 s92, 0\n\t" \
  ".Lpoll%=:\n\t" \
  "buffer_load_dwordx4 v[200:203], v248, %[rs], %[som] offen sc1\n\t" \
  "buffer_load_dwordx4 v[204:207], v248, %[rs], %[som2] offen sc1\n\t" \
  "s_waitcnt vmcnt(1) lgkmcnt(0)\n\t" \
  "ds_write_b128 v249, v[200:203]\n\t" \
  "v_smfmac_f32_16x16x64_bf16 v[192:195], v[200:203], v[208:215], v251\n\t" \
  "v_max3_u32 v244, v200, v201, v202\n\t" \
  "v_smfmac_f32_16x16x64_bf16 v[196:199], v[200:203], v[236:243], v251\n\t" \
  "s_waitcnt vmcnt(0)\n\t" \
  "ds_write_b128 v249, v[204:207] offset:8192\n\t" \
  "v_smfmac_f32_16x16x64_bf16 v[192:195], v[204:207], v[216:223], v251\n\t" \
  "v_max3_u32 v244, v244, v203, v204\n\t" \
  "v_max3_u32 v244, v244, v205, v206\n\t" \
  "v_smfmac_f32_16x16x64_bf16 v[196:199], v[204:207], v[224:231], v251\n\t" \
  "v_max_u32 v244, v244, v207\n\t" \
  "v_cmp_le_u32 vcc, %[sent], v244\n\t" \
  "s_cbranch_vccnz .Lslow%=\n\t" \
  ".Lok%=:\n\t" \
  "ds_read_b128 v[216:219], v250 offset:32768\n\t" \
  "ds_read_b128 v[220:223], v250 offset:33792\n\t" \
  "ds_read_b128 v[224:227], v250 offset:34816\n\t" \
  "ds_read_b128 v[228:231], v250 offset:35840\n\t" \
  "s_waitcnt lgkmcnt(4)\n\t" \
  "s_barrier\n\t" \
  "ds_read_b128 v[208:211], v248 offset:2048\n\t" \
  "ds_read_b128 v[212:215], v248 offset:5120\n\t" \
  "ds_read_b128 v[236:239], v248 offset:6144\n\t" \
  "ds_read_b128 v[240:243], v248 offset:7168\n\t" \
  "s_cmp_eq_u32 %[step], 63\n\t" \
  "s_cbranch_scc1 .Lls%=\n\t" \
  "s_waitcnt lgkmcnt(3)\n\t" \
  "ds_read_b128 v[244:247], v248 offset:12288\n\t" \
  "v_smfmac_f32_16x16x64_bf16 v[192:195], v[208:211], v[216:223], v251\n\t" \
  "ds_read_b128 v[216:219], v250 offset:0\n\t" \
  "v_smfmac_f32_16x16x64_bf16 v[196:199], v[208:211], v[224:231], v251\n\t" \
  "s_waitcnt lgkmcnt(4)\n\t" \
  "ds_read_b128 v[220:223], v250 offset:1024\n\t" \
  "v_smfmac_f32_16x16x64_bf16 v[192:195], v[212:215], v[0:7], v251\n\t" \
  "ds_read_b128 v[224:227], v250 offset:2048\n\t" \
  "v_smfmac_f32_16x16x64_bf16 v[196:199], v[212:215], v[8:15], v251\n\t" \
  "s_waitcnt lgkmcnt(5)\n\t" \
  "ds_read_b128 v[228:231], v250 offset:3072\n\t" \
  "v_smfmac_f32_16x16x64_bf16 v[192:195], v[236:239], v[16:23], v251\n\t" \
  "ds_read_b128 v[200:203], v248 offset:8192\n\t" \
  "v_smfmac_f32_16x16x64_bf16 v[196:199], v[236:239], v[24:31], v251\n\t" \
  "s_waitcnt lgkmcnt(6)\n\t" \
  "ds_read_b128 v[204:207], v248 offset:9216\n\t" \
  "v_smfmac_f32_16x16x64_bf16 v[192:195], v[240:243], v[32:39], v251\n\t" \
  "ds_read_b128 v[208:211], v248 offset:10240\n\t" \
  "v_smfmac_f32_16x16x64_bf16 v[196:199], v[240:243], v[40:47], v251\n\t" \
  "s_waitcnt lgkmcnt(5)\n\t" \
  "ds_read_b128 v[212:215], v248 offset:13312\n\t" \
  "v_smfmac_f32_16x16x64_bf16 v[192:195], v[244:247], v[216:223], v251\n\t" \
  "s_waitcnt lgkmcnt(4)\n\t" \
  "v_smfmac_f32_16x16x64_bf16 v[196:199], v[244:247], v[224:231], v251\n\t" \
  "s_waitcnt lgkmcnt(3)\n\t" \
  "ds_read_b128 v[236:239], v248 offset:14336\n\t" \
  "v_smfmac_f32_16x16x64_bf16 v[192:195], v[200:203], v[48:55], v251\n\t" \
  "ds_read_b128 v[240:243], v248 offset:15360\n\t" \
  "v_smfmac_f32_16x16x64_bf16 v[196:199], v[200:203], v[56:63], v251\n\t" \
  "s_waitcnt lgkmcnt(4)\n\t" \
  "v_smfmac_f32_16x16x64_bf16 v[192:195], v[204:207], v[64:71], v251\n\t" \
  "ds_read_b128 v[244:247], v248 offset:0\n\t" \
  "v_smfmac_f32_16x16x64_bf16 v[196:199], v[204:207], v[72:79], v251\n\t" \
  "s_waitcnt lgkmcnt(4)\n\t" \
  "v_smfmac_f32_16x16x64_bf16 v[192:195], v[208:211], v[80:87], v251\n\t" \
  "ds_read_b128 v[200:203], v248 offset:1024\n\t" \
  "v_smfmac_f32_16x16x64_bf16 v[196:199], v[208:211], v[88:95], v251\n\t" \
  "s_waitcnt lgkmcnt(4)\n\t" \
  "v_smfmac_f32_16x16x64_bf16 v[192:195], v[212:215], v[96:103], v251\n\t" \
  "ds_read_b128 v[204:207], v248 offset:4096\n\t" \
  "v_smfmac_f32_16x16x64_bf16 v[196:199], v[212:215], v[104:111], v251\n\t" \
  "s_waitcnt lgkmcnt(3)\n\t" \
  "v_smfmac_f32_16x16x64_bf16 v[192:195], v[236:239], v[112:119], v251\n\t" \
  "v_smfmac_f32_16x16x64_bf16 v[196:199], v[236:239], v[120:127], v251\n\t" \
  "v_smfmac_f32_16x16x64_bf16 v[192:195], v[240:243], v[128:135], v251\n\t" \
  "v_smfmac_f32_16x16x64_bf16 v[196:199], v[240:243], v[136:143], v251\n\t" \
  "s_waitcnt lgkmcnt(1)\n\t" \
  "v_smfmac_f32_16x16x64_bf16 v[192:195], v[244:247], v[144:151], v251\n\t" \
  "v_smfmac_f32_16x16x64_bf16 v[196:199], v[244:247], v[152:159], v251\n\t" \
  "v_smfmac_f32_16x16x64_bf16 v[192:195], v[200:203], v[160:167], v251\n\t" \
  "v_smfmac_f32_16x16x64_bf16 v[196:199], v[200:203], v[168:175], v251\n\t" \
  "s_waitcnt lgkmcnt(0)\n\t" \
  "v_smfmac_f32_16x16x64_bf16 v[192:195], v[204:207], v[176:183], v251\n\t" \
  "v_smfmac_f32_16x16x64_bf16 v[196:199], v[204:207], v[184:191], v251\n\t" \
  "ds_read_b128 v[200:203], v248 offset:19456\n\t" \
  "ds_read_b128 v[204:207], v248 offset:23552\n\t" \
  "v_mov_b32 v250, v232\n\t" \
  "v_mov_b32 v249, v233\n\t" \
  "s_nop 2\n\t" \
  "v_add_f32 v192, v192, v194\n\t" \
  "v_add_f32 v193, v193, v195\n\t" \
  "v_add_f32 v196, v196, v198\n\t" \
  "v_add_f32 v197, v197, v199\n\t" \
  "v_cvt_pk_bf16_f32 v192, v192, v193\n\t" \
  "v_cvt_pk_bf16_f32 v196, v196, v197\n\t" \
  "ds_write2_b32 v252, v192, v196 offset1:64\n\t" \
  "v_mov_b32 v251, v234\n\t" \
  "ds_read_b64_tr_b16 v[244:245], v250 offset:1536\n\t" \
  "ds_read_b64_tr_b16 v[246:247], v250 offset:1600\n\t" \
  "v_mov_b32 v212, %[step]\n\t" \
  "s_mov_b64 exec, 1\n\t" \
  "ds_write_b32 v248, v212 offset:28720\n\t" \
  "s_mov_b64 exec, -1\n\t" \
  "buffer_load_dwordx4 v[192:195], v248, %[rt], %[svt] offen offset:0 sc1\n\t" \
  "buffer_load_dwordx4 v[196:199], v248, %[rt], %[svt] offen offset:1024 sc1\n\t" \
  "s_waitcnt lgkmcnt(1)\n\t" \
  "v_mfma_f32_16x16x32_bf16 v[200:203], v[200:203], v[244:247], 0\n\t" \
  "v_mfma_f32_16x16x32_bf16 v[204:207], v[204:207], v[244:247], 0\n\t" \
  "s_nop 7\n\t" \
  "v_cndmask_b32 v216, v200, v204, %[nh]\n\t" \
  "v_cndmask_b32 v217, v201, v205, %[nh]\n\t" \
  "v_cndmask_b32 v218, v202, v206, %[nh]\n\t" \
  "v_cndmask_b32 v219, v203, v207, %[nh]\n\t" \
  "ds_write_b128 v248, v[216:219] offset:19456\n\t" \
  "buffer_load_dword v252, v249, %[rs], %[sv] offen offset:0 sc1\n\t" \
  "buffer_load_dword v253, v249, %[rs], %[sv] offen offset:256 sc1\n\t" \
  "ds_read_b32 v212, v248 offset:28672\n\t" \
  "ds_read_b64_tr_b16 v[232:233], v250 offset:0\n\t" \
  "ds_read_b64_tr_b16 v[234:235], v250 offset:64\n\t" \
  "ds_read_b64_tr_b16 v[236:237], v250 offset:512\n\t" \
  "ds_read_b64_tr_b16 v[238:239], v250 offset:576\n\t" \
  "ds_read_b64_tr_b16 v[240:241], v250 offset:1024\n\t" \
  "ds_read_b64_tr_b16 v[242:243], v250 offset:1088\n\t" \
  "s_setprio 3\n\t" \
  "s_waitcnt lgkmcnt(0)\n\t" \
  "v_cmp_ne_u32 vcc, %[step], v212\n\t" \
  "s_barrier\n\t" \
  "s_and_b32 s93, vcc_lo, 0xf\n\t" \
  "s_cbranch_scc1 .Lre%=\n\t" \
  ".Lrb%=:\n\t" \
  "ds_read_b64_tr_b16 v[212:213], v250 offset:3584\n\t" \
  "ds_read_b64_tr_b16 v[214:215], v250 offset:3648\n\t" \
  "ds_read_b64_tr_b16 v[200:201], v250 offset:2048\n\t" \
  "ds_read_b64_tr_b16 v[202:203], v250 offset:2112\n\t" \
  "ds_read_b128 v[220:223], v248 offset:16384\n\t" \
  "ds_read_b128 v[224:227], v248 offset:17408\n\t" \
  "ds_read_b128 v[228:231], v248 offset:18432\n\t" \
  "ds_read_b64_tr_b16 v[204:205], v250 offset:2560\n\t" \
  "ds_read_b64_tr_b16 v[206:207], v250 offset:2624\n\t" \
  "ds_read_b64_tr_b16 v[208:209], v250 offset:3072\n\t" \
  "ds_read_b64_tr_b16 v[210:211], v250 offset:3136\n\t" \
  "s_waitcnt lgkmcnt(9)\n\t" \
  "v_mfma_f32_16x16x32_bf16 v[212:215], v[212:215], v[244:247], 0\n\t" \
  "s_waitcnt lgkmcnt(7)\n\t" \
  "v_mfma_f32_16x16x32_bf16 v[212:215], v[200:203], v[232:235], v[212:215]\n\t" \
  "s_waitcnt lgkmcnt(6)\n\t" \
  "v_pk_add_f32 v[216:217], v[216:217], v[220:221]\n\t" \
  "v_pk_add_f32 v[218:219], v[218:219], v[222:223]\n\t" \
  "s_waitcnt lgkmcnt(4)\n\t" \
  "v_pk_add_f32 v[224:225], v[224:225], v[228:229]\n\t" \
  "v_pk_add_f32 v[226:227], v[226:227], v[230:231]\n\t" \
  "v_pk_add_f32 v[216:217], v[216:217], v[224:225]\n\t" \
  "v_pk_add_f32 v[218:219], v[218:219], v[226:227]\n\t" \
  "s_waitcnt lgkmcnt(2)\n\t" \
  "v_mfma_f32_16x16x32_bf16 v[212:215], v[204:207], v[236:239], v[212:215]\n\t" \
  "v_max3_f32 v224, v216, v217, v218\n\t" \
  "v_max_f32 v224, v224, v219\n\t" \
  "s_waitcnt lgkmcnt(0)\n\t" \
  "v_mfma_f32_16x16x32_bf16 v[212:215], v[208:211], v[240:243], v[212:215]\n\t" \
  "v_max_f32_dpp v225, v224, v224 row_ror:8 row_mask:0xf bank_mask:0xf\n\t" \
  "v_max_f32_dpp v226, v224, v224 row_ror:8 row_mask:0xf bank_mask:0xf\n\t" \
  "s_nop 1\n\t" \
  "v_permlane32_swap_b32 v225, v226\n\t" \
  "v_max_f32 v231, v225, v226\n\t" \
  "v_mov_b32 v220, 0x3f803f80\n\t" \
  "v_mov_b32 v221, 0x3f803f80\n\t" \
  "v_mov_b64 v[222:223], v[220:221]\n\t" \
  "v_mov_b32 v245, 0\n\t" \
  "v_mov_b32 v246, 0\n\t" \
  "v_max3_f32 v200, v212, v213, v231\n\t" \
  "v_max3_f32 v201, v200, v214, v215\n\t" \
  "v_max3_f32 v202, v200, v214, v215\n\t" \
  "v_cndmask_b32 v212, v212, v214, %[up]\n\t" \
  "v_cndmask_b32 v213, v213, v215, %[up]\n\t" \
  "v_permlane16_swap_b32 v201, v202\n\t" \
  "v_max_f32 v200, v201, v202\n\t" \
  "v_mul_f32 v200, %[negc], v200\n\t" \
  "v_fma_f32 v212, v212, %[c], v200\n\t" \
  "v_fma_f32 v213, v213, %[c], v200\n\t" \
  "v_exp_f32 v212, v212\n\t" \
  "v_fma_f32 v216, v216, %[c], v200\n\t" \
  "v_exp_f32 v213, v213\n\t" \
  "v_fma_f32 v217, v217, %[c], v200\n\t" \
  "v_exp_f32 v216, v216\n\t" \
  "v_fma_f32 v218, v218, %[c], v200\n\t" \
  "v_cvt_pk_bf16_f32 v244, v212, v213\n\t" \
  "v_cvt_pk_bf16_f32 v247, v212, v213\n\t" \
  "v_exp_f32 v217, v217\n\t" \
  "v_fma_f32 v219, v219, %[c], v200\n\t" \
  "v_mfma_f32_16x16x16_bf16 v[208:211], v[220:221], v[244:245], 0\n\t" \
  "v_exp_f32 v218, v218\n\t" \
  "s_waitcnt vmcnt(0)\n\t" \
  "v_mfma_f32_16x16x16_bf16 v[224:227], v[252:253], v[244:245], 0\n\t" \
  "v_cvt_pk_bf16_f32 v206, v216, v217\n\t" \
  "v_cvt_pk_bf16_f32 v204, v216, v217\n\t" \
  "v_exp_f32 v219, v219\n\t" \
  "v_mfma_f32_16x16x16_bf16 v[228:231], v[252:253], v[246:247], 0\n\t" \
  "v_cvt_pk_bf16_f32 v207, v218, v219\n\t" \
  "v_cvt_pk_bf16_f32 v205, v218, v219\n\t" \
  "v_mov_b32_dpp v204, v206 row_ror:8 row_mask:0xf bank_mask:0xc\n\t" \
  "v_mov_b32_dpp v206, v206 row_ror:8 row_mask:0xf bank_mask:0x3\n\t" \
  "v_mov_b32_dpp v205, v207 row_ror:8 row_mask:0xf bank_mask:0xc\n\t" \
  "v_mov_b32_dpp v207, v207 row_ror:8 row_mask:0xf bank_mask:0x3\n\t" \
  "v_max_u32 v200, v252, v253\n\t" \
  "v_cmp_le_u32 vcc, %[sent], v200\n\t" \
  "v_mfma_f32_16x16x32_bf16 v[208:211], v[220:223], v[204:207], v[208:211]\n\t" \
  ".Lpv%=:\n\t" \
  "v_mfma_f32_16x16x32_bf16 v[224:227], v[192:195], v[204:207], v[224:227]\n\t" \
  "v_mfma_f32_16x16x32_bf16 v[228:231], v[196:199], v[204:207], v[228:231]\n\t" \
  "s_cbranch_vccnz .Lvs%=\n\t" \
  ".Lvok%=:\n\t" \
  "s_nop 4\n\t" \
  "v_rcp_f32 v200, v208\n\t" \
  "v_cndmask_b32 v224, v224, v226, %[nh]\n\t" \
  "v_cndmask_b32 v225, v225, v227, %[nh]\n\t" \
  "v_cndmask_b32 v228, v228, v230, %[nh]\n\t" \
  "v_cndmask_b32 v229, v229, v231, %[nh]\n\t" \
  "v_pk_mul_f32 v[224:225], v[224:225], v[200:201] op_sel_hi:[1,0]\n\t" \
  "v_pk_mul_f32 v[228:229], v[228:229], v[200:201] op_sel_hi:[1,0]\n\t" \
  "v_cvt_pk_bf16_f32 v224, v224, v225\n\t" \
  "v_cvt_pk_bf16_f32 v225, v228, v229\n\t" \
  "buffer_store_dwordx2 v[224:225], v251, %[rs], %[so] offen sc1\n\t" \
  "s_memtime s[90:91]\n\t" \
  "s_setprio 0\n\t" \
  "s_waitcnt lgkmcnt(0)\n\t" \
  "s_mov_b32 %[tout], s90\n\t" \
  "s_mov_b32 %[spins], s92\n\t" \
  "s_branch .Lend%=\n\t" \
  ".Lls%=:\n\t" \
  "s_waitcnt lgkmcnt(0)\n\t" \
  "s_barrier\n\t" \
  "s_mov_b32 %[spins], 0\n\t" \
  "s_branch .Lend%=\n\t" \
  ".Lre%=:\n\t" \
  "ds_read_b64_tr_b16 v[232:233], v250 offset:0\n\t" \
  "ds_read_b64_tr_b16 v[234:235], v250 offset:64\n\t" \
  "ds_read_b64_tr_b16 v[236:237], v250 offset:512\n\t" \
  "ds_read_b64_tr_b16 v[238:239], v250 offset:576\n\t" \
  "ds_read_b64_tr_b16 v[240:241], v250 offset:1024\n\t" \
  "ds_read_b64_tr_b16 v[242:243], v250 offset:1088\n\t" \
  "s_waitcnt lgkmcnt(0)\n\t" \
  "s_branch .Lrb%=\n\t" \
  ".Lvs%=:\n\t" \
  "v_cmp_eq_u32 vcc, %[sent], v252\n\t" \
  "v_cmp_eq_u32 s[94:95], %[sent], v253\n\t" \
  "s_or_b64 s[94:95], s[94:95], vcc\n\t" \
  "s_cbranch_scc0 .Lvok%=\n\t" \
  "s_add_u32 s92, s92, 1\n\t" \
  "s_sleep 2\n\t" \
  "s_and_b32 s93, s92, 63\n\t" \
  "s_cbranch_scc1 .Lvr%=\n\t" \
  "buffer_load_dword v200, off, %[rs], %[sab] sc1\n\t" \
  "s_waitcnt vmcnt(0)\n\t" \
  "v_readfirstlane_b32 s93, v200\n\t" \
  "s_nop 3\n\t" \
  "s_cmp_eq_u32 s93, 1\n\t" \
  "s_cbranch_scc1 .Lvok%=\n\t" \
  "s_cmp_lt_u32 s92, 0x80000\n\t" \
  "s_cbranch_scc1 .Lvr%=\n\t" \
  "v_mov_b32 v200, 1\n\t" \
  "buffer_store_dword v200, off, %[rs], %[sab] sc1\n\t" \
  "s_branch .Lvok%=\n\t" \
  ".Lvr%=:\n\t" \
  "buffer_load_dword v252, v249, %[rs], %[sv] offen offset:0 sc1\n\t" \
  "buffer_load_dword v253, v249, %[rs], %[sv] offen offset:256 sc1\n\t" \
  "s_waitcnt vmcnt(0)\n\t" \
  "v_mfma_f32_16x16x16_bf16 v[224:227], v[252:253], v[244:245], 0\n\t" \
  "v_mfma_f32_16x16x16_bf16 v[228:231], v[252:253], v[246:247], 0\n\t" \
  "v_max_u32 v200, v252, v253\n\t" \
  "v_cmp_le_u32 vcc, %[sent], v200\n\t" \
  "s_nop 4\n\t" \
  "s_branch .Lpv%=\n\t" \
  "s_branch .Lend%=\n\t" \
  ".Lslow%=:\n\t" \
  "s_mov_b64 s[94:95], 0\n\t" \
  "v_cmp_eq_u32 vcc, %[sent], v200\n\t" \
  "s_or_b64 s[94:95], s[94:95], vcc\n\t" \
  "v_cmp_eq_u32 vcc, %[sent], v201\n\t" \
  "s_or_b64 s[94:95], s[94:95], vcc\n\t" \
  "v_cmp_eq_u32 vcc, %[sent], v202\n\t" \
  "s_or_b64 s[94:95], s[94:95], vcc\n\t" \
  "v_cmp_eq_u32 vcc, %[sent], v203\n\t" \
  "s_or_b64 s[94:95], s[94:95], vcc\n\t" \
  "v_cmp_eq_u32 vcc, %[sent], v204\n\t" \
  "s_or_b64 s[94:95], s[94:95], vcc\n\t" \
  "v_cmp_eq_u32 vcc, %[sent], v205\n\t" \
  "s_or_b64 s[94:95], s[94:95], vcc\n\t" \
  "v_cmp_eq_u32 vcc, %[sent], v206\n\t" \
  "s_or_b64 s[94:95], s[94:95], vcc\n\t" \
  "v_cmp_eq_u32 vcc, %[sent], v207\n\t" \
  "s_or_b64 s[94:95], s[94:95], vcc\n\t" \
  "s_cmp_eq_u64 s[94:95], 0\n\t" \
  "s_cbranch_scc1 .Lok%=\n\t" \
  "v_mov_b64 v[192:193], 0\n\t" \
  "v_mov_b64 v[194:195], 0\n\t" \
  "v_mov_b64 v[196:197], 0\n\t" \
  "v_mov_b64 v[198:199], 0\n\t" \
  "s_add_u32 s92, s92, 1\n\t" \
  "s_cmp_eq_u32 s92, 1\n\t" \
  "s_cbranch_scc1 .Lpoll%=\n\t" \
  "s_sleep 2\n\t" \
  "s_and_b32 s93, s92, 63\n\t" \
  "s_cmp_lg_u32 s93, 0\n\t" \
  "s_cbranch_scc1 .Lpoll%=\n\t" \
  "v_mov_b32 v245, 0\n\t" \
  "buffer_load_dword v244, v245, %[rs], %[sab] offen sc1\n\t" \
  "s_waitcnt vmcnt(0)\n\t" \
  "v_readfirstlane_b32 s93, v244\n\t" \
  "s_nop 3\n\t" \
  "s_cmp_eq_u32 s93, 1\n\t" \
  "s_cbranch_scc1 .Lok%=\n\t" \
  "s_cmp_lt_u32 s92, 0x80000\n\t" \
  "s_cbranch_scc1 .Lpoll%=\n\t" \
  "v_mov_b32 v244, 1\n\t" \
  "buffer_store_dword v244, v245, %[rs], %[sab] offen sc1\n\t" \
  "s_branch .Lok%=\n\t" \
  ".Lend%=:\n\t"
#define QK_ASM_4 \
  "s_waitcnt lgkmcnt(0)\n\t" \
  "v_subrev_u32 v244, 0x8000, v250\n\t" \
  "ds_read_b128 v[216:219], v248 offset:49152\n\t" \
  "ds_read_b128 v[220:223], v248 offset:50176\n\t" \
  "ds_read_b128 v[224:227], v248 offset:51200\n\t" \
  "ds_read_b128 v[228:231], v248 offset:52224\n\t" \
  "ds_read_b128 v[208:211], v244 offset:0\n\t" \
  "ds_read_b128 v[212:215], v244 offset:1024\n\t" \
  "ds_read_b128 v[236:239], v244 offset:2048\n\t" \
  "ds_read_b128 v[240:243], v244 offset:3072\n\t" \
  "v_mov_b64 v[192:193], 0\n\t" \
  "v_mov_b64 v[194:195], 0\n\t" \
  "v_mov_b64 v[196:197], 0\n\t" \
  "v_mov_b64 v[198:199], 0\n\t" \
  "s_movk_i32 s92, 0x2000\n\t" \
  ".Lwt%=:\n\t" \
  "ds_read_b64 v[200:201], v248 offset:30720\n\t" \
  "s_memrealtime s[90:91]\n\t" \
  "s_sub_u32 s92, s92, 1\n\t" \
  "s_waitcnt lgkmcnt(0)\n\t" \
  "v_readfirstlane_b32 s93, v200\n\t" \
  "v_readfirstlane_b32 %[tgt], v201\n\t" \
  "s_cbranch_scc1 .Lgo%=\n\t" \
  "s_cmp_eq_u32 s93, %[step]\n\t" \
  "s_cbranch_scc1 .Lwx%=\n\t" \
  "s_sleep 1\n\t" \
  "s_branch .Lwt%=\n\t" \
  ".Lwx%=:\n\t" \
  "s_lshl_b32 s93, s90, 2\n\t" \
  "s_sub_i32 s93, %[tgt], s93\n\t" \
  "s_cmp_le_i32 s93, 0\n\t" \
  "s_cbranch_scc1 .Lgo%=\n\t" \
  "s_min_i32 s93, s93, 8000\n\t" \
  "s_mov_b32 s94, s93\n\t" \
  ".Lsl%=:\n\t" \
  "s_sub_i32 s94, s94, 18\n\t" \
  "s_cmp_lt_i32 s94, 30\n\t" \
  "s_cbranch_scc1 .Lsx%=\n\t" \
  "s_sleep 1\n\t" \
  "s_branch .Lsl%=\n\t" \
  ".Lsx%=:\n\t" \
  "s_sub_i32 s93, s93, 12\n\t" \
  "s_max_i32 s93, s93, 4\n\t" \
  "s_lshr_b32 s93, s93, 2\n\t" \
  "s_add_u32 s93, s93, s90\n\t" \
  "s_memrealtime s[90:91]\n\t" \
  ".Lpace%=:\n\t" \
  "s_memrealtime s[94:95]\n\t" \
  "s_waitcnt lgkmcnt(1)\n\t" \
  "s_sub_i32 s92, s90, s93\n\t" \
  "s_cmp_ge_i32 s92, 0\n\t" \
  "s_cbranch_scc1 .Le%=\n\t" \
  "s_memrealtime s[90:91]\n\t" \
  "s_waitcnt lgkmcnt(1)\n\t" \
  "s_sub_i32 s92, s94, s93\n\t" \
  "s_cmp_lt_i32 s92, 0\n\t" \
  "s_cbranch_scc1 .Lpace%=\n\t" \
  "s_nop 4\n\t" \
  ".Le%=:\n\t" \
  "s_add_u32 s92, s92, s93\n\t" \
  "s_lshl_b32 s92, s92, 2\n\t" \
  "s_sub_i32 s92, %[tgt], s92\n\t" \
  "s_max_i32 s92, s92, 0\n\t" \
  "s_bitcmp0_b32 s92, 3\n\t" \
  "s_cbranch_scc1 .Lf3%=\n\t" \
  "s_nop 15\n\t" \
  ".Lf3%=:\n\t" \
  "s_bitcmp0_b32 s92, 2\n\t" \
  "s_cbranch_scc1 .Lf2%=\n\t" \
  "s_nop 10\n\t" \
  ".Lf2%=:\n\t" \
  ".Lgo%=:\n\t" \
  "s_mov_b32 s92, 0\n\t" \
  ".Lpoll%=:\n\t" \
  "buffer_load_dwordx4 v[200:203], v248, %[rs], %[som] offen sc1\n\t" \
  "buffer_load_dwordx4 v[204:207], v248, %[rs], %[som2] offen sc1\n\t" \
  "s_waitcnt vmcnt(1) lgkmcnt(0)\n\t" \
  "ds_write_b128 v249, v[200:203]\n\t" \
  "v_smfmac_f32_16x16x64_bf16 v[192:195], v[200:203], v[208:215], v251\n\t" \
  "v_max3_u32 v244, v200, v201, v202\n\t" \
  "v_smfmac_f32_16x16x64_bf16 v[196:199], v[200:203], v[236:243], v251\n\t" \
  "s_waitcnt vmcnt(0)\n\t" \
  "ds_write_b128 v249, v[204:207] offset:8192\n\t" \
  "v_smfmac_f32_16x16x64_bf16 v[192:195], v[204:207], v[216:223], v251\n\t" \
  "v_max3_u32 v244, v244, v203, v204\n\t" \
  "v_max3_u32 v244, v244, v205, v206\n\t" \
  "v_smfmac_f32_16x16x64_bf16 v[196:199], v[204:207], v[224:231], v251\n\t" \
  "v_max_u32 v244, v244, v207\n\t" \
  "v_cmp_le_u32 vcc, %[sent], v244\n\t" \
  "s_cbranch_vccnz .Lslow%=\n\t" \
  ".Lok%=:\n\t" \
  "ds_read_b128 v[216:219], v250 offset:32768\n\t" \
  "ds_read_b128 v[220:223], v250 offset:33792\n\t" \
  "ds_read_b128 v[224:227], v250 offset:34816\n\t" \
  "ds_read_b128 v[228:231], v250 offset:35840\n\t" \
  "s_waitcnt lgkmcnt(4)\n\t" \
  "s_barrier\n\t" \
  "ds_read_b128 v[208:211], v248 offset:5120\n\t" \
  "ds_read_b128 v[212:215], v248 offset:6144\n\t" \
  "ds_read_b128 v[236:239], v248 offset:7168\n\t" \
  "ds_read_b128 v[240:243], v248 offset:8192\n\t" \
  "s_waitcnt lgkmcnt(3)\n\t" \
  "ds_read_b128 v[244:247], v248 offset:13312\n\t" \
  "v_smfmac_f32_16x16x64_bf16 v[192:195], v[208:211], v[216:223], v251\n\t" \
  "ds_read_b128 v[216:219], v250 offset:0\n\t" \
  "v_smfmac_f32_16x16x64_bf16 v[196:199], v[208:211], v[224:231], v251\n\t" \
  "s_waitcnt lgkmcnt(4)\n\t" \
  "ds_read_b128 v[220:223], v250 offset:1024\n\t" \
  "v_smfmac_f32_16x16x64_bf16 v[192:195], v[212:215], v[0:7], v251\n\t" \
  "ds_read_b128 v[224:227], v250 offset:2048\n\t" \
  "v_smfmac_f32_16x16x64_bf16 v[196:199], v[212:215], v[8:15], v251\n\t" \
  "s_waitcnt lgkmcnt(5)\n\t" \
  "ds_read_b128 v[228:231], v250 offset:3072\n\t" \
  "v_smfmac_f32_16x16x64_bf16 v[192:195], v[236:239], v[16:23], v251\n\t" \
  "ds_read_b128 v[200:203], v248 offset:9216\n\t" \
  "v_smfmac_f32_16x16x64_bf16 v[196:199], v[236:239], v[24:31], v251\n\t" \
  "s_waitcnt lgkmcnt(6)\n\t" \
  "ds_read_b128 v[204:207], v248 offset:10240\n\t" \
  "v_smfmac_f32_16x16x64_bf16 v[192:195], v[240:243], v[32:39], v251\n\t" \
  "ds_read_b128 v[208:211], v248 offset:11264\n\t" \
  "v_smfmac_f32_16x16x64_bf16 v[196:199], v[240:243], v[40:47], v251\n\t" \
  "s_waitcnt lgkmcnt(5)\n\t" \
  "ds_read_b128 v[212:215], v248 offset:14336\n\t" \
  "v_smfmac_f32_16x16x64_bf16 v[192:195], v[244:247], v[216:223], v251\n\t" \
  "s_waitcnt lgkmcnt(4)\n\t" \
  "v_smfmac_f32_16x16x64_bf16 v[196:199], v[244:247], v[224:231], v251\n\t" \
  "s_waitcnt lgkmcnt(3)\n\t" \
  "ds_read_b128 v[236:239], v248 offset:15360\n\t" \
  "v_smfmac_f32_16x16x64_bf16 v[192:195], v[200:203], v[48:55], v251\n\t" \
  "ds_read_b128 v[240:243], v248 offset:0\n\t" \
  "v_smfmac_f32_16x16x64_bf16 v[196:199], v[200:203], v[56:63], v251\n\t" \
  "s_waitcnt lgkmcnt(4)\n\t" \
  "v_smfmac_f32_16x16x64_bf16 v[192:195], v[204:207], v[64:71], v251\n\t" \
  "ds_read_b128 v[244:247], v248 offset:1024\n\t" \
  "v_smfmac_f32_16x16x64_bf16 v[196:199], v[204:207], v[72:79], v251\n\t" \
  "s_waitcnt lgkmcnt(4)\n\t" \
  "v_smfmac_f32_16x16x64_bf16 v[192:195], v[208:211], v[80:87], v251\n\t" \
  "ds_read_b128 v[200:203], v248 offset:2048\n\t" \
  "v_smfmac_f32_16x16x64_bf16 v[196:199], v[208:211], v[88:95], v251\n\t" \
  "s_waitcnt lgkmcnt(4)\n\t" \
  "v_smfmac_f32_16x16x64_bf16 v[192:195], v[212:215], v[96:103], v251\n\t" \
  "ds_read_b128 v[204:207], v248 offset:3072\n\t" \
  "v_smfmac_f32_16x16x64_bf16 v[196:199], v[212:215], v[104:111], v251\n\t" \
  "s_waitcnt lgkmcnt(3)\n\t" \
  "v_smfmac_f32_16x16x64_bf16 v[192:195], v[236:239], v[112:119], v251\n\t" \
  "v_smfmac_f32_16x16x64_bf16 v[196:199], v[236:239], v[120:127], v251\n\t" \
  "v_smfmac_f32_16x16x64_bf16 v[192:195], v[240:243], v[128:135], v251\n\t" \
  "v_smfmac_f32_16x16x64_bf16 v[196:199], v[240:243], v[136:143], v251\n\t" \
  "s_waitcnt lgkmcnt(1)\n\t" \
  "v_smfmac_f32_16x16x64_bf16 v[192:195], v[244:247], v[144:151], v251\n\t" \
  "v_smfmac_f32_16x16x64_bf16 v[196:199], v[244:247], v[152:159], v251\n\t" \
  "v_smfmac_f32_16x16x64_bf16 v[192:195], v[200:203], v[160:167], v251\n\t" \
  "v_smfmac_f32_16x16x64_bf16 v[196:199], v[200:203], v[168:175], v251\n\t" \
  "s_waitcnt lgkmcnt(0)\n\t" \
  "v_smfmac_f32_16x16x64_bf16 v[192:195], v[204:207], v[176:183], v251\n\t" \
  "v_smfmac_f32_16x16x64_bf16 v[196:199], v[204:207], v[184:191], v251\n\t" \
  "s_nop 6\n\t" \
  "v_add_f32 v192, v192, v194\n\t" \
  "v_add_f32 v193, v193, v195\n\t" \
  "v_add_f32 v196, v196, v198\n\t" \
  "v_add_f32 v197, v197, v199\n\t" \
  "v_cvt_pk_bf16_f32 v192, v192, v193\n\t" \
  "v_cvt_pk_bf16_f32 v196, v196, v197\n\t" \
  "ds_write2_b32 v252, v192, v196 offset1:64\n\t" \
  "s_waitcnt lgkmcnt(0)\n\t" \
  "s_barrier\n\t" \
  "buffer_store_dword v192, v235, %[rs], %[sk] offen sc1\n\t" \
  "buffer_store_dword v196, v235, %[rs], %[sk] offen offset:256 sc1\n\t" \
  "s_mov_b32 %[spins], s92\n\t" \
  "s_branch .Lend%=\n\t" \
  ".Lslow%=:\n\t" \
  "s_mov_b64 s[94:95], 0\n\t" \
  "v_cmp_eq_u32 vcc, %[sent], v200\n\t" \
  "s_or_b64 s[94:95], s[94:95], vcc\n\t" \
  "v_cmp_eq_u32 vcc, %[sent], v201\n\t" \
  "s_or_b64 s[94:95], s[94:95], vcc\n\t" \
  "v_cmp_eq_u32 vcc, %[sent], v202\n\t" \
  "s_or_b64 s[94:95], s[94:95], vcc\n\t" \
  "v_cmp_eq_u32 vcc, %[sent], v203\n\t" \
  "s_or_b64 s[94:95], s[94:95], vcc\n\t" \
  "v_cmp_eq_u32 vcc, %[sent], v204\n\t" \
  "s_or_b64 s[94:95], s[94:95], vcc\n\t" \
  "v_cmp_eq_u32 vcc, %[sent], v205\n\t" \
  "s_or_b64 s[94:95], s[94:95], vcc\n\t" \
  "v_cmp_eq_u32 vcc, %[sent], v206\n\t" \
  "s_or_b64 s[94:95], s[94:95], vcc\n\t" \
  "v_cmp_eq_u32 vcc, %[sent], v207\n\t" \
  "s_or_b64 s[94:95], s[94:95], vcc\n\t" \
  "s_cmp_eq_u64 s[94:95], 0\n\t" \
  "s_cbranch_scc1 .Lok%=\n\t" \
  "v_mov_b64 v[192:193], 0\n\t" \
  "v_mov_b64 v[194:195], 0\n\t" \
  "v_mov_b64 v[196:197], 0\n\t" \
  "v_mov_b64 v[198:199], 0\n\t" \
  "s_add_u32 s92, s92, 1\n\t" \
  "s_cmp_eq_u32 s92, 1\n\t" \
  "s_cbranch_scc1 .Lpoll%=\n\t" \
  "s_sleep 2\n\t" \
  "s_and_b32 s93, s92, 63\n\t" \
  "s_cmp_lg_u32 s93, 0\n\t" \
  "s_cbranch_scc1 .Lpoll%=\n\t" \
  "v_mov_b32 v245, 0\n\t" \
  "buffer_load_dword v244, v245, %[rs], %[sab] offen sc1\n\t" \
  "s_waitcnt vmcnt(0)\n\t" \
  "v_readfirstlane_b32 s93, v244\n\t" \
  "s_nop 3\n\t" \
  "s_cmp_eq_u32 s93, 1\n\t" \
  "s_cbranch_scc1 .Lok%=\n\t" \
  "s_cmp_lt_u32 s92, 0x80000\n\t" \
  "s_cbranch_scc1 .Lpoll%=\n\t" \
  "v_mov_b32 v244, 1\n\t" \
  "buffer_store_dword v244, v245, %[rs], %[sab] offen sc1\n\t" \
  "s_branch .Lok%=\n\t" \
  ".Lend%=:\n\t"
#define QK_ASM_5 \
  "s_waitcnt lgkmcnt(0)\n\t" \
  "v_subrev_u32 v244, 0x8000, v250\n\t" \
  "ds_read_b128 v[216:219], v248 offset:53248\n\t" \
  "ds_read_b128 v[220:223], v248 offset:54272\n\t" \
  "ds_read_b128 v[224:227], v248 offset:55296\n\t" \
  "ds_read_b128 v[228:231], v248 offset:56320\n\t" \
  "ds_read_b128 v[208:211], v244 offset:0\n\t" \
  "ds_read_b128 v[212:215], v244 offset:1024\n\t" \
  "ds_read_b128 v[236:239], v244 offset:2048\n\t" \
  "ds_read_b128 v[240:243], v244 offset:3072\n\t" \
  "v_mov_b64 v[192:193], 0\n\t" \
  "v_mov_b64 v[194:195], 0\n\t" \
  "v_mov_b64 v[196:197], 0\n\t" \
  "v_mov_b64 v[198:199], 0\n\t" \
  "s_movk_i32 s92, 0x2000\n\t" \
  ".Lwt%=:\n\t" \
  "ds_read_b64 v[200:201], v248 offset:30720\n\t" \
  "s_memrealtime s[90:91]\n\t" \
  "s_sub_u32 s92, s92, 1\n\t" \
  "s_waitcnt lgkmcnt(0)\n\t" \
  "v_readfirstlane_b32 s93, v200\n\t" \
  "v_readfirstlane_b32 %[tgt], v201\n\t" \
  "s_cbranch_scc1 .Lgo%=\n\t" \
  "s_cmp_eq_u32 s93, %[step]\n\t" \
  "s_cbranch_scc1 .Lwx%=\n\t" \
  "s_sleep 1\n\t" \
  "s_branch .Lwt%=\n\t" \
  ".Lwx%=:\n\t" \
  "s_lshl_b32 s93, s90, 2\n\t" \
  "s_sub_i32 s93, %[tgt], s93\n\t" \
  "s_cmp_le_i32 s93, 0\n\t" \
  "s_cbranch_scc1 .Lgo%=\n\t" \
  "s_min_i32 s93, s93, 8000\n\t" \
  "s_mov_b32 s94, s93\n\t" \
  ".Lsl%=:\n\t" \
  "s_sub_i32 s94, s94, 18\n\t" \
  "s_cmp_lt_i32 s94, 30\n\t" \
  "s_cbranch_scc1 .Lsx%=\n\t" \
  "s_sleep 1\n\t" \
  "s_branch .Lsl%=\n\t" \
  ".Lsx%=:\n\t" \
  "s_sub_i32 s93, s93, 12\n\t" \
  "s_max_i32 s93, s93, 4\n\t" \
  "s_lshr_b32 s93, s93, 2\n\t" \
  "s_add_u32 s93, s93, s90\n\t" \
  "s_memrealtime s[90:91]\n\t" \
  ".Lpace%=:\n\t" \
  "s_memrealtime s[94:95]\n\t" \
  "s_waitcnt lgkmcnt(1)\n\t" \
  "s_sub_i32 s92, s90, s93\n\t" \
  "s_cmp_ge_i32 s92, 0\n\t" \
  "s_cbranch_scc1 .Le%=\n\t" \
  "s_memrealtime s[90:91]\n\t" \
  "s_waitcnt lgkmcnt(1)\n\t" \
  "s_sub_i32 s92, s94, s93\n\t" \
  "s_cmp_lt_i32 s92, 0\n\t" \
  "s_cbranch_scc1 .Lpace%=\n\t" \
  "s_nop 4\n\t" \
  ".Le%=:\n\t" \
  "s_add_u32 s92, s92, s93\n\t" \
  "s_lshl_b32 s92, s92, 2\n\t" \
  "s_sub_i32 s92, %[tgt], s92\n\t" \
  "s_max_i32 s92, s92, 0\n\t" \
  "s_bitcmp0_b32 s92, 3\n\t" \
  "s_cbranch_scc1 .Lf3%=\n\t" \
  "s_nop 15\n\t" \
  ".Lf3%=:\n\t" \
  "s_bitcmp0_b32 s92, 2\n\t" \
  "s_cbranch_scc1 .Lf2%=\n\t" \
  "s_nop 10\n\t" \
  ".Lf2%=:\n\t" \
  ".Lgo%=:\n\t" \
  "s_mov_b32 s92, 0\n\t" \
  ".Lpoll%=:\n\t" \
  "buffer_load_dwordx4 v[200:203], v248, %[rs], %[som] offen sc1\n\t" \
  "buffer_load_dwordx4 v[204:207], v248, %[rs], %[som2] offen sc1\n\t" \
  "s_waitcnt vmcnt(1) lgkmcnt(0)\n\t" \
  "ds_write_b128 v249, v[200:203]\n\t" \
  "v_smfmac_f32_16x16x64_bf16 v[192:195], v[200:203], v[208:215], v251\n\t" \
  "v_max3_u32 v244, v200, v201, v202\n\t" \
  "v_smfmac_f32_16x16x64_bf16 v[196:199], v[200:203], v[236:243], v251\n\t" \
  "s_waitcnt vmcnt(0)\n\t" \
  "ds_write_b128 v249, v[204:207] offset:8192\n\t" \
  "v_smfmac_f32_16x16x64_bf16 v[192:195], v[204:207], v[216:223], v251\n\t" \
  "v_max3_u32 v244, v244, v203, v204\n\t" \
  "v_max3_u32 v244, v244, v205, v206\n\t" \
  "v_smfmac_f32_16x16x64_bf16 v[196:199], v[204:207], v[224:231], v251\n\t" \
  "v_max_u32 v244, v244, v207\n\t" \
  "v_cmp_le_u32 vcc, %[sent], v244\n\t" \
  "s_cbranch_vccnz .Lslow%=\n\t" \
  ".Lok%=:\n\t" \
  "ds_read_b128 v[216:219], v250 offset:32768\n\t" \
  "ds_read_b128 v[220:223], v250 offset:33792\n\t" \
  "ds_read_b128 v[224:227], v250 offset:34816\n\t" \
  "ds_read_b128 v[228:231], v250 offset:35840\n\t" \
  "s_waitcnt lgkmcnt(4)\n\t" \
  "s_barrier\n\t" \
  "ds_read_b128 v[208:211], v248 offset:4096\n\t" \
  "ds_read_b128 v[212:215], v248 offset:7168\n\t" \
  "ds_read_b128 v[236:239], v248 offset:8192\n\t" \
  "ds_read_b128 v[240:243], v248 offset:9216\n\t" \
  "s_waitcnt lgkmcnt(3)\n\t" \
  "ds_read_b128 v[244:247], v248 offset:12288\n\t" \
  "v_smfmac_f32_16x16x64_bf16 v[192:195], v[208:211], v[216:223], v251\n\t" \
  "ds_read_b128 v[216:219], v250 offset:0\n\t" \
  "v_smfmac_f32_16x16x64_bf16 v[196:199], v[208:211], v[224:231], v251\n\t" \
  "s_waitcnt lgkmcnt(4)\n\t" \
  "ds_read_b128 v[220:223], v250 offset:1024\n\t" \
  "v_smfmac_f32_16x16x64_bf16 v[192:195], v[212:215], v[0:7], v251\n\t" \
  "ds_read_b128 v[224:227], v250 offset:2048\n\t" \
  "v_smfmac_f32_16x16x64_bf16 v[196:199], v[212:215], v[8:15], v251\n\t" \
  "s_waitcnt lgkmcnt(5)\n\t" \
  "ds_read_b128 v[228:231], v250 offset:3072\n\t" \
  "v_smfmac_f32_16x16x64_bf16 v[192:195], v[236:239], v[16:23], v251\n\t" \
  "ds_read_b128 v[200:203], v248 offset:10240\n\t" \
  "v_smfmac_f32_16x16x64_bf16 v[196:199], v[236:239], v[24:31], v251\n\t" \
  "s_waitcnt lgkmcnt(6)\n\t" \
  "ds_read_b128 v[204:207], v248 offset:11264\n\t" \
  "v_smfmac_f32_16x16x64_bf16 v[192:195], v[240:243], v[32:39], v251\n\t" \
  "ds_read_b128 v[208:211], v248 offset:14336\n\t" \
  "v_smfmac_f32_16x16x64_bf16 v[196:199], v[240:243], v[40:47], v251\n\t" \
  "s_waitcnt lgkmcnt(5)\n\t" \
  "ds_read_b128 v[212:215], v248 offset:15360\n\t" \
  "v_smfmac_f32_16x16x64_bf16 v[192:195], v[244:247], v[216:223], v251\n\t" \
  "s_waitcnt lgkmcnt(4)\n\t" \
  "v_smfmac_f32_16x16x64_bf16 v[196:199], v[244:247], v[224:231], v251\n\t" \
  "s_waitcnt lgkmcnt(3)\n\t" \
  "ds_read_b128 v[236:239], v248 offset:0\n\t" \
  "v_smfmac_f32_16x16x64_bf16 v[192:195], v[200:203], v[48:55], v251\n\t" \
  "ds_read_b128 v[240:243], v248 offset:1024\n\t" \
  "v_smfmac_f32_16x16x64_bf16 v[196:199], v[200:203], v[56:63], v251\n\t" \
  "s_waitcnt lgkmcnt(4)\n\t" \
  "v_smfmac_f32_16x16x64_bf16 v[192:195], v[204:207], v[64:71], v251\n\t" \
  "ds_read_b128 v[244:247], v248 offset:2048\n\t" \
  "v_smfmac_f32_16x16x64_bf16 v[196:199], v[204:207], v[72:79], v251\n\t" \
  "s_waitcnt lgkmcnt(4)\n\t" \
  "v_smfmac_f32_16x16x64_bf16 v[192:195], v[208:211], v[80:87], v251\n\t" \
  "ds_read_b128 v[200:203], v248 offset:3072\n\t" \
  "v_smfmac_f32_16x16x64_bf16 v[196:199], v[208:211], v[88:95], v251\n\t" \
  "s_waitcnt lgkmcnt(4)\n\t" \
  "v_smfmac_f32_16x16x64_bf16 v[192:195], v[212:215], v[96:103], v251\n\t" \
  "ds_read_b128 v[204:207], v248 offset:6144\n\t" \
  "v_smfmac_f32_16x16x64_bf16 v[196:199], v[212:215], v[104:111], v251\n\t" \
  "s_waitcnt lgkmcnt(3)\n\t" \
  "v_smfmac_f32_16x16x64_bf16 v[192:195], v[236:239], v[112:119], v251\n\t" \
  "v_smfmac_f32_16x16x64_bf16 v[196:199], v[236:239], v[120:127], v251\n\t" \
  "v_smfmac_f32_16x16x64_bf16 v[192:195], v[240:243], v[128:135], v251\n\t" \
  "v_smfmac_f32_16x16x64_bf16 v[196:199], v[240:243], v[136:143], v251\n\t" \
  "s_waitcnt lgkmcnt(1)\n\t" \
  "v_smfmac_f32_16x16x64_bf16 v[192:195], v[244:247], v[144:151], v251\n\t" \
  "v_smfmac_f32_16x16x64_bf16 v[196:199], v[244:247], v[152:159], v251\n\t" \
  "v_smfmac_f32_16x16x64_bf16 v[192:195], v[200:203], v[160:167], v251\n\t" \
  "v_smfmac_f32_16x16x64_bf16 v[196:199], v[200:203], v[168:175], v251\n\t" \
  "s_waitcnt lgkmcnt(0)\n\t" \
  "v_smfmac_f32_16x16x64_bf16 v[192:195], v[204:207], v[176:183], v251\n\t" \
  "v_smfmac_f32_16x16x64_bf16 v[196:199], v[204:207], v[184:191], v251\n\t" \
  "s_nop 6\n\t" \
  "v_add_f32 v192, v192, v194\n\t" \
  "v_add_f32 v193, v193, v195\n\t" \
  "v_add_f32 v196, v196, v198\n\t" \
  "v_add_f32 v197, v197, v199\n\t" \
  "v_cvt_pk_bf16_f32 v192, v192, v193\n\t" \
  "v_cvt_pk_bf16_f32 v196, v196, v197\n\t" \
  "ds_write2_b32 v252, v192, v196 offset1:64\n\t" \
  "s_waitcnt lgkmcnt(0)\n\t" \
  "s_barrier\n\t" \
  "buffer_store_dword v192, v235, %[rs], %[sk] offen sc1\n\t" \
  "buffer_store_dword v196, v235, %[rs], %[sk] offen offset:256 sc1\n\t" \
  "s_mov_b32 %[spins], s92\n\t" \
  "s_branch .Lend%=\n\t" \
  ".Lslow%=:\n\t" \
  "s_mov_b64 s[94:95], 0\n\t" \
  "v_cmp_eq_u32 vcc, %[sent], v200\n\t" \
  "s_or_b64 s[94:95], s[94:95], vcc\n\t" \
  "v_cmp_eq_u32 vcc, %[sent], v201\n\t" \
  "s_or_b64 s[94:95], s[94:95], vcc\n\t" \
  "v_cmp_eq_u32 vcc, %[sent], v202\n\t" \
  "s_or_b64 s[94:95], s[94:95], vcc\n\t" \
  "v_cmp_eq_u32 vcc, %[sent], v203\n\t" \
  "s_or_b64 s[94:95], s[94:95], vcc\n\t" \
  "v_cmp_eq_u32 vcc, %[sent], v204\n\t" \
  "s_or_b64 s[94:95], s[94:95], vcc\n\t" \
  "v_cmp_eq_u32 vcc, %[sent], v205\n\t" \
  "s_or_b64 s[94:95], s[94:95], vcc\n\t" \
  "v_cmp_eq_u32 vcc, %[sent], v206\n\t" \
  "s_or_b64 s[94:95], s[94:95], vcc\n\t" \
  "v_cmp_eq_u32 vcc, %[sent], v207\n\t" \
  "s_or_b64 s[94:95], s[94:95], vcc\n\t" \
  "s_cmp_eq_u64 s[94:95], 0\n\t" \
  "s_cbranch_scc1 .Lok%=\n\t" \
  "v_mov_b64 v[192:193], 0\n\t" \
  "v_mov_b64 v[194:195], 0\n\t" \
  "v_mov_b64 v[196:197], 0\n\t" \
  "v_mov_b64 v[198:199], 0\n\t" \
  "s_add_u32 s92, s92, 1\n\t" \
  "s_cmp_eq_u32 s92, 1\n\t" \
  "s_cbranch_scc1 .Lpoll%=\n\t" \
  "s_sleep 2\n\t" \
  "s_and_b32 s93, s92, 63\n\t" \
  "s_cmp_lg_u32 s93, 0\n\t" \
  "s_cbranch_scc1 .Lpoll%=\n\t" \
  "v_mov_b32 v245, 0\n\t" \
  "buffer_load_dword v244, v245, %[rs], %[sab] offen sc1\n\t" \
  "s_waitcnt vmcnt(0)\n\t" \
  "v_readfirstlane_b32 s93, v244\n\t" \
  "s_nop 3\n\t" \
  "s_cmp_eq_u32 s93, 1\n\t" \
  "s_cbranch_scc1 .Lok%=\n\t" \
  "s_cmp_lt_u32 s92, 0x80000\n\t" \
  "s_cbranch_scc1 .Lpoll%=\n\t" \
  "v_mov_b32 v244, 1\n\t" \
  "buffer_store_dword v244, v245, %[rs], %[sab] offen sc1\n\t" \
  "s_branch .Lok%=\n\t" \
  ".Lend%=:\n\t"
#define QK_ASM_6 \
  "s_waitcnt lgkmcnt(0)\n\t" \
  "v_subrev_u32 v244, 0x8000, v250\n\t" \
  "ds_read_b128 v[216:219], v248 offset:57344\n\t" \
  "ds_read_b128 v[220:223], v248 offset:58368\n\t" \
  "ds_read_b128 v[224:227], v248 offset:59392\n\t" \
  "ds_read_b128 v[228:231], v248 offset:60416\n\t" \
  "ds_read_b128 v[208:211], v244 offset:0\n\t" \
  "ds_read_b128 v[212:215], v244 offset:1024\n\t" \
  "ds_read_b128 v[236:239], v244 offset:2048\n\t" \
  "ds_read_b128 v[240:243], v244 offset:3072\n\t" \
  "v_mov_b64 v[192:193], 0\n\t" \
  "v_mov_b64 v[194:195], 0\n\t" \
  "v_mov_b64 v[196:197], 0\n\t" \
  "v_mov_b64 v[198:199], 0\n\t" \
  "s_movk_i32 s92, 0x2000\n\t" \
  ".Lwt%=:\n\t" \
  "ds_read_b64 v[200:201], v248 offset:30720\n\t" \
  "s_memrealtime s[90:91]\n\t" \
  "s_sub_u32 s92, s92, 1\n\t" \
  "s_waitcnt lgkmcnt(0)\n\t" \
  "v_readfirstlane_b32 s93, v200\n\t" \
  "v_readfirstlane_b32 %[tgt], v201\n\t" \
  "s_cbranch_scc1 .Lgo%=\n\t" \
  "s_cmp_eq_u32 s93, %[step]\n\t" \
  "s_cbranch_scc1 .Lwx%=\n\t" \
  "s_sleep 1\n\t" \
  "s_branch .Lwt%=\n\t" \
  ".Lwx%=:\n\t" \
  "s_lshl_b32 s93, s90, 2\n\t" \
  "s_sub_i32 s93, %[tgt], s93\n\t" \
  "s_cmp_le_i32 s93, 0\n\t" \
  "s_cbranch_scc1 .Lgo%=\n\t" \
  "s_min_i32 s93, s93, 8000\n\t" \
  "s_mov_b32 s94, s93\n\t" \
  ".Lsl%=:\n\t" \
  "s_sub_i32 s94, s94, 18\n\t" \
  "s_cmp_lt_i32 s94, 30\n\t" \
  "s_cbranch_scc1 .Lsx%=\n\t" \
  "s_sleep 1\n\t" \
  "s_branch .Lsl%=\n\t" \
  ".Lsx%=:\n\t" \
  "s_sub_i32 s93, s93, 12\n\t" \
  "s_max_i32 s93, s93, 4\n\t" \
  "s_lshr_b32 s93, s93, 2\n\t" \
  "s_add_u32 s93, s93, s90\n\t" \
  "s_memrealtime s[90:91]\n\t" \
  ".Lpace%=:\n\t" \
  "s_memrealtime s[94:95]\n\t" \
  "s_waitcnt lgkmcnt(1)\n\t" \
  "s_sub_i32 s92, s90, s93\n\t" \
  "s_cmp_ge_i32 s92, 0\n\t" \
  "s_cbranch_scc1 .Le%=\n\t" \
  "s_memrealtime s[90:91]\n\t" \
  "s_waitcnt lgkmcnt(1)\n\t" \
  "s_sub_i32 s92, s94, s93\n\t" \
  "s_cmp_lt_i32 s92, 0\n\t" \
  "s_cbranch_scc1 .Lpace%=\n\t" \
  "s_nop 4\n\t" \
  ".Le%=:\n\t" \
  "s_add_u32 s92, s92, s93\n\t" \
  "s_lshl_b32 s92, s92, 2\n\t" \
  "s_sub_i32 s92, %[tgt], s92\n\t" \
  "s_max_i32 s92, s92, 0\n\t" \
  "s_bitcmp0_b32 s92, 3\n\t" \
  "s_cbranch_scc1 .Lf3%=\n\t" \
  "s_nop 15\n\t" \
  ".Lf3%=:\n\t" \
  "s_bitcmp0_b32 s92, 2\n\t" \
  "s_cbranch_scc1 .Lf2%=\n\t" \
  "s_nop 10\n\t" \
  ".Lf2%=:\n\t" \
  ".Lgo%=:\n\t" \
  "s_mov_b32 s92, 0\n\t" \
  ".Lpoll%=:\n\t" \
  "buffer_load_dwordx4 v[200:203], v248, %[rs], %[som] offen sc1\n\t" \
  "buffer_load_dwordx4 v[204:207], v248, %[rs], %[som2] offen sc1\n\t" \
  "s_waitcnt vmcnt(1) lgkmcnt(0)\n\t" \
  "ds_write_b128 v249, v[200:203]\n\t" \
  "v_smfmac_f32_16x16x64_bf16 v[192:195], v[200:203], v[208:215], v251\n\t" \
  "v_max3_u32 v244, v200, v201, v202\n\t" \
  "v_smfmac_f32_16x16x64_bf16 v[196:199], v[200:203], v[236:243], v251\n\t" \
  "s_waitcnt vmcnt(0)\n\t" \
  "ds_write_b128 v249, v[204:207] offset:8192\n\t" \
  "v_smfmac_f32_16x16x64_bf16 v[192:195], v[204:207], v[216:223], v251\n\t" \
  "v_max3_u32 v244, v244, v203, v204\n\t" \
  "v_max3_u32 v244, v244, v205, v206\n\t" \
  "v_smfmac_f32_16x16x64_bf16 v[196:199], v[204:207], v[224:231], v251\n\t" \
  "v_max_u32 v244, v244, v207\n\t" \
  "v_cmp_le_u32 vcc, %[sent], v244\n\t" \
  "s_cbranch_vccnz .Lslow%=\n\t" \
  ".Lok%=:\n\t" \
  "ds_read_b128 v[216:219], v250 offset:32768\n\t" \
  "ds_read_b128 v[220:223], v250 offset:33792\n\t" \
  "ds_read_b128 v[224:227], v250 offset:34816\n\t" \
  "ds_read_b128 v[228:231], v250 offset:35840\n\t" \
  "s_waitcnt lgkmcnt(4)\n\t" \
  "s_barrier\n\t" \
  "ds_read_b128 v[208:211], v248 offset:7168\n\t" \
  "ds_read_b128 v[212:215], v248 offset:8192\n\t" \
  "ds_read_b128 v[236:239], v248 offset:9216\n\t" \
  "ds_read_b128 v[240:243], v248 offset:10240\n\t" \
  "s_waitcnt lgkmcnt(3)\n\t" \
  "ds_read_b128 v[244:247], v248 offset:13312\n\t" \
  "v_smfmac_f32_16x16x64_bf16 v[192:195], v[208:211], v[216:223], v251\n\t" \
  "ds_read_b128 v[216:219], v250 offset:0\n\t" \
  "v_smfmac_f32_16x16x64_bf16 v[196:199], v[208:211], v[224:231], v251\n\t" \
  "s_waitcnt lgkmcnt(4)\n\t" \
  "ds_read_b128 v[220:223], v250 offset:1024\n\t" \
  "v_smfmac_f32_16x16x64_bf16 v[192:195], v[212:215], v[0:7], v251\n\t" \
  "ds_read_b128 v[224:227], v250 offset:2048\n\t" \
  "v_smfmac_f32_16x16x64_bf16 v[196:199], v[212:215], v[8:15], v251\n\t" \
  "s_waitcnt lgkmcnt(5)\n\t" \
  "ds_read_b128 v[228:231], v250 offset:3072\n\t" \
  "v_smfmac_f32_16x16x64_bf16 v[192:195], v[236:239], v[16:23], v251\n\t" \
  "ds_read_b128 v[200:203], v248 offset:11264\n\t" \
  "v_smfmac_f32_16x16x64_bf16 v[196:199], v[236:239], v[24:31], v251\n\t" \
  "s_waitcnt lgkmcnt(6)\n\t" \
  "ds_read_b128 v[204:207], v248 offset:12288\n\t" \
  "v_smfmac_f32_16x16x64_bf16 v[192:195], v[240:243], v[32:39], v251\n\t" \
  "ds_read_b128 v[208:211], v248 offset:15360\n\t" \
  "v_smfmac_f32_16x16x64_bf16 v[196:199], v[240:243], v[40:47], v251\n\t" \
  "s_waitcnt lgkmcnt(5)\n\t" \
  "ds_read_b128 v[212:215], v248 offset:0\n\t" \
  "v_smfmac_f32_16x16x64_bf16 v[192:195], v[244:247], v[216:223], v251\n\t" \
  "s_waitcnt lgkmcnt(4)\n\t" \
  "v_smfmac_f32_16x16x64_bf16 v[196:199], v[244:247], v[224:231], v251\n\t" \
  "s_waitcnt lgkmcnt(3)\n\t" \
  "ds_read_b128 v[236:239], v248 offset:1024\n\t" \
  "v_smfmac_f32_16x16x64_bf16 v[192:195], v[200:203], v[48:55], v251\n\t" \
  "ds_read_b128 v[240:243], v248 offset:2048\n\t" \
  "v_smfmac_f32_16x16x64_bf16 v[196:199], v[200:203], v[56:63], v251\n\t" \
  "s_waitcnt lgkmcnt(4)\n\t" \
  "v_smfmac_f32_16x16x64_bf16 v[192:195], v[204:207], v[64:71], v251\n\t" \
  "ds_read_b128 v[244:247], v248 offset:3072\n\t" \
  "v_smfmac_f32_16x16x64_bf16 v[196:199], v[204:207], v[72:79], v251\n\t" \
  "s_waitcnt lgkmcnt(4)\n\t" \
  "v_smfmac_f32_16x16x64_bf16 v[192:195], v[208:211], v[80:87], v251\n\t" \
  "ds_read_b128 v[200:203], v248 offset:4096\n\t" \
  "v_smfmac_f32_16x16x64_bf16 v[196:199], v[208:211], v[88:95], v251\n\t" \
  "s_waitcnt lgkmcnt(4)\n\t" \
  "v_smfmac_f32_16x16x64_bf16 v[192:195], v[212:215], v[96:103], v251\n\t" \
  "ds_read_b128 v[204:207], v248 offset:5120\n\t" \
  "v_smfmac_f32_16x16x64_bf16 v[196:199], v[212:215], v[104:111], v251\n\t" \
  "s_waitcnt lgkmcnt(3)\n\t" \
  "v_smfmac_f32_16x16x64_bf16 v[192:195], v[236:239], v[112:119], v251\n\t" \
  "v_smfmac_f32_16x16x64_bf16 v[196:199], v[236:239], v[120:127], v251\n\t" \
  "v_smfmac_f32_16x16x64_bf16 v[192:195], v[240:243], v[128:135], v251\n\t" \
  "v_smfmac_f32_16x16x64_bf16 v[196:199], v[240:243], v[136:143], v251\n\t" \
  "s_waitcnt lgkmcnt(1)\n\t" \
  "v_smfmac_f32_16x16x64_bf16 v[192:195], v[244:247], v[144:151], v251\n\t" \
  "v_smfmac_f32_16x16x64_bf16 v[196:199], v[244:247], v[152:159], v251\n\t" \
  "v_smfmac_f32_16x16x64_bf16 v[192:195], v[200:203], v[160:167], v251\n\t" \
  "v_smfmac_f32_16x16x64_bf16 v[196:199], v[200:203], v[168:175], v251\n\t" \
  "s_waitcnt lgkmcnt(0)\n\t" \
  "v_smfmac_f32_16x16x64_bf16 v[192:195], v[204:207], v[176:183], v251\n\t" \
  "v_smfmac_f32_16x16x64_bf16 v[196:199], v[204:207], v[184:191], v251\n\t" \
  "s_nop 6\n\t" \
  "v_add_f32 v192, v192, v194\n\t" \
  "v_add_f32 v193, v193, v195\n\t" \
  "v_add_f32 v196, v196, v198\n\t" \
  "v_add_f32 v197, v197, v199\n\t" \
  "v_cvt_pk_bf16_f32 v192, v192, v193\n\t" \
  "v_cvt_pk_bf16_f32 v196, v196, v197\n\t" \
  "ds_write2_b32 v252, v192, v196 offset1:64\n\t" \
  "s_waitcnt lgkmcnt(0)\n\t" \
  "s_barrier\n\t" \
  "buffer_store_dword v192, v235, %[rs], %[sk] offen sc1\n\t" \
  "buffer_store_dword v196, v235, %[rs], %[sk] offen offset:256 sc1\n\t" \
  "s_mov_b32 %[spins], s92\n\t" \
  "s_branch .Lend%=\n\t" \
  ".Lslow%=:\n\t" \
  "s_mov_b64 s[94:95], 0\n\t" \
  "v_cmp_eq_u32 vcc, %[sent], v200\n\t" \
  "s_or_b64 s[94:95], s[94:95], vcc\n\t" \
  "v_cmp_eq_u32 vcc, %[sent], v201\n\t" \
  "s_or_b64 s[94:95], s[94:95], vcc\n\t" \
  "v_cmp_eq_u32 vcc, %[sent], v202\n\t" \
  "s_or_b64 s[94:95], s[94:95], vcc\n\t" \
  "v_cmp_eq_u32 vcc, %[sent], v203\n\t" \
  "s_or_b64 s[94:95], s[94:95], vcc\n\t" \
  "v_cmp_eq_u32 vcc, %[sent], v204\n\t" \
  "s_or_b64 s[94:95], s[94:95], vcc\n\t" \
  "v_cmp_eq_u32 vcc, %[sent], v205\n\t" \
  "s_or_b64 s[94:95], s[94:95], vcc\n\t" \
  "v_cmp_eq_u32 vcc, %[sent], v206\n\t" \
  "s_or_b64 s[94:95], s[94:95], vcc\n\t" \
  "v_cmp_eq_u32 vcc, %[sent], v207\n\t" \
  "s_or_b64 s[94:95], s[94:95], vcc\n\t" \
  "s_cmp_eq_u64 s[94:95], 0\n\t" \
  "s_cbranch_scc1 .Lok%=\n\t" \
  "v_mov_b64 v[192:193], 0\n\t" \
  "v_mov_b64 v[194:195], 0\n\t" \
  "v_mov_b64 v[196:197], 0\n\t" \
  "v_mov_b64 v[198:199], 0\n\t" \
  "s_add_u32 s92, s92, 1\n\t" \
  "s_cmp_eq_u32 s92, 1\n\t" \
  "s_cbranch_scc1 .Lpoll%=\n\t" \
  "s_sleep 2\n\t" \
  "s_and_b32 s93, s92, 63\n\t" \
  "s_cmp_lg_u32 s93, 0\n\t" \
  "s_cbranch_scc1 .Lpoll%=\n\t" \
  "v_mov_b32 v245, 0\n\t" \
  "buffer_load_dword v244, v245, %[rs], %[sab] offen sc1\n\t" \
  "s_waitcnt vmcnt(0)\n\t" \
  "v_readfirstlane_b32 s93, v244\n\t" \
  "s_nop 3\n\t" \
  "s_cmp_eq_u32 s93, 1\n\t" \
  "s_cbranch_scc1 .Lok%=\n\t" \
  "s_cmp_lt_u32 s92, 0x80000\n\t" \
  "s_cbranch_scc1 .Lpoll%=\n\t" \
  "v_mov_b32 v244, 1\n\t" \
  "buffer_store_dword v244, v245, %[rs], %[sab] offen sc1\n\t" \
  "s_branch .Lok%=\n\t" \
  ".Lend%=:\n\t"
#define QK_ASM_7 \
  "s_waitcnt lgkmcnt(0)\n\t" \
  "v_subrev_u32 v244, 0x8000, v250\n\t" \
  "ds_read_b128 v[216:219], v248 offset:61440\n\t" \
  "ds_read_b128 v[220:223], v248 offset:62464\n\t" \
  "ds_read_b128 v[224:227], v248 offset:63488\n\t" \
  "ds_read_b128 v[228:231], v248 offset:64512\n\t" \
  "ds_read_b128 v[208:211], v244 offset:0\n\t" \
  "ds_read_b128 v[212:215], v244 offset:1024\n\t" \
  "ds_read_b128 v[236:239], v244 offset:2048\n\t" \
  "ds_read_b128 v[240:243], v244 offset:3072\n\t" \
  "v_mov_b64 v[192:193], 0\n\t" \
  "v_mov_b64 v[194:195], 0\n\t" \
  "v_mov_b64 v[196:197], 0\n\t" \
  "v_mov_b64 v[198:199], 0\n\t" \
  "s_movk_i32 s92, 0x2000\n\t" \
  ".Lwt%=:\n\t" \
  "ds_read_b64 v[200:201], v248 offset:30720\n\t" \
  "s_memrealtime s[90:91]\n\t" \
  "s_sub_u32 s92, s92, 1\n\t" \
  "s_waitcnt lgkmcnt(0)\n\t" \
  "v_readfirstlane_b32 s93, v200\n\t" \
  "v_readfirstlane_b32 %[tgt], v201\n\t" \
  "s_cbranch_scc1 .Lgo%=\n\t" \
  "s_cmp_eq_u32 s93, %[step]\n\t" \
  "s_cbranch_scc1 .Lwx%=\n\t" \
  "s_sleep 1\n\t" \
  "s_branch .Lwt%=\n\t" \
  ".Lwx%=:\n\t" \
  "s_lshl_b32 s93, s90, 2\n\t" \
  "s_sub_i32 s93, %[tgt], s93\n\t" \
  "s_cmp_le_i32 s93, 0\n\t" \
  "s_cbranch_scc1 .Lgo%=\n\t" \
  "s_min_i32 s93, s93, 8000\n\t" \
  "s_mov_b32 s94, s93\n\t" \
  ".Lsl%=:\n\t" \
  "s_sub_i32 s94, s94, 18\n\t" \
  "s_cmp_lt_i32 s94, 30\n\t" \
  "s_cbranch_scc1 .Lsx%=\n\t" \
  "s_sleep 1\n\t" \
  "s_branch .Lsl%=\n\t" \
  ".Lsx%=:\n\t" \
  "s_sub_i32 s93, s93, 12\n\t" \
  "s_max_i32 s93, s93, 4\n\t" \
  "s_lshr_b32 s93, s93, 2\n\t" \
  "s_add_u32 s93, s93, s90\n\t" \
  "s_memrealtime s[90:91]\n\t" \
  ".Lpace%=:\n\t" \
  "s_memrealtime s[94:95]\n\t" \
  "s_waitcnt lgkmcnt(1)\n\t" \
  "s_sub_i32 s92, s90, s93\n\t" \
  "s_cmp_ge_i32 s92, 0\n\t" \
  "s_cbranch_scc1 .Le%=\n\t" \
  "s_memrealtime s[90:91]\n\t" \
  "s_waitcnt lgkmcnt(1)\n\t" \
  "s_sub_i32 s92, s94, s93\n\t" \
  "s_cmp_lt_i32 s92, 0\n\t" \
  "s_cbranch_scc1 .Lpace%=\n\t" \
  "s_nop 4\n\t" \
  ".Le%=:\n\t" \
  "s_add_u32 s92, s92, s93\n\t" \
  "s_lshl_b32 s92, s92, 2\n\t" \
  "s_sub_i32 s92, %[tgt], s92\n\t" \
  "s_max_i32 s92, s92, 0\n\t" \
  "s_bitcmp0_b32 s92, 3\n\t" \
  "s_cbranch_scc1 .Lf3%=\n\t" \
  "s_nop 15\n\t" \
  ".Lf3%=:\n\t" \
  "s_bitcmp0_b32 s92, 2\n\t" \
  "s_cbranch_scc1 .Lf2%=\n\t" \
  "s_nop 10\n\t" \
  ".Lf2%=:\n\t" \
  ".Lgo%=:\n\t" \
  "s_mov_b32 s92, 0\n\t" \
  "s_memtime s[88:89]\n\t" \
  ".Lpoll%=:\n\t" \
  "buffer_load_dwordx4 v[200:203], v248, %[rs], %[som] offen sc1\n\t" \
  "buffer_load_dwordx4 v[204:207], v248, %[rs], %[som2] offen sc1\n\t" \
  "s_waitcnt vmcnt(1) lgkmcnt(0)\n\t" \
  "ds_write_b128 v249, v[200:203]\n\t" \
  "v_smfmac_f32_16x16x64_bf16 v[192:195], v[200:203], v[208:215], v251\n\t" \
  "v_max3_u32 v244, v200, v201, v202\n\t" \
  "v_smfmac_f32_16x16x64_bf16 v[196:199], v[200:203], v[236:243], v251\n\t" \
  "s_waitcnt vmcnt(0)\n\t" \
  "ds_write_b128 v249, v[204:207] offset:8192\n\t" \
  "v_smfmac_f32_16x16x64_bf16 v[192:195], v[204:207], v[216:223], v251\n\t" \
  "v_max3_u32 v244, v244, v203, v204\n\t" \
  "v_max3_u32 v244, v244, v205, v206\n\t" \
  "v_smfmac_f32_16x16x64_bf16 v[196:199], v[204:207], v[224:231], v251\n\t" \
  "v_max_u32 v244, v244, v207\n\t" \
  "v_cmp_le_u32 vcc, %[sent], v244\n\t" \
  "s_cbranch_vccnz .Lslow%=\n\t" \
  ".Lok%=:\n\t" \
  "ds_read_b128 v[216:219], v250 offset:32768\n\t" \
  "ds_read_b128 v[220:223], v250 offset:33792\n\t" \
  "ds_read_b128 v[224:227], v250 offset:34816\n\t" \
  "ds_read_b128 v[228:231], v250 offset:35840\n\t" \
  "s_waitcnt lgkmcnt(4)\n\t" \
  "s_setprio 3\n\t" \
  "s_barrier\n\t" \
  "s_memrealtime s[90:91]\n\t" \
  "s_memtime s[94:95]\n\t" \
  "v_mov_b32 v232, %[step]\n\t" \
  "v_lshrrev_b32 v234, 1, v248\n\t" \
  "s_mov_b64 exec, 0xffff\n\t" \
  "s_waitcnt lgkmcnt(0)\n\t" \
  "s_lshl_b32 s90, s90, 2\n\t" \
  "s_sub_u32 s94, s94, s88\n\t" \
  "s_mul_i32 s94, s94, 12200\n\t" \
  "s_lshr_b32 s94, s94, 16\n\t" \
  "s_add_u32 s94, s94, %[tgt]\n\t" \
  "s_sub_i32 s93, s90, s94\n\t" \
  "s_sub_i32 s93, s93, 30\n\t" \
  "s_max_i32 s93, s93, 0\n\t" \
  "s_add_u32 s90, s94, s93\n\t" \
  "s_sub_i32 s93, s90, %[tgt]\n\t" \
  "s_cmp_gt_i32 s93, 270\n\t" \
  "s_cselect_b32 s93, 70, 0\n\t" \
  "s_add_u32 s93, s93, s90\n\t" \
  "s_add_u32 s93, s93, %[lead]\n\t" \
  "v_mov_b32 v233, s93\n\t" \
  "buffer_store_dwordx2 v[232:233], v234, %[rs], %[sbc] offen sc1\n\t" \
  "s_mov_b64 exec, -1\n\t" \
  "s_mov_b32 %[tb1], s90\n\t" \
  "s_setprio 0\n\t" \
  "ds_read_b128 v[208:211], v248 offset:6144\n\t" \
  "ds_read_b128 v[212:215], v248 offset:9216\n\t" \
  "ds_read_b128 v[236:239], v248 offset:10240\n\t" \
  "ds_read_b128 v[240:243], v248 offset:11264\n\t" \
  "s_waitcnt lgkmcnt(3)\n\t" \
  "ds_read_b128 v[244:247], v248 offset:12288\n\t" \
  "v_smfmac_f32_16x16x64_bf16 v[192:195], v[208:211], v[216:223], v251\n\t" \
  "ds_read_b128 v[216:219], v250 offset:0\n\t" \
  "v_smfmac_f32_16x16x64_bf16 v[196:199], v[208:211], v[224:231], v251\n\t" \
  "s_waitcnt lgkmcnt(4)\n\t" \
  "ds_read_b128 v[220:223], v250 offset:1024\n\t" \
  "v_smfmac_f32_16x16x64_bf16 v[192:195], v[212:215], v[0:7], v251\n\t" \
  "ds_read_b128 v[224:227], v250 offset:2048\n\t" \
  "v_smfmac_f32_16x16x64_bf16 v[196:199], v[212:215], v[8:15], v251\n\t" \
  "s_waitcnt lgkmcnt(5)\n\t" \
  "ds_read_b128 v[228:231], v250 offset:3072\n\t" \
  "v_smfmac_f32_16x16x64_bf16 v[192:195], v[236:239], v[16:23], v251\n\t" \
  "ds_read_b128 v[200:203], v248 offset:13312\n\t" \
  "v_smfmac_f32_16x16x64_bf16 v[196:199], v[236:239], v[24:31], v251\n\t" \
  "s_waitcnt lgkmcnt(6)\n\t" \
  "ds_read_b128 v[204:207], v248 offset:14336\n\t" \
  "v_smfmac_f32_16x16x64_bf16 v[192:195], v[240:243], v[32:39], v251\n\t" \
  "ds_read_b128 v[208:211], v248 offset:0\n\t" \
  "v_smfmac_f32_16x16x64_bf16 v[196:199], v[240:243], v[40:47], v251\n\t" \
  "s_waitcnt lgkmcnt(5)\n\t" \
  "ds_read_b128 v[212:215], v248 offset:1024\n\t" \
  "v_smfmac_f32_16x16x64_bf16 v[192:195], v[244:247], v[216:223], v251\n\t" \
  "s_waitcnt lgkmcnt(4)\n\t" \
  "v_smfmac_f32_16x16x64_bf16 v[196:199], v[244:247], v[224:231], v251\n\t" \
  "s_waitcnt lgkmcnt(3)\n\t" \
  "ds_read_b128 v[236:239], v248 offset:2048\n\t" \
  "v_smfmac_f32_16x16x64_bf16 v[192:195], v[200:203], v[48:55], v251\n\t" \
  "ds_read_b128 v[240:243], v248 offset:3072\n\t" \
  "v_smfmac_f32_16x16x64_bf16 v[196:199], v[200:203], v[56:63], v251\n\t" \
  "s_waitcnt lgkmcnt(4)\n\t" \
  "v_smfmac_f32_16x16x64_bf16 v[192:195], v[204:207], v[64:71], v251\n\t" \
  "ds_read_b128 v[244:247], v248 offset:4096\n\t" \
  "v_smfmac_f32_16x16x64_bf16 v[196:199], v[204:207], v[72:79], v251\n\t" \
  "s_waitcnt lgkmcnt(4)\n\t" \
  "v_smfmac_f32_16x16x64_bf16 v[192:195], v[208:211], v[80:87], v251\n\t" \
  "ds_read_b128 v[200:203], v248 offset:5120\n\t" \
  "v_smfmac_f32_16x16x64_bf16 v[196:199], v[208:211], v[88:95], v251\n\t" \
  "s_waitcnt lgkmcnt(4)\n\t" \
  "v_smfmac_f32_16x16x64_bf16 v[192:195], v[212:215], v[96:103], v251\n\t" \
  "ds_read_b128 v[204:207], v248 offset:8192\n\t" \
  "v_smfmac_f32_16x16x64_bf16 v[196:199], v[212:215], v[104:111], v251\n\t" \
  "s_waitcnt lgkmcnt(3)\n\t" \
  "v_smfmac_f32_16x16x64_bf16 v[192:195], v[236:239], v[112:119], v251\n\t" \
  "v_smfmac_f32_16x16x64_bf16 v[196:199], v[236:239], v[120:127], v251\n\t" \
  "v_smfmac_f32_16x16x64_bf16 v[192:195], v[240:243], v[128:135], v251\n\t" \
  "v_smfmac_f32_16x16x64_bf16 v[196:199], v[240:243], v[136:143], v251\n\t" \
  "s_waitcnt lgkmcnt(1)\n\t" \
  "v_smfmac_f32_16x16x64_bf16 v[192:195], v[244:247], v[144:151], v251\n\t" \
  "v_smfmac_f32_16x16x64_bf16 v[196:199], v[244:247], v[152:159], v251\n\t" \
  "v_smfmac_f32_16x16x64_bf16 v[192:195], v[200:203], v[160:167], v251\n\t" \
  "v_smfmac_f32_16x16x64_bf16 v[196:199], v[200:203], v[168:175], v251\n\t" \
  "s_waitcnt lgkmcnt(0)\n\t" \
  "v_smfmac_f32_16x16x64_bf16 v[192:195], v[204:207], v[176:183], v251\n\t" \
  "v_smfmac_f32_16x16x64_bf16 v[196:199], v[204:207], v[184:191], v251\n\t" \
  "s_nop 6\n\t" \
  "v_add_f32 v192, v192, v194\n\t" \
  "v_add_f32 v193, v193, v195\n\t" \
  "v_add_f32 v196, v196, v198\n\t" \
  "v_add_f32 v197, v197, v199\n\t" \
  "v_cvt_pk_bf16_f32 v192, v192, v193\n\t" \
  "v_cvt_pk_bf16_f32 v196, v196, v197\n\t" \
  "ds_write2_b32 v252, v192, v196 offset1:64\n\t" \
  "s_waitcnt lgkmcnt(0)\n\t" \
  "s_barrier\n\t" \
  "buffer_store_dword v192, v235, %[rs], %[sk] offen sc1\n\t" \
  "buffer_store_dword v196, v235, %[rs], %[sk] offen offset:256 sc1\n\t" \
  "s_mov_b32 %[spins], s92\n\t" \
  "s_branch .Lend%=\n\t" \
  ".Lslow%=:\n\t" \
  "s_mov_b64 s[94:95], 0\n\t" \
  "v_cmp_eq_u32 vcc, %[sent], v200\n\t" \
  "s_or_b64 s[94:95], s[94:95], vcc\n\t" \
  "v_cmp_eq_u32 vcc, %[sent], v201\n\t" \
  "s_or_b64 s[94:95], s[94:95], vcc\n\t" \
  "v_cmp_eq_u32 vcc, %[sent], v202\n\t" \
  "s_or_b64 s[94:95], s[94:95], vcc\n\t" \
  "v_cmp_eq_u32 vcc, %[sent], v203\n\t" \
  "s_or_b64 s[94:95], s[94:95], vcc\n\t" \
  "v_cmp_eq_u32 vcc, %[sent], v204\n\t" \
  "s_or_b64 s[94:95], s[94:95], vcc\n\t" \
  "v_cmp_eq_u32 vcc, %[sent], v205\n\t" \
  "s_or_b64 s[94:95], s[94:95], vcc\n\t" \
  "v_cmp_eq_u32 vcc, %[sent], v206\n\t" \
  "s_or_b64 s[94:95], s[94:95], vcc\n\t" \
  "v_cmp_eq_u32 vcc, %[sent], v207\n\t" \
  "s_or_b64 s[94:95], s[94:95], vcc\n\t" \
  "s_cmp_eq_u64 s[94:95], 0\n\t" \
  "s_cbranch_scc1 .Lok%=\n\t" \
  "v_mov_b64 v[192:193], 0\n\t" \
  "v_mov_b64 v[194:195], 0\n\t" \
  "v_mov_b64 v[196:197], 0\n\t" \
  "v_mov_b64 v[198:199], 0\n\t" \
  "s_add_u32 s92, s92, 1\n\t" \
  "s_cmp_eq_u32 s92, 1\n\t" \
  "s_cbranch_scc1 .Lpoll%=\n\t" \
  "s_sleep 2\n\t" \
  "s_and_b32 s93, s92, 63\n\t" \
  "s_cmp_lg_u32 s93, 0\n\t" \
  "s_cbranch_scc1 .Lpoll%=\n\t" \
  "v_mov_b32 v245, 0\n\t" \
  "buffer_load_dword v244, v245, %[rs], %[sab] offen sc1\n\t" \
  "s_waitcnt vmcnt(0)\n\t" \
  "v_readfirstlane_b32 s93, v244\n\t" \
  "s_nop 3\n\t" \
  "s_cmp_eq_u32 s93, 1\n\t" \
  "s_cbranch_scc1 .Lok%=\n\t" \
  "s_cmp_lt_u32 s92, 0x80000\n\t" \
  "s_cbranch_scc1 .Lpoll%=\n\t" \
  "v_mov_b32 v244, 1\n\t" \
  "buffer_store_dword v244, v245, %[rs], %[sab] offen sc1\n\t" \
  "s_branch .Lok%=\n\t" \
  ".Lend%=:\n\t"
#define VV_ASM \
  "s_waitcnt lgkmcnt(0)\n\t" \
  "v_mov_b64 v[128:129], 0\n\t" \
  "v_mov_b64 v[130:131], 0\n\t" \
  "s_movk_i32 s92, 0x2000\n\t" \
  ".Lwt%=:\n\t" \
  "ds_read_b64 v[132:133], v200 offset:30720\n\t" \
  "s_memrealtime s[90:91]\n\t" \
  "s_sub_u32 s92, s92, 1\n\t" \
  "s_waitcnt lgkmcnt(0)\n\t" \
  "v_readfirstlane_b32 s93, v132\n\t" \
  "v_readfirstlane_b32 %[tgt], v133\n\t" \
  "s_cbranch_scc1 .Lgo%=\n\t" \
  "s_cmp_eq_u32 s93, %[step]\n\t" \
  "s_cbranch_scc1 .Lwx%=\n\t" \
  "s_sleep 1\n\t" \
  "s_branch .Lwt%=\n\t" \
  ".Lwx%=:\n\t" \
  "s_lshl_b32 s93, s90, 2\n\t" \
  "s_sub_i32 s93, %[tgt], s93\n\t" \
  "s_cmp_le_i32 s93, 0\n\t" \
  "s_cbranch_scc1 .Lgo%=\n\t" \
  "s_min_i32 s93, s93, 8000\n\t" \
  "s_mov_b32 s94, s93\n\t" \
  ".Lsl%=:\n\t" \
  "s_sub_i32 s94, s94, 18\n\t" \
  "s_cmp_lt_i32 s94, 30\n\t" \
  "s_cbranch_scc1 .Lsx%=\n\t" \
  "s_sleep 1\n\t" \
  "s_branch .Lsl%=\n\t" \
  ".Lsx%=:\n\t" \
  "s_sub_i32 s93, s93, 12\n\t" \
  "s_max_i32 s93, s93, 4\n\t" \
  "s_lshr_b32 s93, s93, 2\n\t" \
  "s_add_u32 s93, s93, s90\n\t" \
  "s_memrealtime s[90:91]\n\t" \
  ".Lpace%=:\n\t" \
  "s_memrealtime s[94:95]\n\t" \
  "s_waitcnt lgkmcnt(1)\n\t" \
  "s_sub_i32 s92, s90, s93\n\t" \
  "s_cmp_ge_i32 s92, 0\n\t" \
  "s_cbranch_scc1 .Le%=\n\t" \
  "s_memrealtime s[90:91]\n\t" \
  "s_waitcnt lgkmcnt(1)\n\t" \
  "s_sub_i32 s92, s94, s93\n\t" \
  "s_cmp_lt_i32 s92, 0\n\t" \
  "s_cbranch_scc1 .Lpace%=\n\t" \
  "s_nop 4\n\t" \
  ".Le%=:\n\t" \
  "s_add_u32 s92, s92, s93\n\t" \
  "s_lshl_b32 s92, s92, 2\n\t" \
  "s_sub_i32 s92, %[tgt], s92\n\t" \
  "s_max_i32 s92, s92, 0\n\t" \
  "s_bitcmp0_b32 s92, 3\n\t" \
  "s_cbranch_scc1 .Lf3%=\n\t" \
  "s_nop 15\n\t" \
  ".Lf3%=:\n\t" \
  "s_bitcmp0_b32 s92, 2\n\t" \
  "s_cbranch_scc1 .Lf2%=\n\t" \
  "s_nop 10\n\t" \
  ".Lf2%=:\n\t" \
  ".Lgo%=:\n\t" \
  "s_mov_b32 s92, 0\n\t" \
  ".Lpoll%=:\n\t" \
  "buffer_load_dwordx4 v[132:135], v200, %[rs], %[som] offen sc1\n\t" \
  "buffer_load_dwordx4 v[136:139], v200, %[rs], %[som2] offen sc1\n\t" \
  "s_waitcnt vmcnt(1) lgkmcnt(0)\n\t" \
  "ds_write_b128 v232, v[132:135]\n\t" \
  "v_smfmac_f32_16x16x64_bf16 v[128:131], v[132:135], v[0:7], v202\n\t" \
  "v_max3_u32 v196, v132, v133, v134\n\t" \
  "s_waitcnt vmcnt(0)\n\t" \
  "ds_write_b128 v232, v[136:139] offset:8192\n\t" \
  "v_smfmac_f32_16x16x64_bf16 v[128:131], v[136:139], v[64:71], v202\n\t" \
  "v_max3_u32 v196, v196, v135, v136\n\t" \
  "v_max3_u32 v196, v196, v137, v138\n\t" \
  "v_max_u32 v196, v196, v139\n\t" \
  "v_cmp_le_u32 vcc, %[sent], v196\n\t" \
  "s_cbranch_vccnz .Lslow%=\n\t" \
  ".Lok%=:\n\t" \
  "s_waitcnt lgkmcnt(0)\n\t" \
  "s_barrier\n\t" \
  "ds_read_b128 v[164:167], v233 offset:0\n\t" \
  "ds_read_b128 v[140:143], v234 offset:0\n\t" \
  "ds_read_b128 v[144:147], v235 offset:0\n\t" \
  "ds_read_b128 v[148:151], v236 offset:0\n\t" \
  "ds_read_b128 v[152:155], v237 offset:0\n\t" \
  "s_waitcnt lgkmcnt(4)\n\t" \
  "ds_read_b128 v[156:159], v238 offset:0\n\t" \
  "v_smfmac_f32_16x16x64_bf16 v[128:131], v[164:167], v[8:15], v202\n\t" \
  "s_waitcnt lgkmcnt(4)\n\t" \
  "ds_read_b128 v[160:163], v239 offset:0\n\t" \
  "v_smfmac_f32_16x16x64_bf16 v[128:131], v[140:143], v[16:23], v202\n\t" \
  "s_waitcnt lgkmcnt(4)\n\t" \
  "ds_read_b128 v[168:171], v233 offset:8192\n\t" \
  "v_smfmac_f32_16x16x64_bf16 v[128:131], v[144:147], v[24:31], v202\n\t" \
  "s_waitcnt lgkmcnt(4)\n\t" \
  "ds_read_b128 v[172:175], v234 offset:8192\n\t" \
  "v_smfmac_f32_16x16x64_bf16 v[128:131], v[148:151], v[32:39], v202\n\t" \
  "s_waitcnt lgkmcnt(4)\n\t" \
  "ds_read_b128 v[176:179], v235 offset:8192\n\t" \
  "v_smfmac_f32_16x16x64_bf16 v[128:131], v[152:155], v[40:47], v202\n\t" \
  "s_waitcnt lgkmcnt(4)\n\t" \
  "ds_read_b128 v[180:183], v236 offset:8192\n\t" \
  "v_smfmac_f32_16x16x64_bf16 v[128:131], v[156:159], v[48:55], v202\n\t" \
  "s_waitcnt lgkmcnt(4)\n\t" \
  "ds_read_b128 v[184:187], v237 offset:8192\n\t" \
  "v_smfmac_f32_16x16x64_bf16 v[128:131], v[160:163], v[56:63], v202\n\t" \
  "s_waitcnt lgkmcnt(4)\n\t" \
  "ds_read_b128 v[188:191], v238 offset:8192\n\t" \
  "v_smfmac_f32_16x16x64_bf16 v[128:131], v[168:171], v[72:79], v202\n\t" \
  "s_waitcnt lgkmcnt(4)\n\t" \
  "ds_read_b128 v[192:195], v239 offset:8192\n\t" \
  "v_smfmac_f32_16x16x64_bf16 v[128:131], v[172:175], v[80:87], v202\n\t" \
  "s_waitcnt lgkmcnt(4)\n\t" \
  "v_smfmac_f32_16x16x64_bf16 v[128:131], v[176:179], v[88:95], v202\n\t" \
  "s_waitcnt lgkmcnt(3)\n\t" \
  "v_smfmac_f32_16x16x64_bf16 v[128:131], v[180:183], v[96:103], v202\n\t" \
  "s_waitcnt lgkmcnt(2)\n\t" \
  "v_smfmac_f32_16x16x64_bf16 v[128:131], v[184:187], v[104:111], v202\n\t" \
  "s_waitcnt lgkmcnt(1)\n\t" \
  "v_smfmac_f32_16x16x64_bf16 v[128:131], v[188:191], v[112:119], v202\n\t" \
  "s_waitcnt lgkmcnt(0)\n\t" \
  "v_smfmac_f32_16x16x64_bf16 v[128:131], v[192:195], v[120:127], v202\n\t" \
  "s_nop 7\n\t" \
  "v_add_f32 v128, v128, v130\n\t" \
  "v_add_f32 v129, v129, v131\n\t" \
  "v_cvt_pk_bf16_f32 v128, v128, v129\n\t" \
  "buffer_store_dword v128, v203, %[rs], %[sv] offen sc1\n\t" \
  "ds_write_b32 v204, v128\n\t" \
  "s_waitcnt lgkmcnt(0)\n\t" \
  "s_barrier\n\t" \
  "s_mov_b32 %[spins], s92\n\t" \
  "s_branch .Lend%=\n\t" \
  ".Lslow%=:\n\t" \
  "s_mov_b64 s[94:95], 0\n\t" \
  "v_cmp_eq_u32 vcc, %[sent], v132\n\t" \
  "s_or_b64 s[94:95], s[94:95], vcc\n\t" \
  "v_cmp_eq_u32 vcc, %[sent], v133\n\t" \
  "s_or_b64 s[94:95], s[94:95], vcc\n\t" \
  "v_cmp_eq_u32 vcc, %[sent], v134\n\t" \
  "s_or_b64 s[94:95], s[94:95], vcc\n\t" \
  "v_cmp_eq_u32 vcc, %[sent], v135\n\t" \
  "s_or_b64 s[94:95], s[94:95], vcc\n\t" \
  "v_cmp_eq_u32 vcc, %[sent], v136\n\t" \
  "s_or_b64 s[94:95], s[94:95], vcc\n\t" \
  "v_cmp_eq_u32 vcc, %[sent], v137\n\t" \
  "s_or_b64 s[94:95], s[94:95], vcc\n\t" \
  "v_cmp_eq_u32 vcc, %[sent], v138\n\t" \
  "s_or_b64 s[94:95], s[94:95], vcc\n\t" \
  "v_cmp_eq_u32 vcc, %[sent], v139\n\t" \
  "s_or_b64 s[94:95], s[94:95], vcc\n\t" \
  "s_cmp_eq_u64 s[94:95], 0\n\t" \
  "s_cbranch_scc1 .Lok%=\n\t" \
  "v_mov_b64 v[128:129], 0\n\t" \
  "v_mov_b64 v[130:131], 0\n\t" \
  "s_add_u32 s92, s92, 1\n\t" \
  "s_cmp_eq_u32 s92, 1\n\t" \
  "s_cbranch_scc1 .Lpoll%=\n\t" \
  "s_sleep 2\n\t" \
  "s_and_b32 s93, s92, 63\n\t" \
  "s_cmp_lg_u32 s93, 0\n\t" \
  "s_cbranch_scc1 .Lpoll%=\n\t" \
  "v_mov_b32 v197, 0\n\t" \
  "buffer_load_dword v196, v197, %[rs], %[sab] offen sc1\n\t" \
  "s_waitcnt vmcnt(0)\n\t" \
  "v_readfirstlane_b32 s93, v196\n\t" \
  "s_nop 3\n\t" \
  "s_cmp_eq_u32 s93, 1\n\t" \
  "s_cbranch_scc1 .Lok%=\n\t" \
  "s_cmp_lt_u32 s92, 0x80000\n\t" \
  "s_cbranch_scc1 .Lpoll%=\n\t" \
  "v_mov_b32 v196, 1\n\t" \
  "buffer_store_dword v196, v197, %[rs], %[sab] offen sc1\n\t" \
  "s_branch .Lok%=\n\t" \
  ".Lend%=:\n\t"
#define TK_ASM \
  "buffer_load_dwordx4 v[224:227], v200, %[rs], %[sk] offen offset:0 sc1\n\t" \
  "buffer_load_dwordx4 v[228:231], v200, %[rs], %[sk] offen offset:1024 sc1\n\t" \
  "s_memrealtime s[88:89]\n\t" \
  "v_add_u32 v205, %[tl], v200\n\t" \
  "v_add_u32 v206, %[tq], v205\n\t" \
  "v_add_u32 v207, %[tv], v205\n\t" \
  "s_waitcnt lgkmcnt(0)\n\t" \
  "ds_read_b128 v[128:131], v206 offset:8192\n\t" \
  "ds_read_b128 v[144:147], v205 offset:0\n\t" \
  "ds_read_b128 v[160:163], v205 offset:4096\n\t" \
  "ds_read_b128 v[132:135], v206 offset:9216\n\t" \
  "ds_read_b128 v[148:151], v205 offset:1024\n\t" \
  "ds_read_b128 v[164:167], v205 offset:5120\n\t" \
  "ds_read_b128 v[136:139], v206 offset:10240\n\t" \
  "ds_read_b128 v[152:155], v205 offset:2048\n\t" \
  "ds_read_b128 v[168:171], v205 offset:6144\n\t" \
  "ds_read_b128 v[140:143], v206 offset:11264\n\t" \
  "ds_read_b128 v[156:159], v205 offset:3072\n\t" \
  "ds_read_b128 v[172:175], v205 offset:7168\n\t" \
  "v_add_u32 v206, %[vp], v218\n\t" \
  "s_lshl_b32 s88, s88, 2\n\t" \
  "s_add_u32 s89, s88, %[od]\n\t" \
  "s_waitcnt lgkmcnt(10)\n\t" \
  "v_mfma_f32_16x16x32_bf16 v[196:199], v[144:147], v[128:131], 0\n\t" \
  "s_waitcnt lgkmcnt(9)\n\t" \
  "v_mfma_f32_16x16x32_bf16 v[144:147], v[160:163], v[128:131], 0\n\t" \
  "s_waitcnt lgkmcnt(7)\n\t" \
  "v_mfma_f32_16x16x32_bf16 v[196:199], v[148:151], v[132:135], v[196:199]\n\t" \
  "s_waitcnt lgkmcnt(6)\n\t" \
  "v_mfma_f32_16x16x32_bf16 v[144:147], v[164:167], v[132:135], v[144:147]\n\t" \
  "s_waitcnt lgkmcnt(4)\n\t" \
  "v_mfma_f32_16x16x32_bf16 v[196:199], v[152:155], v[136:139], v[196:199]\n\t" \
  "s_waitcnt lgkmcnt(3)\n\t" \
  "v_mfma_f32_16x16x32_bf16 v[144:147], v[168:171], v[136:139], v[144:147]\n\t" \
  "s_waitcnt lgkmcnt(1)\n\t" \
  "v_mfma_f32_16x16x32_bf16 v[196:199], v[156:159], v[140:143], v[196:199]\n\t" \
  "s_waitcnt lgkmcnt(0)\n\t" \
  "v_mfma_f32_16x16x32_bf16 v[144:147], v[172:175], v[140:143], v[144:147]\n\t" \
  "ds_read_b128 v[176:179], v207 offset:16384\n\t" \
  "ds_read_b128 v[180:183], v207 offset:17408\n\t" \
  "ds_read_b128 v[184:187], v207 offset:18432\n\t" \
  "ds_read_b128 v[188:191], v207 offset:19456\n\t" \
  "ds_read2st64_b32 v[192:193], v206 offset0:0 offset1:1\n\t" \
  "ds_read2st64_b32 v[194:195], v206 offset0:2 offset1:3\n\t" \
  "v_cvt_pk_bf16_f32 v197, v196, v197\n\t" \
  "v_cvt_pk_bf16_f32 v199, v198, v199\n\t" \
  "v_cvt_pk_bf16_f32 v145, v144, v145\n\t" \
  "v_cvt_pk_bf16_f32 v147, v146, v147\n\t" \
  "v_lshlrev_b32 v196, 16, v197\n\t" \
  "v_lshlrev_b32 v198, 16, v199\n\t" \
  "v_lshlrev_b32 v144, 16, v145\n\t" \
  "v_lshlrev_b32 v146, 16, v147\n\t" \
  "v_and_b32 v197, 0xffff0000, v197\n\t" \
  "v_and_b32 v199, 0xffff0000, v199\n\t" \
  "v_and_b32 v145, 0xffff0000, v145\n\t" \
  "v_and_b32 v147, 0xffff0000, v147\n\t" \
  "v_pk_mul_f32 v[196:197], v[196:197], %[sc] op_sel_hi:[1,0]\n\t" \
  "v_pk_mul_f32 v[198:199], v[198:199], %[sc] op_sel_hi:[1,0]\n\t" \
  "v_pk_mul_f32 v[144:145], v[144:145], %[sc] op_sel_hi:[1,0]\n\t" \
  "v_pk_mul_f32 v[146:147], v[146:147], %[sc] op_sel_hi:[1,0]\n\t" \
  "v_cvt_pk_bf16_f32 v197, v196, v197\n\t" \
  "v_cvt_pk_bf16_f32 v199, v198, v199\n\t" \
  "v_cvt_pk_bf16_f32 v145, v144, v145\n\t" \
  "v_cvt_pk_bf16_f32 v147, v146, v147\n\t" \
  "v_lshlrev_b32 v196, 16, v197\n\t" \
  "v_lshlrev_b32 v198, 16, v199\n\t" \
  "v_lshlrev_b32 v144, 16, v145\n\t" \
  "v_lshlrev_b32 v146, 16, v147\n\t" \
  "v_and_b32 v197, 0xffff0000, v197\n\t" \
  "v_and_b32 v199, 0xffff0000, v199\n\t" \
  "v_and_b32 v145, 0xffff0000, v145\n\t" \
  "v_and_b32 v147, 0xffff0000, v147\n\t" \
  "v_min_f32 v196, v196, v208\n\t" \
  "v_min_f32 v197, v197, v209\n\t" \
  "v_min_f32 v198, v198, v210\n\t" \
  "v_min_f32 v199, v199, v211\n\t" \
  "v_min_f32 v144, v144, v212\n\t" \
  "v_min_f32 v145, v145, v213\n\t" \
  "v_min_f32 v146, v146, v214\n\t" \
  "v_min_f32 v147, v147, v215\n\t" \
  "v_max3_f32 v148, v196, v197, v198\n\t" \
  "v_max3_f32 v149, v199, v144, v145\n\t" \
  "v_max3_f32 v148, v148, v146, v147\n\t" \
  "v_max_f32 v150, v148, v149\n\t" \
  "v_max_f32 v151, v148, v149\n\t" \
  "s_nop 1\n\t" \
  "v_permlane32_swap_b32 v150, v151\n\t" \
  "v_max_f32 v150, v150, v151\n\t" \
  "s_mov_b32 s92, 0\n\t" \
  ".Ltake%=:\n\t" \
  "s_waitcnt vmcnt(1) lgkmcnt(0)\n\t" \
  "ds_write_b128 v216, v[224:227]\n\t" \
  "v_max3_u32 v148, v224, v225, v226\n\t" \
  "s_waitcnt vmcnt(0)\n\t" \
  "ds_write_b128 v216, v[228:231] offset:1024\n\t" \
  "ds_read_b64_tr_b16 v[160:161], v217 offset:0\n\t" \
  "ds_read_b64_tr_b16 v[162:163], v217 offset:64\n\t" \
  "ds_read_b64_tr_b16 v[164:165], v217 offset:512\n\t" \
  "ds_read_b64_tr_b16 v[166:167], v217 offset:576\n\t" \
  "ds_read_b64_tr_b16 v[168:169], v217 offset:1024\n\t" \
  "ds_read_b64_tr_b16 v[170:171], v217 offset:1088\n\t" \
  "ds_read_b64_tr_b16 v[172:173], v217 offset:1536\n\t" \
  "ds_read_b64_tr_b16 v[174:175], v217 offset:1600\n\t" \
  "v_max3_u32 v148, v148, v227, v228\n\t" \
  "v_max3_u32 v148, v148, v229, v230\n\t" \
  "v_max_u32 v148, v148, v231\n\t" \
  "v_cmp_le_u32 vcc, %[sent], v148\n\t" \
  "s_cbranch_vccnz .Lslow%=\n\t" \
  ".Lok%=:\n\t" \
  "s_waitcnt lgkmcnt(6)\n\t" \
  "v_mfma_f32_16x16x32_bf16 v[152:155], v[160:163], v[128:131], 0\n\t" \
  "s_waitcnt lgkmcnt(4)\n\t" \
  "v_mfma_f32_16x16x32_bf16 v[152:155], v[164:167], v[132:135], v[152:155]\n\t" \
  "s_waitcnt lgkmcnt(2)\n\t" \
  "v_mfma_f32_16x16x32_bf16 v[152:155], v[168:171], v[136:139], v[152:155]\n\t" \
  "s_waitcnt lgkmcnt(0)\n\t" \
  "v_mfma_f32_16x16x32_bf16 v[152:155], v[172:175], v[140:143], v[152:155]\n\t" \
  "v_mov_b32 v157, 0\n\t" \
  "v_mov_b32 v158, 0\n\t" \
  "s_nop 5\n\t" \
  "v_cvt_pk_bf16_f32 v153, v152, v153\n\t" \
  "v_cvt_pk_bf16_f32 v155, v154, v155\n\t" \
  "v_lshlrev_b32 v152, 16, v153\n\t" \
  "v_lshlrev_b32 v154, 16, v155\n\t" \
  "v_and_b32 v153, 0xffff0000, v153\n\t" \
  "v_and_b32 v155, 0xffff0000, v155\n\t" \
  "v_pk_mul_f32 v[152:153], v[152:153], %[sc] op_sel_hi:[1,0]\n\t" \
  "v_pk_mul_f32 v[154:155], v[154:155], %[sc] op_sel_hi:[1,0]\n\t" \
  "v_cvt_pk_bf16_f32 v153, v152, v153\n\t" \
  "v_cvt_pk_bf16_f32 v155, v154, v155\n\t" \
  "v_lshlrev_b32 v152, 16, v153\n\t" \
  "v_lshlrev_b32 v154, 16, v155\n\t" \
  "v_and_b32 v153, 0xffff0000, v153\n\t" \
  "v_and_b32 v155, 0xffff0000, v155\n\t" \
  "v_max3_f32 v148, v152, v153, v150\n\t" \
  "v_max3_f32 v149, v148, v154, v155\n\t" \
  "v_max3_f32 v150, v148, v154, v155\n\t" \
  "v_cndmask_b32 v152, v152, v154, %[up]\n\t" \
  "v_cndmask_b32 v153, v153, v155, %[up]\n\t" \
  "v_permlane16_swap_b32 v149, v150\n\t" \
  "v_max_f32 v148, v149, v150\n\t" \
  "v_mul_f32 v148, %[negc], v148\n\t" \
  "v_fma_f32 v152, v152, %[c], v148\n\t" \
  "v_fma_f32 v153, v153, %[c], v148\n\t" \
  "v_exp_f32 v152, v152\n\t" \
  "v_fma_f32 v196, v196, %[c], v148\n\t" \
  "v_exp_f32 v153, v153\n\t" \
  "v_fma_f32 v197, v197, %[c], v148\n\t" \
  "v_exp_f32 v196, v196\n\t" \
  "v_fma_f32 v198, v198, %[c], v148\n\t" \
  "v_exp_f32 v197, v197\n\t" \
  "v_fma_f32 v199, v199, %[c], v148\n\t" \
  "v_exp_f32 v198, v198\n\t" \
  "v_fma_f32 v144, v144, %[c], v148\n\t" \
  "v_pk_add_f32 v[132:133], v[152:153], v[196:197]\n\t" \
  "v_exp_f32 v199, v199\n\t" \
  "v_fma_f32 v145, v145, %[c], v148\n\t" \
  "v_exp_f32 v144, v144\n\t" \
  "v_fma_f32 v146, v146, %[c], v148\n\t" \
  "v_pk_add_f32 v[132:133], v[132:133], v[198:199]\n\t" \
  "v_exp_f32 v145, v145\n\t" \
  "v_fma_f32 v147, v147, %[c], v148\n\t" \
  "v_exp_f32 v146, v146\n\t" \
  "v_pk_add_f32 v[132:133], v[132:133], v[144:145]\n\t" \
  "v_exp_f32 v147, v147\n\t" \
  "s_nop 0\n\t" \
  "v_pk_add_f32 v[132:133], v[132:133], v[146:147]\n\t" \
  "v_add_f32 v134, v132, v133\n\t" \
  "v_add_f32 v135, v132, v133\n\t" \
  "s_nop 1\n\t" \
  "v_permlane16_swap_b32 v134, v135\n\t" \
  "v_add_f32 v132, v134, v135\n\t" \
  "v_add_f32 v133, v134, v135\n\t" \
  "s_nop 1\n\t" \
  "v_permlane32_swap_b32 v132, v133\n\t" \
  "v_add_f32 v132, v132, v133\n\t" \
  "v_rcp_f32 v148, v132\n\t" \
  "s_nop 0\n\t" \
  "v_pk_mul_f32 v[152:153], v[152:153], v[148:149] op_sel_hi:[1,0]\n\t" \
  "v_pk_mul_f32 v[196:197], v[196:197], v[148:149] op_sel_hi:[1,0]\n\t" \
  "v_pk_mul_f32 v[198:199], v[198:199], v[148:149] op_sel_hi:[1,0]\n\t" \
  "v_pk_mul_f32 v[144:145], v[144:145], v[148:149] op_sel_hi:[1,0]\n\t" \
  "v_pk_mul_f32 v[146:147], v[146:147], v[148:149] op_sel_hi:[1,0]\n\t" \
  "v_cvt_pk_bf16_f32 v156, v152, v153\n\t" \
  "v_cvt_pk_bf16_f32 v159, v152, v153\n\t" \
  "v_cvt_pk_bf16_f32 v128, v196, v197\n\t" \
  "v_cvt_pk_bf16_f32 v129, v198, v199\n\t" \
  "v_mfma_f32_16x16x16_bf16 v[160:163], v[192:193], v[156:157], 0\n\t" \
  "v_cvt_pk_bf16_f32 v130, v144, v145\n\t" \
  "v_mfma_f32_16x16x16_bf16 v[164:167], v[192:193], v[158:159], 0\n\t" \
  "v_cvt_pk_bf16_f32 v131, v146, v147\n\t" \
  "v_mfma_f32_16x16x16_bf16 v[168:171], v[194:195], v[156:157], 0\n\t" \
  "v_mfma_f32_16x16x16_bf16 v[172:175], v[194:195], v[158:159], 0\n\t" \
  "s_nop 0\n\t" \
  "v_mfma_f32_16x16x32_bf16 v[160:163], v[176:179], v[128:131], v[160:163]\n\t" \
  "s_nop 0\n\t" \
  "v_mfma_f32_16x16x32_bf16 v[164:167], v[180:183], v[128:131], v[164:167]\n\t" \
  "s_nop 0\n\t" \
  "v_mfma_f32_16x16x32_bf16 v[168:171], v[184:187], v[128:131], v[168:171]\n\t" \
  "v_mfma_f32_16x16x32_bf16 v[172:175], v[188:191], v[128:131], v[172:175]\n\t" \
  "s_nop 2\n\t" \
  "v_cvt_pk_bf16_f32 v136, v160, v161\n\t" \
  "v_cvt_pk_bf16_f32 v137, v162, v163\n\t" \
  "v_cvt_pk_bf16_f32 v138, v164, v165\n\t" \
  "v_cvt_pk_bf16_f32 v139, v166, v167\n\t" \
  "v_cvt_pk_bf16_f32 v140, v168, v169\n\t" \
  "v_cvt_pk_bf16_f32 v141, v170, v171\n\t" \
  "v_cvt_pk_bf16_f32 v142, v172, v173\n\t" \
  "v_cvt_pk_bf16_f32 v143, v174, v175\n\t" \
  "s_memrealtime s[90:91]\n\t" \
  "s_waitcnt lgkmcnt(0)\n\t" \
  "s_lshl_b32 s93, s90, 2\n\t" \
  "s_sub_i32 s93, s89, s93\n\t" \
  "s_cmp_le_i32 s93, 0\n\t" \
  "s_cbranch_scc1 .Lgoo%=\n\t" \
  "s_min_i32 s93, s93, 8000\n\t" \
  "s_mov_b32 s94, s93\n\t" \
  ".Lslo%=:\n\t" \
  "s_sub_i32 s94, s94, 18\n\t" \
  "s_cmp_lt_i32 s94, 30\n\t" \
  "s_cbranch_scc1 .Lsxo%=\n\t" \
  "s_sleep 1\n\t" \
  "s_branch .Lslo%=\n\t" \
  ".Lsxo%=:\n\t" \
  "s_add_u32 s93, s93, 3\n\t" \
  "s_lshr_b32 s93, s93, 2\n\t" \
  "s_add_u32 s93, s93, s90\n\t" \
  "s_memrealtime s[90:91]\n\t" \
  ".Lpaceo%=:\n\t" \
  "s_memrealtime s[94:95]\n\t" \
  "s_waitcnt lgkmcnt(1)\n\t" \
  "s_sub_i32 s92, s90, s93\n\t" \
  "s_cmp_ge_i32 s92, 0\n\t" \
  "s_cbranch_scc1 .Leo%=\n\t" \
  "s_memrealtime s[90:91]\n\t" \
  "s_waitcnt lgkmcnt(1)\n\t" \
  "s_sub_i32 s92, s94, s93\n\t" \
  "s_cmp_lt_i32 s92, 0\n\t" \
  "s_cbranch_scc1 .Lpaceo%=\n\t" \
  ".Leo%=:\n\t" \
  ".Lgoo%=:\n\t" \
  "buffer_store_dwordx4 v[136:139], v200, %[rt], %[so] offen offset:0 sc1\n\t" \
  "buffer_store_dwordx4 v[140:143], v200, %[rt], %[so] offen offset:1024 sc1\n\t" \
  "s_waitcnt lgkmcnt(0)\n\t" \
  "s_branch .Lend%=\n\t" \
  ".Lpoll%=:\n\t" \
  "buffer_load_dwordx4 v[224:227], v200, %[rs], %[sk] offen offset:0 sc1\n\t" \
  "buffer_load_dwordx4 v[228:231], v200, %[rs], %[sk] offen offset:1024 sc1\n\t" \
  "s_branch .Ltake%=\n\t" \
  "s_branch .Lend%=\n\t" \
  ".Lslow%=:\n\t" \
  "s_mov_b64 s[94:95], 0\n\t" \
  "v_cmp_eq_u32 vcc, %[sent], v224\n\t" \
  "s_or_b64 s[94:95], s[94:95], vcc\n\t" \
  "v_cmp_eq_u32 vcc, %[sent], v225\n\t" \
  "s_or_b64 s[94:95], s[94:95], vcc\n\t" \
  "v_cmp_eq_u32 vcc, %[sent], v226\n\t" \
  "s_or_b64 s[94:95], s[94:95], vcc\n\t" \
  "v_cmp_eq_u32 vcc, %[sent], v227\n\t" \
  "s_or_b64 s[94:95], s[94:95], vcc\n\t" \
  "v_cmp_eq_u32 vcc, %[sent], v228\n\t" \
  "s_or_b64 s[94:95], s[94:95], vcc\n\t" \
  "v_cmp_eq_u32 vcc, %[sent], v229\n\t" \
  "s_or_b64 s[94:95], s[94:95], vcc\n\t" \
  "v_cmp_eq_u32 vcc, %[sent], v230\n\t" \
  "s_or_b64 s[94:95], s[94:95], vcc\n\t" \
  "v_cmp_eq_u32 vcc, %[sent], v231\n\t" \
  "s_or_b64 s[94:95], s[94:95], vcc\n\t" \
  "s_cmp_eq_u64 s[94:95], 0\n\t" \
  "s_cbranch_scc1 .Lok%=\n\t" \
  "s_add_u32 s92, s92, 1\n\t" \
  "s_cmp_eq_u32 s92, 1\n\t" \
  "s_cbranch_scc1 .Lpoll%=\n\t" \
  "s_sleep 2\n\t" \
  "s_and_b32 s93, s92, 63\n\t" \
  "s_cmp_lg_u32 s93, 0\n\t" \
  "s_cbranch_scc1 .Lpoll%=\n\t" \
  "v_mov_b32 v149, 0\n\t" \
  "buffer_load_dword v148, v149, %[rs], %[sab] offen sc1\n\t" \
  "s_waitcnt vmcnt(0)\n\t" \
  "v_readfirstlane_b32 s93, v148\n\t" \
  "s_nop 3\n\t" \
  "s_cmp_eq_u32 s93, 1\n\t" \
  "s_cbranch_scc1 .Lok%=\n\t" \
  "s_cmp_lt_u32 s92, 0x80000\n\t" \
  "s_cbranch_scc1 .Lpoll%=\n\t" \
  "v_mov_b32 v148, 1\n\t" \
  "buffer_store_dword v148, v149, %[rs], %[sab] offen sc1\n\t" \
  "s_branch .Lok%=\n\t" \
  ".Lend%=:\n\t"
constexpr int TLAG = 2;
#define RED_ASM \
  "s_mov_b32 %[step], 0\n\t" \
  "s_lshl_b32 %[sh], %[step], 15\n\t" \
  "s_lshr_b32 s93, %[step], 4\n\t" \
  "s_lshr_b32 s93, %[pm], s93\n\t" \
  "s_and_b32 s93, s93, 4\n\t" \
  "s_lshl_b32 s93, s93, 11\n\t" \
  "s_lshl2_add_u32 %[so], %[sh], s93\n\t" \
  "s_sub_i32 s93, %[step], 2\n\t" \
  "s_lshl_b32 %[st], s93, 19\n\t" \
  "s_lshl_b32 s94, s93, 11\n\t" \
  "s_xor_b32 s94, s94, %[s0]\n\t" \
  "s_and_b32 s94, s94, 0x2000\n\t" \
  "s_or_b32 %[st], %[st], s94\n\t" \
  "s_cmp_lt_i32 s93, 0\n\t" \
  "s_cselect_b32 %[st], 0x70000000, %[st]\n\t" \
  "s_add_u32 %[st4], %[st], 4096\n\t" \
  "buffer_load_dwordx2 v[166:167], v173, %[rs], 0 offen sc1\n\t" \
  "v_mov_b64 v[144:145], 0\n\t" \
  "v_mov_b64 v[146:147], 0\n\t" \
  "v_mov_b64 v[148:149], 0\n\t" \
  "v_mov_b64 v[150:151], 0\n\t" \
  "v_mov_b64 v[152:153], 0\n\t" \
  "v_mov_b64 v[154:155], 0\n\t" \
  "v_mov_b64 v[156:157], 0\n\t" \
  "v_mov_b64 v[158:159], 0\n\t" \
  ".Lstep%=:\n\t" \
  "s_setprio 3\n\t" \
  "s_mov_b32 s92, 0\n\t" \
  ".Lcal%=:\n\t" \
  "s_waitcnt vmcnt(0)\n\t" \
  "s_memrealtime s[76:77]\n\t" \
  "v_cmp_ne_u32 vcc, %[step], v166\n\t" \
  "v_readlane_b32 s90, v167, 0\n\t" \
  "v_readlane_b32 s91, v167, 1\n\t" \
  "v_readlane_b32 s94, v167, 2\n\t" \
  "v_readlane_b32 s95, v167, 3\n\t" \
  "s_sub_i32 s90, s90, %[base]\n\t" \
  "s_sub_i32 s91, s91, %[base]\n\t" \
  "s_sub_i32 s94, s94, %[base]\n\t" \
  "s_sub_i32 s95, s95, %[base]\n\t" \
  "s_max_i32 s90, s90, s91\n\t" \
  "s_max_i32 s94, s94, s95\n\t" \
  "s_max_i32 s90, s90, s94\n\t" \
  "s_add_i32 s90, s90, %[ds]\n\t" \
  "s_waitcnt lgkmcnt(0)\n\t" \
  "s_lshl2_add_u32 s76, s76, %[mn]\n\t" \
  "s_sub_i32 s76, s76, %[base]\n\t" \
  "s_add_u32 %[E], s90, %[base]\n\t" \
  "s_and_b32 s93, vcc_lo, 0xf\n\t" \
  "s_cbranch_scc1 .Lbl%=\n\t" \
  ".Lgo%=:\n\t" \
  "v_mov_b32 v166, 7\n\t" \
  "s_mov_b64 exec, 1\n\t" \
  "v_mov_b32 v164, s90\n\t" \
  "ds_max_u32 v168, v164 offset:4100\n\t" \
  "ds_add_u32 v168, v164 offset:4096\n\t" \
  "s_mov_b64 exec, -1\n\t" \
  "s_memrealtime s[90:91]\n\t" \
  "s_waitcnt lgkmcnt(0)\n\t" \
  "s_lshl_b32 s93, s90, 2\n\t" \
  "s_sub_i32 s93, %[E], s93\n\t" \
  "s_cmp_le_i32 s93, 0\n\t" \
  "s_cbranch_scc1 .Lgop%=\n\t" \
  "s_min_i32 s93, s93, 8000\n\t" \
  "s_mov_b32 s94, s93\n\t" \
  ".Lslp%=:\n\t" \
  "s_sub_i32 s94, s94, 18\n\t" \
  "s_cmp_lt_i32 s94, 30\n\t" \
  "s_cbranch_scc1 .Lsxp%=\n\t" \
  "s_sleep 1\n\t" \
  "s_branch .Lslp%=\n\t" \
  ".Lsxp%=:\n\t" \
  "s_sub_i32 s93, s93, 12\n\t" \
  "s_max_i32 s93, s93, 4\n\t" \
  "s_lshr_b32 s93, s93, 2\n\t" \
  "s_add_u32 s93, s93, s90\n\t" \
  "s_memrealtime s[90:91]\n\t" \
  ".Lpacep%=:\n\t" \
  "s_memrealtime s[94:95]\n\t" \
  "s_waitcnt lgkmcnt(1)\n\t" \
  "s_sub_i32 s92, s90, s93\n\t" \
  "s_cmp_ge_i32 s92, 0\n\t" \
  "s_cbranch_scc1 .Lep%=\n\t" \
  "s_memrealtime s[90:91]\n\t" \
  "s_waitcnt lgkmcnt(1)\n\t" \
  "s_sub_i32 s92, s94, s93\n\t" \
  "s_cmp_lt_i32 s92, 0\n\t" \
  "s_cbranch_scc1 .Lpacep%=\n\t" \
  "s_nop 4\n\t" \
  ".Lep%=:\n\t" \
  "s_add_u32 s92, s92, s93\n\t" \
  "s_lshl_b32 s92, s92, 2\n\t" \
  "s_sub_i32 s92, %[E], s92\n\t" \
  "s_max_i32 s92, s92, 0\n\t" \
  "s_bitcmp0_b32 s92, 3\n\t" \
  "s_cbranch_scc1 .Lf3p%=\n\t" \
  "s_nop 15\n\t" \
  ".Lf3p%=:\n\t" \
  "s_bitcmp0_b32 s92, 2\n\t" \
  "s_cbranch_scc1 .Lf2p%=\n\t" \
  "s_nop 10\n\t" \
  ".Lf2p%=:\n\t" \
  ".Lgop%=:\n\t" \
  "s_mov_b32 s92, 0\n\t" \
  "buffer_load_dwordx4 v[128:131], v234, %[rs], %[so] offen sc1\n\t" \
  "s_nop 15\n\t" \
  "buffer_load_dwordx4 v[132:135], v235, %[rs], %[so] offen sc1\n\t" \
  "s_nop 15\n\t" \
  "buffer_load_dwordx4 v[136:139], v236, %[rs], %[so] offen sc1\n\t" \
  "s_nop 15\n\t" \
  "buffer_load_dwordx4 v[140:143], v237, %[rs], %[so] offen sc1\n\t" \
  "s_waitcnt lgkmcnt(0)\n\t" \
  ".Lw%=:\n\t" \
  "s_waitcnt vmcnt(3)\n\t" \
  "v_smfmac_f32_16x16x64_bf16 v[144:147], v[128:131], v[0:7], v172\n\t" \
  "v_max3_u32 v164, v128, v129, 0\n\t" \
  "v_smfmac_f32_16x16x64_bf16 v[148:151], v[128:131], v[8:15], v172\n\t" \
  "v_max3_u32 v164, v130, v131, v164\n\t" \
  "v_smfmac_f32_16x16x64_bf16 v[152:155], v[128:131], v[16:23], v172\n\t" \
  "v_smfmac_f32_16x16x64_bf16 v[156:159], v[128:131], v[24:31], v172\n\t" \
  "buffer_load_dwordx4 v[176:179], v238, %[rt], %[st] offen offset:0 sc1\n\t" \
  "s_waitcnt vmcnt(3)\n\t" \
  "v_smfmac_f32_16x16x64_bf16 v[144:147], v[132:135], v[32:39], v172\n\t" \
  "v_max3_u32 v164, v132, v133, v164\n\t" \
  "v_smfmac_f32_16x16x64_bf16 v[148:151], v[132:135], v[40:47], v172\n\t" \
  "v_max3_u32 v164, v134, v135, v164\n\t" \
  "v_smfmac_f32_16x16x64_bf16 v[152:155], v[132:135], v[48:55], v172\n\t" \
  "v_smfmac_f32_16x16x64_bf16 v[156:159], v[132:135], v[56:63], v172\n\t" \
  "buffer_load_dwordx4 v[180:183], v238, %[rt], %[st] offen offset:1024 sc1\n\t" \
  "s_waitcnt vmcnt(3)\n\t" \
  "v_smfmac_f32_16x16x64_bf16 v[144:147], v[136:139], v[64:71], v172\n\t" \
  "v_max3_u32 v164, v136, v137, v164\n\t" \
  "v_smfmac_f32_16x16x64_bf16 v[148:151], v[136:139], v[72:79], v172\n\t" \
  "v_max3_u32 v164, v138, v139, v164\n\t" \
  "v_smfmac_f32_16x16x64_bf16 v[152:155], v[136:139], v[80:87], v172\n\t" \
  "v_smfmac_f32_16x16x64_bf16 v[156:159], v[136:139], v[88:95], v172\n\t" \
  "buffer_load_dwordx4 v[184:187], v238, %[rt], %[st4] offen offset:0 sc1\n\t" \
  "s_waitcnt vmcnt(3)\n\t" \
  "v_smfmac_f32_16x16x64_bf16 v[144:147], v[140:143], v[96:103], v172\n\t" \
  "v_max3_u32 v164, v140, v141, v164\n\t" \
  "v_smfmac_f32_16x16x64_bf16 v[148:151], v[140:143], v[104:111], v172\n\t" \
  "v_max3_u32 v164, v142, v143, v164\n\t" \
  "v_smfmac_f32_16x16x64_bf16 v[152:155], v[140:143], v[112:119], v172\n\t" \
  "v_cmp_le_u32 vcc, %[sent], v164\n\t" \
  "v_smfmac_f32_16x16x64_bf16 v[156:159], v[140:143], v[120:127], v172\n\t" \
  "buffer_load_dwordx4 v[188:191], v238, %[rt], %[st4] offen offset:1024 sc1\n\t" \
  "s_cbranch_vccnz .Lslow%=\n\t" \
  ".Lok%=:\n\t" \
  "v_add_f32_dpp v160, v144, v146 quad_perm:[0,1,2,3] row_mask:0x3 bank_mask:0xf\n\t" \
  "v_add_f32_dpp v161, v145, v147 quad_perm:[0,1,2,3] row_mask:0x3 bank_mask:0xf\n\t" \
  "v_add_f32_dpp v162, v148, v150 quad_perm:[0,1,2,3] row_mask:0x3 bank_mask:0xf\n\t" \
  "v_add_f32_dpp v163, v149, v151 quad_perm:[0,1,2,3] row_mask:0x3 bank_mask:0xf\n\t" \
  "v_add_f32_dpp v160, v152, v154 quad_perm:[0,1,2,3] row_mask:0xc bank_mask:0xf\n\t" \
  "v_add_f32_dpp v161, v153, v155 quad_perm:[0,1,2,3] row_mask:0xc bank_mask:0xf\n\t" \
  "v_add_f32_dpp v162, v156, v158 quad_perm:[0,1,2,3] row_mask:0xc bank_mask:0xf\n\t" \
  "v_add_f32_dpp v163, v157, v159 quad_perm:[0,1,2,3] row_mask:0xc bank_mask:0xf\n\t" \
  "s_nop 0\n\t" \
  "v_permlane32_swap_b32 v160, v162\n\t" \
  "v_permlane32_swap_b32 v161, v163\n\t" \
  "v_add_f32 v160, v160, v162\n\t" \
  "v_add_f32 v161, v161, v163\n\t" \
  "ds_write_b64 v169, v[160:161]\n\t" \
  "s_mov_b64 exec, 1\n\t" \
  "ds_inc_rtn_u32 v164, v168, v166 offset:4104\n\t" \
  "s_mov_b64 exec, -1\n\t" \
  "ds_read_b64 v[128:129], v170 offset:0\n\t" \
  "ds_read_b64 v[130:131], v170 offset:512\n\t" \
  "ds_read_b64 v[132:133], v170 offset:1024\n\t" \
  "ds_read_b64 v[134:135], v170 offset:1536\n\t" \
  "ds_read_b64 v[136:137], v170 offset:2048\n\t" \
  "ds_read_b64 v[138:139], v170 offset:2560\n\t" \
  "ds_read_b64 v[140:141], v170 offset:3072\n\t" \
  "ds_read_b64 v[142:143], v170 offset:3584\n\t" \
  "s_waitcnt lgkmcnt(8)\n\t" \
  "v_readfirstlane_b32 s93, v164\n\t" \
  "s_cmp_lg_u32 s93, 7\n\t" \
  "s_cbranch_scc1 .Lb1%=\n\t" \
  "s_waitcnt lgkmcnt(4)\n\t" \
  "v_pk_add_f32 v[128:129], v[128:129], v[130:131]\n\t" \
  "v_pk_add_f32 v[132:133], v[132:133], v[134:135]\n\t" \
  "s_waitcnt lgkmcnt(0)\n\t" \
  "v_pk_add_f32 v[136:137], v[136:137], v[138:139]\n\t" \
  "v_pk_add_f32 v[140:141], v[140:141], v[142:143]\n\t" \
  "v_pk_add_f32 v[128:129], v[128:129], v[132:133]\n\t" \
  "v_pk_add_f32 v[136:137], v[136:137], v[140:141]\n\t" \
  "v_pk_add_f32 v[128:129], v[128:129], v[136:137]\n\t" \
  "v_cvt_pk_bf16_f32 v128, v128, v129\n\t" \
  "s_nop 1\n\t" \
  "v_mov_b32_dpp v129, v128 quad_perm:[1,0,3,2] row_mask:0xf bank_mask:0xf\n\t" \
  "v_perm_b32 v128, v129, v128, v174\n\t" \
  "buffer_store_dword v128, v171, %[rs], %[sh] offen sc1\n\t" \
  "s_memtime s[90:91]\n\t" \
  "s_barrier\n\t" \
  "s_memtime s[76:77]\n\t" \
  "s_memrealtime s[78:79]\n\t" \
  "s_nop 3\n\t" \
  "s_memrealtime s[80:81]\n\t" \
  "s_nop 3\n\t" \
  "s_memrealtime s[82:83]\n\t" \
  "s_nop 3\n\t" \
  "s_memrealtime s[84:85]\n\t" \
  "s_nop 3\n\t" \
  "s_memrealtime s[86:87]\n\t" \
  "s_nop 3\n\t" \
  "s_memrealtime s[88:89]\n\t" \
  "s_mov_b32 s93, 0\n\t" \
  "s_waitcnt lgkmcnt(0)\n\t" \
  "s_cmp_eq_u32 s80, s78\n\t" \
  "s_addc_u32 s93, s93, 0\n\t" \
  "s_cmp_eq_u32 s82, s78\n\t" \
  "s_addc_u32 s93, s93, 0\n\t" \
  "s_cmp_eq_u32 s84, s78\n\t" \
  "s_addc_u32 s93, s93, 0\n\t" \
  "s_cmp_eq_u32 s86, s78\n\t" \
  "s_addc_u32 s93, s93, 0\n\t" \
  "s_cmp_eq_u32 s88, s78\n\t" \
  "s_addc_u32 s93, s93, 0\n\t" \
  "s_mul_i32 s93, s93, 21\n\t" \
  "s_add_u32 s76, s76, 15\n\t" \
  "s_add_u32 s76, s76, s93\n\t" \
  "s_lshl2_add_u32 s78, s78, 16\n\t" \
  "s_mov_b64 exec, 1\n\t" \
  "s_sub_i32 s90, s76, s90\n\t" \
  "s_mul_i32 s90, s90, 11900\n\t" \
  "s_ashr_i32 s90, s90, 16\n\t" \
  "s_sub_u32 s90, s78, s90\n\t" \
  "s_sub_u32 s90, s90, %[base]\n\t" \
  "v_mov_b32 v164, s90\n\t" \
  "ds_write_b32 v168, v164 offset:4108\n\t" \
  "s_mov_b64 exec, -1\n\t" \
  "s_waitcnt vmcnt(1)\n\t" \
  ".Lb2%=:\n\t" \
  "s_setprio 0\n\t" \
  "s_bitcmp0_b64 %[am], %[step]\n\t" \
  "s_cbranch_scc1 .La1%=\n\t" \
  "buffer_store_dwordx4 v[224:227], v228, %[ra], 0 offen sc1\n\t" \
  ".La1%=:\n\t" \
  "buffer_load_dwordx4 v[192:195], v239, %[rt], %[st] offen offset:0 sc1\n\t" \
  "buffer_load_dwordx4 v[196:199], v239, %[rt], %[st] offen offset:1024 sc1\n\t" \
  "v_mfma_f32_16x16x32_bf16 v[128:131], v[0:3], v[176:179], 0\n\t" \
  "v_mfma_f32_16x16x32_bf16 v[136:139], v[8:11], v[176:179], 0\n\t" \
  "ds_read2st64_b32 v[144:145], v231 offset0:0 offset1:8\n\t" \
  "ds_read2st64_b32 v[146:147], v231 offset0:16 offset1:24\n\t" \
  "ds_read2st64_b32 v[148:149], v231 offset0:32 offset1:40\n\t" \
  "ds_read2st64_b32 v[150:151], v231 offset0:48 offset1:56\n\t" \
  "buffer_load_dwordx4 v[200:203], v239, %[rt], %[st4] offen offset:0 sc1\n\t" \
  "buffer_load_dwordx4 v[204:207], v239, %[rt], %[st4] offen offset:1024 sc1\n\t" \
  "v_mfma_f32_16x16x32_bf16 v[132:135], v[4:7], v[180:183], 0\n\t" \
  "v_mfma_f32_16x16x32_bf16 v[140:143], v[12:15], v[180:183], 0\n\t" \
  "s_waitcnt lgkmcnt(0)\n\t" \
  "v_add_f32 v144, v144, v145\n\t" \
  "v_add_f32 v146, v146, v147\n\t" \
  "v_add_f32 v148, v148, v149\n\t" \
  "v_add_f32 v150, v150, v151\n\t" \
  "v_add_f32 v144, v144, v146\n\t" \
  "v_add_f32 v148, v148, v150\n\t" \
  "buffer_load_dwordx4 v[208:211], v240, %[rt], %[st] offen offset:0 sc1\n\t" \
  "buffer_load_dwordx4 v[212:215], v240, %[rt], %[st] offen offset:1024 sc1\n\t" \
  "v_mfma_f32_16x16x32_bf16 v[128:131], v[16:19], v[184:187], v[128:131]\n\t" \
  "v_mfma_f32_16x16x32_bf16 v[136:139], v[24:27], v[184:187], v[136:139]\n\t" \
  "v_add_f32 v144, v144, v148\n\t" \
  "v_cvt_pk_bf16_f32 v144, v144, v144\n\t" \
  "ds_write_b16 v232, v144\n\t" \
  "ds_read_b128 v[224:227], v229\n\t" \
  "v_add_u32 v228, %[ainc], v228\n\t" \
  "v_add_u32 v229, %[rinc], v229\n\t" \
  "v_add_u32 v232, 1024, v232\n\t" \
  "v_xor_b32 v231, 0x4000, v231\n\t" \
  "buffer_load_dwordx4 v[216:219], v240, %[rt], %[st4] offen offset:0 sc1\n\t" \
  "buffer_load_dwordx4 v[220:223], v240, %[rt], %[st4] offen offset:1024 sc1\n\t" \
  "v_mfma_f32_16x16x32_bf16 v[132:135], v[20:23], v[188:191], v[132:135]\n\t" \
  "v_mfma_f32_16x16x32_bf16 v[140:143], v[28:31], v[188:191], v[140:143]\n\t" \
  "buffer_load_dwordx4 v[176:179], v241, %[rt], %[st] offen offset:0 sc1\n\t" \
  "s_waitcnt vmcnt(8)\n\t" \
  "v_mfma_f32_16x16x32_bf16 v[128:131], v[32:35], v[192:195], v[128:131]\n\t" \
  "v_mfma_f32_16x16x32_bf16 v[136:139], v[40:43], v[192:195], v[136:139]\n\t" \
  "buffer_load_dwordx4 v[180:183], v241, %[rt], %[st] offen offset:1024 sc1\n\t" \
  "s_waitcnt vmcnt(8)\n\t" \
  "v_mfma_f32_16x16x32_bf16 v[132:135], v[36:39], v[196:199], v[132:135]\n\t" \
  "v_mfma_f32_16x16x32_bf16 v[140:143], v[44:47], v[196:199], v[140:143]\n\t" \
  "buffer_load_dwordx4 v[184:187], v241, %[rt], %[st4] offen offset:0 sc1\n\t" \
  "s_waitcnt vmcnt(8)\n\t" \
  "v_mfma_f32_16x16x32_bf16 v[128:131], v[48:51], v[200:203], v[128:131]\n\t" \
  "v_mfma_f32_16x16x32_bf16 v[136:139], v[56:59], v[200:203], v[136:139]\n\t" \
  "ds_read_b128 v[160:163], v168 offset:4096\n\t" \
  "buffer_load_dwordx4 v[188:191], v241, %[rt], %[st4] offen offset:1024 sc1\n\t" \
  "s_waitcnt vmcnt(8)\n\t" \
  "v_mfma_f32_16x16x32_bf16 v[132:135], v[52:55], v[204:207], v[132:135]\n\t" \
  "v_mfma_f32_16x16x32_bf16 v[140:143], v[60:63], v[204:207], v[140:143]\n\t" \
  "s_waitcnt lgkmcnt(0)\n\t" \
  "v_readfirstlane_b32 s90, v161\n\t" \
  "v_readfirstlane_b32 s91, v163\n\t" \
  "v_readfirstlane_b32 s94, v160\n\t" \
  "s_sub_u32 s93, s94, %[sm]\n\t" \
  "s_mov_b32 %[sm], s94\n\t" \
  "s_lshr_b32 s93, s93, 3\n\t" \
  "s_sub_i32 s93, s90, s93\n\t" \
  "s_mul_i32 s93, s93, %[wm]\n\t" \
  "s_ashr_i32 s93, s93, 2\n\t" \
  "s_min_u32 s93, s93, %[gm]\n\t" \
  "s_sub_i32 s90, s90, s93\n\t" \
  "s_add_u32 s93, s90, %[rl]\n\t" \
  "s_add_u32 s93, s93, %[base]\n\t" \
  "s_add_u32 %[E], s93, %[lk]\n\t" \
  "s_sub_i32 s91, s91, s90\n\t" \
  "s_sub_i32 s91, s91, %[rl]\n\t" \
  "s_min_i32 s93, s91, %[up]\n\t" \
  "s_max_i32 s93, s93, %[dn]\n\t" \
  "s_cmp_gt_i32 s91, 60\n\t" \
  "s_cselect_b32 s93, 2, s93\n\t" \
  "s_add_i32 %[rl], %[rl], s93\n\t" \
  "v_mov_b32 v160, %[rl]\n\t" \
  "buffer_store_dword v160, v175, %[rs], 0 offen sc1\n\t" \
  "s_waitcnt vmcnt(8)\n\t" \
  "v_mfma_f32_16x16x32_bf16 v[128:131], v[64:67], v[208:211], v[128:131]\n\t" \
  "v_mfma_f32_16x16x32_bf16 v[136:139], v[72:75], v[208:211], v[136:139]\n\t" \
  "s_add_u32 %[step], %[step], 1\n\t" \
  "s_lshl_b32 %[sh], %[step], 15\n\t" \
  "s_lshr_b32 s93, %[step], 4\n\t" \
  "s_lshr_b32 s93, %[pm], s93\n\t" \
  "s_and_b32 s93, s93, 4\n\t" \
  "s_lshl_b32 s93, s93, 11\n\t" \
  "s_lshl2_add_u32 %[so], %[sh], s93\n\t" \
  "s_waitcnt vmcnt(7)\n\t" \
  "v_mfma_f32_16x16x32_bf16 v[132:135], v[68:71], v[212:215], v[132:135]\n\t" \
  "v_mfma_f32_16x16x32_bf16 v[140:143], v[76:79], v[212:215], v[140:143]\n\t" \
  "s_sub_i32 s93, %[step], 2\n\t" \
  "s_lshl_b32 %[st], s93, 19\n\t" \
  "s_lshl_b32 s94, s93, 11\n\t" \
  "s_xor_b32 s94, s94, %[s0]\n\t" \
  "s_and_b32 s94, s94, 0x2000\n\t" \
  "s_or_b32 %[st], %[st], s94\n\t" \
  "s_cmp_lt_i32 s93, 0\n\t" \
  "s_cselect_b32 %[st], 0x70000000, %[st]\n\t" \
  "s_add_u32 %[st4], %[st], 4096\n\t" \
  "s_waitcnt vmcnt(6)\n\t" \
  "v_mfma_f32_16x16x32_bf16 v[128:131], v[80:83], v[216:219], v[128:131]\n\t" \
  "v_mfma_f32_16x16x32_bf16 v[136:139], v[88:91], v[216:219], v[136:139]\n\t" \
  "s_waitcnt vmcnt(5)\n\t" \
  "v_mfma_f32_16x16x32_bf16 v[132:135], v[84:87], v[220:223], v[132:135]\n\t" \
  "v_mfma_f32_16x16x32_bf16 v[140:143], v[92:95], v[220:223], v[140:143]\n\t" \
  "s_cmp_eq_u32 %[step], 63\n\t" \
  "s_cbranch_scc1 .Llast%=\n\t" \
  "s_movk_i32 s93, 600\n\t" \
  ".Lpacel%=:\n\t" \
  "s_memrealtime s[90:91]\n\t" \
  "s_waitcnt lgkmcnt(0)\n\t" \
  "s_lshl_b32 s90, s90, 2\n\t" \
  "s_sub_i32 s91, %[E], s90\n\t" \
  "s_cmp_le_i32 s91, 0\n\t" \
  "s_cbranch_scc1 .Lgol%=\n\t" \
  "s_sub_u32 s93, s93, 1\n\t" \
  "s_cmp_lg_u32 s93, 0\n\t" \
  "s_cbranch_scc1 .Lpacel%=\n\t" \
  ".Lgol%=:\n\t" \
  "buffer_load_dwordx2 v[166:167], v173, %[rs], 0 offen sc1\n\t" \
  "s_waitcnt vmcnt(5)\n\t" \
  "v_mfma_f32_16x16x32_bf16 v[128:131], v[96:99], v[176:179], v[128:131]\n\t" \
  "v_mfma_f32_16x16x32_bf16 v[136:139], v[104:107], v[176:179], v[136:139]\n\t" \
  "s_waitcnt vmcnt(4)\n\t" \
  "v_mfma_f32_16x16x32_bf16 v[132:135], v[100:103], v[180:183], v[132:135]\n\t" \
  "v_mfma_f32_16x16x32_bf16 v[140:143], v[108:111], v[180:183], v[140:143]\n\t" \
  "s_waitcnt vmcnt(3)\n\t" \
  "v_mfma_f32_16x16x32_bf16 v[128:131], v[112:115], v[184:187], v[128:131]\n\t" \
  "v_mfma_f32_16x16x32_bf16 v[136:139], v[120:123], v[184:187], v[136:139]\n\t" \
  "s_waitcnt vmcnt(2)\n\t" \
  "v_mfma_f32_16x16x32_bf16 v[132:135], v[116:119], v[188:191], v[132:135]\n\t" \
  "v_mfma_f32_16x16x32_bf16 v[140:143], v[124:127], v[188:191], v[140:143]\n\t" \
  "v_mov_b64 v[144:145], 0\n\t" \
  "v_mov_b64 v[146:147], 0\n\t" \
  "v_mov_b64 v[148:149], 0\n\t" \
  "v_mov_b64 v[150:151], 0\n\t" \
  "v_mov_b64 v[152:153], 0\n\t" \
  "v_mov_b64 v[154:155], 0\n\t" \
  "v_mov_b64 v[156:157], 0\n\t" \
  "v_mov_b64 v[158:159], 0\n\t" \
  "v_pk_add_f32 v[128:129], v[128:129], v[132:133]\n\t" \
  "v_pk_add_f32 v[130:131], v[130:131], v[134:135]\n\t" \
  "ds_write_b128 v230, v[128:131] offset:0\n\t" \
  "v_pk_add_f32 v[136:137], v[136:137], v[140:141]\n\t" \
  "v_pk_add_f32 v[138:139], v[138:139], v[142:143]\n\t" \
  "ds_write_b128 v230, v[136:139] offset:1024\n\t" \
  "v_xor_b32 v230, 0x4000, v230\n\t" \
  "s_cmp_lg_u32 %[step], 63\n\t" \
  "s_cbranch_scc1 .Lstep%=\n\t" \
  ".Lfc%=:\n\t" \
  "s_waitcnt vmcnt(0)\n\t" \
  "v_cmp_eq_u32 vcc, -1, v166\n\t" \
  "s_cbranch_vccnz .Lfw%=\n\t" \
  ".Lfo%=:\n\t" \
  "v_add_u32 v242, 16384, v242\n\t" \
  "buffer_load_dwordx4 v[176:179], v238, %[rt], %[st] offen offset:0 sc1\n\t" \
  "buffer_load_dwordx4 v[180:183], v238, %[rt], %[st] offen offset:1024 sc1\n\t" \
  "buffer_load_dwordx4 v[184:187], v238, %[rt], %[st4] offen offset:0 sc1\n\t" \
  "buffer_load_dwordx4 v[188:191], v238, %[rt], %[st4] offen offset:1024 sc1\n\t" \
  "buffer_load_dwordx4 v[192:195], v239, %[rt], %[st] offen offset:0 sc1\n\t" \
  "buffer_load_dwordx4 v[196:199], v239, %[rt], %[st] offen offset:1024 sc1\n\t" \
  "buffer_load_dwordx4 v[200:203], v239, %[rt], %[st4] offen offset:0 sc1\n\t" \
  "buffer_load_dwordx4 v[204:207], v239, %[rt], %[st4] offen offset:1024 sc1\n\t" \
  "v_subrev_u32 v230, 98304, v230\n\t" \
  ".Lt%=:\n\t" \
  "s_waitcnt vmcnt(7)\n\t" \
  "v_mfma_f32_16x16x32_bf16 v[128:131], v[0:3], v[176:179], 0\n\t" \
  "v_mfma_f32_16x16x32_bf16 v[136:139], v[8:11], v[176:179], 0\n\t" \
  "buffer_load_dwordx4 v[176:179], v240, %[rt], %[st] offen offset:0 sc1\n\t" \
  "s_waitcnt vmcnt(7)\n\t" \
  "v_mfma_f32_16x16x32_bf16 v[132:135], v[4:7], v[180:183], 0\n\t" \
  "v_mfma_f32_16x16x32_bf16 v[140:143], v[12:15], v[180:183], 0\n\t" \
  "buffer_load_dwordx4 v[180:183], v240, %[rt], %[st] offen offset:1024 sc1\n\t" \
  "s_waitcnt vmcnt(7)\n\t" \
  "v_mfma_f32_16x16x32_bf16 v[128:131], v[16:19], v[184:187], v[128:131]\n\t" \
  "v_mfma_f32_16x16x32_bf16 v[136:139], v[24:27], v[184:187], v[136:139]\n\t" \
  "buffer_load_dwordx4 v[184:187], v240, %[rt], %[st4] offen offset:0 sc1\n\t" \
  "buffer_load_dword v166, v242, %[rs], 0 offen sc1\n\t" \
  "s_waitcnt vmcnt(8)\n\t" \
  "v_mfma_f32_16x16x32_bf16 v[132:135], v[20:23], v[188:191], v[132:135]\n\t" \
  "v_mfma_f32_16x16x32_bf16 v[140:143], v[28:31], v[188:191], v[140:143]\n\t" \
  "buffer_load_dwordx4 v[188:191], v240, %[rt], %[st4] offen offset:1024 sc1\n\t" \
  "s_waitcnt vmcnt(8)\n\t" \
  "v_mfma_f32_16x16x32_bf16 v[128:131], v[32:35], v[192:195], v[128:131]\n\t" \
  "v_mfma_f32_16x16x32_bf16 v[136:139], v[40:43], v[192:195], v[136:139]\n\t" \
  "buffer_load_dwordx4 v[192:195], v241, %[rt], %[st] offen offset:0 sc1\n\t" \
  "s_waitcnt vmcnt(8)\n\t" \
  "v_mfma_f32_16x16x32_bf16 v[132:135], v[36:39], v[196:199], v[132:135]\n\t" \
  "v_mfma_f32_16x16x32_bf16 v[140:143], v[44:47], v[196:199], v[140:143]\n\t" \
  "buffer_load_dwordx4 v[196:199], v241, %[rt], %[st] offen offset:1024 sc1\n\t" \
  "s_waitcnt vmcnt(8)\n\t" \
  "v_mfma_f32_16x16x32_bf16 v[128:131], v[48:51], v[200:203], v[128:131]\n\t" \
  "v_mfma_f32_16x16x32_bf16 v[136:139], v[56:59], v[200:203], v[136:139]\n\t" \
  "buffer_load_dwordx4 v[200:203], v241, %[rt], %[st4] offen offset:0 sc1\n\t" \
  "s_waitcnt vmcnt(8)\n\t" \
  "v_mfma_f32_16x16x32_bf16 v[132:135], v[52:55], v[204:207], v[132:135]\n\t" \
  "v_mfma_f32_16x16x32_bf16 v[140:143], v[60:63], v[204:207], v[140:143]\n\t" \
  "buffer_load_dwordx4 v[204:207], v241, %[rt], %[st4] offen offset:1024 sc1\n\t" \
  "s_waitcnt vmcnt(8)\n\t" \
  "v_mfma_f32_16x16x32_bf16 v[128:131], v[64:67], v[176:179], v[128:131]\n\t" \
  "v_mfma_f32_16x16x32_bf16 v[136:139], v[72:75], v[176:179], v[136:139]\n\t" \
  "s_waitcnt vmcnt(5)\n\t" \
  ".Ltc%=:\n\t" \
  "v_cmp_eq_u32 vcc, -1, v166\n\t" \
  "s_cbranch_vccnz .Ltw%=\n\t" \
  ".Lto%=:\n\t" \
  "s_cmp_eq_u32 %[step], 65\n\t" \
  "s_cbranch_scc1 .Ltl%=\n\t" \
  "s_add_u32 %[step], %[step], 1\n\t" \
  "s_sub_i32 s93, %[step], 2\n\t" \
  "s_lshl_b32 %[st], s93, 19\n\t" \
  "s_lshl_b32 s94, s93, 11\n\t" \
  "s_xor_b32 s94, s94, %[s0]\n\t" \
  "s_and_b32 s94, s94, 0x2000\n\t" \
  "s_or_b32 %[st], %[st], s94\n\t" \
  "s_cmp_lt_i32 s93, 0\n\t" \
  "s_cselect_b32 %[st], 0x70000000, %[st]\n\t" \
  "s_add_u32 %[st4], %[st], 4096\n\t" \
  "v_add_u32 v242, 16384, v242\n\t" \
  "s_cmp_eq_u32 %[step], 65\n\t" \
  "s_cbranch_scc0 .Ltn%=\n\t" \
  "v_mov_b32 v242, %[fg]\n\t" \
  ".Ltn%=:\n\t" \
  "buffer_load_dwordx4 v[176:179], v238, %[rt], %[st] offen offset:0 sc1\n\t" \
  "s_waitcnt vmcnt(7)\n\t" \
  "v_mfma_f32_16x16x32_bf16 v[132:135], v[68:71], v[180:183], v[132:135]\n\t" \
  "v_mfma_f32_16x16x32_bf16 v[140:143], v[76:79], v[180:183], v[140:143]\n\t" \
  "buffer_load_dwordx4 v[180:183], v238, %[rt], %[st] offen offset:1024 sc1\n\t" \
  "s_waitcnt vmcnt(7)\n\t" \
  "v_mfma_f32_16x16x32_bf16 v[128:131], v[80:83], v[184:187], v[128:131]\n\t" \
  "v_mfma_f32_16x16x32_bf16 v[136:139], v[88:91], v[184:187], v[136:139]\n\t" \
  "buffer_load_dwordx4 v[184:187], v238, %[rt], %[st4] offen offset:0 sc1\n\t" \
  "s_waitcnt vmcnt(7)\n\t" \
  "v_mfma_f32_16x16x32_bf16 v[132:135], v[84:87], v[188:191], v[132:135]\n\t" \
  "v_mfma_f32_16x16x32_bf16 v[140:143], v[92:95], v[188:191], v[140:143]\n\t" \
  "buffer_load_dwordx4 v[188:191], v238, %[rt], %[st4] offen offset:1024 sc1\n\t" \
  "s_waitcnt vmcnt(7)\n\t" \
  "v_mfma_f32_16x16x32_bf16 v[128:131], v[96:99], v[192:195], v[128:131]\n\t" \
  "v_mfma_f32_16x16x32_bf16 v[136:139], v[104:107], v[192:195], v[136:139]\n\t" \
  "buffer_load_dwordx4 v[192:195], v239, %[rt], %[st] offen offset:0 sc1\n\t" \
  "s_waitcnt vmcnt(7)\n\t" \
  "v_mfma_f32_16x16x32_bf16 v[132:135], v[100:103], v[196:199], v[132:135]\n\t" \
  "v_mfma_f32_16x16x32_bf16 v[140:143], v[108:111], v[196:199], v[140:143]\n\t" \
  "buffer_load_dwordx4 v[196:199], v239, %[rt], %[st] offen offset:1024 sc1\n\t" \
  "s_waitcnt vmcnt(7)\n\t" \
  "v_mfma_f32_16x16x32_bf16 v[128:131], v[112:115], v[200:203], v[128:131]\n\t" \
  "v_mfma_f32_16x16x32_bf16 v[136:139], v[120:123], v[200:203], v[136:139]\n\t" \
  "buffer_load_dwordx4 v[200:203], v239, %[rt], %[st4] offen offset:0 sc1\n\t" \
  "s_waitcnt vmcnt(7)\n\t" \
  "v_mfma_f32_16x16x32_bf16 v[132:135], v[116:119], v[204:207], v[132:135]\n\t" \
  "v_mfma_f32_16x16x32_bf16 v[140:143], v[124:127], v[204:207], v[140:143]\n\t" \
  "buffer_load_dwordx4 v[204:207], v239, %[rt], %[st4] offen offset:1024 sc1\n\t" \
  "s_nop 5\n\t" \
  "v_pk_add_f32 v[128:129], v[128:129], v[132:133]\n\t" \
  "v_pk_add_f32 v[130:131], v[130:131], v[134:135]\n\t" \
  "ds_write_b128 v230, v[128:131] offset:0\n\t" \
  "v_pk_add_f32 v[136:137], v[136:137], v[140:141]\n\t" \
  "v_pk_add_f32 v[138:139], v[138:139], v[142:143]\n\t" \
  "ds_write_b128 v230, v[136:139] offset:1024\n\t" \
  "v_add_u32 v230, 16384, v230\n\t" \
  "s_branch .Lt%=\n\t" \
  ".Ltl%=:\n\t" \
  "buffer_load_dwordx4 v[224:227], v243, %[rg], 0 offen sc1\n\t" \
  "s_waitcnt vmcnt(7)\n\t" \
  "v_mfma_f32_16x16x32_bf16 v[132:135], v[68:71], v[180:183], v[132:135]\n\t" \
  "v_mfma_f32_16x16x32_bf16 v[140:143], v[76:79], v[180:183], v[140:143]\n\t" \
  "s_waitcnt vmcnt(6)\n\t" \
  "v_mfma_f32_16x16x32_bf16 v[128:131], v[80:83], v[184:187], v[128:131]\n\t" \
  "v_mfma_f32_16x16x32_bf16 v[136:139], v[88:91], v[184:187], v[136:139]\n\t" \
  "s_waitcnt vmcnt(5)\n\t" \
  "v_mfma_f32_16x16x32_bf16 v[132:135], v[84:87], v[188:191], v[132:135]\n\t" \
  "v_mfma_f32_16x16x32_bf16 v[140:143], v[92:95], v[188:191], v[140:143]\n\t" \
  "s_waitcnt vmcnt(4)\n\t" \
  "v_mfma_f32_16x16x32_bf16 v[128:131], v[96:99], v[192:195], v[128:131]\n\t" \
  "v_mfma_f32_16x16x32_bf16 v[136:139], v[104:107], v[192:195], v[136:139]\n\t" \
  "s_waitcnt vmcnt(3)\n\t" \
  "v_mfma_f32_16x16x32_bf16 v[132:135], v[100:103], v[196:199], v[132:135]\n\t" \
  "v_mfma_f32_16x16x32_bf16 v[140:143], v[108:111], v[196:199], v[140:143]\n\t" \
  "s_waitcnt vmcnt(2)\n\t" \
  "v_mfma_f32_16x16x32_bf16 v[128:131], v[112:115], v[200:203], v[128:131]\n\t" \
  "v_mfma_f32_16x16x32_bf16 v[136:139], v[120:123], v[200:203], v[136:139]\n\t" \
  "s_waitcnt vmcnt(1)\n\t" \
  "v_mfma_f32_16x16x32_bf16 v[132:135], v[116:119], v[204:207], v[132:135]\n\t" \
  "v_mfma_f32_16x16x32_bf16 v[140:143], v[124:127], v[204:207], v[140:143]\n\t" \
  "s_nop 6\n\t" \
  "v_pk_add_f32 v[128:129], v[128:129], v[132:133]\n\t" \
  "v_pk_add_f32 v[130:131], v[130:131], v[134:135]\n\t" \
  "ds_write_b128 v230, v[128:131] offset:0\n\t" \
  "v_pk_add_f32 v[136:137], v[136:137], v[140:141]\n\t" \
  "v_pk_add_f32 v[138:139], v[138:139], v[142:143]\n\t" \
  "ds_write_b128 v230, v[136:139] offset:1024\n\t" \
  "s_waitcnt vmcnt(0) lgkmcnt(0)\n\t" \
  "s_branch .Lend%=\n\t" \
  ".Ltw%=:\n\t" \
  "s_add_u32 s92, s92, 1\n\t" \
  "s_sleep 2\n\t" \
  "s_and_b32 s93, s92, 63\n\t" \
  "s_cbranch_scc1 .Ltr%=\n\t" \
  "buffer_load_dword v164, off, %[rs], %[sab] sc1\n\t" \
  "s_waitcnt vmcnt(0)\n\t" \
  "v_readfirstlane_b32 s93, v164\n\t" \
  "s_nop 3\n\t" \
  "s_cmp_eq_u32 s93, 1\n\t" \
  "s_cbranch_scc1 .Lto%=\n\t" \
  "s_cmp_lt_u32 s92, 0x80000\n\t" \
  "s_cbranch_scc1 .Ltr%=\n\t" \
  "v_mov_b32 v164, 1\n\t" \
  "buffer_store_dword v164, off, %[rs], %[sab] sc1\n\t" \
  "s_waitcnt vmcnt(0)\n\t" \
  "s_branch .Lto%=\n\t" \
  ".Ltr%=:\n\t" \
  "buffer_load_dword v166, v242, %[rs], 0 offen sc1\n\t" \
  "s_waitcnt vmcnt(0)\n\t" \
  "s_branch .Ltc%=\n\t" \
  ".Llast%=:\n\t" \
  "v_mov_b32 v173, v242\n\t" \
  "s_branch .Lgol%=\n\t" \
  ".Lfw%=:\n\t" \
  "s_add_u32 s92, s92, 1\n\t" \
  "s_sleep 2\n\t" \
  "s_and_b32 s93, s92, 63\n\t" \
  "s_cbranch_scc1 .Lfr%=\n\t" \
  "buffer_load_dword v164, off, %[rs], %[sab] sc1\n\t" \
  "s_waitcnt vmcnt(0)\n\t" \
  "v_readfirstlane_b32 s93, v164\n\t" \
  "s_nop 3\n\t" \
  "s_cmp_eq_u32 s93, 1\n\t" \
  "s_cbranch_scc1 .Lfo%=\n\t" \
  "s_cmp_lt_u32 s92, 0x80000\n\t" \
  "s_cbranch_scc1 .Lfr%=\n\t" \
  "v_mov_b32 v164, 1\n\t" \
  "buffer_store_dword v164, off, %[rs], %[sab] sc1\n\t" \
  "s_waitcnt vmcnt(0)\n\t" \
  "s_branch .Lfo%=\n\t" \
  ".Lfr%=:\n\t" \
  "buffer_load_dwordx2 v[166:167], v173, %[rs], 0 offen sc1\n\t" \
  "s_branch .Lfc%=\n\t" \
  ".Lb1%=:\n\t" \
  "s_waitcnt vmcnt(0) lgkmcnt(0)\n\t" \
  "s_barrier\n\t" \
  "s_branch .Lb2%=\n\t" \
  ".Lbl%=:\n\t" \
  "s_bitset1_b64 %[ps], %[step]\n\t" \
  "s_cmp_lt_u32 s92, 3\n\t" \
  "s_cbranch_scc1 .Lbm%=\n\t" \
  "s_sleep 12\n\t" \
  ".Lbm%=:\n\t" \
  "s_add_u32 s92, s92, 1\n\t" \
  "s_sleep 2\n\t" \
  "s_and_b32 s93, s92, 63\n\t" \
  "s_cbranch_scc1 .Lba%=\n\t" \
  "buffer_load_dword v164, off, %[rs], %[sab] sc1\n\t" \
  "s_waitcnt vmcnt(0)\n\t" \
  "v_readfirstlane_b32 s93, v164\n\t" \
  "s_nop 3\n\t" \
  "s_cmp_eq_u32 s93, 1\n\t" \
  "s_cbranch_scc1 .Lgo%=\n\t" \
  "s_cmp_lt_u32 s92, 0x80000\n\t" \
  "s_cbranch_scc1 .Lba%=\n\t" \
  "v_mov_b32 v164, 1\n\t" \
  "buffer_store_dword v164, off, %[rs], %[sab] sc1\n\t" \
  "s_waitcnt vmcnt(0)\n\t" \
  "s_branch .Lgo%=\n\t" \
  ".Lba%=:\n\t" \
  "buffer_load_dwordx2 v[166:167], v173, %[rs], 0 offen sc1\n\t" \
  "s_branch .Lcal%=\n\t" \
  ".Lslow%=:\n\t" \
  "s_mov_b64 s[94:95], 0\n\t" \
  "v_cmp_eq_u32 vcc, %[sent], v128\n\t" \
  "s_or_b64 s[94:95], s[94:95], vcc\n\t" \
  "v_cmp_eq_u32 vcc, %[sent], v129\n\t" \
  "s_or_b64 s[94:95], s[94:95], vcc\n\t" \
  "v_cmp_eq_u32 vcc, %[sent], v130\n\t" \
  "s_or_b64 s[94:95], s[94:95], vcc\n\t" \
  "v_cmp_eq_u32 vcc, %[sent], v131\n\t" \
  "s_or_b64 s[94:95], s[94:95], vcc\n\t" \
  "v_cmp_eq_u32 vcc, %[sent], v132\n\t" \
  "s_or_b64 s[94:95], s[94:95], vcc\n\t" \
  "v_cmp_eq_u32 vcc, %[sent], v133\n\t" \
  "s_or_b64 s[94:95], s[94:95], vcc\n\t" \
  "v_cmp_eq_u32 vcc, %[sent], v134\n\t" \
  "s_or_b64 s[94:95], s[94:95], vcc\n\t" \
  "v_cmp_eq_u32 vcc, %[sent], v135\n\t" \
  "s_or_b64 s[94:95], s[94:95], vcc\n\t" \
  "v_cmp_eq_u32 vcc, %[sent], v136\n\t" \
  "s_or_b64 s[94:95], s[94:95], vcc\n\t" \
  "v_cmp_eq_u32 vcc, %[sent], v137\n\t" \
  "s_or_b64 s[94:95], s[94:95], vcc\n\t" \
  "v_cmp_eq_u32 vcc, %[sent], v138\n\t" \
  "s_or_b64 s[94:95], s[94:95], vcc\n\t" \
  "v_cmp_eq_u32 vcc, %[sent], v139\n\t" \
  "s_or_b64 s[94:95], s[94:95], vcc\n\t" \
  "v_cmp_eq_u32 vcc, %[sent], v140\n\t" \
  "s_or_b64 s[94:95], s[94:95], vcc\n\t" \
  "v_cmp_eq_u32 vcc, %[sent], v141\n\t" \
  "s_or_b64 s[94:95], s[94:95], vcc\n\t" \
  "v_cmp_eq_u32 vcc, %[sent], v142\n\t" \
  "s_or_b64 s[94:95], s[94:95], vcc\n\t" \
  "v_cmp_eq_u32 vcc, %[sent], v143\n\t" \
  "s_or_b64 s[94:95], s[94:95], vcc\n\t" \
  "s_cbranch_scc0 .Lok%=\n\t" \
  "s_bitset1_b64 %[fm], %[step]\n\t" \
  "s_add_u32 s92, s92, 1\n\t" \
  "s_sleep 1\n\t" \
  "s_and_b32 s93, s92, 63\n\t" \
  "s_cbranch_scc1 .Lre%=\n\t" \
  "buffer_load_dword v164, off, %[rs], %[sab] sc1\n\t" \
  "s_waitcnt vmcnt(0)\n\t" \
  "v_readfirstlane_b32 s93, v164\n\t" \
  "s_nop 3\n\t" \
  "s_cmp_eq_u32 s93, 1\n\t" \
  "s_cbranch_scc1 .Lok%=\n\t" \
  "s_cmp_lt_u32 s92, 0x80000\n\t" \
  "s_cbranch_scc1 .Lre%=\n\t" \
  "v_mov_b32 v164, 1\n\t" \
  "buffer_store_dword v164, off, %[rs], %[sab] sc1\n\t" \
  "s_waitcnt vmcnt(0)\n\t" \
  "s_branch .Lok%=\n\t" \
  ".Lre%=:\n\t" \
  "buffer_load_dwordx4 v[128:131], v234, %[rs], %[so] offen sc1\n\t" \
  "s_nop 15\n\t" \
  "buffer_load_dwordx4 v[132:135], v235, %[rs], %[so] offen sc1\n\t" \
  "s_nop 15\n\t" \
  "buffer_load_dwordx4 v[136:139], v236, %[rs], %[so] offen sc1\n\t" \
  "s_nop 15\n\t" \
  "buffer_load_dwordx4 v[140:143], v237, %[rs], %[so] offen sc1\n\t" \
  "v_mov_b64 v[144:145], 0\n\t" \
  "v_mov_b64 v[146:147], 0\n\t" \
  "v_mov_b64 v[148:149], 0\n\t" \
  "v_mov_b64 v[150:151], 0\n\t" \
  "v_mov_b64 v[152:153], 0\n\t" \
  "v_mov_b64 v[154:155], 0\n\t" \
  "v_mov_b64 v[156:157], 0\n\t" \
  "v_mov_b64 v[158:159], 0\n\t" \
  "s_waitcnt vmcnt(0)\n\t" \
  "s_branch .Lw%=\n\t" \
  ".Lend%=:\n\t"


__device__ __forceinline__ int look_prog(__amdgpu_buffer_rsrc_t rs, int off) {
  asm volatile("" ::: "memory");
  const int v = (int)ld_flag(rs, off);
  return min(__builtin_amdgcn_readlane(v, 0), __builtin_amdgcn_readlane(v, 1));
}
__device__ __forceinline__ int wait_prog(__amdgpu_buffer_rsrc_t rs, int off, int need, gu32* abortw) {
  int v;
  for (unsigned spins = 0;;) {
    v = look_prog(rs, off);
    if (v >= need) break;
    __builtin_amdgcn_s_sleep(4);
    if (spin_fail(spins, abortw)) break;
  }
  asm volatile("" ::: "memory");
  return v;
}
#define CLB_S "s90", "s91", "s92", "s93", "s94", "s95", "vcc", "scc", "memory"
#define W_IN "{v[0:31]}"(W[0]), "{v[32:63]}"(W[1]), "{v[64:95]}"(W[2]), "{v[96:127]}"(W[3]), "{v[128:159]}"(W[4]), "{v[160:191]}"(W[5])

#define LEAD0 408
#define LEAD_N0 5
#define LEAD_SH 2
#define LEAD_X0 8
#define LEAD_U0 24
#define LEAD_D0 0
#define T0_BACK 1000
#define QBJ 3
constexpr int L_MEM = 0, L_KT = 16384, L_QX = 24576, L_KX = 26624, L_VX = 16384, L_MISC = 30720, L_WL = 32768;

#define QK_STEP(TXT)                                                                                                        \
  asm volatile(TXT                                                                                                          \
      : [tout] "=&s"(tout), [spins] "=&s"(spins), [tb1] "=&s"(tb1), [tgt] "=&s"(tgt), "+{v248}"(aL16), "+{v249}"(aMW), "+{v250}"(aWL),                            \
        "+{v251}"(aIDX), "+{v252}"(aQW), "+{v[232:235]}"(QF)                                                                 \
      : W_IN, [rs] "s"(rs4), [som] "s"(o_mem(pm, s) + WV * 1024), [som2] "s"(o_mem(pm, s) + WV * 1024 + 16384), [sent] "s"(DSENT), [sab] "s"(SY_ABORT), \
        [lead] "s"(lead), [step] "s"(s), [sbc] "s"(SY_BA + side(pm, 1) + h * 128), [nh] "s"(0xFF00FF00FF00FF00ull), [up] "s"(0xFFFFFFFF00000000ull), [negc] "s"(-cexp),      \
        [c] "s"(cexp), [so] "s"(o_o(pm, s, h) + WV * 512), [sk] "s"(o_v(pm, s, h) + 2048 + (WV & 3) * 512),                    \
        [sv] "s"(o_v(pm, s, h) + WV * 512), [rt] "s"(rt4), [svt] "s"(t_off(p, T_V + kseg(s, h)) + WV * 2048)                  \
      : CLB8(192, 193, 194, 195, 196, 197, 198, 199), CLB8(200, 201, 202, 203, 204, 205, 206, 207),                          \
        CLB8(208, 209, 210, 211, 212, 213, 214, 215), CLB8(216, 217, 218, 219, 220, 221, 222, 223),                          \
        CLB8(224, 225, 226, 227, 228, 229, 230, 231), CLB8(236, 237, 238, 239, 240, 241, 242, 243), "v244", "v245", "v246", "v247", "v253", "s88", "s89", CLB_S)

template <int WV>
__device__ __forceinline__ void qk_wave(Args& p, int h, char* smem) {
  const int tid = threadIdx.x;
  unsigned pm = 0;
  int have = -1;
  u32x32 W[6];
  {
    int lane = tid & 63;
    asm volatile("" : "+v"(lane));
    const unsigned vo = (WV * 4 + (lane >> 4)) * (HD * 2) + 16 * ((lane & 15) ^ swzB((WV * 4 + (lane >> 4)) & 15));
    auto stage = [&](int j) {
#pragma unroll
      for (int mm = 0; mm < 2; ++mm) {
        const char* src = (const char*)((mm ? p.Wk : p.Wq) + (size_t)j * 128 * HD + h * DH);
        const unsigned dst = lds_off(smem) + (~j & 1) * 65536 + mm * 32768 + WV * 1024;
        glds2(src, 32 * HD * 2, vo, dst);
        if (TH_Q < 63) HOLD_V((h & 3) ^ 1, TH_Q);
        glds2(src + 64 * HD * 2, 32 * HD * 2, vo, dst + 16384);
        if (TH_Q < 63) HOLD_V((h & 3) ^ 1, TH_Q);
      }
    };
    bf16x8 keep[4][4];
    int f0 = -1;
    auto place = [&](int g) {
#pragma unroll
      for (int i = 0; i < 4; ++i)
        *reinterpret_cast<bf16x8*>(smem + (g == 0 ? 65536 : g == 1 ? 32768 : g == 2 ? 131072 : 98304) + WV * 4096 + i * 1024 + lane * 16) = keep[g][i];
    };
    stage(0); stage(1);
    Boot bt;
#pragma unroll
    for (int j = 0; j < 8; ++j) {
      if (j < 7) WAIT_V(8); else WAIT_V(0);
      if (WV == 7 && j == QBJ + 1) pm = boot_done(p.ws, bt);
      if ((WV == 4 || WV == 5) && j == 7) {
        asm volatile("" : "+v"(f0));
        have = min(__builtin_amdgcn_readlane(f0, 0), __builtin_amdgcn_readlane(f0, 1));
      }
      LBAR();
#pragma unroll
      for (int k = 0; k < 8; ++k) {
        const int P = (QK_MAP[WV] >> (4 * (2 * j + (k >> 2)))) & 15;
        const bf16x8 fr = ld_bfrag(smem + (~j & 1) * 65536 + (WV >> 2) * 32768 + (k >> 2) * 16384, k & 1, 2 * (WV & 3) + ((k >> 1) & 1), lane);
        const int f = P * 4 + (k & 3);
        if (P < 12) put4(W[f >> 3], f & 7, fr);
        else keep[P - 12][k & 3] = fr;
      }
      if (j == (WV >> 1)) place(2);
      LBAR();
      if (WV == 7 && j == QBJ) boot_ask(p.ws, bt);
      if (j + 2 < 8) stage(j + 2);
      if (j == 6) {
        if (WV == 4 || WV == 5)
          asm volatile("buffer_load_dword %0, %1, %2, 0 offen sc1" : "=&v"(f0) : "v"(fkq(h, (lane & 1) * 2)), "s"(raw_rsrc(p.ws + WS_SY, SY_TOTAL)) : "memory");
        place(0); place(3);
      }
    }
    place(1);
    if (WV == 7 && lane < 8) lds_at(smem + L_MISC)[lane] = lane == 7 ? pm : (lane == 1 ? now_q() - T0_BACK : 0);
    if (WV == 6) lds_at(smem + 28672)[lane] = ~0u;
    LBAR();
    pm = uni(lds_at(smem + L_MISC)[7]);
    p.s0 = pm >> 31;
    if (WV == 4 || WV == 5) {
      if (have < 0) have = wait_prog(mkrsrc(p.ws + WS_SY, SY_TOTAL), fkq(h, (lane & 1) * 2), 0, (gu32*)(p.ws + WS_SY + SY_ABORT));
      const char* src = p.ws + WS_T + t_off(p, kseg(0, h)) + (WV - 4) * 4096 + lane * 16;
#pragma unroll
      for (int i = 0; i < 4; ++i) glds16_sc1(src + i * 1024, smem + L_KT + (WV - 4) * 4096 + i * 1024);
    }
  }
  const __amdgpu_buffer_rsrc_t rsy = mkrsrc(p.ws + WS_SY, SY_TOTAL);
  const u32x4 rs4 = raw_rsrc(p.ws + WS_SY, SY_TOTAL);
  const u32x4 rt4 = raw_rsrc(p.ws + WS_T, T_BYTES);
  gu32* abortw = (gu32*)(p.ws + WS_SY + SY_ABORT);
  const float cexp = 0.08838834764831845f * 1.4426950408889634f;
  constexpr int m = WV >> 2, b = WV & 3;
  int lead = LEAD0;
  unsigned tb1 = 0;
  unsigned long long fm = 0, ps = 0; (void)fm; (void)ps;
  for (int s = 0; s < NSEG; ++s) {
    int lane = tid & 63;
    asm volatile("" : "+v"(lane));
    const int r = lane & 15, q = lane >> 4;
    unsigned aL16 = lane * 16, aMW = aL16 + WV * 1024, aWL = aL16 + 98304 + WV * 4096;
    unsigned aIDX = (lane & 2) ? 0xEEEE : 0x4444;
    unsigned aQW = (32 * b + r) * 16 + q * 4 + (m ? L_KX : L_QX);
    u32x4 QF;
    asm volatile("" : "={v[232:235]}"(QF));
    if (WV < 4) {
      QF[0] = L_QX + (q * 8 + (r >> 2)) * 16 + (r & 1) * 8;
      QF[1] = ((2 * (q & 1) + (q >> 1)) * 16 + r) * 4;
      QF[2] = ((r >> 2) & 1) * 65536 + ((r >> 1) & 1) * 64 + (r >> 3) * 32 + (r & 1) * 16 + (q >> 1) * 256 + (q & 1) * 8;
    } else QF[3] = r * 16 + q * 4;
    unsigned tout, spins, tgt;
    if (WV == 0) QK_STEP(QK_ASM_0); else if (WV == 1) QK_STEP(QK_ASM_1); else if (WV == 2) QK_STEP(QK_ASM_2);
    else if (WV == 3) QK_STEP(QK_ASM_3); else if (WV == 4) QK_STEP(QK_ASM_4); else if (WV == 5) QK_STEP(QK_ASM_5);
    else if (WV == 6) QK_STEP(QK_ASM_6); else QK_STEP(QK_ASM_7);
    tout = __builtin_amdgcn_readfirstlane(tout);
    tb1 = __builtin_amdgcn_readfirstlane(tb1);
    lane = tid & 63;
    asm volatile("" : "+v"(lane));
    if (WV < 4) {
      lds_at(smem + L_MISC + 8)[WV] = s + 1;
      if (WV == 3) { lds_at(smem + L_MISC)[6] = time_of(tout); lds_at(smem + 28672)[2] = s; }
    }
    if (WV == 6) {
      if (s + 1 < NSEG) watch(rsy, s, lane, now_q(), (const unsigned*)(smem + L_MISC), abortw, side(pm, 1));
    } else if (WV >= 4 && s + 1 < NSEG) {
      unsigned t;
      do {
        __builtin_amdgcn_s_sleep(1);
        t = lds_at(smem + L_MISC + 8)[lane & 3];
      } while (!__all(t == (unsigned)(s + 1)));
      if (WV <= 5) {
        if (s + 2 < NSEG) {
          if (have < s + 1) have = wait_prog(rsy, fkq(h, (lane & 1) * 2), s + 1, abortw);
          const char* src = p.ws + WS_T + t_off(p, kseg(s + 1, h)) + (WV - 4) * 4096 + lane * 16;
#pragma unroll
          for (int i = 0; i < 4; ++i) glds16_sc1(src + i * 1024, smem + L_KT + (WV - 4) * 4096 + i * 1024);
          if (have < s + 2) have = look_prog(rsy, fkq(h, (lane & 1) * 2));
        }
      } else {
        do __builtin_amdgcn_s_sleep(2); while (__builtin_amdgcn_readfirstlane(lds_at(smem + 28672)[2]) != (unsigned)s);
        const int x = (int)(__builtin_amdgcn_readfirstlane(lds_at(smem + L_MISC)[6]) - tb1);
        lead = __builtin_amdgcn_readfirstlane(track(lead, x + (s < LEAD_N0 ? LEAD_X0 : 0), s < LEAD_N0 ? LEAD_U0 : LEAD_UP, s < LEAD_N0 ? LEAD_D0 : (s < 3 * LEAD_N0 ? min(max(LEAD_DN, (lead - x) >> LEAD_SH), 16) : LEAD_DN), 60));
      }
    }
  }
}

__device__ __forceinline__ void qk_role(Args& p, int h, char* smem) {
  switch (__builtin_amdgcn_readfirstlane(threadIdx.x >> 6)) {
    case 0: qk_wave<0>(p, h, smem); break;
    case 1: qk_wave<1>(p, h, smem); break;
    case 2: qk_wave<2>(p, h, smem); break;
    case 3: qk_wave<3>(p, h, smem); break;
    case 4: qk_wave<4>(p, h, smem); break;
    case 5: qk_wave<5>(p, h, smem); break;
    case 6: qk_wave<6>(p, h, smem); break;
    default: qk_wave<7>(p, h, smem); break;
  }
}

#define K_DUE 50
#define K_DUE2 500
#define T_SL 2
#define O_DUE 400

constexpr int V_VM = 16384, V_T = 32768, V_KI = 131072;
#define TK_CALL(sg, od_) \
  asm volatile(TK_ASM \
            : \
            : "{v200}"(lane * 16), "{v[208:215]}"(bias), "{v[216:219]}"(ta), \
              [rs] "s"(rs4), [rt] "s"(raw_rsrc(p.ws + WS_T, T_BYTES)), [sent] "s"(DSENT), [sab] "s"(SY_ABORT), [sk] "s"(o_v(pm, sg, h) + 2048), \
              [tl] "s"(V_T + (sg & 3) * 24576), [vp] "s"((sg & 1) * 2048), [tq] "s"((w & 1) * 4096), [tv] "s"((w & 2) * 2048), \
              [so] "s"(t_off_o(p, T_O + (sg * 2 + (w & 1)) * 16 + (h >> 1)) + (h & 1) * 4096 + (w & 2) * 1024), \
              [up] "s"(0xFFFFFFFF00000000ull), [c] "s"(1.4426950408889634f), [negc] "s"(-1.4426950408889634f), \
              [sc] "s"((unsigned long long)__float_as_uint(0.08838834764831845f)), [od] "s"(od_) \
            : CLB32(128), CLB32(160), CLB8(192, 193, 194, 195, 196, 197, 198, 199), CLB8(224, 225, 226, 227, 228, 229, 230, 231), \
              "v205", "v206", "v207", "s88", "s89", CLB_S)
__device__ __forceinline__ void vu_role(Args& p, int h, char* smem) {
  const int tid = threadIdx.x, lane = tid & 63;
  const int w = __builtin_amdgcn_readfirstlane(tid >> 6);
  u32x32 W[4];
  {
    const unsigned vo = (tid >> 4) * (HD * 2) + 16 * ((lane & 15) ^ swzB((tid >> 4) & 15));
    auto stage = [&](int j) {
      const char* src = (const char*)(p.Wv + (size_t)j * 128 * HD + h * DH);
      const unsigned dst = lds_off(smem) + L_WL + (j & 3) * 32768 + w * 1024;
      glds2(src, 32 * HD * 2, vo, dst);
      glds2(src + 64 * HD * 2, 32 * HD * 2, vo, dst + 16384);
    };
    start_sy(p.ws, p.mem0, h, smem);
    Boot bt;
#pragma unroll
    for (int u = 0; u < 2; ++u) {
#pragma unroll
      for (int j = 0; j < 4; ++j) stage(4 * u + j);
      if (u && w == 7) boot_ask(p.ws, bt);
      WAIT_V(0);
      if (u && w == 7) {
        const unsigned m = boot_done(p.ws, bt);
        if (lane < 8) lds_at(smem + L_MISC)[lane] = lane == 7 ? m : (lane == 1 ? now_q() : 0);
      }
      LBAR();
#pragma unroll
      for (int k = 0; k < 16; ++k) put4(W[2 * u + (k >> 3)], k & 7, ld_bfrag(smem + L_WL + ((k >> 1) ^ w) * 16384, k & 1, w, lane));
      LBAR();
    }
  }
  const __amdgpu_buffer_rsrc_t rsy = mkrsrc(p.ws + WS_SY, SY_TOTAL);
  const u32x4 rs4 = raw_rsrc(p.ws + WS_SY, SY_TOTAL);
  gu32* abortw = (gu32*)(p.ws + WS_SY + SY_ABORT);
  const unsigned pm = uni(lds_at(smem + L_MISC)[7]);
  p.s0 = pm >> 31;
  unsigned long long fm = 0; (void)fm;
  int have = -1;
  auto fetch = [&](int sg) {
    if (have < sg) have = wait_prog(rsy, fkq(h, w - 4), sg, abortw);
    const char* src = p.ws + WS_T + t_off(p, (w - 4) * T_Q + kseg(sg, h)) + lane * 16;
#pragma unroll
    for (int i = 0; i < 8; ++i) glds16_sc1(src + i * 1024, smem + V_T + (sg & 3) * 24576 + (w - 4) * 8192 + i * 1024);
  };
  const int fo = SY_FO + ((w & 3) * NH + h) * 128;
  typedef __attribute__((ext_vector_type(8))) float f32x8;
  f32x8 bias;
  typedef __attribute__((ext_vector_type(8))) unsigned u32x8;
  u32x8 ca;
#pragma unroll
  for (int i = 0; i < 8; ++i) ca[i] = lane * 16 + (i ^ w) * 1024;
  u32x4 ta;
  {
    const int r = lane & 15, q = lane >> 4;
#pragma unroll
    for (int j = 0; j < 8; ++j) bias[j] = (j >> 2) * 16 + 4 * q + (j & 3) <= 16 * (w & 1) + r ? INFINITY : -INFINITY;
    ta[0] = V_KI + w * 2048 + lane * 16;
    ta[1] = V_KI + w * 2048 + (q * 8 + (r >> 2)) * 16 + (r & 1) * 8;
    ta[2] = V_VM + (w & 2) * 512 + ((2 * (q & 1) + (q >> 1)) * 16 + r) * 4;
    ta[3] = 0;
  }
#pragma unroll 1
  for (int s = 0; s < NSEG; ++s) {
    unsigned spins, tgt;
    asm volatile(VV_ASM
        : [spins] "=&s"(spins), [tgt] "=&s"(tgt)
        : W4_IN, "{v200}"(lane * 16), "{v[232:239]}"(ca), "{v202}"((lane & 2) ? 0xEEEE : 0x4444), "{v203}"(lane * 4),
          "{v204}"(tid * 4 + V_VM + (s & 1) * 2048), [rs] "s"(rs4), [som] "s"(o_mem(pm, s) + w * 1024), [som2] "s"(o_mem(pm, s) + w * 1024 + 16384), [step] "s"(s),
          [sent] "s"(DSENT), [sab] "s"(SY_ABORT), [sv] "s"(o_v(pm, s, h) + w * 256)
        : CLB32(128), CLB32(160), CLB8(192, 193, 194, 195, 196, 197, 198, 199), CLB_S);
    if (w < 4 || s == NSEG - 1) {
      const int sg = s - (w < 4);
      if (w >= 4) {
        if (lane == 0) __builtin_amdgcn_raw_buffer_store_b32(1u, rsy, fo + 4 * NH * 128, 0, 16);
        pace_until(now_q() + K_DUE);
      }
      if (sg >= 0 && sg != NSEG - 2) {
        TK_CALL(sg, s < NSEG - 1 ? O_DUE : 0);
        if (sg >= NSEG - 3) {
          static_assert(TLAG == 2, "");
          WAIT_V(0);
          if (lane == 0) __builtin_amdgcn_raw_buffer_store_b32(1u, rsy, fo + (sg - NSEG + 3) * 4 * NH * 128, 0, 16);
        }
      }
    } else if (w == 7) {
      watch(rsy, s, lane, now_q(), (const unsigned*)(smem + L_MISC), abortw, side(pm, 1));
    } else if (w >= 4) {
      __builtin_amdgcn_s_sleep(T_SL);
      const int s1 = w == 5 && s < NSEG - 3 ? s : s + 1;
#pragma unroll 1
      for (int sg = (w == 5 ? s < NSEG - 2 : s == 0) ? s : s + 1; sg <= s1; ++sg) fetch(sg);
      if (have < s1 + 1 && s1 + 1 < NSEG) have = look_prog(rsy, fkq(h, w - 4));
      if (w == 5 && s + NPRE < NSEG) {
#pragma unroll
        for (int i = 0; i < 4; ++i) st_sc1(rsy, o_v(pm, s + NPRE, h) + i * 1024 + lane * 16, (u32x4){DSENT, DSENT, DSENT, DSENT});
      }
    }
    if (s == NSEG - 2 && w >= 4) {
      pace_until(tgt + K_DUE2);
      TK_CALL(s, 0);
    }
  }
}

#define RL_UP 6
#define RL_DN 4
#define RED_MIN 40
#define BA_LOOK 360
constexpr int R_SI = 32768, R_PART = 98304;

__device__ __forceinline__ void red_role(Args& p, int j, char* smem) {
  const int tid = threadIdx.x;
  int lane = tid & 63;
  const int w = __builtin_amdgcn_readfirstlane(tid >> 6);
  const int c = (j & 2) * 8 + (j >> 2), rh = j & 1;
  const int hg = (w + (j >> 2)) & 7, rot = (j >> 2) + (j >> 5);
  const __amdgpu_buffer_rsrc_t rsy = mkrsrc(p.ws + WS_SY, SY_TOTAL);
  const u32x4 rs4 = raw_rsrc(p.ws + WS_SY, SY_TOTAL);
  const __amdgpu_buffer_rsrc_t rt = mkrsrc(p.ws + WS_T, T_BYTES);
  gu32* abortw = (gu32*)(p.ws + WS_SY + SY_ABORT);
  u32x32 W[4];
  u32x2 tag = {0u, 0u};
  {
    const u16* Wo = p.Wo + (size_t)(4 * hg * DH) * DM + 32 * c;
    const int ro = (lane >> 2) * 64 + (lane & 3) * 8;
    Boot bt;
    load_blocks<16>(smem + 32768 + w * 16384,
        [&](int i, char* slot) {
          if (i == 12 && w == 0) boot_ask(p.ws, bt);
          if ((i < 2 || i == 4 || i == 8) && (c & 14) != 6 && lane < 10) {
            const int x = i < 2 ? 3 - i : i >> 3, n = min(((c & 15) - ((c & 8) >> 2)) * 10 + lane, 127);
            touch_line((const char*)p.Wo + (size_t)(w * 512 + (x >> 1) * 256 + (x & 1) * 64 + (n & 64) + n) * (DM * 2) + (c & 16) * 64 + 384, smem + w * 256);
          }
          const int x = ((i >> 2) + rot) & 3;
          blk_dma(Wo + (size_t)((x >> 1) * 256 + (x & 1) * 64 + ((i >> 1) & 1) * 128 + (i & 1) * 32) * DM, DM, slot, lane);
        },
        [&](int i, char* slot) {
#pragma unroll
          for (int t = 0; t < 2; ++t) {
            s16x4 lo = __builtin_amdgcn_ds_read_tr16_b64_v4i16((lds_s16x4_ptr)(slot + ro + t * 32));
            s16x4 hi = __builtin_amdgcn_ds_read_tr16_b64_v4i16((lds_s16x4_ptr)(slot + ro + t * 32 + 1024));
            put4(W[i >> 2], (i & 2) * 2 + 2 * t + (i & 1), (bf16x8){lo[0], lo[1], lo[2], lo[3], hi[0], hi[1], hi[2], hi[3]});
          }
        });
    if (w == 0) { lds_at(smem + 4128)[0] = boot_done(p.ws, bt); tag = next_tag(bt.e); }
    LBAR();
  }
  const unsigned pm = uni(lds_at(smem + 4128)[0]);
  p.s0 = pm >> 31;
  const int hs = side(pm, 1);
  {
    const u32x4 ds = {DSENT, DSENT, DSENT, DSENT};
#pragma unroll
    for (int i = 0; i < 2; ++i) {
      const int s = (w * 2 + i) * 4 + (lane >> 4);
      if (s >= 2) st_sc1(rsy, o_mem(pm, s) + (c >> 4) * 16384 + (c & 15) * 512 + rh * 128 + ((lane >> 3) & 1) * 256 + (lane & 7) * 16, ds);
    }
  }
  if (tid < 4) { reinterpret_cast<unsigned*>(smem + 4096)[tid] = tid == 3 ? now_q() : 0; reinterpret_cast<unsigned*>(smem + 4112)[tid] = DSENT; }
  if (tid < 256) reinterpret_cast<int*>(smem + 5120)[tid] = ((tid >> 2) - TLAG - 6) >> 2;
  LBAR();
  const unsigned base = __builtin_amdgcn_readfirstlane(lds_at(smem + 4096)[3]);
  unsigned long long fm = 0, ps = 0;
  const unsigned aL = lane * 16;
  const int e = ((56 + w) * SEG + rh * 16 + (lane >> 2)) * DM + c * 32 + (lane & 3) * 8;
  u32x4 xv = *reinterpret_cast<const u32x4*>(p.x + e), av = {DSENT, DSENT, DSENT, DSENT};
  asm volatile("" : "+v"(xv));
  {
    const int r = lane & 15, q = lane >> 4;
    const int sm0 = o_mem(pm, 0) + (c >> 4) * 16384 + (c & 15) * 512 + rh * 128;
    u32x4 VO, VT;
    unsigned fo = SY_FO + ((rh + (lane & 4) / 2) * NH + 4 * hg + (lane & 3)) * 128;
    unsigned ao = w == 1 ? (c * S_LEN + rh * 16 - (TLAG + 3) * SEG) * 64 + lane * 16
                         : (w >= 4 ? SY_O + NPRE * 131072 + side(pm, w - 2) + (j >> 3) * 16384 + (j & 7) * 1024 + lane * 16
                                   : SY_FS + (2 * c + rh) * 128 + (lane < 8 ? lane * 16 : 0x70000000));
    unsigned ar = w == 1 ? R_SI - (TLAG + 2) * 1024 + lane * 16 : (w == 2 ? 5120 : 4112);
    const unsigned long long am = w == 1 ? ((1ull << 56) - 1) << (TLAG + 3) : (w == 2 ? 0x11111111111111ull << (TLAG + 7) : (w >= 4 ? (0xFFFFull << (16 * (w - 4))) >> NPRE : 0));
    unsigned pw = R_PART + w * 2048 + lane * 16, pr = R_PART + 16384 + tid * 4;
    unsigned sw = R_SI - (TLAG + 1) * 1024 + (lane >> 2) * 64 + w * 8 + (lane & 3) * 2, stm = 8192 + w * 512;
#pragma unroll
    for (int l = 0; l < 4; ++l) {
      const int x = (l + rot) & 3;
      VO[l] = o_o(0, 0, 4 * hg) + rh * 65536 + x * 1024 + aL;
      VT[l] = (T_O + rh * 16 + 2 * hg + (x >> 1)) * 16384 + (x & 1) * 2048 + aL;
    }
    unsigned ba = lane < 4 ? SY_BA + hs + (4 * hg + lane) * 128 : 0x7F000000u;
    unsigned sm = 0;
    int rl = RL0;
    unsigned step, E, so, st, st4, sh;
    asm volatile(RED_ASM
        : [step] "=&s"(step), [E] "=&s"(E), [so] "=&s"(so), [st] "=&s"(st), [st4] "=&s"(st4), [sh] "=&s"(sh),
          [rl] "+s"(rl), [sm] "+s"(sm), [fm] "+s"(fm), [ps] "+s"(ps),
          "+{v[224:227]}"(av), "+{v228}"(ao), "+{v229}"(ar), "+{v230}"(pw), "+{v231}"(pr), "+{v232}"(sw), "+{v233}"(stm), "+{v242}"(fo), "+{v173}"(ba)
        : W4_IN, "{v168}"(aL), "{v169}"(w * 512 + lane * 8), "{v170}"(lane * 8), "{v174}"((r & 1) ? 0x03020706u : 0x05040100u),
          "{v171}"(sm0 + 32768 + (q >> 1) * 256 + (q & 1) * 64 + ((r >> 1) & 1) * 32 + (r & 1) * 16 + (r >> 2) * 4),
          "{v172}"((lane & 2) ? 0xEEEE : 0x4444),
          "{v175}"(w == 3 && lane < 32 ? SY_BR + hs + j * 128 + lane * 4 : 0x7F000000u), "{v[234:237]}"(VO), "{v[238:241]}"(VT), "{v243}"(e * 2),
          [rg] "s"(raw_rsrc(p.ws + WS_G, S_LEN * DM * 2)), [fg] "s"(SY_FG + ((14 + (w >> 2)) * 8 + (c >> 2)) * 128),
          [rs] "s"(rs4), [ra] "s"(w == 1 ? raw_rsrc(p.ws + WS_S, S_LEN * DM * 2) : rs4), [rt] "s"(raw_rsrc(p.ws + WS_T, T_BYTES)),
          [ds] "n"(DS_RED), [wm] "n"(REF_W), [gm] "n"(REF_G), [mn] "n"(RED_MIN), [lk] "n"(DS_MEM + BA_LOOK), [up] "n"(RL_UP), [dn] "n"(-RL_DN), [sent] "s"(DSENT), [sab] "s"(SY_ABORT),
          [base] "s"(base), [pm] "s"(pm), [s0] "s"((p.s0 ^ TF_O) << 13), [am] "s"(am), [ainc] "s"(w == 1 ? SEG * 64 : (w >= 4 ? 131072 : 0)),
          [rinc] "s"(w == 1 ? 1024 : (w == 2 ? 16 : 0))
        : CLB32(128), CLB8(160, 161, 162, 163, 164, 165, 166, 167), CLB8(176, 177, 178, 179, 180, 181, 182, 183),
          CLB8(184, 185, 186, 187, 188, 189, 190, 191), CLB8(192, 193, 194, 195, 196, 197, 198, 199), CLB8(200, 201, 202, 203, 204, 205, 206, 207),
          CLB8(208, 209, 210, 211, 212, 213, 214, 215), CLB8(216, 217, 218, 219, 220, 221, 222, 223), CLB_E, "s96", CLB_S);
  }
  LBAR();
  u32x4 sv;
  if (w < 6 - TLAG) sv = *reinterpret_cast<const u32x4*>(smem + R_SI + (56 + w) * 1024 + lane * 16);
  else {
    const f32x4* part = reinterpret_cast<const f32x4*>(smem + (w == 6 - TLAG ? R_PART : (w - 6 + TLAG) * 16384)) +
                        (lane & 2) * 32 + (lane & 1) * 32 + (lane >> 2);
    f32x4 x[8][2];
#pragma unroll
    for (int ww = 0; ww < 8; ++ww) { x[ww][0] = part[ww * 128]; x[ww][1] = part[ww * 128 + 16]; }
#pragma unroll
    for (int u = 0; u < 2; ++u) {
      const f32x4 a = ((x[0][u] + x[1][u]) + (x[2][u] + x[3][u])) + ((x[4][u] + x[5][u]) + (x[6][u] + x[7][u]));
      sv[2 * u] = cvtpk(a[0], a[1]); sv[2 * u + 1] = cvtpk(a[2], a[3]);
    }
  }
  st_out(p, e, combine(sv, av, xv));
  if (j == 0 && tid == 0) st2_sc1(rsy, SY_EP, tag);
}


__global__ __launch_bounds__(512, 2) void main_kernel(Args p) {
  char* smem = g_smem;
  p.ws = ws_align(p.ws);
  const int bx = blockIdx.x & 7, bk = blockIdx.x >> 3;
  const bool isg = (bx ^ XPROBE) & 4;
  const int b = (isg ? NCHAIN : 0) + bk * 4 + (bx & 3);
  if (threadIdx.x < 64) {
    unsigned long long pc; unsigned len, keep;
    asm volatile("s_getpc_b64 %0\n.Lpc%=:\n\ts_mov_b32 %1, .Lcode_end-.Lpc%=" : "=s"(pc), "=s"(len));
    asm volatile("s_mov_b32 %0, m0\n\ts_mov_b32 m0, %3\n\ts_nop 0\n\tbuffer_load_dword %1, %2, 0 offen lds\n\ts_mov_b32 m0, %0"
                 : "=&s"(keep) : "v"((bk * 40 + threadIdx.x) * 128), "s"(raw_rsrc((const void*)pc, len)), "s"((b >> 6) == 1 ? 2048 : 163328) : "memory");
  }
  if ((bk >> 3) == (isg ? 3 : 1) && (threadIdx.x >> 6) == 1) {
    constexpr int NI = TLB_NI + 1, NW = (int)(WS_END / TLB_WS);
    const int n = (threadIdx.x & 63) * 8 + (bk & 7), m = n / NI, i = n % NI;
    const u16* q = m == 0 ? p.Wq : m == 1 ? p.Wk : m == 2 ? p.Wv : m == 3 ? p.Wo : m == 4 ? p.wg : m == 5 ? p.wu : m == 6 ? p.wd : m == 7 ? p.x : p.out;
    static_assert(9 * NI + NW <= 512, "");
    if (n < 9 * NI + NW)
      touch_line(m < 9 ? (const char*)q + min(i * (DM * HD * 2 / TLB_NI), (m < 4 ? HD : m < 7 ? FF : S_LEN) * DM * 2 - 128)
                       : p.ws + (size_t)(n - 9 * NI + 1) * TLB_WS - 16384, smem + 163072);
  }
  if (b >= NCHAIN) gemm_role(p, b - NCHAIN, smem);
  else if (b < 32) qk_role(p, (b & 3) * 8 + bk, smem);
  else if (b < 64) vu_role(p, (b & 3) * 8 + bk - 8, smem);
  else red_role(p, b - 64, smem);
}

extern "C" void kernel_launch(void* const* d_in, const int* in_sizes, int n_in,
                              void* d_out, int out_size, void* d_ws, size_t ws_size,
                              hipStream_t stream) {
  Args a;
  a.x = (const u16*)d_in[0]; a.mem0 = (const u16*)d_in[1];
  a.Wq = (const u16*)d_in[2]; a.Wk = (const u16*)d_in[3]; a.Wv = (const u16*)d_in[4]; a.Wo = (const u16*)d_in[5];
  a.wg = (const u16*)d_in[8]; a.wu = (const u16*)d_in[9]; a.wd = (const u16*)d_in[10];
  a.ws = (char*)d_ws; a.out = (u16*)d_out; a.s0 = 0; a.pad1 = 0;
  main_kernel<<<256, 512, 0, stream>>>(a);
}
```

```cpp
#include <hip/hip_runtime.h>
#include <hip/hip_bf16.h>
#include <stdint.h>

constexpr int S_LEN = 2048, DM = 1024, NH = 32, DH = 128, NMEM = 8, SEG = 32;
constexpr int NSEG = S_LEN / SEG;
constexpr int HD = NH * DH;
constexpr int FF = 3584;

typedef unsigned short u16;
typedef __attribute__((ext_vector_type(8))) short bf16x8;
typedef __attribute__((ext_vector_type(4))) short s16x4;
typedef __attribute__((ext_vector_type(4))) float f32x4;
typedef __attribute__((ext_vector_type(4))) unsigned int u32x4;
typedef __attribute__((ext_vector_type(2))) unsigned int u32x2;
typedef __attribute__((address_space(3))) s16x4* lds_s16x4_ptr;
typedef __attribute__((address_space(3))) void* lds_void_ptr;
typedef __attribute__((address_space(1))) unsigned gu32;
typedef __bf16 bf16x2_t __attribute__((ext_vector_type(2)));
typedef float f32x2_t __attribute__((ext_vector_type(2)));

__device__ __forceinline__ unsigned cvtpk(float lo, float hi) {
  bf16x2_t r = __builtin_convertvector((f32x2_t){lo, hi}, bf16x2_t);
  return __builtin_bit_cast(unsigned, r);
}
__device__ __forceinline__ float lo_f(unsigned v) { return __uint_as_float(v << 16); }
__device__ __forceinline__ float hi_f(unsigned v) { return __uint_as_float(v & 0xffff0000u); }
__device__ __forceinline__ float rbf(float f) { return lo_f(cvtpk(f, f)); }
__device__ __forceinline__ bf16x8 as_bf16x8(u32x4 v) { return __builtin_bit_cast(bf16x8, v); }
#define MFMA(a, b, c) __builtin_amdgcn_mfma_f32_16x16x32_bf16((a), (b), (c), 0, 0, 0)

#define KSTR 65
#define XLD 1024
constexpr int PAGE = 2 << 20, NPAGE = 14;
constexpr int T_K = 0, T_Q = 2304, T_V = 4608, T_O = 6912, T_END = T_O + 2048;
constexpr unsigned T_BYTES = T_END * 16384u;
constexpr size_t WS_SY = 0;
constexpr size_t WS_T = (size_t)NPAGE * PAGE;
constexpr size_t WS_XN = WS_T + T_BYTES;
constexpr size_t WS_HF = WS_XN + (size_t)S_LEN * XLD * 2;
constexpr size_t WS_G = WS_HF + (size_t)S_LEN * 16384;
constexpr size_t WS_S = WS_G + (size_t)S_LEN * DM * 2;
constexpr size_t WS_END = WS_S + (size_t)S_LEN * DM * 2;
constexpr unsigned SENT = 0xFFFFFFFFu;
constexpr unsigned DSENT = 0xFFBFFFBFu;
constexpr int SY_MEM = 0;
constexpr int SY_O = 2 * PAGE;
constexpr int SY_V = 6 * PAGE;
constexpr int SY_BR = PAGE, SY_BA = SY_BR + 16384;
constexpr int SY_FKQ = SY_BR + 32768;
constexpr int SY_FG = SY_FKQ + NH * 4 * 128;
constexpr int SY_FO = SY_FG + 16 * 8 * 128;
constexpr int SY_CH = SY_FO + 384 * 128;
constexpr int SY_ABORT = SY_CH + 8 * 128;
constexpr int SY_FS = SY_ABORT + 256;
constexpr int SY_FEND = SY_FS + 64 * 128;
constexpr int SY_EP = SY_FEND, SY_BT = SY_EP + 128;
constexpr int SY_TOTAL = NPAGE * PAGE;
constexpr int PROBE = PAGE - 16384 + 6144;
__device__ __forceinline__ int side(unsigned pm, int page) { return ((pm >> page) & 1u) << 13; }
__device__ __forceinline__ int o_mem(unsigned pm, int s) { return SY_MEM + s * 32768 + side(pm, 0); }
__device__ __forceinline__ int o_o(unsigned pm, int s, int h) {
  return SY_O + s * 131072 + (h >> 3) * 16384 + ((h >> 1) & 3) * 2048 + (h & 1) * 128 + side(pm, 2 + (s >> 4));
}
__device__ __forceinline__ int o_v(unsigned pm, int s, int h) { return SY_V + s * 262144 + (h >> 1) * 16384 + (h & 1) * 4096 + side(pm, 6 + (s >> 3)); }
__device__ __forceinline__ char* ws_align(char* ws) { return ws + ((PAGE - (unsigned)(uintptr_t)ws) & (PAGE - 1)); }
#define NFUSE 12
constexpr int NPRE = 4;
static_assert(WS_END + PAGE <= (size_t)256 << 20 && NH * KSTR <= T_Q && NPAGE % 2 == 0, "");
constexpr int NCHAIN = 128;
__host__ __device__ constexpr int kseg(int sg, int h) { return h * KSTR + sg; }
__host__ __device__ constexpr int fkq(int h, int kind) { return SY_FKQ + (h * 4 + kind) * 128; }

#define TLB_NI 16
#define TLB_WS (2 << 20)
#define XPROBE 0
struct Args {
  const u16 *x, *mem0, *Wq, *Wk, *Wv, *Wo, *wg, *wu, *wd;
  char* ws;
  u16* out;
  int s0, pad1;
};
__device__ __forceinline__ int h_off(const Args& p, int rblk) { return rblk * PAGE + (((p.s0 ^ rblk ^ 1) & 1) << 13); }
static_assert(WS_HF % (2 * PAGE) == 0, "");
#define TF_KQV 1
#define TF_O 0
__device__ __forceinline__ int t_off(const Args& p, int t) { return t * 16384 + (((p.s0 ^ (t >> 7) ^ (TF_KQV == 2 ? t : TF_KQV)) & 1) << 13); }
__device__ __forceinline__ int t_off_o(const Args& p, int t) { return t * 16384 + (((p.s0 ^ (t >> 7) ^ (TF_O == 2 ? t : TF_O)) & 1) << 13); }

__device__ __forceinline__ float wave_sum(float v) {
#pragma unroll
  for (int i = 0; i < 4; ++i) {
    const int x = (int)__float_as_uint(v);
    v += __uint_as_float(i == 0 ? __builtin_amdgcn_update_dpp(0, x, 0x128, 0xf, 0xf, false) : i == 1 ? __builtin_amdgcn_update_dpp(0, x, 0x124, 0xf, 0xf, false)
                         : i == 2 ? __builtin_amdgcn_update_dpp(0, x, 0x122, 0xf, 0xf, false) : __builtin_amdgcn_update_dpp(0, x, 0x121, 0xf, 0xf, false));
  }
  auto r = __builtin_amdgcn_permlane16_swap(__float_as_uint(v), __float_as_uint(v), false, false);
  v = __uint_as_float(r[0]) + __uint_as_float(r[1]);
  r = __builtin_amdgcn_permlane32_swap(__float_as_uint(v), __float_as_uint(v), false, false);
  return __uint_as_float(r[0]) + __uint_as_float(r[1]);
}

__shared__ __attribute__((aligned(16))) char g_smem[163840];
__device__ __forceinline__ unsigned lds_off(const void* p) {
  return (unsigned)(uintptr_t)(lds_void_ptr)g_smem + (unsigned)((const char*)p - g_smem);
}
__device__ __forceinline__ __amdgpu_buffer_rsrc_t mkrsrc(const void* p, unsigned bytes) {
  return __builtin_amdgcn_make_buffer_rsrc((void*)p, 0, bytes, 0x00020000);
}
__device__ __forceinline__ u32x4 ld_sc1(__amdgpu_buffer_rsrc_t rs, int off) {
  return __builtin_amdgcn_raw_buffer_load_b128(rs, off, 0, 16);
}
__device__ __forceinline__ void st_sc1(__amdgpu_buffer_rsrc_t rs, int off, u32x4 v) {
  __builtin_amdgcn_raw_buffer_store_b128(v, rs, off, 0, 16);
}
__device__ __forceinline__ void swap16(unsigned& x, unsigned& y) {
  auto r = __builtin_amdgcn_permlane16_swap(x, y, false, false);
  x = r[0]; y = r[1];
}
__device__ __forceinline__ void swap32(unsigned& x, unsigned& y) {
  auto r = __builtin_amdgcn_permlane32_swap(x, y, false, false);
  x = r[0]; y = r[1];
}
__device__ __forceinline__ bool spin_fail(unsigned& spins, gu32* abortw) {
  ++spins;
  if ((spins & 63u) == 0u) {
    const __amdgpu_buffer_rsrc_t ra = mkrsrc((const void*)abortw, 4);
    unsigned z = 0;
    asm volatile("" : "+v"(z));
    if (__builtin_amdgcn_readfirstlane(__builtin_amdgcn_raw_buffer_load_b32(ra, z, 0, 16)) == 1u) return true;
    if (spins > (1u << 19)) {
      __builtin_amdgcn_raw_buffer_store_b32(z + 1u, ra, z, 0, 16);
      return true;
    }
  }
  return false;
}
__device__ __forceinline__ bf16x8 ldsf(const char* p) { return *reinterpret_cast<const bf16x8*>(p); }
#define SCHED() __builtin_amdgcn_sched_barrier(0)
#define LBAR() do { asm volatile("s_waitcnt lgkmcnt(0)" ::: "memory"); __builtin_amdgcn_s_barrier(); asm volatile("" ::: "memory"); } while (0)
#define WAIT_V(n) asm volatile("s_waitcnt vmcnt(%0)" :: "n"(n) : "memory")

#define HOLD_V(c, n) asm volatile("s_cmp_eq_u32 %0, 0\n\ts_cbranch_scc1 .Lh%=\n\ts_waitcnt vmcnt(%1)\n.Lh%=:" :: "s"((int)(c)), "n"(n) : "memory", "scc")
#define TH_Q 2
__device__ __forceinline__ void wait_flags(__amdgpu_buffer_rsrc_t rs, int off, gu32* abortw) {
  for (unsigned spins = 0;;) {
    asm volatile("" ::: "memory");
    const unsigned v = __builtin_amdgcn_raw_buffer_load_b32(rs, off, 0, 16);
    if (__all(v != SENT)) break;
    __builtin_amdgcn_s_sleep(4);
    if (spin_fail(spins, abortw)) break;
  }
  asm volatile("" ::: "memory");
}

__device__ __forceinline__ unsigned offA(unsigned row, unsigned ch) {
  return row * 128u + 16u * (ch ^ (row & 7u));
}
__device__ __forceinline__ unsigned swzB(unsigned row) { return ((row & 3u) << 2) | ((row >> 2) & 3u); }
__device__ __forceinline__ unsigned offB(unsigned row, unsigned ch) {
  return 256u * row + 16u * (ch ^ swzB(row));
}
__device__ __forceinline__ void glds16(const u16* src, char* dst) {
  const unsigned d = __builtin_amdgcn_readfirstlane(lds_off(dst));
  unsigned keep;
  asm volatile("s_mov_b32 %0, m0\n\ts_mov_b32 m0, %2\n\ts_nop 0\n\tglobal_load_lds_dwordx4 %1, off\n\ts_mov_b32 m0, %0"
               : "=&s"(keep) : "v"(src), "s"(d) : "memory");
}
__device__ __forceinline__ bf16x8 ld_bfrag(const char* sb, int ks, int c, int lane) {
  const unsigned g = lane >> 4, qq = (lane & 15) >> 2, pp = lane & 3;
  s16x4 lo = __builtin_amdgcn_ds_read_tr16_b64_v4i16(
      (lds_s16x4_ptr)(sb + offB(32 * ks + 8 * g + qq, 2 * c + (pp >> 1)) + 8 * (pp & 1)));
  s16x4 hi = __builtin_amdgcn_ds_read_tr16_b64_v4i16(
      (lds_s16x4_ptr)(sb + offB(32 * ks + 8 * g + 4 + qq, 2 * c + (pp >> 1)) + 8 * (pp & 1)));
  return (bf16x8){lo[0], lo[1], lo[2], lo[3], hi[0], hi[1], hi[2], hi[3]};
}

__device__ __forceinline__ void blk_dma(const u16* src, size_t ld, char* slot, int lane) {
  const u16* s = src + (size_t)(lane >> 2) * ld + (lane & 3) * 8;
  glds16(s, slot);
  glds16(s + 16 * ld, slot + 1024);
}
template <int NB, typename ISSUE, typename SINK>
__device__ __forceinline__ void load_blocks(char* slice, ISSUE&& issue, SINK&& sink) {
#pragma unroll
  for (int i = 0; i < 8; ++i) issue(i, slice + i * 2048);
#pragma unroll
  for (int g = 0; g < NB / 4; ++g) {
    if (g + 1 < NB / 4) WAIT_V(8); else WAIT_V(0);
#pragma unroll
    for (int j = 0; j < 4; ++j) sink(4 * g + j, slice + ((4 * g + j) & 7) * 2048);
    asm volatile("s_waitcnt lgkmcnt(0)" ::: "memory");
    if (g + 2 < NB / 4) {
#pragma unroll
      for (int j = 0; j < 4; ++j) issue(4 * g + 8 + j, slice + ((4 * g + j) & 7) * 2048);
    }
  }
}

__device__ __forceinline__ u32x4 widen32(unsigned a0, unsigned a1, unsigned b0, unsigned b1) {
  swap32(a0, b0); swap16(a0, b0);
  swap32(a1, b1); swap16(a1, b1);
  return (u32x4){a0, a1, b0, b1};
}

__device__ __forceinline__ void publish_flags(__amdgpu_buffer_rsrc_t rs, int off, int stride, int n) {
  WAIT_V(0);
  LBAR();
  int tid = threadIdx.x;
  asm volatile("" : "+v"(tid));
  if (tid < n) __builtin_amdgcn_raw_buffer_store_b32(1u, rs, off + tid * stride, 0, 16);
}

__device__ __forceinline__ void wait_s(const Args& p, __amdgpu_buffer_rsrc_t rsy, int rblk, int c0, int n, bool need) {
  if (need) {
    if (threadIdx.x < 64) {
      for (unsigned spins = 0;;) {
        asm volatile("" ::: "memory");
        const int f = (int)__builtin_amdgcn_raw_buffer_load_b32(rsy, SY_FS + (2 * c0 + (threadIdx.x & (2 * n - 1))) * 128, 0, 16);
        if (__all(f >= rblk)) break;
        __builtin_amdgcn_s_sleep(32);
        if (spin_fail(spins, (gu32*)(p.ws + WS_SY + SY_ABORT))) break;
      }
    }
    LBAR();
  }
}
__device__ __forceinline__ u32x4 combine(u32x4 sv, u32x4 g, u32x4 x) {
  u32x4 o;
#pragma unroll
  for (int j = 0; j < 4; ++j) {
    const unsigned t = cvtpk(lo_f(sv[j]) * lo_f(g[j]), hi_f(sv[j]) * hi_f(g[j]));
    o[j] = cvtpk(lo_f(x[j]) + lo_f(t), hi_f(x[j]) + hi_f(t));
  }
  return o;
}
#define OUT_AUX 0
__device__ __forceinline__ void st_out(const Args& p, int e, u32x4 v) {
  __builtin_amdgcn_raw_buffer_store_b128(v, mkrsrc(p.out, S_LEN * DM * 2), e * 2, 0, OUT_AUX);
}
struct SX { u32x4 s, x; };
__device__ __forceinline__ SX get_sx(const Args& p, int rblk, int e) {
  SX r; r.s = (u32x4){0u, 0u, 0u, 0u}; r.x = r.s;
  if (rblk < NFUSE) {
    r.s = ld_sc1(mkrsrc(p.ws + WS_S, S_LEN * DM * 2), (((e & 1023) >> 5) * S_LEN + (e >> 10)) * 64 + (e & 31) * 2);
    r.x = *reinterpret_cast<const u32x4*>(p.x + e);
  }
  return r;
}
__device__ __forceinline__ void put_g(const Args& p, __amdgpu_buffer_rsrc_t rg, int rblk, int e, u32x4 g, const SX& v) {
  if (rblk < NFUSE) {
    st_out(p, e, combine(v.s, g, v.x));
  } else st_sc1(rg, e * 2, g);
}

__device__ __forceinline__ void job_comb(const Args& p, int rblk, int ct, __amdgpu_buffer_rsrc_t rsy) {
  int tid = threadIdx.x;
  asm volatile("" : "+v"(tid));
  u32x4 xv[8];
#pragma unroll
  for (int i = 0; i < 8; ++i) xv[i] = *reinterpret_cast<const u32x4*>(p.x + (rblk * 128 + i * 16 + (tid >> 5)) * DM + ct * 256 + (tid & 31) * 8);
  if (tid < 64) wait_flags(rsy, SY_FG + (rblk * 8 + 2 * ct + (tid & 1)) * 128, (gu32*)(p.ws + WS_SY + SY_ABORT));
  wait_s(p, rsy, rblk, ct * 8, 8, true);
  u32x4 sv[8], gv[8];
#pragma unroll
  for (int i = 0; i < 8; ++i) {
    const int e = (rblk * 128 + i * 16 + (tid >> 5)) * DM + ct * 256 + (tid & 31) * 8;
    sv[i] = ld_sc1(mkrsrc(p.ws + WS_S, S_LEN * DM * 2), (((e & 1023) >> 5) * S_LEN + (e >> 10)) * 64 + (e & 31) * 2);
    gv[i] = ld_sc1(mkrsrc(p.ws + WS_G, S_LEN * DM * 2), e * 2);
  }
#pragma unroll
  for (int i = 0; i < 8; ++i)
    st_out(p, (rblk * 128 + i * 16 + (tid >> 5)) * DM + ct * 256 + (tid & 31) * 8, combine(sv[i], gv[i], xv[i]));
}

__device__ __forceinline__ u32x4 raw_rsrc(const void* p, unsigned bytes) {
  const unsigned long long a = (unsigned long long)p;
  return (u32x4){(unsigned)a, (unsigned)(a >> 32) & 0xffffu, bytes, 0x00020000u};
}
__device__ __forceinline__ u32x2 next_tag(u32x2 e) { return (u32x2){e[0] + 1u, e[1] + (e[0] == 0xFFFFFFFFu)}; }
struct Boot { u32x2 e; u32x4 c; };
__device__ __forceinline__ void boot_ask(const char* ws, Boot& r) {
  const unsigned o = (threadIdx.x & 63) * 128;
  asm volatile("buffer_load_dwordx2 %0, off, %3, %4 sc1\n\tbuffer_load_dwordx4 %1, %2, %3, %5 offen sc1"
               : "=&v"(r.e), "=&v"(r.c) : "v"(o), "s"(raw_rsrc(ws, SY_TOTAL)), "s"(SY_EP), "s"(SY_BT) : "memory");
}
__device__ __forceinline__ void wait_rows(const char* ws, u32x2 tag) {
  const __amdgpu_buffer_rsrc_t rs = mkrsrc(ws, SY_TOTAL);
  int o = (threadIdx.x & 63) * 128;
  asm volatile("" : "+v"(o));
#pragma unroll 1
  for (int i = 0; i < (1 << 17); ++i) {
    asm volatile("" ::: "memory");
    const u32x2 a = __builtin_amdgcn_raw_buffer_load_b64(rs, o, SY_BT + 32 * 128, 16), b = __builtin_amdgcn_raw_buffer_load_b64(rs, o, SY_BT + 96 * 128, 16);
    if (__all(a[0] == tag[0] && a[1] == tag[1] && b[0] == tag[0] && b[1] == tag[1])) break;
    __builtin_amdgcn_s_sleep(8);
  }
  asm volatile("" ::: "memory");
}
__device__ __forceinline__ unsigned boot_done(const char* ws, Boot& r, bool own = false, u32x2 tag = (u32x2){0u, 0u}) {
#pragma unroll 1
  for (int i = 0; i < (1 << 17); ++i) {
    asm volatile("" : "+v"(r.e), "+v"(r.c));
    if (!own) tag = next_tag(r.e);
    if (__all(r.c[0] == tag[0] && r.c[1] == tag[1] && (r.c[2] ^ r.c[3]) == tag[0])) break;
    __builtin_amdgcn_s_sleep(8);
    boot_ask(ws, r);
    WAIT_V(0);
  }
  unsigned q0[NPAGE], q1[NPAGE], ga = 0xFFFFu, gb = 0xFFFFu;
#pragma unroll
  for (int i = 0; i < NPAGE; ++i) {
    const unsigned x = __builtin_amdgcn_readlane(r.c[2], i), y = __builtin_amdgcn_readlane(r.c[2], i + NPAGE);
    q0[i] = min(x >> 16, y >> 16); q1[i] = min(x & 0xFFFFu, y & 0xFFFFu);
    ga = min(ga, (i & 1) ? q1[i] : q0[i]); gb = min(gb, (i & 1) ? q0[i] : q1[i]);
  }
  unsigned m = gb < ga ? 0x80001555u : 0x2AAAu;
  const unsigned thr = min(ga, gb) + 110;
#pragma unroll
  for (int i = 0; i < NPAGE; ++i) {
    const unsigned lo = min(q0[i], q1[i]), hi = max(q0[i], q1[i]);
    const unsigned keep = (lo < thr ? hi : 0u) > lo + 100 ? ~(1u << i) : ~0u;
    m = (m & keep) | ((q1[i] < q0[i] ? ~0u : 0u) & ~keep);
  }
  return m;
}
typedef __attribute__((address_space(3))) volatile unsigned* lds_vu_ptr;
__device__ __forceinline__ lds_vu_ptr lds_at(const void* p) {
  unsigned a = lds_off(p);
  asm volatile("" : "+v"(a));
  return (lds_vu_ptr)(uintptr_t)a;
}
__device__ __forceinline__ unsigned uni(unsigned v) {
  unsigned r;
  asm volatile("s_nop 1\n\tv_readfirstlane_b32 %0, %1\n\ts_nop 3" : "=s"(r) : "v"(v));
  return r;
}
__device__ __forceinline__ void start_sy(char* ws, const u16* mem0, int i, char* smem) {
  const int tid = threadIdx.x, lane = tid & 63, w = __builtin_amdgcn_readfirstlane(tid >> 6);
  const __amdgpu_buffer_rsrc_t rsy = mkrsrc(ws, SY_TOTAL);
  const __amdgpu_buffer_rsrc_t rs = mkrsrc(ws + (size_t)(i % NPAGE) * PAGE + PROBE - (i / NPAGE) * 2048, 16384);
  const u32x2 e = __builtin_amdgcn_raw_buffer_load_b64(rsy, 0, SY_EP, 16);
  const u32x4 ds = {DSENT, DSENT, DSENT, DSENT};
  if (lane < 32) st_sc1(rs, (lane >> 4) * 8192 + w * 256 + (lane & 15) * 16, ds);
  static_assert(NPRE == 4 && SY_FEND - SY_BR <= 32 * 6144, "");
#pragma unroll
  for (int u = 0; u < 6; ++u)
    st_sc1(rsy, (u < 4 ? SY_V + (i * 4 + u) * 8192 : SY_O + (i * 2 + u - 4) * 8192) + tid * 16, ds);
  if (w == 7) {
    const int n = i * 64 + lane, j = (n >> 10) * 512 + (n & 511);
    const unsigned* m0 = reinterpret_cast<const unsigned*>(mem0 + (2 * ((j >> 2) & 3) + (j & 1)) * DM + (j >> 4) * 16) + ((j >> 1) & 1);
    u32x4 m;
#pragma unroll
    for (int k = 0; k < 4; ++k) { const unsigned t = m0[2 * k]; m[k] = t == DSENT ? 0xFFFFFFFFu : t; }
    st_sc1(rsy, n * 16, m);
  } else if (w == 6) st_sc1(rsy, 32768 + i * 1024 + lane * 16, ds);
  else {
    const int o = SY_BR + i * 6144 + tid * 16;
    st_sc1(rsy, o < SY_FEND ? o : 0x7F000000, (u32x4){SENT, SENT, SENT, SENT});
  }
  unsigned t[4];
#pragma unroll
  for (int k = 0; k < 4; ++k) {
    unsigned long long c0, c1; unsigned v, off = ((k ^ (k >> 1)) & 1) * 8192 + w * 256 + (k >> 1) * 128;
    asm volatile("s_waitcnt vmcnt(0)\n\ts_barrier\n\ts_memtime %0\n\ts_waitcnt lgkmcnt(0)\n\tbuffer_load_dword %2, %3, %4, 0 offen sc1\n\ts_waitcnt vmcnt(0)\n\t"
                 "s_memtime %1\n\ts_waitcnt lgkmcnt(0)" : "=&s"(c0), "=&s"(c1), "=&v"(v) : "v"(off), "s"(rs) : "memory");
    t[k] = (unsigned)(c1 - c0);
  }
  lds_vu_ptr tm = lds_at(smem);
  if (lane == 0) { tm[w] = min(t[0], t[3]); tm[8 + w] = min(t[1], t[2]); }
  int ro = SY_BT + i * 128 + lane * 16;
  asm volatile("" : "+v"(ro));
  LBAR();
  if (threadIdx.x < 8) {
    unsigned m0 = tm[0], m1 = tm[8];
#pragma unroll
    for (int k = 1; k < 8; ++k) { m0 = min(m0, tm[k]); m1 = min(m1, tm[8 + k]); }
    const u32x2 tag = next_tag(e);
    const unsigned v = min(m0, 30000u) << 16 | min(m1, 30000u);
    st_sc1(rsy, ro, (u32x4){tag[0], tag[1], v, v ^ tag[0]});
  }
}
template <int NB, typename F>
__device__ __forceinline__ u32x2 start_rows(const Args& p, int g, unsigned on, F&& between) {
  const int tid = threadIdx.x, lane = tid & 63, row0 = g * 16 + (tid >> 6) * 2;
  u32x4 a[4];
  u32x2 e;
  asm volatile("buffer_load_dwordx4 %0, %5, %8, 0 offen\n\tbuffer_load_dwordx4 %1, %5, %8, 0 offen offset:1024\n\t"
               "buffer_load_dwordx4 %2, %5, %8, 0 offen offset:2048\n\tbuffer_load_dwordx4 %3, %5, %8, 0 offen offset:3072\n\t"
               "buffer_load_dwordx2 %4, off, %6, %7 sc1"
               : "=&v"(a[0]), "=&v"(a[1]), "=&v"(a[2]), "=&v"(a[3]), "=&v"(e)
               : "v"(row0 * (DM * 2) + lane * 16), "s"(raw_rsrc(p.ws, SY_TOTAL)), "s"(SY_EP), "s"(raw_rsrc(p.x, on * (S_LEN * DM * 2))) : "memory");
  between();
  asm volatile("s_waitcnt vmcnt(%5)" : "+v"(a[0]), "+v"(a[1]), "+v"(a[2]), "+v"(a[3]), "+v"(e) : "n"(NB) : "memory");
  const __amdgpu_buffer_rsrc_t ro = mkrsrc(p.ws + WS_XN, on * (S_LEN * XLD * 2));
#pragma unroll
  for (int u = 0; u < 2; ++u) {
    float v[16];
#pragma unroll
    for (int i = 0; i < 8; ++i) { v[2 * i] = lo_f(a[2 * u + (i >> 2)][i & 3]); v[2 * i + 1] = hi_f(a[2 * u + (i >> 2)][i & 3]); }
    float s = 0.f;
#pragma unroll
    for (int i = 0; i < 16; ++i) s += v[i];
    const float mean = wave_sum(s) * (1.0f / DM);
    float q = 0.f;
#pragma unroll
    for (int i = 0; i < 16; ++i) { float d = v[i] - mean; q += d * d; }
    const float var = rbf(wave_sum(q) * (1.0f / DM));
    const float mb = rbf(mean);
    const float inv = 1.0f / rbf(sqrtf(rbf(var + 1e-5f)));
#pragma unroll
    for (int k = 0; k < 2; ++k) {
      u32x4 o;
#pragma unroll
      for (int i = 0; i < 4; ++i) {
        const unsigned t = cvtpk(v[8 * k + 2 * i] - mb, v[8 * k + 2 * i + 1] - mb);
        o[i] = cvtpk(lo_f(t) * inv, hi_f(t) * inv);
      }
      st_sc1(ro, (row0 + u) * (XLD * 2) + k * 1024 + lane * 16, o);
    }
  }
  return next_tag(e);
}
__device__ __forceinline__ void report_rows(const Args& p, int g, u32x2 tag) {
  if (threadIdx.x < 8) st_sc1(mkrsrc(p.ws + WS_SY, SY_TOTAL), SY_BT + (32 + g) * 128 + threadIdx.x * 16, (u32x4){tag[0], tag[1], 0u, tag[0]});
}

typedef __attribute__((ext_vector_type(32))) unsigned u32x32;
__device__ __forceinline__ void put4(u32x32& W, int i, bf16x8 f) {
  const u32x4 v = __builtin_bit_cast(u32x4, f);
#pragma unroll
  for (int j = 0; j < 4; ++j) W[4 * i + j] = v[j];
}
#define CLB8(a, b, c, d, e, f, g, h) "v" #a, "v" #b, "v" #c, "v" #d, "v" #e, "v" #f, "v" #g, "v" #h
#define CLB32(a) CLB32_##a
#define CLB32_128 CLB8(128, 129, 130, 131, 132, 133, 134, 135), CLB8(136, 137, 138, 139, 140, 141, 142, 143), CLB8(144, 145, 146, 147, 148, 149, 150, 151), CLB8(152, 153, 154, 155, 156, 157, 158, 159)
#define CLB32_160 CLB8(160, 161, 162, 163, 164, 165, 166, 167), CLB8(168, 169, 170, 171, 172, 173, 174, 175), CLB8(176, 177, 178, 179, 180, 181, 182, 183), CLB8(184, 185, 186, 187, 188, 189, 190, 191)
#define W4_IN "{v[0:31]}"(W[0]), "{v[32:63]}"(W[1]), "{v[64:95]}"(W[2]), "{v[96:127]}"(W[3])
#define CLB32_192 CLB8(192, 193, 194, 195, 196, 197, 198, 199), CLB8(200, 201, 202, 203, 204, 205, 206, 207), CLB8(208, 209, 210, 211, 212, 213, 214, 215), CLB8(216, 217, 218, 219, 220, 221, 222, 223)
__device__ __forceinline__ unsigned ld_flag0(__amdgpu_buffer_rsrc_t rs, int off) {
  return __builtin_amdgcn_raw_buffer_load_b32(rs, off, 0, 16);
}
__device__ __forceinline__ void glds16_sc1(const char* src, char* dst) {
  const unsigned d = __builtin_amdgcn_readfirstlane(lds_off(dst));
  unsigned keep;
  asm volatile("s_mov_b32 %0, m0\n\ts_mov_b32 m0, %2\n\ts_nop 0\n\tglobal_load_lds_dwordx4 %1, off sc1\n\ts_mov_b32 m0, %0"
               : "=&s"(keep) : "v"(src), "s"(d) : "memory");
}

__device__ __forceinline__ void touch_line(const char* src, char* dst) {
  const unsigned d = __builtin_amdgcn_readfirstlane(lds_off(dst));
  unsigned keep;
  asm volatile("s_mov_b32 %0, m0\n\ts_mov_b32 m0, %2\n\ts_nop 0\n\tglobal_load_lds_dword %1, off sc1\n\ts_mov_b32 m0, %0"
               : "=&s"(keep) : "v"(src), "s"(d) : "memory");
}

#define PF_N 12
#define LN_M 3
#define LN_X 1
#define PR_G1 8
#define WREV 1
template <bool DEV = false>
__device__ __forceinline__ void glds2(const char* base, int step, unsigned voff, unsigned dst) {
  unsigned keep;
  const char* base2 = base + step;
  if (DEV)
    asm volatile("s_mov_b32 %0, m0\n\ts_mov_b32 m0, %4\n\ts_nop 0\n\tglobal_load_lds_dwordx4 %1, %2 sc1\n\t"
                 "s_add_u32 m0, m0, 0x2000\n\ts_nop 0\n\tglobal_load_lds_dwordx4 %1, %3 sc1\n\ts_mov_b32 m0, %0"
                 : "=&s"(keep) : "v"(voff), "s"(base), "s"(base2), "s"(dst) : "memory", "scc");
  else
  asm volatile("s_mov_b32 %0, m0\n\ts_mov_b32 m0, %4\n\ts_nop 0\n\tglobal_load_lds_dwordx4 %1, %2\n\t"
               "s_add_u32 m0, m0, 0x2000\n\ts_nop 0\n\tglobal_load_lds_dwordx4 %1, %3\n\ts_mov_b32 m0, %0"
               : "=&s"(keep) : "v"(voff), "s"(base), "s"(base2), "s"(dst) : "memory", "scc");
}

#define DN_ASM \
  "s_mov_b32 s94, m0\n\t" \
  "s_mov_b64 s[84:85], %[pa]\n\t" \
  "s_mov_b64 s[86:87], %[pb]\n\t" \
  "s_movk_i32 s88, 0x4000\n\t" \
  "s_mov_b32 s89, 0x10000\n\t" \
  "s_movk_i32 s90, 13\n\t" \
  "v_mov_b64 v[0:1], 0\n\t" \
  "v_mov_b64 v[2:3], 0\n\t" \
  "v_mov_b64 v[4:5], 0\n\t" \
  "v_mov_b64 v[6:7], 0\n\t" \
  "v_mov_b64 v[8:9], 0\n\t" \
  "v_mov_b64 v[10:11], 0\n\t" \
  "v_mov_b64 v[12:13], 0\n\t" \
  "v_mov_b64 v[14:15], 0\n\t" \
  "v_mov_b64 v[16:17], 0\n\t" \
  "v_mov_b64 v[18:19], 0\n\t" \
  "v_mov_b64 v[20:21], 0\n\t" \
  "v_mov_b64 v[22:23], 0\n\t" \
  "v_mov_b64 v[24:25], 0\n\t" \
  "v_mov_b64 v[26:27], 0\n\t" \
  "v_mov_b64 v[28:29], 0\n\t" \
  "v_mov_b64 v[30:31], 0\n\t" \
  "s_cmp_lg_u32 %[hf], 0\n\t" \
  "s_cbranch_scc1 .Lk%=\n\t" \
  "ds_read_b128 v[32:35], v80 offset:0\n\t" \
  "ds_read_b128 v[36:39], v80 offset:1024\n\t" \
  "ds_read_b128 v[40:43], v80 offset:2048\n\t" \
  "ds_read_b128 v[44:47], v80 offset:3072\n\t" \
  "ds_read_b64_tr_b16 v[64:65], v82 offset:0\n\t" \
  "ds_read_b64_tr_b16 v[66:67], v83 offset:0\n\t" \
  "ds_read_b128 v[48:51], v80 offset:8192\n\t" \
  "ds_read_b64_tr_b16 v[68:69], v84 offset:0\n\t" \
  "ds_read_b64_tr_b16 v[70:71], v85 offset:0\n\t" \
  "ds_read_b128 v[52:55], v80 offset:9216\n\t" \
  "ds_read_b128 v[56:59], v80 offset:10240\n\t" \
  "ds_read_b128 v[60:63], v80 offset:11264\n\t" \
  ".Lp0%=:\n\t" \
  "s_waitcnt vmcnt(4)\n\t" \
  "s_barrier\n\t" \
  "s_waitcnt lgkmcnt(3)\n\t" \
  "v_mfma_f32_16x16x32_bf16 v[0:3], v[64:67], v[32:35], v[0:3]\n\t" \
  "ds_read_b64_tr_b16 v[72:73], v82 offset:8192\n\t" \
  "v_mfma_f32_16x16x32_bf16 v[4:7], v[68:71], v[32:35], v[4:7]\n\t" \
  "ds_read_b64_tr_b16 v[74:75], v83 offset:8192\n\t" \
  "ds_read_b128 v[32:35], v80 offset:32768\n\t" \
  "s_add_u32 m0, %[lw], 98304\n\t" \
  "v_mfma_f32_16x16x32_bf16 v[8:11], v[64:67], v[36:39], v[8:11]\n\t" \
  "ds_read_b64_tr_b16 v[76:77], v84 offset:8192\n\t" \
  "global_load_lds_dwordx4 v90, s[84:85] sc1\n\t" \
  "s_add_u32 s84, s84, s88\n\t" \
  "s_addc_u32 s85, s85, 0\n\t" \
  "v_mfma_f32_16x16x32_bf16 v[12:15], v[68:71], v[36:39], v[12:15]\n\t" \
  "ds_read_b64_tr_b16 v[78:79], v85 offset:8192\n\t" \
  "ds_read_b128 v[36:39], v80 offset:33792\n\t" \
  "v_mfma_f32_16x16x32_bf16 v[16:19], v[64:67], v[40:43], v[16:19]\n\t" \
  "v_mfma_f32_16x16x32_bf16 v[20:23], v[68:71], v[40:43], v[20:23]\n\t" \
  "ds_read_b128 v[40:43], v80 offset:34816\n\t" \
  "s_add_u32 m0, %[lw], 114688\n\t" \
  "v_mfma_f32_16x16x32_bf16 v[24:27], v[64:67], v[44:47], v[24:27]\n\t" \
  "global_load_lds_dwordx4 v91, s[86:87]\n\t" \
  "s_add_u32 s86, s86, s89\n\t" \
  "s_addc_u32 s87, s87, 0\n\t" \
  "v_mfma_f32_16x16x32_bf16 v[28:31], v[68:71], v[44:47], v[28:31]\n\t" \
  "ds_read_b128 v[44:47], v80 offset:35840\n\t" \
  "s_waitcnt lgkmcnt(3)\n\t" \
  "v_mfma_f32_16x16x32_bf16 v[0:3], v[72:75], v[48:51], v[0:3]\n\t" \
  "ds_read_b64_tr_b16 v[64:65], v82 offset:32768\n\t" \
  "v_mfma_f32_16x16x32_bf16 v[4:7], v[76:79], v[48:51], v[4:7]\n\t" \
  "ds_read_b64_tr_b16 v[66:67], v83 offset:32768\n\t" \
  "ds_read_b128 v[48:51], v80 offset:40960\n\t" \
  "s_add_u32 m0, %[lw], 106496\n\t" \
  "v_mfma_f32_16x16x32_bf16 v[8:11], v[72:75], v[52:55], v[8:11]\n\t" \
  "ds_read_b64_tr_b16 v[68:69], v84 offset:32768\n\t" \
  "global_load_lds_dwordx4 v90, s[84:85] sc1\n\t" \
  "s_add_u32 s84, s84, s88\n\t" \
  "s_addc_u32 s85, s85, 0\n\t" \
  "v_mfma_f32_16x16x32_bf16 v[12:15], v[76:79], v[52:55], v[12:15]\n\t" \
  "ds_read_b64_tr_b16 v[70:71], v85 offset:32768\n\t" \
  "ds_read_b128 v[52:55], v80 offset:41984\n\t" \
  "v_mfma_f32_16x16x32_bf16 v[16:19], v[72:75], v[56:59], v[16:19]\n\t" \
  "v_mfma_f32_16x16x32_bf16 v[20:23], v[76:79], v[56:59], v[20:23]\n\t" \
  "ds_read_b128 v[56:59], v80 offset:43008\n\t" \
  "s_add_u32 m0, %[lw], 122880\n\t" \
  "v_mfma_f32_16x16x32_bf16 v[24:27], v[72:75], v[60:63], v[24:27]\n\t" \
  "global_load_lds_dwordx4 v91, s[86:87]\n\t" \
  "s_add_u32 s86, s86, s89\n\t" \
  "s_addc_u32 s87, s87, 0\n\t" \
  "v_mfma_f32_16x16x32_bf16 v[28:31], v[76:79], v[60:63], v[28:31]\n\t" \
  "ds_read_b128 v[60:63], v80 offset:44032\n\t" \
  "s_sub_u32 s90, s90, 1\n\t" \
  "s_cselect_b32 s88, 0, s88\n\t" \
  "s_cselect_b32 s89, 0, s89\n\t" \
  "s_cselect_b32 s91, 16384, 0\n\t" \
  "s_sub_u32 s84, s84, s91\n\t" \
  "s_subb_u32 s85, s85, 0\n\t" \
  "s_lshl_b32 s91, s91, 2\n\t" \
  "s_sub_u32 s86, s86, s91\n\t" \
  "s_subb_u32 s87, s87, 0\n\t" \
  "s_waitcnt vmcnt(4)\n\t" \
  "s_barrier\n\t" \
  "s_waitcnt lgkmcnt(3)\n\t" \
  "v_mfma_f32_16x16x32_bf16 v[0:3], v[64:67], v[32:35], v[0:3]\n\t" \
  "ds_read_b64_tr_b16 v[72:73], v82 offset:40960\n\t" \
  "v_mfma_f32_16x16x32_bf16 v[4:7], v[68:71], v[32:35], v[4:7]\n\t" \
  "ds_read_b64_tr_b16 v[74:75], v83 offset:40960\n\t" \
  "ds_read_b128 v[32:35], v81 offset:0\n\t" \
  "s_add_u32 m0, %[lw], 0\n\t" \
  "v_mfma_f32_16x16x32_bf16 v[8:11], v[64:67], v[36:39], v[8:11]\n\t" \
  "ds_read_b64_tr_b16 v[76:77], v84 offset:40960\n\t" \
  "global_load_lds_dwordx4 v90, s[84:85] sc1\n\t" \
  "s_add_u32 s84, s84, s88\n\t" \
  "s_addc_u32 s85, s85, 0\n\t" \
  "v_mfma_f32_16x16x32_bf16 v[12:15], v[68:71], v[36:39], v[12:15]\n\t" \
  "ds_read_b64_tr_b16 v[78:79], v85 offset:40960\n\t" \
  "ds_read_b128 v[36:39], v81 offset:1024\n\t" \
  "v_mfma_f32_16x16x32_bf16 v[16:19], v[64:67], v[40:43], v[16:19]\n\t" \
  "v_mfma_f32_16x16x32_bf16 v[20:23], v[68:71], v[40:43], v[20:23]\n\t" \
  "ds_read_b128 v[40:43], v81 offset:2048\n\t" \
  "s_add_u32 m0, %[lw], 16384\n\t" \
  "v_mfma_f32_16x16x32_bf16 v[24:27], v[64:67], v[44:47], v[24:27]\n\t" \
  "global_load_lds_dwordx4 v91, s[86:87]\n\t" \
  "s_add_u32 s86, s86, s89\n\t" \
  "s_addc_u32 s87, s87, 0\n\t" \
  "v_mfma_f32_16x16x32_bf16 v[28:31], v[68:71], v[44:47], v[28:31]\n\t" \
  "ds_read_b128 v[44:47], v81 offset:3072\n\t" \
  "s_waitcnt lgkmcnt(3)\n\t" \
  "v_mfma_f32_16x16x32_bf16 v[0:3], v[72:75], v[48:51], v[0:3]\n\t" \
  "ds_read_b64_tr_b16 v[64:65], v86 offset:0\n\t" \
  "v_mfma_f32_16x16x32_bf16 v[4:7], v[76:79], v[48:51], v[4:7]\n\t" \
  "ds_read_b64_tr_b16 v[66:67], v87 offset:0\n\t" \
  "ds_read_b128 v[48:51], v81 offset:8192\n\t" \
  "s_add_u32 m0, %[lw], 8192\n\t" \
  "v_mfma_f32_16x16x32_bf16 v[8:11], v[72:75], v[52:55], v[8:11]\n\t" \
  "ds_read_b64_tr_b16 v[68:69], v88 offset:0\n\t" \
  "global_load_lds_dwordx4 v90, s[84:85] sc1\n\t" \
  "s_add_u32 s84, s84, s88\n\t" \
  "s_addc_u32 s85, s85, 0\n\t" \
  "v_mfma_f32_16x16x32_bf16 v[12:15], v[76:79], v[52:55], v[12:15]\n\t" \
  "ds_read_b64_tr_b16 v[70:71], v89 offset:0\n\t" \
  "ds_read_b128 v[52:55], v81 offset:9216\n\t" \
  "v_mfma_f32_16x16x32_bf16 v[16:19], v[72:75], v[56:59], v[16:19]\n\t" \
  "v_mfma_f32_16x16x32_bf16 v[20:23], v[76:79], v[56:59], v[20:23]\n\t" \
  "ds_read_b128 v[56:59], v81 offset:10240\n\t" \
  "s_add_u32 m0, %[lw], 24576\n\t" \
  "v_mfma_f32_16x16x32_bf16 v[24:27], v[72:75], v[60:63], v[24:27]\n\t" \
  "global_load_lds_dwordx4 v91, s[86:87]\n\t" \
  "s_add_u32 s86, s86, s89\n\t" \
  "s_addc_u32 s87, s87, 0\n\t" \
  "v_mfma_f32_16x16x32_bf16 v[28:31], v[76:79], v[60:63], v[28:31]\n\t" \
  "ds_read_b128 v[60:63], v81 offset:11264\n\t" \
  "s_waitcnt vmcnt(4)\n\t" \
  "s_barrier\n\t" \
  "s_waitcnt lgkmcnt(3)\n\t" \
  "v_mfma_f32_16x16x32_bf16 v[0:3], v[64:67], v[32:35], v[0:3]\n\t" \
  "ds_read_b64_tr_b16 v[72:73], v86 offset:8192\n\t" \
  "v_mfma_f32_16x16x32_bf16 v[4:7], v[68:71], v[32:35], v[4:7]\n\t" \
  "ds_read_b64_tr_b16 v[74:75], v87 offset:8192\n\t" \
  "ds_read_b128 v[32:35], v81 offset:32768\n\t" \
  "s_add_u32 m0, %[lw], 32768\n\t" \
  "v_mfma_f32_16x16x32_bf16 v[8:11], v[64:67], v[36:39], v[8:11]\n\t" \
  "ds_read_b64_tr_b16 v[76:77], v88 offset:8192\n\t" \
  "global_load_lds_dwordx4 v90, s[84:85] sc1\n\t" \
  "s_add_u32 s84, s84, s88\n\t" \
  "s_addc_u32 s85, s85, 0\n\t" \
  "v_mfma_f32_16x16x32_bf16 v[12:15], v[68:71], v[36:39], v[12:15]\n\t" \
  "ds_read_b64_tr_b16 v[78:79], v89 offset:8192\n\t" \
  "ds_read_b128 v[36:39], v81 offset:33792\n\t" \
  "v_mfma_f32_16x16x32_bf16 v[16:19], v[64:67], v[40:43], v[16:19]\n\t" \
  "v_mfma_f32_16x16x32_bf16 v[20:23], v[68:71], v[40:43], v[20:23]\n\t" \
  "ds_read_b128 v[40:43], v81 offset:34816\n\t" \
  "s_add_u32 m0, %[lw], 49152\n\t" \
  "v_mfma_f32_16x16x32_bf16 v[24:27], v[64:67], v[44:47], v[24:27]\n\t" \
  "global_load_lds_dwordx4 v91, s[86:87]\n\t" \
  "s_add_u32 s86, s86, s89\n\t" \
  "s_addc_u32 s87, s87, 0\n\t" \
  "v_mfma_f32_16x16x32_bf16 v[28:31], v[68:71], v[44:47], v[28:31]\n\t" \
  "ds_read_b128 v[44:47], v81 offset:35840\n\t" \
  "s_waitcnt lgkmcnt(3)\n\t" \
  "v_mfma_f32_16x16x32_bf16 v[0:3], v[72:75], v[48:51], v[0:3]\n\t" \
  "ds_read_b64_tr_b16 v[64:65], v86 offset:32768\n\t" \
  "v_mfma_f32_16x16x32_bf16 v[4:7], v[76:79], v[48:51], v[4:7]\n\t" \
  "ds_read_b64_tr_b16 v[66:67], v87 offset:32768\n\t" \
  "ds_read_b128 v[48:51], v81 offset:40960\n\t" \
  "s_add_u32 m0, %[lw], 40960\n\t" \
  "v_mfma_f32_16x16x32_bf16 v[8:11], v[72:75], v[52:55], v[8:11]\n\t" \
  "ds_read_b64_tr_b16 v[68:69], v88 offset:32768\n\t" \
  "global_load_lds_dwordx4 v90, s[84:85] sc1\n\t" \
  "s_add_u32 s84, s84, s88\n\t" \
  "s_addc_u32 s85, s85, 0\n\t" \
  "v_mfma_f32_16x16x32_bf16 v[12:15], v[76:79], v[52:55], v[12:15]\n\t" \
  "ds_read_b64_tr_b16 v[70:71], v89 offset:32768\n\t" \
  "ds_read_b128 v[52:55], v81 offset:41984\n\t" \
  "v_mfma_f32_16x16x32_bf16 v[16:19], v[72:75], v[56:59], v[16:19]\n\t" \
  "v_mfma_f32_16x16x32_bf16 v[20:23], v[76:79], v[56:59], v[20:23]\n\t" \
  "ds_read_b128 v[56:59], v81 offset:43008\n\t" \
  "s_add_u32 m0, %[lw], 57344\n\t" \
  "v_mfma_f32_16x16x32_bf16 v[24:27], v[72:75], v[60:63], v[24:27]\n\t" \
  "global_load_lds_dwordx4 v91, s[86:87]\n\t" \
  "s_add_u32 s86, s86, s89\n\t" \
  "s_addc_u32 s87, s87, 0\n\t" \
  "v_mfma_f32_16x16x32_bf16 v[28:31], v[76:79], v[60:63], v[28:31]\n\t" \
  "ds_read_b128 v[60:63], v81 offset:44032\n\t" \
  "s_waitcnt vmcnt(4)\n\t" \
  "s_barrier\n\t" \
  "s_waitcnt lgkmcnt(3)\n\t" \
  "v_mfma_f32_16x16x32_bf16 v[0:3], v[64:67], v[32:35], v[0:3]\n\t" \
  "ds_read_b64_tr_b16 v[72:73], v86 offset:40960\n\t" \
  "v_mfma_f32_16x16x32_bf16 v[4:7], v[68:71], v[32:35], v[4:7]\n\t" \
  "ds_read_b64_tr_b16 v[74:75], v87 offset:40960\n\t" \
  "ds_read_b128 v[32:35], v80 offset:0\n\t" \
  "s_add_u32 m0, %[lw], 65536\n\t" \
  "v_mfma_f32_16x16x32_bf16 v[8:11], v[64:67], v[36:39], v[8:11]\n\t" \
  "ds_read_b64_tr_b16 v[76:77], v88 offset:40960\n\t" \
  "global_load_lds_dwordx4 v90, s[84:85] sc1\n\t" \
  "s_add_u32 s84, s84, s88\n\t" \
  "s_addc_u32 s85, s85, 0\n\t" \
  "v_mfma_f32_16x16x32_bf16 v[12:15], v[68:71], v[36:39], v[12:15]\n\t" \
  "ds_read_b64_tr_b16 v[78:79], v89 offset:40960\n\t" \
  "ds_read_b128 v[36:39], v80 offset:1024\n\t" \
  "v_mfma_f32_16x16x32_bf16 v[16:19], v[64:67], v[40:43], v[16:19]\n\t" \
  "v_mfma_f32_16x16x32_bf16 v[20:23], v[68:71], v[40:43], v[20:23]\n\t" \
  "ds_read_b128 v[40:43], v80 offset:2048\n\t" \
  "s_add_u32 m0, %[lw], 81920\n\t" \
  "v_mfma_f32_16x16x32_bf16 v[24:27], v[64:67], v[44:47], v[24:27]\n\t" \
  "global_load_lds_dwordx4 v91, s[86:87]\n\t" \
  "s_add_u32 s86, s86, s89\n\t" \
  "s_addc_u32 s87, s87, 0\n\t" \
  "v_mfma_f32_16x16x32_bf16 v[28:31], v[68:71], v[44:47], v[28:31]\n\t" \
  "ds_read_b128 v[44:47], v80 offset:3072\n\t" \
  "s_waitcnt lgkmcnt(3)\n\t" \
  "v_mfma_f32_16x16x32_bf16 v[0:3], v[72:75], v[48:51], v[0:3]\n\t" \
  "ds_read_b64_tr_b16 v[64:65], v82 offset:0\n\t" \
  "v_mfma_f32_16x16x32_bf16 v[4:7], v[76:79], v[48:51], v[4:7]\n\t" \
  "ds_read_b64_tr_b16 v[66:67], v83 offset:0\n\t" \
  "ds_read_b128 v[48:51], v80 offset:8192\n\t" \
  "s_add_u32 m0, %[lw], 73728\n\t" \
  "v_mfma_f32_16x16x32_bf16 v[8:11], v[72:75], v[52:55], v[8:11]\n\t" \
  "ds_read_b64_tr_b16 v[68:69], v84 offset:0\n\t" \
  "global_load_lds_dwordx4 v90, s[84:85] sc1\n\t" \
  "s_add_u32 s84, s84, s88\n\t" \
  "s_addc_u32 s85, s85, 0\n\t" \
  "v_mfma_f32_16x16x32_bf16 v[12:15], v[76:79], v[52:55], v[12:15]\n\t" \
  "ds_read_b64_tr_b16 v[70:71], v85 offset:0\n\t" \
  "ds_read_b128 v[52:55], v80 offset:9216\n\t" \
  "v_mfma_f32_16x16x32_bf16 v[16:19], v[72:75], v[56:59], v[16:19]\n\t" \
  "v_mfma_f32_16x16x32_bf16 v[20:23], v[76:79], v[56:59], v[20:23]\n\t" \
  "ds_read_b128 v[56:59], v80 offset:10240\n\t" \
  "s_add_u32 m0, %[lw], 90112\n\t" \
  "v_mfma_f32_16x16x32_bf16 v[24:27], v[72:75], v[60:63], v[24:27]\n\t" \
  "global_load_lds_dwordx4 v91, s[86:87]\n\t" \
  "s_add_u32 s86, s86, s89\n\t" \
  "s_addc_u32 s87, s87, 0\n\t" \
  "v_mfma_f32_16x16x32_bf16 v[28:31], v[76:79], v[60:63], v[28:31]\n\t" \
  "ds_read_b128 v[60:63], v80 offset:11264\n\t" \
  "s_cmp_lg_u32 s88, 0\n\t" \
  "s_cbranch_scc1 .Lp0%=\n\t" \
  "s_branch .Le%=\n\t" \
  ".Lk%=:\n\t" \
  "ds_read_b128 v[32:35], v80 offset:0\n\t" \
  "ds_read_b128 v[36:39], v80 offset:1024\n\t" \
  "ds_read_b128 v[40:43], v80 offset:2048\n\t" \
  "ds_read_b128 v[44:47], v80 offset:3072\n\t" \
  "ds_read_b64_tr_b16 v[64:65], v82 offset:0\n\t" \
  "ds_read_b64_tr_b16 v[66:67], v83 offset:0\n\t" \
  "ds_read_b128 v[48:51], v80 offset:8192\n\t" \
  "ds_read_b64_tr_b16 v[68:69], v84 offset:0\n\t" \
  "ds_read_b64_tr_b16 v[70:71], v85 offset:0\n\t" \
  "ds_read_b128 v[52:55], v80 offset:9216\n\t" \
  "ds_read_b128 v[56:59], v80 offset:10240\n\t" \
  "ds_read_b128 v[60:63], v80 offset:11264\n\t" \
  ".Lp1%=:\n\t" \
  "s_waitcnt vmcnt(4)\n\t" \
  "s_barrier\n\t" \
  "s_waitcnt lgkmcnt(3)\n\t" \
  "v_mfma_f32_16x16x32_bf16 v[0:3], v[64:67], v[32:35], v[0:3]\n\t" \
  "ds_read_b64_tr_b16 v[72:73], v82 offset:8192\n\t" \
  "v_mfma_f32_16x16x32_bf16 v[4:7], v[68:71], v[32:35], v[4:7]\n\t" \
  "ds_read_b64_tr_b16 v[74:75], v83 offset:8192\n\t" \
  "ds_read_b128 v[32:35], v80 offset:32768\n\t" \
  "v_mfma_f32_16x16x32_bf16 v[8:11], v[64:67], v[36:39], v[8:11]\n\t" \
  "ds_read_b64_tr_b16 v[76:77], v84 offset:8192\n\t" \
  "v_mfma_f32_16x16x32_bf16 v[12:15], v[68:71], v[36:39], v[12:15]\n\t" \
  "ds_read_b64_tr_b16 v[78:79], v85 offset:8192\n\t" \
  "ds_read_b128 v[36:39], v80 offset:33792\n\t" \
  "s_add_u32 m0, %[lw], 98304\n\t" \
  "v_mfma_f32_16x16x32_bf16 v[16:19], v[64:67], v[40:43], v[16:19]\n\t" \
  "global_load_lds_dwordx4 v90, s[84:85] sc1\n\t" \
  "s_add_u32 s84, s84, s88\n\t" \
  "s_addc_u32 s85, s85, 0\n\t" \
  "v_mfma_f32_16x16x32_bf16 v[20:23], v[68:71], v[40:43], v[20:23]\n\t" \
  "ds_read_b128 v[40:43], v80 offset:34816\n\t" \
  "v_mfma_f32_16x16x32_bf16 v[24:27], v[64:67], v[44:47], v[24:27]\n\t" \
  "s_add_u32 m0, %[lw], 114688\n\t" \
  "v_mfma_f32_16x16x32_bf16 v[28:31], v[68:71], v[44:47], v[28:31]\n\t" \
  "ds_read_b128 v[44:47], v80 offset:35840\n\t" \
  "global_load_lds_dwordx4 v91, s[86:87]\n\t" \
  "s_add_u32 s86, s86, s89\n\t" \
  "s_addc_u32 s87, s87, 0\n\t" \
  "s_waitcnt lgkmcnt(3)\n\t" \
  "v_mfma_f32_16x16x32_bf16 v[0:3], v[72:75], v[48:51], v[0:3]\n\t" \
  "ds_read_b64_tr_b16 v[64:65], v82 offset:32768\n\t" \
  "v_mfma_f32_16x16x32_bf16 v[4:7], v[76:79], v[48:51], v[4:7]\n\t" \
  "ds_read_b64_tr_b16 v[66:67], v83 offset:32768\n\t" \
  "ds_read_b128 v[48:51], v80 offset:40960\n\t" \
  "v_mfma_f32_16x16x32_bf16 v[8:11], v[72:75], v[52:55], v[8:11]\n\t" \
  "ds_read_b64_tr_b16 v[68:69], v84 offset:32768\n\t" \
  "v_mfma_f32_16x16x32_bf16 v[12:15], v[76:79], v[52:55], v[12:15]\n\t" \
  "ds_read_b64_tr_b16 v[70:71], v85 offset:32768\n\t" \
  "ds_read_b128 v[52:55], v80 offset:41984\n\t" \
  "s_add_u32 m0, %[lw], 106496\n\t" \
  "v_mfma_f32_16x16x32_bf16 v[16:19], v[72:75], v[56:59], v[16:19]\n\t" \
  "global_load_lds_dwordx4 v90, s[84:85] sc1\n\t" \
  "s_add_u32 s84, s84, s88\n\t" \
  "s_addc_u32 s85, s85, 0\n\t" \
  "v_mfma_f32_16x16x32_bf16 v[20:23], v[76:79], v[56:59], v[20:23]\n\t" \
  "ds_read_b128 v[56:59], v80 offset:43008\n\t" \
  "v_mfma_f32_16x16x32_bf16 v[24:27], v[72:75], v[60:63], v[24:27]\n\t" \
  "s_add_u32 m0, %[lw], 122880\n\t" \
  "v_mfma_f32_16x16x32_bf16 v[28:31], v[76:79], v[60:63], v[28:31]\n\t" \
  "ds_read_b128 v[60:63], v80 offset:44032\n\t" \
  "global_load_lds_dwordx4 v91, s[86:87]\n\t" \
  "s_add_u32 s86, s86, s89\n\t" \
  "s_addc_u32 s87, s87, 0\n\t" \
  "s_sub_u32 s90, s90, 1\n\t" \
  "s_cselect_b32 s88, 0, s88\n\t" \
  "s_cselect_b32 s89, 0, s89\n\t" \
  "s_cselect_b32 s91, 16384, 0\n\t" \
  "s_sub_u32 s84, s84, s91\n\t" \
  "s_subb_u32 s85, s85, 0\n\t" \
  "s_lshl_b32 s91, s91, 2\n\t" \
  "s_sub_u32 s86, s86, s91\n\t" \
  "s_subb_u32 s87, s87, 0\n\t" \
  "s_waitcnt vmcnt(4)\n\t" \
  "s_barrier\n\t" \
  "s_waitcnt lgkmcnt(3)\n\t" \
  "v_mfma_f32_16x16x32_bf16 v[0:3], v[64:67], v[32:35], v[0:3]\n\t" \
  "ds_read_b64_tr_b16 v[72:73], v82 offset:40960\n\t" \
  "v_mfma_f32_16x16x32_bf16 v[4:7], v[68:71], v[32:35], v[4:7]\n\t" \
  "ds_read_b64_tr_b16 v[74:75], v83 offset:40960\n\t" \
  "ds_read_b128 v[32:35], v81 offset:0\n\t" \
  "v_mfma_f32_16x16x32_bf16 v[8:11], v[64:67], v[36:39], v[8:11]\n\t" \
  "ds_read_b64_tr_b16 v[76:77], v84 offset:40960\n\t" \
  "v_mfma_f32_16x16x32_bf16 v[12:15], v[68:71], v[36:39], v[12:15]\n\t" \
  "ds_read_b64_tr_b16 v[78:79], v85 offset:40960\n\t" \
  "ds_read_b128 v[36:39], v81 offset:1024\n\t" \
  "s_add_u32 m0, %[lw], 0\n\t" \
  "v_mfma_f32_16x16x32_bf16 v[16:19], v[64:67], v[40:43], v[16:19]\n\t" \
  "global_load_lds_dwordx4 v90, s[84:85] sc1\n\t" \
  "s_add_u32 s84, s84, s88\n\t" \
  "s_addc_u32 s85, s85, 0\n\t" \
  "v_mfma_f32_16x16x32_bf16 v[20:23], v[68:71], v[40:43], v[20:23]\n\t" \
  "ds_read_b128 v[40:43], v81 offset:2048\n\t" \
  "v_mfma_f32_16x16x32_bf16 v[24:27], v[64:67], v[44:47], v[24:27]\n\t" \
  "s_add_u32 m0, %[lw], 16384\n\t" \
  "v_mfma_f32_16x16x32_bf16 v[28:31], v[68:71], v[44:47], v[28:31]\n\t" \
  "ds_read_b128 v[44:47], v81 offset:3072\n\t" \
  "global_load_lds_dwordx4 v91, s[86:87]\n\t" \
  "s_add_u32 s86, s86, s89\n\t" \
  "s_addc_u32 s87, s87, 0\n\t" \
  "s_waitcnt lgkmcnt(3)\n\t" \
  "v_mfma_f32_16x16x32_bf16 v[0:3], v[72:75], v[48:51], v[0:3]\n\t" \
  "ds_read_b64_tr_b16 v[64:65], v86 offset:0\n\t" \
  "v_mfma_f32_16x16x32_bf16 v[4:7], v[76:79], v[48:51], v[4:7]\n\t" \
  "ds_read_b64_tr_b16 v[66:67], v87 offset:0\n\t" \
  "ds_read_b128 v[48:51], v81 offset:8192\n\t" \
  "v_mfma_f32_16x16x32_bf16 v[8:11], v[72:75], v[52:55], v[8:11]\n\t" \
  "ds_read_b64_tr_b16 v[68:69], v88 offset:0\n\t" \
  "v_mfma_f32_16x16x32_bf16 v[12:15], v[76:79], v[52:55], v[12:15]\n\t" \
  "ds_read_b64_tr_b16 v[70:71], v89 offset:0\n\t" \
  "ds_read_b128 v[52:55], v81 offset:9216\n\t" \
  "s_add_u32 m0, %[lw], 8192\n\t" \
  "v_mfma_f32_16x16x32_bf16 v[16:19], v[72:75], v[56:59], v[16:19]\n\t" \
  "global_load_lds_dwordx4 v90, s[84:85] sc1\n\t" \
  "s_add_u32 s84, s84, s88\n\t" \
  "s_addc_u32 s85, s85, 0\n\t" \
  "v_mfma_f32_16x16x32_bf16 v[20:23], v[76:79], v[56:59], v[20:23]\n\t" \
  "ds_read_b128 v[56:59], v81 offset:10240\n\t" \
  "v_mfma_f32_16x16x32_bf16 v[24:27], v[72:75], v[60:63], v[24:27]\n\t" \
  "s_add_u32 m0, %[lw], 24576\n\t" \
  "v_mfma_f32_16x16x32_bf16 v[28:31], v[76:79], v[60:63], v[28:31]\n\t" \
  "ds_read_b128 v[60:63], v81 offset:11264\n\t" \
  "global_load_lds_dwordx4 v91, s[86:87]\n\t" \
  "s_add_u32 s86, s86, s89\n\t" \
  "s_addc_u32 s87, s87, 0\n\t" \
  "s_waitcnt vmcnt(4)\n\t" \
  "s_barrier\n\t" \
  "s_waitcnt lgkmcnt(3)\n\t" \
  "v_mfma_f32_16x16x32_bf16 v[0:3], v[64:67], v[32:35], v[0:3]\n\t" \
  "ds_read_b64_tr_b16 v[72:73], v86 offset:8192\n\t" \
  "v_mfma_f32_16x16x32_bf16 v[4:7], v[68:71], v[32:35], v[4:7]\n\t" \
  "ds_read_b64_tr_b16 v[74:75], v87 offset:8192\n\t" \
  "ds_read_b128 v[32:35], v81 offset:32768\n\t" \
  "v_mfma_f32_16x16x32_bf16 v[8:11], v[64:67], v[36:39], v[8:11]\n\t" \
  "ds_read_b64_tr_b16 v[76:77], v88 offset:8192\n\t" \
  "v_mfma_f32_16x16x32_bf16 v[12:15], v[68:71], v[36:39], v[12:15]\n\t" \
  "ds_read_b64_tr_b16 v[78:79], v89 offset:8192\n\t" \
  "ds_read_b128 v[36:39], v81 offset:33792\n\t" \
  "s_add_u32 m0, %[lw], 32768\n\t" \
  "v_mfma_f32_16x16x32_bf16 v[16:19], v[64:67], v[40:43], v[16:19]\n\t" \
  "global_load_lds_dwordx4 v90, s[84:85] sc1\n\t" \
  "s_add_u32 s84, s84, s88\n\t" \
  "s_addc_u32 s85, s85, 0\n\t" \
  "v_mfma_f32_16x16x32_bf16 v[20:23], v[68:71], v[40:43], v[20:23]\n\t" \
  "ds_read_b128 v[40:43], v81 offset:34816\n\t" \
  "v_mfma_f32_16x16x32_bf16 v[24:27], v[64:67], v[44:47], v[24:27]\n\t" \
  "s_add_u32 m0, %[lw], 49152\n\t" \
  "v_mfma_f32_16x16x32_bf16 v[28:31], v[68:71], v[44:47], v[28:31]\n\t" \
  "ds_read_b128 v[44:47], v81 offset:35840\n\t" \
  "global_load_lds_dwordx4 v91, s[86:87]\n\t" \
  "s_add_u32 s86, s86, s89\n\t" \
  "s_addc_u32 s87, s87, 0\n\t" \
  "s_waitcnt lgkmcnt(3)\n\t" \
  "v_mfma_f32_16x16x32_bf16 v[0:3], v[72:75], v[48:51], v[0:3]\n\t" \
  "ds_read_b64_tr_b16 v[64:65], v86 offset:32768\n\t" \
  "v_mfma_f32_16x16x32_bf16 v[4:7], v[76:79], v[48:51], v[4:7]\n\t" \
  "ds_read_b64_tr_b16 v[66:67], v87 offset:32768\n\t" \
  "ds_read_b128 v[48:51], v81 offset:40960\n\t" \
  "v_mfma_f32_16x16x32_bf16 v[8:11], v[72:75], v[52:55], v[8:11]\n\t" \
  "ds_read_b64_tr_b16 v[68:69], v88 offset:32768\n\t" \
  "v_mfma_f32_16x16x32_bf16 v[12:15], v[76:79], v[52:55], v[12:15]\n\t" \
  "ds_read_b64_tr_b16 v[70:71], v89 offset:32768\n\t" \
  "ds_read_b128 v[52:55], v81 offset:41984\n\t" \
  "s_add_u32 m0, %[lw], 40960\n\t" \
  "v_mfma_f32_16x16x32_bf16 v[16:19], v[72:75], v[56:59], v[16:19]\n\t" \
  "global_load_lds_dwordx4 v90, s[84:85] sc1\n\t" \
  "s_add_u32 s84, s84, s88\n\t" \
  "s_addc_u32 s85, s85, 0\n\t" \
  "v_mfma_f32_16x16x32_bf16 v[20:23], v[76:79], v[56:59], v[20:23]\n\t" \
  "ds_read_b128 v[56:59], v81 offset:43008\n\t" \
  "v_mfma_f32_16x16x32_bf16 v[24:27], v[72:75], v[60:63], v[24:27]\n\t" \
  "s_add_u32 m0, %[lw], 57344\n\t" \
  "v_mfma_f32_16x16x32_bf16 v[28:31], v[76:79], v[60:63], v[28:31]\n\t" \
  "ds_read_b128 v[60:63], v81 offset:44032\n\t" \
  "global_load_lds_dwordx4 v91, s[86:87]\n\t" \
  "s_add_u32 s86, s86, s89\n\t" \
  "s_addc_u32 s87, s87, 0\n\t" \
  "s_waitcnt vmcnt(4)\n\t" \
  "s_barrier\n\t" \
  "s_waitcnt lgkmcnt(3)\n\t" \
  "v_mfma_f32_16x16x32_bf16 v[0:3], v[64:67], v[32:35], v[0:3]\n\t" \
  "ds_read_b64_tr_b16 v[72:73], v86 offset:40960\n\t" \
  "v_mfma_f32_16x16x32_bf16 v[4:7], v[68:71], v[32:35], v[4:7]\n\t" \
  "ds_read_b64_tr_b16 v[74:75], v87 offset:40960\n\t" \
  "ds_read_b128 v[32:35], v80 offset:0\n\t" \
  "v_mfma_f32_16x16x32_bf16 v[8:11], v[64:67], v[36:39], v[8:11]\n\t" \
  "ds_read_b64_tr_b16 v[76:77], v88 offset:40960\n\t" \
  "v_mfma_f32_16x16x32_bf16 v[12:15], v[68:71], v[36:39], v[12:15]\n\t" \
  "ds_read_b64_tr_b16 v[78:79], v89 offset:40960\n\t" \
  "ds_read_b128 v[36:39], v80 offset:1024\n\t" \
  "s_add_u32 m0, %[lw], 65536\n\t" \
  "v_mfma_f32_16x16x32_bf16 v[16:19], v[64:67], v[40:43], v[16:19]\n\t" \
  "global_load_lds_dwordx4 v90, s[84:85] sc1\n\t" \
  "s_add_u32 s84, s84, s88\n\t" \
  "s_addc_u32 s85, s85, 0\n\t" \
  "v_mfma_f32_16x16x32_bf16 v[20:23], v[68:71], v[40:43], v[20:23]\n\t" \
  "ds_read_b128 v[40:43], v80 offset:2048\n\t" \
  "v_mfma_f32_16x16x32_bf16 v[24:27], v[64:67], v[44:47], v[24:27]\n\t" \
  "s_add_u32 m0, %[lw], 81920\n\t" \
  "v_mfma_f32_16x16x32_bf16 v[28:31], v[68:71], v[44:47], v[28:31]\n\t" \
  "ds_read_b128 v[44:47], v80 offset:3072\n\t" \
  "global_load_lds_dwordx4 v91, s[86:87]\n\t" \
  "s_add_u32 s86, s86, s89\n\t" \
  "s_addc_u32 s87, s87, 0\n\t" \
  "s_waitcnt lgkmcnt(3)\n\t" \
  "v_mfma_f32_16x16x32_bf16 v[0:3], v[72:75], v[48:51], v[0:3]\n\t" \
  "ds_read_b64_tr_b16 v[64:65], v82 offset:0\n\t" \
  "v_mfma_f32_16x16x32_bf16 v[4:7], v[76:79], v[48:51], v[4:7]\n\t" \
  "ds_read_b64_tr_b16 v[66:67], v83 offset:0\n\t" \
  "ds_read_b128 v[48:51], v80 offset:8192\n\t" \
  "v_mfma_f32_16x16x32_bf16 v[8:11], v[72:75], v[52:55], v[8:11]\n\t" \
  "ds_read_b64_tr_b16 v[68:69], v84 offset:0\n\t" \
  "v_mfma_f32_16x16x32_bf16 v[12:15], v[76:79], v[52:55], v[12:15]\n\t" \
  "ds_read_b64_tr_b16 v[70:71], v85 offset:0\n\t" \
  "ds_read_b128 v[52:55], v80 offset:9216\n\t" \
  "s_add_u32 m0, %[lw], 73728\n\t" \
  "v_mfma_f32_16x16x32_bf16 v[16:19], v[72:75], v[56:59], v[16:19]\n\t" \
  "global_load_lds_dwordx4 v90, s[84:85] sc1\n\t" \
  "s_add_u32 s84, s84, s88\n\t" \
  "s_addc_u32 s85, s85, 0\n\t" \
  "v_mfma_f32_16x16x32_bf16 v[20:23], v[76:79], v[56:59], v[20:23]\n\t" \
  "ds_read_b128 v[56:59], v80 offset:10240\n\t" \
  "v_mfma_f32_16x16x32_bf16 v[24:27], v[72:75], v[60:63], v[24:27]\n\t" \
  "s_add_u32 m0, %[lw], 90112\n\t" \
  "v_mfma_f32_16x16x32_bf16 v[28:31], v[76:79], v[60:63], v[28:31]\n\t" \
  "ds_read_b128 v[60:63], v80 offset:11264\n\t" \
  "global_load_lds_dwordx4 v91, s[86:87]\n\t" \
  "s_add_u32 s86, s86, s89\n\t" \
  "s_addc_u32 s87, s87, 0\n\t" \
  "s_cmp_lg_u32 s88, 0\n\t" \
  "s_cbranch_scc1 .Lp1%=\n\t" \
  ".Le%=:\n\t" \
  "s_waitcnt vmcnt(0) lgkmcnt(0)\n\t" \
  "s_mov_b32 m0, s94\n\t" \
  "s_setprio 0\n\t" \
  "s_nop 7\n\t"
#define HSL 16
__device__ __forceinline__ void job_down(const Args& p, int rblk, int ct, char* smem, __amdgpu_buffer_rsrc_t rsy) {
  if (threadIdx.x < 64) {
    for (unsigned spins = 0;;) {
      asm volatile("" ::: "memory");
      if (__builtin_amdgcn_readfirstlane((int)ld_flag0(rsy, SY_CH + (rblk >> 1) * 128)) >= 27) break;
      __builtin_amdgcn_s_sleep(HSL);
      if (spin_fail(spins, (gu32*)(p.ws + WS_SY + SY_ABORT))) break;
    }
  }
  int tid = threadIdx.x;
  asm volatile("" : "+v"(tid));
  const int lane = tid & 63, r = lane & 15, q = lane >> 4;
  const int wid = __builtin_amdgcn_readfirstlane(tid >> 6), wr = wid >> 2, wc = wid & 3;
  typedef __attribute__((ext_vector_type(32))) float f32x32;
  typedef __attribute__((ext_vector_type(8))) unsigned u32x8;
  f32x32 acc;
  {
    const char* A = p.ws + WS_HF + h_off(p, rblk);
    const char* B = (const char*)(p.wd + ct * 128);
    const unsigned rb = tid >> 4, va = tid * 16, vb = ((rb & 4) * 4 + (rb >> 3) * 4 + (rb & 3)) * (DM * 2) + 16 * ((tid & 15) ^ swzB(rb & 15));
    u32x8 lb;
#pragma unroll
    for (int i = 0; i < 8; ++i)
      lb[i] = (i >> 2) * 65536 + 16384 + offB(8 * q + 4 * (i & 1) + (r >> 2), 2 * (wc * 2 + ((i >> 1) & 1)) + ((r & 3) >> 1)) + 8 * (r & 1);
    LBAR();
#pragma unroll
    for (int t = 0; t < 3; ++t) {
      glds2<true>(A + t * 32768, 16384, va, wid * 1024 + t * 32768);
      glds2(B + t * 64 * DM * 2, 32 * DM * 2, vb, wid * 1024 + t * 32768 + 16384);
    }
    WAIT_V(8);
    LBAR();
    asm volatile(DN_ASM
        : "={v[0:31]}"(acc)
        : "{v[80:81]}"((u32x2){wr * 4096u + lane * 16u, 65536u + wr * 4096u + lane * 16u}), "{v[82:89]}"(lb), "{v90}"(va), "{v91}"(vb),
          [pa] "s"(A + 3 * 32768), [pb] "s"(B + 3 * 64 * DM * 2), [lw] "s"(wid * 1024), [hf] "s"(wr)
        : CLB8(32, 33, 34, 35, 36, 37, 38, 39), CLB8(40, 41, 42, 43, 44, 45, 46, 47), CLB8(48, 49, 50, 51, 52, 53, 54, 55),
          CLB8(56, 57, 58, 59, 60, 61, 62, 63), CLB8(64, 65, 66, 67, 68, 69, 70, 71), CLB8(72, 73, 74, 75, 76, 77, 78, 79),
          "s84", "s85", "s86", "s87", "s88", "s89", "s90", "s91", "s94", "scc", "memory");
  }
  wait_s(p, rsy, rblk, ct * 4, 4, rblk < NFUSE);
  const int e0 = (rblk * 128 + wr * 64 + r) * DM + ct * 128 + wc * 32 + q * 8;
  SX sx[4];
#pragma unroll
  for (int m = 0; m < 4; ++m) sx[m] = get_sx(p, rblk, e0 + m * 16 * DM);
  const __amdgpu_buffer_rsrc_t ro = mkrsrc(p.ws + WS_G, S_LEN * DM * 2);
#pragma unroll
  for (int m = 0; m < 4; ++m) {
    float rr[8];
#pragma unroll
    for (int j = 0; j < 8; ++j) { float d = rbf(acc[8 * m + j]); rr[j] = d * __builtin_amdgcn_rcpf(1.0f + __expf(-d)); }
    put_g(p, ro, rblk, e0 + m * 16 * DM, widen32(cvtpk(rr[0], rr[1]), cvtpk(rr[2], rr[3]), cvtpk(rr[4], rr[5]), cvtpk(rr[6], rr[7])), sx[m]);
  }
  if (rblk >= NFUSE) publish_flags(rsy, SY_FG + (rblk * 8 + ct) * 128, 0, 1);
}

#define PR_ASM_0 \
  "s_cmp_lg_u32 %[kh], 0\n\t" \
  "s_cbranch_scc1 .Lk%=\n\t" \
  "s_mov_b32 s94, m0\n\t" \
  "s_mov_b64 s[92:93], %[x0]\n\t" \
  "s_mov_b32 s86, %[g0]\n\t" \
  "s_add_u32 s87, s86, %[t0]\n\t" \
  "s_sub_i32 s88, s86, 3\n\t" \
  "ds_read_b128 v[176:179], v208 offset:0\n\t" \
  "ds_read_b128 v[180:183], v208 offset:16384\n\t" \
  "ds_read_b128 v[184:187], v209 offset:0\n\t" \
  "ds_read_b128 v[188:191], v209 offset:16384\n\t" \
  "ds_read_b128 v[192:195], v210 offset:0\n\t" \
  "ds_read_b128 v[196:199], v210 offset:16384\n\t" \
  "ds_read_b128 v[200:203], v211 offset:0\n\t" \
  "ds_read_b128 v[204:207], v211 offset:16384\n\t" \
  "s_branch .Lfb00%=\n\t" \
  ".Lp0%=:\n\t" \
  "s_cmp_le_u32 s86, 5\n\t" \
  "s_cbranch_scc1 .Lf00%=\n\t" \
  ".Lfb00%=:\n\t" \
  "s_waitcnt lgkmcnt(6)\n\t" \
  "v_mfma_f32_16x16x32_bf16 v[128:131], v[0:3], v[176:179], 0\n\t" \
  "v_mfma_f32_16x16x32_bf16 v[132:135], v[4:7], v[176:179], 0\n\t" \
  "ds_read_b128 v[176:179], v208 offset:256\n\t" \
  "s_add_u32 m0, %[lw], 67584\n\t" \
  "v_mfma_f32_16x16x32_bf16 v[136:139], v[0:3], v[180:183], 0\n\t" \
  "v_mfma_f32_16x16x32_bf16 v[140:143], v[4:7], v[180:183], 0\n\t" \
  "ds_read_b128 v[180:183], v208 offset:16640\n\t" \
  "global_load_lds_dwordx4 v218, s[92:93] sc1\n\t" \
  "s_waitcnt lgkmcnt(6)\n\t" \
  "v_mfma_f32_16x16x32_bf16 v[128:131], v[8:11], v[184:187], v[128:131]\n\t" \
  "v_mfma_f32_16x16x32_bf16 v[132:135], v[12:15], v[184:187], v[132:135]\n\t" \
  "ds_read_b128 v[184:187], v209 offset:256\n\t" \
  "v_mfma_f32_16x16x32_bf16 v[136:139], v[8:11], v[188:191], v[136:139]\n\t" \
  "v_mfma_f32_16x16x32_bf16 v[140:143], v[12:15], v[188:191], v[140:143]\n\t" \
  "ds_read_b128 v[188:191], v209 offset:16640\n\t" \
  "ds_write_b128 v220, v[152:155] offset:0\n\t" \
  "ds_write_b128 v220, v[156:159] offset:1024\n\t" \
  "s_waitcnt lgkmcnt(8)\n\t" \
  "v_mfma_f32_16x16x32_bf16 v[128:131], v[16:19], v[192:195], v[128:131]\n\t" \
  "v_mfma_f32_16x16x32_bf16 v[132:135], v[20:23], v[192:195], v[132:135]\n\t" \
  "ds_read_b128 v[192:195], v210 offset:256\n\t" \
  "s_add_u32 m0, %[lw], 68608\n\t" \
  "v_mfma_f32_16x16x32_bf16 v[136:139], v[16:19], v[196:199], v[136:139]\n\t" \
  "v_mfma_f32_16x16x32_bf16 v[140:143], v[20:23], v[196:199], v[140:143]\n\t" \
  "ds_read_b128 v[196:199], v210 offset:16640\n\t" \
  "global_load_lds_dwordx4 v219, s[92:93] sc1\n\t" \
  "s_lshr_b32 s90, s87, 7\n\t" \
  "s_xor_b32 s90, s90, %[s0]\n\t" \
  "s_and_b32 s90, s90, 1\n\t" \
  "s_lshl1_add_u32 s90, s87, s90\n\t" \
  "s_lshl_b32 s90, s90, 13\n\t" \
  "s_cmp_lt_i32 s86, 6\n\t" \
  "s_cselect_b32 s90, 0x70000000, s90\n\t" \
  "s_waitcnt lgkmcnt(8)\n\t" \
  "v_mfma_f32_16x16x32_bf16 v[128:131], v[24:27], v[200:203], v[128:131]\n\t" \
  "v_mfma_f32_16x16x32_bf16 v[132:135], v[28:31], v[200:203], v[132:135]\n\t" \
  "ds_read_b128 v[200:203], v211 offset:256\n\t" \
  "v_mfma_f32_16x16x32_bf16 v[136:139], v[24:27], v[204:207], v[136:139]\n\t" \
  "v_mfma_f32_16x16x32_bf16 v[140:143], v[28:31], v[204:207], v[140:143]\n\t" \
  "ds_read_b128 v[204:207], v211 offset:16640\n\t" \
  "s_waitcnt lgkmcnt(4)\n\t" \
  "s_cmp_lt_u32 s86, 6\n\t" \
  "s_cbranch_scc1 .Lw00%=\n\t" \
  "s_waitcnt vmcnt(4)\n\t" \
  "s_barrier\n\t" \
  ".Lg00%=:\n\t" \
  "s_add_u32 s92, s92, 512\n\t" \
  "s_addc_u32 s93, s93, 0\n\t" \
  "v_mfma_f32_16x16x32_bf16 v[128:131], v[32:35], v[176:179], v[128:131]\n\t" \
  "v_mfma_f32_16x16x32_bf16 v[132:135], v[36:39], v[176:179], v[132:135]\n\t" \
  "ds_read_b128 v[176:179], v208 offset:32768\n\t" \
  "s_add_u32 m0, %[lw], 98304\n\t" \
  "v_mfma_f32_16x16x32_bf16 v[136:139], v[32:35], v[180:183], v[136:139]\n\t" \
  "v_mfma_f32_16x16x32_bf16 v[140:143], v[36:39], v[180:183], v[140:143]\n\t" \
  "ds_read_b128 v[180:183], v208 offset:49152\n\t" \
  "global_load_lds_dwordx4 v216, s[92:93] sc1\n\t" \
  "ds_read_b128 v[160:163], v221 offset:0\n\t" \
  "ds_read_b128 v[164:167], v221 offset:1024\n\t" \
  "v_mfma_f32_16x16x32_bf16 v[128:131], v[40:43], v[184:187], v[128:131]\n\t" \
  "v_mfma_f32_16x16x32_bf16 v[132:135], v[44:47], v[184:187], v[132:135]\n\t" \
  "ds_read_b128 v[184:187], v209 offset:32768\n\t" \
  "v_mfma_f32_16x16x32_bf16 v[136:139], v[40:43], v[188:191], v[136:139]\n\t" \
  "v_mfma_f32_16x16x32_bf16 v[140:143], v[44:47], v[188:191], v[140:143]\n\t" \
  "ds_read_b128 v[188:191], v209 offset:49152\n\t" \
  "s_waitcnt lgkmcnt(8)\n\t" \
  "v_mfma_f32_16x16x32_bf16 v[128:131], v[48:51], v[192:195], v[128:131]\n\t" \
  "v_mfma_f32_16x16x32_bf16 v[132:135], v[52:55], v[192:195], v[132:135]\n\t" \
  "ds_read_b128 v[192:195], v210 offset:32768\n\t" \
  "s_add_u32 m0, %[lw], 99328\n\t" \
  "v_mfma_f32_16x16x32_bf16 v[136:139], v[48:51], v[196:199], v[136:139]\n\t" \
  "v_mfma_f32_16x16x32_bf16 v[140:143], v[52:55], v[196:199], v[140:143]\n\t" \
  "ds_read_b128 v[196:199], v210 offset:49152\n\t" \
  "global_load_lds_dwordx4 v217, s[92:93] sc1\n\t" \
  "s_waitcnt lgkmcnt(4)\n\t" \
  "v_add_f32 v160, v160, v144\n\t" \
  "v_add_f32 v161, v161, v145\n\t" \
  "v_add_f32 v162, v162, v146\n\t" \
  "v_add_f32 v163, v163, v147\n\t" \
  "v_add_f32 v164, v164, v148\n\t" \
  "v_add_f32 v165, v165, v149\n\t" \
  "v_add_f32 v166, v166, v150\n\t" \
  "v_add_f32 v167, v167, v151\n\t" \
  "v_mfma_f32_16x16x32_bf16 v[128:131], v[56:59], v[200:203], v[128:131]\n\t" \
  "v_mfma_f32_16x16x32_bf16 v[132:135], v[60:63], v[200:203], v[132:135]\n\t" \
  "ds_read_b128 v[200:203], v211 offset:32768\n\t" \
  "v_mfma_f32_16x16x32_bf16 v[136:139], v[56:59], v[204:207], v[136:139]\n\t" \
  "v_mfma_f32_16x16x32_bf16 v[140:143], v[60:63], v[204:207], v[140:143]\n\t" \
  "ds_read_b128 v[204:207], v211 offset:49152\n\t" \
  "v_cvt_pk_bf16_f32 v168, v160, v161\n\t" \
  "v_cvt_pk_bf16_f32 v169, v162, v163\n\t" \
  "v_cvt_pk_bf16_f32 v170, v164, v165\n\t" \
  "v_cvt_pk_bf16_f32 v171, v166, v167\n\t" \
  "buffer_store_dwordx4 v[168:171], v172, %[rt], s90 offen sc1\n\t" \
  "v_mfma_f32_16x16x32_bf16 v[128:131], v[64:67], v[176:179], v[128:131]\n\t" \
  "v_mfma_f32_16x16x32_bf16 v[132:135], v[68:71], v[176:179], v[132:135]\n\t" \
  "ds_read_b128 v[176:179], v208 offset:33024\n\t" \
  "s_add_u32 m0, %[lw], 100352\n\t" \
  "v_mfma_f32_16x16x32_bf16 v[136:139], v[64:67], v[180:183], v[136:139]\n\t" \
  "v_mfma_f32_16x16x32_bf16 v[140:143], v[68:71], v[180:183], v[140:143]\n\t" \
  "ds_read_b128 v[180:183], v208 offset:49408\n\t" \
  "global_load_lds_dwordx4 v218, s[92:93] sc1\n\t" \
  "s_waitcnt lgkmcnt(6)\n\t" \
  "v_mfma_f32_16x16x32_bf16 v[128:131], v[72:75], v[184:187], v[128:131]\n\t" \
  "v_mfma_f32_16x16x32_bf16 v[132:135], v[76:79], v[184:187], v[132:135]\n\t" \
  "ds_read_b128 v[184:187], v209 offset:33024\n\t" \
  "v_mfma_f32_16x16x32_bf16 v[136:139], v[72:75], v[188:191], v[136:139]\n\t" \
  "v_mfma_f32_16x16x32_bf16 v[140:143], v[76:79], v[188:191], v[140:143]\n\t" \
  "ds_read_b128 v[188:191], v209 offset:49408\n\t" \
  "s_waitcnt lgkmcnt(6)\n\t" \
  "v_mfma_f32_16x16x32_bf16 v[128:131], v[80:83], v[192:195], v[128:131]\n\t" \
  "v_mfma_f32_16x16x32_bf16 v[132:135], v[84:87], v[192:195], v[132:135]\n\t" \
  "ds_read_b128 v[192:195], v210 offset:33024\n\t" \
  "s_add_u32 m0, %[lw], 101376\n\t" \
  "v_mfma_f32_16x16x32_bf16 v[136:139], v[80:83], v[196:199], v[136:139]\n\t" \
  "v_mfma_f32_16x16x32_bf16 v[140:143], v[84:87], v[196:199], v[140:143]\n\t" \
  "ds_read_b128 v[196:199], v210 offset:49408\n\t" \
  "global_load_lds_dwordx4 v219, s[92:93] sc1\n\t" \
  "s_waitcnt lgkmcnt(6)\n\t" \
  "v_mfma_f32_16x16x32_bf16 v[128:131], v[88:91], v[200:203], v[128:131]\n\t" \
  "v_mfma_f32_16x16x32_bf16 v[132:135], v[92:95], v[200:203], v[132:135]\n\t" \
  "ds_read_b128 v[200:203], v211 offset:33024\n\t" \
  "v_mfma_f32_16x16x32_bf16 v[136:139], v[88:91], v[204:207], v[136:139]\n\t" \
  "v_mfma_f32_16x16x32_bf16 v[140:143], v[92:95], v[204:207], v[140:143]\n\t" \
  "ds_read_b128 v[204:207], v211 offset:49408\n\t" \
  "s_cmp_lt_u32 s86, 8\n\t" \
  "s_cbranch_scc1 .Lw10%=\n\t" \
  "s_waitcnt vmcnt(5)\n\t" \
  "s_barrier\n\t" \
  ".Lg10%=:\n\t" \
  "s_add_u32 s92, s92, 65024\n\t" \
  "s_addc_u32 s93, s93, 0\n\t" \
  "s_waitcnt lgkmcnt(6)\n\t" \
  "v_mfma_f32_16x16x32_bf16 v[128:131], v[96:99], v[176:179], v[128:131]\n\t" \
  "v_mfma_f32_16x16x32_bf16 v[132:135], v[100:103], v[176:179], v[132:135]\n\t" \
  "ds_read_b128 v[176:179], v212 offset:0\n\t" \
  "s_add_u32 m0, %[lw], 0\n\t" \
  "v_mfma_f32_16x16x32_bf16 v[136:139], v[96:99], v[180:183], v[136:139]\n\t" \
  "v_mfma_f32_16x16x32_bf16 v[140:143], v[100:103], v[180:183], v[140:143]\n\t" \
  "ds_read_b128 v[180:183], v212 offset:16384\n\t" \
  "global_load_lds_dwordx4 v216, s[92:93] sc1\n\t" \
  "s_waitcnt lgkmcnt(6)\n\t" \
  "v_mfma_f32_16x16x32_bf16 v[128:131], v[104:107], v[184:187], v[128:131]\n\t" \
  "v_mfma_f32_16x16x32_bf16 v[132:135], v[108:111], v[184:187], v[132:135]\n\t" \
  "ds_read_b128 v[184:187], v213 offset:0\n\t" \
  "v_mfma_f32_16x16x32_bf16 v[136:139], v[104:107], v[188:191], v[136:139]\n\t" \
  "v_mfma_f32_16x16x32_bf16 v[140:143], v[108:111], v[188:191], v[140:143]\n\t" \
  "ds_read_b128 v[188:191], v213 offset:16384\n\t" \
  "s_waitcnt lgkmcnt(6)\n\t" \
  "v_mfma_f32_16x16x32_bf16 v[128:131], v[112:115], v[192:195], v[128:131]\n\t" \
  "v_mfma_f32_16x16x32_bf16 v[132:135], v[116:119], v[192:195], v[132:135]\n\t" \
  "ds_read_b128 v[192:195], v214 offset:0\n\t" \
  "s_add_u32 m0, %[lw], 1024\n\t" \
  "v_mfma_f32_16x16x32_bf16 v[136:139], v[112:115], v[196:199], v[136:139]\n\t" \
  "v_mfma_f32_16x16x32_bf16 v[140:143], v[116:119], v[196:199], v[140:143]\n\t" \
  "ds_read_b128 v[196:199], v214 offset:16384\n\t" \
  "global_load_lds_dwordx4 v217, s[92:93] sc1\n\t" \
  "s_waitcnt lgkmcnt(6)\n\t" \
  "v_mfma_f32_16x16x32_bf16 v[128:131], v[120:123], v[200:203], v[128:131]\n\t" \
  "v_mfma_f32_16x16x32_bf16 v[132:135], v[124:127], v[200:203], v[132:135]\n\t" \
  "ds_read_b128 v[200:203], v215 offset:0\n\t" \
  "v_mfma_f32_16x16x32_bf16 v[136:139], v[120:123], v[204:207], v[136:139]\n\t" \
  "v_mfma_f32_16x16x32_bf16 v[140:143], v[124:127], v[204:207], v[140:143]\n\t" \
  "ds_read_b128 v[204:207], v215 offset:16384\n\t" \
  "s_add_u32 s86, s86, 1\n\t" \
  "s_add_u32 s87, s87, 1\n\t" \
  "s_cmp_le_u32 s86, 5\n\t" \
  "s_cbranch_scc1 .Lf10%=\n\t" \
  ".Lfb10%=:\n\t" \
  "s_waitcnt lgkmcnt(6)\n\t" \
  "v_mfma_f32_16x16x32_bf16 v[144:147], v[0:3], v[176:179], 0\n\t" \
  "v_mfma_f32_16x16x32_bf16 v[148:151], v[4:7], v[176:179], 0\n\t" \
  "ds_read_b128 v[176:179], v212 offset:256\n\t" \
  "s_add_u32 m0, %[lw], 2048\n\t" \
  "v_mfma_f32_16x16x32_bf16 v[152:155], v[0:3], v[180:183], 0\n\t" \
  "v_mfma_f32_16x16x32_bf16 v[156:159], v[4:7], v[180:183], 0\n\t" \
  "ds_read_b128 v[180:183], v212 offset:16640\n\t" \
  "global_load_lds_dwordx4 v218, s[92:93] sc1\n\t" \
  "s_waitcnt lgkmcnt(6)\n\t" \
  "v_mfma_f32_16x16x32_bf16 v[144:147], v[8:11], v[184:187], v[144:147]\n\t" \
  "v_mfma_f32_16x16x32_bf16 v[148:151], v[12:15], v[184:187], v[148:151]\n\t" \
  "ds_read_b128 v[184:187], v213 offset:256\n\t" \
  "v_mfma_f32_16x16x32_bf16 v[152:155], v[8:11], v[188:191], v[152:155]\n\t" \
  "v_mfma_f32_16x16x32_bf16 v[156:159], v[12:15], v[188:191], v[156:159]\n\t" \
  "ds_read_b128 v[188:191], v213 offset:16640\n\t" \
  "ds_write_b128 v220, v[136:139] offset:0\n\t" \
  "ds_write_b128 v220, v[140:143] offset:1024\n\t" \
  "s_waitcnt lgkmcnt(8)\n\t" \
  "v_mfma_f32_16x16x32_bf16 v[144:147], v[16:19], v[192:195], v[144:147]\n\t" \
  "v_mfma_f32_16x16x32_bf16 v[148:151], v[20:23], v[192:195], v[148:151]\n\t" \
  "ds_read_b128 v[192:195], v214 offset:256\n\t" \
  "s_add_u32 m0, %[lw], 3072\n\t" \
  "v_mfma_f32_16x16x32_bf16 v[152:155], v[16:19], v[196:199], v[152:155]\n\t" \
  "v_mfma_f32_16x16x32_bf16 v[156:159], v[20:23], v[196:199], v[156:159]\n\t" \
  "ds_read_b128 v[196:199], v214 offset:16640\n\t" \
  "global_load_lds_dwordx4 v219, s[92:93] sc1\n\t" \
  "s_lshr_b32 s90, s87, 7\n\t" \
  "s_xor_b32 s90, s90, %[s0]\n\t" \
  "s_and_b32 s90, s90, 1\n\t" \
  "s_lshl1_add_u32 s90, s87, s90\n\t" \
  "s_lshl_b32 s90, s90, 13\n\t" \
  "s_cmp_lt_i32 s86, 6\n\t" \
  "s_cselect_b32 s90, 0x70000000, s90\n\t" \
  "s_waitcnt lgkmcnt(8)\n\t" \
  "v_mfma_f32_16x16x32_bf16 v[144:147], v[24:27], v[200:203], v[144:147]\n\t" \
  "v_mfma_f32_16x16x32_bf16 v[148:151], v[28:31], v[200:203], v[148:151]\n\t" \
  "ds_read_b128 v[200:203], v215 offset:256\n\t" \
  "v_mfma_f32_16x16x32_bf16 v[152:155], v[24:27], v[204:207], v[152:155]\n\t" \
  "v_mfma_f32_16x16x32_bf16 v[156:159], v[28:31], v[204:207], v[156:159]\n\t" \
  "ds_read_b128 v[204:207], v215 offset:16640\n\t" \
  "s_waitcnt lgkmcnt(4)\n\t" \
  "s_cmp_lt_u32 s86, 6\n\t" \
  "s_cbranch_scc1 .Lw20%=\n\t" \
  "s_waitcnt vmcnt(4)\n\t" \
  "s_barrier\n\t" \
  ".Lg20%=:\n\t" \
  "s_add_u32 s92, s92, 512\n\t" \
  "s_addc_u32 s93, s93, 0\n\t" \
  "v_mfma_f32_16x16x32_bf16 v[144:147], v[32:35], v[176:179], v[144:147]\n\t" \
  "v_mfma_f32_16x16x32_bf16 v[148:151], v[36:39], v[176:179], v[148:151]\n\t" \
  "ds_read_b128 v[176:179], v212 offset:32768\n\t" \
  "s_add_u32 m0, %[lw], 32768\n\t" \
  "v_mfma_f32_16x16x32_bf16 v[152:155], v[32:35], v[180:183], v[152:155]\n\t" \
  "v_mfma_f32_16x16x32_bf16 v[156:159], v[36:39], v[180:183], v[156:159]\n\t" \
  "ds_read_b128 v[180:183], v212 offset:49152\n\t" \
  "global_load_lds_dwordx4 v216, s[92:93] sc1\n\t" \
  "ds_read_b128 v[160:163], v221 offset:0\n\t" \
  "ds_read_b128 v[164:167], v221 offset:1024\n\t" \
  "v_mfma_f32_16x16x32_bf16 v[144:147], v[40:43], v[184:187], v[144:147]\n\t" \
  "v_mfma_f32_16x16x32_bf16 v[148:151], v[44:47], v[184:187], v[148:151]\n\t" \
  "ds_read_b128 v[184:187], v213 offset:32768\n\t" \
  "v_mfma_f32_16x16x32_bf16 v[152:155], v[40:43], v[188:191], v[152:155]\n\t" \
  "v_mfma_f32_16x16x32_bf16 v[156:159], v[44:47], v[188:191], v[156:159]\n\t" \
  "ds_read_b128 v[188:191], v213 offset:49152\n\t" \
  "s_waitcnt lgkmcnt(8)\n\t" \
  "v_mfma_f32_16x16x32_bf16 v[144:147], v[48:51], v[192:195], v[144:147]\n\t" \
  "v_mfma_f32_16x16x32_bf16 v[148:151], v[52:55], v[192:195], v[148:151]\n\t" \
  "ds_read_b128 v[192:195], v214 offset:32768\n\t" \
  "s_add_u32 m0, %[lw], 33792\n\t" \
  "v_mfma_f32_16x16x32_bf16 v[152:155], v[48:51], v[196:199], v[152:155]\n\t" \
  "v_mfma_f32_16x16x32_bf16 v[156:159], v[52:55], v[196:199], v[156:159]\n\t" \
  "ds_read_b128 v[196:199], v214 offset:49152\n\t" \
  "global_load_lds_dwordx4 v217, s[92:93] sc1\n\t" \
  "s_waitcnt lgkmcnt(4)\n\t" \
  "v_add_f32 v160, v160, v128\n\t" \
  "v_add_f32 v161, v161, v129\n\t" \
  "v_add_f32 v162, v162, v130\n\t" \
  "v_add_f32 v163, v163, v131\n\t" \
  "v_add_f32 v164, v164, v132\n\t" \
  "v_add_f32 v165, v165, v133\n\t" \
  "v_add_f32 v166, v166, v134\n\t" \
  "v_add_f32 v167, v167, v135\n\t" \
  "v_mfma_f32_16x16x32_bf16 v[144:147], v[56:59], v[200:203], v[144:147]\n\t" \
  "v_mfma_f32_16x16x32_bf16 v[148:151], v[60:63], v[200:203], v[148:151]\n\t" \
  "ds_read_b128 v[200:203], v215 offset:32768\n\t" \
  "v_mfma_f32_16x16x32_bf16 v[152:155], v[56:59], v[204:207], v[152:155]\n\t" \
  "v_mfma_f32_16x16x32_bf16 v[156:159], v[60:63], v[204:207], v[156:159]\n\t" \
  "ds_read_b128 v[204:207], v215 offset:49152\n\t" \
  "v_cvt_pk_bf16_f32 v168, v160, v161\n\t" \
  "v_cvt_pk_bf16_f32 v169, v162, v163\n\t" \
  "v_cvt_pk_bf16_f32 v170, v164, v165\n\t" \
  "v_cvt_pk_bf16_f32 v171, v166, v167\n\t" \
  "buffer_store_dwordx4 v[168:171], v172, %[rt], s90 offen sc1\n\t" \
  "v_mfma_f32_16x16x32_bf16 v[144:147], v[64:67], v[176:179], v[144:147]\n\t" \
  "v_mfma_f32_16x16x32_bf16 v[148:151], v[68:71], v[176:179], v[148:151]\n\t" \
  "ds_read_b128 v[176:179], v212 offset:33024\n\t" \
  "s_add_u32 m0, %[lw], 34816\n\t" \
  "v_mfma_f32_16x16x32_bf16 v[152:155], v[64:67], v[180:183], v[152:155]\n\t" \
  "v_mfma_f32_16x16x32_bf16 v[156:159], v[68:71], v[180:183], v[156:159]\n\t" \
  "ds_read_b128 v[180:183], v212 offset:49408\n\t" \
  "global_load_lds_dwordx4 v218, s[92:93] sc1\n\t" \
  "s_waitcnt lgkmcnt(6)\n\t" \
  "v_mfma_f32_16x16x32_bf16 v[144:147], v[72:75], v[184:187], v[144:147]\n\t" \
  "v_mfma_f32_16x16x32_bf16 v[148:151], v[76:79], v[184:187], v[148:151]\n\t" \
  "ds_read_b128 v[184:187], v213 offset:33024\n\t" \
  "v_mfma_f32_16x16x32_bf16 v[152:155], v[72:75], v[188:191], v[152:155]\n\t" \
  "v_mfma_f32_16x16x32_bf16 v[156:159], v[76:79], v[188:191], v[156:159]\n\t" \
  "ds_read_b128 v[188:191], v213 offset:49408\n\t" \
  "s_waitcnt lgkmcnt(6)\n\t" \
  "v_mfma_f32_16x16x32_bf16 v[144:147], v[80:83], v[192:195], v[144:147]\n\t" \
  "v_mfma_f32_16x16x32_bf16 v[148:151], v[84:87], v[192:195], v[148:151]\n\t" \
  "ds_read_b128 v[192:195], v214 offset:33024\n\t" \
  "s_add_u32 m0, %[lw], 35840\n\t" \
  "v_mfma_f32_16x16x32_bf16 v[152:155], v[80:83], v[196:199], v[152:155]\n\t" \
  "v_mfma_f32_16x16x32_bf16 v[156:159], v[84:87], v[196:199], v[156:159]\n\t" \
  "ds_read_b128 v[196:199], v214 offset:49408\n\t" \
  "global_load_lds_dwordx4 v219, s[92:93] sc1\n\t" \
  "s_waitcnt lgkmcnt(6)\n\t" \
  "v_mfma_f32_16x16x32_bf16 v[144:147], v[88:91], v[200:203], v[144:147]\n\t" \
  "v_mfma_f32_16x16x32_bf16 v[148:151], v[92:95], v[200:203], v[148:151]\n\t" \
  "ds_read_b128 v[200:203], v215 offset:33024\n\t" \
  "v_mfma_f32_16x16x32_bf16 v[152:155], v[88:91], v[204:207], v[152:155]\n\t" \
  "v_mfma_f32_16x16x32_bf16 v[156:159], v[92:95], v[204:207], v[156:159]\n\t" \
  "ds_read_b128 v[204:207], v215 offset:49408\n\t" \
  "s_cmp_lt_u32 s86, 8\n\t" \
  "s_cbranch_scc1 .Lw30%=\n\t" \
  "s_waitcnt vmcnt(5)\n\t" \
  "s_barrier\n\t" \
  ".Lg30%=:\n\t" \
  "s_add_u32 s92, s92, 65024\n\t" \
  "s_addc_u32 s93, s93, 0\n\t" \
  "s_waitcnt lgkmcnt(6)\n\t" \
  "v_mfma_f32_16x16x32_bf16 v[144:147], v[96:99], v[176:179], v[144:147]\n\t" \
  "v_mfma_f32_16x16x32_bf16 v[148:151], v[100:103], v[176:179], v[148:151]\n\t" \
  "ds_read_b128 v[176:179], v208 offset:0\n\t" \
  "s_add_u32 m0, %[lw], 65536\n\t" \
  "v_mfma_f32_16x16x32_bf16 v[152:155], v[96:99], v[180:183], v[152:155]\n\t" \
  "v_mfma_f32_16x16x32_bf16 v[156:159], v[100:103], v[180:183], v[156:159]\n\t" \
  "ds_read_b128 v[180:183], v208 offset:16384\n\t" \
  "global_load_lds_dwordx4 v216, s[92:93] sc1\n\t" \
  "s_waitcnt lgkmcnt(6)\n\t" \
  "v_mfma_f32_16x16x32_bf16 v[144:147], v[104:107], v[184:187], v[144:147]\n\t" \
  "v_mfma_f32_16x16x32_bf16 v[148:151], v[108:111], v[184:187], v[148:151]\n\t" \
  "ds_read_b128 v[184:187], v209 offset:0\n\t" \
  "v_mfma_f32_16x16x32_bf16 v[152:155], v[104:107], v[188:191], v[152:155]\n\t" \
  "v_mfma_f32_16x16x32_bf16 v[156:159], v[108:111], v[188:191], v[156:159]\n\t" \
  "ds_read_b128 v[188:191], v209 offset:16384\n\t" \
  "s_waitcnt lgkmcnt(6)\n\t" \
  "v_mfma_f32_16x16x32_bf16 v[144:147], v[112:115], v[192:195], v[144:147]\n\t" \
  "v_mfma_f32_16x16x32_bf16 v[148:151], v[116:119], v[192:195], v[148:151]\n\t" \
  "ds_read_b128 v[192:195], v210 offset:0\n\t" \
  "s_add_u32 m0, %[lw], 66560\n\t" \
  "v_mfma_f32_16x16x32_bf16 v[152:155], v[112:115], v[196:199], v[152:155]\n\t" \
  "v_mfma_f32_16x16x32_bf16 v[156:159], v[116:119], v[196:199], v[156:159]\n\t" \
  "ds_read_b128 v[196:199], v210 offset:16384\n\t" \
  "global_load_lds_dwordx4 v217, s[92:93] sc1\n\t" \
  "s_waitcnt lgkmcnt(6)\n\t" \
  "v_mfma_f32_16x16x32_bf16 v[144:147], v[120:123], v[200:203], v[144:147]\n\t" \
  "v_mfma_f32_16x16x32_bf16 v[148:151], v[124:127], v[200:203], v[148:151]\n\t" \
  "ds_read_b128 v[200:203], v211 offset:0\n\t" \
  "v_mfma_f32_16x16x32_bf16 v[152:155], v[120:123], v[204:207], v[152:155]\n\t" \
  "v_mfma_f32_16x16x32_bf16 v[156:159], v[124:127], v[204:207], v[156:159]\n\t" \
  "ds_read_b128 v[204:207], v211 offset:16384\n\t" \
  "s_add_u32 s86, s86, 1\n\t" \
  "s_add_u32 s87, s87, 1\n\t" \
  "s_cmp_lt_u32 s86, %[end]\n\t" \
  "s_cbranch_scc1 .Lp0%=\n\t" \
  "s_branch .Lx0%=\n\t" \
  ".Lf00%=:\n\t" \
  "s_nop 7\n\t" \
  "ds_write_b128 v220, v[152:155] offset:0\n\t" \
  "ds_write_b128 v220, v[156:159] offset:1024\n\t" \
  "s_lshr_b32 s90, s87, 7\n\t" \
  "s_xor_b32 s90, s90, %[s0]\n\t" \
  "s_and_b32 s90, s90, 1\n\t" \
  "s_lshl1_add_u32 s90, s87, s90\n\t" \
  "s_lshl_b32 s90, s90, 13\n\t" \
  "s_cmp_lt_i32 s86, 1\n\t" \
  "s_cselect_b32 s90, 0x70000000, s90\n\t" \
  "s_waitcnt lgkmcnt(0)\n\t" \
  "s_barrier\n\t" \
  "ds_read_b128 v[160:163], v221 offset:0\n\t" \
  "ds_read_b128 v[164:167], v221 offset:1024\n\t" \
  "s_waitcnt lgkmcnt(0)\n\t" \
  "v_add_f32 v160, v160, v144\n\t" \
  "v_add_f32 v161, v161, v145\n\t" \
  "v_add_f32 v162, v162, v146\n\t" \
  "v_add_f32 v163, v163, v147\n\t" \
  "v_add_f32 v164, v164, v148\n\t" \
  "v_add_f32 v165, v165, v149\n\t" \
  "v_add_f32 v166, v166, v150\n\t" \
  "v_add_f32 v167, v167, v151\n\t" \
  "v_cvt_pk_bf16_f32 v168, v160, v161\n\t" \
  "v_cvt_pk_bf16_f32 v169, v162, v163\n\t" \
  "v_cvt_pk_bf16_f32 v170, v164, v165\n\t" \
  "v_cvt_pk_bf16_f32 v171, v166, v167\n\t" \
  "buffer_store_dwordx4 v[168:171], v172, %[rt], s90 offen sc1\n\t" \
  "s_branch .Lfb00%=\n\t" \
  ".Lf10%=:\n\t" \
  "s_nop 7\n\t" \
  "ds_write_b128 v220, v[136:139] offset:0\n\t" \
  "ds_write_b128 v220, v[140:143] offset:1024\n\t" \
  "s_lshr_b32 s90, s87, 7\n\t" \
  "s_xor_b32 s90, s90, %[s0]\n\t" \
  "s_and_b32 s90, s90, 1\n\t" \
  "s_lshl1_add_u32 s90, s87, s90\n\t" \
  "s_lshl_b32 s90, s90, 13\n\t" \
  "s_cmp_lt_i32 s86, 1\n\t" \
  "s_cselect_b32 s90, 0x70000000, s90\n\t" \
  "s_waitcnt lgkmcnt(0)\n\t" \
  "s_barrier\n\t" \
  "ds_read_b128 v[160:163], v221 offset:0\n\t" \
  "ds_read_b128 v[164:167], v221 offset:1024\n\t" \
  "s_waitcnt lgkmcnt(0)\n\t" \
  "v_add_f32 v160, v160, v128\n\t" \
  "v_add_f32 v161, v161, v129\n\t" \
  "v_add_f32 v162, v162, v130\n\t" \
  "v_add_f32 v163, v163, v131\n\t" \
  "v_add_f32 v164, v164, v132\n\t" \
  "v_add_f32 v165, v165, v133\n\t" \
  "v_add_f32 v166, v166, v134\n\t" \
  "v_add_f32 v167, v167, v135\n\t" \
  "v_cvt_pk_bf16_f32 v168, v160, v161\n\t" \
  "v_cvt_pk_bf16_f32 v169, v162, v163\n\t" \
  "v_cvt_pk_bf16_f32 v170, v164, v165\n\t" \
  "v_cvt_pk_bf16_f32 v171, v166, v167\n\t" \
  "buffer_store_dwordx4 v[168:171], v172, %[rt], s90 offen sc1\n\t" \
  "s_branch .Lfb10%=\n\t" \
  ".Lw00%=:\n\t" \
  "s_waitcnt vmcnt(2)\n\t" \
  "s_barrier\n\t" \
  "s_branch .Lg00%=\n\t" \
  ".Lw10%=:\n\t" \
  "s_waitcnt vmcnt(2)\n\t" \
  "s_barrier\n\t" \
  "s_branch .Lg10%=\n\t" \
  ".Lw20%=:\n\t" \
  "s_waitcnt vmcnt(2)\n\t" \
  "s_barrier\n\t" \
  "s_branch .Lg20%=\n\t" \
  ".Lw30%=:\n\t" \
  "s_waitcnt vmcnt(2)\n\t" \
  "s_barrier\n\t" \
  "s_branch .Lg30%=\n\t" \
  ".Lx0%=:\n\t" \
  "s_waitcnt lgkmcnt(0)\n\t" \
  "s_setprio 0\n\t" \
  "s_mov_b32 m0, s94\n\t" \
  "s_branch .Le%=\n\t" \
  ".Lk%=:\n\t" \
  "s_mov_b32 s94, m0\n\t" \
  "s_mov_b64 s[92:93], %[x0]\n\t" \
  "s_mov_b32 s86, %[g0]\n\t" \
  "s_add_u32 s87, s86, %[t0]\n\t" \
  "s_sub_i32 s88, s86, 3\n\t" \
  "ds_read_b128 v[176:179], v208 offset:0\n\t" \
  "ds_read_b128 v[180:183], v208 offset:16384\n\t" \
  "ds_read_b128 v[184:187], v209 offset:0\n\t" \
  "ds_read_b128 v[188:191], v209 offset:16384\n\t" \
  "ds_read_b128 v[192:195], v210 offset:0\n\t" \
  "ds_read_b128 v[196:199], v210 offset:16384\n\t" \
  "ds_read_b128 v[200:203], v211 offset:0\n\t" \
  "ds_read_b128 v[204:207], v211 offset:16384\n\t" \
  "s_branch .Lfb01%=\n\t" \
  ".Lp1%=:\n\t" \
  "s_cmp_le_u32 s86, 5\n\t" \
  "s_cbranch_scc1 .Lf01%=\n\t" \
  ".Lfb01%=:\n\t" \
  "s_waitcnt lgkmcnt(6)\n\t" \
  "v_mfma_f32_16x16x32_bf16 v[128:131], v[0:3], v[176:179], 0\n\t" \
  "v_mfma_f32_16x16x32_bf16 v[132:135], v[4:7], v[176:179], 0\n\t" \
  "ds_read_b128 v[176:179], v208 offset:256\n\t" \
  "v_mfma_f32_16x16x32_bf16 v[136:139], v[0:3], v[180:183], 0\n\t" \
  "v_mfma_f32_16x16x32_bf16 v[140:143], v[4:7], v[180:183], 0\n\t" \
  "ds_read_b128 v[180:183], v208 offset:16640\n\t" \
  "s_waitcnt lgkmcnt(6)\n\t" \
  "v_mfma_f32_16x16x32_bf16 v[128:131], v[8:11], v[184:187], v[128:131]\n\t" \
  "v_mfma_f32_16x16x32_bf16 v[132:135], v[12:15], v[184:187], v[132:135]\n\t" \
  "ds_read_b128 v[184:187], v209 offset:256\n\t" \
  "s_add_u32 m0, %[lw], 67584\n\t" \
  "v_mfma_f32_16x16x32_bf16 v[136:139], v[8:11], v[188:191], v[136:139]\n\t" \
  "v_mfma_f32_16x16x32_bf16 v[140:143], v[12:15], v[188:191], v[140:143]\n\t" \
  "ds_read_b128 v[188:191], v209 offset:16640\n\t" \
  "global_load_lds_dwordx4 v218, s[92:93] sc1\n\t" \
  "ds_write_b128 v220, v[152:155] offset:0\n\t" \
  "ds_write_b128 v220, v[156:159] offset:1024\n\t" \
  "s_waitcnt lgkmcnt(8)\n\t" \
  "v_mfma_f32_16x16x32_bf16 v[128:131], v[16:19], v[192:195], v[128:131]\n\t" \
  "v_mfma_f32_16x16x32_bf16 v[132:135], v[20:23], v[192:195], v[132:135]\n\t" \
  "ds_read_b128 v[192:195], v210 offset:256\n\t" \
  "v_mfma_f32_16x16x32_bf16 v[136:139], v[16:19], v[196:199], v[136:139]\n\t" \
  "v_mfma_f32_16x16x32_bf16 v[140:143], v[20:23], v[196:199], v[140:143]\n\t" \
  "ds_read_b128 v[196:199], v210 offset:16640\n\t" \
  "s_lshr_b32 s90, s87, 7\n\t" \
  "s_xor_b32 s90, s90, %[s0]\n\t" \
  "s_and_b32 s90, s90, 1\n\t" \
  "s_lshl1_add_u32 s90, s87, s90\n\t" \
  "s_lshl_b32 s90, s90, 13\n\t" \
  "s_cmp_lt_i32 s86, 6\n\t" \
  "s_cselect_b32 s90, 0x70000000, s90\n\t" \
  "s_waitcnt lgkmcnt(8)\n\t" \
  "v_mfma_f32_16x16x32_bf16 v[128:131], v[24:27], v[200:203], v[128:131]\n\t" \
  "v_mfma_f32_16x16x32_bf16 v[132:135], v[28:31], v[200:203], v[132:135]\n\t" \
  "ds_read_b128 v[200:203], v211 offset:256\n\t" \
  "s_add_u32 m0, %[lw], 68608\n\t" \
  "v_mfma_f32_16x16x32_bf16 v[136:139], v[24:27], v[204:207], v[136:139]\n\t" \
  "v_mfma_f32_16x16x32_bf16 v[140:143], v[28:31], v[204:207], v[140:143]\n\t" \
  "ds_read_b128 v[204:207], v211 offset:16640\n\t" \
  "global_load_lds_dwordx4 v219, s[92:93] sc1\n\t" \
  "s_waitcnt lgkmcnt(4)\n\t" \
  "s_cmp_lt_u32 s86, 6\n\t" \
  "s_cbranch_scc1 .Lw01%=\n\t" \
  "s_waitcnt vmcnt(4)\n\t" \
  "s_barrier\n\t" \
  ".Lg01%=:\n\t" \
  "s_add_u32 s92, s92, 512\n\t" \
  "s_addc_u32 s93, s93, 0\n\t" \
  "v_mfma_f32_16x16x32_bf16 v[128:131], v[32:35], v[176:179], v[128:131]\n\t" \
  "v_mfma_f32_16x16x32_bf16 v[132:135], v[36:39], v[176:179], v[132:135]\n\t" \
  "ds_read_b128 v[176:179], v208 offset:32768\n\t" \
  "v_mfma_f32_16x16x32_bf16 v[136:139], v[32:35], v[180:183], v[136:139]\n\t" \
  "v_mfma_f32_16x16x32_bf16 v[140:143], v[36:39], v[180:183], v[140:143]\n\t" \
  "ds_read_b128 v[180:183], v208 offset:49152\n\t" \
  "ds_read_b128 v[160:163], v221 offset:0\n\t" \
  "ds_read_b128 v[164:167], v221 offset:1024\n\t" \
  "s_sub_i32 s91, s86, 2\n\t" \
  "s_max_i32 s88, s88, s91\n\t" \
  "v_mov_b32 v173, s88\n\t" \
  "buffer_store_dword v173, v174, %[rs], 0 offen sc1\n\t" \
  "v_mfma_f32_16x16x32_bf16 v[128:131], v[40:43], v[184:187], v[128:131]\n\t" \
  "v_mfma_f32_16x16x32_bf16 v[132:135], v[44:47], v[184:187], v[132:135]\n\t" \
  "ds_read_b128 v[184:187], v209 offset:32768\n\t" \
  "s_add_u32 m0, %[lw], 98304\n\t" \
  "v_mfma_f32_16x16x32_bf16 v[136:139], v[40:43], v[188:191], v[136:139]\n\t" \
  "v_mfma_f32_16x16x32_bf16 v[140:143], v[44:47], v[188:191], v[140:143]\n\t" \
  "ds_read_b128 v[188:191], v209 offset:49152\n\t" \
  "global_load_lds_dwordx4 v216, s[92:93] sc1\n\t" \
  "s_waitcnt lgkmcnt(8)\n\t" \
  "v_mfma_f32_16x16x32_bf16 v[128:131], v[48:51], v[192:195], v[128:131]\n\t" \
  "v_mfma_f32_16x16x32_bf16 v[132:135], v[52:55], v[192:195], v[132:135]\n\t" \
  "ds_read_b128 v[192:195], v210 offset:32768\n\t" \
  "v_mfma_f32_16x16x32_bf16 v[136:139], v[48:51], v[196:199], v[136:139]\n\t" \
  "v_mfma_f32_16x16x32_bf16 v[140:143], v[52:55], v[196:199], v[140:143]\n\t" \
  "ds_read_b128 v[196:199], v210 offset:49152\n\t" \
  "s_waitcnt lgkmcnt(4)\n\t" \
  "v_add_f32 v160, v160, v144\n\t" \
  "v_add_f32 v161, v161, v145\n\t" \
  "v_add_f32 v162, v162, v146\n\t" \
  "v_add_f32 v163, v163, v147\n\t" \
  "v_add_f32 v164, v164, v148\n\t" \
  "v_add_f32 v165, v165, v149\n\t" \
  "v_add_f32 v166, v166, v150\n\t" \
  "v_add_f32 v167, v167, v151\n\t" \
  "v_mfma_f32_16x16x32_bf16 v[128:131], v[56:59], v[200:203], v[128:131]\n\t" \
  "v_mfma_f32_16x16x32_bf16 v[132:135], v[60:63], v[200:203], v[132:135]\n\t" \
  "ds_read_b128 v[200:203], v211 offset:32768\n\t" \
  "s_add_u32 m0, %[lw], 99328\n\t" \
  "v_mfma_f32_16x16x32_bf16 v[136:139], v[56:59], v[204:207], v[136:139]\n\t" \
  "v_mfma_f32_16x16x32_bf16 v[140:143], v[60:63], v[204:207], v[140:143]\n\t" \
  "ds_read_b128 v[204:207], v211 offset:49152\n\t" \
  "global_load_lds_dwordx4 v217, s[92:93] sc1\n\t" \
  "v_cvt_pk_bf16_f32 v168, v160, v161\n\t" \
  "v_cvt_pk_bf16_f32 v169, v162, v163\n\t" \
  "v_cvt_pk_bf16_f32 v170, v164, v165\n\t" \
  "v_cvt_pk_bf16_f32 v171, v166, v167\n\t" \
  "buffer_store_dwordx4 v[168:171], v172, %[rt], s90 offen sc1\n\t" \
  "v_mfma_f32_16x16x32_bf16 v[128:131], v[64:67], v[176:179], v[128:131]\n\t" \
  "v_mfma_f32_16x16x32_bf16 v[132:135], v[68:71], v[176:179], v[132:135]\n\t" \
  "ds_read_b128 v[176:179], v208 offset:33024\n\t" \
  "v_mfma_f32_16x16x32_bf16 v[136:139], v[64:67], v[180:183], v[136:139]\n\t" \
  "v_mfma_f32_16x16x32_bf16 v[140:143], v[68:71], v[180:183], v[140:143]\n\t" \
  "ds_read_b128 v[180:183], v208 offset:49408\n\t" \
  "s_waitcnt lgkmcnt(6)\n\t" \
  "v_mfma_f32_16x16x32_bf16 v[128:131], v[72:75], v[184:187], v[128:131]\n\t" \
  "v_mfma_f32_16x16x32_bf16 v[132:135], v[76:79], v[184:187], v[132:135]\n\t" \
  "ds_read_b128 v[184:187], v209 offset:33024\n\t" \
  "s_add_u32 m0, %[lw], 100352\n\t" \
  "v_mfma_f32_16x16x32_bf16 v[136:139], v[72:75], v[188:191], v[136:139]\n\t" \
  "v_mfma_f32_16x16x32_bf16 v[140:143], v[76:79], v[188:191], v[140:143]\n\t" \
  "ds_read_b128 v[188:191], v209 offset:49408\n\t" \
  "global_load_lds_dwordx4 v218, s[92:93] sc1\n\t" \
  "s_waitcnt lgkmcnt(6)\n\t" \
  "v_mfma_f32_16x16x32_bf16 v[128:131], v[80:83], v[192:195], v[128:131]\n\t" \
  "v_mfma_f32_16x16x32_bf16 v[132:135], v[84:87], v[192:195], v[132:135]\n\t" \
  "ds_read_b128 v[192:195], v210 offset:33024\n\t" \
  "v_mfma_f32_16x16x32_bf16 v[136:139], v[80:83], v[196:199], v[136:139]\n\t" \
  "v_mfma_f32_16x16x32_bf16 v[140:143], v[84:87], v[196:199], v[140:143]\n\t" \
  "ds_read_b128 v[196:199], v210 offset:49408\n\t" \
  "s_waitcnt lgkmcnt(6)\n\t" \
  "v_mfma_f32_16x16x32_bf16 v[128:131], v[88:91], v[200:203], v[128:131]\n\t" \
  "v_mfma_f32_16x16x32_bf16 v[132:135], v[92:95], v[200:203], v[132:135]\n\t" \
  "ds_read_b128 v[200:203], v211 offset:33024\n\t" \
  "s_add_u32 m0, %[lw], 101376\n\t" \
  "v_mfma_f32_16x16x32_bf16 v[136:139], v[88:91], v[204:207], v[136:139]\n\t" \
  "v_mfma_f32_16x16x32_bf16 v[140:143], v[92:95], v[204:207], v[140:143]\n\t" \
  "ds_read_b128 v[204:207], v211 offset:49408\n\t" \
  "global_load_lds_dwordx4 v219, s[92:93] sc1\n\t" \
  "s_cmp_lt_u32 s86, 8\n\t" \
  "s_cbranch_scc1 .Lw11%=\n\t" \
  "s_waitcnt vmcnt(6)\n\t" \
  "s_barrier\n\t" \
  ".Lg11%=:\n\t" \
  "s_add_u32 s92, s92, 65024\n\t" \
  "s_addc_u32 s93, s93, 0\n\t" \
  "s_waitcnt lgkmcnt(6)\n\t" \
  "v_mfma_f32_16x16x32_bf16 v[128:131], v[96:99], v[176:179], v[128:131]\n\t" \
  "v_mfma_f32_16x16x32_bf16 v[132:135], v[100:103], v[176:179], v[132:135]\n\t" \
  "ds_read_b128 v[176:179], v212 offset:0\n\t" \
  "v_mfma_f32_16x16x32_bf16 v[136:139], v[96:99], v[180:183], v[136:139]\n\t" \
  "v_mfma_f32_16x16x32_bf16 v[140:143], v[100:103], v[180:183], v[140:143]\n\t" \
  "ds_read_b128 v[180:183], v212 offset:16384\n\t" \
  "s_waitcnt lgkmcnt(6)\n\t" \
  "v_mfma_f32_16x16x32_bf16 v[128:131], v[104:107], v[184:187], v[128:131]\n\t" \
  "v_mfma_f32_16x16x32_bf16 v[132:135], v[108:111], v[184:187], v[132:135]\n\t" \
  "ds_read_b128 v[184:187], v213 offset:0\n\t" \
  "s_add_u32 m0, %[lw], 0\n\t" \
  "v_mfma_f32_16x16x32_bf16 v[136:139], v[104:107], v[188:191], v[136:139]\n\t" \
  "v_mfma_f32_16x16x32_bf16 v[140:143], v[108:111], v[188:191], v[140:143]\n\t" \
  "ds_read_b128 v[188:191], v213 offset:16384\n\t" \
  "global_load_lds_dwordx4 v216, s[92:93] sc1\n\t" \
  "s_waitcnt lgkmcnt(6)\n\t" \
  "v_mfma_f32_16x16x32_bf16 v[128:131], v[112:115], v[192:195], v[128:131]\n\t" \
  "v_mfma_f32_16x16x32_bf16 v[132:135], v[116:119], v[192:195], v[132:135]\n\t" \
  "ds_read_b128 v[192:195], v214 offset:0\n\t" \
  "v_mfma_f32_16x16x32_bf16 v[136:139], v[112:115], v[196:199], v[136:139]\n\t" \
  "v_mfma_f32_16x16x32_bf16 v[140:143], v[116:119], v[196:199], v[140:143]\n\t" \
  "ds_read_b128 v[196:199], v214 offset:16384\n\t" \
  "s_waitcnt lgkmcnt(6)\n\t" \
  "v_mfma_f32_16x16x32_bf16 v[128:131], v[120:123], v[200:203], v[128:131]\n\t" \
  "v_mfma_f32_16x16x32_bf16 v[132:135], v[124:127], v[200:203], v[132:135]\n\t" \
  "ds_read_b128 v[200:203], v215 offset:0\n\t" \
  "s_add_u32 m0, %[lw], 1024\n\t" \
  "v_mfma_f32_16x16x32_bf16 v[136:139], v[120:123], v[204:207], v[136:139]\n\t" \
  "v_mfma_f32_16x16x32_bf16 v[140:143], v[124:127], v[204:207], v[140:143]\n\t" \
  "ds_read_b128 v[204:207], v215 offset:16384\n\t" \
  "global_load_lds_dwordx4 v217, s[92:93] sc1\n\t" \
  "s_add_u32 s86, s86, 1\n\t" \
  "s_add_u32 s87, s87, 1\n\t" \
  "s_cmp_le_u32 s86, 5\n\t" \
  "s_cbranch_scc1 .Lf11%=\n\t" \
  ".Lfb11%=:\n\t" \
  "s_waitcnt lgkmcnt(6)\n\t" \
  "v_mfma_f32_16x16x32_bf16 v[144:147], v[0:3], v[176:179], 0\n\t" \
  "v_mfma_f32_16x16x32_bf16 v[148:151], v[4:7], v[176:179], 0\n\t" \
  "ds_read_b128 v[176:179], v212 offset:256\n\t" \
  "v_mfma_f32_16x16x32_bf16 v[152:155], v[0:3], v[180:183], 0\n\t" \
  "v_mfma_f32_16x16x32_bf16 v[156:159], v[4:7], v[180:183], 0\n\t" \
  "ds_read_b128 v[180:183], v212 offset:16640\n\t" \
  "s_waitcnt lgkmcnt(6)\n\t" \
  "v_mfma_f32_16x16x32_bf16 v[144:147], v[8:11], v[184:187], v[144:147]\n\t" \
  "v_mfma_f32_16x16x32_bf16 v[148:151], v[12:15], v[184:187], v[148:151]\n\t" \
  "ds_read_b128 v[184:187], v213 offset:256\n\t" \
  "s_add_u32 m0, %[lw], 2048\n\t" \
  "v_mfma_f32_16x16x32_bf16 v[152:155], v[8:11], v[188:191], v[152:155]\n\t" \
  "v_mfma_f32_16x16x32_bf16 v[156:159], v[12:15], v[188:191], v[156:159]\n\t" \
  "ds_read_b128 v[188:191], v213 offset:16640\n\t" \
  "global_load_lds_dwordx4 v218, s[92:93] sc1\n\t" \
  "ds_write_b128 v220, v[136:139] offset:0\n\t" \
  "ds_write_b128 v220, v[140:143] offset:1024\n\t" \
  "s_waitcnt lgkmcnt(8)\n\t" \
  "v_mfma_f32_16x16x32_bf16 v[144:147], v[16:19], v[192:195], v[144:147]\n\t" \
  "v_mfma_f32_16x16x32_bf16 v[148:151], v[20:23], v[192:195], v[148:151]\n\t" \
  "ds_read_b128 v[192:195], v214 offset:256\n\t" \
  "v_mfma_f32_16x16x32_bf16 v[152:155], v[16:19], v[196:199], v[152:155]\n\t" \
  "v_mfma_f32_16x16x32_bf16 v[156:159], v[20:23], v[196:199], v[156:159]\n\t" \
  "ds_read_b128 v[196:199], v214 offset:16640\n\t" \
  "s_lshr_b32 s90, s87, 7\n\t" \
  "s_xor_b32 s90, s90, %[s0]\n\t" \
  "s_and_b32 s90, s90, 1\n\t" \
  "s_lshl1_add_u32 s90, s87, s90\n\t" \
  "s_lshl_b32 s90, s90, 13\n\t" \
  "s_cmp_lt_i32 s86, 6\n\t" \
  "s_cselect_b32 s90, 0x70000000, s90\n\t" \
  "s_waitcnt lgkmcnt(8)\n\t" \
  "v_mfma_f32_16x16x32_bf16 v[144:147], v[24:27], v[200:203], v[144:147]\n\t" \
  "v_mfma_f32_16x16x32_bf16 v[148:151], v[28:31], v[200:203], v[148:151]\n\t" \
  "ds_read_b128 v[200:203], v215 offset:256\n\t" \
  "s_add_u32 m0, %[lw], 3072\n\t" \
  "v_mfma_f32_16x16x32_bf16 v[152:155], v[24:27], v[204:207], v[152:155]\n\t" \
  "v_mfma_f32_16x16x32_bf16 v[156:159], v[28:31], v[204:207], v[156:159]\n\t" \
  "ds_read_b128 v[204:207], v215 offset:16640\n\t" \
  "global_load_lds_dwordx4 v219, s[92:93] sc1\n\t" \
  "s_waitcnt lgkmcnt(4)\n\t" \
  "s_cmp_lt_u32 s86, 6\n\t" \
  "s_cbranch_scc1 .Lw21%=\n\t" \
  "s_waitcnt vmcnt(4)\n\t" \
  "s_barrier\n\t" \
  ".Lg21%=:\n\t" \
  "s_add_u32 s92, s92, 512\n\t" \
  "s_addc_u32 s93, s93, 0\n\t" \
  "v_mfma_f32_16x16x32_bf16 v[144:147], v[32:35], v[176:179], v[144:147]\n\t" \
  "v_mfma_f32_16x16x32_bf16 v[148:151], v[36:39], v[176:179], v[148:151]\n\t" \
  "ds_read_b128 v[176:179], v212 offset:32768\n\t" \
  "v_mfma_f32_16x16x32_bf16 v[152:155], v[32:35], v[180:183], v[152:155]\n\t" \
  "v_mfma_f32_16x16x32_bf16 v[156:159], v[36:39], v[180:183], v[156:159]\n\t" \
  "ds_read_b128 v[180:183], v212 offset:49152\n\t" \
  "ds_read_b128 v[160:163], v221 offset:0\n\t" \
  "ds_read_b128 v[164:167], v221 offset:1024\n\t" \
  "s_sub_i32 s91, s86, 2\n\t" \
  "s_max_i32 s88, s88, s91\n\t" \
  "v_mov_b32 v173, s88\n\t" \
  "buffer_store_dword v173, v174, %[rs], 0 offen sc1\n\t" \
  "v_mfma_f32_16x16x32_bf16 v[144:147], v[40:43], v[184:187], v[144:147]\n\t" \
  "v_mfma_f32_16x16x32_bf16 v[148:151], v[44:47], v[184:187], v[148:151]\n\t" \
  "ds_read_b128 v[184:187], v213 offset:32768\n\t" \
  "s_add_u32 m0, %[lw], 32768\n\t" \
  "v_mfma_f32_16x16x32_bf16 v[152:155], v[40:43], v[188:191], v[152:155]\n\t" \
  "v_mfma_f32_16x16x32_bf16 v[156:159], v[44:47], v[188:191], v[156:159]\n\t" \
  "ds_read_b128 v[188:191], v213 offset:49152\n\t" \
  "global_load_lds_dwordx4 v216, s[92:93] sc1\n\t" \
  "s_waitcnt lgkmcnt(8)\n\t" \
  "v_mfma_f32_16x16x32_bf16 v[144:147], v[48:51], v[192:195], v[144:147]\n\t" \
  "v_mfma_f32_16x16x32_bf16 v[148:151], v[52:55], v[192:195], v[148:151]\n\t" \
  "ds_read_b128 v[192:195], v214 offset:32768\n\t" \
  "v_mfma_f32_16x16x32_bf16 v[152:155], v[48:51], v[196:199], v[152:155]\n\t" \
  "v_mfma_f32_16x16x32_bf16 v[156:159], v[52:55], v[196:199], v[156:159]\n\t" \
  "ds_read_b128 v[196:199], v214 offset:49152\n\t" \
  "s_waitcnt lgkmcnt(4)\n\t" \
  "v_add_f32 v160, v160, v128\n\t" \
  "v_add_f32 v161, v161, v129\n\t" \
  "v_add_f32 v162, v162, v130\n\t" \
  "v_add_f32 v163, v163, v131\n\t" \
  "v_add_f32 v164, v164, v132\n\t" \
  "v_add_f32 v165, v165, v133\n\t" \
  "v_add_f32 v166, v166, v134\n\t" \
  "v_add_f32 v167, v167, v135\n\t" \
  "v_mfma_f32_16x16x32_bf16 v[144:147], v[56:59], v[200:203], v[144:147]\n\t" \
  "v_mfma_f32_16x16x32_bf16 v[148:151], v[60:63], v[200:203], v[148:151]\n\t" \
  "ds_read_b128 v[200:203], v215 offset:32768\n\t" \
  "s_add_u32 m0, %[lw], 33792\n\t" \
  "v_mfma_f32_16x16x32_bf16 v[152:155], v[56:59], v[204:207], v[152:155]\n\t" \
  "v_mfma_f32_16x16x32_bf16 v[156:159], v[60:63], v[204:207], v[156:159]\n\t" \
  "ds_read_b128 v[204:207], v215 offset:49152\n\t" \
  "global_load_lds_dwordx4 v217, s[92:93] sc1\n\t" \
  "v_cvt_pk_bf16_f32 v168, v160, v161\n\t" \
  "v_cvt_pk_bf16_f32 v169, v162, v163\n\t" \
  "v_cvt_pk_bf16_f32 v170, v164, v165\n\t" \
  "v_cvt_pk_bf16_f32 v171, v166, v167\n\t" \
  "buffer_store_dwordx4 v[168:171], v172, %[rt], s90 offen sc1\n\t" \
  "v_mfma_f32_16x16x32_bf16 v[144:147], v[64:67], v[176:179], v[144:147]\n\t" \
  "v_mfma_f32_16x16x32_bf16 v[148:151], v[68:71], v[176:179], v[148:151]\n\t" \
  "ds_read_b128 v[176:179], v212 offset:33024\n\t" \
  "v_mfma_f32_16x16x32_bf16 v[152:155], v[64:67], v[180:183], v[152:155]\n\t" \
  "v_mfma_f32_16x16x32_bf16 v[156:159], v[68:71], v[180:183], v[156:159]\n\t" \
  "ds_read_b128 v[180:183], v212 offset:49408\n\t" \
  "s_waitcnt lgkmcnt(6)\n\t" \
  "v_mfma_f32_16x16x32_bf16 v[144:147], v[72:75], v[184:187], v[144:147]\n\t" \
  "v_mfma_f32_16x16x32_bf16 v[148:151], v[76:79], v[184:187], v[148:151]\n\t" \
  "ds_read_b128 v[184:187], v213 offset:33024\n\t" \
  "s_add_u32 m0, %[lw], 34816\n\t" \
  "v_mfma_f32_16x16x32_bf16 v[152:155], v[72:75], v[188:191], v[152:155]\n\t" \
  "v_mfma_f32_16x16x32_bf16 v[156:159], v[76:79], v[188:191], v[156:159]\n\t" \
  "ds_read_b128 v[188:191], v213 offset:49408\n\t" \
  "global_load_lds_dwordx4 v218, s[92:93] sc1\n\t" \
  "s_waitcnt lgkmcnt(6)\n\t" \
  "v_mfma_f32_16x16x32_bf16 v[144:147], v[80:83], v[192:195], v[144:147]\n\t" \
  "v_mfma_f32_16x16x32_bf16 v[148:151], v[84:87], v[192:195], v[148:151]\n\t" \
  "ds_read_b128 v[192:195], v214 offset:33024\n\t" \
  "v_mfma_f32_16x16x32_bf16 v[152:155], v[80:83], v[196:199], v[152:155]\n\t" \
  "v_mfma_f32_16x16x32_bf16 v[156:159], v[84:87], v[196:199], v[156:159]\n\t" \
  "ds_read_b128 v[196:199], v214 offset:49408\n\t" \
  "s_waitcnt lgkmcnt(6)\n\t" \
  "v_mfma_f32_16x16x32_bf16 v[144:147], v[88:91], v[200:203], v[144:147]\n\t" \
  "v_mfma_f32_16x16x32_bf16 v[148:151], v[92:95], v[200:203], v[148:151]\n\t" \
  "ds_read_b128 v[200:203], v215 offset:33024\n\t" \
  "s_add_u32 m0, %[lw], 35840\n\t" \
  "v_mfma_f32_16x16x32_bf16 v[152:155], v[88:91], v[204:207], v[152:155]\n\t" \
  "v_mfma_f32_16x16x32_bf16 v[156:159], v[92:95], v[204:207], v[156:159]\n\t" \
  "ds_read_b128 v[204:207], v215 offset:49408\n\t" \
  "global_load_lds_dwordx4 v219, s[92:93] sc1\n\t" \
  "s_cmp_lt_u32 s86, 8\n\t" \
  "s_cbranch_scc1 .Lw31%=\n\t" \
  "s_waitcnt vmcnt(6)\n\t" \
  "s_barrier\n\t" \
  ".Lg31%=:\n\t" \
  "s_add_u32 s92, s92, 65024\n\t" \
  "s_addc_u32 s93, s93, 0\n\t" \
  "s_waitcnt lgkmcnt(6)\n\t" \
  "v_mfma_f32_16x16x32_bf16 v[144:147], v[96:99], v[176:179], v[144:147]\n\t" \
  "v_mfma_f32_16x16x32_bf16 v[148:151], v[100:103], v[176:179], v[148:151]\n\t" \
  "ds_read_b128 v[176:179], v208 offset:0\n\t" \
  "v_mfma_f32_16x16x32_bf16 v[152:155], v[96:99], v[180:183], v[152:155]\n\t" \
  "v_mfma_f32_16x16x32_bf16 v[156:159], v[100:103], v[180:183], v[156:159]\n\t" \
  "ds_read_b128 v[180:183], v208 offset:16384\n\t" \
  "s_waitcnt lgkmcnt(6)\n\t" \
  "v_mfma_f32_16x16x32_bf16 v[144:147], v[104:107], v[184:187], v[144:147]\n\t" \
  "v_mfma_f32_16x16x32_bf16 v[148:151], v[108:111], v[184:187], v[148:151]\n\t" \
  "ds_read_b128 v[184:187], v209 offset:0\n\t" \
  "s_add_u32 m0, %[lw], 65536\n\t" \
  "v_mfma_f32_16x16x32_bf16 v[152:155], v[104:107], v[188:191], v[152:155]\n\t" \
  "v_mfma_f32_16x16x32_bf16 v[156:159], v[108:111], v[188:191], v[156:159]\n\t" \
  "ds_read_b128 v[188:191], v209 offset:16384\n\t" \
  "global_load_lds_dwordx4 v216, s[92:93] sc1\n\t" \
  "s_waitcnt lgkmcnt(6)\n\t" \
  "v_mfma_f32_16x16x32_bf16 v[144:147], v[112:115], v[192:195], v[144:147]\n\t" \
  "v_mfma_f32_16x16x32_bf16 v[148:151], v[116:119], v[192:195], v[148:151]\n\t" \
  "ds_read_b128 v[192:195], v210 offset:0\n\t" \
  "v_mfma_f32_16x16x32_bf16 v[152:155], v[112:115], v[196:199], v[152:155]\n\t" \
  "v_mfma_f32_16x16x32_bf16 v[156:159], v[116:119], v[196:199], v[156:159]\n\t" \
  "ds_read_b128 v[196:199], v210 offset:16384\n\t" \
  "s_waitcnt lgkmcnt(6)\n\t" \
  "v_mfma_f32_16x16x32_bf16 v[144:147], v[120:123], v[200:203], v[144:147]\n\t" \
  "v_mfma_f32_16x16x32_bf16 v[148:151], v[124:127], v[200:203], v[148:151]\n\t" \
  "ds_read_b128 v[200:203], v211 offset:0\n\t" \
  "s_add_u32 m0, %[lw], 66560\n\t" \
  "v_mfma_f32_16x16x32_bf16 v[152:155], v[120:123], v[204:207], v[152:155]\n\t" \
  "v_mfma_f32_16x16x32_bf16 v[156:159], v[124:127], v[204:207], v[156:159]\n\t" \
  "ds_read_b128 v[204:207], v211 offset:16384\n\t" \
  "global_load_lds_dwordx4 v217, s[92:93] sc1\n\t" \
  "s_add_u32 s86, s86, 1\n\t" \
  "s_add_u32 s87, s87, 1\n\t" \
  "s_cmp_lt_u32 s86, %[end]\n\t" \
  "s_cbranch_scc1 .Lp1%=\n\t" \
  "s_branch .Lx1%=\n\t" \
  ".Lf01%=:\n\t" \
  "s_nop 7\n\t" \
  "ds_write_b128 v220, v[152:155] offset:0\n\t" \
  "ds_write_b128 v220, v[156:159] offset:1024\n\t" \
  "s_lshr_b32 s90, s87, 7\n\t" \
  "s_xor_b32 s90, s90, %[s0]\n\t" \
  "s_and_b32 s90, s90, 1\n\t" \
  "s_lshl1_add_u32 s90, s87, s90\n\t" \
  "s_lshl_b32 s90, s90, 13\n\t" \
  "s_cmp_lt_i32 s86, 1\n\t" \
  "s_cselect_b32 s90, 0x70000000, s90\n\t" \
  "s_waitcnt lgkmcnt(0)\n\t" \
  "s_barrier\n\t" \
  "ds_read_b128 v[160:163], v221 offset:0\n\t" \
  "ds_read_b128 v[164:167], v221 offset:1024\n\t" \
  "s_waitcnt lgkmcnt(0)\n\t" \
  "v_add_f32 v160, v160, v144\n\t" \
  "v_add_f32 v161, v161, v145\n\t" \
  "v_add_f32 v162, v162, v146\n\t" \
  "v_add_f32 v163, v163, v147\n\t" \
  "v_add_f32 v164, v164, v148\n\t" \
  "v_add_f32 v165, v165, v149\n\t" \
  "v_add_f32 v166, v166, v150\n\t" \
  "v_add_f32 v167, v167, v151\n\t" \
  "v_cvt_pk_bf16_f32 v168, v160, v161\n\t" \
  "v_cvt_pk_bf16_f32 v169, v162, v163\n\t" \
  "v_cvt_pk_bf16_f32 v170, v164, v165\n\t" \
  "v_cvt_pk_bf16_f32 v171, v166, v167\n\t" \
  "buffer_store_dwordx4 v[168:171], v172, %[rt], s90 offen sc1\n\t" \
  "s_branch .Lfb01%=\n\t" \
  ".Lf11%=:\n\t" \
  "s_nop 7\n\t" \
  "ds_write_b128 v220, v[136:139] offset:0\n\t" \
  "ds_write_b128 v220, v[140:143] offset:1024\n\t" \
  "s_lshr_b32 s90, s87, 7\n\t" \
  "s_xor_b32 s90, s90, %[s0]\n\t" \
  "s_and_b32 s90, s90, 1\n\t" \
  "s_lshl1_add_u32 s90, s87, s90\n\t" \
  "s_lshl_b32 s90, s90, 13\n\t" \
  "s_cmp_lt_i32 s86, 1\n\t" \
  "s_cselect_b32 s90, 0x70000000, s90\n\t" \
  "s_waitcnt lgkmcnt(0)\n\t" \
  "s_barrier\n\t" \
  "ds_read_b128 v[160:163], v221 offset:0\n\t" \
  "ds_read_b128 v[164:167], v221 offset:1024\n\t" \
  "s_waitcnt lgkmcnt(0)\n\t" \
  "v_add_f32 v160, v160, v128\n\t" \
  "v_add_f32 v161, v161, v129\n\t" \
  "v_add_f32 v162, v162, v130\n\t" \
  "v_add_f32 v163, v163, v131\n\t" \
  "v_add_f32 v164, v164, v132\n\t" \
  "v_add_f32 v165, v165, v133\n\t" \
  "v_add_f32 v166, v166, v134\n\t" \
  "v_add_f32 v167, v167, v135\n\t" \
  "v_cvt_pk_bf16_f32 v168, v160, v161\n\t" \
  "v_cvt_pk_bf16_f32 v169, v162, v163\n\t" \
  "v_cvt_pk_bf16_f32 v170, v164, v165\n\t" \
  "v_cvt_pk_bf16_f32 v171, v166, v167\n\t" \
  "buffer_store_dwordx4 v[168:171], v172, %[rt], s90 offen sc1\n\t" \
  "s_branch .Lfb11%=\n\t" \
  ".Lw01%=:\n\t" \
  "s_waitcnt vmcnt(2)\n\t" \
  "s_barrier\n\t" \
  "s_sub_i32 s91, s86, 1\n\t" \
  "s_max_i32 s88, s88, s91\n\t" \
  "v_mov_b32 v173, s88\n\t" \
  "buffer_store_dword v173, v174, %[rs], 0 offen sc1\n\t" \
  "s_branch .Lg01%=\n\t" \
  ".Lw11%=:\n\t" \
  "s_waitcnt vmcnt(2)\n\t" \
  "s_barrier\n\t" \
  "s_sub_i32 s91, s86, 1\n\t" \
  "s_max_i32 s88, s88, s91\n\t" \
  "v_mov_b32 v173, s88\n\t" \
  "buffer_store_dword v173, v174, %[rs], 0 offen sc1\n\t" \
  "s_branch .Lg11%=\n\t" \
  ".Lw21%=:\n\t" \
  "s_waitcnt vmcnt(2)\n\t" \
  "s_barrier\n\t" \
  "s_sub_i32 s91, s86, 1\n\t" \
  "s_max_i32 s88, s88, s91\n\t" \
  "v_mov_b32 v173, s88\n\t" \
  "buffer_store_dword v173, v174, %[rs], 0 offen sc1\n\t" \
  "s_branch .Lg21%=\n\t" \
  ".Lw31%=:\n\t" \
  "s_waitcnt vmcnt(2)\n\t" \
  "s_barrier\n\t" \
  "s_sub_i32 s91, s86, 1\n\t" \
  "s_max_i32 s88, s88, s91\n\t" \
  "v_mov_b32 v173, s88\n\t" \
  "buffer_store_dword v173, v174, %[rs], 0 offen sc1\n\t" \
  "s_branch .Lg31%=\n\t" \
  ".Lx1%=:\n\t" \
  "s_waitcnt lgkmcnt(0)\n\t" \
  "s_setprio 0\n\t" \
  "s_mov_b32 m0, s94\n\t" \
  ".Le%=:\n\t"
#define PR_ASM_1 \
  "s_cmp_lg_u32 %[kh], 0\n\t" \
  "s_cbranch_scc1 .Lk%=\n\t" \
  "s_mov_b32 s94, m0\n\t" \
  "s_mov_b64 s[92:93], %[x0]\n\t" \
  "s_mov_b32 s86, %[g0]\n\t" \
  "s_add_u32 s87, s86, %[t0]\n\t" \
  "s_sub_i32 s88, s86, 3\n\t" \
  "ds_read_b128 v[176:179], v208 offset:0\n\t" \
  "ds_read_b128 v[180:183], v208 offset:16384\n\t" \
  "ds_read_b128 v[184:187], v209 offset:0\n\t" \
  "ds_read_b128 v[188:191], v209 offset:16384\n\t" \
  "ds_read_b128 v[192:195], v210 offset:0\n\t" \
  "ds_read_b128 v[196:199], v210 offset:16384\n\t" \
  "ds_read_b128 v[200:203], v211 offset:0\n\t" \
  "ds_read_b128 v[204:207], v211 offset:16384\n\t" \
  "s_branch .Lfb00%=\n\t" \
  ".Lp0%=:\n\t" \
  "s_cmp_le_u32 s86, 5\n\t" \
  "s_cbranch_scc1 .Lf00%=\n\t" \
  ".Lfb00%=:\n\t" \
  "s_waitcnt lgkmcnt(6)\n\t" \
  "v_mfma_f32_16x16x32_bf16 v[128:131], v[176:179], v[0:3], 0\n\t" \
  "v_mfma_f32_16x16x32_bf16 v[136:139], v[176:179], v[4:7], 0\n\t" \
  "ds_read_b128 v[176:179], v208 offset:256\n\t" \
  "s_add_u32 m0, %[lw], 67584\n\t" \
  "v_mfma_f32_16x16x32_bf16 v[132:135], v[180:183], v[0:3], 0\n\t" \
  "v_mfma_f32_16x16x32_bf16 v[140:143], v[180:183], v[4:7], 0\n\t" \
  "ds_read_b128 v[180:183], v208 offset:16640\n\t" \
  "global_load_lds_dwordx4 v218, s[92:93] sc1\n\t" \
  "s_waitcnt lgkmcnt(6)\n\t" \
  "v_mfma_f32_16x16x32_bf16 v[128:131], v[184:187], v[8:11], v[128:131]\n\t" \
  "v_mfma_f32_16x16x32_bf16 v[136:139], v[184:187], v[12:15], v[136:139]\n\t" \
  "ds_read_b128 v[184:187], v209 offset:256\n\t" \
  "v_mfma_f32_16x16x32_bf16 v[132:135], v[188:191], v[8:11], v[132:135]\n\t" \
  "v_mfma_f32_16x16x32_bf16 v[140:143], v[188:191], v[12:15], v[140:143]\n\t" \
  "ds_read_b128 v[188:191], v209 offset:16640\n\t" \
  "ds_write_b128 v220, v[152:155] offset:0\n\t" \
  "ds_write_b128 v220, v[156:159] offset:1024\n\t" \
  "s_waitcnt lgkmcnt(8)\n\t" \
  "v_mfma_f32_16x16x32_bf16 v[128:131], v[192:195], v[16:19], v[128:131]\n\t" \
  "v_mfma_f32_16x16x32_bf16 v[136:139], v[192:195], v[20:23], v[136:139]\n\t" \
  "ds_read_b128 v[192:195], v210 offset:256\n\t" \
  "s_add_u32 m0, %[lw], 68608\n\t" \
  "v_mfma_f32_16x16x32_bf16 v[132:135], v[196:199], v[16:19], v[132:135]\n\t" \
  "v_mfma_f32_16x16x32_bf16 v[140:143], v[196:199], v[20:23], v[140:143]\n\t" \
  "ds_read_b128 v[196:199], v210 offset:16640\n\t" \
  "global_load_lds_dwordx4 v219, s[92:93] sc1\n\t" \
  "s_lshr_b32 s90, s87, 7\n\t" \
  "s_xor_b32 s90, s90, %[s0]\n\t" \
  "s_and_b32 s90, s90, 1\n\t" \
  "s_lshl1_add_u32 s90, s87, s90\n\t" \
  "s_lshl_b32 s90, s90, 13\n\t" \
  "s_cmp_lt_i32 s86, 6\n\t" \
  "s_cselect_b32 s90, 0x70000000, s90\n\t" \
  "s_waitcnt lgkmcnt(8)\n\t" \
  "v_mfma_f32_16x16x32_bf16 v[128:131], v[200:203], v[24:27], v[128:131]\n\t" \
  "v_mfma_f32_16x16x32_bf16 v[136:139], v[200:203], v[28:31], v[136:139]\n\t" \
  "ds_read_b128 v[200:203], v211 offset:256\n\t" \
  "v_mfma_f32_16x16x32_bf16 v[132:135], v[204:207], v[24:27], v[132:135]\n\t" \
  "v_mfma_f32_16x16x32_bf16 v[140:143], v[204:207], v[28:31], v[140:143]\n\t" \
  "ds_read_b128 v[204:207], v211 offset:16640\n\t" \
  "s_waitcnt lgkmcnt(4)\n\t" \
  "s_cmp_lt_u32 s86, 6\n\t" \
  "s_cbranch_scc1 .Lw00%=\n\t" \
  "s_waitcnt vmcnt(4)\n\t" \
  "s_barrier\n\t" \
  ".Lg00%=:\n\t" \
  "s_add_u32 s92, s92, 512\n\t" \
  "s_addc_u32 s93, s93, 0\n\t" \
  "v_mfma_f32_16x16x32_bf16 v[128:131], v[176:179], v[32:35], v[128:131]\n\t" \
  "v_mfma_f32_16x16x32_bf16 v[136:139], v[176:179], v[36:39], v[136:139]\n\t" \
  "ds_read_b128 v[176:179], v208 offset:32768\n\t" \
  "s_add_u32 m0, %[lw], 98304\n\t" \
  "v_mfma_f32_16x16x32_bf16 v[132:135], v[180:183], v[32:35], v[132:135]\n\t" \
  "v_mfma_f32_16x16x32_bf16 v[140:143], v[180:183], v[36:39], v[140:143]\n\t" \
  "ds_read_b128 v[180:183], v208 offset:49152\n\t" \
  "global_load_lds_dwordx4 v216, s[92:93] sc1\n\t" \
  "ds_read_b128 v[160:163], v221 offset:0\n\t" \
  "ds_read_b128 v[164:167], v221 offset:1024\n\t" \
  "v_mfma_f32_16x16x32_bf16 v[128:131], v[184:187], v[40:43], v[128:131]\n\t" \
  "v_mfma_f32_16x16x32_bf16 v[136:139], v[184:187], v[44:47], v[136:139]\n\t" \
  "ds_read_b128 v[184:187], v209 offset:32768\n\t" \
  "v_mfma_f32_16x16x32_bf16 v[132:135], v[188:191], v[40:43], v[132:135]\n\t" \
  "v_mfma_f32_16x16x32_bf16 v[140:143], v[188:191], v[44:47], v[140:143]\n\t" \
  "ds_read_b128 v[188:191], v209 offset:49152\n\t" \
  "s_waitcnt lgkmcnt(8)\n\t" \
  "v_mfma_f32_16x16x32_bf16 v[128:131], v[192:195], v[48:51], v[128:131]\n\t" \
  "v_mfma_f32_16x16x32_bf16 v[136:139], v[192:195], v[52:55], v[136:139]\n\t" \
  "ds_read_b128 v[192:195], v210 offset:32768\n\t" \
  "s_add_u32 m0, %[lw], 99328\n\t" \
  "v_mfma_f32_16x16x32_bf16 v[132:135], v[196:199], v[48:51], v[132:135]\n\t" \
  "v_mfma_f32_16x16x32_bf16 v[140:143], v[196:199], v[52:55], v[140:143]\n\t" \
  "ds_read_b128 v[196:199], v210 offset:49152\n\t" \
  "global_load_lds_dwordx4 v217, s[92:93] sc1\n\t" \
  "s_waitcnt lgkmcnt(4)\n\t" \
  "v_add_f32 v160, v160, v144\n\t" \
  "v_add_f32 v161, v161, v145\n\t" \
  "v_add_f32 v162, v162, v146\n\t" \
  "v_add_f32 v163, v163, v147\n\t" \
  "v_add_f32 v164, v164, v148\n\t" \
  "v_add_f32 v165, v165, v149\n\t" \
  "v_add_f32 v166, v166, v150\n\t" \
  "v_add_f32 v167, v167, v151\n\t" \
  "v_mfma_f32_16x16x32_bf16 v[128:131], v[200:203], v[56:59], v[128:131]\n\t" \
  "v_mfma_f32_16x16x32_bf16 v[136:139], v[200:203], v[60:63], v[136:139]\n\t" \
  "ds_read_b128 v[200:203], v211 offset:32768\n\t" \
  "v_mfma_f32_16x16x32_bf16 v[132:135], v[204:207], v[56:59], v[132:135]\n\t" \
  "v_mfma_f32_16x16x32_bf16 v[140:143], v[204:207], v[60:63], v[140:143]\n\t" \
  "ds_read_b128 v[204:207], v211 offset:49152\n\t" \
  "v_cvt_pk_bf16_f32 v168, v160, v161\n\t" \
  "v_cvt_pk_bf16_f32 v169, v162, v163\n\t" \
  "v_cvt_pk_bf16_f32 v170, v164, v165\n\t" \
  "v_cvt_pk_bf16_f32 v171, v166, v167\n\t" \
  "buffer_store_dwordx4 v[168:171], v172, %[rt], s90 offen sc1\n\t" \
  "v_mfma_f32_16x16x32_bf16 v[128:131], v[176:179], v[64:67], v[128:131]\n\t" \
  "v_mfma_f32_16x16x32_bf16 v[136:139], v[176:179], v[68:71], v[136:139]\n\t" \
  "ds_read_b128 v[176:179], v208 offset:33024\n\t" \
  "s_add_u32 m0, %[lw], 100352\n\t" \
  "v_mfma_f32_16x16x32_bf16 v[132:135], v[180:183], v[64:67], v[132:135]\n\t" \
  "v_mfma_f32_16x16x32_bf16 v[140:143], v[180:183], v[68:71], v[140:143]\n\t" \
  "ds_read_b128 v[180:183], v208 offset:49408\n\t" \
  "global_load_lds_dwordx4 v218, s[92:93] sc1\n\t" \
  "s_waitcnt lgkmcnt(6)\n\t" \
  "v_mfma_f32_16x16x32_bf16 v[128:131], v[184:187], v[72:75], v[128:131]\n\t" \
  "v_mfma_f32_16x16x32_bf16 v[136:139], v[184:187], v[76:79], v[136:139]\n\t" \
  "ds_read_b128 v[184:187], v209 offset:33024\n\t" \
  "v_mfma_f32_16x16x32_bf16 v[132:135], v[188:191], v[72:75], v[132:135]\n\t" \
  "v_mfma_f32_16x16x32_bf16 v[140:143], v[188:191], v[76:79], v[140:143]\n\t" \
  "ds_read_b128 v[188:191], v209 offset:49408\n\t" \
  "s_waitcnt lgkmcnt(6)\n\t" \
  "v_mfma_f32_16x16x32_bf16 v[128:131], v[192:195], v[80:83], v[128:131]\n\t" \
  "v_mfma_f32_16x16x32_bf16 v[136:139], v[192:195], v[84:87], v[136:139]\n\t" \
  "ds_read_b128 v[192:195], v210 offset:33024\n\t" \
  "s_add_u32 m0, %[lw], 101376\n\t" \
  "v_mfma_f32_16x16x32_bf16 v[132:135], v[196:199], v[80:83], v[132:135]\n\t" \
  "v_mfma_f32_16x16x32_bf16 v[140:143], v[196:199], v[84:87], v[140:143]\n\t" \
  "ds_read_b128 v[196:199], v210 offset:49408\n\t" \
  "global_load_lds_dwordx4 v219, s[92:93] sc1\n\t" \
  "s_waitcnt lgkmcnt(6)\n\t" \
  "v_mfma_f32_16x16x32_bf16 v[128:131], v[200:203], v[88:91], v[128:131]\n\t" \
  "v_mfma_f32_16x16x32_bf16 v[136:139], v[200:203], v[92:95], v[136:139]\n\t" \
  "ds_read_b128 v[200:203], v211 offset:33024\n\t" \
  "v_mfma_f32_16x16x32_bf16 v[132:135], v[204:207], v[88:91], v[132:135]\n\t" \
  "v_mfma_f32_16x16x32_bf16 v[140:143], v[204:207], v[92:95], v[140:143]\n\t" \
  "ds_read_b128 v[204:207], v211 offset:49408\n\t" \
  "s_cmp_lt_u32 s86, 8\n\t" \
  "s_cbranch_scc1 .Lw10%=\n\t" \
  "s_waitcnt vmcnt(5)\n\t" \
  "s_barrier\n\t" \
  ".Lg10%=:\n\t" \
  "s_add_u32 s92, s92, 65024\n\t" \
  "s_addc_u32 s93, s93, 0\n\t" \
  "s_waitcnt lgkmcnt(6)\n\t" \
  "v_mfma_f32_16x16x32_bf16 v[128:131], v[176:179], v[96:99], v[128:131]\n\t" \
  "v_mfma_f32_16x16x32_bf16 v[136:139], v[176:179], v[100:103], v[136:139]\n\t" \
  "ds_read_b128 v[176:179], v212 offset:0\n\t" \
  "s_add_u32 m0, %[lw], 0\n\t" \
  "v_mfma_f32_16x16x32_bf16 v[132:135], v[180:183], v[96:99], v[132:135]\n\t" \
  "v_mfma_f32_16x16x32_bf16 v[140:143], v[180:183], v[100:103], v[140:143]\n\t" \
  "ds_read_b128 v[180:183], v212 offset:16384\n\t" \
  "global_load_lds_dwordx4 v216, s[92:93] sc1\n\t" \
  "s_waitcnt lgkmcnt(6)\n\t" \
  "v_mfma_f32_16x16x32_bf16 v[128:131], v[184:187], v[104:107], v[128:131]\n\t" \
  "v_mfma_f32_16x16x32_bf16 v[136:139], v[184:187], v[108:111], v[136:139]\n\t" \
  "ds_read_b128 v[184:187], v213 offset:0\n\t" \
  "v_mfma_f32_16x16x32_bf16 v[132:135], v[188:191], v[104:107], v[132:135]\n\t" \
  "v_mfma_f32_16x16x32_bf16 v[140:143], v[188:191], v[108:111], v[140:143]\n\t" \
  "ds_read_b128 v[188:191], v213 offset:16384\n\t" \
  "s_waitcnt lgkmcnt(6)\n\t" \
  "v_mfma_f32_16x16x32_bf16 v[128:131], v[192:195], v[112:115], v[128:131]\n\t" \
  "v_mfma_f32_16x16x32_bf16 v[136:139], v[192:195], v[116:119], v[136:139]\n\t" \
  "ds_read_b128 v[192:195], v214 offset:0\n\t" \
  "s_add_u32 m0, %[lw], 1024\n\t" \
  "v_mfma_f32_16x16x32_bf16 v[132:135], v[196:199], v[112:115], v[132:135]\n\t" \
  "v_mfma_f32_16x16x32_bf16 v[140:143], v[196:199], v[116:119], v[140:143]\n\t" \
  "ds_read_b128 v[196:199], v214 offset:16384\n\t" \
  "global_load_lds_dwordx4 v217, s[92:93] sc1\n\t" \
  "s_waitcnt lgkmcnt(6)\n\t" \
  "v_mfma_f32_16x16x32_bf16 v[128:131], v[200:203], v[120:123], v[128:131]\n\t" \
  "v_mfma_f32_16x16x32_bf16 v[136:139], v[200:203], v[124:127], v[136:139]\n\t" \
  "ds_read_b128 v[200:203], v215 offset:0\n\t" \
  "v_mfma_f32_16x16x32_bf16 v[132:135], v[204:207], v[120:123], v[132:135]\n\t" \
  "v_mfma_f32_16x16x32_bf16 v[140:143], v[204:207], v[124:127], v[140:143]\n\t" \
  "ds_read_b128 v[204:207], v215 offset:16384\n\t" \
  "s_add_u32 s86, s86, 1\n\t" \
  "s_add_u32 s87, s87, 1\n\t" \
  "s_cmp_le_u32 s86, 5\n\t" \
  "s_cbranch_scc1 .Lf10%=\n\t" \
  ".Lfb10%=:\n\t" \
  "s_waitcnt lgkmcnt(6)\n\t" \
  "v_mfma_f32_16x16x32_bf16 v[144:147], v[176:179], v[0:3], 0\n\t" \
  "v_mfma_f32_16x16x32_bf16 v[152:155], v[176:179], v[4:7], 0\n\t" \
  "ds_read_b128 v[176:179], v212 offset:256\n\t" \
  "s_add_u32 m0, %[lw], 2048\n\t" \
  "v_mfma_f32_16x16x32_bf16 v[148:151], v[180:183], v[0:3], 0\n\t" \
  "v_mfma_f32_16x16x32_bf16 v[156:159], v[180:183], v[4:7], 0\n\t" \
  "ds_read_b128 v[180:183], v212 offset:16640\n\t" \
  "global_load_lds_dwordx4 v218, s[92:93] sc1\n\t" \
  "s_waitcnt lgkmcnt(6)\n\t" \
  "v_mfma_f32_16x16x32_bf16 v[144:147], v[184:187], v[8:11], v[144:147]\n\t" \
  "v_mfma_f32_16x16x32_bf16 v[152:155], v[184:187], v[12:15], v[152:155]\n\t" \
  "ds_read_b128 v[184:187], v213 offset:256\n\t" \
  "v_mfma_f32_16x16x32_bf16 v[148:151], v[188:191], v[8:11], v[148:151]\n\t" \
  "v_mfma_f32_16x16x32_bf16 v[156:159], v[188:191], v[12:15], v[156:159]\n\t" \
  "ds_read_b128 v[188:191], v213 offset:16640\n\t" \
  "ds_write_b128 v220, v[136:139] offset:0\n\t" \
  "ds_write_b128 v220, v[140:143] offset:1024\n\t" \
  "s_waitcnt lgkmcnt(8)\n\t" \
  "v_mfma_f32_16x16x32_bf16 v[144:147], v[192:195], v[16:19], v[144:147]\n\t" \
  "v_mfma_f32_16x16x32_bf16 v[152:155], v[192:195], v[20:23], v[152:155]\n\t" \
  "ds_read_b128 v[192:195], v214 offset:256\n\t" \
  "s_add_u32 m0, %[lw], 3072\n\t" \
  "v_mfma_f32_16x16x32_bf16 v[148:151], v[196:199], v[16:19], v[148:151]\n\t" \
  "v_mfma_f32_16x16x32_bf16 v[156:159], v[196:199], v[20:23], v[156:159]\n\t" \
  "ds_read_b128 v[196:199], v214 offset:16640\n\t" \
  "global_load_lds_dwordx4 v219, s[92:93] sc1\n\t" \
  "s_lshr_b32 s90, s87, 7\n\t" \
  "s_xor_b32 s90, s90, %[s0]\n\t" \
  "s_and_b32 s90, s90, 1\n\t" \
  "s_lshl1_add_u32 s90, s87, s90\n\t" \
  "s_lshl_b32 s90, s90, 13\n\t" \
  "s_cmp_lt_i32 s86, 6\n\t" \
  "s_cselect_b32 s90, 0x70000000, s90\n\t" \
  "s_waitcnt lgkmcnt(8)\n\t" \
  "v_mfma_f32_16x16x32_bf16 v[144:147], v[200:203], v[24:27], v[144:147]\n\t" \
  "v_mfma_f32_16x16x32_bf16 v[152:155], v[200:203], v[28:31], v[152:155]\n\t" \
  "ds_read_b128 v[200:203], v215 offset:256\n\t" \
  "v_mfma_f32_16x16x32_bf16 v[148:151], v[204:207], v[24:27], v[148:151]\n\t" \
  "v_mfma_f32_16x16x32_bf16 v[156:159], v[204:207], v[28:31], v[156:159]\n\t" \
  "ds_read_b128 v[204:207], v215 offset:16640\n\t" \
  "s_waitcnt lgkmcnt(4)\n\t" \
  "s_cmp_lt_u32 s86, 6\n\t" \
  "s_cbranch_scc1 .Lw20%=\n\t" \
  "s_waitcnt vmcnt(4)\n\t" \
  "s_barrier\n\t" \
  ".Lg20%=:\n\t" \
  "s_add_u32 s92, s92, 512\n\t" \
  "s_addc_u32 s93, s93, 0\n\t" \
  "v_mfma_f32_16x16x32_bf16 v[144:147], v[176:179], v[32:35], v[144:147]\n\t" \
  "v_mfma_f32_16x16x32_bf16 v[152:155], v[176:179], v[36:39], v[152:155]\n\t" \
  "ds_read_b128 v[176:179], v212 offset:32768\n\t" \
  "s_add_u32 m0, %[lw], 32768\n\t" \
  "v_mfma_f32_16x16x32_bf16 v[148:151], v[180:183], v[32:35], v[148:151]\n\t" \
  "v_mfma_f32_16x16x32_bf16 v[156:159], v[180:183], v[36:39], v[156:159]\n\t" \
  "ds_read_b128 v[180:183], v212 offset:49152\n\t" \
  "global_load_lds_dwordx4 v216, s[92:93] sc1\n\t" \
  "ds_read_b128 v[160:163], v221 offset:0\n\t" \
  "ds_read_b128 v[164:167], v221 offset:1024\n\t" \
  "v_mfma_f32_16x16x32_bf16 v[144:147], v[184:187], v[40:43], v[144:147]\n\t" \
  "v_mfma_f32_16x16x32_bf16 v[152:155], v[184:187], v[44:47], v[152:155]\n\t" \
  "ds_read_b128 v[184:187], v213 offset:32768\n\t" \
  "v_mfma_f32_16x16x32_bf16 v[148:151], v[188:191], v[40:43], v[148:151]\n\t" \
  "v_mfma_f32_16x16x32_bf16 v[156:159], v[188:191], v[44:47], v[156:159]\n\t" \
  "ds_read_b128 v[188:191], v213 offset:49152\n\t" \
  "s_waitcnt lgkmcnt(8)\n\t" \
  "v_mfma_f32_16x16x32_bf16 v[144:147], v[192:195], v[48:51], v[144:147]\n\t" \
  "v_mfma_f32_16x16x32_bf16 v[152:155], v[192:195], v[52:55], v[152:155]\n\t" \
  "ds_read_b128 v[192:195], v214 offset:32768\n\t" \
  "s_add_u32 m0, %[lw], 33792\n\t" \
  "v_mfma_f32_16x16x32_bf16 v[148:151], v[196:199], v[48:51], v[148:151]\n\t" \
  "v_mfma_f32_16x16x32_bf16 v[156:159], v[196:199], v[52:55], v[156:159]\n\t" \
  "ds_read_b128 v[196:199], v214 offset:49152\n\t" \
  "global_load_lds_dwordx4 v217, s[92:93] sc1\n\t" \
  "s_waitcnt lgkmcnt(4)\n\t" \
  "v_add_f32 v160, v160, v128\n\t" \
  "v_add_f32 v161, v161, v129\n\t" \
  "v_add_f32 v162, v162, v130\n\t" \
  "v_add_f32 v163, v163, v131\n\t" \
  "v_add_f32 v164, v164, v132\n\t" \
  "v_add_f32 v165, v165, v133\n\t" \
  "v_add_f32 v166, v166, v134\n\t" \
  "v_add_f32 v167, v167, v135\n\t" \
  "v_mfma_f32_16x16x32_bf16 v[144:147], v[200:203], v[56:59], v[144:147]\n\t" \
  "v_mfma_f32_16x16x32_bf16 v[152:155], v[200:203], v[60:63], v[152:155]\n\t" \
  "ds_read_b128 v[200:203], v215 offset:32768\n\t" \
  "v_mfma_f32_16x16x32_bf16 v[148:151], v[204:207], v[56:59], v[148:151]\n\t" \
  "v_mfma_f32_16x16x32_bf16 v[156:159], v[204:207], v[60:63], v[156:159]\n\t" \
  "ds_read_b128 v[204:207], v215 offset:49152\n\t" \
  "v_cvt_pk_bf16_f32 v168, v160, v161\n\t" \
  "v_cvt_pk_bf16_f32 v169, v162, v163\n\t" \
  "v_cvt_pk_bf16_f32 v170, v164, v165\n\t" \
  "v_cvt_pk_bf16_f32 v171, v166, v167\n\t" \
  "buffer_store_dwordx4 v[168:171], v172, %[rt], s90 offen sc1\n\t" \
  "v_mfma_f32_16x16x32_bf16 v[144:147], v[176:179], v[64:67], v[144:147]\n\t" \
  "v_mfma_f32_16x16x32_bf16 v[152:155], v[176:179], v[68:71], v[152:155]\n\t" \
  "ds_read_b128 v[176:179], v212 offset:33024\n\t" \
  "s_add_u32 m0, %[lw], 34816\n\t" \
  "v_mfma_f32_16x16x32_bf16 v[148:151], v[180:183], v[64:67], v[148:151]\n\t" \
  "v_mfma_f32_16x16x32_bf16 v[156:159], v[180:183], v[68:71], v[156:159]\n\t" \
  "ds_read_b128 v[180:183], v212 offset:49408\n\t" \
  "global_load_lds_dwordx4 v218, s[92:93] sc1\n\t" \
  "s_waitcnt lgkmcnt(6)\n\t" \
  "v_mfma_f32_16x16x32_bf16 v[144:147], v[184:187], v[72:75], v[144:147]\n\t" \
  "v_mfma_f32_16x16x32_bf16 v[152:155], v[184:187], v[76:79], v[152:155]\n\t" \
  "ds_read_b128 v[184:187], v213 offset:33024\n\t" \
  "v_mfma_f32_16x16x32_bf16 v[148:151], v[188:191], v[72:75], v[148:151]\n\t" \
  "v_mfma_f32_16x16x32_bf16 v[156:159], v[188:191], v[76:79], v[156:159]\n\t" \
  "ds_read_b128 v[188:191], v213 offset:49408\n\t" \
  "s_waitcnt lgkmcnt(6)\n\t" \
  "v_mfma_f32_16x16x32_bf16 v[144:147], v[192:195], v[80:83], v[144:147]\n\t" \
  "v_mfma_f32_16x16x32_bf16 v[152:155], v[192:195], v[84:87], v[152:155]\n\t" \
  "ds_read_b128 v[192:195], v214 offset:33024\n\t" \
  "s_add_u32 m0, %[lw], 35840\n\t" \
  "v_mfma_f32_16x16x32_bf16 v[148:151], v[196:199], v[80:83], v[148:151]\n\t" \
  "v_mfma_f32_16x16x32_bf16 v[156:159], v[196:199], v[84:87], v[156:159]\n\t" \
  "ds_read_b128 v[196:199], v214 offset:49408\n\t" \
  "global_load_lds_dwordx4 v219, s[92:93] sc1\n\t" \
  "s_waitcnt lgkmcnt(6)\n\t" \
  "v_mfma_f32_16x16x32_bf16 v[144:147], v[200:203], v[88:91], v[144:147]\n\t" \
  "v_mfma_f32_16x16x32_bf16 v[152:155], v[200:203], v[92:95], v[152:155]\n\t" \
  "ds_read_b128 v[200:203], v215 offset:33024\n\t" \
  "v_mfma_f32_16x16x32_bf16 v[148:151], v[204:207], v[88:91], v[148:151]\n\t" \
  "v_mfma_f32_16x16x32_bf16 v[156:159], v[204:207], v[92:95], v[156:159]\n\t" \
  "ds_read_b128 v[204:207], v215 offset:49408\n\t" \
  "s_cmp_lt_u32 s86, 8\n\t" \
  "s_cbranch_scc1 .Lw30%=\n\t" \
  "s_waitcnt vmcnt(5)\n\t" \
  "s_barrier\n\t" \
  ".Lg30%=:\n\t" \
  "s_add_u32 s92, s92, 65024\n\t" \
  "s_addc_u32 s93, s93, 0\n\t" \
  "s_waitcnt lgkmcnt(6)\n\t" \
  "v_mfma_f32_16x16x32_bf16 v[144:147], v[176:179], v[96:99], v[144:147]\n\t" \
  "v_mfma_f32_16x16x32_bf16 v[152:155], v[176:179], v[100:103], v[152:155]\n\t" \
  "ds_read_b128 v[176:179], v208 offset:0\n\t" \
  "s_add_u32 m0, %[lw], 65536\n\t" \
  "v_mfma_f32_16x16x32_bf16 v[148:151], v[180:183], v[96:99], v[148:151]\n\t" \
  "v_mfma_f32_16x16x32_bf16 v[156:159], v[180:183], v[100:103], v[156:159]\n\t" \
  "ds_read_b128 v[180:183], v208 offset:16384\n\t" \
  "global_load_lds_dwordx4 v216, s[92:93] sc1\n\t" \
  "s_waitcnt lgkmcnt(6)\n\t" \
  "v_mfma_f32_16x16x32_bf16 v[144:147], v[184:187], v[104:107], v[144:147]\n\t" \
  "v_mfma_f32_16x16x32_bf16 v[152:155], v[184:187], v[108:111], v[152:155]\n\t" \
  "ds_read_b128 v[184:187], v209 offset:0\n\t" \
  "v_mfma_f32_16x16x32_bf16 v[148:151], v[188:191], v[104:107], v[148:151]\n\t" \
  "v_mfma_f32_16x16x32_bf16 v[156:159], v[188:191], v[108:111], v[156:159]\n\t" \
  "ds_read_b128 v[188:191], v209 offset:16384\n\t" \
  "s_waitcnt lgkmcnt(6)\n\t" \
  "v_mfma_f32_16x16x32_bf16 v[144:147], v[192:195], v[112:115], v[144:147]\n\t" \
  "v_mfma_f32_16x16x32_bf16 v[152:155], v[192:195], v[116:119], v[152:155]\n\t" \
  "ds_read_b128 v[192:195], v210 offset:0\n\t" \
  "s_add_u32 m0, %[lw], 66560\n\t" \
  "v_mfma_f32_16x16x32_bf16 v[148:151], v[196:199], v[112:115], v[148:151]\n\t" \
  "v_mfma_f32_16x16x32_bf16 v[156:159], v[196:199], v[116:119], v[156:159]\n\t" \
  "ds_read_b128 v[196:199], v210 offset:16384\n\t" \
  "global_load_lds_dwordx4 v217, s[92:93] sc1\n\t" \
  "s_waitcnt lgkmcnt(6)\n\t" \
  "v_mfma_f32_16x16x32_bf16 v[144:147], v[200:203], v[120:123], v[144:147]\n\t" \
  "v_mfma_f32_16x16x32_bf16 v[152:155], v[200:203], v[124:127], v[152:155]\n\t" \
  "ds_read_b128 v[200:203], v211 offset:0\n\t" \
  "v_mfma_f32_16x16x32_bf16 v[148:151], v[204:207], v[120:123], v[148:151]\n\t" \
  "v_mfma_f32_16x16x32_bf16 v[156:159], v[204:207], v[124:127], v[156:159]\n\t" \
  "ds_read_b128 v[204:207], v211 offset:16384\n\t" \
  "s_add_u32 s86, s86, 1\n\t" \
  "s_add_u32 s87, s87, 1\n\t" \
  "s_cmp_lt_u32 s86, %[end]\n\t" \
  "s_cbranch_scc1 .Lp0%=\n\t" \
  "s_branch .Lx0%=\n\t" \
  ".Lf00%=:\n\t" \
  "s_nop 7\n\t" \
  "ds_write_b128 v220, v[152:155] offset:0\n\t" \
  "ds_write_b128 v220, v[156:159] offset:1024\n\t" \
  "s_lshr_b32 s90, s87, 7\n\t" \
  "s_xor_b32 s90, s90, %[s0]\n\t" \
  "s_and_b32 s90, s90, 1\n\t" \
  "s_lshl1_add_u32 s90, s87, s90\n\t" \
  "s_lshl_b32 s90, s90, 13\n\t" \
  "s_cmp_lt_i32 s86, 1\n\t" \
  "s_cselect_b32 s90, 0x70000000, s90\n\t" \
  "s_waitcnt lgkmcnt(0)\n\t" \
  "s_barrier\n\t" \
  "ds_read_b128 v[160:163], v221 offset:0\n\t" \
  "ds_read_b128 v[164:167], v221 offset:1024\n\t" \
  "s_waitcnt lgkmcnt(0)\n\t" \
  "v_add_f32 v160, v160, v144\n\t" \
  "v_add_f32 v161, v161, v145\n\t" \
  "v_add_f32 v162, v162, v146\n\t" \
  "v_add_f32 v163, v163, v147\n\t" \
  "v_add_f32 v164, v164, v148\n\t" \
  "v_add_f32 v165, v165, v149\n\t" \
  "v_add_f32 v166, v166, v150\n\t" \
  "v_add_f32 v167, v167, v151\n\t" \
  "v_cvt_pk_bf16_f32 v168, v160, v161\n\t" \
  "v_cvt_pk_bf16_f32 v169, v162, v163\n\t" \
  "v_cvt_pk_bf16_f32 v170, v164, v165\n\t" \
  "v_cvt_pk_bf16_f32 v171, v166, v167\n\t" \
  "buffer_store_dwordx4 v[168:171], v172, %[rt], s90 offen sc1\n\t" \
  "s_branch .Lfb00%=\n\t" \
  ".Lf10%=:\n\t" \
  "s_nop 7\n\t" \
  "ds_write_b128 v220, v[136:139] offset:0\n\t" \
  "ds_write_b128 v220, v[140:143] offset:1024\n\t" \
  "s_lshr_b32 s90, s87, 7\n\t" \
  "s_xor_b32 s90, s90, %[s0]\n\t" \
  "s_and_b32 s90, s90, 1\n\t" \
  "s_lshl1_add_u32 s90, s87, s90\n\t" \
  "s_lshl_b32 s90, s90, 13\n\t" \
  "s_cmp_lt_i32 s86, 1\n\t" \
  "s_cselect_b32 s90, 0x70000000, s90\n\t" \
  "s_waitcnt lgkmcnt(0)\n\t" \
  "s_barrier\n\t" \
  "ds_read_b128 v[160:163], v221 offset:0\n\t" \
  "ds_read_b128 v[164:167], v221 offset:1024\n\t" \
  "s_waitcnt lgkmcnt(0)\n\t" \
  "v_add_f32 v160, v160, v128\n\t" \
  "v_add_f32 v161, v161, v129\n\t" \
  "v_add_f32 v162, v162, v130\n\t" \
  "v_add_f32 v163, v163, v131\n\t" \
  "v_add_f32 v164, v164, v132\n\t" \
  "v_add_f32 v165, v165, v133\n\t" \
  "v_add_f32 v166, v166, v134\n\t" \
  "v_add_f32 v167, v167, v135\n\t" \
  "v_cvt_pk_bf16_f32 v168, v160, v161\n\t" \
  "v_cvt_pk_bf16_f32 v169, v162, v163\n\t" \
  "v_cvt_pk_bf16_f32 v170, v164, v165\n\t" \
  "v_cvt_pk_bf16_f32 v171, v166, v167\n\t" \
  "buffer_store_dwordx4 v[168:171], v172, %[rt], s90 offen sc1\n\t" \
  "s_branch .Lfb10%=\n\t" \
  ".Lw00%=:\n\t" \
  "s_waitcnt vmcnt(2)\n\t" \
  "s_barrier\n\t" \
  "s_branch .Lg00%=\n\t" \
  ".Lw10%=:\n\t" \
  "s_waitcnt vmcnt(2)\n\t" \
  "s_barrier\n\t" \
  "s_branch .Lg10%=\n\t" \
  ".Lw20%=:\n\t" \
  "s_waitcnt vmcnt(2)\n\t" \
  "s_barrier\n\t" \
  "s_branch .Lg20%=\n\t" \
  ".Lw30%=:\n\t" \
  "s_waitcnt vmcnt(2)\n\t" \
  "s_barrier\n\t" \
  "s_branch .Lg30%=\n\t" \
  ".Lx0%=:\n\t" \
  "s_waitcnt lgkmcnt(0)\n\t" \
  "s_setprio 0\n\t" \
  "s_mov_b32 m0, s94\n\t" \
  "s_branch .Le%=\n\t" \
  ".Lk%=:\n\t" \
  "s_mov_b32 s94, m0\n\t" \
  "s_mov_b64 s[92:93], %[x0]\n\t" \
  "s_mov_b32 s86, %[g0]\n\t" \
  "s_add_u32 s87, s86, %[t0]\n\t" \
  "s_sub_i32 s88, s86, 3\n\t" \
  "ds_read_b128 v[176:179], v208 offset:0\n\t" \
  "ds_read_b128 v[180:183], v208 offset:16384\n\t" \
  "ds_read_b128 v[184:187], v209 offset:0\n\t" \
  "ds_read_b128 v[188:191], v209 offset:16384\n\t" \
  "ds_read_b128 v[192:195], v210 offset:0\n\t" \
  "ds_read_b128 v[196:199], v210 offset:16384\n\t" \
  "ds_read_b128 v[200:203], v211 offset:0\n\t" \
  "ds_read_b128 v[204:207], v211 offset:16384\n\t" \
  "s_branch .Lfb01%=\n\t" \
  ".Lp1%=:\n\t" \
  "s_cmp_le_u32 s86, 5\n\t" \
  "s_cbranch_scc1 .Lf01%=\n\t" \
  ".Lfb01%=:\n\t" \
  "s_waitcnt lgkmcnt(6)\n\t" \
  "v_mfma_f32_16x16x32_bf16 v[128:131], v[176:179], v[0:3], 0\n\t" \
  "v_mfma_f32_16x16x32_bf16 v[136:139], v[176:179], v[4:7], 0\n\t" \
  "ds_read_b128 v[176:179], v208 offset:256\n\t" \
  "v_mfma_f32_16x16x32_bf16 v[132:135], v[180:183], v[0:3], 0\n\t" \
  "v_mfma_f32_16x16x32_bf16 v[140:143], v[180:183], v[4:7], 0\n\t" \
  "ds_read_b128 v[180:183], v208 offset:16640\n\t" \
  "s_waitcnt lgkmcnt(6)\n\t" \
  "v_mfma_f32_16x16x32_bf16 v[128:131], v[184:187], v[8:11], v[128:131]\n\t" \
  "v_mfma_f32_16x16x32_bf16 v[136:139], v[184:187], v[12:15], v[136:139]\n\t" \
  "ds_read_b128 v[184:187], v209 offset:256\n\t" \
  "s_add_u32 m0, %[lw], 67584\n\t" \
  "v_mfma_f32_16x16x32_bf16 v[132:135], v[188:191], v[8:11], v[132:135]\n\t" \
  "v_mfma_f32_16x16x32_bf16 v[140:143], v[188:191], v[12:15], v[140:143]\n\t" \
  "ds_read_b128 v[188:191], v209 offset:16640\n\t" \
  "global_load_lds_dwordx4 v218, s[92:93] sc1\n\t" \
  "ds_write_b128 v220, v[152:155] offset:0\n\t" \
  "ds_write_b128 v220, v[156:159] offset:1024\n\t" \
  "s_waitcnt lgkmcnt(8)\n\t" \
  "v_mfma_f32_16x16x32_bf16 v[128:131], v[192:195], v[16:19], v[128:131]\n\t" \
  "v_mfma_f32_16x16x32_bf16 v[136:139], v[192:195], v[20:23], v[136:139]\n\t" \
  "ds_read_b128 v[192:195], v210 offset:256\n\t" \
  "v_mfma_f32_16x16x32_bf16 v[132:135], v[196:199], v[16:19], v[132:135]\n\t" \
  "v_mfma_f32_16x16x32_bf16 v[140:143], v[196:199], v[20:23], v[140:143]\n\t" \
  "ds_read_b128 v[196:199], v210 offset:16640\n\t" \
  "s_lshr_b32 s90, s87, 7\n\t" \
  "s_xor_b32 s90, s90, %[s0]\n\t" \
  "s_and_b32 s90, s90, 1\n\t" \
  "s_lshl1_add_u32 s90, s87, s90\n\t" \
  "s_lshl_b32 s90, s90, 13\n\t" \
  "s_cmp_lt_i32 s86, 6\n\t" \
  "s_cselect_b32 s90, 0x70000000, s90\n\t" \
  "s_waitcnt lgkmcnt(8)\n\t" \
  "v_mfma_f32_16x16x32_bf16 v[128:131], v[200:203], v[24:27], v[128:131]\n\t" \
  "v_mfma_f32_16x16x32_bf16 v[136:139], v[200:203], v[28:31], v[136:139]\n\t" \
  "ds_read_b128 v[200:203], v211 offset:256\n\t" \
  "s_add_u32 m0, %[lw], 68608\n\t" \
  "v_mfma_f32_16x16x32_bf16 v[132:135], v[204:207], v[24:27], v[132:135]\n\t" \
  "v_mfma_f32_16x16x32_bf16 v[140:143], v[204:207], v[28:31], v[140:143]\n\t" \
  "ds_read_b128 v[204:207], v211 offset:16640\n\t" \
  "global_load_lds_dwordx4 v219, s[92:93] sc1\n\t" \
  "s_waitcnt lgkmcnt(4)\n\t" \
  "s_cmp_lt_u32 s86, 6\n\t" \
  "s_cbranch_scc1 .Lw01%=\n\t" \
  "s_waitcnt vmcnt(4)\n\t" \
  "s_barrier\n\t" \
  ".Lg01%=:\n\t" \
  "s_add_u32 s92, s92, 512\n\t" \
  "s_addc_u32 s93, s93, 0\n\t" \
  "v_mfma_f32_16x16x32_bf16 v[128:131], v[176:179], v[32:35], v[128:131]\n\t" \
  "v_mfma_f32_16x16x32_bf16 v[136:139], v[176:179], v[36:39], v[136:139]\n\t" \
  "ds_read_b128 v[176:179], v208 offset:32768\n\t" \
  "v_mfma_f32_16x16x32_bf16 v[132:135], v[180:183], v[32:35], v[132:135]\n\t" \
  "v_mfma_f32_16x16x32_bf16 v[140:143], v[180:183], v[36:39], v[140:143]\n\t" \
  "ds_read_b128 v[180:183], v208 offset:49152\n\t" \
  "ds_read_b128 v[160:163], v221 offset:0\n\t" \
  "ds_read_b128 v[164:167], v221 offset:1024\n\t" \
  "s_sub_i32 s91, s86, 2\n\t" \
  "s_max_i32 s88, s88, s91\n\t" \
  "v_mov_b32 v173, s88\n\t" \
  "buffer_store_dword v173, v174, %[rs], 0 offen sc1\n\t" \
  "v_mfma_f32_16x16x32_bf16 v[128:131], v[184:187], v[40:43], v[128:131]\n\t" \
  "v_mfma_f32_16x16x32_bf16 v[136:139], v[184:187], v[44:47], v[136:139]\n\t" \
  "ds_read_b128 v[184:187], v209 offset:32768\n\t" \
  "s_add_u32 m0, %[lw], 98304\n\t" \
  "v_mfma_f32_16x16x32_bf16 v[132:135], v[188:191], v[40:43], v[132:135]\n\t" \
  "v_mfma_f32_16x16x32_bf16 v[140:143], v[188:191], v[44:47], v[140:143]\n\t" \
  "ds_read_b128 v[188:191], v209 offset:49152\n\t" \
  "global_load_lds_dwordx4 v216, s[92:93] sc1\n\t" \
  "s_waitcnt lgkmcnt(8)\n\t" \
  "v_mfma_f32_16x16x32_bf16 v[128:131], v[192:195], v[48:51], v[128:131]\n\t" \
  "v_mfma_f32_16x16x32_bf16 v[136:139], v[192:195], v[52:55], v[136:139]\n\t" \
  "ds_read_b128 v[192:195], v210 offset:32768\n\t" \
  "v_mfma_f32_16x16x32_bf16 v[132:135], v[196:199], v[48:51], v[132:135]\n\t" \
  "v_mfma_f32_16x16x32_bf16 v[140:143], v[196:199], v[52:55], v[140:143]\n\t" \
  "ds_read_b128 v[196:199], v210 offset:49152\n\t" \
  "s_waitcnt lgkmcnt(4)\n\t" \
  "v_add_f32 v160, v160, v144\n\t" \
  "v_add_f32 v161, v161, v145\n\t" \
  "v_add_f32 v162, v162, v146\n\t" \
  "v_add_f32 v163, v163, v147\n\t" \
  "v_add_f32 v164, v164, v148\n\t" \
  "v_add_f32 v165, v165, v149\n\t" \
  "v_add_f32 v166, v166, v150\n\t" \
  "v_add_f32 v167, v167, v151\n\t" \
  "v_mfma_f32_16x16x32_bf16 v[128:131], v[200:203], v[56:59], v[128:131]\n\t" \
  "v_mfma_f32_16x16x32_bf16 v[136:139], v[200:203], v[60:63], v[136:139]\n\t" \
  "ds_read_b128 v[200:203], v211 offset:32768\n\t" \
  "s_add_u32 m0, %[lw], 99328\n\t" \
  "v_mfma_f32_16x16x32_bf16 v[132:135], v[204:207], v[56:59], v[132:135]\n\t" \
  "v_mfma_f32_16x16x32_bf16 v[140:143], v[204:207], v[60:63], v[140:143]\n\t" \
  "ds_read_b128 v[204:207], v211 offset:49152\n\t" \
  "global_load_lds_dwordx4 v217, s[92:93] sc1\n\t" \
  "v_cvt_pk_bf16_f32 v168, v160, v161\n\t" \
  "v_cvt_pk_bf16_f32 v169, v162, v163\n\t" \
  "v_cvt_pk_bf16_f32 v170, v164, v165\n\t" \
  "v_cvt_pk_bf16_f32 v171, v166, v167\n\t" \
  "buffer_store_dwordx4 v[168:171], v172, %[rt], s90 offen sc1\n\t" \
  "v_mfma_f32_16x16x32_bf16 v[128:131], v[176:179], v[64:67], v[128:131]\n\t" \
  "v_mfma_f32_16x16x32_bf16 v[136:139], v[176:179], v[68:71], v[136:139]\n\t" \
  "ds_read_b128 v[176:179], v208 offset:33024\n\t" \
  "v_mfma_f32_16x16x32_bf16 v[132:135], v[180:183], v[64:67], v[132:135]\n\t" \
  "v_mfma_f32_16x16x32_bf16 v[140:143], v[180:183], v[68:71], v[140:143]\n\t" \
  "ds_read_b128 v[180:183], v208 offset:49408\n\t" \
  "s_waitcnt lgkmcnt(6)\n\t" \
  "v_mfma_f32_16x16x32_bf16 v[128:131], v[184:187], v[72:75], v[128:131]\n\t" \
  "v_mfma_f32_16x16x32_bf16 v[136:139], v[184:187], v[76:79], v[136:139]\n\t" \
  "ds_read_b128 v[184:187], v209 offset:33024\n\t" \
  "s_add_u32 m0, %[lw], 100352\n\t" \
  "v_mfma_f32_16x16x32_bf16 v[132:135], v[188:191], v[72:75], v[132:135]\n\t" \
  "v_mfma_f32_16x16x32_bf16 v[140:143], v[188:191], v[76:79], v[140:143]\n\t" \
  "ds_read_b128 v[188:191], v209 offset:49408\n\t" \
  "global_load_lds_dwordx4 v218, s[92:93] sc1\n\t" \
  "s_waitcnt lgkmcnt(6)\n\t" \
  "v_mfma_f32_16x16x32_bf16 v[128:131], v[192:195], v[80:83], v[128:131]\n\t" \
  "v_mfma_f32_16x16x32_bf16 v[136:139], v[192:195], v[84:87], v[136:139]\n\t" \
  "ds_read_b128 v[192:195], v210 offset:33024\n\t" \
  "v_mfma_f32_16x16x32_bf16 v[132:135], v[196:199], v[80:83], v[132:135]\n\t" \
  "v_mfma_f32_16x16x32_bf16 v[140:143], v[196:199], v[84:87], v[140:143]\n\t" \
  "ds_read_b128 v[196:199], v210 offset:49408\n\t" \
  "s_waitcnt lgkmcnt(6)\n\t" \
  "v_mfma_f32_16x16x32_bf16 v[128:131], v[200:203], v[88:91], v[128:131]\n\t" \
  "v_mfma_f32_16x16x32_bf16 v[136:139], v[200:203], v[92:95], v[136:139]\n\t" \
  "ds_read_b128 v[200:203], v211 offset:33024\n\t" \
  "s_add_u32 m0, %[lw], 101376\n\t" \
  "v_mfma_f32_16x16x32_bf16 v[132:135], v[204:207], v[88:91], v[132:135]\n\t" \
  "v_mfma_f32_16x16x32_bf16 v[140:143], v[204:207], v[92:95], v[140:143]\n\t" \
  "ds_read_b128 v[204:207], v211 offset:49408\n\t" \
  "global_load_lds_dwordx4 v219, s[92:93] sc1\n\t" \
  "s_cmp_lt_u32 s86, 8\n\t" \
  "s_cbranch_scc1 .Lw11%=\n\t" \
  "s_waitcnt vmcnt(6)\n\t" \
  "s_barrier\n\t" \
  ".Lg11%=:\n\t" \
  "s_add_u32 s92, s92, 65024\n\t" \
  "s_addc_u32 s93, s93, 0\n\t" \
  "s_waitcnt lgkmcnt(6)\n\t" \
  "v_mfma_f32_16x16x32_bf16 v[128:131], v[176:179], v[96:99], v[128:131]\n\t" \
  "v_mfma_f32_16x16x32_bf16 v[136:139], v[176:179], v[100:103], v[136:139]\n\t" \
  "ds_read_b128 v[176:179], v212 offset:0\n\t" \
  "v_mfma_f32_16x16x32_bf16 v[132:135], v[180:183], v[96:99], v[132:135]\n\t" \
  "v_mfma_f32_16x16x32_bf16 v[140:143], v[180:183], v[100:103], v[140:143]\n\t" \
  "ds_read_b128 v[180:183], v212 offset:16384\n\t" \
  "s_waitcnt lgkmcnt(6)\n\t" \
  "v_mfma_f32_16x16x32_bf16 v[128:131], v[184:187], v[104:107], v[128:131]\n\t" \
  "v_mfma_f32_16x16x32_bf16 v[136:139], v[184:187], v[108:111], v[136:139]\n\t" \
  "ds_read_b128 v[184:187], v213 offset:0\n\t" \
  "s_add_u32 m0, %[lw], 0\n\t" \
  "v_mfma_f32_16x16x32_bf16 v[132:135], v[188:191], v[104:107], v[132:135]\n\t" \
  "v_mfma_f32_16x16x32_bf16 v[140:143], v[188:191], v[108:111], v[140:143]\n\t" \
  "ds_read_b128 v[188:191], v213 offset:16384\n\t" \
  "global_load_lds_dwordx4 v216, s[92:93] sc1\n\t" \
  "s_waitcnt lgkmcnt(6)\n\t" \
  "v_mfma_f32_16x16x32_bf16 v[128:131], v[192:195], v[112:115], v[128:131]\n\t" \
  "v_mfma_f32_16x16x32_bf16 v[136:139], v[192:195], v[116:119], v[136:139]\n\t" \
  "ds_read_b128 v[192:195], v214 offset:0\n\t" \
  "v_mfma_f32_16x16x32_bf16 v[132:135], v[196:199], v[112:115], v[132:135]\n\t" \
  "v_mfma_f32_16x16x32_bf16 v[140:143], v[196:199], v[116:119], v[140:143]\n\t" \
  "ds_read_b128 v[196:199], v214 offset:16384\n\t" \
  "s_waitcnt lgkmcnt(6)\n\t" \
  "v_mfma_f32_16x16x32_bf16 v[128:131], v[200:203], v[120:123], v[128:131]\n\t" \
  "v_mfma_f32_16x16x32_bf16 v[136:139], v[200:203], v[124:127], v[136:139]\n\t" \
  "ds_read_b128 v[200:203], v215 offset:0\n\t" \
  "s_add_u32 m0, %[lw], 1024\n\t" \
  "v_mfma_f32_16x16x32_bf16 v[132:135], v[204:207], v[120:123], v[132:135]\n\t" \
  "v_mfma_f32_16x16x32_bf16 v[140:143], v[204:207], v[124:127], v[140:143]\n\t" \
  "ds_read_b128 v[204:207], v215 offset:16384\n\t" \
  "global_load_lds_dwordx4 v217, s[92:93] sc1\n\t" \
  "s_add_u32 s86, s86, 1\n\t" \
  "s_add_u32 s87, s87, 1\n\t" \
  "s_cmp_le_u32 s86, 5\n\t" \
  "s_cbranch_scc1 .Lf11%=\n\t" \
  ".Lfb11%=:\n\t" \
  "s_waitcnt lgkmcnt(6)\n\t" \
  "v_mfma_f32_16x16x32_bf16 v[144:147], v[176:179], v[0:3], 0\n\t" \
  "v_mfma_f32_16x16x32_bf16 v[152:155], v[176:179], v[4:7], 0\n\t" \
  "ds_read_b128 v[176:179], v212 offset:256\n\t" \
  "v_mfma_f32_16x16x32_bf16 v[148:151], v[180:183], v[0:3], 0\n\t" \
  "v_mfma_f32_16x16x32_bf16 v[156:159], v[180:183], v[4:7], 0\n\t" \
  "ds_read_b128 v[180:183], v212 offset:16640\n\t" \
  "s_waitcnt lgkmcnt(6)\n\t" \
  "v_mfma_f32_16x16x32_bf16 v[144:147], v[184:187], v[8:11], v[144:147]\n\t" \
  "v_mfma_f32_16x16x32_bf16 v[152:155], v[184:187], v[12:15], v[152:155]\n\t" \
  "ds_read_b128 v[184:187], v213 offset:256\n\t" \
  "s_add_u32 m0, %[lw], 2048\n\t" \
  "v_mfma_f32_16x16x32_bf16 v[148:151], v[188:191], v[8:11], v[148:151]\n\t" \
  "v_mfma_f32_16x16x32_bf16 v[156:159], v[188:191], v[12:15], v[156:159]\n\t" \
  "ds_read_b128 v[188:191], v213 offset:16640\n\t" \
  "global_load_lds_dwordx4 v218, s[92:93] sc1\n\t" \
  "ds_write_b128 v220, v[136:139] offset:0\n\t" \
  "ds_write_b128 v220, v[140:143] offset:1024\n\t" \
  "s_waitcnt lgkmcnt(8)\n\t" \
  "v_mfma_f32_16x16x32_bf16 v[144:147], v[192:195], v[16:19], v[144:147]\n\t" \
  "v_mfma_f32_16x16x32_bf16 v[152:155], v[192:195], v[20:23], v[152:155]\n\t" \
  "ds_read_b128 v[192:195], v214 offset:256\n\t" \
  "v_mfma_f32_16x16x32_bf16 v[148:151], v[196:199], v[16:19], v[148:151]\n\t" \
  "v_mfma_f32_16x16x32_bf16 v[156:159], v[196:199], v[20:23], v[156:159]\n\t" \
  "ds_read_b128 v[196:199], v214 offset:16640\n\t" \
  "s_lshr_b32 s90, s87, 7\n\t" \
  "s_xor_b32 s90, s90, %[s0]\n\t" \
  "s_and_b32 s90, s90, 1\n\t" \
  "s_lshl1_add_u32 s90, s87, s90\n\t" \
  "s_lshl_b32 s90, s90, 13\n\t" \
  "s_cmp_lt_i32 s86, 6\n\t" \
  "s_cselect_b32 s90, 0x70000000, s90\n\t" \
  "s_waitcnt lgkmcnt(8)\n\t" \
  "v_mfma_f32_16x16x32_bf16 v[144:147], v[200:203], v[24:27], v[144:147]\n\t" \
  "v_mfma_f32_16x16x32_bf16 v[152:155], v[200:203], v[28:31], v[152:155]\n\t" \
  "ds_read_b128 v[200:203], v215 offset:256\n\t" \
  "s_add_u32 m0, %[lw], 3072\n\t" \
  "v_mfma_f32_16x16x32_bf16 v[148:151], v[204:207], v[24:27], v[148:151]\n\t" \
  "v_mfma_f32_16x16x32_bf16 v[156:159], v[204:207], v[28:31], v[156:159]\n\t" \
  "ds_read_b128 v[204:207], v215 offset:16640\n\t" \
  "global_load_lds_dwordx4 v219, s[92:93] sc1\n\t" \
  "s_waitcnt lgkmcnt(4)\n\t" \
  "s_cmp_lt_u32 s86, 6\n\t" \
  "s_cbranch_scc1 .Lw21%=\n\t" \
  "s_waitcnt vmcnt(4)\n\t" \
  "s_barrier\n\t" \
  ".Lg21%=:\n\t" \
  "s_add_u32 s92, s92, 512\n\t" \
  "s_addc_u32 s93, s93, 0\n\t" \
  "v_mfma_f32_16x16x32_bf16 v[144:147], v[176:179], v[32:35], v[144:147]\n\t" \
  "v_mfma_f32_16x16x32_bf16 v[152:155], v[176:179], v[36:39], v[152:155]\n\t" \
  "ds_read_b128 v[176:179], v212 offset:32768\n\t" \
  "v_mfma_f32_16x16x32_bf16 v[148:151], v[180:183], v[32:35], v[148:151]\n\t" \
  "v_mfma_f32_16x16x32_bf16 v[156:159], v[180:183], v[36:39], v[156:159]\n\t" \
  "ds_read_b128 v[180:183], v212 offset:49152\n\t" \
  "ds_read_b128 v[160:163], v221 offset:0\n\t" \
  "ds_read_b128 v[164:167], v221 offset:1024\n\t" \
  "s_sub_i32 s91, s86, 2\n\t" \
  "s_max_i32 s88, s88, s91\n\t" \
  "v_mov_b32 v173, s88\n\t" \
  "buffer_store_dword v173, v174, %[rs], 0 offen sc1\n\t" \
  "v_mfma_f32_16x16x32_bf16 v[144:147], v[184:187], v[40:43], v[144:147]\n\t" \
  "v_mfma_f32_16x16x32_bf16 v[152:155], v[184:187], v[44:47], v[152:155]\n\t" \
  "ds_read_b128 v[184:187], v213 offset:32768\n\t" \
  "s_add_u32 m0, %[lw], 32768\n\t" \
  "v_mfma_f32_16x16x32_bf16 v[148:151], v[188:191], v[40:43], v[148:151]\n\t" \
  "v_mfma_f32_16x16x32_bf16 v[156:159], v[188:191], v[44:47], v[156:159]\n\t" \
  "ds_read_b128 v[188:191], v213 offset:49152\n\t" \
  "global_load_lds_dwordx4 v216, s[92:93] sc1\n\t" \
  "s_waitcnt lgkmcnt(8)\n\t" \
  "v_mfma_f32_16x16x32_bf16 v[144:147], v[192:195], v[48:51], v[144:147]\n\t" \
  "v_mfma_f32_16x16x32_bf16 v[152:155], v[192:195], v[52:55], v[152:155]\n\t" \
  "ds_read_b128 v[192:195], v214 offset:32768\n\t" \
  "v_mfma_f32_16x16x32_bf16 v[148:151], v[196:199], v[48:51], v[148:151]\n\t" \
  "v_mfma_f32_16x16x32_bf16 v[156:159], v[196:199], v[52:55], v[156:159]\n\t" \
  "ds_read_b128 v[196:199], v214 offset:49152\n\t" \
  "s_waitcnt lgkmcnt(4)\n\t" \
  "v_add_f32 v160, v160, v128\n\t" \
  "v_add_f32 v161, v161, v129\n\t" \
  "v_add_f32 v162, v162, v130\n\t" \
  "v_add_f32 v163, v163, v131\n\t" \
  "v_add_f32 v164, v164, v132\n\t" \
  "v_add_f32 v165, v165, v133\n\t" \
  "v_add_f32 v166, v166, v134\n\t" \
  "v_add_f32 v167, v167, v135\n\t" \
  "v_mfma_f32_16x16x32_bf16 v[144:147], v[200:203], v[56:59], v[144:147]\n\t" \
  "v_mfma_f32_16x16x32_bf16 v[152:155], v[200:203], v[60:63], v[152:155]\n\t" \
  "ds_read_b128 v[200:203], v215 offset:32768\n\t" \
  "s_add_u32 m0, %[lw], 33792\n\t" \
  "v_mfma_f32_16x16x32_bf16 v[148:151], v[204:207], v[56:59], v[148:151]\n\t" \
  "v_mfma_f32_16x16x32_bf16 v[156:159], v[204:207], v[60:63], v[156:159]\n\t" \
  "ds_read_b128 v[204:207], v215 offset:49152\n\t" \
  "global_load_lds_dwordx4 v217, s[92:93] sc1\n\t" \
  "v_cvt_pk_bf16_f32 v168, v160, v161\n\t" \
  "v_cvt_pk_bf16_f32 v169, v162, v163\n\t" \
  "v_cvt_pk_bf16_f32 v170, v164, v165\n\t" \
  "v_cvt_pk_bf16_f32 v171, v166, v167\n\t" \
  "buffer_store_dwordx4 v[168:171], v172, %[rt], s90 offen sc1\n\t" \
  "v_mfma_f32_16x16x32_bf16 v[144:147], v[176:179], v[64:67], v[144:147]\n\t" \
  "v_mfma_f32_16x16x32_bf16 v[152:155], v[176:179], v[68:71], v[152:155]\n\t" \
  "ds_read_b128 v[176:179], v212 offset:33024\n\t" \
  "v_mfma_f32_16x16x32_bf16 v[148:151], v[180:183], v[64:67], v[148:151]\n\t" \
  "v_mfma_f32_16x16x32_bf16 v[156:159], v[180:183], v[68:71], v[156:159]\n\t" \
  "ds_read_b128 v[180:183], v212 offset:49408\n\t" \
  "s_waitcnt lgkmcnt(6)\n\t" \
  "v_mfma_f32_16x16x32_bf16 v[144:147], v[184:187], v[72:75], v[144:147]\n\t" \
  "v_mfma_f32_16x16x32_bf16 v[152:155], v[184:187], v[76:79], v[152:155]\n\t" \
  "ds_read_b128 v[184:187], v213 offset:33024\n\t" \
  "s_add_u32 m0, %[lw], 34816\n\t" \
  "v_mfma_f32_16x16x32_bf16 v[148:151], v[188:191], v[72:75], v[148:151]\n\t" \
  "v_mfma_f32_16x16x32_bf16 v[156:159], v[188:191], v[76:79], v[156:159]\n\t" \
  "ds_read_b128 v[188:191], v213 offset:49408\n\t" \
  "global_load_lds_dwordx4 v218, s[92:93] sc1\n\t" \
  "s_waitcnt lgkmcnt(6)\n\t" \
  "v_mfma_f32_16x16x32_bf16 v[144:147], v[192:195], v[80:83], v[144:147]\n\t" \
  "v_mfma_f32_16x16x32_bf16 v[152:155], v[192:195], v[84:87], v[152:155]\n\t" \
  "ds_read_b128 v[192:195], v214 offset:33024\n\t" \
  "v_mfma_f32_16x16x32_bf16 v[148:151], v[196:199], v[80:83], v[148:151]\n\t" \
  "v_mfma_f32_16x16x32_bf16 v[156:159], v[196:199], v[84:87], v[156:159]\n\t" \
  "ds_read_b128 v[196:199], v214 offset:49408\n\t" \
  "s_waitcnt lgkmcnt(6)\n\t" \
  "v_mfma_f32_16x16x32_bf16 v[144:147], v[200:203], v[88:91], v[144:147]\n\t" \
  "v_mfma_f32_16x16x32_bf16 v[152:155], v[200:203], v[92:95], v[152:155]\n\t" \
  "ds_read_b128 v[200:203], v215 offset:33024\n\t" \
  "s_add_u32 m0, %[lw], 35840\n\t" \
  "v_mfma_f32_16x16x32_bf16 v[148:151], v[204:207], v[88:91], v[148:151]\n\t" \
  "v_mfma_f32_16x16x32_bf16 v[156:159], v[204:207], v[92:95], v[156:159]\n\t" \
  "ds_read_b128 v[204:207], v215 offset:49408\n\t" \
  "global_load_lds_dwordx4 v219, s[92:93] sc1\n\t" \
  "s_cmp_lt_u32 s86, 8\n\t" \
  "s_cbranch_scc1 .Lw31%=\n\t" \
  "s_waitcnt vmcnt(6)\n\t" \
  "s_barrier\n\t" \
  ".Lg31%=:\n\t" \
  "s_add_u32 s92, s92, 65024\n\t" \
  "s_addc_u32 s93, s93, 0\n\t" \
  "s_waitcnt lgkmcnt(6)\n\t" \
  "v_mfma_f32_16x16x32_bf16 v[144:147], v[176:179], v[96:99], v[144:147]\n\t" \
  "v_mfma_f32_16x16x32_bf16 v[152:155], v[176:179], v[100:103], v[152:155]\n\t" \
  "ds_read_b128 v[176:179], v208 offset:0\n\t" \
  "v_mfma_f32_16x16x32_bf16 v[148:151], v[180:183], v[96:99], v[148:151]\n\t" \
  "v_mfma_f32_16x16x32_bf16 v[156:159], v[180:183], v[100:103], v[156:159]\n\t" \
  "ds_read_b128 v[180:183], v208 offset:16384\n\t" \
  "s_waitcnt lgkmcnt(6)\n\t" \
  "v_mfma_f32_16x16x32_bf16 v[144:147], v[184:187], v[104:107], v[144:147]\n\t" \
  "v_mfma_f32_16x16x32_bf16 v[152:155], v[184:187], v[108:111], v[152:155]\n\t" \
  "ds_read_b128 v[184:187], v209 offset:0\n\t" \
  "s_add_u32 m0, %[lw], 65536\n\t" \
  "v_mfma_f32_16x16x32_bf16 v[148:151], v[188:191], v[104:107], v[148:151]\n\t" \
  "v_mfma_f32_16x16x32_bf16 v[156:159], v[188:191], v[108:111], v[156:159]\n\t" \
  "ds_read_b128 v[188:191], v209 offset:16384\n\t" \
  "global_load_lds_dwordx4 v216, s[92:93] sc1\n\t" \
  "s_waitcnt lgkmcnt(6)\n\t" \
  "v_mfma_f32_16x16x32_bf16 v[144:147], v[192:195], v[112:115], v[144:147]\n\t" \
  "v_mfma_f32_16x16x32_bf16 v[152:155], v[192:195], v[116:119], v[152:155]\n\t" \
  "ds_read_b128 v[192:195], v210 offset:0\n\t" \
  "v_mfma_f32_16x16x32_bf16 v[148:151], v[196:199], v[112:115], v[148:151]\n\t" \
  "v_mfma_f32_16x16x32_bf16 v[156:159], v[196:199], v[116:119], v[156:159]\n\t" \
  "ds_read_b128 v[196:199], v210 offset:16384\n\t" \
  "s_waitcnt lgkmcnt(6)\n\t" \
  "v_mfma_f32_16x16x32_bf16 v[144:147], v[200:203], v[120:123], v[144:147]\n\t" \
  "v_mfma_f32_16x16x32_bf16 v[152:155], v[200:203], v[124:127], v[152:155]\n\t" \
  "ds_read_b128 v[200:203], v211 offset:0\n\t" \
  "s_add_u32 m0, %[lw], 66560\n\t" \
  "v_mfma_f32_16x16x32_bf16 v[148:151], v[204:207], v[120:123], v[148:151]\n\t" \
  "v_mfma_f32_16x16x32_bf16 v[156:159], v[204:207], v[124:127], v[156:159]\n\t" \
  "ds_read_b128 v[204:207], v211 offset:16384\n\t" \
  "global_load_lds_dwordx4 v217, s[92:93] sc1\n\t" \
  "s_add_u32 s86, s86, 1\n\t" \
  "s_add_u32 s87, s87, 1\n\t" \
  "s_cmp_lt_u32 s86, %[end]\n\t" \
  "s_cbranch_scc1 .Lp1%=\n\t" \
  "s_branch .Lx1%=\n\t" \
  ".Lf01%=:\n\t" \
  "s_nop 7\n\t" \
  "ds_write_b128 v220, v[152:155] offset:0\n\t" \
  "ds_write_b128 v220, v[156:159] offset:1024\n\t" \
  "s_lshr_b32 s90, s87, 7\n\t" \
  "s_xor_b32 s90, s90, %[s0]\n\t" \
  "s_and_b32 s90, s90, 1\n\t" \
  "s_lshl1_add_u32 s90, s87, s90\n\t" \
  "s_lshl_b32 s90, s90, 13\n\t" \
  "s_cmp_lt_i32 s86, 1\n\t" \
  "s_cselect_b32 s90, 0x70000000, s90\n\t" \
  "s_waitcnt lgkmcnt(0)\n\t" \
  "s_barrier\n\t" \
  "ds_read_b128 v[160:163], v221 offset:0\n\t" \
  "ds_read_b128 v[164:167], v221 offset:1024\n\t" \
  "s_waitcnt lgkmcnt(0)\n\t" \
  "v_add_f32 v160, v160, v144\n\t" \
  "v_add_f32 v161, v161, v145\n\t" \
  "v_add_f32 v162, v162, v146\n\t" \
  "v_add_f32 v163, v163, v147\n\t" \
  "v_add_f32 v164, v164, v148\n\t" \
  "v_add_f32 v165, v165, v149\n\t" \
  "v_add_f32 v166, v166, v150\n\t" \
  "v_add_f32 v167, v167, v151\n\t" \
  "v_cvt_pk_bf16_f32 v168, v160, v161\n\t" \
  "v_cvt_pk_bf16_f32 v169, v162, v163\n\t" \
  "v_cvt_pk_bf16_f32 v170, v164, v165\n\t" \
  "v_cvt_pk_bf16_f32 v171, v166, v167\n\t" \
  "buffer_store_dwordx4 v[168:171], v172, %[rt], s90 offen sc1\n\t" \
  "s_branch .Lfb01%=\n\t" \
  ".Lf11%=:\n\t" \
  "s_nop 7\n\t" \
  "ds_write_b128 v220, v[136:139] offset:0\n\t" \
  "ds_write_b128 v220, v[140:143] offset:1024\n\t" \
  "s_lshr_b32 s90, s87, 7\n\t" \
  "s_xor_b32 s90, s90, %[s0]\n\t" \
  "s_and_b32 s90, s90, 1\n\t" \
  "s_lshl1_add_u32 s90, s87, s90\n\t" \
  "s_lshl_b32 s90, s90, 13\n\t" \
  "s_cmp_lt_i32 s86, 1\n\t" \
  "s_cselect_b32 s90, 0x70000000, s90\n\t" \
  "s_waitcnt lgkmcnt(0)\n\t" \
  "s_barrier\n\t" \
  "ds_read_b128 v[160:163], v221 offset:0\n\t" \
  "ds_read_b128 v[164:167], v221 offset:1024\n\t" \
  "s_waitcnt lgkmcnt(0)\n\t" \
  "v_add_f32 v160, v160, v128\n\t" \
  "v_add_f32 v161, v161, v129\n\t" \
  "v_add_f32 v162, v162, v130\n\t" \
  "v_add_f32 v163, v163, v131\n\t" \
  "v_add_f32 v164, v164, v132\n\t" \
  "v_add_f32 v165, v165, v133\n\t" \
  "v_add_f32 v166, v166, v134\n\t" \
  "v_add_f32 v167, v167, v135\n\t" \
  "v_cvt_pk_bf16_f32 v168, v160, v161\n\t" \
  "v_cvt_pk_bf16_f32 v169, v162, v163\n\t" \
  "v_cvt_pk_bf16_f32 v170, v164, v165\n\t" \
  "v_cvt_pk_bf16_f32 v171, v166, v167\n\t" \
  "buffer_store_dwordx4 v[168:171], v172, %[rt], s90 offen sc1\n\t" \
  "s_branch .Lfb11%=\n\t" \
  ".Lw01%=:\n\t" \
  "s_waitcnt vmcnt(2)\n\t" \
  "s_barrier\n\t" \
  "s_sub_i32 s91, s86, 1\n\t" \
  "s_max_i32 s88, s88, s91\n\t" \
  "v_mov_b32 v173, s88\n\t" \
  "buffer_store_dword v173, v174, %[rs], 0 offen sc1\n\t" \
  "s_branch .Lg01%=\n\t" \
  ".Lw11%=:\n\t" \
  "s_waitcnt vmcnt(2)\n\t" \
  "s_barrier\n\t" \
  "s_sub_i32 s91, s86, 1\n\t" \
  "s_max_i32 s88, s88, s91\n\t" \
  "v_mov_b32 v173, s88\n\t" \
  "buffer_store_dword v173, v174, %[rs], 0 offen sc1\n\t" \
  "s_branch .Lg11%=\n\t" \
  ".Lw21%=:\n\t" \
  "s_waitcnt vmcnt(2)\n\t" \
  "s_barrier\n\t" \
  "s_sub_i32 s91, s86, 1\n\t" \
  "s_max_i32 s88, s88, s91\n\t" \
  "v_mov_b32 v173, s88\n\t" \
  "buffer_store_dword v173, v174, %[rs], 0 offen sc1\n\t" \
  "s_branch .Lg21%=\n\t" \
  ".Lw31%=:\n\t" \
  "s_waitcnt vmcnt(2)\n\t" \
  "s_barrier\n\t" \
  "s_sub_i32 s91, s86, 1\n\t" \
  "s_max_i32 s88, s88, s91\n\t" \
  "v_mov_b32 v173, s88\n\t" \
  "buffer_store_dword v173, v174, %[rs], 0 offen sc1\n\t" \
  "s_branch .Lg31%=\n\t" \
  ".Lx1%=:\n\t" \
  "s_waitcnt lgkmcnt(0)\n\t" \
  "s_setprio 0\n\t" \
  "s_mov_b32 m0, s94\n\t" \
  ".Le%=:\n\t"
#define PP_ASM_0 \
  "s_cmp_lg_u32 %[kh], 0\n\t" \
  "s_cbranch_scc1 .Lk%=\n\t" \
  "s_mov_b32 s94, m0\n\t" \
  "s_mov_b64 s[92:93], %[x0]\n\t" \
  "s_mov_b32 s86, %[g0]\n\t" \
  "s_add_u32 s87, s86, %[t0]\n\t" \
  "s_add_u32 m0, %[lw], 67584\n\t" \
  "s_nop 0\n\t" \
  "global_load_lds_dwordx4 v250, s[92:93] sc1\n\t" \
  "s_add_u32 m0, %[lw], 68608\n\t" \
  "s_nop 0\n\t" \
  "global_load_lds_dwordx4 v251, s[92:93] sc1\n\t" \
  "s_barrier\n\t" \
  "s_branch .Ls0%=\n\t" \
  ".Lp0%=:\n\t" \
  "s_waitcnt vmcnt(9)\n\t" \
  "s_barrier\n\t" \
  ".Ls0%=:\n\t" \
  "s_add_u32 s92, s92, 512\n\t" \
  "s_addc_u32 s93, s93, 0\n\t" \
  "ds_read_b128 v[176:179], v240 offset:0\n\t" \
  "ds_read_b128 v[180:183], v240 offset:16384\n\t" \
  "ds_read_b128 v[184:187], v241 offset:0\n\t" \
  "ds_read_b128 v[188:191], v241 offset:16384\n\t" \
  "s_add_u32 m0, %[lw], 98304\n\t" \
  "s_nop 0\n\t" \
  "global_load_lds_dwordx4 v248, s[92:93] sc1\n\t" \
  "ds_read_b128 v[192:195], v242 offset:0\n\t" \
  "ds_read_b128 v[196:199], v242 offset:16384\n\t" \
  "ds_read_b128 v[200:203], v243 offset:0\n\t" \
  "ds_read_b128 v[204:207], v243 offset:16384\n\t" \
  "s_add_u32 m0, %[lw], 99328\n\t" \
  "s_nop 0\n\t" \
  "global_load_lds_dwordx4 v249, s[92:93] sc1\n\t" \
  "ds_read_b128 v[208:211], v240 offset:256\n\t" \
  "ds_read_b128 v[212:215], v240 offset:16640\n\t" \
  "ds_read_b128 v[216:219], v241 offset:256\n\t" \
  "ds_read_b128 v[220:223], v241 offset:16640\n\t" \
  "s_add_u32 m0, %[lw], 100352\n\t" \
  "s_nop 0\n\t" \
  "global_load_lds_dwordx4 v250, s[92:93] sc1\n\t" \
  "ds_read_b128 v[224:227], v242 offset:256\n\t" \
  "ds_read_b128 v[228:231], v242 offset:16640\n\t" \
  "ds_read_b128 v[232:235], v243 offset:256\n\t" \
  "ds_read_b128 v[236:239], v243 offset:16640\n\t" \
  "s_add_u32 m0, %[lw], 101376\n\t" \
  "s_nop 0\n\t" \
  "global_load_lds_dwordx4 v251, s[92:93] sc1\n\t" \
  "ds_write_b128 v252, v[152:155] offset:0\n\t" \
  "ds_write_b128 v252, v[156:159] offset:1024\n\t" \
  "s_waitcnt lgkmcnt(0)\n\t" \
  "s_setprio 1\n\t" \
  "v_mfma_f32_16x16x32_bf16 v[128:131], v[0:3], v[176:179], 0\n\t" \
  "v_mfma_f32_16x16x32_bf16 v[132:135], v[4:7], v[176:179], 0\n\t" \
  "v_mfma_f32_16x16x32_bf16 v[136:139], v[0:3], v[180:183], 0\n\t" \
  "v_mfma_f32_16x16x32_bf16 v[140:143], v[4:7], v[180:183], 0\n\t" \
  "v_mfma_f32_16x16x32_bf16 v[128:131], v[8:11], v[184:187], v[128:131]\n\t" \
  "v_mfma_f32_16x16x32_bf16 v[132:135], v[12:15], v[184:187], v[132:135]\n\t" \
  "v_mfma_f32_16x16x32_bf16 v[136:139], v[8:11], v[188:191], v[136:139]\n\t" \
  "v_mfma_f32_16x16x32_bf16 v[140:143], v[12:15], v[188:191], v[140:143]\n\t" \
  "v_mfma_f32_16x16x32_bf16 v[128:131], v[16:19], v[192:195], v[128:131]\n\t" \
  "v_mfma_f32_16x16x32_bf16 v[132:135], v[20:23], v[192:195], v[132:135]\n\t" \
  "v_mfma_f32_16x16x32_bf16 v[136:139], v[16:19], v[196:199], v[136:139]\n\t" \
  "v_mfma_f32_16x16x32_bf16 v[140:143], v[20:23], v[196:199], v[140:143]\n\t" \
  "v_mfma_f32_16x16x32_bf16 v[128:131], v[24:27], v[200:203], v[128:131]\n\t" \
  "v_mfma_f32_16x16x32_bf16 v[132:135], v[28:31], v[200:203], v[132:135]\n\t" \
  "v_mfma_f32_16x16x32_bf16 v[136:139], v[24:27], v[204:207], v[136:139]\n\t" \
  "v_mfma_f32_16x16x32_bf16 v[140:143], v[28:31], v[204:207], v[140:143]\n\t" \
  "v_mfma_f32_16x16x32_bf16 v[128:131], v[32:35], v[208:211], v[128:131]\n\t" \
  "v_mfma_f32_16x16x32_bf16 v[132:135], v[36:39], v[208:211], v[132:135]\n\t" \
  "v_mfma_f32_16x16x32_bf16 v[136:139], v[32:35], v[212:215], v[136:139]\n\t" \
  "v_mfma_f32_16x16x32_bf16 v[140:143], v[36:39], v[212:215], v[140:143]\n\t" \
  "v_mfma_f32_16x16x32_bf16 v[128:131], v[40:43], v[216:219], v[128:131]\n\t" \
  "v_mfma_f32_16x16x32_bf16 v[132:135], v[44:47], v[216:219], v[132:135]\n\t" \
  "v_mfma_f32_16x16x32_bf16 v[136:139], v[40:43], v[220:223], v[136:139]\n\t" \
  "v_mfma_f32_16x16x32_bf16 v[140:143], v[44:47], v[220:223], v[140:143]\n\t" \
  "v_mfma_f32_16x16x32_bf16 v[128:131], v[48:51], v[224:227], v[128:131]\n\t" \
  "v_mfma_f32_16x16x32_bf16 v[132:135], v[52:55], v[224:227], v[132:135]\n\t" \
  "v_mfma_f32_16x16x32_bf16 v[136:139], v[48:51], v[228:231], v[136:139]\n\t" \
  "v_mfma_f32_16x16x32_bf16 v[140:143], v[52:55], v[228:231], v[140:143]\n\t" \
  "v_mfma_f32_16x16x32_bf16 v[128:131], v[56:59], v[232:235], v[128:131]\n\t" \
  "v_mfma_f32_16x16x32_bf16 v[132:135], v[60:63], v[232:235], v[132:135]\n\t" \
  "v_mfma_f32_16x16x32_bf16 v[136:139], v[56:59], v[236:239], v[136:139]\n\t" \
  "v_mfma_f32_16x16x32_bf16 v[140:143], v[60:63], v[236:239], v[140:143]\n\t" \
  "s_setprio 0\n\t" \
  "s_waitcnt vmcnt(4)\n\t" \
  "s_barrier\n\t" \
  "s_add_u32 s92, s92, 65024\n\t" \
  "s_addc_u32 s93, s93, 0\n\t" \
  "ds_read_b128 v[160:163], v253 offset:0\n\t" \
  "ds_read_b128 v[164:167], v253 offset:1024\n\t" \
  "ds_read_b128 v[176:179], v240 offset:32768\n\t" \
  "ds_read_b128 v[180:183], v240 offset:49152\n\t" \
  "ds_read_b128 v[184:187], v241 offset:32768\n\t" \
  "ds_read_b128 v[188:191], v241 offset:49152\n\t" \
  "s_add_u32 m0, %[lw], 0\n\t" \
  "s_nop 0\n\t" \
  "global_load_lds_dwordx4 v248, s[92:93] sc1\n\t" \
  "ds_read_b128 v[192:195], v242 offset:32768\n\t" \
  "ds_read_b128 v[196:199], v242 offset:49152\n\t" \
  "ds_read_b128 v[200:203], v243 offset:32768\n\t" \
  "ds_read_b128 v[204:207], v243 offset:49152\n\t" \
  "s_add_u32 m0, %[lw], 1024\n\t" \
  "s_nop 0\n\t" \
  "global_load_lds_dwordx4 v249, s[92:93] sc1\n\t" \
  "ds_read_b128 v[208:211], v240 offset:33024\n\t" \
  "ds_read_b128 v[212:215], v240 offset:49408\n\t" \
  "ds_read_b128 v[216:219], v241 offset:33024\n\t" \
  "ds_read_b128 v[220:223], v241 offset:49408\n\t" \
  "s_add_u32 m0, %[lw], 2048\n\t" \
  "s_nop 0\n\t" \
  "global_load_lds_dwordx4 v250, s[92:93] sc1\n\t" \
  "ds_read_b128 v[224:227], v242 offset:33024\n\t" \
  "ds_read_b128 v[228:231], v242 offset:49408\n\t" \
  "ds_read_b128 v[232:235], v243 offset:33024\n\t" \
  "ds_read_b128 v[236:239], v243 offset:49408\n\t" \
  "s_add_u32 m0, %[lw], 3072\n\t" \
  "s_nop 0\n\t" \
  "global_load_lds_dwordx4 v251, s[92:93] sc1\n\t" \
  "s_waitcnt lgkmcnt(15)\n\t" \
  "s_waitcnt lgkmcnt(12)\n\t" \
  "s_lshr_b32 s90, s87, 7\n\t" \
  "s_xor_b32 s90, s90, %[s0]\n\t" \
  "s_and_b32 s90, s90, 1\n\t" \
  "s_lshl1_add_u32 s90, s87, s90\n\t" \
  "s_lshl_b32 s90, s90, 13\n\t" \
  "v_add_f32 v160, v160, v144\n\t" \
  "v_add_f32 v161, v161, v145\n\t" \
  "v_add_f32 v162, v162, v146\n\t" \
  "v_add_f32 v163, v163, v147\n\t" \
  "v_add_f32 v164, v164, v148\n\t" \
  "v_add_f32 v165, v165, v149\n\t" \
  "v_add_f32 v166, v166, v150\n\t" \
  "v_add_f32 v167, v167, v151\n\t" \
  "v_cvt_pk_bf16_f32 v168, v160, v161\n\t" \
  "v_cvt_pk_bf16_f32 v169, v162, v163\n\t" \
  "v_cvt_pk_bf16_f32 v170, v164, v165\n\t" \
  "v_cvt_pk_bf16_f32 v171, v166, v167\n\t" \
  "buffer_store_dwordx4 v[168:171], v172, %[rt], s90 offen sc1\n\t" \
  "s_add_u32 s86, s86, 1\n\t" \
  "s_add_u32 s87, s87, 1\n\t" \
  "s_waitcnt lgkmcnt(0)\n\t" \
  "s_setprio 1\n\t" \
  "v_mfma_f32_16x16x32_bf16 v[128:131], v[64:67], v[176:179], v[128:131]\n\t" \
  "v_mfma_f32_16x16x32_bf16 v[132:135], v[68:71], v[176:179], v[132:135]\n\t" \
  "v_mfma_f32_16x16x32_bf16 v[136:139], v[64:67], v[180:183], v[136:139]\n\t" \
  "v_mfma_f32_16x16x32_bf16 v[140:143], v[68:71], v[180:183], v[140:143]\n\t" \
  "v_mfma_f32_16x16x32_bf16 v[128:131], v[72:75], v[184:187], v[128:131]\n\t" \
  "v_mfma_f32_16x16x32_bf16 v[132:135], v[76:79], v[184:187], v[132:135]\n\t" \
  "v_mfma_f32_16x16x32_bf16 v[136:139], v[72:75], v[188:191], v[136:139]\n\t" \
  "v_mfma_f32_16x16x32_bf16 v[140:143], v[76:79], v[188:191], v[140:143]\n\t" \
  "v_mfma_f32_16x16x32_bf16 v[128:131], v[80:83], v[192:195], v[128:131]\n\t" \
  "v_mfma_f32_16x16x32_bf16 v[132:135], v[84:87], v[192:195], v[132:135]\n\t" \
  "v_mfma_f32_16x16x32_bf16 v[136:139], v[80:83], v[196:199], v[136:139]\n\t" \
  "v_mfma_f32_16x16x32_bf16 v[140:143], v[84:87], v[196:199], v[140:143]\n\t" \
  "v_mfma_f32_16x16x32_bf16 v[128:131], v[88:91], v[200:203], v[128:131]\n\t" \
  "v_mfma_f32_16x16x32_bf16 v[132:135], v[92:95], v[200:203], v[132:135]\n\t" \
  "v_mfma_f32_16x16x32_bf16 v[136:139], v[88:91], v[204:207], v[136:139]\n\t" \
  "v_mfma_f32_16x16x32_bf16 v[140:143], v[92:95], v[204:207], v[140:143]\n\t" \
  "v_mfma_f32_16x16x32_bf16 v[128:131], v[96:99], v[208:211], v[128:131]\n\t" \
  "v_mfma_f32_16x16x32_bf16 v[132:135], v[100:103], v[208:211], v[132:135]\n\t" \
  "v_mfma_f32_16x16x32_bf16 v[136:139], v[96:99], v[212:215], v[136:139]\n\t" \
  "v_mfma_f32_16x16x32_bf16 v[140:143], v[100:103], v[212:215], v[140:143]\n\t" \
  "v_mfma_f32_16x16x32_bf16 v[128:131], v[104:107], v[216:219], v[128:131]\n\t" \
  "v_mfma_f32_16x16x32_bf16 v[132:135], v[108:111], v[216:219], v[132:135]\n\t" \
  "v_mfma_f32_16x16x32_bf16 v[136:139], v[104:107], v[220:223], v[136:139]\n\t" \
  "v_mfma_f32_16x16x32_bf16 v[140:143], v[108:111], v[220:223], v[140:143]\n\t" \
  "v_mfma_f32_16x16x32_bf16 v[128:131], v[112:115], v[224:227], v[128:131]\n\t" \
  "v_mfma_f32_16x16x32_bf16 v[132:135], v[116:119], v[224:227], v[132:135]\n\t" \
  "v_mfma_f32_16x16x32_bf16 v[136:139], v[112:115], v[228:231], v[136:139]\n\t" \
  "v_mfma_f32_16x16x32_bf16 v[140:143], v[116:119], v[228:231], v[140:143]\n\t" \
  "v_mfma_f32_16x16x32_bf16 v[128:131], v[120:123], v[232:235], v[128:131]\n\t" \
  "v_mfma_f32_16x16x32_bf16 v[132:135], v[124:127], v[232:235], v[132:135]\n\t" \
  "v_mfma_f32_16x16x32_bf16 v[136:139], v[120:123], v[236:239], v[136:139]\n\t" \
  "v_mfma_f32_16x16x32_bf16 v[140:143], v[124:127], v[236:239], v[140:143]\n\t" \
  "s_setprio 0\n\t" \
  "s_waitcnt vmcnt(9)\n\t" \
  "s_barrier\n\t" \
  "s_add_u32 s92, s92, 512\n\t" \
  "s_addc_u32 s93, s93, 0\n\t" \
  "ds_read_b128 v[176:179], v244 offset:0\n\t" \
  "ds_read_b128 v[180:183], v244 offset:16384\n\t" \
  "ds_read_b128 v[184:187], v245 offset:0\n\t" \
  "ds_read_b128 v[188:191], v245 offset:16384\n\t" \
  "s_add_u32 m0, %[lw], 32768\n\t" \
  "s_nop 0\n\t" \
  "global_load_lds_dwordx4 v248, s[92:93] sc1\n\t" \
  "ds_read_b128 v[192:195], v246 offset:0\n\t" \
  "ds_read_b128 v[196:199], v246 offset:16384\n\t" \
  "ds_read_b128 v[200:203], v247 offset:0\n\t" \
  "ds_read_b128 v[204:207], v247 offset:16384\n\t" \
  "s_add_u32 m0, %[lw], 33792\n\t" \
  "s_nop 0\n\t" \
  "global_load_lds_dwordx4 v249, s[92:93] sc1\n\t" \
  "ds_read_b128 v[208:211], v244 offset:256\n\t" \
  "ds_read_b128 v[212:215], v244 offset:16640\n\t" \
  "ds_read_b128 v[216:219], v245 offset:256\n\t" \
  "ds_read_b128 v[220:223], v245 offset:16640\n\t" \
  "s_add_u32 m0, %[lw], 34816\n\t" \
  "s_nop 0\n\t" \
  "global_load_lds_dwordx4 v250, s[92:93] sc1\n\t" \
  "ds_read_b128 v[224:227], v246 offset:256\n\t" \
  "ds_read_b128 v[228:231], v246 offset:16640\n\t" \
  "ds_read_b128 v[232:235], v247 offset:256\n\t" \
  "ds_read_b128 v[236:239], v247 offset:16640\n\t" \
  "s_add_u32 m0, %[lw], 35840\n\t" \
  "s_nop 0\n\t" \
  "global_load_lds_dwordx4 v251, s[92:93] sc1\n\t" \
  "ds_write_b128 v252, v[136:139] offset:0\n\t" \
  "ds_write_b128 v252, v[140:143] offset:1024\n\t" \
  "s_waitcnt lgkmcnt(0)\n\t" \
  "s_setprio 1\n\t" \
  "v_mfma_f32_16x16x32_bf16 v[144:147], v[0:3], v[176:179], 0\n\t" \
  "v_mfma_f32_16x16x32_bf16 v[148:151], v[4:7], v[176:179], 0\n\t" \
  "v_mfma_f32_16x16x32_bf16 v[152:155], v[0:3], v[180:183], 0\n\t" \
  "v_mfma_f32_16x16x32_bf16 v[156:159], v[4:7], v[180:183], 0\n\t" \
  "v_mfma_f32_16x16x32_bf16 v[144:147], v[8:11], v[184:187], v[144:147]\n\t" \
  "v_mfma_f32_16x16x32_bf16 v[148:151], v[12:15], v[184:187], v[148:151]\n\t" \
  "v_mfma_f32_16x16x32_bf16 v[152:155], v[8:11], v[188:191], v[152:155]\n\t" \
  "v_mfma_f32_16x16x32_bf16 v[156:159], v[12:15], v[188:191], v[156:159]\n\t" \
  "v_mfma_f32_16x16x32_bf16 v[144:147], v[16:19], v[192:195], v[144:147]\n\t" \
  "v_mfma_f32_16x16x32_bf16 v[148:151], v[20:23], v[192:195], v[148:151]\n\t" \
  "v_mfma_f32_16x16x32_bf16 v[152:155], v[16:19], v[196:199], v[152:155]\n\t" \
  "v_mfma_f32_16x16x32_bf16 v[156:159], v[20:23], v[196:199], v[156:159]\n\t" \
  "v_mfma_f32_16x16x32_bf16 v[144:147], v[24:27], v[200:203], v[144:147]\n\t" \
  "v_mfma_f32_16x16x32_bf16 v[148:151], v[28:31], v[200:203], v[148:151]\n\t" \
  "v_mfma_f32_16x16x32_bf16 v[152:155], v[24:27], v[204:207], v[152:155]\n\t" \
  "v_mfma_f32_16x16x32_bf16 v[156:159], v[28:31], v[204:207], v[156:159]\n\t" \
  "v_mfma_f32_16x16x32_bf16 v[144:147], v[32:35], v[208:211], v[144:147]\n\t" \
  "v_mfma_f32_16x16x32_bf16 v[148:151], v[36:39], v[208:211], v[148:151]\n\t" \
  "v_mfma_f32_16x16x32_bf16 v[152:155], v[32:35], v[212:215], v[152:155]\n\t" \
  "v_mfma_f32_16x16x32_bf16 v[156:159], v[36:39], v[212:215], v[156:159]\n\t" \
  "v_mfma_f32_16x16x32_bf16 v[144:147], v[40:43], v[216:219], v[144:147]\n\t" \
  "v_mfma_f32_16x16x32_bf16 v[148:151], v[44:47], v[216:219], v[148:151]\n\t" \
  "v_mfma_f32_16x16x32_bf16 v[152:155], v[40:43], v[220:223], v[152:155]\n\t" \
  "v_mfma_f32_16x16x32_bf16 v[156:159], v[44:47], v[220:223], v[156:159]\n\t" \
  "v_mfma_f32_16x16x32_bf16 v[144:147], v[48:51], v[224:227], v[144:147]\n\t" \
  "v_mfma_f32_16x16x32_bf16 v[148:151], v[52:55], v[224:227], v[148:151]\n\t" \
  "v_mfma_f32_16x16x32_bf16 v[152:155], v[48:51], v[228:231], v[152:155]\n\t" \
  "v_mfma_f32_16x16x32_bf16 v[156:159], v[52:55], v[228:231], v[156:159]\n\t" \
  "v_mfma_f32_16x16x32_bf16 v[144:147], v[56:59], v[232:235], v[144:147]\n\t" \
  "v_mfma_f32_16x16x32_bf16 v[148:151], v[60:63], v[232:235], v[148:151]\n\t" \
  "v_mfma_f32_16x16x32_bf16 v[152:155], v[56:59], v[236:239], v[152:155]\n\t" \
  "v_mfma_f32_16x16x32_bf16 v[156:159], v[60:63], v[236:239], v[156:159]\n\t" \
  "s_setprio 0\n\t" \
  "s_waitcnt vmcnt(4)\n\t" \
  "s_barrier\n\t" \
  "s_add_u32 s92, s92, 65024\n\t" \
  "s_addc_u32 s93, s93, 0\n\t" \
  "ds_read_b128 v[160:163], v253 offset:0\n\t" \
  "ds_read_b128 v[164:167], v253 offset:1024\n\t" \
  "ds_read_b128 v[176:179], v244 offset:32768\n\t" \
  "ds_read_b128 v[180:183], v244 offset:49152\n\t" \
  "ds_read_b128 v[184:187], v245 offset:32768\n\t" \
  "ds_read_b128 v[188:191], v245 offset:49152\n\t" \
  "s_add_u32 m0, %[lw], 65536\n\t" \
  "s_nop 0\n\t" \
  "global_load_lds_dwordx4 v248, s[92:93] sc1\n\t" \
  "ds_read_b128 v[192:195], v246 offset:32768\n\t" \
  "ds_read_b128 v[196:199], v246 offset:49152\n\t" \
  "ds_read_b128 v[200:203], v247 offset:32768\n\t" \
  "ds_read_b128 v[204:207], v247 offset:49152\n\t" \
  "s_add_u32 m0, %[lw], 66560\n\t" \
  "s_nop 0\n\t" \
  "global_load_lds_dwordx4 v249, s[92:93] sc1\n\t" \
  "ds_read_b128 v[208:211], v244 offset:33024\n\t" \
  "ds_read_b128 v[212:215], v244 offset:49408\n\t" \
  "ds_read_b128 v[216:219], v245 offset:33024\n\t" \
  "ds_read_b128 v[220:223], v245 offset:49408\n\t" \
  "s_add_u32 m0, %[lw], 67584\n\t" \
  "s_nop 0\n\t" \
  "global_load_lds_dwordx4 v250, s[92:93] sc1\n\t" \
  "ds_read_b128 v[224:227], v246 offset:33024\n\t" \
  "ds_read_b128 v[228:231], v246 offset:49408\n\t" \
  "ds_read_b128 v[232:235], v247 offset:33024\n\t" \
  "ds_read_b128 v[236:239], v247 offset:49408\n\t" \
  "s_add_u32 m0, %[lw], 68608\n\t" \
  "s_nop 0\n\t" \
  "global_load_lds_dwordx4 v251, s[92:93] sc1\n\t" \
  "s_waitcnt lgkmcnt(15)\n\t" \
  "s_waitcnt lgkmcnt(12)\n\t" \
  "s_lshr_b32 s90, s87, 7\n\t" \
  "s_xor_b32 s90, s90, %[s0]\n\t" \
  "s_and_b32 s90, s90, 1\n\t" \
  "s_lshl1_add_u32 s90, s87, s90\n\t" \
  "s_lshl_b32 s90, s90, 13\n\t" \
  "v_add_f32 v160, v160, v128\n\t" \
  "v_add_f32 v161, v161, v129\n\t" \
  "v_add_f32 v162, v162, v130\n\t" \
  "v_add_f32 v163, v163, v131\n\t" \
  "v_add_f32 v164, v164, v132\n\t" \
  "v_add_f32 v165, v165, v133\n\t" \
  "v_add_f32 v166, v166, v134\n\t" \
  "v_add_f32 v167, v167, v135\n\t" \
  "v_cvt_pk_bf16_f32 v168, v160, v161\n\t" \
  "v_cvt_pk_bf16_f32 v169, v162, v163\n\t" \
  "v_cvt_pk_bf16_f32 v170, v164, v165\n\t" \
  "v_cvt_pk_bf16_f32 v171, v166, v167\n\t" \
  "buffer_store_dwordx4 v[168:171], v172, %[rt], s90 offen sc1\n\t" \
  "s_add_u32 s86, s86, 1\n\t" \
  "s_add_u32 s87, s87, 1\n\t" \
  "s_waitcnt lgkmcnt(0)\n\t" \
  "s_setprio 1\n\t" \
  "v_mfma_f32_16x16x32_bf16 v[144:147], v[64:67], v[176:179], v[144:147]\n\t" \
  "v_mfma_f32_16x16x32_bf16 v[148:151], v[68:71], v[176:179], v[148:151]\n\t" \
  "v_mfma_f32_16x16x32_bf16 v[152:155], v[64:67], v[180:183], v[152:155]\n\t" \
  "v_mfma_f32_16x16x32_bf16 v[156:159], v[68:71], v[180:183], v[156:159]\n\t" \
  "v_mfma_f32_16x16x32_bf16 v[144:147], v[72:75], v[184:187], v[144:147]\n\t" \
  "v_mfma_f32_16x16x32_bf16 v[148:151], v[76:79], v[184:187], v[148:151]\n\t" \
  "v_mfma_f32_16x16x32_bf16 v[152:155], v[72:75], v[188:191], v[152:155]\n\t" \
  "v_mfma_f32_16x16x32_bf16 v[156:159], v[76:79], v[188:191], v[156:159]\n\t" \
  "v_mfma_f32_16x16x32_bf16 v[144:147], v[80:83], v[192:195], v[144:147]\n\t" \
  "v_mfma_f32_16x16x32_bf16 v[148:151], v[84:87], v[192:195], v[148:151]\n\t" \
  "v_mfma_f32_16x16x32_bf16 v[152:155], v[80:83], v[196:199], v[152:155]\n\t" \
  "v_mfma_f32_16x16x32_bf16 v[156:159], v[84:87], v[196:199], v[156:159]\n\t" \
  "v_mfma_f32_16x16x32_bf16 v[144:147], v[88:91], v[200:203], v[144:147]\n\t" \
  "v_mfma_f32_16x16x32_bf16 v[148:151], v[92:95], v[200:203], v[148:151]\n\t" \
  "v_mfma_f32_16x16x32_bf16 v[152:155], v[88:91], v[204:207], v[152:155]\n\t" \
  "v_mfma_f32_16x16x32_bf16 v[156:159], v[92:95], v[204:207], v[156:159]\n\t" \
  "v_mfma_f32_16x16x32_bf16 v[144:147], v[96:99], v[208:211], v[144:147]\n\t" \
  "v_mfma_f32_16x16x32_bf16 v[148:151], v[100:103], v[208:211], v[148:151]\n\t" \
  "v_mfma_f32_16x16x32_bf16 v[152:155], v[96:99], v[212:215], v[152:155]\n\t" \
  "v_mfma_f32_16x16x32_bf16 v[156:159], v[100:103], v[212:215], v[156:159]\n\t" \
  "v_mfma_f32_16x16x32_bf16 v[144:147], v[104:107], v[216:219], v[144:147]\n\t" \
  "v_mfma_f32_16x16x32_bf16 v[148:151], v[108:111], v[216:219], v[148:151]\n\t" \
  "v_mfma_f32_16x16x32_bf16 v[152:155], v[104:107], v[220:223], v[152:155]\n\t" \
  "v_mfma_f32_16x16x32_bf16 v[156:159], v[108:111], v[220:223], v[156:159]\n\t" \
  "v_mfma_f32_16x16x32_bf16 v[144:147], v[112:115], v[224:227], v[144:147]\n\t" \
  "v_mfma_f32_16x16x32_bf16 v[148:151], v[116:119], v[224:227], v[148:151]\n\t" \
  "v_mfma_f32_16x16x32_bf16 v[152:155], v[112:115], v[228:231], v[152:155]\n\t" \
  "v_mfma_f32_16x16x32_bf16 v[156:159], v[116:119], v[228:231], v[156:159]\n\t" \
  "v_mfma_f32_16x16x32_bf16 v[144:147], v[120:123], v[232:235], v[144:147]\n\t" \
  "v_mfma_f32_16x16x32_bf16 v[148:151], v[124:127], v[232:235], v[148:151]\n\t" \
  "v_mfma_f32_16x16x32_bf16 v[152:155], v[120:123], v[236:239], v[152:155]\n\t" \
  "v_mfma_f32_16x16x32_bf16 v[156:159], v[124:127], v[236:239], v[156:159]\n\t" \
  "s_setprio 0\n\t" \
  "s_cmp_lt_u32 s86, 64\n\t" \
  "s_cbranch_scc1 .Lp0%=\n\t" \
  "s_barrier\n\t" \
  "s_nop 2\n\t" \
  "ds_write_b128 v252, v[152:155] offset:0\n\t" \
  "ds_write_b128 v252, v[156:159] offset:1024\n\t" \
  "s_waitcnt lgkmcnt(0)\n\t" \
  "s_barrier\n\t" \
  "ds_read_b128 v[160:163], v253 offset:0\n\t" \
  "ds_read_b128 v[164:167], v253 offset:1024\n\t" \
  "s_waitcnt lgkmcnt(0)\n\t" \
  "s_lshr_b32 s90, s87, 7\n\t" \
  "s_xor_b32 s90, s90, %[s0]\n\t" \
  "s_and_b32 s90, s90, 1\n\t" \
  "s_lshl1_add_u32 s90, s87, s90\n\t" \
  "s_lshl_b32 s90, s90, 13\n\t" \
  "v_add_f32 v160, v160, v144\n\t" \
  "v_add_f32 v161, v161, v145\n\t" \
  "v_add_f32 v162, v162, v146\n\t" \
  "v_add_f32 v163, v163, v147\n\t" \
  "v_add_f32 v164, v164, v148\n\t" \
  "v_add_f32 v165, v165, v149\n\t" \
  "v_add_f32 v166, v166, v150\n\t" \
  "v_add_f32 v167, v167, v151\n\t" \
  "v_cvt_pk_bf16_f32 v168, v160, v161\n\t" \
  "v_cvt_pk_bf16_f32 v169, v162, v163\n\t" \
  "v_cvt_pk_bf16_f32 v170, v164, v165\n\t" \
  "v_cvt_pk_bf16_f32 v171, v166, v167\n\t" \
  "buffer_store_dwordx4 v[168:171], v172, %[rt], s90 offen sc1\n\t" \
  "s_waitcnt vmcnt(0)\n\t" \
  "s_barrier\n\t" \
  "s_mov_b32 m0, s94\n\t" \
  "s_branch .Le%=\n\t" \
  ".Lk%=:\n\t" \
  "s_mov_b32 s94, m0\n\t" \
  "s_mov_b64 s[92:93], %[x0]\n\t" \
  "s_mov_b32 s86, %[g0]\n\t" \
  "s_add_u32 s87, s86, %[t0]\n\t" \
  "s_add_u32 m0, %[lw], 67584\n\t" \
  "s_nop 0\n\t" \
  "global_load_lds_dwordx4 v250, s[92:93] sc1\n\t" \
  "s_add_u32 m0, %[lw], 68608\n\t" \
  "s_nop 0\n\t" \
  "global_load_lds_dwordx4 v251, s[92:93] sc1\n\t" \
  "s_barrier\n\t" \
  "s_branch .Ls1%=\n\t" \
  ".Lp1%=:\n\t" \
  "s_waitcnt vmcnt(10)\n\t" \
  "s_barrier\n\t" \
  "s_setprio 2\n\t" \
  "v_mfma_f32_16x16x32_bf16 v[144:147], v[64:67], v[176:179], v[144:147]\n\t" \
  "v_mfma_f32_16x16x32_bf16 v[148:151], v[68:71], v[176:179], v[148:151]\n\t" \
  "v_mfma_f32_16x16x32_bf16 v[152:155], v[64:67], v[180:183], v[152:155]\n\t" \
  "v_mfma_f32_16x16x32_bf16 v[156:159], v[68:71], v[180:183], v[156:159]\n\t" \
  "v_mfma_f32_16x16x32_bf16 v[144:147], v[72:75], v[184:187], v[144:147]\n\t" \
  "v_mfma_f32_16x16x32_bf16 v[148:151], v[76:79], v[184:187], v[148:151]\n\t" \
  "v_mfma_f32_16x16x32_bf16 v[152:155], v[72:75], v[188:191], v[152:155]\n\t" \
  "v_mfma_f32_16x16x32_bf16 v[156:159], v[76:79], v[188:191], v[156:159]\n\t" \
  "v_mfma_f32_16x16x32_bf16 v[144:147], v[80:83], v[192:195], v[144:147]\n\t" \
  "v_mfma_f32_16x16x32_bf16 v[148:151], v[84:87], v[192:195], v[148:151]\n\t" \
  "v_mfma_f32_16x16x32_bf16 v[152:155], v[80:83], v[196:199], v[152:155]\n\t" \
  "v_mfma_f32_16x16x32_bf16 v[156:159], v[84:87], v[196:199], v[156:159]\n\t" \
  "v_mfma_f32_16x16x32_bf16 v[144:147], v[88:91], v[200:203], v[144:147]\n\t" \
  "v_mfma_f32_16x16x32_bf16 v[148:151], v[92:95], v[200:203], v[148:151]\n\t" \
  "v_mfma_f32_16x16x32_bf16 v[152:155], v[88:91], v[204:207], v[152:155]\n\t" \
  "v_mfma_f32_16x16x32_bf16 v[156:159], v[92:95], v[204:207], v[156:159]\n\t" \
  "v_mfma_f32_16x16x32_bf16 v[144:147], v[96:99], v[208:211], v[144:147]\n\t" \
  "v_mfma_f32_16x16x32_bf16 v[148:151], v[100:103], v[208:211], v[148:151]\n\t" \
  "v_mfma_f32_16x16x32_bf16 v[152:155], v[96:99], v[212:215], v[152:155]\n\t" \
  "v_mfma_f32_16x16x32_bf16 v[156:159], v[100:103], v[212:215], v[156:159]\n\t" \
  "v_mfma_f32_16x16x32_bf16 v[144:147], v[104:107], v[216:219], v[144:147]\n\t" \
  "v_mfma_f32_16x16x32_bf16 v[148:151], v[108:111], v[216:219], v[148:151]\n\t" \
  "v_mfma_f32_16x16x32_bf16 v[152:155], v[104:107], v[220:223], v[152:155]\n\t" \
  "v_mfma_f32_16x16x32_bf16 v[156:159], v[108:111], v[220:223], v[156:159]\n\t" \
  "v_mfma_f32_16x16x32_bf16 v[144:147], v[112:115], v[224:227], v[144:147]\n\t" \
  "v_mfma_f32_16x16x32_bf16 v[148:151], v[116:119], v[224:227], v[148:151]\n\t" \
  "v_mfma_f32_16x16x32_bf16 v[152:155], v[112:115], v[228:231], v[152:155]\n\t" \
  "v_mfma_f32_16x16x32_bf16 v[156:159], v[116:119], v[228:231], v[156:159]\n\t" \
  "v_mfma_f32_16x16x32_bf16 v[144:147], v[120:123], v[232:235], v[144:147]\n\t" \
  "v_mfma_f32_16x16x32_bf16 v[148:151], v[124:127], v[232:235], v[148:151]\n\t" \
  "v_mfma_f32_16x16x32_bf16 v[152:155], v[120:123], v[236:239], v[152:155]\n\t" \
  "v_mfma_f32_16x16x32_bf16 v[156:159], v[124:127], v[236:239], v[156:159]\n\t" \
  "s_setprio 0\n\t" \
  ".Ls1%=:\n\t" \
  "s_add_u32 s92, s92, 512\n\t" \
  "s_addc_u32 s93, s93, 0\n\t" \
  "ds_read_b128 v[176:179], v240 offset:0\n\t" \
  "ds_read_b128 v[180:183], v240 offset:16384\n\t" \
  "ds_read_b128 v[184:187], v241 offset:0\n\t" \
  "ds_read_b128 v[188:191], v241 offset:16384\n\t" \
  "s_add_u32 m0, %[lw], 98304\n\t" \
  "s_nop 0\n\t" \
  "global_load_lds_dwordx4 v248, s[92:93] sc1\n\t" \
  "ds_read_b128 v[192:195], v242 offset:0\n\t" \
  "ds_read_b128 v[196:199], v242 offset:16384\n\t" \
  "ds_read_b128 v[200:203], v243 offset:0\n\t" \
  "ds_read_b128 v[204:207], v243 offset:16384\n\t" \
  "s_add_u32 m0, %[lw], 99328\n\t" \
  "s_nop 0\n\t" \
  "global_load_lds_dwordx4 v249, s[92:93] sc1\n\t" \
  "ds_read_b128 v[208:211], v240 offset:256\n\t" \
  "ds_read_b128 v[212:215], v240 offset:16640\n\t" \
  "ds_read_b128 v[216:219], v241 offset:256\n\t" \
  "ds_read_b128 v[220:223], v241 offset:16640\n\t" \
  "s_add_u32 m0, %[lw], 100352\n\t" \
  "s_nop 0\n\t" \
  "global_load_lds_dwordx4 v250, s[92:93] sc1\n\t" \
  "ds_read_b128 v[224:227], v242 offset:256\n\t" \
  "ds_read_b128 v[228:231], v242 offset:16640\n\t" \
  "ds_read_b128 v[232:235], v243 offset:256\n\t" \
  "ds_read_b128 v[236:239], v243 offset:16640\n\t" \
  "s_add_u32 m0, %[lw], 101376\n\t" \
  "s_nop 0\n\t" \
  "global_load_lds_dwordx4 v251, s[92:93] sc1\n\t" \
  "ds_write_b128 v252, v[152:155] offset:0\n\t" \
  "ds_write_b128 v252, v[156:159] offset:1024\n\t" \
  "s_waitcnt lgkmcnt(0)\n\t" \
  "s_waitcnt vmcnt(4)\n\t" \
  "s_barrier\n\t" \
  "s_setprio 2\n\t" \
  "v_mfma_f32_16x16x32_bf16 v[128:131], v[0:3], v[176:179], 0\n\t" \
  "v_mfma_f32_16x16x32_bf16 v[132:135], v[4:7], v[176:179], 0\n\t" \
  "v_mfma_f32_16x16x32_bf16 v[136:139], v[0:3], v[180:183], 0\n\t" \
  "v_mfma_f32_16x16x32_bf16 v[140:143], v[4:7], v[180:183], 0\n\t" \
  "v_mfma_f32_16x16x32_bf16 v[128:131], v[8:11], v[184:187], v[128:131]\n\t" \
  "v_mfma_f32_16x16x32_bf16 v[132:135], v[12:15], v[184:187], v[132:135]\n\t" \
  "v_mfma_f32_16x16x32_bf16 v[136:139], v[8:11], v[188:191], v[136:139]\n\t" \
  "v_mfma_f32_16x16x32_bf16 v[140:143], v[12:15], v[188:191], v[140:143]\n\t" \
  "v_mfma_f32_16x16x32_bf16 v[128:131], v[16:19], v[192:195], v[128:131]\n\t" \
  "v_mfma_f32_16x16x32_bf16 v[132:135], v[20:23], v[192:195], v[132:135]\n\t" \
  "v_mfma_f32_16x16x32_bf16 v[136:139], v[16:19], v[196:199], v[136:139]\n\t" \
  "v_mfma_f32_16x16x32_bf16 v[140:143], v[20:23], v[196:199], v[140:143]\n\t" \
  "v_mfma_f32_16x16x32_bf16 v[128:131], v[24:27], v[200:203], v[128:131]\n\t" \
  "v_mfma_f32_16x16x32_bf16 v[132:135], v[28:31], v[200:203], v[132:135]\n\t" \
  "v_mfma_f32_16x16x32_bf16 v[136:139], v[24:27], v[204:207], v[136:139]\n\t" \
  "v_mfma_f32_16x16x32_bf16 v[140:143], v[28:31], v[204:207], v[140:143]\n\t" \
  "v_mfma_f32_16x16x32_bf16 v[128:131], v[32:35], v[208:211], v[128:131]\n\t" \
  "v_mfma_f32_16x16x32_bf16 v[132:135], v[36:39], v[208:211], v[132:135]\n\t" \
  "v_mfma_f32_16x16x32_bf16 v[136:139], v[32:35], v[212:215], v[136:139]\n\t" \
  "v_mfma_f32_16x16x32_bf16 v[140:143], v[36:39], v[212:215], v[140:143]\n\t" \
  "v_mfma_f32_16x16x32_bf16 v[128:131], v[40:43], v[216:219], v[128:131]\n\t" \
  "v_mfma_f32_16x16x32_bf16 v[132:135], v[44:47], v[216:219], v[132:135]\n\t" \
  "v_mfma_f32_16x16x32_bf16 v[136:139], v[40:43], v[220:223], v[136:139]\n\t" \
  "v_mfma_f32_16x16x32_bf16 v[140:143], v[44:47], v[220:223], v[140:143]\n\t" \
  "v_mfma_f32_16x16x32_bf16 v[128:131], v[48:51], v[224:227], v[128:131]\n\t" \
  "v_mfma_f32_16x16x32_bf16 v[132:135], v[52:55], v[224:227], v[132:135]\n\t" \
  "v_mfma_f32_16x16x32_bf16 v[136:139], v[48:51], v[228:231], v[136:139]\n\t" \
  "v_mfma_f32_16x16x32_bf16 v[140:143], v[52:55], v[228:231], v[140:143]\n\t" \
  "v_mfma_f32_16x16x32_bf16 v[128:131], v[56:59], v[232:235], v[128:131]\n\t" \
  "v_mfma_f32_16x16x32_bf16 v[132:135], v[60:63], v[232:235], v[132:135]\n\t" \
  "v_mfma_f32_16x16x32_bf16 v[136:139], v[56:59], v[236:239], v[136:139]\n\t" \
  "v_mfma_f32_16x16x32_bf16 v[140:143], v[60:63], v[236:239], v[140:143]\n\t" \
  "s_setprio 0\n\t" \
  "s_add_u32 s92, s92, 65024\n\t" \
  "s_addc_u32 s93, s93, 0\n\t" \
  "ds_read_b128 v[160:163], v253 offset:0\n\t" \
  "ds_read_b128 v[164:167], v253 offset:1024\n\t" \
  "ds_read_b128 v[176:179], v240 offset:32768\n\t" \
  "ds_read_b128 v[180:183], v240 offset:49152\n\t" \
  "ds_read_b128 v[184:187], v241 offset:32768\n\t" \
  "ds_read_b128 v[188:191], v241 offset:49152\n\t" \
  "s_add_u32 m0, %[lw], 0\n\t" \
  "s_nop 0\n\t" \
  "global_load_lds_dwordx4 v248, s[92:93] sc1\n\t" \
  "ds_read_b128 v[192:195], v242 offset:32768\n\t" \
  "ds_read_b128 v[196:199], v242 offset:49152\n\t" \
  "ds_read_b128 v[200:203], v243 offset:32768\n\t" \
  "ds_read_b128 v[204:207], v243 offset:49152\n\t" \
  "s_add_u32 m0, %[lw], 1024\n\t" \
  "s_nop 0\n\t" \
  "global_load_lds_dwordx4 v249, s[92:93] sc1\n\t" \
  "ds_read_b128 v[208:211], v240 offset:33024\n\t" \
  "ds_read_b128 v[212:215], v240 offset:49408\n\t" \
  "ds_read_b128 v[216:219], v241 offset:33024\n\t" \
  "ds_read_b128 v[220:223], v241 offset:49408\n\t" \
  "s_add_u32 m0, %[lw], 2048\n\t" \
  "s_nop 0\n\t" \
  "global_load_lds_dwordx4 v250, s[92:93] sc1\n\t" \
  "ds_read_b128 v[224:227], v242 offset:33024\n\t" \
  "ds_read_b128 v[228:231], v242 offset:49408\n\t" \
  "ds_read_b128 v[232:235], v243 offset:33024\n\t" \
  "ds_read_b128 v[236:239], v243 offset:49408\n\t" \
  "s_add_u32 m0, %[lw], 3072\n\t" \
  "s_nop 0\n\t" \
  "global_load_lds_dwordx4 v251, s[92:93] sc1\n\t" \
  "s_waitcnt lgkmcnt(15)\n\t" \
  "s_waitcnt lgkmcnt(12)\n\t" \
  "s_sub_i32 s91, s86, 2\n\t" \
  "v_mov_b32 v173, s91\n\t" \
  "buffer_store_dword v173, v174, %[rs], 0 offen sc1\n\t" \
  "s_lshr_b32 s90, s87, 7\n\t" \
  "s_xor_b32 s90, s90, %[s0]\n\t" \
  "s_and_b32 s90, s90, 1\n\t" \
  "s_lshl1_add_u32 s90, s87, s90\n\t" \
  "s_lshl_b32 s90, s90, 13\n\t" \
  "v_add_f32 v160, v160, v144\n\t" \
  "v_add_f32 v161, v161, v145\n\t" \
  "v_add_f32 v162, v162, v146\n\t" \
  "v_add_f32 v163, v163, v147\n\t" \
  "v_add_f32 v164, v164, v148\n\t" \
  "v_add_f32 v165, v165, v149\n\t" \
  "v_add_f32 v166, v166, v150\n\t" \
  "v_add_f32 v167, v167, v151\n\t" \
  "v_cvt_pk_bf16_f32 v168, v160, v161\n\t" \
  "v_cvt_pk_bf16_f32 v169, v162, v163\n\t" \
  "v_cvt_pk_bf16_f32 v170, v164, v165\n\t" \
  "v_cvt_pk_bf16_f32 v171, v166, v167\n\t" \
  "buffer_store_dwordx4 v[168:171], v172, %[rt], s90 offen sc1\n\t" \
  "s_add_u32 s86, s86, 1\n\t" \
  "s_add_u32 s87, s87, 1\n\t" \
  "s_waitcnt lgkmcnt(0)\n\t" \
  "s_waitcnt vmcnt(10)\n\t" \
  "s_barrier\n\t" \
  "s_setprio 2\n\t" \
  "v_mfma_f32_16x16x32_bf16 v[128:131], v[64:67], v[176:179], v[128:131]\n\t" \
  "v_mfma_f32_16x16x32_bf16 v[132:135], v[68:71], v[176:179], v[132:135]\n\t" \
  "v_mfma_f32_16x16x32_bf16 v[136:139], v[64:67], v[180:183], v[136:139]\n\t" \
  "v_mfma_f32_16x16x32_bf16 v[140:143], v[68:71], v[180:183], v[140:143]\n\t" \
  "v_mfma_f32_16x16x32_bf16 v[128:131], v[72:75], v[184:187], v[128:131]\n\t" \
  "v_mfma_f32_16x16x32_bf16 v[132:135], v[76:79], v[184:187], v[132:135]\n\t" \
  "v_mfma_f32_16x16x32_bf16 v[136:139], v[72:75], v[188:191], v[136:139]\n\t" \
  "v_mfma_f32_16x16x32_bf16 v[140:143], v[76:79], v[188:191], v[140:143]\n\t" \
  "v_mfma_f32_16x16x32_bf16 v[128:131], v[80:83], v[192:195], v[128:131]\n\t" \
  "v_mfma_f32_16x16x32_bf16 v[132:135], v[84:87], v[192:195], v[132:135]\n\t" \
  "v_mfma_f32_16x16x32_bf16 v[136:139], v[80:83], v[196:199], v[136:139]\n\t" \
  "v_mfma_f32_16x16x32_bf16 v[140:143], v[84:87], v[196:199], v[140:143]\n\t" \
  "v_mfma_f32_16x16x32_bf16 v[128:131], v[88:91], v[200:203], v[128:131]\n\t" \
  "v_mfma_f32_16x16x32_bf16 v[132:135], v[92:95], v[200:203], v[132:135]\n\t" \
  "v_mfma_f32_16x16x32_bf16 v[136:139], v[88:91], v[204:207], v[136:139]\n\t" \
  "v_mfma_f32_16x16x32_bf16 v[140:143], v[92:95], v[204:207], v[140:143]\n\t" \
  "v_mfma_f32_16x16x32_bf16 v[128:131], v[96:99], v[208:211], v[128:131]\n\t" \
  "v_mfma_f32_16x16x32_bf16 v[132:135], v[100:103], v[208:211], v[132:135]\n\t" \
  "v_mfma_f32_16x16x32_bf16 v[136:139], v[96:99], v[212:215], v[136:139]\n\t" \
  "v_mfma_f32_16x16x32_bf16 v[140:143], v[100:103], v[212:215], v[140:143]\n\t" \
  "v_mfma_f32_16x16x32_bf16 v[128:131], v[104:107], v[216:219], v[128:131]\n\t" \
  "v_mfma_f32_16x16x32_bf16 v[132:135], v[108:111], v[216:219], v[132:135]\n\t" \
  "v_mfma_f32_16x16x32_bf16 v[136:139], v[104:107], v[220:223], v[136:139]\n\t" \
  "v_mfma_f32_16x16x32_bf16 v[140:143], v[108:111], v[220:223], v[140:143]\n\t" \
  "v_mfma_f32_16x16x32_bf16 v[128:131], v[112:115], v[224:227], v[128:131]\n\t" \
  "v_mfma_f32_16x16x32_bf16 v[132:135], v[116:119], v[224:227], v[132:135]\n\t" \
  "v_mfma_f32_16x16x32_bf16 v[136:139], v[112:115], v[228:231], v[136:139]\n\t" \
  "v_mfma_f32_16x16x32_bf16 v[140:143], v[116:119], v[228:231], v[140:143]\n\t" \
  "v_mfma_f32_16x16x32_bf16 v[128:131], v[120:123], v[232:235], v[128:131]\n\t" \
  "v_mfma_f32_16x16x32_bf16 v[132:135], v[124:127], v[232:235], v[132:135]\n\t" \
  "v_mfma_f32_16x16x32_bf16 v[136:139], v[120:123], v[236:239], v[136:139]\n\t" \
  "v_mfma_f32_16x16x32_bf16 v[140:143], v[124:127], v[236:239], v[140:143]\n\t" \
  "s_setprio 0\n\t" \
  "s_add_u32 s92, s92, 512\n\t" \
  "s_addc_u32 s93, s93, 0\n\t" \
  "ds_read_b128 v[176:179], v244 offset:0\n\t" \
  "ds_read_b128 v[180:183], v244 offset:16384\n\t" \
  "ds_read_b128 v[184:187], v245 offset:0\n\t" \
  "ds_read_b128 v[188:191], v245 offset:16384\n\t" \
  "s_add_u32 m0, %[lw], 32768\n\t" \
  "s_nop 0\n\t" \
  "global_load_lds_dwordx4 v248, s[92:93] sc1\n\t" \
  "ds_read_b128 v[192:195], v246 offset:0\n\t" \
  "ds_read_b128 v[196:199], v246 offset:16384\n\t" \
  "ds_read_b128 v[200:203], v247 offset:0\n\t" \
  "ds_read_b128 v[204:207], v247 offset:16384\n\t" \
  "s_add_u32 m0, %[lw], 33792\n\t" \
  "s_nop 0\n\t" \
  "global_load_lds_dwordx4 v249, s[92:93] sc1\n\t" \
  "ds_read_b128 v[208:211], v244 offset:256\n\t" \
  "ds_read_b128 v[212:215], v244 offset:16640\n\t" \
  "ds_read_b128 v[216:219], v245 offset:256\n\t" \
  "ds_read_b128 v[220:223], v245 offset:16640\n\t" \
  "s_add_u32 m0, %[lw], 34816\n\t" \
  "s_nop 0\n\t" \
  "global_load_lds_dwordx4 v250, s[92:93] sc1\n\t" \
  "ds_read_b128 v[224:227], v246 offset:256\n\t" \
  "ds_read_b128 v[228:231], v246 offset:16640\n\t" \
  "ds_read_b128 v[232:235], v247 offset:256\n\t" \
  "ds_read_b128 v[236:239], v247 offset:16640\n\t" \
  "s_add_u32 m0, %[lw], 35840\n\t" \
  "s_nop 0\n\t" \
  "global_load_lds_dwordx4 v251, s[92:93] sc1\n\t" \
  "ds_write_b128 v252, v[136:139] offset:0\n\t" \
  "ds_write_b128 v252, v[140:143] offset:1024\n\t" \
  "s_waitcnt lgkmcnt(0)\n\t" \
  "s_waitcnt vmcnt(4)\n\t" \
  "s_barrier\n\t" \
  "s_setprio 2\n\t" \
  "v_mfma_f32_16x16x32_bf16 v[144:147], v[0:3], v[176:179], 0\n\t" \
  "v_mfma_f32_16x16x32_bf16 v[148:151], v[4:7], v[176:179], 0\n\t" \
  "v_mfma_f32_16x16x32_bf16 v[152:155], v[0:3], v[180:183], 0\n\t" \
  "v_mfma_f32_16x16x32_bf16 v[156:159], v[4:7], v[180:183], 0\n\t" \
  "v_mfma_f32_16x16x32_bf16 v[144:147], v[8:11], v[184:187], v[144:147]\n\t" \
  "v_mfma_f32_16x16x32_bf16 v[148:151], v[12:15], v[184:187], v[148:151]\n\t" \
  "v_mfma_f32_16x16x32_bf16 v[152:155], v[8:11], v[188:191], v[152:155]\n\t" \
  "v_mfma_f32_16x16x32_bf16 v[156:159], v[12:15], v[188:191], v[156:159]\n\t" \
  "v_mfma_f32_16x16x32_bf16 v[144:147], v[16:19], v[192:195], v[144:147]\n\t" \
  "v_mfma_f32_16x16x32_bf16 v[148:151], v[20:23], v[192:195], v[148:151]\n\t" \
  "v_mfma_f32_16x16x32_bf16 v[152:155], v[16:19], v[196:199], v[152:155]\n\t" \
  "v_mfma_f32_16x16x32_bf16 v[156:159], v[20:23], v[196:199], v[156:159]\n\t" \
  "v_mfma_f32_16x16x32_bf16 v[144:147], v[24:27], v[200:203], v[144:147]\n\t" \
  "v_mfma_f32_16x16x32_bf16 v[148:151], v[28:31], v[200:203], v[148:151]\n\t" \
  "v_mfma_f32_16x16x32_bf16 v[152:155], v[24:27], v[204:207], v[152:155]\n\t" \
  "v_mfma_f32_16x16x32_bf16 v[156:159], v[28:31], v[204:207], v[156:159]\n\t" \
  "v_mfma_f32_16x16x32_bf16 v[144:147], v[32:35], v[208:211], v[144:147]\n\t" \
  "v_mfma_f32_16x16x32_bf16 v[148:151], v[36:39], v[208:211], v[148:151]\n\t" \
  "v_mfma_f32_16x16x32_bf16 v[152:155], v[32:35], v[212:215], v[152:155]\n\t" \
  "v_mfma_f32_16x16x32_bf16 v[156:159], v[36:39], v[212:215], v[156:159]\n\t" \
  "v_mfma_f32_16x16x32_bf16 v[144:147], v[40:43], v[216:219], v[144:147]\n\t" \
  "v_mfma_f32_16x16x32_bf16 v[148:151], v[44:47], v[216:219], v[148:151]\n\t" \
  "v_mfma_f32_16x16x32_bf16 v[152:155], v[40:43], v[220:223], v[152:155]\n\t" \
  "v_mfma_f32_16x16x32_bf16 v[156:159], v[44:47], v[220:223], v[156:159]\n\t" \
  "v_mfma_f32_16x16x32_bf16 v[144:147], v[48:51], v[224:227], v[144:147]\n\t" \
  "v_mfma_f32_16x16x32_bf16 v[148:151], v[52:55], v[224:227], v[148:151]\n\t" \
  "v_mfma_f32_16x16x32_bf16 v[152:155], v[48:51], v[228:231], v[152:155]\n\t" \
  "v_mfma_f32_16x16x32_bf16 v[156:159], v[52:55], v[228:231], v[156:159]\n\t" \
  "v_mfma_f32_16x16x32_bf16 v[144:147], v[56:59], v[232:235], v[144:147]\n\t" \
  "v_mfma_f32_16x16x32_bf16 v[148:151], v[60:63], v[232:235], v[148:151]\n\t" \
  "v_mfma_f32_16x16x32_bf16 v[152:155], v[56:59], v[236:239], v[152:155]\n\t" \
  "v_mfma_f32_16x16x32_bf16 v[156:159], v[60:63], v[236:239], v[156:159]\n\t" \
  "s_setprio 0\n\t" \
  "s_add_u32 s92, s92, 65024\n\t" \
  "s_addc_u32 s93, s93, 0\n\t" \
  "ds_read_b128 v[160:163], v253 offset:0\n\t" \
  "ds_read_b128 v[164:167], v253 offset:1024\n\t" \
  "ds_read_b128 v[176:179], v244 offset:32768\n\t" \
  "ds_read_b128 v[180:183], v244 offset:49152\n\t" \
  "ds_read_b128 v[184:187], v245 offset:32768\n\t" \
  "ds_read_b128 v[188:191], v245 offset:49152\n\t" \
  "s_add_u32 m0, %[lw], 65536\n\t" \
  "s_nop 0\n\t" \
  "global_load_lds_dwordx4 v248, s[92:93] sc1\n\t" \
  "ds_read_b128 v[192:195], v246 offset:32768\n\t" \
  "ds_read_b128 v[196:199], v246 offset:49152\n\t" \
  "ds_read_b128 v[200:203], v247 offset:32768\n\t" \
  "ds_read_b128 v[204:207], v247 offset:49152\n\t" \
  "s_add_u32 m0, %[lw], 66560\n\t" \
  "s_nop 0\n\t" \
  "global_load_lds_dwordx4 v249, s[92:93] sc1\n\t" \
  "ds_read_b128 v[208:211], v244 offset:33024\n\t" \
  "ds_read_b128 v[212:215], v244 offset:49408\n\t" \
  "ds_read_b128 v[216:219], v245 offset:33024\n\t" \
  "ds_read_b128 v[220:223], v245 offset:49408\n\t" \
  "s_add_u32 m0, %[lw], 67584\n\t" \
  "s_nop 0\n\t" \
  "global_load_lds_dwordx4 v250, s[92:93] sc1\n\t" \
  "ds_read_b128 v[224:227], v246 offset:33024\n\t" \
  "ds_read_b128 v[228:231], v246 offset:49408\n\t" \
  "ds_read_b128 v[232:235], v247 offset:33024\n\t" \
  "ds_read_b128 v[236:239], v247 offset:49408\n\t" \
  "s_add_u32 m0, %[lw], 68608\n\t" \
  "s_nop 0\n\t" \
  "global_load_lds_dwordx4 v251, s[92:93] sc1\n\t" \
  "s_waitcnt lgkmcnt(15)\n\t" \
  "s_waitcnt lgkmcnt(12)\n\t" \
  "s_sub_i32 s91, s86, 2\n\t" \
  "v_mov_b32 v173, s91\n\t" \
  "buffer_store_dword v173, v174, %[rs], 0 offen sc1\n\t" \
  "s_lshr_b32 s90, s87, 7\n\t" \
  "s_xor_b32 s90, s90, %[s0]\n\t" \
  "s_and_b32 s90, s90, 1\n\t" \
  "s_lshl1_add_u32 s90, s87, s90\n\t" \
  "s_lshl_b32 s90, s90, 13\n\t" \
  "v_add_f32 v160, v160, v128\n\t" \
  "v_add_f32 v161, v161, v129\n\t" \
  "v_add_f32 v162, v162, v130\n\t" \
  "v_add_f32 v163, v163, v131\n\t" \
  "v_add_f32 v164, v164, v132\n\t" \
  "v_add_f32 v165, v165, v133\n\t" \
  "v_add_f32 v166, v166, v134\n\t" \
  "v_add_f32 v167, v167, v135\n\t" \
  "v_cvt_pk_bf16_f32 v168, v160, v161\n\t" \
  "v_cvt_pk_bf16_f32 v169, v162, v163\n\t" \
  "v_cvt_pk_bf16_f32 v170, v164, v165\n\t" \
  "v_cvt_pk_bf16_f32 v171, v166, v167\n\t" \
  "buffer_store_dwordx4 v[168:171], v172, %[rt], s90 offen sc1\n\t" \
  "s_add_u32 s86, s86, 1\n\t" \
  "s_add_u32 s87, s87, 1\n\t" \
  "s_waitcnt lgkmcnt(0)\n\t" \
  "s_cmp_lt_u32 s86, 64\n\t" \
  "s_cbranch_scc1 .Lp1%=\n\t" \
  "s_barrier\n\t" \
  "s_setprio 2\n\t" \
  "v_mfma_f32_16x16x32_bf16 v[144:147], v[64:67], v[176:179], v[144:147]\n\t" \
  "v_mfma_f32_16x16x32_bf16 v[148:151], v[68:71], v[176:179], v[148:151]\n\t" \
  "v_mfma_f32_16x16x32_bf16 v[152:155], v[64:67], v[180:183], v[152:155]\n\t" \
  "v_mfma_f32_16x16x32_bf16 v[156:159], v[68:71], v[180:183], v[156:159]\n\t" \
  "v_mfma_f32_16x16x32_bf16 v[144:147], v[72:75], v[184:187], v[144:147]\n\t" \
  "v_mfma_f32_16x16x32_bf16 v[148:151], v[76:79], v[184:187], v[148:151]\n\t" \
  "v_mfma_f32_16x16x32_bf16 v[152:155], v[72:75], v[188:191], v[152:155]\n\t" \
  "v_mfma_f32_16x16x32_bf16 v[156:159], v[76:79], v[188:191], v[156:159]\n\t" \
  "v_mfma_f32_16x16x32_bf16 v[144:147], v[80:83], v[192:195], v[144:147]\n\t" \
  "v_mfma_f32_16x16x32_bf16 v[148:151], v[84:87], v[192:195], v[148:151]\n\t" \
  "v_mfma_f32_16x16x32_bf16 v[152:155], v[80:83], v[196:199], v[152:155]\n\t" \
  "v_mfma_f32_16x16x32_bf16 v[156:159], v[84:87], v[196:199], v[156:159]\n\t" \
  "v_mfma_f32_16x16x32_bf16 v[144:147], v[88:91], v[200:203], v[144:147]\n\t" \
  "v_mfma_f32_16x16x32_bf16 v[148:151], v[92:95], v[200:203], v[148:151]\n\t" \
  "v_mfma_f32_16x16x32_bf16 v[152:155], v[88:91], v[204:207], v[152:155]\n\t" \
  "v_mfma_f32_16x16x32_bf16 v[156:159], v[92:95], v[204:207], v[156:159]\n\t" \
  "v_mfma_f32_16x16x32_bf16 v[144:147], v[96:99], v[208:211], v[144:147]\n\t" \
  "v_mfma_f32_16x16x32_bf16 v[148:151], v[100:103], v[208:211], v[148:151]\n\t" \
  "v_mfma_f32_16x16x32_bf16 v[152:155], v[96:99], v[212:215], v[152:155]\n\t" \
  "v_mfma_f32_16x16x32_bf16 v[156:159], v[100:103], v[212:215], v[156:159]\n\t" \
  "v_mfma_f32_16x16x32_bf16 v[144:147], v[104:107], v[216:219], v[144:147]\n\t" \
  "v_mfma_f32_16x16x32_bf16 v[148:151], v[108:111], v[216:219], v[148:151]\n\t" \
  "v_mfma_f32_16x16x32_bf16 v[152:155], v[104:107], v[220:223], v[152:155]\n\t" \
  "v_mfma_f32_16x16x32_bf16 v[156:159], v[108:111], v[220:223], v[156:159]\n\t" \
  "v_mfma_f32_16x16x32_bf16 v[144:147], v[112:115], v[224:227], v[144:147]\n\t" \
  "v_mfma_f32_16x16x32_bf16 v[148:151], v[116:119], v[224:227], v[148:151]\n\t" \
  "v_mfma_f32_16x16x32_bf16 v[152:155], v[112:115], v[228:231], v[152:155]\n\t" \
  "v_mfma_f32_16x16x32_bf16 v[156:159], v[116:119], v[228:231], v[156:159]\n\t" \
  "v_mfma_f32_16x16x32_bf16 v[144:147], v[120:123], v[232:235], v[144:147]\n\t" \
  "v_mfma_f32_16x16x32_bf16 v[148:151], v[124:127], v[232:235], v[148:151]\n\t" \
  "v_mfma_f32_16x16x32_bf16 v[152:155], v[120:123], v[236:239], v[152:155]\n\t" \
  "v_mfma_f32_16x16x32_bf16 v[156:159], v[124:127], v[236:239], v[156:159]\n\t" \
  "s_setprio 0\n\t" \
  "s_nop 5\n\t" \
  "ds_write_b128 v252, v[152:155] offset:0\n\t" \
  "ds_write_b128 v252, v[156:159] offset:1024\n\t" \
  "s_waitcnt lgkmcnt(0)\n\t" \
  "s_barrier\n\t" \
  "ds_read_b128 v[160:163], v253 offset:0\n\t" \
  "ds_read_b128 v[164:167], v253 offset:1024\n\t" \
  "s_waitcnt lgkmcnt(0)\n\t" \
  "s_lshr_b32 s90, s87, 7\n\t" \
  "s_xor_b32 s90, s90, %[s0]\n\t" \
  "s_and_b32 s90, s90, 1\n\t" \
  "s_lshl1_add_u32 s90, s87, s90\n\t" \
  "s_lshl_b32 s90, s90, 13\n\t" \
  "v_add_f32 v160, v160, v144\n\t" \
  "v_add_f32 v161, v161, v145\n\t" \
  "v_add_f32 v162, v162, v146\n\t" \
  "v_add_f32 v163, v163, v147\n\t" \
  "v_add_f32 v164, v164, v148\n\t" \
  "v_add_f32 v165, v165, v149\n\t" \
  "v_add_f32 v166, v166, v150\n\t" \
  "v_add_f32 v167, v167, v151\n\t" \
  "v_cvt_pk_bf16_f32 v168, v160, v161\n\t" \
  "v_cvt_pk_bf16_f32 v169, v162, v163\n\t" \
  "v_cvt_pk_bf16_f32 v170, v164, v165\n\t" \
  "v_cvt_pk_bf16_f32 v171, v166, v167\n\t" \
  "buffer_store_dwordx4 v[168:171], v172, %[rt], s90 offen sc1\n\t" \
  "s_waitcnt vmcnt(0)\n\t" \
  "s_barrier\n\t" \
  "s_sub_i32 s91, s86, 1\n\t" \
  "v_mov_b32 v173, s91\n\t" \
  "buffer_store_dword v173, v174, %[rs], 0 offen sc1\n\t" \
  "s_mov_b32 m0, s94\n\t" \
  ".Le%=:\n\t"
#define PP_ASM_1 \
  "s_cmp_lg_u32 %[kh], 0\n\t" \
  "s_cbranch_scc1 .Lk%=\n\t" \
  "s_mov_b32 s94, m0\n\t" \
  "s_mov_b64 s[92:93], %[x0]\n\t" \
  "s_mov_b32 s86, %[g0]\n\t" \
  "s_add_u32 s87, s86, %[t0]\n\t" \
  "s_add_u32 m0, %[lw], 67584\n\t" \
  "s_nop 0\n\t" \
  "global_load_lds_dwordx4 v250, s[92:93] sc1\n\t" \
  "s_add_u32 m0, %[lw], 68608\n\t" \
  "s_nop 0\n\t" \
  "global_load_lds_dwordx4 v251, s[92:93] sc1\n\t" \
  "s_barrier\n\t" \
  "s_branch .Ls0%=\n\t" \
  ".Lp0%=:\n\t" \
  "s_waitcnt vmcnt(9)\n\t" \
  "s_barrier\n\t" \
  ".Ls0%=:\n\t" \
  "s_add_u32 s92, s92, 512\n\t" \
  "s_addc_u32 s93, s93, 0\n\t" \
  "ds_read_b128 v[176:179], v240 offset:0\n\t" \
  "ds_read_b128 v[180:183], v240 offset:16384\n\t" \
  "ds_read_b128 v[184:187], v241 offset:0\n\t" \
  "ds_read_b128 v[188:191], v241 offset:16384\n\t" \
  "s_add_u32 m0, %[lw], 98304\n\t" \
  "s_nop 0\n\t" \
  "global_load_lds_dwordx4 v248, s[92:93] sc1\n\t" \
  "ds_read_b128 v[192:195], v242 offset:0\n\t" \
  "ds_read_b128 v[196:199], v242 offset:16384\n\t" \
  "ds_read_b128 v[200:203], v243 offset:0\n\t" \
  "ds_read_b128 v[204:207], v243 offset:16384\n\t" \
  "s_add_u32 m0, %[lw], 99328\n\t" \
  "s_nop 0\n\t" \
  "global_load_lds_dwordx4 v249, s[92:93] sc1\n\t" \
  "ds_read_b128 v[208:211], v240 offset:256\n\t" \
  "ds_read_b128 v[212:215], v240 offset:16640\n\t" \
  "ds_read_b128 v[216:219], v241 offset:256\n\t" \
  "ds_read_b128 v[220:223], v241 offset:16640\n\t" \
  "s_add_u32 m0, %[lw], 100352\n\t" \
  "s_nop 0\n\t" \
  "global_load_lds_dwordx4 v250, s[92:93] sc1\n\t" \
  "ds_read_b128 v[224:227], v242 offset:256\n\t" \
  "ds_read_b128 v[228:231], v242 offset:16640\n\t" \
  "ds_read_b128 v[232:235], v243 offset:256\n\t" \
  "ds_read_b128 v[236:239], v243 offset:16640\n\t" \
  "s_add_u32 m0, %[lw], 101376\n\t" \
  "s_nop 0\n\t" \
  "global_load_lds_dwordx4 v251, s[92:93] sc1\n\t" \
  "ds_write_b128 v252, v[152:155] offset:0\n\t" \
  "ds_write_b128 v252, v[156:159] offset:1024\n\t" \
  "s_waitcnt lgkmcnt(0)\n\t" \
  "s_setprio 1\n\t" \
  "v_mfma_f32_16x16x32_bf16 v[128:131], v[176:179], v[0:3], 0\n\t" \
  "v_mfma_f32_16x16x32_bf16 v[136:139], v[176:179], v[4:7], 0\n\t" \
  "v_mfma_f32_16x16x32_bf16 v[132:135], v[180:183], v[0:3], 0\n\t" \
  "v_mfma_f32_16x16x32_bf16 v[140:143], v[180:183], v[4:7], 0\n\t" \
  "v_mfma_f32_16x16x32_bf16 v[128:131], v[184:187], v[8:11], v[128:131]\n\t" \
  "v_mfma_f32_16x16x32_bf16 v[136:139], v[184:187], v[12:15], v[136:139]\n\t" \
  "v_mfma_f32_16x16x32_bf16 v[132:135], v[188:191], v[8:11], v[132:135]\n\t" \
  "v_mfma_f32_16x16x32_bf16 v[140:143], v[188:191], v[12:15], v[140:143]\n\t" \
  "v_mfma_f32_16x16x32_bf16 v[128:131], v[192:195], v[16:19], v[128:131]\n\t" \
  "v_mfma_f32_16x16x32_bf16 v[136:139], v[192:195], v[20:23], v[136:139]\n\t" \
  "v_mfma_f32_16x16x32_bf16 v[132:135], v[196:199], v[16:19], v[132:135]\n\t" \
  "v_mfma_f32_16x16x32_bf16 v[140:143], v[196:199], v[20:23], v[140:143]\n\t" \
  "v_mfma_f32_16x16x32_bf16 v[128:131], v[200:203], v[24:27], v[128:131]\n\t" \
  "v_mfma_f32_16x16x32_bf16 v[136:139], v[200:203], v[28:31], v[136:139]\n\t" \
  "v_mfma_f32_16x16x32_bf16 v[132:135], v[204:207], v[24:27], v[132:135]\n\t" \
  "v_mfma_f32_16x16x32_bf16 v[140:143], v[204:207], v[28:31], v[140:143]\n\t" \
  "v_mfma_f32_16x16x32_bf16 v[128:131], v[208:211], v[32:35], v[128:131]\n\t" \
  "v_mfma_f32_16x16x32_bf16 v[136:139], v[208:211], v[36:39], v[136:139]\n\t" \
  "v_mfma_f32_16x16x32_bf16 v[132:135], v[212:215], v[32:35], v[132:135]\n\t" \
  "v_mfma_f32_16x16x32_bf16 v[140:143], v[212:215], v[36:39], v[140:143]\n\t" \
  "v_mfma_f32_16x16x32_bf16 v[128:131], v[216:219], v[40:43], v[128:131]\n\t" \
  "v_mfma_f32_16x16x32_bf16 v[136:139], v[216:219], v[44:47], v[136:139]\n\t" \
  "v_mfma_f32_16x16x32_bf16 v[132:135], v[220:223], v[40:43], v[132:135]\n\t" \
  "v_mfma_f32_16x16x32_bf16 v[140:143], v[220:223], v[44:47], v[140:143]\n\t" \
  "v_mfma_f32_16x16x32_bf16 v[128:131], v[224:227], v[48:51], v[128:131]\n\t" \
  "v_mfma_f32_16x16x32_bf16 v[136:139], v[224:227], v[52:55], v[136:139]\n\t" \
  "v_mfma_f32_16x16x32_bf16 v[132:135], v[228:231], v[48:51], v[132:135]\n\t" \
  "v_mfma_f32_16x16x32_bf16 v[140:143], v[228:231], v[52:55], v[140:143]\n\t" \
  "v_mfma_f32_16x16x32_bf16 v[128:131], v[232:235], v[56:59], v[128:131]\n\t" \
  "v_mfma_f32_16x16x32_bf16 v[136:139], v[232:235], v[60:63], v[136:139]\n\t" \
  "v_mfma_f32_16x16x32_bf16 v[132:135], v[236:239], v[56:59], v[132:135]\n\t" \
  "v_mfma_f32_16x16x32_bf16 v[140:143], v[236:239], v[60:63], v[140:143]\n\t" \
  "s_setprio 0\n\t" \
  "s_waitcnt vmcnt(4)\n\t" \
  "s_barrier\n\t" \
  "s_add_u32 s92, s92, 65024\n\t" \
  "s_addc_u32 s93, s93, 0\n\t" \
  "ds_read_b128 v[160:163], v253 offset:0\n\t" \
  "ds_read_b128 v[164:167], v253 offset:1024\n\t" \
  "ds_read_b128 v[176:179], v240 offset:32768\n\t" \
  "ds_read_b128 v[180:183], v240 offset:49152\n\t" \
  "ds_read_b128 v[184:187], v241 offset:32768\n\t" \
  "ds_read_b128 v[188:191], v241 offset:49152\n\t" \
  "s_add_u32 m0, %[lw], 0\n\t" \
  "s_nop 0\n\t" \
  "global_load_lds_dwordx4 v248, s[92:93] sc1\n\t" \
  "ds_read_b128 v[192:195], v242 offset:32768\n\t" \
  "ds_read_b128 v[196:199], v242 offset:49152\n\t" \
  "ds_read_b128 v[200:203], v243 offset:32768\n\t" \
  "ds_read_b128 v[204:207], v243 offset:49152\n\t" \
  "s_add_u32 m0, %[lw], 1024\n\t" \
  "s_nop 0\n\t" \
  "global_load_lds_dwordx4 v249, s[92:93] sc1\n\t" \
  "ds_read_b128 v[208:211], v240 offset:33024\n\t" \
  "ds_read_b128 v[212:215], v240 offset:49408\n\t" \
  "ds_read_b128 v[216:219], v241 offset:33024\n\t" \
  "ds_read_b128 v[220:223], v241 offset:49408\n\t" \
  "s_add_u32 m0, %[lw], 2048\n\t" \
  "s_nop 0\n\t" \
  "global_load_lds_dwordx4 v250, s[92:93] sc1\n\t" \
  "ds_read_b128 v[224:227], v242 offset:33024\n\t" \
  "ds_read_b128 v[228:231], v242 offset:49408\n\t" \
  "ds_read_b128 v[232:235], v243 offset:33024\n\t" \
  "ds_read_b128 v[236:239], v243 offset:49408\n\t" \
  "s_add_u32 m0, %[lw], 3072\n\t" \
  "s_nop 0\n\t" \
  "global_load_lds_dwordx4 v251, s[92:93] sc1\n\t" \
  "s_waitcnt lgkmcnt(15)\n\t" \
  "s_waitcnt lgkmcnt(12)\n\t" \
  "s_lshr_b32 s90, s87, 7\n\t" \
  "s_xor_b32 s90, s90, %[s0]\n\t" \
  "s_and_b32 s90, s90, 1\n\t" \
  "s_lshl1_add_u32 s90, s87, s90\n\t" \
  "s_lshl_b32 s90, s90, 13\n\t" \
  "v_add_f32 v160, v160, v144\n\t" \
  "v_add_f32 v161, v161, v145\n\t" \
  "v_add_f32 v162, v162, v146\n\t" \
  "v_add_f32 v163, v163, v147\n\t" \
  "v_add_f32 v164, v164, v148\n\t" \
  "v_add_f32 v165, v165, v149\n\t" \
  "v_add_f32 v166, v166, v150\n\t" \
  "v_add_f32 v167, v167, v151\n\t" \
  "v_cvt_pk_bf16_f32 v168, v160, v161\n\t" \
  "v_cvt_pk_bf16_f32 v169, v162, v163\n\t" \
  "v_cvt_pk_bf16_f32 v170, v164, v165\n\t" \
  "v_cvt_pk_bf16_f32 v171, v166, v167\n\t" \
  "buffer_store_dwordx4 v[168:171], v172, %[rt], s90 offen sc1\n\t" \
  "s_add_u32 s86, s86, 1\n\t" \
  "s_add_u32 s87, s87, 1\n\t" \
  "s_waitcnt lgkmcnt(0)\n\t" \
  "s_setprio 1\n\t" \
  "v_mfma_f32_16x16x32_bf16 v[128:131], v[176:179], v[64:67], v[128:131]\n\t" \
  "v_mfma_f32_16x16x32_bf16 v[136:139], v[176:179], v[68:71], v[136:139]\n\t" \
  "v_mfma_f32_16x16x32_bf16 v[132:135], v[180:183], v[64:67], v[132:135]\n\t" \
  "v_mfma_f32_16x16x32_bf16 v[140:143], v[180:183], v[68:71], v[140:143]\n\t" \
  "v_mfma_f32_16x16x32_bf16 v[128:131], v[184:187], v[72:75], v[128:131]\n\t" \
  "v_mfma_f32_16x16x32_bf16 v[136:139], v[184:187], v[76:79], v[136:139]\n\t" \
  "v_mfma_f32_16x16x32_bf16 v[132:135], v[188:191], v[72:75], v[132:135]\n\t" \
  "v_mfma_f32_16x16x32_bf16 v[140:143], v[188:191], v[76:79], v[140:143]\n\t" \
  "v_mfma_f32_16x16x32_bf16 v[128:131], v[192:195], v[80:83], v[128:131]\n\t" \
  "v_mfma_f32_16x16x32_bf16 v[136:139], v[192:195], v[84:87], v[136:139]\n\t" \
  "v_mfma_f32_16x16x32_bf16 v[132:135], v[196:199], v[80:83], v[132:135]\n\t" \
  "v_mfma_f32_16x16x32_bf16 v[140:143], v[196:199], v[84:87], v[140:143]\n\t" \
  "v_mfma_f32_16x16x32_bf16 v[128:131], v[200:203], v[88:91], v[128:131]\n\t" \
  "v_mfma_f32_16x16x32_bf16 v[136:139], v[200:203], v[92:95], v[136:139]\n\t" \
  "v_mfma_f32_16x16x32_bf16 v[132:135], v[204:207], v[88:91], v[132:135]\n\t" \
  "v_mfma_f32_16x16x32_bf16 v[140:143], v[204:207], v[92:95], v[140:143]\n\t" \
  "v_mfma_f32_16x16x32_bf16 v[128:131], v[208:211], v[96:99], v[128:131]\n\t" \
  "v_mfma_f32_16x16x32_bf16 v[136:139], v[208:211], v[100:103], v[136:139]\n\t" \
  "v_mfma_f32_16x16x32_bf16 v[132:135], v[212:215], v[96:99], v[132:135]\n\t" \
  "v_mfma_f32_16x16x32_bf16 v[140:143], v[212:215], v[100:103], v[140:143]\n\t" \
  "v_mfma_f32_16x16x32_bf16 v[128:131], v[216:219], v[104:107], v[128:131]\n\t" \
  "v_mfma_f32_16x16x32_bf16 v[136:139], v[216:219], v[108:111], v[136:139]\n\t" \
  "v_mfma_f32_16x16x32_bf16 v[132:135], v[220:223], v[104:107], v[132:135]\n\t" \
  "v_mfma_f32_16x16x32_bf16 v[140:143], v[220:223], v[108:111], v[140:143]\n\t" \
  "v_mfma_f32_16x16x32_bf16 v[128:131], v[224:227], v[112:115], v[128:131]\n\t" \
  "v_mfma_f32_16x16x32_bf16 v[136:139], v[224:227], v[116:119], v[136:139]\n\t" \
  "v_mfma_f32_16x16x32_bf16 v[132:135], v[228:231], v[112:115], v[132:135]\n\t" \
  "v_mfma_f32_16x16x32_bf16 v[140:143], v[228:231], v[116:119], v[140:143]\n\t" \
  "v_mfma_f32_16x16x32_bf16 v[128:131], v[232:235], v[120:123], v[128:131]\n\t" \
  "v_mfma_f32_16x16x32_bf16 v[136:139], v[232:235], v[124:127], v[136:139]\n\t" \
  "v_mfma_f32_16x16x32_bf16 v[132:135], v[236:239], v[120:123], v[132:135]\n\t" \
  "v_mfma_f32_16x16x32_bf16 v[140:143], v[236:239], v[124:127], v[140:143]\n\t" \
  "s_setprio 0\n\t" \
  "s_waitcnt vmcnt(9)\n\t" \
  "s_barrier\n\t" \
  "s_add_u32 s92, s92, 512\n\t" \
  "s_addc_u32 s93, s93, 0\n\t" \
  "ds_read_b128 v[176:179], v244 offset:0\n\t" \
  "ds_read_b128 v[180:183], v244 offset:16384\n\t" \
  "ds_read_b128 v[184:187], v245 offset:0\n\t" \
  "ds_read_b128 v[188:191], v245 offset:16384\n\t" \
  "s_add_u32 m0, %[lw], 32768\n\t" \
  "s_nop 0\n\t" \
  "global_load_lds_dwordx4 v248, s[92:93] sc1\n\t" \
  "ds_read_b128 v[192:195], v246 offset:0\n\t" \
  "ds_read_b128 v[196:199], v246 offset:16384\n\t" \
  "ds_read_b128 v[200:203], v247 offset:0\n\t" \
  "ds_read_b128 v[204:207], v247 offset:16384\n\t" \
  "s_add_u32 m0, %[lw], 33792\n\t" \
  "s_nop 0\n\t" \
  "global_load_lds_dwordx4 v249, s[92:93] sc1\n\t" \
  "ds_read_b128 v[208:211], v244 offset:256\n\t" \
  "ds_read_b128 v[212:215], v244 offset:16640\n\t" \
  "ds_read_b128 v[216:219], v245 offset:256\n\t" \
  "ds_read_b128 v[220:223], v245 offset:16640\n\t" \
  "s_add_u32 m0, %[lw], 34816\n\t" \
  "s_nop 0\n\t" \
  "global_load_lds_dwordx4 v250, s[92:93] sc1\n\t" \
  "ds_read_b128 v[224:227], v246 offset:256\n\t" \
  "ds_read_b128 v[228:231], v246 offset:16640\n\t" \
  "ds_read_b128 v[232:235], v247 offset:256\n\t" \
  "ds_read_b128 v[236:239], v247 offset:16640\n\t" \
  "s_add_u32 m0, %[lw], 35840\n\t" \
  "s_nop 0\n\t" \
  "global_load_lds_dwordx4 v251, s[92:93] sc1\n\t" \
  "ds_write_b128 v252, v[136:139] offset:0\n\t" \
  "ds_write_b128 v252, v[140:143] offset:1024\n\t" \
  "s_waitcnt lgkmcnt(0)\n\t" \
  "s_setprio 1\n\t" \
  "v_mfma_f32_16x16x32_bf16 v[144:147], v[176:179], v[0:3], 0\n\t" \
  "v_mfma_f32_16x16x32_bf16 v[152:155], v[176:179], v[4:7], 0\n\t" \
  "v_mfma_f32_16x16x32_bf16 v[148:151], v[180:183], v[0:3], 0\n\t" \
  "v_mfma_f32_16x16x32_bf16 v[156:159], v[180:183], v[4:7], 0\n\t" \
  "v_mfma_f32_16x16x32_bf16 v[144:147], v[184:187], v[8:11], v[144:147]\n\t" \
  "v_mfma_f32_16x16x32_bf16 v[152:155], v[184:187], v[12:15], v[152:155]\n\t" \
  "v_mfma_f32_16x16x32_bf16 v[148:151], v[188:191], v[8:11], v[148:151]\n\t" \
  "v_mfma_f32_16x16x32_bf16 v[156:159], v[188:191], v[12:15], v[156:159]\n\t" \
  "v_mfma_f32_16x16x32_bf16 v[144:147], v[192:195], v[16:19], v[144:147]\n\t" \
  "v_mfma_f32_16x16x32_bf16 v[152:155], v[192:195], v[20:23], v[152:155]\n\t" \
  "v_mfma_f32_16x16x32_bf16 v[148:151], v[196:199], v[16:19], v[148:151]\n\t" \
  "v_mfma_f32_16x16x32_bf16 v[156:159], v[196:199], v[20:23], v[156:159]\n\t" \
  "v_mfma_f32_16x16x32_bf16 v[144:147], v[200:203], v[24:27], v[144:147]\n\t" \
  "v_mfma_f32_16x16x32_bf16 v[152:155], v[200:203], v[28:31], v[152:155]\n\t" \
  "v_mfma_f32_16x16x32_bf16 v[148:151], v[204:207], v[24:27], v[148:151]\n\t" \
  "v_mfma_f32_16x16x32_bf16 v[156:159], v[204:207], v[28:31], v[156:159]\n\t" \
  "v_mfma_f32_16x16x32_bf16 v[144:147], v[208:211], v[32:35], v[144:147]\n\t" \
  "v_mfma_f32_16x16x32_bf16 v[152:155], v[208:211], v[36:39], v[152:155]\n\t" \
  "v_mfma_f32_16x16x32_bf16 v[148:151], v[212:215], v[32:35], v[148:151]\n\t" \
  "v_mfma_f32_16x16x32_bf16 v[156:159], v[212:215], v[36:39], v[156:159]\n\t" \
  "v_mfma_f32_16x16x32_bf16 v[144:147], v[216:219], v[40:43], v[144:147]\n\t" \
  "v_mfma_f32_16x16x32_bf16 v[152:155], v[216:219], v[44:47], v[152:155]\n\t" \
  "v_mfma_f32_16x16x32_bf16 v[148:151], v[220:223], v[40:43], v[148:151]\n\t" \
  "v_mfma_f32_16x16x32_bf16 v[156:159], v[220:223], v[44:47], v[156:159]\n\t" \
  "v_mfma_f32_16x16x32_bf16 v[144:147], v[224:227], v[48:51], v[144:147]\n\t" \
  "v_mfma_f32_16x16x32_bf16 v[152:155], v[224:227], v[52:55], v[152:155]\n\t" \
  "v_mfma_f32_16x16x32_bf16 v[148:151], v[228:231], v[48:51], v[148:151]\n\t" \
  "v_mfma_f32_16x16x32_bf16 v[156:159], v[228:231], v[52:55], v[156:159]\n\t" \
  "v_mfma_f32_16x16x32_bf16 v[144:147], v[232:235], v[56:59], v[144:147]\n\t" \
  "v_mfma_f32_16x16x32_bf16 v[152:155], v[232:235], v[60:63], v[152:155]\n\t" \
  "v_mfma_f32_16x16x32_bf16 v[148:151], v[236:239], v[56:59], v[148:151]\n\t" \
  "v_mfma_f32_16x16x32_bf16 v[156:159], v[236:239], v[60:63], v[156:159]\n\t" \
  "s_setprio 0\n\t" \
  "s_waitcnt vmcnt(4)\n\t" \
  "s_barrier\n\t" \
  "s_add_u32 s92, s92, 65024\n\t" \
  "s_addc_u32 s93, s93, 0\n\t" \
  "ds_read_b128 v[160:163], v253 offset:0\n\t" \
  "ds_read_b128 v[164:167], v253 offset:1024\n\t" \
  "ds_read_b128 v[176:179], v244 offset:32768\n\t" \
  "ds_read_b128 v[180:183], v244 offset:49152\n\t" \
  "ds_read_b128 v[184:187], v245 offset:32768\n\t" \
  "ds_read_b128 v[188:191], v245 offset:49152\n\t" \
  "s_add_u32 m0, %[lw], 65536\n\t" \
  "s_nop 0\n\t" \
  "global_load_lds_dwordx4 v248, s[92:93] sc1\n\t" \
  "ds_read_b128 v[192:195], v246 offset:32768\n\t" \
  "ds_read_b128 v[196:199], v246 offset:49152\n\t" \
  "ds_read_b128 v[200:203], v247 offset:32768\n\t" \
  "ds_read_b128 v[204:207], v247 offset:49152\n\t" \
  "s_add_u32 m0, %[lw], 66560\n\t" \
  "s_nop 0\n\t" \
  "global_load_lds_dwordx4 v249, s[92:93] sc1\n\t" \
  "ds_read_b128 v[208:211], v244 offset:33024\n\t" \
  "ds_read_b128 v[212:215], v244 offset:49408\n\t" \
  "ds_read_b128 v[216:219], v245 offset:33024\n\t" \
  "ds_read_b128 v[220:223], v245 offset:49408\n\t" \
  "s_add_u32 m0, %[lw], 67584\n\t" \
  "s_nop 0\n\t" \
  "global_load_lds_dwordx4 v250, s[92:93] sc1\n\t" \
  "ds_read_b128 v[224:227], v246 offset:33024\n\t" \
  "ds_read_b128 v[228:231], v246 offset:49408\n\t" \
  "ds_read_b128 v[232:235], v247 offset:33024\n\t" \
  "ds_read_b128 v[236:239], v247 offset:49408\n\t" \
  "s_add_u32 m0, %[lw], 68608\n\t" \
  "s_nop 0\n\t" \
  "global_load_lds_dwordx4 v251, s[92:93] sc1\n\t" \
  "s_waitcnt lgkmcnt(15)\n\t" \
  "s_waitcnt lgkmcnt(12)\n\t" \
  "s_lshr_b32 s90, s87, 7\n\t" \
  "s_xor_b32 s90, s90, %[s0]\n\t" \
  "s_and_b32 s90, s90, 1\n\t" \
  "s_lshl1_add_u32 s90, s87, s90\n\t" \
  "s_lshl_b32 s90, s90, 13\n\t" \
  "v_add_f32 v160, v160, v128\n\t" \
  "v_add_f32 v161, v161, v129\n\t" \
  "v_add_f32 v162, v162, v130\n\t" \
  "v_add_f32 v163, v163, v131\n\t" \
  "v_add_f32 v164, v164, v132\n\t" \
  "v_add_f32 v165, v165, v133\n\t" \
  "v_add_f32 v166, v166, v134\n\t" \
  "v_add_f32 v167, v167, v135\n\t" \
  "v_cvt_pk_bf16_f32 v168, v160, v161\n\t" \
  "v_cvt_pk_bf16_f32 v169, v162, v163\n\t" \
  "v_cvt_pk_bf16_f32 v170, v164, v165\n\t" \
  "v_cvt_pk_bf16_f32 v171, v166, v167\n\t" \
  "buffer_store_dwordx4 v[168:171], v172, %[rt], s90 offen sc1\n\t" \
  "s_add_u32 s86, s86, 1\n\t" \
  "s_add_u32 s87, s87, 1\n\t" \
  "s_waitcnt lgkmcnt(0)\n\t" \
  "s_setprio 1\n\t" \
  "v_mfma_f32_16x16x32_bf16 v[144:147], v[176:179], v[64:67], v[144:147]\n\t" \
  "v_mfma_f32_16x16x32_bf16 v[152:155], v[176:179], v[68:71], v[152:155]\n\t" \
  "v_mfma_f32_16x16x32_bf16 v[148:151], v[180:183], v[64:67], v[148:151]\n\t" \
  "v_mfma_f32_16x16x32_bf16 v[156:159], v[180:183], v[68:71], v[156:159]\n\t" \
  "v_mfma_f32_16x16x32_bf16 v[144:147], v[184:187], v[72:75], v[144:147]\n\t" \
  "v_mfma_f32_16x16x32_bf16 v[152:155], v[184:187], v[76:79], v[152:155]\n\t" \
  "v_mfma_f32_16x16x32_bf16 v[148:151], v[188:191], v[72:75], v[148:151]\n\t" \
  "v_mfma_f32_16x16x32_bf16 v[156:159], v[188:191], v[76:79], v[156:159]\n\t" \
  "v_mfma_f32_16x16x32_bf16 v[144:147], v[192:195], v[80:83], v[144:147]\n\t" \
  "v_mfma_f32_16x16x32_bf16 v[152:155], v[192:195], v[84:87], v[152:155]\n\t" \
  "v_mfma_f32_16x16x32_bf16 v[148:151], v[196:199], v[80:83], v[148:151]\n\t" \
  "v_mfma_f32_16x16x32_bf16 v[156:159], v[196:199], v[84:87], v[156:159]\n\t" \
  "v_mfma_f32_16x16x32_bf16 v[144:147], v[200:203], v[88:91], v[144:147]\n\t" \
  "v_mfma_f32_16x16x32_bf16 v[152:155], v[200:203], v[92:95], v[152:155]\n\t" \
  "v_mfma_f32_16x16x32_bf16 v[148:151], v[204:207], v[88:91], v[148:151]\n\t" \
  "v_mfma_f32_16x16x32_bf16 v[156:159], v[204:207], v[92:95], v[156:159]\n\t" \
  "v_mfma_f32_16x16x32_bf16 v[144:147], v[208:211], v[96:99], v[144:147]\n\t" \
  "v_mfma_f32_16x16x32_bf16 v[152:155], v[208:211], v[100:103], v[152:155]\n\t" \
  "v_mfma_f32_16x16x32_bf16 v[148:151], v[212:215], v[96:99], v[148:151]\n\t" \
  "v_mfma_f32_16x16x32_bf16 v[156:159], v[212:215], v[100:103], v[156:159]\n\t" \
  "v_mfma_f32_16x16x32_bf16 v[144:147], v[216:219], v[104:107], v[144:147]\n\t" \
  "v_mfma_f32_16x16x32_bf16 v[152:155], v[216:219], v[108:111], v[152:155]\n\t" \
  "v_mfma_f32_16x16x32_bf16 v[148:151], v[220:223], v[104:107], v[148:151]\n\t" \
  "v_mfma_f32_16x16x32_bf16 v[156:159], v[220:223], v[108:111], v[156:159]\n\t" \
  "v_mfma_f32_16x16x32_bf16 v[144:147], v[224:227], v[112:115], v[144:147]\n\t" \
  "v_mfma_f32_16x16x32_bf16 v[152:155], v[224:227], v[116:119], v[152:155]\n\t" \
  "v_mfma_f32_16x16x32_bf16 v[148:151], v[228:231], v[112:115], v[148:151]\n\t" \
  "v_mfma_f32_16x16x32_bf16 v[156:159], v[228:231], v[116:119], v[156:159]\n\t" \
  "v_mfma_f32_16x16x32_bf16 v[144:147], v[232:235], v[120:123], v[144:147]\n\t" \
  "v_mfma_f32_16x16x32_bf16 v[152:155], v[232:235], v[124:127], v[152:155]\n\t" \
  "v_mfma_f32_16x16x32_bf16 v[148:151], v[236:239], v[120:123], v[148:151]\n\t" \
  "v_mfma_f32_16x16x32_bf16 v[156:159], v[236:239], v[124:127], v[156:159]\n\t" \
  "s_setprio 0\n\t" \
  "s_cmp_lt_u32 s86, 64\n\t" \
  "s_cbranch_scc1 .Lp0%=\n\t" \
  "s_barrier\n\t" \
  "s_nop 1\n\t" \
  "ds_write_b128 v252, v[152:155] offset:0\n\t" \
  "s_nop 0\n\t" \
  "ds_write_b128 v252, v[156:159] offset:1024\n\t" \
  "s_waitcnt lgkmcnt(0)\n\t" \
  "s_barrier\n\t" \
  "ds_read_b128 v[160:163], v253 offset:0\n\t" \
  "ds_read_b128 v[164:167], v253 offset:1024\n\t" \
  "s_waitcnt lgkmcnt(0)\n\t" \
  "s_lshr_b32 s90, s87, 7\n\t" \
  "s_xor_b32 s90, s90, %[s0]\n\t" \
  "s_and_b32 s90, s90, 1\n\t" \
  "s_lshl1_add_u32 s90, s87, s90\n\t" \
  "s_lshl_b32 s90, s90, 13\n\t" \
  "v_add_f32 v160, v160, v144\n\t" \
  "v_add_f32 v161, v161, v145\n\t" \
  "v_add_f32 v162, v162, v146\n\t" \
  "v_add_f32 v163, v163, v147\n\t" \
  "v_add_f32 v164, v164, v148\n\t" \
  "v_add_f32 v165, v165, v149\n\t" \
  "v_add_f32 v166, v166, v150\n\t" \
  "v_add_f32 v167, v167, v151\n\t" \
  "v_cvt_pk_bf16_f32 v168, v160, v161\n\t" \
  "v_cvt_pk_bf16_f32 v169, v162, v163\n\t" \
  "v_cvt_pk_bf16_f32 v170, v164, v165\n\t" \
  "v_cvt_pk_bf16_f32 v171, v166, v167\n\t" \
  "buffer_store_dwordx4 v[168:171], v172, %[rt], s90 offen sc1\n\t" \
  "s_waitcnt vmcnt(0)\n\t" \
  "s_barrier\n\t" \
  "s_mov_b32 m0, s94\n\t" \
  "s_branch .Le%=\n\t" \
  ".Lk%=:\n\t" \
  "s_mov_b32 s94, m0\n\t" \
  "s_mov_b64 s[92:93], %[x0]\n\t" \
  "s_mov_b32 s86, %[g0]\n\t" \
  "s_add_u32 s87, s86, %[t0]\n\t" \
  "s_add_u32 m0, %[lw], 67584\n\t" \
  "s_nop 0\n\t" \
  "global_load_lds_dwordx4 v250, s[92:93] sc1\n\t" \
  "s_add_u32 m0, %[lw], 68608\n\t" \
  "s_nop 0\n\t" \
  "global_load_lds_dwordx4 v251, s[92:93] sc1\n\t" \
  "s_barrier\n\t" \
  "s_branch .Ls1%=\n\t" \
  ".Lp1%=:\n\t" \
  "s_waitcnt vmcnt(10)\n\t" \
  "s_barrier\n\t" \
  "s_setprio 2\n\t" \
  "v_mfma_f32_16x16x32_bf16 v[144:147], v[176:179], v[64:67], v[144:147]\n\t" \
  "v_mfma_f32_16x16x32_bf16 v[152:155], v[176:179], v[68:71], v[152:155]\n\t" \
  "v_mfma_f32_16x16x32_bf16 v[148:151], v[180:183], v[64:67], v[148:151]\n\t" \
  "v_mfma_f32_16x16x32_bf16 v[156:159], v[180:183], v[68:71], v[156:159]\n\t" \
  "v_mfma_f32_16x16x32_bf16 v[144:147], v[184:187], v[72:75], v[144:147]\n\t" \
  "v_mfma_f32_16x16x32_bf16 v[152:155], v[184:187], v[76:79], v[152:155]\n\t" \
  "v_mfma_f32_16x16x32_bf16 v[148:151], v[188:191], v[72:75], v[148:151]\n\t" \
  "v_mfma_f32_16x16x32_bf16 v[156:159], v[188:191], v[76:79], v[156:159]\n\t" \
  "v_mfma_f32_16x16x32_bf16 v[144:147], v[192:195], v[80:83], v[144:147]\n\t" \
  "v_mfma_f32_16x16x32_bf16 v[152:155], v[192:195], v[84:87], v[152:155]\n\t" \
  "v_mfma_f32_16x16x32_bf16 v[148:151], v[196:199], v[80:83], v[148:151]\n\t" \
  "v_mfma_f32_16x16x32_bf16 v[156:159], v[196:199], v[84:87], v[156:159]\n\t" \
  "v_mfma_f32_16x16x32_bf16 v[144:147], v[200:203], v[88:91], v[144:147]\n\t" \
  "v_mfma_f32_16x16x32_bf16 v[152:155], v[200:203], v[92:95], v[152:155]\n\t" \
  "v_mfma_f32_16x16x32_bf16 v[148:151], v[204:207], v[88:91], v[148:151]\n\t" \
  "v_mfma_f32_16x16x32_bf16 v[156:159], v[204:207], v[92:95], v[156:159]\n\t" \
  "v_mfma_f32_16x16x32_bf16 v[144:147], v[208:211], v[96:99], v[144:147]\n\t" \
  "v_mfma_f32_16x16x32_bf16 v[152:155], v[208:211], v[100:103], v[152:155]\n\t" \
  "v_mfma_f32_16x16x32_bf16 v[148:151], v[212:215], v[96:99], v[148:151]\n\t" \
  "v_mfma_f32_16x16x32_bf16 v[156:159], v[212:215], v[100:103], v[156:159]\n\t" \
  "v_mfma_f32_16x16x32_bf16 v[144:147], v[216:219], v[104:107], v[144:147]\n\t" \
  "v_mfma_f32_16x16x32_bf16 v[152:155], v[216:219], v[108:111], v[152:155]\n\t" \
  "v_mfma_f32_16x16x32_bf16 v[148:151], v[220:223], v[104:107], v[148:151]\n\t" \
  "v_mfma_f32_16x16x32_bf16 v[156:159], v[220:223], v[108:111], v[156:159]\n\t" \
  "v_mfma_f32_16x16x32_bf16 v[144:147], v[224:227], v[112:115], v[144:147]\n\t" \
  "v_mfma_f32_16x16x32_bf16 v[152:155], v[224:227], v[116:119], v[152:155]\n\t" \
  "v_mfma_f32_16x16x32_bf16 v[148:151], v[228:231], v[112:115], v[148:151]\n\t" \
  "v_mfma_f32_16x16x32_bf16 v[156:159], v[228:231], v[116:119], v[156:159]\n\t" \
  "v_mfma_f32_16x16x32_bf16 v[144:147], v[232:235], v[120:123], v[144:147]\n\t" \
  "v_mfma_f32_16x16x32_bf16 v[152:155], v[232:235], v[124:127], v[152:155]\n\t" \
  "v_mfma_f32_16x16x32_bf16 v[148:151], v[236:239], v[120:123], v[148:151]\n\t" \
  "v_mfma_f32_16x16x32_bf16 v[156:159], v[236:239], v[124:127], v[156:159]\n\t" \
  "s_setprio 0\n\t" \
  ".Ls1%=:\n\t" \
  "s_add_u32 s92, s92, 512\n\t" \
  "s_addc_u32 s93, s93, 0\n\t" \
  "ds_read_b128 v[176:179], v240 offset:0\n\t" \
  "ds_read_b128 v[180:183], v240 offset:16384\n\t" \
  "ds_read_b128 v[184:187], v241 offset:0\n\t" \
  "ds_read_b128 v[188:191], v241 offset:16384\n\t" \
  "s_add_u32 m0, %[lw], 98304\n\t" \
  "s_nop 0\n\t" \
  "global_load_lds_dwordx4 v248, s[92:93] sc1\n\t" \
  "ds_read_b128 v[192:195], v242 offset:0\n\t" \
  "ds_read_b128 v[196:199], v242 offset:16384\n\t" \
  "ds_read_b128 v[200:203], v243 offset:0\n\t" \
  "ds_read_b128 v[204:207], v243 offset:16384\n\t" \
  "s_add_u32 m0, %[lw], 99328\n\t" \
  "s_nop 0\n\t" \
  "global_load_lds_dwordx4 v249, s[92:93] sc1\n\t" \
  "ds_read_b128 v[208:211], v240 offset:256\n\t" \
  "ds_read_b128 v[212:215], v240 offset:16640\n\t" \
  "ds_read_b128 v[216:219], v241 offset:256\n\t" \
  "ds_read_b128 v[220:223], v241 offset:16640\n\t" \
  "s_add_u32 m0, %[lw], 100352\n\t" \
  "s_nop 0\n\t" \
  "global_load_lds_dwordx4 v250, s[92:93] sc1\n\t" \
  "ds_read_b128 v[224:227], v242 offset:256\n\t" \
  "ds_read_b128 v[228:231], v242 offset:16640\n\t" \
  "ds_read_b128 v[232:235], v243 offset:256\n\t" \
  "ds_read_b128 v[236:239], v243 offset:16640\n\t" \
  "s_add_u32 m0, %[lw], 101376\n\t" \
  "s_nop 0\n\t" \
  "global_load_lds_dwordx4 v251, s[92:93] sc1\n\t" \
  "ds_write_b128 v252, v[152:155] offset:0\n\t" \
  "ds_write_b128 v252, v[156:159] offset:1024\n\t" \
  "s_waitcnt lgkmcnt(0)\n\t" \
  "s_waitcnt vmcnt(4)\n\t" \
  "s_barrier\n\t" \
  "s_setprio 2\n\t" \
  "v_mfma_f32_16x16x32_bf16 v[128:131], v[176:179], v[0:3], 0\n\t" \
  "v_mfma_f32_16x16x32_bf16 v[136:139], v[176:179], v[4:7], 0\n\t" \
  "v_mfma_f32_16x16x32_bf16 v[132:135], v[180:183], v[0:3], 0\n\t" \
  "v_mfma_f32_16x16x32_bf16 v[140:143], v[180:183], v[4:7], 0\n\t" \
  "v_mfma_f32_16x16x32_bf16 v[128:131], v[184:187], v[8:11], v[128:131]\n\t" \
  "v_mfma_f32_16x16x32_bf16 v[136:139], v[184:187], v[12:15], v[136:139]\n\t" \
  "v_mfma_f32_16x16x32_bf16 v[132:135], v[188:191], v[8:11], v[132:135]\n\t" \
  "v_mfma_f32_16x16x32_bf16 v[140:143], v[188:191], v[12:15], v[140:143]\n\t" \
  "v_mfma_f32_16x16x32_bf16 v[128:131], v[192:195], v[16:19], v[128:131]\n\t" \
  "v_mfma_f32_16x16x32_bf16 v[136:139], v[192:195], v[20:23], v[136:139]\n\t" \
  "v_mfma_f32_16x16x32_bf16 v[132:135], v[196:199], v[16:19], v[132:135]\n\t" \
  "v_mfma_f32_16x16x32_bf16 v[140:143], v[196:199], v[20:23], v[140:143]\n\t" \
  "v_mfma_f32_16x16x32_bf16 v[128:131], v[200:203], v[24:27], v[128:131]\n\t" \
  "v_mfma_f32_16x16x32_bf16 v[136:139], v[200:203], v[28:31], v[136:139]\n\t" \
  "v_mfma_f32_16x16x32_bf16 v[132:135], v[204:207], v[24:27], v[132:135]\n\t" \
  "v_mfma_f32_16x16x32_bf16 v[140:143], v[204:207], v[28:31], v[140:143]\n\t" \
  "v_mfma_f32_16x16x32_bf16 v[128:131], v[208:211], v[32:35], v[128:131]\n\t" \
  "v_mfma_f32_16x16x32_bf16 v[136:139], v[208:211], v[36:39], v[136:139]\n\t" \
  "v_mfma_f32_16x16x32_bf16 v[132:135], v[212:215], v[32:35], v[132:135]\n\t" \
  "v_mfma_f32_16x16x32_bf16 v[140:143], v[212:215], v[36:39], v[140:143]\n\t" \
  "v_mfma_f32_16x16x32_bf16 v[128:131], v[216:219], v[40:43], v[128:131]\n\t" \
  "v_mfma_f32_16x16x32_bf16 v[136:139], v[216:219], v[44:47], v[136:139]\n\t" \
  "v_mfma_f32_16x16x32_bf16 v[132:135], v[220:223], v[40:43], v[132:135]\n\t" \
  "v_mfma_f32_16x16x32_bf16 v[140:143], v[220:223], v[44:47], v[140:143]\n\t" \
  "v_mfma_f32_16x16x32_bf16 v[128:131], v[224:227], v[48:51], v[128:131]\n\t" \
  "v_mfma_f32_16x16x32_bf16 v[136:139], v[224:227], v[52:55], v[136:139]\n\t" \
  "v_mfma_f32_16x16x32_bf16 v[132:135], v[228:231], v[48:51], v[132:135]\n\t" \
  "v_mfma_f32_16x16x32_bf16 v[140:143], v[228:231], v[52:55], v[140:143]\n\t" \
  "v_mfma_f32_16x16x32_bf16 v[128:131], v[232:235], v[56:59], v[128:131]\n\t" \
  "v_mfma_f32_16x16x32_bf16 v[136:139], v[232:235], v[60:63], v[136:139]\n\t" \
  "v_mfma_f32_16x16x32_bf16 v[132:135], v[236:239], v[56:59], v[132:135]\n\t" \
  "v_mfma_f32_16x16x32_bf16 v[140:143], v[236:239], v[60:63], v[140:143]\n\t" \
  "s_setprio 0\n\t" \
  "s_add_u32 s92, s92, 65024\n\t" \
  "s_addc_u32 s93, s93, 0\n\t" \
  "ds_read_b128 v[160:163], v253 offset:0\n\t" \
  "ds_read_b128 v[164:167], v253 offset:1024\n\t" \
  "ds_read_b128 v[176:179], v240 offset:32768\n\t" \
  "ds_read_b128 v[180:183], v240 offset:49152\n\t" \
  "ds_read_b128 v[184:187], v241 offset:32768\n\t" \
  "ds_read_b128 v[188:191], v241 offset:49152\n\t" \
  "s_add_u32 m0, %[lw], 0\n\t" \
  "s_nop 0\n\t" \
  "global_load_lds_dwordx4 v248, s[92:93] sc1\n\t" \
  "ds_read_b128 v[192:195], v242 offset:32768\n\t" \
  "ds_read_b128 v[196:199], v242 offset:49152\n\t" \
  "ds_read_b128 v[200:203], v243 offset:32768\n\t" \
  "ds_read_b128 v[204:207], v243 offset:49152\n\t" \
  "s_add_u32 m0, %[lw], 1024\n\t" \
  "s_nop 0\n\t" \
  "global_load_lds_dwordx4 v249, s[92:93] sc1\n\t" \
  "ds_read_b128 v[208:211], v240 offset:33024\n\t" \
  "ds_read_b128 v[212:215], v240 offset:49408\n\t" \
  "ds_read_b128 v[216:219], v241 offset:33024\n\t" \
  "ds_read_b128 v[220:223], v241 offset:49408\n\t" \
  "s_add_u32 m0, %[lw], 2048\n\t" \
  "s_nop 0\n\t" \
  "global_load_lds_dwordx4 v250, s[92:93] sc1\n\t" \
  "ds_read_b128 v[224:227], v242 offset:33024\n\t" \
  "ds_read_b128 v[228:231], v242 offset:49408\n\t" \
  "ds_read_b128 v[232:235], v243 offset:33024\n\t" \
  "ds_read_b128 v[236:239], v243 offset:49408\n\t" \
  "s_add_u32 m0, %[lw], 3072\n\t" \
  "s_nop 0\n\t" \
  "global_load_lds_dwordx4 v251, s[92:93] sc1\n\t" \
  "s_waitcnt lgkmcnt(15)\n\t" \
  "s_waitcnt lgkmcnt(12)\n\t" \
  "s_sub_i32 s91, s86, 2\n\t" \
  "v_mov_b32 v173, s91\n\t" \
  "buffer_store_dword v173, v174, %[rs], 0 offen sc1\n\t" \
  "s_lshr_b32 s90, s87, 7\n\t" \
  "s_xor_b32 s90, s90, %[s0]\n\t" \
  "s_and_b32 s90, s90, 1\n\t" \
  "s_lshl1_add_u32 s90, s87, s90\n\t" \
  "s_lshl_b32 s90, s90, 13\n\t" \
  "v_add_f32 v160, v160, v144\n\t" \
  "v_add_f32 v161, v161, v145\n\t" \
  "v_add_f32 v162, v162, v146\n\t" \
  "v_add_f32 v163, v163, v147\n\t" \
  "v_add_f32 v164, v164, v148\n\t" \
  "v_add_f32 v165, v165, v149\n\t" \
  "v_add_f32 v166, v166, v150\n\t" \
  "v_add_f32 v167, v167, v151\n\t" \
  "v_cvt_pk_bf16_f32 v168, v160, v161\n\t" \
  "v_cvt_pk_bf16_f32 v169, v162, v163\n\t" \
  "v_cvt_pk_bf16_f32 v170, v164, v165\n\t" \
  "v_cvt_pk_bf16_f32 v171, v166, v167\n\t" \
  "buffer_store_dwordx4 v[168:171], v172, %[rt], s90 offen sc1\n\t" \
  "s_add_u32 s86, s86, 1\n\t" \
  "s_add_u32 s87, s87, 1\n\t" \
  "s_waitcnt lgkmcnt(0)\n\t" \
  "s_waitcnt vmcnt(10)\n\t" \
  "s_barrier\n\t" \
  "s_setprio 2\n\t" \
  "v_mfma_f32_16x16x32_bf16 v[128:131], v[176:179], v[64:67], v[128:131]\n\t" \
  "v_mfma_f32_16x16x32_bf16 v[136:139], v[176:179], v[68:71], v[136:139]\n\t" \
  "v_mfma_f32_16x16x32_bf16 v[132:135], v[180:183], v[64:67], v[132:135]\n\t" \
  "v_mfma_f32_16x16x32_bf16 v[140:143], v[180:183], v[68:71], v[140:143]\n\t" \
  "v_mfma_f32_16x16x32_bf16 v[128:131], v[184:187], v[72:75], v[128:131]\n\t" \
  "v_mfma_f32_16x16x32_bf16 v[136:139], v[184:187], v[76:79], v[136:139]\n\t" \
  "v_mfma_f32_16x16x32_bf16 v[132:135], v[188:191], v[72:75], v[132:135]\n\t" \
  "v_mfma_f32_16x16x32_bf16 v[140:143], v[188:191], v[76:79], v[140:143]\n\t" \
  "v_mfma_f32_16x16x32_bf16 v[128:131], v[192:195], v[80:83], v[128:131]\n\t" \
  "v_mfma_f32_16x16x32_bf16 v[136:139], v[192:195], v[84:87], v[136:139]\n\t" \
  "v_mfma_f32_16x16x32_bf16 v[132:135], v[196:199], v[80:83], v[132:135]\n\t" \
  "v_mfma_f32_16x16x32_bf16 v[140:143], v[196:199], v[84:87], v[140:143]\n\t" \
  "v_mfma_f32_16x16x32_bf16 v[128:131], v[200:203], v[88:91], v[128:131]\n\t" \
  "v_mfma_f32_16x16x32_bf16 v[136:139], v[200:203], v[92:95], v[136:139]\n\t" \
  "v_mfma_f32_16x16x32_bf16 v[132:135], v[204:207], v[88:91], v[132:135]\n\t" \
  "v_mfma_f32_16x16x32_bf16 v[140:143], v[204:207], v[92:95], v[140:143]\n\t" \
  "v_mfma_f32_16x16x32_bf16 v[128:131], v[208:211], v[96:99], v[128:131]\n\t" \
  "v_mfma_f32_16x16x32_bf16 v[136:139], v[208:211], v[100:103], v[136:139]\n\t" \
  "v_mfma_f32_16x16x32_bf16 v[132:135], v[212:215], v[96:99], v[132:135]\n\t" \
  "v_mfma_f32_16x16x32_bf16 v[140:143], v[212:215], v[100:103], v[140:143]\n\t" \
  "v_mfma_f32_16x16x32_bf16 v[128:131], v[216:219], v[104:107], v[128:131]\n\t" \
  "v_mfma_f32_16x16x32_bf16 v[136:139], v[216:219], v[108:111], v[136:139]\n\t" \
  "v_mfma_f32_16x16x32_bf16 v[132:135], v[220:223], v[104:107], v[132:135]\n\t" \
  "v_mfma_f32_16x16x32_bf16 v[140:143], v[220:223], v[108:111], v[140:143]\n\t" \
  "v_mfma_f32_16x16x32_bf16 v[128:131], v[224:227], v[112:115], v[128:131]\n\t" \
  "v_mfma_f32_16x16x32_bf16 v[136:139], v[224:227], v[116:119], v[136:139]\n\t" \
  "v_mfma_f32_16x16x32_bf16 v[132:135], v[228:231], v[112:115], v[132:135]\n\t" \
  "v_mfma_f32_16x16x32_bf16 v[140:143], v[228:231], v[116:119], v[140:143]\n\t" \
  "v_mfma_f32_16x16x32_bf16 v[128:131], v[232:235], v[120:123], v[128:131]\n\t" \
  "v_mfma_f32_16x16x32_bf16 v[136:139], v[232:235], v[124:127], v[136:139]\n\t" \
  "v_mfma_f32_16x16x32_bf16 v[132:135], v[236:239], v[120:123], v[132:135]\n\t" \
  "v_mfma_f32_16x16x32_bf16 v[140:143], v[236:239], v[124:127], v[140:143]\n\t" \
  "s_setprio 0\n\t" \
  "s_add_u32 s92, s92, 512\n\t" \
  "s_addc_u32 s93, s93, 0\n\t" \
  "ds_read_b128 v[176:179], v244 offset:0\n\t" \
  "ds_read_b128 v[180:183], v244 offset:16384\n\t" \
  "ds_read_b128 v[184:187], v245 offset:0\n\t" \
  "ds_read_b128 v[188:191], v245 offset:16384\n\t" \
  "s_add_u32 m0, %[lw], 32768\n\t" \
  "s_nop 0\n\t" \
  "global_load_lds_dwordx4 v248, s[92:93] sc1\n\t" \
  "ds_read_b128 v[192:195], v246 offset:0\n\t" \
  "ds_read_b128 v[196:199], v246 offset:16384\n\t" \
  "ds_read_b128 v[200:203], v247 offset:0\n\t" \
  "ds_read_b128 v[204:207], v247 offset:16384\n\t" \
  "s_add_u32 m0, %[lw], 33792\n\t" \
  "s_nop 0\n\t" \
  "global_load_lds_dwordx4 v249, s[92:93] sc1\n\t" \
  "ds_read_b128 v[208:211], v244 offset:256\n\t" \
  "ds_read_b128 v[212:215], v244 offset:16640\n\t" \
  "ds_read_b128 v[216:219], v245 offset:256\n\t" \
  "ds_read_b128 v[220:223], v245 offset:16640\n\t" \
  "s_add_u32 m0, %[lw], 34816\n\t" \
  "s_nop 0\n\t" \
  "global_load_lds_dwordx4 v250, s[92:93] sc1\n\t" \
  "ds_read_b128 v[224:227], v246 offset:256\n\t" \
  "ds_read_b128 v[228:231], v246 offset:16640\n\t" \
  "ds_read_b128 v[232:235], v247 offset:256\n\t" \
  "ds_read_b128 v[236:239], v247 offset:16640\n\t" \
  "s_add_u32 m0, %[lw], 35840\n\t" \
  "s_nop 0\n\t" \
  "global_load_lds_dwordx4 v251, s[92:93] sc1\n\t" \
  "ds_write_b128 v252, v[136:139] offset:0\n\t" \
  "ds_write_b128 v252, v[140:143] offset:1024\n\t" \
  "s_waitcnt lgkmcnt(0)\n\t" \
  "s_waitcnt vmcnt(4)\n\t" \
  "s_barrier\n\t" \
  "s_setprio 2\n\t" \
  "v_mfma_f32_16x16x32_bf16 v[144:147], v[176:179], v[0:3], 0\n\t" \
  "v_mfma_f32_16x16x32_bf16 v[152:155], v[176:179], v[4:7], 0\n\t" \
  "v_mfma_f32_16x16x32_bf16 v[148:151], v[180:183], v[0:3], 0\n\t" \
  "v_mfma_f32_16x16x32_bf16 v[156:159], v[180:183], v[4:7], 0\n\t" \
  "v_mfma_f32_16x16x32_bf16 v[144:147], v[184:187], v[8:11], v[144:147]\n\t" \
  "v_mfma_f32_16x16x32_bf16 v[152:155], v[184:187], v[12:15], v[152:155]\n\t" \
  "v_mfma_f32_16x16x32_bf16 v[148:151], v[188:191], v[8:11], v[148:151]\n\t" \
  "v_mfma_f32_16x16x32_bf16 v[156:159], v[188:191], v[12:15], v[156:159]\n\t" \
  "v_mfma_f32_16x16x32_bf16 v[144:147], v[192:195], v[16:19], v[144:147]\n\t" \
  "v_mfma_f32_16x16x32_bf16 v[152:155], v[192:195], v[20:23], v[152:155]\n\t" \
  "v_mfma_f32_16x16x32_bf16 v[148:151], v[196:199], v[16:19], v[148:151]\n\t" \
  "v_mfma_f32_16x16x32_bf16 v[156:159], v[196:199], v[20:23], v[156:159]\n\t" \
  "v_mfma_f32_16x16x32_bf16 v[144:147], v[200:203], v[24:27], v[144:147]\n\t" \
  "v_mfma_f32_16x16x32_bf16 v[152:155], v[200:203], v[28:31], v[152:155]\n\t" \
  "v_mfma_f32_16x16x32_bf16 v[148:151], v[204:207], v[24:27], v[148:151]\n\t" \
  "v_mfma_f32_16x16x32_bf16 v[156:159], v[204:207], v[28:31], v[156:159]\n\t" \
  "v_mfma_f32_16x16x32_bf16 v[144:147], v[208:211], v[32:35], v[144:147]\n\t" \
  "v_mfma_f32_16x16x32_bf16 v[152:155], v[208:211], v[36:39], v[152:155]\n\t" \
  "v_mfma_f32_16x16x32_bf16 v[148:151], v[212:215], v[32:35], v[148:151]\n\t" \
  "v_mfma_f32_16x16x32_bf16 v[156:159], v[212:215], v[36:39], v[156:159]\n\t" \
  "v_mfma_f32_16x16x32_bf16 v[144:147], v[216:219], v[40:43], v[144:147]\n\t" \
  "v_mfma_f32_16x16x32_bf16 v[152:155], v[216:219], v[44:47], v[152:155]\n\t" \
  "v_mfma_f32_16x16x32_bf16 v[148:151], v[220:223], v[40:43], v[148:151]\n\t" \
  "v_mfma_f32_16x16x32_bf16 v[156:159], v[220:223], v[44:47], v[156:159]\n\t" \
  "v_mfma_f32_16x16x32_bf16 v[144:147], v[224:227], v[48:51], v[144:147]\n\t" \
  "v_mfma_f32_16x16x32_bf16 v[152:155], v[224:227], v[52:55], v[152:155]\n\t" \
  "v_mfma_f32_16x16x32_bf16 v[148:151], v[228:231], v[48:51], v[148:151]\n\t" \
  "v_mfma_f32_16x16x32_bf16 v[156:159], v[228:231], v[52:55], v[156:159]\n\t" \
  "v_mfma_f32_16x16x32_bf16 v[144:147], v[232:235], v[56:59], v[144:147]\n\t" \
  "v_mfma_f32_16x16x32_bf16 v[152:155], v[232:235], v[60:63], v[152:155]\n\t" \
  "v_mfma_f32_16x16x32_bf16 v[148:151], v[236:239], v[56:59], v[148:151]\n\t" \
  "v_mfma_f32_16x16x32_bf16 v[156:159], v[236:239], v[60:63], v[156:159]\n\t" \
  "s_setprio 0\n\t" \
  "s_add_u32 s92, s92, 65024\n\t" \
  "s_addc_u32 s93, s93, 0\n\t" \
  "ds_read_b128 v[160:163], v253 offset:0\n\t" \
  "ds_read_b128 v[164:167], v253 offset:1024\n\t" \
  "ds_read_b128 v[176:179], v244 offset:32768\n\t" \
  "ds_read_b128 v[180:183], v244 offset:49152\n\t" \
  "ds_read_b128 v[184:187], v245 offset:32768\n\t" \
  "ds_read_b128 v[188:191], v245 offset:49152\n\t" \
  "s_add_u32 m0, %[lw], 65536\n\t" \
  "s_nop 0\n\t" \
  "global_load_lds_dwordx4 v248, s[92:93] sc1\n\t" \
  "ds_read_b128 v[192:195], v246 offset:32768\n\t" \
  "ds_read_b128 v[196:199], v246 offset:49152\n\t" \
  "ds_read_b128 v[200:203], v247 offset:32768\n\t" \
  "ds_read_b128 v[204:207], v247 offset:49152\n\t" \
  "s_add_u32 m0, %[lw], 66560\n\t" \
  "s_nop 0\n\t" \
  "global_load_lds_dwordx4 v249, s[92:93] sc1\n\t" \
  "ds_read_b128 v[208:211], v244 offset:33024\n\t" \
  "ds_read_b128 v[212:215], v244 offset:49408\n\t" \
  "ds_read_b128 v[216:219], v245 offset:33024\n\t" \
  "ds_read_b128 v[220:223], v245 offset:49408\n\t" \
  "s_add_u32 m0, %[lw], 67584\n\t" \
  "s_nop 0\n\t" \
  "global_load_lds_dwordx4 v250, s[92:93] sc1\n\t" \
  "ds_read_b128 v[224:227], v246 offset:33024\n\t" \
  "ds_read_b128 v[228:231], v246 offset:49408\n\t" \
  "ds_read_b128 v[232:235], v247 offset:33024\n\t" \
  "ds_read_b128 v[236:239], v247 offset:49408\n\t" \
  "s_add_u32 m0, %[lw], 68608\n\t" \
  "s_nop 0\n\t" \
  "global_load_lds_dwordx4 v251, s[92:93] sc1\n\t" \
  "s_waitcnt lgkmcnt(15)\n\t" \
  "s_waitcnt lgkmcnt(12)\n\t" \
  "s_sub_i32 s91, s86, 2\n\t" \
  "v_mov_b32 v173, s91\n\t" \
  "buffer_store_dword v173, v174, %[rs], 0 offen sc1\n\t" \
  "s_lshr_b32 s90, s87, 7\n\t" \
  "s_xor_b32 s90, s90, %[s0]\n\t" \
  "s_and_b32 s90, s90, 1\n\t" \
  "s_lshl1_add_u32 s90, s87, s90\n\t" \
  "s_lshl_b32 s90, s90, 13\n\t" \
  "v_add_f32 v160, v160, v128\n\t" \
  "v_add_f32 v161, v161, v129\n\t" \
  "v_add_f32 v162, v162, v130\n\t" \
  "v_add_f32 v163, v163, v131\n\t" \
  "v_add_f32 v164, v164, v132\n\t" \
  "v_add_f32 v165, v165, v133\n\t" \
  "v_add_f32 v166, v166, v134\n\t" \
  "v_add_f32 v167, v167, v135\n\t" \
  "v_cvt_pk_bf16_f32 v168, v160, v161\n\t" \
  "v_cvt_pk_bf16_f32 v169, v162, v163\n\t" \
  "v_cvt_pk_bf16_f32 v170, v164, v165\n\t" \
  "v_cvt_pk_bf16_f32 v171, v166, v167\n\t" \
  "buffer_store_dwordx4 v[168:171], v172, %[rt], s90 offen sc1\n\t" \
  "s_add_u32 s86, s86, 1\n\t" \
  "s_add_u32 s87, s87, 1\n\t" \
  "s_waitcnt lgkmcnt(0)\n\t" \
  "s_cmp_lt_u32 s86, 64\n\t" \
  "s_cbranch_scc1 .Lp1%=\n\t" \
  "s_barrier\n\t" \
  "s_setprio 2\n\t" \
  "v_mfma_f32_16x16x32_bf16 v[144:147], v[176:179], v[64:67], v[144:147]\n\t" \
  "v_mfma_f32_16x16x32_bf16 v[152:155], v[176:179], v[68:71], v[152:155]\n\t" \
  "v_mfma_f32_16x16x32_bf16 v[148:151], v[180:183], v[64:67], v[148:151]\n\t" \
  "v_mfma_f32_16x16x32_bf16 v[156:159], v[180:183], v[68:71], v[156:159]\n\t" \
  "v_mfma_f32_16x16x32_bf16 v[144:147], v[184:187], v[72:75], v[144:147]\n\t" \
  "v_mfma_f32_16x16x32_bf16 v[152:155], v[184:187], v[76:79], v[152:155]\n\t" \
  "v_mfma_f32_16x16x32_bf16 v[148:151], v[188:191], v[72:75], v[148:151]\n\t" \
  "v_mfma_f32_16x16x32_bf16 v[156:159], v[188:191], v[76:79], v[156:159]\n\t" \
  "v_mfma_f32_16x16x32_bf16 v[144:147], v[192:195], v[80:83], v[144:147]\n\t" \
  "v_mfma_f32_16x16x32_bf16 v[152:155], v[192:195], v[84:87], v[152:155]\n\t" \
  "v_mfma_f32_16x16x32_bf16 v[148:151], v[196:199], v[80:83], v[148:151]\n\t" \
  "v_mfma_f32_16x16x32_bf16 v[156:159], v[196:199], v[84:87], v[156:159]\n\t" \
  "v_mfma_f32_16x16x32_bf16 v[144:147], v[200:203], v[88:91], v[144:147]\n\t" \
  "v_mfma_f32_16x16x32_bf16 v[152:155], v[200:203], v[92:95], v[152:155]\n\t" \
  "v_mfma_f32_16x16x32_bf16 v[148:151], v[204:207], v[88:91], v[148:151]\n\t" \
  "v_mfma_f32_16x16x32_bf16 v[156:159], v[204:207], v[92:95], v[156:159]\n\t" \
  "v_mfma_f32_16x16x32_bf16 v[144:147], v[208:211], v[96:99], v[144:147]\n\t" \
  "v_mfma_f32_16x16x32_bf16 v[152:155], v[208:211], v[100:103], v[152:155]\n\t" \
  "v_mfma_f32_16x16x32_bf16 v[148:151], v[212:215], v[96:99], v[148:151]\n\t" \
  "v_mfma_f32_16x16x32_bf16 v[156:159], v[212:215], v[100:103], v[156:159]\n\t" \
  "v_mfma_f32_16x16x32_bf16 v[144:147], v[216:219], v[104:107], v[144:147]\n\t" \
  "v_mfma_f32_16x16x32_bf16 v[152:155], v[216:219], v[108:111], v[152:155]\n\t" \
  "v_mfma_f32_16x16x32_bf16 v[148:151], v[220:223], v[104:107], v[148:151]\n\t" \
  "v_mfma_f32_16x16x32_bf16 v[156:159], v[220:223], v[108:111], v[156:159]\n\t" \
  "v_mfma_f32_16x16x32_bf16 v[144:147], v[224:227], v[112:115], v[144:147]\n\t" \
  "v_mfma_f32_16x16x32_bf16 v[152:155], v[224:227], v[116:119], v[152:155]\n\t" \
  "v_mfma_f32_16x16x32_bf16 v[148:151], v[228:231], v[112:115], v[148:151]\n\t" \
  "v_mfma_f32_16x16x32_bf16 v[156:159], v[228:231], v[116:119], v[156:159]\n\t" \
  "v_mfma_f32_16x16x32_bf16 v[144:147], v[232:235], v[120:123], v[144:147]\n\t" \
  "v_mfma_f32_16x16x32_bf16 v[152:155], v[232:235], v[124:127], v[152:155]\n\t" \
  "v_mfma_f32_16x16x32_bf16 v[148:151], v[236:239], v[120:123], v[148:151]\n\t" \
  "v_mfma_f32_16x16x32_bf16 v[156:159], v[236:239], v[124:127], v[156:159]\n\t" \
  "s_setprio 0\n\t" \
  "s_nop 4\n\t" \
  "ds_write_b128 v252, v[152:155] offset:0\n\t" \
  "s_nop 0\n\t" \
  "ds_write_b128 v252, v[156:159] offset:1024\n\t" \
  "s_waitcnt lgkmcnt(0)\n\t" \
  "s_barrier\n\t" \
  "ds_read_b128 v[160:163], v253 offset:0\n\t" \
  "ds_read_b128 v[164:167], v253 offset:1024\n\t" \
  "s_waitcnt lgkmcnt(0)\n\t" \
  "s_lshr_b32 s90, s87, 7\n\t" \
  "s_xor_b32 s90, s90, %[s0]\n\t" \
  "s_and_b32 s90, s90, 1\n\t" \
  "s_lshl1_add_u32 s90, s87, s90\n\t" \
  "s_lshl_b32 s90, s90, 13\n\t" \
  "v_add_f32 v160, v160, v144\n\t" \
  "v_add_f32 v161, v161, v145\n\t" \
  "v_add_f32 v162, v162, v146\n\t" \
  "v_add_f32 v163, v163, v147\n\t" \
  "v_add_f32 v164, v164, v148\n\t" \
  "v_add_f32 v165, v165, v149\n\t" \
  "v_add_f32 v166, v166, v150\n\t" \
  "v_add_f32 v167, v167, v151\n\t" \
  "v_cvt_pk_bf16_f32 v168, v160, v161\n\t" \
  "v_cvt_pk_bf16_f32 v169, v162, v163\n\t" \
  "v_cvt_pk_bf16_f32 v170, v164, v165\n\t" \
  "v_cvt_pk_bf16_f32 v171, v166, v167\n\t" \
  "buffer_store_dwordx4 v[168:171], v172, %[rt], s90 offen sc1\n\t" \
  "s_waitcnt vmcnt(0)\n\t" \
  "s_barrier\n\t" \
  "s_sub_i32 s91, s86, 1\n\t" \
  "v_mov_b32 v173, s91\n\t" \
  "buffer_store_dword v173, v174, %[rs], 0 offen sc1\n\t" \
  "s_mov_b32 m0, s94\n\t" \
  ".Le%=:\n\t"
constexpr int W_X = 131072, W_PM = W_X + 16384;
template <bool ISV>
__device__ __forceinline__ void job_proj(Args& p, int g, int which, int h, char* smem, __amdgpu_buffer_rsrc_t rsy) {
  const int tid = threadIdx.x, lane = tid & 63, r = lane & 15, q = lane >> 4;
  const int w = __builtin_amdgcn_readfirstlane(tid >> 6), kh = w >> 2, cg = w & 3;
  u32x32 W[4];
  const unsigned lw = lds_off(smem) + w * 4096;
  u32x4 vx;
  u32x2 tag;
  const char* xn = p.ws + WS_XN;
  auto row = [&](int t, int i) {
    const char* src = xn + (size_t)(min(t, 127) >> 1) * (SEG * XLD * 2) + (t & 1) * 512;
    unsigned keep;
    asm volatile("s_mov_b32 %0, m0\n\ts_mov_b32 m0, %3\n\ts_nop 0\n\tglobal_load_lds_dwordx4 %1, %2 sc1\n\ts_mov_b32 m0, %0"
                 : "=&s"(keep) : "v"(vx[i]), "s"(src), "s"(lw + (t & 3) * 32768 + i * 1024) : "memory");
  };
  {
    const u16* Wm = which == 0 ? p.Wk : p.Wq;
    asm volatile("" : "+s"(Wm));
    if (ISV) Wm = p.Wv;
    const unsigned vo = (tid >> 4) * (HD * 2) + 16 * ((lane & 15) ^ swzB((tid >> 4) & 15));
    auto stage = [&](int j) {
      const char* src = (const char*)(Wm + (size_t)(WREV ? 7 - j : j) * 64 * HD + h * DH);
      const unsigned dst = lds_off(smem) + (j & 3) * 32768 + w * 1024;
      glds2(src, 32 * HD * 2, vo, dst);
      glds2(src + 512 * HD * 2, 32 * HD * 2, vo, dst + 16384);
    };
    const unsigned own = min((h & LN_M) ^ LN_X, 1);
    tag = start_rows<16>(p, 32 + g, own, [&]() { stage(0); stage(1); stage(2); stage(3); });
#pragma unroll
    for (int i = 0; i < 4; ++i) vx[i] = ((4 * w + i) ^ (ISV ? 0 : (lane >> 1) & 16)) * (XLD * 2) + (lane >> 5) * 1024 + (((lane & 31) ^ ((4 * w + i) & 15)) << 4);
    Boot bt;
#pragma unroll
    for (int j = 0; j < 8; ++j) {
      if (j < 4) WAIT_V(16); else if (j == 4) WAIT_V(12); else if (j == 5) WAIT_V(8); else if (j == 6) WAIT_V(4); else WAIT_V(10);
      if (j == 6 && w == 0) lds_at(smem + W_PM)[0] = boot_done(p.ws, bt, true, tag);
      LBAR();
      if (j == 4 && own) report_rows(p, 32 + g, tag);
      const char* sb = smem + (j & 3) * 32768 + kh * 16384;
#pragma unroll
      for (int k = 0; k < 4; ++k) {
        const int ks = k >> 1, a = k & 1;
        const unsigned ch = ISV ? 2 * (2 * cg + (a ^ kh)) + ((r & 3) >> 1) : 4 * cg + (r & 3), by = ISV ? 8 * (r & 1) : 8 * a;
        s16x4 lo = __builtin_amdgcn_ds_read_tr16_b64_v4i16((lds_s16x4_ptr)(sb + offB(32 * ks + 8 * q + (r >> 2), ch) + by));
        s16x4 hi = __builtin_amdgcn_ds_read_tr16_b64_v4i16((lds_s16x4_ptr)(sb + offB(32 * ks + 8 * q + 4 + (r >> 2), ch) + by));
        const int f = 4 * (WREV ? 7 - j : j) + k;
        put4(W[f >> 3], f & 7, (bf16x8){lo[0], lo[1], lo[2], lo[3], hi[0], hi[1], hi[2], hi[3]});
      }
      LBAR();
      if (j == 3 && w == 0) boot_ask(p.ws, bt);
      if (j + 4 < 8) stage(j + 4);
      if (j == 6) {
#pragma unroll
        for (int i = 0; i < 10; ++i) row(i >> 2, i & 3);
      }
    }
    p.s0 = uni(lds_at(smem + W_PM)[0] >> 31);
  }
  typedef __attribute__((ext_vector_type(8))) unsigned u32x8;
  u32x8 la;
#pragma unroll
  for (int i = 0; i < 8; ++i) la[i] = (i >> 2) * 65536 + r * 1024 + kh * 512 + (((4 * (i & 3) + q) ^ r) << 4);
  const unsigned xw = W_X + w * 2048 + lane * 16;
  u32x32 acc;
  asm volatile("" : "={v[128:159]}"(acc));
  WAIT_V(6);
  LBAR();
  {
    const int g0 = 0;
#define PR_LOOP(TXT)                                                                                                              \
    asm volatile(TXT                                                                                                              \
        : "+{v[128:159]}"(acc)                                                                                                    \
        : W4_IN, "{v[208:215]}"(la), "{v[216:219]}"(vx), "{v220}"(xw), "{v221}"(xw ^ 8192),                                       \
          "{v172}"((ISV ? 2 * cg + kh : 4 * kh + cg) * 1024 + lane * 16), "{v174}"(tid == 256 ? fkq(h, which) : 0x7F000000),      \
          [rt] "s"(raw_rsrc(p.ws + WS_T, T_BYTES)), [rs] "s"(raw_rsrc(p.ws + WS_SY, SY_TOTAL)), [s0] "s"(p.s0 ^ TF_KQV),          \
          [lw] "s"(lw), [kh] "s"(kh), [g0] "s"(g0), [end] "s"(g0 ? NSEG : PR_G1), [x0] "s"(xn + (g0 + 1) * (SEG * XLD * 2)),                       \
          [t0] "s"(which * T_Q + kseg(0, h) - 1)                                                                                  \
        : CLB8(160, 161, 162, 163, 164, 165, 166, 167), "v168", "v169", "v170", "v171", "v173", CLB8(176, 177, 178, 179, 180, 181, 182, 183), \
          CLB8(184, 185, 186, 187, 188, 189, 190, 191), CLB8(192, 193, 194, 195, 196, 197, 198, 199),                             \
          CLB8(200, 201, 202, 203, 204, 205, 206, 207), "s86", "s87", "s88", "s90", "s91", "s92", "s93", "s94", "scc", "memory")
    if (ISV) PR_LOOP(PR_ASM_1); else PR_LOOP(PR_ASM_0);
    if (w == 0 && g0 == 0) wait_rows(p.ws, tag);
  }
#define PP_LOOP(TXT)                                                                                                              \
  asm volatile(TXT                                                                                                                \
      : "+{v[128:159]}"(acc)                                                                                                      \
      : W4_IN, "{v[240:247]}"(la), "{v[248:251]}"(vx), "{v252}"(xw), "{v253}"(xw ^ 8192),                                         \
        "{v172}"((ISV ? 2 * cg + kh : 4 * kh + cg) * 1024 + lane * 16), "{v174}"(tid == 256 ? fkq(h, which) : 0x7F000000),        \
        [rt] "s"(raw_rsrc(p.ws + WS_T, T_BYTES)), [rs] "s"(raw_rsrc(p.ws + WS_SY, SY_TOTAL)), [s0] "s"(p.s0 ^ TF_KQV),            \
        [lw] "s"(lw), [kh] "s"(kh), [g0] "n"(PR_G1), [x0] "s"(xn + (PR_G1 + 1) * (SEG * XLD * 2)),                                \
        [t0] "s"(which * T_Q + kseg(0, h) - 1)                                                                                    \
      : CLB8(160, 161, 162, 163, 164, 165, 166, 167), "v168", "v169", "v170", "v171", "v173", CLB8(176, 177, 178, 179, 180, 181, 182, 183), \
        CLB8(184, 185, 186, 187, 188, 189, 190, 191), CLB32(192), CLB8(224, 225, 226, 227, 228, 229, 230, 231),                   \
        CLB8(232, 233, 234, 235, 236, 237, 238, 239), "s86", "s87", "s90", "s91", "s92", "s93", "s94", "scc", "memory")
  if (ISV) PP_LOOP(PP_ASM_1); else PP_LOOP(PP_ASM_0);
}

enum { J_NONE = 0, J_GU = 3, J_PR = 5, J_CB = 8, J_DN = 14 };
struct BigJob { int type, panel, idx; };
struct BigPtr { const char *a, *b0, *b1; };
constexpr int G_LDA = XLD * 2, G_LDB = FF * 2;

__device__ __forceinline__ BigPtr big_ptrs(const Args& p, const BigJob& j) {
  BigPtr r;
  r.a = p.ws + WS_XN + (size_t)j.panel * 128 * G_LDA;
  r.b0 = (const char*)(p.wg + j.idx * 128); r.b1 = (const char*)(p.wu + j.idx * 128);
  return r;
}
__device__ __forceinline__ void big_publish(const Args& p, const BigJob& j) {
  int tid = threadIdx.x;
  asm volatile("" : "+v"(tid));
  if (j.type == J_GU && tid == 0)
    __hip_atomic_fetch_add((gu32*)(p.ws + WS_SY + SY_CH) + j.panel * 16, 1u, __ATOMIC_RELAXED, __HIP_MEMORY_SCOPE_AGENT);
}
typedef f32x4 Acc[2][2][4][2];
__device__ __forceinline__ void big_epilogue(const Args& p, const BigJob& j, Acc& acc, char* smem) {
  int tid = threadIdx.x;
  asm volatile("" : "+v"(tid));
  const int lane = tid & 63;
  const int wid = __builtin_amdgcn_readfirstlane(tid >> 6);
  const int wr = wid >> 2, wc = wid & 3;
  {
    const __amdgpu_buffer_rsrc_t ro = mkrsrc(p.ws + WS_HF, S_LEN * 16384);
#pragma unroll
    for (int i = 0; i < 8; ++i) {
      f32x4 hh[2];
#pragma unroll
      for (int n = 0; n < 2; ++n)
#pragma unroll
        for (int jj = 0; jj < 4; ++jj) {
          const float g = acc[i >> 2][0][i & 3][n][jj], u = acc[i >> 2][1][i & 3][n][jj];
          hh[n][jj] = __builtin_amdgcn_rcpf(1.0f + __builtin_amdgcn_exp2f(g * -1.4426950408889634f)) * u;
        }
      st_sc1(ro, h_off(p, j.panel + (i >> 2)) + (j.idx * 4 + wc) * 16384 + (wr * 4 + (i & 3)) * 1024 + lane * 16,
             ((u32x4){cvtpk(hh[0][0], hh[0][1]), cvtpk(hh[0][2], hh[0][3]), cvtpk(hh[1][0], hh[1][1]), cvtpk(hh[1][2], hh[1][3])}));
    }
  }
}
__device__ __forceinline__ void gemm_role(Args& p, int g, char* smem) {
  const __amdgpu_buffer_rsrc_t rsy = mkrsrc(p.ws + WS_SY, SY_TOTAL);
  const int x = g & 3, k = g >> 2;
  volatile unsigned* slot = reinterpret_cast<volatile unsigned*>(smem + 163824);
  int rnd = k < 24 ? 2 : -1;
  auto claim = [&]() {
    ++rnd;
    const int n = k < 24 ? 32 + k : rnd * 8 + k - 24, pn = (n * 37) >> 8;
    BigJob b;
    b.type = rnd < 4 ? J_GU : (rnd == 4 ? J_DN : (rnd == 5 && k >= 24 && k - 24 < 14 - NFUSE ? J_CB : J_NONE));
    b.panel = rnd < 4 ? 14 - 2 * pn : (rnd == 4 ? 4 * ((2 - (k >> 3)) & 3) + x : NFUSE + k - 24);
    b.idx = rnd < 4 ? x + 4 * (n - 7 * pn) : k & 7;
    return b;
  };
  if (k < 24) {
    if (k >= 16) job_proj<true>(p, g, 2, x * 8 + (k & 7), smem, rsy);
    else job_proj<false>(p, g, k >> 3, x * 8 + (k & 7), smem, rsy);
  } else {
    u32x2 tag;
#pragma unroll 1
    for (int u = 0; u < (k < 30 && LN_M ? 2 : 1); ++u) {
      const int rg = u ? 32 + (4 * (k - 24) + 1) * 4 + x : g - 96;
      tag = start_rows<0>(p, rg, 1, []() {});
      WAIT_V(0);
      LBAR();
      report_rows(p, rg, tag);
    }
#pragma unroll 1
    for (int i = 0; i < PF_N; ++i) {
      const int m = i % 3, o = ((i / 3) * 32 + g - 96) * 448 + (int)threadIdx.x;
      if (threadIdx.x < 448) touch_line((const char*)(m == 0 ? p.wg : (m == 1 ? p.wu : p.wd)) + (size_t)o * 128, smem + (threadIdx.x >> 6) * 256);
      WAIT_V(1);
    }
    WAIT_V(0);
    if (threadIdx.x < 64) {
      wait_rows(p.ws, tag);
      Boot bt;
      boot_ask(p.ws, bt);
      WAIT_V(0);
      *slot = boot_done(p.ws, bt, true, tag);
    }
    LBAR();
    p.s0 = *slot >> 31;
  }
  p.s0 = uni(p.s0);
  BigJob cur = claim();
  while (cur.type != J_NONE) {
    if (cur.type != J_GU) {
      if (cur.type == J_DN) job_down(p, cur.panel, cur.idx, smem, rsy);
      else job_comb(p, cur.panel, x, rsy);
      cur = claim();
      continue;
    }
    int tid = threadIdx.x;
    asm volatile("" : "+v"(tid));
    const int lane = tid & 63;
    const int wid = __builtin_amdgcn_readfirstlane(tid >> 6);
    const int wr = wid >> 2, wc = wid & 3;
    const unsigned ra = tid >> 3, ca = 16 * ((tid & 7) ^ ((tid >> 3) & 7)), rb = tid >> 4, cb = 16 * ((tid & 15) ^ swzB((tid >> 4) & 15));
    const unsigned lw = lds_off(smem) + wid * 1024;
#define B8_SA(b, h) (((b) * 2 + (h)) * 16384)
#define B8_SB(b, h) ((4 + (b) * 2 + (h)) * 16384)
#define B8_STA(b, h, P, kt) glds2<true>((P).a + (size_t)(h) * 128 * G_LDA + (kt) * 128, 64 * G_LDA, ra * G_LDA + ca, lw + B8_SA(b, h))
#define B8_STB(b, h, P, kt) glds2(((h) ? (P).b1 : (P).b0) + (size_t)(kt) * 64 * G_LDB, 32 * G_LDB, rb * G_LDB + cb, lw + B8_SB(b, h))
#define B8_LDA(b, h) do { _Pragma("unroll") for (int m = 0; m < 4; ++m) _Pragma("unroll") for (int kk = 0; kk < 2; ++kk) \
      At[m][kk] = ldsf(smem + B8_SA(b, h) + offA(wr * 64 + m * 16 + (lane & 15), kk * 4 + (lane >> 4))); } while (0)
#define B8_LDB(dst, b, h) do { _Pragma("unroll") for (int n = 0; n < 2; ++n) _Pragma("unroll") for (int kk = 0; kk < 2; ++kk) \
      dst[n][kk] = ld_bfrag(smem + B8_SB(b, h), kk, wc * 2 + n, lane); } while (0)
#define B8_MMA(ai, bj, Bt) do { _Pragma("unroll") for (int m = 0; m < 4; ++m) _Pragma("unroll") for (int n = 0; n < 2; ++n) \
      _Pragma("unroll") for (int kk = 0; kk < 2; ++kk) acc[ai][bj][m][n] = MFMA(Bt[n][kk], At[m][kk], acc[ai][bj][m][n]); } while (0)
#define WAIT_L(n) asm volatile("s_waitcnt lgkmcnt(%0)" :: "n"(n) : "memory")
#define BAR() __builtin_amdgcn_s_barrier()
#define BAR_1() do { SCHED(); asm volatile("s_setprio 1\n\ts_cmp_eq_u32 %0, 0\n\ts_cbranch_scc1 .Lb%=\n\ts_setprio 2\n\ts_barrier\n.Lb%=:" :: "s"(wr) : "memory", "scc"); SCHED(); } while (0)
#define BAR_0() do { SCHED(); asm volatile("s_setprio 0\n\ts_cmp_lg_u32 %0, 0\n\ts_cbranch_scc1 .Lb%=\n\ts_barrier\n.Lb%=:" :: "s"(wr) : "memory", "scc"); SCHED(); } while (0)
#define WAIT_RX(c) asm volatile("s_waitcnt vmcnt(16)\n\ts_cmp_lg_u32 %0, 0\n\ts_cbranch_scc1 .Lrx%=\n\ts_waitcnt vmcnt(8)\n.Lrx%=:" :: "s"(c) : "memory", "scc")
    Acc acc;
    bf16x8 At[4][2], B0[2][2], B1[2][2];
    BigPtr cp = big_ptrs(p, cur);
    BigJob pend; pend.type = J_NONE; pend.panel = 0; pend.idx = 0;
    LBAR();
    B8_STB(0, 0, cp, 0); B8_STB(0, 1, cp, 0); B8_STA(0, 0, cp, 0); B8_STA(0, 1, cp, 0);
    B8_STB(1, 0, cp, 1); B8_STA(1, 0, cp, 1); B8_STB(1, 1, cp, 1);
    WAIT_V(8); BAR();
    BigJob nx = pend;
    for (;;) {
      bool chain = false;
      BigPtr np = cp;
      constexpr int nt = DM / 64;
#pragma unroll
      for (int i = 0; i < 32; ++i) acc[i >> 4][(i >> 3) & 1][(i >> 1) & 3][i & 1] = (f32x4){0.f, 0.f, 0.f, 0.f};
#pragma unroll 1
      for (int t = 0; t < nt; t += 2) {
        const bool last = t == nt - 2;
        if (last) {
          nx = claim();
          chain = nx.type == J_GU;
          if (chain) np = big_ptrs(p, nx);
        }
        const BigPtr sp = np;
        const int t2 = last ? 0 : t + 2;
        if (t == 2) big_publish(p, pend);
        const int rx = __builtin_amdgcn_readfirstlane(t == 0 && pend.type != J_NONE);
        B8_LDB(B0, 0, 0); B8_LDB(B1, 0, 1); SCHED(); B8_LDA(0, 0); B8_STA(1, 1, cp, t + 1);
        WAIT_RX(rx);
        WAIT_L(0); BAR_1(); B8_MMA(0, 0, B0); B8_MMA(0, 1, B1); BAR_0();
        B8_LDA(0, 1); B8_STB(0, 0, sp, t2); B8_STB(0, 1, sp, t2); B8_STA(0, 0, sp, t2);
        WAIT_RX(rx);
        WAIT_L(0); BAR_1(); B8_MMA(1, 0, B0); B8_MMA(1, 1, B1); BAR_0();
        B8_LDB(B0, 1, 0); B8_LDB(B1, 1, 1); SCHED(); B8_LDA(1, 0); B8_STA(0, 1, sp, t2);
        WAIT_V(8); WAIT_L(0); BAR_1(); B8_MMA(0, 0, B0); B8_MMA(0, 1, B1); BAR_0();
        B8_LDA(1, 1); B8_STB(1, 0, sp, t2 + 1); B8_STB(1, 1, sp, t2 + 1); B8_STA(1, 0, sp, t2 + 1);
        WAIT_V(8); WAIT_L(0); BAR_1(); B8_MMA(1, 0, B0); B8_MMA(1, 1, B1); BAR_0();
      }
      big_epilogue(p, cur, acc, smem);
      if (!chain) break;
      pend = cur; cur = nx; cp = np;
    }
    WAIT_V(0);
    BAR();
    asm volatile(".Lcode_end:");
    big_publish(p, cur);
    cur = nx;
  }
}


__device__ __forceinline__ unsigned now_q() { return (unsigned)__builtin_amdgcn_s_memrealtime() * 4u; }
__device__ __forceinline__ void pace_until(unsigned t) {
#pragma unroll 1
  for (int i = 0; i < 256; ++i) {
    const int d = (int)(t - now_q());
    if (d <= 0) break;
    if (d > 32) __builtin_amdgcn_s_sleep(1);
  }
}
#define CLB_E "s76", "s77", "s78", "s79", "s80", "s81", "s82", "s83", "s84", "s85", "s86", "s87", "s88", "s89"
#define EDGE_ASM \
  "s_memtime s[76:77]\n\t" \
  "s_memrealtime s[78:79]\n\t" \
  "s_nop 3\n\t" \
  "s_memrealtime s[80:81]\n\t" \
  "s_nop 3\n\t" \
  "s_memrealtime s[82:83]\n\t" \
  "s_nop 3\n\t" \
  "s_memrealtime s[84:85]\n\t" \
  "s_nop 3\n\t" \
  "s_memrealtime s[86:87]\n\t" \
  "s_nop 3\n\t" \
  "s_memrealtime s[88:89]\n\t" \
  "s_mov_b32 s93, 0\n\t" \
  "s_waitcnt lgkmcnt(0)\n\t" \
  "s_cmp_eq_u32 s80, s78\n\t" \
  "s_addc_u32 s93, s93, 0\n\t" \
  "s_cmp_eq_u32 s82, s78\n\t" \
  "s_addc_u32 s93, s93, 0\n\t" \
  "s_cmp_eq_u32 s84, s78\n\t" \
  "s_addc_u32 s93, s93, 0\n\t" \
  "s_cmp_eq_u32 s86, s78\n\t" \
  "s_addc_u32 s93, s93, 0\n\t" \
  "s_cmp_eq_u32 s88, s78\n\t" \
  "s_addc_u32 s93, s93, 0\n\t" \
  "s_mul_i32 s93, s93, 21\n\t" \
  "s_add_u32 s76, s76, 15\n\t" \
  "s_add_u32 %[kc], s76, s93\n\t" \
  "s_lshl2_add_u32 %[kq], s78, 16\n\t"
__device__ __forceinline__ unsigned time_of(unsigned cyc) {
  unsigned c, q;
  asm volatile(EDGE_ASM : [kc] "=&s"(c), [kq] "=&s"(q) :: CLB_E, "s93", "scc");
  return q - (((c - cyc) * 11900u) >> 16);
}
#define DS_RED 20
#define DS_MEM 13
#define LEAD_DN 2
#define LEAD_UP 6
__device__ __forceinline__ u32x2 ld2_sc1(__amdgpu_buffer_rsrc_t rs, int off, int soff = 0) {
  return __builtin_amdgcn_raw_buffer_load_b64(rs, off, soff, 16);
}
__device__ __forceinline__ void st2_sc1(__amdgpu_buffer_rsrc_t rs, int off, u32x2 v) {
  __builtin_amdgcn_raw_buffer_store_b64(v, rs, off, 0, 16);
}
__device__ __forceinline__ int row_max(int v) {
  v = max(v, __builtin_amdgcn_update_dpp(0, v, 0x128, 0xf, 0xf, false));
  v = max(v, __builtin_amdgcn_update_dpp(0, v, 0x124, 0xf, 0xf, false));
  v = max(v, __builtin_amdgcn_update_dpp(0, v, 0x122, 0xf, 0xf, false));
  v = max(v, __builtin_amdgcn_update_dpp(0, v, 0x121, 0xf, 0xf, false));
  return v;
}
__device__ __forceinline__ int wave_max(int v) {
  v = row_max(v);
  return max(max(__builtin_amdgcn_readlane(v, 0), __builtin_amdgcn_readlane(v, 16)),
             max(__builtin_amdgcn_readlane(v, 32), __builtin_amdgcn_readlane(v, 48)));
}
__device__ __forceinline__ int track(int est, int x, int up, int dn, int out) {
  const int d = x - est;
  return est + (d > out ? 4 : max(min(d, up), -dn));
}
#define WSL 12
#define PLAN_SLEEP 2
#define REF_W 3
#define REF_G 60
#define RL0 315
__device__ __forceinline__ void watch(__amdgpu_buffer_rsrc_t rsy, int s, int lane, unsigned t0, const unsigned* tgt_, gu32* abortw, int hs) {
  const int rl = (int)__builtin_amdgcn_raw_buffer_load_b32(rsy, lane * 128, SY_BR + hs, 16);
  u32x2 ra;
  for (unsigned spins = 0;;) {
    asm volatile("" ::: "memory");
    ra = ld2_sc1(rsy, (lane & 31) * 128, SY_BA + hs);
    const bool all = __all((int)ra[0] >= s);
    if (all) break;
    if (spins < 3) __builtin_amdgcn_s_sleep(PLAN_SLEEP); else __builtin_amdgcn_s_sleep(WSL);
    if (spin_fail(spins, abortw)) break;
  }
  int d = ra[0] == (unsigned)s ? (int)(ra[1] - t0) : 0;
  d = max(d, __builtin_amdgcn_update_dpp(0, d, 0xB1, 0xf, 0xf, false));
  d = max(d, __builtin_amdgcn_update_dpp(0, d, 0x4E, 0xf, 0xf, false));
  int sum = d + __builtin_amdgcn_update_dpp(0, d, 0x128, 0xf, 0xf, false);
  sum += __builtin_amdgcn_update_dpp(0, sum, 0x124, 0xf, 0xf, false);
  const int mx = wave_max(d), mean = (__builtin_amdgcn_readlane(sum, 0) + __builtin_amdgcn_readlane(sum, 16)) >> 3;
  const unsigned t = t0 + mx - min(((mx - mean) * REF_W) >> 2, REF_G) + wave_max(rl < 0 ? RL0 : rl) + (DS_RED + DS_MEM);
  lds_vu_ptr tgt = lds_at(tgt_);
  if (lane < 2) tgt[lane] = lane ? t : s + 1;
}
__device__ __forceinline__ unsigned ld_flag(__amdgpu_buffer_rsrc_t rs, int off) {
  return __builtin_amdgcn_raw_buffer_load_b32(rs, off, 0, 16);
}

constexpr unsigned long long QK_MAP[8] = {0xbaf9876d543210ecull, 0xa98f76d543210bceull, 0x98f76d543210ecbaull, 0x876fd543210bcea9ull, 0x76fd543210ecba98ull, 0x65df43210bcea987ull, 0x5df43210ecba9876ull, 0xd43f210bcea98765ull};
#define QK_ASM_0 \
  "s_waitcnt lgkmcnt(0)\n\t" \
  "v_subrev_u32 v244, 0x8000, v250\n\t" \
  "ds_read_b128 v[216:219], v248 offset:32768\n\t" \
  "ds_read_b128 v[220:223], v248 offset:33792\n\t" \
  "ds_read_b128 v[224:227], v248 offset:34816\n\t" \
  "ds_read_b128 v[228:231], v248 offset:35840\n\t" \
  "ds_read_b128 v[208:211], v244 offset:0\n\t" \
  "ds_read_b128 v[212:215], v244 offset:1024\n\t" \
  "ds_read_b128 v[236:239], v244 offset:2048\n\t" \
  "ds_read_b128 v[240:243], v244 offset:3072\n\t" \
  "v_mov_b64 v[192:193], 0\n\t" \
  "v_mov_b64 v[194:195], 0\n\t" \
  "v_mov_b64 v[196:197], 0\n\t" \
  "v_mov_b64 v[198:199], 0\n\t" \
  "s_movk_i32 s92, 0x2000\n\t" \
  ".Lwt%=:\n\t" \
  "ds_read_b64 v[200:201], v248 offset:30720\n\t" \
  "s_memrealtime s[90:91]\n\t" \
  "s_sub_u32 s92, s92, 1\n\t" \
  "s_waitcnt lgkmcnt(0)\n\t" \
  "v_readfirstlane_b32 s93, v200\n\t" \
  "v_readfirstlane_b32 %[tgt], v201\n\t" \
  "s_cbranch_scc1 .Lgo%=\n\t" \
  "s_cmp_eq_u32 s93, %[step]\n\t" \
  "s_cbranch_scc1 .Lwx%=\n\t" \
  "s_sleep 1\n\t" \
  "s_branch .Lwt%=\n\t" \
  ".Lwx%=:\n\t" \
  "s_lshl_b32 s93, s90, 2\n\t" \
  "s_sub_i32 s93, %[tgt], s93\n\t" \
  "s_cmp_le_i32 s93, 0\n\t" \
  "s_cbranch_scc1 .Lgo%=\n\t" \
  "s_min_i32 s93, s93, 8000\n\t" \
  "s_mov_b32 s94, s93\n\t" \
  ".Lsl%=:\n\t" \
  "s_sub_i32 s94, s94, 18\n\t" \
  "s_cmp_lt_i32 s94, 30\n\t" \
  "s_cbranch_scc1 .Lsx%=\n\t" \
  "s_sleep 1\n\t" \
  "s_branch .Lsl%=\n\t" \
  ".Lsx%=:\n\t" \
  "s_sub_i32 s93, s93, 12\n\t" \
  "s_max_i32 s93, s93, 4\n\t" \
  "s_lshr_b32 s93, s93, 2\n\t" \
  "s_add_u32 s93, s93, s90\n\t" \
  "s_memrealtime s[90:91]\n\t" \
  ".Lpace%=:\n\t" \
  "s_memrealtime s[94:95]\n\t" \
  "s_waitcnt lgkmcnt(1)\n\t" \
  "s_sub_i32 s92, s90, s93\n\t" \
  "s_cmp_ge_i32 s92, 0\n\t" \
  "s_cbranch_scc1 .Le%=\n\t" \
  "s_memrealtime s[90:91]\n\t" \
  "s_waitcnt lgkmcnt(1)\n\t" \
  "s_sub_i32 s92, s94, s93\n\t" \
  "s_cmp_lt_i32 s92, 0\n\t" \
  "s_cbranch_scc1 .Lpace%=\n\t" \
  "s_nop 4\n\t" \
  ".Le%=:\n\t" \
  "s_add_u32 s92, s92, s93\n\t" \
  "s_lshl_b32 s92, s92, 2\n\t" \
  "s_sub_i32 s92, %[tgt], s92\n\t" \
  "s_max_i32 s92, s92, 0\n\t" \
  "s_bitcmp0_b32 s92, 3\n\t" \
  "s_cbranch_scc1 .Lf3%=\n\t" \
  "s_nop 15\n\t" \
  ".Lf3%=:\n\t" \
  "s_bitcmp0_b32 s92, 2\n\t" \
  "s_cbranch_scc1 .Lf2%=\n\t" \
  "s_nop 10\n\t" \
  ".Lf2%=:\n\t" \
  ".Lgo%=:\n\t" \
  "s_mov_b32 s92, 0\n\t" \
  ".Lpoll%=:\n\t" \
  "buffer_load_dwordx4 v[200:203], v248, %[rs], %[som] offen sc1\n\t" \
  "buffer_load_dwordx4 v[204:207], v248, %[rs], %[som2] offen sc1\n\t" \
  "s_waitcnt vmcnt(1) lgkmcnt(0)\n\t" \
  "ds_write_b128 v249, v[200:203]\n\t" \
  "v_smfmac_f32_16x16x64_bf16 v[192:195], v[200:203], v[208:215], v251\n\t" \
  "v_max3_u32 v244, v200, v201, v202\n\t" \
  "v_smfmac_f32_16x16x64_bf16 v[196:199], v[200:203], v[236:243], v251\n\t" \
  "s_waitcnt vmcnt(0)\n\t" \
  "ds_write_b128 v249, v[204:207] offset:8192\n\t" \
  "v_smfmac_f32_16x16x64_bf16 v[192:195], v[204:207], v[216:223], v251\n\t" \
  "v_max3_u32 v244, v244, v203, v204\n\t" \
  "v_max3_u32 v244, v244, v205, v206\n\t" \
  "v_smfmac_f32_16x16x64_bf16 v[196:199], v[204:207], v[224:231], v251\n\t" \
  "v_max_u32 v244, v244, v207\n\t" \
  "v_cmp_le_u32 vcc, %[sent], v244\n\t" \
  "s_cbranch_vccnz .Lslow%=\n\t" \
  ".Lok%=:\n\t" \
  "ds_read_b128 v[216:219], v250 offset:32768\n\t" \
  "ds_read_b128 v[220:223], v250 offset:33792\n\t" \
  "ds_read_b128 v[224:227], v250 offset:34816\n\t" \
  "ds_read_b128 v[228:231], v250 offset:35840\n\t" \
  "s_waitcnt lgkmcnt(4)\n\t" \
  "s_barrier\n\t" \
  "ds_read_b128 v[208:211], v248 offset:1024\n\t" \
  "ds_read_b128 v[212:215], v248 offset:2048\n\t" \
  "ds_read_b128 v[236:239], v248 offset:3072\n\t" \
  "ds_read_b128 v[240:243], v248 offset:4096\n\t" \
  "s_cmp_eq_u32 %[step], 63\n\t" \
  "s_cbranch_scc1 .Lls%=\n\t" \
  "s_waitcnt lgkmcnt(3)\n\t" \
  "ds_read_b128 v[244:247], v248 offset:13312\n\t" \
  "v_smfmac_f32_16x16x64_bf16 v[192:195], v[208:211], v[216:223], v251\n\t" \
  "ds_read_b128 v[216:219], v250 offset:0\n\t" \
  "v_smfmac_f32_16x16x64_bf16 v[196:199], v[208:211], v[224:231], v251\n\t" \
  "s_waitcnt lgkmcnt(4)\n\t" \
  "ds_read_b128 v[220:223], v250 offset:1024\n\t" \
  "v_smfmac_f32_16x16x64_bf16 v[192:195], v[212:215], v[0:7], v251\n\t" \
  "ds_read_b128 v[224:227], v250 offset:2048\n\t" \
  "v_smfmac_f32_16x16x64_bf16 v[196:199], v[212:215], v[8:15], v251\n\t" \
  "s_waitcnt lgkmcnt(5)\n\t" \
  "ds_read_b128 v[228:231], v250 offset:3072\n\t" \
  "v_smfmac_f32_16x16x64_bf16 v[192:195], v[236:239], v[16:23], v251\n\t" \
  "ds_read_b128 v[200:203], v248 offset:5120\n\t" \
  "v_smfmac_f32_16x16x64_bf16 v[196:199], v[236:239], v[24:31], v251\n\t" \
  "s_waitcnt lgkmcnt(6)\n\t" \
  "ds_read_b128 v[204:207], v248 offset:6144\n\t" \
  "v_smfmac_f32_16x16x64_bf16 v[192:195], v[240:243], v[32:39], v251\n\t" \
  "ds_read_b128 v[208:211], v248 offset:7168\n\t" \
  "v_smfmac_f32_16x16x64_bf16 v[196:199], v[240:243], v[40:47], v251\n\t" \
  "s_waitcnt lgkmcnt(5)\n\t" \
  "ds_read_b128 v[212:215], v248 offset:9216\n\t" \
  "v_smfmac_f32_16x16x64_bf16 v[192:195], v[244:247], v[216:223], v251\n\t" \
  "s_waitcnt lgkmcnt(4)\n\t" \
  "v_smfmac_f32_16x16x64_bf16 v[196:199], v[244:247], v[224:231], v251\n\t" \
  "s_waitcnt lgkmcnt(3)\n\t" \
  "ds_read_b128 v[236:239], v248 offset:10240\n\t" \
  "v_smfmac_f32_16x16x64_bf16 v[192:195], v[200:203], v[48:55], v251\n\t" \
  "ds_read_b128 v[240:243], v248 offset:11264\n\t" \
  "v_smfmac_f32_16x16x64_bf16 v[196:199], v[200:203], v[56:63], v251\n\t" \
  "s_waitcnt lgkmcnt(4)\n\t" \
  "v_smfmac_f32_16x16x64_bf16 v[192:195], v[204:207], v[64:71], v251\n\t" \
  "ds_read_b128 v[244:247], v248 offset:12288\n\t" \
  "v_smfmac_f32_16x16x64_bf16 v[196:199], v[204:207], v[72:79], v251\n\t" \
  "s_waitcnt lgkmcnt(4)\n\t" \
  "v_smfmac_f32_16x16x64_bf16 v[192:195], v[208:211], v[80:87], v251\n\t" \
  "ds_read_b128 v[200:203], v248 offset:14336\n\t" \
  "v_smfmac_f32_16x16x64_bf16 v[196:199], v[208:211], v[88:95], v251\n\t" \
  "s_waitcnt lgkmcnt(4)\n\t" \
  "v_smfmac_f32_16x16x64_bf16 v[192:195], v[212:215], v[96:103], v251\n\t" \
  "ds_read_b128 v[204:207], v248 offset:15360\n\t" \
  "v_smfmac_f32_16x16x64_bf16 v[196:199], v[212:215], v[104:111], v251\n\t" \
  "s_waitcnt lgkmcnt(3)\n\t" \
  "v_smfmac_f32_16x16x64_bf16 v[192:195], v[236:239], v[112:119], v251\n\t" \
  "v_smfmac_f32_16x16x64_bf16 v[196:199], v[236:239], v[120:127], v251\n\t" \
  "v_smfmac_f32_16x16x64_bf16 v[192:195], v[240:243], v[128:135], v251\n\t" \
  "v_smfmac_f32_16x16x64_bf16 v[196:199], v[240:243], v[136:143], v251\n\t" \
  "s_waitcnt lgkmcnt(1)\n\t" \
  "v_smfmac_f32_16x16x64_bf16 v[192:195], v[244:247], v[144:151], v251\n\t" \
  "v_smfmac_f32_16x16x64_bf16 v[196:199], v[244:247], v[152:159], v251\n\t" \
  "v_smfmac_f32_16x16x64_bf16 v[192:195], v[200:203], v[160:167], v251\n\t" \
  "v_smfmac_f32_16x16x64_bf16 v[196:199], v[200:203], v[168:175], v251\n\t" \
  "s_waitcnt lgkmcnt(0)\n\t" \
  "v_smfmac_f32_16x16x64_bf16 v[192:195], v[204:207], v[176:183], v251\n\t" \
  "v_smfmac_f32_16x16x64_bf16 v[196:199], v[204:207], v[184:191], v251\n\t" \
  "ds_read_b128 v[200:203], v248 offset:16384\n\t" \
  "ds_read_b128 v[204:207], v248 offset:20480\n\t" \
  "v_mov_b32 v250, v232\n\t" \
  "v_mov_b32 v249, v233\n\t" \
  "s_nop 2\n\t" \
  "v_add_f32 v192, v192, v194\n\t" \
  "v_add_f32 v193, v193, v195\n\t" \
  "v_add_f32 v196, v196, v198\n\t" \
  "v_add_f32 v197, v197, v199\n\t" \
  "v_cvt_pk_bf16_f32 v192, v192, v193\n\t" \
  "v_cvt_pk_bf16_f32 v196, v196, v197\n\t" \
  "ds_write2_b32 v252, v192, v196 offset1:64\n\t" \
  "v_mov_b32 v251, v234\n\t" \
  "ds_read_b64_tr_b16 v[232:233], v250 offset:0\n\t" \
  "ds_read_b64_tr_b16 v[234:235], v250 offset:64\n\t" \
  "v_mov_b32 v212, %[step]\n\t" \
  "s_mov_b64 exec, 1\n\t" \
  "ds_write_b32 v248, v212 offset:28672\n\t" \
  "s_mov_b64 exec, -1\n\t" \
  "buffer_load_dwordx4 v[192:195], v248, %[rt], %[svt] offen offset:0 sc1\n\t" \
  "buffer_load_dwordx4 v[196:199], v248, %[rt], %[svt] offen offset:1024 sc1\n\t" \
  "s_waitcnt lgkmcnt(1)\n\t" \
  "v_mfma_f32_16x16x32_bf16 v[200:203], v[200:203], v[232:235], 0\n\t" \
  "v_mfma_f32_16x16x32_bf16 v[204:207], v[204:207], v[232:235], 0\n\t" \
  "s_nop 7\n\t" \
  "v_cndmask_b32 v216, v200, v204, %[nh]\n\t" \
  "v_cndmask_b32 v217, v201, v205, %[nh]\n\t" \
  "v_cndmask_b32 v218, v202, v206, %[nh]\n\t" \
  "v_cndmask_b32 v219, v203, v207, %[nh]\n\t" \
  "ds_write_b128 v248, v[216:219] offset:16384\n\t" \
  "buffer_load_dword v252, v249, %[rs], %[sv] offen offset:0 sc1\n\t" \
  "buffer_load_dword v253, v249, %[rs], %[sv] offen offset:256 sc1\n\t" \
  "ds_read_b32 v212, v248 offset:28672\n\t" \
  "ds_read_b64_tr_b16 v[236:237], v250 offset:512\n\t" \
  "ds_read_b64_tr_b16 v[238:239], v250 offset:576\n\t" \
  "ds_read_b64_tr_b16 v[240:241], v250 offset:1024\n\t" \
  "ds_read_b64_tr_b16 v[242:243], v250 offset:1088\n\t" \
  "ds_read_b64_tr_b16 v[244:245], v250 offset:1536\n\t" \
  "ds_read_b64_tr_b16 v[246:247], v250 offset:1600\n\t" \
  "s_setprio 3\n\t" \
  "s_waitcnt lgkmcnt(0)\n\t" \
  "v_cmp_ne_u32 vcc, %[step], v212\n\t" \
  "s_barrier\n\t" \
  "s_and_b32 s93, vcc_lo, 0xf\n\t" \
  "s_cbranch_scc1 .Lre%=\n\t" \
  ".Lrb%=:\n\t" \
  "ds_read_b64_tr_b16 v[200:201], v250 offset:2048\n\t" \
  "ds_read_b64_tr_b16 v[202:203], v250 offset:2112\n\t" \
  "ds_read_b64_tr_b16 v[204:205], v250 offset:2560\n\t" \
  "ds_read_b64_tr_b16 v[206:207], v250 offset:2624\n\t" \
  "ds_read_b128 v[220:223], v248 offset:17408\n\t" \
  "ds_read_b128 v[224:227], v248 offset:18432\n\t" \
  "ds_read_b128 v[228:231], v248 offset:19456\n\t" \
  "ds_read_b64_tr_b16 v[208:209], v250 offset:3072\n\t" \
  "ds_read_b64_tr_b16 v[210:211], v250 offset:3136\n\t" \
  "ds_read_b64_tr_b16 v[212:213], v250 offset:3584\n\t" \
  "ds_read_b64_tr_b16 v[214:215], v250 offset:3648\n\t" \
  "s_waitcnt lgkmcnt(9)\n\t" \
  "v_mfma_f32_16x16x32_bf16 v[200:203], v[200:203], v[232:235], 0\n\t" \
  "s_waitcnt lgkmcnt(7)\n\t" \
  "v_mfma_f32_16x16x32_bf16 v[200:203], v[204:207], v[236:239], v[200:203]\n\t" \
  "s_waitcnt lgkmcnt(6)\n\t" \
  "v_pk_add_f32 v[216:217], v[216:217], v[220:221]\n\t" \
  "v_pk_add_f32 v[218:219], v[218:219], v[222:223]\n\t" \
  "s_waitcnt lgkmcnt(4)\n\t" \
  "v_pk_add_f32 v[224:225], v[224:225], v[228:229]\n\t" \
  "v_pk_add_f32 v[226:227], v[226:227], v[230:231]\n\t" \
  "v_pk_add_f32 v[216:217], v[216:217], v[224:225]\n\t" \
  "v_pk_add_f32 v[218:219], v[218:219], v[226:227]\n\t" \
  "s_waitcnt lgkmcnt(2)\n\t" \
  "v_mfma_f32_16x16x32_bf16 v[200:203], v[208:211], v[240:243], v[200:203]\n\t" \
  "v_max3_f32 v224, v216, v217, v218\n\t" \
  "v_max_f32 v224, v224, v219\n\t" \
  "s_waitcnt lgkmcnt(0)\n\t" \
  "v_mfma_f32_16x16x32_bf16 v[200:203], v[212:215], v[244:247], v[200:203]\n\t" \
  "v_max_f32_dpp v225, v224, v224 row_ror:8 row_mask:0xf bank_mask:0xf\n\t" \
  "v_max_f32_dpp v226, v224, v224 row_ror:8 row_mask:0xf bank_mask:0xf\n\t" \
  "s_nop 1\n\t" \
  "v_permlane32_swap_b32 v225, v226\n\t" \
  "v_max_f32 v231, v225, v226\n\t" \
  "v_mov_b32 v220, 0x3f803f80\n\t" \
  "v_mov_b32 v221, 0x3f803f80\n\t" \
  "v_mov_b64 v[222:223], v[220:221]\n\t" \
  "v_mov_b32 v233, 0\n\t" \
  "v_mov_b32 v234, 0\n\t" \
  "v_max3_f32 v204, v200, v201, v231\n\t" \
  "v_max3_f32 v205, v204, v202, v203\n\t" \
  "v_max3_f32 v206, v204, v202, v203\n\t" \
  "v_cndmask_b32 v200, v200, v202, %[up]\n\t" \
  "v_cndmask_b32 v201, v201, v203, %[up]\n\t" \
  "v_permlane16_swap_b32 v205, v206\n\t" \
  "v_max_f32 v204, v205, v206\n\t" \
  "v_mul_f32 v204, %[negc], v204\n\t" \
  "v_fma_f32 v200, v200, %[c], v204\n\t" \
  "v_fma_f32 v201, v201, %[c], v204\n\t" \
  "v_exp_f32 v200, v200\n\t" \
  "v_fma_f32 v216, v216, %[c], v204\n\t" \
  "v_exp_f32 v201, v201\n\t" \
  "v_fma_f32 v217, v217, %[c], v204\n\t" \
  "v_exp_f32 v216, v216\n\t" \
  "v_fma_f32 v218, v218, %[c], v204\n\t" \
  "v_cvt_pk_bf16_f32 v232, v200, v201\n\t" \
  "v_cvt_pk_bf16_f32 v235, v200, v201\n\t" \
  "v_exp_f32 v217, v217\n\t" \
  "v_fma_f32 v219, v219, %[c], v204\n\t" \
  "v_mfma_f32_16x16x16_bf16 v[212:215], v[220:221], v[232:233], 0\n\t" \
  "v_exp_f32 v218, v218\n\t" \
  "s_waitcnt vmcnt(0)\n\t" \
  "v_mfma_f32_16x16x16_bf16 v[224:227], v[252:253], v[232:233], 0\n\t" \
  "v_cvt_pk_bf16_f32 v210, v216, v217\n\t" \
  "v_cvt_pk_bf16_f32 v208, v216, v217\n\t" \
  "v_exp_f32 v219, v219\n\t" \
  "v_mfma_f32_16x16x16_bf16 v[228:231], v[252:253], v[234:235], 0\n\t" \
  "v_cvt_pk_bf16_f32 v211, v218, v219\n\t" \
  "v_cvt_pk_bf16_f32 v209, v218, v219\n\t" \
  "v_mov_b32_dpp v208, v210 row_ror:8 row_mask:0xf bank_mask:0xc\n\t" \
  "v_mov_b32_dpp v210, v210 row_ror:8 row_mask:0xf bank_mask:0x3\n\t" \
  "v_mov_b32_dpp v209, v211 row_ror:8 row_mask:0xf bank_mask:0xc\n\t" \
  "v_mov_b32_dpp v211, v211 row_ror:8 row_mask:0xf bank_mask:0x3\n\t" \
  "v_max_u32 v204, v252, v253\n\t" \
  "v_cmp_le_u32 vcc, %[sent], v204\n\t" \
  "v_mfma_f32_16x16x32_bf16 v[212:215], v[220:223], v[208:211], v[212:215]\n\t" \
  ".Lpv%=:\n\t" \
  "v_mfma_f32_16x16x32_bf16 v[224:227], v[192:195], v[208:211], v[224:227]\n\t" \
  "v_mfma_f32_16x16x32_bf16 v[228:231], v[196:199], v[208:211], v[228:231]\n\t" \
  "s_cbranch_vccnz .Lvs%=\n\t" \
  ".Lvok%=:\n\t" \
  "s_nop 4\n\t" \
  "v_rcp_f32 v204, v212\n\t" \
  "v_cndmask_b32 v224, v224, v226, %[nh]\n\t" \
  "v_cndmask_b32 v225, v225, v227, %[nh]\n\t" \
  "v_cndmask_b32 v228, v228, v230, %[nh]\n\t" \
  "v_cndmask_b32 v229, v229, v231, %[nh]\n\t" \
  "v_pk_mul_f32 v[224:225], v[224:225], v[204:205] op_sel_hi:[1,0]\n\t" \
  "v_pk_mul_f32 v[228:229], v[228:229], v[204:205] op_sel_hi:[1,0]\n\t" \
  "v_cvt_pk_bf16_f32 v224, v224, v225\n\t" \
  "v_cvt_pk_bf16_f32 v225, v228, v229\n\t" \
  "buffer_store_dwordx2 v[224:225], v251, %[rs], %[so] offen sc1\n\t" \
  "s_memtime s[90:91]\n\t" \
  "s_setprio 0\n\t" \
  "s_waitcnt lgkmcnt(0)\n\t" \
  "s_mov_b32 %[tout], s90\n\t" \
  "s_mov_b32 %[spins], s92\n\t" \
  "s_branch .Lend%=\n\t" \
  ".Lls%=:\n\t" \
  "s_waitcnt lgkmcnt(0)\n\t" \
  "s_barrier\n\t" \
  "s_mov_b32 %[spins], 0\n\t" \
  "s_branch .Lend%=\n\t" \
  ".Lre%=:\n\t" \
  "ds_read_b64_tr_b16 v[236:237], v250 offset:512\n\t" \
  "ds_read_b64_tr_b16 v[238:239], v250 offset:576\n\t" \
  "ds_read_b64_tr_b16 v[240:241], v250 offset:1024\n\t" \
  "ds_read_b64_tr_b16 v[242:243], v250 offset:1088\n\t" \
  "ds_read_b64_tr_b16 v[244:245], v250 offset:1536\n\t" \
  "ds_read_b64_tr_b16 v[246:247], v250 offset:1600\n\t" \
  "s_waitcnt lgkmcnt(0)\n\t" \
  "s_branch .Lrb%=\n\t" \
  ".Lvs%=:\n\t" \
  "v_cmp_eq_u32 vcc, %[sent], v252\n\t" \
  "v_cmp_eq_u32 s[94:95], %[sent], v253\n\t" \
  "s_or_b64 s[94:95], s[94:95], vcc\n\t" \
  "s_cbranch_scc0 .Lvok%=\n\t" \
  "s_add_u32 s92, s92, 1\n\t" \
  "s_sleep 2\n\t" \
  "s_and_b32 s93, s92, 63\n\t" \
  "s_cbranch_scc1 .Lvr%=\n\t" \
  "buffer_load_dword v204, off, %[rs], %[sab] sc1\n\t" \
  "s_waitcnt vmcnt(0)\n\t" \
  "v_readfirstlane_b32 s93, v204\n\t" \
  "s_nop 3\n\t" \
  "s_cmp_eq_u32 s93, 1\n\t" \
  "s_cbranch_scc1 .Lvok%=\n\t" \
  "s_cmp_lt_u32 s92, 0x80000\n\t" \
  "s_cbranch_scc1 .Lvr%=\n\t" \
  "v_mov_b32 v204, 1\n\t" \
  "buffer_store_dword v204, off, %[rs], %[sab] sc1\n\t" \
  "s_branch .Lvok%=\n\t" \
  ".Lvr%=:\n\t" \
  "buffer_load_dword v252, v249, %[rs], %[sv] offen offset:0 sc1\n\t" \
  "buffer_load_dword v253, v249, %[rs], %[sv] offen offset:256 sc1\n\t" \
  "s_waitcnt vmcnt(0)\n\t" \
  "v_mfma_f32_16x16x16_bf16 v[224:227], v[252:253], v[232:233], 0\n\t" \
  "v_mfma_f32_16x16x16_bf16 v[228:231], v[252:253], v[234:235], 0\n\t" \
  "v_max_u32 v204, v252, v253\n\t" \
  "v_cmp_le_u32 vcc, %[sent], v204\n\t" \
  "s_nop 4\n\t" \
  "s_branch .Lpv%=\n\t" \
  "s_branch .Lend%=\n\t" \
  ".Lslow%=:\n\t" \
  "s_mov_b64 s[94:95], 0\n\t" \
  "v_cmp_eq_u32 vcc, %[sent], v200\n\t" \
  "s_or_b64 s[94:95], s[94:95], vcc\n\t" \
  "v_cmp_eq_u32 vcc, %[sent], v201\n\t" \
  "s_or_b64 s[94:95], s[94:95], vcc\n\t" \
  "v_cmp_eq_u32 vcc, %[sent], v202\n\t" \
  "s_or_b64 s[94:95], s[94:95], vcc\n\t" \
  "v_cmp_eq_u32 vcc, %[sent], v203\n\t" \
  "s_or_b64 s[94:95], s[94:95], vcc\n\t" \
  "v_cmp_eq_u32 vcc, %[sent], v204\n\t" \
  "s_or_b64 s[94:95], s[94:95], vcc\n\t" \
  "v_cmp_eq_u32 vcc, %[sent], v205\n\t" \
  "s_or_b64 s[94:95], s[94:95], vcc\n\t" \
  "v_cmp_eq_u32 vcc, %[sent], v206\n\t" \
  "s_or_b64 s[94:95], s[94:95], vcc\n\t" \
  "v_cmp_eq_u32 vcc, %[sent], v207\n\t" \
  "s_or_b64 s[94:95], s[94:95], vcc\n\t" \
  "s_cmp_eq_u64 s[94:95], 0\n\t" \
  "s_cbranch_scc1 .Lok%=\n\t" \
  "v_mov_b64 v[192:193], 0\n\t" \
  "v_mov_b64 v[194:195], 0\n\t" \
  "v_mov_b64 v[196:197], 0\n\t" \
  "v_mov_b64 v[198:199], 0\n\t" \
  "s_add_u32 s92, s92, 1\n\t" \
  "s_cmp_eq_u32 s92, 1\n\t" \
  "s_cbranch_scc1 .Lpoll%=\n\t" \
  "s_sleep 2\n\t" \
  "s_and_b32 s93, s92, 63\n\t" \
  "s_cmp_lg_u32 s93, 0\n\t" \
  "s_cbranch_scc1 .Lpoll%=\n\t" \
  "v_mov_b32 v245, 0\n\t" \
  "buffer_load_dword v244, v245, %[rs], %[sab] offen sc1\n\t" \
  "s_waitcnt vmcnt(0)\n\t" \
  "v_readfirstlane_b32 s93, v244\n\t" \
  "s_nop 3\n\t" \
  "s_cmp_eq_u32 s93, 1\n\t" \
  "s_cbranch_scc1 .Lok%=\n\t" \
  "s_cmp_lt_u32 s92, 0x80000\n\t" \
  "s_cbranch_scc1 .Lpoll%=\n\t" \
  "v_mov_b32 v244, 1\n\t" \
  "buffer_store_dword v244, v245, %[rs], %[sab] offen sc1\n\t" \
  "s_branch .Lok%=\n\t" \
  ".Lend%=:\n\t"
#define QK_ASM_1 \
  "s_waitcnt lgkmcnt(0)\n\t" \
  "v_subrev_u32 v244, 0x8000, v250\n\t" \
  "ds_read_b128 v[216:219], v248 offset:36864\n\t" \
  "ds_read_b128 v[220:223], v248 offset:37888\n\t" \
  "ds_read_b128 v[224:227], v248 offset:38912\n\t" \
  "ds_read_b128 v[228:231], v248 offset:39936\n\t" \
  "ds_read_b128 v[208:211], v244 offset:0\n\t" \
  "ds_read_b128 v[212:215], v244 offset:1024\n\t" \
  "ds_read_b128 v[236:239], v244 offset:2048\n\t" \
  "ds_read_b128 v[240:243], v244 offset:3072\n\t" \
  "v_mov_b64 v[192:193], 0\n\t" \
  "v_mov_b64 v[194:195], 0\n\t" \
  "v_mov_b64 v[196:197], 0\n\t" \
  "v_mov_b64 v[198:199], 0\n\t" \
  "s_movk_i32 s92, 0x2000\n\t" \
  ".Lwt%=:\n\t" \
  "ds_read_b64 v[200:201], v248 offset:30720\n\t" \
  "s_memrealtime s[90:91]\n\t" \
  "s_sub_u32 s92, s92, 1\n\t" \
  "s_waitcnt lgkmcnt(0)\n\t" \
  "v_readfirstlane_b32 s93, v200\n\t" \
  "v_readfirstlane_b32 %[tgt], v201\n\t" \
  "s_cbranch_scc1 .Lgo%=\n\t" \
  "s_cmp_eq_u32 s93, %[step]\n\t" \
  "s_cbranch_scc1 .Lwx%=\n\t" \
  "s_sleep 1\n\t" \
  "s_branch .Lwt%=\n\t" \
  ".Lwx%=:\n\t" \
  "s_lshl_b32 s93, s90, 2\n\t" \
  "s_sub_i32 s93, %[tgt], s93\n\t" \
  "s_cmp_le_i32 s93, 0\n\t" \
  "s_cbranch_scc1 .Lgo%=\n\t" \
  "s_min_i32 s93, s93, 8000\n\t" \
  "s_mov_b32 s94, s93\n\t" \
  ".Lsl%=:\n\t" \
  "s_sub_i32 s94, s94, 18\n\t" \
  "s_cmp_lt_i32 s94, 30\n\t" \
  "s_cbranch_scc1 .Lsx%=\n\t" \
  "s_sleep 1\n\t" \
  "s_branch .Lsl%=\n\t" \
  ".Lsx%=:\n\t" \
  "s_sub_i32 s93, s93, 12\n\t" \
  "s_max_i32 s93, s93, 4\n\t" \
  "s_lshr_b32 s93, s93, 2\n\t" \
  "s_add_u32 s93, s93, s90\n\t" \
  "s_memrealtime s[90:91]\n\t" \
  ".Lpace%=:\n\t" \
  "s_memrealtime s[94:95]\n\t" \
  "s_waitcnt lgkmcnt(1)\n\t" \
  "s_sub_i32 s92, s90, s93\n\t" \
  "s_cmp_ge_i32 s92, 0\n\t" \
  "s_cbranch_scc1 .Le%=\n\t" \
  "s_memrealtime s[90:91]\n\t" \
  "s_waitcnt lgkmcnt(1)\n\t" \
  "s_sub_i32 s92, s94, s93\n\t" \
  "s_cmp_lt_i32 s92, 0\n\t" \
  "s_cbranch_scc1 .Lpace%=\n\t" \
  "s_nop 4\n\t" \
  ".Le%=:\n\t" \
  "s_add_u32 s92, s92, s93\n\t" \
  "s_lshl_b32 s92, s92, 2\n\t" \
  "s_sub_i32 s92, %[tgt], s92\n\t" \
  "s_max_i32 s92, s92, 0\n\t" \
  "s_bitcmp0_b32 s92, 3\n\t" \
  "s_cbranch_scc1 .Lf3%=\n\t" \
  "s_nop 15\n\t" \
  ".Lf3%=:\n\t" \
  "s_bitcmp0_b32 s92, 2\n\t" \
  "s_cbranch_scc1 .Lf2%=\n\t" \
  "s_nop 10\n\t" \
  ".Lf2%=:\n\t" \
  ".Lgo%=:\n\t" \
  "s_mov_b32 s92, 0\n\t" \
  ".Lpoll%=:\n\t" \
  "buffer_load_dwordx4 v[200:203], v248, %[rs], %[som] offen sc1\n\t" \
  "buffer_load_dwordx4 v[204:207], v248, %[rs], %[som2] offen sc1\n\t" \
  "s_waitcnt vmcnt(1) lgkmcnt(0)\n\t" \
  "ds_write_b128 v249, v[200:203]\n\t" \
  "v_smfmac_f32_16x16x64_bf16 v[192:195], v[200:203], v[208:215], v251\n\t" \
  "v_max3_u32 v244, v200, v201, v202\n\t" \
  "v_smfmac_f32_16x16x64_bf16 v[196:199], v[200:203], v[236:243], v251\n\t" \
  "s_waitcnt vmcnt(0)\n\t" \
  "ds_write_b128 v249, v[204:207] offset:8192\n\t" \
  "v_smfmac_f32_16x16x64_bf16 v[192:195], v[204:207], v[216:223], v251\n\t" \
  "v_max3_u32 v244, v244, v203, v204\n\t" \
  "v_max3_u32 v244, v244, v205, v206\n\t" \
  "v_smfmac_f32_16x16x64_bf16 v[196:199], v[204:207], v[224:231], v251\n\t" \
  "v_max_u32 v244, v244, v207\n\t" \
  "v_cmp_le_u32 vcc, %[sent], v244\n\t" \
  "s_cbranch_vccnz .Lslow%=\n\t" \
  ".Lok%=:\n\t" \
  "ds_read_b128 v[216:219], v250 offset:32768\n\t" \
  "ds_read_b128 v[220:223], v250 offset:33792\n\t" \
  "ds_read_b128 v[224:227], v250 offset:34816\n\t" \
  "ds_read_b128 v[228:231], v250 offset:35840\n\t" \
  "s_waitcnt lgkmcnt(4)\n\t" \
  "s_barrier\n\t" \
  "ds_read_b128 v[208:211], v248 offset:0\n\t" \
  "ds_read_b128 v[212:215], v248 offset:3072\n\t" \
  "ds_read_b128 v[236:239], v248 offset:4096\n\t" \
  "ds_read_b128 v[240:243], v248 offset:5120\n\t" \
  "s_cmp_eq_u32 %[step], 63\n\t" \
  "s_cbranch_scc1 .Lls%=\n\t" \
  "s_waitcnt lgkmcnt(3)\n\t" \
  "ds_read_b128 v[244:247], v248 offset:12288\n\t" \
  "v_smfmac_f32_16x16x64_bf16 v[192:195], v[208:211], v[216:223], v251\n\t" \
  "ds_read_b128 v[216:219], v250 offset:0\n\t" \
  "v_smfmac_f32_16x16x64_bf16 v[196:199], v[208:211], v[224:231], v251\n\t" \
  "s_waitcnt lgkmcnt(4)\n\t" \
  "ds_read_b128 v[220:223], v250 offset:1024\n\t" \
  "v_smfmac_f32_16x16x64_bf16 v[192:195], v[212:215], v[0:7], v251\n\t" \
  "ds_read_b128 v[224:227], v250 offset:2048\n\t" \
  "v_smfmac_f32_16x16x64_bf16 v[196:199], v[212:215], v[8:15], v251\n\t" \
  "s_waitcnt lgkmcnt(5)\n\t" \
  "ds_read_b128 v[228:231], v250 offset:3072\n\t" \
  "v_smfmac_f32_16x16x64_bf16 v[192:195], v[236:239], v[16:23], v251\n\t" \
  "ds_read_b128 v[200:203], v248 offset:6144\n\t" \
  "v_smfmac_f32_16x16x64_bf16 v[196:199], v[236:239], v[24:31], v251\n\t" \
  "s_waitcnt lgkmcnt(6)\n\t" \
  "ds_read_b128 v[204:207], v248 offset:7168\n\t" \
  "v_smfmac_f32_16x16x64_bf16 v[192:195], v[240:243], v[32:39], v251\n\t" \
  "ds_read_b128 v[208:211], v248 offset:8192\n\t" \
  "v_smfmac_f32_16x16x64_bf16 v[196:199], v[240:243], v[40:47], v251\n\t" \
  "s_waitcnt lgkmcnt(5)\n\t" \
  "ds_read_b128 v[212:215], v248 offset:10240\n\t" \
  "v_smfmac_f32_16x16x64_bf16 v[192:195], v[244:247], v[216:223], v251\n\t" \
  "s_waitcnt lgkmcnt(4)\n\t" \
  "v_smfmac_f32_16x16x64_bf16 v[196:199], v[244:247], v[224:231], v251\n\t" \
  "s_waitcnt lgkmcnt(3)\n\t" \
  "ds_read_b128 v[236:239], v248 offset:11264\n\t" \
  "v_smfmac_f32_16x16x64_bf16 v[192:195], v[200:203], v[48:55], v251\n\t" \
  "ds_read_b128 v[240:243], v248 offset:13312\n\t" \
  "v_smfmac_f32_16x16x64_bf16 v[196:199], v[200:203], v[56:63], v251\n\t" \
  "s_waitcnt lgkmcnt(4)\n\t" \
  "v_smfmac_f32_16x16x64_bf16 v[192:195], v[204:207], v[64:71], v251\n\t" \
  "ds_read_b128 v[244:247], v248 offset:14336\n\t" \
  "v_smfmac_f32_16x16x64_bf16 v[196:199], v[204:207], v[72:79], v251\n\t" \
  "s_waitcnt lgkmcnt(4)\n\t" \
  "v_smfmac_f32_16x16x64_bf16 v[192:195], v[208:211], v[80:87], v251\n\t" \
  "ds_read_b128 v[200:203], v248 offset:15360\n\t" \
  "v_smfmac_f32_16x16x64_bf16 v[196:199], v[208:211], v[88:95], v251\n\t" \
  "s_waitcnt lgkmcnt(4)\n\t" \
  "v_smfmac_f32_16x16x64_bf16 v[192:195], v[212:215], v[96:103], v251\n\t" \
  "ds_read_b128 v[204:207], v248 offset:2048\n\t" \
  "v_smfmac_f32_16x16x64_bf16 v[196:199], v[212:215], v[104:111], v251\n\t" \
  "s_waitcnt lgkmcnt(3)\n\t" \
  "v_smfmac_f32_16x16x64_bf16 v[192:195], v[236:239], v[112:119], v251\n\t" \
  "v_smfmac_f32_16x16x64_bf16 v[196:199], v[236:239], v[120:127], v251\n\t" \
  "v_smfmac_f32_16x16x64_bf16 v[192:195], v[240:243], v[128:135], v251\n\t" \
  "v_smfmac_f32_16x16x64_bf16 v[196:199], v[240:243], v[136:143], v251\n\t" \
  "s_waitcnt lgkmcnt(1)\n\t" \
  "v_smfmac_f32_16x16x64_bf16 v[192:195], v[244:247], v[144:151], v251\n\t" \
  "v_smfmac_f32_16x16x64_bf16 v[196:199], v[244:247], v[152:159], v251\n\t" \
  "v_smfmac_f32_16x16x64_bf16 v[192:195], v[200:203], v[160:167], v251\n\t" \
  "v_smfmac_f32_16x16x64_bf16 v[196:199], v[200:203], v[168:175], v251\n\t" \
  "s_waitcnt lgkmcnt(0)\n\t" \
  "v_smfmac_f32_16x16x64_bf16 v[192:195], v[204:207], v[176:183], v251\n\t" \
  "v_smfmac_f32_16x16x64_bf16 v[196:199], v[204:207], v[184:191], v251\n\t" \
  "ds_read_b128 v[200:203], v248 offset:17408\n\t" \
  "ds_read_b128 v[204:207], v248 offset:21504\n\t" \
  "v_mov_b32 v250, v232\n\t" \
  "v_mov_b32 v249, v233\n\t" \
  "s_nop 2\n\t" \
  "v_add_f32 v192, v192, v194\n\t" \
  "v_add_f32 v193, v193, v195\n\t" \
  "v_add_f32 v196, v196, v198\n\t" \
  "v_add_f32 v197, v197, v199\n\t" \
  "v_cvt_pk_bf16_f32 v192, v192, v193\n\t" \
  "v_cvt_pk_bf16_f32 v196, v196, v197\n\t" \
  "ds_write2_b32 v252, v192, v196 offset1:64\n\t" \
  "v_mov_b32 v251, v234\n\t" \
  "ds_read_b64_tr_b16 v[236:237], v250 offset:512\n\t" \
  "ds_read_b64_tr_b16 v[238:239], v250 offset:576\n\t" \
  "v_mov_b32 v212, %[step]\n\t" \
  "s_mov_b64 exec, 1\n\t" \
  "ds_write_b32 v248, v212 offset:28688\n\t" \
  "s_mov_b64 exec, -1\n\t" \
  "buffer_load_dwordx4 v[192:195], v248, %[rt], %[svt] offen offset:0 sc1\n\t" \
  "buffer_load_dwordx4 v[196:199], v248, %[rt], %[svt] offen offset:1024 sc1\n\t" \
  "s_waitcnt lgkmcnt(1)\n\t" \
  "v_mfma_f32_16x16x32_bf16 v[200:203], v[200:203], v[236:239], 0\n\t" \
  "v_mfma_f32_16x16x32_bf16 v[204:207], v[204:207], v[236:239], 0\n\t" \
  "s_nop 7\n\t" \
  "v_cndmask_b32 v216, v200, v204, %[nh]\n\t" \
  "v_cndmask_b32 v217, v201, v205, %[nh]\n\t" \
  "v_cndmask_b32 v218, v202, v206, %[nh]\n\t" \
  "v_cndmask_b32 v219, v203, v207, %[nh]\n\t" \
  "ds_write_b128 v248, v[216:219] offset:17408\n\t" \
  "buffer_load_dword v252, v249, %[rs], %[sv] offen offset:0 sc1\n\t" \
  "buffer_load_dword v253, v249, %[rs], %[sv] offen offset:256 sc1\n\t" \
  "ds_read_b32 v212, v248 offset:28672\n\t" \
  "ds_read_b64_tr_b16 v[240:241], v250 offset:1024\n\t" \
  "ds_read_b64_tr_b16 v[242:243], v250 offset:1088\n\t" \
  "ds_read_b64_tr_b16 v[244:245], v250 offset:1536\n\t" \
  "ds_read_b64_tr_b16 v[246:247], v250 offset:1600\n\t" \
  "ds_read_b64_tr_b16 v[232:233], v250 offset:0\n\t" \
  "ds_read_b64_tr_b16 v[234:235], v250 offset:64\n\t" \
  "s_setprio 3\n\t" \
  "s_waitcnt lgkmcnt(0)\n\t" \
  "v_cmp_ne_u32 vcc, %[step], v212\n\t" \
  "s_barrier\n\t" \
  "s_and_b32 s93, vcc_lo, 0xf\n\t" \
  "s_cbranch_scc1 .Lre%=\n\t" \
  ".Lrb%=:\n\t" \
  "ds_read_b64_tr_b16 v[204:205], v250 offset:2560\n\t" \
  "ds_read_b64_tr_b16 v[206:207], v250 offset:2624\n\t" \
  "ds_read_b64_tr_b16 v[208:209], v250 offset:3072\n\t" \
  "ds_read_b64_tr_b16 v[210:211], v250 offset:3136\n\t" \
  "ds_read_b128 v[220:223], v248 offset:18432\n\t" \
  "ds_read_b128 v[224:227], v248 offset:19456\n\t" \
  "ds_read_b128 v[228:231], v248 offset:16384\n\t" \
  "ds_read_b64_tr_b16 v[212:213], v250 offset:3584\n\t" \
  "ds_read_b64_tr_b16 v[214:215], v250 offset:3648\n\t" \
  "ds_read_b64_tr_b16 v[200:201], v250 offset:2048\n\t" \
  "ds_read_b64_tr_b16 v[202:203], v250 offset:2112\n\t" \
  "s_waitcnt lgkmcnt(9)\n\t" \
  "v_mfma_f32_16x16x32_bf16 v[204:207], v[204:207], v[236:239], 0\n\t" \
  "s_waitcnt lgkmcnt(7)\n\t" \
  "v_mfma_f32_16x16x32_bf16 v[204:207], v[208:211], v[240:243], v[204:207]\n\t" \
  "s_waitcnt lgkmcnt(6)\n\t" \
  "v_pk_add_f32 v[216:217], v[216:217], v[220:221]\n\t" \
  "v_pk_add_f32 v[218:219], v[218:219], v[222:223]\n\t" \
  "s_waitcnt lgkmcnt(4)\n\t" \
  "v_pk_add_f32 v[224:225], v[224:225], v[228:229]\n\t" \
  "v_pk_add_f32 v[226:227], v[226:227], v[230:231]\n\t" \
  "v_pk_add_f32 v[216:217], v[216:217], v[224:225]\n\t" \
  "v_pk_add_f32 v[218:219], v[218:219], v[226:227]\n\t" \
  "s_waitcnt lgkmcnt(2)\n\t" \
  "v_mfma_f32_16x16x32_bf16 v[204:207], v[212:215], v[244:247], v[204:207]\n\t" \
  "v_max3_f32 v224, v216, v217, v218\n\t" \
  "v_max_f32 v224, v224, v219\n\t" \
  "s_waitcnt lgkmcnt(0)\n\t" \
  "v_mfma_f32_16x16x32_bf16 v[204:207], v[200:203], v[232:235], v[204:207]\n\t" \
  "v_max_f32_dpp v225, v224, v224 row_ror:8 row_mask:0xf bank_mask:0xf\n\t" \
  "v_max_f32_dpp v226, v224, v224 row_ror:8 row_mask:0xf bank_mask:0xf\n\t" \
  "s_nop 1\n\t" \
  "v_permlane32_swap_b32 v225, v226\n\t" \
  "v_max_f32 v231, v225, v226\n\t" \
  "v_mov_b32 v220, 0x3f803f80\n\t" \
  "v_mov_b32 v221, 0x3f803f80\n\t" \
  "v_mov_b64 v[222:223], v[220:221]\n\t" \
  "v_mov_b32 v237, 0\n\t" \
  "v_mov_b32 v238, 0\n\t" \
  "v_max3_f32 v208, v204, v205, v231\n\t" \
  "v_max3_f32 v209, v208, v206, v207\n\t" \
  "v_max3_f32 v210, v208, v206, v207\n\t" \
  "v_cndmask_b32 v204, v204, v206, %[up]\n\t" \
  "v_cndmask_b32 v205, v205, v207, %[up]\n\t" \
  "v_permlane16_swap_b32 v209, v210\n\t" \
  "v_max_f32 v208, v209, v210\n\t" \
  "v_mul_f32 v208, %[negc], v208\n\t" \
  "v_fma_f32 v204, v204, %[c], v208\n\t" \
  "v_fma_f32 v205, v205, %[c], v208\n\t" \
  "v_exp_f32 v204, v204\n\t" \
  "v_fma_f32 v216, v216, %[c], v208\n\t" \
  "v_exp_f32 v205, v205\n\t" \
  "v_fma_f32 v217, v217, %[c], v208\n\t" \
  "v_exp_f32 v216, v216\n\t" \
  "v_fma_f32 v218, v218, %[c], v208\n\t" \
  "v_cvt_pk_bf16_f32 v236, v204, v205\n\t" \
  "v_cvt_pk_bf16_f32 v239, v204, v205\n\t" \
  "v_exp_f32 v217, v217\n\t" \
  "v_fma_f32 v219, v219, %[c], v208\n\t" \
  "v_mfma_f32_16x16x16_bf16 v[200:203], v[220:221], v[236:237], 0\n\t" \
  "v_exp_f32 v218, v218\n\t" \
  "s_waitcnt vmcnt(0)\n\t" \
  "v_mfma_f32_16x16x16_bf16 v[224:227], v[252:253], v[236:237], 0\n\t" \
  "v_cvt_pk_bf16_f32 v214, v216, v217\n\t" \
  "v_cvt_pk_bf16_f32 v212, v216, v217\n\t" \
  "v_exp_f32 v219, v219\n\t" \
  "v_mfma_f32_16x16x16_bf16 v[228:231], v[252:253], v[238:239], 0\n\t" \
  "v_cvt_pk_bf16_f32 v215, v218, v219\n\t" \
  "v_cvt_pk_bf16_f32 v213, v218, v219\n\t" \
  "v_mov_b32_dpp v212, v214 row_ror:8 row_mask:0xf bank_mask:0xc\n\t" \
  "v_mov_b32_dpp v214, v214 row_ror:8 row_mask:0xf bank_mask:0x3\n\t" \
  "v_mov_b32_dpp v213, v215 row_ror:8 row_mask:0xf bank_mask:0xc\n\t" \
  "v_mov_b32_dpp v215, v215 row_ror:8 row_mask:0xf bank_mask:0x3\n\t" \
  "v_max_u32 v208, v252, v253\n\t" \
  "v_cmp_le_u32 vcc, %[sent], v208\n\t" \
  "v_mfma_f32_16x16x32_bf16 v[200:203], v[220:223], v[212:215], v[200:203]\n\t" \
  ".Lpv%=:\n\t" \
  "v_mfma_f32_16x16x32_bf16 v[224:227], v[192:195], v[212:215], v[224:227]\n\t" \
  "v_mfma_f32_16x16x32_bf16 v[228:231], v[196:199], v[212:215], v[228:231]\n\t" \
  "s_cbranch_vccnz .Lvs%=\n\t" \
  ".Lvok%=:\n\t" \
  "s_nop 4\n\t" \
  "v_rcp_f32 v208, v200\n\t" \
  "v_cndmask_b32 v224, v224, v226, %[nh]\n\t" \
  "v_cndmask_b32 v225, v225, v227, %[nh]\n\t" \
  "v_cndmask_b32 v228, v228, v230, %[nh]\n\t" \
  "v_cndmask_b32 v229, v229, v231, %[nh]\n\t" \
  "v_pk_mul_f32 v[224:225], v[224:225], v[208:209] op_sel_hi:[1,0]\n\t" \
  "v_pk_mul_f32 v[228:229], v[228:229], v[208:209] op_sel_hi:[1,0]\n\t" \
  "v_cvt_pk_bf16_f32 v224, v224, v225\n\t" \
  "v_cvt_pk_bf16_f32 v225, v228, v229\n\t" \
  "buffer_store_dwordx2 v[224:225], v251, %[rs], %[so] offen sc1\n\t" \
  "s_memtime s[90:91]\n\t" \
  "s_setprio 0\n\t" \
  "s_waitcnt lgkmcnt(0)\n\t" \
  "s_mov_b32 %[tout], s90\n\t" \
  "s_mov_b32 %[spins], s92\n\t" \
  "s_branch .Lend%=\n\t" \
  ".Lls%=:\n\t" \
  "s_waitcnt lgkmcnt(0)\n\t" \
  "s_barrier\n\t" \
  "s_mov_b32 %[spins], 0\n\t" \
  "s_branch .Lend%=\n\t" \
  ".Lre%=:\n\t" \
  "ds_read_b64_tr_b16 v[240:241], v250 offset:1024\n\t" \
  "ds_read_b64_tr_b16 v[242:243], v250 offset:1088\n\t" \
  "ds_read_b64_tr_b16 v[244:245], v250 offset:1536\n\t" \
  "ds_read_b64_tr_b16 v[246:247], v250 offset:1600\n\t" \
  "ds_read_b64_tr_b16 v[232:233], v250 offset:0\n\t" \
  "ds_read_b64_tr_b16 v[234:235], v250 offset:64\n\t" \
  "s_waitcnt lgkmcnt(0)\n\t" \
  "s_branch .Lrb%=\n\t" \
  ".Lvs%=:\n\t" \
  "v_cmp_eq_u32 vcc, %[sent], v252\n\t" \
  "v_cmp_eq_u32 s[94:95], %[sent], v253\n\t" \
  "s_or_b64 s[94:95], s[94:95], vcc\n\t" \
  "s_cbranch_scc0 .Lvok%=\n\t" \
  "s_add_u32 s92, s92, 1\n\t" \
  "s_sleep 2\n\t" \
  "s_and_b32 s93, s92, 63\n\t" \
  "s_cbranch_scc1 .Lvr%=\n\t" \
  "buffer_load_dword v208, off, %[rs], %[sab] sc1\n\t" \
  "s_waitcnt vmcnt(0)\n\t" \
  "v_readfirstlane_b32 s93, v208\n\t" \
  "s_nop 3\n\t" \
  "s_cmp_eq_u32 s93, 1\n\t" \
  "s_cbranch_scc1 .Lvok%=\n\t" \
  "s_cmp_lt_u32 s92, 0x80000\n\t" \
  "s_cbranch_scc1 .Lvr%=\n\t" \
  "v_mov_b32 v208, 1\n\t" \
  "buffer_store_dword v208, off, %[rs], %[sab] sc1\n\t" \
  "s_branch .Lvok%=\n\t" \
  ".Lvr%=:\n\t" \
  "buffer_load_dword v252, v249, %[rs], %[sv] offen offset:0 sc1\n\t" \
  "buffer_load_dword v253, v249, %[rs], %[sv] offen offset:256 sc1\n\t" \
  "s_waitcnt vmcnt(0)\n\t" \
  "v_mfma_f32_16x16x16_bf16 v[224:227], v[252:253], v[236:237], 0\n\t" \
  "v_mfma_f32_16x16x16_bf16 v[228:231], v[252:253], v[238:239], 0\n\t" \
  "v_max_u32 v208, v252, v253\n\t" \
  "v_cmp_le_u32 vcc, %[sent], v208\n\t" \
  "s_nop 4\n\t" \
  "s_branch .Lpv%=\n\t" \
  "s_branch .Lend%=\n\t" \
  ".Lslow%=:\n\t" \
  "s_mov_b64 s[94:95], 0\n\t" \
  "v_cmp_eq_u32 vcc, %[sent], v200\n\t" \
  "s_or_b64 s[94:95], s[94:95], vcc\n\t" \
  "v_cmp_eq_u32 vcc, %[sent], v201\n\t" \
  "s_or_b64 s[94:95], s[94:95], vcc\n\t" \
  "v_cmp_eq_u32 vcc, %[sent], v202\n\t" \
  "s_or_b64 s[94:95], s[94:95], vcc\n\t" \
  "v_cmp_eq_u32 vcc, %[sent], v203\n\t" \
  "s_or_b64 s[94:95], s[94:95], vcc\n\t" \
  "v_cmp_eq_u32 vcc, %[sent], v204\n\t" \
  "s_or_b64 s[94:95], s[94:95], vcc\n\t" \
  "v_cmp_eq_u32 vcc, %[sent], v205\n\t" \
  "s_or_b64 s[94:95], s[94:95], vcc\n\t" \
  "v_cmp_eq_u32 vcc, %[sent], v206\n\t" \
  "s_or_b64 s[94:95], s[94:95], vcc\n\t" \
  "v_cmp_eq_u32 vcc, %[sent], v207\n\t" \
  "s_or_b64 s[94:95], s[94:95], vcc\n\t" \
  "s_cmp_eq_u64 s[94:95], 0\n\t" \
  "s_cbranch_scc1 .Lok%=\n\t" \
  "v_mov_b64 v[192:193], 0\n\t" \
  "v_mov_b64 v[194:195], 0\n\t" \
  "v_mov_b64 v[196:197], 0\n\t" \
  "v_mov_b64 v[198:199], 0\n\t" \
  "s_add_u32 s92, s92, 1\n\t" \
  "s_cmp_eq_u32 s92, 1\n\t" \
  "s_cbranch_scc1 .Lpoll%=\n\t" \
  "s_sleep 2\n\t" \
  "s_and_b32 s93, s92, 63\n\t" \
  "s_cmp_lg_u32 s93, 0\n\t" \
  "s_cbranch_scc1 .Lpoll%=\n\t" \
  "v_mov_b32 v245, 0\n\t" \
  "buffer_load_dword v244, v245, %[rs], %[sab] offen sc1\n\t" \
  "s_waitcnt vmcnt(0)\n\t" \
  "v_readfirstlane_b32 s93, v244\n\t" \
  "s_nop 3\n\t" \
  "s_cmp_eq_u32 s93, 1\n\t" \
  "s_cbranch_scc1 .Lok%=\n\t" \
  "s_cmp_lt_u32 s92, 0x80000\n\t" \
  "s_cbranch_scc1 .Lpoll%=\n\t" \
  "v_mov_b32 v244, 1\n\t" \
  "buffer_store_dword v244, v245, %[rs], %[sab] offen sc1\n\t" \
  "s_branch .Lok%=\n\t" \
  ".Lend%=:\n\t"
#define QK_ASM_2 \
  "s_waitcnt lgkmcnt(0)\n\t" \
  "v_subrev_u32 v244, 0x8000, v250\n\t" \
  "ds_read_b128 v[216:219], v248 offset:40960\n\t" \
  "ds_read_b128 v[220:223], v248 offset:41984\n\t" \
  "ds_read_b128 v[224:227], v248 offset:43008\n\t" \
  "ds_read_b128 v[228:231], v248 offset:44032\n\t" \
  "ds_read_b128 v[208:211], v244 offset:0\n\t" \
  "ds_read_b128 v[212:215], v244 offset:1024\n\t" \
  "ds_read_b128 v[236:239], v244 offset:2048\n\t" \
  "ds_read_b128 v[240:243], v244 offset:3072\n\t" \
  "v_mov_b64 v[192:193], 0\n\t" \
  "v_mov_b64 v[194:195], 0\n\t" \
  "v_mov_b64 v[196:197], 0\n\t" \
  "v_mov_b64 v[198:199], 0\n\t" \
  "s_movk_i32 s92, 0x2000\n\t" \
  ".Lwt%=:\n\t" \
  "ds_read_b64 v[200:201], v248 offset:30720\n\t" \
  "s_memrealtime s[90:91]\n\t" \
  "s_sub_u32 s92, s92, 1\n\t" \
  "s_waitcnt lgkmcnt(0)\n\t" \
  "v_readfirstlane_b32 s93, v200\n\t" \
  "v_readfirstlane_b32 %[tgt], v201\n\t" \
  "s_cbranch_scc1 .Lgo%=\n\t" \
  "s_cmp_eq_u32 s93, %[step]\n\t" \
  "s_cbranch_scc1 .Lwx%=\n\t" \
  "s_sleep 1\n\t" \
  "s_branch .Lwt%=\n\t" \
  ".Lwx%=:\n\t" \
  "s_lshl_b32 s93, s90, 2\n\t" \
  "s_sub_i32 s93, %[tgt], s93\n\t" \
  "s_cmp_le_i32 s93, 0\n\t" \
  "s_cbranch_scc1 .Lgo%=\n\t" \
  "s_min_i32 s93, s93, 8000\n\t" \
  "s_mov_b32 s94, s93\n\t" \
  ".Lsl%=:\n\t" \
  "s_sub_i32 s94, s94, 18\n\t" \
  "s_cmp_lt_i32 s94, 30\n\t" \
  "s_cbranch_scc1 .Lsx%=\n\t" \
  "s_sleep 1\n\t" \
  "s_branch .Lsl%=\n\t" \
  ".Lsx%=:\n\t" \
  "s_sub_i32 s93, s93, 12\n\t" \
  "s_max_i32 s93, s93, 4\n\t" \
  "s_lshr_b32 s93, s93, 2\n\t" \
  "s_add_u32 s93, s93, s90\n\t" \
  "s_memrealtime s[90:91]\n\t" \
  ".Lpace%=:\n\t" \
  "s_memrealtime s[94:95]\n\t" \
  "s_waitcnt lgkmcnt(1)\n\t" \
  "s_sub_i32 s92, s90, s93\n\t" \
  "s_cmp_ge_i32 s92, 0\n\t" \
  "s_cbranch_scc1 .Le%=\n\t" \
  "s_memrealtime s[90:91]\n\t" \
  "s_waitcnt lgkmcnt(1)\n\t" \
  "s_sub_i32 s92, s94, s93\n\t" \
  "s_cmp_lt_i32 s92, 0\n\t" \
  "s_cbranch_scc1 .Lpace%=\n\t" \
  "s_nop 4\n\t" \
  ".Le%=:\n\t" \
  "s_add_u32 s92, s92, s93\n\t" \
  "s_lshl_b32 s92, s92, 2\n\t" \
  "s_sub_i32 s92, %[tgt], s92\n\t" \
  "s_max_i32 s92, s92, 0\n\t" \
  "s_bitcmp0_b32 s92, 3\n\t" \
  "s_cbranch_scc1 .Lf3%=\n\t" \
  "s_nop 15\n\t" \
  ".Lf3%=:\n\t" \
  "s_bitcmp0_b32 s92, 2\n\t" \
  "s_cbranch_scc1 .Lf2%=\n\t" \
  "s_nop 10\n\t" \
  ".Lf2%=:\n\t" \
  ".Lgo%=:\n\t" \
  "s_mov_b32 s92, 0\n\t" \
  ".Lpoll%=:\n\t" \
  "buffer_load_dwordx4 v[200:203], v248, %[rs], %[som] offen sc1\n\t" \
  "buffer_load_dwordx4 v[204:207], v248, %[rs], %[som2] offen sc1\n\t" \
  "s_waitcnt vmcnt(1) lgkmcnt(0)\n\t" \
  "ds_write_b128 v249, v[200:203]\n\t" \
  "v_smfmac_f32_16x16x64_bf16 v[192:195], v[200:203], v[208:215], v251\n\t" \
  "v_max3_u32 v244, v200, v201, v202\n\t" \
  "v_smfmac_f32_16x16x64_bf16 v[196:199], v[200:203], v[236:243], v251\n\t" \
  "s_waitcnt vmcnt(0)\n\t" \
  "ds_write_b128 v249, v[204:207] offset:8192\n\t" \
  "v_smfmac_f32_16x16x64_bf16 v[192:195], v[204:207], v[216:223], v251\n\t" \
  "v_max3_u32 v244, v244, v203, v204\n\t" \
  "v_max3_u32 v244, v244, v205, v206\n\t" \
  "v_smfmac_f32_16x16x64_bf16 v[196:199], v[204:207], v[224:231], v251\n\t" \
  "v_max_u32 v244, v244, v207\n\t" \
  "v_cmp_le_u32 vcc, %[sent], v244\n\t" \
  "s_cbranch_vccnz .Lslow%=\n\t" \
  ".Lok%=:\n\t" \
  "ds_read_b128 v[216:219], v250 offset:32768\n\t" \
  "ds_read_b128 v[220:223], v250 offset:33792\n\t" \
  "ds_read_b128 v[224:227], v250 offset:34816\n\t" \
  "ds_read_b128 v[228:231], v250 offset:35840\n\t" \
  "s_waitcnt lgkmcnt(4)\n\t" \
  "s_barrier\n\t" \
  "ds_read_b128 v[208:211], v248 offset:3072\n\t" \
  "ds_read_b128 v[212:215], v248 offset:4096\n\t" \
  "ds_read_b128 v[236:239], v248 offset:5120\n\t" \
  "ds_read_b128 v[240:243], v248 offset:6144\n\t" \
  "s_cmp_eq_u32 %[step], 63\n\t" \
  "s_cbranch_scc1 .Lls%=\n\t" \
  "s_waitcnt lgkmcnt(3)\n\t" \
  "ds_read_b128 v[244:247], v248 offset:13312\n\t" \
  "v_smfmac_f32_16x16x64_bf16 v[192:195], v[208:211], v[216:223], v251\n\t" \
  "ds_read_b128 v[216:219], v250 offset:0\n\t" \
  "v_smfmac_f32_16x16x64_bf16 v[196:199], v[208:211], v[224:231], v251\n\t" \
  "s_waitcnt lgkmcnt(4)\n\t" \
  "ds_read_b128 v[220:223], v250 offset:1024\n\t" \
  "v_smfmac_f32_16x16x64_bf16 v[192:195], v[212:215], v[0:7], v251\n\t" \
  "ds_read_b128 v[224:227], v250 offset:2048\n\t" \
  "v_smfmac_f32_16x16x64_bf16 v[196:199], v[212:215], v[8:15], v251\n\t" \
  "s_waitcnt lgkmcnt(5)\n\t" \
  "ds_read_b128 v[228:231], v250 offset:3072\n\t" \
  "v_smfmac_f32_16x16x64_bf16 v[192:195], v[236:239], v[16:23], v251\n\t" \
  "ds_read_b128 v[200:203], v248 offset:7168\n\t" \
  "v_smfmac_f32_16x16x64_bf16 v[196:199], v[236:239], v[24:31], v251\n\t" \
  "s_waitcnt lgkmcnt(6)\n\t" \
  "ds_read_b128 v[204:207], v248 offset:8192\n\t" \
  "v_smfmac_f32_16x16x64_bf16 v[192:195], v[240:243], v[32:39], v251\n\t" \
  "ds_read_b128 v[208:211], v248 offset:9216\n\t" \
  "v_smfmac_f32_16x16x64_bf16 v[196:199], v[240:243], v[40:47], v251\n\t" \
  "s_waitcnt lgkmcnt(5)\n\t" \
  "ds_read_b128 v[212:215], v248 offset:11264\n\t" \
  "v_smfmac_f32_16x16x64_bf16 v[192:195], v[244:247], v[216:223], v251\n\t" \
  "s_waitcnt lgkmcnt(4)\n\t" \
  "v_smfmac_f32_16x16x64_bf16 v[196:199], v[244:247], v[224:231], v251\n\t" \
  "s_waitcnt lgkmcnt(3)\n\t" \
  "ds_read_b128 v[236:239], v248 offset:12288\n\t" \
  "v_smfmac_f32_16x16x64_bf16 v[192:195], v[200:203], v[48:55], v251\n\t" \
  "ds_read_b128 v[240:243], v248 offset:14336\n\t" \
  "v_smfmac_f32_16x16x64_bf16 v[196:199], v[200:203], v[56:63], v251\n\t" \
  "s_waitcnt lgkmcnt(4)\n\t" \
  "v_smfmac_f32_16x16x64_bf16 v[192:195], v[204:207], v[64:71], v251\n\t" \
  "ds_read_b128 v[244:247], v248 offset:15360\n\t" \
  "v_smfmac_f32_16x16x64_bf16 v[196:199], v[204:207], v[72:79], v251\n\t" \
  "s_waitcnt lgkmcnt(4)\n\t" \
  "v_smfmac_f32_16x16x64_bf16 v[192:195], v[208:211], v[80:87], v251\n\t" \
  "ds_read_b128 v[200:203], v248 offset:0\n\t" \
  "v_smfmac_f32_16x16x64_bf16 v[196:199], v[208:211], v[88:95], v251\n\t" \
  "s_waitcnt lgkmcnt(4)\n\t" \
  "v_smfmac_f32_16x16x64_bf16 v[192:195], v[212:215], v[96:103], v251\n\t" \
  "ds_read_b128 v[204:207], v248 offset:1024\n\t" \
  "v_smfmac_f32_16x16x64_bf16 v[196:199], v[212:215], v[104:111], v251\n\t" \
  "s_waitcnt lgkmcnt(3)\n\t" \
  "v_smfmac_f32_16x16x64_bf16 v[192:195], v[236:239], v[112:119], v251\n\t" \
  "v_smfmac_f32_16x16x64_bf16 v[196:199], v[236:239], v[120:127], v251\n\t" \
  "v_smfmac_f32_16x16x64_bf16 v[192:195], v[240:243], v[128:135], v251\n\t" \
  "v_smfmac_f32_16x16x64_bf16 v[196:199], v[240:243], v[136:143], v251\n\t" \
  "s_waitcnt lgkmcnt(1)\n\t" \
  "v_smfmac_f32_16x16x64_bf16 v[192:195], v[244:247], v[144:151], v251\n\t" \
  "v_smfmac_f32_16x16x64_bf16 v[196:199], v[244:247], v[152:159], v251\n\t" \
  "v_smfmac_f32_16x16x64_bf16 v[192:195], v[200:203], v[160:167], v251\n\t" \
  "v_smfmac_f32_16x16x64_bf16 v[196:199], v[200:203], v[168:175], v251\n\t" \
  "s_waitcnt lgkmcnt(0)\n\t" \
  "v_smfmac_f32_16x16x64_bf16 v[192:195], v[204:207], v[176:183], v251\n\t" \
  "v_smfmac_f32_16x16x64_bf16 v[196:199], v[204:207], v[184:191], v251\n\t" \
  "ds_read_b128 v[200:203], v248 offset:18432\n\t" \
  "ds_read_b128 v[204:207], v248 offset:22528\n\t" \
  "v_mov_b32 v250, v232\n\t" \
  "v_mov_b32 v249, v233\n\t" \
  "s_nop 2\n\t" \
  "v_add_f32 v192, v192, v194\n\t" \
  "v_add_f32 v193, v193, v195\n\t" \
  "v_add_f32 v196, v196, v198\n\t" \
  "v_add_f32 v197, v197, v199\n\t" \
  "v_cvt_pk_bf16_f32 v192, v192, v193\n\t" \
  "v_cvt_pk_bf16_f32 v196, v196, v197\n\t" \
  "ds_write2_b32 v252, v192, v196 offset1:64\n\t" \
  "v_mov_b32 v251, v234\n\t" \
  "ds_read_b64_tr_b16 v[240:241], v250 offset:1024\n\t" \
  "ds_read_b64_tr_b16 v[242:243], v250 offset:1088\n\t" \
  "v_mov_b32 v212, %[step]\n\t" \
  "s_mov_b64 exec, 1\n\t" \
  "ds_write_b32 v248, v212 offset:28704\n\t" \
  "s_mov_b64 exec, -1\n\t" \
  "buffer_load_dwordx4 v[192:195], v248, %[rt], %[svt] offen offset:0 sc1\n\t" \
  "buffer_load_dwordx4 v[196:199], v248, %[rt], %[svt] offen offset:1024 sc1\n\t" \
  "s_waitcnt lgkmcnt(1)\n\t" \
  "v_mfma_f32_16x16x32_bf16 v[200:203], v[200:203], v[240:243], 0\n\t" \
  "v_mfma_f32_16x16x32_bf16 v[204:207], v[204:207], v[240:243], 0\n\t" \
  "s_nop 7\n\t" \
  "v_cndmask_b32 v216, v200, v204, %[nh]\n\t" \
  "v_cndmask_b32 v217, v201, v205, %[nh]\n\t" \
  "v_cndmask_b32 v218, v202, v206, %[nh]\n\t" \
  "v_cndmask_b32 v219, v203, v207, %[nh]\n\t" \
  "ds_write_b128 v248, v[216:219] offset:18432\n\t" \
  "buffer_load_dword v252, v249, %[rs], %[sv] offen offset:0 sc1\n\t" \
  "buffer_load_dword v253, v249, %[rs], %[sv] offen offset:256 sc1\n\t" \
  "ds_read_b32 v212, v248 offset:28672\n\t" \
  "ds_read_b64_tr_b16 v[244:245], v250 offset:1536\n\t" \
  "ds_read_b64_tr_b16 v[246:247], v250 offset:1600\n\t" \
  "ds_read_b64_tr_b16 v[232:233], v250 offset:0\n\t" \
  "ds_read_b64_tr_b16 v[234:235], v250 offset:64\n\t" \
  "ds_read_b64_tr_b16 v[236:237], v250 offset:512\n\t" \
  "ds_read_b64_tr_b16 v[238:239], v250 offset:576\n\t" \
  "s_setprio 3\n\t" \
  "s_waitcnt lgkmcnt(0)\n\t" \
  "v_cmp_ne_u32 vcc, %[step], v212\n\t" \
  "s_barrier\n\t" \
  "s_and_b32 s93, vcc_lo, 0xf\n\t" \
  "s_cbranch_scc1 .Lre%=\n\t" \
  ".Lrb%=:\n\t" \
  "ds_read_b64_tr_b16 v[208:209], v250 offset:3072\n\t" \
  "ds_read_b64_tr_b16 v[210:211], v250 offset:3136\n\t" \
  "ds_read_b64_tr_b16 v[212:213], v250 offset:3584\n\t" \
  "ds_read_b64_tr_b16 v[214:215], v250 offset:3648\n\t" \
  "ds_read_b128 v[220:223], v248 offset:19456\n\t" \
  "ds_read_b128 v[224:227], v248 offset:16384\n\t" \
  "ds_read_b128 v[228:231], v248 offset:17408\n\t" \
  "ds_read_b64_tr_b16 v[200:201], v250 offset:2048\n\t" \
  "ds_read_b64_tr_b16 v[202:203], v250 offset:2112\n\t" \
  "ds_read_b64_tr_b16 v[204:205], v250 offset:2560\n\t" \
  "ds_read_b64_tr_b16 v[206:207], v250 offset:2624\n\t" \
  "s_waitcnt lgkmcnt(9)\n\t" \
  "v_mfma_f32_16x16x32_bf16 v[208:211], v[208:211], v[240:243], 0\n\t" \
  "s_waitcnt lgkmcnt(7)\n\t" \
  "v_mfma_f32_16x16x32_bf16 v[208:211], v[212:215], v[244:247], v[208:211]\n\t" \
  "s_waitcnt lgkmcnt(6)\n\t" \
  "v_pk_add_f32 v[216:217], v[216:217], v[220:221]\n\t" \
  "v_pk_add_f32 v[218:219], v[218:219], v[222:223]\n\t" \
  "s_waitcnt lgkmcnt(4)\n\t" \
  "v_pk_add_f32 v[224:225], v[224:225], v[228:229]\n\t" \
  "v_pk_add_f32 v[226:227], v[226:227], v[230:231]\n\t" \
  "v_pk_add_f32 v[216:217], v[216:217], v[224:225]\n\t" \
  "v_pk_add_f32 v[218:219], v[218:219], v[226:227]\n\t" \
  "s_waitcnt lgkmcnt(2)\n\t" \
  "v_mfma_f32_16x16x32_bf16 v[208:211], v[200:203], v[232:235], v[208:211]\n\t" \
  "v_max3_f32 v224, v216, v217, v218\n\t" \
  "v_max_f32 v224, v224, v219\n\t" \
  "s_waitcnt lgkmcnt(0)\n\t" \
  "v_mfma_f32_16x16x32_bf16 v[208:211], v[204:207], v[236:239], v[208:211]\n\t" \
  "v_max_f32_dpp v225, v224, v224 row_ror:8 row_mask:0xf bank_mask:0xf\n\t" \
  "v_max_f32_dpp v226, v224, v224 row_ror:8 row_mask:0xf bank_mask:0xf\n\t" \
  "s_nop 1\n\t" \
  "v_permlane32_swap_b32 v225, v226\n\t" \
  "v_max_f32 v231, v225, v226\n\t" \
  "v_mov_b32 v220, 0x3f803f80\n\t" \
  "v_mov_b32 v221, 0x3f803f80\n\t" \
  "v_mov_b64 v[222:223], v[220:221]\n\t" \
  "v_mov_b32 v241, 0\n\t" \
  "v_mov_b32 v242, 0\n\t" \
  "v_max3_f32 v212, v208, v209, v231\n\t" \
  "v_max3_f32 v213, v212, v210, v211\n\t" \
  "v_max3_f32 v214, v212, v210, v211\n\t" \
  "v_cndmask_b32 v208, v208, v210, %[up]\n\t" \
  "v_cndmask_b32 v209, v209, v211, %[up]\n\t" \
  "v_permlane16_swap_b32 v213, v214\n\t" \
  "v_max_f32 v212, v213, v214\n\t" \
  "v_mul_f32 v212, %[negc], v212\n\t" \
  "v_fma_f32 v208, v208, %[c], v212\n\t" \
  "v_fma_f32 v209, v209, %[c], v212\n\t" \
  "v_exp_f32 v208, v208\n\t" \
  "v_fma_f32 v216, v216, %[c], v212\n\t" \
  "v_exp_f32 v209, v209\n\t" \
  "v_fma_f32 v217, v217, %[c], v212\n\t" \
  "v_exp_f32 v216, v216\n\t" \
  "v_fma_f32 v218, v218, %[c], v212\n\t" \
  "v_cvt_pk_bf16_f32 v240, v208, v209\n\t" \
  "v_cvt_pk_bf16_f32 v243, v208, v209\n\t" \
  "v_exp_f32 v217, v217\n\t" \
  "v_fma_f32 v219, v219, %[c], v212\n\t" \
  "v_mfma_f32_16x16x16_bf16 v[204:207], v[220:221], v[240:241], 0\n\t" \
  "v_exp_f32 v218, v218\n\t" \
  "s_waitcnt vmcnt(0)\n\t" \
  "v_mfma_f32_16x16x16_bf16 v[224:227], v[252:253], v[240:241], 0\n\t" \
  "v_cvt_pk_bf16_f32 v202, v216, v217\n\t" \
  "v_cvt_pk_bf16_f32 v200, v216, v217\n\t" \
  "v_exp_f32 v219, v219\n\t" \
  "v_mfma_f32_16x16x16_bf16 v[228:231], v[252:253], v[242:243], 0\n\t" \
  "v_cvt_pk_bf16_f32 v203, v218, v219\n\t" \
  "v_cvt_pk_bf16_f32 v201, v218, v219\n\t" \
  "v_mov_b32_dpp v200, v202 row_ror:8 row_mask:0xf bank_mask:0xc\n\t" \
  "v_mov_b32_dpp v202, v202 row_ror:8 row_mask:0xf bank_mask:0x3\n\t" \
  "v_mov_b32_dpp v201, v203 row_ror:8 row_mask:0xf bank_mask:0xc\n\t" \
  "v_mov_b32_dpp v203, v203 row_ror:8 row_mask:0xf bank_mask:0x3\n\t" \
  "v_max_u32 v212, v252, v253\n\t" \
  "v_cmp_le_u32 vcc, %[sent], v212\n\t" \
  "v_mfma_f32_16x16x32_bf16 v[204:207], v[220:223], v[200:203], v[204:207]\n\t" \
  ".Lpv%=:\n\t" \
  "v_mfma_f32_16x16x32_bf16 v[224:227], v[192:195], v[200:203], v[224:227]\n\t" \
  "v_mfma_f32_16x16x32_bf16 v[228:231], v[196:199], v[200:203], v[228:231]\n\t" \
  "s_cbranch_vccnz .Lvs%=\n\t" \
  ".Lvok%=:\n\t" \
  "s_nop 4\n\t" \
  "v_rcp_f32 v212, v204\n\t" \
  "v_cndmask_b32 v224, v224, v226, %[nh]\n\t" \
  "v_cndmask_b32 v225, v225, v227, %[nh]\n\t" \
  "v_cndmask_b32 v228, v228, v230, %[nh]\n\t" \
  "v_cndmask_b32 v229, v229, v231, %[nh]\n\t" \
  "v_pk_mul_f32 v[224:225], v[224:225], v[212:213] op_sel_hi:[1,0]\n\t" \
  "v_pk_mul_f32 v[228:229], v[228:229], v[212:213] op_sel_hi:[1,0]\n\t" \
  "v_cvt_pk_bf16_f32 v224, v224, v225\n\t" \
  "v_cvt_pk_bf16_f32 v225, v228, v229\n\t" \
  "buffer_store_dwordx2 v[224:225], v251, %[rs], %[so] offen sc1\n\t" \
  "s_memtime s[90:91]\n\t" \
  "s_setprio 0\n\t" \
  "s_waitcnt lgkmcnt(0)\n\t" \
  "s_mov_b32 %[tout], s90\n\t" \
  "s_mov_b32 %[spins], s92\n\t" \
  "s_branch .Lend%=\n\t" \
  ".Lls%=:\n\t" \
  "s_waitcnt lgkmcnt(0)\n\t" \
  "s_barrier\n\t" \
  "s_mov_b32 %[spins], 0\n\t" \
  "s_branch .Lend%=\n\t" \
  ".Lre%=:\n\t" \
  "ds_read_b64_tr_b16 v[244:245], v250 offset:1536\n\t" \
  "ds_read_b64_tr_b16 v[246:247], v250 offset:1600\n\t" \
  "ds_read_b64_tr_b16 v[232:233], v250 offset:0\n\t" \
  "ds_read_b64_tr_b16 v[234:235], v250 offset:64\n\t" \
  "ds_read_b64_tr_b16 v[236:237], v250 offset:512\n\t" \
  "ds_read_b64_tr_b16 v[238:239], v250 offset:576\n\t" \
  "s_waitcnt lgkmcnt(0)\n\t" \
  "s_branch .Lrb%=\n\t" \
  ".Lvs%=:\n\t" \
  "v_cmp_eq_u32 vcc, %[sent], v252\n\t" \
  "v_cmp_eq_u32 s[94:95], %[sent], v253\n\t" \
  "s_or_b64 s[94:95], s[94:95], vcc\n\t" \
  "s_cbranch_scc0 .Lvok%=\n\t" \
  "s_add_u32 s92, s92, 1\n\t" \
  "s_sleep 2\n\t" \
  "s_and_b32 s93, s92, 63\n\t" \
  "s_cbranch_scc1 .Lvr%=\n\t" \
  "buffer_load_dword v212, off, %[rs], %[sab] sc1\n\t" \
  "s_waitcnt vmcnt(0)\n\t" \
  "v_readfirstlane_b32 s93, v212\n\t" \
  "s_nop 3\n\t" \
  "s_cmp_eq_u32 s93, 1\n\t" \
  "s_cbranch_scc1 .Lvok%=\n\t" \
  "s_cmp_lt_u32 s92, 0x80000\n\t" \
  "s_cbranch_scc1 .Lvr%=\n\t" \
  "v_mov_b32 v212, 1\n\t" \
  "buffer_store_dword v212, off, %[rs], %[sab] sc1\n\t" \
  "s_branch .Lvok%=\n\t" \
  ".Lvr%=:\n\t" \
  "buffer_load_dword v252, v249, %[rs], %[sv] offen offset:0 sc1\n\t" \
  "buffer_load_dword v253, v249, %[rs], %[sv] offen offset:256 sc1\n\t" \
  "s_waitcnt vmcnt(0)\n\t" \
  "v_mfma_f32_16x16x16_bf16 v[224:227], v[252:253], v[240:241], 0\n\t" \
  "v_mfma_f32_16x16x16_bf16 v[228:231], v[252:253], v[242:243], 0\n\t" \
  "v_max_u32 v212, v252, v253\n\t" \
  "v_cmp_le_u32 vcc, %[sent], v212\n\t" \
  "s_nop 4\n\t" \
  "s_branch .Lpv%=\n\t" \
  "s_branch .Lend%=\n\t" \
  ".Lslow%=:\n\t" \
  "s_mov_b64 s[94:95], 0\n\t" \
  "v_cmp_eq_u32 vcc, %[sent], v200\n\t" \
  "s_or_b64 s[94:95], s[94:95], vcc\n\t" \
  "v_cmp_eq_u32 vcc, %[sent], v201\n\t" \
  "s_or_b64 s[94:95], s[94:95], vcc\n\t" \
  "v_cmp_eq_u32 vcc, %[sent], v202\n\t" \
  "s_or_b64 s[94:95], s[94:95], vcc\n\t" \
  "v_cmp_eq_u32 vcc, %[sent], v203\n\t" \
  "s_or_b64 s[94:95], s[94:95], vcc\n\t" \
  "v_cmp_eq_u32 vcc, %[sent], v204\n\t" \
  "s_or_b64 s[94:95], s[94:95], vcc\n\t" \
  "v_cmp_eq_u32 vcc, %[sent], v205\n\t" \
  "s_or_b64 s[94:95], s[94:95], vcc\n\t" \
  "v_cmp_eq_u32 vcc, %[sent], v206\n\t" \
  "s_or_b64 s[94:95], s[94:95], vcc\n\t" \
  "v_cmp_eq_u32 vcc, %[sent], v207\n\t" \
  "s_or_b64 s[94:95], s[94:95], vcc\n\t" \
  "s_cmp_eq_u64 s[94:95], 0\n\t" \
  "s_cbranch_scc1 .Lok%=\n\t" \
  "v_mov_b64 v[192:193], 0\n\t" \
  "v_mov_b64 v[194:195], 0\n\t" \
  "v_mov_b64 v[196:197], 0\n\t" \
  "v_mov_b64 v[198:199], 0\n\t" \
  "s_add_u32 s92, s92, 1\n\t" \
  "s_cmp_eq_u32 s92, 1\n\t" \
  "s_cbranch_scc1 .Lpoll%=\n\t" \
  "s_sleep 2\n\t" \
  "s_and_b32 s93, s92, 63\n\t" \
  "s_cmp_lg_u32 s93, 0\n\t" \
  "s_cbranch_scc1 .Lpoll%=\n\t" \
  "v_mov_b32 v245, 0\n\t" \
  "buffer_load_dword v244, v245, %[rs], %[sab] offen sc1\n\t" \
  "s_waitcnt vmcnt(0)\n\t" \
  "v_readfirstlane_b32 s93, v244\n\t" \
  "s_nop 3\n\t" \
  "s_cmp_eq_u32 s93, 1\n\t" \
  "s_cbranch_scc1 .Lok%=\n\t" \
  "s_cmp_lt_u32 s92, 0x80000\n\t" \
  "s_cbranch_scc1 .Lpoll%=\n\t" \
  "v_mov_b32 v244, 1\n\t" \
  "buffer_store_dword v244, v245, %[rs], %[sab] offen sc1\n\t" \
  "s_branch .Lok%=\n\t" \
  ".Lend%=:\n\t"
#define QK_ASM_3 \
  "s_waitcnt lgkmcnt(0)\n\t" \
  "v_subrev_u32 v244, 0x8000, v250\n\t" \
  "ds_read_b128 v[216:219], v248 offset:45056\n\t" \
  "ds_read_b128 v[220:223], v248 offset:46080\n\t" \
  "ds_read_b128 v[224:227], v248 offset:47104\n\t" \
  "ds_read_b128 v[228:231], v248 offset:48128\n\t" \
  "ds_read_b128 v[208:211], v244 offset:0\n\t" \
  "ds_read_b128 v[212:215], v244 offset:1024\n\t" \
  "ds_read_b128 v[236:239], v244 offset:2048\n\t" \
  "ds_read_b128 v[240:243], v244 offset:3072\n\t" \
  "v_mov_b64 v[192:193], 0\n\t" \
  "v_mov_b64 v[194:195], 0\n\t" \
  "v_mov_b64 v[196:197], 0\n\t" \
  "v_mov_b64 v[198:199], 0\n\t" \
  "s_movk_i32 s92, 0x2000\n\t" \
  ".Lwt%=:\n\t" \
  "ds_read_b64 v[200:201], v248 offset:30720\n\t" \
  "s_memrealtime s[90:91]\n\t" \
  "s_sub_u32 s92, s92, 1\n\t" \
  "s_waitcnt lgkmcnt(0)\n\t" \
  "v_readfirstlane_b32 s93, v200\n\t" \
  "v_readfirstlane_b32 %[tgt], v201\n\t" \
  "s_cbranch_scc1 .Lgo%=\n\t" \
  "s_cmp_eq_u32 s93, %[step]\n\t" \
  "s_cbranch_scc1 .Lwx%=\n\t" \
  "s_sleep 1\n\t" \
  "s_branch .Lwt%=\n\t" \
  ".Lwx%=:\n\t" \
  "s_lshl_b32 s93, s90, 2\n\t" \
  "s_sub_i32 s93, %[tgt], s93\n\t" \
  "s_cmp_le_i32 s93, 0\n\t" \
  "s_cbranch_scc1 .Lgo%=\n\t" \
  "s_min_i32 s93, s93, 8000\n\t" \
  "s_mov_b32 s94, s93\n\t" \
  ".Lsl%=:\n\t" \
  "s_sub_i32 s94, s94, 18\n\t" \
  "s_cmp_lt_i32 s94, 30\n\t" \
  "s_cbranch_scc1 .Lsx%=\n\t" \
  "s_sleep 1\n\t" \
  "s_branch .Lsl%=\n\t" \
  ".Lsx%=:\n\t" \
  "s_sub_i32 s93, s93, 12\n\t" \
  "s_max_i32 s93, s93, 4\n\t" \
  "s_lshr_b32 s93, s93, 2\n\t" \
  "s_add_u32 s93, s93, s90\n\t" \
  "s_memrealtime s[90:91]\n\t" \
  ".Lpace%=:\n\t" \
  "s_memrealtime s[94:95]\n\t" \
  "s_waitcnt lgkmcnt(1)\n\t" \
  "s_sub_i32 s92, s90, s93\n\t" \
  "s_cmp_ge_i32 s92, 0\n\t" \
  "s_cbranch_scc1 .Le%=\n\t" \
  "s_memrealtime s[90:91]\n\t" \
  "s_waitcnt lgkmcnt(1)\n\t" \
  "s_sub_i32 s92, s94, s93\n\t" \
  "s_cmp_lt_i32 s92, 0\n\t" \
  "s_cbranch_scc1 .Lpace%=\n\t" \
  "s_nop 4\n\t" \
  ".Le%=:\n\t" \
  "s_add_u32 s92, s92, s93\n\t" \
  "s_lshl_b32 s92, s92, 2\n\t" \
  "s_sub_i32 s92, %[tgt], s92\n\t" \
  "s_max_i32 s92, s92, 0\n\t" \
  "s_bitcmp0_b32 s92, 3\n\t" \
  "s_cbranch_scc1 .Lf3%=\n\t" \
  "s_nop 15\n\t" \
  ".Lf3%=:\n\t" \
  "s_bitcmp0_b32 s92, 2\n\t" \
  "s_cbranch_scc1 .Lf2%=\n\t" \
  "s_nop 10\n\t" \
  ".Lf2%=:\n\t" \
  ".Lgo%=:\n\t" \
  "s_mov_b32 s92, 0\n\t" \
  ".Lpoll%=:\n\t" \
  "buffer_load_dwordx4 v[200:203], v248, %[rs], %[som] offen sc1\n\t" \
  "buffer_load_dwordx4 v[204:207], v248, %[rs], %[som2] offen sc1\n\t" \
  "s_waitcnt vmcnt(1) lgkmcnt(0)\n\t" \
  "ds_write_b128 v249, v[200:203]\n\t" \
  "v_smfmac_f32_16x16x64_bf16 v[192:195], v[200:203], v[208:215], v251\n\t" \
  "v_max3_u32 v244, v200, v201, v202\n\t" \
  "v_smfmac_f32_16x16x64_bf16 v[196:199], v[200:203], v[236:243], v251\n\t" \
  "s_waitcnt vmcnt(0)\n\t" \
  "ds_write_b128 v249, v[204:207] offset:8192\n\t" \
  "v_smfmac_f32_16x16x64_bf16 v[192:195], v[204:207], v[216:223], v251\n\t" \
  "v_max3_u32 v244, v244, v203, v204\n\t" \
  "v_max3_u32 v244, v244, v205, v206\n\t" \
  "v_smfmac_f32_16x16x64_bf16 v[196:199], v[204:207], v[224:231], v251\n\t" \
  "v_max_u32 v244, v244, v207\n\t" \
  "v_cmp_le_u32 vcc, %[sent], v244\n\t" \
  "s_cbranch_vccnz .Lslow%=\n\t" \
  ".Lok%=:\n\t" \
  "ds_read_b128 v[216:219], v250 offset:32768\n\t" \
  "ds_read_b128 v[220:223], v250 offset:33792\n\t" \
  "ds_read_b128 v[224:227], v250 offset:34816\n\t" \
  "ds_read_b128 v[228:231], v250 offset:35840\n\t" \
  "s_waitcnt lgkmcnt(4)\n\t" \
  "s_barrier\n\t" \
  "ds_read_b128 v[208:211], v248 offset:2048\n\t" \
  "ds_read_b128 v[212:215], v248 offset:5120\n\t" \
  "ds_read_b128 v[236:239], v248 offset:6144\n\t" \
  "ds_read_b128 v[240:243], v248 offset:7168\n\t" \
  "s_cmp_eq_u32 %[step], 63\n\t" \
  "s_cbranch_scc1 .Lls%=\n\t" \
  "s_waitcnt lgkmcnt(3)\n\t" \
  "ds_read_b128 v[244:247], v248 offset:12288\n\t" \
  "v_smfmac_f32_16x16x64_bf16 v[192:195], v[208:211], v[216:223], v251\n\t" \
  "ds_read_b128 v[216:219], v250 offset:0\n\t" \
  "v_smfmac_f32_16x16x64_bf16 v[196:199], v[208:211], v[224:231], v251\n\t" \
  "s_waitcnt lgkmcnt(4)\n\t" \
  "ds_read_b128 v[220:223], v250 offset:1024\n\t" \
  "v_smfmac_f32_16x16x64_bf16 v[192:195], v[212:215], v[0:7], v251\n\t" \
  "ds_read_b128 v[224:227], v250 offset:2048\n\t" \
  "v_smfmac_f32_16x16x64_bf16 v[196:199], v[212:215], v[8:15], v251\n\t" \
  "s_waitcnt lgkmcnt(5)\n\t" \
  "ds_read_b128 v[228:231], v250 offset:3072\n\t" \
  "v_smfmac_f32_16x16x64_bf16 v[192:195], v[236:239], v[16:23], v251\n\t" \
  "ds_read_b128 v[200:203], v248 offset:8192\n\t" \
  "v_smfmac_f32_16x16x64_bf16 v[196:199], v[236:239], v[24:31], v251\n\t" \
  "s_waitcnt lgkmcnt(6)\n\t" \
  "ds_read_b128 v[204:207], v248 offset:9216\n\t" \
  "v_smfmac_f32_16x16x64_bf16 v[192:195], v[240:243], v[32:39], v251\n\t" \
  "ds_read_b128 v[208:211], v248 offset:10240\n\t" \
  "v_smfmac_f32_16x16x64_bf16 v[196:199], v[240:243], v[40:47], v251\n\t" \
  "s_waitcnt lgkmcnt(5)\n\t" \
  "ds_read_b128 v[212:215], v248 offset:13312\n\t" \
  "v_smfmac_f32_16x16x64_bf16 v[192:195], v[244:247], v[216:223], v251\n\t" \
  "s_waitcnt lgkmcnt(4)\n\t" \
  "v_smfmac_f32_16x16x64_bf16 v[196:199], v[244:247], v[224:231], v251\n\t" \
  "s_waitcnt lgkmcnt(3)\n\t" \
  "ds_read_b128 v[236:239], v248 offset:14336\n\t" \
  "v_smfmac_f32_16x16x64_bf16 v[192:195], v[200:203], v[48:55], v251\n\t" \
  "ds_read_b128 v[240:243], v248 offset:15360\n\t" \
  "v_smfmac_f32_16x16x64_bf16 v[196:199], v[200:203], v[56:63], v251\n\t" \
  "s_waitcnt lgkmcnt(4)\n\t" \
  "v_smfmac_f32_16x16x64_bf16 v[192:195], v[204:207], v[64:71], v251\n\t" \
  "ds_read_b128 v[244:247], v248 offset:0\n\t" \
  "v_smfmac_f32_16x16x64_bf16 v[196:199], v[204:207], v[72:79], v251\n\t" \
  "s_waitcnt lgkmcnt(4)\n\t" \
  "v_smfmac_f32_16x16x64_bf16 v[192:195], v[208:211], v[80:87], v251\n\t" \
  "ds_read_b128 v[200:203], v248 offset:1024\n\t" \
  "v_smfmac_f32_16x16x64_bf16 v[196:199], v[208:211], v[88:95], v251\n\t" \
  "s_waitcnt lgkmcnt(4)\n\t" \
  "v_smfmac_f32_16x16x64_bf16 v[192:195], v[212:215], v[96:103], v251\n\t" \
  "ds_read_b128 v[204:207], v248 offset:4096\n\t" \
  "v_smfmac_f32_16x16x64_bf16 v[196:199], v[212:215], v[104:111], v251\n\t" \
  "s_waitcnt lgkmcnt(3)\n\t" \
  "v_smfmac_f32_16x16x64_bf16 v[192:195], v[236:239], v[112:119], v251\n\t" \
  "v_smfmac_f32_16x16x64_bf16 v[196:199], v[236:239], v[120:127], v251\n\t" \
  "v_smfmac_f32_16x16x64_bf16 v[192:195], v[240:243], v[128:135], v251\n\t" \
  "v_smfmac_f32_16x16x64_bf16 v[196:199], v[240:243], v[136:143], v251\n\t" \
  "s_waitcnt lgkmcnt(1)\n\t" \
  "v_smfmac_f32_16x16x64_bf16 v[192:195], v[244:247], v[144:151], v251\n\t" \
  "v_smfmac_f32_16x16x64_bf16 v[196:199], v[244:247], v[152:159], v251\n\t" \
  "v_smfmac_f32_16x16x64_bf16 v[192:195], v[200:203], v[160:167], v251\n\t" \
  "v_smfmac_f32_16x16x64_bf16 v[196:199], v[200:203], v[168:175], v251\n\t" \
  "s_waitcnt lgkmcnt(0)\n\t" \
  "v_smfmac_f32_16x16x64_bf16 v[192:195], v[204:207], v[176:183], v251\n\t" \
  "v_smfmac_f32_16x16x64_bf16 v[196:199], v[204:207], v[184:191], v251\n\t" \
  "ds_read_b128 v[200:203], v248 offset:19456\n\t" \
  "ds_read_b128 v[204:207], v248 offset:23552\n\t" \
  "v_mov_b32 v250, v232\n\t" \
  "v_mov_b32 v249, v233\n\t" \
  "s_nop 2\n\t" \
  "v_add_f32 v192, v192, v194\n\t" \
  "v_add_f32 v193, v193, v195\n\t" \
  "v_add_f32 v196, v196, v198\n\t" \
  "v_add_f32 v197, v197, v199\n\t" \
  "v_cvt_pk_bf16_f32 v192, v192, v193\n\t" \
  "v_cvt_pk_bf16_f32 v196, v196, v197\n\t" \
  "ds_write2_b32 v252, v192, v196 offset1:64\n\t" \
  "v_mov_b32 v251, v234\n\t" \
  "ds_read_b64_tr_b16 v[244:245], v250 offset:1536\n\t" \
  "ds_read_b64_tr_b16 v[246:247], v250 offset:1600\n\t" \
  "v_mov_b32 v212, %[step]\n\t" \
  "s_mov_b64 exec, 1\n\t" \
  "ds_write_b32 v248, v212 offset:28720\n\t" \
  "s_mov_b64 exec, -1\n\t" \
  "buffer_load_dwordx4 v[192:195], v248, %[rt], %[svt] offen offset:0 sc1\n\t" \
  "buffer_load_dwordx4 v[196:199], v248, %[rt], %[svt] offen offset:1024 sc1\n\t" \
  "s_waitcnt lgkmcnt(1)\n\t" \
  "v_mfma_f32_16x16x32_bf16 v[200:203], v[200:203], v[244:247], 0\n\t" \
  "v_mfma_f32_16x16x32_bf16 v[204:207], v[204:207], v[244:247], 0\n\t" \
  "s_nop 7\n\t" \
  "v_cndmask_b32 v216, v200, v204, %[nh]\n\t" \
  "v_cndmask_b32 v217, v201, v205, %[nh]\n\t" \
  "v_cndmask_b32 v218, v202, v206, %[nh]\n\t" \
  "v_cndmask_b32 v219, v203, v207, %[nh]\n\t" \
  "ds_write_b128 v248, v[216:219] offset:19456\n\t" \
  "buffer_load_dword v252, v249, %[rs], %[sv] offen offset:0 sc1\n\t" \
  "buffer_load_dword v253, v249, %[rs], %[sv] offen offset:256 sc1\n\t" \
  "ds_read_b32 v212, v248 offset:28672\n\t" \
  "ds_read_b64_tr_b16 v[232:233], v250 offset:0\n\t" \
  "ds_read_b64_tr_b16 v[234:235], v250 offset:64\n\t" \
  "ds_read_b64_tr_b16 v[236:237], v250 offset:512\n\t" \
  "ds_read_b64_tr_b16 v[238:239], v250 offset:576\n\t" \
  "ds_read_b64_tr_b16 v[240:241], v250 offset:1024\n\t" \
  "ds_read_b64_tr_b16 v[242:243], v250 offset:1088\n\t" \
  "s_setprio 3\n\t" \
  "s_waitcnt lgkmcnt(0)\n\t" \
  "v_cmp_ne_u32 vcc, %[step], v212\n\t" \
  "s_barrier\n\t" \
  "s_and_b32 s93, vcc_lo, 0xf\n\t" \
  "s_cbranch_scc1 .Lre%=\n\t" \
  ".Lrb%=:\n\t" \
  "ds_read_b64_tr_b16 v[212:213], v250 offset:3584\n\t" \
  "ds_read_b64_tr_b16 v[214:215], v250 offset:3648\n\t" \
  "ds_read_b64_tr_b16 v[200:201], v250 offset:2048\n\t" \
  "ds_read_b64_tr_b16 v[202:203], v250 offset:2112\n\t" \
  "ds_read_b128 v[220:223], v248 offset:16384\n\t" \
  "ds_read_b128 v[224:227], v248 offset:17408\n\t" \
  "ds_read_b128 v[228:231], v248 offset:18432\n\t" \
  "ds_read_b64_tr_b16 v[204:205], v250 offset:2560\n\t" \
  "ds_read_b64_tr_b16 v[206:207], v250 offset:2624\n\t" \
  "ds_read_b64_tr_b16 v[208:209], v250 offset:3072\n\t" \
  "ds_read_b64_tr_b16 v[210:211], v250 offset:3136\n\t" \
  "s_waitcnt lgkmcnt(9)\n\t" \
  "v_mfma_f32_16x16x32_bf16 v[212:215], v[212:215], v[244:247], 0\n\t" \
  "s_waitcnt lgkmcnt(7)\n\t" \
  "v_mfma_f32_16x16x32_bf16 v[212:215], v[200:203], v[232:235], v[212:215]\n\t" \
  "s_waitcnt lgkmcnt(6)\n\t" \
  "v_pk_add_f32 v[216:217], v[216:217], v[220:221]\n\t" \
  "v_pk_add_f32 v[218:219], v[218:219], v[222:223]\n\t" \
  "s_waitcnt lgkmcnt(4)\n\t" \
  "v_pk_add_f32 v[224:225], v[224:225], v[228:229]\n\t" \
  "v_pk_add_f32 v[226:227], v[226:227], v[230:231]\n\t" \
  "v_pk_add_f32 v[216:217], v[216:217], v[224:225]\n\t" \
  "v_pk_add_f32 v[218:219], v[218:219], v[226:227]\n\t" \
  "s_waitcnt lgkmcnt(2)\n\t" \
  "v_mfma_f32_16x16x32_bf16 v[212:215], v[204:207], v[236:239], v[212:215]\n\t" \
  "v_max3_f32 v224, v216, v217, v218\n\t" \
  "v_max_f32 v224, v224, v219\n\t" \
  "s_waitcnt lgkmcnt(0)\n\t" \
  "v_mfma_f32_16x16x32_bf16 v[212:215], v[208:211], v[240:243], v[212:215]\n\t" \
  "v_max_f32_dpp v225, v224, v224 row_ror:8 row_mask:0xf bank_mask:0xf\n\t" \
  "v_max_f32_dpp v226, v224, v224 row_ror:8 row_mask:0xf bank_mask:0xf\n\t" \
  "s_nop 1\n\t" \
  "v_permlane32_swap_b32 v225, v226\n\t" \
  "v_max_f32 v231, v225, v226\n\t" \
  "v_mov_b32 v220, 0x3f803f80\n\t" \
  "v_mov_b32 v221, 0x3f803f80\n\t" \
  "v_mov_b64 v[222:223], v[220:221]\n\t" \
  "v_mov_b32 v245, 0\n\t" \
  "v_mov_b32 v246, 0\n\t" \
  "v_max3_f32 v200, v212, v213, v231\n\t" \
  "v_max3_f32 v201, v200, v214, v215\n\t" \
  "v_max3_f32 v202, v200, v214, v215\n\t" \
  "v_cndmask_b32 v212, v212, v214, %[up]\n\t" \
  "v_cndmask_b32 v213, v213, v215, %[up]\n\t" \
  "v_permlane16_swap_b32 v201, v202\n\t" \
  "v_max_f32 v200, v201, v202\n\t" \
  "v_mul_f32 v200, %[negc], v200\n\t" \
  "v_fma_f32 v212, v212, %[c], v200\n\t" \
  "v_fma_f32 v213, v213, %[c], v200\n\t" \
  "v_exp_f32 v212, v212\n\t" \
  "v_fma_f32 v216, v216, %[c], v200\n\t" \
  "v_exp_f32 v213, v213\n\t" \
  "v_fma_f32 v217, v217, %[c], v200\n\t" \
  "v_exp_f32 v216, v216\n\t" \
  "v_fma_f32 v218, v218, %[c], v200\n\t" \
  "v_cvt_pk_bf16_f32 v244, v212, v213\n\t" \
  "v_cvt_pk_bf16_f32 v247, v212, v213\n\t" \
  "v_exp_f32 v217, v217\n\t" \
  "v_fma_f32 v219, v219, %[c], v200\n\t" \
  "v_mfma_f32_16x16x16_bf16 v[208:211], v[220:221], v[244:245], 0\n\t" \
  "v_exp_f32 v218, v218\n\t" \
  "s_waitcnt vmcnt(0)\n\t" \
  "v_mfma_f32_16x16x16_bf16 v[224:227], v[252:253], v[244:245], 0\n\t" \
  "v_cvt_pk_bf16_f32 v206, v216, v217\n\t" \
  "v_cvt_pk_bf16_f32 v204, v216, v217\n\t" \
  "v_exp_f32 v219, v219\n\t" \
  "v_mfma_f32_16x16x16_bf16 v[228:231], v[252:253], v[246:247], 0\n\t" \
  "v_cvt_pk_bf16_f32 v207, v218, v219\n\t" \
  "v_cvt_pk_bf16_f32 v205, v218, v219\n\t" \
  "v_mov_b32_dpp v204, v206 row_ror:8 row_mask:0xf bank_mask:0xc\n\t" \
  "v_mov_b32_dpp v206, v206 row_ror:8 row_mask:0xf bank_mask:0x3\n\t" \
  "v_mov_b32_dpp v205, v207 row_ror:8 row_mask:0xf bank_mask:0xc\n\t" \
  "v_mov_b32_dpp v207, v207 row_ror:8 row_mask:0xf bank_mask:0x3\n\t" \
  "v_max_u32 v200, v252, v253\n\t" \
  "v_cmp_le_u32 vcc, %[sent], v200\n\t" \
  "v_mfma_f32_16x16x32_bf16 v[208:211], v[220:223], v[204:207], v[208:211]\n\t" \
  ".Lpv%=:\n\t" \
  "v_mfma_f32_16x16x32_bf16 v[224:227], v[192:195], v[204:207], v[224:227]\n\t" \
  "v_mfma_f32_16x16x32_bf16 v[228:231], v[196:199], v[204:207], v[228:231]\n\t" \
  "s_cbranch_vccnz .Lvs%=\n\t" \
  ".Lvok%=:\n\t" \
  "s_nop 4\n\t" \
  "v_rcp_f32 v200, v208\n\t" \
  "v_cndmask_b32 v224, v224, v226, %[nh]\n\t" \
  "v_cndmask_b32 v225, v225, v227, %[nh]\n\t" \
  "v_cndmask_b32 v228, v228, v230, %[nh]\n\t" \
  "v_cndmask_b32 v229, v229, v231, %[nh]\n\t" \
  "v_pk_mul_f32 v[224:225], v[224:225], v[200:201] op_sel_hi:[1,0]\n\t" \
  "v_pk_mul_f32 v[228:229], v[228:229], v[200:201] op_sel_hi:[1,0]\n\t" \
  "v_cvt_pk_bf16_f32 v224, v224, v225\n\t" \
  "v_cvt_pk_bf16_f32 v225, v228, v229\n\t" \
  "buffer_store_dwordx2 v[224:225], v251, %[rs], %[so] offen sc1\n\t" \
  "s_memtime s[90:91]\n\t" \
  "s_setprio 0\n\t" \
  "s_waitcnt lgkmcnt(0)\n\t" \
  "s_mov_b32 %[tout], s90\n\t" \
  "s_mov_b32 %[spins], s92\n\t" \
  "s_branch .Lend%=\n\t" \
  ".Lls%=:\n\t" \
  "s_waitcnt lgkmcnt(0)\n\t" \
  "s_barrier\n\t" \
  "s_mov_b32 %[spins], 0\n\t" \
  "s_branch .Lend%=\n\t" \
  ".Lre%=:\n\t" \
  "ds_read_b64_tr_b16 v[232:233], v250 offset:0\n\t" \
  "ds_read_b64_tr_b16 v[234:235], v250 offset:64\n\t" \
  "ds_read_b64_tr_b16 v[236:237], v250 offset:512\n\t" \
  "ds_read_b64_tr_b16 v[238:239], v250 offset:576\n\t" \
  "ds_read_b64_tr_b16 v[240:241], v250 offset:1024\n\t" \
  "ds_read_b64_tr_b16 v[242:243], v250 offset:1088\n\t" \
  "s_waitcnt lgkmcnt(0)\n\t" \
  "s_branch .Lrb%=\n\t" \
  ".Lvs%=:\n\t" \
  "v_cmp_eq_u32 vcc, %[sent], v252\n\t" \
  "v_cmp_eq_u32 s[94:95], %[sent], v253\n\t" \
  "s_or_b64 s[94:95], s[94:95], vcc\n\t" \
  "s_cbranch_scc0 .Lvok%=\n\t" \
  "s_add_u32 s92, s92, 1\n\t" \
  "s_sleep 2\n\t" \
  "s_and_b32 s93, s92, 63\n\t" \
  "s_cbranch_scc1 .Lvr%=\n\t" \
  "buffer_load_dword v200, off, %[rs], %[sab] sc1\n\t" \
  "s_waitcnt vmcnt(0)\n\t" \
  "v_readfirstlane_b32 s93, v200\n\t" \
  "s_nop 3\n\t" \
  "s_cmp_eq_u32 s93, 1\n\t" \
  "s_cbranch_scc1 .Lvok%=\n\t" \
  "s_cmp_lt_u32 s92, 0x80000\n\t" \
  "s_cbranch_scc1 .Lvr%=\n\t" \
  "v_mov_b32 v200, 1\n\t" \
  "buffer_store_dword v200, off, %[rs], %[sab] sc1\n\t" \
  "s_branch .Lvok%=\n\t" \
  ".Lvr%=:\n\t" \
  "buffer_load_dword v252, v249, %[rs], %[sv] offen offset:0 sc1\n\t" \
  "buffer_load_dword v253, v249, %[rs], %[sv] offen offset:256 sc1\n\t" \
  "s_waitcnt vmcnt(0)\n\t" \
  "v_mfma_f32_16x16x16_bf16 v[224:227], v[252:253], v[244:245], 0\n\t" \
  "v_mfma_f32_16x16x16_bf16 v[228:231], v[252:253], v[246:247], 0\n\t" \
  "v_max_u32 v200, v252, v253\n\t" \
  "v_cmp_le_u32 vcc, %[sent], v200\n\t" \
  "s_nop 4\n\t" \
  "s_branch .Lpv%=\n\t" \
  "s_branch .Lend%=\n\t" \
  ".Lslow%=:\n\t" \
  "s_mov_b64 s[94:95], 0\n\t" \
  "v_cmp_eq_u32 vcc, %[sent], v200\n\t" \
  "s_or_b64 s[94:95], s[94:95], vcc\n\t" \
  "v_cmp_eq_u32 vcc, %[sent], v201\n\t" \
  "s_or_b64 s[94:95], s[94:95], vcc\n\t" \
  "v_cmp_eq_u32 vcc, %[sent], v202\n\t" \
  "s_or_b64 s[94:95], s[94:95], vcc\n\t" \
  "v_cmp_eq_u32 vcc, %[sent], v203\n\t" \
  "s_or_b64 s[94:95], s[94:95], vcc\n\t" \
  "v_cmp_eq_u32 vcc, %[sent], v204\n\t" \
  "s_or_b64 s[94:95], s[94:95], vcc\n\t" \
  "v_cmp_eq_u32 vcc, %[sent], v205\n\t" \
  "s_or_b64 s[94:95], s[94:95], vcc\n\t" \
  "v_cmp_eq_u32 vcc, %[sent], v206\n\t" \
  "s_or_b64 s[94:95], s[94:95], vcc\n\t" \
  "v_cmp_eq_u32 vcc, %[sent], v207\n\t" \
  "s_or_b64 s[94:95], s[94:95], vcc\n\t" \
  "s_cmp_eq_u64 s[94:95], 0\n\t" \
  "s_cbranch_scc1 .Lok%=\n\t" \
  "v_mov_b64 v[192:193], 0\n\t" \
  "v_mov_b64 v[194:195], 0\n\t" \
  "v_mov_b64 v[196:197], 0\n\t" \
  "v_mov_b64 v[198:199], 0\n\t" \
  "s_add_u32 s92, s92, 1\n\t" \
  "s_cmp_eq_u32 s92, 1\n\t" \
  "s_cbranch_scc1 .Lpoll%=\n\t" \
  "s_sleep 2\n\t" \
  "s_and_b32 s93, s92, 63\n\t" \
  "s_cmp_lg_u32 s93, 0\n\t" \
  "s_cbranch_scc1 .Lpoll%=\n\t" \
  "v_mov_b32 v245, 0\n\t" \
  "buffer_load_dword v244, v245, %[rs], %[sab] offen sc1\n\t" \
  "s_waitcnt vmcnt(0)\n\t" \
  "v_readfirstlane_b32 s93, v244\n\t" \
  "s_nop 3\n\t" \
  "s_cmp_eq_u32 s93, 1\n\t" \
  "s_cbranch_scc1 .Lok%=\n\t" \
  "s_cmp_lt_u32 s92, 0x80000\n\t" \
  "s_cbranch_scc1 .Lpoll%=\n\t" \
  "v_mov_b32 v244, 1\n\t" \
  "buffer_store_dword v244, v245, %[rs], %[sab] offen sc1\n\t" \
  "s_branch .Lok%=\n\t" \
  ".Lend%=:\n\t"
#define QK_ASM_4 \
  "s_waitcnt lgkmcnt(0)\n\t" \
  "v_subrev_u32 v244, 0x8000, v250\n\t" \
  "ds_read_b128 v[216:219], v248 offset:49152\n\t" \
  "ds_read_b128 v[220:223], v248 offset:50176\n\t" \
  "ds_read_b128 v[224:227], v248 offset:51200\n\t" \
  "ds_read_b128 v[228:231], v248 offset:52224\n\t" \
  "ds_read_b128 v[208:211], v244 offset:0\n\t" \
  "ds_read_b128 v[212:215], v244 offset:1024\n\t" \
  "ds_read_b128 v[236:239], v244 offset:2048\n\t" \
  "ds_read_b128 v[240:243], v244 offset:3072\n\t" \
  "v_mov_b64 v[192:193], 0\n\t" \
  "v_mov_b64 v[194:195], 0\n\t" \
  "v_mov_b64 v[196:197], 0\n\t" \
  "v_mov_b64 v[198:199], 0\n\t" \
  "s_movk_i32 s92, 0x2000\n\t" \
  ".Lwt%=:\n\t" \
  "ds_read_b64 v[200:201], v248 offset:30720\n\t" \
  "s_memrealtime s[90:91]\n\t" \
  "s_sub_u32 s92, s92, 1\n\t" \
  "s_waitcnt lgkmcnt(0)\n\t" \
  "v_readfirstlane_b32 s93, v200\n\t" \
  "v_readfirstlane_b32 %[tgt], v201\n\t" \
  "s_cbranch_scc1 .Lgo%=\n\t" \
  "s_cmp_eq_u32 s93, %[step]\n\t" \
  "s_cbranch_scc1 .Lwx%=\n\t" \
  "s_sleep 1\n\t" \
  "s_branch .Lwt%=\n\t" \
  ".Lwx%=:\n\t" \
  "s_lshl_b32 s93, s90, 2\n\t" \
  "s_sub_i32 s93, %[tgt], s93\n\t" \
  "s_cmp_le_i32 s93, 0\n\t" \
  "s_cbranch_scc1 .Lgo%=\n\t" \
  "s_min_i32 s93, s93, 8000\n\t" \
  "s_mov_b32 s94, s93\n\t" \
  ".Lsl%=:\n\t" \
  "s_sub_i32 s94, s94, 18\n\t" \
  "s_cmp_lt_i32 s94, 30\n\t" \
  "s_cbranch_scc1 .Lsx%=\n\t" \
  "s_sleep 1\n\t" \
  "s_branch .Lsl%=\n\t" \
  ".Lsx%=:\n\t" \
  "s_sub_i32 s93, s93, 12\n\t" \
  "s_max_i32 s93, s93, 4\n\t" \
  "s_lshr_b32 s93, s93, 2\n\t" \
  "s_add_u32 s93, s93, s90\n\t" \
  "s_memrealtime s[90:91]\n\t" \
  ".Lpace%=:\n\t" \
  "s_memrealtime s[94:95]\n\t" \
  "s_waitcnt lgkmcnt(1)\n\t" \
  "s_sub_i32 s92, s90, s93\n\t" \
  "s_cmp_ge_i32 s92, 0\n\t" \
  "s_cbranch_scc1 .Le%=\n\t" \
  "s_memrealtime s[90:91]\n\t" \
  "s_waitcnt lgkmcnt(1)\n\t" \
  "s_sub_i32 s92, s94, s93\n\t" \
  "s_cmp_lt_i32 s92, 0\n\t" \
  "s_cbranch_scc1 .Lpace%=\n\t" \
  "s_nop 4\n\t" \
  ".Le%=:\n\t" \
  "s_add_u32 s92, s92, s93\n\t" \
  "s_lshl_b32 s92, s92, 2\n\t" \
  "s_sub_i32 s92, %[tgt], s92\n\t" \
  "s_max_i32 s92, s92, 0\n\t" \
  "s_bitcmp0_b32 s92, 3\n\t" \
  "s_cbranch_scc1 .Lf3%=\n\t" \
  "s_nop 15\n\t" \
  ".Lf3%=:\n\t" \
  "s_bitcmp0_b32 s92, 2\n\t" \
  "s_cbranch_scc1 .Lf2%=\n\t" \
  "s_nop 10\n\t" \
  ".Lf2%=:\n\t" \
  ".Lgo%=:\n\t" \
  "s_mov_b32 s92, 0\n\t" \
  ".Lpoll%=:\n\t" \
  "buffer_load_dwordx4 v[200:203], v248, %[rs], %[som] offen sc1\n\t" \
  "buffer_load_dwordx4 v[204:207], v248, %[rs], %[som2] offen sc1\n\t" \
  "s_waitcnt vmcnt(1) lgkmcnt(0)\n\t" \
  "ds_write_b128 v249, v[200:203]\n\t" \
  "v_smfmac_f32_16x16x64_bf16 v[192:195], v[200:203], v[208:215], v251\n\t" \
  "v_max3_u32 v244, v200, v201, v202\n\t" \
  "v_smfmac_f32_16x16x64_bf16 v[196:199], v[200:203], v[236:243], v251\n\t" \
  "s_waitcnt vmcnt(0)\n\t" \
  "ds_write_b128 v249, v[204:207] offset:8192\n\t" \
  "v_smfmac_f32_16x16x64_bf16 v[192:195], v[204:207], v[216:223], v251\n\t" \
  "v_max3_u32 v244, v244, v203, v204\n\t" \
  "v_max3_u32 v244, v244, v205, v206\n\t" \
  "v_smfmac_f32_16x16x64_bf16 v[196:199], v[204:207], v[224:231], v251\n\t" \
  "v_max_u32 v244, v244, v207\n\t" \
  "v_cmp_le_u32 vcc, %[sent], v244\n\t" \
  "s_cbranch_vccnz .Lslow%=\n\t" \
  ".Lok%=:\n\t" \
  "ds_read_b128 v[216:219], v250 offset:32768\n\t" \
  "ds_read_b128 v[220:223], v250 offset:33792\n\t" \
  "ds_read_b128 v[224:227], v250 offset:34816\n\t" \
  "ds_read_b128 v[228:231], v250 offset:35840\n\t" \
  "s_waitcnt lgkmcnt(4)\n\t" \
  "s_barrier\n\t" \
  "ds_read_b128 v[208:211], v248 offset:5120\n\t" \
  "ds_read_b128 v[212:215], v248 offset:6144\n\t" \
  "ds_read_b128 v[236:239], v248 offset:7168\n\t" \
  "ds_read_b128 v[240:243], v248 offset:8192\n\t" \
  "s_waitcnt lgkmcnt(3)\n\t" \
  "ds_read_b128 v[244:247], v248 offset:13312\n\t" \
  "v_smfmac_f32_16x16x64_bf16 v[192:195], v[208:211], v[216:223], v251\n\t" \
  "ds_read_b128 v[216:219], v250 offset:0\n\t" \
  "v_smfmac_f32_16x16x64_bf16 v[196:199], v[208:211], v[224:231], v251\n\t" \
  "s_waitcnt lgkmcnt(4)\n\t" \
  "ds_read_b128 v[220:223], v250 offset:1024\n\t" \
  "v_smfmac_f32_16x16x64_bf16 v[192:195], v[212:215], v[0:7], v251\n\t" \
  "ds_read_b128 v[224:227], v250 offset:2048\n\t" \
  "v_smfmac_f32_16x16x64_bf16 v[196:199], v[212:215], v[8:15], v251\n\t" \
  "s_waitcnt lgkmcnt(5)\n\t" \
  "ds_read_b128 v[228:231], v250 offset:3072\n\t" \
  "v_smfmac_f32_16x16x64_bf16 v[192:195], v[236:239], v[16:23], v251\n\t" \
  "ds_read_b128 v[200:203], v248 offset:9216\n\t" \
  "v_smfmac_f32_16x16x64_bf16 v[196:199], v[236:239], v[24:31], v251\n\t" \
  "s_waitcnt lgkmcnt(6)\n\t" \
  "ds_read_b128 v[204:207], v248 offset:10240\n\t" \
  "v_smfmac_f32_16x16x64_bf16 v[192:195], v[240:243], v[32:39], v251\n\t" \
  "ds_read_b128 v[208:211], v248 offset:11264\n\t" \
  "v_smfmac_f32_16x16x64_bf16 v[196:199], v[240:243], v[40:47], v251\n\t" \
  "s_waitcnt lgkmcnt(5)\n\t" \
  "ds_read_b128 v[212:215], v248 offset:14336\n\t" \
  "v_smfmac_f32_16x16x64_bf16 v[192:195], v[244:247], v[216:223], v251\n\t" \
  "s_waitcnt lgkmcnt(4)\n\t" \
  "v_smfmac_f32_16x16x64_bf16 v[196:199], v[244:247], v[224:231], v251\n\t" \
  "s_waitcnt lgkmcnt(3)\n\t" \
  "ds_read_b128 v[236:239], v248 offset:15360\n\t" \
  "v_smfmac_f32_16x16x64_bf16 v[192:195], v[200:203], v[48:55], v251\n\t" \
  "ds_read_b128 v[240:243], v248 offset:0\n\t" \
  "v_smfmac_f32_16x16x64_bf16 v[196:199], v[200:203], v[56:63], v251\n\t" \
  "s_waitcnt lgkmcnt(4)\n\t" \
  "v_smfmac_f32_16x16x64_bf16 v[192:195], v[204:207], v[64:71], v251\n\t" \
  "ds_read_b128 v[244:247], v248 offset:1024\n\t" \
  "v_smfmac_f32_16x16x64_bf16 v[196:199], v[204:207], v[72:79], v251\n\t" \
  "s_waitcnt lgkmcnt(4)\n\t" \
  "v_smfmac_f32_16x16x64_bf16 v[192:195], v[208:211], v[80:87], v251\n\t" \
  "ds_read_b128 v[200:203], v248 offset:2048\n\t" \
  "v_smfmac_f32_16x16x64_bf16 v[196:199], v[208:211], v[88:95], v251\n\t" \
  "s_waitcnt lgkmcnt(4)\n\t" \
  "v_smfmac_f32_16x16x64_bf16 v[192:195], v[212:215], v[96:103], v251\n\t" \
  "ds_read_b128 v[204:207], v248 offset:3072\n\t" \
  "v_smfmac_f32_16x16x64_bf16 v[196:199], v[212:215], v[104:111], v251\n\t" \
  "s_waitcnt lgkmcnt(3)\n\t" \
  "v_smfmac_f32_16x16x64_bf16 v[192:195], v[236:239], v[112:119], v251\n\t" \
  "v_smfmac_f32_16x16x64_bf16 v[196:199], v[236:239], v[120:127], v251\n\t" \
  "v_smfmac_f32_16x16x64_bf16 v[192:195], v[240:243], v[128:135], v251\n\t" \
  "v_smfmac_f32_16x16x64_bf16 v[196:199], v[240:243], v[136:143], v251\n\t" \
  "s_waitcnt lgkmcnt(1)\n\t" \
  "v_smfmac_f32_16x16x64_bf16 v[192:195], v[244:247], v[144:151], v251\n\t" \
  "v_smfmac_f32_16x16x64_bf16 v[196:199], v[244:247], v[152:159], v251\n\t" \
  "v_smfmac_f32_16x16x64_bf16 v[192:195], v[200:203], v[160:167], v251\n\t" \
  "v_smfmac_f32_16x16x64_bf16 v[196:199], v[200:203], v[168:175], v251\n\t" \
  "s_waitcnt lgkmcnt(0)\n\t" \
  "v_smfmac_f32_16x16x64_bf16 v[192:195], v[204:207], v[176:183], v251\n\t" \
  "v_smfmac_f32_16x16x64_bf16 v[196:199], v[204:207], v[184:191], v251\n\t" \
  "s_nop 6\n\t" \
  "v_add_f32 v192, v192, v194\n\t" \
  "v_add_f32 v193, v193, v195\n\t" \
  "v_add_f32 v196, v196, v198\n\t" \
  "v_add_f32 v197, v197, v199\n\t" \
  "v_cvt_pk_bf16_f32 v192, v192, v193\n\t" \
  "v_cvt_pk_bf16_f32 v196, v196, v197\n\t" \
  "ds_write2_b32 v252, v192, v196 offset1:64\n\t" \
  "s_waitcnt lgkmcnt(0)\n\t" \
  "s_barrier\n\t" \
  "buffer_store_dword v192, v235, %[rs], %[sk] offen sc1\n\t" \
  "buffer_store_dword v196, v235, %[rs], %[sk] offen offset:256 sc1\n\t" \
  "s_mov_b32 %[spins], s92\n\t" \
  "s_branch .Lend%=\n\t" \
  ".Lslow%=:\n\t" \
  "s_mov_b64 s[94:95], 0\n\t" \
  "v_cmp_eq_u32 vcc, %[sent], v200\n\t" \
  "s_or_b64 s[94:95], s[94:95], vcc\n\t" \
  "v_cmp_eq_u32 vcc, %[sent], v201\n\t" \
  "s_or_b64 s[94:95], s[94:95], vcc\n\t" \
  "v_cmp_eq_u32 vcc, %[sent], v202\n\t" \
  "s_or_b64 s[94:95], s[94:95], vcc\n\t" \
  "v_cmp_eq_u32 vcc, %[sent], v203\n\t" \
  "s_or_b64 s[94:95], s[94:95], vcc\n\t" \
  "v_cmp_eq_u32 vcc, %[sent], v204\n\t" \
  "s_or_b64 s[94:95], s[94:95], vcc\n\t" \
  "v_cmp_eq_u32 vcc, %[sent], v205\n\t" \
  "s_or_b64 s[94:95], s[94:95], vcc\n\t" \
  "v_cmp_eq_u32 vcc, %[sent], v206\n\t" \
  "s_or_b64 s[94:95], s[94:95], vcc\n\t" \
  "v_cmp_eq_u32 vcc, %[sent], v207\n\t" \
  "s_or_b64 s[94:95], s[94:95], vcc\n\t" \
  "s_cmp_eq_u64 s[94:95], 0\n\t" \
  "s_cbranch_scc1 .Lok%=\n\t" \
  "v_mov_b64 v[192:193], 0\n\t" \
  "v_mov_b64 v[194:195], 0\n\t" \
  "v_mov_b64 v[196:197], 0\n\t" \
  "v_mov_b64 v[198:199], 0\n\t" \
  "s_add_u32 s92, s92, 1\n\t" \
  "s_cmp_eq_u32 s92, 1\n\t" \
  "s_cbranch_scc1 .Lpoll%=\n\t" \
  "s_sleep 2\n\t" \
  "s_and_b32 s93, s92, 63\n\t" \
  "s_cmp_lg_u32 s93, 0\n\t" \
  "s_cbranch_scc1 .Lpoll%=\n\t" \
  "v_mov_b32 v245, 0\n\t" \
  "buffer_load_dword v244, v245, %[rs], %[sab] offen sc1\n\t" \
  "s_waitcnt vmcnt(0)\n\t" \
  "v_readfirstlane_b32 s93, v244\n\t" \
  "s_nop 3\n\t" \
  "s_cmp_eq_u32 s93, 1\n\t" \
  "s_cbranch_scc1 .Lok%=\n\t" \
  "s_cmp_lt_u32 s92, 0x80000\n\t" \
  "s_cbranch_scc1 .Lpoll%=\n\t" \
  "v_mov_b32 v244, 1\n\t" \
  "buffer_store_dword v244, v245, %[rs], %[sab] offen sc1\n\t" \
  "s_branch .Lok%=\n\t" \
  ".Lend%=:\n\t"
#define QK_ASM_5 \
  "s_waitcnt lgkmcnt(0)\n\t" \
  "v_subrev_u32 v244, 0x8000, v250\n\t" \
  "ds_read_b128 v[216:219], v248 offset:53248\n\t" \
  "ds_read_b128 v[220:223], v248 offset:54272\n\t" \
  "ds_read_b128 v[224:227], v248 offset:55296\n\t" \
  "ds_read_b128 v[228:231], v248 offset:56320\n\t" \
  "ds_read_b128 v[208:211], v244 offset:0\n\t" \
  "ds_read_b128 v[212:215], v244 offset:1024\n\t" \
  "ds_read_b128 v[236:239], v244 offset:2048\n\t" \
  "ds_read_b128 v[240:243], v244 offset:3072\n\t" \
  "v_mov_b64 v[192:193], 0\n\t" \
  "v_mov_b64 v[194:195], 0\n\t" \
  "v_mov_b64 v[196:197], 0\n\t" \
  "v_mov_b64 v[198:199], 0\n\t" \
  "s_movk_i32 s92, 0x2000\n\t" \
  ".Lwt%=:\n\t" \
  "ds_read_b64 v[200:201], v248 offset:30720\n\t" \
  "s_memrealtime s[90:91]\n\t" \
  "s_sub_u32 s92, s92, 1\n\t" \
  "s_waitcnt lgkmcnt(0)\n\t" \
  "v_readfirstlane_b32 s93, v200\n\t" \
  "v_readfirstlane_b32 %[tgt], v201\n\t" \
  "s_cbranch_scc1 .Lgo%=\n\t" \
  "s_cmp_eq_u32 s93, %[step]\n\t" \
  "s_cbranch_scc1 .Lwx%=\n\t" \
  "s_sleep 1\n\t" \
  "s_branch .Lwt%=\n\t" \
  ".Lwx%=:\n\t" \
  "s_lshl_b32 s93, s90, 2\n\t" \
  "s_sub_i32 s93, %[tgt], s93\n\t" \
  "s_cmp_le_i32 s93, 0\n\t" \
  "s_cbranch_scc1 .Lgo%=\n\t" \
  "s_min_i32 s93, s93, 8000\n\t" \
  "s_mov_b32 s94, s93\n\t" \
  ".Lsl%=:\n\t" \
  "s_sub_i32 s94, s94, 18\n\t" \
  "s_cmp_lt_i32 s94, 30\n\t" \
  "s_cbranch_scc1 .Lsx%=\n\t" \
  "s_sleep 1\n\t" \
  "s_branch .Lsl%=\n\t" \
  ".Lsx%=:\n\t" \
  "s_sub_i32 s93, s93, 12\n\t" \
  "s_max_i32 s93, s93, 4\n\t" \
  "s_lshr_b32 s93, s93, 2\n\t" \
  "s_add_u32 s93, s93, s90\n\t" \
  "s_memrealtime s[90:91]\n\t" \
  ".Lpace%=:\n\t" \
  "s_memrealtime s[94:95]\n\t" \
  "s_waitcnt lgkmcnt(1)\n\t" \
  "s_sub_i32 s92, s90, s93\n\t" \
  "s_cmp_ge_i32 s92, 0\n\t" \
  "s_cbranch_scc1 .Le%=\n\t" \
  "s_memrealtime s[90:91]\n\t" \
  "s_waitcnt lgkmcnt(1)\n\t" \
  "s_sub_i32 s92, s94, s93\n\t" \
  "s_cmp_lt_i32 s92, 0\n\t" \
  "s_cbranch_scc1 .Lpace%=\n\t" \
  "s_nop 4\n\t" \
  ".Le%=:\n\t" \
  "s_add_u32 s92, s92, s93\n\t" \
  "s_lshl_b32 s92, s92, 2\n\t" \
  "s_sub_i32 s92, %[tgt], s92\n\t" \
  "s_max_i32 s92, s92, 0\n\t" \
  "s_bitcmp0_b32 s92, 3\n\t" \
  "s_cbranch_scc1 .Lf3%=\n\t" \
  "s_nop 15\n\t" \
  ".Lf3%=:\n\t" \
  "s_bitcmp0_b32 s92, 2\n\t" \
  "s_cbranch_scc1 .Lf2%=\n\t" \
  "s_nop 10\n\t" \
  ".Lf2%=:\n\t" \
  ".Lgo%=:\n\t" \
  "s_mov_b32 s92, 0\n\t" \
  ".Lpoll%=:\n\t" \
  "buffer_load_dwordx4 v[200:203], v248, %[rs], %[som] offen sc1\n\t" \
  "buffer_load_dwordx4 v[204:207], v248, %[rs], %[som2] offen sc1\n\t" \
  "s_waitcnt vmcnt(1) lgkmcnt(0)\n\t" \
  "ds_write_b128 v249, v[200:203]\n\t" \
  "v_smfmac_f32_16x16x64_bf16 v[192:195], v[200:203], v[208:215], v251\n\t" \
  "v_max3_u32 v244, v200, v201, v202\n\t" \
  "v_smfmac_f32_16x16x64_bf16 v[196:199], v[200:203], v[236:243], v251\n\t" \
  "s_waitcnt vmcnt(0)\n\t" \
  "ds_write_b128 v249, v[204:207] offset:8192\n\t" \
  "v_smfmac_f32_16x16x64_bf16 v[192:195], v[204:207], v[216:223], v251\n\t" \
  "v_max3_u32 v244, v244, v203, v204\n\t" \
  "v_max3_u32 v244, v244, v205, v206\n\t" \
  "v_smfmac_f32_16x16x64_bf16 v[196:199], v[204:207], v[224:231], v251\n\t" \
  "v_max_u32 v244, v244, v207\n\t" \
  "v_cmp_le_u32 vcc, %[sent], v244\n\t" \
  "s_cbranch_vccnz .Lslow%=\n\t" \
  ".Lok%=:\n\t" \
  "ds_read_b128 v[216:219], v250 offset:32768\n\t" \
  "ds_read_b128 v[220:223], v250 offset:33792\n\t" \
  "ds_read_b128 v[224:227], v250 offset:34816\n\t" \
  "ds_read_b128 v[228:231], v250 offset:35840\n\t" \
  "s_waitcnt lgkmcnt(4)\n\t" \
  "s_barrier\n\t" \
  "ds_read_b128 v[208:211], v248 offset:4096\n\t" \
  "ds_read_b128 v[212:215], v248 offset:7168\n\t" \
  "ds_read_b128 v[236:239], v248 offset:8192\n\t" \
  "ds_read_b128 v[240:243], v248 offset:9216\n\t" \
  "s_waitcnt lgkmcnt(3)\n\t" \
  "ds_read_b128 v[244:247], v248 offset:12288\n\t" \
  "v_smfmac_f32_16x16x64_bf16 v[192:195], v[208:211], v[216:223], v251\n\t" \
  "ds_read_b128 v[216:219], v250 offset:0\n\t" \
  "v_smfmac_f32_16x16x64_bf16 v[196:199], v[208:211], v[224:231], v251\n\t" \
  "s_waitcnt lgkmcnt(4)\n\t" \
  "ds_read_b128 v[220:223], v250 offset:1024\n\t" \
  "v_smfmac_f32_16x16x64_bf16 v[192:195], v[212:215], v[0:7], v251\n\t" \
  "ds_read_b128 v[224:227], v250 offset:2048\n\t" \
  "v_smfmac_f32_16x16x64_bf16 v[196:199], v[212:215], v[8:15], v251\n\t" \
  "s_waitcnt lgkmcnt(5)\n\t" \
  "ds_read_b128 v[228:231], v250 offset:3072\n\t" \
  "v_smfmac_f32_16x16x64_bf16 v[192:195], v[236:239], v[16:23], v251\n\t" \
  "ds_read_b128 v[200:203], v248 offset:10240\n\t" \
  "v_smfmac_f32_16x16x64_bf16 v[196:199], v[236:239], v[24:31], v251\n\t" \
  "s_waitcnt lgkmcnt(6)\n\t" \
  "ds_read_b128 v[204:207], v248 offset:11264\n\t" \
  "v_smfmac_f32_16x16x64_bf16 v[192:195], v[240:243], v[32:39], v251\n\t" \
  "ds_read_b128 v[208:211], v248 offset:14336\n\t" \
  "v_smfmac_f32_16x16x64_bf16 v[196:199], v[240:243], v[40:47], v251\n\t" \
  "s_waitcnt lgkmcnt(5)\n\t" \
  "ds_read_b128 v[212:215], v248 offset:15360\n\t" \
  "v_smfmac_f32_16x16x64_bf16 v[192:195], v[244:247], v[216:223], v251\n\t" \
  "s_waitcnt lgkmcnt(4)\n\t" \
  "v_smfmac_f32_16x16x64_bf16 v[196:199], v[244:247], v[224:231], v251\n\t" \
  "s_waitcnt lgkmcnt(3)\n\t" \
  "ds_read_b128 v[236:239], v248 offset:0\n\t" \
  "v_smfmac_f32_16x16x64_bf16 v[192:195], v[200:203], v[48:55], v251\n\t" \
  "ds_read_b128 v[240:243], v248 offset:1024\n\t" \
  "v_smfmac_f32_16x16x64_bf16 v[196:199], v[200:203], v[56:63], v251\n\t" \
  "s_waitcnt lgkmcnt(4)\n\t" \
  "v_smfmac_f32_16x16x64_bf16 v[192:195], v[204:207], v[64:71], v251\n\t" \
  "ds_read_b128 v[244:247], v248 offset:2048\n\t" \
  "v_smfmac_f32_16x16x64_bf16 v[196:199], v[204:207], v[72:79], v251\n\t" \
  "s_waitcnt lgkmcnt(4)\n\t" \
  "v_smfmac_f32_16x16x64_bf16 v[192:195], v[208:211], v[80:87], v251\n\t" \
  "ds_read_b128 v[200:203], v248 offset:3072\n\t" \
  "v_smfmac_f32_16x16x64_bf16 v[196:199], v[208:211], v[88:95], v251\n\t" \
  "s_waitcnt lgkmcnt(4)\n\t" \
  "v_smfmac_f32_16x16x64_bf16 v[192:195], v[212:215], v[96:103], v251\n\t" \
  "ds_read_b128 v[204:207], v248 offset:6144\n\t" \
  "v_smfmac_f32_16x16x64_bf16 v[196:199], v[212:215], v[104:111], v251\n\t" \
  "s_waitcnt lgkmcnt(3)\n\t" \
  "v_smfmac_f32_16x16x64_bf16 v[192:195], v[236:239], v[112:119], v251\n\t" \
  "v_smfmac_f32_16x16x64_bf16 v[196:199], v[236:239], v[120:127], v251\n\t" \
  "v_smfmac_f32_16x16x64_bf16 v[192:195], v[240:243], v[128:135], v251\n\t" \
  "v_smfmac_f32_16x16x64_bf16 v[196:199], v[240:243], v[136:143], v251\n\t" \
  "s_waitcnt lgkmcnt(1)\n\t" \
  "v_smfmac_f32_16x16x64_bf16 v[192:195], v[244:247], v[144:151], v251\n\t" \
  "v_smfmac_f32_16x16x64_bf16 v[196:199], v[244:247], v[152:159], v251\n\t" \
  "v_smfmac_f32_16x16x64_bf16 v[192:195], v[200:203], v[160:167], v251\n\t" \
  "v_smfmac_f32_16x16x64_bf16 v[196:199], v[200:203], v[168:175], v251\n\t" \
  "s_waitcnt lgkmcnt(0)\n\t" \
  "v_smfmac_f32_16x16x64_bf16 v[192:195], v[204:207], v[176:183], v251\n\t" \
  "v_smfmac_f32_16x16x64_bf16 v[196:199], v[204:207], v[184:191], v251\n\t" \
  "s_nop 6\n\t" \
  "v_add_f32 v192, v192, v194\n\t" \
  "v_add_f32 v193, v193, v195\n\t" \
  "v_add_f32 v196, v196, v198\n\t" \
  "v_add_f32 v197, v197, v199\n\t" \
  "v_cvt_pk_bf16_f32 v192, v192, v193\n\t" \
  "v_cvt_pk_bf16_f32 v196, v196, v197\n\t" \
  "ds_write2_b32 v252, v192, v196 offset1:64\n\t" \
  "s_waitcnt lgkmcnt(0)\n\t" \
  "s_barrier\n\t" \
  "buffer_store_dword v192, v235, %[rs], %[sk] offen sc1\n\t" \
  "buffer_store_dword v196, v235, %[rs], %[sk] offen offset:256 sc1\n\t" \
  "s_mov_b32 %[spins], s92\n\t" \
  "s_branch .Lend%=\n\t" \
  ".Lslow%=:\n\t" \
  "s_mov_b64 s[94:95], 0\n\t" \
  "v_cmp_eq_u32 vcc, %[sent], v200\n\t" \
  "s_or_b64 s[94:95], s[94:95], vcc\n\t" \
  "v_cmp_eq_u32 vcc, %[sent], v201\n\t" \
  "s_or_b64 s[94:95], s[94:95], vcc\n\t" \
  "v_cmp_eq_u32 vcc, %[sent], v202\n\t" \
  "s_or_b64 s[94:95], s[94:95], vcc\n\t" \
  "v_cmp_eq_u32 vcc, %[sent], v203\n\t" \
  "s_or_b64 s[94:95], s[94:95], vcc\n\t" \
  "v_cmp_eq_u32 vcc, %[sent], v204\n\t" \
  "s_or_b64 s[94:95], s[94:95], vcc\n\t" \
  "v_cmp_eq_u32 vcc, %[sent], v205\n\t" \
  "s_or_b64 s[94:95], s[94:95], vcc\n\t" \
  "v_cmp_eq_u32 vcc, %[sent], v206\n\t" \
  "s_or_b64 s[94:95], s[94:95], vcc\n\t" \
  "v_cmp_eq_u32 vcc, %[sent], v207\n\t" \
  "s_or_b64 s[94:95], s[94:95], vcc\n\t" \
  "s_cmp_eq_u64 s[94:95], 0\n\t" \
  "s_cbranch_scc1 .Lok%=\n\t" \
  "v_mov_b64 v[192:193], 0\n\t" \
  "v_mov_b64 v[194:195], 0\n\t" \
  "v_mov_b64 v[196:197], 0\n\t" \
  "v_mov_b64 v[198:199], 0\n\t" \
  "s_add_u32 s92, s92, 1\n\t" \
  "s_cmp_eq_u32 s92, 1\n\t" \
  "s_cbranch_scc1 .Lpoll%=\n\t" \
  "s_sleep 2\n\t" \
  "s_and_b32 s93, s92, 63\n\t" \
  "s_cmp_lg_u32 s93, 0\n\t" \
  "s_cbranch_scc1 .Lpoll%=\n\t" \
  "v_mov_b32 v245, 0\n\t" \
  "buffer_load_dword v244, v245, %[rs], %[sab] offen sc1\n\t" \
  "s_waitcnt vmcnt(0)\n\t" \
  "v_readfirstlane_b32 s93, v244\n\t" \
  "s_nop 3\n\t" \
  "s_cmp_eq_u32 s93, 1\n\t" \
  "s_cbranch_scc1 .Lok%=\n\t" \
  "s_cmp_lt_u32 s92, 0x80000\n\t" \
  "s_cbranch_scc1 .Lpoll%=\n\t" \
  "v_mov_b32 v244, 1\n\t" \
  "buffer_store_dword v244, v245, %[rs], %[sab] offen sc1\n\t" \
  "s_branch .Lok%=\n\t" \
  ".Lend%=:\n\t"
#define QK_ASM_6 \
  "s_waitcnt lgkmcnt(0)\n\t" \
  "v_subrev_u32 v244, 0x8000, v250\n\t" \
  "ds_read_b128 v[216:219], v248 offset:57344\n\t" \
  "ds_read_b128 v[220:223], v248 offset:58368\n\t" \
  "ds_read_b128 v[224:227], v248 offset:59392\n\t" \
  "ds_read_b128 v[228:231], v248 offset:60416\n\t" \
  "ds_read_b128 v[208:211], v244 offset:0\n\t" \
  "ds_read_b128 v[212:215], v244 offset:1024\n\t" \
  "ds_read_b128 v[236:239], v244 offset:2048\n\t" \
  "ds_read_b128 v[240:243], v244 offset:3072\n\t" \
  "v_mov_b64 v[192:193], 0\n\t" \
  "v_mov_b64 v[194:195], 0\n\t" \
  "v_mov_b64 v[196:197], 0\n\t" \
  "v_mov_b64 v[198:199], 0\n\t" \
  "s_movk_i32 s92, 0x2000\n\t" \
  ".Lwt%=:\n\t" \
  "ds_read_b64 v[200:201], v248 offset:30720\n\t" \
  "s_memrealtime s[90:91]\n\t" \
  "s_sub_u32 s92, s92, 1\n\t" \
  "s_waitcnt lgkmcnt(0)\n\t" \
  "v_readfirstlane_b32 s93, v200\n\t" \
  "v_readfirstlane_b32 %[tgt], v201\n\t" \
  "s_cbranch_scc1 .Lgo%=\n\t" \
  "s_cmp_eq_u32 s93, %[step]\n\t" \
  "s_cbranch_scc1 .Lwx%=\n\t" \
  "s_sleep 1\n\t" \
  "s_branch .Lwt%=\n\t" \
  ".Lwx%=:\n\t" \
  "s_lshl_b32 s93, s90, 2\n\t" \
  "s_sub_i32 s93, %[tgt], s93\n\t" \
  "s_cmp_le_i32 s93, 0\n\t" \
  "s_cbranch_scc1 .Lgo%=\n\t" \
  "s_min_i32 s93, s93, 8000\n\t" \
  "s_mov_b32 s94, s93\n\t" \
  ".Lsl%=:\n\t" \
  "s_sub_i32 s94, s94, 18\n\t" \
  "s_cmp_lt_i32 s94, 30\n\t" \
  "s_cbranch_scc1 .Lsx%=\n\t" \
  "s_sleep 1\n\t" \
  "s_branch .Lsl%=\n\t" \
  ".Lsx%=:\n\t" \
  "s_sub_i32 s93, s93, 12\n\t" \
  "s_max_i32 s93, s93, 4\n\t" \
  "s_lshr_b32 s93, s93, 2\n\t" \
  "s_add_u32 s93, s93, s90\n\t" \
  "s_memrealtime s[90:91]\n\t" \
  ".Lpace%=:\n\t" \
  "s_memrealtime s[94:95]\n\t" \
  "s_waitcnt lgkmcnt(1)\n\t" \
  "s_sub_i32 s92, s90, s93\n\t" \
  "s_cmp_ge_i32 s92, 0\n\t" \
  "s_cbranch_scc1 .Le%=\n\t" \
  "s_memrealtime s[90:91]\n\t" \
  "s_waitcnt lgkmcnt(1)\n\t" \
  "s_sub_i32 s92, s94, s93\n\t" \
  "s_cmp_lt_i32 s92, 0\n\t" \
  "s_cbranch_scc1 .Lpace%=\n\t" \
  "s_nop 4\n\t" \
  ".Le%=:\n\t" \
  "s_add_u32 s92, s92, s93\n\t" \
  "s_lshl_b32 s92, s92, 2\n\t" \
  "s_sub_i32 s92, %[tgt], s92\n\t" \
  "s_max_i32 s92, s92, 0\n\t" \
  "s_bitcmp0_b32 s92, 3\n\t" \
  "s_cbranch_scc1 .Lf3%=\n\t" \
  "s_nop 15\n\t" \
  ".Lf3%=:\n\t" \
  "s_bitcmp0_b32 s92, 2\n\t" \
  "s_cbranch_scc1 .Lf2%=\n\t" \
  "s_nop 10\n\t" \
  ".Lf2%=:\n\t" \
  ".Lgo%=:\n\t" \
  "s_mov_b32 s92, 0\n\t" \
  ".Lpoll%=:\n\t" \
  "buffer_load_dwordx4 v[200:203], v248, %[rs], %[som] offen sc1\n\t" \
  "buffer_load_dwordx4 v[204:207], v248, %[rs], %[som2] offen sc1\n\t" \
  "s_waitcnt vmcnt(1) lgkmcnt(0)\n\t" \
  "ds_write_b128 v249, v[200:203]\n\t" \
  "v_smfmac_f32_16x16x64_bf16 v[192:195], v[200:203], v[208:215], v251\n\t" \
  "v_max3_u32 v244, v200, v201, v202\n\t" \
  "v_smfmac_f32_16x16x64_bf16 v[196:199], v[200:203], v[236:243], v251\n\t" \
  "s_waitcnt vmcnt(0)\n\t" \
  "ds_write_b128 v249, v[204:207] offset:8192\n\t" \
  "v_smfmac_f32_16x16x64_bf16 v[192:195], v[204:207], v[216:223], v251\n\t" \
  "v_max3_u32 v244, v244, v203, v204\n\t" \
  "v_max3_u32 v244, v244, v205, v206\n\t" \
  "v_smfmac_f32_16x16x64_bf16 v[196:199], v[204:207], v[224:231], v251\n\t" \
  "v_max_u32 v244, v244, v207\n\t" \
  "v_cmp_le_u32 vcc, %[sent], v244\n\t" \
  "s_cbranch_vccnz .Lslow%=\n\t" \
  ".Lok%=:\n\t" \
  "ds_read_b128 v[216:219], v250 offset:32768\n\t" \
  "ds_read_b128 v[220:223], v250 offset:33792\n\t" \
  "ds_read_b128 v[224:227], v250 offset:34816\n\t" \
  "ds_read_b128 v[228:231], v250 offset:35840\n\t" \
  "s_waitcnt lgkmcnt(4)\n\t" \
  "s_barrier\n\t" \
  "ds_read_b128 v[208:211], v248 offset:7168\n\t" \
  "ds_read_b128 v[212:215], v248 offset:8192\n\t" \
  "ds_read_b128 v[236:239], v248 offset:9216\n\t" \
  "ds_read_b128 v[240:243], v248 offset:10240\n\t" \
  "s_waitcnt lgkmcnt(3)\n\t" \
  "ds_read_b128 v[244:247], v248 offset:13312\n\t" \
  "v_smfmac_f32_16x16x64_bf16 v[192:195], v[208:211], v[216:223], v251\n\t" \
  "ds_read_b128 v[216:219], v250 offset:0\n\t" \
  "v_smfmac_f32_16x16x64_bf16 v[196:199], v[208:211], v[224:231], v251\n\t" \
  "s_waitcnt lgkmcnt(4)\n\t" \
  "ds_read_b128 v[220:223], v250 offset:1024\n\t" \
  "v_smfmac_f32_16x16x64_bf16 v[192:195], v[212:215], v[0:7], v251\n\t" \
  "ds_read_b128 v[224:227], v250 offset:2048\n\t" \
  "v_smfmac_f32_16x16x64_bf16 v[196:199], v[212:215], v[8:15], v251\n\t" \
  "s_waitcnt lgkmcnt(5)\n\t" \
  "ds_read_b128 v[228:231], v250 offset:3072\n\t" \
  "v_smfmac_f32_16x16x64_bf16 v[192:195], v[236:239], v[16:23], v251\n\t" \
  "ds_read_b128 v[200:203], v248 offset:11264\n\t" \
  "v_smfmac_f32_16x16x64_bf16 v[196:199], v[236:239], v[24:31], v251\n\t" \
  "s_waitcnt lgkmcnt(6)\n\t" \
  "ds_read_b128 v[204:207], v248 offset:12288\n\t" \
  "v_smfmac_f32_16x16x64_bf16 v[192:195], v[240:243], v[32:39], v251\n\t" \
  "ds_read_b128 v[208:211], v248 offset:15360\n\t" \
  "v_smfmac_f32_16x16x64_bf16 v[196:199], v[240:243], v[40:47], v251\n\t" \
  "s_waitcnt lgkmcnt(5)\n\t" \
  "ds_read_b128 v[212:215], v248 offset:0\n\t" \
  "v_smfmac_f32_16x16x64_bf16 v[192:195], v[244:247], v[216:223], v251\n\t" \
  "s_waitcnt lgkmcnt(4)\n\t" \
  "v_smfmac_f32_16x16x64_bf16 v[196:199], v[244:247], v[224:231], v251\n\t" \
  "s_waitcnt lgkmcnt(3)\n\t" \
  "ds_read_b128 v[236:239], v248 offset:1024\n\t" \
  "v_smfmac_f32_16x16x64_bf16 v[192:195], v[200:203], v[48:55], v251\n\t" \
  "ds_read_b128 v[240:243], v248 offset:2048\n\t" \
  "v_smfmac_f32_16x16x64_bf16 v[196:199], v[200:203], v[56:63], v251\n\t" \
  "s_waitcnt lgkmcnt(4)\n\t" \
  "v_smfmac_f32_16x16x64_bf16 v[192:195], v[204:207], v[64:71], v251\n\t" \
  "ds_read_b128 v[244:247], v248 offset:3072\n\t" \
  "v_smfmac_f32_16x16x64_bf16 v[196:199], v[204:207], v[72:79], v251\n\t" \
  "s_waitcnt lgkmcnt(4)\n\t" \
  "v_smfmac_f32_16x16x64_bf16 v[192:195], v[208:211], v[80:87], v251\n\t" \
  "ds_read_b128 v[200:203], v248 offset:4096\n\t" \
  "v_smfmac_f32_16x16x64_bf16 v[196:199], v[208:211], v[88:95], v251\n\t" \
  "s_waitcnt lgkmcnt(4)\n\t" \
  "v_smfmac_f32_16x16x64_bf16 v[192:195], v[212:215], v[96:103], v251\n\t" \
  "ds_read_b128 v[204:207], v248 offset:5120\n\t" \
  "v_smfmac_f32_16x16x64_bf16 v[196:199], v[212:215], v[104:111], v251\n\t" \
  "s_waitcnt lgkmcnt(3)\n\t" \
  "v_smfmac_f32_16x16x64_bf16 v[192:195], v[236:239], v[112:119], v251\n\t" \
  "v_smfmac_f32_16x16x64_bf16 v[196:199], v[236:239], v[120:127], v251\n\t" \
  "v_smfmac_f32_16x16x64_bf16 v[192:195], v[240:243], v[128:135], v251\n\t" \
  "v_smfmac_f32_16x16x64_bf16 v[196:199], v[240:243], v[136:143], v251\n\t" \
  "s_waitcnt lgkmcnt(1)\n\t" \
  "v_smfmac_f32_16x16x64_bf16 v[192:195], v[244:247], v[144:151], v251\n\t" \
  "v_smfmac_f32_16x16x64_bf16 v[196:199], v[244:247], v[152:159], v251\n\t" \
  "v_smfmac_f32_16x16x64_bf16 v[192:195], v[200:203], v[160:167], v251\n\t" \
  "v_smfmac_f32_16x16x64_bf16 v[196:199], v[200:203], v[168:175], v251\n\t" \
  "s_waitcnt lgkmcnt(0)\n\t" \
  "v_smfmac_f32_16x16x64_bf16 v[192:195], v[204:207], v[176:183], v251\n\t" \
  "v_smfmac_f32_16x16x64_bf16 v[196:199], v[204:207], v[184:191], v251\n\t" \
  "s_nop 6\n\t" \
  "v_add_f32 v192, v192, v194\n\t" \
  "v_add_f32 v193, v193, v195\n\t" \
  "v_add_f32 v196, v196, v198\n\t" \
  "v_add_f32 v197, v197, v199\n\t" \
  "v_cvt_pk_bf16_f32 v192, v192, v193\n\t" \
  "v_cvt_pk_bf16_f32 v196, v196, v197\n\t" \
  "ds_write2_b32 v252, v192, v196 offset1:64\n\t" \
  "s_waitcnt lgkmcnt(0)\n\t" \
  "s_barrier\n\t" \
  "buffer_store_dword v192, v235, %[rs], %[sk] offen sc1\n\t" \
  "buffer_store_dword v196, v235, %[rs], %[sk] offen offset:256 sc1\n\t" \
  "s_mov_b32 %[spins], s92\n\t" \
  "s_branch .Lend%=\n\t" \
  ".Lslow%=:\n\t" \
  "s_mov_b64 s[94:95], 0\n\t" \
  "v_cmp_eq_u32 vcc, %[sent], v200\n\t" \
  "s_or_b64 s[94:95], s[94:95], vcc\n\t" \
  "v_cmp_eq_u32 vcc, %[sent], v201\n\t" \
  "s_or_b64 s[94:95], s[94:95], vcc\n\t" \
  "v_cmp_eq_u32 vcc, %[sent], v202\n\t" \
  "s_or_b64 s[94:95], s[94:95], vcc\n\t" \
  "v_cmp_eq_u32 vcc, %[sent], v203\n\t" \
  "s_or_b64 s[94:95], s[94:95], vcc\n\t" \
  "v_cmp_eq_u32 vcc, %[sent], v204\n\t" \
  "s_or_b64 s[94:95], s[94:95], vcc\n\t" \
  "v_cmp_eq_u32 vcc, %[sent], v205\n\t" \
  "s_or_b64 s[94:95], s[94:95], vcc\n\t" \
  "v_cmp_eq_u32 vcc, %[sent], v206\n\t" \
  "s_or_b64 s[94:95], s[94:95], vcc\n\t" \
  "v_cmp_eq_u32 vcc, %[sent], v207\n\t" \
  "s_or_b64 s[94:95], s[94:95], vcc\n\t" \
  "s_cmp_eq_u64 s[94:95], 0\n\t" \
  "s_cbranch_scc1 .Lok%=\n\t" \
  "v_mov_b64 v[192:193], 0\n\t" \
  "v_mov_b64 v[194:195], 0\n\t" \
  "v_mov_b64 v[196:197], 0\n\t" \
  "v_mov_b64 v[198:199], 0\n\t" \
  "s_add_u32 s92, s92, 1\n\t" \
  "s_cmp_eq_u32 s92, 1\n\t" \
  "s_cbranch_scc1 .Lpoll%=\n\t" \
  "s_sleep 2\n\t" \
  "s_and_b32 s93, s92, 63\n\t" \
  "s_cmp_lg_u32 s93, 0\n\t" \
  "s_cbranch_scc1 .Lpoll%=\n\t" \
  "v_mov_b32 v245, 0\n\t" \
  "buffer_load_dword v244, v245, %[rs], %[sab] offen sc1\n\t" \
  "s_waitcnt vmcnt(0)\n\t" \
  "v_readfirstlane_b32 s93, v244\n\t" \
  "s_nop 3\n\t" \
  "s_cmp_eq_u32 s93, 1\n\t" \
  "s_cbranch_scc1 .Lok%=\n\t" \
  "s_cmp_lt_u32 s92, 0x80000\n\t" \
  "s_cbranch_scc1 .Lpoll%=\n\t" \
  "v_mov_b32 v244, 1\n\t" \
  "buffer_store_dword v244, v245, %[rs], %[sab] offen sc1\n\t" \
  "s_branch .Lok%=\n\t" \
  ".Lend%=:\n\t"
#define QK_ASM_7 \
  "s_waitcnt lgkmcnt(0)\n\t" \
  "v_subrev_u32 v244, 0x8000, v250\n\t" \
  "ds_read_b128 v[216:219], v248 offset:61440\n\t" \
  "ds_read_b128 v[220:223], v248 offset:62464\n\t" \
  "ds_read_b128 v[224:227], v248 offset:63488\n\t" \
  "ds_read_b128 v[228:231], v248 offset:64512\n\t" \
  "ds_read_b128 v[208:211], v244 offset:0\n\t" \
  "ds_read_b128 v[212:215], v244 offset:1024\n\t" \
  "ds_read_b128 v[236:239], v244 offset:2048\n\t" \
  "ds_read_b128 v[240:243], v244 offset:3072\n\t" \
  "v_mov_b64 v[192:193], 0\n\t" \
  "v_mov_b64 v[194:195], 0\n\t" \
  "v_mov_b64 v[196:197], 0\n\t" \
  "v_mov_b64 v[198:199], 0\n\t" \
  "s_movk_i32 s92, 0x2000\n\t" \
  ".Lwt%=:\n\t" \
  "ds_read_b64 v[200:201], v248 offset:30720\n\t" \
  "s_memrealtime s[90:91]\n\t" \
  "s_sub_u32 s92, s92, 1\n\t" \
  "s_waitcnt lgkmcnt(0)\n\t" \
  "v_readfirstlane_b32 s93, v200\n\t" \
  "v_readfirstlane_b32 %[tgt], v201\n\t" \
  "s_cbranch_scc1 .Lgo%=\n\t" \
  "s_cmp_eq_u32 s93, %[step]\n\t" \
  "s_cbranch_scc1 .Lwx%=\n\t" \
  "s_sleep 1\n\t" \
  "s_branch .Lwt%=\n\t" \
  ".Lwx%=:\n\t" \
  "s_lshl_b32 s93, s90, 2\n\t" \
  "s_sub_i32 s93, %[tgt], s93\n\t" \
  "s_cmp_le_i32 s93, 0\n\t" \
  "s_cbranch_scc1 .Lgo%=\n\t" \
  "s_min_i32 s93, s93, 8000\n\t" \
  "s_mov_b32 s94, s93\n\t" \
  ".Lsl%=:\n\t" \
  "s_sub_i32 s94, s94, 18\n\t" \
  "s_cmp_lt_i32 s94, 30\n\t" \
  "s_cbranch_scc1 .Lsx%=\n\t" \
  "s_sleep 1\n\t" \
  "s_branch .Lsl%=\n\t" \
  ".Lsx%=:\n\t" \
  "s_sub_i32 s93, s93, 12\n\t" \
  "s_max_i32 s93, s93, 4\n\t" \
  "s_lshr_b32 s93, s93, 2\n\t" \
  "s_add_u32 s93, s93, s90\n\t" \
  "s_memrealtime s[90:91]\n\t" \
  ".Lpace%=:\n\t" \
  "s_memrealtime s[94:95]\n\t" \
  "s_waitcnt lgkmcnt(1)\n\t" \
  "s_sub_i32 s92, s90, s93\n\t" \
  "s_cmp_ge_i32 s92, 0\n\t" \
  "s_cbranch_scc1 .Le%=\n\t" \
  "s_memrealtime s[90:91]\n\t" \
  "s_waitcnt lgkmcnt(1)\n\t" \
  "s_sub_i32 s92, s94, s93\n\t" \
  "s_cmp_lt_i32 s92, 0\n\t" \
  "s_cbranch_scc1 .Lpace%=\n\t" \
  "s_nop 4\n\t" \
  ".Le%=:\n\t" \
  "s_add_u32 s92, s92, s93\n\t" \
  "s_lshl_b32 s92, s92, 2\n\t" \
  "s_sub_i32 s92, %[tgt], s92\n\t" \
  "s_max_i32 s92, s92, 0\n\t" \
  "s_bitcmp0_b32 s92, 3\n\t" \
  "s_cbranch_scc1 .Lf3%=\n\t" \
  "s_nop 15\n\t" \
  ".Lf3%=:\n\t" \
  "s_bitcmp0_b32 s92, 2\n\t" \
  "s_cbranch_scc1 .Lf2%=\n\t" \
  "s_nop 10\n\t" \
  ".Lf2%=:\n\t" \
  ".Lgo%=:\n\t" \
  "s_mov_b32 s92, 0\n\t" \
  "s_memtime s[88:89]\n\t" \
  ".Lpoll%=:\n\t" \
  "buffer_load_dwordx4 v[200:203], v248, %[rs], %[som] offen sc1\n\t" \
  "buffer_load_dwordx4 v[204:207], v248, %[rs], %[som2] offen sc1\n\t" \
  "s_waitcnt vmcnt(1) lgkmcnt(0)\n\t" \
  "ds_write_b128 v249, v[200:203]\n\t" \
  "v_smfmac_f32_16x16x64_bf16 v[192:195], v[200:203], v[208:215], v251\n\t" \
  "v_max3_u32 v244, v200, v201, v202\n\t" \
  "v_smfmac_f32_16x16x64_bf16 v[196:199], v[200:203], v[236:243], v251\n\t" \
  "s_waitcnt vmcnt(0)\n\t" \
  "ds_write_b128 v249, v[204:207] offset:8192\n\t" \
  "v_smfmac_f32_16x16x64_bf16 v[192:195], v[204:207], v[216:223], v251\n\t" \
  "v_max3_u32 v244, v244, v203, v204\n\t" \
  "v_max3_u32 v244, v244, v205, v206\n\t" \
  "v_smfmac_f32_16x16x64_bf16 v[196:199], v[204:207], v[224:231], v251\n\t" \
  "v_max_u32 v244, v244, v207\n\t" \
  "v_cmp_le_u32 vcc, %[sent], v244\n\t" \
  "s_cbranch_vccnz .Lslow%=\n\t" \
  ".Lok%=:\n\t" \
  "ds_read_b128 v[216:219], v250 offset:32768\n\t" \
  "ds_read_b128 v[220:223], v250 offset:33792\n\t" \
  "ds_read_b128 v[224:227], v250 offset:34816\n\t" \
  "ds_read_b128 v[228:231], v250 offset:35840\n\t" \
  "s_waitcnt lgkmcnt(4)\n\t" \
  "s_setprio 3\n\t" \
  "s_barrier\n\t" \
  "s_memrealtime s[90:91]\n\t" \
  "s_memtime s[94:95]\n\t" \
  "v_mov_b32 v232, %[step]\n\t" \
  "v_lshrrev_b32 v234, 1, v248\n\t" \
  "s_mov_b64 exec, 0xffff\n\t" \
  "s_waitcnt lgkmcnt(0)\n\t" \
  "s_lshl_b32 s90, s90, 2\n\t" \
  "s_sub_u32 s94, s94, s88\n\t" \
  "s_mul_i32 s94, s94, 12200\n\t" \
  "s_lshr_b32 s94, s94, 16\n\t" \
  "s_add_u32 s94, s94, %[tgt]\n\t" \
  "s_sub_i32 s93, s90, s94\n\t" \
  "s_sub_i32 s93, s93, 30\n\t" \
  "s_max_i32 s93, s93, 0\n\t" \
  "s_add_u32 s90, s94, s93\n\t" \
  "s_sub_i32 s93, s90, %[tgt]\n\t" \
  "s_cmp_gt_i32 s93, 270\n\t" \
  "s_cselect_b32 s93, 70, 0\n\t" \
  "s_add_u32 s93, s93, s90\n\t" \
  "s_add_u32 s93, s93, %[lead]\n\t" \
  "v_mov_b32 v233, s93\n\t" \
  "buffer_store_dwordx2 v[232:233], v234, %[rs], %[sbc] offen sc1\n\t" \
  "s_mov_b64 exec, -1\n\t" \
  "s_mov_b32 %[tb1], s90\n\t" \
  "s_setprio 0\n\t" \
  "ds_read_b128 v[208:211], v248 offset:6144\n\t" \
  "ds_read_b128 v[212:215], v248 offset:9216\n\t" \
  "ds_read_b128 v[236:239], v248 offset:10240\n\t" \
  "ds_read_b128 v[240:243], v248 offset:11264\n\t" \
  "s_waitcnt lgkmcnt(3)\n\t" \
  "ds_read_b128 v[244:247], v248 offset:12288\n\t" \
  "v_smfmac_f32_16x16x64_bf16 v[192:195], v[208:211], v[216:223], v251\n\t" \
  "ds_read_b128 v[216:219], v250 offset:0\n\t" \
  "v_smfmac_f32_16x16x64_bf16 v[196:199], v[208:211], v[224:231], v251\n\t" \
  "s_waitcnt lgkmcnt(4)\n\t" \
  "ds_read_b128 v[220:223], v250 offset:1024\n\t" \
  "v_smfmac_f32_16x16x64_bf16 v[192:195], v[212:215], v[0:7], v251\n\t" \
  "ds_read_b128 v[224:227], v250 offset:2048\n\t" \
  "v_smfmac_f32_16x16x64_bf16 v[196:199], v[212:215], v[8:15], v251\n\t" \
  "s_waitcnt lgkmcnt(5)\n\t" \
  "ds_read_b128 v[228:231], v250 offset:3072\n\t" \
  "v_smfmac_f32_16x16x64_bf16 v[192:195], v[236:239], v[16:23], v251\n\t" \
  "ds_read_b128 v[200:203], v248 offset:13312\n\t" \
  "v_smfmac_f32_16x16x64_bf16 v[196:199], v[236:239], v[24:31], v251\n\t" \
  "s_waitcnt lgkmcnt(6)\n\t" \
  "ds_read_b128 v[204:207], v248 offset:14336\n\t" \
  "v_smfmac_f32_16x16x64_bf16 v[192:195], v[240:243], v[32:39], v251\n\t" \
  "ds_read_b128 v[208:211], v248 offset:0\n\t" \
  "v_smfmac_f32_16x16x64_bf16 v[196:199], v[240:243], v[40:47], v251\n\t" \
  "s_waitcnt lgkmcnt(5)\n\t" \
  "ds_read_b128 v[212:215], v248 offset:1024\n\t" \
  "v_smfmac_f32_16x16x64_bf16 v[192:195], v[244:247], v[216:223], v251\n\t" \
  "s_waitcnt lgkmcnt(4)\n\t" \
  "v_smfmac_f32_16x16x64_bf16 v[196:199], v[244:247], v[224:231], v251\n\t" \
  "s_waitcnt lgkmcnt(3)\n\t" \
  "ds_read_b128 v[236:239], v248 offset:2048\n\t" \
  "v_smfmac_f32_16x16x64_bf16 v[192:195], v[200:203], v[48:55], v251\n\t" \
  "ds_read_b128 v[240:243], v248 offset:3072\n\t" \
  "v_smfmac_f32_16x16x64_bf16 v[196:199], v[200:203], v[56:63], v251\n\t" \
  "s_waitcnt lgkmcnt(4)\n\t" \
  "v_smfmac_f32_16x16x64_bf16 v[192:195], v[204:207], v[64:71], v251\n\t" \
  "ds_read_b128 v[244:247], v248 offset:4096\n\t" \
  "v_smfmac_f32_16x16x64_bf16 v[196:199], v[204:207], v[72:79], v251\n\t" \
  "s_waitcnt lgkmcnt(4)\n\t" \
  "v_smfmac_f32_16x16x64_bf16 v[192:195], v[208:211], v[80:87], v251\n\t" \
  "ds_read_b128 v[200:203], v248 offset:5120\n\t" \
  "v_smfmac_f32_16x16x64_bf16 v[196:199], v[208:211], v[88:95], v251\n\t" \
  "s_waitcnt lgkmcnt(4)\n\t" \
  "v_smfmac_f32_16x16x64_bf16 v[192:195], v[212:215], v[96:103], v251\n\t" \
  "ds_read_b128 v[204:207], v248 offset:8192\n\t" \
  "v_smfmac_f32_16x16x64_bf16 v[196:199], v[212:215], v[104:111], v251\n\t" \
  "s_waitcnt lgkmcnt(3)\n\t" \
  "v_smfmac_f32_16x16x64_bf16 v[192:195], v[236:239], v[112:119], v251\n\t" \
  "v_smfmac_f32_16x16x64_bf16 v[196:199], v[236:239], v[120:127], v251\n\t" \
  "v_smfmac_f32_16x16x64_bf16 v[192:195], v[240:243], v[128:135], v251\n\t" \
  "v_smfmac_f32_16x16x64_bf16 v[196:199], v[240:243], v[136:143], v251\n\t" \
  "s_waitcnt lgkmcnt(1)\n\t" \
  "v_smfmac_f32_16x16x64_bf16 v[192:195], v[244:247], v[144:151], v251\n\t" \
  "v_smfmac_f32_16x16x64_bf16 v[196:199], v[244:247], v[152:159], v251\n\t" \
  "v_smfmac_f32_16x16x64_bf16 v[192:195], v[200:203], v[160:167], v251\n\t" \
  "v_smfmac_f32_16x16x64_bf16 v[196:199], v[200:203], v[168:175], v251\n\t" \
  "s_waitcnt lgkmcnt(0)\n\t" \
  "v_smfmac_f32_16x16x64_bf16 v[192:195], v[204:207], v[176:183], v251\n\t" \
  "v_smfmac_f32_16x16x64_bf16 v[196:199], v[204:207], v[184:191], v251\n\t" \
  "s_nop 6\n\t" \
  "v_add_f32 v192, v192, v194\n\t" \
  "v_add_f32 v193, v193, v195\n\t" \
  "v_add_f32 v196, v196, v198\n\t" \
  "v_add_f32 v197, v197, v199\n\t" \
  "v_cvt_pk_bf16_f32 v192, v192, v193\n\t" \
  "v_cvt_pk_bf16_f32 v196, v196, v197\n\t" \
  "ds_write2_b32 v252, v192, v196 offset1:64\n\t" \
  "s_waitcnt lgkmcnt(0)\n\t" \
  "s_barrier\n\t" \
  "buffer_store_dword v192, v235, %[rs], %[sk] offen sc1\n\t" \
  "buffer_store_dword v196, v235, %[rs], %[sk] offen offset:256 sc1\n\t" \
  "s_mov_b32 %[spins], s92\n\t" \
  "s_branch .Lend%=\n\t" \
  ".Lslow%=:\n\t" \
  "s_mov_b64 s[94:95], 0\n\t" \
  "v_cmp_eq_u32 vcc, %[sent], v200\n\t" \
  "s_or_b64 s[94:95], s[94:95], vcc\n\t" \
  "v_cmp_eq_u32 vcc, %[sent], v201\n\t" \
  "s_or_b64 s[94:95], s[94:95], vcc\n\t" \
  "v_cmp_eq_u32 vcc, %[sent], v202\n\t" \
  "s_or_b64 s[94:95], s[94:95], vcc\n\t" \
  "v_cmp_eq_u32 vcc, %[sent], v203\n\t" \
  "s_or_b64 s[94:95], s[94:95], vcc\n\t" \
  "v_cmp_eq_u32 vcc, %[sent], v204\n\t" \
  "s_or_b64 s[94:95], s[94:95], vcc\n\t" \
  "v_cmp_eq_u32 vcc, %[sent], v205\n\t" \
  "s_or_b64 s[94:95], s[94:95], vcc\n\t" \
  "v_cmp_eq_u32 vcc, %[sent], v206\n\t" \
  "s_or_b64 s[94:95], s[94:95], vcc\n\t" \
  "v_cmp_eq_u32 vcc, %[sent], v207\n\t" \
  "s_or_b64 s[94:95], s[94:95], vcc\n\t" \
  "s_cmp_eq_u64 s[94:95], 0\n\t" \
  "s_cbranch_scc1 .Lok%=\n\t" \
  "v_mov_b64 v[192:193], 0\n\t" \
  "v_mov_b64 v[194:195], 0\n\t" \
  "v_mov_b64 v[196:197], 0\n\t" \
  "v_mov_b64 v[198:199], 0\n\t" \
  "s_add_u32 s92, s92, 1\n\t" \
  "s_cmp_eq_u32 s92, 1\n\t" \
  "s_cbranch_scc1 .Lpoll%=\n\t" \
  "s_sleep 2\n\t" \
  "s_and_b32 s93, s92, 63\n\t" \
  "s_cmp_lg_u32 s93, 0\n\t" \
  "s_cbranch_scc1 .Lpoll%=\n\t" \
  "v_mov_b32 v245, 0\n\t" \
  "buffer_load_dword v244, v245, %[rs], %[sab] offen sc1\n\t" \
  "s_waitcnt vmcnt(0)\n\t" \
  "v_readfirstlane_b32 s93, v244\n\t" \
  "s_nop 3\n\t" \
  "s_cmp_eq_u32 s93, 1\n\t" \
  "s_cbranch_scc1 .Lok%=\n\t" \
  "s_cmp_lt_u32 s92, 0x80000\n\t" \
  "s_cbranch_scc1 .Lpoll%=\n\t" \
  "v_mov_b32 v244, 1\n\t" \
  "buffer_store_dword v244, v245, %[rs], %[sab] offen sc1\n\t" \
  "s_branch .Lok%=\n\t" \
  ".Lend%=:\n\t"
#define VV_ASM \
  "s_waitcnt lgkmcnt(0)\n\t" \
  "v_mov_b64 v[128:129], 0\n\t" \
  "v_mov_b64 v[130:131], 0\n\t" \
  "s_movk_i32 s92, 0x2000\n\t" \
  ".Lwt%=:\n\t" \
  "ds_read_b64 v[132:133], v200 offset:30720\n\t" \
  "s_memrealtime s[90:91]\n\t" \
  "s_sub_u32 s92, s92, 1\n\t" \
  "s_waitcnt lgkmcnt(0)\n\t" \
  "v_readfirstlane_b32 s93, v132\n\t" \
  "v_readfirstlane_b32 %[tgt], v133\n\t" \
  "s_cbranch_scc1 .Lgo%=\n\t" \
  "s_cmp_eq_u32 s93, %[step]\n\t" \
  "s_cbranch_scc1 .Lwx%=\n\t" \
  "s_sleep 1\n\t" \
  "s_branch .Lwt%=\n\t" \
  ".Lwx%=:\n\t" \
  "s_lshl_b32 s93, s90, 2\n\t" \
  "s_sub_i32 s93, %[tgt], s93\n\t" \
  "s_cmp_le_i32 s93, 0\n\t" \
  "s_cbranch_scc1 .Lgo%=\n\t" \
  "s_min_i32 s93, s93, 8000\n\t" \
  "s_mov_b32 s94, s93\n\t" \
  ".Lsl%=:\n\t" \
  "s_sub_i32 s94, s94, 18\n\t" \
  "s_cmp_lt_i32 s94, 30\n\t" \
  "s_cbranch_scc1 .Lsx%=\n\t" \
  "s_sleep 1\n\t" \
  "s_branch .Lsl%=\n\t" \
  ".Lsx%=:\n\t" \
  "s_sub_i32 s93, s93, 12\n\t" \
  "s_max_i32 s93, s93, 4\n\t" \
  "s_lshr_b32 s93, s93, 2\n\t" \
  "s_add_u32 s93, s93, s90\n\t" \
  "s_memrealtime s[90:91]\n\t" \
  ".Lpace%=:\n\t" \
  "s_memrealtime s[94:95]\n\t" \
  "s_waitcnt lgkmcnt(1)\n\t" \
  "s_sub_i32 s92, s90, s93\n\t" \
  "s_cmp_ge_i32 s92, 0\n\t" \
  "s_cbranch_scc1 .Le%=\n\t" \
  "s_memrealtime s[90:91]\n\t" \
  "s_waitcnt lgkmcnt(1)\n\t" \
  "s_sub_i32 s92, s94, s93\n\t" \
  "s_cmp_lt_i32 s92, 0\n\t" \
  "s_cbranch_scc1 .Lpace%=\n\t" \
  "s_nop 4\n\t" \
  ".Le%=:\n\t" \
  "s_add_u32 s92, s92, s93\n\t" \
  "s_lshl_b32 s92, s92, 2\n\t" \
  "s_sub_i32 s92, %[tgt], s92\n\t" \
  "s_max_i32 s92, s92, 0\n\t" \
  "s_bitcmp0_b32 s92, 3\n\t" \
  "s_cbranch_scc1 .Lf3%=\n\t" \
  "s_nop 15\n\t" \
  ".Lf3%=:\n\t" \
  "s_bitcmp0_b32 s92, 2\n\t" \
  "s_cbranch_scc1 .Lf2%=\n\t" \
  "s_nop 10\n\t" \
  ".Lf2%=:\n\t" \
  ".Lgo%=:\n\t" \
  "s_mov_b32 s92, 0\n\t" \
  ".Lpoll%=:\n\t" \
  "buffer_load_dwordx4 v[132:135], v200, %[rs], %[som] offen sc1\n\t" \
  "buffer_load_dwordx4 v[136:139], v200, %[rs], %[som2] offen sc1\n\t" \
  "s_waitcnt vmcnt(1) lgkmcnt(0)\n\t" \
  "ds_write_b128 v232, v[132:135]\n\t" \
  "v_smfmac_f32_16x16x64_bf16 v[128:131], v[132:135], v[0:7], v202\n\t" \
  "v_max3_u32 v196, v132, v133, v134\n\t" \
  "s_waitcnt vmcnt(0)\n\t" \
  "ds_write_b128 v232, v[136:139] offset:8192\n\t" \
  "v_smfmac_f32_16x16x64_bf16 v[128:131], v[136:139], v[64:71], v202\n\t" \
  "v_max3_u32 v196, v196, v135, v136\n\t" \
  "v_max3_u32 v196, v196, v137, v138\n\t" \
  "v_max_u32 v196, v196, v139\n\t" \
  "v_cmp_le_u32 vcc, %[sent], v196\n\t" \
  "s_cbranch_vccnz .Lslow%=\n\t" \
  ".Lok%=:\n\t" \
  "s_waitcnt lgkmcnt(0)\n\t" \
  "s_barrier\n\t" \
  "ds_read_b128 v[164:167], v233 offset:0\n\t" \
  "ds_read_b128 v[140:143], v234 offset:0\n\t" \
  "ds_read_b128 v[144:147], v235 offset:0\n\t" \
  "ds_read_b128 v[148:151], v236 offset:0\n\t" \
  "ds_read_b128 v[152:155], v237 offset:0\n\t" \
  "s_waitcnt lgkmcnt(4)\n\t" \
  "ds_read_b128 v[156:159], v238 offset:0\n\t" \
  "v_smfmac_f32_16x16x64_bf16 v[128:131], v[164:167], v[8:15], v202\n\t" \
  "s_waitcnt lgkmcnt(4)\n\t" \
  "ds_read_b128 v[160:163], v239 offset:0\n\t" \
  "v_smfmac_f32_16x16x64_bf16 v[128:131], v[140:143], v[16:23], v202\n\t" \
  "s_waitcnt lgkmcnt(4)\n\t" \
  "ds_read_b128 v[168:171], v233 offset:8192\n\t" \
  "v_smfmac_f32_16x16x64_bf16 v[128:131], v[144:147], v[24:31], v202\n\t" \
  "s_waitcnt lgkmcnt(4)\n\t" \
  "ds_read_b128 v[172:175], v234 offset:8192\n\t" \
  "v_smfmac_f32_16x16x64_bf16 v[128:131], v[148:151], v[32:39], v202\n\t" \
  "s_waitcnt lgkmcnt(4)\n\t" \
  "ds_read_b128 v[176:179], v235 offset:8192\n\t" \
  "v_smfmac_f32_16x16x64_bf16 v[128:131], v[152:155], v[40:47], v202\n\t" \
  "s_waitcnt lgkmcnt(4)\n\t" \
  "ds_read_b128 v[180:183], v236 offset:8192\n\t" \
  "v_smfmac_f32_16x16x64_bf16 v[128:131], v[156:159], v[48:55], v202\n\t" \
  "s_waitcnt lgkmcnt(4)\n\t" \
  "ds_read_b128 v[184:187], v237 offset:8192\n\t" \
  "v_smfmac_f32_16x16x64_bf16 v[128:131], v[160:163], v[56:63], v202\n\t" \
  "s_waitcnt lgkmcnt(4)\n\t" \
  "ds_read_b128 v[188:191], v238 offset:8192\n\t" \
  "v_smfmac_f32_16x16x64_bf16 v[128:131], v[168:171], v[72:79], v202\n\t" \
  "s_waitcnt lgkmcnt(4)\n\t" \
  "ds_read_b128 v[192:195], v239 offset:8192\n\t" \
  "v_smfmac_f32_16x16x64_bf16 v[128:131], v[172:175], v[80:87], v202\n\t" \
  "s_waitcnt lgkmcnt(4)\n\t" \
  "v_smfmac_f32_16x16x64_bf16 v[128:131], v[176:179], v[88:95], v202\n\t" \
  "s_waitcnt lgkmcnt(3)\n\t" \
  "v_smfmac_f32_16x16x64_bf16 v[128:131], v[180:183], v[96:103], v202\n\t" \
  "s_waitcnt lgkmcnt(2)\n\t" \
  "v_smfmac_f32_16x16x64_bf16 v[128:131], v[184:187], v[104:111], v202\n\t" \
  "s_waitcnt lgkmcnt(1)\n\t" \
  "v_smfmac_f32_16x16x64_bf16 v[128:131], v[188:191], v[112:119], v202\n\t" \
  "s_waitcnt lgkmcnt(0)\n\t" \
  "v_smfmac_f32_16x16x64_bf16 v[128:131], v[192:195], v[120:127], v202\n\t" \
  "s_nop 7\n\t" \
  "v_add_f32 v128, v128, v130\n\t" \
  "v_add_f32 v129, v129, v131\n\t" \
  "v_cvt_pk_bf16_f32 v128, v128, v129\n\t" \
  "buffer_store_dword v128, v203, %[rs], %[sv] offen sc1\n\t" \
  "ds_write_b32 v204, v128\n\t" \
  "s_waitcnt lgkmcnt(0)\n\t" \
  "s_barrier\n\t" \
  "s_mov_b32 %[spins], s92\n\t" \
  "s_branch .Lend%=\n\t" \
  ".Lslow%=:\n\t" \
  "s_mov_b64 s[94:95], 0\n\t" \
  "v_cmp_eq_u32 vcc, %[sent], v132\n\t" \
  "s_or_b64 s[94:95], s[94:95], vcc\n\t" \
  "v_cmp_eq_u32 vcc, %[sent], v133\n\t" \
  "s_or_b64 s[94:95], s[94:95], vcc\n\t" \
  "v_cmp_eq_u32 vcc, %[sent], v134\n\t" \
  "s_or_b64 s[94:95], s[94:95], vcc\n\t" \
  "v_cmp_eq_u32 vcc, %[sent], v135\n\t" \
  "s_or_b64 s[94:95], s[94:95], vcc\n\t" \
  "v_cmp_eq_u32 vcc, %[sent], v136\n\t" \
  "s_or_b64 s[94:95], s[94:95], vcc\n\t" \
  "v_cmp_eq_u32 vcc, %[sent], v137\n\t" \
  "s_or_b64 s[94:95], s[94:95], vcc\n\t" \
  "v_cmp_eq_u32 vcc, %[sent], v138\n\t" \
  "s_or_b64 s[94:95], s[94:95], vcc\n\t" \
  "v_cmp_eq_u32 vcc, %[sent], v139\n\t" \
  "s_or_b64 s[94:95], s[94:95], vcc\n\t" \
  "s_cmp_eq_u64 s[94:95], 0\n\t" \
  "s_cbranch_scc1 .Lok%=\n\t" \
  "v_mov_b64 v[128:129], 0\n\t" \
  "v_mov_b64 v[130:131], 0\n\t" \
  "s_add_u32 s92, s92, 1\n\t" \
  "s_cmp_eq_u32 s92, 1\n\t" \
  "s_cbranch_scc1 .Lpoll%=\n\t" \
  "s_sleep 2\n\t" \
  "s_and_b32 s93, s92, 63\n\t" \
  "s_cmp_lg_u32 s93, 0\n\t" \
  "s_cbranch_scc1 .Lpoll%=\n\t" \
  "v_mov_b32 v197, 0\n\t" \
  "buffer_load_dword v196, v197, %[rs], %[sab] offen sc1\n\t" \
  "s_waitcnt vmcnt(0)\n\t" \
  "v_readfirstlane_b32 s93, v196\n\t" \
  "s_nop 3\n\t" \
  "s_cmp_eq_u32 s93, 1\n\t" \
  "s_cbranch_scc1 .Lok%=\n\t" \
  "s_cmp_lt_u32 s92, 0x80000\n\t" \
  "s_cbranch_scc1 .Lpoll%=\n\t" \
  "v_mov_b32 v196, 1\n\t" \
  "buffer_store_dword v196, v197, %[rs], %[sab] offen sc1\n\t" \
  "s_branch .Lok%=\n\t" \
  ".Lend%=:\n\t"
#define TK_ASM \
  "buffer_load_dwordx4 v[224:227], v200, %[rs], %[sk] offen offset:0 sc1\n\t" \
  "buffer_load_dwordx4 v[228:231], v200, %[rs], %[sk] offen offset:1024 sc1\n\t" \
  "s_memrealtime s[88:89]\n\t" \
  "v_add_u32 v205, %[tl], v200\n\t" \
  "v_add_u32 v206, %[tq], v205\n\t" \
  "v_add_u32 v207, %[tv], v205\n\t" \
  "s_waitcnt lgkmcnt(0)\n\t" \
  "ds_read_b128 v[128:131], v206 offset:8192\n\t" \
  "ds_read_b128 v[144:147], v205 offset:0\n\t" \
  "ds_read_b128 v[160:163], v205 offset:4096\n\t" \
  "ds_read_b128 v[132:135], v206 offset:9216\n\t" \
  "ds_read_b128 v[148:151], v205 offset:1024\n\t" \
  "ds_read_b128 v[164:167], v205 offset:5120\n\t" \
  "ds_read_b128 v[136:139], v206 offset:10240\n\t" \
  "ds_read_b128 v[152:155], v205 offset:2048\n\t" \
  "ds_read_b128 v[168:171], v205 offset:6144\n\t" \
  "ds_read_b128 v[140:143], v206 offset:11264\n\t" \
  "ds_read_b128 v[156:159], v205 offset:3072\n\t" \
  "ds_read_b128 v[172:175], v205 offset:7168\n\t" \
  "v_add_u32 v206, %[vp], v218\n\t" \
  "s_lshl_b32 s88, s88, 2\n\t" \
  "s_add_u32 s89, s88, %[od]\n\t" \
  "s_waitcnt lgkmcnt(10)\n\t" \
  "v_mfma_f32_16x16x32_bf16 v[196:199], v[144:147], v[128:131], 0\n\t" \
  "s_waitcnt lgkmcnt(9)\n\t" \
  "v_mfma_f32_16x16x32_bf16 v[144:147], v[160:163], v[128:131], 0\n\t" \
  "s_waitcnt lgkmcnt(7)\n\t" \
  "v_mfma_f32_16x16x32_bf16 v[196:199], v[148:151], v[132:135], v[196:199]\n\t" \
  "s_waitcnt lgkmcnt(6)\n\t" \
  "v_mfma_f32_16x16x32_bf16 v[144:147], v[164:167], v[132:135], v[144:147]\n\t" \
  "s_waitcnt lgkmcnt(4)\n\t" \
  "v_mfma_f32_16x16x32_bf16 v[196:199], v[152:155], v[136:139], v[196:199]\n\t" \
  "s_waitcnt lgkmcnt(3)\n\t" \
  "v_mfma_f32_16x16x32_bf16 v[144:147], v[168:171], v[136:139], v[144:147]\n\t" \
  "s_waitcnt lgkmcnt(1)\n\t" \
  "v_mfma_f32_16x16x32_bf16 v[196:199], v[156:159], v[140:143], v[196:199]\n\t" \
  "s_waitcnt lgkmcnt(0)\n\t" \
  "v_mfma_f32_16x16x32_bf16 v[144:147], v[172:175], v[140:143], v[144:147]\n\t" \
  "ds_read_b128 v[176:179], v207 offset:16384\n\t" \
  "ds_read_b128 v[180:183], v207 offset:17408\n\t" \
  "ds_read_b128 v[184:187], v207 offset:18432\n\t" \
  "ds_read_b128 v[188:191], v207 offset:19456\n\t" \
  "ds_read2st64_b32 v[192:193], v206 offset0:0 offset1:1\n\t" \
  "ds_read2st64_b32 v[194:195], v206 offset0:2 offset1:3\n\t" \
  "v_cvt_pk_bf16_f32 v197, v196, v197\n\t" \
  "v_cvt_pk_bf16_f32 v199, v198, v199\n\t" \
  "v_cvt_pk_bf16_f32 v145, v144, v145\n\t" \
  "v_cvt_pk_bf16_f32 v147, v146, v147\n\t" \
  "v_lshlrev_b32 v196, 16, v197\n\t" \
  "v_lshlrev_b32 v198, 16, v199\n\t" \
  "v_lshlrev_b32 v144, 16, v145\n\t" \
  "v_lshlrev_b32 v146, 16, v147\n\t" \
  "v_and_b32 v197, 0xffff0000, v197\n\t" \
  "v_and_b32 v199, 0xffff0000, v199\n\t" \
  "v_and_b32 v145, 0xffff0000, v145\n\t" \
  "v_and_b32 v147, 0xffff0000, v147\n\t" \
  "v_pk_mul_f32 v[196:197], v[196:197], %[sc] op_sel_hi:[1,0]\n\t" \
  "v_pk_mul_f32 v[198:199], v[198:199], %[sc] op_sel_hi:[1,0]\n\t" \
  "v_pk_mul_f32 v[144:145], v[144:145], %[sc] op_sel_hi:[1,0]\n\t" \
  "v_pk_mul_f32 v[146:147], v[146:147], %[sc] op_sel_hi:[1,0]\n\t" \
  "v_cvt_pk_bf16_f32 v197, v196, v197\n\t" \
  "v_cvt_pk_bf16_f32 v199, v198, v199\n\t" \
  "v_cvt_pk_bf16_f32 v145, v144, v145\n\t" \
  "v_cvt_pk_bf16_f32 v147, v146, v147\n\t" \
  "v_lshlrev_b32 v196, 16, v197\n\t" \
  "v_lshlrev_b32 v198, 16, v199\n\t" \
  "v_lshlrev_b32 v144, 16, v145\n\t" \
  "v_lshlrev_b32 v146, 16, v147\n\t" \
  "v_and_b32 v197, 0xffff0000, v197\n\t" \
  "v_and_b32 v199, 0xffff0000, v199\n\t" \
  "v_and_b32 v145, 0xffff0000, v145\n\t" \
  "v_and_b32 v147, 0xffff0000, v147\n\t" \
  "v_min_f32 v196, v196, v208\n\t" \
  "v_min_f32 v197, v197, v209\n\t" \
  "v_min_f32 v198, v198, v210\n\t" \
  "v_min_f32 v199, v199, v211\n\t" \
  "v_min_f32 v144, v144, v212\n\t" \
  "v_min_f32 v145, v145, v213\n\t" \
  "v_min_f32 v146, v146, v214\n\t" \
  "v_min_f32 v147, v147, v215\n\t" \
  "v_max3_f32 v148, v196, v197, v198\n\t" \
  "v_max3_f32 v149, v199, v144, v145\n\t" \
  "v_max3_f32 v148, v148, v146, v147\n\t" \
  "v_max_f32 v150, v148, v149\n\t" \
  "v_max_f32 v151, v148, v149\n\t" \
  "s_nop 1\n\t" \
  "v_permlane32_swap_b32 v150, v151\n\t" \
  "v_max_f32 v150, v150, v151\n\t" \
  "s_mov_b32 s92, 0\n\t" \
  ".Ltake%=:\n\t" \
  "s_waitcnt vmcnt(1) lgkmcnt(0)\n\t" \
  "ds_write_b128 v216, v[224:227]\n\t" \
  "v_max3_u32 v148, v224, v225, v226\n\t" \
  "s_waitcnt vmcnt(0)\n\t" \
  "ds_write_b128 v216, v[228:231] offset:1024\n\t" \
  "ds_read_b64_tr_b16 v[160:161], v217 offset:0\n\t" \
  "ds_read_b64_tr_b16 v[162:163], v217 offset:64\n\t" \
  "ds_read_b64_tr_b16 v[164:165], v217 offset:512\n\t" \
  "ds_read_b64_tr_b16 v[166:167], v217 offset:576\n\t" \
  "ds_read_b64_tr_b16 v[168:169], v217 offset:1024\n\t" \
  "ds_read_b64_tr_b16 v[170:171], v217 offset:1088\n\t" \
  "ds_read_b64_tr_b16 v[172:173], v217 offset:1536\n\t" \
  "ds_read_b64_tr_b16 v[174:175], v217 offset:1600\n\t" \
  "v_max3_u32 v148, v148, v227, v228\n\t" \
  "v_max3_u32 v148, v148, v229, v230\n\t" \
  "v_max_u32 v148, v148, v231\n\t" \
  "v_cmp_le_u32 vcc, %[sent], v148\n\t" \
  "s_cbranch_vccnz .Lslow%=\n\t" \
  ".Lok%=:\n\t" \
  "s_waitcnt lgkmcnt(6)\n\t" \
  "v_mfma_f32_16x16x32_bf16 v[152:155], v[160:163], v[128:131], 0\n\t" \
  "s_waitcnt lgkmcnt(4)\n\t" \
  "v_mfma_f32_16x16x32_bf16 v[152:155], v[164:167], v[132:135], v[152:155]\n\t" \
  "s_waitcnt lgkmcnt(2)\n\t" \
  "v_mfma_f32_16x16x32_bf16 v[152:155], v[168:171], v[136:139], v[152:155]\n\t" \
  "s_waitcnt lgkmcnt(0)\n\t" \
  "v_mfma_f32_16x16x32_bf16 v[152:155], v[172:175], v[140:143], v[152:155]\n\t" \
  "v_mov_b32 v157, 0\n\t" \
  "v_mov_b32 v158, 0\n\t" \
  "s_nop 5\n\t" \
  "v_cvt_pk_bf16_f32 v153, v152, v153\n\t" \
  "v_cvt_pk_bf16_f32 v155, v154, v155\n\t" \
  "v_lshlrev_b32 v152, 16, v153\n\t" \
  "v_lshlrev_b32 v154, 16, v155\n\t" \
  "v_and_b32 v153, 0xffff0000, v153\n\t" \
  "v_and_b32 v155, 0xffff0000, v155\n\t" \
  "v_pk_mul_f32 v[152:153], v[152:153], %[sc] op_sel_hi:[1,0]\n\t" \
  "v_pk_mul_f32 v[154:155], v[154:155], %[sc] op_sel_hi:[1,0]\n\t" \
  "v_cvt_pk_bf16_f32 v153, v152, v153\n\t" \
  "v_cvt_pk_bf16_f32 v155, v154, v155\n\t" \
  "v_lshlrev_b32 v152, 16, v153\n\t" \
  "v_lshlrev_b32 v154, 16, v155\n\t" \
  "v_and_b32 v153, 0xffff0000, v153\n\t" \
  "v_and_b32 v155, 0xffff0000, v155\n\t" \
  "v_max3_f32 v148, v152, v153, v150\n\t" \
  "v_max3_f32 v149, v148, v154, v155\n\t" \
  "v_max3_f32 v150, v148, v154, v155\n\t" \
  "v_cndmask_b32 v152, v152, v154, %[up]\n\t" \
  "v_cndmask_b32 v153, v153, v155, %[up]\n\t" \
  "v_permlane16_swap_b32 v149, v150\n\t" \
  "v_max_f32 v148, v149, v150\n\t" \
  "v_mul_f32 v148, %[negc], v148\n\t" \
  "v_fma_f32 v152, v152, %[c], v148\n\t" \
  "v_fma_f32 v153, v153, %[c], v148\n\t" \
  "v_exp_f32 v152, v152\n\t" \
  "v_fma_f32 v196, v196, %[c], v148\n\t" \
  "v_exp_f32 v153, v153\n\t" \
  "v_fma_f32 v197, v197, %[c], v148\n\t" \
  "v_exp_f32 v196, v196\n\t" \
  "v_fma_f32 v198, v198, %[c], v148\n\t" \
  "v_exp_f32 v197, v197\n\t" \
  "v_fma_f32 v199, v199, %[c], v148\n\t" \
  "v_exp_f32 v198, v198\n\t" \
  "v_fma_f32 v144, v144, %[c], v148\n\t" \
  "v_pk_add_f32 v[132:133], v[152:153], v[196:197]\n\t" \
  "v_exp_f32 v199, v199\n\t" \
  "v_fma_f32 v145, v145, %[c], v148\n\t" \
  "v_exp_f32 v144, v144\n\t" \
  "v_fma_f32 v146, v146, %[c], v148\n\t" \
  "v_pk_add_f32 v[132:133], v[132:133], v[198:199]\n\t" \
  "v_exp_f32 v145, v145\n\t" \
  "v_fma_f32 v147, v147, %[c], v148\n\t" \
  "v_exp_f32 v146, v146\n\t" \
  "v_pk_add_f32 v[132:133], v[132:133], v[144:145]\n\t" \
  "v_exp_f32 v147, v147\n\t" \
  "s_nop 0\n\t" \
  "v_pk_add_f32 v[132:133], v[132:133], v[146:147]\n\t" \
  "v_add_f32 v134, v132, v133\n\t" \
  "v_add_f32 v135, v132, v133\n\t" \
  "s_nop 1\n\t" \
  "v_permlane16_swap_b32 v134, v135\n\t" \
  "v_add_f32 v132, v134, v135\n\t" \
  "v_add_f32 v133, v134, v135\n\t" \
  "s_nop 1\n\t" \
  "v_permlane32_swap_b32 v132, v133\n\t" \
  "v_add_f32 v132, v132, v133\n\t" \
  "v_rcp_f32 v148, v132\n\t" \
  "s_nop 0\n\t" \
  "v_pk_mul_f32 v[152:153], v[152:153], v[148:149] op_sel_hi:[1,0]\n\t" \
  "v_pk_mul_f32 v[196:197], v[196:197], v[148:149] op_sel_hi:[1,0]\n\t" \
  "v_pk_mul_f32 v[198:199], v[198:199], v[148:149] op_sel_hi:[1,0]\n\t" \
  "v_pk_mul_f32 v[144:145], v[144:145], v[148:149] op_sel_hi:[1,0]\n\t" \
  "v_pk_mul_f32 v[146:147], v[146:147], v[148:149] op_sel_hi:[1,0]\n\t" \
  "v_cvt_pk_bf16_f32 v156, v152, v153\n\t" \
  "v_cvt_pk_bf16_f32 v159, v152, v153\n\t" \
  "v_cvt_pk_bf16_f32 v128, v196, v197\n\t" \
  "v_cvt_pk_bf16_f32 v129, v198, v199\n\t" \
  "v_mfma_f32_16x16x16_bf16 v[160:163], v[192:193], v[156:157], 0\n\t" \
  "v_cvt_pk_bf16_f32 v130, v144, v145\n\t" \
  "v_mfma_f32_16x16x16_bf16 v[164:167], v[192:193], v[158:159], 0\n\t" \
  "v_cvt_pk_bf16_f32 v131, v146, v147\n\t" \
  "v_mfma_f32_16x16x16_bf16 v[168:171], v[194:195], v[156:157], 0\n\t" \
  "v_mfma_f32_16x16x16_bf16 v[172:175], v[194:195], v[158:159], 0\n\t" \
  "s_nop 0\n\t" \
  "v_mfma_f32_16x16x32_bf16 v[160:163], v[176:179], v[128:131], v[160:163]\n\t" \
  "s_nop 0\n\t" \
  "v_mfma_f32_16x16x32_bf16 v[164:167], v[180:183], v[128:131], v[164:167]\n\t" \
  "s_nop 0\n\t" \
  "v_mfma_f32_16x16x32_bf16 v[168:171], v[184:187], v[128:131], v[168:171]\n\t" \
  "v_mfma_f32_16x16x32_bf16 v[172:175], v[188:191], v[128:131], v[172:175]\n\t" \
  "s_nop 2\n\t" \
  "v_cvt_pk_bf16_f32 v136, v160, v161\n\t" \
  "v_cvt_pk_bf16_f32 v137, v162, v163\n\t" \
  "v_cvt_pk_bf16_f32 v138, v164, v165\n\t" \
  "v_cvt_pk_bf16_f32 v139, v166, v167\n\t" \
  "v_cvt_pk_bf16_f32 v140, v168, v169\n\t" \
  "v_cvt_pk_bf16_f32 v141, v170, v171\n\t" \
  "v_cvt_pk_bf16_f32 v142, v172, v173\n\t" \
  "v_cvt_pk_bf16_f32 v143, v174, v175\n\t" \
  "s_memrealtime s[90:91]\n\t" \
  "s_waitcnt lgkmcnt(0)\n\t" \
  "s_lshl_b32 s93, s90, 2\n\t" \
  "s_sub_i32 s93, s89, s93\n\t" \
  "s_cmp_le_i32 s93, 0\n\t" \
  "s_cbranch_scc1 .Lgoo%=\n\t" \
  "s_min_i32 s93, s93, 8000\n\t" \
  "s_mov_b32 s94, s93\n\t" \
  ".Lslo%=:\n\t" \
  "s_sub_i32 s94, s94, 18\n\t" \
  "s_cmp_lt_i32 s94, 30\n\t" \
  "s_cbranch_scc1 .Lsxo%=\n\t" \
  "s_sleep 1\n\t" \
  "s_branch .Lslo%=\n\t" \
  ".Lsxo%=:\n\t" \
  "s_add_u32 s93, s93, 3\n\t" \
  "s_lshr_b32 s93, s93, 2\n\t" \
  "s_add_u32 s93, s93, s90\n\t" \
  "s_memrealtime s[90:91]\n\t" \
  ".Lpaceo%=:\n\t" \
  "s_memrealtime s[94:95]\n\t" \
  "s_waitcnt lgkmcnt(1)\n\t" \
  "s_sub_i32 s92, s90, s93\n\t" \
  "s_cmp_ge_i32 s92, 0\n\t" \
  "s_cbranch_scc1 .Leo%=\n\t" \
  "s_memrealtime s[90:91]\n\t" \
  "s_waitcnt lgkmcnt(1)\n\t" \
  "s_sub_i32 s92, s94, s93\n\t" \
  "s_cmp_lt_i32 s92, 0\n\t" \
  "s_cbranch_scc1 .Lpaceo%=\n\t" \
  ".Leo%=:\n\t" \
  ".Lgoo%=:\n\t" \
  "buffer_store_dwordx4 v[136:139], v200, %[rt], %[so] offen offset:0 sc1\n\t" \
  "buffer_store_dwordx4 v[140:143], v200, %[rt], %[so] offen offset:1024 sc1\n\t" \
  "s_waitcnt lgkmcnt(0)\n\t" \
  "s_branch .Lend%=\n\t" \
  ".Lpoll%=:\n\t" \
  "buffer_load_dwordx4 v[224:227], v200, %[rs], %[sk] offen offset:0 sc1\n\t" \
  "buffer_load_dwordx4 v[228:231], v200, %[rs], %[sk] offen offset:1024 sc1\n\t" \
  "s_branch .Ltake%=\n\t" \
  "s_branch .Lend%=\n\t" \
  ".Lslow%=:\n\t" \
  "s_mov_b64 s[94:95], 0\n\t" \
  "v_cmp_eq_u32 vcc, %[sent], v224\n\t" \
  "s_or_b64 s[94:95], s[94:95], vcc\n\t" \
  "v_cmp_eq_u32 vcc, %[sent], v225\n\t" \
  "s_or_b64 s[94:95], s[94:95], vcc\n\t" \
  "v_cmp_eq_u32 vcc, %[sent], v226\n\t" \
  "s_or_b64 s[94:95], s[94:95], vcc\n\t" \
  "v_cmp_eq_u32 vcc, %[sent], v227\n\t" \
  "s_or_b64 s[94:95], s[94:95], vcc\n\t" \
  "v_cmp_eq_u32 vcc, %[sent], v228\n\t" \
  "s_or_b64 s[94:95], s[94:95], vcc\n\t" \
  "v_cmp_eq_u32 vcc, %[sent], v229\n\t" \
  "s_or_b64 s[94:95], s[94:95], vcc\n\t" \
  "v_cmp_eq_u32 vcc, %[sent], v230\n\t" \
  "s_or_b64 s[94:95], s[94:95], vcc\n\t" \
  "v_cmp_eq_u32 vcc, %[sent], v231\n\t" \
  "s_or_b64 s[94:95], s[94:95], vcc\n\t" \
  "s_cmp_eq_u64 s[94:95], 0\n\t" \
  "s_cbranch_scc1 .Lok%=\n\t" \
  "s_add_u32 s92, s92, 1\n\t" \
  "s_cmp_eq_u32 s92, 1\n\t" \
  "s_cbranch_scc1 .Lpoll%=\n\t" \
  "s_sleep 2\n\t" \
  "s_and_b32 s93, s92, 63\n\t" \
  "s_cmp_lg_u32 s93, 0\n\t" \
  "s_cbranch_scc1 .Lpoll%=\n\t" \
  "v_mov_b32 v149, 0\n\t" \
  "buffer_load_dword v148, v149, %[rs], %[sab] offen sc1\n\t" \
  "s_waitcnt vmcnt(0)\n\t" \
  "v_readfirstlane_b32 s93, v148\n\t" \
  "s_nop 3\n\t" \
  "s_cmp_eq_u32 s93, 1\n\t" \
  "s_cbranch_scc1 .Lok%=\n\t" \
  "s_cmp_lt_u32 s92, 0x80000\n\t" \
  "s_cbranch_scc1 .Lpoll%=\n\t" \
  "v_mov_b32 v148, 1\n\t" \
  "buffer_store_dword v148, v149, %[rs], %[sab] offen sc1\n\t" \
  "s_branch .Lok%=\n\t" \
  ".Lend%=:\n\t"
constexpr int TLAG = 2;
#define RED_ASM \
  "s_mov_b32 %[step], 0\n\t" \
  "s_lshl_b32 %[sh], %[step], 15\n\t" \
  "s_lshr_b32 s93, %[step], 4\n\t" \
  "s_lshr_b32 s93, %[pm], s93\n\t" \
  "s_and_b32 s93, s93, 4\n\t" \
  "s_lshl_b32 s93, s93, 11\n\t" \
  "s_lshl2_add_u32 %[so], %[sh], s93\n\t" \
  "s_sub_i32 s93, %[step], 2\n\t" \
  "s_lshl_b32 %[st], s93, 19\n\t" \
  "s_lshl_b32 s94, s93, 11\n\t" \
  "s_xor_b32 s94, s94, %[s0]\n\t" \
  "s_and_b32 s94, s94, 0x2000\n\t" \
  "s_or_b32 %[st], %[st], s94\n\t" \
  "s_cmp_lt_i32 s93, 0\n\t" \
  "s_cselect_b32 %[st], 0x70000000, %[st]\n\t" \
  "s_add_u32 %[st4], %[st], 4096\n\t" \
  "buffer_load_dwordx2 v[166:167], v173, %[rs], 0 offen sc1\n\t" \
  "v_mov_b64 v[144:145], 0\n\t" \
  "v_mov_b64 v[146:147], 0\n\t" \
  "v_mov_b64 v[148:149], 0\n\t" \
  "v_mov_b64 v[150:151], 0\n\t" \
  "v_mov_b64 v[152:153], 0\n\t" \
  "v_mov_b64 v[154:155], 0\n\t" \
  "v_mov_b64 v[156:157], 0\n\t" \
  "v_mov_b64 v[158:159], 0\n\t" \
  ".Lstep%=:\n\t" \
  "s_setprio 3\n\t" \
  "s_mov_b32 s92, 0\n\t" \
  ".Lcal%=:\n\t" \
  "s_waitcnt vmcnt(0)\n\t" \
  "s_memrealtime s[76:77]\n\t" \
  "v_cmp_ne_u32 vcc, %[step], v166\n\t" \
  "v_readlane_b32 s90, v167, 0\n\t" \
  "v_readlane_b32 s91, v167, 1\n\t" \
  "v_readlane_b32 s94, v167, 2\n\t" \
  "v_readlane_b32 s95, v167, 3\n\t" \
  "s_sub_i32 s90, s90, %[base]\n\t" \
  "s_sub_i32 s91, s91, %[base]\n\t" \
  "s_sub_i32 s94, s94, %[base]\n\t" \
  "s_sub_i32 s95, s95, %[base]\n\t" \
  "s_max_i32 s90, s90, s91\n\t" \
  "s_max_i32 s94, s94, s95\n\t" \
  "s_max_i32 s90, s90, s94\n\t" \
  "s_add_i32 s90, s90, %[ds]\n\t" \
  "s_waitcnt lgkmcnt(0)\n\t" \
  "s_lshl2_add_u32 s76, s76, %[mn]\n\t" \
  "s_sub_i32 s76, s76, %[base]\n\t" \
  "s_add_u32 %[E], s90, %[base]\n\t" \
  "s_and_b32 s93, vcc_lo, 0xf\n\t" \
  "s_cbranch_scc1 .Lbl%=\n\t" \
  ".Lgo%=:\n\t" \
  "v_mov_b32 v166, 7\n\t" \
  "s_mov_b64 exec, 1\n\t" \
  "v_mov_b32 v164, s90\n\t" \
  "ds_max_u32 v168, v164 offset:4100\n\t" \
  "ds_add_u32 v168, v164 offset:4096\n\t" \
  "s_mov_b64 exec, -1\n\t" \
  "s_memrealtime s[90:91]\n\t" \
  "s_waitcnt lgkmcnt(0)\n\t" \
  "s_lshl_b32 s93, s90, 2\n\t" \
  "s_sub_i32 s93, %[E], s93\n\t" \
  "s_cmp_le_i32 s93, 0\n\t" \
  "s_cbranch_scc1 .Lgop%=\n\t" \
  "s_min_i32 s93, s93, 8000\n\t" \
  "s_mov_b32 s94, s93\n\t" \
  ".Lslp%=:\n\t" \
  "s_sub_i32 s94, s94, 18\n\t" \
  "s_cmp_lt_i32 s94, 30\n\t" \
  "s_cbranch_scc1 .Lsxp%=\n\t" \
  "s_sleep 1\n\t" \
  "s_branch .Lslp%=\n\t" \
  ".Lsxp%=:\n\t" \
  "s_sub_i32 s93, s93, 12\n\t" \
  "s_max_i32 s93, s93, 4\n\t" \
  "s_lshr_b32 s93, s93, 2\n\t" \
  "s_add_u32 s93, s93, s90\n\t" \
  "s_memrealtime s[90:91]\n\t" \
  ".Lpacep%=:\n\t" \
  "s_memrealtime s[94:95]\n\t" \
  "s_waitcnt lgkmcnt(1)\n\t" \
  "s_sub_i32 s92, s90, s93\n\t" \
  "s_cmp_ge_i32 s92, 0\n\t" \
  "s_cbranch_scc1 .Lep%=\n\t" \
  "s_memrealtime s[90:91]\n\t" \
  "s_waitcnt lgkmcnt(1)\n\t" \
  "s_sub_i32 s92, s94, s93\n\t" \
  "s_cmp_lt_i32 s92, 0\n\t" \
  "s_cbranch_scc1 .Lpacep%=\n\t" \
  "s_nop 4\n\t" \
  ".Lep%=:\n\t" \
  "s_add_u32 s92, s92, s93\n\t" \
  "s_lshl_b32 s92, s92, 2\n\t" \
  "s_sub_i32 s92, %[E], s92\n\t" \
  "s_max_i32 s92, s92, 0\n\t" \
  "s_bitcmp0_b32 s92, 3\n\t" \
  "s_cbranch_scc1 .Lf3p%=\n\t" \
  "s_nop 15\n\t" \
  ".Lf3p%=:\n\t" \
  "s_bitcmp0_b32 s92, 2\n\t" \
  "s_cbranch_scc1 .Lf2p%=\n\t" \
  "s_nop 10\n\t" \
  ".Lf2p%=:\n\t" \
  ".Lgop%=:\n\t" \
  "s_mov_b32 s92, 0\n\t" \
  "buffer_load_dwordx4 v[128:131], v234, %[rs], %[so] offen sc1\n\t" \
  "s_nop 15\n\t" \
  "buffer_load_dwordx4 v[132:135], v235, %[rs], %[so] offen sc1\n\t" \
  "s_nop 15\n\t" \
  "buffer_load_dwordx4 v[136:139], v236, %[rs], %[so] offen sc1\n\t" \
  "s_nop 15\n\t" \
  "buffer_load_dwordx4 v[140:143], v237, %[rs], %[so] offen sc1\n\t" \
  "s_waitcnt lgkmcnt(0)\n\t" \
  ".Lw%=:\n\t" \
  "s_waitcnt vmcnt(3)\n\t" \
  "v_smfmac_f32_16x16x64_bf16 v[144:147], v[128:131], v[0:7], v172\n\t" \
  "v_max3_u32 v164, v128, v129, 0\n\t" \
  "v_smfmac_f32_16x16x64_bf16 v[148:151], v[128:131], v[8:15], v172\n\t" \
  "v_max3_u32 v164, v130, v131, v164\n\t" \
  "v_smfmac_f32_16x16x64_bf16 v[152:155], v[128:131], v[16:23], v172\n\t" \
  "v_smfmac_f32_16x16x64_bf16 v[156:159], v[128:131], v[24:31], v172\n\t" \
  "buffer_load_dwordx4 v[176:179], v238, %[rt], %[st] offen offset:0 sc1\n\t" \
  "s_waitcnt vmcnt(3)\n\t" \
  "v_smfmac_f32_16x16x64_bf16 v[144:147], v[132:135], v[32:39], v172\n\t" \
  "v_max3_u32 v164, v132, v133, v164\n\t" \
  "v_smfmac_f32_16x16x64_bf16 v[148:151], v[132:135], v[40:47], v172\n\t" \
  "v_max3_u32 v164, v134, v135, v164\n\t" \
  "v_smfmac_f32_16x16x64_bf16 v[152:155], v[132:135], v[48:55], v172\n\t" \
  "v_smfmac_f32_16x16x64_bf16 v[156:159], v[132:135], v[56:63], v172\n\t" \
  "buffer_load_dwordx4 v[180:183], v238, %[rt], %[st] offen offset:1024 sc1\n\t" \
  "s_waitcnt vmcnt(3)\n\t" \
  "v_smfmac_f32_16x16x64_bf16 v[144:147], v[136:139], v[64:71], v172\n\t" \
  "v_max3_u32 v164, v136, v137, v164\n\t" \
  "v_smfmac_f32_16x16x64_bf16 v[148:151], v[136:139], v[72:79], v172\n\t" \
  "v_max3_u32 v164, v138, v139, v164\n\t" \
  "v_smfmac_f32_16x16x64_bf16 v[152:155], v[136:139], v[80:87], v172\n\t" \
  "v_smfmac_f32_16x16x64_bf16 v[156:159], v[136:139], v[88:95], v172\n\t" \
  "buffer_load_dwordx4 v[184:187], v238, %[rt], %[st4] offen offset:0 sc1\n\t" \
  "s_waitcnt vmcnt(3)\n\t" \
  "v_smfmac_f32_16x16x64_bf16 v[144:147], v[140:143], v[96:103], v172\n\t" \
  "v_max3_u32 v164, v140, v141, v164\n\t" \
  "v_smfmac_f32_16x16x64_bf16 v[148:151], v[140:143], v[104:111], v172\n\t" \
  "v_max3_u32 v164, v142, v143, v164\n\t" \
  "v_smfmac_f32_16x16x64_bf16 v[152:155], v[140:143], v[112:119], v172\n\t" \
  "v_cmp_le_u32 vcc, %[sent], v164\n\t" \
  "v_smfmac_f32_16x16x64_bf16 v[156:159], v[140:143], v[120:127], v172\n\t" \
  "buffer_load_dwordx4 v[188:191], v238, %[rt], %[st4] offen offset:1024 sc1\n\t" \
  "s_cbranch_vccnz .Lslow%=\n\t" \
  ".Lok%=:\n\t" \
  "v_add_f32_dpp v160, v144, v146 quad_perm:[0,1,2,3] row_mask:0x3 bank_mask:0xf\n\t" \
  "v_add_f32_dpp v161, v145, v147 quad_perm:[0,1,2,3] row_mask:0x3 bank_mask:0xf\n\t" \
  "v_add_f32_dpp v162, v148, v150 quad_perm:[0,1,2,3] row_mask:0x3 bank_mask:0xf\n\t" \
  "v_add_f32_dpp v163, v149, v151 quad_perm:[0,1,2,3] row_mask:0x3 bank_mask:0xf\n\t" \
  "v_add_f32_dpp v160, v152, v154 quad_perm:[0,1,2,3] row_mask:0xc bank_mask:0xf\n\t" \
  "v_add_f32_dpp v161, v153, v155 quad_perm:[0,1,2,3] row_mask:0xc bank_mask:0xf\n\t" \
  "v_add_f32_dpp v162, v156, v158 quad_perm:[0,1,2,3] row_mask:0xc bank_mask:0xf\n\t" \
  "v_add_f32_dpp v163, v157, v159 quad_perm:[0,1,2,3] row_mask:0xc bank_mask:0xf\n\t" \
  "s_nop 0\n\t" \
  "v_permlane32_swap_b32 v160, v162\n\t" \
  "v_permlane32_swap_b32 v161, v163\n\t" \
  "v_add_f32 v160, v160, v162\n\t" \
  "v_add_f32 v161, v161, v163\n\t" \
  "ds_write_b64 v169, v[160:161]\n\t" \
  "s_mov_b64 exec, 1\n\t" \
  "ds_inc_rtn_u32 v164, v168, v166 offset:4104\n\t" \
  "s_mov_b64 exec, -1\n\t" \
  "ds_read_b64 v[128:129], v170 offset:0\n\t" \
  "ds_read_b64 v[130:131], v170 offset:512\n\t" \
  "ds_read_b64 v[132:133], v170 offset:1024\n\t" \
  "ds_read_b64 v[134:135], v170 offset:1536\n\t" \
  "ds_read_b64 v[136:137], v170 offset:2048\n\t" \
  "ds_read_b64 v[138:139], v170 offset:2560\n\t" \
  "ds_read_b64 v[140:141], v170 offset:3072\n\t" \
  "ds_read_b64 v[142:143], v170 offset:3584\n\t" \
  "s_waitcnt lgkmcnt(8)\n\t" \
  "v_readfirstlane_b32 s93, v164\n\t" \
  "s_cmp_lg_u32 s93, 7\n\t" \
  "s_cbranch_scc1 .Lb1%=\n\t" \
  "s_waitcnt lgkmcnt(4)\n\t" \
  "v_pk_add_f32 v[128:129], v[128:129], v[130:131]\n\t" \
  "v_pk_add_f32 v[132:133], v[132:133], v[134:135]\n\t" \
  "s_waitcnt lgkmcnt(0)\n\t" \
  "v_pk_add_f32 v[136:137], v[136:137], v[138:139]\n\t" \
  "v_pk_add_f32 v[140:141], v[140:141], v[142:143]\n\t" \
  "v_pk_add_f32 v[128:129], v[128:129], v[132:133]\n\t" \
  "v_pk_add_f32 v[136:137], v[136:137], v[140:141]\n\t" \
  "v_pk_add_f32 v[128:129], v[128:129], v[136:137]\n\t" \
  "v_cvt_pk_bf16_f32 v128, v128, v129\n\t" \
  "s_nop 1\n\t" \
  "v_mov_b32_dpp v129, v128 quad_perm:[1,0,3,2] row_mask:0xf bank_mask:0xf\n\t" \
  "v_perm_b32 v128, v129, v128, v174\n\t" \
  "buffer_store_dword v128, v171, %[rs], %[sh] offen sc1\n\t" \
  "s_memtime s[90:91]\n\t" \
  "s_barrier\n\t" \
  "s_memtime s[76:77]\n\t" \
  "s_memrealtime s[78:79]\n\t" \
  "s_nop 3\n\t" \
  "s_memrealtime s[80:81]\n\t" \
  "s_nop 3\n\t" \
  "s_memrealtime s[82:83]\n\t" \
  "s_nop 3\n\t" \
  "s_memrealtime s[84:85]\n\t" \
  "s_nop 3\n\t" \
  "s_memrealtime s[86:87]\n\t" \
  "s_nop 3\n\t" \
  "s_memrealtime s[88:89]\n\t" \
  "s_mov_b32 s93, 0\n\t" \
  "s_waitcnt lgkmcnt(0)\n\t" \
  "s_cmp_eq_u32 s80, s78\n\t" \
  "s_addc_u32 s93, s93, 0\n\t" \
  "s_cmp_eq_u32 s82, s78\n\t" \
  "s_addc_u32 s93, s93, 0\n\t" \
  "s_cmp_eq_u32 s84, s78\n\t" \
  "s_addc_u32 s93, s93, 0\n\t" \
  "s_cmp_eq_u32 s86, s78\n\t" \
  "s_addc_u32 s93, s93, 0\n\t" \
  "s_cmp_eq_u32 s88, s78\n\t" \
  "s_addc_u32 s93, s93, 0\n\t" \
  "s_mul_i32 s93, s93, 21\n\t" \
  "s_add_u32 s76, s76, 15\n\t" \
  "s_add_u32 s76, s76, s93\n\t" \
  "s_lshl2_add_u32 s78, s78, 16\n\t" \
  "s_mov_b64 exec, 1\n\t" \
  "s_sub_i32 s90, s76, s90\n\t" \
  "s_mul_i32 s90, s90, 11900\n\t" \
  "s_ashr_i32 s90, s90, 16\n\t" \
  "s_sub_u32 s90, s78, s90\n\t" \
  "s_sub_u32 s90, s90, %[base]\n\t" \
  "v_mov_b32 v164, s90\n\t" \
  "ds_write_b32 v168, v164 offset:4108\n\t" \
  "s_mov_b64 exec, -1\n\t" \
  "s_waitcnt vmcnt(1)\n\t" \
  ".Lb2%=:\n\t" \
  "s_setprio 0\n\t" \
  "s_bitcmp0_b64 %[am], %[step]\n\t" \
  "s_cbranch_scc1 .La1%=\n\t" \
  "buffer_store_dwordx4 v[224:227], v228, %[ra], 0 offen sc1\n\t" \
  ".La1%=:\n\t" \
  "buffer_load_dwordx4 v[192:195], v239, %[rt], %[st] offen offset:0 sc1\n\t" \
  "buffer_load_dwordx4 v[196:199], v239, %[rt], %[st] offen offset:1024 sc1\n\t" \
  "v_mfma_f32_16x16x32_bf16 v[128:131], v[0:3], v[176:179], 0\n\t" \
  "v_mfma_f32_16x16x32_bf16 v[136:139], v[8:11], v[176:179], 0\n\t" \
  "ds_read2st64_b32 v[144:145], v231 offset0:0 offset1:8\n\t" \
  "ds_read2st64_b32 v[146:147], v231 offset0:16 offset1:24\n\t" \
  "ds_read2st64_b32 v[148:149], v231 offset0:32 offset1:40\n\t" \
  "ds_read2st64_b32 v[150:151], v231 offset0:48 offset1:56\n\t" \
  "buffer_load_dwordx4 v[200:203], v239, %[rt], %[st4] offen offset:0 sc1\n\t" \
  "buffer_load_dwordx4 v[204:207], v239, %[rt], %[st4] offen offset:1024 sc1\n\t" \
  "v_mfma_f32_16x16x32_bf16 v[132:135], v[4:7], v[180:183], 0\n\t" \
  "v_mfma_f32_16x16x32_bf16 v[140:143], v[12:15], v[180:183], 0\n\t" \
  "s_waitcnt lgkmcnt(0)\n\t" \
  "v_add_f32 v144, v144, v145\n\t" \
  "v_add_f32 v146, v146, v147\n\t" \
  "v_add_f32 v148, v148, v149\n\t" \
  "v_add_f32 v150, v150, v151\n\t" \
  "v_add_f32 v144, v144, v146\n\t" \
  "v_add_f32 v148, v148, v150\n\t" \
  "buffer_load_dwordx4 v[208:211], v240, %[rt], %[st] offen offset:0 sc1\n\t" \
  "buffer_load_dwordx4 v[212:215], v240, %[rt], %[st] offen offset:1024 sc1\n\t" \
  "v_mfma_f32_16x16x32_bf16 v[128:131], v[16:19], v[184:187], v[128:131]\n\t" \
  "v_mfma_f32_16x16x32_bf16 v[136:139], v[24:27], v[184:187], v[136:139]\n\t" \
  "v_add_f32 v144, v144, v148\n\t" \
  "v_cvt_pk_bf16_f32 v144, v144, v144\n\t" \
  "ds_write_b16 v232, v144\n\t" \
  "ds_read_b128 v[224:227], v229\n\t" \
  "v_add_u32 v228, %[ainc], v228\n\t" \
  "v_add_u32 v229, %[rinc], v229\n\t" \
  "v_add_u32 v232, 1024, v232\n\t" \
  "v_xor_b32 v231, 0x4000, v231\n\t" \
  "buffer_load_dwordx4 v[216:219], v240, %[rt], %[st4] offen offset:0 sc1\n\t" \
  "buffer_load_dwordx4 v[220:223], v240, %[rt], %[st4] offen offset:1024 sc1\n\t" \
  "v_mfma_f32_16x16x32_bf16 v[132:135], v[20:23], v[188:191], v[132:135]\n\t" \
  "v_mfma_f32_16x16x32_bf16 v[140:143], v[28:31], v[188:191], v[140:143]\n\t" \
  "buffer_load_dwordx4 v[176:179], v241, %[rt], %[st] offen offset:0 sc1\n\t" \
  "s_waitcnt vmcnt(8)\n\t" \
  "v_mfma_f32_16x16x32_bf16 v[128:131], v[32:35], v[192:195], v[128:131]\n\t" \
  "v_mfma_f32_16x16x32_bf16 v[136:139], v[40:43], v[192:195], v[136:139]\n\t" \
  "buffer_load_dwordx4 v[180:183], v241, %[rt], %[st] offen offset:1024 sc1\n\t" \
  "s_waitcnt vmcnt(8)\n\t" \
  "v_mfma_f32_16x16x32_bf16 v[132:135], v[36:39], v[196:199], v[132:135]\n\t" \
  "v_mfma_f32_16x16x32_bf16 v[140:143], v[44:47], v[196:199], v[140:143]\n\t" \
  "buffer_load_dwordx4 v[184:187], v241, %[rt], %[st4] offen offset:0 sc1\n\t" \
  "s_waitcnt vmcnt(8)\n\t" \
  "v_mfma_f32_16x16x32_bf16 v[128:131], v[48:51], v[200:203], v[128:131]\n\t" \
  "v_mfma_f32_16x16x32_bf16 v[136:139], v[56:59], v[200:203], v[136:139]\n\t" \
  "ds_read_b128 v[160:163], v168 offset:4096\n\t" \
  "buffer_load_dwordx4 v[188:191], v241, %[rt], %[st4] offen offset:1024 sc1\n\t" \
  "s_waitcnt vmcnt(8)\n\t" \
  "v_mfma_f32_16x16x32_bf16 v[132:135], v[52:55], v[204:207], v[132:135]\n\t" \
  "v_mfma_f32_16x16x32_bf16 v[140:143], v[60:63], v[204:207], v[140:143]\n\t" \
  "s_waitcnt lgkmcnt(0)\n\t" \
  "v_readfirstlane_b32 s90, v161\n\t" \
  "v_readfirstlane_b32 s91, v163\n\t" \
  "v_readfirstlane_b32 s94, v160\n\t" \
  "s_sub_u32 s93, s94, %[sm]\n\t" \
  "s_mov_b32 %[sm], s94\n\t" \
  "s_lshr_b32 s93, s93, 3\n\t" \
  "s_sub_i32 s93, s90, s93\n\t" \
  "s_mul_i32 s93, s93, %[wm]\n\t" \
  "s_ashr_i32 s93, s93, 2\n\t" \
  "s_min_u32 s93, s93, %[gm]\n\t" \
  "s_sub_i32 s90, s90, s93\n\t" \
  "s_add_u32 s93, s90, %[rl]\n\t" \
  "s_add_u32 s93, s93, %[base]\n\t" \
  "s_add_u32 %[E], s93, %[lk]\n\t" \
  "s_sub_i32 s91, s91, s90\n\t" \
  "s_sub_i32 s91, s91, %[rl]\n\t" \
  "s_min_i32 s93, s91, %[up]\n\t" \
  "s_max_i32 s93, s93, %[dn]\n\t" \
  "s_cmp_gt_i32 s91, 60\n\t" \
  "s_cselect_b32 s93, 2, s93\n\t" \
  "s_add_i32 %[rl], %[rl], s93\n\t" \
  "v_mov_b32 v160, %[rl]\n\t" \
  "buffer_store_dword v160, v175, %[rs], 0 offen sc1\n\t" \
  "s_waitcnt vmcnt(8)\n\t" \
  "v_mfma_f32_16x16x32_bf16 v[128:131], v[64:67], v[208:211], v[128:131]\n\t" \
  "v_mfma_f32_16x16x32_bf16 v[136:139], v[72:75], v[208:211], v[136:139]\n\t" \
  "s_add_u32 %[step], %[step], 1\n\t" \
  "s_lshl_b32 %[sh], %[step], 15\n\t" \
  "s_lshr_b32 s93, %[step], 4\n\t" \
  "s_lshr_b32 s93, %[pm], s93\n\t" \
  "s_and_b32 s93, s93, 4\n\t" \
  "s_lshl_b32 s93, s93, 11\n\t" \
  "s_lshl2_add_u32 %[so], %[sh], s93\n\t" \
  "s_waitcnt vmcnt(7)\n\t" \
  "v_mfma_f32_16x16x32_bf16 v[132:135], v[68:71], v[212:215], v[132:135]\n\t" \
  "v_mfma_f32_16x16x32_bf16 v[140:143], v[76:79], v[212:215], v[140:143]\n\t" \
  "s_sub_i32 s93, %[step], 2\n\t" \
  "s_lshl_b32 %[st], s93, 19\n\t" \
  "s_lshl_b32 s94, s93, 11\n\t" \
  "s_xor_b32 s94, s94, %[s0]\n\t" \
  "s_and_b32 s94, s94, 0x2000\n\t" \
  "s_or_b32 %[st], %[st], s94\n\t" \
  "s_cmp_lt_i32 s93, 0\n\t" \
  "s_cselect_b32 %[st], 0x70000000, %[st]\n\t" \
  "s_add_u32 %[st4], %[st], 4096\n\t" \
  "s_waitcnt vmcnt(6)\n\t" \
  "v_mfma_f32_16x16x32_bf16 v[128:131], v[80:83], v[216:219], v[128:131]\n\t" \
  "v_mfma_f32_16x16x32_bf16 v[136:139], v[88:91], v[216:219], v[136:139]\n\t" \
  "s_waitcnt vmcnt(5)\n\t" \
  "v_mfma_f32_16x16x32_bf16 v[132:135], v[84:87], v[220:223], v[132:135]\n\t" \
  "v_mfma_f32_16x16x32_bf16 v[140:143], v[92:95], v[220:223], v[140:143]\n\t" \
  "s_cmp_eq_u32 %[step], 63\n\t" \
  "s_cbranch_scc1 .Llast%=\n\t" \
  "s_movk_i32 s93, 600\n\t" \
  ".Lpacel%=:\n\t" \
  "s_memrealtime s[90:91]\n\t" \
  "s_waitcnt lgkmcnt(0)\n\t" \
  "s_lshl_b32 s90, s90, 2\n\t" \
  "s_sub_i32 s91, %[E], s90\n\t" \
  "s_cmp_le_i32 s91, 0\n\t" \
  "s_cbranch_scc1 .Lgol%=\n\t" \
  "s_sub_u32 s93, s93, 1\n\t" \
  "s_cmp_lg_u32 s93, 0\n\t" \
  "s_cbranch_scc1 .Lpacel%=\n\t" \
  ".Lgol%=:\n\t" \
  "buffer_load_dwordx2 v[166:167], v173, %[rs], 0 offen sc1\n\t" \
  "s_waitcnt vmcnt(5)\n\t" \
  "v_mfma_f32_16x16x32_bf16 v[128:131], v[96:99], v[176:179], v[128:131]\n\t" \
  "v_mfma_f32_16x16x32_bf16 v[136:139], v[104:107], v[176:179], v[136:139]\n\t" \
  "s_waitcnt vmcnt(4)\n\t" \
  "v_mfma_f32_16x16x32_bf16 v[132:135], v[100:103], v[180:183], v[132:135]\n\t" \
  "v_mfma_f32_16x16x32_bf16 v[140:143], v[108:111], v[180:183], v[140:143]\n\t" \
  "s_waitcnt vmcnt(3)\n\t" \
  "v_mfma_f32_16x16x32_bf16 v[128:131], v[112:115], v[184:187], v[128:131]\n\t" \
  "v_mfma_f32_16x16x32_bf16 v[136:139], v[120:123], v[184:187], v[136:139]\n\t" \
  "s_waitcnt vmcnt(2)\n\t" \
  "v_mfma_f32_16x16x32_bf16 v[132:135], v[116:119], v[188:191], v[132:135]\n\t" \
  "v_mfma_f32_16x16x32_bf16 v[140:143], v[124:127], v[188:191], v[140:143]\n\t" \
  "v_mov_b64 v[144:145], 0\n\t" \
  "v_mov_b64 v[146:147], 0\n\t" \
  "v_mov_b64 v[148:149], 0\n\t" \
  "v_mov_b64 v[150:151], 0\n\t" \
  "v_mov_b64 v[152:153], 0\n\t" \
  "v_mov_b64 v[154:155], 0\n\t" \
  "v_mov_b64 v[156:157], 0\n\t" \
  "v_mov_b64 v[158:159], 0\n\t" \
  "v_pk_add_f32 v[128:129], v[128:129], v[132:133]\n\t" \
  "v_pk_add_f32 v[130:131], v[130:131], v[134:135]\n\t" \
  "ds_write_b128 v230, v[128:131] offset:0\n\t" \
  "v_pk_add_f32 v[136:137], v[136:137], v[140:141]\n\t" \
  "v_pk_add_f32 v[138:139], v[138:139], v[142:143]\n\t" \
  "ds_write_b128 v230, v[136:139] offset:1024\n\t" \
  "v_xor_b32 v230, 0x4000, v230\n\t" \
  "s_cmp_lg_u32 %[step], 63\n\t" \
  "s_cbranch_scc1 .Lstep%=\n\t" \
  ".Lfc%=:\n\t" \
  "s_waitcnt vmcnt(0)\n\t" \
  "v_cmp_eq_u32 vcc, -1, v166\n\t" \
  "s_cbranch_vccnz .Lfw%=\n\t" \
  ".Lfo%=:\n\t" \
  "v_add_u32 v242, 16384, v242\n\t" \
  "buffer_load_dwordx4 v[176:179], v238, %[rt], %[st] offen offset:0 sc1\n\t" \
  "buffer_load_dwordx4 v[180:183], v238, %[rt], %[st] offen offset:1024 sc1\n\t" \
  "buffer_load_dwordx4 v[184:187], v238, %[rt], %[st4] offen offset:0 sc1\n\t" \
  "buffer_load_dwordx4 v[188:191], v238, %[rt], %[st4] offen offset:1024 sc1\n\t" \
  "buffer_load_dwordx4 v[192:195], v239, %[rt], %[st] offen offset:0 sc1\n\t" \
  "buffer_load_dwordx4 v[196:199], v239, %[rt], %[st] offen offset:1024 sc1\n\t" \
  "buffer_load_dwordx4 v[200:203], v239, %[rt], %[st4] offen offset:0 sc1\n\t" \
  "buffer_load_dwordx4 v[204:207], v239, %[rt], %[st4] offen offset:1024 sc1\n\t" \
  "v_subrev_u32 v230, 98304, v230\n\t" \
  ".Lt%=:\n\t" \
  "s_waitcnt vmcnt(7)\n\t" \
  "v_mfma_f32_16x16x32_bf16 v[128:131], v[0:3], v[176:179], 0\n\t" \
  "v_mfma_f32_16x16x32_bf16 v[136:139], v[8:11], v[176:179], 0\n\t" \
  "buffer_load_dwordx4 v[176:179], v240, %[rt], %[st] offen offset:0 sc1\n\t" \
  "s_waitcnt vmcnt(7)\n\t" \
  "v_mfma_f32_16x16x32_bf16 v[132:135], v[4:7], v[180:183], 0\n\t" \
  "v_mfma_f32_16x16x32_bf16 v[140:143], v[12:15], v[180:183], 0\n\t" \
  "buffer_load_dwordx4 v[180:183], v240, %[rt], %[st] offen offset:1024 sc1\n\t" \
  "s_waitcnt vmcnt(7)\n\t" \
  "v_mfma_f32_16x16x32_bf16 v[128:131], v[16:19], v[184:187], v[128:131]\n\t" \
  "v_mfma_f32_16x16x32_bf16 v[136:139], v[24:27], v[184:187], v[136:139]\n\t" \
  "buffer_load_dwordx4 v[184:187], v240, %[rt], %[st4] offen offset:0 sc1\n\t" \
  "buffer_load_dword v166, v242, %[rs], 0 offen sc1\n\t" \
  "s_waitcnt vmcnt(8)\n\t" \
  "v_mfma_f32_16x16x32_bf16 v[132:135], v[20:23], v[188:191], v[132:135]\n\t" \
  "v_mfma_f32_16x16x32_bf16 v[140:143], v[28:31], v[188:191], v[140:143]\n\t" \
  "buffer_load_dwordx4 v[188:191], v240, %[rt], %[st4] offen offset:1024 sc1\n\t" \
  "s_waitcnt vmcnt(8)\n\t" \
  "v_mfma_f32_16x16x32_bf16 v[128:131], v[32:35], v[192:195], v[128:131]\n\t" \
  "v_mfma_f32_16x16x32_bf16 v[136:139], v[40:43], v[192:195], v[136:139]\n\t" \
  "buffer_load_dwordx4 v[192:195], v241, %[rt], %[st] offen offset:0 sc1\n\t" \
  "s_waitcnt vmcnt(8)\n\t" \
  "v_mfma_f32_16x16x32_bf16 v[132:135], v[36:39], v[196:199], v[132:135]\n\t" \
  "v_mfma_f32_16x16x32_bf16 v[140:143], v[44:47], v[196:199], v[140:143]\n\t" \
  "buffer_load_dwordx4 v[196:199], v241, %[rt], %[st] offen offset:1024 sc1\n\t" \
  "s_waitcnt vmcnt(8)\n\t" \
  "v_mfma_f32_16x16x32_bf16 v[128:131], v[48:51], v[200:203], v[128:131]\n\t" \
  "v_mfma_f32_16x16x32_bf16 v[136:139], v[56:59], v[200:203], v[136:139]\n\t" \
  "buffer_load_dwordx4 v[200:203], v241, %[rt], %[st4] offen offset:0 sc1\n\t" \
  "s_waitcnt vmcnt(8)\n\t" \
  "v_mfma_f32_16x16x32_bf16 v[132:135], v[52:55], v[204:207], v[132:135]\n\t" \
  "v_mfma_f32_16x16x32_bf16 v[140:143], v[60:63], v[204:207], v[140:143]\n\t" \
  "buffer_load_dwordx4 v[204:207], v241, %[rt], %[st4] offen offset:1024 sc1\n\t" \
  "s_waitcnt vmcnt(8)\n\t" \
  "v_mfma_f32_16x16x32_bf16 v[128:131], v[64:67], v[176:179], v[128:131]\n\t" \
  "v_mfma_f32_16x16x32_bf16 v[136:139], v[72:75], v[176:179], v[136:139]\n\t" \
  "s_waitcnt vmcnt(5)\n\t" \
  ".Ltc%=:\n\t" \
  "v_cmp_eq_u32 vcc, -1, v166\n\t" \
  "s_cbranch_vccnz .Ltw%=\n\t" \
  ".Lto%=:\n\t" \
  "s_cmp_eq_u32 %[step], 65\n\t" \
  "s_cbranch_scc1 .Ltl%=\n\t" \
  "s_add_u32 %[step], %[step], 1\n\t" \
  "s_sub_i32 s93, %[step], 2\n\t" \
  "s_lshl_b32 %[st], s93, 19\n\t" \
  "s_lshl_b32 s94, s93, 11\n\t" \
  "s_xor_b32 s94, s94, %[s0]\n\t" \
  "s_and_b32 s94, s94, 0x2000\n\t" \
  "s_or_b32 %[st], %[st], s94\n\t" \
  "s_cmp_lt_i32 s93, 0\n\t" \
  "s_cselect_b32 %[st], 0x70000000, %[st]\n\t" \
  "s_add_u32 %[st4], %[st], 4096\n\t" \
  "v_add_u32 v242, 16384, v242\n\t" \
  "s_cmp_eq_u32 %[step], 65\n\t" \
  "s_cbranch_scc0 .Ltn%=\n\t" \
  "v_mov_b32 v242, %[fg]\n\t" \
  ".Ltn%=:\n\t" \
  "buffer_load_dwordx4 v[176:179], v238, %[rt], %[st] offen offset:0 sc1\n\t" \
  "s_waitcnt vmcnt(7)\n\t" \
  "v_mfma_f32_16x16x32_bf16 v[132:135], v[68:71], v[180:183], v[132:135]\n\t" \
  "v_mfma_f32_16x16x32_bf16 v[140:143], v[76:79], v[180:183], v[140:143]\n\t" \
  "buffer_load_dwordx4 v[180:183], v238, %[rt], %[st] offen offset:1024 sc1\n\t" \
  "s_waitcnt vmcnt(7)\n\t" \
  "v_mfma_f32_16x16x32_bf16 v[128:131], v[80:83], v[184:187], v[128:131]\n\t" \
  "v_mfma_f32_16x16x32_bf16 v[136:139], v[88:91], v[184:187], v[136:139]\n\t" \
  "buffer_load_dwordx4 v[184:187], v238, %[rt], %[st4] offen offset:0 sc1\n\t" \
  "s_waitcnt vmcnt(7)\n\t" \
  "v_mfma_f32_16x16x32_bf16 v[132:135], v[84:87], v[188:191], v[132:135]\n\t" \
  "v_mfma_f32_16x16x32_bf16 v[140:143], v[92:95], v[188:191], v[140:143]\n\t" \
  "buffer_load_dwordx4 v[188:191], v238, %[rt], %[st4] offen offset:1024 sc1\n\t" \
  "s_waitcnt vmcnt(7)\n\t" \
  "v_mfma_f32_16x16x32_bf16 v[128:131], v[96:99], v[192:195], v[128:131]\n\t" \
  "v_mfma_f32_16x16x32_bf16 v[136:139], v[104:107], v[192:195], v[136:139]\n\t" \
  "buffer_load_dwordx4 v[192:195], v239, %[rt], %[st] offen offset:0 sc1\n\t" \
  "s_waitcnt vmcnt(7)\n\t" \
  "v_mfma_f32_16x16x32_bf16 v[132:135], v[100:103], v[196:199], v[132:135]\n\t" \
  "v_mfma_f32_16x16x32_bf16 v[140:143], v[108:111], v[196:199], v[140:143]\n\t" \
  "buffer_load_dwordx4 v[196:199], v239, %[rt], %[st] offen offset:1024 sc1\n\t" \
  "s_waitcnt vmcnt(7)\n\t" \
  "v_mfma_f32_16x16x32_bf16 v[128:131], v[112:115], v[200:203], v[128:131]\n\t" \
  "v_mfma_f32_16x16x32_bf16 v[136:139], v[120:123], v[200:203], v[136:139]\n\t" \
  "buffer_load_dwordx4 v[200:203], v239, %[rt], %[st4] offen offset:0 sc1\n\t" \
  "s_waitcnt vmcnt(7)\n\t" \
  "v_mfma_f32_16x16x32_bf16 v[132:135], v[116:119], v[204:207], v[132:135]\n\t" \
  "v_mfma_f32_16x16x32_bf16 v[140:143], v[124:127], v[204:207], v[140:143]\n\t" \
  "buffer_load_dwordx4 v[204:207], v239, %[rt], %[st4] offen offset:1024 sc1\n\t" \
  "s_nop 5\n\t" \
  "v_pk_add_f32 v[128:129], v[128:129], v[132:133]\n\t" \
  "v_pk_add_f32 v[130:131], v[130:131], v[134:135]\n\t" \
  "ds_write_b128 v230, v[128:131] offset:0\n\t" \
  "v_pk_add_f32 v[136:137], v[136:137], v[140:141]\n\t" \
  "v_pk_add_f32 v[138:139], v[138:139], v[142:143]\n\t" \
  "ds_write_b128 v230, v[136:139] offset:1024\n\t" \
  "v_add_u32 v230, 16384, v230\n\t" \
  "s_branch .Lt%=\n\t" \
  ".Ltl%=:\n\t" \
  "buffer_load_dwordx4 v[224:227], v243, %[rg], 0 offen sc1\n\t" \
  "s_waitcnt vmcnt(7)\n\t" \
  "v_mfma_f32_16x16x32_bf16 v[132:135], v[68:71], v[180:183], v[132:135]\n\t" \
  "v_mfma_f32_16x16x32_bf16 v[140:143], v[76:79], v[180:183], v[140:143]\n\t" \
  "s_waitcnt vmcnt(6)\n\t" \
  "v_mfma_f32_16x16x32_bf16 v[128:131], v[80:83], v[184:187], v[128:131]\n\t" \
  "v_mfma_f32_16x16x32_bf16 v[136:139], v[88:91], v[184:187], v[136:139]\n\t" \
  "s_waitcnt vmcnt(5)\n\t" \
  "v_mfma_f32_16x16x32_bf16 v[132:135], v[84:87], v[188:191], v[132:135]\n\t" \
  "v_mfma_f32_16x16x32_bf16 v[140:143], v[92:95], v[188:191], v[140:143]\n\t" \
  "s_waitcnt vmcnt(4)\n\t" \
  "v_mfma_f32_16x16x32_bf16 v[128:131], v[96:99], v[192:195], v[128:131]\n\t" \
  "v_mfma_f32_16x16x32_bf16 v[136:139], v[104:107], v[192:195], v[136:139]\n\t" \
  "s_waitcnt vmcnt(3)\n\t" \
  "v_mfma_f32_16x16x32_bf16 v[132:135], v[100:103], v[196:199], v[132:135]\n\t" \
  "v_mfma_f32_16x16x32_bf16 v[140:143], v[108:111], v[196:199], v[140:143]\n\t" \
  "s_waitcnt vmcnt(2)\n\t" \
  "v_mfma_f32_16x16x32_bf16 v[128:131], v[112:115], v[200:203], v[128:131]\n\t" \
  "v_mfma_f32_16x16x32_bf16 v[136:139], v[120:123], v[200:203], v[136:139]\n\t" \
  "s_waitcnt vmcnt(1)\n\t" \
  "v_mfma_f32_16x16x32_bf16 v[132:135], v[116:119], v[204:207], v[132:135]\n\t" \
  "v_mfma_f32_16x16x32_bf16 v[140:143], v[124:127], v[204:207], v[140:143]\n\t" \
  "s_nop 6\n\t" \
  "v_pk_add_f32 v[128:129], v[128:129], v[132:133]\n\t" \
  "v_pk_add_f32 v[130:131], v[130:131], v[134:135]\n\t" \
  "ds_write_b128 v230, v[128:131] offset:0\n\t" \
  "v_pk_add_f32 v[136:137], v[136:137], v[140:141]\n\t" \
  "v_pk_add_f32 v[138:139], v[138:139], v[142:143]\n\t" \
  "ds_write_b128 v230, v[136:139] offset:1024\n\t" \
  "s_waitcnt vmcnt(0) lgkmcnt(0)\n\t" \
  "s_branch .Lend%=\n\t" \
  ".Ltw%=:\n\t" \
  "s_add_u32 s92, s92, 1\n\t" \
  "s_sleep 2\n\t" \
  "s_and_b32 s93, s92, 63\n\t" \
  "s_cbranch_scc1 .Ltr%=\n\t" \
  "buffer_load_dword v164, off, %[rs], %[sab] sc1\n\t" \
  "s_waitcnt vmcnt(0)\n\t" \
  "v_readfirstlane_b32 s93, v164\n\t" \
  "s_nop 3\n\t" \
  "s_cmp_eq_u32 s93, 1\n\t" \
  "s_cbranch_scc1 .Lto%=\n\t" \
  "s_cmp_lt_u32 s92, 0x80000\n\t" \
  "s_cbranch_scc1 .Ltr%=\n\t" \
  "v_mov_b32 v164, 1\n\t" \
  "buffer_store_dword v164, off, %[rs], %[sab] sc1\n\t" \
  "s_waitcnt vmcnt(0)\n\t" \
  "s_branch .Lto%=\n\t" \
  ".Ltr%=:\n\t" \
  "buffer_load_dword v166, v242, %[rs], 0 offen sc1\n\t" \
  "s_waitcnt vmcnt(0)\n\t" \
  "s_branch .Ltc%=\n\t" \
  ".Llast%=:\n\t" \
  "v_mov_b32 v173, v242\n\t" \
  "s_branch .Lgol%=\n\t" \
  ".Lfw%=:\n\t" \
  "s_add_u32 s92, s92, 1\n\t" \
  "s_sleep 2\n\t" \
  "s_and_b32 s93, s92, 63\n\t" \
  "s_cbranch_scc1 .Lfr%=\n\t" \
  "buffer_load_dword v164, off, %[rs], %[sab] sc1\n\t" \
  "s_waitcnt vmcnt(0)\n\t" \
  "v_readfirstlane_b32 s93, v164\n\t" \
  "s_nop 3\n\t" \
  "s_cmp_eq_u32 s93, 1\n\t" \
  "s_cbranch_scc1 .Lfo%=\n\t" \
  "s_cmp_lt_u32 s92, 0x80000\n\t" \
  "s_cbranch_scc1 .Lfr%=\n\t" \
  "v_mov_b32 v164, 1\n\t" \
  "buffer_store_dword v164, off, %[rs], %[sab] sc1\n\t" \
  "s_waitcnt vmcnt(0)\n\t" \
  "s_branch .Lfo%=\n\t" \
  ".Lfr%=:\n\t" \
  "buffer_load_dwordx2 v[166:167], v173, %[rs], 0 offen sc1\n\t" \
  "s_branch .Lfc%=\n\t" \
  ".Lb1%=:\n\t" \
  "s_waitcnt vmcnt(0) lgkmcnt(0)\n\t" \
  "s_barrier\n\t" \
  "s_branch .Lb2%=\n\t" \
  ".Lbl%=:\n\t" \
  "s_bitset1_b64 %[ps], %[step]\n\t" \
  "s_cmp_lt_u32 s92, 3\n\t" \
  "s_cbranch_scc1 .Lbm%=\n\t" \
  "s_sleep 12\n\t" \
  ".Lbm%=:\n\t" \
  "s_add_u32 s92, s92, 1\n\t" \
  "s_sleep 2\n\t" \
  "s_and_b32 s93, s92, 63\n\t" \
  "s_cbranch_scc1 .Lba%=\n\t" \
  "buffer_load_dword v164, off, %[rs], %[sab] sc1\n\t" \
  "s_waitcnt vmcnt(0)\n\t" \
  "v_readfirstlane_b32 s93, v164\n\t" \
  "s_nop 3\n\t" \
  "s_cmp_eq_u32 s93, 1\n\t" \
  "s_cbranch_scc1 .Lgo%=\n\t" \
  "s_cmp_lt_u32 s92, 0x80000\n\t" \
  "s_cbranch_scc1 .Lba%=\n\t" \
  "v_mov_b32 v164, 1\n\t" \
  "buffer_store_dword v164, off, %[rs], %[sab] sc1\n\t" \
  "s_waitcnt vmcnt(0)\n\t" \
  "s_branch .Lgo%=\n\t" \
  ".Lba%=:\n\t" \
  "buffer_load_dwordx2 v[166:167], v173, %[rs], 0 offen sc1\n\t" \
  "s_branch .Lcal%=\n\t" \
  ".Lslow%=:\n\t" \
  "s_mov_b64 s[94:95], 0\n\t" \
  "v_cmp_eq_u32 vcc, %[sent], v128\n\t" \
  "s_or_b64 s[94:95], s[94:95], vcc\n\t" \
  "v_cmp_eq_u32 vcc, %[sent], v129\n\t" \
  "s_or_b64 s[94:95], s[94:95], vcc\n\t" \
  "v_cmp_eq_u32 vcc, %[sent], v130\n\t" \
  "s_or_b64 s[94:95], s[94:95], vcc\n\t" \
  "v_cmp_eq_u32 vcc, %[sent], v131\n\t" \
  "s_or_b64 s[94:95], s[94:95], vcc\n\t" \
  "v_cmp_eq_u32 vcc, %[sent], v132\n\t" \
  "s_or_b64 s[94:95], s[94:95], vcc\n\t" \
  "v_cmp_eq_u32 vcc, %[sent], v133\n\t" \
  "s_or_b64 s[94:95], s[94:95], vcc\n\t" \
  "v_cmp_eq_u32 vcc, %[sent], v134\n\t" \
  "s_or_b64 s[94:95], s[94:95], vcc\n\t" \
  "v_cmp_eq_u32 vcc, %[sent], v135\n\t" \
  "s_or_b64 s[94:95], s[94:95], vcc\n\t" \
  "v_cmp_eq_u32 vcc, %[sent], v136\n\t" \
  "s_or_b64 s[94:95], s[94:95], vcc\n\t" \
  "v_cmp_eq_u32 vcc, %[sent], v137\n\t" \
  "s_or_b64 s[94:95], s[94:95], vcc\n\t" \
  "v_cmp_eq_u32 vcc, %[sent], v138\n\t" \
  "s_or_b64 s[94:95], s[94:95], vcc\n\t" \
  "v_cmp_eq_u32 vcc, %[sent], v139\n\t" \
  "s_or_b64 s[94:95], s[94:95], vcc\n\t" \
  "v_cmp_eq_u32 vcc, %[sent], v140\n\t" \
  "s_or_b64 s[94:95], s[94:95], vcc\n\t" \
  "v_cmp_eq_u32 vcc, %[sent], v141\n\t" \
  "s_or_b64 s[94:95], s[94:95], vcc\n\t" \
  "v_cmp_eq_u32 vcc, %[sent], v142\n\t" \
  "s_or_b64 s[94:95], s[94:95], vcc\n\t" \
  "v_cmp_eq_u32 vcc, %[sent], v143\n\t" \
  "s_or_b64 s[94:95], s[94:95], vcc\n\t" \
  "s_cbranch_scc0 .Lok%=\n\t" \
  "s_bitset1_b64 %[fm], %[step]\n\t" \
  "s_add_u32 s92, s92, 1\n\t" \
  "s_sleep 1\n\t" \
  "s_and_b32 s93, s92, 63\n\t" \
  "s_cbranch_scc1 .Lre%=\n\t" \
  "buffer_load_dword v164, off, %[rs], %[sab] sc1\n\t" \
  "s_waitcnt vmcnt(0)\n\t" \
  "v_readfirstlane_b32 s93, v164\n\t" \
  "s_nop 3\n\t" \
  "s_cmp_eq_u32 s93, 1\n\t" \
  "s_cbranch_scc1 .Lok%=\n\t" \
  "s_cmp_lt_u32 s92, 0x80000\n\t" \
  "s_cbranch_scc1 .Lre%=\n\t" \
  "v_mov_b32 v164, 1\n\t" \
  "buffer_store_dword v164, off, %[rs], %[sab] sc1\n\t" \
  "s_waitcnt vmcnt(0)\n\t" \
  "s_branch .Lok%=\n\t" \
  ".Lre%=:\n\t" \
  "buffer_load_dwordx4 v[128:131], v234, %[rs], %[so] offen sc1\n\t" \
  "s_nop 15\n\t" \
  "buffer_load_dwordx4 v[132:135], v235, %[rs], %[so] offen sc1\n\t" \
  "s_nop 15\n\t" \
  "buffer_load_dwordx4 v[136:139], v236, %[rs], %[so] offen sc1\n\t" \
  "s_nop 15\n\t" \
  "buffer_load_dwordx4 v[140:143], v237, %[rs], %[so] offen sc1\n\t" \
  "v_mov_b64 v[144:145], 0\n\t" \
  "v_mov_b64 v[146:147], 0\n\t" \
  "v_mov_b64 v[148:149], 0\n\t" \
  "v_mov_b64 v[150:151], 0\n\t" \
  "v_mov_b64 v[152:153], 0\n\t" \
  "v_mov_b64 v[154:155], 0\n\t" \
  "v_mov_b64 v[156:157], 0\n\t" \
  "v_mov_b64 v[158:159], 0\n\t" \
  "s_waitcnt vmcnt(0)\n\t" \
  "s_branch .Lw%=\n\t" \
  ".Lend%=:\n\t"


__device__ __forceinline__ int look_prog(__amdgpu_buffer_rsrc_t rs, int off) {
  asm volatile("" ::: "memory");
  const int v = (int)ld_flag(rs, off);
  return min(__builtin_amdgcn_readlane(v, 0), __builtin_amdgcn_readlane(v, 1));
}
__device__ __forceinline__ int wait_prog(__amdgpu_buffer_rsrc_t rs, int off, int need, gu32* abortw) {
  int v;
  for (unsigned spins = 0;;) {
    v = look_prog(rs, off);
    if (v >= need) break;
    __builtin_amdgcn_s_sleep(4);
    if (spin_fail(spins, abortw)) break;
  }
  asm volatile("" ::: "memory");
  return v;
}
#define CLB_S "s90", "s91", "s92", "s93", "s94", "s95", "vcc", "scc", "memory"
#define W_IN "{v[0:31]}"(W[0]), "{v[32:63]}"(W[1]), "{v[64:95]}"(W[2]), "{v[96:127]}"(W[3]), "{v[128:159]}"(W[4]), "{v[160:191]}"(W[5])

#define LEAD0 408
#define LEAD_N0 5
#define LEAD_SH 2
#define LEAD_X0 8
#define LEAD_U0 24
#define LEAD_D0 0
#define T0_BACK 1000
#define QBJ 3
constexpr int L_MEM = 0, L_KT = 16384, L_QX = 24576, L_KX = 26624, L_VX = 16384, L_MISC = 30720, L_WL = 32768;

#define QK_STEP(TXT)                                                                                                        \
  asm volatile(TXT                                                                                                          \
      : [tout] "=&s"(tout), [spins] "=&s"(spins), [tb1] "=&s"(tb1), [tgt] "=&s"(tgt), "+{v248}"(aL16), "+{v249}"(aMW), "+{v250}"(aWL),                            \
        "+{v251}"(aIDX), "+{v252}"(aQW), "+{v[232:235]}"(QF)                                                                 \
      : W_IN, [rs] "s"(rs4), [som] "s"(o_mem(pm, s) + WV * 1024), [som2] "s"(o_mem(pm, s) + WV * 1024 + 16384), [sent] "s"(DSENT), [sab] "s"(SY_ABORT), \
        [lead] "s"(lead), [step] "s"(s), [sbc] "s"(SY_BA + side(pm, 1) + h * 128), [nh] "s"(0xFF00FF00FF00FF00ull), [up] "s"(0xFFFFFFFF00000000ull), [negc] "s"(-cexp),      \
        [c] "s"(cexp), [so] "s"(o_o(pm, s, h) + WV * 512), [sk] "s"(o_v(pm, s, h) + 2048 + (WV & 3) * 512),                    \
        [sv] "s"(o_v(pm, s, h) + WV * 512), [rt] "s"(rt4), [svt] "s"(t_off(p, T_V + kseg(s, h)) + WV * 2048)                  \
      : CLB8(192, 193, 194, 195, 196, 197, 198, 199), CLB8(200, 201, 202, 203, 204, 205, 206, 207),                          \
        CLB8(208, 209, 210, 211, 212, 213, 214, 215), CLB8(216, 217, 218, 219, 220, 221, 222, 223),                          \
        CLB8(224, 225, 226, 227, 228, 229, 230, 231), CLB8(236, 237, 238, 239, 240, 241, 242, 243), "v244", "v245", "v246", "v247", "v253", "s88", "s89", CLB_S)

template <int WV>
__device__ __forceinline__ void qk_wave(Args& p, int h, char* smem) {
  const int tid = threadIdx.x;
  unsigned pm = 0;
  int have = -1;
  u32x32 W[6];
  {
    int lane = tid & 63;
    asm volatile("" : "+v"(lane));
    const unsigned vo = (WV * 4 + (lane >> 4)) * (HD * 2) + 16 * ((lane & 15) ^ swzB((WV * 4 + (lane >> 4)) & 15));
    auto stage = [&](int j) {
#pragma unroll
      for (int mm = 0; mm < 2; ++mm) {
        const char* src = (const char*)((mm ? p.Wk : p.Wq) + (size_t)j * 128 * HD + h * DH);
        const unsigned dst = lds_off(smem) + (~j & 1) * 65536 + mm * 32768 + WV * 1024;
        glds2(src, 32 * HD * 2, vo, dst);
        if (TH_Q < 63) HOLD_V((h & 3) ^ 1, TH_Q);
        glds2(src + 64 * HD * 2, 32 * HD * 2, vo, dst + 16384);
        if (TH_Q < 63) HOLD_V((h & 3) ^ 1, TH_Q);
      }
    };
    bf16x8 keep[4][4];
    int f0 = -1;
    auto place = [&](int g) {
#pragma unroll
      for (int i = 0; i < 4; ++i)
        *reinterpret_cast<bf16x8*>(smem + (g == 0 ? 65536 : g == 1 ? 32768 : g == 2 ? 131072 : 98304) + WV * 4096 + i * 1024 + lane * 16) = keep[g][i];
    };
    stage(0); stage(1);
    Boot bt;
#pragma unroll
    for (int j = 0; j < 8; ++j) {
      if (j < 7) WAIT_V(8); else WAIT_V(0);
      if (WV == 7 && j == QBJ + 1) pm = boot_done(p.ws, bt);
      if ((WV == 4 || WV == 5) && j == 7) {
        asm volatile("" : "+v"(f0));
        have = min(__builtin_amdgcn_readlane(f0, 0), __builtin_amdgcn_readlane(f0, 1));
      }
      LBAR();
#pragma unroll
      for (int k = 0; k < 8; ++k) {
        const int P = (QK_MAP[WV] >> (4 * (2 * j + (k >> 2)))) & 15;
        const bf16x8 fr = ld_bfrag(smem + (~j & 1) * 65536 + (WV >> 2) * 32768 + (k >> 2) * 16384, k & 1, 2 * (WV & 3) + ((k >> 1) & 1), lane);
        const int f = P * 4 + (k & 3);
        if (P < 12) put4(W[f >> 3], f & 7, fr);
        else keep[P - 12][k & 3] = fr;
      }
      if (j == (WV >> 1)) place(2);
      LBAR();
      if (WV == 7 && j == QBJ) boot_ask(p.ws, bt);
      if (j + 2 < 8) stage(j + 2);
      if (j == 6) {
        if (WV == 4 || WV == 5)
          asm volatile("buffer_load_dword %0, %1, %2, 0 offen sc1" : "=&v"(f0) : "v"(fkq(h, (lane & 1) * 2)), "s"(raw_rsrc(p.ws + WS_SY, SY_TOTAL)) : "memory");
        place(0); place(3);
      }
    }
    place(1);
    if (WV == 7 && lane < 8) lds_at(smem + L_MISC)[lane] = lane == 7 ? pm : (lane == 1 ? now_q() - T0_BACK : 0);
    if (WV == 6) lds_at(smem + 28672)[lane] = ~0u;
    LBAR();
    pm = uni(lds_at(smem + L_MISC)[7]);
    p.s0 = pm >> 31;
    if (WV == 4 || WV == 5) {
      if (have < 0) have = wait_prog(mkrsrc(p.ws + WS_SY, SY_TOTAL), fkq(h, (lane & 1) * 2), 0, (gu32*)(p.ws + WS_SY + SY_ABORT));
      const char* src = p.ws + WS_T + t_off(p, kseg(0, h)) + (WV - 4) * 4096 + lane * 16;
#pragma unroll
      for (int i = 0; i < 4; ++i) glds16_sc1(src + i * 1024, smem + L_KT + (WV - 4) * 4096 + i * 1024);
    }
  }
  const __amdgpu_buffer_rsrc_t rsy = mkrsrc(p.ws + WS_SY, SY_TOTAL);
  const u32x4 rs4 = raw_rsrc(p.ws + WS_SY, SY_TOTAL);
  const u32x4 rt4 = raw_rsrc(p.ws + WS_T, T_BYTES);
  gu32* abortw = (gu32*)(p.ws + WS_SY + SY_ABORT);
  const float cexp = 0.08838834764831845f * 1.4426950408889634f;
  constexpr int m = WV >> 2, b = WV & 3;
  int lead = LEAD0;
  unsigned tb1 = 0;
  unsigned long long fm = 0, ps = 0; (void)fm; (void)ps;
  for (int s = 0; s < NSEG; ++s) {
    int lane = tid & 63;
    asm volatile("" : "+v"(lane));
    const int r = lane & 15, q = lane >> 4;
    unsigned aL16 = lane * 16, aMW = aL16 + WV * 1024, aWL = aL16 + 98304 + WV * 4096;
    unsigned aIDX = (lane & 2) ? 0xEEEE : 0x4444;
    unsigned aQW = (32 * b + r) * 16 + q * 4 + (m ? L_KX : L_QX);
    u32x4 QF;
    asm volatile("" : "={v[232:235]}"(QF));
    if (WV < 4) {
      QF[0] = L_QX + (q * 8 + (r >> 2)) * 16 + (r & 1) * 8;
      QF[1] = ((2 * (q & 1) + (q >> 1)) * 16 + r) * 4;
      QF[2] = ((r >> 2) & 1) * 65536 + ((r >> 1) & 1) * 64 + (r >> 3) * 32 + (r & 1) * 16 + (q >> 1) * 256 + (q & 1) * 8;
    } else QF[3] = r * 16 + q * 4;
    unsigned tout, spins, tgt;
    if (WV == 0) QK_STEP(QK_ASM_0); else if (WV == 1) QK_STEP(QK_ASM_1); else if (WV == 2) QK_STEP(QK_ASM_2);
    else if (WV == 3) QK_STEP(QK_ASM_3); else if (WV == 4) QK_STEP(QK_ASM_4); else if (WV == 5) QK_STEP(QK_ASM_5);
    else if (WV == 6) QK_STEP(QK_ASM_6); else QK_STEP(QK_ASM_7);
    tout = __builtin_amdgcn_readfirstlane(tout);
    tb1 = __builtin_amdgcn_readfirstlane(tb1);
    lane = tid & 63;
    asm volatile("" : "+v"(lane));
    if (WV < 4) {
      lds_at(smem + L_MISC + 8)[WV] = s + 1;
      if (WV == 3) { lds_at(smem + L_MISC)[6] = time_of(tout); lds_at(smem + 28672)[2] = s; }
    }
    if (WV == 6) {
      if (s + 1 < NSEG) watch(rsy, s, lane, now_q(), (const unsigned*)(smem + L_MISC), abortw, side(pm, 1));
    } else if (WV >= 4 && s + 1 < NSEG) {
      unsigned t;
      do {
        __builtin_amdgcn_s_sleep(1);
        t = lds_at(smem + L_MISC + 8)[lane & 3];
      } while (!__all(t == (unsigned)(s + 1)));
      if (WV <= 5) {
        if (s + 2 < NSEG) {
          if (have < s + 1) have = wait_prog(rsy, fkq(h, (lane & 1) * 2), s + 1, abortw);
          const char* src = p.ws + WS_T + t_off(p, kseg(s + 1, h)) + (WV - 4) * 4096 + lane * 16;
#pragma unroll
          for (int i = 0; i < 4; ++i) glds16_sc1(src + i * 1024, smem + L_KT + (WV - 4) * 4096 + i * 1024);
          if (have < s + 2) have = look_prog(rsy, fkq(h, (lane & 1) * 2));
        }
      } else {
        do __builtin_amdgcn_s_sleep(2); while (__builtin_amdgcn_readfirstlane(lds_at(smem + 28672)[2]) != (unsigned)s);
        const int x = (int)(__builtin_amdgcn_readfirstlane(lds_at(smem + L_MISC)[6]) - tb1);
        lead = __builtin_amdgcn_readfirstlane(track(lead, x + (s < LEAD_N0 ? LEAD_X0 : 0), s < LEAD_N0 ? LEAD_U0 : LEAD_UP, s < LEAD_N0 ? LEAD_D0 : (s < 3 * LEAD_N0 ? min(max(LEAD_DN, (lead - x) >> LEAD_SH), 16) : LEAD_DN), 60));
      }
    }
  }
}

__device__ __forceinline__ void qk_role(Args& p, int h, char* smem) {
  switch (__builtin_amdgcn_readfirstlane(threadIdx.x >> 6)) {
    case 0: qk_wave<0>(p, h, smem); break;
    case 1: qk_wave<1>(p, h, smem); break;
    case 2: qk_wave<2>(p, h, smem); break;
    case 3: qk_wave<3>(p, h, smem); break;
    case 4: qk_wave<4>(p, h, smem); break;
    case 5: qk_wave<5>(p, h, smem); break;
    case 6: qk_wave<6>(p, h, smem); break;
    default: qk_wave<7>(p, h, smem); break;
  }
}

#define K_DUE 50
#define K_DUE2 500
#define T_SL 2
#define O_DUE 400

constexpr int V_VM = 16384, V_T = 32768, V_KI = 131072;
#define TK_CALL(sg, od_) \
  asm volatile(TK_ASM \
            : \
            : "{v200}"(lane * 16), "{v[208:215]}"(bias), "{v[216:219]}"(ta), \
              [rs] "s"(rs4), [rt] "s"(raw_rsrc(p.ws + WS_T, T_BYTES)), [sent] "s"(DSENT), [sab] "s"(SY_ABORT), [sk] "s"(o_v(pm, sg, h) + 2048), \
              [tl] "s"(V_T + (sg & 3) * 24576), [vp] "s"((sg & 1) * 2048), [tq] "s"((w & 1) * 4096), [tv] "s"((w & 2) * 2048), \
              [so] "s"(t_off_o(p, T_O + (sg * 2 + (w & 1)) * 16 + (h >> 1)) + (h & 1) * 4096 + (w & 2) * 1024), \
              [up] "s"(0xFFFFFFFF00000000ull), [c] "s"(1.4426950408889634f), [negc] "s"(-1.4426950408889634f), \
              [sc] "s"((unsigned long long)__float_as_uint(0.08838834764831845f)), [od] "s"(od_) \
            : CLB32(128), CLB32(160), CLB8(192, 193, 194, 195, 196, 197, 198, 199), CLB8(224, 225, 226, 227, 228, 229, 230, 231), \
              "v205", "v206", "v207", "s88", "s89", CLB_S)
__device__ __forceinline__ void vu_role(Args& p, int h, char* smem) {
  const int tid = threadIdx.x, lane = tid & 63;
  const int w = __builtin_amdgcn_readfirstlane(tid >> 6);
  u32x32 W[4];
  {
    const unsigned vo = (tid >> 4) * (HD * 2) + 16 * ((lane & 15) ^ swzB((tid >> 4) & 15));
    auto stage = [&](int j) {
      const char* src = (const char*)(p.Wv + (size_t)j * 128 * HD + h * DH);
      const unsigned dst = lds_off(smem) + L_WL + (j & 3) * 32768 + w * 1024;
      glds2(src, 32 * HD * 2, vo, dst);
      glds2(src + 64 * HD * 2, 32 * HD * 2, vo, dst + 16384);
    };
    start_sy(p.ws, p.mem0, h, smem);
    Boot bt;
#pragma unroll
    for (int u = 0; u < 2; ++u) {
#pragma unroll
      for (int j = 0; j < 4; ++j) stage(4 * u + j);
      if (u && w == 7) boot_ask(p.ws, bt);
      WAIT_V(0);
      if (u && w == 7) {
        const unsigned m = boot_done(p.ws, bt);
        if (lane < 8) lds_at(smem + L_MISC)[lane] = lane == 7 ? m : (lane == 1 ? now_q() : 0);
      }
      LBAR();
#pragma unroll
      for (int k = 0; k < 16; ++k) put4(W[2 * u + (k >> 3)], k & 7, ld_bfrag(smem + L_WL + ((k >> 1) ^ w) * 16384, k & 1, w, lane));
      LBAR();
    }
  }
  const __amdgpu_buffer_rsrc_t rsy = mkrsrc(p.ws + WS_SY, SY_TOTAL);
  const u32x4 rs4 = raw_rsrc(p.ws + WS_SY, SY_TOTAL);
  gu32* abortw = (gu32*)(p.ws + WS_SY + SY_ABORT);
  const unsigned pm = uni(lds_at(smem + L_MISC)[7]);
  p.s0 = pm >> 31;
  unsigned long long fm = 0; (void)fm;
  int have = -1;
  auto fetch = [&](int sg) {
    if (have < sg) have = wait_prog(rsy, fkq(h, w - 4), sg, abortw);
    const char* src = p.ws + WS_T + t_off(p, (w - 4) * T_Q + kseg(sg, h)) + lane * 16;
#pragma unroll
    for (int i = 0; i < 8; ++i) glds16_sc1(src + i * 1024, smem + V_T + (sg & 3) * 24576 + (w - 4) * 8192 + i * 1024);
  };
  const int fo = SY_FO + ((w & 3) * NH + h) * 128;
  typedef __attribute__((ext_vector_type(8))) float f32x8;
  f32x8 bias;
  typedef __attribute__((ext_vector_type(8))) unsigned u32x8;
  u32x8 ca;
#pragma unroll
  for (int i = 0; i < 8; ++i) ca[i] = lane * 16 + (i ^ w) * 1024;
  u32x4 ta;
  {
    const int r = lane & 15, q = lane >> 4;
#pragma unroll
    for (int j = 0; j < 8; ++j) bias[j] = (j >> 2) * 16 + 4 * q + (j & 3) <= 16 * (w & 1) + r ? INFINITY : -INFINITY;
    ta[0] = V_KI + w * 2048 + lane * 16;
    ta[1] = V_KI + w * 2048 + (q * 8 + (r >> 2)) * 16 + (r & 1) * 8;
    ta[2] = V_VM + (w & 2) * 512 + ((2 * (q & 1) + (q >> 1)) * 16 + r) * 4;
    ta[3] = 0;
  }
#pragma unroll 1
  for (int s = 0; s < NSEG; ++s) {
    unsigned spins, tgt;
    asm volatile(VV_ASM
        : [spins] "=&s"(spins), [tgt] "=&s"(tgt)
        : W4_IN, "{v200}"(lane * 16), "{v[232:239]}"(ca), "{v202}"((lane & 2) ? 0xEEEE : 0x4444), "{v203}"(lane * 4),
          "{v204}"(tid * 4 + V_VM + (s & 1) * 2048), [rs] "s"(rs4), [som] "s"(o_mem(pm, s) + w * 1024), [som2] "s"(o_mem(pm, s) + w * 1024 + 16384), [step] "s"(s),
          [sent] "s"(DSENT), [sab] "s"(SY_ABORT), [sv] "s"(o_v(pm, s, h) + w * 256)
        : CLB32(128), CLB32(160), CLB8(192, 193, 194, 195, 196, 197, 198, 199), CLB_S);
    if (w < 4 || s == NSEG - 1) {
      const int sg = s - (w < 4);
      if (w >= 4) {
        if (lane == 0) __builtin_amdgcn_raw_buffer_store_b32(1u, rsy, fo + 4 * NH * 128, 0, 16);
        pace_until(now_q() + K_DUE);
      }
      if (sg >= 0 && sg != NSEG - 2) {
        TK_CALL(sg, s < NSEG - 1 ? O_DUE : 0);
        if (sg >= NSEG - 3) {
          static_assert(TLAG == 2, "");
          WAIT_V(0);
          if (lane == 0) __builtin_amdgcn_raw_buffer_store_b32(1u, rsy, fo + (sg - NSEG + 3) * 4 * NH * 128, 0, 16);
        }
      }
    } else if (w == 7) {
      watch(rsy, s, lane, now_q(), (const unsigned*)(smem + L_MISC), abortw, side(pm, 1));
    } else if (w >= 4) {
      __builtin_amdgcn_s_sleep(T_SL);
      const int s1 = w == 5 && s < NSEG - 3 ? s : s + 1;
#pragma unroll 1
      for (int sg = (w == 5 ? s < NSEG - 2 : s == 0) ? s : s + 1; sg <= s1; ++sg) fetch(sg);
      if (have < s1 + 1 && s1 + 1 < NSEG) have = look_prog(rsy, fkq(h, w - 4));
      if (w == 5 && s + NPRE < NSEG) {
#pragma unroll
        for (int i = 0; i < 4; ++i) st_sc1(rsy, o_v(pm, s + NPRE, h) + i * 1024 + lane * 16, (u32x4){DSENT, DSENT, DSENT, DSENT});
      }
    }
    if (s == NSEG - 2 && w >= 4) {
      pace_until(tgt + K_DUE2);
      TK_CALL(s, 0);
    }
  }
}

#define RL_UP 6
#define RL_DN 4
#define RED_MIN 40
#define BA_LOOK 360
constexpr int R_SI = 32768, R_PART = 98304;

__device__ __forceinline__ void red_role(Args& p, int j, char* smem) {
  const int tid = threadIdx.x;
  int lane = tid & 63;
  const int w = __builtin_amdgcn_readfirstlane(tid >> 6);
  const int c = (j & 2) * 8 + (j >> 2), rh = j & 1;
  const int hg = (w + (j >> 2)) & 7, rot = (j >> 2) + (j >> 5);
  const __amdgpu_buffer_rsrc_t rsy = mkrsrc(p.ws + WS_SY, SY_TOTAL);
  const u32x4 rs4 = raw_rsrc(p.ws + WS_SY, SY_TOTAL);
  const __amdgpu_buffer_rsrc_t rt = mkrsrc(p.ws + WS_T, T_BYTES);
  gu32* abortw = (gu32*)(p.ws + WS_SY + SY_ABORT);
  u32x32 W[4];
  u32x2 tag = {0u, 0u};
  {
    const u16* Wo = p.Wo + (size_t)(4 * hg * DH) * DM + 32 * c;
    const int ro = (lane >> 2) * 64 + (lane & 3) * 8;
    Boot bt;
    load_blocks<16>(smem + 32768 + w * 16384,
        [&](int i, char* slot) {
          if (i == 12 && w == 0) boot_ask(p.ws, bt);
          if ((i < 2 || i == 4 || i == 8) && (c & 14) != 6 && lane < 10) {
            const int x = i < 2 ? 3 - i : i >> 3, n = min(((c & 15) - ((c & 8) >> 2)) * 10 + lane, 127);
            touch_line((const char*)p.Wo + (size_t)(w * 512 + (x >> 1) * 256 + (x & 1) * 64 + (n & 64) + n) * (DM * 2) + (c & 16) * 64 + 384, smem + w * 256);
          }
          const int x = ((i >> 2) + rot) & 3;
          blk_dma(Wo + (size_t)((x >> 1) * 256 + (x & 1) * 64 + ((i >> 1) & 1) * 128 + (i & 1) * 32) * DM, DM, slot, lane);
        },
        [&](int i, char* slot) {
#pragma unroll
          for (int t = 0; t < 2; ++t) {
            s16x4 lo = __builtin_amdgcn_ds_read_tr16_b64_v4i16((lds_s16x4_ptr)(slot + ro + t * 32));
            s16x4 hi = __builtin_amdgcn_ds_read_tr16_b64_v4i16((lds_s16x4_ptr)(slot + ro + t * 32 + 1024));
            put4(W[i >> 2], (i & 2) * 2 + 2 * t + (i & 1), (bf16x8){lo[0], lo[1], lo[2], lo[3], hi[0], hi[1], hi[2], hi[3]});
          }
        });
    if (w == 0) { lds_at(smem + 4128)[0] = boot_done(p.ws, bt); tag = next_tag(bt.e); }
    LBAR();
  }
  const unsigned pm = uni(lds_at(smem + 4128)[0]);
  p.s0 = pm >> 31;
  const int hs = side(pm, 1);
  {
    const u32x4 ds = {DSENT, DSENT, DSENT, DSENT};
#pragma unroll
    for (int i = 0; i < 2; ++i) {
      const int s = (w * 2 + i) * 4 + (lane >> 4);
      if (s >= 2) st_sc1(rsy, o_mem(pm, s) + (c >> 4) * 16384 + (c & 15) * 512 + rh * 128 + ((lane >> 3) & 1) * 256 + (lane & 7) * 16, ds);
    }
  }
  if (tid < 4) { reinterpret_cast<unsigned*>(smem + 4096)[tid] = tid == 3 ? now_q() : 0; reinterpret_cast<unsigned*>(smem + 4112)[tid] = DSENT; }
  if (tid < 256) reinterpret_cast<int*>(smem + 5120)[tid] = ((tid >> 2) - TLAG - 6) >> 2;
  LBAR();
  const unsigned base = __builtin_amdgcn_readfirstlane(lds_at(smem + 4096)[3]);
  unsigned long long fm = 0, ps = 0;
  const unsigned aL = lane * 16;
  const int e = ((56 + w) * SEG + rh * 16 + (lane >> 2)) * DM + c * 32 + (lane & 3) * 8;
  u32x4 xv = *reinterpret_cast<const u32x4*>(p.x + e), av = {DSENT, DSENT, DSENT, DSENT};
  asm volatile("" : "+v"(xv));
  {
    const int r = lane & 15, q = lane >> 4;
    const int sm0 = o_mem(pm, 0) + (c >> 4) * 16384 + (c & 15) * 512 + rh * 128;
    u32x4 VO, VT;
    unsigned fo = SY_FO + ((rh + (lane & 4) / 2) * NH + 4 * hg + (lane & 3)) * 128;
    unsigned ao = w == 1 ? (c * S_LEN + rh * 16 - (TLAG + 3) * SEG) * 64 + lane * 16
                         : (w >= 4 ? SY_O + NPRE * 131072 + side(pm, w - 2) + (j >> 3) * 16384 + (j & 7) * 1024 + lane * 16
                                   : SY_FS + (2 * c + rh) * 128 + (lane < 8 ? lane * 16 : 0x70000000));
    unsigned ar = w == 1 ? R_SI - (TLAG + 2) * 1024 + lane * 16 : (w == 2 ? 5120 : 4112);
    const unsigned long long am = w == 1 ? ((1ull << 56) - 1) << (TLAG + 3) : (w == 2 ? 0x11111111111111ull << (TLAG + 7) : (w >= 4 ? (0xFFFFull << (16 * (w - 4))) >> NPRE : 0));
    unsigned pw = R_PART + w * 2048 + lane * 16, pr = R_PART + 16384 + tid * 4;
    unsigned sw = R_SI - (TLAG + 1) * 1024 + (lane >> 2) * 64 + w * 8 + (lane & 3) * 2, stm = 8192 + w * 512;
#pragma unroll
    for (int l = 0; l < 4; ++l) {
      const int x = (l + rot) & 3;
      VO[l] = o_o(0, 0, 4 * hg) + rh * 65536 + x * 1024 + aL;
      VT[l] = (T_O + rh * 16 + 2 * hg + (x >> 1)) * 16384 + (x & 1) * 2048 + aL;
    }
    unsigned ba = lane < 4 ? SY_BA + hs + (4 * hg + lane) * 128 : 0x7F000000u;
    unsigned sm = 0;
    int rl = RL0;
    unsigned step, E, so, st, st4, sh;
    asm volatile(RED_ASM
        : [step] "=&s"(step), [E] "=&s"(E), [so] "=&s"(so), [st] "=&s"(st), [st4] "=&s"(st4), [sh] "=&s"(sh),
          [rl] "+s"(rl), [sm] "+s"(sm), [fm] "+s"(fm), [ps] "+s"(ps),
          "+{v[224:227]}"(av), "+{v228}"(ao), "+{v229}"(ar), "+{v230}"(pw), "+{v231}"(pr), "+{v232}"(sw), "+{v233}"(stm), "+{v242}"(fo), "+{v173}"(ba)
        : W4_IN, "{v168}"(aL), "{v169}"(w * 512 + lane * 8), "{v170}"(lane * 8), "{v174}"((r & 1) ? 0x03020706u : 0x05040100u),
          "{v171}"(sm0 + 32768 + (q >> 1) * 256 + (q & 1) * 64 + ((r >> 1) & 1) * 32 + (r & 1) * 16 + (r >> 2) * 4),
          "{v172}"((lane & 2) ? 0xEEEE : 0x4444),
          "{v175}"(w == 3 && lane < 32 ? SY_BR + hs + j * 128 + lane * 4 : 0x7F000000u), "{v[234:237]}"(VO), "{v[238:241]}"(VT), "{v243}"(e * 2),
          [rg] "s"(raw_rsrc(p.ws + WS_G, S_LEN * DM * 2)), [fg] "s"(SY_FG + ((14 + (w >> 2)) * 8 + (c >> 2)) * 128),
          [rs] "s"(rs4), [ra] "s"(w == 1 ? raw_rsrc(p.ws + WS_S, S_LEN * DM * 2) : rs4), [rt] "s"(raw_rsrc(p.ws + WS_T, T_BYTES)),
          [ds] "n"(DS_RED), [wm] "n"(REF_W), [gm] "n"(REF_G), [mn] "n"(RED_MIN), [lk] "n"(DS_MEM + BA_LOOK), [up] "n"(RL_UP), [dn] "n"(-RL_DN), [sent] "s"(DSENT), [sab] "s"(SY_ABORT),
          [base] "s"(base), [pm] "s"(pm), [s0] "s"((p.s0 ^ TF_O) << 13), [am] "s"(am), [ainc] "s"(w == 1 ? SEG * 64 : (w >= 4 ? 131072 : 0)),
          [rinc] "s"(w == 1 ? 1024 : (w == 2 ? 16 : 0))
        : CLB32(128), CLB8(160, 161, 162, 163, 164, 165, 166, 167), CLB8(176, 177, 178, 179, 180, 181, 182, 183),
          CLB8(184, 185, 186, 187, 188, 189, 190, 191), CLB8(192, 193, 194, 195, 196, 197, 198, 199), CLB8(200, 201, 202, 203, 204, 205, 206, 207),
          CLB8(208, 209, 210, 211, 212, 213, 214, 215), CLB8(216, 217, 218, 219, 220, 221, 222, 223), CLB_E, "s96", CLB_S);
  }
  LBAR();
  u32x4 sv;
  if (w < 6 - TLAG) sv = *reinterpret_cast<const u32x4*>(smem + R_SI + (56 + w) * 1024 + lane * 16);
  else {
    const f32x4* part = reinterpret_cast<const f32x4*>(smem + (w == 6 - TLAG ? R_PART : (w - 6 + TLAG) * 16384)) +
                        (lane & 2) * 32 + (lane & 1) * 32 + (lane >> 2);
    f32x4 x[8][2];
#pragma unroll
    for (int ww = 0; ww < 8; ++ww) { x[ww][0] = part[ww * 128]; x[ww][1] = part[ww * 128 + 16]; }
#pragma unroll
    for (int u = 0; u < 2; ++u) {
      const f32x4 a = ((x[0][u] + x[1][u]) + (x[2][u] + x[3][u])) + ((x[4][u] + x[5][u]) + (x[6][u] + x[7][u]));
      sv[2 * u] = cvtpk(a[0], a[1]); sv[2 * u + 1] = cvtpk(a[2], a[3]);
    }
  }
  st_out(p, e, combine(sv, av, xv));
  if (j == 0 && tid == 0) st2_sc1(rsy, SY_EP, tag);
}


__global__ __launch_bounds__(512, 2) void main_kernel(Args p) {
  char* smem = g_smem;
  p.ws = ws_align(p.ws);
  const int bx = blockIdx.x & 7, bk = blockIdx.x >> 3;
  const bool isg = (bx ^ XPROBE) & 4;
  const int b = (isg ? NCHAIN : 0) + bk * 4 + (bx & 3);
  if (threadIdx.x < 64) {
    unsigned long long pc; unsigned len, keep;
    asm volatile("s_getpc_b64 %0\n.Lpc%=:\n\ts_mov_b32 %1, .Lcode_end-.Lpc%=" : "=s"(pc), "=s"(len));
    asm volatile("s_mov_b32 %0, m0\n\ts_mov_b32 m0, %3\n\ts_nop 0\n\tbuffer_load_dword %1, %2, 0 offen lds\n\ts_mov_b32 m0, %0"
                 : "=&s"(keep) : "v"((bk * 40 + threadIdx.x) * 128), "s"(raw_rsrc((const void*)pc, len)), "s"((b >> 6) == 1 ? 2048 : 163328) : "memory");
  }
  if ((bk >> 3) == (isg ? 3 : 1) && (threadIdx.x >> 6) == 1) {
    constexpr int NI = TLB_NI + 1, NW = (int)(WS_END / TLB_WS);
    const int n = (threadIdx.x & 63) * 8 + (bk & 7), m = n / NI, i = n % NI;
    const u16* q = m == 0 ? p.Wq : m == 1 ? p.Wk : m == 2 ? p.Wv : m == 3 ? p.Wo : m == 4 ? p.wg : m == 5 ? p.wu : m == 6 ? p.wd : m == 7 ? p.x : p.out;
    static_assert(9 * NI + NW <= 512, "");
    if (n < 9 * NI + NW)
      touch_line(m < 9 ? (const char*)q + min(i * (DM * HD * 2 / TLB_NI), (m < 4 ? HD : m < 7 ? FF : S_LEN) * DM * 2 - 128)
                       : p.ws + (size_t)(n - 9 * NI + 1) * TLB_WS - 16384, smem + 163072);
  }
  if (b >= NCHAIN) gemm_role(p, b - NCHAIN, smem);
  else if (b < 32) qk_role(p, (b & 3) * 8 + bk, smem);
  else if (b < 64) vu_role(p, (b & 3) * 8 + bk - 8, smem);
  else red_role(p, b - 64, smem);
}

extern "C" void kernel_launch(void* const* d_in, const int* in_sizes, int n_in,
                              void* d_out, int out_size, void* d_ws, size_t ws_size,
                              hipStream_t stream) {
  Args a;
  a.x = (const u16*)d_in[0]; a.mem0 = (const u16*)d_in[1];
  a.Wq = (const u16*)d_in[2]; a.Wk = (const u16*)d_in[3]; a.Wv = (const u16*)d_in[4]; a.Wo = (const u16*)d_in[5];
  a.wg = (const u16*)d_in[8]; a.wu = (const u16*)d_in[9]; a.wd = (const u16*)d_in[10];
  a.ws = (char*)d_ws; a.out = (u16*)d_out; a.s0 = 0; a.pad1 = 0;
  main_kernel<<<256, 512, 0, stream>>>(a);
}
```

```cpp
#include <hip/hip_runtime.h>
#include <hip/hip_bf16.h>
#include <stdint.h>

constexpr int S_LEN = 2048, DM = 1024, NH = 32, DH = 128, NMEM = 8, SEG = 32;
constexpr int NSEG = S_LEN / SEG;
constexpr int HD = NH * DH;
constexpr int FF = 3584;

typedef unsigned short u16;
typedef __attribute__((ext_vector_type(8))) short bf16x8;
typedef __attribute__((ext_vector_type(4))) short s16x4;
typedef __attribute__((ext_vector_type(4))) float f32x4;
typedef __attribute__((ext_vector_type(4))) unsigned int u32x4;
typedef __attribute__((ext_vector_type(2))) unsigned int u32x2;
typedef __attribute__((address_space(3))) s16x4* lds_s16x4_ptr;
typedef __attribute__((address_space(3))) void* lds_void_ptr;
typedef __attribute__((address_space(1))) unsigned gu32;
typedef __bf16 bf16x2_t __attribute__((ext_vector_type(2)));
typedef float f32x2_t __attribute__((ext_vector_type(2)));

__device__ __forceinline__ unsigned cvtpk(float lo, float hi) {
  bf16x2_t r = __builtin_convertvector((f32x2_t){lo, hi}, bf16x2_t);
  return __builtin_bit_cast(unsigned, r);
}
__device__ __forceinline__ float lo_f(unsigned v) { return __uint_as_float(v << 16); }
__device__ __forceinline__ float hi_f(unsigned v) { return __uint_as_float(v & 0xffff0000u); }
__device__ __forceinline__ float rbf(float f) { return lo_f(cvtpk(f, f)); }
__device__ __forceinline__ bf16x8 as_bf16x8(u32x4 v) { return __builtin_bit_cast(bf16x8, v); }
#define MFMA(a, b, c) __builtin_amdgcn_mfma_f32_16x16x32_bf16((a), (b), (c), 0, 0, 0)

#define KSTR 65
#define XLD 1024
constexpr int PAGE = 2 << 20, NPAGE = 14;
constexpr int T_K = 0, T_Q = 2304, T_V = 4608, T_O = 6912, T_END = T_O + 2048;
constexpr unsigned T_BYTES = T_END * 16384u;
constexpr size_t WS_SY = 0;
constexpr size_t WS_T = (size_t)NPAGE * PAGE;
constexpr size_t WS_XN = WS_T + T_BYTES;
constexpr size_t WS_HF = WS_XN + (size_t)S_LEN * XLD * 2;
constexpr size_t WS_G = WS_HF + (size_t)S_LEN * 16384;
constexpr size_t WS_S = WS_G + (size_t)S_LEN * DM * 2;
constexpr size_t WS_END = WS_S + (size_t)S_LEN * DM * 2;
constexpr unsigned SENT = 0xFFFFFFFFu;
constexpr unsigned DSENT = 0xFFBFFFBFu;
constexpr int SY_MEM = 0;
constexpr int SY_O = 2 * PAGE;
constexpr int SY_V = 6 * PAGE;
constexpr int SY_BR = PAGE, SY_BA = SY_BR + 16384;
constexpr int SY_FKQ = SY_BR + 32768;
constexpr int SY_FG = SY_FKQ + NH * 4 * 128;
constexpr int SY_FO = SY_FG + 16 * 8 * 128;
constexpr int SY_CH = SY_FO + 384 * 128;
constexpr int SY_ABORT = SY_CH + 8 * 128;
constexpr int SY_FS = SY_ABORT + 256;
constexpr int SY_FEND = SY_FS + 64 * 128;
constexpr int SY_EP = SY_FEND, SY_BT = SY_EP + 128;
constexpr int SY_TOTAL = NPAGE * PAGE;
constexpr int PROBE = PAGE - 16384 + 6144;
__device__ __forceinline__ int side(unsigned pm, int page) { return ((pm >> page) & 1u) << 13; }
__device__ __forceinline__ int o_mem(unsigned pm, int s) { return SY_MEM + s * 32768 + side(pm, 0); }
__device__ __forceinline__ int o_o(unsigned pm, int s, int h) {
  return SY_O + s * 131072 + (h >> 3) * 16384 + ((h >> 1) & 3) * 2048 + (h & 1) * 128 + side(pm, 2 + (s >> 4));
}
__device__ __forceinline__ int o_v(unsigned pm, int s, int h) { return SY_V + s * 262144 + (h >> 1) * 16384 + (h & 1) * 4096 + side(pm, 6 + (s >> 3)); }
__device__ __forceinline__ char* ws_align(char* ws) { return ws + ((PAGE - (unsigned)(uintptr_t)ws) & (PAGE - 1)); }
#define NFUSE 12
constexpr int NPRE = 4;
static_assert(WS_END + PAGE <= (size_t)256 << 20 && NH * KSTR <= T_Q && NPAGE % 2 == 0, "");
constexpr int NCHAIN = 128;
__host__ __device__ constexpr int kseg(int sg, int h) { return h * KSTR + sg; }
__host__ __device__ constexpr int fkq(int h, int kind) { return SY_FKQ + (h * 4 + kind) * 128; }

#define TLB_NI 16
#define TLB_WS (2 << 20)
#define XPROBE 0
struct Args {
  const u16 *x, *mem0, *Wq, *Wk, *Wv, *Wo, *wg, *wu, *wd;
  char* ws;
  u16* out;
  int s0, pad1;
};
__device__ __forceinline__ int h_off(const Args& p, int rblk) { return rblk * PAGE + (((p.s0 ^ rblk ^ 1) & 1) << 13); }
static_assert(WS_HF % (2 * PAGE) == 0, "");
#define TF_KQV 1
#define TF_O 0
__device__ __forceinline__ int t_off(const Args& p, int t) { return t * 16384 + (((p.s0 ^ (t >> 7) ^ (TF_KQV == 2 ? t : TF_KQV)) & 1) << 13); }
__device__ __forceinline__ int t_off_o(const Args& p, int t) { return t * 16384 + (((p.s0 ^ (t >> 7) ^ (TF_O == 2 ? t : TF_O)) & 1) << 13); }

__device__ __forceinline__ float wave_sum(float v) {
#pragma unroll
  for (int i = 0; i < 4; ++i) {
    const int x = (int)__float_as_uint(v);
    v += __uint_as_float(i == 0 ? __builtin_amdgcn_update_dpp(0, x, 0x128, 0xf, 0xf, false) : i == 1 ? __builtin_amdgcn_update_dpp(0, x, 0x124, 0xf, 0xf, false)
                         : i == 2 ? __builtin_amdgcn_update_dpp(0, x, 0x122, 0xf, 0xf, false) : __builtin_amdgcn_update_dpp(0, x, 0x121, 0xf, 0xf, false));
  }
  auto r = __builtin_amdgcn_permlane16_swap(__float_as_uint(v), __float_as_uint(v), false, false);
  v = __uint_as_float(r[0]) + __uint_as_float(r[1]);
  r = __builtin_amdgcn_permlane32_swap(__float_as_uint(v), __float_as_uint(v), false, false);
  return __uint_as_float(r[0]) + __uint_as_float(r[1]);
}

__shared__ __attribute__((aligned(16))) char g_smem[163840];
__device__ __forceinline__ unsigned lds_off(const void* p) {
  return (unsigned)(uintptr_t)(lds_void_ptr)g_smem + (unsigned)((const char*)p - g_smem);
}
__device__ __forceinline__ __amdgpu_buffer_rsrc_t mkrsrc(const void* p, unsigned bytes) {
  return __builtin_amdgcn_make_buffer_rsrc((void*)p, 0, bytes, 0x00020000);
}
__device__ __forceinline__ u32x4 ld_sc1(__amdgpu_buffer_rsrc_t rs, int off) {
  return __builtin_amdgcn_raw_buffer_load_b128(rs, off, 0, 16);
}
__device__ __forceinline__ void st_sc1(__amdgpu_buffer_rsrc_t rs, int off, u32x4 v) {
  __builtin_amdgcn_raw_buffer_store_b128(v, rs, off, 0, 16);
}
__device__ __forceinline__ void swap16(unsigned& x, unsigned& y) {
  auto r = __builtin_amdgcn_permlane16_swap(x, y, false, false);
  x = r[0]; y = r[1];
}
__device__ __forceinline__ void swap32(unsigned& x, unsigned& y) {
  auto r = __builtin_amdgcn_permlane32_swap(x, y, false, false);
  x = r[0]; y = r[1];
}
__device__ __forceinline__ bool spin_fail(unsigned& spins, gu32* abortw) {
  ++spins;
  if ((spins & 63u) == 0u) {
    const __amdgpu_buffer_rsrc_t ra = mkrsrc((const void*)abortw, 4);
    unsigned z = 0;
    asm volatile("" : "+v"(z));
    if (__builtin_amdgcn_readfirstlane(__builtin_amdgcn_raw_buffer_load_b32(ra, z, 0, 16)) == 1u) return true;
    if (spins > (1u << 19)) {
      __builtin_amdgcn_raw_buffer_store_b32(z + 1u, ra, z, 0, 16);
      return true;
    }
  }
  return false;
}
__device__ __forceinline__ bf16x8 ldsf(const char* p) { return *reinterpret_cast<const bf16x8*>(p); }
#define SCHED() __builtin_amdgcn_sched_barrier(0)
#define LBAR() do { asm volatile("s_waitcnt lgkmcnt(0)" ::: "memory"); __builtin_amdgcn_s_barrier(); asm volatile("" ::: "memory"); } while (0)
#define WAIT_V(n) asm volatile("s_waitcnt vmcnt(%0)" :: "n"(n) : "memory")

#define HOLD_V(c, n) asm volatile("s_cmp_eq_u32 %0, 0\n\ts_cbranch_scc1 .Lh%=\n\ts_waitcnt vmcnt(%1)\n.Lh%=:" :: "s"((int)(c)), "n"(n) : "memory", "scc")
#define TH_Q 2
__device__ __forceinline__ void wait_flags(__amdgpu_buffer_rsrc_t rs, int off, gu32* abortw) {
  for (unsigned spins = 0;;) {
    asm volatile("" ::: "memory");
    const unsigned v = __builtin_amdgcn_raw_buffer_load_b32(rs, off, 0, 16);
    if (__all(v != SENT)) break;
    __builtin_amdgcn_s_sleep(4);
    if (spin_fail(spins, abortw)) break;
  }
  asm volatile("" ::: "memory");
}

__device__ __forceinline__ unsigned offA(unsigned row, unsigned ch) {
  return row * 128u + 16u * (ch ^ (row & 7u));
}
__device__ __forceinline__ unsigned swzB(unsigned row) { return ((row & 3u) << 2) | ((row >> 2) & 3u); }
__device__ __forceinline__ unsigned offB(unsigned row, unsigned ch) {
  return 256u * row + 16u * (ch ^ swzB(row));
}
__device__ __forceinline__ void glds16(const u16* src, char* dst) {
  const unsigned d = __builtin_amdgcn_readfirstlane(lds_off(dst));
  unsigned keep;
  asm volatile("s_mov_b32 %0, m0\n\ts_mov_b32 m0, %2\n\ts_nop 0\n\tglobal_load_lds_dwordx4 %1, off\n\ts_mov_b32 m0, %0"
               : "=&s"(keep) : "v"(src), "s"(d) : "memory");
}
__device__ __forceinline__ bf16x8 ld_bfrag(const char* sb, int ks, int c, int lane) {
  const unsigned g = lane >> 4, qq = (lane & 15) >> 2, pp = lane & 3;
  s16x4 lo = __builtin_amdgcn_ds_read_tr16_b64_v4i16(
      (lds_s16x4_ptr)(sb + offB(32 * ks + 8 * g + qq, 2 * c + (pp >> 1)) + 8 * (pp & 1)));
  s16x4 hi = __builtin_amdgcn_ds_read_tr16_b64_v4i16(
      (lds_s16x4_ptr)(sb + offB(32 * ks + 8 * g + 4 + qq, 2 * c + (pp >> 1)) + 8 * (pp & 1)));
  return (bf16x8){lo[0], lo[1], lo[2], lo[3], hi[0], hi[1], hi[2], hi[3]};
}

__device__ __forceinline__ void blk_dma(const u16* src, size_t ld, char* slot, int lane) {
  const u16* s = src + (size_t)(lane >> 2) * ld + (lane & 3) * 8;
  glds16(s, slot);
  glds16(s + 16 * ld, slot + 1024);
}
template <int NB, typename ISSUE, typename SINK>
__device__ __forceinline__ void load_blocks(char* slice, ISSUE&& issue, SINK&& sink) {
#pragma unroll
  for (int i = 0; i < 8; ++i) issue(i, slice + i * 2048);
#pragma unroll
  for (int g = 0; g < NB / 4; ++g) {
    if (g + 1 < NB / 4) WAIT_V(8); else WAIT_V(0);
#pragma unroll
    for (int j = 0; j < 4; ++j) sink(4 * g + j, slice + ((4 * g + j) & 7) * 2048);
    asm volatile("s_waitcnt lgkmcnt(0)" ::: "memory");
    if (g + 2 < NB / 4) {
#pragma unroll
      for (int j = 0; j < 4; ++j) issue(4 * g + 8 + j, slice + ((4 * g + j) & 7) * 2048);
    }
  }
}

__device__ __forceinline__ u32x4 widen32(unsigned a0, unsigned a1, unsigned b0, unsigned b1) {
  swap32(a0, b0); swap16(a0, b0);
  swap32(a1, b1); swap16(a1, b1);
  return (u32x4){a0, a1, b0, b1};
}

__device__ __forceinline__ void publish_flags(__amdgpu_buffer_rsrc_t rs, int off, int stride, int n) {
  WAIT_V(0);
  LBAR();
  int tid = threadIdx.x;
  asm volatile("" : "+v"(tid));
  if (tid < n) __builtin_amdgcn_raw_buffer_store_b32(1u, rs, off + tid * stride, 0, 16);
}

__device__ __forceinline__ void wait_s(const Args& p, __amdgpu_buffer_rsrc_t rsy, int rblk, int c0, int n, bool need) {
  if (need) {
    if (threadIdx.x < 64) {
      for (unsigned spins = 0;;) {
        asm volatile("" ::: "memory");
        const int f = (int)__builtin_amdgcn_raw_buffer_load_b32(rsy, SY_FS + (2 * c0 + (threadIdx.x & (2 * n - 1))) * 128, 0, 16);
        if (__all(f >= rblk)) break;
        __builtin_amdgcn_s_sleep(32);
        if (spin_fail(spins, (gu32*)(p.ws + WS_SY + SY_ABORT))) break;
      }
    }
    LBAR();
  }
}
__device__ __forceinline__ u32x4 combine(u32x4 sv, u32x4 g, u32x4 x) {
  u32x4 o;
#pragma unroll
  for (int j = 0; j < 4; ++j) {
    const unsigned t = cvtpk(lo_f(sv[j]) * lo_f(g[j]), hi_f(sv[j]) * hi_f(g[j]));
    o[j] = cvtpk(lo_f(x[j]) + lo_f(t), hi_f(x[j]) + hi_f(t));
  }
  return o;
}
#define OUT_AUX 0
__device__ __forceinline__ void st_out(const Args& p, int e, u32x4 v) {
  __builtin_amdgcn_raw_buffer_store_b128(v, mkrsrc(p.out, S_LEN * DM * 2), e * 2, 0, OUT_AUX);
}
struct SX { u32x4 s, x; };
__device__ __forceinline__ SX get_sx(const Args& p, int rblk, int e) {
  SX r; r.s = (u32x4){0u, 0u, 0u, 0u}; r.x = r.s;
  if (rblk < NFUSE) {
    r.s = ld_sc1(mkrsrc(p.ws + WS_S, S_LEN * DM * 2), (((e & 1023) >> 5) * S_LEN + (e >> 10)) * 64 + (e & 31) * 2);
    r.x = *reinterpret_cast<const u32x4*>(p.x + e);
  }
  return r;
}
__device__ __forceinline__ void put_g(const Args& p, __amdgpu_buffer_rsrc_t rg, int rblk, int e, u32x4 g, const SX& v) {
  if (rblk < NFUSE) {
    st_out(p, e, combine(v.s, g, v.x));
  } else st_sc1(rg, e * 2, g);
}

__device__ __forceinline__ void job_comb(const Args& p, int rblk, int ct, __amdgpu_buffer_rsrc_t rsy) {
  int tid = threadIdx.x;
  asm volatile("" : "+v"(tid));
  u32x4 xv[8];
#pragma unroll
  for (int i = 0; i < 8; ++i) xv[i] = *reinterpret_cast<const u32x4*>(p.x + (rblk * 128 + i * 16 + (tid >> 5)) * DM + ct * 256 + (tid & 31) * 8);
  if (tid < 64) wait_flags(rsy, SY_FG + (rblk * 8 + 2 * ct + (tid & 1)) * 128, (gu32*)(p.ws + WS_SY + SY_ABORT));
  wait_s(p, rsy, rblk, ct * 8, 8, true);
  u32x4 sv[8], gv[8];
#pragma unroll
  for (int i = 0; i < 8; ++i) {
    const int e = (rblk * 128 + i * 16 + (tid >> 5)) * DM + ct * 256 + (tid & 31) * 8;
    sv[i] = ld_sc1(mkrsrc(p.ws + WS_S, S_LEN * DM * 2), (((e & 1023) >> 5) * S_LEN + (e >> 10)) * 64 + (e & 31) * 2);
    gv[i] = ld_sc1(mkrsrc(p.ws + WS_G, S_LEN * DM * 2), e * 2);
  }
#pragma unroll
  for (int i = 0; i < 8; ++i)
    st_out(p, (rblk * 128 + i * 16 + (tid >> 5)) * DM + ct * 256 + (tid & 31) * 8, combine(sv[i], gv[i], xv[i]));
}

__device__ __forceinline__ u32x4 raw_rsrc(const void* p, unsigned bytes) {
  const unsigned long long a = (unsigned long long)p;
  return (u32x4){(unsigned)a, (unsigned)(a >> 32) & 0xffffu, bytes, 0x00020000u};
}
__device__ __forceinline__ u32x2 next_tag(u32x2 e) { return (u32x2){e[0] + 1u, e[1] + (e[0] == 0xFFFFFFFFu)}; }
struct Boot { u32x2 e; u32x4 c; };
__device__ __forceinline__ void boot_ask(const char* ws, Boot& r) {
  const unsigned o = (threadIdx.x & 63) * 128;
  asm volatile("buffer_load_dwordx2 %0, off, %3, %4 sc1\n\tbuffer_load_dwordx4 %1, %2, %3, %5 offen sc1"
               : "=&v"(r.e), "=&v"(r.c) : "v"(o), "s"(raw_rsrc(ws, SY_TOTAL)), "s"(SY_EP), "s"(SY_BT) : "memory");
}
__device__ __forceinline__ void wait_rows(const char* ws, u32x2 tag) {
  const __amdgpu_buffer_rsrc_t rs = mkrsrc(ws, SY_TOTAL);
  int o = (threadIdx.x & 63) * 128;
  asm volatile("" : "+v"(o));
#pragma unroll 1
  for (int i = 0; i < (1 << 17); ++i) {
    asm volatile("" ::: "memory");
    const u32x2 a = __builtin_amdgcn_raw_buffer_load_b64(rs, o, SY_BT + 32 * 128, 16), b = __builtin_amdgcn_raw_buffer_load_b64(rs, o, SY_BT + 96 * 128, 16);
    if (__all(a[0] == tag[0] && a[1] == tag[1] && b[0] == tag[0] && b[1] == tag[1])) break;
    __builtin_amdgcn_s_sleep(8);
  }
  asm volatile("" ::: "memory");
}
__device__ __forceinline__ unsigned boot_done(const char* ws, Boot& r, bool own = false, u32x2 tag = (u32x2){0u, 0u}) {
#pragma unroll 1
  for (int i = 0; i < (1 << 17); ++i) {
    asm volatile("" : "+v"(r.e), "+v"(r.c));
    if (!own) tag = next_tag(r.e);
    if (__all(r.c[0] == tag[0] && r.c[1] == tag[1] && (r.c[2] ^ r.c[3]) == tag[0])) break;
    __builtin_amdgcn_s_sleep(8);
    boot_ask(ws, r);
    WAIT_V(0);
  }
  unsigned q0[NPAGE], q1[NPAGE], ga = 0xFFFFu, gb = 0xFFFFu;
#pragma unroll
  for (int i = 0; i < NPAGE; ++i) {
    const unsigned x = __builtin_amdgcn_readlane(r.c[2], i), y = __builtin_amdgcn_readlane(r.c[2], i + NPAGE);
    q0[i] = min(x >> 16, y >> 16); q1[i] = min(x & 0xFFFFu, y & 0xFFFFu);
    ga = min(ga, (i & 1) ? q1[i] : q0[i]); gb = min(gb, (i & 1) ? q0[i] : q1[i]);
  }
  unsigned m = gb < ga ? 0x80001555u : 0x2AAAu;
  const unsigned thr = min(ga, gb) + 110;
#pragma unroll
  for (int i = 0; i < NPAGE; ++i) {
    const unsigned lo = min(q0[i], q1[i]), hi = max(q0[i], q1[i]);
    const unsigned keep = (lo < thr ? hi : 0u) > lo + 100 ? ~(1u << i) : ~0u;
    m = (m & keep) | ((q1[i] < q0[i] ? ~0u : 0u) & ~keep);
  }
  return m;
}
typedef __attribute__((address_space(3))) volatile unsigned* lds_vu_ptr;
__device__ __forceinline__ lds_vu_ptr lds_at(const void* p) {
  unsigned a = lds_off(p);
  asm volatile("" : "+v"(a));
  return (lds_vu_ptr)(uintptr_t)a;
}
__device__ __forceinline__ unsigned uni(unsigned v) {
  unsigned r;
  asm volatile("s_nop 1\n\tv_readfirstlane_b32 %0, %1\n\ts_nop 3" : "=s"(r) : "v"(v));
  return r;
}
__device__ __forceinline__ void start_sy(char* ws, const u16* mem0, int i, char* smem) {
  const int tid = threadIdx.x, lane = tid & 63, w = __builtin_amdgcn_readfirstlane(tid >> 6);
  const __amdgpu_buffer_rsrc_t rsy = mkrsrc(ws, SY_TOTAL);
  const __amdgpu_buffer_rsrc_t rs = mkrsrc(ws + (size_t)(i % NPAGE) * PAGE + PROBE - (i / NPAGE) * 2048, 16384);
  const u32x2 e = __builtin_amdgcn_raw_buffer_load_b64(rsy, 0, SY_EP, 16);
  const u32x4 ds = {DSENT, DSENT, DSENT, DSENT};
  if (lane < 32) st_sc1(rs, (lane >> 4) * 8192 + w * 256 + (lane & 15) * 16, ds);
  static_assert(NPRE == 4 && SY_FEND - SY_BR <= 32 * 6144, "");
#pragma unroll
  for (int u = 0; u < 6; ++u)
    st_sc1(rsy, (u < 4 ? SY_V + (i * 4 + u) * 8192 : SY_O + (i * 2 + u - 4) * 8192) + tid * 16, ds);
  if (w == 7) {
    const int n = i * 64 + lane, j = (n >> 10) * 512 + (n & 511);
    const unsigned* m0 = reinterpret_cast<const unsigned*>(mem0 + (2 * ((j >> 2) & 3) + (j & 1)) * DM + (j >> 4) * 16) + ((j >> 1) & 1);
    u32x4 m;
#pragma unroll
    for (int k = 0; k < 4; ++k) { const unsigned t = m0[2 * k]; m[k] = t == DSENT ? 0xFFFFFFFFu : t; }
    st_sc1(rsy, n * 16, m);
  } else if (w == 6) st_sc1(rsy, 32768 + i * 1024 + lane * 16, ds);
  else {
    const int o = SY_BR + i * 6144 + tid * 16;
    st_sc1(rsy, o < SY_FEND ? o : 0x7F000000, (u32x4){SENT, SENT, SENT, SENT});
  }
  unsigned t[4];
#pragma unroll
  for (int k = 0; k < 4; ++k) {
    unsigned long long c0, c1; unsigned v, off = ((k ^ (k >> 1)) & 1) * 8192 + w * 256 + (k >> 1) * 128;
    asm volatile("s_waitcnt vmcnt(0)\n\ts_barrier\n\ts_memtime %0\n\ts_waitcnt lgkmcnt(0)\n\tbuffer_load_dword %2, %3, %4, 0 offen sc1\n\ts_waitcnt vmcnt(0)\n\t"
                 "s_memtime %1\n\ts_waitcnt lgkmcnt(0)" : "=&s"(c0), "=&s"(c1), "=&v"(v) : "v"(off), "s"(rs) : "memory");
    t[k] = (unsigned)(c1 - c0);
  }
  lds_vu_ptr tm = lds_at(smem);
  if (lane == 0) { tm[w] = min(t[0], t[3]); tm[8 + w] = min(t[1], t[2]); }
  int ro = SY_BT + i * 128 + lane * 16;
  asm volatile("" : "+v"(ro));
  LBAR();
  if (threadIdx.x < 8) {
    unsigned m0 = tm[0], m1 = tm[8];
#pragma unroll
    for (int k = 1; k < 8; ++k) { m0 = min(m0, tm[k]); m1 = min(m1, tm[8 + k]); }
    const u32x2 tag = next_tag(e);
    const unsigned v = min(m0, 30000u) << 16 | min(m1, 30000u);
    st_sc1(rsy, ro, (u32x4){tag[0], tag[1], v, v ^ tag[0]});
  }
}
template <int NB, typename F>
__device__ __forceinline__ u32x2 start_rows(const Args& p, int g, unsigned on, F&& between) {
  const int tid = threadIdx.x, lane = tid & 63, row0 = g * 16 + (tid >> 6) * 2;
  u32x4 a[4];
  u32x2 e;
  asm volatile("buffer_load_dwordx4 %0, %5, %8, 0 offen\n\tbuffer_load_dwordx4 %1, %5, %8, 0 offen offset:1024\n\t"
               "buffer_load_dwordx4 %2, %5, %8, 0 offen offset:2048\n\tbuffer_load_dwordx4 %3, %5, %8, 0 offen offset:3072\n\t"
               "buffer_load_dwordx2 %4, off, %6, %7 sc1"
               : "=&v"(a[0]), "=&v"(a[1]), "=&v"(a[2]), "=&v"(a[3]), "=&v"(e)
               : "v"(row0 * (DM * 2) + lane * 16), "s"(raw_rsrc(p.ws, SY_TOTAL)), "s"(SY_EP), "s"(raw_rsrc(p.x, on * (S_LEN * DM * 2))) : "memory");
  between();
  asm volatile("s_waitcnt vmcnt(%5)" : "+v"(a[0]), "+v"(a[1]), "+v"(a[2]), "+v"(a[3]), "+v"(e) : "n"(NB) : "memory");
  const __amdgpu_buffer_rsrc_t ro = mkrsrc(p.ws + WS_XN, on * (S_LEN * XLD * 2));
#pragma unroll
  for (int u = 0; u < 2; ++u) {
    float v[16];
#pragma unroll
    for (int i = 0; i < 8; ++i) { v[2 * i] = lo_f(a[2 * u + (i >> 2)][i & 3]); v[2 * i + 1] = hi_f(a[2 * u + (i >> 2)][i & 3]); }
    float s = 0.f;
#pragma unroll
    for (int i = 0; i < 16; ++i) s += v[i];
    const float mean = wave_sum(s) * (1.0f / DM);
    float q = 0.f;
#pragma unroll
    for (int i = 0; i < 16; ++i) { float d = v[i] - mean; q += d * d; }
    const float var = rbf(wave_sum(q) * (1.0f / DM));
    const float mb = rbf(mean);
    const float inv = 1.0f / rbf(sqrtf(rbf(var + 1e-5f)));
#pragma unroll
    for (int k = 0; k < 2; ++k) {
      u32x4 o;
#pragma unroll
      for (int i = 0; i < 4; ++i) {
        const unsigned t = cvtpk(v[8 * k + 2 * i] - mb, v[8 * k + 2 * i + 1] - mb);
        o[i] = cvtpk(lo_f(t) * inv, hi_f(t) * inv);
      }
      st_sc1(ro, (row0 + u) * (XLD * 2) + k * 1024 + lane * 16, o);
    }
  }
  return next_tag(e);
}
__device__ __forceinline__ void report_rows(const Args& p, int g, u32x2 tag) {
  if (threadIdx.x < 8) st_sc1(mkrsrc(p.ws + WS_SY, SY_TOTAL), SY_BT + (32 + g) * 128 + threadIdx.x * 16, (u32x4){tag[0], tag[1], 0u, tag[0]});
}

typedef __attribute__((ext_vector_type(32))) unsigned u32x32;
__device__ __forceinline__ void put4(u32x32& W, int i, bf16x8 f) {
  const u32x4 v = __builtin_bit_cast(u32x4, f);
#pragma unroll
  for (int j = 0; j < 4; ++j) W[4 * i + j] = v[j];
}
#define CLB8(a, b, c, d, e, f, g, h) "v" #a, "v" #b, "v" #c, "v" #d, "v" #e, "v" #f, "v" #g, "v" #h
#define CLB32(a) CLB32_##a
#define CLB32_128 CLB8(128, 129, 130, 131, 132, 133, 134, 135), CLB8(136, 137, 138, 139, 140, 141, 142, 143), CLB8(144, 145, 146, 147, 148, 149, 150, 151), CLB8(152, 153, 154, 155, 156, 157, 158, 159)
#define CLB32_160 CLB8(160, 161, 162, 163, 164, 165, 166, 167), CLB8(168, 169, 170, 171, 172, 173, 174, 175), CLB8(176, 177, 178, 179, 180, 181, 182, 183), CLB8(184, 185, 186, 187, 188, 189, 190, 191)
#define W4_IN "{v[0:31]}"(W[0]), "{v[32:63]}"(W[1]), "{v[64:95]}"(W[2]), "{v[96:127]}"(W[3])
#define CLB32_192 CLB8(192, 193, 194, 195, 196, 197, 198, 199), CLB8(200, 201, 202, 203, 204, 205, 206, 207), CLB8(208, 209, 210, 211, 212, 213, 214, 215), CLB8(216, 217, 218, 219, 220, 221, 222, 223)
__device__ __forceinline__ unsigned ld_flag0(__amdgpu_buffer_rsrc_t rs, int off) {
  return __builtin_amdgcn_raw_buffer_load_b32(rs, off, 0, 16);
}
__device__ __forceinline__ void glds16_sc1(const char* src, char* dst) {
  const unsigned d = __builtin_amdgcn_readfirstlane(lds_off(dst));
  unsigned keep;
  asm volatile("s_mov_b32 %0, m0\n\ts_mov_b32 m0, %2\n\ts_nop 0\n\tglobal_load_lds_dwordx4 %1, off sc1\n\ts_mov_b32 m0, %0"
               : "=&s"(keep) : "v"(src), "s"(d) : "memory");
}

__device__ __forceinline__ void touch_line(const char* src, char* dst) {
  const unsigned d = __builtin_amdgcn_readfirstlane(lds_off(dst));
  unsigned keep;
  asm volatile("s_mov_b32 %0, m0\n\ts_mov_b32 m0, %2\n\ts_nop 0\n\tglobal_load_lds_dword %1, off sc1\n\ts_mov_b32 m0, %0"
               : "=&s"(keep) : "v"(src), "s"(d) : "memory");
}

#define PF_N 12
#define LN_M 3
#define LN_X 1
#define PR_G1 8
#define WREV 1
template <bool DEV = false>
__device__ __forceinline__ void glds2(const char* base, int step, unsigned voff, unsigned dst) {
  unsigned keep;
  const char* base2 = base + step;
  if (DEV)
    asm volatile("s_mov_b32 %0, m0\n\ts_mov_b32 m0, %4\n\ts_nop 0\n\tglobal_load_lds_dwordx4 %1, %2 sc1\n\t"
                 "s_add_u32 m0, m0, 0x2000\n\ts_nop 0\n\tglobal_load_lds_dwordx4 %1, %3 sc1\n\ts_mov_b32 m0, %0"
                 : "=&s"(keep) : "v"(voff), "s"(base), "s"(base2), "s"(dst) : "memory", "scc");
  else
  asm volatile("s_mov_b32 %0, m0\n\ts_mov_b32 m0, %4\n\ts_nop 0\n\tglobal_load_lds_dwordx4 %1, %2\n\t"
               "s_add_u32 m0, m0, 0x2000\n\ts_nop 0\n\tglobal_load_lds_dwordx4 %1, %3\n\ts_mov_b32 m0, %0"
               : "=&s"(keep) : "v"(voff), "s"(base), "s"(base2), "s"(dst) : "memory", "scc");
}

#define DN_ASM \
  "s_mov_b32 s94, m0\n\t" \
  "s_mov_b64 s[84:85], %[pa]\n\t" \
  "s_mov_b64 s[86:87], %[pb]\n\t" \
  "s_movk_i32 s88, 0x4000\n\t" \
  "s_mov_b32 s89, 0x10000\n\t" \
  "s_movk_i32 s90, 13\n\t" \
  "v_mov_b64 v[0:1], 0\n\t" \
  "v_mov_b64 v[2:3], 0\n\t" \
  "v_mov_b64 v[4:5], 0\n\t" \
  "v_mov_b64 v[6:7], 0\n\t" \
  "v_mov_b64 v[8:9], 0\n\t" \
  "v_mov_b64 v[10:11], 0\n\t" \
  "v_mov_b64 v[12:13], 0\n\t" \
  "v_mov_b64 v[14:15], 0\n\t" \
  "v_mov_b64 v[16:17], 0\n\t" \
  "v_mov_b64 v[18:19], 0\n\t" \
  "v_mov_b64 v[20:21], 0\n\t" \
  "v_mov_b64 v[22:23], 0\n\t" \
  "v_mov_b64 v[24:25], 0\n\t" \
  "v_mov_b64 v[26:27], 0\n\t" \
  "v_mov_b64 v[28:29], 0\n\t" \
  "v_mov_b64 v[30:31], 0\n\t" \
  "s_cmp_lg_u32 %[hf], 0\n\t" \
  "s_cbranch_scc1 .Lk%=\n\t" \
  "ds_read_b128 v[32:35], v80 offset:0\n\t" \
  "ds_read_b128 v[36:39], v80 offset:1024\n\t" \
  "ds_read_b128 v[40:43], v80 offset:2048\n\t" \
  "ds_read_b128 v[44:47], v80 offset:3072\n\t" \
  "ds_read_b64_tr_b16 v[64:65], v82 offset:0\n\t" \
  "ds_read_b64_tr_b16 v[66:67], v83 offset:0\n\t" \
  "ds_read_b128 v[48:51], v80 offset:8192\n\t" \
  "ds_read_b64_tr_b16 v[68:69], v84 offset:0\n\t" \
  "ds_read_b64_tr_b16 v[70:71], v85 offset:0\n\t" \
  "ds_read_b128 v[52:55], v80 offset:9216\n\t" \
  "ds_read_b128 v[56:59], v80 offset:10240\n\t" \
  "ds_read_b128 v[60:63], v80 offset:11264\n\t" \
  ".Lp0%=:\n\t" \
  "s_waitcnt vmcnt(4)\n\t" \
  "s_barrier\n\t" \
  "s_waitcnt lgkmcnt(3)\n\t" \
  "v_mfma_f32_16x16x32_bf16 v[0:3], v[64:67], v[32:35], v[0:3]\n\t" \
  "ds_read_b64_tr_b16 v[72:73], v82 offset:8192\n\t" \
  "v_mfma_f32_16x16x32_bf16 v[4:7], v[68:71], v[32:35], v[4:7]\n\t" \
  "ds_read_b64_tr_b16 v[74:75], v83 offset:8192\n\t" \
  "ds_read_b128 v[32:35], v80 offset:32768\n\t" \
  "s_add_u32 m0, %[lw], 98304\n\t" \
  "v_mfma_f32_16x16x32_bf16 v[8:11], v[64:67], v[36:39], v[8:11]\n\t" \
  "ds_read_b64_tr_b16 v[76:77], v84 offset:8192\n\t" \
  "global_load_lds_dwordx4 v90, s[84:85] sc1\n\t" \
  "s_add_u32 s84, s84, s88\n\t" \
  "s_addc_u32 s85, s85, 0\n\t" \
  "v_mfma_f32_16x16x32_bf16 v[12:15], v[68:71], v[36:39], v[12:15]\n\t" \
  "ds_read_b64_tr_b16 v[78:79], v85 offset:8192\n\t" \
  "ds_read_b128 v[36:39], v80 offset:33792\n\t" \
  "v_mfma_f32_16x16x32_bf16 v[16:19], v[64:67], v[40:43], v[16:19]\n\t" \
  "v_mfma_f32_16x16x32_bf16 v[20:23], v[68:71], v[40:43], v[20:23]\n\t" \
  "ds_read_b128 v[40:43], v80 offset:34816\n\t" \
  "s_add_u32 m0, %[lw], 114688\n\t" \
  "v_mfma_f32_16x16x32_bf16 v[24:27], v[64:67], v[44:47], v[24:27]\n\t" \
  "global_load_lds_dwordx4 v91, s[86:87]\n\t" \
  "s_add_u32 s86, s86, s89\n\t" \
  "s_addc_u32 s87, s87, 0\n\t" \
  "v_mfma_f32_16x16x32_bf16 v[28:31], v[68:71], v[44:47], v[28:31]\n\t" \
  "ds_read_b128 v[44:47], v80 offset:35840\n\t" \
  "s_waitcnt lgkmcnt(3)\n\t" \
  "v_mfma_f32_16x16x32_bf16 v[0:3], v[72:75], v[48:51], v[0:3]\n\t" \
  "ds_read_b64_tr_b16 v[64:65], v82 offset:32768\n\t" \
  "v_mfma_f32_16x16x32_bf16 v[4:7], v[76:79], v[48:51], v[4:7]\n\t" \
  "ds_read_b64_tr_b16 v[66:67], v83 offset:32768\n\t" \
  "ds_read_b128 v[48:51], v80 offset:40960\n\t" \
  "s_add_u32 m0, %[lw], 106496\n\t" \
  "v_mfma_f32_16x16x32_bf16 v[8:11], v[72:75], v[52:55], v[8:11]\n\t" \
  "ds_read_b64_tr_b16 v[68:69], v84 offset:32768\n\t" \
  "global_load_lds_dwordx4 v90, s[84:85] sc1\n\t" \
  "s_add_u32 s84, s84, s88\n\t" \
  "s_addc_u32 s85, s85, 0\n\t" \
  "v_mfma_f32_16x16x32_bf16 v[12:15], v[76:79], v[52:55], v[12:15]\n\t" \
  "ds_read_b64_tr_b16 v[70:71], v85 offset:32768\n\t" \
  "ds_read_b128 v[52:55], v80 offset:41984\n\t" \
  "v_mfma_f32_16x16x32_bf16 v[16:19], v[72:75], v[56:59], v[16:19]\n\t" \
  "v_mfma_f32_16x16x32_bf16 v[20:23], v[76:79], v[56:59], v[20:23]\n\t" \
  "ds_read_b128 v[56:59], v80 offset:43008\n\t" \
  "s_add_u32 m0, %[lw], 122880\n\t" \
  "v_mfma_f32_16x16x32_bf16 v[24:27], v[72:75], v[60:63], v[24:27]\n\t" \
  "global_load_lds_dwordx4 v91, s[86:87]\n\t" \
  "s_add_u32 s86, s86, s89\n\t" \
  "s_addc_u32 s87, s87, 0\n\t" \
  "v_mfma_f32_16x16x32_bf16 v[28:31], v[76:79], v[60:63], v[28:31]\n\t" \
  "ds_read_b128 v[60:63], v80 offset:44032\n\t" \
  "s_sub_u32 s90, s90, 1\n\t" \
  "s_cselect_b32 s88, 0, s88\n\t" \
  "s_cselect_b32 s89, 0, s89\n\t" \
  "s_cselect_b32 s91, 16384, 0\n\t" \
  "s_sub_u32 s84, s84, s91\n\t" \
  "s_subb_u32 s85, s85, 0\n\t" \
  "s_lshl_b32 s91, s91, 2\n\t" \
  "s_sub_u32 s86, s86, s91\n\t" \
  "s_subb_u32 s87, s87, 0\n\t" \
  "s_waitcnt vmcnt(4)\n\t" \
  "s_barrier\n\t" \
  "s_waitcnt lgkmcnt(3)\n\t" \
  "v_mfma_f32_16x16x32_bf16 v[0:3], v[64:67], v[32:35], v[0:3]\n\t" \
  "ds_read_b64_tr_b16 v[72:73], v82 offset:40960\n\t" \
  "v_mfma_f32_16x16x32_bf16 v[4:7], v[68:71], v[32:35], v[4:7]\n\t" \
  "ds_read_b64_tr_b16 v[74:75], v83 offset:40960\n\t" \
  "ds_read_b128 v[32:35], v81 offset:0\n\t" \
  "s_add_u32 m0, %[lw], 0\n\t" \
  "v_mfma_f32_16x16x32_bf16 v[8:11], v[64:67], v[36:39], v[8:11]\n\t" \
  "ds_read_b64_tr_b16 v[76:77], v84 offset:40960\n\t" \
  "global_load_lds_dwordx4 v90, s[84:85] sc1\n\t" \
  "s_add_u32 s84, s84, s88\n\t" \
  "s_addc_u32 s85, s85, 0\n\t" \
  "v_mfma_f32_16x16x32_bf16 v[12:15], v[68:71], v[36:39], v[12:15]\n\t" \
  "ds_read_b64_tr_b16 v[78:79], v85 offset:40960\n\t" \
  "ds_read_b128 v[36:39], v81 offset:1024\n\t" \
  "v_mfma_f32_16x16x32_bf16 v[16:19], v[64:67], v[40:43], v[16:19]\n\t" \
  "v_mfma_f32_16x16x32_bf16 v[20:23], v[68:71], v[40:43], v[20:23]\n\t" \
  "ds_read_b128 v[40:43], v81 offset:2048\n\t" \
  "s_add_u32 m0, %[lw], 16384\n\t" \
  "v_mfma_f32_16x16x32_bf16 v[24:27], v[64:67], v[44:47], v[24:27]\n\t" \
  "global_load_lds_dwordx4 v91, s[86:87]\n\t" \
  "s_add_u32 s86, s86, s89\n\t" \
  "s_addc_u32 s87, s87, 0\n\t" \
  "v_mfma_f32_16x16x32_bf16 v[28:31], v[68:71], v[44:47], v[28:31]\n\t" \
  "ds_read_b128 v[44:47], v81 offset:3072\n\t" \
  "s_waitcnt lgkmcnt(3)\n\t" \
  "v_mfma_f32_16x16x32_bf16 v[0:3], v[72:75], v[48:51], v[0:3]\n\t" \
  "ds_read_b64_tr_b16 v[64:65], v86 offset:0\n\t" \
  "v_mfma_f32_16x16x32_bf16 v[4:7], v[76:79], v[48:51], v[4:7]\n\t" \
  "ds_read_b64_tr_b16 v[66:67], v87 offset:0\n\t" \
  "ds_read_b128 v[48:51], v81 offset:8192\n\t" \
  "s_add_u32 m0, %[lw], 8192\n\t" \
  "v_mfma_f32_16x16x32_bf16 v[8:11], v[72:75], v[52:55], v[8:11]\n\t" \
  "ds_read_b64_tr_b16 v[68:69], v88 offset:0\n\t" \
  "global_load_lds_dwordx4 v90, s[84:85] sc1\n\t" \
  "s_add_u32 s84, s84, s88\n\t" \
  "s_addc_u32 s85, s85, 0\n\t" \
  "v_mfma_f32_16x16x32_bf16 v[12:15], v[76:79], v[52:55], v[12:15]\n\t" \
  "ds_read_b64_tr_b16 v[70:71], v89 offset:0\n\t" \
  "ds_read_b128 v[52:55], v81 offset:9216\n\t" \
  "v_mfma_f32_16x16x32_bf16 v[16:19], v[72:75], v[56:59], v[16:19]\n\t" \
  "v_mfma_f32_16x16x32_bf16 v[20:23], v[76:79], v[56:59], v[20:23]\n\t" \
  "ds_read_b128 v[56:59], v81 offset:10240\n\t" \
  "s_add_u32 m0, %[lw], 24576\n\t" \
  "v_mfma_f32_16x16x32_bf16 v[24:27], v[72:75], v[60:63], v[24:27]\n\t" \
  "global_load_lds_dwordx4 v91, s[86:87]\n\t" \
  "s_add_u32 s86, s86, s89\n\t" \
  "s_addc_u32 s87, s87, 0\n\t" \
  "v_mfma_f32_16x16x32_bf16 v[28:31], v[76:79], v[60:63], v[28:31]\n\t" \
  "ds_read_b128 v[60:63], v81 offset:11264\n\t" \
  "s_waitcnt vmcnt(4)\n\t" \
  "s_barrier\n\t" \
  "s_waitcnt lgkmcnt(3)\n\t" \
  "v_mfma_f32_16x16x32_bf16 v[0:3], v[64:67], v[32:35], v[0:3]\n\t" \
  "ds_read_b64_tr_b16 v[72:73], v86 offset:8192\n\t" \
  "v_mfma_f32_16x16x32_bf16 v[4:7], v[68:71], v[32:35], v[4:7]\n\t" \
  "ds_read_b64_tr_b16 v[74:75], v87 offset:8192\n\t" \
  "ds_read_b128 v[32:35], v81 offset:32768\n\t" \
  "s_add_u32 m0, %[lw], 32768\n\t" \
  "v_mfma_f32_16x16x32_bf16 v[8:11], v[64:67], v[36:39], v[8:11]\n\t" \
  "ds_read_b64_tr_b16 v[76:77], v88 offset:8192\n\t" \
  "global_load_lds_dwordx4 v90, s[84:85] sc1\n\t" \
  "s_add_u32 s84, s84, s88\n\t" \
  "s_addc_u32 s85, s85, 0\n\t" \
  "v_mfma_f32_16x16x32_bf16 v[12:15], v[68:71], v[36:39], v[12:15]\n\t" \
  "ds_read_b64_tr_b16 v[78:79], v89 offset:8192\n\t" \
  "ds_read_b128 v[36:39], v81 offset:33792\n\t" \
  "v_mfma_f32_16x16x32_bf16 v[16:19], v[64:67], v[40:43], v[16:19]\n\t" \
  "v_mfma_f32_16x16x32_bf16 v[20:23], v[68:71], v[40:43], v[20:23]\n\t" \
  "ds_read_b128 v[40:43], v81 offset:34816\n\t" \
  "s_add_u32 m0, %[lw], 49152\n\t" \
  "v_mfma_f32_16x16x32_bf16 v[24:27], v[64:67], v[44:47], v[24:27]\n\t" \
  "global_load_lds_dwordx4 v91, s[86:87]\n\t" \
  "s_add_u32 s86, s86, s89\n\t" \
  "s_addc_u32 s87, s87, 0\n\t" \
  "v_mfma_f32_16x16x32_bf16 v[28:31], v[68:71], v[44:47], v[28:31]\n\t" \
  "ds_read_b128 v[44:47], v81 offset:35840\n\t" \
  "s_waitcnt lgkmcnt(3)\n\t" \
  "v_mfma_f32_16x16x32_bf16 v[0:3], v[72:75], v[48:51], v[0:3]\n\t" \
  "ds_read_b64_tr_b16 v[64:65], v86 offset:32768\n\t" \
  "v_mfma_f32_16x16x32_bf16 v[4:7], v[76:79], v[48:51], v[4:7]\n\t" \
  "ds_read_b64_tr_b16 v[66:67], v87 offset:32768\n\t" \
  "ds_read_b128 v[48:51], v81 offset:40960\n\t" \
  "s_add_u32 m0, %[lw], 40960\n\t" \
  "v_mfma_f32_16x16x32_bf16 v[8:11], v[72:75], v[52:55], v[8:11]\n\t" \
  "ds_read_b64_tr_b16 v[68:69], v88 offset:32768\n\t" \
  "global_load_lds_dwordx4 v90, s[84:85] sc1\n\t" \
  "s_add_u32 s84, s84, s88\n\t" \
  "s_addc_u32 s85, s85, 0\n\t" \
  "v_mfma_f32_16x16x32_bf16 v[12:15], v[76:79], v[52:55], v[12:15]\n\t" \
  "ds_read_b64_tr_b16 v[70:71], v89 offset:32768\n\t" \
  "ds_read_b128 v[52:55], v81 offset:41984\n\t" \
  "v_mfma_f32_16x16x32_bf16 v[16:19], v[72:75], v[56:59], v[16:19]\n\t" \
  "v_mfma_f32_16x16x32_bf16 v[20:23], v[76:79], v[56:59], v[20:23]\n\t" \
  "ds_read_b128 v[56:59], v81 offset:43008\n\t" \
  "s_add_u32 m0, %[lw], 57344\n\t" \
  "v_mfma_f32_16x16x32_bf16 v[24:27], v[72:75], v[60:63], v[24:27]\n\t" \
  "global_load_lds_dwordx4 v91, s[86:87]\n\t" \
  "s_add_u32 s86, s86, s89\n\t" \
  "s_addc_u32 s87, s87, 0\n\t" \
  "v_mfma_f32_16x16x32_bf16 v[28:31], v[76:79], v[60:63], v[28:31]\n\t" \
  "ds_read_b128 v[60:63], v81 offset:44032\n\t" \
  "s_waitcnt vmcnt(4)\n\t" \
  "s_barrier\n\t" \
  "s_waitcnt lgkmcnt(3)\n\t" \
  "v_mfma_f32_16x16x32_bf16 v[0:3], v[64:67], v[32:35], v[0:3]\n\t" \
  "ds_read_b64_tr_b16 v[72:73], v86 offset:40960\n\t" \
  "v_mfma_f32_16x16x32_bf16 v[4:7], v[68:71], v[32:35], v[4:7]\n\t" \
  "ds_read_b64_tr_b16 v[74:75], v87 offset:40960\n\t" \
  "ds_read_b128 v[32:35], v80 offset:0\n\t" \
  "s_add_u32 m0, %[lw], 65536\n\t" \
  "v_mfma_f32_16x16x32_bf16 v[8:11], v[64:67], v[36:39], v[8:11]\n\t" \
  "ds_read_b64_tr_b16 v[76:77], v88 offset:40960\n\t" \
  "global_load_lds_dwordx4 v90, s[84:85] sc1\n\t" \
  "s_add_u32 s84, s84, s88\n\t" \
  "s_addc_u32 s85, s85, 0\n\t" \
  "v_mfma_f32_16x16x32_bf16 v[12:15], v[68:71], v[36:39], v[12:15]\n\t" \
  "ds_read_b64_tr_b16 v[78:79], v89 offset:40960\n\t" \
  "ds_read_b128 v[36:39], v80 offset:1024\n\t" \
  "v_mfma_f32_16x16x32_bf16 v[16:19], v[64:67], v[40:43], v[16:19]\n\t" \
  "v_mfma_f32_16x16x32_bf16 v[20:23], v[68:71], v[40:43], v[20:23]\n\t" \
  "ds_read_b128 v[40:43], v80 offset:2048\n\t" \
  "s_add_u32 m0, %[lw], 81920\n\t" \
  "v_mfma_f32_16x16x32_bf16 v[24:27], v[64:67], v[44:47], v[24:27]\n\t" \
  "global_load_lds_dwordx4 v91, s[86:87]\n\t" \
  "s_add_u32 s86, s86, s89\n\t" \
  "s_addc_u32 s87, s87, 0\n\t" \
  "v_mfma_f32_16x16x32_bf16 v[28:31], v[68:71], v[44:47], v[28:31]\n\t" \
  "ds_read_b128 v[44:47], v80 offset:3072\n\t" \
  "s_waitcnt lgkmcnt(3)\n\t" \
  "v_mfma_f32_16x16x32_bf16 v[0:3], v[72:75], v[48:51], v[0:3]\n\t" \
  "ds_read_b64_tr_b16 v[64:65], v82 offset:0\n\t" \
  "v_mfma_f32_16x16x32_bf16 v[4:7], v[76:79], v[48:51], v[4:7]\n\t" \
  "ds_read_b64_tr_b16 v[66:67], v83 offset:0\n\t" \
  "ds_read_b128 v[48:51], v80 offset:8192\n\t" \
  "s_add_u32 m0, %[lw], 73728\n\t" \
  "v_mfma_f32_16x16x32_bf16 v[8:11], v[72:75], v[52:55], v[8:11]\n\t" \
  "ds_read_b64_tr_b16 v[68:69], v84 offset:0\n\t" \
  "global_load_lds_dwordx4 v90, s[84:85] sc1\n\t" \
  "s_add_u32 s84, s84, s88\n\t" \
  "s_addc_u32 s85, s85, 0\n\t" \
  "v_mfma_f32_16x16x32_bf16 v[12:15], v[76:79], v[52:55], v[12:15]\n\t" \
  "ds_read_b64_tr_b16 v[70:71], v85 offset:0\n\t" \
  "ds_read_b128 v[52:55], v80 offset:9216\n\t" \
  "v_mfma_f32_16x16x32_bf16 v[16:19], v[72:75], v[56:59], v[16:19]\n\t" \
  "v_mfma_f32_16x16x32_bf16 v[20:23], v[76:79], v[56:59], v[20:23]\n\t" \
  "ds_read_b128 v[56:59], v80 offset:10240\n\t" \
  "s_add_u32 m0, %[lw], 90112\n\t" \
  "v_mfma_f32_16x16x32_bf16 v[24:27], v[72:75], v[60:63], v[24:27]\n\t" \
  "global_load_lds_dwordx4 v91, s[86:87]\n\t" \
  "s_add_u32 s86, s86, s89\n\t" \
  "s_addc_u32 s87, s87, 0\n\t" \
  "v_mfma_f32_16x16x32_bf16 v[28:31], v[76:79], v[60:63], v[28:31]\n\t" \
  "ds_read_b128 v[60:63], v80 offset:11264\n\t" \
  "s_cmp_lg_u32 s88, 0\n\t" \
  "s_cbranch_scc1 .Lp0%=\n\t" \
  "s_branch .Le%=\n\t" \
  ".Lk%=:\n\t" \
  "ds_read_b128 v[32:35], v80 offset:0\n\t" \
  "ds_read_b128 v[36:39], v80 offset:1024\n\t" \
  "ds_read_b128 v[40:43], v80 offset:2048\n\t" \
  "ds_read_b128 v[44:47], v80 offset:3072\n\t" \
  "ds_read_b64_tr_b16 v[64:65], v82 offset:0\n\t" \
  "ds_read_b64_tr_b16 v[66:67], v83 offset:0\n\t" \
  "ds_read_b128 v[48:51], v80 offset:8192\n\t" \
  "ds_read_b64_tr_b16 v[68:69], v84 offset:0\n\t" \
  "ds_read_b64_tr_b16 v[70:71], v85 offset:0\n\t" \
  "ds_read_b128 v[52:55], v80 offset:9216\n\t" \
  "ds_read_b128 v[56:59], v80 offset:10240\n\t" \
  "ds_read_b128 v[60:63], v80 offset:11264\n\t" \
  ".Lp1%=:\n\t" \
  "s_waitcnt vmcnt(4)\n\t" \
  "s_barrier\n\t" \
  "s_waitcnt lgkmcnt(3)\n\t" \
  "v_mfma_f32_16x16x32_bf16 v[0:3], v[64:67], v[32:35], v[0:3]\n\t" \
  "ds_read_b64_tr_b16 v[72:73], v82 offset:8192\n\t" \
  "v_mfma_f32_16x16x32_bf16 v[4:7], v[68:71], v[32:35], v[4:7]\n\t" \
  "ds_read_b64_tr_b16 v[74:75], v83 offset:8192\n\t" \
  "ds_read_b128 v[32:35], v80 offset:32768\n\t" \
  "v_mfma_f32_16x16x32_bf16 v[8:11], v[64:67], v[36:39], v[8:11]\n\t" \
  "ds_read_b64_tr_b16 v[76:77], v84 offset:8192\n\t" \
  "v_mfma_f32_16x16x32_bf16 v[12:15], v[68:71], v[36:39], v[12:15]\n\t" \
  "ds_read_b64_tr_b16 v[78:79], v85 offset:8192\n\t" \
  "ds_read_b128 v[36:39], v80 offset:33792\n\t" \
  "s_add_u32 m0, %[lw], 98304\n\t" \
  "v_mfma_f32_16x16x32_bf16 v[16:19], v[64:67], v[40:43], v[16:19]\n\t" \
  "global_load_lds_dwordx4 v90, s[84:85] sc1\n\t" \
  "s_add_u32 s84, s84, s88\n\t" \
  "s_addc_u32 s85, s85, 0\n\t" \
  "v_mfma_f32_16x16x32_bf16 v[20:23], v[68:71], v[40:43], v[20:23]\n\t" \
  "ds_read_b128 v[40:43], v80 offset:34816\n\t" \
  "v_mfma_f32_16x16x32_bf16 v[24:27], v[64:67], v[44:47], v[24:27]\n\t" \
  "s_add_u32 m0, %[lw], 114688\n\t" \
  "v_mfma_f32_16x16x32_bf16 v[28:31], v[68:71], v[44:47], v[28:31]\n\t" \
  "ds_read_b128 v[44:47], v80 offset:35840\n\t" \
  "global_load_lds_dwordx4 v91, s[86:87]\n\t" \
  "s_add_u32 s86, s86, s89\n\t" \
  "s_addc_u32 s87, s87, 0\n\t" \
  "s_waitcnt lgkmcnt(3)\n\t" \
  "v_mfma_f32_16x16x32_bf16 v[0:3], v[72:75], v[48:51], v[0:3]\n\t" \
  "ds_read_b64_tr_b16 v[64:65], v82 offset:32768\n\t" \
  "v_mfma_f32_16x16x32_bf16 v[4:7], v[76:79], v[48:51], v[4:7]\n\t" \
  "ds_read_b64_tr_b16 v[66:67], v83 offset:32768\n\t" \
  "ds_read_b128 v[48:51], v80 offset:40960\n\t" \
  "v_mfma_f32_16x16x32_bf16 v[8:11], v[72:75], v[52:55], v[8:11]\n\t" \
  "ds_read_b64_tr_b16 v[68:69], v84 offset:32768\n\t" \
  "v_mfma_f32_16x16x32_bf16 v[12:15], v[76:79], v[52:55], v[12:15]\n\t" \
  "ds_read_b64_tr_b16 v[70:71], v85 offset:32768\n\t" \
  "ds_read_b128 v[52:55], v80 offset:41984\n\t" \
  "s_add_u32 m0, %[lw], 106496\n\t" \
  "v_mfma_f32_16x16x32_bf16 v[16:19], v[72:75], v[56:59], v[16:19]\n\t" \
  "global_load_lds_dwordx4 v90, s[84:85] sc1\n\t" \
  "s_add_u32 s84, s84, s88\n\t" \
  "s_addc_u32 s85, s85, 0\n\t" \
  "v_mfma_f32_16x16x32_bf16 v[20:23], v[76:79], v[56:59], v[20:23]\n\t" \
  "ds_read_b128 v[56:59], v80 offset:43008\n\t" \
  "v_mfma_f32_16x16x32_bf16 v[24:27], v[72:75], v[60:63], v[24:27]\n\t" \
  "s_add_u32 m0, %[lw], 122880\n\t" \
  "v_mfma_f32_16x16x32_bf16 v[28:31], v[76:79], v[60:63], v[28:31]\n\t" \
  "ds_read_b128 v[60:63], v80 offset:44032\n\t" \
  "global_load_lds_dwordx4 v91, s[86:87]\n\t" \
  "s_add_u32 s86, s86, s89\n\t" \
  "s_addc_u32 s87, s87, 0\n\t" \
  "s_sub_u32 s90, s90, 1\n\t" \
  "s_cselect_b32 s88, 0, s88\n\t" \
  "s_cselect_b32 s89, 0, s89\n\t" \
  "s_cselect_b32 s91, 16384, 0\n\t" \
  "s_sub_u32 s84, s84, s91\n\t" \
  "s_subb_u32 s85, s85, 0\n\t" \
  "s_lshl_b32 s91, s91, 2\n\t" \
  "s_sub_u32 s86, s86, s91\n\t" \
  "s_subb_u32 s87, s87, 0\n\t" \
  "s_waitcnt vmcnt(4)\n\t" \
  "s_barrier\n\t" \
  "s_waitcnt lgkmcnt(3)\n\t" \
  "v_mfma_f32_16x16x32_bf16 v[0:3], v[64:67], v[32:35], v[0:3]\n\t" \
  "ds_read_b64_tr_b16 v[72:73], v82 offset:40960\n\t" \
  "v_mfma_f32_16x16x32_bf16 v[4:7], v[68:71], v[32:35], v[4:7]\n\t" \
  "ds_read_b64_tr_b16 v[74:75], v83 offset:40960\n\t" \
  "ds_read_b128 v[32:35], v81 offset:0\n\t" \
  "v_mfma_f32_16x16x32_bf16 v[8:11], v[64:67], v[36:39], v[8:11]\n\t" \
  "ds_read_b64_tr_b16 v[76:77], v84 offset:40960\n\t" \
  "v_mfma_f32_16x16x32_bf16 v[12:15], v[68:71], v[36:39], v[12:15]\n\t" \
  "ds_read_b64_tr_b16 v[78:79], v85 offset:40960\n\t" \
  "ds_read_b128 v[36:39], v81 offset:1024\n\t" \
  "s_add_u32 m0, %[lw], 0\n\t" \
  "v_mfma_f32_16x16x32_bf16 v[16:19], v[64:67], v[40:43], v[16:19]\n\t" \
  "global_load_lds_dwordx4 v90, s[84:85] sc1\n\t" \
  "s_add_u32 s84, s84, s88\n\t" \
  "s_addc_u32 s85, s85, 0\n\t" \
  "v_mfma_f32_16x16x32_bf16 v[20:23], v[68:71], v[40:43], v[20:23]\n\t" \
  "ds_read_b128 v[40:43], v81 offset:2048\n\t" \
  "v_mfma_f32_16x16x32_bf16 v[24:27], v[64:67], v[44:47], v[24:27]\n\t" \
  "s_add_u32 m0, %[lw], 16384\n\t" \
  "v_mfma_f32_16x16x32_bf16 v[28:31], v[68:71], v[44:47], v[28:31]\n\t" \
  "ds_read_b128 v[44:47], v81 offset:3072\n\t" \
  "global_load_lds_dwordx4 v91, s[86:87]\n\t" \
  "s_add_u32 s86, s86, s89\n\t" \
  "s_addc_u32 s87, s87, 0\n\t" \
  "s_waitcnt lgkmcnt(3)\n\t" \
  "v_mfma_f32_16x16x32_bf16 v[0:3], v[72:75], v[48:51], v[0:3]\n\t" \
  "ds_read_b64_tr_b16 v[64:65], v86 offset:0\n\t" \
  "v_mfma_f32_16x16x32_bf16 v[4:7], v[76:79], v[48:51], v[4:7]\n\t" \
  "ds_read_b64_tr_b16 v[66:67], v87 offset:0\n\t" \
  "ds_read_b128 v[48:51], v81 offset:8192\n\t" \
  "v_mfma_f32_16x16x32_bf16 v[8:11], v[72:75], v[52:55], v[8:11]\n\t" \
  "ds_read_b64_tr_b16 v[68:69], v88 offset:0\n\t" \
  "v_mfma_f32_16x16x32_bf16 v[12:15], v[76:79], v[52:55], v[12:15]\n\t" \
  "ds_read_b64_tr_b16 v[70:71], v89 offset:0\n\t" \
  "ds_read_b128 v[52:55], v81 offset:9216\n\t" \
  "s_add_u32 m0, %[lw], 8192\n\t" \
  "v_mfma_f32_16x16x32_bf16 v[16:19], v[72:75], v[56:59], v[16:19]\n\t" \
  "global_load_lds_dwordx4 v90, s[84:85] sc1\n\t" \
  "s_add_u32 s84, s84, s88\n\t" \
  "s_addc_u32 s85, s85, 0\n\t" \
  "v_mfma_f32_16x16x32_bf16 v[20:23], v[76:79], v[56:59], v[20:23]\n\t" \
  "ds_read_b128 v[56:59], v81 offset:10240\n\t" \
  "v_mfma_f32_16x16x32_bf16 v[24:27], v[72:75], v[60:63], v[24:27]\n\t" \
  "s_add_u32 m0, %[lw], 24576\n\t" \
  "v_mfma_f32_16x16x32_bf16 v[28:31], v[76:79], v[60:63], v[28:31]\n\t" \
  "ds_read_b128 v[60:63], v81 offset:11264\n\t" \
  "global_load_lds_dwordx4 v91, s[86:87]\n\t" \
  "s_add_u32 s86, s86, s89\n\t" \
  "s_addc_u32 s87, s87, 0\n\t" \
  "s_waitcnt vmcnt(4)\n\t" \
  "s_barrier\n\t" \
  "s_waitcnt lgkmcnt(3)\n\t" \
  "v_mfma_f32_16x16x32_bf16 v[0:3], v[64:67], v[32:35], v[0:3]\n\t" \
  "ds_read_b64_tr_b16 v[72:73], v86 offset:8192\n\t" \
  "v_mfma_f32_16x16x32_bf16 v[4:7], v[68:71], v[32:35], v[4:7]\n\t" \
  "ds_read_b64_tr_b16 v[74:75], v87 offset:8192\n\t" \
  "ds_read_b128 v[32:35], v81 offset:32768\n\t" \
  "v_mfma_f32_16x16x32_bf16 v[8:11], v[64:67], v[36:39], v[8:11]\n\t" \
  "ds_read_b64_tr_b16 v[76:77], v88 offset:8192\n\t" \
  "v_mfma_f32_16x16x32_bf16 v[12:15], v[68:71], v[36:39], v[12:15]\n\t" \
  "ds_read_b64_tr_b16 v[78:79], v89 offset:8192\n\t" \
  "ds_read_b128 v[36:39], v81 offset:33792\n\t" \
  "s_add_u32 m0, %[lw], 32768\n\t" \
  "v_mfma_f32_16x16x32_bf16 v[16:19], v[64:67], v[40:43], v[16:19]\n\t" \
  "global_load_lds_dwordx4 v90, s[84:85] sc1\n\t" \
  "s_add_u32 s84, s84, s88\n\t" \
  "s_addc_u32 s85, s85, 0\n\t" \
  "v_mfma_f32_16x16x32_bf16 v[20:23], v[68:71], v[40:43], v[20:23]\n\t" \
  "ds_read_b128 v[40:43], v81 offset:34816\n\t" \
  "v_mfma_f32_16x16x32_bf16 v[24:27], v[64:67], v[44:47], v[24:27]\n\t" \
  "s_add_u32 m0, %[lw], 49152\n\t" \
  "v_mfma_f32_16x16x32_bf16 v[28:31], v[68:71], v[44:47], v[28:31]\n\t" \
  "ds_read_b128 v[44:47], v81 offset:35840\n\t" \
  "global_load_lds_dwordx4 v91, s[86:87]\n\t" \
  "s_add_u32 s86, s86, s89\n\t" \
  "s_addc_u32 s87, s87, 0\n\t" \
  "s_waitcnt lgkmcnt(3)\n\t" \
  "v_mfma_f32_16x16x32_bf16 v[0:3], v[72:75], v[48:51], v[0:3]\n\t" \
  "ds_read_b64_tr_b16 v[64:65], v86 offset:32768\n\t" \
  "v_mfma_f32_16x16x32_bf16 v[4:7], v[76:79], v[48:51], v[4:7]\n\t" \
  "ds_read_b64_tr_b16 v[66:67], v87 offset:32768\n\t" \
  "ds_read_b128 v[48:51], v81 offset:40960\n\t" \
  "v_mfma_f32_16x16x32_bf16 v[8:11], v[72:75], v[52:55], v[8:11]\n\t" \
  "ds_read_b64_tr_b16 v[68:69], v88 offset:32768\n\t" \
  "v_mfma_f32_16x16x32_bf16 v[12:15], v[76:79], v[52:55], v[12:15]\n\t" \
  "ds_read_b64_tr_b16 v[70:71], v89 offset:32768\n\t" \
  "ds_read_b128 v[52:55], v81 offset:41984\n\t" \
  "s_add_u32 m0, %[lw], 40960\n\t" \
  "v_mfma_f32_16x16x32_bf16 v[16:19], v[72:75], v[56:59], v[16:19]\n\t" \
  "global_load_lds_dwordx4 v90, s[84:85] sc1\n\t" \
  "s_add_u32 s84, s84, s88\n\t" \
  "s_addc_u32 s85, s85, 0\n\t" \
  "v_mfma_f32_16x16x32_bf16 v[20:23], v[76:79], v[56:59], v[20:23]\n\t" \
  "ds_read_b128 v[56:59], v81 offset:43008\n\t" \
  "v_mfma_f32_16x16x32_bf16 v[24:27], v[72:75], v[60:63], v[24:27]\n\t" \
  "s_add_u32 m0, %[lw], 57344\n\t" \
  "v_mfma_f32_16x16x32_bf16 v[28:31], v[76:79], v[60:63], v[28:31]\n\t" \
  "ds_read_b128 v[60:63], v81 offset:44032\n\t" \
  "global_load_lds_dwordx4 v91, s[86:87]\n\t" \
  "s_add_u32 s86, s86, s89\n\t" \
  "s_addc_u32 s87, s87, 0\n\t" \
  "s_waitcnt vmcnt(4)\n\t" \
  "s_barrier\n\t" \
  "s_waitcnt lgkmcnt(3)\n\t" \
  "v_mfma_f32_16x16x32_bf16 v[0:3], v[64:67], v[32:35], v[0:3]\n\t" \
  "ds_read_b64_tr_b16 v[72:73], v86 offset:40960\n\t" \
  "v_mfma_f32_16x16x32_bf16 v[4:7], v[68:71], v[32:35], v[4:7]\n\t" \
  "ds_read_b64_tr_b16 v[74:75], v87 offset:40960\n\t" \
  "ds_read_b128 v[32:35], v80 offset:0\n\t" \
  "v_mfma_f32_16x16x32_bf16 v[8:11], v[64:67], v[36:39], v[8:11]\n\t" \
  "ds_read_b64_tr_b16 v[76:77], v88 offset:40960\n\t" \
  "v_mfma_f32_16x16x32_bf16 v[12:15], v[68:71], v[36:39], v[12:15]\n\t" \
  "ds_read_b64_tr_b16 v[78:79], v89 offset:40960\n\t" \
  "ds_read_b128 v[36:39], v80 offset:1024\n\t" \
  "s_add_u32 m0, %[lw], 65536\n\t" \
  "v_mfma_f32_16x16x32_bf16 v[16:19], v[64:67], v[40:43], v[16:19]\n\t" \
  "global_load_lds_dwordx4 v90, s[84:85] sc1\n\t" \
  "s_add_u32 s84, s84, s88\n\t" \
  "s_addc_u32 s85, s85, 0\n\t" \
  "v_mfma_f32_16x16x32_bf16 v[20:23], v[68:71], v[40:43], v[20:23]\n\t" \
  "ds_read_b128 v[40:43], v80 offset:2048\n\t" \
  "v_mfma_f32_16x16x32_bf16 v[24:27], v[64:67], v[44:47], v[24:27]\n\t" \
  "s_add_u32 m0, %[lw], 81920\n\t" \
  "v_mfma_f32_16x16x32_bf16 v[28:31], v[68:71], v[44:47], v[28:31]\n\t" \
  "ds_read_b128 v[44:47], v80 offset:3072\n\t" \
  "global_load_lds_dwordx4 v91, s[86:87]\n\t" \
  "s_add_u32 s86, s86, s89\n\t" \
  "s_addc_u32 s87, s87, 0\n\t" \
  "s_waitcnt lgkmcnt(3)\n\t" \
  "v_mfma_f32_16x16x32_bf16 v[0:3], v[72:75], v[48:51], v[0:3]\n\t" \
  "ds_read_b64_tr_b16 v[64:65], v82 offset:0\n\t" \
  "v_mfma_f32_16x16x32_bf16 v[4:7], v[76:79], v[48:51], v[4:7]\n\t" \
  "ds_read_b64_tr_b16 v[66:67], v83 offset:0\n\t" \
  "ds_read_b128 v[48:51], v80 offset:8192\n\t" \
  "v_mfma_f32_16x16x32_bf16 v[8:11], v[72:75], v[52:55], v[8:11]\n\t" \
  "ds_read_b64_tr_b16 v[68:69], v84 offset:0\n\t" \
  "v_mfma_f32_16x16x32_bf16 v[12:15], v[76:79], v[52:55], v[12:15]\n\t" \
  "ds_read_b64_tr_b16 v[70:71], v85 offset:0\n\t" \
  "ds_read_b128 v[52:55], v80 offset:9216\n\t" \
  "s_add_u32 m0, %[lw], 73728\n\t" \
  "v_mfma_f32_16x16x32_bf16 v[16:19], v[72:75], v[56:59], v[16:19]\n\t" \
  "global_load_lds_dwordx4 v90, s[84:85] sc1\n\t" \
  "s_add_u32 s84, s84, s88\n\t" \
  "s_addc_u32 s85, s85, 0\n\t" \
  "v_mfma_f32_16x16x32_bf16 v[20:23], v[76:79], v[56:59], v[20:23]\n\t" \
  "ds_read_b128 v[56:59], v80 offset:10240\n\t" \
  "v_mfma_f32_16x16x32_bf16 v[24:27], v[72:75], v[60:63], v[24:27]\n\t" \
  "s_add_u32 m0, %[lw], 90112\n\t" \
  "v_mfma_f32_16x16x32_bf16 v[28:31], v[76:79], v[60:63], v[28:31]\n\t" \
  "ds_read_b128 v[60:63], v80 offset:11264\n\t" \
  "global_load_lds_dwordx4 v91, s[86:87]\n\t" \
  "s_add_u32 s86, s86, s89\n\t" \
  "s_addc_u32 s87, s87, 0\n\t" \
  "s_cmp_lg_u32 s88, 0\n\t" \
  "s_cbranch_scc1 .Lp1%=\n\t" \
  ".Le%=:\n\t" \
  "s_waitcnt vmcnt(0) lgkmcnt(0)\n\t" \
  "s_mov_b32 m0, s94\n\t" \
  "s_setprio 0\n\t" \
  "s_nop 7\n\t"
#define HSL 16
__device__ __forceinline__ void job_down(const Args& p, int rblk, int ct, char* smem, __amdgpu_buffer_rsrc_t rsy) {
  if (threadIdx.x < 64) {
    for (unsigned spins = 0;;) {
      asm volatile("" ::: "memory");
      if (__builtin_amdgcn_readfirstlane((int)ld_flag0(rsy, SY_CH + (rblk >> 1) * 128)) >= 27) break;
      __builtin_amdgcn_s_sleep(HSL);
      if (spin_fail(spins, (gu32*)(p.ws + WS_SY + SY_ABORT))) break;
    }
  }
  int tid = threadIdx.x;
  asm volatile("" : "+v"(tid));
  const int lane = tid & 63, r = lane & 15, q = lane >> 4;
  const int wid = __builtin_amdgcn_readfirstlane(tid >> 6), wr = wid >> 2, wc = wid & 3;
  typedef __attribute__((ext_vector_type(32))) float f32x32;
  typedef __attribute__((ext_vector_type(8))) unsigned u32x8;
  f32x32 acc;
  {
    const char* A = p.ws + WS_HF + h_off(p, rblk);
    const char* B = (const char*)(p.wd + ct * 128);
    const unsigned rb = tid >> 4, va = tid * 16, vb = ((rb & 4) * 4 + (rb >> 3) * 4 + (rb & 3)) * (DM * 2) + 16 * ((tid & 15) ^ swzB(rb & 15));
    u32x8 lb;
#pragma unroll
    for (int i = 0; i < 8; ++i)
      lb[i] = (i >> 2) * 65536 + 16384 + offB(8 * q + 4 * (i & 1) + (r >> 2), 2 * (wc * 2 + ((i >> 1) & 1)) + ((r & 3) >> 1)) + 8 * (r & 1);
    LBAR();
#pragma unroll
    for (int t = 0; t < 3; ++t) {
      glds2<true>(A + t * 32768, 16384, va, wid * 1024 + t * 32768);
      glds2(B + t * 64 * DM * 2, 32 * DM * 2, vb, wid * 1024 + t * 32768 + 16384);
    }
    WAIT_V(8);
    LBAR();
    asm volatile(DN_ASM
        : "={v[0:31]}"(acc)
        : "{v[80:81]}"((u32x2){wr * 4096u + lane * 16u, 65536u + wr * 4096u + lane * 16u}), "{v[82:89]}"(lb), "{v90}"(va), "{v91}"(vb),
          [pa] "s"(A + 3 * 32768), [pb] "s"(B + 3 * 64 * DM * 2), [lw] "s"(wid * 1024), [hf] "s"(wr)
        : CLB8(32, 33, 34, 35, 36, 37, 38, 39), CLB8(40, 41, 42, 43, 44, 45, 46, 47), CLB8(48, 49, 50, 51, 52, 53, 54, 55),
          CLB8(56, 57, 58, 59, 60, 61, 62, 63), CLB8(64, 65, 66, 67, 68, 69, 70, 71), CLB8(72, 73, 74, 75, 76, 77, 78, 79),
          "s84", "s85", "s86", "s87", "s88", "s89", "s90", "s91", "s94", "scc", "memory");
  }
  wait_s(p, rsy, rblk, ct * 4, 4, rblk < NFUSE);
  const int e0 = (rblk * 128 + wr * 64 + r) * DM + ct * 128 + wc * 32 + q * 8;
  SX sx[4];
#pragma unroll
  for (int m = 0; m < 4; ++m) sx[m] = get_sx(p, rblk, e0 + m * 16 * DM);
  const __amdgpu_buffer_rsrc_t ro = mkrsrc(p.ws + WS_G, S_LEN * DM * 2);
#pragma unroll
  for (int m = 0; m < 4; ++m) {
    float rr[8];
#pragma unroll
    for (int j = 0; j < 8; ++j) { float d = rbf(acc[8 * m + j]); rr[j] = d * __builtin_amdgcn_rcpf(1.0f + __expf(-d)); }
    put_g(p, ro, rblk, e0 + m * 16 * DM, widen32(cvtpk(rr[0], rr[1]), cvtpk(rr[2], rr[3]), cvtpk(rr[4], rr[5]), cvtpk(rr[6], rr[7])), sx[m]);
  }
  if (rblk >= NFUSE) publish_flags(rsy, SY_FG + (rblk * 8 + ct) * 128, 0, 1);
}

#define PR_ASM_0 \
  "s_cmp_lg_u32 %[kh], 0\n\t" \
  "s_cbranch_scc1 .Lk%=\n\t" \
  "s_mov_b32 s94, m0\n\t" \
  "s_mov_b64 s[92:93], %[x0]\n\t" \
  "s_mov_b32 s86, %[g0]\n\t" \
  "s_add_u32 s87, s86, %[t0]\n\t" \
  "s_sub_i32 s88, s86, 3\n\t" \
  "ds_read_b128 v[176:179], v208 offset:0\n\t" \
  "ds_read_b128 v[180:183], v208 offset:16384\n\t" \
  "ds_read_b128 v[184:187], v209 offset:0\n\t" \
  "ds_read_b128 v[188:191], v209 offset:16384\n\t" \
  "ds_read_b128 v[192:195], v210 offset:0\n\t" \
  "ds_read_b128 v[196:199], v210 offset:16384\n\t" \
  "ds_read_b128 v[200:203], v211 offset:0\n\t" \
  "ds_read_b128 v[204:207], v211 offset:16384\n\t" \
  "s_branch .Lfb00%=\n\t" \
  ".Lp0%=:\n\t" \
  "s_cmp_le_u32 s86, 5\n\t" \
  "s_cbranch_scc1 .Lf00%=\n\t" \
  ".Lfb00%=:\n\t" \
  "s_waitcnt lgkmcnt(6)\n\t" \
  "v_mfma_f32_16x16x32_bf16 v[128:131], v[0:3], v[176:179], 0\n\t" \
  "v_mfma_f32_16x16x32_bf16 v[132:135], v[4:7], v[176:179], 0\n\t" \
  "ds_read_b128 v[176:179], v208 offset:256\n\t" \
  "s_add_u32 m0, %[lw], 67584\n\t" \
  "v_mfma_f32_16x16x32_bf16 v[136:139], v[0:3], v[180:183], 0\n\t" \
  "v_mfma_f32_16x16x32_bf16 v[140:143], v[4:7], v[180:183], 0\n\t" \
  "ds_read_b128 v[180:183], v208 offset:16640\n\t" \
  "global_load_lds_dwordx4 v218, s[92:93] sc1\n\t" \
  "s_waitcnt lgkmcnt(6)\n\t" \
  "v_mfma_f32_16x16x32_bf16 v[128:131], v[8:11], v[184:187], v[128:131]\n\t" \
  "v_mfma_f32_16x16x32_bf16 v[132:135], v[12:15], v[184:187], v[132:135]\n\t" \
  "ds_read_b128 v[184:187], v209 offset:256\n\t" \
  "v_mfma_f32_16x16x32_bf16 v[136:139], v[8:11], v[188:191], v[136:139]\n\t" \
  "v_mfma_f32_16x16x32_bf16 v[140:143], v[12:15], v[188:191], v[140:143]\n\t" \
  "ds_read_b128 v[188:191], v209 offset:16640\n\t" \
  "ds_write_b128 v220, v[152:155] offset:0\n\t" \
  "ds_write_b128 v220, v[156:159] offset:1024\n\t" \
  "s_waitcnt lgkmcnt(8)\n\t" \
  "v_mfma_f32_16x16x32_bf16 v[128:131], v[16:19], v[192:195], v[128:131]\n\t" \
  "v_mfma_f32_16x16x32_bf16 v[132:135], v[20:23], v[192:195], v[132:135]\n\t" \
  "ds_read_b128 v[192:195], v210 offset:256\n\t" \
  "s_add_u32 m0, %[lw], 68608\n\t" \
  "v_mfma_f32_16x16x32_bf16 v[136:139], v[16:19], v[196:199], v[136:139]\n\t" \
  "v_mfma_f32_16x16x32_bf16 v[140:143], v[20:23], v[196:199], v[140:143]\n\t" \
  "ds_read_b128 v[196:199], v210 offset:16640\n\t" \
  "global_load_lds_dwordx4 v219, s[92:93] sc1\n\t" \
  "s_lshr_b32 s90, s87, 7\n\t" \
  "s_xor_b32 s90, s90, %[s0]\n\t" \
  "s_and_b32 s90, s90, 1\n\t" \
  "s_lshl1_add_u32 s90, s87, s90\n\t" \
  "s_lshl_b32 s90, s90, 13\n\t" \
  "s_cmp_lt_i32 s86, 6\n\t" \
  "s_cselect_b32 s90, 0x70000000, s90\n\t" \
  "s_waitcnt lgkmcnt(8)\n\t" \
  "v_mfma_f32_16x16x32_bf16 v[128:131], v[24:27], v[200:203], v[128:131]\n\t" \
  "v_mfma_f32_16x16x32_bf16 v[132:135], v[28:31], v[200:203], v[132:135]\n\t" \
  "ds_read_b128 v[200:203], v211 offset:256\n\t" \
  "v_mfma_f32_16x16x32_bf16 v[136:139], v[24:27], v[204:207], v[136:139]\n\t" \
  "v_mfma_f32_16x16x32_bf16 v[140:143], v[28:31], v[204:207], v[140:143]\n\t" \
  "ds_read_b128 v[204:207], v211 offset:16640\n\t" \
  "s_waitcnt lgkmcnt(4)\n\t" \
  "s_cmp_lt_u32 s86, 6\n\t" \
  "s_cbranch_scc1 .Lw00%=\n\t" \
  "s_waitcnt vmcnt(4)\n\t" \
  "s_barrier\n\t" \
  ".Lg00%=:\n\t" \
  "s_add_u32 s92, s92, 512\n\t" \
  "s_addc_u32 s93, s93, 0\n\t" \
  "v_mfma_f32_16x16x32_bf16 v[128:131], v[32:35], v[176:179], v[128:131]\n\t" \
  "v_mfma_f32_16x16x32_bf16 v[132:135], v[36:39], v[176:179], v[132:135]\n\t" \
  "ds_read_b128 v[176:179], v208 offset:32768\n\t" \
  "s_add_u32 m0, %[lw], 98304\n\t" \
  "v_mfma_f32_16x16x32_bf16 v[136:139], v[32:35], v[180:183], v[136:139]\n\t" \
  "v_mfma_f32_16x16x32_bf16 v[140:143], v[36:39], v[180:183], v[140:143]\n\t" \
  "ds_read_b128 v[180:183], v208 offset:49152\n\t" \
  "global_load_lds_dwordx4 v216, s[92:93] sc1\n\t" \
  "ds_read_b128 v[160:163], v221 offset:0\n\t" \
  "ds_read_b128 v[164:167], v221 offset:1024\n\t" \
  "v_mfma_f32_16x16x32_bf16 v[128:131], v[40:43], v[184:187], v[128:131]\n\t" \
  "v_mfma_f32_16x16x32_bf16 v[132:135], v[44:47], v[184:187], v[132:135]\n\t" \
  "ds_read_b128 v[184:187], v209 offset:32768\n\t" \
  "v_mfma_f32_16x16x32_bf16 v[136:139], v[40:43], v[188:191], v[136:139]\n\t" \
  "v_mfma_f32_16x16x32_bf16 v[140:143], v[44:47], v[188:191], v[140:143]\n\t" \
  "ds_read_b128 v[188:191], v209 offset:49152\n\t" \
  "s_waitcnt lgkmcnt(8)\n\t" \
  "v_mfma_f32_16x16x32_bf16 v[128:131], v[48:51], v[192:195], v[128:131]\n\t" \
  "v_mfma_f32_16x16x32_bf16 v[132:135], v[52:55], v[192:195], v[132:135]\n\t" \
  "ds_read_b128 v[192:195], v210 offset:32768\n\t" \
  "s_add_u32 m0, %[lw], 99328\n\t" \
  "v_mfma_f32_16x16x32_bf16 v[136:139], v[48:51], v[196:199], v[136:139]\n\t" \
  "v_mfma_f32_16x16x32_bf16 v[140:143], v[52:55], v[196:199], v[140:143]\n\t" \
  "ds_read_b128 v[196:199], v210 offset:49152\n\t" \
  "global_load_lds_dwordx4 v217, s[92:93] sc1\n\t" \
  "s_waitcnt lgkmcnt(4)\n\t" \
  "v_add_f32 v160, v160, v144\n\t" \
  "v_add_f32 v161, v161, v145\n\t" \
  "v_add_f32 v162, v162, v146\n\t" \
  "v_add_f32 v163, v163, v147\n\t" \
  "v_add_f32 v164, v164, v148\n\t" \
  "v_add_f32 v165, v165, v149\n\t" \
  "v_add_f32 v166, v166, v150\n\t" \
  "v_add_f32 v167, v167, v151\n\t" \
  "v_mfma_f32_16x16x32_bf16 v[128:131], v[56:59], v[200:203], v[128:131]\n\t" \
  "v_mfma_f32_16x16x32_bf16 v[132:135], v[60:63], v[200:203], v[132:135]\n\t" \
  "ds_read_b128 v[200:203], v211 offset:32768\n\t" \
  "v_mfma_f32_16x16x32_bf16 v[136:139], v[56:59], v[204:207], v[136:139]\n\t" \
  "v_mfma_f32_16x16x32_bf16 v[140:143], v[60:63], v[204:207], v[140:143]\n\t" \
  "ds_read_b128 v[204:207], v211 offset:49152\n\t" \
  "v_cvt_pk_bf16_f32 v168, v160, v161\n\t" \
  "v_cvt_pk_bf16_f32 v169, v162, v163\n\t" \
  "v_cvt_pk_bf16_f32 v170, v164, v165\n\t" \
  "v_cvt_pk_bf16_f32 v171, v166, v167\n\t" \
  "buffer_store_dwordx4 v[168:171], v172, %[rt], s90 offen sc1\n\t" \
  "v_mfma_f32_16x16x32_bf16 v[128:131], v[64:67], v[176:179], v[128:131]\n\t" \
  "v_mfma_f32_16x16x32_bf16 v[132:135], v[68:71], v[176:179], v[132:135]\n\t" \
  "ds_read_b128 v[176:179], v208 offset:33024\n\t" \
  "s_add_u32 m0, %[lw], 100352\n\t" \
  "v_mfma_f32_16x16x32_bf16 v[136:139], v[64:67], v[180:183], v[136:139]\n\t" \
  "v_mfma_f32_16x16x32_bf16 v[140:143], v[68:71], v[180:183], v[140:143]\n\t" \
  "ds_read_b128 v[180:183], v208 offset:49408\n\t" \
  "global_load_lds_dwordx4 v218, s[92:93] sc1\n\t" \
  "s_waitcnt lgkmcnt(6)\n\t" \
  "v_mfma_f32_16x16x32_bf16 v[128:131], v[72:75], v[184:187], v[128:131]\n\t" \
  "v_mfma_f32_16x16x32_bf16 v[132:135], v[76:79], v[184:187], v[132:135]\n\t" \
  "ds_read_b128 v[184:187], v209 offset:33024\n\t" \
  "v_mfma_f32_16x16x32_bf16 v[136:139], v[72:75], v[188:191], v[136:139]\n\t" \
  "v_mfma_f32_16x16x32_bf16 v[140:143], v[76:79], v[188:191], v[140:143]\n\t" \
  "ds_read_b128 v[188:191], v209 offset:49408\n\t" \
  "s_waitcnt lgkmcnt(6)\n\t" \
  "v_mfma_f32_16x16x32_bf16 v[128:131], v[80:83], v[192:195], v[128:131]\n\t" \
  "v_mfma_f32_16x16x32_bf16 v[132:135], v[84:87], v[192:195], v[132:135]\n\t" \
  "ds_read_b128 v[192:195], v210 offset:33024\n\t" \
  "s_add_u32 m0, %[lw], 101376\n\t" \
  "v_mfma_f32_16x16x32_bf16 v[136:139], v[80:83], v[196:199], v[136:139]\n\t" \
  "v_mfma_f32_16x16x32_bf16 v[140:143], v[84:87], v[196:199], v[140:143]\n\t" \
  "ds_read_b128 v[196:199], v210 offset:49408\n\t" \
  "global_load_lds_dwordx4 v219, s[92:93] sc1\n\t" \
  "s_waitcnt lgkmcnt(6)\n\t" \
  "v_mfma_f32_16x16x32_bf16 v[128:131], v[88:91], v[200:203], v[128:131]\n\t" \
  "v_mfma_f32_16x16x32_bf16 v[132:135], v[92:95], v[200:203], v[132:135]\n\t" \
  "ds_read_b128 v[200:203], v211 offset:33024\n\t" \
  "v_mfma_f32_16x16x32_bf16 v[136:139], v[88:91], v[204:207], v[136:139]\n\t" \
  "v_mfma_f32_16x16x32_bf16 v[140:143], v[92:95], v[204:207], v[140:143]\n\t" \
  "ds_read_b128 v[204:207], v211 offset:49408\n\t" \
  "s_cmp_lt_u32 s86, 8\n\t" \
  "s_cbranch_scc1 .Lw10%=\n\t" \
  "s_waitcnt vmcnt(5)\n\t" \
  "s_barrier\n\t" \
  ".Lg10%=:\n\t" \
  "s_add_u32 s92, s92, 65024\n\t" \
  "s_addc_u32 s93, s93, 0\n\t" \
  "s_waitcnt lgkmcnt(6)\n\t" \
  "v_mfma_f32_16x16x32_bf16 v[128:131], v[96:99], v[176:179], v[128:131]\n\t" \
  "v_mfma_f32_16x16x32_bf16 v[132:135], v[100:103], v[176:179], v[132:135]\n\t" \
  "ds_read_b128 v[176:179], v212 offset:0\n\t" \
  "s_add_u32 m0, %[lw], 0\n\t" \
  "v_mfma_f32_16x16x32_bf16 v[136:139], v[96:99], v[180:183], v[136:139]\n\t" \
  "v_mfma_f32_16x16x32_bf16 v[140:143], v[100:103], v[180:183], v[140:143]\n\t" \
  "ds_read_b128 v[180:183], v212 offset:16384\n\t" \
  "global_load_lds_dwordx4 v216, s[92:93] sc1\n\t" \
  "s_waitcnt lgkmcnt(6)\n\t" \
  "v_mfma_f32_16x16x32_bf16 v[128:131], v[104:107], v[184:187], v[128:131]\n\t" \
  "v_mfma_f32_16x16x32_bf16 v[132:135], v[108:111], v[184:187], v[132:135]\n\t" \
  "ds_read_b128 v[184:187], v213 offset:0\n\t" \
  "v_mfma_f32_16x16x32_bf16 v[136:139], v[104:107], v[188:191], v[136:139]\n\t" \
  "v_mfma_f32_16x16x32_bf16 v[140:143], v[108:111], v[188:191], v[140:143]\n\t" \
  "ds_read_b128 v[188:191], v213 offset:16384\n\t" \
  "s_waitcnt lgkmcnt(6)\n\t" \
  "v_mfma_f32_16x16x32_bf16 v[128:131], v[112:115], v[192:195], v[128:131]\n\t" \
  "v_mfma_f32_16x16x32_bf16 v[132:135], v[116:119], v[192:195], v[132:135]\n\t" \
  "ds_read_b128 v[192:195], v214 offset:0\n\t" \
  "s_add_u32 m0, %[lw], 1024\n\t" \
  "v_mfma_f32_16x16x32_bf16 v[136:139], v[112:115], v[196:199], v[136:139]\n\t" \
  "v_mfma_f32_16x16x32_bf16 v[140:143], v[116:119], v[196:199], v[140:143]\n\t" \
  "ds_read_b128 v[196:199], v214 offset:16384\n\t" \
  "global_load_lds_dwordx4 v217, s[92:93] sc1\n\t" \
  "s_waitcnt lgkmcnt(6)\n\t" \
  "v_mfma_f32_16x16x32_bf16 v[128:131], v[120:123], v[200:203], v[128:131]\n\t" \
  "v_mfma_f32_16x16x32_bf16 v[132:135], v[124:127], v[200:203], v[132:135]\n\t" \
  "ds_read_b128 v[200:203], v215 offset:0\n\t" \
  "v_mfma_f32_16x16x32_bf16 v[136:139], v[120:123], v[204:207], v[136:139]\n\t" \
  "v_mfma_f32_16x16x32_bf16 v[140:143], v[124:127], v[204:207], v[140:143]\n\t" \
  "ds_read_b128 v[204:207], v215 offset:16384\n\t" \
  "s_add_u32 s86, s86, 1\n\t" \
  "s_add_u32 s87, s87, 1\n\t" \
  "s_cmp_le_u32 s86, 5\n\t" \
  "s_cbranch_scc1 .Lf10%=\n\t" \
  ".Lfb10%=:\n\t" \
  "s_waitcnt lgkmcnt(6)\n\t" \
  "v_mfma_f32_16x16x32_bf16 v[144:147], v[0:3], v[176:179], 0\n\t" \
  "v_mfma_f32_16x16x32_bf16 v[148:151], v[4:7], v[176:179], 0\n\t" \
  "ds_read_b128 v[176:179], v212 offset:256\n\t" \
  "s_add_u32 m0, %[lw], 2048\n\t" \
  "v_mfma_f32_16x16x32_bf16 v[152:155], v[0:3], v[180:183], 0\n\t" \
  "v_mfma_f32_16x16x32_bf16 v[156:159], v[4:7], v[180:183], 0\n\t" \
  "ds_read_b128 v[180:183], v212 offset:16640\n\t" \
  "global_load_lds_dwordx4 v218, s[92:93] sc1\n\t" \
  "s_waitcnt lgkmcnt(6)\n\t" \
  "v_mfma_f32_16x16x32_bf16 v[144:147], v[8:11], v[184:187], v[144:147]\n\t" \
  "v_mfma_f32_16x16x32_bf16 v[148:151], v[12:15], v[184:187], v[148:151]\n\t" \
  "ds_read_b128 v[184:187], v213 offset:256\n\t" \
  "v_mfma_f32_16x16x32_bf16 v[152:155], v[8:11], v[188:191], v[152:155]\n\t" \
  "v_mfma_f32_16x16x32_bf16 v[156:159], v[12:15], v[188:191], v[156:159]\n\t" \
  "ds_read_b128 v[188:191], v213 offset:16640\n\t" \
  "ds_write_b128 v220, v[136:139] offset:0\n\t" \
  "ds_write_b128 v220, v[140:143] offset:1024\n\t" \
  "s_waitcnt lgkmcnt(8)\n\t" \
  "v_mfma_f32_16x16x32_bf16 v[144:147], v[16:19], v[192:195], v[144:147]\n\t" \
  "v_mfma_f32_16x16x32_bf16 v[148:151], v[20:23], v[192:195], v[148:151]\n\t" \
  "ds_read_b128 v[192:195], v214 offset:256\n\t" \
  "s_add_u32 m0, %[lw], 3072\n\t" \
  "v_mfma_f32_16x16x32_bf16 v[152:155], v[16:19], v[196:199], v[152:155]\n\t" \
  "v_mfma_f32_16x16x32_bf16 v[156:159], v[20:23], v[196:199], v[156:159]\n\t" \
  "ds_read_b128 v[196:199], v214 offset:16640\n\t" \
  "global_load_lds_dwordx4 v219, s[92:93] sc1\n\t" \
  "s_lshr_b32 s90, s87, 7\n\t" \
  "s_xor_b32 s90, s90, %[s0]\n\t" \
  "s_and_b32 s90, s90, 1\n\t" \
  "s_lshl1_add_u32 s90, s87, s90\n\t" \
  "s_lshl_b32 s90, s90, 13\n\t" \
  "s_cmp_lt_i32 s86, 6\n\t" \
  "s_cselect_b32 s90, 0x70000000, s90\n\t" \
  "s_waitcnt lgkmcnt(8)\n\t" \
  "v_mfma_f32_16x16x32_bf16 v[144:147], v[24:27], v[200:203], v[144:147]\n\t" \
  "v_mfma_f32_16x16x32_bf16 v[148:151], v[28:31], v[200:203], v[148:151]\n\t" \
  "ds_read_b128 v[200:203], v215 offset:256\n\t" \
  "v_mfma_f32_16x16x32_bf16 v[152:155], v[24:27], v[204:207], v[152:155]\n\t" \
  "v_mfma_f32_16x16x32_bf16 v[156:159], v[28:31], v[204:207], v[156:159]\n\t" \
  "ds_read_b128 v[204:207], v215 offset:16640\n\t" \
  "s_waitcnt lgkmcnt(4)\n\t" \
  "s_cmp_lt_u32 s86, 6\n\t" \
  "s_cbranch_scc1 .Lw20%=\n\t" \
  "s_waitcnt vmcnt(4)\n\t" \
  "s_barrier\n\t" \
  ".Lg20%=:\n\t" \
  "s_add_u32 s92, s92, 512\n\t" \
  "s_addc_u32 s93, s93, 0\n\t" \
  "v_mfma_f32_16x16x32_bf16 v[144:147], v[32:35], v[176:179], v[144:147]\n\t" \
  "v_mfma_f32_16x16x32_bf16 v[148:151], v[36:39], v[176:179], v[148:151]\n\t" \
  "ds_read_b128 v[176:179], v212 offset:32768\n\t" \
  "s_add_u32 m0, %[lw], 32768\n\t" \
  "v_mfma_f32_16x16x32_bf16 v[152:155], v[32:35], v[180:183], v[152:155]\n\t" \
  "v_mfma_f32_16x16x32_bf16 v[156:159], v[36:39], v[180:183], v[156:159]\n\t" \
  "ds_read_b128 v[180:183], v212 offset:49152\n\t" \
  "global_load_lds_dwordx4 v216, s[92:93] sc1\n\t" \
  "ds_read_b128 v[160:163], v221 offset:0\n\t" \
  "ds_read_b128 v[164:167], v221 offset:1024\n\t" \
  "v_mfma_f32_16x16x32_bf16 v[144:147], v[40:43], v[184:187], v[144:147]\n\t" \
  "v_mfma_f32_16x16x32_bf16 v[148:151], v[44:47], v[184:187], v[148:151]\n\t" \
  "ds_read_b128 v[184:187], v213 offset:32768\n\t" \
  "v_mfma_f32_16x16x32_bf16 v[152:155], v[40:43], v[188:191], v[152:155]\n\t" \
  "v_mfma_f32_16x16x32_bf16 v[156:159], v[44:47], v[188:191], v[156:159]\n\t" \
  "ds_read_b128 v[188:191], v213 offset:49152\n\t" \
  "s_waitcnt lgkmcnt(8)\n\t" \
  "v_mfma_f32_16x16x32_bf16 v[144:147], v[48:51], v[192:195], v[144:147]\n\t" \
  "v_mfma_f32_16x16x32_bf16 v[148:151], v[52:55], v[192:195], v[148:151]\n\t" \
  "ds_read_b128 v[192:195], v214 offset:32768\n\t" \
  "s_add_u32 m0, %[lw], 33792\n\t" \
  "v_mfma_f32_16x16x32_bf16 v[152:155], v[48:51], v[196:199], v[152:155]\n\t" \
  "v_mfma_f32_16x16x32_bf16 v[156:159], v[52:55], v[196:199], v[156:159]\n\t" \
  "ds_read_b128 v[196:199], v214 offset:49152\n\t" \
  "global_load_lds_dwordx4 v217, s[92:93] sc1\n\t" \
  "s_waitcnt lgkmcnt(4)\n\t" \
  "v_add_f32 v160, v160, v128\n\t" \
  "v_add_f32 v161, v161, v129\n\t" \
  "v_add_f32 v162, v162, v130\n\t" \
  "v_add_f32 v163, v163, v131\n\t" \
  "v_add_f32 v164, v164, v132\n\t" \
  "v_add_f32 v165, v165, v133\n\t" \
  "v_add_f32 v166, v166, v134\n\t" \
  "v_add_f32 v167, v167, v135\n\t" \
  "v_mfma_f32_16x16x32_bf16 v[144:147], v[56:59], v[200:203], v[144:147]\n\t" \
  "v_mfma_f32_16x16x32_bf16 v[148:151], v[60:63], v[200:203], v[148:151]\n\t" \
  "ds_read_b128 v[200:203], v215 offset:32768\n\t" \
  "v_mfma_f32_16x16x32_bf16 v[152:155], v[56:59], v[204:207], v[152:155]\n\t" \
  "v_mfma_f32_16x16x32_bf16 v[156:159], v[60:63], v[204:207], v[156:159]\n\t" \
  "ds_read_b128 v[204:207], v215 offset:49152\n\t" \
  "v_cvt_pk_bf16_f32 v168, v160, v161\n\t" \
  "v_cvt_pk_bf16_f32 v169, v162, v163\n\t" \
  "v_cvt_pk_bf16_f32 v170, v164, v165\n\t" \
  "v_cvt_pk_bf16_f32 v171, v166, v167\n\t" \
  "buffer_store_dwordx4 v[168:171], v172, %[rt], s90 offen sc1\n\t" \
  "v_mfma_f32_16x16x32_bf16 v[144:147], v[64:67], v[176:179], v[144:147]\n\t" \
  "v_mfma_f32_16x16x32_bf16 v[148:151], v[68:71], v[176:179], v[148:151]\n\t" \
  "ds_read_b128 v[176:179], v212 offset:33024\n\t" \
  "s_add_u32 m0, %[lw], 34816\n\t" \
  "v_mfma_f32_16x16x32_bf16 v[152:155], v[64:67], v[180:183], v[152:155]\n\t" \
  "v_mfma_f32_16x16x32_bf16 v[156:159], v[68:71], v[180:183], v[156:159]\n\t" \
  "ds_read_b128 v[180:183], v212 offset:49408\n\t" \
  "global_load_lds_dwordx4 v218, s[92:93] sc1\n\t" \
  "s_waitcnt lgkmcnt(6)\n\t" \
  "v_mfma_f32_16x16x32_bf16 v[144:147], v[72:75], v[184:187], v[144:147]\n\t" \
  "v_mfma_f32_16x16x32_bf16 v[148:151], v[76:79], v[184:187], v[148:151]\n\t" \
  "ds_read_b128 v[184:187], v213 offset:33024\n\t" \
  "v_mfma_f32_16x16x32_bf16 v[152:155], v[72:75], v[188:191], v[152:155]\n\t" \
  "v_mfma_f32_16x16x32_bf16 v[156:159], v[76:79], v[188:191], v[156:159]\n\t" \
  "ds_read_b128 v[188:191], v213 offset:49408\n\t" \
  "s_waitcnt lgkmcnt(6)\n\t" \
  "v_mfma_f32_16x16x32_bf16 v[144:147], v[80:83], v[192:195], v[144:147]\n\t" \
  "v_mfma_f32_16x16x32_bf16 v[148:151], v[84:87], v[192:195], v[148:151]\n\t" \
  "ds_read_b128 v[192:195], v214 offset:33024\n\t" \
  "s_add_u32 m0, %[lw], 35840\n\t" \
  "v_mfma_f32_16x16x32_bf16 v[152:155], v[80:83], v[196:199], v[152:155]\n\t" \
  "v_mfma_f32_16x16x32_bf16 v[156:159], v[84:87], v[196:199], v[156:159]\n\t" \
  "ds_read_b128 v[196:199], v214 offset:49408\n\t" \
  "global_load_lds_dwordx4 v219, s[92:93] sc1\n\t" \
  "s_waitcnt lgkmcnt(6)\n\t" \
  "v_mfma_f32_16x16x32_bf16 v[144:147], v[88:91], v[200:203], v[144:147]\n\t" \
  "v_mfma_f32_16x16x32_bf16 v[148:151], v[92:95], v[200:203], v[148:151]\n\t" \
  "ds_read_b128 v[200:203], v215 offset:33024\n\t" \
  "v_mfma_f32_16x16x32_bf16 v[152:155], v[88:91], v[204:207], v[152:155]\n\t" \
  "v_mfma_f32_16x16x32_bf16 v[156:159], v[92:95], v[204:207], v[156:159]\n\t" \
  "ds_read_b128 v[204:207], v215 offset:49408\n\t" \
  "s_cmp_lt_u32 s86, 8\n\t" \
  "s_cbranch_scc1 .Lw30%=\n\t" \
  "s_waitcnt vmcnt(5)\n\t" \
  "s_barrier\n\t" \
  ".Lg30%=:\n\t" \
  "s_add_u32 s92, s92, 65024\n\t" \
  "s_addc_u32 s93, s93, 0\n\t" \
  "s_waitcnt lgkmcnt(6)\n\t" \
  "v_mfma_f32_16x16x32_bf16 v[144:147], v[96:99], v[176:179], v[144:147]\n\t" \
  "v_mfma_f32_16x16x32_bf16 v[148:151], v[100:103], v[176:179], v[148:151]\n\t" \
  "ds_read_b128 v[176:179], v208 offset:0\n\t" \
  "s_add_u32 m0, %[lw], 65536\n\t" \
  "v_mfma_f32_16x16x32_bf16 v[152:155], v[96:99], v[180:183], v[152:155]\n\t" \
  "v_mfma_f32_16x16x32_bf16 v[156:159], v[100:103], v[180:183], v[156:159]\n\t" \
  "ds_read_b128 v[180:183], v208 offset:16384\n\t" \
  "global_load_lds_dwordx4 v216, s[92:93] sc1\n\t" \
  "s_waitcnt lgkmcnt(6)\n\t" \
  "v_mfma_f32_16x16x32_bf16 v[144:147], v[104:107], v[184:187], v[144:147]\n\t" \
  "v_mfma_f32_16x16x32_bf16 v[148:151], v[108:111], v[184:187], v[148:151]\n\t" \
  "ds_read_b128 v[184:187], v209 offset:0\n\t" \
  "v_mfma_f32_16x16x32_bf16 v[152:155], v[104:107], v[188:191], v[152:155]\n\t" \
  "v_mfma_f32_16x16x32_bf16 v[156:159], v[108:111], v[188:191], v[156:159]\n\t" \
  "ds_read_b128 v[188:191], v209 offset:16384\n\t" \
  "s_waitcnt lgkmcnt(6)\n\t" \
  "v_mfma_f32_16x16x32_bf16 v[144:147], v[112:115], v[192:195], v[144:147]\n\t" \
  "v_mfma_f32_16x16x32_bf16 v[148:151], v[116:119], v[192:195], v[148:151]\n\t" \
  "ds_read_b128 v[192:195], v210 offset:0\n\t" \
  "s_add_u32 m0, %[lw], 66560\n\t" \
  "v_mfma_f32_16x16x32_bf16 v[152:155], v[112:115], v[196:199], v[152:155]\n\t" \
  "v_mfma_f32_16x16x32_bf16 v[156:159], v[116:119], v[196:199], v[156:159]\n\t" \
  "ds_read_b128 v[196:199], v210 offset:16384\n\t" \
  "global_load_lds_dwordx4 v217, s[92:93] sc1\n\t" \
  "s_waitcnt lgkmcnt(6)\n\t" \
  "v_mfma_f32_16x16x32_bf16 v[144:147], v[120:123], v[200:203], v[144:147]\n\t" \
  "v_mfma_f32_16x16x32_bf16 v[148:151], v[124:127], v[200:203], v[148:151]\n\t" \
  "ds_read_b128 v[200:203], v211 offset:0\n\t" \
  "v_mfma_f32_16x16x32_bf16 v[152:155], v[120:123], v[204:207], v[152:155]\n\t" \
  "v_mfma_f32_16x16x32_bf16 v[156:159], v[124:127], v[204:207], v[156:159]\n\t" \
  "ds_read_b128 v[204:207], v211 offset:16384\n\t" \
  "s_add_u32 s86, s86, 1\n\t" \
  "s_add_u32 s87, s87, 1\n\t" \
  "s_cmp_lt_u32 s86, %[end]\n\t" \
  "s_cbranch_scc1 .Lp0%=\n\t" \
  "s_branch .Lx0%=\n\t" \
  ".Lf00%=:\n\t" \
  "s_nop 7\n\t" \
  "ds_write_b128 v220, v[152:155] offset:0\n\t" \
  "ds_write_b128 v220, v[156:159] offset:1024\n\t" \
  "s_lshr_b32 s90, s87, 7\n\t" \
  "s_xor_b32 s90, s90, %[s0]\n\t" \
  "s_and_b32 s90, s90, 1\n\t" \
  "s_lshl1_add_u32 s90, s87, s90\n\t" \
  "s_lshl_b32 s90, s90, 13\n\t" \
  "s_cmp_lt_i32 s86, 1\n\t" \
  "s_cselect_b32 s90, 0x70000000, s90\n\t" \
  "s_waitcnt lgkmcnt(0)\n\t" \
  "s_barrier\n\t" \
  "ds_read_b128 v[160:163], v221 offset:0\n\t" \
  "ds_read_b128 v[164:167], v221 offset:1024\n\t" \
  "s_waitcnt lgkmcnt(0)\n\t" \
  "v_add_f32 v160, v160, v144\n\t" \
  "v_add_f32 v161, v161, v145\n\t" \
  "v_add_f32 v162, v162, v146\n\t" \
  "v_add_f32 v163, v163, v147\n\t" \
  "v_add_f32 v164, v164, v148\n\t" \
  "v_add_f32 v165, v165, v149\n\t" \
  "v_add_f32 v166, v166, v150\n\t" \
  "v_add_f32 v167, v167, v151\n\t" \
  "v_cvt_pk_bf16_f32 v168, v160, v161\n\t" \
  "v_cvt_pk_bf16_f32 v169, v162, v163\n\t" \
  "v_cvt_pk_bf16_f32 v170, v164, v165\n\t" \
  "v_cvt_pk_bf16_f32 v171, v166, v167\n\t" \
  "buffer_store_dwordx4 v[168:171], v172, %[rt], s90 offen sc1\n\t" \
  "s_branch .Lfb00%=\n\t" \
  ".Lf10%=:\n\t" \
  "s_nop 7\n\t" \
  "ds_write_b128 v220, v[136:139] offset:0\n\t" \
  "ds_write_b128 v220, v[140:143] offset:1024\n\t" \
  "s_lshr_b32 s90, s87, 7\n\t" \
  "s_xor_b32 s90, s90, %[s0]\n\t" \
  "s_and_b32 s90, s90, 1\n\t" \
  "s_lshl1_add_u32 s90, s87, s90\n\t" \
  "s_lshl_b32 s90, s90, 13\n\t" \
  "s_cmp_lt_i32 s86, 1\n\t" \
  "s_cselect_b32 s90, 0x70000000, s90\n\t" \
  "s_waitcnt lgkmcnt(0)\n\t" \
  "s_barrier\n\t" \
  "ds_read_b128 v[160:163], v221 offset:0\n\t" \
  "ds_read_b128 v[164:167], v221 offset:1024\n\t" \
  "s_waitcnt lgkmcnt(0)\n\t" \
  "v_add_f32 v160, v160, v128\n\t" \
  "v_add_f32 v161, v161, v129\n\t" \
  "v_add_f32 v162, v162, v130\n\t" \
  "v_add_f32 v163, v163, v131\n\t" \
  "v_add_f32 v164, v164, v132\n\t" \
  "v_add_f32 v165, v165, v133\n\t" \
  "v_add_f32 v166, v166, v134\n\t" \
  "v_add_f32 v167, v167, v135\n\t" \
  "v_cvt_pk_bf16_f32 v168, v160, v161\n\t" \
  "v_cvt_pk_bf16_f32 v169, v162, v163\n\t" \
  "v_cvt_pk_bf16_f32 v170, v164, v165\n\t" \
  "v_cvt_pk_bf16_f32 v171, v166, v167\n\t" \
  "buffer_store_dwordx4 v[168:171], v172, %[rt], s90 offen sc1\n\t" \
  "s_branch .Lfb10%=\n\t" \
  ".Lw00%=:\n\t" \
  "s_waitcnt vmcnt(2)\n\t" \
  "s_barrier\n\t" \
  "s_branch .Lg00%=\n\t" \
  ".Lw10%=:\n\t" \
  "s_waitcnt vmcnt(2)\n\t" \
  "s_barrier\n\t" \
  "s_branch .Lg10%=\n\t" \
  ".Lw20%=:\n\t" \
  "s_waitcnt vmcnt(2)\n\t" \
  "s_barrier\n\t" \
  "s_branch .Lg20%=\n\t" \
  ".Lw30%=:\n\t" \
  "s_waitcnt vmcnt(2)\n\t" \
  "s_barrier\n\t" \
  "s_branch .Lg30%=\n\t" \
  ".Lx0%=:\n\t" \
  "s_waitcnt lgkmcnt(0)\n\t" \
  "s_setprio 0\n\t" \
  "s_mov_b32 m0, s94\n\t" \
  "s_branch .Le%=\n\t" \
  ".Lk%=:\n\t" \
  "s_mov_b32 s94, m0\n\t" \
  "s_mov_b64 s[92:93], %[x0]\n\t" \
  "s_mov_b32 s86, %[g0]\n\t" \
  "s_add_u32 s87, s86, %[t0]\n\t" \
  "s_sub_i32 s88, s86, 3\n\t" \
  "ds_read_b128 v[176:179], v208 offset:0\n\t" \
  "ds_read_b128 v[180:183], v208 offset:16384\n\t" \
  "ds_read_b128 v[184:187], v209 offset:0\n\t" \
  "ds_read_b128 v[188:191], v209 offset:16384\n\t" \
  "ds_read_b128 v[192:195], v210 offset:0\n\t" \
  "ds_read_b128 v[196:199], v210 offset:16384\n\t" \
  "ds_read_b128 v[200:203], v211 offset:0\n\t" \
  "ds_read_b128 v[204:207], v211 offset:16384\n\t" \
  "s_branch .Lfb01%=\n\t" \
  ".Lp1%=:\n\t" \
  "s_cmp_le_u32 s86, 5\n\t" \
  "s_cbranch_scc1 .Lf01%=\n\t" \
  ".Lfb01%=:\n\t" \
  "s_waitcnt lgkmcnt(6)\n\t" \
  "v_mfma_f32_16x16x32_bf16 v[128:131], v[0:3], v[176:179], 0\n\t" \
  "v_mfma_f32_16x16x32_bf16 v[132:135], v[4:7], v[176:179], 0\n\t" \
  "ds_read_b128 v[176:179], v208 offset:256\n\t" \
  "v_mfma_f32_16x16x32_bf16 v[136:139], v[0:3], v[180:183], 0\n\t" \
  "v_mfma_f32_16x16x32_bf16 v[140:143], v[4:7], v[180:183], 0\n\t" \
  "ds_read_b128 v[180:183], v208 offset:16640\n\t" \
  "s_waitcnt lgkmcnt(6)\n\t" \
  "v_mfma_f32_16x16x32_bf16 v[128:131], v[8:11], v[184:187], v[128:131]\n\t" \
  "v_mfma_f32_16x16x32_bf16 v[132:135], v[12:15], v[184:187], v[132:135]\n\t" \
  "ds_read_b128 v[184:187], v209 offset:256\n\t" \
  "s_add_u32 m0, %[lw], 67584\n\t" \
  "v_mfma_f32_16x16x32_bf16 v[136:139], v[8:11], v[188:191], v[136:139]\n\t" \
  "v_mfma_f32_16x16x32_bf16 v[140:143], v[12:15], v[188:191], v[140:143]\n\t" \
  "ds_read_b128 v[188:191], v209 offset:16640\n\t" \
  "global_load_lds_dwordx4 v218, s[92:93] sc1\n\t" \
  "ds_write_b128 v220, v[152:155] offset:0\n\t" \
  "ds_write_b128 v220, v[156:159] offset:1024\n\t" \
  "s_waitcnt lgkmcnt(8)\n\t" \
  "v_mfma_f32_16x16x32_bf16 v[128:131], v[16:19], v[192:195], v[128:131]\n\t" \
  "v_mfma_f32_16x16x32_bf16 v[132:135], v[20:23], v[192:195], v[132:135]\n\t" \
  "ds_read_b128 v[192:195], v210 offset:256\n\t" \
  "v_mfma_f32_16x16x32_bf16 v[136:139], v[16:19], v[196:199], v[136:139]\n\t" \
  "v_mfma_f32_16x16x32_bf16 v[140:143], v[20:23], v[196:199], v[140:143]\n\t" \
  "ds_read_b128 v[196:199], v210 offset:16640\n\t" \
  "s_lshr_b32 s90, s87, 7\n\t" \
  "s_xor_b32 s90, s90, %[s0]\n\t" \
  "s_and_b32 s90, s90, 1\n\t" \
  "s_lshl1_add_u32 s90, s87, s90\n\t" \
  "s_lshl_b32 s90, s90, 13\n\t" \
  "s_cmp_lt_i32 s86, 6\n\t" \
  "s_cselect_b32 s90, 0x70000000, s90\n\t" \
  "s_waitcnt lgkmcnt(8)\n\t" \
  "v_mfma_f32_16x16x32_bf16 v[128:131], v[24:27], v[200:203], v[128:131]\n\t" \
  "v_mfma_f32_16x16x32_bf16 v[132:135], v[28:31], v[200:203], v[132:135]\n\t" \
  "ds_read_b128 v[200:203], v211 offset:256\n\t" \
  "s_add_u32 m0, %[lw], 68608\n\t" \
  "v_mfma_f32_16x16x32_bf16 v[136:139], v[24:27], v[204:207], v[136:139]\n\t" \
  "v_mfma_f32_16x16x32_bf16 v[140:143], v[28:31], v[204:207], v[140:143]\n\t" \
  "ds_read_b128 v[204:207], v211 offset:16640\n\t" \
  "global_load_lds_dwordx4 v219, s[92:93] sc1\n\t" \
  "s_waitcnt lgkmcnt(4)\n\t" \
  "s_cmp_lt_u32 s86, 6\n\t" \
  "s_cbranch_scc1 .Lw01%=\n\t" \
  "s_waitcnt vmcnt(4)\n\t" \
  "s_barrier\n\t" \
  ".Lg01%=:\n\t" \
  "s_add_u32 s92, s92, 512\n\t" \
  "s_addc_u32 s93, s93, 0\n\t" \
  "v_mfma_f32_16x16x32_bf16 v[128:131], v[32:35], v[176:179], v[128:131]\n\t" \
  "v_mfma_f32_16x16x32_bf16 v[132:135], v[36:39], v[176:179], v[132:135]\n\t" \
  "ds_read_b128 v[176:179], v208 offset:32768\n\t" \
  "v_mfma_f32_16x16x32_bf16 v[136:139], v[32:35], v[180:183], v[136:139]\n\t" \
  "v_mfma_f32_16x16x32_bf16 v[140:143], v[36:39], v[180:183], v[140:143]\n\t" \
  "ds_read_b128 v[180:183], v208 offset:49152\n\t" \
  "ds_read_b128 v[160:163], v221 offset:0\n\t" \
  "ds_read_b128 v[164:167], v221 offset:1024\n\t" \
  "s_sub_i32 s91, s86, 2\n\t" \
  "s_max_i32 s88, s88, s91\n\t" \
  "v_mov_b32 v173, s88\n\t" \
  "buffer_store_dword v173, v174, %[rs], 0 offen sc1\n\t" \
  "v_mfma_f32_16x16x32_bf16 v[128:131], v[40:43], v[184:187], v[128:131]\n\t" \
  "v_mfma_f32_16x16x32_bf16 v[132:135], v[44:47], v[184:187], v[132:135]\n\t" \
  "ds_read_b128 v[184:187], v209 offset:32768\n\t" \
  "s_add_u32 m0, %[lw], 98304\n\t" \
  "v_mfma_f32_16x16x32_bf16 v[136:139], v[40:43], v[188:191], v[136:139]\n\t" \
  "v_mfma_f32_16x16x32_bf16 v[140:143], v[44:47], v[188:191], v[140:143]\n\t" \
  "ds_read_b128 v[188:191], v209 offset:49152\n\t" \
  "global_load_lds_dwordx4 v216, s[92:93] sc1\n\t" \
  "s_waitcnt lgkmcnt(8)\n\t" \
  "v_mfma_f32_16x16x32_bf16 v[128:131], v[48:51], v[192:195], v[128:131]\n\t" \
  "v_mfma_f32_16x16x32_bf16 v[132:135], v[52:55], v[192:195], v[132:135]\n\t" \
  "ds_read_b128 v[192:195], v210 offset:32768\n\t" \
  "v_mfma_f32_16x16x32_bf16 v[136:139], v[48:51], v[196:199], v[136:139]\n\t" \
  "v_mfma_f32_16x16x32_bf16 v[140:143], v[52:55], v[196:199], v[140:143]\n\t" \
  "ds_read_b128 v[196:199], v210 offset:49152\n\t" \
  "s_waitcnt lgkmcnt(4)\n\t" \
  "v_add_f32 v160, v160, v144\n\t" \
  "v_add_f32 v161, v161, v145\n\t" \
  "v_add_f32 v162, v162, v146\n\t" \
  "v_add_f32 v163, v163, v147\n\t" \
  "v_add_f32 v164, v164, v148\n\t" \
  "v_add_f32 v165, v165, v149\n\t" \
  "v_add_f32 v166, v166, v150\n\t" \
  "v_add_f32 v167, v167, v151\n\t" \
  "v_mfma_f32_16x16x32_bf16 v[128:131], v[56:59], v[200:203], v[128:131]\n\t" \
  "v_mfma_f32_16x16x32_bf16 v[132:135], v[60:63], v[200:203], v[132:135]\n\t" \
  "ds_read_b128 v[200:203], v211 offset:32768\n\t" \
  "s_add_u32 m0, %[lw], 99328\n\t" \
  "v_mfma_f32_16x16x32_bf16 v[136:139], v[56:59], v[204:207], v[136:139]\n\t" \
  "v_mfma_f32_16x16x32_bf16 v[140:143], v[60:63], v[204:207], v[140:143]\n\t" \
  "ds_read_b128 v[204:207], v211 offset:49152\n\t" \
  "global_load_lds_dwordx4 v217, s[92:93] sc1\n\t" \
  "v_cvt_pk_bf16_f32 v168, v160, v161\n\t" \
  "v_cvt_pk_bf16_f32 v169, v162, v163\n\t" \
  "v_cvt_pk_bf16_f32 v170, v164, v165\n\t" \
  "v_cvt_pk_bf16_f32 v171, v166, v167\n\t" \
  "buffer_store_dwordx4 v[168:171], v172, %[rt], s90 offen sc1\n\t" \
  "v_mfma_f32_16x16x32_bf16 v[128:131], v[64:67], v[176:179], v[128:131]\n\t" \
  "v_mfma_f32_16x16x32_bf16 v[132:135], v[68:71], v[176:179], v[132:135]\n\t" \
  "ds_read_b128 v[176:179], v208 offset:33024\n\t" \
  "v_mfma_f32_16x16x32_bf16 v[136:139], v[64:67], v[180:183], v[136:139]\n\t" \
  "v_mfma_f32_16x16x32_bf16 v[140:143], v[68:71], v[180:183], v[140:143]\n\t" \
  "ds_read_b128 v[180:183], v208 offset:49408\n\t" \
  "s_waitcnt lgkmcnt(6)\n\t" \
  "v_mfma_f32_16x16x32_bf16 v[128:131], v[72:75], v[184:187], v[128:131]\n\t" \
  "v_mfma_f32_16x16x32_bf16 v[132:135], v[76:79], v[184:187], v[132:135]\n\t" \
  "ds_read_b128 v[184:187], v209 offset:33024\n\t" \
  "s_add_u32 m0, %[lw], 100352\n\t" \
  "v_mfma_f32_16x16x32_bf16 v[136:139], v[72:75], v[188:191], v[136:139]\n\t" \
  "v_mfma_f32_16x16x32_bf16 v[140:143], v[76:79], v[188:191], v[140:143]\n\t" \
  "ds_read_b128 v[188:191], v209 offset:49408\n\t" \
  "global_load_lds_dwordx4 v218, s[92:93] sc1\n\t" \
  "s_waitcnt lgkmcnt(6)\n\t" \
  "v_mfma_f32_16x16x32_bf16 v[128:131], v[80:83], v[192:195], v[128:131]\n\t" \
  "v_mfma_f32_16x16x32_bf16 v[132:135], v[84:87], v[192:195], v[132:135]\n\t" \
  "ds_read_b128 v[192:195], v210 offset:33024\n\t" \
  "v_mfma_f32_16x16x32_bf16 v[136:139], v[80:83], v[196:199], v[136:139]\n\t" \
  "v_mfma_f32_16x16x32_bf16 v[140:143], v[84:87], v[196:199], v[140:143]\n\t" \
  "ds_read_b128 v[196:199], v210 offset:49408\n\t" \
  "s_waitcnt lgkmcnt(6)\n\t" \
  "v_mfma_f32_16x16x32_bf16 v[128:131], v[88:91], v[200:203], v[128:131]\n\t" \
  "v_mfma_f32_16x16x32_bf16 v[132:135], v[92:95], v[200:203], v[132:135]\n\t" \
  "ds_read_b128 v[200:203], v211 offset:33024\n\t" \
  "s_add_u32 m0, %[lw], 101376\n\t" \
  "v_mfma_f32_16x16x32_bf16 v[136:139], v[88:91], v[204:207], v[136:139]\n\t" \
  "v_mfma_f32_16x16x32_bf16 v[140:143], v[92:95], v[204:207], v[140:143]\n\t" \
  "ds_read_b128 v[204:207], v211 offset:49408\n\t" \
  "global_load_lds_dwordx4 v219, s[92:93] sc1\n\t" \
  "s_cmp_lt_u32 s86, 8\n\t" \
  "s_cbranch_scc1 .Lw11%=\n\t" \
  "s_waitcnt vmcnt(6)\n\t" \
  "s_barrier\n\t" \
  ".Lg11%=:\n\t" \
  "s_add_u32 s92, s92, 65024\n\t" \
  "s_addc_u32 s93, s93, 0\n\t" \
  "s_waitcnt lgkmcnt(6)\n\t" \
  "v_mfma_f32_16x16x32_bf16 v[128:131], v[96:99], v[176:179], v[128:131]\n\t" \
  "v_mfma_f32_16x16x32_bf16 v[132:135], v[100:103], v[176:179], v[132:135]\n\t" \
  "ds_read_b128 v[176:179], v212 offset:0\n\t" \
  "v_mfma_f32_16x16x32_bf16 v[136:139], v[96:99], v[180:183], v[136:139]\n\t" \
  "v_mfma_f32_16x16x32_bf16 v[140:143], v[100:103], v[180:183], v[140:143]\n\t" \
  "ds_read_b128 v[180:183], v212 offset:16384\n\t" \
  "s_waitcnt lgkmcnt(6)\n\t" \
  "v_mfma_f32_16x16x32_bf16 v[128:131], v[104:107], v[184:187], v[128:131]\n\t" \
  "v_mfma_f32_16x16x32_bf16 v[132:135], v[108:111], v[184:187], v[132:135]\n\t" \
  "ds_read_b128 v[184:187], v213 offset:0\n\t" \
  "s_add_u32 m0, %[lw], 0\n\t" \
  "v_mfma_f32_16x16x32_bf16 v[136:139], v[104:107], v[188:191], v[136:139]\n\t" \
  "v_mfma_f32_16x16x32_bf16 v[140:143], v[108:111], v[188:191], v[140:143]\n\t" \
  "ds_read_b128 v[188:191], v213 offset:16384\n\t" \
  "global_load_lds_dwordx4 v216, s[92:93] sc1\n\t" \
  "s_waitcnt lgkmcnt(6)\n\t" \
  "v_mfma_f32_16x16x32_bf16 v[128:131], v[112:115], v[192:195], v[128:131]\n\t" \
  "v_mfma_f32_16x16x32_bf16 v[132:135], v[116:119], v[192:195], v[132:135]\n\t" \
  "ds_read_b128 v[192:195], v214 offset:0\n\t" \
  "v_mfma_f32_16x16x32_bf16 v[136:139], v[112:115], v[196:199], v[136:139]\n\t" \
  "v_mfma_f32_16x16x32_bf16 v[140:143], v[116:119], v[196:199], v[140:143]\n\t" \
  "ds_read_b128 v[196:199], v214 offset:16384\n\t" \
  "s_waitcnt lgkmcnt(6)\n\t" \
  "v_mfma_f32_16x16x32_bf16 v[128:131], v[120:123], v[200:203], v[128:131]\n\t" \
  "v_mfma_f32_16x16x32_bf16 v[132:135], v[124:127], v[200:203], v[132:135]\n\t" \
  "ds_read_b128 v[200:203], v215 offset:0\n\t" \
  "s_add_u32 m0, %[lw], 1024\n\t" \
  "v_mfma_f32_16x16x32_bf16 v[136:139], v[120:123], v[204:207], v[136:139]\n\t" \
  "v_mfma_f32_16x16x32_bf16 v[140:143], v[124:127], v[204:207], v[140:143]\n\t" \
  "ds_read_b128 v[204:207], v215 offset:16384\n\t" \
  "global_load_lds_dwordx4 v217, s[92:93] sc1\n\t" \
  "s_add_u32 s86, s86, 1\n\t" \
  "s_add_u32 s87, s87, 1\n\t" \
  "s_cmp_le_u32 s86, 5\n\t" \
  "s_cbranch_scc1 .Lf11%=\n\t" \
  ".Lfb11%=:\n\t" \
  "s_waitcnt lgkmcnt(6)\n\t" \
  "v_mfma_f32_16x16x32_bf16 v[144:147], v[0:3], v[176:179], 0\n\t" \
  "v_mfma_f32_16x16x32_bf16 v[148:151], v[4:7], v[176:179], 0\n\t" \
  "ds_read_b128 v[176:179], v212 offset:256\n\t" \
  "v_mfma_f32_16x16x32_bf16 v[152:155], v[0:3], v[180:183], 0\n\t" \
  "v_mfma_f32_16x16x32_bf16 v[156:159], v[4:7], v[180:183], 0\n\t" \
  "ds_read_b128 v[180:183], v212 offset:16640\n\t" \
  "s_waitcnt lgkmcnt(6)\n\t" \
  "v_mfma_f32_16x16x32_bf16 v[144:147], v[8:11], v[184:187], v[144:147]\n\t" \
  "v_mfma_f32_16x16x32_bf16 v[148:151], v[12:15], v[184:187], v[148:151]\n\t" \
  "ds_read_b128 v[184:187], v213 offset:256\n\t" \
  "s_add_u32 m0, %[lw], 2048\n\t" \
  "v_mfma_f32_16x16x32_bf16 v[152:155], v[8:11], v[188:191], v[152:155]\n\t" \
  "v_mfma_f32_16x16x32_bf16 v[156:159], v[12:15], v[188:191], v[156:159]\n\t" \
  "ds_read_b128 v[188:191], v213 offset:16640\n\t" \
  "global_load_lds_dwordx4 v218, s[92:93] sc1\n\t" \
  "ds_write_b128 v220, v[136:139] offset:0\n\t" \
  "ds_write_b128 v220, v[140:143] offset:1024\n\t" \
  "s_waitcnt lgkmcnt(8)\n\t" \
  "v_mfma_f32_16x16x32_bf16 v[144:147], v[16:19], v[192:195], v[144:147]\n\t" \
  "v_mfma_f32_16x16x32_bf16 v[148:151], v[20:23], v[192:195], v[148:151]\n\t" \
  "ds_read_b128 v[192:195], v214 offset:256\n\t" \
  "v_mfma_f32_16x16x32_bf16 v[152:155], v[16:19], v[196:199], v[152:155]\n\t" \
  "v_mfma_f32_16x16x32_bf16 v[156:159], v[20:23], v[196:199], v[156:159]\n\t" \
  "ds_read_b128 v[196:199], v214 offset:16640\n\t" \
  "s_lshr_b32 s90, s87, 7\n\t" \
  "s_xor_b32 s90, s90, %[s0]\n\t" \
  "s_and_b32 s90, s90, 1\n\t" \
  "s_lshl1_add_u32 s90, s87, s90\n\t" \
  "s_lshl_b32 s90, s90, 13\n\t" \
  "s_cmp_lt_i32 s86, 6\n\t" \
  "s_cselect_b32 s90, 0x70000000, s90\n\t" \
  "s_waitcnt lgkmcnt(8)\n\t" \
  "v_mfma_f32_16x16x32_bf16 v[144:147], v[24:27], v[200:203], v[144:147]\n\t" \
  "v_mfma_f32_16x16x32_bf16 v[148:151], v[28:31], v[200:203], v[148:151]\n\t" \
  "ds_read_b128 v[200:203], v215 offset:256\n\t" \
  "s_add_u32 m0, %[lw], 3072\n\t" \
  "v_mfma_f32_16x16x32_bf16 v[152:155], v[24:27], v[204:207], v[152:155]\n\t" \
  "v_mfma_f32_16x16x32_bf16 v[156:159], v[28:31], v[204:207], v[156:159]\n\t" \
  "ds_read_b128 v[204:207], v215 offset:16640\n\t" \
  "global_load_lds_dwordx4 v219, s[92:93] sc1\n\t" \
  "s_waitcnt lgkmcnt(4)\n\t" \
  "s_cmp_lt_u32 s86, 6\n\t" \
  "s_cbranch_scc1 .Lw21%=\n\t" \
  "s_waitcnt vmcnt(4)\n\t" \
  "s_barrier\n\t" \
  ".Lg21%=:\n\t" \
  "s_add_u32 s92, s92, 512\n\t" \
  "s_addc_u32 s93, s93, 0\n\t" \
  "v_mfma_f32_16x16x32_bf16 v[144:147], v[32:35], v[176:179], v[144:147]\n\t" \
  "v_mfma_f32_16x16x32_bf16 v[148:151], v[36:39], v[176:179], v[148:151]\n\t" \
  "ds_read_b128 v[176:179], v212 offset:32768\n\t" \
  "v_mfma_f32_16x16x32_bf16 v[152:155], v[32:35], v[180:183], v[152:155]\n\t" \
  "v_mfma_f32_16x16x32_bf16 v[156:159], v[36:39], v[180:183], v[156:159]\n\t" \
  "ds_read_b128 v[180:183], v212 offset:49152\n\t" \
  "ds_read_b128 v[160:163], v221 offset:0\n\t" \
  "ds_read_b128 v[164:167], v221 offset:1024\n\t" \
  "s_sub_i32 s91, s86, 2\n\t" \
  "s_max_i32 s88, s88, s91\n\t" \
  "v_mov_b32 v173, s88\n\t" \
  "buffer_store_dword v173, v174, %[rs], 0 offen sc1\n\t" \
  "v_mfma_f32_16x16x32_bf16 v[144:147], v[40:43], v[184:187], v[144:147]\n\t" \
  "v_mfma_f32_16x16x32_bf16 v[148:151], v[44:47], v[184:187], v[148:151]\n\t" \
  "ds_read_b128 v[184:187], v213 offset:32768\n\t" \
  "s_add_u32 m0, %[lw], 32768\n\t" \
  "v_mfma_f32_16x16x32_bf16 v[152:155], v[40:43], v[188:191], v[152:155]\n\t" \
  "v_mfma_f32_16x16x32_bf16 v[156:159], v[44:47], v[188:191], v[156:159]\n\t" \
  "ds_read_b128 v[188:191], v213 offset:49152\n\t" \
  "global_load_lds_dwordx4 v216, s[92:93] sc1\n\t" \
  "s_waitcnt lgkmcnt(8)\n\t" \
  "v_mfma_f32_16x16x32_bf16 v[144:147], v[48:51], v[192:195], v[144:147]\n\t" \
  "v_mfma_f32_16x16x32_bf16 v[148:151], v[52:55], v[192:195], v[148:151]\n\t" \
  "ds_read_b128 v[192:195], v214 offset:32768\n\t" \
  "v_mfma_f32_16x16x32_bf16 v[152:155], v[48:51], v[196:199], v[152:155]\n\t" \
  "v_mfma_f32_16x16x32_bf16 v[156:159], v[52:55], v[196:199], v[156:159]\n\t" \
  "ds_read_b128 v[196:199], v214 offset:49152\n\t" \
  "s_waitcnt lgkmcnt(4)\n\t" \
  "v_add_f32 v160, v160, v128\n\t" \
  "v_add_f32 v161, v161, v129\n\t" \
  "v_add_f32 v162, v162, v130\n\t" \
  "v_add_f32 v163, v163, v131\n\t" \
  "v_add_f32 v164, v164, v132\n\t" \
  "v_add_f32 v165, v165, v133\n\t" \
  "v_add_f32 v166, v166, v134\n\t" \
  "v_add_f32 v167, v167, v135\n\t" \
  "v_mfma_f32_16x16x32_bf16 v[144:147], v[56:59], v[200:203], v[144:147]\n\t" \
  "v_mfma_f32_16x16x32_bf16 v[148:151], v[60:63], v[200:203], v[148:151]\n\t" \
  "ds_read_b128 v[200:203], v215 offset:32768\n\t" \
  "s_add_u32 m0, %[lw], 33792\n\t" \
  "v_mfma_f32_16x16x32_bf16 v[152:155], v[56:59], v[204:207], v[152:155]\n\t" \
  "v_mfma_f32_16x16x32_bf16 v[156:159], v[60:63], v[204:207], v[156:159]\n\t" \
  "ds_read_b128 v[204:207], v215 offset:49152\n\t" \
  "global_load_lds_dwordx4 v217, s[92:93] sc1\n\t" \
  "v_cvt_pk_bf16_f32 v168, v160, v161\n\t" \
  "v_cvt_pk_bf16_f32 v169, v162, v163\n\t" \
  "v_cvt_pk_bf16_f32 v170, v164, v165\n\t" \
  "v_cvt_pk_bf16_f32 v171, v166, v167\n\t" \
  "buffer_store_dwordx4 v[168:171], v172, %[rt], s90 offen sc1\n\t" \
  "v_mfma_f32_16x16x32_bf16 v[144:147], v[64:67], v[176:179], v[144:147]\n\t" \
  "v_mfma_f32_16x16x32_bf16 v[148:151], v[68:71], v[176:179], v[148:151]\n\t" \
  "ds_read_b128 v[176:179], v212 offset:33024\n\t" \
  "v_mfma_f32_16x16x32_bf16 v[152:155], v[64:67], v[180:183], v[152:155]\n\t" \
  "v_mfma_f32_16x16x32_bf16 v[156:159], v[68:71], v[180:183], v[156:159]\n\t" \
  "ds_read_b128 v[180:183], v212 offset:49408\n\t" \
  "s_waitcnt lgkmcnt(6)\n\t" \
  "v_mfma_f32_16x16x32_bf16 v[144:147], v[72:75], v[184:187], v[144:147]\n\t" \
  "v_mfma_f32_16x16x32_bf16 v[148:151], v[76:79], v[184:187], v[148:151]\n\t" \
  "ds_read_b128 v[184:187], v213 offset:33024\n\t" \
  "s_add_u32 m0, %[lw], 34816\n\t" \
  "v_mfma_f32_16x16x32_bf16 v[152:155], v[72:75], v[188:191], v[152:155]\n\t" \
  "v_mfma_f32_16x16x32_bf16 v[156:159], v[76:79], v[188:191], v[156:159]\n\t" \
  "ds_read_b128 v[188:191], v213 offset:49408\n\t" \
  "global_load_lds_dwordx4 v218, s[92:93] sc1\n\t" \
  "s_waitcnt lgkmcnt(6)\n\t" \
  "v_mfma_f32_16x16x32_bf16 v[144:147], v[80:83], v[192:195], v[144:147]\n\t" \
  "v_mfma_f32_16x16x32_bf16 v[148:151], v[84:87], v[192:195], v[148:151]\n\t" \
  "ds_read_b128 v[192:195], v214 offset:33024\n\t" \
  "v_mfma_f32_16x16x32_bf16 v[152:155], v[80:83], v[196:199], v[152:155]\n\t" \
  "v_mfma_f32_16x16x32_bf16 v[156:159], v[84:87], v[196:199], v[156:159]\n\t" \
  "ds_read_b128 v[196:199], v214 offset:49408\n\t" \
  "s_waitcnt lgkmcnt(6)\n\t" \
  "v_mfma_f32_16x16x32_bf16 v[144:147], v[88:91], v[200:203], v[144:147]\n\t" \
  "v_mfma_f32_16x16x32_bf16 v[148:151], v[92:95], v[200:203], v[148:151]\n\t" \
  "ds_read_b128 v[200:203], v215 offset:33024\n\t" \
  "s_add_u32 m0, %[lw], 35840\n\t" \
  "v_mfma_f32_16x16x32_bf16 v[152:155], v[88:91], v[204:207], v[152:155]\n\t" \
  "v_mfma_f32_16x16x32_bf16 v[156:159], v[92:95], v[204:207], v[156:159]\n\t" \
  "ds_read_b128 v[204:207], v215 offset:49408\n\t" \
  "global_load_lds_dwordx4 v219, s[92:93] sc1\n\t" \
  "s_cmp_lt_u32 s86, 8\n\t" \
  "s_cbranch_scc1 .Lw31%=\n\t" \
  "s_waitcnt vmcnt(6)\n\t" \
  "s_barrier\n\t" \
  ".Lg31%=:\n\t" \
  "s_add_u32 s92, s92, 65024\n\t" \
  "s_addc_u32 s93, s93, 0\n\t" \
  "s_waitcnt lgkmcnt(6)\n\t" \
  "v_mfma_f32_16x16x32_bf16 v[144:147], v[96:99], v[176:179], v[144:147]\n\t" \
  "v_mfma_f32_16x16x32_bf16 v[148:151], v[100:103], v[176:179], v[148:151]\n\t" \
  "ds_read_b128 v[176:179], v208 offset:0\n\t" \
  "v_mfma_f32_16x16x32_bf16 v[152:155], v[96:99], v[180:183], v[152:155]\n\t" \
  "v_mfma_f32_16x16x32_bf16 v[156:159], v[100:103], v[180:183], v[156:159]\n\t" \
  "ds_read_b128 v[180:183], v208 offset:16384\n\t" \
  "s_waitcnt lgkmcnt(6)\n\t" \
  "v_mfma_f32_16x16x32_bf16 v[144:147], v[104:107], v[184:187], v[144:147]\n\t" \
  "v_mfma_f32_16x16x32_bf16 v[148:151], v[108:111], v[184:187], v[148:151]\n\t" \
  "ds_read_b128 v[184:187], v209 offset:0\n\t" \
  "s_add_u32 m0, %[lw], 65536\n\t" \
  "v_mfma_f32_16x16x32_bf16 v[152:155], v[104:107], v[188:191], v[152:155]\n\t" \
  "v_mfma_f32_16x16x32_bf16 v[156:159], v[108:111], v[188:191], v[156:159]\n\t" \
  "ds_read_b128 v[188:191], v209 offset:16384\n\t" \
  "global_load_lds_dwordx4 v216, s[92:93] sc1\n\t" \
  "s_waitcnt lgkmcnt(6)\n\t" \
  "v_mfma_f32_16x16x32_bf16 v[144:147], v[112:115], v[192:195], v[144:147]\n\t" \
  "v_mfma_f32_16x16x32_bf16 v[148:151], v[116:119], v[192:195], v[148:151]\n\t" \
  "ds_read_b128 v[192:195], v210 offset:0\n\t" \
  "v_mfma_f32_16x16x32_bf16 v[152:155], v[112:115], v[196:199], v[152:155]\n\t" \
  "v_mfma_f32_16x16x32_bf16 v[156:159], v[116:119], v[196:199], v[156:159]\n\t" \
  "ds_read_b128 v[196:199], v210 offset:16384\n\t" \
  "s_waitcnt lgkmcnt(6)\n\t" \
  "v_mfma_f32_16x16x32_bf16 v[144:147], v[120:123], v[200:203], v[144:147]\n\t" \
  "v_mfma_f32_16x16x32_bf16 v[148:151], v[124:127], v[200:203], v[148:151]\n\t" \
  "ds_read_b128 v[200:203], v211 offset:0\n\t" \
  "s_add_u32 m0, %[lw], 66560\n\t" \
  "v_mfma_f32_16x16x32_bf16 v[152:155], v[120:123], v[204:207], v[152:155]\n\t" \
  "v_mfma_f32_16x16x32_bf16 v[156:159], v[124:127], v[204:207], v[156:159]\n\t" \
  "ds_read_b128 v[204:207], v211 offset:16384\n\t" \
  "global_load_lds_dwordx4 v217, s[92:93] sc1\n\t" \
  "s_add_u32 s86, s86, 1\n\t" \
  "s_add_u32 s87, s87, 1\n\t" \
  "s_cmp_lt_u32 s86, %[end]\n\t" \
  "s_cbranch_scc1 .Lp1%=\n\t" \
  "s_branch .Lx1%=\n\t" \
  ".Lf01%=:\n\t" \
  "s_nop 7\n\t" \
  "ds_write_b128 v220, v[152:155] offset:0\n\t" \
  "ds_write_b128 v220, v[156:159] offset:1024\n\t" \
  "s_lshr_b32 s90, s87, 7\n\t" \
  "s_xor_b32 s90, s90, %[s0]\n\t" \
  "s_and_b32 s90, s90, 1\n\t" \
  "s_lshl1_add_u32 s90, s87, s90\n\t" \
  "s_lshl_b32 s90, s90, 13\n\t" \
  "s_cmp_lt_i32 s86, 1\n\t" \
  "s_cselect_b32 s90, 0x70000000, s90\n\t" \
  "s_waitcnt lgkmcnt(0)\n\t" \
  "s_barrier\n\t" \
  "ds_read_b128 v[160:163], v221 offset:0\n\t" \
  "ds_read_b128 v[164:167], v221 offset:1024\n\t" \
  "s_waitcnt lgkmcnt(0)\n\t" \
  "v_add_f32 v160, v160, v144\n\t" \
  "v_add_f32 v161, v161, v145\n\t" \
  "v_add_f32 v162, v162, v146\n\t" \
  "v_add_f32 v163, v163, v147\n\t" \
  "v_add_f32 v164, v164, v148\n\t" \
  "v_add_f32 v165, v165, v149\n\t" \
  "v_add_f32 v166, v166, v150\n\t" \
  "v_add_f32 v167, v167, v151\n\t" \
  "v_cvt_pk_bf16_f32 v168, v160, v161\n\t" \
  "v_cvt_pk_bf16_f32 v169, v162, v163\n\t" \
  "v_cvt_pk_bf16_f32 v170, v164, v165\n\t" \
  "v_cvt_pk_bf16_f32 v171, v166, v167\n\t" \
  "buffer_store_dwordx4 v[168:171], v172, %[rt], s90 offen sc1\n\t" \
  "s_branch .Lfb01%=\n\t" \
  ".Lf11%=:\n\t" \
  "s_nop 7\n\t" \
  "ds_write_b128 v220, v[136:139] offset:0\n\t" \
  "ds_write_b128 v220, v[140:143] offset:1024\n\t" \
  "s_lshr_b32 s90, s87, 7\n\t" \
  "s_xor_b32 s90, s90, %[s0]\n\t" \
  "s_and_b32 s90, s90, 1\n\t" \
  "s_lshl1_add_u32 s90, s87, s90\n\t" \
  "s_lshl_b32 s90, s90, 13\n\t" \
  "s_cmp_lt_i32 s86, 1\n\t" \
  "s_cselect_b32 s90, 0x70000000, s90\n\t" \
  "s_waitcnt lgkmcnt(0)\n\t" \
  "s_barrier\n\t" \
  "ds_read_b128 v[160:163], v221 offset:0\n\t" \
  "ds_read_b128 v[164:167], v221 offset:1024\n\t" \
  "s_waitcnt lgkmcnt(0)\n\t" \
  "v_add_f32 v160, v160, v128\n\t" \
  "v_add_f32 v161, v161, v129\n\t" \
  "v_add_f32 v162, v162, v130\n\t" \
  "v_add_f32 v163, v163, v131\n\t" \
  "v_add_f32 v164, v164, v132\n\t" \
  "v_add_f32 v165, v165, v133\n\t" \
  "v_add_f32 v166, v166, v134\n\t" \
  "v_add_f32 v167, v167, v135\n\t" \
  "v_cvt_pk_bf16_f32 v168, v160, v161\n\t" \
  "v_cvt_pk_bf16_f32 v169, v162, v163\n\t" \
  "v_cvt_pk_bf16_f32 v170, v164, v165\n\t" \
  "v_cvt_pk_bf16_f32 v171, v166, v167\n\t" \
  "buffer_store_dwordx4 v[168:171], v172, %[rt], s90 offen sc1\n\t" \
  "s_branch .Lfb11%=\n\t" \
  ".Lw01%=:\n\t" \
  "s_waitcnt vmcnt(2)\n\t" \
  "s_barrier\n\t" \
  "s_sub_i32 s91, s86, 1\n\t" \
  "s_max_i32 s88, s88, s91\n\t" \
  "v_mov_b32 v173, s88\n\t" \
  "buffer_store_dword v173, v174, %[rs], 0 offen sc1\n\t" \
  "s_branch .Lg01%=\n\t" \
  ".Lw11%=:\n\t" \
  "s_waitcnt vmcnt(2)\n\t" \
  "s_barrier\n\t" \
  "s_sub_i32 s91, s86, 1\n\t" \
  "s_max_i32 s88, s88, s91\n\t" \
  "v_mov_b32 v173, s88\n\t" \
  "buffer_store_dword v173, v174, %[rs], 0 offen sc1\n\t" \
  "s_branch .Lg11%=\n\t" \
  ".Lw21%=:\n\t" \
  "s_waitcnt vmcnt(2)\n\t" \
  "s_barrier\n\t" \
  "s_sub_i32 s91, s86, 1\n\t" \
  "s_max_i32 s88, s88, s91\n\t" \
  "v_mov_b32 v173, s88\n\t" \
  "buffer_store_dword v173, v174, %[rs], 0 offen sc1\n\t" \
  "s_branch .Lg21%=\n\t" \
  ".Lw31%=:\n\t" \
  "s_waitcnt vmcnt(2)\n\t" \
  "s_barrier\n\t" \
  "s_sub_i32 s91, s86, 1\n\t" \
  "s_max_i32 s88, s88, s91\n\t" \
  "v_mov_b32 v173, s88\n\t" \
  "buffer_store_dword v173, v174, %[rs], 0 offen sc1\n\t" \
  "s_branch .Lg31%=\n\t" \
  ".Lx1%=:\n\t" \
  "s_waitcnt lgkmcnt(0)\n\t" \
  "s_setprio 0\n\t" \
  "s_mov_b32 m0, s94\n\t" \
  ".Le%=:\n\t"
#define PR_ASM_1 \
  "s_cmp_lg_u32 %[kh], 0\n\t" \
  "s_cbranch_scc1 .Lk%=\n\t" \
  "s_mov_b32 s94, m0\n\t" \
  "s_mov_b64 s[92:93], %[x0]\n\t" \
  "s_mov_b32 s86, %[g0]\n\t" \
  "s_add_u32 s87, s86, %[t0]\n\t" \
  "s_sub_i32 s88, s86, 3\n\t" \
  "ds_read_b128 v[176:179], v208 offset:0\n\t" \
  "ds_read_b128 v[180:183], v208 offset:16384\n\t" \
  "ds_read_b128 v[184:187], v209 offset:0\n\t" \
  "ds_read_b128 v[188:191], v209 offset:16384\n\t" \
  "ds_read_b128 v[192:195], v210 offset:0\n\t" \
  "ds_read_b128 v[196:199], v210 offset:16384\n\t" \
  "ds_read_b128 v[200:203], v211 offset:0\n\t" \
  "ds_read_b128 v[204:207], v211 offset:16384\n\t" \
  "s_branch .Lfb00%=\n\t" \
  ".Lp0%=:\n\t" \
  "s_cmp_le_u32 s86, 5\n\t" \
  "s_cbranch_scc1 .Lf00%=\n\t" \
  ".Lfb00%=:\n\t" \
  "s_waitcnt lgkmcnt(6)\n\t" \
  "v_mfma_f32_16x16x32_bf16 v[128:131], v[176:179], v[0:3], 0\n\t" \
  "v_mfma_f32_16x16x32_bf16 v[136:139], v[176:179], v[4:7], 0\n\t" \
  "ds_read_b128 v[176:179], v208 offset:256\n\t" \
  "s_add_u32 m0, %[lw], 67584\n\t" \
  "v_mfma_f32_16x16x32_bf16 v[132:135], v[180:183], v[0:3], 0\n\t" \
  "v_mfma_f32_16x16x32_bf16 v[140:143], v[180:183], v[4:7], 0\n\t" \
  "ds_read_b128 v[180:183], v208 offset:16640\n\t" \
  "global_load_lds_dwordx4 v218, s[92:93] sc1\n\t" \
  "s_waitcnt lgkmcnt(6)\n\t" \
  "v_mfma_f32_16x16x32_bf16 v[128:131], v[184:187], v[8:11], v[128:131]\n\t" \
  "v_mfma_f32_16x16x32_bf16 v[136:139], v[184:187], v[12:15], v[136:139]\n\t" \
  "ds_read_b128 v[184:187], v209 offset:256\n\t" \
  "v_mfma_f32_16x16x32_bf16 v[132:135], v[188:191], v[8:11], v[132:135]\n\t" \
  "v_mfma_f32_16x16x32_bf16 v[140:143], v[188:191], v[12:15], v[140:143]\n\t" \
  "ds_read_b128 v[188:191], v209 offset:16640\n\t" \
  "ds_write_b128 v220, v[152:155] offset:0\n\t" \
  "ds_write_b128 v220, v[156:159] offset:1024\n\t" \
  "s_waitcnt lgkmcnt(8)\n\t" \
  "v_mfma_f32_16x16x32_bf16 v[128:131], v[192:195], v[16:19], v[128:131]\n\t" \
  "v_mfma_f32_16x16x32_bf16 v[136:139], v[192:195], v[20:23], v[136:139]\n\t" \
  "ds_read_b128 v[192:195], v210 offset:256\n\t" \
  "s_add_u32 m0, %[lw], 68608\n\t" \
  "v_mfma_f32_16x16x32_bf16 v[132:135], v[196:199], v[16:19], v[132:135]\n\t" \
  "v_mfma_f32_16x16x32_bf16 v[140:143], v[196:199], v[20:23], v[140:143]\n\t" \
  "ds_read_b128 v[196:199], v210 offset:16640\n\t" \
  "global_load_lds_dwordx4 v219, s[92:93] sc1\n\t" \
  "s_lshr_b32 s90, s87, 7\n\t" \
  "s_xor_b32 s90, s90, %[s0]\n\t" \
  "s_and_b32 s90, s90, 1\n\t" \
  "s_lshl1_add_u32 s90, s87, s90\n\t" \
  "s_lshl_b32 s90, s90, 13\n\t" \
  "s_cmp_lt_i32 s86, 6\n\t" \
  "s_cselect_b32 s90, 0x70000000, s90\n\t" \
  "s_waitcnt lgkmcnt(8)\n\t" \
  "v_mfma_f32_16x16x32_bf16 v[128:131], v[200:203], v[24:27], v[128:131]\n\t" \
  "v_mfma_f32_16x16x32_bf16 v[136:139], v[200:203], v[28:31], v[136:139]\n\t" \
  "ds_read_b128 v[200:203], v211 offset:256\n\t" \
  "v_mfma_f32_16x16x32_bf16 v[132:135], v[204:207], v[24:27], v[132:135]\n\t" \
  "v_mfma_f32_16x16x32_bf16 v[140:143], v[204:207], v[28:31], v[140:143]\n\t" \
  "ds_read_b128 v[204:207], v211 offset:16640\n\t" \
  "s_waitcnt lgkmcnt(4)\n\t" \
  "s_cmp_lt_u32 s86, 6\n\t" \
  "s_cbranch_scc1 .Lw00%=\n\t" \
  "s_waitcnt vmcnt(4)\n\t" \
  "s_barrier\n\t" \
  ".Lg00%=:\n\t" \
  "s_add_u32 s92, s92, 512\n\t" \
  "s_addc_u32 s93, s93, 0\n\t" \
  "v_mfma_f32_16x16x32_bf16 v[128:131], v[176:179], v[32:35], v[128:131]\n\t" \
  "v_mfma_f32_16x16x32_bf16 v[136:139], v[176:179], v[36:39], v[136:139]\n\t" \
  "ds_read_b128 v[176:179], v208 offset:32768\n\t" \
  "s_add_u32 m0, %[lw], 98304\n\t" \
  "v_mfma_f32_16x16x32_bf16 v[132:135], v[180:183], v[32:35], v[132:135]\n\t" \
  "v_mfma_f32_16x16x32_bf16 v[140:143], v[180:183], v[36:39], v[140:143]\n\t" \
  "ds_read_b128 v[180:183], v208 offset:49152\n\t" \
  "global_load_lds_dwordx4 v216, s[92:93] sc1\n\t" \
  "ds_read_b128 v[160:163], v221 offset:0\n\t" \
  "ds_read_b128 v[164:167], v221 offset:1024\n\t" \
  "v_mfma_f32_16x16x32_bf16 v[128:131], v[184:187], v[40:43], v[128:131]\n\t" \
  "v_mfma_f32_16x16x32_bf16 v[136:139], v[184:187], v[44:47], v[136:139]\n\t" \
  "ds_read_b128 v[184:187], v209 offset:32768\n\t" \
  "v_mfma_f32_16x16x32_bf16 v[132:135], v[188:191], v[40:43], v[132:135]\n\t" \
  "v_mfma_f32_16x16x32_bf16 v[140:143], v[188:191], v[44:47], v[140:143]\n\t" \
  "ds_read_b128 v[188:191], v209 offset:49152\n\t" \
  "s_waitcnt lgkmcnt(8)\n\t" \
  "v_mfma_f32_16x16x32_bf16 v[128:131], v[192:195], v[48:51], v[128:131]\n\t" \
  "v_mfma_f32_16x16x32_bf16 v[136:139], v[192:195], v[52:55], v[136:139]\n\t" \
  "ds_read_b128 v[192:195], v210 offset:32768\n\t" \
  "s_add_u32 m0, %[lw], 99328\n\t" \
  "v_mfma_f32_16x16x32_bf16 v[132:135], v[196:199], v[48:51], v[132:135]\n\t" \
  "v_mfma_f32_16x16x32_bf16 v[140:143], v[196:199], v[52:55], v[140:143]\n\t" \
  "ds_read_b128 v[196:199], v210 offset:49152\n\t" \
  "global_load_lds_dwordx4 v217, s[92:93] sc1\n\t" \
  "s_waitcnt lgkmcnt(4)\n\t" \
  "v_add_f32 v160, v160, v144\n\t" \
  "v_add_f32 v161, v161, v145\n\t" \
  "v_add_f32 v162, v162, v146\n\t" \
  "v_add_f32 v163, v163, v147\n\t" \
  "v_add_f32 v164, v164, v148\n\t" \
  "v_add_f32 v165, v165, v149\n\t" \
  "v_add_f32 v166, v166, v150\n\t" \
  "v_add_f32 v167, v167, v151\n\t" \
  "v_mfma_f32_16x16x32_bf16 v[128:131], v[200:203], v[56:59], v[128:131]\n\t" \
  "v_mfma_f32_16x16x32_bf16 v[136:139], v[200:203], v[60:63], v[136:139]\n\t" \
  "ds_read_b128 v[200:203], v211 offset:32768\n\t" \
  "v_mfma_f32_16x16x32_bf16 v[132:135], v[204:207], v[56:59], v[132:135]\n\t" \
  "v_mfma_f32_16x16x32_bf16 v[140:143], v[204:207], v[60:63], v[140:143]\n\t" \
  "ds_read_b128 v[204:207], v211 offset:49152\n\t" \
  "v_cvt_pk_bf16_f32 v168, v160, v161\n\t" \
  "v_cvt_pk_bf16_f32 v169, v162, v163\n\t" \
  "v_cvt_pk_bf16_f32 v170, v164, v165\n\t" \
  "v_cvt_pk_bf16_f32 v171, v166, v167\n\t" \
  "buffer_store_dwordx4 v[168:171], v172, %[rt], s90 offen sc1\n\t" \
  "v_mfma_f32_16x16x32_bf16 v[128:131], v[176:179], v[64:67], v[128:131]\n\t" \
  "v_mfma_f32_16x16x32_bf16 v[136:139], v[176:179], v[68:71], v[136:139]\n\t" \
  "ds_read_b128 v[176:179], v208 offset:33024\n\t" \
  "s_add_u32 m0, %[lw], 100352\n\t" \
  "v_mfma_f32_16x16x32_bf16 v[132:135], v[180:183], v[64:67], v[132:135]\n\t" \
  "v_mfma_f32_16x16x32_bf16 v[140:143], v[180:183], v[68:71], v[140:143]\n\t" \
  "ds_read_b128 v[180:183], v208 offset:49408\n\t" \
  "global_load_lds_dwordx4 v218, s[92:93] sc1\n\t" \
  "s_waitcnt lgkmcnt(6)\n\t" \
  "v_mfma_f32_16x16x32_bf16 v[128:131], v[184:187], v[72:75], v[128:131]\n\t" \
  "v_mfma_f32_16x16x32_bf16 v[136:139], v[184:187], v[76:79], v[136:139]\n\t" \
  "ds_read_b128 v[184:187], v209 offset:33024\n\t" \
  "v_mfma_f32_16x16x32_bf16 v[132:135], v[188:191], v[72:75], v[132:135]\n\t" \
  "v_mfma_f32_16x16x32_bf16 v[140:143], v[188:191], v[76:79], v[140:143]\n\t" \
  "ds_read_b128 v[188:191], v209 offset:49408\n\t" \
  "s_waitcnt lgkmcnt(6)\n\t" \
  "v_mfma_f32_16x16x32_bf16 v[128:131], v[192:195], v[80:83], v[128:131]\n\t" \
  "v_mfma_f32_16x16x32_bf16 v[136:139], v[192:195], v[84:87], v[136:139]\n\t" \
  "ds_read_b128 v[192:195], v210 offset:33024\n\t" \
  "s_add_u32 m0, %[lw], 101376\n\t" \
  "v_mfma_f32_16x16x32_bf16 v[132:135], v[196:199], v[80:83], v[132:135]\n\t" \
  "v_mfma_f32_16x16x32_bf16 v[140:143], v[196:199], v[84:87], v[140:143]\n\t" \
  "ds_read_b128 v[196:199], v210 offset:49408\n\t" \
  "global_load_lds_dwordx4 v219, s[92:93] sc1\n\t" \
  "s_waitcnt lgkmcnt(6)\n\t" \
  "v_mfma_f32_16x16x32_bf16 v[128:131], v[200:203], v[88:91], v[128:131]\n\t" \
  "v_mfma_f32_16x16x32_bf16 v[136:139], v[200:203], v[92:95], v[136:139]\n\t" \
  "ds_read_b128 v[200:203], v211 offset:33024\n\t" \
  "v_mfma_f32_16x16x32_bf16 v[132:135], v[204:207], v[88:91], v[132:135]\n\t" \
  "v_mfma_f32_16x16x32_bf16 v[140:143], v[204:207], v[92:95], v[140:143]\n\t" \
  "ds_read_b128 v[204:207], v211 offset:49408\n\t" \
  "s_cmp_lt_u32 s86, 8\n\t" \
  "s_cbranch_scc1 .Lw10%=\n\t" \
  "s_waitcnt vmcnt(5)\n\t" \
  "s_barrier\n\t" \
  ".Lg10%=:\n\t" \
  "s_add_u32 s92, s92, 65024\n\t" \
  "s_addc_u32 s93, s93, 0\n\t" \
  "s_waitcnt lgkmcnt(6)\n\t" \
  "v_mfma_f32_16x16x32_bf16 v[128:131], v[176:179], v[96:99], v[128:131]\n\t" \
  "v_mfma_f32_16x16x32_bf16 v[136:139], v[176:179], v[100:103], v[136:139]\n\t" \
  "ds_read_b128 v[176:179], v212 offset:0\n\t" \
  "s_add_u32 m0, %[lw], 0\n\t" \
  "v_mfma_f32_16x16x32_bf16 v[132:135], v[180:183], v[96:99], v[132:135]\n\t" \
  "v_mfma_f32_16x16x32_bf16 v[140:143], v[180:183], v[100:103], v[140:143]\n\t" \
  "ds_read_b128 v[180:183], v212 offset:16384\n\t" \
  "global_load_lds_dwordx4 v216, s[92:93] sc1\n\t" \
  "s_waitcnt lgkmcnt(6)\n\t" \
  "v_mfma_f32_16x16x32_bf16 v[128:131], v[184:187], v[104:107], v[128:131]\n\t" \
  "v_mfma_f32_16x16x32_bf16 v[136:139], v[184:187], v[108:111], v[136:139]\n\t" \
  "ds_read_b128 v[184:187], v213 offset:0\n\t" \
  "v_mfma_f32_16x16x32_bf16 v[132:135], v[188:191], v[104:107], v[132:135]\n\t" \
  "v_mfma_f32_16x16x32_bf16 v[140:143], v[188:191], v[108:111], v[140:143]\n\t" \
  "ds_read_b128 v[188:191], v213 offset:16384\n\t" \
  "s_waitcnt lgkmcnt(6)\n\t" \
  "v_mfma_f32_16x16x32_bf16 v[128:131], v[192:195], v[112:115], v[128:131]\n\t" \
  "v_mfma_f32_16x16x32_bf16 v[136:139], v[192:195], v[116:119], v[136:139]\n\t" \
  "ds_read_b128 v[192:195], v214 offset:0\n\t" \
  "s_add_u32 m0, %[lw], 1024\n\t" \
  "v_mfma_f32_16x16x32_bf16 v[132:135], v[196:199], v[112:115], v[132:135]\n\t" \
  "v_mfma_f32_16x16x32_bf16 v[140:143], v[196:199], v[116:119], v[140:143]\n\t" \
  "ds_read_b128 v[196:199], v214 offset:16384\n\t" \
  "global_load_lds_dwordx4 v217, s[92:93] sc1\n\t" \
  "s_waitcnt lgkmcnt(6)\n\t" \
  "v_mfma_f32_16x16x32_bf16 v[128:131], v[200:203], v[120:123], v[128:131]\n\t" \
  "v_mfma_f32_16x16x32_bf16 v[136:139], v[200:203], v[124:127], v[136:139]\n\t" \
  "ds_read_b128 v[200:203], v215 offset:0\n\t" \
  "v_mfma_f32_16x16x32_bf16 v[132:135], v[204:207], v[120:123], v[132:135]\n\t" \
  "v_mfma_f32_16x16x32_bf16 v[140:143], v[204:207], v[124:127], v[140:143]\n\t" \
  "ds_read_b128 v[204:207], v215 offset:16384\n\t" \
  "s_add_u32 s86, s86, 1\n\t" \
  "s_add_u32 s87, s87, 1\n\t" \
  "s_cmp_le_u32 s86, 5\n\t" \
  "s_cbranch_scc1 .Lf10%=\n\t" \
  ".Lfb10%=:\n\t" \
  "s_waitcnt lgkmcnt(6)\n\t" \
  "v_mfma_f32_16x16x32_bf16 v[144:147], v[176:179], v[0:3], 0\n\t" \
  "v_mfma_f32_16x16x32_bf16 v[152:155], v[176:179], v[4:7], 0\n\t" \
  "ds_read_b128 v[176:179], v212 offset:256\n\t" \
  "s_add_u32 m0, %[lw], 2048\n\t" \
  "v_mfma_f32_16x16x32_bf16 v[148:151], v[180:183], v[0:3], 0\n\t" \
  "v_mfma_f32_16x16x32_bf16 v[156:159], v[180:183], v[4:7], 0\n\t" \
  "ds_read_b128 v[180:183], v212 offset:16640\n\t" \
  "global_load_lds_dwordx4 v218, s[92:93] sc1\n\t" \
  "s_waitcnt lgkmcnt(6)\n\t" \
  "v_mfma_f32_16x16x32_bf16 v[144:147], v[184:187], v[8:11], v[144:147]\n\t" \
  "v_mfma_f32_16x16x32_bf16 v[152:155], v[184:187], v[12:15], v[152:155]\n\t" \
  "ds_read_b128 v[184:187], v213 offset:256\n\t" \
  "v_mfma_f32_16x16x32_bf16 v[148:151], v[188:191], v[8:11], v[148:151]\n\t" \
  "v_mfma_f32_16x16x32_bf16 v[156:159], v[188:191], v[12:15], v[156:159]\n\t" \
  "ds_read_b128 v[188:191], v213 offset:16640\n\t" \
  "ds_write_b128 v220, v[136:139] offset:0\n\t" \
  "ds_write_b128 v220, v[140:143] offset:1024\n\t" \
  "s_waitcnt lgkmcnt(8)\n\t" \
  "v_mfma_f32_16x16x32_bf16 v[144:147], v[192:195], v[16:19], v[144:147]\n\t" \
  "v_mfma_f32_16x16x32_bf16 v[152:155], v[192:195], v[20:23], v[152:155]\n\t" \
  "ds_read_b128 v[192:195], v214 offset:256\n\t" \
  "s_add_u32 m0, %[lw], 3072\n\t" \
  "v_mfma_f32_16x16x32_bf16 v[148:151], v[196:199], v[16:19], v[148:151]\n\t" \
  "v_mfma_f32_16x16x32_bf16 v[156:159], v[196:199], v[20:23], v[156:159]\n\t" \
  "ds_read_b128 v[196:199], v214 offset:16640\n\t" \
  "global_load_lds_dwordx4 v219, s[92:93] sc1\n\t" \
  "s_lshr_b32 s90, s87, 7\n\t" \
  "s_xor_b32 s90, s90, %[s0]\n\t" \
  "s_and_b32 s90, s90, 1\n\t" \
  "s_lshl1_add_u32 s90, s87, s90\n\t" \
  "s_lshl_b32 s90, s90, 13\n\t" \
  "s_cmp_lt_i32 s86, 6\n\t" \
  "s_cselect_b32 s90, 0x70000000, s90\n\t" \
  "s_waitcnt lgkmcnt(8)\n\t" \
  "v_mfma_f32_16x16x32_bf16 v[144:147], v[200:203], v[24:27], v[144:147]\n\t" \
  "v_mfma_f32_16x16x32_bf16 v[152:155], v[200:203], v[28:31], v[152:155]\n\t" \
  "ds_read_b128 v[200:203], v215 offset:256\n\t" \
  "v_mfma_f32_16x16x32_bf16 v[148:151], v[204:207], v[24:27], v[148:151]\n\t" \
  "v_mfma_f32_16x16x32_bf16 v[156:159], v[204:207], v[28:31], v[156:159]\n\t" \
  "ds_read_b128 v[204:207], v215 offset:16640\n\t" \
  "s_waitcnt lgkmcnt(4)\n\t" \
  "s_cmp_lt_u32 s86, 6\n\t" \
  "s_cbranch_scc1 .Lw20%=\n\t" \
  "s_waitcnt vmcnt(4)\n\t" \
  "s_barrier\n\t" \
  ".Lg20%=:\n\t" \
  "s_add_u32 s92, s92, 512\n\t" \
  "s_addc_u32 s93, s93, 0\n\t" \
  "v_mfma_f32_16x16x32_bf16 v[144:147], v[176:179], v[32:35], v[144:147]\n\t" \
  "v_mfma_f32_16x16x32_bf16 v[152:155], v[176:179], v[36:39], v[152:155]\n\t" \
  "ds_read_b128 v[176:179], v212 offset:32768\n\t" \
  "s_add_u32 m0, %[lw], 32768\n\t" \
  "v_mfma_f32_16x16x32_bf16 v[148:151], v[180:183], v[32:35], v[148:151]\n\t" \
  "v_mfma_f32_16x16x32_bf16 v[156:159], v[180:183], v[36:39], v[156:159]\n\t" \
  "ds_read_b128 v[180:183], v212 offset:49152\n\t" \
  "global_load_lds_dwordx4 v216, s[92:93] sc1\n\t" \
  "ds_read_b128 v[160:163], v221 offset:0\n\t" \
  "ds_read_b128 v[164:167], v221 offset:1024\n\t" \
  "v_mfma_f32_16x16x32_bf16 v[144:147], v[184:187], v[40:43], v[144:147]\n\t" \
  "v_mfma_f32_16x16x32_bf16 v[152:155], v[184:187], v[44:47], v[152:155]\n\t" \
  "ds_read_b128 v[184:187], v213 offset:32768\n\t" \
  "v_mfma_f32_16x16x32_bf16 v[148:151], v[188:191], v[40:43], v[148:151]\n\t" \
  "v_mfma_f32_16x16x32_bf16 v[156:159], v[188:191], v[44:47], v[156:159]\n\t" \
  "ds_read_b128 v[188:191], v213 offset:49152\n\t" \
  "s_waitcnt lgkmcnt(8)\n\t" \
  "v_mfma_f32_16x16x32_bf16 v[144:147], v[192:195], v[48:51], v[144:147]\n\t" \
  "v_mfma_f32_16x16x32_bf16 v[152:155], v[192:195], v[52:55], v[152:155]\n\t" \
  "ds_read_b128 v[192:195], v214 offset:32768\n\t" \
  "s_add_u32 m0, %[lw], 33792\n\t" \
  "v_mfma_f32_16x16x32_bf16 v[148:151], v[196:199], v[48:51], v[148:151]\n\t" \
  "v_mfma_f32_16x16x32_bf16 v[156:159], v[196:199], v[52:55], v[156:159]\n\t" \
  "ds_read_b128 v[196:199], v214 offset:49152\n\t" \
  "global_load_lds_dwordx4 v217, s[92:93] sc1\n\t" \
  "s_waitcnt lgkmcnt(4)\n\t" \
  "v_add_f32 v160, v160, v128\n\t" \
  "v_add_f32 v161, v161, v129\n\t" \
  "v_add_f32 v162, v162, v130\n\t" \
  "v_add_f32 v163, v163, v131\n\t" \
  "v_add_f32 v164, v164, v132\n\t" \
  "v_add_f32 v165, v165, v133\n\t" \
  "v_add_f32 v166, v166, v134\n\t" \
  "v_add_f32 v167, v167, v135\n\t" \
  "v_mfma_f32_16x16x32_bf16 v[144:147], v[200:203], v[56:59], v[144:147]\n\t" \
  "v_mfma_f32_16x16x32_bf16 v[152:155], v[200:203], v[60:63], v[152:155]\n\t" \
  "ds_read_b128 v[200:203], v215 offset:32768\n\t" \
  "v_mfma_f32_16x16x32_bf16 v[148:151], v[204:207], v[56:59], v[148:151]\n\t" \
  "v_mfma_f32_16x16x32_bf16 v[156:159], v[204:207], v[60:63], v[156:159]\n\t" \
  "ds_read_b128 v[204:207], v215 offset:49152\n\t" \
  "v_cvt_pk_bf16_f32 v168, v160, v161\n\t" \
  "v_cvt_pk_bf16_f32 v169, v162, v163\n\t" \
  "v_cvt_pk_bf16_f32 v170, v164, v165\n\t" \
  "v_cvt_pk_bf16_f32 v171, v166, v167\n\t" \
  "buffer_store_dwordx4 v[168:171], v172, %[rt], s90 offen sc1\n\t" \
  "v_mfma_f32_16x16x32_bf16 v[144:147], v[176:179], v[64:67], v[144:147]\n\t" \
  "v_mfma_f32_16x16x32_bf16 v[152:155], v[176:179], v[68:71], v[152:155]\n\t" \
  "ds_read_b128 v[176:179], v212 offset:33024\n\t" \
  "s_add_u32 m0, %[lw], 34816\n\t" \
  "v_mfma_f32_16x16x32_bf16 v[148:151], v[180:183], v[64:67], v[148:151]\n\t" \
  "v_mfma_f32_16x16x32_bf16 v[156:159], v[180:183], v[68:71], v[156:159]\n\t" \
  "ds_read_b128 v[180:183], v212 offset:49408\n\t" \
  "global_load_lds_dwordx4 v218, s[92:93] sc1\n\t" \
  "s_waitcnt lgkmcnt(6)\n\t" \
  "v_mfma_f32_16x16x32_bf16 v[144:147], v[184:187], v[72:75], v[144:147]\n\t" \
  "v_mfma_f32_16x16x32_bf16 v[152:155], v[184:187], v[76:79], v[152:155]\n\t" \
  "ds_read_b128 v[184:187], v213 offset:33024\n\t" \
  "v_mfma_f32_16x16x32_bf16 v[148:151], v[188:191], v[72:75], v[148:151]\n\t" \
  "v_mfma_f32_16x16x32_bf16 v[156:159], v[188:191], v[76:79], v[156:159]\n\t" \
  "ds_read_b128 v[188:191], v213 offset:49408\n\t" \
  "s_waitcnt lgkmcnt(6)\n\t" \
  "v_mfma_f32_16x16x32_bf16 v[144:147], v[192:195], v[80:83], v[144:147]\n\t" \
  "v_mfma_f32_16x16x32_bf16 v[152:155], v[192:195], v[84:87], v[152:155]\n\t" \
  "ds_read_b128 v[192:195], v214 offset:33024\n\t" \
  "s_add_u32 m0, %[lw], 35840\n\t" \
  "v_mfma_f32_16x16x32_bf16 v[148:151], v[196:199], v[80:83], v[148:151]\n\t" \
  "v_mfma_f32_16x16x32_bf16 v[156:159], v[196:199], v[84:87], v[156:159]\n\t" \
  "ds_read_b128 v[196:199], v214 offset:49408\n\t" \
  "global_load_lds_dwordx4 v219, s[92:93] sc1\n\t" \
  "s_waitcnt lgkmcnt(6)\n\t" \
  "v_mfma_f32_16x16x32_bf16 v[144:147], v[200:203], v[88:91], v[144:147]\n\t" \
  "v_mfma_f32_16x16x32_bf16 v[152:155], v[200:203], v[92:95], v[152:155]\n\t" \
  "ds_read_b128 v[200:203], v215 offset:33024\n\t" \
  "v_mfma_f32_16x16x32_bf16 v[148:151], v[204:207], v[88:91], v[148:151]\n\t" \
  "v_mfma_f32_16x16x32_bf16 v[156:159], v[204:207], v[92:95], v[156:159]\n\t" \
  "ds_read_b128 v[204:207], v215 offset:49408\n\t" \
  "s_cmp_lt_u32 s86, 8\n\t" \
  "s_cbranch_scc1 .Lw30%=\n\t" \
  "s_waitcnt vmcnt(5)\n\t" \
  "s_barrier\n\t" \
  ".Lg30%=:\n\t" \
  "s_add_u32 s92, s92, 65024\n\t" \
  "s_addc_u32 s93, s93, 0\n\t" \
  "s_waitcnt lgkmcnt(6)\n\t" \
  "v_mfma_f32_16x16x32_bf16 v[144:147], v[176:179], v[96:99], v[144:147]\n\t" \
  "v_mfma_f32_16x16x32_bf16 v[152:155], v[176:179], v[100:103], v[152:155]\n\t" \
  "ds_read_b128 v[176:179], v208 offset:0\n\t" \
  "s_add_u32 m0, %[lw], 65536\n\t" \
  "v_mfma_f32_16x16x32_bf16 v[148:151], v[180:183], v[96:99], v[148:151]\n\t" \
  "v_mfma_f32_16x16x32_bf16 v[156:159], v[180:183], v[100:103], v[156:159]\n\t" \
  "ds_read_b128 v[180:183], v208 offset:16384\n\t" \
  "global_load_lds_dwordx4 v216, s[92:93] sc1\n\t" \
  "s_waitcnt lgkmcnt(6)\n\t" \
  "v_mfma_f32_16x16x32_bf16 v[144:147], v[184:187], v[104:107], v[144:147]\n\t" \
  "v_mfma_f32_16x16x32_bf16 v[152:155], v[184:187], v[108:111], v[152:155]\n\t" \
  "ds_read_b128 v[184:187], v209 offset:0\n\t" \
  "v_mfma_f32_16x16x32_bf16 v[148:151], v[188:191], v[104:107], v[148:151]\n\t" \
  "v_mfma_f32_16x16x32_bf16 v[156:159], v[188:191], v[108:111], v[156:159]\n\t" \
  "ds_read_b128 v[188:191], v209 offset:16384\n\t" \
  "s_waitcnt lgkmcnt(6)\n\t" \
  "v_mfma_f32_16x16x32_bf16 v[144:147], v[192:195], v[112:115], v[144:147]\n\t" \
  "v_mfma_f32_16x16x32_bf16 v[152:155], v[192:195], v[116:119], v[152:155]\n\t" \
  "ds_read_b128 v[192:195], v210 offset:0\n\t" \
  "s_add_u32 m0, %[lw], 66560\n\t" \
  "v_mfma_f32_16x16x32_bf16 v[148:151], v[196:199], v[112:115], v[148:151]\n\t" \
  "v_mfma_f32_16x16x32_bf16 v[156:159], v[196:199], v[116:119], v[156:159]\n\t" \
  "ds_read_b128 v[196:199], v210 offset:16384\n\t" \
  "global_load_lds_dwordx4 v217, s[92:93] sc1\n\t" \
  "s_waitcnt lgkmcnt(6)\n\t" \
  "v_mfma_f32_16x16x32_bf16 v[144:147], v[200:203], v[120:123], v[144:147]\n\t" \
  "v_mfma_f32_16x16x32_bf16 v[152:155], v[200:203], v[124:127], v[152:155]\n\t" \
  "ds_read_b128 v[200:203], v211 offset:0\n\t" \
  "v_mfma_f32_16x16x32_bf16 v[148:151], v[204:207], v[120:123], v[148:151]\n\t" \
  "v_mfma_f32_16x16x32_bf16 v[156:159], v[204:207], v[124:127], v[156:159]\n\t" \
  "ds_read_b128 v[204:207], v211 offset:16384\n\t" \
  "s_add_u32 s86, s86, 1\n\t" \
  "s_add_u32 s87, s87, 1\n\t" \
  "s_cmp_lt_u32 s86, %[end]\n\t" \
  "s_cbranch_scc1 .Lp0%=\n\t" \
  "s_branch .Lx0%=\n\t" \
  ".Lf00%=:\n\t" \
  "s_nop 7\n\t" \
  "ds_write_b128 v220, v[152:155] offset:0\n\t" \
  "ds_write_b128 v220, v[156:159] offset:1024\n\t" \
  "s_lshr_b32 s90, s87, 7\n\t" \
  "s_xor_b32 s90, s90, %[s0]\n\t" \
  "s_and_b32 s90, s90, 1\n\t" \
  "s_lshl1_add_u32 s90, s87, s90\n\t" \
  "s_lshl_b32 s90, s90, 13\n\t" \
  "s_cmp_lt_i32 s86, 1\n\t" \
  "s_cselect_b32 s90, 0x70000000, s90\n\t" \
  "s_waitcnt lgkmcnt(0)\n\t" \
  "s_barrier\n\t" \
  "ds_read_b128 v[160:163], v221 offset:0\n\t" \
  "ds_read_b128 v[164:167], v221 offset:1024\n\t" \
  "s_waitcnt lgkmcnt(0)\n\t" \
  "v_add_f32 v160, v160, v144\n\t" \
  "v_add_f32 v161, v161, v145\n\t" \
  "v_add_f32 v162, v162, v146\n\t" \
  "v_add_f32 v163, v163, v147\n\t" \
  "v_add_f32 v164, v164, v148\n\t" \
  "v_add_f32 v165, v165, v149\n\t" \
  "v_add_f32 v166, v166, v150\n\t" \
  "v_add_f32 v167, v167, v151\n\t" \
  "v_cvt_pk_bf16_f32 v168, v160, v161\n\t" \
  "v_cvt_pk_bf16_f32 v169, v162, v163\n\t" \
  "v_cvt_pk_bf16_f32 v170, v164, v165\n\t" \
  "v_cvt_pk_bf16_f32 v171, v166, v167\n\t" \
  "buffer_store_dwordx4 v[168:171], v172, %[rt], s90 offen sc1\n\t" \
  "s_branch .Lfb00%=\n\t" \
  ".Lf10%=:\n\t" \
  "s_nop 7\n\t" \
  "ds_write_b128 v220, v[136:139] offset:0\n\t" \
  "ds_write_b128 v220, v[140:143] offset:1024\n\t" \
  "s_lshr_b32 s90, s87, 7\n\t" \
  "s_xor_b32 s90, s90, %[s0]\n\t" \
  "s_and_b32 s90, s90, 1\n\t" \
  "s_lshl1_add_u32 s90, s87, s90\n\t" \
  "s_lshl_b32 s90, s90, 13\n\t" \
  "s_cmp_lt_i32 s86, 1\n\t" \
  "s_cselect_b32 s90, 0x70000000, s90\n\t" \
  "s_waitcnt lgkmcnt(0)\n\t" \
  "s_barrier\n\t" \
  "ds_read_b128 v[160:163], v221 offset:0\n\t" \
  "ds_read_b128 v[164:167], v221 offset:1024\n\t" \
  "s_waitcnt lgkmcnt(0)\n\t" \
  "v_add_f32 v160, v160, v128\n\t" \
  "v_add_f32 v161, v161, v129\n\t" \
  "v_add_f32 v162, v162, v130\n\t" \
  "v_add_f32 v163, v163, v131\n\t" \
  "v_add_f32 v164, v164, v132\n\t" \
  "v_add_f32 v165, v165, v133\n\t" \
  "v_add_f32 v166, v166, v134\n\t" \
  "v_add_f32 v167, v167, v135\n\t" \
  "v_cvt_pk_bf16_f32 v168, v160, v161\n\t" \
  "v_cvt_pk_bf16_f32 v169, v162, v163\n\t" \
  "v_cvt_pk_bf16_f32 v170, v164, v165\n\t" \
  "v_cvt_pk_bf16_f32 v171, v166, v167\n\t" \
  "buffer_store_dwordx4 v[168:171], v172, %[rt], s90 offen sc1\n\t" \
  "s_branch .Lfb10%=\n\t" \
  ".Lw00%=:\n\t" \
  "s_waitcnt vmcnt(2)\n\t" \
  "s_barrier\n\t" \
  "s_branch .Lg00%=\n\t" \
  ".Lw10%=:\n\t" \
  "s_waitcnt vmcnt(2)\n\t" \
  "s_barrier\n\t" \
  "s_branch .Lg10%=\n\t" \
  ".Lw20%=:\n\t" \
  "s_waitcnt vmcnt(2)\n\t" \
  "s_barrier\n\t" \
  "s_branch .Lg20%=\n\t" \
  ".Lw30%=:\n\t" \
  "s_waitcnt vmcnt(2)\n\t" \
  "s_barrier\n\t" \
  "s_branch .Lg30%=\n\t" \
  ".Lx0%=:\n\t" \
  "s_waitcnt lgkmcnt(0)\n\t" \
  "s_setprio 0\n\t" \
  "s_mov_b32 m0, s94\n\t" \
  "s_branch .Le%=\n\t" \
  ".Lk%=:\n\t" \
  "s_mov_b32 s94, m0\n\t" \
  "s_mov_b64 s[92:93], %[x0]\n\t" \
  "s_mov_b32 s86, %[g0]\n\t" \
  "s_add_u32 s87, s86, %[t0]\n\t" \
  "s_sub_i32 s88, s86, 3\n\t" \
  "ds_read_b128 v[176:179], v208 offset:0\n\t" \
  "ds_read_b128 v[180:183], v208 offset:16384\n\t" \
  "ds_read_b128 v[184:187], v209 offset:0\n\t" \
  "ds_read_b128 v[188:191], v209 offset:16384\n\t" \
  "ds_read_b128 v[192:195], v210 offset:0\n\t" \
  "ds_read_b128 v[196:199], v210 offset:16384\n\t" \
  "ds_read_b128 v[200:203], v211 offset:0\n\t" \
  "ds_read_b128 v[204:207], v211 offset:16384\n\t" \
  "s_branch .Lfb01%=\n\t" \
  ".Lp1%=:\n\t" \
  "s_cmp_le_u32 s86, 5\n\t" \
  "s_cbranch_scc1 .Lf01%=\n\t" \
  ".Lfb01%=:\n\t" \
  "s_waitcnt lgkmcnt(6)\n\t" \
  "v_mfma_f32_16x16x32_bf16 v[128:131], v[176:179], v[0:3], 0\n\t" \
  "v_mfma_f32_16x16x32_bf16 v[136:139], v[176:179], v[4:7], 0\n\t" \
  "ds_read_b128 v[176:179], v208 offset:256\n\t" \
  "v_mfma_f32_16x16x32_bf16 v[132:135], v[180:183], v[0:3], 0\n\t" \
  "v_mfma_f32_16x16x32_bf16 v[140:143], v[180:183], v[4:7], 0\n\t" \
  "ds_read_b128 v[180:183], v208 offset:16640\n\t" \
  "s_waitcnt lgkmcnt(6)\n\t" \
  "v_mfma_f32_16x16x32_bf16 v[128:131], v[184:187], v[8:11], v[128:131]\n\t" \
  "v_mfma_f32_16x16x32_bf16 v[136:139], v[184:187], v[12:15], v[136:139]\n\t" \
  "ds_read_b128 v[184:187], v209 offset:256\n\t" \
  "s_add_u32 m0, %[lw], 67584\n\t" \
  "v_mfma_f32_16x16x32_bf16 v[132:135], v[188:191], v[8:11], v[132:135]\n\t" \
  "v_mfma_f32_16x16x32_bf16 v[140:143], v[188:191], v[12:15], v[140:143]\n\t" \
  "ds_read_b128 v[188:191], v209 offset:16640\n\t" \
  "global_load_lds_dwordx4 v218, s[92:93] sc1\n\t" \
  "ds_write_b128 v220, v[152:155] offset:0\n\t" \
  "ds_write_b128 v220, v[156:159] offset:1024\n\t" \
  "s_waitcnt lgkmcnt(8)\n\t" \
  "v_mfma_f32_16x16x32_bf16 v[128:131], v[192:195], v[16:19], v[128:131]\n\t" \
  "v_mfma_f32_16x16x32_bf16 v[136:139], v[192:195], v[20:23], v[136:139]\n\t" \
  "ds_read_b128 v[192:195], v210 offset:256\n\t" \
  "v_mfma_f32_16x16x32_bf16 v[132:135], v[196:199], v[16:19], v[132:135]\n\t" \
  "v_mfma_f32_16x16x32_bf16 v[140:143], v[196:199], v[20:23], v[140:143]\n\t" \
  "ds_read_b128 v[196:199], v210 offset:16640\n\t" \
  "s_lshr_b32 s90, s87, 7\n\t" \
  "s_xor_b32 s90, s90, %[s0]\n\t" \
  "s_and_b32 s90, s90, 1\n\t" \
  "s_lshl1_add_u32 s90, s87, s90\n\t" \
  "s_lshl_b32 s90, s90, 13\n\t" \
  "s_cmp_lt_i32 s86, 6\n\t" \
  "s_cselect_b32 s90, 0x70000000, s90\n\t" \
  "s_waitcnt lgkmcnt(8)\n\t" \
  "v_mfma_f32_16x16x32_bf16 v[128:131], v[200:203], v[24:27], v[128:131]\n\t" \
  "v_mfma_f32_16x16x32_bf16 v[136:139], v[200:203], v[28:31], v[136:139]\n\t" \
  "ds_read_b128 v[200:203], v211 offset:256\n\t" \
  "s_add_u32 m0, %[lw], 68608\n\t" \
  "v_mfma_f32_16x16x32_bf16 v[132:135], v[204:207], v[24:27], v[132:135]\n\t" \
  "v_mfma_f32_16x16x32_bf16 v[140:143], v[204:207], v[28:31], v[140:143]\n\t" \
  "ds_read_b128 v[204:207], v211 offset:16640\n\t" \
  "global_load_lds_dwordx4 v219, s[92:93] sc1\n\t" \
  "s_waitcnt lgkmcnt(4)\n\t" \
  "s_cmp_lt_u32 s86, 6\n\t" \
  "s_cbranch_scc1 .Lw01%=\n\t" \
  "s_waitcnt vmcnt(4)\n\t" \
  "s_barrier\n\t" \
  ".Lg01%=:\n\t" \
  "s_add_u32 s92, s92, 512\n\t" \
  "s_addc_u32 s93, s93, 0\n\t" \
  "v_mfma_f32_16x16x32_bf16 v[128:131], v[176:179], v[32:35], v[128:131]\n\t" \
  "v_mfma_f32_16x16x32_bf16 v[136:139], v[176:179], v[36:39], v[136:139]\n\t" \
  "ds_read_b128 v[176:179], v208 offset:32768\n\t" \
  "v_mfma_f32_16x16x32_bf16 v[132:135], v[180:183], v[32:35], v[132:135]\n\t" \
  "v_mfma_f32_16x16x32_bf16 v[140:143], v[180:183], v[36:39], v[140:143]\n\t" \
  "ds_read_b128 v[180:183], v208 offset:49152\n\t" \
  "ds_read_b128 v[160:163], v221 offset:0\n\t" \
  "ds_read_b128 v[164:167], v221 offset:1024\n\t" \
  "s_sub_i32 s91, s86, 2\n\t" \
  "s_max_i32 s88, s88, s91\n\t" \
  "v_mov_b32 v173, s88\n\t" \
  "buffer_store_dword v173, v174, %[rs], 0 offen sc1\n\t" \
  "v_mfma_f32_16x16x32_bf16 v[128:131], v[184:187], v[40:43], v[128:131]\n\t" \
  "v_mfma_f32_16x16x32_bf16 v[136:139], v[184:187], v[44:47], v[136:139]\n\t" \
  "ds_read_b128 v[184:187], v209 offset:32768\n\t" \
  "s_add_u32 m0, %[lw], 98304\n\t" \
  "v_mfma_f32_16x16x32_bf16 v[132:135], v[188:191], v[40:43], v[132:135]\n\t" \
  "v_mfma_f32_16x16x32_bf16 v[140:143], v[188:191], v[44:47], v[140:143]\n\t" \
  "ds_read_b128 v[188:191], v209 offset:49152\n\t" \
  "global_load_lds_dwordx4 v216, s[92:93] sc1\n\t" \
  "s_waitcnt lgkmcnt(8)\n\t" \
  "v_mfma_f32_16x16x32_bf16 v[128:131], v[192:195], v[48:51], v[128:131]\n\t" \
  "v_mfma_f32_16x16x32_bf16 v[136:139], v[192:195], v[52:55], v[136:139]\n\t" \
  "ds_read_b128 v[192:195], v210 offset:32768\n\t" \
  "v_mfma_f32_16x16x32_bf16 v[132:135], v[196:199], v[48:51], v[132:135]\n\t" \
  "v_mfma_f32_16x16x32_bf16 v[140:143], v[196:199], v[52:55], v[140:143]\n\t" \
  "ds_read_b128 v[196:199], v210 offset:49152\n\t" \
  "s_waitcnt lgkmcnt(4)\n\t" \
  "v_add_f32 v160, v160, v144\n\t" \
  "v_add_f32 v161, v161, v145\n\t" \
  "v_add_f32 v162, v162, v146\n\t" \
  "v_add_f32 v163, v163, v147\n\t" \
  "v_add_f32 v164, v164, v148\n\t" \
  "v_add_f32 v165, v165, v149\n\t" \
  "v_add_f32 v166, v166, v150\n\t" \
  "v_add_f32 v167, v167, v151\n\t" \
  "v_mfma_f32_16x16x32_bf16 v[128:131], v[200:203], v[56:59], v[128:131]\n\t" \
  "v_mfma_f32_16x16x32_bf16 v[136:139], v[200:203], v[60:63], v[136:139]\n\t" \
  "ds_read_b128 v[200:203], v211 offset:32768\n\t" \
  "s_add_u32 m0, %[lw], 99328\n\t" \
  "v_mfma_f32_16x16x32_bf16 v[132:135], v[204:207], v[56:59], v[132:135]\n\t" \
  "v_mfma_f32_16x16x32_bf16 v[140:143], v[204:207], v[60:63], v[140:143]\n\t" \
  "ds_read_b128 v[204:207], v211 offset:49152\n\t" \
  "global_load_lds_dwordx4 v217, s[92:93] sc1\n\t" \
  "v_cvt_pk_bf16_f32 v168, v160, v161\n\t" \
  "v_cvt_pk_bf16_f32 v169, v162, v163\n\t" \
  "v_cvt_pk_bf16_f32 v170, v164, v165\n\t" \
  "v_cvt_pk_bf16_f32 v171, v166, v167\n\t" \
  "buffer_store_dwordx4 v[168:171], v172, %[rt], s90 offen sc1\n\t" \
  "v_mfma_f32_16x16x32_bf16 v[128:131], v[176:179], v[64:67], v[128:131]\n\t" \
  "v_mfma_f32_16x16x32_bf16 v[136:139], v[176:179], v[68:71], v[136:139]\n\t" \
  "ds_read_b128 v[176:179], v208 offset:33024\n\t" \
  "v_mfma_f32_16x16x32_bf16 v[132:135], v[180:183], v[64:67], v[132:135]\n\t" \
  "v_mfma_f32_16x16x32_bf16 v[140:143], v[180:183], v[68:71], v[140:143]\n\t" \
  "ds_read_b128 v[180:183], v208 offset:49408\n\t" \
  "s_waitcnt lgkmcnt(6)\n\t" \
  "v_mfma_f32_16x16x32_bf16 v[128:131], v[184:187], v[72:75], v[128:131]\n\t" \
  "v_mfma_f32_16x16x32_bf16 v[136:139], v[184:187], v[76:79], v[136:139]\n\t" \
  "ds_read_b128 v[184:187], v209 offset:33024\n\t" \
  "s_add_u32 m0, %[lw], 100352\n\t" \
  "v_mfma_f32_16x16x32_bf16 v[132:135], v[188:191], v[72:75], v[132:135]\n\t" \
  "v_mfma_f32_16x16x32_bf16 v[140:143], v[188:191], v[76:79], v[140:143]\n\t" \
  "ds_read_b128 v[188:191], v209 offset:49408\n\t" \
  "global_load_lds_dwordx4 v218, s[92:93] sc1\n\t" \
  "s_waitcnt lgkmcnt(6)\n\t" \
  "v_mfma_f32_16x16x32_bf16 v[128:131], v[192:195], v[80:83], v[128:131]\n\t" \
  "v_mfma_f32_16x16x32_bf16 v[136:139], v[192:195], v[84:87], v[136:139]\n\t" \
  "ds_read_b128 v[192:195], v210 offset:33024\n\t" \
  "v_mfma_f32_16x16x32_bf16 v[132:135], v[196:199], v[80:83], v[132:135]\n\t" \
  "v_mfma_f32_16x16x32_bf16 v[140:143], v[196:199], v[84:87], v[140:143]\n\t" \
  "ds_read_b128 v[196:199], v210 offset:49408\n\t" \
  "s_waitcnt lgkmcnt(6)\n\t" \
  "v_mfma_f32_16x16x32_bf16 v[128:131], v[200:203], v[88:91], v[128:131]\n\t" \
  "v_mfma_f32_16x16x32_bf16 v[136:139], v[200:203], v[92:95], v[136:139]\n\t" \
  "ds_read_b128 v[200:203], v211 offset:33024\n\t" \
  "s_add_u32 m0, %[lw], 101376\n\t" \
  "v_mfma_f32_16x16x32_bf16 v[132:135], v[204:207], v[88:91], v[132:135]\n\t" \
  "v_mfma_f32_16x16x32_bf16 v[140:143], v[204:207], v[92:95], v[140:143]\n\t" \
  "ds_read_b128 v[204:207], v211 offset:49408\n\t" \
  "global_load_lds_dwordx4 v219, s[92:93] sc1\n\t" \
  "s_cmp_lt_u32 s86, 8\n\t" \
  "s_cbranch_scc1 .Lw11%=\n\t" \
  "s_waitcnt vmcnt(6)\n\t" \
  "s_barrier\n\t" \
  ".Lg11%=:\n\t" \
  "s_add_u32 s92, s92, 65024\n\t" \
  "s_addc_u32 s93, s93, 0\n\t" \
  "s_waitcnt lgkmcnt(6)\n\t" \
  "v_mfma_f32_16x16x32_bf16 v[128:131], v[176:179], v[96:99], v[128:131]\n\t" \
  "v_mfma_f32_16x16x32_bf16 v[136:139], v[176:179], v[100:103], v[136:139]\n\t" \
  "ds_read_b128 v[176:179], v212 offset:0\n\t" \
  "v_mfma_f32_16x16x32_bf16 v[132:135], v[180:183], v[96:99], v[132:135]\n\t" \
  "v_mfma_f32_16x16x32_bf16 v[140:143], v[180:183], v[100:103], v[140:143]\n\t" \
  "ds_read_b128 v[180:183], v212 offset:16384\n\t" \
  "s_waitcnt lgkmcnt(6)\n\t" \
  "v_mfma_f32_16x16x32_bf16 v[128:131], v[184:187], v[104:107], v[128:131]\n\t" \
  "v_mfma_f32_16x16x32_bf16 v[136:139], v[184:187], v[108:111], v[136:139]\n\t" \
  "ds_read_b128 v[184:187], v213 offset:0\n\t" \
  "s_add_u32 m0, %[lw], 0\n\t" \
  "v_mfma_f32_16x16x32_bf16 v[132:135], v[188:191], v[104:107], v[132:135]\n\t" \
  "v_mfma_f32_16x16x32_bf16 v[140:143], v[188:191], v[108:111], v[140:143]\n\t" \
  "ds_read_b128 v[188:191], v213 offset:16384\n\t" \
  "global_load_lds_dwordx4 v216, s[92:93] sc1\n\t" \
  "s_waitcnt lgkmcnt(6)\n\t" \
  "v_mfma_f32_16x16x32_bf16 v[128:131], v[192:195], v[112:115], v[128:131]\n\t" \
  "v_mfma_f32_16x16x32_bf16 v[136:139], v[192:195], v[116:119], v[136:139]\n\t" \
  "ds_read_b128 v[192:195], v214 offset:0\n\t" \
  "v_mfma_f32_16x16x32_bf16 v[132:135], v[196:199], v[112:115], v[132:135]\n\t" \
  "v_mfma_f32_16x16x32_bf16 v[140:143], v[196:199], v[116:119], v[140:143]\n\t" \
  "ds_read_b128 v[196:199], v214 offset:16384\n\t" \
  "s_waitcnt lgkmcnt(6)\n\t" \
  "v_mfma_f32_16x16x32_bf16 v[128:131], v[200:203], v[120:123], v[128:131]\n\t" \
  "v_mfma_f32_16x16x32_bf16 v[136:139], v[200:203], v[124:127], v[136:139]\n\t" \
  "ds_read_b128 v[200:203], v215 offset:0\n\t" \
  "s_add_u32 m0, %[lw], 1024\n\t" \
  "v_mfma_f32_16x16x32_bf16 v[132:135], v[204:207], v[120:123], v[132:135]\n\t" \
  "v_mfma_f32_16x16x32_bf16 v[140:143], v[204:207], v[124:127], v[140:143]\n\t" \
  "ds_read_b128 v[204:207], v215 offset:16384\n\t" \
  "global_load_lds_dwordx4 v217, s[92:93] sc1\n\t" \
  "s_add_u32 s86, s86, 1\n\t" \
  "s_add_u32 s87, s87, 1\n\t" \
  "s_cmp_le_u32 s86, 5\n\t" \
  "s_cbranch_scc1 .Lf11%=\n\t" \
  ".Lfb11%=:\n\t" \
  "s_waitcnt lgkmcnt(6)\n\t" \
  "v_mfma_f32_16x16x32_bf16 v[144:147], v[176:179], v[0:3], 0\n\t" \
  "v_mfma_f32_16x16x32_bf16 v[152:155], v[176:179], v[4:7], 0\n\t" \
  "ds_read_b128 v[176:179], v212 offset:256\n\t" \
  "v_mfma_f32_16x16x32_bf16 v[148:151], v[180:183], v[0:3], 0\n\t" \
  "v_mfma_f32_16x16x32_bf16 v[156:159], v[180:183], v[4:7], 0\n\t" \
  "ds_read_b128 v[180:183], v212 offset:16640\n\t" \
  "s_waitcnt lgkmcnt(6)\n\t" \
  "v_mfma_f32_16x16x32_bf16 v[144:147], v[184:187], v[8:11], v[144:147]\n\t" \
  "v_mfma_f32_16x16x32_bf16 v[152:155], v[184:187], v[12:15], v[152:155]\n\t" \
  "ds_read_b128 v[184:187], v213 offset:256\n\t" \
  "s_add_u32 m0, %[lw], 2048\n\t" \
  "v_mfma_f32_16x16x32_bf16 v[148:151], v[188:191], v[8:11], v[148:151]\n\t" \
  "v_mfma_f32_16x16x32_bf16 v[156:159], v[188:191], v[12:15], v[156:159]\n\t" \
  "ds_read_b128 v[188:191], v213 offset:16640\n\t" \
  "global_load_lds_dwordx4 v218, s[92:93] sc1\n\t" \
  "ds_write_b128 v220, v[136:139] offset:0\n\t" \
  "ds_write_b128 v220, v[140:143] offset:1024\n\t" \
  "s_waitcnt lgkmcnt(8)\n\t" \
  "v_mfma_f32_16x16x32_bf16 v[144:147], v[192:195], v[16:19], v[144:147]\n\t" \
  "v_mfma_f32_16x16x32_bf16 v[152:155], v[192:195], v[20:23], v[152:155]\n\t" \
  "ds_read_b128 v[192:195], v214 offset:256\n\t" \
  "v_mfma_f32_16x16x32_bf16 v[148:151], v[196:199], v[16:19], v[148:151]\n\t" \
  "v_mfma_f32_16x16x32_bf16 v[156:159], v[196:199], v[20:23], v[156:159]\n\t" \
  "ds_read_b128 v[196:199], v214 offset:16640\n\t" \
  "s_lshr_b32 s90, s87, 7\n\t" \
  "s_xor_b32 s90, s90, %[s0]\n\t" \
  "s_and_b32 s90, s90, 1\n\t" \
  "s_lshl1_add_u32 s90, s87, s90\n\t" \
  "s_lshl_b32 s90, s90, 13\n\t" \
  "s_cmp_lt_i32 s86, 6\n\t" \
  "s_cselect_b32 s90, 0x70000000, s90\n\t" \
  "s_waitcnt lgkmcnt(8)\n\t" \
  "v_mfma_f32_16x16x32_bf16 v[144:147], v[200:203], v[24:27], v[144:147]\n\t" \
  "v_mfma_f32_16x16x32_bf16 v[152:155], v[200:203], v[28:31], v[152:155]\n\t" \
  "ds_read_b128 v[200:203], v215 offset:256\n\t" \
  "s_add_u32 m0, %[lw], 3072\n\t" \
  "v_mfma_f32_16x16x32_bf16 v[148:151], v[204:207], v[24:27], v[148:151]\n\t" \
  "v_mfma_f32_16x16x32_bf16 v[156:159], v[204:207], v[28:31], v[156:159]\n\t" \
  "ds_read_b128 v[204:207], v215 offset:16640\n\t" \
  "global_load_lds_dwordx4 v219, s[92:93] sc1\n\t" \
  "s_waitcnt lgkmcnt(4)\n\t" \
  "s_cmp_lt_u32 s86, 6\n\t" \
  "s_cbranch_scc1 .Lw21%=\n\t" \
  "s_waitcnt vmcnt(4)\n\t" \
  "s_barrier\n\t" \
  ".Lg21%=:\n\t" \
  "s_add_u32 s92, s92, 512\n\t" \
  "s_addc_u32 s93, s93, 0\n\t" \
  "v_mfma_f32_16x16x32_bf16 v[144:147], v[176:179], v[32:35], v[144:147]\n\t" \
  "v_mfma_f32_16x16x32_bf16 v[152:155], v[176:179], v[36:39], v[152:155]\n\t" \
  "ds_read_b128 v[176:179], v212 offset:32768\n\t" \
  "v_mfma_f32_16x16x32_bf16 v[148:151], v[180:183], v[32:35], v[148:151]\n\t" \
  "v_mfma_f32_16x16x32_bf16 v[156:159], v[180:183], v[36:39], v[156:159]\n\t" \
  "ds_read_b128 v[180:183], v212 offset:49152\n\t" \
  "ds_read_b128 v[160:163], v221 offset:0\n\t" \
  "ds_read_b128 v[164:167], v221 offset:1024\n\t" \
  "s_sub_i32 s91, s86, 2\n\t" \
  "s_max_i32 s88, s88, s91\n\t" \
  "v_mov_b32 v173, s88\n\t" \
  "buffer_store_dword v173, v174, %[rs], 0 offen sc1\n\t" \
  "v_mfma_f32_16x16x32_bf16 v[144:147], v[184:187], v[40:43], v[144:147]\n\t" \
  "v_mfma_f32_16x16x32_bf16 v[152:155], v[184:187], v[44:47], v[152:155]\n\t" \
  "ds_read_b128 v[184:187], v213 offset:32768\n\t" \
  "s_add_u32 m0, %[lw], 32768\n\t" \
  "v_mfma_f32_16x16x32_bf16 v[148:151], v[188:191], v[40:43], v[148:151]\n\t" \
  "v_mfma_f32_16x16x32_bf16 v[156:159], v[188:191], v[44:47], v[156:159]\n\t" \
  "ds_read_b128 v[188:191], v213 offset:49152\n\t" \
  "global_load_lds_dwordx4 v216, s[92:93] sc1\n\t" \
  "s_waitcnt lgkmcnt(8)\n\t" \
  "v_mfma_f32_16x16x32_bf16 v[144:147], v[192:195], v[48:51], v[144:147]\n\t" \
  "v_mfma_f32_16x16x32_bf16 v[152:155], v[192:195], v[52:55], v[152:155]\n\t" \
  "ds_read_b128 v[192:195], v214 offset:32768\n\t" \
  "v_mfma_f32_16x16x32_bf16 v[148:151], v[196:199], v[48:51], v[148:151]\n\t" \
  "v_mfma_f32_16x16x32_bf16 v[156:159], v[196:199], v[52:55], v[156:159]\n\t" \
  "ds_read_b128 v[196:199], v214 offset:49152\n\t" \
  "s_waitcnt lgkmcnt(4)\n\t" \
  "v_add_f32 v160, v160, v128\n\t" \
  "v_add_f32 v161, v161, v129\n\t" \
  "v_add_f32 v162, v162, v130\n\t" \
  "v_add_f32 v163, v163, v131\n\t" \
  "v_add_f32 v164, v164, v132\n\t" \
  "v_add_f32 v165, v165, v133\n\t" \
  "v_add_f32 v166, v166, v134\n\t" \
  "v_add_f32 v167, v167, v135\n\t" \
  "v_mfma_f32_16x16x32_bf16 v[144:147], v[200:203], v[56:59], v[144:147]\n\t" \
  "v_mfma_f32_16x16x32_bf16 v[152:155], v[200:203], v[60:63], v[152:155]\n\t" \
  "ds_read_b128 v[200:203], v215 offset:32768\n\t" \
  "s_add_u32 m0, %[lw], 33792\n\t" \
  "v_mfma_f32_16x16x32_bf16 v[148:151], v[204:207], v[56:59], v[148:151]\n\t" \
  "v_mfma_f32_16x16x32_bf16 v[156:159], v[204:207], v[60:63], v[156:159]\n\t" \
  "ds_read_b128 v[204:207], v215 offset:49152\n\t" \
  "global_load_lds_dwordx4 v217, s[92:93] sc1\n\t" \
  "v_cvt_pk_bf16_f32 v168, v160, v161\n\t" \
  "v_cvt_pk_bf16_f32 v169, v162, v163\n\t" \
  "v_cvt_pk_bf16_f32 v170, v164, v165\n\t" \
  "v_cvt_pk_bf16_f32 v171, v166, v167\n\t" \
  "buffer_store_dwordx4 v[168:171], v172, %[rt], s90 offen sc1\n\t" \
  "v_mfma_f32_16x16x32_bf16 v[144:147], v[176:179], v[64:67], v[144:147]\n\t" \
  "v_mfma_f32_16x16x32_bf16 v[152:155], v[176:179], v[68:71], v[152:155]\n\t" \
  "ds_read_b128 v[176:179], v212 offset:33024\n\t" \
  "v_mfma_f32_16x16x32_bf16 v[148:151], v[180:183], v[64:67], v[148:151]\n\t" \
  "v_mfma_f32_16x16x32_bf16 v[156:159], v[180:183], v[68:71], v[156:159]\n\t" \
  "ds_read_b128 v[180:183], v212 offset:49408\n\t" \
  "s_waitcnt lgkmcnt(6)\n\t" \
  "v_mfma_f32_16x16x32_bf16 v[144:147], v[184:187], v[72:75], v[144:147]\n\t" \
  "v_mfma_f32_16x16x32_bf16 v[152:155], v[184:187], v[76:79], v[152:155]\n\t" \
  "ds_read_b128 v[184:187], v213 offset:33024\n\t" \
  "s_add_u32 m0, %[lw], 34816\n\t" \
  "v_mfma_f32_16x16x32_bf16 v[148:151], v[188:191], v[72:75], v[148:151]\n\t" \
  "v_mfma_f32_16x16x32_bf16 v[156:159], v[188:191], v[76:79], v[156:159]\n\t" \
  "ds_read_b128 v[188:191], v213 offset:49408\n\t" \
  "global_load_lds_dwordx4 v218, s[92:93] sc1\n\t" \
  "s_waitcnt lgkmcnt(6)\n\t" \
  "v_mfma_f32_16x16x32_bf16 v[144:147], v[192:195], v[80:83], v[144:147]\n\t" \
  "v_mfma_f32_16x16x32_bf16 v[152:155], v[192:195], v[84:87], v[152:155]\n\t" \
  "ds_read_b128 v[192:195], v214 offset:33024\n\t" \
  "v_mfma_f32_16x16x32_bf16 v[148:151], v[196:199], v[80:83], v[148:151]\n\t" \
  "v_mfma_f32_16x16x32_bf16 v[156:159], v[196:199], v[84:87], v[156:159]\n\t" \
  "ds_read_b128 v[196:199], v214 offset:49408\n\t" \
  "s_waitcnt lgkmcnt(6)\n\t" \
  "v_mfma_f32_16x16x32_bf16 v[144:147], v[200:203], v[88:91], v[144:147]\n\t" \
  "v_mfma_f32_16x16x32_bf16 v[152:155], v[200:203], v[92:95], v[152:155]\n\t" \
  "ds_read_b128 v[200:203], v215 offset:33024\n\t" \
  "s_add_u32 m0, %[lw], 35840\n\t" \
  "v_mfma_f32_16x16x32_bf16 v[148:151], v[204:207], v[88:91], v[148:151]\n\t" \
  "v_mfma_f32_16x16x32_bf16 v[156:159], v[204:207], v[92:95], v[156:159]\n\t" \
  "ds_read_b128 v[204:207], v215 offset:49408\n\t" \
  "global_load_lds_dwordx4 v219, s[92:93] sc1\n\t" \
  "s_cmp_lt_u32 s86, 8\n\t" \
  "s_cbranch_scc1 .Lw31%=\n\t" \
  "s_waitcnt vmcnt(6)\n\t" \
  "s_barrier\n\t" \
  ".Lg31%=:\n\t" \
  "s_add_u32 s92, s92, 65024\n\t" \
  "s_addc_u32 s93, s93, 0\n\t" \
  "s_waitcnt lgkmcnt(6)\n\t" \
  "v_mfma_f32_16x16x32_bf16 v[144:147], v[176:179], v[96:99], v[144:147]\n\t" \
  "v_mfma_f32_16x16x32_bf16 v[152:155], v[176:179], v[100:103], v[152:155]\n\t" \
  "ds_read_b128 v[176:179], v208 offset:0\n\t" \
  "v_mfma_f32_16x16x32_bf16 v[148:151], v[180:183], v[96:99], v[148:151]\n\t" \
  "v_mfma_f32_16x16x32_bf16 v[156:159], v[180:183], v[100:103], v[156:159]\n\t" \
  "ds_read_b128 v[180:183], v208 offset:16384\n\t" \
  "s_waitcnt lgkmcnt(6)\n\t" \
  "v_mfma_f32_16x16x32_bf16 v[144:147], v[184:187], v[104:107], v[144:147]\n\t" \
  "v_mfma_f32_16x16x32_bf16 v[152:155], v[184:187], v[108:111], v[152:155]\n\t" \
  "ds_read_b128 v[184:187], v209 offset:0\n\t" \
  "s_add_u32 m0, %[lw], 65536\n\t" \
  "v_mfma_f32_16x16x32_bf16 v[148:151], v[188:191], v[104:107], v[148:151]\n\t" \
  "v_mfma_f32_16x16x32_bf16 v[156:159], v[188:191], v[108:111], v[156:159]\n\t" \
  "ds_read_b128 v[188:191], v209 offset:16384\n\t" \
  "global_load_lds_dwordx4 v216, s[92:93] sc1\n\t" \
  "s_waitcnt lgkmcnt(6)\n\t" \
  "v_mfma_f32_16x16x32_bf16 v[144:147], v[192:195], v[112:115], v[144:147]\n\t" \
  "v_mfma_f32_16x16x32_bf16 v[152:155], v[192:195], v[116:119], v[152:155]\n\t" \
  "ds_read_b128 v[192:195], v210 offset:0\n\t" \
  "v_mfma_f32_16x16x32_bf16 v[148:151], v[196:199], v[112:115], v[148:151]\n\t" \
  "v_mfma_f32_16x16x32_bf16 v[156:159], v[196:199], v[116:119], v[156:159]\n\t" \
  "ds_read_b128 v[196:199], v210 offset:16384\n\t" \
  "s_waitcnt lgkmcnt(6)\n\t" \
  "v_mfma_f32_16x16x32_bf16 v[144:147], v[200:203], v[120:123], v[144:147]\n\t" \
  "v_mfma_f32_16x16x32_bf16 v[152:155], v[200:203], v[124:127], v[152:155]\n\t" \
  "ds_read_b128 v[200:203], v211 offset:0\n\t" \
  "s_add_u32 m0, %[lw], 66560\n\t" \
  "v_mfma_f32_16x16x32_bf16 v[148:151], v[204:207], v[120:123], v[148:151]\n\t" \
  "v_mfma_f32_16x16x32_bf16 v[156:159], v[204:207], v[124:127], v[156:159]\n\t" \
  "ds_read_b128 v[204:207], v211 offset:16384\n\t" \
  "global_load_lds_dwordx4 v217, s[92:93] sc1\n\t" \
  "s_add_u32 s86, s86, 1\n\t" \
  "s_add_u32 s87, s87, 1\n\t" \
  "s_cmp_lt_u32 s86, %[end]\n\t" \
  "s_cbranch_scc1 .Lp1%=\n\t" \
  "s_branch .Lx1%=\n\t" \
  ".Lf01%=:\n\t" \
  "s_nop 7\n\t" \
  "ds_write_b128 v220, v[152:155] offset:0\n\t" \
  "ds_write_b128 v220, v[156:159] offset:1024\n\t" \
  "s_lshr_b32 s90, s87, 7\n\t" \
  "s_xor_b32 s90, s90, %[s0]\n\t" \
  "s_and_b32 s90, s90, 1\n\t" \
  "s_lshl1_add_u32 s90, s87, s90\n\t" \
  "s_lshl_b32 s90, s90, 13\n\t" \
  "s_cmp_lt_i32 s86, 1\n\t" \
  "s_cselect_b32 s90, 0x70000000, s90\n\t" \
  "s_waitcnt lgkmcnt(0)\n\t" \
  "s_barrier\n\t" \
  "ds_read_b128 v[160:163], v221 offset:0\n\t" \
  "ds_read_b128 v[164:167], v221 offset:1024\n\t" \
  "s_waitcnt lgkmcnt(0)\n\t" \
  "v_add_f32 v160, v160, v144\n\t" \
  "v_add_f32 v161, v161, v145\n\t" \
  "v_add_f32 v162, v162, v146\n\t" \
  "v_add_f32 v163, v163, v147\n\t" \
  "v_add_f32 v164, v164, v148\n\t" \
  "v_add_f32 v165, v165, v149\n\t" \
  "v_add_f32 v166, v166, v150\n\t" \
  "v_add_f32 v167, v167, v151\n\t" \
  "v_cvt_pk_bf16_f32 v168, v160, v161\n\t" \
  "v_cvt_pk_bf16_f32 v169, v162, v163\n\t" \
  "v_cvt_pk_bf16_f32 v170, v164, v165\n\t" \
  "v_cvt_pk_bf16_f32 v171, v166, v167\n\t" \
  "buffer_store_dwordx4 v[168:171], v172, %[rt], s90 offen sc1\n\t" \
  "s_branch .Lfb01%=\n\t" \
  ".Lf11%=:\n\t" \
  "s_nop 7\n\t" \
  "ds_write_b128 v220, v[136:139] offset:0\n\t" \
  "ds_write_b128 v220, v[140:143] offset:1024\n\t" \
  "s_lshr_b32 s90, s87, 7\n\t" \
  "s_xor_b32 s90, s90, %[s0]\n\t" \
  "s_and_b32 s90, s90, 1\n\t" \
  "s_lshl1_add_u32 s90, s87, s90\n\t" \
  "s_lshl_b32 s90, s90, 13\n\t" \
  "s_cmp_lt_i32 s86, 1\n\t" \
  "s_cselect_b32 s90, 0x70000000, s90\n\t" \
  "s_waitcnt lgkmcnt(0)\n\t" \
  "s_barrier\n\t" \
  "ds_read_b128 v[160:163], v221 offset:0\n\t" \
  "ds_read_b128 v[164:167], v221 offset:1024\n\t" \
  "s_waitcnt lgkmcnt(0)\n\t" \
  "v_add_f32 v160, v160, v128\n\t" \
  "v_add_f32 v161, v161, v129\n\t" \
  "v_add_f32 v162, v162, v130\n\t" \
  "v_add_f32 v163, v163, v131\n\t" \
  "v_add_f32 v164, v164, v132\n\t" \
  "v_add_f32 v165, v165, v133\n\t" \
  "v_add_f32 v166, v166, v134\n\t" \
  "v_add_f32 v167, v167, v135\n\t" \
  "v_cvt_pk_bf16_f32 v168, v160, v161\n\t" \
  "v_cvt_pk_bf16_f32 v169, v162, v163\n\t" \
  "v_cvt_pk_bf16_f32 v170, v164, v165\n\t" \
  "v_cvt_pk_bf16_f32 v171, v166, v167\n\t" \
  "buffer_store_dwordx4 v[168:171], v172, %[rt], s90 offen sc1\n\t" \
  "s_branch .Lfb11%=\n\t" \
  ".Lw01%=:\n\t" \
  "s_waitcnt vmcnt(2)\n\t" \
  "s_barrier\n\t" \
  "s_sub_i32 s91, s86, 1\n\t" \
  "s_max_i32 s88, s88, s91\n\t" \
  "v_mov_b32 v173, s88\n\t" \
  "buffer_store_dword v173, v174, %[rs], 0 offen sc1\n\t" \
  "s_branch .Lg01%=\n\t" \
  ".Lw11%=:\n\t" \
  "s_waitcnt vmcnt(2)\n\t" \
  "s_barrier\n\t" \
  "s_sub_i32 s91, s86, 1\n\t" \
  "s_max_i32 s88, s88, s91\n\t" \
  "v_mov_b32 v173, s88\n\t" \
  "buffer_store_dword v173, v174, %[rs], 0 offen sc1\n\t" \
  "s_branch .Lg11%=\n\t" \
  ".Lw21%=:\n\t" \
  "s_waitcnt vmcnt(2)\n\t" \
  "s_barrier\n\t" \
  "s_sub_i32 s91, s86, 1\n\t" \
  "s_max_i32 s88, s88, s91\n\t" \
  "v_mov_b32 v173, s88\n\t" \
  "buffer_store_dword v173, v174, %[rs], 0 offen sc1\n\t" \
  "s_branch .Lg21%=\n\t" \
  ".Lw31%=:\n\t" \
  "s_waitcnt vmcnt(2)\n\t" \
  "s_barrier\n\t" \
  "s_sub_i32 s91, s86, 1\n\t" \
  "s_max_i32 s88, s88, s91\n\t" \
  "v_mov_b32 v173, s88\n\t" \
  "buffer_store_dword v173, v174, %[rs], 0 offen sc1\n\t" \
  "s_branch .Lg31%=\n\t" \
  ".Lx1%=:\n\t" \
  "s_waitcnt lgkmcnt(0)\n\t" \
  "s_setprio 0\n\t" \
  "s_mov_b32 m0, s94\n\t" \
  ".Le%=:\n\t"
#define PP_ASM_0 \
  "s_cmp_lg_u32 %[kh], 0\n\t" \
  "s_cbranch_scc1 .Lk%=\n\t" \
  "s_mov_b32 s94, m0\n\t" \
  "s_mov_b64 s[92:93], %[x0]\n\t" \
  "s_mov_b32 s86, %[g0]\n\t" \
  "s_add_u32 s87, s86, %[t0]\n\t" \
  "s_add_u32 m0, %[lw], 67584\n\t" \
  "s_nop 0\n\t" \
  "global_load_lds_dwordx4 v250, s[92:93] sc1\n\t" \
  "s_add_u32 m0, %[lw], 68608\n\t" \
  "s_nop 0\n\t" \
  "global_load_lds_dwordx4 v251, s[92:93] sc1\n\t" \
  "s_barrier\n\t" \
  "s_branch .Ls0%=\n\t" \
  ".Lp0%=:\n\t" \
  "s_waitcnt vmcnt(9)\n\t" \
  "s_barrier\n\t" \
  ".Ls0%=:\n\t" \
  "s_add_u32 s92, s92, 512\n\t" \
  "s_addc_u32 s93, s93, 0\n\t" \
  "ds_read_b128 v[176:179], v240 offset:0\n\t" \
  "ds_read_b128 v[180:183], v240 offset:16384\n\t" \
  "ds_read_b128 v[184:187], v241 offset:0\n\t" \
  "ds_read_b128 v[188:191], v241 offset:16384\n\t" \
  "s_add_u32 m0, %[lw], 98304\n\t" \
  "s_nop 0\n\t" \
  "global_load_lds_dwordx4 v248, s[92:93] sc1\n\t" \
  "ds_read_b128 v[192:195], v242 offset:0\n\t" \
  "ds_read_b128 v[196:199], v242 offset:16384\n\t" \
  "ds_read_b128 v[200:203], v243 offset:0\n\t" \
  "ds_read_b128 v[204:207], v243 offset:16384\n\t" \
  "s_add_u32 m0, %[lw], 99328\n\t" \
  "s_nop 0\n\t" \
  "global_load_lds_dwordx4 v249, s[92:93] sc1\n\t" \
  "ds_read_b128 v[208:211], v240 offset:256\n\t" \
  "ds_read_b128 v[212:215], v240 offset:16640\n\t" \
  "ds_read_b128 v[216:219], v241 offset:256\n\t" \
  "ds_read_b128 v[220:223], v241 offset:16640\n\t" \
  "s_add_u32 m0, %[lw], 100352\n\t" \
  "s_nop 0\n\t" \
  "global_load_lds_dwordx4 v250, s[92:93] sc1\n\t" \
  "ds_read_b128 v[224:227], v242 offset:256\n\t" \
  "ds_read_b128 v[228:231], v242 offset:16640\n\t" \
  "ds_read_b128 v[232:235], v243 offset:256\n\t" \
  "ds_read_b128 v[236:239], v243 offset:16640\n\t" \
  "s_add_u32 m0, %[lw], 101376\n\t" \
  "s_nop 0\n\t" \
  "global_load_lds_dwordx4 v251, s[92:93] sc1\n\t" \
  "ds_write_b128 v252, v[152:155] offset:0\n\t" \
  "ds_write_b128 v252, v[156:159] offset:1024\n\t" \
  "s_waitcnt lgkmcnt(0)\n\t" \
  "s_setprio 1\n\t" \
  "v_mfma_f32_16x16x32_bf16 v[128:131], v[0:3], v[176:179], 0\n\t" \
  "v_mfma_f32_16x16x32_bf16 v[132:135], v[4:7], v[176:179], 0\n\t" \
  "v_mfma_f32_16x16x32_bf16 v[136:139], v[0:3], v[180:183], 0\n\t" \
  "v_mfma_f32_16x16x32_bf16 v[140:143], v[4:7], v[180:183], 0\n\t" \
  "v_mfma_f32_16x16x32_bf16 v[128:131], v[8:11], v[184:187], v[128:131]\n\t" \
  "v_mfma_f32_16x16x32_bf16 v[132:135], v[12:15], v[184:187], v[132:135]\n\t" \
  "v_mfma_f32_16x16x32_bf16 v[136:139], v[8:11], v[188:191], v[136:139]\n\t" \
  "v_mfma_f32_16x16x32_bf16 v[140:143], v[12:15], v[188:191], v[140:143]\n\t" \
  "v_mfma_f32_16x16x32_bf16 v[128:131], v[16:19], v[192:195], v[128:131]\n\t" \
  "v_mfma_f32_16x16x32_bf16 v[132:135], v[20:23], v[192:195], v[132:135]\n\t" \
  "v_mfma_f32_16x16x32_bf16 v[136:139], v[16:19], v[196:199], v[136:139]\n\t" \
  "v_mfma_f32_16x16x32_bf16 v[140:143], v[20:23], v[196:199], v[140:143]\n\t" \
  "v_mfma_f32_16x16x32_bf16 v[128:131], v[24:27], v[200:203], v[128:131]\n\t" \
  "v_mfma_f32_16x16x32_bf16 v[132:135], v[28:31], v[200:203], v[132:135]\n\t" \
  "v_mfma_f32_16x16x32_bf16 v[136:139], v[24:27], v[204:207], v[136:139]\n\t" \
  "v_mfma_f32_16x16x32_bf16 v[140:143], v[28:31], v[204:207], v[140:143]\n\t" \
  "v_mfma_f32_16x16x32_bf16 v[128:131], v[32:35], v[208:211], v[128:131]\n\t" \
  "v_mfma_f32_16x16x32_bf16 v[132:135], v[36:39], v[208:211], v[132:135]\n\t" \
  "v_mfma_f32_16x16x32_bf16 v[136:139], v[32:35], v[212:215], v[136:139]\n\t" \
  "v_mfma_f32_16x16x32_bf16 v[140:143], v[36:39], v[212:215], v[140:143]\n\t" \
  "v_mfma_f32_16x16x32_bf16 v[128:131], v[40:43], v[216:219], v[128:131]\n\t" \
  "v_mfma_f32_16x16x32_bf16 v[132:135], v[44:47], v[216:219], v[132:135]\n\t" \
  "v_mfma_f32_16x16x32_bf16 v[136:139], v[40:43], v[220:223], v[136:139]\n\t" \
  "v_mfma_f32_16x16x32_bf16 v[140:143], v[44:47], v[220:223], v[140:143]\n\t" \
  "v_mfma_f32_16x16x32_bf16 v[128:131], v[48:51], v[224:227], v[128:131]\n\t" \
  "v_mfma_f32_16x16x32_bf16 v[132:135], v[52:55], v[224:227], v[132:135]\n\t" \
  "v_mfma_f32_16x16x32_bf16 v[136:139], v[48:51], v[228:231], v[136:139]\n\t" \
  "v_mfma_f32_16x16x32_bf16 v[140:143], v[52:55], v[228:231], v[140:143]\n\t" \
  "v_mfma_f32_16x16x32_bf16 v[128:131], v[56:59], v[232:235], v[128:131]\n\t" \
  "v_mfma_f32_16x16x32_bf16 v[132:135], v[60:63], v[232:235], v[132:135]\n\t" \
  "v_mfma_f32_16x16x32_bf16 v[136:139], v[56:59], v[236:239], v[136:139]\n\t" \
  "v_mfma_f32_16x16x32_bf16 v[140:143], v[60:63], v[236:239], v[140:143]\n\t" \
  "s_setprio 0\n\t" \
  "s_waitcnt vmcnt(4)\n\t" \
  "s_barrier\n\t" \
  "s_add_u32 s92, s92, 65024\n\t" \
  "s_addc_u32 s93, s93, 0\n\t" \
  "ds_read_b128 v[160:163], v253 offset:0\n\t" \
  "ds_read_b128 v[164:167], v253 offset:1024\n\t" \
  "ds_read_b128 v[176:179], v240 offset:32768\n\t" \
  "ds_read_b128 v[180:183], v240 offset:49152\n\t" \
  "ds_read_b128 v[184:187], v241 offset:32768\n\t" \
  "ds_read_b128 v[188:191], v241 offset:49152\n\t" \
  "s_add_u32 m0, %[lw], 0\n\t" \
  "s_nop 0\n\t" \
  "global_load_lds_dwordx4 v248, s[92:93] sc1\n\t" \
  "ds_read_b128 v[192:195], v242 offset:32768\n\t" \
  "ds_read_b128 v[196:199], v242 offset:49152\n\t" \
  "ds_read_b128 v[200:203], v243 offset:32768\n\t" \
  "ds_read_b128 v[204:207], v243 offset:49152\n\t" \
  "s_add_u32 m0, %[lw], 1024\n\t" \
  "s_nop 0\n\t" \
  "global_load_lds_dwordx4 v249, s[92:93] sc1\n\t" \
  "ds_read_b128 v[208:211], v240 offset:33024\n\t" \
  "ds_read_b128 v[212:215], v240 offset:49408\n\t" \
  "ds_read_b128 v[216:219], v241 offset:33024\n\t" \
  "ds_read_b128 v[220:223], v241 offset:49408\n\t" \
  "s_add_u32 m0, %[lw], 2048\n\t" \
  "s_nop 0\n\t" \
  "global_load_lds_dwordx4 v250, s[92:93] sc1\n\t" \
  "ds_read_b128 v[224:227], v242 offset:33024\n\t" \
  "ds_read_b128 v[228:231], v242 offset:49408\n\t" \
  "ds_read_b128 v[232:235], v243 offset:33024\n\t" \
  "ds_read_b128 v[236:239], v243 offset:49408\n\t" \
  "s_add_u32 m0, %[lw], 3072\n\t" \
  "s_nop 0\n\t" \
  "global_load_lds_dwordx4 v251, s[92:93] sc1\n\t" \
  "s_waitcnt lgkmcnt(15)\n\t" \
  "s_waitcnt lgkmcnt(12)\n\t" \
  "s_lshr_b32 s90, s87, 7\n\t" \
  "s_xor_b32 s90, s90, %[s0]\n\t" \
  "s_and_b32 s90, s90, 1\n\t" \
  "s_lshl1_add_u32 s90, s87, s90\n\t" \
  "s_lshl_b32 s90, s90, 13\n\t" \
  "v_add_f32 v160, v160, v144\n\t" \
  "v_add_f32 v161, v161, v145\n\t" \
  "v_add_f32 v162, v162, v146\n\t" \
  "v_add_f32 v163, v163, v147\n\t" \
  "v_add_f32 v164, v164, v148\n\t" \
  "v_add_f32 v165, v165, v149\n\t" \
  "v_add_f32 v166, v166, v150\n\t" \
  "v_add_f32 v167, v167, v151\n\t" \
  "v_cvt_pk_bf16_f32 v168, v160, v161\n\t" \
  "v_cvt_pk_bf16_f32 v169, v162, v163\n\t" \
  "v_cvt_pk_bf16_f32 v170, v164, v165\n\t" \
  "v_cvt_pk_bf16_f32 v171, v166, v167\n\t" \
  "buffer_store_dwordx4 v[168:171], v172, %[rt], s90 offen sc1\n\t" \
  "s_add_u32 s86, s86, 1\n\t" \
  "s_add_u32 s87, s87, 1\n\t" \
  "s_waitcnt lgkmcnt(0)\n\t" \
  "s_setprio 1\n\t" \
  "v_mfma_f32_16x16x32_bf16 v[128:131], v[64:67], v[176:179], v[128:131]\n\t" \
  "v_mfma_f32_16x16x32_bf16 v[132:135], v[68:71], v[176:179], v[132:135]\n\t" \
  "v_mfma_f32_16x16x32_bf16 v[136:139], v[64:67], v[180:183], v[136:139]\n\t" \
  "v_mfma_f32_16x16x32_bf16 v[140:143], v[68:71], v[180:183], v[140:143]\n\t" \
  "v_mfma_f32_16x16x32_bf16 v[128:131], v[72:75], v[184:187], v[128:131]\n\t" \
  "v_mfma_f32_16x16x32_bf16 v[132:135], v[76:79], v[184:187], v[132:135]\n\t" \
  "v_mfma_f32_16x16x32_bf16 v[136:139], v[72:75], v[188:191], v[136:139]\n\t" \
  "v_mfma_f32_16x16x32_bf16 v[140:143], v[76:79], v[188:191], v[140:143]\n\t" \
  "v_mfma_f32_16x16x32_bf16 v[128:131], v[80:83], v[192:195], v[128:131]\n\t" \
  "v_mfma_f32_16x16x32_bf16 v[132:135], v[84:87], v[192:195], v[132:135]\n\t" \
  "v_mfma_f32_16x16x32_bf16 v[136:139], v[80:83], v[196:199], v[136:139]\n\t" \
  "v_mfma_f32_16x16x32_bf16 v[140:143], v[84:87], v[196:199], v[140:143]\n\t" \
  "v_mfma_f32_16x16x32_bf16 v[128:131], v[88:91], v[200:203], v[128:131]\n\t" \
  "v_mfma_f32_16x16x32_bf16 v[132:135], v[92:95], v[200:203], v[132:135]\n\t" \
  "v_mfma_f32_16x16x32_bf16 v[136:139], v[88:91], v[204:207], v[136:139]\n\t" \
  "v_mfma_f32_16x16x32_bf16 v[140:143], v[92:95], v[204:207], v[140:143]\n\t" \
  "v_mfma_f32_16x16x32_bf16 v[128:131], v[96:99], v[208:211], v[128:131]\n\t" \
  "v_mfma_f32_16x16x32_bf16 v[132:135], v[100:103], v[208:211], v[132:135]\n\t" \
  "v_mfma_f32_16x16x32_bf16 v[136:139], v[96:99], v[212:215], v[136:139]\n\t" \
  "v_mfma_f32_16x16x32_bf16 v[140:143], v[100:103], v[212:215], v[140:143]\n\t" \
  "v_mfma_f32_16x16x32_bf16 v[128:131], v[104:107], v[216:219], v[128:131]\n\t" \
  "v_mfma_f32_16x16x32_bf16 v[132:135], v[108:111], v[216:219], v[132:135]\n\t" \
  "v_mfma_f32_16x16x32_bf16 v[136:139], v[104:107], v[220:223], v[136:139]\n\t" \
  "v_mfma_f32_16x16x32_bf16 v[140:143], v[108:111], v[220:223], v[140:143]\n\t" \
  "v_mfma_f32_16x16x32_bf16 v[128:131], v[112:115], v[224:227], v[128:131]\n\t" \
  "v_mfma_f32_16x16x32_bf16 v[132:135], v[116:119], v[224:227], v[132:135]\n\t" \
  "v_mfma_f32_16x16x32_bf16 v[136:139], v[112:115], v[228:231], v[136:139]\n\t" \
  "v_mfma_f32_16x16x32_bf16 v[140:143], v[116:119], v[228:231], v[140:143]\n\t" \
  "v_mfma_f32_16x16x32_bf16 v[128:131], v[120:123], v[232:235], v[128:131]\n\t" \
  "v_mfma_f32_16x16x32_bf16 v[132:135], v[124:127], v[232:235], v[132:135]\n\t" \
  "v_mfma_f32_16x16x32_bf16 v[136:139], v[120:123], v[236:239], v[136:139]\n\t" \
  "v_mfma_f32_16x16x32_bf16 v[140:143], v[124:127], v[236:239], v[140:143]\n\t" \
  "s_setprio 0\n\t" \
  "s_waitcnt vmcnt(9)\n\t" \
  "s_barrier\n\t" \
  "s_add_u32 s92, s92, 512\n\t" \
  "s_addc_u32 s93, s93, 0\n\t" \
  "ds_read_b128 v[176:179], v244 offset:0\n\t" \
  "ds_read_b128 v[180:183], v244 offset:16384\n\t" \
  "ds_read_b128 v[184:187], v245 offset:0\n\t" \
  "ds_read_b128 v[188:191], v245 offset:16384\n\t" \
  "s_add_u32 m0, %[lw], 32768\n\t" \
  "s_nop 0\n\t" \
  "global_load_lds_dwordx4 v248, s[92:93] sc1\n\t" \
  "ds_read_b128 v[192:195], v246 offset:0\n\t" \
  "ds_read_b128 v[196:199], v246 offset:16384\n\t" \
  "ds_read_b128 v[200:203], v247 offset:0\n\t" \
  "ds_read_b128 v[204:207], v247 offset:16384\n\t" \
  "s_add_u32 m0, %[lw], 33792\n\t" \
  "s_nop 0\n\t" \
  "global_load_lds_dwordx4 v249, s[92:93] sc1\n\t" \
  "ds_read_b128 v[208:211], v244 offset:256\n\t" \
  "ds_read_b128 v[212:215], v244 offset:16640\n\t" \
  "ds_read_b128 v[216:219], v245 offset:256\n\t" \
  "ds_read_b128 v[220:223], v245 offset:16640\n\t" \
  "s_add_u32 m0, %[lw], 34816\n\t" \
  "s_nop 0\n\t" \
  "global_load_lds_dwordx4 v250, s[92:93] sc1\n\t" \
  "ds_read_b128 v[224:227], v246 offset:256\n\t" \
  "ds_read_b128 v[228:231], v246 offset:16640\n\t" \
  "ds_read_b128 v[232:235], v247 offset:256\n\t" \
  "ds_read_b128 v[236:239], v247 offset:16640\n\t" \
  "s_add_u32 m0, %[lw], 35840\n\t" \
  "s_nop 0\n\t" \
  "global_load_lds_dwordx4 v251, s[92:93] sc1\n\t" \
  "ds_write_b128 v252, v[136:139] offset:0\n\t" \
  "ds_write_b128 v252, v[140:143] offset:1024\n\t" \
  "s_waitcnt lgkmcnt(0)\n\t" \
  "s_setprio 1\n\t" \
  "v_mfma_f32_16x16x32_bf16 v[144:147], v[0:3], v[176:179], 0\n\t" \
  "v_mfma_f32_16x16x32_bf16 v[148:151], v[4:7], v[176:179], 0\n\t" \
  "v_mfma_f32_16x16x32_bf16 v[152:155], v[0:3], v[180:183], 0\n\t" \
  "v_mfma_f32_16x16x32_bf16 v[156:159], v[4:7], v[180:183], 0\n\t" \
  "v_mfma_f32_16x16x32_bf16 v[144:147], v[8:11], v[184:187], v[144:147]\n\t" \
  "v_mfma_f32_16x16x32_bf16 v[148:151], v[12:15], v[184:187], v[148:151]\n\t" \
  "v_mfma_f32_16x16x32_bf16 v[152:155], v[8:11], v[188:191], v[152:155]\n\t" \
  "v_mfma_f32_16x16x32_bf16 v[156:159], v[12:15], v[188:191], v[156:159]\n\t" \
  "v_mfma_f32_16x16x32_bf16 v[144:147], v[16:19], v[192:195], v[144:147]\n\t" \
  "v_mfma_f32_16x16x32_bf16 v[148:151], v[20:23], v[192:195], v[148:151]\n\t" \
  "v_mfma_f32_16x16x32_bf16 v[152:155], v[16:19], v[196:199], v[152:155]\n\t" \
  "v_mfma_f32_16x16x32_bf16 v[156:159], v[20:23], v[196:199], v[156:159]\n\t" \
  "v_mfma_f32_16x16x32_bf16 v[144:147], v[24:27], v[200:203], v[144:147]\n\t" \
  "v_mfma_f32_16x16x32_bf16 v[148:151], v[28:31], v[200:203], v[148:151]\n\t" \
  "v_mfma_f32_16x16x32_bf16 v[152:155], v[24:27], v[204:207], v[152:155]\n\t" \
  "v_mfma_f32_16x16x32_bf16 v[156:159], v[28:31], v[204:207], v[156:159]\n\t" \
  "v_mfma_f32_16x16x32_bf16 v[144:147], v[32:35], v[208:211], v[144:147]\n\t" \
  "v_mfma_f32_16x16x32_bf16 v[148:151], v[36:39], v[208:211], v[148:151]\n\t" \
  "v_mfma_f32_16x16x32_bf16 v[152:155], v[32:35], v[212:215], v[152:155]\n\t" \
  "v_mfma_f32_16x16x32_bf16 v[156:159], v[36:39], v[212:215], v[156:159]\n\t" \
  "v_mfma_f32_16x16x32_bf16 v[144:147], v[40:43], v[216:219], v[144:147]\n\t" \
  "v_mfma_f32_16x16x32_bf16 v[148:151], v[44:47], v[216:219], v[148:151]\n\t" \
  "v_mfma_f32_16x16x32_bf16 v[152:155], v[40:43], v[220:223], v[152:155]\n\t" \
  "v_mfma_f32_16x16x32_bf16 v[156:159], v[44:47], v[220:223], v[156:159]\n\t" \
  "v_mfma_f32_16x16x32_bf16 v[144:147], v[48:51], v[224:227], v[144:147]\n\t" \
  "v_mfma_f32_16x16x32_bf16 v[148:151], v[52:55], v[224:227], v[148:151]\n\t" \
  "v_mfma_f32_16x16x32_bf16 v[152:155], v[48:51], v[228:231], v[152:155]\n\t" \
  "v_mfma_f32_16x16x32_bf16 v[156:159], v[52:55], v[228:231], v[156:159]\n\t" \
  "v_mfma_f32_16x16x32_bf16 v[144:147], v[56:59], v[232:235], v[144:147]\n\t" \
  "v_mfma_f32_16x16x32_bf16 v[148:151], v[60:63], v[232:235], v[148:151]\n\t" \
  "v_mfma_f32_16x16x32_bf16 v[152:155], v[56:59], v[236:239], v[152:155]\n\t" \
  "v_mfma_f32_16x16x32_bf16 v[156:159], v[60:63], v[236:239], v[156:159]\n\t" \
  "s_setprio 0\n\t" \
  "s_waitcnt vmcnt(4)\n\t" \
  "s_barrier\n\t" \
  "s_add_u32 s92, s92, 65024\n\t" \
  "s_addc_u32 s93, s93, 0\n\t" \
  "ds_read_b128 v[160:163], v253 offset:0\n\t" \
  "ds_read_b128 v[164:167], v253 offset:1024\n\t" \
  "ds_read_b128 v[176:179], v244 offset:32768\n\t" \
  "ds_read_b128 v[180:183], v244 offset:49152\n\t" \
  "ds_read_b128 v[184:187], v245 offset:32768\n\t" \
  "ds_read_b128 v[188:191], v245 offset:49152\n\t" \
  "s_add_u32 m0, %[lw], 65536\n\t" \
  "s_nop 0\n\t" \
  "global_load_lds_dwordx4 v248, s[92:93] sc1\n\t" \
  "ds_read_b128 v[192:195], v246 offset:32768\n\t" \
  "ds_read_b128 v[196:199], v246 offset:49152\n\t" \
  "ds_read_b128 v[200:203], v247 offset:32768\n\t" \
  "ds_read_b128 v[204:207], v247 offset:49152\n\t" \
  "s_add_u32 m0, %[lw], 66560\n\t" \
  "s_nop 0\n\t" \
  "global_load_lds_dwordx4 v249, s[92:93] sc1\n\t" \
  "ds_read_b128 v[208:211], v244 offset:33024\n\t" \
  "ds_read_b128 v[212:215], v244 offset:49408\n\t" \
  "ds_read_b128 v[216:219], v245 offset:33024\n\t" \
  "ds_read_b128 v[220:223], v245 offset:49408\n\t" \
  "s_add_u32 m0, %[lw], 67584\n\t" \
  "s_nop 0\n\t" \
  "global_load_lds_dwordx4 v250, s[92:93] sc1\n\t" \
  "ds_read_b128 v[224:227], v246 offset:33024\n\t" \
  "ds_read_b128 v[228:231], v246 offset:49408\n\t" \
  "ds_read_b128 v[232:235], v247 offset:33024\n\t" \
  "ds_read_b128 v[236:239], v247 offset:49408\n\t" \
  "s_add_u32 m0, %[lw], 68608\n\t" \
  "s_nop 0\n\t" \
  "global_load_lds_dwordx4 v251, s[92:93] sc1\n\t" \
  "s_waitcnt lgkmcnt(15)\n\t" \
  "s_waitcnt lgkmcnt(12)\n\t" \
  "s_lshr_b32 s90, s87, 7\n\t" \
  "s_xor_b32 s90, s90, %[s0]\n\t" \
  "s_and_b32 s90, s90, 1\n\t" \
  "s_lshl1_add_u32 s90, s87, s90\n\t" \
  "s_lshl_b32 s90, s90, 13\n\t" \
  "v_add_f32 v160, v160, v128\n\t" \
  "v_add_f32 v161, v161, v129\n\t" \
  "v_add_f32 v162, v162, v130\n\t" \
  "v_add_f32 v163, v163, v131\n\t" \
  "v_add_f32 v164, v164, v132\n\t" \
  "v_add_f32 v165, v165, v133\n\t" \
  "v_add_f32 v166, v166, v134\n\t" \
  "v_add_f32 v167, v167, v135\n\t" \
  "v_cvt_pk_bf16_f32 v168, v160, v161\n\t" \
  "v_cvt_pk_bf16_f32 v169, v162, v163\n\t" \
  "v_cvt_pk_bf16_f32 v170, v164, v165\n\t" \
  "v_cvt_pk_bf16_f32 v171, v166, v167\n\t" \
  "buffer_store_dwordx4 v[168:171], v172, %[rt], s90 offen sc1\n\t" \
  "s_add_u32 s86, s86, 1\n\t" \
  "s_add_u32 s87, s87, 1\n\t" \
  "s_waitcnt lgkmcnt(0)\n\t" \
  "s_setprio 1\n\t" \
  "v_mfma_f32_16x16x32_bf16 v[144:147], v[64:67], v[176:179], v[144:147]\n\t" \
  "v_mfma_f32_16x16x32_bf16 v[148:151], v[68:71], v[176:179], v[148:151]\n\t" \
  "v_mfma_f32_16x16x32_bf16 v[152:155], v[64:67], v[180:183], v[152:155]\n\t" \
  "v_mfma_f32_16x16x32_bf16 v[156:159], v[68:71], v[180:183], v[156:159]\n\t" \
  "v_mfma_f32_16x16x32_bf16 v[144:147], v[72:75], v[184:187], v[144:147]\n\t" \
  "v_mfma_f32_16x16x32_bf16 v[148:151], v[76:79], v[184:187], v[148:151]\n\t" \
  "v_mfma_f32_16x16x32_bf16 v[152:155], v[72:75], v[188:191], v[152:155]\n\t" \
  "v_mfma_f32_16x16x32_bf16 v[156:159], v[76:79], v[188:191], v[156:159]\n\t" \
  "v_mfma_f32_16x16x32_bf16 v[144:147], v[80:83], v[192:195], v[144:147]\n\t" \
  "v_mfma_f32_16x16x32_bf16 v[148:151], v[84:87], v[192:195], v[148:151]\n\t" \
  "v_mfma_f32_16x16x32_bf16 v[152:155], v[80:83], v[196:199], v[152:155]\n\t" \
  "v_mfma_f32_16x16x32_bf16 v[156:159], v[84:87], v[196:199], v[156:159]\n\t" \
  "v_mfma_f32_16x16x32_bf16 v[144:147], v[88:91], v[200:203], v[144:147]\n\t" \
  "v_mfma_f32_16x16x32_bf16 v[148:151], v[92:95], v[200:203], v[148:151]\n\t" \
  "v_mfma_f32_16x16x32_bf16 v[152:155], v[88:91], v[204:207], v[152:155]\n\t" \
  "v_mfma_f32_16x16x32_bf16 v[156:159], v[92:95], v[204:207], v[156:159]\n\t" \
  "v_mfma_f32_16x16x32_bf16 v[144:147], v[96:99], v[208:211], v[144:147]\n\t" \
  "v_mfma_f32_16x16x32_bf16 v[148:151], v[100:103], v[208:211], v[148:151]\n\t" \
  "v_mfma_f32_16x16x32_bf16 v[152:155], v[96:99], v[212:215], v[152:155]\n\t" \
  "v_mfma_f32_16x16x32_bf16 v[156:159], v[100:103], v[212:215], v[156:159]\n\t" \
  "v_mfma_f32_16x16x32_bf16 v[144:147], v[104:107], v[216:219], v[144:147]\n\t" \
  "v_mfma_f32_16x16x32_bf16 v[148:151], v[108:111], v[216:219], v[148:151]\n\t" \
  "v_mfma_f32_16x16x32_bf16 v[152:155], v[104:107], v[220:223], v[152:155]\n\t" \
  "v_mfma_f32_16x16x32_bf16 v[156:159], v[108:111], v[220:223], v[156:159]\n\t" \
  "v_mfma_f32_16x16x32_bf16 v[144:147], v[112:115], v[224:227], v[144:147]\n\t" \
  "v_mfma_f32_16x16x32_bf16 v[148:151], v[116:119], v[224:227], v[148:151]\n\t" \
  "v_mfma_f32_16x16x32_bf16 v[152:155], v[112:115], v[228:231], v[152:155]\n\t" \
  "v_mfma_f32_16x16x32_bf16 v[156:159], v[116:119], v[228:231], v[156:159]\n\t" \
  "v_mfma_f32_16x16x32_bf16 v[144:147], v[120:123], v[232:235], v[144:147]\n\t" \
  "v_mfma_f32_16x16x32_bf16 v[148:151], v[124:127], v[232:235], v[148:151]\n\t" \
  "v_mfma_f32_16x16x32_bf16 v[152:155], v[120:123], v[236:239], v[152:155]\n\t" \
  "v_mfma_f32_16x16x32_bf16 v[156:159], v[124:127], v[236:239], v[156:159]\n\t" \
  "s_setprio 0\n\t" \
  "s_cmp_lt_u32 s86, 64\n\t" \
  "s_cbranch_scc1 .Lp0%=\n\t" \
  "s_barrier\n\t" \
  "s_nop 2\n\t" \
  "ds_write_b128 v252, v[152:155] offset:0\n\t" \
  "ds_write_b128 v252, v[156:159] offset:1024\n\t" \
  "s_waitcnt lgkmcnt(0)\n\t" \
  "s_barrier\n\t" \
  "ds_read_b128 v[160:163], v253 offset:0\n\t" \
  "ds_read_b128 v[164:167], v253 offset:1024\n\t" \
  "s_waitcnt lgkmcnt(0)\n\t" \
  "s_lshr_b32 s90, s87, 7\n\t" \
  "s_xor_b32 s90, s90, %[s0]\n\t" \
  "s_and_b32 s90, s90, 1\n\t" \
  "s_lshl1_add_u32 s90, s87, s90\n\t" \
  "s_lshl_b32 s90, s90, 13\n\t" \
  "v_add_f32 v160, v160, v144\n\t" \
  "v_add_f32 v161, v161, v145\n\t" \
  "v_add_f32 v162, v162, v146\n\t" \
  "v_add_f32 v163, v163, v147\n\t" \
  "v_add_f32 v164, v164, v148\n\t" \
  "v_add_f32 v165, v165, v149\n\t" \
  "v_add_f32 v166, v166, v150\n\t" \
  "v_add_f32 v167, v167, v151\n\t" \
  "v_cvt_pk_bf16_f32 v168, v160, v161\n\t" \
  "v_cvt_pk_bf16_f32 v169, v162, v163\n\t" \
  "v_cvt_pk_bf16_f32 v170, v164, v165\n\t" \
  "v_cvt_pk_bf16_f32 v171, v166, v167\n\t" \
  "buffer_store_dwordx4 v[168:171], v172, %[rt], s90 offen sc1\n\t" \
  "s_waitcnt vmcnt(0)\n\t" \
  "s_barrier\n\t" \
  "s_mov_b32 m0, s94\n\t" \
  "s_branch .Le%=\n\t" \
  ".Lk%=:\n\t" \
  "s_mov_b32 s94, m0\n\t" \
  "s_mov_b64 s[92:93], %[x0]\n\t" \
  "s_mov_b32 s86, %[g0]\n\t" \
  "s_add_u32 s87, s86, %[t0]\n\t" \
  "s_add_u32 m0, %[lw], 67584\n\t" \
  "s_nop 0\n\t" \
  "global_load_lds_dwordx4 v250, s[92:93] sc1\n\t" \
  "s_add_u32 m0, %[lw], 68608\n\t" \
  "s_nop 0\n\t" \
  "global_load_lds_dwordx4 v251, s[92:93] sc1\n\t" \
  "s_barrier\n\t" \
  "s_branch .Ls1%=\n\t" \
  ".Lp1%=:\n\t" \
  "s_waitcnt vmcnt(10)\n\t" \
  "s_barrier\n\t" \
  "s_setprio 2\n\t" \
  "v_mfma_f32_16x16x32_bf16 v[144:147], v[64:67], v[176:179], v[144:147]\n\t" \
  "v_mfma_f32_16x16x32_bf16 v[148:151], v[68:71], v[176:179], v[148:151]\n\t" \
  "v_mfma_f32_16x16x32_bf16 v[152:155], v[64:67], v[180:183], v[152:155]\n\t" \
  "v_mfma_f32_16x16x32_bf16 v[156:159], v[68:71], v[180:183], v[156:159]\n\t" \
  "v_mfma_f32_16x16x32_bf16 v[144:147], v[72:75], v[184:187], v[144:147]\n\t" \
  "v_mfma_f32_16x16x32_bf16 v[148:151], v[76:79], v[184:187], v[148:151]\n\t" \
  "v_mfma_f32_16x16x32_bf16 v[152:155], v[72:75], v[188:191], v[152:155]\n\t" \
  "v_mfma_f32_16x16x32_bf16 v[156:159], v[76:79], v[188:191], v[156:159]\n\t" \
  "v_mfma_f32_16x16x32_bf16 v[144:147], v[80:83], v[192:195], v[144:147]\n\t" \
  "v_mfma_f32_16x16x32_bf16 v[148:151], v[84:87], v[192:195], v[148:151]\n\t" \
  "v_mfma_f32_16x16x32_bf16 v[152:155], v[80:83], v[196:199], v[152:155]\n\t" \
  "v_mfma_f32_16x16x32_bf16 v[156:159], v[84:87], v[196:199], v[156:159]\n\t" \
  "v_mfma_f32_16x16x32_bf16 v[144:147], v[88:91], v[200:203], v[144:147]\n\t" \
  "v_mfma_f32_16x16x32_bf16 v[148:151], v[92:95], v[200:203], v[148:151]\n\t" \
  "v_mfma_f32_16x16x32_bf16 v[152:155], v[88:91], v[204:207], v[152:155]\n\t" \
  "v_mfma_f32_16x16x32_bf16 v[156:159], v[92:95], v[204:207], v[156:159]\n\t" \
  "v_mfma_f32_16x16x32_bf16 v[144:147], v[96:99], v[208:211], v[144:147]\n\t" \
  "v_mfma_f32_16x16x32_bf16 v[148:151], v[100:103], v[208:211], v[148:151]\n\t" \
  "v_mfma_f32_16x16x32_bf16 v[152:155], v[96:99], v[212:215], v[152:155]\n\t" \
  "v_mfma_f32_16x16x32_bf16 v[156:159], v[100:103], v[212:215], v[156:159]\n\t" \
  "v_mfma_f32_16x16x32_bf16 v[144:147], v[104:107], v[216:219], v[144:147]\n\t" \
  "v_mfma_f32_16x16x32_bf16 v[148:151], v[108:111], v[216:219], v[148:151]\n\t" \
  "v_mfma_f32_16x16x32_bf16 v[152:155], v[104:107], v[220:223], v[152:155]\n\t" \
  "v_mfma_f32_16x16x32_bf16 v[156:159], v[108:111], v[220:223], v[156:159]\n\t" \
  "v_mfma_f32_16x16x32_bf16 v[144:147], v[112:115], v[224:227], v[144:147]\n\t" \
  "v_mfma_f32_16x16x32_bf16 v[148:151], v[116:119], v[224:227], v[148:151]\n\t" \
  "v_mfma_f32_16x16x32_bf16 v[152:155], v[112:115], v[228:231], v[152:155]\n\t" \
  "v_mfma_f32_16x16x32_bf16 v[156:159], v[116:119], v[228:231], v[156:159]\n\t" \
  "v_mfma_f32_16x16x32_bf16 v[144:147], v[120:123], v[232:235], v[144:147]\n\t" \
  "v_mfma_f32_16x16x32_bf16 v[148:151], v[124:127], v[232:235], v[148:151]\n\t" \
  "v_mfma_f32_16x16x32_bf16 v[152:155], v[120:123], v[236:239], v[152:155]\n\t" \
  "v_mfma_f32_16x16x32_bf16 v[156:159], v[124:127], v[236:239], v[156:159]\n\t" \
  "s_setprio 0\n\t" \
  ".Ls1%=:\n\t" \
  "s_add_u32 s92, s92, 512\n\t" \
  "s_addc_u32 s93, s93, 0\n\t" \
  "ds_read_b128 v[176:179], v240 offset:0\n\t" \
  "ds_read_b128 v[180:183], v240 offset:16384\n\t" \
  "ds_read_b128 v[184:187], v241 offset:0\n\t" \
  "ds_read_b128 v[188:191], v241 offset:16384\n\t" \
  "s_add_u32 m0, %[lw], 98304\n\t" \
  "s_nop 0\n\t" \
  "global_load_lds_dwordx4 v248, s[92:93] sc1\n\t" \
  "ds_read_b128 v[192:195], v242 offset:0\n\t" \
  "ds_read_b128 v[196:199], v242 offset:16384\n\t" \
  "ds_read_b128 v[200:203], v243 offset:0\n\t" \
  "ds_read_b128 v[204:207], v243 offset:16384\n\t" \
  "s_add_u32 m0, %[lw], 99328\n\t" \
  "s_nop 0\n\t" \
  "global_load_lds_dwordx4 v249, s[92:93] sc1\n\t" \
  "ds_read_b128 v[208:211], v240 offset:256\n\t" \
  "ds_read_b128 v[212:215], v240 offset:16640\n\t" \
  "ds_read_b128 v[216:219], v241 offset:256\n\t" \
  "ds_read_b128 v[220:223], v241 offset:16640\n\t" \
  "s_add_u32 m0, %[lw], 100352\n\t" \
  "s_nop 0\n\t" \
  "global_load_lds_dwordx4 v250, s[92:93] sc1\n\t" \
  "ds_read_b128 v[224:227], v242 offset:256\n\t" \
  "ds_read_b128 v[228:231], v242 offset:16640\n\t" \
  "ds_read_b128 v[232:235], v243 offset:256\n\t" \
  "ds_read_b128 v[236:239], v243 offset:16640\n\t" \
  "s_add_u32 m0, %[lw], 101376\n\t" \
  "s_nop 0\n\t" \
  "global_load_lds_dwordx4 v251, s[92:93] sc1\n\t" \
  "ds_write_b128 v252, v[152:155] offset:0\n\t" \
  "ds_write_b128 v252, v[156:159] offset:1024\n\t" \
  "s_waitcnt lgkmcnt(0)\n\t" \
  "s_waitcnt vmcnt(4)\n\t" \
  "s_barrier\n\t" \
  "s_setprio 2\n\t" \
  "v_mfma_f32_16x16x32_bf16 v[128:131], v[0:3], v[176:179], 0\n\t" \
  "v_mfma_f32_16x16x32_bf16 v[132:135], v[4:7], v[176:179], 0\n\t" \
  "v_mfma_f32_16x16x32_bf16 v[136:139], v[0:3], v[180:183], 0\n\t" \
  "v_mfma_f32_16x16x32_bf16 v[140:143], v[4:7], v[180:183], 0\n\t" \
  "v_mfma_f32_16x16x32_bf16 v[128:131], v[8:11], v[184:187], v[128:131]\n\t" \
  "v_mfma_f32_16x16x32_bf16 v[132:135], v[12:15], v[184:187], v[132:135]\n\t" \
  "v_mfma_f32_16x16x32_bf16 v[136:139], v[8:11], v[188:191], v[136:139]\n\t" \
  "v_mfma_f32_16x16x32_bf16 v[140:143], v[12:15], v[188:191], v[140:143]\n\t" \
  "v_mfma_f32_16x16x32_bf16 v[128:131], v[16:19], v[192:195], v[128:131]\n\t" \
  "v_mfma_f32_16x16x32_bf16 v[132:135], v[20:23], v[192:195], v[132:135]\n\t" \
  "v_mfma_f32_16x16x32_bf16 v[136:139], v[16:19], v[196:199], v[136:139]\n\t" \
  "v_mfma_f32_16x16x32_bf16 v[140:143], v[20:23], v[196:199], v[140:143]\n\t" \
  "v_mfma_f32_16x16x32_bf16 v[128:131], v[24:27], v[200:203], v[128:131]\n\t" \
  "v_mfma_f32_16x16x32_bf16 v[132:135], v[28:31], v[200:203], v[132:135]\n\t" \
  "v_mfma_f32_16x16x32_bf16 v[136:139], v[24:27], v[204:207], v[136:139]\n\t" \
  "v_mfma_f32_16x16x32_bf16 v[140:143], v[28:31], v[204:207], v[140:143]\n\t" \
  "v_mfma_f32_16x16x32_bf16 v[128:131], v[32:35], v[208:211], v[128:131]\n\t" \
  "v_mfma_f32_16x16x32_bf16 v[132:135], v[36:39], v[208:211], v[132:135]\n\t" \
  "v_mfma_f32_16x16x32_bf16 v[136:139], v[32:35], v[212:215], v[136:139]\n\t" \
  "v_mfma_f32_16x16x32_bf16 v[140:143], v[36:39], v[212:215], v[140:143]\n\t" \
  "v_mfma_f32_16x16x32_bf16 v[128:131], v[40:43], v[216:219], v[128:131]\n\t" \
  "v_mfma_f32_16x16x32_bf16 v[132:135], v[44:47], v[216:219], v[132:135]\n\t" \
  "v_mfma_f32_16x16x32_bf16 v[136:139], v[40:43], v[220:223], v[136:139]\n\t" \
  "v_mfma_f32_16x16x32_bf16 v[140:143], v[44:47], v[220:223], v[140:143]\n\t" \
  "v_mfma_f32_16x16x32_bf16 v[128:131], v[48:51], v[224:227], v[128:131]\n\t" \
  "v_mfma_f32_16x16x32_bf16 v[132:135], v[52:55], v[224:227], v[132:135]\n\t" \
  "v_mfma_f32_16x16x32_bf16 v[136:139], v[48:51], v[228:231], v[136:139]\n\t" \
  "v_mfma_f32_16x16x32_bf16 v[140:143], v[52:55], v[228:231], v[140:143]\n\t" \
  "v_mfma_f32_16x16x32_bf16 v[128:131], v[56:59], v[232:235], v[128:131]\n\t" \
  "v_mfma_f32_16x16x32_bf16 v[132:135], v[60:63], v[232:235], v[132:135]\n\t" \
  "v_mfma_f32_16x16x32_bf16 v[136:139], v[56:59], v[236:239], v[136:139]\n\t" \
  "v_mfma_f32_16x16x32_bf16 v[140:143], v[60:63], v[236:239], v[140:143]\n\t" \
  "s_setprio 0\n\t" \
  "s_add_u32 s92, s92, 65024\n\t" \
  "s_addc_u32 s93, s93, 0\n\t" \
  "ds_read_b128 v[160:163], v253 offset:0\n\t" \
  "ds_read_b128 v[164:167], v253 offset:1024\n\t" \
  "ds_read_b128 v[176:179], v240 offset:32768\n\t" \
  "ds_read_b128 v[180:183], v240 offset:49152\n\t" \
  "ds_read_b128 v[184:187], v241 offset:32768\n\t" \
  "ds_read_b128 v[188:191], v241 offset:49152\n\t" \
  "s_add_u32 m0, %[lw], 0\n\t" \
  "s_nop 0\n\t" \
  "global_load_lds_dwordx4 v248, s[92:93] sc1\n\t" \
  "ds_read_b128 v[192:195], v242 offset:32768\n\t" \
  "ds_read_b128 v[196:199], v242 offset:49152\n\t" \
  "ds_read_b128 v[200:203], v243 offset:32768\n\t" \
  "ds_read_b128 v[204:207], v243 offset:49152\n\t" \
  "s_add_u32 m0, %[lw], 1024\n\t" \
  "s_nop 0\n\t" \
  "global_load_lds_dwordx4 v249, s[92:93] sc1\n\t" \
  "ds_read_b128 v[208:211], v240 offset:33024\n\t" \
  "ds_read_b128 v[212:215], v240 offset:49408\n\t" \
  "ds_read_b128 v[216:219], v241 offset:33024\n\t" \
  "ds_read_b128 v[220:223], v241 offset:49408\n\t" \
  "s_add_u32 m0, %[lw], 2048\n\t" \
  "s_nop 0\n\t" \
  "global_load_lds_dwordx4 v250, s[92:93] sc1\n\t" \
  "ds_read_b128 v[224:227], v242 offset:33024\n\t" \
  "ds_read_b128 v[228:231], v242 offset:49408\n\t" \
  "ds_read_b128 v[232:235], v243 offset:33024\n\t" \
  "ds_read_b128 v[236:239], v243 offset:49408\n\t" \
  "s_add_u32 m0, %[lw], 3072\n\t" \
  "s_nop 0\n\t" \
  "global_load_lds_dwordx4 v251, s[92:93] sc1\n\t" \
  "s_waitcnt lgkmcnt(15)\n\t" \
  "s_waitcnt lgkmcnt(12)\n\t" \
  "s_sub_i32 s91, s86, 2\n\t" \
  "v_mov_b32 v173, s91\n\t" \
  "buffer_store_dword v173, v174, %[rs], 0 offen sc1\n\t" \
  "s_lshr_b32 s90, s87, 7\n\t" \
  "s_xor_b32 s90, s90, %[s0]\n\t" \
  "s_and_b32 s90, s90, 1\n\t" \
  "s_lshl1_add_u32 s90, s87, s90\n\t" \
  "s_lshl_b32 s90, s90, 13\n\t" \
  "v_add_f32 v160, v160, v144\n\t" \
  "v_add_f32 v161, v161, v145\n\t" \
  "v_add_f32 v162, v162, v146\n\t" \
  "v_add_f32 v163, v163, v147\n\t" \
  "v_add_f32 v164, v164, v148\n\t" \
  "v_add_f32 v165, v165, v149\n\t" \
  "v_add_f32 v166, v166, v150\n\t" \
  "v_add_f32 v167, v167, v151\n\t" \
  "v_cvt_pk_bf16_f32 v168, v160, v161\n\t" \
  "v_cvt_pk_bf16_f32 v169, v162, v163\n\t" \
  "v_cvt_pk_bf16_f32 v170, v164, v165\n\t" \
  "v_cvt_pk_bf16_f32 v171, v166, v167\n\t" \
  "buffer_store_dwordx4 v[168:171], v172, %[rt], s90 offen sc1\n\t" \
  "s_add_u32 s86, s86, 1\n\t" \
  "s_add_u32 s87, s87, 1\n\t" \
  "s_waitcnt lgkmcnt(0)\n\t" \
  "s_waitcnt vmcnt(10)\n\t" \
  "s_barrier\n\t" \
  "s_setprio 2\n\t" \
  "v_mfma_f32_16x16x32_bf16 v[128:131], v[64:67], v[176:179], v[128:131]\n\t" \
  "v_mfma_f32_16x16x32_bf16 v[132:135], v[68:71], v[176:179], v[132:135]\n\t" \
  "v_mfma_f32_16x16x32_bf16 v[136:139], v[64:67], v[180:183], v[136:139]\n\t" \
  "v_mfma_f32_16x16x32_bf16 v[140:143], v[68:71], v[180:183], v[140:143]\n\t" \
  "v_mfma_f32_16x16x32_bf16 v[128:131], v[72:75], v[184:187], v[128:131]\n\t" \
  "v_mfma_f32_16x16x32_bf16 v[132:135], v[76:79], v[184:187], v[132:135]\n\t" \
  "v_mfma_f32_16x16x32_bf16 v[136:139], v[72:75], v[188:191], v[136:139]\n\t" \
  "v_mfma_f32_16x16x32_bf16 v[140:143], v[76:79], v[188:191], v[140:143]\n\t" \
  "v_mfma_f32_16x16x32_bf16 v[128:131], v[80:83], v[192:195], v[128:131]\n\t" \
  "v_mfma_f32_16x16x32_bf16 v[132:135], v[84:87], v[192:195], v[132:135]\n\t" \
  "v_mfma_f32_16x16x32_bf16 v[136:139], v[80:83], v[196:199], v[136:139]\n\t" \
  "v_mfma_f32_16x16x32_bf16 v[140:143], v[84:87], v[196:199], v[140:143]\n\t" \
  "v_mfma_f32_16x16x32_bf16 v[128:131], v[88:91], v[200:203], v[128:131]\n\t" \
  "v_mfma_f32_16x16x32_bf16 v[132:135], v[92:95], v[200:203], v[132:135]\n\t" \
  "v_mfma_f32_16x16x32_bf16 v[136:139], v[88:91], v[204:207], v[136:139]\n\t" \
  "v_mfma_f32_16x16x32_bf16 v[140:143], v[92:95], v[204:207], v[140:143]\n\t" \
  "v_mfma_f32_16x16x32_bf16 v[128:131], v[96:99], v[208:211], v[128:131]\n\t" \
  "v_mfma_f32_16x16x32_bf16 v[132:135], v[100:103], v[208:211], v[132:135]\n\t" \
  "v_mfma_f32_16x16x32_bf16 v[136:139], v[96:99], v[212:215], v[136:139]\n\t" \
  "v_mfma_f32_16x16x32_bf16 v[140:143], v[100:103], v[212:215], v[140:143]\n\t" \
  "v_mfma_f32_16x16x32_bf16 v[128:131], v[104:107], v[216:219], v[128:131]\n\t" \
  "v_mfma_f32_16x16x32_bf16 v[132:135], v[108:111], v[216:219], v[132:135]\n\t" \
  "v_mfma_f32_16x16x32_bf16 v[136:139], v[104:107], v[220:223], v[136:139]\n\t" \
  "v_mfma_f32_16x16x32_bf16 v[140:143], v[108:111], v[220:223], v[140:143]\n\t" \
  "v_mfma_f32_16x16x32_bf16 v[128:131], v[112:115], v[224:227], v[128:131]\n\t" \
  "v_mfma_f32_16x16x32_bf16 v[132:135], v[116:119], v[224:227], v[132:135]\n\t" \
  "v_mfma_f32_16x16x32_bf16 v[136:139], v[112:115], v[228:231], v[136:139]\n\t" \
  "v_mfma_f32_16x16x32_bf16 v[140:143], v[116:119], v[228:231], v[140:143]\n\t" \
  "v_mfma_f32_16x16x32_bf16 v[128:131], v[120:123], v[232:235], v[128:131]\n\t" \
  "v_mfma_f32_16x16x32_bf16 v[132:135], v[124:127], v[232:235], v[132:135]\n\t" \
  "v_mfma_f32_16x16x32_bf16 v[136:139], v[120:123], v[236:239], v[136:139]\n\t" \
  "v_mfma_f32_16x16x32_bf16 v[140:143], v[124:127], v[236:239], v[140:143]\n\t" \
  "s_setprio 0\n\t" \
  "s_add_u32 s92, s92, 512\n\t" \
  "s_addc_u32 s93, s93, 0\n\t" \
  "ds_read_b128 v[176:179], v244 offset:0\n\t" \
  "ds_read_b128 v[180:183], v244 offset:16384\n\t" \
  "ds_read_b128 v[184:187], v245 offset:0\n\t" \
  "ds_read_b128 v[188:191], v245 offset:16384\n\t" \
  "s_add_u32 m0, %[lw], 32768\n\t" \
  "s_nop 0\n\t" \
  "global_load_lds_dwordx4 v248, s[92:93] sc1\n\t" \
  "ds_read_b128 v[192:195], v246 offset:0\n\t" \
  "ds_read_b128 v[196:199], v246 offset:16384\n\t" \
  "ds_read_b128 v[200:203], v247 offset:0\n\t" \
  "ds_read_b128 v[204:207], v247 offset:16384\n\t" \
  "s_add_u32 m0, %[lw], 33792\n\t" \
  "s_nop 0\n\t" \
  "global_load_lds_dwordx4 v249, s[92:93] sc1\n\t" \
  "ds_read_b128 v[208:211], v244 offset:256\n\t" \
  "ds_read_b128 v[212:215], v244 offset:16640\n\t" \
  "ds_read_b128 v[216:219], v245 offset:256\n\t" \
  "ds_read_b128 v[220:223], v245 offset:16640\n\t" \
  "s_add_u32 m0, %[lw], 34816\n\t" \
  "s_nop 0\n\t" \
  "global_load_lds_dwordx4 v250, s[92:93] sc1\n\t" \
  "ds_read_b128 v[224:227], v246 offset:256\n\t" \
  "ds_read_b128 v[228:231], v246 offset:16640\n\t" \
  "ds_read_b128 v[232:235], v247 offset:256\n\t" \
  "ds_read_b128 v[236:239], v247 offset:16640\n\t" \
  "s_add_u32 m0, %[lw], 35840\n\t" \
  "s_nop 0\n\t" \
  "global_load_lds_dwordx4 v251, s[92:93] sc1\n\t" \
  "ds_write_b128 v252, v[136:139] offset:0\n\t" \
  "ds_write_b128 v252, v[140:143] offset:1024\n\t" \
  "s_waitcnt lgkmcnt(0)\n\t" \
  "s_waitcnt vmcnt(4)\n\t" \
  "s_barrier\n\t" \
  "s_setprio 2\n\t" \
  "v_mfma_f32_16x16x32_bf16 v[144:147], v[0:3], v[176:179], 0\n\t" \
  "v_mfma_f32_16x16x32_bf16 v[148:151], v[4:7], v[176:179], 0\n\t" \
  "v_mfma_f32_16x16x32_bf16 v[152:155], v[0:3], v[180:183], 0\n\t" \
  "v_mfma_f32_16x16x32_bf16 v[156:159], v[4:7], v[180:183], 0\n\t" \
  "v_mfma_f32_16x16x32_bf16 v[144:147], v[8:11], v[184:187], v[144:147]\n\t" \
  "v_mfma_f32_16x16x32_bf16 v[148:151], v[12:15], v[184:187], v[148:151]\n\t" \
  "v_mfma_f32_16x16x32_bf16 v[152:155], v[8:11], v[188:191], v[152:155]\n\t" \
  "v_mfma_f32_16x16x32_bf16 v[156:159], v[12:15], v[188:191], v[156:159]\n\t" \
  "v_mfma_f32_16x16x32_bf16 v[144:147], v[16:19], v[192:195], v[144:147]\n\t" \
  "v_mfma_f32_16x16x32_bf16 v[148:151], v[20:23], v[192:195], v[148:151]\n\t" \
  "v_mfma_f32_16x16x32_bf16 v[152:155], v[16:19], v[196:199], v[152:155]\n\t" \
  "v_mfma_f32_16x16x32_bf16 v[156:159], v[20:23], v[196:199], v[156:159]\n\t" \
  "v_mfma_f32_16x16x32_bf16 v[144:147], v[24:27], v[200:203], v[144:147]\n\t" \
  "v_mfma_f32_16x16x32_bf16 v[148:151], v[28:31], v[200:203], v[148:151]\n\t" \
  "v_mfma_f32_16x16x32_bf16 v[152:155], v[24:27], v[204:207], v[152:155]\n\t" \
  "v_mfma_f32_16x16x32_bf16 v[156:159], v[28:31], v[204:207], v[156:159]\n\t" \
  "v_mfma_f32_16x16x32_bf16 v[144:147], v[32:35], v[208:211], v[144:147]\n\t" \
  "v_mfma_f32_16x16x32_bf16 v[148:151], v[36:39], v[208:211], v[148:151]\n\t" \
  "v_mfma_f32_16x16x32_bf16 v[152:155], v[32:35], v[212:215], v[152:155]\n\t" \
  "v_mfma_f32_16x16x32_bf16 v[156:159], v[36:39], v[212:215], v[156:159]\n\t" \
  "v_mfma_f32_16x16x32_bf16 v[144:147], v[40:43], v[216:219], v[144:147]\n\t" \
  "v_mfma_f32_16x16x32_bf16 v[148:151], v[44:47], v[216:219], v[148:151]\n\t" \
  "v_mfma_f32_16x16x32_bf16 v[152:155], v[40:43], v[220:223], v[152:155]\n\t" \
  "v_mfma_f32_16x16x32_bf16 v[156:159], v[44:47], v[220:223], v[156:159]\n\t" \
  "v_mfma_f32_16x16x32_bf16 v[144:147], v[48:51], v[224:227], v[144:147]\n\t" \
  "v_mfma_f32_16x16x32_bf16 v[148:151], v[52:55], v[224:227], v[148:151]\n\t" \
  "v_mfma_f32_16x16x32_bf16 v[152:155], v[48:51], v[228:231], v[152:155]\n\t" \
  "v_mfma_f32_16x16x32_bf16 v[156:159], v[52:55], v[228:231], v[156:159]\n\t" \
  "v_mfma_f32_16x16x32_bf16 v[144:147], v[56:59], v[232:235], v[144:147]\n\t" \
  "v_mfma_f32_16x16x32_bf16 v[148:151], v[60:63], v[232:235], v[148:151]\n\t" \
  "v_mfma_f32_16x16x32_bf16 v[152:155], v[56:59], v[236:239], v[152:155]\n\t" \
  "v_mfma_f32_16x16x32_bf16 v[156:159], v[60:63], v[236:239], v[156:159]\n\t" \
  "s_setprio 0\n\t" \
  "s_add_u32 s92, s92, 65024\n\t" \
  "s_addc_u32 s93, s93, 0\n\t" \
  "ds_read_b128 v[160:163], v253 offset:0\n\t" \
  "ds_read_b128 v[164:167], v253 offset:1024\n\t" \
  "ds_read_b128 v[176:179], v244 offset:32768\n\t" \
  "ds_read_b128 v[180:183], v244 offset:49152\n\t" \
  "ds_read_b128 v[184:187], v245 offset:32768\n\t" \
  "ds_read_b128 v[188:191], v245 offset:49152\n\t" \
  "s_add_u32 m0, %[lw], 65536\n\t" \
  "s_nop 0\n\t" \
  "global_load_lds_dwordx4 v248, s[92:93] sc1\n\t" \
  "ds_read_b128 v[192:195], v246 offset:32768\n\t" \
  "ds_read_b128 v[196:199], v246 offset:49152\n\t" \
  "ds_read_b128 v[200:203], v247 offset:32768\n\t" \
  "ds_read_b128 v[204:207], v247 offset:49152\n\t" \
  "s_add_u32 m0, %[lw], 66560\n\t" \
  "s_nop 0\n\t" \
  "global_load_lds_dwordx4 v249, s[92:93] sc1\n\t" \
  "ds_read_b128 v[208:211], v244 offset:33024\n\t" \
  "ds_read_b128 v[212:215], v244 offset:49408\n\t" \
  "ds_read_b128 v[216:219], v245 offset:33024\n\t" \
  "ds_read_b128 v[220:223], v245 offset:49408\n\t" \
  "s_add_u32 m0, %[lw], 67584\n\t" \
  "s_nop 0\n\t" \
  "global_load_lds_dwordx4 v250, s[92:93] sc1\n\t" \
  "ds_read_b128 v[224:227], v246 offset:33024\n\t" \
  "ds_read_b128 v[228:231], v246 offset:49408\n\t" \
  "ds_read_b128 v[232:235], v247 offset:33024\n\t" \
  "ds_read_b128 v[236:239], v247 offset:49408\n\t" \
  "s_add_u32 m0, %[lw], 68608\n\t" \
  "s_nop 0\n\t" \
  "global_load_lds_dwordx4 v251, s[92:93] sc1\n\t" \
  "s_waitcnt lgkmcnt(15)\n\t" \
  "s_waitcnt lgkmcnt(12)\n\t" \
  "s_sub_i32 s91, s86, 2\n\t" \
  "v_mov_b32 v173, s91\n\t" \
  "buffer_store_dword v173, v174, %[rs], 0 offen sc1\n\t" \
  "s_lshr_b32 s90, s87, 7\n\t" \
  "s_xor_b32 s90, s90, %[s0]\n\t" \
  "s_and_b32 s90, s90, 1\n\t" \
  "s_lshl1_add_u32 s90, s87, s90\n\t" \
  "s_lshl_b32 s90, s90, 13\n\t" \
  "v_add_f32 v160, v160, v128\n\t" \
  "v_add_f32 v161, v161, v129\n\t" \
  "v_add_f32 v162, v162, v130\n\t" \
  "v_add_f32 v163, v163, v131\n\t" \
  "v_add_f32 v164, v164, v132\n\t" \
  "v_add_f32 v165, v165, v133\n\t" \
  "v_add_f32 v166, v166, v134\n\t" \
  "v_add_f32 v167, v167, v135\n\t" \
  "v_cvt_pk_bf16_f32 v168, v160, v161\n\t" \
  "v_cvt_pk_bf16_f32 v169, v162, v163\n\t" \
  "v_cvt_pk_bf16_f32 v170, v164, v165\n\t" \
  "v_cvt_pk_bf16_f32 v171, v166, v167\n\t" \
  "buffer_store_dwordx4 v[168:171], v172, %[rt], s90 offen sc1\n\t" \
  "s_add_u32 s86, s86, 1\n\t" \
  "s_add_u32 s87, s87, 1\n\t" \
  "s_waitcnt lgkmcnt(0)\n\t" \
  "s_cmp_lt_u32 s86, 64\n\t" \
  "s_cbranch_scc1 .Lp1%=\n\t" \
  "s_barrier\n\t" \
  "s_setprio 2\n\t" \
  "v_mfma_f32_16x16x32_bf16 v[144:147], v[64:67], v[176:179], v[144:147]\n\t" \
  "v_mfma_f32_16x16x32_bf16 v[148:151], v[68:71], v[176:179], v[148:151]\n\t" \
  "v_mfma_f32_16x16x32_bf16 v[152:155], v[64:67], v[180:183], v[152:155]\n\t" \
  "v_mfma_f32_16x16x32_bf16 v[156:159], v[68:71], v[180:183], v[156:159]\n\t" \
  "v_mfma_f32_16x16x32_bf16 v[144:147], v[72:75], v[184:187], v[144:147]\n\t" \
  "v_mfma_f32_16x16x32_bf16 v[148:151], v[76:79], v[184:187], v[148:151]\n\t" \
  "v_mfma_f32_16x16x32_bf16 v[152:155], v[72:75], v[188:191], v[152:155]\n\t" \
  "v_mfma_f32_16x16x32_bf16 v[156:159], v[76:79], v[188:191], v[156:159]\n\t" \
  "v_mfma_f32_16x16x32_bf16 v[144:147], v[80:83], v[192:195], v[144:147]\n\t" \
  "v_mfma_f32_16x16x32_bf16 v[148:151], v[84:87], v[192:195], v[148:151]\n\t" \
  "v_mfma_f32_16x16x32_bf16 v[152:155], v[80:83], v[196:199], v[152:155]\n\t" \
  "v_mfma_f32_16x16x32_bf16 v[156:159], v[84:87], v[196:199], v[156:159]\n\t" \
  "v_mfma_f32_16x16x32_bf16 v[144:147], v[88:91], v[200:203], v[144:147]\n\t" \
  "v_mfma_f32_16x16x32_bf16 v[148:151], v[92:95], v[200:203], v[148:151]\n\t" \
  "v_mfma_f32_16x16x32_bf16 v[152:155], v[88:91], v[204:207], v[152:155]\n\t" \
  "v_mfma_f32_16x16x32_bf16 v[156:159], v[92:95], v[204:207], v[156:159]\n\t" \
  "v_mfma_f32_16x16x32_bf16 v[144:147], v[96:99], v[208:211], v[144:147]\n\t" \
  "v_mfma_f32_16x16x32_bf16 v[148:151], v[100:103], v[208:211], v[148:151]\n\t" \
  "v_mfma_f32_16x16x32_bf16 v[152:155], v[96:99], v[212:215], v[152:155]\n\t" \
  "v_mfma_f32_16x16x32_bf16 v[156:159], v[100:103], v[212:215], v[156:159]\n\t" \
  "v_mfma_f32_16x16x32_bf16 v[144:147], v[104:107], v[216:219], v[144:147]\n\t" \
  "v_mfma_f32_16x16x32_bf16 v[148:151], v[108:111], v[216:219], v[148:151]\n\t" \
  "v_mfma_f32_16x16x32_bf16 v[152:155], v[104:107], v[220:223], v[152:155]\n\t" \
  "v_mfma_f32_16x16x32_bf16 v[156:159], v[108:111], v[220:223], v[156:159]\n\t" \
  "v_mfma_f32_16x16x32_bf16 v[144:147], v[112:115], v[224:227], v[144:147]\n\t" \
  "v_mfma_f32_16x16x32_bf16 v[148:151], v[116:119], v[224:227], v[148:151]\n\t" \
  "v_mfma_f32_16x16x32_bf16 v[152:155], v[112:115], v[228:231], v[152:155]\n\t" \
  "v_mfma_f32_16x16x32_bf16 v[156:159], v[116:119], v[228:231], v[156:159]\n\t" \
  "v_mfma_f32_16x16x32_bf16 v[144:147], v[120:123], v[232:235], v[144:147]\n\t" \
  "v_mfma_f32_16x16x32_bf16 v[148:151], v[124:127], v[232:235], v[148:151]\n\t" \
  "v_mfma_f32_16x16x32_bf16 v[152:155], v[120:123], v[236:239], v[152:155]\n\t" \
  "v_mfma_f32_16x16x32_bf16 v[156:159], v[124:127], v[236:239], v[156:159]\n\t" \
  "s_setprio 0\n\t" \
  "s_nop 5\n\t" \
  "ds_write_b128 v252, v[152:155] offset:0\n\t" \
  "ds_write_b128 v252, v[156:159] offset:1024\n\t" \
  "s_waitcnt lgkmcnt(0)\n\t" \
  "s_barrier\n\t" \
  "ds_read_b128 v[160:163], v253 offset:0\n\t" \
  "ds_read_b128 v[164:167], v253 offset:1024\n\t" \
  "s_waitcnt lgkmcnt(0)\n\t" \
  "s_lshr_b32 s90, s87, 7\n\t" \
  "s_xor_b32 s90, s90, %[s0]\n\t" \
  "s_and_b32 s90, s90, 1\n\t" \
  "s_lshl1_add_u32 s90, s87, s90\n\t" \
  "s_lshl_b32 s90, s90, 13\n\t" \
  "v_add_f32 v160, v160, v144\n\t" \
  "v_add_f32 v161, v161, v145\n\t" \
  "v_add_f32 v162, v162, v146\n\t" \
  "v_add_f32 v163, v163, v147\n\t" \
  "v_add_f32 v164, v164, v148\n\t" \
  "v_add_f32 v165, v165, v149\n\t" \
  "v_add_f32 v166, v166, v150\n\t" \
  "v_add_f32 v167, v167, v151\n\t" \
  "v_cvt_pk_bf16_f32 v168, v160, v161\n\t" \
  "v_cvt_pk_bf16_f32 v169, v162, v163\n\t" \
  "v_cvt_pk_bf16_f32 v170, v164, v165\n\t" \
  "v_cvt_pk_bf16_f32 v171, v166, v167\n\t" \
  "buffer_store_dwordx4 v[168:171], v172, %[rt], s90 offen sc1\n\t" \
  "s_waitcnt vmcnt(0)\n\t" \
  "s_barrier\n\t" \
  "s_sub_i32 s91, s86, 1\n\t" \
  "v_mov_b32 v173, s91\n\t" \
  "buffer_store_dword v173, v174, %[rs], 0 offen sc1\n\t" \
  "s_mov_b32 m0, s94\n\t" \
  ".Le%=:\n\t"
#define PP_ASM_1 \
  "s_cmp_lg_u32 %[kh], 0\n\t" \
  "s_cbranch_scc1 .Lk%=\n\t" \
  "s_mov_b32 s94, m0\n\t" \
  "s_mov_b64 s[92:93], %[x0]\n\t" \
  "s_mov_b32 s86, %[g0]\n\t" \
  "s_add_u32 s87, s86, %[t0]\n\t" \
  "s_add_u32 m0, %[lw], 67584\n\t" \
  "s_nop 0\n\t" \
  "global_load_lds_dwordx4 v250, s[92:93] sc1\n\t" \
  "s_add_u32 m0, %[lw], 68608\n\t" \
  "s_nop 0\n\t" \
  "global_load_lds_dwordx4 v251, s[92:93] sc1\n\t" \
  "s_barrier\n\t" \
  "s_branch .Ls0%=\n\t" \
  ".Lp0%=:\n\t" \
  "s_waitcnt vmcnt(9)\n\t" \
  "s_barrier\n\t" \
  ".Ls0%=:\n\t" \
  "s_add_u32 s92, s92, 512\n\t" \
  "s_addc_u32 s93, s93, 0\n\t" \
  "ds_read_b128 v[176:179], v240 offset:0\n\t" \
  "ds_read_b128 v[180:183], v240 offset:16384\n\t" \
  "ds_read_b128 v[184:187], v241 offset:0\n\t" \
  "ds_read_b128 v[188:191], v241 offset:16384\n\t" \
  "s_add_u32 m0, %[lw], 98304\n\t" \
  "s_nop 0\n\t" \
  "global_load_lds_dwordx4 v248, s[92:93] sc1\n\t" \
  "ds_read_b128 v[192:195], v242 offset:0\n\t" \
  "ds_read_b128 v[196:199], v242 offset:16384\n\t" \
  "ds_read_b128 v[200:203], v243 offset:0\n\t" \
  "ds_read_b128 v[204:207], v243 offset:16384\n\t" \
  "s_add_u32 m0, %[lw], 99328\n\t" \
  "s_nop 0\n\t" \
  "global_load_lds_dwordx4 v249, s[92:93] sc1\n\t" \
  "ds_read_b128 v[208:211], v240 offset:256\n\t" \
  "ds_read_b128 v[212:215], v240 offset:16640\n\t" \
  "ds_read_b128 v[216:219], v241 offset:256\n\t" \
  "ds_read_b128 v[220:223], v241 offset:16640\n\t" \
  "s_add_u32 m0, %[lw], 100352\n\t" \
  "s_nop 0\n\t" \
  "global_load_lds_dwordx4 v250, s[92:93] sc1\n\t" \
  "ds_read_b128 v[224:227], v242 offset:256\n\t" \
  "ds_read_b128 v[228:231], v242 offset:16640\n\t" \
  "ds_read_b128 v[232:235], v243 offset:256\n\t" \
  "ds_read_b128 v[236:239], v243 offset:16640\n\t" \
  "s_add_u32 m0, %[lw], 101376\n\t" \
  "s_nop 0\n\t" \
  "global_load_lds_dwordx4 v251, s[92:93] sc1\n\t" \
  "ds_write_b128 v252, v[152:155] offset:0\n\t" \
  "ds_write_b128 v252, v[156:159] offset:1024\n\t" \
  "s_waitcnt lgkmcnt(0)\n\t" \
  "s_setprio 1\n\t" \
  "v_mfma_f32_16x16x32_bf16 v[128:131], v[176:179], v[0:3], 0\n\t" \
  "v_mfma_f32_16x16x32_bf16 v[136:139], v[176:179], v[4:7], 0\n\t" \
  "v_mfma_f32_16x16x32_bf16 v[132:135], v[180:183], v[0:3], 0\n\t" \
  "v_mfma_f32_16x16x32_bf16 v[140:143], v[180:183], v[4:7], 0\n\t" \
  "v_mfma_f32_16x16x32_bf16 v[128:131], v[184:187], v[8:11], v[128:131]\n\t" \
  "v_mfma_f32_16x16x32_bf16 v[136:139], v[184:187], v[12:15], v[136:139]\n\t" \
  "v_mfma_f32_16x16x32_bf16 v[132:135], v[188:191], v[8:11], v[132:135]\n\t" \
  "v_mfma_f32_16x16x32_bf16 v[140:143], v[188:191], v[12:15], v[140:143]\n\t" \
  "v_mfma_f32_16x16x32_bf16 v[128:131], v[192:195], v[16:19], v[128:131]\n\t" \
  "v_mfma_f32_16x16x32_bf16 v[136:139], v[192:195], v[20:23], v[136:139]\n\t" \
  "v_mfma_f32_16x16x32_bf16 v[132:135], v[196:199], v[16:19], v[132:135]\n\t" \
  "v_mfma_f32_16x16x32_bf16 v[140:143], v[196:199], v[20:23], v[140:143]\n\t" \
  "v_mfma_f32_16x16x32_bf16 v[128:131], v[200:203], v[24:27], v[128:131]\n\t" \
  "v_mfma_f32_16x16x32_bf16 v[136:139], v[200:203], v[28:31], v[136:139]\n\t" \
  "v_mfma_f32_16x16x32_bf16 v[132:135], v[204:207], v[24:27], v[132:135]\n\t" \
  "v_mfma_f32_16x16x32_bf16 v[140:143], v[204:207], v[28:31], v[140:143]\n\t" \
  "v_mfma_f32_16x16x32_bf16 v[128:131], v[208:211], v[32:35], v[128:131]\n\t" \
  "v_mfma_f32_16x16x32_bf16 v[136:139], v[208:211], v[36:39], v[136:139]\n\t" \
  "v_mfma_f32_16x16x32_bf16 v[132:135], v[212:215], v[32:35], v[132:135]\n\t" \
  "v_mfma_f32_16x16x32_bf16 v[140:143], v[212:215], v[36:39], v[140:143]\n\t" \
  "v_mfma_f32_16x16x32_bf16 v[128:131], v[216:219], v[40:43], v[128:131]\n\t" \
  "v_mfma_f32_16x16x32_bf16 v[136:139], v[216:219], v[44:47], v[136:139]\n\t" \
  "v_mfma_f32_16x16x32_bf16 v[132:135], v[220:223], v[40:43], v[132:135]\n\t" \
  "v_mfma_f32_16x16x32_bf16 v[140:143], v[220:223], v[44:47], v[140:143]\n\t" \
  "v_mfma_f32_16x16x32_bf16 v[128:131], v[224:227], v[48:51], v[128:131]\n\t" \
  "v_mfma_f32_16x16x32_bf16 v[136:139], v[224:227], v[52:55], v[136:139]\n\t" \
  "v_mfma_f32_16x16x32_bf16 v[132:135], v[228:231], v[48:51], v[132:135]\n\t" \
  "v_mfma_f32_16x16x32_bf16 v[140:143], v[228:231], v[52:55], v[140:143]\n\t" \
  "v_mfma_f32_16x16x32_bf16 v[128:131], v[232:235], v[56:59], v[128:131]\n\t" \
  "v_mfma_f32_16x16x32_bf16 v[136:139], v[232:235], v[60:63], v[136:139]\n\t" \
  "v_mfma_f32_16x16x32_bf16 v[132:135], v[236:239], v[56:59], v[132:135]\n\t" \
  "v_mfma_f32_16x16x32_bf16 v[140:143], v[236:239], v[60:63], v[140:143]\n\t" \
  "s_setprio 0\n\t" \
  "s_waitcnt vmcnt(4)\n\t" \
  "s_barrier\n\t" \
  "s_add_u32 s92, s92, 65024\n\t" \
  "s_addc_u32 s93, s93, 0\n\t" \
  "ds_read_b128 v[160:163], v253 offset:0\n\t" \
  "ds_read_b128 v[164:167], v253 offset:1024\n\t" \
  "ds_read_b128 v[176:179], v240 offset:32768\n\t" \
  "ds_read_b128 v[180:183], v240 offset:49152\n\t" \
  "ds_read_b128 v[184:187], v241 offset:32768\n\t" \
  "ds_read_b128 v[188:191], v241 offset:49152\n\t" \
  "s_add_u32 m0, %[lw], 0\n\t" \
  "s_nop 0\n\t" \
  "global_load_lds_dwordx4 v248, s[92:93] sc1\n\t" \
  "ds_read_b128 v[192:195], v242 offset:32768\n\t" \
  "ds_read_b128 v[196:199], v242 offset:49152\n\t" \
  "ds_read_b128 v[200:203], v243 offset:32768\n\t" \
  "ds_read_b128 v[204:207], v243 offset:49152\n\t" \
  "s_add_u32 m0, %[lw], 1024\n\t" \
  "s_nop 0\n\t" \
  "global_load_lds_dwordx4 v249, s[92:93] sc1\n\t" \
  "ds_read_b128 v[208:211], v240 offset:33024\n\t" \
  "ds_read_b128 v[212:215], v240 offset:49408\n\t" \
  "ds_read_b128 v[216:219], v241 offset:33024\n\t" \
  "ds_read_b128 v[220:223], v241 offset:49408\n\t" \
  "s_add_u32 m0, %[lw], 2048\n\t" \
  "s_nop 0\n\t" \
  "global_load_lds_dwordx4 v250, s[92:93] sc1\n\t" \
  "ds_read_b128 v[224:227], v242 offset:33024\n\t" \
  "ds_read_b128 v[228:231], v242 offset:49408\n\t" \
  "ds_read_b128 v[232:235], v243 offset:33024\n\t" \
  "ds_read_b128 v[236:239], v243 offset:49408\n\t" \
  "s_add_u32 m0, %[lw], 3072\n\t" \
  "s_nop 0\n\t" \
  "global_load_lds_dwordx4 v251, s[92:93] sc1\n\t" \
  "s_waitcnt lgkmcnt(15)\n\t" \
  "s_waitcnt lgkmcnt(12)\n\t" \
  "s_lshr_b32 s90, s87, 7\n\t" \
  "s_xor_b32 s90, s90, %[s0]\n\t" \
  "s_and_b32 s90, s90, 1\n\t" \
  "s_lshl1_add_u32 s90, s87, s90\n\t" \
  "s_lshl_b32 s90, s90, 13\n\t" \
  "v_add_f32 v160, v160, v144\n\t" \
  "v_add_f32 v161, v161, v145\n\t" \
  "v_add_f32 v162, v162, v146\n\t" \
  "v_add_f32 v163, v163, v147\n\t" \
  "v_add_f32 v164, v164, v148\n\t" \
  "v_add_f32 v165, v165, v149\n\t" \
  "v_add_f32 v166, v166, v150\n\t" \
  "v_add_f32 v167, v167, v151\n\t" \
  "v_cvt_pk_bf16_f32 v168, v160, v161\n\t" \
  "v_cvt_pk_bf16_f32 v169, v162, v163\n\t" \
  "v_cvt_pk_bf16_f32 v170, v164, v165\n\t" \
  "v_cvt_pk_bf16_f32 v171, v166, v167\n\t" \
  "buffer_store_dwordx4 v[168:171], v172, %[rt], s90 offen sc1\n\t" \
  "s_add_u32 s86, s86, 1\n\t" \
  "s_add_u32 s87, s87, 1\n\t" \
  "s_waitcnt lgkmcnt(0)\n\t" \
  "s_setprio 1\n\t" \
  "v_mfma_f32_16x16x32_bf16 v[128:131], v[176:179], v[64:67], v[128:131]\n\t" \
  "v_mfma_f32_16x16x32_bf16 v[136:139], v[176:179], v[68:71], v[136:139]\n\t" \
  "v_mfma_f32_16x16x32_bf16 v[132:135], v[180:183], v[64:67], v[132:135]\n\t" \
  "v_mfma_f32_16x16x32_bf16 v[140:143], v[180:183], v[68:71], v[140:143]\n\t" \
  "v_mfma_f32_16x16x32_bf16 v[128:131], v[184:187], v[72:75], v[128:131]\n\t" \
  "v_mfma_f32_16x16x32_bf16 v[136:139], v[184:187], v[76:79], v[136:139]\n\t" \
  "v_mfma_f32_16x16x32_bf16 v[132:135], v[188:191], v[72:75], v[132:135]\n\t" \
  "v_mfma_f32_16x16x32_bf16 v[140:143], v[188:191], v[76:79], v[140:143]\n\t" \
  "v_mfma_f32_16x16x32_bf16 v[128:131], v[192:195], v[80:83], v[128:131]\n\t" \
  "v_mfma_f32_16x16x32_bf16 v[136:139], v[192:195], v[84:87], v[136:139]\n\t" \
  "v_mfma_f32_16x16x32_bf16 v[132:135], v[196:199], v[80:83], v[132:135]\n\t" \
  "v_mfma_f32_16x16x32_bf16 v[140:143], v[196:199], v[84:87], v[140:143]\n\t" \
  "v_mfma_f32_16x16x32_bf16 v[128:131], v[200:203], v[88:91], v[128:131]\n\t" \
  "v_mfma_f32_16x16x32_bf16 v[136:139], v[200:203], v[92:95], v[136:139]\n\t" \
  "v_mfma_f32_16x16x32_bf16 v[132:135], v[204:207], v[88:91], v[132:135]\n\t" \
  "v_mfma_f32_16x16x32_bf16 v[140:143], v[204:207], v[92:95], v[140:143]\n\t" \
  "v_mfma_f32_16x16x32_bf16 v[128:131], v[208:211], v[96:99], v[128:131]\n\t" \
  "v_mfma_f32_16x16x32_bf16 v[136:139], v[208:211], v[100:103], v[136:139]\n\t" \
  "v_mfma_f32_16x16x32_bf16 v[132:135], v[212:215], v[96:99], v[132:135]\n\t" \
  "v_mfma_f32_16x16x32_bf16 v[140:143], v[212:215], v[100:103], v[140:143]\n\t" \
  "v_mfma_f32_16x16x32_bf16 v[128:131], v[216:219], v[104:107], v[128:131]\n\t" \
  "v_mfma_f32_16x16x32_bf16 v[136:139], v[216:219], v[108:111], v[136:139]\n\t" \
  "v_mfma_f32_16x16x32_bf16 v[132:135], v[220:223], v[104:107], v[132:135]\n\t" \
  "v_mfma_f32_16x16x32_bf16 v[140:143], v[220:223], v[108:111], v[140:143]\n\t" \
  "v_mfma_f32_16x16x32_bf16 v[128:131], v[224:227], v[112:115], v[128:131]\n\t" \
  "v_mfma_f32_16x16x32_bf16 v[136:139], v[224:227], v[116:119], v[136:139]\n\t" \
  "v_mfma_f32_16x16x32_bf16 v[132:135], v[228:231], v[112:115], v[132:135]\n\t" \
  "v_mfma_f32_16x16x32_bf16 v[140:143], v[228:231], v[116:119], v[140:143]\n\t" \
  "v_mfma_f32_16x16x32_bf16 v[128:131], v[232:235], v[120:123], v[128:131]\n\t" \
  "v_mfma_f32_16x16x32_bf16 v[136:139], v[232:235], v[124:127], v[136:139]\n\t" \
  "v_mfma_f32_16x16x32_bf16 v[132:135], v[236:239], v[120:123], v[132:135]\n\t" \
  "v_mfma_f32_16x16x32_bf16 v[140:143], v[236:239], v[124:127], v[140:143]\n\t" \
  "s_setprio 0\n\t" \
  "s_waitcnt vmcnt(9)\n\t" \
  "s_barrier\n\t" \
  "s_add_u32 s92, s92, 512\n\t" \
  "s_addc_u32 s93, s93, 0\n\t" \
  "ds_read_b128 v[176:179], v244 offset:0\n\t" \
  "ds_read_b128 v[180:183], v244 offset:16384\n\t" \
  "ds_read_b128 v[184:187], v245 offset:0\n\t" \
  "ds_read_b128 v[188:191], v245 offset:16384\n\t" \
  "s_add_u32 m0, %[lw], 32768\n\t" \
  "s_nop 0\n\t" \
  "global_load_lds_dwordx4 v248, s[92:93] sc1\n\t" \
  "ds_read_b128 v[192:195], v246 offset:0\n\t" \
  "ds_read_b128 v[196:199], v246 offset:16384\n\t" \
  "ds_read_b128 v[200:203], v247 offset:0\n\t" \
  "ds_read_b128 v[204:207], v247 offset:16384\n\t" \
  "s_add_u32 m0, %[lw], 33792\n\t" \
  "s_nop 0\n\t" \
  "global_load_lds_dwordx4 v249, s[92:93] sc1\n\t" \
  "ds_read_b128 v[208:211], v244 offset:256\n\t" \
  "ds_read_b128 v[212:215], v244 offset:16640\n\t" \
  "ds_read_b128 v[216:219], v245 offset:256\n\t" \
  "ds_read_b128 v[220:223], v245 offset:16640\n\t" \
  "s_add_u32 m0, %[lw], 34816\n\t" \
  "s_nop 0\n\t" \
  "global_load_lds_dwordx4 v250, s[92:93] sc1\n\t" \
  "ds_read_b128 v[224:227], v246 offset:256\n\t" \
  "ds_read_b128 v[228:231], v246 offset:16640\n\t" \
  "ds_read_b128 v[232:235], v247 offset:256\n\t" \
  "ds_read_b128 v[236:239], v247 offset:16640\n\t" \
  "s_add_u32 m0, %[lw], 35840\n\t" \
  "s_nop 0\n\t" \
  "global_load_lds_dwordx4 v251, s[92:93] sc1\n\t" \
  "ds_write_b128 v252, v[136:139] offset:0\n\t" \
  "ds_write_b128 v252, v[140:143] offset:1024\n\t" \
  "s_waitcnt lgkmcnt(0)\n\t" \
  "s_setprio 1\n\t" \
  "v_mfma_f32_16x16x32_bf16 v[144:147], v[176:179], v[0:3], 0\n\t" \
  "v_mfma_f32_16x16x32_bf16 v[152:155], v[176:179], v[4:7], 0\n\t" \
  "v_mfma_f32_16x16x32_bf16 v[148:151], v[180:183], v[0:3], 0\n\t" \
  "v_mfma_f32_16x16x32_bf16 v[156:159], v[180:183], v[4:7], 0\n\t" \
  "v_mfma_f32_16x16x32_bf16 v[144:147], v[184:187], v[8:11], v[144:147]\n\t" \
  "v_mfma_f32_16x16x32_bf16 v[152:155], v[184:187], v[12:15], v[152:155]\n\t" \
  "v_mfma_f32_16x16x32_bf16 v[148:151], v[188:191], v[8:11], v[148:151]\n\t" \
  "v_mfma_f32_16x16x32_bf16 v[156:159], v[188:191], v[12:15], v[156:159]\n\t" \
  "v_mfma_f32_16x16x32_bf16 v[144:147], v[192:195], v[16:19], v[144:147]\n\t" \
  "v_mfma_f32_16x16x32_bf16 v[152:155], v[192:195], v[20:23], v[152:155]\n\t" \
  "v_mfma_f32_16x16x32_bf16 v[148:151], v[196:199], v[16:19], v[148:151]\n\t" \
  "v_mfma_f32_16x16x32_bf16 v[156:159], v[196:199], v[20:23], v[156:159]\n\t" \
  "v_mfma_f32_16x16x32_bf16 v[144:147], v[200:203], v[24:27], v[144:147]\n\t" \
  "v_mfma_f32_16x16x32_bf16 v[152:155], v[200:203], v[28:31], v[152:155]\n\t" \
  "v_mfma_f32_16x16x32_bf16 v[148:151], v[204:207], v[24:27], v[148:151]\n\t" \
  "v_mfma_f32_16x16x32_bf16 v[156:159], v[204:207], v[28:31], v[156:159]\n\t" \
  "v_mfma_f32_16x16x32_bf16 v[144:147], v[208:211], v[32:35], v[144:147]\n\t" \
  "v_mfma_f32_16x16x32_bf16 v[152:155], v[208:211], v[36:39], v[152:155]\n\t" \
  "v_mfma_f32_16x16x32_bf16 v[148:151], v[212:215], v[32:35], v[148:151]\n\t" \
  "v_mfma_f32_16x16x32_bf16 v[156:159], v[212:215], v[36:39], v[156:159]\n\t" \
  "v_mfma_f32_16x16x32_bf16 v[144:147], v[216:219], v[40:43], v[144:147]\n\t" \
  "v_mfma_f32_16x16x32_bf16 v[152:155], v[216:219], v[44:47], v[152:155]\n\t" \
  "v_mfma_f32_16x16x32_bf16 v[148:151], v[220:223], v[40:43], v[148:151]\n\t" \
  "v_mfma_f32_16x16x32_bf16 v[156:159], v[220:223], v[44:47], v[156:159]\n\t" \
  "v_mfma_f32_16x16x32_bf16 v[144:147], v[224:227], v[48:51], v[144:147]\n\t" \
  "v_mfma_f32_16x16x32_bf16 v[152:155], v[224:227], v[52:55], v[152:155]\n\t" \
  "v_mfma_f32_16x16x32_bf16 v[148:151], v[228:231], v[48:51], v[148:151]\n\t" \
  "v_mfma_f32_16x16x32_bf16 v[156:159], v[228:231], v[52:55], v[156:159]\n\t" \
  "v_mfma_f32_16x16x32_bf16 v[144:147], v[232:235], v[56:59], v[144:147]\n\t" \
  "v_mfma_f32_16x16x32_bf16 v[152:155], v[232:235], v[60:63], v[152:155]\n\t" \
  "v_mfma_f32_16x16x32_bf16 v[148:151], v[236:239], v[56:59], v[148:151]\n\t" \
  "v_mfma_f32_16x16x32_bf16 v[156:159], v[236:239], v[60:63], v[156:159]\n\t" \
  "s_setprio 0\n\t" \
  "s_waitcnt vmcnt(4)\n\t" \
  "s_barrier\n\t" \
  "s_add_u32 s92, s92, 65024\n\t" \
  "s_addc_u32 s93, s93, 0\n\t" \
  "ds_read_b128 v[160:163], v253 offset:0\n\t" \
  "ds_read_b128 v[164:167], v253 offset:1024\n\t" \
  "ds_read_b128 v[176:179], v244 offset:32768\n\t" \
  "ds_read_b128 v[180:183], v244 offset:49152\n\t" \
  "ds_read_b128 v[184:187], v245 offset:32768\n\t" \
  "ds_read_b128 v[188:191], v245 offset:49152\n\t" \
  "s_add_u32 m0, %[lw], 65536\n\t" \
  "s_nop 0\n\t" \
  "global_load_lds_dwordx4 v248, s[92:93] sc1\n\t" \
  "ds_read_b128 v[192:195], v246 offset:32768\n\t" \
  "ds_read_b128 v[196:199], v246 offset:49152\n\t" \
  "ds_read_b128 v[200:203], v247 offset:32768\n\t" \
  "ds_read_b128 v[204:207], v247 offset:49152\n\t" \
  "s_add_u32 m0, %[lw], 66560\n\t" \
  "s_nop 0\n\t" \
  "global_load_lds_dwordx4 v249, s[92:93] sc1\n\t" \
  "ds_read_b128 v[208:211], v244 offset:33024\n\t" \
  "ds_read_b128 v[212:215], v244 offset:49408\n\t" \
  "ds_read_b128 v[216:219], v245 offset:33024\n\t" \
  "ds_read_b128 v[220:223], v245 offset:49408\n\t" \
  "s_add_u32 m0, %[lw], 67584\n\t" \
  "s_nop 0\n\t" \
  "global_load_lds_dwordx4 v250, s[92:93] sc1\n\t" \
  "ds_read_b128 v[224:227], v246 offset:33024\n\t" \
  "ds_read_b128 v[228:231], v246 offset:49408\n\t" \
  "ds_read_b128 v[232:235], v247 offset:33024\n\t" \
  "ds_read_b128 v[236:239], v247 offset:49408\n\t" \
  "s_add_u32 m0, %[lw], 68608\n\t" \
  "s_nop 0\n\t" \
  "global_load_lds_dwordx4 v251, s[92:93] sc1\n\t" \
  "s_waitcnt lgkmcnt(15)\n\t" \
  "s_waitcnt lgkmcnt(12)\n\t" \
  "s_lshr_b32 s90, s87, 7\n\t" \
  "s_xor_b32 s90, s90, %[s0]\n\t" \
  "s_and_b32 s90, s90, 1\n\t" \
  "s_lshl1_add_u32 s90, s87, s90\n\t" \
  "s_lshl_b32 s90, s90, 13\n\t" \
  "v_add_f32 v160, v160, v128\n\t" \
  "v_add_f32 v161, v161, v129\n\t" \
  "v_add_f32 v162, v162, v130\n\t" \
  "v_add_f32 v163, v163, v131\n\t" \
  "v_add_f32 v164, v164, v132\n\t" \
  "v_add_f32 v165, v165, v133\n\t" \
  "v_add_f32 v166, v166, v134\n\t" \
  "v_add_f32 v167, v167, v135\n\t" \
  "v_cvt_pk_bf16_f32 v168, v160, v161\n\t" \
  "v_cvt_pk_bf16_f32 v169, v162, v163\n\t" \
  "v_cvt_pk_bf16_f32 v170, v164, v165\n\t" \
  "v_cvt_pk_bf16_f32 v171, v166, v167\n\t" \
  "buffer_store_dwordx4 v[168:171], v172, %[rt], s90 offen sc1\n\t" \
  "s_add_u32 s86, s86, 1\n\t" \
  "s_add_u32 s87, s87, 1\n\t" \
  "s_waitcnt lgkmcnt(0)\n\t" \
  "s_setprio 1\n\t" \
  "v_mfma_f32_16x16x32_bf16 v[144:147], v[176:179], v[64:67], v[144:147]\n\t" \
  "v_mfma_f32_16x16x32_bf16 v[152:155], v[176:179], v[68:71], v[152:155]\n\t" \
  "v_mfma_f32_16x16x32_bf16 v[148:151], v[180:183], v[64:67], v[148:151]\n\t" \
  "v_mfma_f32_16x16x32_bf16 v[156:159], v[180:183], v[68:71], v[156:159]\n\t" \
  "v_mfma_f32_16x16x32_bf16 v[144:147], v[184:187], v[72:75], v[144:147]\n\t" \
  "v_mfma_f32_16x16x32_bf16 v[152:155], v[184:187], v[76:79], v[152:155]\n\t" \
  "v_mfma_f32_16x16x32_bf16 v[148:151], v[188:191], v[72:75], v[148:151]\n\t" \
  "v_mfma_f32_16x16x32_bf16 v[156:159], v[188:191], v[76:79], v[156:159]\n\t" \
  "v_mfma_f32_16x16x32_bf16 v[144:147], v[192:195], v[80:83], v[144:147]\n\t" \
  "v_mfma_f32_16x16x32_bf16 v[152:155], v[192:195], v[84:87], v[152:155]\n\t" \
  "v_mfma_f32_16x16x32_bf16 v[148:151], v[196:199], v[80:83], v[148:151]\n\t" \
  "v_mfma_f32_16x16x32_bf16 v[156:159], v[196:199], v[84:87], v[156:159]\n\t" \
  "v_mfma_f32_16x16x32_bf16 v[144:147], v[200:203], v[88:91], v[144:147]\n\t" \
  "v_mfma_f32_16x16x32_bf16 v[152:155], v[200:203], v[92:95], v[152:155]\n\t" \
  "v_mfma_f32_16x16x32_bf16 v[148:151], v[204:207], v[88:91], v[148:151]\n\t" \
  "v_mfma_f32_16x16x32_bf16 v[156:159], v[204:207], v[92:95], v[156:159]\n\t" \
  "v_mfma_f32_16x16x32_bf16 v[144:147], v[208:211], v[96:99], v[144:147]\n\t" \
  "v_mfma_f32_16x16x32_bf16 v[152:155], v[208:211], v[100:103], v[152:155]\n\t" \
  "v_mfma_f32_16x16x32_bf16 v[148:151], v[212:215], v[96:99], v[148:151]\n\t" \
  "v_mfma_f32_16x16x32_bf16 v[156:159], v[212:215], v[100:103], v[156:159]\n\t" \
  "v_mfma_f32_16x16x32_bf16 v[144:147], v[216:219], v[104:107], v[144:147]\n\t" \
  "v_mfma_f32_16x16x32_bf16 v[152:155], v[216:219], v[108:111], v[152:155]\n\t" \
  "v_mfma_f32_16x16x32_bf16 v[148:151], v[220:223], v[104:107], v[148:151]\n\t" \
  "v_mfma_f32_16x16x32_bf16 v[156:159], v[220:223], v[108:111], v[156:159]\n\t" \
  "v_mfma_f32_16x16x32_bf16 v[144:147], v[224:227], v[112:115], v[144:147]\n\t" \
  "v_mfma_f32_16x16x32_bf16 v[152:155], v[224:227], v[116:119], v[152:155]\n\t" \
  "v_mfma_f32_16x16x32_bf16 v[148:151], v[228:231], v[112:115], v[148:151]\n\t" \
  "v_mfma_f32_16x16x32_bf16 v[156:159], v[228:231], v[116:119], v[156:159]\n\t" \
  "v_mfma_f32_16x16x32_bf16 v[144:147], v[232:235], v[120:123], v[144:147]\n\t" \
  "v_mfma_f32_16x16x32_bf16 v[152:155], v[232:235], v[124:127], v[152:155]\n\t" \
  "v_mfma_f32_16x16x32_bf16 v[148:151], v[236:239], v[120:123], v[148:151]\n\t" \
  "v_mfma_f32_16x16x32_bf16 v[156:159], v[236:239], v[124:127], v[156:159]\n\t" \
  "s_setprio 0\n\t" \
  "s_cmp_lt_u32 s86, 64\n\t" \
  "s_cbranch_scc1 .Lp0%=\n\t" \
  "s_barrier\n\t" \
  "s_nop 1\n\t" \
  "ds_write_b128 v252, v[152:155] offset:0\n\t" \
  "s_nop 0\n\t" \
  "ds_write_b128 v252, v[156:159] offset:1024\n\t" \
  "s_waitcnt lgkmcnt(0)\n\t" \
  "s_barrier\n\t" \
  "ds_read_b128 v[160:163], v253 offset:0\n\t" \
  "ds_read_b128 v[164:167], v253 offset:1024\n\t" \
  "s_waitcnt lgkmcnt(0)\n\t" \
  "s_lshr_b32 s90, s87, 7\n\t" \
  "s_xor_b32 s90, s90, %[s0]\n\t" \
  "s_and_b32 s90, s90, 1\n\t" \
  "s_lshl1_add_u32 s90, s87, s90\n\t" \
  "s_lshl_b32 s90, s90, 13\n\t" \
  "v_add_f32 v160, v160, v144\n\t" \
  "v_add_f32 v161, v161, v145\n\t" \
  "v_add_f32 v162, v162, v146\n\t" \
  "v_add_f32 v163, v163, v147\n\t" \
  "v_add_f32 v164, v164, v148\n\t" \
  "v_add_f32 v165, v165, v149\n\t" \
  "v_add_f32 v166, v166, v150\n\t" \
  "v_add_f32 v167, v167, v151\n\t" \
  "v_cvt_pk_bf16_f32 v168, v160, v161\n\t" \
  "v_cvt_pk_bf16_f32 v169, v162, v163\n\t" \
  "v_cvt_pk_bf16_f32 v170, v164, v165\n\t" \
  "v_cvt_pk_bf16_f32 v171, v166, v167\n\t" \
  "buffer_store_dwordx4 v[168:171], v172, %[rt], s90 offen sc1\n\t" \
  "s_waitcnt vmcnt(0)\n\t" \
  "s_barrier\n\t" \
  "s_mov_b32 m0, s94\n\t" \
  "s_branch .Le%=\n\t" \
  ".Lk%=:\n\t" \
  "s_mov_b32 s94, m0\n\t" \
  "s_mov_b64 s[92:93], %[x0]\n\t" \
  "s_mov_b32 s86, %[g0]\n\t" \
  "s_add_u32 s87, s86, %[t0]\n\t" \
  "s_add_u32 m0, %[lw], 67584\n\t" \
  "s_nop 0\n\t" \
  "global_load_lds_dwordx4 v250, s[92:93] sc1\n\t" \
  "s_add_u32 m0, %[lw], 68608\n\t" \
  "s_nop 0\n\t" \
  "global_load_lds_dwordx4 v251, s[92:93] sc1\n\t" \
  "s_barrier\n\t" \
  "s_branch .Ls1%=\n\t" \
  ".Lp1%=:\n\t" \
  "s_waitcnt vmcnt(10)\n\t" \
  "s_barrier\n\t" \
  "s_setprio 2\n\t" \
  "v_mfma_f32_16x16x32_bf16 v[144:147], v[176:179], v[64:67], v[144:147]\n\t" \
  "v_mfma_f32_16x16x32_bf16 v[152:155], v[176:179], v[68:71], v[152:155]\n\t" \
  "v_mfma_f32_16x16x32_bf16 v[148:151], v[180:183], v[64:67], v[148:151]\n\t" \
  "v_mfma_f32_16x16x32_bf16 v[156:159], v[180:183], v[68:71], v[156:159]\n\t" \
  "v_mfma_f32_16x16x32_bf16 v[144:147], v[184:187], v[72:75], v[144:147]\n\t" \
  "v_mfma_f32_16x16x32_bf16 v[152:155], v[184:187], v[76:79], v[152:155]\n\t" \
  "v_mfma_f32_16x16x32_bf16 v[148:151], v[188:191], v[72:75], v[148:151]\n\t" \
  "v_mfma_f32_16x16x32_bf16 v[156:159], v[188:191], v[76:79], v[156:159]\n\t" \
  "v_mfma_f32_16x16x32_bf16 v[144:147], v[192:195], v[80:83], v[144:147]\n\t" \
  "v_mfma_f32_16x16x32_bf16 v[152:155], v[192:195], v[84:87], v[152:155]\n\t" \
  "v_mfma_f32_16x16x32_bf16 v[148:151], v[196:199], v[80:83], v[148:151]\n\t" \
  "v_mfma_f32_16x16x32_bf16 v[156:159], v[196:199], v[84:87], v[156:159]\n\t" \
  "v_mfma_f32_16x16x32_bf16 v[144:147], v[200:203], v[88:91], v[144:147]\n\t" \
  "v_mfma_f32_16x16x32_bf16 v[152:155], v[200:203], v[92:95], v[152:155]\n\t" \
  "v_mfma_f32_16x16x32_bf16 v[148:151], v[204:207], v[88:91], v[148:151]\n\t" \
  "v_mfma_f32_16x16x32_bf16 v[156:159], v[204:207], v[92:95], v[156:159]\n\t" \
  "v_mfma_f32_16x16x32_bf16 v[144:147], v[208:211], v[96:99], v[144:147]\n\t" \
  "v_mfma_f32_16x16x32_bf16 v[152:155], v[208:211], v[100:103], v[152:155]\n\t" \
  "v_mfma_f32_16x16x32_bf16 v[148:151], v[212:215], v[96:99], v[148:151]\n\t" \
  "v_mfma_f32_16x16x32_bf16 v[156:159], v[212:215], v[100:103], v[156:159]\n\t" \
  "v_mfma_f32_16x16x32_bf16 v[144:147], v[216:219], v[104:107], v[144:147]\n\t" \
  "v_mfma_f32_16x16x32_bf16 v[152:155], v[216:219], v[108:111], v[152:155]\n\t" \
  "v_mfma_f32_16x16x32_bf16 v[148:151], v[220:223], v[104:107], v[148:151]\n\t" \
  "v_mfma_f32_16x16x32_bf16 v[156:159], v[220:223], v[108:111], v[156:159]\n\t" \
  "v_mfma_f32_16x16x32_bf16 v[144:147], v[224:227], v[112:115], v[144:147]\n\t" \
  "v_mfma_f32_16x16x32_bf16 v[152:155], v[224:227], v[116:119], v[152:155]\n\t" \
  "v_mfma_f32_16x16x32_bf16 v[148:151], v[228:231], v[112:115], v[148:151]\n\t" \
  "v_mfma_f32_16x16x32_bf16 v[156:159], v[228:231], v[116:119], v[156:159]\n\t" \
  "v_mfma_f32_16x16x32_bf16 v[144:147], v[232:235], v[120:123], v[144:147]\n\t" \
  "v_mfma_f32_16x16x32_bf16 v[152:155], v[232:235], v[124:127], v[152:155]\n\t" \
  "v_mfma_f32_16x16x32_bf16 v[148:151], v[236:239], v[120:123], v[148:151]\n\t" \
  "v_mfma_f32_16x16x32_bf16 v[156:159], v[236:239], v[124:127], v[156:159]\n\t" \
  "s_setprio 0\n\t" \
  ".Ls1%=:\n\t" \
  "s_add_u32 s92, s92, 512\n\t" \
  "s_addc_u32 s93, s93, 0\n\t" \
  "ds_read_b128 v[176:179], v240 offset:0\n\t" \
  "ds_read_b128 v[180:183], v240 offset:16384\n\t" \
  "ds_read_b128 v[184:187], v241 offset:0\n\t" \
  "ds_read_b128 v[188:191], v241 offset:16384\n\t" \
  "s_add_u32 m0, %[lw], 98304\n\t" \
  "s_nop 0\n\t" \
  "global_load_lds_dwordx4 v248, s[92:93] sc1\n\t" \
  "ds_read_b128 v[192:195], v242 offset:0\n\t" \
  "ds_read_b128 v[196:199], v242 offset:16384\n\t" \
  "ds_read_b128 v[200:203], v243 offset:0\n\t" \
  "ds_read_b128 v[204:207], v243 offset:16384\n\t" \
  "s_add_u32 m0, %[lw], 99328\n\t" \
  "s_nop 0\n\t" \
  "global_load_lds_dwordx4 v249, s[92:93] sc1\n\t" \
  "ds_read_b128 v[208:211], v240 offset:256\n\t" \
  "ds_read_b128 v[212:215], v240 offset:16640\n\t" \
  "ds_read_b128 v[216:219], v241 offset:256\n\t" \
  "ds_read_b128 v[220:223], v241 offset:16640\n\t" \
  "s_add_u32 m0, %[lw], 100352\n\t" \
  "s_nop 0\n\t" \
  "global_load_lds_dwordx4 v250, s[92:93] sc1\n\t" \
  "ds_read_b128 v[224:227], v242 offset:256\n\t" \
  "ds_read_b128 v[228:231], v242 offset:16640\n\t" \
  "ds_read_b128 v[232:235], v243 offset:256\n\t" \
  "ds_read_b128 v[236:239], v243 offset:16640\n\t" \
  "s_add_u32 m0, %[lw], 101376\n\t" \
  "s_nop 0\n\t" \
  "global_load_lds_dwordx4 v251, s[92:93] sc1\n\t" \
  "ds_write_b128 v252, v[152:155] offset:0\n\t" \
  "ds_write_b128 v252, v[156:159] offset:1024\n\t" \
  "s_waitcnt lgkmcnt(0)\n\t" \
  "s_waitcnt vmcnt(4)\n\t" \
  "s_barrier\n\t" \
  "s_setprio 2\n\t" \
  "v_mfma_f32_16x16x32_bf16 v[128:131], v[176:179], v[0:3], 0\n\t" \
  "v_mfma_f32_16x16x32_bf16 v[136:139], v[176:179], v[4:7], 0\n\t" \
  "v_mfma_f32_16x16x32_bf16 v[132:135], v[180:183], v[0:3], 0\n\t" \
  "v_mfma_f32_16x16x32_bf16 v[140:143], v[180:183], v[4:7], 0\n\t" \
  "v_mfma_f32_16x16x32_bf16 v[128:131], v[184:187], v[8:11], v[128:131]\n\t" \
  "v_mfma_f32_16x16x32_bf16 v[136:139], v[184:187], v[12:15], v[136:139]\n\t" \
  "v_mfma_f32_16x16x32_bf16 v[132:135], v[188:191], v[8:11], v[132:135]\n\t" \
  "v_mfma_f32_16x16x32_bf16 v[140:143], v[188:191], v[12:15], v[140:143]\n\t" \
  "v_mfma_f32_16x16x32_bf16 v[128:131], v[192:195], v[16:19], v[128:131]\n\t" \
  "v_mfma_f32_16x16x32_bf16 v[136:139], v[192:195], v[20:23], v[136:139]\n\t" \
  "v_mfma_f32_16x16x32_bf16 v[132:135], v[196:199], v[16:19], v[132:135]\n\t" \
  "v_mfma_f32_16x16x32_bf16 v[140:143], v[196:199], v[20:23], v[140:143]\n\t" \
  "v_mfma_f32_16x16x32_bf16 v[128:131], v[200:203], v[24:27], v[128:131]\n\t" \
  "v_mfma_f32_16x16x32_bf16 v[136:139], v[200:203], v[28:31], v[136:139]\n\t" \
  "v_mfma_f32_16x16x32_bf16 v[132:135], v[204:207], v[24:27], v[132:135]\n\t" \
  "v_mfma_f32_16x16x32_bf16 v[140:143], v[204:207], v[28:31], v[140:143]\n\t" \
  "v_mfma_f32_16x16x32_bf16 v[128:131], v[208:211], v[32:35], v[128:131]\n\t" \
  "v_mfma_f32_16x16x32_bf16 v[136:139], v[208:211], v[36:39], v[136:139]\n\t" \
  "v_mfma_f32_16x16x32_bf16 v[132:135], v[212:215], v[32:35], v[132:135]\n\t" \
  "v_mfma_f32_16x16x32_bf16 v[140:143], v[212:215], v[36:39], v[140:143]\n\t" \
  "v_mfma_f32_16x16x32_bf16 v[128:131], v[216:219], v[40:43], v[128:131]\n\t" \
  "v_mfma_f32_16x16x32_bf16 v[136:139], v[216:219], v[44:47], v[136:139]\n\t" \
  "v_mfma_f32_16x16x32_bf16 v[132:135], v[220:223], v[40:43], v[132:135]\n\t" \
  "v_mfma_f32_16x16x32_bf16 v[140:143], v[220:223], v[44:47], v[140:143]\n\t" \
  "v_mfma_f32_16x16x32_bf16 v[128:131], v[224:227], v[48:51], v[128:131]\n\t" \
  "v_mfma_f32_16x16x32_bf16 v[136:139], v[224:227], v[52:55], v[136:139]\n\t" \
  "v_mfma_f32_16x16x32_bf16 v[132:135], v[228:231], v[48:51], v[132:135]\n\t" \
  "v_mfma_f32_16x16x32_bf16 v[140:143], v[228:231], v[52:55], v[140:143]\n\t" \
  "v_mfma_f32_16x16x32_bf16 v[128:131], v[232:235], v[56:59], v[128:131]\n\t" \
  "v_mfma_f32_16x16x32_bf16 v[136:139], v[232:235], v[60:63], v[136:139]\n\t" \
  "v_mfma_f32_16x16x32_bf16 v[132:135], v[236:239], v[56:59], v[132:135]\n\t" \
  "v_mfma_f32_16x16x32_bf16 v[140:143], v[236:239], v[60:63], v[140:143]\n\t" \
  "s_setprio 0\n\t" \
  "s_add_u32 s92, s92, 65024\n\t" \
  "s_addc_u32 s93, s93, 0\n\t" \
  "ds_read_b128 v[160:163], v253 offset:0\n\t" \
  "ds_read_b128 v[164:167], v253 offset:1024\n\t" \
  "ds_read_b128 v[176:179], v240 offset:32768\n\t" \
  "ds_read_b128 v[180:183], v240 offset:49152\n\t" \
  "ds_read_b128 v[184:187], v241 offset:32768\n\t" \
  "ds_read_b128 v[188:191], v241 offset:49152\n\t" \
  "s_add_u32 m0, %[lw], 0\n\t" \
  "s_nop 0\n\t" \
  "global_load_lds_dwordx4 v248, s[92:93] sc1\n\t" \
  "ds_read_b128 v[192:195], v242 offset:32768\n\t" \
  "ds_read_b128 v[196:199], v242 offset:49152\n\t" \
  "ds_read_b128 v[200:203], v243 offset:32768\n\t" \
  "ds_read_b128 v[204:207], v243 offset:49152\n\t" \
  "s_add_u32 m0, %[lw], 1024\n\t" \
  "s_nop 0\n\t" \
  "global_load_lds_dwordx4 v249, s[92:93] sc1\n\t" \
  "ds_read_b128 v[208:211], v240 offset:33024\n\t" \
  "ds_read_b128 v[212:215], v240 offset:49408\n\t" \
  "ds_read_b128 v[216:219], v241 offset:33024\n\t" \
  "ds_read_b128 v[220:223], v241 offset:49408\n\t" \
  "s_add_u32 m0, %[lw], 2048\n\t" \
  "s_nop 0\n\t" \
  "global_load_lds_dwordx4 v250, s[92:93] sc1\n\t" \
  "ds_read_b128 v[224:227], v242 offset:33024\n\t" \
  "ds_read_b128 v[228:231], v242 offset:49408\n\t" \
  "ds_read_b128 v[232:235], v243 offset:33024\n\t" \
  "ds_read_b128 v[236:239], v243 offset:49408\n\t" \
  "s_add_u32 m0, %[lw], 3072\n\t" \
  "s_nop 0\n\t" \
  "global_load_lds_dwordx4 v251, s[92:93] sc1\n\t" \
  "s_waitcnt lgkmcnt(15)\n\t" \
  "s_waitcnt lgkmcnt(12)\n\t" \
  "s_sub_i32 s91, s86, 2\n\t" \
  "v_mov_b32 v173, s91\n\t" \
  "buffer_store_dword v173, v174, %[rs], 0 offen sc1\n\t" \
  "s_lshr_b32 s90, s87, 7\n\t" \
  "s_xor_b32 s90, s90, %[s0]\n\t" \
  "s_and_b32 s90, s90, 1\n\t" \
  "s_lshl1_add_u32 s90, s87, s90\n\t" \
  "s_lshl_b32 s90, s90, 13\n\t" \
  "v_add_f32 v160, v160, v144\n\t" \
  "v_add_f32 v161, v161, v145\n\t" \
  "v_add_f32 v162, v162, v146\n\t" \
  "v_add_f32 v163, v163, v147\n\t" \
  "v_add_f32 v164, v164, v148\n\t" \
  "v_add_f32 v165, v165, v149\n\t" \
  "v_add_f32 v166, v166, v150\n\t" \
  "v_add_f32 v167, v167, v151\n\t" \
  "v_cvt_pk_bf16_f32 v168, v160, v161\n\t" \
  "v_cvt_pk_bf16_f32 v169, v162, v163\n\t" \
  "v_cvt_pk_bf16_f32 v170, v164, v165\n\t" \
  "v_cvt_pk_bf16_f32 v171, v166, v167\n\t" \
  "buffer_store_dwordx4 v[168:171], v172, %[rt], s90 offen sc1\n\t" \
  "s_add_u32 s86, s86, 1\n\t" \
  "s_add_u32 s87, s87, 1\n\t" \
  "s_waitcnt lgkmcnt(0)\n\t" \
  "s_waitcnt vmcnt(10)\n\t" \
  "s_barrier\n\t" \
  "s_setprio 2\n\t" \
  "v_mfma_f32_16x16x32_bf16 v[128:131], v[176:179], v[64:67], v[128:131]\n\t" \
  "v_mfma_f32_16x16x32_bf16 v[136:139], v[176:179], v[68:71], v[136:139]\n\t" \
  "v_mfma_f32_16x16x32_bf16 v[132:135], v[180:183], v[64:67], v[132:135]\n\t" \
  "v_mfma_f32_16x16x32_bf16 v[140:143], v[180:183], v[68:71], v[140:143]\n\t" \
  "v_mfma_f32_16x16x32_bf16 v[128:131], v[184:187], v[72:75], v[128:131]\n\t" \
  "v_mfma_f32_16x16x32_bf16 v[136:139], v[184:187], v[76:79], v[136:139]\n\t" \
  "v_mfma_f32_16x16x32_bf16 v[132:135], v[188:191], v[72:75], v[132:135]\n\t" \
  "v_mfma_f32_16x16x32_bf16 v[140:143], v[188:191], v[76:79], v[140:143]\n\t" \
  "v_mfma_f32_16x16x32_bf16 v[128:131], v[192:195], v[80:83], v[128:131]\n\t" \
  "v_mfma_f32_16x16x32_bf16 v[136:139], v[192:195], v[84:87], v[136:139]\n\t" \
  "v_mfma_f32_16x16x32_bf16 v[132:135], v[196:199], v[80:83], v[132:135]\n\t" \
  "v_mfma_f32_16x16x32_bf16 v[140:143], v[196:199], v[84:87], v[140:143]\n\t" \
  "v_mfma_f32_16x16x32_bf16 v[128:131], v[200:203], v[88:91], v[128:131]\n\t" \
  "v_mfma_f32_16x16x32_bf16 v[136:139], v[200:203], v[92:95], v[136:139]\n\t" \
  "v_mfma_f32_16x16x32_bf16 v[132:135], v[204:207], v[88:91], v[132:135]\n\t" \
  "v_mfma_f32_16x16x32_bf16 v[140:143], v[204:207], v[92:95], v[140:143]\n\t" \
  "v_mfma_f32_16x16x32_bf16 v[128:131], v[208:211], v[96:99], v[128:131]\n\t" \
  "v_mfma_f32_16x16x32_bf16 v[136:139], v[208:211], v[100:103], v[136:139]\n\t" \
  "v_mfma_f32_16x16x32_bf16 v[132:135], v[212:215], v[96:99], v[132:135]\n\t" \
  "v_mfma_f32_16x16x32_bf16 v[140:143], v[212:215], v[100:103], v[140:143]\n\t" \
  "v_mfma_f32_16x16x32_bf16 v[128:131], v[216:219], v[104:107], v[128:131]\n\t" \
  "v_mfma_f32_16x16x32_bf16 v[136:139], v[216:219], v[108:111], v[136:139]\n\t" \
  "v_mfma_f32_16x16x32_bf16 v[132:135], v[220:223], v[104:107], v[132:135]\n\t" \
  "v_mfma_f32_16x16x32_bf16 v[140:143], v[220:223], v[108:111], v[140:143]\n\t" \
  "v_mfma_f32_16x16x32_bf16 v[128:131], v[224:227], v[112:115], v[128:131]\n\t" \
  "v_mfma_f32_16x16x32_bf16 v[136:139], v[224:227], v[116:119], v[136:139]\n\t" \
  "v_mfma_f32_16x16x32_bf16 v[132:135], v[228:231], v[112:115], v[132:135]\n\t" \
  "v_mfma_f32_16x16x32_bf16 v[140:143], v[228:231], v[116:119], v[140:143]\n\t" \
  "v_mfma_f32_16x16x32_bf16 v[128:131], v[232:235], v[120:123], v[128:131]\n\t" \
  "v_mfma_f32_16x16x32_bf16 v[136:139], v[232:235], v[124:127], v[136:139]\n\t" \
  "v_mfma_f32_16x16x32_bf16 v[132:135], v[236:239], v[120:123], v[132:135]\n\t" \
  "v_mfma_f32_16x16x32_bf16 v[140:143], v[236:239], v[124:127], v[140:143]\n\t" \
  "s_setprio 0\n\t" \
  "s_add_u32 s92, s92, 512\n\t" \
  "s_addc_u32 s93, s93, 0\n\t" \
  "ds_read_b128 v[176:179], v244 offset:0\n\t" \
  "ds_read_b128 v[180:183], v244 offset:16384\n\t" \
  "ds_read_b128 v[184:187], v245 offset:0\n\t" \
  "ds_read_b128 v[188:191], v245 offset:16384\n\t" \
  "s_add_u32 m0, %[lw], 32768\n\t" \
  "s_nop 0\n\t" \
  "global_load_lds_dwordx4 v248, s[92:93] sc1\n\t" \
  "ds_read_b128 v[192:195], v246 offset:0\n\t" \
  "ds_read_b128 v[196:199], v246 offset:16384\n\t" \
  "ds_read_b128 v[200:203], v247 offset:0\n\t" \
  "ds_read_b128 v[204:207], v247 offset:16384\n\t" \
  "s_add_u32 m0, %[lw], 33792\n\t" \
  "s_nop 0\n\t" \
  "global_load_lds_dwordx4 v249, s[92:93] sc1\n\t" \
  "ds_read_b128 v[208:211], v244 offset:256\n\t" \
  "ds_read_b128 v[212:215], v244 offset:16640\n\t" \
  "ds_read_b128 v[216:219], v245 offset:256\n\t" \
  "ds_read_b128 v[220:223], v245 offset:16640\n\t" \
  "s_add_u32 m0, %[lw], 34816\n\t" \
  "s_nop 0\n\t" \
  "global_load_lds_dwordx4 v250, s[92:93] sc1\n\t" \
  "ds_read_b128 v[224:227], v246 offset:256\n\t" \
  "ds_read_b128 v[228:231], v246 offset:16640\n\t" \
  "ds_read_b128 v[232:235], v247 offset:256\n\t" \
  "ds_read_b128 v[236:239], v247 offset:16640\n\t" \
  "s_add_u32 m0, %[lw], 35840\n\t" \
  "s_nop 0\n\t" \
  "global_load_lds_dwordx4 v251, s[92:93] sc1\n\t" \
  "ds_write_b128 v252, v[136:139] offset:0\n\t" \
  "ds_write_b128 v252, v[140:143] offset:1024\n\t" \
  "s_waitcnt lgkmcnt(0)\n\t" \
  "s_waitcnt vmcnt(4)\n\t" \
  "s_barrier\n\t" \
  "s_setprio 2\n\t" \
  "v_mfma_f32_16x16x32_bf16 v[144:147], v[176:179], v[0:3], 0\n\t" \
  "v_mfma_f32_16x16x32_bf16 v[152:155], v[176:179], v[4:7], 0\n\t" \
  "v_mfma_f32_16x16x32_bf16 v[148:151], v[180:183], v[0:3], 0\n\t" \
  "v_mfma_f32_16x16x32_bf16 v[156:159], v[180:183], v[4:7], 0\n\t" \
  "v_mfma_f32_16x16x32_bf16 v[144:147], v[184:187], v[8:11], v[144:147]\n\t" \
  "v_mfma_f32_16x16x32_bf16 v[152:155], v[184:187], v[12:15], v[152:155]\n\t" \
  "v_mfma_f32_16x16x32_bf16 v[148:151], v[188:191], v[8:11], v[148:151]\n\t" \
  "v_mfma_f32_16x16x32_bf16 v[156:159], v[188:191], v[12:15], v[156:159]\n\t" \
  "v_mfma_f32_16x16x32_bf16 v[144:147], v[192:195], v[16:19], v[144:147]\n\t" \
  "v_mfma_f32_16x16x32_bf16 v[152:155], v[192:195], v[20:23], v[152:155]\n\t" \
  "v_mfma_f32_16x16x32_bf16 v[148:151], v[196:199], v[16:19], v[148:151]\n\t" \
  "v_mfma_f32_16x16x32_bf16 v[156:159], v[196:199], v[20:23], v[156:159]\n\t" \
  "v_mfma_f32_16x16x32_bf16 v[144:147], v[200:203], v[24:27], v[144:147]\n\t" \
  "v_mfma_f32_16x16x32_bf16 v[152:155], v[200:203], v[28:31], v[152:155]\n\t" \
  "v_mfma_f32_16x16x32_bf16 v[148:151], v[204:207], v[24:27], v[148:151]\n\t" \
  "v_mfma_f32_16x16x32_bf16 v[156:159], v[204:207], v[28:31], v[156:159]\n\t" \
  "v_mfma_f32_16x16x32_bf16 v[144:147], v[208:211], v[32:35], v[144:147]\n\t" \
  "v_mfma_f32_16x16x32_bf16 v[152:155], v[208:211], v[36:39], v[152:155]\n\t" \
  "v_mfma_f32_16x16x32_bf16 v[148:151], v[212:215], v[32:35], v[148:151]\n\t" \
  "v_mfma_f32_16x16x32_bf16 v[156:159], v[212:215], v[36:39], v[156:159]\n\t" \
  "v_mfma_f32_16x16x32_bf16 v[144:147], v[216:219], v[40:43], v[144:147]\n\t" \
  "v_mfma_f32_16x16x32_bf16 v[152:155], v[216:219], v[44:47], v[152:155]\n\t" \
  "v_mfma_f32_16x16x32_bf16 v[148:151], v[220:223], v[40:43], v[148:151]\n\t" \
  "v_mfma_f32_16x16x32_bf16 v[156:159], v[220:223], v[44:47], v[156:159]\n\t" \
  "v_mfma_f32_16x16x32_bf16 v[144:147], v[224:227], v[48:51], v[144:147]\n\t" \
  "v_mfma_f32_16x16x32_bf16 v[152:155], v[224:227], v[52:55], v[152:155]\n\t" \
  "v_mfma_f32_16x16x32_bf16 v[148:151], v[228:231], v[48:51], v[148:151]\n\t" \
  "v_mfma_f32_16x16x32_bf16 v[156:159], v[228:231], v[52:55], v[156:159]\n\t" \
  "v_mfma_f32_16x16x32_bf16 v[144:147], v[232:235], v[56:59], v[144:147]\n\t" \
  "v_mfma_f32_16x16x32_bf16 v[152:155], v[232:235], v[60:63], v[152:155]\n\t" \
  "v_mfma_f32_16x16x32_bf16 v[148:151], v[236:239], v[56:59], v[148:151]\n\t" \
  "v_mfma_f32_16x16x32_bf16 v[156:159], v[236:239], v[60:63], v[156:159]\n\t" \
  "s_setprio 0\n\t" \
  "s_add_u32 s92, s92, 65024\n\t" \
  "s_addc_u32 s93, s93, 0\n\t" \
  "ds_read_b128 v[160:163], v253 offset:0\n\t" \
  "ds_read_b128 v[164:167], v253 offset:1024\n\t" \
  "ds_read_b128 v[176:179], v244 offset:32768\n\t" \
  "ds_read_b128 v[180:183], v244 offset:49152\n\t" \
  "ds_read_b128 v[184:187], v245 offset:32768\n\t" \
  "ds_read_b128 v[188:191], v245 offset:49152\n\t" \
  "s_add_u32 m0, %[lw], 65536\n\t" \
  "s_nop 0\n\t" \
  "global_load_lds_dwordx4 v248, s[92:93] sc1\n\t" \
  "ds_read_b128 v[192:195], v246 offset:32768\n\t" \
  "ds_read_b128 v[196:199], v246 offset:49152\n\t" \
  "ds_read_b128 v[200:203], v247 offset:32768\n\t" \
  "ds_read_b128 v[204:207], v247 offset:49152\n\t" \
  "s_add_u32 m0, %[lw], 66560\n\t" \
  "s_nop 0\n\t" \
  "global_load_lds_dwordx4 v249, s[92:93] sc1\n\t" \
  "ds_read_b128 v[208:211], v244 offset:33024\n\t" \
  "ds_read_b128 v[212:215], v244 offset:49408\n\t" \
  "ds_read_b128 v[216:219], v245 offset:33024\n\t" \
  "ds_read_b128 v[220:223], v245 offset:49408\n\t" \
  "s_add_u32 m0, %[lw], 67584\n\t" \
  "s_nop 0\n\t" \
  "global_load_lds_dwordx4 v250, s[92:93] sc1\n\t" \
  "ds_read_b128 v[224:227], v246 offset:33024\n\t" \
  "ds_read_b128 v[228:231], v246 offset:49408\n\t" \
  "ds_read_b128 v[232:235], v247 offset:33024\n\t" \
  "ds_read_b128 v[236:239], v247 offset:49408\n\t" \
  "s_add_u32 m0, %[lw], 68608\n\t" \
  "s_nop 0\n\t" \
  "global_load_lds_dwordx4 v251, s[92:93] sc1\n\t" \
  "s_waitcnt lgkmcnt(15)\n\t" \
  "s_waitcnt lgkmcnt(12)\n\t" \
  "s_sub_i32 s91, s86, 2\n\t" \
  "v_mov_b32 v173, s91\n\t" \
  "buffer_store_dword v173, v174, %[rs], 0 offen sc1\n\t" \
  "s_lshr_b32 s90, s87, 7\n\t" \
  "s_xor_b32 s90, s90, %[s0]\n\t" \
  "s_and_b32 s90, s90, 1\n\t" \
  "s_lshl1_add_u32 s90, s87, s90\n\t" \
  "s_lshl_b32 s90, s90, 13\n\t" \
  "v_add_f32 v160, v160, v128\n\t" \
  "v_add_f32 v161, v161, v129\n\t" \
  "v_add_f32 v162, v162, v130\n\t" \
  "v_add_f32 v163, v163, v131\n\t" \
  "v_add_f32 v164, v164, v132\n\t" \
  "v_add_f32 v165, v165, v133\n\t" \
  "v_add_f32 v166, v166, v134\n\t" \
  "v_add_f32 v167, v167, v135\n\t" \
  "v_cvt_pk_bf16_f32 v168, v160, v161\n\t" \
  "v_cvt_pk_bf16_f32 v169, v162, v163\n\t" \
  "v_cvt_pk_bf16_f32 v170, v164, v165\n\t" \
  "v_cvt_pk_bf16_f32 v171, v166, v167\n\t" \
  "buffer_store_dwordx4 v[168:171], v172, %[rt], s90 offen sc1\n\t" \
  "s_add_u32 s86, s86, 1\n\t" \
  "s_add_u32 s87, s87, 1\n\t" \
  "s_waitcnt lgkmcnt(0)\n\t" \
  "s_cmp_lt_u32 s86, 64\n\t" \
  "s_cbranch_scc1 .Lp1%=\n\t" \
  "s_barrier\n\t" \
  "s_setprio 2\n\t" \
  "v_mfma_f32_16x16x32_bf16 v[144:147], v[176:179], v[64:67], v[144:147]\n\t" \
  "v_mfma_f32_16x16x32_bf16 v[152:155], v[176:179], v[68:71], v[152:155]\n\t" \
  "v_mfma_f32_16x16x32_bf16 v[148:151], v[180:183], v[64:67], v[148:151]\n\t" \
  "v_mfma_f32_16x16x32_bf16 v[156:159], v[180:183], v[68:71], v[156:159]\n\t" \
  "v_mfma_f32_16x16x32_bf16 v[144:147], v[184:187], v[72:75], v[144:147]\n\t" \
  "v_mfma_f32_16x16x32_bf16 v[152:155], v[184:187], v[76:79], v[152:155]\n\t" \
  "v_mfma_f32_16x16x32_bf16 v[148:151], v[188:191], v[72:75], v[148:151]\n\t" \
  "v_mfma_f32_16x16x32_bf16 v[156:159], v[188:191], v[76:79], v[156:159]\n\t" \
  "v_mfma_f32_16x16x32_bf16 v[144:147], v[192:195], v[80:83], v[144:147]\n\t" \
  "v_mfma_f32_16x16x32_bf16 v[152:155], v[192:195], v[84:87], v[152:155]\n\t" \
  "v_mfma_f32_16x16x32_bf16 v[148:151], v[196:199], v[80:83], v[148:151]\n\t" \
  "v_mfma_f32_16x16x32_bf16 v[156:159], v[196:199], v[84:87], v[156:159]\n\t" \
  "v_mfma_f32_16x16x32_bf16 v[144:147], v[200:203], v[88:91], v[144:147]\n\t" \
  "v_mfma_f32_16x16x32_bf16 v[152:155], v[200:203], v[92:95], v[152:155]\n\t" \
  "v_mfma_f32_16x16x32_bf16 v[148:151], v[204:207], v[88:91], v[148:151]\n\t" \
  "v_mfma_f32_16x16x32_bf16 v[156:159], v[204:207], v[92:95], v[156:159]\n\t" \
  "v_mfma_f32_16x16x32_bf16 v[144:147], v[208:211], v[96:99], v[144:147]\n\t" \
  "v_mfma_f32_16x16x32_bf16 v[152:155], v[208:211], v[100:103], v[152:155]\n\t" \
  "v_mfma_f32_16x16x32_bf16 v[148:151], v[212:215], v[96:99], v[148:151]\n\t" \
  "v_mfma_f32_16x16x32_bf16 v[156:159], v[212:215], v[100:103], v[156:159]\n\t" \
  "v_mfma_f32_16x16x32_bf16 v[144:147], v[216:219], v[104:107], v[144:147]\n\t" \
  "v_mfma_f32_16x16x32_bf16 v[152:155], v[216:219], v[108:111], v[152:155]\n\t" \
  "v_mfma_f32_16x16x32_bf16 v[148:151], v[220:223], v[104:107], v[148:151]\n\t" \
  "v_mfma_f32_16x16x32_bf16 v[156:159], v[220:223], v[108:111], v[156:159]\n\t" \
  "v_mfma_f32_16x16x32_bf16 v[144:147], v[224:227], v[112:115], v[144:147]\n\t" \
  "v_mfma_f32_16x16x32_bf16 v[152:155], v[224:227], v[116:119], v[152:155]\n\t" \
  "v_mfma_f32_16x16x32_bf16 v[148:151], v[228:231], v[112:115], v[148:151]\n\t" \
  "v_mfma_f32_16x16x32_bf16 v[156:159], v[228:231], v[116:119], v[156:159]\n\t" \
  "v_mfma_f32_16x16x32_bf16 v[144:147], v[232:235], v[120:123], v[144:147]\n\t" \
  "v_mfma_f32_16x16x32_bf16 v[152:155], v[232:235], v[124:127], v[152:155]\n\t" \
  "v_mfma_f32_16x16x32_bf16 v[148:151], v[236:239], v[120:123], v[148:151]\n\t" \
  "v_mfma_f32_16x16x32_bf16 v[156:159], v[236:239], v[124:127], v[156:159]\n\t" \
  "s_setprio 0\n\t" \
  "s_nop 4\n\t" \
  "ds_write_b128 v252, v[152:155] offset:0\n\t" \
  "s_nop 0\n\t" \
  "ds_write_b128 v252, v[156:159] offset:1024\n\t" \
  "s_waitcnt lgkmcnt(0)\n\t" \
  "s_barrier\n\t" \
  "ds_read_b128 v[160:163], v253 offset:0\n\t" \
  "ds_read_b128 v[164:167], v253 offset:1024\n\t" \
  "s_waitcnt lgkmcnt(0)\n\t" \
  "s_lshr_b32 s90, s87, 7\n\t" \
  "s_xor_b32 s90, s90, %[s0]\n\t" \
  "s_and_b32 s90, s90, 1\n\t" \
  "s_lshl1_add_u32 s90, s87, s90\n\t" \
  "s_lshl_b32 s90, s90, 13\n\t" \
  "v_add_f32 v160, v160, v144\n\t" \
  "v_add_f32 v161, v161, v145\n\t" \
  "v_add_f32 v162, v162, v146\n\t" \
  "v_add_f32 v163, v163, v147\n\t" \
  "v_add_f32 v164, v164, v148\n\t" \
  "v_add_f32 v165, v165, v149\n\t" \
  "v_add_f32 v166, v166, v150\n\t" \
  "v_add_f32 v167, v167, v151\n\t" \
  "v_cvt_pk_bf16_f32 v168, v160, v161\n\t" \
  "v_cvt_pk_bf16_f32 v169, v162, v163\n\t" \
  "v_cvt_pk_bf16_f32 v170, v164, v165\n\t" \
  "v_cvt_pk_bf16_f32 v171, v166, v167\n\t" \
  "buffer_store_dwordx4 v[168:171], v172, %[rt], s90 offen sc1\n\t" \
  "s_waitcnt vmcnt(0)\n\t" \
  "s_barrier\n\t" \
  "s_sub_i32 s91, s86, 1\n\t" \
  "v_mov_b32 v173, s91\n\t" \
  "buffer_store_dword v173, v174, %[rs], 0 offen sc1\n\t" \
  "s_mov_b32 m0, s94\n\t" \
  ".Le%=:\n\t"
constexpr int W_X = 131072, W_PM = W_X + 16384;
template <bool ISV>
__device__ __forceinline__ void job_proj(Args& p, int g, int which, int h, char* smem, __amdgpu_buffer_rsrc_t rsy) {
  const int tid = threadIdx.x, lane = tid & 63, r = lane & 15, q = lane >> 4;
  const int w = __builtin_amdgcn_readfirstlane(tid >> 6), kh = w >> 2, cg = w & 3;
  u32x32 W[4];
  const unsigned lw = lds_off(smem) + w * 4096;
  u32x4 vx;
  u32x2 tag;
  const char* xn = p.ws + WS_XN;
  auto row = [&](int t, int i) {
    const char* src = xn + (size_t)(min(t, 127) >> 1) * (SEG * XLD * 2) + (t & 1) * 512;
    unsigned keep;
    asm volatile("s_mov_b32 %0, m0\n\ts_mov_b32 m0, %3\n\ts_nop 0\n\tglobal_load_lds_dwordx4 %1, %2 sc1\n\ts_mov_b32 m0, %0"
                 : "=&s"(keep) : "v"(vx[i]), "s"(src), "s"(lw + (t & 3) * 32768 + i * 1024) : "memory");
  };
  {
    const u16* Wm = which == 0 ? p.Wk : p.Wq;
    asm volatile("" : "+s"(Wm));
    if (ISV) Wm = p.Wv;
    const unsigned vo = (tid >> 4) * (HD * 2) + 16 * ((lane & 15) ^ swzB((tid >> 4) & 15));
    auto stage = [&](int j) {
      const char* src = (const char*)(Wm + (size_t)(WREV ? 7 - j : j) * 64 * HD + h * DH);
      const unsigned dst = lds_off(smem) + (j & 3) * 32768 + w * 1024;
      glds2(src, 32 * HD * 2, vo, dst);
      glds2(src + 512 * HD * 2, 32 * HD * 2, vo, dst + 16384);
    };
    const unsigned own = min((h & LN_M) ^ LN_X, 1);
    tag = start_rows<16>(p, 32 + g, own, [&]() { stage(0); stage(1); stage(2); stage(3); });
#pragma unroll
    for (int i = 0; i < 4; ++i) vx[i] = ((4 * w + i) ^ (ISV ? 0 : (lane >> 1) & 16)) * (XLD * 2) + (lane >> 5) * 1024 + (((lane & 31) ^ ((4 * w + i) & 15)) << 4);
    Boot bt;
#pragma unroll
    for (int j = 0; j < 8; ++j) {
      if (j < 4) WAIT_V(16); else if (j == 4) WAIT_V(12); else if (j == 5) WAIT_V(8); else if (j == 6) WAIT_V(4); else WAIT_V(10);
      if (j == 6 && w == 0) lds_at(smem + W_PM)[0] = boot_done(p.ws, bt, true, tag);
      LBAR();
      if (j == 4 && own) report_rows(p, 32 + g, tag);
      const char* sb = smem + (j & 3) * 32768 + kh * 16384;
#pragma unroll
      for (int k = 0; k < 4; ++k) {
        const int ks = k >> 1, a = k & 1;
        const unsigned ch = ISV ? 2 * (2 * cg + (a ^ kh)) + ((r & 3) >> 1) : 4 * cg + (r & 3), by = ISV ? 8 * (r & 1) : 8 * a;
        s16x4 lo = __builtin_amdgcn_ds_read_tr16_b64_v4i16((lds_s16x4_ptr)(sb + offB(32 * ks + 8 * q + (r >> 2), ch) + by));
        s16x4 hi = __builtin_amdgcn_ds_read_tr16_b64_v4i16((lds_s16x4_ptr)(sb + offB(32 * ks + 8 * q + 4 + (r >> 2), ch) + by));
        const int f = 4 * (WREV ? 7 - j : j) + k;
        put4(W[f >> 3], f & 7, (bf16x8){lo[0], lo[1], lo[2], lo[3], hi[0], hi[1], hi[2], hi[3]});
      }
      LBAR();
      if (j == 3 && w == 0) boot_ask(p.ws, bt);
      if (j + 4 < 8) stage(j + 4);
      if (j == 6) {
#pragma unroll
        for (int i = 0; i < 10; ++i) row(i >> 2, i & 3);
      }
    }
    p.s0 = uni(lds_at(smem + W_PM)[0] >> 31);
  }
  typedef __attribute__((ext_vector_type(8))) unsigned u32x8;
  u32x8 la;
#pragma unroll
  for (int i = 0; i < 8; ++i) la[i] = (i >> 2) * 65536 + r * 1024 + kh * 512 + (((4 * (i & 3) + q) ^ r) << 4);
  const unsigned xw = W_X + w * 2048 + lane * 16;
  u32x32 acc;
  asm volatile("" : "={v[128:159]}"(acc));
  WAIT_V(6);
  LBAR();
  {
    const int g0 = 0;
#define PR_LOOP(TXT)                                                                                                              \
    asm volatile(TXT                                                                                                              \
        : "+{v[128:159]}"(acc)                                                                                                    \
        : W4_IN, "{v[208:215]}"(la), "{v[216:219]}"(vx), "{v220}"(xw), "{v221}"(xw ^ 8192),                                       \
          "{v172}"((ISV ? 2 * cg + kh : 4 * kh + cg) * 1024 + lane * 16), "{v174}"(tid == 256 ? fkq(h, which) : 0x7F000000),      \
          [rt] "s"(raw_rsrc(p.ws + WS_T, T_BYTES)), [rs] "s"(raw_rsrc(p.ws + WS_SY, SY_TOTAL)), [s0] "s"(p.s0 ^ TF_KQV),          \
          [lw] "s"(lw), [kh] "s"(kh), [g0] "s"(g0), [end] "s"(g0 ? NSEG : PR_G1), [x0] "s"(xn + (g0 + 1) * (SEG * XLD * 2)),                       \
          [t0] "s"(which * T_Q + kseg(0, h) - 1)                                                                                  \
        : CLB8(160, 161, 162, 163, 164, 165, 166, 167), "v168", "v169", "v170", "v171", "v173", CLB8(176, 177, 178, 179, 180, 181, 182, 183), \
          CLB8(184, 185, 186, 187, 188, 189, 190, 191), CLB8(192, 193, 194, 195, 196, 197, 198, 199),                             \
          CLB8(200, 201, 202, 203, 204, 205, 206, 207), "s86", "s87", "s88", "s90", "s91", "s92", "s93", "s94", "scc", "memory")
    if (ISV) PR_LOOP(PR_ASM_1); else PR_LOOP(PR_ASM_0);
    if (w == 0 && g0 == 0) wait_rows(p.ws, tag);
  }
#define PP_LOOP(TXT)                                                                                                              \
  asm volatile(TXT                                                                                                                \
      : "+{v[128:159]}"(acc)                                                                                                      \
      : W4_IN, "{v[240:247]}"(la), "{v[248:251]}"(vx), "{v252}"(xw), "{v253}"(xw ^ 8192),                                         \
        "{v172}"((ISV ? 2 * cg + kh : 4 * kh + cg) * 1024 + lane * 16), "{v174}"(tid == 256 ? fkq(h, which) : 0x7F000000),        \
        [rt] "s"(raw_rsrc(p.ws + WS_T, T_BYTES)), [rs] "s"(raw_rsrc(p.ws + WS_SY, SY_TOTAL)), [s0] "s"(p.s0 ^ TF_KQV),            \
        [lw] "s"(lw), [kh] "s"(kh), [g0] "n"(PR_G1), [x0] "s"(xn + (PR_G1 + 1) * (SEG * XLD * 2)),                                \
        [t0] "s"(which * T_Q + kseg(0, h) - 1)                                                                                    \
      : CLB8(160, 161, 162, 163, 164, 165, 166, 167), "v168", "v169", "v170", "v171", "v173", CLB8(176, 177, 178, 179, 180, 181, 182, 183), \
        CLB8(184, 185, 186, 187, 188, 189, 190, 191), CLB32(192), CLB8(224, 225, 226, 227, 228, 229, 230, 231),                   \
        CLB8(232, 233, 234, 235, 236, 237, 238, 239), "s86", "s87", "s90", "s91", "s92", "s93", "s94", "scc", "memory")
  if (ISV) PP_LOOP(PP_ASM_1); else PP_LOOP(PP_ASM_0);
}

enum { J_NONE = 0, J_GU = 3, J_PR = 5, J_CB = 8, J_DN = 14 };
struct BigJob { int type, panel, idx; };
struct BigPtr { const char *a, *b0, *b1; };
constexpr int G_LDA = XLD * 2, G_LDB = FF * 2;

__device__ __forceinline__ BigPtr big_ptrs(const Args& p, const BigJob& j) {
  BigPtr r;
  r.a = p.ws + WS_XN + (size_t)j.panel * 128 * G_LDA;
  r.b0 = (const char*)(p.wg + j.idx * 128); r.b1 = (const char*)(p.wu + j.idx * 128);
  return r;
}
__device__ __forceinline__ void big_publish(const Args& p, const BigJob& j) {
  int tid = threadIdx.x;
  asm volatile("" : "+v"(tid));
  if (j.type == J_GU && tid == 0)
    __hip_atomic_fetch_add((gu32*)(p.ws + WS_SY + SY_CH) + j.panel * 16, 1u, __ATOMIC_RELAXED, __HIP_MEMORY_SCOPE_AGENT);
}
typedef f32x4 Acc[2][2][4][2];
__device__ __forceinline__ void big_epilogue(const Args& p, const BigJob& j, Acc& acc, char* smem) {
  int tid = threadIdx.x;
  asm volatile("" : "+v"(tid));
  const int lane = tid & 63;
  const int wid = __builtin_amdgcn_readfirstlane(tid >> 6);
  const int wr = wid >> 2, wc = wid & 3;
  {
    const __amdgpu_buffer_rsrc_t ro = mkrsrc(p.ws + WS_HF, S_LEN * 16384);
#pragma unroll
    for (int i = 0; i < 8; ++i) {
      f32x4 hh[2];
#pragma unroll
      for (int n = 0; n < 2; ++n)
#pragma unroll
        for (int jj = 0; jj < 4; ++jj) {
          const float g = acc[i >> 2][0][i & 3][n][jj], u = acc[i >> 2][1][i & 3][n][jj];
          hh[n][jj] = __builtin_amdgcn_rcpf(1.0f + __builtin_amdgcn_exp2f(g * -1.4426950408889634f)) * u;
        }
      st_sc1(ro, h_off(p, j.panel + (i >> 2)) + (j.idx * 4 + wc) * 16384 + (wr * 4 + (i & 3)) * 1024 + lane * 16,
             ((u32x4){cvtpk(hh[0][0], hh[0][1]), cvtpk(hh[0][2], hh[0][3]), cvtpk(hh[1][0], hh[1][1]), cvtpk(hh[1][2], hh[1][3])}));
    }
  }
}
__device__ __forceinline__ void gemm_role(Args& p, int g, char* smem) {
  const __amdgpu_buffer_rsrc_t rsy = mkrsrc(p.ws + WS_SY, SY_TOTAL);
  const int x = g & 3, k = g >> 2;
  volatile unsigned* slot = reinterpret_cast<volatile unsigned*>(smem + 163824);
  int rnd = k < 24 ? 2 : -1;
  auto claim = [&]() {
    ++rnd;
    const int n = k < 24 ? 32 + k : rnd * 8 + k - 24, pn = (n * 37) >> 8;
    BigJob b;
    b.type = rnd < 4 ? J_GU : (rnd == 4 ? J_DN : (rnd == 5 && k >= 24 && k - 24 < 14 - NFUSE ? J_CB : J_NONE));
    b.panel = rnd < 4 ? 14 - 2 * pn : (rnd == 4 ? 4 * ((2 - (k >> 3)) & 3) + x : NFUSE + k - 24);
    b.idx = rnd < 4 ? x + 4 * (n - 7 * pn) : k & 7;
    return b;
  };
  if (k < 24) {
    if (k >= 16) job_proj<true>(p, g, 2, x * 8 + (k & 7), smem, rsy);
    else job_proj<false>(p, g, k >> 3, x * 8 + (k & 7), smem, rsy);
  } else {
    u32x2 tag;
#pragma unroll 1
    for (int u = 0; u < (k < 30 && LN_M ? 2 : 1); ++u) {
      const int rg = u ? 32 + (4 * (k - 24) + 1) * 4 + x : g - 96;
      tag = start_rows<0>(p, rg, 1, []() {});
      WAIT_V(0);
      LBAR();
      report_rows(p, rg, tag);
    }
#pragma unroll 1
    for (int i = 0; i < PF_N; ++i) {
      const int m = i % 3, o = ((i / 3) * 32 + g - 96) * 448 + (int)threadIdx.x;
      if (threadIdx.x < 448) touch_line((const char*)(m == 0 ? p.wg : (m == 1 ? p.wu : p.wd)) + (size_t)o * 128, smem + (threadIdx.x >> 6) * 256);
      WAIT_V(1);
    }
    WAIT_V(0);
    if (threadIdx.x < 64) {
      wait_rows(p.ws, tag);
      Boot bt;
      boot_ask(p.ws, bt);
      WAIT_V(0);
      *slot = boot_done(p.ws, bt, true, tag);
    }
    LBAR();
    p.s0 = *slot >> 31;
  }
  p.s0 = uni(p.s0);
  BigJob cur = claim();
  while (cur.type != J_NONE) {
    if (cur.type != J_GU) {
      if (cur.type == J_DN) job_down(p, cur.panel, cur.idx, smem, rsy);
      else job_comb(p, cur.panel, x, rsy);
      cur = claim();
      continue;
    }
    int tid = threadIdx.x;
    asm volatile("" : "+v"(tid));
    const int lane = tid & 63;
    const int wid = __builtin_amdgcn_readfirstlane(tid >> 6);
    const int wr = wid >> 2, wc = wid & 3;
    const unsigned ra = tid >> 3, ca = 16 * ((tid & 7) ^ ((tid >> 3) & 7)), rb = tid >> 4, cb = 16 * ((tid & 15) ^ swzB((tid >> 4) & 15));
    const unsigned lw = lds_off(smem) + wid * 1024;
#define B8_SA(b, h) (((b) * 2 + (h)) * 16384)
#define B8_SB(b, h) ((4 + (b) * 2 + (h)) * 16384)
#define B8_STA(b, h, P, kt) glds2<true>((P).a + (size_t)(h) * 128 * G_LDA + (kt) * 128, 64 * G_LDA, ra * G_LDA + ca, lw + B8_SA(b, h))
#define B8_STB(b, h, P, kt) glds2(((h) ? (P).b1 : (P).b0) + (size_t)(kt) * 64 * G_LDB, 32 * G_LDB, rb * G_LDB + cb, lw + B8_SB(b, h))
#define B8_LDA(b, h) do { _Pragma("unroll") for (int m = 0; m < 4; ++m) _Pragma("unroll") for (int kk = 0; kk < 2; ++kk) \
      At[m][kk] = ldsf(smem + B8_SA(b, h) + offA(wr * 64 + m * 16 + (lane & 15), kk * 4 + (lane >> 4))); } while (0)
#define B8_LDB(dst, b, h) do { _Pragma("unroll") for (int n = 0; n < 2; ++n) _Pragma("unroll") for (int kk = 0; kk < 2; ++kk) \
      dst[n][kk] = ld_bfrag(smem + B8_SB(b, h), kk, wc * 2 + n, lane); } while (0)
#define B8_MMA(ai, bj, Bt) do { _Pragma("unroll") for (int m = 0; m < 4; ++m) _Pragma("unroll") for (int n = 0; n < 2; ++n) \
      _Pragma("unroll") for (int kk = 0; kk < 2; ++kk) acc[ai][bj][m][n] = MFMA(Bt[n][kk], At[m][kk], acc[ai][bj][m][n]); } while (0)
#define WAIT_L(n) asm volatile("s_waitcnt lgkmcnt(%0)" :: "n"(n) : "memory")
#define BAR() __builtin_amdgcn_s_barrier()
#define BAR_1() do { SCHED(); asm volatile("s_setprio 1\n\ts_cmp_eq_u32 %0, 0\n\ts_cbranch_scc1 .Lb%=\n\ts_setprio 2\n\ts_barrier\n.Lb%=:" :: "s"(wr) : "memory", "scc"); SCHED(); } while (0)
#define BAR_0() do { SCHED(); asm volatile("s_setprio 0\n\ts_cmp_lg_u32 %0, 0\n\ts_cbranch_scc1 .Lb%=\n\ts_barrier\n.Lb%=:" :: "s"(wr) : "memory", "scc"); SCHED(); } while (0)
#define WAIT_RX(c) asm volatile("s_waitcnt vmcnt(16)\n\ts_cmp_lg_u32 %0, 0\n\ts_cbranch_scc1 .Lrx%=\n\ts_waitcnt vmcnt(8)\n.Lrx%=:" :: "s"(c) : "memory", "scc")
    Acc acc;
    bf16x8 At[4][2], B0[2][2], B1[2][2];
    BigPtr cp = big_ptrs(p, cur);
    BigJob pend; pend.type = J_NONE; pend.panel = 0; pend.idx = 0;
    LBAR();
    B8_STB(0, 0, cp, 0); B8_STB(0, 1, cp, 0); B8_STA(0, 0, cp, 0); B8_STA(0, 1, cp, 0);
    B8_STB(1, 0, cp, 1); B8_STA(1, 0, cp, 1); B8_STB(1, 1, cp, 1);
    WAIT_V(8); BAR();
    BigJob nx = pend;
    for (;;) {
      bool chain = false;
      BigPtr np = cp;
      constexpr int nt = DM / 64;
#pragma unroll
      for (int i = 0; i < 32; ++i) acc[i >> 4][(i >> 3) & 1][(i >> 1) & 3][i & 1] = (f32x4){0.f, 0.f, 0.f, 0.f};
#pragma unroll 1
      for (int t = 0; t < nt; t += 2) {
        const bool last = t == nt - 2;
        if (last) {
          nx = claim();
          chain = nx.type == J_GU;
          if (chain) np = big_ptrs(p, nx);
        }
        const BigPtr sp = np;
        const int t2 = last ? 0 : t + 2;
        if (t == 2) big_publish(p, pend);
        const int rx = __builtin_amdgcn_readfirstlane(t == 0 && pend.type != J_NONE);
        B8_LDB(B0, 0, 0); B8_LDB(B1, 0, 1); SCHED(); B8_LDA(0, 0); B8_STA(1, 1, cp, t + 1);
        WAIT_RX(rx);
        WAIT_L(0); BAR_1(); B8_MMA(0, 0, B0); B8_MMA(0, 1, B1); BAR_0();
        B8_LDA(0, 1); B8_STB(0, 0, sp, t2); B8_STB(0, 1, sp, t2); B8_STA(0, 0, sp, t2);
        WAIT_RX(rx);
        WAIT_L(0); BAR_1(); B8_MMA(1, 0, B0); B8_MMA(1, 1, B1); BAR_0();
        B8_LDB(B0, 1, 0); B8_LDB(B1, 1, 1); SCHED(); B8_LDA(1, 0); B8_STA(0, 1, sp, t2);
        WAIT_V(8); WAIT_L(0); BAR_1(); B8_MMA(0, 0, B0); B8_MMA(0, 1, B1); BAR_0();
        B8_LDA(1, 1); B8_STB(1, 0, sp, t2 + 1); B8_STB(1, 1, sp, t2 + 1); B8_STA(1, 0, sp, t2 + 1);
        WAIT_V(8); WAIT_L(0); BAR_1(); B8_MMA(1, 0, B0); B8_MMA(1, 1, B1); BAR_0();
      }
      big_epilogue(p, cur, acc, smem);
      if (!chain) break;
      pend = cur; cur = nx; cp = np;
    }
    WAIT_V(0);
    BAR();
    asm volatile(".Lcode_end:");
    big_publish(p, cur);
    cur = nx;
  }
}


__device__ __forceinline__ unsigned now_q() { return (unsigned)__builtin_amdgcn_s_memrealtime() * 4u; }
__device__ __forceinline__ void pace_until(unsigned t) {
#pragma unroll 1
  for (int i = 0; i < 256; ++i) {
    const int d = (int)(t - now_q());
    if (d <= 0) break;
    if (d > 32) __builtin_amdgcn_s_sleep(1);
  }
}
#define CLB_E "s76", "s77", "s78", "s79", "s80", "s81", "s82", "s83", "s84", "s85", "s86", "s87", "s88", "s89"
#define EDGE_ASM \
  "s_memtime s[76:77]\n\t" \
  "s_memrealtime s[78:79]\n\t" \
  "s_nop 3\n\t" \
  "s_memrealtime s[80:81]\n\t" \
  "s_nop 3\n\t" \
  "s_memrealtime s[82:83]\n\t" \
  "s_nop 3\n\t" \
  "s_memrealtime s[84:85]\n\t" \
  "s_nop 3\n\t" \
  "s_memrealtime s[86:87]\n\t" \
  "s_nop 3\n\t" \
  "s_memrealtime s[88:89]\n\t" \
  "s_mov_b32 s93, 0\n\t" \
  "s_waitcnt lgkmcnt(0)\n\t" \
  "s_cmp_eq_u32 s80, s78\n\t" \
  "s_addc_u32 s93, s93, 0\n\t" \
  "s_cmp_eq_u32 s82, s78\n\t" \
  "s_addc_u32 s93, s93, 0\n\t" \
  "s_cmp_eq_u32 s84, s78\n\t" \
  "s_addc_u32 s93, s93, 0\n\t" \
  "s_cmp_eq_u32 s86, s78\n\t" \
  "s_addc_u32 s93, s93, 0\n\t" \
  "s_cmp_eq_u32 s88, s78\n\t" \
  "s_addc_u32 s93, s93, 0\n\t" \
  "s_mul_i32 s93, s93, 21\n\t" \
  "s_add_u32 s76, s76, 15\n\t" \
  "s_add_u32 %[kc], s76, s93\n\t" \
  "s_lshl2_add_u32 %[kq], s78, 16\n\t"
__device__ __forceinline__ unsigned time_of(unsigned cyc) {
  unsigned c, q;
  asm volatile(EDGE_ASM : [kc] "=&s"(c), [kq] "=&s"(q) :: CLB_E, "s93", "scc");
  return q - (((c - cyc) * 11900u) >> 16);
}
#define DS_RED 20
#define DS_MEM 16
#define LEAD_DN 2
#define LEAD_UP 6
__device__ __forceinline__ u32x2 ld2_sc1(__amdgpu_buffer_rsrc_t rs, int off, int soff = 0) {
  return __builtin_amdgcn_raw_buffer_load_b64(rs, off, soff, 16);
}
__device__ __forceinline__ void st2_sc1(__amdgpu_buffer_rsrc_t rs, int off, u32x2 v) {
  __builtin_amdgcn_raw_buffer_store_b64(v, rs, off, 0, 16);
}
__device__ __forceinline__ int row_max(int v) {
  v = max(v, __builtin_amdgcn_update_dpp(0, v, 0x128, 0xf, 0xf, false));
  v = max(v, __builtin_amdgcn_update_dpp(0, v, 0x124, 0xf, 0xf, false));
  v = max(v, __builtin_amdgcn_update_dpp(0, v, 0x122, 0xf, 0xf, false));
  v = max(v, __builtin_amdgcn_update_dpp(0, v, 0x121, 0xf, 0xf, false));
  return v;
}
__device__ __forceinline__ int wave_max(int v) {
  v = row_max(v);
  return max(max(__builtin_amdgcn_readlane(v, 0), __builtin_amdgcn_readlane(v, 16)),
             max(__builtin_amdgcn_readlane(v, 32), __builtin_amdgcn_readlane(v, 48)));
}
__device__ __forceinline__ int track(int est, int x, int up, int dn, int out) {
  const int d = x - est;
  return est + (d > out ? 4 : max(min(d, up), -dn));
}
#define WSL 12
#define PLAN_SLEEP 2
#define REF_W 3
#define REF_G 60
#define RL0 315
__device__ __forceinline__ void watch(__amdgpu_buffer_rsrc_t rsy, int s, int lane, unsigned t0, const unsigned* tgt_, gu32* abortw, int hs) {
  const int rl = (int)__builtin_amdgcn_raw_buffer_load_b32(rsy, lane * 128, SY_BR + hs, 16);
  u32x2 ra;
  for (unsigned spins = 0;;) {
    asm volatile("" ::: "memory");
    ra = ld2_sc1(rsy, (lane & 31) * 128, SY_BA + hs);
    const bool all = __all((int)ra[0] >= s);
    if (all) break;
    if (spins < 3) __builtin_amdgcn_s_sleep(PLAN_SLEEP); else __builtin_amdgcn_s_sleep(WSL);
    if (spin_fail(spins, abortw)) break;
  }
  int d = ra[0] == (unsigned)s ? (int)(ra[1] - t0) : 0;
  d = max(d, __builtin_amdgcn_update_dpp(0, d, 0xB1, 0xf, 0xf, false));
  d = max(d, __builtin_amdgcn_update_dpp(0, d, 0x4E, 0xf, 0xf, false));
  int sum = d + __builtin_amdgcn_update_dpp(0, d, 0x128, 0xf, 0xf, false);
  sum += __builtin_amdgcn_update_dpp(0, sum, 0x124, 0xf, 0xf, false);
  const int mx = wave_max(d), mean = (__builtin_amdgcn_readlane(sum, 0) + __builtin_amdgcn_readlane(sum, 16)) >> 3;
  const unsigned t = t0 + mx - min(((mx - mean) * REF_W) >> 2, REF_G) + wave_max(rl < 0 ? RL0 : rl) + (DS_RED + DS_MEM);
  lds_vu_ptr tgt = lds_at(tgt_);
  if (lane < 2) tgt[lane] = lane ? t : s + 1;
}
__device__ __forceinline__ unsigned ld_flag(__amdgpu_buffer_rsrc_t rs, int off) {
  return __builtin_amdgcn_raw_buffer_load_b32(rs, off, 0, 16);
}

constexpr unsigned long long QK_MAP[8] = {0xbaf9876d543210ecull, 0xa98f76d543210bceull, 0x98f76d543210ecbaull, 0x876fd543210bcea9ull, 0x76fd543210ecba98ull, 0x65df43210bcea987ull, 0x5df43210ecba9876ull, 0xd43f210bcea98765ull};
#define QK_ASM_0 \
  "s_waitcnt lgkmcnt(0)\n\t" \
  "v_subrev_u32 v244, 0x8000, v250\n\t" \
  "ds_read_b128 v[216:219], v248 offset:32768\n\t" \
  "ds_read_b128 v[220:223], v248 offset:33792\n\t" \
  "ds_read_b128 v[224:227], v248 offset:34816\n\t" \
  "ds_read_b128 v[228:231], v248 offset:35840\n\t" \
  "ds_read_b128 v[208:211], v244 offset:0\n\t" \
  "ds_read_b128 v[212:215], v244 offset:1024\n\t" \
  "ds_read_b128 v[236:239], v244 offset:2048\n\t" \
  "ds_read_b128 v[240:243], v244 offset:3072\n\t" \
  "v_mov_b64 v[192:193], 0\n\t" \
  "v_mov_b64 v[194:195], 0\n\t" \
  "v_mov_b64 v[196:197], 0\n\t" \
  "v_mov_b64 v[198:199], 0\n\t" \
  "s_movk_i32 s92, 0x2000\n\t" \
  ".Lwt%=:\n\t" \
  "ds_read_b64 v[200:201], v248 offset:30720\n\t" \
  "s_memrealtime s[90:91]\n\t" \
  "s_sub_u32 s92, s92, 1\n\t" \
  "s_waitcnt lgkmcnt(0)\n\t" \
  "v_readfirstlane_b32 s93, v200\n\t" \
  "v_readfirstlane_b32 %[tgt], v201\n\t" \
  "s_cbranch_scc1 .Lgo%=\n\t" \
  "s_cmp_eq_u32 s93, %[step]\n\t" \
  "s_cbranch_scc1 .Lwx%=\n\t" \
  "s_sleep 1\n\t" \
  "s_branch .Lwt%=\n\t" \
  ".Lwx%=:\n\t" \
  "s_lshl_b32 s93, s90, 2\n\t" \
  "s_sub_i32 s93, %[tgt], s93\n\t" \
  "s_cmp_le_i32 s93, 0\n\t" \
  "s_cbranch_scc1 .Lgo%=\n\t" \
  "s_min_i32 s93, s93, 8000\n\t" \
  "s_mov_b32 s94, s93\n\t" \
  ".Lsl%=:\n\t" \
  "s_sub_i32 s94, s94, 18\n\t" \
  "s_cmp_lt_i32 s94, 30\n\t" \
  "s_cbranch_scc1 .Lsx%=\n\t" \
  "s_sleep 1\n\t" \
  "s_branch .Lsl%=\n\t" \
  ".Lsx%=:\n\t" \
  "s_sub_i32 s93, s93, 12\n\t" \
  "s_max_i32 s93, s93, 4\n\t" \
  "s_lshr_b32 s93, s93, 2\n\t" \
  "s_add_u32 s93, s93, s90\n\t" \
  "s_memrealtime s[90:91]\n\t" \
  ".Lpace%=:\n\t" \
  "s_memrealtime s[94:95]\n\t" \
  "s_waitcnt lgkmcnt(1)\n\t" \
  "s_sub_i32 s92, s90, s93\n\t" \
  "s_cmp_ge_i32 s92, 0\n\t" \
  "s_cbranch_scc1 .Le%=\n\t" \
  "s_memrealtime s[90:91]\n\t" \
  "s_waitcnt lgkmcnt(1)\n\t" \
  "s_sub_i32 s92, s94, s93\n\t" \
  "s_cmp_lt_i32 s92, 0\n\t" \
  "s_cbranch_scc1 .Lpace%=\n\t" \
  "s_nop 4\n\t" \
  ".Le%=:\n\t" \
  "s_add_u32 s92, s92, s93\n\t" \
  "s_lshl_b32 s92, s92, 2\n\t" \
  "s_sub_i32 s92, %[tgt], s92\n\t" \
  "s_max_i32 s92, s92, 0\n\t" \
  "s_bitcmp0_b32 s92, 3\n\t" \
  "s_cbranch_scc1 .Lf3%=\n\t" \
  "s_nop 15\n\t" \
  ".Lf3%=:\n\t" \
  "s_bitcmp0_b32 s92, 2\n\t" \
  "s_cbranch_scc1 .Lf2%=\n\t" \
  "s_nop 10\n\t" \
  ".Lf2%=:\n\t" \
  ".Lgo%=:\n\t" \
  "s_mov_b32 s92, 0\n\t" \
  ".Lpoll%=:\n\t" \
  "buffer_load_dwordx4 v[200:203], v248, %[rs], %[som] offen sc1\n\t" \
  "buffer_load_dwordx4 v[204:207], v248, %[rs], %[som2] offen sc1\n\t" \
  "s_waitcnt vmcnt(1) lgkmcnt(0)\n\t" \
  "ds_write_b128 v249, v[200:203]\n\t" \
  "v_smfmac_f32_16x16x64_bf16 v[192:195], v[200:203], v[208:215], v251\n\t" \
  "v_max3_u32 v244, v200, v201, v202\n\t" \
  "v_smfmac_f32_16x16x64_bf16 v[196:199], v[200:203], v[236:243], v251\n\t" \
  "s_waitcnt vmcnt(0)\n\t" \
  "ds_write_b128 v249, v[204:207] offset:8192\n\t" \
  "v_smfmac_f32_16x16x64_bf16 v[192:195], v[204:207], v[216:223], v251\n\t" \
  "v_max3_u32 v244, v244, v203, v204\n\t" \
  "v_max3_u32 v244, v244, v205, v206\n\t" \
  "v_smfmac_f32_16x16x64_bf16 v[196:199], v[204:207], v[224:231], v251\n\t" \
  "v_max_u32 v244, v244, v207\n\t" \
  "v_cmp_le_u32 vcc, %[sent], v244\n\t" \
  "s_cbranch_vccnz .Lslow%=\n\t" \
  ".Lok%=:\n\t" \
  "ds_read_b128 v[216:219], v250 offset:32768\n\t" \
  "ds_read_b128 v[220:223], v250 offset:33792\n\t" \
  "ds_read_b128 v[224:227], v250 offset:34816\n\t" \
  "ds_read_b128 v[228:231], v250 offset:35840\n\t" \
  "s_waitcnt lgkmcnt(4)\n\t" \
  "s_barrier\n\t" \
  "ds_read_b128 v[208:211], v248 offset:1024\n\t" \
  "ds_read_b128 v[212:215], v248 offset:2048\n\t" \
  "ds_read_b128 v[236:239], v248 offset:3072\n\t" \
  "ds_read_b128 v[240:243], v248 offset:4096\n\t" \
  "s_cmp_eq_u32 %[step], 63\n\t" \
  "s_cbranch_scc1 .Lls%=\n\t" \
  "s_waitcnt lgkmcnt(3)\n\t" \
  "v_smfmac_f32_16x16x64_bf16 v[192:195], v[208:211], v[216:223], v251\n\t" \
  "ds_read_b128 v[244:247], v248 offset:5120\n\t" \
  "v_smfmac_f32_16x16x64_bf16 v[196:199], v[208:211], v[224:231], v251\n\t" \
  "s_waitcnt lgkmcnt(3)\n\t" \
  "ds_read_b128 v[200:203], v248 offset:13312\n\t" \
  "v_smfmac_f32_16x16x64_bf16 v[192:195], v[212:215], v[0:7], v251\n\t" \
  "ds_read_b128 v[216:219], v250 offset:0\n\t" \
  "v_smfmac_f32_16x16x64_bf16 v[196:199], v[212:215], v[8:15], v251\n\t" \
  "s_waitcnt lgkmcnt(4)\n\t" \
  "ds_read_b128 v[220:223], v250 offset:1024\n\t" \
  "v_smfmac_f32_16x16x64_bf16 v[192:195], v[236:239], v[16:23], v251\n\t" \
  "ds_read_b128 v[224:227], v250 offset:2048\n\t" \
  "v_smfmac_f32_16x16x64_bf16 v[196:199], v[236:239], v[24:31], v251\n\t" \
  "s_waitcnt lgkmcnt(4)\n\t" \
  "ds_read_b128 v[228:231], v250 offset:3072\n\t" \
  "v_smfmac_f32_16x16x64_bf16 v[192:195], v[240:243], v[32:39], v251\n\t" \
  "ds_read_b128 v[204:207], v248 offset:6144\n\t" \
  "v_smfmac_f32_16x16x64_bf16 v[196:199], v[240:243], v[40:47], v251\n\t" \
  "ds_read_b128 v[208:211], v248 offset:7168\n\t" \
  "v_smfmac_f32_16x16x64_bf16 v[192:195], v[244:247], v[48:55], v251\n\t" \
  "ds_read_b128 v[212:215], v248 offset:9216\n\t" \
  "v_smfmac_f32_16x16x64_bf16 v[196:199], v[244:247], v[56:63], v251\n\t" \
  "s_waitcnt lgkmcnt(5)\n\t" \
  "v_smfmac_f32_16x16x64_bf16 v[192:195], v[200:203], v[216:223], v251\n\t" \
  "s_waitcnt lgkmcnt(3)\n\t" \
  "ds_read_b128 v[236:239], v248 offset:10240\n\t" \
  "v_smfmac_f32_16x16x64_bf16 v[196:199], v[200:203], v[224:231], v251\n\t" \
  "s_waitcnt lgkmcnt(3)\n\t" \
  "v_smfmac_f32_16x16x64_bf16 v[192:195], v[204:207], v[64:71], v251\n\t" \
  "ds_read_b128 v[240:243], v248 offset:11264\n\t" \
  "v_smfmac_f32_16x16x64_bf16 v[196:199], v[204:207], v[72:79], v251\n\t" \
  "s_waitcnt lgkmcnt(2)\n\t" \
  "v_smfmac_f32_16x16x64_bf16 v[192:195], v[208:211], v[80:87], v251\n\t" \
  "ds_read_b128 v[244:247], v248 offset:12288\n\t" \
  "v_smfmac_f32_16x16x64_bf16 v[196:199], v[208:211], v[88:95], v251\n\t" \
  "ds_read_b128 v[200:203], v248 offset:14336\n\t" \
  "v_smfmac_f32_16x16x64_bf16 v[192:195], v[212:215], v[96:103], v251\n\t" \
  "ds_read_b128 v[204:207], v248 offset:15360\n\t" \
  "v_smfmac_f32_16x16x64_bf16 v[196:199], v[212:215], v[104:111], v251\n\t" \
  "s_waitcnt lgkmcnt(3)\n\t" \
  "v_smfmac_f32_16x16x64_bf16 v[192:195], v[236:239], v[112:119], v251\n\t" \
  "v_smfmac_f32_16x16x64_bf16 v[196:199], v[236:239], v[120:127], v251\n\t" \
  "v_smfmac_f32_16x16x64_bf16 v[192:195], v[240:243], v[128:135], v251\n\t" \
  "v_smfmac_f32_16x16x64_bf16 v[196:199], v[240:243], v[136:143], v251\n\t" \
  "s_waitcnt lgkmcnt(0)\n\t" \
  "v_smfmac_f32_16x16x64_bf16 v[192:195], v[244:247], v[144:151], v251\n\t" \
  "v_smfmac_f32_16x16x64_bf16 v[196:199], v[244:247], v[152:159], v251\n\t" \
  "v_smfmac_f32_16x16x64_bf16 v[192:195], v[200:203], v[160:167], v251\n\t" \
  "v_smfmac_f32_16x16x64_bf16 v[196:199], v[200:203], v[168:175], v251\n\t" \
  "v_smfmac_f32_16x16x64_bf16 v[192:195], v[204:207], v[176:183], v251\n\t" \
  "v_smfmac_f32_16x16x64_bf16 v[196:199], v[204:207], v[184:191], v251\n\t" \
  "ds_read_b128 v[200:203], v248 offset:16384\n\t" \
  "ds_read_b128 v[204:207], v248 offset:20480\n\t" \
  "v_mov_b32 v250, v232\n\t" \
  "v_mov_b32 v249, v233\n\t" \
  "s_nop 2\n\t" \
  "v_add_f32 v192, v192, v194\n\t" \
  "v_add_f32 v193, v193, v195\n\t" \
  "v_add_f32 v196, v196, v198\n\t" \
  "v_add_f32 v197, v197, v199\n\t" \
  "v_cvt_pk_bf16_f32 v192, v192, v193\n\t" \
  "v_cvt_pk_bf16_f32 v196, v196, v197\n\t" \
  "ds_write2_b32 v252, v192, v196 offset1:64\n\t" \
  "v_mov_b32 v251, v234\n\t" \
  "ds_read_b64_tr_b16 v[232:233], v250 offset:0\n\t" \
  "ds_read_b64_tr_b16 v[234:235], v250 offset:64\n\t" \
  "v_mov_b32 v212, %[step]\n\t" \
  "s_mov_b64 exec, 1\n\t" \
  "ds_write_b32 v248, v212 offset:28672\n\t" \
  "s_mov_b64 exec, -1\n\t" \
  "buffer_load_dwordx4 v[192:195], v248, %[rt], %[svt] offen offset:0 sc1\n\t" \
  "buffer_load_dwordx4 v[196:199], v248, %[rt], %[svt] offen offset:1024 sc1\n\t" \
  "s_waitcnt lgkmcnt(1)\n\t" \
  "v_mfma_f32_16x16x32_bf16 v[200:203], v[200:203], v[232:235], 0\n\t" \
  "v_mfma_f32_16x16x32_bf16 v[204:207], v[204:207], v[232:235], 0\n\t" \
  "s_nop 7\n\t" \
  "v_cndmask_b32 v216, v200, v204, %[nh]\n\t" \
  "v_cndmask_b32 v217, v201, v205, %[nh]\n\t" \
  "v_cndmask_b32 v218, v202, v206, %[nh]\n\t" \
  "v_cndmask_b32 v219, v203, v207, %[nh]\n\t" \
  "ds_write_b128 v248, v[216:219] offset:16384\n\t" \
  "buffer_load_dword v252, v249, %[rs], %[sv] offen offset:0 sc1\n\t" \
  "buffer_load_dword v253, v249, %[rs], %[sv] offen offset:256 sc1\n\t" \
  "ds_read_b32 v212, v248 offset:28672\n\t" \
  "ds_read_b64_tr_b16 v[236:237], v250 offset:512\n\t" \
  "ds_read_b64_tr_b16 v[238:239], v250 offset:576\n\t" \
  "ds_read_b64_tr_b16 v[240:241], v250 offset:1024\n\t" \
  "ds_read_b64_tr_b16 v[242:243], v250 offset:1088\n\t" \
  "ds_read_b64_tr_b16 v[244:245], v250 offset:1536\n\t" \
  "ds_read_b64_tr_b16 v[246:247], v250 offset:1600\n\t" \
  "s_setprio 3\n\t" \
  "s_waitcnt lgkmcnt(0)\n\t" \
  "v_cmp_ne_u32 vcc, %[step], v212\n\t" \
  "s_and_b32 s93, vcc_lo, 0xf\n\t" \
  "s_barrier\n\t" \
  "s_cbranch_scc1 .Lre%=\n\t" \
  ".Lrb%=:\n\t" \
  "ds_read_b64_tr_b16 v[200:201], v250 offset:2048\n\t" \
  "ds_read_b64_tr_b16 v[202:203], v250 offset:2112\n\t" \
  "ds_read_b64_tr_b16 v[204:205], v250 offset:2560\n\t" \
  "ds_read_b64_tr_b16 v[206:207], v250 offset:2624\n\t" \
  "ds_read_b64_tr_b16 v[208:209], v250 offset:3072\n\t" \
  "ds_read_b64_tr_b16 v[210:211], v250 offset:3136\n\t" \
  "ds_read_b64_tr_b16 v[212:213], v250 offset:3584\n\t" \
  "ds_read_b64_tr_b16 v[214:215], v250 offset:3648\n\t" \
  "ds_read_b128 v[220:223], v248 offset:17408\n\t" \
  "ds_read_b128 v[224:227], v248 offset:18432\n\t" \
  "ds_read_b128 v[228:231], v248 offset:19456\n\t" \
  "s_waitcnt lgkmcnt(9)\n\t" \
  "v_mfma_f32_16x16x32_bf16 v[200:203], v[200:203], v[232:235], 0\n\t" \
  "s_waitcnt lgkmcnt(7)\n\t" \
  "v_mfma_f32_16x16x32_bf16 v[200:203], v[204:207], v[236:239], v[200:203]\n\t" \
  "s_waitcnt lgkmcnt(5)\n\t" \
  "v_mfma_f32_16x16x32_bf16 v[200:203], v[208:211], v[240:243], v[200:203]\n\t" \
  "s_waitcnt lgkmcnt(3)\n\t" \
  "v_mfma_f32_16x16x32_bf16 v[200:203], v[212:215], v[244:247], v[200:203]\n\t" \
  "s_waitcnt lgkmcnt(2)\n\t" \
  "v_pk_add_f32 v[216:217], v[216:217], v[220:221]\n\t" \
  "v_pk_add_f32 v[218:219], v[218:219], v[222:223]\n\t" \
  "s_waitcnt lgkmcnt(0)\n\t" \
  "v_pk_add_f32 v[224:225], v[224:225], v[228:229]\n\t" \
  "v_pk_add_f32 v[226:227], v[226:227], v[230:231]\n\t" \
  "v_pk_add_f32 v[216:217], v[216:217], v[224:225]\n\t" \
  "v_pk_add_f32 v[218:219], v[218:219], v[226:227]\n\t" \
  "v_max3_f32 v224, v216, v217, v218\n\t" \
  "v_max_f32 v224, v224, v219\n\t" \
  "s_nop 1\n\t" \
  "v_max_f32_dpp v225, v224, v224 row_ror:8 row_mask:0xf bank_mask:0xf\n\t" \
  "v_max_f32_dpp v226, v224, v224 row_ror:8 row_mask:0xf bank_mask:0xf\n\t" \
  "s_nop 1\n\t" \
  "v_permlane32_swap_b32 v225, v226\n\t" \
  "v_max_f32 v231, v225, v226\n\t" \
  "v_mov_b32 v220, 0x3f803f80\n\t" \
  "v_mov_b32 v221, 0x3f803f80\n\t" \
  "v_mov_b64 v[222:223], v[220:221]\n\t" \
  "v_mov_b32 v233, 0\n\t" \
  "v_mov_b32 v234, 0\n\t" \
  "v_max3_f32 v204, v200, v201, v231\n\t" \
  "v_max3_f32 v205, v204, v202, v203\n\t" \
  "v_max3_f32 v206, v204, v202, v203\n\t" \
  "v_cndmask_b32 v200, v200, v202, %[up]\n\t" \
  "v_cndmask_b32 v201, v201, v203, %[up]\n\t" \
  "v_permlane16_swap_b32 v205, v206\n\t" \
  "v_max_f32 v204, v205, v206\n\t" \
  "v_mul_f32 v204, %[negc], v204\n\t" \
  "v_fma_f32 v200, v200, %[c], v204\n\t" \
  "v_fma_f32 v201, v201, %[c], v204\n\t" \
  "v_exp_f32 v200, v200\n\t" \
  "v_fma_f32 v216, v216, %[c], v204\n\t" \
  "v_exp_f32 v201, v201\n\t" \
  "v_fma_f32 v217, v217, %[c], v204\n\t" \
  "v_exp_f32 v216, v216\n\t" \
  "v_fma_f32 v218, v218, %[c], v204\n\t" \
  "v_cvt_pk_bf16_f32 v232, v200, v201\n\t" \
  "v_cvt_pk_bf16_f32 v235, v200, v201\n\t" \
  "v_exp_f32 v217, v217\n\t" \
  "v_fma_f32 v219, v219, %[c], v204\n\t" \
  "v_mfma_f32_16x16x16_bf16 v[212:215], v[220:221], v[232:233], 0\n\t" \
  "v_exp_f32 v218, v218\n\t" \
  "s_waitcnt vmcnt(0)\n\t" \
  "v_mfma_f32_16x16x16_bf16 v[224:227], v[252:253], v[232:233], 0\n\t" \
  "v_cvt_pk_bf16_f32 v210, v216, v217\n\t" \
  "v_cvt_pk_bf16_f32 v208, v216, v217\n\t" \
  "v_exp_f32 v219, v219\n\t" \
  "v_mfma_f32_16x16x16_bf16 v[228:231], v[252:253], v[234:235], 0\n\t" \
  "v_cvt_pk_bf16_f32 v211, v218, v219\n\t" \
  "v_cvt_pk_bf16_f32 v209, v218, v219\n\t" \
  "v_mov_b32_dpp v208, v210 row_ror:8 row_mask:0xf bank_mask:0xc\n\t" \
  "v_mov_b32_dpp v210, v210 row_ror:8 row_mask:0xf bank_mask:0x3\n\t" \
  "v_mov_b32_dpp v209, v211 row_ror:8 row_mask:0xf bank_mask:0xc\n\t" \
  "v_mov_b32_dpp v211, v211 row_ror:8 row_mask:0xf bank_mask:0x3\n\t" \
  "v_max_u32 v204, v252, v253\n\t" \
  "v_cmp_le_u32 vcc, %[sent], v204\n\t" \
  "v_mfma_f32_16x16x32_bf16 v[212:215], v[220:223], v[208:211], v[212:215]\n\t" \
  ".Lpv%=:\n\t" \
  "v_mfma_f32_16x16x32_bf16 v[224:227], v[192:195], v[208:211], v[224:227]\n\t" \
  "v_mfma_f32_16x16x32_bf16 v[228:231], v[196:199], v[208:211], v[228:231]\n\t" \
  "s_cbranch_vccnz .Lvs%=\n\t" \
  ".Lvok%=:\n\t" \
  "s_nop 4\n\t" \
  "v_rcp_f32 v204, v212\n\t" \
  "v_cndmask_b32 v224, v224, v226, %[nh]\n\t" \
  "v_cndmask_b32 v225, v225, v227, %[nh]\n\t" \
  "v_cndmask_b32 v228, v228, v230, %[nh]\n\t" \
  "v_cndmask_b32 v229, v229, v231, %[nh]\n\t" \
  "v_pk_mul_f32 v[224:225], v[224:225], v[204:205] op_sel_hi:[1,0]\n\t" \
  "v_pk_mul_f32 v[228:229], v[228:229], v[204:205] op_sel_hi:[1,0]\n\t" \
  "v_cvt_pk_bf16_f32 v224, v224, v225\n\t" \
  "v_cvt_pk_bf16_f32 v225, v228, v229\n\t" \
  "buffer_store_dwordx2 v[224:225], v251, %[rs], %[so] offen sc1\n\t" \
  "s_memtime s[90:91]\n\t" \
  "s_setprio 0\n\t" \
  "s_waitcnt lgkmcnt(0)\n\t" \
  "s_mov_b32 %[tout], s90\n\t" \
  "s_mov_b32 %[spins], s92\n\t" \
  "s_branch .Lend%=\n\t" \
  ".Lls%=:\n\t" \
  "s_waitcnt lgkmcnt(0)\n\t" \
  "s_barrier\n\t" \
  "s_mov_b32 %[spins], 0\n\t" \
  "s_branch .Lend%=\n\t" \
  ".Lre%=:\n\t" \
  "ds_read_b64_tr_b16 v[236:237], v250 offset:512\n\t" \
  "ds_read_b64_tr_b16 v[238:239], v250 offset:576\n\t" \
  "ds_read_b64_tr_b16 v[240:241], v250 offset:1024\n\t" \
  "ds_read_b64_tr_b16 v[242:243], v250 offset:1088\n\t" \
  "ds_read_b64_tr_b16 v[244:245], v250 offset:1536\n\t" \
  "ds_read_b64_tr_b16 v[246:247], v250 offset:1600\n\t" \
  "s_waitcnt lgkmcnt(0)\n\t" \
  "s_branch .Lrb%=\n\t" \
  ".Lvs%=:\n\t" \
  "v_cmp_eq_u32 vcc, %[sent], v252\n\t" \
  "v_cmp_eq_u32 s[94:95], %[sent], v253\n\t" \
  "s_or_b64 s[94:95], s[94:95], vcc\n\t" \
  "s_cbranch_scc0 .Lvok%=\n\t" \
  "s_add_u32 s92, s92, 1\n\t" \
  "s_sleep 2\n\t" \
  "s_and_b32 s93, s92, 63\n\t" \
  "s_cbranch_scc1 .Lvr%=\n\t" \
  "buffer_load_dword v204, off, %[rs], %[sab] sc1\n\t" \
  "s_waitcnt vmcnt(0)\n\t" \
  "v_readfirstlane_b32 s93, v204\n\t" \
  "s_nop 3\n\t" \
  "s_cmp_eq_u32 s93, 1\n\t" \
  "s_cbranch_scc1 .Lvok%=\n\t" \
  "s_cmp_lt_u32 s92, 0x80000\n\t" \
  "s_cbranch_scc1 .Lvr%=\n\t" \
  "v_mov_b32 v204, 1\n\t" \
  "buffer_store_dword v204, off, %[rs], %[sab] sc1\n\t" \
  "s_branch .Lvok%=\n\t" \
  ".Lvr%=:\n\t" \
  "buffer_load_dword v252, v249, %[rs], %[sv] offen offset:0 sc1\n\t" \
  "buffer_load_dword v253, v249, %[rs], %[sv] offen offset:256 sc1\n\t" \
  "s_waitcnt vmcnt(0)\n\t" \
  "v_mfma_f32_16x16x16_bf16 v[224:227], v[252:253], v[232:233], 0\n\t" \
  "v_mfma_f32_16x16x16_bf16 v[228:231], v[252:253], v[234:235], 0\n\t" \
  "v_max_u32 v204, v252, v253\n\t" \
  "v_cmp_le_u32 vcc, %[sent], v204\n\t" \
  "s_nop 4\n\t" \
  "s_branch .Lpv%=\n\t" \
  "s_branch .Lend%=\n\t" \
  ".Lslow%=:\n\t" \
  "s_mov_b64 s[94:95], 0\n\t" \
  "v_cmp_eq_u32 vcc, %[sent], v200\n\t" \
  "s_or_b64 s[94:95], s[94:95], vcc\n\t" \
  "v_cmp_eq_u32 vcc, %[sent], v201\n\t" \
  "s_or_b64 s[94:95], s[94:95], vcc\n\t" \
  "v_cmp_eq_u32 vcc, %[sent], v202\n\t" \
  "s_or_b64 s[94:95], s[94:95], vcc\n\t" \
  "v_cmp_eq_u32 vcc, %[sent], v203\n\t" \
  "s_or_b64 s[94:95], s[94:95], vcc\n\t" \
  "v_cmp_eq_u32 vcc, %[sent], v204\n\t" \
  "s_or_b64 s[94:95], s[94:95], vcc\n\t" \
  "v_cmp_eq_u32 vcc, %[sent], v205\n\t" \
  "s_or_b64 s[94:95], s[94:95], vcc\n\t" \
  "v_cmp_eq_u32 vcc, %[sent], v206\n\t" \
  "s_or_b64 s[94:95], s[94:95], vcc\n\t" \
  "v_cmp_eq_u32 vcc, %[sent], v207\n\t" \
  "s_or_b64 s[94:95], s[94:95], vcc\n\t" \
  "s_cmp_eq_u64 s[94:95], 0\n\t" \
  "s_cbranch_scc1 .Lok%=\n\t" \
  "v_mov_b64 v[192:193], 0\n\t" \
  "v_mov_b64 v[194:195], 0\n\t" \
  "v_mov_b64 v[196:197], 0\n\t" \
  "v_mov_b64 v[198:199], 0\n\t" \
  "s_add_u32 s92, s92, 1\n\t" \
  "s_cmp_eq_u32 s92, 1\n\t" \
  "s_cbranch_scc1 .Lpoll%=\n\t" \
  "s_sleep 2\n\t" \
  "s_and_b32 s93, s92, 63\n\t" \
  "s_cmp_lg_u32 s93, 0\n\t" \
  "s_cbranch_scc1 .Lpoll%=\n\t" \
  "v_mov_b32 v245, 0\n\t" \
  "buffer_load_dword v244, v245, %[rs], %[sab] offen sc1\n\t" \
  "s_waitcnt vmcnt(0)\n\t" \
  "v_readfirstlane_b32 s93, v244\n\t" \
  "s_nop 3\n\t" \
  "s_cmp_eq_u32 s93, 1\n\t" \
  "s_cbranch_scc1 .Lok%=\n\t" \
  "s_cmp_lt_u32 s92, 0x80000\n\t" \
  "s_cbranch_scc1 .Lpoll%=\n\t" \
  "v_mov_b32 v244, 1\n\t" \
  "buffer_store_dword v244, v245, %[rs], %[sab] offen sc1\n\t" \
  "s_branch .Lok%=\n\t" \
  ".Lend%=:\n\t"
#define QK_ASM_1 \
  "s_waitcnt lgkmcnt(0)\n\t" \
  "v_subrev_u32 v244, 0x8000, v250\n\t" \
  "ds_read_b128 v[216:219], v248 offset:36864\n\t" \
  "ds_read_b128 v[220:223], v248 offset:37888\n\t" \
  "ds_read_b128 v[224:227], v248 offset:38912\n\t" \
  "ds_read_b128 v[228:231], v248 offset:39936\n\t" \
  "ds_read_b128 v[208:211], v244 offset:0\n\t" \
  "ds_read_b128 v[212:215], v244 offset:1024\n\t" \
  "ds_read_b128 v[236:239], v244 offset:2048\n\t" \
  "ds_read_b128 v[240:243], v244 offset:3072\n\t" \
  "v_mov_b64 v[192:193], 0\n\t" \
  "v_mov_b64 v[194:195], 0\n\t" \
  "v_mov_b64 v[196:197], 0\n\t" \
  "v_mov_b64 v[198:199], 0\n\t" \
  "s_movk_i32 s92, 0x2000\n\t" \
  ".Lwt%=:\n\t" \
  "ds_read_b64 v[200:201], v248 offset:30720\n\t" \
  "s_memrealtime s[90:91]\n\t" \
  "s_sub_u32 s92, s92, 1\n\t" \
  "s_waitcnt lgkmcnt(0)\n\t" \
  "v_readfirstlane_b32 s93, v200\n\t" \
  "v_readfirstlane_b32 %[tgt], v201\n\t" \
  "s_cbranch_scc1 .Lgo%=\n\t" \
  "s_cmp_eq_u32 s93, %[step]\n\t" \
  "s_cbranch_scc1 .Lwx%=\n\t" \
  "s_sleep 1\n\t" \
  "s_branch .Lwt%=\n\t" \
  ".Lwx%=:\n\t" \
  "s_lshl_b32 s93, s90, 2\n\t" \
  "s_sub_i32 s93, %[tgt], s93\n\t" \
  "s_cmp_le_i32 s93, 0\n\t" \
  "s_cbranch_scc1 .Lgo%=\n\t" \
  "s_min_i32 s93, s93, 8000\n\t" \
  "s_mov_b32 s94, s93\n\t" \
  ".Lsl%=:\n\t" \
  "s_sub_i32 s94, s94, 18\n\t" \
  "s_cmp_lt_i32 s94, 30\n\t" \
  "s_cbranch_scc1 .Lsx%=\n\t" \
  "s_sleep 1\n\t" \
  "s_branch .Lsl%=\n\t" \
  ".Lsx%=:\n\t" \
  "s_sub_i32 s93, s93, 12\n\t" \
  "s_max_i32 s93, s93, 4\n\t" \
  "s_lshr_b32 s93, s93, 2\n\t" \
  "s_add_u32 s93, s93, s90\n\t" \
  "s_memrealtime s[90:91]\n\t" \
  ".Lpace%=:\n\t" \
  "s_memrealtime s[94:95]\n\t" \
  "s_waitcnt lgkmcnt(1)\n\t" \
  "s_sub_i32 s92, s90, s93\n\t" \
  "s_cmp_ge_i32 s92, 0\n\t" \
  "s_cbranch_scc1 .Le%=\n\t" \
  "s_memrealtime s[90:91]\n\t" \
  "s_waitcnt lgkmcnt(1)\n\t" \
  "s_sub_i32 s92, s94, s93\n\t" \
  "s_cmp_lt_i32 s92, 0\n\t" \
  "s_cbranch_scc1 .Lpace%=\n\t" \
  "s_nop 4\n\t" \
  ".Le%=:\n\t" \
  "s_add_u32 s92, s92, s93\n\t" \
  "s_lshl_b32 s92, s92, 2\n\t" \
  "s_sub_i32 s92, %[tgt], s92\n\t" \
  "s_max_i32 s92, s92, 0\n\t" \
  "s_bitcmp0_b32 s92, 3\n\t" \
  "s_cbranch_scc1 .Lf3%=\n\t" \
  "s_nop 15\n\t" \
  ".Lf3%=:\n\t" \
  "s_bitcmp0_b32 s92, 2\n\t" \
  "s_cbranch_scc1 .Lf2%=\n\t" \
  "s_nop 10\n\t" \
  ".Lf2%=:\n\t" \
  ".Lgo%=:\n\t" \
  "s_mov_b32 s92, 0\n\t" \
  ".Lpoll%=:\n\t" \
  "buffer_load_dwordx4 v[200:203], v248, %[rs], %[som] offen sc1\n\t" \
  "buffer_load_dwordx4 v[204:207], v248, %[rs], %[som2] offen sc1\n\t" \
  "s_waitcnt vmcnt(1) lgkmcnt(0)\n\t" \
  "ds_write_b128 v249, v[200:203]\n\t" \
  "v_smfmac_f32_16x16x64_bf16 v[192:195], v[200:203], v[208:215], v251\n\t" \
  "v_max3_u32 v244, v200, v201, v202\n\t" \
  "v_smfmac_f32_16x16x64_bf16 v[196:199], v[200:203], v[236:243], v251\n\t" \
  "s_waitcnt vmcnt(0)\n\t" \
  "ds_write_b128 v249, v[204:207] offset:8192\n\t" \
  "v_smfmac_f32_16x16x64_bf16 v[192:195], v[204:207], v[216:223], v251\n\t" \
  "v_max3_u32 v244, v244, v203, v204\n\t" \
  "v_max3_u32 v244, v244, v205, v206\n\t" \
  "v_smfmac_f32_16x16x64_bf16 v[196:199], v[204:207], v[224:231], v251\n\t" \
  "v_max_u32 v244, v244, v207\n\t" \
  "v_cmp_le_u32 vcc, %[sent], v244\n\t" \
  "s_cbranch_vccnz .Lslow%=\n\t" \
  ".Lok%=:\n\t" \
  "ds_read_b128 v[216:219], v250 offset:32768\n\t" \
  "ds_read_b128 v[220:223], v250 offset:33792\n\t" \
  "ds_read_b128 v[224:227], v250 offset:34816\n\t" \
  "ds_read_b128 v[228:231], v250 offset:35840\n\t" \
  "s_waitcnt lgkmcnt(4)\n\t" \
  "s_barrier\n\t" \
  "ds_read_b128 v[208:211], v248 offset:0\n\t" \
  "ds_read_b128 v[212:215], v248 offset:3072\n\t" \
  "ds_read_b128 v[236:239], v248 offset:4096\n\t" \
  "ds_read_b128 v[240:243], v248 offset:5120\n\t" \
  "s_cmp_eq_u32 %[step], 63\n\t" \
  "s_cbranch_scc1 .Lls%=\n\t" \
  "s_waitcnt lgkmcnt(3)\n\t" \
  "v_smfmac_f32_16x16x64_bf16 v[192:195], v[208:211], v[216:223], v251\n\t" \
  "ds_read_b128 v[244:247], v248 offset:6144\n\t" \
  "v_smfmac_f32_16x16x64_bf16 v[196:199], v[208:211], v[224:231], v251\n\t" \
  "s_waitcnt lgkmcnt(3)\n\t" \
  "ds_read_b128 v[200:203], v248 offset:12288\n\t" \
  "v_smfmac_f32_16x16x64_bf16 v[192:195], v[212:215], v[0:7], v251\n\t" \
  "ds_read_b128 v[216:219], v250 offset:0\n\t" \
  "v_smfmac_f32_16x16x64_bf16 v[196:199], v[212:215], v[8:15], v251\n\t" \
  "s_waitcnt lgkmcnt(4)\n\t" \
  "ds_read_b128 v[220:223], v250 offset:1024\n\t" \
  "v_smfmac_f32_16x16x64_bf16 v[192:195], v[236:239], v[16:23], v251\n\t" \
  "ds_read_b128 v[224:227], v250 offset:2048\n\t" \
  "v_smfmac_f32_16x16x64_bf16 v[196:199], v[236:239], v[24:31], v251\n\t" \
  "s_waitcnt lgkmcnt(4)\n\t" \
  "ds_read_b128 v[228:231], v250 offset:3072\n\t" \
  "v_smfmac_f32_16x16x64_bf16 v[192:195], v[240:243], v[32:39], v251\n\t" \
  "ds_read_b128 v[204:207], v248 offset:7168\n\t" \
  "v_smfmac_f32_16x16x64_bf16 v[196:199], v[240:243], v[40:47], v251\n\t" \
  "ds_read_b128 v[208:211], v248 offset:8192\n\t" \
  "v_smfmac_f32_16x16x64_bf16 v[192:195], v[244:247], v[48:55], v251\n\t" \
  "ds_read_b128 v[212:215], v248 offset:10240\n\t" \
  "v_smfmac_f32_16x16x64_bf16 v[196:199], v[244:247], v[56:63], v251\n\t" \
  "s_waitcnt lgkmcnt(5)\n\t" \
  "v_smfmac_f32_16x16x64_bf16 v[192:195], v[200:203], v[216:223], v251\n\t" \
  "s_waitcnt lgkmcnt(3)\n\t" \
  "ds_read_b128 v[236:239], v248 offset:11264\n\t" \
  "v_smfmac_f32_16x16x64_bf16 v[196:199], v[200:203], v[224:231], v251\n\t" \
  "s_waitcnt lgkmcnt(3)\n\t" \
  "v_smfmac_f32_16x16x64_bf16 v[192:195], v[204:207], v[64:71], v251\n\t" \
  "ds_read_b128 v[240:243], v248 offset:13312\n\t" \
  "v_smfmac_f32_16x16x64_bf16 v[196:199], v[204:207], v[72:79], v251\n\t" \
  "s_waitcnt lgkmcnt(2)\n\t" \
  "v_smfmac_f32_16x16x64_bf16 v[192:195], v[208:211], v[80:87], v251\n\t" \
  "ds_read_b128 v[244:247], v248 offset:14336\n\t" \
  "v_smfmac_f32_16x16x64_bf16 v[196:199], v[208:211], v[88:95], v251\n\t" \
  "ds_read_b128 v[200:203], v248 offset:15360\n\t" \
  "v_smfmac_f32_16x16x64_bf16 v[192:195], v[212:215], v[96:103], v251\n\t" \
  "ds_read_b128 v[204:207], v248 offset:2048\n\t" \
  "v_smfmac_f32_16x16x64_bf16 v[196:199], v[212:215], v[104:111], v251\n\t" \
  "s_waitcnt lgkmcnt(3)\n\t" \
  "v_smfmac_f32_16x16x64_bf16 v[192:195], v[236:239], v[112:119], v251\n\t" \
  "v_smfmac_f32_16x16x64_bf16 v[196:199], v[236:239], v[120:127], v251\n\t" \
  "v_smfmac_f32_16x16x64_bf16 v[192:195], v[240:243], v[128:135], v251\n\t" \
  "v_smfmac_f32_16x16x64_bf16 v[196:199], v[240:243], v[136:143], v251\n\t" \
  "s_waitcnt lgkmcnt(0)\n\t" \
  "v_smfmac_f32_16x16x64_bf16 v[192:195], v[244:247], v[144:151], v251\n\t" \
  "v_smfmac_f32_16x16x64_bf16 v[196:199], v[244:247], v[152:159], v251\n\t" \
  "v_smfmac_f32_16x16x64_bf16 v[192:195], v[200:203], v[160:167], v251\n\t" \
  "v_smfmac_f32_16x16x64_bf16 v[196:199], v[200:203], v[168:175], v251\n\t" \
  "v_smfmac_f32_16x16x64_bf16 v[192:195], v[204:207], v[176:183], v251\n\t" \
  "v_smfmac_f32_16x16x64_bf16 v[196:199], v[204:207], v[184:191], v251\n\t" \
  "ds_read_b128 v[200:203], v248 offset:17408\n\t" \
  "ds_read_b128 v[204:207], v248 offset:21504\n\t" \
  "v_mov_b32 v250, v232\n\t" \
  "v_mov_b32 v249, v233\n\t" \
  "s_nop 2\n\t" \
  "v_add_f32 v192, v192, v194\n\t" \
  "v_add_f32 v193, v193, v195\n\t" \
  "v_add_f32 v196, v196, v198\n\t" \
  "v_add_f32 v197, v197, v199\n\t" \
  "v_cvt_pk_bf16_f32 v192, v192, v193\n\t" \
  "v_cvt_pk_bf16_f32 v196, v196, v197\n\t" \
  "ds_write2_b32 v252, v192, v196 offset1:64\n\t" \
  "v_mov_b32 v251, v234\n\t" \
  "ds_read_b64_tr_b16 v[236:237], v250 offset:512\n\t" \
  "ds_read_b64_tr_b16 v[238:239], v250 offset:576\n\t" \
  "v_mov_b32 v212, %[step]\n\t" \
  "s_mov_b64 exec, 1\n\t" \
  "ds_write_b32 v248, v212 offset:28688\n\t" \
  "s_mov_b64 exec, -1\n\t" \
  "buffer_load_dwordx4 v[192:195], v248, %[rt], %[svt] offen offset:0 sc1\n\t" \
  "buffer_load_dwordx4 v[196:199], v248, %[rt], %[svt] offen offset:1024 sc1\n\t" \
  "s_waitcnt lgkmcnt(1)\n\t" \
  "v_mfma_f32_16x16x32_bf16 v[200:203], v[200:203], v[236:239], 0\n\t" \
  "v_mfma_f32_16x16x32_bf16 v[204:207], v[204:207], v[236:239], 0\n\t" \
  "s_nop 7\n\t" \
  "v_cndmask_b32 v216, v200, v204, %[nh]\n\t" \
  "v_cndmask_b32 v217, v201, v205, %[nh]\n\t" \
  "v_cndmask_b32 v218, v202, v206, %[nh]\n\t" \
  "v_cndmask_b32 v219, v203, v207, %[nh]\n\t" \
  "ds_write_b128 v248, v[216:219] offset:17408\n\t" \
  "buffer_load_dword v252, v249, %[rs], %[sv] offen offset:0 sc1\n\t" \
  "buffer_load_dword v253, v249, %[rs], %[sv] offen offset:256 sc1\n\t" \
  "ds_read_b32 v212, v248 offset:28672\n\t" \
  "ds_read_b64_tr_b16 v[240:241], v250 offset:1024\n\t" \
  "ds_read_b64_tr_b16 v[242:243], v250 offset:1088\n\t" \
  "ds_read_b64_tr_b16 v[244:245], v250 offset:1536\n\t" \
  "ds_read_b64_tr_b16 v[246:247], v250 offset:1600\n\t" \
  "ds_read_b64_tr_b16 v[232:233], v250 offset:0\n\t" \
  "ds_read_b64_tr_b16 v[234:235], v250 offset:64\n\t" \
  "s_setprio 3\n\t" \
  "s_waitcnt lgkmcnt(0)\n\t" \
  "v_cmp_ne_u32 vcc, %[step], v212\n\t" \
  "s_and_b32 s93, vcc_lo, 0xf\n\t" \
  "s_barrier\n\t" \
  "s_cbranch_scc1 .Lre%=\n\t" \
  ".Lrb%=:\n\t" \
  "ds_read_b64_tr_b16 v[204:205], v250 offset:2560\n\t" \
  "ds_read_b64_tr_b16 v[206:207], v250 offset:2624\n\t" \
  "ds_read_b64_tr_b16 v[208:209], v250 offset:3072\n\t" \
  "ds_read_b64_tr_b16 v[210:211], v250 offset:3136\n\t" \
  "ds_read_b64_tr_b16 v[212:213], v250 offset:3584\n\t" \
  "ds_read_b64_tr_b16 v[214:215], v250 offset:3648\n\t" \
  "ds_read_b64_tr_b16 v[200:201], v250 offset:2048\n\t" \
  "ds_read_b64_tr_b16 v[202:203], v250 offset:2112\n\t" \
  "ds_read_b128 v[220:223], v248 offset:18432\n\t" \
  "ds_read_b128 v[224:227], v248 offset:19456\n\t" \
  "ds_read_b128 v[228:231], v248 offset:16384\n\t" \
  "s_waitcnt lgkmcnt(9)\n\t" \
  "v_mfma_f32_16x16x32_bf16 v[204:207], v[204:207], v[236:239], 0\n\t" \
  "s_waitcnt lgkmcnt(7)\n\t" \
  "v_mfma_f32_16x16x32_bf16 v[204:207], v[208:211], v[240:243], v[204:207]\n\t" \
  "s_waitcnt lgkmcnt(5)\n\t" \
  "v_mfma_f32_16x16x32_bf16 v[204:207], v[212:215], v[244:247], v[204:207]\n\t" \
  "s_waitcnt lgkmcnt(3)\n\t" \
  "v_mfma_f32_16x16x32_bf16 v[204:207], v[200:203], v[232:235], v[204:207]\n\t" \
  "s_waitcnt lgkmcnt(2)\n\t" \
  "v_pk_add_f32 v[216:217], v[216:217], v[220:221]\n\t" \
  "v_pk_add_f32 v[218:219], v[218:219], v[222:223]\n\t" \
  "s_waitcnt lgkmcnt(0)\n\t" \
  "v_pk_add_f32 v[224:225], v[224:225], v[228:229]\n\t" \
  "v_pk_add_f32 v[226:227], v[226:227], v[230:231]\n\t" \
  "v_pk_add_f32 v[216:217], v[216:217], v[224:225]\n\t" \
  "v_pk_add_f32 v[218:219], v[218:219], v[226:227]\n\t" \
  "v_max3_f32 v224, v216, v217, v218\n\t" \
  "v_max_f32 v224, v224, v219\n\t" \
  "s_nop 1\n\t" \
  "v_max_f32_dpp v225, v224, v224 row_ror:8 row_mask:0xf bank_mask:0xf\n\t" \
  "v_max_f32_dpp v226, v224, v224 row_ror:8 row_mask:0xf bank_mask:0xf\n\t" \
  "s_nop 1\n\t" \
  "v_permlane32_swap_b32 v225, v226\n\t" \
  "v_max_f32 v231, v225, v226\n\t" \
  "v_mov_b32 v220, 0x3f803f80\n\t" \
  "v_mov_b32 v221, 0x3f803f80\n\t" \
  "v_mov_b64 v[222:223], v[220:221]\n\t" \
  "v_mov_b32 v237, 0\n\t" \
  "v_mov_b32 v238, 0\n\t" \
  "v_max3_f32 v208, v204, v205, v231\n\t" \
  "v_max3_f32 v209, v208, v206, v207\n\t" \
  "v_max3_f32 v210, v208, v206, v207\n\t" \
  "v_cndmask_b32 v204, v204, v206, %[up]\n\t" \
  "v_cndmask_b32 v205, v205, v207, %[up]\n\t" \
  "v_permlane16_swap_b32 v209, v210\n\t" \
  "v_max_f32 v208, v209, v210\n\t" \
  "v_mul_f32 v208, %[negc], v208\n\t" \
  "v_fma_f32 v204, v204, %[c], v208\n\t" \
  "v_fma_f32 v205, v205, %[c], v208\n\t" \
  "v_exp_f32 v204, v204\n\t" \
  "v_fma_f32 v216, v216, %[c], v208\n\t" \
  "v_exp_f32 v205, v205\n\t" \
  "v_fma_f32 v217, v217, %[c], v208\n\t" \
  "v_exp_f32 v216, v216\n\t" \
  "v_fma_f32 v218, v218, %[c], v208\n\t" \
  "v_cvt_pk_bf16_f32 v236, v204, v205\n\t" \
  "v_cvt_pk_bf16_f32 v239, v204, v205\n\t" \
  "v_exp_f32 v217, v217\n\t" \
  "v_fma_f32 v219, v219, %[c], v208\n\t" \
  "v_mfma_f32_16x16x16_bf16 v[200:203], v[220:221], v[236:237], 0\n\t" \
  "v_exp_f32 v218, v218\n\t" \
  "s_waitcnt vmcnt(0)\n\t" \
  "v_mfma_f32_16x16x16_bf16 v[224:227], v[252:253], v[236:237], 0\n\t" \
  "v_cvt_pk_bf16_f32 v214, v216, v217\n\t" \
  "v_cvt_pk_bf16_f32 v212, v216, v217\n\t" \
  "v_exp_f32 v219, v219\n\t" \
  "v_mfma_f32_16x16x16_bf16 v[228:231], v[252:253], v[238:239], 0\n\t" \
  "v_cvt_pk_bf16_f32 v215, v218, v219\n\t" \
  "v_cvt_pk_bf16_f32 v213, v218, v219\n\t" \
  "v_mov_b32_dpp v212, v214 row_ror:8 row_mask:0xf bank_mask:0xc\n\t" \
  "v_mov_b32_dpp v214, v214 row_ror:8 row_mask:0xf bank_mask:0x3\n\t" \
  "v_mov_b32_dpp v213, v215 row_ror:8 row_mask:0xf bank_mask:0xc\n\t" \
  "v_mov_b32_dpp v215, v215 row_ror:8 row_mask:0xf bank_mask:0x3\n\t" \
  "v_max_u32 v208, v252, v253\n\t" \
  "v_cmp_le_u32 vcc, %[sent], v208\n\t" \
  "v_mfma_f32_16x16x32_bf16 v[200:203], v[220:223], v[212:215], v[200:203]\n\t" \
  ".Lpv%=:\n\t" \
  "v_mfma_f32_16x16x32_bf16 v[224:227], v[192:195], v[212:215], v[224:227]\n\t" \
  "v_mfma_f32_16x16x32_bf16 v[228:231], v[196:199], v[212:215], v[228:231]\n\t" \
  "s_cbranch_vccnz .Lvs%=\n\t" \
  ".Lvok%=:\n\t" \
  "s_nop 4\n\t" \
  "v_rcp_f32 v208, v200\n\t" \
  "v_cndmask_b32 v224, v224, v226, %[nh]\n\t" \
  "v_cndmask_b32 v225, v225, v227, %[nh]\n\t" \
  "v_cndmask_b32 v228, v228, v230, %[nh]\n\t" \
  "v_cndmask_b32 v229, v229, v231, %[nh]\n\t" \
  "v_pk_mul_f32 v[224:225], v[224:225], v[208:209] op_sel_hi:[1,0]\n\t" \
  "v_pk_mul_f32 v[228:229], v[228:229], v[208:209] op_sel_hi:[1,0]\n\t" \
  "v_cvt_pk_bf16_f32 v224, v224, v225\n\t" \
  "v_cvt_pk_bf16_f32 v225, v228, v229\n\t" \
  "buffer_store_dwordx2 v[224:225], v251, %[rs], %[so] offen sc1\n\t" \
  "s_memtime s[90:91]\n\t" \
  "s_setprio 0\n\t" \
  "s_waitcnt lgkmcnt(0)\n\t" \
  "s_mov_b32 %[tout], s90\n\t" \
  "s_mov_b32 %[spins], s92\n\t" \
  "s_branch .Lend%=\n\t" \
  ".Lls%=:\n\t" \
  "s_waitcnt lgkmcnt(0)\n\t" \
  "s_barrier\n\t" \
  "s_mov_b32 %[spins], 0\n\t" \
  "s_branch .Lend%=\n\t" \
  ".Lre%=:\n\t" \
  "ds_read_b64_tr_b16 v[240:241], v250 offset:1024\n\t" \
  "ds_read_b64_tr_b16 v[242:243], v250 offset:1088\n\t" \
  "ds_read_b64_tr_b16 v[244:245], v250 offset:1536\n\t" \
  "ds_read_b64_tr_b16 v[246:247], v250 offset:1600\n\t" \
  "ds_read_b64_tr_b16 v[232:233], v250 offset:0\n\t" \
  "ds_read_b64_tr_b16 v[234:235], v250 offset:64\n\t" \
  "s_waitcnt lgkmcnt(0)\n\t" \
  "s_branch .Lrb%=\n\t" \
  ".Lvs%=:\n\t" \
  "v_cmp_eq_u32 vcc, %[sent], v252\n\t" \
  "v_cmp_eq_u32 s[94:95], %[sent], v253\n\t" \
  "s_or_b64 s[94:95], s[94:95], vcc\n\t" \
  "s_cbranch_scc0 .Lvok%=\n\t" \
  "s_add_u32 s92, s92, 1\n\t" \
  "s_sleep 2\n\t" \
  "s_and_b32 s93, s92, 63\n\t" \
  "s_cbranch_scc1 .Lvr%=\n\t" \
  "buffer_load_dword v208, off, %[rs], %[sab] sc1\n\t" \
  "s_waitcnt vmcnt(0)\n\t" \
  "v_readfirstlane_b32 s93, v208\n\t" \
  "s_nop 3\n\t" \
  "s_cmp_eq_u32 s93, 1\n\t" \
  "s_cbranch_scc1 .Lvok%=\n\t" \
  "s_cmp_lt_u32 s92, 0x80000\n\t" \
  "s_cbranch_scc1 .Lvr%=\n\t" \
  "v_mov_b32 v208, 1\n\t" \
  "buffer_store_dword v208, off, %[rs], %[sab] sc1\n\t" \
  "s_branch .Lvok%=\n\t" \
  ".Lvr%=:\n\t" \
  "buffer_load_dword v252, v249, %[rs], %[sv] offen offset:0 sc1\n\t" \
  "buffer_load_dword v253, v249, %[rs], %[sv] offen offset:256 sc1\n\t" \
  "s_waitcnt vmcnt(0)\n\t" \
  "v_mfma_f32_16x16x16_bf16 v[224:227], v[252:253], v[236:237], 0\n\t" \
  "v_mfma_f32_16x16x16_bf16 v[228:231], v[252:253], v[238:239], 0\n\t" \
  "v_max_u32 v208, v252, v253\n\t" \
  "v_cmp_le_u32 vcc, %[sent], v208\n\t" \
  "s_nop 4\n\t" \
  "s_branch .Lpv%=\n\t" \
  "s_branch .Lend%=\n\t" \
  ".Lslow%=:\n\t" \
  "s_mov_b64 s[94:95], 0\n\t" \
  "v_cmp_eq_u32 vcc, %[sent], v200\n\t" \
  "s_or_b64 s[94:95], s[94:95], vcc\n\t" \
  "v_cmp_eq_u32 vcc, %[sent], v201\n\t" \
  "s_or_b64 s[94:95], s[94:95], vcc\n\t" \
  "v_cmp_eq_u32 vcc, %[sent], v202\n\t" \
  "s_or_b64 s[94:95], s[94:95], vcc\n\t" \
  "v_cmp_eq_u32 vcc, %[sent], v203\n\t" \
  "s_or_b64 s[94:95], s[94:95], vcc\n\t" \
  "v_cmp_eq_u32 vcc, %[sent], v204\n\t" \
  "s_or_b64 s[94:95], s[94:95], vcc\n\t" \
  "v_cmp_eq_u32 vcc, %[sent], v205\n\t" \
  "s_or_b64 s[94:95], s[94:95], vcc\n\t" \
  "v_cmp_eq_u32 vcc, %[sent], v206\n\t" \
  "s_or_b64 s[94:95], s[94:95], vcc\n\t" \
  "v_cmp_eq_u32 vcc, %[sent], v207\n\t" \
  "s_or_b64 s[94:95], s[94:95], vcc\n\t" \
  "s_cmp_eq_u64 s[94:95], 0\n\t" \
  "s_cbranch_scc1 .Lok%=\n\t" \
  "v_mov_b64 v[192:193], 0\n\t" \
  "v_mov_b64 v[194:195], 0\n\t" \
  "v_mov_b64 v[196:197], 0\n\t" \
  "v_mov_b64 v[198:199], 0\n\t" \
  "s_add_u32 s92, s92, 1\n\t" \
  "s_cmp_eq_u32 s92, 1\n\t" \
  "s_cbranch_scc1 .Lpoll%=\n\t" \
  "s_sleep 2\n\t" \
  "s_and_b32 s93, s92, 63\n\t" \
  "s_cmp_lg_u32 s93, 0\n\t" \
  "s_cbranch_scc1 .Lpoll%=\n\t" \
  "v_mov_b32 v245, 0\n\t" \
  "buffer_load_dword v244, v245, %[rs], %[sab] offen sc1\n\t" \
  "s_waitcnt vmcnt(0)\n\t" \
  "v_readfirstlane_b32 s93, v244\n\t" \
  "s_nop 3\n\t" \
  "s_cmp_eq_u32 s93, 1\n\t" \
  "s_cbranch_scc1 .Lok%=\n\t" \
  "s_cmp_lt_u32 s92, 0x80000\n\t" \
  "s_cbranch_scc1 .Lpoll%=\n\t" \
  "v_mov_b32 v244, 1\n\t" \
  "buffer_store_dword v244, v245, %[rs], %[sab] offen sc1\n\t" \
  "s_branch .Lok%=\n\t" \
  ".Lend%=:\n\t"
#define QK_ASM_2 \
  "s_waitcnt lgkmcnt(0)\n\t" \
  "v_subrev_u32 v244, 0x8000, v250\n\t" \
  "ds_read_b128 v[216:219], v248 offset:40960\n\t" \
  "ds_read_b128 v[220:223], v248 offset:41984\n\t" \
  "ds_read_b128 v[224:227], v248 offset:43008\n\t" \
  "ds_read_b128 v[228:231], v248 offset:44032\n\t" \
  "ds_read_b128 v[208:211], v244 offset:0\n\t" \
  "ds_read_b128 v[212:215], v244 offset:1024\n\t" \
  "ds_read_b128 v[236:239], v244 offset:2048\n\t" \
  "ds_read_b128 v[240:243], v244 offset:3072\n\t" \
  "v_mov_b64 v[192:193], 0\n\t" \
  "v_mov_b64 v[194:195], 0\n\t" \
  "v_mov_b64 v[196:197], 0\n\t" \
  "v_mov_b64 v[198:199], 0\n\t" \
  "s_movk_i32 s92, 0x2000\n\t" \
  ".Lwt%=:\n\t" \
  "ds_read_b64 v[200:201], v248 offset:30720\n\t" \
  "s_memrealtime s[90:91]\n\t" \
  "s_sub_u32 s92, s92, 1\n\t" \
  "s_waitcnt lgkmcnt(0)\n\t" \
  "v_readfirstlane_b32 s93, v200\n\t" \
  "v_readfirstlane_b32 %[tgt], v201\n\t" \
  "s_cbranch_scc1 .Lgo%=\n\t" \
  "s_cmp_eq_u32 s93, %[step]\n\t" \
  "s_cbranch_scc1 .Lwx%=\n\t" \
  "s_sleep 1\n\t" \
  "s_branch .Lwt%=\n\t" \
  ".Lwx%=:\n\t" \
  "s_lshl_b32 s93, s90, 2\n\t" \
  "s_sub_i32 s93, %[tgt], s93\n\t" \
  "s_cmp_le_i32 s93, 0\n\t" \
  "s_cbranch_scc1 .Lgo%=\n\t" \
  "s_min_i32 s93, s93, 8000\n\t" \
  "s_mov_b32 s94, s93\n\t" \
  ".Lsl%=:\n\t" \
  "s_sub_i32 s94, s94, 18\n\t" \
  "s_cmp_lt_i32 s94, 30\n\t" \
  "s_cbranch_scc1 .Lsx%=\n\t" \
  "s_sleep 1\n\t" \
  "s_branch .Lsl%=\n\t" \
  ".Lsx%=:\n\t" \
  "s_sub_i32 s93, s93, 12\n\t" \
  "s_max_i32 s93, s93, 4\n\t" \
  "s_lshr_b32 s93, s93, 2\n\t" \
  "s_add_u32 s93, s93, s90\n\t" \
  "s_memrealtime s[90:91]\n\t" \
  ".Lpace%=:\n\t" \
  "s_memrealtime s[94:95]\n\t" \
  "s_waitcnt lgkmcnt(1)\n\t" \
  "s_sub_i32 s92, s90, s93\n\t" \
  "s_cmp_ge_i32 s92, 0\n\t" \
  "s_cbranch_scc1 .Le%=\n\t" \
  "s_memrealtime s[90:91]\n\t" \
  "s_waitcnt lgkmcnt(1)\n\t" \
  "s_sub_i32 s92, s94, s93\n\t" \
  "s_cmp_lt_i32 s92, 0\n\t" \
  "s_cbranch_scc1 .Lpace%=\n\t" \
  "s_nop 4\n\t" \
  ".Le%=:\n\t" \
  "s_add_u32 s92, s92, s93\n\t" \
  "s_lshl_b32 s92, s92, 2\n\t" \
  "s_sub_i32 s92, %[tgt], s92\n\t" \
  "s_max_i32 s92, s92, 0\n\t" \
  "s_bitcmp0_b32 s92, 3\n\t" \
  "s_cbranch_scc1 .Lf3%=\n\t" \
  "s_nop 15\n\t" \
  ".Lf3%=:\n\t" \
  "s_bitcmp0_b32 s92, 2\n\t" \
  "s_cbranch_scc1 .Lf2%=\n\t" \
  "s_nop 10\n\t" \
  ".Lf2%=:\n\t" \
  ".Lgo%=:\n\t" \
  "s_mov_b32 s92, 0\n\t" \
  ".Lpoll%=:\n\t" \
  "buffer_load_dwordx4 v[200:203], v248, %[rs], %[som] offen sc1\n\t" \
  "buffer_load_dwordx4 v[204:207], v248, %[rs], %[som2] offen sc1\n\t" \
  "s_waitcnt vmcnt(1) lgkmcnt(0)\n\t" \
  "ds_write_b128 v249, v[200:203]\n\t" \
  "v_smfmac_f32_16x16x64_bf16 v[192:195], v[200:203], v[208:215], v251\n\t" \
  "v_max3_u32 v244, v200, v201, v202\n\t" \
  "v_smfmac_f32_16x16x64_bf16 v[196:199], v[200:203], v[236:243], v251\n\t" \
  "s_waitcnt vmcnt(0)\n\t" \
  "ds_write_b128 v249, v[204:207] offset:8192\n\t" \
  "v_smfmac_f32_16x16x64_bf16 v[192:195], v[204:207], v[216:223], v251\n\t" \
  "v_max3_u32 v244, v244, v203, v204\n\t" \
  "v_max3_u32 v244, v244, v205, v206\n\t" \
  "v_smfmac_f32_16x16x64_bf16 v[196:199], v[204:207], v[224:231], v251\n\t" \
  "v_max_u32 v244, v244, v207\n\t" \
  "v_cmp_le_u32 vcc, %[sent], v244\n\t" \
  "s_cbranch_vccnz .Lslow%=\n\t" \
  ".Lok%=:\n\t" \
  "ds_read_b128 v[216:219], v250 offset:32768\n\t" \
  "ds_read_b128 v[220:223], v250 offset:33792\n\t" \
  "ds_read_b128 v[224:227], v250 offset:34816\n\t" \
  "ds_read_b128 v[228:231], v250 offset:35840\n\t" \
  "s_waitcnt lgkmcnt(4)\n\t" \
  "s_barrier\n\t" \
  "ds_read_b128 v[208:211], v248 offset:3072\n\t" \
  "ds_read_b128 v[212:215], v248 offset:4096\n\t" \
  "ds_read_b128 v[236:239], v248 offset:5120\n\t" \
  "ds_read_b128 v[240:243], v248 offset:6144\n\t" \
  "s_cmp_eq_u32 %[step], 63\n\t" \
  "s_cbranch_scc1 .Lls%=\n\t" \
  "s_waitcnt lgkmcnt(3)\n\t" \
  "v_smfmac_f32_16x16x64_bf16 v[192:195], v[208:211], v[216:223], v251\n\t" \
  "ds_read_b128 v[244:247], v248 offset:7168\n\t" \
  "v_smfmac_f32_16x16x64_bf16 v[196:199], v[208:211], v[224:231], v251\n\t" \
  "s_waitcnt lgkmcnt(3)\n\t" \
  "ds_read_b128 v[200:203], v248 offset:13312\n\t" \
  "v_smfmac_f32_16x16x64_bf16 v[192:195], v[212:215], v[0:7], v251\n\t" \
  "ds_read_b128 v[216:219], v250 offset:0\n\t" \
  "v_smfmac_f32_16x16x64_bf16 v[196:199], v[212:215], v[8:15], v251\n\t" \
  "s_waitcnt lgkmcnt(4)\n\t" \
  "ds_read_b128 v[220:223], v250 offset:1024\n\t" \
  "v_smfmac_f32_16x16x64_bf16 v[192:195], v[236:239], v[16:23], v251\n\t" \
  "ds_read_b128 v[224:227], v250 offset:2048\n\t" \
  "v_smfmac_f32_16x16x64_bf16 v[196:199], v[236:239], v[24:31], v251\n\t" \
  "s_waitcnt lgkmcnt(4)\n\t" \
  "ds_read_b128 v[228:231], v250 offset:3072\n\t" \
  "v_smfmac_f32_16x16x64_bf16 v[192:195], v[240:243], v[32:39], v251\n\t" \
  "ds_read_b128 v[204:207], v248 offset:8192\n\t" \
  "v_smfmac_f32_16x16x64_bf16 v[196:199], v[240:243], v[40:47], v251\n\t" \
  "ds_read_b128 v[208:211], v248 offset:9216\n\t" \
  "v_smfmac_f32_16x16x64_bf16 v[192:195], v[244:247], v[48:55], v251\n\t" \
  "ds_read_b128 v[212:215], v248 offset:11264\n\t" \
  "v_smfmac_f32_16x16x64_bf16 v[196:199], v[244:247], v[56:63], v251\n\t" \
  "s_waitcnt lgkmcnt(5)\n\t" \
  "v_smfmac_f32_16x16x64_bf16 v[192:195], v[200:203], v[216:223], v251\n\t" \
  "s_waitcnt lgkmcnt(3)\n\t" \
  "ds_read_b128 v[236:239], v248 offset:12288\n\t" \
  "v_smfmac_f32_16x16x64_bf16 v[196:199], v[200:203], v[224:231], v251\n\t" \
  "s_waitcnt lgkmcnt(3)\n\t" \
  "v_smfmac_f32_16x16x64_bf16 v[192:195], v[204:207], v[64:71], v251\n\t" \
  "ds_read_b128 v[240:243], v248 offset:14336\n\t" \
  "v_smfmac_f32_16x16x64_bf16 v[196:199], v[204:207], v[72:79], v251\n\t" \
  "s_waitcnt lgkmcnt(2)\n\t" \
  "v_smfmac_f32_16x16x64_bf16 v[192:195], v[208:211], v[80:87], v251\n\t" \
  "ds_read_b128 v[244:247], v248 offset:15360\n\t" \
  "v_smfmac_f32_16x16x64_bf16 v[196:199], v[208:211], v[88:95], v251\n\t" \
  "ds_read_b128 v[200:203], v248 offset:0\n\t" \
  "v_smfmac_f32_16x16x64_bf16 v[192:195], v[212:215], v[96:103], v251\n\t" \
  "ds_read_b128 v[204:207], v248 offset:1024\n\t" \
  "v_smfmac_f32_16x16x64_bf16 v[196:199], v[212:215], v[104:111], v251\n\t" \
  "s_waitcnt lgkmcnt(3)\n\t" \
  "v_smfmac_f32_16x16x64_bf16 v[192:195], v[236:239], v[112:119], v251\n\t" \
  "v_smfmac_f32_16x16x64_bf16 v[196:199], v[236:239], v[120:127], v251\n\t" \
  "v_smfmac_f32_16x16x64_bf16 v[192:195], v[240:243], v[128:135], v251\n\t" \
  "v_smfmac_f32_16x16x64_bf16 v[196:199], v[240:243], v[136:143], v251\n\t" \
  "s_waitcnt lgkmcnt(0)\n\t" \
  "v_smfmac_f32_16x16x64_bf16 v[192:195], v[244:247], v[144:151], v251\n\t" \
  "v_smfmac_f32_16x16x64_bf16 v[196:199], v[244:247], v[152:159], v251\n\t" \
  "v_smfmac_f32_16x16x64_bf16 v[192:195], v[200:203], v[160:167], v251\n\t" \
  "v_smfmac_f32_16x16x64_bf16 v[196:199], v[200:203], v[168:175], v251\n\t" \
  "v_smfmac_f32_16x16x64_bf16 v[192:195], v[204:207], v[176:183], v251\n\t" \
  "v_smfmac_f32_16x16x64_bf16 v[196:199], v[204:207], v[184:191], v251\n\t" \
  "ds_read_b128 v[200:203], v248 offset:18432\n\t" \
  "ds_read_b128 v[204:207], v248 offset:22528\n\t" \
  "v_mov_b32 v250, v232\n\t" \
  "v_mov_b32 v249, v233\n\t" \
  "s_nop 2\n\t" \
  "v_add_f32 v192, v192, v194\n\t" \
  "v_add_f32 v193, v193, v195\n\t" \
  "v_add_f32 v196, v196, v198\n\t" \
  "v_add_f32 v197, v197, v199\n\t" \
  "v_cvt_pk_bf16_f32 v192, v192, v193\n\t" \
  "v_cvt_pk_bf16_f32 v196, v196, v197\n\t" \
  "ds_write2_b32 v252, v192, v196 offset1:64\n\t" \
  "v_mov_b32 v251, v234\n\t" \
  "ds_read_b64_tr_b16 v[240:241], v250 offset:1024\n\t" \
  "ds_read_b64_tr_b16 v[242:243], v250 offset:1088\n\t" \
  "v_mov_b32 v212, %[step]\n\t" \
  "s_mov_b64 exec, 1\n\t" \
  "ds_write_b32 v248, v212 offset:28704\n\t" \
  "s_mov_b64 exec, -1\n\t" \
  "buffer_load_dwordx4 v[192:195], v248, %[rt], %[svt] offen offset:0 sc1\n\t" \
  "buffer_load_dwordx4 v[196:199], v248, %[rt], %[svt] offen offset:1024 sc1\n\t" \
  "s_waitcnt lgkmcnt(1)\n\t" \
  "v_mfma_f32_16x16x32_bf16 v[200:203], v[200:203], v[240:243], 0\n\t" \
  "v_mfma_f32_16x16x32_bf16 v[204:207], v[204:207], v[240:243], 0\n\t" \
  "s_nop 7\n\t" \
  "v_cndmask_b32 v216, v200, v204, %[nh]\n\t" \
  "v_cndmask_b32 v217, v201, v205, %[nh]\n\t" \
  "v_cndmask_b32 v218, v202, v206, %[nh]\n\t" \
  "v_cndmask_b32 v219, v203, v207, %[nh]\n\t" \
  "ds_write_b128 v248, v[216:219] offset:18432\n\t" \
  "buffer_load_dword v252, v249, %[rs], %[sv] offen offset:0 sc1\n\t" \
  "buffer_load_dword v253, v249, %[rs], %[sv] offen offset:256 sc1\n\t" \
  "ds_read_b32 v212, v248 offset:28672\n\t" \
  "ds_read_b64_tr_b16 v[244:245], v250 offset:1536\n\t" \
  "ds_read_b64_tr_b16 v[246:247], v250 offset:1600\n\t" \
  "ds_read_b64_tr_b16 v[232:233], v250 offset:0\n\t" \
  "ds_read_b64_tr_b16 v[234:235], v250 offset:64\n\t" \
  "ds_read_b64_tr_b16 v[236:237], v250 offset:512\n\t" \
  "ds_read_b64_tr_b16 v[238:239], v250 offset:576\n\t" \
  "s_setprio 3\n\t" \
  "s_waitcnt lgkmcnt(0)\n\t" \
  "v_cmp_ne_u32 vcc, %[step], v212\n\t" \
  "s_and_b32 s93, vcc_lo, 0xf\n\t" \
  "s_barrier\n\t" \
  "s_cbranch_scc1 .Lre%=\n\t" \
  ".Lrb%=:\n\t" \
  "ds_read_b64_tr_b16 v[208:209], v250 offset:3072\n\t" \
  "ds_read_b64_tr_b16 v[210:211], v250 offset:3136\n\t" \
  "ds_read_b64_tr_b16 v[212:213], v250 offset:3584\n\t" \
  "ds_read_b64_tr_b16 v[214:215], v250 offset:3648\n\t" \
  "ds_read_b64_tr_b16 v[200:201], v250 offset:2048\n\t" \
  "ds_read_b64_tr_b16 v[202:203], v250 offset:2112\n\t" \
  "ds_read_b64_tr_b16 v[204:205], v250 offset:2560\n\t" \
  "ds_read_b64_tr_b16 v[206:207], v250 offset:2624\n\t" \
  "ds_read_b128 v[220:223], v248 offset:19456\n\t" \
  "ds_read_b128 v[224:227], v248 offset:16384\n\t" \
  "ds_read_b128 v[228:231], v248 offset:17408\n\t" \
  "s_waitcnt lgkmcnt(9)\n\t" \
  "v_mfma_f32_16x16x32_bf16 v[208:211], v[208:211], v[240:243], 0\n\t" \
  "s_waitcnt lgkmcnt(7)\n\t" \
  "v_mfma_f32_16x16x32_bf16 v[208:211], v[212:215], v[244:247], v[208:211]\n\t" \
  "s_waitcnt lgkmcnt(5)\n\t" \
  "v_mfma_f32_16x16x32_bf16 v[208:211], v[200:203], v[232:235], v[208:211]\n\t" \
  "s_waitcnt lgkmcnt(3)\n\t" \
  "v_mfma_f32_16x16x32_bf16 v[208:211], v[204:207], v[236:239], v[208:211]\n\t" \
  "s_waitcnt lgkmcnt(2)\n\t" \
  "v_pk_add_f32 v[216:217], v[216:217], v[220:221]\n\t" \
  "v_pk_add_f32 v[218:219], v[218:219], v[222:223]\n\t" \
  "s_waitcnt lgkmcnt(0)\n\t" \
  "v_pk_add_f32 v[224:225], v[224:225], v[228:229]\n\t" \
  "v_pk_add_f32 v[226:227], v[226:227], v[230:231]\n\t" \
  "v_pk_add_f32 v[216:217], v[216:217], v[224:225]\n\t" \
  "v_pk_add_f32 v[218:219], v[218:219], v[226:227]\n\t" \
  "v_max3_f32 v224, v216, v217, v218\n\t" \
  "v_max_f32 v224, v224, v219\n\t" \
  "s_nop 1\n\t" \
  "v_max_f32_dpp v225, v224, v224 row_ror:8 row_mask:0xf bank_mask:0xf\n\t" \
  "v_max_f32_dpp v226, v224, v224 row_ror:8 row_mask:0xf bank_mask:0xf\n\t" \
  "s_nop 1\n\t" \
  "v_permlane32_swap_b32 v225, v226\n\t" \
  "v_max_f32 v231, v225, v226\n\t" \
  "v_mov_b32 v220, 0x3f803f80\n\t" \
  "v_mov_b32 v221, 0x3f803f80\n\t" \
  "v_mov_b64 v[222:223], v[220:221]\n\t" \
  "v_mov_b32 v241, 0\n\t" \
  "v_mov_b32 v242, 0\n\t" \
  "v_max3_f32 v212, v208, v209, v231\n\t" \
  "v_max3_f32 v213, v212, v210, v211\n\t" \
  "v_max3_f32 v214, v212, v210, v211\n\t" \
  "v_cndmask_b32 v208, v208, v210, %[up]\n\t" \
  "v_cndmask_b32 v209, v209, v211, %[up]\n\t" \
  "v_permlane16_swap_b32 v213, v214\n\t" \
  "v_max_f32 v212, v213, v214\n\t" \
  "v_mul_f32 v212, %[negc], v212\n\t" \
  "v_fma_f32 v208, v208, %[c], v212\n\t" \
  "v_fma_f32 v209, v209, %[c], v212\n\t" \
  "v_exp_f32 v208, v208\n\t" \
  "v_fma_f32 v216, v216, %[c], v212\n\t" \
  "v_exp_f32 v209, v209\n\t" \
  "v_fma_f32 v217, v217, %[c], v212\n\t" \
  "v_exp_f32 v216, v216\n\t" \
  "v_fma_f32 v218, v218, %[c], v212\n\t" \
  "v_cvt_pk_bf16_f32 v240, v208, v209\n\t" \
  "v_cvt_pk_bf16_f32 v243, v208, v209\n\t" \
  "v_exp_f32 v217, v217\n\t" \
  "v_fma_f32 v219, v219, %[c], v212\n\t" \
  "v_mfma_f32_16x16x16_bf16 v[204:207], v[220:221], v[240:241], 0\n\t" \
  "v_exp_f32 v218, v218\n\t" \
  "s_waitcnt vmcnt(0)\n\t" \
  "v_mfma_f32_16x16x16_bf16 v[224:227], v[252:253], v[240:241], 0\n\t" \
  "v_cvt_pk_bf16_f32 v202, v216, v217\n\t" \
  "v_cvt_pk_bf16_f32 v200, v216, v217\n\t" \
  "v_exp_f32 v219, v219\n\t" \
  "v_mfma_f32_16x16x16_bf16 v[228:231], v[252:253], v[242:243], 0\n\t" \
  "v_cvt_pk_bf16_f32 v203, v218, v219\n\t" \
  "v_cvt_pk_bf16_f32 v201, v218, v219\n\t" \
  "v_mov_b32_dpp v200, v202 row_ror:8 row_mask:0xf bank_mask:0xc\n\t" \
  "v_mov_b32_dpp v202, v202 row_ror:8 row_mask:0xf bank_mask:0x3\n\t" \
  "v_mov_b32_dpp v201, v203 row_ror:8 row_mask:0xf bank_mask:0xc\n\t" \
  "v_mov_b32_dpp v203, v203 row_ror:8 row_mask:0xf bank_mask:0x3\n\t" \
  "v_max_u32 v212, v252, v253\n\t" \
  "v_cmp_le_u32 vcc, %[sent], v212\n\t" \
  "v_mfma_f32_16x16x32_bf16 v[204:207], v[220:223], v[200:203], v[204:207]\n\t" \
  ".Lpv%=:\n\t" \
  "v_mfma_f32_16x16x32_bf16 v[224:227], v[192:195], v[200:203], v[224:227]\n\t" \
  "v_mfma_f32_16x16x32_bf16 v[228:231], v[196:199], v[200:203], v[228:231]\n\t" \
  "s_cbranch_vccnz .Lvs%=\n\t" \
  ".Lvok%=:\n\t" \
  "s_nop 4\n\t" \
  "v_rcp_f32 v212, v204\n\t" \
  "v_cndmask_b32 v224, v224, v226, %[nh]\n\t" \
  "v_cndmask_b32 v225, v225, v227, %[nh]\n\t" \
  "v_cndmask_b32 v228, v228, v230, %[nh]\n\t" \
  "v_cndmask_b32 v229, v229, v231, %[nh]\n\t" \
  "v_pk_mul_f32 v[224:225], v[224:225], v[212:213] op_sel_hi:[1,0]\n\t" \
  "v_pk_mul_f32 v[228:229], v[228:229], v[212:213] op_sel_hi:[1,0]\n\t" \
  "v_cvt_pk_bf16_f32 v224, v224, v225\n\t" \
  "v_cvt_pk_bf16_f32 v225, v228, v229\n\t" \
  "buffer_store_dwordx2 v[224:225], v251, %[rs], %[so] offen sc1\n\t" \
  "s_memtime s[90:91]\n\t" \
  "s_setprio 0\n\t" \
  "s_waitcnt lgkmcnt(0)\n\t" \
  "s_mov_b32 %[tout], s90\n\t" \
  "s_mov_b32 %[spins], s92\n\t" \
  "s_branch .Lend%=\n\t" \
  ".Lls%=:\n\t" \
  "s_waitcnt lgkmcnt(0)\n\t" \
  "s_barrier\n\t" \
  "s_mov_b32 %[spins], 0\n\t" \
  "s_branch .Lend%=\n\t" \
  ".Lre%=:\n\t" \
  "ds_read_b64_tr_b16 v[244:245], v250 offset:1536\n\t" \
  "ds_read_b64_tr_b16 v[246:247], v250 offset:1600\n\t" \
  "ds_read_b64_tr_b16 v[232:233], v250 offset:0\n\t" \
  "ds_read_b64_tr_b16 v[234:235], v250 offset:64\n\t" \
  "ds_read_b64_tr_b16 v[236:237], v250 offset:512\n\t" \
  "ds_read_b64_tr_b16 v[238:239], v250 offset:576\n\t" \
  "s_waitcnt lgkmcnt(0)\n\t" \
  "s_branch .Lrb%=\n\t" \
  ".Lvs%=:\n\t" \
  "v_cmp_eq_u32 vcc, %[sent], v252\n\t" \
  "v_cmp_eq_u32 s[94:95], %[sent], v253\n\t" \
  "s_or_b64 s[94:95], s[94:95], vcc\n\t" \
  "s_cbranch_scc0 .Lvok%=\n\t" \
  "s_add_u32 s92, s92, 1\n\t" \
  "s_sleep 2\n\t" \
  "s_and_b32 s93, s92, 63\n\t" \
  "s_cbranch_scc1 .Lvr%=\n\t" \
  "buffer_load_dword v212, off, %[rs], %[sab] sc1\n\t" \
  "s_waitcnt vmcnt(0)\n\t" \
  "v_readfirstlane_b32 s93, v212\n\t" \
  "s_nop 3\n\t" \
  "s_cmp_eq_u32 s93, 1\n\t" \
  "s_cbranch_scc1 .Lvok%=\n\t" \
  "s_cmp_lt_u32 s92, 0x80000\n\t" \
  "s_cbranch_scc1 .Lvr%=\n\t" \
  "v_mov_b32 v212, 1\n\t" \
  "buffer_store_dword v212, off, %[rs], %[sab] sc1\n\t" \
  "s_branch .Lvok%=\n\t" \
  ".Lvr%=:\n\t" \
  "buffer_load_dword v252, v249, %[rs], %[sv] offen offset:0 sc1\n\t" \
  "buffer_load_dword v253, v249, %[rs], %[sv] offen offset:256 sc1\n\t" \
  "s_waitcnt vmcnt(0)\n\t" \
  "v_mfma_f32_16x16x16_bf16 v[224:227], v[252:253], v[240:241], 0\n\t" \
  "v_mfma_f32_16x16x16_bf16 v[228:231], v[252:253], v[242:243], 0\n\t" \
  "v_max_u32 v212, v252, v253\n\t" \
  "v_cmp_le_u32 vcc, %[sent], v212\n\t" \
  "s_nop 4\n\t" \
  "s_branch .Lpv%=\n\t" \
  "s_branch .Lend%=\n\t" \
  ".Lslow%=:\n\t" \
  "s_mov_b64 s[94:95], 0\n\t" \
  "v_cmp_eq_u32 vcc, %[sent], v200\n\t" \
  "s_or_b64 s[94:95], s[94:95], vcc\n\t" \
  "v_cmp_eq_u32 vcc, %[sent], v201\n\t" \
  "s_or_b64 s[94:95], s[94:95], vcc\n\t" \
  "v_cmp_eq_u32 vcc, %[sent], v202\n\t" \
  "s_or_b64 s[94:95], s[94:95], vcc\n\t" \
  "v_cmp_eq_u32 vcc, %[sent], v203\n\t" \
  "s_or_b64 s[94:95], s[94:95], vcc\n\t" \
  "v_cmp_eq_u32 vcc, %[sent], v204\n\t" \
  "s_or_b64 s[94:95], s[94:95], vcc\n\t" \
  "v_cmp_eq_u32 vcc, %[sent], v205\n\t" \
  "s_or_b64 s[94:95], s[94:95], vcc\n\t" \
  "v_cmp_eq_u32 vcc, %[sent], v206\n\t" \
  "s_or_b64 s[94:95], s[94:95], vcc\n\t" \
  "v_cmp_eq_u32 vcc, %[sent], v207\n\t" \
  "s_or_b64 s[94:95], s[94:95], vcc\n\t" \
  "s_cmp_eq_u64 s[94:95], 0\n\t" \
  "s_cbranch_scc1 .Lok%=\n\t" \
  "v_mov_b64 v[192:193], 0\n\t" \
  "v_mov_b64 v[194:195], 0\n\t" \
  "v_mov_b64 v[196:197], 0\n\t" \
  "v_mov_b64 v[198:199], 0\n\t" \
  "s_add_u32 s92, s92, 1\n\t" \
  "s_cmp_eq_u32 s92, 1\n\t" \
  "s_cbranch_scc1 .Lpoll%=\n\t" \
  "s_sleep 2\n\t" \
  "s_and_b32 s93, s92, 63\n\t" \
  "s_cmp_lg_u32 s93, 0\n\t" \
  "s_cbranch_scc1 .Lpoll%=\n\t" \
  "v_mov_b32 v245, 0\n\t" \
  "buffer_load_dword v244, v245, %[rs], %[sab] offen sc1\n\t" \
  "s_waitcnt vmcnt(0)\n\t" \
  "v_readfirstlane_b32 s93, v244\n\t" \
  "s_nop 3\n\t" \
  "s_cmp_eq_u32 s93, 1\n\t" \
  "s_cbranch_scc1 .Lok%=\n\t" \
  "s_cmp_lt_u32 s92, 0x80000\n\t" \
  "s_cbranch_scc1 .Lpoll%=\n\t" \
  "v_mov_b32 v244, 1\n\t" \
  "buffer_store_dword v244, v245, %[rs], %[sab] offen sc1\n\t" \
  "s_branch .Lok%=\n\t" \
  ".Lend%=:\n\t"
#define QK_ASM_3 \
  "s_waitcnt lgkmcnt(0)\n\t" \
  "v_subrev_u32 v244, 0x8000, v250\n\t" \
  "ds_read_b128 v[216:219], v248 offset:45056\n\t" \
  "ds_read_b128 v[220:223], v248 offset:46080\n\t" \
  "ds_read_b128 v[224:227], v248 offset:47104\n\t" \
  "ds_read_b128 v[228:231], v248 offset:48128\n\t" \
  "ds_read_b128 v[208:211], v244 offset:0\n\t" \
  "ds_read_b128 v[212:215], v244 offset:1024\n\t" \
  "ds_read_b128 v[236:239], v244 offset:2048\n\t" \
  "ds_read_b128 v[240:243], v244 offset:3072\n\t" \
  "v_mov_b64 v[192:193], 0\n\t" \
  "v_mov_b64 v[194:195], 0\n\t" \
  "v_mov_b64 v[196:197], 0\n\t" \
  "v_mov_b64 v[198:199], 0\n\t" \
  "s_movk_i32 s92, 0x2000\n\t" \
  ".Lwt%=:\n\t" \
  "ds_read_b64 v[200:201], v248 offset:30720\n\t" \
  "s_memrealtime s[90:91]\n\t" \
  "s_sub_u32 s92, s92, 1\n\t" \
  "s_waitcnt lgkmcnt(0)\n\t" \
  "v_readfirstlane_b32 s93, v200\n\t" \
  "v_readfirstlane_b32 %[tgt], v201\n\t" \
  "s_cbranch_scc1 .Lgo%=\n\t" \
  "s_cmp_eq_u32 s93, %[step]\n\t" \
  "s_cbranch_scc1 .Lwx%=\n\t" \
  "s_sleep 1\n\t" \
  "s_branch .Lwt%=\n\t" \
  ".Lwx%=:\n\t" \
  "s_lshl_b32 s93, s90, 2\n\t" \
  "s_sub_i32 s93, %[tgt], s93\n\t" \
  "s_cmp_le_i32 s93, 0\n\t" \
  "s_cbranch_scc1 .Lgo%=\n\t" \
  "s_min_i32 s93, s93, 8000\n\t" \
  "s_mov_b32 s94, s93\n\t" \
  ".Lsl%=:\n\t" \
  "s_sub_i32 s94, s94, 18\n\t" \
  "s_cmp_lt_i32 s94, 30\n\t" \
  "s_cbranch_scc1 .Lsx%=\n\t" \
  "s_sleep 1\n\t" \
  "s_branch .Lsl%=\n\t" \
  ".Lsx%=:\n\t" \
  "s_sub_i32 s93, s93, 12\n\t" \
  "s_max_i32 s93, s93, 4\n\t" \
  "s_lshr_b32 s93, s93, 2\n\t" \
  "s_add_u32 s93, s93, s90\n\t" \
  "s_memrealtime s[90:91]\n\t" \
  ".Lpace%=:\n\t" \
  "s_memrealtime s[94:95]\n\t" \
  "s_waitcnt lgkmcnt(1)\n\t" \
  "s_sub_i32 s92, s90, s93\n\t" \
  "s_cmp_ge_i32 s92, 0\n\t" \
  "s_cbranch_scc1 .Le%=\n\t" \
  "s_memrealtime s[90:91]\n\t" \
  "s_waitcnt lgkmcnt(1)\n\t" \
  "s_sub_i32 s92, s94, s93\n\t" \
  "s_cmp_lt_i32 s92, 0\n\t" \
  "s_cbranch_scc1 .Lpace%=\n\t" \
  "s_nop 4\n\t" \
  ".Le%=:\n\t" \
  "s_add_u32 s92, s92, s93\n\t" \
  "s_lshl_b32 s92, s92, 2\n\t" \
  "s_sub_i32 s92, %[tgt], s92\n\t" \
  "s_max_i32 s92, s92, 0\n\t" \
  "s_bitcmp0_b32 s92, 3\n\t" \
  "s_cbranch_scc1 .Lf3%=\n\t" \
  "s_nop 15\n\t" \
  ".Lf3%=:\n\t" \
  "s_bitcmp0_b32 s92, 2\n\t" \
  "s_cbranch_scc1 .Lf2%=\n\t" \
  "s_nop 10\n\t" \
  ".Lf2%=:\n\t" \
  ".Lgo%=:\n\t" \
  "s_mov_b32 s92, 0\n\t" \
  ".Lpoll%=:\n\t" \
  "buffer_load_dwordx4 v[200:203], v248, %[rs], %[som] offen sc1\n\t" \
  "buffer_load_dwordx4 v[204:207], v248, %[rs], %[som2] offen sc1\n\t" \
  "s_waitcnt vmcnt(1) lgkmcnt(0)\n\t" \
  "ds_write_b128 v249, v[200:203]\n\t" \
  "v_smfmac_f32_16x16x64_bf16 v[192:195], v[200:203], v[208:215], v251\n\t" \
  "v_max3_u32 v244, v200, v201, v202\n\t" \
  "v_smfmac_f32_16x16x64_bf16 v[196:199], v[200:203], v[236:243], v251\n\t" \
  "s_waitcnt vmcnt(0)\n\t" \
  "ds_write_b128 v249, v[204:207] offset:8192\n\t" \
  "v_smfmac_f32_16x16x64_bf16 v[192:195], v[204:207], v[216:223], v251\n\t" \
  "v_max3_u32 v244, v244, v203, v204\n\t" \
  "v_max3_u32 v244, v244, v205, v206\n\t" \
  "v_smfmac_f32_16x16x64_bf16 v[196:199], v[204:207], v[224:231], v251\n\t" \
  "v_max_u32 v244, v244, v207\n\t" \
  "v_cmp_le_u32 vcc, %[sent], v244\n\t" \
  "s_cbranch_vccnz .Lslow%=\n\t" \
  ".Lok%=:\n\t" \
  "ds_read_b128 v[216:219], v250 offset:32768\n\t" \
  "ds_read_b128 v[220:223], v250 offset:33792\n\t" \
  "ds_read_b128 v[224:227], v250 offset:34816\n\t" \
  "ds_read_b128 v[228:231], v250 offset:35840\n\t" \
  "s_waitcnt lgkmcnt(4)\n\t" \
  "s_barrier\n\t" \
  "ds_read_b128 v[208:211], v248 offset:2048\n\t" \
  "ds_read_b128 v[212:215], v248 offset:5120\n\t" \
  "ds_read_b128 v[236:239], v248 offset:6144\n\t" \
  "ds_read_b128 v[240:243], v248 offset:7168\n\t" \
  "s_cmp_eq_u32 %[step], 63\n\t" \
  "s_cbranch_scc1 .Lls%=\n\t" \
  "s_waitcnt lgkmcnt(3)\n\t" \
  "v_smfmac_f32_16x16x64_bf16 v[192:195], v[208:211], v[216:223], v251\n\t" \
  "ds_read_b128 v[244:247], v248 offset:8192\n\t" \
  "v_smfmac_f32_16x16x64_bf16 v[196:199], v[208:211], v[224:231], v251\n\t" \
  "s_waitcnt lgkmcnt(3)\n\t" \
  "ds_read_b128 v[200:203], v248 offset:12288\n\t" \
  "v_smfmac_f32_16x16x64_bf16 v[192:195], v[212:215], v[0:7], v251\n\t" \
  "ds_read_b128 v[216:219], v250 offset:0\n\t" \
  "v_smfmac_f32_16x16x64_bf16 v[196:199], v[212:215], v[8:15], v251\n\t" \
  "s_waitcnt lgkmcnt(4)\n\t" \
  "ds_read_b128 v[220:223], v250 offset:1024\n\t" \
  "v_smfmac_f32_16x16x64_bf16 v[192:195], v[236:239], v[16:23], v251\n\t" \
  "ds_read_b128 v[224:227], v250 offset:2048\n\t" \
  "v_smfmac_f32_16x16x64_bf16 v[196:199], v[236:239], v[24:31], v251\n\t" \
  "s_waitcnt lgkmcnt(4)\n\t" \
  "ds_read_b128 v[228:231], v250 offset:3072\n\t" \
  "v_smfmac_f32_16x16x64_bf16 v[192:195], v[240:243], v[32:39], v251\n\t" \
  "ds_read_b128 v[204:207], v248 offset:9216\n\t" \
  "v_smfmac_f32_16x16x64_bf16 v[196:199], v[240:243], v[40:47], v251\n\t" \
  "ds_read_b128 v[208:211], v248 offset:10240\n\t" \
  "v_smfmac_f32_16x16x64_bf16 v[192:195], v[244:247], v[48:55], v251\n\t" \
  "ds_read_b128 v[212:215], v248 offset:13312\n\t" \
  "v_smfmac_f32_16x16x64_bf16 v[196:199], v[244:247], v[56:63], v251\n\t" \
  "s_waitcnt lgkmcnt(5)\n\t" \
  "v_smfmac_f32_16x16x64_bf16 v[192:195], v[200:203], v[216:223], v251\n\t" \
  "s_waitcnt lgkmcnt(3)\n\t" \
  "ds_read_b128 v[236:239], v248 offset:14336\n\t" \
  "v_smfmac_f32_16x16x64_bf16 v[196:199], v[200:203], v[224:231], v251\n\t" \
  "s_waitcnt lgkmcnt(3)\n\t" \
  "v_smfmac_f32_16x16x64_bf16 v[192:195], v[204:207], v[64:71], v251\n\t" \
  "ds_read_b128 v[240:243], v248 offset:15360\n\t" \
  "v_smfmac_f32_16x16x64_bf16 v[196:199], v[204:207], v[72:79], v251\n\t" \
  "s_waitcnt lgkmcnt(2)\n\t" \
  "v_smfmac_f32_16x16x64_bf16 v[192:195], v[208:211], v[80:87], v251\n\t" \
  "ds_read_b128 v[244:247], v248 offset:0\n\t" \
  "v_smfmac_f32_16x16x64_bf16 v[196:199], v[208:211], v[88:95], v251\n\t" \
  "ds_read_b128 v[200:203], v248 offset:1024\n\t" \
  "v_smfmac_f32_16x16x64_bf16 v[192:195], v[212:215], v[96:103], v251\n\t" \
  "ds_read_b128 v[204:207], v248 offset:4096\n\t" \
  "v_smfmac_f32_16x16x64_bf16 v[196:199], v[212:215], v[104:111], v251\n\t" \
  "s_waitcnt lgkmcnt(3)\n\t" \
  "v_smfmac_f32_16x16x64_bf16 v[192:195], v[236:239], v[112:119], v251\n\t" \
  "v_smfmac_f32_16x16x64_bf16 v[196:199], v[236:239], v[120:127], v251\n\t" \
  "v_smfmac_f32_16x16x64_bf16 v[192:195], v[240:243], v[128:135], v251\n\t" \
  "v_smfmac_f32_16x16x64_bf16 v[196:199], v[240:243], v[136:143], v251\n\t" \
  "s_waitcnt lgkmcnt(0)\n\t" \
  "v_smfmac_f32_16x16x64_bf16 v[192:195], v[244:247], v[144:151], v251\n\t" \
  "v_smfmac_f32_16x16x64_bf16 v[196:199], v[244:247], v[152:159], v251\n\t" \
  "v_smfmac_f32_16x16x64_bf16 v[192:195], v[200:203], v[160:167], v251\n\t" \
  "v_smfmac_f32_16x16x64_bf16 v[196:199], v[200:203], v[168:175], v251\n\t" \
  "v_smfmac_f32_16x16x64_bf16 v[192:195], v[204:207], v[176:183], v251\n\t" \
  "v_smfmac_f32_16x16x64_bf16 v[196:199], v[204:207], v[184:191], v251\n\t" \
  "ds_read_b128 v[200:203], v248 offset:19456\n\t" \
  "ds_read_b128 v[204:207], v248 offset:23552\n\t" \
  "v_mov_b32 v250, v232\n\t" \
  "v_mov_b32 v249, v233\n\t" \
  "s_nop 2\n\t" \
  "v_add_f32 v192, v192, v194\n\t" \
  "v_add_f32 v193, v193, v195\n\t" \
  "v_add_f32 v196, v196, v198\n\t" \
  "v_add_f32 v197, v197, v199\n\t" \
  "v_cvt_pk_bf16_f32 v192, v192, v193\n\t" \
  "v_cvt_pk_bf16_f32 v196, v196, v197\n\t" \
  "ds_write2_b32 v252, v192, v196 offset1:64\n\t" \
  "v_mov_b32 v251, v234\n\t" \
  "ds_read_b64_tr_b16 v[244:245], v250 offset:1536\n\t" \
  "ds_read_b64_tr_b16 v[246:247], v250 offset:1600\n\t" \
  "v_mov_b32 v212, %[step]\n\t" \
  "s_mov_b64 exec, 1\n\t" \
  "ds_write_b32 v248, v212 offset:28720\n\t" \
  "s_mov_b64 exec, -1\n\t" \
  "buffer_load_dwordx4 v[192:195], v248, %[rt], %[svt] offen offset:0 sc1\n\t" \
  "buffer_load_dwordx4 v[196:199], v248, %[rt], %[svt] offen offset:1024 sc1\n\t" \
  "s_waitcnt lgkmcnt(1)\n\t" \
  "v_mfma_f32_16x16x32_bf16 v[200:203], v[200:203], v[244:247], 0\n\t" \
  "v_mfma_f32_16x16x32_bf16 v[204:207], v[204:207], v[244:247], 0\n\t" \
  "s_nop 7\n\t" \
  "v_cndmask_b32 v216, v200, v204, %[nh]\n\t" \
  "v_cndmask_b32 v217, v201, v205, %[nh]\n\t" \
  "v_cndmask_b32 v218, v202, v206, %[nh]\n\t" \
  "v_cndmask_b32 v219, v203, v207, %[nh]\n\t" \
  "ds_write_b128 v248, v[216:219] offset:19456\n\t" \
  "buffer_load_dword v252, v249, %[rs], %[sv] offen offset:0 sc1\n\t" \
  "buffer_load_dword v253, v249, %[rs], %[sv] offen offset:256 sc1\n\t" \
  "ds_read_b32 v212, v248 offset:28672\n\t" \
  "ds_read_b64_tr_b16 v[232:233], v250 offset:0\n\t" \
  "ds_read_b64_tr_b16 v[234:235], v250 offset:64\n\t" \
  "ds_read_b64_tr_b16 v[236:237], v250 offset:512\n\t" \
  "ds_read_b64_tr_b16 v[238:239], v250 offset:576\n\t" \
  "ds_read_b64_tr_b16 v[240:241], v250 offset:1024\n\t" \
  "ds_read_b64_tr_b16 v[242:243], v250 offset:1088\n\t" \
  "s_setprio 3\n\t" \
  "s_waitcnt lgkmcnt(0)\n\t" \
  "v_cmp_ne_u32 vcc, %[step], v212\n\t" \
  "s_and_b32 s93, vcc_lo, 0xf\n\t" \
  "s_barrier\n\t" \
  "s_cbranch_scc1 .Lre%=\n\t" \
  ".Lrb%=:\n\t" \
  "ds_read_b64_tr_b16 v[212:213], v250 offset:3584\n\t" \
  "ds_read_b64_tr_b16 v[214:215], v250 offset:3648\n\t" \
  "ds_read_b64_tr_b16 v[200:201], v250 offset:2048\n\t" \
  "ds_read_b64_tr_b16 v[202:203], v250 offset:2112\n\t" \
  "ds_read_b64_tr_b16 v[204:205], v250 offset:2560\n\t" \
  "ds_read_b64_tr_b16 v[206:207], v250 offset:2624\n\t" \
  "ds_read_b64_tr_b16 v[208:209], v250 offset:3072\n\t" \
  "ds_read_b64_tr_b16 v[210:211], v250 offset:3136\n\t" \
  "ds_read_b128 v[220:223], v248 offset:16384\n\t" \
  "ds_read_b128 v[224:227], v248 offset:17408\n\t" \
  "ds_read_b128 v[228:231], v248 offset:18432\n\t" \
  "s_waitcnt lgkmcnt(9)\n\t" \
  "v_mfma_f32_16x16x32_bf16 v[212:215], v[212:215], v[244:247], 0\n\t" \
  "s_waitcnt lgkmcnt(7)\n\t" \
  "v_mfma_f32_16x16x32_bf16 v[212:215], v[200:203], v[232:235], v[212:215]\n\t" \
  "s_waitcnt lgkmcnt(5)\n\t" \
  "v_mfma_f32_16x16x32_bf16 v[212:215], v[204:207], v[236:239], v[212:215]\n\t" \
  "s_waitcnt lgkmcnt(3)\n\t" \
  "v_mfma_f32_16x16x32_bf16 v[212:215], v[208:211], v[240:243], v[212:215]\n\t" \
  "s_waitcnt lgkmcnt(2)\n\t" \
  "v_pk_add_f32 v[216:217], v[216:217], v[220:221]\n\t" \
  "v_pk_add_f32 v[218:219], v[218:219], v[222:223]\n\t" \
  "s_waitcnt lgkmcnt(0)\n\t" \
  "v_pk_add_f32 v[224:225], v[224:225], v[228:229]\n\t" \
  "v_pk_add_f32 v[226:227], v[226:227], v[230:231]\n\t" \
  "v_pk_add_f32 v[216:217], v[216:217], v[224:225]\n\t" \
  "v_pk_add_f32 v[218:219], v[218:219], v[226:227]\n\t" \
  "v_max3_f32 v224, v216, v217, v218\n\t" \
  "v_max_f32 v224, v224, v219\n\t" \
  "s_nop 1\n\t" \
  "v_max_f32_dpp v225, v224, v224 row_ror:8 row_mask:0xf bank_mask:0xf\n\t" \
  "v_max_f32_dpp v226, v224, v224 row_ror:8 row_mask:0xf bank_mask:0xf\n\t" \
  "s_nop 1\n\t" \
  "v_permlane32_swap_b32 v225, v226\n\t" \
  "v_max_f32 v231, v225, v226\n\t" \
  "v_mov_b32 v220, 0x3f803f80\n\t" \
  "v_mov_b32 v221, 0x3f803f80\n\t" \
  "v_mov_b64 v[222:223], v[220:221]\n\t" \
  "v_mov_b32 v245, 0\n\t" \
  "v_mov_b32 v246, 0\n\t" \
  "v_max3_f32 v200, v212, v213, v231\n\t" \
  "v_max3_f32 v201, v200, v214, v215\n\t" \
  "v_max3_f32 v202, v200, v214, v215\n\t" \
  "v_cndmask_b32 v212, v212, v214, %[up]\n\t" \
  "v_cndmask_b32 v213, v213, v215, %[up]\n\t" \
  "v_permlane16_swap_b32 v201, v202\n\t" \
  "v_max_f32 v200, v201, v202\n\t" \
  "v_mul_f32 v200, %[negc], v200\n\t" \
  "v_fma_f32 v212, v212, %[c], v200\n\t" \
  "v_fma_f32 v213, v213, %[c], v200\n\t" \
  "v_exp_f32 v212, v212\n\t" \
  "v_fma_f32 v216, v216, %[c], v200\n\t" \
  "v_exp_f32 v213, v213\n\t" \
  "v_fma_f32 v217, v217, %[c], v200\n\t" \
  "v_exp_f32 v216, v216\n\t" \
  "v_fma_f32 v218, v218, %[c], v200\n\t" \
  "v_cvt_pk_bf16_f32 v244, v212, v213\n\t" \
  "v_cvt_pk_bf16_f32 v247, v212, v213\n\t" \
  "v_exp_f32 v217, v217\n\t" \
  "v_fma_f32 v219, v219, %[c], v200\n\t" \
  "v_mfma_f32_16x16x16_bf16 v[208:211], v[220:221], v[244:245], 0\n\t" \
  "v_exp_f32 v218, v218\n\t" \
  "s_waitcnt vmcnt(0)\n\t" \
  "v_mfma_f32_16x16x16_bf16 v[224:227], v[252:253], v[244:245], 0\n\t" \
  "v_cvt_pk_bf16_f32 v206, v216, v217\n\t" \
  "v_cvt_pk_bf16_f32 v204, v216, v217\n\t" \
  "v_exp_f32 v219, v219\n\t" \
  "v_mfma_f32_16x16x16_bf16 v[228:231], v[252:253], v[246:247], 0\n\t" \
  "v_cvt_pk_bf16_f32 v207, v218, v219\n\t" \
  "v_cvt_pk_bf16_f32 v205, v218, v219\n\t" \
  "v_mov_b32_dpp v204, v206 row_ror:8 row_mask:0xf bank_mask:0xc\n\t" \
  "v_mov_b32_dpp v206, v206 row_ror:8 row_mask:0xf bank_mask:0x3\n\t" \
  "v_mov_b32_dpp v205, v207 row_ror:8 row_mask:0xf bank_mask:0xc\n\t" \
  "v_mov_b32_dpp v207, v207 row_ror:8 row_mask:0xf bank_mask:0x3\n\t" \
  "v_max_u32 v200, v252, v253\n\t" \
  "v_cmp_le_u32 vcc, %[sent], v200\n\t" \
  "v_mfma_f32_16x16x32_bf16 v[208:211], v[220:223], v[204:207], v[208:211]\n\t" \
  ".Lpv%=:\n\t" \
  "v_mfma_f32_16x16x32_bf16 v[224:227], v[192:195], v[204:207], v[224:227]\n\t" \
  "v_mfma_f32_16x16x32_bf16 v[228:231], v[196:199], v[204:207], v[228:231]\n\t" \
  "s_cbranch_vccnz .Lvs%=\n\t" \
  ".Lvok%=:\n\t" \
  "s_nop 4\n\t" \
  "v_rcp_f32 v200, v208\n\t" \
  "v_cndmask_b32 v224, v224, v226, %[nh]\n\t" \
  "v_cndmask_b32 v225, v225, v227, %[nh]\n\t" \
  "v_cndmask_b32 v228, v228, v230, %[nh]\n\t" \
  "v_cndmask_b32 v229, v229, v231, %[nh]\n\t" \
  "v_pk_mul_f32 v[224:225], v[224:225], v[200:201] op_sel_hi:[1,0]\n\t" \
  "v_pk_mul_f32 v[228:229], v[228:229], v[200:201] op_sel_hi:[1,0]\n\t" \
  "v_cvt_pk_bf16_f32 v224, v224, v225\n\t" \
  "v_cvt_pk_bf16_f32 v225, v228, v229\n\t" \
  "buffer_store_dwordx2 v[224:225], v251, %[rs], %[so] offen sc1\n\t" \
  "s_memtime s[90:91]\n\t" \
  "s_setprio 0\n\t" \
  "s_waitcnt lgkmcnt(0)\n\t" \
  "s_mov_b32 %[tout], s90\n\t" \
  "s_mov_b32 %[spins], s92\n\t" \
  "s_branch .Lend%=\n\t" \
  ".Lls%=:\n\t" \
  "s_waitcnt lgkmcnt(0)\n\t" \
  "s_barrier\n\t" \
  "s_mov_b32 %[spins], 0\n\t" \
  "s_branch .Lend%=\n\t" \
  ".Lre%=:\n\t" \
  "ds_read_b64_tr_b16 v[232:233], v250 offset:0\n\t" \
  "ds_read_b64_tr_b16 v[234:235], v250 offset:64\n\t" \
  "ds_read_b64_tr_b16 v[236:237], v250 offset:512\n\t" \
  "ds_read_b64_tr_b16 v[238:239], v250 offset:576\n\t" \
  "ds_read_b64_tr_b16 v[240:241], v250 offset:1024\n\t" \
  "ds_read_b64_tr_b16 v[242:243], v250 offset:1088\n\t" \
  "s_waitcnt lgkmcnt(0)\n\t" \
  "s_branch .Lrb%=\n\t" \
  ".Lvs%=:\n\t" \
  "v_cmp_eq_u32 vcc, %[sent], v252\n\t" \
  "v_cmp_eq_u32 s[94:95], %[sent], v253\n\t" \
  "s_or_b64 s[94:95], s[94:95], vcc\n\t" \
  "s_cbranch_scc0 .Lvok%=\n\t" \
  "s_add_u32 s92, s92, 1\n\t" \
  "s_sleep 2\n\t" \
  "s_and_b32 s93, s92, 63\n\t" \
  "s_cbranch_scc1 .Lvr%=\n\t" \
  "buffer_load_dword v200, off, %[rs], %[sab] sc1\n\t" \
  "s_waitcnt vmcnt(0)\n\t" \
  "v_readfirstlane_b32 s93, v200\n\t" \
  "s_nop 3\n\t" \
  "s_cmp_eq_u32 s93, 1\n\t" \
  "s_cbranch_scc1 .Lvok%=\n\t" \
  "s_cmp_lt_u32 s92, 0x80000\n\t" \
  "s_cbranch_scc1 .Lvr%=\n\t" \
  "v_mov_b32 v200, 1\n\t" \
  "buffer_store_dword v200, off, %[rs], %[sab] sc1\n\t" \
  "s_branch .Lvok%=\n\t" \
  ".Lvr%=:\n\t" \
  "buffer_load_dword v252, v249, %[rs], %[sv] offen offset:0 sc1\n\t" \
  "buffer_load_dword v253, v249, %[rs], %[sv] offen offset:256 sc1\n\t" \
  "s_waitcnt vmcnt(0)\n\t" \
  "v_mfma_f32_16x16x16_bf16 v[224:227], v[252:253], v[244:245], 0\n\t" \
  "v_mfma_f32_16x16x16_bf16 v[228:231], v[252:253], v[246:247], 0\n\t" \
  "v_max_u32 v200, v252, v253\n\t" \
  "v_cmp_le_u32 vcc, %[sent], v200\n\t" \
  "s_nop 4\n\t" \
  "s_branch .Lpv%=\n\t" \
  "s_branch .Lend%=\n\t" \
  ".Lslow%=:\n\t" \
  "s_mov_b64 s[94:95], 0\n\t" \
  "v_cmp_eq_u32 vcc, %[sent], v200\n\t" \
  "s_or_b64 s[94:95], s[94:95], vcc\n\t" \
  "v_cmp_eq_u32 vcc, %[sent], v201\n\t" \
  "s_or_b64 s[94:95], s[94:95], vcc\n\t" \
  "v_cmp_eq_u32 vcc, %[sent], v202\n\t" \
  "s_or_b64 s[94:95], s[94:95], vcc\n\t" \
  "v_cmp_eq_u32 vcc, %[sent], v203\n\t" \
  "s_or_b64 s[94:95], s[94:95], vcc\n\t" \
  "v_cmp_eq_u32 vcc, %[sent], v204\n\t" \
  "s_or_b64 s[94:95], s[94:95], vcc\n\t" \
  "v_cmp_eq_u32 vcc, %[sent], v205\n\t" \
  "s_or_b64 s[94:95], s[94:95], vcc\n\t" \
  "v_cmp_eq_u32 vcc, %[sent], v206\n\t" \
  "s_or_b64 s[94:95], s[94:95], vcc\n\t" \
  "v_cmp_eq_u32 vcc, %[sent], v207\n\t" \
  "s_or_b64 s[94:95], s[94:95], vcc\n\t" \
  "s_cmp_eq_u64 s[94:95], 0\n\t" \
  "s_cbranch_scc1 .Lok%=\n\t" \
  "v_mov_b64 v[192:193], 0\n\t" \
  "v_mov_b64 v[194:195], 0\n\t" \
  "v_mov_b64 v[196:197], 0\n\t" \
  "v_mov_b64 v[198:199], 0\n\t" \
  "s_add_u32 s92, s92, 1\n\t" \
  "s_cmp_eq_u32 s92, 1\n\t" \
  "s_cbranch_scc1 .Lpoll%=\n\t" \
  "s_sleep 2\n\t" \
  "s_and_b32 s93, s92, 63\n\t" \
  "s_cmp_lg_u32 s93, 0\n\t" \
  "s_cbranch_scc1 .Lpoll%=\n\t" \
  "v_mov_b32 v245, 0\n\t" \
  "buffer_load_dword v244, v245, %[rs], %[sab] offen sc1\n\t" \
  "s_waitcnt vmcnt(0)\n\t" \
  "v_readfirstlane_b32 s93, v244\n\t" \
  "s_nop 3\n\t" \
  "s_cmp_eq_u32 s93, 1\n\t" \
  "s_cbranch_scc1 .Lok%=\n\t" \
  "s_cmp_lt_u32 s92, 0x80000\n\t" \
  "s_cbranch_scc1 .Lpoll%=\n\t" \
  "v_mov_b32 v244, 1\n\t" \
  "buffer_store_dword v244, v245, %[rs], %[sab] offen sc1\n\t" \
  "s_branch .Lok%=\n\t" \
  ".Lend%=:\n\t"
#define QK_ASM_4 \
  "s_waitcnt lgkmcnt(0)\n\t" \
  "v_subrev_u32 v244, 0x8000, v250\n\t" \
  "ds_read_b128 v[216:219], v248 offset:49152\n\t" \
  "ds_read_b128 v[220:223], v248 offset:50176\n\t" \
  "ds_read_b128 v[224:227], v248 offset:51200\n\t" \
  "ds_read_b128 v[228:231], v248 offset:52224\n\t" \
  "ds_read_b128 v[208:211], v244 offset:0\n\t" \
  "ds_read_b128 v[212:215], v244 offset:1024\n\t" \
  "ds_read_b128 v[236:239], v244 offset:2048\n\t" \
  "ds_read_b128 v[240:243], v244 offset:3072\n\t" \
  "v_mov_b64 v[192:193], 0\n\t" \
  "v_mov_b64 v[194:195], 0\n\t" \
  "v_mov_b64 v[196:197], 0\n\t" \
  "v_mov_b64 v[198:199], 0\n\t" \
  "s_movk_i32 s92, 0x2000\n\t" \
  ".Lwt%=:\n\t" \
  "ds_read_b64 v[200:201], v248 offset:30720\n\t" \
  "s_memrealtime s[90:91]\n\t" \
  "s_sub_u32 s92, s92, 1\n\t" \
  "s_waitcnt lgkmcnt(0)\n\t" \
  "v_readfirstlane_b32 s93, v200\n\t" \
  "v_readfirstlane_b32 %[tgt], v201\n\t" \
  "s_cbranch_scc1 .Lgo%=\n\t" \
  "s_cmp_eq_u32 s93, %[step]\n\t" \
  "s_cbranch_scc1 .Lwx%=\n\t" \
  "s_sleep 1\n\t" \
  "s_branch .Lwt%=\n\t" \
  ".Lwx%=:\n\t" \
  "s_lshl_b32 s93, s90, 2\n\t" \
  "s_sub_i32 s93, %[tgt], s93\n\t" \
  "s_cmp_le_i32 s93, 0\n\t" \
  "s_cbranch_scc1 .Lgo%=\n\t" \
  "s_min_i32 s93, s93, 8000\n\t" \
  "s_mov_b32 s94, s93\n\t" \
  ".Lsl%=:\n\t" \
  "s_sub_i32 s94, s94, 18\n\t" \
  "s_cmp_lt_i32 s94, 30\n\t" \
  "s_cbranch_scc1 .Lsx%=\n\t" \
  "s_sleep 1\n\t" \
  "s_branch .Lsl%=\n\t" \
  ".Lsx%=:\n\t" \
  "s_sub_i32 s93, s93, 12\n\t" \
  "s_max_i32 s93, s93, 4\n\t" \
  "s_lshr_b32 s93, s93, 2\n\t" \
  "s_add_u32 s93, s93, s90\n\t" \
  "s_memrealtime s[90:91]\n\t" \
  ".Lpace%=:\n\t" \
  "s_memrealtime s[94:95]\n\t" \
  "s_waitcnt lgkmcnt(1)\n\t" \
  "s_sub_i32 s92, s90, s93\n\t" \
  "s_cmp_ge_i32 s92, 0\n\t" \
  "s_cbranch_scc1 .Le%=\n\t" \
  "s_memrealtime s[90:91]\n\t" \
  "s_waitcnt lgkmcnt(1)\n\t" \
  "s_sub_i32 s92, s94, s93\n\t" \
  "s_cmp_lt_i32 s92, 0\n\t" \
  "s_cbranch_scc1 .Lpace%=\n\t" \
  "s_nop 4\n\t" \
  ".Le%=:\n\t" \
  "s_add_u32 s92, s92, s93\n\t" \
  "s_lshl_b32 s92, s92, 2\n\t" \
  "s_sub_i32 s92, %[tgt], s92\n\t" \
  "s_max_i32 s92, s92, 0\n\t" \
  "s_bitcmp0_b32 s92, 3\n\t" \
  "s_cbranch_scc1 .Lf3%=\n\t" \
  "s_nop 15\n\t" \
  ".Lf3%=:\n\t" \
  "s_bitcmp0_b32 s92, 2\n\t" \
  "s_cbranch_scc1 .Lf2%=\n\t" \
  "s_nop 10\n\t" \
  ".Lf2%=:\n\t" \
  ".Lgo%=:\n\t" \
  "s_mov_b32 s92, 0\n\t" \
  ".Lpoll%=:\n\t" \
  "buffer_load_dwordx4 v[200:203], v248, %[rs], %[som] offen sc1\n\t" \
  "buffer_load_dwordx4 v[204:207], v248, %[rs], %[som2] offen sc1\n\t" \
  "s_waitcnt vmcnt(1) lgkmcnt(0)\n\t" \
  "ds_write_b128 v249, v[200:203]\n\t" \
  "v_smfmac_f32_16x16x64_bf16 v[192:195], v[200:203], v[208:215], v251\n\t" \
  "v_max3_u32 v244, v200, v201, v202\n\t" \
  "v_smfmac_f32_16x16x64_bf16 v[196:199], v[200:203], v[236:243], v251\n\t" \
  "s_waitcnt vmcnt(0)\n\t" \
  "ds_write_b128 v249, v[204:207] offset:8192\n\t" \
  "v_smfmac_f32_16x16x64_bf16 v[192:195], v[204:207], v[216:223], v251\n\t" \
  "v_max3_u32 v244, v244, v203, v204\n\t" \
  "v_max3_u32 v244, v244, v205, v206\n\t" \
  "v_smfmac_f32_16x16x64_bf16 v[196:199], v[204:207], v[224:231], v251\n\t" \
  "v_max_u32 v244, v244, v207\n\t" \
  "v_cmp_le_u32 vcc, %[sent], v244\n\t" \
  "s_cbranch_vccnz .Lslow%=\n\t" \
  ".Lok%=:\n\t" \
  "s_waitcnt lgkmcnt(0)\n\t" \
  "s_barrier\n\t" \
  "ds_read_b128 v[208:211], v248 offset:6144\n\t" \
  "ds_read_b128 v[212:215], v248 offset:7168\n\t" \
  "ds_read_b128 v[236:239], v248 offset:8192\n\t" \
  "ds_read_b128 v[240:243], v248 offset:9216\n\t" \
  "ds_read_b128 v[244:247], v248 offset:10240\n\t" \
  "s_waitcnt lgkmcnt(4)\n\t" \
  "ds_read_b128 v[200:203], v248 offset:5120\n\t" \
  "v_smfmac_f32_16x16x64_bf16 v[192:195], v[208:211], v[0:7], v251\n\t" \
  "ds_read_b128 v[216:219], v250 offset:32768\n\t" \
  "v_smfmac_f32_16x16x64_bf16 v[196:199], v[208:211], v[8:15], v251\n\t" \
  "s_waitcnt lgkmcnt(5)\n\t" \
  "ds_read_b128 v[220:223], v250 offset:33792\n\t" \
  "v_smfmac_f32_16x16x64_bf16 v[192:195], v[212:215], v[16:23], v251\n\t" \
  "ds_read_b128 v[224:227], v250 offset:34816\n\t" \
  "v_smfmac_f32_16x16x64_bf16 v[196:199], v[212:215], v[24:31], v251\n\t" \
  "s_waitcnt lgkmcnt(6)\n\t" \
  "ds_read_b128 v[228:231], v250 offset:35840\n\t" \
  "v_smfmac_f32_16x16x64_bf16 v[192:195], v[236:239], v[32:39], v251\n\t" \
  "ds_read_b128 v[204:207], v248 offset:11264\n\t" \
  "v_smfmac_f32_16x16x64_bf16 v[196:199], v[236:239], v[40:47], v251\n\t" \
  "s_waitcnt lgkmcnt(7)\n\t" \
  "ds_read_b128 v[208:211], v248 offset:14336\n\t" \
  "v_smfmac_f32_16x16x64_bf16 v[192:195], v[240:243], v[48:55], v251\n\t" \
  "ds_read_b128 v[212:215], v248 offset:15360\n\t" \
  "v_smfmac_f32_16x16x64_bf16 v[196:199], v[240:243], v[56:63], v251\n\t" \
  "s_waitcnt lgkmcnt(7)\n\t" \
  "v_smfmac_f32_16x16x64_bf16 v[192:195], v[244:247], v[64:71], v251\n\t" \
  "ds_read_b128 v[236:239], v248 offset:0\n\t" \
  "v_smfmac_f32_16x16x64_bf16 v[196:199], v[244:247], v[72:79], v251\n\t" \
  "s_waitcnt lgkmcnt(6)\n\t" \
  "v_smfmac_f32_16x16x64_bf16 v[192:195], v[200:203], v[216:223], v251\n\t" \
  "s_waitcnt lgkmcnt(4)\n\t" \
  "ds_read_b128 v[240:243], v248 offset:13312\n\t" \
  "v_smfmac_f32_16x16x64_bf16 v[196:199], v[200:203], v[224:231], v251\n\t" \
  "s_waitcnt lgkmcnt(4)\n\t" \
  "ds_read_b128 v[216:219], v250 offset:0\n\t" \
  "v_smfmac_f32_16x16x64_bf16 v[192:195], v[204:207], v[80:87], v251\n\t" \
  "ds_read_b128 v[220:223], v250 offset:1024\n\t" \
  "v_smfmac_f32_16x16x64_bf16 v[196:199], v[204:207], v[88:95], v251\n\t" \
  "s_waitcnt lgkmcnt(4)\n\t" \
  "ds_read_b128 v[224:227], v250 offset:2048\n\t" \
  "v_smfmac_f32_16x16x64_bf16 v[192:195], v[208:211], v[96:103], v251\n\t" \
  "ds_read_b128 v[228:231], v250 offset:3072\n\t" \
  "v_smfmac_f32_16x16x64_bf16 v[196:199], v[208:211], v[104:111], v251\n\t" \
  "ds_read_b128 v[244:247], v248 offset:1024\n\t" \
  "v_smfmac_f32_16x16x64_bf16 v[192:195], v[212:215], v[112:119], v251\n\t" \
  "ds_read_b128 v[200:203], v248 offset:2048\n\t" \
  "v_smfmac_f32_16x16x64_bf16 v[196:199], v[212:215], v[120:127], v251\n\t" \
  "s_waitcnt lgkmcnt(6)\n\t" \
  "v_smfmac_f32_16x16x64_bf16 v[192:195], v[236:239], v[128:135], v251\n\t" \
  "ds_read_b128 v[204:207], v248 offset:3072\n\t" \
  "v_smfmac_f32_16x16x64_bf16 v[196:199], v[236:239], v[136:143], v251\n\t" \
  "s_waitcnt lgkmcnt(5)\n\t" \
  "v_smfmac_f32_16x16x64_bf16 v[192:195], v[240:243], v[216:223], v251\n\t" \
  "s_waitcnt lgkmcnt(3)\n\t" \
  "v_smfmac_f32_16x16x64_bf16 v[196:199], v[240:243], v[224:231], v251\n\t" \
  "s_waitcnt lgkmcnt(2)\n\t" \
  "v_smfmac_f32_16x16x64_bf16 v[192:195], v[244:247], v[144:151], v251\n\t" \
  "v_smfmac_f32_16x16x64_bf16 v[196:199], v[244:247], v[152:159], v251\n\t" \
  "s_waitcnt lgkmcnt(1)\n\t" \
  "v_smfmac_f32_16x16x64_bf16 v[192:195], v[200:203], v[160:167], v251\n\t" \
  "v_smfmac_f32_16x16x64_bf16 v[196:199], v[200:203], v[168:175], v251\n\t" \
  "s_waitcnt lgkmcnt(0)\n\t" \
  "v_smfmac_f32_16x16x64_bf16 v[192:195], v[204:207], v[176:183], v251\n\t" \
  "v_smfmac_f32_16x16x64_bf16 v[196:199], v[204:207], v[184:191], v251\n\t" \
  "s_nop 6\n\t" \
  "v_pk_add_f32 v[192:193], v[192:193], v[194:195]\n\t" \
  "v_pk_add_f32 v[196:197], v[196:197], v[198:199]\n\t" \
  "v_cvt_pk_bf16_f32 v192, v192, v193\n\t" \
  "v_cvt_pk_bf16_f32 v196, v196, v197\n\t" \
  "ds_write2_b32 v252, v192, v196 offset1:64\n\t" \
  "s_waitcnt lgkmcnt(0)\n\t" \
  "s_barrier\n\t" \
  "buffer_store_dword v192, v235, %[rs], %[sk] offen sc1\n\t" \
  "buffer_store_dword v196, v235, %[rs], %[sk] offen offset:256 sc1\n\t" \
  "s_mov_b32 %[spins], s92\n\t" \
  "s_branch .Lend%=\n\t" \
  ".Lslow%=:\n\t" \
  "s_mov_b64 s[94:95], 0\n\t" \
  "v_cmp_eq_u32 vcc, %[sent], v200\n\t" \
  "s_or_b64 s[94:95], s[94:95], vcc\n\t" \
  "v_cmp_eq_u32 vcc, %[sent], v201\n\t" \
  "s_or_b64 s[94:95], s[94:95], vcc\n\t" \
  "v_cmp_eq_u32 vcc, %[sent], v202\n\t" \
  "s_or_b64 s[94:95], s[94:95], vcc\n\t" \
  "v_cmp_eq_u32 vcc, %[sent], v203\n\t" \
  "s_or_b64 s[94:95], s[94:95], vcc\n\t" \
  "v_cmp_eq_u32 vcc, %[sent], v204\n\t" \
  "s_or_b64 s[94:95], s[94:95], vcc\n\t" \
  "v_cmp_eq_u32 vcc, %[sent], v205\n\t" \
  "s_or_b64 s[94:95], s[94:95], vcc\n\t" \
  "v_cmp_eq_u32 vcc, %[sent], v206\n\t" \
  "s_or_b64 s[94:95], s[94:95], vcc\n\t" \
  "v_cmp_eq_u32 vcc, %[sent], v207\n\t" \
  "s_or_b64 s[94:95], s[94:95], vcc\n\t" \
  "s_cmp_eq_u64 s[94:95], 0\n\t" \
  "s_cbranch_scc1 .Lok%=\n\t" \
  "v_mov_b64 v[192:193], 0\n\t" \
  "v_mov_b64 v[194:195], 0\n\t" \
  "v_mov_b64 v[196:197], 0\n\t" \
  "v_mov_b64 v[198:199], 0\n\t" \
  "s_add_u32 s92, s92, 1\n\t" \
  "s_cmp_eq_u32 s92, 1\n\t" \
  "s_cbranch_scc1 .Lpoll%=\n\t" \
  "s_sleep 2\n\t" \
  "s_and_b32 s93, s92, 63\n\t" \
  "s_cmp_lg_u32 s93, 0\n\t" \
  "s_cbranch_scc1 .Lpoll%=\n\t" \
  "v_mov_b32 v245, 0\n\t" \
  "buffer_load_dword v244, v245, %[rs], %[sab] offen sc1\n\t" \
  "s_waitcnt vmcnt(0)\n\t" \
  "v_readfirstlane_b32 s93, v244\n\t" \
  "s_nop 3\n\t" \
  "s_cmp_eq_u32 s93, 1\n\t" \
  "s_cbranch_scc1 .Lok%=\n\t" \
  "s_cmp_lt_u32 s92, 0x80000\n\t" \
  "s_cbranch_scc1 .Lpoll%=\n\t" \
  "v_mov_b32 v244, 1\n\t" \
  "buffer_store_dword v244, v245, %[rs], %[sab] offen sc1\n\t" \
  "s_branch .Lok%=\n\t" \
  ".Lend%=:\n\t"
#define QK_ASM_5 \
  "s_waitcnt lgkmcnt(0)\n\t" \
  "v_subrev_u32 v244, 0x8000, v250\n\t" \
  "ds_read_b128 v[216:219], v248 offset:53248\n\t" \
  "ds_read_b128 v[220:223], v248 offset:54272\n\t" \
  "ds_read_b128 v[224:227], v248 offset:55296\n\t" \
  "ds_read_b128 v[228:231], v248 offset:56320\n\t" \
  "ds_read_b128 v[208:211], v244 offset:0\n\t" \
  "ds_read_b128 v[212:215], v244 offset:1024\n\t" \
  "ds_read_b128 v[236:239], v244 offset:2048\n\t" \
  "ds_read_b128 v[240:243], v244 offset:3072\n\t" \
  "v_mov_b64 v[192:193], 0\n\t" \
  "v_mov_b64 v[194:195], 0\n\t" \
  "v_mov_b64 v[196:197], 0\n\t" \
  "v_mov_b64 v[198:199], 0\n\t" \
  "s_movk_i32 s92, 0x2000\n\t" \
  ".Lwt%=:\n\t" \
  "ds_read_b64 v[200:201], v248 offset:30720\n\t" \
  "s_memrealtime s[90:91]\n\t" \
  "s_sub_u32 s92, s92, 1\n\t" \
  "s_waitcnt lgkmcnt(0)\n\t" \
  "v_readfirstlane_b32 s93, v200\n\t" \
  "v_readfirstlane_b32 %[tgt], v201\n\t" \
  "s_cbranch_scc1 .Lgo%=\n\t" \
  "s_cmp_eq_u32 s93, %[step]\n\t" \
  "s_cbranch_scc1 .Lwx%=\n\t" \
  "s_sleep 1\n\t" \
  "s_branch .Lwt%=\n\t" \
  ".Lwx%=:\n\t" \
  "s_lshl_b32 s93, s90, 2\n\t" \
  "s_sub_i32 s93, %[tgt], s93\n\t" \
  "s_cmp_le_i32 s93, 0\n\t" \
  "s_cbranch_scc1 .Lgo%=\n\t" \
  "s_min_i32 s93, s93, 8000\n\t" \
  "s_mov_b32 s94, s93\n\t" \
  ".Lsl%=:\n\t" \
  "s_sub_i32 s94, s94, 18\n\t" \
  "s_cmp_lt_i32 s94, 30\n\t" \
  "s_cbranch_scc1 .Lsx%=\n\t" \
  "s_sleep 1\n\t" \
  "s_branch .Lsl%=\n\t" \
  ".Lsx%=:\n\t" \
  "s_sub_i32 s93, s93, 12\n\t" \
  "s_max_i32 s93, s93, 4\n\t" \
  "s_lshr_b32 s93, s93, 2\n\t" \
  "s_add_u32 s93, s93, s90\n\t" \
  "s_memrealtime s[90:91]\n\t" \
  ".Lpace%=:\n\t" \
  "s_memrealtime s[94:95]\n\t" \
  "s_waitcnt lgkmcnt(1)\n\t" \
  "s_sub_i32 s92, s90, s93\n\t" \
  "s_cmp_ge_i32 s92, 0\n\t" \
  "s_cbranch_scc1 .Le%=\n\t" \
  "s_memrealtime s[90:91]\n\t" \
  "s_waitcnt lgkmcnt(1)\n\t" \
  "s_sub_i32 s92, s94, s93\n\t" \
  "s_cmp_lt_i32 s92, 0\n\t" \
  "s_cbranch_scc1 .Lpace%=\n\t" \
  "s_nop 4\n\t" \
  ".Le%=:\n\t" \
  "s_add_u32 s92, s92, s93\n\t" \
  "s_lshl_b32 s92, s92, 2\n\t" \
  "s_sub_i32 s92, %[tgt], s92\n\t" \
  "s_max_i32 s92, s92, 0\n\t" \
  "s_bitcmp0_b32 s92, 3\n\t" \
  "s_cbranch_scc1 .Lf3%=\n\t" \
  "s_nop 15\n\t" \
  ".Lf3%=:\n\t" \
  "s_bitcmp0_b32 s92, 2\n\t" \
  "s_cbranch_scc1 .Lf2%=\n\t" \
  "s_nop 10\n\t" \
  ".Lf2%=:\n\t" \
  ".Lgo%=:\n\t" \
  "s_mov_b32 s92, 0\n\t" \
  ".Lpoll%=:\n\t" \
  "buffer_load_dwordx4 v[200:203], v248, %[rs], %[som] offen sc1\n\t" \
  "buffer_load_dwordx4 v[204:207], v248, %[rs], %[som2] offen sc1\n\t" \
  "s_waitcnt vmcnt(1) lgkmcnt(0)\n\t" \
  "ds_write_b128 v249, v[200:203]\n\t" \
  "v_smfmac_f32_16x16x64_bf16 v[192:195], v[200:203], v[208:215], v251\n\t" \
  "v_max3_u32 v244, v200, v201, v202\n\t" \
  "v_smfmac_f32_16x16x64_bf16 v[196:199], v[200:203], v[236:243], v251\n\t" \
  "s_waitcnt vmcnt(0)\n\t" \
  "ds_write_b128 v249, v[204:207] offset:8192\n\t" \
  "v_smfmac_f32_16x16x64_bf16 v[192:195], v[204:207], v[216:223], v251\n\t" \
  "v_max3_u32 v244, v244, v203, v204\n\t" \
  "v_max3_u32 v244, v244, v205, v206\n\t" \
  "v_smfmac_f32_16x16x64_bf16 v[196:199], v[204:207], v[224:231], v251\n\t" \
  "v_max_u32 v244, v244, v207\n\t" \
  "v_cmp_le_u32 vcc, %[sent], v244\n\t" \
  "s_cbranch_vccnz .Lslow%=\n\t" \
  ".Lok%=:\n\t" \
  "s_waitcnt lgkmcnt(0)\n\t" \
  "s_barrier\n\t" \
  "ds_read_b128 v[208:211], v248 offset:7168\n\t" \
  "ds_read_b128 v[212:215], v248 offset:8192\n\t" \
  "ds_read_b128 v[236:239], v248 offset:9216\n\t" \
  "ds_read_b128 v[240:243], v248 offset:10240\n\t" \
  "ds_read_b128 v[244:247], v248 offset:11264\n\t" \
  "s_waitcnt lgkmcnt(4)\n\t" \
  "ds_read_b128 v[200:203], v248 offset:4096\n\t" \
  "v_smfmac_f32_16x16x64_bf16 v[192:195], v[208:211], v[0:7], v251\n\t" \
  "ds_read_b128 v[216:219], v250 offset:32768\n\t" \
  "v_smfmac_f32_16x16x64_bf16 v[196:199], v[208:211], v[8:15], v251\n\t" \
  "s_waitcnt lgkmcnt(5)\n\t" \
  "ds_read_b128 v[220:223], v250 offset:33792\n\t" \
  "v_smfmac_f32_16x16x64_bf16 v[192:195], v[212:215], v[16:23], v251\n\t" \
  "ds_read_b128 v[224:227], v250 offset:34816\n\t" \
  "v_smfmac_f32_16x16x64_bf16 v[196:199], v[212:215], v[24:31], v251\n\t" \
  "s_waitcnt lgkmcnt(6)\n\t" \
  "ds_read_b128 v[228:231], v250 offset:35840\n\t" \
  "v_smfmac_f32_16x16x64_bf16 v[192:195], v[236:239], v[32:39], v251\n\t" \
  "ds_read_b128 v[204:207], v248 offset:14336\n\t" \
  "v_smfmac_f32_16x16x64_bf16 v[196:199], v[236:239], v[40:47], v251\n\t" \
  "s_waitcnt lgkmcnt(7)\n\t" \
  "ds_read_b128 v[208:211], v248 offset:15360\n\t" \
  "v_smfmac_f32_16x16x64_bf16 v[192:195], v[240:243], v[48:55], v251\n\t" \
  "ds_read_b128 v[212:215], v248 offset:0\n\t" \
  "v_smfmac_f32_16x16x64_bf16 v[196:199], v[240:243], v[56:63], v251\n\t" \
  "s_waitcnt lgkmcnt(7)\n\t" \
  "v_smfmac_f32_16x16x64_bf16 v[192:195], v[244:247], v[64:71], v251\n\t" \
  "ds_read_b128 v[236:239], v248 offset:1024\n\t" \
  "v_smfmac_f32_16x16x64_bf16 v[196:199], v[244:247], v[72:79], v251\n\t" \
  "s_waitcnt lgkmcnt(6)\n\t" \
  "v_smfmac_f32_16x16x64_bf16 v[192:195], v[200:203], v[216:223], v251\n\t" \
  "s_waitcnt lgkmcnt(4)\n\t" \
  "ds_read_b128 v[240:243], v248 offset:12288\n\t" \
  "v_smfmac_f32_16x16x64_bf16 v[196:199], v[200:203], v[224:231], v251\n\t" \
  "s_waitcnt lgkmcnt(4)\n\t" \
  "ds_read_b128 v[216:219], v250 offset:0\n\t" \
  "v_smfmac_f32_16x16x64_bf16 v[192:195], v[204:207], v[80:87], v251\n\t" \
  "ds_read_b128 v[220:223], v250 offset:1024\n\t" \
  "v_smfmac_f32_16x16x64_bf16 v[196:199], v[204:207], v[88:95], v251\n\t" \
  "s_waitcnt lgkmcnt(4)\n\t" \
  "ds_read_b128 v[224:227], v250 offset:2048\n\t" \
  "v_smfmac_f32_16x16x64_bf16 v[192:195], v[208:211], v[96:103], v251\n\t" \
  "ds_read_b128 v[228:231], v250 offset:3072\n\t" \
  "v_smfmac_f32_16x16x64_bf16 v[196:199], v[208:211], v[104:111], v251\n\t" \
  "ds_read_b128 v[244:247], v248 offset:2048\n\t" \
  "v_smfmac_f32_16x16x64_bf16 v[192:195], v[212:215], v[112:119], v251\n\t" \
  "ds_read_b128 v[200:203], v248 offset:3072\n\t" \
  "v_smfmac_f32_16x16x64_bf16 v[196:199], v[212:215], v[120:127], v251\n\t" \
  "s_waitcnt lgkmcnt(6)\n\t" \
  "v_smfmac_f32_16x16x64_bf16 v[192:195], v[236:239], v[128:135], v251\n\t" \
  "ds_read_b128 v[204:207], v248 offset:6144\n\t" \
  "v_smfmac_f32_16x16x64_bf16 v[196:199], v[236:239], v[136:143], v251\n\t" \
  "s_waitcnt lgkmcnt(5)\n\t" \
  "v_smfmac_f32_16x16x64_bf16 v[192:195], v[240:243], v[216:223], v251\n\t" \
  "s_waitcnt lgkmcnt(3)\n\t" \
  "v_smfmac_f32_16x16x64_bf16 v[196:199], v[240:243], v[224:231], v251\n\t" \
  "s_waitcnt lgkmcnt(2)\n\t" \
  "v_smfmac_f32_16x16x64_bf16 v[192:195], v[244:247], v[144:151], v251\n\t" \
  "v_smfmac_f32_16x16x64_bf16 v[196:199], v[244:247], v[152:159], v251\n\t" \
  "s_waitcnt lgkmcnt(1)\n\t" \
  "v_smfmac_f32_16x16x64_bf16 v[192:195], v[200:203], v[160:167], v251\n\t" \
  "v_smfmac_f32_16x16x64_bf16 v[196:199], v[200:203], v[168:175], v251\n\t" \
  "s_waitcnt lgkmcnt(0)\n\t" \
  "v_smfmac_f32_16x16x64_bf16 v[192:195], v[204:207], v[176:183], v251\n\t" \
  "v_smfmac_f32_16x16x64_bf16 v[196:199], v[204:207], v[184:191], v251\n\t" \
  "s_nop 6\n\t" \
  "v_pk_add_f32 v[192:193], v[192:193], v[194:195]\n\t" \
  "v_pk_add_f32 v[196:197], v[196:197], v[198:199]\n\t" \
  "v_cvt_pk_bf16_f32 v192, v192, v193\n\t" \
  "v_cvt_pk_bf16_f32 v196, v196, v197\n\t" \
  "ds_write2_b32 v252, v192, v196 offset1:64\n\t" \
  "s_waitcnt lgkmcnt(0)\n\t" \
  "s_barrier\n\t" \
  "buffer_store_dword v192, v235, %[rs], %[sk] offen sc1\n\t" \
  "buffer_store_dword v196, v235, %[rs], %[sk] offen offset:256 sc1\n\t" \
  "s_mov_b32 %[spins], s92\n\t" \
  "s_branch .Lend%=\n\t" \
  ".Lslow%=:\n\t" \
  "s_mov_b64 s[94:95], 0\n\t" \
  "v_cmp_eq_u32 vcc, %[sent], v200\n\t" \
  "s_or_b64 s[94:95], s[94:95], vcc\n\t" \
  "v_cmp_eq_u32 vcc, %[sent], v201\n\t" \
  "s_or_b64 s[94:95], s[94:95], vcc\n\t" \
  "v_cmp_eq_u32 vcc, %[sent], v202\n\t" \
  "s_or_b64 s[94:95], s[94:95], vcc\n\t" \
  "v_cmp_eq_u32 vcc, %[sent], v203\n\t" \
  "s_or_b64 s[94:95], s[94:95], vcc\n\t" \
  "v_cmp_eq_u32 vcc, %[sent], v204\n\t" \
  "s_or_b64 s[94:95], s[94:95], vcc\n\t" \
  "v_cmp_eq_u32 vcc, %[sent], v205\n\t" \
  "s_or_b64 s[94:95], s[94:95], vcc\n\t" \
  "v_cmp_eq_u32 vcc, %[sent], v206\n\t" \
  "s_or_b64 s[94:95], s[94:95], vcc\n\t" \
  "v_cmp_eq_u32 vcc, %[sent], v207\n\t" \
  "s_or_b64 s[94:95], s[94:95], vcc\n\t" \
  "s_cmp_eq_u64 s[94:95], 0\n\t" \
  "s_cbranch_scc1 .Lok%=\n\t" \
  "v_mov_b64 v[192:193], 0\n\t" \
  "v_mov_b64 v[194:195], 0\n\t" \
  "v_mov_b64 v[196:197], 0\n\t" \
  "v_mov_b64 v[198:199], 0\n\t" \
  "s_add_u32 s92, s92, 1\n\t" \
  "s_cmp_eq_u32 s92, 1\n\t" \
  "s_cbranch_scc1 .Lpoll%=\n\t" \
  "s_sleep 2\n\t" \
  "s_and_b32 s93, s92, 63\n\t" \
  "s_cmp_lg_u32 s93, 0\n\t" \
  "s_cbranch_scc1 .Lpoll%=\n\t" \
  "v_mov_b32 v245, 0\n\t" \
  "buffer_load_dword v244, v245, %[rs], %[sab] offen sc1\n\t" \
  "s_waitcnt vmcnt(0)\n\t" \
  "v_readfirstlane_b32 s93, v244\n\t" \
  "s_nop 3\n\t" \
  "s_cmp_eq_u32 s93, 1\n\t" \
  "s_cbranch_scc1 .Lok%=\n\t" \
  "s_cmp_lt_u32 s92, 0x80000\n\t" \
  "s_cbranch_scc1 .Lpoll%=\n\t" \
  "v_mov_b32 v244, 1\n\t" \
  "buffer_store_dword v244, v245, %[rs], %[sab] offen sc1\n\t" \
  "s_branch .Lok%=\n\t" \
  ".Lend%=:\n\t"
#define QK_ASM_6 \
  "s_waitcnt lgkmcnt(0)\n\t" \
  "v_subrev_u32 v244, 0x8000, v250\n\t" \
  "ds_read_b128 v[216:219], v248 offset:57344\n\t" \
  "ds_read_b128 v[220:223], v248 offset:58368\n\t" \
  "ds_read_b128 v[224:227], v248 offset:59392\n\t" \
  "ds_read_b128 v[228:231], v248 offset:60416\n\t" \
  "ds_read_b128 v[208:211], v244 offset:0\n\t" \
  "ds_read_b128 v[212:215], v244 offset:1024\n\t" \
  "ds_read_b128 v[236:239], v244 offset:2048\n\t" \
  "ds_read_b128 v[240:243], v244 offset:3072\n\t" \
  "v_mov_b64 v[192:193], 0\n\t" \
  "v_mov_b64 v[194:195], 0\n\t" \
  "v_mov_b64 v[196:197], 0\n\t" \
  "v_mov_b64 v[198:199], 0\n\t" \
  "s_movk_i32 s92, 0x2000\n\t" \
  ".Lwt%=:\n\t" \
  "ds_read_b64 v[200:201], v248 offset:30720\n\t" \
  "s_memrealtime s[90:91]\n\t" \
  "s_sub_u32 s92, s92, 1\n\t" \
  "s_waitcnt lgkmcnt(0)\n\t" \
  "v_readfirstlane_b32 s93, v200\n\t" \
  "v_readfirstlane_b32 %[tgt], v201\n\t" \
  "s_cbranch_scc1 .Lgo%=\n\t" \
  "s_cmp_eq_u32 s93, %[step]\n\t" \
  "s_cbranch_scc1 .Lwx%=\n\t" \
  "s_sleep 1\n\t" \
  "s_branch .Lwt%=\n\t" \
  ".Lwx%=:\n\t" \
  "s_lshl_b32 s93, s90, 2\n\t" \
  "s_sub_i32 s93, %[tgt], s93\n\t" \
  "s_cmp_le_i32 s93, 0\n\t" \
  "s_cbranch_scc1 .Lgo%=\n\t" \
  "s_min_i32 s93, s93, 8000\n\t" \
  "s_mov_b32 s94, s93\n\t" \
  ".Lsl%=:\n\t" \
  "s_sub_i32 s94, s94, 18\n\t" \
  "s_cmp_lt_i32 s94, 30\n\t" \
  "s_cbranch_scc1 .Lsx%=\n\t" \
  "s_sleep 1\n\t" \
  "s_branch .Lsl%=\n\t" \
  ".Lsx%=:\n\t" \
  "s_sub_i32 s93, s93, 12\n\t" \
  "s_max_i32 s93, s93, 4\n\t" \
  "s_lshr_b32 s93, s93, 2\n\t" \
  "s_add_u32 s93, s93, s90\n\t" \
  "s_memrealtime s[90:91]\n\t" \
  ".Lpace%=:\n\t" \
  "s_memrealtime s[94:95]\n\t" \
  "s_waitcnt lgkmcnt(1)\n\t" \
  "s_sub_i32 s92, s90, s93\n\t" \
  "s_cmp_ge_i32 s92, 0\n\t" \
  "s_cbranch_scc1 .Le%=\n\t" \
  "s_memrealtime s[90:91]\n\t" \
  "s_waitcnt lgkmcnt(1)\n\t" \
  "s_sub_i32 s92, s94, s93\n\t" \
  "s_cmp_lt_i32 s92, 0\n\t" \
  "s_cbranch_scc1 .Lpace%=\n\t" \
  "s_nop 4\n\t" \
  ".Le%=:\n\t" \
  "s_add_u32 s92, s92, s93\n\t" \
  "s_lshl_b32 s92, s92, 2\n\t" \
  "s_sub_i32 s92, %[tgt], s92\n\t" \
  "s_max_i32 s92, s92, 0\n\t" \
  "s_bitcmp0_b32 s92, 3\n\t" \
  "s_cbranch_scc1 .Lf3%=\n\t" \
  "s_nop 15\n\t" \
  ".Lf3%=:\n\t" \
  "s_bitcmp0_b32 s92, 2\n\t" \
  "s_cbranch_scc1 .Lf2%=\n\t" \
  "s_nop 10\n\t" \
  ".Lf2%=:\n\t" \
  ".Lgo%=:\n\t" \
  "s_mov_b32 s92, 0\n\t" \
  ".Lpoll%=:\n\t" \
  "buffer_load_dwordx4 v[200:203], v248, %[rs], %[som] offen sc1\n\t" \
  "buffer_load_dwordx4 v[204:207], v248, %[rs], %[som2] offen sc1\n\t" \
  "s_waitcnt vmcnt(1) lgkmcnt(0)\n\t" \
  "ds_write_b128 v249, v[200:203]\n\t" \
  "v_smfmac_f32_16x16x64_bf16 v[192:195], v[200:203], v[208:215], v251\n\t" \
  "v_max3_u32 v244, v200, v201, v202\n\t" \
  "v_smfmac_f32_16x16x64_bf16 v[196:199], v[200:203], v[236:243], v251\n\t" \
  "s_waitcnt vmcnt(0)\n\t" \
  "ds_write_b128 v249, v[204:207] offset:8192\n\t" \
  "v_smfmac_f32_16x16x64_bf16 v[192:195], v[204:207], v[216:223], v251\n\t" \
  "v_max3_u32 v244, v244, v203, v204\n\t" \
  "v_max3_u32 v244, v244, v205, v206\n\t" \
  "v_smfmac_f32_16x16x64_bf16 v[196:199], v[204:207], v[224:231], v251\n\t" \
  "v_max_u32 v244, v244, v207\n\t" \
  "v_cmp_le_u32 vcc, %[sent], v244\n\t" \
  "s_cbranch_vccnz .Lslow%=\n\t" \
  ".Lok%=:\n\t" \
  "s_waitcnt lgkmcnt(0)\n\t" \
  "s_barrier\n\t" \
  "ds_read_b128 v[208:211], v248 offset:8192\n\t" \
  "ds_read_b128 v[212:215], v248 offset:9216\n\t" \
  "ds_read_b128 v[236:239], v248 offset:10240\n\t" \
  "ds_read_b128 v[240:243], v248 offset:11264\n\t" \
  "ds_read_b128 v[244:247], v248 offset:12288\n\t" \
  "s_waitcnt lgkmcnt(4)\n\t" \
  "ds_read_b128 v[200:203], v248 offset:7168\n\t" \
  "v_smfmac_f32_16x16x64_bf16 v[192:195], v[208:211], v[0:7], v251\n\t" \
  "ds_read_b128 v[216:219], v250 offset:32768\n\t" \
  "v_smfmac_f32_16x16x64_bf16 v[196:199], v[208:211], v[8:15], v251\n\t" \
  "s_waitcnt lgkmcnt(5)\n\t" \
  "ds_read_b128 v[220:223], v250 offset:33792\n\t" \
  "v_smfmac_f32_16x16x64_bf16 v[192:195], v[212:215], v[16:23], v251\n\t" \
  "ds_read_b128 v[224:227], v250 offset:34816\n\t" \
  "v_smfmac_f32_16x16x64_bf16 v[196:199], v[212:215], v[24:31], v251\n\t" \
  "s_waitcnt lgkmcnt(6)\n\t" \
  "ds_read_b128 v[228:231], v250 offset:35840\n\t" \
  "v_smfmac_f32_16x16x64_bf16 v[192:195], v[236:239], v[32:39], v251\n\t" \
  "ds_read_b128 v[204:207], v248 offset:15360\n\t" \
  "v_smfmac_f32_16x16x64_bf16 v[196:199], v[236:239], v[40:47], v251\n\t" \
  "s_waitcnt lgkmcnt(7)\n\t" \
  "ds_read_b128 v[208:211], v248 offset:0\n\t" \
  "v_smfmac_f32_16x16x64_bf16 v[192:195], v[240:243], v[48:55], v251\n\t" \
  "ds_read_b128 v[212:215], v248 offset:1024\n\t" \
  "v_smfmac_f32_16x16x64_bf16 v[196:199], v[240:243], v[56:63], v251\n\t" \
  "s_waitcnt lgkmcnt(7)\n\t" \
  "v_smfmac_f32_16x16x64_bf16 v[192:195], v[244:247], v[64:71], v251\n\t" \
  "ds_read_b128 v[236:239], v248 offset:2048\n\t" \
  "v_smfmac_f32_16x16x64_bf16 v[196:199], v[244:247], v[72:79], v251\n\t" \
  "s_waitcnt lgkmcnt(6)\n\t" \
  "v_smfmac_f32_16x16x64_bf16 v[192:195], v[200:203], v[216:223], v251\n\t" \
  "s_waitcnt lgkmcnt(4)\n\t" \
  "ds_read_b128 v[240:243], v248 offset:13312\n\t" \
  "v_smfmac_f32_16x16x64_bf16 v[196:199], v[200:203], v[224:231], v251\n\t" \
  "s_waitcnt lgkmcnt(4)\n\t" \
  "ds_read_b128 v[216:219], v250 offset:0\n\t" \
  "v_smfmac_f32_16x16x64_bf16 v[192:195], v[204:207], v[80:87], v251\n\t" \
  "ds_read_b128 v[220:223], v250 offset:1024\n\t" \
  "v_smfmac_f32_16x16x64_bf16 v[196:199], v[204:207], v[88:95], v251\n\t" \
  "s_waitcnt lgkmcnt(4)\n\t" \
  "ds_read_b128 v[224:227], v250 offset:2048\n\t" \
  "v_smfmac_f32_16x16x64_bf16 v[192:195], v[208:211], v[96:103], v251\n\t" \
  "ds_read_b128 v[228:231], v250 offset:3072\n\t" \
  "v_smfmac_f32_16x16x64_bf16 v[196:199], v[208:211], v[104:111], v251\n\t" \
  "ds_read_b128 v[244:247], v248 offset:3072\n\t" \
  "v_smfmac_f32_16x16x64_bf16 v[192:195], v[212:215], v[112:119], v251\n\t" \
  "ds_read_b128 v[200:203], v248 offset:4096\n\t" \
  "v_smfmac_f32_16x16x64_bf16 v[196:199], v[212:215], v[120:127], v251\n\t" \
  "s_waitcnt lgkmcnt(6)\n\t" \
  "v_smfmac_f32_16x16x64_bf16 v[192:195], v[236:239], v[128:135], v251\n\t" \
  "ds_read_b128 v[204:207], v248 offset:5120\n\t" \
  "v_smfmac_f32_16x16x64_bf16 v[196:199], v[236:239], v[136:143], v251\n\t" \
  "s_waitcnt lgkmcnt(5)\n\t" \
  "v_smfmac_f32_16x16x64_bf16 v[192:195], v[240:243], v[216:223], v251\n\t" \
  "s_waitcnt lgkmcnt(3)\n\t" \
  "v_smfmac_f32_16x16x64_bf16 v[196:199], v[240:243], v[224:231], v251\n\t" \
  "s_waitcnt lgkmcnt(2)\n\t" \
  "v_smfmac_f32_16x16x64_bf16 v[192:195], v[244:247], v[144:151], v251\n\t" \
  "v_smfmac_f32_16x16x64_bf16 v[196:199], v[244:247], v[152:159], v251\n\t" \
  "s_waitcnt lgkmcnt(1)\n\t" \
  "v_smfmac_f32_16x16x64_bf16 v[192:195], v[200:203], v[160:167], v251\n\t" \
  "v_smfmac_f32_16x16x64_bf16 v[196:199], v[200:203], v[168:175], v251\n\t" \
  "s_waitcnt lgkmcnt(0)\n\t" \
  "v_smfmac_f32_16x16x64_bf16 v[192:195], v[204:207], v[176:183], v251\n\t" \
  "v_smfmac_f32_16x16x64_bf16 v[196:199], v[204:207], v[184:191], v251\n\t" \
  "s_nop 6\n\t" \
  "v_pk_add_f32 v[192:193], v[192:193], v[194:195]\n\t" \
  "v_pk_add_f32 v[196:197], v[196:197], v[198:199]\n\t" \
  "v_cvt_pk_bf16_f32 v192, v192, v193\n\t" \
  "v_cvt_pk_bf16_f32 v196, v196, v197\n\t" \
  "ds_write2_b32 v252, v192, v196 offset1:64\n\t" \
  "s_waitcnt lgkmcnt(0)\n\t" \
  "s_barrier\n\t" \
  "buffer_store_dword v192, v235, %[rs], %[sk] offen sc1\n\t" \
  "buffer_store_dword v196, v235, %[rs], %[sk] offen offset:256 sc1\n\t" \
  "s_mov_b32 %[spins], s92\n\t" \
  "s_branch .Lend%=\n\t" \
  ".Lslow%=:\n\t" \
  "s_mov_b64 s[94:95], 0\n\t" \
  "v_cmp_eq_u32 vcc, %[sent], v200\n\t" \
  "s_or_b64 s[94:95], s[94:95], vcc\n\t" \
  "v_cmp_eq_u32 vcc, %[sent], v201\n\t" \
  "s_or_b64 s[94:95], s[94:95], vcc\n\t" \
  "v_cmp_eq_u32 vcc, %[sent], v202\n\t" \
  "s_or_b64 s[94:95], s[94:95], vcc\n\t" \
  "v_cmp_eq_u32 vcc, %[sent], v203\n\t" \
  "s_or_b64 s[94:95], s[94:95], vcc\n\t" \
  "v_cmp_eq_u32 vcc, %[sent], v204\n\t" \
  "s_or_b64 s[94:95], s[94:95], vcc\n\t" \
  "v_cmp_eq_u32 vcc, %[sent], v205\n\t" \
  "s_or_b64 s[94:95], s[94:95], vcc\n\t" \
  "v_cmp_eq_u32 vcc, %[sent], v206\n\t" \
  "s_or_b64 s[94:95], s[94:95], vcc\n\t" \
  "v_cmp_eq_u32 vcc, %[sent], v207\n\t" \
  "s_or_b64 s[94:95], s[94:95], vcc\n\t" \
  "s_cmp_eq_u64 s[94:95], 0\n\t" \
  "s_cbranch_scc1 .Lok%=\n\t" \
  "v_mov_b64 v[192:193], 0\n\t" \
  "v_mov_b64 v[194:195], 0\n\t" \
  "v_mov_b64 v[196:197], 0\n\t" \
  "v_mov_b64 v[198:199], 0\n\t" \
  "s_add_u32 s92, s92, 1\n\t" \
  "s_cmp_eq_u32 s92, 1\n\t" \
  "s_cbranch_scc1 .Lpoll%=\n\t" \
  "s_sleep 2\n\t" \
  "s_and_b32 s93, s92, 63\n\t" \
  "s_cmp_lg_u32 s93, 0\n\t" \
  "s_cbranch_scc1 .Lpoll%=\n\t" \
  "v_mov_b32 v245, 0\n\t" \
  "buffer_load_dword v244, v245, %[rs], %[sab] offen sc1\n\t" \
  "s_waitcnt vmcnt(0)\n\t" \
  "v_readfirstlane_b32 s93, v244\n\t" \
  "s_nop 3\n\t" \
  "s_cmp_eq_u32 s93, 1\n\t" \
  "s_cbranch_scc1 .Lok%=\n\t" \
  "s_cmp_lt_u32 s92, 0x80000\n\t" \
  "s_cbranch_scc1 .Lpoll%=\n\t" \
  "v_mov_b32 v244, 1\n\t" \
  "buffer_store_dword v244, v245, %[rs], %[sab] offen sc1\n\t" \
  "s_branch .Lok%=\n\t" \
  ".Lend%=:\n\t"
#define QK_ASM_7 \
  "s_waitcnt lgkmcnt(0)\n\t" \
  "v_subrev_u32 v244, 0x8000, v250\n\t" \
  "ds_read_b128 v[216:219], v248 offset:61440\n\t" \
  "ds_read_b128 v[220:223], v248 offset:62464\n\t" \
  "ds_read_b128 v[224:227], v248 offset:63488\n\t" \
  "ds_read_b128 v[228:231], v248 offset:64512\n\t" \
  "ds_read_b128 v[208:211], v244 offset:0\n\t" \
  "ds_read_b128 v[212:215], v244 offset:1024\n\t" \
  "ds_read_b128 v[236:239], v244 offset:2048\n\t" \
  "ds_read_b128 v[240:243], v244 offset:3072\n\t" \
  "v_mov_b64 v[192:193], 0\n\t" \
  "v_mov_b64 v[194:195], 0\n\t" \
  "v_mov_b64 v[196:197], 0\n\t" \
  "v_mov_b64 v[198:199], 0\n\t" \
  "s_movk_i32 s92, 0x2000\n\t" \
  ".Lwt%=:\n\t" \
  "ds_read_b64 v[200:201], v248 offset:30720\n\t" \
  "s_memrealtime s[90:91]\n\t" \
  "s_sub_u32 s92, s92, 1\n\t" \
  "s_waitcnt lgkmcnt(0)\n\t" \
  "v_readfirstlane_b32 s93, v200\n\t" \
  "v_readfirstlane_b32 %[tgt], v201\n\t" \
  "s_cbranch_scc1 .Lgo%=\n\t" \
  "s_cmp_eq_u32 s93, %[step]\n\t" \
  "s_cbranch_scc1 .Lwx%=\n\t" \
  "s_sleep 1\n\t" \
  "s_branch .Lwt%=\n\t" \
  ".Lwx%=:\n\t" \
  "s_lshl_b32 s93, s90, 2\n\t" \
  "s_sub_i32 s93, %[tgt], s93\n\t" \
  "s_cmp_le_i32 s93, 0\n\t" \
  "s_cbranch_scc1 .Lgo%=\n\t" \
  "s_min_i32 s93, s93, 8000\n\t" \
  "s_mov_b32 s94, s93\n\t" \
  ".Lsl%=:\n\t" \
  "s_sub_i32 s94, s94, 18\n\t" \
  "s_cmp_lt_i32 s94, 30\n\t" \
  "s_cbranch_scc1 .Lsx%=\n\t" \
  "s_sleep 1\n\t" \
  "s_branch .Lsl%=\n\t" \
  ".Lsx%=:\n\t" \
  "s_sub_i32 s93, s93, 12\n\t" \
  "s_max_i32 s93, s93, 4\n\t" \
  "s_lshr_b32 s93, s93, 2\n\t" \
  "s_add_u32 s93, s93, s90\n\t" \
  "s_memrealtime s[90:91]\n\t" \
  ".Lpace%=:\n\t" \
  "s_memrealtime s[94:95]\n\t" \
  "s_waitcnt lgkmcnt(1)\n\t" \
  "s_sub_i32 s92, s90, s93\n\t" \
  "s_cmp_ge_i32 s92, 0\n\t" \
  "s_cbranch_scc1 .Le%=\n\t" \
  "s_memrealtime s[90:91]\n\t" \
  "s_waitcnt lgkmcnt(1)\n\t" \
  "s_sub_i32 s92, s94, s93\n\t" \
  "s_cmp_lt_i32 s92, 0\n\t" \
  "s_cbranch_scc1 .Lpace%=\n\t" \
  "s_nop 4\n\t" \
  ".Le%=:\n\t" \
  "s_add_u32 s92, s92, s93\n\t" \
  "s_lshl_b32 s92, s92, 2\n\t" \
  "s_sub_i32 s92, %[tgt], s92\n\t" \
  "s_max_i32 s92, s92, 0\n\t" \
  "s_bitcmp0_b32 s92, 3\n\t" \
  "s_cbranch_scc1 .Lf3%=\n\t" \
  "s_nop 15\n\t" \
  ".Lf3%=:\n\t" \
  "s_bitcmp0_b32 s92, 2\n\t" \
  "s_cbranch_scc1 .Lf2%=\n\t" \
  "s_nop 10\n\t" \
  ".Lf2%=:\n\t" \
  ".Lgo%=:\n\t" \
  "s_mov_b32 s92, 0\n\t" \
  "s_memtime s[88:89]\n\t" \
  ".Lpoll%=:\n\t" \
  "buffer_load_dwordx4 v[200:203], v248, %[rs], %[som] offen sc1\n\t" \
  "buffer_load_dwordx4 v[204:207], v248, %[rs], %[som2] offen sc1\n\t" \
  "s_waitcnt vmcnt(1) lgkmcnt(0)\n\t" \
  "ds_write_b128 v249, v[200:203]\n\t" \
  "v_smfmac_f32_16x16x64_bf16 v[192:195], v[200:203], v[208:215], v251\n\t" \
  "v_max3_u32 v244, v200, v201, v202\n\t" \
  "v_smfmac_f32_16x16x64_bf16 v[196:199], v[200:203], v[236:243], v251\n\t" \
  "s_waitcnt vmcnt(0)\n\t" \
  "ds_write_b128 v249, v[204:207] offset:8192\n\t" \
  "v_smfmac_f32_16x16x64_bf16 v[192:195], v[204:207], v[216:223], v251\n\t" \
  "v_max3_u32 v244, v244, v203, v204\n\t" \
  "v_max3_u32 v244, v244, v205, v206\n\t" \
  "v_smfmac_f32_16x16x64_bf16 v[196:199], v[204:207], v[224:231], v251\n\t" \
  "v_max_u32 v244, v244, v207\n\t" \
  "v_cmp_le_u32 vcc, %[sent], v244\n\t" \
  "s_cbranch_vccnz .Lslow%=\n\t" \
  ".Lok%=:\n\t" \
  "s_waitcnt lgkmcnt(0)\n\t" \
  "s_setprio 3\n\t" \
  "s_barrier\n\t" \
  "s_memrealtime s[90:91]\n\t" \
  "s_memtime s[94:95]\n\t" \
  "v_mov_b32 v232, %[step]\n\t" \
  "v_lshrrev_b32 v234, 1, v248\n\t" \
  "s_mov_b64 exec, 0xffff\n\t" \
  "s_waitcnt lgkmcnt(0)\n\t" \
  "s_lshl_b32 s90, s90, 2\n\t" \
  "s_sub_u32 s94, s94, s88\n\t" \
  "s_mul_i32 s94, s94, 12200\n\t" \
  "s_lshr_b32 s94, s94, 16\n\t" \
  "s_add_u32 s94, s94, %[tgt]\n\t" \
  "s_sub_i32 s93, s90, s94\n\t" \
  "s_sub_i32 s93, s93, 30\n\t" \
  "s_max_i32 s93, s93, 0\n\t" \
  "s_add_u32 s90, s94, s93\n\t" \
  "s_sub_i32 s93, s90, %[tgt]\n\t" \
  "s_cmp_gt_i32 s93, 270\n\t" \
  "s_cselect_b32 s93, 70, 0\n\t" \
  "s_add_u32 s93, s93, s90\n\t" \
  "s_add_u32 s93, s93, %[lead]\n\t" \
  "v_mov_b32 v233, s93\n\t" \
  "buffer_store_dwordx2 v[232:233], v234, %[rs], %[sbc] offen sc1\n\t" \
  "s_mov_b64 exec, -1\n\t" \
  "s_mov_b32 %[tb1], s90\n\t" \
  "s_setprio 0\n\t" \
  "ds_read_b128 v[208:211], v248 offset:9216\n\t" \
  "ds_read_b128 v[212:215], v248 offset:10240\n\t" \
  "ds_read_b128 v[236:239], v248 offset:11264\n\t" \
  "ds_read_b128 v[240:243], v248 offset:13312\n\t" \
  "ds_read_b128 v[244:247], v248 offset:14336\n\t" \
  "s_waitcnt lgkmcnt(4)\n\t" \
  "ds_read_b128 v[200:203], v248 offset:6144\n\t" \
  "v_smfmac_f32_16x16x64_bf16 v[192:195], v[208:211], v[0:7], v251\n\t" \
  "ds_read_b128 v[216:219], v250 offset:32768\n\t" \
  "v_smfmac_f32_16x16x64_bf16 v[196:199], v[208:211], v[8:15], v251\n\t" \
  "s_waitcnt lgkmcnt(5)\n\t" \
  "ds_read_b128 v[220:223], v250 offset:33792\n\t" \
  "v_smfmac_f32_16x16x64_bf16 v[192:195], v[212:215], v[16:23], v251\n\t" \
  "ds_read_b128 v[224:227], v250 offset:34816\n\t" \
  "v_smfmac_f32_16x16x64_bf16 v[196:199], v[212:215], v[24:31], v251\n\t" \
  "s_waitcnt lgkmcnt(6)\n\t" \
  "ds_read_b128 v[228:231], v250 offset:35840\n\t" \
  "v_smfmac_f32_16x16x64_bf16 v[192:195], v[236:239], v[32:39], v251\n\t" \
  "ds_read_b128 v[204:207], v248 offset:0\n\t" \
  "v_smfmac_f32_16x16x64_bf16 v[196:199], v[236:239], v[40:47], v251\n\t" \
  "s_waitcnt lgkmcnt(7)\n\t" \
  "ds_read_b128 v[208:211], v248 offset:1024\n\t" \
  "v_smfmac_f32_16x16x64_bf16 v[192:195], v[240:243], v[48:55], v251\n\t" \
  "ds_read_b128 v[212:215], v248 offset:2048\n\t" \
  "v_smfmac_f32_16x16x64_bf16 v[196:199], v[240:243], v[56:63], v251\n\t" \
  "s_waitcnt lgkmcnt(7)\n\t" \
  "v_smfmac_f32_16x16x64_bf16 v[192:195], v[244:247], v[64:71], v251\n\t" \
  "ds_read_b128 v[236:239], v248 offset:3072\n\t" \
  "v_smfmac_f32_16x16x64_bf16 v[196:199], v[244:247], v[72:79], v251\n\t" \
  "s_waitcnt lgkmcnt(6)\n\t" \
  "v_smfmac_f32_16x16x64_bf16 v[192:195], v[200:203], v[216:223], v251\n\t" \
  "s_waitcnt lgkmcnt(4)\n\t" \
  "ds_read_b128 v[240:243], v248 offset:12288\n\t" \
  "v_smfmac_f32_16x16x64_bf16 v[196:199], v[200:203], v[224:231], v251\n\t" \
  "s_waitcnt lgkmcnt(4)\n\t" \
  "ds_read_b128 v[216:219], v250 offset:0\n\t" \
  "v_smfmac_f32_16x16x64_bf16 v[192:195], v[204:207], v[80:87], v251\n\t" \
  "ds_read_b128 v[220:223], v250 offset:1024\n\t" \
  "v_smfmac_f32_16x16x64_bf16 v[196:199], v[204:207], v[88:95], v251\n\t" \
  "s_waitcnt lgkmcnt(4)\n\t" \
  "ds_read_b128 v[224:227], v250 offset:2048\n\t" \
  "v_smfmac_f32_16x16x64_bf16 v[192:195], v[208:211], v[96:103], v251\n\t" \
  "ds_read_b128 v[228:231], v250 offset:3072\n\t" \
  "v_smfmac_f32_16x16x64_bf16 v[196:199], v[208:211], v[104:111], v251\n\t" \
  "ds_read_b128 v[244:247], v248 offset:4096\n\t" \
  "v_smfmac_f32_16x16x64_bf16 v[192:195], v[212:215], v[112:119], v251\n\t" \
  "ds_read_b128 v[200:203], v248 offset:5120\n\t" \
  "v_smfmac_f32_16x16x64_bf16 v[196:199], v[212:215], v[120:127], v251\n\t" \
  "s_waitcnt lgkmcnt(6)\n\t" \
  "v_smfmac_f32_16x16x64_bf16 v[192:195], v[236:239], v[128:135], v251\n\t" \
  "ds_read_b128 v[204:207], v248 offset:8192\n\t" \
  "v_smfmac_f32_16x16x64_bf16 v[196:199], v[236:239], v[136:143], v251\n\t" \
  "s_waitcnt lgkmcnt(5)\n\t" \
  "v_smfmac_f32_16x16x64_bf16 v[192:195], v[240:243], v[216:223], v251\n\t" \
  "s_waitcnt lgkmcnt(3)\n\t" \
  "v_smfmac_f32_16x16x64_bf16 v[196:199], v[240:243], v[224:231], v251\n\t" \
  "s_waitcnt lgkmcnt(2)\n\t" \
  "v_smfmac_f32_16x16x64_bf16 v[192:195], v[244:247], v[144:151], v251\n\t" \
  "v_smfmac_f32_16x16x64_bf16 v[196:199], v[244:247], v[152:159], v251\n\t" \
  "s_waitcnt lgkmcnt(1)\n\t" \
  "v_smfmac_f32_16x16x64_bf16 v[192:195], v[200:203], v[160:167], v251\n\t" \
  "v_smfmac_f32_16x16x64_bf16 v[196:199], v[200:203], v[168:175], v251\n\t" \
  "s_waitcnt lgkmcnt(0)\n\t" \
  "v_smfmac_f32_16x16x64_bf16 v[192:195], v[204:207], v[176:183], v251\n\t" \
  "v_smfmac_f32_16x16x64_bf16 v[196:199], v[204:207], v[184:191], v251\n\t" \
  "s_nop 6\n\t" \
  "v_pk_add_f32 v[192:193], v[192:193], v[194:195]\n\t" \
  "v_pk_add_f32 v[196:197], v[196:197], v[198:199]\n\t" \
  "v_cvt_pk_bf16_f32 v192, v192, v193\n\t" \
  "v_cvt_pk_bf16_f32 v196, v196, v197\n\t" \
  "ds_write2_b32 v252, v192, v196 offset1:64\n\t" \
  "s_waitcnt lgkmcnt(0)\n\t" \
  "s_barrier\n\t" \
  "buffer_store_dword v192, v235, %[rs], %[sk] offen sc1\n\t" \
  "buffer_store_dword v196, v235, %[rs], %[sk] offen offset:256 sc1\n\t" \
  "s_mov_b32 %[spins], s92\n\t" \
  "s_branch .Lend%=\n\t" \
  ".Lslow%=:\n\t" \
  "s_mov_b64 s[94:95], 0\n\t" \
  "v_cmp_eq_u32 vcc, %[sent], v200\n\t" \
  "s_or_b64 s[94:95], s[94:95], vcc\n\t" \
  "v_cmp_eq_u32 vcc, %[sent], v201\n\t" \
  "s_or_b64 s[94:95], s[94:95], vcc\n\t" \
  "v_cmp_eq_u32 vcc, %[sent], v202\n\t" \
  "s_or_b64 s[94:95], s[94:95], vcc\n\t" \
  "v_cmp_eq_u32 vcc, %[sent], v203\n\t" \
  "s_or_b64 s[94:95], s[94:95], vcc\n\t" \
  "v_cmp_eq_u32 vcc, %[sent], v204\n\t" \
  "s_or_b64 s[94:95], s[94:95], vcc\n\t" \
  "v_cmp_eq_u32 vcc, %[sent], v205\n\t" \
  "s_or_b64 s[94:95], s[94:95], vcc\n\t" \
  "v_cmp_eq_u32 vcc, %[sent], v206\n\t" \
  "s_or_b64 s[94:95], s[94:95], vcc\n\t" \
  "v_cmp_eq_u32 vcc, %[sent], v207\n\t" \
  "s_or_b64 s[94:95], s[94:95], vcc\n\t" \
  "s_cmp_eq_u64 s[94:95], 0\n\t" \
  "s_cbranch_scc1 .Lok%=\n\t" \
  "v_mov_b64 v[192:193], 0\n\t" \
  "v_mov_b64 v[194:195], 0\n\t" \
  "v_mov_b64 v[196:197], 0\n\t" \
  "v_mov_b64 v[198:199], 0\n\t" \
  "s_add_u32 s92, s92, 1\n\t" \
  "s_cmp_eq_u32 s92, 1\n\t" \
  "s_cbranch_scc1 .Lpoll%=\n\t" \
  "s_sleep 2\n\t" \
  "s_and_b32 s93, s92, 63\n\t" \
  "s_cmp_lg_u32 s93, 0\n\t" \
  "s_cbranch_scc1 .Lpoll%=\n\t" \
  "v_mov_b32 v245, 0\n\t" \
  "buffer_load_dword v244, v245, %[rs], %[sab] offen sc1\n\t" \
  "s_waitcnt vmcnt(0)\n\t" \
  "v_readfirstlane_b32 s93, v244\n\t" \
  "s_nop 3\n\t" \
  "s_cmp_eq_u32 s93, 1\n\t" \
  "s_cbranch_scc1 .Lok%=\n\t" \
  "s_cmp_lt_u32 s92, 0x80000\n\t" \
  "s_cbranch_scc1 .Lpoll%=\n\t" \
  "v_mov_b32 v244, 1\n\t" \
  "buffer_store_dword v244, v245, %[rs], %[sab] offen sc1\n\t" \
  "s_branch .Lok%=\n\t" \
  ".Lend%=:\n\t"
#define VV_ASM \
  "s_waitcnt lgkmcnt(0)\n\t" \
  "v_mov_b64 v[128:129], 0\n\t" \
  "v_mov_b64 v[130:131], 0\n\t" \
  "s_movk_i32 s92, 0x2000\n\t" \
  ".Lwt%=:\n\t" \
  "ds_read_b64 v[132:133], v200 offset:30720\n\t" \
  "s_memrealtime s[90:91]\n\t" \
  "s_sub_u32 s92, s92, 1\n\t" \
  "s_waitcnt lgkmcnt(0)\n\t" \
  "v_readfirstlane_b32 s93, v132\n\t" \
  "v_readfirstlane_b32 %[tgt], v133\n\t" \
  "s_cbranch_scc1 .Lgo%=\n\t" \
  "s_cmp_eq_u32 s93, %[step]\n\t" \
  "s_cbranch_scc1 .Lwx%=\n\t" \
  "s_sleep 1\n\t" \
  "s_branch .Lwt%=\n\t" \
  ".Lwx%=:\n\t" \
  "s_lshl_b32 s93, s90, 2\n\t" \
  "s_sub_i32 s93, %[tgt], s93\n\t" \
  "s_cmp_le_i32 s93, 0\n\t" \
  "s_cbranch_scc1 .Lgo%=\n\t" \
  "s_min_i32 s93, s93, 8000\n\t" \
  "s_mov_b32 s94, s93\n\t" \
  ".Lsl%=:\n\t" \
  "s_sub_i32 s94, s94, 18\n\t" \
  "s_cmp_lt_i32 s94, 30\n\t" \
  "s_cbranch_scc1 .Lsx%=\n\t" \
  "s_sleep 1\n\t" \
  "s_branch .Lsl%=\n\t" \
  ".Lsx%=:\n\t" \
  "s_sub_i32 s93, s93, 12\n\t" \
  "s_max_i32 s93, s93, 4\n\t" \
  "s_lshr_b32 s93, s93, 2\n\t" \
  "s_add_u32 s93, s93, s90\n\t" \
  "s_memrealtime s[90:91]\n\t" \
  ".Lpace%=:\n\t" \
  "s_memrealtime s[94:95]\n\t" \
  "s_waitcnt lgkmcnt(1)\n\t" \
  "s_sub_i32 s92, s90, s93\n\t" \
  "s_cmp_ge_i32 s92, 0\n\t" \
  "s_cbranch_scc1 .Le%=\n\t" \
  "s_memrealtime s[90:91]\n\t" \
  "s_waitcnt lgkmcnt(1)\n\t" \
  "s_sub_i32 s92, s94, s93\n\t" \
  "s_cmp_lt_i32 s92, 0\n\t" \
  "s_cbranch_scc1 .Lpace%=\n\t" \
  "s_nop 4\n\t" \
  ".Le%=:\n\t" \
  "s_add_u32 s92, s92, s93\n\t" \
  "s_lshl_b32 s92, s92, 2\n\t" \
  "s_sub_i32 s92, %[tgt], s92\n\t" \
  "s_max_i32 s92, s92, 0\n\t" \
  "s_bitcmp0_b32 s92, 3\n\t" \
  "s_cbranch_scc1 .Lf3%=\n\t" \
  "s_nop 15\n\t" \
  ".Lf3%=:\n\t" \
  "s_bitcmp0_b32 s92, 2\n\t" \
  "s_cbranch_scc1 .Lf2%=\n\t" \
  "s_nop 10\n\t" \
  ".Lf2%=:\n\t" \
  ".Lgo%=:\n\t" \
  "s_mov_b32 s92, 0\n\t" \
  ".Lpoll%=:\n\t" \
  "buffer_load_dwordx4 v[132:135], v200, %[rs], %[som] offen sc1\n\t" \
  "buffer_load_dwordx4 v[136:139], v200, %[rs], %[som2] offen sc1\n\t" \
  "s_waitcnt vmcnt(1) lgkmcnt(0)\n\t" \
  "ds_write_b128 v232, v[132:135]\n\t" \
  "v_smfmac_f32_16x16x64_bf16 v[128:131], v[132:135], v[0:7], v202\n\t" \
  "v_max3_u32 v196, v132, v133, v134\n\t" \
  "s_waitcnt vmcnt(0)\n\t" \
  "ds_write_b128 v232, v[136:139] offset:8192\n\t" \
  "v_smfmac_f32_16x16x64_bf16 v[128:131], v[136:139], v[64:71], v202\n\t" \
  "v_max3_u32 v196, v196, v135, v136\n\t" \
  "v_max3_u32 v196, v196, v137, v138\n\t" \
  "v_max_u32 v196, v196, v139\n\t" \
  "v_cmp_le_u32 vcc, %[sent], v196\n\t" \
  "s_cbranch_vccnz .Lslow%=\n\t" \
  ".Lok%=:\n\t" \
  "s_waitcnt lgkmcnt(0)\n\t" \
  "s_barrier\n\t" \
  "ds_read_b128 v[164:167], v233 offset:0\n\t" \
  "ds_read_b128 v[140:143], v234 offset:0\n\t" \
  "ds_read_b128 v[144:147], v235 offset:0\n\t" \
  "ds_read_b128 v[148:151], v236 offset:0\n\t" \
  "ds_read_b128 v[152:155], v237 offset:0\n\t" \
  "s_waitcnt lgkmcnt(4)\n\t" \
  "ds_read_b128 v[156:159], v238 offset:0\n\t" \
  "v_smfmac_f32_16x16x64_bf16 v[128:131], v[164:167], v[8:15], v202\n\t" \
  "s_waitcnt lgkmcnt(4)\n\t" \
  "ds_read_b128 v[160:163], v239 offset:0\n\t" \
  "v_smfmac_f32_16x16x64_bf16 v[128:131], v[140:143], v[16:23], v202\n\t" \
  "s_waitcnt lgkmcnt(4)\n\t" \
  "ds_read_b128 v[168:171], v233 offset:8192\n\t" \
  "v_smfmac_f32_16x16x64_bf16 v[128:131], v[144:147], v[24:31], v202\n\t" \
  "s_waitcnt lgkmcnt(4)\n\t" \
  "ds_read_b128 v[172:175], v234 offset:8192\n\t" \
  "v_smfmac_f32_16x16x64_bf16 v[128:131], v[148:151], v[32:39], v202\n\t" \
  "s_waitcnt lgkmcnt(4)\n\t" \
  "ds_read_b128 v[176:179], v235 offset:8192\n\t" \
  "v_smfmac_f32_16x16x64_bf16 v[128:131], v[152:155], v[40:47], v202\n\t" \
  "s_waitcnt lgkmcnt(4)\n\t" \
  "ds_read_b128 v[180:183], v236 offset:8192\n\t" \
  "v_smfmac_f32_16x16x64_bf16 v[128:131], v[156:159], v[48:55], v202\n\t" \
  "s_waitcnt lgkmcnt(4)\n\t" \
  "ds_read_b128 v[184:187], v237 offset:8192\n\t" \
  "v_smfmac_f32_16x16x64_bf16 v[128:131], v[160:163], v[56:63], v202\n\t" \
  "s_waitcnt lgkmcnt(4)\n\t" \
  "ds_read_b128 v[188:191], v238 offset:8192\n\t" \
  "v_smfmac_f32_16x16x64_bf16 v[128:131], v[168:171], v[72:79], v202\n\t" \
  "s_waitcnt lgkmcnt(4)\n\t" \
  "ds_read_b128 v[192:195], v239 offset:8192\n\t" \
  "v_smfmac_f32_16x16x64_bf16 v[128:131], v[172:175], v[80:87], v202\n\t" \
  "s_waitcnt lgkmcnt(4)\n\t" \
  "v_smfmac_f32_16x16x64_bf16 v[128:131], v[176:179], v[88:95], v202\n\t" \
  "s_waitcnt lgkmcnt(3)\n\t" \
  "v_smfmac_f32_16x16x64_bf16 v[128:131], v[180:183], v[96:103], v202\n\t" \
  "s_waitcnt lgkmcnt(2)\n\t" \
  "v_smfmac_f32_16x16x64_bf16 v[128:131], v[184:187], v[104:111], v202\n\t" \
  "s_waitcnt lgkmcnt(1)\n\t" \
  "v_smfmac_f32_16x16x64_bf16 v[128:131], v[188:191], v[112:119], v202\n\t" \
  "s_waitcnt lgkmcnt(0)\n\t" \
  "v_smfmac_f32_16x16x64_bf16 v[128:131], v[192:195], v[120:127], v202\n\t" \
  "s_nop 7\n\t" \
  "v_add_f32 v128, v128, v130\n\t" \
  "v_add_f32 v129, v129, v131\n\t" \
  "v_cvt_pk_bf16_f32 v128, v128, v129\n\t" \
  "buffer_store_dword v128, v203, %[rs], %[sv] offen sc1\n\t" \
  "ds_write_b32 v204, v128\n\t" \
  "s_waitcnt lgkmcnt(0)\n\t" \
  "s_barrier\n\t" \
  "s_mov_b32 %[spins], s92\n\t" \
  "s_branch .Lend%=\n\t" \
  ".Lslow%=:\n\t" \
  "s_mov_b64 s[94:95], 0\n\t" \
  "v_cmp_eq_u32 vcc, %[sent], v132\n\t" \
  "s_or_b64 s[94:95], s[94:95], vcc\n\t" \
  "v_cmp_eq_u32 vcc, %[sent], v133\n\t" \
  "s_or_b64 s[94:95], s[94:95], vcc\n\t" \
  "v_cmp_eq_u32 vcc, %[sent], v134\n\t" \
  "s_or_b64 s[94:95], s[94:95], vcc\n\t" \
  "v_cmp_eq_u32 vcc, %[sent], v135\n\t" \
  "s_or_b64 s[94:95], s[94:95], vcc\n\t" \
  "v_cmp_eq_u32 vcc, %[sent], v136\n\t" \
  "s_or_b64 s[94:95], s[94:95], vcc\n\t" \
  "v_cmp_eq_u32 vcc, %[sent], v137\n\t" \
  "s_or_b64 s[94:95], s[94:95], vcc\n\t" \
  "v_cmp_eq_u32 vcc, %[sent], v138\n\t" \
  "s_or_b64 s[94:95], s[94:95], vcc\n\t" \
  "v_cmp_eq_u32 vcc, %[sent], v139\n\t" \
  "s_or_b64 s[94:95], s[94:95], vcc\n\t" \
  "s_cmp_eq_u64 s[94:95], 0\n\t" \
  "s_cbranch_scc1 .Lok%=\n\t" \
  "v_mov_b64 v[128:129], 0\n\t" \
  "v_mov_b64 v[130:131], 0\n\t" \
  "s_add_u32 s92, s92, 1\n\t" \
  "s_cmp_eq_u32 s92, 1\n\t" \
  "s_cbranch_scc1 .Lpoll%=\n\t" \
  "s_sleep 2\n\t" \
  "s_and_b32 s93, s92, 63\n\t" \
  "s_cmp_lg_u32 s93, 0\n\t" \
  "s_cbranch_scc1 .Lpoll%=\n\t" \
  "v_mov_b32 v197, 0\n\t" \
  "buffer_load_dword v196, v197, %[rs], %[sab] offen sc1\n\t" \
  "s_waitcnt vmcnt(0)\n\t" \
  "v_readfirstlane_b32 s93, v196\n\t" \
  "s_nop 3\n\t" \
  "s_cmp_eq_u32 s93, 1\n\t" \
  "s_cbranch_scc1 .Lok%=\n\t" \
  "s_cmp_lt_u32 s92, 0x80000\n\t" \
  "s_cbranch_scc1 .Lpoll%=\n\t" \
  "v_mov_b32 v196, 1\n\t" \
  "buffer_store_dword v196, v197, %[rs], %[sab] offen sc1\n\t" \
  "s_branch .Lok%=\n\t" \
  ".Lend%=:\n\t"
#define TK_ASM \
  "buffer_load_dwordx4 v[224:227], v200, %[rs], %[sk] offen offset:0 sc1\n\t" \
  "buffer_load_dwordx4 v[228:231], v200, %[rs], %[sk] offen offset:1024 sc1\n\t" \
  "s_memrealtime s[88:89]\n\t" \
  "v_add_u32 v205, %[tl], v200\n\t" \
  "v_add_u32 v206, %[tq], v205\n\t" \
  "v_add_u32 v207, %[tv], v205\n\t" \
  "s_waitcnt lgkmcnt(0)\n\t" \
  "ds_read_b128 v[128:131], v206 offset:8192\n\t" \
  "ds_read_b128 v[144:147], v205 offset:0\n\t" \
  "ds_read_b128 v[160:163], v205 offset:4096\n\t" \
  "ds_read_b128 v[132:135], v206 offset:9216\n\t" \
  "ds_read_b128 v[148:151], v205 offset:1024\n\t" \
  "ds_read_b128 v[164:167], v205 offset:5120\n\t" \
  "ds_read_b128 v[136:139], v206 offset:10240\n\t" \
  "ds_read_b128 v[152:155], v205 offset:2048\n\t" \
  "ds_read_b128 v[168:171], v205 offset:6144\n\t" \
  "ds_read_b128 v[140:143], v206 offset:11264\n\t" \
  "ds_read_b128 v[156:159], v205 offset:3072\n\t" \
  "ds_read_b128 v[172:175], v205 offset:7168\n\t" \
  "v_add_u32 v206, %[vp], v218\n\t" \
  "s_lshl_b32 s88, s88, 2\n\t" \
  "s_add_u32 s89, s88, %[od]\n\t" \
  "s_waitcnt lgkmcnt(10)\n\t" \
  "v_mfma_f32_16x16x32_bf16 v[196:199], v[144:147], v[128:131], 0\n\t" \
  "s_waitcnt lgkmcnt(9)\n\t" \
  "v_mfma_f32_16x16x32_bf16 v[144:147], v[160:163], v[128:131], 0\n\t" \
  "s_waitcnt lgkmcnt(7)\n\t" \
  "v_mfma_f32_16x16x32_bf16 v[196:199], v[148:151], v[132:135], v[196:199]\n\t" \
  "s_waitcnt lgkmcnt(6)\n\t" \
  "v_mfma_f32_16x16x32_bf16 v[144:147], v[164:167], v[132:135], v[144:147]\n\t" \
  "s_waitcnt lgkmcnt(4)\n\t" \
  "v_mfma_f32_16x16x32_bf16 v[196:199], v[152:155], v[136:139], v[196:199]\n\t" \
  "s_waitcnt lgkmcnt(3)\n\t" \
  "v_mfma_f32_16x16x32_bf16 v[144:147], v[168:171], v[136:139], v[144:147]\n\t" \
  "s_waitcnt lgkmcnt(1)\n\t" \
  "v_mfma_f32_16x16x32_bf16 v[196:199], v[156:159], v[140:143], v[196:199]\n\t" \
  "s_waitcnt lgkmcnt(0)\n\t" \
  "v_mfma_f32_16x16x32_bf16 v[144:147], v[172:175], v[140:143], v[144:147]\n\t" \
  "ds_read_b128 v[176:179], v207 offset:16384\n\t" \
  "ds_read_b128 v[180:183], v207 offset:17408\n\t" \
  "ds_read_b128 v[184:187], v207 offset:18432\n\t" \
  "ds_read_b128 v[188:191], v207 offset:19456\n\t" \
  "ds_read2st64_b32 v[192:193], v206 offset0:0 offset1:1\n\t" \
  "ds_read2st64_b32 v[194:195], v206 offset0:2 offset1:3\n\t" \
  "v_cvt_pk_bf16_f32 v197, v196, v197\n\t" \
  "v_cvt_pk_bf16_f32 v199, v198, v199\n\t" \
  "v_cvt_pk_bf16_f32 v145, v144, v145\n\t" \
  "v_cvt_pk_bf16_f32 v147, v146, v147\n\t" \
  "v_lshlrev_b32 v196, 16, v197\n\t" \
  "v_lshlrev_b32 v198, 16, v199\n\t" \
  "v_lshlrev_b32 v144, 16, v145\n\t" \
  "v_lshlrev_b32 v146, 16, v147\n\t" \
  "v_and_b32 v197, 0xffff0000, v197\n\t" \
  "v_and_b32 v199, 0xffff0000, v199\n\t" \
  "v_and_b32 v145, 0xffff0000, v145\n\t" \
  "v_and_b32 v147, 0xffff0000, v147\n\t" \
  "v_pk_mul_f32 v[196:197], v[196:197], %[sc] op_sel_hi:[1,0]\n\t" \
  "v_pk_mul_f32 v[198:199], v[198:199], %[sc] op_sel_hi:[1,0]\n\t" \
  "v_pk_mul_f32 v[144:145], v[144:145], %[sc] op_sel_hi:[1,0]\n\t" \
  "v_pk_mul_f32 v[146:147], v[146:147], %[sc] op_sel_hi:[1,0]\n\t" \
  "v_cvt_pk_bf16_f32 v197, v196, v197\n\t" \
  "v_cvt_pk_bf16_f32 v199, v198, v199\n\t" \
  "v_cvt_pk_bf16_f32 v145, v144, v145\n\t" \
  "v_cvt_pk_bf16_f32 v147, v146, v147\n\t" \
  "v_lshlrev_b32 v196, 16, v197\n\t" \
  "v_lshlrev_b32 v198, 16, v199\n\t" \
  "v_lshlrev_b32 v144, 16, v145\n\t" \
  "v_lshlrev_b32 v146, 16, v147\n\t" \
  "v_and_b32 v197, 0xffff0000, v197\n\t" \
  "v_and_b32 v199, 0xffff0000, v199\n\t" \
  "v_and_b32 v145, 0xffff0000, v145\n\t" \
  "v_and_b32 v147, 0xffff0000, v147\n\t" \
  "v_min_f32 v196, v196, v208\n\t" \
  "v_min_f32 v197, v197, v209\n\t" \
  "v_min_f32 v198, v198, v210\n\t" \
  "v_min_f32 v199, v199, v211\n\t" \
  "v_min_f32 v144, v144, v212\n\t" \
  "v_min_f32 v145, v145, v213\n\t" \
  "v_min_f32 v146, v146, v214\n\t" \
  "v_min_f32 v147, v147, v215\n\t" \
  "v_max3_f32 v148, v196, v197, v198\n\t" \
  "v_max3_f32 v149, v199, v144, v145\n\t" \
  "v_max3_f32 v148, v148, v146, v147\n\t" \
  "v_max_f32 v150, v148, v149\n\t" \
  "v_max_f32 v151, v148, v149\n\t" \
  "s_nop 1\n\t" \
  "v_permlane32_swap_b32 v150, v151\n\t" \
  "v_max_f32 v150, v150, v151\n\t" \
  "s_mov_b32 s92, 0\n\t" \
  ".Ltake%=:\n\t" \
  "s_waitcnt vmcnt(1) lgkmcnt(0)\n\t" \
  "ds_write_b128 v216, v[224:227]\n\t" \
  "v_max3_u32 v148, v224, v225, v226\n\t" \
  "s_waitcnt vmcnt(0)\n\t" \
  "ds_write_b128 v216, v[228:231] offset:1024\n\t" \
  "ds_read_b64_tr_b16 v[160:161], v217 offset:0\n\t" \
  "ds_read_b64_tr_b16 v[162:163], v217 offset:64\n\t" \
  "ds_read_b64_tr_b16 v[164:165], v217 offset:512\n\t" \
  "ds_read_b64_tr_b16 v[166:167], v217 offset:576\n\t" \
  "ds_read_b64_tr_b16 v[168:169], v217 offset:1024\n\t" \
  "ds_read_b64_tr_b16 v[170:171], v217 offset:1088\n\t" \
  "ds_read_b64_tr_b16 v[172:173], v217 offset:1536\n\t" \
  "ds_read_b64_tr_b16 v[174:175], v217 offset:1600\n\t" \
  "v_max3_u32 v148, v148, v227, v228\n\t" \
  "v_max3_u32 v148, v148, v229, v230\n\t" \
  "v_max_u32 v148, v148, v231\n\t" \
  "v_cmp_le_u32 vcc, %[sent], v148\n\t" \
  "s_cbranch_vccnz .Lslow%=\n\t" \
  ".Lok%=:\n\t" \
  "s_waitcnt lgkmcnt(6)\n\t" \
  "v_mfma_f32_16x16x32_bf16 v[152:155], v[160:163], v[128:131], 0\n\t" \
  "s_waitcnt lgkmcnt(4)\n\t" \
  "v_mfma_f32_16x16x32_bf16 v[152:155], v[164:167], v[132:135], v[152:155]\n\t" \
  "s_waitcnt lgkmcnt(2)\n\t" \
  "v_mfma_f32_16x16x32_bf16 v[152:155], v[168:171], v[136:139], v[152:155]\n\t" \
  "s_waitcnt lgkmcnt(0)\n\t" \
  "v_mfma_f32_16x16x32_bf16 v[152:155], v[172:175], v[140:143], v[152:155]\n\t" \
  "v_mov_b32 v157, 0\n\t" \
  "v_mov_b32 v158, 0\n\t" \
  "s_nop 5\n\t" \
  "v_cvt_pk_bf16_f32 v153, v152, v153\n\t" \
  "v_cvt_pk_bf16_f32 v155, v154, v155\n\t" \
  "v_lshlrev_b32 v152, 16, v153\n\t" \
  "v_lshlrev_b32 v154, 16, v155\n\t" \
  "v_and_b32 v153, 0xffff0000, v153\n\t" \
  "v_and_b32 v155, 0xffff0000, v155\n\t" \
  "v_pk_mul_f32 v[152:153], v[152:153], %[sc] op_sel_hi:[1,0]\n\t" \
  "v_pk_mul_f32 v[154:155], v[154:155], %[sc] op_sel_hi:[1,0]\n\t" \
  "v_cvt_pk_bf16_f32 v153, v152, v153\n\t" \
  "v_cvt_pk_bf16_f32 v155, v154, v155\n\t" \
  "v_lshlrev_b32 v152, 16, v153\n\t" \
  "v_lshlrev_b32 v154, 16, v155\n\t" \
  "v_and_b32 v153, 0xffff0000, v153\n\t" \
  "v_and_b32 v155, 0xffff0000, v155\n\t" \
  "v_max3_f32 v148, v152, v153, v150\n\t" \
  "v_max3_f32 v149, v148, v154, v155\n\t" \
  "v_max3_f32 v150, v148, v154, v155\n\t" \
  "v_cndmask_b32 v152, v152, v154, %[up]\n\t" \
  "v_cndmask_b32 v153, v153, v155, %[up]\n\t" \
  "v_permlane16_swap_b32 v149, v150\n\t" \
  "v_max_f32 v148, v149, v150\n\t" \
  "v_mul_f32 v148, %[negc], v148\n\t" \
  "v_fma_f32 v152, v152, %[c], v148\n\t" \
  "v_fma_f32 v153, v153, %[c], v148\n\t" \
  "v_exp_f32 v152, v152\n\t" \
  "v_fma_f32 v196, v196, %[c], v148\n\t" \
  "v_exp_f32 v153, v153\n\t" \
  "v_fma_f32 v197, v197, %[c], v148\n\t" \
  "v_exp_f32 v196, v196\n\t" \
  "v_fma_f32 v198, v198, %[c], v148\n\t" \
  "v_exp_f32 v197, v197\n\t" \
  "v_fma_f32 v199, v199, %[c], v148\n\t" \
  "v_exp_f32 v198, v198\n\t" \
  "v_fma_f32 v144, v144, %[c], v148\n\t" \
  "v_pk_add_f32 v[132:133], v[152:153], v[196:197]\n\t" \
  "v_exp_f32 v199, v199\n\t" \
  "v_fma_f32 v145, v145, %[c], v148\n\t" \
  "v_exp_f32 v144, v144\n\t" \
  "v_fma_f32 v146, v146, %[c], v148\n\t" \
  "v_pk_add_f32 v[132:133], v[132:133], v[198:199]\n\t" \
  "v_exp_f32 v145, v145\n\t" \
  "v_fma_f32 v147, v147, %[c], v148\n\t" \
  "v_exp_f32 v146, v146\n\t" \
  "v_pk_add_f32 v[132:133], v[132:133], v[144:145]\n\t" \
  "v_exp_f32 v147, v147\n\t" \
  "s_nop 0\n\t" \
  "v_pk_add_f32 v[132:133], v[132:133], v[146:147]\n\t" \
  "v_add_f32 v134, v132, v133\n\t" \
  "v_add_f32 v135, v132, v133\n\t" \
  "s_nop 1\n\t" \
  "v_permlane16_swap_b32 v134, v135\n\t" \
  "v_add_f32 v132, v134, v135\n\t" \
  "v_add_f32 v133, v134, v135\n\t" \
  "s_nop 1\n\t" \
  "v_permlane32_swap_b32 v132, v133\n\t" \
  "v_add_f32 v132, v132, v133\n\t" \
  "v_rcp_f32 v148, v132\n\t" \
  "s_nop 0\n\t" \
  "v_pk_mul_f32 v[152:153], v[152:153], v[148:149] op_sel_hi:[1,0]\n\t" \
  "v_pk_mul_f32 v[196:197], v[196:197], v[148:149] op_sel_hi:[1,0]\n\t" \
  "v_pk_mul_f32 v[198:199], v[198:199], v[148:149] op_sel_hi:[1,0]\n\t" \
  "v_pk_mul_f32 v[144:145], v[144:145], v[148:149] op_sel_hi:[1,0]\n\t" \
  "v_pk_mul_f32 v[146:147], v[146:147], v[148:149] op_sel_hi:[1,0]\n\t" \
  "v_cvt_pk_bf16_f32 v156, v152, v153\n\t" \
  "v_cvt_pk_bf16_f32 v159, v152, v153\n\t" \
  "v_cvt_pk_bf16_f32 v128, v196, v197\n\t" \
  "v_cvt_pk_bf16_f32 v129, v198, v199\n\t" \
  "v_mfma_f32_16x16x16_bf16 v[160:163], v[192:193], v[156:157], 0\n\t" \
  "v_cvt_pk_bf16_f32 v130, v144, v145\n\t" \
  "v_mfma_f32_16x16x16_bf16 v[164:167], v[192:193], v[158:159], 0\n\t" \
  "v_cvt_pk_bf16_f32 v131, v146, v147\n\t" \
  "v_mfma_f32_16x16x16_bf16 v[168:171], v[194:195], v[156:157], 0\n\t" \
  "v_mfma_f32_16x16x16_bf16 v[172:175], v[194:195], v[158:159], 0\n\t" \
  "s_nop 0\n\t" \
  "v_mfma_f32_16x16x32_bf16 v[160:163], v[176:179], v[128:131], v[160:163]\n\t" \
  "s_nop 0\n\t" \
  "v_mfma_f32_16x16x32_bf16 v[164:167], v[180:183], v[128:131], v[164:167]\n\t" \
  "s_nop 0\n\t" \
  "v_mfma_f32_16x16x32_bf16 v[168:171], v[184:187], v[128:131], v[168:171]\n\t" \
  "v_mfma_f32_16x16x32_bf16 v[172:175], v[188:191], v[128:131], v[172:175]\n\t" \
  "s_nop 2\n\t" \
  "v_cvt_pk_bf16_f32 v136, v160, v161\n\t" \
  "v_cvt_pk_bf16_f32 v137, v162, v163\n\t" \
  "v_cvt_pk_bf16_f32 v138, v164, v165\n\t" \
  "v_cvt_pk_bf16_f32 v139, v166, v167\n\t" \
  "v_cvt_pk_bf16_f32 v140, v168, v169\n\t" \
  "v_cvt_pk_bf16_f32 v141, v170, v171\n\t" \
  "v_cvt_pk_bf16_f32 v142, v172, v173\n\t" \
  "v_cvt_pk_bf16_f32 v143, v174, v175\n\t" \
  "s_memrealtime s[90:91]\n\t" \
  "s_waitcnt lgkmcnt(0)\n\t" \
  "s_lshl_b32 s93, s90, 2\n\t" \
  "s_sub_i32 s93, s89, s93\n\t" \
  "s_cmp_le_i32 s93, 0\n\t" \
  "s_cbranch_scc1 .Lgoo%=\n\t" \
  "s_min_i32 s93, s93, 8000\n\t" \
  "s_mov_b32 s94, s93\n\t" \
  ".Lslo%=:\n\t" \
  "s_sub_i32 s94, s94, 18\n\t" \
  "s_cmp_lt_i32 s94, 30\n\t" \
  "s_cbranch_scc1 .Lsxo%=\n\t" \
  "s_sleep 1\n\t" \
  "s_branch .Lslo%=\n\t" \
  ".Lsxo%=:\n\t" \
  "s_add_u32 s93, s93, 3\n\t" \
  "s_lshr_b32 s93, s93, 2\n\t" \
  "s_add_u32 s93, s93, s90\n\t" \
  "s_memrealtime s[90:91]\n\t" \
  ".Lpaceo%=:\n\t" \
  "s_memrealtime s[94:95]\n\t" \
  "s_waitcnt lgkmcnt(1)\n\t" \
  "s_sub_i32 s92, s90, s93\n\t" \
  "s_cmp_ge_i32 s92, 0\n\t" \
  "s_cbranch_scc1 .Leo%=\n\t" \
  "s_memrealtime s[90:91]\n\t" \
  "s_waitcnt lgkmcnt(1)\n\t" \
  "s_sub_i32 s92, s94, s93\n\t" \
  "s_cmp_lt_i32 s92, 0\n\t" \
  "s_cbranch_scc1 .Lpaceo%=\n\t" \
  ".Leo%=:\n\t" \
  ".Lgoo%=:\n\t" \
  "buffer_store_dwordx4 v[136:139], v200, %[rt], %[so] offen offset:0 sc1\n\t" \
  "buffer_store_dwordx4 v[140:143], v200, %[rt], %[so] offen offset:1024 sc1\n\t" \
  "s_waitcnt lgkmcnt(0)\n\t" \
  "s_branch .Lend%=\n\t" \
  ".Lpoll%=:\n\t" \
  "buffer_load_dwordx4 v[224:227], v200, %[rs], %[sk] offen offset:0 sc1\n\t" \
  "buffer_load_dwordx4 v[228:231], v200, %[rs], %[sk] offen offset:1024 sc1\n\t" \
  "s_branch .Ltake%=\n\t" \
  "s_branch .Lend%=\n\t" \
  ".Lslow%=:\n\t" \
  "s_mov_b64 s[94:95], 0\n\t" \
  "v_cmp_eq_u32 vcc, %[sent], v224\n\t" \
  "s_or_b64 s[94:95], s[94:95], vcc\n\t" \
  "v_cmp_eq_u32 vcc, %[sent], v225\n\t" \
  "s_or_b64 s[94:95], s[94:95], vcc\n\t" \
  "v_cmp_eq_u32 vcc, %[sent], v226\n\t" \
  "s_or_b64 s[94:95], s[94:95], vcc\n\t" \
  "v_cmp_eq_u32 vcc, %[sent], v227\n\t" \
  "s_or_b64 s[94:95], s[94:95], vcc\n\t" \
  "v_cmp_eq_u32 vcc, %[sent], v228\n\t" \
  "s_or_b64 s[94:95], s[94:95], vcc\n\t" \
  "v_cmp_eq_u32 vcc, %[sent], v229\n\t" \
  "s_or_b64 s[94:95], s[94:95], vcc\n\t" \
  "v_cmp_eq_u32 vcc, %[sent], v230\n\t" \
  "s_or_b64 s[94:95], s[94:95], vcc\n\t" \
  "v_cmp_eq_u32 vcc, %[sent], v231\n\t" \
  "s_or_b64 s[94:95], s[94:95], vcc\n\t" \
  "s_cmp_eq_u64 s[94:95], 0\n\t" \
  "s_cbranch_scc1 .Lok%=\n\t" \
  "s_add_u32 s92, s92, 1\n\t" \
  "s_cmp_eq_u32 s92, 1\n\t" \
  "s_cbranch_scc1 .Lpoll%=\n\t" \
  "s_sleep 2\n\t" \
  "s_and_b32 s93, s92, 63\n\t" \
  "s_cmp_lg_u32 s93, 0\n\t" \
  "s_cbranch_scc1 .Lpoll%=\n\t" \
  "v_mov_b32 v149, 0\n\t" \
  "buffer_load_dword v148, v149, %[rs], %[sab] offen sc1\n\t" \
  "s_waitcnt vmcnt(0)\n\t" \
  "v_readfirstlane_b32 s93, v148\n\t" \
  "s_nop 3\n\t" \
  "s_cmp_eq_u32 s93, 1\n\t" \
  "s_cbranch_scc1 .Lok%=\n\t" \
  "s_cmp_lt_u32 s92, 0x80000\n\t" \
  "s_cbranch_scc1 .Lpoll%=\n\t" \
  "v_mov_b32 v148, 1\n\t" \
  "buffer_store_dword v148, v149, %[rs], %[sab] offen sc1\n\t" \
  "s_branch .Lok%=\n\t" \
  ".Lend%=:\n\t"
constexpr int TLAG = 2;
#define RED_ASM \
  "s_mov_b32 %[step], 0\n\t" \
  "s_lshl_b32 %[sh], %[step], 15\n\t" \
  "s_lshr_b32 s93, %[step], 4\n\t" \
  "s_lshr_b32 s93, %[pm], s93\n\t" \
  "s_and_b32 s93, s93, 4\n\t" \
  "s_lshl_b32 s93, s93, 11\n\t" \
  "s_lshl2_add_u32 %[so], %[sh], s93\n\t" \
  "s_sub_i32 s93, %[step], 2\n\t" \
  "s_lshl_b32 %[st], s93, 19\n\t" \
  "s_lshl_b32 s94, s93, 11\n\t" \
  "s_xor_b32 s94, s94, %[s0]\n\t" \
  "s_and_b32 s94, s94, 0x2000\n\t" \
  "s_or_b32 %[st], %[st], s94\n\t" \
  "s_cmp_lt_i32 s93, 0\n\t" \
  "s_cselect_b32 %[st], 0x70000000, %[st]\n\t" \
  "s_add_u32 %[st4], %[st], 4096\n\t" \
  "buffer_load_dwordx2 v[166:167], v173, %[rs], 0 offen sc1\n\t" \
  "v_mov_b64 v[144:145], 0\n\t" \
  "v_mov_b64 v[146:147], 0\n\t" \
  "v_mov_b64 v[148:149], 0\n\t" \
  "v_mov_b64 v[150:151], 0\n\t" \
  "v_mov_b64 v[152:153], 0\n\t" \
  "v_mov_b64 v[154:155], 0\n\t" \
  "v_mov_b64 v[156:157], 0\n\t" \
  "v_mov_b64 v[158:159], 0\n\t" \
  ".Lstep%=:\n\t" \
  "s_setprio 3\n\t" \
  "s_mov_b32 s92, 0\n\t" \
  ".Lcal%=:\n\t" \
  "s_waitcnt vmcnt(0)\n\t" \
  "s_memrealtime s[76:77]\n\t" \
  "v_cmp_ne_u32 vcc, %[step], v166\n\t" \
  "v_readlane_b32 s90, v167, 0\n\t" \
  "v_readlane_b32 s91, v167, 1\n\t" \
  "v_readlane_b32 s94, v167, 2\n\t" \
  "v_readlane_b32 s95, v167, 3\n\t" \
  "s_sub_i32 s90, s90, %[base]\n\t" \
  "s_sub_i32 s91, s91, %[base]\n\t" \
  "s_sub_i32 s94, s94, %[base]\n\t" \
  "s_sub_i32 s95, s95, %[base]\n\t" \
  "s_max_i32 s90, s90, s91\n\t" \
  "s_max_i32 s94, s94, s95\n\t" \
  "s_max_i32 s90, s90, s94\n\t" \
  "s_add_i32 s90, s90, %[ds]\n\t" \
  "s_waitcnt lgkmcnt(0)\n\t" \
  "s_lshl2_add_u32 s76, s76, %[mn]\n\t" \
  "s_sub_i32 s76, s76, %[base]\n\t" \
  "s_add_u32 %[E], s90, %[base]\n\t" \
  "s_and_b32 s93, vcc_lo, 0xf\n\t" \
  "s_cbranch_scc1 .Lbl%=\n\t" \
  ".Lgo%=:\n\t" \
  "v_mov_b32 v166, 7\n\t" \
  "s_mov_b64 exec, 1\n\t" \
  "v_mov_b32 v164, s90\n\t" \
  "ds_max_u32 v168, v164 offset:4100\n\t" \
  "ds_add_u32 v168, v164 offset:4096\n\t" \
  "s_mov_b64 exec, -1\n\t" \
  "s_memrealtime s[90:91]\n\t" \
  "s_waitcnt lgkmcnt(0)\n\t" \
  "s_lshl_b32 s93, s90, 2\n\t" \
  "s_sub_i32 s93, %[E], s93\n\t" \
  "s_cmp_le_i32 s93, 0\n\t" \
  "s_cbranch_scc1 .Lgop%=\n\t" \
  "s_min_i32 s93, s93, 8000\n\t" \
  "s_mov_b32 s94, s93\n\t" \
  ".Lslp%=:\n\t" \
  "s_sub_i32 s94, s94, 18\n\t" \
  "s_cmp_lt_i32 s94, 30\n\t" \
  "s_cbranch_scc1 .Lsxp%=\n\t" \
  "s_sleep 1\n\t" \
  "s_branch .Lslp%=\n\t" \
  ".Lsxp%=:\n\t" \
  "s_sub_i32 s93, s93, 12\n\t" \
  "s_max_i32 s93, s93, 4\n\t" \
  "s_lshr_b32 s93, s93, 2\n\t" \
  "s_add_u32 s93, s93, s90\n\t" \
  "s_memrealtime s[90:91]\n\t" \
  ".Lpacep%=:\n\t" \
  "s_memrealtime s[94:95]\n\t" \
  "s_waitcnt lgkmcnt(1)\n\t" \
  "s_sub_i32 s92, s90, s93\n\t" \
  "s_cmp_ge_i32 s92, 0\n\t" \
  "s_cbranch_scc1 .Lep%=\n\t" \
  "s_memrealtime s[90:91]\n\t" \
  "s_waitcnt lgkmcnt(1)\n\t" \
  "s_sub_i32 s92, s94, s93\n\t" \
  "s_cmp_lt_i32 s92, 0\n\t" \
  "s_cbranch_scc1 .Lpacep%=\n\t" \
  "s_nop 4\n\t" \
  ".Lep%=:\n\t" \
  "s_add_u32 s92, s92, s93\n\t" \
  "s_lshl_b32 s92, s92, 2\n\t" \
  "s_sub_i32 s92, %[E], s92\n\t" \
  "s_max_i32 s92, s92, 0\n\t" \
  "s_bitcmp0_b32 s92, 3\n\t" \
  "s_cbranch_scc1 .Lf3p%=\n\t" \
  "s_nop 15\n\t" \
  ".Lf3p%=:\n\t" \
  "s_bitcmp0_b32 s92, 2\n\t" \
  "s_cbranch_scc1 .Lf2p%=\n\t" \
  "s_nop 10\n\t" \
  ".Lf2p%=:\n\t" \
  ".Lgop%=:\n\t" \
  "s_mov_b32 s92, 0\n\t" \
  "buffer_load_dwordx4 v[128:131], v234, %[rs], %[so] offen sc1\n\t" \
  "s_nop 15\n\t" \
  "buffer_load_dwordx4 v[132:135], v235, %[rs], %[so] offen sc1\n\t" \
  "s_nop 15\n\t" \
  "buffer_load_dwordx4 v[136:139], v236, %[rs], %[so] offen sc1\n\t" \
  "s_nop 15\n\t" \
  "buffer_load_dwordx4 v[140:143], v237, %[rs], %[so] offen sc1\n\t" \
  "s_waitcnt lgkmcnt(0)\n\t" \
  ".Lw%=:\n\t" \
  "s_waitcnt vmcnt(3)\n\t" \
  "v_smfmac_f32_16x16x64_bf16 v[144:147], v[128:131], v[0:7], v172\n\t" \
  "v_max3_u32 v164, v128, v129, 0\n\t" \
  "v_smfmac_f32_16x16x64_bf16 v[148:151], v[128:131], v[8:15], v172\n\t" \
  "v_max3_u32 v164, v130, v131, v164\n\t" \
  "v_smfmac_f32_16x16x64_bf16 v[152:155], v[128:131], v[16:23], v172\n\t" \
  "v_smfmac_f32_16x16x64_bf16 v[156:159], v[128:131], v[24:31], v172\n\t" \
  "buffer_load_dwordx4 v[176:179], v238, %[rt], %[st] offen offset:0 sc1\n\t" \
  "s_waitcnt vmcnt(3)\n\t" \
  "v_smfmac_f32_16x16x64_bf16 v[144:147], v[132:135], v[32:39], v172\n\t" \
  "v_max3_u32 v164, v132, v133, v164\n\t" \
  "v_smfmac_f32_16x16x64_bf16 v[148:151], v[132:135], v[40:47], v172\n\t" \
  "v_max3_u32 v164, v134, v135, v164\n\t" \
  "v_smfmac_f32_16x16x64_bf16 v[152:155], v[132:135], v[48:55], v172\n\t" \
  "v_smfmac_f32_16x16x64_bf16 v[156:159], v[132:135], v[56:63], v172\n\t" \
  "buffer_load_dwordx4 v[180:183], v238, %[rt], %[st] offen offset:1024 sc1\n\t" \
  "s_waitcnt vmcnt(3)\n\t" \
  "v_smfmac_f32_16x16x64_bf16 v[144:147], v[136:139], v[64:71], v172\n\t" \
  "v_max3_u32 v164, v136, v137, v164\n\t" \
  "v_smfmac_f32_16x16x64_bf16 v[148:151], v[136:139], v[72:79], v172\n\t" \
  "v_max3_u32 v164, v138, v139, v164\n\t" \
  "v_smfmac_f32_16x16x64_bf16 v[152:155], v[136:139], v[80:87], v172\n\t" \
  "v_smfmac_f32_16x16x64_bf16 v[156:159], v[136:139], v[88:95], v172\n\t" \
  "buffer_load_dwordx4 v[184:187], v238, %[rt], %[st4] offen offset:0 sc1\n\t" \
  "s_waitcnt vmcnt(3)\n\t" \
  "v_smfmac_f32_16x16x64_bf16 v[144:147], v[140:143], v[96:103], v172\n\t" \
  "v_max3_u32 v164, v140, v141, v164\n\t" \
  "v_smfmac_f32_16x16x64_bf16 v[148:151], v[140:143], v[104:111], v172\n\t" \
  "v_max3_u32 v164, v142, v143, v164\n\t" \
  "v_smfmac_f32_16x16x64_bf16 v[152:155], v[140:143], v[112:119], v172\n\t" \
  "v_cmp_le_u32 vcc, %[sent], v164\n\t" \
  "v_smfmac_f32_16x16x64_bf16 v[156:159], v[140:143], v[120:127], v172\n\t" \
  "buffer_load_dwordx4 v[188:191], v238, %[rt], %[st4] offen offset:1024 sc1\n\t" \
  "s_cbranch_vccnz .Lslow%=\n\t" \
  ".Lok%=:\n\t" \
  "v_add_f32_dpp v160, v144, v146 quad_perm:[0,1,2,3] row_mask:0x3 bank_mask:0xf\n\t" \
  "v_add_f32_dpp v161, v145, v147 quad_perm:[0,1,2,3] row_mask:0x3 bank_mask:0xf\n\t" \
  "v_add_f32_dpp v162, v148, v150 quad_perm:[0,1,2,3] row_mask:0x3 bank_mask:0xf\n\t" \
  "v_add_f32_dpp v163, v149, v151 quad_perm:[0,1,2,3] row_mask:0x3 bank_mask:0xf\n\t" \
  "v_add_f32_dpp v160, v152, v154 quad_perm:[0,1,2,3] row_mask:0xc bank_mask:0xf\n\t" \
  "v_add_f32_dpp v161, v153, v155 quad_perm:[0,1,2,3] row_mask:0xc bank_mask:0xf\n\t" \
  "v_add_f32_dpp v162, v156, v158 quad_perm:[0,1,2,3] row_mask:0xc bank_mask:0xf\n\t" \
  "v_add_f32_dpp v163, v157, v159 quad_perm:[0,1,2,3] row_mask:0xc bank_mask:0xf\n\t" \
  "s_nop 0\n\t" \
  "v_permlane32_swap_b32 v160, v162\n\t" \
  "v_permlane32_swap_b32 v161, v163\n\t" \
  "v_add_f32 v160, v160, v162\n\t" \
  "v_add_f32 v161, v161, v163\n\t" \
  "ds_write_b64 v169, v[160:161]\n\t" \
  "s_mov_b64 exec, 1\n\t" \
  "ds_inc_rtn_u32 v164, v168, v166 offset:4104\n\t" \
  "s_mov_b64 exec, -1\n\t" \
  "ds_read_b64 v[128:129], v170 offset:0\n\t" \
  "ds_read_b64 v[130:131], v170 offset:512\n\t" \
  "ds_read_b64 v[132:133], v170 offset:1024\n\t" \
  "ds_read_b64 v[134:135], v170 offset:1536\n\t" \
  "ds_read_b64 v[136:137], v170 offset:2048\n\t" \
  "ds_read_b64 v[138:139], v170 offset:2560\n\t" \
  "ds_read_b64 v[140:141], v170 offset:3072\n\t" \
  "ds_read_b64 v[142:143], v170 offset:3584\n\t" \
  "s_waitcnt lgkmcnt(8)\n\t" \
  "v_readfirstlane_b32 s93, v164\n\t" \
  "s_cmp_lg_u32 s93, 7\n\t" \
  "s_cbranch_scc1 .Lb1%=\n\t" \
  "s_waitcnt lgkmcnt(4)\n\t" \
  "v_pk_add_f32 v[128:129], v[128:129], v[130:131]\n\t" \
  "v_pk_add_f32 v[132:133], v[132:133], v[134:135]\n\t" \
  "s_waitcnt lgkmcnt(0)\n\t" \
  "v_pk_add_f32 v[136:137], v[136:137], v[138:139]\n\t" \
  "v_pk_add_f32 v[140:141], v[140:141], v[142:143]\n\t" \
  "v_pk_add_f32 v[128:129], v[128:129], v[132:133]\n\t" \
  "v_pk_add_f32 v[136:137], v[136:137], v[140:141]\n\t" \
  "v_pk_add_f32 v[128:129], v[128:129], v[136:137]\n\t" \
  "v_cvt_pk_bf16_f32 v128, v128, v129\n\t" \
  "s_nop 1\n\t" \
  "v_mov_b32_dpp v129, v128 quad_perm:[1,0,3,2] row_mask:0xf bank_mask:0xf\n\t" \
  "v_perm_b32 v128, v129, v128, v174\n\t" \
  "buffer_store_dword v128, v171, %[rs], %[sh] offen sc1\n\t" \
  "s_memtime s[90:91]\n\t" \
  "s_barrier\n\t" \
  "s_memtime s[76:77]\n\t" \
  "s_memrealtime s[78:79]\n\t" \
  "s_nop 3\n\t" \
  "s_memrealtime s[80:81]\n\t" \
  "s_nop 3\n\t" \
  "s_memrealtime s[82:83]\n\t" \
  "s_nop 3\n\t" \
  "s_memrealtime s[84:85]\n\t" \
  "s_nop 3\n\t" \
  "s_memrealtime s[86:87]\n\t" \
  "s_nop 3\n\t" \
  "s_memrealtime s[88:89]\n\t" \
  "s_mov_b32 s93, 0\n\t" \
  "s_waitcnt lgkmcnt(0)\n\t" \
  "s_cmp_eq_u32 s80, s78\n\t" \
  "s_addc_u32 s93, s93, 0\n\t" \
  "s_cmp_eq_u32 s82, s78\n\t" \
  "s_addc_u32 s93, s93, 0\n\t" \
  "s_cmp_eq_u32 s84, s78\n\t" \
  "s_addc_u32 s93, s93, 0\n\t" \
  "s_cmp_eq_u32 s86, s78\n\t" \
  "s_addc_u32 s93, s93, 0\n\t" \
  "s_cmp_eq_u32 s88, s78\n\t" \
  "s_addc_u32 s93, s93, 0\n\t" \
  "s_mul_i32 s93, s93, 21\n\t" \
  "s_add_u32 s76, s76, 15\n\t" \
  "s_add_u32 s76, s76, s93\n\t" \
  "s_lshl2_add_u32 s78, s78, 16\n\t" \
  "s_mov_b64 exec, 1\n\t" \
  "s_sub_i32 s90, s76, s90\n\t" \
  "s_mul_i32 s90, s90, 11900\n\t" \
  "s_ashr_i32 s90, s90, 16\n\t" \
  "s_sub_u32 s90, s78, s90\n\t" \
  "s_sub_u32 s90, s90, %[base]\n\t" \
  "v_mov_b32 v164, s90\n\t" \
  "ds_write_b32 v168, v164 offset:4108\n\t" \
  "s_mov_b64 exec, -1\n\t" \
  "s_waitcnt vmcnt(1)\n\t" \
  ".Lb2%=:\n\t" \
  "s_setprio 0\n\t" \
  "s_bitcmp0_b64 %[am], %[step]\n\t" \
  "s_cbranch_scc1 .La1%=\n\t" \
  "buffer_store_dwordx4 v[224:227], v228, %[ra], 0 offen sc1\n\t" \
  ".La1%=:\n\t" \
  "buffer_load_dwordx4 v[192:195], v239, %[rt], %[st] offen offset:0 sc1\n\t" \
  "buffer_load_dwordx4 v[196:199], v239, %[rt], %[st] offen offset:1024 sc1\n\t" \
  "v_mfma_f32_16x16x32_bf16 v[128:131], v[0:3], v[176:179], 0\n\t" \
  "v_mfma_f32_16x16x32_bf16 v[136:139], v[8:11], v[176:179], 0\n\t" \
  "ds_read2st64_b32 v[144:145], v231 offset0:0 offset1:8\n\t" \
  "ds_read2st64_b32 v[146:147], v231 offset0:16 offset1:24\n\t" \
  "ds_read2st64_b32 v[148:149], v231 offset0:32 offset1:40\n\t" \
  "ds_read2st64_b32 v[150:151], v231 offset0:48 offset1:56\n\t" \
  "buffer_load_dwordx4 v[200:203], v239, %[rt], %[st4] offen offset:0 sc1\n\t" \
  "buffer_load_dwordx4 v[204:207], v239, %[rt], %[st4] offen offset:1024 sc1\n\t" \
  "v_mfma_f32_16x16x32_bf16 v[132:135], v[4:7], v[180:183], 0\n\t" \
  "v_mfma_f32_16x16x32_bf16 v[140:143], v[12:15], v[180:183], 0\n\t" \
  "s_waitcnt lgkmcnt(0)\n\t" \
  "v_add_f32 v144, v144, v145\n\t" \
  "v_add_f32 v146, v146, v147\n\t" \
  "v_add_f32 v148, v148, v149\n\t" \
  "v_add_f32 v150, v150, v151\n\t" \
  "v_add_f32 v144, v144, v146\n\t" \
  "v_add_f32 v148, v148, v150\n\t" \
  "buffer_load_dwordx4 v[208:211], v240, %[rt], %[st] offen offset:0 sc1\n\t" \
  "buffer_load_dwordx4 v[212:215], v240, %[rt], %[st] offen offset:1024 sc1\n\t" \
  "v_mfma_f32_16x16x32_bf16 v[128:131], v[16:19], v[184:187], v[128:131]\n\t" \
  "v_mfma_f32_16x16x32_bf16 v[136:139], v[24:27], v[184:187], v[136:139]\n\t" \
  "v_add_f32 v144, v144, v148\n\t" \
  "v_cvt_pk_bf16_f32 v144, v144, v144\n\t" \
  "ds_write_b16 v232, v144\n\t" \
  "ds_read_b128 v[224:227], v229\n\t" \
  "v_add_u32 v228, %[ainc], v228\n\t" \
  "v_add_u32 v229, %[rinc], v229\n\t" \
  "v_add_u32 v232, 1024, v232\n\t" \
  "v_xor_b32 v231, 0x4000, v231\n\t" \
  "buffer_load_dwordx4 v[216:219], v240, %[rt], %[st4] offen offset:0 sc1\n\t" \
  "buffer_load_dwordx4 v[220:223], v240, %[rt], %[st4] offen offset:1024 sc1\n\t" \
  "v_mfma_f32_16x16x32_bf16 v[132:135], v[20:23], v[188:191], v[132:135]\n\t" \
  "v_mfma_f32_16x16x32_bf16 v[140:143], v[28:31], v[188:191], v[140:143]\n\t" \
  "buffer_load_dwordx4 v[176:179], v241, %[rt], %[st] offen offset:0 sc1\n\t" \
  "s_waitcnt vmcnt(8)\n\t" \
  "v_mfma_f32_16x16x32_bf16 v[128:131], v[32:35], v[192:195], v[128:131]\n\t" \
  "v_mfma_f32_16x16x32_bf16 v[136:139], v[40:43], v[192:195], v[136:139]\n\t" \
  "buffer_load_dwordx4 v[180:183], v241, %[rt], %[st] offen offset:1024 sc1\n\t" \
  "s_waitcnt vmcnt(8)\n\t" \
  "v_mfma_f32_16x16x32_bf16 v[132:135], v[36:39], v[196:199], v[132:135]\n\t" \
  "v_mfma_f32_16x16x32_bf16 v[140:143], v[44:47], v[196:199], v[140:143]\n\t" \
  "buffer_load_dwordx4 v[184:187], v241, %[rt], %[st4] offen offset:0 sc1\n\t" \
  "s_waitcnt vmcnt(8)\n\t" \
  "v_mfma_f32_16x16x32_bf16 v[128:131], v[48:51], v[200:203], v[128:131]\n\t" \
  "v_mfma_f32_16x16x32_bf16 v[136:139], v[56:59], v[200:203], v[136:139]\n\t" \
  "ds_read_b128 v[160:163], v168 offset:4096\n\t" \
  "buffer_load_dwordx4 v[188:191], v241, %[rt], %[st4] offen offset:1024 sc1\n\t" \
  "s_waitcnt vmcnt(8)\n\t" \
  "v_mfma_f32_16x16x32_bf16 v[132:135], v[52:55], v[204:207], v[132:135]\n\t" \
  "v_mfma_f32_16x16x32_bf16 v[140:143], v[60:63], v[204:207], v[140:143]\n\t" \
  "s_waitcnt lgkmcnt(0)\n\t" \
  "v_readfirstlane_b32 s90, v161\n\t" \
  "v_readfirstlane_b32 s91, v163\n\t" \
  "v_readfirstlane_b32 s94, v160\n\t" \
  "s_sub_u32 s93, s94, %[sm]\n\t" \
  "s_mov_b32 %[sm], s94\n\t" \
  "s_lshr_b32 s93, s93, 3\n\t" \
  "s_sub_i32 s93, s90, s93\n\t" \
  "s_mul_i32 s93, s93, %[wm]\n\t" \
  "s_ashr_i32 s93, s93, 2\n\t" \
  "s_min_u32 s93, s93, %[gm]\n\t" \
  "s_sub_i32 s90, s90, s93\n\t" \
  "s_add_u32 s93, s90, %[rl]\n\t" \
  "s_add_u32 s93, s93, %[base]\n\t" \
  "s_add_u32 %[E], s93, %[lk]\n\t" \
  "s_sub_i32 s91, s91, s90\n\t" \
  "s_sub_i32 s91, s91, %[rl]\n\t" \
  "s_min_i32 s93, s91, %[up]\n\t" \
  "s_max_i32 s93, s93, %[dn]\n\t" \
  "s_cmp_gt_i32 s91, 60\n\t" \
  "s_cselect_b32 s93, 2, s93\n\t" \
  "s_add_i32 %[rl], %[rl], s93\n\t" \
  "v_mov_b32 v160, %[rl]\n\t" \
  "buffer_store_dword v160, v175, %[rs], 0 offen sc1\n\t" \
  "s_waitcnt vmcnt(8)\n\t" \
  "v_mfma_f32_16x16x32_bf16 v[128:131], v[64:67], v[208:211], v[128:131]\n\t" \
  "v_mfma_f32_16x16x32_bf16 v[136:139], v[72:75], v[208:211], v[136:139]\n\t" \
  "s_add_u32 %[step], %[step], 1\n\t" \
  "s_lshl_b32 %[sh], %[step], 15\n\t" \
  "s_lshr_b32 s93, %[step], 4\n\t" \
  "s_lshr_b32 s93, %[pm], s93\n\t" \
  "s_and_b32 s93, s93, 4\n\t" \
  "s_lshl_b32 s93, s93, 11\n\t" \
  "s_lshl2_add_u32 %[so], %[sh], s93\n\t" \
  "s_waitcnt vmcnt(7)\n\t" \
  "v_mfma_f32_16x16x32_bf16 v[132:135], v[68:71], v[212:215], v[132:135]\n\t" \
  "v_mfma_f32_16x16x32_bf16 v[140:143], v[76:79], v[212:215], v[140:143]\n\t" \
  "s_sub_i32 s93, %[step], 2\n\t" \
  "s_lshl_b32 %[st], s93, 19\n\t" \
  "s_lshl_b32 s94, s93, 11\n\t" \
  "s_xor_b32 s94, s94, %[s0]\n\t" \
  "s_and_b32 s94, s94, 0x2000\n\t" \
  "s_or_b32 %[st], %[st], s94\n\t" \
  "s_cmp_lt_i32 s93, 0\n\t" \
  "s_cselect_b32 %[st], 0x70000000, %[st]\n\t" \
  "s_add_u32 %[st4], %[st], 4096\n\t" \
  "s_waitcnt vmcnt(6)\n\t" \
  "v_mfma_f32_16x16x32_bf16 v[128:131], v[80:83], v[216:219], v[128:131]\n\t" \
  "v_mfma_f32_16x16x32_bf16 v[136:139], v[88:91], v[216:219], v[136:139]\n\t" \
  "s_waitcnt vmcnt(5)\n\t" \
  "v_mfma_f32_16x16x32_bf16 v[132:135], v[84:87], v[220:223], v[132:135]\n\t" \
  "v_mfma_f32_16x16x32_bf16 v[140:143], v[92:95], v[220:223], v[140:143]\n\t" \
  "s_cmp_eq_u32 %[step], 63\n\t" \
  "s_cbranch_scc1 .Llast%=\n\t" \
  "s_movk_i32 s93, 600\n\t" \
  ".Lpacel%=:\n\t" \
  "s_memrealtime s[90:91]\n\t" \
  "s_waitcnt lgkmcnt(0)\n\t" \
  "s_lshl_b32 s90, s90, 2\n\t" \
  "s_sub_i32 s91, %[E], s90\n\t" \
  "s_cmp_le_i32 s91, 0\n\t" \
  "s_cbranch_scc1 .Lgol%=\n\t" \
  "s_sub_u32 s93, s93, 1\n\t" \
  "s_cmp_lg_u32 s93, 0\n\t" \
  "s_cbranch_scc1 .Lpacel%=\n\t" \
  ".Lgol%=:\n\t" \
  "buffer_load_dwordx2 v[166:167], v173, %[rs], 0 offen sc1\n\t" \
  "s_waitcnt vmcnt(5)\n\t" \
  "v_mfma_f32_16x16x32_bf16 v[128:131], v[96:99], v[176:179], v[128:131]\n\t" \
  "v_mfma_f32_16x16x32_bf16 v[136:139], v[104:107], v[176:179], v[136:139]\n\t" \
  "s_waitcnt vmcnt(4)\n\t" \
  "v_mfma_f32_16x16x32_bf16 v[132:135], v[100:103], v[180:183], v[132:135]\n\t" \
  "v_mfma_f32_16x16x32_bf16 v[140:143], v[108:111], v[180:183], v[140:143]\n\t" \
  "s_waitcnt vmcnt(3)\n\t" \
  "v_mfma_f32_16x16x32_bf16 v[128:131], v[112:115], v[184:187], v[128:131]\n\t" \
  "v_mfma_f32_16x16x32_bf16 v[136:139], v[120:123], v[184:187], v[136:139]\n\t" \
  "s_waitcnt vmcnt(2)\n\t" \
  "v_mfma_f32_16x16x32_bf16 v[132:135], v[116:119], v[188:191], v[132:135]\n\t" \
  "v_mfma_f32_16x16x32_bf16 v[140:143], v[124:127], v[188:191], v[140:143]\n\t" \
  "v_mov_b64 v[144:145], 0\n\t" \
  "v_mov_b64 v[146:147], 0\n\t" \
  "v_mov_b64 v[148:149], 0\n\t" \
  "v_mov_b64 v[150:151], 0\n\t" \
  "v_mov_b64 v[152:153], 0\n\t" \
  "v_mov_b64 v[154:155], 0\n\t" \
  "v_mov_b64 v[156:157], 0\n\t" \
  "v_mov_b64 v[158:159], 0\n\t" \
  "v_pk_add_f32 v[128:129], v[128:129], v[132:133]\n\t" \
  "v_pk_add_f32 v[130:131], v[130:131], v[134:135]\n\t" \
  "ds_write_b128 v230, v[128:131] offset:0\n\t" \
  "v_pk_add_f32 v[136:137], v[136:137], v[140:141]\n\t" \
  "v_pk_add_f32 v[138:139], v[138:139], v[142:143]\n\t" \
  "ds_write_b128 v230, v[136:139] offset:1024\n\t" \
  "v_xor_b32 v230, 0x4000, v230\n\t" \
  "s_cmp_lg_u32 %[step], 63\n\t" \
  "s_cbranch_scc1 .Lstep%=\n\t" \
  ".Lfc%=:\n\t" \
  "s_waitcnt vmcnt(0)\n\t" \
  "v_cmp_eq_u32 vcc, -1, v166\n\t" \
  "s_cbranch_vccnz .Lfw%=\n\t" \
  ".Lfo%=:\n\t" \
  "v_add_u32 v242, 16384, v242\n\t" \
  "buffer_load_dwordx4 v[176:179], v238, %[rt], %[st] offen offset:0 sc1\n\t" \
  "buffer_load_dwordx4 v[180:183], v238, %[rt], %[st] offen offset:1024 sc1\n\t" \
  "buffer_load_dwordx4 v[184:187], v238, %[rt], %[st4] offen offset:0 sc1\n\t" \
  "buffer_load_dwordx4 v[188:191], v238, %[rt], %[st4] offen offset:1024 sc1\n\t" \
  "buffer_load_dwordx4 v[192:195], v239, %[rt], %[st] offen offset:0 sc1\n\t" \
  "buffer_load_dwordx4 v[196:199], v239, %[rt], %[st] offen offset:1024 sc1\n\t" \
  "buffer_load_dwordx4 v[200:203], v239, %[rt], %[st4] offen offset:0 sc1\n\t" \
  "buffer_load_dwordx4 v[204:207], v239, %[rt], %[st4] offen offset:1024 sc1\n\t" \
  "v_subrev_u32 v230, 98304, v230\n\t" \
  ".Lt%=:\n\t" \
  "s_waitcnt vmcnt(7)\n\t" \
  "v_mfma_f32_16x16x32_bf16 v[128:131], v[0:3], v[176:179], 0\n\t" \
  "v_mfma_f32_16x16x32_bf16 v[136:139], v[8:11], v[176:179], 0\n\t" \
  "buffer_load_dwordx4 v[176:179], v240, %[rt], %[st] offen offset:0 sc1\n\t" \
  "s_waitcnt vmcnt(7)\n\t" \
  "v_mfma_f32_16x16x32_bf16 v[132:135], v[4:7], v[180:183], 0\n\t" \
  "v_mfma_f32_16x16x32_bf16 v[140:143], v[12:15], v[180:183], 0\n\t" \
  "buffer_load_dwordx4 v[180:183], v240, %[rt], %[st] offen offset:1024 sc1\n\t" \
  "s_waitcnt vmcnt(7)\n\t" \
  "v_mfma_f32_16x16x32_bf16 v[128:131], v[16:19], v[184:187], v[128:131]\n\t" \
  "v_mfma_f32_16x16x32_bf16 v[136:139], v[24:27], v[184:187], v[136:139]\n\t" \
  "buffer_load_dwordx4 v[184:187], v240, %[rt], %[st4] offen offset:0 sc1\n\t" \
  "buffer_load_dword v166, v242, %[rs], 0 offen sc1\n\t" \
  "s_waitcnt vmcnt(8)\n\t" \
  "v_mfma_f32_16x16x32_bf16 v[132:135], v[20:23], v[188:191], v[132:135]\n\t" \
  "v_mfma_f32_16x16x32_bf16 v[140:143], v[28:31], v[188:191], v[140:143]\n\t" \
  "buffer_load_dwordx4 v[188:191], v240, %[rt], %[st4] offen offset:1024 sc1\n\t" \
  "s_waitcnt vmcnt(8)\n\t" \
  "v_mfma_f32_16x16x32_bf16 v[128:131], v[32:35], v[192:195], v[128:131]\n\t" \
  "v_mfma_f32_16x16x32_bf16 v[136:139], v[40:43], v[192:195], v[136:139]\n\t" \
  "buffer_load_dwordx4 v[192:195], v241, %[rt], %[st] offen offset:0 sc1\n\t" \
  "s_waitcnt vmcnt(8)\n\t" \
  "v_mfma_f32_16x16x32_bf16 v[132:135], v[36:39], v[196:199], v[132:135]\n\t" \
  "v_mfma_f32_16x16x32_bf16 v[140:143], v[44:47], v[196:199], v[140:143]\n\t" \
  "buffer_load_dwordx4 v[196:199], v241, %[rt], %[st] offen offset:1024 sc1\n\t" \
  "s_waitcnt vmcnt(8)\n\t" \
  "v_mfma_f32_16x16x32_bf16 v[128:131], v[48:51], v[200:203], v[128:131]\n\t" \
  "v_mfma_f32_16x16x32_bf16 v[136:139], v[56:59], v[200:203], v[136:139]\n\t" \
  "buffer_load_dwordx4 v[200:203], v241, %[rt], %[st4] offen offset:0 sc1\n\t" \
  "s_waitcnt vmcnt(8)\n\t" \
  "v_mfma_f32_16x16x32_bf16 v[132:135], v[52:55], v[204:207], v[132:135]\n\t" \
  "v_mfma_f32_16x16x32_bf16 v[140:143], v[60:63], v[204:207], v[140:143]\n\t" \
  "buffer_load_dwordx4 v[204:207], v241, %[rt], %[st4] offen offset:1024 sc1\n\t" \
  "s_waitcnt vmcnt(8)\n\t" \
  "v_mfma_f32_16x16x32_bf16 v[128:131], v[64:67], v[176:179], v[128:131]\n\t" \
  "v_mfma_f32_16x16x32_bf16 v[136:139], v[72:75], v[176:179], v[136:139]\n\t" \
  "s_waitcnt vmcnt(5)\n\t" \
  ".Ltc%=:\n\t" \
  "v_cmp_eq_u32 vcc, -1, v166\n\t" \
  "s_cbranch_vccnz .Ltw%=\n\t" \
  ".Lto%=:\n\t" \
  "s_cmp_eq_u32 %[step], 65\n\t" \
  "s_cbranch_scc1 .Ltl%=\n\t" \
  "s_add_u32 %[step], %[step], 1\n\t" \
  "s_sub_i32 s93, %[step], 2\n\t" \
  "s_lshl_b32 %[st], s93, 19\n\t" \
  "s_lshl_b32 s94, s93, 11\n\t" \
  "s_xor_b32 s94, s94, %[s0]\n\t" \
  "s_and_b32 s94, s94, 0x2000\n\t" \
  "s_or_b32 %[st], %[st], s94\n\t" \
  "s_cmp_lt_i32 s93, 0\n\t" \
  "s_cselect_b32 %[st], 0x70000000, %[st]\n\t" \
  "s_add_u32 %[st4], %[st], 4096\n\t" \
  "v_add_u32 v242, 16384, v242\n\t" \
  "s_cmp_eq_u32 %[step], 65\n\t" \
  "s_cbranch_scc0 .Ltn%=\n\t" \
  "v_mov_b32 v242, %[fg]\n\t" \
  ".Ltn%=:\n\t" \
  "buffer_load_dwordx4 v[176:179], v238, %[rt], %[st] offen offset:0 sc1\n\t" \
  "s_waitcnt vmcnt(7)\n\t" \
  "v_mfma_f32_16x16x32_bf16 v[132:135], v[68:71], v[180:183], v[132:135]\n\t" \
  "v_mfma_f32_16x16x32_bf16 v[140:143], v[76:79], v[180:183], v[140:143]\n\t" \
  "buffer_load_dwordx4 v[180:183], v238, %[rt], %[st] offen offset:1024 sc1\n\t" \
  "s_waitcnt vmcnt(7)\n\t" \
  "v_mfma_f32_16x16x32_bf16 v[128:131], v[80:83], v[184:187], v[128:131]\n\t" \
  "v_mfma_f32_16x16x32_bf16 v[136:139], v[88:91], v[184:187], v[136:139]\n\t" \
  "buffer_load_dwordx4 v[184:187], v238, %[rt], %[st4] offen offset:0 sc1\n\t" \
  "s_waitcnt vmcnt(7)\n\t" \
  "v_mfma_f32_16x16x32_bf16 v[132:135], v[84:87], v[188:191], v[132:135]\n\t" \
  "v_mfma_f32_16x16x32_bf16 v[140:143], v[92:95], v[188:191], v[140:143]\n\t" \
  "buffer_load_dwordx4 v[188:191], v238, %[rt], %[st4] offen offset:1024 sc1\n\t" \
  "s_waitcnt vmcnt(7)\n\t" \
  "v_mfma_f32_16x16x32_bf16 v[128:131], v[96:99], v[192:195], v[128:131]\n\t" \
  "v_mfma_f32_16x16x32_bf16 v[136:139], v[104:107], v[192:195], v[136:139]\n\t" \
  "buffer_load_dwordx4 v[192:195], v239, %[rt], %[st] offen offset:0 sc1\n\t" \
  "s_waitcnt vmcnt(7)\n\t" \
  "v_mfma_f32_16x16x32_bf16 v[132:135], v[100:103], v[196:199], v[132:135]\n\t" \
  "v_mfma_f32_16x16x32_bf16 v[140:143], v[108:111], v[196:199], v[140:143]\n\t" \
  "buffer_load_dwordx4 v[196:199], v239, %[rt], %[st] offen offset:1024 sc1\n\t" \
  "s_waitcnt vmcnt(7)\n\t" \
  "v_mfma_f32_16x16x32_bf16 v[128:131], v[112:115], v[200:203], v[128:131]\n\t" \
  "v_mfma_f32_16x16x32_bf16 v[136:139], v[120:123], v[200:203], v[136:139]\n\t" \
  "buffer_load_dwordx4 v[200:203], v239, %[rt], %[st4] offen offset:0 sc1\n\t" \
  "s_waitcnt vmcnt(7)\n\t" \
  "v_mfma_f32_16x16x32_bf16 v[132:135], v[116:119], v[204:207], v[132:135]\n\t" \
  "v_mfma_f32_16x16x32_bf16 v[140:143], v[124:127], v[204:207], v[140:143]\n\t" \
  "buffer_load_dwordx4 v[204:207], v239, %[rt], %[st4] offen offset:1024 sc1\n\t" \
  "s_nop 5\n\t" \
  "v_pk_add_f32 v[128:129], v[128:129], v[132:133]\n\t" \
  "v_pk_add_f32 v[130:131], v[130:131], v[134:135]\n\t" \
  "ds_write_b128 v230, v[128:131] offset:0\n\t" \
  "v_pk_add_f32 v[136:137], v[136:137], v[140:141]\n\t" \
  "v_pk_add_f32 v[138:139], v[138:139], v[142:143]\n\t" \
  "ds_write_b128 v230, v[136:139] offset:1024\n\t" \
  "v_add_u32 v230, 16384, v230\n\t" \
  "s_branch .Lt%=\n\t" \
  ".Ltl%=:\n\t" \
  "buffer_load_dwordx4 v[224:227], v243, %[rg], 0 offen sc1\n\t" \
  "s_waitcnt vmcnt(7)\n\t" \
  "v_mfma_f32_16x16x32_bf16 v[132:135], v[68:71], v[180:183], v[132:135]\n\t" \
  "v_mfma_f32_16x16x32_bf16 v[140:143], v[76:79], v[180:183], v[140:143]\n\t" \
  "s_waitcnt vmcnt(6)\n\t" \
  "v_mfma_f32_16x16x32_bf16 v[128:131], v[80:83], v[184:187], v[128:131]\n\t" \
  "v_mfma_f32_16x16x32_bf16 v[136:139], v[88:91], v[184:187], v[136:139]\n\t" \
  "s_waitcnt vmcnt(5)\n\t" \
  "v_mfma_f32_16x16x32_bf16 v[132:135], v[84:87], v[188:191], v[132:135]\n\t" \
  "v_mfma_f32_16x16x32_bf16 v[140:143], v[92:95], v[188:191], v[140:143]\n\t" \
  "s_waitcnt vmcnt(4)\n\t" \
  "v_mfma_f32_16x16x32_bf16 v[128:131], v[96:99], v[192:195], v[128:131]\n\t" \
  "v_mfma_f32_16x16x32_bf16 v[136:139], v[104:107], v[192:195], v[136:139]\n\t" \
  "s_waitcnt vmcnt(3)\n\t" \
  "v_mfma_f32_16x16x32_bf16 v[132:135], v[100:103], v[196:199], v[132:135]\n\t" \
  "v_mfma_f32_16x16x32_bf16 v[140:143], v[108:111], v[196:199], v[140:143]\n\t" \
  "s_waitcnt vmcnt(2)\n\t" \
  "v_mfma_f32_16x16x32_bf16 v[128:131], v[112:115], v[200:203], v[128:131]\n\t" \
  "v_mfma_f32_16x16x32_bf16 v[136:139], v[120:123], v[200:203], v[136:139]\n\t" \
  "s_waitcnt vmcnt(1)\n\t" \
  "v_mfma_f32_16x16x32_bf16 v[132:135], v[116:119], v[204:207], v[132:135]\n\t" \
  "v_mfma_f32_16x16x32_bf16 v[140:143], v[124:127], v[204:207], v[140:143]\n\t" \
  "s_nop 6\n\t" \
  "v_pk_add_f32 v[128:129], v[128:129], v[132:133]\n\t" \
  "v_pk_add_f32 v[130:131], v[130:131], v[134:135]\n\t" \
  "ds_write_b128 v230, v[128:131] offset:0\n\t" \
  "v_pk_add_f32 v[136:137], v[136:137], v[140:141]\n\t" \
  "v_pk_add_f32 v[138:139], v[138:139], v[142:143]\n\t" \
  "ds_write_b128 v230, v[136:139] offset:1024\n\t" \
  "s_waitcnt vmcnt(0) lgkmcnt(0)\n\t" \
  "s_branch .Lend%=\n\t" \
  ".Ltw%=:\n\t" \
  "s_add_u32 s92, s92, 1\n\t" \
  "s_sleep 2\n\t" \
  "s_and_b32 s93, s92, 63\n\t" \
  "s_cbranch_scc1 .Ltr%=\n\t" \
  "buffer_load_dword v164, off, %[rs], %[sab] sc1\n\t" \
  "s_waitcnt vmcnt(0)\n\t" \
  "v_readfirstlane_b32 s93, v164\n\t" \
  "s_nop 3\n\t" \
  "s_cmp_eq_u32 s93, 1\n\t" \
  "s_cbranch_scc1 .Lto%=\n\t" \
  "s_cmp_lt_u32 s92, 0x80000\n\t" \
  "s_cbranch_scc1 .Ltr%=\n\t" \
  "v_mov_b32 v164, 1\n\t" \
  "buffer_store_dword v164, off, %[rs], %[sab] sc1\n\t" \
  "s_waitcnt vmcnt(0)\n\t" \
  "s_branch .Lto%=\n\t" \
  ".Ltr%=:\n\t" \
  "buffer_load_dword v166, v242, %[rs], 0 offen sc1\n\t" \
  "s_waitcnt vmcnt(0)\n\t" \
  "s_branch .Ltc%=\n\t" \
  ".Llast%=:\n\t" \
  "v_mov_b32 v173, v242\n\t" \
  "s_branch .Lgol%=\n\t" \
  ".Lfw%=:\n\t" \
  "s_add_u32 s92, s92, 1\n\t" \
  "s_sleep 2\n\t" \
  "s_and_b32 s93, s92, 63\n\t" \
  "s_cbranch_scc1 .Lfr%=\n\t" \
  "buffer_load_dword v164, off, %[rs], %[sab] sc1\n\t" \
  "s_waitcnt vmcnt(0)\n\t" \
  "v_readfirstlane_b32 s93, v164\n\t" \
  "s_nop 3\n\t" \
  "s_cmp_eq_u32 s93, 1\n\t" \
  "s_cbranch_scc1 .Lfo%=\n\t" \
  "s_cmp_lt_u32 s92, 0x80000\n\t" \
  "s_cbranch_scc1 .Lfr%=\n\t" \
  "v_mov_b32 v164, 1\n\t" \
  "buffer_store_dword v164, off, %[rs], %[sab] sc1\n\t" \
  "s_waitcnt vmcnt(0)\n\t" \
  "s_branch .Lfo%=\n\t" \
  ".Lfr%=:\n\t" \
  "buffer_load_dwordx2 v[166:167], v173, %[rs], 0 offen sc1\n\t" \
  "s_branch .Lfc%=\n\t" \
  ".Lb1%=:\n\t" \
  "s_waitcnt vmcnt(0) lgkmcnt(0)\n\t" \
  "s_barrier\n\t" \
  "s_branch .Lb2%=\n\t" \
  ".Lbl%=:\n\t" \
  "s_bitset1_b64 %[ps], %[step]\n\t" \
  "s_cmp_lt_u32 s92, 3\n\t" \
  "s_cbranch_scc1 .Lbm%=\n\t" \
  "s_sleep 12\n\t" \
  ".Lbm%=:\n\t" \
  "s_add_u32 s92, s92, 1\n\t" \
  "s_sleep 2\n\t" \
  "s_and_b32 s93, s92, 63\n\t" \
  "s_cbranch_scc1 .Lba%=\n\t" \
  "buffer_load_dword v164, off, %[rs], %[sab] sc1\n\t" \
  "s_waitcnt vmcnt(0)\n\t" \
  "v_readfirstlane_b32 s93, v164\n\t" \
  "s_nop 3\n\t" \
  "s_cmp_eq_u32 s93, 1\n\t" \
  "s_cbranch_scc1 .Lgo%=\n\t" \
  "s_cmp_lt_u32 s92, 0x80000\n\t" \
  "s_cbranch_scc1 .Lba%=\n\t" \
  "v_mov_b32 v164, 1\n\t" \
  "buffer_store_dword v164, off, %[rs], %[sab] sc1\n\t" \
  "s_waitcnt vmcnt(0)\n\t" \
  "s_branch .Lgo%=\n\t" \
  ".Lba%=:\n\t" \
  "buffer_load_dwordx2 v[166:167], v173, %[rs], 0 offen sc1\n\t" \
  "s_branch .Lcal%=\n\t" \
  ".Lslow%=:\n\t" \
  "s_mov_b64 s[94:95], 0\n\t" \
  "v_cmp_eq_u32 vcc, %[sent], v128\n\t" \
  "s_or_b64 s[94:95], s[94:95], vcc\n\t" \
  "v_cmp_eq_u32 vcc, %[sent], v129\n\t" \
  "s_or_b64 s[94:95], s[94:95], vcc\n\t" \
  "v_cmp_eq_u32 vcc, %[sent], v130\n\t" \
  "s_or_b64 s[94:95], s[94:95], vcc\n\t" \
  "v_cmp_eq_u32 vcc, %[sent], v131\n\t" \
  "s_or_b64 s[94:95], s[94:95], vcc\n\t" \
  "v_cmp_eq_u32 vcc, %[sent], v132\n\t" \
  "s_or_b64 s[94:95], s[94:95], vcc\n\t" \
  "v_cmp_eq_u32 vcc, %[sent], v133\n\t" \
  "s_or_b64 s[94:95], s[94:95], vcc\n\t" \
  "v_cmp_eq_u32 vcc, %[sent], v134\n\t" \
  "s_or_b64 s[94:95], s[94:95], vcc\n\t" \
  "v_cmp_eq_u32 vcc, %[sent], v135\n\t" \
  "s_or_b64 s[94:95], s[94:95], vcc\n\t" \
  "v_cmp_eq_u32 vcc, %[sent], v136\n\t" \
  "s_or_b64 s[94:95], s[94:95], vcc\n\t" \
  "v_cmp_eq_u32 vcc, %[sent], v137\n\t" \
  "s_or_b64 s[94:95], s[94:95], vcc\n\t" \
  "v_cmp_eq_u32 vcc, %[sent], v138\n\t" \
  "s_or_b64 s[94:95], s[94:95], vcc\n\t" \
  "v_cmp_eq_u32 vcc, %[sent], v139\n\t" \
  "s_or_b64 s[94:95], s[94:95], vcc\n\t" \
  "v_cmp_eq_u32 vcc, %[sent], v140\n\t" \
  "s_or_b64 s[94:95], s[94:95], vcc\n\t" \
  "v_cmp_eq_u32 vcc, %[sent], v141\n\t" \
  "s_or_b64 s[94:95], s[94:95], vcc\n\t" \
  "v_cmp_eq_u32 vcc, %[sent], v142\n\t" \
  "s_or_b64 s[94:95], s[94:95], vcc\n\t" \
  "v_cmp_eq_u32 vcc, %[sent], v143\n\t" \
  "s_or_b64 s[94:95], s[94:95], vcc\n\t" \
  "s_cbranch_scc0 .Lok%=\n\t" \
  "s_bitset1_b64 %[fm], %[step]\n\t" \
  "s_add_u32 s92, s92, 1\n\t" \
  "s_sleep 1\n\t" \
  "s_and_b32 s93, s92, 63\n\t" \
  "s_cbranch_scc1 .Lre%=\n\t" \
  "buffer_load_dword v164, off, %[rs], %[sab] sc1\n\t" \
  "s_waitcnt vmcnt(0)\n\t" \
  "v_readfirstlane_b32 s93, v164\n\t" \
  "s_nop 3\n\t" \
  "s_cmp_eq_u32 s93, 1\n\t" \
  "s_cbranch_scc1 .Lok%=\n\t" \
  "s_cmp_lt_u32 s92, 0x80000\n\t" \
  "s_cbranch_scc1 .Lre%=\n\t" \
  "v_mov_b32 v164, 1\n\t" \
  "buffer_store_dword v164, off, %[rs], %[sab] sc1\n\t" \
  "s_waitcnt vmcnt(0)\n\t" \
  "s_branch .Lok%=\n\t" \
  ".Lre%=:\n\t" \
  "buffer_load_dwordx4 v[128:131], v234, %[rs], %[so] offen sc1\n\t" \
  "s_nop 15\n\t" \
  "buffer_load_dwordx4 v[132:135], v235, %[rs], %[so] offen sc1\n\t" \
  "s_nop 15\n\t" \
  "buffer_load_dwordx4 v[136:139], v236, %[rs], %[so] offen sc1\n\t" \
  "s_nop 15\n\t" \
  "buffer_load_dwordx4 v[140:143], v237, %[rs], %[so] offen sc1\n\t" \
  "v_mov_b64 v[144:145], 0\n\t" \
  "v_mov_b64 v[146:147], 0\n\t" \
  "v_mov_b64 v[148:149], 0\n\t" \
  "v_mov_b64 v[150:151], 0\n\t" \
  "v_mov_b64 v[152:153], 0\n\t" \
  "v_mov_b64 v[154:155], 0\n\t" \
  "v_mov_b64 v[156:157], 0\n\t" \
  "v_mov_b64 v[158:159], 0\n\t" \
  "s_waitcnt vmcnt(0)\n\t" \
  "s_branch .Lw%=\n\t" \
  ".Lend%=:\n\t"


__device__ __forceinline__ int look_prog(__amdgpu_buffer_rsrc_t rs, int off) {
  asm volatile("" ::: "memory");
  const int v = (int)ld_flag(rs, off);
  return min(__builtin_amdgcn_readlane(v, 0), __builtin_amdgcn_readlane(v, 1));
}
__device__ __forceinline__ int wait_prog(__amdgpu_buffer_rsrc_t rs, int off, int need, gu32* abortw) {
  int v;
  for (unsigned spins = 0;;) {
    v = look_prog(rs, off);
    if (v >= need) break;
    __builtin_amdgcn_s_sleep(4);
    if (spin_fail(spins, abortw)) break;
  }
  asm volatile("" ::: "memory");
  return v;
}
#define CLB_S "s90", "s91", "s92", "s93", "s94", "s95", "vcc", "scc", "memory"
#define W_IN "{v[0:31]}"(W[0]), "{v[32:63]}"(W[1]), "{v[64:95]}"(W[2]), "{v[96:127]}"(W[3]), "{v[128:159]}"(W[4]), "{v[160:191]}"(W[5])

#define LEAD0 408
#define LEAD_N0 5
#define LEAD_SH 2
#define LEAD_X0 8
#define LEAD_U0 24
#define LEAD_D0 0
#define T0_BACK 1000
#define QBJ 3
constexpr int L_MEM = 0, L_KT = 16384, L_QX = 24576, L_KX = 26624, L_VX = 16384, L_MISC = 30720, L_WL = 32768;

#define QK_STEP(TXT)                                                                                                        \
  asm volatile(TXT                                                                                                          \
      : [tout] "=&s"(tout), [spins] "=&s"(spins), [tb1] "=&s"(tb1), [tgt] "=&s"(tgt), "+{v248}"(aL16), "+{v249}"(aMW), "+{v250}"(aWL),                            \
        "+{v251}"(aIDX), "+{v252}"(aQW), "+{v[232:235]}"(QF)                                                                 \
      : W_IN, [rs] "s"(rs4), [som] "s"(o_mem(pm, s) + WV * 1024), [som2] "s"(o_mem(pm, s) + WV * 1024 + 16384), [sent] "s"(DSENT), [sab] "s"(SY_ABORT), \
        [lead] "s"(lead), [step] "s"(s), [sbc] "s"(SY_BA + side(pm, 1) + h * 128), [nh] "s"(0xFF00FF00FF00FF00ull), [up] "s"(0xFFFFFFFF00000000ull), [negc] "s"(-cexp),      \
        [c] "s"(cexp), [so] "s"(o_o(pm, s, h) + WV * 512), [sk] "s"(o_v(pm, s, h) + 2048 + (WV & 3) * 512),                    \
        [sv] "s"(o_v(pm, s, h) + WV * 512), [rt] "s"(rt4), [svt] "s"(t_off(p, T_V + kseg(s, h)) + WV * 2048)                  \
      : CLB8(192, 193, 194, 195, 196, 197, 198, 199), CLB8(200, 201, 202, 203, 204, 205, 206, 207),                          \
        CLB8(208, 209, 210, 211, 212, 213, 214, 215), CLB8(216, 217, 218, 219, 220, 221, 222, 223),                          \
        CLB8(224, 225, 226, 227, 228, 229, 230, 231), CLB8(236, 237, 238, 239, 240, 241, 242, 243), "v244", "v245", "v246", "v247", "v253", "s88", "s89", CLB_S)

template <int WV>
__device__ __forceinline__ void qk_wave(Args& p, int h, char* smem) {
  const int tid = threadIdx.x;
  unsigned pm = 0;
  int have = -1;
  u32x32 W[6];
  {
    int lane = tid & 63;
    asm volatile("" : "+v"(lane));
    const unsigned vo = (WV * 4 + (lane >> 4)) * (HD * 2) + 16 * ((lane & 15) ^ swzB((WV * 4 + (lane >> 4)) & 15));
    auto stage = [&](int j) {
#pragma unroll
      for (int mm = 0; mm < 2; ++mm) {
        const char* src = (const char*)((mm ? p.Wk : p.Wq) + (size_t)j * 128 * HD + h * DH);
        const unsigned dst = lds_off(smem) + (~j & 1) * 65536 + mm * 32768 + WV * 1024;
        glds2(src, 32 * HD * 2, vo, dst);
        if (TH_Q < 63) HOLD_V((h & 3) ^ 1, TH_Q);
        glds2(src + 64 * HD * 2, 32 * HD * 2, vo, dst + 16384);
        if (TH_Q < 63) HOLD_V((h & 3) ^ 1, TH_Q);
      }
    };
    bf16x8 keep[4][4];
    int f0 = -1;
    auto place = [&](int g) {
#pragma unroll
      for (int i = 0; i < 4; ++i)
        *reinterpret_cast<bf16x8*>(smem + (g == 0 ? 65536 : g == 1 ? 32768 : g == 2 ? 131072 : 98304) + WV * 4096 + i * 1024 + lane * 16) = keep[g][i];
    };
    stage(0); stage(1);
    Boot bt;
#pragma unroll
    for (int j = 0; j < 8; ++j) {
      if (j < 7) WAIT_V(8); else WAIT_V(0);
      if (WV == 7 && j == QBJ + 1) pm = boot_done(p.ws, bt);
      if ((WV == 4 || WV == 5) && j == 7) {
        asm volatile("" : "+v"(f0));
        have = min(__builtin_amdgcn_readlane(f0, 0), __builtin_amdgcn_readlane(f0, 1));
      }
      LBAR();
#pragma unroll
      for (int k = 0; k < 8; ++k) {
        const int P = (QK_MAP[WV] >> (4 * (2 * j + (k >> 2)))) & 15;
        const bf16x8 fr = ld_bfrag(smem + (~j & 1) * 65536 + (WV >> 2) * 32768 + (k >> 2) * 16384, k & 1, 2 * (WV & 3) + ((k >> 1) & 1), lane);
        const int f = P * 4 + (k & 3);
        if (P < 12) put4(W[f >> 3], f & 7, fr);
        else keep[P - 12][k & 3] = fr;
      }
      if (j == (WV >> 1)) place(2);
      LBAR();
      if (WV == 7 && j == QBJ) boot_ask(p.ws, bt);
      if (j + 2 < 8) stage(j + 2);
      if (j == 6) {
        if (WV == 4 || WV == 5)
          asm volatile("buffer_load_dword %0, %1, %2, 0 offen sc1" : "=&v"(f0) : "v"(fkq(h, (lane & 1) * 2)), "s"(raw_rsrc(p.ws + WS_SY, SY_TOTAL)) : "memory");
        place(0); place(3);
      }
    }
    place(1);
    if (WV == 7 && lane < 8) lds_at(smem + L_MISC)[lane] = lane == 7 ? pm : (lane == 1 ? now_q() - T0_BACK : 0);
    if (WV == 6) lds_at(smem + 28672)[lane] = ~0u;
    LBAR();
    pm = uni(lds_at(smem + L_MISC)[7]);
    p.s0 = pm >> 31;
    if (WV == 4 || WV == 5) {
      if (have < 0) have = wait_prog(mkrsrc(p.ws + WS_SY, SY_TOTAL), fkq(h, (lane & 1) * 2), 0, (gu32*)(p.ws + WS_SY + SY_ABORT));
      const char* src = p.ws + WS_T + t_off(p, kseg(0, h)) + (WV - 4) * 4096 + lane * 16;
#pragma unroll
      for (int i = 0; i < 4; ++i) glds16_sc1(src + i * 1024, smem + L_KT + (WV - 4) * 4096 + i * 1024);
    }
  }
  const __amdgpu_buffer_rsrc_t rsy = mkrsrc(p.ws + WS_SY, SY_TOTAL);
  const u32x4 rs4 = raw_rsrc(p.ws + WS_SY, SY_TOTAL);
  const u32x4 rt4 = raw_rsrc(p.ws + WS_T, T_BYTES);
  gu32* abortw = (gu32*)(p.ws + WS_SY + SY_ABORT);
  const float cexp = 0.08838834764831845f * 1.4426950408889634f;
  constexpr int m = WV >> 2, b = WV & 3;
  int lead = LEAD0;
  unsigned tb1 = 0;
  unsigned long long fm = 0, ps = 0; (void)fm; (void)ps;
  for (int s = 0; s < NSEG; ++s) {
    int lane = tid & 63;
    asm volatile("" : "+v"(lane));
    const int r = lane & 15, q = lane >> 4;
    unsigned aL16 = lane * 16, aMW = aL16 + WV * 1024, aWL = aL16 + 98304 + WV * 4096;
    unsigned aIDX = (lane & 2) ? 0xEEEE : 0x4444;
    unsigned aQW = (32 * b + r) * 16 + q * 4 + (m ? L_KX : L_QX);
    u32x4 QF;
    asm volatile("" : "={v[232:235]}"(QF));
    if (WV < 4) {
      QF[0] = L_QX + (q * 8 + (r >> 2)) * 16 + (r & 1) * 8;
      QF[1] = ((2 * (q & 1) + (q >> 1)) * 16 + r) * 4;
      QF[2] = ((r >> 2) & 1) * 65536 + ((r >> 1) & 1) * 64 + (r >> 3) * 32 + (r & 1) * 16 + (q >> 1) * 256 + (q & 1) * 8;
    } else QF[3] = r * 16 + q * 4;
    unsigned tout, spins, tgt;
    if (WV == 0) QK_STEP(QK_ASM_0); else if (WV == 1) QK_STEP(QK_ASM_1); else if (WV == 2) QK_STEP(QK_ASM_2);
    else if (WV == 3) QK_STEP(QK_ASM_3); else if (WV == 4) QK_STEP(QK_ASM_4); else if (WV == 5) QK_STEP(QK_ASM_5);
    else if (WV == 6) QK_STEP(QK_ASM_6); else QK_STEP(QK_ASM_7);
    tout = __builtin_amdgcn_readfirstlane(tout);
    tb1 = __builtin_amdgcn_readfirstlane(tb1);
    lane = tid & 63;
    asm volatile("" : "+v"(lane));
    if (WV < 4) {
      lds_at(smem + L_MISC + 8)[WV] = s + 1;
      if (WV == 3) { lds_at(smem + L_MISC)[6] = time_of(tout); lds_at(smem + 28672)[2] = s; }
    }
    if (WV == 6) {
      if (s + 1 < NSEG) watch(rsy, s, lane, now_q(), (const unsigned*)(smem + L_MISC), abortw, side(pm, 1));
    } else if (WV >= 4 && s + 1 < NSEG) {
      unsigned t;
      do {
        __builtin_amdgcn_s_sleep(1);
        t = lds_at(smem + L_MISC + 8)[lane & 3];
      } while (!__all(t == (unsigned)(s + 1)));
      if (WV <= 5) {
        if (s + 2 < NSEG) {
          if (have < s + 1) have = wait_prog(rsy, fkq(h, (lane & 1) * 2), s + 1, abortw);
          const char* src = p.ws + WS_T + t_off(p, kseg(s + 1, h)) + (WV - 4) * 4096 + lane * 16;
#pragma unroll
          for (int i = 0; i < 4; ++i) glds16_sc1(src + i * 1024, smem + L_KT + (WV - 4) * 4096 + i * 1024);
          if (have < s + 2) have = look_prog(rsy, fkq(h, (lane & 1) * 2));
        }
      } else {
        do __builtin_amdgcn_s_sleep(2); while (__builtin_amdgcn_readfirstlane(lds_at(smem + 28672)[2]) != (unsigned)s);
        const int x = (int)(__builtin_amdgcn_readfirstlane(lds_at(smem + L_MISC)[6]) - tb1);
        lead = __builtin_amdgcn_readfirstlane(track(lead, x + (s < LEAD_N0 ? LEAD_X0 : 0), s < LEAD_N0 ? LEAD_U0 : LEAD_UP, s < LEAD_N0 ? LEAD_D0 : (s < 3 * LEAD_N0 ? min(max(LEAD_DN, (lead - x) >> LEAD_SH), 16) : LEAD_DN), 60));
      }
    }
  }
}

__device__ __forceinline__ void qk_role(Args& p, int h, char* smem) {
  switch (__builtin_amdgcn_readfirstlane(threadIdx.x >> 6)) {
    case 0: qk_wave<0>(p, h, smem); break;
    case 1: qk_wave<1>(p, h, smem); break;
    case 2: qk_wave<2>(p, h, smem); break;
    case 3: qk_wave<3>(p, h, smem); break;
    case 4: qk_wave<4>(p, h, smem); break;
    case 5: qk_wave<5>(p, h, smem); break;
    case 6: qk_wave<6>(p, h, smem); break;
    default: qk_wave<7>(p, h, smem); break;
  }
}

#define K_DUE 50
#define K_DUE2 500
#define T_SL 2
#define O_DUE 400

constexpr int V_VM = 16384, V_T = 32768, V_KI = 131072;
#define TK_CALL(sg, od_) \
  asm volatile(TK_ASM \
            : \
            : "{v200}"(lane * 16), "{v[208:215]}"(bias), "{v[216:219]}"(ta), \
              [rs] "s"(rs4), [rt] "s"(raw_rsrc(p.ws + WS_T, T_BYTES)), [sent] "s"(DSENT), [sab] "s"(SY_ABORT), [sk] "s"(o_v(pm, sg, h) + 2048), \
              [tl] "s"(V_T + (sg & 3) * 24576), [vp] "s"((sg & 1) * 2048), [tq] "s"((w & 1) * 4096), [tv] "s"((w & 2) * 2048), \
              [so] "s"(t_off_o(p, T_O + (sg * 2 + (w & 1)) * 16 + (h >> 1)) + (h & 1) * 4096 + (w & 2) * 1024), \
              [up] "s"(0xFFFFFFFF00000000ull), [c] "s"(1.4426950408889634f), [negc] "s"(-1.4426950408889634f), \
              [sc] "s"((unsigned long long)__float_as_uint(0.08838834764831845f)), [od] "s"(od_) \
            : CLB32(128), CLB32(160), CLB8(192, 193, 194, 195, 196, 197, 198, 199), CLB8(224, 225, 226, 227, 228, 229, 230, 231), \
              "v205", "v206", "v207", "s88", "s89", CLB_S)
__device__ __forceinline__ void vu_role(Args& p, int h, char* smem) {
  const int tid = threadIdx.x, lane = tid & 63;
  const int w = __builtin_amdgcn_readfirstlane(tid >> 6);
  u32x32 W[4];
  {
    const unsigned vo = (tid >> 4) * (HD * 2) + 16 * ((lane & 15) ^ swzB((tid >> 4) & 15));
    auto stage = [&](int j) {
      const char* src = (const char*)(p.Wv + (size_t)j * 128 * HD + h * DH);
      const unsigned dst = lds_off(smem) + L_WL + (j & 3) * 32768 + w * 1024;
      glds2(src, 32 * HD * 2, vo, dst);
      glds2(src + 64 * HD * 2, 32 * HD * 2, vo, dst + 16384);
    };
    start_sy(p.ws, p.mem0, h, smem);
    Boot bt;
#pragma unroll
    for (int u = 0; u < 2; ++u) {
#pragma unroll
      for (int j = 0; j < 4; ++j) stage(4 * u + j);
      if (u && w == 7) boot_ask(p.ws, bt);
      WAIT_V(0);
      if (u && w == 7) {
        const unsigned m = boot_done(p.ws, bt);
        if (lane < 8) lds_at(smem + L_MISC)[lane] = lane == 7 ? m : (lane == 1 ? now_q() : 0);
      }
      LBAR();
#pragma unroll
      for (int k = 0; k < 16; ++k) put4(W[2 * u + (k >> 3)], k & 7, ld_bfrag(smem + L_WL + ((k >> 1) ^ w) * 16384, k & 1, w, lane));
      LBAR();
    }
  }
  const __amdgpu_buffer_rsrc_t rsy = mkrsrc(p.ws + WS_SY, SY_TOTAL);
  const u32x4 rs4 = raw_rsrc(p.ws + WS_SY, SY_TOTAL);
  gu32* abortw = (gu32*)(p.ws + WS_SY + SY_ABORT);
  const unsigned pm = uni(lds_at(smem + L_MISC)[7]);
  p.s0 = pm >> 31;
  unsigned long long fm = 0; (void)fm;
  int have = -1;
  auto fetch = [&](int sg) {
    if (have < sg) have = wait_prog(rsy, fkq(h, w - 4), sg, abortw);
    const char* src = p.ws + WS_T + t_off(p, (w - 4) * T_Q + kseg(sg, h)) + lane * 16;
#pragma unroll
    for (int i = 0; i < 8; ++i) glds16_sc1(src + i * 1024, smem + V_T + (sg & 3) * 24576 + (w - 4) * 8192 + i * 1024);
  };
  const int fo = SY_FO + ((w & 3) * NH + h) * 128;
  typedef __attribute__((ext_vector_type(8))) float f32x8;
  f32x8 bias;
  typedef __attribute__((ext_vector_type(8))) unsigned u32x8;
  u32x8 ca;
#pragma unroll
  for (int i = 0; i < 8; ++i) ca[i] = lane * 16 + (i ^ w) * 1024;
  u32x4 ta;
  {
    const int r = lane & 15, q = lane >> 4;
#pragma unroll
    for (int j = 0; j < 8; ++j) bias[j] = (j >> 2) * 16 + 4 * q + (j & 3) <= 16 * (w & 1) + r ? INFINITY : -INFINITY;
    ta[0] = V_KI + w * 2048 + lane * 16;
    ta[1] = V_KI + w * 2048 + (q * 8 + (r >> 2)) * 16 + (r & 1) * 8;
    ta[2] = V_VM + (w & 2) * 512 + ((2 * (q & 1) + (q >> 1)) * 16 + r) * 4;
    ta[3] = 0;
  }
#pragma unroll 1
  for (int s = 0; s < NSEG; ++s) {
    unsigned spins, tgt;
    asm volatile(VV_ASM
        : [spins] "=&s"(spins), [tgt] "=&s"(tgt)
        : W4_IN, "{v200}"(lane * 16), "{v[232:239]}"(ca), "{v202}"((lane & 2) ? 0xEEEE : 0x4444), "{v203}"(lane * 4),
          "{v204}"(tid * 4 + V_VM + (s & 1) * 2048), [rs] "s"(rs4), [som] "s"(o_mem(pm, s) + w * 1024), [som2] "s"(o_mem(pm, s) + w * 1024 + 16384), [step] "s"(s),
          [sent] "s"(DSENT), [sab] "s"(SY_ABORT), [sv] "s"(o_v(pm, s, h) + w * 256)
        : CLB32(128), CLB32(160), CLB8(192, 193, 194, 195, 196, 197, 198, 199), CLB_S);
    if (w < 4 || s == NSEG - 1) {
      const int sg = s - (w < 4);
      if (w >= 4) {
        if (lane == 0) __builtin_amdgcn_raw_buffer_store_b32(1u, rsy, fo + 4 * NH * 128, 0, 16);
        pace_until(now_q() + K_DUE);
      }
      if (sg >= 0 && sg != NSEG - 2) {
        TK_CALL(sg, s < NSEG - 1 ? O_DUE : 0);
        if (sg >= NSEG - 3) {
          static_assert(TLAG == 2, "");
          WAIT_V(0);
          if (lane == 0) __builtin_amdgcn_raw_buffer_store_b32(1u, rsy, fo + (sg - NSEG + 3) * 4 * NH * 128, 0, 16);
        }
      }
    } else if (w == 7) {
      watch(rsy, s, lane, now_q(), (const unsigned*)(smem + L_MISC), abortw, side(pm, 1));
    } else if (w >= 4) {
      __builtin_amdgcn_s_sleep(T_SL);
      const int s1 = w == 5 && s < NSEG - 3 ? s : s + 1;
#pragma unroll 1
      for (int sg = (w == 5 ? s < NSEG - 2 : s == 0) ? s : s + 1; sg <= s1; ++sg) fetch(sg);
      if (have < s1 + 1 && s1 + 1 < NSEG) have = look_prog(rsy, fkq(h, w - 4));
      if (w == 5 && s + NPRE < NSEG) {
#pragma unroll
        for (int i = 0; i < 4; ++i) st_sc1(rsy, o_v(pm, s + NPRE, h) + i * 1024 + lane * 16, (u32x4){DSENT, DSENT, DSENT, DSENT});
      }
    }
    if (s == NSEG - 2 && w >= 4) {
      pace_until(tgt + K_DUE2);
      TK_CALL(s, 0);
    }
  }
}

#define RL_UP 6
#define RL_DN 4
#define RED_MIN 40
#define BA_LOOK 360
constexpr int R_SI = 32768, R_PART = 98304;

__device__ __forceinline__ void red_role(Args& p, int j, char* smem) {
  const int tid = threadIdx.x;
  int lane = tid & 63;
  const int w = __builtin_amdgcn_readfirstlane(tid >> 6);
  const int c = (j & 2) * 8 + (j >> 2), rh = j & 1;
  const int hg = (w + (j >> 2)) & 7, rot = (j >> 2) + (j >> 5);
  const __amdgpu_buffer_rsrc_t rsy = mkrsrc(p.ws + WS_SY, SY_TOTAL);
  const u32x4 rs4 = raw_rsrc(p.ws + WS_SY, SY_TOTAL);
  const __amdgpu_buffer_rsrc_t rt = mkrsrc(p.ws + WS_T, T_BYTES);
  gu32* abortw = (gu32*)(p.ws + WS_SY + SY_ABORT);
  u32x32 W[4];
  u32x2 tag = {0u, 0u};
  {
    const u16* Wo = p.Wo + (size_t)(4 * hg * DH) * DM + 32 * c;
    const int ro = (lane >> 2) * 64 + (lane & 3) * 8;
    Boot bt;
    load_blocks<16>(smem + 32768 + w * 16384,
        [&](int i, char* slot) {
          if (i == 12 && w == 0) boot_ask(p.ws, bt);
          if ((i < 2 || i == 4 || i == 8) && (c & 14) != 6 && lane < 10) {
            const int x = i < 2 ? 3 - i : i >> 3, n = min(((c & 15) - ((c & 8) >> 2)) * 10 + lane, 127);
            touch_line((const char*)p.Wo + (size_t)(w * 512 + (x >> 1) * 256 + (x & 1) * 64 + (n & 64) + n) * (DM * 2) + (c & 16) * 64 + 384, smem + w * 256);
          }
          const int x = ((i >> 2) + rot) & 3;
          blk_dma(Wo + (size_t)((x >> 1) * 256 + (x & 1) * 64 + ((i >> 1) & 1) * 128 + (i & 1) * 32) * DM, DM, slot, lane);
        },
        [&](int i, char* slot) {
#pragma unroll
          for (int t = 0; t < 2; ++t) {
            s16x4 lo = __builtin_amdgcn_ds_read_tr16_b64_v4i16((lds_s16x4_ptr)(slot + ro + t * 32));
            s16x4 hi = __builtin_amdgcn_ds_read_tr16_b64_v4i16((lds_s16x4_ptr)(slot + ro + t * 32 + 1024));
            put4(W[i >> 2], (i & 2) * 2 + 2 * t + (i & 1), (bf16x8){lo[0], lo[1], lo[2], lo[3], hi[0], hi[1], hi[2], hi[3]});
          }
        });
    if (w == 0) { lds_at(smem + 4128)[0] = boot_done(p.ws, bt); tag = next_tag(bt.e); }
    LBAR();
  }
  const unsigned pm = uni(lds_at(smem + 4128)[0]);
  p.s0 = pm >> 31;
  const int hs = side(pm, 1);
  {
    const u32x4 ds = {DSENT, DSENT, DSENT, DSENT};
#pragma unroll
    for (int i = 0; i < 2; ++i) {
      const int s = (w * 2 + i) * 4 + (lane >> 4);
      if (s >= 2) st_sc1(rsy, o_mem(pm, s) + (c >> 4) * 16384 + (c & 15) * 512 + rh * 128 + ((lane >> 3) & 1) * 256 + (lane & 7) * 16, ds);
    }
  }
  if (tid < 4) { reinterpret_cast<unsigned*>(smem + 4096)[tid] = tid == 3 ? now_q() : 0; reinterpret_cast<unsigned*>(smem + 4112)[tid] = DSENT; }
  if (tid < 256) reinterpret_cast<int*>(smem + 5120)[tid] = ((tid >> 2) - TLAG - 6) >> 2;
  LBAR();
  const unsigned base = __builtin_amdgcn_readfirstlane(lds_at(smem + 4096)[3]);
  unsigned long long fm = 0, ps = 0;
  const unsigned aL = lane * 16;
  const int e = ((56 + w) * SEG + rh * 16 + (lane >> 2)) * DM + c * 32 + (lane & 3) * 8;
  u32x4 xv = *reinterpret_cast<const u32x4*>(p.x + e), av = {DSENT, DSENT, DSENT, DSENT};
  asm volatile("" : "+v"(xv));
  {
    const int r = lane & 15, q = lane >> 4;
    const int sm0 = o_mem(pm, 0) + (c >> 4) * 16384 + (c & 15) * 512 + rh * 128;
    u32x4 VO, VT;
    unsigned fo = SY_FO + ((rh + (lane & 4) / 2) * NH + 4 * hg + (lane & 3)) * 128;
    unsigned ao = w == 1 ? (c * S_LEN + rh * 16 - (TLAG + 3) * SEG) * 64 + lane * 16
                         : (w >= 4 ? SY_O + NPRE * 131072 + side(pm, w - 2) + (j >> 3) * 16384 + (j & 7) * 1024 + lane * 16
                                   : SY_FS + (2 * c + rh) * 128 + (lane < 8 ? lane * 16 : 0x70000000));
    unsigned ar = w == 1 ? R_SI - (TLAG + 2) * 1024 + lane * 16 : (w == 2 ? 5120 : 4112);
    const unsigned long long am = w == 1 ? ((1ull << 56) - 1) << (TLAG + 3) : (w == 2 ? 0x11111111111111ull << (TLAG + 7) : (w >= 4 ? (0xFFFFull << (16 * (w - 4))) >> NPRE : 0));
    unsigned pw = R_PART + w * 2048 + lane * 16, pr = R_PART + 16384 + tid * 4;
    unsigned sw = R_SI - (TLAG + 1) * 1024 + (lane >> 2) * 64 + w * 8 + (lane & 3) * 2, stm = 8192 + w * 512;
#pragma unroll
    for (int l = 0; l < 4; ++l) {
      const int x = (l + rot) & 3;
      VO[l] = o_o(0, 0, 4 * hg) + rh * 65536 + x * 1024 + aL;
      VT[l] = (T_O + rh * 16 + 2 * hg + (x >> 1)) * 16384 + (x & 1) * 2048 + aL;
    }
    unsigned ba = lane < 4 ? SY_BA + hs + (4 * hg + lane) * 128 : 0x7F000000u;
    unsigned sm = 0;
    int rl = RL0;
    unsigned step, E, so, st, st4, sh;
    asm volatile(RED_ASM
        : [step] "=&s"(step), [E] "=&s"(E), [so] "=&s"(so), [st] "=&s"(st), [st4] "=&s"(st4), [sh] "=&s"(sh),
          [rl] "+s"(rl), [sm] "+s"(sm), [fm] "+s"(fm), [ps] "+s"(ps),
          "+{v[224:227]}"(av), "+{v228}"(ao), "+{v229}"(ar), "+{v230}"(pw), "+{v231}"(pr), "+{v232}"(sw), "+{v233}"(stm), "+{v242}"(fo), "+{v173}"(ba)
        : W4_IN, "{v168}"(aL), "{v169}"(w * 512 + lane * 8), "{v170}"(lane * 8), "{v174}"((r & 1) ? 0x03020706u : 0x05040100u),
          "{v171}"(sm0 + 32768 + (q >> 1) * 256 + (q & 1) * 64 + ((r >> 1) & 1) * 32 + (r & 1) * 16 + (r >> 2) * 4),
          "{v172}"((lane & 2) ? 0xEEEE : 0x4444),
          "{v175}"(w == 3 && lane < 32 ? SY_BR + hs + j * 128 + lane * 4 : 0x7F000000u), "{v[234:237]}"(VO), "{v[238:241]}"(VT), "{v243}"(e * 2),
          [rg] "s"(raw_rsrc(p.ws + WS_G, S_LEN * DM * 2)), [fg] "s"(SY_FG + ((14 + (w >> 2)) * 8 + (c >> 2)) * 128),
          [rs] "s"(rs4), [ra] "s"(w == 1 ? raw_rsrc(p.ws + WS_S, S_LEN * DM * 2) : rs4), [rt] "s"(raw_rsrc(p.ws + WS_T, T_BYTES)),
          [ds] "n"(DS_RED), [wm] "n"(REF_W), [gm] "n"(REF_G), [mn] "n"(RED_MIN), [lk] "n"(DS_MEM + BA_LOOK), [up] "n"(RL_UP), [dn] "n"(-RL_DN), [sent] "s"(DSENT), [sab] "s"(SY_ABORT),
          [base] "s"(base), [pm] "s"(pm), [s0] "s"((p.s0 ^ TF_O) << 13), [am] "s"(am), [ainc] "s"(w == 1 ? SEG * 64 : (w >= 4 ? 131072 : 0)),
          [rinc] "s"(w == 1 ? 1024 : (w == 2 ? 16 : 0))
        : CLB32(128), CLB8(160, 161, 162, 163, 164, 165, 166, 167), CLB8(176, 177, 178, 179, 180, 181, 182, 183),
          CLB8(184, 185, 186, 187, 188, 189, 190, 191), CLB8(192, 193, 194, 195, 196, 197, 198, 199), CLB8(200, 201, 202, 203, 204, 205, 206, 207),
          CLB8(208, 209, 210, 211, 212, 213, 214, 215), CLB8(216, 217, 218, 219, 220, 221, 222, 223), CLB_E, "s96", CLB_S);
  }
  LBAR();
  u32x4 sv;
  if (w < 6 - TLAG) sv = *reinterpret_cast<const u32x4*>(smem + R_SI + (56 + w) * 1024 + lane * 16);
  else {
    const f32x4* part = reinterpret_cast<const f32x4*>(smem + (w == 6 - TLAG ? R_PART : (w - 6 + TLAG) * 16384)) +
                        (lane & 2) * 32 + (lane & 1) * 32 + (lane >> 2);
    f32x4 x[8][2];
#pragma unroll
    for (int ww = 0; ww < 8; ++ww) { x[ww][0] = part[ww * 128]; x[ww][1] = part[ww * 128 + 16]; }
#pragma unroll
    for (int u = 0; u < 2; ++u) {
      const f32x4 a = ((x[0][u] + x[1][u]) + (x[2][u] + x[3][u])) + ((x[4][u] + x[5][u]) + (x[6][u] + x[7][u]));
      sv[2 * u] = cvtpk(a[0], a[1]); sv[2 * u + 1] = cvtpk(a[2], a[3]);
    }
  }
  st_out(p, e, combine(sv, av, xv));
  if (j == 0 && tid == 0) st2_sc1(rsy, SY_EP, tag);
}


__global__ __launch_bounds__(512, 2) void main_kernel(Args p) {
  char* smem = g_smem;
  p.ws = ws_align(p.ws);
  const int bx = blockIdx.x & 7, bk = blockIdx.x >> 3;
  const bool isg = (bx ^ XPROBE) & 4;
  const int b = (isg ? NCHAIN : 0) + bk * 4 + (bx & 3);
  if (threadIdx.x < 64) {
    unsigned long long pc; unsigned len, keep;
    asm volatile("s_getpc_b64 %0\n.Lpc%=:\n\ts_mov_b32 %1, .Lcode_end-.Lpc%=" : "=s"(pc), "=s"(len));
    asm volatile("s_mov_b32 %0, m0\n\ts_mov_b32 m0, %3\n\ts_nop 0\n\tbuffer_load_dword %1, %2, 0 offen lds\n\ts_mov_b32 m0, %0"
                 : "=&s"(keep) : "v"((bk * 40 + threadIdx.x) * 128), "s"(raw_rsrc((const void*)pc, len)), "s"((b >> 6) == 1 ? 2048 : 163328) : "memory");
  }
  if ((bk >> 3) == (isg ? 3 : 1) && (threadIdx.x >> 6) == 1) {
    constexpr int NI = TLB_NI + 1, NW = (int)(WS_END / TLB_WS);
    const int n = (threadIdx.x & 63) * 8 + (bk & 7), m = n / NI, i = n % NI;
    const u16* q = m == 0 ? p.Wq : m == 1 ? p.Wk : m == 2 ? p.Wv : m == 3 ? p.Wo : m == 4 ? p.wg : m == 5 ? p.wu : m == 6 ? p.wd : m == 7 ? p.x : p.out;
    static_assert(9 * NI + NW <= 512, "");
    if (n < 9 * NI + NW)
      touch_line(m < 9 ? (const char*)q + min(i * (DM * HD * 2 / TLB_NI), (m < 4 ? HD : m < 7 ? FF : S_LEN) * DM * 2 - 128)
                       : p.ws + (size_t)(n - 9 * NI + 1) * TLB_WS - 16384, smem + 163072);
  }
  if (b >= NCHAIN) gemm_role(p, b - NCHAIN, smem);
  else if (b < 32) qk_role(p, (b & 3) * 8 + bk, smem);
  else if (b < 64) vu_role(p, (b & 3) * 8 + bk - 8, smem);
  else red_role(p, b - 64, smem);
}

extern "C" void kernel_launch(void* const* d_in, const int* in_sizes, int n_in,
                              void* d_out, int out_size, void* d_ws, size_t ws_size,
                              hipStream_t stream) {
  Args a;
  a.x = (const u16*)d_in[0]; a.mem0 = (const u16*)d_in[1];
  a.Wq = (const u16*)d_in[2]; a.Wk = (const u16*)d_in[3]; a.Wv = (const u16*)d_in[4]; a.Wo = (const u16*)d_in[5];
  a.wg = (const u16*)d_in[8]; a.wu = (const u16*)d_in[9]; a.wd = (const u16*)d_in[10];
  a.ws = (char*)d_ws; a.out = (u16*)d_out; a.s0 = 0; a.pad1 = 0;
  main_kernel<<<256, 512, 0, stream>>>(a);
}
```

```cpp
#include <hip/hip_runtime.h>
#include <hip/hip_bf16.h>
#include <stdint.h>

constexpr int S_LEN = 2048, DM = 1024, NH = 32, DH = 128, NMEM = 8, SEG = 32;
constexpr int NSEG = S_LEN / SEG;
constexpr int HD = NH * DH;
constexpr int FF = 3584;

typedef unsigned short u16;
typedef __attribute__((ext_vector_type(8))) short bf16x8;
typedef __attribute__((ext_vector_type(4))) short s16x4;
typedef __attribute__((ext_vector_type(4))) float f32x4;
typedef __attribute__((ext_vector_type(4))) unsigned int u32x4;
typedef __attribute__((ext_vector_type(2))) unsigned int u32x2;
typedef __attribute__((address_space(3))) s16x4* lds_s16x4_ptr;
typedef __attribute__((address_space(3))) void* lds_void_ptr;
typedef __attribute__((address_space(1))) unsigned gu32;
typedef __bf16 bf16x2_t __attribute__((ext_vector_type(2)));
typedef float f32x2_t __attribute__((ext_vector_type(2)));

__device__ __forceinline__ unsigned cvtpk(float lo, float hi) {
  bf16x2_t r = __builtin_convertvector((f32x2_t){lo, hi}, bf16x2_t);
  return __builtin_bit_cast(unsigned, r);
}
__device__ __forceinline__ float lo_f(unsigned v) { return __uint_as_float(v << 16); }
__device__ __forceinline__ float hi_f(unsigned v) { return __uint_as_float(v & 0xffff0000u); }
__device__ __forceinline__ float rbf(float f) { return lo_f(cvtpk(f, f)); }
__device__ __forceinline__ bf16x8 as_bf16x8(u32x4 v) { return __builtin_bit_cast(bf16x8, v); }
#define MFMA(a, b, c) __builtin_amdgcn_mfma_f32_16x16x32_bf16((a), (b), (c), 0, 0, 0)

#define KSTR 65
#define XLD 1024
constexpr int PAGE = 2 << 20, NPAGE = 14;
constexpr int T_K = 0, T_Q = 2304, T_V = 4608, T_O = 6912, T_END = T_O + 2048;
constexpr unsigned T_BYTES = T_END * 16384u;
constexpr size_t WS_SY = 0;
constexpr size_t WS_T = (size_t)NPAGE * PAGE;
constexpr size_t WS_XN = WS_T + T_BYTES;
constexpr size_t WS_HF = WS_XN + (size_t)S_LEN * XLD * 2;
constexpr size_t WS_G = WS_HF + (size_t)S_LEN * 16384;
constexpr size_t WS_S = WS_G + (size_t)S_LEN * DM * 2;
constexpr size_t WS_END = WS_S + (size_t)S_LEN * DM * 2;
constexpr unsigned SENT = 0xFFFFFFFFu;
constexpr unsigned DSENT = 0xFFBFFFBFu;
constexpr int SY_MEM = 0;
constexpr int SY_O = 2 * PAGE;
constexpr int SY_V = 6 * PAGE;
constexpr int SY_BR = PAGE, SY_BA = SY_BR + 16384;
constexpr int SY_FKQ = SY_BR + 32768;
constexpr int SY_FG = SY_FKQ + NH * 4 * 128;
constexpr int SY_FO = SY_FG + 16 * 8 * 128;
constexpr int SY_CH = SY_FO + 384 * 128;
constexpr int SY_ABORT = SY_CH + 8 * 128;
constexpr int SY_FS = SY_ABORT + 256;
constexpr int SY_FEND = SY_FS + 64 * 128;
constexpr int SY_EP = SY_FEND, SY_BT = SY_EP + 128;
constexpr int SY_TOTAL = NPAGE * PAGE;
constexpr int PROBE = PAGE - 16384 + 6144;
__device__ __forceinline__ int side(unsigned pm, int page) { return ((pm >> page) & 1u) << 13; }
__device__ __forceinline__ int o_mem(unsigned pm, int s) { return SY_MEM + s * 32768 + side(pm, 0); }
__device__ __forceinline__ int o_o(unsigned pm, int s, int h) {
  return SY_O + s * 131072 + (h >> 3) * 16384 + ((h >> 1) & 3) * 2048 + (h & 1) * 128 + side(pm, 2 + (s >> 4));
}
__device__ __forceinline__ int o_v(unsigned pm, int s, int h) { return SY_V + s * 262144 + (h >> 1) * 16384 + (h & 1) * 4096 + side(pm, 6 + (s >> 3)); }
__device__ __forceinline__ char* ws_align(char* ws) { return ws + ((PAGE - (unsigned)(uintptr_t)ws) & (PAGE - 1)); }
#define NFUSE 12
constexpr int NPRE = 4;
static_assert(WS_END + PAGE <= (size_t)256 << 20 && NH * KSTR <= T_Q && NPAGE % 2 == 0, "");
constexpr int NCHAIN = 128;
__host__ __device__ constexpr int kseg(int sg, int h) { return h * KSTR + sg; }
__host__ __device__ constexpr int fkq(int h, int kind) { return SY_FKQ + (h * 4 + kind) * 128; }

#define TLB_NI 16
#define TLB_WS (2 << 20)
#define XPROBE 0
struct Args {
  const u16 *x, *mem0, *Wq, *Wk, *Wv, *Wo, *wg, *wu, *wd;
  char* ws;
  u16* out;
  int s0, pad1;
};
__device__ __forceinline__ int h_off(const Args& p, int rblk) { return rblk * PAGE + (((p.s0 ^ rblk ^ 1) & 1) << 13); }
static_assert(WS_HF % (2 * PAGE) == 0, "");
#define TF_KQV 1
#define TF_O 0
__device__ __forceinline__ int t_off(const Args& p, int t) { return t * 16384 + (((p.s0 ^ (t >> 7) ^ (TF_KQV == 2 ? t : TF_KQV)) & 1) << 13); }
__device__ __forceinline__ int t_off_o(const Args& p, int t) { return t * 16384 + (((p.s0 ^ (t >> 7) ^ (TF_O == 2 ? t : TF_O)) & 1) << 13); }

__device__ __forceinline__ float wave_sum(float v) {
#pragma unroll
  for (int i = 0; i < 4; ++i) {
    const int x = (int)__float_as_uint(v);
    v += __uint_as_float(i == 0 ? __builtin_amdgcn_update_dpp(0, x, 0x128, 0xf, 0xf, false) : i == 1 ? __builtin_amdgcn_update_dpp(0, x, 0x124, 0xf, 0xf, false)
                         : i == 2 ? __builtin_amdgcn_update_dpp(0, x, 0x122, 0xf, 0xf, false) : __builtin_amdgcn_update_dpp(0, x, 0x121, 0xf, 0xf, false));
  }
  auto r = __builtin_amdgcn_permlane16_swap(__float_as_uint(v), __float_as_uint(v), false, false);
  v = __uint_as_float(r[0]) + __uint_as_float(r[1]);
  r = __builtin_amdgcn_permlane32_swap(__float_as_uint(v), __float_as_uint(v), false, false);
  return __uint_as_float(r[0]) + __uint_as_float(r[1]);
}

__shared__ __attribute__((aligned(16))) char g_smem[163840];
__device__ __forceinline__ unsigned lds_off(const void* p) {
  return (unsigned)(uintptr_t)(lds_void_ptr)g_smem + (unsigned)((const char*)p - g_smem);
}
__device__ __forceinline__ __amdgpu_buffer_rsrc_t mkrsrc(const void* p, unsigned bytes) {
  return __builtin_amdgcn_make_buffer_rsrc((void*)p, 0, bytes, 0x00020000);
}
__device__ __forceinline__ u32x4 ld_sc1(__amdgpu_buffer_rsrc_t rs, int off) {
  return __builtin_amdgcn_raw_buffer_load_b128(rs, off, 0, 16);
}
__device__ __forceinline__ void st_sc1(__amdgpu_buffer_rsrc_t rs, int off, u32x4 v) {
  __builtin_amdgcn_raw_buffer_store_b128(v, rs, off, 0, 16);
}
__device__ __forceinline__ void swap16(unsigned& x, unsigned& y) {
  auto r = __builtin_amdgcn_permlane16_swap(x, y, false, false);
  x = r[0]; y = r[1];
}
__device__ __forceinline__ void swap32(unsigned& x, unsigned& y) {
  auto r = __builtin_amdgcn_permlane32_swap(x, y, false, false);
  x = r[0]; y = r[1];
}
__device__ __forceinline__ bool spin_fail(unsigned& spins, gu32* abortw) {
  ++spins;
  if ((spins & 63u) == 0u) {
    const __amdgpu_buffer_rsrc_t ra = mkrsrc((const void*)abortw, 4);
    unsigned z = 0;
    asm volatile("" : "+v"(z));
    if (__builtin_amdgcn_readfirstlane(__builtin_amdgcn_raw_buffer_load_b32(ra, z, 0, 16)) == 1u) return true;
    if (spins > (1u << 19)) {
      __builtin_amdgcn_raw_buffer_store_b32(z + 1u, ra, z, 0, 16);
      return true;
    }
  }
  return false;
}
__device__ __forceinline__ bf16x8 ldsf(const char* p) { return *reinterpret_cast<const bf16x8*>(p); }
#define SCHED() __builtin_amdgcn_sched_barrier(0)
#define LBAR() do { asm volatile("s_waitcnt lgkmcnt(0)" ::: "memory"); __builtin_amdgcn_s_barrier(); asm volatile("" ::: "memory"); } while (0)
#define WAIT_V(n) asm volatile("s_waitcnt vmcnt(%0)" :: "n"(n) : "memory")

#define HOLD_V(c, n) asm volatile("s_cmp_eq_u32 %0, 0\n\ts_cbranch_scc1 .Lh%=\n\ts_waitcnt vmcnt(%1)\n.Lh%=:" :: "s"((int)(c)), "n"(n) : "memory", "scc")
#define TH_Q 2
__device__ __forceinline__ void wait_flags(__amdgpu_buffer_rsrc_t rs, int off, gu32* abortw) {
  for (unsigned spins = 0;;) {
    asm volatile("" ::: "memory");
    const unsigned v = __builtin_amdgcn_raw_buffer_load_b32(rs, off, 0, 16);
    if (__all(v != SENT)) break;
    __builtin_amdgcn_s_sleep(4);
    if (spin_fail(spins, abortw)) break;
  }
  asm volatile("" ::: "memory");
}

__device__ __forceinline__ unsigned offA(unsigned row, unsigned ch) {
  return row * 128u + 16u * (ch ^ (row & 7u));
}
__device__ __forceinline__ unsigned swzB(unsigned row) { return ((row & 3u) << 2) | ((row >> 2) & 3u); }
__device__ __forceinline__ unsigned offB(unsigned row, unsigned ch) {
  return 256u * row + 16u * (ch ^ swzB(row));
}
__device__ __forceinline__ void glds16(const u16* src, char* dst) {
  const unsigned d = __builtin_amdgcn_readfirstlane(lds_off(dst));
  unsigned keep;
  asm volatile("s_mov_b32 %0, m0\n\ts_mov_b32 m0, %2\n\ts_nop 0\n\tglobal_load_lds_dwordx4 %1, off\n\ts_mov_b32 m0, %0"
               : "=&s"(keep) : "v"(src), "s"(d) : "memory");
}
__device__ __forceinline__ bf16x8 ld_bfrag(const char* sb, int ks, int c, int lane) {
  const unsigned g = lane >> 4, qq = (lane & 15) >> 2, pp = lane & 3;
  s16x4 lo = __builtin_amdgcn_ds_read_tr16_b64_v4i16(
      (lds_s16x4_ptr)(sb + offB(32 * ks + 8 * g + qq, 2 * c + (pp >> 1)) + 8 * (pp & 1)));
  s16x4 hi = __builtin_amdgcn_ds_read_tr16_b64_v4i16(
      (lds_s16x4_ptr)(sb + offB(32 * ks + 8 * g + 4 + qq, 2 * c + (pp >> 1)) + 8 * (pp & 1)));
  return (bf16x8){lo[0], lo[1], lo[2], lo[3], hi[0], hi[1], hi[2], hi[3]};
}

__device__ __forceinline__ void blk_dma(const u16* src, size_t ld, char* slot, int lane) {
  const u16* s = src + (size_t)(lane >> 2) * ld + (lane & 3) * 8;
  glds16(s, slot);
  glds16(s + 16 * ld, slot + 1024);
}
template <int NB, typename ISSUE, typename SINK>
__device__ __forceinline__ void load_blocks(char* slice, ISSUE&& issue, SINK&& sink) {
#pragma unroll
  for (int i = 0; i < 8; ++i) issue(i, slice + i * 2048);
#pragma unroll
  for (int g = 0; g < NB / 4; ++g) {
    if (g + 1 < NB / 4) WAIT_V(8); else WAIT_V(0);
#pragma unroll
    for (int j = 0; j < 4; ++j) sink(4 * g + j, slice + ((4 * g + j) & 7) * 2048);
    asm volatile("s_waitcnt lgkmcnt(0)" ::: "memory");
    if (g + 2 < NB / 4) {
#pragma unroll
      for (int j = 0; j < 4; ++j) issue(4 * g + 8 + j, slice + ((4 * g + j) & 7) * 2048);
    }
  }
}

__device__ __forceinline__ u32x4 widen32(unsigned a0, unsigned a1, unsigned b0, unsigned b1) {
  swap32(a0, b0); swap16(a0, b0);
  swap32(a1, b1); swap16(a1, b1);
  return (u32x4){a0, a1, b0, b1};
}

__device__ __forceinline__ void publish_flags(__amdgpu_buffer_rsrc_t rs, int off, int stride, int n) {
  WAIT_V(0);
  LBAR();
  int tid = threadIdx.x;
  asm volatile("" : "+v"(tid));
  if (tid < n) __builtin_amdgcn_raw_buffer_store_b32(1u, rs, off + tid * stride, 0, 16);
}

__device__ __forceinline__ void wait_s(const Args& p, __amdgpu_buffer_rsrc_t rsy, int rblk, int c0, int n, bool need) {
  if (need) {
    if (threadIdx.x < 64) {
      for (unsigned spins = 0;;) {
        asm volatile("" ::: "memory");
        const int f = (int)__builtin_amdgcn_raw_buffer_load_b32(rsy, SY_FS + (2 * c0 + (threadIdx.x & (2 * n - 1))) * 128, 0, 16);
        if (__all(f >= rblk)) break;
        __builtin_amdgcn_s_sleep(32);
        if (spin_fail(spins, (gu32*)(p.ws + WS_SY + SY_ABORT))) break;
      }
    }
    LBAR();
  }
}
__device__ __forceinline__ u32x4 combine(u32x4 sv, u32x4 g, u32x4 x) {
  u32x4 o;
#pragma unroll
  for (int j = 0; j < 4; ++j) {
    const unsigned t = cvtpk(lo_f(sv[j]) * lo_f(g[j]), hi_f(sv[j]) * hi_f(g[j]));
    o[j] = cvtpk(lo_f(x[j]) + lo_f(t), hi_f(x[j]) + hi_f(t));
  }
  return o;
}
#define OUT_AUX 0
__device__ __forceinline__ void st_out(const Args& p, int e, u32x4 v) {
  __builtin_amdgcn_raw_buffer_store_b128(v, mkrsrc(p.out, S_LEN * DM * 2), e * 2, 0, OUT_AUX);
}
struct SX { u32x4 s, x; };
__device__ __forceinline__ SX get_sx(const Args& p, int rblk, int e) {
  SX r; r.s = (u32x4){0u, 0u, 0u, 0u}; r.x = r.s;
  if (rblk < NFUSE) {
    r.s = ld_sc1(mkrsrc(p.ws + WS_S, S_LEN * DM * 2), (((e & 1023) >> 5) * S_LEN + (e >> 10)) * 64 + (e & 31) * 2);
    r.x = *reinterpret_cast<const u32x4*>(p.x + e);
  }
  return r;
}
__device__ __forceinline__ void put_g(const Args& p, __amdgpu_buffer_rsrc_t rg, int rblk, int e, u32x4 g, const SX& v) {
  if (rblk < NFUSE) {
    st_out(p, e, combine(v.s, g, v.x));
  } else st_sc1(rg, e * 2, g);
}

__device__ __forceinline__ void job_comb(const Args& p, int rblk, int ct, __amdgpu_buffer_rsrc_t rsy) {
  int tid = threadIdx.x;
  asm volatile("" : "+v"(tid));
  u32x4 xv[8];
#pragma unroll
  for (int i = 0; i < 8; ++i) xv[i] = *reinterpret_cast<const u32x4*>(p.x + (rblk * 128 + i * 16 + (tid >> 5)) * DM + ct * 256 + (tid & 31) * 8);
  if (tid < 64) wait_flags(rsy, SY_FG + (rblk * 8 + 2 * ct + (tid & 1)) * 128, (gu32*)(p.ws + WS_SY + SY_ABORT));
  wait_s(p, rsy, rblk, ct * 8, 8, true);
  u32x4 sv[8], gv[8];
#pragma unroll
  for (int i = 0; i < 8; ++i) {
    const int e = (rblk * 128 + i * 16 + (tid >> 5)) * DM + ct * 256 + (tid & 31) * 8;
    sv[i] = ld_sc1(mkrsrc(p.ws + WS_S, S_LEN * DM * 2), (((e & 1023) >> 5) * S_LEN + (e >> 10)) * 64 + (e & 31) * 2);
    gv[i] = ld_sc1(mkrsrc(p.ws + WS_G, S_LEN * DM * 2), e * 2);
  }
#pragma unroll
  for (int i = 0; i < 8; ++i)
    st_out(p, (rblk * 128 + i * 16 + (tid >> 5)) * DM + ct * 256 + (tid & 31) * 8, combine(sv[i], gv[i], xv[i]));
}

__device__ __forceinline__ u32x4 raw_rsrc(const void* p, unsigned bytes) {
  const unsigned long long a = (unsigned long long)p;
  return (u32x4){(unsigned)a, (unsigned)(a >> 32) & 0xffffu, bytes, 0x00020000u};
}
__device__ __forceinline__ u32x2 next_tag(u32x2 e) { return (u32x2){e[0] + 1u, e[1] + (e[0] == 0xFFFFFFFFu)}; }
struct Boot { u32x2 e; u32x4 c; };
__device__ __forceinline__ void boot_ask(const char* ws, Boot& r) {
  const unsigned o = (threadIdx.x & 63) * 128;
  asm volatile("buffer_load_dwordx2 %0, off, %3, %4 sc1\n\tbuffer_load_dwordx4 %1, %2, %3, %5 offen sc1"
               : "=&v"(r.e), "=&v"(r.c) : "v"(o), "s"(raw_rsrc(ws, SY_TOTAL)), "s"(SY_EP), "s"(SY_BT) : "memory");
}
__device__ __forceinline__ void wait_rows(const char* ws, u32x2 tag) {
  const __amdgpu_buffer_rsrc_t rs = mkrsrc(ws, SY_TOTAL);
  int o = (threadIdx.x & 63) * 128;
  asm volatile("" : "+v"(o));
#pragma unroll 1
  for (int i = 0; i < (1 << 17); ++i) {
    asm volatile("" ::: "memory");
    const u32x2 a = __builtin_amdgcn_raw_buffer_load_b64(rs, o, SY_BT + 32 * 128, 16), b = __builtin_amdgcn_raw_buffer_load_b64(rs, o, SY_BT + 96 * 128, 16);
    if (__all(a[0] == tag[0] && a[1] == tag[1] && b[0] == tag[0] && b[1] == tag[1])) break;
    __builtin_amdgcn_s_sleep(8);
  }
  asm volatile("" ::: "memory");
}
__device__ __forceinline__ unsigned boot_done(const char* ws, Boot& r, bool own = false, u32x2 tag = (u32x2){0u, 0u}) {
#pragma unroll 1
  for (int i = 0; i < (1 << 17); ++i) {
    asm volatile("" : "+v"(r.e), "+v"(r.c));
    if (!own) tag = next_tag(r.e);
    if (__all(r.c[0] == tag[0] && r.c[1] == tag[1] && (r.c[2] ^ r.c[3]) == tag[0])) break;
    __builtin_amdgcn_s_sleep(8);
    boot_ask(ws, r);
    WAIT_V(0);
  }
  unsigned q0[NPAGE], q1[NPAGE], ga = 0xFFFFu, gb = 0xFFFFu;
#pragma unroll
  for (int i = 0; i < NPAGE; ++i) {
    const unsigned x = __builtin_amdgcn_readlane(r.c[2], i), y = __builtin_amdgcn_readlane(r.c[2], i + NPAGE);
    q0[i] = min(x >> 16, y >> 16); q1[i] = min(x & 0xFFFFu, y & 0xFFFFu);
    ga = min(ga, (i & 1) ? q1[i] : q0[i]); gb = min(gb, (i & 1) ? q0[i] : q1[i]);
  }
  unsigned m = gb < ga ? 0x80001555u : 0x2AAAu;
  const unsigned thr = min(ga, gb) + 110;
#pragma unroll
  for (int i = 0; i < NPAGE; ++i) {
    const unsigned lo = min(q0[i], q1[i]), hi = max(q0[i], q1[i]);
    const unsigned keep = (lo < thr ? hi : 0u) > lo + 100 ? ~(1u << i) : ~0u;
    m = (m & keep) | ((q1[i] < q0[i] ? ~0u : 0u) & ~keep);
  }
  return m;
}
typedef __attribute__((address_space(3))) volatile unsigned* lds_vu_ptr;
__device__ __forceinline__ lds_vu_ptr lds_at(const void* p) {
  unsigned a = lds_off(p);
  asm volatile("" : "+v"(a));
  return (lds_vu_ptr)(uintptr_t)a;
}
__device__ __forceinline__ unsigned uni(unsigned v) {
  unsigned r;
  asm volatile("s_nop 1\n\tv_readfirstlane_b32 %0, %1\n\ts_nop 3" : "=s"(r) : "v"(v));
  return r;
}
__device__ __forceinline__ void start_sy(char* ws, const u16* mem0, int i, char* smem) {
  const int tid = threadIdx.x, lane = tid & 63, w = __builtin_amdgcn_readfirstlane(tid >> 6);
  const __amdgpu_buffer_rsrc_t rsy = mkrsrc(ws, SY_TOTAL);
  const __amdgpu_buffer_rsrc_t rs = mkrsrc(ws + (size_t)(i % NPAGE) * PAGE + PROBE - (i / NPAGE) * 2048, 16384);
  const u32x2 e = __builtin_amdgcn_raw_buffer_load_b64(rsy, 0, SY_EP, 16);
  const u32x4 ds = {DSENT, DSENT, DSENT, DSENT};
  if (lane < 32) st_sc1(rs, (lane >> 4) * 8192 + w * 256 + (lane & 15) * 16, ds);
  static_assert(NPRE == 4 && SY_FEND - SY_BR <= 32 * 6144, "");
#pragma unroll
  for (int u = 0; u < 6; ++u)
    st_sc1(rsy, (u < 4 ? SY_V + (i * 4 + u) * 8192 : SY_O + (i * 2 + u - 4) * 8192) + tid * 16, ds);
  if (w == 7) {
    const int n = i * 64 + lane, j = (n >> 10) * 512 + (n & 511);
    const unsigned* m0 = reinterpret_cast<const unsigned*>(mem0 + (2 * ((j >> 2) & 3) + (j & 1)) * DM + (j >> 4) * 16) + ((j >> 1) & 1);
    u32x4 m;
#pragma unroll
    for (int k = 0; k < 4; ++k) { const unsigned t = m0[2 * k]; m[k] = t == DSENT ? 0xFFFFFFFFu : t; }
    st_sc1(rsy, n * 16, m);
  } else if (w == 6) st_sc1(rsy, 32768 + i * 1024 + lane * 16, ds);
  else {
    const int o = SY_BR + i * 6144 + tid * 16;
    st_sc1(rsy, o < SY_FEND ? o : 0x7F000000, (u32x4){SENT, SENT, SENT, SENT});
  }
  unsigned t[4];
#pragma unroll
  for (int k = 0; k < 4; ++k) {
    unsigned long long c0, c1; unsigned v, off = ((k ^ (k >> 1)) & 1) * 8192 + w * 256 + (k >> 1) * 128;
    asm volatile("s_waitcnt vmcnt(0)\n\ts_barrier\n\ts_memtime %0\n\ts_waitcnt lgkmcnt(0)\n\tbuffer_load_dword %2, %3, %4, 0 offen sc1\n\ts_waitcnt vmcnt(0)\n\t"
                 "s_memtime %1\n\ts_waitcnt lgkmcnt(0)" : "=&s"(c0), "=&s"(c1), "=&v"(v) : "v"(off), "s"(rs) : "memory");
    t[k] = (unsigned)(c1 - c0);
  }
  lds_vu_ptr tm = lds_at(smem);
  if (lane == 0) { tm[w] = min(t[0], t[3]); tm[8 + w] = min(t[1], t[2]); }
  int ro = SY_BT + i * 128 + lane * 16;
  asm volatile("" : "+v"(ro));
  LBAR();
  if (threadIdx.x < 8) {
    unsigned m0 = tm[0], m1 = tm[8];
#pragma unroll
    for (int k = 1; k < 8; ++k) { m0 = min(m0, tm[k]); m1 = min(m1, tm[8 + k]); }
    const u32x2 tag = next_tag(e);
    const unsigned v = min(m0, 30000u) << 16 | min(m1, 30000u);
    st_sc1(rsy, ro, (u32x4){tag[0], tag[1], v, v ^ tag[0]});
  }
}
template <int NB, typename F>
__device__ __forceinline__ u32x2 start_rows(const Args& p, int g, unsigned on, F&& between) {
  const int tid = threadIdx.x, lane = tid & 63, row0 = g * 16 + (tid >> 6) * 2;
  u32x4 a[4];
  u32x2 e;
  asm volatile("buffer_load_dwordx4 %0, %5, %8, 0 offen\n\tbuffer_load_dwordx4 %1, %5, %8, 0 offen offset:1024\n\t"
               "buffer_load_dwordx4 %2, %5, %8, 0 offen offset:2048\n\tbuffer_load_dwordx4 %3, %5, %8, 0 offen offset:3072\n\t"
               "buffer_load_dwordx2 %4, off, %6, %7 sc1"
               : "=&v"(a[0]), "=&v"(a[1]), "=&v"(a[2]), "=&v"(a[3]), "=&v"(e)
               : "v"(row0 * (DM * 2) + lane * 16), "s"(raw_rsrc(p.ws, SY_TOTAL)), "s"(SY_EP), "s"(raw_rsrc(p.x, on * (S_LEN * DM * 2))) : "memory");
  between();
  asm volatile("s_waitcnt vmcnt(%5)" : "+v"(a[0]), "+v"(a[1]), "+v"(a[2]), "+v"(a[3]), "+v"(e) : "n"(NB) : "memory");
  const __amdgpu_buffer_rsrc_t ro = mkrsrc(p.ws + WS_XN, on * (S_LEN * XLD * 2));
#pragma unroll
  for (int u = 0; u < 2; ++u) {
    float v[16];
#pragma unroll
    for (int i = 0; i < 8; ++i) { v[2 * i] = lo_f(a[2 * u + (i >> 2)][i & 3]); v[2 * i + 1] = hi_f(a[2 * u + (i >> 2)][i & 3]); }
    float s = 0.f;
#pragma unroll
    for (int i = 0; i < 16; ++i) s += v[i];
    const float mean = wave_sum(s) * (1.0f / DM);
    float q = 0.f;
#pragma unroll
    for (int i = 0; i < 16; ++i) { float d = v[i] - mean; q += d * d; }
    const float var = rbf(wave_sum(q) * (1.0f / DM));
    const float mb = rbf(mean);
    const float inv = 1.0f / rbf(sqrtf(rbf(var + 1e-5f)));
#pragma unroll
    for (int k = 0; k < 2; ++k) {
      u32x4 o;
#pragma unroll
      for (int i = 0; i < 4; ++i) {
        const unsigned t = cvtpk(v[8 * k + 2 * i] - mb, v[8 * k + 2 * i + 1] - mb);
        o[i] = cvtpk(lo_f(t) * inv, hi_f(t) * inv);
      }
      st_sc1(ro, (row0 + u) * (XLD * 2) + k * 1024 + lane * 16, o);
    }
  }
  return next_tag(e);
}
__device__ __forceinline__ void report_rows(const Args& p, int g, u32x2 tag) {
  if (threadIdx.x < 8) st_sc1(mkrsrc(p.ws + WS_SY, SY_TOTAL), SY_BT + (32 + g) * 128 + threadIdx.x * 16, (u32x4){tag[0], tag[1], 0u, tag[0]});
}

typedef __attribute__((ext_vector_type(32))) unsigned u32x32;
__device__ __forceinline__ void put4(u32x32& W, int i, bf16x8 f) {
  const u32x4 v = __builtin_bit_cast(u32x4, f);
#pragma unroll
  for (int j = 0; j < 4; ++j) W[4 * i + j] = v[j];
}
#define CLB8(a, b, c, d, e, f, g, h) "v" #a, "v" #b, "v" #c, "v" #d, "v" #e, "v" #f, "v" #g, "v" #h
#define CLB32(a) CLB32_##a
#define CLB32_128 CLB8(128, 129, 130, 131, 132, 133, 134, 135), CLB8(136, 137, 138, 139, 140, 141, 142, 143), CLB8(144, 145, 146, 147, 148, 149, 150, 151), CLB8(152, 153, 154, 155, 156, 157, 158, 159)
#define CLB32_160 CLB8(160, 161, 162, 163, 164, 165, 166, 167), CLB8(168, 169, 170, 171, 172, 173, 174, 175), CLB8(176, 177, 178, 179, 180, 181, 182, 183), CLB8(184, 185, 186, 187, 188, 189, 190, 191)
#define W4_IN "{v[0:31]}"(W[0]), "{v[32:63]}"(W[1]), "{v[64:95]}"(W[2]), "{v[96:127]}"(W[3])
#define CLB32_192 CLB8(192, 193, 194, 195, 196, 197, 198, 199), CLB8(200, 201, 202, 203, 204, 205, 206, 207), CLB8(208, 209, 210, 211, 212, 213, 214, 215), CLB8(216, 217, 218, 219, 220, 221, 222, 223)
__device__ __forceinline__ unsigned ld_flag0(__amdgpu_buffer_rsrc_t rs, int off) {
  return __builtin_amdgcn_raw_buffer_load_b32(rs, off, 0, 16);
}
__device__ __forceinline__ void glds16_sc1(const char* src, char* dst) {
  const unsigned d = __builtin_amdgcn_readfirstlane(lds_off(dst));
  unsigned keep;
  asm volatile("s_mov_b32 %0, m0\n\ts_mov_b32 m0, %2\n\ts_nop 0\n\tglobal_load_lds_dwordx4 %1, off sc1\n\ts_mov_b32 m0, %0"
               : "=&s"(keep) : "v"(src), "s"(d) : "memory");
}

__device__ __forceinline__ void touch_line(const char* src, char* dst) {
  const unsigned d = __builtin_amdgcn_readfirstlane(lds_off(dst));
  unsigned keep;
  asm volatile("s_mov_b32 %0, m0\n\ts_mov_b32 m0, %2\n\ts_nop 0\n\tglobal_load_lds_dword %1, off sc1\n\ts_mov_b32 m0, %0"
               : "=&s"(keep) : "v"(src), "s"(d) : "memory");
}

#define PF_N 12
#define LN_M 3
#define LN_X 1
#define PR_G1 8
#define WREV 1
template <bool DEV = false>
__device__ __forceinline__ void glds2(const char* base, int step, unsigned voff, unsigned dst) {
  unsigned keep;
  const char* base2 = base + step;
  if (DEV)
    asm volatile("s_mov_b32 %0, m0\n\ts_mov_b32 m0, %4\n\ts_nop 0\n\tglobal_load_lds_dwordx4 %1, %2 sc1\n\t"
                 "s_add_u32 m0, m0, 0x2000\n\ts_nop 0\n\tglobal_load_lds_dwordx4 %1, %3 sc1\n\ts_mov_b32 m0, %0"
                 : "=&s"(keep) : "v"(voff), "s"(base), "s"(base2), "s"(dst) : "memory", "scc");
  else
  asm volatile("s_mov_b32 %0, m0\n\ts_mov_b32 m0, %4\n\ts_nop 0\n\tglobal_load_lds_dwordx4 %1, %2\n\t"
               "s_add_u32 m0, m0, 0x2000\n\ts_nop 0\n\tglobal_load_lds_dwordx4 %1, %3\n\ts_mov_b32 m0, %0"
               : "=&s"(keep) : "v"(voff), "s"(base), "s"(base2), "s"(dst) : "memory", "scc");
}

#define DN_ASM \
  "s_mov_b32 s94, m0\n\t" \
  "s_mov_b64 s[84:85], %[pa]\n\t" \
  "s_mov_b64 s[86:87], %[pb]\n\t" \
  "s_movk_i32 s88, 0x4000\n\t" \
  "s_mov_b32 s89, 0x10000\n\t" \
  "s_movk_i32 s90, 13\n\t" \
  "v_mov_b64 v[0:1], 0\n\t" \
  "v_mov_b64 v[2:3], 0\n\t" \
  "v_mov_b64 v[4:5], 0\n\t" \
  "v_mov_b64 v[6:7], 0\n\t" \
  "v_mov_b64 v[8:9], 0\n\t" \
  "v_mov_b64 v[10:11], 0\n\t" \
  "v_mov_b64 v[12:13], 0\n\t" \
  "v_mov_b64 v[14:15], 0\n\t" \
  "v_mov_b64 v[16:17], 0\n\t" \
  "v_mov_b64 v[18:19], 0\n\t" \
  "v_mov_b64 v[20:21], 0\n\t" \
  "v_mov_b64 v[22:23], 0\n\t" \
  "v_mov_b64 v[24:25], 0\n\t" \
  "v_mov_b64 v[26:27], 0\n\t" \
  "v_mov_b64 v[28:29], 0\n\t" \
  "v_mov_b64 v[30:31], 0\n\t" \
  "s_cmp_lg_u32 %[hf], 0\n\t" \
  "s_cbranch_scc1 .Lk%=\n\t" \
  "ds_read_b128 v[32:35], v80 offset:0\n\t" \
  "ds_read_b128 v[36:39], v80 offset:1024\n\t" \
  "ds_read_b128 v[40:43], v80 offset:2048\n\t" \
  "ds_read_b128 v[44:47], v80 offset:3072\n\t" \
  "ds_read_b64_tr_b16 v[64:65], v82 offset:0\n\t" \
  "ds_read_b64_tr_b16 v[66:67], v83 offset:0\n\t" \
  "ds_read_b128 v[48:51], v80 offset:8192\n\t" \
  "ds_read_b64_tr_b16 v[68:69], v84 offset:0\n\t" \
  "ds_read_b64_tr_b16 v[70:71], v85 offset:0\n\t" \
  "ds_read_b128 v[52:55], v80 offset:9216\n\t" \
  "ds_read_b128 v[56:59], v80 offset:10240\n\t" \
  "ds_read_b128 v[60:63], v80 offset:11264\n\t" \
  ".Lp0%=:\n\t" \
  "s_waitcnt vmcnt(4)\n\t" \
  "s_barrier\n\t" \
  "s_waitcnt lgkmcnt(3)\n\t" \
  "v_mfma_f32_16x16x32_bf16 v[0:3], v[64:67], v[32:35], v[0:3]\n\t" \
  "ds_read_b64_tr_b16 v[72:73], v82 offset:8192\n\t" \
  "v_mfma_f32_16x16x32_bf16 v[4:7], v[68:71], v[32:35], v[4:7]\n\t" \
  "ds_read_b64_tr_b16 v[74:75], v83 offset:8192\n\t" \
  "ds_read_b128 v[32:35], v80 offset:32768\n\t" \
  "s_add_u32 m0, %[lw], 98304\n\t" \
  "v_mfma_f32_16x16x32_bf16 v[8:11], v[64:67], v[36:39], v[8:11]\n\t" \
  "ds_read_b64_tr_b16 v[76:77], v84 offset:8192\n\t" \
  "global_load_lds_dwordx4 v90, s[84:85] sc1\n\t" \
  "s_add_u32 s84, s84, s88\n\t" \
  "s_addc_u32 s85, s85, 0\n\t" \
  "v_mfma_f32_16x16x32_bf16 v[12:15], v[68:71], v[36:39], v[12:15]\n\t" \
  "ds_read_b64_tr_b16 v[78:79], v85 offset:8192\n\t" \
  "ds_read_b128 v[36:39], v80 offset:33792\n\t" \
  "v_mfma_f32_16x16x32_bf16 v[16:19], v[64:67], v[40:43], v[16:19]\n\t" \
  "v_mfma_f32_16x16x32_bf16 v[20:23], v[68:71], v[40:43], v[20:23]\n\t" \
  "ds_read_b128 v[40:43], v80 offset:34816\n\t" \
  "s_add_u32 m0, %[lw], 114688\n\t" \
  "v_mfma_f32_16x16x32_bf16 v[24:27], v[64:67], v[44:47], v[24:27]\n\t" \
  "global_load_lds_dwordx4 v91, s[86:87]\n\t" \
  "s_add_u32 s86, s86, s89\n\t" \
  "s_addc_u32 s87, s87, 0\n\t" \
  "v_mfma_f32_16x16x32_bf16 v[28:31], v[68:71], v[44:47], v[28:31]\n\t" \
  "ds_read_b128 v[44:47], v80 offset:35840\n\t" \
  "s_waitcnt lgkmcnt(3)\n\t" \
  "v_mfma_f32_16x16x32_bf16 v[0:3], v[72:75], v[48:51], v[0:3]\n\t" \
  "ds_read_b64_tr_b16 v[64:65], v82 offset:32768\n\t" \
  "v_mfma_f32_16x16x32_bf16 v[4:7], v[76:79], v[48:51], v[4:7]\n\t" \
  "ds_read_b64_tr_b16 v[66:67], v83 offset:32768\n\t" \
  "ds_read_b128 v[48:51], v80 offset:40960\n\t" \
  "s_add_u32 m0, %[lw], 106496\n\t" \
  "v_mfma_f32_16x16x32_bf16 v[8:11], v[72:75], v[52:55], v[8:11]\n\t" \
  "ds_read_b64_tr_b16 v[68:69], v84 offset:32768\n\t" \
  "global_load_lds_dwordx4 v90, s[84:85] sc1\n\t" \
  "s_add_u32 s84, s84, s88\n\t" \
  "s_addc_u32 s85, s85, 0\n\t" \
  "v_mfma_f32_16x16x32_bf16 v[12:15], v[76:79], v[52:55], v[12:15]\n\t" \
  "ds_read_b64_tr_b16 v[70:71], v85 offset:32768\n\t" \
  "ds_read_b128 v[52:55], v80 offset:41984\n\t" \
  "v_mfma_f32_16x16x32_bf16 v[16:19], v[72:75], v[56:59], v[16:19]\n\t" \
  "v_mfma_f32_16x16x32_bf16 v[20:23], v[76:79], v[56:59], v[20:23]\n\t" \
  "ds_read_b128 v[56:59], v80 offset:43008\n\t" \
  "s_add_u32 m0, %[lw], 122880\n\t" \
  "v_mfma_f32_16x16x32_bf16 v[24:27], v[72:75], v[60:63], v[24:27]\n\t" \
  "global_load_lds_dwordx4 v91, s[86:87]\n\t" \
  "s_add_u32 s86, s86, s89\n\t" \
  "s_addc_u32 s87, s87, 0\n\t" \
  "v_mfma_f32_16x16x32_bf16 v[28:31], v[76:79], v[60:63], v[28:31]\n\t" \
  "ds_read_b128 v[60:63], v80 offset:44032\n\t" \
  "s_sub_u32 s90, s90, 1\n\t" \
  "s_cselect_b32 s88, 0, s88\n\t" \
  "s_cselect_b32 s89, 0, s89\n\t" \
  "s_cselect_b32 s91, 16384, 0\n\t" \
  "s_sub_u32 s84, s84, s91\n\t" \
  "s_subb_u32 s85, s85, 0\n\t" \
  "s_lshl_b32 s91, s91, 2\n\t" \
  "s_sub_u32 s86, s86, s91\n\t" \
  "s_subb_u32 s87, s87, 0\n\t" \
  "s_waitcnt vmcnt(4)\n\t" \
  "s_barrier\n\t" \
  "s_waitcnt lgkmcnt(3)\n\t" \
  "v_mfma_f32_16x16x32_bf16 v[0:3], v[64:67], v[32:35], v[0:3]\n\t" \
  "ds_read_b64_tr_b16 v[72:73], v82 offset:40960\n\t" \
  "v_mfma_f32_16x16x32_bf16 v[4:7], v[68:71], v[32:35], v[4:7]\n\t" \
  "ds_read_b64_tr_b16 v[74:75], v83 offset:40960\n\t" \
  "ds_read_b128 v[32:35], v81 offset:0\n\t" \
  "s_add_u32 m0, %[lw], 0\n\t" \
  "v_mfma_f32_16x16x32_bf16 v[8:11], v[64:67], v[36:39], v[8:11]\n\t" \
  "ds_read_b64_tr_b16 v[76:77], v84 offset:40960\n\t" \
  "global_load_lds_dwordx4 v90, s[84:85] sc1\n\t" \
  "s_add_u32 s84, s84, s88\n\t" \
  "s_addc_u32 s85, s85, 0\n\t" \
  "v_mfma_f32_16x16x32_bf16 v[12:15], v[68:71], v[36:39], v[12:15]\n\t" \
  "ds_read_b64_tr_b16 v[78:79], v85 offset:40960\n\t" \
  "ds_read_b128 v[36:39], v81 offset:1024\n\t" \
  "v_mfma_f32_16x16x32_bf16 v[16:19], v[64:67], v[40:43], v[16:19]\n\t" \
  "v_mfma_f32_16x16x32_bf16 v[20:23], v[68:71], v[40:43], v[20:23]\n\t" \
  "ds_read_b128 v[40:43], v81 offset:2048\n\t" \
  "s_add_u32 m0, %[lw], 16384\n\t" \
  "v_mfma_f32_16x16x32_bf16 v[24:27], v[64:67], v[44:47], v[24:27]\n\t" \
  "global_load_lds_dwordx4 v91, s[86:87]\n\t" \
  "s_add_u32 s86, s86, s89\n\t" \
  "s_addc_u32 s87, s87, 0\n\t" \
  "v_mfma_f32_16x16x32_bf16 v[28:31], v[68:71], v[44:47], v[28:31]\n\t" \
  "ds_read_b128 v[44:47], v81 offset:3072\n\t" \
  "s_waitcnt lgkmcnt(3)\n\t" \
  "v_mfma_f32_16x16x32_bf16 v[0:3], v[72:75], v[48:51], v[0:3]\n\t" \
  "ds_read_b64_tr_b16 v[64:65], v86 offset:0\n\t" \
  "v_mfma_f32_16x16x32_bf16 v[4:7], v[76:79], v[48:51], v[4:7]\n\t" \
  "ds_read_b64_tr_b16 v[66:67], v87 offset:0\n\t" \
  "ds_read_b128 v[48:51], v81 offset:8192\n\t" \
  "s_add_u32 m0, %[lw], 8192\n\t" \
  "v_mfma_f32_16x16x32_bf16 v[8:11], v[72:75], v[52:55], v[8:11]\n\t" \
  "ds_read_b64_tr_b16 v[68:69], v88 offset:0\n\t" \
  "global_load_lds_dwordx4 v90, s[84:85] sc1\n\t" \
  "s_add_u32 s84, s84, s88\n\t" \
  "s_addc_u32 s85, s85, 0\n\t" \
  "v_mfma_f32_16x16x32_bf16 v[12:15], v[76:79], v[52:55], v[12:15]\n\t" \
  "ds_read_b64_tr_b16 v[70:71], v89 offset:0\n\t" \
  "ds_read_b128 v[52:55], v81 offset:9216\n\t" \
  "v_mfma_f32_16x16x32_bf16 v[16:19], v[72:75], v[56:59], v[16:19]\n\t" \
  "v_mfma_f32_16x16x32_bf16 v[20:23], v[76:79], v[56:59], v[20:23]\n\t" \
  "ds_read_b128 v[56:59], v81 offset:10240\n\t" \
  "s_add_u32 m0, %[lw], 24576\n\t" \
  "v_mfma_f32_16x16x32_bf16 v[24:27], v[72:75], v[60:63], v[24:27]\n\t" \
  "global_load_lds_dwordx4 v91, s[86:87]\n\t" \
  "s_add_u32 s86, s86, s89\n\t" \
  "s_addc_u32 s87, s87, 0\n\t" \
  "v_mfma_f32_16x16x32_bf16 v[28:31], v[76:79], v[60:63], v[28:31]\n\t" \
  "ds_read_b128 v[60:63], v81 offset:11264\n\t" \
  "s_waitcnt vmcnt(4)\n\t" \
  "s_barrier\n\t" \
  "s_waitcnt lgkmcnt(3)\n\t" \
  "v_mfma_f32_16x16x32_bf16 v[0:3], v[64:67], v[32:35], v[0:3]\n\t" \
  "ds_read_b64_tr_b16 v[72:73], v86 offset:8192\n\t" \
  "v_mfma_f32_16x16x32_bf16 v[4:7], v[68:71], v[32:35], v[4:7]\n\t" \
  "ds_read_b64_tr_b16 v[74:75], v87 offset:8192\n\t" \
  "ds_read_b128 v[32:35], v81 offset:32768\n\t" \
  "s_add_u32 m0, %[lw], 32768\n\t" \
  "v_mfma_f32_16x16x32_bf16 v[8:11], v[64:67], v[36:39], v[8:11]\n\t" \
  "ds_read_b64_tr_b16 v[76:77], v88 offset:8192\n\t" \
  "global_load_lds_dwordx4 v90, s[84:85] sc1\n\t" \
  "s_add_u32 s84, s84, s88\n\t" \
  "s_addc_u32 s85, s85, 0\n\t" \
  "v_mfma_f32_16x16x32_bf16 v[12:15], v[68:71], v[36:39], v[12:15]\n\t" \
  "ds_read_b64_tr_b16 v[78:79], v89 offset:8192\n\t" \
  "ds_read_b128 v[36:39], v81 offset:33792\n\t" \
  "v_mfma_f32_16x16x32_bf16 v[16:19], v[64:67], v[40:43], v[16:19]\n\t" \
  "v_mfma_f32_16x16x32_bf16 v[20:23], v[68:71], v[40:43], v[20:23]\n\t" \
  "ds_read_b128 v[40:43], v81 offset:34816\n\t" \
  "s_add_u32 m0, %[lw], 49152\n\t" \
  "v_mfma_f32_16x16x32_bf16 v[24:27], v[64:67], v[44:47], v[24:27]\n\t" \
  "global_load_lds_dwordx4 v91, s[86:87]\n\t" \
  "s_add_u32 s86, s86, s89\n\t" \
  "s_addc_u32 s87, s87, 0\n\t" \
  "v_mfma_f32_16x16x32_bf16 v[28:31], v[68:71], v[44:47], v[28:31]\n\t" \
  "ds_read_b128 v[44:47], v81 offset:35840\n\t" \
  "s_waitcnt lgkmcnt(3)\n\t" \
  "v_mfma_f32_16x16x32_bf16 v[0:3], v[72:75], v[48:51], v[0:3]\n\t" \
  "ds_read_b64_tr_b16 v[64:65], v86 offset:32768\n\t" \
  "v_mfma_f32_16x16x32_bf16 v[4:7], v[76:79], v[48:51], v[4:7]\n\t" \
  "ds_read_b64_tr_b16 v[66:67], v87 offset:32768\n\t" \
  "ds_read_b128 v[48:51], v81 offset:40960\n\t" \
  "s_add_u32 m0, %[lw], 40960\n\t" \
  "v_mfma_f32_16x16x32_bf16 v[8:11], v[72:75], v[52:55], v[8:11]\n\t" \
  "ds_read_b64_tr_b16 v[68:69], v88 offset:32768\n\t" \
  "global_load_lds_dwordx4 v90, s[84:85] sc1\n\t" \
  "s_add_u32 s84, s84, s88\n\t" \
  "s_addc_u32 s85, s85, 0\n\t" \
  "v_mfma_f32_16x16x32_bf16 v[12:15], v[76:79], v[52:55], v[12:15]\n\t" \
  "ds_read_b64_tr_b16 v[70:71], v89 offset:32768\n\t" \
  "ds_read_b128 v[52:55], v81 offset:41984\n\t" \
  "v_mfma_f32_16x16x32_bf16 v[16:19], v[72:75], v[56:59], v[16:19]\n\t" \
  "v_mfma_f32_16x16x32_bf16 v[20:23], v[76:79], v[56:59], v[20:23]\n\t" \
  "ds_read_b128 v[56:59], v81 offset:43008\n\t" \
  "s_add_u32 m0, %[lw], 57344\n\t" \
  "v_mfma_f32_16x16x32_bf16 v[24:27], v[72:75], v[60:63], v[24:27]\n\t" \
  "global_load_lds_dwordx4 v91, s[86:87]\n\t" \
  "s_add_u32 s86, s86, s89\n\t" \
  "s_addc_u32 s87, s87, 0\n\t" \
  "v_mfma_f32_16x16x32_bf16 v[28:31], v[76:79], v[60:63], v[28:31]\n\t" \
  "ds_read_b128 v[60:63], v81 offset:44032\n\t" \
  "s_waitcnt vmcnt(4)\n\t" \
  "s_barrier\n\t" \
  "s_waitcnt lgkmcnt(3)\n\t" \
  "v_mfma_f32_16x16x32_bf16 v[0:3], v[64:67], v[32:35], v[0:3]\n\t" \
  "ds_read_b64_tr_b16 v[72:73], v86 offset:40960\n\t" \
  "v_mfma_f32_16x16x32_bf16 v[4:7], v[68:71], v[32:35], v[4:7]\n\t" \
  "ds_read_b64_tr_b16 v[74:75], v87 offset:40960\n\t" \
  "ds_read_b128 v[32:35], v80 offset:0\n\t" \
  "s_add_u32 m0, %[lw], 65536\n\t" \
  "v_mfma_f32_16x16x32_bf16 v[8:11], v[64:67], v[36:39], v[8:11]\n\t" \
  "ds_read_b64_tr_b16 v[76:77], v88 offset:40960\n\t" \
  "global_load_lds_dwordx4 v90, s[84:85] sc1\n\t" \
  "s_add_u32 s84, s84, s88\n\t" \
  "s_addc_u32 s85, s85, 0\n\t" \
  "v_mfma_f32_16x16x32_bf16 v[12:15], v[68:71], v[36:39], v[12:15]\n\t" \
  "ds_read_b64_tr_b16 v[78:79], v89 offset:40960\n\t" \
  "ds_read_b128 v[36:39], v80 offset:1024\n\t" \
  "v_mfma_f32_16x16x32_bf16 v[16:19], v[64:67], v[40:43], v[16:19]\n\t" \
  "v_mfma_f32_16x16x32_bf16 v[20:23], v[68:71], v[40:43], v[20:23]\n\t" \
  "ds_read_b128 v[40:43], v80 offset:2048\n\t" \
  "s_add_u32 m0, %[lw], 81920\n\t" \
  "v_mfma_f32_16x16x32_bf16 v[24:27], v[64:67], v[44:47], v[24:27]\n\t" \
  "global_load_lds_dwordx4 v91, s[86:87]\n\t" \
  "s_add_u32 s86, s86, s89\n\t" \
  "s_addc_u32 s87, s87, 0\n\t" \
  "v_mfma_f32_16x16x32_bf16 v[28:31], v[68:71], v[44:47], v[28:31]\n\t" \
  "ds_read_b128 v[44:47], v80 offset:3072\n\t" \
  "s_waitcnt lgkmcnt(3)\n\t" \
  "v_mfma_f32_16x16x32_bf16 v[0:3], v[72:75], v[48:51], v[0:3]\n\t" \
  "ds_read_b64_tr_b16 v[64:65], v82 offset:0\n\t" \
  "v_mfma_f32_16x16x32_bf16 v[4:7], v[76:79], v[48:51], v[4:7]\n\t" \
  "ds_read_b64_tr_b16 v[66:67], v83 offset:0\n\t" \
  "ds_read_b128 v[48:51], v80 offset:8192\n\t" \
  "s_add_u32 m0, %[lw], 73728\n\t" \
  "v_mfma_f32_16x16x32_bf16 v[8:11], v[72:75], v[52:55], v[8:11]\n\t" \
  "ds_read_b64_tr_b16 v[68:69], v84 offset:0\n\t" \
  "global_load_lds_dwordx4 v90, s[84:85] sc1\n\t" \
  "s_add_u32 s84, s84, s88\n\t" \
  "s_addc_u32 s85, s85, 0\n\t" \
  "v_mfma_f32_16x16x32_bf16 v[12:15], v[76:79], v[52:55], v[12:15]\n\t" \
  "ds_read_b64_tr_b16 v[70:71], v85 offset:0\n\t" \
  "ds_read_b128 v[52:55], v80 offset:9216\n\t" \
  "v_mfma_f32_16x16x32_bf16 v[16:19], v[72:75], v[56:59], v[16:19]\n\t" \
  "v_mfma_f32_16x16x32_bf16 v[20:23], v[76:79], v[56:59], v[20:23]\n\t" \
  "ds_read_b128 v[56:59], v80 offset:10240\n\t" \
  "s_add_u32 m0, %[lw], 90112\n\t" \
  "v_mfma_f32_16x16x32_bf16 v[24:27], v[72:75], v[60:63], v[24:27]\n\t" \
  "global_load_lds_dwordx4 v91, s[86:87]\n\t" \
  "s_add_u32 s86, s86, s89\n\t" \
  "s_addc_u32 s87, s87, 0\n\t" \
  "v_mfma_f32_16x16x32_bf16 v[28:31], v[76:79], v[60:63], v[28:31]\n\t" \
  "ds_read_b128 v[60:63], v80 offset:11264\n\t" \
  "s_cmp_lg_u32 s88, 0\n\t" \
  "s_cbranch_scc1 .Lp0%=\n\t" \
  "s_branch .Le%=\n\t" \
  ".Lk%=:\n\t" \
  "ds_read_b128 v[32:35], v80 offset:0\n\t" \
  "ds_read_b128 v[36:39], v80 offset:1024\n\t" \
  "ds_read_b128 v[40:43], v80 offset:2048\n\t" \
  "ds_read_b128 v[44:47], v80 offset:3072\n\t" \
  "ds_read_b64_tr_b16 v[64:65], v82 offset:0\n\t" \
  "ds_read_b64_tr_b16 v[66:67], v83 offset:0\n\t" \
  "ds_read_b128 v[48:51], v80 offset:8192\n\t" \
  "ds_read_b64_tr_b16 v[68:69], v84 offset:0\n\t" \
  "ds_read_b64_tr_b16 v[70:71], v85 offset:0\n\t" \
  "ds_read_b128 v[52:55], v80 offset:9216\n\t" \
  "ds_read_b128 v[56:59], v80 offset:10240\n\t" \
  "ds_read_b128 v[60:63], v80 offset:11264\n\t" \
  ".Lp1%=:\n\t" \
  "s_waitcnt vmcnt(4)\n\t" \
  "s_barrier\n\t" \
  "s_waitcnt lgkmcnt(3)\n\t" \
  "v_mfma_f32_16x16x32_bf16 v[0:3], v[64:67], v[32:35], v[0:3]\n\t" \
  "ds_read_b64_tr_b16 v[72:73], v82 offset:8192\n\t" \
  "v_mfma_f32_16x16x32_bf16 v[4:7], v[68:71], v[32:35], v[4:7]\n\t" \
  "ds_read_b64_tr_b16 v[74:75], v83 offset:8192\n\t" \
  "ds_read_b128 v[32:35], v80 offset:32768\n\t" \
  "v_mfma_f32_16x16x32_bf16 v[8:11], v[64:67], v[36:39], v[8:11]\n\t" \
  "ds_read_b64_tr_b16 v[76:77], v84 offset:8192\n\t" \
  "v_mfma_f32_16x16x32_bf16 v[12:15], v[68:71], v[36:39], v[12:15]\n\t" \
  "ds_read_b64_tr_b16 v[78:79], v85 offset:8192\n\t" \
  "ds_read_b128 v[36:39], v80 offset:33792\n\t" \
  "s_add_u32 m0, %[lw], 98304\n\t" \
  "v_mfma_f32_16x16x32_bf16 v[16:19], v[64:67], v[40:43], v[16:19]\n\t" \
  "global_load_lds_dwordx4 v90, s[84:85] sc1\n\t" \
  "s_add_u32 s84, s84, s88\n\t" \
  "s_addc_u32 s85, s85, 0\n\t" \
  "v_mfma_f32_16x16x32_bf16 v[20:23], v[68:71], v[40:43], v[20:23]\n\t" \
  "ds_read_b128 v[40:43], v80 offset:34816\n\t" \
  "v_mfma_f32_16x16x32_bf16 v[24:27], v[64:67], v[44:47], v[24:27]\n\t" \
  "s_add_u32 m0, %[lw], 114688\n\t" \
  "v_mfma_f32_16x16x32_bf16 v[28:31], v[68:71], v[44:47], v[28:31]\n\t" \
  "ds_read_b128 v[44:47], v80 offset:35840\n\t" \
  "global_load_lds_dwordx4 v91, s[86:87]\n\t" \
  "s_add_u32 s86, s86, s89\n\t" \
  "s_addc_u32 s87, s87, 0\n\t" \
  "s_waitcnt lgkmcnt(3)\n\t" \
  "v_mfma_f32_16x16x32_bf16 v[0:3], v[72:75], v[48:51], v[0:3]\n\t" \
  "ds_read_b64_tr_b16 v[64:65], v82 offset:32768\n\t" \
  "v_mfma_f32_16x16x32_bf16 v[4:7], v[76:79], v[48:51], v[4:7]\n\t" \
  "ds_read_b64_tr_b16 v[66:67], v83 offset:32768\n\t" \
  "ds_read_b128 v[48:51], v80 offset:40960\n\t" \
  "v_mfma_f32_16x16x32_bf16 v[8:11], v[72:75], v[52:55], v[8:11]\n\t" \
  "ds_read_b64_tr_b16 v[68:69], v84 offset:32768\n\t" \
  "v_mfma_f32_16x16x32_bf16 v[12:15], v[76:79], v[52:55], v[12:15]\n\t" \
  "ds_read_b64_tr_b16 v[70:71], v85 offset:32768\n\t" \
  "ds_read_b128 v[52:55], v80 offset:41984\n\t" \
  "s_add_u32 m0, %[lw], 106496\n\t" \
  "v_mfma_f32_16x16x32_bf16 v[16:19], v[72:75], v[56:59], v[16:19]\n\t" \
  "global_load_lds_dwordx4 v90, s[84:85] sc1\n\t" \
  "s_add_u32 s84, s84, s88\n\t" \
  "s_addc_u32 s85, s85, 0\n\t" \
  "v_mfma_f32_16x16x32_bf16 v[20:23], v[76:79], v[56:59], v[20:23]\n\t" \
  "ds_read_b128 v[56:59], v80 offset:43008\n\t" \
  "v_mfma_f32_16x16x32_bf16 v[24:27], v[72:75], v[60:63], v[24:27]\n\t" \
  "s_add_u32 m0, %[lw], 122880\n\t" \
  "v_mfma_f32_16x16x32_bf16 v[28:31], v[76:79], v[60:63], v[28:31]\n\t" \
  "ds_read_b128 v[60:63], v80 offset:44032\n\t" \
  "global_load_lds_dwordx4 v91, s[86:87]\n\t" \
  "s_add_u32 s86, s86, s89\n\t" \
  "s_addc_u32 s87, s87, 0\n\t" \
  "s_sub_u32 s90, s90, 1\n\t" \
  "s_cselect_b32 s88, 0, s88\n\t" \
  "s_cselect_b32 s89, 0, s89\n\t" \
  "s_cselect_b32 s91, 16384, 0\n\t" \
  "s_sub_u32 s84, s84, s91\n\t" \
  "s_subb_u32 s85, s85, 0\n\t" \
  "s_lshl_b32 s91, s91, 2\n\t" \
  "s_sub_u32 s86, s86, s91\n\t" \
  "s_subb_u32 s87, s87, 0\n\t" \
  "s_waitcnt vmcnt(4)\n\t" \
  "s_barrier\n\t" \
  "s_waitcnt lgkmcnt(3)\n\t" \
  "v_mfma_f32_16x16x32_bf16 v[0:3], v[64:67], v[32:35], v[0:3]\n\t" \
  "ds_read_b64_tr_b16 v[72:73], v82 offset:40960\n\t" \
  "v_mfma_f32_16x16x32_bf16 v[4:7], v[68:71], v[32:35], v[4:7]\n\t" \
  "ds_read_b64_tr_b16 v[74:75], v83 offset:40960\n\t" \
  "ds_read_b128 v[32:35], v81 offset:0\n\t" \
  "v_mfma_f32_16x16x32_bf16 v[8:11], v[64:67], v[36:39], v[8:11]\n\t" \
  "ds_read_b64_tr_b16 v[76:77], v84 offset:40960\n\t" \
  "v_mfma_f32_16x16x32_bf16 v[12:15], v[68:71], v[36:39], v[12:15]\n\t" \
  "ds_read_b64_tr_b16 v[78:79], v85 offset:40960\n\t" \
  "ds_read_b128 v[36:39], v81 offset:1024\n\t" \
  "s_add_u32 m0, %[lw], 0\n\t" \
  "v_mfma_f32_16x16x32_bf16 v[16:19], v[64:67], v[40:43], v[16:19]\n\t" \
  "global_load_lds_dwordx4 v90, s[84:85] sc1\n\t" \
  "s_add_u32 s84, s84, s88\n\t" \
  "s_addc_u32 s85, s85, 0\n\t" \
  "v_mfma_f32_16x16x32_bf16 v[20:23], v[68:71], v[40:43], v[20:23]\n\t" \
  "ds_read_b128 v[40:43], v81 offset:2048\n\t" \
  "v_mfma_f32_16x16x32_bf16 v[24:27], v[64:67], v[44:47], v[24:27]\n\t" \
  "s_add_u32 m0, %[lw], 16384\n\t" \
  "v_mfma_f32_16x16x32_bf16 v[28:31], v[68:71], v[44:47], v[28:31]\n\t" \
  "ds_read_b128 v[44:47], v81 offset:3072\n\t" \
  "global_load_lds_dwordx4 v91, s[86:87]\n\t" \
  "s_add_u32 s86, s86, s89\n\t" \
  "s_addc_u32 s87, s87, 0\n\t" \
  "s_waitcnt lgkmcnt(3)\n\t" \
  "v_mfma_f32_16x16x32_bf16 v[0:3], v[72:75], v[48:51], v[0:3]\n\t" \
  "ds_read_b64_tr_b16 v[64:65], v86 offset:0\n\t" \
  "v_mfma_f32_16x16x32_bf16 v[4:7], v[76:79], v[48:51], v[4:7]\n\t" \
  "ds_read_b64_tr_b16 v[66:67], v87 offset:0\n\t" \
  "ds_read_b128 v[48:51], v81 offset:8192\n\t" \
  "v_mfma_f32_16x16x32_bf16 v[8:11], v[72:75], v[52:55], v[8:11]\n\t" \
  "ds_read_b64_tr_b16 v[68:69], v88 offset:0\n\t" \
  "v_mfma_f32_16x16x32_bf16 v[12:15], v[76:79], v[52:55], v[12:15]\n\t" \
  "ds_read_b64_tr_b16 v[70:71], v89 offset:0\n\t" \
  "ds_read_b128 v[52:55], v81 offset:9216\n\t" \
  "s_add_u32 m0, %[lw], 8192\n\t" \
  "v_mfma_f32_16x16x32_bf16 v[16:19], v[72:75], v[56:59], v[16:19]\n\t" \
  "global_load_lds_dwordx4 v90, s[84:85] sc1\n\t" \
  "s_add_u32 s84, s84, s88\n\t" \
  "s_addc_u32 s85, s85, 0\n\t" \
  "v_mfma_f32_16x16x32_bf16 v[20:23], v[76:79], v[56:59], v[20:23]\n\t" \
  "ds_read_b128 v[56:59], v81 offset:10240\n\t" \
  "v_mfma_f32_16x16x32_bf16 v[24:27], v[72:75], v[60:63], v[24:27]\n\t" \
  "s_add_u32 m0, %[lw], 24576\n\t" \
  "v_mfma_f32_16x16x32_bf16 v[28:31], v[76:79], v[60:63], v[28:31]\n\t" \
  "ds_read_b128 v[60:63], v81 offset:11264\n\t" \
  "global_load_lds_dwordx4 v91, s[86:87]\n\t" \
  "s_add_u32 s86, s86, s89\n\t" \
  "s_addc_u32 s87, s87, 0\n\t" \
  "s_waitcnt vmcnt(4)\n\t" \
  "s_barrier\n\t" \
  "s_waitcnt lgkmcnt(3)\n\t" \
  "v_mfma_f32_16x16x32_bf16 v[0:3], v[64:67], v[32:35], v[0:3]\n\t" \
  "ds_read_b64_tr_b16 v[72:73], v86 offset:8192\n\t" \
  "v_mfma_f32_16x16x32_bf16 v[4:7], v[68:71], v[32:35], v[4:7]\n\t" \
  "ds_read_b64_tr_b16 v[74:75], v87 offset:8192\n\t" \
  "ds_read_b128 v[32:35], v81 offset:32768\n\t" \
  "v_mfma_f32_16x16x32_bf16 v[8:11], v[64:67], v[36:39], v[8:11]\n\t" \
  "ds_read_b64_tr_b16 v[76:77], v88 offset:8192\n\t" \
  "v_mfma_f32_16x16x32_bf16 v[12:15], v[68:71], v[36:39], v[12:15]\n\t" \
  "ds_read_b64_tr_b16 v[78:79], v89 offset:8192\n\t" \
  "ds_read_b128 v[36:39], v81 offset:33792\n\t" \
  "s_add_u32 m0, %[lw], 32768\n\t" \
  "v_mfma_f32_16x16x32_bf16 v[16:19], v[64:67], v[40:43], v[16:19]\n\t" \
  "global_load_lds_dwordx4 v90, s[84:85] sc1\n\t" \
  "s_add_u32 s84, s84, s88\n\t" \
  "s_addc_u32 s85, s85, 0\n\t" \
  "v_mfma_f32_16x16x32_bf16 v[20:23], v[68:71], v[40:43], v[20:23]\n\t" \
  "ds_read_b128 v[40:43], v81 offset:34816\n\t" \
  "v_mfma_f32_16x16x32_bf16 v[24:27], v[64:67], v[44:47], v[24:27]\n\t" \
  "s_add_u32 m0, %[lw], 49152\n\t" \
  "v_mfma_f32_16x16x32_bf16 v[28:31], v[68:71], v[44:47], v[28:31]\n\t" \
  "ds_read_b128 v[44:47], v81 offset:35840\n\t" \
  "global_load_lds_dwordx4 v91, s[86:87]\n\t" \
  "s_add_u32 s86, s86, s89\n\t" \
  "s_addc_u32 s87, s87, 0\n\t" \
  "s_waitcnt lgkmcnt(3)\n\t" \
  "v_mfma_f32_16x16x32_bf16 v[0:3], v[72:75], v[48:51], v[0:3]\n\t" \
  "ds_read_b64_tr_b16 v[64:65], v86 offset:32768\n\t" \
  "v_mfma_f32_16x16x32_bf16 v[4:7], v[76:79], v[48:51], v[4:7]\n\t" \
  "ds_read_b64_tr_b16 v[66:67], v87 offset:32768\n\t" \
  "ds_read_b128 v[48:51], v81 offset:40960\n\t" \
  "v_mfma_f32_16x16x32_bf16 v[8:11], v[72:75], v[52:55], v[8:11]\n\t" \
  "ds_read_b64_tr_b16 v[68:69], v88 offset:32768\n\t" \
  "v_mfma_f32_16x16x32_bf16 v[12:15], v[76:79], v[52:55], v[12:15]\n\t" \
  "ds_read_b64_tr_b16 v[70:71], v89 offset:32768\n\t" \
  "ds_read_b128 v[52:55], v81 offset:41984\n\t" \
  "s_add_u32 m0, %[lw], 40960\n\t" \
  "v_mfma_f32_16x16x32_bf16 v[16:19], v[72:75], v[56:59], v[16:19]\n\t" \
  "global_load_lds_dwordx4 v90, s[84:85] sc1\n\t" \
  "s_add_u32 s84, s84, s88\n\t" \
  "s_addc_u32 s85, s85, 0\n\t" \
  "v_mfma_f32_16x16x32_bf16 v[20:23], v[76:79], v[56:59], v[20:23]\n\t" \
  "ds_read_b128 v[56:59], v81 offset:43008\n\t" \
  "v_mfma_f32_16x16x32_bf16 v[24:27], v[72:75], v[60:63], v[24:27]\n\t" \
  "s_add_u32 m0, %[lw], 57344\n\t" \
  "v_mfma_f32_16x16x32_bf16 v[28:31], v[76:79], v[60:63], v[28:31]\n\t" \
  "ds_read_b128 v[60:63], v81 offset:44032\n\t" \
  "global_load_lds_dwordx4 v91, s[86:87]\n\t" \
  "s_add_u32 s86, s86, s89\n\t" \
  "s_addc_u32 s87, s87, 0\n\t" \
  "s_waitcnt vmcnt(4)\n\t" \
  "s_barrier\n\t" \
  "s_waitcnt lgkmcnt(3)\n\t" \
  "v_mfma_f32_16x16x32_bf16 v[0:3], v[64:67], v[32:35], v[0:3]\n\t" \
  "ds_read_b64_tr_b16 v[72:73], v86 offset:40960\n\t" \
  "v_mfma_f32_16x16x32_bf16 v[4:7], v[68:71], v[32:35], v[4:7]\n\t" \
  "ds_read_b64_tr_b16 v[74:75], v87 offset:40960\n\t" \
  "ds_read_b128 v[32:35], v80 offset:0\n\t" \
  "v_mfma_f32_16x16x32_bf16 v[8:11], v[64:67], v[36:39], v[8:11]\n\t" \
  "ds_read_b64_tr_b16 v[76:77], v88 offset:40960\n\t" \
  "v_mfma_f32_16x16x32_bf16 v[12:15], v[68:71], v[36:39], v[12:15]\n\t" \
  "ds_read_b64_tr_b16 v[78:79], v89 offset:40960\n\t" \
  "ds_read_b128 v[36:39], v80 offset:1024\n\t" \
  "s_add_u32 m0, %[lw], 65536\n\t" \
  "v_mfma_f32_16x16x32_bf16 v[16:19], v[64:67], v[40:43], v[16:19]\n\t" \
  "global_load_lds_dwordx4 v90, s[84:85] sc1\n\t" \
  "s_add_u32 s84, s84, s88\n\t" \
  "s_addc_u32 s85, s85, 0\n\t" \
  "v_mfma_f32_16x16x32_bf16 v[20:23], v[68:71], v[40:43], v[20:23]\n\t" \
  "ds_read_b128 v[40:43], v80 offset:2048\n\t" \
  "v_mfma_f32_16x16x32_bf16 v[24:27], v[64:67], v[44:47], v[24:27]\n\t" \
  "s_add_u32 m0, %[lw], 81920\n\t" \
  "v_mfma_f32_16x16x32_bf16 v[28:31], v[68:71], v[44:47], v[28:31]\n\t" \
  "ds_read_b128 v[44:47], v80 offset:3072\n\t" \
  "global_load_lds_dwordx4 v91, s[86:87]\n\t" \
  "s_add_u32 s86, s86, s89\n\t" \
  "s_addc_u32 s87, s87, 0\n\t" \
  "s_waitcnt lgkmcnt(3)\n\t" \
  "v_mfma_f32_16x16x32_bf16 v[0:3], v[72:75], v[48:51], v[0:3]\n\t" \
  "ds_read_b64_tr_b16 v[64:65], v82 offset:0\n\t" \
  "v_mfma_f32_16x16x32_bf16 v[4:7], v[76:79], v[48:51], v[4:7]\n\t" \
  "ds_read_b64_tr_b16 v[66:67], v83 offset:0\n\t" \
  "ds_read_b128 v[48:51], v80 offset:8192\n\t" \
  "v_mfma_f32_16x16x32_bf16 v[8:11], v[72:75], v[52:55], v[8:11]\n\t" \
  "ds_read_b64_tr_b16 v[68:69], v84 offset:0\n\t" \
  "v_mfma_f32_16x16x32_bf16 v[12:15], v[76:79], v[52:55], v[12:15]\n\t" \
  "ds_read_b64_tr_b16 v[70:71], v85 offset:0\n\t" \
  "ds_read_b128 v[52:55], v80 offset:9216\n\t" \
  "s_add_u32 m0, %[lw], 73728\n\t" \
  "v_mfma_f32_16x16x32_bf16 v[16:19], v[72:75], v[56:59], v[16:19]\n\t" \
  "global_load_lds_dwordx4 v90, s[84:85] sc1\n\t" \
  "s_add_u32 s84, s84, s88\n\t" \
  "s_addc_u32 s85, s85, 0\n\t" \
  "v_mfma_f32_16x16x32_bf16 v[20:23], v[76:79], v[56:59], v[20:23]\n\t" \
  "ds_read_b128 v[56:59], v80 offset:10240\n\t" \
  "v_mfma_f32_16x16x32_bf16 v[24:27], v[72:75], v[60:63], v[24:27]\n\t" \
  "s_add_u32 m0, %[lw], 90112\n\t" \
  "v_mfma_f32_16x16x32_bf16 v[28:31], v[76:79], v[60:63], v[28:31]\n\t" \
  "ds_read_b128 v[60:63], v80 offset:11264\n\t" \
  "global_load_lds_dwordx4 v91, s[86:87]\n\t" \
  "s_add_u32 s86, s86, s89\n\t" \
  "s_addc_u32 s87, s87, 0\n\t" \
  "s_cmp_lg_u32 s88, 0\n\t" \
  "s_cbranch_scc1 .Lp1%=\n\t" \
  ".Le%=:\n\t" \
  "s_waitcnt vmcnt(0) lgkmcnt(0)\n\t" \
  "s_mov_b32 m0, s94\n\t" \
  "s_setprio 0\n\t" \
  "s_nop 7\n\t"
#define HSL 16
__device__ __forceinline__ void job_down(const Args& p, int rblk, int ct, char* smem, __amdgpu_buffer_rsrc_t rsy) {
  if (threadIdx.x < 64) {
    for (unsigned spins = 0;;) {
      asm volatile("" ::: "memory");
      if (__builtin_amdgcn_readfirstlane((int)ld_flag0(rsy, SY_CH + (rblk >> 1) * 128)) >= 27) break;
      __builtin_amdgcn_s_sleep(HSL);
      if (spin_fail(spins, (gu32*)(p.ws + WS_SY + SY_ABORT))) break;
    }
  }
  int tid = threadIdx.x;
  asm volatile("" : "+v"(tid));
  const int lane = tid & 63, r = lane & 15, q = lane >> 4;
  const int wid = __builtin_amdgcn_readfirstlane(tid >> 6), wr = wid >> 2, wc = wid & 3;
  typedef __attribute__((ext_vector_type(32))) float f32x32;
  typedef __attribute__((ext_vector_type(8))) unsigned u32x8;
  f32x32 acc;
  {
    const char* A = p.ws + WS_HF + h_off(p, rblk);
    const char* B = (const char*)(p.wd + ct * 128);
    const unsigned rb = tid >> 4, va = tid * 16, vb = ((rb & 4) * 4 + (rb >> 3) * 4 + (rb & 3)) * (DM * 2) + 16 * ((tid & 15) ^ swzB(rb & 15));
    u32x8 lb;
#pragma unroll
    for (int i = 0; i < 8; ++i)
      lb[i] = (i >> 2) * 65536 + 16384 + offB(8 * q + 4 * (i & 1) + (r >> 2), 2 * (wc * 2 + ((i >> 1) & 1)) + ((r & 3) >> 1)) + 8 * (r & 1);
    LBAR();
#pragma unroll
    for (int t = 0; t < 3; ++t) {
      glds2<true>(A + t * 32768, 16384, va, wid * 1024 + t * 32768);
      glds2(B + t * 64 * DM * 2, 32 * DM * 2, vb, wid * 1024 + t * 32768 + 16384);
    }
    WAIT_V(8);
    LBAR();
    asm volatile(DN_ASM
        : "={v[0:31]}"(acc)
        : "{v[80:81]}"((u32x2){wr * 4096u + lane * 16u, 65536u + wr * 4096u + lane * 16u}), "{v[82:89]}"(lb), "{v90}"(va), "{v91}"(vb),
          [pa] "s"(A + 3 * 32768), [pb] "s"(B + 3 * 64 * DM * 2), [lw] "s"(wid * 1024), [hf] "s"(wr)
        : CLB8(32, 33, 34, 35, 36, 37, 38, 39), CLB8(40, 41, 42, 43, 44, 45, 46, 47), CLB8(48, 49, 50, 51, 52, 53, 54, 55),
          CLB8(56, 57, 58, 59, 60, 61, 62, 63), CLB8(64, 65, 66, 67, 68, 69, 70, 71), CLB8(72, 73, 74, 75, 76, 77, 78, 79),
          "s84", "s85", "s86", "s87", "s88", "s89", "s90", "s91", "s94", "scc", "memory");
  }
  wait_s(p, rsy, rblk, ct * 4, 4, rblk < NFUSE);
  const int e0 = (rblk * 128 + wr * 64 + r) * DM + ct * 128 + wc * 32 + q * 8;
  SX sx[4];
#pragma unroll
  for (int m = 0; m < 4; ++m) sx[m] = get_sx(p, rblk, e0 + m * 16 * DM);
  const __amdgpu_buffer_rsrc_t ro = mkrsrc(p.ws + WS_G, S_LEN * DM * 2);
#pragma unroll
  for (int m = 0; m < 4; ++m) {
    float rr[8];
#pragma unroll
    for (int j = 0; j < 8; ++j) { float d = rbf(acc[8 * m + j]); rr[j] = d * __builtin_amdgcn_rcpf(1.0f + __expf(-d)); }
    put_g(p, ro, rblk, e0 + m * 16 * DM, widen32(cvtpk(rr[0], rr[1]), cvtpk(rr[2], rr[3]), cvtpk(rr[4], rr[5]), cvtpk(rr[6], rr[7])), sx[m]);
  }
  if (rblk >= NFUSE) publish_flags(rsy, SY_FG + (rblk * 8 + ct) * 128, 0, 1);
}

#define PR_ASM_0 \
  "s_cmp_lg_u32 %[kh], 0\n\t" \
  "s_cbranch_scc1 .Lk%=\n\t" \
  "s_mov_b32 s94, m0\n\t" \
  "s_mov_b64 s[92:93], %[x0]\n\t" \
  "s_mov_b32 s86, %[g0]\n\t" \
  "s_add_u32 s87, s86, %[t0]\n\t" \
  "s_sub_i32 s88, s86, 3\n\t" \
  "ds_read_b128 v[176:179], v208 offset:0\n\t" \
  "ds_read_b128 v[180:183], v208 offset:16384\n\t" \
  "ds_read_b128 v[184:187], v209 offset:0\n\t" \
  "ds_read_b128 v[188:191], v209 offset:16384\n\t" \
  "ds_read_b128 v[192:195], v210 offset:0\n\t" \
  "ds_read_b128 v[196:199], v210 offset:16384\n\t" \
  "ds_read_b128 v[200:203], v211 offset:0\n\t" \
  "ds_read_b128 v[204:207], v211 offset:16384\n\t" \
  "s_branch .Lfb00%=\n\t" \
  ".Lp0%=:\n\t" \
  "s_cmp_le_u32 s86, 5\n\t" \
  "s_cbranch_scc1 .Lf00%=\n\t" \
  ".Lfb00%=:\n\t" \
  "s_waitcnt lgkmcnt(6)\n\t" \
  "v_mfma_f32_16x16x32_bf16 v[128:131], v[0:3], v[176:179], 0\n\t" \
  "v_mfma_f32_16x16x32_bf16 v[132:135], v[4:7], v[176:179], 0\n\t" \
  "ds_read_b128 v[176:179], v208 offset:256\n\t" \
  "s_add_u32 m0, %[lw], 67584\n\t" \
  "v_mfma_f32_16x16x32_bf16 v[136:139], v[0:3], v[180:183], 0\n\t" \
  "v_mfma_f32_16x16x32_bf16 v[140:143], v[4:7], v[180:183], 0\n\t" \
  "ds_read_b128 v[180:183], v208 offset:16640\n\t" \
  "global_load_lds_dwordx4 v218, s[92:93] sc1\n\t" \
  "s_waitcnt lgkmcnt(6)\n\t" \
  "v_mfma_f32_16x16x32_bf16 v[128:131], v[8:11], v[184:187], v[128:131]\n\t" \
  "v_mfma_f32_16x16x32_bf16 v[132:135], v[12:15], v[184:187], v[132:135]\n\t" \
  "ds_read_b128 v[184:187], v209 offset:256\n\t" \
  "v_mfma_f32_16x16x32_bf16 v[136:139], v[8:11], v[188:191], v[136:139]\n\t" \
  "v_mfma_f32_16x16x32_bf16 v[140:143], v[12:15], v[188:191], v[140:143]\n\t" \
  "ds_read_b128 v[188:191], v209 offset:16640\n\t" \
  "ds_write_b128 v220, v[152:155] offset:0\n\t" \
  "ds_write_b128 v220, v[156:159] offset:1024\n\t" \
  "s_waitcnt lgkmcnt(8)\n\t" \
  "v_mfma_f32_16x16x32_bf16 v[128:131], v[16:19], v[192:195], v[128:131]\n\t" \
  "v_mfma_f32_16x16x32_bf16 v[132:135], v[20:23], v[192:195], v[132:135]\n\t" \
  "ds_read_b128 v[192:195], v210 offset:256\n\t" \
  "s_add_u32 m0, %[lw], 68608\n\t" \
  "v_mfma_f32_16x16x32_bf16 v[136:139], v[16:19], v[196:199], v[136:139]\n\t" \
  "v_mfma_f32_16x16x32_bf16 v[140:143], v[20:23], v[196:199], v[140:143]\n\t" \
  "ds_read_b128 v[196:199], v210 offset:16640\n\t" \
  "global_load_lds_dwordx4 v219, s[92:93] sc1\n\t" \
  "s_lshr_b32 s90, s87, 7\n\t" \
  "s_xor_b32 s90, s90, %[s0]\n\t" \
  "s_and_b32 s90, s90, 1\n\t" \
  "s_lshl1_add_u32 s90, s87, s90\n\t" \
  "s_lshl_b32 s90, s90, 13\n\t" \
  "s_cmp_lt_i32 s86, 6\n\t" \
  "s_cselect_b32 s90, 0x70000000, s90\n\t" \
  "s_waitcnt lgkmcnt(8)\n\t" \
  "v_mfma_f32_16x16x32_bf16 v[128:131], v[24:27], v[200:203], v[128:131]\n\t" \
  "v_mfma_f32_16x16x32_bf16 v[132:135], v[28:31], v[200:203], v[132:135]\n\t" \
  "ds_read_b128 v[200:203], v211 offset:256\n\t" \
  "v_mfma_f32_16x16x32_bf16 v[136:139], v[24:27], v[204:207], v[136:139]\n\t" \
  "v_mfma_f32_16x16x32_bf16 v[140:143], v[28:31], v[204:207], v[140:143]\n\t" \
  "ds_read_b128 v[204:207], v211 offset:16640\n\t" \
  "s_waitcnt lgkmcnt(4)\n\t" \
  "s_cmp_lt_u32 s86, 6\n\t" \
  "s_cbranch_scc1 .Lw00%=\n\t" \
  "s_waitcnt vmcnt(4)\n\t" \
  "s_barrier\n\t" \
  ".Lg00%=:\n\t" \
  "s_add_u32 s92, s92, 512\n\t" \
  "s_addc_u32 s93, s93, 0\n\t" \
  "v_mfma_f32_16x16x32_bf16 v[128:131], v[32:35], v[176:179], v[128:131]\n\t" \
  "v_mfma_f32_16x16x32_bf16 v[132:135], v[36:39], v[176:179], v[132:135]\n\t" \
  "ds_read_b128 v[176:179], v208 offset:32768\n\t" \
  "s_add_u32 m0, %[lw], 98304\n\t" \
  "v_mfma_f32_16x16x32_bf16 v[136:139], v[32:35], v[180:183], v[136:139]\n\t" \
  "v_mfma_f32_16x16x32_bf16 v[140:143], v[36:39], v[180:183], v[140:143]\n\t" \
  "ds_read_b128 v[180:183], v208 offset:49152\n\t" \
  "global_load_lds_dwordx4 v216, s[92:93] sc1\n\t" \
  "ds_read_b128 v[160:163], v221 offset:0\n\t" \
  "ds_read_b128 v[164:167], v221 offset:1024\n\t" \
  "v_mfma_f32_16x16x32_bf16 v[128:131], v[40:43], v[184:187], v[128:131]\n\t" \
  "v_mfma_f32_16x16x32_bf16 v[132:135], v[44:47], v[184:187], v[132:135]\n\t" \
  "ds_read_b128 v[184:187], v209 offset:32768\n\t" \
  "v_mfma_f32_16x16x32_bf16 v[136:139], v[40:43], v[188:191], v[136:139]\n\t" \
  "v_mfma_f32_16x16x32_bf16 v[140:143], v[44:47], v[188:191], v[140:143]\n\t" \
  "ds_read_b128 v[188:191], v209 offset:49152\n\t" \
  "s_waitcnt lgkmcnt(8)\n\t" \
  "v_mfma_f32_16x16x32_bf16 v[128:131], v[48:51], v[192:195], v[128:131]\n\t" \
  "v_mfma_f32_16x16x32_bf16 v[132:135], v[52:55], v[192:195], v[132:135]\n\t" \
  "ds_read_b128 v[192:195], v210 offset:32768\n\t" \
  "s_add_u32 m0, %[lw], 99328\n\t" \
  "v_mfma_f32_16x16x32_bf16 v[136:139], v[48:51], v[196:199], v[136:139]\n\t" \
  "v_mfma_f32_16x16x32_bf16 v[140:143], v[52:55], v[196:199], v[140:143]\n\t" \
  "ds_read_b128 v[196:199], v210 offset:49152\n\t" \
  "global_load_lds_dwordx4 v217, s[92:93] sc1\n\t" \
  "s_waitcnt lgkmcnt(4)\n\t" \
  "v_add_f32 v160, v160, v144\n\t" \
  "v_add_f32 v161, v161, v145\n\t" \
  "v_add_f32 v162, v162, v146\n\t" \
  "v_add_f32 v163, v163, v147\n\t" \
  "v_add_f32 v164, v164, v148\n\t" \
  "v_add_f32 v165, v165, v149\n\t" \
  "v_add_f32 v166, v166, v150\n\t" \
  "v_add_f32 v167, v167, v151\n\t" \
  "v_mfma_f32_16x16x32_bf16 v[128:131], v[56:59], v[200:203], v[128:131]\n\t" \
  "v_mfma_f32_16x16x32_bf16 v[132:135], v[60:63], v[200:203], v[132:135]\n\t" \
  "ds_read_b128 v[200:203], v211 offset:32768\n\t" \
  "v_mfma_f32_16x16x32_bf16 v[136:139], v[56:59], v[204:207], v[136:139]\n\t" \
  "v_mfma_f32_16x16x32_bf16 v[140:143], v[60:63], v[204:207], v[140:143]\n\t" \
  "ds_read_b128 v[204:207], v211 offset:49152\n\t" \
  "v_cvt_pk_bf16_f32 v168, v160, v161\n\t" \
  "v_cvt_pk_bf16_f32 v169, v162, v163\n\t" \
  "v_cvt_pk_bf16_f32 v170, v164, v165\n\t" \
  "v_cvt_pk_bf16_f32 v171, v166, v167\n\t" \
  "buffer_store_dwordx4 v[168:171], v172, %[rt], s90 offen sc1\n\t" \
  "v_mfma_f32_16x16x32_bf16 v[128:131], v[64:67], v[176:179], v[128:131]\n\t" \
  "v_mfma_f32_16x16x32_bf16 v[132:135], v[68:71], v[176:179], v[132:135]\n\t" \
  "ds_read_b128 v[176:179], v208 offset:33024\n\t" \
  "s_add_u32 m0, %[lw], 100352\n\t" \
  "v_mfma_f32_16x16x32_bf16 v[136:139], v[64:67], v[180:183], v[136:139]\n\t" \
  "v_mfma_f32_16x16x32_bf16 v[140:143], v[68:71], v[180:183], v[140:143]\n\t" \
  "ds_read_b128 v[180:183], v208 offset:49408\n\t" \
  "global_load_lds_dwordx4 v218, s[92:93] sc1\n\t" \
  "s_waitcnt lgkmcnt(6)\n\t" \
  "v_mfma_f32_16x16x32_bf16 v[128:131], v[72:75], v[184:187], v[128:131]\n\t" \
  "v_mfma_f32_16x16x32_bf16 v[132:135], v[76:79], v[184:187], v[132:135]\n\t" \
  "ds_read_b128 v[184:187], v209 offset:33024\n\t" \
  "v_mfma_f32_16x16x32_bf16 v[136:139], v[72:75], v[188:191], v[136:139]\n\t" \
  "v_mfma_f32_16x16x32_bf16 v[140:143], v[76:79], v[188:191], v[140:143]\n\t" \
  "ds_read_b128 v[188:191], v209 offset:49408\n\t" \
  "s_waitcnt lgkmcnt(6)\n\t" \
  "v_mfma_f32_16x16x32_bf16 v[128:131], v[80:83], v[192:195], v[128:131]\n\t" \
  "v_mfma_f32_16x16x32_bf16 v[132:135], v[84:87], v[192:195], v[132:135]\n\t" \
  "ds_read_b128 v[192:195], v210 offset:33024\n\t" \
  "s_add_u32 m0, %[lw], 101376\n\t" \
  "v_mfma_f32_16x16x32_bf16 v[136:139], v[80:83], v[196:199], v[136:139]\n\t" \
  "v_mfma_f32_16x16x32_bf16 v[140:143], v[84:87], v[196:199], v[140:143]\n\t" \
  "ds_read_b128 v[196:199], v210 offset:49408\n\t" \
  "global_load_lds_dwordx4 v219, s[92:93] sc1\n\t" \
  "s_waitcnt lgkmcnt(6)\n\t" \
  "v_mfma_f32_16x16x32_bf16 v[128:131], v[88:91], v[200:203], v[128:131]\n\t" \
  "v_mfma_f32_16x16x32_bf16 v[132:135], v[92:95], v[200:203], v[132:135]\n\t" \
  "ds_read_b128 v[200:203], v211 offset:33024\n\t" \
  "v_mfma_f32_16x16x32_bf16 v[136:139], v[88:91], v[204:207], v[136:139]\n\t" \
  "v_mfma_f32_16x16x32_bf16 v[140:143], v[92:95], v[204:207], v[140:143]\n\t" \
  "ds_read_b128 v[204:207], v211 offset:49408\n\t" \
  "s_cmp_lt_u32 s86, 8\n\t" \
  "s_cbranch_scc1 .Lw10%=\n\t" \
  "s_waitcnt vmcnt(5)\n\t" \
  "s_barrier\n\t" \
  ".Lg10%=:\n\t" \
  "s_add_u32 s92, s92, 65024\n\t" \
  "s_addc_u32 s93, s93, 0\n\t" \
  "s_waitcnt lgkmcnt(6)\n\t" \
  "v_mfma_f32_16x16x32_bf16 v[128:131], v[96:99], v[176:179], v[128:131]\n\t" \
  "v_mfma_f32_16x16x32_bf16 v[132:135], v[100:103], v[176:179], v[132:135]\n\t" \
  "ds_read_b128 v[176:179], v212 offset:0\n\t" \
  "s_add_u32 m0, %[lw], 0\n\t" \
  "v_mfma_f32_16x16x32_bf16 v[136:139], v[96:99], v[180:183], v[136:139]\n\t" \
  "v_mfma_f32_16x16x32_bf16 v[140:143], v[100:103], v[180:183], v[140:143]\n\t" \
  "ds_read_b128 v[180:183], v212 offset:16384\n\t" \
  "global_load_lds_dwordx4 v216, s[92:93] sc1\n\t" \
  "s_waitcnt lgkmcnt(6)\n\t" \
  "v_mfma_f32_16x16x32_bf16 v[128:131], v[104:107], v[184:187], v[128:131]\n\t" \
  "v_mfma_f32_16x16x32_bf16 v[132:135], v[108:111], v[184:187], v[132:135]\n\t" \
  "ds_read_b128 v[184:187], v213 offset:0\n\t" \
  "v_mfma_f32_16x16x32_bf16 v[136:139], v[104:107], v[188:191], v[136:139]\n\t" \
  "v_mfma_f32_16x16x32_bf16 v[140:143], v[108:111], v[188:191], v[140:143]\n\t" \
  "ds_read_b128 v[188:191], v213 offset:16384\n\t" \
  "s_waitcnt lgkmcnt(6)\n\t" \
  "v_mfma_f32_16x16x32_bf16 v[128:131], v[112:115], v[192:195], v[128:131]\n\t" \
  "v_mfma_f32_16x16x32_bf16 v[132:135], v[116:119], v[192:195], v[132:135]\n\t" \
  "ds_read_b128 v[192:195], v214 offset:0\n\t" \
  "s_add_u32 m0, %[lw], 1024\n\t" \
  "v_mfma_f32_16x16x32_bf16 v[136:139], v[112:115], v[196:199], v[136:139]\n\t" \
  "v_mfma_f32_16x16x32_bf16 v[140:143], v[116:119], v[196:199], v[140:143]\n\t" \
  "ds_read_b128 v[196:199], v214 offset:16384\n\t" \
  "global_load_lds_dwordx4 v217, s[92:93] sc1\n\t" \
  "s_waitcnt lgkmcnt(6)\n\t" \
  "v_mfma_f32_16x16x32_bf16 v[128:131], v[120:123], v[200:203], v[128:131]\n\t" \
  "v_mfma_f32_16x16x32_bf16 v[132:135], v[124:127], v[200:203], v[132:135]\n\t" \
  "ds_read_b128 v[200:203], v215 offset:0\n\t" \
  "v_mfma_f32_16x16x32_bf16 v[136:139], v[120:123], v[204:207], v[136:139]\n\t" \
  "v_mfma_f32_16x16x32_bf16 v[140:143], v[124:127], v[204:207], v[140:143]\n\t" \
  "ds_read_b128 v[204:207], v215 offset:16384\n\t" \
  "s_add_u32 s86, s86, 1\n\t" \
  "s_add_u32 s87, s87, 1\n\t" \
  "s_cmp_le_u32 s86, 5\n\t" \
  "s_cbranch_scc1 .Lf10%=\n\t" \
  ".Lfb10%=:\n\t" \
  "s_waitcnt lgkmcnt(6)\n\t" \
  "v_mfma_f32_16x16x32_bf16 v[144:147], v[0:3], v[176:179], 0\n\t" \
  "v_mfma_f32_16x16x32_bf16 v[148:151], v[4:7], v[176:179], 0\n\t" \
  "ds_read_b128 v[176:179], v212 offset:256\n\t" \
  "s_add_u32 m0, %[lw], 2048\n\t" \
  "v_mfma_f32_16x16x32_bf16 v[152:155], v[0:3], v[180:183], 0\n\t" \
  "v_mfma_f32_16x16x32_bf16 v[156:159], v[4:7], v[180:183], 0\n\t" \
  "ds_read_b128 v[180:183], v212 offset:16640\n\t" \
  "global_load_lds_dwordx4 v218, s[92:93] sc1\n\t" \
  "s_waitcnt lgkmcnt(6)\n\t" \
  "v_mfma_f32_16x16x32_bf16 v[144:147], v[8:11], v[184:187], v[144:147]\n\t" \
  "v_mfma_f32_16x16x32_bf16 v[148:151], v[12:15], v[184:187], v[148:151]\n\t" \
  "ds_read_b128 v[184:187], v213 offset:256\n\t" \
  "v_mfma_f32_16x16x32_bf16 v[152:155], v[8:11], v[188:191], v[152:155]\n\t" \
  "v_mfma_f32_16x16x32_bf16 v[156:159], v[12:15], v[188:191], v[156:159]\n\t" \
  "ds_read_b128 v[188:191], v213 offset:16640\n\t" \
  "ds_write_b128 v220, v[136:139] offset:0\n\t" \
  "ds_write_b128 v220, v[140:143] offset:1024\n\t" \
  "s_waitcnt lgkmcnt(8)\n\t" \
  "v_mfma_f32_16x16x32_bf16 v[144:147], v[16:19], v[192:195], v[144:147]\n\t" \
  "v_mfma_f32_16x16x32_bf16 v[148:151], v[20:23], v[192:195], v[148:151]\n\t" \
  "ds_read_b128 v[192:195], v214 offset:256\n\t" \
  "s_add_u32 m0, %[lw], 3072\n\t" \
  "v_mfma_f32_16x16x32_bf16 v[152:155], v[16:19], v[196:199], v[152:155]\n\t" \
  "v_mfma_f32_16x16x32_bf16 v[156:159], v[20:23], v[196:199], v[156:159]\n\t" \
  "ds_read_b128 v[196:199], v214 offset:16640\n\t" \
  "global_load_lds_dwordx4 v219, s[92:93] sc1\n\t" \
  "s_lshr_b32 s90, s87, 7\n\t" \
  "s_xor_b32 s90, s90, %[s0]\n\t" \
  "s_and_b32 s90, s90, 1\n\t" \
  "s_lshl1_add_u32 s90, s87, s90\n\t" \
  "s_lshl_b32 s90, s90, 13\n\t" \
  "s_cmp_lt_i32 s86, 6\n\t" \
  "s_cselect_b32 s90, 0x70000000, s90\n\t" \
  "s_waitcnt lgkmcnt(8)\n\t" \
  "v_mfma_f32_16x16x32_bf16 v[144:147], v[24:27], v[200:203], v[144:147]\n\t" \
  "v_mfma_f32_16x16x32_bf16 v[148:151], v[28:31], v[200:203], v[148:151]\n\t" \
  "ds_read_b128 v[200:203], v215 offset:256\n\t" \
  "v_mfma_f32_16x16x32_bf16 v[152:155], v[24:27], v[204:207], v[152:155]\n\t" \
  "v_mfma_f32_16x16x32_bf16 v[156:159], v[28:31], v[204:207], v[156:159]\n\t" \
  "ds_read_b128 v[204:207], v215 offset:16640\n\t" \
  "s_waitcnt lgkmcnt(4)\n\t" \
  "s_cmp_lt_u32 s86, 6\n\t" \
  "s_cbranch_scc1 .Lw20%=\n\t" \
  "s_waitcnt vmcnt(4)\n\t" \
  "s_barrier\n\t" \
  ".Lg20%=:\n\t" \
  "s_add_u32 s92, s92, 512\n\t" \
  "s_addc_u32 s93, s93, 0\n\t" \
  "v_mfma_f32_16x16x32_bf16 v[144:147], v[32:35], v[176:179], v[144:147]\n\t" \
  "v_mfma_f32_16x16x32_bf16 v[148:151], v[36:39], v[176:179], v[148:151]\n\t" \
  "ds_read_b128 v[176:179], v212 offset:32768\n\t" \
  "s_add_u32 m0, %[lw], 32768\n\t" \
  "v_mfma_f32_16x16x32_bf16 v[152:155], v[32:35], v[180:183], v[152:155]\n\t" \
  "v_mfma_f32_16x16x32_bf16 v[156:159], v[36:39], v[180:183], v[156:159]\n\t" \
  "ds_read_b128 v[180:183], v212 offset:49152\n\t" \
  "global_load_lds_dwordx4 v216, s[92:93] sc1\n\t" \
  "ds_read_b128 v[160:163], v221 offset:0\n\t" \
  "ds_read_b128 v[164:167], v221 offset:1024\n\t" \
  "v_mfma_f32_16x16x32_bf16 v[144:147], v[40:43], v[184:187], v[144:147]\n\t" \
  "v_mfma_f32_16x16x32_bf16 v[148:151], v[44:47], v[184:187], v[148:151]\n\t" \
  "ds_read_b128 v[184:187], v213 offset:32768\n\t" \
  "v_mfma_f32_16x16x32_bf16 v[152:155], v[40:43], v[188:191], v[152:155]\n\t" \
  "v_mfma_f32_16x16x32_bf16 v[156:159], v[44:47], v[188:191], v[156:159]\n\t" \
  "ds_read_b128 v[188:191], v213 offset:49152\n\t" \
  "s_waitcnt lgkmcnt(8)\n\t" \
  "v_mfma_f32_16x16x32_bf16 v[144:147], v[48:51], v[192:195], v[144:147]\n\t" \
  "v_mfma_f32_16x16x32_bf16 v[148:151], v[52:55], v[192:195], v[148:151]\n\t" \
  "ds_read_b128 v[192:195], v214 offset:32768\n\t" \
  "s_add_u32 m0, %[lw], 33792\n\t" \
  "v_mfma_f32_16x16x32_bf16 v[152:155], v[48:51], v[196:199], v[152:155]\n\t" \
  "v_mfma_f32_16x16x32_bf16 v[156:159], v[52:55], v[196:199], v[156:159]\n\t" \
  "ds_read_b128 v[196:199], v214 offset:49152\n\t" \
  "global_load_lds_dwordx4 v217, s[92:93] sc1\n\t" \
  "s_waitcnt lgkmcnt(4)\n\t" \
  "v_add_f32 v160, v160, v128\n\t" \
  "v_add_f32 v161, v161, v129\n\t" \
  "v_add_f32 v162, v162, v130\n\t" \
  "v_add_f32 v163, v163, v131\n\t" \
  "v_add_f32 v164, v164, v132\n\t" \
  "v_add_f32 v165, v165, v133\n\t" \
  "v_add_f32 v166, v166, v134\n\t" \
  "v_add_f32 v167, v167, v135\n\t" \
  "v_mfma_f32_16x16x32_bf16 v[144:147], v[56:59], v[200:203], v[144:147]\n\t" \
  "v_mfma_f32_16x16x32_bf16 v[148:151], v[60:63], v[200:203], v[148:151]\n\t" \
  "ds_read_b128 v[200:203], v215 offset:32768\n\t" \
  "v_mfma_f32_16x16x32_bf16 v[152:155], v[56:59], v[204:207], v[152:155]\n\t" \
  "v_mfma_f32_16x16x32_bf16 v[156:159], v[60:63], v[204:207], v[156:159]\n\t" \
  "ds_read_b128 v[204:207], v215 offset:49152\n\t" \
  "v_cvt_pk_bf16_f32 v168, v160, v161\n\t" \
  "v_cvt_pk_bf16_f32 v169, v162, v163\n\t" \
  "v_cvt_pk_bf16_f32 v170, v164, v165\n\t" \
  "v_cvt_pk_bf16_f32 v171, v166, v167\n\t" \
  "buffer_store_dwordx4 v[168:171], v172, %[rt], s90 offen sc1\n\t" \
  "v_mfma_f32_16x16x32_bf16 v[144:147], v[64:67], v[176:179], v[144:147]\n\t" \
  "v_mfma_f32_16x16x32_bf16 v[148:151], v[68:71], v[176:179], v[148:151]\n\t" \
  "ds_read_b128 v[176:179], v212 offset:33024\n\t" \
  "s_add_u32 m0, %[lw], 34816\n\t" \
  "v_mfma_f32_16x16x32_bf16 v[152:155], v[64:67], v[180:183], v[152:155]\n\t" \
  "v_mfma_f32_16x16x32_bf16 v[156:159], v[68:71], v[180:183], v[156:159]\n\t" \
  "ds_read_b128 v[180:183], v212 offset:49408\n\t" \
  "global_load_lds_dwordx4 v218, s[92:93] sc1\n\t" \
  "s_waitcnt lgkmcnt(6)\n\t" \
  "v_mfma_f32_16x16x32_bf16 v[144:147], v[72:75], v[184:187], v[144:147]\n\t" \
  "v_mfma_f32_16x16x32_bf16 v[148:151], v[76:79], v[184:187], v[148:151]\n\t" \
  "ds_read_b128 v[184:187], v213 offset:33024\n\t" \
  "v_mfma_f32_16x16x32_bf16 v[152:155], v[72:75], v[188:191], v[152:155]\n\t" \
  "v_mfma_f32_16x16x32_bf16 v[156:159], v[76:79], v[188:191], v[156:159]\n\t" \
  "ds_read_b128 v[188:191], v213 offset:49408\n\t" \
  "s_waitcnt lgkmcnt(6)\n\t" \
  "v_mfma_f32_16x16x32_bf16 v[144:147], v[80:83], v[192:195], v[144:147]\n\t" \
  "v_mfma_f32_16x16x32_bf16 v[148:151], v[84:87], v[192:195], v[148:151]\n\t" \
  "ds_read_b128 v[192:195], v214 offset:33024\n\t" \
  "s_add_u32 m0, %[lw], 35840\n\t" \
  "v_mfma_f32_16x16x32_bf16 v[152:155], v[80:83], v[196:199], v[152:155]\n\t" \
  "v_mfma_f32_16x16x32_bf16 v[156:159], v[84:87], v[196:199], v[156:159]\n\t" \
  "ds_read_b128 v[196:199], v214 offset:49408\n\t" \
  "global_load_lds_dwordx4 v219, s[92:93] sc1\n\t" \
  "s_waitcnt lgkmcnt(6)\n\t" \
  "v_mfma_f32_16x16x32_bf16 v[144:147], v[88:91], v[200:203], v[144:147]\n\t" \
  "v_mfma_f32_16x16x32_bf16 v[148:151], v[92:95], v[200:203], v[148:151]\n\t" \
  "ds_read_b128 v[200:203], v215 offset:33024\n\t" \
  "v_mfma_f32_16x16x32_bf16 v[152:155], v[88:91], v[204:207], v[152:155]\n\t" \
  "v_mfma_f32_16x16x32_bf16 v[156:159], v[92:95], v[204:207], v[156:159]\n\t" \
  "ds_read_b128 v[204:207], v215 offset:49408\n\t" \
  "s_cmp_lt_u32 s86, 8\n\t" \
  "s_cbranch_scc1 .Lw30%=\n\t" \
  "s_waitcnt vmcnt(5)\n\t" \
  "s_barrier\n\t" \
  ".Lg30%=:\n\t" \
  "s_add_u32 s92, s92, 65024\n\t" \
  "s_addc_u32 s93, s93, 0\n\t" \
  "s_waitcnt lgkmcnt(6)\n\t" \
  "v_mfma_f32_16x16x32_bf16 v[144:147], v[96:99], v[176:179], v[144:147]\n\t" \
  "v_mfma_f32_16x16x32_bf16 v[148:151], v[100:103], v[176:179], v[148:151]\n\t" \
  "ds_read_b128 v[176:179], v208 offset:0\n\t" \
  "s_add_u32 m0, %[lw], 65536\n\t" \
  "v_mfma_f32_16x16x32_bf16 v[152:155], v[96:99], v[180:183], v[152:155]\n\t" \
  "v_mfma_f32_16x16x32_bf16 v[156:159], v[100:103], v[180:183], v[156:159]\n\t" \
  "ds_read_b128 v[180:183], v208 offset:16384\n\t" \
  "global_load_lds_dwordx4 v216, s[92:93] sc1\n\t" \
  "s_waitcnt lgkmcnt(6)\n\t" \
  "v_mfma_f32_16x16x32_bf16 v[144:147], v[104:107], v[184:187], v[144:147]\n\t" \
  "v_mfma_f32_16x16x32_bf16 v[148:151], v[108:111], v[184:187], v[148:151]\n\t" \
  "ds_read_b128 v[184:187], v209 offset:0\n\t" \
  "v_mfma_f32_16x16x32_bf16 v[152:155], v[104:107], v[188:191], v[152:155]\n\t" \
  "v_mfma_f32_16x16x32_bf16 v[156:159], v[108:111], v[188:191], v[156:159]\n\t" \
  "ds_read_b128 v[188:191], v209 offset:16384\n\t" \
  "s_waitcnt lgkmcnt(6)\n\t" \
  "v_mfma_f32_16x16x32_bf16 v[144:147], v[112:115], v[192:195], v[144:147]\n\t" \
  "v_mfma_f32_16x16x32_bf16 v[148:151], v[116:119], v[192:195], v[148:151]\n\t" \
  "ds_read_b128 v[192:195], v210 offset:0\n\t" \
  "s_add_u32 m0, %[lw], 66560\n\t" \
  "v_mfma_f32_16x16x32_bf16 v[152:155], v[112:115], v[196:199], v[152:155]\n\t" \
  "v_mfma_f32_16x16x32_bf16 v[156:159], v[116:119], v[196:199], v[156:159]\n\t" \
  "ds_read_b128 v[196:199], v210 offset:16384\n\t" \
  "global_load_lds_dwordx4 v217, s[92:93] sc1\n\t" \
  "s_waitcnt lgkmcnt(6)\n\t" \
  "v_mfma_f32_16x16x32_bf16 v[144:147], v[120:123], v[200:203], v[144:147]\n\t" \
  "v_mfma_f32_16x16x32_bf16 v[148:151], v[124:127], v[200:203], v[148:151]\n\t" \
  "ds_read_b128 v[200:203], v211 offset:0\n\t" \
  "v_mfma_f32_16x16x32_bf16 v[152:155], v[120:123], v[204:207], v[152:155]\n\t" \
  "v_mfma_f32_16x16x32_bf16 v[156:159], v[124:127], v[204:207], v[156:159]\n\t" \
  "ds_read_b128 v[204:207], v211 offset:16384\n\t" \
  "s_add_u32 s86, s86, 1\n\t" \
  "s_add_u32 s87, s87, 1\n\t" \
  "s_cmp_lt_u32 s86, %[end]\n\t" \
  "s_cbranch_scc1 .Lp0%=\n\t" \
  "s_branch .Lx0%=\n\t" \
  ".Lf00%=:\n\t" \
  "s_nop 7\n\t" \
  "ds_write_b128 v220, v[152:155] offset:0\n\t" \
  "ds_write_b128 v220, v[156:159] offset:1024\n\t" \
  "s_lshr_b32 s90, s87, 7\n\t" \
  "s_xor_b32 s90, s90, %[s0]\n\t" \
  "s_and_b32 s90, s90, 1\n\t" \
  "s_lshl1_add_u32 s90, s87, s90\n\t" \
  "s_lshl_b32 s90, s90, 13\n\t" \
  "s_cmp_lt_i32 s86, 1\n\t" \
  "s_cselect_b32 s90, 0x70000000, s90\n\t" \
  "s_waitcnt lgkmcnt(0)\n\t" \
  "s_barrier\n\t" \
  "ds_read_b128 v[160:163], v221 offset:0\n\t" \
  "ds_read_b128 v[164:167], v221 offset:1024\n\t" \
  "s_waitcnt lgkmcnt(0)\n\t" \
  "v_add_f32 v160, v160, v144\n\t" \
  "v_add_f32 v161, v161, v145\n\t" \
  "v_add_f32 v162, v162, v146\n\t" \
  "v_add_f32 v163, v163, v147\n\t" \
  "v_add_f32 v164, v164, v148\n\t" \
  "v_add_f32 v165, v165, v149\n\t" \
  "v_add_f32 v166, v166, v150\n\t" \
  "v_add_f32 v167, v167, v151\n\t" \
  "v_cvt_pk_bf16_f32 v168, v160, v161\n\t" \
  "v_cvt_pk_bf16_f32 v169, v162, v163\n\t" \
  "v_cvt_pk_bf16_f32 v170, v164, v165\n\t" \
  "v_cvt_pk_bf16_f32 v171, v166, v167\n\t" \
  "buffer_store_dwordx4 v[168:171], v172, %[rt], s90 offen sc1\n\t" \
  "s_branch .Lfb00%=\n\t" \
  ".Lf10%=:\n\t" \
  "s_nop 7\n\t" \
  "ds_write_b128 v220, v[136:139] offset:0\n\t" \
  "ds_write_b128 v220, v[140:143] offset:1024\n\t" \
  "s_lshr_b32 s90, s87, 7\n\t" \
  "s_xor_b32 s90, s90, %[s0]\n\t" \
  "s_and_b32 s90, s90, 1\n\t" \
  "s_lshl1_add_u32 s90, s87, s90\n\t" \
  "s_lshl_b32 s90, s90, 13\n\t" \
  "s_cmp_lt_i32 s86, 1\n\t" \
  "s_cselect_b32 s90, 0x70000000, s90\n\t" \
  "s_waitcnt lgkmcnt(0)\n\t" \
  "s_barrier\n\t" \
  "ds_read_b128 v[160:163], v221 offset:0\n\t" \
  "ds_read_b128 v[164:167], v221 offset:1024\n\t" \
  "s_waitcnt lgkmcnt(0)\n\t" \
  "v_add_f32 v160, v160, v128\n\t" \
  "v_add_f32 v161, v161, v129\n\t" \
  "v_add_f32 v162, v162, v130\n\t" \
  "v_add_f32 v163, v163, v131\n\t" \
  "v_add_f32 v164, v164, v132\n\t" \
  "v_add_f32 v165, v165, v133\n\t" \
  "v_add_f32 v166, v166, v134\n\t" \
  "v_add_f32 v167, v167, v135\n\t" \
  "v_cvt_pk_bf16_f32 v168, v160, v161\n\t" \
  "v_cvt_pk_bf16_f32 v169, v162, v163\n\t" \
  "v_cvt_pk_bf16_f32 v170, v164, v165\n\t" \
  "v_cvt_pk_bf16_f32 v171, v166, v167\n\t" \
  "buffer_store_dwordx4 v[168:171], v172, %[rt], s90 offen sc1\n\t" \
  "s_branch .Lfb10%=\n\t" \
  ".Lw00%=:\n\t" \
  "s_waitcnt vmcnt(2)\n\t" \
  "s_barrier\n\t" \
  "s_branch .Lg00%=\n\t" \
  ".Lw10%=:\n\t" \
  "s_waitcnt vmcnt(2)\n\t" \
  "s_barrier\n\t" \
  "s_branch .Lg10%=\n\t" \
  ".Lw20%=:\n\t" \
  "s_waitcnt vmcnt(2)\n\t" \
  "s_barrier\n\t" \
  "s_branch .Lg20%=\n\t" \
  ".Lw30%=:\n\t" \
  "s_waitcnt vmcnt(2)\n\t" \
  "s_barrier\n\t" \
  "s_branch .Lg30%=\n\t" \
  ".Lx0%=:\n\t" \
  "s_waitcnt lgkmcnt(0)\n\t" \
  "s_setprio 0\n\t" \
  "s_mov_b32 m0, s94\n\t" \
  "s_branch .Le%=\n\t" \
  ".Lk%=:\n\t" \
  "s_mov_b32 s94, m0\n\t" \
  "s_mov_b64 s[92:93], %[x0]\n\t" \
  "s_mov_b32 s86, %[g0]\n\t" \
  "s_add_u32 s87, s86, %[t0]\n\t" \
  "s_sub_i32 s88, s86, 3\n\t" \
  "ds_read_b128 v[176:179], v208 offset:0\n\t" \
  "ds_read_b128 v[180:183], v208 offset:16384\n\t" \
  "ds_read_b128 v[184:187], v209 offset:0\n\t" \
  "ds_read_b128 v[188:191], v209 offset:16384\n\t" \
  "ds_read_b128 v[192:195], v210 offset:0\n\t" \
  "ds_read_b128 v[196:199], v210 offset:16384\n\t" \
  "ds_read_b128 v[200:203], v211 offset:0\n\t" \
  "ds_read_b128 v[204:207], v211 offset:16384\n\t" \
  "s_branch .Lfb01%=\n\t" \
  ".Lp1%=:\n\t" \
  "s_cmp_le_u32 s86, 5\n\t" \
  "s_cbranch_scc1 .Lf01%=\n\t" \
  ".Lfb01%=:\n\t" \
  "s_waitcnt lgkmcnt(6)\n\t" \
  "v_mfma_f32_16x16x32_bf16 v[128:131], v[0:3], v[176:179], 0\n\t" \
  "v_mfma_f32_16x16x32_bf16 v[132:135], v[4:7], v[176:179], 0\n\t" \
  "ds_read_b128 v[176:179], v208 offset:256\n\t" \
  "v_mfma_f32_16x16x32_bf16 v[136:139], v[0:3], v[180:183], 0\n\t" \
  "v_mfma_f32_16x16x32_bf16 v[140:143], v[4:7], v[180:183], 0\n\t" \
  "ds_read_b128 v[180:183], v208 offset:16640\n\t" \
  "s_waitcnt lgkmcnt(6)\n\t" \
  "v_mfma_f32_16x16x32_bf16 v[128:131], v[8:11], v[184:187], v[128:131]\n\t" \
  "v_mfma_f32_16x16x32_bf16 v[132:135], v[12:15], v[184:187], v[132:135]\n\t" \
  "ds_read_b128 v[184:187], v209 offset:256\n\t" \
  "s_add_u32 m0, %[lw], 67584\n\t" \
  "v_mfma_f32_16x16x32_bf16 v[136:139], v[8:11], v[188:191], v[136:139]\n\t" \
  "v_mfma_f32_16x16x32_bf16 v[140:143], v[12:15], v[188:191], v[140:143]\n\t" \
  "ds_read_b128 v[188:191], v209 offset:16640\n\t" \
  "global_load_lds_dwordx4 v218, s[92:93] sc1\n\t" \
  "ds_write_b128 v220, v[152:155] offset:0\n\t" \
  "ds_write_b128 v220, v[156:159] offset:1024\n\t" \
  "s_waitcnt lgkmcnt(8)\n\t" \
  "v_mfma_f32_16x16x32_bf16 v[128:131], v[16:19], v[192:195], v[128:131]\n\t" \
  "v_mfma_f32_16x16x32_bf16 v[132:135], v[20:23], v[192:195], v[132:135]\n\t" \
  "ds_read_b128 v[192:195], v210 offset:256\n\t" \
  "v_mfma_f32_16x16x32_bf16 v[136:139], v[16:19], v[196:199], v[136:139]\n\t" \
  "v_mfma_f32_16x16x32_bf16 v[140:143], v[20:23], v[196:199], v[140:143]\n\t" \
  "ds_read_b128 v[196:199], v210 offset:16640\n\t" \
  "s_lshr_b32 s90, s87, 7\n\t" \
  "s_xor_b32 s90, s90, %[s0]\n\t" \
  "s_and_b32 s90, s90, 1\n\t" \
  "s_lshl1_add_u32 s90, s87, s90\n\t" \
  "s_lshl_b32 s90, s90, 13\n\t" \
  "s_cmp_lt_i32 s86, 6\n\t" \
  "s_cselect_b32 s90, 0x70000000, s90\n\t" \
  "s_waitcnt lgkmcnt(8)\n\t" \
  "v_mfma_f32_16x16x32_bf16 v[128:131], v[24:27], v[200:203], v[128:131]\n\t" \
  "v_mfma_f32_16x16x32_bf16 v[132:135], v[28:31], v[200:203], v[132:135]\n\t" \
  "ds_read_b128 v[200:203], v211 offset:256\n\t" \
  "s_add_u32 m0, %[lw], 68608\n\t" \
  "v_mfma_f32_16x16x32_bf16 v[136:139], v[24:27], v[204:207], v[136:139]\n\t" \
  "v_mfma_f32_16x16x32_bf16 v[140:143], v[28:31], v[204:207], v[140:143]\n\t" \
  "ds_read_b128 v[204:207], v211 offset:16640\n\t" \
  "global_load_lds_dwordx4 v219, s[92:93] sc1\n\t" \
  "s_waitcnt lgkmcnt(4)\n\t" \
  "s_cmp_lt_u32 s86, 6\n\t" \
  "s_cbranch_scc1 .Lw01%=\n\t" \
  "s_waitcnt vmcnt(4)\n\t" \
  "s_barrier\n\t" \
  ".Lg01%=:\n\t" \
  "s_add_u32 s92, s92, 512\n\t" \
  "s_addc_u32 s93, s93, 0\n\t" \
  "v_mfma_f32_16x16x32_bf16 v[128:131], v[32:35], v[176:179], v[128:131]\n\t" \
  "v_mfma_f32_16x16x32_bf16 v[132:135], v[36:39], v[176:179], v[132:135]\n\t" \
  "ds_read_b128 v[176:179], v208 offset:32768\n\t" \
  "v_mfma_f32_16x16x32_bf16 v[136:139], v[32:35], v[180:183], v[136:139]\n\t" \
  "v_mfma_f32_16x16x32_bf16 v[140:143], v[36:39], v[180:183], v[140:143]\n\t" \
  "ds_read_b128 v[180:183], v208 offset:49152\n\t" \
  "ds_read_b128 v[160:163], v221 offset:0\n\t" \
  "ds_read_b128 v[164:167], v221 offset:1024\n\t" \
  "s_sub_i32 s91, s86, 2\n\t" \
  "s_max_i32 s88, s88, s91\n\t" \
  "v_mov_b32 v173, s88\n\t" \
  "buffer_store_dword v173, v174, %[rs], 0 offen sc1\n\t" \
  "v_mfma_f32_16x16x32_bf16 v[128:131], v[40:43], v[184:187], v[128:131]\n\t" \
  "v_mfma_f32_16x16x32_bf16 v[132:135], v[44:47], v[184:187], v[132:135]\n\t" \
  "ds_read_b128 v[184:187], v209 offset:32768\n\t" \
  "s_add_u32 m0, %[lw], 98304\n\t" \
  "v_mfma_f32_16x16x32_bf16 v[136:139], v[40:43], v[188:191], v[136:139]\n\t" \
  "v_mfma_f32_16x16x32_bf16 v[140:143], v[44:47], v[188:191], v[140:143]\n\t" \
  "ds_read_b128 v[188:191], v209 offset:49152\n\t" \
  "global_load_lds_dwordx4 v216, s[92:93] sc1\n\t" \
  "s_waitcnt lgkmcnt(8)\n\t" \
  "v_mfma_f32_16x16x32_bf16 v[128:131], v[48:51], v[192:195], v[128:131]\n\t" \
  "v_mfma_f32_16x16x32_bf16 v[132:135], v[52:55], v[192:195], v[132:135]\n\t" \
  "ds_read_b128 v[192:195], v210 offset:32768\n\t" \
  "v_mfma_f32_16x16x32_bf16 v[136:139], v[48:51], v[196:199], v[136:139]\n\t" \
  "v_mfma_f32_16x16x32_bf16 v[140:143], v[52:55], v[196:199], v[140:143]\n\t" \
  "ds_read_b128 v[196:199], v210 offset:49152\n\t" \
  "s_waitcnt lgkmcnt(4)\n\t" \
  "v_add_f32 v160, v160, v144\n\t" \
  "v_add_f32 v161, v161, v145\n\t" \
  "v_add_f32 v162, v162, v146\n\t" \
  "v_add_f32 v163, v163, v147\n\t" \
  "v_add_f32 v164, v164, v148\n\t" \
  "v_add_f32 v165, v165, v149\n\t" \
  "v_add_f32 v166, v166, v150\n\t" \
  "v_add_f32 v167, v167, v151\n\t" \
  "v_mfma_f32_16x16x32_bf16 v[128:131], v[56:59], v[200:203], v[128:131]\n\t" \
  "v_mfma_f32_16x16x32_bf16 v[132:135], v[60:63], v[200:203], v[132:135]\n\t" \
  "ds_read_b128 v[200:203], v211 offset:32768\n\t" \
  "s_add_u32 m0, %[lw], 99328\n\t" \
  "v_mfma_f32_16x16x32_bf16 v[136:139], v[56:59], v[204:207], v[136:139]\n\t" \
  "v_mfma_f32_16x16x32_bf16 v[140:143], v[60:63], v[204:207], v[140:143]\n\t" \
  "ds_read_b128 v[204:207], v211 offset:49152\n\t" \
  "global_load_lds_dwordx4 v217, s[92:93] sc1\n\t" \
  "v_cvt_pk_bf16_f32 v168, v160, v161\n\t" \
  "v_cvt_pk_bf16_f32 v169, v162, v163\n\t" \
  "v_cvt_pk_bf16_f32 v170, v164, v165\n\t" \
  "v_cvt_pk_bf16_f32 v171, v166, v167\n\t" \
  "buffer_store_dwordx4 v[168:171], v172, %[rt], s90 offen sc1\n\t" \
  "v_mfma_f32_16x16x32_bf16 v[128:131], v[64:67], v[176:179], v[128:131]\n\t" \
  "v_mfma_f32_16x16x32_bf16 v[132:135], v[68:71], v[176:179], v[132:135]\n\t" \
  "ds_read_b128 v[176:179], v208 offset:33024\n\t" \
  "v_mfma_f32_16x16x32_bf16 v[136:139], v[64:67], v[180:183], v[136:139]\n\t" \
  "v_mfma_f32_16x16x32_bf16 v[140:143], v[68:71], v[180:183], v[140:143]\n\t" \
  "ds_read_b128 v[180:183], v208 offset:49408\n\t" \
  "s_waitcnt lgkmcnt(6)\n\t" \
  "v_mfma_f32_16x16x32_bf16 v[128:131], v[72:75], v[184:187], v[128:131]\n\t" \
  "v_mfma_f32_16x16x32_bf16 v[132:135], v[76:79], v[184:187], v[132:135]\n\t" \
  "ds_read_b128 v[184:187], v209 offset:33024\n\t" \
  "s_add_u32 m0, %[lw], 100352\n\t" \
  "v_mfma_f32_16x16x32_bf16 v[136:139], v[72:75], v[188:191], v[136:139]\n\t" \
  "v_mfma_f32_16x16x32_bf16 v[140:143], v[76:79], v[188:191], v[140:143]\n\t" \
  "ds_read_b128 v[188:191], v209 offset:49408\n\t" \
  "global_load_lds_dwordx4 v218, s[92:93] sc1\n\t" \
  "s_waitcnt lgkmcnt(6)\n\t" \
  "v_mfma_f32_16x16x32_bf16 v[128:131], v[80:83], v[192:195], v[128:131]\n\t" \
  "v_mfma_f32_16x16x32_bf16 v[132:135], v[84:87], v[192:195], v[132:135]\n\t" \
  "ds_read_b128 v[192:195], v210 offset:33024\n\t" \
  "v_mfma_f32_16x16x32_bf16 v[136:139], v[80:83], v[196:199], v[136:139]\n\t" \
  "v_mfma_f32_16x16x32_bf16 v[140:143], v[84:87], v[196:199], v[140:143]\n\t" \
  "ds_read_b128 v[196:199], v210 offset:49408\n\t" \
  "s_waitcnt lgkmcnt(6)\n\t" \
  "v_mfma_f32_16x16x32_bf16 v[128:131], v[88:91], v[200:203], v[128:131]\n\t" \
  "v_mfma_f32_16x16x32_bf16 v[132:135], v[92:95], v[200:203], v[132:135]\n\t" \
  "ds_read_b128 v[200:203], v211 offset:33024\n\t" \
  "s_add_u32 m0, %[lw], 101376\n\t" \
  "v_mfma_f32_16x16x32_bf16 v[136:139], v[88:91], v[204:207], v[136:139]\n\t" \
  "v_mfma_f32_16x16x32_bf16 v[140:143], v[92:95], v[204:207], v[140:143]\n\t" \
  "ds_read_b128 v[204:207], v211 offset:49408\n\t" \
  "global_load_lds_dwordx4 v219, s[92:93] sc1\n\t" \
  "s_cmp_lt_u32 s86, 8\n\t" \
  "s_cbranch_scc1 .Lw11%=\n\t" \
  "s_waitcnt vmcnt(6)\n\t" \
  "s_barrier\n\t" \
  ".Lg11%=:\n\t" \
  "s_add_u32 s92, s92, 65024\n\t" \
  "s_addc_u32 s93, s93, 0\n\t" \
  "s_waitcnt lgkmcnt(6)\n\t" \
  "v_mfma_f32_16x16x32_bf16 v[128:131], v[96:99], v[176:179], v[128:131]\n\t" \
  "v_mfma_f32_16x16x32_bf16 v[132:135], v[100:103], v[176:179], v[132:135]\n\t" \
  "ds_read_b128 v[176:179], v212 offset:0\n\t" \
  "v_mfma_f32_16x16x32_bf16 v[136:139], v[96:99], v[180:183], v[136:139]\n\t" \
  "v_mfma_f32_16x16x32_bf16 v[140:143], v[100:103], v[180:183], v[140:143]\n\t" \
  "ds_read_b128 v[180:183], v212 offset:16384\n\t" \
  "s_waitcnt lgkmcnt(6)\n\t" \
  "v_mfma_f32_16x16x32_bf16 v[128:131], v[104:107], v[184:187], v[128:131]\n\t" \
  "v_mfma_f32_16x16x32_bf16 v[132:135], v[108:111], v[184:187], v[132:135]\n\t" \
  "ds_read_b128 v[184:187], v213 offset:0\n\t" \
  "s_add_u32 m0, %[lw], 0\n\t" \
  "v_mfma_f32_16x16x32_bf16 v[136:139], v[104:107], v[188:191], v[136:139]\n\t" \
  "v_mfma_f32_16x16x32_bf16 v[140:143], v[108:111], v[188:191], v[140:143]\n\t" \
  "ds_read_b128 v[188:191], v213 offset:16384\n\t" \
  "global_load_lds_dwordx4 v216, s[92:93] sc1\n\t" \
  "s_waitcnt lgkmcnt(6)\n\t" \
  "v_mfma_f32_16x16x32_bf16 v[128:131], v[112:115], v[192:195], v[128:131]\n\t" \
  "v_mfma_f32_16x16x32_bf16 v[132:135], v[116:119], v[192:195], v[132:135]\n\t" \
  "ds_read_b128 v[192:195], v214 offset:0\n\t" \
  "v_mfma_f32_16x16x32_bf16 v[136:139], v[112:115], v[196:199], v[136:139]\n\t" \
  "v_mfma_f32_16x16x32_bf16 v[140:143], v[116:119], v[196:199], v[140:143]\n\t" \
  "ds_read_b128 v[196:199], v214 offset:16384\n\t" \
  "s_waitcnt lgkmcnt(6)\n\t" \
  "v_mfma_f32_16x16x32_bf16 v[128:131], v[120:123], v[200:203], v[128:131]\n\t" \
  "v_mfma_f32_16x16x32_bf16 v[132:135], v[124:127], v[200:203], v[132:135]\n\t" \
  "ds_read_b128 v[200:203], v215 offset:0\n\t" \
  "s_add_u32 m0, %[lw], 1024\n\t" \
  "v_mfma_f32_16x16x32_bf16 v[136:139], v[120:123], v[204:207], v[136:139]\n\t" \
  "v_mfma_f32_16x16x32_bf16 v[140:143], v[124:127], v[204:207], v[140:143]\n\t" \
  "ds_read_b128 v[204:207], v215 offset:16384\n\t" \
  "global_load_lds_dwordx4 v217, s[92:93] sc1\n\t" \
  "s_add_u32 s86, s86, 1\n\t" \
  "s_add_u32 s87, s87, 1\n\t" \
  "s_cmp_le_u32 s86, 5\n\t" \
  "s_cbranch_scc1 .Lf11%=\n\t" \
  ".Lfb11%=:\n\t" \
  "s_waitcnt lgkmcnt(6)\n\t" \
  "v_mfma_f32_16x16x32_bf16 v[144:147], v[0:3], v[176:179], 0\n\t" \
  "v_mfma_f32_16x16x32_bf16 v[148:151], v[4:7], v[176:179], 0\n\t" \
  "ds_read_b128 v[176:179], v212 offset:256\n\t" \
  "v_mfma_f32_16x16x32_bf16 v[152:155], v[0:3], v[180:183], 0\n\t" \
  "v_mfma_f32_16x16x32_bf16 v[156:159], v[4:7], v[180:183], 0\n\t" \
  "ds_read_b128 v[180:183], v212 offset:16640\n\t" \
  "s_waitcnt lgkmcnt(6)\n\t" \
  "v_mfma_f32_16x16x32_bf16 v[144:147], v[8:11], v[184:187], v[144:147]\n\t" \
  "v_mfma_f32_16x16x32_bf16 v[148:151], v[12:15], v[184:187], v[148:151]\n\t" \
  "ds_read_b128 v[184:187], v213 offset:256\n\t" \
  "s_add_u32 m0, %[lw], 2048\n\t" \
  "v_mfma_f32_16x16x32_bf16 v[152:155], v[8:11], v[188:191], v[152:155]\n\t" \
  "v_mfma_f32_16x16x32_bf16 v[156:159], v[12:15], v[188:191], v[156:159]\n\t" \
  "ds_read_b128 v[188:191], v213 offset:16640\n\t" \
  "global_load_lds_dwordx4 v218, s[92:93] sc1\n\t" \
  "ds_write_b128 v220, v[136:139] offset:0\n\t" \
  "ds_write_b128 v220, v[140:143] offset:1024\n\t" \
  "s_waitcnt lgkmcnt(8)\n\t" \
  "v_mfma_f32_16x16x32_bf16 v[144:147], v[16:19], v[192:195], v[144:147]\n\t" \
  "v_mfma_f32_16x16x32_bf16 v[148:151], v[20:23], v[192:195], v[148:151]\n\t" \
  "ds_read_b128 v[192:195], v214 offset:256\n\t" \
  "v_mfma_f32_16x16x32_bf16 v[152:155], v[16:19], v[196:199], v[152:155]\n\t" \
  "v_mfma_f32_16x16x32_bf16 v[156:159], v[20:23], v[196:199], v[156:159]\n\t" \
  "ds_read_b128 v[196:199], v214 offset:16640\n\t" \
  "s_lshr_b32 s90, s87, 7\n\t" \
  "s_xor_b32 s90, s90, %[s0]\n\t" \
  "s_and_b32 s90, s90, 1\n\t" \
  "s_lshl1_add_u32 s90, s87, s90\n\t" \
  "s_lshl_b32 s90, s90, 13\n\t" \
  "s_cmp_lt_i32 s86, 6\n\t" \
  "s_cselect_b32 s90, 0x70000000, s90\n\t" \
  "s_waitcnt lgkmcnt(8)\n\t" \
  "v_mfma_f32_16x16x32_bf16 v[144:147], v[24:27], v[200:203], v[144:147]\n\t" \
  "v_mfma_f32_16x16x32_bf16 v[148:151], v[28:31], v[200:203], v[148:151]\n\t" \
  "ds_read_b128 v[200:203], v215 offset:256\n\t" \
  "s_add_u32 m0, %[lw], 3072\n\t" \
  "v_mfma_f32_16x16x32_bf16 v[152:155], v[24:27], v[204:207], v[152:155]\n\t" \
  "v_mfma_f32_16x16x32_bf16 v[156:159], v[28:31], v[204:207], v[156:159]\n\t" \
  "ds_read_b128 v[204:207], v215 offset:16640\n\t" \
  "global_load_lds_dwordx4 v219, s[92:93] sc1\n\t" \
  "s_waitcnt lgkmcnt(4)\n\t" \
  "s_cmp_lt_u32 s86, 6\n\t" \
  "s_cbranch_scc1 .Lw21%=\n\t" \
  "s_waitcnt vmcnt(4)\n\t" \
  "s_barrier\n\t" \
  ".Lg21%=:\n\t" \
  "s_add_u32 s92, s92, 512\n\t" \
  "s_addc_u32 s93, s93, 0\n\t" \
  "v_mfma_f32_16x16x32_bf16 v[144:147], v[32:35], v[176:179], v[144:147]\n\t" \
  "v_mfma_f32_16x16x32_bf16 v[148:151], v[36:39], v[176:179], v[148:151]\n\t" \
  "ds_read_b128 v[176:179], v212 offset:32768\n\t" \
  "v_mfma_f32_16x16x32_bf16 v[152:155], v[32:35], v[180:183], v[152:155]\n\t" \
  "v_mfma_f32_16x16x32_bf16 v[156:159], v[36:39], v[180:183], v[156:159]\n\t" \
  "ds_read_b128 v[180:183], v212 offset:49152\n\t" \
  "ds_read_b128 v[160:163], v221 offset:0\n\t" \
  "ds_read_b128 v[164:167], v221 offset:1024\n\t" \
  "s_sub_i32 s91, s86, 2\n\t" \
  "s_max_i32 s88, s88, s91\n\t" \
  "v_mov_b32 v173, s88\n\t" \
  "buffer_store_dword v173, v174, %[rs], 0 offen sc1\n\t" \
  "v_mfma_f32_16x16x32_bf16 v[144:147], v[40:43], v[184:187], v[144:147]\n\t" \
  "v_mfma_f32_16x16x32_bf16 v[148:151], v[44:47], v[184:187], v[148:151]\n\t" \
  "ds_read_b128 v[184:187], v213 offset:32768\n\t" \
  "s_add_u32 m0, %[lw], 32768\n\t" \
  "v_mfma_f32_16x16x32_bf16 v[152:155], v[40:43], v[188:191], v[152:155]\n\t" \
  "v_mfma_f32_16x16x32_bf16 v[156:159], v[44:47], v[188:191], v[156:159]\n\t" \
  "ds_read_b128 v[188:191], v213 offset:49152\n\t" \
  "global_load_lds_dwordx4 v216, s[92:93] sc1\n\t" \
  "s_waitcnt lgkmcnt(8)\n\t" \
  "v_mfma_f32_16x16x32_bf16 v[144:147], v[48:51], v[192:195], v[144:147]\n\t" \
  "v_mfma_f32_16x16x32_bf16 v[148:151], v[52:55], v[192:195], v[148:151]\n\t" \
  "ds_read_b128 v[192:195], v214 offset:32768\n\t" \
  "v_mfma_f32_16x16x32_bf16 v[152:155], v[48:51], v[196:199], v[152:155]\n\t" \
  "v_mfma_f32_16x16x32_bf16 v[156:159], v[52:55], v[196:199], v[156:159]\n\t" \
  "ds_read_b128 v[196:199], v214 offset:49152\n\t" \
  "s_waitcnt lgkmcnt(4)\n\t" \
  "v_add_f32 v160, v160, v128\n\t" \
  "v_add_f32 v161, v161, v129\n\t" \
  "v_add_f32 v162, v162, v130\n\t" \
  "v_add_f32 v163, v163, v131\n\t" \
  "v_add_f32 v164, v164, v132\n\t" \
  "v_add_f32 v165, v165, v133\n\t" \
  "v_add_f32 v166, v166, v134\n\t" \
  "v_add_f32 v167, v167, v135\n\t" \
  "v_mfma_f32_16x16x32_bf16 v[144:147], v[56:59], v[200:203], v[144:147]\n\t" \
  "v_mfma_f32_16x16x32_bf16 v[148:151], v[60:63], v[200:203], v[148:151]\n\t" \
  "ds_read_b128 v[200:203], v215 offset:32768\n\t" \
  "s_add_u32 m0, %[lw], 33792\n\t" \
  "v_mfma_f32_16x16x32_bf16 v[152:155], v[56:59], v[204:207], v[152:155]\n\t" \
  "v_mfma_f32_16x16x32_bf16 v[156:159], v[60:63], v[204:207], v[156:159]\n\t" \
  "ds_read_b128 v[204:207], v215 offset:49152\n\t" \
  "global_load_lds_dwordx4 v217, s[92:93] sc1\n\t" \
  "v_cvt_pk_bf16_f32 v168, v160, v161\n\t" \
  "v_cvt_pk_bf16_f32 v169, v162, v163\n\t" \
  "v_cvt_pk_bf16_f32 v170, v164, v165\n\t" \
  "v_cvt_pk_bf16_f32 v171, v166, v167\n\t" \
  "buffer_store_dwordx4 v[168:171], v172, %[rt], s90 offen sc1\n\t" \
  "v_mfma_f32_16x16x32_bf16 v[144:147], v[64:67], v[176:179], v[144:147]\n\t" \
  "v_mfma_f32_16x16x32_bf16 v[148:151], v[68:71], v[176:179], v[148:151]\n\t" \
  "ds_read_b128 v[176:179], v212 offset:33024\n\t" \
  "v_mfma_f32_16x16x32_bf16 v[152:155], v[64:67], v[180:183], v[152:155]\n\t" \
  "v_mfma_f32_16x16x32_bf16 v[156:159], v[68:71], v[180:183], v[156:159]\n\t" \
  "ds_read_b128 v[180:183], v212 offset:49408\n\t" \
  "s_waitcnt lgkmcnt(6)\n\t" \
  "v_mfma_f32_16x16x32_bf16 v[144:147], v[72:75], v[184:187], v[144:147]\n\t" \
  "v_mfma_f32_16x16x32_bf16 v[148:151], v[76:79], v[184:187], v[148:151]\n\t" \
  "ds_read_b128 v[184:187], v213 offset:33024\n\t" \
  "s_add_u32 m0, %[lw], 34816\n\t" \
  "v_mfma_f32_16x16x32_bf16 v[152:155], v[72:75], v[188:191], v[152:155]\n\t" \
  "v_mfma_f32_16x16x32_bf16 v[156:159], v[76:79], v[188:191], v[156:159]\n\t" \
  "ds_read_b128 v[188:191], v213 offset:49408\n\t" \
  "global_load_lds_dwordx4 v218, s[92:93] sc1\n\t" \
  "s_waitcnt lgkmcnt(6)\n\t" \
  "v_mfma_f32_16x16x32_bf16 v[144:147], v[80:83], v[192:195], v[144:147]\n\t" \
  "v_mfma_f32_16x16x32_bf16 v[148:151], v[84:87], v[192:195], v[148:151]\n\t" \
  "ds_read_b128 v[192:195], v214 offset:33024\n\t" \
  "v_mfma_f32_16x16x32_bf16 v[152:155], v[80:83], v[196:199], v[152:155]\n\t" \
  "v_mfma_f32_16x16x32_bf16 v[156:159], v[84:87], v[196:199], v[156:159]\n\t" \
  "ds_read_b128 v[196:199], v214 offset:49408\n\t" \
  "s_waitcnt lgkmcnt(6)\n\t" \
  "v_mfma_f32_16x16x32_bf16 v[144:147], v[88:91], v[200:203], v[144:147]\n\t" \
  "v_mfma_f32_16x16x32_bf16 v[148:151], v[92:95], v[200:203], v[148:151]\n\t" \
  "ds_read_b128 v[200:203], v215 offset:33024\n\t" \
  "s_add_u32 m0, %[lw], 35840\n\t" \
  "v_mfma_f32_16x16x32_bf16 v[152:155], v[88:91], v[204:207], v[152:155]\n\t" \
  "v_mfma_f32_16x16x32_bf16 v[156:159], v[92:95], v[204:207], v[156:159]\n\t" \
  "ds_read_b128 v[204:207], v215 offset:49408\n\t" \
  "global_load_lds_dwordx4 v219, s[92:93] sc1\n\t" \
  "s_cmp_lt_u32 s86, 8\n\t" \
  "s_cbranch_scc1 .Lw31%=\n\t" \
  "s_waitcnt vmcnt(6)\n\t" \
  "s_barrier\n\t" \
  ".Lg31%=:\n\t" \
  "s_add_u32 s92, s92, 65024\n\t" \
  "s_addc_u32 s93, s93, 0\n\t" \
  "s_waitcnt lgkmcnt(6)\n\t" \
  "v_mfma_f32_16x16x32_bf16 v[144:147], v[96:99], v[176:179], v[144:147]\n\t" \
  "v_mfma_f32_16x16x32_bf16 v[148:151], v[100:103], v[176:179], v[148:151]\n\t" \
  "ds_read_b128 v[176:179], v208 offset:0\n\t" \
  "v_mfma_f32_16x16x32_bf16 v[152:155], v[96:99], v[180:183], v[152:155]\n\t" \
  "v_mfma_f32_16x16x32_bf16 v[156:159], v[100:103], v[180:183], v[156:159]\n\t" \
  "ds_read_b128 v[180:183], v208 offset:16384\n\t" \
  "s_waitcnt lgkmcnt(6)\n\t" \
  "v_mfma_f32_16x16x32_bf16 v[144:147], v[104:107], v[184:187], v[144:147]\n\t" \
  "v_mfma_f32_16x16x32_bf16 v[148:151], v[108:111], v[184:187], v[148:151]\n\t" \
  "ds_read_b128 v[184:187], v209 offset:0\n\t" \
  "s_add_u32 m0, %[lw], 65536\n\t" \
  "v_mfma_f32_16x16x32_bf16 v[152:155], v[104:107], v[188:191], v[152:155]\n\t" \
  "v_mfma_f32_16x16x32_bf16 v[156:159], v[108:111], v[188:191], v[156:159]\n\t" \
  "ds_read_b128 v[188:191], v209 offset:16384\n\t" \
  "global_load_lds_dwordx4 v216, s[92:93] sc1\n\t" \
  "s_waitcnt lgkmcnt(6)\n\t" \
  "v_mfma_f32_16x16x32_bf16 v[144:147], v[112:115], v[192:195], v[144:147]\n\t" \
  "v_mfma_f32_16x16x32_bf16 v[148:151], v[116:119], v[192:195], v[148:151]\n\t" \
  "ds_read_b128 v[192:195], v210 offset:0\n\t" \
  "v_mfma_f32_16x16x32_bf16 v[152:155], v[112:115], v[196:199], v[152:155]\n\t" \
  "v_mfma_f32_16x16x32_bf16 v[156:159], v[116:119], v[196:199], v[156:159]\n\t" \
  "ds_read_b128 v[196:199], v210 offset:16384\n\t" \
  "s_waitcnt lgkmcnt(6)\n\t" \
  "v_mfma_f32_16x16x32_bf16 v[144:147], v[120:123], v[200:203], v[144:147]\n\t" \
  "v_mfma_f32_16x16x32_bf16 v[148:151], v[124:127], v[200:203], v[148:151]\n\t" \
  "ds_read_b128 v[200:203], v211 offset:0\n\t" \
  "s_add_u32 m0, %[lw], 66560\n\t" \
  "v_mfma_f32_16x16x32_bf16 v[152:155], v[120:123], v[204:207], v[152:155]\n\t" \
  "v_mfma_f32_16x16x32_bf16 v[156:159], v[124:127], v[204:207], v[156:159]\n\t" \
  "ds_read_b128 v[204:207], v211 offset:16384\n\t" \
  "global_load_lds_dwordx4 v217, s[92:93] sc1\n\t" \
  "s_add_u32 s86, s86, 1\n\t" \
  "s_add_u32 s87, s87, 1\n\t" \
  "s_cmp_lt_u32 s86, %[end]\n\t" \
  "s_cbranch_scc1 .Lp1%=\n\t" \
  "s_branch .Lx1%=\n\t" \
  ".Lf01%=:\n\t" \
  "s_nop 7\n\t" \
  "ds_write_b128 v220, v[152:155] offset:0\n\t" \
  "ds_write_b128 v220, v[156:159] offset:1024\n\t" \
  "s_lshr_b32 s90, s87, 7\n\t" \
  "s_xor_b32 s90, s90, %[s0]\n\t" \
  "s_and_b32 s90, s90, 1\n\t" \
  "s_lshl1_add_u32 s90, s87, s90\n\t" \
  "s_lshl_b32 s90, s90, 13\n\t" \
  "s_cmp_lt_i32 s86, 1\n\t" \
  "s_cselect_b32 s90, 0x70000000, s90\n\t" \
  "s_waitcnt lgkmcnt(0)\n\t" \
  "s_barrier\n\t" \
  "ds_read_b128 v[160:163], v221 offset:0\n\t" \
  "ds_read_b128 v[164:167], v221 offset:1024\n\t" \
  "s_waitcnt lgkmcnt(0)\n\t" \
  "v_add_f32 v160, v160, v144\n\t" \
  "v_add_f32 v161, v161, v145\n\t" \
  "v_add_f32 v162, v162, v146\n\t" \
  "v_add_f32 v163, v163, v147\n\t" \
  "v_add_f32 v164, v164, v148\n\t" \
  "v_add_f32 v165, v165, v149\n\t" \
  "v_add_f32 v166, v166, v150\n\t" \
  "v_add_f32 v167, v167, v151\n\t" \
  "v_cvt_pk_bf16_f32 v168, v160, v161\n\t" \
  "v_cvt_pk_bf16_f32 v169, v162, v163\n\t" \
  "v_cvt_pk_bf16_f32 v170, v164, v165\n\t" \
  "v_cvt_pk_bf16_f32 v171, v166, v167\n\t" \
  "buffer_store_dwordx4 v[168:171], v172, %[rt], s90 offen sc1\n\t" \
  "s_branch .Lfb01%=\n\t" \
  ".Lf11%=:\n\t" \
  "s_nop 7\n\t" \
  "ds_write_b128 v220, v[136:139] offset:0\n\t" \
  "ds_write_b128 v220, v[140:143] offset:1024\n\t" \
  "s_lshr_b32 s90, s87, 7\n\t" \
  "s_xor_b32 s90, s90, %[s0]\n\t" \
  "s_and_b32 s90, s90, 1\n\t" \
  "s_lshl1_add_u32 s90, s87, s90\n\t" \
  "s_lshl_b32 s90, s90, 13\n\t" \
  "s_cmp_lt_i32 s86, 1\n\t" \
  "s_cselect_b32 s90, 0x70000000, s90\n\t" \
  "s_waitcnt lgkmcnt(0)\n\t" \
  "s_barrier\n\t" \
  "ds_read_b128 v[160:163], v221 offset:0\n\t" \
  "ds_read_b128 v[164:167], v221 offset:1024\n\t" \
  "s_waitcnt lgkmcnt(0)\n\t" \
  "v_add_f32 v160, v160, v128\n\t" \
  "v_add_f32 v161, v161, v129\n\t" \
  "v_add_f32 v162, v162, v130\n\t" \
  "v_add_f32 v163, v163, v131\n\t" \
  "v_add_f32 v164, v164, v132\n\t" \
  "v_add_f32 v165, v165, v133\n\t" \
  "v_add_f32 v166, v166, v134\n\t" \
  "v_add_f32 v167, v167, v135\n\t" \
  "v_cvt_pk_bf16_f32 v168, v160, v161\n\t" \
  "v_cvt_pk_bf16_f32 v169, v162, v163\n\t" \
  "v_cvt_pk_bf16_f32 v170, v164, v165\n\t" \
  "v_cvt_pk_bf16_f32 v171, v166, v167\n\t" \
  "buffer_store_dwordx4 v[168:171], v172, %[rt], s90 offen sc1\n\t" \
  "s_branch .Lfb11%=\n\t" \
  ".Lw01%=:\n\t" \
  "s_waitcnt vmcnt(2)\n\t" \
  "s_barrier\n\t" \
  "s_sub_i32 s91, s86, 1\n\t" \
  "s_max_i32 s88, s88, s91\n\t" \
  "v_mov_b32 v173, s88\n\t" \
  "buffer_store_dword v173, v174, %[rs], 0 offen sc1\n\t" \
  "s_branch .Lg01%=\n\t" \
  ".Lw11%=:\n\t" \
  "s_waitcnt vmcnt(2)\n\t" \
  "s_barrier\n\t" \
  "s_sub_i32 s91, s86, 1\n\t" \
  "s_max_i32 s88, s88, s91\n\t" \
  "v_mov_b32 v173, s88\n\t" \
  "buffer_store_dword v173, v174, %[rs], 0 offen sc1\n\t" \
  "s_branch .Lg11%=\n\t" \
  ".Lw21%=:\n\t" \
  "s_waitcnt vmcnt(2)\n\t" \
  "s_barrier\n\t" \
  "s_sub_i32 s91, s86, 1\n\t" \
  "s_max_i32 s88, s88, s91\n\t" \
  "v_mov_b32 v173, s88\n\t" \
  "buffer_store_dword v173, v174, %[rs], 0 offen sc1\n\t" \
  "s_branch .Lg21%=\n\t" \
  ".Lw31%=:\n\t" \
  "s_waitcnt vmcnt(2)\n\t" \
  "s_barrier\n\t" \
  "s_sub_i32 s91, s86, 1\n\t" \
  "s_max_i32 s88, s88, s91\n\t" \
  "v_mov_b32 v173, s88\n\t" \
  "buffer_store_dword v173, v174, %[rs], 0 offen sc1\n\t" \
  "s_branch .Lg31%=\n\t" \
  ".Lx1%=:\n\t" \
  "s_waitcnt lgkmcnt(0)\n\t" \
  "s_setprio 0\n\t" \
  "s_mov_b32 m0, s94\n\t" \
  ".Le%=:\n\t"
#define PR_ASM_1 \
  "s_cmp_lg_u32 %[kh], 0\n\t" \
  "s_cbranch_scc1 .Lk%=\n\t" \
  "s_mov_b32 s94, m0\n\t" \
  "s_mov_b64 s[92:93], %[x0]\n\t" \
  "s_mov_b32 s86, %[g0]\n\t" \
  "s_add_u32 s87, s86, %[t0]\n\t" \
  "s_sub_i32 s88, s86, 3\n\t" \
  "ds_read_b128 v[176:179], v208 offset:0\n\t" \
  "ds_read_b128 v[180:183], v208 offset:16384\n\t" \
  "ds_read_b128 v[184:187], v209 offset:0\n\t" \
  "ds_read_b128 v[188:191], v209 offset:16384\n\t" \
  "ds_read_b128 v[192:195], v210 offset:0\n\t" \
  "ds_read_b128 v[196:199], v210 offset:16384\n\t" \
  "ds_read_b128 v[200:203], v211 offset:0\n\t" \
  "ds_read_b128 v[204:207], v211 offset:16384\n\t" \
  "s_branch .Lfb00%=\n\t" \
  ".Lp0%=:\n\t" \
  "s_cmp_le_u32 s86, 5\n\t" \
  "s_cbranch_scc1 .Lf00%=\n\t" \
  ".Lfb00%=:\n\t" \
  "s_waitcnt lgkmcnt(6)\n\t" \
  "v_mfma_f32_16x16x32_bf16 v[128:131], v[176:179], v[0:3], 0\n\t" \
  "v_mfma_f32_16x16x32_bf16 v[136:139], v[176:179], v[4:7], 0\n\t" \
  "ds_read_b128 v[176:179], v208 offset:256\n\t" \
  "s_add_u32 m0, %[lw], 67584\n\t" \
  "v_mfma_f32_16x16x32_bf16 v[132:135], v[180:183], v[0:3], 0\n\t" \
  "v_mfma_f32_16x16x32_bf16 v[140:143], v[180:183], v[4:7], 0\n\t" \
  "ds_read_b128 v[180:183], v208 offset:16640\n\t" \
  "global_load_lds_dwordx4 v218, s[92:93] sc1\n\t" \
  "s_waitcnt lgkmcnt(6)\n\t" \
  "v_mfma_f32_16x16x32_bf16 v[128:131], v[184:187], v[8:11], v[128:131]\n\t" \
  "v_mfma_f32_16x16x32_bf16 v[136:139], v[184:187], v[12:15], v[136:139]\n\t" \
  "ds_read_b128 v[184:187], v209 offset:256\n\t" \
  "v_mfma_f32_16x16x32_bf16 v[132:135], v[188:191], v[8:11], v[132:135]\n\t" \
  "v_mfma_f32_16x16x32_bf16 v[140:143], v[188:191], v[12:15], v[140:143]\n\t" \
  "ds_read_b128 v[188:191], v209 offset:16640\n\t" \
  "ds_write_b128 v220, v[152:155] offset:0\n\t" \
  "ds_write_b128 v220, v[156:159] offset:1024\n\t" \
  "s_waitcnt lgkmcnt(8)\n\t" \
  "v_mfma_f32_16x16x32_bf16 v[128:131], v[192:195], v[16:19], v[128:131]\n\t" \
  "v_mfma_f32_16x16x32_bf16 v[136:139], v[192:195], v[20:23], v[136:139]\n\t" \
  "ds_read_b128 v[192:195], v210 offset:256\n\t" \
  "s_add_u32 m0, %[lw], 68608\n\t" \
  "v_mfma_f32_16x16x32_bf16 v[132:135], v[196:199], v[16:19], v[132:135]\n\t" \
  "v_mfma_f32_16x16x32_bf16 v[140:143], v[196:199], v[20:23], v[140:143]\n\t" \
  "ds_read_b128 v[196:199], v210 offset:16640\n\t" \
  "global_load_lds_dwordx4 v219, s[92:93] sc1\n\t" \
  "s_lshr_b32 s90, s87, 7\n\t" \
  "s_xor_b32 s90, s90, %[s0]\n\t" \
  "s_and_b32 s90, s90, 1\n\t" \
  "s_lshl1_add_u32 s90, s87, s90\n\t" \
  "s_lshl_b32 s90, s90, 13\n\t" \
  "s_cmp_lt_i32 s86, 6\n\t" \
  "s_cselect_b32 s90, 0x70000000, s90\n\t" \
  "s_waitcnt lgkmcnt(8)\n\t" \
  "v_mfma_f32_16x16x32_bf16 v[128:131], v[200:203], v[24:27], v[128:131]\n\t" \
  "v_mfma_f32_16x16x32_bf16 v[136:139], v[200:203], v[28:31], v[136:139]\n\t" \
  "ds_read_b128 v[200:203], v211 offset:256\n\t" \
  "v_mfma_f32_16x16x32_bf16 v[132:135], v[204:207], v[24:27], v[132:135]\n\t" \
  "v_mfma_f32_16x16x32_bf16 v[140:143], v[204:207], v[28:31], v[140:143]\n\t" \
  "ds_read_b128 v[204:207], v211 offset:16640\n\t" \
  "s_waitcnt lgkmcnt(4)\n\t" \
  "s_cmp_lt_u32 s86, 6\n\t" \
  "s_cbranch_scc1 .Lw00%=\n\t" \
  "s_waitcnt vmcnt(4)\n\t" \
  "s_barrier\n\t" \
  ".Lg00%=:\n\t" \
  "s_add_u32 s92, s92, 512\n\t" \
  "s_addc_u32 s93, s93, 0\n\t" \
  "v_mfma_f32_16x16x32_bf16 v[128:131], v[176:179], v[32:35], v[128:131]\n\t" \
  "v_mfma_f32_16x16x32_bf16 v[136:139], v[176:179], v[36:39], v[136:139]\n\t" \
  "ds_read_b128 v[176:179], v208 offset:32768\n\t" \
  "s_add_u32 m0, %[lw], 98304\n\t" \
  "v_mfma_f32_16x16x32_bf16 v[132:135], v[180:183], v[32:35], v[132:135]\n\t" \
  "v_mfma_f32_16x16x32_bf16 v[140:143], v[180:183], v[36:39], v[140:143]\n\t" \
  "ds_read_b128 v[180:183], v208 offset:49152\n\t" \
  "global_load_lds_dwordx4 v216, s[92:93] sc1\n\t" \
  "ds_read_b128 v[160:163], v221 offset:0\n\t" \
  "ds_read_b128 v[164:167], v221 offset:1024\n\t" \
  "v_mfma_f32_16x16x32_bf16 v[128:131], v[184:187], v[40:43], v[128:131]\n\t" \
  "v_mfma_f32_16x16x32_bf16 v[136:139], v[184:187], v[44:47], v[136:139]\n\t" \
  "ds_read_b128 v[184:187], v209 offset:32768\n\t" \
  "v_mfma_f32_16x16x32_bf16 v[132:135], v[188:191], v[40:43], v[132:135]\n\t" \
  "v_mfma_f32_16x16x32_bf16 v[140:143], v[188:191], v[44:47], v[140:143]\n\t" \
  "ds_read_b128 v[188:191], v209 offset:49152\n\t" \
  "s_waitcnt lgkmcnt(8)\n\t" \
  "v_mfma_f32_16x16x32_bf16 v[128:131], v[192:195], v[48:51], v[128:131]\n\t" \
  "v_mfma_f32_16x16x32_bf16 v[136:139], v[192:195], v[52:55], v[136:139]\n\t" \
  "ds_read_b128 v[192:195], v210 offset:32768\n\t" \
  "s_add_u32 m0, %[lw], 99328\n\t" \
  "v_mfma_f32_16x16x32_bf16 v[132:135], v[196:199], v[48:51], v[132:135]\n\t" \
  "v_mfma_f32_16x16x32_bf16 v[140:143], v[196:199], v[52:55], v[140:143]\n\t" \
  "ds_read_b128 v[196:199], v210 offset:49152\n\t" \
  "global_load_lds_dwordx4 v217, s[92:93] sc1\n\t" \
  "s_waitcnt lgkmcnt(4)\n\t" \
  "v_add_f32 v160, v160, v144\n\t" \
  "v_add_f32 v161, v161, v145\n\t" \
  "v_add_f32 v162, v162, v146\n\t" \
  "v_add_f32 v163, v163, v147\n\t" \
  "v_add_f32 v164, v164, v148\n\t" \
  "v_add_f32 v165, v165, v149\n\t" \
  "v_add_f32 v166, v166, v150\n\t" \
  "v_add_f32 v167, v167, v151\n\t" \
  "v_mfma_f32_16x16x32_bf16 v[128:131], v[200:203], v[56:59], v[128:131]\n\t" \
  "v_mfma_f32_16x16x32_bf16 v[136:139], v[200:203], v[60:63], v[136:139]\n\t" \
  "ds_read_b128 v[200:203], v211 offset:32768\n\t" \
  "v_mfma_f32_16x16x32_bf16 v[132:135], v[204:207], v[56:59], v[132:135]\n\t" \
  "v_mfma_f32_16x16x32_bf16 v[140:143], v[204:207], v[60:63], v[140:143]\n\t" \
  "ds_read_b128 v[204:207], v211 offset:49152\n\t" \
  "v_cvt_pk_bf16_f32 v168, v160, v161\n\t" \
  "v_cvt_pk_bf16_f32 v169, v162, v163\n\t" \
  "v_cvt_pk_bf16_f32 v170, v164, v165\n\t" \
  "v_cvt_pk_bf16_f32 v171, v166, v167\n\t" \
  "buffer_store_dwordx4 v[168:171], v172, %[rt], s90 offen sc1\n\t" \
  "v_mfma_f32_16x16x32_bf16 v[128:131], v[176:179], v[64:67], v[128:131]\n\t" \
  "v_mfma_f32_16x16x32_bf16 v[136:139], v[176:179], v[68:71], v[136:139]\n\t" \
  "ds_read_b128 v[176:179], v208 offset:33024\n\t" \
  "s_add_u32 m0, %[lw], 100352\n\t" \
  "v_mfma_f32_16x16x32_bf16 v[132:135], v[180:183], v[64:67], v[132:135]\n\t" \
  "v_mfma_f32_16x16x32_bf16 v[140:143], v[180:183], v[68:71], v[140:143]\n\t" \
  "ds_read_b128 v[180:183], v208 offset:49408\n\t" \
  "global_load_lds_dwordx4 v218, s[92:93] sc1\n\t" \
  "s_waitcnt lgkmcnt(6)\n\t" \
  "v_mfma_f32_16x16x32_bf16 v[128:131], v[184:187], v[72:75], v[128:131]\n\t" \
  "v_mfma_f32_16x16x32_bf16 v[136:139], v[184:187], v[76:79], v[136:139]\n\t" \
  "ds_read_b128 v[184:187], v209 offset:33024\n\t" \
  "v_mfma_f32_16x16x32_bf16 v[132:135], v[188:191], v[72:75], v[132:135]\n\t" \
  "v_mfma_f32_16x16x32_bf16 v[140:143], v[188:191], v[76:79], v[140:143]\n\t" \
  "ds_read_b128 v[188:191], v209 offset:49408\n\t" \
  "s_waitcnt lgkmcnt(6)\n\t" \
  "v_mfma_f32_16x16x32_bf16 v[128:131], v[192:195], v[80:83], v[128:131]\n\t" \
  "v_mfma_f32_16x16x32_bf16 v[136:139], v[192:195], v[84:87], v[136:139]\n\t" \
  "ds_read_b128 v[192:195], v210 offset:33024\n\t" \
  "s_add_u32 m0, %[lw], 101376\n\t" \
  "v_mfma_f32_16x16x32_bf16 v[132:135], v[196:199], v[80:83], v[132:135]\n\t" \
  "v_mfma_f32_16x16x32_bf16 v[140:143], v[196:199], v[84:87], v[140:143]\n\t" \
  "ds_read_b128 v[196:199], v210 offset:49408\n\t" \
  "global_load_lds_dwordx4 v219, s[92:93] sc1\n\t" \
  "s_waitcnt lgkmcnt(6)\n\t" \
  "v_mfma_f32_16x16x32_bf16 v[128:131], v[200:203], v[88:91], v[128:131]\n\t" \
  "v_mfma_f32_16x16x32_bf16 v[136:139], v[200:203], v[92:95], v[136:139]\n\t" \
  "ds_read_b128 v[200:203], v211 offset:33024\n\t" \
  "v_mfma_f32_16x16x32_bf16 v[132:135], v[204:207], v[88:91], v[132:135]\n\t" \
  "v_mfma_f32_16x16x32_bf16 v[140:143], v[204:207], v[92:95], v[140:143]\n\t" \
  "ds_read_b128 v[204:207], v211 offset:49408\n\t" \
  "s_cmp_lt_u32 s86, 8\n\t" \
  "s_cbranch_scc1 .Lw10%=\n\t" \
  "s_waitcnt vmcnt(5)\n\t" \
  "s_barrier\n\t" \
  ".Lg10%=:\n\t" \
  "s_add_u32 s92, s92, 65024\n\t" \
  "s_addc_u32 s93, s93, 0\n\t" \
  "s_waitcnt lgkmcnt(6)\n\t" \
  "v_mfma_f32_16x16x32_bf16 v[128:131], v[176:179], v[96:99], v[128:131]\n\t" \
  "v_mfma_f32_16x16x32_bf16 v[136:139], v[176:179], v[100:103], v[136:139]\n\t" \
  "ds_read_b128 v[176:179], v212 offset:0\n\t" \
  "s_add_u32 m0, %[lw], 0\n\t" \
  "v_mfma_f32_16x16x32_bf16 v[132:135], v[180:183], v[96:99], v[132:135]\n\t" \
  "v_mfma_f32_16x16x32_bf16 v[140:143], v[180:183], v[100:103], v[140:143]\n\t" \
  "ds_read_b128 v[180:183], v212 offset:16384\n\t" \
  "global_load_lds_dwordx4 v216, s[92:93] sc1\n\t" \
  "s_waitcnt lgkmcnt(6)\n\t" \
  "v_mfma_f32_16x16x32_bf16 v[128:131], v[184:187], v[104:107], v[128:131]\n\t" \
  "v_mfma_f32_16x16x32_bf16 v[136:139], v[184:187], v[108:111], v[136:139]\n\t" \
  "ds_read_b128 v[184:187], v213 offset:0\n\t" \
  "v_mfma_f32_16x16x32_bf16 v[132:135], v[188:191], v[104:107], v[132:135]\n\t" \
  "v_mfma_f32_16x16x32_bf16 v[140:143], v[188:191], v[108:111], v[140:143]\n\t" \
  "ds_read_b128 v[188:191], v213 offset:16384\n\t" \
  "s_waitcnt lgkmcnt(6)\n\t" \
  "v_mfma_f32_16x16x32_bf16 v[128:131], v[192:195], v[112:115], v[128:131]\n\t" \
  "v_mfma_f32_16x16x32_bf16 v[136:139], v[192:195], v[116:119], v[136:139]\n\t" \
  "ds_read_b128 v[192:195], v214 offset:0\n\t" \
  "s_add_u32 m0, %[lw], 1024\n\t" \
  "v_mfma_f32_16x16x32_bf16 v[132:135], v[196:199], v[112:115], v[132:135]\n\t" \
  "v_mfma_f32_16x16x32_bf16 v[140:143], v[196:199], v[116:119], v[140:143]\n\t" \
  "ds_read_b128 v[196:199], v214 offset:16384\n\t" \
  "global_load_lds_dwordx4 v217, s[92:93] sc1\n\t" \
  "s_waitcnt lgkmcnt(6)\n\t" \
  "v_mfma_f32_16x16x32_bf16 v[128:131], v[200:203], v[120:123], v[128:131]\n\t" \
  "v_mfma_f32_16x16x32_bf16 v[136:139], v[200:203], v[124:127], v[136:139]\n\t" \
  "ds_read_b128 v[200:203], v215 offset:0\n\t" \
  "v_mfma_f32_16x16x32_bf16 v[132:135], v[204:207], v[120:123], v[132:135]\n\t" \
  "v_mfma_f32_16x16x32_bf16 v[140:143], v[204:207], v[124:127], v[140:143]\n\t" \
  "ds_read_b128 v[204:207], v215 offset:16384\n\t" \
  "s_add_u32 s86, s86, 1\n\t" \
  "s_add_u32 s87, s87, 1\n\t" \
  "s_cmp_le_u32 s86, 5\n\t" \
  "s_cbranch_scc1 .Lf10%=\n\t" \
  ".Lfb10%=:\n\t" \
  "s_waitcnt lgkmcnt(6)\n\t" \
  "v_mfma_f32_16x16x32_bf16 v[144:147], v[176:179], v[0:3], 0\n\t" \
  "v_mfma_f32_16x16x32_bf16 v[152:155], v[176:179], v[4:7], 0\n\t" \
  "ds_read_b128 v[176:179], v212 offset:256\n\t" \
  "s_add_u32 m0, %[lw], 2048\n\t" \
  "v_mfma_f32_16x16x32_bf16 v[148:151], v[180:183], v[0:3], 0\n\t" \
  "v_mfma_f32_16x16x32_bf16 v[156:159], v[180:183], v[4:7], 0\n\t" \
  "ds_read_b128 v[180:183], v212 offset:16640\n\t" \
  "global_load_lds_dwordx4 v218, s[92:93] sc1\n\t" \
  "s_waitcnt lgkmcnt(6)\n\t" \
  "v_mfma_f32_16x16x32_bf16 v[144:147], v[184:187], v[8:11], v[144:147]\n\t" \
  "v_mfma_f32_16x16x32_bf16 v[152:155], v[184:187], v[12:15], v[152:155]\n\t" \
  "ds_read_b128 v[184:187], v213 offset:256\n\t" \
  "v_mfma_f32_16x16x32_bf16 v[148:151], v[188:191], v[8:11], v[148:151]\n\t" \
  "v_mfma_f32_16x16x32_bf16 v[156:159], v[188:191], v[12:15], v[156:159]\n\t" \
  "ds_read_b128 v[188:191], v213 offset:16640\n\t" \
  "ds_write_b128 v220, v[136:139] offset:0\n\t" \
  "ds_write_b128 v220, v[140:143] offset:1024\n\t" \
  "s_waitcnt lgkmcnt(8)\n\t" \
  "v_mfma_f32_16x16x32_bf16 v[144:147], v[192:195], v[16:19], v[144:147]\n\t" \
  "v_mfma_f32_16x16x32_bf16 v[152:155], v[192:195], v[20:23], v[152:155]\n\t" \
  "ds_read_b128 v[192:195], v214 offset:256\n\t" \
  "s_add_u32 m0, %[lw], 3072\n\t" \
  "v_mfma_f32_16x16x32_bf16 v[148:151], v[196:199], v[16:19], v[148:151]\n\t" \
  "v_mfma_f32_16x16x32_bf16 v[156:159], v[196:199], v[20:23], v[156:159]\n\t" \
  "ds_read_b128 v[196:199], v214 offset:16640\n\t" \
  "global_load_lds_dwordx4 v219, s[92:93] sc1\n\t" \
  "s_lshr_b32 s90, s87, 7\n\t" \
  "s_xor_b32 s90, s90, %[s0]\n\t" \
  "s_and_b32 s90, s90, 1\n\t" \
  "s_lshl1_add_u32 s90, s87, s90\n\t" \
  "s_lshl_b32 s90, s90, 13\n\t" \
  "s_cmp_lt_i32 s86, 6\n\t" \
  "s_cselect_b32 s90, 0x70000000, s90\n\t" \
  "s_waitcnt lgkmcnt(8)\n\t" \
  "v_mfma_f32_16x16x32_bf16 v[144:147], v[200:203], v[24:27], v[144:147]\n\t" \
  "v_mfma_f32_16x16x32_bf16 v[152:155], v[200:203], v[28:31], v[152:155]\n\t" \
  "ds_read_b128 v[200:203], v215 offset:256\n\t" \
  "v_mfma_f32_16x16x32_bf16 v[148:151], v[204:207], v[24:27], v[148:151]\n\t" \
  "v_mfma_f32_16x16x32_bf16 v[156:159], v[204:207], v[28:31], v[156:159]\n\t" \
  "ds_read_b128 v[204:207], v215 offset:16640\n\t" \
  "s_waitcnt lgkmcnt(4)\n\t" \
  "s_cmp_lt_u32 s86, 6\n\t" \
  "s_cbranch_scc1 .Lw20%=\n\t" \
  "s_waitcnt vmcnt(4)\n\t" \
  "s_barrier\n\t" \
  ".Lg20%=:\n\t" \
  "s_add_u32 s92, s92, 512\n\t" \
  "s_addc_u32 s93, s93, 0\n\t" \
  "v_mfma_f32_16x16x32_bf16 v[144:147], v[176:179], v[32:35], v[144:147]\n\t" \
  "v_mfma_f32_16x16x32_bf16 v[152:155], v[176:179], v[36:39], v[152:155]\n\t" \
  "ds_read_b128 v[176:179], v212 offset:32768\n\t" \
  "s_add_u32 m0, %[lw], 32768\n\t" \
  "v_mfma_f32_16x16x32_bf16 v[148:151], v[180:183], v[32:35], v[148:151]\n\t" \
  "v_mfma_f32_16x16x32_bf16 v[156:159], v[180:183], v[36:39], v[156:159]\n\t" \
  "ds_read_b128 v[180:183], v212 offset:49152\n\t" \
  "global_load_lds_dwordx4 v216, s[92:93] sc1\n\t" \
  "ds_read_b128 v[160:163], v221 offset:0\n\t" \
  "ds_read_b128 v[164:167], v221 offset:1024\n\t" \
  "v_mfma_f32_16x16x32_bf16 v[144:147], v[184:187], v[40:43], v[144:147]\n\t" \
  "v_mfma_f32_16x16x32_bf16 v[152:155], v[184:187], v[44:47], v[152:155]\n\t" \
  "ds_read_b128 v[184:187], v213 offset:32768\n\t" \
  "v_mfma_f32_16x16x32_bf16 v[148:151], v[188:191], v[40:43], v[148:151]\n\t" \
  "v_mfma_f32_16x16x32_bf16 v[156:159], v[188:191], v[44:47], v[156:159]\n\t" \
  "ds_read_b128 v[188:191], v213 offset:49152\n\t" \
  "s_waitcnt lgkmcnt(8)\n\t" \
  "v_mfma_f32_16x16x32_bf16 v[144:147], v[192:195], v[48:51], v[144:147]\n\t" \
  "v_mfma_f32_16x16x32_bf16 v[152:155], v[192:195], v[52:55], v[152:155]\n\t" \
  "ds_read_b128 v[192:195], v214 offset:32768\n\t" \
  "s_add_u32 m0, %[lw], 33792\n\t" \
  "v_mfma_f32_16x16x32_bf16 v[148:151], v[196:199], v[48:51], v[148:151]\n\t" \
  "v_mfma_f32_16x16x32_bf16 v[156:159], v[196:199], v[52:55], v[156:159]\n\t" \
  "ds_read_b128 v[196:199], v214 offset:49152\n\t" \
  "global_load_lds_dwordx4 v217, s[92:93] sc1\n\t" \
  "s_waitcnt lgkmcnt(4)\n\t" \
  "v_add_f32 v160, v160, v128\n\t" \
  "v_add_f32 v161, v161, v129\n\t" \
  "v_add_f32 v162, v162, v130\n\t" \
  "v_add_f32 v163, v163, v131\n\t" \
  "v_add_f32 v164, v164, v132\n\t" \
  "v_add_f32 v165, v165, v133\n\t" \
  "v_add_f32 v166, v166, v134\n\t" \
  "v_add_f32 v167, v167, v135\n\t" \
  "v_mfma_f32_16x16x32_bf16 v[144:147], v[200:203], v[56:59], v[144:147]\n\t" \
  "v_mfma_f32_16x16x32_bf16 v[152:155], v[200:203], v[60:63], v[152:155]\n\t" \
  "ds_read_b128 v[200:203], v215 offset:32768\n\t" \
  "v_mfma_f32_16x16x32_bf16 v[148:151], v[204:207], v[56:59], v[148:151]\n\t" \
  "v_mfma_f32_16x16x32_bf16 v[156:159], v[204:207], v[60:63], v[156:159]\n\t" \
  "ds_read_b128 v[204:207], v215 offset:49152\n\t" \
  "v_cvt_pk_bf16_f32 v168, v160, v161\n\t" \
  "v_cvt_pk_bf16_f32 v169, v162, v163\n\t" \
  "v_cvt_pk_bf16_f32 v170, v164, v165\n\t" \
  "v_cvt_pk_bf16_f32 v171, v166, v167\n\t" \
  "buffer_store_dwordx4 v[168:171], v172, %[rt], s90 offen sc1\n\t" \
  "v_mfma_f32_16x16x32_bf16 v[144:147], v[176:179], v[64:67], v[144:147]\n\t" \
  "v_mfma_f32_16x16x32_bf16 v[152:155], v[176:179], v[68:71], v[152:155]\n\t" \
  "ds_read_b128 v[176:179], v212 offset:33024\n\t" \
  "s_add_u32 m0, %[lw], 34816\n\t" \
  "v_mfma_f32_16x16x32_bf16 v[148:151], v[180:183], v[64:67], v[148:151]\n\t" \
  "v_mfma_f32_16x16x32_bf16 v[156:159], v[180:183], v[68:71], v[156:159]\n\t" \
  "ds_read_b128 v[180:183], v212 offset:49408\n\t" \
  "global_load_lds_dwordx4 v218, s[92:93] sc1\n\t" \
  "s_waitcnt lgkmcnt(6)\n\t" \
  "v_mfma_f32_16x16x32_bf16 v[144:147], v[184:187], v[72:75], v[144:147]\n\t" \
  "v_mfma_f32_16x16x32_bf16 v[152:155], v[184:187], v[76:79], v[152:155]\n\t" \
  "ds_read_b128 v[184:187], v213 offset:33024\n\t" \
  "v_mfma_f32_16x16x32_bf16 v[148:151], v[188:191], v[72:75], v[148:151]\n\t" \
  "v_mfma_f32_16x16x32_bf16 v[156:159], v[188:191], v[76:79], v[156:159]\n\t" \
  "ds_read_b128 v[188:191], v213 offset:49408\n\t" \
  "s_waitcnt lgkmcnt(6)\n\t" \
  "v_mfma_f32_16x16x32_bf16 v[144:147], v[192:195], v[80:83], v[144:147]\n\t" \
  "v_mfma_f32_16x16x32_bf16 v[152:155], v[192:195], v[84:87], v[152:155]\n\t" \
  "ds_read_b128 v[192:195], v214 offset:33024\n\t" \
  "s_add_u32 m0, %[lw], 35840\n\t" \
  "v_mfma_f32_16x16x32_bf16 v[148:151], v[196:199], v[80:83], v[148:151]\n\t" \
  "v_mfma_f32_16x16x32_bf16 v[156:159], v[196:199], v[84:87], v[156:159]\n\t" \
  "ds_read_b128 v[196:199], v214 offset:49408\n\t" \
  "global_load_lds_dwordx4 v219, s[92:93] sc1\n\t" \
  "s_waitcnt lgkmcnt(6)\n\t" \
  "v_mfma_f32_16x16x32_bf16 v[144:147], v[200:203], v[88:91], v[144:147]\n\t" \
  "v_mfma_f32_16x16x32_bf16 v[152:155], v[200:203], v[92:95], v[152:155]\n\t" \
  "ds_read_b128 v[200:203], v215 offset:33024\n\t" \
  "v_mfma_f32_16x16x32_bf16 v[148:151], v[204:207], v[88:91], v[148:151]\n\t" \
  "v_mfma_f32_16x16x32_bf16 v[156:159], v[204:207], v[92:95], v[156:159]\n\t" \
  "ds_read_b128 v[204:207], v215 offset:49408\n\t" \
  "s_cmp_lt_u32 s86, 8\n\t" \
  "s_cbranch_scc1 .Lw30%=\n\t" \
  "s_waitcnt vmcnt(5)\n\t" \
  "s_barrier\n\t" \
  ".Lg30%=:\n\t" \
  "s_add_u32 s92, s92, 65024\n\t" \
  "s_addc_u32 s93, s93, 0\n\t" \
  "s_waitcnt lgkmcnt(6)\n\t" \
  "v_mfma_f32_16x16x32_bf16 v[144:147], v[176:179], v[96:99], v[144:147]\n\t" \
  "v_mfma_f32_16x16x32_bf16 v[152:155], v[176:179], v[100:103], v[152:155]\n\t" \
  "ds_read_b128 v[176:179], v208 offset:0\n\t" \
  "s_add_u32 m0, %[lw], 65536\n\t" \
  "v_mfma_f32_16x16x32_bf16 v[148:151], v[180:183], v[96:99], v[148:151]\n\t" \
  "v_mfma_f32_16x16x32_bf16 v[156:159], v[180:183], v[100:103], v[156:159]\n\t" \
  "ds_read_b128 v[180:183], v208 offset:16384\n\t" \
  "global_load_lds_dwordx4 v216, s[92:93] sc1\n\t" \
  "s_waitcnt lgkmcnt(6)\n\t" \
  "v_mfma_f32_16x16x32_bf16 v[144:147], v[184:187], v[104:107], v[144:147]\n\t" \
  "v_mfma_f32_16x16x32_bf16 v[152:155], v[184:187], v[108:111], v[152:155]\n\t" \
  "ds_read_b128 v[184:187], v209 offset:0\n\t" \
  "v_mfma_f32_16x16x32_bf16 v[148:151], v[188:191], v[104:107], v[148:151]\n\t" \
  "v_mfma_f32_16x16x32_bf16 v[156:159], v[188:191], v[108:111], v[156:159]\n\t" \
  "ds_read_b128 v[188:191], v209 offset:16384\n\t" \
  "s_waitcnt lgkmcnt(6)\n\t" \
  "v_mfma_f32_16x16x32_bf16 v[144:147], v[192:195], v[112:115], v[144:147]\n\t" \
  "v_mfma_f32_16x16x32_bf16 v[152:155], v[192:195], v[116:119], v[152:155]\n\t" \
  "ds_read_b128 v[192:195], v210 offset:0\n\t" \
  "s_add_u32 m0, %[lw], 66560\n\t" \
  "v_mfma_f32_16x16x32_bf16 v[148:151], v[196:199], v[112:115], v[148:151]\n\t" \
  "v_mfma_f32_16x16x32_bf16 v[156:159], v[196:199], v[116:119], v[156:159]\n\t" \
  "ds_read_b128 v[196:199], v210 offset:16384\n\t" \
  "global_load_lds_dwordx4 v217, s[92:93] sc1\n\t" \
  "s_waitcnt lgkmcnt(6)\n\t" \
  "v_mfma_f32_16x16x32_bf16 v[144:147], v[200:203], v[120:123], v[144:147]\n\t" \
  "v_mfma_f32_16x16x32_bf16 v[152:155], v[200:203], v[124:127], v[152:155]\n\t" \
  "ds_read_b128 v[200:203], v211 offset:0\n\t" \
  "v_mfma_f32_16x16x32_bf16 v[148:151], v[204:207], v[120:123], v[148:151]\n\t" \
  "v_mfma_f32_16x16x32_bf16 v[156:159], v[204:207], v[124:127], v[156:159]\n\t" \
  "ds_read_b128 v[204:207], v211 offset:16384\n\t" \
  "s_add_u32 s86, s86, 1\n\t" \
  "s_add_u32 s87, s87, 1\n\t" \
  "s_cmp_lt_u32 s86, %[end]\n\t" \
  "s_cbranch_scc1 .Lp0%=\n\t" \
  "s_branch .Lx0%=\n\t" \
  ".Lf00%=:\n\t" \
  "s_nop 7\n\t" \
  "ds_write_b128 v220, v[152:155] offset:0\n\t" \
  "ds_write_b128 v220, v[156:159] offset:1024\n\t" \
  "s_lshr_b32 s90, s87, 7\n\t" \
  "s_xor_b32 s90, s90, %[s0]\n\t" \
  "s_and_b32 s90, s90, 1\n\t" \
  "s_lshl1_add_u32 s90, s87, s90\n\t" \
  "s_lshl_b32 s90, s90, 13\n\t" \
  "s_cmp_lt_i32 s86, 1\n\t" \
  "s_cselect_b32 s90, 0x70000000, s90\n\t" \
  "s_waitcnt lgkmcnt(0)\n\t" \
  "s_barrier\n\t" \
  "ds_read_b128 v[160:163], v221 offset:0\n\t" \
  "ds_read_b128 v[164:167], v221 offset:1024\n\t" \
  "s_waitcnt lgkmcnt(0)\n\t" \
  "v_add_f32 v160, v160, v144\n\t" \
  "v_add_f32 v161, v161, v145\n\t" \
  "v_add_f32 v162, v162, v146\n\t" \
  "v_add_f32 v163, v163, v147\n\t" \
  "v_add_f32 v164, v164, v148\n\t" \
  "v_add_f32 v165, v165, v149\n\t" \
  "v_add_f32 v166, v166, v150\n\t" \
  "v_add_f32 v167, v167, v151\n\t" \
  "v_cvt_pk_bf16_f32 v168, v160, v161\n\t" \
  "v_cvt_pk_bf16_f32 v169, v162, v163\n\t" \
  "v_cvt_pk_bf16_f32 v170, v164, v165\n\t" \
  "v_cvt_pk_bf16_f32 v171, v166, v167\n\t" \
  "buffer_store_dwordx4 v[168:171], v172, %[rt], s90 offen sc1\n\t" \
  "s_branch .Lfb00%=\n\t" \
  ".Lf10%=:\n\t" \
  "s_nop 7\n\t" \
  "ds_write_b128 v220, v[136:139] offset:0\n\t" \
  "ds_write_b128 v220, v[140:143] offset:1024\n\t" \
  "s_lshr_b32 s90, s87, 7\n\t" \
  "s_xor_b32 s90, s90, %[s0]\n\t" \
  "s_and_b32 s90, s90, 1\n\t" \
  "s_lshl1_add_u32 s90, s87, s90\n\t" \
  "s_lshl_b32 s90, s90, 13\n\t" \
  "s_cmp_lt_i32 s86, 1\n\t" \
  "s_cselect_b32 s90, 0x70000000, s90\n\t" \
  "s_waitcnt lgkmcnt(0)\n\t" \
  "s_barrier\n\t" \
  "ds_read_b128 v[160:163], v221 offset:0\n\t" \
  "ds_read_b128 v[164:167], v221 offset:1024\n\t" \
  "s_waitcnt lgkmcnt(0)\n\t" \
  "v_add_f32 v160, v160, v128\n\t" \
  "v_add_f32 v161, v161, v129\n\t" \
  "v_add_f32 v162, v162, v130\n\t" \
  "v_add_f32 v163, v163, v131\n\t" \
  "v_add_f32 v164, v164, v132\n\t" \
  "v_add_f32 v165, v165, v133\n\t" \
  "v_add_f32 v166, v166, v134\n\t" \
  "v_add_f32 v167, v167, v135\n\t" \
  "v_cvt_pk_bf16_f32 v168, v160, v161\n\t" \
  "v_cvt_pk_bf16_f32 v169, v162, v163\n\t" \
  "v_cvt_pk_bf16_f32 v170, v164, v165\n\t" \
  "v_cvt_pk_bf16_f32 v171, v166, v167\n\t" \
  "buffer_store_dwordx4 v[168:171], v172, %[rt], s90 offen sc1\n\t" \
  "s_branch .Lfb10%=\n\t" \
  ".Lw00%=:\n\t" \
  "s_waitcnt vmcnt(2)\n\t" \
  "s_barrier\n\t" \
  "s_branch .Lg00%=\n\t" \
  ".Lw10%=:\n\t" \
  "s_waitcnt vmcnt(2)\n\t" \
  "s_barrier\n\t" \
  "s_branch .Lg10%=\n\t" \
  ".Lw20%=:\n\t" \
  "s_waitcnt vmcnt(2)\n\t" \
  "s_barrier\n\t" \
  "s_branch .Lg20%=\n\t" \
  ".Lw30%=:\n\t" \
  "s_waitcnt vmcnt(2)\n\t" \
  "s_barrier\n\t" \
  "s_branch .Lg30%=\n\t" \
  ".Lx0%=:\n\t" \
  "s_waitcnt lgkmcnt(0)\n\t" \
  "s_setprio 0\n\t" \
  "s_mov_b32 m0, s94\n\t" \
  "s_branch .Le%=\n\t" \
  ".Lk%=:\n\t" \
  "s_mov_b32 s94, m0\n\t" \
  "s_mov_b64 s[92:93], %[x0]\n\t" \
  "s_mov_b32 s86, %[g0]\n\t" \
  "s_add_u32 s87, s86, %[t0]\n\t" \
  "s_sub_i32 s88, s86, 3\n\t" \
  "ds_read_b128 v[176:179], v208 offset:0\n\t" \
  "ds_read_b128 v[180:183], v208 offset:16384\n\t" \
  "ds_read_b128 v[184:187], v209 offset:0\n\t" \
  "ds_read_b128 v[188:191], v209 offset:16384\n\t" \
  "ds_read_b128 v[192:195], v210 offset:0\n\t" \
  "ds_read_b128 v[196:199], v210 offset:16384\n\t" \
  "ds_read_b128 v[200:203], v211 offset:0\n\t" \
  "ds_read_b128 v[204:207], v211 offset:16384\n\t" \
  "s_branch .Lfb01%=\n\t" \
  ".Lp1%=:\n\t" \
  "s_cmp_le_u32 s86, 5\n\t" \
  "s_cbranch_scc1 .Lf01%=\n\t" \
  ".Lfb01%=:\n\t" \
  "s_waitcnt lgkmcnt(6)\n\t" \
  "v_mfma_f32_16x16x32_bf16 v[128:131], v[176:179], v[0:3], 0\n\t" \
  "v_mfma_f32_16x16x32_bf16 v[136:139], v[176:179], v[4:7], 0\n\t" \
  "ds_read_b128 v[176:179], v208 offset:256\n\t" \
  "v_mfma_f32_16x16x32_bf16 v[132:135], v[180:183], v[0:3], 0\n\t" \
  "v_mfma_f32_16x16x32_bf16 v[140:143], v[180:183], v[4:7], 0\n\t" \
  "ds_read_b128 v[180:183], v208 offset:16640\n\t" \
  "s_waitcnt lgkmcnt(6)\n\t" \
  "v_mfma_f32_16x16x32_bf16 v[128:131], v[184:187], v[8:11], v[128:131]\n\t" \
  "v_mfma_f32_16x16x32_bf16 v[136:139], v[184:187], v[12:15], v[136:139]\n\t" \
  "ds_read_b128 v[184:187], v209 offset:256\n\t" \
  "s_add_u32 m0, %[lw], 67584\n\t" \
  "v_mfma_f32_16x16x32_bf16 v[132:135], v[188:191], v[8:11], v[132:135]\n\t" \
  "v_mfma_f32_16x16x32_bf16 v[140:143], v[188:191], v[12:15], v[140:143]\n\t" \
  "ds_read_b128 v[188:191], v209 offset:16640\n\t" \
  "global_load_lds_dwordx4 v218, s[92:93] sc1\n\t" \
  "ds_write_b128 v220, v[152:155] offset:0\n\t" \
  "ds_write_b128 v220, v[156:159] offset:1024\n\t" \
  "s_waitcnt lgkmcnt(8)\n\t" \
  "v_mfma_f32_16x16x32_bf16 v[128:131], v[192:195], v[16:19], v[128:131]\n\t" \
  "v_mfma_f32_16x16x32_bf16 v[136:139], v[192:195], v[20:23], v[136:139]\n\t" \
  "ds_read_b128 v[192:195], v210 offset:256\n\t" \
  "v_mfma_f32_16x16x32_bf16 v[132:135], v[196:199], v[16:19], v[132:135]\n\t" \
  "v_mfma_f32_16x16x32_bf16 v[140:143], v[196:199], v[20:23], v[140:143]\n\t" \
  "ds_read_b128 v[196:199], v210 offset:16640\n\t" \
  "s_lshr_b32 s90, s87, 7\n\t" \
  "s_xor_b32 s90, s90, %[s0]\n\t" \
  "s_and_b32 s90, s90, 1\n\t" \
  "s_lshl1_add_u32 s90, s87, s90\n\t" \
  "s_lshl_b32 s90, s90, 13\n\t" \
  "s_cmp_lt_i32 s86, 6\n\t" \
  "s_cselect_b32 s90, 0x70000000, s90\n\t" \
  "s_waitcnt lgkmcnt(8)\n\t" \
  "v_mfma_f32_16x16x32_bf16 v[128:131], v[200:203], v[24:27], v[128:131]\n\t" \
  "v_mfma_f32_16x16x32_bf16 v[136:139], v[200:203], v[28:31], v[136:139]\n\t" \
  "ds_read_b128 v[200:203], v211 offset:256\n\t" \
  "s_add_u32 m0, %[lw], 68608\n\t" \
  "v_mfma_f32_16x16x32_bf16 v[132:135], v[204:207], v[24:27], v[132:135]\n\t" \
  "v_mfma_f32_16x16x32_bf16 v[140:143], v[204:207], v[28:31], v[140:143]\n\t" \
  "ds_read_b128 v[204:207], v211 offset:16640\n\t" \
  "global_load_lds_dwordx4 v219, s[92:93] sc1\n\t" \
  "s_waitcnt lgkmcnt(4)\n\t" \
  "s_cmp_lt_u32 s86, 6\n\t" \
  "s_cbranch_scc1 .Lw01%=\n\t" \
  "s_waitcnt vmcnt(4)\n\t" \
  "s_barrier\n\t" \
  ".Lg01%=:\n\t" \
  "s_add_u32 s92, s92, 512\n\t" \
  "s_addc_u32 s93, s93, 0\n\t" \
  "v_mfma_f32_16x16x32_bf16 v[128:131], v[176:179], v[32:35], v[128:131]\n\t" \
  "v_mfma_f32_16x16x32_bf16 v[136:139], v[176:179], v[36:39], v[136:139]\n\t" \
  "ds_read_b128 v[176:179], v208 offset:32768\n\t" \
  "v_mfma_f32_16x16x32_bf16 v[132:135], v[180:183], v[32:35], v[132:135]\n\t" \
  "v_mfma_f32_16x16x32_bf16 v[140:143], v[180:183], v[36:39], v[140:143]\n\t" \
  "ds_read_b128 v[180:183], v208 offset:49152\n\t" \
  "ds_read_b128 v[160:163], v221 offset:0\n\t" \
  "ds_read_b128 v[164:167], v221 offset:1024\n\t" \
  "s_sub_i32 s91, s86, 2\n\t" \
  "s_max_i32 s88, s88, s91\n\t" \
  "v_mov_b32 v173, s88\n\t" \
  "buffer_store_dword v173, v174, %[rs], 0 offen sc1\n\t" \
  "v_mfma_f32_16x16x32_bf16 v[128:131], v[184:187], v[40:43], v[128:131]\n\t" \
  "v_mfma_f32_16x16x32_bf16 v[136:139], v[184:187], v[44:47], v[136:139]\n\t" \
  "ds_read_b128 v[184:187], v209 offset:32768\n\t" \
  "s_add_u32 m0, %[lw], 98304\n\t" \
  "v_mfma_f32_16x16x32_bf16 v[132:135], v[188:191], v[40:43], v[132:135]\n\t" \
  "v_mfma_f32_16x16x32_bf16 v[140:143], v[188:191], v[44:47], v[140:143]\n\t" \
  "ds_read_b128 v[188:191], v209 offset:49152\n\t" \
  "global_load_lds_dwordx4 v216, s[92:93] sc1\n\t" \
  "s_waitcnt lgkmcnt(8)\n\t" \
  "v_mfma_f32_16x16x32_bf16 v[128:131], v[192:195], v[48:51], v[128:131]\n\t" \
  "v_mfma_f32_16x16x32_bf16 v[136:139], v[192:195], v[52:55], v[136:139]\n\t" \
  "ds_read_b128 v[192:195], v210 offset:32768\n\t" \
  "v_mfma_f32_16x16x32_bf16 v[132:135], v[196:199], v[48:51], v[132:135]\n\t" \
  "v_mfma_f32_16x16x32_bf16 v[140:143], v[196:199], v[52:55], v[140:143]\n\t" \
  "ds_read_b128 v[196:199], v210 offset:49152\n\t" \
  "s_waitcnt lgkmcnt(4)\n\t" \
  "v_add_f32 v160, v160, v144\n\t" \
  "v_add_f32 v161, v161, v145\n\t" \
  "v_add_f32 v162, v162, v146\n\t" \
  "v_add_f32 v163, v163, v147\n\t" \
  "v_add_f32 v164, v164, v148\n\t" \
  "v_add_f32 v165, v165, v149\n\t" \
  "v_add_f32 v166, v166, v150\n\t" \
  "v_add_f32 v167, v167, v151\n\t" \
  "v_mfma_f32_16x16x32_bf16 v[128:131], v[200:203], v[56:59], v[128:131]\n\t" \
  "v_mfma_f32_16x16x32_bf16 v[136:139], v[200:203], v[60:63], v[136:139]\n\t" \
  "ds_read_b128 v[200:203], v211 offset:32768\n\t" \
  "s_add_u32 m0, %[lw], 99328\n\t" \
  "v_mfma_f32_16x16x32_bf16 v[132:135], v[204:207], v[56:59], v[132:135]\n\t" \
  "v_mfma_f32_16x16x32_bf16 v[140:143], v[204:207], v[60:63], v[140:143]\n\t" \
  "ds_read_b128 v[204:207], v211 offset:49152\n\t" \
  "global_load_lds_dwordx4 v217, s[92:93] sc1\n\t" \
  "v_cvt_pk_bf16_f32 v168, v160, v161\n\t" \
  "v_cvt_pk_bf16_f32 v169, v162, v163\n\t" \
  "v_cvt_pk_bf16_f32 v170, v164, v165\n\t" \
  "v_cvt_pk_bf16_f32 v171, v166, v167\n\t" \
  "buffer_store_dwordx4 v[168:171], v172, %[rt], s90 offen sc1\n\t" \
  "v_mfma_f32_16x16x32_bf16 v[128:131], v[176:179], v[64:67], v[128:131]\n\t" \
  "v_mfma_f32_16x16x32_bf16 v[136:139], v[176:179], v[68:71], v[136:139]\n\t" \
  "ds_read_b128 v[176:179], v208 offset:33024\n\t" \
  "v_mfma_f32_16x16x32_bf16 v[132:135], v[180:183], v[64:67], v[132:135]\n\t" \
  "v_mfma_f32_16x16x32_bf16 v[140:143], v[180:183], v[68:71], v[140:143]\n\t" \
  "ds_read_b128 v[180:183], v208 offset:49408\n\t" \
  "s_waitcnt lgkmcnt(6)\n\t" \
  "v_mfma_f32_16x16x32_bf16 v[128:131], v[184:187], v[72:75], v[128:131]\n\t" \
  "v_mfma_f32_16x16x32_bf16 v[136:139], v[184:187], v[76:79], v[136:139]\n\t" \
  "ds_read_b128 v[184:187], v209 offset:33024\n\t" \
  "s_add_u32 m0, %[lw], 100352\n\t" \
  "v_mfma_f32_16x16x32_bf16 v[132:135], v[188:191], v[72:75], v[132:135]\n\t" \
  "v_mfma_f32_16x16x32_bf16 v[140:143], v[188:191], v[76:79], v[140:143]\n\t" \
  "ds_read_b128 v[188:191], v209 offset:49408\n\t" \
  "global_load_lds_dwordx4 v218, s[92:93] sc1\n\t" \
  "s_waitcnt lgkmcnt(6)\n\t" \
  "v_mfma_f32_16x16x32_bf16 v[128:131], v[192:195], v[80:83], v[128:131]\n\t" \
  "v_mfma_f32_16x16x32_bf16 v[136:139], v[192:195], v[84:87], v[136:139]\n\t" \
  "ds_read_b128 v[192:195], v210 offset:33024\n\t" \
  "v_mfma_f32_16x16x32_bf16 v[132:135], v[196:199], v[80:83], v[132:135]\n\t" \
  "v_mfma_f32_16x16x32_bf16 v[140:143], v[196:199], v[84:87], v[140:143]\n\t" \
  "ds_read_b128 v[196:199], v210 offset:49408\n\t" \
  "s_waitcnt lgkmcnt(6)\n\t" \
  "v_mfma_f32_16x16x32_bf16 v[128:131], v[200:203], v[88:91], v[128:131]\n\t" \
  "v_mfma_f32_16x16x32_bf16 v[136:139], v[200:203], v[92:95], v[136:139]\n\t" \
  "ds_read_b128 v[200:203], v211 offset:33024\n\t" \
  "s_add_u32 m0, %[lw], 101376\n\t" \
  "v_mfma_f32_16x16x32_bf16 v[132:135], v[204:207], v[88:91], v[132:135]\n\t" \
  "v_mfma_f32_16x16x32_bf16 v[140:143], v[204:207], v[92:95], v[140:143]\n\t" \
  "ds_read_b128 v[204:207], v211 offset:49408\n\t" \
  "global_load_lds_dwordx4 v219, s[92:93] sc1\n\t" \
  "s_cmp_lt_u32 s86, 8\n\t" \
  "s_cbranch_scc1 .Lw11%=\n\t" \
  "s_waitcnt vmcnt(6)\n\t" \
  "s_barrier\n\t" \
  ".Lg11%=:\n\t" \
  "s_add_u32 s92, s92, 65024\n\t" \
  "s_addc_u32 s93, s93, 0\n\t" \
  "s_waitcnt lgkmcnt(6)\n\t" \
  "v_mfma_f32_16x16x32_bf16 v[128:131], v[176:179], v[96:99], v[128:131]\n\t" \
  "v_mfma_f32_16x16x32_bf16 v[136:139], v[176:179], v[100:103], v[136:139]\n\t" \
  "ds_read_b128 v[176:179], v212 offset:0\n\t" \
  "v_mfma_f32_16x16x32_bf16 v[132:135], v[180:183], v[96:99], v[132:135]\n\t" \
  "v_mfma_f32_16x16x32_bf16 v[140:143], v[180:183], v[100:103], v[140:143]\n\t" \
  "ds_read_b128 v[180:183], v212 offset:16384\n\t" \
  "s_waitcnt lgkmcnt(6)\n\t" \
  "v_mfma_f32_16x16x32_bf16 v[128:131], v[184:187], v[104:107], v[128:131]\n\t" \
  "v_mfma_f32_16x16x32_bf16 v[136:139], v[184:187], v[108:111], v[136:139]\n\t" \
  "ds_read_b128 v[184:187], v213 offset:0\n\t" \
  "s_add_u32 m0, %[lw], 0\n\t" \
  "v_mfma_f32_16x16x32_bf16 v[132:135], v[188:191], v[104:107], v[132:135]\n\t" \
  "v_mfma_f32_16x16x32_bf16 v[140:143], v[188:191], v[108:111], v[140:143]\n\t" \
  "ds_read_b128 v[188:191], v213 offset:16384\n\t" \
  "global_load_lds_dwordx4 v216, s[92:93] sc1\n\t" \
  "s_waitcnt lgkmcnt(6)\n\t" \
  "v_mfma_f32_16x16x32_bf16 v[128:131], v[192:195], v[112:115], v[128:131]\n\t" \
  "v_mfma_f32_16x16x32_bf16 v[136:139], v[192:195], v[116:119], v[136:139]\n\t" \
  "ds_read_b128 v[192:195], v214 offset:0\n\t" \
  "v_mfma_f32_16x16x32_bf16 v[132:135], v[196:199], v[112:115], v[132:135]\n\t" \
  "v_mfma_f32_16x16x32_bf16 v[140:143], v[196:199], v[116:119], v[140:143]\n\t" \
  "ds_read_b128 v[196:199], v214 offset:16384\n\t" \
  "s_waitcnt lgkmcnt(6)\n\t" \
  "v_mfma_f32_16x16x32_bf16 v[128:131], v[200:203], v[120:123], v[128:131]\n\t" \
  "v_mfma_f32_16x16x32_bf16 v[136:139], v[200:203], v[124:127], v[136:139]\n\t" \
  "ds_read_b128 v[200:203], v215 offset:0\n\t" \
  "s_add_u32 m0, %[lw], 1024\n\t" \
  "v_mfma_f32_16x16x32_bf16 v[132:135], v[204:207], v[120:123], v[132:135]\n\t" \
  "v_mfma_f32_16x16x32_bf16 v[140:143], v[204:207], v[124:127], v[140:143]\n\t" \
  "ds_read_b128 v[204:207], v215 offset:16384\n\t" \
  "global_load_lds_dwordx4 v217, s[92:93] sc1\n\t" \
  "s_add_u32 s86, s86, 1\n\t" \
  "s_add_u32 s87, s87, 1\n\t" \
  "s_cmp_le_u32 s86, 5\n\t" \
  "s_cbranch_scc1 .Lf11%=\n\t" \
  ".Lfb11%=:\n\t" \
  "s_waitcnt lgkmcnt(6)\n\t" \
  "v_mfma_f32_16x16x32_bf16 v[144:147], v[176:179], v[0:3], 0\n\t" \
  "v_mfma_f32_16x16x32_bf16 v[152:155], v[176:179], v[4:7], 0\n\t" \
  "ds_read_b128 v[176:179], v212 offset:256\n\t" \
  "v_mfma_f32_16x16x32_bf16 v[148:151], v[180:183], v[0:3], 0\n\t" \
  "v_mfma_f32_16x16x32_bf16 v[156:159], v[180:183], v[4:7], 0\n\t" \
  "ds_read_b128 v[180:183], v212 offset:16640\n\t" \
  "s_waitcnt lgkmcnt(6)\n\t" \
  "v_mfma_f32_16x16x32_bf16 v[144:147], v[184:187], v[8:11], v[144:147]\n\t" \
  "v_mfma_f32_16x16x32_bf16 v[152:155], v[184:187], v[12:15], v[152:155]\n\t" \
  "ds_read_b128 v[184:187], v213 offset:256\n\t" \
  "s_add_u32 m0, %[lw], 2048\n\t" \
  "v_mfma_f32_16x16x32_bf16 v[148:151], v[188:191], v[8:11], v[148:151]\n\t" \
  "v_mfma_f32_16x16x32_bf16 v[156:159], v[188:191], v[12:15], v[156:159]\n\t" \
  "ds_read_b128 v[188:191], v213 offset:16640\n\t" \
  "global_load_lds_dwordx4 v218, s[92:93] sc1\n\t" \
  "ds_write_b128 v220, v[136:139] offset:0\n\t" \
  "ds_write_b128 v220, v[140:143] offset:1024\n\t" \
  "s_waitcnt lgkmcnt(8)\n\t" \
  "v_mfma_f32_16x16x32_bf16 v[144:147], v[192:195], v[16:19], v[144:147]\n\t" \
  "v_mfma_f32_16x16x32_bf16 v[152:155], v[192:195], v[20:23], v[152:155]\n\t" \
  "ds_read_b128 v[192:195], v214 offset:256\n\t" \
  "v_mfma_f32_16x16x32_bf16 v[148:151], v[196:199], v[16:19], v[148:151]\n\t" \
  "v_mfma_f32_16x16x32_bf16 v[156:159], v[196:199], v[20:23], v[156:159]\n\t" \
  "ds_read_b128 v[196:199], v214 offset:16640\n\t" \
  "s_lshr_b32 s90, s87, 7\n\t" \
  "s_xor_b32 s90, s90, %[s0]\n\t" \
  "s_and_b32 s90, s90, 1\n\t" \
  "s_lshl1_add_u32 s90, s87, s90\n\t" \
  "s_lshl_b32 s90, s90, 13\n\t" \
  "s_cmp_lt_i32 s86, 6\n\t" \
  "s_cselect_b32 s90, 0x70000000, s90\n\t" \
  "s_waitcnt lgkmcnt(8)\n\t" \
  "v_mfma_f32_16x16x32_bf16 v[144:147], v[200:203], v[24:27], v[144:147]\n\t" \
  "v_mfma_f32_16x16x32_bf16 v[152:155], v[200:203], v[28:31], v[152:155]\n\t" \
  "ds_read_b128 v[200:203], v215 offset:256\n\t" \
  "s_add_u32 m0, %[lw], 3072\n\t" \
  "v_mfma_f32_16x16x32_bf16 v[148:151], v[204:207], v[24:27], v[148:151]\n\t" \
  "v_mfma_f32_16x16x32_bf16 v[156:159], v[204:207], v[28:31], v[156:159]\n\t" \
  "ds_read_b128 v[204:207], v215 offset:16640\n\t" \
  "global_load_lds_dwordx4 v219, s[92:93] sc1\n\t" \
  "s_waitcnt lgkmcnt(4)\n\t" \
  "s_cmp_lt_u32 s86, 6\n\t" \
  "s_cbranch_scc1 .Lw21%=\n\t" \
  "s_waitcnt vmcnt(4)\n\t" \
  "s_barrier\n\t" \
  ".Lg21%=:\n\t" \
  "s_add_u32 s92, s92, 512\n\t" \
  "s_addc_u32 s93, s93, 0\n\t" \
  "v_mfma_f32_16x16x32_bf16 v[144:147], v[176:179], v[32:35], v[144:147]\n\t" \
  "v_mfma_f32_16x16x32_bf16 v[152:155], v[176:179], v[36:39], v[152:155]\n\t" \
  "ds_read_b128 v[176:179], v212 offset:32768\n\t" \
  "v_mfma_f32_16x16x32_bf16 v[148:151], v[180:183], v[32:35], v[148:151]\n\t" \
  "v_mfma_f32_16x16x32_bf16 v[156:159], v[180:183], v[36:39], v[156:159]\n\t" \
  "ds_read_b128 v[180:183], v212 offset:49152\n\t" \
  "ds_read_b128 v[160:163], v221 offset:0\n\t" \
  "ds_read_b128 v[164:167], v221 offset:1024\n\t" \
  "s_sub_i32 s91, s86, 2\n\t" \
  "s_max_i32 s88, s88, s91\n\t" \
  "v_mov_b32 v173, s88\n\t" \
  "buffer_store_dword v173, v174, %[rs], 0 offen sc1\n\t" \
  "v_mfma_f32_16x16x32_bf16 v[144:147], v[184:187], v[40:43], v[144:147]\n\t" \
  "v_mfma_f32_16x16x32_bf16 v[152:155], v[184:187], v[44:47], v[152:155]\n\t" \
  "ds_read_b128 v[184:187], v213 offset:32768\n\t" \
  "s_add_u32 m0, %[lw], 32768\n\t" \
  "v_mfma_f32_16x16x32_bf16 v[148:151], v[188:191], v[40:43], v[148:151]\n\t" \
  "v_mfma_f32_16x16x32_bf16 v[156:159], v[188:191], v[44:47], v[156:159]\n\t" \
  "ds_read_b128 v[188:191], v213 offset:49152\n\t" \
  "global_load_lds_dwordx4 v216, s[92:93] sc1\n\t" \
  "s_waitcnt lgkmcnt(8)\n\t" \
  "v_mfma_f32_16x16x32_bf16 v[144:147], v[192:195], v[48:51], v[144:147]\n\t" \
  "v_mfma_f32_16x16x32_bf16 v[152:155], v[192:195], v[52:55], v[152:155]\n\t" \
  "ds_read_b128 v[192:195], v214 offset:32768\n\t" \
  "v_mfma_f32_16x16x32_bf16 v[148:151], v[196:199], v[48:51], v[148:151]\n\t" \
  "v_mfma_f32_16x16x32_bf16 v[156:159], v[196:199], v[52:55], v[156:159]\n\t" \
  "ds_read_b128 v[196:199], v214 offset:49152\n\t" \
  "s_waitcnt lgkmcnt(4)\n\t" \
  "v_add_f32 v160, v160, v128\n\t" \
  "v_add_f32 v161, v161, v129\n\t" \
  "v_add_f32 v162, v162, v130\n\t" \
  "v_add_f32 v163, v163, v131\n\t" \
  "v_add_f32 v164, v164, v132\n\t" \
  "v_add_f32 v165, v165, v133\n\t" \
  "v_add_f32 v166, v166, v134\n\t" \
  "v_add_f32 v167, v167, v135\n\t" \
  "v_mfma_f32_16x16x32_bf16 v[144:147], v[200:203], v[56:59], v[144:147]\n\t" \
  "v_mfma_f32_16x16x32_bf16 v[152:155], v[200:203], v[60:63], v[152:155]\n\t" \
  "ds_read_b128 v[200:203], v215 offset:32768\n\t" \
  "s_add_u32 m0, %[lw], 33792\n\t" \
  "v_mfma_f32_16x16x32_bf16 v[148:151], v[204:207], v[56:59], v[148:151]\n\t" \
  "v_mfma_f32_16x16x32_bf16 v[156:159], v[204:207], v[60:63], v[156:159]\n\t" \
  "ds_read_b128 v[204:207], v215 offset:49152\n\t" \
  "global_load_lds_dwordx4 v217, s[92:93] sc1\n\t" \
  "v_cvt_pk_bf16_f32 v168, v160, v161\n\t" \
  "v_cvt_pk_bf16_f32 v169, v162, v163\n\t" \
  "v_cvt_pk_bf16_f32 v170, v164, v165\n\t" \
  "v_cvt_pk_bf16_f32 v171, v166, v167\n\t" \
  "buffer_store_dwordx4 v[168:171], v172, %[rt], s90 offen sc1\n\t" \
  "v_mfma_f32_16x16x32_bf16 v[144:147], v[176:179], v[64:67], v[144:147]\n\t" \
  "v_mfma_f32_16x16x32_bf16 v[152:155], v[176:179], v[68:71], v[152:155]\n\t" \
  "ds_read_b128 v[176:179], v212 offset:33024\n\t" \
  "v_mfma_f32_16x16x32_bf16 v[148:151], v[180:183], v[64:67], v[148:151]\n\t" \
  "v_mfma_f32_16x16x32_bf16 v[156:159], v[180:183], v[68:71], v[156:159]\n\t" \
  "ds_read_b128 v[180:183], v212 offset:49408\n\t" \
  "s_waitcnt lgkmcnt(6)\n\t" \
  "v_mfma_f32_16x16x32_bf16 v[144:147], v[184:187], v[72:75], v[144:147]\n\t" \
  "v_mfma_f32_16x16x32_bf16 v[152:155], v[184:187], v[76:79], v[152:155]\n\t" \
  "ds_read_b128 v[184:187], v213 offset:33024\n\t" \
  "s_add_u32 m0, %[lw], 34816\n\t" \
  "v_mfma_f32_16x16x32_bf16 v[148:151], v[188:191], v[72:75], v[148:151]\n\t" \
  "v_mfma_f32_16x16x32_bf16 v[156:159], v[188:191], v[76:79], v[156:159]\n\t" \
  "ds_read_b128 v[188:191], v213 offset:49408\n\t" \
  "global_load_lds_dwordx4 v218, s[92:93] sc1\n\t" \
  "s_waitcnt lgkmcnt(6)\n\t" \
  "v_mfma_f32_16x16x32_bf16 v[144:147], v[192:195], v[80:83], v[144:147]\n\t" \
  "v_mfma_f32_16x16x32_bf16 v[152:155], v[192:195], v[84:87], v[152:155]\n\t" \
  "ds_read_b128 v[192:195], v214 offset:33024\n\t" \
  "v_mfma_f32_16x16x32_bf16 v[148:151], v[196:199], v[80:83], v[148:151]\n\t" \
  "v_mfma_f32_16x16x32_bf16 v[156:159], v[196:199], v[84:87], v[156:159]\n\t" \
  "ds_read_b128 v[196:199], v214 offset:49408\n\t" \
  "s_waitcnt lgkmcnt(6)\n\t" \
  "v_mfma_f32_16x16x32_bf16 v[144:147], v[200:203], v[88:91], v[144:147]\n\t" \
  "v_mfma_f32_16x16x32_bf16 v[152:155], v[200:203], v[92:95], v[152:155]\n\t" \
  "ds_read_b128 v[200:203], v215 offset:33024\n\t" \
  "s_add_u32 m0, %[lw], 35840\n\t" \
  "v_mfma_f32_16x16x32_bf16 v[148:151], v[204:207], v[88:91], v[148:151]\n\t" \
  "v_mfma_f32_16x16x32_bf16 v[156:159], v[204:207], v[92:95], v[156:159]\n\t" \
  "ds_read_b128 v[204:207], v215 offset:49408\n\t" \
  "global_load_lds_dwordx4 v219, s[92:93] sc1\n\t" \
  "s_cmp_lt_u32 s86, 8\n\t" \
  "s_cbranch_scc1 .Lw31%=\n\t" \
  "s_waitcnt vmcnt(6)\n\t" \
  "s_barrier\n\t" \
  ".Lg31%=:\n\t" \
  "s_add_u32 s92, s92, 65024\n\t" \
  "s_addc_u32 s93, s93, 0\n\t" \
  "s_waitcnt lgkmcnt(6)\n\t" \
  "v_mfma_f32_16x16x32_bf16 v[144:147], v[176:179], v[96:99], v[144:147]\n\t" \
  "v_mfma_f32_16x16x32_bf16 v[152:155], v[176:179], v[100:103], v[152:155]\n\t" \
  "ds_read_b128 v[176:179], v208 offset:0\n\t" \
  "v_mfma_f32_16x16x32_bf16 v[148:151], v[180:183], v[96:99], v[148:151]\n\t" \
  "v_mfma_f32_16x16x32_bf16 v[156:159], v[180:183], v[100:103], v[156:159]\n\t" \
  "ds_read_b128 v[180:183], v208 offset:16384\n\t" \
  "s_waitcnt lgkmcnt(6)\n\t" \
  "v_mfma_f32_16x16x32_bf16 v[144:147], v[184:187], v[104:107], v[144:147]\n\t" \
  "v_mfma_f32_16x16x32_bf16 v[152:155], v[184:187], v[108:111], v[152:155]\n\t" \
  "ds_read_b128 v[184:187], v209 offset:0\n\t" \
  "s_add_u32 m0, %[lw], 65536\n\t" \
  "v_mfma_f32_16x16x32_bf16 v[148:151], v[188:191], v[104:107], v[148:151]\n\t" \
  "v_mfma_f32_16x16x32_bf16 v[156:159], v[188:191], v[108:111], v[156:159]\n\t" \
  "ds_read_b128 v[188:191], v209 offset:16384\n\t" \
  "global_load_lds_dwordx4 v216, s[92:93] sc1\n\t" \
  "s_waitcnt lgkmcnt(6)\n\t" \
  "v_mfma_f32_16x16x32_bf16 v[144:147], v[192:195], v[112:115], v[144:147]\n\t" \
  "v_mfma_f32_16x16x32_bf16 v[152:155], v[192:195], v[116:119], v[152:155]\n\t" \
  "ds_read_b128 v[192:195], v210 offset:0\n\t" \
  "v_mfma_f32_16x16x32_bf16 v[148:151], v[196:199], v[112:115], v[148:151]\n\t" \
  "v_mfma_f32_16x16x32_bf16 v[156:159], v[196:199], v[116:119], v[156:159]\n\t" \
  "ds_read_b128 v[196:199], v210 offset:16384\n\t" \
  "s_waitcnt lgkmcnt(6)\n\t" \
  "v_mfma_f32_16x16x32_bf16 v[144:147], v[200:203], v[120:123], v[144:147]\n\t" \
  "v_mfma_f32_16x16x32_bf16 v[152:155], v[200:203], v[124:127], v[152:155]\n\t" \
  "ds_read_b128 v[200:203], v211 offset:0\n\t" \
  "s_add_u32 m0, %[lw], 66560\n\t" \
  "v_mfma_f32_16x16x32_bf16 v[148:151], v[204:207], v[120:123], v[148:151]\n\t" \
  "v_mfma_f32_16x16x32_bf16 v[156:159], v[204:207], v[124:127], v[156:159]\n\t" \
  "ds_read_b128 v[204:207], v211 offset:16384\n\t" \
  "global_load_lds_dwordx4 v217, s[92:93] sc1\n\t" \
  "s_add_u32 s86, s86, 1\n\t" \
  "s_add_u32 s87, s87, 1\n\t" \
  "s_cmp_lt_u32 s86, %[end]\n\t" \
  "s_cbranch_scc1 .Lp1%=\n\t" \
  "s_branch .Lx1%=\n\t" \
  ".Lf01%=:\n\t" \
  "s_nop 7\n\t" \
  "ds_write_b128 v220, v[152:155] offset:0\n\t" \
  "ds_write_b128 v220, v[156:159] offset:1024\n\t" \
  "s_lshr_b32 s90, s87, 7\n\t" \
  "s_xor_b32 s90, s90, %[s0]\n\t" \
  "s_and_b32 s90, s90, 1\n\t" \
  "s_lshl1_add_u32 s90, s87, s90\n\t" \
  "s_lshl_b32 s90, s90, 13\n\t" \
  "s_cmp_lt_i32 s86, 1\n\t" \
  "s_cselect_b32 s90, 0x70000000, s90\n\t" \
  "s_waitcnt lgkmcnt(0)\n\t" \
  "s_barrier\n\t" \
  "ds_read_b128 v[160:163], v221 offset:0\n\t" \
  "ds_read_b128 v[164:167], v221 offset:1024\n\t" \
  "s_waitcnt lgkmcnt(0)\n\t" \
  "v_add_f32 v160, v160, v144\n\t" \
  "v_add_f32 v161, v161, v145\n\t" \
  "v_add_f32 v162, v162, v146\n\t" \
  "v_add_f32 v163, v163, v147\n\t" \
  "v_add_f32 v164, v164, v148\n\t" \
  "v_add_f32 v165, v165, v149\n\t" \
  "v_add_f32 v166, v166, v150\n\t" \
  "v_add_f32 v167, v167, v151\n\t" \
  "v_cvt_pk_bf16_f32 v168, v160, v161\n\t" \
  "v_cvt_pk_bf16_f32 v169, v162, v163\n\t" \
  "v_cvt_pk_bf16_f32 v170, v164, v165\n\t" \
  "v_cvt_pk_bf16_f32 v171, v166, v167\n\t" \
  "buffer_store_dwordx4 v[168:171], v172, %[rt], s90 offen sc1\n\t" \
  "s_branch .Lfb01%=\n\t" \
  ".Lf11%=:\n\t" \
  "s_nop 7\n\t" \
  "ds_write_b128 v220, v[136:139] offset:0\n\t" \
  "ds_write_b128 v220, v[140:143] offset:1024\n\t" \
  "s_lshr_b32 s90, s87, 7\n\t" \
  "s_xor_b32 s90, s90, %[s0]\n\t" \
  "s_and_b32 s90, s90, 1\n\t" \
  "s_lshl1_add_u32 s90, s87, s90\n\t" \
  "s_lshl_b32 s90, s90, 13\n\t" \
  "s_cmp_lt_i32 s86, 1\n\t" \
  "s_cselect_b32 s90, 0x70000000, s90\n\t" \
  "s_waitcnt lgkmcnt(0)\n\t" \
  "s_barrier\n\t" \
  "ds_read_b128 v[160:163], v221 offset:0\n\t" \
  "ds_read_b128 v[164:167], v221 offset:1024\n\t" \
  "s_waitcnt lgkmcnt(0)\n\t" \
  "v_add_f32 v160, v160, v128\n\t" \
  "v_add_f32 v161, v161, v129\n\t" \
  "v_add_f32 v162, v162, v130\n\t" \
  "v_add_f32 v163, v163, v131\n\t" \
  "v_add_f32 v164, v164, v132\n\t" \
  "v_add_f32 v165, v165, v133\n\t" \
  "v_add_f32 v166, v166, v134\n\t" \
  "v_add_f32 v167, v167, v135\n\t" \
  "v_cvt_pk_bf16_f32 v168, v160, v161\n\t" \
  "v_cvt_pk_bf16_f32 v169, v162, v163\n\t" \
  "v_cvt_pk_bf16_f32 v170, v164, v165\n\t" \
  "v_cvt_pk_bf16_f32 v171, v166, v167\n\t" \
  "buffer_store_dwordx4 v[168:171], v172, %[rt], s90 offen sc1\n\t" \
  "s_branch .Lfb11%=\n\t" \
  ".Lw01%=:\n\t" \
  "s_waitcnt vmcnt(2)\n\t" \
  "s_barrier\n\t" \
  "s_sub_i32 s91, s86, 1\n\t" \
  "s_max_i32 s88, s88, s91\n\t" \
  "v_mov_b32 v173, s88\n\t" \
  "buffer_store_dword v173, v174, %[rs], 0 offen sc1\n\t" \
  "s_branch .Lg01%=\n\t" \
  ".Lw11%=:\n\t" \
  "s_waitcnt vmcnt(2)\n\t" \
  "s_barrier\n\t" \
  "s_sub_i32 s91, s86, 1\n\t" \
  "s_max_i32 s88, s88, s91\n\t" \
  "v_mov_b32 v173, s88\n\t" \
  "buffer_store_dword v173, v174, %[rs], 0 offen sc1\n\t" \
  "s_branch .Lg11%=\n\t" \
  ".Lw21%=:\n\t" \
  "s_waitcnt vmcnt(2)\n\t" \
  "s_barrier\n\t" \
  "s_sub_i32 s91, s86, 1\n\t" \
  "s_max_i32 s88, s88, s91\n\t" \
  "v_mov_b32 v173, s88\n\t" \
  "buffer_store_dword v173, v174, %[rs], 0 offen sc1\n\t" \
  "s_branch .Lg21%=\n\t" \
  ".Lw31%=:\n\t" \
  "s_waitcnt vmcnt(2)\n\t" \
  "s_barrier\n\t" \
  "s_sub_i32 s91, s86, 1\n\t" \
  "s_max_i32 s88, s88, s91\n\t" \
  "v_mov_b32 v173, s88\n\t" \
  "buffer_store_dword v173, v174, %[rs], 0 offen sc1\n\t" \
  "s_branch .Lg31%=\n\t" \
  ".Lx1%=:\n\t" \
  "s_waitcnt lgkmcnt(0)\n\t" \
  "s_setprio 0\n\t" \
  "s_mov_b32 m0, s94\n\t" \
  ".Le%=:\n\t"
#define PP_ASM_0 \
  "s_cmp_lg_u32 %[kh], 0\n\t" \
  "s_cbranch_scc1 .Lk%=\n\t" \
  "s_mov_b32 s94, m0\n\t" \
  "s_mov_b64 s[92:93], %[x0]\n\t" \
  "s_mov_b32 s86, %[g0]\n\t" \
  "s_add_u32 s87, s86, %[t0]\n\t" \
  "s_add_u32 m0, %[lw], 67584\n\t" \
  "s_nop 0\n\t" \
  "global_load_lds_dwordx4 v250, s[92:93] sc1\n\t" \
  "s_add_u32 m0, %[lw], 68608\n\t" \
  "s_nop 0\n\t" \
  "global_load_lds_dwordx4 v251, s[92:93] sc1\n\t" \
  "s_barrier\n\t" \
  "s_branch .Ls0%=\n\t" \
  ".Lp0%=:\n\t" \
  "s_waitcnt vmcnt(9)\n\t" \
  "s_barrier\n\t" \
  ".Ls0%=:\n\t" \
  "s_add_u32 s92, s92, 512\n\t" \
  "s_addc_u32 s93, s93, 0\n\t" \
  "ds_read_b128 v[176:179], v240 offset:0\n\t" \
  "ds_read_b128 v[180:183], v240 offset:16384\n\t" \
  "ds_read_b128 v[184:187], v241 offset:0\n\t" \
  "ds_read_b128 v[188:191], v241 offset:16384\n\t" \
  "s_add_u32 m0, %[lw], 98304\n\t" \
  "s_nop 0\n\t" \
  "global_load_lds_dwordx4 v248, s[92:93] sc1\n\t" \
  "ds_read_b128 v[192:195], v242 offset:0\n\t" \
  "ds_read_b128 v[196:199], v242 offset:16384\n\t" \
  "ds_read_b128 v[200:203], v243 offset:0\n\t" \
  "ds_read_b128 v[204:207], v243 offset:16384\n\t" \
  "s_add_u32 m0, %[lw], 99328\n\t" \
  "s_nop 0\n\t" \
  "global_load_lds_dwordx4 v249, s[92:93] sc1\n\t" \
  "ds_read_b128 v[208:211], v240 offset:256\n\t" \
  "ds_read_b128 v[212:215], v240 offset:16640\n\t" \
  "ds_read_b128 v[216:219], v241 offset:256\n\t" \
  "ds_read_b128 v[220:223], v241 offset:16640\n\t" \
  "s_add_u32 m0, %[lw], 100352\n\t" \
  "s_nop 0\n\t" \
  "global_load_lds_dwordx4 v250, s[92:93] sc1\n\t" \
  "ds_read_b128 v[224:227], v242 offset:256\n\t" \
  "ds_read_b128 v[228:231], v242 offset:16640\n\t" \
  "ds_read_b128 v[232:235], v243 offset:256\n\t" \
  "ds_read_b128 v[236:239], v243 offset:16640\n\t" \
  "s_add_u32 m0, %[lw], 101376\n\t" \
  "s_nop 0\n\t" \
  "global_load_lds_dwordx4 v251, s[92:93] sc1\n\t" \
  "ds_write_b128 v252, v[152:155] offset:0\n\t" \
  "ds_write_b128 v252, v[156:159] offset:1024\n\t" \
  "s_waitcnt lgkmcnt(0)\n\t" \
  "s_setprio 1\n\t" \
  "v_mfma_f32_16x16x32_bf16 v[128:131], v[0:3], v[176:179], 0\n\t" \
  "v_mfma_f32_16x16x32_bf16 v[132:135], v[4:7], v[176:179], 0\n\t" \
  "v_mfma_f32_16x16x32_bf16 v[136:139], v[0:3], v[180:183], 0\n\t" \
  "v_mfma_f32_16x16x32_bf16 v[140:143], v[4:7], v[180:183], 0\n\t" \
  "v_mfma_f32_16x16x32_bf16 v[128:131], v[8:11], v[184:187], v[128:131]\n\t" \
  "v_mfma_f32_16x16x32_bf16 v[132:135], v[12:15], v[184:187], v[132:135]\n\t" \
  "v_mfma_f32_16x16x32_bf16 v[136:139], v[8:11], v[188:191], v[136:139]\n\t" \
  "v_mfma_f32_16x16x32_bf16 v[140:143], v[12:15], v[188:191], v[140:143]\n\t" \
  "v_mfma_f32_16x16x32_bf16 v[128:131], v[16:19], v[192:195], v[128:131]\n\t" \
  "v_mfma_f32_16x16x32_bf16 v[132:135], v[20:23], v[192:195], v[132:135]\n\t" \
  "v_mfma_f32_16x16x32_bf16 v[136:139], v[16:19], v[196:199], v[136:139]\n\t" \
  "v_mfma_f32_16x16x32_bf16 v[140:143], v[20:23], v[196:199], v[140:143]\n\t" \
  "v_mfma_f32_16x16x32_bf16 v[128:131], v[24:27], v[200:203], v[128:131]\n\t" \
  "v_mfma_f32_16x16x32_bf16 v[132:135], v[28:31], v[200:203], v[132:135]\n\t" \
  "v_mfma_f32_16x16x32_bf16 v[136:139], v[24:27], v[204:207], v[136:139]\n\t" \
  "v_mfma_f32_16x16x32_bf16 v[140:143], v[28:31], v[204:207], v[140:143]\n\t" \
  "v_mfma_f32_16x16x32_bf16 v[128:131], v[32:35], v[208:211], v[128:131]\n\t" \
  "v_mfma_f32_16x16x32_bf16 v[132:135], v[36:39], v[208:211], v[132:135]\n\t" \
  "v_mfma_f32_16x16x32_bf16 v[136:139], v[32:35], v[212:215], v[136:139]\n\t" \
  "v_mfma_f32_16x16x32_bf16 v[140:143], v[36:39], v[212:215], v[140:143]\n\t" \
  "v_mfma_f32_16x16x32_bf16 v[128:131], v[40:43], v[216:219], v[128:131]\n\t" \
  "v_mfma_f32_16x16x32_bf16 v[132:135], v[44:47], v[216:219], v[132:135]\n\t" \
  "v_mfma_f32_16x16x32_bf16 v[136:139], v[40:43], v[220:223], v[136:139]\n\t" \
  "v_mfma_f32_16x16x32_bf16 v[140:143], v[44:47], v[220:223], v[140:143]\n\t" \
  "v_mfma_f32_16x16x32_bf16 v[128:131], v[48:51], v[224:227], v[128:131]\n\t" \
  "v_mfma_f32_16x16x32_bf16 v[132:135], v[52:55], v[224:227], v[132:135]\n\t" \
  "v_mfma_f32_16x16x32_bf16 v[136:139], v[48:51], v[228:231], v[136:139]\n\t" \
  "v_mfma_f32_16x16x32_bf16 v[140:143], v[52:55], v[228:231], v[140:143]\n\t" \
  "v_mfma_f32_16x16x32_bf16 v[128:131], v[56:59], v[232:235], v[128:131]\n\t" \
  "v_mfma_f32_16x16x32_bf16 v[132:135], v[60:63], v[232:235], v[132:135]\n\t" \
  "v_mfma_f32_16x16x32_bf16 v[136:139], v[56:59], v[236:239], v[136:139]\n\t" \
  "v_mfma_f32_16x16x32_bf16 v[140:143], v[60:63], v[236:239], v[140:143]\n\t" \
  "s_setprio 0\n\t" \
  "s_waitcnt vmcnt(4)\n\t" \
  "s_barrier\n\t" \
  "s_add_u32 s92, s92, 65024\n\t" \
  "s_addc_u32 s93, s93, 0\n\t" \
  "ds_read_b128 v[160:163], v253 offset:0\n\t" \
  "ds_read_b128 v[164:167], v253 offset:1024\n\t" \
  "ds_read_b128 v[176:179], v240 offset:32768\n\t" \
  "ds_read_b128 v[180:183], v240 offset:49152\n\t" \
  "ds_read_b128 v[184:187], v241 offset:32768\n\t" \
  "ds_read_b128 v[188:191], v241 offset:49152\n\t" \
  "s_add_u32 m0, %[lw], 0\n\t" \
  "s_nop 0\n\t" \
  "global_load_lds_dwordx4 v248, s[92:93] sc1\n\t" \
  "ds_read_b128 v[192:195], v242 offset:32768\n\t" \
  "ds_read_b128 v[196:199], v242 offset:49152\n\t" \
  "ds_read_b128 v[200:203], v243 offset:32768\n\t" \
  "ds_read_b128 v[204:207], v243 offset:49152\n\t" \
  "s_add_u32 m0, %[lw], 1024\n\t" \
  "s_nop 0\n\t" \
  "global_load_lds_dwordx4 v249, s[92:93] sc1\n\t" \
  "ds_read_b128 v[208:211], v240 offset:33024\n\t" \
  "ds_read_b128 v[212:215], v240 offset:49408\n\t" \
  "ds_read_b128 v[216:219], v241 offset:33024\n\t" \
  "ds_read_b128 v[220:223], v241 offset:49408\n\t" \
  "s_add_u32 m0, %[lw], 2048\n\t" \
  "s_nop 0\n\t" \
  "global_load_lds_dwordx4 v250, s[92:93] sc1\n\t" \
  "ds_read_b128 v[224:227], v242 offset:33024\n\t" \
  "ds_read_b128 v[228:231], v242 offset:49408\n\t" \
  "ds_read_b128 v[232:235], v243 offset:33024\n\t" \
  "ds_read_b128 v[236:239], v243 offset:49408\n\t" \
  "s_add_u32 m0, %[lw], 3072\n\t" \
  "s_nop 0\n\t" \
  "global_load_lds_dwordx4 v251, s[92:93] sc1\n\t" \
  "s_waitcnt lgkmcnt(15)\n\t" \
  "s_waitcnt lgkmcnt(12)\n\t" \
  "s_lshr_b32 s90, s87, 7\n\t" \
  "s_xor_b32 s90, s90, %[s0]\n\t" \
  "s_and_b32 s90, s90, 1\n\t" \
  "s_lshl1_add_u32 s90, s87, s90\n\t" \
  "s_lshl_b32 s90, s90, 13\n\t" \
  "v_add_f32 v160, v160, v144\n\t" \
  "v_add_f32 v161, v161, v145\n\t" \
  "v_add_f32 v162, v162, v146\n\t" \
  "v_add_f32 v163, v163, v147\n\t" \
  "v_add_f32 v164, v164, v148\n\t" \
  "v_add_f32 v165, v165, v149\n\t" \
  "v_add_f32 v166, v166, v150\n\t" \
  "v_add_f32 v167, v167, v151\n\t" \
  "v_cvt_pk_bf16_f32 v168, v160, v161\n\t" \
  "v_cvt_pk_bf16_f32 v169, v162, v163\n\t" \
  "v_cvt_pk_bf16_f32 v170, v164, v165\n\t" \
  "v_cvt_pk_bf16_f32 v171, v166, v167\n\t" \
  "buffer_store_dwordx4 v[168:171], v172, %[rt], s90 offen sc1\n\t" \
  "s_add_u32 s86, s86, 1\n\t" \
  "s_add_u32 s87, s87, 1\n\t" \
  "s_waitcnt lgkmcnt(0)\n\t" \
  "s_setprio 1\n\t" \
  "v_mfma_f32_16x16x32_bf16 v[128:131], v[64:67], v[176:179], v[128:131]\n\t" \
  "v_mfma_f32_16x16x32_bf16 v[132:135], v[68:71], v[176:179], v[132:135]\n\t" \
  "v_mfma_f32_16x16x32_bf16 v[136:139], v[64:67], v[180:183], v[136:139]\n\t" \
  "v_mfma_f32_16x16x32_bf16 v[140:143], v[68:71], v[180:183], v[140:143]\n\t" \
  "v_mfma_f32_16x16x32_bf16 v[128:131], v[72:75], v[184:187], v[128:131]\n\t" \
  "v_mfma_f32_16x16x32_bf16 v[132:135], v[76:79], v[184:187], v[132:135]\n\t" \
  "v_mfma_f32_16x16x32_bf16 v[136:139], v[72:75], v[188:191], v[136:139]\n\t" \
  "v_mfma_f32_16x16x32_bf16 v[140:143], v[76:79], v[188:191], v[140:143]\n\t" \
  "v_mfma_f32_16x16x32_bf16 v[128:131], v[80:83], v[192:195], v[128:131]\n\t" \
  "v_mfma_f32_16x16x32_bf16 v[132:135], v[84:87], v[192:195], v[132:135]\n\t" \
  "v_mfma_f32_16x16x32_bf16 v[136:139], v[80:83], v[196:199], v[136:139]\n\t" \
  "v_mfma_f32_16x16x32_bf16 v[140:143], v[84:87], v[196:199], v[140:143]\n\t" \
  "v_mfma_f32_16x16x32_bf16 v[128:131], v[88:91], v[200:203], v[128:131]\n\t" \
  "v_mfma_f32_16x16x32_bf16 v[132:135], v[92:95], v[200:203], v[132:135]\n\t" \
  "v_mfma_f32_16x16x32_bf16 v[136:139], v[88:91], v[204:207], v[136:139]\n\t" \
  "v_mfma_f32_16x16x32_bf16 v[140:143], v[92:95], v[204:207], v[140:143]\n\t" \
  "v_mfma_f32_16x16x32_bf16 v[128:131], v[96:99], v[208:211], v[128:131]\n\t" \
  "v_mfma_f32_16x16x32_bf16 v[132:135], v[100:103], v[208:211], v[132:135]\n\t" \
  "v_mfma_f32_16x16x32_bf16 v[136:139], v[96:99], v[212:215], v[136:139]\n\t" \
  "v_mfma_f32_16x16x32_bf16 v[140:143], v[100:103], v[212:215], v[140:143]\n\t" \
  "v_mfma_f32_16x16x32_bf16 v[128:131], v[104:107], v[216:219], v[128:131]\n\t" \
  "v_mfma_f32_16x16x32_bf16 v[132:135], v[108:111], v[216:219], v[132:135]\n\t" \
  "v_mfma_f32_16x16x32_bf16 v[136:139], v[104:107], v[220:223], v[136:139]\n\t" \
  "v_mfma_f32_16x16x32_bf16 v[140:143], v[108:111], v[220:223], v[140:143]\n\t" \
  "v_mfma_f32_16x16x32_bf16 v[128:131], v[112:115], v[224:227], v[128:131]\n\t" \
  "v_mfma_f32_16x16x32_bf16 v[132:135], v[116:119], v[224:227], v[132:135]\n\t" \
  "v_mfma_f32_16x16x32_bf16 v[136:139], v[112:115], v[228:231], v[136:139]\n\t" \
  "v_mfma_f32_16x16x32_bf16 v[140:143], v[116:119], v[228:231], v[140:143]\n\t" \
  "v_mfma_f32_16x16x32_bf16 v[128:131], v[120:123], v[232:235], v[128:131]\n\t" \
  "v_mfma_f32_16x16x32_bf16 v[132:135], v[124:127], v[232:235], v[132:135]\n\t" \
  "v_mfma_f32_16x16x32_bf16 v[136:139], v[120:123], v[236:239], v[136:139]\n\t" \
  "v_mfma_f32_16x16x32_bf16 v[140:143], v[124:127], v[236:239], v[140:143]\n\t" \
  "s_setprio 0\n\t" \
  "s_waitcnt vmcnt(9)\n\t" \
  "s_barrier\n\t" \
  "s_add_u32 s92, s92, 512\n\t" \
  "s_addc_u32 s93, s93, 0\n\t" \
  "ds_read_b128 v[176:179], v244 offset:0\n\t" \
  "ds_read_b128 v[180:183], v244 offset:16384\n\t" \
  "ds_read_b128 v[184:187], v245 offset:0\n\t" \
  "ds_read_b128 v[188:191], v245 offset:16384\n\t" \
  "s_add_u32 m0, %[lw], 32768\n\t" \
  "s_nop 0\n\t" \
  "global_load_lds_dwordx4 v248, s[92:93] sc1\n\t" \
  "ds_read_b128 v[192:195], v246 offset:0\n\t" \
  "ds_read_b128 v[196:199], v246 offset:16384\n\t" \
  "ds_read_b128 v[200:203], v247 offset:0\n\t" \
  "ds_read_b128 v[204:207], v247 offset:16384\n\t" \
  "s_add_u32 m0, %[lw], 33792\n\t" \
  "s_nop 0\n\t" \
  "global_load_lds_dwordx4 v249, s[92:93] sc1\n\t" \
  "ds_read_b128 v[208:211], v244 offset:256\n\t" \
  "ds_read_b128 v[212:215], v244 offset:16640\n\t" \
  "ds_read_b128 v[216:219], v245 offset:256\n\t" \
  "ds_read_b128 v[220:223], v245 offset:16640\n\t" \
  "s_add_u32 m0, %[lw], 34816\n\t" \
  "s_nop 0\n\t" \
  "global_load_lds_dwordx4 v250, s[92:93] sc1\n\t" \
  "ds_read_b128 v[224:227], v246 offset:256\n\t" \
  "ds_read_b128 v[228:231], v246 offset:16640\n\t" \
  "ds_read_b128 v[232:235], v247 offset:256\n\t" \
  "ds_read_b128 v[236:239], v247 offset:16640\n\t" \
  "s_add_u32 m0, %[lw], 35840\n\t" \
  "s_nop 0\n\t" \
  "global_load_lds_dwordx4 v251, s[92:93] sc1\n\t" \
  "ds_write_b128 v252, v[136:139] offset:0\n\t" \
  "ds_write_b128 v252, v[140:143] offset:1024\n\t" \
  "s_waitcnt lgkmcnt(0)\n\t" \
  "s_setprio 1\n\t" \
  "v_mfma_f32_16x16x32_bf16 v[144:147], v[0:3], v[176:179], 0\n\t" \
  "v_mfma_f32_16x16x32_bf16 v[148:151], v[4:7], v[176:179], 0\n\t" \
  "v_mfma_f32_16x16x32_bf16 v[152:155], v[0:3], v[180:183], 0\n\t" \
  "v_mfma_f32_16x16x32_bf16 v[156:159], v[4:7], v[180:183], 0\n\t" \
  "v_mfma_f32_16x16x32_bf16 v[144:147], v[8:11], v[184:187], v[144:147]\n\t" \
  "v_mfma_f32_16x16x32_bf16 v[148:151], v[12:15], v[184:187], v[148:151]\n\t" \
  "v_mfma_f32_16x16x32_bf16 v[152:155], v[8:11], v[188:191], v[152:155]\n\t" \
  "v_mfma_f32_16x16x32_bf16 v[156:159], v[12:15], v[188:191], v[156:159]\n\t" \
  "v_mfma_f32_16x16x32_bf16 v[144:147], v[16:19], v[192:195], v[144:147]\n\t" \
  "v_mfma_f32_16x16x32_bf16 v[148:151], v[20:23], v[192:195], v[148:151]\n\t" \
  "v_mfma_f32_16x16x32_bf16 v[152:155], v[16:19], v[196:199], v[152:155]\n\t" \
  "v_mfma_f32_16x16x32_bf16 v[156:159], v[20:23], v[196:199], v[156:159]\n\t" \
  "v_mfma_f32_16x16x32_bf16 v[144:147], v[24:27], v[200:203], v[144:147]\n\t" \
  "v_mfma_f32_16x16x32_bf16 v[148:151], v[28:31], v[200:203], v[148:151]\n\t" \
  "v_mfma_f32_16x16x32_bf16 v[152:155], v[24:27], v[204:207], v[152:155]\n\t" \
  "v_mfma_f32_16x16x32_bf16 v[156:159], v[28:31], v[204:207], v[156:159]\n\t" \
  "v_mfma_f32_16x16x32_bf16 v[144:147], v[32:35], v[208:211], v[144:147]\n\t" \
  "v_mfma_f32_16x16x32_bf16 v[148:151], v[36:39], v[208:211], v[148:151]\n\t" \
  "v_mfma_f32_16x16x32_bf16 v[152:155], v[32:35], v[212:215], v[152:155]\n\t" \
  "v_mfma_f32_16x16x32_bf16 v[156:159], v[36:39], v[212:215], v[156:159]\n\t" \
  "v_mfma_f32_16x16x32_bf16 v[144:147], v[40:43], v[216:219], v[144:147]\n\t" \
  "v_mfma_f32_16x16x32_bf16 v[148:151], v[44:47], v[216:219], v[148:151]\n\t" \
  "v_mfma_f32_16x16x32_bf16 v[152:155], v[40:43], v[220:223], v[152:155]\n\t" \
  "v_mfma_f32_16x16x32_bf16 v[156:159], v[44:47], v[220:223], v[156:159]\n\t" \
  "v_mfma_f32_16x16x32_bf16 v[144:147], v[48:51], v[224:227], v[144:147]\n\t" \
  "v_mfma_f32_16x16x32_bf16 v[148:151], v[52:55], v[224:227], v[148:151]\n\t" \
  "v_mfma_f32_16x16x32_bf16 v[152:155], v[48:51], v[228:231], v[152:155]\n\t" \
  "v_mfma_f32_16x16x32_bf16 v[156:159], v[52:55], v[228:231], v[156:159]\n\t" \
  "v_mfma_f32_16x16x32_bf16 v[144:147], v[56:59], v[232:235], v[144:147]\n\t" \
  "v_mfma_f32_16x16x32_bf16 v[148:151], v[60:63], v[232:235], v[148:151]\n\t" \
  "v_mfma_f32_16x16x32_bf16 v[152:155], v[56:59], v[236:239], v[152:155]\n\t" \
  "v_mfma_f32_16x16x32_bf16 v[156:159], v[60:63], v[236:239], v[156:159]\n\t" \
  "s_setprio 0\n\t" \
  "s_waitcnt vmcnt(4)\n\t" \
  "s_barrier\n\t" \
  "s_add_u32 s92, s92, 65024\n\t" \
  "s_addc_u32 s93, s93, 0\n\t" \
  "ds_read_b128 v[160:163], v253 offset:0\n\t" \
  "ds_read_b128 v[164:167], v253 offset:1024\n\t" \
  "ds_read_b128 v[176:179], v244 offset:32768\n\t" \
  "ds_read_b128 v[180:183], v244 offset:49152\n\t" \
  "ds_read_b128 v[184:187], v245 offset:32768\n\t" \
  "ds_read_b128 v[188:191], v245 offset:49152\n\t" \
  "s_add_u32 m0, %[lw], 65536\n\t" \
  "s_nop 0\n\t" \
  "global_load_lds_dwordx4 v248, s[92:93] sc1\n\t" \
  "ds_read_b128 v[192:195], v246 offset:32768\n\t" \
  "ds_read_b128 v[196:199], v246 offset:49152\n\t" \
  "ds_read_b128 v[200:203], v247 offset:32768\n\t" \
  "ds_read_b128 v[204:207], v247 offset:49152\n\t" \
  "s_add_u32 m0, %[lw], 66560\n\t" \
  "s_nop 0\n\t" \
  "global_load_lds_dwordx4 v249, s[92:93] sc1\n\t" \
  "ds_read_b128 v[208:211], v244 offset:33024\n\t" \
  "ds_read_b128 v[212:215], v244 offset:49408\n\t" \
  "ds_read_b128 v[216:219], v245 offset:33024\n\t" \
  "ds_read_b128 v[220:223], v245 offset:49408\n\t" \
  "s_add_u32 m0, %[lw], 67584\n\t" \
  "s_nop 0\n\t" \
  "global_load_lds_dwordx4 v250, s[92:93] sc1\n\t" \
  "ds_read_b128 v[224:227], v246 offset:33024\n\t" \
  "ds_read_b128 v[228:231], v246 offset:49408\n\t" \
  "ds_read_b128 v[232:235], v247 offset:33024\n\t" \
  "ds_read_b128 v[236:239], v247 offset:49408\n\t" \
  "s_add_u32 m0, %[lw], 68608\n\t" \
  "s_nop 0\n\t" \
  "global_load_lds_dwordx4 v251, s[92:93] sc1\n\t" \
  "s_waitcnt lgkmcnt(15)\n\t" \
  "s_waitcnt lgkmcnt(12)\n\t" \
  "s_lshr_b32 s90, s87, 7\n\t" \
  "s_xor_b32 s90, s90, %[s0]\n\t" \
  "s_and_b32 s90, s90, 1\n\t" \
  "s_lshl1_add_u32 s90, s87, s90\n\t" \
  "s_lshl_b32 s90, s90, 13\n\t" \
  "v_add_f32 v160, v160, v128\n\t" \
  "v_add_f32 v161, v161, v129\n\t" \
  "v_add_f32 v162, v162, v130\n\t" \
  "v_add_f32 v163, v163, v131\n\t" \
  "v_add_f32 v164, v164, v132\n\t" \
  "v_add_f32 v165, v165, v133\n\t" \
  "v_add_f32 v166, v166, v134\n\t" \
  "v_add_f32 v167, v167, v135\n\t" \
  "v_cvt_pk_bf16_f32 v168, v160, v161\n\t" \
  "v_cvt_pk_bf16_f32 v169, v162, v163\n\t" \
  "v_cvt_pk_bf16_f32 v170, v164, v165\n\t" \
  "v_cvt_pk_bf16_f32 v171, v166, v167\n\t" \
  "buffer_store_dwordx4 v[168:171], v172, %[rt], s90 offen sc1\n\t" \
  "s_add_u32 s86, s86, 1\n\t" \
  "s_add_u32 s87, s87, 1\n\t" \
  "s_waitcnt lgkmcnt(0)\n\t" \
  "s_setprio 1\n\t" \
  "v_mfma_f32_16x16x32_bf16 v[144:147], v[64:67], v[176:179], v[144:147]\n\t" \
  "v_mfma_f32_16x16x32_bf16 v[148:151], v[68:71], v[176:179], v[148:151]\n\t" \
  "v_mfma_f32_16x16x32_bf16 v[152:155], v[64:67], v[180:183], v[152:155]\n\t" \
  "v_mfma_f32_16x16x32_bf16 v[156:159], v[68:71], v[180:183], v[156:159]\n\t" \
  "v_mfma_f32_16x16x32_bf16 v[144:147], v[72:75], v[184:187], v[144:147]\n\t" \
  "v_mfma_f32_16x16x32_bf16 v[148:151], v[76:79], v[184:187], v[148:151]\n\t" \
  "v_mfma_f32_16x16x32_bf16 v[152:155], v[72:75], v[188:191], v[152:155]\n\t" \
  "v_mfma_f32_16x16x32_bf16 v[156:159], v[76:79], v[188:191], v[156:159]\n\t" \
  "v_mfma_f32_16x16x32_bf16 v[144:147], v[80:83], v[192:195], v[144:147]\n\t" \
  "v_mfma_f32_16x16x32_bf16 v[148:151], v[84:87], v[192:195], v[148:151]\n\t" \
  "v_mfma_f32_16x16x32_bf16 v[152:155], v[80:83], v[196:199], v[152:155]\n\t" \
  "v_mfma_f32_16x16x32_bf16 v[156:159], v[84:87], v[196:199], v[156:159]\n\t" \
  "v_mfma_f32_16x16x32_bf16 v[144:147], v[88:91], v[200:203], v[144:147]\n\t" \
  "v_mfma_f32_16x16x32_bf16 v[148:151], v[92:95], v[200:203], v[148:151]\n\t" \
  "v_mfma_f32_16x16x32_bf16 v[152:155], v[88:91], v[204:207], v[152:155]\n\t" \
  "v_mfma_f32_16x16x32_bf16 v[156:159], v[92:95], v[204:207], v[156:159]\n\t" \
  "v_mfma_f32_16x16x32_bf16 v[144:147], v[96:99], v[208:211], v[144:147]\n\t" \
  "v_mfma_f32_16x16x32_bf16 v[148:151], v[100:103], v[208:211], v[148:151]\n\t" \
  "v_mfma_f32_16x16x32_bf16 v[152:155], v[96:99], v[212:215], v[152:155]\n\t" \
  "v_mfma_f32_16x16x32_bf16 v[156:159], v[100:103], v[212:215], v[156:159]\n\t" \
  "v_mfma_f32_16x16x32_bf16 v[144:147], v[104:107], v[216:219], v[144:147]\n\t" \
  "v_mfma_f32_16x16x32_bf16 v[148:151], v[108:111], v[216:219], v[148:151]\n\t" \
  "v_mfma_f32_16x16x32_bf16 v[152:155], v[104:107], v[220:223], v[152:155]\n\t" \
  "v_mfma_f32_16x16x32_bf16 v[156:159], v[108:111], v[220:223], v[156:159]\n\t" \
  "v_mfma_f32_16x16x32_bf16 v[144:147], v[112:115], v[224:227], v[144:147]\n\t" \
  "v_mfma_f32_16x16x32_bf16 v[148:151], v[116:119], v[224:227], v[148:151]\n\t" \
  "v_mfma_f32_16x16x32_bf16 v[152:155], v[112:115], v[228:231], v[152:155]\n\t" \
  "v_mfma_f32_16x16x32_bf16 v[156:159], v[116:119], v[228:231], v[156:159]\n\t" \
  "v_mfma_f32_16x16x32_bf16 v[144:147], v[120:123], v[232:235], v[144:147]\n\t" \
  "v_mfma_f32_16x16x32_bf16 v[148:151], v[124:127], v[232:235], v[148:151]\n\t" \
  "v_mfma_f32_16x16x32_bf16 v[152:155], v[120:123], v[236:239], v[152:155]\n\t" \
  "v_mfma_f32_16x16x32_bf16 v[156:159], v[124:127], v[236:239], v[156:159]\n\t" \
  "s_setprio 0\n\t" \
  "s_cmp_lt_u32 s86, 64\n\t" \
  "s_cbranch_scc1 .Lp0%=\n\t" \
  "s_barrier\n\t" \
  "s_nop 2\n\t" \
  "ds_write_b128 v252, v[152:155] offset:0\n\t" \
  "ds_write_b128 v252, v[156:159] offset:1024\n\t" \
  "s_waitcnt lgkmcnt(0)\n\t" \
  "s_barrier\n\t" \
  "ds_read_b128 v[160:163], v253 offset:0\n\t" \
  "ds_read_b128 v[164:167], v253 offset:1024\n\t" \
  "s_waitcnt lgkmcnt(0)\n\t" \
  "s_lshr_b32 s90, s87, 7\n\t" \
  "s_xor_b32 s90, s90, %[s0]\n\t" \
  "s_and_b32 s90, s90, 1\n\t" \
  "s_lshl1_add_u32 s90, s87, s90\n\t" \
  "s_lshl_b32 s90, s90, 13\n\t" \
  "v_add_f32 v160, v160, v144\n\t" \
  "v_add_f32 v161, v161, v145\n\t" \
  "v_add_f32 v162, v162, v146\n\t" \
  "v_add_f32 v163, v163, v147\n\t" \
  "v_add_f32 v164, v164, v148\n\t" \
  "v_add_f32 v165, v165, v149\n\t" \
  "v_add_f32 v166, v166, v150\n\t" \
  "v_add_f32 v167, v167, v151\n\t" \
  "v_cvt_pk_bf16_f32 v168, v160, v161\n\t" \
  "v_cvt_pk_bf16_f32 v169, v162, v163\n\t" \
  "v_cvt_pk_bf16_f32 v170, v164, v165\n\t" \
  "v_cvt_pk_bf16_f32 v171, v166, v167\n\t" \
  "buffer_store_dwordx4 v[168:171], v172, %[rt], s90 offen sc1\n\t" \
  "s_waitcnt vmcnt(0)\n\t" \
  "s_barrier\n\t" \
  "s_mov_b32 m0, s94\n\t" \
  "s_branch .Le%=\n\t" \
  ".Lk%=:\n\t" \
  "s_mov_b32 s94, m0\n\t" \
  "s_mov_b64 s[92:93], %[x0]\n\t" \
  "s_mov_b32 s86, %[g0]\n\t" \
  "s_add_u32 s87, s86, %[t0]\n\t" \
  "s_add_u32 m0, %[lw], 67584\n\t" \
  "s_nop 0\n\t" \
  "global_load_lds_dwordx4 v250, s[92:93] sc1\n\t" \
  "s_add_u32 m0, %[lw], 68608\n\t" \
  "s_nop 0\n\t" \
  "global_load_lds_dwordx4 v251, s[92:93] sc1\n\t" \
  "s_barrier\n\t" \
  "s_branch .Ls1%=\n\t" \
  ".Lp1%=:\n\t" \
  "s_waitcnt vmcnt(10)\n\t" \
  "s_barrier\n\t" \
  "s_setprio 2\n\t" \
  "v_mfma_f32_16x16x32_bf16 v[144:147], v[64:67], v[176:179], v[144:147]\n\t" \
  "v_mfma_f32_16x16x32_bf16 v[148:151], v[68:71], v[176:179], v[148:151]\n\t" \
  "v_mfma_f32_16x16x32_bf16 v[152:155], v[64:67], v[180:183], v[152:155]\n\t" \
  "v_mfma_f32_16x16x32_bf16 v[156:159], v[68:71], v[180:183], v[156:159]\n\t" \
  "v_mfma_f32_16x16x32_bf16 v[144:147], v[72:75], v[184:187], v[144:147]\n\t" \
  "v_mfma_f32_16x16x32_bf16 v[148:151], v[76:79], v[184:187], v[148:151]\n\t" \
  "v_mfma_f32_16x16x32_bf16 v[152:155], v[72:75], v[188:191], v[152:155]\n\t" \
  "v_mfma_f32_16x16x32_bf16 v[156:159], v[76:79], v[188:191], v[156:159]\n\t" \
  "v_mfma_f32_16x16x32_bf16 v[144:147], v[80:83], v[192:195], v[144:147]\n\t" \
  "v_mfma_f32_16x16x32_bf16 v[148:151], v[84:87], v[192:195], v[148:151]\n\t" \
  "v_mfma_f32_16x16x32_bf16 v[152:155], v[80:83], v[196:199], v[152:155]\n\t" \
  "v_mfma_f32_16x16x32_bf16 v[156:159], v[84:87], v[196:199], v[156:159]\n\t" \
  "v_mfma_f32_16x16x32_bf16 v[144:147], v[88:91], v[200:203], v[144:147]\n\t" \
  "v_mfma_f32_16x16x32_bf16 v[148:151], v[92:95], v[200:203], v[148:151]\n\t" \
  "v_mfma_f32_16x16x32_bf16 v[152:155], v[88:91], v[204:207], v[152:155]\n\t" \
  "v_mfma_f32_16x16x32_bf16 v[156:159], v[92:95], v[204:207], v[156:159]\n\t" \
  "v_mfma_f32_16x16x32_bf16 v[144:147], v[96:99], v[208:211], v[144:147]\n\t" \
  "v_mfma_f32_16x16x32_bf16 v[148:151], v[100:103], v[208:211], v[148:151]\n\t" \
  "v_mfma_f32_16x16x32_bf16 v[152:155], v[96:99], v[212:215], v[152:155]\n\t" \
  "v_mfma_f32_16x16x32_bf16 v[156:159], v[100:103], v[212:215], v[156:159]\n\t" \
  "v_mfma_f32_16x16x32_bf16 v[144:147], v[104:107], v[216:219], v[144:147]\n\t" \
  "v_mfma_f32_16x16x32_bf16 v[148:151], v[108:111], v[216:219], v[148:151]\n\t" \
  "v_mfma_f32_16x16x32_bf16 v[152:155], v[104:107], v[220:223], v[152:155]\n\t" \
  "v_mfma_f32_16x16x32_bf16 v[156:159], v[108:111], v[220:223], v[156:159]\n\t" \
  "v_mfma_f32_16x16x32_bf16 v[144:147], v[112:115], v[224:227], v[144:147]\n\t" \
  "v_mfma_f32_16x16x32_bf16 v[148:151], v[116:119], v[224:227], v[148:151]\n\t" \
  "v_mfma_f32_16x16x32_bf16 v[152:155], v[112:115], v[228:231], v[152:155]\n\t" \
  "v_mfma_f32_16x16x32_bf16 v[156:159], v[116:119], v[228:231], v[156:159]\n\t" \
  "v_mfma_f32_16x16x32_bf16 v[144:147], v[120:123], v[232:235], v[144:147]\n\t" \
  "v_mfma_f32_16x16x32_bf16 v[148:151], v[124:127], v[232:235], v[148:151]\n\t" \
  "v_mfma_f32_16x16x32_bf16 v[152:155], v[120:123], v[236:239], v[152:155]\n\t" \
  "v_mfma_f32_16x16x32_bf16 v[156:159], v[124:127], v[236:239], v[156:159]\n\t" \
  "s_setprio 0\n\t" \
  ".Ls1%=:\n\t" \
  "s_add_u32 s92, s92, 512\n\t" \
  "s_addc_u32 s93, s93, 0\n\t" \
  "ds_read_b128 v[176:179], v240 offset:0\n\t" \
  "ds_read_b128 v[180:183], v240 offset:16384\n\t" \
  "ds_read_b128 v[184:187], v241 offset:0\n\t" \
  "ds_read_b128 v[188:191], v241 offset:16384\n\t" \
  "s_add_u32 m0, %[lw], 98304\n\t" \
  "s_nop 0\n\t" \
  "global_load_lds_dwordx4 v248, s[92:93] sc1\n\t" \
  "ds_read_b128 v[192:195], v242 offset:0\n\t" \
  "ds_read_b128 v[196:199], v242 offset:16384\n\t" \
  "ds_read_b128 v[200:203], v243 offset:0\n\t" \
  "ds_read_b128 v[204:207], v243 offset:16384\n\t" \
  "s_add_u32 m0, %[lw], 99328\n\t" \
  "s_nop 0\n\t" \
  "global_load_lds_dwordx4 v249, s[92:93] sc1\n\t" \
  "ds_read_b128 v[208:211], v240 offset:256\n\t" \
  "ds_read_b128 v[212:215], v240 offset:16640\n\t" \
  "ds_read_b128 v[216:219], v241 offset:256\n\t" \
  "ds_read_b128 v[220:223], v241 offset:16640\n\t" \
  "s_add_u32 m0, %[lw], 100352\n\t" \
  "s_nop 0\n\t" \
  "global_load_lds_dwordx4 v250, s[92:93] sc1\n\t" \
  "ds_read_b128 v[224:227], v242 offset:256\n\t" \
  "ds_read_b128 v[228:231], v242 offset:16640\n\t" \
  "ds_read_b128 v[232:235], v243 offset:256\n\t" \
  "ds_read_b128 v[236:239], v243 offset:16640\n\t" \
  "s_add_u32 m0, %[lw], 101376\n\t" \
  "s_nop 0\n\t" \
  "global_load_lds_dwordx4 v251, s[92:93] sc1\n\t" \
  "ds_write_b128 v252, v[152:155] offset:0\n\t" \
  "ds_write_b128 v252, v[156:159] offset:1024\n\t" \
  "s_waitcnt lgkmcnt(0)\n\t" \
  "s_waitcnt vmcnt(4)\n\t" \
  "s_barrier\n\t" \
  "s_setprio 2\n\t" \
  "v_mfma_f32_16x16x32_bf16 v[128:131], v[0:3], v[176:179], 0\n\t" \
  "v_mfma_f32_16x16x32_bf16 v[132:135], v[4:7], v[176:179], 0\n\t" \
  "v_mfma_f32_16x16x32_bf16 v[136:139], v[0:3], v[180:183], 0\n\t" \
  "v_mfma_f32_16x16x32_bf16 v[140:143], v[4:7], v[180:183], 0\n\t" \
  "v_mfma_f32_16x16x32_bf16 v[128:131], v[8:11], v[184:187], v[128:131]\n\t" \
  "v_mfma_f32_16x16x32_bf16 v[132:135], v[12:15], v[184:187], v[132:135]\n\t" \
  "v_mfma_f32_16x16x32_bf16 v[136:139], v[8:11], v[188:191], v[136:139]\n\t" \
  "v_mfma_f32_16x16x32_bf16 v[140:143], v[12:15], v[188:191], v[140:143]\n\t" \
  "v_mfma_f32_16x16x32_bf16 v[128:131], v[16:19], v[192:195], v[128:131]\n\t" \
  "v_mfma_f32_16x16x32_bf16 v[132:135], v[20:23], v[192:195], v[132:135]\n\t" \
  "v_mfma_f32_16x16x32_bf16 v[136:139], v[16:19], v[196:199], v[136:139]\n\t" \
  "v_mfma_f32_16x16x32_bf16 v[140:143], v[20:23], v[196:199], v[140:143]\n\t" \
  "v_mfma_f32_16x16x32_bf16 v[128:131], v[24:27], v[200:203], v[128:131]\n\t" \
  "v_mfma_f32_16x16x32_bf16 v[132:135], v[28:31], v[200:203], v[132:135]\n\t" \
  "v_mfma_f32_16x16x32_bf16 v[136:139], v[24:27], v[204:207], v[136:139]\n\t" \
  "v_mfma_f32_16x16x32_bf16 v[140:143], v[28:31], v[204:207], v[140:143]\n\t" \
  "v_mfma_f32_16x16x32_bf16 v[128:131], v[32:35], v[208:211], v[128:131]\n\t" \
  "v_mfma_f32_16x16x32_bf16 v[132:135], v[36:39], v[208:211], v[132:135]\n\t" \
  "v_mfma_f32_16x16x32_bf16 v[136:139], v[32:35], v[212:215], v[136:139]\n\t" \
  "v_mfma_f32_16x16x32_bf16 v[140:143], v[36:39], v[212:215], v[140:143]\n\t" \
  "v_mfma_f32_16x16x32_bf16 v[128:131], v[40:43], v[216:219], v[128:131]\n\t" \
  "v_mfma_f32_16x16x32_bf16 v[132:135], v[44:47], v[216:219], v[132:135]\n\t" \
  "v_mfma_f32_16x16x32_bf16 v[136:139], v[40:43], v[220:223], v[136:139]\n\t" \
  "v_mfma_f32_16x16x32_bf16 v[140:143], v[44:47], v[220:223], v[140:143]\n\t" \
  "v_mfma_f32_16x16x32_bf16 v[128:131], v[48:51], v[224:227], v[128:131]\n\t" \
  "v_mfma_f32_16x16x32_bf16 v[132:135], v[52:55], v[224:227], v[132:135]\n\t" \
  "v_mfma_f32_16x16x32_bf16 v[136:139], v[48:51], v[228:231], v[136:139]\n\t" \
  "v_mfma_f32_16x16x32_bf16 v[140:143], v[52:55], v[228:231], v[140:143]\n\t" \
  "v_mfma_f32_16x16x32_bf16 v[128:131], v[56:59], v[232:235], v[128:131]\n\t" \
  "v_mfma_f32_16x16x32_bf16 v[132:135], v[60:63], v[232:235], v[132:135]\n\t" \
  "v_mfma_f32_16x16x32_bf16 v[136:139], v[56:59], v[236:239], v[136:139]\n\t" \
  "v_mfma_f32_16x16x32_bf16 v[140:143], v[60:63], v[236:239], v[140:143]\n\t" \
  "s_setprio 0\n\t" \
  "s_add_u32 s92, s92, 65024\n\t" \
  "s_addc_u32 s93, s93, 0\n\t" \
  "ds_read_b128 v[160:163], v253 offset:0\n\t" \
  "ds_read_b128 v[164:167], v253 offset:1024\n\t" \
  "ds_read_b128 v[176:179], v240 offset:32768\n\t" \
  "ds_read_b128 v[180:183], v240 offset:49152\n\t" \
  "ds_read_b128 v[184:187], v241 offset:32768\n\t" \
  "ds_read_b128 v[188:191], v241 offset:49152\n\t" \
  "s_add_u32 m0, %[lw], 0\n\t" \
  "s_nop 0\n\t" \
  "global_load_lds_dwordx4 v248, s[92:93] sc1\n\t" \
  "ds_read_b128 v[192:195], v242 offset:32768\n\t" \
  "ds_read_b128 v[196:199], v242 offset:49152\n\t" \
  "ds_read_b128 v[200:203], v243 offset:32768\n\t" \
  "ds_read_b128 v[204:207], v243 offset:49152\n\t" \
  "s_add_u32 m0, %[lw], 1024\n\t" \
  "s_nop 0\n\t" \
  "global_load_lds_dwordx4 v249, s[92:93] sc1\n\t" \
  "ds_read_b128 v[208:211], v240 offset:33024\n\t" \
  "ds_read_b128 v[212:215], v240 offset:49408\n\t" \
  "ds_read_b128 v[216:219], v241 offset:33024\n\t" \
  "ds_read_b128 v[220:223], v241 offset:49408\n\t" \
  "s_add_u32 m0, %[lw], 2048\n\t" \
  "s_nop 0\n\t" \
  "global_load_lds_dwordx4 v250, s[92:93] sc1\n\t" \
  "ds_read_b128 v[224:227], v242 offset:33024\n\t" \
  "ds_read_b128 v[228:231], v242 offset:49408\n\t" \
  "ds_read_b128 v[232:235], v243 offset:33024\n\t" \
  "ds_read_b128 v[236:239], v243 offset:49408\n\t" \
  "s_add_u32 m0, %[lw], 3072\n\t" \
  "s_nop 0\n\t" \
  "global_load_lds_dwordx4 v251, s[92:93] sc1\n\t" \
  "s_waitcnt lgkmcnt(15)\n\t" \
  "s_waitcnt lgkmcnt(12)\n\t" \
  "s_sub_i32 s91, s86, 2\n\t" \
  "v_mov_b32 v173, s91\n\t" \
  "buffer_store_dword v173, v174, %[rs], 0 offen sc1\n\t" \
  "s_lshr_b32 s90, s87, 7\n\t" \
  "s_xor_b32 s90, s90, %[s0]\n\t" \
  "s_and_b32 s90, s90, 1\n\t" \
  "s_lshl1_add_u32 s90, s87, s90\n\t" \
  "s_lshl_b32 s90, s90, 13\n\t" \
  "v_add_f32 v160, v160, v144\n\t" \
  "v_add_f32 v161, v161, v145\n\t" \
  "v_add_f32 v162, v162, v146\n\t" \
  "v_add_f32 v163, v163, v147\n\t" \
  "v_add_f32 v164, v164, v148\n\t" \
  "v_add_f32 v165, v165, v149\n\t" \
  "v_add_f32 v166, v166, v150\n\t" \
  "v_add_f32 v167, v167, v151\n\t" \
  "v_cvt_pk_bf16_f32 v168, v160, v161\n\t" \
  "v_cvt_pk_bf16_f32 v169, v162, v163\n\t" \
  "v_cvt_pk_bf16_f32 v170, v164, v165\n\t" \
  "v_cvt_pk_bf16_f32 v171, v166, v167\n\t" \
  "buffer_store_dwordx4 v[168:171], v172, %[rt], s90 offen sc1\n\t" \
  "s_add_u32 s86, s86, 1\n\t" \
  "s_add_u32 s87, s87, 1\n\t" \
  "s_waitcnt lgkmcnt(0)\n\t" \
  "s_waitcnt vmcnt(10)\n\t" \
  "s_barrier\n\t" \
  "s_setprio 2\n\t" \
  "v_mfma_f32_16x16x32_bf16 v[128:131], v[64:67], v[176:179], v[128:131]\n\t" \
  "v_mfma_f32_16x16x32_bf16 v[132:135], v[68:71], v[176:179], v[132:135]\n\t" \
  "v_mfma_f32_16x16x32_bf16 v[136:139], v[64:67], v[180:183], v[136:139]\n\t" \
  "v_mfma_f32_16x16x32_bf16 v[140:143], v[68:71], v[180:183], v[140:143]\n\t" \
  "v_mfma_f32_16x16x32_bf16 v[128:131], v[72:75], v[184:187], v[128:131]\n\t" \
  "v_mfma_f32_16x16x32_bf16 v[132:135], v[76:79], v[184:187], v[132:135]\n\t" \
  "v_mfma_f32_16x16x32_bf16 v[136:139], v[72:75], v[188:191], v[136:139]\n\t" \
  "v_mfma_f32_16x16x32_bf16 v[140:143], v[76:79], v[188:191], v[140:143]\n\t" \
  "v_mfma_f32_16x16x32_bf16 v[128:131], v[80:83], v[192:195], v[128:131]\n\t" \
  "v_mfma_f32_16x16x32_bf16 v[132:135], v[84:87], v[192:195], v[132:135]\n\t" \
  "v_mfma_f32_16x16x32_bf16 v[136:139], v[80:83], v[196:199], v[136:139]\n\t" \
  "v_mfma_f32_16x16x32_bf16 v[140:143], v[84:87], v[196:199], v[140:143]\n\t" \
  "v_mfma_f32_16x16x32_bf16 v[128:131], v[88:91], v[200:203], v[128:131]\n\t" \
  "v_mfma_f32_16x16x32_bf16 v[132:135], v[92:95], v[200:203], v[132:135]\n\t" \
  "v_mfma_f32_16x16x32_bf16 v[136:139], v[88:91], v[204:207], v[136:139]\n\t" \
  "v_mfma_f32_16x16x32_bf16 v[140:143], v[92:95], v[204:207], v[140:143]\n\t" \
  "v_mfma_f32_16x16x32_bf16 v[128:131], v[96:99], v[208:211], v[128:131]\n\t" \
  "v_mfma_f32_16x16x32_bf16 v[132:135], v[100:103], v[208:211], v[132:135]\n\t" \
  "v_mfma_f32_16x16x32_bf16 v[136:139], v[96:99], v[212:215], v[136:139]\n\t" \
  "v_mfma_f32_16x16x32_bf16 v[140:143], v[100:103], v[212:215], v[140:143]\n\t" \
  "v_mfma_f32_16x16x32_bf16 v[128:131], v[104:107], v[216:219], v[128:131]\n\t" \
  "v_mfma_f32_16x16x32_bf16 v[132:135], v[108:111], v[216:219], v[132:135]\n\t" \
  "v_mfma_f32_16x16x32_bf16 v[136:139], v[104:107], v[220:223], v[136:139]\n\t" \
  "v_mfma_f32_16x16x32_bf16 v[140:143], v[108:111], v[220:223], v[140:143]\n\t" \
  "v_mfma_f32_16x16x32_bf16 v[128:131], v[112:115], v[224:227], v[128:131]\n\t" \
  "v_mfma_f32_16x16x32_bf16 v[132:135], v[116:119], v[224:227], v[132:135]\n\t" \
  "v_mfma_f32_16x16x32_bf16 v[136:139], v[112:115], v[228:231], v[136:139]\n\t" \
  "v_mfma_f32_16x16x32_bf16 v[140:143], v[116:119], v[228:231], v[140:143]\n\t" \
  "v_mfma_f32_16x16x32_bf16 v[128:131], v[120:123], v[232:235], v[128:131]\n\t" \
  "v_mfma_f32_16x16x32_bf16 v[132:135], v[124:127], v[232:235], v[132:135]\n\t" \
  "v_mfma_f32_16x16x32_bf16 v[136:139], v[120:123], v[236:239], v[136:139]\n\t" \
  "v_mfma_f32_16x16x32_bf16 v[140:143], v[124:127], v[236:239], v[140:143]\n\t" \
  "s_setprio 0\n\t" \
  "s_add_u32 s92, s92, 512\n\t" \
  "s_addc_u32 s93, s93, 0\n\t" \
  "ds_read_b128 v[176:179], v244 offset:0\n\t" \
  "ds_read_b128 v[180:183], v244 offset:16384\n\t" \
  "ds_read_b128 v[184:187], v245 offset:0\n\t" \
  "ds_read_b128 v[188:191], v245 offset:16384\n\t" \
  "s_add_u32 m0, %[lw], 32768\n\t" \
  "s_nop 0\n\t" \
  "global_load_lds_dwordx4 v248, s[92:93] sc1\n\t" \
  "ds_read_b128 v[192:195], v246 offset:0\n\t" \
  "ds_read_b128 v[196:199], v246 offset:16384\n\t" \
  "ds_read_b128 v[200:203], v247 offset:0\n\t" \
  "ds_read_b128 v[204:207], v247 offset:16384\n\t" \
  "s_add_u32 m0, %[lw], 33792\n\t" \
  "s_nop 0\n\t" \
  "global_load_lds_dwordx4 v249, s[92:93] sc1\n\t" \
  "ds_read_b128 v[208:211], v244 offset:256\n\t" \
  "ds_read_b128 v[212:215], v244 offset:16640\n\t" \
  "ds_read_b128 v[216:219], v245 offset:256\n\t" \
  "ds_read_b128 v[220:223], v245 offset:16640\n\t" \
  "s_add_u32 m0, %[lw], 34816\n\t" \
  "s_nop 0\n\t" \
  "global_load_lds_dwordx4 v250, s[92:93] sc1\n\t" \
  "ds_read_b128 v[224:227], v246 offset:256\n\t" \
  "ds_read_b128 v[228:231], v246 offset:16640\n\t" \
  "ds_read_b128 v[232:235], v247 offset:256\n\t" \
  "ds_read_b128 v[236:239], v247 offset:16640\n\t" \
  "s_add_u32 m0, %[lw], 35840\n\t" \
  "s_nop 0\n\t" \
  "global_load_lds_dwordx4 v251, s[92:93] sc1\n\t" \
  "ds_write_b128 v252, v[136:139] offset:0\n\t" \
  "ds_write_b128 v252, v[140:143] offset:1024\n\t" \
  "s_waitcnt lgkmcnt(0)\n\t" \
  "s_waitcnt vmcnt(4)\n\t" \
  "s_barrier\n\t" \
  "s_setprio 2\n\t" \
  "v_mfma_f32_16x16x32_bf16 v[144:147], v[0:3], v[176:179], 0\n\t" \
  "v_mfma_f32_16x16x32_bf16 v[148:151], v[4:7], v[176:179], 0\n\t" \
  "v_mfma_f32_16x16x32_bf16 v[152:155], v[0:3], v[180:183], 0\n\t" \
  "v_mfma_f32_16x16x32_bf16 v[156:159], v[4:7], v[180:183], 0\n\t" \
  "v_mfma_f32_16x16x32_bf16 v[144:147], v[8:11], v[184:187], v[144:147]\n\t" \
  "v_mfma_f32_16x16x32_bf16 v[148:151], v[12:15], v[184:187], v[148:151]\n\t" \
  "v_mfma_f32_16x16x32_bf16 v[152:155], v[8:11], v[188:191], v[152:155]\n\t" \
  "v_mfma_f32_16x16x32_bf16 v[156:159], v[12:15], v[188:191], v[156:159]\n\t" \
  "v_mfma_f32_16x16x32_bf16 v[144:147], v[16:19], v[192:195], v[144:147]\n\t" \
  "v_mfma_f32_16x16x32_bf16 v[148:151], v[20:23], v[192:195], v[148:151]\n\t" \
  "v_mfma_f32_16x16x32_bf16 v[152:155], v[16:19], v[196:199], v[152:155]\n\t" \
  "v_mfma_f32_16x16x32_bf16 v[156:159], v[20:23], v[196:199], v[156:159]\n\t" \
  "v_mfma_f32_16x16x32_bf16 v[144:147], v[24:27], v[200:203], v[144:147]\n\t" \
  "v_mfma_f32_16x16x32_bf16 v[148:151], v[28:31], v[200:203], v[148:151]\n\t" \
  "v_mfma_f32_16x16x32_bf16 v[152:155], v[24:27], v[204:207], v[152:155]\n\t" \
  "v_mfma_f32_16x16x32_bf16 v[156:159], v[28:31], v[204:207], v[156:159]\n\t" \
  "v_mfma_f32_16x16x32_bf16 v[144:147], v[32:35], v[208:211], v[144:147]\n\t" \
  "v_mfma_f32_16x16x32_bf16 v[148:151], v[36:39], v[208:211], v[148:151]\n\t" \
  "v_mfma_f32_16x16x32_bf16 v[152:155], v[32:35], v[212:215], v[152:155]\n\t" \
  "v_mfma_f32_16x16x32_bf16 v[156:159], v[36:39], v[212:215], v[156:159]\n\t" \
  "v_mfma_f32_16x16x32_bf16 v[144:147], v[40:43], v[216:219], v[144:147]\n\t" \
  "v_mfma_f32_16x16x32_bf16 v[148:151], v[44:47], v[216:219], v[148:151]\n\t" \
  "v_mfma_f32_16x16x32_bf16 v[152:155], v[40:43], v[220:223], v[152:155]\n\t" \
  "v_mfma_f32_16x16x32_bf16 v[156:159], v[44:47], v[220:223], v[156:159]\n\t" \
  "v_mfma_f32_16x16x32_bf16 v[144:147], v[48:51], v[224:227], v[144:147]\n\t" \
  "v_mfma_f32_16x16x32_bf16 v[148:151], v[52:55], v[224:227], v[148:151]\n\t" \
  "v_mfma_f32_16x16x32_bf16 v[152:155], v[48:51], v[228:231], v[152:155]\n\t" \
  "v_mfma_f32_16x16x32_bf16 v[156:159], v[52:55], v[228:231], v[156:159]\n\t" \
  "v_mfma_f32_16x16x32_bf16 v[144:147], v[56:59], v[232:235], v[144:147]\n\t" \
  "v_mfma_f32_16x16x32_bf16 v[148:151], v[60:63], v[232:235], v[148:151]\n\t" \
  "v_mfma_f32_16x16x32_bf16 v[152:155], v[56:59], v[236:239], v[152:155]\n\t" \
  "v_mfma_f32_16x16x32_bf16 v[156:159], v[60:63], v[236:239], v[156:159]\n\t" \
  "s_setprio 0\n\t" \
  "s_add_u32 s92, s92, 65024\n\t" \
  "s_addc_u32 s93, s93, 0\n\t" \
  "ds_read_b128 v[160:163], v253 offset:0\n\t" \
  "ds_read_b128 v[164:167], v253 offset:1024\n\t" \
  "ds_read_b128 v[176:179], v244 offset:32768\n\t" \
  "ds_read_b128 v[180:183], v244 offset:49152\n\t" \
  "ds_read_b128 v[184:187], v245 offset:32768\n\t" \
  "ds_read_b128 v[188:191], v245 offset:49152\n\t" \
  "s_add_u32 m0, %[lw], 65536\n\t" \
  "s_nop 0\n\t" \
  "global_load_lds_dwordx4 v248, s[92:93] sc1\n\t" \
  "ds_read_b128 v[192:195], v246 offset:32768\n\t" \
  "ds_read_b128 v[196:199], v246 offset:49152\n\t" \
  "ds_read_b128 v[200:203], v247 offset:32768\n\t" \
  "ds_read_b128 v[204:207], v247 offset:49152\n\t" \
  "s_add_u32 m0, %[lw], 66560\n\t" \
  "s_nop 0\n\t" \
  "global_load_lds_dwordx4 v249, s[92:93] sc1\n\t" \
  "ds_read_b128 v[208:211], v244 offset:33024\n\t" \
  "ds_read_b128 v[212:215], v244 offset:49408\n\t" \
  "ds_read_b128 v[216:219], v245 offset:33024\n\t" \
  "ds_read_b128 v[220:223], v245 offset:49408\n\t" \
  "s_add_u32 m0, %[lw], 67584\n\t" \
  "s_nop 0\n\t" \
  "global_load_lds_dwordx4 v250, s[92:93] sc1\n\t" \
  "ds_read_b128 v[224:227], v246 offset:33024\n\t" \
  "ds_read_b128 v[228:231], v246 offset:49408\n\t" \
  "ds_read_b128 v[232:235], v247 offset:33024\n\t" \
  "ds_read_b128 v[236:239], v247 offset:49408\n\t" \
  "s_add_u32 m0, %[lw], 68608\n\t" \
  "s_nop 0\n\t" \
  "global_load_lds_dwordx4 v251, s[92:93] sc1\n\t" \
  "s_waitcnt lgkmcnt(15)\n\t" \
  "s_waitcnt lgkmcnt(12)\n\t" \
  "s_sub_i32 s91, s86, 2\n\t" \
  "v_mov_b32 v173, s91\n\t" \
  "buffer_store_dword v173, v174, %[rs], 0 offen sc1\n\t" \
  "s_lshr_b32 s90, s87, 7\n\t" \
  "s_xor_b32 s90, s90, %[s0]\n\t" \
  "s_and_b32 s90, s90, 1\n\t" \
  "s_lshl1_add_u32 s90, s87, s90\n\t" \
  "s_lshl_b32 s90, s90, 13\n\t" \
  "v_add_f32 v160, v160, v128\n\t" \
  "v_add_f32 v161, v161, v129\n\t" \
  "v_add_f32 v162, v162, v130\n\t" \
  "v_add_f32 v163, v163, v131\n\t" \
  "v_add_f32 v164, v164, v132\n\t" \
  "v_add_f32 v165, v165, v133\n\t" \
  "v_add_f32 v166, v166, v134\n\t" \
  "v_add_f32 v167, v167, v135\n\t" \
  "v_cvt_pk_bf16_f32 v168, v160, v161\n\t" \
  "v_cvt_pk_bf16_f32 v169, v162, v163\n\t" \
  "v_cvt_pk_bf16_f32 v170, v164, v165\n\t" \
  "v_cvt_pk_bf16_f32 v171, v166, v167\n\t" \
  "buffer_store_dwordx4 v[168:171], v172, %[rt], s90 offen sc1\n\t" \
  "s_add_u32 s86, s86, 1\n\t" \
  "s_add_u32 s87, s87, 1\n\t" \
  "s_waitcnt lgkmcnt(0)\n\t" \
  "s_cmp_lt_u32 s86, 64\n\t" \
  "s_cbranch_scc1 .Lp1%=\n\t" \
  "s_barrier\n\t" \
  "s_setprio 2\n\t" \
  "v_mfma_f32_16x16x32_bf16 v[144:147], v[64:67], v[176:179], v[144:147]\n\t" \
  "v_mfma_f32_16x16x32_bf16 v[148:151], v[68:71], v[176:179], v[148:151]\n\t" \
  "v_mfma_f32_16x16x32_bf16 v[152:155], v[64:67], v[180:183], v[152:155]\n\t" \
  "v_mfma_f32_16x16x32_bf16 v[156:159], v[68:71], v[180:183], v[156:159]\n\t" \
  "v_mfma_f32_16x16x32_bf16 v[144:147], v[72:75], v[184:187], v[144:147]\n\t" \
  "v_mfma_f32_16x16x32_bf16 v[148:151], v[76:79], v[184:187], v[148:151]\n\t" \
  "v_mfma_f32_16x16x32_bf16 v[152:155], v[72:75], v[188:191], v[152:155]\n\t" \
  "v_mfma_f32_16x16x32_bf16 v[156:159], v[76:79], v[188:191], v[156:159]\n\t" \
  "v_mfma_f32_16x16x32_bf16 v[144:147], v[80:83], v[192:195], v[144:147]\n\t" \
  "v_mfma_f32_16x16x32_bf16 v[148:151], v[84:87], v[192:195], v[148:151]\n\t" \
  "v_mfma_f32_16x16x32_bf16 v[152:155], v[80:83], v[196:199], v[152:155]\n\t" \
  "v_mfma_f32_16x16x32_bf16 v[156:159], v[84:87], v[196:199], v[156:159]\n\t" \
  "v_mfma_f32_16x16x32_bf16 v[144:147], v[88:91], v[200:203], v[144:147]\n\t" \
  "v_mfma_f32_16x16x32_bf16 v[148:151], v[92:95], v[200:203], v[148:151]\n\t" \
  "v_mfma_f32_16x16x32_bf16 v[152:155], v[88:91], v[204:207], v[152:155]\n\t" \
  "v_mfma_f32_16x16x32_bf16 v[156:159], v[92:95], v[204:207], v[156:159]\n\t" \
  "v_mfma_f32_16x16x32_bf16 v[144:147], v[96:99], v[208:211], v[144:147]\n\t" \
  "v_mfma_f32_16x16x32_bf16 v[148:151], v[100:103], v[208:211], v[148:151]\n\t" \
  "v_mfma_f32_16x16x32_bf16 v[152:155], v[96:99], v[212:215], v[152:155]\n\t" \
  "v_mfma_f32_16x16x32_bf16 v[156:159], v[100:103], v[212:215], v[156:159]\n\t" \
  "v_mfma_f32_16x16x32_bf16 v[144:147], v[104:107], v[216:219], v[144:147]\n\t" \
  "v_mfma_f32_16x16x32_bf16 v[148:151], v[108:111], v[216:219], v[148:151]\n\t" \
  "v_mfma_f32_16x16x32_bf16 v[152:155], v[104:107], v[220:223], v[152:155]\n\t" \
  "v_mfma_f32_16x16x32_bf16 v[156:159], v[108:111], v[220:223], v[156:159]\n\t" \
  "v_mfma_f32_16x16x32_bf16 v[144:147], v[112:115], v[224:227], v[144:147]\n\t" \
  "v_mfma_f32_16x16x32_bf16 v[148:151], v[116:119], v[224:227], v[148:151]\n\t" \
  "v_mfma_f32_16x16x32_bf16 v[152:155], v[112:115], v[228:231], v[152:155]\n\t" \
  "v_mfma_f32_16x16x32_bf16 v[156:159], v[116:119], v[228:231], v[156:159]\n\t" \
  "v_mfma_f32_16x16x32_bf16 v[144:147], v[120:123], v[232:235], v[144:147]\n\t" \
  "v_mfma_f32_16x16x32_bf16 v[148:151], v[124:127], v[232:235], v[148:151]\n\t" \
  "v_mfma_f32_16x16x32_bf16 v[152:155], v[120:123], v[236:239], v[152:155]\n\t" \
  "v_mfma_f32_16x16x32_bf16 v[156:159], v[124:127], v[236:239], v[156:159]\n\t" \
  "s_setprio 0\n\t" \
  "s_nop 5\n\t" \
  "ds_write_b128 v252, v[152:155] offset:0\n\t" \
  "ds_write_b128 v252, v[156:159] offset:1024\n\t" \
  "s_waitcnt lgkmcnt(0)\n\t" \
  "s_barrier\n\t" \
  "ds_read_b128 v[160:163], v253 offset:0\n\t" \
  "ds_read_b128 v[164:167], v253 offset:1024\n\t" \
  "s_waitcnt lgkmcnt(0)\n\t" \
  "s_lshr_b32 s90, s87, 7\n\t" \
  "s_xor_b32 s90, s90, %[s0]\n\t" \
  "s_and_b32 s90, s90, 1\n\t" \
  "s_lshl1_add_u32 s90, s87, s90\n\t" \
  "s_lshl_b32 s90, s90, 13\n\t" \
  "v_add_f32 v160, v160, v144\n\t" \
  "v_add_f32 v161, v161, v145\n\t" \
  "v_add_f32 v162, v162, v146\n\t" \
  "v_add_f32 v163, v163, v147\n\t" \
  "v_add_f32 v164, v164, v148\n\t" \
  "v_add_f32 v165, v165, v149\n\t" \
  "v_add_f32 v166, v166, v150\n\t" \
  "v_add_f32 v167, v167, v151\n\t" \
  "v_cvt_pk_bf16_f32 v168, v160, v161\n\t" \
  "v_cvt_pk_bf16_f32 v169, v162, v163\n\t" \
  "v_cvt_pk_bf16_f32 v170, v164, v165\n\t" \
  "v_cvt_pk_bf16_f32 v171, v166, v167\n\t" \
  "buffer_store_dwordx4 v[168:171], v172, %[rt], s90 offen sc1\n\t" \
  "s_waitcnt vmcnt(0)\n\t" \
  "s_barrier\n\t" \
  "s_sub_i32 s91, s86, 1\n\t" \
  "v_mov_b32 v173, s91\n\t" \
  "buffer_store_dword v173, v174, %[rs], 0 offen sc1\n\t" \
  "s_mov_b32 m0, s94\n\t" \
  ".Le%=:\n\t"
#define PP_ASM_1 \
  "s_cmp_lg_u32 %[kh], 0\n\t" \
  "s_cbranch_scc1 .Lk%=\n\t" \
  "s_mov_b32 s94, m0\n\t" \
  "s_mov_b64 s[92:93], %[x0]\n\t" \
  "s_mov_b32 s86, %[g0]\n\t" \
  "s_add_u32 s87, s86, %[t0]\n\t" \
  "s_add_u32 m0, %[lw], 67584\n\t" \
  "s_nop 0\n\t" \
  "global_load_lds_dwordx4 v250, s[92:93] sc1\n\t" \
  "s_add_u32 m0, %[lw], 68608\n\t" \
  "s_nop 0\n\t" \
  "global_load_lds_dwordx4 v251, s[92:93] sc1\n\t" \
  "s_barrier\n\t" \
  "s_branch .Ls0%=\n\t" \
  ".Lp0%=:\n\t" \
  "s_waitcnt vmcnt(9)\n\t" \
  "s_barrier\n\t" \
  ".Ls0%=:\n\t" \
  "s_add_u32 s92, s92, 512\n\t" \
  "s_addc_u32 s93, s93, 0\n\t" \
  "ds_read_b128 v[176:179], v240 offset:0\n\t" \
  "ds_read_b128 v[180:183], v240 offset:16384\n\t" \
  "ds_read_b128 v[184:187], v241 offset:0\n\t" \
  "ds_read_b128 v[188:191], v241 offset:16384\n\t" \
  "s_add_u32 m0, %[lw], 98304\n\t" \
  "s_nop 0\n\t" \
  "global_load_lds_dwordx4 v248, s[92:93] sc1\n\t" \
  "ds_read_b128 v[192:195], v242 offset:0\n\t" \
  "ds_read_b128 v[196:199], v242 offset:16384\n\t" \
  "ds_read_b128 v[200:203], v243 offset:0\n\t" \
  "ds_read_b128 v[204:207], v243 offset:16384\n\t" \
  "s_add_u32 m0, %[lw], 99328\n\t" \
  "s_nop 0\n\t" \
  "global_load_lds_dwordx4 v249, s[92:93] sc1\n\t" \
  "ds_read_b128 v[208:211], v240 offset:256\n\t" \
  "ds_read_b128 v[212:215], v240 offset:16640\n\t" \
  "ds_read_b128 v[216:219], v241 offset:256\n\t" \
  "ds_read_b128 v[220:223], v241 offset:16640\n\t" \
  "s_add_u32 m0, %[lw], 100352\n\t" \
  "s_nop 0\n\t" \
  "global_load_lds_dwordx4 v250, s[92:93] sc1\n\t" \
  "ds_read_b128 v[224:227], v242 offset:256\n\t" \
  "ds_read_b128 v[228:231], v242 offset:16640\n\t" \
  "ds_read_b128 v[232:235], v243 offset:256\n\t" \
  "ds_read_b128 v[236:239], v243 offset:16640\n\t" \
  "s_add_u32 m0, %[lw], 101376\n\t" \
  "s_nop 0\n\t" \
  "global_load_lds_dwordx4 v251, s[92:93] sc1\n\t" \
  "ds_write_b128 v252, v[152:155] offset:0\n\t" \
  "ds_write_b128 v252, v[156:159] offset:1024\n\t" \
  "s_waitcnt lgkmcnt(0)\n\t" \
  "s_setprio 1\n\t" \
  "v_mfma_f32_16x16x32_bf16 v[128:131], v[176:179], v[0:3], 0\n\t" \
  "v_mfma_f32_16x16x32_bf16 v[136:139], v[176:179], v[4:7], 0\n\t" \
  "v_mfma_f32_16x16x32_bf16 v[132:135], v[180:183], v[0:3], 0\n\t" \
  "v_mfma_f32_16x16x32_bf16 v[140:143], v[180:183], v[4:7], 0\n\t" \
  "v_mfma_f32_16x16x32_bf16 v[128:131], v[184:187], v[8:11], v[128:131]\n\t" \
  "v_mfma_f32_16x16x32_bf16 v[136:139], v[184:187], v[12:15], v[136:139]\n\t" \
  "v_mfma_f32_16x16x32_bf16 v[132:135], v[188:191], v[8:11], v[132:135]\n\t" \
  "v_mfma_f32_16x16x32_bf16 v[140:143], v[188:191], v[12:15], v[140:143]\n\t" \
  "v_mfma_f32_16x16x32_bf16 v[128:131], v[192:195], v[16:19], v[128:131]\n\t" \
  "v_mfma_f32_16x16x32_bf16 v[136:139], v[192:195], v[20:23], v[136:139]\n\t" \
  "v_mfma_f32_16x16x32_bf16 v[132:135], v[196:199], v[16:19], v[132:135]\n\t" \
  "v_mfma_f32_16x16x32_bf16 v[140:143], v[196:199], v[20:23], v[140:143]\n\t" \
  "v_mfma_f32_16x16x32_bf16 v[128:131], v[200:203], v[24:27], v[128:131]\n\t" \
  "v_mfma_f32_16x16x32_bf16 v[136:139], v[200:203], v[28:31], v[136:139]\n\t" \
  "v_mfma_f32_16x16x32_bf16 v[132:135], v[204:207], v[24:27], v[132:135]\n\t" \
  "v_mfma_f32_16x16x32_bf16 v[140:143], v[204:207], v[28:31], v[140:143]\n\t" \
  "v_mfma_f32_16x16x32_bf16 v[128:131], v[208:211], v[32:35], v[128:131]\n\t" \
  "v_mfma_f32_16x16x32_bf16 v[136:139], v[208:211], v[36:39], v[136:139]\n\t" \
  "v_mfma_f32_16x16x32_bf16 v[132:135], v[212:215], v[32:35], v[132:135]\n\t" \
  "v_mfma_f32_16x16x32_bf16 v[140:143], v[212:215], v[36:39], v[140:143]\n\t" \
  "v_mfma_f32_16x16x32_bf16 v[128:131], v[216:219], v[40:43], v[128:131]\n\t" \
  "v_mfma_f32_16x16x32_bf16 v[136:139], v[216:219], v[44:47], v[136:139]\n\t" \
  "v_mfma_f32_16x16x32_bf16 v[132:135], v[220:223], v[40:43], v[132:135]\n\t" \
  "v_mfma_f32_16x16x32_bf16 v[140:143], v[220:223], v[44:47], v[140:143]\n\t" \
  "v_mfma_f32_16x16x32_bf16 v[128:131], v[224:227], v[48:51], v[128:131]\n\t" \
  "v_mfma_f32_16x16x32_bf16 v[136:139], v[224:227], v[52:55], v[136:139]\n\t" \
  "v_mfma_f32_16x16x32_bf16 v[132:135], v[228:231], v[48:51], v[132:135]\n\t" \
  "v_mfma_f32_16x16x32_bf16 v[140:143], v[228:231], v[52:55], v[140:143]\n\t" \
  "v_mfma_f32_16x16x32_bf16 v[128:131], v[232:235], v[56:59], v[128:131]\n\t" \
  "v_mfma_f32_16x16x32_bf16 v[136:139], v[232:235], v[60:63], v[136:139]\n\t" \
  "v_mfma_f32_16x16x32_bf16 v[132:135], v[236:239], v[56:59], v[132:135]\n\t" \
  "v_mfma_f32_16x16x32_bf16 v[140:143], v[236:239], v[60:63], v[140:143]\n\t" \
  "s_setprio 0\n\t" \
  "s_waitcnt vmcnt(4)\n\t" \
  "s_barrier\n\t" \
  "s_add_u32 s92, s92, 65024\n\t" \
  "s_addc_u32 s93, s93, 0\n\t" \
  "ds_read_b128 v[160:163], v253 offset:0\n\t" \
  "ds_read_b128 v[164:167], v253 offset:1024\n\t" \
  "ds_read_b128 v[176:179], v240 offset:32768\n\t" \
  "ds_read_b128 v[180:183], v240 offset:49152\n\t" \
  "ds_read_b128 v[184:187], v241 offset:32768\n\t" \
  "ds_read_b128 v[188:191], v241 offset:49152\n\t" \
  "s_add_u32 m0, %[lw], 0\n\t" \
  "s_nop 0\n\t" \
  "global_load_lds_dwordx4 v248, s[92:93] sc1\n\t" \
  "ds_read_b128 v[192:195], v242 offset:32768\n\t" \
  "ds_read_b128 v[196:199], v242 offset:49152\n\t" \
  "ds_read_b128 v[200:203], v243 offset:32768\n\t" \
  "ds_read_b128 v[204:207], v243 offset:49152\n\t" \
  "s_add_u32 m0, %[lw], 1024\n\t" \
  "s_nop 0\n\t" \
  "global_load_lds_dwordx4 v249, s[92:93] sc1\n\t" \
  "ds_read_b128 v[208:211], v240 offset:33024\n\t" \
  "ds_read_b128 v[212:215], v240 offset:49408\n\t" \
  "ds_read_b128 v[216:219], v241 offset:33024\n\t" \
  "ds_read_b128 v[220:223], v241 offset:49408\n\t" \
  "s_add_u32 m0, %[lw], 2048\n\t" \
  "s_nop 0\n\t" \
  "global_load_lds_dwordx4 v250, s[92:93] sc1\n\t" \
  "ds_read_b128 v[224:227], v242 offset:33024\n\t" \
  "ds_read_b128 v[228:231], v242 offset:49408\n\t" \
  "ds_read_b128 v[232:235], v243 offset:33024\n\t" \
  "ds_read_b128 v[236:239], v243 offset:49408\n\t" \
  "s_add_u32 m0, %[lw], 3072\n\t" \
  "s_nop 0\n\t" \
  "global_load_lds_dwordx4 v251, s[92:93] sc1\n\t" \
  "s_waitcnt lgkmcnt(15)\n\t" \
  "s_waitcnt lgkmcnt(12)\n\t" \
  "s_lshr_b32 s90, s87, 7\n\t" \
  "s_xor_b32 s90, s90, %[s0]\n\t" \
  "s_and_b32 s90, s90, 1\n\t" \
  "s_lshl1_add_u32 s90, s87, s90\n\t" \
  "s_lshl_b32 s90, s90, 13\n\t" \
  "v_add_f32 v160, v160, v144\n\t" \
  "v_add_f32 v161, v161, v145\n\t" \
  "v_add_f32 v162, v162, v146\n\t" \
  "v_add_f32 v163, v163, v147\n\t" \
  "v_add_f32 v164, v164, v148\n\t" \
  "v_add_f32 v165, v165, v149\n\t" \
  "v_add_f32 v166, v166, v150\n\t" \
  "v_add_f32 v167, v167, v151\n\t" \
  "v_cvt_pk_bf16_f32 v168, v160, v161\n\t" \
  "v_cvt_pk_bf16_f32 v169, v162, v163\n\t" \
  "v_cvt_pk_bf16_f32 v170, v164, v165\n\t" \
  "v_cvt_pk_bf16_f32 v171, v166, v167\n\t" \
  "buffer_store_dwordx4 v[168:171], v172, %[rt], s90 offen sc1\n\t" \
  "s_add_u32 s86, s86, 1\n\t" \
  "s_add_u32 s87, s87, 1\n\t" \
  "s_waitcnt lgkmcnt(0)\n\t" \
  "s_setprio 1\n\t" \
  "v_mfma_f32_16x16x32_bf16 v[128:131], v[176:179], v[64:67], v[128:131]\n\t" \
  "v_mfma_f32_16x16x32_bf16 v[136:139], v[176:179], v[68:71], v[136:139]\n\t" \
  "v_mfma_f32_16x16x32_bf16 v[132:135], v[180:183], v[64:67], v[132:135]\n\t" \
  "v_mfma_f32_16x16x32_bf16 v[140:143], v[180:183], v[68:71], v[140:143]\n\t" \
  "v_mfma_f32_16x16x32_bf16 v[128:131], v[184:187], v[72:75], v[128:131]\n\t" \
  "v_mfma_f32_16x16x32_bf16 v[136:139], v[184:187], v[76:79], v[136:139]\n\t" \
  "v_mfma_f32_16x16x32_bf16 v[132:135], v[188:191], v[72:75], v[132:135]\n\t" \
  "v_mfma_f32_16x16x32_bf16 v[140:143], v[188:191], v[76:79], v[140:143]\n\t" \
  "v_mfma_f32_16x16x32_bf16 v[128:131], v[192:195], v[80:83], v[128:131]\n\t" \
  "v_mfma_f32_16x16x32_bf16 v[136:139], v[192:195], v[84:87], v[136:139]\n\t" \
  "v_mfma_f32_16x16x32_bf16 v[132:135], v[196:199], v[80:83], v[132:135]\n\t" \
  "v_mfma_f32_16x16x32_bf16 v[140:143], v[196:199], v[84:87], v[140:143]\n\t" \
  "v_mfma_f32_16x16x32_bf16 v[128:131], v[200:203], v[88:91], v[128:131]\n\t" \
  "v_mfma_f32_16x16x32_bf16 v[136:139], v[200:203], v[92:95], v[136:139]\n\t" \
  "v_mfma_f32_16x16x32_bf16 v[132:135], v[204:207], v[88:91], v[132:135]\n\t" \
  "v_mfma_f32_16x16x32_bf16 v[140:143], v[204:207], v[92:95], v[140:143]\n\t" \
  "v_mfma_f32_16x16x32_bf16 v[128:131], v[208:211], v[96:99], v[128:131]\n\t" \
  "v_mfma_f32_16x16x32_bf16 v[136:139], v[208:211], v[100:103], v[136:139]\n\t" \
  "v_mfma_f32_16x16x32_bf16 v[132:135], v[212:215], v[96:99], v[132:135]\n\t" \
  "v_mfma_f32_16x16x32_bf16 v[140:143], v[212:215], v[100:103], v[140:143]\n\t" \
  "v_mfma_f32_16x16x32_bf16 v[128:131], v[216:219], v[104:107], v[128:131]\n\t" \
  "v_mfma_f32_16x16x32_bf16 v[136:139], v[216:219], v[108:111], v[136:139]\n\t" \
  "v_mfma_f32_16x16x32_bf16 v[132:135], v[220:223], v[104:107], v[132:135]\n\t" \
  "v_mfma_f32_16x16x32_bf16 v[140:143], v[220:223], v[108:111], v[140:143]\n\t" \
  "v_mfma_f32_16x16x32_bf16 v[128:131], v[224:227], v[112:115], v[128:131]\n\t" \
  "v_mfma_f32_16x16x32_bf16 v[136:139], v[224:227], v[116:119], v[136:139]\n\t" \
  "v_mfma_f32_16x16x32_bf16 v[132:135], v[228:231], v[112:115], v[132:135]\n\t" \
  "v_mfma_f32_16x16x32_bf16 v[140:143], v[228:231], v[116:119], v[140:143]\n\t" \
  "v_mfma_f32_16x16x32_bf16 v[128:131], v[232:235], v[120:123], v[128:131]\n\t" \
  "v_mfma_f32_16x16x32_bf16 v[136:139], v[232:235], v[124:127], v[136:139]\n\t" \
  "v_mfma_f32_16x16x32_bf16 v[132:135], v[236:239], v[120:123], v[132:135]\n\t" \
  "v_mfma_f32_16x16x32_bf16 v[140:143], v[236:239], v[124:127], v[140:143]\n\t" \
  "s_setprio 0\n\t" \
  "s_waitcnt vmcnt(9)\n\t" \
  "s_barrier\n\t" \
  "s_add_u32 s92, s92, 512\n\t" \
  "s_addc_u32 s93, s93, 0\n\t" \
  "ds_read_b128 v[176:179], v244 offset:0\n\t" \
  "ds_read_b128 v[180:183], v244 offset:16384\n\t" \
  "ds_read_b128 v[184:187], v245 offset:0\n\t" \
  "ds_read_b128 v[188:191], v245 offset:16384\n\t" \
  "s_add_u32 m0, %[lw], 32768\n\t" \
  "s_nop 0\n\t" \
  "global_load_lds_dwordx4 v248, s[92:93] sc1\n\t" \
  "ds_read_b128 v[192:195], v246 offset:0\n\t" \
  "ds_read_b128 v[196:199], v246 offset:16384\n\t" \
  "ds_read_b128 v[200:203], v247 offset:0\n\t" \
  "ds_read_b128 v[204:207], v247 offset:16384\n\t" \
  "s_add_u32 m0, %[lw], 33792\n\t" \
  "s_nop 0\n\t" \
  "global_load_lds_dwordx4 v249, s[92:93] sc1\n\t" \
  "ds_read_b128 v[208:211], v244 offset:256\n\t" \
  "ds_read_b128 v[212:215], v244 offset:16640\n\t" \
  "ds_read_b128 v[216:219], v245 offset:256\n\t" \
  "ds_read_b128 v[220:223], v245 offset:16640\n\t" \
  "s_add_u32 m0, %[lw], 34816\n\t" \
  "s_nop 0\n\t" \
  "global_load_lds_dwordx4 v250, s[92:93] sc1\n\t" \
  "ds_read_b128 v[224:227], v246 offset:256\n\t" \
  "ds_read_b128 v[228:231], v246 offset:16640\n\t" \
  "ds_read_b128 v[232:235], v247 offset:256\n\t" \
  "ds_read_b128 v[236:239], v247 offset:16640\n\t" \
  "s_add_u32 m0, %[lw], 35840\n\t" \
  "s_nop 0\n\t" \
  "global_load_lds_dwordx4 v251, s[92:93] sc1\n\t" \
  "ds_write_b128 v252, v[136:139] offset:0\n\t" \
  "ds_write_b128 v252, v[140:143] offset:1024\n\t" \
  "s_waitcnt lgkmcnt(0)\n\t" \
  "s_setprio 1\n\t" \
  "v_mfma_f32_16x16x32_bf16 v[144:147], v[176:179], v[0:3], 0\n\t" \
  "v_mfma_f32_16x16x32_bf16 v[152:155], v[176:179], v[4:7], 0\n\t" \
  "v_mfma_f32_16x16x32_bf16 v[148:151], v[180:183], v[0:3], 0\n\t" \
  "v_mfma_f32_16x16x32_bf16 v[156:159], v[180:183], v[4:7], 0\n\t" \
  "v_mfma_f32_16x16x32_bf16 v[144:147], v[184:187], v[8:11], v[144:147]\n\t" \
  "v_mfma_f32_16x16x32_bf16 v[152:155], v[184:187], v[12:15], v[152:155]\n\t" \
  "v_mfma_f32_16x16x32_bf16 v[148:151], v[188:191], v[8:11], v[148:151]\n\t" \
  "v_mfma_f32_16x16x32_bf16 v[156:159], v[188:191], v[12:15], v[156:159]\n\t" \
  "v_mfma_f32_16x16x32_bf16 v[144:147], v[192:195], v[16:19], v[144:147]\n\t" \
  "v_mfma_f32_16x16x32_bf16 v[152:155], v[192:195], v[20:23], v[152:155]\n\t" \
  "v_mfma_f32_16x16x32_bf16 v[148:151], v[196:199], v[16:19], v[148:151]\n\t" \
  "v_mfma_f32_16x16x32_bf16 v[156:159], v[196:199], v[20:23], v[156:159]\n\t" \
  "v_mfma_f32_16x16x32_bf16 v[144:147], v[200:203], v[24:27], v[144:147]\n\t" \
  "v_mfma_f32_16x16x32_bf16 v[152:155], v[200:203], v[28:31], v[152:155]\n\t" \
  "v_mfma_f32_16x16x32_bf16 v[148:151], v[204:207], v[24:27], v[148:151]\n\t" \
  "v_mfma_f32_16x16x32_bf16 v[156:159], v[204:207], v[28:31], v[156:159]\n\t" \
  "v_mfma_f32_16x16x32_bf16 v[144:147], v[208:211], v[32:35], v[144:147]\n\t" \
  "v_mfma_f32_16x16x32_bf16 v[152:155], v[208:211], v[36:39], v[152:155]\n\t" \
  "v_mfma_f32_16x16x32_bf16 v[148:151], v[212:215], v[32:35], v[148:151]\n\t" \
  "v_mfma_f32_16x16x32_bf16 v[156:159], v[212:215], v[36:39], v[156:159]\n\t" \
  "v_mfma_f32_16x16x32_bf16 v[144:147], v[216:219], v[40:43], v[144:147]\n\t" \
  "v_mfma_f32_16x16x32_bf16 v[152:155], v[216:219], v[44:47], v[152:155]\n\t" \
  "v_mfma_f32_16x16x32_bf16 v[148:151], v[220:223], v[40:43], v[148:151]\n\t" \
  "v_mfma_f32_16x16x32_bf16 v[156:159], v[220:223], v[44:47], v[156:159]\n\t" \
  "v_mfma_f32_16x16x32_bf16 v[144:147], v[224:227], v[48:51], v[144:147]\n\t" \
  "v_mfma_f32_16x16x32_bf16 v[152:155], v[224:227], v[52:55], v[152:155]\n\t" \
  "v_mfma_f32_16x16x32_bf16 v[148:151], v[228:231], v[48:51], v[148:151]\n\t" \
  "v_mfma_f32_16x16x32_bf16 v[156:159], v[228:231], v[52:55], v[156:159]\n\t" \
  "v_mfma_f32_16x16x32_bf16 v[144:147], v[232:235], v[56:59], v[144:147]\n\t" \
  "v_mfma_f32_16x16x32_bf16 v[152:155], v[232:235], v[60:63], v[152:155]\n\t" \
  "v_mfma_f32_16x16x32_bf16 v[148:151], v[236:239], v[56:59], v[148:151]\n\t" \
  "v_mfma_f32_16x16x32_bf16 v[156:159], v[236:239], v[60:63], v[156:159]\n\t" \
  "s_setprio 0\n\t" \
  "s_waitcnt vmcnt(4)\n\t" \
  "s_barrier\n\t" \
  "s_add_u32 s92, s92, 65024\n\t" \
  "s_addc_u32 s93, s93, 0\n\t" \
  "ds_read_b128 v[160:163], v253 offset:0\n\t" \
  "ds_read_b128 v[164:167], v253 offset:1024\n\t" \
  "ds_read_b128 v[176:179], v244 offset:32768\n\t" \
  "ds_read_b128 v[180:183], v244 offset:49152\n\t" \
  "ds_read_b128 v[184:187], v245 offset:32768\n\t" \
  "ds_read_b128 v[188:191], v245 offset:49152\n\t" \
  "s_add_u32 m0, %[lw], 65536\n\t" \
  "s_nop 0\n\t" \
  "global_load_lds_dwordx4 v248, s[92:93] sc1\n\t" \
  "ds_read_b128 v[192:195], v246 offset:32768\n\t" \
  "ds_read_b128 v[196:199], v246 offset:49152\n\t" \
  "ds_read_b128 v[200:203], v247 offset:32768\n\t" \
  "ds_read_b128 v[204:207], v247 offset:49152\n\t" \
  "s_add_u32 m0, %[lw], 66560\n\t" \
  "s_nop 0\n\t" \
  "global_load_lds_dwordx4 v249, s[92:93] sc1\n\t" \
  "ds_read_b128 v[208:211], v244 offset:33024\n\t" \
  "ds_read_b128 v[212:215], v244 offset:49408\n\t" \
  "ds_read_b128 v[216:219], v245 offset:33024\n\t" \
  "ds_read_b128 v[220:223], v245 offset:49408\n\t" \
  "s_add_u32 m0, %[lw], 67584\n\t" \
  "s_nop 0\n\t" \
  "global_load_lds_dwordx4 v250, s[92:93] sc1\n\t" \
  "ds_read_b128 v[224:227], v246 offset:33024\n\t" \
  "ds_read_b128 v[228:231], v246 offset:49408\n\t" \
  "ds_read_b128 v[232:235], v247 offset:33024\n\t" \
  "ds_read_b128 v[236:239], v247 offset:49408\n\t" \
  "s_add_u32 m0, %[lw], 68608\n\t" \
  "s_nop 0\n\t" \
  "global_load_lds_dwordx4 v251, s[92:93] sc1\n\t" \
  "s_waitcnt lgkmcnt(15)\n\t" \
  "s_waitcnt lgkmcnt(12)\n\t" \
  "s_lshr_b32 s90, s87, 7\n\t" \
  "s_xor_b32 s90, s90, %[s0]\n\t" \
  "s_and_b32 s90, s90, 1\n\t" \
  "s_lshl1_add_u32 s90, s87, s90\n\t" \
  "s_lshl_b32 s90, s90, 13\n\t" \
  "v_add_f32 v160, v160, v128\n\t" \
  "v_add_f32 v161, v161, v129\n\t" \
  "v_add_f32 v162, v162, v130\n\t" \
  "v_add_f32 v163, v163, v131\n\t" \
  "v_add_f32 v164, v164, v132\n\t" \
  "v_add_f32 v165, v165, v133\n\t" \
  "v_add_f32 v166, v166, v134\n\t" \
  "v_add_f32 v167, v167, v135\n\t" \
  "v_cvt_pk_bf16_f32 v168, v160, v161\n\t" \
  "v_cvt_pk_bf16_f32 v169, v162, v163\n\t" \
  "v_cvt_pk_bf16_f32 v170, v164, v165\n\t" \
  "v_cvt_pk_bf16_f32 v171, v166, v167\n\t" \
  "buffer_store_dwordx4 v[168:171], v172, %[rt], s90 offen sc1\n\t" \
  "s_add_u32 s86, s86, 1\n\t" \
  "s_add_u32 s87, s87, 1\n\t" \
  "s_waitcnt lgkmcnt(0)\n\t" \
  "s_setprio 1\n\t" \
  "v_mfma_f32_16x16x32_bf16 v[144:147], v[176:179], v[64:67], v[144:147]\n\t" \
  "v_mfma_f32_16x16x32_bf16 v[152:155], v[176:179], v[68:71], v[152:155]\n\t" \
  "v_mfma_f32_16x16x32_bf16 v[148:151], v[180:183], v[64:67], v[148:151]\n\t" \
  "v_mfma_f32_16x16x32_bf16 v[156:159], v[180:183], v[68:71], v[156:159]\n\t" \
  "v_mfma_f32_16x16x32_bf16 v[144:147], v[184:187], v[72:75], v[144:147]\n\t" \
  "v_mfma_f32_16x16x32_bf16 v[152:155], v[184:187], v[76:79], v[152:155]\n\t" \
  "v_mfma_f32_16x16x32_bf16 v[148:151], v[188:191], v[72:75], v[148:151]\n\t" \
  "v_mfma_f32_16x16x32_bf16 v[156:159], v[188:191], v[76:79], v[156:159]\n\t" \
  "v_mfma_f32_16x16x32_bf16 v[144:147], v[192:195], v[80:83], v[144:147]\n\t" \
  "v_mfma_f32_16x16x32_bf16 v[152:155], v[192:195], v[84:87], v[152:155]\n\t" \
  "v_mfma_f32_16x16x32_bf16 v[148:151], v[196:199], v[80:83], v[148:151]\n\t" \
  "v_mfma_f32_16x16x32_bf16 v[156:159], v[196:199], v[84:87], v[156:159]\n\t" \
  "v_mfma_f32_16x16x32_bf16 v[144:147], v[200:203], v[88:91], v[144:147]\n\t" \
  "v_mfma_f32_16x16x32_bf16 v[152:155], v[200:203], v[92:95], v[152:155]\n\t" \
  "v_mfma_f32_16x16x32_bf16 v[148:151], v[204:207], v[88:91], v[148:151]\n\t" \
  "v_mfma_f32_16x16x32_bf16 v[156:159], v[204:207], v[92:95], v[156:159]\n\t" \
  "v_mfma_f32_16x16x32_bf16 v[144:147], v[208:211], v[96:99], v[144:147]\n\t" \
  "v_mfma_f32_16x16x32_bf16 v[152:155], v[208:211], v[100:103], v[152:155]\n\t" \
  "v_mfma_f32_16x16x32_bf16 v[148:151], v[212:215], v[96:99], v[148:151]\n\t" \
  "v_mfma_f32_16x16x32_bf16 v[156:159], v[212:215], v[100:103], v[156:159]\n\t" \
  "v_mfma_f32_16x16x32_bf16 v[144:147], v[216:219], v[104:107], v[144:147]\n\t" \
  "v_mfma_f32_16x16x32_bf16 v[152:155], v[216:219], v[108:111], v[152:155]\n\t" \
  "v_mfma_f32_16x16x32_bf16 v[148:151], v[220:223], v[104:107], v[148:151]\n\t" \
  "v_mfma_f32_16x16x32_bf16 v[156:159], v[220:223], v[108:111], v[156:159]\n\t" \
  "v_mfma_f32_16x16x32_bf16 v[144:147], v[224:227], v[112:115], v[144:147]\n\t" \
  "v_mfma_f32_16x16x32_bf16 v[152:155], v[224:227], v[116:119], v[152:155]\n\t" \
  "v_mfma_f32_16x16x32_bf16 v[148:151], v[228:231], v[112:115], v[148:151]\n\t" \
  "v_mfma_f32_16x16x32_bf16 v[156:159], v[228:231], v[116:119], v[156:159]\n\t" \
  "v_mfma_f32_16x16x32_bf16 v[144:147], v[232:235], v[120:123], v[144:147]\n\t" \
  "v_mfma_f32_16x16x32_bf16 v[152:155], v[232:235], v[124:127], v[152:155]\n\t" \
  "v_mfma_f32_16x16x32_bf16 v[148:151], v[236:239], v[120:123], v[148:151]\n\t" \
  "v_mfma_f32_16x16x32_bf16 v[156:159], v[236:239], v[124:127], v[156:159]\n\t" \
  "s_setprio 0\n\t" \
  "s_cmp_lt_u32 s86, 64\n\t" \
  "s_cbranch_scc1 .Lp0%=\n\t" \
  "s_barrier\n\t" \
  "s_nop 1\n\t" \
  "ds_write_b128 v252, v[152:155] offset:0\n\t" \
  "s_nop 0\n\t" \
  "ds_write_b128 v252, v[156:159] offset:1024\n\t" \
  "s_waitcnt lgkmcnt(0)\n\t" \
  "s_barrier\n\t" \
  "ds_read_b128 v[160:163], v253 offset:0\n\t" \
  "ds_read_b128 v[164:167], v253 offset:1024\n\t" \
  "s_waitcnt lgkmcnt(0)\n\t" \
  "s_lshr_b32 s90, s87, 7\n\t" \
  "s_xor_b32 s90, s90, %[s0]\n\t" \
  "s_and_b32 s90, s90, 1\n\t" \
  "s_lshl1_add_u32 s90, s87, s90\n\t" \
  "s_lshl_b32 s90, s90, 13\n\t" \
  "v_add_f32 v160, v160, v144\n\t" \
  "v_add_f32 v161, v161, v145\n\t" \
  "v_add_f32 v162, v162, v146\n\t" \
  "v_add_f32 v163, v163, v147\n\t" \
  "v_add_f32 v164, v164, v148\n\t" \
  "v_add_f32 v165, v165, v149\n\t" \
  "v_add_f32 v166, v166, v150\n\t" \
  "v_add_f32 v167, v167, v151\n\t" \
  "v_cvt_pk_bf16_f32 v168, v160, v161\n\t" \
  "v_cvt_pk_bf16_f32 v169, v162, v163\n\t" \
  "v_cvt_pk_bf16_f32 v170, v164, v165\n\t" \
  "v_cvt_pk_bf16_f32 v171, v166, v167\n\t" \
  "buffer_store_dwordx4 v[168:171], v172, %[rt], s90 offen sc1\n\t" \
  "s_waitcnt vmcnt(0)\n\t" \
  "s_barrier\n\t" \
  "s_mov_b32 m0, s94\n\t" \
  "s_branch .Le%=\n\t" \
  ".Lk%=:\n\t" \
  "s_mov_b32 s94, m0\n\t" \
  "s_mov_b64 s[92:93], %[x0]\n\t" \
  "s_mov_b32 s86, %[g0]\n\t" \
  "s_add_u32 s87, s86, %[t0]\n\t" \
  "s_add_u32 m0, %[lw], 67584\n\t" \
  "s_nop 0\n\t" \
  "global_load_lds_dwordx4 v250, s[92:93] sc1\n\t" \
  "s_add_u32 m0, %[lw], 68608\n\t" \
  "s_nop 0\n\t" \
  "global_load_lds_dwordx4 v251, s[92:93] sc1\n\t" \
  "s_barrier\n\t" \
  "s_branch .Ls1%=\n\t" \
  ".Lp1%=:\n\t" \
  "s_waitcnt vmcnt(10)\n\t" \
  "s_barrier\n\t" \
  "s_setprio 2\n\t" \
  "v_mfma_f32_16x16x32_bf16 v[144:147], v[176:179], v[64:67], v[144:147]\n\t" \
  "v_mfma_f32_16x16x32_bf16 v[152:155], v[176:179], v[68:71], v[152:155]\n\t" \
  "v_mfma_f32_16x16x32_bf16 v[148:151], v[180:183], v[64:67], v[148:151]\n\t" \
  "v_mfma_f32_16x16x32_bf16 v[156:159], v[180:183], v[68:71], v[156:159]\n\t" \
  "v_mfma_f32_16x16x32_bf16 v[144:147], v[184:187], v[72:75], v[144:147]\n\t" \
  "v_mfma_f32_16x16x32_bf16 v[152:155], v[184:187], v[76:79], v[152:155]\n\t" \
  "v_mfma_f32_16x16x32_bf16 v[148:151], v[188:191], v[72:75], v[148:151]\n\t" \
  "v_mfma_f32_16x16x32_bf16 v[156:159], v[188:191], v[76:79], v[156:159]\n\t" \
  "v_mfma_f32_16x16x32_bf16 v[144:147], v[192:195], v[80:83], v[144:147]\n\t" \
  "v_mfma_f32_16x16x32_bf16 v[152:155], v[192:195], v[84:87], v[152:155]\n\t" \
  "v_mfma_f32_16x16x32_bf16 v[148:151], v[196:199], v[80:83], v[148:151]\n\t" \
  "v_mfma_f32_16x16x32_bf16 v[156:159], v[196:199], v[84:87], v[156:159]\n\t" \
  "v_mfma_f32_16x16x32_bf16 v[144:147], v[200:203], v[88:91], v[144:147]\n\t" \
  "v_mfma_f32_16x16x32_bf16 v[152:155], v[200:203], v[92:95], v[152:155]\n\t" \
  "v_mfma_f32_16x16x32_bf16 v[148:151], v[204:207], v[88:91], v[148:151]\n\t" \
  "v_mfma_f32_16x16x32_bf16 v[156:159], v[204:207], v[92:95], v[156:159]\n\t" \
  "v_mfma_f32_16x16x32_bf16 v[144:147], v[208:211], v[96:99], v[144:147]\n\t" \
  "v_mfma_f32_16x16x32_bf16 v[152:155], v[208:211], v[100:103], v[152:155]\n\t" \
  "v_mfma_f32_16x16x32_bf16 v[148:151], v[212:215], v[96:99], v[148:151]\n\t" \
  "v_mfma_f32_16x16x32_bf16 v[156:159], v[212:215], v[100:103], v[156:159]\n\t" \
  "v_mfma_f32_16x16x32_bf16 v[144:147], v[216:219], v[104:107], v[144:147]\n\t" \
  "v_mfma_f32_16x16x32_bf16 v[152:155], v[216:219], v[108:111], v[152:155]\n\t" \
  "v_mfma_f32_16x16x32_bf16 v[148:151], v[220:223], v[104:107], v[148:151]\n\t" \
  "v_mfma_f32_16x16x32_bf16 v[156:159], v[220:223], v[108:111], v[156:159]\n\t" \
  "v_mfma_f32_16x16x32_bf16 v[144:147], v[224:227], v[112:115], v[144:147]\n\t" \
  "v_mfma_f32_16x16x32_bf16 v[152:155], v[224:227], v[116:119], v[152:155]\n\t" \
  "v_mfma_f32_16x16x32_bf16 v[148:151], v[228:231], v[112:115], v[148:151]\n\t" \
  "v_mfma_f32_16x16x32_bf16 v[156:159], v[228:231], v[116:119], v[156:159]\n\t" \
  "v_mfma_f32_16x16x32_bf16 v[144:147], v[232:235], v[120:123], v[144:147]\n\t" \
  "v_mfma_f32_16x16x32_bf16 v[152:155], v[232:235], v[124:127], v[152:155]\n\t" \
  "v_mfma_f32_16x16x32_bf16 v[148:151], v[236:239], v[120:123], v[148:151]\n\t" \
  "v_mfma_f32_16x16x32_bf16 v[156:159], v[236:239], v[124:127], v[156:159]\n\t" \
  "s_setprio 0\n\t" \
  ".Ls1%=:\n\t" \
  "s_add_u32 s92, s92, 512\n\t" \
  "s_addc_u32 s93, s93, 0\n\t" \
  "ds_read_b128 v[176:179], v240 offset:0\n\t" \
  "ds_read_b128 v[180:183], v240 offset:16384\n\t" \
  "ds_read_b128 v[184:187], v241 offset:0\n\t" \
  "ds_read_b128 v[188:191], v241 offset:16384\n\t" \
  "s_add_u32 m0, %[lw], 98304\n\t" \
  "s_nop 0\n\t" \
  "global_load_lds_dwordx4 v248, s[92:93] sc1\n\t" \
  "ds_read_b128 v[192:195], v242 offset:0\n\t" \
  "ds_read_b128 v[196:199], v242 offset:16384\n\t" \
  "ds_read_b128 v[200:203], v243 offset:0\n\t" \
  "ds_read_b128 v[204:207], v243 offset:16384\n\t" \
  "s_add_u32 m0, %[lw], 99328\n\t" \
  "s_nop 0\n\t" \
  "global_load_lds_dwordx4 v249, s[92:93] sc1\n\t" \
  "ds_read_b128 v[208:211], v240 offset:256\n\t" \
  "ds_read_b128 v[212:215], v240 offset:16640\n\t" \
  "ds_read_b128 v[216:219], v241 offset:256\n\t" \
  "ds_read_b128 v[220:223], v241 offset:16640\n\t" \
  "s_add_u32 m0, %[lw], 100352\n\t" \
  "s_nop 0\n\t" \
  "global_load_lds_dwordx4 v250, s[92:93] sc1\n\t" \
  "ds_read_b128 v[224:227], v242 offset:256\n\t" \
  "ds_read_b128 v[228:231], v242 offset:16640\n\t" \
  "ds_read_b128 v[232:235], v243 offset:256\n\t" \
  "ds_read_b128 v[236:239], v243 offset:16640\n\t" \
  "s_add_u32 m0, %[lw], 101376\n\t" \
  "s_nop 0\n\t" \
  "global_load_lds_dwordx4 v251, s[92:93] sc1\n\t" \
  "ds_write_b128 v252, v[152:155] offset:0\n\t" \
  "ds_write_b128 v252, v[156:159] offset:1024\n\t" \
  "s_waitcnt lgkmcnt(0)\n\t" \
  "s_waitcnt vmcnt(4)\n\t" \
  "s_barrier\n\t" \
  "s_setprio 2\n\t" \
  "v_mfma_f32_16x16x32_bf16 v[128:131], v[176:179], v[0:3], 0\n\t" \
  "v_mfma_f32_16x16x32_bf16 v[136:139], v[176:179], v[4:7], 0\n\t" \
  "v_mfma_f32_16x16x32_bf16 v[132:135], v[180:183], v[0:3], 0\n\t" \
  "v_mfma_f32_16x16x32_bf16 v[140:143], v[180:183], v[4:7], 0\n\t" \
  "v_mfma_f32_16x16x32_bf16 v[128:131], v[184:187], v[8:11], v[128:131]\n\t" \
  "v_mfma_f32_16x16x32_bf16 v[136:139], v[184:187], v[12:15], v[136:139]\n\t" \
  "v_mfma_f32_16x16x32_bf16 v[132:135], v[188:191], v[8:11], v[132:135]\n\t" \
  "v_mfma_f32_16x16x32_bf16 v[140:143], v[188:191], v[12:15], v[140:143]\n\t" \
  "v_mfma_f32_16x16x32_bf16 v[128:131], v[192:195], v[16:19], v[128:131]\n\t" \
  "v_mfma_f32_16x16x32_bf16 v[136:139], v[192:195], v[20:23], v[136:139]\n\t" \
  "v_mfma_f32_16x16x32_bf16 v[132:135], v[196:199], v[16:19], v[132:135]\n\t" \
  "v_mfma_f32_16x16x32_bf16 v[140:143], v[196:199], v[20:23], v[140:143]\n\t" \
  "v_mfma_f32_16x16x32_bf16 v[128:131], v[200:203], v[24:27], v[128:131]\n\t" \
  "v_mfma_f32_16x16x32_bf16 v[136:139], v[200:203], v[28:31], v[136:139]\n\t" \
  "v_mfma_f32_16x16x32_bf16 v[132:135], v[204:207], v[24:27], v[132:135]\n\t" \
  "v_mfma_f32_16x16x32_bf16 v[140:143], v[204:207], v[28:31], v[140:143]\n\t" \
  "v_mfma_f32_16x16x32_bf16 v[128:131], v[208:211], v[32:35], v[128:131]\n\t" \
  "v_mfma_f32_16x16x32_bf16 v[136:139], v[208:211], v[36:39], v[136:139]\n\t" \
  "v_mfma_f32_16x16x32_bf16 v[132:135], v[212:215], v[32:35], v[132:135]\n\t" \
  "v_mfma_f32_16x16x32_bf16 v[140:143], v[212:215], v[36:39], v[140:143]\n\t" \
  "v_mfma_f32_16x16x32_bf16 v[128:131], v[216:219], v[40:43], v[128:131]\n\t" \
  "v_mfma_f32_16x16x32_bf16 v[136:139], v[216:219], v[44:47], v[136:139]\n\t" \
  "v_mfma_f32_16x16x32_bf16 v[132:135], v[220:223], v[40:43], v[132:135]\n\t" \
  "v_mfma_f32_16x16x32_bf16 v[140:143], v[220:223], v[44:47], v[140:143]\n\t" \
  "v_mfma_f32_16x16x32_bf16 v[128:131], v[224:227], v[48:51], v[128:131]\n\t" \
  "v_mfma_f32_16x16x32_bf16 v[136:139], v[224:227], v[52:55], v[136:139]\n\t" \
  "v_mfma_f32_16x16x32_bf16 v[132:135], v[228:231], v[48:51], v[132:135]\n\t" \
  "v_mfma_f32_16x16x32_bf16 v[140:143], v[228:231], v[52:55], v[140:143]\n\t" \
  "v_mfma_f32_16x16x32_bf16 v[128:131], v[232:235], v[56:59], v[128:131]\n\t" \
  "v_mfma_f32_16x16x32_bf16 v[136:139], v[232:235], v[60:63], v[136:139]\n\t" \
  "v_mfma_f32_16x16x32_bf16 v[132:135], v[236:239], v[56:59], v[132:135]\n\t" \
  "v_mfma_f32_16x16x32_bf16 v[140:143], v[236:239], v[60:63], v[140:143]\n\t" \
  "s_setprio 0\n\t" \
  "s_add_u32 s92, s92, 65024\n\t" \
  "s_addc_u32 s93, s93, 0\n\t" \
  "ds_read_b128 v[160:163], v253 offset:0\n\t" \
  "ds_read_b128 v[164:167], v253 offset:1024\n\t" \
  "ds_read_b128 v[176:179], v240 offset:32768\n\t" \
  "ds_read_b128 v[180:183], v240 offset:49152\n\t" \
  "ds_read_b128 v[184:187], v241 offset:32768\n\t" \
  "ds_read_b128 v[188:191], v241 offset:49152\n\t" \
  "s_add_u32 m0, %[lw], 0\n\t" \
  "s_nop 0\n\t" \
  "global_load_lds_dwordx4 v248, s[92:93] sc1\n\t" \
  "ds_read_b128 v[192:195], v242 offset:32768\n\t" \
  "ds_read_b128 v[196:199], v242 offset:49152\n\t" \
  "ds_read_b128 v[200:203], v243 offset:32768\n\t" \
  "ds_read_b128 v[204:207], v243 offset:49152\n\t" \
  "s_add_u32 m0, %[lw], 1024\n\t" \
  "s_nop 0\n\t" \
  "global_load_lds_dwordx4 v249, s[92:93] sc1\n\t" \
  "ds_read_b128 v[208:211], v240 offset:33024\n\t" \
  "ds_read_b128 v[212:215], v240 offset:49408\n\t" \
  "ds_read_b128 v[216:219], v241 offset:33024\n\t" \
  "ds_read_b128 v[220:223], v241 offset:49408\n\t" \
  "s_add_u32 m0, %[lw], 2048\n\t" \
  "s_nop 0\n\t" \
  "global_load_lds_dwordx4 v250, s[92:93] sc1\n\t" \
  "ds_read_b128 v[224:227], v242 offset:33024\n\t" \
  "ds_read_b128 v[228:231], v242 offset:49408\n\t" \
  "ds_read_b128 v[232:235], v243 offset:33024\n\t" \
  "ds_read_b128 v[236:239], v243 offset:49408\n\t" \
  "s_add_u32 m0, %[lw], 3072\n\t" \
  "s_nop 0\n\t" \
  "global_load_lds_dwordx4 v251, s[92:93] sc1\n\t" \
  "s_waitcnt lgkmcnt(15)\n\t" \
  "s_waitcnt lgkmcnt(12)\n\t" \
  "s_sub_i32 s91, s86, 2\n\t" \
  "v_mov_b32 v173, s91\n\t" \
  "buffer_store_dword v173, v174, %[rs], 0 offen sc1\n\t" \
  "s_lshr_b32 s90, s87, 7\n\t" \
  "s_xor_b32 s90, s90, %[s0]\n\t" \
  "s_and_b32 s90, s90, 1\n\t" \
  "s_lshl1_add_u32 s90, s87, s90\n\t" \
  "s_lshl_b32 s90, s90, 13\n\t" \
  "v_add_f32 v160, v160, v144\n\t" \
  "v_add_f32 v161, v161, v145\n\t" \
  "v_add_f32 v162, v162, v146\n\t" \
  "v_add_f32 v163, v163, v147\n\t" \
  "v_add_f32 v164, v164, v148\n\t" \
  "v_add_f32 v165, v165, v149\n\t" \
  "v_add_f32 v166, v166, v150\n\t" \
  "v_add_f32 v167, v167, v151\n\t" \
  "v_cvt_pk_bf16_f32 v168, v160, v161\n\t" \
  "v_cvt_pk_bf16_f32 v169, v162, v163\n\t" \
  "v_cvt_pk_bf16_f32 v170, v164, v165\n\t" \
  "v_cvt_pk_bf16_f32 v171, v166, v167\n\t" \
  "buffer_store_dwordx4 v[168:171], v172, %[rt], s90 offen sc1\n\t" \
  "s_add_u32 s86, s86, 1\n\t" \
  "s_add_u32 s87, s87, 1\n\t" \
  "s_waitcnt lgkmcnt(0)\n\t" \
  "s_waitcnt vmcnt(10)\n\t" \
  "s_barrier\n\t" \
  "s_setprio 2\n\t" \
  "v_mfma_f32_16x16x32_bf16 v[128:131], v[176:179], v[64:67], v[128:131]\n\t" \
  "v_mfma_f32_16x16x32_bf16 v[136:139], v[176:179], v[68:71], v[136:139]\n\t" \
  "v_mfma_f32_16x16x32_bf16 v[132:135], v[180:183], v[64:67], v[132:135]\n\t" \
  "v_mfma_f32_16x16x32_bf16 v[140:143], v[180:183], v[68:71], v[140:143]\n\t" \
  "v_mfma_f32_16x16x32_bf16 v[128:131], v[184:187], v[72:75], v[128:131]\n\t" \
  "v_mfma_f32_16x16x32_bf16 v[136:139], v[184:187], v[76:79], v[136:139]\n\t" \
  "v_mfma_f32_16x16x32_bf16 v[132:135], v[188:191], v[72:75], v[132:135]\n\t" \
  "v_mfma_f32_16x16x32_bf16 v[140:143], v[188:191], v[76:79], v[140:143]\n\t" \
  "v_mfma_f32_16x16x32_bf16 v[128:131], v[192:195], v[80:83], v[128:131]\n\t" \
  "v_mfma_f32_16x16x32_bf16 v[136:139], v[192:195], v[84:87], v[136:139]\n\t" \
  "v_mfma_f32_16x16x32_bf16 v[132:135], v[196:199], v[80:83], v[132:135]\n\t" \
  "v_mfma_f32_16x16x32_bf16 v[140:143], v[196:199], v[84:87], v[140:143]\n\t" \
  "v_mfma_f32_16x16x32_bf16 v[128:131], v[200:203], v[88:91], v[128:131]\n\t" \
  "v_mfma_f32_16x16x32_bf16 v[136:139], v[200:203], v[92:95], v[136:139]\n\t" \
  "v_mfma_f32_16x16x32_bf16 v[132:135], v[204:207], v[88:91], v[132:135]\n\t" \
  "v_mfma_f32_16x16x32_bf16 v[140:143], v[204:207], v[92:95], v[140:143]\n\t" \
  "v_mfma_f32_16x16x32_bf16 v[128:131], v[208:211], v[96:99], v[128:131]\n\t" \
  "v_mfma_f32_16x16x32_bf16 v[136:139], v[208:211], v[100:103], v[136:139]\n\t" \
  "v_mfma_f32_16x16x32_bf16 v[132:135], v[212:215], v[96:99], v[132:135]\n\t" \
  "v_mfma_f32_16x16x32_bf16 v[140:143], v[212:215], v[100:103], v[140:143]\n\t" \
  "v_mfma_f32_16x16x32_bf16 v[128:131], v[216:219], v[104:107], v[128:131]\n\t" \
  "v_mfma_f32_16x16x32_bf16 v[136:139], v[216:219], v[108:111], v[136:139]\n\t" \
  "v_mfma_f32_16x16x32_bf16 v[132:135], v[220:223], v[104:107], v[132:135]\n\t" \
  "v_mfma_f32_16x16x32_bf16 v[140:143], v[220:223], v[108:111], v[140:143]\n\t" \
  "v_mfma_f32_16x16x32_bf16 v[128:131], v[224:227], v[112:115], v[128:131]\n\t" \
  "v_mfma_f32_16x16x32_bf16 v[136:139], v[224:227], v[116:119], v[136:139]\n\t" \
  "v_mfma_f32_16x16x32_bf16 v[132:135], v[228:231], v[112:115], v[132:135]\n\t" \
  "v_mfma_f32_16x16x32_bf16 v[140:143], v[228:231], v[116:119], v[140:143]\n\t" \
  "v_mfma_f32_16x16x32_bf16 v[128:131], v[232:235], v[120:123], v[128:131]\n\t" \
  "v_mfma_f32_16x16x32_bf16 v[136:139], v[232:235], v[124:127], v[136:139]\n\t" \
  "v_mfma_f32_16x16x32_bf16 v[132:135], v[236:239], v[120:123], v[132:135]\n\t" \
  "v_mfma_f32_16x16x32_bf16 v[140:143], v[236:239], v[124:127], v[140:143]\n\t" \
  "s_setprio 0\n\t" \
  "s_add_u32 s92, s92, 512\n\t" \
  "s_addc_u32 s93, s93, 0\n\t" \
  "ds_read_b128 v[176:179], v244 offset:0\n\t" \
  "ds_read_b128 v[180:183], v244 offset:16384\n\t" \
  "ds_read_b128 v[184:187], v245 offset:0\n\t" \
  "ds_read_b128 v[188:191], v245 offset:16384\n\t" \
  "s_add_u32 m0, %[lw], 32768\n\t" \
  "s_nop 0\n\t" \
  "global_load_lds_dwordx4 v248, s[92:93] sc1\n\t" \
  "ds_read_b128 v[192:195], v246 offset:0\n\t" \
  "ds_read_b128 v[196:199], v246 offset:16384\n\t" \
  "ds_read_b128 v[200:203], v247 offset:0\n\t" \
  "ds_read_b128 v[204:207], v247 offset:16384\n\t" \
  "s_add_u32 m0, %[lw], 33792\n\t" \
  "s_nop 0\n\t" \
  "global_load_lds_dwordx4 v249, s[92:93] sc1\n\t" \
  "ds_read_b128 v[208:211], v244 offset:256\n\t" \
  "ds_read_b128 v[212:215], v244 offset:16640\n\t" \
  "ds_read_b128 v[216:219], v245 offset:256\n\t" \
  "ds_read_b128 v[220:223], v245 offset:16640\n\t" \
  "s_add_u32 m0, %[lw], 34816\n\t" \
  "s_nop 0\n\t" \
  "global_load_lds_dwordx4 v250, s[92:93] sc1\n\t" \
  "ds_read_b128 v[224:227], v246 offset:256\n\t" \
  "ds_read_b128 v[228:231], v246 offset:16640\n\t" \
  "ds_read_b128 v[232:235], v247 offset:256\n\t" \
  "ds_read_b128 v[236:239], v247 offset:16640\n\t" \
  "s_add_u32 m0, %[lw], 35840\n\t" \
  "s_nop 0\n\t" \
  "global_load_lds_dwordx4 v251, s[92:93] sc1\n\t" \
  "ds_write_b128 v252, v[136:139] offset:0\n\t" \
  "ds_write_b128 v252, v[140:143] offset:1024\n\t" \
  "s_waitcnt lgkmcnt(0)\n\t" \
  "s_waitcnt vmcnt(4)\n\t" \
  "s_barrier\n\t" \
  "s_setprio 2\n\t" \
  "v_mfma_f32_16x16x32_bf16 v[144:147], v[176:179], v[0:3], 0\n\t" \
  "v_mfma_f32_16x16x32_bf16 v[152:155], v[176:179], v[4:7], 0\n\t" \
  "v_mfma_f32_16x16x32_bf16 v[148:151], v[180:183], v[0:3], 0\n\t" \
  "v_mfma_f32_16x16x32_bf16 v[156:159], v[180:183], v[4:7], 0\n\t" \
  "v_mfma_f32_16x16x32_bf16 v[144:147], v[184:187], v[8:11], v[144:147]\n\t" \
  "v_mfma_f32_16x16x32_bf16 v[152:155], v[184:187], v[12:15], v[152:155]\n\t" \
  "v_mfma_f32_16x16x32_bf16 v[148:151], v[188:191], v[8:11], v[148:151]\n\t" \
  "v_mfma_f32_16x16x32_bf16 v[156:159], v[188:191], v[12:15], v[156:159]\n\t" \
  "v_mfma_f32_16x16x32_bf16 v[144:147], v[192:195], v[16:19], v[144:147]\n\t" \
  "v_mfma_f32_16x16x32_bf16 v[152:155], v[192:195], v[20:23], v[152:155]\n\t" \
  "v_mfma_f32_16x16x32_bf16 v[148:151], v[196:199], v[16:19], v[148:151]\n\t" \
  "v_mfma_f32_16x16x32_bf16 v[156:159], v[196:199], v[20:23], v[156:159]\n\t" \
  "v_mfma_f32_16x16x32_bf16 v[144:147], v[200:203], v[24:27], v[144:147]\n\t" \
  "v_mfma_f32_16x16x32_bf16 v[152:155], v[200:203], v[28:31], v[152:155]\n\t" \
  "v_mfma_f32_16x16x32_bf16 v[148:151], v[204:207], v[24:27], v[148:151]\n\t" \
  "v_mfma_f32_16x16x32_bf16 v[156:159], v[204:207], v[28:31], v[156:159]\n\t" \
  "v_mfma_f32_16x16x32_bf16 v[144:147], v[208:211], v[32:35], v[144:147]\n\t" \
  "v_mfma_f32_16x16x32_bf16 v[152:155], v[208:211], v[36:39], v[152:155]\n\t" \
  "v_mfma_f32_16x16x32_bf16 v[148:151], v[212:215], v[32:35], v[148:151]\n\t" \
  "v_mfma_f32_16x16x32_bf16 v[156:159], v[212:215], v[36:39], v[156:159]\n\t" \
  "v_mfma_f32_16x16x32_bf16 v[144:147], v[216:219], v[40:43], v[144:147]\n\t" \
  "v_mfma_f32_16x16x32_bf16 v[152:155], v[216:219], v[44:47], v[152:155]\n\t" \
  "v_mfma_f32_16x16x32_bf16 v[148:151], v[220:223], v[40:43], v[148:151]\n\t" \
  "v_mfma_f32_16x16x32_bf16 v[156:159], v[220:223], v[44:47], v[156:159]\n\t" \
  "v_mfma_f32_16x16x32_bf16 v[144:147], v[224:227], v[48:51], v[144:147]\n\t" \
  "v_mfma_f32_16x16x32_bf16 v[152:155], v[224:227], v[52:55], v[152:155]\n\t" \
  "v_mfma_f32_16x16x32_bf16 v[148:151], v[228:231], v[48:51], v[148:151]\n\t" \
  "v_mfma_f32_16x16x32_bf16 v[156:159], v[228:231], v[52:55], v[156:159]\n\t" \
  "v_mfma_f32_16x16x32_bf16 v[144:147], v[232:235], v[56:59], v[144:147]\n\t" \
  "v_mfma_f32_16x16x32_bf16 v[152:155], v[232:235], v[60:63], v[152:155]\n\t" \
  "v_mfma_f32_16x16x32_bf16 v[148:151], v[236:239], v[56:59], v[148:151]\n\t" \
  "v_mfma_f32_16x16x32_bf16 v[156:159], v[236:239], v[60:63], v[156:159]\n\t" \
  "s_setprio 0\n\t" \
  "s_add_u32 s92, s92, 65024\n\t" \
  "s_addc_u32 s93, s93, 0\n\t" \
  "ds_read_b128 v[160:163], v253 offset:0\n\t" \
  "ds_read_b128 v[164:167], v253 offset:1024\n\t" \
  "ds_read_b128 v[176:179], v244 offset:32768\n\t" \
  "ds_read_b128 v[180:183], v244 offset:49152\n\t" \
  "ds_read_b128 v[184:187], v245 offset:32768\n\t" \
  "ds_read_b128 v[188:191], v245 offset:49152\n\t" \
  "s_add_u32 m0, %[lw], 65536\n\t" \
  "s_nop 0\n\t" \
  "global_load_lds_dwordx4 v248, s[92:93] sc1\n\t" \
  "ds_read_b128 v[192:195], v246 offset:32768\n\t" \
  "ds_read_b128 v[196:199], v246 offset:49152\n\t" \
  "ds_read_b128 v[200:203], v247 offset:32768\n\t" \
  "ds_read_b128 v[204:207], v247 offset:49152\n\t" \
  "s_add_u32 m0, %[lw], 66560\n\t" \
  "s_nop 0\n\t" \
  "global_load_lds_dwordx4 v249, s[92:93] sc1\n\t" \
  "ds_read_b128 v[208:211], v244 offset:33024\n\t" \
  "ds_read_b128 v[212:215], v244 offset:49408\n\t" \
  "ds_read_b128 v[216:219], v245 offset:33024\n\t" \
  "ds_read_b128 v[220:223], v245 offset:49408\n\t" \
  "s_add_u32 m0, %[lw], 67584\n\t" \
  "s_nop 0\n\t" \
  "global_load_lds_dwordx4 v250, s[92:93] sc1\n\t" \
  "ds_read_b128 v[224:227], v246 offset:33024\n\t" \
  "ds_read_b128 v[228:231], v246 offset:49408\n\t" \
  "ds_read_b128 v[232:235], v247 offset:33024\n\t" \
  "ds_read_b128 v[236:239], v247 offset:49408\n\t" \
  "s_add_u32 m0, %[lw], 68608\n\t" \
  "s_nop 0\n\t" \
  "global_load_lds_dwordx4 v251, s[92:93] sc1\n\t" \
  "s_waitcnt lgkmcnt(15)\n\t" \
  "s_waitcnt lgkmcnt(12)\n\t" \
  "s_sub_i32 s91, s86, 2\n\t" \
  "v_mov_b32 v173, s91\n\t" \
  "buffer_store_dword v173, v174, %[rs], 0 offen sc1\n\t" \
  "s_lshr_b32 s90, s87, 7\n\t" \
  "s_xor_b32 s90, s90, %[s0]\n\t" \
  "s_and_b32 s90, s90, 1\n\t" \
  "s_lshl1_add_u32 s90, s87, s90\n\t" \
  "s_lshl_b32 s90, s90, 13\n\t" \
  "v_add_f32 v160, v160, v128\n\t" \
  "v_add_f32 v161, v161, v129\n\t" \
  "v_add_f32 v162, v162, v130\n\t" \
  "v_add_f32 v163, v163, v131\n\t" \
  "v_add_f32 v164, v164, v132\n\t" \
  "v_add_f32 v165, v165, v133\n\t" \
  "v_add_f32 v166, v166, v134\n\t" \
  "v_add_f32 v167, v167, v135\n\t" \
  "v_cvt_pk_bf16_f32 v168, v160, v161\n\t" \
  "v_cvt_pk_bf16_f32 v169, v162, v163\n\t" \
  "v_cvt_pk_bf16_f32 v170, v164, v165\n\t" \
  "v_cvt_pk_bf16_f32 v171, v166, v167\n\t" \
  "buffer_store_dwordx4 v[168:171], v172, %[rt], s90 offen sc1\n\t" \
  "s_add_u32 s86, s86, 1\n\t" \
  "s_add_u32 s87, s87, 1\n\t" \
  "s_waitcnt lgkmcnt(0)\n\t" \
  "s_cmp_lt_u32 s86, 64\n\t" \
  "s_cbranch_scc1 .Lp1%=\n\t" \
  "s_barrier\n\t" \
  "s_setprio 2\n\t" \
  "v_mfma_f32_16x16x32_bf16 v[144:147], v[176:179], v[64:67], v[144:147]\n\t" \
  "v_mfma_f32_16x16x32_bf16 v[152:155], v[176:179], v[68:71], v[152:155]\n\t" \
  "v_mfma_f32_16x16x32_bf16 v[148:151], v[180:183], v[64:67], v[148:151]\n\t" \
  "v_mfma_f32_16x16x32_bf16 v[156:159], v[180:183], v[68:71], v[156:159]\n\t" \
  "v_mfma_f32_16x16x32_bf16 v[144:147], v[184:187], v[72:75], v[144:147]\n\t" \
  "v_mfma_f32_16x16x32_bf16 v[152:155], v[184:187], v[76:79], v[152:155]\n\t" \
  "v_mfma_f32_16x16x32_bf16 v[148:151], v[188:191], v[72:75], v[148:151]\n\t" \
  "v_mfma_f32_16x16x32_bf16 v[156:159], v[188:191], v[76:79], v[156:159]\n\t" \
  "v_mfma_f32_16x16x32_bf16 v[144:147], v[192:195], v[80:83], v[144:147]\n\t" \
  "v_mfma_f32_16x16x32_bf16 v[152:155], v[192:195], v[84:87], v[152:155]\n\t" \
  "v_mfma_f32_16x16x32_bf16 v[148:151], v[196:199], v[80:83], v[148:151]\n\t" \
  "v_mfma_f32_16x16x32_bf16 v[156:159], v[196:199], v[84:87], v[156:159]\n\t" \
  "v_mfma_f32_16x16x32_bf16 v[144:147], v[200:203], v[88:91], v[144:147]\n\t" \
  "v_mfma_f32_16x16x32_bf16 v[152:155], v[200:203], v[92:95], v[152:155]\n\t" \
  "v_mfma_f32_16x16x32_bf16 v[148:151], v[204:207], v[88:91], v[148:151]\n\t" \
  "v_mfma_f32_16x16x32_bf16 v[156:159], v[204:207], v[92:95], v[156:159]\n\t" \
  "v_mfma_f32_16x16x32_bf16 v[144:147], v[208:211], v[96:99], v[144:147]\n\t" \
  "v_mfma_f32_16x16x32_bf16 v[152:155], v[208:211], v[100:103], v[152:155]\n\t" \
  "v_mfma_f32_16x16x32_bf16 v[148:151], v[212:215], v[96:99], v[148:151]\n\t" \
  "v_mfma_f32_16x16x32_bf16 v[156:159], v[212:215], v[100:103], v[156:159]\n\t" \
  "v_mfma_f32_16x16x32_bf16 v[144:147], v[216:219], v[104:107], v[144:147]\n\t" \
  "v_mfma_f32_16x16x32_bf16 v[152:155], v[216:219], v[108:111], v[152:155]\n\t" \
  "v_mfma_f32_16x16x32_bf16 v[148:151], v[220:223], v[104:107], v[148:151]\n\t" \
  "v_mfma_f32_16x16x32_bf16 v[156:159], v[220:223], v[108:111], v[156:159]\n\t" \
  "v_mfma_f32_16x16x32_bf16 v[144:147], v[224:227], v[112:115], v[144:147]\n\t" \
  "v_mfma_f32_16x16x32_bf16 v[152:155], v[224:227], v[116:119], v[152:155]\n\t" \
  "v_mfma_f32_16x16x32_bf16 v[148:151], v[228:231], v[112:115], v[148:151]\n\t" \
  "v_mfma_f32_16x16x32_bf16 v[156:159], v[228:231], v[116:119], v[156:159]\n\t" \
  "v_mfma_f32_16x16x32_bf16 v[144:147], v[232:235], v[120:123], v[144:147]\n\t" \
  "v_mfma_f32_16x16x32_bf16 v[152:155], v[232:235], v[124:127], v[152:155]\n\t" \
  "v_mfma_f32_16x16x32_bf16 v[148:151], v[236:239], v[120:123], v[148:151]\n\t" \
  "v_mfma_f32_16x16x32_bf16 v[156:159], v[236:239], v[124:127], v[156:159]\n\t" \
  "s_setprio 0\n\t" \
  "s_nop 4\n\t" \
  "ds_write_b128 v252, v[152:155] offset:0\n\t" \
  "s_nop 0\n\t" \
  "ds_write_b128 v252, v[156:159] offset:1024\n\t" \
  "s_waitcnt lgkmcnt(0)\n\t" \
  "s_barrier\n\t" \
  "ds_read_b128 v[160:163], v253 offset:0\n\t" \
  "ds_read_b128 v[164:167], v253 offset:1024\n\t" \
  "s_waitcnt lgkmcnt(0)\n\t" \
  "s_lshr_b32 s90, s87, 7\n\t" \
  "s_xor_b32 s90, s90, %[s0]\n\t" \
  "s_and_b32 s90, s90, 1\n\t" \
  "s_lshl1_add_u32 s90, s87, s90\n\t" \
  "s_lshl_b32 s90, s90, 13\n\t" \
  "v_add_f32 v160, v160, v144\n\t" \
  "v_add_f32 v161, v161, v145\n\t" \
  "v_add_f32 v162, v162, v146\n\t" \
  "v_add_f32 v163, v163, v147\n\t" \
  "v_add_f32 v164, v164, v148\n\t" \
  "v_add_f32 v165, v165, v149\n\t" \
  "v_add_f32 v166, v166, v150\n\t" \
  "v_add_f32 v167, v167, v151\n\t" \
  "v_cvt_pk_bf16_f32 v168, v160, v161\n\t" \
  "v_cvt_pk_bf16_f32 v169, v162, v163\n\t" \
  "v_cvt_pk_bf16_f32 v170, v164, v165\n\t" \
  "v_cvt_pk_bf16_f32 v171, v166, v167\n\t" \
  "buffer_store_dwordx4 v[168:171], v172, %[rt], s90 offen sc1\n\t" \
  "s_waitcnt vmcnt(0)\n\t" \
  "s_barrier\n\t" \
  "s_sub_i32 s91, s86, 1\n\t" \
  "v_mov_b32 v173, s91\n\t" \
  "buffer_store_dword v173, v174, %[rs], 0 offen sc1\n\t" \
  "s_mov_b32 m0, s94\n\t" \
  ".Le%=:\n\t"
constexpr int W_X = 131072, W_PM = W_X + 16384;
template <bool ISV>
__device__ __forceinline__ void job_proj(Args& p, int g, int which, int h, char* smem, __amdgpu_buffer_rsrc_t rsy) {
  const int tid = threadIdx.x, lane = tid & 63, r = lane & 15, q = lane >> 4;
  const int w = __builtin_amdgcn_readfirstlane(tid >> 6), kh = w >> 2, cg = w & 3;
  u32x32 W[4];
  const unsigned lw = lds_off(smem) + w * 4096;
  u32x4 vx;
  u32x2 tag;
  const char* xn = p.ws + WS_XN;
  auto row = [&](int t, int i) {
    const char* src = xn + (size_t)(min(t, 127) >> 1) * (SEG * XLD * 2) + (t & 1) * 512;
    unsigned keep;
    asm volatile("s_mov_b32 %0, m0\n\ts_mov_b32 m0, %3\n\ts_nop 0\n\tglobal_load_lds_dwordx4 %1, %2 sc1\n\ts_mov_b32 m0, %0"
                 : "=&s"(keep) : "v"(vx[i]), "s"(src), "s"(lw + (t & 3) * 32768 + i * 1024) : "memory");
  };
  {
    const u16* Wm = which == 0 ? p.Wk : p.Wq;
    asm volatile("" : "+s"(Wm));
    if (ISV) Wm = p.Wv;
    const unsigned vo = (tid >> 4) * (HD * 2) + 16 * ((lane & 15) ^ swzB((tid >> 4) & 15));
    auto stage = [&](int j) {
      const char* src = (const char*)(Wm + (size_t)(WREV ? 7 - j : j) * 64 * HD + h * DH);
      const unsigned dst = lds_off(smem) + (j & 3) * 32768 + w * 1024;
      glds2(src, 32 * HD * 2, vo, dst);
      glds2(src + 512 * HD * 2, 32 * HD * 2, vo, dst + 16384);
    };
    const unsigned own = min((h & LN_M) ^ LN_X, 1);
    tag = start_rows<16>(p, 32 + g, own, [&]() { stage(0); stage(1); stage(2); stage(3); });
#pragma unroll
    for (int i = 0; i < 4; ++i) vx[i] = ((4 * w + i) ^ (ISV ? 0 : (lane >> 1) & 16)) * (XLD * 2) + (lane >> 5) * 1024 + (((lane & 31) ^ ((4 * w + i) & 15)) << 4);
    Boot bt;
#pragma unroll
    for (int j = 0; j < 8; ++j) {
      if (j < 4) WAIT_V(16); else if (j == 4) WAIT_V(12); else if (j == 5) WAIT_V(8); else if (j == 6) WAIT_V(4); else WAIT_V(10);
      if (j == 6 && w == 0) lds_at(smem + W_PM)[0] = boot_done(p.ws, bt, true, tag);
      LBAR();
      if (j == 4 && own) report_rows(p, 32 + g, tag);
      const char* sb = smem + (j & 3) * 32768 + kh * 16384;
#pragma unroll
      for (int k = 0; k < 4; ++k) {
        const int ks = k >> 1, a = k & 1;
        const unsigned ch = ISV ? 2 * (2 * cg + (a ^ kh)) + ((r & 3) >> 1) : 4 * cg + (r & 3), by = ISV ? 8 * (r & 1) : 8 * a;
        s16x4 lo = __builtin_amdgcn_ds_read_tr16_b64_v4i16((lds_s16x4_ptr)(sb + offB(32 * ks + 8 * q + (r >> 2), ch) + by));
        s16x4 hi = __builtin_amdgcn_ds_read_tr16_b64_v4i16((lds_s16x4_ptr)(sb + offB(32 * ks + 8 * q + 4 + (r >> 2), ch) + by));
        const int f = 4 * (WREV ? 7 - j : j) + k;
        put4(W[f >> 3], f & 7, (bf16x8){lo[0], lo[1], lo[2], lo[3], hi[0], hi[1], hi[2], hi[3]});
      }
      LBAR();
      if (j == 3 && w == 0) boot_ask(p.ws, bt);
      if (j + 4 < 8) stage(j + 4);
      if (j == 6) {
#pragma unroll
        for (int i = 0; i < 10; ++i) row(i >> 2, i & 3);
      }
    }
    p.s0 = uni(lds_at(smem + W_PM)[0] >> 31);
  }
  typedef __attribute__((ext_vector_type(8))) unsigned u32x8;
  u32x8 la;
#pragma unroll
  for (int i = 0; i < 8; ++i) la[i] = (i >> 2) * 65536 + r * 1024 + kh * 512 + (((4 * (i & 3) + q) ^ r) << 4);
  const unsigned xw = W_X + w * 2048 + lane * 16;
  u32x32 acc;
  asm volatile("" : "={v[128:159]}"(acc));
  WAIT_V(6);
  LBAR();
  {
    const int g0 = 0;
#define PR_LOOP(TXT)                                                                                                              \
    asm volatile(TXT                                                                                                              \
        : "+{v[128:159]}"(acc)                                                                                                    \
        : W4_IN, "{v[208:215]}"(la), "{v[216:219]}"(vx), "{v220}"(xw), "{v221}"(xw ^ 8192),                                       \
          "{v172}"((ISV ? 2 * cg + kh : 4 * kh + cg) * 1024 + lane * 16), "{v174}"(tid == 256 ? fkq(h, which) : 0x7F000000),      \
          [rt] "s"(raw_rsrc(p.ws + WS_T, T_BYTES)), [rs] "s"(raw_rsrc(p.ws + WS_SY, SY_TOTAL)), [s0] "s"(p.s0 ^ TF_KQV),          \
          [lw] "s"(lw), [kh] "s"(kh), [g0] "s"(g0), [end] "s"(g0 ? NSEG : PR_G1), [x0] "s"(xn + (g0 + 1) * (SEG * XLD * 2)),                       \
          [t0] "s"(which * T_Q + kseg(0, h) - 1)                                                                                  \
        : CLB8(160, 161, 162, 163, 164, 165, 166, 167), "v168", "v169", "v170", "v171", "v173", CLB8(176, 177, 178, 179, 180, 181, 182, 183), \
          CLB8(184, 185, 186, 187, 188, 189, 190, 191), CLB8(192, 193, 194, 195, 196, 197, 198, 199),                             \
          CLB8(200, 201, 202, 203, 204, 205, 206, 207), "s86", "s87", "s88", "s90", "s91", "s92", "s93", "s94", "scc", "memory")
    if (ISV) PR_LOOP(PR_ASM_1); else PR_LOOP(PR_ASM_0);
    if (w == 0 && g0 == 0) wait_rows(p.ws, tag);
  }
#define PP_LOOP(TXT)                                                                                                              \
  asm volatile(TXT                                                                                                                \
      : "+{v[128:159]}"(acc)                                                                                                      \
      : W4_IN, "{v[240:247]}"(la), "{v[248:251]}"(vx), "{v252}"(xw), "{v253}"(xw ^ 8192),                                         \
        "{v172}"((ISV ? 2 * cg + kh : 4 * kh + cg) * 1024 + lane * 16), "{v174}"(tid == 256 ? fkq(h, which) : 0x7F000000),        \
        [rt] "s"(raw_rsrc(p.ws + WS_T, T_BYTES)), [rs] "s"(raw_rsrc(p.ws + WS_SY, SY_TOTAL)), [s0] "s"(p.s0 ^ TF_KQV),            \
        [lw] "s"(lw), [kh] "s"(kh), [g0] "n"(PR_G1), [x0] "s"(xn + (PR_G1 + 1) * (SEG * XLD * 2)),                                \
        [t0] "s"(which * T_Q + kseg(0, h) - 1)                                                                                    \
      : CLB8(160, 161, 162, 163, 164, 165, 166, 167), "v168", "v169", "v170", "v171", "v173", CLB8(176, 177, 178, 179, 180, 181, 182, 183), \
        CLB8(184, 185, 186, 187, 188, 189, 190, 191), CLB32(192), CLB8(224, 225, 226, 227, 228, 229, 230, 231),                   \
        CLB8(232, 233, 234, 235, 236, 237, 238, 239), "s86", "s87", "s90", "s91", "s92", "s93", "s94", "scc", "memory")
  if (ISV) PP_LOOP(PP_ASM_1); else PP_LOOP(PP_ASM_0);
}

enum { J_NONE = 0, J_GU = 3, J_PR = 5, J_CB = 8, J_DN = 14 };
struct BigJob { int type, panel, idx; };
struct BigPtr { const char *a, *b0, *b1; };
constexpr int G_LDA = XLD * 2, G_LDB = FF * 2;

__device__ __forceinline__ BigPtr big_ptrs(const Args& p, const BigJob& j) {
  BigPtr r;
  r.a = p.ws + WS_XN + (size_t)j.panel * 128 * G_LDA;
  r.b0 = (const char*)(p.wg + j.idx * 128); r.b1 = (const char*)(p.wu + j.idx * 128);
  return r;
}
__device__ __forceinline__ void big_publish(const Args& p, const BigJob& j) {
  int tid = threadIdx.x;
  asm volatile("" : "+v"(tid));
  if (j.type == J_GU && tid == 0)
    __hip_atomic_fetch_add((gu32*)(p.ws + WS_SY + SY_CH) + j.panel * 16, 1u, __ATOMIC_RELAXED, __HIP_MEMORY_SCOPE_AGENT);
}
typedef f32x4 Acc[2][2][4][2];
__device__ __forceinline__ void big_epilogue(const Args& p, const BigJob& j, Acc& acc, char* smem) {
  int tid = threadIdx.x;
  asm volatile("" : "+v"(tid));
  const int lane = tid & 63;
  const int wid = __builtin_amdgcn_readfirstlane(tid >> 6);
  const int wr = wid >> 2, wc = wid & 3;
  {
    const __amdgpu_buffer_rsrc_t ro = mkrsrc(p.ws + WS_HF, S_LEN * 16384);
#pragma unroll
    for (int i = 0; i < 8; ++i) {
      f32x4 hh[2];
#pragma unroll
      for (int n = 0; n < 2; ++n)
#pragma unroll
        for (int jj = 0; jj < 4; ++jj) {
          const float g = acc[i >> 2][0][i & 3][n][jj], u = acc[i >> 2][1][i & 3][n][jj];
          hh[n][jj] = __builtin_amdgcn_rcpf(1.0f + __builtin_amdgcn_exp2f(g * -1.4426950408889634f)) * u;
        }
      st_sc1(ro, h_off(p, j.panel + (i >> 2)) + (j.idx * 4 + wc) * 16384 + (wr * 4 + (i & 3)) * 1024 + lane * 16,
             ((u32x4){cvtpk(hh[0][0], hh[0][1]), cvtpk(hh[0][2], hh[0][3]), cvtpk(hh[1][0], hh[1][1]), cvtpk(hh[1][2], hh[1][3])}));
    }
  }
}
__device__ __forceinline__ void gemm_role(Args& p, int g, char* smem) {
  const __amdgpu_buffer_rsrc_t rsy = mkrsrc(p.ws + WS_SY, SY_TOTAL);
  const int x = g & 3, k = g >> 2;
  volatile unsigned* slot = reinterpret_cast<volatile unsigned*>(smem + 163824);
  int rnd = k < 24 ? 2 : -1;
  auto claim = [&]() {
    ++rnd;
    const int n = k < 24 ? 32 + k : rnd * 8 + k - 24, pn = (n * 37) >> 8;
    BigJob b;
    b.type = rnd < 4 ? J_GU : (rnd == 4 ? J_DN : (rnd == 5 && k >= 24 && k - 24 < 14 - NFUSE ? J_CB : J_NONE));
    b.panel = rnd < 4 ? 14 - 2 * pn : (rnd == 4 ? 4 * ((2 - (k >> 3)) & 3) + x : NFUSE + k - 24);
    b.idx = rnd < 4 ? x + 4 * (n - 7 * pn) : k & 7;
    return b;
  };
  if (k < 24) {
    if (k >= 16) job_proj<true>(p, g, 2, x * 8 + (k & 7), smem, rsy);
    else job_proj<false>(p, g, k >> 3, x * 8 + (k & 7), smem, rsy);
  } else {
    u32x2 tag;
#pragma unroll 1
    for (int u = 0; u < (k < 30 && LN_M ? 2 : 1); ++u) {
      const int rg = u ? 32 + (4 * (k - 24) + 1) * 4 + x : g - 96;
      tag = start_rows<0>(p, rg, 1, []() {});
      WAIT_V(0);
      LBAR();
      report_rows(p, rg, tag);
    }
#pragma unroll 1
    for (int i = 0; i < PF_N; ++i) {
      const int m = i % 3, o = ((i / 3) * 32 + g - 96) * 448 + (int)threadIdx.x;
      if (threadIdx.x < 448) touch_line((const char*)(m == 0 ? p.wg : (m == 1 ? p.wu : p.wd)) + (size_t)o * 128, smem + (threadIdx.x >> 6) * 256);
      WAIT_V(1);
    }
    WAIT_V(0);
    if (threadIdx.x < 64) {
      wait_rows(p.ws, tag);
      Boot bt;
      boot_ask(p.ws, bt);
      WAIT_V(0);
      *slot = boot_done(p.ws, bt, true, tag);
    }
    LBAR();
    p.s0 = *slot >> 31;
  }
  p.s0 = uni(p.s0);
  BigJob cur = claim();
  while (cur.type != J_NONE) {
    if (cur.type != J_GU) {
      if (cur.type == J_DN) job_down(p, cur.panel, cur.idx, smem, rsy);
      else job_comb(p, cur.panel, x, rsy);
      cur = claim();
      continue;
    }
    int tid = threadIdx.x;
    asm volatile("" : "+v"(tid));
    const int lane = tid & 63;
    const int wid = __builtin_amdgcn_readfirstlane(tid >> 6);
    const int wr = wid >> 2, wc = wid & 3;
    const unsigned ra = tid >> 3, ca = 16 * ((tid & 7) ^ ((tid >> 3) & 7)), rb = tid >> 4, cb = 16 * ((tid & 15) ^ swzB((tid >> 4) & 15));
    const unsigned lw = lds_off(smem) + wid * 1024;
#define B8_SA(b, h) (((b) * 2 + (h)) * 16384)
#define B8_SB(b, h) ((4 + (b) * 2 + (h)) * 16384)
#define B8_STA(b, h, P, kt) glds2<true>((P).a + (size_t)(h) * 128 * G_LDA + (kt) * 128, 64 * G_LDA, ra * G_LDA + ca, lw + B8_SA(b, h))
#define B8_STB(b, h, P, kt) glds2(((h) ? (P).b1 : (P).b0) + (size_t)(kt) * 64 * G_LDB, 32 * G_LDB, rb * G_LDB + cb, lw + B8_SB(b, h))
#define B8_LDA(b, h) do { _Pragma("unroll") for (int m = 0; m < 4; ++m) _Pragma("unroll") for (int kk = 0; kk < 2; ++kk) \
      At[m][kk] = ldsf(smem + B8_SA(b, h) + offA(wr * 64 + m * 16 + (lane & 15), kk * 4 + (lane >> 4))); } while (0)
#define B8_LDB(dst, b, h) do { _Pragma("unroll") for (int n = 0; n < 2; ++n) _Pragma("unroll") for (int kk = 0; kk < 2; ++kk) \
      dst[n][kk] = ld_bfrag(smem + B8_SB(b, h), kk, wc * 2 + n, lane); } while (0)
#define B8_MMA(ai, bj, Bt) do { _Pragma("unroll") for (int m = 0; m < 4; ++m) _Pragma("unroll") for (int n = 0; n < 2; ++n) \
      _Pragma("unroll") for (int kk = 0; kk < 2; ++kk) acc[ai][bj][m][n] = MFMA(Bt[n][kk], At[m][kk], acc[ai][bj][m][n]); } while (0)
#define WAIT_L(n) asm volatile("s_waitcnt lgkmcnt(%0)" :: "n"(n) : "memory")
#define BAR() __builtin_amdgcn_s_barrier()
#define BAR_1() do { SCHED(); asm volatile("s_setprio 1\n\ts_cmp_eq_u32 %0, 0\n\ts_cbranch_scc1 .Lb%=\n\ts_setprio 2\n\ts_barrier\n.Lb%=:" :: "s"(wr) : "memory", "scc"); SCHED(); } while (0)
#define BAR_0() do { SCHED(); asm volatile("s_setprio 0\n\ts_cmp_lg_u32 %0, 0\n\ts_cbranch_scc1 .Lb%=\n\ts_barrier\n.Lb%=:" :: "s"(wr) : "memory", "scc"); SCHED(); } while (0)
#define WAIT_RX(c) asm volatile("s_waitcnt vmcnt(16)\n\ts_cmp_lg_u32 %0, 0\n\ts_cbranch_scc1 .Lrx%=\n\ts_waitcnt vmcnt(8)\n.Lrx%=:" :: "s"(c) : "memory", "scc")
    Acc acc;
    bf16x8 At[4][2], B0[2][2], B1[2][2];
    BigPtr cp = big_ptrs(p, cur);
    BigJob pend; pend.type = J_NONE; pend.panel = 0; pend.idx = 0;
    LBAR();
    B8_STB(0, 0, cp, 0); B8_STB(0, 1, cp, 0); B8_STA(0, 0, cp, 0); B8_STA(0, 1, cp, 0);
    B8_STB(1, 0, cp, 1); B8_STA(1, 0, cp, 1); B8_STB(1, 1, cp, 1);
    WAIT_V(8); BAR();
    BigJob nx = pend;
    for (;;) {
      bool chain = false;
      BigPtr np = cp;
      constexpr int nt = DM / 64;
#pragma unroll
      for (int i = 0; i < 32; ++i) acc[i >> 4][(i >> 3) & 1][(i >> 1) & 3][i & 1] = (f32x4){0.f, 0.f, 0.f, 0.f};
#pragma unroll 1
      for (int t = 0; t < nt; t += 2) {
        const bool last = t == nt - 2;
        if (last) {
          nx = claim();
          chain = nx.type == J_GU;
          if (chain) np = big_ptrs(p, nx);
        }
        const BigPtr sp = np;
        const int t2 = last ? 0 : t + 2;
        if (t == 2) big_publish(p, pend);
        const int rx = __builtin_amdgcn_readfirstlane(t == 0 && pend.type != J_NONE);
        B8_LDB(B0, 0, 0); B8_LDB(B1, 0, 1); SCHED(); B8_LDA(0, 0); B8_STA(1, 1, cp, t + 1);
        WAIT_RX(rx);
        WAIT_L(0); BAR_1(); B8_MMA(0, 0, B0); B8_MMA(0, 1, B1); BAR_0();
        B8_LDA(0, 1); B8_STB(0, 0, sp, t2); B8_STB(0, 1, sp, t2); B8_STA(0, 0, sp, t2);
        WAIT_RX(rx);
        WAIT_L(0); BAR_1(); B8_MMA(1, 0, B0); B8_MMA(1, 1, B1); BAR_0();
        B8_LDB(B0, 1, 0); B8_LDB(B1, 1, 1); SCHED(); B8_LDA(1, 0); B8_STA(0, 1, sp, t2);
        WAIT_V(8); WAIT_L(0); BAR_1(); B8_MMA(0, 0, B0); B8_MMA(0, 1, B1); BAR_0();
        B8_LDA(1, 1); B8_STB(1, 0, sp, t2 + 1); B8_STB(1, 1, sp, t2 + 1); B8_STA(1, 0, sp, t2 + 1);
        WAIT_V(8); WAIT_L(0); BAR_1(); B8_MMA(1, 0, B0); B8_MMA(1, 1, B1); BAR_0();
      }
      big_epilogue(p, cur, acc, smem);
      if (!chain) break;
      pend = cur; cur = nx; cp = np;
    }
    WAIT_V(0);
    BAR();
    asm volatile(".Lcode_end:");
    big_publish(p, cur);
    cur = nx;
  }
}


__device__ __forceinline__ unsigned now_q() { return (unsigned)__builtin_amdgcn_s_memrealtime() * 4u; }
__device__ __forceinline__ void pace_until(unsigned t) {
#pragma unroll 1
  for (int i = 0; i < 256; ++i) {
    const int d = (int)(t - now_q());
    if (d <= 0) break;
    if (d > 32) __builtin_amdgcn_s_sleep(1);
  }
}
#define CLB_E "s76", "s77", "s78", "s79", "s80", "s81", "s82", "s83", "s84", "s85", "s86", "s87", "s88", "s89"
#define EDGE_ASM \
  "s_memtime s[76:77]\n\t" \
  "s_memrealtime s[78:79]\n\t" \
  "s_nop 3\n\t" \
  "s_memrealtime s[80:81]\n\t" \
  "s_nop 3\n\t" \
  "s_memrealtime s[82:83]\n\t" \
  "s_nop 3\n\t" \
  "s_memrealtime s[84:85]\n\t" \
  "s_nop 3\n\t" \
  "s_memrealtime s[86:87]\n\t" \
  "s_nop 3\n\t" \
  "s_memrealtime s[88:89]\n\t" \
  "s_mov_b32 s93, 0\n\t" \
  "s_waitcnt lgkmcnt(0)\n\t" \
  "s_cmp_eq_u32 s80, s78\n\t" \
  "s_addc_u32 s93, s93, 0\n\t" \
  "s_cmp_eq_u32 s82, s78\n\t" \
  "s_addc_u32 s93, s93, 0\n\t" \
  "s_cmp_eq_u32 s84, s78\n\t" \
  "s_addc_u32 s93, s93, 0\n\t" \
  "s_cmp_eq_u32 s86, s78\n\t" \
  "s_addc_u32 s93, s93, 0\n\t" \
  "s_cmp_eq_u32 s88, s78\n\t" \
  "s_addc_u32 s93, s93, 0\n\t" \
  "s_mul_i32 s93, s93, 21\n\t" \
  "s_add_u32 s76, s76, 15\n\t" \
  "s_add_u32 %[kc], s76, s93\n\t" \
  "s_lshl2_add_u32 %[kq], s78, 16\n\t"
__device__ __forceinline__ unsigned time_of(unsigned cyc) {
  unsigned c, q;
  asm volatile(EDGE_ASM : [kc] "=&s"(c), [kq] "=&s"(q) :: CLB_E, "s93", "scc");
  return q - (((c - cyc) * 11900u) >> 16);
}
#define DS_RED 18
#define DS_MEM 11
#define LEAD_DN 2
#define LEAD_UP 6
__device__ __forceinline__ u32x2 ld2_sc1(__amdgpu_buffer_rsrc_t rs, int off, int soff = 0) {
  return __builtin_amdgcn_raw_buffer_load_b64(rs, off, soff, 16);
}
__device__ __forceinline__ void st2_sc1(__amdgpu_buffer_rsrc_t rs, int off, u32x2 v) {
  __builtin_amdgcn_raw_buffer_store_b64(v, rs, off, 0, 16);
}
__device__ __forceinline__ int row_max(int v) {
  v = max(v, __builtin_amdgcn_update_dpp(0, v, 0x128, 0xf, 0xf, false));
  v = max(v, __builtin_amdgcn_update_dpp(0, v, 0x124, 0xf, 0xf, false));
  v = max(v, __builtin_amdgcn_update_dpp(0, v, 0x122, 0xf, 0xf, false));
  v = max(v, __builtin_amdgcn_update_dpp(0, v, 0x121, 0xf, 0xf, false));
  return v;
}
__device__ __forceinline__ int wave_max(int v) {
  v = row_max(v);
  return max(max(__builtin_amdgcn_readlane(v, 0), __builtin_amdgcn_readlane(v, 16)),
             max(__builtin_amdgcn_readlane(v, 32), __builtin_amdgcn_readlane(v, 48)));
}
__device__ __forceinline__ int track(int est, int x, int up, int dn, int out) {
  const int d = x - est;
  return est + (d > out ? 4 : max(min(d, up), -dn));
}
#define WSL 12
#define PLAN_SLEEP 2
#define REF_W 3
#define REF_G 60
#define RL0 310
__device__ __forceinline__ void watch(__amdgpu_buffer_rsrc_t rsy, int s, int lane, unsigned t0, const unsigned* tgt_, gu32* abortw, int hs) {
  const int rl = (int)__builtin_amdgcn_raw_buffer_load_b32(rsy, lane * 128, SY_BR + hs, 16);
  u32x2 ra;
  for (unsigned spins = 0;;) {
    asm volatile("" ::: "memory");
    ra = ld2_sc1(rsy, (lane & 31) * 128, SY_BA + hs);
    const bool all = __all((int)ra[0] >= s);
    if (all) break;
    if (spins < 3) __builtin_amdgcn_s_sleep(PLAN_SLEEP); else __builtin_amdgcn_s_sleep(WSL);
    if (spin_fail(spins, abortw)) break;
  }
  int d = ra[0] == (unsigned)s ? (int)(ra[1] - t0) : 0;
  d = max(d, __builtin_amdgcn_update_dpp(0, d, 0xB1, 0xf, 0xf, false));
  d = max(d, __builtin_amdgcn_update_dpp(0, d, 0x4E, 0xf, 0xf, false));
  int sum = d + __builtin_amdgcn_update_dpp(0, d, 0x128, 0xf, 0xf, false);
  sum += __builtin_amdgcn_update_dpp(0, sum, 0x124, 0xf, 0xf, false);
  const int mx = wave_max(d), mean = (__builtin_amdgcn_readlane(sum, 0) + __builtin_amdgcn_readlane(sum, 16)) >> 3;
  const unsigned t = t0 + mx - min(((mx - mean) * REF_W) >> 2, REF_G) + wave_max(rl < 0 ? RL0 : rl) + (DS_RED + DS_MEM);
  lds_vu_ptr tgt = lds_at(tgt_);
  if (lane < 2) tgt[lane] = lane ? t : s + 1;
}
__device__ __forceinline__ unsigned ld_flag(__amdgpu_buffer_rsrc_t rs, int off) {
  return __builtin_amdgcn_raw_buffer_load_b32(rs, off, 0, 16);
}

constexpr unsigned long long QK_MAP[8] = {0xbaf9876d543210ecull, 0xa98f76d543210bceull, 0x98f76d543210ecbaull, 0x876fd543210bcea9ull, 0x76fd543210ecba98ull, 0x65df43210bcea987ull, 0x5df43210ecba9876ull, 0xd43f210bcea98765ull};
#define QK_ASM_0 \
  "s_waitcnt lgkmcnt(0)\n\t" \
  "v_subrev_u32 v244, 0x8000, v250\n\t" \
  "ds_read_b128 v[216:219], v248 offset:32768\n\t" \
  "ds_read_b128 v[220:223], v248 offset:33792\n\t" \
  "ds_read_b128 v[224:227], v248 offset:34816\n\t" \
  "ds_read_b128 v[228:231], v248 offset:35840\n\t" \
  "ds_read_b128 v[208:211], v244 offset:0\n\t" \
  "ds_read_b128 v[212:215], v244 offset:1024\n\t" \
  "ds_read_b128 v[236:239], v244 offset:2048\n\t" \
  "ds_read_b128 v[240:243], v244 offset:3072\n\t" \
  "v_mov_b64 v[192:193], 0\n\t" \
  "v_mov_b64 v[194:195], 0\n\t" \
  "v_mov_b64 v[196:197], 0\n\t" \
  "v_mov_b64 v[198:199], 0\n\t" \
  ".p2align 5\n\t" \
  "s_nop 0\n\t" \
  "s_nop 0\n\t" \
  "s_nop 0\n\t" \
  "s_nop 0\n\t" \
  "s_movk_i32 s92, 0x2000\n\t" \
  ".Lwt%=:\n\t" \
  "ds_read_b64 v[200:201], v248 offset:30720\n\t" \
  "s_memrealtime s[90:91]\n\t" \
  "s_sub_u32 s92, s92, 1\n\t" \
  "s_waitcnt lgkmcnt(0)\n\t" \
  "v_readfirstlane_b32 s93, v200\n\t" \
  "v_readfirstlane_b32 %[tgt], v201\n\t" \
  "s_cbranch_scc1 .Lgo%=\n\t" \
  "s_cmp_eq_u32 s93, %[step]\n\t" \
  "s_cbranch_scc1 .Lwx%=\n\t" \
  "s_sleep 1\n\t" \
  "s_branch .Lwt%=\n\t" \
  ".Lwx%=:\n\t" \
  "s_lshl_b32 s93, s90, 2\n\t" \
  "s_sub_i32 s93, %[tgt], s93\n\t" \
  "s_cmp_le_i32 s93, 0\n\t" \
  "s_cbranch_scc1 .Lgo%=\n\t" \
  "s_min_i32 s93, s93, 8000\n\t" \
  "s_mov_b32 s94, s93\n\t" \
  ".Lsl%=:\n\t" \
  "s_sub_i32 s94, s94, 18\n\t" \
  "s_cmp_lt_i32 s94, 30\n\t" \
  "s_cbranch_scc1 .Lsx%=\n\t" \
  "s_sleep 1\n\t" \
  "s_branch .Lsl%=\n\t" \
  ".Lsx%=:\n\t" \
  "s_sub_i32 s93, s93, 12\n\t" \
  "s_max_i32 s93, s93, 4\n\t" \
  "s_lshr_b32 s93, s93, 2\n\t" \
  "s_add_u32 s93, s93, s90\n\t" \
  "s_memrealtime s[90:91]\n\t" \
  ".Lpace%=:\n\t" \
  "s_memrealtime s[94:95]\n\t" \
  "s_waitcnt lgkmcnt(1)\n\t" \
  "s_sub_i32 s92, s90, s93\n\t" \
  "s_cmp_ge_i32 s92, 0\n\t" \
  "s_cbranch_scc1 .Le%=\n\t" \
  "s_memrealtime s[90:91]\n\t" \
  "s_waitcnt lgkmcnt(1)\n\t" \
  "s_sub_i32 s92, s94, s93\n\t" \
  "s_cmp_lt_i32 s92, 0\n\t" \
  "s_cbranch_scc1 .Lpace%=\n\t" \
  "s_nop 4\n\t" \
  ".Le%=:\n\t" \
  "s_add_u32 s92, s92, s93\n\t" \
  "s_lshl_b32 s92, s92, 2\n\t" \
  "s_sub_i32 s92, %[tgt], s92\n\t" \
  "s_max_i32 s92, s92, 0\n\t" \
  "s_bitcmp0_b32 s92, 3\n\t" \
  "s_cbranch_scc1 .Lf3%=\n\t" \
  "s_nop 15\n\t" \
  ".Lf3%=:\n\t" \
  "s_bitcmp0_b32 s92, 2\n\t" \
  "s_cbranch_scc1 .Lf2%=\n\t" \
  "s_nop 10\n\t" \
  ".Lf2%=:\n\t" \
  ".Lgo%=:\n\t" \
  "s_mov_b32 s92, 0\n\t" \
  ".Lpoll%=:\n\t" \
  "buffer_load_dwordx4 v[200:203], v248, %[rs], %[som] offen sc1\n\t" \
  "buffer_load_dwordx4 v[204:207], v248, %[rs], %[som2] offen sc1\n\t" \
  ".p2align 5\n\t" \
  "s_waitcnt vmcnt(1) lgkmcnt(0)\n\t" \
  "ds_write_b128 v249, v[200:203]\n\t" \
  "v_smfmac_f32_16x16x64_bf16 v[192:195], v[200:203], v[208:215], v251\n\t" \
  "v_max3_u32 v244, v200, v201, v202\n\t" \
  "v_smfmac_f32_16x16x64_bf16 v[196:199], v[200:203], v[236:243], v251\n\t" \
  "s_waitcnt vmcnt(0)\n\t" \
  "ds_write_b128 v249, v[204:207] offset:8192\n\t" \
  "v_smfmac_f32_16x16x64_bf16 v[192:195], v[204:207], v[216:223], v251\n\t" \
  "v_max3_u32 v244, v244, v203, v204\n\t" \
  "v_max3_u32 v244, v244, v205, v206\n\t" \
  "v_smfmac_f32_16x16x64_bf16 v[196:199], v[204:207], v[224:231], v251\n\t" \
  "v_max_u32 v244, v244, v207\n\t" \
  "v_cmp_le_u32 vcc, %[sent], v244\n\t" \
  "s_cbranch_vccnz .Lslow%=\n\t" \
  ".Lok%=:\n\t" \
  "ds_read_b128 v[216:219], v250 offset:32768\n\t" \
  "ds_read_b128 v[220:223], v250 offset:33792\n\t" \
  "ds_read_b128 v[224:227], v250 offset:34816\n\t" \
  "ds_read_b128 v[228:231], v250 offset:35840\n\t" \
  "s_waitcnt lgkmcnt(4)\n\t" \
  "s_barrier\n\t" \
  "ds_read_b128 v[208:211], v248 offset:1024\n\t" \
  "ds_read_b128 v[212:215], v248 offset:2048\n\t" \
  "ds_read_b128 v[236:239], v248 offset:3072\n\t" \
  "ds_read_b128 v[240:243], v248 offset:4096\n\t" \
  "s_cmp_eq_u32 %[step], 63\n\t" \
  "s_cbranch_scc1 .Lls%=\n\t" \
  "s_waitcnt lgkmcnt(3)\n\t" \
  "v_smfmac_f32_16x16x64_bf16 v[192:195], v[208:211], v[216:223], v251\n\t" \
  "ds_read_b128 v[244:247], v248 offset:5120\n\t" \
  "v_smfmac_f32_16x16x64_bf16 v[196:199], v[208:211], v[224:231], v251\n\t" \
  "s_waitcnt lgkmcnt(3)\n\t" \
  "ds_read_b128 v[200:203], v248 offset:13312\n\t" \
  "v_smfmac_f32_16x16x64_bf16 v[192:195], v[212:215], v[0:7], v251\n\t" \
  "ds_read_b128 v[216:219], v250 offset:0\n\t" \
  "v_smfmac_f32_16x16x64_bf16 v[196:199], v[212:215], v[8:15], v251\n\t" \
  "s_waitcnt lgkmcnt(4)\n\t" \
  "ds_read_b128 v[220:223], v250 offset:1024\n\t" \
  "v_smfmac_f32_16x16x64_bf16 v[192:195], v[236:239], v[16:23], v251\n\t" \
  "ds_read_b128 v[224:227], v250 offset:2048\n\t" \
  "v_smfmac_f32_16x16x64_bf16 v[196:199], v[236:239], v[24:31], v251\n\t" \
  "s_waitcnt lgkmcnt(4)\n\t" \
  "ds_read_b128 v[228:231], v250 offset:3072\n\t" \
  "v_smfmac_f32_16x16x64_bf16 v[192:195], v[240:243], v[32:39], v251\n\t" \
  "ds_read_b128 v[204:207], v248 offset:6144\n\t" \
  "v_smfmac_f32_16x16x64_bf16 v[196:199], v[240:243], v[40:47], v251\n\t" \
  "ds_read_b128 v[208:211], v248 offset:7168\n\t" \
  "v_smfmac_f32_16x16x64_bf16 v[192:195], v[244:247], v[48:55], v251\n\t" \
  "ds_read_b128 v[212:215], v248 offset:9216\n\t" \
  "v_smfmac_f32_16x16x64_bf16 v[196:199], v[244:247], v[56:63], v251\n\t" \
  "s_waitcnt lgkmcnt(5)\n\t" \
  "v_smfmac_f32_16x16x64_bf16 v[192:195], v[200:203], v[216:223], v251\n\t" \
  "s_waitcnt lgkmcnt(3)\n\t" \
  "ds_read_b128 v[236:239], v248 offset:10240\n\t" \
  "v_smfmac_f32_16x16x64_bf16 v[196:199], v[200:203], v[224:231], v251\n\t" \
  "s_waitcnt lgkmcnt(3)\n\t" \
  "v_smfmac_f32_16x16x64_bf16 v[192:195], v[204:207], v[64:71], v251\n\t" \
  "ds_read_b128 v[240:243], v248 offset:11264\n\t" \
  "v_smfmac_f32_16x16x64_bf16 v[196:199], v[204:207], v[72:79], v251\n\t" \
  "s_waitcnt lgkmcnt(2)\n\t" \
  "v_smfmac_f32_16x16x64_bf16 v[192:195], v[208:211], v[80:87], v251\n\t" \
  "ds_read_b128 v[244:247], v248 offset:12288\n\t" \
  "v_smfmac_f32_16x16x64_bf16 v[196:199], v[208:211], v[88:95], v251\n\t" \
  "ds_read_b128 v[200:203], v248 offset:14336\n\t" \
  "v_smfmac_f32_16x16x64_bf16 v[192:195], v[212:215], v[96:103], v251\n\t" \
  "ds_read_b128 v[204:207], v248 offset:15360\n\t" \
  "v_smfmac_f32_16x16x64_bf16 v[196:199], v[212:215], v[104:111], v251\n\t" \
  "s_waitcnt lgkmcnt(3)\n\t" \
  "v_smfmac_f32_16x16x64_bf16 v[192:195], v[236:239], v[112:119], v251\n\t" \
  "v_smfmac_f32_16x16x64_bf16 v[196:199], v[236:239], v[120:127], v251\n\t" \
  "v_smfmac_f32_16x16x64_bf16 v[192:195], v[240:243], v[128:135], v251\n\t" \
  "v_smfmac_f32_16x16x64_bf16 v[196:199], v[240:243], v[136:143], v251\n\t" \
  "s_waitcnt lgkmcnt(0)\n\t" \
  "v_smfmac_f32_16x16x64_bf16 v[192:195], v[244:247], v[144:151], v251\n\t" \
  "v_smfmac_f32_16x16x64_bf16 v[196:199], v[244:247], v[152:159], v251\n\t" \
  "v_smfmac_f32_16x16x64_bf16 v[192:195], v[200:203], v[160:167], v251\n\t" \
  "v_smfmac_f32_16x16x64_bf16 v[196:199], v[200:203], v[168:175], v251\n\t" \
  "v_smfmac_f32_16x16x64_bf16 v[192:195], v[204:207], v[176:183], v251\n\t" \
  "v_smfmac_f32_16x16x64_bf16 v[196:199], v[204:207], v[184:191], v251\n\t" \
  "ds_read_b128 v[200:203], v248 offset:16384\n\t" \
  "ds_read_b128 v[204:207], v248 offset:20480\n\t" \
  "v_mov_b32 v250, v232\n\t" \
  "v_mov_b32 v249, v233\n\t" \
  "s_nop 2\n\t" \
  "v_add_f32 v192, v192, v194\n\t" \
  "v_add_f32 v193, v193, v195\n\t" \
  "v_add_f32 v196, v196, v198\n\t" \
  "v_add_f32 v197, v197, v199\n\t" \
  "v_cvt_pk_bf16_f32 v192, v192, v193\n\t" \
  "v_cvt_pk_bf16_f32 v196, v196, v197\n\t" \
  "ds_write2_b32 v252, v192, v196 offset1:64\n\t" \
  "v_mov_b32 v251, v234\n\t" \
  "ds_read_b64_tr_b16 v[232:233], v250 offset:0\n\t" \
  "ds_read_b64_tr_b16 v[234:235], v250 offset:64\n\t" \
  "v_mov_b32 v212, %[step]\n\t" \
  "s_mov_b64 exec, 1\n\t" \
  "ds_write_b32 v248, v212 offset:28672\n\t" \
  "s_mov_b64 exec, -1\n\t" \
  "buffer_load_dwordx4 v[192:195], v248, %[rt], %[svt] offen offset:0 sc1\n\t" \
  "buffer_load_dwordx4 v[196:199], v248, %[rt], %[svt] offen offset:1024 sc1\n\t" \
  "s_waitcnt lgkmcnt(1)\n\t" \
  "v_mfma_f32_16x16x32_bf16 v[200:203], v[200:203], v[232:235], 0\n\t" \
  "v_mfma_f32_16x16x32_bf16 v[204:207], v[204:207], v[232:235], 0\n\t" \
  "s_nop 7\n\t" \
  "v_cndmask_b32 v216, v200, v204, %[nh]\n\t" \
  "v_cndmask_b32 v217, v201, v205, %[nh]\n\t" \
  "v_cndmask_b32 v218, v202, v206, %[nh]\n\t" \
  "v_cndmask_b32 v219, v203, v207, %[nh]\n\t" \
  "ds_write_b128 v248, v[216:219] offset:16384\n\t" \
  "buffer_load_dword v252, v249, %[rs], %[sv] offen offset:0 sc1\n\t" \
  "buffer_load_dword v253, v249, %[rs], %[sv] offen offset:256 sc1\n\t" \
  "ds_read_b32 v212, v248 offset:28672\n\t" \
  "ds_read_b64_tr_b16 v[236:237], v250 offset:512\n\t" \
  "ds_read_b64_tr_b16 v[238:239], v250 offset:576\n\t" \
  "ds_read_b64_tr_b16 v[240:241], v250 offset:1024\n\t" \
  "ds_read_b64_tr_b16 v[242:243], v250 offset:1088\n\t" \
  "ds_read_b64_tr_b16 v[244:245], v250 offset:1536\n\t" \
  "ds_read_b64_tr_b16 v[246:247], v250 offset:1600\n\t" \
  "s_setprio 3\n\t" \
  "s_waitcnt lgkmcnt(0)\n\t" \
  "v_cmp_ne_u32 vcc, %[step], v212\n\t" \
  "s_and_b32 s93, vcc_lo, 0xf\n\t" \
  "s_barrier\n\t" \
  "s_cbranch_scc1 .Lre%=\n\t" \
  ".Lrb%=:\n\t" \
  "ds_read_b64_tr_b16 v[200:201], v250 offset:2048\n\t" \
  "ds_read_b64_tr_b16 v[202:203], v250 offset:2112\n\t" \
  "ds_read_b64_tr_b16 v[204:205], v250 offset:2560\n\t" \
  "ds_read_b64_tr_b16 v[206:207], v250 offset:2624\n\t" \
  "ds_read_b64_tr_b16 v[208:209], v250 offset:3072\n\t" \
  "ds_read_b64_tr_b16 v[210:211], v250 offset:3136\n\t" \
  "ds_read_b64_tr_b16 v[212:213], v250 offset:3584\n\t" \
  "ds_read_b64_tr_b16 v[214:215], v250 offset:3648\n\t" \
  "ds_read_b128 v[220:223], v248 offset:17408\n\t" \
  "ds_read_b128 v[224:227], v248 offset:18432\n\t" \
  "ds_read_b128 v[228:231], v248 offset:19456\n\t" \
  "s_waitcnt lgkmcnt(9)\n\t" \
  "v_mfma_f32_16x16x32_bf16 v[200:203], v[200:203], v[232:235], 0\n\t" \
  "s_waitcnt lgkmcnt(7)\n\t" \
  "v_mfma_f32_16x16x32_bf16 v[200:203], v[204:207], v[236:239], v[200:203]\n\t" \
  "s_waitcnt lgkmcnt(5)\n\t" \
  "v_mfma_f32_16x16x32_bf16 v[200:203], v[208:211], v[240:243], v[200:203]\n\t" \
  "s_waitcnt lgkmcnt(3)\n\t" \
  "v_mfma_f32_16x16x32_bf16 v[200:203], v[212:215], v[244:247], v[200:203]\n\t" \
  "s_waitcnt lgkmcnt(2)\n\t" \
  "v_pk_add_f32 v[216:217], v[216:217], v[220:221]\n\t" \
  "v_pk_add_f32 v[218:219], v[218:219], v[222:223]\n\t" \
  "s_waitcnt lgkmcnt(0)\n\t" \
  "v_pk_add_f32 v[224:225], v[224:225], v[228:229]\n\t" \
  "v_pk_add_f32 v[226:227], v[226:227], v[230:231]\n\t" \
  "v_pk_add_f32 v[216:217], v[216:217], v[224:225]\n\t" \
  "v_pk_add_f32 v[218:219], v[218:219], v[226:227]\n\t" \
  "v_max3_f32 v224, v216, v217, v218\n\t" \
  "v_max_f32 v224, v224, v219\n\t" \
  "s_nop 1\n\t" \
  "v_max_f32_dpp v225, v224, v224 row_ror:8 row_mask:0xf bank_mask:0xf\n\t" \
  "v_max_f32_dpp v226, v224, v224 row_ror:8 row_mask:0xf bank_mask:0xf\n\t" \
  "s_nop 1\n\t" \
  "v_permlane32_swap_b32 v225, v226\n\t" \
  "v_max_f32 v231, v225, v226\n\t" \
  "v_mov_b32 v220, 0x3f803f80\n\t" \
  "v_mov_b32 v221, 0x3f803f80\n\t" \
  "v_mov_b64 v[222:223], v[220:221]\n\t" \
  "v_mov_b32 v233, 0\n\t" \
  "v_mov_b32 v234, 0\n\t" \
  "v_max3_f32 v204, v200, v201, v231\n\t" \
  "v_max3_f32 v205, v204, v202, v203\n\t" \
  "v_max3_f32 v206, v204, v202, v203\n\t" \
  "v_cndmask_b32 v200, v200, v202, %[up]\n\t" \
  "v_cndmask_b32 v201, v201, v203, %[up]\n\t" \
  "v_permlane16_swap_b32 v205, v206\n\t" \
  "v_max_f32 v204, v205, v206\n\t" \
  "v_mul_f32 v204, %[negc], v204\n\t" \
  "v_fma_f32 v200, v200, %[c], v204\n\t" \
  "v_fma_f32 v201, v201, %[c], v204\n\t" \
  "v_exp_f32 v200, v200\n\t" \
  "v_fma_f32 v216, v216, %[c], v204\n\t" \
  "v_exp_f32 v201, v201\n\t" \
  "v_fma_f32 v217, v217, %[c], v204\n\t" \
  "v_exp_f32 v216, v216\n\t" \
  "v_fma_f32 v218, v218, %[c], v204\n\t" \
  "v_cvt_pk_bf16_f32 v232, v200, v201\n\t" \
  "v_cvt_pk_bf16_f32 v235, v200, v201\n\t" \
  "v_exp_f32 v217, v217\n\t" \
  "v_fma_f32 v219, v219, %[c], v204\n\t" \
  "v_mfma_f32_16x16x16_bf16 v[212:215], v[220:221], v[232:233], 0\n\t" \
  "v_exp_f32 v218, v218\n\t" \
  "s_waitcnt vmcnt(0)\n\t" \
  "v_mfma_f32_16x16x16_bf16 v[224:227], v[252:253], v[232:233], 0\n\t" \
  "v_cvt_pk_bf16_f32 v210, v216, v217\n\t" \
  "v_cvt_pk_bf16_f32 v208, v216, v217\n\t" \
  "v_exp_f32 v219, v219\n\t" \
  "v_mfma_f32_16x16x16_bf16 v[228:231], v[252:253], v[234:235], 0\n\t" \
  "v_cvt_pk_bf16_f32 v211, v218, v219\n\t" \
  "v_cvt_pk_bf16_f32 v209, v218, v219\n\t" \
  "v_mov_b32_dpp v208, v210 row_ror:8 row_mask:0xf bank_mask:0xc\n\t" \
  "v_mov_b32_dpp v210, v210 row_ror:8 row_mask:0xf bank_mask:0x3\n\t" \
  "v_mov_b32_dpp v209, v211 row_ror:8 row_mask:0xf bank_mask:0xc\n\t" \
  "v_mov_b32_dpp v211, v211 row_ror:8 row_mask:0xf bank_mask:0x3\n\t" \
  "v_max_u32 v204, v252, v253\n\t" \
  "v_cmp_le_u32 vcc, %[sent], v204\n\t" \
  "v_mfma_f32_16x16x32_bf16 v[212:215], v[220:223], v[208:211], v[212:215]\n\t" \
  ".Lpv%=:\n\t" \
  "v_mfma_f32_16x16x32_bf16 v[224:227], v[192:195], v[208:211], v[224:227]\n\t" \
  "v_mfma_f32_16x16x32_bf16 v[228:231], v[196:199], v[208:211], v[228:231]\n\t" \
  "s_cbranch_vccnz .Lvs%=\n\t" \
  ".Lvok%=:\n\t" \
  "s_nop 4\n\t" \
  "v_rcp_f32 v204, v212\n\t" \
  "v_cndmask_b32 v224, v224, v226, %[nh]\n\t" \
  "v_cndmask_b32 v225, v225, v227, %[nh]\n\t" \
  "v_cndmask_b32 v228, v228, v230, %[nh]\n\t" \
  "v_cndmask_b32 v229, v229, v231, %[nh]\n\t" \
  "v_pk_mul_f32 v[224:225], v[224:225], v[204:205] op_sel_hi:[1,0]\n\t" \
  "v_pk_mul_f32 v[228:229], v[228:229], v[204:205] op_sel_hi:[1,0]\n\t" \
  "v_cvt_pk_bf16_f32 v224, v224, v225\n\t" \
  "v_cvt_pk_bf16_f32 v225, v228, v229\n\t" \
  "buffer_store_dwordx2 v[224:225], v251, %[rs], %[so] offen sc1\n\t" \
  "s_memtime s[90:91]\n\t" \
  "s_setprio 0\n\t" \
  "s_waitcnt lgkmcnt(0)\n\t" \
  "s_mov_b32 %[tout], s90\n\t" \
  "s_mov_b32 %[spins], s92\n\t" \
  "s_branch .Lend%=\n\t" \
  ".Lls%=:\n\t" \
  "s_waitcnt lgkmcnt(0)\n\t" \
  "s_barrier\n\t" \
  "s_mov_b32 %[spins], 0\n\t" \
  "s_branch .Lend%=\n\t" \
  ".Lre%=:\n\t" \
  "ds_read_b64_tr_b16 v[236:237], v250 offset:512\n\t" \
  "ds_read_b64_tr_b16 v[238:239], v250 offset:576\n\t" \
  "ds_read_b64_tr_b16 v[240:241], v250 offset:1024\n\t" \
  "ds_read_b64_tr_b16 v[242:243], v250 offset:1088\n\t" \
  "ds_read_b64_tr_b16 v[244:245], v250 offset:1536\n\t" \
  "ds_read_b64_tr_b16 v[246:247], v250 offset:1600\n\t" \
  "s_waitcnt lgkmcnt(0)\n\t" \
  "s_branch .Lrb%=\n\t" \
  ".Lvs%=:\n\t" \
  "v_cmp_eq_u32 vcc, %[sent], v252\n\t" \
  "v_cmp_eq_u32 s[94:95], %[sent], v253\n\t" \
  "s_or_b64 s[94:95], s[94:95], vcc\n\t" \
  "s_cbranch_scc0 .Lvok%=\n\t" \
  "s_add_u32 s92, s92, 1\n\t" \
  "s_sleep 2\n\t" \
  "s_and_b32 s93, s92, 63\n\t" \
  "s_cbranch_scc1 .Lvr%=\n\t" \
  "buffer_load_dword v204, off, %[rs], %[sab] sc1\n\t" \
  "s_waitcnt vmcnt(0)\n\t" \
  "v_readfirstlane_b32 s93, v204\n\t" \
  "s_nop 3\n\t" \
  "s_cmp_eq_u32 s93, 1\n\t" \
  "s_cbranch_scc1 .Lvok%=\n\t" \
  "s_cmp_lt_u32 s92, 0x80000\n\t" \
  "s_cbranch_scc1 .Lvr%=\n\t" \
  "v_mov_b32 v204, 1\n\t" \
  "buffer_store_dword v204, off, %[rs], %[sab] sc1\n\t" \
  "s_branch .Lvok%=\n\t" \
  ".Lvr%=:\n\t" \
  "buffer_load_dword v252, v249, %[rs], %[sv] offen offset:0 sc1\n\t" \
  "buffer_load_dword v253, v249, %[rs], %[sv] offen offset:256 sc1\n\t" \
  "s_waitcnt vmcnt(0)\n\t" \
  "v_mfma_f32_16x16x16_bf16 v[224:227], v[252:253], v[232:233], 0\n\t" \
  "v_mfma_f32_16x16x16_bf16 v[228:231], v[252:253], v[234:235], 0\n\t" \
  "v_max_u32 v204, v252, v253\n\t" \
  "v_cmp_le_u32 vcc, %[sent], v204\n\t" \
  "s_nop 4\n\t" \
  "s_branch .Lpv%=\n\t" \
  "s_branch .Lend%=\n\t" \
  ".Lslow%=:\n\t" \
  "s_mov_b64 s[94:95], 0\n\t" \
  "v_cmp_eq_u32 vcc, %[sent], v200\n\t" \
  "s_or_b64 s[94:95], s[94:95], vcc\n\t" \
  "v_cmp_eq_u32 vcc, %[sent], v201\n\t" \
  "s_or_b64 s[94:95], s[94:95], vcc\n\t" \
  "v_cmp_eq_u32 vcc, %[sent], v202\n\t" \
  "s_or_b64 s[94:95], s[94:95], vcc\n\t" \
  "v_cmp_eq_u32 vcc, %[sent], v203\n\t" \
  "s_or_b64 s[94:95], s[94:95], vcc\n\t" \
  "v_cmp_eq_u32 vcc, %[sent], v204\n\t" \
  "s_or_b64 s[94:95], s[94:95], vcc\n\t" \
  "v_cmp_eq_u32 vcc, %[sent], v205\n\t" \
  "s_or_b64 s[94:95], s[94:95], vcc\n\t" \
  "v_cmp_eq_u32 vcc, %[sent], v206\n\t" \
  "s_or_b64 s[94:95], s[94:95], vcc\n\t" \
  "v_cmp_eq_u32 vcc, %[sent], v207\n\t" \
  "s_or_b64 s[94:95], s[94:95], vcc\n\t" \
  "s_cmp_eq_u64 s[94:95], 0\n\t" \
  "s_cbranch_scc1 .Lok%=\n\t" \
  "v_mov_b64 v[192:193], 0\n\t" \
  "v_mov_b64 v[194:195], 0\n\t" \
  "v_mov_b64 v[196:197], 0\n\t" \
  "v_mov_b64 v[198:199], 0\n\t" \
  "s_add_u32 s92, s92, 1\n\t" \
  "s_cmp_eq_u32 s92, 1\n\t" \
  "s_cbranch_scc1 .Lpoll%=\n\t" \
  "s_sleep 2\n\t" \
  "s_and_b32 s93, s92, 63\n\t" \
  "s_cmp_lg_u32 s93, 0\n\t" \
  "s_cbranch_scc1 .Lpoll%=\n\t" \
  "v_mov_b32 v245, 0\n\t" \
  "buffer_load_dword v244, v245, %[rs], %[sab] offen sc1\n\t" \
  "s_waitcnt vmcnt(0)\n\t" \
  "v_readfirstlane_b32 s93, v244\n\t" \
  "s_nop 3\n\t" \
  "s_cmp_eq_u32 s93, 1\n\t" \
  "s_cbranch_scc1 .Lok%=\n\t" \
  "s_cmp_lt_u32 s92, 0x80000\n\t" \
  "s_cbranch_scc1 .Lpoll%=\n\t" \
  "v_mov_b32 v244, 1\n\t" \
  "buffer_store_dword v244, v245, %[rs], %[sab] offen sc1\n\t" \
  "s_branch .Lok%=\n\t" \
  ".Lend%=:\n\t"
#define QK_ASM_1 \
  "s_waitcnt lgkmcnt(0)\n\t" \
  "v_subrev_u32 v244, 0x8000, v250\n\t" \
  "ds_read_b128 v[216:219], v248 offset:36864\n\t" \
  "ds_read_b128 v[220:223], v248 offset:37888\n\t" \
  "ds_read_b128 v[224:227], v248 offset:38912\n\t" \
  "ds_read_b128 v[228:231], v248 offset:39936\n\t" \
  "ds_read_b128 v[208:211], v244 offset:0\n\t" \
  "ds_read_b128 v[212:215], v244 offset:1024\n\t" \
  "ds_read_b128 v[236:239], v244 offset:2048\n\t" \
  "ds_read_b128 v[240:243], v244 offset:3072\n\t" \
  "v_mov_b64 v[192:193], 0\n\t" \
  "v_mov_b64 v[194:195], 0\n\t" \
  "v_mov_b64 v[196:197], 0\n\t" \
  "v_mov_b64 v[198:199], 0\n\t" \
  ".p2align 5\n\t" \
  "s_nop 0\n\t" \
  "s_nop 0\n\t" \
  "s_nop 0\n\t" \
  "s_nop 0\n\t" \
  "s_movk_i32 s92, 0x2000\n\t" \
  ".Lwt%=:\n\t" \
  "ds_read_b64 v[200:201], v248 offset:30720\n\t" \
  "s_memrealtime s[90:91]\n\t" \
  "s_sub_u32 s92, s92, 1\n\t" \
  "s_waitcnt lgkmcnt(0)\n\t" \
  "v_readfirstlane_b32 s93, v200\n\t" \
  "v_readfirstlane_b32 %[tgt], v201\n\t" \
  "s_cbranch_scc1 .Lgo%=\n\t" \
  "s_cmp_eq_u32 s93, %[step]\n\t" \
  "s_cbranch_scc1 .Lwx%=\n\t" \
  "s_sleep 1\n\t" \
  "s_branch .Lwt%=\n\t" \
  ".Lwx%=:\n\t" \
  "s_lshl_b32 s93, s90, 2\n\t" \
  "s_sub_i32 s93, %[tgt], s93\n\t" \
  "s_cmp_le_i32 s93, 0\n\t" \
  "s_cbranch_scc1 .Lgo%=\n\t" \
  "s_min_i32 s93, s93, 8000\n\t" \
  "s_mov_b32 s94, s93\n\t" \
  ".Lsl%=:\n\t" \
  "s_sub_i32 s94, s94, 18\n\t" \
  "s_cmp_lt_i32 s94, 30\n\t" \
  "s_cbranch_scc1 .Lsx%=\n\t" \
  "s_sleep 1\n\t" \
  "s_branch .Lsl%=\n\t" \
  ".Lsx%=:\n\t" \
  "s_sub_i32 s93, s93, 12\n\t" \
  "s_max_i32 s93, s93, 4\n\t" \
  "s_lshr_b32 s93, s93, 2\n\t" \
  "s_add_u32 s93, s93, s90\n\t" \
  "s_memrealtime s[90:91]\n\t" \
  ".Lpace%=:\n\t" \
  "s_memrealtime s[94:95]\n\t" \
  "s_waitcnt lgkmcnt(1)\n\t" \
  "s_sub_i32 s92, s90, s93\n\t" \
  "s_cmp_ge_i32 s92, 0\n\t" \
  "s_cbranch_scc1 .Le%=\n\t" \
  "s_memrealtime s[90:91]\n\t" \
  "s_waitcnt lgkmcnt(1)\n\t" \
  "s_sub_i32 s92, s94, s93\n\t" \
  "s_cmp_lt_i32 s92, 0\n\t" \
  "s_cbranch_scc1 .Lpace%=\n\t" \
  "s_nop 4\n\t" \
  ".Le%=:\n\t" \
  "s_add_u32 s92, s92, s93\n\t" \
  "s_lshl_b32 s92, s92, 2\n\t" \
  "s_sub_i32 s92, %[tgt], s92\n\t" \
  "s_max_i32 s92, s92, 0\n\t" \
  "s_bitcmp0_b32 s92, 3\n\t" \
  "s_cbranch_scc1 .Lf3%=\n\t" \
  "s_nop 15\n\t" \
  ".Lf3%=:\n\t" \
  "s_bitcmp0_b32 s92, 2\n\t" \
  "s_cbranch_scc1 .Lf2%=\n\t" \
  "s_nop 10\n\t" \
  ".Lf2%=:\n\t" \
  ".Lgo%=:\n\t" \
  "s_mov_b32 s92, 0\n\t" \
  ".Lpoll%=:\n\t" \
  "buffer_load_dwordx4 v[200:203], v248, %[rs], %[som] offen sc1\n\t" \
  "buffer_load_dwordx4 v[204:207], v248, %[rs], %[som2] offen sc1\n\t" \
  ".p2align 5\n\t" \
  "s_waitcnt vmcnt(1) lgkmcnt(0)\n\t" \
  "ds_write_b128 v249, v[200:203]\n\t" \
  "v_smfmac_f32_16x16x64_bf16 v[192:195], v[200:203], v[208:215], v251\n\t" \
  "v_max3_u32 v244, v200, v201, v202\n\t" \
  "v_smfmac_f32_16x16x64_bf16 v[196:199], v[200:203], v[236:243], v251\n\t" \
  "s_waitcnt vmcnt(0)\n\t" \
  "ds_write_b128 v249, v[204:207] offset:8192\n\t" \
  "v_smfmac_f32_16x16x64_bf16 v[192:195], v[204:207], v[216:223], v251\n\t" \
  "v_max3_u32 v244, v244, v203, v204\n\t" \
  "v_max3_u32 v244, v244, v205, v206\n\t" \
  "v_smfmac_f32_16x16x64_bf16 v[196:199], v[204:207], v[224:231], v251\n\t" \
  "v_max_u32 v244, v244, v207\n\t" \
  "v_cmp_le_u32 vcc, %[sent], v244\n\t" \
  "s_cbranch_vccnz .Lslow%=\n\t" \
  ".Lok%=:\n\t" \
  "ds_read_b128 v[216:219], v250 offset:32768\n\t" \
  "ds_read_b128 v[220:223], v250 offset:33792\n\t" \
  "ds_read_b128 v[224:227], v250 offset:34816\n\t" \
  "ds_read_b128 v[228:231], v250 offset:35840\n\t" \
  "s_waitcnt lgkmcnt(4)\n\t" \
  "s_barrier\n\t" \
  "ds_read_b128 v[208:211], v248 offset:0\n\t" \
  "ds_read_b128 v[212:215], v248 offset:3072\n\t" \
  "ds_read_b128 v[236:239], v248 offset:4096\n\t" \
  "ds_read_b128 v[240:243], v248 offset:5120\n\t" \
  "s_cmp_eq_u32 %[step], 63\n\t" \
  "s_cbranch_scc1 .Lls%=\n\t" \
  "s_waitcnt lgkmcnt(3)\n\t" \
  "v_smfmac_f32_16x16x64_bf16 v[192:195], v[208:211], v[216:223], v251\n\t" \
  "ds_read_b128 v[244:247], v248 offset:6144\n\t" \
  "v_smfmac_f32_16x16x64_bf16 v[196:199], v[208:211], v[224:231], v251\n\t" \
  "s_waitcnt lgkmcnt(3)\n\t" \
  "ds_read_b128 v[200:203], v248 offset:12288\n\t" \
  "v_smfmac_f32_16x16x64_bf16 v[192:195], v[212:215], v[0:7], v251\n\t" \
  "ds_read_b128 v[216:219], v250 offset:0\n\t" \
  "v_smfmac_f32_16x16x64_bf16 v[196:199], v[212:215], v[8:15], v251\n\t" \
  "s_waitcnt lgkmcnt(4)\n\t" \
  "ds_read_b128 v[220:223], v250 offset:1024\n\t" \
  "v_smfmac_f32_16x16x64_bf16 v[192:195], v[236:239], v[16:23], v251\n\t" \
  "ds_read_b128 v[224:227], v250 offset:2048\n\t" \
  "v_smfmac_f32_16x16x64_bf16 v[196:199], v[236:239], v[24:31], v251\n\t" \
  "s_waitcnt lgkmcnt(4)\n\t" \
  "ds_read_b128 v[228:231], v250 offset:3072\n\t" \
  "v_smfmac_f32_16x16x64_bf16 v[192:195], v[240:243], v[32:39], v251\n\t" \
  "ds_read_b128 v[204:207], v248 offset:7168\n\t" \
  "v_smfmac_f32_16x16x64_bf16 v[196:199], v[240:243], v[40:47], v251\n\t" \
  "ds_read_b128 v[208:211], v248 offset:8192\n\t" \
  "v_smfmac_f32_16x16x64_bf16 v[192:195], v[244:247], v[48:55], v251\n\t" \
  "ds_read_b128 v[212:215], v248 offset:10240\n\t" \
  "v_smfmac_f32_16x16x64_bf16 v[196:199], v[244:247], v[56:63], v251\n\t" \
  "s_waitcnt lgkmcnt(5)\n\t" \
  "v_smfmac_f32_16x16x64_bf16 v[192:195], v[200:203], v[216:223], v251\n\t" \
  "s_waitcnt lgkmcnt(3)\n\t" \
  "ds_read_b128 v[236:239], v248 offset:11264\n\t" \
  "v_smfmac_f32_16x16x64_bf16 v[196:199], v[200:203], v[224:231], v251\n\t" \
  "s_waitcnt lgkmcnt(3)\n\t" \
  "v_smfmac_f32_16x16x64_bf16 v[192:195], v[204:207], v[64:71], v251\n\t" \
  "ds_read_b128 v[240:243], v248 offset:13312\n\t" \
  "v_smfmac_f32_16x16x64_bf16 v[196:199], v[204:207], v[72:79], v251\n\t" \
  "s_waitcnt lgkmcnt(2)\n\t" \
  "v_smfmac_f32_16x16x64_bf16 v[192:195], v[208:211], v[80:87], v251\n\t" \
  "ds_read_b128 v[244:247], v248 offset:14336\n\t" \
  "v_smfmac_f32_16x16x64_bf16 v[196:199], v[208:211], v[88:95], v251\n\t" \
  "ds_read_b128 v[200:203], v248 offset:15360\n\t" \
  "v_smfmac_f32_16x16x64_bf16 v[192:195], v[212:215], v[96:103], v251\n\t" \
  "ds_read_b128 v[204:207], v248 offset:2048\n\t" \
  "v_smfmac_f32_16x16x64_bf16 v[196:199], v[212:215], v[104:111], v251\n\t" \
  "s_waitcnt lgkmcnt(3)\n\t" \
  "v_smfmac_f32_16x16x64_bf16 v[192:195], v[236:239], v[112:119], v251\n\t" \
  "v_smfmac_f32_16x16x64_bf16 v[196:199], v[236:239], v[120:127], v251\n\t" \
  "v_smfmac_f32_16x16x64_bf16 v[192:195], v[240:243], v[128:135], v251\n\t" \
  "v_smfmac_f32_16x16x64_bf16 v[196:199], v[240:243], v[136:143], v251\n\t" \
  "s_waitcnt lgkmcnt(0)\n\t" \
  "v_smfmac_f32_16x16x64_bf16 v[192:195], v[244:247], v[144:151], v251\n\t" \
  "v_smfmac_f32_16x16x64_bf16 v[196:199], v[244:247], v[152:159], v251\n\t" \
  "v_smfmac_f32_16x16x64_bf16 v[192:195], v[200:203], v[160:167], v251\n\t" \
  "v_smfmac_f32_16x16x64_bf16 v[196:199], v[200:203], v[168:175], v251\n\t" \
  "v_smfmac_f32_16x16x64_bf16 v[192:195], v[204:207], v[176:183], v251\n\t" \
  "v_smfmac_f32_16x16x64_bf16 v[196:199], v[204:207], v[184:191], v251\n\t" \
  "ds_read_b128 v[200:203], v248 offset:17408\n\t" \
  "ds_read_b128 v[204:207], v248 offset:21504\n\t" \
  "v_mov_b32 v250, v232\n\t" \
  "v_mov_b32 v249, v233\n\t" \
  "s_nop 2\n\t" \
  "v_add_f32 v192, v192, v194\n\t" \
  "v_add_f32 v193, v193, v195\n\t" \
  "v_add_f32 v196, v196, v198\n\t" \
  "v_add_f32 v197, v197, v199\n\t" \
  "v_cvt_pk_bf16_f32 v192, v192, v193\n\t" \
  "v_cvt_pk_bf16_f32 v196, v196, v197\n\t" \
  "ds_write2_b32 v252, v192, v196 offset1:64\n\t" \
  "v_mov_b32 v251, v234\n\t" \
  "ds_read_b64_tr_b16 v[236:237], v250 offset:512\n\t" \
  "ds_read_b64_tr_b16 v[238:239], v250 offset:576\n\t" \
  "v_mov_b32 v212, %[step]\n\t" \
  "s_mov_b64 exec, 1\n\t" \
  "ds_write_b32 v248, v212 offset:28688\n\t" \
  "s_mov_b64 exec, -1\n\t" \
  "buffer_load_dwordx4 v[192:195], v248, %[rt], %[svt] offen offset:0 sc1\n\t" \
  "buffer_load_dwordx4 v[196:199], v248, %[rt], %[svt] offen offset:1024 sc1\n\t" \
  "s_waitcnt lgkmcnt(1)\n\t" \
  "v_mfma_f32_16x16x32_bf16 v[200:203], v[200:203], v[236:239], 0\n\t" \
  "v_mfma_f32_16x16x32_bf16 v[204:207], v[204:207], v[236:239], 0\n\t" \
  "s_nop 7\n\t" \
  "v_cndmask_b32 v216, v200, v204, %[nh]\n\t" \
  "v_cndmask_b32 v217, v201, v205, %[nh]\n\t" \
  "v_cndmask_b32 v218, v202, v206, %[nh]\n\t" \
  "v_cndmask_b32 v219, v203, v207, %[nh]\n\t" \
  "ds_write_b128 v248, v[216:219] offset:17408\n\t" \
  "buffer_load_dword v252, v249, %[rs], %[sv] offen offset:0 sc1\n\t" \
  "buffer_load_dword v253, v249, %[rs], %[sv] offen offset:256 sc1\n\t" \
  "ds_read_b32 v212, v248 offset:28672\n\t" \
  "ds_read_b64_tr_b16 v[240:241], v250 offset:1024\n\t" \
  "ds_read_b64_tr_b16 v[242:243], v250 offset:1088\n\t" \
  "ds_read_b64_tr_b16 v[244:245], v250 offset:1536\n\t" \
  "ds_read_b64_tr_b16 v[246:247], v250 offset:1600\n\t" \
  "ds_read_b64_tr_b16 v[232:233], v250 offset:0\n\t" \
  "ds_read_b64_tr_b16 v[234:235], v250 offset:64\n\t" \
  "s_setprio 3\n\t" \
  "s_waitcnt lgkmcnt(0)\n\t" \
  "v_cmp_ne_u32 vcc, %[step], v212\n\t" \
  "s_and_b32 s93, vcc_lo, 0xf\n\t" \
  "s_barrier\n\t" \
  "s_cbranch_scc1 .Lre%=\n\t" \
  ".Lrb%=:\n\t" \
  "ds_read_b64_tr_b16 v[204:205], v250 offset:2560\n\t" \
  "ds_read_b64_tr_b16 v[206:207], v250 offset:2624\n\t" \
  "ds_read_b64_tr_b16 v[208:209], v250 offset:3072\n\t" \
  "ds_read_b64_tr_b16 v[210:211], v250 offset:3136\n\t" \
  "ds_read_b64_tr_b16 v[212:213], v250 offset:3584\n\t" \
  "ds_read_b64_tr_b16 v[214:215], v250 offset:3648\n\t" \
  "ds_read_b64_tr_b16 v[200:201], v250 offset:2048\n\t" \
  "ds_read_b64_tr_b16 v[202:203], v250 offset:2112\n\t" \
  "ds_read_b128 v[220:223], v248 offset:18432\n\t" \
  "ds_read_b128 v[224:227], v248 offset:19456\n\t" \
  "ds_read_b128 v[228:231], v248 offset:16384\n\t" \
  "s_waitcnt lgkmcnt(9)\n\t" \
  "v_mfma_f32_16x16x32_bf16 v[204:207], v[204:207], v[236:239], 0\n\t" \
  "s_waitcnt lgkmcnt(7)\n\t" \
  "v_mfma_f32_16x16x32_bf16 v[204:207], v[208:211], v[240:243], v[204:207]\n\t" \
  "s_waitcnt lgkmcnt(5)\n\t" \
  "v_mfma_f32_16x16x32_bf16 v[204:207], v[212:215], v[244:247], v[204:207]\n\t" \
  "s_waitcnt lgkmcnt(3)\n\t" \
  "v_mfma_f32_16x16x32_bf16 v[204:207], v[200:203], v[232:235], v[204:207]\n\t" \
  "s_waitcnt lgkmcnt(2)\n\t" \
  "v_pk_add_f32 v[216:217], v[216:217], v[220:221]\n\t" \
  "v_pk_add_f32 v[218:219], v[218:219], v[222:223]\n\t" \
  "s_waitcnt lgkmcnt(0)\n\t" \
  "v_pk_add_f32 v[224:225], v[224:225], v[228:229]\n\t" \
  "v_pk_add_f32 v[226:227], v[226:227], v[230:231]\n\t" \
  "v_pk_add_f32 v[216:217], v[216:217], v[224:225]\n\t" \
  "v_pk_add_f32 v[218:219], v[218:219], v[226:227]\n\t" \
  "v_max3_f32 v224, v216, v217, v218\n\t" \
  "v_max_f32 v224, v224, v219\n\t" \
  "s_nop 1\n\t" \
  "v_max_f32_dpp v225, v224, v224 row_ror:8 row_mask:0xf bank_mask:0xf\n\t" \
  "v_max_f32_dpp v226, v224, v224 row_ror:8 row_mask:0xf bank_mask:0xf\n\t" \
  "s_nop 1\n\t" \
  "v_permlane32_swap_b32 v225, v226\n\t" \
  "v_max_f32 v231, v225, v226\n\t" \
  "v_mov_b32 v220, 0x3f803f80\n\t" \
  "v_mov_b32 v221, 0x3f803f80\n\t" \
  "v_mov_b64 v[222:223], v[220:221]\n\t" \
  "v_mov_b32 v237, 0\n\t" \
  "v_mov_b32 v238, 0\n\t" \
  "v_max3_f32 v208, v204, v205, v231\n\t" \
  "v_max3_f32 v209, v208, v206, v207\n\t" \
  "v_max3_f32 v210, v208, v206, v207\n\t" \
  "v_cndmask_b32 v204, v204, v206, %[up]\n\t" \
  "v_cndmask_b32 v205, v205, v207, %[up]\n\t" \
  "v_permlane16_swap_b32 v209, v210\n\t" \
  "v_max_f32 v208, v209, v210\n\t" \
  "v_mul_f32 v208, %[negc], v208\n\t" \
  "v_fma_f32 v204, v204, %[c], v208\n\t" \
  "v_fma_f32 v205, v205, %[c], v208\n\t" \
  "v_exp_f32 v204, v204\n\t" \
  "v_fma_f32 v216, v216, %[c], v208\n\t" \
  "v_exp_f32 v205, v205\n\t" \
  "v_fma_f32 v217, v217, %[c], v208\n\t" \
  "v_exp_f32 v216, v216\n\t" \
  "v_fma_f32 v218, v218, %[c], v208\n\t" \
  "v_cvt_pk_bf16_f32 v236, v204, v205\n\t" \
  "v_cvt_pk_bf16_f32 v239, v204, v205\n\t" \
  "v_exp_f32 v217, v217\n\t" \
  "v_fma_f32 v219, v219, %[c], v208\n\t" \
  "v_mfma_f32_16x16x16_bf16 v[200:203], v[220:221], v[236:237], 0\n\t" \
  "v_exp_f32 v218, v218\n\t" \
  "s_waitcnt vmcnt(0)\n\t" \
  "v_mfma_f32_16x16x16_bf16 v[224:227], v[252:253], v[236:237], 0\n\t" \
  "v_cvt_pk_bf16_f32 v214, v216, v217\n\t" \
  "v_cvt_pk_bf16_f32 v212, v216, v217\n\t" \
  "v_exp_f32 v219, v219\n\t" \
  "v_mfma_f32_16x16x16_bf16 v[228:231], v[252:253], v[238:239], 0\n\t" \
  "v_cvt_pk_bf16_f32 v215, v218, v219\n\t" \
  "v_cvt_pk_bf16_f32 v213, v218, v219\n\t" \
  "v_mov_b32_dpp v212, v214 row_ror:8 row_mask:0xf bank_mask:0xc\n\t" \
  "v_mov_b32_dpp v214, v214 row_ror:8 row_mask:0xf bank_mask:0x3\n\t" \
  "v_mov_b32_dpp v213, v215 row_ror:8 row_mask:0xf bank_mask:0xc\n\t" \
  "v_mov_b32_dpp v215, v215 row_ror:8 row_mask:0xf bank_mask:0x3\n\t" \
  "v_max_u32 v208, v252, v253\n\t" \
  "v_cmp_le_u32 vcc, %[sent], v208\n\t" \
  "v_mfma_f32_16x16x32_bf16 v[200:203], v[220:223], v[212:215], v[200:203]\n\t" \
  ".Lpv%=:\n\t" \
  "v_mfma_f32_16x16x32_bf16 v[224:227], v[192:195], v[212:215], v[224:227]\n\t" \
  "v_mfma_f32_16x16x32_bf16 v[228:231], v[196:199], v[212:215], v[228:231]\n\t" \
  "s_cbranch_vccnz .Lvs%=\n\t" \
  ".Lvok%=:\n\t" \
  "s_nop 4\n\t" \
  "v_rcp_f32 v208, v200\n\t" \
  "v_cndmask_b32 v224, v224, v226, %[nh]\n\t" \
  "v_cndmask_b32 v225, v225, v227, %[nh]\n\t" \
  "v_cndmask_b32 v228, v228, v230, %[nh]\n\t" \
  "v_cndmask_b32 v229, v229, v231, %[nh]\n\t" \
  "v_pk_mul_f32 v[224:225], v[224:225], v[208:209] op_sel_hi:[1,0]\n\t" \
  "v_pk_mul_f32 v[228:229], v[228:229], v[208:209] op_sel_hi:[1,0]\n\t" \
  "v_cvt_pk_bf16_f32 v224, v224, v225\n\t" \
  "v_cvt_pk_bf16_f32 v225, v228, v229\n\t" \
  "buffer_store_dwordx2 v[224:225], v251, %[rs], %[so] offen sc1\n\t" \
  "s_memtime s[90:91]\n\t" \
  "s_setprio 0\n\t" \
  "s_waitcnt lgkmcnt(0)\n\t" \
  "s_mov_b32 %[tout], s90\n\t" \
  "s_mov_b32 %[spins], s92\n\t" \
  "s_branch .Lend%=\n\t" \
  ".Lls%=:\n\t" \
  "s_waitcnt lgkmcnt(0)\n\t" \
  "s_barrier\n\t" \
  "s_mov_b32 %[spins], 0\n\t" \
  "s_branch .Lend%=\n\t" \
  ".Lre%=:\n\t" \
  "ds_read_b64_tr_b16 v[240:241], v250 offset:1024\n\t" \
  "ds_read_b64_tr_b16 v[242:243], v250 offset:1088\n\t" \
  "ds_read_b64_tr_b16 v[244:245], v250 offset:1536\n\t" \
  "ds_read_b64_tr_b16 v[246:247], v250 offset:1600\n\t" \
  "ds_read_b64_tr_b16 v[232:233], v250 offset:0\n\t" \
  "ds_read_b64_tr_b16 v[234:235], v250 offset:64\n\t" \
  "s_waitcnt lgkmcnt(0)\n\t" \
  "s_branch .Lrb%=\n\t" \
  ".Lvs%=:\n\t" \
  "v_cmp_eq_u32 vcc, %[sent], v252\n\t" \
  "v_cmp_eq_u32 s[94:95], %[sent], v253\n\t" \
  "s_or_b64 s[94:95], s[94:95], vcc\n\t" \
  "s_cbranch_scc0 .Lvok%=\n\t" \
  "s_add_u32 s92, s92, 1\n\t" \
  "s_sleep 2\n\t" \
  "s_and_b32 s93, s92, 63\n\t" \
  "s_cbranch_scc1 .Lvr%=\n\t" \
  "buffer_load_dword v208, off, %[rs], %[sab] sc1\n\t" \
  "s_waitcnt vmcnt(0)\n\t" \
  "v_readfirstlane_b32 s93, v208\n\t" \
  "s_nop 3\n\t" \
  "s_cmp_eq_u32 s93, 1\n\t" \
  "s_cbranch_scc1 .Lvok%=\n\t" \
  "s_cmp_lt_u32 s92, 0x80000\n\t" \
  "s_cbranch_scc1 .Lvr%=\n\t" \
  "v_mov_b32 v208, 1\n\t" \
  "buffer_store_dword v208, off, %[rs], %[sab] sc1\n\t" \
  "s_branch .Lvok%=\n\t" \
  ".Lvr%=:\n\t" \
  "buffer_load_dword v252, v249, %[rs], %[sv] offen offset:0 sc1\n\t" \
  "buffer_load_dword v253, v249, %[rs], %[sv] offen offset:256 sc1\n\t" \
  "s_waitcnt vmcnt(0)\n\t" \
  "v_mfma_f32_16x16x16_bf16 v[224:227], v[252:253], v[236:237], 0\n\t" \
  "v_mfma_f32_16x16x16_bf16 v[228:231], v[252:253], v[238:239], 0\n\t" \
  "v_max_u32 v208, v252, v253\n\t" \
  "v_cmp_le_u32 vcc, %[sent], v208\n\t" \
  "s_nop 4\n\t" \
  "s_branch .Lpv%=\n\t" \
  "s_branch .Lend%=\n\t" \
  ".Lslow%=:\n\t" \
  "s_mov_b64 s[94:95], 0\n\t" \
  "v_cmp_eq_u32 vcc, %[sent], v200\n\t" \
  "s_or_b64 s[94:95], s[94:95], vcc\n\t" \
  "v_cmp_eq_u32 vcc, %[sent], v201\n\t" \
  "s_or_b64 s[94:95], s[94:95], vcc\n\t" \
  "v_cmp_eq_u32 vcc, %[sent], v202\n\t" \
  "s_or_b64 s[94:95], s[94:95], vcc\n\t" \
  "v_cmp_eq_u32 vcc, %[sent], v203\n\t" \
  "s_or_b64 s[94:95], s[94:95], vcc\n\t" \
  "v_cmp_eq_u32 vcc, %[sent], v204\n\t" \
  "s_or_b64 s[94:95], s[94:95], vcc\n\t" \
  "v_cmp_eq_u32 vcc, %[sent], v205\n\t" \
  "s_or_b64 s[94:95], s[94:95], vcc\n\t" \
  "v_cmp_eq_u32 vcc, %[sent], v206\n\t" \
  "s_or_b64 s[94:95], s[94:95], vcc\n\t" \
  "v_cmp_eq_u32 vcc, %[sent], v207\n\t" \
  "s_or_b64 s[94:95], s[94:95], vcc\n\t" \
  "s_cmp_eq_u64 s[94:95], 0\n\t" \
  "s_cbranch_scc1 .Lok%=\n\t" \
  "v_mov_b64 v[192:193], 0\n\t" \
  "v_mov_b64 v[194:195], 0\n\t" \
  "v_mov_b64 v[196:197], 0\n\t" \
  "v_mov_b64 v[198:199], 0\n\t" \
  "s_add_u32 s92, s92, 1\n\t" \
  "s_cmp_eq_u32 s92, 1\n\t" \
  "s_cbranch_scc1 .Lpoll%=\n\t" \
  "s_sleep 2\n\t" \
  "s_and_b32 s93, s92, 63\n\t" \
  "s_cmp_lg_u32 s93, 0\n\t" \
  "s_cbranch_scc1 .Lpoll%=\n\t" \
  "v_mov_b32 v245, 0\n\t" \
  "buffer_load_dword v244, v245, %[rs], %[sab] offen sc1\n\t" \
  "s_waitcnt vmcnt(0)\n\t" \
  "v_readfirstlane_b32 s93, v244\n\t" \
  "s_nop 3\n\t" \
  "s_cmp_eq_u32 s93, 1\n\t" \
  "s_cbranch_scc1 .Lok%=\n\t" \
  "s_cmp_lt_u32 s92, 0x80000\n\t" \
  "s_cbranch_scc1 .Lpoll%=\n\t" \
  "v_mov_b32 v244, 1\n\t" \
  "buffer_store_dword v244, v245, %[rs], %[sab] offen sc1\n\t" \
  "s_branch .Lok%=\n\t" \
  ".Lend%=:\n\t"
#define QK_ASM_2 \
  "s_waitcnt lgkmcnt(0)\n\t" \
  "v_subrev_u32 v244, 0x8000, v250\n\t" \
  "ds_read_b128 v[216:219], v248 offset:40960\n\t" \
  "ds_read_b128 v[220:223], v248 offset:41984\n\t" \
  "ds_read_b128 v[224:227], v248 offset:43008\n\t" \
  "ds_read_b128 v[228:231], v248 offset:44032\n\t" \
  "ds_read_b128 v[208:211], v244 offset:0\n\t" \
  "ds_read_b128 v[212:215], v244 offset:1024\n\t" \
  "ds_read_b128 v[236:239], v244 offset:2048\n\t" \
  "ds_read_b128 v[240:243], v244 offset:3072\n\t" \
  "v_mov_b64 v[192:193], 0\n\t" \
  "v_mov_b64 v[194:195], 0\n\t" \
  "v_mov_b64 v[196:197], 0\n\t" \
  "v_mov_b64 v[198:199], 0\n\t" \
  ".p2align 5\n\t" \
  "s_nop 0\n\t" \
  "s_nop 0\n\t" \
  "s_nop 0\n\t" \
  "s_nop 0\n\t" \
  "s_movk_i32 s92, 0x2000\n\t" \
  ".Lwt%=:\n\t" \
  "ds_read_b64 v[200:201], v248 offset:30720\n\t" \
  "s_memrealtime s[90:91]\n\t" \
  "s_sub_u32 s92, s92, 1\n\t" \
  "s_waitcnt lgkmcnt(0)\n\t" \
  "v_readfirstlane_b32 s93, v200\n\t" \
  "v_readfirstlane_b32 %[tgt], v201\n\t" \
  "s_cbranch_scc1 .Lgo%=\n\t" \
  "s_cmp_eq_u32 s93, %[step]\n\t" \
  "s_cbranch_scc1 .Lwx%=\n\t" \
  "s_sleep 1\n\t" \
  "s_branch .Lwt%=\n\t" \
  ".Lwx%=:\n\t" \
  "s_lshl_b32 s93, s90, 2\n\t" \
  "s_sub_i32 s93, %[tgt], s93\n\t" \
  "s_cmp_le_i32 s93, 0\n\t" \
  "s_cbranch_scc1 .Lgo%=\n\t" \
  "s_min_i32 s93, s93, 8000\n\t" \
  "s_mov_b32 s94, s93\n\t" \
  ".Lsl%=:\n\t" \
  "s_sub_i32 s94, s94, 18\n\t" \
  "s_cmp_lt_i32 s94, 30\n\t" \
  "s_cbranch_scc1 .Lsx%=\n\t" \
  "s_sleep 1\n\t" \
  "s_branch .Lsl%=\n\t" \
  ".Lsx%=:\n\t" \
  "s_sub_i32 s93, s93, 12\n\t" \
  "s_max_i32 s93, s93, 4\n\t" \
  "s_lshr_b32 s93, s93, 2\n\t" \
  "s_add_u32 s93, s93, s90\n\t" \
  "s_memrealtime s[90:91]\n\t" \
  ".Lpace%=:\n\t" \
  "s_memrealtime s[94:95]\n\t" \
  "s_waitcnt lgkmcnt(1)\n\t" \
  "s_sub_i32 s92, s90, s93\n\t" \
  "s_cmp_ge_i32 s92, 0\n\t" \
  "s_cbranch_scc1 .Le%=\n\t" \
  "s_memrealtime s[90:91]\n\t" \
  "s_waitcnt lgkmcnt(1)\n\t" \
  "s_sub_i32 s92, s94, s93\n\t" \
  "s_cmp_lt_i32 s92, 0\n\t" \
  "s_cbranch_scc1 .Lpace%=\n\t" \
  "s_nop 4\n\t" \
  ".Le%=:\n\t" \
  "s_add_u32 s92, s92, s93\n\t" \
  "s_lshl_b32 s92, s92, 2\n\t" \
  "s_sub_i32 s92, %[tgt], s92\n\t" \
  "s_max_i32 s92, s92, 0\n\t" \
  "s_bitcmp0_b32 s92, 3\n\t" \
  "s_cbranch_scc1 .Lf3%=\n\t" \
  "s_nop 15\n\t" \
  ".Lf3%=:\n\t" \
  "s_bitcmp0_b32 s92, 2\n\t" \
  "s_cbranch_scc1 .Lf2%=\n\t" \
  "s_nop 10\n\t" \
  ".Lf2%=:\n\t" \
  ".Lgo%=:\n\t" \
  "s_mov_b32 s92, 0\n\t" \
  ".Lpoll%=:\n\t" \
  "buffer_load_dwordx4 v[200:203], v248, %[rs], %[som] offen sc1\n\t" \
  "buffer_load_dwordx4 v[204:207], v248, %[rs], %[som2] offen sc1\n\t" \
  ".p2align 5\n\t" \
  "s_waitcnt vmcnt(1) lgkmcnt(0)\n\t" \
  "ds_write_b128 v249, v[200:203]\n\t" \
  "v_smfmac_f32_16x16x64_bf16 v[192:195], v[200:203], v[208:215], v251\n\t" \
  "v_max3_u32 v244, v200, v201, v202\n\t" \
  "v_smfmac_f32_16x16x64_bf16 v[196:199], v[200:203], v[236:243], v251\n\t" \
  "s_waitcnt vmcnt(0)\n\t" \
  "ds_write_b128 v249, v[204:207] offset:8192\n\t" \
  "v_smfmac_f32_16x16x64_bf16 v[192:195], v[204:207], v[216:223], v251\n\t" \
  "v_max3_u32 v244, v244, v203, v204\n\t" \
  "v_max3_u32 v244, v244, v205, v206\n\t" \
  "v_smfmac_f32_16x16x64_bf16 v[196:199], v[204:207], v[224:231], v251\n\t" \
  "v_max_u32 v244, v244, v207\n\t" \
  "v_cmp_le_u32 vcc, %[sent], v244\n\t" \
  "s_cbranch_vccnz .Lslow%=\n\t" \
  ".Lok%=:\n\t" \
  "ds_read_b128 v[216:219], v250 offset:32768\n\t" \
  "ds_read_b128 v[220:223], v250 offset:33792\n\t" \
  "ds_read_b128 v[224:227], v250 offset:34816\n\t" \
  "ds_read_b128 v[228:231], v250 offset:35840\n\t" \
  "s_waitcnt lgkmcnt(4)\n\t" \
  "s_barrier\n\t" \
  "ds_read_b128 v[208:211], v248 offset:3072\n\t" \
  "ds_read_b128 v[212:215], v248 offset:4096\n\t" \
  "ds_read_b128 v[236:239], v248 offset:5120\n\t" \
  "ds_read_b128 v[240:243], v248 offset:6144\n\t" \
  "s_cmp_eq_u32 %[step], 63\n\t" \
  "s_cbranch_scc1 .Lls%=\n\t" \
  "s_waitcnt lgkmcnt(3)\n\t" \
  "v_smfmac_f32_16x16x64_bf16 v[192:195], v[208:211], v[216:223], v251\n\t" \
  "ds_read_b128 v[244:247], v248 offset:7168\n\t" \
  "v_smfmac_f32_16x16x64_bf16 v[196:199], v[208:211], v[224:231], v251\n\t" \
  "s_waitcnt lgkmcnt(3)\n\t" \
  "ds_read_b128 v[200:203], v248 offset:13312\n\t" \
  "v_smfmac_f32_16x16x64_bf16 v[192:195], v[212:215], v[0:7], v251\n\t" \
  "ds_read_b128 v[216:219], v250 offset:0\n\t" \
  "v_smfmac_f32_16x16x64_bf16 v[196:199], v[212:215], v[8:15], v251\n\t" \
  "s_waitcnt lgkmcnt(4)\n\t" \
  "ds_read_b128 v[220:223], v250 offset:1024\n\t" \
  "v_smfmac_f32_16x16x64_bf16 v[192:195], v[236:239], v[16:23], v251\n\t" \
  "ds_read_b128 v[224:227], v250 offset:2048\n\t" \
  "v_smfmac_f32_16x16x64_bf16 v[196:199], v[236:239], v[24:31], v251\n\t" \
  "s_waitcnt lgkmcnt(4)\n\t" \
  "ds_read_b128 v[228:231], v250 offset:3072\n\t" \
  "v_smfmac_f32_16x16x64_bf16 v[192:195], v[240:243], v[32:39], v251\n\t" \
  "ds_read_b128 v[204:207], v248 offset:8192\n\t" \
  "v_smfmac_f32_16x16x64_bf16 v[196:199], v[240:243], v[40:47], v251\n\t" \
  "ds_read_b128 v[208:211], v248 offset:9216\n\t" \
  "v_smfmac_f32_16x16x64_bf16 v[192:195], v[244:247], v[48:55], v251\n\t" \
  "ds_read_b128 v[212:215], v248 offset:11264\n\t" \
  "v_smfmac_f32_16x16x64_bf16 v[196:199], v[244:247], v[56:63], v251\n\t" \
  "s_waitcnt lgkmcnt(5)\n\t" \
  "v_smfmac_f32_16x16x64_bf16 v[192:195], v[200:203], v[216:223], v251\n\t" \
  "s_waitcnt lgkmcnt(3)\n\t" \
  "ds_read_b128 v[236:239], v248 offset:12288\n\t" \
  "v_smfmac_f32_16x16x64_bf16 v[196:199], v[200:203], v[224:231], v251\n\t" \
  "s_waitcnt lgkmcnt(3)\n\t" \
  "v_smfmac_f32_16x16x64_bf16 v[192:195], v[204:207], v[64:71], v251\n\t" \
  "ds_read_b128 v[240:243], v248 offset:14336\n\t" \
  "v_smfmac_f32_16x16x64_bf16 v[196:199], v[204:207], v[72:79], v251\n\t" \
  "s_waitcnt lgkmcnt(2)\n\t" \
  "v_smfmac_f32_16x16x64_bf16 v[192:195], v[208:211], v[80:87], v251\n\t" \
  "ds_read_b128 v[244:247], v248 offset:15360\n\t" \
  "v_smfmac_f32_16x16x64_bf16 v[196:199], v[208:211], v[88:95], v251\n\t" \
  "ds_read_b128 v[200:203], v248 offset:0\n\t" \
  "v_smfmac_f32_16x16x64_bf16 v[192:195], v[212:215], v[96:103], v251\n\t" \
  "ds_read_b128 v[204:207], v248 offset:1024\n\t" \
  "v_smfmac_f32_16x16x64_bf16 v[196:199], v[212:215], v[104:111], v251\n\t" \
  "s_waitcnt lgkmcnt(3)\n\t" \
  "v_smfmac_f32_16x16x64_bf16 v[192:195], v[236:239], v[112:119], v251\n\t" \
  "v_smfmac_f32_16x16x64_bf16 v[196:199], v[236:239], v[120:127], v251\n\t" \
  "v_smfmac_f32_16x16x64_bf16 v[192:195], v[240:243], v[128:135], v251\n\t" \
  "v_smfmac_f32_16x16x64_bf16 v[196:199], v[240:243], v[136:143], v251\n\t" \
  "s_waitcnt lgkmcnt(0)\n\t" \
  "v_smfmac_f32_16x16x64_bf16 v[192:195], v[244:247], v[144:151], v251\n\t" \
  "v_smfmac_f32_16x16x64_bf16 v[196:199], v[244:247], v[152:159], v251\n\t" \
  "v_smfmac_f32_16x16x64_bf16 v[192:195], v[200:203], v[160:167], v251\n\t" \
  "v_smfmac_f32_16x16x64_bf16 v[196:199], v[200:203], v[168:175], v251\n\t" \
  "v_smfmac_f32_16x16x64_bf16 v[192:195], v[204:207], v[176:183], v251\n\t" \
  "v_smfmac_f32_16x16x64_bf16 v[196:199], v[204:207], v[184:191], v251\n\t" \
  "ds_read_b128 v[200:203], v248 offset:18432\n\t" \
  "ds_read_b128 v[204:207], v248 offset:22528\n\t" \
  "v_mov_b32 v250, v232\n\t" \
  "v_mov_b32 v249, v233\n\t" \
  "s_nop 2\n\t" \
  "v_add_f32 v192, v192, v194\n\t" \
  "v_add_f32 v193, v193, v195\n\t" \
  "v_add_f32 v196, v196, v198\n\t" \
  "v_add_f32 v197, v197, v199\n\t" \
  "v_cvt_pk_bf16_f32 v192, v192, v193\n\t" \
  "v_cvt_pk_bf16_f32 v196, v196, v197\n\t" \
  "ds_write2_b32 v252, v192, v196 offset1:64\n\t" \
  "v_mov_b32 v251, v234\n\t" \
  "ds_read_b64_tr_b16 v[240:241], v250 offset:1024\n\t" \
  "ds_read_b64_tr_b16 v[242:243], v250 offset:1088\n\t" \
  "v_mov_b32 v212, %[step]\n\t" \
  "s_mov_b64 exec, 1\n\t" \
  "ds_write_b32 v248, v212 offset:28704\n\t" \
  "s_mov_b64 exec, -1\n\t" \
  "buffer_load_dwordx4 v[192:195], v248, %[rt], %[svt] offen offset:0 sc1\n\t" \
  "buffer_load_dwordx4 v[196:199], v248, %[rt], %[svt] offen offset:1024 sc1\n\t" \
  "s_waitcnt lgkmcnt(1)\n\t" \
  "v_mfma_f32_16x16x32_bf16 v[200:203], v[200:203], v[240:243], 0\n\t" \
  "v_mfma_f32_16x16x32_bf16 v[204:207], v[204:207], v[240:243], 0\n\t" \
  "s_nop 7\n\t" \
  "v_cndmask_b32 v216, v200, v204, %[nh]\n\t" \
  "v_cndmask_b32 v217, v201, v205, %[nh]\n\t" \
  "v_cndmask_b32 v218, v202, v206, %[nh]\n\t" \
  "v_cndmask_b32 v219, v203, v207, %[nh]\n\t" \
  "ds_write_b128 v248, v[216:219] offset:18432\n\t" \
  "buffer_load_dword v252, v249, %[rs], %[sv] offen offset:0 sc1\n\t" \
  "buffer_load_dword v253, v249, %[rs], %[sv] offen offset:256 sc1\n\t" \
  "ds_read_b32 v212, v248 offset:28672\n\t" \
  "ds_read_b64_tr_b16 v[244:245], v250 offset:1536\n\t" \
  "ds_read_b64_tr_b16 v[246:247], v250 offset:1600\n\t" \
  "ds_read_b64_tr_b16 v[232:233], v250 offset:0\n\t" \
  "ds_read_b64_tr_b16 v[234:235], v250 offset:64\n\t" \
  "ds_read_b64_tr_b16 v[236:237], v250 offset:512\n\t" \
  "ds_read_b64_tr_b16 v[238:239], v250 offset:576\n\t" \
  "s_setprio 3\n\t" \
  "s_waitcnt lgkmcnt(0)\n\t" \
  "v_cmp_ne_u32 vcc, %[step], v212\n\t" \
  "s_and_b32 s93, vcc_lo, 0xf\n\t" \
  "s_barrier\n\t" \
  "s_cbranch_scc1 .Lre%=\n\t" \
  ".Lrb%=:\n\t" \
  "ds_read_b64_tr_b16 v[208:209], v250 offset:3072\n\t" \
  "ds_read_b64_tr_b16 v[210:211], v250 offset:3136\n\t" \
  "ds_read_b64_tr_b16 v[212:213], v250 offset:3584\n\t" \
  "ds_read_b64_tr_b16 v[214:215], v250 offset:3648\n\t" \
  "ds_read_b64_tr_b16 v[200:201], v250 offset:2048\n\t" \
  "ds_read_b64_tr_b16 v[202:203], v250 offset:2112\n\t" \
  "ds_read_b64_tr_b16 v[204:205], v250 offset:2560\n\t" \
  "ds_read_b64_tr_b16 v[206:207], v250 offset:2624\n\t" \
  "ds_read_b128 v[220:223], v248 offset:19456\n\t" \
  "ds_read_b128 v[224:227], v248 offset:16384\n\t" \
  "ds_read_b128 v[228:231], v248 offset:17408\n\t" \
  "s_waitcnt lgkmcnt(9)\n\t" \
  "v_mfma_f32_16x16x32_bf16 v[208:211], v[208:211], v[240:243], 0\n\t" \
  "s_waitcnt lgkmcnt(7)\n\t" \
  "v_mfma_f32_16x16x32_bf16 v[208:211], v[212:215], v[244:247], v[208:211]\n\t" \
  "s_waitcnt lgkmcnt(5)\n\t" \
  "v_mfma_f32_16x16x32_bf16 v[208:211], v[200:203], v[232:235], v[208:211]\n\t" \
  "s_waitcnt lgkmcnt(3)\n\t" \
  "v_mfma_f32_16x16x32_bf16 v[208:211], v[204:207], v[236:239], v[208:211]\n\t" \
  "s_waitcnt lgkmcnt(2)\n\t" \
  "v_pk_add_f32 v[216:217], v[216:217], v[220:221]\n\t" \
  "v_pk_add_f32 v[218:219], v[218:219], v[222:223]\n\t" \
  "s_waitcnt lgkmcnt(0)\n\t" \
  "v_pk_add_f32 v[224:225], v[224:225], v[228:229]\n\t" \
  "v_pk_add_f32 v[226:227], v[226:227], v[230:231]\n\t" \
  "v_pk_add_f32 v[216:217], v[216:217], v[224:225]\n\t" \
  "v_pk_add_f32 v[218:219], v[218:219], v[226:227]\n\t" \
  "v_max3_f32 v224, v216, v217, v218\n\t" \
  "v_max_f32 v224, v224, v219\n\t" \
  "s_nop 1\n\t" \
  "v_max_f32_dpp v225, v224, v224 row_ror:8 row_mask:0xf bank_mask:0xf\n\t" \
  "v_max_f32_dpp v226, v224, v224 row_ror:8 row_mask:0xf bank_mask:0xf\n\t" \
  "s_nop 1\n\t" \
  "v_permlane32_swap_b32 v225, v226\n\t" \
  "v_max_f32 v231, v225, v226\n\t" \
  "v_mov_b32 v220, 0x3f803f80\n\t" \
  "v_mov_b32 v221, 0x3f803f80\n\t" \
  "v_mov_b64 v[222:223], v[220:221]\n\t" \
  "v_mov_b32 v241, 0\n\t" \
  "v_mov_b32 v242, 0\n\t" \
  "v_max3_f32 v212, v208, v209, v231\n\t" \
  "v_max3_f32 v213, v212, v210, v211\n\t" \
  "v_max3_f32 v214, v212, v210, v211\n\t" \
  "v_cndmask_b32 v208, v208, v210, %[up]\n\t" \
  "v_cndmask_b32 v209, v209, v211, %[up]\n\t" \
  "v_permlane16_swap_b32 v213, v214\n\t" \
  "v_max_f32 v212, v213, v214\n\t" \
  "v_mul_f32 v212, %[negc], v212\n\t" \
  "v_fma_f32 v208, v208, %[c], v212\n\t" \
  "v_fma_f32 v209, v209, %[c], v212\n\t" \
  "v_exp_f32 v208, v208\n\t" \
  "v_fma_f32 v216, v216, %[c], v212\n\t" \
  "v_exp_f32 v209, v209\n\t" \
  "v_fma_f32 v217, v217, %[c], v212\n\t" \
  "v_exp_f32 v216, v216\n\t" \
  "v_fma_f32 v218, v218, %[c], v212\n\t" \
  "v_cvt_pk_bf16_f32 v240, v208, v209\n\t" \
  "v_cvt_pk_bf16_f32 v243, v208, v209\n\t" \
  "v_exp_f32 v217, v217\n\t" \
  "v_fma_f32 v219, v219, %[c], v212\n\t" \
  "v_mfma_f32_16x16x16_bf16 v[204:207], v[220:221], v[240:241], 0\n\t" \
  "v_exp_f32 v218, v218\n\t" \
  "s_waitcnt vmcnt(0)\n\t" \
  "v_mfma_f32_16x16x16_bf16 v[224:227], v[252:253], v[240:241], 0\n\t" \
  "v_cvt_pk_bf16_f32 v202, v216, v217\n\t" \
  "v_cvt_pk_bf16_f32 v200, v216, v217\n\t" \
  "v_exp_f32 v219, v219\n\t" \
  "v_mfma_f32_16x16x16_bf16 v[228:231], v[252:253], v[242:243], 0\n\t" \
  "v_cvt_pk_bf16_f32 v203, v218, v219\n\t" \
  "v_cvt_pk_bf16_f32 v201, v218, v219\n\t" \
  "v_mov_b32_dpp v200, v202 row_ror:8 row_mask:0xf bank_mask:0xc\n\t" \
  "v_mov_b32_dpp v202, v202 row_ror:8 row_mask:0xf bank_mask:0x3\n\t" \
  "v_mov_b32_dpp v201, v203 row_ror:8 row_mask:0xf bank_mask:0xc\n\t" \
  "v_mov_b32_dpp v203, v203 row_ror:8 row_mask:0xf bank_mask:0x3\n\t" \
  "v_max_u32 v212, v252, v253\n\t" \
  "v_cmp_le_u32 vcc, %[sent], v212\n\t" \
  "v_mfma_f32_16x16x32_bf16 v[204:207], v[220:223], v[200:203], v[204:207]\n\t" \
  ".Lpv%=:\n\t" \
  "v_mfma_f32_16x16x32_bf16 v[224:227], v[192:195], v[200:203], v[224:227]\n\t" \
  "v_mfma_f32_16x16x32_bf16 v[228:231], v[196:199], v[200:203], v[228:231]\n\t" \
  "s_cbranch_vccnz .Lvs%=\n\t" \
  ".Lvok%=:\n\t" \
  "s_nop 4\n\t" \
  "v_rcp_f32 v212, v204\n\t" \
  "v_cndmask_b32 v224, v224, v226, %[nh]\n\t" \
  "v_cndmask_b32 v225, v225, v227, %[nh]\n\t" \
  "v_cndmask_b32 v228, v228, v230, %[nh]\n\t" \
  "v_cndmask_b32 v229, v229, v231, %[nh]\n\t" \
  "v_pk_mul_f32 v[224:225], v[224:225], v[212:213] op_sel_hi:[1,0]\n\t" \
  "v_pk_mul_f32 v[228:229], v[228:229], v[212:213] op_sel_hi:[1,0]\n\t" \
  "v_cvt_pk_bf16_f32 v224, v224, v225\n\t" \
  "v_cvt_pk_bf16_f32 v225, v228, v229\n\t" \
  "buffer_store_dwordx2 v[224:225], v251, %[rs], %[so] offen sc1\n\t" \
  "s_memtime s[90:91]\n\t" \
  "s_setprio 0\n\t" \
  "s_waitcnt lgkmcnt(0)\n\t" \
  "s_mov_b32 %[tout], s90\n\t" \
  "s_mov_b32 %[spins], s92\n\t" \
  "s_branch .Lend%=\n\t" \
  ".Lls%=:\n\t" \
  "s_waitcnt lgkmcnt(0)\n\t" \
  "s_barrier\n\t" \
  "s_mov_b32 %[spins], 0\n\t" \
  "s_branch .Lend%=\n\t" \
  ".Lre%=:\n\t" \
  "ds_read_b64_tr_b16 v[244:245], v250 offset:1536\n\t" \
  "ds_read_b64_tr_b16 v[246:247], v250 offset:1600\n\t" \
  "ds_read_b64_tr_b16 v[232:233], v250 offset:0\n\t" \
  "ds_read_b64_tr_b16 v[234:235], v250 offset:64\n\t" \
  "ds_read_b64_tr_b16 v[236:237], v250 offset:512\n\t" \
  "ds_read_b64_tr_b16 v[238:239], v250 offset:576\n\t" \
  "s_waitcnt lgkmcnt(0)\n\t" \
  "s_branch .Lrb%=\n\t" \
  ".Lvs%=:\n\t" \
  "v_cmp_eq_u32 vcc, %[sent], v252\n\t" \
  "v_cmp_eq_u32 s[94:95], %[sent], v253\n\t" \
  "s_or_b64 s[94:95], s[94:95], vcc\n\t" \
  "s_cbranch_scc0 .Lvok%=\n\t" \
  "s_add_u32 s92, s92, 1\n\t" \
  "s_sleep 2\n\t" \
  "s_and_b32 s93, s92, 63\n\t" \
  "s_cbranch_scc1 .Lvr%=\n\t" \
  "buffer_load_dword v212, off, %[rs], %[sab] sc1\n\t" \
  "s_waitcnt vmcnt(0)\n\t" \
  "v_readfirstlane_b32 s93, v212\n\t" \
  "s_nop 3\n\t" \
  "s_cmp_eq_u32 s93, 1\n\t" \
  "s_cbranch_scc1 .Lvok%=\n\t" \
  "s_cmp_lt_u32 s92, 0x80000\n\t" \
  "s_cbranch_scc1 .Lvr%=\n\t" \
  "v_mov_b32 v212, 1\n\t" \
  "buffer_store_dword v212, off, %[rs], %[sab] sc1\n\t" \
  "s_branch .Lvok%=\n\t" \
  ".Lvr%=:\n\t" \
  "buffer_load_dword v252, v249, %[rs], %[sv] offen offset:0 sc1\n\t" \
  "buffer_load_dword v253, v249, %[rs], %[sv] offen offset:256 sc1\n\t" \
  "s_waitcnt vmcnt(0)\n\t" \
  "v_mfma_f32_16x16x16_bf16 v[224:227], v[252:253], v[240:241], 0\n\t" \
  "v_mfma_f32_16x16x16_bf16 v[228:231], v[252:253], v[242:243], 0\n\t" \
  "v_max_u32 v212, v252, v253\n\t" \
  "v_cmp_le_u32 vcc, %[sent], v212\n\t" \
  "s_nop 4\n\t" \
  "s_branch .Lpv%=\n\t" \
  "s_branch .Lend%=\n\t" \
  ".Lslow%=:\n\t" \
  "s_mov_b64 s[94:95], 0\n\t" \
  "v_cmp_eq_u32 vcc, %[sent], v200\n\t" \
  "s_or_b64 s[94:95], s[94:95], vcc\n\t" \
  "v_cmp_eq_u32 vcc, %[sent], v201\n\t" \
  "s_or_b64 s[94:95], s[94:95], vcc\n\t" \
  "v_cmp_eq_u32 vcc, %[sent], v202\n\t" \
  "s_or_b64 s[94:95], s[94:95], vcc\n\t" \
  "v_cmp_eq_u32 vcc, %[sent], v203\n\t" \
  "s_or_b64 s[94:95], s[94:95], vcc\n\t" \
  "v_cmp_eq_u32 vcc, %[sent], v204\n\t" \
  "s_or_b64 s[94:95], s[94:95], vcc\n\t" \
  "v_cmp_eq_u32 vcc, %[sent], v205\n\t" \
  "s_or_b64 s[94:95], s[94:95], vcc\n\t" \
  "v_cmp_eq_u32 vcc, %[sent], v206\n\t" \
  "s_or_b64 s[94:95], s[94:95], vcc\n\t" \
  "v_cmp_eq_u32 vcc, %[sent], v207\n\t" \
  "s_or_b64 s[94:95], s[94:95], vcc\n\t" \
  "s_cmp_eq_u64 s[94:95], 0\n\t" \
  "s_cbranch_scc1 .Lok%=\n\t" \
  "v_mov_b64 v[192:193], 0\n\t" \
  "v_mov_b64 v[194:195], 0\n\t" \
  "v_mov_b64 v[196:197], 0\n\t" \
  "v_mov_b64 v[198:199], 0\n\t" \
  "s_add_u32 s92, s92, 1\n\t" \
  "s_cmp_eq_u32 s92, 1\n\t" \
  "s_cbranch_scc1 .Lpoll%=\n\t" \
  "s_sleep 2\n\t" \
  "s_and_b32 s93, s92, 63\n\t" \
  "s_cmp_lg_u32 s93, 0\n\t" \
  "s_cbranch_scc1 .Lpoll%=\n\t" \
  "v_mov_b32 v245, 0\n\t" \
  "buffer_load_dword v244, v245, %[rs], %[sab] offen sc1\n\t" \
  "s_waitcnt vmcnt(0)\n\t" \
  "v_readfirstlane_b32 s93, v244\n\t" \
  "s_nop 3\n\t" \
  "s_cmp_eq_u32 s93, 1\n\t" \
  "s_cbranch_scc1 .Lok%=\n\t" \
  "s_cmp_lt_u32 s92, 0x80000\n\t" \
  "s_cbranch_scc1 .Lpoll%=\n\t" \
  "v_mov_b32 v244, 1\n\t" \
  "buffer_store_dword v244, v245, %[rs], %[sab] offen sc1\n\t" \
  "s_branch .Lok%=\n\t" \
  ".Lend%=:\n\t"
#define QK_ASM_3 \
  "s_waitcnt lgkmcnt(0)\n\t" \
  "v_subrev_u32 v244, 0x8000, v250\n\t" \
  "ds_read_b128 v[216:219], v248 offset:45056\n\t" \
  "ds_read_b128 v[220:223], v248 offset:46080\n\t" \
  "ds_read_b128 v[224:227], v248 offset:47104\n\t" \
  "ds_read_b128 v[228:231], v248 offset:48128\n\t" \
  "ds_read_b128 v[208:211], v244 offset:0\n\t" \
  "ds_read_b128 v[212:215], v244 offset:1024\n\t" \
  "ds_read_b128 v[236:239], v244 offset:2048\n\t" \
  "ds_read_b128 v[240:243], v244 offset:3072\n\t" \
  "v_mov_b64 v[192:193], 0\n\t" \
  "v_mov_b64 v[194:195], 0\n\t" \
  "v_mov_b64 v[196:197], 0\n\t" \
  "v_mov_b64 v[198:199], 0\n\t" \
  ".p2align 5\n\t" \
  "s_nop 0\n\t" \
  "s_nop 0\n\t" \
  "s_nop 0\n\t" \
  "s_nop 0\n\t" \
  "s_movk_i32 s92, 0x2000\n\t" \
  ".Lwt%=:\n\t" \
  "ds_read_b64 v[200:201], v248 offset:30720\n\t" \
  "s_memrealtime s[90:91]\n\t" \
  "s_sub_u32 s92, s92, 1\n\t" \
  "s_waitcnt lgkmcnt(0)\n\t" \
  "v_readfirstlane_b32 s93, v200\n\t" \
  "v_readfirstlane_b32 %[tgt], v201\n\t" \
  "s_cbranch_scc1 .Lgo%=\n\t" \
  "s_cmp_eq_u32 s93, %[step]\n\t" \
  "s_cbranch_scc1 .Lwx%=\n\t" \
  "s_sleep 1\n\t" \
  "s_branch .Lwt%=\n\t" \
  ".Lwx%=:\n\t" \
  "s_lshl_b32 s93, s90, 2\n\t" \
  "s_sub_i32 s93, %[tgt], s93\n\t" \
  "s_cmp_le_i32 s93, 0\n\t" \
  "s_cbranch_scc1 .Lgo%=\n\t" \
  "s_min_i32 s93, s93, 8000\n\t" \
  "s_mov_b32 s94, s93\n\t" \
  ".Lsl%=:\n\t" \
  "s_sub_i32 s94, s94, 18\n\t" \
  "s_cmp_lt_i32 s94, 30\n\t" \
  "s_cbranch_scc1 .Lsx%=\n\t" \
  "s_sleep 1\n\t" \
  "s_branch .Lsl%=\n\t" \
  ".Lsx%=:\n\t" \
  "s_sub_i32 s93, s93, 12\n\t" \
  "s_max_i32 s93, s93, 4\n\t" \
  "s_lshr_b32 s93, s93, 2\n\t" \
  "s_add_u32 s93, s93, s90\n\t" \
  "s_memrealtime s[90:91]\n\t" \
  ".Lpace%=:\n\t" \
  "s_memrealtime s[94:95]\n\t" \
  "s_waitcnt lgkmcnt(1)\n\t" \
  "s_sub_i32 s92, s90, s93\n\t" \
  "s_cmp_ge_i32 s92, 0\n\t" \
  "s_cbranch_scc1 .Le%=\n\t" \
  "s_memrealtime s[90:91]\n\t" \
  "s_waitcnt lgkmcnt(1)\n\t" \
  "s_sub_i32 s92, s94, s93\n\t" \
  "s_cmp_lt_i32 s92, 0\n\t" \
  "s_cbranch_scc1 .Lpace%=\n\t" \
  "s_nop 4\n\t" \
  ".Le%=:\n\t" \
  "s_add_u32 s92, s92, s93\n\t" \
  "s_lshl_b32 s92, s92, 2\n\t" \
  "s_sub_i32 s92, %[tgt], s92\n\t" \
  "s_max_i32 s92, s92, 0\n\t" \
  "s_bitcmp0_b32 s92, 3\n\t" \
  "s_cbranch_scc1 .Lf3%=\n\t" \
  "s_nop 15\n\t" \
  ".Lf3%=:\n\t" \
  "s_bitcmp0_b32 s92, 2\n\t" \
  "s_cbranch_scc1 .Lf2%=\n\t" \
  "s_nop 10\n\t" \
  ".Lf2%=:\n\t" \
  ".Lgo%=:\n\t" \
  "s_mov_b32 s92, 0\n\t" \
  ".Lpoll%=:\n\t" \
  "buffer_load_dwordx4 v[200:203], v248, %[rs], %[som] offen sc1\n\t" \
  "buffer_load_dwordx4 v[204:207], v248, %[rs], %[som2] offen sc1\n\t" \
  ".p2align 5\n\t" \
  "s_waitcnt vmcnt(1) lgkmcnt(0)\n\t" \
  "ds_write_b128 v249, v[200:203]\n\t" \
  "v_smfmac_f32_16x16x64_bf16 v[192:195], v[200:203], v[208:215], v251\n\t" \
  "v_max3_u32 v244, v200, v201, v202\n\t" \
  "v_smfmac_f32_16x16x64_bf16 v[196:199], v[200:203], v[236:243], v251\n\t" \
  "s_waitcnt vmcnt(0)\n\t" \
  "ds_write_b128 v249, v[204:207] offset:8192\n\t" \
  "v_smfmac_f32_16x16x64_bf16 v[192:195], v[204:207], v[216:223], v251\n\t" \
  "v_max3_u32 v244, v244, v203, v204\n\t" \
  "v_max3_u32 v244, v244, v205, v206\n\t" \
  "v_smfmac_f32_16x16x64_bf16 v[196:199], v[204:207], v[224:231], v251\n\t" \
  "v_max_u32 v244, v244, v207\n\t" \
  "v_cmp_le_u32 vcc, %[sent], v244\n\t" \
  "s_cbranch_vccnz .Lslow%=\n\t" \
  ".Lok%=:\n\t" \
  "ds_read_b128 v[216:219], v250 offset:32768\n\t" \
  "ds_read_b128 v[220:223], v250 offset:33792\n\t" \
  "ds_read_b128 v[224:227], v250 offset:34816\n\t" \
  "ds_read_b128 v[228:231], v250 offset:35840\n\t" \
  "s_waitcnt lgkmcnt(4)\n\t" \
  "s_barrier\n\t" \
  "ds_read_b128 v[208:211], v248 offset:2048\n\t" \
  "ds_read_b128 v[212:215], v248 offset:5120\n\t" \
  "ds_read_b128 v[236:239], v248 offset:6144\n\t" \
  "ds_read_b128 v[240:243], v248 offset:7168\n\t" \
  "s_cmp_eq_u32 %[step], 63\n\t" \
  "s_cbranch_scc1 .Lls%=\n\t" \
  "s_waitcnt lgkmcnt(3)\n\t" \
  "v_smfmac_f32_16x16x64_bf16 v[192:195], v[208:211], v[216:223], v251\n\t" \
  "ds_read_b128 v[244:247], v248 offset:8192\n\t" \
  "v_smfmac_f32_16x16x64_bf16 v[196:199], v[208:211], v[224:231], v251\n\t" \
  "s_waitcnt lgkmcnt(3)\n\t" \
  "ds_read_b128 v[200:203], v248 offset:12288\n\t" \
  "v_smfmac_f32_16x16x64_bf16 v[192:195], v[212:215], v[0:7], v251\n\t" \
  "ds_read_b128 v[216:219], v250 offset:0\n\t" \
  "v_smfmac_f32_16x16x64_bf16 v[196:199], v[212:215], v[8:15], v251\n\t" \
  "s_waitcnt lgkmcnt(4)\n\t" \
  "ds_read_b128 v[220:223], v250 offset:1024\n\t" \
  "v_smfmac_f32_16x16x64_bf16 v[192:195], v[236:239], v[16:23], v251\n\t" \
  "ds_read_b128 v[224:227], v250 offset:2048\n\t" \
  "v_smfmac_f32_16x16x64_bf16 v[196:199], v[236:239], v[24:31], v251\n\t" \
  "s_waitcnt lgkmcnt(4)\n\t" \
  "ds_read_b128 v[228:231], v250 offset:3072\n\t" \
  "v_smfmac_f32_16x16x64_bf16 v[192:195], v[240:243], v[32:39], v251\n\t" \
  "ds_read_b128 v[204:207], v248 offset:9216\n\t" \
  "v_smfmac_f32_16x16x64_bf16 v[196:199], v[240:243], v[40:47], v251\n\t" \
  "ds_read_b128 v[208:211], v248 offset:10240\n\t" \
  "v_smfmac_f32_16x16x64_bf16 v[192:195], v[244:247], v[48:55], v251\n\t" \
  "ds_read_b128 v[212:215], v248 offset:13312\n\t" \
  "v_smfmac_f32_16x16x64_bf16 v[196:199], v[244:247], v[56:63], v251\n\t" \
  "s_waitcnt lgkmcnt(5)\n\t" \
  "v_smfmac_f32_16x16x64_bf16 v[192:195], v[200:203], v[216:223], v251\n\t" \
  "s_waitcnt lgkmcnt(3)\n\t" \
  "ds_read_b128 v[236:239], v248 offset:14336\n\t" \
  "v_smfmac_f32_16x16x64_bf16 v[196:199], v[200:203], v[224:231], v251\n\t" \
  "s_waitcnt lgkmcnt(3)\n\t" \
  "v_smfmac_f32_16x16x64_bf16 v[192:195], v[204:207], v[64:71], v251\n\t" \
  "ds_read_b128 v[240:243], v248 offset:15360\n\t" \
  "v_smfmac_f32_16x16x64_bf16 v[196:199], v[204:207], v[72:79], v251\n\t" \
  "s_waitcnt lgkmcnt(2)\n\t" \
  "v_smfmac_f32_16x16x64_bf16 v[192:195], v[208:211], v[80:87], v251\n\t" \
  "ds_read_b128 v[244:247], v248 offset:0\n\t" \
  "v_smfmac_f32_16x16x64_bf16 v[196:199], v[208:211], v[88:95], v251\n\t" \
  "ds_read_b128 v[200:203], v248 offset:1024\n\t" \
  "v_smfmac_f32_16x16x64_bf16 v[192:195], v[212:215], v[96:103], v251\n\t" \
  "ds_read_b128 v[204:207], v248 offset:4096\n\t" \
  "v_smfmac_f32_16x16x64_bf16 v[196:199], v[212:215], v[104:111], v251\n\t" \
  "s_waitcnt lgkmcnt(3)\n\t" \
  "v_smfmac_f32_16x16x64_bf16 v[192:195], v[236:239], v[112:119], v251\n\t" \
  "v_smfmac_f32_16x16x64_bf16 v[196:199], v[236:239], v[120:127], v251\n\t" \
  "v_smfmac_f32_16x16x64_bf16 v[192:195], v[240:243], v[128:135], v251\n\t" \
  "v_smfmac_f32_16x16x64_bf16 v[196:199], v[240:243], v[136:143], v251\n\t" \
  "s_waitcnt lgkmcnt(0)\n\t" \
  "v_smfmac_f32_16x16x64_bf16 v[192:195], v[244:247], v[144:151], v251\n\t" \
  "v_smfmac_f32_16x16x64_bf16 v[196:199], v[244:247], v[152:159], v251\n\t" \
  "v_smfmac_f32_16x16x64_bf16 v[192:195], v[200:203], v[160:167], v251\n\t" \
  "v_smfmac_f32_16x16x64_bf16 v[196:199], v[200:203], v[168:175], v251\n\t" \
  "v_smfmac_f32_16x16x64_bf16 v[192:195], v[204:207], v[176:183], v251\n\t" \
  "v_smfmac_f32_16x16x64_bf16 v[196:199], v[204:207], v[184:191], v251\n\t" \
  "ds_read_b128 v[200:203], v248 offset:19456\n\t" \
  "ds_read_b128 v[204:207], v248 offset:23552\n\t" \
  "v_mov_b32 v250, v232\n\t" \
  "v_mov_b32 v249, v233\n\t" \
  "s_nop 2\n\t" \
  "v_add_f32 v192, v192, v194\n\t" \
  "v_add_f32 v193, v193, v195\n\t" \
  "v_add_f32 v196, v196, v198\n\t" \
  "v_add_f32 v197, v197, v199\n\t" \
  "v_cvt_pk_bf16_f32 v192, v192, v193\n\t" \
  "v_cvt_pk_bf16_f32 v196, v196, v197\n\t" \
  "ds_write2_b32 v252, v192, v196 offset1:64\n\t" \
  "v_mov_b32 v251, v234\n\t" \
  "ds_read_b64_tr_b16 v[244:245], v250 offset:1536\n\t" \
  "ds_read_b64_tr_b16 v[246:247], v250 offset:1600\n\t" \
  "v_mov_b32 v212, %[step]\n\t" \
  "s_mov_b64 exec, 1\n\t" \
  "ds_write_b32 v248, v212 offset:28720\n\t" \
  "s_mov_b64 exec, -1\n\t" \
  "buffer_load_dwordx4 v[192:195], v248, %[rt], %[svt] offen offset:0 sc1\n\t" \
  "buffer_load_dwordx4 v[196:199], v248, %[rt], %[svt] offen offset:1024 sc1\n\t" \
  "s_waitcnt lgkmcnt(1)\n\t" \
  "v_mfma_f32_16x16x32_bf16 v[200:203], v[200:203], v[244:247], 0\n\t" \
  "v_mfma_f32_16x16x32_bf16 v[204:207], v[204:207], v[244:247], 0\n\t" \
  "s_nop 7\n\t" \
  "v_cndmask_b32 v216, v200, v204, %[nh]\n\t" \
  "v_cndmask_b32 v217, v201, v205, %[nh]\n\t" \
  "v_cndmask_b32 v218, v202, v206, %[nh]\n\t" \
  "v_cndmask_b32 v219, v203, v207, %[nh]\n\t" \
  "ds_write_b128 v248, v[216:219] offset:19456\n\t" \
  "buffer_load_dword v252, v249, %[rs], %[sv] offen offset:0 sc1\n\t" \
  "buffer_load_dword v253, v249, %[rs], %[sv] offen offset:256 sc1\n\t" \
  "ds_read_b32 v212, v248 offset:28672\n\t" \
  "ds_read_b64_tr_b16 v[232:233], v250 offset:0\n\t" \
  "ds_read_b64_tr_b16 v[234:235], v250 offset:64\n\t" \
  "ds_read_b64_tr_b16 v[236:237], v250 offset:512\n\t" \
  "ds_read_b64_tr_b16 v[238:239], v250 offset:576\n\t" \
  "ds_read_b64_tr_b16 v[240:241], v250 offset:1024\n\t" \
  "ds_read_b64_tr_b16 v[242:243], v250 offset:1088\n\t" \
  "s_setprio 3\n\t" \
  "s_waitcnt lgkmcnt(0)\n\t" \
  "v_cmp_ne_u32 vcc, %[step], v212\n\t" \
  "s_and_b32 s93, vcc_lo, 0xf\n\t" \
  "s_barrier\n\t" \
  "s_cbranch_scc1 .Lre%=\n\t" \
  ".Lrb%=:\n\t" \
  "ds_read_b64_tr_b16 v[212:213], v250 offset:3584\n\t" \
  "ds_read_b64_tr_b16 v[214:215], v250 offset:3648\n\t" \
  "ds_read_b64_tr_b16 v[200:201], v250 offset:2048\n\t" \
  "ds_read_b64_tr_b16 v[202:203], v250 offset:2112\n\t" \
  "ds_read_b64_tr_b16 v[204:205], v250 offset:2560\n\t" \
  "ds_read_b64_tr_b16 v[206:207], v250 offset:2624\n\t" \
  "ds_read_b64_tr_b16 v[208:209], v250 offset:3072\n\t" \
  "ds_read_b64_tr_b16 v[210:211], v250 offset:3136\n\t" \
  "ds_read_b128 v[220:223], v248 offset:16384\n\t" \
  "ds_read_b128 v[224:227], v248 offset:17408\n\t" \
  "ds_read_b128 v[228:231], v248 offset:18432\n\t" \
  "s_waitcnt lgkmcnt(9)\n\t" \
  "v_mfma_f32_16x16x32_bf16 v[212:215], v[212:215], v[244:247], 0\n\t" \
  "s_waitcnt lgkmcnt(7)\n\t" \
  "v_mfma_f32_16x16x32_bf16 v[212:215], v[200:203], v[232:235], v[212:215]\n\t" \
  "s_waitcnt lgkmcnt(5)\n\t" \
  "v_mfma_f32_16x16x32_bf16 v[212:215], v[204:207], v[236:239], v[212:215]\n\t" \
  "s_waitcnt lgkmcnt(3)\n\t" \
  "v_mfma_f32_16x16x32_bf16 v[212:215], v[208:211], v[240:243], v[212:215]\n\t" \
  "s_waitcnt lgkmcnt(2)\n\t" \
  "v_pk_add_f32 v[216:217], v[216:217], v[220:221]\n\t" \
  "v_pk_add_f32 v[218:219], v[218:219], v[222:223]\n\t" \
  "s_waitcnt lgkmcnt(0)\n\t" \
  "v_pk_add_f32 v[224:225], v[224:225], v[228:229]\n\t" \
  "v_pk_add_f32 v[226:227], v[226:227], v[230:231]\n\t" \
  "v_pk_add_f32 v[216:217], v[216:217], v[224:225]\n\t" \
  "v_pk_add_f32 v[218:219], v[218:219], v[226:227]\n\t" \
  "v_max3_f32 v224, v216, v217, v218\n\t" \
  "v_max_f32 v224, v224, v219\n\t" \
  "s_nop 1\n\t" \
  "v_max_f32_dpp v225, v224, v224 row_ror:8 row_mask:0xf bank_mask:0xf\n\t" \
  "v_max_f32_dpp v226, v224, v224 row_ror:8 row_mask:0xf bank_mask:0xf\n\t" \
  "s_nop 1\n\t" \
  "v_permlane32_swap_b32 v225, v226\n\t" \
  "v_max_f32 v231, v225, v226\n\t" \
  "v_mov_b32 v220, 0x3f803f80\n\t" \
  "v_mov_b32 v221, 0x3f803f80\n\t" \
  "v_mov_b64 v[222:223], v[220:221]\n\t" \
  "v_mov_b32 v245, 0\n\t" \
  "v_mov_b32 v246, 0\n\t" \
  "v_max3_f32 v200, v212, v213, v231\n\t" \
  "v_max3_f32 v201, v200, v214, v215\n\t" \
  "v_max3_f32 v202, v200, v214, v215\n\t" \
  "v_cndmask_b32 v212, v212, v214, %[up]\n\t" \
  "v_cndmask_b32 v213, v213, v215, %[up]\n\t" \
  "v_permlane16_swap_b32 v201, v202\n\t" \
  "v_max_f32 v200, v201, v202\n\t" \
  "v_mul_f32 v200, %[negc], v200\n\t" \
  "v_fma_f32 v212, v212, %[c], v200\n\t" \
  "v_fma_f32 v213, v213, %[c], v200\n\t" \
  "v_exp_f32 v212, v212\n\t" \
  "v_fma_f32 v216, v216, %[c], v200\n\t" \
  "v_exp_f32 v213, v213\n\t" \
  "v_fma_f32 v217, v217, %[c], v200\n\t" \
  "v_exp_f32 v216, v216\n\t" \
  "v_fma_f32 v218, v218, %[c], v200\n\t" \
  "v_cvt_pk_bf16_f32 v244, v212, v213\n\t" \
  "v_cvt_pk_bf16_f32 v247, v212, v213\n\t" \
  "v_exp_f32 v217, v217\n\t" \
  "v_fma_f32 v219, v219, %[c], v200\n\t" \
  "v_mfma_f32_16x16x16_bf16 v[208:211], v[220:221], v[244:245], 0\n\t" \
  "v_exp_f32 v218, v218\n\t" \
  "s_waitcnt vmcnt(0)\n\t" \
  "v_mfma_f32_16x16x16_bf16 v[224:227], v[252:253], v[244:245], 0\n\t" \
  "v_cvt_pk_bf16_f32 v206, v216, v217\n\t" \
  "v_cvt_pk_bf16_f32 v204, v216, v217\n\t" \
  "v_exp_f32 v219, v219\n\t" \
  "v_mfma_f32_16x16x16_bf16 v[228:231], v[252:253], v[246:247], 0\n\t" \
  "v_cvt_pk_bf16_f32 v207, v218, v219\n\t" \
  "v_cvt_pk_bf16_f32 v205, v218, v219\n\t" \
  "v_mov_b32_dpp v204, v206 row_ror:8 row_mask:0xf bank_mask:0xc\n\t" \
  "v_mov_b32_dpp v206, v206 row_ror:8 row_mask:0xf bank_mask:0x3\n\t" \
  "v_mov_b32_dpp v205, v207 row_ror:8 row_mask:0xf bank_mask:0xc\n\t" \
  "v_mov_b32_dpp v207, v207 row_ror:8 row_mask:0xf bank_mask:0x3\n\t" \
  "v_max_u32 v200, v252, v253\n\t" \
  "v_cmp_le_u32 vcc, %[sent], v200\n\t" \
  "v_mfma_f32_16x16x32_bf16 v[208:211], v[220:223], v[204:207], v[208:211]\n\t" \
  ".Lpv%=:\n\t" \
  "v_mfma_f32_16x16x32_bf16 v[224:227], v[192:195], v[204:207], v[224:227]\n\t" \
  "v_mfma_f32_16x16x32_bf16 v[228:231], v[196:199], v[204:207], v[228:231]\n\t" \
  "s_cbranch_vccnz .Lvs%=\n\t" \
  ".Lvok%=:\n\t" \
  "s_nop 4\n\t" \
  "v_rcp_f32 v200, v208\n\t" \
  "v_cndmask_b32 v224, v224, v226, %[nh]\n\t" \
  "v_cndmask_b32 v225, v225, v227, %[nh]\n\t" \
  "v_cndmask_b32 v228, v228, v230, %[nh]\n\t" \
  "v_cndmask_b32 v229, v229, v231, %[nh]\n\t" \
  "v_pk_mul_f32 v[224:225], v[224:225], v[200:201] op_sel_hi:[1,0]\n\t" \
  "v_pk_mul_f32 v[228:229], v[228:229], v[200:201] op_sel_hi:[1,0]\n\t" \
  "v_cvt_pk_bf16_f32 v224, v224, v225\n\t" \
  "v_cvt_pk_bf16_f32 v225, v228, v229\n\t" \
  "buffer_store_dwordx2 v[224:225], v251, %[rs], %[so] offen sc1\n\t" \
  "s_memtime s[90:91]\n\t" \
  "s_setprio 0\n\t" \
  "s_waitcnt lgkmcnt(0)\n\t" \
  "s_mov_b32 %[tout], s90\n\t" \
  "s_mov_b32 %[spins], s92\n\t" \
  "s_branch .Lend%=\n\t" \
  ".Lls%=:\n\t" \
  "s_waitcnt lgkmcnt(0)\n\t" \
  "s_barrier\n\t" \
  "s_mov_b32 %[spins], 0\n\t" \
  "s_branch .Lend%=\n\t" \
  ".Lre%=:\n\t" \
  "ds_read_b64_tr_b16 v[232:233], v250 offset:0\n\t" \
  "ds_read_b64_tr_b16 v[234:235], v250 offset:64\n\t" \
  "ds_read_b64_tr_b16 v[236:237], v250 offset:512\n\t" \
  "ds_read_b64_tr_b16 v[238:239], v250 offset:576\n\t" \
  "ds_read_b64_tr_b16 v[240:241], v250 offset:1024\n\t" \
  "ds_read_b64_tr_b16 v[242:243], v250 offset:1088\n\t" \
  "s_waitcnt lgkmcnt(0)\n\t" \
  "s_branch .Lrb%=\n\t" \
  ".Lvs%=:\n\t" \
  "v_cmp_eq_u32 vcc, %[sent], v252\n\t" \
  "v_cmp_eq_u32 s[94:95], %[sent], v253\n\t" \
  "s_or_b64 s[94:95], s[94:95], vcc\n\t" \
  "s_cbranch_scc0 .Lvok%=\n\t" \
  "s_add_u32 s92, s92, 1\n\t" \
  "s_sleep 2\n\t" \
  "s_and_b32 s93, s92, 63\n\t" \
  "s_cbranch_scc1 .Lvr%=\n\t" \
  "buffer_load_dword v200, off, %[rs], %[sab] sc1\n\t" \
  "s_waitcnt vmcnt(0)\n\t" \
  "v_readfirstlane_b32 s93, v200\n\t" \
  "s_nop 3\n\t" \
  "s_cmp_eq_u32 s93, 1\n\t" \
  "s_cbranch_scc1 .Lvok%=\n\t" \
  "s_cmp_lt_u32 s92, 0x80000\n\t" \
  "s_cbranch_scc1 .Lvr%=\n\t" \
  "v_mov_b32 v200, 1\n\t" \
  "buffer_store_dword v200, off, %[rs], %[sab] sc1\n\t" \
  "s_branch .Lvok%=\n\t" \
  ".Lvr%=:\n\t" \
  "buffer_load_dword v252, v249, %[rs], %[sv] offen offset:0 sc1\n\t" \
  "buffer_load_dword v253, v249, %[rs], %[sv] offen offset:256 sc1\n\t" \
  "s_waitcnt vmcnt(0)\n\t" \
  "v_mfma_f32_16x16x16_bf16 v[224:227], v[252:253], v[244:245], 0\n\t" \
  "v_mfma_f32_16x16x16_bf16 v[228:231], v[252:253], v[246:247], 0\n\t" \
  "v_max_u32 v200, v252, v253\n\t" \
  "v_cmp_le_u32 vcc, %[sent], v200\n\t" \
  "s_nop 4\n\t" \
  "s_branch .Lpv%=\n\t" \
  "s_branch .Lend%=\n\t" \
  ".Lslow%=:\n\t" \
  "s_mov_b64 s[94:95], 0\n\t" \
  "v_cmp_eq_u32 vcc, %[sent], v200\n\t" \
  "s_or_b64 s[94:95], s[94:95], vcc\n\t" \
  "v_cmp_eq_u32 vcc, %[sent], v201\n\t" \
  "s_or_b64 s[94:95], s[94:95], vcc\n\t" \
  "v_cmp_eq_u32 vcc, %[sent], v202\n\t" \
  "s_or_b64 s[94:95], s[94:95], vcc\n\t" \
  "v_cmp_eq_u32 vcc, %[sent], v203\n\t" \
  "s_or_b64 s[94:95], s[94:95], vcc\n\t" \
  "v_cmp_eq_u32 vcc, %[sent], v204\n\t" \
  "s_or_b64 s[94:95], s[94:95], vcc\n\t" \
  "v_cmp_eq_u32 vcc, %[sent], v205\n\t" \
  "s_or_b64 s[94:95], s[94:95], vcc\n\t" \
  "v_cmp_eq_u32 vcc, %[sent], v206\n\t" \
  "s_or_b64 s[94:95], s[94:95], vcc\n\t" \
  "v_cmp_eq_u32 vcc, %[sent], v207\n\t" \
  "s_or_b64 s[94:95], s[94:95], vcc\n\t" \
  "s_cmp_eq_u64 s[94:95], 0\n\t" \
  "s_cbranch_scc1 .Lok%=\n\t" \
  "v_mov_b64 v[192:193], 0\n\t" \
  "v_mov_b64 v[194:195], 0\n\t" \
  "v_mov_b64 v[196:197], 0\n\t" \
  "v_mov_b64 v[198:199], 0\n\t" \
  "s_add_u32 s92, s92, 1\n\t" \
  "s_cmp_eq_u32 s92, 1\n\t" \
  "s_cbranch_scc1 .Lpoll%=\n\t" \
  "s_sleep 2\n\t" \
  "s_and_b32 s93, s92, 63\n\t" \
  "s_cmp_lg_u32 s93, 0\n\t" \
  "s_cbranch_scc1 .Lpoll%=\n\t" \
  "v_mov_b32 v245, 0\n\t" \
  "buffer_load_dword v244, v245, %[rs], %[sab] offen sc1\n\t" \
  "s_waitcnt vmcnt(0)\n\t" \
  "v_readfirstlane_b32 s93, v244\n\t" \
  "s_nop 3\n\t" \
  "s_cmp_eq_u32 s93, 1\n\t" \
  "s_cbranch_scc1 .Lok%=\n\t" \
  "s_cmp_lt_u32 s92, 0x80000\n\t" \
  "s_cbranch_scc1 .Lpoll%=\n\t" \
  "v_mov_b32 v244, 1\n\t" \
  "buffer_store_dword v244, v245, %[rs], %[sab] offen sc1\n\t" \
  "s_branch .Lok%=\n\t" \
  ".Lend%=:\n\t"
#define QK_ASM_4 \
  "s_waitcnt lgkmcnt(0)\n\t" \
  "v_subrev_u32 v244, 0x8000, v250\n\t" \
  "ds_read_b128 v[216:219], v248 offset:49152\n\t" \
  "ds_read_b128 v[220:223], v248 offset:50176\n\t" \
  "ds_read_b128 v[224:227], v248 offset:51200\n\t" \
  "ds_read_b128 v[228:231], v248 offset:52224\n\t" \
  "ds_read_b128 v[208:211], v244 offset:0\n\t" \
  "ds_read_b128 v[212:215], v244 offset:1024\n\t" \
  "ds_read_b128 v[236:239], v244 offset:2048\n\t" \
  "ds_read_b128 v[240:243], v244 offset:3072\n\t" \
  "v_mov_b64 v[192:193], 0\n\t" \
  "v_mov_b64 v[194:195], 0\n\t" \
  "v_mov_b64 v[196:197], 0\n\t" \
  "v_mov_b64 v[198:199], 0\n\t" \
  ".p2align 5\n\t" \
  "s_nop 0\n\t" \
  "s_nop 0\n\t" \
  "s_nop 0\n\t" \
  "s_nop 0\n\t" \
  "s_movk_i32 s92, 0x2000\n\t" \
  ".Lwt%=:\n\t" \
  "ds_read_b64 v[200:201], v248 offset:30720\n\t" \
  "s_memrealtime s[90:91]\n\t" \
  "s_sub_u32 s92, s92, 1\n\t" \
  "s_waitcnt lgkmcnt(0)\n\t" \
  "v_readfirstlane_b32 s93, v200\n\t" \
  "v_readfirstlane_b32 %[tgt], v201\n\t" \
  "s_cbranch_scc1 .Lgo%=\n\t" \
  "s_cmp_eq_u32 s93, %[step]\n\t" \
  "s_cbranch_scc1 .Lwx%=\n\t" \
  "s_sleep 1\n\t" \
  "s_branch .Lwt%=\n\t" \
  ".Lwx%=:\n\t" \
  "s_lshl_b32 s93, s90, 2\n\t" \
  "s_sub_i32 s93, %[tgt], s93\n\t" \
  "s_cmp_le_i32 s93, 0\n\t" \
  "s_cbranch_scc1 .Lgo%=\n\t" \
  "s_min_i32 s93, s93, 8000\n\t" \
  "s_mov_b32 s94, s93\n\t" \
  ".Lsl%=:\n\t" \
  "s_sub_i32 s94, s94, 18\n\t" \
  "s_cmp_lt_i32 s94, 30\n\t" \
  "s_cbranch_scc1 .Lsx%=\n\t" \
  "s_sleep 1\n\t" \
  "s_branch .Lsl%=\n\t" \
  ".Lsx%=:\n\t" \
  "s_sub_i32 s93, s93, 12\n\t" \
  "s_max_i32 s93, s93, 4\n\t" \
  "s_lshr_b32 s93, s93, 2\n\t" \
  "s_add_u32 s93, s93, s90\n\t" \
  "s_memrealtime s[90:91]\n\t" \
  ".Lpace%=:\n\t" \
  "s_memrealtime s[94:95]\n\t" \
  "s_waitcnt lgkmcnt(1)\n\t" \
  "s_sub_i32 s92, s90, s93\n\t" \
  "s_cmp_ge_i32 s92, 0\n\t" \
  "s_cbranch_scc1 .Le%=\n\t" \
  "s_memrealtime s[90:91]\n\t" \
  "s_waitcnt lgkmcnt(1)\n\t" \
  "s_sub_i32 s92, s94, s93\n\t" \
  "s_cmp_lt_i32 s92, 0\n\t" \
  "s_cbranch_scc1 .Lpace%=\n\t" \
  "s_nop 4\n\t" \
  ".Le%=:\n\t" \
  "s_add_u32 s92, s92, s93\n\t" \
  "s_lshl_b32 s92, s92, 2\n\t" \
  "s_sub_i32 s92, %[tgt], s92\n\t" \
  "s_max_i32 s92, s92, 0\n\t" \
  "s_bitcmp0_b32 s92, 3\n\t" \
  "s_cbranch_scc1 .Lf3%=\n\t" \
  "s_nop 15\n\t" \
  ".Lf3%=:\n\t" \
  "s_bitcmp0_b32 s92, 2\n\t" \
  "s_cbranch_scc1 .Lf2%=\n\t" \
  "s_nop 10\n\t" \
  ".Lf2%=:\n\t" \
  ".Lgo%=:\n\t" \
  "s_mov_b32 s92, 0\n\t" \
  ".Lpoll%=:\n\t" \
  "buffer_load_dwordx4 v[200:203], v248, %[rs], %[som] offen sc1\n\t" \
  "buffer_load_dwordx4 v[204:207], v248, %[rs], %[som2] offen sc1\n\t" \
  ".p2align 5\n\t" \
  "s_nop 0\n\t" \
  "s_nop 0\n\t" \
  "s_waitcnt vmcnt(1) lgkmcnt(0)\n\t" \
  "ds_write_b128 v249, v[200:203]\n\t" \
  "v_smfmac_f32_16x16x64_bf16 v[192:195], v[200:203], v[208:215], v251\n\t" \
  "v_max3_u32 v244, v200, v201, v202\n\t" \
  "v_smfmac_f32_16x16x64_bf16 v[196:199], v[200:203], v[236:243], v251\n\t" \
  "s_waitcnt vmcnt(0)\n\t" \
  "ds_write_b128 v249, v[204:207] offset:8192\n\t" \
  "v_smfmac_f32_16x16x64_bf16 v[192:195], v[204:207], v[216:223], v251\n\t" \
  "v_max3_u32 v244, v244, v203, v204\n\t" \
  "v_max3_u32 v244, v244, v205, v206\n\t" \
  "v_smfmac_f32_16x16x64_bf16 v[196:199], v[204:207], v[224:231], v251\n\t" \
  "v_max_u32 v244, v244, v207\n\t" \
  "v_cmp_le_u32 vcc, %[sent], v244\n\t" \
  "s_cbranch_vccnz .Lslow%=\n\t" \
  ".Lok%=:\n\t" \
  "s_waitcnt lgkmcnt(0)\n\t" \
  "s_barrier\n\t" \
  "ds_read_b128 v[208:211], v248 offset:6144\n\t" \
  "ds_read_b128 v[212:215], v248 offset:7168\n\t" \
  "ds_read_b128 v[236:239], v248 offset:8192\n\t" \
  "ds_read_b128 v[240:243], v248 offset:9216\n\t" \
  "ds_read_b128 v[244:247], v248 offset:10240\n\t" \
  "s_waitcnt lgkmcnt(4)\n\t" \
  "ds_read_b128 v[200:203], v248 offset:5120\n\t" \
  "v_smfmac_f32_16x16x64_bf16 v[192:195], v[208:211], v[0:7], v251\n\t" \
  "ds_read_b128 v[216:219], v250 offset:32768\n\t" \
  "v_smfmac_f32_16x16x64_bf16 v[196:199], v[208:211], v[8:15], v251\n\t" \
  "s_waitcnt lgkmcnt(5)\n\t" \
  "ds_read_b128 v[220:223], v250 offset:33792\n\t" \
  "v_smfmac_f32_16x16x64_bf16 v[192:195], v[212:215], v[16:23], v251\n\t" \
  "ds_read_b128 v[224:227], v250 offset:34816\n\t" \
  "v_smfmac_f32_16x16x64_bf16 v[196:199], v[212:215], v[24:31], v251\n\t" \
  "s_waitcnt lgkmcnt(6)\n\t" \
  "ds_read_b128 v[228:231], v250 offset:35840\n\t" \
  "v_smfmac_f32_16x16x64_bf16 v[192:195], v[236:239], v[32:39], v251\n\t" \
  "ds_read_b128 v[204:207], v248 offset:11264\n\t" \
  "v_smfmac_f32_16x16x64_bf16 v[196:199], v[236:239], v[40:47], v251\n\t" \
  "s_waitcnt lgkmcnt(7)\n\t" \
  "ds_read_b128 v[208:211], v248 offset:14336\n\t" \
  "v_smfmac_f32_16x16x64_bf16 v[192:195], v[240:243], v[48:55], v251\n\t" \
  "ds_read_b128 v[212:215], v248 offset:15360\n\t" \
  "v_smfmac_f32_16x16x64_bf16 v[196:199], v[240:243], v[56:63], v251\n\t" \
  "s_waitcnt lgkmcnt(7)\n\t" \
  "v_smfmac_f32_16x16x64_bf16 v[192:195], v[244:247], v[64:71], v251\n\t" \
  "ds_read_b128 v[236:239], v248 offset:0\n\t" \
  "v_smfmac_f32_16x16x64_bf16 v[196:199], v[244:247], v[72:79], v251\n\t" \
  "s_waitcnt lgkmcnt(6)\n\t" \
  "v_smfmac_f32_16x16x64_bf16 v[192:195], v[200:203], v[216:223], v251\n\t" \
  "s_waitcnt lgkmcnt(4)\n\t" \
  "ds_read_b128 v[240:243], v248 offset:13312\n\t" \
  "v_smfmac_f32_16x16x64_bf16 v[196:199], v[200:203], v[224:231], v251\n\t" \
  "s_waitcnt lgkmcnt(4)\n\t" \
  "ds_read_b128 v[216:219], v250 offset:0\n\t" \
  "v_smfmac_f32_16x16x64_bf16 v[192:195], v[204:207], v[80:87], v251\n\t" \
  "ds_read_b128 v[220:223], v250 offset:1024\n\t" \
  "v_smfmac_f32_16x16x64_bf16 v[196:199], v[204:207], v[88:95], v251\n\t" \
  "s_waitcnt lgkmcnt(4)\n\t" \
  "ds_read_b128 v[224:227], v250 offset:2048\n\t" \
  "v_smfmac_f32_16x16x64_bf16 v[192:195], v[208:211], v[96:103], v251\n\t" \
  "ds_read_b128 v[228:231], v250 offset:3072\n\t" \
  "v_smfmac_f32_16x16x64_bf16 v[196:199], v[208:211], v[104:111], v251\n\t" \
  "ds_read_b128 v[244:247], v248 offset:1024\n\t" \
  "v_smfmac_f32_16x16x64_bf16 v[192:195], v[212:215], v[112:119], v251\n\t" \
  "ds_read_b128 v[200:203], v248 offset:2048\n\t" \
  "v_smfmac_f32_16x16x64_bf16 v[196:199], v[212:215], v[120:127], v251\n\t" \
  "s_waitcnt lgkmcnt(6)\n\t" \
  "v_smfmac_f32_16x16x64_bf16 v[192:195], v[236:239], v[128:135], v251\n\t" \
  "ds_read_b128 v[204:207], v248 offset:3072\n\t" \
  "v_smfmac_f32_16x16x64_bf16 v[196:199], v[236:239], v[136:143], v251\n\t" \
  "s_waitcnt lgkmcnt(5)\n\t" \
  "v_smfmac_f32_16x16x64_bf16 v[192:195], v[240:243], v[216:223], v251\n\t" \
  "s_waitcnt lgkmcnt(3)\n\t" \
  "v_smfmac_f32_16x16x64_bf16 v[196:199], v[240:243], v[224:231], v251\n\t" \
  "s_waitcnt lgkmcnt(2)\n\t" \
  "v_smfmac_f32_16x16x64_bf16 v[192:195], v[244:247], v[144:151], v251\n\t" \
  "v_smfmac_f32_16x16x64_bf16 v[196:199], v[244:247], v[152:159], v251\n\t" \
  "s_waitcnt lgkmcnt(1)\n\t" \
  "v_smfmac_f32_16x16x64_bf16 v[192:195], v[200:203], v[160:167], v251\n\t" \
  "v_smfmac_f32_16x16x64_bf16 v[196:199], v[200:203], v[168:175], v251\n\t" \
  "s_waitcnt lgkmcnt(0)\n\t" \
  "v_smfmac_f32_16x16x64_bf16 v[192:195], v[204:207], v[176:183], v251\n\t" \
  "v_smfmac_f32_16x16x64_bf16 v[196:199], v[204:207], v[184:191], v251\n\t" \
  "s_nop 6\n\t" \
  "v_pk_add_f32 v[192:193], v[192:193], v[194:195]\n\t" \
  "v_pk_add_f32 v[196:197], v[196:197], v[198:199]\n\t" \
  "v_cvt_pk_bf16_f32 v192, v192, v193\n\t" \
  "v_cvt_pk_bf16_f32 v196, v196, v197\n\t" \
  "ds_write2_b32 v252, v192, v196 offset1:64\n\t" \
  "s_waitcnt lgkmcnt(0)\n\t" \
  "s_barrier\n\t" \
  "buffer_store_dword v192, v235, %[rs], %[sk] offen sc1\n\t" \
  "buffer_store_dword v196, v235, %[rs], %[sk] offen offset:256 sc1\n\t" \
  "s_mov_b32 %[spins], s92\n\t" \
  "s_branch .Lend%=\n\t" \
  ".Lslow%=:\n\t" \
  "s_mov_b64 s[94:95], 0\n\t" \
  "v_cmp_eq_u32 vcc, %[sent], v200\n\t" \
  "s_or_b64 s[94:95], s[94:95], vcc\n\t" \
  "v_cmp_eq_u32 vcc, %[sent], v201\n\t" \
  "s_or_b64 s[94:95], s[94:95], vcc\n\t" \
  "v_cmp_eq_u32 vcc, %[sent], v202\n\t" \
  "s_or_b64 s[94:95], s[94:95], vcc\n\t" \
  "v_cmp_eq_u32 vcc, %[sent], v203\n\t" \
  "s_or_b64 s[94:95], s[94:95], vcc\n\t" \
  "v_cmp_eq_u32 vcc, %[sent], v204\n\t" \
  "s_or_b64 s[94:95], s[94:95], vcc\n\t" \
  "v_cmp_eq_u32 vcc, %[sent], v205\n\t" \
  "s_or_b64 s[94:95], s[94:95], vcc\n\t" \
  "v_cmp_eq_u32 vcc, %[sent], v206\n\t" \
  "s_or_b64 s[94:95], s[94:95], vcc\n\t" \
  "v_cmp_eq_u32 vcc, %[sent], v207\n\t" \
  "s_or_b64 s[94:95], s[94:95], vcc\n\t" \
  "s_cmp_eq_u64 s[94:95], 0\n\t" \
  "s_cbranch_scc1 .Lok%=\n\t" \
  "v_mov_b64 v[192:193], 0\n\t" \
  "v_mov_b64 v[194:195], 0\n\t" \
  "v_mov_b64 v[196:197], 0\n\t" \
  "v_mov_b64 v[198:199], 0\n\t" \
  "s_add_u32 s92, s92, 1\n\t" \
  "s_cmp_eq_u32 s92, 1\n\t" \
  "s_cbranch_scc1 .Lpoll%=\n\t" \
  "s_sleep 2\n\t" \
  "s_and_b32 s93, s92, 63\n\t" \
  "s_cmp_lg_u32 s93, 0\n\t" \
  "s_cbranch_scc1 .Lpoll%=\n\t" \
  "v_mov_b32 v245, 0\n\t" \
  "buffer_load_dword v244, v245, %[rs], %[sab] offen sc1\n\t" \
  "s_waitcnt vmcnt(0)\n\t" \
  "v_readfirstlane_b32 s93, v244\n\t" \
  "s_nop 3\n\t" \
  "s_cmp_eq_u32 s93, 1\n\t" \
  "s_cbranch_scc1 .Lok%=\n\t" \
  "s_cmp_lt_u32 s92, 0x80000\n\t" \
  "s_cbranch_scc1 .Lpoll%=\n\t" \
  "v_mov_b32 v244, 1\n\t" \
  "buffer_store_dword v244, v245, %[rs], %[sab] offen sc1\n\t" \
  "s_branch .Lok%=\n\t" \
  ".Lend%=:\n\t"
#define QK_ASM_5 \
  "s_waitcnt lgkmcnt(0)\n\t" \
  "v_subrev_u32 v244, 0x8000, v250\n\t" \
  "ds_read_b128 v[216:219], v248 offset:53248\n\t" \
  "ds_read_b128 v[220:223], v248 offset:54272\n\t" \
  "ds_read_b128 v[224:227], v248 offset:55296\n\t" \
  "ds_read_b128 v[228:231], v248 offset:56320\n\t" \
  "ds_read_b128 v[208:211], v244 offset:0\n\t" \
  "ds_read_b128 v[212:215], v244 offset:1024\n\t" \
  "ds_read_b128 v[236:239], v244 offset:2048\n\t" \
  "ds_read_b128 v[240:243], v244 offset:3072\n\t" \
  "v_mov_b64 v[192:193], 0\n\t" \
  "v_mov_b64 v[194:195], 0\n\t" \
  "v_mov_b64 v[196:197], 0\n\t" \
  "v_mov_b64 v[198:199], 0\n\t" \
  ".p2align 5\n\t" \
  "s_nop 0\n\t" \
  "s_nop 0\n\t" \
  "s_nop 0\n\t" \
  "s_nop 0\n\t" \
  "s_movk_i32 s92, 0x2000\n\t" \
  ".Lwt%=:\n\t" \
  "ds_read_b64 v[200:201], v248 offset:30720\n\t" \
  "s_memrealtime s[90:91]\n\t" \
  "s_sub_u32 s92, s92, 1\n\t" \
  "s_waitcnt lgkmcnt(0)\n\t" \
  "v_readfirstlane_b32 s93, v200\n\t" \
  "v_readfirstlane_b32 %[tgt], v201\n\t" \
  "s_cbranch_scc1 .Lgo%=\n\t" \
  "s_cmp_eq_u32 s93, %[step]\n\t" \
  "s_cbranch_scc1 .Lwx%=\n\t" \
  "s_sleep 1\n\t" \
  "s_branch .Lwt%=\n\t" \
  ".Lwx%=:\n\t" \
  "s_lshl_b32 s93, s90, 2\n\t" \
  "s_sub_i32 s93, %[tgt], s93\n\t" \
  "s_cmp_le_i32 s93, 0\n\t" \
  "s_cbranch_scc1 .Lgo%=\n\t" \
  "s_min_i32 s93, s93, 8000\n\t" \
  "s_mov_b32 s94, s93\n\t" \
  ".Lsl%=:\n\t" \
  "s_sub_i32 s94, s94, 18\n\t" \
  "s_cmp_lt_i32 s94, 30\n\t" \
  "s_cbranch_scc1 .Lsx%=\n\t" \
  "s_sleep 1\n\t" \
  "s_branch .Lsl%=\n\t" \
  ".Lsx%=:\n\t" \
  "s_sub_i32 s93, s93, 12\n\t" \
  "s_max_i32 s93, s93, 4\n\t" \
  "s_lshr_b32 s93, s93, 2\n\t" \
  "s_add_u32 s93, s93, s90\n\t" \
  "s_memrealtime s[90:91]\n\t" \
  ".Lpace%=:\n\t" \
  "s_memrealtime s[94:95]\n\t" \
  "s_waitcnt lgkmcnt(1)\n\t" \
  "s_sub_i32 s92, s90, s93\n\t" \
  "s_cmp_ge_i32 s92, 0\n\t" \
  "s_cbranch_scc1 .Le%=\n\t" \
  "s_memrealtime s[90:91]\n\t" \
  "s_waitcnt lgkmcnt(1)\n\t" \
  "s_sub_i32 s92, s94, s93\n\t" \
  "s_cmp_lt_i32 s92, 0\n\t" \
  "s_cbranch_scc1 .Lpace%=\n\t" \
  "s_nop 4\n\t" \
  ".Le%=:\n\t" \
  "s_add_u32 s92, s92, s93\n\t" \
  "s_lshl_b32 s92, s92, 2\n\t" \
  "s_sub_i32 s92, %[tgt], s92\n\t" \
  "s_max_i32 s92, s92, 0\n\t" \
  "s_bitcmp0_b32 s92, 3\n\t" \
  "s_cbranch_scc1 .Lf3%=\n\t" \
  "s_nop 15\n\t" \
  ".Lf3%=:\n\t" \
  "s_bitcmp0_b32 s92, 2\n\t" \
  "s_cbranch_scc1 .Lf2%=\n\t" \
  "s_nop 10\n\t" \
  ".Lf2%=:\n\t" \
  ".Lgo%=:\n\t" \
  "s_mov_b32 s92, 0\n\t" \
  ".Lpoll%=:\n\t" \
  "buffer_load_dwordx4 v[200:203], v248, %[rs], %[som] offen sc1\n\t" \
  "buffer_load_dwordx4 v[204:207], v248, %[rs], %[som2] offen sc1\n\t" \
  ".p2align 5\n\t" \
  "s_nop 0\n\t" \
  "s_nop 0\n\t" \
  "s_waitcnt vmcnt(1) lgkmcnt(0)\n\t" \
  "ds_write_b128 v249, v[200:203]\n\t" \
  "v_smfmac_f32_16x16x64_bf16 v[192:195], v[200:203], v[208:215], v251\n\t" \
  "v_max3_u32 v244, v200, v201, v202\n\t" \
  "v_smfmac_f32_16x16x64_bf16 v[196:199], v[200:203], v[236:243], v251\n\t" \
  "s_waitcnt vmcnt(0)\n\t" \
  "ds_write_b128 v249, v[204:207] offset:8192\n\t" \
  "v_smfmac_f32_16x16x64_bf16 v[192:195], v[204:207], v[216:223], v251\n\t" \
  "v_max3_u32 v244, v244, v203, v204\n\t" \
  "v_max3_u32 v244, v244, v205, v206\n\t" \
  "v_smfmac_f32_16x16x64_bf16 v[196:199], v[204:207], v[224:231], v251\n\t" \
  "v_max_u32 v244, v244, v207\n\t" \
  "v_cmp_le_u32 vcc, %[sent], v244\n\t" \
  "s_cbranch_vccnz .Lslow%=\n\t" \
  ".Lok%=:\n\t" \
  "s_waitcnt lgkmcnt(0)\n\t" \
  "s_barrier\n\t" \
  "ds_read_b128 v[208:211], v248 offset:7168\n\t" \
  "ds_read_b128 v[212:215], v248 offset:8192\n\t" \
  "ds_read_b128 v[236:239], v248 offset:9216\n\t" \
  "ds_read_b128 v[240:243], v248 offset:10240\n\t" \
  "ds_read_b128 v[244:247], v248 offset:11264\n\t" \
  "s_waitcnt lgkmcnt(4)\n\t" \
  "ds_read_b128 v[200:203], v248 offset:4096\n\t" \
  "v_smfmac_f32_16x16x64_bf16 v[192:195], v[208:211], v[0:7], v251\n\t" \
  "ds_read_b128 v[216:219], v250 offset:32768\n\t" \
  "v_smfmac_f32_16x16x64_bf16 v[196:199], v[208:211], v[8:15], v251\n\t" \
  "s_waitcnt lgkmcnt(5)\n\t" \
  "ds_read_b128 v[220:223], v250 offset:33792\n\t" \
  "v_smfmac_f32_16x16x64_bf16 v[192:195], v[212:215], v[16:23], v251\n\t" \
  "ds_read_b128 v[224:227], v250 offset:34816\n\t" \
  "v_smfmac_f32_16x16x64_bf16 v[196:199], v[212:215], v[24:31], v251\n\t" \
  "s_waitcnt lgkmcnt(6)\n\t" \
  "ds_read_b128 v[228:231], v250 offset:35840\n\t" \
  "v_smfmac_f32_16x16x64_bf16 v[192:195], v[236:239], v[32:39], v251\n\t" \
  "ds_read_b128 v[204:207], v248 offset:14336\n\t" \
  "v_smfmac_f32_16x16x64_bf16 v[196:199], v[236:239], v[40:47], v251\n\t" \
  "s_waitcnt lgkmcnt(7)\n\t" \
  "ds_read_b128 v[208:211], v248 offset:15360\n\t" \
  "v_smfmac_f32_16x16x64_bf16 v[192:195], v[240:243], v[48:55], v251\n\t" \
  "ds_read_b128 v[212:215], v248 offset:0\n\t" \
  "v_smfmac_f32_16x16x64_bf16 v[196:199], v[240:243], v[56:63], v251\n\t" \
  "s_waitcnt lgkmcnt(7)\n\t" \
  "v_smfmac_f32_16x16x64_bf16 v[192:195], v[244:247], v[64:71], v251\n\t" \
  "ds_read_b128 v[236:239], v248 offset:1024\n\t" \
  "v_smfmac_f32_16x16x64_bf16 v[196:199], v[244:247], v[72:79], v251\n\t" \
  "s_waitcnt lgkmcnt(6)\n\t" \
  "v_smfmac_f32_16x16x64_bf16 v[192:195], v[200:203], v[216:223], v251\n\t" \
  "s_waitcnt lgkmcnt(4)\n\t" \
  "ds_read_b128 v[240:243], v248 offset:12288\n\t" \
  "v_smfmac_f32_16x16x64_bf16 v[196:199], v[200:203], v[224:231], v251\n\t" \
  "s_waitcnt lgkmcnt(4)\n\t" \
  "ds_read_b128 v[216:219], v250 offset:0\n\t" \
  "v_smfmac_f32_16x16x64_bf16 v[192:195], v[204:207], v[80:87], v251\n\t" \
  "ds_read_b128 v[220:223], v250 offset:1024\n\t" \
  "v_smfmac_f32_16x16x64_bf16 v[196:199], v[204:207], v[88:95], v251\n\t" \
  "s_waitcnt lgkmcnt(4)\n\t" \
  "ds_read_b128 v[224:227], v250 offset:2048\n\t" \
  "v_smfmac_f32_16x16x64_bf16 v[192:195], v[208:211], v[96:103], v251\n\t" \
  "ds_read_b128 v[228:231], v250 offset:3072\n\t" \
  "v_smfmac_f32_16x16x64_bf16 v[196:199], v[208:211], v[104:111], v251\n\t" \
  "ds_read_b128 v[244:247], v248 offset:2048\n\t" \
  "v_smfmac_f32_16x16x64_bf16 v[192:195], v[212:215], v[112:119], v251\n\t" \
  "ds_read_b128 v[200:203], v248 offset:3072\n\t" \
  "v_smfmac_f32_16x16x64_bf16 v[196:199], v[212:215], v[120:127], v251\n\t" \
  "s_waitcnt lgkmcnt(6)\n\t" \
  "v_smfmac_f32_16x16x64_bf16 v[192:195], v[236:239], v[128:135], v251\n\t" \
  "ds_read_b128 v[204:207], v248 offset:6144\n\t" \
  "v_smfmac_f32_16x16x64_bf16 v[196:199], v[236:239], v[136:143], v251\n\t" \
  "s_waitcnt lgkmcnt(5)\n\t" \
  "v_smfmac_f32_16x16x64_bf16 v[192:195], v[240:243], v[216:223], v251\n\t" \
  "s_waitcnt lgkmcnt(3)\n\t" \
  "v_smfmac_f32_16x16x64_bf16 v[196:199], v[240:243], v[224:231], v251\n\t" \
  "s_waitcnt lgkmcnt(2)\n\t" \
  "v_smfmac_f32_16x16x64_bf16 v[192:195], v[244:247], v[144:151], v251\n\t" \
  "v_smfmac_f32_16x16x64_bf16 v[196:199], v[244:247], v[152:159], v251\n\t" \
  "s_waitcnt lgkmcnt(1)\n\t" \
  "v_smfmac_f32_16x16x64_bf16 v[192:195], v[200:203], v[160:167], v251\n\t" \
  "v_smfmac_f32_16x16x64_bf16 v[196:199], v[200:203], v[168:175], v251\n\t" \
  "s_waitcnt lgkmcnt(0)\n\t" \
  "v_smfmac_f32_16x16x64_bf16 v[192:195], v[204:207], v[176:183], v251\n\t" \
  "v_smfmac_f32_16x16x64_bf16 v[196:199], v[204:207], v[184:191], v251\n\t" \
  "s_nop 6\n\t" \
  "v_pk_add_f32 v[192:193], v[192:193], v[194:195]\n\t" \
  "v_pk_add_f32 v[196:197], v[196:197], v[198:199]\n\t" \
  "v_cvt_pk_bf16_f32 v192, v192, v193\n\t" \
  "v_cvt_pk_bf16_f32 v196, v196, v197\n\t" \
  "ds_write2_b32 v252, v192, v196 offset1:64\n\t" \
  "s_waitcnt lgkmcnt(0)\n\t" \
  "s_barrier\n\t" \
  "buffer_store_dword v192, v235, %[rs], %[sk] offen sc1\n\t" \
  "buffer_store_dword v196, v235, %[rs], %[sk] offen offset:256 sc1\n\t" \
  "s_mov_b32 %[spins], s92\n\t" \
  "s_branch .Lend%=\n\t" \
  ".Lslow%=:\n\t" \
  "s_mov_b64 s[94:95], 0\n\t" \
  "v_cmp_eq_u32 vcc, %[sent], v200\n\t" \
  "s_or_b64 s[94:95], s[94:95], vcc\n\t" \
  "v_cmp_eq_u32 vcc, %[sent], v201\n\t" \
  "s_or_b64 s[94:95], s[94:95], vcc\n\t" \
  "v_cmp_eq_u32 vcc, %[sent], v202\n\t" \
  "s_or_b64 s[94:95], s[94:95], vcc\n\t" \
  "v_cmp_eq_u32 vcc, %[sent], v203\n\t" \
  "s_or_b64 s[94:95], s[94:95], vcc\n\t" \
  "v_cmp_eq_u32 vcc, %[sent], v204\n\t" \
  "s_or_b64 s[94:95], s[94:95], vcc\n\t" \
  "v_cmp_eq_u32 vcc, %[sent], v205\n\t" \
  "s_or_b64 s[94:95], s[94:95], vcc\n\t" \
  "v_cmp_eq_u32 vcc, %[sent], v206\n\t" \
  "s_or_b64 s[94:95], s[94:95], vcc\n\t" \
  "v_cmp_eq_u32 vcc, %[sent], v207\n\t" \
  "s_or_b64 s[94:95], s[94:95], vcc\n\t" \
  "s_cmp_eq_u64 s[94:95], 0\n\t" \
  "s_cbranch_scc1 .Lok%=\n\t" \
  "v_mov_b64 v[192:193], 0\n\t" \
  "v_mov_b64 v[194:195], 0\n\t" \
  "v_mov_b64 v[196:197], 0\n\t" \
  "v_mov_b64 v[198:199], 0\n\t" \
  "s_add_u32 s92, s92, 1\n\t" \
  "s_cmp_eq_u32 s92, 1\n\t" \
  "s_cbranch_scc1 .Lpoll%=\n\t" \
  "s_sleep 2\n\t" \
  "s_and_b32 s93, s92, 63\n\t" \
  "s_cmp_lg_u32 s93, 0\n\t" \
  "s_cbranch_scc1 .Lpoll%=\n\t" \
  "v_mov_b32 v245, 0\n\t" \
  "buffer_load_dword v244, v245, %[rs], %[sab] offen sc1\n\t" \
  "s_waitcnt vmcnt(0)\n\t" \
  "v_readfirstlane_b32 s93, v244\n\t" \
  "s_nop 3\n\t" \
  "s_cmp_eq_u32 s93, 1\n\t" \
  "s_cbranch_scc1 .Lok%=\n\t" \
  "s_cmp_lt_u32 s92, 0x80000\n\t" \
  "s_cbranch_scc1 .Lpoll%=\n\t" \
  "v_mov_b32 v244, 1\n\t" \
  "buffer_store_dword v244, v245, %[rs], %[sab] offen sc1\n\t" \
  "s_branch .Lok%=\n\t" \
  ".Lend%=:\n\t"
#define QK_ASM_6 \
  "s_waitcnt lgkmcnt(0)\n\t" \
  "v_subrev_u32 v244, 0x8000, v250\n\t" \
  "ds_read_b128 v[216:219], v248 offset:57344\n\t" \
  "ds_read_b128 v[220:223], v248 offset:58368\n\t" \
  "ds_read_b128 v[224:227], v248 offset:59392\n\t" \
  "ds_read_b128 v[228:231], v248 offset:60416\n\t" \
  "ds_read_b128 v[208:211], v244 offset:0\n\t" \
  "ds_read_b128 v[212:215], v244 offset:1024\n\t" \
  "ds_read_b128 v[236:239], v244 offset:2048\n\t" \
  "ds_read_b128 v[240:243], v244 offset:3072\n\t" \
  "v_mov_b64 v[192:193], 0\n\t" \
  "v_mov_b64 v[194:195], 0\n\t" \
  "v_mov_b64 v[196:197], 0\n\t" \
  "v_mov_b64 v[198:199], 0\n\t" \
  ".p2align 5\n\t" \
  "s_nop 0\n\t" \
  "s_nop 0\n\t" \
  "s_nop 0\n\t" \
  "s_nop 0\n\t" \
  "s_movk_i32 s92, 0x2000\n\t" \
  ".Lwt%=:\n\t" \
  "ds_read_b64 v[200:201], v248 offset:30720\n\t" \
  "s_memrealtime s[90:91]\n\t" \
  "s_sub_u32 s92, s92, 1\n\t" \
  "s_waitcnt lgkmcnt(0)\n\t" \
  "v_readfirstlane_b32 s93, v200\n\t" \
  "v_readfirstlane_b32 %[tgt], v201\n\t" \
  "s_cbranch_scc1 .Lgo%=\n\t" \
  "s_cmp_eq_u32 s93, %[step]\n\t" \
  "s_cbranch_scc1 .Lwx%=\n\t" \
  "s_sleep 1\n\t" \
  "s_branch .Lwt%=\n\t" \
  ".Lwx%=:\n\t" \
  "s_lshl_b32 s93, s90, 2\n\t" \
  "s_sub_i32 s93, %[tgt], s93\n\t" \
  "s_cmp_le_i32 s93, 0\n\t" \
  "s_cbranch_scc1 .Lgo%=\n\t" \
  "s_min_i32 s93, s93, 8000\n\t" \
  "s_mov_b32 s94, s93\n\t" \
  ".Lsl%=:\n\t" \
  "s_sub_i32 s94, s94, 18\n\t" \
  "s_cmp_lt_i32 s94, 30\n\t" \
  "s_cbranch_scc1 .Lsx%=\n\t" \
  "s_sleep 1\n\t" \
  "s_branch .Lsl%=\n\t" \
  ".Lsx%=:\n\t" \
  "s_sub_i32 s93, s93, 12\n\t" \
  "s_max_i32 s93, s93, 4\n\t" \
  "s_lshr_b32 s93, s93, 2\n\t" \
  "s_add_u32 s93, s93, s90\n\t" \
  "s_memrealtime s[90:91]\n\t" \
  ".Lpace%=:\n\t" \
  "s_memrealtime s[94:95]\n\t" \
  "s_waitcnt lgkmcnt(1)\n\t" \
  "s_sub_i32 s92, s90, s93\n\t" \
  "s_cmp_ge_i32 s92, 0\n\t" \
  "s_cbranch_scc1 .Le%=\n\t" \
  "s_memrealtime s[90:91]\n\t" \
  "s_waitcnt lgkmcnt(1)\n\t" \
  "s_sub_i32 s92, s94, s93\n\t" \
  "s_cmp_lt_i32 s92, 0\n\t" \
  "s_cbranch_scc1 .Lpace%=\n\t" \
  "s_nop 4\n\t" \
  ".Le%=:\n\t" \
  "s_add_u32 s92, s92, s93\n\t" \
  "s_lshl_b32 s92, s92, 2\n\t" \
  "s_sub_i32 s92, %[tgt], s92\n\t" \
  "s_max_i32 s92, s92, 0\n\t" \
  "s_bitcmp0_b32 s92, 3\n\t" \
  "s_cbranch_scc1 .Lf3%=\n\t" \
  "s_nop 15\n\t" \
  ".Lf3%=:\n\t" \
  "s_bitcmp0_b32 s92, 2\n\t" \
  "s_cbranch_scc1 .Lf2%=\n\t" \
  "s_nop 10\n\t" \
  ".Lf2%=:\n\t" \
  ".Lgo%=:\n\t" \
  "s_mov_b32 s92, 0\n\t" \
  ".Lpoll%=:\n\t" \
  "buffer_load_dwordx4 v[200:203], v248, %[rs], %[som] offen sc1\n\t" \
  "buffer_load_dwordx4 v[204:207], v248, %[rs], %[som2] offen sc1\n\t" \
  ".p2align 5\n\t" \
  "s_nop 0\n\t" \
  "s_nop 0\n\t" \
  "s_waitcnt vmcnt(1) lgkmcnt(0)\n\t" \
  "ds_write_b128 v249, v[200:203]\n\t" \
  "v_smfmac_f32_16x16x64_bf16 v[192:195], v[200:203], v[208:215], v251\n\t" \
  "v_max3_u32 v244, v200, v201, v202\n\t" \
  "v_smfmac_f32_16x16x64_bf16 v[196:199], v[200:203], v[236:243], v251\n\t" \
  "s_waitcnt vmcnt(0)\n\t" \
  "ds_write_b128 v249, v[204:207] offset:8192\n\t" \
  "v_smfmac_f32_16x16x64_bf16 v[192:195], v[204:207], v[216:223], v251\n\t" \
  "v_max3_u32 v244, v244, v203, v204\n\t" \
  "v_max3_u32 v244, v244, v205, v206\n\t" \
  "v_smfmac_f32_16x16x64_bf16 v[196:199], v[204:207], v[224:231], v251\n\t" \
  "v_max_u32 v244, v244, v207\n\t" \
  "v_cmp_le_u32 vcc, %[sent], v244\n\t" \
  "s_cbranch_vccnz .Lslow%=\n\t" \
  ".Lok%=:\n\t" \
  "s_waitcnt lgkmcnt(0)\n\t" \
  "s_barrier\n\t" \
  "ds_read_b128 v[208:211], v248 offset:8192\n\t" \
  "ds_read_b128 v[212:215], v248 offset:9216\n\t" \
  "ds_read_b128 v[236:239], v248 offset:10240\n\t" \
  "ds_read_b128 v[240:243], v248 offset:11264\n\t" \
  "ds_read_b128 v[244:247], v248 offset:12288\n\t" \
  "s_waitcnt lgkmcnt(4)\n\t" \
  "ds_read_b128 v[200:203], v248 offset:7168\n\t" \
  "v_smfmac_f32_16x16x64_bf16 v[192:195], v[208:211], v[0:7], v251\n\t" \
  "ds_read_b128 v[216:219], v250 offset:32768\n\t" \
  "v_smfmac_f32_16x16x64_bf16 v[196:199], v[208:211], v[8:15], v251\n\t" \
  "s_waitcnt lgkmcnt(5)\n\t" \
  "ds_read_b128 v[220:223], v250 offset:33792\n\t" \
  "v_smfmac_f32_16x16x64_bf16 v[192:195], v[212:215], v[16:23], v251\n\t" \
  "ds_read_b128 v[224:227], v250 offset:34816\n\t" \
  "v_smfmac_f32_16x16x64_bf16 v[196:199], v[212:215], v[24:31], v251\n\t" \
  "s_waitcnt lgkmcnt(6)\n\t" \
  "ds_read_b128 v[228:231], v250 offset:35840\n\t" \
  "v_smfmac_f32_16x16x64_bf16 v[192:195], v[236:239], v[32:39], v251\n\t" \
  "ds_read_b128 v[204:207], v248 offset:15360\n\t" \
  "v_smfmac_f32_16x16x64_bf16 v[196:199], v[236:239], v[40:47], v251\n\t" \
  "s_waitcnt lgkmcnt(7)\n\t" \
  "ds_read_b128 v[208:211], v248 offset:0\n\t" \
  "v_smfmac_f32_16x16x64_bf16 v[192:195], v[240:243], v[48:55], v251\n\t" \
  "ds_read_b128 v[212:215], v248 offset:1024\n\t" \
  "v_smfmac_f32_16x16x64_bf16 v[196:199], v[240:243], v[56:63], v251\n\t" \
  "s_waitcnt lgkmcnt(7)\n\t" \
  "v_smfmac_f32_16x16x64_bf16 v[192:195], v[244:247], v[64:71], v251\n\t" \
  "ds_read_b128 v[236:239], v248 offset:2048\n\t" \
  "v_smfmac_f32_16x16x64_bf16 v[196:199], v[244:247], v[72:79], v251\n\t" \
  "s_waitcnt lgkmcnt(6)\n\t" \
  "v_smfmac_f32_16x16x64_bf16 v[192:195], v[200:203], v[216:223], v251\n\t" \
  "s_waitcnt lgkmcnt(4)\n\t" \
  "ds_read_b128 v[240:243], v248 offset:13312\n\t" \
  "v_smfmac_f32_16x16x64_bf16 v[196:199], v[200:203], v[224:231], v251\n\t" \
  "s_waitcnt lgkmcnt(4)\n\t" \
  "ds_read_b128 v[216:219], v250 offset:0\n\t" \
  "v_smfmac_f32_16x16x64_bf16 v[192:195], v[204:207], v[80:87], v251\n\t" \
  "ds_read_b128 v[220:223], v250 offset:1024\n\t" \
  "v_smfmac_f32_16x16x64_bf16 v[196:199], v[204:207], v[88:95], v251\n\t" \
  "s_waitcnt lgkmcnt(4)\n\t" \
  "ds_read_b128 v[224:227], v250 offset:2048\n\t" \
  "v_smfmac_f32_16x16x64_bf16 v[192:195], v[208:211], v[96:103], v251\n\t" \
  "ds_read_b128 v[228:231], v250 offset:3072\n\t" \
  "v_smfmac_f32_16x16x64_bf16 v[196:199], v[208:211], v[104:111], v251\n\t" \
  "ds_read_b128 v[244:247], v248 offset:3072\n\t" \
  "v_smfmac_f32_16x16x64_bf16 v[192:195], v[212:215], v[112:119], v251\n\t" \
  "ds_read_b128 v[200:203], v248 offset:4096\n\t" \
  "v_smfmac_f32_16x16x64_bf16 v[196:199], v[212:215], v[120:127], v251\n\t" \
  "s_waitcnt lgkmcnt(6)\n\t" \
  "v_smfmac_f32_16x16x64_bf16 v[192:195], v[236:239], v[128:135], v251\n\t" \
  "ds_read_b128 v[204:207], v248 offset:5120\n\t" \
  "v_smfmac_f32_16x16x64_bf16 v[196:199], v[236:239], v[136:143], v251\n\t" \
  "s_waitcnt lgkmcnt(5)\n\t" \
  "v_smfmac_f32_16x16x64_bf16 v[192:195], v[240:243], v[216:223], v251\n\t" \
  "s_waitcnt lgkmcnt(3)\n\t" \
  "v_smfmac_f32_16x16x64_bf16 v[196:199], v[240:243], v[224:231], v251\n\t" \
  "s_waitcnt lgkmcnt(2)\n\t" \
  "v_smfmac_f32_16x16x64_bf16 v[192:195], v[244:247], v[144:151], v251\n\t" \
  "v_smfmac_f32_16x16x64_bf16 v[196:199], v[244:247], v[152:159], v251\n\t" \
  "s_waitcnt lgkmcnt(1)\n\t" \
  "v_smfmac_f32_16x16x64_bf16 v[192:195], v[200:203], v[160:167], v251\n\t" \
  "v_smfmac_f32_16x16x64_bf16 v[196:199], v[200:203], v[168:175], v251\n\t" \
  "s_waitcnt lgkmcnt(0)\n\t" \
  "v_smfmac_f32_16x16x64_bf16 v[192:195], v[204:207], v[176:183], v251\n\t" \
  "v_smfmac_f32_16x16x64_bf16 v[196:199], v[204:207], v[184:191], v251\n\t" \
  "s_nop 6\n\t" \
  "v_pk_add_f32 v[192:193], v[192:193], v[194:195]\n\t" \
  "v_pk_add_f32 v[196:197], v[196:197], v[198:199]\n\t" \
  "v_cvt_pk_bf16_f32 v192, v192, v193\n\t" \
  "v_cvt_pk_bf16_f32 v196, v196, v197\n\t" \
  "ds_write2_b32 v252, v192, v196 offset1:64\n\t" \
  "s_waitcnt lgkmcnt(0)\n\t" \
  "s_barrier\n\t" \
  "buffer_store_dword v192, v235, %[rs], %[sk] offen sc1\n\t" \
  "buffer_store_dword v196, v235, %[rs], %[sk] offen offset:256 sc1\n\t" \
  "s_mov_b32 %[spins], s92\n\t" \
  "s_branch .Lend%=\n\t" \
  ".Lslow%=:\n\t" \
  "s_mov_b64 s[94:95], 0\n\t" \
  "v_cmp_eq_u32 vcc, %[sent], v200\n\t" \
  "s_or_b64 s[94:95], s[94:95], vcc\n\t" \
  "v_cmp_eq_u32 vcc, %[sent], v201\n\t" \
  "s_or_b64 s[94:95], s[94:95], vcc\n\t" \
  "v_cmp_eq_u32 vcc, %[sent], v202\n\t" \
  "s_or_b64 s[94:95], s[94:95], vcc\n\t" \
  "v_cmp_eq_u32 vcc, %[sent], v203\n\t" \
  "s_or_b64 s[94:95], s[94:95], vcc\n\t" \
  "v_cmp_eq_u32 vcc, %[sent], v204\n\t" \
  "s_or_b64 s[94:95], s[94:95], vcc\n\t" \
  "v_cmp_eq_u32 vcc, %[sent], v205\n\t" \
  "s_or_b64 s[94:95], s[94:95], vcc\n\t" \
  "v_cmp_eq_u32 vcc, %[sent], v206\n\t" \
  "s_or_b64 s[94:95], s[94:95], vcc\n\t" \
  "v_cmp_eq_u32 vcc, %[sent], v207\n\t" \
  "s_or_b64 s[94:95], s[94:95], vcc\n\t" \
  "s_cmp_eq_u64 s[94:95], 0\n\t" \
  "s_cbranch_scc1 .Lok%=\n\t" \
  "v_mov_b64 v[192:193], 0\n\t" \
  "v_mov_b64 v[194:195], 0\n\t" \
  "v_mov_b64 v[196:197], 0\n\t" \
  "v_mov_b64 v[198:199], 0\n\t" \
  "s_add_u32 s92, s92, 1\n\t" \
  "s_cmp_eq_u32 s92, 1\n\t" \
  "s_cbranch_scc1 .Lpoll%=\n\t" \
  "s_sleep 2\n\t" \
  "s_and_b32 s93, s92, 63\n\t" \
  "s_cmp_lg_u32 s93, 0\n\t" \
  "s_cbranch_scc1 .Lpoll%=\n\t" \
  "v_mov_b32 v245, 0\n\t" \
  "buffer_load_dword v244, v245, %[rs], %[sab] offen sc1\n\t" \
  "s_waitcnt vmcnt(0)\n\t" \
  "v_readfirstlane_b32 s93, v244\n\t" \
  "s_nop 3\n\t" \
  "s_cmp_eq_u32 s93, 1\n\t" \
  "s_cbranch_scc1 .Lok%=\n\t" \
  "s_cmp_lt_u32 s92, 0x80000\n\t" \
  "s_cbranch_scc1 .Lpoll%=\n\t" \
  "v_mov_b32 v244, 1\n\t" \
  "buffer_store_dword v244, v245, %[rs], %[sab] offen sc1\n\t" \
  "s_branch .Lok%=\n\t" \
  ".Lend%=:\n\t"
#define QK_ASM_7 \
  "s_waitcnt lgkmcnt(0)\n\t" \
  "v_subrev_u32 v244, 0x8000, v250\n\t" \
  "ds_read_b128 v[216:219], v248 offset:61440\n\t" \
  "ds_read_b128 v[220:223], v248 offset:62464\n\t" \
  "ds_read_b128 v[224:227], v248 offset:63488\n\t" \
  "ds_read_b128 v[228:231], v248 offset:64512\n\t" \
  "ds_read_b128 v[208:211], v244 offset:0\n\t" \
  "ds_read_b128 v[212:215], v244 offset:1024\n\t" \
  "ds_read_b128 v[236:239], v244 offset:2048\n\t" \
  "ds_read_b128 v[240:243], v244 offset:3072\n\t" \
  "v_mov_b64 v[192:193], 0\n\t" \
  "v_mov_b64 v[194:195], 0\n\t" \
  "v_mov_b64 v[196:197], 0\n\t" \
  "v_mov_b64 v[198:199], 0\n\t" \
  ".p2align 5\n\t" \
  "s_nop 0\n\t" \
  "s_nop 0\n\t" \
  "s_nop 0\n\t" \
  "s_nop 0\n\t" \
  "s_movk_i32 s92, 0x2000\n\t" \
  ".Lwt%=:\n\t" \
  "ds_read_b64 v[200:201], v248 offset:30720\n\t" \
  "s_memrealtime s[90:91]\n\t" \
  "s_sub_u32 s92, s92, 1\n\t" \
  "s_waitcnt lgkmcnt(0)\n\t" \
  "v_readfirstlane_b32 s93, v200\n\t" \
  "v_readfirstlane_b32 %[tgt], v201\n\t" \
  "s_cbranch_scc1 .Lgo%=\n\t" \
  "s_cmp_eq_u32 s93, %[step]\n\t" \
  "s_cbranch_scc1 .Lwx%=\n\t" \
  "s_sleep 1\n\t" \
  "s_branch .Lwt%=\n\t" \
  ".Lwx%=:\n\t" \
  "s_lshl_b32 s93, s90, 2\n\t" \
  "s_sub_i32 s93, %[tgt], s93\n\t" \
  "s_cmp_le_i32 s93, 0\n\t" \
  "s_cbranch_scc1 .Lgo%=\n\t" \
  "s_min_i32 s93, s93, 8000\n\t" \
  "s_mov_b32 s94, s93\n\t" \
  ".Lsl%=:\n\t" \
  "s_sub_i32 s94, s94, 18\n\t" \
  "s_cmp_lt_i32 s94, 30\n\t" \
  "s_cbranch_scc1 .Lsx%=\n\t" \
  "s_sleep 1\n\t" \
  "s_branch .Lsl%=\n\t" \
  ".Lsx%=:\n\t" \
  "s_sub_i32 s93, s93, 12\n\t" \
  "s_max_i32 s93, s93, 4\n\t" \
  "s_lshr_b32 s93, s93, 2\n\t" \
  "s_add_u32 s93, s93, s90\n\t" \
  "s_memrealtime s[90:91]\n\t" \
  ".Lpace%=:\n\t" \
  "s_memrealtime s[94:95]\n\t" \
  "s_waitcnt lgkmcnt(1)\n\t" \
  "s_sub_i32 s92, s90, s93\n\t" \
  "s_cmp_ge_i32 s92, 0\n\t" \
  "s_cbranch_scc1 .Le%=\n\t" \
  "s_memrealtime s[90:91]\n\t" \
  "s_waitcnt lgkmcnt(1)\n\t" \
  "s_sub_i32 s92, s94, s93\n\t" \
  "s_cmp_lt_i32 s92, 0\n\t" \
  "s_cbranch_scc1 .Lpace%=\n\t" \
  "s_nop 4\n\t" \
  ".Le%=:\n\t" \
  "s_add_u32 s92, s92, s93\n\t" \
  "s_lshl_b32 s92, s92, 2\n\t" \
  "s_sub_i32 s92, %[tgt], s92\n\t" \
  "s_max_i32 s92, s92, 0\n\t" \
  "s_bitcmp0_b32 s92, 3\n\t" \
  "s_cbranch_scc1 .Lf3%=\n\t" \
  "s_nop 15\n\t" \
  ".Lf3%=:\n\t" \
  "s_bitcmp0_b32 s92, 2\n\t" \
  "s_cbranch_scc1 .Lf2%=\n\t" \
  "s_nop 10\n\t" \
  ".Lf2%=:\n\t" \
  ".Lgo%=:\n\t" \
  "s_mov_b32 s92, 0\n\t" \
  "s_memtime s[88:89]\n\t" \
  ".Lpoll%=:\n\t" \
  "buffer_load_dwordx4 v[200:203], v248, %[rs], %[som] offen sc1\n\t" \
  "buffer_load_dwordx4 v[204:207], v248, %[rs], %[som2] offen sc1\n\t" \
  ".p2align 5\n\t" \
  "s_nop 0\n\t" \
  "s_nop 0\n\t" \
  "s_waitcnt vmcnt(1) lgkmcnt(0)\n\t" \
  "ds_write_b128 v249, v[200:203]\n\t" \
  "v_smfmac_f32_16x16x64_bf16 v[192:195], v[200:203], v[208:215], v251\n\t" \
  "v_max3_u32 v244, v200, v201, v202\n\t" \
  "v_smfmac_f32_16x16x64_bf16 v[196:199], v[200:203], v[236:243], v251\n\t" \
  "s_waitcnt vmcnt(0)\n\t" \
  "ds_write_b128 v249, v[204:207] offset:8192\n\t" \
  "v_smfmac_f32_16x16x64_bf16 v[192:195], v[204:207], v[216:223], v251\n\t" \
  "v_max3_u32 v244, v244, v203, v204\n\t" \
  "v_max3_u32 v244, v244, v205, v206\n\t" \
  "v_smfmac_f32_16x16x64_bf16 v[196:199], v[204:207], v[224:231], v251\n\t" \
  "v_max_u32 v244, v244, v207\n\t" \
  "v_cmp_le_u32 vcc, %[sent], v244\n\t" \
  "s_cbranch_vccnz .Lslow%=\n\t" \
  ".Lok%=:\n\t" \
  "s_waitcnt lgkmcnt(0)\n\t" \
  "s_setprio 3\n\t" \
  "s_barrier\n\t" \
  "s_memrealtime s[90:91]\n\t" \
  "s_memtime s[94:95]\n\t" \
  "v_mov_b32 v232, %[step]\n\t" \
  "v_lshrrev_b32 v234, 1, v248\n\t" \
  "s_mov_b64 exec, 0xffff\n\t" \
  "s_waitcnt lgkmcnt(0)\n\t" \
  "s_lshl_b32 s90, s90, 2\n\t" \
  "s_sub_u32 s94, s94, s88\n\t" \
  "s_mul_i32 s94, s94, 12200\n\t" \
  "s_lshr_b32 s94, s94, 16\n\t" \
  "s_add_u32 s94, s94, %[tgt]\n\t" \
  "s_sub_i32 s93, s90, s94\n\t" \
  "s_sub_i32 s93, s93, 30\n\t" \
  "s_max_i32 s93, s93, 0\n\t" \
  "s_add_u32 s90, s94, s93\n\t" \
  "s_sub_i32 s93, s90, %[tgt]\n\t" \
  "s_cmp_gt_i32 s93, 270\n\t" \
  "s_cselect_b32 s93, 70, 0\n\t" \
  "s_add_u32 s93, s93, s90\n\t" \
  "s_add_u32 s93, s93, %[lead]\n\t" \
  "v_mov_b32 v233, s93\n\t" \
  "buffer_store_dwordx2 v[232:233], v234, %[rs], %[sbc] offen sc1\n\t" \
  "s_mov_b64 exec, -1\n\t" \
  "s_mov_b32 %[tb1], s90\n\t" \
  "s_setprio 0\n\t" \
  "ds_read_b128 v[208:211], v248 offset:9216\n\t" \
  "ds_read_b128 v[212:215], v248 offset:10240\n\t" \
  "ds_read_b128 v[236:239], v248 offset:11264\n\t" \
  "ds_read_b128 v[240:243], v248 offset:13312\n\t" \
  "ds_read_b128 v[244:247], v248 offset:14336\n\t" \
  "s_waitcnt lgkmcnt(4)\n\t" \
  "ds_read_b128 v[200:203], v248 offset:6144\n\t" \
  "v_smfmac_f32_16x16x64_bf16 v[192:195], v[208:211], v[0:7], v251\n\t" \
  "ds_read_b128 v[216:219], v250 offset:32768\n\t" \
  "v_smfmac_f32_16x16x64_bf16 v[196:199], v[208:211], v[8:15], v251\n\t" \
  "s_waitcnt lgkmcnt(5)\n\t" \
  "ds_read_b128 v[220:223], v250 offset:33792\n\t" \
  "v_smfmac_f32_16x16x64_bf16 v[192:195], v[212:215], v[16:23], v251\n\t" \
  "ds_read_b128 v[224:227], v250 offset:34816\n\t" \
  "v_smfmac_f32_16x16x64_bf16 v[196:199], v[212:215], v[24:31], v251\n\t" \
  "s_waitcnt lgkmcnt(6)\n\t" \
  "ds_read_b128 v[228:231], v250 offset:35840\n\t" \
  "v_smfmac_f32_16x16x64_bf16 v[192:195], v[236:239], v[32:39], v251\n\t" \
  "ds_read_b128 v[204:207], v248 offset:0\n\t" \
  "v_smfmac_f32_16x16x64_bf16 v[196:199], v[236:239], v[40:47], v251\n\t" \
  "s_waitcnt lgkmcnt(7)\n\t" \
  "ds_read_b128 v[208:211], v248 offset:1024\n\t" \
  "v_smfmac_f32_16x16x64_bf16 v[192:195], v[240:243], v[48:55], v251\n\t" \
  "ds_read_b128 v[212:215], v248 offset:2048\n\t" \
  "v_smfmac_f32_16x16x64_bf16 v[196:199], v[240:243], v[56:63], v251\n\t" \
  "s_waitcnt lgkmcnt(7)\n\t" \
  "v_smfmac_f32_16x16x64_bf16 v[192:195], v[244:247], v[64:71], v251\n\t" \
  "ds_read_b128 v[236:239], v248 offset:3072\n\t" \
  "v_smfmac_f32_16x16x64_bf16 v[196:199], v[244:247], v[72:79], v251\n\t" \
  "s_waitcnt lgkmcnt(6)\n\t" \
  "v_smfmac_f32_16x16x64_bf16 v[192:195], v[200:203], v[216:223], v251\n\t" \
  "s_waitcnt lgkmcnt(4)\n\t" \
  "ds_read_b128 v[240:243], v248 offset:12288\n\t" \
  "v_smfmac_f32_16x16x64_bf16 v[196:199], v[200:203], v[224:231], v251\n\t" \
  "s_waitcnt lgkmcnt(4)\n\t" \
  "ds_read_b128 v[216:219], v250 offset:0\n\t" \
  "v_smfmac_f32_16x16x64_bf16 v[192:195], v[204:207], v[80:87], v251\n\t" \
  "ds_read_b128 v[220:223], v250 offset:1024\n\t" \
  "v_smfmac_f32_16x16x64_bf16 v[196:199], v[204:207], v[88:95], v251\n\t" \
  "s_waitcnt lgkmcnt(4)\n\t" \
  "ds_read_b128 v[224:227], v250 offset:2048\n\t" \
  "v_smfmac_f32_16x16x64_bf16 v[192:195], v[208:211], v[96:103], v251\n\t" \
  "ds_read_b128 v[228:231], v250 offset:3072\n\t" \
  "v_smfmac_f32_16x16x64_bf16 v[196:199], v[208:211], v[104:111], v251\n\t" \
  "ds_read_b128 v[244:247], v248 offset:4096\n\t" \
  "v_smfmac_f32_16x16x64_bf16 v[192:195], v[212:215], v[112:119], v251\n\t" \
  "ds_read_b128 v[200:203], v248 offset:5120\n\t" \
  "v_smfmac_f32_16x16x64_bf16 v[196:199], v[212:215], v[120:127], v251\n\t" \
  "s_waitcnt lgkmcnt(6)\n\t" \
  "v_smfmac_f32_16x16x64_bf16 v[192:195], v[236:239], v[128:135], v251\n\t" \
  "ds_read_b128 v[204:207], v248 offset:8192\n\t" \
  "v_smfmac_f32_16x16x64_bf16 v[196:199], v[236:239], v[136:143], v251\n\t" \
  "s_waitcnt lgkmcnt(5)\n\t" \
  "v_smfmac_f32_16x16x64_bf16 v[192:195], v[240:243], v[216:223], v251\n\t" \
  "s_waitcnt lgkmcnt(3)\n\t" \
  "v_smfmac_f32_16x16x64_bf16 v[196:199], v[240:243], v[224:231], v251\n\t" \
  "s_waitcnt lgkmcnt(2)\n\t" \
  "v_smfmac_f32_16x16x64_bf16 v[192:195], v[244:247], v[144:151], v251\n\t" \
  "v_smfmac_f32_16x16x64_bf16 v[196:199], v[244:247], v[152:159], v251\n\t" \
  "s_waitcnt lgkmcnt(1)\n\t" \
  "v_smfmac_f32_16x16x64_bf16 v[192:195], v[200:203], v[160:167], v251\n\t" \
  "v_smfmac_f32_16x16x64_bf16 v[196:199], v[200:203], v[168:175], v251\n\t" \
  "s_waitcnt lgkmcnt(0)\n\t" \
  "v_smfmac_f32_16x16x64_bf16 v[192:195], v[204:207], v[176:183], v251\n\t" \
  "v_smfmac_f32_16x16x64_bf16 v[196:199], v[204:207], v[184:191], v251\n\t" \
  "s_nop 6\n\t" \
  "v_pk_add_f32 v[192:193], v[192:193], v[194:195]\n\t" \
  "v_pk_add_f32 v[196:197], v[196:197], v[198:199]\n\t" \
  "v_cvt_pk_bf16_f32 v192, v192, v193\n\t" \
  "v_cvt_pk_bf16_f32 v196, v196, v197\n\t" \
  "ds_write2_b32 v252, v192, v196 offset1:64\n\t" \
  "s_waitcnt lgkmcnt(0)\n\t" \
  "s_barrier\n\t" \
  "buffer_store_dword v192, v235, %[rs], %[sk] offen sc1\n\t" \
  "buffer_store_dword v196, v235, %[rs], %[sk] offen offset:256 sc1\n\t" \
  "s_mov_b32 %[spins], s92\n\t" \
  "s_branch .Lend%=\n\t" \
  ".Lslow%=:\n\t" \
  "s_mov_b64 s[94:95], 0\n\t" \
  "v_cmp_eq_u32 vcc, %[sent], v200\n\t" \
  "s_or_b64 s[94:95], s[94:95], vcc\n\t" \
  "v_cmp_eq_u32 vcc, %[sent], v201\n\t" \
  "s_or_b64 s[94:95], s[94:95], vcc\n\t" \
  "v_cmp_eq_u32 vcc, %[sent], v202\n\t" \
  "s_or_b64 s[94:95], s[94:95], vcc\n\t" \
  "v_cmp_eq_u32 vcc, %[sent], v203\n\t" \
  "s_or_b64 s[94:95], s[94:95], vcc\n\t" \
  "v_cmp_eq_u32 vcc, %[sent], v204\n\t" \
  "s_or_b64 s[94:95], s[94:95], vcc\n\t" \
  "v_cmp_eq_u32 vcc, %[sent], v205\n\t" \
  "s_or_b64 s[94:95], s[94:95], vcc\n\t" \
  "v_cmp_eq_u32 vcc, %[sent], v206\n\t" \
  "s_or_b64 s[94:95], s[94:95], vcc\n\t" \
  "v_cmp_eq_u32 vcc, %[sent], v207\n\t" \
  "s_or_b64 s[94:95], s[94:95], vcc\n\t" \
  "s_cmp_eq_u64 s[94:95], 0\n\t" \
  "s_cbranch_scc1 .Lok%=\n\t" \
  "v_mov_b64 v[192:193], 0\n\t" \
  "v_mov_b64 v[194:195], 0\n\t" \
  "v_mov_b64 v[196:197], 0\n\t" \
  "v_mov_b64 v[198:199], 0\n\t" \
  "s_add_u32 s92, s92, 1\n\t" \
  "s_cmp_eq_u32 s92, 1\n\t" \
  "s_cbranch_scc1 .Lpoll%=\n\t" \
  "s_sleep 2\n\t" \
  "s_and_b32 s93, s92, 63\n\t" \
  "s_cmp_lg_u32 s93, 0\n\t" \
  "s_cbranch_scc1 .Lpoll%=\n\t" \
  "v_mov_b32 v245, 0\n\t" \
  "buffer_load_dword v244, v245, %[rs], %[sab] offen sc1\n\t" \
  "s_waitcnt vmcnt(0)\n\t" \
  "v_readfirstlane_b32 s93, v244\n\t" \
  "s_nop 3\n\t" \
  "s_cmp_eq_u32 s93, 1\n\t" \
  "s_cbranch_scc1 .Lok%=\n\t" \
  "s_cmp_lt_u32 s92, 0x80000\n\t" \
  "s_cbranch_scc1 .Lpoll%=\n\t" \
  "v_mov_b32 v244, 1\n\t" \
  "buffer_store_dword v244, v245, %[rs], %[sab] offen sc1\n\t" \
  "s_branch .Lok%=\n\t" \
  ".Lend%=:\n\t"
#define VV_ASM \
  "s_waitcnt lgkmcnt(0)\n\t" \
  "v_mov_b64 v[128:129], 0\n\t" \
  "v_mov_b64 v[130:131], 0\n\t" \
  ".p2align 5\n\t" \
  "s_nop 0\n\t" \
  "s_nop 0\n\t" \
  "s_nop 0\n\t" \
  "s_nop 0\n\t" \
  "s_movk_i32 s92, 0x2000\n\t" \
  ".Lwt%=:\n\t" \
  "ds_read_b64 v[132:133], v200 offset:30720\n\t" \
  "s_memrealtime s[90:91]\n\t" \
  "s_sub_u32 s92, s92, 1\n\t" \
  "s_waitcnt lgkmcnt(0)\n\t" \
  "v_readfirstlane_b32 s93, v132\n\t" \
  "v_readfirstlane_b32 %[tgt], v133\n\t" \
  "s_cbranch_scc1 .Lgo%=\n\t" \
  "s_cmp_eq_u32 s93, %[step]\n\t" \
  "s_cbranch_scc1 .Lwx%=\n\t" \
  "s_sleep 1\n\t" \
  "s_branch .Lwt%=\n\t" \
  ".Lwx%=:\n\t" \
  "s_lshl_b32 s93, s90, 2\n\t" \
  "s_sub_i32 s93, %[tgt], s93\n\t" \
  "s_cmp_le_i32 s93, 0\n\t" \
  "s_cbranch_scc1 .Lgo%=\n\t" \
  "s_min_i32 s93, s93, 8000\n\t" \
  "s_mov_b32 s94, s93\n\t" \
  ".Lsl%=:\n\t" \
  "s_sub_i32 s94, s94, 18\n\t" \
  "s_cmp_lt_i32 s94, 30\n\t" \
  "s_cbranch_scc1 .Lsx%=\n\t" \
  "s_sleep 1\n\t" \
  "s_branch .Lsl%=\n\t" \
  ".Lsx%=:\n\t" \
  "s_sub_i32 s93, s93, 12\n\t" \
  "s_max_i32 s93, s93, 4\n\t" \
  "s_lshr_b32 s93, s93, 2\n\t" \
  "s_add_u32 s93, s93, s90\n\t" \
  "s_memrealtime s[90:91]\n\t" \
  ".Lpace%=:\n\t" \
  "s_memrealtime s[94:95]\n\t" \
  "s_waitcnt lgkmcnt(1)\n\t" \
  "s_sub_i32 s92, s90, s93\n\t" \
  "s_cmp_ge_i32 s92, 0\n\t" \
  "s_cbranch_scc1 .Le%=\n\t" \
  "s_memrealtime s[90:91]\n\t" \
  "s_waitcnt lgkmcnt(1)\n\t" \
  "s_sub_i32 s92, s94, s93\n\t" \
  "s_cmp_lt_i32 s92, 0\n\t" \
  "s_cbranch_scc1 .Lpace%=\n\t" \
  "s_nop 4\n\t" \
  ".Le%=:\n\t" \
  "s_add_u32 s92, s92, s93\n\t" \
  "s_lshl_b32 s92, s92, 2\n\t" \
  "s_sub_i32 s92, %[tgt], s92\n\t" \
  "s_max_i32 s92, s92, 0\n\t" \
  "s_bitcmp0_b32 s92, 3\n\t" \
  "s_cbranch_scc1 .Lf3%=\n\t" \
  "s_nop 15\n\t" \
  ".Lf3%=:\n\t" \
  "s_bitcmp0_b32 s92, 2\n\t" \
  "s_cbranch_scc1 .Lf2%=\n\t" \
  "s_nop 10\n\t" \
  ".Lf2%=:\n\t" \
  ".Lgo%=:\n\t" \
  "s_mov_b32 s92, 0\n\t" \
  ".Lpoll%=:\n\t" \
  "buffer_load_dwordx4 v[132:135], v200, %[rs], %[som] offen sc1\n\t" \
  "buffer_load_dwordx4 v[136:139], v200, %[rs], %[som2] offen sc1\n\t" \
  ".p2align 5\n\t" \
  "s_waitcnt vmcnt(1) lgkmcnt(0)\n\t" \
  "ds_write_b128 v232, v[132:135]\n\t" \
  "v_smfmac_f32_16x16x64_bf16 v[128:131], v[132:135], v[0:7], v202\n\t" \
  "v_max3_u32 v196, v132, v133, v134\n\t" \
  "s_waitcnt vmcnt(0)\n\t" \
  "ds_write_b128 v232, v[136:139] offset:8192\n\t" \
  "v_smfmac_f32_16x16x64_bf16 v[128:131], v[136:139], v[64:71], v202\n\t" \
  "v_max3_u32 v196, v196, v135, v136\n\t" \
  "v_max3_u32 v196, v196, v137, v138\n\t" \
  "v_max_u32 v196, v196, v139\n\t" \
  "v_cmp_le_u32 vcc, %[sent], v196\n\t" \
  "s_cbranch_vccnz .Lslow%=\n\t" \
  ".Lok%=:\n\t" \
  "s_waitcnt lgkmcnt(0)\n\t" \
  "s_barrier\n\t" \
  "ds_read_b128 v[164:167], v233 offset:0\n\t" \
  "ds_read_b128 v[140:143], v234 offset:0\n\t" \
  "ds_read_b128 v[144:147], v235 offset:0\n\t" \
  "ds_read_b128 v[148:151], v236 offset:0\n\t" \
  "ds_read_b128 v[152:155], v237 offset:0\n\t" \
  "s_waitcnt lgkmcnt(4)\n\t" \
  "ds_read_b128 v[156:159], v238 offset:0\n\t" \
  "v_smfmac_f32_16x16x64_bf16 v[128:131], v[164:167], v[8:15], v202\n\t" \
  "s_waitcnt lgkmcnt(4)\n\t" \
  "ds_read_b128 v[160:163], v239 offset:0\n\t" \
  "v_smfmac_f32_16x16x64_bf16 v[128:131], v[140:143], v[16:23], v202\n\t" \
  "s_waitcnt lgkmcnt(4)\n\t" \
  "ds_read_b128 v[168:171], v233 offset:8192\n\t" \
  "v_smfmac_f32_16x16x64_bf16 v[128:131], v[144:147], v[24:31], v202\n\t" \
  "s_waitcnt lgkmcnt(4)\n\t" \
  "ds_read_b128 v[172:175], v234 offset:8192\n\t" \
  "v_smfmac_f32_16x16x64_bf16 v[128:131], v[148:151], v[32:39], v202\n\t" \
  "s_waitcnt lgkmcnt(4)\n\t" \
  "ds_read_b128 v[176:179], v235 offset:8192\n\t" \
  "v_smfmac_f32_16x16x64_bf16 v[128:131], v[152:155], v[40:47], v202\n\t" \
  "s_waitcnt lgkmcnt(4)\n\t" \
  "ds_read_b128 v[180:183], v236 offset:8192\n\t" \
  "v_smfmac_f32_16x16x64_bf16 v[128:131], v[156:159], v[48:55], v202\n\t" \
  "s_waitcnt lgkmcnt(4)\n\t" \
  "ds_read_b128 v[184:187], v237 offset:8192\n\t" \
  "v_smfmac_f32_16x16x64_bf16 v[128:131], v[160:163], v[56:63], v202\n\t" \
  "s_waitcnt lgkmcnt(4)\n\t" \
  "ds_read_b128 v[188:191], v238 offset:8192\n\t" \
  "v_smfmac_f32_16x16x64_bf16 v[128:131], v[168:171], v[72:79], v202\n\t" \
  "s_waitcnt lgkmcnt(4)\n\t" \
  "ds_read_b128 v[192:195], v239 offset:8192\n\t" \
  "v_smfmac_f32_16x16x64_bf16 v[128:131], v[172:175], v[80:87], v202\n\t" \
  "s_waitcnt lgkmcnt(4)\n\t" \
  "v_smfmac_f32_16x16x64_bf16 v[128:131], v[176:179], v[88:95], v202\n\t" \
  "s_waitcnt lgkmcnt(3)\n\t" \
  "v_smfmac_f32_16x16x64_bf16 v[128:131], v[180:183], v[96:103], v202\n\t" \
  "s_waitcnt lgkmcnt(2)\n\t" \
  "v_smfmac_f32_16x16x64_bf16 v[128:131], v[184:187], v[104:111], v202\n\t" \
  "s_waitcnt lgkmcnt(1)\n\t" \
  "v_smfmac_f32_16x16x64_bf16 v[128:131], v[188:191], v[112:119], v202\n\t" \
  "s_waitcnt lgkmcnt(0)\n\t" \
  "v_smfmac_f32_16x16x64_bf16 v[128:131], v[192:195], v[120:127], v202\n\t" \
  "s_nop 7\n\t" \
  "v_add_f32 v128, v128, v130\n\t" \
  "v_add_f32 v129, v129, v131\n\t" \
  "v_cvt_pk_bf16_f32 v128, v128, v129\n\t" \
  "buffer_store_dword v128, v203, %[rs], %[sv] offen sc1\n\t" \
  "ds_write_b32 v204, v128\n\t" \
  "s_waitcnt lgkmcnt(0)\n\t" \
  "s_barrier\n\t" \
  "s_mov_b32 %[spins], s92\n\t" \
  "s_branch .Lend%=\n\t" \
  ".Lslow%=:\n\t" \
  "s_mov_b64 s[94:95], 0\n\t" \
  "v_cmp_eq_u32 vcc, %[sent], v132\n\t" \
  "s_or_b64 s[94:95], s[94:95], vcc\n\t" \
  "v_cmp_eq_u32 vcc, %[sent], v133\n\t" \
  "s_or_b64 s[94:95], s[94:95], vcc\n\t" \
  "v_cmp_eq_u32 vcc, %[sent], v134\n\t" \
  "s_or_b64 s[94:95], s[94:95], vcc\n\t" \
  "v_cmp_eq_u32 vcc, %[sent], v135\n\t" \
  "s_or_b64 s[94:95], s[94:95], vcc\n\t" \
  "v_cmp_eq_u32 vcc, %[sent], v136\n\t" \
  "s_or_b64 s[94:95], s[94:95], vcc\n\t" \
  "v_cmp_eq_u32 vcc, %[sent], v137\n\t" \
  "s_or_b64 s[94:95], s[94:95], vcc\n\t" \
  "v_cmp_eq_u32 vcc, %[sent], v138\n\t" \
  "s_or_b64 s[94:95], s[94:95], vcc\n\t" \
  "v_cmp_eq_u32 vcc, %[sent], v139\n\t" \
  "s_or_b64 s[94:95], s[94:95], vcc\n\t" \
  "s_cmp_eq_u64 s[94:95], 0\n\t" \
  "s_cbranch_scc1 .Lok%=\n\t" \
  "v_mov_b64 v[128:129], 0\n\t" \
  "v_mov_b64 v[130:131], 0\n\t" \
  "s_add_u32 s92, s92, 1\n\t" \
  "s_cmp_eq_u32 s92, 1\n\t" \
  "s_cbranch_scc1 .Lpoll%=\n\t" \
  "s_sleep 2\n\t" \
  "s_and_b32 s93, s92, 63\n\t" \
  "s_cmp_lg_u32 s93, 0\n\t" \
  "s_cbranch_scc1 .Lpoll%=\n\t" \
  "v_mov_b32 v197, 0\n\t" \
  "buffer_load_dword v196, v197, %[rs], %[sab] offen sc1\n\t" \
  "s_waitcnt vmcnt(0)\n\t" \
  "v_readfirstlane_b32 s93, v196\n\t" \
  "s_nop 3\n\t" \
  "s_cmp_eq_u32 s93, 1\n\t" \
  "s_cbranch_scc1 .Lok%=\n\t" \
  "s_cmp_lt_u32 s92, 0x80000\n\t" \
  "s_cbranch_scc1 .Lpoll%=\n\t" \
  "v_mov_b32 v196, 1\n\t" \
  "buffer_store_dword v196, v197, %[rs], %[sab] offen sc1\n\t" \
  "s_branch .Lok%=\n\t" \
  ".Lend%=:\n\t"
#define TK_ASM \
  "buffer_load_dwordx4 v[224:227], v200, %[rs], %[sk] offen offset:0 sc1\n\t" \
  "buffer_load_dwordx4 v[228:231], v200, %[rs], %[sk] offen offset:1024 sc1\n\t" \
  "s_memrealtime s[88:89]\n\t" \
  "v_add_u32 v205, %[tl], v200\n\t" \
  "v_add_u32 v206, %[tq], v205\n\t" \
  "v_add_u32 v207, %[tv], v205\n\t" \
  "s_waitcnt lgkmcnt(0)\n\t" \
  "ds_read_b128 v[128:131], v206 offset:8192\n\t" \
  "ds_read_b128 v[144:147], v205 offset:0\n\t" \
  "ds_read_b128 v[160:163], v205 offset:4096\n\t" \
  "ds_read_b128 v[132:135], v206 offset:9216\n\t" \
  "ds_read_b128 v[148:151], v205 offset:1024\n\t" \
  "ds_read_b128 v[164:167], v205 offset:5120\n\t" \
  "ds_read_b128 v[136:139], v206 offset:10240\n\t" \
  "ds_read_b128 v[152:155], v205 offset:2048\n\t" \
  "ds_read_b128 v[168:171], v205 offset:6144\n\t" \
  "ds_read_b128 v[140:143], v206 offset:11264\n\t" \
  "ds_read_b128 v[156:159], v205 offset:3072\n\t" \
  "ds_read_b128 v[172:175], v205 offset:7168\n\t" \
  "v_add_u32 v206, %[vp], v218\n\t" \
  "s_lshl_b32 s88, s88, 2\n\t" \
  "s_add_u32 s89, s88, %[od]\n\t" \
  "s_waitcnt lgkmcnt(10)\n\t" \
  "v_mfma_f32_16x16x32_bf16 v[196:199], v[144:147], v[128:131], 0\n\t" \
  "s_waitcnt lgkmcnt(9)\n\t" \
  "v_mfma_f32_16x16x32_bf16 v[144:147], v[160:163], v[128:131], 0\n\t" \
  "s_waitcnt lgkmcnt(7)\n\t" \
  "v_mfma_f32_16x16x32_bf16 v[196:199], v[148:151], v[132:135], v[196:199]\n\t" \
  "s_waitcnt lgkmcnt(6)\n\t" \
  "v_mfma_f32_16x16x32_bf16 v[144:147], v[164:167], v[132:135], v[144:147]\n\t" \
  "s_waitcnt lgkmcnt(4)\n\t" \
  "v_mfma_f32_16x16x32_bf16 v[196:199], v[152:155], v[136:139], v[196:199]\n\t" \
  "s_waitcnt lgkmcnt(3)\n\t" \
  "v_mfma_f32_16x16x32_bf16 v[144:147], v[168:171], v[136:139], v[144:147]\n\t" \
  "s_waitcnt lgkmcnt(1)\n\t" \
  "v_mfma_f32_16x16x32_bf16 v[196:199], v[156:159], v[140:143], v[196:199]\n\t" \
  "s_waitcnt lgkmcnt(0)\n\t" \
  "v_mfma_f32_16x16x32_bf16 v[144:147], v[172:175], v[140:143], v[144:147]\n\t" \
  "ds_read_b128 v[176:179], v207 offset:16384\n\t" \
  "ds_read_b128 v[180:183], v207 offset:17408\n\t" \
  "ds_read_b128 v[184:187], v207 offset:18432\n\t" \
  "ds_read_b128 v[188:191], v207 offset:19456\n\t" \
  "ds_read2st64_b32 v[192:193], v206 offset0:0 offset1:1\n\t" \
  "ds_read2st64_b32 v[194:195], v206 offset0:2 offset1:3\n\t" \
  "v_cvt_pk_bf16_f32 v197, v196, v197\n\t" \
  "v_cvt_pk_bf16_f32 v199, v198, v199\n\t" \
  "v_cvt_pk_bf16_f32 v145, v144, v145\n\t" \
  "v_cvt_pk_bf16_f32 v147, v146, v147\n\t" \
  "v_lshlrev_b32 v196, 16, v197\n\t" \
  "v_lshlrev_b32 v198, 16, v199\n\t" \
  "v_lshlrev_b32 v144, 16, v145\n\t" \
  "v_lshlrev_b32 v146, 16, v147\n\t" \
  "v_and_b32 v197, 0xffff0000, v197\n\t" \
  "v_and_b32 v199, 0xffff0000, v199\n\t" \
  "v_and_b32 v145, 0xffff0000, v145\n\t" \
  "v_and_b32 v147, 0xffff0000, v147\n\t" \
  "v_pk_mul_f32 v[196:197], v[196:197], %[sc] op_sel_hi:[1,0]\n\t" \
  "v_pk_mul_f32 v[198:199], v[198:199], %[sc] op_sel_hi:[1,0]\n\t" \
  "v_pk_mul_f32 v[144:145], v[144:145], %[sc] op_sel_hi:[1,0]\n\t" \
  "v_pk_mul_f32 v[146:147], v[146:147], %[sc] op_sel_hi:[1,0]\n\t" \
  "v_cvt_pk_bf16_f32 v197, v196, v197\n\t" \
  "v_cvt_pk_bf16_f32 v199, v198, v199\n\t" \
  "v_cvt_pk_bf16_f32 v145, v144, v145\n\t" \
  "v_cvt_pk_bf16_f32 v147, v146, v147\n\t" \
  "v_lshlrev_b32 v196, 16, v197\n\t" \
  "v_lshlrev_b32 v198, 16, v199\n\t" \
  "v_lshlrev_b32 v144, 16, v145\n\t" \
  "v_lshlrev_b32 v146, 16, v147\n\t" \
  "v_and_b32 v197, 0xffff0000, v197\n\t" \
  "v_and_b32 v199, 0xffff0000, v199\n\t" \
  "v_and_b32 v145, 0xffff0000, v145\n\t" \
  "v_and_b32 v147, 0xffff0000, v147\n\t" \
  "v_min_f32 v196, v196, v208\n\t" \
  "v_min_f32 v197, v197, v209\n\t" \
  "v_min_f32 v198, v198, v210\n\t" \
  "v_min_f32 v199, v199, v211\n\t" \
  "v_min_f32 v144, v144, v212\n\t" \
  "v_min_f32 v145, v145, v213\n\t" \
  "v_min_f32 v146, v146, v214\n\t" \
  "v_min_f32 v147, v147, v215\n\t" \
  "v_max3_f32 v148, v196, v197, v198\n\t" \
  "v_max3_f32 v149, v199, v144, v145\n\t" \
  "v_max3_f32 v148, v148, v146, v147\n\t" \
  "v_max_f32 v150, v148, v149\n\t" \
  "v_max_f32 v151, v148, v149\n\t" \
  "s_nop 1\n\t" \
  "v_permlane32_swap_b32 v150, v151\n\t" \
  "v_max_f32 v150, v150, v151\n\t" \
  "s_mov_b32 s92, 0\n\t" \
  ".Ltake%=:\n\t" \
  "s_waitcnt vmcnt(1) lgkmcnt(0)\n\t" \
  "ds_write_b128 v216, v[224:227]\n\t" \
  "v_max3_u32 v148, v224, v225, v226\n\t" \
  "s_waitcnt vmcnt(0)\n\t" \
  "ds_write_b128 v216, v[228:231] offset:1024\n\t" \
  "ds_read_b64_tr_b16 v[160:161], v217 offset:0\n\t" \
  "ds_read_b64_tr_b16 v[162:163], v217 offset:64\n\t" \
  "ds_read_b64_tr_b16 v[164:165], v217 offset:512\n\t" \
  "ds_read_b64_tr_b16 v[166:167], v217 offset:576\n\t" \
  "ds_read_b64_tr_b16 v[168:169], v217 offset:1024\n\t" \
  "ds_read_b64_tr_b16 v[170:171], v217 offset:1088\n\t" \
  "ds_read_b64_tr_b16 v[172:173], v217 offset:1536\n\t" \
  "ds_read_b64_tr_b16 v[174:175], v217 offset:1600\n\t" \
  "v_max3_u32 v148, v148, v227, v228\n\t" \
  "v_max3_u32 v148, v148, v229, v230\n\t" \
  "v_max_u32 v148, v148, v231\n\t" \
  "v_cmp_le_u32 vcc, %[sent], v148\n\t" \
  "s_cbranch_vccnz .Lslow%=\n\t" \
  ".Lok%=:\n\t" \
  "s_waitcnt lgkmcnt(6)\n\t" \
  "v_mfma_f32_16x16x32_bf16 v[152:155], v[160:163], v[128:131], 0\n\t" \
  "s_waitcnt lgkmcnt(4)\n\t" \
  "v_mfma_f32_16x16x32_bf16 v[152:155], v[164:167], v[132:135], v[152:155]\n\t" \
  "s_waitcnt lgkmcnt(2)\n\t" \
  "v_mfma_f32_16x16x32_bf16 v[152:155], v[168:171], v[136:139], v[152:155]\n\t" \
  "s_waitcnt lgkmcnt(0)\n\t" \
  "v_mfma_f32_16x16x32_bf16 v[152:155], v[172:175], v[140:143], v[152:155]\n\t" \
  "v_mov_b32 v157, 0\n\t" \
  "v_mov_b32 v158, 0\n\t" \
  "s_nop 5\n\t" \
  "v_cvt_pk_bf16_f32 v153, v152, v153\n\t" \
  "v_cvt_pk_bf16_f32 v155, v154, v155\n\t" \
  "v_lshlrev_b32 v152, 16, v153\n\t" \
  "v_lshlrev_b32 v154, 16, v155\n\t" \
  "v_and_b32 v153, 0xffff0000, v153\n\t" \
  "v_and_b32 v155, 0xffff0000, v155\n\t" \
  "v_pk_mul_f32 v[152:153], v[152:153], %[sc] op_sel_hi:[1,0]\n\t" \
  "v_pk_mul_f32 v[154:155], v[154:155], %[sc] op_sel_hi:[1,0]\n\t" \
  "v_cvt_pk_bf16_f32 v153, v152, v153\n\t" \
  "v_cvt_pk_bf16_f32 v155, v154, v155\n\t" \
  "v_lshlrev_b32 v152, 16, v153\n\t" \
  "v_lshlrev_b32 v154, 16, v155\n\t" \
  "v_and_b32 v153, 0xffff0000, v153\n\t" \
  "v_and_b32 v155, 0xffff0000, v155\n\t" \
  "v_max3_f32 v148, v152, v153, v150\n\t" \
  "v_max3_f32 v149, v148, v154, v155\n\t" \
  "v_max3_f32 v150, v148, v154, v155\n\t" \
  "v_cndmask_b32 v152, v152, v154, %[up]\n\t" \
  "v_cndmask_b32 v153, v153, v155, %[up]\n\t" \
  "v_permlane16_swap_b32 v149, v150\n\t" \
  "v_max_f32 v148, v149, v150\n\t" \
  "v_mul_f32 v148, %[negc], v148\n\t" \
  "v_fma_f32 v152, v152, %[c], v148\n\t" \
  "v_fma_f32 v153, v153, %[c], v148\n\t" \
  "v_exp_f32 v152, v152\n\t" \
  "v_fma_f32 v196, v196, %[c], v148\n\t" \
  "v_exp_f32 v153, v153\n\t" \
  "v_fma_f32 v197, v197, %[c], v148\n\t" \
  "v_exp_f32 v196, v196\n\t" \
  "v_fma_f32 v198, v198, %[c], v148\n\t" \
  "v_exp_f32 v197, v197\n\t" \
  "v_fma_f32 v199, v199, %[c], v148\n\t" \
  "v_exp_f32 v198, v198\n\t" \
  "v_fma_f32 v144, v144, %[c], v148\n\t" \
  "v_pk_add_f32 v[132:133], v[152:153], v[196:197]\n\t" \
  "v_exp_f32 v199, v199\n\t" \
  "v_fma_f32 v145, v145, %[c], v148\n\t" \
  "v_exp_f32 v144, v144\n\t" \
  "v_fma_f32 v146, v146, %[c], v148\n\t" \
  "v_pk_add_f32 v[132:133], v[132:133], v[198:199]\n\t" \
  "v_exp_f32 v145, v145\n\t" \
  "v_fma_f32 v147, v147, %[c], v148\n\t" \
  "v_exp_f32 v146, v146\n\t" \
  "v_pk_add_f32 v[132:133], v[132:133], v[144:145]\n\t" \
  "v_exp_f32 v147, v147\n\t" \
  "s_nop 0\n\t" \
  "v_pk_add_f32 v[132:133], v[132:133], v[146:147]\n\t" \
  "v_add_f32 v134, v132, v133\n\t" \
  "v_add_f32 v135, v132, v133\n\t" \
  "s_nop 1\n\t" \
  "v_permlane16_swap_b32 v134, v135\n\t" \
  "v_add_f32 v132, v134, v135\n\t" \
  "v_add_f32 v133, v134, v135\n\t" \
  "s_nop 1\n\t" \
  "v_permlane32_swap_b32 v132, v133\n\t" \
  "v_add_f32 v132, v132, v133\n\t" \
  "v_rcp_f32 v148, v132\n\t" \
  "s_nop 0\n\t" \
  "v_pk_mul_f32 v[152:153], v[152:153], v[148:149] op_sel_hi:[1,0]\n\t" \
  "v_pk_mul_f32 v[196:197], v[196:197], v[148:149] op_sel_hi:[1,0]\n\t" \
  "v_pk_mul_f32 v[198:199], v[198:199], v[148:149] op_sel_hi:[1,0]\n\t" \
  "v_pk_mul_f32 v[144:145], v[144:145], v[148:149] op_sel_hi:[1,0]\n\t" \
  "v_pk_mul_f32 v[146:147], v[146:147], v[148:149] op_sel_hi:[1,0]\n\t" \
  "v_cvt_pk_bf16_f32 v156, v152, v153\n\t" \
  "v_cvt_pk_bf16_f32 v159, v152, v153\n\t" \
  "v_cvt_pk_bf16_f32 v128, v196, v197\n\t" \
  "v_cvt_pk_bf16_f32 v129, v198, v199\n\t" \
  "v_mfma_f32_16x16x16_bf16 v[160:163], v[192:193], v[156:157], 0\n\t" \
  "v_cvt_pk_bf16_f32 v130, v144, v145\n\t" \
  "v_mfma_f32_16x16x16_bf16 v[164:167], v[192:193], v[158:159], 0\n\t" \
  "v_cvt_pk_bf16_f32 v131, v146, v147\n\t" \
  "v_mfma_f32_16x16x16_bf16 v[168:171], v[194:195], v[156:157], 0\n\t" \
  "v_mfma_f32_16x16x16_bf16 v[172:175], v[194:195], v[158:159], 0\n\t" \
  "s_nop 0\n\t" \
  "v_mfma_f32_16x16x32_bf16 v[160:163], v[176:179], v[128:131], v[160:163]\n\t" \
  "s_nop 0\n\t" \
  "v_mfma_f32_16x16x32_bf16 v[164:167], v[180:183], v[128:131], v[164:167]\n\t" \
  "s_nop 0\n\t" \
  "v_mfma_f32_16x16x32_bf16 v[168:171], v[184:187], v[128:131], v[168:171]\n\t" \
  "v_mfma_f32_16x16x32_bf16 v[172:175], v[188:191], v[128:131], v[172:175]\n\t" \
  "s_nop 2\n\t" \
  "v_cvt_pk_bf16_f32 v136, v160, v161\n\t" \
  "v_cvt_pk_bf16_f32 v137, v162, v163\n\t" \
  "v_cvt_pk_bf16_f32 v138, v164, v165\n\t" \
  "v_cvt_pk_bf16_f32 v139, v166, v167\n\t" \
  "v_cvt_pk_bf16_f32 v140, v168, v169\n\t" \
  "v_cvt_pk_bf16_f32 v141, v170, v171\n\t" \
  "v_cvt_pk_bf16_f32 v142, v172, v173\n\t" \
  "v_cvt_pk_bf16_f32 v143, v174, v175\n\t" \
  "s_memrealtime s[90:91]\n\t" \
  "s_waitcnt lgkmcnt(0)\n\t" \
  "s_lshl_b32 s93, s90, 2\n\t" \
  "s_sub_i32 s93, s89, s93\n\t" \
  "s_cmp_le_i32 s93, 0\n\t" \
  "s_cbranch_scc1 .Lgoo%=\n\t" \
  "s_min_i32 s93, s93, 8000\n\t" \
  "s_mov_b32 s94, s93\n\t" \
  ".Lslo%=:\n\t" \
  "s_sub_i32 s94, s94, 18\n\t" \
  "s_cmp_lt_i32 s94, 30\n\t" \
  "s_cbranch_scc1 .Lsxo%=\n\t" \
  "s_sleep 1\n\t" \
  "s_branch .Lslo%=\n\t" \
  ".Lsxo%=:\n\t" \
  "s_add_u32 s93, s93, 3\n\t" \
  "s_lshr_b32 s93, s93, 2\n\t" \
  "s_add_u32 s93, s93, s90\n\t" \
  "s_memrealtime s[90:91]\n\t" \
  ".Lpaceo%=:\n\t" \
  "s_memrealtime s[94:95]\n\t" \
  "s_waitcnt lgkmcnt(1)\n\t" \
  "s_sub_i32 s92, s90, s93\n\t" \
  "s_cmp_ge_i32 s92, 0\n\t" \
  "s_cbranch_scc1 .Leo%=\n\t" \
  "s_memrealtime s[90:91]\n\t" \
  "s_waitcnt lgkmcnt(1)\n\t" \
  "s_sub_i32 s92, s94, s93\n\t" \
  "s_cmp_lt_i32 s92, 0\n\t" \
  "s_cbranch_scc1 .Lpaceo%=\n\t" \
  ".Leo%=:\n\t" \
  ".Lgoo%=:\n\t" \
  "buffer_store_dwordx4 v[136:139], v200, %[rt], %[so] offen offset:0 sc1\n\t" \
  "buffer_store_dwordx4 v[140:143], v200, %[rt], %[so] offen offset:1024 sc1\n\t" \
  "s_waitcnt lgkmcnt(0)\n\t" \
  "s_branch .Lend%=\n\t" \
  ".Lpoll%=:\n\t" \
  "buffer_load_dwordx4 v[224:227], v200, %[rs], %[sk] offen offset:0 sc1\n\t" \
  "buffer_load_dwordx4 v[228:231], v200, %[rs], %[sk] offen offset:1024 sc1\n\t" \
  "s_branch .Ltake%=\n\t" \
  "s_branch .Lend%=\n\t" \
  ".Lslow%=:\n\t" \
  "s_mov_b64 s[94:95], 0\n\t" \
  "v_cmp_eq_u32 vcc, %[sent], v224\n\t" \
  "s_or_b64 s[94:95], s[94:95], vcc\n\t" \
  "v_cmp_eq_u32 vcc, %[sent], v225\n\t" \
  "s_or_b64 s[94:95], s[94:95], vcc\n\t" \
  "v_cmp_eq_u32 vcc, %[sent], v226\n\t" \
  "s_or_b64 s[94:95], s[94:95], vcc\n\t" \
  "v_cmp_eq_u32 vcc, %[sent], v227\n\t" \
  "s_or_b64 s[94:95], s[94:95], vcc\n\t" \
  "v_cmp_eq_u32 vcc, %[sent], v228\n\t" \
  "s_or_b64 s[94:95], s[94:95], vcc\n\t" \
  "v_cmp_eq_u32 vcc, %[sent], v229\n\t" \
  "s_or_b64 s[94:95], s[94:95], vcc\n\t" \
  "v_cmp_eq_u32 vcc, %[sent], v230\n\t" \
  "s_or_b64 s[94:95], s[94:95], vcc\n\t" \
  "v_cmp_eq_u32 vcc, %[sent], v231\n\t" \
  "s_or_b64 s[94:95], s[94:95], vcc\n\t" \
  "s_cmp_eq_u64 s[94:95], 0\n\t" \
  "s_cbranch_scc1 .Lok%=\n\t" \
  "s_add_u32 s92, s92, 1\n\t" \
  "s_cmp_eq_u32 s92, 1\n\t" \
  "s_cbranch_scc1 .Lpoll%=\n\t" \
  "s_sleep 2\n\t" \
  "s_and_b32 s93, s92, 63\n\t" \
  "s_cmp_lg_u32 s93, 0\n\t" \
  "s_cbranch_scc1 .Lpoll%=\n\t" \
  "v_mov_b32 v149, 0\n\t" \
  "buffer_load_dword v148, v149, %[rs], %[sab] offen sc1\n\t" \
  "s_waitcnt vmcnt(0)\n\t" \
  "v_readfirstlane_b32 s93, v148\n\t" \
  "s_nop 3\n\t" \
  "s_cmp_eq_u32 s93, 1\n\t" \
  "s_cbranch_scc1 .Lok%=\n\t" \
  "s_cmp_lt_u32 s92, 0x80000\n\t" \
  "s_cbranch_scc1 .Lpoll%=\n\t" \
  "v_mov_b32 v148, 1\n\t" \
  "buffer_store_dword v148, v149, %[rs], %[sab] offen sc1\n\t" \
  "s_branch .Lok%=\n\t" \
  ".Lend%=:\n\t"
constexpr int TLAG = 2, R4 = 0;
#define RED_ASM \
  "s_mov_b32 %[step], 0\n\t" \
  "s_lshl_b32 %[sh], %[step], 15\n\t" \
  "s_lshr_b32 s93, %[step], 4\n\t" \
  "s_lshr_b32 s93, %[pm], s93\n\t" \
  "s_and_b32 s93, s93, 4\n\t" \
  "s_lshl_b32 s93, s93, 11\n\t" \
  "s_lshl2_add_u32 %[so], %[sh], s93\n\t" \
  "s_sub_i32 s93, %[step], 2\n\t" \
  "s_lshl_b32 %[st], s93, 19\n\t" \
  "s_lshl_b32 s94, s93, 11\n\t" \
  "s_xor_b32 s94, s94, %[s0]\n\t" \
  "s_and_b32 s94, s94, 0x2000\n\t" \
  "s_or_b32 %[st], %[st], s94\n\t" \
  "s_cmp_lt_i32 s93, 0\n\t" \
  "s_cselect_b32 %[st], 0x70000000, %[st]\n\t" \
  "s_add_u32 %[st4], %[st], 4096\n\t" \
  "buffer_load_dwordx2 v[166:167], v173, %[rs], 0 offen sc1\n\t" \
  "v_mov_b64 v[144:145], 0\n\t" \
  "v_mov_b64 v[146:147], 0\n\t" \
  "v_mov_b64 v[148:149], 0\n\t" \
  "v_mov_b64 v[150:151], 0\n\t" \
  "v_mov_b64 v[152:153], 0\n\t" \
  "v_mov_b64 v[154:155], 0\n\t" \
  "v_mov_b64 v[156:157], 0\n\t" \
  "v_mov_b64 v[158:159], 0\n\t" \
  ".Lstep%=:\n\t" \
  "s_setprio 3\n\t" \
  "s_mov_b32 s92, 0\n\t" \
  ".Lcal%=:\n\t" \
  "s_waitcnt vmcnt(0)\n\t" \
  "s_memrealtime s[76:77]\n\t" \
  "v_cmp_ne_u32 vcc, %[step], v166\n\t" \
  "v_readlane_b32 s90, v167, 0\n\t" \
  "v_readlane_b32 s91, v167, 1\n\t" \
  "v_readlane_b32 s94, v167, 2\n\t" \
  "v_readlane_b32 s95, v167, 3\n\t" \
  "s_sub_i32 s90, s90, %[base]\n\t" \
  "s_sub_i32 s91, s91, %[base]\n\t" \
  "s_sub_i32 s94, s94, %[base]\n\t" \
  "s_sub_i32 s95, s95, %[base]\n\t" \
  "s_max_i32 s90, s90, s91\n\t" \
  "s_max_i32 s94, s94, s95\n\t" \
  "s_max_i32 s90, s90, s94\n\t" \
  "s_add_i32 s90, s90, %[ds]\n\t" \
  "s_waitcnt lgkmcnt(0)\n\t" \
  "s_lshl2_add_u32 s76, s76, %[mn]\n\t" \
  "s_sub_i32 s76, s76, %[base]\n\t" \
  "s_add_u32 %[E], s90, %[base]\n\t" \
  "s_and_b32 s93, vcc_lo, 0xf\n\t" \
  "s_cbranch_scc1 .Lbl%=\n\t" \
  ".Lgo%=:\n\t" \
  "v_mov_b32 v166, 7\n\t" \
  "s_mov_b64 exec, 1\n\t" \
  "v_mov_b32 v164, s90\n\t" \
  "ds_max_u32 v168, v164 offset:4100\n\t" \
  "ds_add_u32 v168, v164 offset:4096\n\t" \
  "s_mov_b64 exec, -1\n\t" \
  "s_memrealtime s[90:91]\n\t" \
  ".p2align 5\n\t" \
  "s_waitcnt lgkmcnt(0)\n\t" \
  "s_lshl_b32 s93, s90, 2\n\t" \
  "s_sub_i32 s93, %[E], s93\n\t" \
  "s_cmp_le_i32 s93, 0\n\t" \
  "s_cbranch_scc1 .Lgop%=\n\t" \
  "s_min_i32 s93, s93, 8000\n\t" \
  "s_mov_b32 s94, s93\n\t" \
  ".Lslp%=:\n\t" \
  "s_sub_i32 s94, s94, 18\n\t" \
  "s_cmp_lt_i32 s94, 30\n\t" \
  "s_cbranch_scc1 .Lsxp%=\n\t" \
  "s_sleep 1\n\t" \
  "s_branch .Lslp%=\n\t" \
  ".Lsxp%=:\n\t" \
  "s_sub_i32 s93, s93, 12\n\t" \
  "s_max_i32 s93, s93, 4\n\t" \
  "s_lshr_b32 s93, s93, 2\n\t" \
  "s_add_u32 s93, s93, s90\n\t" \
  "s_memrealtime s[90:91]\n\t" \
  ".Lpacep%=:\n\t" \
  "s_memrealtime s[94:95]\n\t" \
  "s_waitcnt lgkmcnt(1)\n\t" \
  "s_sub_i32 s92, s90, s93\n\t" \
  "s_cmp_ge_i32 s92, 0\n\t" \
  "s_cbranch_scc1 .Lep%=\n\t" \
  "s_memrealtime s[90:91]\n\t" \
  "s_waitcnt lgkmcnt(1)\n\t" \
  "s_sub_i32 s92, s94, s93\n\t" \
  "s_cmp_lt_i32 s92, 0\n\t" \
  "s_cbranch_scc1 .Lpacep%=\n\t" \
  "s_nop 4\n\t" \
  ".Lep%=:\n\t" \
  "s_add_u32 s92, s92, s93\n\t" \
  "s_lshl_b32 s92, s92, 2\n\t" \
  "s_sub_i32 s92, %[E], s92\n\t" \
  "s_max_i32 s92, s92, 0\n\t" \
  "s_bitcmp0_b32 s92, 3\n\t" \
  "s_cbranch_scc1 .Lf3p%=\n\t" \
  "s_nop 15\n\t" \
  ".Lf3p%=:\n\t" \
  "s_bitcmp0_b32 s92, 2\n\t" \
  "s_cbranch_scc1 .Lf2p%=\n\t" \
  "s_nop 10\n\t" \
  ".Lf2p%=:\n\t" \
  ".Lgop%=:\n\t" \
  "s_mov_b32 s92, 0\n\t" \
  "buffer_load_dwordx4 v[128:131], v234, %[rs], %[so] offen sc1\n\t" \
  "s_nop 15\n\t" \
  "s_nop 5\n\t" \
  "buffer_load_dwordx4 v[132:135], v235, %[rs], %[so] offen sc1\n\t" \
  "s_nop 15\n\t" \
  "s_nop 5\n\t" \
  "buffer_load_dwordx4 v[136:139], v236, %[rs], %[so] offen sc1\n\t" \
  "s_nop 15\n\t" \
  "s_nop 5\n\t" \
  "buffer_load_dwordx4 v[140:143], v237, %[rs], %[so] offen sc1\n\t" \
  ".p2align 5\n\t" \
  "s_nop 0\n\t" \
  "s_nop 0\n\t" \
  "s_nop 0\n\t" \
  ".Lw%=:\n\t" \
  "s_waitcnt lgkmcnt(0)\n\t" \
  "s_waitcnt vmcnt(3)\n\t" \
  "v_smfmac_f32_16x16x64_bf16 v[144:147], v[128:131], v[0:7], v172\n\t" \
  "v_max3_u32 v164, v128, v129, 0\n\t" \
  "v_smfmac_f32_16x16x64_bf16 v[148:151], v[128:131], v[8:15], v172\n\t" \
  "v_max3_u32 v164, v130, v131, v164\n\t" \
  "v_smfmac_f32_16x16x64_bf16 v[152:155], v[128:131], v[16:23], v172\n\t" \
  "buffer_load_dwordx4 v[176:179], v238, %[rt], %[st] offen offset:0 sc1\n\t" \
  "v_smfmac_f32_16x16x64_bf16 v[156:159], v[128:131], v[24:31], v172\n\t" \
  "s_waitcnt vmcnt(3)\n\t" \
  "v_smfmac_f32_16x16x64_bf16 v[144:147], v[132:135], v[32:39], v172\n\t" \
  "v_max3_u32 v164, v132, v133, v164\n\t" \
  "v_smfmac_f32_16x16x64_bf16 v[148:151], v[132:135], v[40:47], v172\n\t" \
  "v_max3_u32 v164, v134, v135, v164\n\t" \
  "v_smfmac_f32_16x16x64_bf16 v[152:155], v[132:135], v[48:55], v172\n\t" \
  "buffer_load_dwordx4 v[180:183], v238, %[rt], %[st] offen offset:1024 sc1\n\t" \
  "v_smfmac_f32_16x16x64_bf16 v[156:159], v[132:135], v[56:63], v172\n\t" \
  "s_waitcnt vmcnt(3)\n\t" \
  "v_smfmac_f32_16x16x64_bf16 v[144:147], v[136:139], v[64:71], v172\n\t" \
  "v_max3_u32 v164, v136, v137, v164\n\t" \
  "v_smfmac_f32_16x16x64_bf16 v[148:151], v[136:139], v[72:79], v172\n\t" \
  "v_max3_u32 v164, v138, v139, v164\n\t" \
  "v_smfmac_f32_16x16x64_bf16 v[152:155], v[136:139], v[80:87], v172\n\t" \
  "buffer_load_dwordx4 v[184:187], v238, %[rt], %[st4] offen offset:0 sc1\n\t" \
  "v_smfmac_f32_16x16x64_bf16 v[156:159], v[136:139], v[88:95], v172\n\t" \
  "s_waitcnt vmcnt(3)\n\t" \
  "v_smfmac_f32_16x16x64_bf16 v[144:147], v[140:143], v[96:103], v172\n\t" \
  "v_max3_u32 v164, v140, v141, v164\n\t" \
  "v_smfmac_f32_16x16x64_bf16 v[148:151], v[140:143], v[104:111], v172\n\t" \
  "v_max3_u32 v164, v142, v143, v164\n\t" \
  "v_smfmac_f32_16x16x64_bf16 v[152:155], v[140:143], v[112:119], v172\n\t" \
  "v_cmp_le_u32 vcc, %[sent], v164\n\t" \
  "buffer_load_dwordx4 v[188:191], v238, %[rt], %[st4] offen offset:1024 sc1\n\t" \
  "v_smfmac_f32_16x16x64_bf16 v[156:159], v[140:143], v[120:127], v172\n\t" \
  "s_cbranch_vccnz .Lslow%=\n\t" \
  ".Lok%=:\n\t" \
  "v_add_f32_dpp v160, v144, v146 quad_perm:[0,1,2,3] row_mask:0x3 bank_mask:0xf\n\t" \
  "v_add_f32_dpp v161, v145, v147 quad_perm:[0,1,2,3] row_mask:0x3 bank_mask:0xf\n\t" \
  "v_add_f32_dpp v162, v148, v150 quad_perm:[0,1,2,3] row_mask:0x3 bank_mask:0xf\n\t" \
  "v_add_f32_dpp v163, v149, v151 quad_perm:[0,1,2,3] row_mask:0x3 bank_mask:0xf\n\t" \
  "v_add_f32_dpp v160, v152, v154 quad_perm:[0,1,2,3] row_mask:0xc bank_mask:0xf\n\t" \
  "v_add_f32_dpp v161, v153, v155 quad_perm:[0,1,2,3] row_mask:0xc bank_mask:0xf\n\t" \
  "s_nop 0\n\t" \
  "v_add_f32_dpp v162, v156, v158 quad_perm:[0,1,2,3] row_mask:0xc bank_mask:0xf\n\t" \
  "v_add_f32_dpp v163, v157, v159 quad_perm:[0,1,2,3] row_mask:0xc bank_mask:0xf\n\t" \
  "s_nop 0\n\t" \
  "v_permlane32_swap_b32 v160, v162\n\t" \
  "v_permlane32_swap_b32 v161, v163\n\t" \
  "v_add_f32 v160, v160, v162\n\t" \
  "v_add_f32 v161, v161, v163\n\t" \
  "ds_write_b64 v169, v[160:161]\n\t" \
  "s_mov_b64 exec, 1\n\t" \
  "ds_inc_rtn_u32 v164, v168, v166 offset:4104\n\t" \
  "s_mov_b64 exec, -1\n\t" \
  "ds_read_b64 v[128:129], v170 offset:0\n\t" \
  "ds_read_b64 v[130:131], v170 offset:512\n\t" \
  "ds_read_b64 v[132:133], v170 offset:1024\n\t" \
  "ds_read_b64 v[134:135], v170 offset:1536\n\t" \
  "ds_read_b64 v[136:137], v170 offset:2048\n\t" \
  "ds_read_b64 v[138:139], v170 offset:2560\n\t" \
  "ds_read_b64 v[140:141], v170 offset:3072\n\t" \
  "ds_read_b64 v[142:143], v170 offset:3584\n\t" \
  "s_waitcnt lgkmcnt(8)\n\t" \
  "v_readfirstlane_b32 s93, v164\n\t" \
  "s_cmp_lg_u32 s93, 7\n\t" \
  "s_cbranch_scc1 .Lb1%=\n\t" \
  "s_waitcnt lgkmcnt(4)\n\t" \
  "v_pk_add_f32 v[128:129], v[128:129], v[130:131]\n\t" \
  "v_pk_add_f32 v[132:133], v[132:133], v[134:135]\n\t" \
  "s_waitcnt lgkmcnt(0)\n\t" \
  "v_pk_add_f32 v[136:137], v[136:137], v[138:139]\n\t" \
  "v_pk_add_f32 v[140:141], v[140:141], v[142:143]\n\t" \
  "v_pk_add_f32 v[128:129], v[128:129], v[132:133]\n\t" \
  "v_pk_add_f32 v[136:137], v[136:137], v[140:141]\n\t" \
  "v_pk_add_f32 v[128:129], v[128:129], v[136:137]\n\t" \
  "v_cvt_pk_bf16_f32 v128, v128, v129\n\t" \
  "s_nop 1\n\t" \
  "v_mov_b32_dpp v129, v128 quad_perm:[1,0,3,2] row_mask:0xf bank_mask:0xf\n\t" \
  "v_perm_b32 v128, v129, v128, v174\n\t" \
  "buffer_store_dword v128, v171, %[rs], %[sh] offen sc1\n\t" \
  "s_memtime s[90:91]\n\t" \
  "s_barrier\n\t" \
  "s_memtime s[76:77]\n\t" \
  "s_memrealtime s[78:79]\n\t" \
  "s_nop 3\n\t" \
  "s_memrealtime s[80:81]\n\t" \
  "s_nop 3\n\t" \
  "s_memrealtime s[82:83]\n\t" \
  "s_nop 3\n\t" \
  "s_memrealtime s[84:85]\n\t" \
  "s_nop 3\n\t" \
  "s_memrealtime s[86:87]\n\t" \
  "s_nop 3\n\t" \
  "s_memrealtime s[88:89]\n\t" \
  "s_mov_b32 s93, 0\n\t" \
  "s_waitcnt lgkmcnt(0)\n\t" \
  "s_cmp_eq_u32 s80, s78\n\t" \
  "s_addc_u32 s93, s93, 0\n\t" \
  "s_cmp_eq_u32 s82, s78\n\t" \
  "s_addc_u32 s93, s93, 0\n\t" \
  "s_cmp_eq_u32 s84, s78\n\t" \
  "s_addc_u32 s93, s93, 0\n\t" \
  "s_cmp_eq_u32 s86, s78\n\t" \
  "s_addc_u32 s93, s93, 0\n\t" \
  "s_cmp_eq_u32 s88, s78\n\t" \
  "s_addc_u32 s93, s93, 0\n\t" \
  "s_mul_i32 s93, s93, 21\n\t" \
  "s_add_u32 s76, s76, 15\n\t" \
  "s_add_u32 s76, s76, s93\n\t" \
  "s_lshl2_add_u32 s78, s78, 16\n\t" \
  "s_mov_b64 exec, 1\n\t" \
  "s_sub_i32 s90, s76, s90\n\t" \
  "s_mul_i32 s90, s90, 11900\n\t" \
  "s_ashr_i32 s90, s90, 16\n\t" \
  "s_sub_u32 s90, s78, s90\n\t" \
  "s_sub_u32 s90, s90, %[base]\n\t" \
  "v_mov_b32 v164, s90\n\t" \
  "ds_write_b32 v168, v164 offset:4108\n\t" \
  "s_mov_b64 exec, -1\n\t" \
  "s_waitcnt vmcnt(1)\n\t" \
  ".Lb2%=:\n\t" \
  "s_setprio 0\n\t" \
  "s_bitcmp0_b64 %[am], %[step]\n\t" \
  "s_cbranch_scc1 .La1%=\n\t" \
  "buffer_store_dwordx4 v[224:227], v228, %[ra], 0 offen sc1\n\t" \
  ".La1%=:\n\t" \
  "buffer_load_dwordx4 v[192:195], v239, %[rt], %[st] offen offset:0 sc1\n\t" \
  "buffer_load_dwordx4 v[196:199], v239, %[rt], %[st] offen offset:1024 sc1\n\t" \
  "v_mfma_f32_16x16x32_bf16 v[128:131], v[0:3], v[176:179], 0\n\t" \
  "v_mfma_f32_16x16x32_bf16 v[136:139], v[8:11], v[176:179], 0\n\t" \
  "ds_read2st64_b32 v[144:145], v231 offset0:0 offset1:8\n\t" \
  "ds_read2st64_b32 v[146:147], v231 offset0:16 offset1:24\n\t" \
  "ds_read2st64_b32 v[148:149], v231 offset0:32 offset1:40\n\t" \
  "ds_read2st64_b32 v[150:151], v231 offset0:48 offset1:56\n\t" \
  "buffer_load_dwordx4 v[200:203], v239, %[rt], %[st4] offen offset:0 sc1\n\t" \
  "buffer_load_dwordx4 v[204:207], v239, %[rt], %[st4] offen offset:1024 sc1\n\t" \
  "v_mfma_f32_16x16x32_bf16 v[132:135], v[4:7], v[180:183], 0\n\t" \
  "v_mfma_f32_16x16x32_bf16 v[140:143], v[12:15], v[180:183], 0\n\t" \
  "s_waitcnt lgkmcnt(0)\n\t" \
  "v_add_f32 v144, v144, v145\n\t" \
  "v_add_f32 v146, v146, v147\n\t" \
  "v_add_f32 v148, v148, v149\n\t" \
  "v_add_f32 v150, v150, v151\n\t" \
  "v_add_f32 v144, v144, v146\n\t" \
  "v_add_f32 v148, v148, v150\n\t" \
  "buffer_load_dwordx4 v[208:211], v240, %[rt], %[st] offen offset:0 sc1\n\t" \
  "buffer_load_dwordx4 v[212:215], v240, %[rt], %[st] offen offset:1024 sc1\n\t" \
  "v_mfma_f32_16x16x32_bf16 v[128:131], v[16:19], v[184:187], v[128:131]\n\t" \
  "v_mfma_f32_16x16x32_bf16 v[136:139], v[24:27], v[184:187], v[136:139]\n\t" \
  "v_add_f32 v144, v144, v148\n\t" \
  "v_cvt_pk_bf16_f32 v144, v144, v144\n\t" \
  "ds_write_b16 v232, v144\n\t" \
  "ds_read_b128 v[224:227], v229\n\t" \
  "v_add_u32 v228, %[ainc], v228\n\t" \
  "v_add_u32 v229, %[rinc], v229\n\t" \
  "v_add_u32 v232, 1024, v232\n\t" \
  "v_xor_b32 v231, 0x4000, v231\n\t" \
  "buffer_load_dwordx4 v[216:219], v240, %[rt], %[st4] offen offset:0 sc1\n\t" \
  "buffer_load_dwordx4 v[220:223], v240, %[rt], %[st4] offen offset:1024 sc1\n\t" \
  "v_mfma_f32_16x16x32_bf16 v[132:135], v[20:23], v[188:191], v[132:135]\n\t" \
  "v_mfma_f32_16x16x32_bf16 v[140:143], v[28:31], v[188:191], v[140:143]\n\t" \
  "buffer_load_dwordx4 v[176:179], v241, %[rt], %[st] offen offset:0 sc1\n\t" \
  "s_waitcnt vmcnt(8)\n\t" \
  "v_mfma_f32_16x16x32_bf16 v[128:131], v[32:35], v[192:195], v[128:131]\n\t" \
  "v_mfma_f32_16x16x32_bf16 v[136:139], v[40:43], v[192:195], v[136:139]\n\t" \
  "buffer_load_dwordx4 v[180:183], v241, %[rt], %[st] offen offset:1024 sc1\n\t" \
  "s_waitcnt vmcnt(8)\n\t" \
  "v_mfma_f32_16x16x32_bf16 v[132:135], v[36:39], v[196:199], v[132:135]\n\t" \
  "v_mfma_f32_16x16x32_bf16 v[140:143], v[44:47], v[196:199], v[140:143]\n\t" \
  "buffer_load_dwordx4 v[184:187], v241, %[rt], %[st4] offen offset:0 sc1\n\t" \
  "s_waitcnt vmcnt(8)\n\t" \
  "v_mfma_f32_16x16x32_bf16 v[128:131], v[48:51], v[200:203], v[128:131]\n\t" \
  "v_mfma_f32_16x16x32_bf16 v[136:139], v[56:59], v[200:203], v[136:139]\n\t" \
  "ds_read_b128 v[160:163], v168 offset:4096\n\t" \
  "buffer_load_dwordx4 v[188:191], v241, %[rt], %[st4] offen offset:1024 sc1\n\t" \
  "s_waitcnt vmcnt(8)\n\t" \
  "v_mfma_f32_16x16x32_bf16 v[132:135], v[52:55], v[204:207], v[132:135]\n\t" \
  "v_mfma_f32_16x16x32_bf16 v[140:143], v[60:63], v[204:207], v[140:143]\n\t" \
  "s_waitcnt lgkmcnt(0)\n\t" \
  "v_readfirstlane_b32 s90, v161\n\t" \
  "v_readfirstlane_b32 s91, v163\n\t" \
  "v_readfirstlane_b32 s94, v160\n\t" \
  "s_sub_u32 s93, s94, %[sm]\n\t" \
  "s_mov_b32 %[sm], s94\n\t" \
  "s_lshr_b32 s93, s93, 3\n\t" \
  "s_sub_i32 s93, s90, s93\n\t" \
  "s_mul_i32 s93, s93, %[wm]\n\t" \
  "s_ashr_i32 s93, s93, 2\n\t" \
  "s_min_u32 s93, s93, %[gm]\n\t" \
  "s_sub_i32 s90, s90, s93\n\t" \
  "s_add_u32 s93, s90, %[rl]\n\t" \
  "s_add_u32 s93, s93, %[base]\n\t" \
  "s_add_u32 %[E], s93, %[lk]\n\t" \
  "s_sub_i32 s91, s91, s90\n\t" \
  "s_sub_i32 s91, s91, %[rl]\n\t" \
  "s_min_i32 s93, s91, %[up]\n\t" \
  "s_max_i32 s93, s93, %[dn]\n\t" \
  "s_cmp_gt_i32 s91, 60\n\t" \
  "s_cselect_b32 s93, 2, s93\n\t" \
  "s_add_i32 %[rl], %[rl], s93\n\t" \
  "v_mov_b32 v160, %[rl]\n\t" \
  "buffer_store_dword v160, v175, %[rs], 0 offen sc1\n\t" \
  "s_waitcnt vmcnt(8)\n\t" \
  "v_mfma_f32_16x16x32_bf16 v[128:131], v[64:67], v[208:211], v[128:131]\n\t" \
  "v_mfma_f32_16x16x32_bf16 v[136:139], v[72:75], v[208:211], v[136:139]\n\t" \
  "s_add_u32 %[step], %[step], 1\n\t" \
  "s_lshl_b32 %[sh], %[step], 15\n\t" \
  "s_lshr_b32 s93, %[step], 4\n\t" \
  "s_lshr_b32 s93, %[pm], s93\n\t" \
  "s_and_b32 s93, s93, 4\n\t" \
  "s_lshl_b32 s93, s93, 11\n\t" \
  "s_lshl2_add_u32 %[so], %[sh], s93\n\t" \
  "s_waitcnt vmcnt(7)\n\t" \
  "v_mfma_f32_16x16x32_bf16 v[132:135], v[68:71], v[212:215], v[132:135]\n\t" \
  "v_mfma_f32_16x16x32_bf16 v[140:143], v[76:79], v[212:215], v[140:143]\n\t" \
  "s_sub_i32 s93, %[step], 2\n\t" \
  "s_lshl_b32 %[st], s93, 19\n\t" \
  "s_lshl_b32 s94, s93, 11\n\t" \
  "s_xor_b32 s94, s94, %[s0]\n\t" \
  "s_and_b32 s94, s94, 0x2000\n\t" \
  "s_or_b32 %[st], %[st], s94\n\t" \
  "s_cmp_lt_i32 s93, 0\n\t" \
  "s_cselect_b32 %[st], 0x70000000, %[st]\n\t" \
  "s_add_u32 %[st4], %[st], 4096\n\t" \
  "s_waitcnt vmcnt(6)\n\t" \
  "v_mfma_f32_16x16x32_bf16 v[128:131], v[80:83], v[216:219], v[128:131]\n\t" \
  "v_mfma_f32_16x16x32_bf16 v[136:139], v[88:91], v[216:219], v[136:139]\n\t" \
  "s_waitcnt vmcnt(5)\n\t" \
  "v_mfma_f32_16x16x32_bf16 v[132:135], v[84:87], v[220:223], v[132:135]\n\t" \
  "v_mfma_f32_16x16x32_bf16 v[140:143], v[92:95], v[220:223], v[140:143]\n\t" \
  "s_cmp_eq_u32 %[step], 63\n\t" \
  "s_cbranch_scc1 .Llast%=\n\t" \
  "s_movk_i32 s93, 600\n\t" \
  ".Lpacel%=:\n\t" \
  "s_memrealtime s[90:91]\n\t" \
  "s_waitcnt lgkmcnt(0)\n\t" \
  "s_lshl_b32 s90, s90, 2\n\t" \
  "s_sub_i32 s91, %[E], s90\n\t" \
  "s_cmp_le_i32 s91, 0\n\t" \
  "s_cbranch_scc1 .Lgol%=\n\t" \
  "s_sub_u32 s93, s93, 1\n\t" \
  "s_cmp_lg_u32 s93, 0\n\t" \
  "s_cbranch_scc1 .Lpacel%=\n\t" \
  ".Lgol%=:\n\t" \
  "buffer_load_dwordx2 v[166:167], v173, %[rs], 0 offen sc1\n\t" \
  "s_waitcnt vmcnt(5)\n\t" \
  "v_mfma_f32_16x16x32_bf16 v[128:131], v[96:99], v[176:179], v[128:131]\n\t" \
  "v_mfma_f32_16x16x32_bf16 v[136:139], v[104:107], v[176:179], v[136:139]\n\t" \
  "s_waitcnt vmcnt(4)\n\t" \
  "v_mfma_f32_16x16x32_bf16 v[132:135], v[100:103], v[180:183], v[132:135]\n\t" \
  "v_mfma_f32_16x16x32_bf16 v[140:143], v[108:111], v[180:183], v[140:143]\n\t" \
  "s_waitcnt vmcnt(3)\n\t" \
  "v_mfma_f32_16x16x32_bf16 v[128:131], v[112:115], v[184:187], v[128:131]\n\t" \
  "v_mfma_f32_16x16x32_bf16 v[136:139], v[120:123], v[184:187], v[136:139]\n\t" \
  "s_waitcnt vmcnt(2)\n\t" \
  "v_mfma_f32_16x16x32_bf16 v[132:135], v[116:119], v[188:191], v[132:135]\n\t" \
  "v_mfma_f32_16x16x32_bf16 v[140:143], v[124:127], v[188:191], v[140:143]\n\t" \
  "v_mov_b64 v[144:145], 0\n\t" \
  "v_mov_b64 v[146:147], 0\n\t" \
  "v_mov_b64 v[148:149], 0\n\t" \
  "v_mov_b64 v[150:151], 0\n\t" \
  "v_mov_b64 v[152:153], 0\n\t" \
  "v_mov_b64 v[154:155], 0\n\t" \
  "v_mov_b64 v[156:157], 0\n\t" \
  "v_mov_b64 v[158:159], 0\n\t" \
  "v_pk_add_f32 v[128:129], v[128:129], v[132:133]\n\t" \
  "v_pk_add_f32 v[130:131], v[130:131], v[134:135]\n\t" \
  "ds_write_b128 v230, v[128:131] offset:0\n\t" \
  "v_pk_add_f32 v[136:137], v[136:137], v[140:141]\n\t" \
  "v_pk_add_f32 v[138:139], v[138:139], v[142:143]\n\t" \
  "ds_write_b128 v230, v[136:139] offset:1024\n\t" \
  "v_xor_b32 v230, 0x4000, v230\n\t" \
  "s_cmp_lg_u32 %[step], 63\n\t" \
  "s_cbranch_scc1 .Lstep%=\n\t" \
  ".Lfc%=:\n\t" \
  "s_waitcnt vmcnt(0)\n\t" \
  "v_cmp_eq_u32 vcc, -1, v166\n\t" \
  "s_cbranch_vccnz .Lfw%=\n\t" \
  ".Lfo%=:\n\t" \
  "v_add_u32 v242, 16384, v242\n\t" \
  "buffer_load_dwordx4 v[176:179], v238, %[rt], %[st] offen offset:0 sc1\n\t" \
  "buffer_load_dwordx4 v[180:183], v238, %[rt], %[st] offen offset:1024 sc1\n\t" \
  "buffer_load_dwordx4 v[184:187], v238, %[rt], %[st4] offen offset:0 sc1\n\t" \
  "buffer_load_dwordx4 v[188:191], v238, %[rt], %[st4] offen offset:1024 sc1\n\t" \
  "buffer_load_dwordx4 v[192:195], v239, %[rt], %[st] offen offset:0 sc1\n\t" \
  "buffer_load_dwordx4 v[196:199], v239, %[rt], %[st] offen offset:1024 sc1\n\t" \
  "buffer_load_dwordx4 v[200:203], v239, %[rt], %[st4] offen offset:0 sc1\n\t" \
  "buffer_load_dwordx4 v[204:207], v239, %[rt], %[st4] offen offset:1024 sc1\n\t" \
  "v_subrev_u32 v230, 98304, v230\n\t" \
  ".Lt%=:\n\t" \
  "s_waitcnt vmcnt(7)\n\t" \
  "v_mfma_f32_16x16x32_bf16 v[128:131], v[0:3], v[176:179], 0\n\t" \
  "v_mfma_f32_16x16x32_bf16 v[136:139], v[8:11], v[176:179], 0\n\t" \
  "buffer_load_dwordx4 v[176:179], v240, %[rt], %[st] offen offset:0 sc1\n\t" \
  "s_waitcnt vmcnt(7)\n\t" \
  "v_mfma_f32_16x16x32_bf16 v[132:135], v[4:7], v[180:183], 0\n\t" \
  "v_mfma_f32_16x16x32_bf16 v[140:143], v[12:15], v[180:183], 0\n\t" \
  "buffer_load_dwordx4 v[180:183], v240, %[rt], %[st] offen offset:1024 sc1\n\t" \
  "s_waitcnt vmcnt(7)\n\t" \
  "v_mfma_f32_16x16x32_bf16 v[128:131], v[16:19], v[184:187], v[128:131]\n\t" \
  "v_mfma_f32_16x16x32_bf16 v[136:139], v[24:27], v[184:187], v[136:139]\n\t" \
  "buffer_load_dwordx4 v[184:187], v240, %[rt], %[st4] offen offset:0 sc1\n\t" \
  "buffer_load_dword v166, v242, %[rs], 0 offen sc1\n\t" \
  "s_waitcnt vmcnt(8)\n\t" \
  "v_mfma_f32_16x16x32_bf16 v[132:135], v[20:23], v[188:191], v[132:135]\n\t" \
  "v_mfma_f32_16x16x32_bf16 v[140:143], v[28:31], v[188:191], v[140:143]\n\t" \
  "buffer_load_dwordx4 v[188:191], v240, %[rt], %[st4] offen offset:1024 sc1\n\t" \
  "s_waitcnt vmcnt(8)\n\t" \
  "v_mfma_f32_16x16x32_bf16 v[128:131], v[32:35], v[192:195], v[128:131]\n\t" \
  "v_mfma_f32_16x16x32_bf16 v[136:139], v[40:43], v[192:195], v[136:139]\n\t" \
  "buffer_load_dwordx4 v[192:195], v241, %[rt], %[st] offen offset:0 sc1\n\t" \
  "s_waitcnt vmcnt(8)\n\t" \
  "v_mfma_f32_16x16x32_bf16 v[132:135], v[36:39], v[196:199], v[132:135]\n\t" \
  "v_mfma_f32_16x16x32_bf16 v[140:143], v[44:47], v[196:199], v[140:143]\n\t" \
  "buffer_load_dwordx4 v[196:199], v241, %[rt], %[st] offen offset:1024 sc1\n\t" \
  "s_waitcnt vmcnt(8)\n\t" \
  "v_mfma_f32_16x16x32_bf16 v[128:131], v[48:51], v[200:203], v[128:131]\n\t" \
  "v_mfma_f32_16x16x32_bf16 v[136:139], v[56:59], v[200:203], v[136:139]\n\t" \
  "buffer_load_dwordx4 v[200:203], v241, %[rt], %[st4] offen offset:0 sc1\n\t" \
  "s_waitcnt vmcnt(8)\n\t" \
  "v_mfma_f32_16x16x32_bf16 v[132:135], v[52:55], v[204:207], v[132:135]\n\t" \
  "v_mfma_f32_16x16x32_bf16 v[140:143], v[60:63], v[204:207], v[140:143]\n\t" \
  "buffer_load_dwordx4 v[204:207], v241, %[rt], %[st4] offen offset:1024 sc1\n\t" \
  "s_waitcnt vmcnt(8)\n\t" \
  "v_mfma_f32_16x16x32_bf16 v[128:131], v[64:67], v[176:179], v[128:131]\n\t" \
  "v_mfma_f32_16x16x32_bf16 v[136:139], v[72:75], v[176:179], v[136:139]\n\t" \
  "s_waitcnt vmcnt(5)\n\t" \
  ".Ltc%=:\n\t" \
  "v_cmp_eq_u32 vcc, -1, v166\n\t" \
  "s_cbranch_vccnz .Ltw%=\n\t" \
  ".Lto%=:\n\t" \
  "s_cmp_eq_u32 %[step], 65\n\t" \
  "s_cbranch_scc1 .Ltl%=\n\t" \
  "s_add_u32 %[step], %[step], 1\n\t" \
  "s_sub_i32 s93, %[step], 2\n\t" \
  "s_lshl_b32 %[st], s93, 19\n\t" \
  "s_lshl_b32 s94, s93, 11\n\t" \
  "s_xor_b32 s94, s94, %[s0]\n\t" \
  "s_and_b32 s94, s94, 0x2000\n\t" \
  "s_or_b32 %[st], %[st], s94\n\t" \
  "s_cmp_lt_i32 s93, 0\n\t" \
  "s_cselect_b32 %[st], 0x70000000, %[st]\n\t" \
  "s_add_u32 %[st4], %[st], 4096\n\t" \
  "v_add_u32 v242, 16384, v242\n\t" \
  "s_cmp_eq_u32 %[step], 65\n\t" \
  "s_cbranch_scc0 .Ltn%=\n\t" \
  "v_mov_b32 v242, %[fg]\n\t" \
  ".Ltn%=:\n\t" \
  "buffer_load_dwordx4 v[176:179], v238, %[rt], %[st] offen offset:0 sc1\n\t" \
  "s_waitcnt vmcnt(7)\n\t" \
  "v_mfma_f32_16x16x32_bf16 v[132:135], v[68:71], v[180:183], v[132:135]\n\t" \
  "v_mfma_f32_16x16x32_bf16 v[140:143], v[76:79], v[180:183], v[140:143]\n\t" \
  "buffer_load_dwordx4 v[180:183], v238, %[rt], %[st] offen offset:1024 sc1\n\t" \
  "s_waitcnt vmcnt(7)\n\t" \
  "v_mfma_f32_16x16x32_bf16 v[128:131], v[80:83], v[184:187], v[128:131]\n\t" \
  "v_mfma_f32_16x16x32_bf16 v[136:139], v[88:91], v[184:187], v[136:139]\n\t" \
  "buffer_load_dwordx4 v[184:187], v238, %[rt], %[st4] offen offset:0 sc1\n\t" \
  "s_waitcnt vmcnt(7)\n\t" \
  "v_mfma_f32_16x16x32_bf16 v[132:135], v[84:87], v[188:191], v[132:135]\n\t" \
  "v_mfma_f32_16x16x32_bf16 v[140:143], v[92:95], v[188:191], v[140:143]\n\t" \
  "buffer_load_dwordx4 v[188:191], v238, %[rt], %[st4] offen offset:1024 sc1\n\t" \
  "s_waitcnt vmcnt(7)\n\t" \
  "v_mfma_f32_16x16x32_bf16 v[128:131], v[96:99], v[192:195], v[128:131]\n\t" \
  "v_mfma_f32_16x16x32_bf16 v[136:139], v[104:107], v[192:195], v[136:139]\n\t" \
  "buffer_load_dwordx4 v[192:195], v239, %[rt], %[st] offen offset:0 sc1\n\t" \
  "s_waitcnt vmcnt(7)\n\t" \
  "v_mfma_f32_16x16x32_bf16 v[132:135], v[100:103], v[196:199], v[132:135]\n\t" \
  "v_mfma_f32_16x16x32_bf16 v[140:143], v[108:111], v[196:199], v[140:143]\n\t" \
  "buffer_load_dwordx4 v[196:199], v239, %[rt], %[st] offen offset:1024 sc1\n\t" \
  "s_waitcnt vmcnt(7)\n\t" \
  "v_mfma_f32_16x16x32_bf16 v[128:131], v[112:115], v[200:203], v[128:131]\n\t" \
  "v_mfma_f32_16x16x32_bf16 v[136:139], v[120:123], v[200:203], v[136:139]\n\t" \
  "buffer_load_dwordx4 v[200:203], v239, %[rt], %[st4] offen offset:0 sc1\n\t" \
  "s_waitcnt vmcnt(7)\n\t" \
  "v_mfma_f32_16x16x32_bf16 v[132:135], v[116:119], v[204:207], v[132:135]\n\t" \
  "v_mfma_f32_16x16x32_bf16 v[140:143], v[124:127], v[204:207], v[140:143]\n\t" \
  "buffer_load_dwordx4 v[204:207], v239, %[rt], %[st4] offen offset:1024 sc1\n\t" \
  "s_nop 5\n\t" \
  "v_pk_add_f32 v[128:129], v[128:129], v[132:133]\n\t" \
  "v_pk_add_f32 v[130:131], v[130:131], v[134:135]\n\t" \
  "ds_write_b128 v230, v[128:131] offset:0\n\t" \
  "v_pk_add_f32 v[136:137], v[136:137], v[140:141]\n\t" \
  "v_pk_add_f32 v[138:139], v[138:139], v[142:143]\n\t" \
  "ds_write_b128 v230, v[136:139] offset:1024\n\t" \
  "v_add_u32 v230, 16384, v230\n\t" \
  "s_branch .Lt%=\n\t" \
  ".Ltl%=:\n\t" \
  "buffer_load_dwordx4 v[224:227], v243, %[rg], 0 offen sc1\n\t" \
  "s_waitcnt vmcnt(7)\n\t" \
  "v_mfma_f32_16x16x32_bf16 v[132:135], v[68:71], v[180:183], v[132:135]\n\t" \
  "v_mfma_f32_16x16x32_bf16 v[140:143], v[76:79], v[180:183], v[140:143]\n\t" \
  "s_waitcnt vmcnt(6)\n\t" \
  "v_mfma_f32_16x16x32_bf16 v[128:131], v[80:83], v[184:187], v[128:131]\n\t" \
  "v_mfma_f32_16x16x32_bf16 v[136:139], v[88:91], v[184:187], v[136:139]\n\t" \
  "s_waitcnt vmcnt(5)\n\t" \
  "v_mfma_f32_16x16x32_bf16 v[132:135], v[84:87], v[188:191], v[132:135]\n\t" \
  "v_mfma_f32_16x16x32_bf16 v[140:143], v[92:95], v[188:191], v[140:143]\n\t" \
  "s_waitcnt vmcnt(4)\n\t" \
  "v_mfma_f32_16x16x32_bf16 v[128:131], v[96:99], v[192:195], v[128:131]\n\t" \
  "v_mfma_f32_16x16x32_bf16 v[136:139], v[104:107], v[192:195], v[136:139]\n\t" \
  "s_waitcnt vmcnt(3)\n\t" \
  "v_mfma_f32_16x16x32_bf16 v[132:135], v[100:103], v[196:199], v[132:135]\n\t" \
  "v_mfma_f32_16x16x32_bf16 v[140:143], v[108:111], v[196:199], v[140:143]\n\t" \
  "s_waitcnt vmcnt(2)\n\t" \
  "v_mfma_f32_16x16x32_bf16 v[128:131], v[112:115], v[200:203], v[128:131]\n\t" \
  "v_mfma_f32_16x16x32_bf16 v[136:139], v[120:123], v[200:203], v[136:139]\n\t" \
  "s_waitcnt vmcnt(1)\n\t" \
  "v_mfma_f32_16x16x32_bf16 v[132:135], v[116:119], v[204:207], v[132:135]\n\t" \
  "v_mfma_f32_16x16x32_bf16 v[140:143], v[124:127], v[204:207], v[140:143]\n\t" \
  "s_nop 6\n\t" \
  "v_pk_add_f32 v[128:129], v[128:129], v[132:133]\n\t" \
  "v_pk_add_f32 v[130:131], v[130:131], v[134:135]\n\t" \
  "ds_write_b128 v230, v[128:131] offset:0\n\t" \
  "v_pk_add_f32 v[136:137], v[136:137], v[140:141]\n\t" \
  "v_pk_add_f32 v[138:139], v[138:139], v[142:143]\n\t" \
  "ds_write_b128 v230, v[136:139] offset:1024\n\t" \
  "s_waitcnt vmcnt(0) lgkmcnt(0)\n\t" \
  "s_branch .Lend%=\n\t" \
  ".Ltw%=:\n\t" \
  "s_add_u32 s92, s92, 1\n\t" \
  "s_sleep 2\n\t" \
  "s_and_b32 s93, s92, 63\n\t" \
  "s_cbranch_scc1 .Ltr%=\n\t" \
  "buffer_load_dword v164, off, %[rs], %[sab] sc1\n\t" \
  "s_waitcnt vmcnt(0)\n\t" \
  "v_readfirstlane_b32 s93, v164\n\t" \
  "s_nop 3\n\t" \
  "s_cmp_eq_u32 s93, 1\n\t" \
  "s_cbranch_scc1 .Lto%=\n\t" \
  "s_cmp_lt_u32 s92, 0x80000\n\t" \
  "s_cbranch_scc1 .Ltr%=\n\t" \
  "v_mov_b32 v164, 1\n\t" \
  "buffer_store_dword v164, off, %[rs], %[sab] sc1\n\t" \
  "s_waitcnt vmcnt(0)\n\t" \
  "s_branch .Lto%=\n\t" \
  ".Ltr%=:\n\t" \
  "buffer_load_dword v166, v242, %[rs], 0 offen sc1\n\t" \
  "s_waitcnt vmcnt(0)\n\t" \
  "s_branch .Ltc%=\n\t" \
  ".Llast%=:\n\t" \
  "v_mov_b32 v173, v242\n\t" \
  "s_branch .Lgol%=\n\t" \
  ".Lfw%=:\n\t" \
  "s_add_u32 s92, s92, 1\n\t" \
  "s_sleep 2\n\t" \
  "s_and_b32 s93, s92, 63\n\t" \
  "s_cbranch_scc1 .Lfr%=\n\t" \
  "buffer_load_dword v164, off, %[rs], %[sab] sc1\n\t" \
  "s_waitcnt vmcnt(0)\n\t" \
  "v_readfirstlane_b32 s93, v164\n\t" \
  "s_nop 3\n\t" \
  "s_cmp_eq_u32 s93, 1\n\t" \
  "s_cbranch_scc1 .Lfo%=\n\t" \
  "s_cmp_lt_u32 s92, 0x80000\n\t" \
  "s_cbranch_scc1 .Lfr%=\n\t" \
  "v_mov_b32 v164, 1\n\t" \
  "buffer_store_dword v164, off, %[rs], %[sab] sc1\n\t" \
  "s_waitcnt vmcnt(0)\n\t" \
  "s_branch .Lfo%=\n\t" \
  ".Lfr%=:\n\t" \
  "buffer_load_dwordx2 v[166:167], v173, %[rs], 0 offen sc1\n\t" \
  "s_branch .Lfc%=\n\t" \
  ".Lb1%=:\n\t" \
  "s_waitcnt vmcnt(0) lgkmcnt(0)\n\t" \
  "s_barrier\n\t" \
  "s_branch .Lb2%=\n\t" \
  ".Lbl%=:\n\t" \
  "s_bitset1_b64 %[ps], %[step]\n\t" \
  "s_cmp_lt_u32 s92, 3\n\t" \
  "s_cbranch_scc1 .Lbm%=\n\t" \
  "s_sleep 12\n\t" \
  ".Lbm%=:\n\t" \
  "s_add_u32 s92, s92, 1\n\t" \
  "s_sleep 2\n\t" \
  "s_and_b32 s93, s92, 63\n\t" \
  "s_cbranch_scc1 .Lba%=\n\t" \
  "buffer_load_dword v164, off, %[rs], %[sab] sc1\n\t" \
  "s_waitcnt vmcnt(0)\n\t" \
  "v_readfirstlane_b32 s93, v164\n\t" \
  "s_nop 3\n\t" \
  "s_cmp_eq_u32 s93, 1\n\t" \
  "s_cbranch_scc1 .Lgo%=\n\t" \
  "s_cmp_lt_u32 s92, 0x80000\n\t" \
  "s_cbranch_scc1 .Lba%=\n\t" \
  "v_mov_b32 v164, 1\n\t" \
  "buffer_store_dword v164, off, %[rs], %[sab] sc1\n\t" \
  "s_waitcnt vmcnt(0)\n\t" \
  "s_branch .Lgo%=\n\t" \
  ".Lba%=:\n\t" \
  "buffer_load_dwordx2 v[166:167], v173, %[rs], 0 offen sc1\n\t" \
  "s_branch .Lcal%=\n\t" \
  ".Lslow%=:\n\t" \
  "s_mov_b64 s[94:95], 0\n\t" \
  "v_cmp_eq_u32 vcc, %[sent], v128\n\t" \
  "s_or_b64 s[94:95], s[94:95], vcc\n\t" \
  "v_cmp_eq_u32 vcc, %[sent], v129\n\t" \
  "s_or_b64 s[94:95], s[94:95], vcc\n\t" \
  "v_cmp_eq_u32 vcc, %[sent], v130\n\t" \
  "s_or_b64 s[94:95], s[94:95], vcc\n\t" \
  "v_cmp_eq_u32 vcc, %[sent], v131\n\t" \
  "s_or_b64 s[94:95], s[94:95], vcc\n\t" \
  "v_cmp_eq_u32 vcc, %[sent], v132\n\t" \
  "s_or_b64 s[94:95], s[94:95], vcc\n\t" \
  "v_cmp_eq_u32 vcc, %[sent], v133\n\t" \
  "s_or_b64 s[94:95], s[94:95], vcc\n\t" \
  "v_cmp_eq_u32 vcc, %[sent], v134\n\t" \
  "s_or_b64 s[94:95], s[94:95], vcc\n\t" \
  "v_cmp_eq_u32 vcc, %[sent], v135\n\t" \
  "s_or_b64 s[94:95], s[94:95], vcc\n\t" \
  "v_cmp_eq_u32 vcc, %[sent], v136\n\t" \
  "s_or_b64 s[94:95], s[94:95], vcc\n\t" \
  "v_cmp_eq_u32 vcc, %[sent], v137\n\t" \
  "s_or_b64 s[94:95], s[94:95], vcc\n\t" \
  "v_cmp_eq_u32 vcc, %[sent], v138\n\t" \
  "s_or_b64 s[94:95], s[94:95], vcc\n\t" \
  "v_cmp_eq_u32 vcc, %[sent], v139\n\t" \
  "s_or_b64 s[94:95], s[94:95], vcc\n\t" \
  "v_cmp_eq_u32 vcc, %[sent], v140\n\t" \
  "s_or_b64 s[94:95], s[94:95], vcc\n\t" \
  "v_cmp_eq_u32 vcc, %[sent], v141\n\t" \
  "s_or_b64 s[94:95], s[94:95], vcc\n\t" \
  "v_cmp_eq_u32 vcc, %[sent], v142\n\t" \
  "s_or_b64 s[94:95], s[94:95], vcc\n\t" \
  "v_cmp_eq_u32 vcc, %[sent], v143\n\t" \
  "s_or_b64 s[94:95], s[94:95], vcc\n\t" \
  "s_cbranch_scc0 .Lok%=\n\t" \
  "s_bitset1_b64 %[fm], %[step]\n\t" \
  "s_add_u32 s92, s92, 1\n\t" \
  "s_sleep 1\n\t" \
  "s_and_b32 s93, s92, 63\n\t" \
  "s_cbranch_scc1 .Lre%=\n\t" \
  "buffer_load_dword v164, off, %[rs], %[sab] sc1\n\t" \
  "s_waitcnt vmcnt(0)\n\t" \
  "v_readfirstlane_b32 s93, v164\n\t" \
  "s_nop 3\n\t" \
  "s_cmp_eq_u32 s93, 1\n\t" \
  "s_cbranch_scc1 .Lok%=\n\t" \
  "s_cmp_lt_u32 s92, 0x80000\n\t" \
  "s_cbranch_scc1 .Lre%=\n\t" \
  "v_mov_b32 v164, 1\n\t" \
  "buffer_store_dword v164, off, %[rs], %[sab] sc1\n\t" \
  "s_waitcnt vmcnt(0)\n\t" \
  "s_branch .Lok%=\n\t" \
  ".Lre%=:\n\t" \
  "buffer_load_dwordx4 v[128:131], v234, %[rs], %[so] offen sc1\n\t" \
  "s_nop 15\n\t" \
  "s_nop 5\n\t" \
  "buffer_load_dwordx4 v[132:135], v235, %[rs], %[so] offen sc1\n\t" \
  "s_nop 15\n\t" \
  "s_nop 5\n\t" \
  "buffer_load_dwordx4 v[136:139], v236, %[rs], %[so] offen sc1\n\t" \
  "s_nop 15\n\t" \
  "s_nop 5\n\t" \
  "buffer_load_dwordx4 v[140:143], v237, %[rs], %[so] offen sc1\n\t" \
  "v_mov_b64 v[144:145], 0\n\t" \
  "v_mov_b64 v[146:147], 0\n\t" \
  "v_mov_b64 v[148:149], 0\n\t" \
  "v_mov_b64 v[150:151], 0\n\t" \
  "v_mov_b64 v[152:153], 0\n\t" \
  "v_mov_b64 v[154:155], 0\n\t" \
  "v_mov_b64 v[156:157], 0\n\t" \
  "v_mov_b64 v[158:159], 0\n\t" \
  "s_waitcnt vmcnt(0)\n\t" \
  "s_branch .Lw%=\n\t" \
  ".Lend%=:\n\t"


__device__ __forceinline__ int look_prog(__amdgpu_buffer_rsrc_t rs, int off) {
  asm volatile("" ::: "memory");
  const int v = (int)ld_flag(rs, off);
  return min(__builtin_amdgcn_readlane(v, 0), __builtin_amdgcn_readlane(v, 1));
}
__device__ __forceinline__ int wait_prog(__amdgpu_buffer_rsrc_t rs, int off, int need, gu32* abortw) {
  int v;
  for (unsigned spins = 0;;) {
    v = look_prog(rs, off);
    if (v >= need) break;
    __builtin_amdgcn_s_sleep(4);
    if (spin_fail(spins, abortw)) break;
  }
  asm volatile("" ::: "memory");
  return v;
}
#define CLB_S "s90", "s91", "s92", "s93", "s94", "s95", "vcc", "scc", "memory"
#define W_IN "{v[0:31]}"(W[0]), "{v[32:63]}"(W[1]), "{v[64:95]}"(W[2]), "{v[96:127]}"(W[3]), "{v[128:159]}"(W[4]), "{v[160:191]}"(W[5])

#define LEAD0 408
#define LEAD_N0 5
#define LEAD_SH 2
#define LEAD_X0 8
#define LEAD_U0 24
#define LEAD_D0 0
#define T0_BACK 1000
#define QBJ 3
constexpr int L_MEM = 0, L_KT = 16384, L_QX = 24576, L_KX = 26624, L_VX = 16384, L_MISC = 30720, L_WL = 32768;

#define QK_STEP(TXT)                                                                                                        \
  asm volatile(TXT                                                                                                          \
      : [tout] "=&s"(tout), [spins] "=&s"(spins), [tb1] "=&s"(tb1), [tgt] "=&s"(tgt), "+{v248}"(aL16), "+{v249}"(aMW), "+{v250}"(aWL),                            \
        "+{v251}"(aIDX), "+{v252}"(aQW), "+{v[232:235]}"(QF)                                                                 \
      : W_IN, [rs] "s"(rs4), [som] "s"(o_mem(pm, s) + WV * 1024), [som2] "s"(o_mem(pm, s) + WV * 1024 + 16384), [sent] "s"(DSENT), [sab] "s"(SY_ABORT), \
        [lead] "s"(lead), [step] "s"(s), [sbc] "s"(SY_BA + side(pm, 1) + h * 128), [nh] "s"(0xFF00FF00FF00FF00ull), [up] "s"(0xFFFFFFFF00000000ull), [negc] "s"(-cexp),      \
        [c] "s"(cexp), [so] "s"(o_o(pm, s, h) + WV * 512), [sk] "s"(o_v(pm, s, h) + 2048 + (WV & 3) * 512),                    \
        [sv] "s"(o_v(pm, s, h) + WV * 512), [rt] "s"(rt4), [svt] "s"(t_off(p, T_V + kseg(s, h)) + WV * 2048)                  \
      : CLB8(192, 193, 194, 195, 196, 197, 198, 199), CLB8(200, 201, 202, 203, 204, 205, 206, 207),                          \
        CLB8(208, 209, 210, 211, 212, 213, 214, 215), CLB8(216, 217, 218, 219, 220, 221, 222, 223),                          \
        CLB8(224, 225, 226, 227, 228, 229, 230, 231), CLB8(236, 237, 238, 239, 240, 241, 242, 243), "v244", "v245", "v246", "v247", "v253", "s88", "s89", CLB_S)

template <int WV>
__device__ __forceinline__ void qk_wave(Args& p, int h, char* smem) {
  const int tid = threadIdx.x;
  unsigned pm = 0;
  int have = -1;
  u32x32 W[6];
  {
    int lane = tid & 63;
    asm volatile("" : "+v"(lane));
    const unsigned vo = (WV * 4 + (lane >> 4)) * (HD * 2) + 16 * ((lane & 15) ^ swzB((WV * 4 + (lane >> 4)) & 15));
    auto stage = [&](int j) {
#pragma unroll
      for (int mm = 0; mm < 2; ++mm) {
        const char* src = (const char*)((mm ? p.Wk : p.Wq) + (size_t)j * 128 * HD + h * DH);
        const unsigned dst = lds_off(smem) + (~j & 1) * 65536 + mm * 32768 + WV * 1024;
        glds2(src, 32 * HD * 2, vo, dst);
        if (TH_Q < 63) HOLD_V((h & 3) ^ 1, TH_Q);
        glds2(src + 64 * HD * 2, 32 * HD * 2, vo, dst + 16384);
        if (TH_Q < 63) HOLD_V((h & 3) ^ 1, TH_Q);
      }
    };
    bf16x8 keep[4][4];
    int f0 = -1;
    auto place = [&](int g) {
#pragma unroll
      for (int i = 0; i < 4; ++i)
        *reinterpret_cast<bf16x8*>(smem + (g == 0 ? 65536 : g == 1 ? 32768 : g == 2 ? 131072 : 98304) + WV * 4096 + i * 1024 + lane * 16) = keep[g][i];
    };
    stage(0); stage(1);
    Boot bt;
#pragma unroll
    for (int j = 0; j < 8; ++j) {
      if (j < 7) WAIT_V(8); else WAIT_V(0);
      if (WV == 7 && j == QBJ + 1) pm = boot_done(p.ws, bt);
      if ((WV == 4 || WV == 5) && j == 7) {
        asm volatile("" : "+v"(f0));
        have = min(__builtin_amdgcn_readlane(f0, 0), __builtin_amdgcn_readlane(f0, 1));
      }
      LBAR();
#pragma unroll
      for (int k = 0; k < 8; ++k) {
        const int P = (QK_MAP[WV] >> (4 * (2 * j + (k >> 2)))) & 15;
        const bf16x8 fr = ld_bfrag(smem + (~j & 1) * 65536 + (WV >> 2) * 32768 + (k >> 2) * 16384, k & 1, 2 * (WV & 3) + ((k >> 1) & 1), lane);
        const int f = P * 4 + (k & 3);
        if (P < 12) put4(W[f >> 3], f & 7, fr);
        else keep[P - 12][k & 3] = fr;
      }
      if (j == (WV >> 1)) place(2);
      LBAR();
      if (WV == 7 && j == QBJ) boot_ask(p.ws, bt);
      if (j + 2 < 8) stage(j + 2);
      if (j == 6) {
        if (WV == 4 || WV == 5)
          asm volatile("buffer_load_dword %0, %1, %2, 0 offen sc1" : "=&v"(f0) : "v"(fkq(h, (lane & 1) * 2)), "s"(raw_rsrc(p.ws + WS_SY, SY_TOTAL)) : "memory");
        place(0); place(3);
      }
    }
    place(1);
    if (WV == 7 && lane < 8) lds_at(smem + L_MISC)[lane] = lane == 7 ? pm : (lane == 1 ? now_q() - T0_BACK : 0);
    if (WV == 6) lds_at(smem + 28672)[lane] = ~0u;
    LBAR();
    pm = uni(lds_at(smem + L_MISC)[7]);
    p.s0 = pm >> 31;
    if (WV == 4 || WV == 5) {
      if (have < 0) have = wait_prog(mkrsrc(p.ws + WS_SY, SY_TOTAL), fkq(h, (lane & 1) * 2), 0, (gu32*)(p.ws + WS_SY + SY_ABORT));
      const char* src = p.ws + WS_T + t_off(p, kseg(0, h)) + (WV - 4) * 4096 + lane * 16;
#pragma unroll
      for (int i = 0; i < 4; ++i) glds16_sc1(src + i * 1024, smem + L_KT + (WV - 4) * 4096 + i * 1024);
    }
  }
  const __amdgpu_buffer_rsrc_t rsy = mkrsrc(p.ws + WS_SY, SY_TOTAL);
  const u32x4 rs4 = raw_rsrc(p.ws + WS_SY, SY_TOTAL);
  const u32x4 rt4 = raw_rsrc(p.ws + WS_T, T_BYTES);
  gu32* abortw = (gu32*)(p.ws + WS_SY + SY_ABORT);
  const float cexp = 0.08838834764831845f * 1.4426950408889634f;
  constexpr int m = WV >> 2, b = WV & 3;
  int lead = LEAD0;
  unsigned tb1 = 0;
  unsigned long long fm = 0, ps = 0; (void)fm; (void)ps;
  for (int s = 0; s < NSEG; ++s) {
    int lane = tid & 63;
    asm volatile("" : "+v"(lane));
    const int r = lane & 15, q = lane >> 4;
    unsigned aL16 = lane * 16, aMW = aL16 + WV * 1024, aWL = aL16 + 98304 + WV * 4096;
    unsigned aIDX = (lane & 2) ? 0xEEEE : 0x4444;
    unsigned aQW = (32 * b + r) * 16 + q * 4 + (m ? L_KX : L_QX);
    u32x4 QF;
    asm volatile("" : "={v[232:235]}"(QF));
    if (WV < 4) {
      QF[0] = L_QX + (q * 8 + (r >> 2)) * 16 + (r & 1) * 8;
      QF[1] = ((2 * (q & 1) + (q >> 1)) * 16 + r) * 4;
      QF[2] = ((r >> 2) & 1) * 65536 + ((r >> 1) & 1) * 64 + (r >> 3) * 32 + (r & 1) * 16 + (q >> 1) * 256 + (q & 1) * 8;
    } else QF[3] = r * 16 + q * 4;
    unsigned tout, spins, tgt;
    if (WV == 0) QK_STEP(QK_ASM_0); else if (WV == 1) QK_STEP(QK_ASM_1); else if (WV == 2) QK_STEP(QK_ASM_2);
    else if (WV == 3) QK_STEP(QK_ASM_3); else if (WV == 4) QK_STEP(QK_ASM_4); else if (WV == 5) QK_STEP(QK_ASM_5);
    else if (WV == 6) QK_STEP(QK_ASM_6); else QK_STEP(QK_ASM_7);
    tout = __builtin_amdgcn_readfirstlane(tout);
    tb1 = __builtin_amdgcn_readfirstlane(tb1);
    lane = tid & 63;
    asm volatile("" : "+v"(lane));
    if (WV < 4) {
      lds_at(smem + L_MISC + 8)[WV] = s + 1;
      if (WV == 3) { lds_at(smem + L_MISC)[6] = time_of(tout); lds_at(smem + 28672)[2] = s; }
    }
    if (WV == 6) {
      if (s + 1 < NSEG) watch(rsy, s, lane, now_q(), (const unsigned*)(smem + L_MISC), abortw, side(pm, 1));
    } else if (WV >= 4 && s + 1 < NSEG) {
      unsigned t;
      do {
        __builtin_amdgcn_s_sleep(1);
        t = lds_at(smem + L_MISC + 8)[lane & 3];
      } while (!__all(t == (unsigned)(s + 1)));
      if (WV <= 5) {
        if (s + 2 < NSEG) {
          if (have < s + 1) have = wait_prog(rsy, fkq(h, (lane & 1) * 2), s + 1, abortw);
          const char* src = p.ws + WS_T + t_off(p, kseg(s + 1, h)) + (WV - 4) * 4096 + lane * 16;
#pragma unroll
          for (int i = 0; i < 4; ++i) glds16_sc1(src + i * 1024, smem + L_KT + (WV - 4) * 4096 + i * 1024);
          if (have < s + 2) have = look_prog(rsy, fkq(h, (lane & 1) * 2));
        }
      } else {
        do __builtin_amdgcn_s_sleep(2); while (__builtin_amdgcn_readfirstlane(lds_at(smem + 28672)[2]) != (unsigned)s);
        const int x = (int)(__builtin_amdgcn_readfirstlane(lds_at(smem + L_MISC)[6]) - tb1);
        lead = __builtin_amdgcn_readfirstlane(track(lead, x + (s < LEAD_N0 ? LEAD_X0 : 0), s < LEAD_N0 ? LEAD_U0 : LEAD_UP, s < LEAD_N0 ? LEAD_D0 : (s < 3 * LEAD_N0 ? min(max(LEAD_DN, (lead - x) >> LEAD_SH), 16) : LEAD_DN), 60));
      }
    }
  }
}

__device__ __forceinline__ void qk_role(Args& p, int h, char* smem) {
  switch (__builtin_amdgcn_readfirstlane(threadIdx.x >> 6)) {
    case 0: qk_wave<0>(p, h, smem); break;
    case 1: qk_wave<1>(p, h, smem); break;
    case 2: qk_wave<2>(p, h, smem); break;
    case 3: qk_wave<3>(p, h, smem); break;
    case 4: qk_wave<4>(p, h, smem); break;
    case 5: qk_wave<5>(p, h, smem); break;
    case 6: qk_wave<6>(p, h, smem); break;
    default: qk_wave<7>(p, h, smem); break;
  }
}

#define K_DUE 50
#define K_DUE2 500
#define T_SL 2
#define O_DUE 400

constexpr int V_VM = 16384, V_T = 32768, V_KI = 131072;
#define TK_CALL(sg, od_) \
  asm volatile(TK_ASM \
            : \
            : "{v200}"(lane * 16), "{v[208:215]}"(bias), "{v[216:219]}"(ta), \
              [rs] "s"(rs4), [rt] "s"(raw_rsrc(p.ws + WS_T, T_BYTES)), [sent] "s"(DSENT), [sab] "s"(SY_ABORT), [sk] "s"(o_v(pm, sg, h) + 2048), \
              [tl] "s"(V_T + (sg & 3) * 24576), [vp] "s"((sg & 1) * 2048), [tq] "s"((w & 1) * 4096), [tv] "s"((w & 2) * 2048), \
              [so] "s"(t_off_o(p, T_O + (sg * 2 + (w & 1)) * 16 + (h >> 1)) + (h & 1) * 4096 + (w & 2) * 1024), \
              [up] "s"(0xFFFFFFFF00000000ull), [c] "s"(1.4426950408889634f), [negc] "s"(-1.4426950408889634f), \
              [sc] "s"((unsigned long long)__float_as_uint(0.08838834764831845f)), [od] "s"(od_) \
            : CLB32(128), CLB32(160), CLB8(192, 193, 194, 195, 196, 197, 198, 199), CLB8(224, 225, 226, 227, 228, 229, 230, 231), \
              "v205", "v206", "v207", "s88", "s89", CLB_S)
__device__ __forceinline__ void vu_role(Args& p, int h, char* smem) {
  const int tid = threadIdx.x, lane = tid & 63;
  const int w = __builtin_amdgcn_readfirstlane(tid >> 6);
  u32x32 W[4];
  {
    const unsigned vo = (tid >> 4) * (HD * 2) + 16 * ((lane & 15) ^ swzB((tid >> 4) & 15));
    auto stage = [&](int j) {
      const char* src = (const char*)(p.Wv + (size_t)j * 128 * HD + h * DH);
      const unsigned dst = lds_off(smem) + L_WL + (j & 3) * 32768 + w * 1024;
      glds2(src, 32 * HD * 2, vo, dst);
      glds2(src + 64 * HD * 2, 32 * HD * 2, vo, dst + 16384);
    };
    start_sy(p.ws, p.mem0, h, smem);
    Boot bt;
#pragma unroll
    for (int u = 0; u < 2; ++u) {
#pragma unroll
      for (int j = 0; j < 4; ++j) stage(4 * u + j);
      if (u && w == 7) boot_ask(p.ws, bt);
      WAIT_V(0);
      if (u && w == 7) {
        const unsigned m = boot_done(p.ws, bt);
        if (lane < 8) lds_at(smem + L_MISC)[lane] = lane == 7 ? m : (lane == 1 ? now_q() : 0);
      }
      LBAR();
#pragma unroll
      for (int k = 0; k < 16; ++k) put4(W[2 * u + (k >> 3)], k & 7, ld_bfrag(smem + L_WL + ((k >> 1) ^ w) * 16384, k & 1, w, lane));
      LBAR();
    }
  }
  const __amdgpu_buffer_rsrc_t rsy = mkrsrc(p.ws + WS_SY, SY_TOTAL);
  const u32x4 rs4 = raw_rsrc(p.ws + WS_SY, SY_TOTAL);
  gu32* abortw = (gu32*)(p.ws + WS_SY + SY_ABORT);
  const unsigned pm = uni(lds_at(smem + L_MISC)[7]);
  p.s0 = pm >> 31;
  unsigned long long fm = 0; (void)fm;
  int have = -1;
  auto fetch = [&](int sg) {
    if (have < sg) have = wait_prog(rsy, fkq(h, w - 4), sg, abortw);
    const char* src = p.ws + WS_T + t_off(p, (w - 4) * T_Q + kseg(sg, h)) + lane * 16;
#pragma unroll
    for (int i = 0; i < 8; ++i) glds16_sc1(src + i * 1024, smem + V_T + (sg & 3) * 24576 + (w - 4) * 8192 + i * 1024);
  };
  const int fo = SY_FO + ((w & 3) * NH + h) * 128;
  typedef __attribute__((ext_vector_type(8))) float f32x8;
  f32x8 bias;
  typedef __attribute__((ext_vector_type(8))) unsigned u32x8;
  u32x8 ca;
#pragma unroll
  for (int i = 0; i < 8; ++i) ca[i] = lane * 16 + (i ^ w) * 1024;
  u32x4 ta;
  {
    const int r = lane & 15, q = lane >> 4;
#pragma unroll
    for (int j = 0; j < 8; ++j) bias[j] = (j >> 2) * 16 + 4 * q + (j & 3) <= 16 * (w & 1) + r ? INFINITY : -INFINITY;
    ta[0] = V_KI + w * 2048 + lane * 16;
    ta[1] = V_KI + w * 2048 + (q * 8 + (r >> 2)) * 16 + (r & 1) * 8;
    ta[2] = V_VM + (w & 2) * 512 + ((2 * (q & 1) + (q >> 1)) * 16 + r) * 4;
    ta[3] = 0;
  }
#pragma unroll 1
  for (int s = 0; s < NSEG; ++s) {
    unsigned spins, tgt;
    asm volatile(VV_ASM
        : [spins] "=&s"(spins), [tgt] "=&s"(tgt)
        : W4_IN, "{v200}"(lane * 16), "{v[232:239]}"(ca), "{v202}"((lane & 2) ? 0xEEEE : 0x4444), "{v203}"(lane * 4),
          "{v204}"(tid * 4 + V_VM + (s & 1) * 2048), [rs] "s"(rs4), [som] "s"(o_mem(pm, s) + w * 1024), [som2] "s"(o_mem(pm, s) + w * 1024 + 16384), [step] "s"(s),
          [sent] "s"(DSENT), [sab] "s"(SY_ABORT), [sv] "s"(o_v(pm, s, h) + w * 256)
        : CLB32(128), CLB32(160), CLB8(192, 193, 194, 195, 196, 197, 198, 199), CLB_S);
    if (w < 4 || s == NSEG - 1) {
      const int sg = s - (w < 4);
      if (w >= 4) {
        if (lane == 0) __builtin_amdgcn_raw_buffer_store_b32(1u, rsy, fo + 4 * NH * 128, 0, 16);
        pace_until(now_q() + K_DUE);
      }
      if (sg >= 0 && sg != NSEG - 2) {
        TK_CALL(sg, s < NSEG - 1 ? O_DUE : 0);
        if (sg >= NSEG - 3) {
          static_assert(TLAG == 2, "");
          WAIT_V(0);
          if (lane == 0) __builtin_amdgcn_raw_buffer_store_b32(1u, rsy, fo + (sg - NSEG + 3) * 4 * NH * 128, 0, 16);
        }
      }
    } else if (w == 7) {
      watch(rsy, s, lane, now_q(), (const unsigned*)(smem + L_MISC), abortw, side(pm, 1));
    } else if (w >= 4) {
      __builtin_amdgcn_s_sleep(T_SL);
      const int s1 = w == 5 && s < NSEG - 3 ? s : s + 1;
#pragma unroll 1
      for (int sg = (w == 5 ? s < NSEG - 2 : s == 0) ? s : s + 1; sg <= s1; ++sg) fetch(sg);
      if (have < s1 + 1 && s1 + 1 < NSEG) have = look_prog(rsy, fkq(h, w - 4));
      if (w == 5 && s + NPRE < NSEG) {
#pragma unroll
        for (int i = 0; i < 4; ++i) st_sc1(rsy, o_v(pm, s + NPRE, h) + i * 1024 + lane * 16, (u32x4){DSENT, DSENT, DSENT, DSENT});
      }
    }
    if (s == NSEG - 2 && w >= 4) {
      pace_until(tgt + K_DUE2);
      TK_CALL(s, 0);
    }
  }
}

#define RL_UP 6
#define RL_DN 4
#define RED_MIN 40
#define BA_LOOK 360
constexpr int R_SI = 32768, R_PART = 98304;

__device__ __forceinline__ void red_role(Args& p, int j, char* smem) {
  const int tid = threadIdx.x;
  int lane = tid & 63;
  const int w = __builtin_amdgcn_readfirstlane(tid >> 6);
  const int c = (j & 2) * 8 + (j >> 2), rh = j & 1;
  const int hg = (w + (j >> 2)) & 7, rot = (j >> 2) + (j >> 5);
  const __amdgpu_buffer_rsrc_t rsy = mkrsrc(p.ws + WS_SY, SY_TOTAL);
  const u32x4 rs4 = raw_rsrc(p.ws + WS_SY, SY_TOTAL);
  const __amdgpu_buffer_rsrc_t rt = mkrsrc(p.ws + WS_T, T_BYTES);
  gu32* abortw = (gu32*)(p.ws + WS_SY + SY_ABORT);
  u32x32 W[4];
  u32x2 tag = {0u, 0u};
  {
    const u16* Wo = p.Wo + (size_t)(4 * hg * DH) * DM + 32 * c;
    const int ro = (lane >> 2) * 64 + (lane & 3) * 8;
    Boot bt;
    load_blocks<16>(smem + 32768 + w * 16384,
        [&](int i, char* slot) {
          if (i == 12 && w == 0) boot_ask(p.ws, bt);
          if ((i < 2 || i == 4 || i == 8) && (c & 14) != 6 && lane < 10) {
            const int x = i < 2 ? 3 - i : i >> 3, n = min(((c & 15) - ((c & 8) >> 2)) * 10 + lane, 127);
            touch_line((const char*)p.Wo + (size_t)(w * 512 + (x >> 1) * 256 + (x & 1) * 64 + (n & 64) + n) * (DM * 2) + (c & 16) * 64 + 384, smem + w * 256);
          }
          const int x = ((i >> 2) + rot) & 3;
          blk_dma(Wo + (size_t)((x >> 1) * 256 + (x & 1) * 64 + ((i >> 1) & 1) * 128 + (i & 1) * 32) * DM, DM, slot, lane);
        },
        [&](int i, char* slot) {
#pragma unroll
          for (int t = 0; t < 2; ++t) {
            s16x4 lo = __builtin_amdgcn_ds_read_tr16_b64_v4i16((lds_s16x4_ptr)(slot + ro + t * 32));
            s16x4 hi = __builtin_amdgcn_ds_read_tr16_b64_v4i16((lds_s16x4_ptr)(slot + ro + t * 32 + 1024));
            put4(W[i >> 2], (i & 2) * 2 + 2 * t + (i & 1), (bf16x8){lo[0], lo[1], lo[2], lo[3], hi[0], hi[1], hi[2], hi[3]});
          }
        });
    if (w == 0) { lds_at(smem + 4128)[0] = boot_done(p.ws, bt); tag = next_tag(bt.e); }
    LBAR();
  }
  const unsigned pm = uni(lds_at(smem + 4128)[0]);
  p.s0 = pm >> 31;
  const int hs = side(pm, 1);
  {
    const u32x4 ds = {DSENT, DSENT, DSENT, DSENT};
#pragma unroll
    for (int i = 0; i < 2; ++i) {
      const int s = (w * 2 + i) * 4 + (lane >> 4);
      if (s >= 2) st_sc1(rsy, o_mem(pm, s) + (c >> 4) * 16384 + (c & 15) * 512 + rh * 128 + ((lane >> 3) & 1) * 256 + (lane & 7) * 16, ds);
    }
  }
  if (tid < 4) { reinterpret_cast<unsigned*>(smem + 4096)[tid] = tid == 3 ? now_q() : 0; reinterpret_cast<unsigned*>(smem + 4112)[tid] = DSENT; }
  if (tid < 256) reinterpret_cast<int*>(smem + 5120)[tid] = ((tid >> 2) - TLAG - 6) >> 2;
  LBAR();
  const unsigned base = __builtin_amdgcn_readfirstlane(lds_at(smem + 4096)[3]);
  unsigned long long fm = 0, ps = 0;
  const unsigned aL = lane * 16;
  const int e = ((56 + w) * SEG + rh * 16 + (lane >> 2)) * DM + c * 32 + (lane & 3) * 8;
  u32x4 xv = *reinterpret_cast<const u32x4*>(p.x + e), av = {DSENT, DSENT, DSENT, DSENT};
  asm volatile("" : "+v"(xv));
  {
    const int r = lane & 15, q = lane >> 4;
    const int sm0 = o_mem(pm, 0) + (c >> 4) * 16384 + (c & 15) * 512 + rh * 128;
    u32x4 VO, VT;
    unsigned fo = SY_FO + ((rh + (lane & 4) / 2) * NH + 4 * hg + (lane & 3)) * 128;
    unsigned ao = w == 1 ? (c * S_LEN + rh * 16 - (TLAG + 3) * SEG) * 64 + lane * 16
                         : (w >= 4 ? SY_O + NPRE * 131072 + side(pm, w - 2) + (j >> 3) * 16384 + (j & 7) * 1024 + lane * 16
                                   : SY_FS + (2 * c + rh) * 128 + (lane < 8 ? lane * 16 : 0x70000000));
    unsigned ar = w == 1 ? R_SI - (TLAG + 2) * 1024 + lane * 16 : (w == 2 ? 5120 : 4112);
    const unsigned long long am = w == 1 ? ((1ull << 56) - 1) << (TLAG + 3) : (w == 2 ? 0x11111111111111ull << (TLAG + 7) : (w >= 4 ? (0xFFFFull << (16 * (w - 4))) >> NPRE : 0));
    unsigned pw = R_PART + w * 2048 + lane * 16, pr = R_PART + 16384 + tid * 4;
    unsigned sw = R_SI - (TLAG + 1) * 1024 + (lane >> 2) * 64 + w * 8 + (lane & 3) * 2, stm = 8192 + w * 512;
#pragma unroll
    for (int l = 0; l < 4; ++l) {
      const int x = (l + rot) & 3;
      VO[l] = o_o(0, 0, 4 * hg) + rh * 65536 + x * 1024 + aL;
      VT[l] = (T_O + rh * 16 + 2 * hg + (x >> 1)) * 16384 + (x & 1) * 2048 + aL;
    }
    unsigned ba = lane < 4 ? SY_BA + hs + (4 * hg + lane) * 128 : 0x7F000000u;
    unsigned sm = 0;
    int rl = RL0;
    unsigned step, E, so, st, st4, sh;
    asm volatile(RED_ASM
        : [step] "=&s"(step), [E] "=&s"(E), [so] "=&s"(so), [st] "=&s"(st), [st4] "=&s"(st4), [sh] "=&s"(sh),
          [rl] "+s"(rl), [sm] "+s"(sm), [fm] "+s"(fm), [ps] "+s"(ps),
          "+{v[224:227]}"(av), "+{v228}"(ao), "+{v229}"(ar), "+{v230}"(pw), "+{v231}"(pr), "+{v232}"(sw), "+{v233}"(stm), "+{v242}"(fo), "+{v173}"(ba)
        : W4_IN, "{v168}"(aL), "{v169}"(R4 ? (w >> 1) * 1024 + lane * 16 + (w & 1) * 8 : w * 512 + lane * 8), "{v170}"(lane * 8), "{v174}"((r & 1) ? 0x03020706u : 0x05040100u),
          "{v171}"(sm0 + 32768 + (q >> 1) * 256 + (q & 1) * 64 + ((r >> 1) & 1) * 32 + (r & 1) * 16 + (r >> 2) * 4),
          "{v172}"((lane & 2) ? 0xEEEE : 0x4444),
          "{v175}"(w == 3 && lane < 32 ? SY_BR + hs + j * 128 + lane * 4 : 0x7F000000u), "{v[234:237]}"(VO), "{v[238:241]}"(VT), "{v243}"(e * 2),
          [rg] "s"(raw_rsrc(p.ws + WS_G, S_LEN * DM * 2)), [fg] "s"(SY_FG + ((14 + (w >> 2)) * 8 + (c >> 2)) * 128),
          [rs] "s"(rs4), [ra] "s"(w == 1 ? raw_rsrc(p.ws + WS_S, S_LEN * DM * 2) : rs4), [rt] "s"(raw_rsrc(p.ws + WS_T, T_BYTES)),
          [ds] "n"(DS_RED), [wm] "n"(REF_W), [gm] "n"(REF_G), [mn] "n"(RED_MIN), [lk] "n"(DS_MEM + BA_LOOK), [up] "n"(RL_UP), [dn] "n"(-RL_DN), [sent] "s"(DSENT), [sab] "s"(SY_ABORT),
          [base] "s"(base), [pm] "s"(pm), [s0] "s"((p.s0 ^ TF_O) << 13), [am] "s"(am), [ainc] "s"(w == 1 ? SEG * 64 : (w >= 4 ? 131072 : 0)),
          [rinc] "s"(w == 1 ? 1024 : (w == 2 ? 16 : 0))
        : CLB32(128), CLB8(160, 161, 162, 163, 164, 165, 166, 167), CLB8(176, 177, 178, 179, 180, 181, 182, 183),
          CLB8(184, 185, 186, 187, 188, 189, 190, 191), CLB8(192, 193, 194, 195, 196, 197, 198, 199), CLB8(200, 201, 202, 203, 204, 205, 206, 207),
          CLB8(208, 209, 210, 211, 212, 213, 214, 215), CLB8(216, 217, 218, 219, 220, 221, 222, 223), CLB_E, "s96", CLB_S);
  }
  LBAR();
  u32x4 sv;
  if (w < 6 - TLAG) sv = *reinterpret_cast<const u32x4*>(smem + R_SI + (56 + w) * 1024 + lane * 16);
  else {
    const f32x4* part = reinterpret_cast<const f32x4*>(smem + (w == 6 - TLAG ? R_PART : (w - 6 + TLAG) * 16384)) +
                        (lane & 2) * 32 + (lane & 1) * 32 + (lane >> 2);
    f32x4 x[8][2];
#pragma unroll
    for (int ww = 0; ww < 8; ++ww) { x[ww][0] = part[ww * 128]; x[ww][1] = part[ww * 128 + 16]; }
#pragma unroll
    for (int u = 0; u < 2; ++u) {
      const f32x4 a = ((x[0][u] + x[1][u]) + (x[2][u] + x[3][u])) + ((x[4][u] + x[5][u]) + (x[6][u] + x[7][u]));
      sv[2 * u] = cvtpk(a[0], a[1]); sv[2 * u + 1] = cvtpk(a[2], a[3]);
    }
  }
  st_out(p, e, combine(sv, av, xv));
  if (j == 0 && tid == 0) st2_sc1(rsy, SY_EP, tag);
}


__global__ __launch_bounds__(512, 2) void main_kernel(Args p) {
  char* smem = g_smem;
  p.ws = ws_align(p.ws);
  const int bx = blockIdx.x & 7, bk = blockIdx.x >> 3;
  const bool isg = (bx ^ XPROBE) & 4;
  const int b = (isg ? NCHAIN : 0) + bk * 4 + (bx & 3);
  if (threadIdx.x < 64) {
    unsigned long long pc; unsigned len, keep;
    asm volatile("s_getpc_b64 %0\n.Lpc%=:\n\ts_mov_b32 %1, .Lcode_end-.Lpc%=" : "=s"(pc), "=s"(len));
    asm volatile("s_mov_b32 %0, m0\n\ts_mov_b32 m0, %3\n\ts_nop 0\n\tbuffer_load_dword %1, %2, 0 offen lds\n\ts_mov_b32 m0, %0"
                 : "=&s"(keep) : "v"((bk * 40 + threadIdx.x) * 128), "s"(raw_rsrc((const void*)pc, len)), "s"((b >> 6) == 1 ? 2048 : 163328) : "memory");
  }
  if ((bk >> 3) == (isg ? 3 : 1) && (threadIdx.x >> 6) == 1) {
    constexpr int NI = TLB_NI + 1, NW = (int)(WS_END / TLB_WS);
    const int n = (threadIdx.x & 63) * 8 + (bk & 7), m = n / NI, i = n % NI;
    const u16* q = m == 0 ? p.Wq : m == 1 ? p.Wk : m == 2 ? p.Wv : m == 3 ? p.Wo : m == 4 ? p.wg : m == 5 ? p.wu : m == 6 ? p.wd : m == 7 ? p.x : p.out;
    static_assert(9 * NI + NW <= 512, "");
    if (n < 9 * NI + NW)
      touch_line(m < 9 ? (const char*)q + min(i * (DM * HD * 2 / TLB_NI), (m < 4 ? HD : m < 7 ? FF : S_LEN) * DM * 2 - 128)
                       : p.ws + (size_t)(n - 9 * NI + 1) * TLB_WS - 16384, smem + 163072);
  }
  if (b >= NCHAIN) gemm_role(p, b - NCHAIN, smem);
  else if (b < 32) qk_role(p, (b & 3) * 8 + bk, smem);
  else if (b < 64) vu_role(p, (b & 3) * 8 + bk - 8, smem);
  else red_role(p, b - 64, smem);
}

extern "C" void kernel_launch(void* const* d_in, const int* in_sizes, int n_in,
                              void* d_out, int out_size, void* d_ws, size_t ws_size,
                              hipStream_t stream) {
  Args a;
  a.x = (const u16*)d_in[0]; a.mem0 = (const u16*)d_in[1];
  a.Wq = (const u16*)d_in[2]; a.Wk = (const u16*)d_in[3]; a.Wv = (const u16*)d_in[4]; a.Wo = (const u16*)d_in[5];
  a.wg = (const u16*)d_in[8]; a.wu = (const u16*)d_in[9]; a.wd = (const u16*)d_in[10];
  a.ws = (char*)d_ws; a.out = (u16*)d_out; a.s0 = 0; a.pad1 = 0;
  main_kernel<<<256, 512, 0, stream>>>(a);
}
```

```cpp
#include <hip/hip_runtime.h>
#include <hip/hip_bf16.h>
#include <stdint.h>

constexpr int S_LEN = 2048, DM = 1024, NH = 32, DH = 128, NMEM = 8, SEG = 32;
constexpr int NSEG = S_LEN / SEG;
constexpr int HD = NH * DH;
constexpr int FF = 3584;

typedef unsigned short u16;
typedef __attribute__((ext_vector_type(8))) short bf16x8;
typedef __attribute__((ext_vector_type(4))) short s16x4;
typedef __attribute__((ext_vector_type(4))) float f32x4;
typedef __attribute__((ext_vector_type(4))) unsigned int u32x4;
typedef __attribute__((ext_vector_type(2))) unsigned int u32x2;
typedef __attribute__((address_space(3))) s16x4* lds_s16x4_ptr;
typedef __attribute__((address_space(3))) void* lds_void_ptr;
typedef __attribute__((address_space(1))) unsigned gu32;
typedef __bf16 bf16x2_t __attribute__((ext_vector_type(2)));
typedef float f32x2_t __attribute__((ext_vector_type(2)));

__device__ __forceinline__ unsigned cvtpk(float lo, float hi) {
  bf16x2_t r = __builtin_convertvector((f32x2_t){lo, hi}, bf16x2_t);
  return __builtin_bit_cast(unsigned, r);
}
__device__ __forceinline__ float lo_f(unsigned v) { return __uint_as_float(v << 16); }
__device__ __forceinline__ float hi_f(unsigned v) { return __uint_as_float(v & 0xffff0000u); }
__device__ __forceinline__ float rbf(float f) { return lo_f(cvtpk(f, f)); }
__device__ __forceinline__ bf16x8 as_bf16x8(u32x4 v) { return __builtin_bit_cast(bf16x8, v); }
#define MFMA(a, b, c) __builtin_amdgcn_mfma_f32_16x16x32_bf16((a), (b), (c), 0, 0, 0)

#define KSTR 65
#define XLD 1024
constexpr int PAGE = 2 << 20, NPAGE = 14;
constexpr int T_K = 0, T_Q = 2304, T_V = 4608, T_O = 6912, T_END = T_O + 2048;
constexpr unsigned T_BYTES = T_END * 16384u;
constexpr size_t WS_SY = 0;
constexpr size_t WS_T = (size_t)NPAGE * PAGE;
constexpr size_t WS_XN = WS_T + T_BYTES;
constexpr size_t WS_HF = WS_XN + (size_t)S_LEN * XLD * 2;
constexpr size_t WS_G = WS_HF + (size_t)S_LEN * 16384;
constexpr size_t WS_S = WS_G + (size_t)S_LEN * DM * 2;
constexpr size_t WS_END = WS_S + (size_t)S_LEN * DM * 2;
constexpr unsigned SENT = 0xFFFFFFFFu;
constexpr unsigned DSENT = 0xFFBFFFBFu;
constexpr int SY_MEM = 0;
constexpr int SY_O = 2 * PAGE;
constexpr int SY_V = 6 * PAGE;
constexpr int SY_BR = PAGE, SY_BA = SY_BR + 16384;
constexpr int SY_FKQ = SY_BR + 32768;
constexpr int SY_FG = SY_FKQ + NH * 4 * 128;
constexpr int SY_FO = SY_FG + 16 * 8 * 128;
constexpr int SY_CH = SY_FO + 384 * 128;
constexpr int SY_ABORT = SY_CH + 8 * 128;
constexpr int SY_FS = SY_ABORT + 256;
constexpr int SY_FEND = SY_FS + 64 * 128;
constexpr int SY_EP = SY_FEND, SY_BT = SY_EP + 128;
constexpr int SY_TOTAL = NPAGE * PAGE;
constexpr int PROBE = PAGE - 16384 + 6144;
__device__ __forceinline__ int side(unsigned pm, int page) { return ((pm >> page) & 1u) << 13; }
__device__ __forceinline__ int o_mem(unsigned pm, int s) { return SY_MEM + s * 32768 + side(pm, 0); }
__device__ __forceinline__ int o_o(unsigned pm, int s, int h) {
  return SY_O + s * 131072 + (h >> 3) * 16384 + ((h >> 1) & 3) * 2048 + (h & 1) * 128 + side(pm, 2 + (s >> 4));
}
__device__ __forceinline__ int o_v(unsigned pm, int s, int h) { return SY_V + s * 262144 + (h >> 1) * 16384 + (h & 1) * 4096 + side(pm, 6 + (s >> 3)); }
__device__ __forceinline__ char* ws_align(char* ws) { return ws + ((PAGE - (unsigned)(uintptr_t)ws) & (PAGE - 1)); }
#define NFUSE 12
constexpr int NPRE = 4;
static_assert(WS_END + PAGE <= (size_t)256 << 20 && NH * KSTR <= T_Q && NPAGE % 2 == 0, "");
constexpr int NCHAIN = 128;
__host__ __device__ constexpr int kseg(int sg, int h) { return h * KSTR + sg; }
__host__ __device__ constexpr int fkq(int h, int kind) { return SY_FKQ + (h * 4 + kind) * 128; }

#define TLB_NI 16
#define TLB_WS (2 << 20)
#define XPROBE 0
struct Args {
  const u16 *x, *mem0, *Wq, *Wk, *Wv, *Wo, *wg, *wu, *wd;
  char* ws;
  u16* out;
  int s0, pad1;
};
__device__ __forceinline__ int h_off(const Args& p, int rblk) { return rblk * PAGE + (((p.s0 ^ rblk ^ 1) & 1) << 13); }
static_assert(WS_HF % (2 * PAGE) == 0, "");
#define TF_KQV 1
#define TF_O 0
__device__ __forceinline__ int t_off(const Args& p, int t) { return t * 16384 + (((p.s0 ^ (t >> 7) ^ (TF_KQV == 2 ? t : TF_KQV)) & 1) << 13); }
__device__ __forceinline__ int t_off_o(const Args& p, int t) { return t * 16384 + (((p.s0 ^ (t >> 7) ^ (TF_O == 2 ? t : TF_O)) & 1) << 13); }

__device__ __forceinline__ float wave_sum(float v) {
#pragma unroll
  for (int i = 0; i < 4; ++i) {
    const int x = (int)__float_as_uint(v);
    v += __uint_as_float(i == 0 ? __builtin_amdgcn_update_dpp(0, x, 0x128, 0xf, 0xf, false) : i == 1 ? __builtin_amdgcn_update_dpp(0, x, 0x124, 0xf, 0xf, false)
                         : i == 2 ? __builtin_amdgcn_update_dpp(0, x, 0x122, 0xf, 0xf, false) : __builtin_amdgcn_update_dpp(0, x, 0x121, 0xf, 0xf, false));
  }
  auto r = __builtin_amdgcn_permlane16_swap(__float_as_uint(v), __float_as_uint(v), false, false);
  v = __uint_as_float(r[0]) + __uint_as_float(r[1]);
  r = __builtin_amdgcn_permlane32_swap(__float_as_uint(v), __float_as_uint(v), false, false);
  return __uint_as_float(r[0]) + __uint_as_float(r[1]);
}

__shared__ __attribute__((aligned(16))) char g_smem[163840];
__device__ __forceinline__ unsigned lds_off(const void* p) {
  return (unsigned)(uintptr_t)(lds_void_ptr)g_smem + (unsigned)((const char*)p - g_smem);
}
__device__ __forceinline__ __amdgpu_buffer_rsrc_t mkrsrc(const void* p, unsigned bytes) {
  return __builtin_amdgcn_make_buffer_rsrc((void*)p, 0, bytes, 0x00020000);
}
__device__ __forceinline__ u32x4 ld_sc1(__amdgpu_buffer_rsrc_t rs, int off) {
  return __builtin_amdgcn_raw_buffer_load_b128(rs, off, 0, 16);
}
__device__ __forceinline__ void st_sc1(__amdgpu_buffer_rsrc_t rs, int off, u32x4 v) {
  __builtin_amdgcn_raw_buffer_store_b128(v, rs, off, 0, 16);
}
__device__ __forceinline__ void swap16(unsigned& x, unsigned& y) {
  auto r = __builtin_amdgcn_permlane16_swap(x, y, false, false);
  x = r[0]; y = r[1];
}
__device__ __forceinline__ void swap32(unsigned& x, unsigned& y) {
  auto r = __builtin_amdgcn_permlane32_swap(x, y, false, false);
  x = r[0]; y = r[1];
}
__device__ __forceinline__ bool spin_fail(unsigned& spins, gu32* abortw) {
  ++spins;
  if ((spins & 63u) == 0u) {
    const __amdgpu_buffer_rsrc_t ra = mkrsrc((const void*)abortw, 4);
    unsigned z = 0;
    asm volatile("" : "+v"(z));
    if (__builtin_amdgcn_readfirstlane(__builtin_amdgcn_raw_buffer_load_b32(ra, z, 0, 16)) == 1u) return true;
    if (spins > (1u << 19)) {
      __builtin_amdgcn_raw_buffer_store_b32(z + 1u, ra, z, 0, 16);
      return true;
    }
  }
  return false;
}
__device__ __forceinline__ bf16x8 ldsf(const char* p) { return *reinterpret_cast<const bf16x8*>(p); }
#define SCHED() __builtin_amdgcn_sched_barrier(0)
#define LBAR() do { asm volatile("s_waitcnt lgkmcnt(0)" ::: "memory"); __builtin_amdgcn_s_barrier(); asm volatile("" ::: "memory"); } while (0)
#define WAIT_V(n) asm volatile("s_waitcnt vmcnt(%0)" :: "n"(n) : "memory")

#define HOLD_V(c, n) asm volatile("s_cmp_eq_u32 %0, 0\n\ts_cbranch_scc1 .Lh%=\n\ts_waitcnt vmcnt(%1)\n.Lh%=:" :: "s"((int)(c)), "n"(n) : "memory", "scc")
#define TH_Q 2
__device__ __forceinline__ void wait_flags(__amdgpu_buffer_rsrc_t rs, int off, gu32* abortw) {
  for (unsigned spins = 0;;) {
    asm volatile("" ::: "memory");
    const unsigned v = __builtin_amdgcn_raw_buffer_load_b32(rs, off, 0, 16);
    if (__all(v != SENT)) break;
    __builtin_amdgcn_s_sleep(4);
    if (spin_fail(spins, abortw)) break;
  }
  asm volatile("" ::: "memory");
}

__device__ __forceinline__ unsigned offA(unsigned row, unsigned ch) {
  return row * 128u + 16u * (ch ^ (row & 7u));
}
__device__ __forceinline__ unsigned swzB(unsigned row) { return ((row & 3u) << 2) | ((row >> 2) & 3u); }
__device__ __forceinline__ unsigned offB(unsigned row, unsigned ch) {
  return 256u * row + 16u * (ch ^ swzB(row));
}
__device__ __forceinline__ void glds16(const u16* src, char* dst) {
  const unsigned d = __builtin_amdgcn_readfirstlane(lds_off(dst));
  unsigned keep;
  asm volatile("s_mov_b32 %0, m0\n\ts_mov_b32 m0, %2\n\ts_nop 0\n\tglobal_load_lds_dwordx4 %1, off\n\ts_mov_b32 m0, %0"
               : "=&s"(keep) : "v"(src), "s"(d) : "memory");
}
__device__ __forceinline__ bf16x8 ld_bfrag(const char* sb, int ks, int c, int lane) {
  const unsigned g = lane >> 4, qq = (lane & 15) >> 2, pp = lane & 3;
  s16x4 lo = __builtin_amdgcn_ds_read_tr16_b64_v4i16(
      (lds_s16x4_ptr)(sb + offB(32 * ks + 8 * g + qq, 2 * c + (pp >> 1)) + 8 * (pp & 1)));
  s16x4 hi = __builtin_amdgcn_ds_read_tr16_b64_v4i16(
      (lds_s16x4_ptr)(sb + offB(32 * ks + 8 * g + 4 + qq, 2 * c + (pp >> 1)) + 8 * (pp & 1)));
  return (bf16x8){lo[0], lo[1], lo[2], lo[3], hi[0], hi[1], hi[2], hi[3]};
}

__device__ __forceinline__ void blk_dma(const u16* src, size_t ld, char* slot, int lane) {
  const u16* s = src + (size_t)(lane >> 2) * ld + (lane & 3) * 8;
  glds16(s, slot);
  glds16(s + 16 * ld, slot + 1024);
}
template <int NB, typename ISSUE, typename SINK>
__device__ __forceinline__ void load_blocks(char* slice, ISSUE&& issue, SINK&& sink) {
#pragma unroll
  for (int i = 0; i < 8; ++i) issue(i, slice + i * 2048);
#pragma unroll
  for (int g = 0; g < NB / 4; ++g) {
    if (g + 1 < NB / 4) WAIT_V(8); else WAIT_V(0);
#pragma unroll
    for (int j = 0; j < 4; ++j) sink(4 * g + j, slice + ((4 * g + j) & 7) * 2048);
    asm volatile("s_waitcnt lgkmcnt(0)" ::: "memory");
    if (g + 2 < NB / 4) {
#pragma unroll
      for (int j = 0; j < 4; ++j) issue(4 * g + 8 + j, slice + ((4 * g + j) & 7) * 2048);
    }
  }
}

__device__ __forceinline__ u32x4 widen32(unsigned a0, unsigned a1, unsigned b0, unsigned b1) {
  swap32(a0, b0); swap16(a0, b0);
  swap32(a1, b1); swap16(a1, b1);
  return (u32x4){a0, a1, b0, b1};
}

__device__ __forceinline__ void publish_flags(__amdgpu_buffer_rsrc_t rs, int off, int stride, int n) {
  WAIT_V(0);
  LBAR();
  int tid = threadIdx.x;
  asm volatile("" : "+v"(tid));
  if (tid < n) __builtin_amdgcn_raw_buffer_store_b32(1u, rs, off + tid * stride, 0, 16);
}

__device__ __forceinline__ void wait_s(const Args& p, __amdgpu_buffer_rsrc_t rsy, int rblk, int c0, int n, bool need) {
  if (need) {
    if (threadIdx.x < 64) {
      for (unsigned spins = 0;;) {
        asm volatile("" ::: "memory");
        const int f = (int)__builtin_amdgcn_raw_buffer_load_b32(rsy, SY_FS + (2 * c0 + (threadIdx.x & (2 * n - 1))) * 128, 0, 16);
        if (__all(f >= rblk)) break;
        __builtin_amdgcn_s_sleep(32);
        if (spin_fail(spins, (gu32*)(p.ws + WS_SY + SY_ABORT))) break;
      }
    }
    LBAR();
  }
}
__device__ __forceinline__ u32x4 combine(u32x4 sv, u32x4 g, u32x4 x) {
  u32x4 o;
#pragma unroll
  for (int j = 0; j < 4; ++j) {
    const unsigned t = cvtpk(lo_f(sv[j]) * lo_f(g[j]), hi_f(sv[j]) * hi_f(g[j]));
    o[j] = cvtpk(lo_f(x[j]) + lo_f(t), hi_f(x[j]) + hi_f(t));
  }
  return o;
}
#define OUT_AUX 0
__device__ __forceinline__ void st_out(const Args& p, int e, u32x4 v) {
  __builtin_amdgcn_raw_buffer_store_b128(v, mkrsrc(p.out, S_LEN * DM * 2), e * 2, 0, OUT_AUX);
}
struct SX { u32x4 s, x; };
__device__ __forceinline__ SX get_sx(const Args& p, int rblk, int e) {
  SX r; r.s = (u32x4){0u, 0u, 0u, 0u}; r.x = r.s;
  if (rblk < NFUSE) {
    r.s = ld_sc1(mkrsrc(p.ws + WS_S, S_LEN * DM * 2), (((e & 1023) >> 5) * S_LEN + (e >> 10)) * 64 + (e & 31) * 2);
    r.x = *reinterpret_cast<const u32x4*>(p.x + e);
  }
  return r;
}
__device__ __forceinline__ void put_g(const Args& p, __amdgpu_buffer_rsrc_t rg, int rblk, int e, u32x4 g, const SX& v) {
  if (rblk < NFUSE) {
    st_out(p, e, combine(v.s, g, v.x));
  } else st_sc1(rg, e * 2, g);
}

__device__ __forceinline__ void job_comb(const Args& p, int rblk, int ct, __amdgpu_buffer_rsrc_t rsy) {
  int tid = threadIdx.x;
  asm volatile("" : "+v"(tid));
  u32x4 xv[8];
#pragma unroll
  for (int i = 0; i < 8; ++i) xv[i] = *reinterpret_cast<const u32x4*>(p.x + (rblk * 128 + i * 16 + (tid >> 5)) * DM + ct * 256 + (tid & 31) * 8);
  if (tid < 64) wait_flags(rsy, SY_FG + (rblk * 8 + 2 * ct + (tid & 1)) * 128, (gu32*)(p.ws + WS_SY + SY_ABORT));
  wait_s(p, rsy, rblk, ct * 8, 8, true);
  u32x4 sv[8], gv[8];
#pragma unroll
  for (int i = 0; i < 8; ++i) {
    const int e = (rblk * 128 + i * 16 + (tid >> 5)) * DM + ct * 256 + (tid & 31) * 8;
    sv[i] = ld_sc1(mkrsrc(p.ws + WS_S, S_LEN * DM * 2), (((e & 1023) >> 5) * S_LEN + (e >> 10)) * 64 + (e & 31) * 2);
    gv[i] = ld_sc1(mkrsrc(p.ws + WS_G, S_LEN * DM * 2), e * 2);
  }
#pragma unroll
  for (int i = 0; i < 8; ++i)
    st_out(p, (rblk * 128 + i * 16 + (tid >> 5)) * DM + ct * 256 + (tid & 31) * 8, combine(sv[i], gv[i], xv[i]));
}

__device__ __forceinline__ u32x4 raw_rsrc(const void* p, unsigned bytes) {
  const unsigned long long a = (unsigned long long)p;
  return (u32x4){(unsigned)a, (unsigned)(a >> 32) & 0xffffu, bytes, 0x00020000u};
}
__device__ __forceinline__ u32x2 next_tag(u32x2 e) { return (u32x2){e[0] + 1u, e[1] + (e[0] == 0xFFFFFFFFu)}; }
struct Boot { u32x2 e; u32x4 c; };
__device__ __forceinline__ void boot_ask(const char* ws, Boot& r) {
  const unsigned o = (threadIdx.x & 63) * 128;
  asm volatile("buffer_load_dwordx2 %0, off, %3, %4 sc1\n\tbuffer_load_dwordx4 %1, %2, %3, %5 offen sc1"
               : "=&v"(r.e), "=&v"(r.c) : "v"(o), "s"(raw_rsrc(ws, SY_TOTAL)), "s"(SY_EP), "s"(SY_BT) : "memory");
}
__device__ __forceinline__ void wait_rows(const char* ws, u32x2 tag) {
  const __amdgpu_buffer_rsrc_t rs = mkrsrc(ws, SY_TOTAL);
  int o = (threadIdx.x & 63) * 128;
  asm volatile("" : "+v"(o));
#pragma unroll 1
  for (int i = 0; i < (1 << 17); ++i) {
    asm volatile("" ::: "memory");
    const u32x2 a = __builtin_amdgcn_raw_buffer_load_b64(rs, o, SY_BT + 32 * 128, 16), b = __builtin_amdgcn_raw_buffer_load_b64(rs, o, SY_BT + 96 * 128, 16);
    if (__all(a[0] == tag[0] && a[1] == tag[1] && b[0] == tag[0] && b[1] == tag[1])) break;
    __builtin_amdgcn_s_sleep(8);
  }
  asm volatile("" ::: "memory");
}
__device__ __forceinline__ unsigned boot_done(const char* ws, Boot& r, bool own = false, u32x2 tag = (u32x2){0u, 0u}) {
#pragma unroll 1
  for (int i = 0; i < (1 << 17); ++i) {
    asm volatile("" : "+v"(r.e), "+v"(r.c));
    if (!own) tag = next_tag(r.e);
    if (__all(r.c[0] == tag[0] && r.c[1] == tag[1] && (r.c[2] ^ r.c[3]) == tag[0])) break;
    __builtin_amdgcn_s_sleep(8);
    boot_ask(ws, r);
    WAIT_V(0);
  }
  unsigned q0[NPAGE], q1[NPAGE], ga = 0xFFFFu, gb = 0xFFFFu;
#pragma unroll
  for (int i = 0; i < NPAGE; ++i) {
    const unsigned x = __builtin_amdgcn_readlane(r.c[2], i), y = __builtin_amdgcn_readlane(r.c[2], i + NPAGE);
    q0[i] = min(x >> 16, y >> 16); q1[i] = min(x & 0xFFFFu, y & 0xFFFFu);
    ga = min(ga, (i & 1) ? q1[i] : q0[i]); gb = min(gb, (i & 1) ? q0[i] : q1[i]);
  }
  unsigned m = gb < ga ? 0x80001555u : 0x2AAAu;
  const unsigned thr = min(ga, gb) + 110;
#pragma unroll
  for (int i = 0; i < NPAGE; ++i) {
    const unsigned lo = min(q0[i], q1[i]), hi = max(q0[i], q1[i]);
    const unsigned keep = (lo < thr ? hi : 0u) > lo + 100 ? ~(1u << i) : ~0u;
    m = (m & keep) | ((q1[i] < q0[i] ? ~0u : 0u) & ~keep);
  }
  return m;
}
typedef __attribute__((address_space(3))) volatile unsigned* lds_vu_ptr;
__device__ __forceinline__ lds_vu_ptr lds_at(const void* p) {
  unsigned a = lds_off(p);
  asm volatile("" : "+v"(a));
  return (lds_vu_ptr)(uintptr_t)a;
}
__device__ __forceinline__ unsigned uni(unsigned v) {
  unsigned r;
  asm volatile("s_nop 1\n\tv_readfirstlane_b32 %0, %1\n\ts_nop 3" : "=s"(r) : "v"(v));
  return r;
}
__device__ __forceinline__ void start_sy(char* ws, const u16* mem0, int i, char* smem) {
  const int tid = threadIdx.x, lane = tid & 63, w = __builtin_amdgcn_readfirstlane(tid >> 6);
  const __amdgpu_buffer_rsrc_t rsy = mkrsrc(ws, SY_TOTAL);
  const __amdgpu_buffer_rsrc_t rs = mkrsrc(ws + (size_t)(i % NPAGE) * PAGE + PROBE - (i / NPAGE) * 2048, 16384);
  const u32x2 e = __builtin_amdgcn_raw_buffer_load_b64(rsy, 0, SY_EP, 16);
  const u32x4 ds = {DSENT, DSENT, DSENT, DSENT};
  if (lane < 32) st_sc1(rs, (lane >> 4) * 8192 + w * 256 + (lane & 15) * 16, ds);
  static_assert(NPRE == 4 && SY_FEND - SY_BR <= 32 * 6144, "");
#pragma unroll
  for (int u = 0; u < 6; ++u)
    st_sc1(rsy, (u < 4 ? SY_V + (i * 4 + u) * 8192 : SY_O + (i * 2 + u - 4) * 8192) + tid * 16, ds);
  if (w == 7) {
    const int n = i * 64 + lane, j = (n >> 10) * 512 + (n & 511);
    const unsigned* m0 = reinterpret_cast<const unsigned*>(mem0 + (2 * ((j >> 2) & 3) + (j & 1)) * DM + (j >> 4) * 16) + ((j >> 1) & 1);
    u32x4 m;
#pragma unroll
    for (int k = 0; k < 4; ++k) { const unsigned t = m0[2 * k]; m[k] = t == DSENT ? 0xFFFFFFFFu : t; }
    st_sc1(rsy, n * 16, m);
  } else if (w == 6) st_sc1(rsy, 32768 + i * 1024 + lane * 16, ds);
  else {
    const int o = SY_BR + i * 6144 + tid * 16;
    st_sc1(rsy, o < SY_FEND ? o : 0x7F000000, (u32x4){SENT, SENT, SENT, SENT});
  }
  unsigned t[4];
#pragma unroll
  for (int k = 0; k < 4; ++k) {
    unsigned long long c0, c1; unsigned v, off = ((k ^ (k >> 1)) & 1) * 8192 + w * 256 + (k >> 1) * 128;
    asm volatile("s_waitcnt vmcnt(0)\n\ts_barrier\n\ts_memtime %0\n\ts_waitcnt lgkmcnt(0)\n\tbuffer_load_dword %2, %3, %4, 0 offen sc1\n\ts_waitcnt vmcnt(0)\n\t"
                 "s_memtime %1\n\ts_waitcnt lgkmcnt(0)" : "=&s"(c0), "=&s"(c1), "=&v"(v) : "v"(off), "s"(rs) : "memory");
    t[k] = (unsigned)(c1 - c0);
  }
  lds_vu_ptr tm = lds_at(smem);
  if (lane == 0) { tm[w] = min(t[0], t[3]); tm[8 + w] = min(t[1], t[2]); }
  int ro = SY_BT + i * 128 + lane * 16;
  asm volatile("" : "+v"(ro));
  LBAR();
  if (threadIdx.x < 8) {
    unsigned m0 = tm[0], m1 = tm[8];
#pragma unroll
    for (int k = 1; k < 8; ++k) { m0 = min(m0, tm[k]); m1 = min(m1, tm[8 + k]); }
    const u32x2 tag = next_tag(e);
    const unsigned v = min(m0, 30000u) << 16 | min(m1, 30000u);
    st_sc1(rsy, ro, (u32x4){tag[0], tag[1], v, v ^ tag[0]});
  }
}
template <int NB, typename F>
__device__ __forceinline__ u32x2 start_rows(const Args& p, int g, unsigned on, F&& between) {
  const int tid = threadIdx.x, lane = tid & 63, row0 = g * 16 + (tid >> 6) * 2;
  u32x4 a[4];
  u32x2 e;
  asm volatile("buffer_load_dwordx4 %0, %5, %8, 0 offen\n\tbuffer_load_dwordx4 %1, %5, %8, 0 offen offset:1024\n\t"
               "buffer_load_dwordx4 %2, %5, %8, 0 offen offset:2048\n\tbuffer_load_dwordx4 %3, %5, %8, 0 offen offset:3072\n\t"
               "buffer_load_dwordx2 %4, off, %6, %7 sc1"
               : "=&v"(a[0]), "=&v"(a[1]), "=&v"(a[2]), "=&v"(a[3]), "=&v"(e)
               : "v"(row0 * (DM * 2) + lane * 16), "s"(raw_rsrc(p.ws, SY_TOTAL)), "s"(SY_EP), "s"(raw_rsrc(p.x, on * (S_LEN * DM * 2))) : "memory");
  between();
  asm volatile("s_waitcnt vmcnt(%5)" : "+v"(a[0]), "+v"(a[1]), "+v"(a[2]), "+v"(a[3]), "+v"(e) : "n"(NB) : "memory");
  const __amdgpu_buffer_rsrc_t ro = mkrsrc(p.ws + WS_XN, on * (S_LEN * XLD * 2));
#pragma unroll
  for (int u = 0; u < 2; ++u) {
    float v[16];
#pragma unroll
    for (int i = 0; i < 8; ++i) { v[2 * i] = lo_f(a[2 * u + (i >> 2)][i & 3]); v[2 * i + 1] = hi_f(a[2 * u + (i >> 2)][i & 3]); }
    float s = 0.f;
#pragma unroll
    for (int i = 0; i < 16; ++i) s += v[i];
    const float mean = wave_sum(s) * (1.0f / DM);
    float q = 0.f;
#pragma unroll
    for (int i = 0; i < 16; ++i) { float d = v[i] - mean; q += d * d; }
    const float var = rbf(wave_sum(q) * (1.0f / DM));
    const float mb = rbf(mean);
    const float inv = 1.0f / rbf(sqrtf(rbf(var + 1e-5f)));
#pragma unroll
    for (int k = 0; k < 2; ++k) {
      u32x4 o;
#pragma unroll
      for (int i = 0; i < 4; ++i) {
        const unsigned t = cvtpk(v[8 * k + 2 * i] - mb, v[8 * k + 2 * i + 1] - mb);
        o[i] = cvtpk(lo_f(t) * inv, hi_f(t) * inv);
      }
      st_sc1(ro, (row0 + u) * (XLD * 2) + k * 1024 + lane * 16, o);
    }
  }
  return next_tag(e);
}
__device__ __forceinline__ void report_rows(const Args& p, int g, u32x2 tag) {
  if (threadIdx.x < 8) st_sc1(mkrsrc(p.ws + WS_SY, SY_TOTAL), SY_BT + (32 + g) * 128 + threadIdx.x * 16, (u32x4){tag[0], tag[1], 0u, tag[0]});
}

typedef __attribute__((ext_vector_type(32))) unsigned u32x32;
__device__ __forceinline__ void put4(u32x32& W, int i, bf16x8 f) {
  const u32x4 v = __builtin_bit_cast(u32x4, f);
#pragma unroll
  for (int j = 0; j < 4; ++j) W[4 * i + j] = v[j];
}
#define CLB8(a, b, c, d, e, f, g, h) "v" #a, "v" #b, "v" #c, "v" #d, "v" #e, "v" #f, "v" #g, "v" #h
#define CLB32(a) CLB32_##a
#define CLB32_128 CLB8(128, 129, 130, 131, 132, 133, 134, 135), CLB8(136, 137, 138, 139, 140, 141, 142, 143), CLB8(144, 145, 146, 147, 148, 149, 150, 151), CLB8(152, 153, 154, 155, 156, 157, 158, 159)
#define CLB32_160 CLB8(160, 161, 162, 163, 164, 165, 166, 167), CLB8(168, 169, 170, 171, 172, 173, 174, 175), CLB8(176, 177, 178, 179, 180, 181, 182, 183), CLB8(184, 185, 186, 187, 188, 189, 190, 191)
#define W4_IN "{v[0:31]}"(W[0]), "{v[32:63]}"(W[1]), "{v[64:95]}"(W[2]), "{v[96:127]}"(W[3])
#define CLB32_192 CLB8(192, 193, 194, 195, 196, 197, 198, 199), CLB8(200, 201, 202, 203, 204, 205, 206, 207), CLB8(208, 209, 210, 211, 212, 213, 214, 215), CLB8(216, 217, 218, 219, 220, 221, 222, 223)
__device__ __forceinline__ unsigned ld_flag0(__amdgpu_buffer_rsrc_t rs, int off) {
  return __builtin_amdgcn_raw_buffer_load_b32(rs, off, 0, 16);
}
__device__ __forceinline__ void glds16_sc1(const char* src, char* dst) {
  const unsigned d = __builtin_amdgcn_readfirstlane(lds_off(dst));
  unsigned keep;
  asm volatile("s_mov_b32 %0, m0\n\ts_mov_b32 m0, %2\n\ts_nop 0\n\tglobal_load_lds_dwordx4 %1, off sc1\n\ts_mov_b32 m0, %0"
               : "=&s"(keep) : "v"(src), "s"(d) : "memory");
}

__device__ __forceinline__ void touch_line(const char* src, char* dst) {
  const unsigned d = __builtin_amdgcn_readfirstlane(lds_off(dst));
  unsigned keep;
  asm volatile("s_mov_b32 %0, m0\n\ts_mov_b32 m0, %2\n\ts_nop 0\n\tglobal_load_lds_dword %1, off sc1\n\ts_mov_b32 m0, %0"
               : "=&s"(keep) : "v"(src), "s"(d) : "memory");
}

#define PF_N 12
#define LN_M 3
#define LN_X 1
#define PR_G1 8
#define WREV 1
template <bool DEV = false>
__device__ __forceinline__ void glds2(const char* base, int step, unsigned voff, unsigned dst) {
  unsigned keep;
  const char* base2 = base + step;
  if (DEV)
    asm volatile("s_mov_b32 %0, m0\n\ts_mov_b32 m0, %4\n\ts_nop 0\n\tglobal_load_lds_dwordx4 %1, %2 sc1\n\t"
                 "s_add_u32 m0, m0, 0x2000\n\ts_nop 0\n\tglobal_load_lds_dwordx4 %1, %3 sc1\n\ts_mov_b32 m0, %0"
                 : "=&s"(keep) : "v"(voff), "s"(base), "s"(base2), "s"(dst) : "memory", "scc");
  else
  asm volatile("s_mov_b32 %0, m0\n\ts_mov_b32 m0, %4\n\ts_nop 0\n\tglobal_load_lds_dwordx4 %1, %2\n\t"
               "s_add_u32 m0, m0, 0x2000\n\ts_nop 0\n\tglobal_load_lds_dwordx4 %1, %3\n\ts_mov_b32 m0, %0"
               : "=&s"(keep) : "v"(voff), "s"(base), "s"(base2), "s"(dst) : "memory", "scc");
}

#define DN_ASM \
  "s_mov_b32 s94, m0\n\t" \
  "s_mov_b64 s[84:85], %[pa]\n\t" \
  "s_mov_b64 s[86:87], %[pb]\n\t" \
  "s_movk_i32 s88, 0x4000\n\t" \
  "s_mov_b32 s89, 0x10000\n\t" \
  "s_movk_i32 s90, 13\n\t" \
  "v_mov_b64 v[0:1], 0\n\t" \
  "v_mov_b64 v[2:3], 0\n\t" \
  "v_mov_b64 v[4:5], 0\n\t" \
  "v_mov_b64 v[6:7], 0\n\t" \
  "v_mov_b64 v[8:9], 0\n\t" \
  "v_mov_b64 v[10:11], 0\n\t" \
  "v_mov_b64 v[12:13], 0\n\t" \
  "v_mov_b64 v[14:15], 0\n\t" \
  "v_mov_b64 v[16:17], 0\n\t" \
  "v_mov_b64 v[18:19], 0\n\t" \
  "v_mov_b64 v[20:21], 0\n\t" \
  "v_mov_b64 v[22:23], 0\n\t" \
  "v_mov_b64 v[24:25], 0\n\t" \
  "v_mov_b64 v[26:27], 0\n\t" \
  "v_mov_b64 v[28:29], 0\n\t" \
  "v_mov_b64 v[30:31], 0\n\t" \
  "s_cmp_lg_u32 %[hf], 0\n\t" \
  "s_cbranch_scc1 .Lk%=\n\t" \
  "ds_read_b128 v[32:35], v80 offset:0\n\t" \
  "ds_read_b128 v[36:39], v80 offset:1024\n\t" \
  "ds_read_b128 v[40:43], v80 offset:2048\n\t" \
  "ds_read_b128 v[44:47], v80 offset:3072\n\t" \
  "ds_read_b64_tr_b16 v[64:65], v82 offset:0\n\t" \
  "ds_read_b64_tr_b16 v[66:67], v83 offset:0\n\t" \
  "ds_read_b128 v[48:51], v80 offset:8192\n\t" \
  "ds_read_b64_tr_b16 v[68:69], v84 offset:0\n\t" \
  "ds_read_b64_tr_b16 v[70:71], v85 offset:0\n\t" \
  "ds_read_b128 v[52:55], v80 offset:9216\n\t" \
  "ds_read_b128 v[56:59], v80 offset:10240\n\t" \
  "ds_read_b128 v[60:63], v80 offset:11264\n\t" \
  ".Lp0%=:\n\t" \
  "s_waitcnt vmcnt(4)\n\t" \
  "s_barrier\n\t" \
  "s_waitcnt lgkmcnt(3)\n\t" \
  "v_mfma_f32_16x16x32_bf16 v[0:3], v[64:67], v[32:35], v[0:3]\n\t" \
  "ds_read_b64_tr_b16 v[72:73], v82 offset:8192\n\t" \
  "v_mfma_f32_16x16x32_bf16 v[4:7], v[68:71], v[32:35], v[4:7]\n\t" \
  "ds_read_b64_tr_b16 v[74:75], v83 offset:8192\n\t" \
  "ds_read_b128 v[32:35], v80 offset:32768\n\t" \
  "s_add_u32 m0, %[lw], 98304\n\t" \
  "v_mfma_f32_16x16x32_bf16 v[8:11], v[64:67], v[36:39], v[8:11]\n\t" \
  "ds_read_b64_tr_b16 v[76:77], v84 offset:8192\n\t" \
  "global_load_lds_dwordx4 v90, s[84:85] sc1\n\t" \
  "s_add_u32 s84, s84, s88\n\t" \
  "s_addc_u32 s85, s85, 0\n\t" \
  "v_mfma_f32_16x16x32_bf16 v[12:15], v[68:71], v[36:39], v[12:15]\n\t" \
  "ds_read_b64_tr_b16 v[78:79], v85 offset:8192\n\t" \
  "ds_read_b128 v[36:39], v80 offset:33792\n\t" \
  "v_mfma_f32_16x16x32_bf16 v[16:19], v[64:67], v[40:43], v[16:19]\n\t" \
  "v_mfma_f32_16x16x32_bf16 v[20:23], v[68:71], v[40:43], v[20:23]\n\t" \
  "ds_read_b128 v[40:43], v80 offset:34816\n\t" \
  "s_add_u32 m0, %[lw], 114688\n\t" \
  "v_mfma_f32_16x16x32_bf16 v[24:27], v[64:67], v[44:47], v[24:27]\n\t" \
  "global_load_lds_dwordx4 v91, s[86:87]\n\t" \
  "s_add_u32 s86, s86, s89\n\t" \
  "s_addc_u32 s87, s87, 0\n\t" \
  "v_mfma_f32_16x16x32_bf16 v[28:31], v[68:71], v[44:47], v[28:31]\n\t" \
  "ds_read_b128 v[44:47], v80 offset:35840\n\t" \
  "s_waitcnt lgkmcnt(3)\n\t" \
  "v_mfma_f32_16x16x32_bf16 v[0:3], v[72:75], v[48:51], v[0:3]\n\t" \
  "ds_read_b64_tr_b16 v[64:65], v82 offset:32768\n\t" \
  "v_mfma_f32_16x16x32_bf16 v[4:7], v[76:79], v[48:51], v[4:7]\n\t" \
  "ds_read_b64_tr_b16 v[66:67], v83 offset:32768\n\t" \
  "ds_read_b128 v[48:51], v80 offset:40960\n\t" \
  "s_add_u32 m0, %[lw], 106496\n\t" \
  "v_mfma_f32_16x16x32_bf16 v[8:11], v[72:75], v[52:55], v[8:11]\n\t" \
  "ds_read_b64_tr_b16 v[68:69], v84 offset:32768\n\t" \
  "global_load_lds_dwordx4 v90, s[84:85] sc1\n\t" \
  "s_add_u32 s84, s84, s88\n\t" \
  "s_addc_u32 s85, s85, 0\n\t" \
  "v_mfma_f32_16x16x32_bf16 v[12:15], v[76:79], v[52:55], v[12:15]\n\t" \
  "ds_read_b64_tr_b16 v[70:71], v85 offset:32768\n\t" \
  "ds_read_b128 v[52:55], v80 offset:41984\n\t" \
  "v_mfma_f32_16x16x32_bf16 v[16:19], v[72:75], v[56:59], v[16:19]\n\t" \
  "v_mfma_f32_16x16x32_bf16 v[20:23], v[76:79], v[56:59], v[20:23]\n\t" \
  "ds_read_b128 v[56:59], v80 offset:43008\n\t" \
  "s_add_u32 m0, %[lw], 122880\n\t" \
  "v_mfma_f32_16x16x32_bf16 v[24:27], v[72:75], v[60:63], v[24:27]\n\t" \
  "global_load_lds_dwordx4 v91, s[86:87]\n\t" \
  "s_add_u32 s86, s86, s89\n\t" \
  "s_addc_u32 s87, s87, 0\n\t" \
  "v_mfma_f32_16x16x32_bf16 v[28:31], v[76:79], v[60:63], v[28:31]\n\t" \
  "ds_read_b128 v[60:63], v80 offset:44032\n\t" \
  "s_sub_u32 s90, s90, 1\n\t" \
  "s_cselect_b32 s88, 0, s88\n\t" \
  "s_cselect_b32 s89, 0, s89\n\t" \
  "s_cselect_b32 s91, 16384, 0\n\t" \
  "s_sub_u32 s84, s84, s91\n\t" \
  "s_subb_u32 s85, s85, 0\n\t" \
  "s_lshl_b32 s91, s91, 2\n\t" \
  "s_sub_u32 s86, s86, s91\n\t" \
  "s_subb_u32 s87, s87, 0\n\t" \
  "s_waitcnt vmcnt(4)\n\t" \
  "s_barrier\n\t" \
  "s_waitcnt lgkmcnt(3)\n\t" \
  "v_mfma_f32_16x16x32_bf16 v[0:3], v[64:67], v[32:35], v[0:3]\n\t" \
  "ds_read_b64_tr_b16 v[72:73], v82 offset:40960\n\t" \
  "v_mfma_f32_16x16x32_bf16 v[4:7], v[68:71], v[32:35], v[4:7]\n\t" \
  "ds_read_b64_tr_b16 v[74:75], v83 offset:40960\n\t" \
  "ds_read_b128 v[32:35], v81 offset:0\n\t" \
  "s_add_u32 m0, %[lw], 0\n\t" \
  "v_mfma_f32_16x16x32_bf16 v[8:11], v[64:67], v[36:39], v[8:11]\n\t" \
  "ds_read_b64_tr_b16 v[76:77], v84 offset:40960\n\t" \
  "global_load_lds_dwordx4 v90, s[84:85] sc1\n\t" \
  "s_add_u32 s84, s84, s88\n\t" \
  "s_addc_u32 s85, s85, 0\n\t" \
  "v_mfma_f32_16x16x32_bf16 v[12:15], v[68:71], v[36:39], v[12:15]\n\t" \
  "ds_read_b64_tr_b16 v[78:79], v85 offset:40960\n\t" \
  "ds_read_b128 v[36:39], v81 offset:1024\n\t" \
  "v_mfma_f32_16x16x32_bf16 v[16:19], v[64:67], v[40:43], v[16:19]\n\t" \
  "v_mfma_f32_16x16x32_bf16 v[20:23], v[68:71], v[40:43], v[20:23]\n\t" \
  "ds_read_b128 v[40:43], v81 offset:2048\n\t" \
  "s_add_u32 m0, %[lw], 16384\n\t" \
  "v_mfma_f32_16x16x32_bf16 v[24:27], v[64:67], v[44:47], v[24:27]\n\t" \
  "global_load_lds_dwordx4 v91, s[86:87]\n\t" \
  "s_add_u32 s86, s86, s89\n\t" \
  "s_addc_u32 s87, s87, 0\n\t" \
  "v_mfma_f32_16x16x32_bf16 v[28:31], v[68:71], v[44:47], v[28:31]\n\t" \
  "ds_read_b128 v[44:47], v81 offset:3072\n\t" \
  "s_waitcnt lgkmcnt(3)\n\t" \
  "v_mfma_f32_16x16x32_bf16 v[0:3], v[72:75], v[48:51], v[0:3]\n\t" \
  "ds_read_b64_tr_b16 v[64:65], v86 offset:0\n\t" \
  "v_mfma_f32_16x16x32_bf16 v[4:7], v[76:79], v[48:51], v[4:7]\n\t" \
  "ds_read_b64_tr_b16 v[66:67], v87 offset:0\n\t" \
  "ds_read_b128 v[48:51], v81 offset:8192\n\t" \
  "s_add_u32 m0, %[lw], 8192\n\t" \
  "v_mfma_f32_16x16x32_bf16 v[8:11], v[72:75], v[52:55], v[8:11]\n\t" \
  "ds_read_b64_tr_b16 v[68:69], v88 offset:0\n\t" \
  "global_load_lds_dwordx4 v90, s[84:85] sc1\n\t" \
  "s_add_u32 s84, s84, s88\n\t" \
  "s_addc_u32 s85, s85, 0\n\t" \
  "v_mfma_f32_16x16x32_bf16 v[12:15], v[76:79], v[52:55], v[12:15]\n\t" \
  "ds_read_b64_tr_b16 v[70:71], v89 offset:0\n\t" \
  "ds_read_b128 v[52:55], v81 offset:9216\n\t" \
  "v_mfma_f32_16x16x32_bf16 v[16:19], v[72:75], v[56:59], v[16:19]\n\t" \
  "v_mfma_f32_16x16x32_bf16 v[20:23], v[76:79], v[56:59], v[20:23]\n\t" \
  "ds_read_b128 v[56:59], v81 offset:10240\n\t" \
  "s_add_u32 m0, %[lw], 24576\n\t" \
  "v_mfma_f32_16x16x32_bf16 v[24:27], v[72:75], v[60:63], v[24:27]\n\t" \
  "global_load_lds_dwordx4 v91, s[86:87]\n\t" \
  "s_add_u32 s86, s86, s89\n\t" \
  "s_addc_u32 s87, s87, 0\n\t" \
  "v_mfma_f32_16x16x32_bf16 v[28:31], v[76:79], v[60:63], v[28:31]\n\t" \
  "ds_read_b128 v[60:63], v81 offset:11264\n\t" \
  "s_waitcnt vmcnt(4)\n\t" \
  "s_barrier\n\t" \
  "s_waitcnt lgkmcnt(3)\n\t" \
  "v_mfma_f32_16x16x32_bf16 v[0:3], v[64:67], v[32:35], v[0:3]\n\t" \
  "ds_read_b64_tr_b16 v[72:73], v86 offset:8192\n\t" \
  "v_mfma_f32_16x16x32_bf16 v[4:7], v[68:71], v[32:35], v[4:7]\n\t" \
  "ds_read_b64_tr_b16 v[74:75], v87 offset:8192\n\t" \
  "ds_read_b128 v[32:35], v81 offset:32768\n\t" \
  "s_add_u32 m0, %[lw], 32768\n\t" \
  "v_mfma_f32_16x16x32_bf16 v[8:11], v[64:67], v[36:39], v[8:11]\n\t" \
  "ds_read_b64_tr_b16 v[76:77], v88 offset:8192\n\t" \
  "global_load_lds_dwordx4 v90, s[84:85] sc1\n\t" \
  "s_add_u32 s84, s84, s88\n\t" \
  "s_addc_u32 s85, s85, 0\n\t" \
  "v_mfma_f32_16x16x32_bf16 v[12:15], v[68:71], v[36:39], v[12:15]\n\t" \
  "ds_read_b64_tr_b16 v[78:79], v89 offset:8192\n\t" \
  "ds_read_b128 v[36:39], v81 offset:33792\n\t" \
  "v_mfma_f32_16x16x32_bf16 v[16:19], v[64:67], v[40:43], v[16:19]\n\t" \
  "v_mfma_f32_16x16x32_bf16 v[20:23], v[68:71], v[40:43], v[20:23]\n\t" \
  "ds_read_b128 v[40:43], v81 offset:34816\n\t" \
  "s_add_u32 m0, %[lw], 49152\n\t" \
  "v_mfma_f32_16x16x32_bf16 v[24:27], v[64:67], v[44:47], v[24:27]\n\t" \
  "global_load_lds_dwordx4 v91, s[86:87]\n\t" \
  "s_add_u32 s86, s86, s89\n\t" \
  "s_addc_u32 s87, s87, 0\n\t" \
  "v_mfma_f32_16x16x32_bf16 v[28:31], v[68:71], v[44:47], v[28:31]\n\t" \
  "ds_read_b128 v[44:47], v81 offset:35840\n\t" \
  "s_waitcnt lgkmcnt(3)\n\t" \
  "v_mfma_f32_16x16x32_bf16 v[0:3], v[72:75], v[48:51], v[0:3]\n\t" \
  "ds_read_b64_tr_b16 v[64:65], v86 offset:32768\n\t" \
  "v_mfma_f32_16x16x32_bf16 v[4:7], v[76:79], v[48:51], v[4:7]\n\t" \
  "ds_read_b64_tr_b16 v[66:67], v87 offset:32768\n\t" \
  "ds_read_b128 v[48:51], v81 offset:40960\n\t" \
  "s_add_u32 m0, %[lw], 40960\n\t" \
  "v_mfma_f32_16x16x32_bf16 v[8:11], v[72:75], v[52:55], v[8:11]\n\t" \
  "ds_read_b64_tr_b16 v[68:69], v88 offset:32768\n\t" \
  "global_load_lds_dwordx4 v90, s[84:85] sc1\n\t" \
  "s_add_u32 s84, s84, s88\n\t" \
  "s_addc_u32 s85, s85, 0\n\t" \
  "v_mfma_f32_16x16x32_bf16 v[12:15], v[76:79], v[52:55], v[12:15]\n\t" \
  "ds_read_b64_tr_b16 v[70:71], v89 offset:32768\n\t" \
  "ds_read_b128 v[52:55], v81 offset:41984\n\t" \
  "v_mfma_f32_16x16x32_bf16 v[16:19], v[72:75], v[56:59], v[16:19]\n\t" \
  "v_mfma_f32_16x16x32_bf16 v[20:23], v[76:79], v[56:59], v[20:23]\n\t" \
  "ds_read_b128 v[56:59], v81 offset:43008\n\t" \
  "s_add_u32 m0, %[lw], 57344\n\t" \
  "v_mfma_f32_16x16x32_bf16 v[24:27], v[72:75], v[60:63], v[24:27]\n\t" \
  "global_load_lds_dwordx4 v91, s[86:87]\n\t" \
  "s_add_u32 s86, s86, s89\n\t" \
  "s_addc_u32 s87, s87, 0\n\t" \
  "v_mfma_f32_16x16x32_bf16 v[28:31], v[76:79], v[60:63], v[28:31]\n\t" \
  "ds_read_b128 v[60:63], v81 offset:44032\n\t" \
  "s_waitcnt vmcnt(4)\n\t" \
  "s_barrier\n\t" \
  "s_waitcnt lgkmcnt(3)\n\t" \
  "v_mfma_f32_16x16x32_bf16 v[0:3], v[64:67], v[32:35], v[0:3]\n\t" \
  "ds_read_b64_tr_b16 v[72:73], v86 offset:40960\n\t" \
  "v_mfma_f32_16x16x32_bf16 v[4:7], v[68:71], v[32:35], v[4:7]\n\t" \
  "ds_read_b64_tr_b16 v[74:75], v87 offset:40960\n\t" \
  "ds_read_b128 v[32:35], v80 offset:0\n\t" \
  "s_add_u32 m0, %[lw], 65536\n\t" \
  "v_mfma_f32_16x16x32_bf16 v[8:11], v[64:67], v[36:39], v[8:11]\n\t" \
  "ds_read_b64_tr_b16 v[76:77], v88 offset:40960\n\t" \
  "global_load_lds_dwordx4 v90, s[84:85] sc1\n\t" \
  "s_add_u32 s84, s84, s88\n\t" \
  "s_addc_u32 s85, s85, 0\n\t" \
  "v_mfma_f32_16x16x32_bf16 v[12:15], v[68:71], v[36:39], v[12:15]\n\t" \
  "ds_read_b64_tr_b16 v[78:79], v89 offset:40960\n\t" \
  "ds_read_b128 v[36:39], v80 offset:1024\n\t" \
  "v_mfma_f32_16x16x32_bf16 v[16:19], v[64:67], v[40:43], v[16:19]\n\t" \
  "v_mfma_f32_16x16x32_bf16 v[20:23], v[68:71], v[40:43], v[20:23]\n\t" \
  "ds_read_b128 v[40:43], v80 offset:2048\n\t" \
  "s_add_u32 m0, %[lw], 81920\n\t" \
  "v_mfma_f32_16x16x32_bf16 v[24:27], v[64:67], v[44:47], v[24:27]\n\t" \
  "global_load_lds_dwordx4 v91, s[86:87]\n\t" \
  "s_add_u32 s86, s86, s89\n\t" \
  "s_addc_u32 s87, s87, 0\n\t" \
  "v_mfma_f32_16x16x32_bf16 v[28:31], v[68:71], v[44:47], v[28:31]\n\t" \
  "ds_read_b128 v[44:47], v80 offset:3072\n\t" \
  "s_waitcnt lgkmcnt(3)\n\t" \
  "v_mfma_f32_16x16x32_bf16 v[0:3], v[72:75], v[48:51], v[0:3]\n\t" \
  "ds_read_b64_tr_b16 v[64:65], v82 offset:0\n\t" \
  "v_mfma_f32_16x16x32_bf16 v[4:7], v[76:79], v[48:51], v[4:7]\n\t" \
  "ds_read_b64_tr_b16 v[66:67], v83 offset:0\n\t" \
  "ds_read_b128 v[48:51], v80 offset:8192\n\t" \
  "s_add_u32 m0, %[lw], 73728\n\t" \
  "v_mfma_f32_16x16x32_bf16 v[8:11], v[72:75], v[52:55], v[8:11]\n\t" \
  "ds_read_b64_tr_b16 v[68:69], v84 offset:0\n\t" \
  "global_load_lds_dwordx4 v90, s[84:85] sc1\n\t" \
  "s_add_u32 s84, s84, s88\n\t" \
  "s_addc_u32 s85, s85, 0\n\t" \
  "v_mfma_f32_16x16x32_bf16 v[12:15], v[76:79], v[52:55], v[12:15]\n\t" \
  "ds_read_b64_tr_b16 v[70:71], v85 offset:0\n\t" \
  "ds_read_b128 v[52:55], v80 offset:9216\n\t" \
  "v_mfma_f32_16x16x32_bf16 v[16:19], v[72:75], v[56:59], v[16:19]\n\t" \
  "v_mfma_f32_16x16x32_bf16 v[20:23], v[76:79], v[56:59], v[20:23]\n\t" \
  "ds_read_b128 v[56:59], v80 offset:10240\n\t" \
  "s_add_u32 m0, %[lw], 90112\n\t" \
  "v_mfma_f32_16x16x32_bf16 v[24:27], v[72:75], v[60:63], v[24:27]\n\t" \
  "global_load_lds_dwordx4 v91, s[86:87]\n\t" \
  "s_add_u32 s86, s86, s89\n\t" \
  "s_addc_u32 s87, s87, 0\n\t" \
  "v_mfma_f32_16x16x32_bf16 v[28:31], v[76:79], v[60:63], v[28:31]\n\t" \
  "ds_read_b128 v[60:63], v80 offset:11264\n\t" \
  "s_cmp_lg_u32 s88, 0\n\t" \
  "s_cbranch_scc1 .Lp0%=\n\t" \
  "s_branch .Le%=\n\t" \
  ".Lk%=:\n\t" \
  "ds_read_b128 v[32:35], v80 offset:0\n\t" \
  "ds_read_b128 v[36:39], v80 offset:1024\n\t" \
  "ds_read_b128 v[40:43], v80 offset:2048\n\t" \
  "ds_read_b128 v[44:47], v80 offset:3072\n\t" \
  "ds_read_b64_tr_b16 v[64:65], v82 offset:0\n\t" \
  "ds_read_b64_tr_b16 v[66:67], v83 offset:0\n\t" \
  "ds_read_b128 v[48:51], v80 offset:8192\n\t" \
  "ds_read_b64_tr_b16 v[68:69], v84 offset:0\n\t" \
  "ds_read_b64_tr_b16 v[70:71], v85 offset:0\n\t" \
  "ds_read_b128 v[52:55], v80 offset:9216\n\t" \
  "ds_read_b128 v[56:59], v80 offset:10240\n\t" \
  "ds_read_b128 v[60:63], v80 offset:11264\n\t" \
  ".Lp1%=:\n\t" \
  "s_waitcnt vmcnt(4)\n\t" \
  "s_barrier\n\t" \
  "s_waitcnt lgkmcnt(3)\n\t" \
  "v_mfma_f32_16x16x32_bf16 v[0:3], v[64:67], v[32:35], v[0:3]\n\t" \
  "ds_read_b64_tr_b16 v[72:73], v82 offset:8192\n\t" \
  "v_mfma_f32_16x16x32_bf16 v[4:7], v[68:71], v[32:35], v[4:7]\n\t" \
  "ds_read_b64_tr_b16 v[74:75], v83 offset:8192\n\t" \
  "ds_read_b128 v[32:35], v80 offset:32768\n\t" \
  "v_mfma_f32_16x16x32_bf16 v[8:11], v[64:67], v[36:39], v[8:11]\n\t" \
  "ds_read_b64_tr_b16 v[76:77], v84 offset:8192\n\t" \
  "v_mfma_f32_16x16x32_bf16 v[12:15], v[68:71], v[36:39], v[12:15]\n\t" \
  "ds_read_b64_tr_b16 v[78:79], v85 offset:8192\n\t" \
  "ds_read_b128 v[36:39], v80 offset:33792\n\t" \
  "s_add_u32 m0, %[lw], 98304\n\t" \
  "v_mfma_f32_16x16x32_bf16 v[16:19], v[64:67], v[40:43], v[16:19]\n\t" \
  "global_load_lds_dwordx4 v90, s[84:85] sc1\n\t" \
  "s_add_u32 s84, s84, s88\n\t" \
  "s_addc_u32 s85, s85, 0\n\t" \
  "v_mfma_f32_16x16x32_bf16 v[20:23], v[68:71], v[40:43], v[20:23]\n\t" \
  "ds_read_b128 v[40:43], v80 offset:34816\n\t" \
  "v_mfma_f32_16x16x32_bf16 v[24:27], v[64:67], v[44:47], v[24:27]\n\t" \
  "s_add_u32 m0, %[lw], 114688\n\t" \
  "v_mfma_f32_16x16x32_bf16 v[28:31], v[68:71], v[44:47], v[28:31]\n\t" \
  "ds_read_b128 v[44:47], v80 offset:35840\n\t" \
  "global_load_lds_dwordx4 v91, s[86:87]\n\t" \
  "s_add_u32 s86, s86, s89\n\t" \
  "s_addc_u32 s87, s87, 0\n\t" \
  "s_waitcnt lgkmcnt(3)\n\t" \
  "v_mfma_f32_16x16x32_bf16 v[0:3], v[72:75], v[48:51], v[0:3]\n\t" \
  "ds_read_b64_tr_b16 v[64:65], v82 offset:32768\n\t" \
  "v_mfma_f32_16x16x32_bf16 v[4:7], v[76:79], v[48:51], v[4:7]\n\t" \
  "ds_read_b64_tr_b16 v[66:67], v83 offset:32768\n\t" \
  "ds_read_b128 v[48:51], v80 offset:40960\n\t" \
  "v_mfma_f32_16x16x32_bf16 v[8:11], v[72:75], v[52:55], v[8:11]\n\t" \
  "ds_read_b64_tr_b16 v[68:69], v84 offset:32768\n\t" \
  "v_mfma_f32_16x16x32_bf16 v[12:15], v[76:79], v[52:55], v[12:15]\n\t" \
  "ds_read_b64_tr_b16 v[70:71], v85 offset:32768\n\t" \
  "ds_read_b128 v[52:55], v80 offset:41984\n\t" \
  "s_add_u32 m0, %[lw], 106496\n\t" \
  "v_mfma_f32_16x16x32_bf16 v[16:19], v[72:75], v[56:59], v[16:19]\n\t" \
  "global_load_lds_dwordx4 v90, s[84:85] sc1\n\t" \
  "s_add_u32 s84, s84, s88\n\t" \
  "s_addc_u32 s85, s85, 0\n\t" \
  "v_mfma_f32_16x16x32_bf16 v[20:23], v[76:79], v[56:59], v[20:23]\n\t" \
  "ds_read_b128 v[56:59], v80 offset:43008\n\t" \
  "v_mfma_f32_16x16x32_bf16 v[24:27], v[72:75], v[60:63], v[24:27]\n\t" \
  "s_add_u32 m0, %[lw], 122880\n\t" \
  "v_mfma_f32_16x16x32_bf16 v[28:31], v[76:79], v[60:63], v[28:31]\n\t" \
  "ds_read_b128 v[60:63], v80 offset:44032\n\t" \
  "global_load_lds_dwordx4 v91, s[86:87]\n\t" \
  "s_add_u32 s86, s86, s89\n\t" \
  "s_addc_u32 s87, s87, 0\n\t" \
  "s_sub_u32 s90, s90, 1\n\t" \
  "s_cselect_b32 s88, 0, s88\n\t" \
  "s_cselect_b32 s89, 0, s89\n\t" \
  "s_cselect_b32 s91, 16384, 0\n\t" \
  "s_sub_u32 s84, s84, s91\n\t" \
  "s_subb_u32 s85, s85, 0\n\t" \
  "s_lshl_b32 s91, s91, 2\n\t" \
  "s_sub_u32 s86, s86, s91\n\t" \
  "s_subb_u32 s87, s87, 0\n\t" \
  "s_waitcnt vmcnt(4)\n\t" \
  "s_barrier\n\t" \
  "s_waitcnt lgkmcnt(3)\n\t" \
  "v_mfma_f32_16x16x32_bf16 v[0:3], v[64:67], v[32:35], v[0:3]\n\t" \
  "ds_read_b64_tr_b16 v[72:73], v82 offset:40960\n\t" \
  "v_mfma_f32_16x16x32_bf16 v[4:7], v[68:71], v[32:35], v[4:7]\n\t" \
  "ds_read_b64_tr_b16 v[74:75], v83 offset:40960\n\t" \
  "ds_read_b128 v[32:35], v81 offset:0\n\t" \
  "v_mfma_f32_16x16x32_bf16 v[8:11], v[64:67], v[36:39], v[8:11]\n\t" \
  "ds_read_b64_tr_b16 v[76:77], v84 offset:40960\n\t" \
  "v_mfma_f32_16x16x32_bf16 v[12:15], v[68:71], v[36:39], v[12:15]\n\t" \
  "ds_read_b64_tr_b16 v[78:79], v85 offset:40960\n\t" \
  "ds_read_b128 v[36:39], v81 offset:1024\n\t" \
  "s_add_u32 m0, %[lw], 0\n\t" \
  "v_mfma_f32_16x16x32_bf16 v[16:19], v[64:67], v[40:43], v[16:19]\n\t" \
  "global_load_lds_dwordx4 v90, s[84:85] sc1\n\t" \
  "s_add_u32 s84, s84, s88\n\t" \
  "s_addc_u32 s85, s85, 0\n\t" \
  "v_mfma_f32_16x16x32_bf16 v[20:23], v[68:71], v[40:43], v[20:23]\n\t" \
  "ds_read_b128 v[40:43], v81 offset:2048\n\t" \
  "v_mfma_f32_16x16x32_bf16 v[24:27], v[64:67], v[44:47], v[24:27]\n\t" \
  "s_add_u32 m0, %[lw], 16384\n\t" \
  "v_mfma_f32_16x16x32_bf16 v[28:31], v[68:71], v[44:47], v[28:31]\n\t" \
  "ds_read_b128 v[44:47], v81 offset:3072\n\t" \
  "global_load_lds_dwordx4 v91, s[86:87]\n\t" \
  "s_add_u32 s86, s86, s89\n\t" \
  "s_addc_u32 s87, s87, 0\n\t" \
  "s_waitcnt lgkmcnt(3)\n\t" \
  "v_mfma_f32_16x16x32_bf16 v[0:3], v[72:75], v[48:51], v[0:3]\n\t" \
  "ds_read_b64_tr_b16 v[64:65], v86 offset:0\n\t" \
  "v_mfma_f32_16x16x32_bf16 v[4:7], v[76:79], v[48:51], v[4:7]\n\t" \
  "ds_read_b64_tr_b16 v[66:67], v87 offset:0\n\t" \
  "ds_read_b128 v[48:51], v81 offset:8192\n\t" \
  "v_mfma_f32_16x16x32_bf16 v[8:11], v[72:75], v[52:55], v[8:11]\n\t" \
  "ds_read_b64_tr_b16 v[68:69], v88 offset:0\n\t" \
  "v_mfma_f32_16x16x32_bf16 v[12:15], v[76:79], v[52:55], v[12:15]\n\t" \
  "ds_read_b64_tr_b16 v[70:71], v89 offset:0\n\t" \
  "ds_read_b128 v[52:55], v81 offset:9216\n\t" \
  "s_add_u32 m0, %[lw], 8192\n\t" \
  "v_mfma_f32_16x16x32_bf16 v[16:19], v[72:75], v[56:59], v[16:19]\n\t" \
  "global_load_lds_dwordx4 v90, s[84:85] sc1\n\t" \
  "s_add_u32 s84, s84, s88\n\t" \
  "s_addc_u32 s85, s85, 0\n\t" \
  "v_mfma_f32_16x16x32_bf16 v[20:23], v[76:79], v[56:59], v[20:23]\n\t" \
  "ds_read_b128 v[56:59], v81 offset:10240\n\t" \
  "v_mfma_f32_16x16x32_bf16 v[24:27], v[72:75], v[60:63], v[24:27]\n\t" \
  "s_add_u32 m0, %[lw], 24576\n\t" \
  "v_mfma_f32_16x16x32_bf16 v[28:31], v[76:79], v[60:63], v[28:31]\n\t" \
  "ds_read_b128 v[60:63], v81 offset:11264\n\t" \
  "global_load_lds_dwordx4 v91, s[86:87]\n\t" \
  "s_add_u32 s86, s86, s89\n\t" \
  "s_addc_u32 s87, s87, 0\n\t" \
  "s_waitcnt vmcnt(4)\n\t" \
  "s_barrier\n\t" \
  "s_waitcnt lgkmcnt(3)\n\t" \
  "v_mfma_f32_16x16x32_bf16 v[0:3], v[64:67], v[32:35], v[0:3]\n\t" \
  "ds_read_b64_tr_b16 v[72:73], v86 offset:8192\n\t" \
  "v_mfma_f32_16x16x32_bf16 v[4:7], v[68:71], v[32:35], v[4:7]\n\t" \
  "ds_read_b64_tr_b16 v[74:75], v87 offset:8192\n\t" \
  "ds_read_b128 v[32:35], v81 offset:32768\n\t" \
  "v_mfma_f32_16x16x32_bf16 v[8:11], v[64:67], v[36:39], v[8:11]\n\t" \
  "ds_read_b64_tr_b16 v[76:77], v88 offset:8192\n\t" \
  "v_mfma_f32_16x16x32_bf16 v[12:15], v[68:71], v[36:39], v[12:15]\n\t" \
  "ds_read_b64_tr_b16 v[78:79], v89 offset:8192\n\t" \
  "ds_read_b128 v[36:39], v81 offset:33792\n\t" \
  "s_add_u32 m0, %[lw], 32768\n\t" \
  "v_mfma_f32_16x16x32_bf16 v[16:19], v[64:67], v[40:43], v[16:19]\n\t" \
  "global_load_lds_dwordx4 v90, s[84:85] sc1\n\t" \
  "s_add_u32 s84, s84, s88\n\t" \
  "s_addc_u32 s85, s85, 0\n\t" \
  "v_mfma_f32_16x16x32_bf16 v[20:23], v[68:71], v[40:43], v[20:23]\n\t" \
  "ds_read_b128 v[40:43], v81 offset:34816\n\t" \
  "v_mfma_f32_16x16x32_bf16 v[24:27], v[64:67], v[44:47], v[24:27]\n\t" \
  "s_add_u32 m0, %[lw], 49152\n\t" \
  "v_mfma_f32_16x16x32_bf16 v[28:31], v[68:71], v[44:47], v[28:31]\n\t" \
  "ds_read_b128 v[44:47], v81 offset:35840\n\t" \
  "global_load_lds_dwordx4 v91, s[86:87]\n\t" \
  "s_add_u32 s86, s86, s89\n\t" \
  "s_addc_u32 s87, s87, 0\n\t" \
  "s_waitcnt lgkmcnt(3)\n\t" \
  "v_mfma_f32_16x16x32_bf16 v[0:3], v[72:75], v[48:51], v[0:3]\n\t" \
  "ds_read_b64_tr_b16 v[64:65], v86 offset:32768\n\t" \
  "v_mfma_f32_16x16x32_bf16 v[4:7], v[76:79], v[48:51], v[4:7]\n\t" \
  "ds_read_b64_tr_b16 v[66:67], v87 offset:32768\n\t" \
  "ds_read_b128 v[48:51], v81 offset:40960\n\t" \
  "v_mfma_f32_16x16x32_bf16 v[8:11], v[72:75], v[52:55], v[8:11]\n\t" \
  "ds_read_b64_tr_b16 v[68:69], v88 offset:32768\n\t" \
  "v_mfma_f32_16x16x32_bf16 v[12:15], v[76:79], v[52:55], v[12:15]\n\t" \
  "ds_read_b64_tr_b16 v[70:71], v89 offset:32768\n\t" \
  "ds_read_b128 v[52:55], v81 offset:41984\n\t" \
  "s_add_u32 m0, %[lw], 40960\n\t" \
  "v_mfma_f32_16x16x32_bf16 v[16:19], v[72:75], v[56:59], v[16:19]\n\t" \
  "global_load_lds_dwordx4 v90, s[84:85] sc1\n\t" \
  "s_add_u32 s84, s84, s88\n\t" \
  "s_addc_u32 s85, s85, 0\n\t" \
  "v_mfma_f32_16x16x32_bf16 v[20:23], v[76:79], v[56:59], v[20:23]\n\t" \
  "ds_read_b128 v[56:59], v81 offset:43008\n\t" \
  "v_mfma_f32_16x16x32_bf16 v[24:27], v[72:75], v[60:63], v[24:27]\n\t" \
  "s_add_u32 m0, %[lw], 57344\n\t" \
  "v_mfma_f32_16x16x32_bf16 v[28:31], v[76:79], v[60:63], v[28:31]\n\t" \
  "ds_read_b128 v[60:63], v81 offset:44032\n\t" \
  "global_load_lds_dwordx4 v91, s[86:87]\n\t" \
  "s_add_u32 s86, s86, s89\n\t" \
  "s_addc_u32 s87, s87, 0\n\t" \
  "s_waitcnt vmcnt(4)\n\t" \
  "s_barrier\n\t" \
  "s_waitcnt lgkmcnt(3)\n\t" \
  "v_mfma_f32_16x16x32_bf16 v[0:3], v[64:67], v[32:35], v[0:3]\n\t" \
  "ds_read_b64_tr_b16 v[72:73], v86 offset:40960\n\t" \
  "v_mfma_f32_16x16x32_bf16 v[4:7], v[68:71], v[32:35], v[4:7]\n\t" \
  "ds_read_b64_tr_b16 v[74:75], v87 offset:40960\n\t" \
  "ds_read_b128 v[32:35], v80 offset:0\n\t" \
  "v_mfma_f32_16x16x32_bf16 v[8:11], v[64:67], v[36:39], v[8:11]\n\t" \
  "ds_read_b64_tr_b16 v[76:77], v88 offset:40960\n\t" \
  "v_mfma_f32_16x16x32_bf16 v[12:15], v[68:71], v[36:39], v[12:15]\n\t" \
  "ds_read_b64_tr_b16 v[78:79], v89 offset:40960\n\t" \
  "ds_read_b128 v[36:39], v80 offset:1024\n\t" \
  "s_add_u32 m0, %[lw], 65536\n\t" \
  "v_mfma_f32_16x16x32_bf16 v[16:19], v[64:67], v[40:43], v[16:19]\n\t" \
  "global_load_lds_dwordx4 v90, s[84:85] sc1\n\t" \
  "s_add_u32 s84, s84, s88\n\t" \
  "s_addc_u32 s85, s85, 0\n\t" \
  "v_mfma_f32_16x16x32_bf16 v[20:23], v[68:71], v[40:43], v[20:23]\n\t" \
  "ds_read_b128 v[40:43], v80 offset:2048\n\t" \
  "v_mfma_f32_16x16x32_bf16 v[24:27], v[64:67], v[44:47], v[24:27]\n\t" \
  "s_add_u32 m0, %[lw], 81920\n\t" \
  "v_mfma_f32_16x16x32_bf16 v[28:31], v[68:71], v[44:47], v[28:31]\n\t" \
  "ds_read_b128 v[44:47], v80 offset:3072\n\t" \
  "global_load_lds_dwordx4 v91, s[86:87]\n\t" \
  "s_add_u32 s86, s86, s89\n\t" \
  "s_addc_u32 s87, s87, 0\n\t" \
  "s_waitcnt lgkmcnt(3)\n\t" \
  "v_mfma_f32_16x16x32_bf16 v[0:3], v[72:75], v[48:51], v[0:3]\n\t" \
  "ds_read_b64_tr_b16 v[64:65], v82 offset:0\n\t" \
  "v_mfma_f32_16x16x32_bf16 v[4:7], v[76:79], v[48:51], v[4:7]\n\t" \
  "ds_read_b64_tr_b16 v[66:67], v83 offset:0\n\t" \
  "ds_read_b128 v[48:51], v80 offset:8192\n\t" \
  "v_mfma_f32_16x16x32_bf16 v[8:11], v[72:75], v[52:55], v[8:11]\n\t" \
  "ds_read_b64_tr_b16 v[68:69], v84 offset:0\n\t" \
  "v_mfma_f32_16x16x32_bf16 v[12:15], v[76:79], v[52:55], v[12:15]\n\t" \
  "ds_read_b64_tr_b16 v[70:71], v85 offset:0\n\t" \
  "ds_read_b128 v[52:55], v80 offset:9216\n\t" \
  "s_add_u32 m0, %[lw], 73728\n\t" \
  "v_mfma_f32_16x16x32_bf16 v[16:19], v[72:75], v[56:59], v[16:19]\n\t" \
  "global_load_lds_dwordx4 v90, s[84:85] sc1\n\t" \
  "s_add_u32 s84, s84, s88\n\t" \
  "s_addc_u32 s85, s85, 0\n\t" \
  "v_mfma_f32_16x16x32_bf16 v[20:23], v[76:79], v[56:59], v[20:23]\n\t" \
  "ds_read_b128 v[56:59], v80 offset:10240\n\t" \
  "v_mfma_f32_16x16x32_bf16 v[24:27], v[72:75], v[60:63], v[24:27]\n\t" \
  "s_add_u32 m0, %[lw], 90112\n\t" \
  "v_mfma_f32_16x16x32_bf16 v[28:31], v[76:79], v[60:63], v[28:31]\n\t" \
  "ds_read_b128 v[60:63], v80 offset:11264\n\t" \
  "global_load_lds_dwordx4 v91, s[86:87]\n\t" \
  "s_add_u32 s86, s86, s89\n\t" \
  "s_addc_u32 s87, s87, 0\n\t" \
  "s_cmp_lg_u32 s88, 0\n\t" \
  "s_cbranch_scc1 .Lp1%=\n\t" \
  ".Le%=:\n\t" \
  "s_waitcnt vmcnt(0) lgkmcnt(0)\n\t" \
  "s_mov_b32 m0, s94\n\t" \
  "s_setprio 0\n\t" \
  "s_nop 7\n\t"
#define HSL 16
__device__ __forceinline__ void job_down(const Args& p, int rblk, int ct, char* smem, __amdgpu_buffer_rsrc_t rsy) {
  if (threadIdx.x < 64) {
    for (unsigned spins = 0;;) {
      asm volatile("" ::: "memory");
      if (__builtin_amdgcn_readfirstlane((int)ld_flag0(rsy, SY_CH + (rblk >> 1) * 128)) >= 27) break;
      __builtin_amdgcn_s_sleep(HSL);
      if (spin_fail(spins, (gu32*)(p.ws + WS_SY + SY_ABORT))) break;
    }
  }
  int tid = threadIdx.x;
  asm volatile("" : "+v"(tid));
  const int lane = tid & 63, r = lane & 15, q = lane >> 4;
  const int wid = __builtin_amdgcn_readfirstlane(tid >> 6), wr = wid >> 2, wc = wid & 3;
  typedef __attribute__((ext_vector_type(32))) float f32x32;
  typedef __attribute__((ext_vector_type(8))) unsigned u32x8;
  f32x32 acc;
  {
    const char* A = p.ws + WS_HF + h_off(p, rblk);
    const char* B = (const char*)(p.wd + ct * 128);
    const unsigned rb = tid >> 4, va = tid * 16, vb = ((rb & 4) * 4 + (rb >> 3) * 4 + (rb & 3)) * (DM * 2) + 16 * ((tid & 15) ^ swzB(rb & 15));
    u32x8 lb;
#pragma unroll
    for (int i = 0; i < 8; ++i)
      lb[i] = (i >> 2) * 65536 + 16384 + offB(8 * q + 4 * (i & 1) + (r >> 2), 2 * (wc * 2 + ((i >> 1) & 1)) + ((r & 3) >> 1)) + 8 * (r & 1);
    LBAR();
#pragma unroll
    for (int t = 0; t < 3; ++t) {
      glds2<true>(A + t * 32768, 16384, va, wid * 1024 + t * 32768);
      glds2(B + t * 64 * DM * 2, 32 * DM * 2, vb, wid * 1024 + t * 32768 + 16384);
    }
    WAIT_V(8);
    LBAR();
    asm volatile(DN_ASM
        : "={v[0:31]}"(acc)
        : "{v[80:81]}"((u32x2){wr * 4096u + lane * 16u, 65536u + wr * 4096u + lane * 16u}), "{v[82:89]}"(lb), "{v90}"(va), "{v91}"(vb),
          [pa] "s"(A + 3 * 32768), [pb] "s"(B + 3 * 64 * DM * 2), [lw] "s"(wid * 1024), [hf] "s"(wr)
        : CLB8(32, 33, 34, 35, 36, 37, 38, 39), CLB8(40, 41, 42, 43, 44, 45, 46, 47), CLB8(48, 49, 50, 51, 52, 53, 54, 55),
          CLB8(56, 57, 58, 59, 60, 61, 62, 63), CLB8(64, 65, 66, 67, 68, 69, 70, 71), CLB8(72, 73, 74, 75, 76, 77, 78, 79),
          "s84", "s85", "s86", "s87", "s88", "s89", "s90", "s91", "s94", "scc", "memory");
  }
  wait_s(p, rsy, rblk, ct * 4, 4, rblk < NFUSE);
  const int e0 = (rblk * 128 + wr * 64 + r) * DM + ct * 128 + wc * 32 + q * 8;
  SX sx[4];
#pragma unroll
  for (int m = 0; m < 4; ++m) sx[m] = get_sx(p, rblk, e0 + m * 16 * DM);
  const __amdgpu_buffer_rsrc_t ro = mkrsrc(p.ws + WS_G, S_LEN * DM * 2);
#pragma unroll
  for (int m = 0; m < 4; ++m) {
    float rr[8];
#pragma unroll
    for (int j = 0; j < 8; ++j) { float d = rbf(acc[8 * m + j]); rr[j] = d * __builtin_amdgcn_rcpf(1.0f + __expf(-d)); }
    put_g(p, ro, rblk, e0 + m * 16 * DM, widen32(cvtpk(rr[0], rr[1]), cvtpk(rr[2], rr[3]), cvtpk(rr[4], rr[5]), cvtpk(rr[6], rr[7])), sx[m]);
  }
  if (rblk >= NFUSE) publish_flags(rsy, SY_FG + (rblk * 8 + ct) * 128, 0, 1);
}

#define PR_ASM_0 \
  "s_cmp_lg_u32 %[kh], 0\n\t" \
  "s_cbranch_scc1 .Lk%=\n\t" \
  "s_mov_b32 s94, m0\n\t" \
  "s_mov_b64 s[92:93], %[x0]\n\t" \
  "s_mov_b32 s86, %[g0]\n\t" \
  "s_add_u32 s87, s86, %[t0]\n\t" \
  "s_sub_i32 s88, s86, 3\n\t" \
  "ds_read_b128 v[176:179], v208 offset:0\n\t" \
  "ds_read_b128 v[180:183], v208 offset:16384\n\t" \
  "ds_read_b128 v[184:187], v209 offset:0\n\t" \
  "ds_read_b128 v[188:191], v209 offset:16384\n\t" \
  "ds_read_b128 v[192:195], v210 offset:0\n\t" \
  "ds_read_b128 v[196:199], v210 offset:16384\n\t" \
  "ds_read_b128 v[200:203], v211 offset:0\n\t" \
  "ds_read_b128 v[204:207], v211 offset:16384\n\t" \
  "s_branch .Lfb00%=\n\t" \
  ".Lp0%=:\n\t" \
  "s_cmp_le_u32 s86, 5\n\t" \
  "s_cbranch_scc1 .Lf00%=\n\t" \
  ".Lfb00%=:\n\t" \
  "s_waitcnt lgkmcnt(6)\n\t" \
  "v_mfma_f32_16x16x32_bf16 v[128:131], v[0:3], v[176:179], 0\n\t" \
  "v_mfma_f32_16x16x32_bf16 v[132:135], v[4:7], v[176:179], 0\n\t" \
  "ds_read_b128 v[176:179], v208 offset:256\n\t" \
  "s_add_u32 m0, %[lw], 67584\n\t" \
  "v_mfma_f32_16x16x32_bf16 v[136:139], v[0:3], v[180:183], 0\n\t" \
  "v_mfma_f32_16x16x32_bf16 v[140:143], v[4:7], v[180:183], 0\n\t" \
  "ds_read_b128 v[180:183], v208 offset:16640\n\t" \
  "global_load_lds_dwordx4 v218, s[92:93] sc1\n\t" \
  "s_waitcnt lgkmcnt(6)\n\t" \
  "v_mfma_f32_16x16x32_bf16 v[128:131], v[8:11], v[184:187], v[128:131]\n\t" \
  "v_mfma_f32_16x16x32_bf16 v[132:135], v[12:15], v[184:187], v[132:135]\n\t" \
  "ds_read_b128 v[184:187], v209 offset:256\n\t" \
  "v_mfma_f32_16x16x32_bf16 v[136:139], v[8:11], v[188:191], v[136:139]\n\t" \
  "v_mfma_f32_16x16x32_bf16 v[140:143], v[12:15], v[188:191], v[140:143]\n\t" \
  "ds_read_b128 v[188:191], v209 offset:16640\n\t" \
  "ds_write_b128 v220, v[152:155] offset:0\n\t" \
  "ds_write_b128 v220, v[156:159] offset:1024\n\t" \
  "s_waitcnt lgkmcnt(8)\n\t" \
  "v_mfma_f32_16x16x32_bf16 v[128:131], v[16:19], v[192:195], v[128:131]\n\t" \
  "v_mfma_f32_16x16x32_bf16 v[132:135], v[20:23], v[192:195], v[132:135]\n\t" \
  "ds_read_b128 v[192:195], v210 offset:256\n\t" \
  "s_add_u32 m0, %[lw], 68608\n\t" \
  "v_mfma_f32_16x16x32_bf16 v[136:139], v[16:19], v[196:199], v[136:139]\n\t" \
  "v_mfma_f32_16x16x32_bf16 v[140:143], v[20:23], v[196:199], v[140:143]\n\t" \
  "ds_read_b128 v[196:199], v210 offset:16640\n\t" \
  "global_load_lds_dwordx4 v219, s[92:93] sc1\n\t" \
  "s_lshr_b32 s90, s87, 7\n\t" \
  "s_xor_b32 s90, s90, %[s0]\n\t" \
  "s_and_b32 s90, s90, 1\n\t" \
  "s_lshl1_add_u32 s90, s87, s90\n\t" \
  "s_lshl_b32 s90, s90, 13\n\t" \
  "s_cmp_lt_i32 s86, 6\n\t" \
  "s_cselect_b32 s90, 0x70000000, s90\n\t" \
  "s_waitcnt lgkmcnt(8)\n\t" \
  "v_mfma_f32_16x16x32_bf16 v[128:131], v[24:27], v[200:203], v[128:131]\n\t" \
  "v_mfma_f32_16x16x32_bf16 v[132:135], v[28:31], v[200:203], v[132:135]\n\t" \
  "ds_read_b128 v[200:203], v211 offset:256\n\t" \
  "v_mfma_f32_16x16x32_bf16 v[136:139], v[24:27], v[204:207], v[136:139]\n\t" \
  "v_mfma_f32_16x16x32_bf16 v[140:143], v[28:31], v[204:207], v[140:143]\n\t" \
  "ds_read_b128 v[204:207], v211 offset:16640\n\t" \
  "s_waitcnt lgkmcnt(4)\n\t" \
  "s_cmp_lt_u32 s86, 6\n\t" \
  "s_cbranch_scc1 .Lw00%=\n\t" \
  "s_waitcnt vmcnt(4)\n\t" \
  "s_barrier\n\t" \
  ".Lg00%=:\n\t" \
  "s_add_u32 s92, s92, 512\n\t" \
  "s_addc_u32 s93, s93, 0\n\t" \
  "v_mfma_f32_16x16x32_bf16 v[128:131], v[32:35], v[176:179], v[128:131]\n\t" \
  "v_mfma_f32_16x16x32_bf16 v[132:135], v[36:39], v[176:179], v[132:135]\n\t" \
  "ds_read_b128 v[176:179], v208 offset:32768\n\t" \
  "s_add_u32 m0, %[lw], 98304\n\t" \
  "v_mfma_f32_16x16x32_bf16 v[136:139], v[32:35], v[180:183], v[136:139]\n\t" \
  "v_mfma_f32_16x16x32_bf16 v[140:143], v[36:39], v[180:183], v[140:143]\n\t" \
  "ds_read_b128 v[180:183], v208 offset:49152\n\t" \
  "global_load_lds_dwordx4 v216, s[92:93] sc1\n\t" \
  "ds_read_b128 v[160:163], v221 offset:0\n\t" \
  "ds_read_b128 v[164:167], v221 offset:1024\n\t" \
  "v_mfma_f32_16x16x32_bf16 v[128:131], v[40:43], v[184:187], v[128:131]\n\t" \
  "v_mfma_f32_16x16x32_bf16 v[132:135], v[44:47], v[184:187], v[132:135]\n\t" \
  "ds_read_b128 v[184:187], v209 offset:32768\n\t" \
  "v_mfma_f32_16x16x32_bf16 v[136:139], v[40:43], v[188:191], v[136:139]\n\t" \
  "v_mfma_f32_16x16x32_bf16 v[140:143], v[44:47], v[188:191], v[140:143]\n\t" \
  "ds_read_b128 v[188:191], v209 offset:49152\n\t" \
  "s_waitcnt lgkmcnt(8)\n\t" \
  "v_mfma_f32_16x16x32_bf16 v[128:131], v[48:51], v[192:195], v[128:131]\n\t" \
  "v_mfma_f32_16x16x32_bf16 v[132:135], v[52:55], v[192:195], v[132:135]\n\t" \
  "ds_read_b128 v[192:195], v210 offset:32768\n\t" \
  "s_add_u32 m0, %[lw], 99328\n\t" \
  "v_mfma_f32_16x16x32_bf16 v[136:139], v[48:51], v[196:199], v[136:139]\n\t" \
  "v_mfma_f32_16x16x32_bf16 v[140:143], v[52:55], v[196:199], v[140:143]\n\t" \
  "ds_read_b128 v[196:199], v210 offset:49152\n\t" \
  "global_load_lds_dwordx4 v217, s[92:93] sc1\n\t" \
  "s_waitcnt lgkmcnt(4)\n\t" \
  "v_add_f32 v160, v160, v144\n\t" \
  "v_add_f32 v161, v161, v145\n\t" \
  "v_add_f32 v162, v162, v146\n\t" \
  "v_add_f32 v163, v163, v147\n\t" \
  "v_add_f32 v164, v164, v148\n\t" \
  "v_add_f32 v165, v165, v149\n\t" \
  "v_add_f32 v166, v166, v150\n\t" \
  "v_add_f32 v167, v167, v151\n\t" \
  "v_mfma_f32_16x16x32_bf16 v[128:131], v[56:59], v[200:203], v[128:131]\n\t" \
  "v_mfma_f32_16x16x32_bf16 v[132:135], v[60:63], v[200:203], v[132:135]\n\t" \
  "ds_read_b128 v[200:203], v211 offset:32768\n\t" \
  "v_mfma_f32_16x16x32_bf16 v[136:139], v[56:59], v[204:207], v[136:139]\n\t" \
  "v_mfma_f32_16x16x32_bf16 v[140:143], v[60:63], v[204:207], v[140:143]\n\t" \
  "ds_read_b128 v[204:207], v211 offset:49152\n\t" \
  "v_cvt_pk_bf16_f32 v168, v160, v161\n\t" \
  "v_cvt_pk_bf16_f32 v169, v162, v163\n\t" \
  "v_cvt_pk_bf16_f32 v170, v164, v165\n\t" \
  "v_cvt_pk_bf16_f32 v171, v166, v167\n\t" \
  "buffer_store_dwordx4 v[168:171], v172, %[rt], s90 offen sc1\n\t" \
  "v_mfma_f32_16x16x32_bf16 v[128:131], v[64:67], v[176:179], v[128:131]\n\t" \
  "v_mfma_f32_16x16x32_bf16 v[132:135], v[68:71], v[176:179], v[132:135]\n\t" \
  "ds_read_b128 v[176:179], v208 offset:33024\n\t" \
  "s_add_u32 m0, %[lw], 100352\n\t" \
  "v_mfma_f32_16x16x32_bf16 v[136:139], v[64:67], v[180:183], v[136:139]\n\t" \
  "v_mfma_f32_16x16x32_bf16 v[140:143], v[68:71], v[180:183], v[140:143]\n\t" \
  "ds_read_b128 v[180:183], v208 offset:49408\n\t" \
  "global_load_lds_dwordx4 v218, s[92:93] sc1\n\t" \
  "s_waitcnt lgkmcnt(6)\n\t" \
  "v_mfma_f32_16x16x32_bf16 v[128:131], v[72:75], v[184:187], v[128:131]\n\t" \
  "v_mfma_f32_16x16x32_bf16 v[132:135], v[76:79], v[184:187], v[132:135]\n\t" \
  "ds_read_b128 v[184:187], v209 offset:33024\n\t" \
  "v_mfma_f32_16x16x32_bf16 v[136:139], v[72:75], v[188:191], v[136:139]\n\t" \
  "v_mfma_f32_16x16x32_bf16 v[140:143], v[76:79], v[188:191], v[140:143]\n\t" \
  "ds_read_b128 v[188:191], v209 offset:49408\n\t" \
  "s_waitcnt lgkmcnt(6)\n\t" \
  "v_mfma_f32_16x16x32_bf16 v[128:131], v[80:83], v[192:195], v[128:131]\n\t" \
  "v_mfma_f32_16x16x32_bf16 v[132:135], v[84:87], v[192:195], v[132:135]\n\t" \
  "ds_read_b128 v[192:195], v210 offset:33024\n\t" \
  "s_add_u32 m0, %[lw], 101376\n\t" \
  "v_mfma_f32_16x16x32_bf16 v[136:139], v[80:83], v[196:199], v[136:139]\n\t" \
  "v_mfma_f32_16x16x32_bf16 v[140:143], v[84:87], v[196:199], v[140:143]\n\t" \
  "ds_read_b128 v[196:199], v210 offset:49408\n\t" \
  "global_load_lds_dwordx4 v219, s[92:93] sc1\n\t" \
  "s_waitcnt lgkmcnt(6)\n\t" \
  "v_mfma_f32_16x16x32_bf16 v[128:131], v[88:91], v[200:203], v[128:131]\n\t" \
  "v_mfma_f32_16x16x32_bf16 v[132:135], v[92:95], v[200:203], v[132:135]\n\t" \
  "ds_read_b128 v[200:203], v211 offset:33024\n\t" \
  "v_mfma_f32_16x16x32_bf16 v[136:139], v[88:91], v[204:207], v[136:139]\n\t" \
  "v_mfma_f32_16x16x32_bf16 v[140:143], v[92:95], v[204:207], v[140:143]\n\t" \
  "ds_read_b128 v[204:207], v211 offset:49408\n\t" \
  "s_cmp_lt_u32 s86, 8\n\t" \
  "s_cbranch_scc1 .Lw10%=\n\t" \
  "s_waitcnt vmcnt(5)\n\t" \
  "s_barrier\n\t" \
  ".Lg10%=:\n\t" \
  "s_add_u32 s92, s92, 65024\n\t" \
  "s_addc_u32 s93, s93, 0\n\t" \
  "s_waitcnt lgkmcnt(6)\n\t" \
  "v_mfma_f32_16x16x32_bf16 v[128:131], v[96:99], v[176:179], v[128:131]\n\t" \
  "v_mfma_f32_16x16x32_bf16 v[132:135], v[100:103], v[176:179], v[132:135]\n\t" \
  "ds_read_b128 v[176:179], v212 offset:0\n\t" \
  "s_add_u32 m0, %[lw], 0\n\t" \
  "v_mfma_f32_16x16x32_bf16 v[136:139], v[96:99], v[180:183], v[136:139]\n\t" \
  "v_mfma_f32_16x16x32_bf16 v[140:143], v[100:103], v[180:183], v[140:143]\n\t" \
  "ds_read_b128 v[180:183], v212 offset:16384\n\t" \
  "global_load_lds_dwordx4 v216, s[92:93] sc1\n\t" \
  "s_waitcnt lgkmcnt(6)\n\t" \
  "v_mfma_f32_16x16x32_bf16 v[128:131], v[104:107], v[184:187], v[128:131]\n\t" \
  "v_mfma_f32_16x16x32_bf16 v[132:135], v[108:111], v[184:187], v[132:135]\n\t" \
  "ds_read_b128 v[184:187], v213 offset:0\n\t" \
  "v_mfma_f32_16x16x32_bf16 v[136:139], v[104:107], v[188:191], v[136:139]\n\t" \
  "v_mfma_f32_16x16x32_bf16 v[140:143], v[108:111], v[188:191], v[140:143]\n\t" \
  "ds_read_b128 v[188:191], v213 offset:16384\n\t" \
  "s_waitcnt lgkmcnt(6)\n\t" \
  "v_mfma_f32_16x16x32_bf16 v[128:131], v[112:115], v[192:195], v[128:131]\n\t" \
  "v_mfma_f32_16x16x32_bf16 v[132:135], v[116:119], v[192:195], v[132:135]\n\t" \
  "ds_read_b128 v[192:195], v214 offset:0\n\t" \
  "s_add_u32 m0, %[lw], 1024\n\t" \
  "v_mfma_f32_16x16x32_bf16 v[136:139], v[112:115], v[196:199], v[136:139]\n\t" \
  "v_mfma_f32_16x16x32_bf16 v[140:143], v[116:119], v[196:199], v[140:143]\n\t" \
  "ds_read_b128 v[196:199], v214 offset:16384\n\t" \
  "global_load_lds_dwordx4 v217, s[92:93] sc1\n\t" \
  "s_waitcnt lgkmcnt(6)\n\t" \
  "v_mfma_f32_16x16x32_bf16 v[128:131], v[120:123], v[200:203], v[128:131]\n\t" \
  "v_mfma_f32_16x16x32_bf16 v[132:135], v[124:127], v[200:203], v[132:135]\n\t" \
  "ds_read_b128 v[200:203], v215 offset:0\n\t" \
  "v_mfma_f32_16x16x32_bf16 v[136:139], v[120:123], v[204:207], v[136:139]\n\t" \
  "v_mfma_f32_16x16x32_bf16 v[140:143], v[124:127], v[204:207], v[140:143]\n\t" \
  "ds_read_b128 v[204:207], v215 offset:16384\n\t" \
  "s_add_u32 s86, s86, 1\n\t" \
  "s_add_u32 s87, s87, 1\n\t" \
  "s_cmp_le_u32 s86, 5\n\t" \
  "s_cbranch_scc1 .Lf10%=\n\t" \
  ".Lfb10%=:\n\t" \
  "s_waitcnt lgkmcnt(6)\n\t" \
  "v_mfma_f32_16x16x32_bf16 v[144:147], v[0:3], v[176:179], 0\n\t" \
  "v_mfma_f32_16x16x32_bf16 v[148:151], v[4:7], v[176:179], 0\n\t" \
  "ds_read_b128 v[176:179], v212 offset:256\n\t" \
  "s_add_u32 m0, %[lw], 2048\n\t" \
  "v_mfma_f32_16x16x32_bf16 v[152:155], v[0:3], v[180:183], 0\n\t" \
  "v_mfma_f32_16x16x32_bf16 v[156:159], v[4:7], v[180:183], 0\n\t" \
  "ds_read_b128 v[180:183], v212 offset:16640\n\t" \
  "global_load_lds_dwordx4 v218, s[92:93] sc1\n\t" \
  "s_waitcnt lgkmcnt(6)\n\t" \
  "v_mfma_f32_16x16x32_bf16 v[144:147], v[8:11], v[184:187], v[144:147]\n\t" \
  "v_mfma_f32_16x16x32_bf16 v[148:151], v[12:15], v[184:187], v[148:151]\n\t" \
  "ds_read_b128 v[184:187], v213 offset:256\n\t" \
  "v_mfma_f32_16x16x32_bf16 v[152:155], v[8:11], v[188:191], v[152:155]\n\t" \
  "v_mfma_f32_16x16x32_bf16 v[156:159], v[12:15], v[188:191], v[156:159]\n\t" \
  "ds_read_b128 v[188:191], v213 offset:16640\n\t" \
  "ds_write_b128 v220, v[136:139] offset:0\n\t" \
  "ds_write_b128 v220, v[140:143] offset:1024\n\t" \
  "s_waitcnt lgkmcnt(8)\n\t" \
  "v_mfma_f32_16x16x32_bf16 v[144:147], v[16:19], v[192:195], v[144:147]\n\t" \
  "v_mfma_f32_16x16x32_bf16 v[148:151], v[20:23], v[192:195], v[148:151]\n\t" \
  "ds_read_b128 v[192:195], v214 offset:256\n\t" \
  "s_add_u32 m0, %[lw], 3072\n\t" \
  "v_mfma_f32_16x16x32_bf16 v[152:155], v[16:19], v[196:199], v[152:155]\n\t" \
  "v_mfma_f32_16x16x32_bf16 v[156:159], v[20:23], v[196:199], v[156:159]\n\t" \
  "ds_read_b128 v[196:199], v214 offset:16640\n\t" \
  "global_load_lds_dwordx4 v219, s[92:93] sc1\n\t" \
  "s_lshr_b32 s90, s87, 7\n\t" \
  "s_xor_b32 s90, s90, %[s0]\n\t" \
  "s_and_b32 s90, s90, 1\n\t" \
  "s_lshl1_add_u32 s90, s87, s90\n\t" \
  "s_lshl_b32 s90, s90, 13\n\t" \
  "s_cmp_lt_i32 s86, 6\n\t" \
  "s_cselect_b32 s90, 0x70000000, s90\n\t" \
  "s_waitcnt lgkmcnt(8)\n\t" \
  "v_mfma_f32_16x16x32_bf16 v[144:147], v[24:27], v[200:203], v[144:147]\n\t" \
  "v_mfma_f32_16x16x32_bf16 v[148:151], v[28:31], v[200:203], v[148:151]\n\t" \
  "ds_read_b128 v[200:203], v215 offset:256\n\t" \
  "v_mfma_f32_16x16x32_bf16 v[152:155], v[24:27], v[204:207], v[152:155]\n\t" \
  "v_mfma_f32_16x16x32_bf16 v[156:159], v[28:31], v[204:207], v[156:159]\n\t" \
  "ds_read_b128 v[204:207], v215 offset:16640\n\t" \
  "s_waitcnt lgkmcnt(4)\n\t" \
  "s_cmp_lt_u32 s86, 6\n\t" \
  "s_cbranch_scc1 .Lw20%=\n\t" \
  "s_waitcnt vmcnt(4)\n\t" \
  "s_barrier\n\t" \
  ".Lg20%=:\n\t" \
  "s_add_u32 s92, s92, 512\n\t" \
  "s_addc_u32 s93, s93, 0\n\t" \
  "v_mfma_f32_16x16x32_bf16 v[144:147], v[32:35], v[176:179], v[144:147]\n\t" \
  "v_mfma_f32_16x16x32_bf16 v[148:151], v[36:39], v[176:179], v[148:151]\n\t" \
  "ds_read_b128 v[176:179], v212 offset:32768\n\t" \
  "s_add_u32 m0, %[lw], 32768\n\t" \
  "v_mfma_f32_16x16x32_bf16 v[152:155], v[32:35], v[180:183], v[152:155]\n\t" \
  "v_mfma_f32_16x16x32_bf16 v[156:159], v[36:39], v[180:183], v[156:159]\n\t" \
  "ds_read_b128 v[180:183], v212 offset:49152\n\t" \
  "global_load_lds_dwordx4 v216, s[92:93] sc1\n\t" \
  "ds_read_b128 v[160:163], v221 offset:0\n\t" \
  "ds_read_b128 v[164:167], v221 offset:1024\n\t" \
  "v_mfma_f32_16x16x32_bf16 v[144:147], v[40:43], v[184:187], v[144:147]\n\t" \
  "v_mfma_f32_16x16x32_bf16 v[148:151], v[44:47], v[184:187], v[148:151]\n\t" \
  "ds_read_b128 v[184:187], v213 offset:32768\n\t" \
  "v_mfma_f32_16x16x32_bf16 v[152:155], v[40:43], v[188:191], v[152:155]\n\t" \
  "v_mfma_f32_16x16x32_bf16 v[156:159], v[44:47], v[188:191], v[156:159]\n\t" \
  "ds_read_b128 v[188:191], v213 offset:49152\n\t" \
  "s_waitcnt lgkmcnt(8)\n\t" \
  "v_mfma_f32_16x16x32_bf16 v[144:147], v[48:51], v[192:195], v[144:147]\n\t" \
  "v_mfma_f32_16x16x32_bf16 v[148:151], v[52:55], v[192:195], v[148:151]\n\t" \
  "ds_read_b128 v[192:195], v214 offset:32768\n\t" \
  "s_add_u32 m0, %[lw], 33792\n\t" \
  "v_mfma_f32_16x16x32_bf16 v[152:155], v[48:51], v[196:199], v[152:155]\n\t" \
  "v_mfma_f32_16x16x32_bf16 v[156:159], v[52:55], v[196:199], v[156:159]\n\t" \
  "ds_read_b128 v[196:199], v214 offset:49152\n\t" \
  "global_load_lds_dwordx4 v217, s[92:93] sc1\n\t" \
  "s_waitcnt lgkmcnt(4)\n\t" \
  "v_add_f32 v160, v160, v128\n\t" \
  "v_add_f32 v161, v161, v129\n\t" \
  "v_add_f32 v162, v162, v130\n\t" \
  "v_add_f32 v163, v163, v131\n\t" \
  "v_add_f32 v164, v164, v132\n\t" \
  "v_add_f32 v165, v165, v133\n\t" \
  "v_add_f32 v166, v166, v134\n\t" \
  "v_add_f32 v167, v167, v135\n\t" \
  "v_mfma_f32_16x16x32_bf16 v[144:147], v[56:59], v[200:203], v[144:147]\n\t" \
  "v_mfma_f32_16x16x32_bf16 v[148:151], v[60:63], v[200:203], v[148:151]\n\t" \
  "ds_read_b128 v[200:203], v215 offset:32768\n\t" \
  "v_mfma_f32_16x16x32_bf16 v[152:155], v[56:59], v[204:207], v[152:155]\n\t" \
  "v_mfma_f32_16x16x32_bf16 v[156:159], v[60:63], v[204:207], v[156:159]\n\t" \
  "ds_read_b128 v[204:207], v215 offset:49152\n\t" \
  "v_cvt_pk_bf16_f32 v168, v160, v161\n\t" \
  "v_cvt_pk_bf16_f32 v169, v162, v163\n\t" \
  "v_cvt_pk_bf16_f32 v170, v164, v165\n\t" \
  "v_cvt_pk_bf16_f32 v171, v166, v167\n\t" \
  "buffer_store_dwordx4 v[168:171], v172, %[rt], s90 offen sc1\n\t" \
  "v_mfma_f32_16x16x32_bf16 v[144:147], v[64:67], v[176:179], v[144:147]\n\t" \
  "v_mfma_f32_16x16x32_bf16 v[148:151], v[68:71], v[176:179], v[148:151]\n\t" \
  "ds_read_b128 v[176:179], v212 offset:33024\n\t" \
  "s_add_u32 m0, %[lw], 34816\n\t" \
  "v_mfma_f32_16x16x32_bf16 v[152:155], v[64:67], v[180:183], v[152:155]\n\t" \
  "v_mfma_f32_16x16x32_bf16 v[156:159], v[68:71], v[180:183], v[156:159]\n\t" \
  "ds_read_b128 v[180:183], v212 offset:49408\n\t" \
  "global_load_lds_dwordx4 v218, s[92:93] sc1\n\t" \
  "s_waitcnt lgkmcnt(6)\n\t" \
  "v_mfma_f32_16x16x32_bf16 v[144:147], v[72:75], v[184:187], v[144:147]\n\t" \
  "v_mfma_f32_16x16x32_bf16 v[148:151], v[76:79], v[184:187], v[148:151]\n\t" \
  "ds_read_b128 v[184:187], v213 offset:33024\n\t" \
  "v_mfma_f32_16x16x32_bf16 v[152:155], v[72:75], v[188:191], v[152:155]\n\t" \
  "v_mfma_f32_16x16x32_bf16 v[156:159], v[76:79], v[188:191], v[156:159]\n\t" \
  "ds_read_b128 v[188:191], v213 offset:49408\n\t" \
  "s_waitcnt lgkmcnt(6)\n\t" \
  "v_mfma_f32_16x16x32_bf16 v[144:147], v[80:83], v[192:195], v[144:147]\n\t" \
  "v_mfma_f32_16x16x32_bf16 v[148:151], v[84:87], v[192:195], v[148:151]\n\t" \
  "ds_read_b128 v[192:195], v214 offset:33024\n\t" \
  "s_add_u32 m0, %[lw], 35840\n\t" \
  "v_mfma_f32_16x16x32_bf16 v[152:155], v[80:83], v[196:199], v[152:155]\n\t" \
  "v_mfma_f32_16x16x32_bf16 v[156:159], v[84:87], v[196:199], v[156:159]\n\t" \
  "ds_read_b128 v[196:199], v214 offset:49408\n\t" \
  "global_load_lds_dwordx4 v219, s[92:93] sc1\n\t" \
  "s_waitcnt lgkmcnt(6)\n\t" \
  "v_mfma_f32_16x16x32_bf16 v[144:147], v[88:91], v[200:203], v[144:147]\n\t" \
  "v_mfma_f32_16x16x32_bf16 v[148:151], v[92:95], v[200:203], v[148:151]\n\t" \
  "ds_read_b128 v[200:203], v215 offset:33024\n\t" \
  "v_mfma_f32_16x16x32_bf16 v[152:155], v[88:91], v[204:207], v[152:155]\n\t" \
  "v_mfma_f32_16x16x32_bf16 v[156:159], v[92:95], v[204:207], v[156:159]\n\t" \
  "ds_read_b128 v[204:207], v215 offset:49408\n\t" \
  "s_cmp_lt_u32 s86, 8\n\t" \
  "s_cbranch_scc1 .Lw30%=\n\t" \
  "s_waitcnt vmcnt(5)\n\t" \
  "s_barrier\n\t" \
  ".Lg30%=:\n\t" \
  "s_add_u32 s92, s92, 65024\n\t" \
  "s_addc_u32 s93, s93, 0\n\t" \
  "s_waitcnt lgkmcnt(6)\n\t" \
  "v_mfma_f32_16x16x32_bf16 v[144:147], v[96:99], v[176:179], v[144:147]\n\t" \
  "v_mfma_f32_16x16x32_bf16 v[148:151], v[100:103], v[176:179], v[148:151]\n\t" \
  "ds_read_b128 v[176:179], v208 offset:0\n\t" \
  "s_add_u32 m0, %[lw], 65536\n\t" \
  "v_mfma_f32_16x16x32_bf16 v[152:155], v[96:99], v[180:183], v[152:155]\n\t" \
  "v_mfma_f32_16x16x32_bf16 v[156:159], v[100:103], v[180:183], v[156:159]\n\t" \
  "ds_read_b128 v[180:183], v208 offset:16384\n\t" \
  "global_load_lds_dwordx4 v216, s[92:93] sc1\n\t" \
  "s_waitcnt lgkmcnt(6)\n\t" \
  "v_mfma_f32_16x16x32_bf16 v[144:147], v[104:107], v[184:187], v[144:147]\n\t" \
  "v_mfma_f32_16x16x32_bf16 v[148:151], v[108:111], v[184:187], v[148:151]\n\t" \
  "ds_read_b128 v[184:187], v209 offset:0\n\t" \
  "v_mfma_f32_16x16x32_bf16 v[152:155], v[104:107], v[188:191], v[152:155]\n\t" \
  "v_mfma_f32_16x16x32_bf16 v[156:159], v[108:111], v[188:191], v[156:159]\n\t" \
  "ds_read_b128 v[188:191], v209 offset:16384\n\t" \
  "s_waitcnt lgkmcnt(6)\n\t" \
  "v_mfma_f32_16x16x32_bf16 v[144:147], v[112:115], v[192:195], v[144:147]\n\t" \
  "v_mfma_f32_16x16x32_bf16 v[148:151], v[116:119], v[192:195], v[148:151]\n\t" \
  "ds_read_b128 v[192:195], v210 offset:0\n\t" \
  "s_add_u32 m0, %[lw], 66560\n\t" \
  "v_mfma_f32_16x16x32_bf16 v[152:155], v[112:115], v[196:199], v[152:155]\n\t" \
  "v_mfma_f32_16x16x32_bf16 v[156:159], v[116:119], v[196:199], v[156:159]\n\t" \
  "ds_read_b128 v[196:199], v210 offset:16384\n\t" \
  "global_load_lds_dwordx4 v217, s[92:93] sc1\n\t" \
  "s_waitcnt lgkmcnt(6)\n\t" \
  "v_mfma_f32_16x16x32_bf16 v[144:147], v[120:123], v[200:203], v[144:147]\n\t" \
  "v_mfma_f32_16x16x32_bf16 v[148:151], v[124:127], v[200:203], v[148:151]\n\t" \
  "ds_read_b128 v[200:203], v211 offset:0\n\t" \
  "v_mfma_f32_16x16x32_bf16 v[152:155], v[120:123], v[204:207], v[152:155]\n\t" \
  "v_mfma_f32_16x16x32_bf16 v[156:159], v[124:127], v[204:207], v[156:159]\n\t" \
  "ds_read_b128 v[204:207], v211 offset:16384\n\t" \
  "s_add_u32 s86, s86, 1\n\t" \
  "s_add_u32 s87, s87, 1\n\t" \
  "s_cmp_lt_u32 s86, %[end]\n\t" \
  "s_cbranch_scc1 .Lp0%=\n\t" \
  "s_branch .Lx0%=\n\t" \
  ".Lf00%=:\n\t" \
  "s_nop 7\n\t" \
  "ds_write_b128 v220, v[152:155] offset:0\n\t" \
  "ds_write_b128 v220, v[156:159] offset:1024\n\t" \
  "s_lshr_b32 s90, s87, 7\n\t" \
  "s_xor_b32 s90, s90, %[s0]\n\t" \
  "s_and_b32 s90, s90, 1\n\t" \
  "s_lshl1_add_u32 s90, s87, s90\n\t" \
  "s_lshl_b32 s90, s90, 13\n\t" \
  "s_cmp_lt_i32 s86, 1\n\t" \
  "s_cselect_b32 s90, 0x70000000, s90\n\t" \
  "s_waitcnt lgkmcnt(0)\n\t" \
  "s_barrier\n\t" \
  "ds_read_b128 v[160:163], v221 offset:0\n\t" \
  "ds_read_b128 v[164:167], v221 offset:1024\n\t" \
  "s_waitcnt lgkmcnt(0)\n\t" \
  "v_add_f32 v160, v160, v144\n\t" \
  "v_add_f32 v161, v161, v145\n\t" \
  "v_add_f32 v162, v162, v146\n\t" \
  "v_add_f32 v163, v163, v147\n\t" \
  "v_add_f32 v164, v164, v148\n\t" \
  "v_add_f32 v165, v165, v149\n\t" \
  "v_add_f32 v166, v166, v150\n\t" \
  "v_add_f32 v167, v167, v151\n\t" \
  "v_cvt_pk_bf16_f32 v168, v160, v161\n\t" \
  "v_cvt_pk_bf16_f32 v169, v162, v163\n\t" \
  "v_cvt_pk_bf16_f32 v170, v164, v165\n\t" \
  "v_cvt_pk_bf16_f32 v171, v166, v167\n\t" \
  "buffer_store_dwordx4 v[168:171], v172, %[rt], s90 offen sc1\n\t" \
  "s_branch .Lfb00%=\n\t" \
  ".Lf10%=:\n\t" \
  "s_nop 7\n\t" \
  "ds_write_b128 v220, v[136:139] offset:0\n\t" \
  "ds_write_b128 v220, v[140:143] offset:1024\n\t" \
  "s_lshr_b32 s90, s87, 7\n\t" \
  "s_xor_b32 s90, s90, %[s0]\n\t" \
  "s_and_b32 s90, s90, 1\n\t" \
  "s_lshl1_add_u32 s90, s87, s90\n\t" \
  "s_lshl_b32 s90, s90, 13\n\t" \
  "s_cmp_lt_i32 s86, 1\n\t" \
  "s_cselect_b32 s90, 0x70000000, s90\n\t" \
  "s_waitcnt lgkmcnt(0)\n\t" \
  "s_barrier\n\t" \
  "ds_read_b128 v[160:163], v221 offset:0\n\t" \
  "ds_read_b128 v[164:167], v221 offset:1024\n\t" \
  "s_waitcnt lgkmcnt(0)\n\t" \
  "v_add_f32 v160, v160, v128\n\t" \
  "v_add_f32 v161, v161, v129\n\t" \
  "v_add_f32 v162, v162, v130\n\t" \
  "v_add_f32 v163, v163, v131\n\t" \
  "v_add_f32 v164, v164, v132\n\t" \
  "v_add_f32 v165, v165, v133\n\t" \
  "v_add_f32 v166, v166, v134\n\t" \
  "v_add_f32 v167, v167, v135\n\t" \
  "v_cvt_pk_bf16_f32 v168, v160, v161\n\t" \
  "v_cvt_pk_bf16_f32 v169, v162, v163\n\t" \
  "v_cvt_pk_bf16_f32 v170, v164, v165\n\t" \
  "v_cvt_pk_bf16_f32 v171, v166, v167\n\t" \
  "buffer_store_dwordx4 v[168:171], v172, %[rt], s90 offen sc1\n\t" \
  "s_branch .Lfb10%=\n\t" \
  ".Lw00%=:\n\t" \
  "s_waitcnt vmcnt(2)\n\t" \
  "s_barrier\n\t" \
  "s_branch .Lg00%=\n\t" \
  ".Lw10%=:\n\t" \
  "s_waitcnt vmcnt(2)\n\t" \
  "s_barrier\n\t" \
  "s_branch .Lg10%=\n\t" \
  ".Lw20%=:\n\t" \
  "s_waitcnt vmcnt(2)\n\t" \
  "s_barrier\n\t" \
  "s_branch .Lg20%=\n\t" \
  ".Lw30%=:\n\t" \
  "s_waitcnt vmcnt(2)\n\t" \
  "s_barrier\n\t" \
  "s_branch .Lg30%=\n\t" \
  ".Lx0%=:\n\t" \
  "s_waitcnt lgkmcnt(0)\n\t" \
  "s_setprio 0\n\t" \
  "s_mov_b32 m0, s94\n\t" \
  "s_branch .Le%=\n\t" \
  ".Lk%=:\n\t" \
  "s_mov_b32 s94, m0\n\t" \
  "s_mov_b64 s[92:93], %[x0]\n\t" \
  "s_mov_b32 s86, %[g0]\n\t" \
  "s_add_u32 s87, s86, %[t0]\n\t" \
  "s_sub_i32 s88, s86, 3\n\t" \
  "ds_read_b128 v[176:179], v208 offset:0\n\t" \
  "ds_read_b128 v[180:183], v208 offset:16384\n\t" \
  "ds_read_b128 v[184:187], v209 offset:0\n\t" \
  "ds_read_b128 v[188:191], v209 offset:16384\n\t" \
  "ds_read_b128 v[192:195], v210 offset:0\n\t" \
  "ds_read_b128 v[196:199], v210 offset:16384\n\t" \
  "ds_read_b128 v[200:203], v211 offset:0\n\t" \
  "ds_read_b128 v[204:207], v211 offset:16384\n\t" \
  "s_branch .Lfb01%=\n\t" \
  ".Lp1%=:\n\t" \
  "s_cmp_le_u32 s86, 5\n\t" \
  "s_cbranch_scc1 .Lf01%=\n\t" \
  ".Lfb01%=:\n\t" \
  "s_waitcnt lgkmcnt(6)\n\t" \
  "v_mfma_f32_16x16x32_bf16 v[128:131], v[0:3], v[176:179], 0\n\t" \
  "v_mfma_f32_16x16x32_bf16 v[132:135], v[4:7], v[176:179], 0\n\t" \
  "ds_read_b128 v[176:179], v208 offset:256\n\t" \
  "v_mfma_f32_16x16x32_bf16 v[136:139], v[0:3], v[180:183], 0\n\t" \
  "v_mfma_f32_16x16x32_bf16 v[140:143], v[4:7], v[180:183], 0\n\t" \
  "ds_read_b128 v[180:183], v208 offset:16640\n\t" \
  "s_waitcnt lgkmcnt(6)\n\t" \
  "v_mfma_f32_16x16x32_bf16 v[128:131], v[8:11], v[184:187], v[128:131]\n\t" \
  "v_mfma_f32_16x16x32_bf16 v[132:135], v[12:15], v[184:187], v[132:135]\n\t" \
  "ds_read_b128 v[184:187], v209 offset:256\n\t" \
  "s_add_u32 m0, %[lw], 67584\n\t" \
  "v_mfma_f32_16x16x32_bf16 v[136:139], v[8:11], v[188:191], v[136:139]\n\t" \
  "v_mfma_f32_16x16x32_bf16 v[140:143], v[12:15], v[188:191], v[140:143]\n\t" \
  "ds_read_b128 v[188:191], v209 offset:16640\n\t" \
  "global_load_lds_dwordx4 v218, s[92:93] sc1\n\t" \
  "ds_write_b128 v220, v[152:155] offset:0\n\t" \
  "ds_write_b128 v220, v[156:159] offset:1024\n\t" \
  "s_waitcnt lgkmcnt(8)\n\t" \
  "v_mfma_f32_16x16x32_bf16 v[128:131], v[16:19], v[192:195], v[128:131]\n\t" \
  "v_mfma_f32_16x16x32_bf16 v[132:135], v[20:23], v[192:195], v[132:135]\n\t" \
  "ds_read_b128 v[192:195], v210 offset:256\n\t" \
  "v_mfma_f32_16x16x32_bf16 v[136:139], v[16:19], v[196:199], v[136:139]\n\t" \
  "v_mfma_f32_16x16x32_bf16 v[140:143], v[20:23], v[196:199], v[140:143]\n\t" \
  "ds_read_b128 v[196:199], v210 offset:16640\n\t" \
  "s_lshr_b32 s90, s87, 7\n\t" \
  "s_xor_b32 s90, s90, %[s0]\n\t" \
  "s_and_b32 s90, s90, 1\n\t" \
  "s_lshl1_add_u32 s90, s87, s90\n\t" \
  "s_lshl_b32 s90, s90, 13\n\t" \
  "s_cmp_lt_i32 s86, 6\n\t" \
  "s_cselect_b32 s90, 0x70000000, s90\n\t" \
  "s_waitcnt lgkmcnt(8)\n\t" \
  "v_mfma_f32_16x16x32_bf16 v[128:131], v[24:27], v[200:203], v[128:131]\n\t" \
  "v_mfma_f32_16x16x32_bf16 v[132:135], v[28:31], v[200:203], v[132:135]\n\t" \
  "ds_read_b128 v[200:203], v211 offset:256\n\t" \
  "s_add_u32 m0, %[lw], 68608\n\t" \
  "v_mfma_f32_16x16x32_bf16 v[136:139], v[24:27], v[204:207], v[136:139]\n\t" \
  "v_mfma_f32_16x16x32_bf16 v[140:143], v[28:31], v[204:207], v[140:143]\n\t" \
  "ds_read_b128 v[204:207], v211 offset:16640\n\t" \
  "global_load_lds_dwordx4 v219, s[92:93] sc1\n\t" \
  "s_waitcnt lgkmcnt(4)\n\t" \
  "s_cmp_lt_u32 s86, 6\n\t" \
  "s_cbranch_scc1 .Lw01%=\n\t" \
  "s_waitcnt vmcnt(4)\n\t" \
  "s_barrier\n\t" \
  ".Lg01%=:\n\t" \
  "s_add_u32 s92, s92, 512\n\t" \
  "s_addc_u32 s93, s93, 0\n\t" \
  "v_mfma_f32_16x16x32_bf16 v[128:131], v[32:35], v[176:179], v[128:131]\n\t" \
  "v_mfma_f32_16x16x32_bf16 v[132:135], v[36:39], v[176:179], v[132:135]\n\t" \
  "ds_read_b128 v[176:179], v208 offset:32768\n\t" \
  "v_mfma_f32_16x16x32_bf16 v[136:139], v[32:35], v[180:183], v[136:139]\n\t" \
  "v_mfma_f32_16x16x32_bf16 v[140:143], v[36:39], v[180:183], v[140:143]\n\t" \
  "ds_read_b128 v[180:183], v208 offset:49152\n\t" \
  "ds_read_b128 v[160:163], v221 offset:0\n\t" \
  "ds_read_b128 v[164:167], v221 offset:1024\n\t" \
  "s_sub_i32 s91, s86, 2\n\t" \
  "s_max_i32 s88, s88, s91\n\t" \
  "v_mov_b32 v173, s88\n\t" \
  "buffer_store_dword v173, v174, %[rs], 0 offen sc1\n\t" \
  "v_mfma_f32_16x16x32_bf16 v[128:131], v[40:43], v[184:187], v[128:131]\n\t" \
  "v_mfma_f32_16x16x32_bf16 v[132:135], v[44:47], v[184:187], v[132:135]\n\t" \
  "ds_read_b128 v[184:187], v209 offset:32768\n\t" \
  "s_add_u32 m0, %[lw], 98304\n\t" \
  "v_mfma_f32_16x16x32_bf16 v[136:139], v[40:43], v[188:191], v[136:139]\n\t" \
  "v_mfma_f32_16x16x32_bf16 v[140:143], v[44:47], v[188:191], v[140:143]\n\t" \
  "ds_read_b128 v[188:191], v209 offset:49152\n\t" \
  "global_load_lds_dwordx4 v216, s[92:93] sc1\n\t" \
  "s_waitcnt lgkmcnt(8)\n\t" \
  "v_mfma_f32_16x16x32_bf16 v[128:131], v[48:51], v[192:195], v[128:131]\n\t" \
  "v_mfma_f32_16x16x32_bf16 v[132:135], v[52:55], v[192:195], v[132:135]\n\t" \
  "ds_read_b128 v[192:195], v210 offset:32768\n\t" \
  "v_mfma_f32_16x16x32_bf16 v[136:139], v[48:51], v[196:199], v[136:139]\n\t" \
  "v_mfma_f32_16x16x32_bf16 v[140:143], v[52:55], v[196:199], v[140:143]\n\t" \
  "ds_read_b128 v[196:199], v210 offset:49152\n\t" \
  "s_waitcnt lgkmcnt(4)\n\t" \
  "v_add_f32 v160, v160, v144\n\t" \
  "v_add_f32 v161, v161, v145\n\t" \
  "v_add_f32 v162, v162, v146\n\t" \
  "v_add_f32 v163, v163, v147\n\t" \
  "v_add_f32 v164, v164, v148\n\t" \
  "v_add_f32 v165, v165, v149\n\t" \
  "v_add_f32 v166, v166, v150\n\t" \
  "v_add_f32 v167, v167, v151\n\t" \
  "v_mfma_f32_16x16x32_bf16 v[128:131], v[56:59], v[200:203], v[128:131]\n\t" \
  "v_mfma_f32_16x16x32_bf16 v[132:135], v[60:63], v[200:203], v[132:135]\n\t" \
  "ds_read_b128 v[200:203], v211 offset:32768\n\t" \
  "s_add_u32 m0, %[lw], 99328\n\t" \
  "v_mfma_f32_16x16x32_bf16 v[136:139], v[56:59], v[204:207], v[136:139]\n\t" \
  "v_mfma_f32_16x16x32_bf16 v[140:143], v[60:63], v[204:207], v[140:143]\n\t" \
  "ds_read_b128 v[204:207], v211 offset:49152\n\t" \
  "global_load_lds_dwordx4 v217, s[92:93] sc1\n\t" \
  "v_cvt_pk_bf16_f32 v168, v160, v161\n\t" \
  "v_cvt_pk_bf16_f32 v169, v162, v163\n\t" \
  "v_cvt_pk_bf16_f32 v170, v164, v165\n\t" \
  "v_cvt_pk_bf16_f32 v171, v166, v167\n\t" \
  "buffer_store_dwordx4 v[168:171], v172, %[rt], s90 offen sc1\n\t" \
  "v_mfma_f32_16x16x32_bf16 v[128:131], v[64:67], v[176:179], v[128:131]\n\t" \
  "v_mfma_f32_16x16x32_bf16 v[132:135], v[68:71], v[176:179], v[132:135]\n\t" \
  "ds_read_b128 v[176:179], v208 offset:33024\n\t" \
  "v_mfma_f32_16x16x32_bf16 v[136:139], v[64:67], v[180:183], v[136:139]\n\t" \
  "v_mfma_f32_16x16x32_bf16 v[140:143], v[68:71], v[180:183], v[140:143]\n\t" \
  "ds_read_b128 v[180:183], v208 offset:49408\n\t" \
  "s_waitcnt lgkmcnt(6)\n\t" \
  "v_mfma_f32_16x16x32_bf16 v[128:131], v[72:75], v[184:187], v[128:131]\n\t" \
  "v_mfma_f32_16x16x32_bf16 v[132:135], v[76:79], v[184:187], v[132:135]\n\t" \
  "ds_read_b128 v[184:187], v209 offset:33024\n\t" \
  "s_add_u32 m0, %[lw], 100352\n\t" \
  "v_mfma_f32_16x16x32_bf16 v[136:139], v[72:75], v[188:191], v[136:139]\n\t" \
  "v_mfma_f32_16x16x32_bf16 v[140:143], v[76:79], v[188:191], v[140:143]\n\t" \
  "ds_read_b128 v[188:191], v209 offset:49408\n\t" \
  "global_load_lds_dwordx4 v218, s[92:93] sc1\n\t" \
  "s_waitcnt lgkmcnt(6)\n\t" \
  "v_mfma_f32_16x16x32_bf16 v[128:131], v[80:83], v[192:195], v[128:131]\n\t" \
  "v_mfma_f32_16x16x32_bf16 v[132:135], v[84:87], v[192:195], v[132:135]\n\t" \
  "ds_read_b128 v[192:195], v210 offset:33024\n\t" \
  "v_mfma_f32_16x16x32_bf16 v[136:139], v[80:83], v[196:199], v[136:139]\n\t" \
  "v_mfma_f32_16x16x32_bf16 v[140:143], v[84:87], v[196:199], v[140:143]\n\t" \
  "ds_read_b128 v[196:199], v210 offset:49408\n\t" \
  "s_waitcnt lgkmcnt(6)\n\t" \
  "v_mfma_f32_16x16x32_bf16 v[128:131], v[88:91], v[200:203], v[128:131]\n\t" \
  "v_mfma_f32_16x16x32_bf16 v[132:135], v[92:95], v[200:203], v[132:135]\n\t" \
  "ds_read_b128 v[200:203], v211 offset:33024\n\t" \
  "s_add_u32 m0, %[lw], 101376\n\t" \
  "v_mfma_f32_16x16x32_bf16 v[136:139], v[88:91], v[204:207], v[136:139]\n\t" \
  "v_mfma_f32_16x16x32_bf16 v[140:143], v[92:95], v[204:207], v[140:143]\n\t" \
  "ds_read_b128 v[204:207], v211 offset:49408\n\t" \
  "global_load_lds_dwordx4 v219, s[92:93] sc1\n\t" \
  "s_cmp_lt_u32 s86, 8\n\t" \
  "s_cbranch_scc1 .Lw11%=\n\t" \
  "s_waitcnt vmcnt(6)\n\t" \
  "s_barrier\n\t" \
  ".Lg11%=:\n\t" \
  "s_add_u32 s92, s92, 65024\n\t" \
  "s_addc_u32 s93, s93, 0\n\t" \
  "s_waitcnt lgkmcnt(6)\n\t" \
  "v_mfma_f32_16x16x32_bf16 v[128:131], v[96:99], v[176:179], v[128:131]\n\t" \
  "v_mfma_f32_16x16x32_bf16 v[132:135], v[100:103], v[176:179], v[132:135]\n\t" \
  "ds_read_b128 v[176:179], v212 offset:0\n\t" \
  "v_mfma_f32_16x16x32_bf16 v[136:139], v[96:99], v[180:183], v[136:139]\n\t" \
  "v_mfma_f32_16x16x32_bf16 v[140:143], v[100:103], v[180:183], v[140:143]\n\t" \
  "ds_read_b128 v[180:183], v212 offset:16384\n\t" \
  "s_waitcnt lgkmcnt(6)\n\t" \
  "v_mfma_f32_16x16x32_bf16 v[128:131], v[104:107], v[184:187], v[128:131]\n\t" \
  "v_mfma_f32_16x16x32_bf16 v[132:135], v[108:111], v[184:187], v[132:135]\n\t" \
  "ds_read_b128 v[184:187], v213 offset:0\n\t" \
  "s_add_u32 m0, %[lw], 0\n\t" \
  "v_mfma_f32_16x16x32_bf16 v[136:139], v[104:107], v[188:191], v[136:139]\n\t" \
  "v_mfma_f32_16x16x32_bf16 v[140:143], v[108:111], v[188:191], v[140:143]\n\t" \
  "ds_read_b128 v[188:191], v213 offset:16384\n\t" \
  "global_load_lds_dwordx4 v216, s[92:93] sc1\n\t" \
  "s_waitcnt lgkmcnt(6)\n\t" \
  "v_mfma_f32_16x16x32_bf16 v[128:131], v[112:115], v[192:195], v[128:131]\n\t" \
  "v_mfma_f32_16x16x32_bf16 v[132:135], v[116:119], v[192:195], v[132:135]\n\t" \
  "ds_read_b128 v[192:195], v214 offset:0\n\t" \
  "v_mfma_f32_16x16x32_bf16 v[136:139], v[112:115], v[196:199], v[136:139]\n\t" \
  "v_mfma_f32_16x16x32_bf16 v[140:143], v[116:119], v[196:199], v[140:143]\n\t" \
  "ds_read_b128 v[196:199], v214 offset:16384\n\t" \
  "s_waitcnt lgkmcnt(6)\n\t" \
  "v_mfma_f32_16x16x32_bf16 v[128:131], v[120:123], v[200:203], v[128:131]\n\t" \
  "v_mfma_f32_16x16x32_bf16 v[132:135], v[124:127], v[200:203], v[132:135]\n\t" \
  "ds_read_b128 v[200:203], v215 offset:0\n\t" \
  "s_add_u32 m0, %[lw], 1024\n\t" \
  "v_mfma_f32_16x16x32_bf16 v[136:139], v[120:123], v[204:207], v[136:139]\n\t" \
  "v_mfma_f32_16x16x32_bf16 v[140:143], v[124:127], v[204:207], v[140:143]\n\t" \
  "ds_read_b128 v[204:207], v215 offset:16384\n\t" \
  "global_load_lds_dwordx4 v217, s[92:93] sc1\n\t" \
  "s_add_u32 s86, s86, 1\n\t" \
  "s_add_u32 s87, s87, 1\n\t" \
  "s_cmp_le_u32 s86, 5\n\t" \
  "s_cbranch_scc1 .Lf11%=\n\t" \
  ".Lfb11%=:\n\t" \
  "s_waitcnt lgkmcnt(6)\n\t" \
  "v_mfma_f32_16x16x32_bf16 v[144:147], v[0:3], v[176:179], 0\n\t" \
  "v_mfma_f32_16x16x32_bf16 v[148:151], v[4:7], v[176:179], 0\n\t" \
  "ds_read_b128 v[176:179], v212 offset:256\n\t" \
  "v_mfma_f32_16x16x32_bf16 v[152:155], v[0:3], v[180:183], 0\n\t" \
  "v_mfma_f32_16x16x32_bf16 v[156:159], v[4:7], v[180:183], 0\n\t" \
  "ds_read_b128 v[180:183], v212 offset:16640\n\t" \
  "s_waitcnt lgkmcnt(6)\n\t" \
  "v_mfma_f32_16x16x32_bf16 v[144:147], v[8:11], v[184:187], v[144:147]\n\t" \
  "v_mfma_f32_16x16x32_bf16 v[148:151], v[12:15], v[184:187], v[148:151]\n\t" \
  "ds_read_b128 v[184:187], v213 offset:256\n\t" \
  "s_add_u32 m0, %[lw], 2048\n\t" \
  "v_mfma_f32_16x16x32_bf16 v[152:155], v[8:11], v[188:191], v[152:155]\n\t" \
  "v_mfma_f32_16x16x32_bf16 v[156:159], v[12:15], v[188:191], v[156:159]\n\t" \
  "ds_read_b128 v[188:191], v213 offset:16640\n\t" \
  "global_load_lds_dwordx4 v218, s[92:93] sc1\n\t" \
  "ds_write_b128 v220, v[136:139] offset:0\n\t" \
  "ds_write_b128 v220, v[140:143] offset:1024\n\t" \
  "s_waitcnt lgkmcnt(8)\n\t" \
  "v_mfma_f32_16x16x32_bf16 v[144:147], v[16:19], v[192:195], v[144:147]\n\t" \
  "v_mfma_f32_16x16x32_bf16 v[148:151], v[20:23], v[192:195], v[148:151]\n\t" \
  "ds_read_b128 v[192:195], v214 offset:256\n\t" \
  "v_mfma_f32_16x16x32_bf16 v[152:155], v[16:19], v[196:199], v[152:155]\n\t" \
  "v_mfma_f32_16x16x32_bf16 v[156:159], v[20:23], v[196:199], v[156:159]\n\t" \
  "ds_read_b128 v[196:199], v214 offset:16640\n\t" \
  "s_lshr_b32 s90, s87, 7\n\t" \
  "s_xor_b32 s90, s90, %[s0]\n\t" \
  "s_and_b32 s90, s90, 1\n\t" \
  "s_lshl1_add_u32 s90, s87, s90\n\t" \
  "s_lshl_b32 s90, s90, 13\n\t" \
  "s_cmp_lt_i32 s86, 6\n\t" \
  "s_cselect_b32 s90, 0x70000000, s90\n\t" \
  "s_waitcnt lgkmcnt(8)\n\t" \
  "v_mfma_f32_16x16x32_bf16 v[144:147], v[24:27], v[200:203], v[144:147]\n\t" \
  "v_mfma_f32_16x16x32_bf16 v[148:151], v[28:31], v[200:203], v[148:151]\n\t" \
  "ds_read_b128 v[200:203], v215 offset:256\n\t" \
  "s_add_u32 m0, %[lw], 3072\n\t" \
  "v_mfma_f32_16x16x32_bf16 v[152:155], v[24:27], v[204:207], v[152:155]\n\t" \
  "v_mfma_f32_16x16x32_bf16 v[156:159], v[28:31], v[204:207], v[156:159]\n\t" \
  "ds_read_b128 v[204:207], v215 offset:16640\n\t" \
  "global_load_lds_dwordx4 v219, s[92:93] sc1\n\t" \
  "s_waitcnt lgkmcnt(4)\n\t" \
  "s_cmp_lt_u32 s86, 6\n\t" \
  "s_cbranch_scc1 .Lw21%=\n\t" \
  "s_waitcnt vmcnt(4)\n\t" \
  "s_barrier\n\t" \
  ".Lg21%=:\n\t" \
  "s_add_u32 s92, s92, 512\n\t" \
  "s_addc_u32 s93, s93, 0\n\t" \
  "v_mfma_f32_16x16x32_bf16 v[144:147], v[32:35], v[176:179], v[144:147]\n\t" \
  "v_mfma_f32_16x16x32_bf16 v[148:151], v[36:39], v[176:179], v[148:151]\n\t" \
  "ds_read_b128 v[176:179], v212 offset:32768\n\t" \
  "v_mfma_f32_16x16x32_bf16 v[152:155], v[32:35], v[180:183], v[152:155]\n\t" \
  "v_mfma_f32_16x16x32_bf16 v[156:159], v[36:39], v[180:183], v[156:159]\n\t" \
  "ds_read_b128 v[180:183], v212 offset:49152\n\t" \
  "ds_read_b128 v[160:163], v221 offset:0\n\t" \
  "ds_read_b128 v[164:167], v221 offset:1024\n\t" \
  "s_sub_i32 s91, s86, 2\n\t" \
  "s_max_i32 s88, s88, s91\n\t" \
  "v_mov_b32 v173, s88\n\t" \
  "buffer_store_dword v173, v174, %[rs], 0 offen sc1\n\t" \
  "v_mfma_f32_16x16x32_bf16 v[144:147], v[40:43], v[184:187], v[144:147]\n\t" \
  "v_mfma_f32_16x16x32_bf16 v[148:151], v[44:47], v[184:187], v[148:151]\n\t" \
  "ds_read_b128 v[184:187], v213 offset:32768\n\t" \
  "s_add_u32 m0, %[lw], 32768\n\t" \
  "v_mfma_f32_16x16x32_bf16 v[152:155], v[40:43], v[188:191], v[152:155]\n\t" \
  "v_mfma_f32_16x16x32_bf16 v[156:159], v[44:47], v[188:191], v[156:159]\n\t" \
  "ds_read_b128 v[188:191], v213 offset:49152\n\t" \
  "global_load_lds_dwordx4 v216, s[92:93] sc1\n\t" \
  "s_waitcnt lgkmcnt(8)\n\t" \
  "v_mfma_f32_16x16x32_bf16 v[144:147], v[48:51], v[192:195], v[144:147]\n\t" \
  "v_mfma_f32_16x16x32_bf16 v[148:151], v[52:55], v[192:195], v[148:151]\n\t" \
  "ds_read_b128 v[192:195], v214 offset:32768\n\t" \
  "v_mfma_f32_16x16x32_bf16 v[152:155], v[48:51], v[196:199], v[152:155]\n\t" \
  "v_mfma_f32_16x16x32_bf16 v[156:159], v[52:55], v[196:199], v[156:159]\n\t" \
  "ds_read_b128 v[196:199], v214 offset:49152\n\t" \
  "s_waitcnt lgkmcnt(4)\n\t" \
  "v_add_f32 v160, v160, v128\n\t" \
  "v_add_f32 v161, v161, v129\n\t" \
  "v_add_f32 v162, v162, v130\n\t" \
  "v_add_f32 v163, v163, v131\n\t" \
  "v_add_f32 v164, v164, v132\n\t" \
  "v_add_f32 v165, v165, v133\n\t" \
  "v_add_f32 v166, v166, v134\n\t" \
  "v_add_f32 v167, v167, v135\n\t" \
  "v_mfma_f32_16x16x32_bf16 v[144:147], v[56:59], v[200:203], v[144:147]\n\t" \
  "v_mfma_f32_16x16x32_bf16 v[148:151], v[60:63], v[200:203], v[148:151]\n\t" \
  "ds_read_b128 v[200:203], v215 offset:32768\n\t" \
  "s_add_u32 m0, %[lw], 33792\n\t" \
  "v_mfma_f32_16x16x32_bf16 v[152:155], v[56:59], v[204:207], v[152:155]\n\t" \
  "v_mfma_f32_16x16x32_bf16 v[156:159], v[60:63], v[204:207], v[156:159]\n\t" \
  "ds_read_b128 v[204:207], v215 offset:49152\n\t" \
  "global_load_lds_dwordx4 v217, s[92:93] sc1\n\t" \
  "v_cvt_pk_bf16_f32 v168, v160, v161\n\t" \
  "v_cvt_pk_bf16_f32 v169, v162, v163\n\t" \
  "v_cvt_pk_bf16_f32 v170, v164, v165\n\t" \
  "v_cvt_pk_bf16_f32 v171, v166, v167\n\t" \
  "buffer_store_dwordx4 v[168:171], v172, %[rt], s90 offen sc1\n\t" \
  "v_mfma_f32_16x16x32_bf16 v[144:147], v[64:67], v[176:179], v[144:147]\n\t" \
  "v_mfma_f32_16x16x32_bf16 v[148:151], v[68:71], v[176:179], v[148:151]\n\t" \
  "ds_read_b128 v[176:179], v212 offset:33024\n\t" \
  "v_mfma_f32_16x16x32_bf16 v[152:155], v[64:67], v[180:183], v[152:155]\n\t" \
  "v_mfma_f32_16x16x32_bf16 v[156:159], v[68:71], v[180:183], v[156:159]\n\t" \
  "ds_read_b128 v[180:183], v212 offset:49408\n\t" \
  "s_waitcnt lgkmcnt(6)\n\t" \
  "v_mfma_f32_16x16x32_bf16 v[144:147], v[72:75], v[184:187], v[144:147]\n\t" \
  "v_mfma_f32_16x16x32_bf16 v[148:151], v[76:79], v[184:187], v[148:151]\n\t" \
  "ds_read_b128 v[184:187], v213 offset:33024\n\t" \
  "s_add_u32 m0, %[lw], 34816\n\t" \
  "v_mfma_f32_16x16x32_bf16 v[152:155], v[72:75], v[188:191], v[152:155]\n\t" \
  "v_mfma_f32_16x16x32_bf16 v[156:159], v[76:79], v[188:191], v[156:159]\n\t" \
  "ds_read_b128 v[188:191], v213 offset:49408\n\t" \
  "global_load_lds_dwordx4 v218, s[92:93] sc1\n\t" \
  "s_waitcnt lgkmcnt(6)\n\t" \
  "v_mfma_f32_16x16x32_bf16 v[144:147], v[80:83], v[192:195], v[144:147]\n\t" \
  "v_mfma_f32_16x16x32_bf16 v[148:151], v[84:87], v[192:195], v[148:151]\n\t" \
  "ds_read_b128 v[192:195], v214 offset:33024\n\t" \
  "v_mfma_f32_16x16x32_bf16 v[152:155], v[80:83], v[196:199], v[152:155]\n\t" \
  "v_mfma_f32_16x16x32_bf16 v[156:159], v[84:87], v[196:199], v[156:159]\n\t" \
  "ds_read_b128 v[196:199], v214 offset:49408\n\t" \
  "s_waitcnt lgkmcnt(6)\n\t" \
  "v_mfma_f32_16x16x32_bf16 v[144:147], v[88:91], v[200:203], v[144:147]\n\t" \
  "v_mfma_f32_16x16x32_bf16 v[148:151], v[92:95], v[200:203], v[148:151]\n\t" \
  "ds_read_b128 v[200:203], v215 offset:33024\n\t" \
  "s_add_u32 m0, %[lw], 35840\n\t" \
  "v_mfma_f32_16x16x32_bf16 v[152:155], v[88:91], v[204:207], v[152:155]\n\t" \
  "v_mfma_f32_16x16x32_bf16 v[156:159], v[92:95], v[204:207], v[156:159]\n\t" \
  "ds_read_b128 v[204:207], v215 offset:49408\n\t" \
  "global_load_lds_dwordx4 v219, s[92:93] sc1\n\t" \
  "s_cmp_lt_u32 s86, 8\n\t" \
  "s_cbranch_scc1 .Lw31%=\n\t" \
  "s_waitcnt vmcnt(6)\n\t" \
  "s_barrier\n\t" \
  ".Lg31%=:\n\t" \
  "s_add_u32 s92, s92, 65024\n\t" \
  "s_addc_u32 s93, s93, 0\n\t" \
  "s_waitcnt lgkmcnt(6)\n\t" \
  "v_mfma_f32_16x16x32_bf16 v[144:147], v[96:99], v[176:179], v[144:147]\n\t" \
  "v_mfma_f32_16x16x32_bf16 v[148:151], v[100:103], v[176:179], v[148:151]\n\t" \
  "ds_read_b128 v[176:179], v208 offset:0\n\t" \
  "v_mfma_f32_16x16x32_bf16 v[152:155], v[96:99], v[180:183], v[152:155]\n\t" \
  "v_mfma_f32_16x16x32_bf16 v[156:159], v[100:103], v[180:183], v[156:159]\n\t" \
  "ds_read_b128 v[180:183], v208 offset:16384\n\t" \
  "s_waitcnt lgkmcnt(6)\n\t" \
  "v_mfma_f32_16x16x32_bf16 v[144:147], v[104:107], v[184:187], v[144:147]\n\t" \
  "v_mfma_f32_16x16x32_bf16 v[148:151], v[108:111], v[184:187], v[148:151]\n\t" \
  "ds_read_b128 v[184:187], v209 offset:0\n\t" \
  "s_add_u32 m0, %[lw], 65536\n\t" \
  "v_mfma_f32_16x16x32_bf16 v[152:155], v[104:107], v[188:191], v[152:155]\n\t" \
  "v_mfma_f32_16x16x32_bf16 v[156:159], v[108:111], v[188:191], v[156:159]\n\t" \
  "ds_read_b128 v[188:191], v209 offset:16384\n\t" \
  "global_load_lds_dwordx4 v216, s[92:93] sc1\n\t" \
  "s_waitcnt lgkmcnt(6)\n\t" \
  "v_mfma_f32_16x16x32_bf16 v[144:147], v[112:115], v[192:195], v[144:147]\n\t" \
  "v_mfma_f32_16x16x32_bf16 v[148:151], v[116:119], v[192:195], v[148:151]\n\t" \
  "ds_read_b128 v[192:195], v210 offset:0\n\t" \
  "v_mfma_f32_16x16x32_bf16 v[152:155], v[112:115], v[196:199], v[152:155]\n\t" \
  "v_mfma_f32_16x16x32_bf16 v[156:159], v[116:119], v[196:199], v[156:159]\n\t" \
  "ds_read_b128 v[196:199], v210 offset:16384\n\t" \
  "s_waitcnt lgkmcnt(6)\n\t" \
  "v_mfma_f32_16x16x32_bf16 v[144:147], v[120:123], v[200:203], v[144:147]\n\t" \
  "v_mfma_f32_16x16x32_bf16 v[148:151], v[124:127], v[200:203], v[148:151]\n\t" \
  "ds_read_b128 v[200:203], v211 offset:0\n\t" \
  "s_add_u32 m0, %[lw], 66560\n\t" \
  "v_mfma_f32_16x16x32_bf16 v[152:155], v[120:123], v[204:207], v[152:155]\n\t" \
  "v_mfma_f32_16x16x32_bf16 v[156:159], v[124:127], v[204:207], v[156:159]\n\t" \
  "ds_read_b128 v[204:207], v211 offset:16384\n\t" \
  "global_load_lds_dwordx4 v217, s[92:93] sc1\n\t" \
  "s_add_u32 s86, s86, 1\n\t" \
  "s_add_u32 s87, s87, 1\n\t" \
  "s_cmp_lt_u32 s86, %[end]\n\t" \
  "s_cbranch_scc1 .Lp1%=\n\t" \
  "s_branch .Lx1%=\n\t" \
  ".Lf01%=:\n\t" \
  "s_nop 7\n\t" \
  "ds_write_b128 v220, v[152:155] offset:0\n\t" \
  "ds_write_b128 v220, v[156:159] offset:1024\n\t" \
  "s_lshr_b32 s90, s87, 7\n\t" \
  "s_xor_b32 s90, s90, %[s0]\n\t" \
  "s_and_b32 s90, s90, 1\n\t" \
  "s_lshl1_add_u32 s90, s87, s90\n\t" \
  "s_lshl_b32 s90, s90, 13\n\t" \
  "s_cmp_lt_i32 s86, 1\n\t" \
  "s_cselect_b32 s90, 0x70000000, s90\n\t" \
  "s_waitcnt lgkmcnt(0)\n\t" \
  "s_barrier\n\t" \
  "ds_read_b128 v[160:163], v221 offset:0\n\t" \
  "ds_read_b128 v[164:167], v221 offset:1024\n\t" \
  "s_waitcnt lgkmcnt(0)\n\t" \
  "v_add_f32 v160, v160, v144\n\t" \
  "v_add_f32 v161, v161, v145\n\t" \
  "v_add_f32 v162, v162, v146\n\t" \
  "v_add_f32 v163, v163, v147\n\t" \
  "v_add_f32 v164, v164, v148\n\t" \
  "v_add_f32 v165, v165, v149\n\t" \
  "v_add_f32 v166, v166, v150\n\t" \
  "v_add_f32 v167, v167, v151\n\t" \
  "v_cvt_pk_bf16_f32 v168, v160, v161\n\t" \
  "v_cvt_pk_bf16_f32 v169, v162, v163\n\t" \
  "v_cvt_pk_bf16_f32 v170, v164, v165\n\t" \
  "v_cvt_pk_bf16_f32 v171, v166, v167\n\t" \
  "buffer_store_dwordx4 v[168:171], v172, %[rt], s90 offen sc1\n\t" \
  "s_branch .Lfb01%=\n\t" \
  ".Lf11%=:\n\t" \
  "s_nop 7\n\t" \
  "ds_write_b128 v220, v[136:139] offset:0\n\t" \
  "ds_write_b128 v220, v[140:143] offset:1024\n\t" \
  "s_lshr_b32 s90, s87, 7\n\t" \
  "s_xor_b32 s90, s90, %[s0]\n\t" \
  "s_and_b32 s90, s90, 1\n\t" \
  "s_lshl1_add_u32 s90, s87, s90\n\t" \
  "s_lshl_b32 s90, s90, 13\n\t" \
  "s_cmp_lt_i32 s86, 1\n\t" \
  "s_cselect_b32 s90, 0x70000000, s90\n\t" \
  "s_waitcnt lgkmcnt(0)\n\t" \
  "s_barrier\n\t" \
  "ds_read_b128 v[160:163], v221 offset:0\n\t" \
  "ds_read_b128 v[164:167], v221 offset:1024\n\t" \
  "s_waitcnt lgkmcnt(0)\n\t" \
  "v_add_f32 v160, v160, v128\n\t" \
  "v_add_f32 v161, v161, v129\n\t" \
  "v_add_f32 v162, v162, v130\n\t" \
  "v_add_f32 v163, v163, v131\n\t" \
  "v_add_f32 v164, v164, v132\n\t" \
  "v_add_f32 v165, v165, v133\n\t" \
  "v_add_f32 v166, v166, v134\n\t" \
  "v_add_f32 v167, v167, v135\n\t" \
  "v_cvt_pk_bf16_f32 v168, v160, v161\n\t" \
  "v_cvt_pk_bf16_f32 v169, v162, v163\n\t" \
  "v_cvt_pk_bf16_f32 v170, v164, v165\n\t" \
  "v_cvt_pk_bf16_f32 v171, v166, v167\n\t" \
  "buffer_store_dwordx4 v[168:171], v172, %[rt], s90 offen sc1\n\t" \
  "s_branch .Lfb11%=\n\t" \
  ".Lw01%=:\n\t" \
  "s_waitcnt vmcnt(2)\n\t" \
  "s_barrier\n\t" \
  "s_sub_i32 s91, s86, 1\n\t" \
  "s_max_i32 s88, s88, s91\n\t" \
  "v_mov_b32 v173, s88\n\t" \
  "buffer_store_dword v173, v174, %[rs], 0 offen sc1\n\t" \
  "s_branch .Lg01%=\n\t" \
  ".Lw11%=:\n\t" \
  "s_waitcnt vmcnt(2)\n\t" \
  "s_barrier\n\t" \
  "s_sub_i32 s91, s86, 1\n\t" \
  "s_max_i32 s88, s88, s91\n\t" \
  "v_mov_b32 v173, s88\n\t" \
  "buffer_store_dword v173, v174, %[rs], 0 offen sc1\n\t" \
  "s_branch .Lg11%=\n\t" \
  ".Lw21%=:\n\t" \
  "s_waitcnt vmcnt(2)\n\t" \
  "s_barrier\n\t" \
  "s_sub_i32 s91, s86, 1\n\t" \
  "s_max_i32 s88, s88, s91\n\t" \
  "v_mov_b32 v173, s88\n\t" \
  "buffer_store_dword v173, v174, %[rs], 0 offen sc1\n\t" \
  "s_branch .Lg21%=\n\t" \
  ".Lw31%=:\n\t" \
  "s_waitcnt vmcnt(2)\n\t" \
  "s_barrier\n\t" \
  "s_sub_i32 s91, s86, 1\n\t" \
  "s_max_i32 s88, s88, s91\n\t" \
  "v_mov_b32 v173, s88\n\t" \
  "buffer_store_dword v173, v174, %[rs], 0 offen sc1\n\t" \
  "s_branch .Lg31%=\n\t" \
  ".Lx1%=:\n\t" \
  "s_waitcnt lgkmcnt(0)\n\t" \
  "s_setprio 0\n\t" \
  "s_mov_b32 m0, s94\n\t" \
  ".Le%=:\n\t"
#define PR_ASM_1 \
  "s_cmp_lg_u32 %[kh], 0\n\t" \
  "s_cbranch_scc1 .Lk%=\n\t" \
  "s_mov_b32 s94, m0\n\t" \
  "s_mov_b64 s[92:93], %[x0]\n\t" \
  "s_mov_b32 s86, %[g0]\n\t" \
  "s_add_u32 s87, s86, %[t0]\n\t" \
  "s_sub_i32 s88, s86, 3\n\t" \
  "ds_read_b128 v[176:179], v208 offset:0\n\t" \
  "ds_read_b128 v[180:183], v208 offset:16384\n\t" \
  "ds_read_b128 v[184:187], v209 offset:0\n\t" \
  "ds_read_b128 v[188:191], v209 offset:16384\n\t" \
  "ds_read_b128 v[192:195], v210 offset:0\n\t" \
  "ds_read_b128 v[196:199], v210 offset:16384\n\t" \
  "ds_read_b128 v[200:203], v211 offset:0\n\t" \
  "ds_read_b128 v[204:207], v211 offset:16384\n\t" \
  "s_branch .Lfb00%=\n\t" \
  ".Lp0%=:\n\t" \
  "s_cmp_le_u32 s86, 5\n\t" \
  "s_cbranch_scc1 .Lf00%=\n\t" \
  ".Lfb00%=:\n\t" \
  "s_waitcnt lgkmcnt(6)\n\t" \
  "v_mfma_f32_16x16x32_bf16 v[128:131], v[176:179], v[0:3], 0\n\t" \
  "v_mfma_f32_16x16x32_bf16 v[136:139], v[176:179], v[4:7], 0\n\t" \
  "ds_read_b128 v[176:179], v208 offset:256\n\t" \
  "s_add_u32 m0, %[lw], 67584\n\t" \
  "v_mfma_f32_16x16x32_bf16 v[132:135], v[180:183], v[0:3], 0\n\t" \
  "v_mfma_f32_16x16x32_bf16 v[140:143], v[180:183], v[4:7], 0\n\t" \
  "ds_read_b128 v[180:183], v208 offset:16640\n\t" \
  "global_load_lds_dwordx4 v218, s[92:93] sc1\n\t" \
  "s_waitcnt lgkmcnt(6)\n\t" \
  "v_mfma_f32_16x16x32_bf16 v[128:131], v[184:187], v[8:11], v[128:131]\n\t" \
  "v_mfma_f32_16x16x32_bf16 v[136:139], v[184:187], v[12:15], v[136:139]\n\t" \
  "ds_read_b128 v[184:187], v209 offset:256\n\t" \
  "v_mfma_f32_16x16x32_bf16 v[132:135], v[188:191], v[8:11], v[132:135]\n\t" \
  "v_mfma_f32_16x16x32_bf16 v[140:143], v[188:191], v[12:15], v[140:143]\n\t" \
  "ds_read_b128 v[188:191], v209 offset:16640\n\t" \
  "ds_write_b128 v220, v[152:155] offset:0\n\t" \
  "ds_write_b128 v220, v[156:159] offset:1024\n\t" \
  "s_waitcnt lgkmcnt(8)\n\t" \
  "v_mfma_f32_16x16x32_bf16 v[128:131], v[192:195], v[16:19], v[128:131]\n\t" \
  "v_mfma_f32_16x16x32_bf16 v[136:139], v[192:195], v[20:23], v[136:139]\n\t" \
  "ds_read_b128 v[192:195], v210 offset:256\n\t" \
  "s_add_u32 m0, %[lw], 68608\n\t" \
  "v_mfma_f32_16x16x32_bf16 v[132:135], v[196:199], v[16:19], v[132:135]\n\t" \
  "v_mfma_f32_16x16x32_bf16 v[140:143], v[196:199], v[20:23], v[140:143]\n\t" \
  "ds_read_b128 v[196:199], v210 offset:16640\n\t" \
  "global_load_lds_dwordx4 v219, s[92:93] sc1\n\t" \
  "s_lshr_b32 s90, s87, 7\n\t" \
  "s_xor_b32 s90, s90, %[s0]\n\t" \
  "s_and_b32 s90, s90, 1\n\t" \
  "s_lshl1_add_u32 s90, s87, s90\n\t" \
  "s_lshl_b32 s90, s90, 13\n\t" \
  "s_cmp_lt_i32 s86, 6\n\t" \
  "s_cselect_b32 s90, 0x70000000, s90\n\t" \
  "s_waitcnt lgkmcnt(8)\n\t" \
  "v_mfma_f32_16x16x32_bf16 v[128:131], v[200:203], v[24:27], v[128:131]\n\t" \
  "v_mfma_f32_16x16x32_bf16 v[136:139], v[200:203], v[28:31], v[136:139]\n\t" \
  "ds_read_b128 v[200:203], v211 offset:256\n\t" \
  "v_mfma_f32_16x16x32_bf16 v[132:135], v[204:207], v[24:27], v[132:135]\n\t" \
  "v_mfma_f32_16x16x32_bf16 v[140:143], v[204:207], v[28:31], v[140:143]\n\t" \
  "ds_read_b128 v[204:207], v211 offset:16640\n\t" \
  "s_waitcnt lgkmcnt(4)\n\t" \
  "s_cmp_lt_u32 s86, 6\n\t" \
  "s_cbranch_scc1 .Lw00%=\n\t" \
  "s_waitcnt vmcnt(4)\n\t" \
  "s_barrier\n\t" \
  ".Lg00%=:\n\t" \
  "s_add_u32 s92, s92, 512\n\t" \
  "s_addc_u32 s93, s93, 0\n\t" \
  "v_mfma_f32_16x16x32_bf16 v[128:131], v[176:179], v[32:35], v[128:131]\n\t" \
  "v_mfma_f32_16x16x32_bf16 v[136:139], v[176:179], v[36:39], v[136:139]\n\t" \
  "ds_read_b128 v[176:179], v208 offset:32768\n\t" \
  "s_add_u32 m0, %[lw], 98304\n\t" \
  "v_mfma_f32_16x16x32_bf16 v[132:135], v[180:183], v[32:35], v[132:135]\n\t" \
  "v_mfma_f32_16x16x32_bf16 v[140:143], v[180:183], v[36:39], v[140:143]\n\t" \
  "ds_read_b128 v[180:183], v208 offset:49152\n\t" \
  "global_load_lds_dwordx4 v216, s[92:93] sc1\n\t" \
  "ds_read_b128 v[160:163], v221 offset:0\n\t" \
  "ds_read_b128 v[164:167], v221 offset:1024\n\t" \
  "v_mfma_f32_16x16x32_bf16 v[128:131], v[184:187], v[40:43], v[128:131]\n\t" \
  "v_mfma_f32_16x16x32_bf16 v[136:139], v[184:187], v[44:47], v[136:139]\n\t" \
  "ds_read_b128 v[184:187], v209 offset:32768\n\t" \
  "v_mfma_f32_16x16x32_bf16 v[132:135], v[188:191], v[40:43], v[132:135]\n\t" \
  "v_mfma_f32_16x16x32_bf16 v[140:143], v[188:191], v[44:47], v[140:143]\n\t" \
  "ds_read_b128 v[188:191], v209 offset:49152\n\t" \
  "s_waitcnt lgkmcnt(8)\n\t" \
  "v_mfma_f32_16x16x32_bf16 v[128:131], v[192:195], v[48:51], v[128:131]\n\t" \
  "v_mfma_f32_16x16x32_bf16 v[136:139], v[192:195], v[52:55], v[136:139]\n\t" \
  "ds_read_b128 v[192:195], v210 offset:32768\n\t" \
  "s_add_u32 m0, %[lw], 99328\n\t" \
  "v_mfma_f32_16x16x32_bf16 v[132:135], v[196:199], v[48:51], v[132:135]\n\t" \
  "v_mfma_f32_16x16x32_bf16 v[140:143], v[196:199], v[52:55], v[140:143]\n\t" \
  "ds_read_b128 v[196:199], v210 offset:49152\n\t" \
  "global_load_lds_dwordx4 v217, s[92:93] sc1\n\t" \
  "s_waitcnt lgkmcnt(4)\n\t" \
  "v_add_f32 v160, v160, v144\n\t" \
  "v_add_f32 v161, v161, v145\n\t" \
  "v_add_f32 v162, v162, v146\n\t" \
  "v_add_f32 v163, v163, v147\n\t" \
  "v_add_f32 v164, v164, v148\n\t" \
  "v_add_f32 v165, v165, v149\n\t" \
  "v_add_f32 v166, v166, v150\n\t" \
  "v_add_f32 v167, v167, v151\n\t" \
  "v_mfma_f32_16x16x32_bf16 v[128:131], v[200:203], v[56:59], v[128:131]\n\t" \
  "v_mfma_f32_16x16x32_bf16 v[136:139], v[200:203], v[60:63], v[136:139]\n\t" \
  "ds_read_b128 v[200:203], v211 offset:32768\n\t" \
  "v_mfma_f32_16x16x32_bf16 v[132:135], v[204:207], v[56:59], v[132:135]\n\t" \
  "v_mfma_f32_16x16x32_bf16 v[140:143], v[204:207], v[60:63], v[140:143]\n\t" \
  "ds_read_b128 v[204:207], v211 offset:49152\n\t" \
  "v_cvt_pk_bf16_f32 v168, v160, v161\n\t" \
  "v_cvt_pk_bf16_f32 v169, v162, v163\n\t" \
  "v_cvt_pk_bf16_f32 v170, v164, v165\n\t" \
  "v_cvt_pk_bf16_f32 v171, v166, v167\n\t" \
  "buffer_store_dwordx4 v[168:171], v172, %[rt], s90 offen sc1\n\t" \
  "v_mfma_f32_16x16x32_bf16 v[128:131], v[176:179], v[64:67], v[128:131]\n\t" \
  "v_mfma_f32_16x16x32_bf16 v[136:139], v[176:179], v[68:71], v[136:139]\n\t" \
  "ds_read_b128 v[176:179], v208 offset:33024\n\t" \
  "s_add_u32 m0, %[lw], 100352\n\t" \
  "v_mfma_f32_16x16x32_bf16 v[132:135], v[180:183], v[64:67], v[132:135]\n\t" \
  "v_mfma_f32_16x16x32_bf16 v[140:143], v[180:183], v[68:71], v[140:143]\n\t" \
  "ds_read_b128 v[180:183], v208 offset:49408\n\t" \
  "global_load_lds_dwordx4 v218, s[92:93] sc1\n\t" \
  "s_waitcnt lgkmcnt(6)\n\t" \
  "v_mfma_f32_16x16x32_bf16 v[128:131], v[184:187], v[72:75], v[128:131]\n\t" \
  "v_mfma_f32_16x16x32_bf16 v[136:139], v[184:187], v[76:79], v[136:139]\n\t" \
  "ds_read_b128 v[184:187], v209 offset:33024\n\t" \
  "v_mfma_f32_16x16x32_bf16 v[132:135], v[188:191], v[72:75], v[132:135]\n\t" \
  "v_mfma_f32_16x16x32_bf16 v[140:143], v[188:191], v[76:79], v[140:143]\n\t" \
  "ds_read_b128 v[188:191], v209 offset:49408\n\t" \
  "s_waitcnt lgkmcnt(6)\n\t" \
  "v_mfma_f32_16x16x32_bf16 v[128:131], v[192:195], v[80:83], v[128:131]\n\t" \
  "v_mfma_f32_16x16x32_bf16 v[136:139], v[192:195], v[84:87], v[136:139]\n\t" \
  "ds_read_b128 v[192:195], v210 offset:33024\n\t" \
  "s_add_u32 m0, %[lw], 101376\n\t" \
  "v_mfma_f32_16x16x32_bf16 v[132:135], v[196:199], v[80:83], v[132:135]\n\t" \
  "v_mfma_f32_16x16x32_bf16 v[140:143], v[196:199], v[84:87], v[140:143]\n\t" \
  "ds_read_b128 v[196:199], v210 offset:49408\n\t" \
  "global_load_lds_dwordx4 v219, s[92:93] sc1\n\t" \
  "s_waitcnt lgkmcnt(6)\n\t" \
  "v_mfma_f32_16x16x32_bf16 v[128:131], v[200:203], v[88:91], v[128:131]\n\t" \
  "v_mfma_f32_16x16x32_bf16 v[136:139], v[200:203], v[92:95], v[136:139]\n\t" \
  "ds_read_b128 v[200:203], v211 offset:33024\n\t" \
  "v_mfma_f32_16x16x32_bf16 v[132:135], v[204:207], v[88:91], v[132:135]\n\t" \
  "v_mfma_f32_16x16x32_bf16 v[140:143], v[204:207], v[92:95], v[140:143]\n\t" \
  "ds_read_b128 v[204:207], v211 offset:49408\n\t" \
  "s_cmp_lt_u32 s86, 8\n\t" \
  "s_cbranch_scc1 .Lw10%=\n\t" \
  "s_waitcnt vmcnt(5)\n\t" \
  "s_barrier\n\t" \
  ".Lg10%=:\n\t" \
  "s_add_u32 s92, s92, 65024\n\t" \
  "s_addc_u32 s93, s93, 0\n\t" \
  "s_waitcnt lgkmcnt(6)\n\t" \
  "v_mfma_f32_16x16x32_bf16 v[128:131], v[176:179], v[96:99], v[128:131]\n\t" \
  "v_mfma_f32_16x16x32_bf16 v[136:139], v[176:179], v[100:103], v[136:139]\n\t" \
  "ds_read_b128 v[176:179], v212 offset:0\n\t" \
  "s_add_u32 m0, %[lw], 0\n\t" \
  "v_mfma_f32_16x16x32_bf16 v[132:135], v[180:183], v[96:99], v[132:135]\n\t" \
  "v_mfma_f32_16x16x32_bf16 v[140:143], v[180:183], v[100:103], v[140:143]\n\t" \
  "ds_read_b128 v[180:183], v212 offset:16384\n\t" \
  "global_load_lds_dwordx4 v216, s[92:93] sc1\n\t" \
  "s_waitcnt lgkmcnt(6)\n\t" \
  "v_mfma_f32_16x16x32_bf16 v[128:131], v[184:187], v[104:107], v[128:131]\n\t" \
  "v_mfma_f32_16x16x32_bf16 v[136:139], v[184:187], v[108:111], v[136:139]\n\t" \
  "ds_read_b128 v[184:187], v213 offset:0\n\t" \
  "v_mfma_f32_16x16x32_bf16 v[132:135], v[188:191], v[104:107], v[132:135]\n\t" \
  "v_mfma_f32_16x16x32_bf16 v[140:143], v[188:191], v[108:111], v[140:143]\n\t" \
  "ds_read_b128 v[188:191], v213 offset:16384\n\t" \
  "s_waitcnt lgkmcnt(6)\n\t" \
  "v_mfma_f32_16x16x32_bf16 v[128:131], v[192:195], v[112:115], v[128:131]\n\t" \
  "v_mfma_f32_16x16x32_bf16 v[136:139], v[192:195], v[116:119], v[136:139]\n\t" \
  "ds_read_b128 v[192:195], v214 offset:0\n\t" \
  "s_add_u32 m0, %[lw], 1024\n\t" \
  "v_mfma_f32_16x16x32_bf16 v[132:135], v[196:199], v[112:115], v[132:135]\n\t" \
  "v_mfma_f32_16x16x32_bf16 v[140:143], v[196:199], v[116:119], v[140:143]\n\t" \
  "ds_read_b128 v[196:199], v214 offset:16384\n\t" \
  "global_load_lds_dwordx4 v217, s[92:93] sc1\n\t" \
  "s_waitcnt lgkmcnt(6)\n\t" \
  "v_mfma_f32_16x16x32_bf16 v[128:131], v[200:203], v[120:123], v[128:131]\n\t" \
  "v_mfma_f32_16x16x32_bf16 v[136:139], v[200:203], v[124:127], v[136:139]\n\t" \
  "ds_read_b128 v[200:203], v215 offset:0\n\t" \
  "v_mfma_f32_16x16x32_bf16 v[132:135], v[204:207], v[120:123], v[132:135]\n\t" \
  "v_mfma_f32_16x16x32_bf16 v[140:143], v[204:207], v[124:127], v[140:143]\n\t" \
  "ds_read_b128 v[204:207], v215 offset:16384\n\t" \
  "s_add_u32 s86, s86, 1\n\t" \
  "s_add_u32 s87, s87, 1\n\t" \
  "s_cmp_le_u32 s86, 5\n\t" \
  "s_cbranch_scc1 .Lf10%=\n\t" \
  ".Lfb10%=:\n\t" \
  "s_waitcnt lgkmcnt(6)\n\t" \
  "v_mfma_f32_16x16x32_bf16 v[144:147], v[176:179], v[0:3], 0\n\t" \
  "v_mfma_f32_16x16x32_bf16 v[152:155], v[176:179], v[4:7], 0\n\t" \
  "ds_read_b128 v[176:179], v212 offset:256\n\t" \
  "s_add_u32 m0, %[lw], 2048\n\t" \
  "v_mfma_f32_16x16x32_bf16 v[148:151], v[180:183], v[0:3], 0\n\t" \
  "v_mfma_f32_16x16x32_bf16 v[156:159], v[180:183], v[4:7], 0\n\t" \
  "ds_read_b128 v[180:183], v212 offset:16640\n\t" \
  "global_load_lds_dwordx4 v218, s[92:93] sc1\n\t" \
  "s_waitcnt lgkmcnt(6)\n\t" \
  "v_mfma_f32_16x16x32_bf16 v[144:147], v[184:187], v[8:11], v[144:147]\n\t" \
  "v_mfma_f32_16x16x32_bf16 v[152:155], v[184:187], v[12:15], v[152:155]\n\t" \
  "ds_read_b128 v[184:187], v213 offset:256\n\t" \
  "v_mfma_f32_16x16x32_bf16 v[148:151], v[188:191], v[8:11], v[148:151]\n\t" \
  "v_mfma_f32_16x16x32_bf16 v[156:159], v[188:191], v[12:15], v[156:159]\n\t" \
  "ds_read_b128 v[188:191], v213 offset:16640\n\t" \
  "ds_write_b128 v220, v[136:139] offset:0\n\t" \
  "ds_write_b128 v220, v[140:143] offset:1024\n\t" \
  "s_waitcnt lgkmcnt(8)\n\t" \
  "v_mfma_f32_16x16x32_bf16 v[144:147], v[192:195], v[16:19], v[144:147]\n\t" \
  "v_mfma_f32_16x16x32_bf16 v[152:155], v[192:195], v[20:23], v[152:155]\n\t" \
  "ds_read_b128 v[192:195], v214 offset:256\n\t" \
  "s_add_u32 m0, %[lw], 3072\n\t" \
  "v_mfma_f32_16x16x32_bf16 v[148:151], v[196:199], v[16:19], v[148:151]\n\t" \
  "v_mfma_f32_16x16x32_bf16 v[156:159], v[196:199], v[20:23], v[156:159]\n\t" \
  "ds_read_b128 v[196:199], v214 offset:16640\n\t" \
  "global_load_lds_dwordx4 v219, s[92:93] sc1\n\t" \
  "s_lshr_b32 s90, s87, 7\n\t" \
  "s_xor_b32 s90, s90, %[s0]\n\t" \
  "s_and_b32 s90, s90, 1\n\t" \
  "s_lshl1_add_u32 s90, s87, s90\n\t" \
  "s_lshl_b32 s90, s90, 13\n\t" \
  "s_cmp_lt_i32 s86, 6\n\t" \
  "s_cselect_b32 s90, 0x70000000, s90\n\t" \
  "s_waitcnt lgkmcnt(8)\n\t" \
  "v_mfma_f32_16x16x32_bf16 v[144:147], v[200:203], v[24:27], v[144:147]\n\t" \
  "v_mfma_f32_16x16x32_bf16 v[152:155], v[200:203], v[28:31], v[152:155]\n\t" \
  "ds_read_b128 v[200:203], v215 offset:256\n\t" \
  "v_mfma_f32_16x16x32_bf16 v[148:151], v[204:207], v[24:27], v[148:151]\n\t" \
  "v_mfma_f32_16x16x32_bf16 v[156:159], v[204:207], v[28:31], v[156:159]\n\t" \
  "ds_read_b128 v[204:207], v215 offset:16640\n\t" \
  "s_waitcnt lgkmcnt(4)\n\t" \
  "s_cmp_lt_u32 s86, 6\n\t" \
  "s_cbranch_scc1 .Lw20%=\n\t" \
  "s_waitcnt vmcnt(4)\n\t" \
  "s_barrier\n\t" \
  ".Lg20%=:\n\t" \
  "s_add_u32 s92, s92, 512\n\t" \
  "s_addc_u32 s93, s93, 0\n\t" \
  "v_mfma_f32_16x16x32_bf16 v[144:147], v[176:179], v[32:35], v[144:147]\n\t" \
  "v_mfma_f32_16x16x32_bf16 v[152:155], v[176:179], v[36:39], v[152:155]\n\t" \
  "ds_read_b128 v[176:179], v212 offset:32768\n\t" \
  "s_add_u32 m0, %[lw], 32768\n\t" \
  "v_mfma_f32_16x16x32_bf16 v[148:151], v[180:183], v[32:35], v[148:151]\n\t" \
  "v_mfma_f32_16x16x32_bf16 v[156:159], v[180:183], v[36:39], v[156:159]\n\t" \
  "ds_read_b128 v[180:183], v212 offset:49152\n\t" \
  "global_load_lds_dwordx4 v216, s[92:93] sc1\n\t" \
  "ds_read_b128 v[160:163], v221 offset:0\n\t" \
  "ds_read_b128 v[164:167], v221 offset:1024\n\t" \
  "v_mfma_f32_16x16x32_bf16 v[144:147], v[184:187], v[40:43], v[144:147]\n\t" \
  "v_mfma_f32_16x16x32_bf16 v[152:155], v[184:187], v[44:47], v[152:155]\n\t" \
  "ds_read_b128 v[184:187], v213 offset:32768\n\t" \
  "v_mfma_f32_16x16x32_bf16 v[148:151], v[188:191], v[40:43], v[148:151]\n\t" \
  "v_mfma_f32_16x16x32_bf16 v[156:159], v[188:191], v[44:47], v[156:159]\n\t" \
  "ds_read_b128 v[188:191], v213 offset:49152\n\t" \
  "s_waitcnt lgkmcnt(8)\n\t" \
  "v_mfma_f32_16x16x32_bf16 v[144:147], v[192:195], v[48:51], v[144:147]\n\t" \
  "v_mfma_f32_16x16x32_bf16 v[152:155], v[192:195], v[52:55], v[152:155]\n\t" \
  "ds_read_b128 v[192:195], v214 offset:32768\n\t" \
  "s_add_u32 m0, %[lw], 33792\n\t" \
  "v_mfma_f32_16x16x32_bf16 v[148:151], v[196:199], v[48:51], v[148:151]\n\t" \
  "v_mfma_f32_16x16x32_bf16 v[156:159], v[196:199], v[52:55], v[156:159]\n\t" \
  "ds_read_b128 v[196:199], v214 offset:49152\n\t" \
  "global_load_lds_dwordx4 v217, s[92:93] sc1\n\t" \
  "s_waitcnt lgkmcnt(4)\n\t" \
  "v_add_f32 v160, v160, v128\n\t" \
  "v_add_f32 v161, v161, v129\n\t" \
  "v_add_f32 v162, v162, v130\n\t" \
  "v_add_f32 v163, v163, v131\n\t" \
  "v_add_f32 v164, v164, v132\n\t" \
  "v_add_f32 v165, v165, v133\n\t" \
  "v_add_f32 v166, v166, v134\n\t" \
  "v_add_f32 v167, v167, v135\n\t" \
  "v_mfma_f32_16x16x32_bf16 v[144:147], v[200:203], v[56:59], v[144:147]\n\t" \
  "v_mfma_f32_16x16x32_bf16 v[152:155], v[200:203], v[60:63], v[152:155]\n\t" \
  "ds_read_b128 v[200:203], v215 offset:32768\n\t" \
  "v_mfma_f32_16x16x32_bf16 v[148:151], v[204:207], v[56:59], v[148:151]\n\t" \
  "v_mfma_f32_16x16x32_bf16 v[156:159], v[204:207], v[60:63], v[156:159]\n\t" \
  "ds_read_b128 v[204:207], v215 offset:49152\n\t" \
  "v_cvt_pk_bf16_f32 v168, v160, v161\n\t" \
  "v_cvt_pk_bf16_f32 v169, v162, v163\n\t" \
  "v_cvt_pk_bf16_f32 v170, v164, v165\n\t" \
  "v_cvt_pk_bf16_f32 v171, v166, v167\n\t" \
  "buffer_store_dwordx4 v[168:171], v172, %[rt], s90 offen sc1\n\t" \
  "v_mfma_f32_16x16x32_bf16 v[144:147], v[176:179], v[64:67], v[144:147]\n\t" \
  "v_mfma_f32_16x16x32_bf16 v[152:155], v[176:179], v[68:71], v[152:155]\n\t" \
  "ds_read_b128 v[176:179], v212 offset:33024\n\t" \
  "s_add_u32 m0, %[lw], 34816\n\t" \
  "v_mfma_f32_16x16x32_bf16 v[148:151], v[180:183], v[64:67], v[148:151]\n\t" \
  "v_mfma_f32_16x16x32_bf16 v[156:159], v[180:183], v[68:71], v[156:159]\n\t" \
  "ds_read_b128 v[180:183], v212 offset:49408\n\t" \
  "global_load_lds_dwordx4 v218, s[92:93] sc1\n\t" \
  "s_waitcnt lgkmcnt(6)\n\t" \
  "v_mfma_f32_16x16x32_bf16 v[144:147], v[184:187], v[72:75], v[144:147]\n\t" \
  "v_mfma_f32_16x16x32_bf16 v[152:155], v[184:187], v[76:79], v[152:155]\n\t" \
  "ds_read_b128 v[184:187], v213 offset:33024\n\t" \
  "v_mfma_f32_16x16x32_bf16 v[148:151], v[188:191], v[72:75], v[148:151]\n\t" \
  "v_mfma_f32_16x16x32_bf16 v[156:159], v[188:191], v[76:79], v[156:159]\n\t" \
  "ds_read_b128 v[188:191], v213 offset:49408\n\t" \
  "s_waitcnt lgkmcnt(6)\n\t" \
  "v_mfma_f32_16x16x32_bf16 v[144:147], v[192:195], v[80:83], v[144:147]\n\t" \
  "v_mfma_f32_16x16x32_bf16 v[152:155], v[192:195], v[84:87], v[152:155]\n\t" \
  "ds_read_b128 v[192:195], v214 offset:33024\n\t" \
  "s_add_u32 m0, %[lw], 35840\n\t" \
  "v_mfma_f32_16x16x32_bf16 v[148:151], v[196:199], v[80:83], v[148:151]\n\t" \
  "v_mfma_f32_16x16x32_bf16 v[156:159], v[196:199], v[84:87], v[156:159]\n\t" \
  "ds_read_b128 v[196:199], v214 offset:49408\n\t" \
  "global_load_lds_dwordx4 v219, s[92:93] sc1\n\t" \
  "s_waitcnt lgkmcnt(6)\n\t" \
  "v_mfma_f32_16x16x32_bf16 v[144:147], v[200:203], v[88:91], v[144:147]\n\t" \
  "v_mfma_f32_16x16x32_bf16 v[152:155], v[200:203], v[92:95], v[152:155]\n\t" \
  "ds_read_b128 v[200:203], v215 offset:33024\n\t" \
  "v_mfma_f32_16x16x32_bf16 v[148:151], v[204:207], v[88:91], v[148:151]\n\t" \
  "v_mfma_f32_16x16x32_bf16 v[156:159], v[204:207], v[92:95], v[156:159]\n\t" \
  "ds_read_b128 v[204:207], v215 offset:49408\n\t" \
  "s_cmp_lt_u32 s86, 8\n\t" \
  "s_cbranch_scc1 .Lw30%=\n\t" \
  "s_waitcnt vmcnt(5)\n\t" \
  "s_barrier\n\t" \
  ".Lg30%=:\n\t" \
  "s_add_u32 s92, s92, 65024\n\t" \
  "s_addc_u32 s93, s93, 0\n\t" \
  "s_waitcnt lgkmcnt(6)\n\t" \
  "v_mfma_f32_16x16x32_bf16 v[144:147], v[176:179], v[96:99], v[144:147]\n\t" \
  "v_mfma_f32_16x16x32_bf16 v[152:155], v[176:179], v[100:103], v[152:155]\n\t" \
  "ds_read_b128 v[176:179], v208 offset:0\n\t" \
  "s_add_u32 m0, %[lw], 65536\n\t" \
  "v_mfma_f32_16x16x32_bf16 v[148:151], v[180:183], v[96:99], v[148:151]\n\t" \
  "v_mfma_f32_16x16x32_bf16 v[156:159], v[180:183], v[100:103], v[156:159]\n\t" \
  "ds_read_b128 v[180:183], v208 offset:16384\n\t" \
  "global_load_lds_dwordx4 v216, s[92:93] sc1\n\t" \
  "s_waitcnt lgkmcnt(6)\n\t" \
  "v_mfma_f32_16x16x32_bf16 v[144:147], v[184:187], v[104:107], v[144:147]\n\t" \
  "v_mfma_f32_16x16x32_bf16 v[152:155], v[184:187], v[108:111], v[152:155]\n\t" \
  "ds_read_b128 v[184:187], v209 offset:0\n\t" \
  "v_mfma_f32_16x16x32_bf16 v[148:151], v[188:191], v[104:107], v[148:151]\n\t" \
  "v_mfma_f32_16x16x32_bf16 v[156:159], v[188:191], v[108:111], v[156:159]\n\t" \
  "ds_read_b128 v[188:191], v209 offset:16384\n\t" \
  "s_waitcnt lgkmcnt(6)\n\t" \
  "v_mfma_f32_16x16x32_bf16 v[144:147], v[192:195], v[112:115], v[144:147]\n\t" \
  "v_mfma_f32_16x16x32_bf16 v[152:155], v[192:195], v[116:119], v[152:155]\n\t" \
  "ds_read_b128 v[192:195], v210 offset:0\n\t" \
  "s_add_u32 m0, %[lw], 66560\n\t" \
  "v_mfma_f32_16x16x32_bf16 v[148:151], v[196:199], v[112:115], v[148:151]\n\t" \
  "v_mfma_f32_16x16x32_bf16 v[156:159], v[196:199], v[116:119], v[156:159]\n\t" \
  "ds_read_b128 v[196:199], v210 offset:16384\n\t" \
  "global_load_lds_dwordx4 v217, s[92:93] sc1\n\t" \
  "s_waitcnt lgkmcnt(6)\n\t" \
  "v_mfma_f32_16x16x32_bf16 v[144:147], v[200:203], v[120:123], v[144:147]\n\t" \
  "v_mfma_f32_16x16x32_bf16 v[152:155], v[200:203], v[124:127], v[152:155]\n\t" \
  "ds_read_b128 v[200:203], v211 offset:0\n\t" \
  "v_mfma_f32_16x16x32_bf16 v[148:151], v[204:207], v[120:123], v[148:151]\n\t" \
  "v_mfma_f32_16x16x32_bf16 v[156:159], v[204:207], v[124:127], v[156:159]\n\t" \
  "ds_read_b128 v[204:207], v211 offset:16384\n\t" \
  "s_add_u32 s86, s86, 1\n\t" \
  "s_add_u32 s87, s87, 1\n\t" \
  "s_cmp_lt_u32 s86, %[end]\n\t" \
  "s_cbranch_scc1 .Lp0%=\n\t" \
  "s_branch .Lx0%=\n\t" \
  ".Lf00%=:\n\t" \
  "s_nop 7\n\t" \
  "ds_write_b128 v220, v[152:155] offset:0\n\t" \
  "ds_write_b128 v220, v[156:159] offset:1024\n\t" \
  "s_lshr_b32 s90, s87, 7\n\t" \
  "s_xor_b32 s90, s90, %[s0]\n\t" \
  "s_and_b32 s90, s90, 1\n\t" \
  "s_lshl1_add_u32 s90, s87, s90\n\t" \
  "s_lshl_b32 s90, s90, 13\n\t" \
  "s_cmp_lt_i32 s86, 1\n\t" \
  "s_cselect_b32 s90, 0x70000000, s90\n\t" \
  "s_waitcnt lgkmcnt(0)\n\t" \
  "s_barrier\n\t" \
  "ds_read_b128 v[160:163], v221 offset:0\n\t" \
  "ds_read_b128 v[164:167], v221 offset:1024\n\t" \
  "s_waitcnt lgkmcnt(0)\n\t" \
  "v_add_f32 v160, v160, v144\n\t" \
  "v_add_f32 v161, v161, v145\n\t" \
  "v_add_f32 v162, v162, v146\n\t" \
  "v_add_f32 v163, v163, v147\n\t" \
  "v_add_f32 v164, v164, v148\n\t" \
  "v_add_f32 v165, v165, v149\n\t" \
  "v_add_f32 v166, v166, v150\n\t" \
  "v_add_f32 v167, v167, v151\n\t" \
  "v_cvt_pk_bf16_f32 v168, v160, v161\n\t" \
  "v_cvt_pk_bf16_f32 v169, v162, v163\n\t" \
  "v_cvt_pk_bf16_f32 v170, v164, v165\n\t" \
  "v_cvt_pk_bf16_f32 v171, v166, v167\n\t" \
  "buffer_store_dwordx4 v[168:171], v172, %[rt], s90 offen sc1\n\t" \
  "s_branch .Lfb00%=\n\t" \
  ".Lf10%=:\n\t" \
  "s_nop 7\n\t" \
  "ds_write_b128 v220, v[136:139] offset:0\n\t" \
  "ds_write_b128 v220, v[140:143] offset:1024\n\t" \
  "s_lshr_b32 s90, s87, 7\n\t" \
  "s_xor_b32 s90, s90, %[s0]\n\t" \
  "s_and_b32 s90, s90, 1\n\t" \
  "s_lshl1_add_u32 s90, s87, s90\n\t" \
  "s_lshl_b32 s90, s90, 13\n\t" \
  "s_cmp_lt_i32 s86, 1\n\t" \
  "s_cselect_b32 s90, 0x70000000, s90\n\t" \
  "s_waitcnt lgkmcnt(0)\n\t" \
  "s_barrier\n\t" \
  "ds_read_b128 v[160:163], v221 offset:0\n\t" \
  "ds_read_b128 v[164:167], v221 offset:1024\n\t" \
  "s_waitcnt lgkmcnt(0)\n\t" \
  "v_add_f32 v160, v160, v128\n\t" \
  "v_add_f32 v161, v161, v129\n\t" \
  "v_add_f32 v162, v162, v130\n\t" \
  "v_add_f32 v163, v163, v131\n\t" \
  "v_add_f32 v164, v164, v132\n\t" \
  "v_add_f32 v165, v165, v133\n\t" \
  "v_add_f32 v166, v166, v134\n\t" \
  "v_add_f32 v167, v167, v135\n\t" \
  "v_cvt_pk_bf16_f32 v168, v160, v161\n\t" \
  "v_cvt_pk_bf16_f32 v169, v162, v163\n\t" \
  "v_cvt_pk_bf16_f32 v170, v164, v165\n\t" \
  "v_cvt_pk_bf16_f32 v171, v166, v167\n\t" \
  "buffer_store_dwordx4 v[168:171], v172, %[rt], s90 offen sc1\n\t" \
  "s_branch .Lfb10%=\n\t" \
  ".Lw00%=:\n\t" \
  "s_waitcnt vmcnt(2)\n\t" \
  "s_barrier\n\t" \
  "s_branch .Lg00%=\n\t" \
  ".Lw10%=:\n\t" \
  "s_waitcnt vmcnt(2)\n\t" \
  "s_barrier\n\t" \
  "s_branch .Lg10%=\n\t" \
  ".Lw20%=:\n\t" \
  "s_waitcnt vmcnt(2)\n\t" \
  "s_barrier\n\t" \
  "s_branch .Lg20%=\n\t" \
  ".Lw30%=:\n\t" \
  "s_waitcnt vmcnt(2)\n\t" \
  "s_barrier\n\t" \
  "s_branch .Lg30%=\n\t" \
  ".Lx0%=:\n\t" \
  "s_waitcnt lgkmcnt(0)\n\t" \
  "s_setprio 0\n\t" \
  "s_mov_b32 m0, s94\n\t" \
  "s_branch .Le%=\n\t" \
  ".Lk%=:\n\t" \
  "s_mov_b32 s94, m0\n\t" \
  "s_mov_b64 s[92:93], %[x0]\n\t" \
  "s_mov_b32 s86, %[g0]\n\t" \
  "s_add_u32 s87, s86, %[t0]\n\t" \
  "s_sub_i32 s88, s86, 3\n\t" \
  "ds_read_b128 v[176:179], v208 offset:0\n\t" \
  "ds_read_b128 v[180:183], v208 offset:16384\n\t" \
  "ds_read_b128 v[184:187], v209 offset:0\n\t" \
  "ds_read_b128 v[188:191], v209 offset:16384\n\t" \
  "ds_read_b128 v[192:195], v210 offset:0\n\t" \
  "ds_read_b128 v[196:199], v210 offset:16384\n\t" \
  "ds_read_b128 v[200:203], v211 offset:0\n\t" \
  "ds_read_b128 v[204:207], v211 offset:16384\n\t" \
  "s_branch .Lfb01%=\n\t" \
  ".Lp1%=:\n\t" \
  "s_cmp_le_u32 s86, 5\n\t" \
  "s_cbranch_scc1 .Lf01%=\n\t" \
  ".Lfb01%=:\n\t" \
  "s_waitcnt lgkmcnt(6)\n\t" \
  "v_mfma_f32_16x16x32_bf16 v[128:131], v[176:179], v[0:3], 0\n\t" \
  "v_mfma_f32_16x16x32_bf16 v[136:139], v[176:179], v[4:7], 0\n\t" \
  "ds_read_b128 v[176:179], v208 offset:256\n\t" \
  "v_mfma_f32_16x16x32_bf16 v[132:135], v[180:183], v[0:3], 0\n\t" \
  "v_mfma_f32_16x16x32_bf16 v[140:143], v[180:183], v[4:7], 0\n\t" \
  "ds_read_b128 v[180:183], v208 offset:16640\n\t" \
  "s_waitcnt lgkmcnt(6)\n\t" \
  "v_mfma_f32_16x16x32_bf16 v[128:131], v[184:187], v[8:11], v[128:131]\n\t" \
  "v_mfma_f32_16x16x32_bf16 v[136:139], v[184:187], v[12:15], v[136:139]\n\t" \
  "ds_read_b128 v[184:187], v209 offset:256\n\t" \
  "s_add_u32 m0, %[lw], 67584\n\t" \
  "v_mfma_f32_16x16x32_bf16 v[132:135], v[188:191], v[8:11], v[132:135]\n\t" \
  "v_mfma_f32_16x16x32_bf16 v[140:143], v[188:191], v[12:15], v[140:143]\n\t" \
  "ds_read_b128 v[188:191], v209 offset:16640\n\t" \
  "global_load_lds_dwordx4 v218, s[92:93] sc1\n\t" \
  "ds_write_b128 v220, v[152:155] offset:0\n\t" \
  "ds_write_b128 v220, v[156:159] offset:1024\n\t" \
  "s_waitcnt lgkmcnt(8)\n\t" \
  "v_mfma_f32_16x16x32_bf16 v[128:131], v[192:195], v[16:19], v[128:131]\n\t" \
  "v_mfma_f32_16x16x32_bf16 v[136:139], v[192:195], v[20:23], v[136:139]\n\t" \
  "ds_read_b128 v[192:195], v210 offset:256\n\t" \
  "v_mfma_f32_16x16x32_bf16 v[132:135], v[196:199], v[16:19], v[132:135]\n\t" \
  "v_mfma_f32_16x16x32_bf16 v[140:143], v[196:199], v[20:23], v[140:143]\n\t" \
  "ds_read_b128 v[196:199], v210 offset:16640\n\t" \
  "s_lshr_b32 s90, s87, 7\n\t" \
  "s_xor_b32 s90, s90, %[s0]\n\t" \
  "s_and_b32 s90, s90, 1\n\t" \
  "s_lshl1_add_u32 s90, s87, s90\n\t" \
  "s_lshl_b32 s90, s90, 13\n\t" \
  "s_cmp_lt_i32 s86, 6\n\t" \
  "s_cselect_b32 s90, 0x70000000, s90\n\t" \
  "s_waitcnt lgkmcnt(8)\n\t" \
  "v_mfma_f32_16x16x32_bf16 v[128:131], v[200:203], v[24:27], v[128:131]\n\t" \
  "v_mfma_f32_16x16x32_bf16 v[136:139], v[200:203], v[28:31], v[136:139]\n\t" \
  "ds_read_b128 v[200:203], v211 offset:256\n\t" \
  "s_add_u32 m0, %[lw], 68608\n\t" \
  "v_mfma_f32_16x16x32_bf16 v[132:135], v[204:207], v[24:27], v[132:135]\n\t" \
  "v_mfma_f32_16x16x32_bf16 v[140:143], v[204:207], v[28:31], v[140:143]\n\t" \
  "ds_read_b128 v[204:207], v211 offset:16640\n\t" \
  "global_load_lds_dwordx4 v219, s[92:93] sc1\n\t" \
  "s_waitcnt lgkmcnt(4)\n\t" \
  "s_cmp_lt_u32 s86, 6\n\t" \
  "s_cbranch_scc1 .Lw01%=\n\t" \
  "s_waitcnt vmcnt(4)\n\t" \
  "s_barrier\n\t" \
  ".Lg01%=:\n\t" \
  "s_add_u32 s92, s92, 512\n\t" \
  "s_addc_u32 s93, s93, 0\n\t" \
  "v_mfma_f32_16x16x32_bf16 v[128:131], v[176:179], v[32:35], v[128:131]\n\t" \
  "v_mfma_f32_16x16x32_bf16 v[136:139], v[176:179], v[36:39], v[136:139]\n\t" \
  "ds_read_b128 v[176:179], v208 offset:32768\n\t" \
  "v_mfma_f32_16x16x32_bf16 v[132:135], v[180:183], v[32:35], v[132:135]\n\t" \
  "v_mfma_f32_16x16x32_bf16 v[140:143], v[180:183], v[36:39], v[140:143]\n\t" \
  "ds_read_b128 v[180:183], v208 offset:49152\n\t" \
  "ds_read_b128 v[160:163], v221 offset:0\n\t" \
  "ds_read_b128 v[164:167], v221 offset:1024\n\t" \
  "s_sub_i32 s91, s86, 2\n\t" \
  "s_max_i32 s88, s88, s91\n\t" \
  "v_mov_b32 v173, s88\n\t" \
  "buffer_store_dword v173, v174, %[rs], 0 offen sc1\n\t" \
  "v_mfma_f32_16x16x32_bf16 v[128:131], v[184:187], v[40:43], v[128:131]\n\t" \
  "v_mfma_f32_16x16x32_bf16 v[136:139], v[184:187], v[44:47], v[136:139]\n\t" \
  "ds_read_b128 v[184:187], v209 offset:32768\n\t" \
  "s_add_u32 m0, %[lw], 98304\n\t" \
  "v_mfma_f32_16x16x32_bf16 v[132:135], v[188:191], v[40:43], v[132:135]\n\t" \
  "v_mfma_f32_16x16x32_bf16 v[140:143], v[188:191], v[44:47], v[140:143]\n\t" \
  "ds_read_b128 v[188:191], v209 offset:49152\n\t" \
  "global_load_lds_dwordx4 v216, s[92:93] sc1\n\t" \
  "s_waitcnt lgkmcnt(8)\n\t" \
  "v_mfma_f32_16x16x32_bf16 v[128:131], v[192:195], v[48:51], v[128:131]\n\t" \
  "v_mfma_f32_16x16x32_bf16 v[136:139], v[192:195], v[52:55], v[136:139]\n\t" \
  "ds_read_b128 v[192:195], v210 offset:32768\n\t" \
  "v_mfma_f32_16x16x32_bf16 v[132:135], v[196:199], v[48:51], v[132:135]\n\t" \
  "v_mfma_f32_16x16x32_bf16 v[140:143], v[196:199], v[52:55], v[140:143]\n\t" \
  "ds_read_b128 v[196:199], v210 offset:49152\n\t" \
  "s_waitcnt lgkmcnt(4)\n\t" \
  "v_add_f32 v160, v160, v144\n\t" \
  "v_add_f32 v161, v161, v145\n\t" \
  "v_add_f32 v162, v162, v146\n\t" \
  "v_add_f32 v163, v163, v147\n\t" \
  "v_add_f32 v164, v164, v148\n\t" \
  "v_add_f32 v165, v165, v149\n\t" \
  "v_add_f32 v166, v166, v150\n\t" \
  "v_add_f32 v167, v167, v151\n\t" \
  "v_mfma_f32_16x16x32_bf16 v[128:131], v[200:203], v[56:59], v[128:131]\n\t" \
  "v_mfma_f32_16x16x32_bf16 v[136:139], v[200:203], v[60:63], v[136:139]\n\t" \
  "ds_read_b128 v[200:203], v211 offset:32768\n\t" \
  "s_add_u32 m0, %[lw], 99328\n\t" \
  "v_mfma_f32_16x16x32_bf16 v[132:135], v[204:207], v[56:59], v[132:135]\n\t" \
  "v_mfma_f32_16x16x32_bf16 v[140:143], v[204:207], v[60:63], v[140:143]\n\t" \
  "ds_read_b128 v[204:207], v211 offset:49152\n\t" \
  "global_load_lds_dwordx4 v217, s[92:93] sc1\n\t" \
  "v_cvt_pk_bf16_f32 v168, v160, v161\n\t" \
  "v_cvt_pk_bf16_f32 v169, v162, v163\n\t" \
  "v_cvt_pk_bf16_f32 v170, v164, v165\n\t" \
  "v_cvt_pk_bf16_f32 v171, v166, v167\n\t" \
  "buffer_store_dwordx4 v[168:171], v172, %[rt], s90 offen sc1\n\t" \
  "v_mfma_f32_16x16x32_bf16 v[128:131], v[176:179], v[64:67], v[128:131]\n\t" \
  "v_mfma_f32_16x16x32_bf16 v[136:139], v[176:179], v[68:71], v[136:139]\n\t" \
  "ds_read_b128 v[176:179], v208 offset:33024\n\t" \
  "v_mfma_f32_16x16x32_bf16 v[132:135], v[180:183], v[64:67], v[132:135]\n\t" \
  "v_mfma_f32_16x16x32_bf16 v[140:143], v[180:183], v[68:71], v[140:143]\n\t" \
  "ds_read_b128 v[180:183], v208 offset:49408\n\t" \
  "s_waitcnt lgkmcnt(6)\n\t" \
  "v_mfma_f32_16x16x32_bf16 v[128:131], v[184:187], v[72:75], v[128:131]\n\t" \
  "v_mfma_f32_16x16x32_bf16 v[136:139], v[184:187], v[76:79], v[136:139]\n\t" \
  "ds_read_b128 v[184:187], v209 offset:33024\n\t" \
  "s_add_u32 m0, %[lw], 100352\n\t" \
  "v_mfma_f32_16x16x32_bf16 v[132:135], v[188:191], v[72:75], v[132:135]\n\t" \
  "v_mfma_f32_16x16x32_bf16 v[140:143], v[188:191], v[76:79], v[140:143]\n\t" \
  "ds_read_b128 v[188:191], v209 offset:49408\n\t" \
  "global_load_lds_dwordx4 v218, s[92:93] sc1\n\t" \
  "s_waitcnt lgkmcnt(6)\n\t" \
  "v_mfma_f32_16x16x32_bf16 v[128:131], v[192:195], v[80:83], v[128:131]\n\t" \
  "v_mfma_f32_16x16x32_bf16 v[136:139], v[192:195], v[84:87], v[136:139]\n\t" \
  "ds_read_b128 v[192:195], v210 offset:33024\n\t" \
  "v_mfma_f32_16x16x32_bf16 v[132:135], v[196:199], v[80:83], v[132:135]\n\t" \
  "v_mfma_f32_16x16x32_bf16 v[140:143], v[196:199], v[84:87], v[140:143]\n\t" \
  "ds_read_b128 v[196:199], v210 offset:49408\n\t" \
  "s_waitcnt lgkmcnt(6)\n\t" \
  "v_mfma_f32_16x16x32_bf16 v[128:131], v[200:203], v[88:91], v[128:131]\n\t" \
  "v_mfma_f32_16x16x32_bf16 v[136:139], v[200:203], v[92:95], v[136:139]\n\t" \
  "ds_read_b128 v[200:203], v211 offset:33024\n\t" \
  "s_add_u32 m0, %[lw], 101376\n\t" \
  "v_mfma_f32_16x16x32_bf16 v[132:135], v[204:207], v[88:91], v[132:135]\n\t" \
  "v_mfma_f32_16x16x32_bf16 v[140:143], v[204:207], v[92:95], v[140:143]\n\t" \
  "ds_read_b128 v[204:207], v211 offset:49408\n\t" \
  "global_load_lds_dwordx4 v219, s[92:93] sc1\n\t" \
  "s_cmp_lt_u32 s86, 8\n\t" \
  "s_cbranch_scc1 .Lw11%=\n\t" \
  "s_waitcnt vmcnt(6)\n\t" \
  "s_barrier\n\t" \
  ".Lg11%=:\n\t" \
  "s_add_u32 s92, s92, 65024\n\t" \
  "s_addc_u32 s93, s93, 0\n\t" \
  "s_waitcnt lgkmcnt(6)\n\t" \
  "v_mfma_f32_16x16x32_bf16 v[128:131], v[176:179], v[96:99], v[128:131]\n\t" \
  "v_mfma_f32_16x16x32_bf16 v[136:139], v[176:179], v[100:103], v[136:139]\n\t" \
  "ds_read_b128 v[176:179], v212 offset:0\n\t" \
  "v_mfma_f32_16x16x32_bf16 v[132:135], v[180:183], v[96:99], v[132:135]\n\t" \
  "v_mfma_f32_16x16x32_bf16 v[140:143], v[180:183], v[100:103], v[140:143]\n\t" \
  "ds_read_b128 v[180:183], v212 offset:16384\n\t" \
  "s_waitcnt lgkmcnt(6)\n\t" \
  "v_mfma_f32_16x16x32_bf16 v[128:131], v[184:187], v[104:107], v[128:131]\n\t" \
  "v_mfma_f32_16x16x32_bf16 v[136:139], v[184:187], v[108:111], v[136:139]\n\t" \
  "ds_read_b128 v[184:187], v213 offset:0\n\t" \
  "s_add_u32 m0, %[lw], 0\n\t" \
  "v_mfma_f32_16x16x32_bf16 v[132:135], v[188:191], v[104:107], v[132:135]\n\t" \
  "v_mfma_f32_16x16x32_bf16 v[140:143], v[188:191], v[108:111], v[140:143]\n\t" \
  "ds_read_b128 v[188:191], v213 offset:16384\n\t" \
  "global_load_lds_dwordx4 v216, s[92:93] sc1\n\t" \
  "s_waitcnt lgkmcnt(6)\n\t" \
  "v_mfma_f32_16x16x32_bf16 v[128:131], v[192:195], v[112:115], v[128:131]\n\t" \
  "v_mfma_f32_16x16x32_bf16 v[136:139], v[192:195], v[116:119], v[136:139]\n\t" \
  "ds_read_b128 v[192:195], v214 offset:0\n\t" \
  "v_mfma_f32_16x16x32_bf16 v[132:135], v[196:199], v[112:115], v[132:135]\n\t" \
  "v_mfma_f32_16x16x32_bf16 v[140:143], v[196:199], v[116:119], v[140:143]\n\t" \
  "ds_read_b128 v[196:199], v214 offset:16384\n\t" \
  "s_waitcnt lgkmcnt(6)\n\t" \
  "v_mfma_f32_16x16x32_bf16 v[128:131], v[200:203], v[120:123], v[128:131]\n\t" \
  "v_mfma_f32_16x16x32_bf16 v[136:139], v[200:203], v[124:127], v[136:139]\n\t" \
  "ds_read_b128 v[200:203], v215 offset:0\n\t" \
  "s_add_u32 m0, %[lw], 1024\n\t" \
  "v_mfma_f32_16x16x32_bf16 v[132:135], v[204:207], v[120:123], v[132:135]\n\t" \
  "v_mfma_f32_16x16x32_bf16 v[140:143], v[204:207], v[124:127], v[140:143]\n\t" \
  "ds_read_b128 v[204:207], v215 offset:16384\n\t" \
  "global_load_lds_dwordx4 v217, s[92:93] sc1\n\t" \
  "s_add_u32 s86, s86, 1\n\t" \
  "s_add_u32 s87, s87, 1\n\t" \
  "s_cmp_le_u32 s86, 5\n\t" \
  "s_cbranch_scc1 .Lf11%=\n\t" \
  ".Lfb11%=:\n\t" \
  "s_waitcnt lgkmcnt(6)\n\t" \
  "v_mfma_f32_16x16x32_bf16 v[144:147], v[176:179], v[0:3], 0\n\t" \
  "v_mfma_f32_16x16x32_bf16 v[152:155], v[176:179], v[4:7], 0\n\t" \
  "ds_read_b128 v[176:179], v212 offset:256\n\t" \
  "v_mfma_f32_16x16x32_bf16 v[148:151], v[180:183], v[0:3], 0\n\t" \
  "v_mfma_f32_16x16x32_bf16 v[156:159], v[180:183], v[4:7], 0\n\t" \
  "ds_read_b128 v[180:183], v212 offset:16640\n\t" \
  "s_waitcnt lgkmcnt(6)\n\t" \
  "v_mfma_f32_16x16x32_bf16 v[144:147], v[184:187], v[8:11], v[144:147]\n\t" \
  "v_mfma_f32_16x16x32_bf16 v[152:155], v[184:187], v[12:15], v[152:155]\n\t" \
  "ds_read_b128 v[184:187], v213 offset:256\n\t" \
  "s_add_u32 m0, %[lw], 2048\n\t" \
  "v_mfma_f32_16x16x32_bf16 v[148:151], v[188:191], v[8:11], v[148:151]\n\t" \
  "v_mfma_f32_16x16x32_bf16 v[156:159], v[188:191], v[12:15], v[156:159]\n\t" \
  "ds_read_b128 v[188:191], v213 offset:16640\n\t" \
  "global_load_lds_dwordx4 v218, s[92:93] sc1\n\t" \
  "ds_write_b128 v220, v[136:139] offset:0\n\t" \
  "ds_write_b128 v220, v[140:143] offset:1024\n\t" \
  "s_waitcnt lgkmcnt(8)\n\t" \
  "v_mfma_f32_16x16x32_bf16 v[144:147], v[192:195], v[16:19], v[144:147]\n\t" \
  "v_mfma_f32_16x16x32_bf16 v[152:155], v[192:195], v[20:23], v[152:155]\n\t" \
  "ds_read_b128 v[192:195], v214 offset:256\n\t" \
  "v_mfma_f32_16x16x32_bf16 v[148:151], v[196:199], v[16:19], v[148:151]\n\t" \
  "v_mfma_f32_16x16x32_bf16 v[156:159], v[196:199], v[20:23], v[156:159]\n\t" \
  "ds_read_b128 v[196:199], v214 offset:16640\n\t" \
  "s_lshr_b32 s90, s87, 7\n\t" \
  "s_xor_b32 s90, s90, %[s0]\n\t" \
  "s_and_b32 s90, s90, 1\n\t" \
  "s_lshl1_add_u32 s90, s87, s90\n\t" \
  "s_lshl_b32 s90, s90, 13\n\t" \
  "s_cmp_lt_i32 s86, 6\n\t" \
  "s_cselect_b32 s90, 0x70000000, s90\n\t" \
  "s_waitcnt lgkmcnt(8)\n\t" \
  "v_mfma_f32_16x16x32_bf16 v[144:147], v[200:203], v[24:27], v[144:147]\n\t" \
  "v_mfma_f32_16x16x32_bf16 v[152:155], v[200:203], v[28:31], v[152:155]\n\t" \
  "ds_read_b128 v[200:203], v215 offset:256\n\t" \
  "s_add_u32 m0, %[lw], 3072\n\t" \
  "v_mfma_f32_16x16x32_bf16 v[148:151], v[204:207], v[24:27], v[148:151]\n\t" \
  "v_mfma_f32_16x16x32_bf16 v[156:159], v[204:207], v[28:31], v[156:159]\n\t" \
  "ds_read_b128 v[204:207], v215 offset:16640\n\t" \
  "global_load_lds_dwordx4 v219, s[92:93] sc1\n\t" \
  "s_waitcnt lgkmcnt(4)\n\t" \
  "s_cmp_lt_u32 s86, 6\n\t" \
  "s_cbranch_scc1 .Lw21%=\n\t" \
  "s_waitcnt vmcnt(4)\n\t" \
  "s_barrier\n\t" \
  ".Lg21%=:\n\t" \
  "s_add_u32 s92, s92, 512\n\t" \
  "s_addc_u32 s93, s93, 0\n\t" \
  "v_mfma_f32_16x16x32_bf16 v[144:147], v[176:179], v[32:35], v[144:147]\n\t" \
  "v_mfma_f32_16x16x32_bf16 v[152:155], v[176:179], v[36:39], v[152:155]\n\t" \
  "ds_read_b128 v[176:179], v212 offset:32768\n\t" \
  "v_mfma_f32_16x16x32_bf16 v[148:151], v[180:183], v[32:35], v[148:151]\n\t" \
  "v_mfma_f32_16x16x32_bf16 v[156:159], v[180:183], v[36:39], v[156:159]\n\t" \
  "ds_read_b128 v[180:183], v212 offset:49152\n\t" \
  "ds_read_b128 v[160:163], v221 offset:0\n\t" \
  "ds_read_b128 v[164:167], v221 offset:1024\n\t" \
  "s_sub_i32 s91, s86, 2\n\t" \
  "s_max_i32 s88, s88, s91\n\t" \
  "v_mov_b32 v173, s88\n\t" \
  "buffer_store_dword v173, v174, %[rs], 0 offen sc1\n\t" \
  "v_mfma_f32_16x16x32_bf16 v[144:147], v[184:187], v[40:43], v[144:147]\n\t" \
  "v_mfma_f32_16x16x32_bf16 v[152:155], v[184:187], v[44:47], v[152:155]\n\t" \
  "ds_read_b128 v[184:187], v213 offset:32768\n\t" \
  "s_add_u32 m0, %[lw], 32768\n\t" \
  "v_mfma_f32_16x16x32_bf16 v[148:151], v[188:191], v[40:43], v[148:151]\n\t" \
  "v_mfma_f32_16x16x32_bf16 v[156:159], v[188:191], v[44:47], v[156:159]\n\t" \
  "ds_read_b128 v[188:191], v213 offset:49152\n\t" \
  "global_load_lds_dwordx4 v216, s[92:93] sc1\n\t" \
  "s_waitcnt lgkmcnt(8)\n\t" \
  "v_mfma_f32_16x16x32_bf16 v[144:147], v[192:195], v[48:51], v[144:147]\n\t" \
  "v_mfma_f32_16x16x32_bf16 v[152:155], v[192:195], v[52:55], v[152:155]\n\t" \
  "ds_read_b128 v[192:195], v214 offset:32768\n\t" \
  "v_mfma_f32_16x16x32_bf16 v[148:151], v[196:199], v[48:51], v[148:151]\n\t" \
  "v_mfma_f32_16x16x32_bf16 v[156:159], v[196:199], v[52:55], v[156:159]\n\t" \
  "ds_read_b128 v[196:199], v214 offset:49152\n\t" \
  "s_waitcnt lgkmcnt(4)\n\t" \
  "v_add_f32 v160, v160, v128\n\t" \
  "v_add_f32 v161, v161, v129\n\t" \
  "v_add_f32 v162, v162, v130\n\t" \
  "v_add_f32 v163, v163, v131\n\t" \
  "v_add_f32 v164, v164, v132\n\t" \
  "v_add_f32 v165, v165, v133\n\t" \
  "v_add_f32 v166, v166, v134\n\t" \
  "v_add_f32 v167, v167, v135\n\t" \
  "v_mfma_f32_16x16x32_bf16 v[144:147], v[200:203], v[56:59], v[144:147]\n\t" \
  "v_mfma_f32_16x16x32_bf16 v[152:155], v[200:203], v[60:63], v[152:155]\n\t" \
  "ds_read_b128 v[200:203], v215 offset:32768\n\t" \
  "s_add_u32 m0, %[lw], 33792\n\t" \
  "v_mfma_f32_16x16x32_bf16 v[148:151], v[204:207], v[56:59], v[148:151]\n\t" \
  "v_mfma_f32_16x16x32_bf16 v[156:159], v[204:207], v[60:63], v[156:159]\n\t" \
  "ds_read_b128 v[204:207], v215 offset:49152\n\t" \
  "global_load_lds_dwordx4 v217, s[92:93] sc1\n\t" \
  "v_cvt_pk_bf16_f32 v168, v160, v161\n\t" \
  "v_cvt_pk_bf16_f32 v169, v162, v163\n\t" \
  "v_cvt_pk_bf16_f32 v170, v164, v165\n\t" \
  "v_cvt_pk_bf16_f32 v171, v166, v167\n\t" \
  "buffer_store_dwordx4 v[168:171], v172, %[rt], s90 offen sc1\n\t" \
  "v_mfma_f32_16x16x32_bf16 v[144:147], v[176:179], v[64:67], v[144:147]\n\t" \
  "v_mfma_f32_16x16x32_bf16 v[152:155], v[176:179], v[68:71], v[152:155]\n\t" \
  "ds_read_b128 v[176:179], v212 offset:33024\n\t" \
  "v_mfma_f32_16x16x32_bf16 v[148:151], v[180:183], v[64:67], v[148:151]\n\t" \
  "v_mfma_f32_16x16x32_bf16 v[156:159], v[180:183], v[68:71], v[156:159]\n\t" \
  "ds_read_b128 v[180:183], v212 offset:49408\n\t" \
  "s_waitcnt lgkmcnt(6)\n\t" \
  "v_mfma_f32_16x16x32_bf16 v[144:147], v[184:187], v[72:75], v[144:147]\n\t" \
  "v_mfma_f32_16x16x32_bf16 v[152:155], v[184:187], v[76:79], v[152:155]\n\t" \
  "ds_read_b128 v[184:187], v213 offset:33024\n\t" \
  "s_add_u32 m0, %[lw], 34816\n\t" \
  "v_mfma_f32_16x16x32_bf16 v[148:151], v[188:191], v[72:75], v[148:151]\n\t" \
  "v_mfma_f32_16x16x32_bf16 v[156:159], v[188:191], v[76:79], v[156:159]\n\t" \
  "ds_read_b128 v[188:191], v213 offset:49408\n\t" \
  "global_load_lds_dwordx4 v218, s[92:93] sc1\n\t" \
  "s_waitcnt lgkmcnt(6)\n\t" \
  "v_mfma_f32_16x16x32_bf16 v[144:147], v[192:195], v[80:83], v[144:147]\n\t" \
  "v_mfma_f32_16x16x32_bf16 v[152:155], v[192:195], v[84:87], v[152:155]\n\t" \
  "ds_read_b128 v[192:195], v214 offset:33024\n\t" \
  "v_mfma_f32_16x16x32_bf16 v[148:151], v[196:199], v[80:83], v[148:151]\n\t" \
  "v_mfma_f32_16x16x32_bf16 v[156:159], v[196:199], v[84:87], v[156:159]\n\t" \
  "ds_read_b128 v[196:199], v214 offset:49408\n\t" \
  "s_waitcnt lgkmcnt(6)\n\t" \
  "v_mfma_f32_16x16x32_bf16 v[144:147], v[200:203], v[88:91], v[144:147]\n\t" \
  "v_mfma_f32_16x16x32_bf16 v[152:155], v[200:203], v[92:95], v[152:155]\n\t" \
  "ds_read_b128 v[200:203], v215 offset:33024\n\t" \
  "s_add_u32 m0, %[lw], 35840\n\t" \
  "v_mfma_f32_16x16x32_bf16 v[148:151], v[204:207], v[88:91], v[148:151]\n\t" \
  "v_mfma_f32_16x16x32_bf16 v[156:159], v[204:207], v[92:95], v[156:159]\n\t" \
  "ds_read_b128 v[204:207], v215 offset:49408\n\t" \
  "global_load_lds_dwordx4 v219, s[92:93] sc1\n\t" \
  "s_cmp_lt_u32 s86, 8\n\t" \
  "s_cbranch_scc1 .Lw31%=\n\t" \
  "s_waitcnt vmcnt(6)\n\t" \
  "s_barrier\n\t" \
  ".Lg31%=:\n\t" \
  "s_add_u32 s92, s92, 65024\n\t" \
  "s_addc_u32 s93, s93, 0\n\t" \
  "s_waitcnt lgkmcnt(6)\n\t" \
  "v_mfma_f32_16x16x32_bf16 v[144:147], v[176:179], v[96:99], v[144:147]\n\t" \
  "v_mfma_f32_16x16x32_bf16 v[152:155], v[176:179], v[100:103], v[152:155]\n\t" \
  "ds_read_b128 v[176:179], v208 offset:0\n\t" \
  "v_mfma_f32_16x16x32_bf16 v[148:151], v[180:183], v[96:99], v[148:151]\n\t" \
  "v_mfma_f32_16x16x32_bf16 v[156:159], v[180:183], v[100:103], v[156:159]\n\t" \
  "ds_read_b128 v[180:183], v208 offset:16384\n\t" \
  "s_waitcnt lgkmcnt(6)\n\t" \
  "v_mfma_f32_16x16x32_bf16 v[144:147], v[184:187], v[104:107], v[144:147]\n\t" \
  "v_mfma_f32_16x16x32_bf16 v[152:155], v[184:187], v[108:111], v[152:155]\n\t" \
  "ds_read_b128 v[184:187], v209 offset:0\n\t" \
  "s_add_u32 m0, %[lw], 65536\n\t" \
  "v_mfma_f32_16x16x32_bf16 v[148:151], v[188:191], v[104:107], v[148:151]\n\t" \
  "v_mfma_f32_16x16x32_bf16 v[156:159], v[188:191], v[108:111], v[156:159]\n\t" \
  "ds_read_b128 v[188:191], v209 offset:16384\n\t" \
  "global_load_lds_dwordx4 v216, s[92:93] sc1\n\t" \
  "s_waitcnt lgkmcnt(6)\n\t" \
  "v_mfma_f32_16x16x32_bf16 v[144:147], v[192:195], v[112:115], v[144:147]\n\t" \
  "v_mfma_f32_16x16x32_bf16 v[152:155], v[192:195], v[116:119], v[152:155]\n\t" \
  "ds_read_b128 v[192:195], v210 offset:0\n\t" \
  "v_mfma_f32_16x16x32_bf16 v[148:151], v[196:199], v[112:115], v[148:151]\n\t" \
  "v_mfma_f32_16x16x32_bf16 v[156:159], v[196:199], v[116:119], v[156:159]\n\t" \
  "ds_read_b128 v[196:199], v210 offset:16384\n\t" \
  "s_waitcnt lgkmcnt(6)\n\t" \
  "v_mfma_f32_16x16x32_bf16 v[144:147], v[200:203], v[120:123], v[144:147]\n\t" \
  "v_mfma_f32_16x16x32_bf16 v[152:155], v[200:203], v[124:127], v[152:155]\n\t" \
  "ds_read_b128 v[200:203], v211 offset:0\n\t" \
  "s_add_u32 m0, %[lw], 66560\n\t" \
  "v_mfma_f32_16x16x32_bf16 v[148:151], v[204:207], v[120:123], v[148:151]\n\t" \
  "v_mfma_f32_16x16x32_bf16 v[156:159], v[204:207], v[124:127], v[156:159]\n\t" \
  "ds_read_b128 v[204:207], v211 offset:16384\n\t" \
  "global_load_lds_dwordx4 v217, s[92:93] sc1\n\t" \
  "s_add_u32 s86, s86, 1\n\t" \
  "s_add_u32 s87, s87, 1\n\t" \
  "s_cmp_lt_u32 s86, %[end]\n\t" \
  "s_cbranch_scc1 .Lp1%=\n\t" \
  "s_branch .Lx1%=\n\t" \
  ".Lf01%=:\n\t" \
  "s_nop 7\n\t" \
  "ds_write_b128 v220, v[152:155] offset:0\n\t" \
  "ds_write_b128 v220, v[156:159] offset:1024\n\t" \
  "s_lshr_b32 s90, s87, 7\n\t" \
  "s_xor_b32 s90, s90, %[s0]\n\t" \
  "s_and_b32 s90, s90, 1\n\t" \
  "s_lshl1_add_u32 s90, s87, s90\n\t" \
  "s_lshl_b32 s90, s90, 13\n\t" \
  "s_cmp_lt_i32 s86, 1\n\t" \
  "s_cselect_b32 s90, 0x70000000, s90\n\t" \
  "s_waitcnt lgkmcnt(0)\n\t" \
  "s_barrier\n\t" \
  "ds_read_b128 v[160:163], v221 offset:0\n\t" \
  "ds_read_b128 v[164:167], v221 offset:1024\n\t" \
  "s_waitcnt lgkmcnt(0)\n\t" \
  "v_add_f32 v160, v160, v144\n\t" \
  "v_add_f32 v161, v161, v145\n\t" \
  "v_add_f32 v162, v162, v146\n\t" \
  "v_add_f32 v163, v163, v147\n\t" \
  "v_add_f32 v164, v164, v148\n\t" \
  "v_add_f32 v165, v165, v149\n\t" \
  "v_add_f32 v166, v166, v150\n\t" \
  "v_add_f32 v167, v167, v151\n\t" \
  "v_cvt_pk_bf16_f32 v168, v160, v161\n\t" \
  "v_cvt_pk_bf16_f32 v169, v162, v163\n\t" \
  "v_cvt_pk_bf16_f32 v170, v164, v165\n\t" \
  "v_cvt_pk_bf16_f32 v171, v166, v167\n\t" \
  "buffer_store_dwordx4 v[168:171], v172, %[rt], s90 offen sc1\n\t" \
  "s_branch .Lfb01%=\n\t" \
  ".Lf11%=:\n\t" \
  "s_nop 7\n\t" \
  "ds_write_b128 v220, v[136:139] offset:0\n\t" \
  "ds_write_b128 v220, v[140:143] offset:1024\n\t" \
  "s_lshr_b32 s90, s87, 7\n\t" \
  "s_xor_b32 s90, s90, %[s0]\n\t" \
  "s_and_b32 s90, s90, 1\n\t" \
  "s_lshl1_add_u32 s90, s87, s90\n\t" \
  "s_lshl_b32 s90, s90, 13\n\t" \
  "s_cmp_lt_i32 s86, 1\n\t" \
  "s_cselect_b32 s90, 0x70000000, s90\n\t" \
  "s_waitcnt lgkmcnt(0)\n\t" \
  "s_barrier\n\t" \
  "ds_read_b128 v[160:163], v221 offset:0\n\t" \
  "ds_read_b128 v[164:167], v221 offset:1024\n\t" \
  "s_waitcnt lgkmcnt(0)\n\t" \
  "v_add_f32 v160, v160, v128\n\t" \
  "v_add_f32 v161, v161, v129\n\t" \
  "v_add_f32 v162, v162, v130\n\t" \
  "v_add_f32 v163, v163, v131\n\t" \
  "v_add_f32 v164, v164, v132\n\t" \
  "v_add_f32 v165, v165, v133\n\t" \
  "v_add_f32 v166, v166, v134\n\t" \
  "v_add_f32 v167, v167, v135\n\t" \
  "v_cvt_pk_bf16_f32 v168, v160, v161\n\t" \
  "v_cvt_pk_bf16_f32 v169, v162, v163\n\t" \
  "v_cvt_pk_bf16_f32 v170, v164, v165\n\t" \
  "v_cvt_pk_bf16_f32 v171, v166, v167\n\t" \
  "buffer_store_dwordx4 v[168:171], v172, %[rt], s90 offen sc1\n\t" \
  "s_branch .Lfb11%=\n\t" \
  ".Lw01%=:\n\t" \
  "s_waitcnt vmcnt(2)\n\t" \
  "s_barrier\n\t" \
  "s_sub_i32 s91, s86, 1\n\t" \
  "s_max_i32 s88, s88, s91\n\t" \
  "v_mov_b32 v173, s88\n\t" \
  "buffer_store_dword v173, v174, %[rs], 0 offen sc1\n\t" \
  "s_branch .Lg01%=\n\t" \
  ".Lw11%=:\n\t" \
  "s_waitcnt vmcnt(2)\n\t" \
  "s_barrier\n\t" \
  "s_sub_i32 s91, s86, 1\n\t" \
  "s_max_i32 s88, s88, s91\n\t" \
  "v_mov_b32 v173, s88\n\t" \
  "buffer_store_dword v173, v174, %[rs], 0 offen sc1\n\t" \
  "s_branch .Lg11%=\n\t" \
  ".Lw21%=:\n\t" \
  "s_waitcnt vmcnt(2)\n\t" \
  "s_barrier\n\t" \
  "s_sub_i32 s91, s86, 1\n\t" \
  "s_max_i32 s88, s88, s91\n\t" \
  "v_mov_b32 v173, s88\n\t" \
  "buffer_store_dword v173, v174, %[rs], 0 offen sc1\n\t" \
  "s_branch .Lg21%=\n\t" \
  ".Lw31%=:\n\t" \
  "s_waitcnt vmcnt(2)\n\t" \
  "s_barrier\n\t" \
  "s_sub_i32 s91, s86, 1\n\t" \
  "s_max_i32 s88, s88, s91\n\t" \
  "v_mov_b32 v173, s88\n\t" \
  "buffer_store_dword v173, v174, %[rs], 0 offen sc1\n\t" \
  "s_branch .Lg31%=\n\t" \
  ".Lx1%=:\n\t" \
  "s_waitcnt lgkmcnt(0)\n\t" \
  "s_setprio 0\n\t" \
  "s_mov_b32 m0, s94\n\t" \
  ".Le%=:\n\t"
#define PP_ASM_0 \
  "s_cmp_lg_u32 %[kh], 0\n\t" \
  "s_cbranch_scc1 .Lk%=\n\t" \
  "s_mov_b32 s94, m0\n\t" \
  "s_mov_b64 s[92:93], %[x0]\n\t" \
  "s_mov_b32 s86, %[g0]\n\t" \
  "s_add_u32 s87, s86, %[t0]\n\t" \
  "s_add_u32 m0, %[lw], 67584\n\t" \
  "s_nop 0\n\t" \
  "global_load_lds_dwordx4 v250, s[92:93] sc1\n\t" \
  "s_add_u32 m0, %[lw], 68608\n\t" \
  "s_nop 0\n\t" \
  "global_load_lds_dwordx4 v251, s[92:93] sc1\n\t" \
  "s_barrier\n\t" \
  "s_branch .Ls0%=\n\t" \
  ".Lp0%=:\n\t" \
  "s_waitcnt vmcnt(9)\n\t" \
  "s_barrier\n\t" \
  ".Ls0%=:\n\t" \
  "s_add_u32 s92, s92, 512\n\t" \
  "s_addc_u32 s93, s93, 0\n\t" \
  "ds_read_b128 v[176:179], v240 offset:0\n\t" \
  "ds_read_b128 v[180:183], v240 offset:16384\n\t" \
  "ds_read_b128 v[184:187], v241 offset:0\n\t" \
  "ds_read_b128 v[188:191], v241 offset:16384\n\t" \
  "s_add_u32 m0, %[lw], 98304\n\t" \
  "s_nop 0\n\t" \
  "global_load_lds_dwordx4 v248, s[92:93] sc1\n\t" \
  "ds_read_b128 v[192:195], v242 offset:0\n\t" \
  "ds_read_b128 v[196:199], v242 offset:16384\n\t" \
  "ds_read_b128 v[200:203], v243 offset:0\n\t" \
  "ds_read_b128 v[204:207], v243 offset:16384\n\t" \
  "s_add_u32 m0, %[lw], 99328\n\t" \
  "s_nop 0\n\t" \
  "global_load_lds_dwordx4 v249, s[92:93] sc1\n\t" \
  "ds_read_b128 v[208:211], v240 offset:256\n\t" \
  "ds_read_b128 v[212:215], v240 offset:16640\n\t" \
  "ds_read_b128 v[216:219], v241 offset:256\n\t" \
  "ds_read_b128 v[220:223], v241 offset:16640\n\t" \
  "s_add_u32 m0, %[lw], 100352\n\t" \
  "s_nop 0\n\t" \
  "global_load_lds_dwordx4 v250, s[92:93] sc1\n\t" \
  "ds_read_b128 v[224:227], v242 offset:256\n\t" \
  "ds_read_b128 v[228:231], v242 offset:16640\n\t" \
  "ds_read_b128 v[232:235], v243 offset:256\n\t" \
  "ds_read_b128 v[236:239], v243 offset:16640\n\t" \
  "s_add_u32 m0, %[lw], 101376\n\t" \
  "s_nop 0\n\t" \
  "global_load_lds_dwordx4 v251, s[92:93] sc1\n\t" \
  "ds_write_b128 v252, v[152:155] offset:0\n\t" \
  "ds_write_b128 v252, v[156:159] offset:1024\n\t" \
  "s_waitcnt lgkmcnt(0)\n\t" \
  "s_setprio 1\n\t" \
  "v_mfma_f32_16x16x32_bf16 v[128:131], v[0:3], v[176:179], 0\n\t" \
  "v_mfma_f32_16x16x32_bf16 v[132:135], v[4:7], v[176:179], 0\n\t" \
  "v_mfma_f32_16x16x32_bf16 v[136:139], v[0:3], v[180:183], 0\n\t" \
  "v_mfma_f32_16x16x32_bf16 v[140:143], v[4:7], v[180:183], 0\n\t" \
  "v_mfma_f32_16x16x32_bf16 v[128:131], v[8:11], v[184:187], v[128:131]\n\t" \
  "v_mfma_f32_16x16x32_bf16 v[132:135], v[12:15], v[184:187], v[132:135]\n\t" \
  "v_mfma_f32_16x16x32_bf16 v[136:139], v[8:11], v[188:191], v[136:139]\n\t" \
  "v_mfma_f32_16x16x32_bf16 v[140:143], v[12:15], v[188:191], v[140:143]\n\t" \
  "v_mfma_f32_16x16x32_bf16 v[128:131], v[16:19], v[192:195], v[128:131]\n\t" \
  "v_mfma_f32_16x16x32_bf16 v[132:135], v[20:23], v[192:195], v[132:135]\n\t" \
  "v_mfma_f32_16x16x32_bf16 v[136:139], v[16:19], v[196:199], v[136:139]\n\t" \
  "v_mfma_f32_16x16x32_bf16 v[140:143], v[20:23], v[196:199], v[140:143]\n\t" \
  "v_mfma_f32_16x16x32_bf16 v[128:131], v[24:27], v[200:203], v[128:131]\n\t" \
  "v_mfma_f32_16x16x32_bf16 v[132:135], v[28:31], v[200:203], v[132:135]\n\t" \
  "v_mfma_f32_16x16x32_bf16 v[136:139], v[24:27], v[204:207], v[136:139]\n\t" \
  "v_mfma_f32_16x16x32_bf16 v[140:143], v[28:31], v[204:207], v[140:143]\n\t" \
  "v_mfma_f32_16x16x32_bf16 v[128:131], v[32:35], v[208:211], v[128:131]\n\t" \
  "v_mfma_f32_16x16x32_bf16 v[132:135], v[36:39], v[208:211], v[132:135]\n\t" \
  "v_mfma_f32_16x16x32_bf16 v[136:139], v[32:35], v[212:215], v[136:139]\n\t" \
  "v_mfma_f32_16x16x32_bf16 v[140:143], v[36:39], v[212:215], v[140:143]\n\t" \
  "v_mfma_f32_16x16x32_bf16 v[128:131], v[40:43], v[216:219], v[128:131]\n\t" \
  "v_mfma_f32_16x16x32_bf16 v[132:135], v[44:47], v[216:219], v[132:135]\n\t" \
  "v_mfma_f32_16x16x32_bf16 v[136:139], v[40:43], v[220:223], v[136:139]\n\t" \
  "v_mfma_f32_16x16x32_bf16 v[140:143], v[44:47], v[220:223], v[140:143]\n\t" \
  "v_mfma_f32_16x16x32_bf16 v[128:131], v[48:51], v[224:227], v[128:131]\n\t" \
  "v_mfma_f32_16x16x32_bf16 v[132:135], v[52:55], v[224:227], v[132:135]\n\t" \
  "v_mfma_f32_16x16x32_bf16 v[136:139], v[48:51], v[228:231], v[136:139]\n\t" \
  "v_mfma_f32_16x16x32_bf16 v[140:143], v[52:55], v[228:231], v[140:143]\n\t" \
  "v_mfma_f32_16x16x32_bf16 v[128:131], v[56:59], v[232:235], v[128:131]\n\t" \
  "v_mfma_f32_16x16x32_bf16 v[132:135], v[60:63], v[232:235], v[132:135]\n\t" \
  "v_mfma_f32_16x16x32_bf16 v[136:139], v[56:59], v[236:239], v[136:139]\n\t" \
  "v_mfma_f32_16x16x32_bf16 v[140:143], v[60:63], v[236:239], v[140:143]\n\t" \
  "s_setprio 0\n\t" \
  "s_waitcnt vmcnt(4)\n\t" \
  "s_barrier\n\t" \
  "s_add_u32 s92, s92, 65024\n\t" \
  "s_addc_u32 s93, s93, 0\n\t" \
  "ds_read_b128 v[160:163], v253 offset:0\n\t" \
  "ds_read_b128 v[164:167], v253 offset:1024\n\t" \
  "ds_read_b128 v[176:179], v240 offset:32768\n\t" \
  "ds_read_b128 v[180:183], v240 offset:49152\n\t" \
  "ds_read_b128 v[184:187], v241 offset:32768\n\t" \
  "ds_read_b128 v[188:191], v241 offset:49152\n\t" \
  "s_add_u32 m0, %[lw], 0\n\t" \
  "s_nop 0\n\t" \
  "global_load_lds_dwordx4 v248, s[92:93] sc1\n\t" \
  "ds_read_b128 v[192:195], v242 offset:32768\n\t" \
  "ds_read_b128 v[196:199], v242 offset:49152\n\t" \
  "ds_read_b128 v[200:203], v243 offset:32768\n\t" \
  "ds_read_b128 v[204:207], v243 offset:49152\n\t" \
  "s_add_u32 m0, %[lw], 1024\n\t" \
  "s_nop 0\n\t" \
  "global_load_lds_dwordx4 v249, s[92:93] sc1\n\t" \
  "ds_read_b128 v[208:211], v240 offset:33024\n\t" \
  "ds_read_b128 v[212:215], v240 offset:49408\n\t" \
  "ds_read_b128 v[216:219], v241 offset:33024\n\t" \
  "ds_read_b128 v[220:223], v241 offset:49408\n\t" \
  "s_add_u32 m0, %[lw], 2048\n\t" \
  "s_nop 0\n\t" \
  "global_load_lds_dwordx4 v250, s[92:93] sc1\n\t" \
  "ds_read_b128 v[224:227], v242 offset:33024\n\t" \
  "ds_read_b128 v[228:231], v242 offset:49408\n\t" \
  "ds_read_b128 v[232:235], v243 offset:33024\n\t" \
  "ds_read_b128 v[236:239], v243 offset:49408\n\t" \
  "s_add_u32 m0, %[lw], 3072\n\t" \
  "s_nop 0\n\t" \
  "global_load_lds_dwordx4 v251, s[92:93] sc1\n\t" \
  "s_waitcnt lgkmcnt(15)\n\t" \
  "s_waitcnt lgkmcnt(12)\n\t" \
  "s_lshr_b32 s90, s87, 7\n\t" \
  "s_xor_b32 s90, s90, %[s0]\n\t" \
  "s_and_b32 s90, s90, 1\n\t" \
  "s_lshl1_add_u32 s90, s87, s90\n\t" \
  "s_lshl_b32 s90, s90, 13\n\t" \
  "v_add_f32 v160, v160, v144\n\t" \
  "v_add_f32 v161, v161, v145\n\t" \
  "v_add_f32 v162, v162, v146\n\t" \
  "v_add_f32 v163, v163, v147\n\t" \
  "v_add_f32 v164, v164, v148\n\t" \
  "v_add_f32 v165, v165, v149\n\t" \
  "v_add_f32 v166, v166, v150\n\t" \
  "v_add_f32 v167, v167, v151\n\t" \
  "v_cvt_pk_bf16_f32 v168, v160, v161\n\t" \
  "v_cvt_pk_bf16_f32 v169, v162, v163\n\t" \
  "v_cvt_pk_bf16_f32 v170, v164, v165\n\t" \
  "v_cvt_pk_bf16_f32 v171, v166, v167\n\t" \
  "buffer_store_dwordx4 v[168:171], v172, %[rt], s90 offen sc1\n\t" \
  "s_add_u32 s86, s86, 1\n\t" \
  "s_add_u32 s87, s87, 1\n\t" \
  "s_waitcnt lgkmcnt(0)\n\t" \
  "s_setprio 1\n\t" \
  "v_mfma_f32_16x16x32_bf16 v[128:131], v[64:67], v[176:179], v[128:131]\n\t" \
  "v_mfma_f32_16x16x32_bf16 v[132:135], v[68:71], v[176:179], v[132:135]\n\t" \
  "v_mfma_f32_16x16x32_bf16 v[136:139], v[64:67], v[180:183], v[136:139]\n\t" \
  "v_mfma_f32_16x16x32_bf16 v[140:143], v[68:71], v[180:183], v[140:143]\n\t" \
  "v_mfma_f32_16x16x32_bf16 v[128:131], v[72:75], v[184:187], v[128:131]\n\t" \
  "v_mfma_f32_16x16x32_bf16 v[132:135], v[76:79], v[184:187], v[132:135]\n\t" \
  "v_mfma_f32_16x16x32_bf16 v[136:139], v[72:75], v[188:191], v[136:139]\n\t" \
  "v_mfma_f32_16x16x32_bf16 v[140:143], v[76:79], v[188:191], v[140:143]\n\t" \
  "v_mfma_f32_16x16x32_bf16 v[128:131], v[80:83], v[192:195], v[128:131]\n\t" \
  "v_mfma_f32_16x16x32_bf16 v[132:135], v[84:87], v[192:195], v[132:135]\n\t" \
  "v_mfma_f32_16x16x32_bf16 v[136:139], v[80:83], v[196:199], v[136:139]\n\t" \
  "v_mfma_f32_16x16x32_bf16 v[140:143], v[84:87], v[196:199], v[140:143]\n\t" \
  "v_mfma_f32_16x16x32_bf16 v[128:131], v[88:91], v[200:203], v[128:131]\n\t" \
  "v_mfma_f32_16x16x32_bf16 v[132:135], v[92:95], v[200:203], v[132:135]\n\t" \
  "v_mfma_f32_16x16x32_bf16 v[136:139], v[88:91], v[204:207], v[136:139]\n\t" \
  "v_mfma_f32_16x16x32_bf16 v[140:143], v[92:95], v[204:207], v[140:143]\n\t" \
  "v_mfma_f32_16x16x32_bf16 v[128:131], v[96:99], v[208:211], v[128:131]\n\t" \
  "v_mfma_f32_16x16x32_bf16 v[132:135], v[100:103], v[208:211], v[132:135]\n\t" \
  "v_mfma_f32_16x16x32_bf16 v[136:139], v[96:99], v[212:215], v[136:139]\n\t" \
  "v_mfma_f32_16x16x32_bf16 v[140:143], v[100:103], v[212:215], v[140:143]\n\t" \
  "v_mfma_f32_16x16x32_bf16 v[128:131], v[104:107], v[216:219], v[128:131]\n\t" \
  "v_mfma_f32_16x16x32_bf16 v[132:135], v[108:111], v[216:219], v[132:135]\n\t" \
  "v_mfma_f32_16x16x32_bf16 v[136:139], v[104:107], v[220:223], v[136:139]\n\t" \
  "v_mfma_f32_16x16x32_bf16 v[140:143], v[108:111], v[220:223], v[140:143]\n\t" \
  "v_mfma_f32_16x16x32_bf16 v[128:131], v[112:115], v[224:227], v[128:131]\n\t" \
  "v_mfma_f32_16x16x32_bf16 v[132:135], v[116:119], v[224:227], v[132:135]\n\t" \
  "v_mfma_f32_16x16x32_bf16 v[136:139], v[112:115], v[228:231], v[136:139]\n\t" \
  "v_mfma_f32_16x16x32_bf16 v[140:143], v[116:119], v[228:231], v[140:143]\n\t" \
  "v_mfma_f32_16x16x32_bf16 v[128:131], v[120:123], v[232:235], v[128:131]\n\t" \
  "v_mfma_f32_16x16x32_bf16 v[132:135], v[124:127], v[232:235], v[132:135]\n\t" \
  "v_mfma_f32_16x16x32_bf16 v[136:139], v[120:123], v[236:239], v[136:139]\n\t" \
  "v_mfma_f32_16x16x32_bf16 v[140:143], v[124:127], v[236:239], v[140:143]\n\t" \
  "s_setprio 0\n\t" \
  "s_waitcnt vmcnt(9)\n\t" \
  "s_barrier\n\t" \
  "s_add_u32 s92, s92, 512\n\t" \
  "s_addc_u32 s93, s93, 0\n\t" \
  "ds_read_b128 v[176:179], v244 offset:0\n\t" \
  "ds_read_b128 v[180:183], v244 offset:16384\n\t" \
  "ds_read_b128 v[184:187], v245 offset:0\n\t" \
  "ds_read_b128 v[188:191], v245 offset:16384\n\t" \
  "s_add_u32 m0, %[lw], 32768\n\t" \
  "s_nop 0\n\t" \
  "global_load_lds_dwordx4 v248, s[92:93] sc1\n\t" \
  "ds_read_b128 v[192:195], v246 offset:0\n\t" \
  "ds_read_b128 v[196:199], v246 offset:16384\n\t" \
  "ds_read_b128 v[200:203], v247 offset:0\n\t" \
  "ds_read_b128 v[204:207], v247 offset:16384\n\t" \
  "s_add_u32 m0, %[lw], 33792\n\t" \
  "s_nop 0\n\t" \
  "global_load_lds_dwordx4 v249, s[92:93] sc1\n\t" \
  "ds_read_b128 v[208:211], v244 offset:256\n\t" \
  "ds_read_b128 v[212:215], v244 offset:16640\n\t" \
  "ds_read_b128 v[216:219], v245 offset:256\n\t" \
  "ds_read_b128 v[220:223], v245 offset:16640\n\t" \
  "s_add_u32 m0, %[lw], 34816\n\t" \
  "s_nop 0\n\t" \
  "global_load_lds_dwordx4 v250, s[92:93] sc1\n\t" \
  "ds_read_b128 v[224:227], v246 offset:256\n\t" \
  "ds_read_b128 v[228:231], v246 offset:16640\n\t" \
  "ds_read_b128 v[232:235], v247 offset:256\n\t" \
  "ds_read_b128 v[236:239], v247 offset:16640\n\t" \
  "s_add_u32 m0, %[lw], 35840\n\t" \
  "s_nop 0\n\t" \
  "global_load_lds_dwordx4 v251, s[92:93] sc1\n\t" \
  "ds_write_b128 v252, v[136:139] offset:0\n\t" \
  "ds_write_b128 v252, v[140:143] offset:1024\n\t" \
  "s_waitcnt lgkmcnt(0)\n\t" \
  "s_setprio 1\n\t" \
  "v_mfma_f32_16x16x32_bf16 v[144:147], v[0:3], v[176:179], 0\n\t" \
  "v_mfma_f32_16x16x32_bf16 v[148:151], v[4:7], v[176:179], 0\n\t" \
  "v_mfma_f32_16x16x32_bf16 v[152:155], v[0:3], v[180:183], 0\n\t" \
  "v_mfma_f32_16x16x32_bf16 v[156:159], v[4:7], v[180:183], 0\n\t" \
  "v_mfma_f32_16x16x32_bf16 v[144:147], v[8:11], v[184:187], v[144:147]\n\t" \
  "v_mfma_f32_16x16x32_bf16 v[148:151], v[12:15], v[184:187], v[148:151]\n\t" \
  "v_mfma_f32_16x16x32_bf16 v[152:155], v[8:11], v[188:191], v[152:155]\n\t" \
  "v_mfma_f32_16x16x32_bf16 v[156:159], v[12:15], v[188:191], v[156:159]\n\t" \
  "v_mfma_f32_16x16x32_bf16 v[144:147], v[16:19], v[192:195], v[144:147]\n\t" \
  "v_mfma_f32_16x16x32_bf16 v[148:151], v[20:23], v[192:195], v[148:151]\n\t" \
  "v_mfma_f32_16x16x32_bf16 v[152:155], v[16:19], v[196:199], v[152:155]\n\t" \
  "v_mfma_f32_16x16x32_bf16 v[156:159], v[20:23], v[196:199], v[156:159]\n\t" \
  "v_mfma_f32_16x16x32_bf16 v[144:147], v[24:27], v[200:203], v[144:147]\n\t" \
  "v_mfma_f32_16x16x32_bf16 v[148:151], v[28:31], v[200:203], v[148:151]\n\t" \
  "v_mfma_f32_16x16x32_bf16 v[152:155], v[24:27], v[204:207], v[152:155]\n\t" \
  "v_mfma_f32_16x16x32_bf16 v[156:159], v[28:31], v[204:207], v[156:159]\n\t" \
  "v_mfma_f32_16x16x32_bf16 v[144:147], v[32:35], v[208:211], v[144:147]\n\t" \
  "v_mfma_f32_16x16x32_bf16 v[148:151], v[36:39], v[208:211], v[148:151]\n\t" \
  "v_mfma_f32_16x16x32_bf16 v[152:155], v[32:35], v[212:215], v[152:155]\n\t" \
  "v_mfma_f32_16x16x32_bf16 v[156:159], v[36:39], v[212:215], v[156:159]\n\t" \
  "v_mfma_f32_16x16x32_bf16 v[144:147], v[40:43], v[216:219], v[144:147]\n\t" \
  "v_mfma_f32_16x16x32_bf16 v[148:151], v[44:47], v[216:219], v[148:151]\n\t" \
  "v_mfma_f32_16x16x32_bf16 v[152:155], v[40:43], v[220:223], v[152:155]\n\t" \
  "v_mfma_f32_16x16x32_bf16 v[156:159], v[44:47], v[220:223], v[156:159]\n\t" \
  "v_mfma_f32_16x16x32_bf16 v[144:147], v[48:51], v[224:227], v[144:147]\n\t" \
  "v_mfma_f32_16x16x32_bf16 v[148:151], v[52:55], v[224:227], v[148:151]\n\t" \
  "v_mfma_f32_16x16x32_bf16 v[152:155], v[48:51], v[228:231], v[152:155]\n\t" \
  "v_mfma_f32_16x16x32_bf16 v[156:159], v[52:55], v[228:231], v[156:159]\n\t" \
  "v_mfma_f32_16x16x32_bf16 v[144:147], v[56:59], v[232:235], v[144:147]\n\t" \
  "v_mfma_f32_16x16x32_bf16 v[148:151], v[60:63], v[232:235], v[148:151]\n\t" \
  "v_mfma_f32_16x16x32_bf16 v[152:155], v[56:59], v[236:239], v[152:155]\n\t" \
  "v_mfma_f32_16x16x32_bf16 v[156:159], v[60:63], v[236:239], v[156:159]\n\t" \
  "s_setprio 0\n\t" \
  "s_waitcnt vmcnt(4)\n\t" \
  "s_barrier\n\t" \
  "s_add_u32 s92, s92, 65024\n\t" \
  "s_addc_u32 s93, s93, 0\n\t" \
  "ds_read_b128 v[160:163], v253 offset:0\n\t" \
  "ds_read_b128 v[164:167], v253 offset:1024\n\t" \
  "ds_read_b128 v[176:179], v244 offset:32768\n\t" \
  "ds_read_b128 v[180:183], v244 offset:49152\n\t" \
  "ds_read_b128 v[184:187], v245 offset:32768\n\t" \
  "ds_read_b128 v[188:191], v245 offset:49152\n\t" \
  "s_add_u32 m0, %[lw], 65536\n\t" \
  "s_nop 0\n\t" \
  "global_load_lds_dwordx4 v248, s[92:93] sc1\n\t" \
  "ds_read_b128 v[192:195], v246 offset:32768\n\t" \
  "ds_read_b128 v[196:199], v246 offset:49152\n\t" \
  "ds_read_b128 v[200:203], v247 offset:32768\n\t" \
  "ds_read_b128 v[204:207], v247 offset:49152\n\t" \
  "s_add_u32 m0, %[lw], 66560\n\t" \
  "s_nop 0\n\t" \
  "global_load_lds_dwordx4 v249, s[92:93] sc1\n\t" \
  "ds_read_b128 v[208:211], v244 offset:33024\n\t" \
  "ds_read_b128 v[212:215], v244 offset:49408\n\t" \
  "ds_read_b128 v[216:219], v245 offset:33024\n\t" \
  "ds_read_b128 v[220:223], v245 offset:49408\n\t" \
  "s_add_u32 m0, %[lw], 67584\n\t" \
  "s_nop 0\n\t" \
  "global_load_lds_dwordx4 v250, s[92:93] sc1\n\t" \
  "ds_read_b128 v[224:227], v246 offset:33024\n\t" \
  "ds_read_b128 v[228:231], v246 offset:49408\n\t" \
  "ds_read_b128 v[232:235], v247 offset:33024\n\t" \
  "ds_read_b128 v[236:239], v247 offset:49408\n\t" \
  "s_add_u32 m0, %[lw], 68608\n\t" \
  "s_nop 0\n\t" \
  "global_load_lds_dwordx4 v251, s[92:93] sc1\n\t" \
  "s_waitcnt lgkmcnt(15)\n\t" \
  "s_waitcnt lgkmcnt(12)\n\t" \
  "s_lshr_b32 s90, s87, 7\n\t" \
  "s_xor_b32 s90, s90, %[s0]\n\t" \
  "s_and_b32 s90, s90, 1\n\t" \
  "s_lshl1_add_u32 s90, s87, s90\n\t" \
  "s_lshl_b32 s90, s90, 13\n\t" \
  "v_add_f32 v160, v160, v128\n\t" \
  "v_add_f32 v161, v161, v129\n\t" \
  "v_add_f32 v162, v162, v130\n\t" \
  "v_add_f32 v163, v163, v131\n\t" \
  "v_add_f32 v164, v164, v132\n\t" \
  "v_add_f32 v165, v165, v133\n\t" \
  "v_add_f32 v166, v166, v134\n\t" \
  "v_add_f32 v167, v167, v135\n\t" \
  "v_cvt_pk_bf16_f32 v168, v160, v161\n\t" \
  "v_cvt_pk_bf16_f32 v169, v162, v163\n\t" \
  "v_cvt_pk_bf16_f32 v170, v164, v165\n\t" \
  "v_cvt_pk_bf16_f32 v171, v166, v167\n\t" \
  "buffer_store_dwordx4 v[168:171], v172, %[rt], s90 offen sc1\n\t" \
  "s_add_u32 s86, s86, 1\n\t" \
  "s_add_u32 s87, s87, 1\n\t" \
  "s_waitcnt lgkmcnt(0)\n\t" \
  "s_setprio 1\n\t" \
  "v_mfma_f32_16x16x32_bf16 v[144:147], v[64:67], v[176:179], v[144:147]\n\t" \
  "v_mfma_f32_16x16x32_bf16 v[148:151], v[68:71], v[176:179], v[148:151]\n\t" \
  "v_mfma_f32_16x16x32_bf16 v[152:155], v[64:67], v[180:183], v[152:155]\n\t" \
  "v_mfma_f32_16x16x32_bf16 v[156:159], v[68:71], v[180:183], v[156:159]\n\t" \
  "v_mfma_f32_16x16x32_bf16 v[144:147], v[72:75], v[184:187], v[144:147]\n\t" \
  "v_mfma_f32_16x16x32_bf16 v[148:151], v[76:79], v[184:187], v[148:151]\n\t" \
  "v_mfma_f32_16x16x32_bf16 v[152:155], v[72:75], v[188:191], v[152:155]\n\t" \
  "v_mfma_f32_16x16x32_bf16 v[156:159], v[76:79], v[188:191], v[156:159]\n\t" \
  "v_mfma_f32_16x16x32_bf16 v[144:147], v[80:83], v[192:195], v[144:147]\n\t" \
  "v_mfma_f32_16x16x32_bf16 v[148:151], v[84:87], v[192:195], v[148:151]\n\t" \
  "v_mfma_f32_16x16x32_bf16 v[152:155], v[80:83], v[196:199], v[152:155]\n\t" \
  "v_mfma_f32_16x16x32_bf16 v[156:159], v[84:87], v[196:199], v[156:159]\n\t" \
  "v_mfma_f32_16x16x32_bf16 v[144:147], v[88:91], v[200:203], v[144:147]\n\t" \
  "v_mfma_f32_16x16x32_bf16 v[148:151], v[92:95], v[200:203], v[148:151]\n\t" \
  "v_mfma_f32_16x16x32_bf16 v[152:155], v[88:91], v[204:207], v[152:155]\n\t" \
  "v_mfma_f32_16x16x32_bf16 v[156:159], v[92:95], v[204:207], v[156:159]\n\t" \
  "v_mfma_f32_16x16x32_bf16 v[144:147], v[96:99], v[208:211], v[144:147]\n\t" \
  "v_mfma_f32_16x16x32_bf16 v[148:151], v[100:103], v[208:211], v[148:151]\n\t" \
  "v_mfma_f32_16x16x32_bf16 v[152:155], v[96:99], v[212:215], v[152:155]\n\t" \
  "v_mfma_f32_16x16x32_bf16 v[156:159], v[100:103], v[212:215], v[156:159]\n\t" \
  "v_mfma_f32_16x16x32_bf16 v[144:147], v[104:107], v[216:219], v[144:147]\n\t" \
  "v_mfma_f32_16x16x32_bf16 v[148:151], v[108:111], v[216:219], v[148:151]\n\t" \
  "v_mfma_f32_16x16x32_bf16 v[152:155], v[104:107], v[220:223], v[152:155]\n\t" \
  "v_mfma_f32_16x16x32_bf16 v[156:159], v[108:111], v[220:223], v[156:159]\n\t" \
  "v_mfma_f32_16x16x32_bf16 v[144:147], v[112:115], v[224:227], v[144:147]\n\t" \
  "v_mfma_f32_16x16x32_bf16 v[148:151], v[116:119], v[224:227], v[148:151]\n\t" \
  "v_mfma_f32_16x16x32_bf16 v[152:155], v[112:115], v[228:231], v[152:155]\n\t" \
  "v_mfma_f32_16x16x32_bf16 v[156:159], v[116:119], v[228:231], v[156:159]\n\t" \
  "v_mfma_f32_16x16x32_bf16 v[144:147], v[120:123], v[232:235], v[144:147]\n\t" \
  "v_mfma_f32_16x16x32_bf16 v[148:151], v[124:127], v[232:235], v[148:151]\n\t" \
  "v_mfma_f32_16x16x32_bf16 v[152:155], v[120:123], v[236:239], v[152:155]\n\t" \
  "v_mfma_f32_16x16x32_bf16 v[156:159], v[124:127], v[236:239], v[156:159]\n\t" \
  "s_setprio 0\n\t" \
  "s_cmp_lt_u32 s86, 64\n\t" \
  "s_cbranch_scc1 .Lp0%=\n\t" \
  "s_barrier\n\t" \
  "s_nop 2\n\t" \
  "ds_write_b128 v252, v[152:155] offset:0\n\t" \
  "ds_write_b128 v252, v[156:159] offset:1024\n\t" \
  "s_waitcnt lgkmcnt(0)\n\t" \
  "s_barrier\n\t" \
  "ds_read_b128 v[160:163], v253 offset:0\n\t" \
  "ds_read_b128 v[164:167], v253 offset:1024\n\t" \
  "s_waitcnt lgkmcnt(0)\n\t" \
  "s_lshr_b32 s90, s87, 7\n\t" \
  "s_xor_b32 s90, s90, %[s0]\n\t" \
  "s_and_b32 s90, s90, 1\n\t" \
  "s_lshl1_add_u32 s90, s87, s90\n\t" \
  "s_lshl_b32 s90, s90, 13\n\t" \
  "v_add_f32 v160, v160, v144\n\t" \
  "v_add_f32 v161, v161, v145\n\t" \
  "v_add_f32 v162, v162, v146\n\t" \
  "v_add_f32 v163, v163, v147\n\t" \
  "v_add_f32 v164, v164, v148\n\t" \
  "v_add_f32 v165, v165, v149\n\t" \
  "v_add_f32 v166, v166, v150\n\t" \
  "v_add_f32 v167, v167, v151\n\t" \
  "v_cvt_pk_bf16_f32 v168, v160, v161\n\t" \
  "v_cvt_pk_bf16_f32 v169, v162, v163\n\t" \
  "v_cvt_pk_bf16_f32 v170, v164, v165\n\t" \
  "v_cvt_pk_bf16_f32 v171, v166, v167\n\t" \
  "buffer_store_dwordx4 v[168:171], v172, %[rt], s90 offen sc1\n\t" \
  "s_waitcnt vmcnt(0)\n\t" \
  "s_barrier\n\t" \
  "s_mov_b32 m0, s94\n\t" \
  "s_branch .Le%=\n\t" \
  ".Lk%=:\n\t" \
  "s_mov_b32 s94, m0\n\t" \
  "s_mov_b64 s[92:93], %[x0]\n\t" \
  "s_mov_b32 s86, %[g0]\n\t" \
  "s_add_u32 s87, s86, %[t0]\n\t" \
  "s_add_u32 m0, %[lw], 67584\n\t" \
  "s_nop 0\n\t" \
  "global_load_lds_dwordx4 v250, s[92:93] sc1\n\t" \
  "s_add_u32 m0, %[lw], 68608\n\t" \
  "s_nop 0\n\t" \
  "global_load_lds_dwordx4 v251, s[92:93] sc1\n\t" \
  "s_barrier\n\t" \
  "s_branch .Ls1%=\n\t" \
  ".Lp1%=:\n\t" \
  "s_waitcnt vmcnt(10)\n\t" \
  "s_barrier\n\t" \
  "s_setprio 2\n\t" \
  "v_mfma_f32_16x16x32_bf16 v[144:147], v[64:67], v[176:179], v[144:147]\n\t" \
  "v_mfma_f32_16x16x32_bf16 v[148:151], v[68:71], v[176:179], v[148:151]\n\t" \
  "v_mfma_f32_16x16x32_bf16 v[152:155], v[64:67], v[180:183], v[152:155]\n\t" \
  "v_mfma_f32_16x16x32_bf16 v[156:159], v[68:71], v[180:183], v[156:159]\n\t" \
  "v_mfma_f32_16x16x32_bf16 v[144:147], v[72:75], v[184:187], v[144:147]\n\t" \
  "v_mfma_f32_16x16x32_bf16 v[148:151], v[76:79], v[184:187], v[148:151]\n\t" \
  "v_mfma_f32_16x16x32_bf16 v[152:155], v[72:75], v[188:191], v[152:155]\n\t" \
  "v_mfma_f32_16x16x32_bf16 v[156:159], v[76:79], v[188:191], v[156:159]\n\t" \
  "v_mfma_f32_16x16x32_bf16 v[144:147], v[80:83], v[192:195], v[144:147]\n\t" \
  "v_mfma_f32_16x16x32_bf16 v[148:151], v[84:87], v[192:195], v[148:151]\n\t" \
  "v_mfma_f32_16x16x32_bf16 v[152:155], v[80:83], v[196:199], v[152:155]\n\t" \
  "v_mfma_f32_16x16x32_bf16 v[156:159], v[84:87], v[196:199], v[156:159]\n\t" \
  "v_mfma_f32_16x16x32_bf16 v[144:147], v[88:91], v[200:203], v[144:147]\n\t" \
  "v_mfma_f32_16x16x32_bf16 v[148:151], v[92:95], v[200:203], v[148:151]\n\t" \
  "v_mfma_f32_16x16x32_bf16 v[152:155], v[88:91], v[204:207], v[152:155]\n\t" \
  "v_mfma_f32_16x16x32_bf16 v[156:159], v[92:95], v[204:207], v[156:159]\n\t" \
  "v_mfma_f32_16x16x32_bf16 v[144:147], v[96:99], v[208:211], v[144:147]\n\t" \
  "v_mfma_f32_16x16x32_bf16 v[148:151], v[100:103], v[208:211], v[148:151]\n\t" \
  "v_mfma_f32_16x16x32_bf16 v[152:155], v[96:99], v[212:215], v[152:155]\n\t" \
  "v_mfma_f32_16x16x32_bf16 v[156:159], v[100:103], v[212:215], v[156:159]\n\t" \
  "v_mfma_f32_16x16x32_bf16 v[144:147], v[104:107], v[216:219], v[144:147]\n\t" \
  "v_mfma_f32_16x16x32_bf16 v[148:151], v[108:111], v[216:219], v[148:151]\n\t" \
  "v_mfma_f32_16x16x32_bf16 v[152:155], v[104:107], v[220:223], v[152:155]\n\t" \
  "v_mfma_f32_16x16x32_bf16 v[156:159], v[108:111], v[220:223], v[156:159]\n\t" \
  "v_mfma_f32_16x16x32_bf16 v[144:147], v[112:115], v[224:227], v[144:147]\n\t" \
  "v_mfma_f32_16x16x32_bf16 v[148:151], v[116:119], v[224:227], v[148:151]\n\t" \
  "v_mfma_f32_16x16x32_bf16 v[152:155], v[112:115], v[228:231], v[152:155]\n\t" \
  "v_mfma_f32_16x16x32_bf16 v[156:159], v[116:119], v[228:231], v[156:159]\n\t" \
  "v_mfma_f32_16x16x32_bf16 v[144:147], v[120:123], v[232:235], v[144:147]\n\t" \
  "v_mfma_f32_16x16x32_bf16 v[148:151], v[124:127], v[232:235], v[148:151]\n\t" \
  "v_mfma_f32_16x16x32_bf16 v[152:155], v[120:123], v[236:239], v[152:155]\n\t" \
  "v_mfma_f32_16x16x32_bf16 v[156:159], v[124:127], v[236:239], v[156:159]\n\t" \
  "s_setprio 0\n\t" \
  ".Ls1%=:\n\t" \
  "s_add_u32 s92, s92, 512\n\t" \
  "s_addc_u32 s93, s93, 0\n\t" \
  "ds_read_b128 v[176:179], v240 offset:0\n\t" \
  "ds_read_b128 v[180:183], v240 offset:16384\n\t" \
  "ds_read_b128 v[184:187], v241 offset:0\n\t" \
  "ds_read_b128 v[188:191], v241 offset:16384\n\t" \
  "s_add_u32 m0, %[lw], 98304\n\t" \
  "s_nop 0\n\t" \
  "global_load_lds_dwordx4 v248, s[92:93] sc1\n\t" \
  "ds_read_b128 v[192:195], v242 offset:0\n\t" \
  "ds_read_b128 v[196:199], v242 offset:16384\n\t" \
  "ds_read_b128 v[200:203], v243 offset:0\n\t" \
  "ds_read_b128 v[204:207], v243 offset:16384\n\t" \
  "s_add_u32 m0, %[lw], 99328\n\t" \
  "s_nop 0\n\t" \
  "global_load_lds_dwordx4 v249, s[92:93] sc1\n\t" \
  "ds_read_b128 v[208:211], v240 offset:256\n\t" \
  "ds_read_b128 v[212:215], v240 offset:16640\n\t" \
  "ds_read_b128 v[216:219], v241 offset:256\n\t" \
  "ds_read_b128 v[220:223], v241 offset:16640\n\t" \
  "s_add_u32 m0, %[lw], 100352\n\t" \
  "s_nop 0\n\t" \
  "global_load_lds_dwordx4 v250, s[92:93] sc1\n\t" \
  "ds_read_b128 v[224:227], v242 offset:256\n\t" \
  "ds_read_b128 v[228:231], v242 offset:16640\n\t" \
  "ds_read_b128 v[232:235], v243 offset:256\n\t" \
  "ds_read_b128 v[236:239], v243 offset:16640\n\t" \
  "s_add_u32 m0, %[lw], 101376\n\t" \
  "s_nop 0\n\t" \
  "global_load_lds_dwordx4 v251, s[92:93] sc1\n\t" \
  "ds_write_b128 v252, v[152:155] offset:0\n\t" \
  "ds_write_b128 v252, v[156:159] offset:1024\n\t" \
  "s_waitcnt lgkmcnt(0)\n\t" \
  "s_waitcnt vmcnt(4)\n\t" \
  "s_barrier\n\t" \
  "s_setprio 2\n\t" \
  "v_mfma_f32_16x16x32_bf16 v[128:131], v[0:3], v[176:179], 0\n\t" \
  "v_mfma_f32_16x16x32_bf16 v[132:135], v[4:7], v[176:179], 0\n\t" \
  "v_mfma_f32_16x16x32_bf16 v[136:139], v[0:3], v[180:183], 0\n\t" \
  "v_mfma_f32_16x16x32_bf16 v[140:143], v[4:7], v[180:183], 0\n\t" \
  "v_mfma_f32_16x16x32_bf16 v[128:131], v[8:11], v[184:187], v[128:131]\n\t" \
  "v_mfma_f32_16x16x32_bf16 v[132:135], v[12:15], v[184:187], v[132:135]\n\t" \
  "v_mfma_f32_16x16x32_bf16 v[136:139], v[8:11], v[188:191], v[136:139]\n\t" \
  "v_mfma_f32_16x16x32_bf16 v[140:143], v[12:15], v[188:191], v[140:143]\n\t" \
  "v_mfma_f32_16x16x32_bf16 v[128:131], v[16:19], v[192:195], v[128:131]\n\t" \
  "v_mfma_f32_16x16x32_bf16 v[132:135], v[20:23], v[192:195], v[132:135]\n\t" \
  "v_mfma_f32_16x16x32_bf16 v[136:139], v[16:19], v[196:199], v[136:139]\n\t" \
  "v_mfma_f32_16x16x32_bf16 v[140:143], v[20:23], v[196:199], v[140:143]\n\t" \
  "v_mfma_f32_16x16x32_bf16 v[128:131], v[24:27], v[200:203], v[128:131]\n\t" \
  "v_mfma_f32_16x16x32_bf16 v[132:135], v[28:31], v[200:203], v[132:135]\n\t" \
  "v_mfma_f32_16x16x32_bf16 v[136:139], v[24:27], v[204:207], v[136:139]\n\t" \
  "v_mfma_f32_16x16x32_bf16 v[140:143], v[28:31], v[204:207], v[140:143]\n\t" \
  "v_mfma_f32_16x16x32_bf16 v[128:131], v[32:35], v[208:211], v[128:131]\n\t" \
  "v_mfma_f32_16x16x32_bf16 v[132:135], v[36:39], v[208:211], v[132:135]\n\t" \
  "v_mfma_f32_16x16x32_bf16 v[136:139], v[32:35], v[212:215], v[136:139]\n\t" \
  "v_mfma_f32_16x16x32_bf16 v[140:143], v[36:39], v[212:215], v[140:143]\n\t" \
  "v_mfma_f32_16x16x32_bf16 v[128:131], v[40:43], v[216:219], v[128:131]\n\t" \
  "v_mfma_f32_16x16x32_bf16 v[132:135], v[44:47], v[216:219], v[132:135]\n\t" \
  "v_mfma_f32_16x16x32_bf16 v[136:139], v[40:43], v[220:223], v[136:139]\n\t" \
  "v_mfma_f32_16x16x32_bf16 v[140:143], v[44:47], v[220:223], v[140:143]\n\t" \
  "v_mfma_f32_16x16x32_bf16 v[128:131], v[48:51], v[224:227], v[128:131]\n\t" \
  "v_mfma_f32_16x16x32_bf16 v[132:135], v[52:55], v[224:227], v[132:135]\n\t" \
  "v_mfma_f32_16x16x32_bf16 v[136:139], v[48:51], v[228:231], v[136:139]\n\t" \
  "v_mfma_f32_16x16x32_bf16 v[140:143], v[52:55], v[228:231], v[140:143]\n\t" \
  "v_mfma_f32_16x16x32_bf16 v[128:131], v[56:59], v[232:235], v[128:131]\n\t" \
  "v_mfma_f32_16x16x32_bf16 v[132:135], v[60:63], v[232:235], v[132:135]\n\t" \
  "v_mfma_f32_16x16x32_bf16 v[136:139], v[56:59], v[236:239], v[136:139]\n\t" \
  "v_mfma_f32_16x16x32_bf16 v[140:143], v[60:63], v[236:239], v[140:143]\n\t" \
  "s_setprio 0\n\t" \
  "s_add_u32 s92, s92, 65024\n\t" \
  "s_addc_u32 s93, s93, 0\n\t" \
  "ds_read_b128 v[160:163], v253 offset:0\n\t" \
  "ds_read_b128 v[164:167], v253 offset:1024\n\t" \
  "ds_read_b128 v[176:179], v240 offset:32768\n\t" \
  "ds_read_b128 v[180:183], v240 offset:49152\n\t" \
  "ds_read_b128 v[184:187], v241 offset:32768\n\t" \
  "ds_read_b128 v[188:191], v241 offset:49152\n\t" \
  "s_add_u32 m0, %[lw], 0\n\t" \
  "s_nop 0\n\t" \
  "global_load_lds_dwordx4 v248, s[92:93] sc1\n\t" \
  "ds_read_b128 v[192:195], v242 offset:32768\n\t" \
  "ds_read_b128 v[196:199], v242 offset:49152\n\t" \
  "ds_read_b128 v[200:203], v243 offset:32768\n\t" \
  "ds_read_b128 v[204:207], v243 offset:49152\n\t" \
  "s_add_u32 m0, %[lw], 1024\n\t" \
  "s_nop 0\n\t" \
  "global_load_lds_dwordx4 v249, s[92:93] sc1\n\t" \
  "ds_read_b128 v[208:211], v240 offset:33024\n\t" \
  "ds_read_b128 v[212:215], v240 offset:49408\n\t" \
  "ds_read_b128 v[216:219], v241 offset:33024\n\t" \
  "ds_read_b128 v[220:223], v241 offset:49408\n\t" \
  "s_add_u32 m0, %[lw], 2048\n\t" \
  "s_nop 0\n\t" \
  "global_load_lds_dwordx4 v250, s[92:93] sc1\n\t" \
  "ds_read_b128 v[224:227], v242 offset:33024\n\t" \
  "ds_read_b128 v[228:231], v242 offset:49408\n\t" \
  "ds_read_b128 v[232:235], v243 offset:33024\n\t" \
  "ds_read_b128 v[236:239], v243 offset:49408\n\t" \
  "s_add_u32 m0, %[lw], 3072\n\t" \
  "s_nop 0\n\t" \
  "global_load_lds_dwordx4 v251, s[92:93] sc1\n\t" \
  "s_waitcnt lgkmcnt(15)\n\t" \
  "s_waitcnt lgkmcnt(12)\n\t" \
  "s_sub_i32 s91, s86, 2\n\t" \
  "v_mov_b32 v173, s91\n\t" \
  "buffer_store_dword v173, v174, %[rs], 0 offen sc1\n\t" \
  "s_lshr_b32 s90, s87, 7\n\t" \
  "s_xor_b32 s90, s90, %[s0]\n\t" \
  "s_and_b32 s90, s90, 1\n\t" \
  "s_lshl1_add_u32 s90, s87, s90\n\t" \
  "s_lshl_b32 s90, s90, 13\n\t" \
  "v_add_f32 v160, v160, v144\n\t" \
  "v_add_f32 v161, v161, v145\n\t" \
  "v_add_f32 v162, v162, v146\n\t" \
  "v_add_f32 v163, v163, v147\n\t" \
  "v_add_f32 v164, v164, v148\n\t" \
  "v_add_f32 v165, v165, v149\n\t" \
  "v_add_f32 v166, v166, v150\n\t" \
  "v_add_f32 v167, v167, v151\n\t" \
  "v_cvt_pk_bf16_f32 v168, v160, v161\n\t" \
  "v_cvt_pk_bf16_f32 v169, v162, v163\n\t" \
  "v_cvt_pk_bf16_f32 v170, v164, v165\n\t" \
  "v_cvt_pk_bf16_f32 v171, v166, v167\n\t" \
  "buffer_store_dwordx4 v[168:171], v172, %[rt], s90 offen sc1\n\t" \
  "s_add_u32 s86, s86, 1\n\t" \
  "s_add_u32 s87, s87, 1\n\t" \
  "s_waitcnt lgkmcnt(0)\n\t" \
  "s_waitcnt vmcnt(10)\n\t" \
  "s_barrier\n\t" \
  "s_setprio 2\n\t" \
  "v_mfma_f32_16x16x32_bf16 v[128:131], v[64:67], v[176:179], v[128:131]\n\t" \
  "v_mfma_f32_16x16x32_bf16 v[132:135], v[68:71], v[176:179], v[132:135]\n\t" \
  "v_mfma_f32_16x16x32_bf16 v[136:139], v[64:67], v[180:183], v[136:139]\n\t" \
  "v_mfma_f32_16x16x32_bf16 v[140:143], v[68:71], v[180:183], v[140:143]\n\t" \
  "v_mfma_f32_16x16x32_bf16 v[128:131], v[72:75], v[184:187], v[128:131]\n\t" \
  "v_mfma_f32_16x16x32_bf16 v[132:135], v[76:79], v[184:187], v[132:135]\n\t" \
  "v_mfma_f32_16x16x32_bf16 v[136:139], v[72:75], v[188:191], v[136:139]\n\t" \
  "v_mfma_f32_16x16x32_bf16 v[140:143], v[76:79], v[188:191], v[140:143]\n\t" \
  "v_mfma_f32_16x16x32_bf16 v[128:131], v[80:83], v[192:195], v[128:131]\n\t" \
  "v_mfma_f32_16x16x32_bf16 v[132:135], v[84:87], v[192:195], v[132:135]\n\t" \
  "v_mfma_f32_16x16x32_bf16 v[136:139], v[80:83], v[196:199], v[136:139]\n\t" \
  "v_mfma_f32_16x16x32_bf16 v[140:143], v[84:87], v[196:199], v[140:143]\n\t" \
  "v_mfma_f32_16x16x32_bf16 v[128:131], v[88:91], v[200:203], v[128:131]\n\t" \
  "v_mfma_f32_16x16x32_bf16 v[132:135], v[92:95], v[200:203], v[132:135]\n\t" \
  "v_mfma_f32_16x16x32_bf16 v[136:139], v[88:91], v[204:207], v[136:139]\n\t" \
  "v_mfma_f32_16x16x32_bf16 v[140:143], v[92:95], v[204:207], v[140:143]\n\t" \
  "v_mfma_f32_16x16x32_bf16 v[128:131], v[96:99], v[208:211], v[128:131]\n\t" \
  "v_mfma_f32_16x16x32_bf16 v[132:135], v[100:103], v[208:211], v[132:135]\n\t" \
  "v_mfma_f32_16x16x32_bf16 v[136:139], v[96:99], v[212:215], v[136:139]\n\t" \
  "v_mfma_f32_16x16x32_bf16 v[140:143], v[100:103], v[212:215], v[140:143]\n\t" \
  "v_mfma_f32_16x16x32_bf16 v[128:131], v[104:107], v[216:219], v[128:131]\n\t" \
  "v_mfma_f32_16x16x32_bf16 v[132:135], v[108:111], v[216:219], v[132:135]\n\t" \
  "v_mfma_f32_16x16x32_bf16 v[136:139], v[104:107], v[220:223], v[136:139]\n\t" \
  "v_mfma_f32_16x16x32_bf16 v[140:143], v[108:111], v[220:223], v[140:143]\n\t" \
  "v_mfma_f32_16x16x32_bf16 v[128:131], v[112:115], v[224:227], v[128:131]\n\t" \
  "v_mfma_f32_16x16x32_bf16 v[132:135], v[116:119], v[224:227], v[132:135]\n\t" \
  "v_mfma_f32_16x16x32_bf16 v[136:139], v[112:115], v[228:231], v[136:139]\n\t" \
  "v_mfma_f32_16x16x32_bf16 v[140:143], v[116:119], v[228:231], v[140:143]\n\t" \
  "v_mfma_f32_16x16x32_bf16 v[128:131], v[120:123], v[232:235], v[128:131]\n\t" \
  "v_mfma_f32_16x16x32_bf16 v[132:135], v[124:127], v[232:235], v[132:135]\n\t" \
  "v_mfma_f32_16x16x32_bf16 v[136:139], v[120:123], v[236:239], v[136:139]\n\t" \
  "v_mfma_f32_16x16x32_bf16 v[140:143], v[124:127], v[236:239], v[140:143]\n\t" \
  "s_setprio 0\n\t" \
  "s_add_u32 s92, s92, 512\n\t" \
  "s_addc_u32 s93, s93, 0\n\t" \
  "ds_read_b128 v[176:179], v244 offset:0\n\t" \
  "ds_read_b128 v[180:183], v244 offset:16384\n\t" \
  "ds_read_b128 v[184:187], v245 offset:0\n\t" \
  "ds_read_b128 v[188:191], v245 offset:16384\n\t" \
  "s_add_u32 m0, %[lw], 32768\n\t" \
  "s_nop 0\n\t" \
  "global_load_lds_dwordx4 v248, s[92:93] sc1\n\t" \
  "ds_read_b128 v[192:195], v246 offset:0\n\t" \
  "ds_read_b128 v[196:199], v246 offset:16384\n\t" \
  "ds_read_b128 v[200:203], v247 offset:0\n\t" \
  "ds_read_b128 v[204:207], v247 offset:16384\n\t" \
  "s_add_u32 m0, %[lw], 33792\n\t" \
  "s_nop 0\n\t" \
  "global_load_lds_dwordx4 v249, s[92:93] sc1\n\t" \
  "ds_read_b128 v[208:211], v244 offset:256\n\t" \
  "ds_read_b128 v[212:215], v244 offset:16640\n\t" \
  "ds_read_b128 v[216:219], v245 offset:256\n\t" \
  "ds_read_b128 v[220:223], v245 offset:16640\n\t" \
  "s_add_u32 m0, %[lw], 34816\n\t" \
  "s_nop 0\n\t" \
  "global_load_lds_dwordx4 v250, s[92:93] sc1\n\t" \
  "ds_read_b128 v[224:227], v246 offset:256\n\t" \
  "ds_read_b128 v[228:231], v246 offset:16640\n\t" \
  "ds_read_b128 v[232:235], v247 offset:256\n\t" \
  "ds_read_b128 v[236:239], v247 offset:16640\n\t" \
  "s_add_u32 m0, %[lw], 35840\n\t" \
  "s_nop 0\n\t" \
  "global_load_lds_dwordx4 v251, s[92:93] sc1\n\t" \
  "ds_write_b128 v252, v[136:139] offset:0\n\t" \
  "ds_write_b128 v252, v[140:143] offset:1024\n\t" \
  "s_waitcnt lgkmcnt(0)\n\t" \
  "s_waitcnt vmcnt(4)\n\t" \
  "s_barrier\n\t" \
  "s_setprio 2\n\t" \
  "v_mfma_f32_16x16x32_bf16 v[144:147], v[0:3], v[176:179], 0\n\t" \
  "v_mfma_f32_16x16x32_bf16 v[148:151], v[4:7], v[176:179], 0\n\t" \
  "v_mfma_f32_16x16x32_bf16 v[152:155], v[0:3], v[180:183], 0\n\t" \
  "v_mfma_f32_16x16x32_bf16 v[156:159], v[4:7], v[180:183], 0\n\t" \
  "v_mfma_f32_16x16x32_bf16 v[144:147], v[8:11], v[184:187], v[144:147]\n\t" \
  "v_mfma_f32_16x16x32_bf16 v[148:151], v[12:15], v[184:187], v[148:151]\n\t" \
  "v_mfma_f32_16x16x32_bf16 v[152:155], v[8:11], v[188:191], v[152:155]\n\t" \
  "v_mfma_f32_16x16x32_bf16 v[156:159], v[12:15], v[188:191], v[156:159]\n\t" \
  "v_mfma_f32_16x16x32_bf16 v[144:147], v[16:19], v[192:195], v[144:147]\n\t" \
  "v_mfma_f32_16x16x32_bf16 v[148:151], v[20:23], v[192:195], v[148:151]\n\t" \
  "v_mfma_f32_16x16x32_bf16 v[152:155], v[16:19], v[196:199], v[152:155]\n\t" \
  "v_mfma_f32_16x16x32_bf16 v[156:159], v[20:23], v[196:199], v[156:159]\n\t" \
  "v_mfma_f32_16x16x32_bf16 v[144:147], v[24:27], v[200:203], v[144:147]\n\t" \
  "v_mfma_f32_16x16x32_bf16 v[148:151], v[28:31], v[200:203], v[148:151]\n\t" \
  "v_mfma_f32_16x16x32_bf16 v[152:155], v[24:27], v[204:207], v[152:155]\n\t" \
  "v_mfma_f32_16x16x32_bf16 v[156:159], v[28:31], v[204:207], v[156:159]\n\t" \
  "v_mfma_f32_16x16x32_bf16 v[144:147], v[32:35], v[208:211], v[144:147]\n\t" \
  "v_mfma_f32_16x16x32_bf16 v[148:151], v[36:39], v[208:211], v[148:151]\n\t" \
  "v_mfma_f32_16x16x32_bf16 v[152:155], v[32:35], v[212:215], v[152:155]\n\t" \
  "v_mfma_f32_16x16x32_bf16 v[156:159], v[36:39], v[212:215], v[156:159]\n\t" \
  "v_mfma_f32_16x16x32_bf16 v[144:147], v[40:43], v[216:219], v[144:147]\n\t" \
  "v_mfma_f32_16x16x32_bf16 v[148:151], v[44:47], v[216:219], v[148:151]\n\t" \
  "v_mfma_f32_16x16x32_bf16 v[152:155], v[40:43], v[220:223], v[152:155]\n\t" \
  "v_mfma_f32_16x16x32_bf16 v[156:159], v[44:47], v[220:223], v[156:159]\n\t" \
  "v_mfma_f32_16x16x32_bf16 v[144:147], v[48:51], v[224:227], v[144:147]\n\t" \
  "v_mfma_f32_16x16x32_bf16 v[148:151], v[52:55], v[224:227], v[148:151]\n\t" \
  "v_mfma_f32_16x16x32_bf16 v[152:155], v[48:51], v[228:231], v[152:155]\n\t" \
  "v_mfma_f32_16x16x32_bf16 v[156:159], v[52:55], v[228:231], v[156:159]\n\t" \
  "v_mfma_f32_16x16x32_bf16 v[144:147], v[56:59], v[232:235], v[144:147]\n\t" \
  "v_mfma_f32_16x16x32_bf16 v[148:151], v[60:63], v[232:235], v[148:151]\n\t" \
  "v_mfma_f32_16x16x32_bf16 v[152:155], v[56:59], v[236:239], v[152:155]\n\t" \
  "v_mfma_f32_16x16x32_bf16 v[156:159], v[60:63], v[236:239], v[156:159]\n\t" \
  "s_setprio 0\n\t" \
  "s_add_u32 s92, s92, 65024\n\t" \
  "s_addc_u32 s93, s93, 0\n\t" \
  "ds_read_b128 v[160:163], v253 offset:0\n\t" \
  "ds_read_b128 v[164:167], v253 offset:1024\n\t" \
  "ds_read_b128 v[176:179], v244 offset:32768\n\t" \
  "ds_read_b128 v[180:183], v244 offset:49152\n\t" \
  "ds_read_b128 v[184:187], v245 offset:32768\n\t" \
  "ds_read_b128 v[188:191], v245 offset:49152\n\t" \
  "s_add_u32 m0, %[lw], 65536\n\t" \
  "s_nop 0\n\t" \
  "global_load_lds_dwordx4 v248, s[92:93] sc1\n\t" \
  "ds_read_b128 v[192:195], v246 offset:32768\n\t" \
  "ds_read_b128 v[196:199], v246 offset:49152\n\t" \
  "ds_read_b128 v[200:203], v247 offset:32768\n\t" \
  "ds_read_b128 v[204:207], v247 offset:49152\n\t" \
  "s_add_u32 m0, %[lw], 66560\n\t" \
  "s_nop 0\n\t" \
  "global_load_lds_dwordx4 v249, s[92:93] sc1\n\t" \
  "ds_read_b128 v[208:211], v244 offset:33024\n\t" \
  "ds_read_b128 v[212:215], v244 offset:49408\n\t" \
  "ds_read_b128 v[216:219], v245 offset:33024\n\t" \
  "ds_read_b128 v[220:223], v245 offset:49408\n\t" \
  "s_add_u32 m0, %[lw], 67584\n\t" \
  "s_nop 0\n\t" \
  "global_load_lds_dwordx4 v250, s[92:93] sc1\n\t" \
  "ds_read_b128 v[224:227], v246 offset:33024\n\t" \
  "ds_read_b128 v[228:231], v246 offset:49408\n\t" \
  "ds_read_b128 v[232:235], v247 offset:33024\n\t" \
  "ds_read_b128 v[236:239], v247 offset:49408\n\t" \
  "s_add_u32 m0, %[lw], 68608\n\t" \
  "s_nop 0\n\t" \
  "global_load_lds_dwordx4 v251, s[92:93] sc1\n\t" \
  "s_waitcnt lgkmcnt(15)\n\t" \
  "s_waitcnt lgkmcnt(12)\n\t" \
  "s_sub_i32 s91, s86, 2\n\t" \
  "v_mov_b32 v173, s91\n\t" \
  "buffer_store_dword v173, v174, %[rs], 0 offen sc1\n\t" \
  "s_lshr_b32 s90, s87, 7\n\t" \
  "s_xor_b32 s90, s90, %[s0]\n\t" \
  "s_and_b32 s90, s90, 1\n\t" \
  "s_lshl1_add_u32 s90, s87, s90\n\t" \
  "s_lshl_b32 s90, s90, 13\n\t" \
  "v_add_f32 v160, v160, v128\n\t" \
  "v_add_f32 v161, v161, v129\n\t" \
  "v_add_f32 v162, v162, v130\n\t" \
  "v_add_f32 v163, v163, v131\n\t" \
  "v_add_f32 v164, v164, v132\n\t" \
  "v_add_f32 v165, v165, v133\n\t" \
  "v_add_f32 v166, v166, v134\n\t" \
  "v_add_f32 v167, v167, v135\n\t" \
  "v_cvt_pk_bf16_f32 v168, v160, v161\n\t" \
  "v_cvt_pk_bf16_f32 v169, v162, v163\n\t" \
  "v_cvt_pk_bf16_f32 v170, v164, v165\n\t" \
  "v_cvt_pk_bf16_f32 v171, v166, v167\n\t" \
  "buffer_store_dwordx4 v[168:171], v172, %[rt], s90 offen sc1\n\t" \
  "s_add_u32 s86, s86, 1\n\t" \
  "s_add_u32 s87, s87, 1\n\t" \
  "s_waitcnt lgkmcnt(0)\n\t" \
  "s_cmp_lt_u32 s86, 64\n\t" \
  "s_cbranch_scc1 .Lp1%=\n\t" \
  "s_barrier\n\t" \
  "s_setprio 2\n\t" \
  "v_mfma_f32_16x16x32_bf16 v[144:147], v[64:67], v[176:179], v[144:147]\n\t" \
  "v_mfma_f32_16x16x32_bf16 v[148:151], v[68:71], v[176:179], v[148:151]\n\t" \
  "v_mfma_f32_16x16x32_bf16 v[152:155], v[64:67], v[180:183], v[152:155]\n\t" \
  "v_mfma_f32_16x16x32_bf16 v[156:159], v[68:71], v[180:183], v[156:159]\n\t" \
  "v_mfma_f32_16x16x32_bf16 v[144:147], v[72:75], v[184:187], v[144:147]\n\t" \
  "v_mfma_f32_16x16x32_bf16 v[148:151], v[76:79], v[184:187], v[148:151]\n\t" \
  "v_mfma_f32_16x16x32_bf16 v[152:155], v[72:75], v[188:191], v[152:155]\n\t" \
  "v_mfma_f32_16x16x32_bf16 v[156:159], v[76:79], v[188:191], v[156:159]\n\t" \
  "v_mfma_f32_16x16x32_bf16 v[144:147], v[80:83], v[192:195], v[144:147]\n\t" \
  "v_mfma_f32_16x16x32_bf16 v[148:151], v[84:87], v[192:195], v[148:151]\n\t" \
  "v_mfma_f32_16x16x32_bf16 v[152:155], v[80:83], v[196:199], v[152:155]\n\t" \
  "v_mfma_f32_16x16x32_bf16 v[156:159], v[84:87], v[196:199], v[156:159]\n\t" \
  "v_mfma_f32_16x16x32_bf16 v[144:147], v[88:91], v[200:203], v[144:147]\n\t" \
  "v_mfma_f32_16x16x32_bf16 v[148:151], v[92:95], v[200:203], v[148:151]\n\t" \
  "v_mfma_f32_16x16x32_bf16 v[152:155], v[88:91], v[204:207], v[152:155]\n\t" \
  "v_mfma_f32_16x16x32_bf16 v[156:159], v[92:95], v[204:207], v[156:159]\n\t" \
  "v_mfma_f32_16x16x32_bf16 v[144:147], v[96:99], v[208:211], v[144:147]\n\t" \
  "v_mfma_f32_16x16x32_bf16 v[148:151], v[100:103], v[208:211], v[148:151]\n\t" \
  "v_mfma_f32_16x16x32_bf16 v[152:155], v[96:99], v[212:215], v[152:155]\n\t" \
  "v_mfma_f32_16x16x32_bf16 v[156:159], v[100:103], v[212:215], v[156:159]\n\t" \
  "v_mfma_f32_16x16x32_bf16 v[144:147], v[104:107], v[216:219], v[144:147]\n\t" \
  "v_mfma_f32_16x16x32_bf16 v[148:151], v[108:111], v[216:219], v[148:151]\n\t" \
  "v_mfma_f32_16x16x32_bf16 v[152:155], v[104:107], v[220:223], v[152:155]\n\t" \
  "v_mfma_f32_16x16x32_bf16 v[156:159], v[108:111], v[220:223], v[156:159]\n\t" \
  "v_mfma_f32_16x16x32_bf16 v[144:147], v[112:115], v[224:227], v[144:147]\n\t" \
  "v_mfma_f32_16x16x32_bf16 v[148:151], v[116:119], v[224:227], v[148:151]\n\t" \
  "v_mfma_f32_16x16x32_bf16 v[152:155], v[112:115], v[228:231], v[152:155]\n\t" \
  "v_mfma_f32_16x16x32_bf16 v[156:159], v[116:119], v[228:231], v[156:159]\n\t" \
  "v_mfma_f32_16x16x32_bf16 v[144:147], v[120:123], v[232:235], v[144:147]\n\t" \
  "v_mfma_f32_16x16x32_bf16 v[148:151], v[124:127], v[232:235], v[148:151]\n\t" \
  "v_mfma_f32_16x16x32_bf16 v[152:155], v[120:123], v[236:239], v[152:155]\n\t" \
  "v_mfma_f32_16x16x32_bf16 v[156:159], v[124:127], v[236:239], v[156:159]\n\t" \
  "s_setprio 0\n\t" \
  "s_nop 5\n\t" \
  "ds_write_b128 v252, v[152:155] offset:0\n\t" \
  "ds_write_b128 v252, v[156:159] offset:1024\n\t" \
  "s_waitcnt lgkmcnt(0)\n\t" \
  "s_barrier\n\t" \
  "ds_read_b128 v[160:163], v253 offset:0\n\t" \
  "ds_read_b128 v[164:167], v253 offset:1024\n\t" \
  "s_waitcnt lgkmcnt(0)\n\t" \
  "s_lshr_b32 s90, s87, 7\n\t" \
  "s_xor_b32 s90, s90, %[s0]\n\t" \
  "s_and_b32 s90, s90, 1\n\t" \
  "s_lshl1_add_u32 s90, s87, s90\n\t" \
  "s_lshl_b32 s90, s90, 13\n\t" \
  "v_add_f32 v160, v160, v144\n\t" \
  "v_add_f32 v161, v161, v145\n\t" \
  "v_add_f32 v162, v162, v146\n\t" \
  "v_add_f32 v163, v163, v147\n\t" \
  "v_add_f32 v164, v164, v148\n\t" \
  "v_add_f32 v165, v165, v149\n\t" \
  "v_add_f32 v166, v166, v150\n\t" \
  "v_add_f32 v167, v167, v151\n\t" \
  "v_cvt_pk_bf16_f32 v168, v160, v161\n\t" \
  "v_cvt_pk_bf16_f32 v169, v162, v163\n\t" \
  "v_cvt_pk_bf16_f32 v170, v164, v165\n\t" \
  "v_cvt_pk_bf16_f32 v171, v166, v167\n\t" \
  "buffer_store_dwordx4 v[168:171], v172, %[rt], s90 offen sc1\n\t" \
  "s_waitcnt vmcnt(0)\n\t" \
  "s_barrier\n\t" \
  "s_sub_i32 s91, s86, 1\n\t" \
  "v_mov_b32 v173, s91\n\t" \
  "buffer_store_dword v173, v174, %[rs], 0 offen sc1\n\t" \
  "s_mov_b32 m0, s94\n\t" \
  ".Le%=:\n\t"
#define PP_ASM_1 \
  "s_cmp_lg_u32 %[kh], 0\n\t" \
  "s_cbranch_scc1 .Lk%=\n\t" \
  "s_mov_b32 s94, m0\n\t" \
  "s_mov_b64 s[92:93], %[x0]\n\t" \
  "s_mov_b32 s86, %[g0]\n\t" \
  "s_add_u32 s87, s86, %[t0]\n\t" \
  "s_add_u32 m0, %[lw], 67584\n\t" \
  "s_nop 0\n\t" \
  "global_load_lds_dwordx4 v250, s[92:93] sc1\n\t" \
  "s_add_u32 m0, %[lw], 68608\n\t" \
  "s_nop 0\n\t" \
  "global_load_lds_dwordx4 v251, s[92:93] sc1\n\t" \
  "s_barrier\n\t" \
  "s_branch .Ls0%=\n\t" \
  ".Lp0%=:\n\t" \
  "s_waitcnt vmcnt(9)\n\t" \
  "s_barrier\n\t" \
  ".Ls0%=:\n\t" \
  "s_add_u32 s92, s92, 512\n\t" \
  "s_addc_u32 s93, s93, 0\n\t" \
  "ds_read_b128 v[176:179], v240 offset:0\n\t" \
  "ds_read_b128 v[180:183], v240 offset:16384\n\t" \
  "ds_read_b128 v[184:187], v241 offset:0\n\t" \
  "ds_read_b128 v[188:191], v241 offset:16384\n\t" \
  "s_add_u32 m0, %[lw], 98304\n\t" \
  "s_nop 0\n\t" \
  "global_load_lds_dwordx4 v248, s[92:93] sc1\n\t" \
  "ds_read_b128 v[192:195], v242 offset:0\n\t" \
  "ds_read_b128 v[196:199], v242 offset:16384\n\t" \
  "ds_read_b128 v[200:203], v243 offset:0\n\t" \
  "ds_read_b128 v[204:207], v243 offset:16384\n\t" \
  "s_add_u32 m0, %[lw], 99328\n\t" \
  "s_nop 0\n\t" \
  "global_load_lds_dwordx4 v249, s[92:93] sc1\n\t" \
  "ds_read_b128 v[208:211], v240 offset:256\n\t" \
  "ds_read_b128 v[212:215], v240 offset:16640\n\t" \
  "ds_read_b128 v[216:219], v241 offset:256\n\t" \
  "ds_read_b128 v[220:223], v241 offset:16640\n\t" \
  "s_add_u32 m0, %[lw], 100352\n\t" \
  "s_nop 0\n\t" \
  "global_load_lds_dwordx4 v250, s[92:93] sc1\n\t" \
  "ds_read_b128 v[224:227], v242 offset:256\n\t" \
  "ds_read_b128 v[228:231], v242 offset:16640\n\t" \
  "ds_read_b128 v[232:235], v243 offset:256\n\t" \
  "ds_read_b128 v[236:239], v243 offset:16640\n\t" \
  "s_add_u32 m0, %[lw], 101376\n\t" \
  "s_nop 0\n\t" \
  "global_load_lds_dwordx4 v251, s[92:93] sc1\n\t" \
  "ds_write_b128 v252, v[152:155] offset:0\n\t" \
  "ds_write_b128 v252, v[156:159] offset:1024\n\t" \
  "s_waitcnt lgkmcnt(0)\n\t" \
  "s_setprio 1\n\t" \
  "v_mfma_f32_16x16x32_bf16 v[128:131], v[176:179], v[0:3], 0\n\t" \
  "v_mfma_f32_16x16x32_bf16 v[136:139], v[176:179], v[4:7], 0\n\t" \
  "v_mfma_f32_16x16x32_bf16 v[132:135], v[180:183], v[0:3], 0\n\t" \
  "v_mfma_f32_16x16x32_bf16 v[140:143], v[180:183], v[4:7], 0\n\t" \
  "v_mfma_f32_16x16x32_bf16 v[128:131], v[184:187], v[8:11], v[128:131]\n\t" \
  "v_mfma_f32_16x16x32_bf16 v[136:139], v[184:187], v[12:15], v[136:139]\n\t" \
  "v_mfma_f32_16x16x32_bf16 v[132:135], v[188:191], v[8:11], v[132:135]\n\t" \
  "v_mfma_f32_16x16x32_bf16 v[140:143], v[188:191], v[12:15], v[140:143]\n\t" \
  "v_mfma_f32_16x16x32_bf16 v[128:131], v[192:195], v[16:19], v[128:131]\n\t" \
  "v_mfma_f32_16x16x32_bf16 v[136:139], v[192:195], v[20:23], v[136:139]\n\t" \
  "v_mfma_f32_16x16x32_bf16 v[132:135], v[196:199], v[16:19], v[132:135]\n\t" \
  "v_mfma_f32_16x16x32_bf16 v[140:143], v[196:199], v[20:23], v[140:143]\n\t" \
  "v_mfma_f32_16x16x32_bf16 v[128:131], v[200:203], v[24:27], v[128:131]\n\t" \
  "v_mfma_f32_16x16x32_bf16 v[136:139], v[200:203], v[28:31], v[136:139]\n\t" \
  "v_mfma_f32_16x16x32_bf16 v[132:135], v[204:207], v[24:27], v[132:135]\n\t" \
  "v_mfma_f32_16x16x32_bf16 v[140:143], v[204:207], v[28:31], v[140:143]\n\t" \
  "v_mfma_f32_16x16x32_bf16 v[128:131], v[208:211], v[32:35], v[128:131]\n\t" \
  "v_mfma_f32_16x16x32_bf16 v[136:139], v[208:211], v[36:39], v[136:139]\n\t" \
  "v_mfma_f32_16x16x32_bf16 v[132:135], v[212:215], v[32:35], v[132:135]\n\t" \
  "v_mfma_f32_16x16x32_bf16 v[140:143], v[212:215], v[36:39], v[140:143]\n\t" \
  "v_mfma_f32_16x16x32_bf16 v[128:131], v[216:219], v[40:43], v[128:131]\n\t" \
  "v_mfma_f32_16x16x32_bf16 v[136:139], v[216:219], v[44:47], v[136:139]\n\t" \
  "v_mfma_f32_16x16x32_bf16 v[132:135], v[220:223], v[40:43], v[132:135]\n\t" \
  "v_mfma_f32_16x16x32_bf16 v[140:143], v[220:223], v[44:47], v[140:143]\n\t" \
  "v_mfma_f32_16x16x32_bf16 v[128:131], v[224:227], v[48:51], v[128:131]\n\t" \
  "v_mfma_f32_16x16x32_bf16 v[136:139], v[224:227], v[52:55], v[136:139]\n\t" \
  "v_mfma_f32_16x16x32_bf16 v[132:135], v[228:231], v[48:51], v[132:135]\n\t" \
  "v_mfma_f32_16x16x32_bf16 v[140:143], v[228:231], v[52:55], v[140:143]\n\t" \
  "v_mfma_f32_16x16x32_bf16 v[128:131], v[232:235], v[56:59], v[128:131]\n\t" \
  "v_mfma_f32_16x16x32_bf16 v[136:139], v[232:235], v[60:63], v[136:139]\n\t" \
  "v_mfma_f32_16x16x32_bf16 v[132:135], v[236:239], v[56:59], v[132:135]\n\t" \
  "v_mfma_f32_16x16x32_bf16 v[140:143], v[236:239], v[60:63], v[140:143]\n\t" \
  "s_setprio 0\n\t" \
  "s_waitcnt vmcnt(4)\n\t" \
  "s_barrier\n\t" \
  "s_add_u32 s92, s92, 65024\n\t" \
  "s_addc_u32 s93, s93, 0\n\t" \
  "ds_read_b128 v[160:163], v253 offset:0\n\t" \
  "ds_read_b128 v[164:167], v253 offset:1024\n\t" \
  "ds_read_b128 v[176:179], v240 offset:32768\n\t" \
  "ds_read_b128 v[180:183], v240 offset:49152\n\t" \
  "ds_read_b128 v[184:187], v241 offset:32768\n\t" \
  "ds_read_b128 v[188:191], v241 offset:49152\n\t" \
  "s_add_u32 m0, %[lw], 0\n\t" \
  "s_nop 0\n\t" \
  "global_load_lds_dwordx4 v248, s[92:93] sc1\n\t" \
  "ds_read_b128 v[192:195], v242 offset:32768\n\t" \
  "ds_read_b128 v[196:199], v242 offset:49152\n\t" \
  "ds_read_b128 v[200:203], v243 offset:32768\n\t" \
  "ds_read_b128 v[204:207], v243 offset:49152\n\t" \
  "s_add_u32 m0, %[lw], 1024\n\t" \
  "s_nop 0\n\t" \
  "global_load_lds_dwordx4 v249, s[92:93] sc1\n\t" \
  "ds_read_b128 v[208:211], v240 offset:33024\n\t" \
  "ds_read_b128 v[212:215], v240 offset:49408\n\t" \
  "ds_read_b128 v[216:219], v241 offset:33024\n\t" \
  "ds_read_b128 v[220:223], v241 offset:49408\n\t" \
  "s_add_u32 m0, %[lw], 2048\n\t" \
  "s_nop 0\n\t" \
  "global_load_lds_dwordx4 v250, s[92:93] sc1\n\t" \
  "ds_read_b128 v[224:227], v242 offset:33024\n\t" \
  "ds_read_b128 v[228:231], v242 offset:49408\n\t" \
  "ds_read_b128 v[232:235], v243 offset:33024\n\t" \
  "ds_read_b128 v[236:239], v243 offset:49408\n\t" \
  "s_add_u32 m0, %[lw], 3072\n\t" \
  "s_nop 0\n\t" \
  "global_load_lds_dwordx4 v251, s[92:93] sc1\n\t" \
  "s_waitcnt lgkmcnt(15)\n\t" \
  "s_waitcnt lgkmcnt(12)\n\t" \
  "s_lshr_b32 s90, s87, 7\n\t" \
  "s_xor_b32 s90, s90, %[s0]\n\t" \
  "s_and_b32 s90, s90, 1\n\t" \
  "s_lshl1_add_u32 s90, s87, s90\n\t" \
  "s_lshl_b32 s90, s90, 13\n\t" \
  "v_add_f32 v160, v160, v144\n\t" \
  "v_add_f32 v161, v161, v145\n\t" \
  "v_add_f32 v162, v162, v146\n\t" \
  "v_add_f32 v163, v163, v147\n\t" \
  "v_add_f32 v164, v164, v148\n\t" \
  "v_add_f32 v165, v165, v149\n\t" \
  "v_add_f32 v166, v166, v150\n\t" \
  "v_add_f32 v167, v167, v151\n\t" \
  "v_cvt_pk_bf16_f32 v168, v160, v161\n\t" \
  "v_cvt_pk_bf16_f32 v169, v162, v163\n\t" \
  "v_cvt_pk_bf16_f32 v170, v164, v165\n\t" \
  "v_cvt_pk_bf16_f32 v171, v166, v167\n\t" \
  "buffer_store_dwordx4 v[168:171], v172, %[rt], s90 offen sc1\n\t" \
  "s_add_u32 s86, s86, 1\n\t" \
  "s_add_u32 s87, s87, 1\n\t" \
  "s_waitcnt lgkmcnt(0)\n\t" \
  "s_setprio 1\n\t" \
  "v_mfma_f32_16x16x32_bf16 v[128:131], v[176:179], v[64:67], v[128:131]\n\t" \
  "v_mfma_f32_16x16x32_bf16 v[136:139], v[176:179], v[68:71], v[136:139]\n\t" \
  "v_mfma_f32_16x16x32_bf16 v[132:135], v[180:183], v[64:67], v[132:135]\n\t" \
  "v_mfma_f32_16x16x32_bf16 v[140:143], v[180:183], v[68:71], v[140:143]\n\t" \
  "v_mfma_f32_16x16x32_bf16 v[128:131], v[184:187], v[72:75], v[128:131]\n\t" \
  "v_mfma_f32_16x16x32_bf16 v[136:139], v[184:187], v[76:79], v[136:139]\n\t" \
  "v_mfma_f32_16x16x32_bf16 v[132:135], v[188:191], v[72:75], v[132:135]\n\t" \
  "v_mfma_f32_16x16x32_bf16 v[140:143], v[188:191], v[76:79], v[140:143]\n\t" \
  "v_mfma_f32_16x16x32_bf16 v[128:131], v[192:195], v[80:83], v[128:131]\n\t" \
  "v_mfma_f32_16x16x32_bf16 v[136:139], v[192:195], v[84:87], v[136:139]\n\t" \
  "v_mfma_f32_16x16x32_bf16 v[132:135], v[196:199], v[80:83], v[132:135]\n\t" \
  "v_mfma_f32_16x16x32_bf16 v[140:143], v[196:199], v[84:87], v[140:143]\n\t" \
  "v_mfma_f32_16x16x32_bf16 v[128:131], v[200:203], v[88:91], v[128:131]\n\t" \
  "v_mfma_f32_16x16x32_bf16 v[136:139], v[200:203], v[92:95], v[136:139]\n\t" \
  "v_mfma_f32_16x16x32_bf16 v[132:135], v[204:207], v[88:91], v[132:135]\n\t" \
  "v_mfma_f32_16x16x32_bf16 v[140:143], v[204:207], v[92:95], v[140:143]\n\t" \
  "v_mfma_f32_16x16x32_bf16 v[128:131], v[208:211], v[96:99], v[128:131]\n\t" \
  "v_mfma_f32_16x16x32_bf16 v[136:139], v[208:211], v[100:103], v[136:139]\n\t" \
  "v_mfma_f32_16x16x32_bf16 v[132:135], v[212:215], v[96:99], v[132:135]\n\t" \
  "v_mfma_f32_16x16x32_bf16 v[140:143], v[212:215], v[100:103], v[140:143]\n\t" \
  "v_mfma_f32_16x16x32_bf16 v[128:131], v[216:219], v[104:107], v[128:131]\n\t" \
  "v_mfma_f32_16x16x32_bf16 v[136:139], v[216:219], v[108:111], v[136:139]\n\t" \
  "v_mfma_f32_16x16x32_bf16 v[132:135], v[220:223], v[104:107], v[132:135]\n\t" \
  "v_mfma_f32_16x16x32_bf16 v[140:143], v[220:223], v[108:111], v[140:143]\n\t" \
  "v_mfma_f32_16x16x32_bf16 v[128:131], v[224:227], v[112:115], v[128:131]\n\t" \
  "v_mfma_f32_16x16x32_bf16 v[136:139], v[224:227], v[116:119], v[136:139]\n\t" \
  "v_mfma_f32_16x16x32_bf16 v[132:135], v[228:231], v[112:115], v[132:135]\n\t" \
  "v_mfma_f32_16x16x32_bf16 v[140:143], v[228:231], v[116:119], v[140:143]\n\t" \
  "v_mfma_f32_16x16x32_bf16 v[128:131], v[232:235], v[120:123], v[128:131]\n\t" \
  "v_mfma_f32_16x16x32_bf16 v[136:139], v[232:235], v[124:127], v[136:139]\n\t" \
  "v_mfma_f32_16x16x32_bf16 v[132:135], v[236:239], v[120:123], v[132:135]\n\t" \
  "v_mfma_f32_16x16x32_bf16 v[140:143], v[236:239], v[124:127], v[140:143]\n\t" \
  "s_setprio 0\n\t" \
  "s_waitcnt vmcnt(9)\n\t" \
  "s_barrier\n\t" \
  "s_add_u32 s92, s92, 512\n\t" \
  "s_addc_u32 s93, s93, 0\n\t" \
  "ds_read_b128 v[176:179], v244 offset:0\n\t" \
  "ds_read_b128 v[180:183], v244 offset:16384\n\t" \
  "ds_read_b128 v[184:187], v245 offset:0\n\t" \
  "ds_read_b128 v[188:191], v245 offset:16384\n\t" \
  "s_add_u32 m0, %[lw], 32768\n\t" \
  "s_nop 0\n\t" \
  "global_load_lds_dwordx4 v248, s[92:93] sc1\n\t" \
  "ds_read_b128 v[192:195], v246 offset:0\n\t" \
  "ds_read_b128 v[196:199], v246 offset:16384\n\t" \
  "ds_read_b128 v[200:203], v247 offset:0\n\t" \
  "ds_read_b128 v[204:207], v247 offset:16384\n\t" \
  "s_add_u32 m0, %[lw], 33792\n\t" \
  "s_nop 0\n\t" \
  "global_load_lds_dwordx4 v249, s[92:93] sc1\n\t" \
  "ds_read_b128 v[208:211], v244 offset:256\n\t" \
  "ds_read_b128 v[212:215], v244 offset:16640\n\t" \
  "ds_read_b128 v[216:219], v245 offset:256\n\t" \
  "ds_read_b128 v[220:223], v245 offset:16640\n\t" \
  "s_add_u32 m0, %[lw], 34816\n\t" \
  "s_nop 0\n\t" \
  "global_load_lds_dwordx4 v250, s[92:93] sc1\n\t" \
  "ds_read_b128 v[224:227], v246 offset:256\n\t" \
  "ds_read_b128 v[228:231], v246 offset:16640\n\t" \
  "ds_read_b128 v[232:235], v247 offset:256\n\t" \
  "ds_read_b128 v[236:239], v247 offset:16640\n\t" \
  "s_add_u32 m0, %[lw], 35840\n\t" \
  "s_nop 0\n\t" \
  "global_load_lds_dwordx4 v251, s[92:93] sc1\n\t" \
  "ds_write_b128 v252, v[136:139] offset:0\n\t" \
  "ds_write_b128 v252, v[140:143] offset:1024\n\t" \
  "s_waitcnt lgkmcnt(0)\n\t" \
  "s_setprio 1\n\t" \
  "v_mfma_f32_16x16x32_bf16 v[144:147], v[176:179], v[0:3], 0\n\t" \
  "v_mfma_f32_16x16x32_bf16 v[152:155], v[176:179], v[4:7], 0\n\t" \
  "v_mfma_f32_16x16x32_bf16 v[148:151], v[180:183], v[0:3], 0\n\t" \
  "v_mfma_f32_16x16x32_bf16 v[156:159], v[180:183], v[4:7], 0\n\t" \
  "v_mfma_f32_16x16x32_bf16 v[144:147], v[184:187], v[8:11], v[144:147]\n\t" \
  "v_mfma_f32_16x16x32_bf16 v[152:155], v[184:187], v[12:15], v[152:155]\n\t" \
  "v_mfma_f32_16x16x32_bf16 v[148:151], v[188:191], v[8:11], v[148:151]\n\t" \
  "v_mfma_f32_16x16x32_bf16 v[156:159], v[188:191], v[12:15], v[156:159]\n\t" \
  "v_mfma_f32_16x16x32_bf16 v[144:147], v[192:195], v[16:19], v[144:147]\n\t" \
  "v_mfma_f32_16x16x32_bf16 v[152:155], v[192:195], v[20:23], v[152:155]\n\t" \
  "v_mfma_f32_16x16x32_bf16 v[148:151], v[196:199], v[16:19], v[148:151]\n\t" \
  "v_mfma_f32_16x16x32_bf16 v[156:159], v[196:199], v[20:23], v[156:159]\n\t" \
  "v_mfma_f32_16x16x32_bf16 v[144:147], v[200:203], v[24:27], v[144:147]\n\t" \
  "v_mfma_f32_16x16x32_bf16 v[152:155], v[200:203], v[28:31], v[152:155]\n\t" \
  "v_mfma_f32_16x16x32_bf16 v[148:151], v[204:207], v[24:27], v[148:151]\n\t" \
  "v_mfma_f32_16x16x32_bf16 v[156:159], v[204:207], v[28:31], v[156:159]\n\t" \
  "v_mfma_f32_16x16x32_bf16 v[144:147], v[208:211], v[32:35], v[144:147]\n\t" \
  "v_mfma_f32_16x16x32_bf16 v[152:155], v[208:211], v[36:39], v[152:155]\n\t" \
  "v_mfma_f32_16x16x32_bf16 v[148:151], v[212:215], v[32:35], v[148:151]\n\t" \
  "v_mfma_f32_16x16x32_bf16 v[156:159], v[212:215], v[36:39], v[156:159]\n\t" \
  "v_mfma_f32_16x16x32_bf16 v[144:147], v[216:219], v[40:43], v[144:147]\n\t" \
  "v_mfma_f32_16x16x32_bf16 v[152:155], v[216:219], v[44:47], v[152:155]\n\t" \
  "v_mfma_f32_16x16x32_bf16 v[148:151], v[220:223], v[40:43], v[148:151]\n\t" \
  "v_mfma_f32_16x16x32_bf16 v[156:159], v[220:223], v[44:47], v[156:159]\n\t" \
  "v_mfma_f32_16x16x32_bf16 v[144:147], v[224:227], v[48:51], v[144:147]\n\t" \
  "v_mfma_f32_16x16x32_bf16 v[152:155], v[224:227], v[52:55], v[152:155]\n\t" \
  "v_mfma_f32_16x16x32_bf16 v[148:151], v[228:231], v[48:51], v[148:151]\n\t" \
  "v_mfma_f32_16x16x32_bf16 v[156:159], v[228:231], v[52:55], v[156:159]\n\t" \
  "v_mfma_f32_16x16x32_bf16 v[144:147], v[232:235], v[56:59], v[144:147]\n\t" \
  "v_mfma_f32_16x16x32_bf16 v[152:155], v[232:235], v[60:63], v[152:155]\n\t" \
  "v_mfma_f32_16x16x32_bf16 v[148:151], v[236:239], v[56:59], v[148:151]\n\t" \
  "v_mfma_f32_16x16x32_bf16 v[156:159], v[236:239], v[60:63], v[156:159]\n\t" \
  "s_setprio 0\n\t" \
  "s_waitcnt vmcnt(4)\n\t" \
  "s_barrier\n\t" \
  "s_add_u32 s92, s92, 65024\n\t" \
  "s_addc_u32 s93, s93, 0\n\t" \
  "ds_read_b128 v[160:163], v253 offset:0\n\t" \
  "ds_read_b128 v[164:167], v253 offset:1024\n\t" \
  "ds_read_b128 v[176:179], v244 offset:32768\n\t" \
  "ds_read_b128 v[180:183], v244 offset:49152\n\t" \
  "ds_read_b128 v[184:187], v245 offset:32768\n\t" \
  "ds_read_b128 v[188:191], v245 offset:49152\n\t" \
  "s_add_u32 m0, %[lw], 65536\n\t" \
  "s_nop 0\n\t" \
  "global_load_lds_dwordx4 v248, s[92:93] sc1\n\t" \
  "ds_read_b128 v[192:195], v246 offset:32768\n\t" \
  "ds_read_b128 v[196:199], v246 offset:49152\n\t" \
  "ds_read_b128 v[200:203], v247 offset:32768\n\t" \
  "ds_read_b128 v[204:207], v247 offset:49152\n\t" \
  "s_add_u32 m0, %[lw], 66560\n\t" \
  "s_nop 0\n\t" \
  "global_load_lds_dwordx4 v249, s[92:93] sc1\n\t" \
  "ds_read_b128 v[208:211], v244 offset:33024\n\t" \
  "ds_read_b128 v[212:215], v244 offset:49408\n\t" \
  "ds_read_b128 v[216:219], v245 offset:33024\n\t" \
  "ds_read_b128 v[220:223], v245 offset:49408\n\t" \
  "s_add_u32 m0, %[lw], 67584\n\t" \
  "s_nop 0\n\t" \
  "global_load_lds_dwordx4 v250, s[92:93] sc1\n\t" \
  "ds_read_b128 v[224:227], v246 offset:33024\n\t" \
  "ds_read_b128 v[228:231], v246 offset:49408\n\t" \
  "ds_read_b128 v[232:235], v247 offset:33024\n\t" \
  "ds_read_b128 v[236:239], v247 offset:49408\n\t" \
  "s_add_u32 m0, %[lw], 68608\n\t" \
  "s_nop 0\n\t" \
  "global_load_lds_dwordx4 v251, s[92:93] sc1\n\t" \
  "s_waitcnt lgkmcnt(15)\n\t" \
  "s_waitcnt lgkmcnt(12)\n\t" \
  "s_lshr_b32 s90, s87, 7\n\t" \
  "s_xor_b32 s90, s90, %[s0]\n\t" \
  "s_and_b32 s90, s90, 1\n\t" \
  "s_lshl1_add_u32 s90, s87, s90\n\t" \
  "s_lshl_b32 s90, s90, 13\n\t" \
  "v_add_f32 v160, v160, v128\n\t" \
  "v_add_f32 v161, v161, v129\n\t" \
  "v_add_f32 v162, v162, v130\n\t" \
  "v_add_f32 v163, v163, v131\n\t" \
  "v_add_f32 v164, v164, v132\n\t" \
  "v_add_f32 v165, v165, v133\n\t" \
  "v_add_f32 v166, v166, v134\n\t" \
  "v_add_f32 v167, v167, v135\n\t" \
  "v_cvt_pk_bf16_f32 v168, v160, v161\n\t" \
  "v_cvt_pk_bf16_f32 v169, v162, v163\n\t" \
  "v_cvt_pk_bf16_f32 v170, v164, v165\n\t" \
  "v_cvt_pk_bf16_f32 v171, v166, v167\n\t" \
  "buffer_store_dwordx4 v[168:171], v172, %[rt], s90 offen sc1\n\t" \
  "s_add_u32 s86, s86, 1\n\t" \
  "s_add_u32 s87, s87, 1\n\t" \
  "s_waitcnt lgkmcnt(0)\n\t" \
  "s_setprio 1\n\t" \
  "v_mfma_f32_16x16x32_bf16 v[144:147], v[176:179], v[64:67], v[144:147]\n\t" \
  "v_mfma_f32_16x16x32_bf16 v[152:155], v[176:179], v[68:71], v[152:155]\n\t" \
  "v_mfma_f32_16x16x32_bf16 v[148:151], v[180:183], v[64:67], v[148:151]\n\t" \
  "v_mfma_f32_16x16x32_bf16 v[156:159], v[180:183], v[68:71], v[156:159]\n\t" \
  "v_mfma_f32_16x16x32_bf16 v[144:147], v[184:187], v[72:75], v[144:147]\n\t" \
  "v_mfma_f32_16x16x32_bf16 v[152:155], v[184:187], v[76:79], v[152:155]\n\t" \
  "v_mfma_f32_16x16x32_bf16 v[148:151], v[188:191], v[72:75], v[148:151]\n\t" \
  "v_mfma_f32_16x16x32_bf16 v[156:159], v[188:191], v[76:79], v[156:159]\n\t" \
  "v_mfma_f32_16x16x32_bf16 v[144:147], v[192:195], v[80:83], v[144:147]\n\t" \
  "v_mfma_f32_16x16x32_bf16 v[152:155], v[192:195], v[84:87], v[152:155]\n\t" \
  "v_mfma_f32_16x16x32_bf16 v[148:151], v[196:199], v[80:83], v[148:151]\n\t" \
  "v_mfma_f32_16x16x32_bf16 v[156:159], v[196:199], v[84:87], v[156:159]\n\t" \
  "v_mfma_f32_16x16x32_bf16 v[144:147], v[200:203], v[88:91], v[144:147]\n\t" \
  "v_mfma_f32_16x16x32_bf16 v[152:155], v[200:203], v[92:95], v[152:155]\n\t" \
  "v_mfma_f32_16x16x32_bf16 v[148:151], v[204:207], v[88:91], v[148:151]\n\t" \
  "v_mfma_f32_16x16x32_bf16 v[156:159], v[204:207], v[92:95], v[156:159]\n\t" \
  "v_mfma_f32_16x16x32_bf16 v[144:147], v[208:211], v[96:99], v[144:147]\n\t" \
  "v_mfma_f32_16x16x32_bf16 v[152:155], v[208:211], v[100:103], v[152:155]\n\t" \
  "v_mfma_f32_16x16x32_bf16 v[148:151], v[212:215], v[96:99], v[148:151]\n\t" \
  "v_mfma_f32_16x16x32_bf16 v[156:159], v[212:215], v[100:103], v[156:159]\n\t" \
  "v_mfma_f32_16x16x32_bf16 v[144:147], v[216:219], v[104:107], v[144:147]\n\t" \
  "v_mfma_f32_16x16x32_bf16 v[152:155], v[216:219], v[108:111], v[152:155]\n\t" \
  "v_mfma_f32_16x16x32_bf16 v[148:151], v[220:223], v[104:107], v[148:151]\n\t" \
  "v_mfma_f32_16x16x32_bf16 v[156:159], v[220:223], v[108:111], v[156:159]\n\t" \
  "v_mfma_f32_16x16x32_bf16 v[144:147], v[224:227], v[112:115], v[144:147]\n\t" \
  "v_mfma_f32_16x16x32_bf16 v[152:155], v[224:227], v[116:119], v[152:155]\n\t" \
  "v_mfma_f32_16x16x32_bf16 v[148:151], v[228:231], v[112:115], v[148:151]\n\t" \
  "v_mfma_f32_16x16x32_bf16 v[156:159], v[228:231], v[116:119], v[156:159]\n\t" \
  "v_mfma_f32_16x16x32_bf16 v[144:147], v[232:235], v[120:123], v[144:147]\n\t" \
  "v_mfma_f32_16x16x32_bf16 v[152:155], v[232:235], v[124:127], v[152:155]\n\t" \
  "v_mfma_f32_16x16x32_bf16 v[148:151], v[236:239], v[120:123], v[148:151]\n\t" \
  "v_mfma_f32_16x16x32_bf16 v[156:159], v[236:239], v[124:127], v[156:159]\n\t" \
  "s_setprio 0\n\t" \
  "s_cmp_lt_u32 s86, 64\n\t" \
  "s_cbranch_scc1 .Lp0%=\n\t" \
  "s_barrier\n\t" \
  "s_nop 1\n\t" \
  "ds_write_b128 v252, v[152:155] offset:0\n\t" \
  "s_nop 0\n\t" \
  "ds_write_b128 v252, v[156:159] offset:1024\n\t" \
  "s_waitcnt lgkmcnt(0)\n\t" \
  "s_barrier\n\t" \
  "ds_read_b128 v[160:163], v253 offset:0\n\t" \
  "ds_read_b128 v[164:167], v253 offset:1024\n\t" \
  "s_waitcnt lgkmcnt(0)\n\t" \
  "s_lshr_b32 s90, s87, 7\n\t" \
  "s_xor_b32 s90, s90, %[s0]\n\t" \
  "s_and_b32 s90, s90, 1\n\t" \
  "s_lshl1_add_u32 s90, s87, s90\n\t" \
  "s_lshl_b32 s90, s90, 13\n\t" \
  "v_add_f32 v160, v160, v144\n\t" \
  "v_add_f32 v161, v161, v145\n\t" \
  "v_add_f32 v162, v162, v146\n\t" \
  "v_add_f32 v163, v163, v147\n\t" \
  "v_add_f32 v164, v164, v148\n\t" \
  "v_add_f32 v165, v165, v149\n\t" \
  "v_add_f32 v166, v166, v150\n\t" \
  "v_add_f32 v167, v167, v151\n\t" \
  "v_cvt_pk_bf16_f32 v168, v160, v161\n\t" \
  "v_cvt_pk_bf16_f32 v169, v162, v163\n\t" \
  "v_cvt_pk_bf16_f32 v170, v164, v165\n\t" \
  "v_cvt_pk_bf16_f32 v171, v166, v167\n\t" \
  "buffer_store_dwordx4 v[168:171], v172, %[rt], s90 offen sc1\n\t" \
  "s_waitcnt vmcnt(0)\n\t" \
  "s_barrier\n\t" \
  "s_mov_b32 m0, s94\n\t" \
  "s_branch .Le%=\n\t" \
  ".Lk%=:\n\t" \
  "s_mov_b32 s94, m0\n\t" \
  "s_mov_b64 s[92:93], %[x0]\n\t" \
  "s_mov_b32 s86, %[g0]\n\t" \
  "s_add_u32 s87, s86, %[t0]\n\t" \
  "s_add_u32 m0, %[lw], 67584\n\t" \
  "s_nop 0\n\t" \
  "global_load_lds_dwordx4 v250, s[92:93] sc1\n\t" \
  "s_add_u32 m0, %[lw], 68608\n\t" \
  "s_nop 0\n\t" \
  "global_load_lds_dwordx4 v251, s[92:93] sc1\n\t" \
  "s_barrier\n\t" \
  "s_branch .Ls1%=\n\t" \
  ".Lp1%=:\n\t" \
  "s_waitcnt vmcnt(10)\n\t" \
  "s_barrier\n\t" \
  "s_setprio 2\n\t" \
  "v_mfma_f32_16x16x32_bf16 v[144:147], v[176:179], v[64:67], v[144:147]\n\t" \
  "v_mfma_f32_16x16x32_bf16 v[152:155], v[176:179], v[68:71], v[152:155]\n\t" \
  "v_mfma_f32_16x16x32_bf16 v[148:151], v[180:183], v[64:67], v[148:151]\n\t" \
  "v_mfma_f32_16x16x32_bf16 v[156:159], v[180:183], v[68:71], v[156:159]\n\t" \
  "v_mfma_f32_16x16x32_bf16 v[144:147], v[184:187], v[72:75], v[144:147]\n\t" \
  "v_mfma_f32_16x16x32_bf16 v[152:155], v[184:187], v[76:79], v[152:155]\n\t" \
  "v_mfma_f32_16x16x32_bf16 v[148:151], v[188:191], v[72:75], v[148:151]\n\t" \
  "v_mfma_f32_16x16x32_bf16 v[156:159], v[188:191], v[76:79], v[156:159]\n\t" \
  "v_mfma_f32_16x16x32_bf16 v[144:147], v[192:195], v[80:83], v[144:147]\n\t" \
  "v_mfma_f32_16x16x32_bf16 v[152:155], v[192:195], v[84:87], v[152:155]\n\t" \
  "v_mfma_f32_16x16x32_bf16 v[148:151], v[196:199], v[80:83], v[148:151]\n\t" \
  "v_mfma_f32_16x16x32_bf16 v[156:159], v[196:199], v[84:87], v[156:159]\n\t" \
  "v_mfma_f32_16x16x32_bf16 v[144:147], v[200:203], v[88:91], v[144:147]\n\t" \
  "v_mfma_f32_16x16x32_bf16 v[152:155], v[200:203], v[92:95], v[152:155]\n\t" \
  "v_mfma_f32_16x16x32_bf16 v[148:151], v[204:207], v[88:91], v[148:151]\n\t" \
  "v_mfma_f32_16x16x32_bf16 v[156:159], v[204:207], v[92:95], v[156:159]\n\t" \
  "v_mfma_f32_16x16x32_bf16 v[144:147], v[208:211], v[96:99], v[144:147]\n\t" \
  "v_mfma_f32_16x16x32_bf16 v[152:155], v[208:211], v[100:103], v[152:155]\n\t" \
  "v_mfma_f32_16x16x32_bf16 v[148:151], v[212:215], v[96:99], v[148:151]\n\t" \
  "v_mfma_f32_16x16x32_bf16 v[156:159], v[212:215], v[100:103], v[156:159]\n\t" \
  "v_mfma_f32_16x16x32_bf16 v[144:147], v[216:219], v[104:107], v[144:147]\n\t" \
  "v_mfma_f32_16x16x32_bf16 v[152:155], v[216:219], v[108:111], v[152:155]\n\t" \
  "v_mfma_f32_16x16x32_bf16 v[148:151], v[220:223], v[104:107], v[148:151]\n\t" \
  "v_mfma_f32_16x16x32_bf16 v[156:159], v[220:223], v[108:111], v[156:159]\n\t" \
  "v_mfma_f32_16x16x32_bf16 v[144:147], v[224:227], v[112:115], v[144:147]\n\t" \
  "v_mfma_f32_16x16x32_bf16 v[152:155], v[224:227], v[116:119], v[152:155]\n\t" \
  "v_mfma_f32_16x16x32_bf16 v[148:151], v[228:231], v[112:115], v[148:151]\n\t" \
  "v_mfma_f32_16x16x32_bf16 v[156:159], v[228:231], v[116:119], v[156:159]\n\t" \
  "v_mfma_f32_16x16x32_bf16 v[144:147], v[232:235], v[120:123], v[144:147]\n\t" \
  "v_mfma_f32_16x16x32_bf16 v[152:155], v[232:235], v[124:127], v[152:155]\n\t" \
  "v_mfma_f32_16x16x32_bf16 v[148:151], v[236:239], v[120:123], v[148:151]\n\t" \
  "v_mfma_f32_16x16x32_bf16 v[156:159], v[236:239], v[124:127], v[156:159]\n\t" \
  "s_setprio 0\n\t" \
  ".Ls1%=:\n\t" \
  "s_add_u32 s92, s92, 512\n\t" \
  "s_addc_u32 s93, s93, 0\n\t" \
  "ds_read_b128 v[176:179], v240 offset:0\n\t" \
  "ds_read_b128 v[180:183], v240 offset:16384\n\t" \
  "ds_read_b128 v[184:187], v241 offset:0\n\t" \
  "ds_read_b128 v[188:191], v241 offset:16384\n\t" \
  "s_add_u32 m0, %[lw], 98304\n\t" \
  "s_nop 0\n\t" \
  "global_load_lds_dwordx4 v248, s[92:93] sc1\n\t" \
  "ds_read_b128 v[192:195], v242 offset:0\n\t" \
  "ds_read_b128 v[196:199], v242 offset:16384\n\t" \
  "ds_read_b128 v[200:203], v243 offset:0\n\t" \
  "ds_read_b128 v[204:207], v243 offset:16384\n\t" \
  "s_add_u32 m0, %[lw], 99328\n\t" \
  "s_nop 0\n\t" \
  "global_load_lds_dwordx4 v249, s[92:93] sc1\n\t" \
  "ds_read_b128 v[208:211], v240 offset:256\n\t" \
  "ds_read_b128 v[212:215], v240 offset:16640\n\t" \
  "ds_read_b128 v[216:219], v241 offset:256\n\t" \
  "ds_read_b128 v[220:223], v241 offset:16640\n\t" \
  "s_add_u32 m0, %[lw], 100352\n\t" \
  "s_nop 0\n\t" \
  "global_load_lds_dwordx4 v250, s[92:93] sc1\n\t" \
  "ds_read_b128 v[224:227], v242 offset:256\n\t" \
  "ds_read_b128 v[228:231], v242 offset:16640\n\t" \
  "ds_read_b128 v[232:235], v243 offset:256\n\t" \
  "ds_read_b128 v[236:239], v243 offset:16640\n\t" \
  "s_add_u32 m0, %[lw], 101376\n\t" \
  "s_nop 0\n\t" \
  "global_load_lds_dwordx4 v251, s[92:93] sc1\n\t" \
  "ds_write_b128 v252, v[152:155] offset:0\n\t" \
  "ds_write_b128 v252, v[156:159] offset:1024\n\t" \
  "s_waitcnt lgkmcnt(0)\n\t" \
  "s_waitcnt vmcnt(4)\n\t" \
  "s_barrier\n\t" \
  "s_setprio 2\n\t" \
  "v_mfma_f32_16x16x32_bf16 v[128:131], v[176:179], v[0:3], 0\n\t" \
  "v_mfma_f32_16x16x32_bf16 v[136:139], v[176:179], v[4:7], 0\n\t" \
  "v_mfma_f32_16x16x32_bf16 v[132:135], v[180:183], v[0:3], 0\n\t" \
  "v_mfma_f32_16x16x32_bf16 v[140:143], v[180:183], v[4:7], 0\n\t" \
  "v_mfma_f32_16x16x32_bf16 v[128:131], v[184:187], v[8:11], v[128:131]\n\t" \
  "v_mfma_f32_16x16x32_bf16 v[136:139], v[184:187], v[12:15], v[136:139]\n\t" \
  "v_mfma_f32_16x16x32_bf16 v[132:135], v[188:191], v[8:11], v[132:135]\n\t" \
  "v_mfma_f32_16x16x32_bf16 v[140:143], v[188:191], v[12:15], v[140:143]\n\t" \
  "v_mfma_f32_16x16x32_bf16 v[128:131], v[192:195], v[16:19], v[128:131]\n\t" \
  "v_mfma_f32_16x16x32_bf16 v[136:139], v[192:195], v[20:23], v[136:139]\n\t" \
  "v_mfma_f32_16x16x32_bf16 v[132:135], v[196:199], v[16:19], v[132:135]\n\t" \
  "v_mfma_f32_16x16x32_bf16 v[140:143], v[196:199], v[20:23], v[140:143]\n\t" \
  "v_mfma_f32_16x16x32_bf16 v[128:131], v[200:203], v[24:27], v[128:131]\n\t" \
  "v_mfma_f32_16x16x32_bf16 v[136:139], v[200:203], v[28:31], v[136:139]\n\t" \
  "v_mfma_f32_16x16x32_bf16 v[132:135], v[204:207], v[24:27], v[132:135]\n\t" \
  "v_mfma_f32_16x16x32_bf16 v[140:143], v[204:207], v[28:31], v[140:143]\n\t" \
  "v_mfma_f32_16x16x32_bf16 v[128:131], v[208:211], v[32:35], v[128:131]\n\t" \
  "v_mfma_f32_16x16x32_bf16 v[136:139], v[208:211], v[36:39], v[136:139]\n\t" \
  "v_mfma_f32_16x16x32_bf16 v[132:135], v[212:215], v[32:35], v[132:135]\n\t" \
  "v_mfma_f32_16x16x32_bf16 v[140:143], v[212:215], v[36:39], v[140:143]\n\t" \
  "v_mfma_f32_16x16x32_bf16 v[128:131], v[216:219], v[40:43], v[128:131]\n\t" \
  "v_mfma_f32_16x16x32_bf16 v[136:139], v[216:219], v[44:47], v[136:139]\n\t" \
  "v_mfma_f32_16x16x32_bf16 v[132:135], v[220:223], v[40:43], v[132:135]\n\t" \
  "v_mfma_f32_16x16x32_bf16 v[140:143], v[220:223], v[44:47], v[140:143]\n\t" \
  "v_mfma_f32_16x16x32_bf16 v[128:131], v[224:227], v[48:51], v[128:131]\n\t" \
  "v_mfma_f32_16x16x32_bf16 v[136:139], v[224:227], v[52:55], v[136:139]\n\t" \
  "v_mfma_f32_16x16x32_bf16 v[132:135], v[228:231], v[48:51], v[132:135]\n\t" \
  "v_mfma_f32_16x16x32_bf16 v[140:143], v[228:231], v[52:55], v[140:143]\n\t" \
  "v_mfma_f32_16x16x32_bf16 v[128:131], v[232:235], v[56:59], v[128:131]\n\t" \
  "v_mfma_f32_16x16x32_bf16 v[136:139], v[232:235], v[60:63], v[136:139]\n\t" \
  "v_mfma_f32_16x16x32_bf16 v[132:135], v[236:239], v[56:59], v[132:135]\n\t" \
  "v_mfma_f32_16x16x32_bf16 v[140:143], v[236:239], v[60:63], v[140:143]\n\t" \
  "s_setprio 0\n\t" \
  "s_add_u32 s92, s92, 65024\n\t" \
  "s_addc_u32 s93, s93, 0\n\t" \
  "ds_read_b128 v[160:163], v253 offset:0\n\t" \
  "ds_read_b128 v[164:167], v253 offset:1024\n\t" \
  "ds_read_b128 v[176:179], v240 offset:32768\n\t" \
  "ds_read_b128 v[180:183], v240 offset:49152\n\t" \
  "ds_read_b128 v[184:187], v241 offset:32768\n\t" \
  "ds_read_b128 v[188:191], v241 offset:49152\n\t" \
  "s_add_u32 m0, %[lw], 0\n\t" \
  "s_nop 0\n\t" \
  "global_load_lds_dwordx4 v248, s[92:93] sc1\n\t" \
  "ds_read_b128 v[192:195], v242 offset:32768\n\t" \
  "ds_read_b128 v[196:199], v242 offset:49152\n\t" \
  "ds_read_b128 v[200:203], v243 offset:32768\n\t" \
  "ds_read_b128 v[204:207], v243 offset:49152\n\t" \
  "s_add_u32 m0, %[lw], 1024\n\t" \
  "s_nop 0\n\t" \
  "global_load_lds_dwordx4 v249, s[92:93] sc1\n\t" \
  "ds_read_b128 v[208:211], v240 offset:33024\n\t" \
  "ds_read_b128 v[212:215], v240 offset:49408\n\t" \
  "ds_read_b128 v[216:219], v241 offset:33024\n\t" \
  "ds_read_b128 v[220:223], v241 offset:49408\n\t" \
  "s_add_u32 m0, %[lw], 2048\n\t" \
  "s_nop 0\n\t" \
  "global_load_lds_dwordx4 v250, s[92:93] sc1\n\t" \
  "ds_read_b128 v[224:227], v242 offset:33024\n\t" \
  "ds_read_b128 v[228:231], v242 offset:49408\n\t" \
  "ds_read_b128 v[232:235], v243 offset:33024\n\t" \
  "ds_read_b128 v[236:239], v243 offset:49408\n\t" \
  "s_add_u32 m0, %[lw], 3072\n\t" \
  "s_nop 0\n\t" \
  "global_load_lds_dwordx4 v251, s[92:93] sc1\n\t" \
  "s_waitcnt lgkmcnt(15)\n\t" \
  "s_waitcnt lgkmcnt(12)\n\t" \
  "s_sub_i32 s91, s86, 2\n\t" \
  "v_mov_b32 v173, s91\n\t" \
  "buffer_store_dword v173, v174, %[rs], 0 offen sc1\n\t" \
  "s_lshr_b32 s90, s87, 7\n\t" \
  "s_xor_b32 s90, s90, %[s0]\n\t" \
  "s_and_b32 s90, s90, 1\n\t" \
  "s_lshl1_add_u32 s90, s87, s90\n\t" \
  "s_lshl_b32 s90, s90, 13\n\t" \
  "v_add_f32 v160, v160, v144\n\t" \
  "v_add_f32 v161, v161, v145\n\t" \
  "v_add_f32 v162, v162, v146\n\t" \
  "v_add_f32 v163, v163, v147\n\t" \
  "v_add_f32 v164, v164, v148\n\t" \
  "v_add_f32 v165, v165, v149\n\t" \
  "v_add_f32 v166, v166, v150\n\t" \
  "v_add_f32 v167, v167, v151\n\t" \
  "v_cvt_pk_bf16_f32 v168, v160, v161\n\t" \
  "v_cvt_pk_bf16_f32 v169, v162, v163\n\t" \
  "v_cvt_pk_bf16_f32 v170, v164, v165\n\t" \
  "v_cvt_pk_bf16_f32 v171, v166, v167\n\t" \
  "buffer_store_dwordx4 v[168:171], v172, %[rt], s90 offen sc1\n\t" \
  "s_add_u32 s86, s86, 1\n\t" \
  "s_add_u32 s87, s87, 1\n\t" \
  "s_waitcnt lgkmcnt(0)\n\t" \
  "s_waitcnt vmcnt(10)\n\t" \
  "s_barrier\n\t" \
  "s_setprio 2\n\t" \
  "v_mfma_f32_16x16x32_bf16 v[128:131], v[176:179], v[64:67], v[128:131]\n\t" \
  "v_mfma_f32_16x16x32_bf16 v[136:139], v[176:179], v[68:71], v[136:139]\n\t" \
  "v_mfma_f32_16x16x32_bf16 v[132:135], v[180:183], v[64:67], v[132:135]\n\t" \
  "v_mfma_f32_16x16x32_bf16 v[140:143], v[180:183], v[68:71], v[140:143]\n\t" \
  "v_mfma_f32_16x16x32_bf16 v[128:131], v[184:187], v[72:75], v[128:131]\n\t" \
  "v_mfma_f32_16x16x32_bf16 v[136:139], v[184:187], v[76:79], v[136:139]\n\t" \
  "v_mfma_f32_16x16x32_bf16 v[132:135], v[188:191], v[72:75], v[132:135]\n\t" \
  "v_mfma_f32_16x16x32_bf16 v[140:143], v[188:191], v[76:79], v[140:143]\n\t" \
  "v_mfma_f32_16x16x32_bf16 v[128:131], v[192:195], v[80:83], v[128:131]\n\t" \
  "v_mfma_f32_16x16x32_bf16 v[136:139], v[192:195], v[84:87], v[136:139]\n\t" \
  "v_mfma_f32_16x16x32_bf16 v[132:135], v[196:199], v[80:83], v[132:135]\n\t" \
  "v_mfma_f32_16x16x32_bf16 v[140:143], v[196:199], v[84:87], v[140:143]\n\t" \
  "v_mfma_f32_16x16x32_bf16 v[128:131], v[200:203], v[88:91], v[128:131]\n\t" \
  "v_mfma_f32_16x16x32_bf16 v[136:139], v[200:203], v[92:95], v[136:139]\n\t" \
  "v_mfma_f32_16x16x32_bf16 v[132:135], v[204:207], v[88:91], v[132:135]\n\t" \
  "v_mfma_f32_16x16x32_bf16 v[140:143], v[204:207], v[92:95], v[140:143]\n\t" \
  "v_mfma_f32_16x16x32_bf16 v[128:131], v[208:211], v[96:99], v[128:131]\n\t" \
  "v_mfma_f32_16x16x32_bf16 v[136:139], v[208:211], v[100:103], v[136:139]\n\t" \
  "v_mfma_f32_16x16x32_bf16 v[132:135], v[212:215], v[96:99], v[132:135]\n\t" \
  "v_mfma_f32_16x16x32_bf16 v[140:143], v[212:215], v[100:103], v[140:143]\n\t" \
  "v_mfma_f32_16x16x32_bf16 v[128:131], v[216:219], v[104:107], v[128:131]\n\t" \
  "v_mfma_f32_16x16x32_bf16 v[136:139], v[216:219], v[108:111], v[136:139]\n\t" \
  "v_mfma_f32_16x16x32_bf16 v[132:135], v[220:223], v[104:107], v[132:135]\n\t" \
  "v_mfma_f32_16x16x32_bf16 v[140:143], v[220:223], v[108:111], v[140:143]\n\t" \
  "v_mfma_f32_16x16x32_bf16 v[128:131], v[224:227], v[112:115], v[128:131]\n\t" \
  "v_mfma_f32_16x16x32_bf16 v[136:139], v[224:227], v[116:119], v[136:139]\n\t" \
  "v_mfma_f32_16x16x32_bf16 v[132:135], v[228:231], v[112:115], v[132:135]\n\t" \
  "v_mfma_f32_16x16x32_bf16 v[140:143], v[228:231], v[116:119], v[140:143]\n\t" \
  "v_mfma_f32_16x16x32_bf16 v[128:131], v[232:235], v[120:123], v[128:131]\n\t" \
  "v_mfma_f32_16x16x32_bf16 v[136:139], v[232:235], v[124:127], v[136:139]\n\t" \
  "v_mfma_f32_16x16x32_bf16 v[132:135], v[236:239], v[120:123], v[132:135]\n\t" \
  "v_mfma_f32_16x16x32_bf16 v[140:143], v[236:239], v[124:127], v[140:143]\n\t" \
  "s_setprio 0\n\t" \
  "s_add_u32 s92, s92, 512\n\t" \
  "s_addc_u32 s93, s93, 0\n\t" \
  "ds_read_b128 v[176:179], v244 offset:0\n\t" \
  "ds_read_b128 v[180:183], v244 offset:16384\n\t" \
  "ds_read_b128 v[184:187], v245 offset:0\n\t" \
  "ds_read_b128 v[188:191], v245 offset:16384\n\t" \
  "s_add_u32 m0, %[lw], 32768\n\t" \
  "s_nop 0\n\t" \
  "global_load_lds_dwordx4 v248, s[92:93] sc1\n\t" \
  "ds_read_b128 v[192:195], v246 offset:0\n\t" \
  "ds_read_b128 v[196:199], v246 offset:16384\n\t" \
  "ds_read_b128 v[200:203], v247 offset:0\n\t" \
  "ds_read_b128 v[204:207], v247 offset:16384\n\t" \
  "s_add_u32 m0, %[lw], 33792\n\t" \
  "s_nop 0\n\t" \
  "global_load_lds_dwordx4 v249, s[92:93] sc1\n\t" \
  "ds_read_b128 v[208:211], v244 offset:256\n\t" \
  "ds_read_b128 v[212:215], v244 offset:16640\n\t" \
  "ds_read_b128 v[216:219], v245 offset:256\n\t" \
  "ds_read_b128 v[220:223], v245 offset:16640\n\t" \
  "s_add_u32 m0, %[lw], 34816\n\t" \
  "s_nop 0\n\t" \
  "global_load_lds_dwordx4 v250, s[92:93] sc1\n\t" \
  "ds_read_b128 v[224:227], v246 offset:256\n\t" \
  "ds_read_b128 v[228:231], v246 offset:16640\n\t" \
  "ds_read_b128 v[232:235], v247 offset:256\n\t" \
  "ds_read_b128 v[236:239], v247 offset:16640\n\t" \
  "s_add_u32 m0, %[lw], 35840\n\t" \
  "s_nop 0\n\t" \
  "global_load_lds_dwordx4 v251, s[92:93] sc1\n\t" \
  "ds_write_b128 v252, v[136:139] offset:0\n\t" \
  "ds_write_b128 v252, v[140:143] offset:1024\n\t" \
  "s_waitcnt lgkmcnt(0)\n\t" \
  "s_waitcnt vmcnt(4)\n\t" \
  "s_barrier\n\t" \
  "s_setprio 2\n\t" \
  "v_mfma_f32_16x16x32_bf16 v[144:147], v[176:179], v[0:3], 0\n\t" \
  "v_mfma_f32_16x16x32_bf16 v[152:155], v[176:179], v[4:7], 0\n\t" \
  "v_mfma_f32_16x16x32_bf16 v[148:151], v[180:183], v[0:3], 0\n\t" \
  "v_mfma_f32_16x16x32_bf16 v[156:159], v[180:183], v[4:7], 0\n\t" \
  "v_mfma_f32_16x16x32_bf16 v[144:147], v[184:187], v[8:11], v[144:147]\n\t" \
  "v_mfma_f32_16x16x32_bf16 v[152:155], v[184:187], v[12:15], v[152:155]\n\t" \
  "v_mfma_f32_16x16x32_bf16 v[148:151], v[188:191], v[8:11], v[148:151]\n\t" \
  "v_mfma_f32_16x16x32_bf16 v[156:159], v[188:191], v[12:15], v[156:159]\n\t" \
  "v_mfma_f32_16x16x32_bf16 v[144:147], v[192:195], v[16:19], v[144:147]\n\t" \
  "v_mfma_f32_16x16x32_bf16 v[152:155], v[192:195], v[20:23], v[152:155]\n\t" \
  "v_mfma_f32_16x16x32_bf16 v[148:151], v[196:199], v[16:19], v[148:151]\n\t" \
  "v_mfma_f32_16x16x32_bf16 v[156:159], v[196:199], v[20:23], v[156:159]\n\t" \
  "v_mfma_f32_16x16x32_bf16 v[144:147], v[200:203], v[24:27], v[144:147]\n\t" \
  "v_mfma_f32_16x16x32_bf16 v[152:155], v[200:203], v[28:31], v[152:155]\n\t" \
  "v_mfma_f32_16x16x32_bf16 v[148:151], v[204:207], v[24:27], v[148:151]\n\t" \
  "v_mfma_f32_16x16x32_bf16 v[156:159], v[204:207], v[28:31], v[156:159]\n\t" \
  "v_mfma_f32_16x16x32_bf16 v[144:147], v[208:211], v[32:35], v[144:147]\n\t" \
  "v_mfma_f32_16x16x32_bf16 v[152:155], v[208:211], v[36:39], v[152:155]\n\t" \
  "v_mfma_f32_16x16x32_bf16 v[148:151], v[212:215], v[32:35], v[148:151]\n\t" \
  "v_mfma_f32_16x16x32_bf16 v[156:159], v[212:215], v[36:39], v[156:159]\n\t" \
  "v_mfma_f32_16x16x32_bf16 v[144:147], v[216:219], v[40:43], v[144:147]\n\t" \
  "v_mfma_f32_16x16x32_bf16 v[152:155], v[216:219], v[44:47], v[152:155]\n\t" \
  "v_mfma_f32_16x16x32_bf16 v[148:151], v[220:223], v[40:43], v[148:151]\n\t" \
  "v_mfma_f32_16x16x32_bf16 v[156:159], v[220:223], v[44:47], v[156:159]\n\t" \
  "v_mfma_f32_16x16x32_bf16 v[144:147], v[224:227], v[48:51], v[144:147]\n\t" \
  "v_mfma_f32_16x16x32_bf16 v[152:155], v[224:227], v[52:55], v[152:155]\n\t" \
  "v_mfma_f32_16x16x32_bf16 v[148:151], v[228:231], v[48:51], v[148:151]\n\t" \
  "v_mfma_f32_16x16x32_bf16 v[156:159], v[228:231], v[52:55], v[156:159]\n\t" \
  "v_mfma_f32_16x16x32_bf16 v[144:147], v[232:235], v[56:59], v[144:147]\n\t" \
  "v_mfma_f32_16x16x32_bf16 v[152:155], v[232:235], v[60:63], v[152:155]\n\t" \
  "v_mfma_f32_16x16x32_bf16 v[148:151], v[236:239], v[56:59], v[148:151]\n\t" \
  "v_mfma_f32_16x16x32_bf16 v[156:159], v[236:239], v[60:63], v[156:159]\n\t" \
  "s_setprio 0\n\t" \
  "s_add_u32 s92, s92, 65024\n\t" \
  "s_addc_u32 s93, s93, 0\n\t" \
  "ds_read_b128 v[160:163], v253 offset:0\n\t" \
  "ds_read_b128 v[164:167], v253 offset:1024\n\t" \
  "ds_read_b128 v[176:179], v244 offset:32768\n\t" \
  "ds_read_b128 v[180:183], v244 offset:49152\n\t" \
  "ds_read_b128 v[184:187], v245 offset:32768\n\t" \
  "ds_read_b128 v[188:191], v245 offset:49152\n\t" \
  "s_add_u32 m0, %[lw], 65536\n\t" \
  "s_nop 0\n\t" \
  "global_load_lds_dwordx4 v248, s[92:93] sc1\n\t" \
  "ds_read_b128 v[192:195], v246 offset:32768\n\t" \
  "ds_read_b128 v[196:199], v246 offset:49152\n\t" \
  "ds_read_b128 v[200:203], v247 offset:32768\n\t" \
  "ds_read_b128 v[204:207], v247 offset:49152\n\t" \
  "s_add_u32 m0, %[lw], 66560\n\t" \
  "s_nop 0\n\t" \
  "global_load_lds_dwordx4 v249, s[92:93] sc1\n\t" \
  "ds_read_b128 v[208:211], v244 offset:33024\n\t" \
  "ds_read_b128 v[212:215], v244 offset:49408\n\t" \
  "ds_read_b128 v[216:219], v245 offset:33024\n\t" \
  "ds_read_b128 v[220:223], v245 offset:49408\n\t" \
  "s_add_u32 m0, %[lw], 67584\n\t" \
  "s_nop 0\n\t" \
  "global_load_lds_dwordx4 v250, s[92:93] sc1\n\t" \
  "ds_read_b128 v[224:227], v246 offset:33024\n\t" \
  "ds_read_b128 v[228:231], v246 offset:49408\n\t" \
  "ds_read_b128 v[232:235], v247 offset:33024\n\t" \
  "ds_read_b128 v[236:239], v247 offset:49408\n\t" \
  "s_add_u32 m0, %[lw], 68608\n\t" \
  "s_nop 0\n\t" \
  "global_load_lds_dwordx4 v251, s[92:93] sc1\n\t" \
  "s_waitcnt lgkmcnt(15)\n\t" \
  "s_waitcnt lgkmcnt(12)\n\t" \
  "s_sub_i32 s91, s86, 2\n\t" \
  "v_mov_b32 v173, s91\n\t" \
  "buffer_store_dword v173, v174, %[rs], 0 offen sc1\n\t" \
  "s_lshr_b32 s90, s87, 7\n\t" \
  "s_xor_b32 s90, s90, %[s0]\n\t" \
  "s_and_b32 s90, s90, 1\n\t" \
  "s_lshl1_add_u32 s90, s87, s90\n\t" \
  "s_lshl_b32 s90, s90, 13\n\t" \
  "v_add_f32 v160, v160, v128\n\t" \
  "v_add_f32 v161, v161, v129\n\t" \
  "v_add_f32 v162, v162, v130\n\t" \
  "v_add_f32 v163, v163, v131\n\t" \
  "v_add_f32 v164, v164, v132\n\t" \
  "v_add_f32 v165, v165, v133\n\t" \
  "v_add_f32 v166, v166, v134\n\t" \
  "v_add_f32 v167, v167, v135\n\t" \
  "v_cvt_pk_bf16_f32 v168, v160, v161\n\t" \
  "v_cvt_pk_bf16_f32 v169, v162, v163\n\t" \
  "v_cvt_pk_bf16_f32 v170, v164, v165\n\t" \
  "v_cvt_pk_bf16_f32 v171, v166, v167\n\t" \
  "buffer_store_dwordx4 v[168:171], v172, %[rt], s90 offen sc1\n\t" \
  "s_add_u32 s86, s86, 1\n\t" \
  "s_add_u32 s87, s87, 1\n\t" \
  "s_waitcnt lgkmcnt(0)\n\t" \
  "s_cmp_lt_u32 s86, 64\n\t" \
  "s_cbranch_scc1 .Lp1%=\n\t" \
  "s_barrier\n\t" \
  "s_setprio 2\n\t" \
  "v_mfma_f32_16x16x32_bf16 v[144:147], v[176:179], v[64:67], v[144:147]\n\t" \
  "v_mfma_f32_16x16x32_bf16 v[152:155], v[176:179], v[68:71], v[152:155]\n\t" \
  "v_mfma_f32_16x16x32_bf16 v[148:151], v[180:183], v[64:67], v[148:151]\n\t" \
  "v_mfma_f32_16x16x32_bf16 v[156:159], v[180:183], v[68:71], v[156:159]\n\t" \
  "v_mfma_f32_16x16x32_bf16 v[144:147], v[184:187], v[72:75], v[144:147]\n\t" \
  "v_mfma_f32_16x16x32_bf16 v[152:155], v[184:187], v[76:79], v[152:155]\n\t" \
  "v_mfma_f32_16x16x32_bf16 v[148:151], v[188:191], v[72:75], v[148:151]\n\t" \
  "v_mfma_f32_16x16x32_bf16 v[156:159], v[188:191], v[76:79], v[156:159]\n\t" \
  "v_mfma_f32_16x16x32_bf16 v[144:147], v[192:195], v[80:83], v[144:147]\n\t" \
  "v_mfma_f32_16x16x32_bf16 v[152:155], v[192:195], v[84:87], v[152:155]\n\t" \
  "v_mfma_f32_16x16x32_bf16 v[148:151], v[196:199], v[80:83], v[148:151]\n\t" \
  "v_mfma_f32_16x16x32_bf16 v[156:159], v[196:199], v[84:87], v[156:159]\n\t" \
  "v_mfma_f32_16x16x32_bf16 v[144:147], v[200:203], v[88:91], v[144:147]\n\t" \
  "v_mfma_f32_16x16x32_bf16 v[152:155], v[200:203], v[92:95], v[152:155]\n\t" \
  "v_mfma_f32_16x16x32_bf16 v[148:151], v[204:207], v[88:91], v[148:151]\n\t" \
  "v_mfma_f32_16x16x32_bf16 v[156:159], v[204:207], v[92:95], v[156:159]\n\t" \
  "v_mfma_f32_16x16x32_bf16 v[144:147], v[208:211], v[96:99], v[144:147]\n\t" \
  "v_mfma_f32_16x16x32_bf16 v[152:155], v[208:211], v[100:103], v[152:155]\n\t" \
  "v_mfma_f32_16x16x32_bf16 v[148:151], v[212:215], v[96:99], v[148:151]\n\t" \
  "v_mfma_f32_16x16x32_bf16 v[156:159], v[212:215], v[100:103], v[156:159]\n\t" \
  "v_mfma_f32_16x16x32_bf16 v[144:147], v[216:219], v[104:107], v[144:147]\n\t" \
  "v_mfma_f32_16x16x32_bf16 v[152:155], v[216:219], v[108:111], v[152:155]\n\t" \
  "v_mfma_f32_16x16x32_bf16 v[148:151], v[220:223], v[104:107], v[148:151]\n\t" \
  "v_mfma_f32_16x16x32_bf16 v[156:159], v[220:223], v[108:111], v[156:159]\n\t" \
  "v_mfma_f32_16x16x32_bf16 v[144:147], v[224:227], v[112:115], v[144:147]\n\t" \
  "v_mfma_f32_16x16x32_bf16 v[152:155], v[224:227], v[116:119], v[152:155]\n\t" \
  "v_mfma_f32_16x16x32_bf16 v[148:151], v[228:231], v[112:115], v[148:151]\n\t" \
  "v_mfma_f32_16x16x32_bf16 v[156:159], v[228:231], v[116:119], v[156:159]\n\t" \
  "v_mfma_f32_16x16x32_bf16 v[144:147], v[232:235], v[120:123], v[144:147]\n\t" \
  "v_mfma_f32_16x16x32_bf16 v[152:155], v[232:235], v[124:127], v[152:155]\n\t" \
  "v_mfma_f32_16x16x32_bf16 v[148:151], v[236:239], v[120:123], v[148:151]\n\t" \
  "v_mfma_f32_16x16x32_bf16 v[156:159], v[236:239], v[124:127], v[156:159]\n\t" \
  "s_setprio 0\n\t" \
  "s_nop 4\n\t" \
  "ds_write_b128 v252, v[152:155] offset:0\n\t" \
  "s_nop 0\n\t" \
  "ds_write_b128 v252, v[156:159] offset:1024\n\t" \
  "s_waitcnt lgkmcnt(0)\n\t" \
  "s_barrier\n\t" \
  "ds_read_b128 v[160:163], v253 offset:0\n\t" \
  "ds_read_b128 v[164:167], v253 offset:1024\n\t" \
  "s_waitcnt lgkmcnt(0)\n\t" \
  "s_lshr_b32 s90, s87, 7\n\t" \
  "s_xor_b32 s90, s90, %[s0]\n\t" \
  "s_and_b32 s90, s90, 1\n\t" \
  "s_lshl1_add_u32 s90, s87, s90\n\t" \
  "s_lshl_b32 s90, s90, 13\n\t" \
  "v_add_f32 v160, v160, v144\n\t" \
  "v_add_f32 v161, v161, v145\n\t" \
  "v_add_f32 v162, v162, v146\n\t" \
  "v_add_f32 v163, v163, v147\n\t" \
  "v_add_f32 v164, v164, v148\n\t" \
  "v_add_f32 v165, v165, v149\n\t" \
  "v_add_f32 v166, v166, v150\n\t" \
  "v_add_f32 v167, v167, v151\n\t" \
  "v_cvt_pk_bf16_f32 v168, v160, v161\n\t" \
  "v_cvt_pk_bf16_f32 v169, v162, v163\n\t" \
  "v_cvt_pk_bf16_f32 v170, v164, v165\n\t" \
  "v_cvt_pk_bf16_f32 v171, v166, v167\n\t" \
  "buffer_store_dwordx4 v[168:171], v172, %[rt], s90 offen sc1\n\t" \
  "s_waitcnt vmcnt(0)\n\t" \
  "s_barrier\n\t" \
  "s_sub_i32 s91, s86, 1\n\t" \
  "v_mov_b32 v173, s91\n\t" \
  "buffer_store_dword v173, v174, %[rs], 0 offen sc1\n\t" \
  "s_mov_b32 m0, s94\n\t" \
  ".Le%=:\n\t"
constexpr int W_X = 131072, W_PM = W_X + 16384;
template <bool ISV>
__device__ __forceinline__ void job_proj(Args& p, int g, int which, int h, char* smem, __amdgpu_buffer_rsrc_t rsy) {
  const int tid = threadIdx.x, lane = tid & 63, r = lane & 15, q = lane >> 4;
  const int w = __builtin_amdgcn_readfirstlane(tid >> 6), kh = w >> 2, cg = w & 3;
  u32x32 W[4];
  const unsigned lw = lds_off(smem) + w * 4096;
  u32x4 vx;
  u32x2 tag;
  const char* xn = p.ws + WS_XN;
  auto row = [&](int t, int i) {
    const char* src = xn + (size_t)(min(t, 127) >> 1) * (SEG * XLD * 2) + (t & 1) * 512;
    unsigned keep;
    asm volatile("s_mov_b32 %0, m0\n\ts_mov_b32 m0, %3\n\ts_nop 0\n\tglobal_load_lds_dwordx4 %1, %2 sc1\n\ts_mov_b32 m0, %0"
                 : "=&s"(keep) : "v"(vx[i]), "s"(src), "s"(lw + (t & 3) * 32768 + i * 1024) : "memory");
  };
  {
    const u16* Wm = which == 0 ? p.Wk : p.Wq;
    asm volatile("" : "+s"(Wm));
    if (ISV) Wm = p.Wv;
    const unsigned vo = (tid >> 4) * (HD * 2) + 16 * ((lane & 15) ^ swzB((tid >> 4) & 15));
    auto stage = [&](int j) {
      const char* src = (const char*)(Wm + (size_t)(WREV ? 7 - j : j) * 64 * HD + h * DH);
      const unsigned dst = lds_off(smem) + (j & 3) * 32768 + w * 1024;
      glds2(src, 32 * HD * 2, vo, dst);
      glds2(src + 512 * HD * 2, 32 * HD * 2, vo, dst + 16384);
    };
    const unsigned own = min((h & LN_M) ^ LN_X, 1);
    tag = start_rows<16>(p, 32 + g, own, [&]() { stage(0); stage(1); stage(2); stage(3); });
#pragma unroll
    for (int i = 0; i < 4; ++i) vx[i] = ((4 * w + i) ^ (ISV ? 0 : (lane >> 1) & 16)) * (XLD * 2) + (lane >> 5) * 1024 + (((lane & 31) ^ ((4 * w + i) & 15)) << 4);
    Boot bt;
#pragma unroll
    for (int j = 0; j < 8; ++j) {
      if (j < 4) WAIT_V(16); else if (j == 4) WAIT_V(12); else if (j == 5) WAIT_V(8); else if (j == 6) WAIT_V(4); else WAIT_V(10);
      if (j == 6 && w == 0) lds_at(smem + W_PM)[0] = boot_done(p.ws, bt, true, tag);
      LBAR();
      if (j == 4 && own) report_rows(p, 32 + g, tag);
      const char* sb = smem + (j & 3) * 32768 + kh * 16384;
#pragma unroll
      for (int k = 0; k < 4; ++k) {
        const int ks = k >> 1, a = k & 1;
        const unsigned ch = ISV ? 2 * (2 * cg + (a ^ kh)) + ((r & 3) >> 1) : 4 * cg + (r & 3), by = ISV ? 8 * (r & 1) : 8 * a;
        s16x4 lo = __builtin_amdgcn_ds_read_tr16_b64_v4i16((lds_s16x4_ptr)(sb + offB(32 * ks + 8 * q + (r >> 2), ch) + by));
        s16x4 hi = __builtin_amdgcn_ds_read_tr16_b64_v4i16((lds_s16x4_ptr)(sb + offB(32 * ks + 8 * q + 4 + (r >> 2), ch) + by));
        const int f = 4 * (WREV ? 7 - j : j) + k;
        put4(W[f >> 3], f & 7, (bf16x8){lo[0], lo[1], lo[2], lo[3], hi[0], hi[1], hi[2], hi[3]});
      }
      LBAR();
      if (j == 3 && w == 0) boot_ask(p.ws, bt);
      if (j + 4 < 8) stage(j + 4);
      if (j == 6) {
#pragma unroll
        for (int i = 0; i < 10; ++i) row(i >> 2, i & 3);
      }
    }
    p.s0 = uni(lds_at(smem + W_PM)[0] >> 31);
  }
  typedef __attribute__((ext_vector_type(8))) unsigned u32x8;
  u32x8 la;
#pragma unroll
  for (int i = 0; i < 8; ++i) la[i] = (i >> 2) * 65536 + r * 1024 + kh * 512 + (((4 * (i & 3) + q) ^ r) << 4);
  const unsigned xw = W_X + w * 2048 + lane * 16;
  u32x32 acc;
  asm volatile("" : "={v[128:159]}"(acc));
  WAIT_V(6);
  LBAR();
  {
    const int g0 = 0;
#define PR_LOOP(TXT)                                                                                                              \
    asm volatile(TXT                                                                                                              \
        : "+{v[128:159]}"(acc)                                                                                                    \
        : W4_IN, "{v[208:215]}"(la), "{v[216:219]}"(vx), "{v220}"(xw), "{v221}"(xw ^ 8192),                                       \
          "{v172}"((ISV ? 2 * cg + kh : 4 * kh + cg) * 1024 + lane * 16), "{v174}"(tid == 256 ? fkq(h, which) : 0x7F000000),      \
          [rt] "s"(raw_rsrc(p.ws + WS_T, T_BYTES)), [rs] "s"(raw_rsrc(p.ws + WS_SY, SY_TOTAL)), [s0] "s"(p.s0 ^ TF_KQV),          \
          [lw] "s"(lw), [kh] "s"(kh), [g0] "s"(g0), [end] "s"(g0 ? NSEG : PR_G1), [x0] "s"(xn + (g0 + 1) * (SEG * XLD * 2)),                       \
          [t0] "s"(which * T_Q + kseg(0, h) - 1)                                                                                  \
        : CLB8(160, 161, 162, 163, 164, 165, 166, 167), "v168", "v169", "v170", "v171", "v173", CLB8(176, 177, 178, 179, 180, 181, 182, 183), \
          CLB8(184, 185, 186, 187, 188, 189, 190, 191), CLB8(192, 193, 194, 195, 196, 197, 198, 199),                             \
          CLB8(200, 201, 202, 203, 204, 205, 206, 207), "s86", "s87", "s88", "s90", "s91", "s92", "s93", "s94", "scc", "memory")
    if (ISV) PR_LOOP(PR_ASM_1); else PR_LOOP(PR_ASM_0);
    if (w == 0 && g0 == 0) wait_rows(p.ws, tag);
  }
#define PP_LOOP(TXT)                                                                                                              \
  asm volatile(TXT                                                                                                                \
      : "+{v[128:159]}"(acc)                                                                                                      \
      : W4_IN, "{v[240:247]}"(la), "{v[248:251]}"(vx), "{v252}"(xw), "{v253}"(xw ^ 8192),                                         \
        "{v172}"((ISV ? 2 * cg + kh : 4 * kh + cg) * 1024 + lane * 16), "{v174}"(tid == 256 ? fkq(h, which) : 0x7F000000),        \
        [rt] "s"(raw_rsrc(p.ws + WS_T, T_BYTES)), [rs] "s"(raw_rsrc(p.ws + WS_SY, SY_TOTAL)), [s0] "s"(p.s0 ^ TF_KQV),            \
        [lw] "s"(lw), [kh] "s"(kh), [g0] "n"(PR_G1), [x0] "s"(xn + (PR_G1 + 1) * (SEG * XLD * 2)),                                \
        [t0] "s"(which * T_Q + kseg(0, h) - 1)                                                                                    \
      : CLB8(160, 161, 162, 163, 164, 165, 166, 167), "v168", "v169", "v170", "v171", "v173", CLB8(176, 177, 178, 179, 180, 181, 182, 183), \
        CLB8(184, 185, 186, 187, 188, 189, 190, 191), CLB32(192), CLB8(224, 225, 226, 227, 228, 229, 230, 231),                   \
        CLB8(232, 233, 234, 235, 236, 237, 238, 239), "s86", "s87", "s90", "s91", "s92", "s93", "s94", "scc", "memory")
  if (ISV) PP_LOOP(PP_ASM_1); else PP_LOOP(PP_ASM_0);
}

enum { J_NONE = 0, J_GU = 3, J_PR = 5, J_CB = 8, J_DN = 14 };
struct BigJob { int type, panel, idx; };
struct BigPtr { const char *a, *b0, *b1; };
constexpr int G_LDA = XLD * 2, G_LDB = FF * 2;

__device__ __forceinline__ BigPtr big_ptrs(const Args& p, const BigJob& j) {
  BigPtr r;
  r.a = p.ws + WS_XN + (size_t)j.panel * 128 * G_LDA;
  r.b0 = (const char*)(p.wg + j.idx * 128); r.b1 = (const char*)(p.wu + j.idx * 128);
  return r;
}
__device__ __forceinline__ void big_publish(const Args& p, const BigJob& j) {
  int tid = threadIdx.x;
  asm volatile("" : "+v"(tid));
  if (j.type == J_GU && tid == 0)
    __hip_atomic_fetch_add((gu32*)(p.ws + WS_SY + SY_CH) + j.panel * 16, 1u, __ATOMIC_RELAXED, __HIP_MEMORY_SCOPE_AGENT);
}
typedef f32x4 Acc[2][2][4][2];
__device__ __forceinline__ void big_epilogue(const Args& p, const BigJob& j, Acc& acc, char* smem) {
  int tid = threadIdx.x;
  asm volatile("" : "+v"(tid));
  const int lane = tid & 63;
  const int wid = __builtin_amdgcn_readfirstlane(tid >> 6);
  const int wr = wid >> 2, wc = wid & 3;
  {
    const __amdgpu_buffer_rsrc_t ro = mkrsrc(p.ws + WS_HF, S_LEN * 16384);
#pragma unroll
    for (int i = 0; i < 8; ++i) {
      f32x4 hh[2];
#pragma unroll
      for (int n = 0; n < 2; ++n)
#pragma unroll
        for (int jj = 0; jj < 4; ++jj) {
          const float g = acc[i >> 2][0][i & 3][n][jj], u = acc[i >> 2][1][i & 3][n][jj];
          hh[n][jj] = __builtin_amdgcn_rcpf(1.0f + __builtin_amdgcn_exp2f(g * -1.4426950408889634f)) * u;
        }
      st_sc1(ro, h_off(p, j.panel + (i >> 2)) + (j.idx * 4 + wc) * 16384 + (wr * 4 + (i & 3)) * 1024 + lane * 16,
             ((u32x4){cvtpk(hh[0][0], hh[0][1]), cvtpk(hh[0][2], hh[0][3]), cvtpk(hh[1][0], hh[1][1]), cvtpk(hh[1][2], hh[1][3])}));
    }
  }
}
__device__ __forceinline__ void gemm_role(Args& p, int g, char* smem) {
  const __amdgpu_buffer_rsrc_t rsy = mkrsrc(p.ws + WS_SY, SY_TOTAL);
  const int x = g & 3, k = g >> 2;
  volatile unsigned* slot = reinterpret_cast<volatile unsigned*>(smem + 163824);
  int rnd = k < 24 ? 2 : -1;
  auto claim = [&]() {
    ++rnd;
    const int n = k < 24 ? 32 + k : rnd * 8 + k - 24, pn = (n * 37) >> 8;
    BigJob b;
    b.type = rnd < 4 ? J_GU : (rnd == 4 ? J_DN : (rnd == 5 && k >= 24 && k - 24 < 14 - NFUSE ? J_CB : J_NONE));
    b.panel = rnd < 4 ? 14 - 2 * pn : (rnd == 4 ? 4 * ((2 - (k >> 3)) & 3) + x : NFUSE + k - 24);
    b.idx = rnd < 4 ? x + 4 * (n - 7 * pn) : k & 7;
    return b;
  };
  if (k < 24) {
    if (k >= 16) job_proj<true>(p, g, 2, x * 8 + (k & 7), smem, rsy);
    else job_proj<false>(p, g, k >> 3, x * 8 + (k & 7), smem, rsy);
  } else {
    u32x2 tag;
#pragma unroll 1
    for (int u = 0; u < (k < 30 && LN_M ? 2 : 1); ++u) {
      const int rg = u ? 32 + (4 * (k - 24) + 1) * 4 + x : g - 96;
      tag = start_rows<0>(p, rg, 1, []() {});
      WAIT_V(0);
      LBAR();
      report_rows(p, rg, tag);
    }
#pragma unroll 1
    for (int i = 0; i < PF_N; ++i) {
      const int m = i % 3, o = ((i / 3) * 32 + g - 96) * 448 + (int)threadIdx.x;
      if (threadIdx.x < 448) touch_line((const char*)(m == 0 ? p.wg : (m == 1 ? p.wu : p.wd)) + (size_t)o * 128, smem + (threadIdx.x >> 6) * 256);
      WAIT_V(1);
    }
    WAIT_V(0);
    if (threadIdx.x < 64) {
      wait_rows(p.ws, tag);
      Boot bt;
      boot_ask(p.ws, bt);
      WAIT_V(0);
      *slot = boot_done(p.ws, bt, true, tag);
    }
    LBAR();
    p.s0 = *slot >> 31;
  }
  p.s0 = uni(p.s0);
  BigJob cur = claim();
  while (cur.type != J_NONE) {
    if (cur.type != J_GU) {
      if (cur.type == J_DN) job_down(p, cur.panel, cur.idx, smem, rsy);
      else job_comb(p, cur.panel, x, rsy);
      cur = claim();
      continue;
    }
    int tid = threadIdx.x;
    asm volatile("" : "+v"(tid));
    const int lane = tid & 63;
    const int wid = __builtin_amdgcn_readfirstlane(tid >> 6);
    const int wr = wid >> 2, wc = wid & 3;
    const unsigned ra = tid >> 3, ca = 16 * ((tid & 7) ^ ((tid >> 3) & 7)), rb = tid >> 4, cb = 16 * ((tid & 15) ^ swzB((tid >> 4) & 15));
    const unsigned lw = lds_off(smem) + wid * 1024;
#define B8_SA(b, h) (((b) * 2 + (h)) * 16384)
#define B8_SB(b, h) ((4 + (b) * 2 + (h)) * 16384)
#define B8_STA(b, h, P, kt) glds2<true>((P).a + (size_t)(h) * 128 * G_LDA + (kt) * 128, 64 * G_LDA, ra * G_LDA + ca, lw + B8_SA(b, h))
#define B8_STB(b, h, P, kt) glds2(((h) ? (P).b1 : (P).b0) + (size_t)(kt) * 64 * G_LDB, 32 * G_LDB, rb * G_LDB + cb, lw + B8_SB(b, h))
#define B8_LDA(b, h) do { _Pragma("unroll") for (int m = 0; m < 4; ++m) _Pragma("unroll") for (int kk = 0; kk < 2; ++kk) \
      At[m][kk] = ldsf(smem + B8_SA(b, h) + offA(wr * 64 + m * 16 + (lane & 15), kk * 4 + (lane >> 4))); } while (0)
#define B8_LDB(dst, b, h) do { _Pragma("unroll") for (int n = 0; n < 2; ++n) _Pragma("unroll") for (int kk = 0; kk < 2; ++kk) \
      dst[n][kk] = ld_bfrag(smem + B8_SB(b, h), kk, wc * 2 + n, lane); } while (0)
#define B8_MMA(ai, bj, Bt) do { _Pragma("unroll") for (int m = 0; m < 4; ++m) _Pragma("unroll") for (int n = 0; n < 2; ++n) \
      _Pragma("unroll") for (int kk = 0; kk < 2; ++kk) acc[ai][bj][m][n] = MFMA(Bt[n][kk], At[m][kk], acc[ai][bj][m][n]); } while (0)
#define WAIT_L(n) asm volatile("s_waitcnt lgkmcnt(%0)" :: "n"(n) : "memory")
#define BAR() __builtin_amdgcn_s_barrier()
#define BAR_1() do { SCHED(); asm volatile("s_setprio 1\n\ts_cmp_eq_u32 %0, 0\n\ts_cbranch_scc1 .Lb%=\n\ts_setprio 2\n\ts_barrier\n.Lb%=:" :: "s"(wr) : "memory", "scc"); SCHED(); } while (0)
#define BAR_0() do { SCHED(); asm volatile("s_setprio 0\n\ts_cmp_lg_u32 %0, 0\n\ts_cbranch_scc1 .Lb%=\n\ts_barrier\n.Lb%=:" :: "s"(wr) : "memory", "scc"); SCHED(); } while (0)
#define WAIT_RX(c) asm volatile("s_waitcnt vmcnt(16)\n\ts_cmp_lg_u32 %0, 0\n\ts_cbranch_scc1 .Lrx%=\n\ts_waitcnt vmcnt(8)\n.Lrx%=:" :: "s"(c) : "memory", "scc")
    Acc acc;
    bf16x8 At[4][2], B0[2][2], B1[2][2];
    BigPtr cp = big_ptrs(p, cur);
    BigJob pend; pend.type = J_NONE; pend.panel = 0; pend.idx = 0;
    LBAR();
    B8_STB(0, 0, cp, 0); B8_STB(0, 1, cp, 0); B8_STA(0, 0, cp, 0); B8_STA(0, 1, cp, 0);
    B8_STB(1, 0, cp, 1); B8_STA(1, 0, cp, 1); B8_STB(1, 1, cp, 1);
    WAIT_V(8); BAR();
    BigJob nx = pend;
    for (;;) {
      bool chain = false;
      BigPtr np = cp;
      constexpr int nt = DM / 64;
#pragma unroll
      for (int i = 0; i < 32; ++i) acc[i >> 4][(i >> 3) & 1][(i >> 1) & 3][i & 1] = (f32x4){0.f, 0.f, 0.f, 0.f};
#pragma unroll 1
      for (int t = 0; t < nt; t += 2) {
        const bool last = t == nt - 2;
        if (last) {
          nx = claim();
          chain = nx.type == J_GU;
          if (chain) np = big_ptrs(p, nx);
        }
        const BigPtr sp = np;
        const int t2 = last ? 0 : t + 2;
        if (t == 2) big_publish(p, pend);
        const int rx = __builtin_amdgcn_readfirstlane(t == 0 && pend.type != J_NONE);
        B8_LDB(B0, 0, 0); B8_LDB(B1, 0, 1); SCHED(); B8_LDA(0, 0); B8_STA(1, 1, cp, t + 1);
        WAIT_RX(rx);
        WAIT_L(0); BAR_1(); B8_MMA(0, 0, B0); B8_MMA(0, 1, B1); BAR_0();
        B8_LDA(0, 1); B8_STB(0, 0, sp, t2); B8_STB(0, 1, sp, t2); B8_STA(0, 0, sp, t2);
        WAIT_RX(rx);
        WAIT_L(0); BAR_1(); B8_MMA(1, 0, B0); B8_MMA(1, 1, B1); BAR_0();
        B8_LDB(B0, 1, 0); B8_LDB(B1, 1, 1); SCHED(); B8_LDA(1, 0); B8_STA(0, 1, sp, t2);
        WAIT_V(8); WAIT_L(0); BAR_1(); B8_MMA(0, 0, B0); B8_MMA(0, 1, B1); BAR_0();
        B8_LDA(1, 1); B8_STB(1, 0, sp, t2 + 1); B8_STB(1, 1, sp, t2 + 1); B8_STA(1, 0, sp, t2 + 1);
        WAIT_V(8); WAIT_L(0); BAR_1(); B8_MMA(1, 0, B0); B8_MMA(1, 1, B1); BAR_0();
      }
      big_epilogue(p, cur, acc, smem);
      if (!chain) break;
      pend = cur; cur = nx; cp = np;
    }
    WAIT_V(0);
    BAR();
    asm volatile(".Lcode_end:");
    big_publish(p, cur);
    cur = nx;
  }
}


__device__ __forceinline__ unsigned now_q() { return (unsigned)__builtin_amdgcn_s_memrealtime() * 4u; }
__device__ __forceinline__ void pace_until(unsigned t) {
#pragma unroll 1
  for (int i = 0; i < 256; ++i) {
    const int d = (int)(t - now_q());
    if (d <= 0) break;
    if (d > 32) __builtin_amdgcn_s_sleep(1);
  }
}
#define CLB_E "s76", "s77", "s78", "s79", "s80", "s81", "s82", "s83", "s84", "s85", "s86", "s87", "s88", "s89"
#define EDGE_ASM \
  "s_memtime s[76:77]\n\t" \
  "s_memrealtime s[78:79]\n\t" \
  "s_nop 3\n\t" \
  "s_memrealtime s[80:81]\n\t" \
  "s_nop 3\n\t" \
  "s_memrealtime s[82:83]\n\t" \
  "s_nop 3\n\t" \
  "s_memrealtime s[84:85]\n\t" \
  "s_nop 3\n\t" \
  "s_memrealtime s[86:87]\n\t" \
  "s_nop 3\n\t" \
  "s_memrealtime s[88:89]\n\t" \
  "s_mov_b32 s93, 0\n\t" \
  "s_waitcnt lgkmcnt(0)\n\t" \
  "s_cmp_eq_u32 s80, s78\n\t" \
  "s_addc_u32 s93, s93, 0\n\t" \
  "s_cmp_eq_u32 s82, s78\n\t" \
  "s_addc_u32 s93, s93, 0\n\t" \
  "s_cmp_eq_u32 s84, s78\n\t" \
  "s_addc_u32 s93, s93, 0\n\t" \
  "s_cmp_eq_u32 s86, s78\n\t" \
  "s_addc_u32 s93, s93, 0\n\t" \
  "s_cmp_eq_u32 s88, s78\n\t" \
  "s_addc_u32 s93, s93, 0\n\t" \
  "s_mul_i32 s93, s93, 21\n\t" \
  "s_add_u32 s76, s76, 15\n\t" \
  "s_add_u32 %[kc], s76, s93\n\t" \
  "s_lshl2_add_u32 %[kq], s78, 16\n\t"
__device__ __forceinline__ unsigned time_of(unsigned cyc) {
  unsigned c, q;
  asm volatile(EDGE_ASM : [kc] "=&s"(c), [kq] "=&s"(q) :: CLB_E, "s93", "scc");
  return q - (((c - cyc) * 11900u) >> 16);
}
#define DS_RED 18
#define DS_MEM 11
#define LEAD_DN 2
#define LEAD_UP 6
__device__ __forceinline__ u32x2 ld2_sc1(__amdgpu_buffer_rsrc_t rs, int off, int soff = 0) {
  return __builtin_amdgcn_raw_buffer_load_b64(rs, off, soff, 16);
}
__device__ __forceinline__ void st2_sc1(__amdgpu_buffer_rsrc_t rs, int off, u32x2 v) {
  __builtin_amdgcn_raw_buffer_store_b64(v, rs, off, 0, 16);
}
__device__ __forceinline__ int row_max(int v) {
  v = max(v, __builtin_amdgcn_update_dpp(0, v, 0x128, 0xf, 0xf, false));
  v = max(v, __builtin_amdgcn_update_dpp(0, v, 0x124, 0xf, 0xf, false));
  v = max(v, __builtin_amdgcn_update_dpp(0, v, 0x122, 0xf, 0xf, false));
  v = max(v, __builtin_amdgcn_update_dpp(0, v, 0x121, 0xf, 0xf, false));
  return v;
}
__device__ __forceinline__ int wave_max(int v) {
  v = row_max(v);
  return max(max(__builtin_amdgcn_readlane(v, 0), __builtin_amdgcn_readlane(v, 16)),
             max(__builtin_amdgcn_readlane(v, 32), __builtin_amdgcn_readlane(v, 48)));
}
__device__ __forceinline__ int track(int est, int x, int up, int dn, int out) {
  const int d = x - est;
  return est + (d > out ? 4 : max(min(d, up), -dn));
}
#define WSL 12
#define PLAN_SLEEP 2
#define REF_W 3
#define REF_G 60
#define RL0 310
__device__ __forceinline__ void watch(__amdgpu_buffer_rsrc_t rsy, int s, int lane, unsigned t0, const unsigned* tgt_, gu32* abortw, int hs) {
  const int rl = (int)__builtin_amdgcn_raw_buffer_load_b32(rsy, lane * 128, SY_BR + hs, 16);
  u32x2 ra;
  for (unsigned spins = 0;;) {
    asm volatile("" ::: "memory");
    ra = ld2_sc1(rsy, (lane & 31) * 128, SY_BA + hs);
    const bool all = __all((int)ra[0] >= s);
    if (all) break;
    if (spins < 3) __builtin_amdgcn_s_sleep(PLAN_SLEEP); else __builtin_amdgcn_s_sleep(WSL);
    if (spin_fail(spins, abortw)) break;
  }
  int d = ra[0] == (unsigned)s ? (int)(ra[1] - t0) : 0;
  d = max(d, __builtin_amdgcn_update_dpp(0, d, 0xB1, 0xf, 0xf, false));
  d = max(d, __builtin_amdgcn_update_dpp(0, d, 0x4E, 0xf, 0xf, false));
  int sum = d + __builtin_amdgcn_update_dpp(0, d, 0x128, 0xf, 0xf, false);
  sum += __builtin_amdgcn_update_dpp(0, sum, 0x124, 0xf, 0xf, false);
  const int mx = wave_max(d), mean = (__builtin_amdgcn_readlane(sum, 0) + __builtin_amdgcn_readlane(sum, 16)) >> 3;
  const unsigned t = t0 + mx - min(((mx - mean) * REF_W) >> 2, REF_G) + wave_max(rl < 0 ? RL0 : rl) + (DS_RED + DS_MEM);
  lds_vu_ptr tgt = lds_at(tgt_);
  if (lane < 2) tgt[lane] = lane ? t : s + 1;
}
__device__ __forceinline__ unsigned ld_flag(__amdgpu_buffer_rsrc_t rs, int off) {
  return __builtin_amdgcn_raw_buffer_load_b32(rs, off, 0, 16);
}

constexpr unsigned long long QK_MAP[8] = {0xbaf9876d543210ecull, 0xa98f76d543210bceull, 0x98f76d543210ecbaull, 0x876fd543210bcea9ull, 0x76fd543210ecba98ull, 0x65df43210bcea987ull, 0x5df43210ecba9876ull, 0xd43f210bcea98765ull};
#define QK_ASM_0 \
  "s_waitcnt lgkmcnt(0)\n\t" \
  "v_subrev_u32 v244, 0x8000, v250\n\t" \
  "ds_read_b128 v[216:219], v248 offset:32768\n\t" \
  "ds_read_b128 v[220:223], v248 offset:33792\n\t" \
  "ds_read_b128 v[224:227], v248 offset:34816\n\t" \
  "ds_read_b128 v[228:231], v248 offset:35840\n\t" \
  "ds_read_b128 v[208:211], v244 offset:0\n\t" \
  "ds_read_b128 v[212:215], v244 offset:1024\n\t" \
  "ds_read_b128 v[236:239], v244 offset:2048\n\t" \
  "ds_read_b128 v[240:243], v244 offset:3072\n\t" \
  "v_mov_b64 v[192:193], 0\n\t" \
  "v_mov_b64 v[194:195], 0\n\t" \
  "v_mov_b64 v[196:197], 0\n\t" \
  "v_mov_b64 v[198:199], 0\n\t" \
  ".p2align 5\n\t" \
  "s_nop 0\n\t" \
  "s_nop 0\n\t" \
  "s_nop 0\n\t" \
  "s_nop 0\n\t" \
  "s_movk_i32 s92, 0x2000\n\t" \
  ".Lwt%=:\n\t" \
  "ds_read_b64 v[200:201], v248 offset:30720\n\t" \
  "s_memrealtime s[90:91]\n\t" \
  "s_sub_u32 s92, s92, 1\n\t" \
  "s_waitcnt lgkmcnt(0)\n\t" \
  "v_readfirstlane_b32 s93, v200\n\t" \
  "v_readfirstlane_b32 %[tgt], v201\n\t" \
  "s_cbranch_scc1 .Lgo%=\n\t" \
  "s_cmp_eq_u32 s93, %[step]\n\t" \
  "s_cbranch_scc1 .Lwx%=\n\t" \
  "s_sleep 1\n\t" \
  "s_branch .Lwt%=\n\t" \
  ".Lwx%=:\n\t" \
  "s_lshl_b32 s93, s90, 2\n\t" \
  "s_sub_i32 s93, %[tgt], s93\n\t" \
  "s_cmp_le_i32 s93, 0\n\t" \
  "s_cbranch_scc1 .Lgo%=\n\t" \
  "s_min_i32 s93, s93, 8000\n\t" \
  "s_mov_b32 s94, s93\n\t" \
  ".Lsl%=:\n\t" \
  "s_sub_i32 s94, s94, 18\n\t" \
  "s_cmp_lt_i32 s94, 30\n\t" \
  "s_cbranch_scc1 .Lsx%=\n\t" \
  "s_sleep 1\n\t" \
  "s_branch .Lsl%=\n\t" \
  ".Lsx%=:\n\t" \
  "s_sub_i32 s93, s93, 12\n\t" \
  "s_max_i32 s93, s93, 4\n\t" \
  "s_lshr_b32 s93, s93, 2\n\t" \
  "s_add_u32 s93, s93, s90\n\t" \
  "s_memrealtime s[90:91]\n\t" \
  ".Lpace%=:\n\t" \
  "s_memrealtime s[94:95]\n\t" \
  "s_waitcnt lgkmcnt(1)\n\t" \
  "s_sub_i32 s92, s90, s93\n\t" \
  "s_cmp_ge_i32 s92, 0\n\t" \
  "s_cbranch_scc1 .Le%=\n\t" \
  "s_memrealtime s[90:91]\n\t" \
  "s_waitcnt lgkmcnt(1)\n\t" \
  "s_sub_i32 s92, s94, s93\n\t" \
  "s_cmp_lt_i32 s92, 0\n\t" \
  "s_cbranch_scc1 .Lpace%=\n\t" \
  "s_nop 4\n\t" \
  ".Le%=:\n\t" \
  "s_add_u32 s92, s92, s93\n\t" \
  "s_lshl_b32 s92, s92, 2\n\t" \
  "s_sub_i32 s92, %[tgt], s92\n\t" \
  "s_max_i32 s92, s92, 0\n\t" \
  "s_bitcmp0_b32 s92, 3\n\t" \
  "s_cbranch_scc1 .Lf3%=\n\t" \
  "s_nop 15\n\t" \
  ".Lf3%=:\n\t" \
  "s_bitcmp0_b32 s92, 2\n\t" \
  "s_cbranch_scc1 .Lf2%=\n\t" \
  "s_nop 10\n\t" \
  ".Lf2%=:\n\t" \
  ".Lgo%=:\n\t" \
  "s_mov_b32 s92, 0\n\t" \
  ".Lpoll%=:\n\t" \
  "buffer_load_dwordx4 v[200:203], v248, %[rs], %[som] offen sc1\n\t" \
  "buffer_load_dwordx4 v[204:207], v248, %[rs], %[som2] offen sc1\n\t" \
  ".p2align 5\n\t" \
  "s_waitcnt vmcnt(1) lgkmcnt(0)\n\t" \
  "ds_write_b128 v249, v[200:203]\n\t" \
  "v_smfmac_f32_16x16x64_bf16 v[192:195], v[200:203], v[208:215], v251\n\t" \
  "v_max3_u32 v244, v200, v201, v202\n\t" \
  "v_smfmac_f32_16x16x64_bf16 v[196:199], v[200:203], v[236:243], v251\n\t" \
  "s_waitcnt vmcnt(0)\n\t" \
  "ds_write_b128 v249, v[204:207] offset:8192\n\t" \
  "v_smfmac_f32_16x16x64_bf16 v[192:195], v[204:207], v[216:223], v251\n\t" \
  "v_max3_u32 v244, v244, v203, v204\n\t" \
  "v_max3_u32 v244, v244, v205, v206\n\t" \
  "v_smfmac_f32_16x16x64_bf16 v[196:199], v[204:207], v[224:231], v251\n\t" \
  "v_max_u32 v244, v244, v207\n\t" \
  "v_cmp_le_u32_e64 vcc, %[sent], v244\n\t" \
  "s_cbranch_vccnz .Lslow%=\n\t" \
  ".Lok%=:\n\t" \
  "ds_read_b128 v[216:219], v250 offset:32768\n\t" \
  "ds_read_b128 v[220:223], v250 offset:33792\n\t" \
  "ds_read_b128 v[224:227], v250 offset:34816\n\t" \
  "ds_read_b128 v[228:231], v250 offset:35840\n\t" \
  "s_waitcnt lgkmcnt(4)\n\t" \
  "s_barrier\n\t" \
  "ds_read_b128 v[208:211], v248 offset:1024\n\t" \
  "ds_read_b128 v[212:215], v248 offset:2048\n\t" \
  "ds_read_b128 v[236:239], v248 offset:3072\n\t" \
  "ds_read_b128 v[240:243], v248 offset:4096\n\t" \
  "s_cmp_eq_u32 %[step], 63\n\t" \
  "s_cbranch_scc1 .Lls%=\n\t" \
  "s_waitcnt lgkmcnt(3)\n\t" \
  "ds_read_b128 v[244:247], v248 offset:5120\n\t" \
  "v_smfmac_f32_16x16x64_bf16 v[192:195], v[208:211], v[216:223], v251\n\t" \
  "ds_read_b128 v[200:203], v248 offset:13312\n\t" \
  "v_smfmac_f32_16x16x64_bf16 v[196:199], v[208:211], v[224:231], v251\n\t" \
  "s_waitcnt lgkmcnt(4)\n\t" \
  "ds_read_b128 v[216:219], v250 offset:0\n\t" \
  "v_smfmac_f32_16x16x64_bf16 v[192:195], v[212:215], v[0:7], v251\n\t" \
  "ds_read_b128 v[220:223], v250 offset:1024\n\t" \
  "v_smfmac_f32_16x16x64_bf16 v[196:199], v[212:215], v[8:15], v251\n\t" \
  "s_waitcnt lgkmcnt(5)\n\t" \
  "ds_read_b128 v[224:227], v250 offset:2048\n\t" \
  "v_smfmac_f32_16x16x64_bf16 v[192:195], v[236:239], v[16:23], v251\n\t" \
  "ds_read_b128 v[228:231], v250 offset:3072\n\t" \
  "v_smfmac_f32_16x16x64_bf16 v[196:199], v[236:239], v[24:31], v251\n\t" \
  "s_waitcnt lgkmcnt(5)\n\t" \
  "ds_read_b128 v[204:207], v248 offset:6144\n\t" \
  "v_smfmac_f32_16x16x64_bf16 v[192:195], v[240:243], v[32:39], v251\n\t" \
  "ds_read_b128 v[208:211], v248 offset:7168\n\t" \
  "v_smfmac_f32_16x16x64_bf16 v[196:199], v[240:243], v[40:47], v251\n\t" \
  "ds_read_b128 v[212:215], v248 offset:9216\n\t" \
  "v_smfmac_f32_16x16x64_bf16 v[192:195], v[244:247], v[48:55], v251\n\t" \
  "ds_read_b128 v[236:239], v248 offset:10240\n\t" \
  "v_smfmac_f32_16x16x64_bf16 v[196:199], v[244:247], v[56:63], v251\n\t" \
  "s_waitcnt lgkmcnt(6)\n\t" \
  "v_smfmac_f32_16x16x64_bf16 v[192:195], v[200:203], v[216:223], v251\n\t" \
  "s_waitcnt lgkmcnt(4)\n\t" \
  "v_smfmac_f32_16x16x64_bf16 v[196:199], v[200:203], v[224:231], v251\n\t" \
  "s_waitcnt lgkmcnt(3)\n\t" \
  "ds_read_b128 v[240:243], v248 offset:11264\n\t" \
  "v_smfmac_f32_16x16x64_bf16 v[192:195], v[204:207], v[64:71], v251\n\t" \
  "ds_read_b128 v[244:247], v248 offset:12288\n\t" \
  "v_smfmac_f32_16x16x64_bf16 v[196:199], v[204:207], v[72:79], v251\n\t" \
  "s_waitcnt lgkmcnt(3)\n\t" \
  "v_smfmac_f32_16x16x64_bf16 v[192:195], v[208:211], v[80:87], v251\n\t" \
  "ds_read_b128 v[200:203], v248 offset:14336\n\t" \
  "v_smfmac_f32_16x16x64_bf16 v[196:199], v[208:211], v[88:95], v251\n\t" \
  "ds_read_b128 v[204:207], v248 offset:15360\n\t" \
  "v_smfmac_f32_16x16x64_bf16 v[192:195], v[212:215], v[96:103], v251\n\t" \
  "v_smfmac_f32_16x16x64_bf16 v[196:199], v[212:215], v[104:111], v251\n\t" \
  "s_waitcnt lgkmcnt(3)\n\t" \
  "v_smfmac_f32_16x16x64_bf16 v[192:195], v[236:239], v[112:119], v251\n\t" \
  "v_smfmac_f32_16x16x64_bf16 v[196:199], v[236:239], v[120:127], v251\n\t" \
  "v_smfmac_f32_16x16x64_bf16 v[192:195], v[240:243], v[128:135], v251\n\t" \
  "v_smfmac_f32_16x16x64_bf16 v[196:199], v[240:243], v[136:143], v251\n\t" \
  "s_waitcnt lgkmcnt(0)\n\t" \
  "v_smfmac_f32_16x16x64_bf16 v[192:195], v[244:247], v[144:151], v251\n\t" \
  "v_smfmac_f32_16x16x64_bf16 v[196:199], v[244:247], v[152:159], v251\n\t" \
  "v_smfmac_f32_16x16x64_bf16 v[192:195], v[200:203], v[160:167], v251\n\t" \
  "v_smfmac_f32_16x16x64_bf16 v[196:199], v[200:203], v[168:175], v251\n\t" \
  "v_smfmac_f32_16x16x64_bf16 v[192:195], v[204:207], v[176:183], v251\n\t" \
  "v_smfmac_f32_16x16x64_bf16 v[196:199], v[204:207], v[184:191], v251\n\t" \
  "ds_read_b128 v[200:203], v248 offset:16384\n\t" \
  "ds_read_b128 v[204:207], v248 offset:20480\n\t" \
  "v_mov_b32 v250, v232\n\t" \
  "v_mov_b32 v249, v233\n\t" \
  "s_nop 2\n\t" \
  "v_add_f32 v192, v192, v194\n\t" \
  "v_add_f32 v193, v193, v195\n\t" \
  "v_add_f32 v196, v196, v198\n\t" \
  "v_add_f32_e64 v197, v197, v199\n\t" \
  "v_cvt_pk_bf16_f32 v192, v192, v193\n\t" \
  "v_cvt_pk_bf16_f32 v196, v196, v197\n\t" \
  "ds_write2_b32 v252, v192, v196 offset1:64\n\t" \
  "v_mov_b32_e64 v251, v234\n\t" \
  "ds_read_b64_tr_b16 v[232:233], v250 offset:0\n\t" \
  "ds_read_b64_tr_b16 v[234:235], v250 offset:64\n\t" \
  "v_mov_b32_e64 v212, %[step]\n\t" \
  "s_mov_b64 exec, 1\n\t" \
  "ds_write_b32 v248, v212 offset:28672\n\t" \
  "s_mov_b64 exec, -1\n\t" \
  "buffer_load_dwordx4 v[192:195], v248, %[rt], %[svt] offen offset:0 sc1\n\t" \
  "buffer_load_dwordx4 v[196:199], v248, %[rt], %[svt] offen offset:1024 sc1\n\t" \
  "s_waitcnt lgkmcnt(1)\n\t" \
  "v_mfma_f32_16x16x32_bf16 v[200:203], v[200:203], v[232:235], 0\n\t" \
  "v_mfma_f32_16x16x32_bf16 v[204:207], v[204:207], v[232:235], 0\n\t" \
  "s_nop 7\n\t" \
  "v_cndmask_b32 v216, v200, v204, %[nh]\n\t" \
  "v_cndmask_b32 v217, v201, v205, %[nh]\n\t" \
  "v_cndmask_b32 v218, v202, v206, %[nh]\n\t" \
  "v_cndmask_b32 v219, v203, v207, %[nh]\n\t" \
  "ds_write_b128 v248, v[216:219] offset:16384\n\t" \
  "buffer_load_dword v252, v249, %[rs], %[sv] offen offset:0 sc1\n\t" \
  "buffer_load_dword v253, v249, %[rs], %[sv] offen offset:256 sc1\n\t" \
  "ds_read_b32 v212, v248 offset:28672\n\t" \
  "ds_read_b64_tr_b16 v[236:237], v250 offset:512\n\t" \
  "ds_read_b64_tr_b16 v[238:239], v250 offset:576\n\t" \
  "ds_read_b64_tr_b16 v[240:241], v250 offset:1024\n\t" \
  "ds_read_b64_tr_b16 v[242:243], v250 offset:1088\n\t" \
  "ds_read_b64_tr_b16 v[244:245], v250 offset:1536\n\t" \
  "ds_read_b64_tr_b16 v[246:247], v250 offset:1600\n\t" \
  "s_setprio 3\n\t" \
  "s_waitcnt lgkmcnt(0)\n\t" \
  "v_cmp_ne_u32 vcc, %[step], v212\n\t" \
  "s_and_b32 s93, vcc_lo, 0xf\n\t" \
  "s_barrier\n\t" \
  "s_cbranch_scc1 .Lre%=\n\t" \
  ".Lrb%=:\n\t" \
  "ds_read_b64_tr_b16 v[200:201], v250 offset:2048\n\t" \
  "ds_read_b64_tr_b16 v[202:203], v250 offset:2112\n\t" \
  "ds_read_b64_tr_b16 v[204:205], v250 offset:2560\n\t" \
  "ds_read_b64_tr_b16 v[206:207], v250 offset:2624\n\t" \
  "ds_read_b64_tr_b16 v[208:209], v250 offset:3072\n\t" \
  "ds_read_b64_tr_b16 v[210:211], v250 offset:3136\n\t" \
  "ds_read_b64_tr_b16 v[212:213], v250 offset:3584\n\t" \
  "ds_read_b64_tr_b16 v[214:215], v250 offset:3648\n\t" \
  "ds_read_b128 v[220:223], v248 offset:17408\n\t" \
  "ds_read_b128 v[224:227], v248 offset:18432\n\t" \
  "ds_read_b128 v[228:231], v248 offset:19456\n\t" \
  "s_waitcnt lgkmcnt(9)\n\t" \
  "v_mfma_f32_16x16x32_bf16 v[200:203], v[200:203], v[232:235], 0\n\t" \
  "s_waitcnt lgkmcnt(7)\n\t" \
  "v_mfma_f32_16x16x32_bf16 v[200:203], v[204:207], v[236:239], v[200:203]\n\t" \
  "v_mov_b32 v233, 0\n\t" \
  "s_waitcnt lgkmcnt(5)\n\t" \
  "v_mfma_f32_16x16x32_bf16 v[200:203], v[208:211], v[240:243], v[200:203]\n\t" \
  "v_mov_b32 v234, 0\n\t" \
  "s_waitcnt lgkmcnt(3)\n\t" \
  "v_mfma_f32_16x16x32_bf16 v[200:203], v[212:215], v[244:247], v[200:203]\n\t" \
  "s_waitcnt lgkmcnt(2)\n\t" \
  "v_pk_add_f32 v[216:217], v[216:217], v[220:221]\n\t" \
  "v_pk_add_f32 v[218:219], v[218:219], v[222:223]\n\t" \
  "s_waitcnt lgkmcnt(0)\n\t" \
  "v_pk_add_f32 v[224:225], v[224:225], v[228:229]\n\t" \
  "v_pk_add_f32 v[226:227], v[226:227], v[230:231]\n\t" \
  "v_pk_add_f32 v[216:217], v[216:217], v[224:225]\n\t" \
  "v_pk_add_f32 v[218:219], v[218:219], v[226:227]\n\t" \
  "v_max3_f32 v224, v216, v217, v218\n\t" \
  "v_max_f32 v224, v224, v219\n\t" \
  "v_max3_f32 v231, v200, v201, v202\n\t" \
  "v_max_f32 v231, v231, v203\n\t" \
  "v_max_f32_dpp v225, v224, v224 row_ror:8 row_mask:0xf bank_mask:0xf\n\t" \
  "v_max_f32_dpp v226, v224, v224 row_ror:8 row_mask:0xf bank_mask:0xf\n\t" \
  "v_cndmask_b32 v200, v200, v202, %[up]\n\t" \
  "v_cndmask_b32 v201, v201, v203, %[up]\n\t" \
  "v_permlane32_swap_b32_e64 v225, v226\n\t" \
  "v_max3_f32 v205, v225, v226, v231\n\t" \
  "v_max3_f32 v206, v225, v226, v231\n\t" \
  "v_mov_b64 v[220:221], %[one]\n\t" \
  "v_mov_b64 v[222:223], %[one]\n\t" \
  "v_permlane16_swap_b32 v205, v206\n\t" \
  "v_max_f32 v204, v205, v206\n\t" \
  "v_mul_f32_e64 v204, %[negc], v204\n\t" \
  "v_fma_f32 v200, v200, %[c], v204\n\t" \
  "v_fma_f32 v201, v201, %[c], v204\n\t" \
  "v_exp_f32 v200, v200\n\t" \
  "v_fma_f32 v216, v216, %[c], v204\n\t" \
  "v_exp_f32 v201, v201\n\t" \
  "v_fma_f32 v217, v217, %[c], v204\n\t" \
  "v_exp_f32_e64 v216, v216\n\t" \
  "v_fma_f32 v218, v218, %[c], v204\n\t" \
  "v_cvt_pk_bf16_f32 v232, v200, v201\n\t" \
  "v_cvt_pk_bf16_f32 v235, v200, v201\n\t" \
  "v_exp_f32 v217, v217\n\t" \
  "v_fma_f32 v219, v219, %[c], v204\n\t" \
  "v_mfma_f32_16x16x16_bf16 v[212:215], v[220:221], v[232:233], 0\n\t" \
  "v_exp_f32 v218, v218\n\t" \
  "s_waitcnt vmcnt(0)\n\t" \
  "v_mfma_f32_16x16x16_bf16 v[224:227], v[252:253], v[232:233], 0\n\t" \
  "v_cvt_pk_bf16_f32 v210, v216, v217\n\t" \
  "v_cvt_pk_bf16_f32 v208, v216, v217\n\t" \
  "v_exp_f32 v219, v219\n\t" \
  "v_mfma_f32_16x16x16_bf16 v[228:231], v[252:253], v[234:235], 0\n\t" \
  "v_cvt_pk_bf16_f32 v211, v218, v219\n\t" \
  "v_cvt_pk_bf16_f32 v209, v218, v219\n\t" \
  "v_mov_b32_dpp v208, v210 row_ror:8 row_mask:0xf bank_mask:0xc\n\t" \
  "v_mov_b32_dpp v210, v210 row_ror:8 row_mask:0xf bank_mask:0x3\n\t" \
  "v_mov_b32_dpp v209, v211 row_ror:8 row_mask:0xf bank_mask:0xc\n\t" \
  "v_mov_b32_dpp v211, v211 row_ror:8 row_mask:0xf bank_mask:0x3\n\t" \
  "v_max_u32 v204, v252, v253\n\t" \
  "v_cmp_le_u32 vcc, %[sent], v204\n\t" \
  "v_mfma_f32_16x16x32_bf16 v[212:215], v[220:223], v[208:211], v[212:215]\n\t" \
  ".Lpv%=:\n\t" \
  "v_mfma_f32_16x16x32_bf16 v[224:227], v[192:195], v[208:211], v[224:227]\n\t" \
  "v_mfma_f32_16x16x32_bf16 v[228:231], v[196:199], v[208:211], v[228:231]\n\t" \
  "s_cbranch_vccnz .Lvs%=\n\t" \
  ".Lvok%=:\n\t" \
  "s_nop 4\n\t" \
  "v_rcp_f32_e64 v204, v212\n\t" \
  "v_cndmask_b32 v224, v224, v226, %[nh]\n\t" \
  "v_cndmask_b32 v225, v225, v227, %[nh]\n\t" \
  "v_cndmask_b32 v228, v228, v230, %[nh]\n\t" \
  "v_cndmask_b32 v229, v229, v231, %[nh]\n\t" \
  "v_pk_mul_f32 v[224:225], v[224:225], v[204:205] op_sel_hi:[1,0]\n\t" \
  "v_pk_mul_f32 v[228:229], v[228:229], v[204:205] op_sel_hi:[1,0]\n\t" \
  "v_cvt_pk_bf16_f32 v224, v224, v225\n\t" \
  "v_cvt_pk_bf16_f32 v225, v228, v229\n\t" \
  "buffer_store_dwordx2 v[224:225], v251, %[rs], %[so] offen sc1\n\t" \
  "s_memtime s[90:91]\n\t" \
  "s_setprio 0\n\t" \
  "s_waitcnt lgkmcnt(0)\n\t" \
  "s_mov_b32 %[tout], s90\n\t" \
  "s_mov_b32 %[spins], s92\n\t" \
  "s_branch .Lend%=\n\t" \
  ".Lls%=:\n\t" \
  "s_waitcnt lgkmcnt(0)\n\t" \
  "s_barrier\n\t" \
  "s_mov_b32 %[spins], 0\n\t" \
  "s_branch .Lend%=\n\t" \
  ".Lre%=:\n\t" \
  "ds_read_b64_tr_b16 v[236:237], v250 offset:512\n\t" \
  "ds_read_b64_tr_b16 v[238:239], v250 offset:576\n\t" \
  "ds_read_b64_tr_b16 v[240:241], v250 offset:1024\n\t" \
  "ds_read_b64_tr_b16 v[242:243], v250 offset:1088\n\t" \
  "ds_read_b64_tr_b16 v[244:245], v250 offset:1536\n\t" \
  "ds_read_b64_tr_b16 v[246:247], v250 offset:1600\n\t" \
  "s_waitcnt lgkmcnt(0)\n\t" \
  "s_branch .Lrb%=\n\t" \
  ".Lvs%=:\n\t" \
  "v_cmp_eq_u32 vcc, %[sent], v252\n\t" \
  "v_cmp_eq_u32 s[94:95], %[sent], v253\n\t" \
  "s_or_b64 s[94:95], s[94:95], vcc\n\t" \
  "s_cbranch_scc0 .Lvok%=\n\t" \
  "s_add_u32 s92, s92, 1\n\t" \
  "s_sleep 2\n\t" \
  "s_and_b32 s93, s92, 63\n\t" \
  "s_cbranch_scc1 .Lvr%=\n\t" \
  "buffer_load_dword v204, off, %[rs], %[sab] sc1\n\t" \
  "s_waitcnt vmcnt(0)\n\t" \
  "v_readfirstlane_b32 s93, v204\n\t" \
  "s_nop 3\n\t" \
  "s_cmp_eq_u32 s93, 1\n\t" \
  "s_cbranch_scc1 .Lvok%=\n\t" \
  "s_cmp_lt_u32 s92, 0x80000\n\t" \
  "s_cbranch_scc1 .Lvr%=\n\t" \
  "v_mov_b32 v204, 1\n\t" \
  "buffer_store_dword v204, off, %[rs], %[sab] sc1\n\t" \
  "s_branch .Lvok%=\n\t" \
  ".Lvr%=:\n\t" \
  "buffer_load_dword v252, v249, %[rs], %[sv] offen offset:0 sc1\n\t" \
  "buffer_load_dword v253, v249, %[rs], %[sv] offen offset:256 sc1\n\t" \
  "s_waitcnt vmcnt(0)\n\t" \
  "v_mfma_f32_16x16x16_bf16 v[224:227], v[252:253], v[232:233], 0\n\t" \
  "v_mfma_f32_16x16x16_bf16 v[228:231], v[252:253], v[234:235], 0\n\t" \
  "v_max_u32 v204, v252, v253\n\t" \
  "v_cmp_le_u32 vcc, %[sent], v204\n\t" \
  "s_nop 4\n\t" \
  "s_branch .Lpv%=\n\t" \
  "s_branch .Lend%=\n\t" \
  ".Lslow%=:\n\t" \
  "s_mov_b64 s[94:95], 0\n\t" \
  "v_cmp_eq_u32 vcc, %[sent], v200\n\t" \
  "s_or_b64 s[94:95], s[94:95], vcc\n\t" \
  "v_cmp_eq_u32 vcc, %[sent], v201\n\t" \
  "s_or_b64 s[94:95], s[94:95], vcc\n\t" \
  "v_cmp_eq_u32 vcc, %[sent], v202\n\t" \
  "s_or_b64 s[94:95], s[94:95], vcc\n\t" \
  "v_cmp_eq_u32 vcc, %[sent], v203\n\t" \
  "s_or_b64 s[94:95], s[94:95], vcc\n\t" \
  "v_cmp_eq_u32 vcc, %[sent], v204\n\t" \
  "s_or_b64 s[94:95], s[94:95], vcc\n\t" \
  "v_cmp_eq_u32 vcc, %[sent], v205\n\t" \
  "s_or_b64 s[94:95], s[94:95], vcc\n\t" \
  "v_cmp_eq_u32 vcc, %[sent], v206\n\t" \
  "s_or_b64 s[94:95], s[94:95], vcc\n\t" \
  "v_cmp_eq_u32 vcc, %[sent], v207\n\t" \
  "s_or_b64 s[94:95], s[94:95], vcc\n\t" \
  "s_cmp_eq_u64 s[94:95], 0\n\t" \
  "s_cbranch_scc1 .Lok%=\n\t" \
  "v_mov_b64 v[192:193], 0\n\t" \
  "v_mov_b64 v[194:195], 0\n\t" \
  "v_mov_b64 v[196:197], 0\n\t" \
  "v_mov_b64 v[198:199], 0\n\t" \
  "s_add_u32 s92, s92, 1\n\t" \
  "s_cmp_eq_u32 s92, 1\n\t" \
  "s_cbranch_scc1 .Lpoll%=\n\t" \
  "s_sleep 2\n\t" \
  "s_and_b32 s93, s92, 63\n\t" \
  "s_cmp_lg_u32 s93, 0\n\t" \
  "s_cbranch_scc1 .Lpoll%=\n\t" \
  "v_mov_b32 v245, 0\n\t" \
  "buffer_load_dword v244, v245, %[rs], %[sab] offen sc1\n\t" \
  "s_waitcnt vmcnt(0)\n\t" \
  "v_readfirstlane_b32 s93, v244\n\t" \
  "s_nop 3\n\t" \
  "s_cmp_eq_u32 s93, 1\n\t" \
  "s_cbranch_scc1 .Lok%=\n\t" \
  "s_cmp_lt_u32 s92, 0x80000\n\t" \
  "s_cbranch_scc1 .Lpoll%=\n\t" \
  "v_mov_b32 v244, 1\n\t" \
  "buffer_store_dword v244, v245, %[rs], %[sab] offen sc1\n\t" \
  "s_branch .Lok%=\n\t" \
  ".Lend%=:\n\t"
#define QK_ASM_1 \
  "s_waitcnt lgkmcnt(0)\n\t" \
  "v_subrev_u32 v244, 0x8000, v250\n\t" \
  "ds_read_b128 v[216:219], v248 offset:36864\n\t" \
  "ds_read_b128 v[220:223], v248 offset:37888\n\t" \
  "ds_read_b128 v[224:227], v248 offset:38912\n\t" \
  "ds_read_b128 v[228:231], v248 offset:39936\n\t" \
  "ds_read_b128 v[208:211], v244 offset:0\n\t" \
  "ds_read_b128 v[212:215], v244 offset:1024\n\t" \
  "ds_read_b128 v[236:239], v244 offset:2048\n\t" \
  "ds_read_b128 v[240:243], v244 offset:3072\n\t" \
  "v_mov_b64 v[192:193], 0\n\t" \
  "v_mov_b64 v[194:195], 0\n\t" \
  "v_mov_b64 v[196:197], 0\n\t" \
  "v_mov_b64 v[198:199], 0\n\t" \
  ".p2align 5\n\t" \
  "s_nop 0\n\t" \
  "s_nop 0\n\t" \
  "s_nop 0\n\t" \
  "s_nop 0\n\t" \
  "s_movk_i32 s92, 0x2000\n\t" \
  ".Lwt%=:\n\t" \
  "ds_read_b64 v[200:201], v248 offset:30720\n\t" \
  "s_memrealtime s[90:91]\n\t" \
  "s_sub_u32 s92, s92, 1\n\t" \
  "s_waitcnt lgkmcnt(0)\n\t" \
  "v_readfirstlane_b32 s93, v200\n\t" \
  "v_readfirstlane_b32 %[tgt], v201\n\t" \
  "s_cbranch_scc1 .Lgo%=\n\t" \
  "s_cmp_eq_u32 s93, %[step]\n\t" \
  "s_cbranch_scc1 .Lwx%=\n\t" \
  "s_sleep 1\n\t" \
  "s_branch .Lwt%=\n\t" \
  ".Lwx%=:\n\t" \
  "s_lshl_b32 s93, s90, 2\n\t" \
  "s_sub_i32 s93, %[tgt], s93\n\t" \
  "s_cmp_le_i32 s93, 0\n\t" \
  "s_cbranch_scc1 .Lgo%=\n\t" \
  "s_min_i32 s93, s93, 8000\n\t" \
  "s_mov_b32 s94, s93\n\t" \
  ".Lsl%=:\n\t" \
  "s_sub_i32 s94, s94, 18\n\t" \
  "s_cmp_lt_i32 s94, 30\n\t" \
  "s_cbranch_scc1 .Lsx%=\n\t" \
  "s_sleep 1\n\t" \
  "s_branch .Lsl%=\n\t" \
  ".Lsx%=:\n\t" \
  "s_sub_i32 s93, s93, 12\n\t" \
  "s_max_i32 s93, s93, 4\n\t" \
  "s_lshr_b32 s93, s93, 2\n\t" \
  "s_add_u32 s93, s93, s90\n\t" \
  "s_memrealtime s[90:91]\n\t" \
  ".Lpace%=:\n\t" \
  "s_memrealtime s[94:95]\n\t" \
  "s_waitcnt lgkmcnt(1)\n\t" \
  "s_sub_i32 s92, s90, s93\n\t" \
  "s_cmp_ge_i32 s92, 0\n\t" \
  "s_cbranch_scc1 .Le%=\n\t" \
  "s_memrealtime s[90:91]\n\t" \
  "s_waitcnt lgkmcnt(1)\n\t" \
  "s_sub_i32 s92, s94, s93\n\t" \
  "s_cmp_lt_i32 s92, 0\n\t" \
  "s_cbranch_scc1 .Lpace%=\n\t" \
  "s_nop 4\n\t" \
  ".Le%=:\n\t" \
  "s_add_u32 s92, s92, s93\n\t" \
  "s_lshl_b32 s92, s92, 2\n\t" \
  "s_sub_i32 s92, %[tgt], s92\n\t" \
  "s_max_i32 s92, s92, 0\n\t" \
  "s_bitcmp0_b32 s92, 3\n\t" \
  "s_cbranch_scc1 .Lf3%=\n\t" \
  "s_nop 15\n\t" \
  ".Lf3%=:\n\t" \
  "s_bitcmp0_b32 s92, 2\n\t" \
  "s_cbranch_scc1 .Lf2%=\n\t" \
  "s_nop 10\n\t" \
  ".Lf2%=:\n\t" \
  ".Lgo%=:\n\t" \
  "s_mov_b32 s92, 0\n\t" \
  ".Lpoll%=:\n\t" \
  "buffer_load_dwordx4 v[200:203], v248, %[rs], %[som] offen sc1\n\t" \
  "buffer_load_dwordx4 v[204:207], v248, %[rs], %[som2] offen sc1\n\t" \
  ".p2align 5\n\t" \
  "s_waitcnt vmcnt(1) lgkmcnt(0)\n\t" \
  "ds_write_b128 v249, v[200:203]\n\t" \
  "v_smfmac_f32_16x16x64_bf16 v[192:195], v[200:203], v[208:215], v251\n\t" \
  "v_max3_u32 v244, v200, v201, v202\n\t" \
  "v_smfmac_f32_16x16x64_bf16 v[196:199], v[200:203], v[236:243], v251\n\t" \
  "s_waitcnt vmcnt(0)\n\t" \
  "ds_write_b128 v249, v[204:207] offset:8192\n\t" \
  "v_smfmac_f32_16x16x64_bf16 v[192:195], v[204:207], v[216:223], v251\n\t" \
  "v_max3_u32 v244, v244, v203, v204\n\t" \
  "v_max3_u32 v244, v244, v205, v206\n\t" \
  "v_smfmac_f32_16x16x64_bf16 v[196:199], v[204:207], v[224:231], v251\n\t" \
  "v_max_u32 v244, v244, v207\n\t" \
  "v_cmp_le_u32_e64 vcc, %[sent], v244\n\t" \
  "s_cbranch_vccnz .Lslow%=\n\t" \
  ".Lok%=:\n\t" \
  "ds_read_b128 v[216:219], v250 offset:32768\n\t" \
  "ds_read_b128 v[220:223], v250 offset:33792\n\t" \
  "ds_read_b128 v[224:227], v250 offset:34816\n\t" \
  "ds_read_b128 v[228:231], v250 offset:35840\n\t" \
  "s_waitcnt lgkmcnt(4)\n\t" \
  "s_barrier\n\t" \
  "ds_read_b128 v[208:211], v248 offset:0\n\t" \
  "ds_read_b128 v[212:215], v248 offset:3072\n\t" \
  "ds_read_b128 v[236:239], v248 offset:4096\n\t" \
  "ds_read_b128 v[240:243], v248 offset:5120\n\t" \
  "s_cmp_eq_u32 %[step], 63\n\t" \
  "s_cbranch_scc1 .Lls%=\n\t" \
  "s_waitcnt lgkmcnt(3)\n\t" \
  "ds_read_b128 v[244:247], v248 offset:6144\n\t" \
  "v_smfmac_f32_16x16x64_bf16 v[192:195], v[208:211], v[216:223], v251\n\t" \
  "ds_read_b128 v[200:203], v248 offset:12288\n\t" \
  "v_smfmac_f32_16x16x64_bf16 v[196:199], v[208:211], v[224:231], v251\n\t" \
  "s_waitcnt lgkmcnt(4)\n\t" \
  "ds_read_b128 v[216:219], v250 offset:0\n\t" \
  "v_smfmac_f32_16x16x64_bf16 v[192:195], v[212:215], v[0:7], v251\n\t" \
  "ds_read_b128 v[220:223], v250 offset:1024\n\t" \
  "v_smfmac_f32_16x16x64_bf16 v[196:199], v[212:215], v[8:15], v251\n\t" \
  "s_waitcnt lgkmcnt(5)\n\t" \
  "ds_read_b128 v[224:227], v250 offset:2048\n\t" \
  "v_smfmac_f32_16x16x64_bf16 v[192:195], v[236:239], v[16:23], v251\n\t" \
  "ds_read_b128 v[228:231], v250 offset:3072\n\t" \
  "v_smfmac_f32_16x16x64_bf16 v[196:199], v[236:239], v[24:31], v251\n\t" \
  "s_waitcnt lgkmcnt(5)\n\t" \
  "ds_read_b128 v[204:207], v248 offset:7168\n\t" \
  "v_smfmac_f32_16x16x64_bf16 v[192:195], v[240:243], v[32:39], v251\n\t" \
  "ds_read_b128 v[208:211], v248 offset:8192\n\t" \
  "v_smfmac_f32_16x16x64_bf16 v[196:199], v[240:243], v[40:47], v251\n\t" \
  "ds_read_b128 v[212:215], v248 offset:10240\n\t" \
  "v_smfmac_f32_16x16x64_bf16 v[192:195], v[244:247], v[48:55], v251\n\t" \
  "ds_read_b128 v[236:239], v248 offset:11264\n\t" \
  "v_smfmac_f32_16x16x64_bf16 v[196:199], v[244:247], v[56:63], v251\n\t" \
  "s_waitcnt lgkmcnt(6)\n\t" \
  "v_smfmac_f32_16x16x64_bf16 v[192:195], v[200:203], v[216:223], v251\n\t" \
  "s_waitcnt lgkmcnt(4)\n\t" \
  "v_smfmac_f32_16x16x64_bf16 v[196:199], v[200:203], v[224:231], v251\n\t" \
  "s_waitcnt lgkmcnt(3)\n\t" \
  "ds_read_b128 v[240:243], v248 offset:13312\n\t" \
  "v_smfmac_f32_16x16x64_bf16 v[192:195], v[204:207], v[64:71], v251\n\t" \
  "ds_read_b128 v[244:247], v248 offset:14336\n\t" \
  "v_smfmac_f32_16x16x64_bf16 v[196:199], v[204:207], v[72:79], v251\n\t" \
  "s_waitcnt lgkmcnt(3)\n\t" \
  "v_smfmac_f32_16x16x64_bf16 v[192:195], v[208:211], v[80:87], v251\n\t" \
  "ds_read_b128 v[200:203], v248 offset:15360\n\t" \
  "v_smfmac_f32_16x16x64_bf16 v[196:199], v[208:211], v[88:95], v251\n\t" \
  "ds_read_b128 v[204:207], v248 offset:2048\n\t" \
  "v_smfmac_f32_16x16x64_bf16 v[192:195], v[212:215], v[96:103], v251\n\t" \
  "v_smfmac_f32_16x16x64_bf16 v[196:199], v[212:215], v[104:111], v251\n\t" \
  "s_waitcnt lgkmcnt(3)\n\t" \
  "v_smfmac_f32_16x16x64_bf16 v[192:195], v[236:239], v[112:119], v251\n\t" \
  "v_smfmac_f32_16x16x64_bf16 v[196:199], v[236:239], v[120:127], v251\n\t" \
  "v_smfmac_f32_16x16x64_bf16 v[192:195], v[240:243], v[128:135], v251\n\t" \
  "v_smfmac_f32_16x16x64_bf16 v[196:199], v[240:243], v[136:143], v251\n\t" \
  "s_waitcnt lgkmcnt(0)\n\t" \
  "v_smfmac_f32_16x16x64_bf16 v[192:195], v[244:247], v[144:151], v251\n\t" \
  "v_smfmac_f32_16x16x64_bf16 v[196:199], v[244:247], v[152:159], v251\n\t" \
  "v_smfmac_f32_16x16x64_bf16 v[192:195], v[200:203], v[160:167], v251\n\t" \
  "v_smfmac_f32_16x16x64_bf16 v[196:199], v[200:203], v[168:175], v251\n\t" \
  "v_smfmac_f32_16x16x64_bf16 v[192:195], v[204:207], v[176:183], v251\n\t" \
  "v_smfmac_f32_16x16x64_bf16 v[196:199], v[204:207], v[184:191], v251\n\t" \
  "ds_read_b128 v[200:203], v248 offset:17408\n\t" \
  "ds_read_b128 v[204:207], v248 offset:21504\n\t" \
  "v_mov_b32 v250, v232\n\t" \
  "v_mov_b32 v249, v233\n\t" \
  "s_nop 2\n\t" \
  "v_add_f32 v192, v192, v194\n\t" \
  "v_add_f32 v193, v193, v195\n\t" \
  "v_add_f32 v196, v196, v198\n\t" \
  "v_add_f32_e64 v197, v197, v199\n\t" \
  "v_cvt_pk_bf16_f32 v192, v192, v193\n\t" \
  "v_cvt_pk_bf16_f32 v196, v196, v197\n\t" \
  "ds_write2_b32 v252, v192, v196 offset1:64\n\t" \
  "v_mov_b32_e64 v251, v234\n\t" \
  "ds_read_b64_tr_b16 v[236:237], v250 offset:512\n\t" \
  "ds_read_b64_tr_b16 v[238:239], v250 offset:576\n\t" \
  "v_mov_b32_e64 v212, %[step]\n\t" \
  "s_mov_b64 exec, 1\n\t" \
  "ds_write_b32 v248, v212 offset:28688\n\t" \
  "s_mov_b64 exec, -1\n\t" \
  "buffer_load_dwordx4 v[192:195], v248, %[rt], %[svt] offen offset:0 sc1\n\t" \
  "buffer_load_dwordx4 v[196:199], v248, %[rt], %[svt] offen offset:1024 sc1\n\t" \
  "s_waitcnt lgkmcnt(1)\n\t" \
  "v_mfma_f32_16x16x32_bf16 v[200:203], v[200:203], v[236:239], 0\n\t" \
  "v_mfma_f32_16x16x32_bf16 v[204:207], v[204:207], v[236:239], 0\n\t" \
  "s_nop 7\n\t" \
  "v_cndmask_b32 v216, v200, v204, %[nh]\n\t" \
  "v_cndmask_b32 v217, v201, v205, %[nh]\n\t" \
  "v_cndmask_b32 v218, v202, v206, %[nh]\n\t" \
  "v_cndmask_b32 v219, v203, v207, %[nh]\n\t" \
  "ds_write_b128 v248, v[216:219] offset:17408\n\t" \
  "buffer_load_dword v252, v249, %[rs], %[sv] offen offset:0 sc1\n\t" \
  "buffer_load_dword v253, v249, %[rs], %[sv] offen offset:256 sc1\n\t" \
  "ds_read_b32 v212, v248 offset:28672\n\t" \
  "ds_read_b64_tr_b16 v[240:241], v250 offset:1024\n\t" \
  "ds_read_b64_tr_b16 v[242:243], v250 offset:1088\n\t" \
  "ds_read_b64_tr_b16 v[244:245], v250 offset:1536\n\t" \
  "ds_read_b64_tr_b16 v[246:247], v250 offset:1600\n\t" \
  "ds_read_b64_tr_b16 v[232:233], v250 offset:0\n\t" \
  "ds_read_b64_tr_b16 v[234:235], v250 offset:64\n\t" \
  "s_setprio 3\n\t" \
  "s_waitcnt lgkmcnt(0)\n\t" \
  "v_cmp_ne_u32 vcc, %[step], v212\n\t" \
  "s_and_b32 s93, vcc_lo, 0xf\n\t" \
  "s_barrier\n\t" \
  "s_cbranch_scc1 .Lre%=\n\t" \
  ".Lrb%=:\n\t" \
  "ds_read_b64_tr_b16 v[204:205], v250 offset:2560\n\t" \
  "ds_read_b64_tr_b16 v[206:207], v250 offset:2624\n\t" \
  "ds_read_b64_tr_b16 v[208:209], v250 offset:3072\n\t" \
  "ds_read_b64_tr_b16 v[210:211], v250 offset:3136\n\t" \
  "ds_read_b64_tr_b16 v[212:213], v250 offset:3584\n\t" \
  "ds_read_b64_tr_b16 v[214:215], v250 offset:3648\n\t" \
  "ds_read_b64_tr_b16 v[200:201], v250 offset:2048\n\t" \
  "ds_read_b64_tr_b16 v[202:203], v250 offset:2112\n\t" \
  "ds_read_b128 v[220:223], v248 offset:18432\n\t" \
  "ds_read_b128 v[224:227], v248 offset:19456\n\t" \
  "ds_read_b128 v[228:231], v248 offset:16384\n\t" \
  "s_waitcnt lgkmcnt(9)\n\t" \
  "v_mfma_f32_16x16x32_bf16 v[204:207], v[204:207], v[236:239], 0\n\t" \
  "s_waitcnt lgkmcnt(7)\n\t" \
  "v_mfma_f32_16x16x32_bf16 v[204:207], v[208:211], v[240:243], v[204:207]\n\t" \
  "v_mov_b32 v237, 0\n\t" \
  "s_waitcnt lgkmcnt(5)\n\t" \
  "v_mfma_f32_16x16x32_bf16 v[204:207], v[212:215], v[244:247], v[204:207]\n\t" \
  "v_mov_b32 v238, 0\n\t" \
  "s_waitcnt lgkmcnt(3)\n\t" \
  "v_mfma_f32_16x16x32_bf16 v[204:207], v[200:203], v[232:235], v[204:207]\n\t" \
  "s_waitcnt lgkmcnt(2)\n\t" \
  "v_pk_add_f32 v[216:217], v[216:217], v[220:221]\n\t" \
  "v_pk_add_f32 v[218:219], v[218:219], v[222:223]\n\t" \
  "s_waitcnt lgkmcnt(0)\n\t" \
  "v_pk_add_f32 v[224:225], v[224:225], v[228:229]\n\t" \
  "v_pk_add_f32 v[226:227], v[226:227], v[230:231]\n\t" \
  "v_pk_add_f32 v[216:217], v[216:217], v[224:225]\n\t" \
  "v_pk_add_f32 v[218:219], v[218:219], v[226:227]\n\t" \
  "v_max3_f32 v224, v216, v217, v218\n\t" \
  "v_max_f32 v224, v224, v219\n\t" \
  "v_max3_f32 v231, v204, v205, v206\n\t" \
  "v_max_f32 v231, v231, v207\n\t" \
  "v_max_f32_dpp v225, v224, v224 row_ror:8 row_mask:0xf bank_mask:0xf\n\t" \
  "v_max_f32_dpp v226, v224, v224 row_ror:8 row_mask:0xf bank_mask:0xf\n\t" \
  "v_cndmask_b32 v204, v204, v206, %[up]\n\t" \
  "v_cndmask_b32 v205, v205, v207, %[up]\n\t" \
  "v_permlane32_swap_b32_e64 v225, v226\n\t" \
  "v_max3_f32 v209, v225, v226, v231\n\t" \
  "v_max3_f32 v210, v225, v226, v231\n\t" \
  "v_mov_b64 v[220:221], %[one]\n\t" \
  "v_mov_b64 v[222:223], %[one]\n\t" \
  "v_permlane16_swap_b32 v209, v210\n\t" \
  "v_max_f32 v208, v209, v210\n\t" \
  "v_mul_f32_e64 v208, %[negc], v208\n\t" \
  "v_fma_f32 v204, v204, %[c], v208\n\t" \
  "v_fma_f32 v205, v205, %[c], v208\n\t" \
  "v_exp_f32 v204, v204\n\t" \
  "v_fma_f32 v216, v216, %[c], v208\n\t" \
  "v_exp_f32 v205, v205\n\t" \
  "v_fma_f32 v217, v217, %[c], v208\n\t" \
  "v_exp_f32_e64 v216, v216\n\t" \
  "v_fma_f32 v218, v218, %[c], v208\n\t" \
  "v_cvt_pk_bf16_f32 v236, v204, v205\n\t" \
  "v_cvt_pk_bf16_f32 v239, v204, v205\n\t" \
  "v_exp_f32 v217, v217\n\t" \
  "v_fma_f32 v219, v219, %[c], v208\n\t" \
  "v_mfma_f32_16x16x16_bf16 v[200:203], v[220:221], v[236:237], 0\n\t" \
  "v_exp_f32 v218, v218\n\t" \
  "s_waitcnt vmcnt(0)\n\t" \
  "v_mfma_f32_16x16x16_bf16 v[224:227], v[252:253], v[236:237], 0\n\t" \
  "v_cvt_pk_bf16_f32 v214, v216, v217\n\t" \
  "v_cvt_pk_bf16_f32 v212, v216, v217\n\t" \
  "v_exp_f32 v219, v219\n\t" \
  "v_mfma_f32_16x16x16_bf16 v[228:231], v[252:253], v[238:239], 0\n\t" \
  "v_cvt_pk_bf16_f32 v215, v218, v219\n\t" \
  "v_cvt_pk_bf16_f32 v213, v218, v219\n\t" \
  "v_mov_b32_dpp v212, v214 row_ror:8 row_mask:0xf bank_mask:0xc\n\t" \
  "v_mov_b32_dpp v214, v214 row_ror:8 row_mask:0xf bank_mask:0x3\n\t" \
  "v_mov_b32_dpp v213, v215 row_ror:8 row_mask:0xf bank_mask:0xc\n\t" \
  "v_mov_b32_dpp v215, v215 row_ror:8 row_mask:0xf bank_mask:0x3\n\t" \
  "v_max_u32 v208, v252, v253\n\t" \
  "v_cmp_le_u32 vcc, %[sent], v208\n\t" \
  "v_mfma_f32_16x16x32_bf16 v[200:203], v[220:223], v[212:215], v[200:203]\n\t" \
  ".Lpv%=:\n\t" \
  "v_mfma_f32_16x16x32_bf16 v[224:227], v[192:195], v[212:215], v[224:227]\n\t" \
  "v_mfma_f32_16x16x32_bf16 v[228:231], v[196:199], v[212:215], v[228:231]\n\t" \
  "s_cbranch_vccnz .Lvs%=\n\t" \
  ".Lvok%=:\n\t" \
  "s_nop 4\n\t" \
  "v_rcp_f32_e64 v208, v200\n\t" \
  "v_cndmask_b32 v224, v224, v226, %[nh]\n\t" \
  "v_cndmask_b32 v225, v225, v227, %[nh]\n\t" \
  "v_cndmask_b32 v228, v228, v230, %[nh]\n\t" \
  "v_cndmask_b32 v229, v229, v231, %[nh]\n\t" \
  "v_pk_mul_f32 v[224:225], v[224:225], v[208:209] op_sel_hi:[1,0]\n\t" \
  "v_pk_mul_f32 v[228:229], v[228:229], v[208:209] op_sel_hi:[1,0]\n\t" \
  "v_cvt_pk_bf16_f32 v224, v224, v225\n\t" \
  "v_cvt_pk_bf16_f32 v225, v228, v229\n\t" \
  "buffer_store_dwordx2 v[224:225], v251, %[rs], %[so] offen sc1\n\t" \
  "s_memtime s[90:91]\n\t" \
  "s_setprio 0\n\t" \
  "s_waitcnt lgkmcnt(0)\n\t" \
  "s_mov_b32 %[tout], s90\n\t" \
  "s_mov_b32 %[spins], s92\n\t" \
  "s_branch .Lend%=\n\t" \
  ".Lls%=:\n\t" \
  "s_waitcnt lgkmcnt(0)\n\t" \
  "s_barrier\n\t" \
  "s_mov_b32 %[spins], 0\n\t" \
  "s_branch .Lend%=\n\t" \
  ".Lre%=:\n\t" \
  "ds_read_b64_tr_b16 v[240:241], v250 offset:1024\n\t" \
  "ds_read_b64_tr_b16 v[242:243], v250 offset:1088\n\t" \
  "ds_read_b64_tr_b16 v[244:245], v250 offset:1536\n\t" \
  "ds_read_b64_tr_b16 v[246:247], v250 offset:1600\n\t" \
  "ds_read_b64_tr_b16 v[232:233], v250 offset:0\n\t" \
  "ds_read_b64_tr_b16 v[234:235], v250 offset:64\n\t" \
  "s_waitcnt lgkmcnt(0)\n\t" \
  "s_branch .Lrb%=\n\t" \
  ".Lvs%=:\n\t" \
  "v_cmp_eq_u32 vcc, %[sent], v252\n\t" \
  "v_cmp_eq_u32 s[94:95], %[sent], v253\n\t" \
  "s_or_b64 s[94:95], s[94:95], vcc\n\t" \
  "s_cbranch_scc0 .Lvok%=\n\t" \
  "s_add_u32 s92, s92, 1\n\t" \
  "s_sleep 2\n\t" \
  "s_and_b32 s93, s92, 63\n\t" \
  "s_cbranch_scc1 .Lvr%=\n\t" \
  "buffer_load_dword v208, off, %[rs], %[sab] sc1\n\t" \
  "s_waitcnt vmcnt(0)\n\t" \
  "v_readfirstlane_b32 s93, v208\n\t" \
  "s_nop 3\n\t" \
  "s_cmp_eq_u32 s93, 1\n\t" \
  "s_cbranch_scc1 .Lvok%=\n\t" \
  "s_cmp_lt_u32 s92, 0x80000\n\t" \
  "s_cbranch_scc1 .Lvr%=\n\t" \
  "v_mov_b32 v208, 1\n\t" \
  "buffer_store_dword v208, off, %[rs], %[sab] sc1\n\t" \
  "s_branch .Lvok%=\n\t" \
  ".Lvr%=:\n\t" \
  "buffer_load_dword v252, v249, %[rs], %[sv] offen offset:0 sc1\n\t" \
  "buffer_load_dword v253, v249, %[rs], %[sv] offen offset:256 sc1\n\t" \
  "s_waitcnt vmcnt(0)\n\t" \
  "v_mfma_f32_16x16x16_bf16 v[224:227], v[252:253], v[236:237], 0\n\t" \
  "v_mfma_f32_16x16x16_bf16 v[228:231], v[252:253], v[238:239], 0\n\t" \
  "v_max_u32 v208, v252, v253\n\t" \
  "v_cmp_le_u32 vcc, %[sent], v208\n\t" \
  "s_nop 4\n\t" \
  "s_branch .Lpv%=\n\t" \
  "s_branch .Lend%=\n\t" \
  ".Lslow%=:\n\t" \
  "s_mov_b64 s[94:95], 0\n\t" \
  "v_cmp_eq_u32 vcc, %[sent], v200\n\t" \
  "s_or_b64 s[94:95], s[94:95], vcc\n\t" \
  "v_cmp_eq_u32 vcc, %[sent], v201\n\t" \
  "s_or_b64 s[94:95], s[94:95], vcc\n\t" \
  "v_cmp_eq_u32 vcc, %[sent], v202\n\t" \
  "s_or_b64 s[94:95], s[94:95], vcc\n\t" \
  "v_cmp_eq_u32 vcc, %[sent], v203\n\t" \
  "s_or_b64 s[94:95], s[94:95], vcc\n\t" \
  "v_cmp_eq_u32 vcc, %[sent], v204\n\t" \
  "s_or_b64 s[94:95], s[94:95], vcc\n\t" \
  "v_cmp_eq_u32 vcc, %[sent], v205\n\t" \
  "s_or_b64 s[94:95], s[94:95], vcc\n\t" \
  "v_cmp_eq_u32 vcc, %[sent], v206\n\t" \
  "s_or_b64 s[94:95], s[94:95], vcc\n\t" \
  "v_cmp_eq_u32 vcc, %[sent], v207\n\t" \
  "s_or_b64 s[94:95], s[94:95], vcc\n\t" \
  "s_cmp_eq_u64 s[94:95], 0\n\t" \
  "s_cbranch_scc1 .Lok%=\n\t" \
  "v_mov_b64 v[192:193], 0\n\t" \
  "v_mov_b64 v[194:195], 0\n\t" \
  "v_mov_b64 v[196:197], 0\n\t" \
  "v_mov_b64 v[198:199], 0\n\t" \
  "s_add_u32 s92, s92, 1\n\t" \
  "s_cmp_eq_u32 s92, 1\n\t" \
  "s_cbranch_scc1 .Lpoll%=\n\t" \
  "s_sleep 2\n\t" \
  "s_and_b32 s93, s92, 63\n\t" \
  "s_cmp_lg_u32 s93, 0\n\t" \
  "s_cbranch_scc1 .Lpoll%=\n\t" \
  "v_mov_b32 v245, 0\n\t" \
  "buffer_load_dword v244, v245, %[rs], %[sab] offen sc1\n\t" \
  "s_waitcnt vmcnt(0)\n\t" \
  "v_readfirstlane_b32 s93, v244\n\t" \
  "s_nop 3\n\t" \
  "s_cmp_eq_u32 s93, 1\n\t" \
  "s_cbranch_scc1 .Lok%=\n\t" \
  "s_cmp_lt_u32 s92, 0x80000\n\t" \
  "s_cbranch_scc1 .Lpoll%=\n\t" \
  "v_mov_b32 v244, 1\n\t" \
  "buffer_store_dword v244, v245, %[rs], %[sab] offen sc1\n\t" \
  "s_branch .Lok%=\n\t" \
  ".Lend%=:\n\t"
#define QK_ASM_2 \
  "s_waitcnt lgkmcnt(0)\n\t" \
  "v_subrev_u32 v244, 0x8000, v250\n\t" \
  "ds_read_b128 v[216:219], v248 offset:40960\n\t" \
  "ds_read_b128 v[220:223], v248 offset:41984\n\t" \
  "ds_read_b128 v[224:227], v248 offset:43008\n\t" \
  "ds_read_b128 v[228:231], v248 offset:44032\n\t" \
  "ds_read_b128 v[208:211], v244 offset:0\n\t" \
  "ds_read_b128 v[212:215], v244 offset:1024\n\t" \
  "ds_read_b128 v[236:239], v244 offset:2048\n\t" \
  "ds_read_b128 v[240:243], v244 offset:3072\n\t" \
  "v_mov_b64 v[192:193], 0\n\t" \
  "v_mov_b64 v[194:195], 0\n\t" \
  "v_mov_b64 v[196:197], 0\n\t" \
  "v_mov_b64 v[198:199], 0\n\t" \
  ".p2align 5\n\t" \
  "s_nop 0\n\t" \
  "s_nop 0\n\t" \
  "s_nop 0\n\t" \
  "s_nop 0\n\t" \
  "s_movk_i32 s92, 0x2000\n\t" \
  ".Lwt%=:\n\t" \
  "ds_read_b64 v[200:201], v248 offset:30720\n\t" \
  "s_memrealtime s[90:91]\n\t" \
  "s_sub_u32 s92, s92, 1\n\t" \
  "s_waitcnt lgkmcnt(0)\n\t" \
  "v_readfirstlane_b32 s93, v200\n\t" \
  "v_readfirstlane_b32 %[tgt], v201\n\t" \
  "s_cbranch_scc1 .Lgo%=\n\t" \
  "s_cmp_eq_u32 s93, %[step]\n\t" \
  "s_cbranch_scc1 .Lwx%=\n\t" \
  "s_sleep 1\n\t" \
  "s_branch .Lwt%=\n\t" \
  ".Lwx%=:\n\t" \
  "s_lshl_b32 s93, s90, 2\n\t" \
  "s_sub_i32 s93, %[tgt], s93\n\t" \
  "s_cmp_le_i32 s93, 0\n\t" \
  "s_cbranch_scc1 .Lgo%=\n\t" \
  "s_min_i32 s93, s93, 8000\n\t" \
  "s_mov_b32 s94, s93\n\t" \
  ".Lsl%=:\n\t" \
  "s_sub_i32 s94, s94, 18\n\t" \
  "s_cmp_lt_i32 s94, 30\n\t" \
  "s_cbranch_scc1 .Lsx%=\n\t" \
  "s_sleep 1\n\t" \
  "s_branch .Lsl%=\n\t" \
  ".Lsx%=:\n\t" \
  "s_sub_i32 s93, s93, 12\n\t" \
  "s_max_i32 s93, s93, 4\n\t" \
  "s_lshr_b32 s93, s93, 2\n\t" \
  "s_add_u32 s93, s93, s90\n\t" \
  "s_memrealtime s[90:91]\n\t" \
  ".Lpace%=:\n\t" \
  "s_memrealtime s[94:95]\n\t" \
  "s_waitcnt lgkmcnt(1)\n\t" \
  "s_sub_i32 s92, s90, s93\n\t" \
  "s_cmp_ge_i32 s92, 0\n\t" \
  "s_cbranch_scc1 .Le%=\n\t" \
  "s_memrealtime s[90:91]\n\t" \
  "s_waitcnt lgkmcnt(1)\n\t" \
  "s_sub_i32 s92, s94, s93\n\t" \
  "s_cmp_lt_i32 s92, 0\n\t" \
  "s_cbranch_scc1 .Lpace%=\n\t" \
  "s_nop 4\n\t" \
  ".Le%=:\n\t" \
  "s_add_u32 s92, s92, s93\n\t" \
  "s_lshl_b32 s92, s92, 2\n\t" \
  "s_sub_i32 s92, %[tgt], s92\n\t" \
  "s_max_i32 s92, s92, 0\n\t" \
  "s_bitcmp0_b32 s92, 3\n\t" \
  "s_cbranch_scc1 .Lf3%=\n\t" \
  "s_nop 15\n\t" \
  ".Lf3%=:\n\t" \
  "s_bitcmp0_b32 s92, 2\n\t" \
  "s_cbranch_scc1 .Lf2%=\n\t" \
  "s_nop 10\n\t" \
  ".Lf2%=:\n\t" \
  ".Lgo%=:\n\t" \
  "s_mov_b32 s92, 0\n\t" \
  ".Lpoll%=:\n\t" \
  "buffer_load_dwordx4 v[200:203], v248, %[rs], %[som] offen sc1\n\t" \
  "buffer_load_dwordx4 v[204:207], v248, %[rs], %[som2] offen sc1\n\t" \
  ".p2align 5\n\t" \
  "s_waitcnt vmcnt(1) lgkmcnt(0)\n\t" \
  "ds_write_b128 v249, v[200:203]\n\t" \
  "v_smfmac_f32_16x16x64_bf16 v[192:195], v[200:203], v[208:215], v251\n\t" \
  "v_max3_u32 v244, v200, v201, v202\n\t" \
  "v_smfmac_f32_16x16x64_bf16 v[196:199], v[200:203], v[236:243], v251\n\t" \
  "s_waitcnt vmcnt(0)\n\t" \
  "ds_write_b128 v249, v[204:207] offset:8192\n\t" \
  "v_smfmac_f32_16x16x64_bf16 v[192:195], v[204:207], v[216:223], v251\n\t" \
  "v_max3_u32 v244, v244, v203, v204\n\t" \
  "v_max3_u32 v244, v244, v205, v206\n\t" \
  "v_smfmac_f32_16x16x64_bf16 v[196:199], v[204:207], v[224:231], v251\n\t" \
  "v_max_u32 v244, v244, v207\n\t" \
  "v_cmp_le_u32_e64 vcc, %[sent], v244\n\t" \
  "s_cbranch_vccnz .Lslow%=\n\t" \
  ".Lok%=:\n\t" \
  "ds_read_b128 v[216:219], v250 offset:32768\n\t" \
  "ds_read_b128 v[220:223], v250 offset:33792\n\t" \
  "ds_read_b128 v[224:227], v250 offset:34816\n\t" \
  "ds_read_b128 v[228:231], v250 offset:35840\n\t" \
  "s_waitcnt lgkmcnt(4)\n\t" \
  "s_barrier\n\t" \
  "ds_read_b128 v[208:211], v248 offset:3072\n\t" \
  "ds_read_b128 v[212:215], v248 offset:4096\n\t" \
  "ds_read_b128 v[236:239], v248 offset:5120\n\t" \
  "ds_read_b128 v[240:243], v248 offset:6144\n\t" \
  "s_cmp_eq_u32 %[step], 63\n\t" \
  "s_cbranch_scc1 .Lls%=\n\t" \
  "s_waitcnt lgkmcnt(3)\n\t" \
  "ds_read_b128 v[244:247], v248 offset:7168\n\t" \
  "v_smfmac_f32_16x16x64_bf16 v[192:195], v[208:211], v[216:223], v251\n\t" \
  "ds_read_b128 v[200:203], v248 offset:13312\n\t" \
  "v_smfmac_f32_16x16x64_bf16 v[196:199], v[208:211], v[224:231], v251\n\t" \
  "s_waitcnt lgkmcnt(4)\n\t" \
  "ds_read_b128 v[216:219], v250 offset:0\n\t" \
  "v_smfmac_f32_16x16x64_bf16 v[192:195], v[212:215], v[0:7], v251\n\t" \
  "ds_read_b128 v[220:223], v250 offset:1024\n\t" \
  "v_smfmac_f32_16x16x64_bf16 v[196:199], v[212:215], v[8:15], v251\n\t" \
  "s_waitcnt lgkmcnt(5)\n\t" \
  "ds_read_b128 v[224:227], v250 offset:2048\n\t" \
  "v_smfmac_f32_16x16x64_bf16 v[192:195], v[236:239], v[16:23], v251\n\t" \
  "ds_read_b128 v[228:231], v250 offset:3072\n\t" \
  "v_smfmac_f32_16x16x64_bf16 v[196:199], v[236:239], v[24:31], v251\n\t" \
  "s_waitcnt lgkmcnt(5)\n\t" \
  "ds_read_b128 v[204:207], v248 offset:8192\n\t" \
  "v_smfmac_f32_16x16x64_bf16 v[192:195], v[240:243], v[32:39], v251\n\t" \
  "ds_read_b128 v[208:211], v248 offset:9216\n\t" \
  "v_smfmac_f32_16x16x64_bf16 v[196:199], v[240:243], v[40:47], v251\n\t" \
  "ds_read_b128 v[212:215], v248 offset:11264\n\t" \
  "v_smfmac_f32_16x16x64_bf16 v[192:195], v[244:247], v[48:55], v251\n\t" \
  "ds_read_b128 v[236:239], v248 offset:12288\n\t" \
  "v_smfmac_f32_16x16x64_bf16 v[196:199], v[244:247], v[56:63], v251\n\t" \
  "s_waitcnt lgkmcnt(6)\n\t" \
  "v_smfmac_f32_16x16x64_bf16 v[192:195], v[200:203], v[216:223], v251\n\t" \
  "s_waitcnt lgkmcnt(4)\n\t" \
  "v_smfmac_f32_16x16x64_bf16 v[196:199], v[200:203], v[224:231], v251\n\t" \
  "s_waitcnt lgkmcnt(3)\n\t" \
  "ds_read_b128 v[240:243], v248 offset:14336\n\t" \
  "v_smfmac_f32_16x16x64_bf16 v[192:195], v[204:207], v[64:71], v251\n\t" \
  "ds_read_b128 v[244:247], v248 offset:15360\n\t" \
  "v_smfmac_f32_16x16x64_bf16 v[196:199], v[204:207], v[72:79], v251\n\t" \
  "s_waitcnt lgkmcnt(3)\n\t" \
  "v_smfmac_f32_16x16x64_bf16 v[192:195], v[208:211], v[80:87], v251\n\t" \
  "ds_read_b128 v[200:203], v248 offset:0\n\t" \
  "v_smfmac_f32_16x16x64_bf16 v[196:199], v[208:211], v[88:95], v251\n\t" \
  "ds_read_b128 v[204:207], v248 offset:1024\n\t" \
  "v_smfmac_f32_16x16x64_bf16 v[192:195], v[212:215], v[96:103], v251\n\t" \
  "v_smfmac_f32_16x16x64_bf16 v[196:199], v[212:215], v[104:111], v251\n\t" \
  "s_waitcnt lgkmcnt(3)\n\t" \
  "v_smfmac_f32_16x16x64_bf16 v[192:195], v[236:239], v[112:119], v251\n\t" \
  "v_smfmac_f32_16x16x64_bf16 v[196:199], v[236:239], v[120:127], v251\n\t" \
  "v_smfmac_f32_16x16x64_bf16 v[192:195], v[240:243], v[128:135], v251\n\t" \
  "v_smfmac_f32_16x16x64_bf16 v[196:199], v[240:243], v[136:143], v251\n\t" \
  "s_waitcnt lgkmcnt(0)\n\t" \
  "v_smfmac_f32_16x16x64_bf16 v[192:195], v[244:247], v[144:151], v251\n\t" \
  "v_smfmac_f32_16x16x64_bf16 v[196:199], v[244:247], v[152:159], v251\n\t" \
  "v_smfmac_f32_16x16x64_bf16 v[192:195], v[200:203], v[160:167], v251\n\t" \
  "v_smfmac_f32_16x16x64_bf16 v[196:199], v[200:203], v[168:175], v251\n\t" \
  "v_smfmac_f32_16x16x64_bf16 v[192:195], v[204:207], v[176:183], v251\n\t" \
  "v_smfmac_f32_16x16x64_bf16 v[196:199], v[204:207], v[184:191], v251\n\t" \
  "ds_read_b128 v[200:203], v248 offset:18432\n\t" \
  "ds_read_b128 v[204:207], v248 offset:22528\n\t" \
  "v_mov_b32 v250, v232\n\t" \
  "v_mov_b32 v249, v233\n\t" \
  "s_nop 2\n\t" \
  "v_add_f32 v192, v192, v194\n\t" \
  "v_add_f32 v193, v193, v195\n\t" \
  "v_add_f32 v196, v196, v198\n\t" \
  "v_add_f32_e64 v197, v197, v199\n\t" \
  "v_cvt_pk_bf16_f32 v192, v192, v193\n\t" \
  "v_cvt_pk_bf16_f32 v196, v196, v197\n\t" \
  "ds_write2_b32 v252, v192, v196 offset1:64\n\t" \
  "v_mov_b32_e64 v251, v234\n\t" \
  "ds_read_b64_tr_b16 v[240:241], v250 offset:1024\n\t" \
  "ds_read_b64_tr_b16 v[242:243], v250 offset:1088\n\t" \
  "v_mov_b32_e64 v212, %[step]\n\t" \
  "s_mov_b64 exec, 1\n\t" \
  "ds_write_b32 v248, v212 offset:28704\n\t" \
  "s_mov_b64 exec, -1\n\t" \
  "buffer_load_dwordx4 v[192:195], v248, %[rt], %[svt] offen offset:0 sc1\n\t" \
  "buffer_load_dwordx4 v[196:199], v248, %[rt], %[svt] offen offset:1024 sc1\n\t" \
  "s_waitcnt lgkmcnt(1)\n\t" \
  "v_mfma_f32_16x16x32_bf16 v[200:203], v[200:203], v[240:243], 0\n\t" \
  "v_mfma_f32_16x16x32_bf16 v[204:207], v[204:207], v[240:243], 0\n\t" \
  "s_nop 7\n\t" \
  "v_cndmask_b32 v216, v200, v204, %[nh]\n\t" \
  "v_cndmask_b32 v217, v201, v205, %[nh]\n\t" \
  "v_cndmask_b32 v218, v202, v206, %[nh]\n\t" \
  "v_cndmask_b32 v219, v203, v207, %[nh]\n\t" \
  "ds_write_b128 v248, v[216:219] offset:18432\n\t" \
  "buffer_load_dword v252, v249, %[rs], %[sv] offen offset:0 sc1\n\t" \
  "buffer_load_dword v253, v249, %[rs], %[sv] offen offset:256 sc1\n\t" \
  "ds_read_b32 v212, v248 offset:28672\n\t" \
  "ds_read_b64_tr_b16 v[244:245], v250 offset:1536\n\t" \
  "ds_read_b64_tr_b16 v[246:247], v250 offset:1600\n\t" \
  "ds_read_b64_tr_b16 v[232:233], v250 offset:0\n\t" \
  "ds_read_b64_tr_b16 v[234:235], v250 offset:64\n\t" \
  "ds_read_b64_tr_b16 v[236:237], v250 offset:512\n\t" \
  "ds_read_b64_tr_b16 v[238:239], v250 offset:576\n\t" \
  "s_setprio 3\n\t" \
  "s_waitcnt lgkmcnt(0)\n\t" \
  "v_cmp_ne_u32 vcc, %[step], v212\n\t" \
  "s_and_b32 s93, vcc_lo, 0xf\n\t" \
  "s_barrier\n\t" \
  "s_cbranch_scc1 .Lre%=\n\t" \
  ".Lrb%=:\n\t" \
  "ds_read_b64_tr_b16 v[208:209], v250 offset:3072\n\t" \
  "ds_read_b64_tr_b16 v[210:211], v250 offset:3136\n\t" \
  "ds_read_b64_tr_b16 v[212:213], v250 offset:3584\n\t" \
  "ds_read_b64_tr_b16 v[214:215], v250 offset:3648\n\t" \
  "ds_read_b64_tr_b16 v[200:201], v250 offset:2048\n\t" \
  "ds_read_b64_tr_b16 v[202:203], v250 offset:2112\n\t" \
  "ds_read_b64_tr_b16 v[204:205], v250 offset:2560\n\t" \
  "ds_read_b64_tr_b16 v[206:207], v250 offset:2624\n\t" \
  "ds_read_b128 v[220:223], v248 offset:19456\n\t" \
  "ds_read_b128 v[224:227], v248 offset:16384\n\t" \
  "ds_read_b128 v[228:231], v248 offset:17408\n\t" \
  "s_waitcnt lgkmcnt(9)\n\t" \
  "v_mfma_f32_16x16x32_bf16 v[208:211], v[208:211], v[240:243], 0\n\t" \
  "s_waitcnt lgkmcnt(7)\n\t" \
  "v_mfma_f32_16x16x32_bf16 v[208:211], v[212:215], v[244:247], v[208:211]\n\t" \
  "v_mov_b32 v241, 0\n\t" \
  "s_waitcnt lgkmcnt(5)\n\t" \
  "v_mfma_f32_16x16x32_bf16 v[208:211], v[200:203], v[232:235], v[208:211]\n\t" \
  "v_mov_b32 v242, 0\n\t" \
  "s_waitcnt lgkmcnt(3)\n\t" \
  "v_mfma_f32_16x16x32_bf16 v[208:211], v[204:207], v[236:239], v[208:211]\n\t" \
  "s_waitcnt lgkmcnt(2)\n\t" \
  "v_pk_add_f32 v[216:217], v[216:217], v[220:221]\n\t" \
  "v_pk_add_f32 v[218:219], v[218:219], v[222:223]\n\t" \
  "s_waitcnt lgkmcnt(0)\n\t" \
  "v_pk_add_f32 v[224:225], v[224:225], v[228:229]\n\t" \
  "v_pk_add_f32 v[226:227], v[226:227], v[230:231]\n\t" \
  "v_pk_add_f32 v[216:217], v[216:217], v[224:225]\n\t" \
  "v_pk_add_f32 v[218:219], v[218:219], v[226:227]\n\t" \
  "v_max3_f32 v224, v216, v217, v218\n\t" \
  "v_max_f32 v224, v224, v219\n\t" \
  "v_max3_f32 v231, v208, v209, v210\n\t" \
  "v_max_f32 v231, v231, v211\n\t" \
  "v_max_f32_dpp v225, v224, v224 row_ror:8 row_mask:0xf bank_mask:0xf\n\t" \
  "v_max_f32_dpp v226, v224, v224 row_ror:8 row_mask:0xf bank_mask:0xf\n\t" \
  "v_cndmask_b32 v208, v208, v210, %[up]\n\t" \
  "v_cndmask_b32 v209, v209, v211, %[up]\n\t" \
  "v_permlane32_swap_b32_e64 v225, v226\n\t" \
  "v_max3_f32 v213, v225, v226, v231\n\t" \
  "v_max3_f32 v214, v225, v226, v231\n\t" \
  "v_mov_b64 v[220:221], %[one]\n\t" \
  "v_mov_b64 v[222:223], %[one]\n\t" \
  "v_permlane16_swap_b32 v213, v214\n\t" \
  "v_max_f32 v212, v213, v214\n\t" \
  "v_mul_f32_e64 v212, %[negc], v212\n\t" \
  "v_fma_f32 v208, v208, %[c], v212\n\t" \
  "v_fma_f32 v209, v209, %[c], v212\n\t" \
  "v_exp_f32 v208, v208\n\t" \
  "v_fma_f32 v216, v216, %[c], v212\n\t" \
  "v_exp_f32 v209, v209\n\t" \
  "v_fma_f32 v217, v217, %[c], v212\n\t" \
  "v_exp_f32_e64 v216, v216\n\t" \
  "v_fma_f32 v218, v218, %[c], v212\n\t" \
  "v_cvt_pk_bf16_f32 v240, v208, v209\n\t" \
  "v_cvt_pk_bf16_f32 v243, v208, v209\n\t" \
  "v_exp_f32 v217, v217\n\t" \
  "v_fma_f32 v219, v219, %[c], v212\n\t" \
  "v_mfma_f32_16x16x16_bf16 v[204:207], v[220:221], v[240:241], 0\n\t" \
  "v_exp_f32 v218, v218\n\t" \
  "s_waitcnt vmcnt(0)\n\t" \
  "v_mfma_f32_16x16x16_bf16 v[224:227], v[252:253], v[240:241], 0\n\t" \
  "v_cvt_pk_bf16_f32 v202, v216, v217\n\t" \
  "v_cvt_pk_bf16_f32 v200, v216, v217\n\t" \
  "v_exp_f32 v219, v219\n\t" \
  "v_mfma_f32_16x16x16_bf16 v[228:231], v[252:253], v[242:243], 0\n\t" \
  "v_cvt_pk_bf16_f32 v203, v218, v219\n\t" \
  "v_cvt_pk_bf16_f32 v201, v218, v219\n\t" \
  "v_mov_b32_dpp v200, v202 row_ror:8 row_mask:0xf bank_mask:0xc\n\t" \
  "v_mov_b32_dpp v202, v202 row_ror:8 row_mask:0xf bank_mask:0x3\n\t" \
  "v_mov_b32_dpp v201, v203 row_ror:8 row_mask:0xf bank_mask:0xc\n\t" \
  "v_mov_b32_dpp v203, v203 row_ror:8 row_mask:0xf bank_mask:0x3\n\t" \
  "v_max_u32 v212, v252, v253\n\t" \
  "v_cmp_le_u32 vcc, %[sent], v212\n\t" \
  "v_mfma_f32_16x16x32_bf16 v[204:207], v[220:223], v[200:203], v[204:207]\n\t" \
  ".Lpv%=:\n\t" \
  "v_mfma_f32_16x16x32_bf16 v[224:227], v[192:195], v[200:203], v[224:227]\n\t" \
  "v_mfma_f32_16x16x32_bf16 v[228:231], v[196:199], v[200:203], v[228:231]\n\t" \
  "s_cbranch_vccnz .Lvs%=\n\t" \
  ".Lvok%=:\n\t" \
  "s_nop 4\n\t" \
  "v_rcp_f32_e64 v212, v204\n\t" \
  "v_cndmask_b32 v224, v224, v226, %[nh]\n\t" \
  "v_cndmask_b32 v225, v225, v227, %[nh]\n\t" \
  "v_cndmask_b32 v228, v228, v230, %[nh]\n\t" \
  "v_cndmask_b32 v229, v229, v231, %[nh]\n\t" \
  "v_pk_mul_f32 v[224:225], v[224:225], v[212:213] op_sel_hi:[1,0]\n\t" \
  "v_pk_mul_f32 v[228:229], v[228:229], v[212:213] op_sel_hi:[1,0]\n\t" \
  "v_cvt_pk_bf16_f32 v224, v224, v225\n\t" \
  "v_cvt_pk_bf16_f32 v225, v228, v229\n\t" \
  "buffer_store_dwordx2 v[224:225], v251, %[rs], %[so] offen sc1\n\t" \
  "s_memtime s[90:91]\n\t" \
  "s_setprio 0\n\t" \
  "s_waitcnt lgkmcnt(0)\n\t" \
  "s_mov_b32 %[tout], s90\n\t" \
  "s_mov_b32 %[spins], s92\n\t" \
  "s_branch .Lend%=\n\t" \
  ".Lls%=:\n\t" \
  "s_waitcnt lgkmcnt(0)\n\t" \
  "s_barrier\n\t" \
  "s_mov_b32 %[spins], 0\n\t" \
  "s_branch .Lend%=\n\t" \
  ".Lre%=:\n\t" \
  "ds_read_b64_tr_b16 v[244:245], v250 offset:1536\n\t" \
  "ds_read_b64_tr_b16 v[246:247], v250 offset:1600\n\t" \
  "ds_read_b64_tr_b16 v[232:233], v250 offset:0\n\t" \
  "ds_read_b64_tr_b16 v[234:235], v250 offset:64\n\t" \
  "ds_read_b64_tr_b16 v[236:237], v250 offset:512\n\t" \
  "ds_read_b64_tr_b16 v[238:239], v250 offset:576\n\t" \
  "s_waitcnt lgkmcnt(0)\n\t" \
  "s_branch .Lrb%=\n\t" \
  ".Lvs%=:\n\t" \
  "v_cmp_eq_u32 vcc, %[sent], v252\n\t" \
  "v_cmp_eq_u32 s[94:95], %[sent], v253\n\t" \
  "s_or_b64 s[94:95], s[94:95], vcc\n\t" \
  "s_cbranch_scc0 .Lvok%=\n\t" \
  "s_add_u32 s92, s92, 1\n\t" \
  "s_sleep 2\n\t" \
  "s_and_b32 s93, s92, 63\n\t" \
  "s_cbranch_scc1 .Lvr%=\n\t" \
  "buffer_load_dword v212, off, %[rs], %[sab] sc1\n\t" \
  "s_waitcnt vmcnt(0)\n\t" \
  "v_readfirstlane_b32 s93, v212\n\t" \
  "s_nop 3\n\t" \
  "s_cmp_eq_u32 s93, 1\n\t" \
  "s_cbranch_scc1 .Lvok%=\n\t" \
  "s_cmp_lt_u32 s92, 0x80000\n\t" \
  "s_cbranch_scc1 .Lvr%=\n\t" \
  "v_mov_b32 v212, 1\n\t" \
  "buffer_store_dword v212, off, %[rs], %[sab] sc1\n\t" \
  "s_branch .Lvok%=\n\t" \
  ".Lvr%=:\n\t" \
  "buffer_load_dword v252, v249, %[rs], %[sv] offen offset:0 sc1\n\t" \
  "buffer_load_dword v253, v249, %[rs], %[sv] offen offset:256 sc1\n\t" \
  "s_waitcnt vmcnt(0)\n\t" \
  "v_mfma_f32_16x16x16_bf16 v[224:227], v[252:253], v[240:241], 0\n\t" \
  "v_mfma_f32_16x16x16_bf16 v[228:231], v[252:253], v[242:243], 0\n\t" \
  "v_max_u32 v212, v252, v253\n\t" \
  "v_cmp_le_u32 vcc, %[sent], v212\n\t" \
  "s_nop 4\n\t" \
  "s_branch .Lpv%=\n\t" \
  "s_branch .Lend%=\n\t" \
  ".Lslow%=:\n\t" \
  "s_mov_b64 s[94:95], 0\n\t" \
  "v_cmp_eq_u32 vcc, %[sent], v200\n\t" \
  "s_or_b64 s[94:95], s[94:95], vcc\n\t" \
  "v_cmp_eq_u32 vcc, %[sent], v201\n\t" \
  "s_or_b64 s[94:95], s[94:95], vcc\n\t" \
  "v_cmp_eq_u32 vcc, %[sent], v202\n\t" \
  "s_or_b64 s[94:95], s[94:95], vcc\n\t" \
  "v_cmp_eq_u32 vcc, %[sent], v203\n\t" \
  "s_or_b64 s[94:95], s[94:95], vcc\n\t" \
  "v_cmp_eq_u32 vcc, %[sent], v204\n\t" \
  "s_or_b64 s[94:95], s[94:95], vcc\n\t" \
  "v_cmp_eq_u32 vcc, %[sent], v205\n\t" \
  "s_or_b64 s[94:95], s[94:95], vcc\n\t" \
  "v_cmp_eq_u32 vcc, %[sent], v206\n\t" \
  "s_or_b64 s[94:95], s[94:95], vcc\n\t" \
  "v_cmp_eq_u32 vcc, %[sent], v207\n\t" \
  "s_or_b64 s[94:95], s[94:95], vcc\n\t" \
  "s_cmp_eq_u64 s[94:95], 0\n\t" \
  "s_cbranch_scc1 .Lok%=\n\t" \
  "v_mov_b64 v[192:193], 0\n\t" \
  "v_mov_b64 v[194:195], 0\n\t" \
  "v_mov_b64 v[196:197], 0\n\t" \
  "v_mov_b64 v[198:199], 0\n\t" \
  "s_add_u32 s92, s92, 1\n\t" \
  "s_cmp_eq_u32 s92, 1\n\t" \
  "s_cbranch_scc1 .Lpoll%=\n\t" \
  "s_sleep 2\n\t" \
  "s_and_b32 s93, s92, 63\n\t" \
  "s_cmp_lg_u32 s93, 0\n\t" \
  "s_cbranch_scc1 .Lpoll%=\n\t" \
  "v_mov_b32 v245, 0\n\t" \
  "buffer_load_dword v244, v245, %[rs], %[sab] offen sc1\n\t" \
  "s_waitcnt vmcnt(0)\n\t" \
  "v_readfirstlane_b32 s93, v244\n\t" \
  "s_nop 3\n\t" \
  "s_cmp_eq_u32 s93, 1\n\t" \
  "s_cbranch_scc1 .Lok%=\n\t" \
  "s_cmp_lt_u32 s92, 0x80000\n\t" \
  "s_cbranch_scc1 .Lpoll%=\n\t" \
  "v_mov_b32 v244, 1\n\t" \
  "buffer_store_dword v244, v245, %[rs], %[sab] offen sc1\n\t" \
  "s_branch .Lok%=\n\t" \
  ".Lend%=:\n\t"
#define QK_ASM_3 \
  "s_waitcnt lgkmcnt(0)\n\t" \
  "v_subrev_u32 v244, 0x8000, v250\n\t" \
  "ds_read_b128 v[216:219], v248 offset:45056\n\t" \
  "ds_read_b128 v[220:223], v248 offset:46080\n\t" \
  "ds_read_b128 v[224:227], v248 offset:47104\n\t" \
  "ds_read_b128 v[228:231], v248 offset:48128\n\t" \
  "ds_read_b128 v[208:211], v244 offset:0\n\t" \
  "ds_read_b128 v[212:215], v244 offset:1024\n\t" \
  "ds_read_b128 v[236:239], v244 offset:2048\n\t" \
  "ds_read_b128 v[240:243], v244 offset:3072\n\t" \
  "v_mov_b64 v[192:193], 0\n\t" \
  "v_mov_b64 v[194:195], 0\n\t" \
  "v_mov_b64 v[196:197], 0\n\t" \
  "v_mov_b64 v[198:199], 0\n\t" \
  ".p2align 5\n\t" \
  "s_nop 0\n\t" \
  "s_nop 0\n\t" \
  "s_nop 0\n\t" \
  "s_nop 0\n\t" \
  "s_movk_i32 s92, 0x2000\n\t" \
  ".Lwt%=:\n\t" \
  "ds_read_b64 v[200:201], v248 offset:30720\n\t" \
  "s_memrealtime s[90:91]\n\t" \
  "s_sub_u32 s92, s92, 1\n\t" \
  "s_waitcnt lgkmcnt(0)\n\t" \
  "v_readfirstlane_b32 s93, v200\n\t" \
  "v_readfirstlane_b32 %[tgt], v201\n\t" \
  "s_cbranch_scc1 .Lgo%=\n\t" \
  "s_cmp_eq_u32 s93, %[step]\n\t" \
  "s_cbranch_scc1 .Lwx%=\n\t" \
  "s_sleep 1\n\t" \
  "s_branch .Lwt%=\n\t" \
  ".Lwx%=:\n\t" \
  "s_lshl_b32 s93, s90, 2\n\t" \
  "s_sub_i32 s93, %[tgt], s93\n\t" \
  "s_cmp_le_i32 s93, 0\n\t" \
  "s_cbranch_scc1 .Lgo%=\n\t" \
  "s_min_i32 s93, s93, 8000\n\t" \
  "s_mov_b32 s94, s93\n\t" \
  ".Lsl%=:\n\t" \
  "s_sub_i32 s94, s94, 18\n\t" \
  "s_cmp_lt_i32 s94, 30\n\t" \
  "s_cbranch_scc1 .Lsx%=\n\t" \
  "s_sleep 1\n\t" \
  "s_branch .Lsl%=\n\t" \
  ".Lsx%=:\n\t" \
  "s_sub_i32 s93, s93, 12\n\t" \
  "s_max_i32 s93, s93, 4\n\t" \
  "s_lshr_b32 s93, s93, 2\n\t" \
  "s_add_u32 s93, s93, s90\n\t" \
  "s_memrealtime s[90:91]\n\t" \
  ".Lpace%=:\n\t" \
  "s_memrealtime s[94:95]\n\t" \
  "s_waitcnt lgkmcnt(1)\n\t" \
  "s_sub_i32 s92, s90, s93\n\t" \
  "s_cmp_ge_i32 s92, 0\n\t" \
  "s_cbranch_scc1 .Le%=\n\t" \
  "s_memrealtime s[90:91]\n\t" \
  "s_waitcnt lgkmcnt(1)\n\t" \
  "s_sub_i32 s92, s94, s93\n\t" \
  "s_cmp_lt_i32 s92, 0\n\t" \
  "s_cbranch_scc1 .Lpace%=\n\t" \
  "s_nop 4\n\t" \
  ".Le%=:\n\t" \
  "s_add_u32 s92, s92, s93\n\t" \
  "s_lshl_b32 s92, s92, 2\n\t" \
  "s_sub_i32 s92, %[tgt], s92\n\t" \
  "s_max_i32 s92, s92, 0\n\t" \
  "s_bitcmp0_b32 s92, 3\n\t" \
  "s_cbranch_scc1 .Lf3%=\n\t" \
  "s_nop 15\n\t" \
  ".Lf3%=:\n\t" \
  "s_bitcmp0_b32 s92, 2\n\t" \
  "s_cbranch_scc1 .Lf2%=\n\t" \
  "s_nop 10\n\t" \
  ".Lf2%=:\n\t" \
  ".Lgo%=:\n\t" \
  "s_mov_b32 s92, 0\n\t" \
  ".Lpoll%=:\n\t" \
  "buffer_load_dwordx4 v[200:203], v248, %[rs], %[som] offen sc1\n\t" \
  "buffer_load_dwordx4 v[204:207], v248, %[rs], %[som2] offen sc1\n\t" \
  ".p2align 5\n\t" \
  "s_waitcnt vmcnt(1) lgkmcnt(0)\n\t" \
  "ds_write_b128 v249, v[200:203]\n\t" \
  "v_smfmac_f32_16x16x64_bf16 v[192:195], v[200:203], v[208:215], v251\n\t" \
  "v_max3_u32 v244, v200, v201, v202\n\t" \
  "v_smfmac_f32_16x16x64_bf16 v[196:199], v[200:203], v[236:243], v251\n\t" \
  "s_waitcnt vmcnt(0)\n\t" \
  "ds_write_b128 v249, v[204:207] offset:8192\n\t" \
  "v_smfmac_f32_16x16x64_bf16 v[192:195], v[204:207], v[216:223], v251\n\t" \
  "v_max3_u32 v244, v244, v203, v204\n\t" \
  "v_max3_u32 v244, v244, v205, v206\n\t" \
  "v_smfmac_f32_16x16x64_bf16 v[196:199], v[204:207], v[224:231], v251\n\t" \
  "v_max_u32 v244, v244, v207\n\t" \
  "v_cmp_le_u32_e64 vcc, %[sent], v244\n\t" \
  "s_cbranch_vccnz .Lslow%=\n\t" \
  ".Lok%=:\n\t" \
  "ds_read_b128 v[216:219], v250 offset:32768\n\t" \
  "ds_read_b128 v[220:223], v250 offset:33792\n\t" \
  "ds_read_b128 v[224:227], v250 offset:34816\n\t" \
  "ds_read_b128 v[228:231], v250 offset:35840\n\t" \
  "s_waitcnt lgkmcnt(4)\n\t" \
  "s_barrier\n\t" \
  "ds_read_b128 v[208:211], v248 offset:2048\n\t" \
  "ds_read_b128 v[212:215], v248 offset:5120\n\t" \
  "ds_read_b128 v[236:239], v248 offset:6144\n\t" \
  "ds_read_b128 v[240:243], v248 offset:7168\n\t" \
  "s_cmp_eq_u32 %[step], 63\n\t" \
  "s_cbranch_scc1 .Lls%=\n\t" \
  "s_waitcnt lgkmcnt(3)\n\t" \
  "ds_read_b128 v[244:247], v248 offset:8192\n\t" \
  "v_smfmac_f32_16x16x64_bf16 v[192:195], v[208:211], v[216:223], v251\n\t" \
  "ds_read_b128 v[200:203], v248 offset:12288\n\t" \
  "v_smfmac_f32_16x16x64_bf16 v[196:199], v[208:211], v[224:231], v251\n\t" \
  "s_waitcnt lgkmcnt(4)\n\t" \
  "ds_read_b128 v[216:219], v250 offset:0\n\t" \
  "v_smfmac_f32_16x16x64_bf16 v[192:195], v[212:215], v[0:7], v251\n\t" \
  "ds_read_b128 v[220:223], v250 offset:1024\n\t" \
  "v_smfmac_f32_16x16x64_bf16 v[196:199], v[212:215], v[8:15], v251\n\t" \
  "s_waitcnt lgkmcnt(5)\n\t" \
  "ds_read_b128 v[224:227], v250 offset:2048\n\t" \
  "v_smfmac_f32_16x16x64_bf16 v[192:195], v[236:239], v[16:23], v251\n\t" \
  "ds_read_b128 v[228:231], v250 offset:3072\n\t" \
  "v_smfmac_f32_16x16x64_bf16 v[196:199], v[236:239], v[24:31], v251\n\t" \
  "s_waitcnt lgkmcnt(5)\n\t" \
  "ds_read_b128 v[204:207], v248 offset:9216\n\t" \
  "v_smfmac_f32_16x16x64_bf16 v[192:195], v[240:243], v[32:39], v251\n\t" \
  "ds_read_b128 v[208:211], v248 offset:10240\n\t" \
  "v_smfmac_f32_16x16x64_bf16 v[196:199], v[240:243], v[40:47], v251\n\t" \
  "ds_read_b128 v[212:215], v248 offset:13312\n\t" \
  "v_smfmac_f32_16x16x64_bf16 v[192:195], v[244:247], v[48:55], v251\n\t" \
  "ds_read_b128 v[236:239], v248 offset:14336\n\t" \
  "v_smfmac_f32_16x16x64_bf16 v[196:199], v[244:247], v[56:63], v251\n\t" \
  "s_waitcnt lgkmcnt(6)\n\t" \
  "v_smfmac_f32_16x16x64_bf16 v[192:195], v[200:203], v[216:223], v251\n\t" \
  "s_waitcnt lgkmcnt(4)\n\t" \
  "v_smfmac_f32_16x16x64_bf16 v[196:199], v[200:203], v[224:231], v251\n\t" \
  "s_waitcnt lgkmcnt(3)\n\t" \
  "ds_read_b128 v[240:243], v248 offset:15360\n\t" \
  "v_smfmac_f32_16x16x64_bf16 v[192:195], v[204:207], v[64:71], v251\n\t" \
  "ds_read_b128 v[244:247], v248 offset:0\n\t" \
  "v_smfmac_f32_16x16x64_bf16 v[196:199], v[204:207], v[72:79], v251\n\t" \
  "s_waitcnt lgkmcnt(3)\n\t" \
  "v_smfmac_f32_16x16x64_bf16 v[192:195], v[208:211], v[80:87], v251\n\t" \
  "ds_read_b128 v[200:203], v248 offset:1024\n\t" \
  "v_smfmac_f32_16x16x64_bf16 v[196:199], v[208:211], v[88:95], v251\n\t" \
  "ds_read_b128 v[204:207], v248 offset:4096\n\t" \
  "v_smfmac_f32_16x16x64_bf16 v[192:195], v[212:215], v[96:103], v251\n\t" \
  "v_smfmac_f32_16x16x64_bf16 v[196:199], v[212:215], v[104:111], v251\n\t" \
  "s_waitcnt lgkmcnt(3)\n\t" \
  "v_smfmac_f32_16x16x64_bf16 v[192:195], v[236:239], v[112:119], v251\n\t" \
  "v_smfmac_f32_16x16x64_bf16 v[196:199], v[236:239], v[120:127], v251\n\t" \
  "v_smfmac_f32_16x16x64_bf16 v[192:195], v[240:243], v[128:135], v251\n\t" \
  "v_smfmac_f32_16x16x64_bf16 v[196:199], v[240:243], v[136:143], v251\n\t" \
  "s_waitcnt lgkmcnt(0)\n\t" \
  "v_smfmac_f32_16x16x64_bf16 v[192:195], v[244:247], v[144:151], v251\n\t" \
  "v_smfmac_f32_16x16x64_bf16 v[196:199], v[244:247], v[152:159], v251\n\t" \
  "v_smfmac_f32_16x16x64_bf16 v[192:195], v[200:203], v[160:167], v251\n\t" \
  "v_smfmac_f32_16x16x64_bf16 v[196:199], v[200:203], v[168:175], v251\n\t" \
  "v_smfmac_f32_16x16x64_bf16 v[192:195], v[204:207], v[176:183], v251\n\t" \
  "v_smfmac_f32_16x16x64_bf16 v[196:199], v[204:207], v[184:191], v251\n\t" \
  "ds_read_b128 v[200:203], v248 offset:19456\n\t" \
  "ds_read_b128 v[204:207], v248 offset:23552\n\t" \
  "v_mov_b32 v250, v232\n\t" \
  "v_mov_b32 v249, v233\n\t" \
  "s_nop 2\n\t" \
  "v_add_f32 v192, v192, v194\n\t" \
  "v_add_f32 v193, v193, v195\n\t" \
  "v_add_f32 v196, v196, v198\n\t" \
  "v_add_f32_e64 v197, v197, v199\n\t" \
  "v_cvt_pk_bf16_f32 v192, v192, v193\n\t" \
  "v_cvt_pk_bf16_f32 v196, v196, v197\n\t" \
  "ds_write2_b32 v252, v192, v196 offset1:64\n\t" \
  "v_mov_b32_e64 v251, v234\n\t" \
  "ds_read_b64_tr_b16 v[244:245], v250 offset:1536\n\t" \
  "ds_read_b64_tr_b16 v[246:247], v250 offset:1600\n\t" \
  "v_mov_b32_e64 v212, %[step]\n\t" \
  "s_mov_b64 exec, 1\n\t" \
  "ds_write_b32 v248, v212 offset:28720\n\t" \
  "s_mov_b64 exec, -1\n\t" \
  "buffer_load_dwordx4 v[192:195], v248, %[rt], %[svt] offen offset:0 sc1\n\t" \
  "buffer_load_dwordx4 v[196:199], v248, %[rt], %[svt] offen offset:1024 sc1\n\t" \
  "s_waitcnt lgkmcnt(1)\n\t" \
  "v_mfma_f32_16x16x32_bf16 v[200:203], v[200:203], v[244:247], 0\n\t" \
  "v_mfma_f32_16x16x32_bf16 v[204:207], v[204:207], v[244:247], 0\n\t" \
  "s_nop 7\n\t" \
  "v_cndmask_b32 v216, v200, v204, %[nh]\n\t" \
  "v_cndmask_b32 v217, v201, v205, %[nh]\n\t" \
  "v_cndmask_b32 v218, v202, v206, %[nh]\n\t" \
  "v_cndmask_b32 v219, v203, v207, %[nh]\n\t" \
  "ds_write_b128 v248, v[216:219] offset:19456\n\t" \
  "buffer_load_dword v252, v249, %[rs], %[sv] offen offset:0 sc1\n\t" \
  "buffer_load_dword v253, v249, %[rs], %[sv] offen offset:256 sc1\n\t" \
  "ds_read_b32 v212, v248 offset:28672\n\t" \
  "ds_read_b64_tr_b16 v[232:233], v250 offset:0\n\t" \
  "ds_read_b64_tr_b16 v[234:235], v250 offset:64\n\t" \
  "ds_read_b64_tr_b16 v[236:237], v250 offset:512\n\t" \
  "ds_read_b64_tr_b16 v[238:239], v250 offset:576\n\t" \
  "ds_read_b64_tr_b16 v[240:241], v250 offset:1024\n\t" \
  "ds_read_b64_tr_b16 v[242:243], v250 offset:1088\n\t" \
  "s_setprio 3\n\t" \
  "s_waitcnt lgkmcnt(0)\n\t" \
  "v_cmp_ne_u32 vcc, %[step], v212\n\t" \
  "s_and_b32 s93, vcc_lo, 0xf\n\t" \
  "s_barrier\n\t" \
  "s_cbranch_scc1 .Lre%=\n\t" \
  ".Lrb%=:\n\t" \
  "ds_read_b64_tr_b16 v[212:213], v250 offset:3584\n\t" \
  "ds_read_b64_tr_b16 v[214:215], v250 offset:3648\n\t" \
  "ds_read_b64_tr_b16 v[200:201], v250 offset:2048\n\t" \
  "ds_read_b64_tr_b16 v[202:203], v250 offset:2112\n\t" \
  "ds_read_b64_tr_b16 v[204:205], v250 offset:2560\n\t" \
  "ds_read_b64_tr_b16 v[206:207], v250 offset:2624\n\t" \
  "ds_read_b64_tr_b16 v[208:209], v250 offset:3072\n\t" \
  "ds_read_b64_tr_b16 v[210:211], v250 offset:3136\n\t" \
  "ds_read_b128 v[220:223], v248 offset:16384\n\t" \
  "ds_read_b128 v[224:227], v248 offset:17408\n\t" \
  "ds_read_b128 v[228:231], v248 offset:18432\n\t" \
  "s_waitcnt lgkmcnt(9)\n\t" \
  "v_mfma_f32_16x16x32_bf16 v[212:215], v[212:215], v[244:247], 0\n\t" \
  "s_waitcnt lgkmcnt(7)\n\t" \
  "v_mfma_f32_16x16x32_bf16 v[212:215], v[200:203], v[232:235], v[212:215]\n\t" \
  "v_mov_b32 v245, 0\n\t" \
  "s_waitcnt lgkmcnt(5)\n\t" \
  "v_mfma_f32_16x16x32_bf16 v[212:215], v[204:207], v[236:239], v[212:215]\n\t" \
  "v_mov_b32 v246, 0\n\t" \
  "s_waitcnt lgkmcnt(3)\n\t" \
  "v_mfma_f32_16x16x32_bf16 v[212:215], v[208:211], v[240:243], v[212:215]\n\t" \
  "s_waitcnt lgkmcnt(2)\n\t" \
  "v_pk_add_f32 v[216:217], v[216:217], v[220:221]\n\t" \
  "v_pk_add_f32 v[218:219], v[218:219], v[222:223]\n\t" \
  "s_waitcnt lgkmcnt(0)\n\t" \
  "v_pk_add_f32 v[224:225], v[224:225], v[228:229]\n\t" \
  "v_pk_add_f32 v[226:227], v[226:227], v[230:231]\n\t" \
  "v_pk_add_f32 v[216:217], v[216:217], v[224:225]\n\t" \
  "v_pk_add_f32 v[218:219], v[218:219], v[226:227]\n\t" \
  "v_max3_f32 v224, v216, v217, v218\n\t" \
  "v_max_f32 v224, v224, v219\n\t" \
  "v_max3_f32 v231, v212, v213, v214\n\t" \
  "v_max_f32 v231, v231, v215\n\t" \
  "v_max_f32_dpp v225, v224, v224 row_ror:8 row_mask:0xf bank_mask:0xf\n\t" \
  "v_max_f32_dpp v226, v224, v224 row_ror:8 row_mask:0xf bank_mask:0xf\n\t" \
  "v_cndmask_b32 v212, v212, v214, %[up]\n\t" \
  "v_cndmask_b32 v213, v213, v215, %[up]\n\t" \
  "v_permlane32_swap_b32_e64 v225, v226\n\t" \
  "v_max3_f32 v201, v225, v226, v231\n\t" \
  "v_max3_f32 v202, v225, v226, v231\n\t" \
  "v_mov_b64 v[220:221], %[one]\n\t" \
  "v_mov_b64 v[222:223], %[one]\n\t" \
  "v_permlane16_swap_b32 v201, v202\n\t" \
  "v_max_f32 v200, v201, v202\n\t" \
  "v_mul_f32_e64 v200, %[negc], v200\n\t" \
  "v_fma_f32 v212, v212, %[c], v200\n\t" \
  "v_fma_f32 v213, v213, %[c], v200\n\t" \
  "v_exp_f32 v212, v212\n\t" \
  "v_fma_f32 v216, v216, %[c], v200\n\t" \
  "v_exp_f32 v213, v213\n\t" \
  "v_fma_f32 v217, v217, %[c], v200\n\t" \
  "v_exp_f32_e64 v216, v216\n\t" \
  "v_fma_f32 v218, v218, %[c], v200\n\t" \
  "v_cvt_pk_bf16_f32 v244, v212, v213\n\t" \
  "v_cvt_pk_bf16_f32 v247, v212, v213\n\t" \
  "v_exp_f32 v217, v217\n\t" \
  "v_fma_f32 v219, v219, %[c], v200\n\t" \
  "v_mfma_f32_16x16x16_bf16 v[208:211], v[220:221], v[244:245], 0\n\t" \
  "v_exp_f32 v218, v218\n\t" \
  "s_waitcnt vmcnt(0)\n\t" \
  "v_mfma_f32_16x16x16_bf16 v[224:227], v[252:253], v[244:245], 0\n\t" \
  "v_cvt_pk_bf16_f32 v206, v216, v217\n\t" \
  "v_cvt_pk_bf16_f32 v204, v216, v217\n\t" \
  "v_exp_f32 v219, v219\n\t" \
  "v_mfma_f32_16x16x16_bf16 v[228:231], v[252:253], v[246:247], 0\n\t" \
  "v_cvt_pk_bf16_f32 v207, v218, v219\n\t" \
  "v_cvt_pk_bf16_f32 v205, v218, v219\n\t" \
  "v_mov_b32_dpp v204, v206 row_ror:8 row_mask:0xf bank_mask:0xc\n\t" \
  "v_mov_b32_dpp v206, v206 row_ror:8 row_mask:0xf bank_mask:0x3\n\t" \
  "v_mov_b32_dpp v205, v207 row_ror:8 row_mask:0xf bank_mask:0xc\n\t" \
  "v_mov_b32_dpp v207, v207 row_ror:8 row_mask:0xf bank_mask:0x3\n\t" \
  "v_max_u32 v200, v252, v253\n\t" \
  "v_cmp_le_u32 vcc, %[sent], v200\n\t" \
  "v_mfma_f32_16x16x32_bf16 v[208:211], v[220:223], v[204:207], v[208:211]\n\t" \
  ".Lpv%=:\n\t" \
  "v_mfma_f32_16x16x32_bf16 v[224:227], v[192:195], v[204:207], v[224:227]\n\t" \
  "v_mfma_f32_16x16x32_bf16 v[228:231], v[196:199], v[204:207], v[228:231]\n\t" \
  "s_cbranch_vccnz .Lvs%=\n\t" \
  ".Lvok%=:\n\t" \
  "s_nop 4\n\t" \
  "v_rcp_f32_e64 v200, v208\n\t" \
  "v_cndmask_b32 v224, v224, v226, %[nh]\n\t" \
  "v_cndmask_b32 v225, v225, v227, %[nh]\n\t" \
  "v_cndmask_b32 v228, v228, v230, %[nh]\n\t" \
  "v_cndmask_b32 v229, v229, v231, %[nh]\n\t" \
  "v_pk_mul_f32 v[224:225], v[224:225], v[200:201] op_sel_hi:[1,0]\n\t" \
  "v_pk_mul_f32 v[228:229], v[228:229], v[200:201] op_sel_hi:[1,0]\n\t" \
  "v_cvt_pk_bf16_f32 v224, v224, v225\n\t" \
  "v_cvt_pk_bf16_f32 v225, v228, v229\n\t" \
  "buffer_store_dwordx2 v[224:225], v251, %[rs], %[so] offen sc1\n\t" \
  "s_memtime s[90:91]\n\t" \
  "s_setprio 0\n\t" \
  "s_waitcnt lgkmcnt(0)\n\t" \
  "s_mov_b32 %[tout], s90\n\t" \
  "s_mov_b32 %[spins], s92\n\t" \
  "s_branch .Lend%=\n\t" \
  ".Lls%=:\n\t" \
  "s_waitcnt lgkmcnt(0)\n\t" \
  "s_barrier\n\t" \
  "s_mov_b32 %[spins], 0\n\t" \
  "s_branch .Lend%=\n\t" \
  ".Lre%=:\n\t" \
  "ds_read_b64_tr_b16 v[232:233], v250 offset:0\n\t" \
  "ds_read_b64_tr_b16 v[234:235], v250 offset:64\n\t" \
  "ds_read_b64_tr_b16 v[236:237], v250 offset:512\n\t" \
  "ds_read_b64_tr_b16 v[238:239], v250 offset:576\n\t" \
  "ds_read_b64_tr_b16 v[240:241], v250 offset:1024\n\t" \
  "ds_read_b64_tr_b16 v[242:243], v250 offset:1088\n\t" \
  "s_waitcnt lgkmcnt(0)\n\t" \
  "s_branch .Lrb%=\n\t" \
  ".Lvs%=:\n\t" \
  "v_cmp_eq_u32 vcc, %[sent], v252\n\t" \
  "v_cmp_eq_u32 s[94:95], %[sent], v253\n\t" \
  "s_or_b64 s[94:95], s[94:95], vcc\n\t" \
  "s_cbranch_scc0 .Lvok%=\n\t" \
  "s_add_u32 s92, s92, 1\n\t" \
  "s_sleep 2\n\t" \
  "s_and_b32 s93, s92, 63\n\t" \
  "s_cbranch_scc1 .Lvr%=\n\t" \
  "buffer_load_dword v200, off, %[rs], %[sab] sc1\n\t" \
  "s_waitcnt vmcnt(0)\n\t" \
  "v_readfirstlane_b32 s93, v200\n\t" \
  "s_nop 3\n\t" \
  "s_cmp_eq_u32 s93, 1\n\t" \
  "s_cbranch_scc1 .Lvok%=\n\t" \
  "s_cmp_lt_u32 s92, 0x80000\n\t" \
  "s_cbranch_scc1 .Lvr%=\n\t" \
  "v_mov_b32 v200, 1\n\t" \
  "buffer_store_dword v200, off, %[rs], %[sab] sc1\n\t" \
  "s_branch .Lvok%=\n\t" \
  ".Lvr%=:\n\t" \
  "buffer_load_dword v252, v249, %[rs], %[sv] offen offset:0 sc1\n\t" \
  "buffer_load_dword v253, v249, %[rs], %[sv] offen offset:256 sc1\n\t" \
  "s_waitcnt vmcnt(0)\n\t" \
  "v_mfma_f32_16x16x16_bf16 v[224:227], v[252:253], v[244:245], 0\n\t" \
  "v_mfma_f32_16x16x16_bf16 v[228:231], v[252:253], v[246:247], 0\n\t" \
  "v_max_u32 v200, v252, v253\n\t" \
  "v_cmp_le_u32 vcc, %[sent], v200\n\t" \
  "s_nop 4\n\t" \
  "s_branch .Lpv%=\n\t" \
  "s_branch .Lend%=\n\t" \
  ".Lslow%=:\n\t" \
  "s_mov_b64 s[94:95], 0\n\t" \
  "v_cmp_eq_u32 vcc, %[sent], v200\n\t" \
  "s_or_b64 s[94:95], s[94:95], vcc\n\t" \
  "v_cmp_eq_u32 vcc, %[sent], v201\n\t" \
  "s_or_b64 s[94:95], s[94:95], vcc\n\t" \
  "v_cmp_eq_u32 vcc, %[sent], v202\n\t" \
  "s_or_b64 s[94:95], s[94:95], vcc\n\t" \
  "v_cmp_eq_u32 vcc, %[sent], v203\n\t" \
  "s_or_b64 s[94:95], s[94:95], vcc\n\t" \
  "v_cmp_eq_u32 vcc, %[sent], v204\n\t" \
  "s_or_b64 s[94:95], s[94:95], vcc\n\t" \
  "v_cmp_eq_u32 vcc, %[sent], v205\n\t" \
  "s_or_b64 s[94:95], s[94:95], vcc\n\t" \
  "v_cmp_eq_u32 vcc, %[sent], v206\n\t" \
  "s_or_b64 s[94:95], s[94:95], vcc\n\t" \
  "v_cmp_eq_u32 vcc, %[sent], v207\n\t" \
  "s_or_b64 s[94:95], s[94:95], vcc\n\t" \
  "s_cmp_eq_u64 s[94:95], 0\n\t" \
  "s_cbranch_scc1 .Lok%=\n\t" \
  "v_mov_b64 v[192:193], 0\n\t" \
  "v_mov_b64 v[194:195], 0\n\t" \
  "v_mov_b64 v[196:197], 0\n\t" \
  "v_mov_b64 v[198:199], 0\n\t" \
  "s_add_u32 s92, s92, 1\n\t" \
  "s_cmp_eq_u32 s92, 1\n\t" \
  "s_cbranch_scc1 .Lpoll%=\n\t" \
  "s_sleep 2\n\t" \
  "s_and_b32 s93, s92, 63\n\t" \
  "s_cmp_lg_u32 s93, 0\n\t" \
  "s_cbranch_scc1 .Lpoll%=\n\t" \
  "v_mov_b32 v245, 0\n\t" \
  "buffer_load_dword v244, v245, %[rs], %[sab] offen sc1\n\t" \
  "s_waitcnt vmcnt(0)\n\t" \
  "v_readfirstlane_b32 s93, v244\n\t" \
  "s_nop 3\n\t" \
  "s_cmp_eq_u32 s93, 1\n\t" \
  "s_cbranch_scc1 .Lok%=\n\t" \
  "s_cmp_lt_u32 s92, 0x80000\n\t" \
  "s_cbranch_scc1 .Lpoll%=\n\t" \
  "v_mov_b32 v244, 1\n\t" \
  "buffer_store_dword v244, v245, %[rs], %[sab] offen sc1\n\t" \
  "s_branch .Lok%=\n\t" \
  ".Lend%=:\n\t"
#define QK_ASM_4 \
  "s_waitcnt lgkmcnt(0)\n\t" \
  "v_subrev_u32 v244, 0x8000, v250\n\t" \
  "ds_read_b128 v[216:219], v248 offset:49152\n\t" \
  "ds_read_b128 v[220:223], v248 offset:50176\n\t" \
  "ds_read_b128 v[224:227], v248 offset:51200\n\t" \
  "ds_read_b128 v[228:231], v248 offset:52224\n\t" \
  "ds_read_b128 v[208:211], v244 offset:0\n\t" \
  "ds_read_b128 v[212:215], v244 offset:1024\n\t" \
  "ds_read_b128 v[236:239], v244 offset:2048\n\t" \
  "ds_read_b128 v[240:243], v244 offset:3072\n\t" \
  "v_mov_b64 v[192:193], 0\n\t" \
  "v_mov_b64 v[194:195], 0\n\t" \
  "v_mov_b64 v[196:197], 0\n\t" \
  "v_mov_b64 v[198:199], 0\n\t" \
  ".p2align 5\n\t" \
  "s_nop 0\n\t" \
  "s_nop 0\n\t" \
  "s_nop 0\n\t" \
  "s_nop 0\n\t" \
  "s_movk_i32 s92, 0x2000\n\t" \
  ".Lwt%=:\n\t" \
  "ds_read_b64 v[200:201], v248 offset:30720\n\t" \
  "s_memrealtime s[90:91]\n\t" \
  "s_sub_u32 s92, s92, 1\n\t" \
  "s_waitcnt lgkmcnt(0)\n\t" \
  "v_readfirstlane_b32 s93, v200\n\t" \
  "v_readfirstlane_b32 %[tgt], v201\n\t" \
  "s_cbranch_scc1 .Lgo%=\n\t" \
  "s_cmp_eq_u32 s93, %[step]\n\t" \
  "s_cbranch_scc1 .Lwx%=\n\t" \
  "s_sleep 1\n\t" \
  "s_branch .Lwt%=\n\t" \
  ".Lwx%=:\n\t" \
  "s_lshl_b32 s93, s90, 2\n\t" \
  "s_sub_i32 s93, %[tgt], s93\n\t" \
  "s_cmp_le_i32 s93, 0\n\t" \
  "s_cbranch_scc1 .Lgo%=\n\t" \
  "s_min_i32 s93, s93, 8000\n\t" \
  "s_mov_b32 s94, s93\n\t" \
  ".Lsl%=:\n\t" \
  "s_sub_i32 s94, s94, 18\n\t" \
  "s_cmp_lt_i32 s94, 30\n\t" \
  "s_cbranch_scc1 .Lsx%=\n\t" \
  "s_sleep 1\n\t" \
  "s_branch .Lsl%=\n\t" \
  ".Lsx%=:\n\t" \
  "s_sub_i32 s93, s93, 12\n\t" \
  "s_max_i32 s93, s93, 4\n\t" \
  "s_lshr_b32 s93, s93, 2\n\t" \
  "s_add_u32 s93, s93, s90\n\t" \
  "s_memrealtime s[90:91]\n\t" \
  ".Lpace%=:\n\t" \
  "s_memrealtime s[94:95]\n\t" \
  "s_waitcnt lgkmcnt(1)\n\t" \
  "s_sub_i32 s92, s90, s93\n\t" \
  "s_cmp_ge_i32 s92, 0\n\t" \
  "s_cbranch_scc1 .Le%=\n\t" \
  "s_memrealtime s[90:91]\n\t" \
  "s_waitcnt lgkmcnt(1)\n\t" \
  "s_sub_i32 s92, s94, s93\n\t" \
  "s_cmp_lt_i32 s92, 0\n\t" \
  "s_cbranch_scc1 .Lpace%=\n\t" \
  "s_nop 4\n\t" \
  ".Le%=:\n\t" \
  "s_add_u32 s92, s92, s93\n\t" \
  "s_lshl_b32 s92, s92, 2\n\t" \
  "s_sub_i32 s92, %[tgt], s92\n\t" \
  "s_max_i32 s92, s92, 0\n\t" \
  "s_bitcmp0_b32 s92, 3\n\t" \
  "s_cbranch_scc1 .Lf3%=\n\t" \
  "s_nop 15\n\t" \
  ".Lf3%=:\n\t" \
  "s_bitcmp0_b32 s92, 2\n\t" \
  "s_cbranch_scc1 .Lf2%=\n\t" \
  "s_nop 10\n\t" \
  ".Lf2%=:\n\t" \
  ".Lgo%=:\n\t" \
  "s_mov_b32 s92, 0\n\t" \
  ".Lpoll%=:\n\t" \
  "buffer_load_dwordx4 v[200:203], v248, %[rs], %[som] offen sc1\n\t" \
  "buffer_load_dwordx4 v[204:207], v248, %[rs], %[som2] offen sc1\n\t" \
  ".p2align 5\n\t" \
  "s_nop 0\n\t" \
  "s_nop 0\n\t" \
  "s_nop 0\n\t" \
  "s_nop 0\n\t" \
  "s_nop 0\n\t" \
  "s_nop 0\n\t" \
  "s_nop 0\n\t" \
  "s_waitcnt vmcnt(1) lgkmcnt(0)\n\t" \
  "ds_write_b128 v249, v[200:203]\n\t" \
  "v_smfmac_f32_16x16x64_bf16 v[192:195], v[200:203], v[208:215], v251\n\t" \
  "v_max3_u32 v244, v200, v201, v202\n\t" \
  "v_smfmac_f32_16x16x64_bf16 v[196:199], v[200:203], v[236:243], v251\n\t" \
  "s_waitcnt vmcnt(0)\n\t" \
  "ds_write_b128 v249, v[204:207] offset:8192\n\t" \
  "v_smfmac_f32_16x16x64_bf16 v[192:195], v[204:207], v[216:223], v251\n\t" \
  "v_max3_u32 v244, v244, v203, v204\n\t" \
  "v_max3_u32 v244, v244, v205, v206\n\t" \
  "v_smfmac_f32_16x16x64_bf16 v[196:199], v[204:207], v[224:231], v251\n\t" \
  "v_max_u32 v244, v244, v207\n\t" \
  "v_cmp_le_u32 vcc, %[sent], v244\n\t" \
  "s_cbranch_vccnz .Lslow%=\n\t" \
  ".Lok%=:\n\t" \
  "s_waitcnt lgkmcnt(0)\n\t" \
  "s_barrier\n\t" \
  "ds_read_b128 v[208:211], v248 offset:6144\n\t" \
  "ds_read_b128 v[212:215], v248 offset:7168\n\t" \
  "ds_read_b128 v[236:239], v248 offset:8192\n\t" \
  "ds_read_b128 v[240:243], v248 offset:9216\n\t" \
  "ds_read_b128 v[244:247], v248 offset:10240\n\t" \
  "s_waitcnt lgkmcnt(4)\n\t" \
  "ds_read_b128 v[200:203], v248 offset:5120\n\t" \
  "v_smfmac_f32_16x16x64_bf16 v[192:195], v[208:211], v[0:7], v251\n\t" \
  "ds_read_b128 v[216:219], v250 offset:32768\n\t" \
  "v_smfmac_f32_16x16x64_bf16 v[196:199], v[208:211], v[8:15], v251\n\t" \
  "s_waitcnt lgkmcnt(5)\n\t" \
  "ds_read_b128 v[220:223], v250 offset:33792\n\t" \
  "v_smfmac_f32_16x16x64_bf16 v[192:195], v[212:215], v[16:23], v251\n\t" \
  "ds_read_b128 v[224:227], v250 offset:34816\n\t" \
  "v_smfmac_f32_16x16x64_bf16 v[196:199], v[212:215], v[24:31], v251\n\t" \
  "s_waitcnt lgkmcnt(6)\n\t" \
  "ds_read_b128 v[228:231], v250 offset:35840\n\t" \
  "v_smfmac_f32_16x16x64_bf16 v[192:195], v[236:239], v[32:39], v251\n\t" \
  "ds_read_b128 v[204:207], v248 offset:11264\n\t" \
  "v_smfmac_f32_16x16x64_bf16 v[196:199], v[236:239], v[40:47], v251\n\t" \
  "s_waitcnt lgkmcnt(7)\n\t" \
  "ds_read_b128 v[208:211], v248 offset:14336\n\t" \
  "v_smfmac_f32_16x16x64_bf16 v[192:195], v[240:243], v[48:55], v251\n\t" \
  "ds_read_b128 v[212:215], v248 offset:15360\n\t" \
  "v_smfmac_f32_16x16x64_bf16 v[196:199], v[240:243], v[56:63], v251\n\t" \
  "s_waitcnt lgkmcnt(7)\n\t" \
  "v_smfmac_f32_16x16x64_bf16 v[192:195], v[244:247], v[64:71], v251\n\t" \
  "ds_read_b128 v[236:239], v248 offset:0\n\t" \
  "v_smfmac_f32_16x16x64_bf16 v[196:199], v[244:247], v[72:79], v251\n\t" \
  "s_waitcnt lgkmcnt(6)\n\t" \
  "v_smfmac_f32_16x16x64_bf16 v[192:195], v[200:203], v[216:223], v251\n\t" \
  "s_waitcnt lgkmcnt(4)\n\t" \
  "ds_read_b128 v[240:243], v248 offset:13312\n\t" \
  "v_smfmac_f32_16x16x64_bf16 v[196:199], v[200:203], v[224:231], v251\n\t" \
  "s_waitcnt lgkmcnt(4)\n\t" \
  "ds_read_b128 v[216:219], v250 offset:0\n\t" \
  "v_smfmac_f32_16x16x64_bf16 v[192:195], v[204:207], v[80:87], v251\n\t" \
  "ds_read_b128 v[220:223], v250 offset:1024\n\t" \
  "v_smfmac_f32_16x16x64_bf16 v[196:199], v[204:207], v[88:95], v251\n\t" \
  "s_waitcnt lgkmcnt(4)\n\t" \
  "ds_read_b128 v[224:227], v250 offset:2048\n\t" \
  "v_smfmac_f32_16x16x64_bf16 v[192:195], v[208:211], v[96:103], v251\n\t" \
  "ds_read_b128 v[228:231], v250 offset:3072\n\t" \
  "v_smfmac_f32_16x16x64_bf16 v[196:199], v[208:211], v[104:111], v251\n\t" \
  "ds_read_b128 v[244:247], v248 offset:1024\n\t" \
  "v_smfmac_f32_16x16x64_bf16 v[192:195], v[212:215], v[112:119], v251\n\t" \
  "ds_read_b128 v[200:203], v248 offset:2048\n\t" \
  "v_smfmac_f32_16x16x64_bf16 v[196:199], v[212:215], v[120:127], v251\n\t" \
  "s_waitcnt lgkmcnt(6)\n\t" \
  "v_smfmac_f32_16x16x64_bf16 v[192:195], v[236:239], v[128:135], v251\n\t" \
  "ds_read_b128 v[204:207], v248 offset:3072\n\t" \
  "v_smfmac_f32_16x16x64_bf16 v[196:199], v[236:239], v[136:143], v251\n\t" \
  "s_waitcnt lgkmcnt(5)\n\t" \
  "v_smfmac_f32_16x16x64_bf16 v[192:195], v[240:243], v[216:223], v251\n\t" \
  "s_waitcnt lgkmcnt(3)\n\t" \
  "v_smfmac_f32_16x16x64_bf16 v[196:199], v[240:243], v[224:231], v251\n\t" \
  "s_waitcnt lgkmcnt(2)\n\t" \
  "v_smfmac_f32_16x16x64_bf16 v[192:195], v[244:247], v[144:151], v251\n\t" \
  "v_smfmac_f32_16x16x64_bf16 v[196:199], v[244:247], v[152:159], v251\n\t" \
  "s_waitcnt lgkmcnt(1)\n\t" \
  "v_smfmac_f32_16x16x64_bf16 v[192:195], v[200:203], v[160:167], v251\n\t" \
  "v_smfmac_f32_16x16x64_bf16 v[196:199], v[200:203], v[168:175], v251\n\t" \
  "s_waitcnt lgkmcnt(0)\n\t" \
  "v_smfmac_f32_16x16x64_bf16 v[192:195], v[204:207], v[176:183], v251\n\t" \
  "v_smfmac_f32_16x16x64_bf16 v[196:199], v[204:207], v[184:191], v251\n\t" \
  "s_nop 6\n\t" \
  "v_pk_add_f32 v[192:193], v[192:193], v[194:195]\n\t" \
  "v_pk_add_f32 v[196:197], v[196:197], v[198:199]\n\t" \
  "v_cvt_pk_bf16_f32 v192, v192, v193\n\t" \
  "v_cvt_pk_bf16_f32 v196, v196, v197\n\t" \
  "ds_write2_b32 v252, v192, v196 offset1:64\n\t" \
  "s_waitcnt lgkmcnt(0)\n\t" \
  "s_barrier\n\t" \
  "buffer_store_dword v192, v235, %[rs], %[sk] offen sc1\n\t" \
  "buffer_store_dword v196, v235, %[rs], %[sk] offen offset:256 sc1\n\t" \
  "s_mov_b32 %[spins], s92\n\t" \
  "s_branch .Lend%=\n\t" \
  ".Lslow%=:\n\t" \
  "s_mov_b64 s[94:95], 0\n\t" \
  "v_cmp_eq_u32 vcc, %[sent], v200\n\t" \
  "s_or_b64 s[94:95], s[94:95], vcc\n\t" \
  "v_cmp_eq_u32 vcc, %[sent], v201\n\t" \
  "s_or_b64 s[94:95], s[94:95], vcc\n\t" \
  "v_cmp_eq_u32 vcc, %[sent], v202\n\t" \
  "s_or_b64 s[94:95], s[94:95], vcc\n\t" \
  "v_cmp_eq_u32 vcc, %[sent], v203\n\t" \
  "s_or_b64 s[94:95], s[94:95], vcc\n\t" \
  "v_cmp_eq_u32 vcc, %[sent], v204\n\t" \
  "s_or_b64 s[94:95], s[94:95], vcc\n\t" \
  "v_cmp_eq_u32 vcc, %[sent], v205\n\t" \
  "s_or_b64 s[94:95], s[94:95], vcc\n\t" \
  "v_cmp_eq_u32 vcc, %[sent], v206\n\t" \
  "s_or_b64 s[94:95], s[94:95], vcc\n\t" \
  "v_cmp_eq_u32 vcc, %[sent], v207\n\t" \
  "s_or_b64 s[94:95], s[94:95], vcc\n\t" \
  "s_cmp_eq_u64 s[94:95], 0\n\t" \
  "s_cbranch_scc1 .Lok%=\n\t" \
  "v_mov_b64 v[192:193], 0\n\t" \
  "v_mov_b64 v[194:195], 0\n\t" \
  "v_mov_b64 v[196:197], 0\n\t" \
  "v_mov_b64 v[198:199], 0\n\t" \
  "s_add_u32 s92, s92, 1\n\t" \
  "s_cmp_eq_u32 s92, 1\n\t" \
  "s_cbranch_scc1 .Lpoll%=\n\t" \
  "s_sleep 2\n\t" \
  "s_and_b32 s93, s92, 63\n\t" \
  "s_cmp_lg_u32 s93, 0\n\t" \
  "s_cbranch_scc1 .Lpoll%=\n\t" \
  "v_mov_b32 v245, 0\n\t" \
  "buffer_load_dword v244, v245, %[rs], %[sab] offen sc1\n\t" \
  "s_waitcnt vmcnt(0)\n\t" \
  "v_readfirstlane_b32 s93, v244\n\t" \
  "s_nop 3\n\t" \
  "s_cmp_eq_u32 s93, 1\n\t" \
  "s_cbranch_scc1 .Lok%=\n\t" \
  "s_cmp_lt_u32 s92, 0x80000\n\t" \
  "s_cbranch_scc1 .Lpoll%=\n\t" \
  "v_mov_b32 v244, 1\n\t" \
  "buffer_store_dword v244, v245, %[rs], %[sab] offen sc1\n\t" \
  "s_branch .Lok%=\n\t" \
  ".Lend%=:\n\t"
#define QK_ASM_5 \
  "s_waitcnt lgkmcnt(0)\n\t" \
  "v_subrev_u32 v244, 0x8000, v250\n\t" \
  "ds_read_b128 v[216:219], v248 offset:53248\n\t" \
  "ds_read_b128 v[220:223], v248 offset:54272\n\t" \
  "ds_read_b128 v[224:227], v248 offset:55296\n\t" \
  "ds_read_b128 v[228:231], v248 offset:56320\n\t" \
  "ds_read_b128 v[208:211], v244 offset:0\n\t" \
  "ds_read_b128 v[212:215], v244 offset:1024\n\t" \
  "ds_read_b128 v[236:239], v244 offset:2048\n\t" \
  "ds_read_b128 v[240:243], v244 offset:3072\n\t" \
  "v_mov_b64 v[192:193], 0\n\t" \
  "v_mov_b64 v[194:195], 0\n\t" \
  "v_mov_b64 v[196:197], 0\n\t" \
  "v_mov_b64 v[198:199], 0\n\t" \
  ".p2align 5\n\t" \
  "s_nop 0\n\t" \
  "s_nop 0\n\t" \
  "s_nop 0\n\t" \
  "s_nop 0\n\t" \
  "s_movk_i32 s92, 0x2000\n\t" \
  ".Lwt%=:\n\t" \
  "ds_read_b64 v[200:201], v248 offset:30720\n\t" \
  "s_memrealtime s[90:91]\n\t" \
  "s_sub_u32 s92, s92, 1\n\t" \
  "s_waitcnt lgkmcnt(0)\n\t" \
  "v_readfirstlane_b32 s93, v200\n\t" \
  "v_readfirstlane_b32 %[tgt], v201\n\t" \
  "s_cbranch_scc1 .Lgo%=\n\t" \
  "s_cmp_eq_u32 s93, %[step]\n\t" \
  "s_cbranch_scc1 .Lwx%=\n\t" \
  "s_sleep 1\n\t" \
  "s_branch .Lwt%=\n\t" \
  ".Lwx%=:\n\t" \
  "s_lshl_b32 s93, s90, 2\n\t" \
  "s_sub_i32 s93, %[tgt], s93\n\t" \
  "s_cmp_le_i32 s93, 0\n\t" \
  "s_cbranch_scc1 .Lgo%=\n\t" \
  "s_min_i32 s93, s93, 8000\n\t" \
  "s_mov_b32 s94, s93\n\t" \
  ".Lsl%=:\n\t" \
  "s_sub_i32 s94, s94, 18\n\t" \
  "s_cmp_lt_i32 s94, 30\n\t" \
  "s_cbranch_scc1 .Lsx%=\n\t" \
  "s_sleep 1\n\t" \
  "s_branch .Lsl%=\n\t" \
  ".Lsx%=:\n\t" \
  "s_sub_i32 s93, s93, 12\n\t" \
  "s_max_i32 s93, s93, 4\n\t" \
  "s_lshr_b32 s93, s93, 2\n\t" \
  "s_add_u32 s93, s93, s90\n\t" \
  "s_memrealtime s[90:91]\n\t" \
  ".Lpace%=:\n\t" \
  "s_memrealtime s[94:95]\n\t" \
  "s_waitcnt lgkmcnt(1)\n\t" \
  "s_sub_i32 s92, s90, s93\n\t" \
  "s_cmp_ge_i32 s92, 0\n\t" \
  "s_cbranch_scc1 .Le%=\n\t" \
  "s_memrealtime s[90:91]\n\t" \
  "s_waitcnt lgkmcnt(1)\n\t" \
  "s_sub_i32 s92, s94, s93\n\t" \
  "s_cmp_lt_i32 s92, 0\n\t" \
  "s_cbranch_scc1 .Lpace%=\n\t" \
  "s_nop 4\n\t" \
  ".Le%=:\n\t" \
  "s_add_u32 s92, s92, s93\n\t" \
  "s_lshl_b32 s92, s92, 2\n\t" \
  "s_sub_i32 s92, %[tgt], s92\n\t" \
  "s_max_i32 s92, s92, 0\n\t" \
  "s_bitcmp0_b32 s92, 3\n\t" \
  "s_cbranch_scc1 .Lf3%=\n\t" \
  "s_nop 15\n\t" \
  ".Lf3%=:\n\t" \
  "s_bitcmp0_b32 s92, 2\n\t" \
  "s_cbranch_scc1 .Lf2%=\n\t" \
  "s_nop 10\n\t" \
  ".Lf2%=:\n\t" \
  ".Lgo%=:\n\t" \
  "s_mov_b32 s92, 0\n\t" \
  ".Lpoll%=:\n\t" \
  "buffer_load_dwordx4 v[200:203], v248, %[rs], %[som] offen sc1\n\t" \
  "buffer_load_dwordx4 v[204:207], v248, %[rs], %[som2] offen sc1\n\t" \
  ".p2align 5\n\t" \
  "s_nop 0\n\t" \
  "s_nop 0\n\t" \
  "s_nop 0\n\t" \
  "s_nop 0\n\t" \
  "s_nop 0\n\t" \
  "s_nop 0\n\t" \
  "s_nop 0\n\t" \
  "s_waitcnt vmcnt(1) lgkmcnt(0)\n\t" \
  "ds_write_b128 v249, v[200:203]\n\t" \
  "v_smfmac_f32_16x16x64_bf16 v[192:195], v[200:203], v[208:215], v251\n\t" \
  "v_max3_u32 v244, v200, v201, v202\n\t" \
  "v_smfmac_f32_16x16x64_bf16 v[196:199], v[200:203], v[236:243], v251\n\t" \
  "s_waitcnt vmcnt(0)\n\t" \
  "ds_write_b128 v249, v[204:207] offset:8192\n\t" \
  "v_smfmac_f32_16x16x64_bf16 v[192:195], v[204:207], v[216:223], v251\n\t" \
  "v_max3_u32 v244, v244, v203, v204\n\t" \
  "v_max3_u32 v244, v244, v205, v206\n\t" \
  "v_smfmac_f32_16x16x64_bf16 v[196:199], v[204:207], v[224:231], v251\n\t" \
  "v_max_u32 v244, v244, v207\n\t" \
  "v_cmp_le_u32 vcc, %[sent], v244\n\t" \
  "s_cbranch_vccnz .Lslow%=\n\t" \
  ".Lok%=:\n\t" \
  "s_waitcnt lgkmcnt(0)\n\t" \
  "s_barrier\n\t" \
  "ds_read_b128 v[208:211], v248 offset:7168\n\t" \
  "ds_read_b128 v[212:215], v248 offset:8192\n\t" \
  "ds_read_b128 v[236:239], v248 offset:9216\n\t" \
  "ds_read_b128 v[240:243], v248 offset:10240\n\t" \
  "ds_read_b128 v[244:247], v248 offset:11264\n\t" \
  "s_waitcnt lgkmcnt(4)\n\t" \
  "ds_read_b128 v[200:203], v248 offset:4096\n\t" \
  "v_smfmac_f32_16x16x64_bf16 v[192:195], v[208:211], v[0:7], v251\n\t" \
  "ds_read_b128 v[216:219], v250 offset:32768\n\t" \
  "v_smfmac_f32_16x16x64_bf16 v[196:199], v[208:211], v[8:15], v251\n\t" \
  "s_waitcnt lgkmcnt(5)\n\t" \
  "ds_read_b128 v[220:223], v250 offset:33792\n\t" \
  "v_smfmac_f32_16x16x64_bf16 v[192:195], v[212:215], v[16:23], v251\n\t" \
  "ds_read_b128 v[224:227], v250 offset:34816\n\t" \
  "v_smfmac_f32_16x16x64_bf16 v[196:199], v[212:215], v[24:31], v251\n\t" \
  "s_waitcnt lgkmcnt(6)\n\t" \
  "ds_read_b128 v[228:231], v250 offset:35840\n\t" \
  "v_smfmac_f32_16x16x64_bf16 v[192:195], v[236:239], v[32:39], v251\n\t" \
  "ds_read_b128 v[204:207], v248 offset:14336\n\t" \
  "v_smfmac_f32_16x16x64_bf16 v[196:199], v[236:239], v[40:47], v251\n\t" \
  "s_waitcnt lgkmcnt(7)\n\t" \
  "ds_read_b128 v[208:211], v248 offset:15360\n\t" \
  "v_smfmac_f32_16x16x64_bf16 v[192:195], v[240:243], v[48:55], v251\n\t" \
  "ds_read_b128 v[212:215], v248 offset:0\n\t" \
  "v_smfmac_f32_16x16x64_bf16 v[196:199], v[240:243], v[56:63], v251\n\t" \
  "s_waitcnt lgkmcnt(7)\n\t" \
  "v_smfmac_f32_16x16x64_bf16 v[192:195], v[244:247], v[64:71], v251\n\t" \
  "ds_read_b128 v[236:239], v248 offset:1024\n\t" \
  "v_smfmac_f32_16x16x64_bf16 v[196:199], v[244:247], v[72:79], v251\n\t" \
  "s_waitcnt lgkmcnt(6)\n\t" \
  "v_smfmac_f32_16x16x64_bf16 v[192:195], v[200:203], v[216:223], v251\n\t" \
  "s_waitcnt lgkmcnt(4)\n\t" \
  "ds_read_b128 v[240:243], v248 offset:12288\n\t" \
  "v_smfmac_f32_16x16x64_bf16 v[196:199], v[200:203], v[224:231], v251\n\t" \
  "s_waitcnt lgkmcnt(4)\n\t" \
  "ds_read_b128 v[216:219], v250 offset:0\n\t" \
  "v_smfmac_f32_16x16x64_bf16 v[192:195], v[204:207], v[80:87], v251\n\t" \
  "ds_read_b128 v[220:223], v250 offset:1024\n\t" \
  "v_smfmac_f32_16x16x64_bf16 v[196:199], v[204:207], v[88:95], v251\n\t" \
  "s_waitcnt lgkmcnt(4)\n\t" \
  "ds_read_b128 v[224:227], v250 offset:2048\n\t" \
  "v_smfmac_f32_16x16x64_bf16 v[192:195], v[208:211], v[96:103], v251\n\t" \
  "ds_read_b128 v[228:231], v250 offset:3072\n\t" \
  "v_smfmac_f32_16x16x64_bf16 v[196:199], v[208:211], v[104:111], v251\n\t" \
  "ds_read_b128 v[244:247], v248 offset:2048\n\t" \
  "v_smfmac_f32_16x16x64_bf16 v[192:195], v[212:215], v[112:119], v251\n\t" \
  "ds_read_b128 v[200:203], v248 offset:3072\n\t" \
  "v_smfmac_f32_16x16x64_bf16 v[196:199], v[212:215], v[120:127], v251\n\t" \
  "s_waitcnt lgkmcnt(6)\n\t" \
  "v_smfmac_f32_16x16x64_bf16 v[192:195], v[236:239], v[128:135], v251\n\t" \
  "ds_read_b128 v[204:207], v248 offset:6144\n\t" \
  "v_smfmac_f32_16x16x64_bf16 v[196:199], v[236:239], v[136:143], v251\n\t" \
  "s_waitcnt lgkmcnt(5)\n\t" \
  "v_smfmac_f32_16x16x64_bf16 v[192:195], v[240:243], v[216:223], v251\n\t" \
  "s_waitcnt lgkmcnt(3)\n\t" \
  "v_smfmac_f32_16x16x64_bf16 v[196:199], v[240:243], v[224:231], v251\n\t" \
  "s_waitcnt lgkmcnt(2)\n\t" \
  "v_smfmac_f32_16x16x64_bf16 v[192:195], v[244:247], v[144:151], v251\n\t" \
  "v_smfmac_f32_16x16x64_bf16 v[196:199], v[244:247], v[152:159], v251\n\t" \
  "s_waitcnt lgkmcnt(1)\n\t" \
  "v_smfmac_f32_16x16x64_bf16 v[192:195], v[200:203], v[160:167], v251\n\t" \
  "v_smfmac_f32_16x16x64_bf16 v[196:199], v[200:203], v[168:175], v251\n\t" \
  "s_waitcnt lgkmcnt(0)\n\t" \
  "v_smfmac_f32_16x16x64_bf16 v[192:195], v[204:207], v[176:183], v251\n\t" \
  "v_smfmac_f32_16x16x64_bf16 v[196:199], v[204:207], v[184:191], v251\n\t" \
  "s_nop 6\n\t" \
  "v_pk_add_f32 v[192:193], v[192:193], v[194:195]\n\t" \
  "v_pk_add_f32 v[196:197], v[196:197], v[198:199]\n\t" \
  "v_cvt_pk_bf16_f32 v192, v192, v193\n\t" \
  "v_cvt_pk_bf16_f32 v196, v196, v197\n\t" \
  "ds_write2_b32 v252, v192, v196 offset1:64\n\t" \
  "s_waitcnt lgkmcnt(0)\n\t" \
  "s_barrier\n\t" \
  "buffer_store_dword v192, v235, %[rs], %[sk] offen sc1\n\t" \
  "buffer_store_dword v196, v235, %[rs], %[sk] offen offset:256 sc1\n\t" \
  "s_mov_b32 %[spins], s92\n\t" \
  "s_branch .Lend%=\n\t" \
  ".Lslow%=:\n\t" \
  "s_mov_b64 s[94:95], 0\n\t" \
  "v_cmp_eq_u32 vcc, %[sent], v200\n\t" \
  "s_or_b64 s[94:95], s[94:95], vcc\n\t" \
  "v_cmp_eq_u32 vcc, %[sent], v201\n\t" \
  "s_or_b64 s[94:95], s[94:95], vcc\n\t" \
  "v_cmp_eq_u32 vcc, %[sent], v202\n\t" \
  "s_or_b64 s[94:95], s[94:95], vcc\n\t" \
  "v_cmp_eq_u32 vcc, %[sent], v203\n\t" \
  "s_or_b64 s[94:95], s[94:95], vcc\n\t" \
  "v_cmp_eq_u32 vcc, %[sent], v204\n\t" \
  "s_or_b64 s[94:95], s[94:95], vcc\n\t" \
  "v_cmp_eq_u32 vcc, %[sent], v205\n\t" \
  "s_or_b64 s[94:95], s[94:95], vcc\n\t" \
  "v_cmp_eq_u32 vcc, %[sent], v206\n\t" \
  "s_or_b64 s[94:95], s[94:95], vcc\n\t" \
  "v_cmp_eq_u32 vcc, %[sent], v207\n\t" \
  "s_or_b64 s[94:95], s[94:95], vcc\n\t" \
  "s_cmp_eq_u64 s[94:95], 0\n\t" \
  "s_cbranch_scc1 .Lok%=\n\t" \
  "v_mov_b64 v[192:193], 0\n\t" \
  "v_mov_b64 v[194:195], 0\n\t" \
  "v_mov_b64 v[196:197], 0\n\t" \
  "v_mov_b64 v[198:199], 0\n\t" \
  "s_add_u32 s92, s92, 1\n\t" \
  "s_cmp_eq_u32 s92, 1\n\t" \
  "s_cbranch_scc1 .Lpoll%=\n\t" \
  "s_sleep 2\n\t" \
  "s_and_b32 s93, s92, 63\n\t" \
  "s_cmp_lg_u32 s93, 0\n\t" \
  "s_cbranch_scc1 .Lpoll%=\n\t" \
  "v_mov_b32 v245, 0\n\t" \
  "buffer_load_dword v244, v245, %[rs], %[sab] offen sc1\n\t" \
  "s_waitcnt vmcnt(0)\n\t" \
  "v_readfirstlane_b32 s93, v244\n\t" \
  "s_nop 3\n\t" \
  "s_cmp_eq_u32 s93, 1\n\t" \
  "s_cbranch_scc1 .Lok%=\n\t" \
  "s_cmp_lt_u32 s92, 0x80000\n\t" \
  "s_cbranch_scc1 .Lpoll%=\n\t" \
  "v_mov_b32 v244, 1\n\t" \
  "buffer_store_dword v244, v245, %[rs], %[sab] offen sc1\n\t" \
  "s_branch .Lok%=\n\t" \
  ".Lend%=:\n\t"
#define QK_ASM_6 \
  "s_waitcnt lgkmcnt(0)\n\t" \
  "v_subrev_u32 v244, 0x8000, v250\n\t" \
  "ds_read_b128 v[216:219], v248 offset:57344\n\t" \
  "ds_read_b128 v[220:223], v248 offset:58368\n\t" \
  "ds_read_b128 v[224:227], v248 offset:59392\n\t" \
  "ds_read_b128 v[228:231], v248 offset:60416\n\t" \
  "ds_read_b128 v[208:211], v244 offset:0\n\t" \
  "ds_read_b128 v[212:215], v244 offset:1024\n\t" \
  "ds_read_b128 v[236:239], v244 offset:2048\n\t" \
  "ds_read_b128 v[240:243], v244 offset:3072\n\t" \
  "v_mov_b64 v[192:193], 0\n\t" \
  "v_mov_b64 v[194:195], 0\n\t" \
  "v_mov_b64 v[196:197], 0\n\t" \
  "v_mov_b64 v[198:199], 0\n\t" \
  ".p2align 5\n\t" \
  "s_nop 0\n\t" \
  "s_nop 0\n\t" \
  "s_nop 0\n\t" \
  "s_nop 0\n\t" \
  "s_movk_i32 s92, 0x2000\n\t" \
  ".Lwt%=:\n\t" \
  "ds_read_b64 v[200:201], v248 offset:30720\n\t" \
  "s_memrealtime s[90:91]\n\t" \
  "s_sub_u32 s92, s92, 1\n\t" \
  "s_waitcnt lgkmcnt(0)\n\t" \
  "v_readfirstlane_b32 s93, v200\n\t" \
  "v_readfirstlane_b32 %[tgt], v201\n\t" \
  "s_cbranch_scc1 .Lgo%=\n\t" \
  "s_cmp_eq_u32 s93, %[step]\n\t" \
  "s_cbranch_scc1 .Lwx%=\n\t" \
  "s_sleep 1\n\t" \
  "s_branch .Lwt%=\n\t" \
  ".Lwx%=:\n\t" \
  "s_lshl_b32 s93, s90, 2\n\t" \
  "s_sub_i32 s93, %[tgt], s93\n\t" \
  "s_cmp_le_i32 s93, 0\n\t" \
  "s_cbranch_scc1 .Lgo%=\n\t" \
  "s_min_i32 s93, s93, 8000\n\t" \
  "s_mov_b32 s94, s93\n\t" \
  ".Lsl%=:\n\t" \
  "s_sub_i32 s94, s94, 18\n\t" \
  "s_cmp_lt_i32 s94, 30\n\t" \
  "s_cbranch_scc1 .Lsx%=\n\t" \
  "s_sleep 1\n\t" \
  "s_branch .Lsl%=\n\t" \
  ".Lsx%=:\n\t" \
  "s_sub_i32 s93, s93, 12\n\t" \
  "s_max_i32 s93, s93, 4\n\t" \
  "s_lshr_b32 s93, s93, 2\n\t" \
  "s_add_u32 s93, s93, s90\n\t" \
  "s_memrealtime s[90:91]\n\t" \
  ".Lpace%=:\n\t" \
  "s_memrealtime s[94:95]\n\t" \
  "s_waitcnt lgkmcnt(1)\n\t" \
  "s_sub_i32 s92, s90, s93\n\t" \
  "s_cmp_ge_i32 s92, 0\n\t" \
  "s_cbranch_scc1 .Le%=\n\t" \
  "s_memrealtime s[90:91]\n\t" \
  "s_waitcnt lgkmcnt(1)\n\t" \
  "s_sub_i32 s92, s94, s93\n\t" \
  "s_cmp_lt_i32 s92, 0\n\t" \
  "s_cbranch_scc1 .Lpace%=\n\t" \
  "s_nop 4\n\t" \
  ".Le%=:\n\t" \
  "s_add_u32 s92, s92, s93\n\t" \
  "s_lshl_b32 s92, s92, 2\n\t" \
  "s_sub_i32 s92, %[tgt], s92\n\t" \
  "s_max_i32 s92, s92, 0\n\t" \
  "s_bitcmp0_b32 s92, 3\n\t" \
  "s_cbranch_scc1 .Lf3%=\n\t" \
  "s_nop 15\n\t" \
  ".Lf3%=:\n\t" \
  "s_bitcmp0_b32 s92, 2\n\t" \
  "s_cbranch_scc1 .Lf2%=\n\t" \
  "s_nop 10\n\t" \
  ".Lf2%=:\n\t" \
  ".Lgo%=:\n\t" \
  "s_mov_b32 s92, 0\n\t" \
  ".Lpoll%=:\n\t" \
  "buffer_load_dwordx4 v[200:203], v248, %[rs], %[som] offen sc1\n\t" \
  "buffer_load_dwordx4 v[204:207], v248, %[rs], %[som2] offen sc1\n\t" \
  ".p2align 5\n\t" \
  "s_nop 0\n\t" \
  "s_nop 0\n\t" \
  "s_nop 0\n\t" \
  "s_nop 0\n\t" \
  "s_nop 0\n\t" \
  "s_nop 0\n\t" \
  "s_nop 0\n\t" \
  "s_waitcnt vmcnt(1) lgkmcnt(0)\n\t" \
  "ds_write_b128 v249, v[200:203]\n\t" \
  "v_smfmac_f32_16x16x64_bf16 v[192:195], v[200:203], v[208:215], v251\n\t" \
  "v_max3_u32 v244, v200, v201, v202\n\t" \
  "v_smfmac_f32_16x16x64_bf16 v[196:199], v[200:203], v[236:243], v251\n\t" \
  "s_waitcnt vmcnt(0)\n\t" \
  "ds_write_b128 v249, v[204:207] offset:8192\n\t" \
  "v_smfmac_f32_16x16x64_bf16 v[192:195], v[204:207], v[216:223], v251\n\t" \
  "v_max3_u32 v244, v244, v203, v204\n\t" \
  "v_max3_u32 v244, v244, v205, v206\n\t" \
  "v_smfmac_f32_16x16x64_bf16 v[196:199], v[204:207], v[224:231], v251\n\t" \
  "v_max_u32 v244, v244, v207\n\t" \
  "v_cmp_le_u32 vcc, %[sent], v244\n\t" \
  "s_cbranch_vccnz .Lslow%=\n\t" \
  ".Lok%=:\n\t" \
  "s_waitcnt lgkmcnt(0)\n\t" \
  "s_barrier\n\t" \
  "ds_read_b128 v[208:211], v248 offset:8192\n\t" \
  "ds_read_b128 v[212:215], v248 offset:9216\n\t" \
  "ds_read_b128 v[236:239], v248 offset:10240\n\t" \
  "ds_read_b128 v[240:243], v248 offset:11264\n\t" \
  "ds_read_b128 v[244:247], v248 offset:12288\n\t" \
  "s_waitcnt lgkmcnt(4)\n\t" \
  "ds_read_b128 v[200:203], v248 offset:7168\n\t" \
  "v_smfmac_f32_16x16x64_bf16 v[192:195], v[208:211], v[0:7], v251\n\t" \
  "ds_read_b128 v[216:219], v250 offset:32768\n\t" \
  "v_smfmac_f32_16x16x64_bf16 v[196:199], v[208:211], v[8:15], v251\n\t" \
  "s_waitcnt lgkmcnt(5)\n\t" \
  "ds_read_b128 v[220:223], v250 offset:33792\n\t" \
  "v_smfmac_f32_16x16x64_bf16 v[192:195], v[212:215], v[16:23], v251\n\t" \
  "ds_read_b128 v[224:227], v250 offset:34816\n\t" \
  "v_smfmac_f32_16x16x64_bf16 v[196:199], v[212:215], v[24:31], v251\n\t" \
  "s_waitcnt lgkmcnt(6)\n\t" \
  "ds_read_b128 v[228:231], v250 offset:35840\n\t" \
  "v_smfmac_f32_16x16x64_bf16 v[192:195], v[236:239], v[32:39], v251\n\t" \
  "ds_read_b128 v[204:207], v248 offset:15360\n\t" \
  "v_smfmac_f32_16x16x64_bf16 v[196:199], v[236:239], v[40:47], v251\n\t" \
  "s_waitcnt lgkmcnt(7)\n\t" \
  "ds_read_b128 v[208:211], v248 offset:0\n\t" \
  "v_smfmac_f32_16x16x64_bf16 v[192:195], v[240:243], v[48:55], v251\n\t" \
  "ds_read_b128 v[212:215], v248 offset:1024\n\t" \
  "v_smfmac_f32_16x16x64_bf16 v[196:199], v[240:243], v[56:63], v251\n\t" \
  "s_waitcnt lgkmcnt(7)\n\t" \
  "v_smfmac_f32_16x16x64_bf16 v[192:195], v[244:247], v[64:71], v251\n\t" \
  "ds_read_b128 v[236:239], v248 offset:2048\n\t" \
  "v_smfmac_f32_16x16x64_bf16 v[196:199], v[244:247], v[72:79], v251\n\t" \
  "s_waitcnt lgkmcnt(6)\n\t" \
  "v_smfmac_f32_16x16x64_bf16 v[192:195], v[200:203], v[216:223], v251\n\t" \
  "s_waitcnt lgkmcnt(4)\n\t" \
  "ds_read_b128 v[240:243], v248 offset:13312\n\t" \
  "v_smfmac_f32_16x16x64_bf16 v[196:199], v[200:203], v[224:231], v251\n\t" \
  "s_waitcnt lgkmcnt(4)\n\t" \
  "ds_read_b128 v[216:219], v250 offset:0\n\t" \
  "v_smfmac_f32_16x16x64_bf16 v[192:195], v[204:207], v[80:87], v251\n\t" \
  "ds_read_b128 v[220:223], v250 offset:1024\n\t" \
  "v_smfmac_f32_16x16x64_bf16 v[196:199], v[204:207], v[88:95], v251\n\t" \
  "s_waitcnt lgkmcnt(4)\n\t" \
  "ds_read_b128 v[224:227], v250 offset:2048\n\t" \
  "v_smfmac_f32_16x16x64_bf16 v[192:195], v[208:211], v[96:103], v251\n\t" \
  "ds_read_b128 v[228:231], v250 offset:3072\n\t" \
  "v_smfmac_f32_16x16x64_bf16 v[196:199], v[208:211], v[104:111], v251\n\t" \
  "ds_read_b128 v[244:247], v248 offset:3072\n\t" \
  "v_smfmac_f32_16x16x64_bf16 v[192:195], v[212:215], v[112:119], v251\n\t" \
  "ds_read_b128 v[200:203], v248 offset:4096\n\t" \
  "v_smfmac_f32_16x16x64_bf16 v[196:199], v[212:215], v[120:127], v251\n\t" \
  "s_waitcnt lgkmcnt(6)\n\t" \
  "v_smfmac_f32_16x16x64_bf16 v[192:195], v[236:239], v[128:135], v251\n\t" \
  "ds_read_b128 v[204:207], v248 offset:5120\n\t" \
  "v_smfmac_f32_16x16x64_bf16 v[196:199], v[236:239], v[136:143], v251\n\t" \
  "s_waitcnt lgkmcnt(5)\n\t" \
  "v_smfmac_f32_16x16x64_bf16 v[192:195], v[240:243], v[216:223], v251\n\t" \
  "s_waitcnt lgkmcnt(3)\n\t" \
  "v_smfmac_f32_16x16x64_bf16 v[196:199], v[240:243], v[224:231], v251\n\t" \
  "s_waitcnt lgkmcnt(2)\n\t" \
  "v_smfmac_f32_16x16x64_bf16 v[192:195], v[244:247], v[144:151], v251\n\t" \
  "v_smfmac_f32_16x16x64_bf16 v[196:199], v[244:247], v[152:159], v251\n\t" \
  "s_waitcnt lgkmcnt(1)\n\t" \
  "v_smfmac_f32_16x16x64_bf16 v[192:195], v[200:203], v[160:167], v251\n\t" \
  "v_smfmac_f32_16x16x64_bf16 v[196:199], v[200:203], v[168:175], v251\n\t" \
  "s_waitcnt lgkmcnt(0)\n\t" \
  "v_smfmac_f32_16x16x64_bf16 v[192:195], v[204:207], v[176:183], v251\n\t" \
  "v_smfmac_f32_16x16x64_bf16 v[196:199], v[204:207], v[184:191], v251\n\t" \
  "s_nop 6\n\t" \
  "v_pk_add_f32 v[192:193], v[192:193], v[194:195]\n\t" \
  "v_pk_add_f32 v[196:197], v[196:197], v[198:199]\n\t" \
  "v_cvt_pk_bf16_f32 v192, v192, v193\n\t" \
  "v_cvt_pk_bf16_f32 v196, v196, v197\n\t" \
  "ds_write2_b32 v252, v192, v196 offset1:64\n\t" \
  "s_waitcnt lgkmcnt(0)\n\t" \
  "s_barrier\n\t" \
  "buffer_store_dword v192, v235, %[rs], %[sk] offen sc1\n\t" \
  "buffer_store_dword v196, v235, %[rs], %[sk] offen offset:256 sc1\n\t" \
  "s_mov_b32 %[spins], s92\n\t" \
  "s_branch .Lend%=\n\t" \
  ".Lslow%=:\n\t" \
  "s_mov_b64 s[94:95], 0\n\t" \
  "v_cmp_eq_u32 vcc, %[sent], v200\n\t" \
  "s_or_b64 s[94:95], s[94:95], vcc\n\t" \
  "v_cmp_eq_u32 vcc, %[sent], v201\n\t" \
  "s_or_b64 s[94:95], s[94:95], vcc\n\t" \
  "v_cmp_eq_u32 vcc, %[sent], v202\n\t" \
  "s_or_b64 s[94:95], s[94:95], vcc\n\t" \
  "v_cmp_eq_u32 vcc, %[sent], v203\n\t" \
  "s_or_b64 s[94:95], s[94:95], vcc\n\t" \
  "v_cmp_eq_u32 vcc, %[sent], v204\n\t" \
  "s_or_b64 s[94:95], s[94:95], vcc\n\t" \
  "v_cmp_eq_u32 vcc, %[sent], v205\n\t" \
  "s_or_b64 s[94:95], s[94:95], vcc\n\t" \
  "v_cmp_eq_u32 vcc, %[sent], v206\n\t" \
  "s_or_b64 s[94:95], s[94:95], vcc\n\t" \
  "v_cmp_eq_u32 vcc, %[sent], v207\n\t" \
  "s_or_b64 s[94:95], s[94:95], vcc\n\t" \
  "s_cmp_eq_u64 s[94:95], 0\n\t" \
  "s_cbranch_scc1 .Lok%=\n\t" \
  "v_mov_b64 v[192:193], 0\n\t" \
  "v_mov_b64 v[194:195], 0\n\t" \
  "v_mov_b64 v[196:197], 0\n\t" \
  "v_mov_b64 v[198:199], 0\n\t" \
  "s_add_u32 s92, s92, 1\n\t" \
  "s_cmp_eq_u32 s92, 1\n\t" \
  "s_cbranch_scc1 .Lpoll%=\n\t" \
  "s_sleep 2\n\t" \
  "s_and_b32 s93, s92, 63\n\t" \
  "s_cmp_lg_u32 s93, 0\n\t" \
  "s_cbranch_scc1 .Lpoll%=\n\t" \
  "v_mov_b32 v245, 0\n\t" \
  "buffer_load_dword v244, v245, %[rs], %[sab] offen sc1\n\t" \
  "s_waitcnt vmcnt(0)\n\t" \
  "v_readfirstlane_b32 s93, v244\n\t" \
  "s_nop 3\n\t" \
  "s_cmp_eq_u32 s93, 1\n\t" \
  "s_cbranch_scc1 .Lok%=\n\t" \
  "s_cmp_lt_u32 s92, 0x80000\n\t" \
  "s_cbranch_scc1 .Lpoll%=\n\t" \
  "v_mov_b32 v244, 1\n\t" \
  "buffer_store_dword v244, v245, %[rs], %[sab] offen sc1\n\t" \
  "s_branch .Lok%=\n\t" \
  ".Lend%=:\n\t"
#define QK_ASM_7 \
  "s_waitcnt lgkmcnt(0)\n\t" \
  "v_subrev_u32 v244, 0x8000, v250\n\t" \
  "ds_read_b128 v[216:219], v248 offset:61440\n\t" \
  "ds_read_b128 v[220:223], v248 offset:62464\n\t" \
  "ds_read_b128 v[224:227], v248 offset:63488\n\t" \
  "ds_read_b128 v[228:231], v248 offset:64512\n\t" \
  "ds_read_b128 v[208:211], v244 offset:0\n\t" \
  "ds_read_b128 v[212:215], v244 offset:1024\n\t" \
  "ds_read_b128 v[236:239], v244 offset:2048\n\t" \
  "ds_read_b128 v[240:243], v244 offset:3072\n\t" \
  "v_mov_b64 v[192:193], 0\n\t" \
  "v_mov_b64 v[194:195], 0\n\t" \
  "v_mov_b64 v[196:197], 0\n\t" \
  "v_mov_b64 v[198:199], 0\n\t" \
  ".p2align 5\n\t" \
  "s_nop 0\n\t" \
  "s_nop 0\n\t" \
  "s_nop 0\n\t" \
  "s_nop 0\n\t" \
  "s_movk_i32 s92, 0x2000\n\t" \
  ".Lwt%=:\n\t" \
  "ds_read_b64 v[200:201], v248 offset:30720\n\t" \
  "s_memrealtime s[90:91]\n\t" \
  "s_sub_u32 s92, s92, 1\n\t" \
  "s_waitcnt lgkmcnt(0)\n\t" \
  "v_readfirstlane_b32 s93, v200\n\t" \
  "v_readfirstlane_b32 %[tgt], v201\n\t" \
  "s_cbranch_scc1 .Lgo%=\n\t" \
  "s_cmp_eq_u32 s93, %[step]\n\t" \
  "s_cbranch_scc1 .Lwx%=\n\t" \
  "s_sleep 1\n\t" \
  "s_branch .Lwt%=\n\t" \
  ".Lwx%=:\n\t" \
  "s_lshl_b32 s93, s90, 2\n\t" \
  "s_sub_i32 s93, %[tgt], s93\n\t" \
  "s_cmp_le_i32 s93, 0\n\t" \
  "s_cbranch_scc1 .Lgo%=\n\t" \
  "s_min_i32 s93, s93, 8000\n\t" \
  "s_mov_b32 s94, s93\n\t" \
  ".Lsl%=:\n\t" \
  "s_sub_i32 s94, s94, 18\n\t" \
  "s_cmp_lt_i32 s94, 30\n\t" \
  "s_cbranch_scc1 .Lsx%=\n\t" \
  "s_sleep 1\n\t" \
  "s_branch .Lsl%=\n\t" \
  ".Lsx%=:\n\t" \
  "s_sub_i32 s93, s93, 12\n\t" \
  "s_max_i32 s93, s93, 4\n\t" \
  "s_lshr_b32 s93, s93, 2\n\t" \
  "s_add_u32 s93, s93, s90\n\t" \
  "s_memrealtime s[90:91]\n\t" \
  ".Lpace%=:\n\t" \
  "s_memrealtime s[94:95]\n\t" \
  "s_waitcnt lgkmcnt(1)\n\t" \
  "s_sub_i32 s92, s90, s93\n\t" \
  "s_cmp_ge_i32 s92, 0\n\t" \
  "s_cbranch_scc1 .Le%=\n\t" \
  "s_memrealtime s[90:91]\n\t" \
  "s_waitcnt lgkmcnt(1)\n\t" \
  "s_sub_i32 s92, s94, s93\n\t" \
  "s_cmp_lt_i32 s92, 0\n\t" \
  "s_cbranch_scc1 .Lpace%=\n\t" \
  "s_nop 4\n\t" \
  ".Le%=:\n\t" \
  "s_add_u32 s92, s92, s93\n\t" \
  "s_lshl_b32 s92, s92, 2\n\t" \
  "s_sub_i32 s92, %[tgt], s92\n\t" \
  "s_max_i32 s92, s92, 0\n\t" \
  "s_bitcmp0_b32 s92, 3\n\t" \
  "s_cbranch_scc1 .Lf3%=\n\t" \
  "s_nop 15\n\t" \
  ".Lf3%=:\n\t" \
  "s_bitcmp0_b32 s92, 2\n\t" \
  "s_cbranch_scc1 .Lf2%=\n\t" \
  "s_nop 10\n\t" \
  ".Lf2%=:\n\t" \
  ".Lgo%=:\n\t" \
  "s_mov_b32 s92, 0\n\t" \
  "s_memtime s[88:89]\n\t" \
  ".Lpoll%=:\n\t" \
  "buffer_load_dwordx4 v[200:203], v248, %[rs], %[som] offen sc1\n\t" \
  "buffer_load_dwordx4 v[204:207], v248, %[rs], %[som2] offen sc1\n\t" \
  ".p2align 5\n\t" \
  "s_nop 0\n\t" \
  "s_nop 0\n\t" \
  "s_nop 0\n\t" \
  "s_nop 0\n\t" \
  "s_nop 0\n\t" \
  "s_nop 0\n\t" \
  "s_nop 0\n\t" \
  "s_waitcnt vmcnt(1) lgkmcnt(0)\n\t" \
  "ds_write_b128 v249, v[200:203]\n\t" \
  "v_smfmac_f32_16x16x64_bf16 v[192:195], v[200:203], v[208:215], v251\n\t" \
  "v_max3_u32 v244, v200, v201, v202\n\t" \
  "v_smfmac_f32_16x16x64_bf16 v[196:199], v[200:203], v[236:243], v251\n\t" \
  "s_waitcnt vmcnt(0)\n\t" \
  "ds_write_b128 v249, v[204:207] offset:8192\n\t" \
  "v_smfmac_f32_16x16x64_bf16 v[192:195], v[204:207], v[216:223], v251\n\t" \
  "v_max3_u32 v244, v244, v203, v204\n\t" \
  "v_max3_u32 v244, v244, v205, v206\n\t" \
  "v_smfmac_f32_16x16x64_bf16 v[196:199], v[204:207], v[224:231], v251\n\t" \
  "v_max_u32 v244, v244, v207\n\t" \
  "v_cmp_le_u32 vcc, %[sent], v244\n\t" \
  "s_cbranch_vccnz .Lslow%=\n\t" \
  ".Lok%=:\n\t" \
  "s_waitcnt lgkmcnt(0)\n\t" \
  "s_setprio 3\n\t" \
  "s_barrier\n\t" \
  "s_memrealtime s[90:91]\n\t" \
  "s_memtime s[94:95]\n\t" \
  "v_mov_b32 v232, %[step]\n\t" \
  "v_lshrrev_b32_e64 v234, 1, v248\n\t" \
  "s_mov_b64 exec, 0xffff\n\t" \
  "s_waitcnt lgkmcnt(0)\n\t" \
  "s_lshl_b32 s90, s90, 2\n\t" \
  "s_sub_u32 s94, s94, s88\n\t" \
  "s_mul_i32 s94, s94, 12200\n\t" \
  "s_lshr_b32 s94, s94, 16\n\t" \
  "s_add_u32 s94, s94, %[tgt]\n\t" \
  "s_sub_i32 s93, s90, s94\n\t" \
  "s_sub_i32 s93, s93, 30\n\t" \
  "s_max_i32 s93, s93, 0\n\t" \
  "s_add_u32 s90, s94, s93\n\t" \
  "s_sub_i32 s93, s90, %[tgt]\n\t" \
  "s_cmp_gt_i32 s93, 270\n\t" \
  "s_cselect_b32 s93, 70, 0\n\t" \
  "s_add_u32 s93, s93, s90\n\t" \
  "s_add_u32 s93, s93, %[lead]\n\t" \
  "v_mov_b32 v233, s93\n\t" \
  "buffer_store_dwordx2 v[232:233], v234, %[rs], %[sbc] offen sc1\n\t" \
  "s_mov_b64 exec, -1\n\t" \
  "s_mov_b32 %[tb1], s90\n\t" \
  "s_setprio 0\n\t" \
  "ds_read_b128 v[208:211], v248 offset:9216\n\t" \
  "ds_read_b128 v[212:215], v248 offset:10240\n\t" \
  "ds_read_b128 v[236:239], v248 offset:11264\n\t" \
  "ds_read_b128 v[240:243], v248 offset:13312\n\t" \
  "ds_read_b128 v[244:247], v248 offset:14336\n\t" \
  "s_waitcnt lgkmcnt(4)\n\t" \
  "ds_read_b128 v[200:203], v248 offset:6144\n\t" \
  "v_smfmac_f32_16x16x64_bf16 v[192:195], v[208:211], v[0:7], v251\n\t" \
  "ds_read_b128 v[216:219], v250 offset:32768\n\t" \
  "v_smfmac_f32_16x16x64_bf16 v[196:199], v[208:211], v[8:15], v251\n\t" \
  "s_waitcnt lgkmcnt(5)\n\t" \
  "ds_read_b128 v[220:223], v250 offset:33792\n\t" \
  "v_smfmac_f32_16x16x64_bf16 v[192:195], v[212:215], v[16:23], v251\n\t" \
  "ds_read_b128 v[224:227], v250 offset:34816\n\t" \
  "v_smfmac_f32_16x16x64_bf16 v[196:199], v[212:215], v[24:31], v251\n\t" \
  "s_waitcnt lgkmcnt(6)\n\t" \
  "ds_read_b128 v[228:231], v250 offset:35840\n\t" \
  "v_smfmac_f32_16x16x64_bf16 v[192:195], v[236:239], v[32:39], v251\n\t" \
  "ds_read_b128 v[204:207], v248 offset:0\n\t" \
  "v_smfmac_f32_16x16x64_bf16 v[196:199], v[236:239], v[40:47], v251\n\t" \
  "s_waitcnt lgkmcnt(7)\n\t" \
  "ds_read_b128 v[208:211], v248 offset:1024\n\t" \
  "v_smfmac_f32_16x16x64_bf16 v[192:195], v[240:243], v[48:55], v251\n\t" \
  "ds_read_b128 v[212:215], v248 offset:2048\n\t" \
  "v_smfmac_f32_16x16x64_bf16 v[196:199], v[240:243], v[56:63], v251\n\t" \
  "s_waitcnt lgkmcnt(7)\n\t" \
  "v_smfmac_f32_16x16x64_bf16 v[192:195], v[244:247], v[64:71], v251\n\t" \
  "ds_read_b128 v[236:239], v248 offset:3072\n\t" \
  "v_smfmac_f32_16x16x64_bf16 v[196:199], v[244:247], v[72:79], v251\n\t" \
  "s_waitcnt lgkmcnt(6)\n\t" \
  "v_smfmac_f32_16x16x64_bf16 v[192:195], v[200:203], v[216:223], v251\n\t" \
  "s_waitcnt lgkmcnt(4)\n\t" \
  "ds_read_b128 v[240:243], v248 offset:12288\n\t" \
  "v_smfmac_f32_16x16x64_bf16 v[196:199], v[200:203], v[224:231], v251\n\t" \
  "s_waitcnt lgkmcnt(4)\n\t" \
  "ds_read_b128 v[216:219], v250 offset:0\n\t" \
  "v_smfmac_f32_16x16x64_bf16 v[192:195], v[204:207], v[80:87], v251\n\t" \
  "ds_read_b128 v[220:223], v250 offset:1024\n\t" \
  "v_smfmac_f32_16x16x64_bf16 v[196:199], v[204:207], v[88:95], v251\n\t" \
  "s_waitcnt lgkmcnt(4)\n\t" \
  "ds_read_b128 v[224:227], v250 offset:2048\n\t" \
  "v_smfmac_f32_16x16x64_bf16 v[192:195], v[208:211], v[96:103], v251\n\t" \
  "ds_read_b128 v[228:231], v250 offset:3072\n\t" \
  "v_smfmac_f32_16x16x64_bf16 v[196:199], v[208:211], v[104:111], v251\n\t" \
  "ds_read_b128 v[244:247], v248 offset:4096\n\t" \
  "v_smfmac_f32_16x16x64_bf16 v[192:195], v[212:215], v[112:119], v251\n\t" \
  "ds_read_b128 v[200:203], v248 offset:5120\n\t" \
  "v_smfmac_f32_16x16x64_bf16 v[196:199], v[212:215], v[120:127], v251\n\t" \
  "s_waitcnt lgkmcnt(6)\n\t" \
  "v_smfmac_f32_16x16x64_bf16 v[192:195], v[236:239], v[128:135], v251\n\t" \
  "ds_read_b128 v[204:207], v248 offset:8192\n\t" \
  "v_smfmac_f32_16x16x64_bf16 v[196:199], v[236:239], v[136:143], v251\n\t" \
  "s_waitcnt lgkmcnt(5)\n\t" \
  "v_smfmac_f32_16x16x64_bf16 v[192:195], v[240:243], v[216:223], v251\n\t" \
  "s_waitcnt lgkmcnt(3)\n\t" \
  "v_smfmac_f32_16x16x64_bf16 v[196:199], v[240:243], v[224:231], v251\n\t" \
  "s_waitcnt lgkmcnt(2)\n\t" \
  "v_smfmac_f32_16x16x64_bf16 v[192:195], v[244:247], v[144:151], v251\n\t" \
  "v_smfmac_f32_16x16x64_bf16 v[196:199], v[244:247], v[152:159], v251\n\t" \
  "s_waitcnt lgkmcnt(1)\n\t" \
  "v_smfmac_f32_16x16x64_bf16 v[192:195], v[200:203], v[160:167], v251\n\t" \
  "v_smfmac_f32_16x16x64_bf16 v[196:199], v[200:203], v[168:175], v251\n\t" \
  "s_waitcnt lgkmcnt(0)\n\t" \
  "v_smfmac_f32_16x16x64_bf16 v[192:195], v[204:207], v[176:183], v251\n\t" \
  "v_smfmac_f32_16x16x64_bf16 v[196:199], v[204:207], v[184:191], v251\n\t" \
  "s_nop 6\n\t" \
  "v_pk_add_f32 v[192:193], v[192:193], v[194:195]\n\t" \
  "v_pk_add_f32 v[196:197], v[196:197], v[198:199]\n\t" \
  "v_cvt_pk_bf16_f32 v192, v192, v193\n\t" \
  "v_cvt_pk_bf16_f32 v196, v196, v197\n\t" \
  "ds_write2_b32 v252, v192, v196 offset1:64\n\t" \
  "s_waitcnt lgkmcnt(0)\n\t" \
  "s_barrier\n\t" \
  "buffer_store_dword v192, v235, %[rs], %[sk] offen sc1\n\t" \
  "buffer_store_dword v196, v235, %[rs], %[sk] offen offset:256 sc1\n\t" \
  "s_mov_b32 %[spins], s92\n\t" \
  "s_branch .Lend%=\n\t" \
  ".Lslow%=:\n\t" \
  "s_mov_b64 s[94:95], 0\n\t" \
  "v_cmp_eq_u32 vcc, %[sent], v200\n\t" \
  "s_or_b64 s[94:95], s[94:95], vcc\n\t" \
  "v_cmp_eq_u32 vcc, %[sent], v201\n\t" \
  "s_or_b64 s[94:95], s[94:95], vcc\n\t" \
  "v_cmp_eq_u32 vcc, %[sent], v202\n\t" \
  "s_or_b64 s[94:95], s[94:95], vcc\n\t" \
  "v_cmp_eq_u32 vcc, %[sent], v203\n\t" \
  "s_or_b64 s[94:95], s[94:95], vcc\n\t" \
  "v_cmp_eq_u32 vcc, %[sent], v204\n\t" \
  "s_or_b64 s[94:95], s[94:95], vcc\n\t" \
  "v_cmp_eq_u32 vcc, %[sent], v205\n\t" \
  "s_or_b64 s[94:95], s[94:95], vcc\n\t" \
  "v_cmp_eq_u32 vcc, %[sent], v206\n\t" \
  "s_or_b64 s[94:95], s[94:95], vcc\n\t" \
  "v_cmp_eq_u32 vcc, %[sent], v207\n\t" \
  "s_or_b64 s[94:95], s[94:95], vcc\n\t" \
  "s_cmp_eq_u64 s[94:95], 0\n\t" \
  "s_cbranch_scc1 .Lok%=\n\t" \
  "v_mov_b64 v[192:193], 0\n\t" \
  "v_mov_b64 v[194:195], 0\n\t" \
  "v_mov_b64 v[196:197], 0\n\t" \
  "v_mov_b64 v[198:199], 0\n\t" \
  "s_add_u32 s92, s92, 1\n\t" \
  "s_cmp_eq_u32 s92, 1\n\t" \
  "s_cbranch_scc1 .Lpoll%=\n\t" \
  "s_sleep 2\n\t" \
  "s_and_b32 s93, s92, 63\n\t" \
  "s_cmp_lg_u32 s93, 0\n\t" \
  "s_cbranch_scc1 .Lpoll%=\n\t" \
  "v_mov_b32 v245, 0\n\t" \
  "buffer_load_dword v244, v245, %[rs], %[sab] offen sc1\n\t" \
  "s_waitcnt vmcnt(0)\n\t" \
  "v_readfirstlane_b32 s93, v244\n\t" \
  "s_nop 3\n\t" \
  "s_cmp_eq_u32 s93, 1\n\t" \
  "s_cbranch_scc1 .Lok%=\n\t" \
  "s_cmp_lt_u32 s92, 0x80000\n\t" \
  "s_cbranch_scc1 .Lpoll%=\n\t" \
  "v_mov_b32 v244, 1\n\t" \
  "buffer_store_dword v244, v245, %[rs], %[sab] offen sc1\n\t" \
  "s_branch .Lok%=\n\t" \
  ".Lend%=:\n\t"
#define VV_ASM \
  "s_waitcnt lgkmcnt(0)\n\t" \
  "v_mov_b64 v[128:129], 0\n\t" \
  "v_mov_b64 v[130:131], 0\n\t" \
  ".p2align 5\n\t" \
  "s_nop 0\n\t" \
  "s_nop 0\n\t" \
  "s_nop 0\n\t" \
  "s_nop 0\n\t" \
  "s_movk_i32 s92, 0x2000\n\t" \
  ".Lwt%=:\n\t" \
  "ds_read_b64 v[132:133], v200 offset:30720\n\t" \
  "s_memrealtime s[90:91]\n\t" \
  "s_sub_u32 s92, s92, 1\n\t" \
  "s_waitcnt lgkmcnt(0)\n\t" \
  "v_readfirstlane_b32 s93, v132\n\t" \
  "v_readfirstlane_b32 %[tgt], v133\n\t" \
  "s_cbranch_scc1 .Lgo%=\n\t" \
  "s_cmp_eq_u32 s93, %[step]\n\t" \
  "s_cbranch_scc1 .Lwx%=\n\t" \
  "s_sleep 1\n\t" \
  "s_branch .Lwt%=\n\t" \
  ".Lwx%=:\n\t" \
  "s_lshl_b32 s93, s90, 2\n\t" \
  "s_sub_i32 s93, %[tgt], s93\n\t" \
  "s_cmp_le_i32 s93, 0\n\t" \
  "s_cbranch_scc1 .Lgo%=\n\t" \
  "s_min_i32 s93, s93, 8000\n\t" \
  "s_mov_b32 s94, s93\n\t" \
  ".Lsl%=:\n\t" \
  "s_sub_i32 s94, s94, 18\n\t" \
  "s_cmp_lt_i32 s94, 30\n\t" \
  "s_cbranch_scc1 .Lsx%=\n\t" \
  "s_sleep 1\n\t" \
  "s_branch .Lsl%=\n\t" \
  ".Lsx%=:\n\t" \
  "s_sub_i32 s93, s93, 12\n\t" \
  "s_max_i32 s93, s93, 4\n\t" \
  "s_lshr_b32 s93, s93, 2\n\t" \
  "s_add_u32 s93, s93, s90\n\t" \
  "s_memrealtime s[90:91]\n\t" \
  ".Lpace%=:\n\t" \
  "s_memrealtime s[94:95]\n\t" \
  "s_waitcnt lgkmcnt(1)\n\t" \
  "s_sub_i32 s92, s90, s93\n\t" \
  "s_cmp_ge_i32 s92, 0\n\t" \
  "s_cbranch_scc1 .Le%=\n\t" \
  "s_memrealtime s[90:91]\n\t" \
  "s_waitcnt lgkmcnt(1)\n\t" \
  "s_sub_i32 s92, s94, s93\n\t" \
  "s_cmp_lt_i32 s92, 0\n\t" \
  "s_cbranch_scc1 .Lpace%=\n\t" \
  "s_nop 4\n\t" \
  ".Le%=:\n\t" \
  "s_add_u32 s92, s92, s93\n\t" \
  "s_lshl_b32 s92, s92, 2\n\t" \
  "s_sub_i32 s92, %[tgt], s92\n\t" \
  "s_max_i32 s92, s92, 0\n\t" \
  "s_bitcmp0_b32 s92, 3\n\t" \
  "s_cbranch_scc1 .Lf3%=\n\t" \
  "s_nop 15\n\t" \
  ".Lf3%=:\n\t" \
  "s_bitcmp0_b32 s92, 2\n\t" \
  "s_cbranch_scc1 .Lf2%=\n\t" \
  "s_nop 10\n\t" \
  ".Lf2%=:\n\t" \
  ".Lgo%=:\n\t" \
  "s_mov_b32 s92, 0\n\t" \
  ".Lpoll%=:\n\t" \
  "buffer_load_dwordx4 v[132:135], v200, %[rs], %[som] offen sc1\n\t" \
  "buffer_load_dwordx4 v[136:139], v200, %[rs], %[som2] offen sc1\n\t" \
  ".p2align 5\n\t" \
  "s_waitcnt vmcnt(1) lgkmcnt(0)\n\t" \
  "ds_write_b128 v232, v[132:135]\n\t" \
  "v_smfmac_f32_16x16x64_bf16 v[128:131], v[132:135], v[0:7], v202\n\t" \
  "v_max3_u32 v196, v132, v133, v134\n\t" \
  "s_waitcnt vmcnt(0)\n\t" \
  "ds_write_b128 v232, v[136:139] offset:8192\n\t" \
  "v_smfmac_f32_16x16x64_bf16 v[128:131], v[136:139], v[64:71], v202\n\t" \
  "v_max3_u32 v196, v196, v135, v136\n\t" \
  "v_max3_u32 v196, v196, v137, v138\n\t" \
  "v_max_u32 v196, v196, v139\n\t" \
  "v_cmp_le_u32_e64 vcc, %[sent], v196\n\t" \
  "s_cbranch_vccnz .Lslow%=\n\t" \
  ".Lok%=:\n\t" \
  "s_waitcnt lgkmcnt(0)\n\t" \
  "s_barrier\n\t" \
  "ds_read_b128 v[164:167], v233 offset:0\n\t" \
  "ds_read_b128 v[140:143], v234 offset:0\n\t" \
  "ds_read_b128 v[144:147], v235 offset:0\n\t" \
  "ds_read_b128 v[148:151], v236 offset:0\n\t" \
  "ds_read_b128 v[152:155], v237 offset:0\n\t" \
  "s_waitcnt lgkmcnt(4)\n\t" \
  "ds_read_b128 v[156:159], v238 offset:0\n\t" \
  "v_smfmac_f32_16x16x64_bf16 v[128:131], v[164:167], v[8:15], v202\n\t" \
  "s_waitcnt lgkmcnt(4)\n\t" \
  "ds_read_b128 v[160:163], v239 offset:0\n\t" \
  "v_smfmac_f32_16x16x64_bf16 v[128:131], v[140:143], v[16:23], v202\n\t" \
  "s_waitcnt lgkmcnt(4)\n\t" \
  "ds_read_b128 v[168:171], v233 offset:8192\n\t" \
  "v_smfmac_f32_16x16x64_bf16 v[128:131], v[144:147], v[24:31], v202\n\t" \
  "s_waitcnt lgkmcnt(4)\n\t" \
  "ds_read_b128 v[172:175], v234 offset:8192\n\t" \
  "v_smfmac_f32_16x16x64_bf16 v[128:131], v[148:151], v[32:39], v202\n\t" \
  "s_waitcnt lgkmcnt(4)\n\t" \
  "ds_read_b128 v[176:179], v235 offset:8192\n\t" \
  "v_smfmac_f32_16x16x64_bf16 v[128:131], v[152:155], v[40:47], v202\n\t" \
  "s_waitcnt lgkmcnt(4)\n\t" \
  "ds_read_b128 v[180:183], v236 offset:8192\n\t" \
  "v_smfmac_f32_16x16x64_bf16 v[128:131], v[156:159], v[48:55], v202\n\t" \
  "s_waitcnt lgkmcnt(4)\n\t" \
  "ds_read_b128 v[184:187], v237 offset:8192\n\t" \
  "v_smfmac_f32_16x16x64_bf16 v[128:131], v[160:163], v[56:63], v202\n\t" \
  "s_waitcnt lgkmcnt(4)\n\t" \
  "ds_read_b128 v[188:191], v238 offset:8192\n\t" \
  "v_smfmac_f32_16x16x64_bf16 v[128:131], v[168:171], v[72:79], v202\n\t" \
  "s_waitcnt lgkmcnt(4)\n\t" \
  "ds_read_b128 v[192:195], v239 offset:8192\n\t" \
  "v_smfmac_f32_16x16x64_bf16 v[128:131], v[172:175], v[80:87], v202\n\t" \
  "s_waitcnt lgkmcnt(4)\n\t" \
  "v_smfmac_f32_16x16x64_bf16 v[128:131], v[176:179], v[88:95], v202\n\t" \
  "s_waitcnt lgkmcnt(3)\n\t" \
  "v_smfmac_f32_16x16x64_bf16 v[128:131], v[180:183], v[96:103], v202\n\t" \
  "s_waitcnt lgkmcnt(2)\n\t" \
  "v_smfmac_f32_16x16x64_bf16 v[128:131], v[184:187], v[104:111], v202\n\t" \
  "s_waitcnt lgkmcnt(1)\n\t" \
  "v_smfmac_f32_16x16x64_bf16 v[128:131], v[188:191], v[112:119], v202\n\t" \
  "s_waitcnt lgkmcnt(0)\n\t" \
  "v_smfmac_f32_16x16x64_bf16 v[128:131], v[192:195], v[120:127], v202\n\t" \
  "s_nop 7\n\t" \
  "v_add_f32 v128, v128, v130\n\t" \
  "v_add_f32_e64 v129, v129, v131\n\t" \
  "v_cvt_pk_bf16_f32 v128, v128, v129\n\t" \
  "buffer_store_dword v128, v203, %[rs], %[sv] offen sc1\n\t" \
  "ds_write_b32 v204, v128\n\t" \
  "s_waitcnt lgkmcnt(0)\n\t" \
  "s_barrier\n\t" \
  "s_mov_b32 %[spins], s92\n\t" \
  "s_branch .Lend%=\n\t" \
  ".Lslow%=:\n\t" \
  "s_mov_b64 s[94:95], 0\n\t" \
  "v_cmp_eq_u32 vcc, %[sent], v132\n\t" \
  "s_or_b64 s[94:95], s[94:95], vcc\n\t" \
  "v_cmp_eq_u32 vcc, %[sent], v133\n\t" \
  "s_or_b64 s[94:95], s[94:95], vcc\n\t" \
  "v_cmp_eq_u32 vcc, %[sent], v134\n\t" \
  "s_or_b64 s[94:95], s[94:95], vcc\n\t" \
  "v_cmp_eq_u32 vcc, %[sent], v135\n\t" \
  "s_or_b64 s[94:95], s[94:95], vcc\n\t" \
  "v_cmp_eq_u32 vcc, %[sent], v136\n\t" \
  "s_or_b64 s[94:95], s[94:95], vcc\n\t" \
  "v_cmp_eq_u32 vcc, %[sent], v137\n\t" \
  "s_or_b64 s[94:95], s[94:95], vcc\n\t" \
  "v_cmp_eq_u32 vcc, %[sent], v138\n\t" \
  "s_or_b64 s[94:95], s[94:95], vcc\n\t" \
  "v_cmp_eq_u32 vcc, %[sent], v139\n\t" \
  "s_or_b64 s[94:95], s[94:95], vcc\n\t" \
  "s_cmp_eq_u64 s[94:95], 0\n\t" \
  "s_cbranch_scc1 .Lok%=\n\t" \
  "v_mov_b64 v[128:129], 0\n\t" \
  "v_mov_b64 v[130:131], 0\n\t" \
  "s_add_u32 s92, s92, 1\n\t" \
  "s_cmp_eq_u32 s92, 1\n\t" \
  "s_cbranch_scc1 .Lpoll%=\n\t" \
  "s_sleep 2\n\t" \
  "s_and_b32 s93, s92, 63\n\t" \
  "s_cmp_lg_u32 s93, 0\n\t" \
  "s_cbranch_scc1 .Lpoll%=\n\t" \
  "v_mov_b32 v197, 0\n\t" \
  "buffer_load_dword v196, v197, %[rs], %[sab] offen sc1\n\t" \
  "s_waitcnt vmcnt(0)\n\t" \
  "v_readfirstlane_b32 s93, v196\n\t" \
  "s_nop 3\n\t" \
  "s_cmp_eq_u32 s93, 1\n\t" \
  "s_cbranch_scc1 .Lok%=\n\t" \
  "s_cmp_lt_u32 s92, 0x80000\n\t" \
  "s_cbranch_scc1 .Lpoll%=\n\t" \
  "v_mov_b32 v196, 1\n\t" \
  "buffer_store_dword v196, v197, %[rs], %[sab] offen sc1\n\t" \
  "s_branch .Lok%=\n\t" \
  ".Lend%=:\n\t"
#define TK_ASM \
  "buffer_load_dwordx4 v[224:227], v200, %[rs], %[sk] offen offset:0 sc1\n\t" \
  "buffer_load_dwordx4 v[228:231], v200, %[rs], %[sk] offen offset:1024 sc1\n\t" \
  "s_memrealtime s[88:89]\n\t" \
  "v_add_u32 v205, %[tl], v200\n\t" \
  "v_add_u32 v206, %[tq], v205\n\t" \
  "v_add_u32 v207, %[tv], v205\n\t" \
  "s_waitcnt lgkmcnt(0)\n\t" \
  "ds_read_b128 v[128:131], v206 offset:8192\n\t" \
  "ds_read_b128 v[144:147], v205 offset:0\n\t" \
  "ds_read_b128 v[160:163], v205 offset:4096\n\t" \
  "ds_read_b128 v[132:135], v206 offset:9216\n\t" \
  "ds_read_b128 v[148:151], v205 offset:1024\n\t" \
  "ds_read_b128 v[164:167], v205 offset:5120\n\t" \
  "ds_read_b128 v[136:139], v206 offset:10240\n\t" \
  "ds_read_b128 v[152:155], v205 offset:2048\n\t" \
  "ds_read_b128 v[168:171], v205 offset:6144\n\t" \
  "ds_read_b128 v[140:143], v206 offset:11264\n\t" \
  "ds_read_b128 v[156:159], v205 offset:3072\n\t" \
  "ds_read_b128 v[172:175], v205 offset:7168\n\t" \
  "v_add_u32 v206, %[vp], v218\n\t" \
  "s_lshl_b32 s88, s88, 2\n\t" \
  "s_add_u32 s89, s88, %[od]\n\t" \
  "s_waitcnt lgkmcnt(10)\n\t" \
  "v_mfma_f32_16x16x32_bf16 v[196:199], v[144:147], v[128:131], 0\n\t" \
  "s_waitcnt lgkmcnt(9)\n\t" \
  "v_mfma_f32_16x16x32_bf16 v[144:147], v[160:163], v[128:131], 0\n\t" \
  "s_waitcnt lgkmcnt(7)\n\t" \
  "v_mfma_f32_16x16x32_bf16 v[196:199], v[148:151], v[132:135], v[196:199]\n\t" \
  "s_waitcnt lgkmcnt(6)\n\t" \
  "v_mfma_f32_16x16x32_bf16 v[144:147], v[164:167], v[132:135], v[144:147]\n\t" \
  "s_waitcnt lgkmcnt(4)\n\t" \
  "v_mfma_f32_16x16x32_bf16 v[196:199], v[152:155], v[136:139], v[196:199]\n\t" \
  "s_waitcnt lgkmcnt(3)\n\t" \
  "v_mfma_f32_16x16x32_bf16 v[144:147], v[168:171], v[136:139], v[144:147]\n\t" \
  "s_waitcnt lgkmcnt(1)\n\t" \
  "v_mfma_f32_16x16x32_bf16 v[196:199], v[156:159], v[140:143], v[196:199]\n\t" \
  "s_waitcnt lgkmcnt(0)\n\t" \
  "v_mfma_f32_16x16x32_bf16 v[144:147], v[172:175], v[140:143], v[144:147]\n\t" \
  "ds_read_b128 v[176:179], v207 offset:16384\n\t" \
  "ds_read_b128 v[180:183], v207 offset:17408\n\t" \
  "ds_read_b128 v[184:187], v207 offset:18432\n\t" \
  "ds_read_b128 v[188:191], v207 offset:19456\n\t" \
  "ds_read2st64_b32 v[192:193], v206 offset0:0 offset1:1\n\t" \
  "ds_read2st64_b32 v[194:195], v206 offset0:2 offset1:3\n\t" \
  "v_cvt_pk_bf16_f32 v197, v196, v197\n\t" \
  "v_cvt_pk_bf16_f32 v199, v198, v199\n\t" \
  "v_cvt_pk_bf16_f32 v145, v144, v145\n\t" \
  "v_cvt_pk_bf16_f32 v147, v146, v147\n\t" \
  "v_lshlrev_b32 v196, 16, v197\n\t" \
  "v_lshlrev_b32 v198, 16, v199\n\t" \
  "v_lshlrev_b32 v144, 16, v145\n\t" \
  "v_lshlrev_b32 v146, 16, v147\n\t" \
  "v_and_b32 v197, 0xffff0000, v197\n\t" \
  "v_and_b32 v199, 0xffff0000, v199\n\t" \
  "v_and_b32 v145, 0xffff0000, v145\n\t" \
  "v_and_b32 v147, 0xffff0000, v147\n\t" \
  "v_pk_mul_f32 v[196:197], v[196:197], %[sc] op_sel_hi:[1,0]\n\t" \
  "v_pk_mul_f32 v[198:199], v[198:199], %[sc] op_sel_hi:[1,0]\n\t" \
  "v_pk_mul_f32 v[144:145], v[144:145], %[sc] op_sel_hi:[1,0]\n\t" \
  "v_pk_mul_f32 v[146:147], v[146:147], %[sc] op_sel_hi:[1,0]\n\t" \
  "v_cvt_pk_bf16_f32 v197, v196, v197\n\t" \
  "v_cvt_pk_bf16_f32 v199, v198, v199\n\t" \
  "v_cvt_pk_bf16_f32 v145, v144, v145\n\t" \
  "v_cvt_pk_bf16_f32 v147, v146, v147\n\t" \
  "v_lshlrev_b32 v196, 16, v197\n\t" \
  "v_lshlrev_b32 v198, 16, v199\n\t" \
  "v_lshlrev_b32 v144, 16, v145\n\t" \
  "v_lshlrev_b32 v146, 16, v147\n\t" \
  "v_and_b32 v197, 0xffff0000, v197\n\t" \
  "v_and_b32 v199, 0xffff0000, v199\n\t" \
  "v_and_b32 v145, 0xffff0000, v145\n\t" \
  "v_and_b32 v147, 0xffff0000, v147\n\t" \
  "v_min_f32 v196, v196, v208\n\t" \
  "v_min_f32 v197, v197, v209\n\t" \
  "v_min_f32 v198, v198, v210\n\t" \
  "v_min_f32 v199, v199, v211\n\t" \
  "v_min_f32 v144, v144, v212\n\t" \
  "v_min_f32 v145, v145, v213\n\t" \
  "v_min_f32 v146, v146, v214\n\t" \
  "v_min_f32 v147, v147, v215\n\t" \
  "v_max3_f32 v148, v196, v197, v198\n\t" \
  "v_max3_f32 v149, v199, v144, v145\n\t" \
  "v_max3_f32 v148, v148, v146, v147\n\t" \
  "v_max_f32 v150, v148, v149\n\t" \
  "v_max_f32 v151, v148, v149\n\t" \
  "s_nop 1\n\t" \
  "v_permlane32_swap_b32 v150, v151\n\t" \
  "v_max_f32 v150, v150, v151\n\t" \
  "s_mov_b32 s92, 0\n\t" \
  ".Ltake%=:\n\t" \
  "s_waitcnt vmcnt(1) lgkmcnt(0)\n\t" \
  "ds_write_b128 v216, v[224:227]\n\t" \
  "v_max3_u32 v148, v224, v225, v226\n\t" \
  "s_waitcnt vmcnt(0)\n\t" \
  "ds_write_b128 v216, v[228:231] offset:1024\n\t" \
  "ds_read_b64_tr_b16 v[160:161], v217 offset:0\n\t" \
  "ds_read_b64_tr_b16 v[162:163], v217 offset:64\n\t" \
  "ds_read_b64_tr_b16 v[164:165], v217 offset:512\n\t" \
  "ds_read_b64_tr_b16 v[166:167], v217 offset:576\n\t" \
  "ds_read_b64_tr_b16 v[168:169], v217 offset:1024\n\t" \
  "ds_read_b64_tr_b16 v[170:171], v217 offset:1088\n\t" \
  "ds_read_b64_tr_b16 v[172:173], v217 offset:1536\n\t" \
  "ds_read_b64_tr_b16 v[174:175], v217 offset:1600\n\t" \
  "v_max3_u32 v148, v148, v227, v228\n\t" \
  "v_max3_u32 v148, v148, v229, v230\n\t" \
  "v_max_u32 v148, v148, v231\n\t" \
  "v_cmp_le_u32 vcc, %[sent], v148\n\t" \
  "s_cbranch_vccnz .Lslow%=\n\t" \
  ".Lok%=:\n\t" \
  "s_waitcnt lgkmcnt(6)\n\t" \
  "v_mfma_f32_16x16x32_bf16 v[152:155], v[160:163], v[128:131], 0\n\t" \
  "s_waitcnt lgkmcnt(4)\n\t" \
  "v_mfma_f32_16x16x32_bf16 v[152:155], v[164:167], v[132:135], v[152:155]\n\t" \
  "s_waitcnt lgkmcnt(2)\n\t" \
  "v_mfma_f32_16x16x32_bf16 v[152:155], v[168:171], v[136:139], v[152:155]\n\t" \
  "s_waitcnt lgkmcnt(0)\n\t" \
  "v_mfma_f32_16x16x32_bf16 v[152:155], v[172:175], v[140:143], v[152:155]\n\t" \
  "v_mov_b32 v157, 0\n\t" \
  "v_mov_b32 v158, 0\n\t" \
  "s_nop 5\n\t" \
  "v_cvt_pk_bf16_f32 v153, v152, v153\n\t" \
  "v_cvt_pk_bf16_f32 v155, v154, v155\n\t" \
  "v_lshlrev_b32 v152, 16, v153\n\t" \
  "v_lshlrev_b32 v154, 16, v155\n\t" \
  "v_and_b32 v153, 0xffff0000, v153\n\t" \
  "v_and_b32 v155, 0xffff0000, v155\n\t" \
  "v_pk_mul_f32 v[152:153], v[152:153], %[sc] op_sel_hi:[1,0]\n\t" \
  "v_pk_mul_f32 v[154:155], v[154:155], %[sc] op_sel_hi:[1,0]\n\t" \
  "v_cvt_pk_bf16_f32 v153, v152, v153\n\t" \
  "v_cvt_pk_bf16_f32 v155, v154, v155\n\t" \
  "v_lshlrev_b32 v152, 16, v153\n\t" \
  "v_lshlrev_b32 v154, 16, v155\n\t" \
  "v_and_b32 v153, 0xffff0000, v153\n\t" \
  "v_and_b32 v155, 0xffff0000, v155\n\t" \
  "v_max3_f32 v148, v152, v153, v150\n\t" \
  "v_max3_f32 v149, v148, v154, v155\n\t" \
  "v_max3_f32 v150, v148, v154, v155\n\t" \
  "v_cndmask_b32 v152, v152, v154, %[up]\n\t" \
  "v_cndmask_b32 v153, v153, v155, %[up]\n\t" \
  "v_permlane16_swap_b32 v149, v150\n\t" \
  "v_max_f32 v148, v149, v150\n\t" \
  "v_mul_f32 v148, %[negc], v148\n\t" \
  "v_fma_f32 v152, v152, %[c], v148\n\t" \
  "v_fma_f32 v153, v153, %[c], v148\n\t" \
  "v_exp_f32 v152, v152\n\t" \
  "v_fma_f32 v196, v196, %[c], v148\n\t" \
  "v_exp_f32 v153, v153\n\t" \
  "v_fma_f32 v197, v197, %[c], v148\n\t" \
  "v_exp_f32 v196, v196\n\t" \
  "v_fma_f32 v198, v198, %[c], v148\n\t" \
  "v_exp_f32 v197, v197\n\t" \
  "v_fma_f32 v199, v199, %[c], v148\n\t" \
  "v_exp_f32 v198, v198\n\t" \
  "v_fma_f32 v144, v144, %[c], v148\n\t" \
  "v_pk_add_f32 v[132:133], v[152:153], v[196:197]\n\t" \
  "v_exp_f32 v199, v199\n\t" \
  "v_fma_f32 v145, v145, %[c], v148\n\t" \
  "v_exp_f32 v144, v144\n\t" \
  "v_fma_f32 v146, v146, %[c], v148\n\t" \
  "v_pk_add_f32 v[132:133], v[132:133], v[198:199]\n\t" \
  "v_exp_f32 v145, v145\n\t" \
  "v_fma_f32 v147, v147, %[c], v148\n\t" \
  "v_exp_f32 v146, v146\n\t" \
  "v_pk_add_f32 v[132:133], v[132:133], v[144:145]\n\t" \
  "v_exp_f32 v147, v147\n\t" \
  "s_nop 0\n\t" \
  "v_pk_add_f32 v[132:133], v[132:133], v[146:147]\n\t" \
  "v_add_f32 v134, v132, v133\n\t" \
  "v_add_f32 v135, v132, v133\n\t" \
  "s_nop 1\n\t" \
  "v_permlane16_swap_b32 v134, v135\n\t" \
  "v_add_f32 v132, v134, v135\n\t" \
  "v_add_f32 v133, v134, v135\n\t" \
  "s_nop 1\n\t" \
  "v_permlane32_swap_b32 v132, v133\n\t" \
  "v_add_f32 v132, v132, v133\n\t" \
  "v_rcp_f32 v148, v132\n\t" \
  "s_nop 0\n\t" \
  "v_pk_mul_f32 v[152:153], v[152:153], v[148:149] op_sel_hi:[1,0]\n\t" \
  "v_pk_mul_f32 v[196:197], v[196:197], v[148:149] op_sel_hi:[1,0]\n\t" \
  "v_pk_mul_f32 v[198:199], v[198:199], v[148:149] op_sel_hi:[1,0]\n\t" \
  "v_pk_mul_f32 v[144:145], v[144:145], v[148:149] op_sel_hi:[1,0]\n\t" \
  "v_pk_mul_f32 v[146:147], v[146:147], v[148:149] op_sel_hi:[1,0]\n\t" \
  "v_cvt_pk_bf16_f32 v156, v152, v153\n\t" \
  "v_cvt_pk_bf16_f32 v159, v152, v153\n\t" \
  "v_cvt_pk_bf16_f32 v128, v196, v197\n\t" \
  "v_cvt_pk_bf16_f32 v129, v198, v199\n\t" \
  "v_mfma_f32_16x16x16_bf16 v[160:163], v[192:193], v[156:157], 0\n\t" \
  "v_cvt_pk_bf16_f32 v130, v144, v145\n\t" \
  "v_mfma_f32_16x16x16_bf16 v[164:167], v[192:193], v[158:159], 0\n\t" \
  "v_cvt_pk_bf16_f32 v131, v146, v147\n\t" \
  "v_mfma_f32_16x16x16_bf16 v[168:171], v[194:195], v[156:157], 0\n\t" \
  "v_mfma_f32_16x16x16_bf16 v[172:175], v[194:195], v[158:159], 0\n\t" \
  "s_nop 0\n\t" \
  "v_mfma_f32_16x16x32_bf16 v[160:163], v[176:179], v[128:131], v[160:163]\n\t" \
  "s_nop 0\n\t" \
  "v_mfma_f32_16x16x32_bf16 v[164:167], v[180:183], v[128:131], v[164:167]\n\t" \
  "s_nop 0\n\t" \
  "v_mfma_f32_16x16x32_bf16 v[168:171], v[184:187], v[128:131], v[168:171]\n\t" \
  "v_mfma_f32_16x16x32_bf16 v[172:175], v[188:191], v[128:131], v[172:175]\n\t" \
  "s_nop 2\n\t" \
  "v_cvt_pk_bf16_f32 v136, v160, v161\n\t" \
  "v_cvt_pk_bf16_f32 v137, v162, v163\n\t" \
  "v_cvt_pk_bf16_f32 v138, v164, v165\n\t" \
  "v_cvt_pk_bf16_f32 v139, v166, v167\n\t" \
  "v_cvt_pk_bf16_f32 v140, v168, v169\n\t" \
  "v_cvt_pk_bf16_f32 v141, v170, v171\n\t" \
  "v_cvt_pk_bf16_f32 v142, v172, v173\n\t" \
  "v_cvt_pk_bf16_f32 v143, v174, v175\n\t" \
  "s_memrealtime s[90:91]\n\t" \
  "s_waitcnt lgkmcnt(0)\n\t" \
  "s_lshl_b32 s93, s90, 2\n\t" \
  "s_sub_i32 s93, s89, s93\n\t" \
  "s_cmp_le_i32 s93, 0\n\t" \
  "s_cbranch_scc1 .Lgoo%=\n\t" \
  "s_min_i32 s93, s93, 8000\n\t" \
  "s_mov_b32 s94, s93\n\t" \
  ".Lslo%=:\n\t" \
  "s_sub_i32 s94, s94, 18\n\t" \
  "s_cmp_lt_i32 s94, 30\n\t" \
  "s_cbranch_scc1 .Lsxo%=\n\t" \
  "s_sleep 1\n\t" \
  "s_branch .Lslo%=\n\t" \
  ".Lsxo%=:\n\t" \
  "s_add_u32 s93, s93, 3\n\t" \
  "s_lshr_b32 s93, s93, 2\n\t" \
  "s_add_u32 s93, s93, s90\n\t" \
  "s_memrealtime s[90:91]\n\t" \
  ".Lpaceo%=:\n\t" \
  "s_memrealtime s[94:95]\n\t" \
  "s_waitcnt lgkmcnt(1)\n\t" \
  "s_sub_i32 s92, s90, s93\n\t" \
  "s_cmp_ge_i32 s92, 0\n\t" \
  "s_cbranch_scc1 .Leo%=\n\t" \
  "s_memrealtime s[90:91]\n\t" \
  "s_waitcnt lgkmcnt(1)\n\t" \
  "s_sub_i32 s92, s94, s93\n\t" \
  "s_cmp_lt_i32 s92, 0\n\t" \
  "s_cbranch_scc1 .Lpaceo%=\n\t" \
  ".Leo%=:\n\t" \
  ".Lgoo%=:\n\t" \
  "buffer_store_dwordx4 v[136:139], v200, %[rt], %[so] offen offset:0 sc1\n\t" \
  "buffer_store_dwordx4 v[140:143], v200, %[rt], %[so] offen offset:1024 sc1\n\t" \
  "s_waitcnt lgkmcnt(0)\n\t" \
  "s_branch .Lend%=\n\t" \
  ".Lpoll%=:\n\t" \
  "buffer_load_dwordx4 v[224:227], v200, %[rs], %[sk] offen offset:0 sc1\n\t" \
  "buffer_load_dwordx4 v[228:231], v200, %[rs], %[sk] offen offset:1024 sc1\n\t" \
  "s_branch .Ltake%=\n\t" \
  "s_branch .Lend%=\n\t" \
  ".Lslow%=:\n\t" \
  "s_mov_b64 s[94:95], 0\n\t" \
  "v_cmp_eq_u32 vcc, %[sent], v224\n\t" \
  "s_or_b64 s[94:95], s[94:95], vcc\n\t" \
  "v_cmp_eq_u32 vcc, %[sent], v225\n\t" \
  "s_or_b64 s[94:95], s[94:95], vcc\n\t" \
  "v_cmp_eq_u32 vcc, %[sent], v226\n\t" \
  "s_or_b64 s[94:95], s[94:95], vcc\n\t" \
  "v_cmp_eq_u32 vcc, %[sent], v227\n\t" \
  "s_or_b64 s[94:95], s[94:95], vcc\n\t" \
  "v_cmp_eq_u32 vcc, %[sent], v228\n\t" \
  "s_or_b64 s[94:95], s[94:95], vcc\n\t" \
  "v_cmp_eq_u32 vcc, %[sent], v229\n\t" \
  "s_or_b64 s[94:95], s[94:95], vcc\n\t" \
  "v_cmp_eq_u32 vcc, %[sent], v230\n\t" \
  "s_or_b64 s[94:95], s[94:95], vcc\n\t" \
  "v_cmp_eq_u32 vcc, %[sent], v231\n\t" \
  "s_or_b64 s[94:95], s[94:95], vcc\n\t" \
  "s_cmp_eq_u64 s[94:95], 0\n\t" \
  "s_cbranch_scc1 .Lok%=\n\t" \
  "s_add_u32 s92, s92, 1\n\t" \
  "s_cmp_eq_u32 s92, 1\n\t" \
  "s_cbranch_scc1 .Lpoll%=\n\t" \
  "s_sleep 2\n\t" \
  "s_and_b32 s93, s92, 63\n\t" \
  "s_cmp_lg_u32 s93, 0\n\t" \
  "s_cbranch_scc1 .Lpoll%=\n\t" \
  "v_mov_b32 v149, 0\n\t" \
  "buffer_load_dword v148, v149, %[rs], %[sab] offen sc1\n\t" \
  "s_waitcnt vmcnt(0)\n\t" \
  "v_readfirstlane_b32 s93, v148\n\t" \
  "s_nop 3\n\t" \
  "s_cmp_eq_u32 s93, 1\n\t" \
  "s_cbranch_scc1 .Lok%=\n\t" \
  "s_cmp_lt_u32 s92, 0x80000\n\t" \
  "s_cbranch_scc1 .Lpoll%=\n\t" \
  "v_mov_b32 v148, 1\n\t" \
  "buffer_store_dword v148, v149, %[rs], %[sab] offen sc1\n\t" \
  "s_branch .Lok%=\n\t" \
  ".Lend%=:\n\t"
constexpr int TLAG = 2, R4 = 0;
#define RED_ASM \
  "s_mov_b32 %[step], 0\n\t" \
  "s_lshl_b32 %[sh], %[step], 15\n\t" \
  "s_lshr_b32 s93, %[step], 4\n\t" \
  "s_lshr_b32 s93, %[pm], s93\n\t" \
  "s_and_b32 s93, s93, 4\n\t" \
  "s_lshl_b32 s93, s93, 11\n\t" \
  "s_lshl2_add_u32 %[so], %[sh], s93\n\t" \
  "s_sub_i32 s93, %[step], 2\n\t" \
  "s_lshl_b32 %[st], s93, 19\n\t" \
  "s_lshl_b32 s94, s93, 11\n\t" \
  "s_xor_b32 s94, s94, %[s0]\n\t" \
  "s_and_b32 s94, s94, 0x2000\n\t" \
  "s_or_b32 %[st], %[st], s94\n\t" \
  "s_cmp_lt_i32 s93, 0\n\t" \
  "s_cselect_b32 %[st], 0x70000000, %[st]\n\t" \
  "s_add_u32 %[st4], %[st], 4096\n\t" \
  "buffer_load_dwordx2 v[166:167], v173, %[rs], 0 offen sc1\n\t" \
  "v_mov_b64 v[144:145], 0\n\t" \
  "v_mov_b64 v[146:147], 0\n\t" \
  "v_mov_b64 v[148:149], 0\n\t" \
  "v_mov_b64 v[150:151], 0\n\t" \
  "v_mov_b64 v[152:153], 0\n\t" \
  "v_mov_b64 v[154:155], 0\n\t" \
  "v_mov_b64 v[156:157], 0\n\t" \
  "v_mov_b64 v[158:159], 0\n\t" \
  ".Lstep%=:\n\t" \
  "s_setprio 3\n\t" \
  "s_mov_b32 s92, 0\n\t" \
  ".Lcal%=:\n\t" \
  "s_waitcnt vmcnt(0)\n\t" \
  "s_memrealtime s[76:77]\n\t" \
  "v_cmp_ne_u32 vcc, %[step], v166\n\t" \
  "v_readlane_b32 s90, v167, 0\n\t" \
  "v_readlane_b32 s91, v167, 1\n\t" \
  "v_readlane_b32 s94, v167, 2\n\t" \
  "v_readlane_b32 s95, v167, 3\n\t" \
  "s_sub_i32 s90, s90, %[base]\n\t" \
  "s_sub_i32 s91, s91, %[base]\n\t" \
  "s_sub_i32 s94, s94, %[base]\n\t" \
  "s_sub_i32 s95, s95, %[base]\n\t" \
  "s_max_i32 s90, s90, s91\n\t" \
  "s_max_i32 s94, s94, s95\n\t" \
  "s_max_i32 s90, s90, s94\n\t" \
  "s_add_i32 s90, s90, %[ds]\n\t" \
  "s_waitcnt lgkmcnt(0)\n\t" \
  "s_add_u32 %[E], s90, %[base]\n\t" \
  "s_and_b32 s93, vcc_lo, 0xf\n\t" \
  "s_cbranch_scc1 .Lbl%=\n\t" \
  ".Lgo%=:\n\t" \
  "v_mov_b32 v166, 7\n\t" \
  "s_mov_b64 exec, 1\n\t" \
  "v_mov_b32 v164, s90\n\t" \
  "ds_max_u32 v168, v164 offset:4100\n\t" \
  "ds_add_u32 v168, v164 offset:4096\n\t" \
  "s_mov_b64 exec, -1\n\t" \
  "s_memrealtime s[90:91]\n\t" \
  ".p2align 5\n\t" \
  "s_waitcnt lgkmcnt(0)\n\t" \
  "s_lshl_b32 s93, s90, 2\n\t" \
  "s_sub_i32 s93, %[E], s93\n\t" \
  "s_cmp_le_i32 s93, 0\n\t" \
  "s_cbranch_scc1 .Lgop%=\n\t" \
  "s_min_i32 s93, s93, 8000\n\t" \
  "s_mov_b32 s94, s93\n\t" \
  ".Lslp%=:\n\t" \
  "s_sub_i32 s94, s94, 18\n\t" \
  "s_cmp_lt_i32 s94, 30\n\t" \
  "s_cbranch_scc1 .Lsxp%=\n\t" \
  "s_sleep 1\n\t" \
  "s_branch .Lslp%=\n\t" \
  ".Lsxp%=:\n\t" \
  "s_sub_i32 s93, s93, 12\n\t" \
  "s_max_i32 s93, s93, 4\n\t" \
  "s_lshr_b32 s93, s93, 2\n\t" \
  "s_add_u32 s93, s93, s90\n\t" \
  "s_memrealtime s[90:91]\n\t" \
  ".Lpacep%=:\n\t" \
  "s_memrealtime s[94:95]\n\t" \
  "s_waitcnt lgkmcnt(1)\n\t" \
  "s_sub_i32 s92, s90, s93\n\t" \
  "s_cmp_ge_i32 s92, 0\n\t" \
  "s_cbranch_scc1 .Lep%=\n\t" \
  "s_memrealtime s[90:91]\n\t" \
  "s_waitcnt lgkmcnt(1)\n\t" \
  "s_sub_i32 s92, s94, s93\n\t" \
  "s_cmp_lt_i32 s92, 0\n\t" \
  "s_cbranch_scc1 .Lpacep%=\n\t" \
  "s_nop 4\n\t" \
  ".Lep%=:\n\t" \
  "s_add_u32 s92, s92, s93\n\t" \
  "s_lshl_b32 s92, s92, 2\n\t" \
  "s_sub_i32 s92, %[E], s92\n\t" \
  "s_max_i32 s92, s92, 0\n\t" \
  "s_bitcmp0_b32 s92, 3\n\t" \
  "s_cbranch_scc1 .Lf3p%=\n\t" \
  "s_nop 15\n\t" \
  ".Lf3p%=:\n\t" \
  "s_bitcmp0_b32 s92, 2\n\t" \
  "s_cbranch_scc1 .Lf2p%=\n\t" \
  "s_nop 10\n\t" \
  ".Lf2p%=:\n\t" \
  ".Lgop%=:\n\t" \
  "s_mov_b32 s92, 0\n\t" \
  "buffer_load_dwordx4 v[128:131], v234, %[rs], %[so] offen sc1\n\t" \
  "s_nop 15\n\t" \
  "s_nop 5\n\t" \
  "buffer_load_dwordx4 v[132:135], v235, %[rs], %[so] offen sc1\n\t" \
  "s_nop 15\n\t" \
  "s_nop 5\n\t" \
  "buffer_load_dwordx4 v[136:139], v236, %[rs], %[so] offen sc1\n\t" \
  "s_nop 15\n\t" \
  "s_nop 5\n\t" \
  "buffer_load_dwordx4 v[140:143], v237, %[rs], %[so] offen sc1\n\t" \
  ".p2align 5\n\t" \
  "s_nop 0\n\t" \
  "s_nop 0\n\t" \
  ".Lw%=:\n\t" \
  "s_waitcnt lgkmcnt(0)\n\t" \
  "s_waitcnt vmcnt(3)\n\t" \
  "v_smfmac_f32_16x16x64_bf16 v[144:147], v[128:131], v[0:7], v172\n\t" \
  "v_max3_u32 v164, v128, v129, 0\n\t" \
  "v_smfmac_f32_16x16x64_bf16 v[148:151], v[128:131], v[8:15], v172\n\t" \
  "v_max3_u32 v164, v130, v131, v164\n\t" \
  "v_smfmac_f32_16x16x64_bf16 v[152:155], v[128:131], v[16:23], v172\n\t" \
  "buffer_load_dwordx4 v[176:179], v238, %[rt], %[st] offen offset:0 sc1\n\t" \
  "v_smfmac_f32_16x16x64_bf16 v[156:159], v[128:131], v[24:31], v172\n\t" \
  "s_waitcnt vmcnt(3)\n\t" \
  "v_smfmac_f32_16x16x64_bf16 v[144:147], v[132:135], v[32:39], v172\n\t" \
  "v_max3_u32 v164, v132, v133, v164\n\t" \
  "v_smfmac_f32_16x16x64_bf16 v[148:151], v[132:135], v[40:47], v172\n\t" \
  "v_max3_u32 v164, v134, v135, v164\n\t" \
  "v_smfmac_f32_16x16x64_bf16 v[152:155], v[132:135], v[48:55], v172\n\t" \
  "buffer_load_dwordx4 v[180:183], v238, %[rt], %[st] offen offset:1024 sc1\n\t" \
  "v_smfmac_f32_16x16x64_bf16 v[156:159], v[132:135], v[56:63], v172\n\t" \
  "s_waitcnt vmcnt(3)\n\t" \
  "v_smfmac_f32_16x16x64_bf16 v[144:147], v[136:139], v[64:71], v172\n\t" \
  "v_max3_u32 v164, v136, v137, v164\n\t" \
  "v_smfmac_f32_16x16x64_bf16 v[148:151], v[136:139], v[72:79], v172\n\t" \
  "v_max3_u32 v164, v138, v139, v164\n\t" \
  "v_smfmac_f32_16x16x64_bf16 v[152:155], v[136:139], v[80:87], v172\n\t" \
  "buffer_load_dwordx4 v[184:187], v238, %[rt], %[st4] offen offset:0 sc1\n\t" \
  "v_smfmac_f32_16x16x64_bf16 v[156:159], v[136:139], v[88:95], v172\n\t" \
  "s_waitcnt vmcnt(3)\n\t" \
  "v_smfmac_f32_16x16x64_bf16 v[144:147], v[140:143], v[96:103], v172\n\t" \
  "v_max3_u32 v164, v140, v141, v164\n\t" \
  "v_smfmac_f32_16x16x64_bf16 v[148:151], v[140:143], v[104:111], v172\n\t" \
  "v_max3_u32 v164, v142, v143, v164\n\t" \
  "v_smfmac_f32_16x16x64_bf16 v[152:155], v[140:143], v[112:119], v172\n\t" \
  "v_cmp_le_u32 vcc, %[sent], v164\n\t" \
  "buffer_load_dwordx4 v[188:191], v238, %[rt], %[st4] offen offset:1024 sc1\n\t" \
  "v_smfmac_f32_16x16x64_bf16 v[156:159], v[140:143], v[120:127], v172\n\t" \
  "s_cbranch_vccnz .Lslow%=\n\t" \
  ".Lok%=:\n\t" \
  "v_add_f32_dpp v160, v144, v146 quad_perm:[0,1,2,3] row_mask:0x3 bank_mask:0xf\n\t" \
  "v_add_f32_dpp v161, v145, v147 quad_perm:[0,1,2,3] row_mask:0x3 bank_mask:0xf\n\t" \
  "v_add_f32_dpp v162, v148, v150 quad_perm:[0,1,2,3] row_mask:0x3 bank_mask:0xf\n\t" \
  "v_add_f32_dpp v163, v149, v151 quad_perm:[0,1,2,3] row_mask:0x3 bank_mask:0xf\n\t" \
  "v_add_f32_dpp v160, v152, v154 quad_perm:[0,1,2,3] row_mask:0xc bank_mask:0xf\n\t" \
  "v_add_f32_dpp v161, v153, v155 quad_perm:[0,1,2,3] row_mask:0xc bank_mask:0xf\n\t" \
  "s_nop 0\n\t" \
  "v_add_f32_dpp v162, v156, v158 quad_perm:[0,1,2,3] row_mask:0xc bank_mask:0xf\n\t" \
  "v_add_f32_dpp v163, v157, v159 quad_perm:[0,1,2,3] row_mask:0xc bank_mask:0xf\n\t" \
  "s_nop 0\n\t" \
  "v_permlane32_swap_b32 v160, v162\n\t" \
  "v_permlane32_swap_b32 v161, v163\n\t" \
  "v_add_f32 v160, v160, v162\n\t" \
  "v_add_f32_e64 v161, v161, v163\n\t" \
  "ds_write_b64 v169, v[160:161]\n\t" \
  "s_mov_b64 exec, 1\n\t" \
  "ds_inc_rtn_u32 v164, v168, v166 offset:4104\n\t" \
  "s_mov_b64 exec, -1\n\t" \
  "ds_read_b64 v[128:129], v170 offset:0\n\t" \
  "ds_read_b64 v[130:131], v170 offset:512\n\t" \
  "ds_read_b64 v[132:133], v170 offset:1024\n\t" \
  "ds_read_b64 v[134:135], v170 offset:1536\n\t" \
  "ds_read_b64 v[136:137], v170 offset:2048\n\t" \
  "ds_read_b64 v[138:139], v170 offset:2560\n\t" \
  "ds_read_b64 v[140:141], v170 offset:3072\n\t" \
  "ds_read_b64 v[142:143], v170 offset:3584\n\t" \
  "s_waitcnt lgkmcnt(8)\n\t" \
  "v_readfirstlane_b32 s93, v164\n\t" \
  "s_cmp_lg_u32 s93, 7\n\t" \
  "s_cbranch_scc1 .Lb1%=\n\t" \
  "s_waitcnt lgkmcnt(4)\n\t" \
  "v_pk_add_f32 v[128:129], v[128:129], v[130:131]\n\t" \
  "v_pk_add_f32 v[132:133], v[132:133], v[134:135]\n\t" \
  "s_waitcnt lgkmcnt(0)\n\t" \
  "v_pk_add_f32 v[136:137], v[136:137], v[138:139]\n\t" \
  "v_pk_add_f32 v[140:141], v[140:141], v[142:143]\n\t" \
  "v_pk_add_f32 v[128:129], v[128:129], v[132:133]\n\t" \
  "v_pk_add_f32 v[136:137], v[136:137], v[140:141]\n\t" \
  "v_pk_add_f32 v[128:129], v[128:129], v[136:137]\n\t" \
  "v_cvt_pk_bf16_f32 v128, v128, v129\n\t" \
  "s_nop 1\n\t" \
  "v_mov_b32_dpp v129, v128 quad_perm:[1,0,3,2] row_mask:0xf bank_mask:0xf\n\t" \
  "v_perm_b32 v128, v129, v128, v174\n\t" \
  "buffer_store_dword v128, v171, %[rs], %[sh] offen sc1\n\t" \
  "s_memtime s[90:91]\n\t" \
  "s_barrier\n\t" \
  "s_memtime s[76:77]\n\t" \
  "s_memrealtime s[78:79]\n\t" \
  "s_nop 3\n\t" \
  "s_memrealtime s[80:81]\n\t" \
  "s_nop 3\n\t" \
  "s_memrealtime s[82:83]\n\t" \
  "s_nop 3\n\t" \
  "s_memrealtime s[84:85]\n\t" \
  "s_nop 3\n\t" \
  "s_memrealtime s[86:87]\n\t" \
  "s_nop 3\n\t" \
  "s_memrealtime s[88:89]\n\t" \
  "s_mov_b32 s93, 0\n\t" \
  "s_waitcnt lgkmcnt(0)\n\t" \
  "s_cmp_eq_u32 s80, s78\n\t" \
  "s_addc_u32 s93, s93, 0\n\t" \
  "s_cmp_eq_u32 s82, s78\n\t" \
  "s_addc_u32 s93, s93, 0\n\t" \
  "s_cmp_eq_u32 s84, s78\n\t" \
  "s_addc_u32 s93, s93, 0\n\t" \
  "s_cmp_eq_u32 s86, s78\n\t" \
  "s_addc_u32 s93, s93, 0\n\t" \
  "s_cmp_eq_u32 s88, s78\n\t" \
  "s_addc_u32 s93, s93, 0\n\t" \
  "s_mul_i32 s93, s93, 21\n\t" \
  "s_add_u32 s76, s76, 15\n\t" \
  "s_add_u32 s76, s76, s93\n\t" \
  "s_lshl2_add_u32 s78, s78, 16\n\t" \
  "s_mov_b64 exec, 1\n\t" \
  "s_sub_i32 s90, s76, s90\n\t" \
  "s_mul_i32 s90, s90, 11900\n\t" \
  "s_ashr_i32 s90, s90, 16\n\t" \
  "s_sub_u32 s90, s78, s90\n\t" \
  "s_sub_u32 s90, s90, %[base]\n\t" \
  "v_mov_b32 v164, s90\n\t" \
  "ds_write_b32 v168, v164 offset:4108\n\t" \
  "s_mov_b64 exec, -1\n\t" \
  "s_waitcnt vmcnt(1)\n\t" \
  ".Lb2%=:\n\t" \
  "s_setprio 0\n\t" \
  "s_bitcmp0_b64 %[am], %[step]\n\t" \
  "s_cbranch_scc1 .La1%=\n\t" \
  "buffer_store_dwordx4 v[224:227], v228, %[ra], 0 offen sc1\n\t" \
  ".La1%=:\n\t" \
  "buffer_load_dwordx4 v[192:195], v239, %[rt], %[st] offen offset:0 sc1\n\t" \
  "buffer_load_dwordx4 v[196:199], v239, %[rt], %[st] offen offset:1024 sc1\n\t" \
  "v_mfma_f32_16x16x32_bf16 v[128:131], v[0:3], v[176:179], 0\n\t" \
  "v_mfma_f32_16x16x32_bf16 v[136:139], v[8:11], v[176:179], 0\n\t" \
  "ds_read2st64_b32 v[144:145], v231 offset0:0 offset1:8\n\t" \
  "ds_read2st64_b32 v[146:147], v231 offset0:16 offset1:24\n\t" \
  "ds_read2st64_b32 v[148:149], v231 offset0:32 offset1:40\n\t" \
  "ds_read2st64_b32 v[150:151], v231 offset0:48 offset1:56\n\t" \
  "buffer_load_dwordx4 v[200:203], v239, %[rt], %[st4] offen offset:0 sc1\n\t" \
  "buffer_load_dwordx4 v[204:207], v239, %[rt], %[st4] offen offset:1024 sc1\n\t" \
  "v_mfma_f32_16x16x32_bf16 v[132:135], v[4:7], v[180:183], 0\n\t" \
  "v_mfma_f32_16x16x32_bf16 v[140:143], v[12:15], v[180:183], 0\n\t" \
  "s_waitcnt lgkmcnt(0)\n\t" \
  "v_add_f32 v144, v144, v145\n\t" \
  "v_add_f32 v146, v146, v147\n\t" \
  "v_add_f32 v148, v148, v149\n\t" \
  "v_add_f32 v150, v150, v151\n\t" \
  "v_add_f32 v144, v144, v146\n\t" \
  "v_add_f32 v148, v148, v150\n\t" \
  "buffer_load_dwordx4 v[208:211], v240, %[rt], %[st] offen offset:0 sc1\n\t" \
  "buffer_load_dwordx4 v[212:215], v240, %[rt], %[st] offen offset:1024 sc1\n\t" \
  "v_mfma_f32_16x16x32_bf16 v[128:131], v[16:19], v[184:187], v[128:131]\n\t" \
  "v_mfma_f32_16x16x32_bf16 v[136:139], v[24:27], v[184:187], v[136:139]\n\t" \
  "v_add_f32 v144, v144, v148\n\t" \
  "v_cvt_pk_bf16_f32 v144, v144, v144\n\t" \
  "ds_write_b16 v232, v144\n\t" \
  "ds_read_b128 v[224:227], v229\n\t" \
  "v_add_u32 v228, %[ainc], v228\n\t" \
  "v_add_u32 v229, %[rinc], v229\n\t" \
  "v_add_u32 v232, 1024, v232\n\t" \
  "v_xor_b32 v231, 0x4000, v231\n\t" \
  "buffer_load_dwordx4 v[216:219], v240, %[rt], %[st4] offen offset:0 sc1\n\t" \
  "buffer_load_dwordx4 v[220:223], v240, %[rt], %[st4] offen offset:1024 sc1\n\t" \
  "v_mfma_f32_16x16x32_bf16 v[132:135], v[20:23], v[188:191], v[132:135]\n\t" \
  "v_mfma_f32_16x16x32_bf16 v[140:143], v[28:31], v[188:191], v[140:143]\n\t" \
  "buffer_load_dwordx4 v[176:179], v241, %[rt], %[st] offen offset:0 sc1\n\t" \
  "s_waitcnt vmcnt(8)\n\t" \
  "v_mfma_f32_16x16x32_bf16 v[128:131], v[32:35], v[192:195], v[128:131]\n\t" \
  "v_mfma_f32_16x16x32_bf16 v[136:139], v[40:43], v[192:195], v[136:139]\n\t" \
  "buffer_load_dwordx4 v[180:183], v241, %[rt], %[st] offen offset:1024 sc1\n\t" \
  "s_waitcnt vmcnt(8)\n\t" \
  "v_mfma_f32_16x16x32_bf16 v[132:135], v[36:39], v[196:199], v[132:135]\n\t" \
  "v_mfma_f32_16x16x32_bf16 v[140:143], v[44:47], v[196:199], v[140:143]\n\t" \
  "buffer_load_dwordx4 v[184:187], v241, %[rt], %[st4] offen offset:0 sc1\n\t" \
  "s_waitcnt vmcnt(8)\n\t" \
  "v_mfma_f32_16x16x32_bf16 v[128:131], v[48:51], v[200:203], v[128:131]\n\t" \
  "v_mfma_f32_16x16x32_bf16 v[136:139], v[56:59], v[200:203], v[136:139]\n\t" \
  "ds_read_b128 v[160:163], v168 offset:4096\n\t" \
  "buffer_load_dwordx4 v[188:191], v241, %[rt], %[st4] offen offset:1024 sc1\n\t" \
  "s_waitcnt vmcnt(8)\n\t" \
  "v_mfma_f32_16x16x32_bf16 v[132:135], v[52:55], v[204:207], v[132:135]\n\t" \
  "v_mfma_f32_16x16x32_bf16 v[140:143], v[60:63], v[204:207], v[140:143]\n\t" \
  "s_waitcnt lgkmcnt(0)\n\t" \
  "v_readfirstlane_b32 s90, v161\n\t" \
  "v_readfirstlane_b32 s91, v163\n\t" \
  "v_readfirstlane_b32 s94, v160\n\t" \
  "s_sub_u32 s93, s94, %[sm]\n\t" \
  "s_mov_b32 %[sm], s94\n\t" \
  "s_lshr_b32 s93, s93, 3\n\t" \
  "s_sub_i32 s93, s90, s93\n\t" \
  "s_mul_i32 s93, s93, %[wm]\n\t" \
  "s_ashr_i32 s93, s93, 2\n\t" \
  "s_min_u32 s93, s93, %[gm]\n\t" \
  "s_sub_i32 s90, s90, s93\n\t" \
  "s_add_u32 s93, s90, %[rl]\n\t" \
  "s_add_u32 s93, s93, %[base]\n\t" \
  "s_add_u32 %[E], s93, %[lk]\n\t" \
  "s_sub_i32 s91, s91, s90\n\t" \
  "s_sub_i32 s91, s91, %[rl]\n\t" \
  "s_min_i32 s93, s91, %[up]\n\t" \
  "s_max_i32 s93, s93, %[dn]\n\t" \
  "s_cmp_gt_i32 s91, 60\n\t" \
  "s_cselect_b32 s93, 2, s93\n\t" \
  "s_add_i32 %[rl], %[rl], s93\n\t" \
  "v_mov_b32 v160, %[rl]\n\t" \
  "buffer_store_dword v160, v175, %[rs], 0 offen sc1\n\t" \
  "s_waitcnt vmcnt(8)\n\t" \
  "v_mfma_f32_16x16x32_bf16 v[128:131], v[64:67], v[208:211], v[128:131]\n\t" \
  "v_mfma_f32_16x16x32_bf16 v[136:139], v[72:75], v[208:211], v[136:139]\n\t" \
  "s_add_u32 %[step], %[step], 1\n\t" \
  "s_lshl_b32 %[sh], %[step], 15\n\t" \
  "s_lshr_b32 s93, %[step], 4\n\t" \
  "s_lshr_b32 s93, %[pm], s93\n\t" \
  "s_and_b32 s93, s93, 4\n\t" \
  "s_lshl_b32 s93, s93, 11\n\t" \
  "s_lshl2_add_u32 %[so], %[sh], s93\n\t" \
  "s_waitcnt vmcnt(7)\n\t" \
  "v_mfma_f32_16x16x32_bf16 v[132:135], v[68:71], v[212:215], v[132:135]\n\t" \
  "v_mfma_f32_16x16x32_bf16 v[140:143], v[76:79], v[212:215], v[140:143]\n\t" \
  "s_sub_i32 s93, %[step], 2\n\t" \
  "s_lshl_b32 %[st], s93, 19\n\t" \
  "s_lshl_b32 s94, s93, 11\n\t" \
  "s_xor_b32 s94, s94, %[s0]\n\t" \
  "s_and_b32 s94, s94, 0x2000\n\t" \
  "s_or_b32 %[st], %[st], s94\n\t" \
  "s_cmp_lt_i32 s93, 0\n\t" \
  "s_cselect_b32 %[st], 0x70000000, %[st]\n\t" \
  "s_add_u32 %[st4], %[st], 4096\n\t" \
  "s_waitcnt vmcnt(6)\n\t" \
  "v_mfma_f32_16x16x32_bf16 v[128:131], v[80:83], v[216:219], v[128:131]\n\t" \
  "v_mfma_f32_16x16x32_bf16 v[136:139], v[88:91], v[216:219], v[136:139]\n\t" \
  "s_waitcnt vmcnt(5)\n\t" \
  "v_mfma_f32_16x16x32_bf16 v[132:135], v[84:87], v[220:223], v[132:135]\n\t" \
  "v_mfma_f32_16x16x32_bf16 v[140:143], v[92:95], v[220:223], v[140:143]\n\t" \
  "s_cmp_eq_u32 %[step], 63\n\t" \
  "s_cbranch_scc1 .Llast%=\n\t" \
  "s_movk_i32 s93, 600\n\t" \
  ".Lpacel%=:\n\t" \
  "s_memrealtime s[90:91]\n\t" \
  "s_waitcnt lgkmcnt(0)\n\t" \
  "s_lshl_b32 s90, s90, 2\n\t" \
  "s_sub_i32 s91, %[E], s90\n\t" \
  "s_cmp_le_i32 s91, 0\n\t" \
  "s_cbranch_scc1 .Lgol%=\n\t" \
  "s_sub_u32 s93, s93, 1\n\t" \
  "s_cmp_lg_u32 s93, 0\n\t" \
  "s_cbranch_scc1 .Lpacel%=\n\t" \
  ".Lgol%=:\n\t" \
  "buffer_load_dwordx2 v[166:167], v173, %[rs], 0 offen sc1\n\t" \
  "s_waitcnt vmcnt(5)\n\t" \
  "v_mfma_f32_16x16x32_bf16 v[128:131], v[96:99], v[176:179], v[128:131]\n\t" \
  "v_mfma_f32_16x16x32_bf16 v[136:139], v[104:107], v[176:179], v[136:139]\n\t" \
  "s_waitcnt vmcnt(4)\n\t" \
  "v_mfma_f32_16x16x32_bf16 v[132:135], v[100:103], v[180:183], v[132:135]\n\t" \
  "v_mfma_f32_16x16x32_bf16 v[140:143], v[108:111], v[180:183], v[140:143]\n\t" \
  "s_waitcnt vmcnt(3)\n\t" \
  "v_mfma_f32_16x16x32_bf16 v[128:131], v[112:115], v[184:187], v[128:131]\n\t" \
  "v_mfma_f32_16x16x32_bf16 v[136:139], v[120:123], v[184:187], v[136:139]\n\t" \
  "s_waitcnt vmcnt(2)\n\t" \
  "v_mfma_f32_16x16x32_bf16 v[132:135], v[116:119], v[188:191], v[132:135]\n\t" \
  "v_mfma_f32_16x16x32_bf16 v[140:143], v[124:127], v[188:191], v[140:143]\n\t" \
  "v_mov_b64 v[144:145], 0\n\t" \
  "v_mov_b64 v[146:147], 0\n\t" \
  "v_mov_b64 v[148:149], 0\n\t" \
  "v_mov_b64 v[150:151], 0\n\t" \
  "v_mov_b64 v[152:153], 0\n\t" \
  "v_mov_b64 v[154:155], 0\n\t" \
  "v_mov_b64 v[156:157], 0\n\t" \
  "v_mov_b64 v[158:159], 0\n\t" \
  "v_pk_add_f32 v[128:129], v[128:129], v[132:133]\n\t" \
  "v_pk_add_f32 v[130:131], v[130:131], v[134:135]\n\t" \
  "ds_write_b128 v230, v[128:131] offset:0\n\t" \
  "v_pk_add_f32 v[136:137], v[136:137], v[140:141]\n\t" \
  "v_pk_add_f32 v[138:139], v[138:139], v[142:143]\n\t" \
  "ds_write_b128 v230, v[136:139] offset:1024\n\t" \
  "v_xor_b32 v230, 0x4000, v230\n\t" \
  "s_cmp_lg_u32 %[step], 63\n\t" \
  "s_cbranch_scc1 .Lstep%=\n\t" \
  ".Lfc%=:\n\t" \
  "s_waitcnt vmcnt(0)\n\t" \
  "v_cmp_eq_u32 vcc, -1, v166\n\t" \
  "s_cbranch_vccnz .Lfw%=\n\t" \
  ".Lfo%=:\n\t" \
  "v_add_u32 v242, 16384, v242\n\t" \
  "buffer_load_dwordx4 v[176:179], v238, %[rt], %[st] offen offset:0 sc1\n\t" \
  "buffer_load_dwordx4 v[180:183], v238, %[rt], %[st] offen offset:1024 sc1\n\t" \
  "buffer_load_dwordx4 v[184:187], v238, %[rt], %[st4] offen offset:0 sc1\n\t" \
  "buffer_load_dwordx4 v[188:191], v238, %[rt], %[st4] offen offset:1024 sc1\n\t" \
  "buffer_load_dwordx4 v[192:195], v239, %[rt], %[st] offen offset:0 sc1\n\t" \
  "buffer_load_dwordx4 v[196:199], v239, %[rt], %[st] offen offset:1024 sc1\n\t" \
  "buffer_load_dwordx4 v[200:203], v239, %[rt], %[st4] offen offset:0 sc1\n\t" \
  "buffer_load_dwordx4 v[204:207], v239, %[rt], %[st4] offen offset:1024 sc1\n\t" \
  "v_subrev_u32 v230, 98304, v230\n\t" \
  ".Lt%=:\n\t" \
  "s_waitcnt vmcnt(7)\n\t" \
  "v_mfma_f32_16x16x32_bf16 v[128:131], v[0:3], v[176:179], 0\n\t" \
  "v_mfma_f32_16x16x32_bf16 v[136:139], v[8:11], v[176:179], 0\n\t" \
  "buffer_load_dwordx4 v[176:179], v240, %[rt], %[st] offen offset:0 sc1\n\t" \
  "s_waitcnt vmcnt(7)\n\t" \
  "v_mfma_f32_16x16x32_bf16 v[132:135], v[4:7], v[180:183], 0\n\t" \
  "v_mfma_f32_16x16x32_bf16 v[140:143], v[12:15], v[180:183], 0\n\t" \
  "buffer_load_dwordx4 v[180:183], v240, %[rt], %[st] offen offset:1024 sc1\n\t" \
  "s_waitcnt vmcnt(7)\n\t" \
  "v_mfma_f32_16x16x32_bf16 v[128:131], v[16:19], v[184:187], v[128:131]\n\t" \
  "v_mfma_f32_16x16x32_bf16 v[136:139], v[24:27], v[184:187], v[136:139]\n\t" \
  "buffer_load_dwordx4 v[184:187], v240, %[rt], %[st4] offen offset:0 sc1\n\t" \
  "buffer_load_dword v166, v242, %[rs], 0 offen sc1\n\t" \
  "s_waitcnt vmcnt(8)\n\t" \
  "v_mfma_f32_16x16x32_bf16 v[132:135], v[20:23], v[188:191], v[132:135]\n\t" \
  "v_mfma_f32_16x16x32_bf16 v[140:143], v[28:31], v[188:191], v[140:143]\n\t" \
  "buffer_load_dwordx4 v[188:191], v240, %[rt], %[st4] offen offset:1024 sc1\n\t" \
  "s_waitcnt vmcnt(8)\n\t" \
  "v_mfma_f32_16x16x32_bf16 v[128:131], v[32:35], v[192:195], v[128:131]\n\t" \
  "v_mfma_f32_16x16x32_bf16 v[136:139], v[40:43], v[192:195], v[136:139]\n\t" \
  "buffer_load_dwordx4 v[192:195], v241, %[rt], %[st] offen offset:0 sc1\n\t" \
  "s_waitcnt vmcnt(8)\n\t" \
  "v_mfma_f32_16x16x32_bf16 v[132:135], v[36:39], v[196:199], v[132:135]\n\t" \
  "v_mfma_f32_16x16x32_bf16 v[140:143], v[44:47], v[196:199], v[140:143]\n\t" \
  "buffer_load_dwordx4 v[196:199], v241, %[rt], %[st] offen offset:1024 sc1\n\t" \
  "s_waitcnt vmcnt(8)\n\t" \
  "v_mfma_f32_16x16x32_bf16 v[128:131], v[48:51], v[200:203], v[128:131]\n\t" \
  "v_mfma_f32_16x16x32_bf16 v[136:139], v[56:59], v[200:203], v[136:139]\n\t" \
  "buffer_load_dwordx4 v[200:203], v241, %[rt], %[st4] offen offset:0 sc1\n\t" \
  "s_waitcnt vmcnt(8)\n\t" \
  "v_mfma_f32_16x16x32_bf16 v[132:135], v[52:55], v[204:207], v[132:135]\n\t" \
  "v_mfma_f32_16x16x32_bf16 v[140:143], v[60:63], v[204:207], v[140:143]\n\t" \
  "buffer_load_dwordx4 v[204:207], v241, %[rt], %[st4] offen offset:1024 sc1\n\t" \
  "s_waitcnt vmcnt(8)\n\t" \
  "v_mfma_f32_16x16x32_bf16 v[128:131], v[64:67], v[176:179], v[128:131]\n\t" \
  "v_mfma_f32_16x16x32_bf16 v[136:139], v[72:75], v[176:179], v[136:139]\n\t" \
  "s_waitcnt vmcnt(5)\n\t" \
  ".Ltc%=:\n\t" \
  "v_cmp_eq_u32 vcc, -1, v166\n\t" \
  "s_cbranch_vccnz .Ltw%=\n\t" \
  ".Lto%=:\n\t" \
  "s_cmp_eq_u32 %[step], 65\n\t" \
  "s_cbranch_scc1 .Ltl%=\n\t" \
  "s_add_u32 %[step], %[step], 1\n\t" \
  "s_sub_i32 s93, %[step], 2\n\t" \
  "s_lshl_b32 %[st], s93, 19\n\t" \
  "s_lshl_b32 s94, s93, 11\n\t" \
  "s_xor_b32 s94, s94, %[s0]\n\t" \
  "s_and_b32 s94, s94, 0x2000\n\t" \
  "s_or_b32 %[st], %[st], s94\n\t" \
  "s_cmp_lt_i32 s93, 0\n\t" \
  "s_cselect_b32 %[st], 0x70000000, %[st]\n\t" \
  "s_add_u32 %[st4], %[st], 4096\n\t" \
  "v_add_u32 v242, 16384, v242\n\t" \
  "s_cmp_eq_u32 %[step], 65\n\t" \
  "s_cbranch_scc0 .Ltn%=\n\t" \
  "v_mov_b32 v242, %[fg]\n\t" \
  ".Ltn%=:\n\t" \
  "buffer_load_dwordx4 v[176:179], v238, %[rt], %[st] offen offset:0 sc1\n\t" \
  "s_waitcnt vmcnt(7)\n\t" \
  "v_mfma_f32_16x16x32_bf16 v[132:135], v[68:71], v[180:183], v[132:135]\n\t" \
  "v_mfma_f32_16x16x32_bf16 v[140:143], v[76:79], v[180:183], v[140:143]\n\t" \
  "buffer_load_dwordx4 v[180:183], v238, %[rt], %[st] offen offset:1024 sc1\n\t" \
  "s_waitcnt vmcnt(7)\n\t" \
  "v_mfma_f32_16x16x32_bf16 v[128:131], v[80:83], v[184:187], v[128:131]\n\t" \
  "v_mfma_f32_16x16x32_bf16 v[136:139], v[88:91], v[184:187], v[136:139]\n\t" \
  "buffer_load_dwordx4 v[184:187], v238, %[rt], %[st4] offen offset:0 sc1\n\t" \
  "s_waitcnt vmcnt(7)\n\t" \
  "v_mfma_f32_16x16x32_bf16 v[132:135], v[84:87], v[188:191], v[132:135]\n\t" \
  "v_mfma_f32_16x16x32_bf16 v[140:143], v[92:95], v[188:191], v[140:143]\n\t" \
  "buffer_load_dwordx4 v[188:191], v238, %[rt], %[st4] offen offset:1024 sc1\n\t" \
  "s_waitcnt vmcnt(7)\n\t" \
  "v_mfma_f32_16x16x32_bf16 v[128:131], v[96:99], v[192:195], v[128:131]\n\t" \
  "v_mfma_f32_16x16x32_bf16 v[136:139], v[104:107], v[192:195], v[136:139]\n\t" \
  "buffer_load_dwordx4 v[192:195], v239, %[rt], %[st] offen offset:0 sc1\n\t" \
  "s_waitcnt vmcnt(7)\n\t" \
  "v_mfma_f32_16x16x32_bf16 v[132:135], v[100:103], v[196:199], v[132:135]\n\t" \
  "v_mfma_f32_16x16x32_bf16 v[140:143], v[108:111], v[196:199], v[140:143]\n\t" \
  "buffer_load_dwordx4 v[196:199], v239, %[rt], %[st] offen offset:1024 sc1\n\t" \
  "s_waitcnt vmcnt(7)\n\t" \
  "v_mfma_f32_16x16x32_bf16 v[128:131], v[112:115], v[200:203], v[128:131]\n\t" \
  "v_mfma_f32_16x16x32_bf16 v[136:139], v[120:123], v[200:203], v[136:139]\n\t" \
  "buffer_load_dwordx4 v[200:203], v239, %[rt], %[st4] offen offset:0 sc1\n\t" \
  "s_waitcnt vmcnt(7)\n\t" \
  "v_mfma_f32_16x16x32_bf16 v[132:135], v[116:119], v[204:207], v[132:135]\n\t" \
  "v_mfma_f32_16x16x32_bf16 v[140:143], v[124:127], v[204:207], v[140:143]\n\t" \
  "buffer_load_dwordx4 v[204:207], v239, %[rt], %[st4] offen offset:1024 sc1\n\t" \
  "s_nop 5\n\t" \
  "v_pk_add_f32 v[128:129], v[128:129], v[132:133]\n\t" \
  "v_pk_add_f32 v[130:131], v[130:131], v[134:135]\n\t" \
  "ds_write_b128 v230, v[128:131] offset:0\n\t" \
  "v_pk_add_f32 v[136:137], v[136:137], v[140:141]\n\t" \
  "v_pk_add_f32 v[138:139], v[138:139], v[142:143]\n\t" \
  "ds_write_b128 v230, v[136:139] offset:1024\n\t" \
  "v_add_u32 v230, 16384, v230\n\t" \
  "s_branch .Lt%=\n\t" \
  ".Ltl%=:\n\t" \
  "buffer_load_dwordx4 v[224:227], v243, %[rg], 0 offen sc1\n\t" \
  "s_waitcnt vmcnt(7)\n\t" \
  "v_mfma_f32_16x16x32_bf16 v[132:135], v[68:71], v[180:183], v[132:135]\n\t" \
  "v_mfma_f32_16x16x32_bf16 v[140:143], v[76:79], v[180:183], v[140:143]\n\t" \
  "s_waitcnt vmcnt(6)\n\t" \
  "v_mfma_f32_16x16x32_bf16 v[128:131], v[80:83], v[184:187], v[128:131]\n\t" \
  "v_mfma_f32_16x16x32_bf16 v[136:139], v[88:91], v[184:187], v[136:139]\n\t" \
  "s_waitcnt vmcnt(5)\n\t" \
  "v_mfma_f32_16x16x32_bf16 v[132:135], v[84:87], v[188:191], v[132:135]\n\t" \
  "v_mfma_f32_16x16x32_bf16 v[140:143], v[92:95], v[188:191], v[140:143]\n\t" \
  "s_waitcnt vmcnt(4)\n\t" \
  "v_mfma_f32_16x16x32_bf16 v[128:131], v[96:99], v[192:195], v[128:131]\n\t" \
  "v_mfma_f32_16x16x32_bf16 v[136:139], v[104:107], v[192:195], v[136:139]\n\t" \
  "s_waitcnt vmcnt(3)\n\t" \
  "v_mfma_f32_16x16x32_bf16 v[132:135], v[100:103], v[196:199], v[132:135]\n\t" \
  "v_mfma_f32_16x16x32_bf16 v[140:143], v[108:111], v[196:199], v[140:143]\n\t" \
  "s_waitcnt vmcnt(2)\n\t" \
  "v_mfma_f32_16x16x32_bf16 v[128:131], v[112:115], v[200:203], v[128:131]\n\t" \
  "v_mfma_f32_16x16x32_bf16 v[136:139], v[120:123], v[200:203], v[136:139]\n\t" \
  "s_waitcnt vmcnt(1)\n\t" \
  "v_mfma_f32_16x16x32_bf16 v[132:135], v[116:119], v[204:207], v[132:135]\n\t" \
  "v_mfma_f32_16x16x32_bf16 v[140:143], v[124:127], v[204:207], v[140:143]\n\t" \
  "s_nop 6\n\t" \
  "v_pk_add_f32 v[128:129], v[128:129], v[132:133]\n\t" \
  "v_pk_add_f32 v[130:131], v[130:131], v[134:135]\n\t" \
  "ds_write_b128 v230, v[128:131] offset:0\n\t" \
  "v_pk_add_f32 v[136:137], v[136:137], v[140:141]\n\t" \
  "v_pk_add_f32 v[138:139], v[138:139], v[142:143]\n\t" \
  "ds_write_b128 v230, v[136:139] offset:1024\n\t" \
  "s_waitcnt vmcnt(0) lgkmcnt(0)\n\t" \
  "s_branch .Lend%=\n\t" \
  ".Ltw%=:\n\t" \
  "s_add_u32 s92, s92, 1\n\t" \
  "s_sleep 2\n\t" \
  "s_and_b32 s93, s92, 63\n\t" \
  "s_cbranch_scc1 .Ltr%=\n\t" \
  "buffer_load_dword v164, off, %[rs], %[sab] sc1\n\t" \
  "s_waitcnt vmcnt(0)\n\t" \
  "v_readfirstlane_b32 s93, v164\n\t" \
  "s_nop 3\n\t" \
  "s_cmp_eq_u32 s93, 1\n\t" \
  "s_cbranch_scc1 .Lto%=\n\t" \
  "s_cmp_lt_u32 s92, 0x80000\n\t" \
  "s_cbranch_scc1 .Ltr%=\n\t" \
  "v_mov_b32 v164, 1\n\t" \
  "buffer_store_dword v164, off, %[rs], %[sab] sc1\n\t" \
  "s_waitcnt vmcnt(0)\n\t" \
  "s_branch .Lto%=\n\t" \
  ".Ltr%=:\n\t" \
  "buffer_load_dword v166, v242, %[rs], 0 offen sc1\n\t" \
  "s_waitcnt vmcnt(0)\n\t" \
  "s_branch .Ltc%=\n\t" \
  ".Llast%=:\n\t" \
  "v_mov_b32 v173, v242\n\t" \
  "s_branch .Lgol%=\n\t" \
  ".Lfw%=:\n\t" \
  "s_add_u32 s92, s92, 1\n\t" \
  "s_sleep 2\n\t" \
  "s_and_b32 s93, s92, 63\n\t" \
  "s_cbranch_scc1 .Lfr%=\n\t" \
  "buffer_load_dword v164, off, %[rs], %[sab] sc1\n\t" \
  "s_waitcnt vmcnt(0)\n\t" \
  "v_readfirstlane_b32 s93, v164\n\t" \
  "s_nop 3\n\t" \
  "s_cmp_eq_u32 s93, 1\n\t" \
  "s_cbranch_scc1 .Lfo%=\n\t" \
  "s_cmp_lt_u32 s92, 0x80000\n\t" \
  "s_cbranch_scc1 .Lfr%=\n\t" \
  "v_mov_b32 v164, 1\n\t" \
  "buffer_store_dword v164, off, %[rs], %[sab] sc1\n\t" \
  "s_waitcnt vmcnt(0)\n\t" \
  "s_branch .Lfo%=\n\t" \
  ".Lfr%=:\n\t" \
  "buffer_load_dwordx2 v[166:167], v173, %[rs], 0 offen sc1\n\t" \
  "s_branch .Lfc%=\n\t" \
  ".Lb1%=:\n\t" \
  "s_waitcnt vmcnt(0) lgkmcnt(0)\n\t" \
  "s_barrier\n\t" \
  "s_branch .Lb2%=\n\t" \
  ".Lbl%=:\n\t" \
  "s_bitset1_b64 %[ps], %[step]\n\t" \
  "s_cmp_lt_u32 s92, 3\n\t" \
  "s_cbranch_scc1 .Lbm%=\n\t" \
  "s_sleep 12\n\t" \
  ".Lbm%=:\n\t" \
  "s_add_u32 s92, s92, 1\n\t" \
  "s_sleep 2\n\t" \
  "s_and_b32 s93, s92, 63\n\t" \
  "s_cbranch_scc1 .Lba%=\n\t" \
  "buffer_load_dword v164, off, %[rs], %[sab] sc1\n\t" \
  "s_waitcnt vmcnt(0)\n\t" \
  "v_readfirstlane_b32 s93, v164\n\t" \
  "s_nop 3\n\t" \
  "s_cmp_eq_u32 s93, 1\n\t" \
  "s_cbranch_scc1 .Lgo%=\n\t" \
  "s_cmp_lt_u32 s92, 0x80000\n\t" \
  "s_cbranch_scc1 .Lba%=\n\t" \
  "v_mov_b32 v164, 1\n\t" \
  "buffer_store_dword v164, off, %[rs], %[sab] sc1\n\t" \
  "s_waitcnt vmcnt(0)\n\t" \
  "s_branch .Lgo%=\n\t" \
  ".Lba%=:\n\t" \
  "buffer_load_dwordx2 v[166:167], v173, %[rs], 0 offen sc1\n\t" \
  "s_branch .Lcal%=\n\t" \
  ".Lslow%=:\n\t" \
  "s_mov_b64 s[94:95], 0\n\t" \
  "v_cmp_eq_u32 vcc, %[sent], v128\n\t" \
  "s_or_b64 s[94:95], s[94:95], vcc\n\t" \
  "v_cmp_eq_u32 vcc, %[sent], v129\n\t" \
  "s_or_b64 s[94:95], s[94:95], vcc\n\t" \
  "v_cmp_eq_u32 vcc, %[sent], v130\n\t" \
  "s_or_b64 s[94:95], s[94:95], vcc\n\t" \
  "v_cmp_eq_u32 vcc, %[sent], v131\n\t" \
  "s_or_b64 s[94:95], s[94:95], vcc\n\t" \
  "v_cmp_eq_u32 vcc, %[sent], v132\n\t" \
  "s_or_b64 s[94:95], s[94:95], vcc\n\t" \
  "v_cmp_eq_u32 vcc, %[sent], v133\n\t" \
  "s_or_b64 s[94:95], s[94:95], vcc\n\t" \
  "v_cmp_eq_u32 vcc, %[sent], v134\n\t" \
  "s_or_b64 s[94:95], s[94:95], vcc\n\t" \
  "v_cmp_eq_u32 vcc, %[sent], v135\n\t" \
  "s_or_b64 s[94:95], s[94:95], vcc\n\t" \
  "v_cmp_eq_u32 vcc, %[sent], v136\n\t" \
  "s_or_b64 s[94:95], s[94:95], vcc\n\t" \
  "v_cmp_eq_u32 vcc, %[sent], v137\n\t" \
  "s_or_b64 s[94:95], s[94:95], vcc\n\t" \
  "v_cmp_eq_u32 vcc, %[sent], v138\n\t" \
  "s_or_b64 s[94:95], s[94:95], vcc\n\t" \
  "v_cmp_eq_u32 vcc, %[sent], v139\n\t" \
  "s_or_b64 s[94:95], s[94:95], vcc\n\t" \
  "v_cmp_eq_u32 vcc, %[sent], v140\n\t" \
  "s_or_b64 s[94:95], s[94:95], vcc\n\t" \
  "v_cmp_eq_u32 vcc, %[sent], v141\n\t" \
  "s_or_b64 s[94:95], s[94:95], vcc\n\t" \
  "v_cmp_eq_u32 vcc, %[sent], v142\n\t" \
  "s_or_b64 s[94:95], s[94:95], vcc\n\t" \
  "v_cmp_eq_u32 vcc, %[sent], v143\n\t" \
  "s_or_b64 s[94:95], s[94:95], vcc\n\t" \
  "s_cbranch_scc0 .Lok%=\n\t" \
  "s_bitset1_b64 %[fm], %[step]\n\t" \
  "s_add_u32 s92, s92, 1\n\t" \
  "s_sleep 1\n\t" \
  "s_and_b32 s93, s92, 63\n\t" \
  "s_cbranch_scc1 .Lre%=\n\t" \
  "buffer_load_dword v164, off, %[rs], %[sab] sc1\n\t" \
  "s_waitcnt vmcnt(0)\n\t" \
  "v_readfirstlane_b32 s93, v164\n\t" \
  "s_nop 3\n\t" \
  "s_cmp_eq_u32 s93, 1\n\t" \
  "s_cbranch_scc1 .Lok%=\n\t" \
  "s_cmp_lt_u32 s92, 0x80000\n\t" \
  "s_cbranch_scc1 .Lre%=\n\t" \
  "v_mov_b32 v164, 1\n\t" \
  "buffer_store_dword v164, off, %[rs], %[sab] sc1\n\t" \
  "s_waitcnt vmcnt(0)\n\t" \
  "s_branch .Lok%=\n\t" \
  ".Lre%=:\n\t" \
  "buffer_load_dwordx4 v[128:131], v234, %[rs], %[so] offen sc1\n\t" \
  "s_nop 15\n\t" \
  "s_nop 5\n\t" \
  "buffer_load_dwordx4 v[132:135], v235, %[rs], %[so] offen sc1\n\t" \
  "s_nop 15\n\t" \
  "s_nop 5\n\t" \
  "buffer_load_dwordx4 v[136:139], v236, %[rs], %[so] offen sc1\n\t" \
  "s_nop 15\n\t" \
  "s_nop 5\n\t" \
  "buffer_load_dwordx4 v[140:143], v237, %[rs], %[so] offen sc1\n\t" \
  "v_mov_b64 v[144:145], 0\n\t" \
  "v_mov_b64 v[146:147], 0\n\t" \
  "v_mov_b64 v[148:149], 0\n\t" \
  "v_mov_b64 v[150:151], 0\n\t" \
  "v_mov_b64 v[152:153], 0\n\t" \
  "v_mov_b64 v[154:155], 0\n\t" \
  "v_mov_b64 v[156:157], 0\n\t" \
  "v_mov_b64 v[158:159], 0\n\t" \
  "s_waitcnt vmcnt(0)\n\t" \
  "s_branch .Lw%=\n\t" \
  ".Lend%=:\n\t"


__device__ __forceinline__ int look_prog(__amdgpu_buffer_rsrc_t rs, int off) {
  asm volatile("" ::: "memory");
  const int v = (int)ld_flag(rs, off);
  return min(__builtin_amdgcn_readlane(v, 0), __builtin_amdgcn_readlane(v, 1));
}
__device__ __forceinline__ int wait_prog(__amdgpu_buffer_rsrc_t rs, int off, int need, gu32* abortw) {
  int v;
  for (unsigned spins = 0;;) {
    v = look_prog(rs, off);
    if (v >= need) break;
    __builtin_amdgcn_s_sleep(4);
    if (spin_fail(spins, abortw)) break;
  }
  asm volatile("" ::: "memory");
  return v;
}
#define CLB_S "s90", "s91", "s92", "s93", "s94", "s95", "vcc", "scc", "memory"
#define W_IN "{v[0:31]}"(W[0]), "{v[32:63]}"(W[1]), "{v[64:95]}"(W[2]), "{v[96:127]}"(W[3]), "{v[128:159]}"(W[4]), "{v[160:191]}"(W[5])

#define LEAD0 402
#define LEAD_N0 5
#define LEAD_SH 2
#define LEAD_X0 8
#define LEAD_U0 24
#define LEAD_D0 0
#define T0_BACK 1000
#define QBJ 3
constexpr int L_MEM = 0, L_KT = 16384, L_QX = 24576, L_KX = 26624, L_VX = 16384, L_MISC = 30720, L_WL = 32768;

#define QK_STEP(TXT)                                                                                                        \
  asm volatile(TXT                                                                                                          \
      : [tout] "=&s"(tout), [spins] "=&s"(spins), [tb1] "=&s"(tb1), [tgt] "=&s"(tgt), "+{v248}"(aL16), "+{v249}"(aMW), "+{v250}"(aWL),                            \
        "+{v251}"(aIDX), "+{v252}"(aQW), "+{v[232:235]}"(QF)                                                                 \
      : W_IN, [rs] "s"(rs4), [som] "s"(o_mem(pm, s) + WV * 1024), [som2] "s"(o_mem(pm, s) + WV * 1024 + 16384), [sent] "s"(DSENT), [sab] "s"(SY_ABORT), \
        [lead] "s"(lead), [step] "s"(s), [sbc] "s"(SY_BA + side(pm, 1) + h * 128), [nh] "s"(0xFF00FF00FF00FF00ull), [one] "s"(0x3F803F803F803F80ull), [up] "s"(0xFFFFFFFF00000000ull), [negc] "s"(-cexp),      \
        [c] "s"(cexp), [so] "s"(o_o(pm, s, h) + WV * 512), [sk] "s"(o_v(pm, s, h) + 2048 + (WV & 3) * 512),                    \
        [sv] "s"(o_v(pm, s, h) + WV * 512), [rt] "s"(rt4), [svt] "s"(t_off(p, T_V + kseg(s, h)) + WV * 2048)                  \
      : CLB8(192, 193, 194, 195, 196, 197, 198, 199), CLB8(200, 201, 202, 203, 204, 205, 206, 207),                          \
        CLB8(208, 209, 210, 211, 212, 213, 214, 215), CLB8(216, 217, 218, 219, 220, 221, 222, 223),                          \
        CLB8(224, 225, 226, 227, 228, 229, 230, 231), CLB8(236, 237, 238, 239, 240, 241, 242, 243), "v244", "v245", "v246", "v247", "v253", "s88", "s89", CLB_S)

template <int WV>
__device__ __forceinline__ void qk_wave(Args& p, int h, char* smem) {
  const int tid = threadIdx.x;
  unsigned pm = 0;
  int have = -1;
  u32x32 W[6];
  {
    int lane = tid & 63;
    asm volatile("" : "+v"(lane));
    const unsigned vo = (WV * 4 + (lane >> 4)) * (HD * 2) + 16 * ((lane & 15) ^ swzB((WV * 4 + (lane >> 4)) & 15));
    auto stage = [&](int j) {
#pragma unroll
      for (int mm = 0; mm < 2; ++mm) {
        const char* src = (const char*)((mm ? p.Wk : p.Wq) + (size_t)j * 128 * HD + h * DH);
        const unsigned dst = lds_off(smem) + (~j & 1) * 65536 + mm * 32768 + WV * 1024;
        glds2(src, 32 * HD * 2, vo, dst);
        if (TH_Q < 63) HOLD_V((h & 3) ^ 1, TH_Q);
        glds2(src + 64 * HD * 2, 32 * HD * 2, vo, dst + 16384);
        if (TH_Q < 63) HOLD_V((h & 3) ^ 1, TH_Q);
      }
    };
    bf16x8 keep[4][4];
    int f0 = -1;
    auto place = [&](int g) {
#pragma unroll
      for (int i = 0; i < 4; ++i)
        *reinterpret_cast<bf16x8*>(smem + (g == 0 ? 65536 : g == 1 ? 32768 : g == 2 ? 131072 : 98304) + WV * 4096 + i * 1024 + lane * 16) = keep[g][i];
    };
    stage(0); stage(1);
    Boot bt;
#pragma unroll
    for (int j = 0; j < 8; ++j) {
      if (j < 7) WAIT_V(8); else WAIT_V(0);
      if (WV == 7 && j == QBJ + 1) pm = boot_done(p.ws, bt);
      if ((WV == 4 || WV == 5) && j == 7) {
        asm volatile("" : "+v"(f0));
        have = min(__builtin_amdgcn_readlane(f0, 0), __builtin_amdgcn_readlane(f0, 1));
      }
      LBAR();
#pragma unroll
      for (int k = 0; k < 8; ++k) {
        const int P = (QK_MAP[WV] >> (4 * (2 * j + (k >> 2)))) & 15;
        const bf16x8 fr = ld_bfrag(smem + (~j & 1) * 65536 + (WV >> 2) * 32768 + (k >> 2) * 16384, k & 1, 2 * (WV & 3) + ((k >> 1) & 1), lane);
        const int f = P * 4 + (k & 3);
        if (P < 12) put4(W[f >> 3], f & 7, fr);
        else keep[P - 12][k & 3] = fr;
      }
      if (j == (WV >> 1)) place(2);
      LBAR();
      if (WV == 7 && j == QBJ) boot_ask(p.ws, bt);
      if (j + 2 < 8) stage(j + 2);
      if (j == 6) {
        if (WV == 4 || WV == 5)
          asm volatile("buffer_load_dword %0, %1, %2, 0 offen sc1" : "=&v"(f0) : "v"(fkq(h, (lane & 1) * 2)), "s"(raw_rsrc(p.ws + WS_SY, SY_TOTAL)) : "memory");
        place(0); place(3);
      }
    }
    place(1);
    if (WV == 7 && lane < 8) lds_at(smem + L_MISC)[lane] = lane == 7 ? pm : (lane == 1 ? now_q() - T0_BACK : 0);
    if (WV == 6) lds_at(smem + 28672)[lane] = ~0u;
    LBAR();
    pm = uni(lds_at(smem + L_MISC)[7]);
    p.s0 = pm >> 31;
    if (WV == 4 || WV == 5) {
      if (have < 0) have = wait_prog(mkrsrc(p.ws + WS_SY, SY_TOTAL), fkq(h, (lane & 1) * 2), 0, (gu32*)(p.ws + WS_SY + SY_ABORT));
      const char* src = p.ws + WS_T + t_off(p, kseg(0, h)) + (WV - 4) * 4096 + lane * 16;
#pragma unroll
      for (int i = 0; i < 4; ++i) glds16_sc1(src + i * 1024, smem + L_KT + (WV - 4) * 4096 + i * 1024);
    }
  }
  const __amdgpu_buffer_rsrc_t rsy = mkrsrc(p.ws + WS_SY, SY_TOTAL);
  const u32x4 rs4 = raw_rsrc(p.ws + WS_SY, SY_TOTAL);
  const u32x4 rt4 = raw_rsrc(p.ws + WS_T, T_BYTES);
  gu32* abortw = (gu32*)(p.ws + WS_SY + SY_ABORT);
  const float cexp = 0.08838834764831845f * 1.4426950408889634f;
  constexpr int m = WV >> 2, b = WV & 3;
  int lead = LEAD0;
  unsigned tb1 = 0;
  unsigned long long fm = 0, ps = 0; (void)fm; (void)ps;
  for (int s = 0; s < NSEG; ++s) {
    int lane = tid & 63;
    asm volatile("" : "+v"(lane));
    const int r = lane & 15, q = lane >> 4;
    unsigned aL16 = lane * 16, aMW = aL16 + WV * 1024, aWL = aL16 + 98304 + WV * 4096;
    unsigned aIDX = (lane & 2) ? 0xEEEE : 0x4444;
    unsigned aQW = (32 * b + r) * 16 + q * 4 + (m ? L_KX : L_QX);
    u32x4 QF;
    asm volatile("" : "={v[232:235]}"(QF));
    if (WV < 4) {
      QF[0] = L_QX + (q * 8 + (r >> 2)) * 16 + (r & 1) * 8;
      QF[1] = ((2 * (q & 1) + (q >> 1)) * 16 + r) * 4;
      QF[2] = ((r >> 2) & 1) * 65536 + ((r >> 1) & 1) * 64 + (r >> 3) * 32 + (r & 1) * 16 + (q >> 1) * 256 + (q & 1) * 8;
    } else QF[3] = r * 16 + q * 4;
    unsigned tout, spins, tgt;
    if (WV == 0) QK_STEP(QK_ASM_0); else if (WV == 1) QK_STEP(QK_ASM_1); else if (WV == 2) QK_STEP(QK_ASM_2);
    else if (WV == 3) QK_STEP(QK_ASM_3); else if (WV == 4) QK_STEP(QK_ASM_4); else if (WV == 5) QK_STEP(QK_ASM_5);
    else if (WV == 6) QK_STEP(QK_ASM_6); else QK_STEP(QK_ASM_7);
    tout = __builtin_amdgcn_readfirstlane(tout);
    tb1 = __builtin_amdgcn_readfirstlane(tb1);
    lane = tid & 63;
    asm volatile("" : "+v"(lane));
    if (WV < 4) {
      lds_at(smem + L_MISC + 8)[WV] = s + 1;
      if (WV == 3) { lds_at(smem + L_MISC)[6] = time_of(tout); lds_at(smem + 28672)[2] = s; }
    }
    if (WV == 6) {
      if (s + 1 < NSEG) watch(rsy, s, lane, now_q(), (const unsigned*)(smem + L_MISC), abortw, side(pm, 1));
    } else if (WV >= 4 && s + 1 < NSEG) {
      unsigned t;
      do {
        __builtin_amdgcn_s_sleep(1);
        t = lds_at(smem + L_MISC + 8)[lane & 3];
      } while (!__all(t == (unsigned)(s + 1)));
      if (WV <= 5) {
        if (s + 2 < NSEG) {
          if (have < s + 1) have = wait_prog(rsy, fkq(h, (lane & 1) * 2), s + 1, abortw);
          const char* src = p.ws + WS_T + t_off(p, kseg(s + 1, h)) + (WV - 4) * 4096 + lane * 16;
#pragma unroll
          for (int i = 0; i < 4; ++i) glds16_sc1(src + i * 1024, smem + L_KT + (WV - 4) * 4096 + i * 1024);
          if (have < s + 2) have = look_prog(rsy, fkq(h, (lane & 1) * 2));
        }
      } else {
        do __builtin_amdgcn_s_sleep(2); while (__builtin_amdgcn_readfirstlane(lds_at(smem + 28672)[2]) != (unsigned)s);
        const int x = (int)(__builtin_amdgcn_readfirstlane(lds_at(smem + L_MISC)[6]) - tb1);
        lead = __builtin_amdgcn_readfirstlane(track(lead, x + (s < LEAD_N0 ? LEAD_X0 : 0), s < LEAD_N0 ? LEAD_U0 : LEAD_UP, s < LEAD_N0 ? LEAD_D0 : (s < 3 * LEAD_N0 ? min(max(LEAD_DN, (lead - x) >> LEAD_SH), 16) : LEAD_DN), 60));
      }
    }
  }
}

__device__ __forceinline__ void qk_role(Args& p, int h, char* smem) {
  switch (__builtin_amdgcn_readfirstlane(threadIdx.x >> 6)) {
    case 0: qk_wave<0>(p, h, smem); break;
    case 1: qk_wave<1>(p, h, smem); break;
    case 2: qk_wave<2>(p, h, smem); break;
    case 3: qk_wave<3>(p, h, smem); break;
    case 4: qk_wave<4>(p, h, smem); break;
    case 5: qk_wave<5>(p, h, smem); break;
    case 6: qk_wave<6>(p, h, smem); break;
    default: qk_wave<7>(p, h, smem); break;
  }
}

#define K_DUE 50
#define K_DUE2 500
#define T_SL 2
#define O_DUE 400

constexpr int V_VM = 16384, V_T = 32768, V_KI = 131072;
#define TK_CALL(sg, od_) \
  asm volatile(TK_ASM \
            : \
            : "{v200}"(lane * 16), "{v[208:215]}"(bias), "{v[216:219]}"(ta), \
              [rs] "s"(rs4), [rt] "s"(raw_rsrc(p.ws + WS_T, T_BYTES)), [sent] "s"(DSENT), [sab] "s"(SY_ABORT), [sk] "s"(o_v(pm, sg, h) + 2048), \
              [tl] "s"(V_T + (sg & 3) * 24576), [vp] "s"((sg & 1) * 2048), [tq] "s"((w & 1) * 4096), [tv] "s"((w & 2) * 2048), \
              [so] "s"(t_off_o(p, T_O + (sg * 2 + (w & 1)) * 16 + (h >> 1)) + (h & 1) * 4096 + (w & 2) * 1024), \
              [up] "s"(0xFFFFFFFF00000000ull), [c] "s"(1.4426950408889634f), [negc] "s"(-1.4426950408889634f), \
              [sc] "s"((unsigned long long)__float_as_uint(0.08838834764831845f)), [od] "s"(od_) \
            : CLB32(128), CLB32(160), CLB8(192, 193, 194, 195, 196, 197, 198, 199), CLB8(224, 225, 226, 227, 228, 229, 230, 231), \
              "v205", "v206", "v207", "s88", "s89", CLB_S)
__device__ __forceinline__ void vu_role(Args& p, int h, char* smem) {
  const int tid = threadIdx.x, lane = tid & 63;
  const int w = __builtin_amdgcn_readfirstlane(tid >> 6);
  u32x32 W[4];
  {
    const unsigned vo = (tid >> 4) * (HD * 2) + 16 * ((lane & 15) ^ swzB((tid >> 4) & 15));
    auto stage = [&](int j) {
      const char* src = (const char*)(p.Wv + (size_t)j * 128 * HD + h * DH);
      const unsigned dst = lds_off(smem) + L_WL + (j & 3) * 32768 + w * 1024;
      glds2(src, 32 * HD * 2, vo, dst);
      glds2(src + 64 * HD * 2, 32 * HD * 2, vo, dst + 16384);
    };
    start_sy(p.ws, p.mem0, h, smem);
    Boot bt;
#pragma unroll
    for (int u = 0; u < 2; ++u) {
#pragma unroll
      for (int j = 0; j < 4; ++j) stage(4 * u + j);
      if (u && w == 7) boot_ask(p.ws, bt);
      WAIT_V(0);
      if (u && w == 7) {
        const unsigned m = boot_done(p.ws, bt);
        if (lane < 8) lds_at(smem + L_MISC)[lane] = lane == 7 ? m : (lane == 1 ? now_q() : 0);
      }
      LBAR();
#pragma unroll
      for (int k = 0; k < 16; ++k) put4(W[2 * u + (k >> 3)], k & 7, ld_bfrag(smem + L_WL + ((k >> 1) ^ w) * 16384, k & 1, w, lane));
      LBAR();
    }
  }
  const __amdgpu_buffer_rsrc_t rsy = mkrsrc(p.ws + WS_SY, SY_TOTAL);
  const u32x4 rs4 = raw_rsrc(p.ws + WS_SY, SY_TOTAL);
  gu32* abortw = (gu32*)(p.ws + WS_SY + SY_ABORT);
  const unsigned pm = uni(lds_at(smem + L_MISC)[7]);
  p.s0 = pm >> 31;
  unsigned long long fm = 0; (void)fm;
  int have = -1;
  auto fetch = [&](int sg) {
    if (have < sg) have = wait_prog(rsy, fkq(h, w - 4), sg, abortw);
    const char* src = p.ws + WS_T + t_off(p, (w - 4) * T_Q + kseg(sg, h)) + lane * 16;
#pragma unroll
    for (int i = 0; i < 8; ++i) glds16_sc1(src + i * 1024, smem + V_T + (sg & 3) * 24576 + (w - 4) * 8192 + i * 1024);
  };
  const int fo = SY_FO + ((w & 3) * NH + h) * 128;
  typedef __attribute__((ext_vector_type(8))) float f32x8;
  f32x8 bias;
  typedef __attribute__((ext_vector_type(8))) unsigned u32x8;
  u32x8 ca;
#pragma unroll
  for (int i = 0; i < 8; ++i) ca[i] = lane * 16 + (i ^ w) * 1024;
  u32x4 ta;
  {
    const int r = lane & 15, q = lane >> 4;
#pragma unroll
    for (int j = 0; j < 8; ++j) bias[j] = (j >> 2) * 16 + 4 * q + (j & 3) <= 16 * (w & 1) + r ? INFINITY : -INFINITY;
    ta[0] = V_KI + w * 2048 + lane * 16;
    ta[1] = V_KI + w * 2048 + (q * 8 + (r >> 2)) * 16 + (r & 1) * 8;
    ta[2] = V_VM + (w & 2) * 512 + ((2 * (q & 1) + (q >> 1)) * 16 + r) * 4;
    ta[3] = 0;
  }
#pragma unroll 1
  for (int s = 0; s < NSEG; ++s) {
    unsigned spins, tgt;
    asm volatile(VV_ASM
        : [spins] "=&s"(spins), [tgt] "=&s"(tgt)
        : W4_IN, "{v200}"(lane * 16), "{v[232:239]}"(ca), "{v202}"((lane & 2) ? 0xEEEE : 0x4444), "{v203}"(lane * 4),
          "{v204}"(tid * 4 + V_VM + (s & 1) * 2048), [rs] "s"(rs4), [som] "s"(o_mem(pm, s) + w * 1024), [som2] "s"(o_mem(pm, s) + w * 1024 + 16384), [step] "s"(s),
          [sent] "s"(DSENT), [sab] "s"(SY_ABORT), [sv] "s"(o_v(pm, s, h) + w * 256)
        : CLB32(128), CLB32(160), CLB8(192, 193, 194, 195, 196, 197, 198, 199), CLB_S);
    if (w < 4 || s == NSEG - 1) {
      const int sg = s - (w < 4);
      if (w >= 4) {
        if (lane == 0) __builtin_amdgcn_raw_buffer_store_b32(1u, rsy, fo + 4 * NH * 128, 0, 16);
        pace_until(now_q() + K_DUE);
      }
      if (sg >= 0 && sg != NSEG - 2) {
        TK_CALL(sg, s < NSEG - 1 ? O_DUE : 0);
        if (sg >= NSEG - 3) {
          static_assert(TLAG == 2, "");
          WAIT_V(0);
          if (lane == 0) __builtin_amdgcn_raw_buffer_store_b32(1u, rsy, fo + (sg - NSEG + 3) * 4 * NH * 128, 0, 16);
        }
      }
    } else if (w == 7) {
      watch(rsy, s, lane, now_q(), (const unsigned*)(smem + L_MISC), abortw, side(pm, 1));
    } else if (w >= 4) {
      __builtin_amdgcn_s_sleep(T_SL);
      const int s1 = w == 5 && s < NSEG - 3 ? s : s + 1;
#pragma unroll 1
      for (int sg = (w == 5 ? s < NSEG - 2 : s == 0) ? s : s + 1; sg <= s1; ++sg) fetch(sg);
      if (have < s1 + 1 && s1 + 1 < NSEG) have = look_prog(rsy, fkq(h, w - 4));
      if (w == 5 && s + NPRE < NSEG) {
#pragma unroll
        for (int i = 0; i < 4; ++i) st_sc1(rsy, o_v(pm, s + NPRE, h) + i * 1024 + lane * 16, (u32x4){DSENT, DSENT, DSENT, DSENT});
      }
    }
    if (s == NSEG - 2 && w >= 4) {
      pace_until(tgt + K_DUE2);
      TK_CALL(s, 0);
    }
  }
}

#define RL_UP 6
#define RL_DN 4
#define RED_MIN 40
#define BA_LOOK 360
constexpr int R_SI = 32768, R_PART = 98304;

__device__ __forceinline__ void red_role(Args& p, int j, char* smem) {
  const int tid = threadIdx.x;
  int lane = tid & 63;
  const int w = __builtin_amdgcn_readfirstlane(tid >> 6);
  const int c = (j & 2) * 8 + (j >> 2), rh = j & 1;
  const int hg = (w + (j >> 2)) & 7, rot = (j >> 2) + (j >> 5);
  const __amdgpu_buffer_rsrc_t rsy = mkrsrc(p.ws + WS_SY, SY_TOTAL);
  const u32x4 rs4 = raw_rsrc(p.ws + WS_SY, SY_TOTAL);
  const __amdgpu_buffer_rsrc_t rt = mkrsrc(p.ws + WS_T, T_BYTES);
  gu32* abortw = (gu32*)(p.ws + WS_SY + SY_ABORT);
  u32x32 W[4];
  u32x2 tag = {0u, 0u};
  {
    const u16* Wo = p.Wo + (size_t)(4 * hg * DH) * DM + 32 * c;
    const int ro = (lane >> 2) * 64 + (lane & 3) * 8;
    Boot bt;
    load_blocks<16>(smem + 32768 + w * 16384,
        [&](int i, char* slot) {
          if (i == 12 && w == 0) boot_ask(p.ws, bt);
          if ((i < 2 || i == 4 || i == 8) && (c & 14) != 6 && lane < 10) {
            const int x = i < 2 ? 3 - i : i >> 3, n = min(((c & 15) - ((c & 8) >> 2)) * 10 + lane, 127);
            touch_line((const char*)p.Wo + (size_t)(w * 512 + (x >> 1) * 256 + (x & 1) * 64 + (n & 64) + n) * (DM * 2) + (c & 16) * 64 + 384, smem + w * 256);
          }
          const int x = ((i >> 2) + rot) & 3;
          blk_dma(Wo + (size_t)((x >> 1) * 256 + (x & 1) * 64 + ((i >> 1) & 1) * 128 + (i & 1) * 32) * DM, DM, slot, lane);
        },
        [&](int i, char* slot) {
#pragma unroll
          for (int t = 0; t < 2; ++t) {
            s16x4 lo = __builtin_amdgcn_ds_read_tr16_b64_v4i16((lds_s16x4_ptr)(slot + ro + t * 32));
            s16x4 hi = __builtin_amdgcn_ds_read_tr16_b64_v4i16((lds_s16x4_ptr)(slot + ro + t * 32 + 1024));
            put4(W[i >> 2], (i & 2) * 2 + 2 * t + (i & 1), (bf16x8){lo[0], lo[1], lo[2], lo[3], hi[0], hi[1], hi[2], hi[3]});
          }
        });
    if (w == 0) { lds_at(smem + 4128)[0] = boot_done(p.ws, bt); tag = next_tag(bt.e); }
    LBAR();
  }
  const unsigned pm = uni(lds_at(smem + 4128)[0]);
  p.s0 = pm >> 31;
  const int hs = side(pm, 1);
  {
    const u32x4 ds = {DSENT, DSENT, DSENT, DSENT};
#pragma unroll
    for (int i = 0; i < 2; ++i) {
      const int s = (w * 2 + i) * 4 + (lane >> 4);
      if (s >= 2) st_sc1(rsy, o_mem(pm, s) + (c >> 4) * 16384 + (c & 15) * 512 + rh * 128 + ((lane >> 3) & 1) * 256 + (lane & 7) * 16, ds);
    }
  }
  if (tid < 4) { reinterpret_cast<unsigned*>(smem + 4096)[tid] = tid == 3 ? now_q() : 0; reinterpret_cast<unsigned*>(smem + 4112)[tid] = DSENT; }
  if (tid < 256) reinterpret_cast<int*>(smem + 5120)[tid] = ((tid >> 2) - TLAG - 6) >> 2;
  LBAR();
  const unsigned base = __builtin_amdgcn_readfirstlane(lds_at(smem + 4096)[3]);
  unsigned long long fm = 0, ps = 0;
  const unsigned aL = lane * 16;
  const int e = ((56 + w) * SEG + rh * 16 + (lane >> 2)) * DM + c * 32 + (lane & 3) * 8;
  u32x4 xv = *reinterpret_cast<const u32x4*>(p.x + e), av = {DSENT, DSENT, DSENT, DSENT};
  asm volatile("" : "+v"(xv));
  {
    const int r = lane & 15, q = lane >> 4;
    const int sm0 = o_mem(pm, 0) + (c >> 4) * 16384 + (c & 15) * 512 + rh * 128;
    u32x4 VO, VT;
    unsigned fo = SY_FO + ((rh + (lane & 4) / 2) * NH + 4 * hg + (lane & 3)) * 128;
    unsigned ao = w == 1 ? (c * S_LEN + rh * 16 - (TLAG + 3) * SEG) * 64 + lane * 16
                         : (w >= 4 ? SY_O + NPRE * 131072 + side(pm, w - 2) + (j >> 3) * 16384 + (j & 7) * 1024 + lane * 16
                                   : SY_FS + (2 * c + rh) * 128 + (lane < 8 ? lane * 16 : 0x70000000));
    unsigned ar = w == 1 ? R_SI - (TLAG + 2) * 1024 + lane * 16 : (w == 2 ? 5120 : 4112);
    const unsigned long long am = w == 1 ? ((1ull << 56) - 1) << (TLAG + 3) : (w == 2 ? 0x11111111111111ull << (TLAG + 7) : (w >= 4 ? (0xFFFFull << (16 * (w - 4))) >> NPRE : 0));
    unsigned pw = R_PART + w * 2048 + lane * 16, pr = R_PART + 16384 + tid * 4;
    unsigned sw = R_SI - (TLAG + 1) * 1024 + (lane >> 2) * 64 + w * 8 + (lane & 3) * 2, stm = 8192 + w * 512;
#pragma unroll
    for (int l = 0; l < 4; ++l) {
      const int x = (l + rot) & 3;
      VO[l] = o_o(0, 0, 4 * hg) + rh * 65536 + x * 1024 + aL;
      VT[l] = (T_O + rh * 16 + 2 * hg + (x >> 1)) * 16384 + (x & 1) * 2048 + aL;
    }
    unsigned ba = lane < 4 ? SY_BA + hs + (4 * hg + lane) * 128 : 0x7F000000u;
    unsigned sm = 0;
    int rl = RL0;
    unsigned step, E, so, st, st4, sh;
    asm volatile(RED_ASM
        : [step] "=&s"(step), [E] "=&s"(E), [so] "=&s"(so), [st] "=&s"(st), [st4] "=&s"(st4), [sh] "=&s"(sh),
          [rl] "+s"(rl), [sm] "+s"(sm), [fm] "+s"(fm), [ps] "+s"(ps),
          "+{v[224:227]}"(av), "+{v228}"(ao), "+{v229}"(ar), "+{v230}"(pw), "+{v231}"(pr), "+{v232}"(sw), "+{v233}"(stm), "+{v242}"(fo), "+{v173}"(ba)
        : W4_IN, "{v168}"(aL), "{v169}"(R4 ? (w >> 1) * 1024 + lane * 16 + (w & 1) * 8 : w * 512 + lane * 8), "{v170}"(lane * 8), "{v174}"((r & 1) ? 0x03020706u : 0x05040100u),
          "{v171}"(sm0 + 32768 + (q >> 1) * 256 + (q & 1) * 64 + ((r >> 1) & 1) * 32 + (r & 1) * 16 + (r >> 2) * 4),
          "{v172}"((lane & 2) ? 0xEEEE : 0x4444),
          "{v175}"(w == 3 && lane < 32 ? SY_BR + hs + j * 128 + lane * 4 : 0x7F000000u), "{v[234:237]}"(VO), "{v[238:241]}"(VT), "{v243}"(e * 2),
          [rg] "s"(raw_rsrc(p.ws + WS_G, S_LEN * DM * 2)), [fg] "s"(SY_FG + ((14 + (w >> 2)) * 8 + (c >> 2)) * 128),
          [rs] "s"(rs4), [ra] "s"(w == 1 ? raw_rsrc(p.ws + WS_S, S_LEN * DM * 2) : rs4), [rt] "s"(raw_rsrc(p.ws + WS_T, T_BYTES)),
          [ds] "n"(DS_RED), [wm] "n"(REF_W), [gm] "n"(REF_G), [mn] "n"(RED_MIN), [lk] "n"(DS_MEM + BA_LOOK), [up] "n"(RL_UP), [dn] "n"(-RL_DN), [sent] "s"(DSENT), [sab] "s"(SY_ABORT),
          [base] "s"(base), [pm] "s"(pm), [s0] "s"((p.s0 ^ TF_O) << 13), [am] "s"(am), [ainc] "s"(w == 1 ? SEG * 64 : (w >= 4 ? 131072 : 0)),
          [rinc] "s"(w == 1 ? 1024 : (w == 2 ? 16 : 0))
        : CLB32(128), CLB8(160, 161, 162, 163, 164, 165, 166, 167), CLB8(176, 177, 178, 179, 180, 181, 182, 183),
          CLB8(184, 185, 186, 187, 188, 189, 190, 191), CLB8(192, 193, 194, 195, 196, 197, 198, 199), CLB8(200, 201, 202, 203, 204, 205, 206, 207),
          CLB8(208, 209, 210, 211, 212, 213, 214, 215), CLB8(216, 217, 218, 219, 220, 221, 222, 223), CLB_E, "s96", CLB_S);
  }
  LBAR();
  u32x4 sv;
  if (w < 6 - TLAG) sv = *reinterpret_cast<const u32x4*>(smem + R_SI + (56 + w) * 1024 + lane * 16);
  else {
    const f32x4* part = reinterpret_cast<const f32x4*>(smem + (w == 6 - TLAG ? R_PART : (w - 6 + TLAG) * 16384)) +
                        (lane & 2) * 32 + (lane & 1) * 32 + (lane >> 2);
    f32x4 x[8][2];
#pragma unroll
    for (int ww = 0; ww < 8; ++ww) { x[ww][0] = part[ww * 128]; x[ww][1] = part[ww * 128 + 16]; }
#pragma unroll
    for (int u = 0; u < 2; ++u) {
      const f32x4 a = ((x[0][u] + x[1][u]) + (x[2][u] + x[3][u])) + ((x[4][u] + x[5][u]) + (x[6][u] + x[7][u]));
      sv[2 * u] = cvtpk(a[0], a[1]); sv[2 * u + 1] = cvtpk(a[2], a[3]);
    }
  }
  st_out(p, e, combine(sv, av, xv));
  if (j == 0 && tid == 0) st2_sc1(rsy, SY_EP, tag);
}


__global__ __launch_bounds__(512, 2) void main_kernel(Args p) {
  char* smem = g_smem;
  p.ws = ws_align(p.ws);
  const int bx = blockIdx.x & 7, bk = blockIdx.x >> 3;
  const bool isg = (bx ^ XPROBE) & 4;
  const int b = (isg ? NCHAIN : 0) + bk * 4 + (bx & 3);
  if (threadIdx.x < 64) {
    unsigned long long pc; unsigned len, keep;
    asm volatile("s_getpc_b64 %0\n.Lpc%=:\n\ts_mov_b32 %1, .Lcode_end-.Lpc%=" : "=s"(pc), "=s"(len));
    asm volatile("s_mov_b32 %0, m0\n\ts_mov_b32 m0, %3\n\ts_nop 0\n\tbuffer_load_dword %1, %2, 0 offen lds\n\ts_mov_b32 m0, %0"
                 : "=&s"(keep) : "v"((bk * 40 + threadIdx.x) * 128), "s"(raw_rsrc((const void*)pc, len)), "s"((b >> 6) == 1 ? 2048 : 163328) : "memory");
  }
  if ((bk >> 3) == (isg ? 3 : 1) && (threadIdx.x >> 6) == 1) {
    constexpr int NI = TLB_NI + 1, NW = (int)(WS_END / TLB_WS);
    const int n = (threadIdx.x & 63) * 8 + (bk & 7), m = n / NI, i = n % NI;
    const u16* q = m == 0 ? p.Wq : m == 1 ? p.Wk : m == 2 ? p.Wv : m == 3 ? p.Wo : m == 4 ? p.wg : m == 5 ? p.wu : m == 6 ? p.wd : m == 7 ? p.x : p.out;
    static_assert(9 * NI + NW <= 512, "");
    if (n < 9 * NI + NW)
      touch_line(m < 9 ? (const char*)q + min(i * (DM * HD * 2 / TLB_NI), (m < 4 ? HD : m < 7 ? FF : S_LEN) * DM * 2 - 128)
                       : p.ws + (size_t)(n - 9 * NI + 1) * TLB_WS - 16384, smem + 163072);
  }
  if (b >= NCHAIN) gemm_role(p, b - NCHAIN, smem);
  else if (b < 32) qk_role(p, (b & 3) * 8 + bk, smem);
  else if (b < 64) vu_role(p, (b & 3) * 8 + bk - 8, smem);
  else red_role(p, b - 64, smem);
}

extern "C" void kernel_launch(void* const* d_in, const int* in_sizes, int n_in,
                              void* d_out, int out_size, void* d_ws, size_t ws_size,
                              hipStream_t stream) {
  Args a;
  a.x = (const u16*)d_in[0]; a.mem0 = (const u16*)d_in[1];
  a.Wq = (const u16*)d_in[2]; a.Wk = (const u16*)d_in[3]; a.Wv = (const u16*)d_in[4]; a.Wo = (const u16*)d_in[5];
  a.wg = (const u16*)d_in[8]; a.wu = (const u16*)d_in[9]; a.wd = (const u16*)d_in[10];
  a.ws = (char*)d_ws; a.out = (u16*)d_out; a.s0 = 0; a.pad1 = 0;
  main_kernel<<<256, 512, 0, stream>>>(a);
}
```

```cpp
#include <hip/hip_runtime.h>
#include <hip/hip_bf16.h>
#include <stdint.h>

constexpr int S_LEN = 2048, DM = 1024, NH = 32, DH = 128, NMEM = 8, SEG = 32;
constexpr int NSEG = S_LEN / SEG;
constexpr int HD = NH * DH;
constexpr int FF = 3584;

typedef unsigned short u16;
typedef __attribute__((ext_vector_type(8))) short bf16x8;
typedef __attribute__((ext_vector_type(4))) short s16x4;
typedef __attribute__((ext_vector_type(4))) float f32x4;
typedef __attribute__((ext_vector_type(4))) unsigned int u32x4;
typedef __attribute__((ext_vector_type(2))) unsigned int u32x2;
typedef __attribute__((address_space(3))) s16x4* lds_s16x4_ptr;
typedef __attribute__((address_space(3))) void* lds_void_ptr;
typedef __attribute__((address_space(1))) unsigned gu32;
typedef __bf16 bf16x2_t __attribute__((ext_vector_type(2)));
typedef float f32x2_t __attribute__((ext_vector_type(2)));

__device__ __forceinline__ unsigned cvtpk(float lo, float hi) {
  bf16x2_t r = __builtin_convertvector((f32x2_t){lo, hi}, bf16x2_t);
  return __builtin_bit_cast(unsigned, r);
}
__device__ __forceinline__ float lo_f(unsigned v) { return __uint_as_float(v << 16); }
__device__ __forceinline__ float hi_f(unsigned v) { return __uint_as_float(v & 0xffff0000u); }
__device__ __forceinline__ float rbf(float f) { return lo_f(cvtpk(f, f)); }
__device__ __forceinline__ bf16x8 as_bf16x8(u32x4 v) { return __builtin_bit_cast(bf16x8, v); }
#define MFMA(a, b, c) __builtin_amdgcn_mfma_f32_16x16x32_bf16((a), (b), (c), 0, 0, 0)

#define KSTR 65
#define XLD 1024
constexpr int PAGE = 2 << 20, NPAGE = 14;
constexpr int T_K = 0, T_Q = 2304, T_V = 4608, T_O = 6912, T_END = T_O + 2048;
constexpr unsigned T_BYTES = T_END * 16384u;
constexpr size_t WS_SY = 0;
constexpr size_t WS_T = (size_t)NPAGE * PAGE;
constexpr size_t WS_XN = WS_T + T_BYTES;
constexpr size_t WS_HF = WS_XN + (size_t)S_LEN * XLD * 2;
constexpr size_t WS_G = WS_HF + (size_t)S_LEN * 16384;
constexpr size_t WS_S = WS_G + (size_t)S_LEN * DM * 2;
constexpr size_t WS_END = WS_S + (size_t)S_LEN * DM * 2;
constexpr unsigned SENT = 0xFFFFFFFFu;
constexpr unsigned DSENT = 0xFFBFFFBFu;
constexpr int SY_MEM = 0;
constexpr int SY_O = 2 * PAGE;
constexpr int SY_V = 6 * PAGE;
constexpr int SY_BR = PAGE, SY_BA = SY_BR + 16384;
constexpr int SY_FKQ = SY_BR + 32768;
constexpr int SY_FG = SY_FKQ + NH * 4 * 128;
constexpr int SY_FO = SY_FG + 16 * 8 * 128;
constexpr int SY_CH = SY_FO + 384 * 128;
constexpr int SY_ABORT = SY_CH + 8 * 128;
constexpr int SY_FS = SY_ABORT + 256;
constexpr int SY_FEND = SY_FS + 64 * 128;
constexpr int SY_EP = SY_FEND, SY_BT = SY_EP + 128;
constexpr int SY_TOTAL = NPAGE * PAGE;
constexpr int PROBE = PAGE - 16384 + 6144;
__device__ __forceinline__ int side(unsigned pm, int page) { return ((pm >> page) & 1u) << 13; }
__device__ __forceinline__ int o_mem(unsigned pm, int s) { return SY_MEM + s * 32768 + side(pm, 0); }
__device__ __forceinline__ int o_o(unsigned pm, int s, int h) {
  return SY_O + s * 131072 + (h >> 3) * 16384 + ((h >> 1) & 3) * 2048 + (h & 1) * 128 + side(pm, 2 + (s >> 4));
}
__device__ __forceinline__ int o_v(unsigned pm, int s, int h) { return SY_V + s * 262144 + (h >> 1) * 16384 + (h & 1) * 4096 + side(pm, 6 + (s >> 3)); }
__device__ __forceinline__ char* ws_align(char* ws) { return ws + ((PAGE - (unsigned)(uintptr_t)ws) & (PAGE - 1)); }
#define NFUSE 12
constexpr int NPRE = 4;
static_assert(WS_END + PAGE <= (size_t)256 << 20 && NH * KSTR <= T_Q && NPAGE % 2 == 0, "");
constexpr int NCHAIN = 128;
__host__ __device__ constexpr int kseg(int sg, int h) { return h * KSTR + sg; }
__host__ __device__ constexpr int fkq(int h, int kind) { return SY_FKQ + (h * 4 + kind) * 128; }

#define TLB_NI 16
#define TLB_WS (2 << 20)
#define XPROBE 0
struct Args {
  const u16 *x, *mem0, *Wq, *Wk, *Wv, *Wo, *wg, *wu, *wd;
  char* ws;
  u16* out;
  int s0, pad1;
};
__device__ __forceinline__ int h_off(const Args& p, int rblk) { return rblk * PAGE + (((p.s0 ^ rblk ^ 1) & 1) << 13); }
static_assert(WS_HF % (2 * PAGE) == 0, "");
#define TF_KQV 1
#define TF_O 0
__device__ __forceinline__ int t_off(const Args& p, int t) { return t * 16384 + (((p.s0 ^ (t >> 7) ^ (TF_KQV == 2 ? t : TF_KQV)) & 1) << 13); }
__device__ __forceinline__ int t_off_o(const Args& p, int t) { return t * 16384 + (((p.s0 ^ (t >> 7) ^ (TF_O == 2 ? t : TF_O)) & 1) << 13); }

__device__ __forceinline__ float wave_sum(float v) {
#pragma unroll
  for (int i = 0; i < 4; ++i) {
    const int x = (int)__float_as_uint(v);
    v += __uint_as_float(i == 0 ? __builtin_amdgcn_update_dpp(0, x, 0x128, 0xf, 0xf, false) : i == 1 ? __builtin_amdgcn_update_dpp(0, x, 0x124, 0xf, 0xf, false)
                         : i == 2 ? __builtin_amdgcn_update_dpp(0, x, 0x122, 0xf, 0xf, false) : __builtin_amdgcn_update_dpp(0, x, 0x121, 0xf, 0xf, false));
  }
  auto r = __builtin_amdgcn_permlane16_swap(__float_as_uint(v), __float_as_uint(v), false, false);
  v = __uint_as_float(r[0]) + __uint_as_float(r[1]);
  r = __builtin_amdgcn_permlane32_swap(__float_as_uint(v), __float_as_uint(v), false, false);
  return __uint_as_float(r[0]) + __uint_as_float(r[1]);
}

__shared__ __attribute__((aligned(16))) char g_smem[163840];
__device__ __forceinline__ unsigned lds_off(const void* p) {
  return (unsigned)(uintptr_t)(lds_void_ptr)g_smem + (unsigned)((const char*)p - g_smem);
}
__device__ __forceinline__ __amdgpu_buffer_rsrc_t mkrsrc(const void* p, unsigned bytes) {
  return __builtin_amdgcn_make_buffer_rsrc((void*)p, 0, bytes, 0x00020000);
}
__device__ __forceinline__ u32x4 ld_sc1(__amdgpu_buffer_rsrc_t rs, int off) {
  return __builtin_amdgcn_raw_buffer_load_b128(rs, off, 0, 16);
}
__device__ __forceinline__ void st_sc1(__amdgpu_buffer_rsrc_t rs, int off, u32x4 v) {
  __builtin_amdgcn_raw_buffer_store_b128(v, rs, off, 0, 16);
}
__device__ __forceinline__ void swap16(unsigned& x, unsigned& y) {
  auto r = __builtin_amdgcn_permlane16_swap(x, y, false, false);
  x = r[0]; y = r[1];
}
__device__ __forceinline__ void swap32(unsigned& x, unsigned& y) {
  auto r = __builtin_amdgcn_permlane32_swap(x, y, false, false);
  x = r[0]; y = r[1];
}
__device__ __forceinline__ bool spin_fail(unsigned& spins, gu32* abortw) {
  ++spins;
  if ((spins & 63u) == 0u) {
    const __amdgpu_buffer_rsrc_t ra = mkrsrc((const void*)abortw, 4);
    unsigned z = 0;
    asm volatile("" : "+v"(z));
    if (__builtin_amdgcn_readfirstlane(__builtin_amdgcn_raw_buffer_load_b32(ra, z, 0, 16)) == 1u) return true;
    if (spins > (1u << 19)) {
      __builtin_amdgcn_raw_buffer_store_b32(z + 1u, ra, z, 0, 16);
      return true;
    }
  }
  return false;
}
__device__ __forceinline__ bf16x8 ldsf(const char* p) { return *reinterpret_cast<const bf16x8*>(p); }
#define SCHED() __builtin_amdgcn_sched_barrier(0)
#define LBAR() do { asm volatile("s_waitcnt lgkmcnt(0)" ::: "memory"); __builtin_amdgcn_s_barrier(); asm volatile("" ::: "memory"); } while (0)
#define WAIT_V(n) asm volatile("s_waitcnt vmcnt(%0)" :: "n"(n) : "memory")

#define HOLD_V(c, n) asm volatile("s_cmp_eq_u32 %0, 0\n\ts_cbranch_scc1 .Lh_%=\n\ts_waitcnt vmcnt(%1)\n.Lh_%=:" :: "s"((int)(c)), "n"(n) : "memory", "scc")
#define TH_Q 2
__device__ __forceinline__ void wait_flags(__amdgpu_buffer_rsrc_t rs, int off, gu32* abortw) {
  for (unsigned spins = 0;;) {
    asm volatile("" ::: "memory");
    const unsigned v = __builtin_amdgcn_raw_buffer_load_b32(rs, off, 0, 16);
    if (__all(v != SENT)) break;
    __builtin_amdgcn_s_sleep(4);
    if (spin_fail(spins, abortw)) break;
  }
  asm volatile("" ::: "memory");
}

__device__ __forceinline__ unsigned offA(unsigned row, unsigned ch) {
  return row * 128u + 16u * (ch ^ (row & 7u));
}
__device__ __forceinline__ unsigned swzB(unsigned row) { return ((row & 3u) << 2) | ((row >> 2) & 3u); }
__device__ __forceinline__ unsigned offB(unsigned row, unsigned ch) {
  return 256u * row + 16u * (ch ^ swzB(row));
}
__device__ __forceinline__ void glds16(const u16* src, char* dst) {
  const unsigned d = __builtin_amdgcn_readfirstlane(lds_off(dst));
  unsigned keep;
  asm volatile("s_mov_b32 %0, m0\n\ts_mov_b32 m0, %2\n\ts_nop 0\n\tglobal_load_lds_dwordx4 %1, off\n\ts_mov_b32 m0, %0"
               : "=&s"(keep) : "v"(src), "s"(d) : "memory");
}
__device__ __forceinline__ bf16x8 ld_bfrag(const char* sb, int ks, int c, int lane) {
  const unsigned g = lane >> 4, qq = (lane & 15) >> 2, pp = lane & 3;
  s16x4 lo = __builtin_amdgcn_ds_read_tr16_b64_v4i16(
      (lds_s16x4_ptr)(sb + offB(32 * ks + 8 * g + qq, 2 * c + (pp >> 1)) + 8 * (pp & 1)));
  s16x4 hi = __builtin_amdgcn_ds_read_tr16_b64_v4i16(
      (lds_s16x4_ptr)(sb + offB(32 * ks + 8 * g + 4 + qq, 2 * c + (pp >> 1)) + 8 * (pp & 1)));
  return (bf16x8){lo[0], lo[1], lo[2], lo[3], hi[0], hi[1], hi[2], hi[3]};
}

__device__ __forceinline__ void blk_dma(const u16* src, size_t ld, char* slot, int lane) {
  const u16* s = src + (size_t)(lane >> 2) * ld + (lane & 3) * 8;
  glds16(s, slot);
  glds16(s + 16 * ld, slot + 1024);
}
template <int NB, typename ISSUE, typename SINK>
__device__ __forceinline__ void load_blocks(char* slice, ISSUE&& issue, SINK&& sink) {
#pragma unroll
  for (int i = 0; i < 8; ++i) issue(i, slice + i * 2048);
#pragma unroll
  for (int g = 0; g < NB / 4; ++g) {
    if (g + 1 < NB / 4) WAIT_V(8); else WAIT_V(0);
#pragma unroll
    for (int j = 0; j < 4; ++j) sink(4 * g + j, slice + ((4 * g + j) & 7) * 2048);
    asm volatile("s_waitcnt lgkmcnt(0)" ::: "memory");
    if (g + 2 < NB / 4) {
#pragma unroll
      for (int j = 0; j < 4; ++j) issue(4 * g + 8 + j, slice + ((4 * g + j) & 7) * 2048);
    }
  }
}

__device__ __forceinline__ u32x4 widen32(unsigned a0, unsigned a1, unsigned b0, unsigned b1) {
  swap32(a0, b0); swap16(a0, b0);
  swap32(a1, b1); swap16(a1, b1);
  return (u32x4){a0, a1, b0, b1};
}

__device__ __forceinline__ void publish_flags(__amdgpu_buffer_rsrc_t rs, int off, int stride, int n) {
  WAIT_V(0);
  LBAR();
  int tid = threadIdx.x;
  asm volatile("" : "+v"(tid));
  if (tid < n) __builtin_amdgcn_raw_buffer_store_b32(1u, rs, off + tid * stride, 0, 16);
}

__device__ __forceinline__ void wait_s(const Args& p, __amdgpu_buffer_rsrc_t rsy, int rblk, int c0, int n, bool need) {
  if (need) {
    if (threadIdx.x < 64) {
      for (unsigned spins = 0;;) {
        asm volatile("" ::: "memory");
        const int f = (int)__builtin_amdgcn_raw_buffer_load_b32(rsy, SY_FS + (2 * c0 + (threadIdx.x & (2 * n - 1))) * 128, 0, 16);
        if (__all(f >= rblk)) break;
        __builtin_amdgcn_s_sleep(32);
        if (spin_fail(spins, (gu32*)(p.ws + WS_SY + SY_ABORT))) break;
      }
    }
    LBAR();
  }
}
__device__ __forceinline__ u32x4 combine(u32x4 sv, u32x4 g, u32x4 x) {
  u32x4 o;
#pragma unroll
  for (int j = 0; j < 4; ++j) {
    const unsigned t = cvtpk(lo_f(sv[j]) * lo_f(g[j]), hi_f(sv[j]) * hi_f(g[j]));
    o[j] = cvtpk(lo_f(x[j]) + lo_f(t), hi_f(x[j]) + hi_f(t));
  }
  return o;
}
#define OUT_AUX 0
__device__ __forceinline__ void st_out(const Args& p, int e, u32x4 v) {
  __builtin_amdgcn_raw_buffer_store_b128(v, mkrsrc(p.out, S_LEN * DM * 2), e * 2, 0, OUT_AUX);
}
struct SX { u32x4 s, x; };
__device__ __forceinline__ SX get_sx(const Args& p, int rblk, int e) {
  SX r; r.s = (u32x4){0u, 0u, 0u, 0u}; r.x = r.s;
  if (rblk < NFUSE) {
    r.s = ld_sc1(mkrsrc(p.ws + WS_S, S_LEN * DM * 2), (((e & 1023) >> 5) * S_LEN + (e >> 10)) * 64 + (e & 31) * 2);
    r.x = *reinterpret_cast<const u32x4*>(p.x + e);
  }
  return r;
}
__device__ __forceinline__ void put_g(const Args& p, __amdgpu_buffer_rsrc_t rg, int rblk, int e, u32x4 g, const SX& v) {
  if (rblk < NFUSE) {
    st_out(p, e, combine(v.s, g, v.x));
  } else st_sc1(rg, e * 2, g);
}

__device__ __forceinline__ void job_comb(const Args& p, int rblk, int ct, __amdgpu_buffer_rsrc_t rsy) {
  int tid = threadIdx.x;
  asm volatile("" : "+v"(tid));
  u32x4 xv[8];
#pragma unroll
  for (int i = 0; i < 8; ++i) xv[i] = *reinterpret_cast<const u32x4*>(p.x + (rblk * 128 + i * 16 + (tid >> 5)) * DM + ct * 256 + (tid & 31) * 8);
  if (tid < 64) wait_flags(rsy, SY_FG + (rblk * 8 + 2 * ct + (tid & 1)) * 128, (gu32*)(p.ws + WS_SY + SY_ABORT));
  wait_s(p, rsy, rblk, ct * 8, 8, true);
  u32x4 sv[8], gv[8];
#pragma unroll
  for (int i = 0; i < 8; ++i) {
    const int e = (rblk * 128 + i * 16 + (tid >> 5)) * DM + ct * 256 + (tid & 31) * 8;
    sv[i] = ld_sc1(mkrsrc(p.ws + WS_S, S_LEN * DM * 2), (((e & 1023) >> 5) * S_LEN + (e >> 10)) * 64 + (e & 31) * 2);
    gv[i] = ld_sc1(mkrsrc(p.ws + WS_G, S_LEN * DM * 2), e * 2);
  }
#pragma unroll
  for (int i = 0; i < 8; ++i)
    st_out(p, (rblk * 128 + i * 16 + (tid >> 5)) * DM + ct * 256 + (tid & 31) * 8, combine(sv[i], gv[i], xv[i]));
}

__device__ __forceinline__ u32x4 raw_rsrc(const void* p, unsigned bytes) {
  const unsigned long long a = (unsigned long long)p;
  return (u32x4){(unsigned)a, (unsigned)(a >> 32) & 0xffffu, bytes, 0x00020000u};
}
__device__ __forceinline__ u32x2 next_tag(u32x2 e) { return (u32x2){e[0] + 1u, e[1] + (e[0] == 0xFFFFFFFFu)}; }
struct Boot { u32x2 e; u32x4 c; };
__device__ __forceinline__ void boot_ask(const char* ws, Boot& r) {
  const unsigned o = (threadIdx.x & 63) * 128;
  asm volatile("buffer_load_dwordx2 %0, off, %3, %4 sc1\n\tbuffer_load_dwordx4 %1, %2, %3, %5 offen sc1"
               : "=&v"(r.e), "=&v"(r.c) : "v"(o), "s"(raw_rsrc(ws, SY_TOTAL)), "s"(SY_EP), "s"(SY_BT) : "memory");
}
__device__ __forceinline__ void wait_rows(const char* ws, u32x2 tag) {
  const __amdgpu_buffer_rsrc_t rs = mkrsrc(ws, SY_TOTAL);
  int o = (threadIdx.x & 63) * 128;
  asm volatile("" : "+v"(o));
#pragma unroll 1
  for (int i = 0; i < (1 << 17); ++i) {
    asm volatile("" ::: "memory");
    const u32x2 a = __builtin_amdgcn_raw_buffer_load_b64(rs, o, SY_BT + 32 * 128, 16);
    if (__all(a[0] == tag[0] && a[1] == tag[1])) break;
    __builtin_amdgcn_s_sleep(8);
  }
  asm volatile("" ::: "memory");
}
__device__ __forceinline__ unsigned boot_done(const char* ws, Boot& r, bool own = false, u32x2 tag = (u32x2){0u, 0u}) {
#pragma unroll 1
  for (int i = 0; i < (1 << 17); ++i) {
    asm volatile("" : "+v"(r.e), "+v"(r.c));
    if (!own) tag = next_tag(r.e);
    if (__all(r.c[0] == tag[0] && r.c[1] == tag[1] && (r.c[2] ^ r.c[3]) == tag[0])) break;
    __builtin_amdgcn_s_sleep(8);
    boot_ask(ws, r);
    WAIT_V(0);
  }
  unsigned q0[NPAGE], q1[NPAGE], ga = 0xFFFFu, gb = 0xFFFFu;
#pragma unroll
  for (int i = 0; i < NPAGE; ++i) {
    const unsigned x = __builtin_amdgcn_readlane(r.c[2], i), y = __builtin_amdgcn_readlane(r.c[2], i + NPAGE);
    q0[i] = min(x >> 16, y >> 16); q1[i] = min(x & 0xFFFFu, y & 0xFFFFu);
    ga = min(ga, (i & 1) ? q1[i] : q0[i]); gb = min(gb, (i & 1) ? q0[i] : q1[i]);
  }
  unsigned m = gb < ga ? 0x80001555u : 0x2AAAu;
  const unsigned thr = min(ga, gb) + 110;
#pragma unroll
  for (int i = 0; i < NPAGE; ++i) {
    const unsigned lo = min(q0[i], q1[i]), hi = max(q0[i], q1[i]);
    const unsigned keep = (lo < thr ? hi : 0u) > lo + 100 ? ~(1u << i) : ~0u;
    m = (m & keep) | ((q1[i] < q0[i] ? ~0u : 0u) & ~keep);
  }
  return m;
}
typedef __attribute__((address_space(3))) volatile unsigned* lds_vu_ptr;
__device__ __forceinline__ lds_vu_ptr lds_at(const void* p) {
  unsigned a = lds_off(p);
  asm volatile("" : "+v"(a));
  return (lds_vu_ptr)(uintptr_t)a;
}
__device__ __forceinline__ unsigned uni(unsigned v) {
  unsigned r;
  asm volatile("s_nop 1\n\tv_readfirstlane_b32 %0, %1\n\ts_nop 3" : "=s"(r) : "v"(v));
  return r;
}
__device__ __forceinline__ void start_sy(char* ws, const u16* mem0, int i, char* smem) {
  const int tid = threadIdx.x, lane = tid & 63, w = __builtin_amdgcn_readfirstlane(tid >> 6);
  const __amdgpu_buffer_rsrc_t rsy = mkrsrc(ws, SY_TOTAL);
  const __amdgpu_buffer_rsrc_t rs = mkrsrc(ws + (size_t)(i % NPAGE) * PAGE + PROBE - (i / NPAGE) * 2048, 16384);
  const u32x2 e = __builtin_amdgcn_raw_buffer_load_b64(rsy, 0, SY_EP, 16);
  const u32x4 ds = {DSENT, DSENT, DSENT, DSENT};
  if (lane < 32) st_sc1(rs, (lane >> 4) * 8192 + w * 256 + (lane & 15) * 16, ds);
  static_assert(NPRE == 4 && SY_FEND - SY_BR <= 32 * 6144, "");
#pragma unroll
  for (int u = 0; u < 6; ++u)
    st_sc1(rsy, (u < 4 ? SY_V + (i * 4 + u) * 8192 : SY_O + (i * 2 + u - 4) * 8192) + tid * 16, ds);
  if (w == 7) {
    const int n = i * 64 + lane, j = (n >> 10) * 512 + (n & 511);
    const unsigned* m0 = reinterpret_cast<const unsigned*>(mem0 + (2 * ((j >> 2) & 3) + (j & 1)) * DM + (j >> 4) * 16) + ((j >> 1) & 1);
    u32x4 m;
#pragma unroll
    for (int k = 0; k < 4; ++k) { const unsigned t = m0[2 * k]; m[k] = t == DSENT ? 0xFFFFFFFFu : t; }
    st_sc1(rsy, n * 16, m);
  } else if (w == 6) st_sc1(rsy, 32768 + i * 1024 + lane * 16, ds);
  else {
    const int o = SY_BR + i * 6144 + tid * 16;
    st_sc1(rsy, o < SY_FEND ? o : 0x7F000000, (u32x4){SENT, SENT, SENT, SENT});
  }
  unsigned t[4];
#pragma unroll
  for (int k = 0; k < 4; ++k) {
    unsigned long long c0, c1; unsigned v, off = ((k ^ (k >> 1)) & 1) * 8192 + w * 256 + (k >> 1) * 128;
    asm volatile("s_waitcnt vmcnt(0)\n\ts_barrier\n\ts_memtime %0\n\ts_waitcnt lgkmcnt(0)\n\tbuffer_load_dword %2, %3, %4, 0 offen sc1\n\ts_waitcnt vmcnt(0)\n\t"
                 "s_memtime %1\n\ts_waitcnt lgkmcnt(0)" : "=&s"(c0), "=&s"(c1), "=&v"(v) : "v"(off), "s"(rs) : "memory");
    t[k] = (unsigned)(c1 - c0);
  }
  lds_vu_ptr tm = lds_at(smem);
  if (lane == 0) { tm[w] = min(t[0], t[3]); tm[8 + w] = min(t[1], t[2]); }
  int ro = SY_BT + i * 128 + lane * 16;
  asm volatile("" : "+v"(ro));
  LBAR();
  if (threadIdx.x < 8) {
    unsigned m0 = tm[0], m1 = tm[8];
#pragma unroll
    for (int k = 1; k < 8; ++k) { m0 = min(m0, tm[k]); m1 = min(m1, tm[8 + k]); }
    const u32x2 tag = next_tag(e);
    const unsigned v = min(m0, 30000u) << 16 | min(m1, 30000u);
    st_sc1(rsy, ro, (u32x4){tag[0], tag[1], v, v ^ tag[0]});
  }
}
__device__ __forceinline__ u32x2 start_rows(const Args& p, int g) {
  const int tid = threadIdx.x, lane = tid & 63, row0 = g * 16 + (tid >> 6) * 2;
  u32x4 a[4];
#pragma unroll
  for (int i = 0; i < 4; ++i) a[i] = *reinterpret_cast<const u32x4*>(p.x + row0 * DM + i * 512 + lane * 8);
  const u32x2 e = __builtin_amdgcn_raw_buffer_load_b64(mkrsrc(p.ws, SY_TOTAL), 0, SY_EP, 16);
  const __amdgpu_buffer_rsrc_t ro = mkrsrc(p.ws + WS_XN, S_LEN * XLD * 2);
#pragma unroll
  for (int u = 0; u < 2; ++u) {
    float v[16];
#pragma unroll
    for (int i = 0; i < 8; ++i) { v[2 * i] = lo_f(a[2 * u + (i >> 2)][i & 3]); v[2 * i + 1] = hi_f(a[2 * u + (i >> 2)][i & 3]); }
    float s = 0.f;
#pragma unroll
    for (int i = 0; i < 16; ++i) s += v[i];
    const float mean = wave_sum(s) * (1.0f / DM);
    float q = 0.f;
#pragma unroll
    for (int i = 0; i < 16; ++i) { float d = v[i] - mean; q += d * d; }
    const float var = rbf(wave_sum(q) * (1.0f / DM));
    const float mb = rbf(mean);
    const float inv = 1.0f / rbf(sqrtf(rbf(var + 1e-5f)));
#pragma unroll
    for (int k = 0; k < 2; ++k) {
      u32x4 o;
#pragma unroll
      for (int i = 0; i < 4; ++i) {
        const unsigned t = cvtpk(v[8 * k + 2 * i] - mb, v[8 * k + 2 * i + 1] - mb);
        o[i] = cvtpk(lo_f(t) * inv, hi_f(t) * inv);
      }
      st_sc1(ro, (row0 + u) * (XLD * 2) + k * 1024 + lane * 16, o);
    }
  }
  return next_tag(e);
}
__device__ __forceinline__ void report_rows(const Args& p, int g, u32x2 tag) {
  if (threadIdx.x < 8) st_sc1(mkrsrc(p.ws + WS_SY, SY_TOTAL), SY_BT + (32 + g) * 128 + threadIdx.x * 16, (u32x4){tag[0], tag[1], 0u, tag[0]});
}

typedef __attribute__((ext_vector_type(32))) unsigned u32x32;
__device__ __forceinline__ void put4(u32x32& W, int i, bf16x8 f) {
  const u32x4 v = __builtin_bit_cast(u32x4, f);
#pragma unroll
  for (int j = 0; j < 4; ++j) W[4 * i + j] = v[j];
}
#define CLB8(a, b, c, d, e, f, g, h) "v" #a, "v" #b, "v" #c, "v" #d, "v" #e, "v" #f, "v" #g, "v" #h
#define CLB32(a) CLB32_##a
#define CLB32_128 CLB8(128, 129, 130, 131, 132, 133, 134, 135), CLB8(136, 137, 138, 139, 140, 141, 142, 143), CLB8(144, 145, 146, 147, 148, 149, 150, 151), CLB8(152, 153, 154, 155, 156, 157, 158, 159)
#define CLB32_160 CLB8(160, 161, 162, 163, 164, 165, 166, 167), CLB8(168, 169, 170, 171, 172, 173, 174, 175), CLB8(176, 177, 178, 179, 180, 181, 182, 183), CLB8(184, 185, 186, 187, 188, 189, 190, 191)
#define W4_IN "{v[0:31]}"(W[0]), "{v[32:63]}"(W[1]), "{v[64:95]}"(W[2]), "{v[96:127]}"(W[3])
#define CLB32_192 CLB8(192, 193, 194, 195, 196, 197, 198, 199), CLB8(200, 201, 202, 203, 204, 205, 206, 207), CLB8(208, 209, 210, 211, 212, 213, 214, 215), CLB8(216, 217, 218, 219, 220, 221, 222, 223)
__device__ __forceinline__ unsigned ld_flag0(__amdgpu_buffer_rsrc_t rs, int off) {
  return __builtin_amdgcn_raw_buffer_load_b32(rs, off, 0, 16);
}
__device__ __forceinline__ void glds16_sc1(const char* src, char* dst) {
  const unsigned d = __builtin_amdgcn_readfirstlane(lds_off(dst));
  unsigned keep;
  asm volatile("s_mov_b32 %0, m0\n\ts_mov_b32 m0, %2\n\ts_nop 0\n\tglobal_load_lds_dwordx4 %1, off sc1\n\ts_mov_b32 m0, %0"
               : "=&s"(keep) : "v"(src), "s"(d) : "memory");
}

__device__ __forceinline__ void touch_line(const char* src, char* dst) {
  const unsigned d = __builtin_amdgcn_readfirstlane(lds_off(dst));
  unsigned keep;
  asm volatile("s_mov_b32 %0, m0\n\ts_mov_b32 m0, %2\n\ts_nop 0\n\tglobal_load_lds_dword %1, off sc1\n\ts_mov_b32 m0, %0"
               : "=&s"(keep) : "v"(src), "s"(d) : "memory");
}

#define PF_N 12
#define PR_G1 8
#define PF_T0 0
#define WREV 1
template <bool DEV = false>
__device__ __forceinline__ void glds2(const char* base, int step, unsigned voff, unsigned dst, bool nt = false) {
  unsigned keep;
  const char* base2 = base + step;
  if (nt)
    asm volatile("s_mov_b32 %0, m0\n\ts_mov_b32 m0, %4\n\ts_nop 0\n\tglobal_load_lds_dwordx4 %1, %2 nt\n\t"
                 "s_add_u32 m0, m0, 0x2000\n\ts_nop 0\n\tglobal_load_lds_dwordx4 %1, %3 nt\n\ts_mov_b32 m0, %0"
                 : "=&s"(keep) : "v"(voff), "s"(base), "s"(base2), "s"(dst) : "memory", "scc");
  else
  if (DEV)
    asm volatile("s_mov_b32 %0, m0\n\ts_mov_b32 m0, %4\n\ts_nop 0\n\tglobal_load_lds_dwordx4 %1, %2 sc1\n\t"
                 "s_add_u32 m0, m0, 0x2000\n\ts_nop 0\n\tglobal_load_lds_dwordx4 %1, %3 sc1\n\ts_mov_b32 m0, %0"
                 : "=&s"(keep) : "v"(voff), "s"(base), "s"(base2), "s"(dst) : "memory", "scc");
  else
  asm volatile("s_mov_b32 %0, m0\n\ts_mov_b32 m0, %4\n\ts_nop 0\n\tglobal_load_lds_dwordx4 %1, %2\n\t"
               "s_add_u32 m0, m0, 0x2000\n\ts_nop 0\n\tglobal_load_lds_dwordx4 %1, %3\n\ts_mov_b32 m0, %0"
               : "=&s"(keep) : "v"(voff), "s"(base), "s"(base2), "s"(dst) : "memory", "scc");
}

#define DN_ASM \
  "s_mov_b32 s94, m0\n\t" \
  "s_mov_b64 s[84:85], %[pa]\n\t" \
  "s_mov_b64 s[86:87], %[pb]\n\t" \
  "s_movk_i32 s88, 0x4000\n\t" \
  "s_mov_b32 s89, 0x10000\n\t" \
  "s_movk_i32 s90, 13\n\t" \
  "v_mov_b64 v[0:1], 0\n\t" \
  "v_mov_b64 v[2:3], 0\n\t" \
  "v_mov_b64 v[4:5], 0\n\t" \
  "v_mov_b64 v[6:7], 0\n\t" \
  "v_mov_b64 v[8:9], 0\n\t" \
  "v_mov_b64 v[10:11], 0\n\t" \
  "v_mov_b64 v[12:13], 0\n\t" \
  "v_mov_b64 v[14:15], 0\n\t" \
  "v_mov_b64 v[16:17], 0\n\t" \
  "v_mov_b64 v[18:19], 0\n\t" \
  "v_mov_b64 v[20:21], 0\n\t" \
  "v_mov_b64 v[22:23], 0\n\t" \
  "v_mov_b64 v[24:25], 0\n\t" \
  "v_mov_b64 v[26:27], 0\n\t" \
  "v_mov_b64 v[28:29], 0\n\t" \
  "v_mov_b64 v[30:31], 0\n\t" \
  "s_cmp_lg_u32 %[hf], 0\n\t" \
  "s_cbranch_scc1 .Lk_%=\n\t" \
  "ds_read_b128 v[32:35], v80 offset:0\n\t" \
  "ds_read_b128 v[36:39], v80 offset:1024\n\t" \
  "ds_read_b128 v[40:43], v80 offset:2048\n\t" \
  "ds_read_b128 v[44:47], v80 offset:3072\n\t" \
  "ds_read_b64_tr_b16 v[64:65], v82 offset:0\n\t" \
  "ds_read_b64_tr_b16 v[66:67], v83 offset:0\n\t" \
  "ds_read_b128 v[48:51], v80 offset:8192\n\t" \
  "ds_read_b64_tr_b16 v[68:69], v84 offset:0\n\t" \
  "ds_read_b64_tr_b16 v[70:71], v85 offset:0\n\t" \
  "ds_read_b128 v[52:55], v80 offset:9216\n\t" \
  "ds_read_b128 v[56:59], v80 offset:10240\n\t" \
  "ds_read_b128 v[60:63], v80 offset:11264\n\t" \
  ".Lp0_%=:\n\t" \
  "s_waitcnt vmcnt(4)\n\t" \
  "s_barrier\n\t" \
  "s_waitcnt lgkmcnt(3)\n\t" \
  "v_mfma_f32_16x16x32_bf16 v[0:3], v[64:67], v[32:35], v[0:3]\n\t" \
  "ds_read_b64_tr_b16 v[72:73], v82 offset:8192\n\t" \
  "v_mfma_f32_16x16x32_bf16 v[4:7], v[68:71], v[32:35], v[4:7]\n\t" \
  "ds_read_b64_tr_b16 v[74:75], v83 offset:8192\n\t" \
  "ds_read_b128 v[32:35], v80 offset:32768\n\t" \
  "s_add_u32 m0, %[lw], 98304\n\t" \
  "v_mfma_f32_16x16x32_bf16 v[8:11], v[64:67], v[36:39], v[8:11]\n\t" \
  "ds_read_b64_tr_b16 v[76:77], v84 offset:8192\n\t" \
  "global_load_lds_dwordx4 v90, s[84:85] sc1\n\t" \
  "s_add_u32 s84, s84, s88\n\t" \
  "s_addc_u32 s85, s85, 0\n\t" \
  "v_mfma_f32_16x16x32_bf16 v[12:15], v[68:71], v[36:39], v[12:15]\n\t" \
  "ds_read_b64_tr_b16 v[78:79], v85 offset:8192\n\t" \
  "ds_read_b128 v[36:39], v80 offset:33792\n\t" \
  "v_mfma_f32_16x16x32_bf16 v[16:19], v[64:67], v[40:43], v[16:19]\n\t" \
  "v_mfma_f32_16x16x32_bf16 v[20:23], v[68:71], v[40:43], v[20:23]\n\t" \
  "ds_read_b128 v[40:43], v80 offset:34816\n\t" \
  "s_add_u32 m0, %[lw], 114688\n\t" \
  "v_mfma_f32_16x16x32_bf16 v[24:27], v[64:67], v[44:47], v[24:27]\n\t" \
  "global_load_lds_dwordx4 v91, s[86:87]\n\t" \
  "s_add_u32 s86, s86, s89\n\t" \
  "s_addc_u32 s87, s87, 0\n\t" \
  "v_mfma_f32_16x16x32_bf16 v[28:31], v[68:71], v[44:47], v[28:31]\n\t" \
  "ds_read_b128 v[44:47], v80 offset:35840\n\t" \
  "s_waitcnt lgkmcnt(3)\n\t" \
  "v_mfma_f32_16x16x32_bf16 v[0:3], v[72:75], v[48:51], v[0:3]\n\t" \
  "ds_read_b64_tr_b16 v[64:65], v82 offset:32768\n\t" \
  "v_mfma_f32_16x16x32_bf16 v[4:7], v[76:79], v[48:51], v[4:7]\n\t" \
  "ds_read_b64_tr_b16 v[66:67], v83 offset:32768\n\t" \
  "ds_read_b128 v[48:51], v80 offset:40960\n\t" \
  "s_add_u32 m0, %[lw], 106496\n\t" \
  "v_mfma_f32_16x16x32_bf16 v[8:11], v[72:75], v[52:55], v[8:11]\n\t" \
  "ds_read_b64_tr_b16 v[68:69], v84 offset:32768\n\t" \
  "global_load_lds_dwordx4 v90, s[84:85] sc1\n\t" \
  "s_add_u32 s84, s84, s88\n\t" \
  "s_addc_u32 s85, s85, 0\n\t" \
  "v_mfma_f32_16x16x32_bf16 v[12:15], v[76:79], v[52:55], v[12:15]\n\t" \
  "ds_read_b64_tr_b16 v[70:71], v85 offset:32768\n\t" \
  "ds_read_b128 v[52:55], v80 offset:41984\n\t" \
  "v_mfma_f32_16x16x32_bf16 v[16:19], v[72:75], v[56:59], v[16:19]\n\t" \
  "v_mfma_f32_16x16x32_bf16 v[20:23], v[76:79], v[56:59], v[20:23]\n\t" \
  "ds_read_b128 v[56:59], v80 offset:43008\n\t" \
  "s_add_u32 m0, %[lw], 122880\n\t" \
  "v_mfma_f32_16x16x32_bf16 v[24:27], v[72:75], v[60:63], v[24:27]\n\t" \
  "global_load_lds_dwordx4 v91, s[86:87]\n\t" \
  "s_add_u32 s86, s86, s89\n\t" \
  "s_addc_u32 s87, s87, 0\n\t" \
  "v_mfma_f32_16x16x32_bf16 v[28:31], v[76:79], v[60:63], v[28:31]\n\t" \
  "ds_read_b128 v[60:63], v80 offset:44032\n\t" \
  "s_sub_u32 s90, s90, 1\n\t" \
  "s_cselect_b32 s88, 0, s88\n\t" \
  "s_cselect_b32 s89, 0, s89\n\t" \
  "s_cselect_b32 s91, 16384, 0\n\t" \
  "s_sub_u32 s84, s84, s91\n\t" \
  "s_subb_u32 s85, s85, 0\n\t" \
  "s_lshl_b32 s91, s91, 2\n\t" \
  "s_sub_u32 s86, s86, s91\n\t" \
  "s_subb_u32 s87, s87, 0\n\t" \
  "s_waitcnt vmcnt(4)\n\t" \
  "s_barrier\n\t" \
  "s_waitcnt lgkmcnt(3)\n\t" \
  "v_mfma_f32_16x16x32_bf16 v[0:3], v[64:67], v[32:35], v[0:3]\n\t" \
  "ds_read_b64_tr_b16 v[72:73], v82 offset:40960\n\t" \
  "v_mfma_f32_16x16x32_bf16 v[4:7], v[68:71], v[32:35], v[4:7]\n\t" \
  "ds_read_b64_tr_b16 v[74:75], v83 offset:40960\n\t" \
  "ds_read_b128 v[32:35], v81 offset:0\n\t" \
  "s_add_u32 m0, %[lw], 0\n\t" \
  "v_mfma_f32_16x16x32_bf16 v[8:11], v[64:67], v[36:39], v[8:11]\n\t" \
  "ds_read_b64_tr_b16 v[76:77], v84 offset:40960\n\t" \
  "global_load_lds_dwordx4 v90, s[84:85] sc1\n\t" \
  "s_add_u32 s84, s84, s88\n\t" \
  "s_addc_u32 s85, s85, 0\n\t" \
  "v_mfma_f32_16x16x32_bf16 v[12:15], v[68:71], v[36:39], v[12:15]\n\t" \
  "ds_read_b64_tr_b16 v[78:79], v85 offset:40960\n\t" \
  "ds_read_b128 v[36:39], v81 offset:1024\n\t" \
  "v_mfma_f32_16x16x32_bf16 v[16:19], v[64:67], v[40:43], v[16:19]\n\t" \
  "v_mfma_f32_16x16x32_bf16 v[20:23], v[68:71], v[40:43], v[20:23]\n\t" \
  "ds_read_b128 v[40:43], v81 offset:2048\n\t" \
  "s_add_u32 m0, %[lw], 16384\n\t" \
  "v_mfma_f32_16x16x32_bf16 v[24:27], v[64:67], v[44:47], v[24:27]\n\t" \
  "global_load_lds_dwordx4 v91, s[86:87]\n\t" \
  "s_add_u32 s86, s86, s89\n\t" \
  "s_addc_u32 s87, s87, 0\n\t" \
  "v_mfma_f32_16x16x32_bf16 v[28:31], v[68:71], v[44:47], v[28:31]\n\t" \
  "ds_read_b128 v[44:47], v81 offset:3072\n\t" \
  "s_waitcnt lgkmcnt(3)\n\t" \
  "v_mfma_f32_16x16x32_bf16 v[0:3], v[72:75], v[48:51], v[0:3]\n\t" \
  "ds_read_b64_tr_b16 v[64:65], v86 offset:0\n\t" \
  "v_mfma_f32_16x16x32_bf16 v[4:7], v[76:79], v[48:51], v[4:7]\n\t" \
  "ds_read_b64_tr_b16 v[66:67], v87 offset:0\n\t" \
  "ds_read_b128 v[48:51], v81 offset:8192\n\t" \
  "s_add_u32 m0, %[lw], 8192\n\t" \
  "v_mfma_f32_16x16x32_bf16 v[8:11], v[72:75], v[52:55], v[8:11]\n\t" \
  "ds_read_b64_tr_b16 v[68:69], v88 offset:0\n\t" \
  "global_load_lds_dwordx4 v90, s[84:85] sc1\n\t" \
  "s_add_u32 s84, s84, s88\n\t" \
  "s_addc_u32 s85, s85, 0\n\t" \
  "v_mfma_f32_16x16x32_bf16 v[12:15], v[76:79], v[52:55], v[12:15]\n\t" \
  "ds_read_b64_tr_b16 v[70:71], v89 offset:0\n\t" \
  "ds_read_b128 v[52:55], v81 offset:9216\n\t" \
  "v_mfma_f32_16x16x32_bf16 v[16:19], v[72:75], v[56:59], v[16:19]\n\t" \
  "v_mfma_f32_16x16x32_bf16 v[20:23], v[76:79], v[56:59], v[20:23]\n\t" \
  "ds_read_b128 v[56:59], v81 offset:10240\n\t" \
  "s_add_u32 m0, %[lw], 24576\n\t" \
  "v_mfma_f32_16x16x32_bf16 v[24:27], v[72:75], v[60:63], v[24:27]\n\t" \
  "global_load_lds_dwordx4 v91, s[86:87]\n\t" \
  "s_add_u32 s86, s86, s89\n\t" \
  "s_addc_u32 s87, s87, 0\n\t" \
  "v_mfma_f32_16x16x32_bf16 v[28:31], v[76:79], v[60:63], v[28:31]\n\t" \
  "ds_read_b128 v[60:63], v81 offset:11264\n\t" \
  "s_waitcnt vmcnt(4)\n\t" \
  "s_barrier\n\t" \
  "s_waitcnt lgkmcnt(3)\n\t" \
  "v_mfma_f32_16x16x32_bf16 v[0:3], v[64:67], v[32:35], v[0:3]\n\t" \
  "ds_read_b64_tr_b16 v[72:73], v86 offset:8192\n\t" \
  "v_mfma_f32_16x16x32_bf16 v[4:7], v[68:71], v[32:35], v[4:7]\n\t" \
  "ds_read_b64_tr_b16 v[74:75], v87 offset:8192\n\t" \
  "ds_read_b128 v[32:35], v81 offset:32768\n\t" \
  "s_add_u32 m0, %[lw], 32768\n\t" \
  "v_mfma_f32_16x16x32_bf16 v[8:11], v[64:67], v[36:39], v[8:11]\n\t" \
  "ds_read_b64_tr_b16 v[76:77], v88 offset:8192\n\t" \
  "global_load_lds_dwordx4 v90, s[84:85] sc1\n\t" \
  "s_add_u32 s84, s84, s88\n\t" \
  "s_addc_u32 s85, s85, 0\n\t" \
  "v_mfma_f32_16x16x32_bf16 v[12:15], v[68:71], v[36:39], v[12:15]\n\t" \
  "ds_read_b64_tr_b16 v[78:79], v89 offset:8192\n\t" \
  "ds_read_b128 v[36:39], v81 offset:33792\n\t" \
  "v_mfma_f32_16x16x32_bf16 v[16:19], v[64:67], v[40:43], v[16:19]\n\t" \
  "v_mfma_f32_16x16x32_bf16 v[20:23], v[68:71], v[40:43], v[20:23]\n\t" \
  "ds_read_b128 v[40:43], v81 offset:34816\n\t" \
  "s_add_u32 m0, %[lw], 49152\n\t" \
  "v_mfma_f32_16x16x32_bf16 v[24:27], v[64:67], v[44:47], v[24:27]\n\t" \
  "global_load_lds_dwordx4 v91, s[86:87]\n\t" \
  "s_add_u32 s86, s86, s89\n\t" \
  "s_addc_u32 s87, s87, 0\n\t" \
  "v_mfma_f32_16x16x32_bf16 v[28:31], v[68:71], v[44:47], v[28:31]\n\t" \
  "ds_read_b128 v[44:47], v81 offset:35840\n\t" \
  "s_waitcnt lgkmcnt(3)\n\t" \
  "v_mfma_f32_16x16x32_bf16 v[0:3], v[72:75], v[48:51], v[0:3]\n\t" \
  "ds_read_b64_tr_b16 v[64:65], v86 offset:32768\n\t" \
  "v_mfma_f32_16x16x32_bf16 v[4:7], v[76:79], v[48:51], v[4:7]\n\t" \
  "ds_read_b64_tr_b16 v[66:67], v87 offset:32768\n\t" \
  "ds_read_b128 v[48:51], v81 offset:40960\n\t" \
  "s_add_u32 m0, %[lw], 40960\n\t" \
  "v_mfma_f32_16x16x32_bf16 v[8:11], v[72:75], v[52:55], v[8:11]\n\t" \
  "ds_read_b64_tr_b16 v[68:69], v88 offset:32768\n\t" \
  "global_load_lds_dwordx4 v90, s[84:85] sc1\n\t" \
  "s_add_u32 s84, s84, s88\n\t" \
  "s_addc_u32 s85, s85, 0\n\t" \
  "v_mfma_f32_16x16x32_bf16 v[12:15], v[76:79], v[52:55], v[12:15]\n\t" \
  "ds_read_b64_tr_b16 v[70:71], v89 offset:32768\n\t" \
  "ds_read_b128 v[52:55], v81 offset:41984\n\t" \
  "v_mfma_f32_16x16x32_bf16 v[16:19], v[72:75], v[56:59], v[16:19]\n\t" \
  "v_mfma_f32_16x16x32_bf16 v[20:23], v[76:79], v[56:59], v[20:23]\n\t" \
  "ds_read_b128 v[56:59], v81 offset:43008\n\t" \
  "s_add_u32 m0, %[lw], 57344\n\t" \
  "v_mfma_f32_16x16x32_bf16 v[24:27], v[72:75], v[60:63], v[24:27]\n\t" \
  "global_load_lds_dwordx4 v91, s[86:87]\n\t" \
  "s_add_u32 s86, s86, s89\n\t" \
  "s_addc_u32 s87, s87, 0\n\t" \
  "v_mfma_f32_16x16x32_bf16 v[28:31], v[76:79], v[60:63], v[28:31]\n\t" \
  "ds_read_b128 v[60:63], v81 offset:44032\n\t" \
  "s_waitcnt vmcnt(4)\n\t" \
  "s_barrier\n\t" \
  "s_waitcnt lgkmcnt(3)\n\t" \
  "v_mfma_f32_16x16x32_bf16 v[0:3], v[64:67], v[32:35], v[0:3]\n\t" \
  "ds_read_b64_tr_b16 v[72:73], v86 offset:40960\n\t" \
  "v_mfma_f32_16x16x32_bf16 v[4:7], v[68:71], v[32:35], v[4:7]\n\t" \
  "ds_read_b64_tr_b16 v[74:75], v87 offset:40960\n\t" \
  "ds_read_b128 v[32:35], v80 offset:0\n\t" \
  "s_add_u32 m0, %[lw], 65536\n\t" \
  "v_mfma_f32_16x16x32_bf16 v[8:11], v[64:67], v[36:39], v[8:11]\n\t" \
  "ds_read_b64_tr_b16 v[76:77], v88 offset:40960\n\t" \
  "global_load_lds_dwordx4 v90, s[84:85] sc1\n\t" \
  "s_add_u32 s84, s84, s88\n\t" \
  "s_addc_u32 s85, s85, 0\n\t" \
  "v_mfma_f32_16x16x32_bf16 v[12:15], v[68:71], v[36:39], v[12:15]\n\t" \
  "ds_read_b64_tr_b16 v[78:79], v89 offset:40960\n\t" \
  "ds_read_b128 v[36:39], v80 offset:1024\n\t" \
  "v_mfma_f32_16x16x32_bf16 v[16:19], v[64:67], v[40:43], v[16:19]\n\t" \
  "v_mfma_f32_16x16x32_bf16 v[20:23], v[68:71], v[40:43], v[20:23]\n\t" \
  "ds_read_b128 v[40:43], v80 offset:2048\n\t" \
  "s_add_u32 m0, %[lw], 81920\n\t" \
  "v_mfma_f32_16x16x32_bf16 v[24:27], v[64:67], v[44:47], v[24:27]\n\t" \
  "global_load_lds_dwordx4 v91, s[86:87]\n\t" \
  "s_add_u32 s86, s86, s89\n\t" \
  "s_addc_u32 s87, s87, 0\n\t" \
  "v_mfma_f32_16x16x32_bf16 v[28:31], v[68:71], v[44:47], v[28:31]\n\t" \
  "ds_read_b128 v[44:47], v80 offset:3072\n\t" \
  "s_waitcnt lgkmcnt(3)\n\t" \
  "v_mfma_f32_16x16x32_bf16 v[0:3], v[72:75], v[48:51], v[0:3]\n\t" \
  "ds_read_b64_tr_b16 v[64:65], v82 offset:0\n\t" \
  "v_mfma_f32_16x16x32_bf16 v[4:7], v[76:79], v[48:51], v[4:7]\n\t" \
  "ds_read_b64_tr_b16 v[66:67], v83 offset:0\n\t" \
  "ds_read_b128 v[48:51], v80 offset:8192\n\t" \
  "s_add_u32 m0, %[lw], 73728\n\t" \
  "v_mfma_f32_16x16x32_bf16 v[8:11], v[72:75], v[52:55], v[8:11]\n\t" \
  "ds_read_b64_tr_b16 v[68:69], v84 offset:0\n\t" \
  "global_load_lds_dwordx4 v90, s[84:85] sc1\n\t" \
  "s_add_u32 s84, s84, s88\n\t" \
  "s_addc_u32 s85, s85, 0\n\t" \
  "v_mfma_f32_16x16x32_bf16 v[12:15], v[76:79], v[52:55], v[12:15]\n\t" \
  "ds_read_b64_tr_b16 v[70:71], v85 offset:0\n\t" \
  "ds_read_b128 v[52:55], v80 offset:9216\n\t" \
  "v_mfma_f32_16x16x32_bf16 v[16:19], v[72:75], v[56:59], v[16:19]\n\t" \
  "v_mfma_f32_16x16x32_bf16 v[20:23], v[76:79], v[56:59], v[20:23]\n\t" \
  "ds_read_b128 v[56:59], v80 offset:10240\n\t" \
  "s_add_u32 m0, %[lw], 90112\n\t" \
  "v_mfma_f32_16x16x32_bf16 v[24:27], v[72:75], v[60:63], v[24:27]\n\t" \
  "global_load_lds_dwordx4 v91, s[86:87]\n\t" \
  "s_add_u32 s86, s86, s89\n\t" \
  "s_addc_u32 s87, s87, 0\n\t" \
  "v_mfma_f32_16x16x32_bf16 v[28:31], v[76:79], v[60:63], v[28:31]\n\t" \
  "ds_read_b128 v[60:63], v80 offset:11264\n\t" \
  "s_cmp_lg_u32 s88, 0\n\t" \
  "s_cbranch_scc1 .Lp0_%=\n\t" \
  "s_branch .Le_%=\n\t" \
  ".Lk_%=:\n\t" \
  "ds_read_b128 v[32:35], v80 offset:0\n\t" \
  "ds_read_b128 v[36:39], v80 offset:1024\n\t" \
  "ds_read_b128 v[40:43], v80 offset:2048\n\t" \
  "ds_read_b128 v[44:47], v80 offset:3072\n\t" \
  "ds_read_b64_tr_b16 v[64:65], v82 offset:0\n\t" \
  "ds_read_b64_tr_b16 v[66:67], v83 offset:0\n\t" \
  "ds_read_b128 v[48:51], v80 offset:8192\n\t" \
  "ds_read_b64_tr_b16 v[68:69], v84 offset:0\n\t" \
  "ds_read_b64_tr_b16 v[70:71], v85 offset:0\n\t" \
  "ds_read_b128 v[52:55], v80 offset:9216\n\t" \
  "ds_read_b128 v[56:59], v80 offset:10240\n\t" \
  "ds_read_b128 v[60:63], v80 offset:11264\n\t" \
  ".Lp1_%=:\n\t" \
  "s_waitcnt vmcnt(4)\n\t" \
  "s_barrier\n\t" \
  "s_waitcnt lgkmcnt(3)\n\t" \
  "v_mfma_f32_16x16x32_bf16 v[0:3], v[64:67], v[32:35], v[0:3]\n\t" \
  "ds_read_b64_tr_b16 v[72:73], v82 offset:8192\n\t" \
  "v_mfma_f32_16x16x32_bf16 v[4:7], v[68:71], v[32:35], v[4:7]\n\t" \
  "ds_read_b64_tr_b16 v[74:75], v83 offset:8192\n\t" \
  "ds_read_b128 v[32:35], v80 offset:32768\n\t" \
  "v_mfma_f32_16x16x32_bf16 v[8:11], v[64:67], v[36:39], v[8:11]\n\t" \
  "ds_read_b64_tr_b16 v[76:77], v84 offset:8192\n\t" \
  "v_mfma_f32_16x16x32_bf16 v[12:15], v[68:71], v[36:39], v[12:15]\n\t" \
  "ds_read_b64_tr_b16 v[78:79], v85 offset:8192\n\t" \
  "ds_read_b128 v[36:39], v80 offset:33792\n\t" \
  "s_add_u32 m0, %[lw], 98304\n\t" \
  "v_mfma_f32_16x16x32_bf16 v[16:19], v[64:67], v[40:43], v[16:19]\n\t" \
  "global_load_lds_dwordx4 v90, s[84:85] sc1\n\t" \
  "s_add_u32 s84, s84, s88\n\t" \
  "s_addc_u32 s85, s85, 0\n\t" \
  "v_mfma_f32_16x16x32_bf16 v[20:23], v[68:71], v[40:43], v[20:23]\n\t" \
  "ds_read_b128 v[40:43], v80 offset:34816\n\t" \
  "v_mfma_f32_16x16x32_bf16 v[24:27], v[64:67], v[44:47], v[24:27]\n\t" \
  "s_add_u32 m0, %[lw], 114688\n\t" \
  "v_mfma_f32_16x16x32_bf16 v[28:31], v[68:71], v[44:47], v[28:31]\n\t" \
  "ds_read_b128 v[44:47], v80 offset:35840\n\t" \
  "global_load_lds_dwordx4 v91, s[86:87]\n\t" \
  "s_add_u32 s86, s86, s89\n\t" \
  "s_addc_u32 s87, s87, 0\n\t" \
  "s_waitcnt lgkmcnt(3)\n\t" \
  "v_mfma_f32_16x16x32_bf16 v[0:3], v[72:75], v[48:51], v[0:3]\n\t" \
  "ds_read_b64_tr_b16 v[64:65], v82 offset:32768\n\t" \
  "v_mfma_f32_16x16x32_bf16 v[4:7], v[76:79], v[48:51], v[4:7]\n\t" \
  "ds_read_b64_tr_b16 v[66:67], v83 offset:32768\n\t" \
  "ds_read_b128 v[48:51], v80 offset:40960\n\t" \
  "v_mfma_f32_16x16x32_bf16 v[8:11], v[72:75], v[52:55], v[8:11]\n\t" \
  "ds_read_b64_tr_b16 v[68:69], v84 offset:32768\n\t" \
  "v_mfma_f32_16x16x32_bf16 v[12:15], v[76:79], v[52:55], v[12:15]\n\t" \
  "ds_read_b64_tr_b16 v[70:71], v85 offset:32768\n\t" \
  "ds_read_b128 v[52:55], v80 offset:41984\n\t" \
  "s_add_u32 m0, %[lw], 106496\n\t" \
  "v_mfma_f32_16x16x32_bf16 v[16:19], v[72:75], v[56:59], v[16:19]\n\t" \
  "global_load_lds_dwordx4 v90, s[84:85] sc1\n\t" \
  "s_add_u32 s84, s84, s88\n\t" \
  "s_addc_u32 s85, s85, 0\n\t" \
  "v_mfma_f32_16x16x32_bf16 v[20:23], v[76:79], v[56:59], v[20:23]\n\t" \
  "ds_read_b128 v[56:59], v80 offset:43008\n\t" \
  "v_mfma_f32_16x16x32_bf16 v[24:27], v[72:75], v[60:63], v[24:27]\n\t" \
  "s_add_u32 m0, %[lw], 122880\n\t" \
  "v_mfma_f32_16x16x32_bf16 v[28:31], v[76:79], v[60:63], v[28:31]\n\t" \
  "ds_read_b128 v[60:63], v80 offset:44032\n\t" \
  "global_load_lds_dwordx4 v91, s[86:87]\n\t" \
  "s_add_u32 s86, s86, s89\n\t" \
  "s_addc_u32 s87, s87, 0\n\t" \
  "s_sub_u32 s90, s90, 1\n\t" \
  "s_cselect_b32 s88, 0, s88\n\t" \
  "s_cselect_b32 s89, 0, s89\n\t" \
  "s_cselect_b32 s91, 16384, 0\n\t" \
  "s_sub_u32 s84, s84, s91\n\t" \
  "s_subb_u32 s85, s85, 0\n\t" \
  "s_lshl_b32 s91, s91, 2\n\t" \
  "s_sub_u32 s86, s86, s91\n\t" \
  "s_subb_u32 s87, s87, 0\n\t" \
  "s_waitcnt vmcnt(4)\n\t" \
  "s_barrier\n\t" \
  "s_waitcnt lgkmcnt(3)\n\t" \
  "v_mfma_f32_16x16x32_bf16 v[0:3], v[64:67], v[32:35], v[0:3]\n\t" \
  "ds_read_b64_tr_b16 v[72:73], v82 offset:40960\n\t" \
  "v_mfma_f32_16x16x32_bf16 v[4:7], v[68:71], v[32:35], v[4:7]\n\t" \
  "ds_read_b64_tr_b16 v[74:75], v83 offset:40960\n\t" \
  "ds_read_b128 v[32:35], v81 offset:0\n\t" \
  "v_mfma_f32_16x16x32_bf16 v[8:11], v[64:67], v[36:39], v[8:11]\n\t" \
  "ds_read_b64_tr_b16 v[76:77], v84 offset:40960\n\t" \
  "v_mfma_f32_16x16x32_bf16 v[12:15], v[68:71], v[36:39], v[12:15]\n\t" \
  "ds_read_b64_tr_b16 v[78:79], v85 offset:40960\n\t" \
  "ds_read_b128 v[36:39], v81 offset:1024\n\t" \
  "s_add_u32 m0, %[lw], 0\n\t" \
  "v_mfma_f32_16x16x32_bf16 v[16:19], v[64:67], v[40:43], v[16:19]\n\t" \
  "global_load_lds_dwordx4 v90, s[84:85] sc1\n\t" \
  "s_add_u32 s84, s84, s88\n\t" \
  "s_addc_u32 s85, s85, 0\n\t" \
  "v_mfma_f32_16x16x32_bf16 v[20:23], v[68:71], v[40:43], v[20:23]\n\t" \
  "ds_read_b128 v[40:43], v81 offset:2048\n\t" \
  "v_mfma_f32_16x16x32_bf16 v[24:27], v[64:67], v[44:47], v[24:27]\n\t" \
  "s_add_u32 m0, %[lw], 16384\n\t" \
  "v_mfma_f32_16x16x32_bf16 v[28:31], v[68:71], v[44:47], v[28:31]\n\t" \
  "ds_read_b128 v[44:47], v81 offset:3072\n\t" \
  "global_load_lds_dwordx4 v91, s[86:87]\n\t" \
  "s_add_u32 s86, s86, s89\n\t" \
  "s_addc_u32 s87, s87, 0\n\t" \
  "s_waitcnt lgkmcnt(3)\n\t" \
  "v_mfma_f32_16x16x32_bf16 v[0:3], v[72:75], v[48:51], v[0:3]\n\t" \
  "ds_read_b64_tr_b16 v[64:65], v86 offset:0\n\t" \
  "v_mfma_f32_16x16x32_bf16 v[4:7], v[76:79], v[48:51], v[4:7]\n\t" \
  "ds_read_b64_tr_b16 v[66:67], v87 offset:0\n\t" \
  "ds_read_b128 v[48:51], v81 offset:8192\n\t" \
  "v_mfma_f32_16x16x32_bf16 v[8:11], v[72:75], v[52:55], v[8:11]\n\t" \
  "ds_read_b64_tr_b16 v[68:69], v88 offset:0\n\t" \
  "v_mfma_f32_16x16x32_bf16 v[12:15], v[76:79], v[52:55], v[12:15]\n\t" \
  "ds_read_b64_tr_b16 v[70:71], v89 offset:0\n\t" \
  "ds_read_b128 v[52:55], v81 offset:9216\n\t" \
  "s_add_u32 m0, %[lw], 8192\n\t" \
  "v_mfma_f32_16x16x32_bf16 v[16:19], v[72:75], v[56:59], v[16:19]\n\t" \
  "global_load_lds_dwordx4 v90, s[84:85] sc1\n\t" \
  "s_add_u32 s84, s84, s88\n\t" \
  "s_addc_u32 s85, s85, 0\n\t" \
  "v_mfma_f32_16x16x32_bf16 v[20:23], v[76:79], v[56:59], v[20:23]\n\t" \
  "ds_read_b128 v[56:59], v81 offset:10240\n\t" \
  "v_mfma_f32_16x16x32_bf16 v[24:27], v[72:75], v[60:63], v[24:27]\n\t" \
  "s_add_u32 m0, %[lw], 24576\n\t" \
  "v_mfma_f32_16x16x32_bf16 v[28:31], v[76:79], v[60:63], v[28:31]\n\t" \
  "ds_read_b128 v[60:63], v81 offset:11264\n\t" \
  "global_load_lds_dwordx4 v91, s[86:87]\n\t" \
  "s_add_u32 s86, s86, s89\n\t" \
  "s_addc_u32 s87, s87, 0\n\t" \
  "s_waitcnt vmcnt(4)\n\t" \
  "s_barrier\n\t" \
  "s_waitcnt lgkmcnt(3)\n\t" \
  "v_mfma_f32_16x16x32_bf16 v[0:3], v[64:67], v[32:35], v[0:3]\n\t" \
  "ds_read_b64_tr_b16 v[72:73], v86 offset:8192\n\t" \
  "v_mfma_f32_16x16x32_bf16 v[4:7], v[68:71], v[32:35], v[4:7]\n\t" \
  "ds_read_b64_tr_b16 v[74:75], v87 offset:8192\n\t" \
  "ds_read_b128 v[32:35], v81 offset:32768\n\t" \
  "v_mfma_f32_16x16x32_bf16 v[8:11], v[64:67], v[36:39], v[8:11]\n\t" \
  "ds_read_b64_tr_b16 v[76:77], v88 offset:8192\n\t" \
  "v_mfma_f32_16x16x32_bf16 v[12:15], v[68:71], v[36:39], v[12:15]\n\t" \
  "ds_read_b64_tr_b16 v[78:79], v89 offset:8192\n\t" \
  "ds_read_b128 v[36:39], v81 offset:33792\n\t" \
  "s_add_u32 m0, %[lw], 32768\n\t" \
  "v_mfma_f32_16x16x32_bf16 v[16:19], v[64:67], v[40:43], v[16:19]\n\t" \
  "global_load_lds_dwordx4 v90, s[84:85] sc1\n\t" \
  "s_add_u32 s84, s84, s88\n\t" \
  "s_addc_u32 s85, s85, 0\n\t" \
  "v_mfma_f32_16x16x32_bf16 v[20:23], v[68:71], v[40:43], v[20:23]\n\t" \
  "ds_read_b128 v[40:43], v81 offset:34816\n\t" \
  "v_mfma_f32_16x16x32_bf16 v[24:27], v[64:67], v[44:47], v[24:27]\n\t" \
  "s_add_u32 m0, %[lw], 49152\n\t" \
  "v_mfma_f32_16x16x32_bf16 v[28:31], v[68:71], v[44:47], v[28:31]\n\t" \
  "ds_read_b128 v[44:47], v81 offset:35840\n\t" \
  "global_load_lds_dwordx4 v91, s[86:87]\n\t" \
  "s_add_u32 s86, s86, s89\n\t" \
  "s_addc_u32 s87, s87, 0\n\t" \
  "s_waitcnt lgkmcnt(3)\n\t" \
  "v_mfma_f32_16x16x32_bf16 v[0:3], v[72:75], v[48:51], v[0:3]\n\t" \
  "ds_read_b64_tr_b16 v[64:65], v86 offset:32768\n\t" \
  "v_mfma_f32_16x16x32_bf16 v[4:7], v[76:79], v[48:51], v[4:7]\n\t" \
  "ds_read_b64_tr_b16 v[66:67], v87 offset:32768\n\t" \
  "ds_read_b128 v[48:51], v81 offset:40960\n\t" \
  "v_mfma_f32_16x16x32_bf16 v[8:11], v[72:75], v[52:55], v[8:11]\n\t" \
  "ds_read_b64_tr_b16 v[68:69], v88 offset:32768\n\t" \
  "v_mfma_f32_16x16x32_bf16 v[12:15], v[76:79], v[52:55], v[12:15]\n\t" \
  "ds_read_b64_tr_b16 v[70:71], v89 offset:32768\n\t" \
  "ds_read_b128 v[52:55], v81 offset:41984\n\t" \
  "s_add_u32 m0, %[lw], 40960\n\t" \
  "v_mfma_f32_16x16x32_bf16 v[16:19], v[72:75], v[56:59], v[16:19]\n\t" \
  "global_load_lds_dwordx4 v90, s[84:85] sc1\n\t" \
  "s_add_u32 s84, s84, s88\n\t" \
  "s_addc_u32 s85, s85, 0\n\t" \
  "v_mfma_f32_16x16x32_bf16 v[20:23], v[76:79], v[56:59], v[20:23]\n\t" \
  "ds_read_b128 v[56:59], v81 offset:43008\n\t" \
  "v_mfma_f32_16x16x32_bf16 v[24:27], v[72:75], v[60:63], v[24:27]\n\t" \
  "s_add_u32 m0, %[lw], 57344\n\t" \
  "v_mfma_f32_16x16x32_bf16 v[28:31], v[76:79], v[60:63], v[28:31]\n\t" \
  "ds_read_b128 v[60:63], v81 offset:44032\n\t" \
  "global_load_lds_dwordx4 v91, s[86:87]\n\t" \
  "s_add_u32 s86, s86, s89\n\t" \
  "s_addc_u32 s87, s87, 0\n\t" \
  "s_waitcnt vmcnt(4)\n\t" \
  "s_barrier\n\t" \
  "s_waitcnt lgkmcnt(3)\n\t" \
  "v_mfma_f32_16x16x32_bf16 v[0:3], v[64:67], v[32:35], v[0:3]\n\t" \
  "ds_read_b64_tr_b16 v[72:73], v86 offset:40960\n\t" \
  "v_mfma_f32_16x16x32_bf16 v[4:7], v[68:71], v[32:35], v[4:7]\n\t" \
  "ds_read_b64_tr_b16 v[74:75], v87 offset:40960\n\t" \
  "ds_read_b128 v[32:35], v80 offset:0\n\t" \
  "v_mfma_f32_16x16x32_bf16 v[8:11], v[64:67], v[36:39], v[8:11]\n\t" \
  "ds_read_b64_tr_b16 v[76:77], v88 offset:40960\n\t" \
  "v_mfma_f32_16x16x32_bf16 v[12:15], v[68:71], v[36:39], v[12:15]\n\t" \
  "ds_read_b64_tr_b16 v[78:79], v89 offset:40960\n\t" \
  "ds_read_b128 v[36:39], v80 offset:1024\n\t" \
  "s_add_u32 m0, %[lw], 65536\n\t" \
  "v_mfma_f32_16x16x32_bf16 v[16:19], v[64:67], v[40:43], v[16:19]\n\t" \
  "global_load_lds_dwordx4 v90, s[84:85] sc1\n\t" \
  "s_add_u32 s84, s84, s88\n\t" \
  "s_addc_u32 s85, s85, 0\n\t" \
  "v_mfma_f32_16x16x32_bf16 v[20:23], v[68:71], v[40:43], v[20:23]\n\t" \
  "ds_read_b128 v[40:43], v80 offset:2048\n\t" \
  "v_mfma_f32_16x16x32_bf16 v[24:27], v[64:67], v[44:47], v[24:27]\n\t" \
  "s_add_u32 m0, %[lw], 81920\n\t" \
  "v_mfma_f32_16x16x32_bf16 v[28:31], v[68:71], v[44:47], v[28:31]\n\t" \
  "ds_read_b128 v[44:47], v80 offset:3072\n\t" \
  "global_load_lds_dwordx4 v91, s[86:87]\n\t" \
  "s_add_u32 s86, s86, s89\n\t" \
  "s_addc_u32 s87, s87, 0\n\t" \
  "s_waitcnt lgkmcnt(3)\n\t" \
  "v_mfma_f32_16x16x32_bf16 v[0:3], v[72:75], v[48:51], v[0:3]\n\t" \
  "ds_read_b64_tr_b16 v[64:65], v82 offset:0\n\t" \
  "v_mfma_f32_16x16x32_bf16 v[4:7], v[76:79], v[48:51], v[4:7]\n\t" \
  "ds_read_b64_tr_b16 v[66:67], v83 offset:0\n\t" \
  "ds_read_b128 v[48:51], v80 offset:8192\n\t" \
  "v_mfma_f32_16x16x32_bf16 v[8:11], v[72:75], v[52:55], v[8:11]\n\t" \
  "ds_read_b64_tr_b16 v[68:69], v84 offset:0\n\t" \
  "v_mfma_f32_16x16x32_bf16 v[12:15], v[76:79], v[52:55], v[12:15]\n\t" \
  "ds_read_b64_tr_b16 v[70:71], v85 offset:0\n\t" \
  "ds_read_b128 v[52:55], v80 offset:9216\n\t" \
  "s_add_u32 m0, %[lw], 73728\n\t" \
  "v_mfma_f32_16x16x32_bf16 v[16:19], v[72:75], v[56:59], v[16:19]\n\t" \
  "global_load_lds_dwordx4 v90, s[84:85] sc1\n\t" \
  "s_add_u32 s84, s84, s88\n\t" \
  "s_addc_u32 s85, s85, 0\n\t" \
  "v_mfma_f32_16x16x32_bf16 v[20:23], v[76:79], v[56:59], v[20:23]\n\t" \
  "ds_read_b128 v[56:59], v80 offset:10240\n\t" \
  "v_mfma_f32_16x16x32_bf16 v[24:27], v[72:75], v[60:63], v[24:27]\n\t" \
  "s_add_u32 m0, %[lw], 90112\n\t" \
  "v_mfma_f32_16x16x32_bf16 v[28:31], v[76:79], v[60:63], v[28:31]\n\t" \
  "ds_read_b128 v[60:63], v80 offset:11264\n\t" \
  "global_load_lds_dwordx4 v91, s[86:87]\n\t" \
  "s_add_u32 s86, s86, s89\n\t" \
  "s_addc_u32 s87, s87, 0\n\t" \
  "s_cmp_lg_u32 s88, 0\n\t" \
  "s_cbranch_scc1 .Lp1_%=\n\t" \
  ".Le_%=:\n\t" \
  "s_waitcnt vmcnt(0) lgkmcnt(0)\n\t" \
  "s_mov_b32 m0, s94\n\t" \
  "s_setprio 0\n\t" \
  "s_nop 7\n\t"
#define HSL 16
__device__ __forceinline__ void job_down(const Args& p, int rblk, int ct, char* smem, __amdgpu_buffer_rsrc_t rsy) {
  if (threadIdx.x < 64) {
    for (unsigned spins = 0;;) {
      asm volatile("" ::: "memory");
      if (__builtin_amdgcn_readfirstlane((int)ld_flag0(rsy, SY_CH + (rblk >> 1) * 128)) >= 27) break;
      __builtin_amdgcn_s_sleep(HSL);
      if (spin_fail(spins, (gu32*)(p.ws + WS_SY + SY_ABORT))) break;
    }
  }
  int tid = threadIdx.x;
  asm volatile("" : "+v"(tid));
  const int lane = tid & 63, r = lane & 15, q = lane >> 4;
  const int wid = __builtin_amdgcn_readfirstlane(tid >> 6), wr = wid >> 2, wc = wid & 3;
  typedef __attribute__((ext_vector_type(32))) float f32x32;
  typedef __attribute__((ext_vector_type(8))) unsigned u32x8;
  f32x32 acc;
  {
    const char* A = p.ws + WS_HF + h_off(p, rblk);
    const char* B = (const char*)(p.wd + ct * 128);
    const unsigned rb = tid >> 4, va = tid * 16, vb = ((rb & 4) * 4 + (rb >> 3) * 4 + (rb & 3)) * (DM * 2) + 16 * ((tid & 15) ^ swzB(rb & 15));
    u32x8 lb;
#pragma unroll
    for (int i = 0; i < 8; ++i)
      lb[i] = (i >> 2) * 65536 + 16384 + offB(8 * q + 4 * (i & 1) + (r >> 2), 2 * (wc * 2 + ((i >> 1) & 1)) + ((r & 3) >> 1)) + 8 * (r & 1);
    LBAR();
#pragma unroll
    for (int t = 0; t < 3; ++t) {
      glds2<true>(A + t * 32768, 16384, va, wid * 1024 + t * 32768);
      glds2(B + t * 64 * DM * 2, 32 * DM * 2, vb, wid * 1024 + t * 32768 + 16384);
    }
    WAIT_V(8);
    LBAR();
    asm volatile(DN_ASM
        : "={v[0:31]}"(acc)
        : "{v[80:81]}"((u32x2){wr * 4096u + lane * 16u, 65536u + wr * 4096u + lane * 16u}), "{v[82:89]}"(lb), "{v90}"(va), "{v91}"(vb),
          [pa] "s"(A + 3 * 32768), [pb] "s"(B + 3 * 64 * DM * 2), [lw] "s"(wid * 1024), [hf] "s"(wr)
        : CLB8(32, 33, 34, 35, 36, 37, 38, 39), CLB8(40, 41, 42, 43, 44, 45, 46, 47), CLB8(48, 49, 50, 51, 52, 53, 54, 55),
          CLB8(56, 57, 58, 59, 60, 61, 62, 63), CLB8(64, 65, 66, 67, 68, 69, 70, 71), CLB8(72, 73, 74, 75, 76, 77, 78, 79),
          "s84", "s85", "s86", "s87", "s88", "s89", "s90", "s91", "s94", "scc", "memory");
  }
  wait_s(p, rsy, rblk, ct * 4, 4, rblk < NFUSE);
  const int e0 = (rblk * 128 + wr * 64 + r) * DM + ct * 128 + wc * 32 + q * 8;
  SX sx[4];
#pragma unroll
  for (int m = 0; m < 4; ++m) sx[m] = get_sx(p, rblk, e0 + m * 16 * DM);
  const __amdgpu_buffer_rsrc_t ro = mkrsrc(p.ws + WS_G, S_LEN * DM * 2);
#pragma unroll
  for (int m = 0; m < 4; ++m) {
    float rr[8];
#pragma unroll
    for (int j = 0; j < 8; ++j) { float d = rbf(acc[8 * m + j]); rr[j] = d * __builtin_amdgcn_rcpf(1.0f + __expf(-d)); }
    put_g(p, ro, rblk, e0 + m * 16 * DM, widen32(cvtpk(rr[0], rr[1]), cvtpk(rr[2], rr[3]), cvtpk(rr[4], rr[5]), cvtpk(rr[6], rr[7])), sx[m]);
  }
  if (rblk >= NFUSE) publish_flags(rsy, SY_FG + (rblk * 8 + ct) * 128, 0, 1);
}

#define PR_ASM_0 \
  "s_cmp_lg_u32 %[kh], 0\n\t" \
  "s_cbranch_scc1 .Lk_%=\n\t" \
  "s_mov_b32 s94, m0\n\t" \
  "s_mov_b64 s[92:93], %[x0]\n\t" \
  "s_mov_b32 s86, %[g0]\n\t" \
  "s_add_u32 s87, s86, %[t0]\n\t" \
  "s_sub_i32 s88, s86, 3\n\t" \
  "ds_read_b128 v[176:179], v208 offset:0\n\t" \
  "ds_read_b128 v[180:183], v208 offset:16384\n\t" \
  "ds_read_b128 v[184:187], v209 offset:0\n\t" \
  "ds_read_b128 v[188:191], v209 offset:16384\n\t" \
  "ds_read_b128 v[192:195], v210 offset:0\n\t" \
  "ds_read_b128 v[196:199], v210 offset:16384\n\t" \
  "ds_read_b128 v[200:203], v211 offset:0\n\t" \
  "ds_read_b128 v[204:207], v211 offset:16384\n\t" \
  "s_branch .Lfb00_%=\n\t" \
  ".Lp0_%=:\n\t" \
  "s_cmp_le_u32 s86, 5\n\t" \
  "s_cbranch_scc1 .Lf00_%=\n\t" \
  ".Lfb00_%=:\n\t" \
  "s_waitcnt lgkmcnt(6)\n\t" \
  "v_mfma_f32_16x16x32_bf16 v[128:131], v[0:3], v[176:179], 0\n\t" \
  "v_mfma_f32_16x16x32_bf16 v[132:135], v[4:7], v[176:179], 0\n\t" \
  "ds_read_b128 v[176:179], v208 offset:256\n\t" \
  "s_add_u32 m0, %[lw], 67584\n\t" \
  "v_mfma_f32_16x16x32_bf16 v[136:139], v[0:3], v[180:183], 0\n\t" \
  "v_mfma_f32_16x16x32_bf16 v[140:143], v[4:7], v[180:183], 0\n\t" \
  "ds_read_b128 v[180:183], v208 offset:16640\n\t" \
  "global_load_lds_dwordx4 v218, s[92:93] sc1\n\t" \
  "s_waitcnt lgkmcnt(6)\n\t" \
  "v_mfma_f32_16x16x32_bf16 v[128:131], v[8:11], v[184:187], v[128:131]\n\t" \
  "v_mfma_f32_16x16x32_bf16 v[132:135], v[12:15], v[184:187], v[132:135]\n\t" \
  "ds_read_b128 v[184:187], v209 offset:256\n\t" \
  "v_mfma_f32_16x16x32_bf16 v[136:139], v[8:11], v[188:191], v[136:139]\n\t" \
  "v_mfma_f32_16x16x32_bf16 v[140:143], v[12:15], v[188:191], v[140:143]\n\t" \
  "ds_read_b128 v[188:191], v209 offset:16640\n\t" \
  "ds_write_b128 v220, v[152:155] offset:0\n\t" \
  "ds_write_b128 v220, v[156:159] offset:1024\n\t" \
  "s_waitcnt lgkmcnt(8)\n\t" \
  "v_mfma_f32_16x16x32_bf16 v[128:131], v[16:19], v[192:195], v[128:131]\n\t" \
  "v_mfma_f32_16x16x32_bf16 v[132:135], v[20:23], v[192:195], v[132:135]\n\t" \
  "ds_read_b128 v[192:195], v210 offset:256\n\t" \
  "s_add_u32 m0, %[lw], 68608\n\t" \
  "v_mfma_f32_16x16x32_bf16 v[136:139], v[16:19], v[196:199], v[136:139]\n\t" \
  "v_mfma_f32_16x16x32_bf16 v[140:143], v[20:23], v[196:199], v[140:143]\n\t" \
  "ds_read_b128 v[196:199], v210 offset:16640\n\t" \
  "global_load_lds_dwordx4 v219, s[92:93] sc1\n\t" \
  "s_lshr_b32 s90, s87, 7\n\t" \
  "s_xor_b32 s90, s90, %[s0]\n\t" \
  "s_and_b32 s90, s90, 1\n\t" \
  "s_lshl1_add_u32 s90, s87, s90\n\t" \
  "s_lshl_b32 s90, s90, 13\n\t" \
  "s_cmp_lt_i32 s86, 6\n\t" \
  "s_cselect_b32 s90, 0x70000000, s90\n\t" \
  "s_waitcnt lgkmcnt(8)\n\t" \
  "v_mfma_f32_16x16x32_bf16 v[128:131], v[24:27], v[200:203], v[128:131]\n\t" \
  "v_mfma_f32_16x16x32_bf16 v[132:135], v[28:31], v[200:203], v[132:135]\n\t" \
  "ds_read_b128 v[200:203], v211 offset:256\n\t" \
  "v_mfma_f32_16x16x32_bf16 v[136:139], v[24:27], v[204:207], v[136:139]\n\t" \
  "v_mfma_f32_16x16x32_bf16 v[140:143], v[28:31], v[204:207], v[140:143]\n\t" \
  "ds_read_b128 v[204:207], v211 offset:16640\n\t" \
  "s_waitcnt lgkmcnt(4)\n\t" \
  "s_cmp_lt_u32 s86, 6\n\t" \
  "s_cbranch_scc1 .Lw00_%=\n\t" \
  "s_waitcnt vmcnt(4)\n\t" \
  "s_barrier\n\t" \
  ".Lg00_%=:\n\t" \
  "s_add_u32 s92, s92, 512\n\t" \
  "s_addc_u32 s93, s93, 0\n\t" \
  "v_mfma_f32_16x16x32_bf16 v[128:131], v[32:35], v[176:179], v[128:131]\n\t" \
  "v_mfma_f32_16x16x32_bf16 v[132:135], v[36:39], v[176:179], v[132:135]\n\t" \
  "ds_read_b128 v[176:179], v208 offset:32768\n\t" \
  "s_add_u32 m0, %[lw], 98304\n\t" \
  "v_mfma_f32_16x16x32_bf16 v[136:139], v[32:35], v[180:183], v[136:139]\n\t" \
  "v_mfma_f32_16x16x32_bf16 v[140:143], v[36:39], v[180:183], v[140:143]\n\t" \
  "ds_read_b128 v[180:183], v208 offset:49152\n\t" \
  "global_load_lds_dwordx4 v216, s[92:93] sc1\n\t" \
  "ds_read_b128 v[160:163], v221 offset:0\n\t" \
  "ds_read_b128 v[164:167], v221 offset:1024\n\t" \
  "v_mfma_f32_16x16x32_bf16 v[128:131], v[40:43], v[184:187], v[128:131]\n\t" \
  "v_mfma_f32_16x16x32_bf16 v[132:135], v[44:47], v[184:187], v[132:135]\n\t" \
  "ds_read_b128 v[184:187], v209 offset:32768\n\t" \
  "v_mfma_f32_16x16x32_bf16 v[136:139], v[40:43], v[188:191], v[136:139]\n\t" \
  "v_mfma_f32_16x16x32_bf16 v[140:143], v[44:47], v[188:191], v[140:143]\n\t" \
  "ds_read_b128 v[188:191], v209 offset:49152\n\t" \
  "s_waitcnt lgkmcnt(8)\n\t" \
  "v_mfma_f32_16x16x32_bf16 v[128:131], v[48:51], v[192:195], v[128:131]\n\t" \
  "v_mfma_f32_16x16x32_bf16 v[132:135], v[52:55], v[192:195], v[132:135]\n\t" \
  "ds_read_b128 v[192:195], v210 offset:32768\n\t" \
  "s_add_u32 m0, %[lw], 99328\n\t" \
  "v_mfma_f32_16x16x32_bf16 v[136:139], v[48:51], v[196:199], v[136:139]\n\t" \
  "v_mfma_f32_16x16x32_bf16 v[140:143], v[52:55], v[196:199], v[140:143]\n\t" \
  "ds_read_b128 v[196:199], v210 offset:49152\n\t" \
  "global_load_lds_dwordx4 v217, s[92:93] sc1\n\t" \
  "s_waitcnt lgkmcnt(4)\n\t" \
  "v_add_f32 v160, v160, v144\n\t" \
  "v_add_f32 v161, v161, v145\n\t" \
  "v_add_f32 v162, v162, v146\n\t" \
  "v_add_f32 v163, v163, v147\n\t" \
  "v_add_f32 v164, v164, v148\n\t" \
  "v_add_f32 v165, v165, v149\n\t" \
  "v_add_f32 v166, v166, v150\n\t" \
  "v_add_f32 v167, v167, v151\n\t" \
  "v_mfma_f32_16x16x32_bf16 v[128:131], v[56:59], v[200:203], v[128:131]\n\t" \
  "v_mfma_f32_16x16x32_bf16 v[132:135], v[60:63], v[200:203], v[132:135]\n\t" \
  "ds_read_b128 v[200:203], v211 offset:32768\n\t" \
  "v_mfma_f32_16x16x32_bf16 v[136:139], v[56:59], v[204:207], v[136:139]\n\t" \
  "v_mfma_f32_16x16x32_bf16 v[140:143], v[60:63], v[204:207], v[140:143]\n\t" \
  "ds_read_b128 v[204:207], v211 offset:49152\n\t" \
  "v_cvt_pk_bf16_f32 v168, v160, v161\n\t" \
  "v_cvt_pk_bf16_f32 v169, v162, v163\n\t" \
  "v_cvt_pk_bf16_f32 v170, v164, v165\n\t" \
  "v_cvt_pk_bf16_f32 v171, v166, v167\n\t" \
  "buffer_store_dwordx4 v[168:171], v172, %[rt], s90 offen sc1\n\t" \
  "v_mfma_f32_16x16x32_bf16 v[128:131], v[64:67], v[176:179], v[128:131]\n\t" \
  "v_mfma_f32_16x16x32_bf16 v[132:135], v[68:71], v[176:179], v[132:135]\n\t" \
  "ds_read_b128 v[176:179], v208 offset:33024\n\t" \
  "s_add_u32 m0, %[lw], 100352\n\t" \
  "v_mfma_f32_16x16x32_bf16 v[136:139], v[64:67], v[180:183], v[136:139]\n\t" \
  "v_mfma_f32_16x16x32_bf16 v[140:143], v[68:71], v[180:183], v[140:143]\n\t" \
  "ds_read_b128 v[180:183], v208 offset:49408\n\t" \
  "global_load_lds_dwordx4 v218, s[92:93] sc1\n\t" \
  "s_waitcnt lgkmcnt(6)\n\t" \
  "v_mfma_f32_16x16x32_bf16 v[128:131], v[72:75], v[184:187], v[128:131]\n\t" \
  "v_mfma_f32_16x16x32_bf16 v[132:135], v[76:79], v[184:187], v[132:135]\n\t" \
  "ds_read_b128 v[184:187], v209 offset:33024\n\t" \
  "v_mfma_f32_16x16x32_bf16 v[136:139], v[72:75], v[188:191], v[136:139]\n\t" \
  "v_mfma_f32_16x16x32_bf16 v[140:143], v[76:79], v[188:191], v[140:143]\n\t" \
  "ds_read_b128 v[188:191], v209 offset:49408\n\t" \
  "s_waitcnt lgkmcnt(6)\n\t" \
  "v_mfma_f32_16x16x32_bf16 v[128:131], v[80:83], v[192:195], v[128:131]\n\t" \
  "v_mfma_f32_16x16x32_bf16 v[132:135], v[84:87], v[192:195], v[132:135]\n\t" \
  "ds_read_b128 v[192:195], v210 offset:33024\n\t" \
  "s_add_u32 m0, %[lw], 101376\n\t" \
  "v_mfma_f32_16x16x32_bf16 v[136:139], v[80:83], v[196:199], v[136:139]\n\t" \
  "v_mfma_f32_16x16x32_bf16 v[140:143], v[84:87], v[196:199], v[140:143]\n\t" \
  "ds_read_b128 v[196:199], v210 offset:49408\n\t" \
  "global_load_lds_dwordx4 v219, s[92:93] sc1\n\t" \
  "s_waitcnt lgkmcnt(6)\n\t" \
  "v_mfma_f32_16x16x32_bf16 v[128:131], v[88:91], v[200:203], v[128:131]\n\t" \
  "v_mfma_f32_16x16x32_bf16 v[132:135], v[92:95], v[200:203], v[132:135]\n\t" \
  "ds_read_b128 v[200:203], v211 offset:33024\n\t" \
  "v_mfma_f32_16x16x32_bf16 v[136:139], v[88:91], v[204:207], v[136:139]\n\t" \
  "v_mfma_f32_16x16x32_bf16 v[140:143], v[92:95], v[204:207], v[140:143]\n\t" \
  "ds_read_b128 v[204:207], v211 offset:49408\n\t" \
  "s_cmp_lt_u32 s86, 8\n\t" \
  "s_cbranch_scc1 .Lw10_%=\n\t" \
  "s_waitcnt vmcnt(5)\n\t" \
  "s_barrier\n\t" \
  ".Lg10_%=:\n\t" \
  "s_add_u32 s92, s92, 65024\n\t" \
  "s_addc_u32 s93, s93, 0\n\t" \
  "s_waitcnt lgkmcnt(6)\n\t" \
  "v_mfma_f32_16x16x32_bf16 v[128:131], v[96:99], v[176:179], v[128:131]\n\t" \
  "v_mfma_f32_16x16x32_bf16 v[132:135], v[100:103], v[176:179], v[132:135]\n\t" \
  "ds_read_b128 v[176:179], v212 offset:0\n\t" \
  "s_add_u32 m0, %[lw], 0\n\t" \
  "v_mfma_f32_16x16x32_bf16 v[136:139], v[96:99], v[180:183], v[136:139]\n\t" \
  "v_mfma_f32_16x16x32_bf16 v[140:143], v[100:103], v[180:183], v[140:143]\n\t" \
  "ds_read_b128 v[180:183], v212 offset:16384\n\t" \
  "global_load_lds_dwordx4 v216, s[92:93] sc1\n\t" \
  "s_waitcnt lgkmcnt(6)\n\t" \
  "v_mfma_f32_16x16x32_bf16 v[128:131], v[104:107], v[184:187], v[128:131]\n\t" \
  "v_mfma_f32_16x16x32_bf16 v[132:135], v[108:111], v[184:187], v[132:135]\n\t" \
  "ds_read_b128 v[184:187], v213 offset:0\n\t" \
  "v_mfma_f32_16x16x32_bf16 v[136:139], v[104:107], v[188:191], v[136:139]\n\t" \
  "v_mfma_f32_16x16x32_bf16 v[140:143], v[108:111], v[188:191], v[140:143]\n\t" \
  "ds_read_b128 v[188:191], v213 offset:16384\n\t" \
  "s_waitcnt lgkmcnt(6)\n\t" \
  "v_mfma_f32_16x16x32_bf16 v[128:131], v[112:115], v[192:195], v[128:131]\n\t" \
  "v_mfma_f32_16x16x32_bf16 v[132:135], v[116:119], v[192:195], v[132:135]\n\t" \
  "ds_read_b128 v[192:195], v214 offset:0\n\t" \
  "s_add_u32 m0, %[lw], 1024\n\t" \
  "v_mfma_f32_16x16x32_bf16 v[136:139], v[112:115], v[196:199], v[136:139]\n\t" \
  "v_mfma_f32_16x16x32_bf16 v[140:143], v[116:119], v[196:199], v[140:143]\n\t" \
  "ds_read_b128 v[196:199], v214 offset:16384\n\t" \
  "global_load_lds_dwordx4 v217, s[92:93] sc1\n\t" \
  "s_waitcnt lgkmcnt(6)\n\t" \
  "v_mfma_f32_16x16x32_bf16 v[128:131], v[120:123], v[200:203], v[128:131]\n\t" \
  "v_mfma_f32_16x16x32_bf16 v[132:135], v[124:127], v[200:203], v[132:135]\n\t" \
  "ds_read_b128 v[200:203], v215 offset:0\n\t" \
  "v_mfma_f32_16x16x32_bf16 v[136:139], v[120:123], v[204:207], v[136:139]\n\t" \
  "v_mfma_f32_16x16x32_bf16 v[140:143], v[124:127], v[204:207], v[140:143]\n\t" \
  "ds_read_b128 v[204:207], v215 offset:16384\n\t" \
  "s_add_u32 s86, s86, 1\n\t" \
  "s_add_u32 s87, s87, 1\n\t" \
  "s_cmp_le_u32 s86, 5\n\t" \
  "s_cbranch_scc1 .Lf10_%=\n\t" \
  ".Lfb10_%=:\n\t" \
  "s_waitcnt lgkmcnt(6)\n\t" \
  "v_mfma_f32_16x16x32_bf16 v[144:147], v[0:3], v[176:179], 0\n\t" \
  "v_mfma_f32_16x16x32_bf16 v[148:151], v[4:7], v[176:179], 0\n\t" \
  "ds_read_b128 v[176:179], v212 offset:256\n\t" \
  "s_add_u32 m0, %[lw], 2048\n\t" \
  "v_mfma_f32_16x16x32_bf16 v[152:155], v[0:3], v[180:183], 0\n\t" \
  "v_mfma_f32_16x16x32_bf16 v[156:159], v[4:7], v[180:183], 0\n\t" \
  "ds_read_b128 v[180:183], v212 offset:16640\n\t" \
  "global_load_lds_dwordx4 v218, s[92:93] sc1\n\t" \
  "s_waitcnt lgkmcnt(6)\n\t" \
  "v_mfma_f32_16x16x32_bf16 v[144:147], v[8:11], v[184:187], v[144:147]\n\t" \
  "v_mfma_f32_16x16x32_bf16 v[148:151], v[12:15], v[184:187], v[148:151]\n\t" \
  "ds_read_b128 v[184:187], v213 offset:256\n\t" \
  "v_mfma_f32_16x16x32_bf16 v[152:155], v[8:11], v[188:191], v[152:155]\n\t" \
  "v_mfma_f32_16x16x32_bf16 v[156:159], v[12:15], v[188:191], v[156:159]\n\t" \
  "ds_read_b128 v[188:191], v213 offset:16640\n\t" \
  "ds_write_b128 v220, v[136:139] offset:0\n\t" \
  "ds_write_b128 v220, v[140:143] offset:1024\n\t" \
  "s_waitcnt lgkmcnt(8)\n\t" \
  "v_mfma_f32_16x16x32_bf16 v[144:147], v[16:19], v[192:195], v[144:147]\n\t" \
  "v_mfma_f32_16x16x32_bf16 v[148:151], v[20:23], v[192:195], v[148:151]\n\t" \
  "ds_read_b128 v[192:195], v214 offset:256\n\t" \
  "s_add_u32 m0, %[lw], 3072\n\t" \
  "v_mfma_f32_16x16x32_bf16 v[152:155], v[16:19], v[196:199], v[152:155]\n\t" \
  "v_mfma_f32_16x16x32_bf16 v[156:159], v[20:23], v[196:199], v[156:159]\n\t" \
  "ds_read_b128 v[196:199], v214 offset:16640\n\t" \
  "global_load_lds_dwordx4 v219, s[92:93] sc1\n\t" \
  "s_lshr_b32 s90, s87, 7\n\t" \
  "s_xor_b32 s90, s90, %[s0]\n\t" \
  "s_and_b32 s90, s90, 1\n\t" \
  "s_lshl1_add_u32 s90, s87, s90\n\t" \
  "s_lshl_b32 s90, s90, 13\n\t" \
  "s_cmp_lt_i32 s86, 6\n\t" \
  "s_cselect_b32 s90, 0x70000000, s90\n\t" \
  "s_waitcnt lgkmcnt(8)\n\t" \
  "v_mfma_f32_16x16x32_bf16 v[144:147], v[24:27], v[200:203], v[144:147]\n\t" \
  "v_mfma_f32_16x16x32_bf16 v[148:151], v[28:31], v[200:203], v[148:151]\n\t" \
  "ds_read_b128 v[200:203], v215 offset:256\n\t" \
  "v_mfma_f32_16x16x32_bf16 v[152:155], v[24:27], v[204:207], v[152:155]\n\t" \
  "v_mfma_f32_16x16x32_bf16 v[156:159], v[28:31], v[204:207], v[156:159]\n\t" \
  "ds_read_b128 v[204:207], v215 offset:16640\n\t" \
  "s_waitcnt lgkmcnt(4)\n\t" \
  "s_cmp_lt_u32 s86, 6\n\t" \
  "s_cbranch_scc1 .Lw20_%=\n\t" \
  "s_waitcnt vmcnt(4)\n\t" \
  "s_barrier\n\t" \
  ".Lg20_%=:\n\t" \
  "s_add_u32 s92, s92, 512\n\t" \
  "s_addc_u32 s93, s93, 0\n\t" \
  "v_mfma_f32_16x16x32_bf16 v[144:147], v[32:35], v[176:179], v[144:147]\n\t" \
  "v_mfma_f32_16x16x32_bf16 v[148:151], v[36:39], v[176:179], v[148:151]\n\t" \
  "ds_read_b128 v[176:179], v212 offset:32768\n\t" \
  "s_add_u32 m0, %[lw], 32768\n\t" \
  "v_mfma_f32_16x16x32_bf16 v[152:155], v[32:35], v[180:183], v[152:155]\n\t" \
  "v_mfma_f32_16x16x32_bf16 v[156:159], v[36:39], v[180:183], v[156:159]\n\t" \
  "ds_read_b128 v[180:183], v212 offset:49152\n\t" \
  "global_load_lds_dwordx4 v216, s[92:93] sc1\n\t" \
  "ds_read_b128 v[160:163], v221 offset:0\n\t" \
  "ds_read_b128 v[164:167], v221 offset:1024\n\t" \
  "v_mfma_f32_16x16x32_bf16 v[144:147], v[40:43], v[184:187], v[144:147]\n\t" \
  "v_mfma_f32_16x16x32_bf16 v[148:151], v[44:47], v[184:187], v[148:151]\n\t" \
  "ds_read_b128 v[184:187], v213 offset:32768\n\t" \
  "v_mfma_f32_16x16x32_bf16 v[152:155], v[40:43], v[188:191], v[152:155]\n\t" \
  "v_mfma_f32_16x16x32_bf16 v[156:159], v[44:47], v[188:191], v[156:159]\n\t" \
  "ds_read_b128 v[188:191], v213 offset:49152\n\t" \
  "s_waitcnt lgkmcnt(8)\n\t" \
  "v_mfma_f32_16x16x32_bf16 v[144:147], v[48:51], v[192:195], v[144:147]\n\t" \
  "v_mfma_f32_16x16x32_bf16 v[148:151], v[52:55], v[192:195], v[148:151]\n\t" \
  "ds_read_b128 v[192:195], v214 offset:32768\n\t" \
  "s_add_u32 m0, %[lw], 33792\n\t" \
  "v_mfma_f32_16x16x32_bf16 v[152:155], v[48:51], v[196:199], v[152:155]\n\t" \
  "v_mfma_f32_16x16x32_bf16 v[156:159], v[52:55], v[196:199], v[156:159]\n\t" \
  "ds_read_b128 v[196:199], v214 offset:49152\n\t" \
  "global_load_lds_dwordx4 v217, s[92:93] sc1\n\t" \
  "s_waitcnt lgkmcnt(4)\n\t" \
  "v_add_f32 v160, v160, v128\n\t" \
  "v_add_f32 v161, v161, v129\n\t" \
  "v_add_f32 v162, v162, v130\n\t" \
  "v_add_f32 v163, v163, v131\n\t" \
  "v_add_f32 v164, v164, v132\n\t" \
  "v_add_f32 v165, v165, v133\n\t" \
  "v_add_f32 v166, v166, v134\n\t" \
  "v_add_f32 v167, v167, v135\n\t" \
  "v_mfma_f32_16x16x32_bf16 v[144:147], v[56:59], v[200:203], v[144:147]\n\t" \
  "v_mfma_f32_16x16x32_bf16 v[148:151], v[60:63], v[200:203], v[148:151]\n\t" \
  "ds_read_b128 v[200:203], v215 offset:32768\n\t" \
  "v_mfma_f32_16x16x32_bf16 v[152:155], v[56:59], v[204:207], v[152:155]\n\t" \
  "v_mfma_f32_16x16x32_bf16 v[156:159], v[60:63], v[204:207], v[156:159]\n\t" \
  "ds_read_b128 v[204:207], v215 offset:49152\n\t" \
  "v_cvt_pk_bf16_f32 v168, v160, v161\n\t" \
  "v_cvt_pk_bf16_f32 v169, v162, v163\n\t" \
  "v_cvt_pk_bf16_f32 v170, v164, v165\n\t" \
  "v_cvt_pk_bf16_f32 v171, v166, v167\n\t" \
  "buffer_store_dwordx4 v[168:171], v172, %[rt], s90 offen sc1\n\t" \
  "v_mfma_f32_16x16x32_bf16 v[144:147], v[64:67], v[176:179], v[144:147]\n\t" \
  "v_mfma_f32_16x16x32_bf16 v[148:151], v[68:71], v[176:179], v[148:151]\n\t" \
  "ds_read_b128 v[176:179], v212 offset:33024\n\t" \
  "s_add_u32 m0, %[lw], 34816\n\t" \
  "v_mfma_f32_16x16x32_bf16 v[152:155], v[64:67], v[180:183], v[152:155]\n\t" \
  "v_mfma_f32_16x16x32_bf16 v[156:159], v[68:71], v[180:183], v[156:159]\n\t" \
  "ds_read_b128 v[180:183], v212 offset:49408\n\t" \
  "global_load_lds_dwordx4 v218, s[92:93] sc1\n\t" \
  "s_waitcnt lgkmcnt(6)\n\t" \
  "v_mfma_f32_16x16x32_bf16 v[144:147], v[72:75], v[184:187], v[144:147]\n\t" \
  "v_mfma_f32_16x16x32_bf16 v[148:151], v[76:79], v[184:187], v[148:151]\n\t" \
  "ds_read_b128 v[184:187], v213 offset:33024\n\t" \
  "v_mfma_f32_16x16x32_bf16 v[152:155], v[72:75], v[188:191], v[152:155]\n\t" \
  "v_mfma_f32_16x16x32_bf16 v[156:159], v[76:79], v[188:191], v[156:159]\n\t" \
  "ds_read_b128 v[188:191], v213 offset:49408\n\t" \
  "s_waitcnt lgkmcnt(6)\n\t" \
  "v_mfma_f32_16x16x32_bf16 v[144:147], v[80:83], v[192:195], v[144:147]\n\t" \
  "v_mfma_f32_16x16x32_bf16 v[148:151], v[84:87], v[192:195], v[148:151]\n\t" \
  "ds_read_b128 v[192:195], v214 offset:33024\n\t" \
  "s_add_u32 m0, %[lw], 35840\n\t" \
  "v_mfma_f32_16x16x32_bf16 v[152:155], v[80:83], v[196:199], v[152:155]\n\t" \
  "v_mfma_f32_16x16x32_bf16 v[156:159], v[84:87], v[196:199], v[156:159]\n\t" \
  "ds_read_b128 v[196:199], v214 offset:49408\n\t" \
  "global_load_lds_dwordx4 v219, s[92:93] sc1\n\t" \
  "s_waitcnt lgkmcnt(6)\n\t" \
  "v_mfma_f32_16x16x32_bf16 v[144:147], v[88:91], v[200:203], v[144:147]\n\t" \
  "v_mfma_f32_16x16x32_bf16 v[148:151], v[92:95], v[200:203], v[148:151]\n\t" \
  "ds_read_b128 v[200:203], v215 offset:33024\n\t" \
  "v_mfma_f32_16x16x32_bf16 v[152:155], v[88:91], v[204:207], v[152:155]\n\t" \
  "v_mfma_f32_16x16x32_bf16 v[156:159], v[92:95], v[204:207], v[156:159]\n\t" \
  "ds_read_b128 v[204:207], v215 offset:49408\n\t" \
  "s_cmp_lt_u32 s86, 8\n\t" \
  "s_cbranch_scc1 .Lw30_%=\n\t" \
  "s_waitcnt vmcnt(5)\n\t" \
  "s_barrier\n\t" \
  ".Lg30_%=:\n\t" \
  "s_add_u32 s92, s92, 65024\n\t" \
  "s_addc_u32 s93, s93, 0\n\t" \
  "s_waitcnt lgkmcnt(6)\n\t" \
  "v_mfma_f32_16x16x32_bf16 v[144:147], v[96:99], v[176:179], v[144:147]\n\t" \
  "v_mfma_f32_16x16x32_bf16 v[148:151], v[100:103], v[176:179], v[148:151]\n\t" \
  "ds_read_b128 v[176:179], v208 offset:0\n\t" \
  "s_add_u32 m0, %[lw], 65536\n\t" \
  "v_mfma_f32_16x16x32_bf16 v[152:155], v[96:99], v[180:183], v[152:155]\n\t" \
  "v_mfma_f32_16x16x32_bf16 v[156:159], v[100:103], v[180:183], v[156:159]\n\t" \
  "ds_read_b128 v[180:183], v208 offset:16384\n\t" \
  "global_load_lds_dwordx4 v216, s[92:93] sc1\n\t" \
  "s_waitcnt lgkmcnt(6)\n\t" \
  "v_mfma_f32_16x16x32_bf16 v[144:147], v[104:107], v[184:187], v[144:147]\n\t" \
  "v_mfma_f32_16x16x32_bf16 v[148:151], v[108:111], v[184:187], v[148:151]\n\t" \
  "ds_read_b128 v[184:187], v209 offset:0\n\t" \
  "v_mfma_f32_16x16x32_bf16 v[152:155], v[104:107], v[188:191], v[152:155]\n\t" \
  "v_mfma_f32_16x16x32_bf16 v[156:159], v[108:111], v[188:191], v[156:159]\n\t" \
  "ds_read_b128 v[188:191], v209 offset:16384\n\t" \
  "s_waitcnt lgkmcnt(6)\n\t" \
  "v_mfma_f32_16x16x32_bf16 v[144:147], v[112:115], v[192:195], v[144:147]\n\t" \
  "v_mfma_f32_16x16x32_bf16 v[148:151], v[116:119], v[192:195], v[148:151]\n\t" \
  "ds_read_b128 v[192:195], v210 offset:0\n\t" \
  "s_add_u32 m0, %[lw], 66560\n\t" \
  "v_mfma_f32_16x16x32_bf16 v[152:155], v[112:115], v[196:199], v[152:155]\n\t" \
  "v_mfma_f32_16x16x32_bf16 v[156:159], v[116:119], v[196:199], v[156:159]\n\t" \
  "ds_read_b128 v[196:199], v210 offset:16384\n\t" \
  "global_load_lds_dwordx4 v217, s[92:93] sc1\n\t" \
  "s_waitcnt lgkmcnt(6)\n\t" \
  "v_mfma_f32_16x16x32_bf16 v[144:147], v[120:123], v[200:203], v[144:147]\n\t" \
  "v_mfma_f32_16x16x32_bf16 v[148:151], v[124:127], v[200:203], v[148:151]\n\t" \
  "ds_read_b128 v[200:203], v211 offset:0\n\t" \
  "v_mfma_f32_16x16x32_bf16 v[152:155], v[120:123], v[204:207], v[152:155]\n\t" \
  "v_mfma_f32_16x16x32_bf16 v[156:159], v[124:127], v[204:207], v[156:159]\n\t" \
  "ds_read_b128 v[204:207], v211 offset:16384\n\t" \
  "s_add_u32 s86, s86, 1\n\t" \
  "s_add_u32 s87, s87, 1\n\t" \
  "s_cmp_lt_u32 s86, %[end]\n\t" \
  "s_cbranch_scc1 .Lp0_%=\n\t" \
  "s_branch .Lx0_%=\n\t" \
  ".Lf00_%=:\n\t" \
  "s_nop 7\n\t" \
  "ds_write_b128 v220, v[152:155] offset:0\n\t" \
  "ds_write_b128 v220, v[156:159] offset:1024\n\t" \
  "s_lshr_b32 s90, s87, 7\n\t" \
  "s_xor_b32 s90, s90, %[s0]\n\t" \
  "s_and_b32 s90, s90, 1\n\t" \
  "s_lshl1_add_u32 s90, s87, s90\n\t" \
  "s_lshl_b32 s90, s90, 13\n\t" \
  "s_cmp_lt_i32 s86, 1\n\t" \
  "s_cselect_b32 s90, 0x70000000, s90\n\t" \
  "s_waitcnt lgkmcnt(0)\n\t" \
  "s_barrier\n\t" \
  "ds_read_b128 v[160:163], v221 offset:0\n\t" \
  "ds_read_b128 v[164:167], v221 offset:1024\n\t" \
  "s_waitcnt lgkmcnt(0)\n\t" \
  "v_add_f32 v160, v160, v144\n\t" \
  "v_add_f32 v161, v161, v145\n\t" \
  "v_add_f32 v162, v162, v146\n\t" \
  "v_add_f32 v163, v163, v147\n\t" \
  "v_add_f32 v164, v164, v148\n\t" \
  "v_add_f32 v165, v165, v149\n\t" \
  "v_add_f32 v166, v166, v150\n\t" \
  "v_add_f32 v167, v167, v151\n\t" \
  "v_cvt_pk_bf16_f32 v168, v160, v161\n\t" \
  "v_cvt_pk_bf16_f32 v169, v162, v163\n\t" \
  "v_cvt_pk_bf16_f32 v170, v164, v165\n\t" \
  "v_cvt_pk_bf16_f32 v171, v166, v167\n\t" \
  "buffer_store_dwordx4 v[168:171], v172, %[rt], s90 offen sc1\n\t" \
  "s_branch .Lfb00_%=\n\t" \
  ".Lf10_%=:\n\t" \
  "s_nop 7\n\t" \
  "ds_write_b128 v220, v[136:139] offset:0\n\t" \
  "ds_write_b128 v220, v[140:143] offset:1024\n\t" \
  "s_lshr_b32 s90, s87, 7\n\t" \
  "s_xor_b32 s90, s90, %[s0]\n\t" \
  "s_and_b32 s90, s90, 1\n\t" \
  "s_lshl1_add_u32 s90, s87, s90\n\t" \
  "s_lshl_b32 s90, s90, 13\n\t" \
  "s_cmp_lt_i32 s86, 1\n\t" \
  "s_cselect_b32 s90, 0x70000000, s90\n\t" \
  "s_waitcnt lgkmcnt(0)\n\t" \
  "s_barrier\n\t" \
  "ds_read_b128 v[160:163], v221 offset:0\n\t" \
  "ds_read_b128 v[164:167], v221 offset:1024\n\t" \
  "s_waitcnt lgkmcnt(0)\n\t" \
  "v_add_f32 v160, v160, v128\n\t" \
  "v_add_f32 v161, v161, v129\n\t" \
  "v_add_f32 v162, v162, v130\n\t" \
  "v_add_f32 v163, v163, v131\n\t" \
  "v_add_f32 v164, v164, v132\n\t" \
  "v_add_f32 v165, v165, v133\n\t" \
  "v_add_f32 v166, v166, v134\n\t" \
  "v_add_f32 v167, v167, v135\n\t" \
  "v_cvt_pk_bf16_f32 v168, v160, v161\n\t" \
  "v_cvt_pk_bf16_f32 v169, v162, v163\n\t" \
  "v_cvt_pk_bf16_f32 v170, v164, v165\n\t" \
  "v_cvt_pk_bf16_f32 v171, v166, v167\n\t" \
  "buffer_store_dwordx4 v[168:171], v172, %[rt], s90 offen sc1\n\t" \
  "s_branch .Lfb10_%=\n\t" \
  ".Lw00_%=:\n\t" \
  "s_waitcnt vmcnt(2)\n\t" \
  "s_barrier\n\t" \
  "s_branch .Lg00_%=\n\t" \
  ".Lw10_%=:\n\t" \
  "s_waitcnt vmcnt(2)\n\t" \
  "s_barrier\n\t" \
  "s_branch .Lg10_%=\n\t" \
  ".Lw20_%=:\n\t" \
  "s_waitcnt vmcnt(2)\n\t" \
  "s_barrier\n\t" \
  "s_branch .Lg20_%=\n\t" \
  ".Lw30_%=:\n\t" \
  "s_waitcnt vmcnt(2)\n\t" \
  "s_barrier\n\t" \
  "s_branch .Lg30_%=\n\t" \
  ".Lx0_%=:\n\t" \
  "s_waitcnt lgkmcnt(0)\n\t" \
  "s_setprio 0\n\t" \
  "s_mov_b32 m0, s94\n\t" \
  "s_branch .Le_%=\n\t" \
  ".Lk_%=:\n\t" \
  "s_mov_b32 s94, m0\n\t" \
  "s_mov_b64 s[92:93], %[x0]\n\t" \
  "s_mov_b32 s86, %[g0]\n\t" \
  "s_add_u32 s87, s86, %[t0]\n\t" \
  "s_sub_i32 s88, s86, 3\n\t" \
  "ds_read_b128 v[176:179], v208 offset:0\n\t" \
  "ds_read_b128 v[180:183], v208 offset:16384\n\t" \
  "ds_read_b128 v[184:187], v209 offset:0\n\t" \
  "ds_read_b128 v[188:191], v209 offset:16384\n\t" \
  "ds_read_b128 v[192:195], v210 offset:0\n\t" \
  "ds_read_b128 v[196:199], v210 offset:16384\n\t" \
  "ds_read_b128 v[200:203], v211 offset:0\n\t" \
  "ds_read_b128 v[204:207], v211 offset:16384\n\t" \
  "s_branch .Lfb01_%=\n\t" \
  ".Lp1_%=:\n\t" \
  "s_cmp_le_u32 s86, 5\n\t" \
  "s_cbranch_scc1 .Lf01_%=\n\t" \
  ".Lfb01_%=:\n\t" \
  "s_waitcnt lgkmcnt(6)\n\t" \
  "v_mfma_f32_16x16x32_bf16 v[128:131], v[0:3], v[176:179], 0\n\t" \
  "v_mfma_f32_16x16x32_bf16 v[132:135], v[4:7], v[176:179], 0\n\t" \
  "ds_read_b128 v[176:179], v208 offset:256\n\t" \
  "v_mfma_f32_16x16x32_bf16 v[136:139], v[0:3], v[180:183], 0\n\t" \
  "v_mfma_f32_16x16x32_bf16 v[140:143], v[4:7], v[180:183], 0\n\t" \
  "ds_read_b128 v[180:183], v208 offset:16640\n\t" \
  "s_waitcnt lgkmcnt(6)\n\t" \
  "v_mfma_f32_16x16x32_bf16 v[128:131], v[8:11], v[184:187], v[128:131]\n\t" \
  "v_mfma_f32_16x16x32_bf16 v[132:135], v[12:15], v[184:187], v[132:135]\n\t" \
  "ds_read_b128 v[184:187], v209 offset:256\n\t" \
  "s_add_u32 m0, %[lw], 67584\n\t" \
  "v_mfma_f32_16x16x32_bf16 v[136:139], v[8:11], v[188:191], v[136:139]\n\t" \
  "v_mfma_f32_16x16x32_bf16 v[140:143], v[12:15], v[188:191], v[140:143]\n\t" \
  "ds_read_b128 v[188:191], v209 offset:16640\n\t" \
  "global_load_lds_dwordx4 v218, s[92:93] sc1\n\t" \
  "ds_write_b128 v220, v[152:155] offset:0\n\t" \
  "ds_write_b128 v220, v[156:159] offset:1024\n\t" \
  "s_waitcnt lgkmcnt(8)\n\t" \
  "v_mfma_f32_16x16x32_bf16 v[128:131], v[16:19], v[192:195], v[128:131]\n\t" \
  "v_mfma_f32_16x16x32_bf16 v[132:135], v[20:23], v[192:195], v[132:135]\n\t" \
  "ds_read_b128 v[192:195], v210 offset:256\n\t" \
  "v_mfma_f32_16x16x32_bf16 v[136:139], v[16:19], v[196:199], v[136:139]\n\t" \
  "v_mfma_f32_16x16x32_bf16 v[140:143], v[20:23], v[196:199], v[140:143]\n\t" \
  "ds_read_b128 v[196:199], v210 offset:16640\n\t" \
  "s_lshr_b32 s90, s87, 7\n\t" \
  "s_xor_b32 s90, s90, %[s0]\n\t" \
  "s_and_b32 s90, s90, 1\n\t" \
  "s_lshl1_add_u32 s90, s87, s90\n\t" \
  "s_lshl_b32 s90, s90, 13\n\t" \
  "s_cmp_lt_i32 s86, 6\n\t" \
  "s_cselect_b32 s90, 0x70000000, s90\n\t" \
  "s_waitcnt lgkmcnt(8)\n\t" \
  "v_mfma_f32_16x16x32_bf16 v[128:131], v[24:27], v[200:203], v[128:131]\n\t" \
  "v_mfma_f32_16x16x32_bf16 v[132:135], v[28:31], v[200:203], v[132:135]\n\t" \
  "ds_read_b128 v[200:203], v211 offset:256\n\t" \
  "s_add_u32 m0, %[lw], 68608\n\t" \
  "v_mfma_f32_16x16x32_bf16 v[136:139], v[24:27], v[204:207], v[136:139]\n\t" \
  "v_mfma_f32_16x16x32_bf16 v[140:143], v[28:31], v[204:207], v[140:143]\n\t" \
  "ds_read_b128 v[204:207], v211 offset:16640\n\t" \
  "global_load_lds_dwordx4 v219, s[92:93] sc1\n\t" \
  "s_waitcnt lgkmcnt(4)\n\t" \
  "s_cmp_lt_u32 s86, 6\n\t" \
  "s_cbranch_scc1 .Lw01_%=\n\t" \
  "s_waitcnt vmcnt(4)\n\t" \
  "s_barrier\n\t" \
  ".Lg01_%=:\n\t" \
  "s_add_u32 s92, s92, 512\n\t" \
  "s_addc_u32 s93, s93, 0\n\t" \
  "v_mfma_f32_16x16x32_bf16 v[128:131], v[32:35], v[176:179], v[128:131]\n\t" \
  "v_mfma_f32_16x16x32_bf16 v[132:135], v[36:39], v[176:179], v[132:135]\n\t" \
  "ds_read_b128 v[176:179], v208 offset:32768\n\t" \
  "v_mfma_f32_16x16x32_bf16 v[136:139], v[32:35], v[180:183], v[136:139]\n\t" \
  "v_mfma_f32_16x16x32_bf16 v[140:143], v[36:39], v[180:183], v[140:143]\n\t" \
  "ds_read_b128 v[180:183], v208 offset:49152\n\t" \
  "ds_read_b128 v[160:163], v221 offset:0\n\t" \
  "ds_read_b128 v[164:167], v221 offset:1024\n\t" \
  "s_sub_i32 s91, s86, 2\n\t" \
  "s_max_i32 s88, s88, s91\n\t" \
  "v_mov_b32 v173, s88\n\t" \
  "buffer_store_dword v173, v174, %[rs], 0 offen sc1\n\t" \
  "v_mfma_f32_16x16x32_bf16 v[128:131], v[40:43], v[184:187], v[128:131]\n\t" \
  "v_mfma_f32_16x16x32_bf16 v[132:135], v[44:47], v[184:187], v[132:135]\n\t" \
  "ds_read_b128 v[184:187], v209 offset:32768\n\t" \
  "s_add_u32 m0, %[lw], 98304\n\t" \
  "v_mfma_f32_16x16x32_bf16 v[136:139], v[40:43], v[188:191], v[136:139]\n\t" \
  "v_mfma_f32_16x16x32_bf16 v[140:143], v[44:47], v[188:191], v[140:143]\n\t" \
  "ds_read_b128 v[188:191], v209 offset:49152\n\t" \
  "global_load_lds_dwordx4 v216, s[92:93] sc1\n\t" \
  "s_waitcnt lgkmcnt(8)\n\t" \
  "v_mfma_f32_16x16x32_bf16 v[128:131], v[48:51], v[192:195], v[128:131]\n\t" \
  "v_mfma_f32_16x16x32_bf16 v[132:135], v[52:55], v[192:195], v[132:135]\n\t" \
  "ds_read_b128 v[192:195], v210 offset:32768\n\t" \
  "v_mfma_f32_16x16x32_bf16 v[136:139], v[48:51], v[196:199], v[136:139]\n\t" \
  "v_mfma_f32_16x16x32_bf16 v[140:143], v[52:55], v[196:199], v[140:143]\n\t" \
  "ds_read_b128 v[196:199], v210 offset:49152\n\t" \
  "s_waitcnt lgkmcnt(4)\n\t" \
  "v_add_f32 v160, v160, v144\n\t" \
  "v_add_f32 v161, v161, v145\n\t" \
  "v_add_f32 v162, v162, v146\n\t" \
  "v_add_f32 v163, v163, v147\n\t" \
  "v_add_f32 v164, v164, v148\n\t" \
  "v_add_f32 v165, v165, v149\n\t" \
  "v_add_f32 v166, v166, v150\n\t" \
  "v_add_f32 v167, v167, v151\n\t" \
  "v_mfma_f32_16x16x32_bf16 v[128:131], v[56:59], v[200:203], v[128:131]\n\t" \
  "v_mfma_f32_16x16x32_bf16 v[132:135], v[60:63], v[200:203], v[132:135]\n\t" \
  "ds_read_b128 v[200:203], v211 offset:32768\n\t" \
  "s_add_u32 m0, %[lw], 99328\n\t" \
  "v_mfma_f32_16x16x32_bf16 v[136:139], v[56:59], v[204:207], v[136:139]\n\t" \
  "v_mfma_f32_16x16x32_bf16 v[140:143], v[60:63], v[204:207], v[140:143]\n\t" \
  "ds_read_b128 v[204:207], v211 offset:49152\n\t" \
  "global_load_lds_dwordx4 v217, s[92:93] sc1\n\t" \
  "v_cvt_pk_bf16_f32 v168, v160, v161\n\t" \
  "v_cvt_pk_bf16_f32 v169, v162, v163\n\t" \
  "v_cvt_pk_bf16_f32 v170, v164, v165\n\t" \
  "v_cvt_pk_bf16_f32 v171, v166, v167\n\t" \
  "buffer_store_dwordx4 v[168:171], v172, %[rt], s90 offen sc1\n\t" \
  "v_mfma_f32_16x16x32_bf16 v[128:131], v[64:67], v[176:179], v[128:131]\n\t" \
  "v_mfma_f32_16x16x32_bf16 v[132:135], v[68:71], v[176:179], v[132:135]\n\t" \
  "ds_read_b128 v[176:179], v208 offset:33024\n\t" \
  "v_mfma_f32_16x16x32_bf16 v[136:139], v[64:67], v[180:183], v[136:139]\n\t" \
  "v_mfma_f32_16x16x32_bf16 v[140:143], v[68:71], v[180:183], v[140:143]\n\t" \
  "ds_read_b128 v[180:183], v208 offset:49408\n\t" \
  "s_waitcnt lgkmcnt(6)\n\t" \
  "v_mfma_f32_16x16x32_bf16 v[128:131], v[72:75], v[184:187], v[128:131]\n\t" \
  "v_mfma_f32_16x16x32_bf16 v[132:135], v[76:79], v[184:187], v[132:135]\n\t" \
  "ds_read_b128 v[184:187], v209 offset:33024\n\t" \
  "s_add_u32 m0, %[lw], 100352\n\t" \
  "v_mfma_f32_16x16x32_bf16 v[136:139], v[72:75], v[188:191], v[136:139]\n\t" \
  "v_mfma_f32_16x16x32_bf16 v[140:143], v[76:79], v[188:191], v[140:143]\n\t" \
  "ds_read_b128 v[188:191], v209 offset:49408\n\t" \
  "global_load_lds_dwordx4 v218, s[92:93] sc1\n\t" \
  "s_waitcnt lgkmcnt(6)\n\t" \
  "v_mfma_f32_16x16x32_bf16 v[128:131], v[80:83], v[192:195], v[128:131]\n\t" \
  "v_mfma_f32_16x16x32_bf16 v[132:135], v[84:87], v[192:195], v[132:135]\n\t" \
  "ds_read_b128 v[192:195], v210 offset:33024\n\t" \
  "v_mfma_f32_16x16x32_bf16 v[136:139], v[80:83], v[196:199], v[136:139]\n\t" \
  "v_mfma_f32_16x16x32_bf16 v[140:143], v[84:87], v[196:199], v[140:143]\n\t" \
  "ds_read_b128 v[196:199], v210 offset:49408\n\t" \
  "s_waitcnt lgkmcnt(6)\n\t" \
  "v_mfma_f32_16x16x32_bf16 v[128:131], v[88:91], v[200:203], v[128:131]\n\t" \
  "v_mfma_f32_16x16x32_bf16 v[132:135], v[92:95], v[200:203], v[132:135]\n\t" \
  "ds_read_b128 v[200:203], v211 offset:33024\n\t" \
  "s_add_u32 m0, %[lw], 101376\n\t" \
  "v_mfma_f32_16x16x32_bf16 v[136:139], v[88:91], v[204:207], v[136:139]\n\t" \
  "v_mfma_f32_16x16x32_bf16 v[140:143], v[92:95], v[204:207], v[140:143]\n\t" \
  "ds_read_b128 v[204:207], v211 offset:49408\n\t" \
  "global_load_lds_dwordx4 v219, s[92:93] sc1\n\t" \
  "s_cmp_lt_u32 s86, 8\n\t" \
  "s_cbranch_scc1 .Lw11_%=\n\t" \
  "s_waitcnt vmcnt(6)\n\t" \
  "s_barrier\n\t" \
  ".Lg11_%=:\n\t" \
  "s_add_u32 s92, s92, 65024\n\t" \
  "s_addc_u32 s93, s93, 0\n\t" \
  "s_waitcnt lgkmcnt(6)\n\t" \
  "v_mfma_f32_16x16x32_bf16 v[128:131], v[96:99], v[176:179], v[128:131]\n\t" \
  "v_mfma_f32_16x16x32_bf16 v[132:135], v[100:103], v[176:179], v[132:135]\n\t" \
  "ds_read_b128 v[176:179], v212 offset:0\n\t" \
  "v_mfma_f32_16x16x32_bf16 v[136:139], v[96:99], v[180:183], v[136:139]\n\t" \
  "v_mfma_f32_16x16x32_bf16 v[140:143], v[100:103], v[180:183], v[140:143]\n\t" \
  "ds_read_b128 v[180:183], v212 offset:16384\n\t" \
  "s_waitcnt lgkmcnt(6)\n\t" \
  "v_mfma_f32_16x16x32_bf16 v[128:131], v[104:107], v[184:187], v[128:131]\n\t" \
  "v_mfma_f32_16x16x32_bf16 v[132:135], v[108:111], v[184:187], v[132:135]\n\t" \
  "ds_read_b128 v[184:187], v213 offset:0\n\t" \
  "s_add_u32 m0, %[lw], 0\n\t" \
  "v_mfma_f32_16x16x32_bf16 v[136:139], v[104:107], v[188:191], v[136:139]\n\t" \
  "v_mfma_f32_16x16x32_bf16 v[140:143], v[108:111], v[188:191], v[140:143]\n\t" \
  "ds_read_b128 v[188:191], v213 offset:16384\n\t" \
  "global_load_lds_dwordx4 v216, s[92:93] sc1\n\t" \
  "s_waitcnt lgkmcnt(6)\n\t" \
  "v_mfma_f32_16x16x32_bf16 v[128:131], v[112:115], v[192:195], v[128:131]\n\t" \
  "v_mfma_f32_16x16x32_bf16 v[132:135], v[116:119], v[192:195], v[132:135]\n\t" \
  "ds_read_b128 v[192:195], v214 offset:0\n\t" \
  "v_mfma_f32_16x16x32_bf16 v[136:139], v[112:115], v[196:199], v[136:139]\n\t" \
  "v_mfma_f32_16x16x32_bf16 v[140:143], v[116:119], v[196:199], v[140:143]\n\t" \
  "ds_read_b128 v[196:199], v214 offset:16384\n\t" \
  "s_waitcnt lgkmcnt(6)\n\t" \
  "v_mfma_f32_16x16x32_bf16 v[128:131], v[120:123], v[200:203], v[128:131]\n\t" \
  "v_mfma_f32_16x16x32_bf16 v[132:135], v[124:127], v[200:203], v[132:135]\n\t" \
  "ds_read_b128 v[200:203], v215 offset:0\n\t" \
  "s_add_u32 m0, %[lw], 1024\n\t" \
  "v_mfma_f32_16x16x32_bf16 v[136:139], v[120:123], v[204:207], v[136:139]\n\t" \
  "v_mfma_f32_16x16x32_bf16 v[140:143], v[124:127], v[204:207], v[140:143]\n\t" \
  "ds_read_b128 v[204:207], v215 offset:16384\n\t" \
  "global_load_lds_dwordx4 v217, s[92:93] sc1\n\t" \
  "s_add_u32 s86, s86, 1\n\t" \
  "s_add_u32 s87, s87, 1\n\t" \
  "s_cmp_le_u32 s86, 5\n\t" \
  "s_cbranch_scc1 .Lf11_%=\n\t" \
  ".Lfb11_%=:\n\t" \
  "s_waitcnt lgkmcnt(6)\n\t" \
  "v_mfma_f32_16x16x32_bf16 v[144:147], v[0:3], v[176:179], 0\n\t" \
  "v_mfma_f32_16x16x32_bf16 v[148:151], v[4:7], v[176:179], 0\n\t" \
  "ds_read_b128 v[176:179], v212 offset:256\n\t" \
  "v_mfma_f32_16x16x32_bf16 v[152:155], v[0:3], v[180:183], 0\n\t" \
  "v_mfma_f32_16x16x32_bf16 v[156:159], v[4:7], v[180:183], 0\n\t" \
  "ds_read_b128 v[180:183], v212 offset:16640\n\t" \
  "s_waitcnt lgkmcnt(6)\n\t" \
  "v_mfma_f32_16x16x32_bf16 v[144:147], v[8:11], v[184:187], v[144:147]\n\t" \
  "v_mfma_f32_16x16x32_bf16 v[148:151], v[12:15], v[184:187], v[148:151]\n\t" \
  "ds_read_b128 v[184:187], v213 offset:256\n\t" \
  "s_add_u32 m0, %[lw], 2048\n\t" \
  "v_mfma_f32_16x16x32_bf16 v[152:155], v[8:11], v[188:191], v[152:155]\n\t" \
  "v_mfma_f32_16x16x32_bf16 v[156:159], v[12:15], v[188:191], v[156:159]\n\t" \
  "ds_read_b128 v[188:191], v213 offset:16640\n\t" \
  "global_load_lds_dwordx4 v218, s[92:93] sc1\n\t" \
  "ds_write_b128 v220, v[136:139] offset:0\n\t" \
  "ds_write_b128 v220, v[140:143] offset:1024\n\t" \
  "s_waitcnt lgkmcnt(8)\n\t" \
  "v_mfma_f32_16x16x32_bf16 v[144:147], v[16:19], v[192:195], v[144:147]\n\t" \
  "v_mfma_f32_16x16x32_bf16 v[148:151], v[20:23], v[192:195], v[148:151]\n\t" \
  "ds_read_b128 v[192:195], v214 offset:256\n\t" \
  "v_mfma_f32_16x16x32_bf16 v[152:155], v[16:19], v[196:199], v[152:155]\n\t" \
  "v_mfma_f32_16x16x32_bf16 v[156:159], v[20:23], v[196:199], v[156:159]\n\t" \
  "ds_read_b128 v[196:199], v214 offset:16640\n\t" \
  "s_lshr_b32 s90, s87, 7\n\t" \
  "s_xor_b32 s90, s90, %[s0]\n\t" \
  "s_and_b32 s90, s90, 1\n\t" \
  "s_lshl1_add_u32 s90, s87, s90\n\t" \
  "s_lshl_b32 s90, s90, 13\n\t" \
  "s_cmp_lt_i32 s86, 6\n\t" \
  "s_cselect_b32 s90, 0x70000000, s90\n\t" \
  "s_waitcnt lgkmcnt(8)\n\t" \
  "v_mfma_f32_16x16x32_bf16 v[144:147], v[24:27], v[200:203], v[144:147]\n\t" \
  "v_mfma_f32_16x16x32_bf16 v[148:151], v[28:31], v[200:203], v[148:151]\n\t" \
  "ds_read_b128 v[200:203], v215 offset:256\n\t" \
  "s_add_u32 m0, %[lw], 3072\n\t" \
  "v_mfma_f32_16x16x32_bf16 v[152:155], v[24:27], v[204:207], v[152:155]\n\t" \
  "v_mfma_f32_16x16x32_bf16 v[156:159], v[28:31], v[204:207], v[156:159]\n\t" \
  "ds_read_b128 v[204:207], v215 offset:16640\n\t" \
  "global_load_lds_dwordx4 v219, s[92:93] sc1\n\t" \
  "s_waitcnt lgkmcnt(4)\n\t" \
  "s_cmp_lt_u32 s86, 6\n\t" \
  "s_cbranch_scc1 .Lw21_%=\n\t" \
  "s_waitcnt vmcnt(4)\n\t" \
  "s_barrier\n\t" \
  ".Lg21_%=:\n\t" \
  "s_add_u32 s92, s92, 512\n\t" \
  "s_addc_u32 s93, s93, 0\n\t" \
  "v_mfma_f32_16x16x32_bf16 v[144:147], v[32:35], v[176:179], v[144:147]\n\t" \
  "v_mfma_f32_16x16x32_bf16 v[148:151], v[36:39], v[176:179], v[148:151]\n\t" \
  "ds_read_b128 v[176:179], v212 offset:32768\n\t" \
  "v_mfma_f32_16x16x32_bf16 v[152:155], v[32:35], v[180:183], v[152:155]\n\t" \
  "v_mfma_f32_16x16x32_bf16 v[156:159], v[36:39], v[180:183], v[156:159]\n\t" \
  "ds_read_b128 v[180:183], v212 offset:49152\n\t" \
  "ds_read_b128 v[160:163], v221 offset:0\n\t" \
  "ds_read_b128 v[164:167], v221 offset:1024\n\t" \
  "s_sub_i32 s91, s86, 2\n\t" \
  "s_max_i32 s88, s88, s91\n\t" \
  "v_mov_b32 v173, s88\n\t" \
  "buffer_store_dword v173, v174, %[rs], 0 offen sc1\n\t" \
  "v_mfma_f32_16x16x32_bf16 v[144:147], v[40:43], v[184:187], v[144:147]\n\t" \
  "v_mfma_f32_16x16x32_bf16 v[148:151], v[44:47], v[184:187], v[148:151]\n\t" \
  "ds_read_b128 v[184:187], v213 offset:32768\n\t" \
  "s_add_u32 m0, %[lw], 32768\n\t" \
  "v_mfma_f32_16x16x32_bf16 v[152:155], v[40:43], v[188:191], v[152:155]\n\t" \
  "v_mfma_f32_16x16x32_bf16 v[156:159], v[44:47], v[188:191], v[156:159]\n\t" \
  "ds_read_b128 v[188:191], v213 offset:49152\n\t" \
  "global_load_lds_dwordx4 v216, s[92:93] sc1\n\t" \
  "s_waitcnt lgkmcnt(8)\n\t" \
  "v_mfma_f32_16x16x32_bf16 v[144:147], v[48:51], v[192:195], v[144:147]\n\t" \
  "v_mfma_f32_16x16x32_bf16 v[148:151], v[52:55], v[192:195], v[148:151]\n\t" \
  "ds_read_b128 v[192:195], v214 offset:32768\n\t" \
  "v_mfma_f32_16x16x32_bf16 v[152:155], v[48:51], v[196:199], v[152:155]\n\t" \
  "v_mfma_f32_16x16x32_bf16 v[156:159], v[52:55], v[196:199], v[156:159]\n\t" \
  "ds_read_b128 v[196:199], v214 offset:49152\n\t" \
  "s_waitcnt lgkmcnt(4)\n\t" \
  "v_add_f32 v160, v160, v128\n\t" \
  "v_add_f32 v161, v161, v129\n\t" \
  "v_add_f32 v162, v162, v130\n\t" \
  "v_add_f32 v163, v163, v131\n\t" \
  "v_add_f32 v164, v164, v132\n\t" \
  "v_add_f32 v165, v165, v133\n\t" \
  "v_add_f32 v166, v166, v134\n\t" \
  "v_add_f32 v167, v167, v135\n\t" \
  "v_mfma_f32_16x16x32_bf16 v[144:147], v[56:59], v[200:203], v[144:147]\n\t" \
  "v_mfma_f32_16x16x32_bf16 v[148:151], v[60:63], v[200:203], v[148:151]\n\t" \
  "ds_read_b128 v[200:203], v215 offset:32768\n\t" \
  "s_add_u32 m0, %[lw], 33792\n\t" \
  "v_mfma_f32_16x16x32_bf16 v[152:155], v[56:59], v[204:207], v[152:155]\n\t" \
  "v_mfma_f32_16x16x32_bf16 v[156:159], v[60:63], v[204:207], v[156:159]\n\t" \
  "ds_read_b128 v[204:207], v215 offset:49152\n\t" \
  "global_load_lds_dwordx4 v217, s[92:93] sc1\n\t" \
  "v_cvt_pk_bf16_f32 v168, v160, v161\n\t" \
  "v_cvt_pk_bf16_f32 v169, v162, v163\n\t" \
  "v_cvt_pk_bf16_f32 v170, v164, v165\n\t" \
  "v_cvt_pk_bf16_f32 v171, v166, v167\n\t" \
  "buffer_store_dwordx4 v[168:171], v172, %[rt], s90 offen sc1\n\t" \
  "v_mfma_f32_16x16x32_bf16 v[144:147], v[64:67], v[176:179], v[144:147]\n\t" \
  "v_mfma_f32_16x16x32_bf16 v[148:151], v[68:71], v[176:179], v[148:151]\n\t" \
  "ds_read_b128 v[176:179], v212 offset:33024\n\t" \
  "v_mfma_f32_16x16x32_bf16 v[152:155], v[64:67], v[180:183], v[152:155]\n\t" \
  "v_mfma_f32_16x16x32_bf16 v[156:159], v[68:71], v[180:183], v[156:159]\n\t" \
  "ds_read_b128 v[180:183], v212 offset:49408\n\t" \
  "s_waitcnt lgkmcnt(6)\n\t" \
  "v_mfma_f32_16x16x32_bf16 v[144:147], v[72:75], v[184:187], v[144:147]\n\t" \
  "v_mfma_f32_16x16x32_bf16 v[148:151], v[76:79], v[184:187], v[148:151]\n\t" \
  "ds_read_b128 v[184:187], v213 offset:33024\n\t" \
  "s_add_u32 m0, %[lw], 34816\n\t" \
  "v_mfma_f32_16x16x32_bf16 v[152:155], v[72:75], v[188:191], v[152:155]\n\t" \
  "v_mfma_f32_16x16x32_bf16 v[156:159], v[76:79], v[188:191], v[156:159]\n\t" \
  "ds_read_b128 v[188:191], v213 offset:49408\n\t" \
  "global_load_lds_dwordx4 v218, s[92:93] sc1\n\t" \
  "s_waitcnt lgkmcnt(6)\n\t" \
  "v_mfma_f32_16x16x32_bf16 v[144:147], v[80:83], v[192:195], v[144:147]\n\t" \
  "v_mfma_f32_16x16x32_bf16 v[148:151], v[84:87], v[192:195], v[148:151]\n\t" \
  "ds_read_b128 v[192:195], v214 offset:33024\n\t" \
  "v_mfma_f32_16x16x32_bf16 v[152:155], v[80:83], v[196:199], v[152:155]\n\t" \
  "v_mfma_f32_16x16x32_bf16 v[156:159], v[84:87], v[196:199], v[156:159]\n\t" \
  "ds_read_b128 v[196:199], v214 offset:49408\n\t" \
  "s_waitcnt lgkmcnt(6)\n\t" \
  "v_mfma_f32_16x16x32_bf16 v[144:147], v[88:91], v[200:203], v[144:147]\n\t" \
  "v_mfma_f32_16x16x32_bf16 v[148:151], v[92:95], v[200:203], v[148:151]\n\t" \
  "ds_read_b128 v[200:203], v215 offset:33024\n\t" \
  "s_add_u32 m0, %[lw], 35840\n\t" \
  "v_mfma_f32_16x16x32_bf16 v[152:155], v[88:91], v[204:207], v[152:155]\n\t" \
  "v_mfma_f32_16x16x32_bf16 v[156:159], v[92:95], v[204:207], v[156:159]\n\t" \
  "ds_read_b128 v[204:207], v215 offset:49408\n\t" \
  "global_load_lds_dwordx4 v219, s[92:93] sc1\n\t" \
  "s_cmp_lt_u32 s86, 8\n\t" \
  "s_cbranch_scc1 .Lw31_%=\n\t" \
  "s_waitcnt vmcnt(6)\n\t" \
  "s_barrier\n\t" \
  ".Lg31_%=:\n\t" \
  "s_add_u32 s92, s92, 65024\n\t" \
  "s_addc_u32 s93, s93, 0\n\t" \
  "s_waitcnt lgkmcnt(6)\n\t" \
  "v_mfma_f32_16x16x32_bf16 v[144:147], v[96:99], v[176:179], v[144:147]\n\t" \
  "v_mfma_f32_16x16x32_bf16 v[148:151], v[100:103], v[176:179], v[148:151]\n\t" \
  "ds_read_b128 v[176:179], v208 offset:0\n\t" \
  "v_mfma_f32_16x16x32_bf16 v[152:155], v[96:99], v[180:183], v[152:155]\n\t" \
  "v_mfma_f32_16x16x32_bf16 v[156:159], v[100:103], v[180:183], v[156:159]\n\t" \
  "ds_read_b128 v[180:183], v208 offset:16384\n\t" \
  "s_waitcnt lgkmcnt(6)\n\t" \
  "v_mfma_f32_16x16x32_bf16 v[144:147], v[104:107], v[184:187], v[144:147]\n\t" \
  "v_mfma_f32_16x16x32_bf16 v[148:151], v[108:111], v[184:187], v[148:151]\n\t" \
  "ds_read_b128 v[184:187], v209 offset:0\n\t" \
  "s_add_u32 m0, %[lw], 65536\n\t" \
  "v_mfma_f32_16x16x32_bf16 v[152:155], v[104:107], v[188:191], v[152:155]\n\t" \
  "v_mfma_f32_16x16x32_bf16 v[156:159], v[108:111], v[188:191], v[156:159]\n\t" \
  "ds_read_b128 v[188:191], v209 offset:16384\n\t" \
  "global_load_lds_dwordx4 v216, s[92:93] sc1\n\t" \
  "s_waitcnt lgkmcnt(6)\n\t" \
  "v_mfma_f32_16x16x32_bf16 v[144:147], v[112:115], v[192:195], v[144:147]\n\t" \
  "v_mfma_f32_16x16x32_bf16 v[148:151], v[116:119], v[192:195], v[148:151]\n\t" \
  "ds_read_b128 v[192:195], v210 offset:0\n\t" \
  "v_mfma_f32_16x16x32_bf16 v[152:155], v[112:115], v[196:199], v[152:155]\n\t" \
  "v_mfma_f32_16x16x32_bf16 v[156:159], v[116:119], v[196:199], v[156:159]\n\t" \
  "ds_read_b128 v[196:199], v210 offset:16384\n\t" \
  "s_waitcnt lgkmcnt(6)\n\t" \
  "v_mfma_f32_16x16x32_bf16 v[144:147], v[120:123], v[200:203], v[144:147]\n\t" \
  "v_mfma_f32_16x16x32_bf16 v[148:151], v[124:127], v[200:203], v[148:151]\n\t" \
  "ds_read_b128 v[200:203], v211 offset:0\n\t" \
  "s_add_u32 m0, %[lw], 66560\n\t" \
  "v_mfma_f32_16x16x32_bf16 v[152:155], v[120:123], v[204:207], v[152:155]\n\t" \
  "v_mfma_f32_16x16x32_bf16 v[156:159], v[124:127], v[204:207], v[156:159]\n\t" \
  "ds_read_b128 v[204:207], v211 offset:16384\n\t" \
  "global_load_lds_dwordx4 v217, s[92:93] sc1\n\t" \
  "s_add_u32 s86, s86, 1\n\t" \
  "s_add_u32 s87, s87, 1\n\t" \
  "s_cmp_lt_u32 s86, %[end]\n\t" \
  "s_cbranch_scc1 .Lp1_%=\n\t" \
  "s_branch .Lx1_%=\n\t" \
  ".Lf01_%=:\n\t" \
  "s_nop 7\n\t" \
  "ds_write_b128 v220, v[152:155] offset:0\n\t" \
  "ds_write_b128 v220, v[156:159] offset:1024\n\t" \
  "s_lshr_b32 s90, s87, 7\n\t" \
  "s_xor_b32 s90, s90, %[s0]\n\t" \
  "s_and_b32 s90, s90, 1\n\t" \
  "s_lshl1_add_u32 s90, s87, s90\n\t" \
  "s_lshl_b32 s90, s90, 13\n\t" \
  "s_cmp_lt_i32 s86, 1\n\t" \
  "s_cselect_b32 s90, 0x70000000, s90\n\t" \
  "s_waitcnt lgkmcnt(0)\n\t" \
  "s_barrier\n\t" \
  "ds_read_b128 v[160:163], v221 offset:0\n\t" \
  "ds_read_b128 v[164:167], v221 offset:1024\n\t" \
  "s_waitcnt lgkmcnt(0)\n\t" \
  "v_add_f32 v160, v160, v144\n\t" \
  "v_add_f32 v161, v161, v145\n\t" \
  "v_add_f32 v162, v162, v146\n\t" \
  "v_add_f32 v163, v163, v147\n\t" \
  "v_add_f32 v164, v164, v148\n\t" \
  "v_add_f32 v165, v165, v149\n\t" \
  "v_add_f32 v166, v166, v150\n\t" \
  "v_add_f32 v167, v167, v151\n\t" \
  "v_cvt_pk_bf16_f32 v168, v160, v161\n\t" \
  "v_cvt_pk_bf16_f32 v169, v162, v163\n\t" \
  "v_cvt_pk_bf16_f32 v170, v164, v165\n\t" \
  "v_cvt_pk_bf16_f32 v171, v166, v167\n\t" \
  "buffer_store_dwordx4 v[168:171], v172, %[rt], s90 offen sc1\n\t" \
  "s_branch .Lfb01_%=\n\t" \
  ".Lf11_%=:\n\t" \
  "s_nop 7\n\t" \
  "ds_write_b128 v220, v[136:139] offset:0\n\t" \
  "ds_write_b128 v220, v[140:143] offset:1024\n\t" \
  "s_lshr_b32 s90, s87, 7\n\t" \
  "s_xor_b32 s90, s90, %[s0]\n\t" \
  "s_and_b32 s90, s90, 1\n\t" \
  "s_lshl1_add_u32 s90, s87, s90\n\t" \
  "s_lshl_b32 s90, s90, 13\n\t" \
  "s_cmp_lt_i32 s86, 1\n\t" \
  "s_cselect_b32 s90, 0x70000000, s90\n\t" \
  "s_waitcnt lgkmcnt(0)\n\t" \
  "s_barrier\n\t" \
  "ds_read_b128 v[160:163], v221 offset:0\n\t" \
  "ds_read_b128 v[164:167], v221 offset:1024\n\t" \
  "s_waitcnt lgkmcnt(0)\n\t" \
  "v_add_f32 v160, v160, v128\n\t" \
  "v_add_f32 v161, v161, v129\n\t" \
  "v_add_f32 v162, v162, v130\n\t" \
  "v_add_f32 v163, v163, v131\n\t" \
  "v_add_f32 v164, v164, v132\n\t" \
  "v_add_f32 v165, v165, v133\n\t" \
  "v_add_f32 v166, v166, v134\n\t" \
  "v_add_f32 v167, v167, v135\n\t" \
  "v_cvt_pk_bf16_f32 v168, v160, v161\n\t" \
  "v_cvt_pk_bf16_f32 v169, v162, v163\n\t" \
  "v_cvt_pk_bf16_f32 v170, v164, v165\n\t" \
  "v_cvt_pk_bf16_f32 v171, v166, v167\n\t" \
  "buffer_store_dwordx4 v[168:171], v172, %[rt], s90 offen sc1\n\t" \
  "s_branch .Lfb11_%=\n\t" \
  ".Lw01_%=:\n\t" \
  "s_waitcnt vmcnt(2)\n\t" \
  "s_barrier\n\t" \
  "s_sub_i32 s91, s86, 1\n\t" \
  "s_max_i32 s88, s88, s91\n\t" \
  "v_mov_b32 v173, s88\n\t" \
  "buffer_store_dword v173, v174, %[rs], 0 offen sc1\n\t" \
  "s_branch .Lg01_%=\n\t" \
  ".Lw11_%=:\n\t" \
  "s_waitcnt vmcnt(2)\n\t" \
  "s_barrier\n\t" \
  "s_sub_i32 s91, s86, 1\n\t" \
  "s_max_i32 s88, s88, s91\n\t" \
  "v_mov_b32 v173, s88\n\t" \
  "buffer_store_dword v173, v174, %[rs], 0 offen sc1\n\t" \
  "s_branch .Lg11_%=\n\t" \
  ".Lw21_%=:\n\t" \
  "s_waitcnt vmcnt(2)\n\t" \
  "s_barrier\n\t" \
  "s_sub_i32 s91, s86, 1\n\t" \
  "s_max_i32 s88, s88, s91\n\t" \
  "v_mov_b32 v173, s88\n\t" \
  "buffer_store_dword v173, v174, %[rs], 0 offen sc1\n\t" \
  "s_branch .Lg21_%=\n\t" \
  ".Lw31_%=:\n\t" \
  "s_waitcnt vmcnt(2)\n\t" \
  "s_barrier\n\t" \
  "s_sub_i32 s91, s86, 1\n\t" \
  "s_max_i32 s88, s88, s91\n\t" \
  "v_mov_b32 v173, s88\n\t" \
  "buffer_store_dword v173, v174, %[rs], 0 offen sc1\n\t" \
  "s_branch .Lg31_%=\n\t" \
  ".Lx1_%=:\n\t" \
  "s_waitcnt lgkmcnt(0)\n\t" \
  "s_setprio 0\n\t" \
  "s_mov_b32 m0, s94\n\t" \
  ".Le_%=:\n\t"
#define PR_ASM_1 \
  "s_cmp_lg_u32 %[kh], 0\n\t" \
  "s_cbranch_scc1 .Lk_%=\n\t" \
  "s_mov_b32 s94, m0\n\t" \
  "s_mov_b64 s[92:93], %[x0]\n\t" \
  "s_mov_b32 s86, %[g0]\n\t" \
  "s_add_u32 s87, s86, %[t0]\n\t" \
  "s_sub_i32 s88, s86, 3\n\t" \
  "ds_read_b128 v[176:179], v208 offset:0\n\t" \
  "ds_read_b128 v[180:183], v208 offset:16384\n\t" \
  "ds_read_b128 v[184:187], v209 offset:0\n\t" \
  "ds_read_b128 v[188:191], v209 offset:16384\n\t" \
  "ds_read_b128 v[192:195], v210 offset:0\n\t" \
  "ds_read_b128 v[196:199], v210 offset:16384\n\t" \
  "ds_read_b128 v[200:203], v211 offset:0\n\t" \
  "ds_read_b128 v[204:207], v211 offset:16384\n\t" \
  "s_branch .Lfb00_%=\n\t" \
  ".Lp0_%=:\n\t" \
  "s_cmp_le_u32 s86, 5\n\t" \
  "s_cbranch_scc1 .Lf00_%=\n\t" \
  ".Lfb00_%=:\n\t" \
  "s_waitcnt lgkmcnt(6)\n\t" \
  "v_mfma_f32_16x16x32_bf16 v[128:131], v[176:179], v[0:3], 0\n\t" \
  "v_mfma_f32_16x16x32_bf16 v[136:139], v[176:179], v[4:7], 0\n\t" \
  "ds_read_b128 v[176:179], v208 offset:256\n\t" \
  "s_add_u32 m0, %[lw], 67584\n\t" \
  "v_mfma_f32_16x16x32_bf16 v[132:135], v[180:183], v[0:3], 0\n\t" \
  "v_mfma_f32_16x16x32_bf16 v[140:143], v[180:183], v[4:7], 0\n\t" \
  "ds_read_b128 v[180:183], v208 offset:16640\n\t" \
  "global_load_lds_dwordx4 v218, s[92:93] sc1\n\t" \
  "s_waitcnt lgkmcnt(6)\n\t" \
  "v_mfma_f32_16x16x32_bf16 v[128:131], v[184:187], v[8:11], v[128:131]\n\t" \
  "v_mfma_f32_16x16x32_bf16 v[136:139], v[184:187], v[12:15], v[136:139]\n\t" \
  "ds_read_b128 v[184:187], v209 offset:256\n\t" \
  "v_mfma_f32_16x16x32_bf16 v[132:135], v[188:191], v[8:11], v[132:135]\n\t" \
  "v_mfma_f32_16x16x32_bf16 v[140:143], v[188:191], v[12:15], v[140:143]\n\t" \
  "ds_read_b128 v[188:191], v209 offset:16640\n\t" \
  "ds_write_b128 v220, v[152:155] offset:0\n\t" \
  "ds_write_b128 v220, v[156:159] offset:1024\n\t" \
  "s_waitcnt lgkmcnt(8)\n\t" \
  "v_mfma_f32_16x16x32_bf16 v[128:131], v[192:195], v[16:19], v[128:131]\n\t" \
  "v_mfma_f32_16x16x32_bf16 v[136:139], v[192:195], v[20:23], v[136:139]\n\t" \
  "ds_read_b128 v[192:195], v210 offset:256\n\t" \
  "s_add_u32 m0, %[lw], 68608\n\t" \
  "v_mfma_f32_16x16x32_bf16 v[132:135], v[196:199], v[16:19], v[132:135]\n\t" \
  "v_mfma_f32_16x16x32_bf16 v[140:143], v[196:199], v[20:23], v[140:143]\n\t" \
  "ds_read_b128 v[196:199], v210 offset:16640\n\t" \
  "global_load_lds_dwordx4 v219, s[92:93] sc1\n\t" \
  "s_lshr_b32 s90, s87, 7\n\t" \
  "s_xor_b32 s90, s90, %[s0]\n\t" \
  "s_and_b32 s90, s90, 1\n\t" \
  "s_lshl1_add_u32 s90, s87, s90\n\t" \
  "s_lshl_b32 s90, s90, 13\n\t" \
  "s_cmp_lt_i32 s86, 6\n\t" \
  "s_cselect_b32 s90, 0x70000000, s90\n\t" \
  "s_waitcnt lgkmcnt(8)\n\t" \
  "v_mfma_f32_16x16x32_bf16 v[128:131], v[200:203], v[24:27], v[128:131]\n\t" \
  "v_mfma_f32_16x16x32_bf16 v[136:139], v[200:203], v[28:31], v[136:139]\n\t" \
  "ds_read_b128 v[200:203], v211 offset:256\n\t" \
  "v_mfma_f32_16x16x32_bf16 v[132:135], v[204:207], v[24:27], v[132:135]\n\t" \
  "v_mfma_f32_16x16x32_bf16 v[140:143], v[204:207], v[28:31], v[140:143]\n\t" \
  "ds_read_b128 v[204:207], v211 offset:16640\n\t" \
  "s_waitcnt lgkmcnt(4)\n\t" \
  "s_cmp_lt_u32 s86, 6\n\t" \
  "s_cbranch_scc1 .Lw00_%=\n\t" \
  "s_waitcnt vmcnt(4)\n\t" \
  "s_barrier\n\t" \
  ".Lg00_%=:\n\t" \
  "s_add_u32 s92, s92, 512\n\t" \
  "s_addc_u32 s93, s93, 0\n\t" \
  "v_mfma_f32_16x16x32_bf16 v[128:131], v[176:179], v[32:35], v[128:131]\n\t" \
  "v_mfma_f32_16x16x32_bf16 v[136:139], v[176:179], v[36:39], v[136:139]\n\t" \
  "ds_read_b128 v[176:179], v208 offset:32768\n\t" \
  "s_add_u32 m0, %[lw], 98304\n\t" \
  "v_mfma_f32_16x16x32_bf16 v[132:135], v[180:183], v[32:35], v[132:135]\n\t" \
  "v_mfma_f32_16x16x32_bf16 v[140:143], v[180:183], v[36:39], v[140:143]\n\t" \
  "ds_read_b128 v[180:183], v208 offset:49152\n\t" \
  "global_load_lds_dwordx4 v216, s[92:93] sc1\n\t" \
  "ds_read_b128 v[160:163], v221 offset:0\n\t" \
  "ds_read_b128 v[164:167], v221 offset:1024\n\t" \
  "v_mfma_f32_16x16x32_bf16 v[128:131], v[184:187], v[40:43], v[128:131]\n\t" \
  "v_mfma_f32_16x16x32_bf16 v[136:139], v[184:187], v[44:47], v[136:139]\n\t" \
  "ds_read_b128 v[184:187], v209 offset:32768\n\t" \
  "v_mfma_f32_16x16x32_bf16 v[132:135], v[188:191], v[40:43], v[132:135]\n\t" \
  "v_mfma_f32_16x16x32_bf16 v[140:143], v[188:191], v[44:47], v[140:143]\n\t" \
  "ds_read_b128 v[188:191], v209 offset:49152\n\t" \
  "s_waitcnt lgkmcnt(8)\n\t" \
  "v_mfma_f32_16x16x32_bf16 v[128:131], v[192:195], v[48:51], v[128:131]\n\t" \
  "v_mfma_f32_16x16x32_bf16 v[136:139], v[192:195], v[52:55], v[136:139]\n\t" \
  "ds_read_b128 v[192:195], v210 offset:32768\n\t" \
  "s_add_u32 m0, %[lw], 99328\n\t" \
  "v_mfma_f32_16x16x32_bf16 v[132:135], v[196:199], v[48:51], v[132:135]\n\t" \
  "v_mfma_f32_16x16x32_bf16 v[140:143], v[196:199], v[52:55], v[140:143]\n\t" \
  "ds_read_b128 v[196:199], v210 offset:49152\n\t" \
  "global_load_lds_dwordx4 v217, s[92:93] sc1\n\t" \
  "s_waitcnt lgkmcnt(4)\n\t" \
  "v_add_f32 v160, v160, v144\n\t" \
  "v_add_f32 v161, v161, v145\n\t" \
  "v_add_f32 v162, v162, v146\n\t" \
  "v_add_f32 v163, v163, v147\n\t" \
  "v_add_f32 v164, v164, v148\n\t" \
  "v_add_f32 v165, v165, v149\n\t" \
  "v_add_f32 v166, v166, v150\n\t" \
  "v_add_f32 v167, v167, v151\n\t" \
  "v_mfma_f32_16x16x32_bf16 v[128:131], v[200:203], v[56:59], v[128:131]\n\t" \
  "v_mfma_f32_16x16x32_bf16 v[136:139], v[200:203], v[60:63], v[136:139]\n\t" \
  "ds_read_b128 v[200:203], v211 offset:32768\n\t" \
  "v_mfma_f32_16x16x32_bf16 v[132:135], v[204:207], v[56:59], v[132:135]\n\t" \
  "v_mfma_f32_16x16x32_bf16 v[140:143], v[204:207], v[60:63], v[140:143]\n\t" \
  "ds_read_b128 v[204:207], v211 offset:49152\n\t" \
  "v_cvt_pk_bf16_f32 v168, v160, v161\n\t" \
  "v_cvt_pk_bf16_f32 v169, v162, v163\n\t" \
  "v_cvt_pk_bf16_f32 v170, v164, v165\n\t" \
  "v_cvt_pk_bf16_f32 v171, v166, v167\n\t" \
  "buffer_store_dwordx4 v[168:171], v172, %[rt], s90 offen sc1\n\t" \
  "v_mfma_f32_16x16x32_bf16 v[128:131], v[176:179], v[64:67], v[128:131]\n\t" \
  "v_mfma_f32_16x16x32_bf16 v[136:139], v[176:179], v[68:71], v[136:139]\n\t" \
  "ds_read_b128 v[176:179], v208 offset:33024\n\t" \
  "s_add_u32 m0, %[lw], 100352\n\t" \
  "v_mfma_f32_16x16x32_bf16 v[132:135], v[180:183], v[64:67], v[132:135]\n\t" \
  "v_mfma_f32_16x16x32_bf16 v[140:143], v[180:183], v[68:71], v[140:143]\n\t" \
  "ds_read_b128 v[180:183], v208 offset:49408\n\t" \
  "global_load_lds_dwordx4 v218, s[92:93] sc1\n\t" \
  "s_waitcnt lgkmcnt(6)\n\t" \
  "v_mfma_f32_16x16x32_bf16 v[128:131], v[184:187], v[72:75], v[128:131]\n\t" \
  "v_mfma_f32_16x16x32_bf16 v[136:139], v[184:187], v[76:79], v[136:139]\n\t" \
  "ds_read_b128 v[184:187], v209 offset:33024\n\t" \
  "v_mfma_f32_16x16x32_bf16 v[132:135], v[188:191], v[72:75], v[132:135]\n\t" \
  "v_mfma_f32_16x16x32_bf16 v[140:143], v[188:191], v[76:79], v[140:143]\n\t" \
  "ds_read_b128 v[188:191], v209 offset:49408\n\t" \
  "s_waitcnt lgkmcnt(6)\n\t" \
  "v_mfma_f32_16x16x32_bf16 v[128:131], v[192:195], v[80:83], v[128:131]\n\t" \
  "v_mfma_f32_16x16x32_bf16 v[136:139], v[192:195], v[84:87], v[136:139]\n\t" \
  "ds_read_b128 v[192:195], v210 offset:33024\n\t" \
  "s_add_u32 m0, %[lw], 101376\n\t" \
  "v_mfma_f32_16x16x32_bf16 v[132:135], v[196:199], v[80:83], v[132:135]\n\t" \
  "v_mfma_f32_16x16x32_bf16 v[140:143], v[196:199], v[84:87], v[140:143]\n\t" \
  "ds_read_b128 v[196:199], v210 offset:49408\n\t" \
  "global_load_lds_dwordx4 v219, s[92:93] sc1\n\t" \
  "s_waitcnt lgkmcnt(6)\n\t" \
  "v_mfma_f32_16x16x32_bf16 v[128:131], v[200:203], v[88:91], v[128:131]\n\t" \
  "v_mfma_f32_16x16x32_bf16 v[136:139], v[200:203], v[92:95], v[136:139]\n\t" \
  "ds_read_b128 v[200:203], v211 offset:33024\n\t" \
  "v_mfma_f32_16x16x32_bf16 v[132:135], v[204:207], v[88:91], v[132:135]\n\t" \
  "v_mfma_f32_16x16x32_bf16 v[140:143], v[204:207], v[92:95], v[140:143]\n\t" \
  "ds_read_b128 v[204:207], v211 offset:49408\n\t" \
  "s_cmp_lt_u32 s86, 8\n\t" \
  "s_cbranch_scc1 .Lw10_%=\n\t" \
  "s_waitcnt vmcnt(5)\n\t" \
  "s_barrier\n\t" \
  ".Lg10_%=:\n\t" \
  "s_add_u32 s92, s92, 65024\n\t" \
  "s_addc_u32 s93, s93, 0\n\t" \
  "s_waitcnt lgkmcnt(6)\n\t" \
  "v_mfma_f32_16x16x32_bf16 v[128:131], v[176:179], v[96:99], v[128:131]\n\t" \
  "v_mfma_f32_16x16x32_bf16 v[136:139], v[176:179], v[100:103], v[136:139]\n\t" \
  "ds_read_b128 v[176:179], v212 offset:0\n\t" \
  "s_add_u32 m0, %[lw], 0\n\t" \
  "v_mfma_f32_16x16x32_bf16 v[132:135], v[180:183], v[96:99], v[132:135]\n\t" \
  "v_mfma_f32_16x16x32_bf16 v[140:143], v[180:183], v[100:103], v[140:143]\n\t" \
  "ds_read_b128 v[180:183], v212 offset:16384\n\t" \
  "global_load_lds_dwordx4 v216, s[92:93] sc1\n\t" \
  "s_waitcnt lgkmcnt(6)\n\t" \
  "v_mfma_f32_16x16x32_bf16 v[128:131], v[184:187], v[104:107], v[128:131]\n\t" \
  "v_mfma_f32_16x16x32_bf16 v[136:139], v[184:187], v[108:111], v[136:139]\n\t" \
  "ds_read_b128 v[184:187], v213 offset:0\n\t" \
  "v_mfma_f32_16x16x32_bf16 v[132:135], v[188:191], v[104:107], v[132:135]\n\t" \
  "v_mfma_f32_16x16x32_bf16 v[140:143], v[188:191], v[108:111], v[140:143]\n\t" \
  "ds_read_b128 v[188:191], v213 offset:16384\n\t" \
  "s_waitcnt lgkmcnt(6)\n\t" \
  "v_mfma_f32_16x16x32_bf16 v[128:131], v[192:195], v[112:115], v[128:131]\n\t" \
  "v_mfma_f32_16x16x32_bf16 v[136:139], v[192:195], v[116:119], v[136:139]\n\t" \
  "ds_read_b128 v[192:195], v214 offset:0\n\t" \
  "s_add_u32 m0, %[lw], 1024\n\t" \
  "v_mfma_f32_16x16x32_bf16 v[132:135], v[196:199], v[112:115], v[132:135]\n\t" \
  "v_mfma_f32_16x16x32_bf16 v[140:143], v[196:199], v[116:119], v[140:143]\n\t" \
  "ds_read_b128 v[196:199], v214 offset:16384\n\t" \
  "global_load_lds_dwordx4 v217, s[92:93] sc1\n\t" \
  "s_waitcnt lgkmcnt(6)\n\t" \
  "v_mfma_f32_16x16x32_bf16 v[128:131], v[200:203], v[120:123], v[128:131]\n\t" \
  "v_mfma_f32_16x16x32_bf16 v[136:139], v[200:203], v[124:127], v[136:139]\n\t" \
  "ds_read_b128 v[200:203], v215 offset:0\n\t" \
  "v_mfma_f32_16x16x32_bf16 v[132:135], v[204:207], v[120:123], v[132:135]\n\t" \
  "v_mfma_f32_16x16x32_bf16 v[140:143], v[204:207], v[124:127], v[140:143]\n\t" \
  "ds_read_b128 v[204:207], v215 offset:16384\n\t" \
  "s_add_u32 s86, s86, 1\n\t" \
  "s_add_u32 s87, s87, 1\n\t" \
  "s_cmp_le_u32 s86, 5\n\t" \
  "s_cbranch_scc1 .Lf10_%=\n\t" \
  ".Lfb10_%=:\n\t" \
  "s_waitcnt lgkmcnt(6)\n\t" \
  "v_mfma_f32_16x16x32_bf16 v[144:147], v[176:179], v[0:3], 0\n\t" \
  "v_mfma_f32_16x16x32_bf16 v[152:155], v[176:179], v[4:7], 0\n\t" \
  "ds_read_b128 v[176:179], v212 offset:256\n\t" \
  "s_add_u32 m0, %[lw], 2048\n\t" \
  "v_mfma_f32_16x16x32_bf16 v[148:151], v[180:183], v[0:3], 0\n\t" \
  "v_mfma_f32_16x16x32_bf16 v[156:159], v[180:183], v[4:7], 0\n\t" \
  "ds_read_b128 v[180:183], v212 offset:16640\n\t" \
  "global_load_lds_dwordx4 v218, s[92:93] sc1\n\t" \
  "s_waitcnt lgkmcnt(6)\n\t" \
  "v_mfma_f32_16x16x32_bf16 v[144:147], v[184:187], v[8:11], v[144:147]\n\t" \
  "v_mfma_f32_16x16x32_bf16 v[152:155], v[184:187], v[12:15], v[152:155]\n\t" \
  "ds_read_b128 v[184:187], v213 offset:256\n\t" \
  "v_mfma_f32_16x16x32_bf16 v[148:151], v[188:191], v[8:11], v[148:151]\n\t" \
  "v_mfma_f32_16x16x32_bf16 v[156:159], v[188:191], v[12:15], v[156:159]\n\t" \
  "ds_read_b128 v[188:191], v213 offset:16640\n\t" \
  "ds_write_b128 v220, v[136:139] offset:0\n\t" \
  "ds_write_b128 v220, v[140:143] offset:1024\n\t" \
  "s_waitcnt lgkmcnt(8)\n\t" \
  "v_mfma_f32_16x16x32_bf16 v[144:147], v[192:195], v[16:19], v[144:147]\n\t" \
  "v_mfma_f32_16x16x32_bf16 v[152:155], v[192:195], v[20:23], v[152:155]\n\t" \
  "ds_read_b128 v[192:195], v214 offset:256\n\t" \
  "s_add_u32 m0, %[lw], 3072\n\t" \
  "v_mfma_f32_16x16x32_bf16 v[148:151], v[196:199], v[16:19], v[148:151]\n\t" \
  "v_mfma_f32_16x16x32_bf16 v[156:159], v[196:199], v[20:23], v[156:159]\n\t" \
  "ds_read_b128 v[196:199], v214 offset:16640\n\t" \
  "global_load_lds_dwordx4 v219, s[92:93] sc1\n\t" \
  "s_lshr_b32 s90, s87, 7\n\t" \
  "s_xor_b32 s90, s90, %[s0]\n\t" \
  "s_and_b32 s90, s90, 1\n\t" \
  "s_lshl1_add_u32 s90, s87, s90\n\t" \
  "s_lshl_b32 s90, s90, 13\n\t" \
  "s_cmp_lt_i32 s86, 6\n\t" \
  "s_cselect_b32 s90, 0x70000000, s90\n\t" \
  "s_waitcnt lgkmcnt(8)\n\t" \
  "v_mfma_f32_16x16x32_bf16 v[144:147], v[200:203], v[24:27], v[144:147]\n\t" \
  "v_mfma_f32_16x16x32_bf16 v[152:155], v[200:203], v[28:31], v[152:155]\n\t" \
  "ds_read_b128 v[200:203], v215 offset:256\n\t" \
  "v_mfma_f32_16x16x32_bf16 v[148:151], v[204:207], v[24:27], v[148:151]\n\t" \
  "v_mfma_f32_16x16x32_bf16 v[156:159], v[204:207], v[28:31], v[156:159]\n\t" \
  "ds_read_b128 v[204:207], v215 offset:16640\n\t" \
  "s_waitcnt lgkmcnt(4)\n\t" \
  "s_cmp_lt_u32 s86, 6\n\t" \
  "s_cbranch_scc1 .Lw20_%=\n\t" \
  "s_waitcnt vmcnt(4)\n\t" \
  "s_barrier\n\t" \
  ".Lg20_%=:\n\t" \
  "s_add_u32 s92, s92, 512\n\t" \
  "s_addc_u32 s93, s93, 0\n\t" \
  "v_mfma_f32_16x16x32_bf16 v[144:147], v[176:179], v[32:35], v[144:147]\n\t" \
  "v_mfma_f32_16x16x32_bf16 v[152:155], v[176:179], v[36:39], v[152:155]\n\t" \
  "ds_read_b128 v[176:179], v212 offset:32768\n\t" \
  "s_add_u32 m0, %[lw], 32768\n\t" \
  "v_mfma_f32_16x16x32_bf16 v[148:151], v[180:183], v[32:35], v[148:151]\n\t" \
  "v_mfma_f32_16x16x32_bf16 v[156:159], v[180:183], v[36:39], v[156:159]\n\t" \
  "ds_read_b128 v[180:183], v212 offset:49152\n\t" \
  "global_load_lds_dwordx4 v216, s[92:93] sc1\n\t" \
  "ds_read_b128 v[160:163], v221 offset:0\n\t" \
  "ds_read_b128 v[164:167], v221 offset:1024\n\t" \
  "v_mfma_f32_16x16x32_bf16 v[144:147], v[184:187], v[40:43], v[144:147]\n\t" \
  "v_mfma_f32_16x16x32_bf16 v[152:155], v[184:187], v[44:47], v[152:155]\n\t" \
  "ds_read_b128 v[184:187], v213 offset:32768\n\t" \
  "v_mfma_f32_16x16x32_bf16 v[148:151], v[188:191], v[40:43], v[148:151]\n\t" \
  "v_mfma_f32_16x16x32_bf16 v[156:159], v[188:191], v[44:47], v[156:159]\n\t" \
  "ds_read_b128 v[188:191], v213 offset:49152\n\t" \
  "s_waitcnt lgkmcnt(8)\n\t" \
  "v_mfma_f32_16x16x32_bf16 v[144:147], v[192:195], v[48:51], v[144:147]\n\t" \
  "v_mfma_f32_16x16x32_bf16 v[152:155], v[192:195], v[52:55], v[152:155]\n\t" \
  "ds_read_b128 v[192:195], v214 offset:32768\n\t" \
  "s_add_u32 m0, %[lw], 33792\n\t" \
  "v_mfma_f32_16x16x32_bf16 v[148:151], v[196:199], v[48:51], v[148:151]\n\t" \
  "v_mfma_f32_16x16x32_bf16 v[156:159], v[196:199], v[52:55], v[156:159]\n\t" \
  "ds_read_b128 v[196:199], v214 offset:49152\n\t" \
  "global_load_lds_dwordx4 v217, s[92:93] sc1\n\t" \
  "s_waitcnt lgkmcnt(4)\n\t" \
  "v_add_f32 v160, v160, v128\n\t" \
  "v_add_f32 v161, v161, v129\n\t" \
  "v_add_f32 v162, v162, v130\n\t" \
  "v_add_f32 v163, v163, v131\n\t" \
  "v_add_f32 v164, v164, v132\n\t" \
  "v_add_f32 v165, v165, v133\n\t" \
  "v_add_f32 v166, v166, v134\n\t" \
  "v_add_f32 v167, v167, v135\n\t" \
  "v_mfma_f32_16x16x32_bf16 v[144:147], v[200:203], v[56:59], v[144:147]\n\t" \
  "v_mfma_f32_16x16x32_bf16 v[152:155], v[200:203], v[60:63], v[152:155]\n\t" \
  "ds_read_b128 v[200:203], v215 offset:32768\n\t" \
  "v_mfma_f32_16x16x32_bf16 v[148:151], v[204:207], v[56:59], v[148:151]\n\t" \
  "v_mfma_f32_16x16x32_bf16 v[156:159], v[204:207], v[60:63], v[156:159]\n\t" \
  "ds_read_b128 v[204:207], v215 offset:49152\n\t" \
  "v_cvt_pk_bf16_f32 v168, v160, v161\n\t" \
  "v_cvt_pk_bf16_f32 v169, v162, v163\n\t" \
  "v_cvt_pk_bf16_f32 v170, v164, v165\n\t" \
  "v_cvt_pk_bf16_f32 v171, v166, v167\n\t" \
  "buffer_store_dwordx4 v[168:171], v172, %[rt], s90 offen sc1\n\t" \
  "v_mfma_f32_16x16x32_bf16 v[144:147], v[176:179], v[64:67], v[144:147]\n\t" \
  "v_mfma_f32_16x16x32_bf16 v[152:155], v[176:179], v[68:71], v[152:155]\n\t" \
  "ds_read_b128 v[176:179], v212 offset:33024\n\t" \
  "s_add_u32 m0, %[lw], 34816\n\t" \
  "v_mfma_f32_16x16x32_bf16 v[148:151], v[180:183], v[64:67], v[148:151]\n\t" \
  "v_mfma_f32_16x16x32_bf16 v[156:159], v[180:183], v[68:71], v[156:159]\n\t" \
  "ds_read_b128 v[180:183], v212 offset:49408\n\t" \
  "global_load_lds_dwordx4 v218, s[92:93] sc1\n\t" \
  "s_waitcnt lgkmcnt(6)\n\t" \
  "v_mfma_f32_16x16x32_bf16 v[144:147], v[184:187], v[72:75], v[144:147]\n\t" \
  "v_mfma_f32_16x16x32_bf16 v[152:155], v[184:187], v[76:79], v[152:155]\n\t" \
  "ds_read_b128 v[184:187], v213 offset:33024\n\t" \
  "v_mfma_f32_16x16x32_bf16 v[148:151], v[188:191], v[72:75], v[148:151]\n\t" \
  "v_mfma_f32_16x16x32_bf16 v[156:159], v[188:191], v[76:79], v[156:159]\n\t" \
  "ds_read_b128 v[188:191], v213 offset:49408\n\t" \
  "s_waitcnt lgkmcnt(6)\n\t" \
  "v_mfma_f32_16x16x32_bf16 v[144:147], v[192:195], v[80:83], v[144:147]\n\t" \
  "v_mfma_f32_16x16x32_bf16 v[152:155], v[192:195], v[84:87], v[152:155]\n\t" \
  "ds_read_b128 v[192:195], v214 offset:33024\n\t" \
  "s_add_u32 m0, %[lw], 35840\n\t" \
  "v_mfma_f32_16x16x32_bf16 v[148:151], v[196:199], v[80:83], v[148:151]\n\t" \
  "v_mfma_f32_16x16x32_bf16 v[156:159], v[196:199], v[84:87], v[156:159]\n\t" \
  "ds_read_b128 v[196:199], v214 offset:49408\n\t" \
  "global_load_lds_dwordx4 v219, s[92:93] sc1\n\t" \
  "s_waitcnt lgkmcnt(6)\n\t" \
  "v_mfma_f32_16x16x32_bf16 v[144:147], v[200:203], v[88:91], v[144:147]\n\t" \
  "v_mfma_f32_16x16x32_bf16 v[152:155], v[200:203], v[92:95], v[152:155]\n\t" \
  "ds_read_b128 v[200:203], v215 offset:33024\n\t" \
  "v_mfma_f32_16x16x32_bf16 v[148:151], v[204:207], v[88:91], v[148:151]\n\t" \
  "v_mfma_f32_16x16x32_bf16 v[156:159], v[204:207], v[92:95], v[156:159]\n\t" \
  "ds_read_b128 v[204:207], v215 offset:49408\n\t" \
  "s_cmp_lt_u32 s86, 8\n\t" \
  "s_cbranch_scc1 .Lw30_%=\n\t" \
  "s_waitcnt vmcnt(5)\n\t" \
  "s_barrier\n\t" \
  ".Lg30_%=:\n\t" \
  "s_add_u32 s92, s92, 65024\n\t" \
  "s_addc_u32 s93, s93, 0\n\t" \
  "s_waitcnt lgkmcnt(6)\n\t" \
  "v_mfma_f32_16x16x32_bf16 v[144:147], v[176:179], v[96:99], v[144:147]\n\t" \
  "v_mfma_f32_16x16x32_bf16 v[152:155], v[176:179], v[100:103], v[152:155]\n\t" \
  "ds_read_b128 v[176:179], v208 offset:0\n\t" \
  "s_add_u32 m0, %[lw], 65536\n\t" \
  "v_mfma_f32_16x16x32_bf16 v[148:151], v[180:183], v[96:99], v[148:151]\n\t" \
  "v_mfma_f32_16x16x32_bf16 v[156:159], v[180:183], v[100:103], v[156:159]\n\t" \
  "ds_read_b128 v[180:183], v208 offset:16384\n\t" \
  "global_load_lds_dwordx4 v216, s[92:93] sc1\n\t" \
  "s_waitcnt lgkmcnt(6)\n\t" \
  "v_mfma_f32_16x16x32_bf16 v[144:147], v[184:187], v[104:107], v[144:147]\n\t" \
  "v_mfma_f32_16x16x32_bf16 v[152:155], v[184:187], v[108:111], v[152:155]\n\t" \
  "ds_read_b128 v[184:187], v209 offset:0\n\t" \
  "v_mfma_f32_16x16x32_bf16 v[148:151], v[188:191], v[104:107], v[148:151]\n\t" \
  "v_mfma_f32_16x16x32_bf16 v[156:159], v[188:191], v[108:111], v[156:159]\n\t" \
  "ds_read_b128 v[188:191], v209 offset:16384\n\t" \
  "s_waitcnt lgkmcnt(6)\n\t" \
  "v_mfma_f32_16x16x32_bf16 v[144:147], v[192:195], v[112:115], v[144:147]\n\t" \
  "v_mfma_f32_16x16x32_bf16 v[152:155], v[192:195], v[116:119], v[152:155]\n\t" \
  "ds_read_b128 v[192:195], v210 offset:0\n\t" \
  "s_add_u32 m0, %[lw], 66560\n\t" \
  "v_mfma_f32_16x16x32_bf16 v[148:151], v[196:199], v[112:115], v[148:151]\n\t" \
  "v_mfma_f32_16x16x32_bf16 v[156:159], v[196:199], v[116:119], v[156:159]\n\t" \
  "ds_read_b128 v[196:199], v210 offset:16384\n\t" \
  "global_load_lds_dwordx4 v217, s[92:93] sc1\n\t" \
  "s_waitcnt lgkmcnt(6)\n\t" \
  "v_mfma_f32_16x16x32_bf16 v[144:147], v[200:203], v[120:123], v[144:147]\n\t" \
  "v_mfma_f32_16x16x32_bf16 v[152:155], v[200:203], v[124:127], v[152:155]\n\t" \
  "ds_read_b128 v[200:203], v211 offset:0\n\t" \
  "v_mfma_f32_16x16x32_bf16 v[148:151], v[204:207], v[120:123], v[148:151]\n\t" \
  "v_mfma_f32_16x16x32_bf16 v[156:159], v[204:207], v[124:127], v[156:159]\n\t" \
  "ds_read_b128 v[204:207], v211 offset:16384\n\t" \
  "s_add_u32 s86, s86, 1\n\t" \
  "s_add_u32 s87, s87, 1\n\t" \
  "s_cmp_lt_u32 s86, %[end]\n\t" \
  "s_cbranch_scc1 .Lp0_%=\n\t" \
  "s_branch .Lx0_%=\n\t" \
  ".Lf00_%=:\n\t" \
  "s_nop 7\n\t" \
  "ds_write_b128 v220, v[152:155] offset:0\n\t" \
  "ds_write_b128 v220, v[156:159] offset:1024\n\t" \
  "s_lshr_b32 s90, s87, 7\n\t" \
  "s_xor_b32 s90, s90, %[s0]\n\t" \
  "s_and_b32 s90, s90, 1\n\t" \
  "s_lshl1_add_u32 s90, s87, s90\n\t" \
  "s_lshl_b32 s90, s90, 13\n\t" \
  "s_cmp_lt_i32 s86, 1\n\t" \
  "s_cselect_b32 s90, 0x70000000, s90\n\t" \
  "s_waitcnt lgkmcnt(0)\n\t" \
  "s_barrier\n\t" \
  "ds_read_b128 v[160:163], v221 offset:0\n\t" \
  "ds_read_b128 v[164:167], v221 offset:1024\n\t" \
  "s_waitcnt lgkmcnt(0)\n\t" \
  "v_add_f32 v160, v160, v144\n\t" \
  "v_add_f32 v161, v161, v145\n\t" \
  "v_add_f32 v162, v162, v146\n\t" \
  "v_add_f32 v163, v163, v147\n\t" \
  "v_add_f32 v164, v164, v148\n\t" \
  "v_add_f32 v165, v165, v149\n\t" \
  "v_add_f32 v166, v166, v150\n\t" \
  "v_add_f32 v167, v167, v151\n\t" \
  "v_cvt_pk_bf16_f32 v168, v160, v161\n\t" \
  "v_cvt_pk_bf16_f32 v169, v162, v163\n\t" \
  "v_cvt_pk_bf16_f32 v170, v164, v165\n\t" \
  "v_cvt_pk_bf16_f32 v171, v166, v167\n\t" \
  "buffer_store_dwordx4 v[168:171], v172, %[rt], s90 offen sc1\n\t" \
  "s_branch .Lfb00_%=\n\t" \
  ".Lf10_%=:\n\t" \
  "s_nop 7\n\t" \
  "ds_write_b128 v220, v[136:139] offset:0\n\t" \
  "ds_write_b128 v220, v[140:143] offset:1024\n\t" \
  "s_lshr_b32 s90, s87, 7\n\t" \
  "s_xor_b32 s90, s90, %[s0]\n\t" \
  "s_and_b32 s90, s90, 1\n\t" \
  "s_lshl1_add_u32 s90, s87, s90\n\t" \
  "s_lshl_b32 s90, s90, 13\n\t" \
  "s_cmp_lt_i32 s86, 1\n\t" \
  "s_cselect_b32 s90, 0x70000000, s90\n\t" \
  "s_waitcnt lgkmcnt(0)\n\t" \
  "s_barrier\n\t" \
  "ds_read_b128 v[160:163], v221 offset:0\n\t" \
  "ds_read_b128 v[164:167], v221 offset:1024\n\t" \
  "s_waitcnt lgkmcnt(0)\n\t" \
  "v_add_f32 v160, v160, v128\n\t" \
  "v_add_f32 v161, v161, v129\n\t" \
  "v_add_f32 v162, v162, v130\n\t" \
  "v_add_f32 v163, v163, v131\n\t" \
  "v_add_f32 v164, v164, v132\n\t" \
  "v_add_f32 v165, v165, v133\n\t" \
  "v_add_f32 v166, v166, v134\n\t" \
  "v_add_f32 v167, v167, v135\n\t" \
  "v_cvt_pk_bf16_f32 v168, v160, v161\n\t" \
  "v_cvt_pk_bf16_f32 v169, v162, v163\n\t" \
  "v_cvt_pk_bf16_f32 v170, v164, v165\n\t" \
  "v_cvt_pk_bf16_f32 v171, v166, v167\n\t" \
  "buffer_store_dwordx4 v[168:171], v172, %[rt], s90 offen sc1\n\t" \
  "s_branch .Lfb10_%=\n\t" \
  ".Lw00_%=:\n\t" \
  "s_waitcnt vmcnt(2)\n\t" \
  "s_barrier\n\t" \
  "s_branch .Lg00_%=\n\t" \
  ".Lw10_%=:\n\t" \
  "s_waitcnt vmcnt(2)\n\t" \
  "s_barrier\n\t" \
  "s_branch .Lg10_%=\n\t" \
  ".Lw20_%=:\n\t" \
  "s_waitcnt vmcnt(2)\n\t" \
  "s_barrier\n\t" \
  "s_branch .Lg20_%=\n\t" \
  ".Lw30_%=:\n\t" \
  "s_waitcnt vmcnt(2)\n\t" \
  "s_barrier\n\t" \
  "s_branch .Lg30_%=\n\t" \
  ".Lx0_%=:\n\t" \
  "s_waitcnt lgkmcnt(0)\n\t" \
  "s_setprio 0\n\t" \
  "s_mov_b32 m0, s94\n\t" \
  "s_branch .Le_%=\n\t" \
  ".Lk_%=:\n\t" \
  "s_mov_b32 s94, m0\n\t" \
  "s_mov_b64 s[92:93], %[x0]\n\t" \
  "s_mov_b32 s86, %[g0]\n\t" \
  "s_add_u32 s87, s86, %[t0]\n\t" \
  "s_sub_i32 s88, s86, 3\n\t" \
  "ds_read_b128 v[176:179], v208 offset:0\n\t" \
  "ds_read_b128 v[180:183], v208 offset:16384\n\t" \
  "ds_read_b128 v[184:187], v209 offset:0\n\t" \
  "ds_read_b128 v[188:191], v209 offset:16384\n\t" \
  "ds_read_b128 v[192:195], v210 offset:0\n\t" \
  "ds_read_b128 v[196:199], v210 offset:16384\n\t" \
  "ds_read_b128 v[200:203], v211 offset:0\n\t" \
  "ds_read_b128 v[204:207], v211 offset:16384\n\t" \
  "s_branch .Lfb01_%=\n\t" \
  ".Lp1_%=:\n\t" \
  "s_cmp_le_u32 s86, 5\n\t" \
  "s_cbranch_scc1 .Lf01_%=\n\t" \
  ".Lfb01_%=:\n\t" \
  "s_waitcnt lgkmcnt(6)\n\t" \
  "v_mfma_f32_16x16x32_bf16 v[128:131], v[176:179], v[0:3], 0\n\t" \
  "v_mfma_f32_16x16x32_bf16 v[136:139], v[176:179], v[4:7], 0\n\t" \
  "ds_read_b128 v[176:179], v208 offset:256\n\t" \
  "v_mfma_f32_16x16x32_bf16 v[132:135], v[180:183], v[0:3], 0\n\t" \
  "v_mfma_f32_16x16x32_bf16 v[140:143], v[180:183], v[4:7], 0\n\t" \
  "ds_read_b128 v[180:183], v208 offset:16640\n\t" \
  "s_waitcnt lgkmcnt(6)\n\t" \
  "v_mfma_f32_16x16x32_bf16 v[128:131], v[184:187], v[8:11], v[128:131]\n\t" \
  "v_mfma_f32_16x16x32_bf16 v[136:139], v[184:187], v[12:15], v[136:139]\n\t" \
  "ds_read_b128 v[184:187], v209 offset:256\n\t" \
  "s_add_u32 m0, %[lw], 67584\n\t" \
  "v_mfma_f32_16x16x32_bf16 v[132:135], v[188:191], v[8:11], v[132:135]\n\t" \
  "v_mfma_f32_16x16x32_bf16 v[140:143], v[188:191], v[12:15], v[140:143]\n\t" \
  "ds_read_b128 v[188:191], v209 offset:16640\n\t" \
  "global_load_lds_dwordx4 v218, s[92:93] sc1\n\t" \
  "ds_write_b128 v220, v[152:155] offset:0\n\t" \
  "ds_write_b128 v220, v[156:159] offset:1024\n\t" \
  "s_waitcnt lgkmcnt(8)\n\t" \
  "v_mfma_f32_16x16x32_bf16 v[128:131], v[192:195], v[16:19], v[128:131]\n\t" \
  "v_mfma_f32_16x16x32_bf16 v[136:139], v[192:195], v[20:23], v[136:139]\n\t" \
  "ds_read_b128 v[192:195], v210 offset:256\n\t" \
  "v_mfma_f32_16x16x32_bf16 v[132:135], v[196:199], v[16:19], v[132:135]\n\t" \
  "v_mfma_f32_16x16x32_bf16 v[140:143], v[196:199], v[20:23], v[140:143]\n\t" \
  "ds_read_b128 v[196:199], v210 offset:16640\n\t" \
  "s_lshr_b32 s90, s87, 7\n\t" \
  "s_xor_b32 s90, s90, %[s0]\n\t" \
  "s_and_b32 s90, s90, 1\n\t" \
  "s_lshl1_add_u32 s90, s87, s90\n\t" \
  "s_lshl_b32 s90, s90, 13\n\t" \
  "s_cmp_lt_i32 s86, 6\n\t" \
  "s_cselect_b32 s90, 0x70000000, s90\n\t" \
  "s_waitcnt lgkmcnt(8)\n\t" \
  "v_mfma_f32_16x16x32_bf16 v[128:131], v[200:203], v[24:27], v[128:131]\n\t" \
  "v_mfma_f32_16x16x32_bf16 v[136:139], v[200:203], v[28:31], v[136:139]\n\t" \
  "ds_read_b128 v[200:203], v211 offset:256\n\t" \
  "s_add_u32 m0, %[lw], 68608\n\t" \
  "v_mfma_f32_16x16x32_bf16 v[132:135], v[204:207], v[24:27], v[132:135]\n\t" \
  "v_mfma_f32_16x16x32_bf16 v[140:143], v[204:207], v[28:31], v[140:143]\n\t" \
  "ds_read_b128 v[204:207], v211 offset:16640\n\t" \
  "global_load_lds_dwordx4 v219, s[92:93] sc1\n\t" \
  "s_waitcnt lgkmcnt(4)\n\t" \
  "s_cmp_lt_u32 s86, 6\n\t" \
  "s_cbranch_scc1 .Lw01_%=\n\t" \
  "s_waitcnt vmcnt(4)\n\t" \
  "s_barrier\n\t" \
  ".Lg01_%=:\n\t" \
  "s_add_u32 s92, s92, 512\n\t" \
  "s_addc_u32 s93, s93, 0\n\t" \
  "v_mfma_f32_16x16x32_bf16 v[128:131], v[176:179], v[32:35], v[128:131]\n\t" \
  "v_mfma_f32_16x16x32_bf16 v[136:139], v[176:179], v[36:39], v[136:139]\n\t" \
  "ds_read_b128 v[176:179], v208 offset:32768\n\t" \
  "v_mfma_f32_16x16x32_bf16 v[132:135], v[180:183], v[32:35], v[132:135]\n\t" \
  "v_mfma_f32_16x16x32_bf16 v[140:143], v[180:183], v[36:39], v[140:143]\n\t" \
  "ds_read_b128 v[180:183], v208 offset:49152\n\t" \
  "ds_read_b128 v[160:163], v221 offset:0\n\t" \
  "ds_read_b128 v[164:167], v221 offset:1024\n\t" \
  "s_sub_i32 s91, s86, 2\n\t" \
  "s_max_i32 s88, s88, s91\n\t" \
  "v_mov_b32 v173, s88\n\t" \
  "buffer_store_dword v173, v174, %[rs], 0 offen sc1\n\t" \
  "v_mfma_f32_16x16x32_bf16 v[128:131], v[184:187], v[40:43], v[128:131]\n\t" \
  "v_mfma_f32_16x16x32_bf16 v[136:139], v[184:187], v[44:47], v[136:139]\n\t" \
  "ds_read_b128 v[184:187], v209 offset:32768\n\t" \
  "s_add_u32 m0, %[lw], 98304\n\t" \
  "v_mfma_f32_16x16x32_bf16 v[132:135], v[188:191], v[40:43], v[132:135]\n\t" \
  "v_mfma_f32_16x16x32_bf16 v[140:143], v[188:191], v[44:47], v[140:143]\n\t" \
  "ds_read_b128 v[188:191], v209 offset:49152\n\t" \
  "global_load_lds_dwordx4 v216, s[92:93] sc1\n\t" \
  "s_waitcnt lgkmcnt(8)\n\t" \
  "v_mfma_f32_16x16x32_bf16 v[128:131], v[192:195], v[48:51], v[128:131]\n\t" \
  "v_mfma_f32_16x16x32_bf16 v[136:139], v[192:195], v[52:55], v[136:139]\n\t" \
  "ds_read_b128 v[192:195], v210 offset:32768\n\t" \
  "v_mfma_f32_16x16x32_bf16 v[132:135], v[196:199], v[48:51], v[132:135]\n\t" \
  "v_mfma_f32_16x16x32_bf16 v[140:143], v[196:199], v[52:55], v[140:143]\n\t" \
  "ds_read_b128 v[196:199], v210 offset:49152\n\t" \
  "s_waitcnt lgkmcnt(4)\n\t" \
  "v_add_f32 v160, v160, v144\n\t" \
  "v_add_f32 v161, v161, v145\n\t" \
  "v_add_f32 v162, v162, v146\n\t" \
  "v_add_f32 v163, v163, v147\n\t" \
  "v_add_f32 v164, v164, v148\n\t" \
  "v_add_f32 v165, v165, v149\n\t" \
  "v_add_f32 v166, v166, v150\n\t" \
  "v_add_f32 v167, v167, v151\n\t" \
  "v_mfma_f32_16x16x32_bf16 v[128:131], v[200:203], v[56:59], v[128:131]\n\t" \
  "v_mfma_f32_16x16x32_bf16 v[136:139], v[200:203], v[60:63], v[136:139]\n\t" \
  "ds_read_b128 v[200:203], v211 offset:32768\n\t" \
  "s_add_u32 m0, %[lw], 99328\n\t" \
  "v_mfma_f32_16x16x32_bf16 v[132:135], v[204:207], v[56:59], v[132:135]\n\t" \
  "v_mfma_f32_16x16x32_bf16 v[140:143], v[204:207], v[60:63], v[140:143]\n\t" \
  "ds_read_b128 v[204:207], v211 offset:49152\n\t" \
  "global_load_lds_dwordx4 v217, s[92:93] sc1\n\t" \
  "v_cvt_pk_bf16_f32 v168, v160, v161\n\t" \
  "v_cvt_pk_bf16_f32 v169, v162, v163\n\t" \
  "v_cvt_pk_bf16_f32 v170, v164, v165\n\t" \
  "v_cvt_pk_bf16_f32 v171, v166, v167\n\t" \
  "buffer_store_dwordx4 v[168:171], v172, %[rt], s90 offen sc1\n\t" \
  "v_mfma_f32_16x16x32_bf16 v[128:131], v[176:179], v[64:67], v[128:131]\n\t" \
  "v_mfma_f32_16x16x32_bf16 v[136:139], v[176:179], v[68:71], v[136:139]\n\t" \
  "ds_read_b128 v[176:179], v208 offset:33024\n\t" \
  "v_mfma_f32_16x16x32_bf16 v[132:135], v[180:183], v[64:67], v[132:135]\n\t" \
  "v_mfma_f32_16x16x32_bf16 v[140:143], v[180:183], v[68:71], v[140:143]\n\t" \
  "ds_read_b128 v[180:183], v208 offset:49408\n\t" \
  "s_waitcnt lgkmcnt(6)\n\t" \
  "v_mfma_f32_16x16x32_bf16 v[128:131], v[184:187], v[72:75], v[128:131]\n\t" \
  "v_mfma_f32_16x16x32_bf16 v[136:139], v[184:187], v[76:79], v[136:139]\n\t" \
  "ds_read_b128 v[184:187], v209 offset:33024\n\t" \
  "s_add_u32 m0, %[lw], 100352\n\t" \
  "v_mfma_f32_16x16x32_bf16 v[132:135], v[188:191], v[72:75], v[132:135]\n\t" \
  "v_mfma_f32_16x16x32_bf16 v[140:143], v[188:191], v[76:79], v[140:143]\n\t" \
  "ds_read_b128 v[188:191], v209 offset:49408\n\t" \
  "global_load_lds_dwordx4 v218, s[92:93] sc1\n\t" \
  "s_waitcnt lgkmcnt(6)\n\t" \
  "v_mfma_f32_16x16x32_bf16 v[128:131], v[192:195], v[80:83], v[128:131]\n\t" \
  "v_mfma_f32_16x16x32_bf16 v[136:139], v[192:195], v[84:87], v[136:139]\n\t" \
  "ds_read_b128 v[192:195], v210 offset:33024\n\t" \
  "v_mfma_f32_16x16x32_bf16 v[132:135], v[196:199], v[80:83], v[132:135]\n\t" \
  "v_mfma_f32_16x16x32_bf16 v[140:143], v[196:199], v[84:87], v[140:143]\n\t" \
  "ds_read_b128 v[196:199], v210 offset:49408\n\t" \
  "s_waitcnt lgkmcnt(6)\n\t" \
  "v_mfma_f32_16x16x32_bf16 v[128:131], v[200:203], v[88:91], v[128:131]\n\t" \
  "v_mfma_f32_16x16x32_bf16 v[136:139], v[200:203], v[92:95], v[136:139]\n\t" \
  "ds_read_b128 v[200:203], v211 offset:33024\n\t" \
  "s_add_u32 m0, %[lw], 101376\n\t" \
  "v_mfma_f32_16x16x32_bf16 v[132:135], v[204:207], v[88:91], v[132:135]\n\t" \
  "v_mfma_f32_16x16x32_bf16 v[140:143], v[204:207], v[92:95], v[140:143]\n\t" \
  "ds_read_b128 v[204:207], v211 offset:49408\n\t" \
  "global_load_lds_dwordx4 v219, s[92:93] sc1\n\t" \
  "s_cmp_lt_u32 s86, 8\n\t" \
  "s_cbranch_scc1 .Lw11_%=\n\t" \
  "s_waitcnt vmcnt(6)\n\t" \
  "s_barrier\n\t" \
  ".Lg11_%=:\n\t" \
  "s_add_u32 s92, s92, 65024\n\t" \
  "s_addc_u32 s93, s93, 0\n\t" \
  "s_waitcnt lgkmcnt(6)\n\t" \
  "v_mfma_f32_16x16x32_bf16 v[128:131], v[176:179], v[96:99], v[128:131]\n\t" \
  "v_mfma_f32_16x16x32_bf16 v[136:139], v[176:179], v[100:103], v[136:139]\n\t" \
  "ds_read_b128 v[176:179], v212 offset:0\n\t" \
  "v_mfma_f32_16x16x32_bf16 v[132:135], v[180:183], v[96:99], v[132:135]\n\t" \
  "v_mfma_f32_16x16x32_bf16 v[140:143], v[180:183], v[100:103], v[140:143]\n\t" \
  "ds_read_b128 v[180:183], v212 offset:16384\n\t" \
  "s_waitcnt lgkmcnt(6)\n\t" \
  "v_mfma_f32_16x16x32_bf16 v[128:131], v[184:187], v[104:107], v[128:131]\n\t" \
  "v_mfma_f32_16x16x32_bf16 v[136:139], v[184:187], v[108:111], v[136:139]\n\t" \
  "ds_read_b128 v[184:187], v213 offset:0\n\t" \
  "s_add_u32 m0, %[lw], 0\n\t" \
  "v_mfma_f32_16x16x32_bf16 v[132:135], v[188:191], v[104:107], v[132:135]\n\t" \
  "v_mfma_f32_16x16x32_bf16 v[140:143], v[188:191], v[108:111], v[140:143]\n\t" \
  "ds_read_b128 v[188:191], v213 offset:16384\n\t" \
  "global_load_lds_dwordx4 v216, s[92:93] sc1\n\t" \
  "s_waitcnt lgkmcnt(6)\n\t" \
  "v_mfma_f32_16x16x32_bf16 v[128:131], v[192:195], v[112:115], v[128:131]\n\t" \
  "v_mfma_f32_16x16x32_bf16 v[136:139], v[192:195], v[116:119], v[136:139]\n\t" \
  "ds_read_b128 v[192:195], v214 offset:0\n\t" \
  "v_mfma_f32_16x16x32_bf16 v[132:135], v[196:199], v[112:115], v[132:135]\n\t" \
  "v_mfma_f32_16x16x32_bf16 v[140:143], v[196:199], v[116:119], v[140:143]\n\t" \
  "ds_read_b128 v[196:199], v214 offset:16384\n\t" \
  "s_waitcnt lgkmcnt(6)\n\t" \
  "v_mfma_f32_16x16x32_bf16 v[128:131], v[200:203], v[120:123], v[128:131]\n\t" \
  "v_mfma_f32_16x16x32_bf16 v[136:139], v[200:203], v[124:127], v[136:139]\n\t" \
  "ds_read_b128 v[200:203], v215 offset:0\n\t" \
  "s_add_u32 m0, %[lw], 1024\n\t" \
  "v_mfma_f32_16x16x32_bf16 v[132:135], v[204:207], v[120:123], v[132:135]\n\t" \
  "v_mfma_f32_16x16x32_bf16 v[140:143], v[204:207], v[124:127], v[140:143]\n\t" \
  "ds_read_b128 v[204:207], v215 offset:16384\n\t" \
  "global_load_lds_dwordx4 v217, s[92:93] sc1\n\t" \
  "s_add_u32 s86, s86, 1\n\t" \
  "s_add_u32 s87, s87, 1\n\t" \
  "s_cmp_le_u32 s86, 5\n\t" \
  "s_cbranch_scc1 .Lf11_%=\n\t" \
  ".Lfb11_%=:\n\t" \
  "s_waitcnt lgkmcnt(6)\n\t" \
  "v_mfma_f32_16x16x32_bf16 v[144:147], v[176:179], v[0:3], 0\n\t" \
  "v_mfma_f32_16x16x32_bf16 v[152:155], v[176:179], v[4:7], 0\n\t" \
  "ds_read_b128 v[176:179], v212 offset:256\n\t" \
  "v_mfma_f32_16x16x32_bf16 v[148:151], v[180:183], v[0:3], 0\n\t" \
  "v_mfma_f32_16x16x32_bf16 v[156:159], v[180:183], v[4:7], 0\n\t" \
  "ds_read_b128 v[180:183], v212 offset:16640\n\t" \
  "s_waitcnt lgkmcnt(6)\n\t" \
  "v_mfma_f32_16x16x32_bf16 v[144:147], v[184:187], v[8:11], v[144:147]\n\t" \
  "v_mfma_f32_16x16x32_bf16 v[152:155], v[184:187], v[12:15], v[152:155]\n\t" \
  "ds_read_b128 v[184:187], v213 offset:256\n\t" \
  "s_add_u32 m0, %[lw], 2048\n\t" \
  "v_mfma_f32_16x16x32_bf16 v[148:151], v[188:191], v[8:11], v[148:151]\n\t" \
  "v_mfma_f32_16x16x32_bf16 v[156:159], v[188:191], v[12:15], v[156:159]\n\t" \
  "ds_read_b128 v[188:191], v213 offset:16640\n\t" \
  "global_load_lds_dwordx4 v218, s[92:93] sc1\n\t" \
  "ds_write_b128 v220, v[136:139] offset:0\n\t" \
  "ds_write_b128 v220, v[140:143] offset:1024\n\t" \
  "s_waitcnt lgkmcnt(8)\n\t" \
  "v_mfma_f32_16x16x32_bf16 v[144:147], v[192:195], v[16:19], v[144:147]\n\t" \
  "v_mfma_f32_16x16x32_bf16 v[152:155], v[192:195], v[20:23], v[152:155]\n\t" \
  "ds_read_b128 v[192:195], v214 offset:256\n\t" \
  "v_mfma_f32_16x16x32_bf16 v[148:151], v[196:199], v[16:19], v[148:151]\n\t" \
  "v_mfma_f32_16x16x32_bf16 v[156:159], v[196:199], v[20:23], v[156:159]\n\t" \
  "ds_read_b128 v[196:199], v214 offset:16640\n\t" \
  "s_lshr_b32 s90, s87, 7\n\t" \
  "s_xor_b32 s90, s90, %[s0]\n\t" \
  "s_and_b32 s90, s90, 1\n\t" \
  "s_lshl1_add_u32 s90, s87, s90\n\t" \
  "s_lshl_b32 s90, s90, 13\n\t" \
  "s_cmp_lt_i32 s86, 6\n\t" \
  "s_cselect_b32 s90, 0x70000000, s90\n\t" \
  "s_waitcnt lgkmcnt(8)\n\t" \
  "v_mfma_f32_16x16x32_bf16 v[144:147], v[200:203], v[24:27], v[144:147]\n\t" \
  "v_mfma_f32_16x16x32_bf16 v[152:155], v[200:203], v[28:31], v[152:155]\n\t" \
  "ds_read_b128 v[200:203], v215 offset:256\n\t" \
  "s_add_u32 m0, %[lw], 3072\n\t" \
  "v_mfma_f32_16x16x32_bf16 v[148:151], v[204:207], v[24:27], v[148:151]\n\t" \
  "v_mfma_f32_16x16x32_bf16 v[156:159], v[204:207], v[28:31], v[156:159]\n\t" \
  "ds_read_b128 v[204:207], v215 offset:16640\n\t" \
  "global_load_lds_dwordx4 v219, s[92:93] sc1\n\t" \
  "s_waitcnt lgkmcnt(4)\n\t" \
  "s_cmp_lt_u32 s86, 6\n\t" \
  "s_cbranch_scc1 .Lw21_%=\n\t" \
  "s_waitcnt vmcnt(4)\n\t" \
  "s_barrier\n\t" \
  ".Lg21_%=:\n\t" \
  "s_add_u32 s92, s92, 512\n\t" \
  "s_addc_u32 s93, s93, 0\n\t" \
  "v_mfma_f32_16x16x32_bf16 v[144:147], v[176:179], v[32:35], v[144:147]\n\t" \
  "v_mfma_f32_16x16x32_bf16 v[152:155], v[176:179], v[36:39], v[152:155]\n\t" \
  "ds_read_b128 v[176:179], v212 offset:32768\n\t" \
  "v_mfma_f32_16x16x32_bf16 v[148:151], v[180:183], v[32:35], v[148:151]\n\t" \
  "v_mfma_f32_16x16x32_bf16 v[156:159], v[180:183], v[36:39], v[156:159]\n\t" \
  "ds_read_b128 v[180:183], v212 offset:49152\n\t" \
  "ds_read_b128 v[160:163], v221 offset:0\n\t" \
  "ds_read_b128 v[164:167], v221 offset:1024\n\t" \
  "s_sub_i32 s91, s86, 2\n\t" \
  "s_max_i32 s88, s88, s91\n\t" \
  "v_mov_b32 v173, s88\n\t" \
  "buffer_store_dword v173, v174, %[rs], 0 offen sc1\n\t" \
  "v_mfma_f32_16x16x32_bf16 v[144:147], v[184:187], v[40:43], v[144:147]\n\t" \
  "v_mfma_f32_16x16x32_bf16 v[152:155], v[184:187], v[44:47], v[152:155]\n\t" \
  "ds_read_b128 v[184:187], v213 offset:32768\n\t" \
  "s_add_u32 m0, %[lw], 32768\n\t" \
  "v_mfma_f32_16x16x32_bf16 v[148:151], v[188:191], v[40:43], v[148:151]\n\t" \
  "v_mfma_f32_16x16x32_bf16 v[156:159], v[188:191], v[44:47], v[156:159]\n\t" \
  "ds_read_b128 v[188:191], v213 offset:49152\n\t" \
  "global_load_lds_dwordx4 v216, s[92:93] sc1\n\t" \
  "s_waitcnt lgkmcnt(8)\n\t" \
  "v_mfma_f32_16x16x32_bf16 v[144:147], v[192:195], v[48:51], v[144:147]\n\t" \
  "v_mfma_f32_16x16x32_bf16 v[152:155], v[192:195], v[52:55], v[152:155]\n\t" \
  "ds_read_b128 v[192:195], v214 offset:32768\n\t" \
  "v_mfma_f32_16x16x32_bf16 v[148:151], v[196:199], v[48:51], v[148:151]\n\t" \
  "v_mfma_f32_16x16x32_bf16 v[156:159], v[196:199], v[52:55], v[156:159]\n\t" \
  "ds_read_b128 v[196:199], v214 offset:49152\n\t" \
  "s_waitcnt lgkmcnt(4)\n\t" \
  "v_add_f32 v160, v160, v128\n\t" \
  "v_add_f32 v161, v161, v129\n\t" \
  "v_add_f32 v162, v162, v130\n\t" \
  "v_add_f32 v163, v163, v131\n\t" \
  "v_add_f32 v164, v164, v132\n\t" \
  "v_add_f32 v165, v165, v133\n\t" \
  "v_add_f32 v166, v166, v134\n\t" \
  "v_add_f32 v167, v167, v135\n\t" \
  "v_mfma_f32_16x16x32_bf16 v[144:147], v[200:203], v[56:59], v[144:147]\n\t" \
  "v_mfma_f32_16x16x32_bf16 v[152:155], v[200:203], v[60:63], v[152:155]\n\t" \
  "ds_read_b128 v[200:203], v215 offset:32768\n\t" \
  "s_add_u32 m0, %[lw], 33792\n\t" \
  "v_mfma_f32_16x16x32_bf16 v[148:151], v[204:207], v[56:59], v[148:151]\n\t" \
  "v_mfma_f32_16x16x32_bf16 v[156:159], v[204:207], v[60:63], v[156:159]\n\t" \
  "ds_read_b128 v[204:207], v215 offset:49152\n\t" \
  "global_load_lds_dwordx4 v217, s[92:93] sc1\n\t" \
  "v_cvt_pk_bf16_f32 v168, v160, v161\n\t" \
  "v_cvt_pk_bf16_f32 v169, v162, v163\n\t" \
  "v_cvt_pk_bf16_f32 v170, v164, v165\n\t" \
  "v_cvt_pk_bf16_f32 v171, v166, v167\n\t" \
  "buffer_store_dwordx4 v[168:171], v172, %[rt], s90 offen sc1\n\t" \
  "v_mfma_f32_16x16x32_bf16 v[144:147], v[176:179], v[64:67], v[144:147]\n\t" \
  "v_mfma_f32_16x16x32_bf16 v[152:155], v[176:179], v[68:71], v[152:155]\n\t" \
  "ds_read_b128 v[176:179], v212 offset:33024\n\t" \
  "v_mfma_f32_16x16x32_bf16 v[148:151], v[180:183], v[64:67], v[148:151]\n\t" \
  "v_mfma_f32_16x16x32_bf16 v[156:159], v[180:183], v[68:71], v[156:159]\n\t" \
  "ds_read_b128 v[180:183], v212 offset:49408\n\t" \
  "s_waitcnt lgkmcnt(6)\n\t" \
  "v_mfma_f32_16x16x32_bf16 v[144:147], v[184:187], v[72:75], v[144:147]\n\t" \
  "v_mfma_f32_16x16x32_bf16 v[152:155], v[184:187], v[76:79], v[152:155]\n\t" \
  "ds_read_b128 v[184:187], v213 offset:33024\n\t" \
  "s_add_u32 m0, %[lw], 34816\n\t" \
  "v_mfma_f32_16x16x32_bf16 v[148:151], v[188:191], v[72:75], v[148:151]\n\t" \
  "v_mfma_f32_16x16x32_bf16 v[156:159], v[188:191], v[76:79], v[156:159]\n\t" \
  "ds_read_b128 v[188:191], v213 offset:49408\n\t" \
  "global_load_lds_dwordx4 v218, s[92:93] sc1\n\t" \
  "s_waitcnt lgkmcnt(6)\n\t" \
  "v_mfma_f32_16x16x32_bf16 v[144:147], v[192:195], v[80:83], v[144:147]\n\t" \
  "v_mfma_f32_16x16x32_bf16 v[152:155], v[192:195], v[84:87], v[152:155]\n\t" \
  "ds_read_b128 v[192:195], v214 offset:33024\n\t" \
  "v_mfma_f32_16x16x32_bf16 v[148:151], v[196:199], v[80:83], v[148:151]\n\t" \
  "v_mfma_f32_16x16x32_bf16 v[156:159], v[196:199], v[84:87], v[156:159]\n\t" \
  "ds_read_b128 v[196:199], v214 offset:49408\n\t" \
  "s_waitcnt lgkmcnt(6)\n\t" \
  "v_mfma_f32_16x16x32_bf16 v[144:147], v[200:203], v[88:91], v[144:147]\n\t" \
  "v_mfma_f32_16x16x32_bf16 v[152:155], v[200:203], v[92:95], v[152:155]\n\t" \
  "ds_read_b128 v[200:203], v215 offset:33024\n\t" \
  "s_add_u32 m0, %[lw], 35840\n\t" \
  "v_mfma_f32_16x16x32_bf16 v[148:151], v[204:207], v[88:91], v[148:151]\n\t" \
  "v_mfma_f32_16x16x32_bf16 v[156:159], v[204:207], v[92:95], v[156:159]\n\t" \
  "ds_read_b128 v[204:207], v215 offset:49408\n\t" \
  "global_load_lds_dwordx4 v219, s[92:93] sc1\n\t" \
  "s_cmp_lt_u32 s86, 8\n\t" \
  "s_cbranch_scc1 .Lw31_%=\n\t" \
  "s_waitcnt vmcnt(6)\n\t" \
  "s_barrier\n\t" \
  ".Lg31_%=:\n\t" \
  "s_add_u32 s92, s92, 65024\n\t" \
  "s_addc_u32 s93, s93, 0\n\t" \
  "s_waitcnt lgkmcnt(6)\n\t" \
  "v_mfma_f32_16x16x32_bf16 v[144:147], v[176:179], v[96:99], v[144:147]\n\t" \
  "v_mfma_f32_16x16x32_bf16 v[152:155], v[176:179], v[100:103], v[152:155]\n\t" \
  "ds_read_b128 v[176:179], v208 offset:0\n\t" \
  "v_mfma_f32_16x16x32_bf16 v[148:151], v[180:183], v[96:99], v[148:151]\n\t" \
  "v_mfma_f32_16x16x32_bf16 v[156:159], v[180:183], v[100:103], v[156:159]\n\t" \
  "ds_read_b128 v[180:183], v208 offset:16384\n\t" \
  "s_waitcnt lgkmcnt(6)\n\t" \
  "v_mfma_f32_16x16x32_bf16 v[144:147], v[184:187], v[104:107], v[144:147]\n\t" \
  "v_mfma_f32_16x16x32_bf16 v[152:155], v[184:187], v[108:111], v[152:155]\n\t" \
  "ds_read_b128 v[184:187], v209 offset:0\n\t" \
  "s_add_u32 m0, %[lw], 65536\n\t" \
  "v_mfma_f32_16x16x32_bf16 v[148:151], v[188:191], v[104:107], v[148:151]\n\t" \
  "v_mfma_f32_16x16x32_bf16 v[156:159], v[188:191], v[108:111], v[156:159]\n\t" \
  "ds_read_b128 v[188:191], v209 offset:16384\n\t" \
  "global_load_lds_dwordx4 v216, s[92:93] sc1\n\t" \
  "s_waitcnt lgkmcnt(6)\n\t" \
  "v_mfma_f32_16x16x32_bf16 v[144:147], v[192:195], v[112:115], v[144:147]\n\t" \
  "v_mfma_f32_16x16x32_bf16 v[152:155], v[192:195], v[116:119], v[152:155]\n\t" \
  "ds_read_b128 v[192:195], v210 offset:0\n\t" \
  "v_mfma_f32_16x16x32_bf16 v[148:151], v[196:199], v[112:115], v[148:151]\n\t" \
  "v_mfma_f32_16x16x32_bf16 v[156:159], v[196:199], v[116:119], v[156:159]\n\t" \
  "ds_read_b128 v[196:199], v210 offset:16384\n\t" \
  "s_waitcnt lgkmcnt(6)\n\t" \
  "v_mfma_f32_16x16x32_bf16 v[144:147], v[200:203], v[120:123], v[144:147]\n\t" \
  "v_mfma_f32_16x16x32_bf16 v[152:155], v[200:203], v[124:127], v[152:155]\n\t" \
  "ds_read_b128 v[200:203], v211 offset:0\n\t" \
  "s_add_u32 m0, %[lw], 66560\n\t" \
  "v_mfma_f32_16x16x32_bf16 v[148:151], v[204:207], v[120:123], v[148:151]\n\t" \
  "v_mfma_f32_16x16x32_bf16 v[156:159], v[204:207], v[124:127], v[156:159]\n\t" \
  "ds_read_b128 v[204:207], v211 offset:16384\n\t" \
  "global_load_lds_dwordx4 v217, s[92:93] sc1\n\t" \
  "s_add_u32 s86, s86, 1\n\t" \
  "s_add_u32 s87, s87, 1\n\t" \
  "s_cmp_lt_u32 s86, %[end]\n\t" \
  "s_cbranch_scc1 .Lp1_%=\n\t" \
  "s_branch .Lx1_%=\n\t" \
  ".Lf01_%=:\n\t" \
  "s_nop 7\n\t" \
  "ds_write_b128 v220, v[152:155] offset:0\n\t" \
  "ds_write_b128 v220, v[156:159] offset:1024\n\t" \
  "s_lshr_b32 s90, s87, 7\n\t" \
  "s_xor_b32 s90, s90, %[s0]\n\t" \
  "s_and_b32 s90, s90, 1\n\t" \
  "s_lshl1_add_u32 s90, s87, s90\n\t" \
  "s_lshl_b32 s90, s90, 13\n\t" \
  "s_cmp_lt_i32 s86, 1\n\t" \
  "s_cselect_b32 s90, 0x70000000, s90\n\t" \
  "s_waitcnt lgkmcnt(0)\n\t" \
  "s_barrier\n\t" \
  "ds_read_b128 v[160:163], v221 offset:0\n\t" \
  "ds_read_b128 v[164:167], v221 offset:1024\n\t" \
  "s_waitcnt lgkmcnt(0)\n\t" \
  "v_add_f32 v160, v160, v144\n\t" \
  "v_add_f32 v161, v161, v145\n\t" \
  "v_add_f32 v162, v162, v146\n\t" \
  "v_add_f32 v163, v163, v147\n\t" \
  "v_add_f32 v164, v164, v148\n\t" \
  "v_add_f32 v165, v165, v149\n\t" \
  "v_add_f32 v166, v166, v150\n\t" \
  "v_add_f32 v167, v167, v151\n\t" \
  "v_cvt_pk_bf16_f32 v168, v160, v161\n\t" \
  "v_cvt_pk_bf16_f32 v169, v162, v163\n\t" \
  "v_cvt_pk_bf16_f32 v170, v164, v165\n\t" \
  "v_cvt_pk_bf16_f32 v171, v166, v167\n\t" \
  "buffer_store_dwordx4 v[168:171], v172, %[rt], s90 offen sc1\n\t" \
  "s_branch .Lfb01_%=\n\t" \
  ".Lf11_%=:\n\t" \
  "s_nop 7\n\t" \
  "ds_write_b128 v220, v[136:139] offset:0\n\t" \
  "ds_write_b128 v220, v[140:143] offset:1024\n\t" \
  "s_lshr_b32 s90, s87, 7\n\t" \
  "s_xor_b32 s90, s90, %[s0]\n\t" \
  "s_and_b32 s90, s90, 1\n\t" \
  "s_lshl1_add_u32 s90, s87, s90\n\t" \
  "s_lshl_b32 s90, s90, 13\n\t" \
  "s_cmp_lt_i32 s86, 1\n\t" \
  "s_cselect_b32 s90, 0x70000000, s90\n\t" \
  "s_waitcnt lgkmcnt(0)\n\t" \
  "s_barrier\n\t" \
  "ds_read_b128 v[160:163], v221 offset:0\n\t" \
  "ds_read_b128 v[164:167], v221 offset:1024\n\t" \
  "s_waitcnt lgkmcnt(0)\n\t" \
  "v_add_f32 v160, v160, v128\n\t" \
  "v_add_f32 v161, v161, v129\n\t" \
  "v_add_f32 v162, v162, v130\n\t" \
  "v_add_f32 v163, v163, v131\n\t" \
  "v_add_f32 v164, v164, v132\n\t" \
  "v_add_f32 v165, v165, v133\n\t" \
  "v_add_f32 v166, v166, v134\n\t" \
  "v_add_f32 v167, v167, v135\n\t" \
  "v_cvt_pk_bf16_f32 v168, v160, v161\n\t" \
  "v_cvt_pk_bf16_f32 v169, v162, v163\n\t" \
  "v_cvt_pk_bf16_f32 v170, v164, v165\n\t" \
  "v_cvt_pk_bf16_f32 v171, v166, v167\n\t" \
  "buffer_store_dwordx4 v[168:171], v172, %[rt], s90 offen sc1\n\t" \
  "s_branch .Lfb11_%=\n\t" \
  ".Lw01_%=:\n\t" \
  "s_waitcnt vmcnt(2)\n\t" \
  "s_barrier\n\t" \
  "s_sub_i32 s91, s86, 1\n\t" \
  "s_max_i32 s88, s88, s91\n\t" \
  "v_mov_b32 v173, s88\n\t" \
  "buffer_store_dword v173, v174, %[rs], 0 offen sc1\n\t" \
  "s_branch .Lg01_%=\n\t" \
  ".Lw11_%=:\n\t" \
  "s_waitcnt vmcnt(2)\n\t" \
  "s_barrier\n\t" \
  "s_sub_i32 s91, s86, 1\n\t" \
  "s_max_i32 s88, s88, s91\n\t" \
  "v_mov_b32 v173, s88\n\t" \
  "buffer_store_dword v173, v174, %[rs], 0 offen sc1\n\t" \
  "s_branch .Lg11_%=\n\t" \
  ".Lw21_%=:\n\t" \
  "s_waitcnt vmcnt(2)\n\t" \
  "s_barrier\n\t" \
  "s_sub_i32 s91, s86, 1\n\t" \
  "s_max_i32 s88, s88, s91\n\t" \
  "v_mov_b32 v173, s88\n\t" \
  "buffer_store_dword v173, v174, %[rs], 0 offen sc1\n\t" \
  "s_branch .Lg21_%=\n\t" \
  ".Lw31_%=:\n\t" \
  "s_waitcnt vmcnt(2)\n\t" \
  "s_barrier\n\t" \
  "s_sub_i32 s91, s86, 1\n\t" \
  "s_max_i32 s88, s88, s91\n\t" \
  "v_mov_b32 v173, s88\n\t" \
  "buffer_store_dword v173, v174, %[rs], 0 offen sc1\n\t" \
  "s_branch .Lg31_%=\n\t" \
  ".Lx1_%=:\n\t" \
  "s_waitcnt lgkmcnt(0)\n\t" \
  "s_setprio 0\n\t" \
  "s_mov_b32 m0, s94\n\t" \
  ".Le_%=:\n\t"
#define PP_ASM_0 \
  "s_cmp_lg_u32 %[kh], 0\n\t" \
  "s_cbranch_scc1 .Lk_%=\n\t" \
  "s_mov_b32 s94, m0\n\t" \
  "s_mov_b64 s[92:93], %[x0]\n\t" \
  "s_mov_b32 s86, %[g0]\n\t" \
  "s_add_u32 s87, s86, %[t0]\n\t" \
  "s_add_u32 m0, %[lw], 67584\n\t" \
  "s_nop 0\n\t" \
  "global_load_lds_dwordx4 v250, s[92:93] sc1\n\t" \
  "s_add_u32 m0, %[lw], 68608\n\t" \
  "s_nop 0\n\t" \
  "global_load_lds_dwordx4 v251, s[92:93] sc1\n\t" \
  "s_barrier\n\t" \
  "s_branch .Ls0_%=\n\t" \
  ".Lp0_%=:\n\t" \
  "s_waitcnt vmcnt(9)\n\t" \
  "s_barrier\n\t" \
  ".Ls0_%=:\n\t" \
  "s_add_u32 s92, s92, 512\n\t" \
  "s_addc_u32 s93, s93, 0\n\t" \
  "ds_read_b128 v[176:179], v240 offset:0\n\t" \
  "ds_read_b128 v[180:183], v240 offset:16384\n\t" \
  "ds_read_b128 v[184:187], v241 offset:0\n\t" \
  "ds_read_b128 v[188:191], v241 offset:16384\n\t" \
  "s_add_u32 m0, %[lw], 98304\n\t" \
  "s_nop 0\n\t" \
  "global_load_lds_dwordx4 v248, s[92:93] sc1\n\t" \
  "ds_read_b128 v[192:195], v242 offset:0\n\t" \
  "ds_read_b128 v[196:199], v242 offset:16384\n\t" \
  "ds_read_b128 v[200:203], v243 offset:0\n\t" \
  "ds_read_b128 v[204:207], v243 offset:16384\n\t" \
  "s_add_u32 m0, %[lw], 99328\n\t" \
  "s_nop 0\n\t" \
  "global_load_lds_dwordx4 v249, s[92:93] sc1\n\t" \
  "ds_read_b128 v[208:211], v240 offset:256\n\t" \
  "ds_read_b128 v[212:215], v240 offset:16640\n\t" \
  "ds_read_b128 v[216:219], v241 offset:256\n\t" \
  "ds_read_b128 v[220:223], v241 offset:16640\n\t" \
  "s_add_u32 m0, %[lw], 100352\n\t" \
  "s_nop 0\n\t" \
  "global_load_lds_dwordx4 v250, s[92:93] sc1\n\t" \
  "ds_read_b128 v[224:227], v242 offset:256\n\t" \
  "ds_read_b128 v[228:231], v242 offset:16640\n\t" \
  "ds_read_b128 v[232:235], v243 offset:256\n\t" \
  "ds_read_b128 v[236:239], v243 offset:16640\n\t" \
  "s_add_u32 m0, %[lw], 101376\n\t" \
  "s_nop 0\n\t" \
  "global_load_lds_dwordx4 v251, s[92:93] sc1\n\t" \
  "ds_write_b128 v252, v[152:155] offset:0\n\t" \
  "ds_write_b128 v252, v[156:159] offset:1024\n\t" \
  "s_waitcnt lgkmcnt(0)\n\t" \
  "s_setprio 1\n\t" \
  "v_mfma_f32_16x16x32_bf16 v[128:131], v[0:3], v[176:179], 0\n\t" \
  "v_mfma_f32_16x16x32_bf16 v[132:135], v[4:7], v[176:179], 0\n\t" \
  "v_mfma_f32_16x16x32_bf16 v[136:139], v[0:3], v[180:183], 0\n\t" \
  "v_mfma_f32_16x16x32_bf16 v[140:143], v[4:7], v[180:183], 0\n\t" \
  "v_mfma_f32_16x16x32_bf16 v[128:131], v[8:11], v[184:187], v[128:131]\n\t" \
  "v_mfma_f32_16x16x32_bf16 v[132:135], v[12:15], v[184:187], v[132:135]\n\t" \
  "v_mfma_f32_16x16x32_bf16 v[136:139], v[8:11], v[188:191], v[136:139]\n\t" \
  "v_mfma_f32_16x16x32_bf16 v[140:143], v[12:15], v[188:191], v[140:143]\n\t" \
  "v_mfma_f32_16x16x32_bf16 v[128:131], v[16:19], v[192:195], v[128:131]\n\t" \
  "v_mfma_f32_16x16x32_bf16 v[132:135], v[20:23], v[192:195], v[132:135]\n\t" \
  "v_mfma_f32_16x16x32_bf16 v[136:139], v[16:19], v[196:199], v[136:139]\n\t" \
  "v_mfma_f32_16x16x32_bf16 v[140:143], v[20:23], v[196:199], v[140:143]\n\t" \
  "v_mfma_f32_16x16x32_bf16 v[128:131], v[24:27], v[200:203], v[128:131]\n\t" \
  "v_mfma_f32_16x16x32_bf16 v[132:135], v[28:31], v[200:203], v[132:135]\n\t" \
  "v_mfma_f32_16x16x32_bf16 v[136:139], v[24:27], v[204:207], v[136:139]\n\t" \
  "v_mfma_f32_16x16x32_bf16 v[140:143], v[28:31], v[204:207], v[140:143]\n\t" \
  "v_mfma_f32_16x16x32_bf16 v[128:131], v[32:35], v[208:211], v[128:131]\n\t" \
  "v_mfma_f32_16x16x32_bf16 v[132:135], v[36:39], v[208:211], v[132:135]\n\t" \
  "v_mfma_f32_16x16x32_bf16 v[136:139], v[32:35], v[212:215], v[136:139]\n\t" \
  "v_mfma_f32_16x16x32_bf16 v[140:143], v[36:39], v[212:215], v[140:143]\n\t" \
  "v_mfma_f32_16x16x32_bf16 v[128:131], v[40:43], v[216:219], v[128:131]\n\t" \
  "v_mfma_f32_16x16x32_bf16 v[132:135], v[44:47], v[216:219], v[132:135]\n\t" \
  "v_mfma_f32_16x16x32_bf16 v[136:139], v[40:43], v[220:223], v[136:139]\n\t" \
  "v_mfma_f32_16x16x32_bf16 v[140:143], v[44:47], v[220:223], v[140:143]\n\t" \
  "v_mfma_f32_16x16x32_bf16 v[128:131], v[48:51], v[224:227], v[128:131]\n\t" \
  "v_mfma_f32_16x16x32_bf16 v[132:135], v[52:55], v[224:227], v[132:135]\n\t" \
  "v_mfma_f32_16x16x32_bf16 v[136:139], v[48:51], v[228:231], v[136:139]\n\t" \
  "v_mfma_f32_16x16x32_bf16 v[140:143], v[52:55], v[228:231], v[140:143]\n\t" \
  "v_mfma_f32_16x16x32_bf16 v[128:131], v[56:59], v[232:235], v[128:131]\n\t" \
  "v_mfma_f32_16x16x32_bf16 v[132:135], v[60:63], v[232:235], v[132:135]\n\t" \
  "v_mfma_f32_16x16x32_bf16 v[136:139], v[56:59], v[236:239], v[136:139]\n\t" \
  "v_mfma_f32_16x16x32_bf16 v[140:143], v[60:63], v[236:239], v[140:143]\n\t" \
  "s_setprio 0\n\t" \
  "s_waitcnt vmcnt(4)\n\t" \
  "s_barrier\n\t" \
  "s_add_u32 s92, s92, 65024\n\t" \
  "s_addc_u32 s93, s93, 0\n\t" \
  "ds_read_b128 v[160:163], v253 offset:0\n\t" \
  "ds_read_b128 v[164:167], v253 offset:1024\n\t" \
  "ds_read_b128 v[176:179], v240 offset:32768\n\t" \
  "ds_read_b128 v[180:183], v240 offset:49152\n\t" \
  "ds_read_b128 v[184:187], v241 offset:32768\n\t" \
  "ds_read_b128 v[188:191], v241 offset:49152\n\t" \
  "s_add_u32 m0, %[lw], 0\n\t" \
  "s_nop 0\n\t" \
  "global_load_lds_dwordx4 v248, s[92:93] sc1\n\t" \
  "ds_read_b128 v[192:195], v242 offset:32768\n\t" \
  "ds_read_b128 v[196:199], v242 offset:49152\n\t" \
  "ds_read_b128 v[200:203], v243 offset:32768\n\t" \
  "ds_read_b128 v[204:207], v243 offset:49152\n\t" \
  "s_add_u32 m0, %[lw], 1024\n\t" \
  "s_nop 0\n\t" \
  "global_load_lds_dwordx4 v249, s[92:93] sc1\n\t" \
  "ds_read_b128 v[208:211], v240 offset:33024\n\t" \
  "ds_read_b128 v[212:215], v240 offset:49408\n\t" \
  "ds_read_b128 v[216:219], v241 offset:33024\n\t" \
  "ds_read_b128 v[220:223], v241 offset:49408\n\t" \
  "s_add_u32 m0, %[lw], 2048\n\t" \
  "s_nop 0\n\t" \
  "global_load_lds_dwordx4 v250, s[92:93] sc1\n\t" \
  "ds_read_b128 v[224:227], v242 offset:33024\n\t" \
  "ds_read_b128 v[228:231], v242 offset:49408\n\t" \
  "ds_read_b128 v[232:235], v243 offset:33024\n\t" \
  "ds_read_b128 v[236:239], v243 offset:49408\n\t" \
  "s_add_u32 m0, %[lw], 3072\n\t" \
  "s_nop 0\n\t" \
  "global_load_lds_dwordx4 v251, s[92:93] sc1\n\t" \
  "s_waitcnt lgkmcnt(15)\n\t" \
  "s_waitcnt lgkmcnt(12)\n\t" \
  "s_lshr_b32 s90, s87, 7\n\t" \
  "s_xor_b32 s90, s90, %[s0]\n\t" \
  "s_and_b32 s90, s90, 1\n\t" \
  "s_lshl1_add_u32 s90, s87, s90\n\t" \
  "s_lshl_b32 s90, s90, 13\n\t" \
  "v_add_f32 v160, v160, v144\n\t" \
  "v_add_f32 v161, v161, v145\n\t" \
  "v_add_f32 v162, v162, v146\n\t" \
  "v_add_f32 v163, v163, v147\n\t" \
  "v_add_f32 v164, v164, v148\n\t" \
  "v_add_f32 v165, v165, v149\n\t" \
  "v_add_f32 v166, v166, v150\n\t" \
  "v_add_f32 v167, v167, v151\n\t" \
  "v_cvt_pk_bf16_f32 v168, v160, v161\n\t" \
  "v_cvt_pk_bf16_f32 v169, v162, v163\n\t" \
  "v_cvt_pk_bf16_f32 v170, v164, v165\n\t" \
  "v_cvt_pk_bf16_f32 v171, v166, v167\n\t" \
  "buffer_store_dwordx4 v[168:171], v172, %[rt], s90 offen sc1\n\t" \
  "s_add_u32 s86, s86, 1\n\t" \
  "s_add_u32 s87, s87, 1\n\t" \
  "s_waitcnt lgkmcnt(0)\n\t" \
  "s_setprio 1\n\t" \
  "v_mfma_f32_16x16x32_bf16 v[128:131], v[64:67], v[176:179], v[128:131]\n\t" \
  "v_mfma_f32_16x16x32_bf16 v[132:135], v[68:71], v[176:179], v[132:135]\n\t" \
  "v_mfma_f32_16x16x32_bf16 v[136:139], v[64:67], v[180:183], v[136:139]\n\t" \
  "v_mfma_f32_16x16x32_bf16 v[140:143], v[68:71], v[180:183], v[140:143]\n\t" \
  "v_mfma_f32_16x16x32_bf16 v[128:131], v[72:75], v[184:187], v[128:131]\n\t" \
  "v_mfma_f32_16x16x32_bf16 v[132:135], v[76:79], v[184:187], v[132:135]\n\t" \
  "v_mfma_f32_16x16x32_bf16 v[136:139], v[72:75], v[188:191], v[136:139]\n\t" \
  "v_mfma_f32_16x16x32_bf16 v[140:143], v[76:79], v[188:191], v[140:143]\n\t" \
  "v_mfma_f32_16x16x32_bf16 v[128:131], v[80:83], v[192:195], v[128:131]\n\t" \
  "v_mfma_f32_16x16x32_bf16 v[132:135], v[84:87], v[192:195], v[132:135]\n\t" \
  "v_mfma_f32_16x16x32_bf16 v[136:139], v[80:83], v[196:199], v[136:139]\n\t" \
  "v_mfma_f32_16x16x32_bf16 v[140:143], v[84:87], v[196:199], v[140:143]\n\t" \
  "v_mfma_f32_16x16x32_bf16 v[128:131], v[88:91], v[200:203], v[128:131]\n\t" \
  "v_mfma_f32_16x16x32_bf16 v[132:135], v[92:95], v[200:203], v[132:135]\n\t" \
  "v_mfma_f32_16x16x32_bf16 v[136:139], v[88:91], v[204:207], v[136:139]\n\t" \
  "v_mfma_f32_16x16x32_bf16 v[140:143], v[92:95], v[204:207], v[140:143]\n\t" \
  "v_mfma_f32_16x16x32_bf16 v[128:131], v[96:99], v[208:211], v[128:131]\n\t" \
  "v_mfma_f32_16x16x32_bf16 v[132:135], v[100:103], v[208:211], v[132:135]\n\t" \
  "v_mfma_f32_16x16x32_bf16 v[136:139], v[96:99], v[212:215], v[136:139]\n\t" \
  "v_mfma_f32_16x16x32_bf16 v[140:143], v[100:103], v[212:215], v[140:143]\n\t" \
  "v_mfma_f32_16x16x32_bf16 v[128:131], v[104:107], v[216:219], v[128:131]\n\t" \
  "v_mfma_f32_16x16x32_bf16 v[132:135], v[108:111], v[216:219], v[132:135]\n\t" \
  "v_mfma_f32_16x16x32_bf16 v[136:139], v[104:107], v[220:223], v[136:139]\n\t" \
  "v_mfma_f32_16x16x32_bf16 v[140:143], v[108:111], v[220:223], v[140:143]\n\t" \
  "v_mfma_f32_16x16x32_bf16 v[128:131], v[112:115], v[224:227], v[128:131]\n\t" \
  "v_mfma_f32_16x16x32_bf16 v[132:135], v[116:119], v[224:227], v[132:135]\n\t" \
  "v_mfma_f32_16x16x32_bf16 v[136:139], v[112:115], v[228:231], v[136:139]\n\t" \
  "v_mfma_f32_16x16x32_bf16 v[140:143], v[116:119], v[228:231], v[140:143]\n\t" \
  "v_mfma_f32_16x16x32_bf16 v[128:131], v[120:123], v[232:235], v[128:131]\n\t" \
  "v_mfma_f32_16x16x32_bf16 v[132:135], v[124:127], v[232:235], v[132:135]\n\t" \
  "v_mfma_f32_16x16x32_bf16 v[136:139], v[120:123], v[236:239], v[136:139]\n\t" \
  "v_mfma_f32_16x16x32_bf16 v[140:143], v[124:127], v[236:239], v[140:143]\n\t" \
  "s_setprio 0\n\t" \
  "s_waitcnt vmcnt(9)\n\t" \
  "s_barrier\n\t" \
  "s_add_u32 s92, s92, 512\n\t" \
  "s_addc_u32 s93, s93, 0\n\t" \
  "ds_read_b128 v[176:179], v244 offset:0\n\t" \
  "ds_read_b128 v[180:183], v244 offset:16384\n\t" \
  "ds_read_b128 v[184:187], v245 offset:0\n\t" \
  "ds_read_b128 v[188:191], v245 offset:16384\n\t" \
  "s_add_u32 m0, %[lw], 32768\n\t" \
  "s_nop 0\n\t" \
  "global_load_lds_dwordx4 v248, s[92:93] sc1\n\t" \
  "ds_read_b128 v[192:195], v246 offset:0\n\t" \
  "ds_read_b128 v[196:199], v246 offset:16384\n\t" \
  "ds_read_b128 v[200:203], v247 offset:0\n\t" \
  "ds_read_b128 v[204:207], v247 offset:16384\n\t" \
  "s_add_u32 m0, %[lw], 33792\n\t" \
  "s_nop 0\n\t" \
  "global_load_lds_dwordx4 v249, s[92:93] sc1\n\t" \
  "ds_read_b128 v[208:211], v244 offset:256\n\t" \
  "ds_read_b128 v[212:215], v244 offset:16640\n\t" \
  "ds_read_b128 v[216:219], v245 offset:256\n\t" \
  "ds_read_b128 v[220:223], v245 offset:16640\n\t" \
  "s_add_u32 m0, %[lw], 34816\n\t" \
  "s_nop 0\n\t" \
  "global_load_lds_dwordx4 v250, s[92:93] sc1\n\t" \
  "ds_read_b128 v[224:227], v246 offset:256\n\t" \
  "ds_read_b128 v[228:231], v246 offset:16640\n\t" \
  "ds_read_b128 v[232:235], v247 offset:256\n\t" \
  "ds_read_b128 v[236:239], v247 offset:16640\n\t" \
  "s_add_u32 m0, %[lw], 35840\n\t" \
  "s_nop 0\n\t" \
  "global_load_lds_dwordx4 v251, s[92:93] sc1\n\t" \
  "ds_write_b128 v252, v[136:139] offset:0\n\t" \
  "ds_write_b128 v252, v[140:143] offset:1024\n\t" \
  "s_waitcnt lgkmcnt(0)\n\t" \
  "s_setprio 1\n\t" \
  "v_mfma_f32_16x16x32_bf16 v[144:147], v[0:3], v[176:179], 0\n\t" \
  "v_mfma_f32_16x16x32_bf16 v[148:151], v[4:7], v[176:179], 0\n\t" \
  "v_mfma_f32_16x16x32_bf16 v[152:155], v[0:3], v[180:183], 0\n\t" \
  "v_mfma_f32_16x16x32_bf16 v[156:159], v[4:7], v[180:183], 0\n\t" \
  "v_mfma_f32_16x16x32_bf16 v[144:147], v[8:11], v[184:187], v[144:147]\n\t" \
  "v_mfma_f32_16x16x32_bf16 v[148:151], v[12:15], v[184:187], v[148:151]\n\t" \
  "v_mfma_f32_16x16x32_bf16 v[152:155], v[8:11], v[188:191], v[152:155]\n\t" \
  "v_mfma_f32_16x16x32_bf16 v[156:159], v[12:15], v[188:191], v[156:159]\n\t" \
  "v_mfma_f32_16x16x32_bf16 v[144:147], v[16:19], v[192:195], v[144:147]\n\t" \
  "v_mfma_f32_16x16x32_bf16 v[148:151], v[20:23], v[192:195], v[148:151]\n\t" \
  "v_mfma_f32_16x16x32_bf16 v[152:155], v[16:19], v[196:199], v[152:155]\n\t" \
  "v_mfma_f32_16x16x32_bf16 v[156:159], v[20:23], v[196:199], v[156:159]\n\t" \
  "v_mfma_f32_16x16x32_bf16 v[144:147], v[24:27], v[200:203], v[144:147]\n\t" \
  "v_mfma_f32_16x16x32_bf16 v[148:151], v[28:31], v[200:203], v[148:151]\n\t" \
  "v_mfma_f32_16x16x32_bf16 v[152:155], v[24:27], v[204:207], v[152:155]\n\t" \
  "v_mfma_f32_16x16x32_bf16 v[156:159], v[28:31], v[204:207], v[156:159]\n\t" \
  "v_mfma_f32_16x16x32_bf16 v[144:147], v[32:35], v[208:211], v[144:147]\n\t" \
  "v_mfma_f32_16x16x32_bf16 v[148:151], v[36:39], v[208:211], v[148:151]\n\t" \
  "v_mfma_f32_16x16x32_bf16 v[152:155], v[32:35], v[212:215], v[152:155]\n\t" \
  "v_mfma_f32_16x16x32_bf16 v[156:159], v[36:39], v[212:215], v[156:159]\n\t" \
  "v_mfma_f32_16x16x32_bf16 v[144:147], v[40:43], v[216:219], v[144:147]\n\t" \
  "v_mfma_f32_16x16x32_bf16 v[148:151], v[44:47], v[216:219], v[148:151]\n\t" \
  "v_mfma_f32_16x16x32_bf16 v[152:155], v[40:43], v[220:223], v[152:155]\n\t" \
  "v_mfma_f32_16x16x32_bf16 v[156:159], v[44:47], v[220:223], v[156:159]\n\t" \
  "v_mfma_f32_16x16x32_bf16 v[144:147], v[48:51], v[224:227], v[144:147]\n\t" \
  "v_mfma_f32_16x16x32_bf16 v[148:151], v[52:55], v[224:227], v[148:151]\n\t" \
  "v_mfma_f32_16x16x32_bf16 v[152:155], v[48:51], v[228:231], v[152:155]\n\t" \
  "v_mfma_f32_16x16x32_bf16 v[156:159], v[52:55], v[228:231], v[156:159]\n\t" \
  "v_mfma_f32_16x16x32_bf16 v[144:147], v[56:59], v[232:235], v[144:147]\n\t" \
  "v_mfma_f32_16x16x32_bf16 v[148:151], v[60:63], v[232:235], v[148:151]\n\t" \
  "v_mfma_f32_16x16x32_bf16 v[152:155], v[56:59], v[236:239], v[152:155]\n\t" \
  "v_mfma_f32_16x16x32_bf16 v[156:159], v[60:63], v[236:239], v[156:159]\n\t" \
  "s_setprio 0\n\t" \
  "s_waitcnt vmcnt(4)\n\t" \
  "s_barrier\n\t" \
  "s_add_u32 s92, s92, 65024\n\t" \
  "s_addc_u32 s93, s93, 0\n\t" \
  "ds_read_b128 v[160:163], v253 offset:0\n\t" \
  "ds_read_b128 v[164:167], v253 offset:1024\n\t" \
  "ds_read_b128 v[176:179], v244 offset:32768\n\t" \
  "ds_read_b128 v[180:183], v244 offset:49152\n\t" \
  "ds_read_b128 v[184:187], v245 offset:32768\n\t" \
  "ds_read_b128 v[188:191], v245 offset:49152\n\t" \
  "s_add_u32 m0, %[lw], 65536\n\t" \
  "s_nop 0\n\t" \
  "global_load_lds_dwordx4 v248, s[92:93] sc1\n\t" \
  "ds_read_b128 v[192:195], v246 offset:32768\n\t" \
  "ds_read_b128 v[196:199], v246 offset:49152\n\t" \
  "ds_read_b128 v[200:203], v247 offset:32768\n\t" \
  "ds_read_b128 v[204:207], v247 offset:49152\n\t" \
  "s_add_u32 m0, %[lw], 66560\n\t" \
  "s_nop 0\n\t" \
  "global_load_lds_dwordx4 v249, s[92:93] sc1\n\t" \
  "ds_read_b128 v[208:211], v244 offset:33024\n\t" \
  "ds_read_b128 v[212:215], v244 offset:49408\n\t" \
  "ds_read_b128 v[216:219], v245 offset:33024\n\t" \
  "ds_read_b128 v[220:223], v245 offset:49408\n\t" \
  "s_add_u32 m0, %[lw], 67584\n\t" \
  "s_nop 0\n\t" \
  "global_load_lds_dwordx4 v250, s[92:93] sc1\n\t" \
  "ds_read_b128 v[224:227], v246 offset:33024\n\t" \
  "ds_read_b128 v[228:231], v246 offset:49408\n\t" \
  "ds_read_b128 v[232:235], v247 offset:33024\n\t" \
  "ds_read_b128 v[236:239], v247 offset:49408\n\t" \
  "s_add_u32 m0, %[lw], 68608\n\t" \
  "s_nop 0\n\t" \
  "global_load_lds_dwordx4 v251, s[92:93] sc1\n\t" \
  "s_waitcnt lgkmcnt(15)\n\t" \
  "s_waitcnt lgkmcnt(12)\n\t" \
  "s_lshr_b32 s90, s87, 7\n\t" \
  "s_xor_b32 s90, s90, %[s0]\n\t" \
  "s_and_b32 s90, s90, 1\n\t" \
  "s_lshl1_add_u32 s90, s87, s90\n\t" \
  "s_lshl_b32 s90, s90, 13\n\t" \
  "v_add_f32 v160, v160, v128\n\t" \
  "v_add_f32 v161, v161, v129\n\t" \
  "v_add_f32 v162, v162, v130\n\t" \
  "v_add_f32 v163, v163, v131\n\t" \
  "v_add_f32 v164, v164, v132\n\t" \
  "v_add_f32 v165, v165, v133\n\t" \
  "v_add_f32 v166, v166, v134\n\t" \
  "v_add_f32 v167, v167, v135\n\t" \
  "v_cvt_pk_bf16_f32 v168, v160, v161\n\t" \
  "v_cvt_pk_bf16_f32 v169, v162, v163\n\t" \
  "v_cvt_pk_bf16_f32 v170, v164, v165\n\t" \
  "v_cvt_pk_bf16_f32 v171, v166, v167\n\t" \
  "buffer_store_dwordx4 v[168:171], v172, %[rt], s90 offen sc1\n\t" \
  "s_add_u32 s86, s86, 1\n\t" \
  "s_add_u32 s87, s87, 1\n\t" \
  "s_waitcnt lgkmcnt(0)\n\t" \
  "s_setprio 1\n\t" \
  "v_mfma_f32_16x16x32_bf16 v[144:147], v[64:67], v[176:179], v[144:147]\n\t" \
  "v_mfma_f32_16x16x32_bf16 v[148:151], v[68:71], v[176:179], v[148:151]\n\t" \
  "v_mfma_f32_16x16x32_bf16 v[152:155], v[64:67], v[180:183], v[152:155]\n\t" \
  "v_mfma_f32_16x16x32_bf16 v[156:159], v[68:71], v[180:183], v[156:159]\n\t" \
  "v_mfma_f32_16x16x32_bf16 v[144:147], v[72:75], v[184:187], v[144:147]\n\t" \
  "v_mfma_f32_16x16x32_bf16 v[148:151], v[76:79], v[184:187], v[148:151]\n\t" \
  "v_mfma_f32_16x16x32_bf16 v[152:155], v[72:75], v[188:191], v[152:155]\n\t" \
  "v_mfma_f32_16x16x32_bf16 v[156:159], v[76:79], v[188:191], v[156:159]\n\t" \
  "v_mfma_f32_16x16x32_bf16 v[144:147], v[80:83], v[192:195], v[144:147]\n\t" \
  "v_mfma_f32_16x16x32_bf16 v[148:151], v[84:87], v[192:195], v[148:151]\n\t" \
  "v_mfma_f32_16x16x32_bf16 v[152:155], v[80:83], v[196:199], v[152:155]\n\t" \
  "v_mfma_f32_16x16x32_bf16 v[156:159], v[84:87], v[196:199], v[156:159]\n\t" \
  "v_mfma_f32_16x16x32_bf16 v[144:147], v[88:91], v[200:203], v[144:147]\n\t" \
  "v_mfma_f32_16x16x32_bf16 v[148:151], v[92:95], v[200:203], v[148:151]\n\t" \
  "v_mfma_f32_16x16x32_bf16 v[152:155], v[88:91], v[204:207], v[152:155]\n\t" \
  "v_mfma_f32_16x16x32_bf16 v[156:159], v[92:95], v[204:207], v[156:159]\n\t" \
  "v_mfma_f32_16x16x32_bf16 v[144:147], v[96:99], v[208:211], v[144:147]\n\t" \
  "v_mfma_f32_16x16x32_bf16 v[148:151], v[100:103], v[208:211], v[148:151]\n\t" \
  "v_mfma_f32_16x16x32_bf16 v[152:155], v[96:99], v[212:215], v[152:155]\n\t" \
  "v_mfma_f32_16x16x32_bf16 v[156:159], v[100:103], v[212:215], v[156:159]\n\t" \
  "v_mfma_f32_16x16x32_bf16 v[144:147], v[104:107], v[216:219], v[144:147]\n\t" \
  "v_mfma_f32_16x16x32_bf16 v[148:151], v[108:111], v[216:219], v[148:151]\n\t" \
  "v_mfma_f32_16x16x32_bf16 v[152:155], v[104:107], v[220:223], v[152:155]\n\t" \
  "v_mfma_f32_16x16x32_bf16 v[156:159], v[108:111], v[220:223], v[156:159]\n\t" \
  "v_mfma_f32_16x16x32_bf16 v[144:147], v[112:115], v[224:227], v[144:147]\n\t" \
  "v_mfma_f32_16x16x32_bf16 v[148:151], v[116:119], v[224:227], v[148:151]\n\t" \
  "v_mfma_f32_16x16x32_bf16 v[152:155], v[112:115], v[228:231], v[152:155]\n\t" \
  "v_mfma_f32_16x16x32_bf16 v[156:159], v[116:119], v[228:231], v[156:159]\n\t" \
  "v_mfma_f32_16x16x32_bf16 v[144:147], v[120:123], v[232:235], v[144:147]\n\t" \
  "v_mfma_f32_16x16x32_bf16 v[148:151], v[124:127], v[232:235], v[148:151]\n\t" \
  "v_mfma_f32_16x16x32_bf16 v[152:155], v[120:123], v[236:239], v[152:155]\n\t" \
  "v_mfma_f32_16x16x32_bf16 v[156:159], v[124:127], v[236:239], v[156:159]\n\t" \
  "s_setprio 0\n\t" \
  "s_cmp_lt_u32 s86, 64\n\t" \
  "s_cbranch_scc1 .Lp0_%=\n\t" \
  "s_barrier\n\t" \
  "s_nop 2\n\t" \
  "ds_write_b128 v252, v[152:155] offset:0\n\t" \
  "ds_write_b128 v252, v[156:159] offset:1024\n\t" \
  "s_waitcnt lgkmcnt(0)\n\t" \
  "s_barrier\n\t" \
  "ds_read_b128 v[160:163], v253 offset:0\n\t" \
  "ds_read_b128 v[164:167], v253 offset:1024\n\t" \
  "s_waitcnt lgkmcnt(0)\n\t" \
  "s_lshr_b32 s90, s87, 7\n\t" \
  "s_xor_b32 s90, s90, %[s0]\n\t" \
  "s_and_b32 s90, s90, 1\n\t" \
  "s_lshl1_add_u32 s90, s87, s90\n\t" \
  "s_lshl_b32 s90, s90, 13\n\t" \
  "v_add_f32 v160, v160, v144\n\t" \
  "v_add_f32 v161, v161, v145\n\t" \
  "v_add_f32 v162, v162, v146\n\t" \
  "v_add_f32 v163, v163, v147\n\t" \
  "v_add_f32 v164, v164, v148\n\t" \
  "v_add_f32 v165, v165, v149\n\t" \
  "v_add_f32 v166, v166, v150\n\t" \
  "v_add_f32 v167, v167, v151\n\t" \
  "v_cvt_pk_bf16_f32 v168, v160, v161\n\t" \
  "v_cvt_pk_bf16_f32 v169, v162, v163\n\t" \
  "v_cvt_pk_bf16_f32 v170, v164, v165\n\t" \
  "v_cvt_pk_bf16_f32 v171, v166, v167\n\t" \
  "buffer_store_dwordx4 v[168:171], v172, %[rt], s90 offen sc1\n\t" \
  "s_waitcnt vmcnt(0)\n\t" \
  "s_barrier\n\t" \
  "s_mov_b32 m0, s94\n\t" \
  "s_branch .Le_%=\n\t" \
  ".Lk_%=:\n\t" \
  "s_mov_b32 s94, m0\n\t" \
  "s_mov_b64 s[92:93], %[x0]\n\t" \
  "s_mov_b32 s86, %[g0]\n\t" \
  "s_add_u32 s87, s86, %[t0]\n\t" \
  "s_add_u32 m0, %[lw], 67584\n\t" \
  "s_nop 0\n\t" \
  "global_load_lds_dwordx4 v250, s[92:93] sc1\n\t" \
  "s_add_u32 m0, %[lw], 68608\n\t" \
  "s_nop 0\n\t" \
  "global_load_lds_dwordx4 v251, s[92:93] sc1\n\t" \
  "s_barrier\n\t" \
  "s_branch .Ls1_%=\n\t" \
  ".Lp1_%=:\n\t" \
  "s_waitcnt vmcnt(10)\n\t" \
  "s_barrier\n\t" \
  "s_setprio 2\n\t" \
  "v_mfma_f32_16x16x32_bf16 v[144:147], v[64:67], v[176:179], v[144:147]\n\t" \
  "v_mfma_f32_16x16x32_bf16 v[148:151], v[68:71], v[176:179], v[148:151]\n\t" \
  "v_mfma_f32_16x16x32_bf16 v[152:155], v[64:67], v[180:183], v[152:155]\n\t" \
  "v_mfma_f32_16x16x32_bf16 v[156:159], v[68:71], v[180:183], v[156:159]\n\t" \
  "v_mfma_f32_16x16x32_bf16 v[144:147], v[72:75], v[184:187], v[144:147]\n\t" \
  "v_mfma_f32_16x16x32_bf16 v[148:151], v[76:79], v[184:187], v[148:151]\n\t" \
  "v_mfma_f32_16x16x32_bf16 v[152:155], v[72:75], v[188:191], v[152:155]\n\t" \
  "v_mfma_f32_16x16x32_bf16 v[156:159], v[76:79], v[188:191], v[156:159]\n\t" \
  "v_mfma_f32_16x16x32_bf16 v[144:147], v[80:83], v[192:195], v[144:147]\n\t" \
  "v_mfma_f32_16x16x32_bf16 v[148:151], v[84:87], v[192:195], v[148:151]\n\t" \
  "v_mfma_f32_16x16x32_bf16 v[152:155], v[80:83], v[196:199], v[152:155]\n\t" \
  "v_mfma_f32_16x16x32_bf16 v[156:159], v[84:87], v[196:199], v[156:159]\n\t" \
  "v_mfma_f32_16x16x32_bf16 v[144:147], v[88:91], v[200:203], v[144:147]\n\t" \
  "v_mfma_f32_16x16x32_bf16 v[148:151], v[92:95], v[200:203], v[148:151]\n\t" \
  "v_mfma_f32_16x16x32_bf16 v[152:155], v[88:91], v[204:207], v[152:155]\n\t" \
  "v_mfma_f32_16x16x32_bf16 v[156:159], v[92:95], v[204:207], v[156:159]\n\t" \
  "v_mfma_f32_16x16x32_bf16 v[144:147], v[96:99], v[208:211], v[144:147]\n\t" \
  "v_mfma_f32_16x16x32_bf16 v[148:151], v[100:103], v[208:211], v[148:151]\n\t" \
  "v_mfma_f32_16x16x32_bf16 v[152:155], v[96:99], v[212:215], v[152:155]\n\t" \
  "v_mfma_f32_16x16x32_bf16 v[156:159], v[100:103], v[212:215], v[156:159]\n\t" \
  "v_mfma_f32_16x16x32_bf16 v[144:147], v[104:107], v[216:219], v[144:147]\n\t" \
  "v_mfma_f32_16x16x32_bf16 v[148:151], v[108:111], v[216:219], v[148:151]\n\t" \
  "v_mfma_f32_16x16x32_bf16 v[152:155], v[104:107], v[220:223], v[152:155]\n\t" \
  "v_mfma_f32_16x16x32_bf16 v[156:159], v[108:111], v[220:223], v[156:159]\n\t" \
  "v_mfma_f32_16x16x32_bf16 v[144:147], v[112:115], v[224:227], v[144:147]\n\t" \
  "v_mfma_f32_16x16x32_bf16 v[148:151], v[116:119], v[224:227], v[148:151]\n\t" \
  "v_mfma_f32_16x16x32_bf16 v[152:155], v[112:115], v[228:231], v[152:155]\n\t" \
  "v_mfma_f32_16x16x32_bf16 v[156:159], v[116:119], v[228:231], v[156:159]\n\t" \
  "v_mfma_f32_16x16x32_bf16 v[144:147], v[120:123], v[232:235], v[144:147]\n\t" \
  "v_mfma_f32_16x16x32_bf16 v[148:151], v[124:127], v[232:235], v[148:151]\n\t" \
  "v_mfma_f32_16x16x32_bf16 v[152:155], v[120:123], v[236:239], v[152:155]\n\t" \
  "v_mfma_f32_16x16x32_bf16 v[156:159], v[124:127], v[236:239], v[156:159]\n\t" \
  "s_setprio 0\n\t" \
  ".Ls1_%=:\n\t" \
  "s_add_u32 s92, s92, 512\n\t" \
  "s_addc_u32 s93, s93, 0\n\t" \
  "ds_read_b128 v[176:179], v240 offset:0\n\t" \
  "ds_read_b128 v[180:183], v240 offset:16384\n\t" \
  "ds_read_b128 v[184:187], v241 offset:0\n\t" \
  "ds_read_b128 v[188:191], v241 offset:16384\n\t" \
  "s_add_u32 m0, %[lw], 98304\n\t" \
  "s_nop 0\n\t" \
  "global_load_lds_dwordx4 v248, s[92:93] sc1\n\t" \
  "ds_read_b128 v[192:195], v242 offset:0\n\t" \
  "ds_read_b128 v[196:199], v242 offset:16384\n\t" \
  "ds_read_b128 v[200:203], v243 offset:0\n\t" \
  "ds_read_b128 v[204:207], v243 offset:16384\n\t" \
  "s_add_u32 m0, %[lw], 99328\n\t" \
  "s_nop 0\n\t" \
  "global_load_lds_dwordx4 v249, s[92:93] sc1\n\t" \
  "ds_read_b128 v[208:211], v240 offset:256\n\t" \
  "ds_read_b128 v[212:215], v240 offset:16640\n\t" \
  "ds_read_b128 v[216:219], v241 offset:256\n\t" \
  "ds_read_b128 v[220:223], v241 offset:16640\n\t" \
  "s_add_u32 m0, %[lw], 100352\n\t" \
  "s_nop 0\n\t" \
  "global_load_lds_dwordx4 v250, s[92:93] sc1\n\t" \
  "ds_read_b128 v[224:227], v242 offset:256\n\t" \
  "ds_read_b128 v[228:231], v242 offset:16640\n\t" \
  "ds_read_b128 v[232:235], v243 offset:256\n\t" \
  "ds_read_b128 v[236:239], v243 offset:16640\n\t" \
  "s_add_u32 m0, %[lw], 101376\n\t" \
  "s_nop 0\n\t" \
  "global_load_lds_dwordx4 v251, s[92:93] sc1\n\t" \
  "ds_write_b128 v252, v[152:155] offset:0\n\t" \
  "ds_write_b128 v252, v[156:159] offset:1024\n\t" \
  "s_waitcnt lgkmcnt(0)\n\t" \
  "s_waitcnt vmcnt(4)\n\t" \
  "s_barrier\n\t" \
  "s_setprio 2\n\t" \
  "v_mfma_f32_16x16x32_bf16 v[128:131], v[0:3], v[176:179], 0\n\t" \
  "v_mfma_f32_16x16x32_bf16 v[132:135], v[4:7], v[176:179], 0\n\t" \
  "v_mfma_f32_16x16x32_bf16 v[136:139], v[0:3], v[180:183], 0\n\t" \
  "v_mfma_f32_16x16x32_bf16 v[140:143], v[4:7], v[180:183], 0\n\t" \
  "v_mfma_f32_16x16x32_bf16 v[128:131], v[8:11], v[184:187], v[128:131]\n\t" \
  "v_mfma_f32_16x16x32_bf16 v[132:135], v[12:15], v[184:187], v[132:135]\n\t" \
  "v_mfma_f32_16x16x32_bf16 v[136:139], v[8:11], v[188:191], v[136:139]\n\t" \
  "v_mfma_f32_16x16x32_bf16 v[140:143], v[12:15], v[188:191], v[140:143]\n\t" \
  "v_mfma_f32_16x16x32_bf16 v[128:131], v[16:19], v[192:195], v[128:131]\n\t" \
  "v_mfma_f32_16x16x32_bf16 v[132:135], v[20:23], v[192:195], v[132:135]\n\t" \
  "v_mfma_f32_16x16x32_bf16 v[136:139], v[16:19], v[196:199], v[136:139]\n\t" \
  "v_mfma_f32_16x16x32_bf16 v[140:143], v[20:23], v[196:199], v[140:143]\n\t" \
  "v_mfma_f32_16x16x32_bf16 v[128:131], v[24:27], v[200:203], v[128:131]\n\t" \
  "v_mfma_f32_16x16x32_bf16 v[132:135], v[28:31], v[200:203], v[132:135]\n\t" \
  "v_mfma_f32_16x16x32_bf16 v[136:139], v[24:27], v[204:207], v[136:139]\n\t" \
  "v_mfma_f32_16x16x32_bf16 v[140:143], v[28:31], v[204:207], v[140:143]\n\t" \
  "v_mfma_f32_16x16x32_bf16 v[128:131], v[32:35], v[208:211], v[128:131]\n\t" \
  "v_mfma_f32_16x16x32_bf16 v[132:135], v[36:39], v[208:211], v[132:135]\n\t" \
  "v_mfma_f32_16x16x32_bf16 v[136:139], v[32:35], v[212:215], v[136:139]\n\t" \
  "v_mfma_f32_16x16x32_bf16 v[140:143], v[36:39], v[212:215], v[140:143]\n\t" \
  "v_mfma_f32_16x16x32_bf16 v[128:131], v[40:43], v[216:219], v[128:131]\n\t" \
  "v_mfma_f32_16x16x32_bf16 v[132:135], v[44:47], v[216:219], v[132:135]\n\t" \
  "v_mfma_f32_16x16x32_bf16 v[136:139], v[40:43], v[220:223], v[136:139]\n\t" \
  "v_mfma_f32_16x16x32_bf16 v[140:143], v[44:47], v[220:223], v[140:143]\n\t" \
  "v_mfma_f32_16x16x32_bf16 v[128:131], v[48:51], v[224:227], v[128:131]\n\t" \
  "v_mfma_f32_16x16x32_bf16 v[132:135], v[52:55], v[224:227], v[132:135]\n\t" \
  "v_mfma_f32_16x16x32_bf16 v[136:139], v[48:51], v[228:231], v[136:139]\n\t" \
  "v_mfma_f32_16x16x32_bf16 v[140:143], v[52:55], v[228:231], v[140:143]\n\t" \
  "v_mfma_f32_16x16x32_bf16 v[128:131], v[56:59], v[232:235], v[128:131]\n\t" \
  "v_mfma_f32_16x16x32_bf16 v[132:135], v[60:63], v[232:235], v[132:135]\n\t" \
  "v_mfma_f32_16x16x32_bf16 v[136:139], v[56:59], v[236:239], v[136:139]\n\t" \
  "v_mfma_f32_16x16x32_bf16 v[140:143], v[60:63], v[236:239], v[140:143]\n\t" \
  "s_setprio 0\n\t" \
  "s_add_u32 s92, s92, 65024\n\t" \
  "s_addc_u32 s93, s93, 0\n\t" \
  "ds_read_b128 v[160:163], v253 offset:0\n\t" \
  "ds_read_b128 v[164:167], v253 offset:1024\n\t" \
  "ds_read_b128 v[176:179], v240 offset:32768\n\t" \
  "ds_read_b128 v[180:183], v240 offset:49152\n\t" \
  "ds_read_b128 v[184:187], v241 offset:32768\n\t" \
  "ds_read_b128 v[188:191], v241 offset:49152\n\t" \
  "s_add_u32 m0, %[lw], 0\n\t" \
  "s_nop 0\n\t" \
  "global_load_lds_dwordx4 v248, s[92:93] sc1\n\t" \
  "ds_read_b128 v[192:195], v242 offset:32768\n\t" \
  "ds_read_b128 v[196:199], v242 offset:49152\n\t" \
  "ds_read_b128 v[200:203], v243 offset:32768\n\t" \
  "ds_read_b128 v[204:207], v243 offset:49152\n\t" \
  "s_add_u32 m0, %[lw], 1024\n\t" \
  "s_nop 0\n\t" \
  "global_load_lds_dwordx4 v249, s[92:93] sc1\n\t" \
  "ds_read_b128 v[208:211], v240 offset:33024\n\t" \
  "ds_read_b128 v[212:215], v240 offset:49408\n\t" \
  "ds_read_b128 v[216:219], v241 offset:33024\n\t" \
  "ds_read_b128 v[220:223], v241 offset:49408\n\t" \
  "s_add_u32 m0, %[lw], 2048\n\t" \
  "s_nop 0\n\t" \
  "global_load_lds_dwordx4 v250, s[92:93] sc1\n\t" \
  "ds_read_b128 v[224:227], v242 offset:33024\n\t" \
  "ds_read_b128 v[228:231], v242 offset:49408\n\t" \
  "ds_read_b128 v[232:235], v243 offset:33024\n\t" \
  "ds_read_b128 v[236:239], v243 offset:49408\n\t" \
  "s_add_u32 m0, %[lw], 3072\n\t" \
  "s_nop 0\n\t" \
  "global_load_lds_dwordx4 v251, s[92:93] sc1\n\t" \
  "s_waitcnt lgkmcnt(15)\n\t" \
  "s_waitcnt lgkmcnt(12)\n\t" \
  "s_sub_i32 s91, s86, 2\n\t" \
  "v_mov_b32 v173, s91\n\t" \
  "buffer_store_dword v173, v174, %[rs], 0 offen sc1\n\t" \
  "s_lshr_b32 s90, s87, 7\n\t" \
  "s_xor_b32 s90, s90, %[s0]\n\t" \
  "s_and_b32 s90, s90, 1\n\t" \
  "s_lshl1_add_u32 s90, s87, s90\n\t" \
  "s_lshl_b32 s90, s90, 13\n\t" \
  "v_add_f32 v160, v160, v144\n\t" \
  "v_add_f32 v161, v161, v145\n\t" \
  "v_add_f32 v162, v162, v146\n\t" \
  "v_add_f32 v163, v163, v147\n\t" \
  "v_add_f32 v164, v164, v148\n\t" \
  "v_add_f32 v165, v165, v149\n\t" \
  "v_add_f32 v166, v166, v150\n\t" \
  "v_add_f32 v167, v167, v151\n\t" \
  "v_cvt_pk_bf16_f32 v168, v160, v161\n\t" \
  "v_cvt_pk_bf16_f32 v169, v162, v163\n\t" \
  "v_cvt_pk_bf16_f32 v170, v164, v165\n\t" \
  "v_cvt_pk_bf16_f32 v171, v166, v167\n\t" \
  "buffer_store_dwordx4 v[168:171], v172, %[rt], s90 offen sc1\n\t" \
  "s_add_u32 s86, s86, 1\n\t" \
  "s_add_u32 s87, s87, 1\n\t" \
  "s_waitcnt lgkmcnt(0)\n\t" \
  "s_waitcnt vmcnt(10)\n\t" \
  "s_barrier\n\t" \
  "s_setprio 2\n\t" \
  "v_mfma_f32_16x16x32_bf16 v[128:131], v[64:67], v[176:179], v[128:131]\n\t" \
  "v_mfma_f32_16x16x32_bf16 v[132:135], v[68:71], v[176:179], v[132:135]\n\t" \
  "v_mfma_f32_16x16x32_bf16 v[136:139], v[64:67], v[180:183], v[136:139]\n\t" \
  "v_mfma_f32_16x16x32_bf16 v[140:143], v[68:71], v[180:183], v[140:143]\n\t" \
  "v_mfma_f32_16x16x32_bf16 v[128:131], v[72:75], v[184:187], v[128:131]\n\t" \
  "v_mfma_f32_16x16x32_bf16 v[132:135], v[76:79], v[184:187], v[132:135]\n\t" \
  "v_mfma_f32_16x16x32_bf16 v[136:139], v[72:75], v[188:191], v[136:139]\n\t" \
  "v_mfma_f32_16x16x32_bf16 v[140:143], v[76:79], v[188:191], v[140:143]\n\t" \
  "v_mfma_f32_16x16x32_bf16 v[128:131], v[80:83], v[192:195], v[128:131]\n\t" \
  "v_mfma_f32_16x16x32_bf16 v[132:135], v[84:87], v[192:195], v[132:135]\n\t" \
  "v_mfma_f32_16x16x32_bf16 v[136:139], v[80:83], v[196:199], v[136:139]\n\t" \
  "v_mfma_f32_16x16x32_bf16 v[140:143], v[84:87], v[196:199], v[140:143]\n\t" \
  "v_mfma_f32_16x16x32_bf16 v[128:131], v[88:91], v[200:203], v[128:131]\n\t" \
  "v_mfma_f32_16x16x32_bf16 v[132:135], v[92:95], v[200:203], v[132:135]\n\t" \
  "v_mfma_f32_16x16x32_bf16 v[136:139], v[88:91], v[204:207], v[136:139]\n\t" \
  "v_mfma_f32_16x16x32_bf16 v[140:143], v[92:95], v[204:207], v[140:143]\n\t" \
  "v_mfma_f32_16x16x32_bf16 v[128:131], v[96:99], v[208:211], v[128:131]\n\t" \
  "v_mfma_f32_16x16x32_bf16 v[132:135], v[100:103], v[208:211], v[132:135]\n\t" \
  "v_mfma_f32_16x16x32_bf16 v[136:139], v[96:99], v[212:215], v[136:139]\n\t" \
  "v_mfma_f32_16x16x32_bf16 v[140:143], v[100:103], v[212:215], v[140:143]\n\t" \
  "v_mfma_f32_16x16x32_bf16 v[128:131], v[104:107], v[216:219], v[128:131]\n\t" \
  "v_mfma_f32_16x16x32_bf16 v[132:135], v[108:111], v[216:219], v[132:135]\n\t" \
  "v_mfma_f32_16x16x32_bf16 v[136:139], v[104:107], v[220:223], v[136:139]\n\t" \
  "v_mfma_f32_16x16x32_bf16 v[140:143], v[108:111], v[220:223], v[140:143]\n\t" \
  "v_mfma_f32_16x16x32_bf16 v[128:131], v[112:115], v[224:227], v[128:131]\n\t" \
  "v_mfma_f32_16x16x32_bf16 v[132:135], v[116:119], v[224:227], v[132:135]\n\t" \
  "v_mfma_f32_16x16x32_bf16 v[136:139], v[112:115], v[228:231], v[136:139]\n\t" \
  "v_mfma_f32_16x16x32_bf16 v[140:143], v[116:119], v[228:231], v[140:143]\n\t" \
  "v_mfma_f32_16x16x32_bf16 v[128:131], v[120:123], v[232:235], v[128:131]\n\t" \
  "v_mfma_f32_16x16x32_bf16 v[132:135], v[124:127], v[232:235], v[132:135]\n\t" \
  "v_mfma_f32_16x16x32_bf16 v[136:139], v[120:123], v[236:239], v[136:139]\n\t" \
  "v_mfma_f32_16x16x32_bf16 v[140:143], v[124:127], v[236:239], v[140:143]\n\t" \
  "s_setprio 0\n\t" \
  "s_add_u32 s92, s92, 512\n\t" \
  "s_addc_u32 s93, s93, 0\n\t" \
  "ds_read_b128 v[176:179], v244 offset:0\n\t" \
  "ds_read_b128 v[180:183], v244 offset:16384\n\t" \
  "ds_read_b128 v[184:187], v245 offset:0\n\t" \
  "ds_read_b128 v[188:191], v245 offset:16384\n\t" \
  "s_add_u32 m0, %[lw], 32768\n\t" \
  "s_nop 0\n\t" \
  "global_load_lds_dwordx4 v248, s[92:93] sc1\n\t" \
  "ds_read_b128 v[192:195], v246 offset:0\n\t" \
  "ds_read_b128 v[196:199], v246 offset:16384\n\t" \
  "ds_read_b128 v[200:203], v247 offset:0\n\t" \
  "ds_read_b128 v[204:207], v247 offset:16384\n\t" \
  "s_add_u32 m0, %[lw], 33792\n\t" \
  "s_nop 0\n\t" \
  "global_load_lds_dwordx4 v249, s[92:93] sc1\n\t" \
  "ds_read_b128 v[208:211], v244 offset:256\n\t" \
  "ds_read_b128 v[212:215], v244 offset:16640\n\t" \
  "ds_read_b128 v[216:219], v245 offset:256\n\t" \
  "ds_read_b128 v[220:223], v245 offset:16640\n\t" \
  "s_add_u32 m0, %[lw], 34816\n\t" \
  "s_nop 0\n\t" \
  "global_load_lds_dwordx4 v250, s[92:93] sc1\n\t" \
  "ds_read_b128 v[224:227], v246 offset:256\n\t" \
  "ds_read_b128 v[228:231], v246 offset:16640\n\t" \
  "ds_read_b128 v[232:235], v247 offset:256\n\t" \
  "ds_read_b128 v[236:239], v247 offset:16640\n\t" \
  "s_add_u32 m0, %[lw], 35840\n\t" \
  "s_nop 0\n\t" \
  "global_load_lds_dwordx4 v251, s[92:93] sc1\n\t" \
  "ds_write_b128 v252, v[136:139] offset:0\n\t" \
  "ds_write_b128 v252, v[140:143] offset:1024\n\t" \
  "s_waitcnt lgkmcnt(0)\n\t" \
  "s_waitcnt vmcnt(4)\n\t" \
  "s_barrier\n\t" \
  "s_setprio 2\n\t" \
  "v_mfma_f32_16x16x32_bf16 v[144:147], v[0:3], v[176:179], 0\n\t" \
  "v_mfma_f32_16x16x32_bf16 v[148:151], v[4:7], v[176:179], 0\n\t" \
  "v_mfma_f32_16x16x32_bf16 v[152:155], v[0:3], v[180:183], 0\n\t" \
  "v_mfma_f32_16x16x32_bf16 v[156:159], v[4:7], v[180:183], 0\n\t" \
  "v_mfma_f32_16x16x32_bf16 v[144:147], v[8:11], v[184:187], v[144:147]\n\t" \
  "v_mfma_f32_16x16x32_bf16 v[148:151], v[12:15], v[184:187], v[148:151]\n\t" \
  "v_mfma_f32_16x16x32_bf16 v[152:155], v[8:11], v[188:191], v[152:155]\n\t" \
  "v_mfma_f32_16x16x32_bf16 v[156:159], v[12:15], v[188:191], v[156:159]\n\t" \
  "v_mfma_f32_16x16x32_bf16 v[144:147], v[16:19], v[192:195], v[144:147]\n\t" \
  "v_mfma_f32_16x16x32_bf16 v[148:151], v[20:23], v[192:195], v[148:151]\n\t" \
  "v_mfma_f32_16x16x32_bf16 v[152:155], v[16:19], v[196:199], v[152:155]\n\t" \
  "v_mfma_f32_16x16x32_bf16 v[156:159], v[20:23], v[196:199], v[156:159]\n\t" \
  "v_mfma_f32_16x16x32_bf16 v[144:147], v[24:27], v[200:203], v[144:147]\n\t" \
  "v_mfma_f32_16x16x32_bf16 v[148:151], v[28:31], v[200:203], v[148:151]\n\t" \
  "v_mfma_f32_16x16x32_bf16 v[152:155], v[24:27], v[204:207], v[152:155]\n\t" \
  "v_mfma_f32_16x16x32_bf16 v[156:159], v[28:31], v[204:207], v[156:159]\n\t" \
  "v_mfma_f32_16x16x32_bf16 v[144:147], v[32:35], v[208:211], v[144:147]\n\t" \
  "v_mfma_f32_16x16x32_bf16 v[148:151], v[36:39], v[208:211], v[148:151]\n\t" \
  "v_mfma_f32_16x16x32_bf16 v[152:155], v[32:35], v[212:215], v[152:155]\n\t" \
  "v_mfma_f32_16x16x32_bf16 v[156:159], v[36:39], v[212:215], v[156:159]\n\t" \
  "v_mfma_f32_16x16x32_bf16 v[144:147], v[40:43], v[216:219], v[144:147]\n\t" \
  "v_mfma_f32_16x16x32_bf16 v[148:151], v[44:47], v[216:219], v[148:151]\n\t" \
  "v_mfma_f32_16x16x32_bf16 v[152:155], v[40:43], v[220:223], v[152:155]\n\t" \
  "v_mfma_f32_16x16x32_bf16 v[156:159], v[44:47], v[220:223], v[156:159]\n\t" \
  "v_mfma_f32_16x16x32_bf16 v[144:147], v[48:51], v[224:227], v[144:147]\n\t" \
  "v_mfma_f32_16x16x32_bf16 v[148:151], v[52:55], v[224:227], v[148:151]\n\t" \
  "v_mfma_f32_16x16x32_bf16 v[152:155], v[48:51], v[228:231], v[152:155]\n\t" \
  "v_mfma_f32_16x16x32_bf16 v[156:159], v[52:55], v[228:231], v[156:159]\n\t" \
  "v_mfma_f32_16x16x32_bf16 v[144:147], v[56:59], v[232:235], v[144:147]\n\t" \
  "v_mfma_f32_16x16x32_bf16 v[148:151], v[60:63], v[232:235], v[148:151]\n\t" \
  "v_mfma_f32_16x16x32_bf16 v[152:155], v[56:59], v[236:239], v[152:155]\n\t" \
  "v_mfma_f32_16x16x32_bf16 v[156:159], v[60:63], v[236:239], v[156:159]\n\t" \
  "s_setprio 0\n\t" \
  "s_add_u32 s92, s92, 65024\n\t" \
  "s_addc_u32 s93, s93, 0\n\t" \
  "ds_read_b128 v[160:163], v253 offset:0\n\t" \
  "ds_read_b128 v[164:167], v253 offset:1024\n\t" \
  "ds_read_b128 v[176:179], v244 offset:32768\n\t" \
  "ds_read_b128 v[180:183], v244 offset:49152\n\t" \
  "ds_read_b128 v[184:187], v245 offset:32768\n\t" \
  "ds_read_b128 v[188:191], v245 offset:49152\n\t" \
  "s_add_u32 m0, %[lw], 65536\n\t" \
  "s_nop 0\n\t" \
  "global_load_lds_dwordx4 v248, s[92:93] sc1\n\t" \
  "ds_read_b128 v[192:195], v246 offset:32768\n\t" \
  "ds_read_b128 v[196:199], v246 offset:49152\n\t" \
  "ds_read_b128 v[200:203], v247 offset:32768\n\t" \
  "ds_read_b128 v[204:207], v247 offset:49152\n\t" \
  "s_add_u32 m0, %[lw], 66560\n\t" \
  "s_nop 0\n\t" \
  "global_load_lds_dwordx4 v249, s[92:93] sc1\n\t" \
  "ds_read_b128 v[208:211], v244 offset:33024\n\t" \
  "ds_read_b128 v[212:215], v244 offset:49408\n\t" \
  "ds_read_b128 v[216:219], v245 offset:33024\n\t" \
  "ds_read_b128 v[220:223], v245 offset:49408\n\t" \
  "s_add_u32 m0, %[lw], 67584\n\t" \
  "s_nop 0\n\t" \
  "global_load_lds_dwordx4 v250, s[92:93] sc1\n\t" \
  "ds_read_b128 v[224:227], v246 offset:33024\n\t" \
  "ds_read_b128 v[228:231], v246 offset:49408\n\t" \
  "ds_read_b128 v[232:235], v247 offset:33024\n\t" \
  "ds_read_b128 v[236:239], v247 offset:49408\n\t" \
  "s_add_u32 m0, %[lw], 68608\n\t" \
  "s_nop 0\n\t" \
  "global_load_lds_dwordx4 v251, s[92:93] sc1\n\t" \
  "s_waitcnt lgkmcnt(15)\n\t" \
  "s_waitcnt lgkmcnt(12)\n\t" \
  "s_sub_i32 s91, s86, 2\n\t" \
  "v_mov_b32 v173, s91\n\t" \
  "buffer_store_dword v173, v174, %[rs], 0 offen sc1\n\t" \
  "s_lshr_b32 s90, s87, 7\n\t" \
  "s_xor_b32 s90, s90, %[s0]\n\t" \
  "s_and_b32 s90, s90, 1\n\t" \
  "s_lshl1_add_u32 s90, s87, s90\n\t" \
  "s_lshl_b32 s90, s90, 13\n\t" \
  "v_add_f32 v160, v160, v128\n\t" \
  "v_add_f32 v161, v161, v129\n\t" \
  "v_add_f32 v162, v162, v130\n\t" \
  "v_add_f32 v163, v163, v131\n\t" \
  "v_add_f32 v164, v164, v132\n\t" \
  "v_add_f32 v165, v165, v133\n\t" \
  "v_add_f32 v166, v166, v134\n\t" \
  "v_add_f32 v167, v167, v135\n\t" \
  "v_cvt_pk_bf16_f32 v168, v160, v161\n\t" \
  "v_cvt_pk_bf16_f32 v169, v162, v163\n\t" \
  "v_cvt_pk_bf16_f32 v170, v164, v165\n\t" \
  "v_cvt_pk_bf16_f32 v171, v166, v167\n\t" \
  "buffer_store_dwordx4 v[168:171], v172, %[rt], s90 offen sc1\n\t" \
  "s_add_u32 s86, s86, 1\n\t" \
  "s_add_u32 s87, s87, 1\n\t" \
  "s_waitcnt lgkmcnt(0)\n\t" \
  "s_cmp_lt_u32 s86, 64\n\t" \
  "s_cbranch_scc1 .Lp1_%=\n\t" \
  "s_barrier\n\t" \
  "s_setprio 2\n\t" \
  "v_mfma_f32_16x16x32_bf16 v[144:147], v[64:67], v[176:179], v[144:147]\n\t" \
  "v_mfma_f32_16x16x32_bf16 v[148:151], v[68:71], v[176:179], v[148:151]\n\t" \
  "v_mfma_f32_16x16x32_bf16 v[152:155], v[64:67], v[180:183], v[152:155]\n\t" \
  "v_mfma_f32_16x16x32_bf16 v[156:159], v[68:71], v[180:183], v[156:159]\n\t" \
  "v_mfma_f32_16x16x32_bf16 v[144:147], v[72:75], v[184:187], v[144:147]\n\t" \
  "v_mfma_f32_16x16x32_bf16 v[148:151], v[76:79], v[184:187], v[148:151]\n\t" \
  "v_mfma_f32_16x16x32_bf16 v[152:155], v[72:75], v[188:191], v[152:155]\n\t" \
  "v_mfma_f32_16x16x32_bf16 v[156:159], v[76:79], v[188:191], v[156:159]\n\t" \
  "v_mfma_f32_16x16x32_bf16 v[144:147], v[80:83], v[192:195], v[144:147]\n\t" \
  "v_mfma_f32_16x16x32_bf16 v[148:151], v[84:87], v[192:195], v[148:151]\n\t" \
  "v_mfma_f32_16x16x32_bf16 v[152:155], v[80:83], v[196:199], v[152:155]\n\t" \
  "v_mfma_f32_16x16x32_bf16 v[156:159], v[84:87], v[196:199], v[156:159]\n\t" \
  "v_mfma_f32_16x16x32_bf16 v[144:147], v[88:91], v[200:203], v[144:147]\n\t" \
  "v_mfma_f32_16x16x32_bf16 v[148:151], v[92:95], v[200:203], v[148:151]\n\t" \
  "v_mfma_f32_16x16x32_bf16 v[152:155], v[88:91], v[204:207], v[152:155]\n\t" \
  "v_mfma_f32_16x16x32_bf16 v[156:159], v[92:95], v[204:207], v[156:159]\n\t" \
  "v_mfma_f32_16x16x32_bf16 v[144:147], v[96:99], v[208:211], v[144:147]\n\t" \
  "v_mfma_f32_16x16x32_bf16 v[148:151], v[100:103], v[208:211], v[148:151]\n\t" \
  "v_mfma_f32_16x16x32_bf16 v[152:155], v[96:99], v[212:215], v[152:155]\n\t" \
  "v_mfma_f32_16x16x32_bf16 v[156:159], v[100:103], v[212:215], v[156:159]\n\t" \
  "v_mfma_f32_16x16x32_bf16 v[144:147], v[104:107], v[216:219], v[144:147]\n\t" \
  "v_mfma_f32_16x16x32_bf16 v[148:151], v[108:111], v[216:219], v[148:151]\n\t" \
  "v_mfma_f32_16x16x32_bf16 v[152:155], v[104:107], v[220:223], v[152:155]\n\t" \
  "v_mfma_f32_16x16x32_bf16 v[156:159], v[108:111], v[220:223], v[156:159]\n\t" \
  "v_mfma_f32_16x16x32_bf16 v[144:147], v[112:115], v[224:227], v[144:147]\n\t" \
  "v_mfma_f32_16x16x32_bf16 v[148:151], v[116:119], v[224:227], v[148:151]\n\t" \
  "v_mfma_f32_16x16x32_bf16 v[152:155], v[112:115], v[228:231], v[152:155]\n\t" \
  "v_mfma_f32_16x16x32_bf16 v[156:159], v[116:119], v[228:231], v[156:159]\n\t" \
  "v_mfma_f32_16x16x32_bf16 v[144:147], v[120:123], v[232:235], v[144:147]\n\t" \
  "v_mfma_f32_16x16x32_bf16 v[148:151], v[124:127], v[232:235], v[148:151]\n\t" \
  "v_mfma_f32_16x16x32_bf16 v[152:155], v[120:123], v[236:239], v[152:155]\n\t" \
  "v_mfma_f32_16x16x32_bf16 v[156:159], v[124:127], v[236:239], v[156:159]\n\t" \
  "s_setprio 0\n\t" \
  "s_nop 5\n\t" \
  "ds_write_b128 v252, v[152:155] offset:0\n\t" \
  "ds_write_b128 v252, v[156:159] offset:1024\n\t" \
  "s_waitcnt lgkmcnt(0)\n\t" \
  "s_barrier\n\t" \
  "ds_read_b128 v[160:163], v253 offset:0\n\t" \
  "ds_read_b128 v[164:167], v253 offset:1024\n\t" \
  "s_waitcnt lgkmcnt(0)\n\t" \
  "s_lshr_b32 s90, s87, 7\n\t" \
  "s_xor_b32 s90, s90, %[s0]\n\t" \
  "s_and_b32 s90, s90, 1\n\t" \
  "s_lshl1_add_u32 s90, s87, s90\n\t" \
  "s_lshl_b32 s90, s90, 13\n\t" \
  "v_add_f32 v160, v160, v144\n\t" \
  "v_add_f32 v161, v161, v145\n\t" \
  "v_add_f32 v162, v162, v146\n\t" \
  "v_add_f32 v163, v163, v147\n\t" \
  "v_add_f32 v164, v164, v148\n\t" \
  "v_add_f32 v165, v165, v149\n\t" \
  "v_add_f32 v166, v166, v150\n\t" \
  "v_add_f32 v167, v167, v151\n\t" \
  "v_cvt_pk_bf16_f32 v168, v160, v161\n\t" \
  "v_cvt_pk_bf16_f32 v169, v162, v163\n\t" \
  "v_cvt_pk_bf16_f32 v170, v164, v165\n\t" \
  "v_cvt_pk_bf16_f32 v171, v166, v167\n\t" \
  "buffer_store_dwordx4 v[168:171], v172, %[rt], s90 offen sc1\n\t" \
  "s_waitcnt vmcnt(0)\n\t" \
  "s_barrier\n\t" \
  "s_sub_i32 s91, s86, 1\n\t" \
  "v_mov_b32 v173, s91\n\t" \
  "buffer_store_dword v173, v174, %[rs], 0 offen sc1\n\t" \
  "s_mov_b32 m0, s94\n\t" \
  ".Le_%=:\n\t"
#define PP_ASM_1 \
  "s_cmp_lg_u32 %[kh], 0\n\t" \
  "s_cbranch_scc1 .Lk_%=\n\t" \
  "s_mov_b32 s94, m0\n\t" \
  "s_mov_b64 s[92:93], %[x0]\n\t" \
  "s_mov_b32 s86, %[g0]\n\t" \
  "s_add_u32 s87, s86, %[t0]\n\t" \
  "s_add_u32 m0, %[lw], 67584\n\t" \
  "s_nop 0\n\t" \
  "global_load_lds_dwordx4 v250, s[92:93] sc1\n\t" \
  "s_add_u32 m0, %[lw], 68608\n\t" \
  "s_nop 0\n\t" \
  "global_load_lds_dwordx4 v251, s[92:93] sc1\n\t" \
  "s_barrier\n\t" \
  "s_branch .Ls0_%=\n\t" \
  ".Lp0_%=:\n\t" \
  "s_waitcnt vmcnt(9)\n\t" \
  "s_barrier\n\t" \
  ".Ls0_%=:\n\t" \
  "s_add_u32 s92, s92, 512\n\t" \
  "s_addc_u32 s93, s93, 0\n\t" \
  "ds_read_b128 v[176:179], v240 offset:0\n\t" \
  "ds_read_b128 v[180:183], v240 offset:16384\n\t" \
  "ds_read_b128 v[184:187], v241 offset:0\n\t" \
  "ds_read_b128 v[188:191], v241 offset:16384\n\t" \
  "s_add_u32 m0, %[lw], 98304\n\t" \
  "s_nop 0\n\t" \
  "global_load_lds_dwordx4 v248, s[92:93] sc1\n\t" \
  "ds_read_b128 v[192:195], v242 offset:0\n\t" \
  "ds_read_b128 v[196:199], v242 offset:16384\n\t" \
  "ds_read_b128 v[200:203], v243 offset:0\n\t" \
  "ds_read_b128 v[204:207], v243 offset:16384\n\t" \
  "s_add_u32 m0, %[lw], 99328\n\t" \
  "s_nop 0\n\t" \
  "global_load_lds_dwordx4 v249, s[92:93] sc1\n\t" \
  "ds_read_b128 v[208:211], v240 offset:256\n\t" \
  "ds_read_b128 v[212:215], v240 offset:16640\n\t" \
  "ds_read_b128 v[216:219], v241 offset:256\n\t" \
  "ds_read_b128 v[220:223], v241 offset:16640\n\t" \
  "s_add_u32 m0, %[lw], 100352\n\t" \
  "s_nop 0\n\t" \
  "global_load_lds_dwordx4 v250, s[92:93] sc1\n\t" \
  "ds_read_b128 v[224:227], v242 offset:256\n\t" \
  "ds_read_b128 v[228:231], v242 offset:16640\n\t" \
  "ds_read_b128 v[232:235], v243 offset:256\n\t" \
  "ds_read_b128 v[236:239], v243 offset:16640\n\t" \
  "s_add_u32 m0, %[lw], 101376\n\t" \
  "s_nop 0\n\t" \
  "global_load_lds_dwordx4 v251, s[92:93] sc1\n\t" \
  "ds_write_b128 v252, v[152:155] offset:0\n\t" \
  "ds_write_b128 v252, v[156:159] offset:1024\n\t" \
  "s_waitcnt lgkmcnt(0)\n\t" \
  "s_setprio 1\n\t" \
  "v_mfma_f32_16x16x32_bf16 v[128:131], v[176:179], v[0:3], 0\n\t" \
  "v_mfma_f32_16x16x32_bf16 v[136:139], v[176:179], v[4:7], 0\n\t" \
  "v_mfma_f32_16x16x32_bf16 v[132:135], v[180:183], v[0:3], 0\n\t" \
  "v_mfma_f32_16x16x32_bf16 v[140:143], v[180:183], v[4:7], 0\n\t" \
  "v_mfma_f32_16x16x32_bf16 v[128:131], v[184:187], v[8:11], v[128:131]\n\t" \
  "v_mfma_f32_16x16x32_bf16 v[136:139], v[184:187], v[12:15], v[136:139]\n\t" \
  "v_mfma_f32_16x16x32_bf16 v[132:135], v[188:191], v[8:11], v[132:135]\n\t" \
  "v_mfma_f32_16x16x32_bf16 v[140:143], v[188:191], v[12:15], v[140:143]\n\t" \
  "v_mfma_f32_16x16x32_bf16 v[128:131], v[192:195], v[16:19], v[128:131]\n\t" \
  "v_mfma_f32_16x16x32_bf16 v[136:139], v[192:195], v[20:23], v[136:139]\n\t" \
  "v_mfma_f32_16x16x32_bf16 v[132:135], v[196:199], v[16:19], v[132:135]\n\t" \
  "v_mfma_f32_16x16x32_bf16 v[140:143], v[196:199], v[20:23], v[140:143]\n\t" \
  "v_mfma_f32_16x16x32_bf16 v[128:131], v[200:203], v[24:27], v[128:131]\n\t" \
  "v_mfma_f32_16x16x32_bf16 v[136:139], v[200:203], v[28:31], v[136:139]\n\t" \
  "v_mfma_f32_16x16x32_bf16 v[132:135], v[204:207], v[24:27], v[132:135]\n\t" \
  "v_mfma_f32_16x16x32_bf16 v[140:143], v[204:207], v[28:31], v[140:143]\n\t" \
  "v_mfma_f32_16x16x32_bf16 v[128:131], v[208:211], v[32:35], v[128:131]\n\t" \
  "v_mfma_f32_16x16x32_bf16 v[136:139], v[208:211], v[36:39], v[136:139]\n\t" \
  "v_mfma_f32_16x16x32_bf16 v[132:135], v[212:215], v[32:35], v[132:135]\n\t" \
  "v_mfma_f32_16x16x32_bf16 v[140:143], v[212:215], v[36:39], v[140:143]\n\t" \
  "v_mfma_f32_16x16x32_bf16 v[128:131], v[216:219], v[40:43], v[128:131]\n\t" \
  "v_mfma_f32_16x16x32_bf16 v[136:139], v[216:219], v[44:47], v[136:139]\n\t" \
  "v_mfma_f32_16x16x32_bf16 v[132:135], v[220:223], v[40:43], v[132:135]\n\t" \
  "v_mfma_f32_16x16x32_bf16 v[140:143], v[220:223], v[44:47], v[140:143]\n\t" \
  "v_mfma_f32_16x16x32_bf16 v[128:131], v[224:227], v[48:51], v[128:131]\n\t" \
  "v_mfma_f32_16x16x32_bf16 v[136:139], v[224:227], v[52:55], v[136:139]\n\t" \
  "v_mfma_f32_16x16x32_bf16 v[132:135], v[228:231], v[48:51], v[132:135]\n\t" \
  "v_mfma_f32_16x16x32_bf16 v[140:143], v[228:231], v[52:55], v[140:143]\n\t" \
  "v_mfma_f32_16x16x32_bf16 v[128:131], v[232:235], v[56:59], v[128:131]\n\t" \
  "v_mfma_f32_16x16x32_bf16 v[136:139], v[232:235], v[60:63], v[136:139]\n\t" \
  "v_mfma_f32_16x16x32_bf16 v[132:135], v[236:239], v[56:59], v[132:135]\n\t" \
  "v_mfma_f32_16x16x32_bf16 v[140:143], v[236:239], v[60:63], v[140:143]\n\t" \
  "s_setprio 0\n\t" \
  "s_waitcnt vmcnt(4)\n\t" \
  "s_barrier\n\t" \
  "s_add_u32 s92, s92, 65024\n\t" \
  "s_addc_u32 s93, s93, 0\n\t" \
  "ds_read_b128 v[160:163], v253 offset:0\n\t" \
  "ds_read_b128 v[164:167], v253 offset:1024\n\t" \
  "ds_read_b128 v[176:179], v240 offset:32768\n\t" \
  "ds_read_b128 v[180:183], v240 offset:49152\n\t" \
  "ds_read_b128 v[184:187], v241 offset:32768\n\t" \
  "ds_read_b128 v[188:191], v241 offset:49152\n\t" \
  "s_add_u32 m0, %[lw], 0\n\t" \
  "s_nop 0\n\t" \
  "global_load_lds_dwordx4 v248, s[92:93] sc1\n\t" \
  "ds_read_b128 v[192:195], v242 offset:32768\n\t" \
  "ds_read_b128 v[196:199], v242 offset:49152\n\t" \
  "ds_read_b128 v[200:203], v243 offset:32768\n\t" \
  "ds_read_b128 v[204:207], v243 offset:49152\n\t" \
  "s_add_u32 m0, %[lw], 1024\n\t" \
  "s_nop 0\n\t" \
  "global_load_lds_dwordx4 v249, s[92:93] sc1\n\t" \
  "ds_read_b128 v[208:211], v240 offset:33024\n\t" \
  "ds_read_b128 v[212:215], v240 offset:49408\n\t" \
  "ds_read_b128 v[216:219], v241 offset:33024\n\t" \
  "ds_read_b128 v[220:223], v241 offset:49408\n\t" \
  "s_add_u32 m0, %[lw], 2048\n\t" \
  "s_nop 0\n\t" \
  "global_load_lds_dwordx4 v250, s[92:93] sc1\n\t" \
  "ds_read_b128 v[224:227], v242 offset:33024\n\t" \
  "ds_read_b128 v[228:231], v242 offset:49408\n\t" \
  "ds_read_b128 v[232:235], v243 offset:33024\n\t" \
  "ds_read_b128 v[236:239], v243 offset:49408\n\t" \
  "s_add_u32 m0, %[lw], 3072\n\t" \
  "s_nop 0\n\t" \
  "global_load_lds_dwordx4 v251, s[92:93] sc1\n\t" \
  "s_waitcnt lgkmcnt(15)\n\t" \
  "s_waitcnt lgkmcnt(12)\n\t" \
  "s_lshr_b32 s90, s87, 7\n\t" \
  "s_xor_b32 s90, s90, %[s0]\n\t" \
  "s_and_b32 s90, s90, 1\n\t" \
  "s_lshl1_add_u32 s90, s87, s90\n\t" \
  "s_lshl_b32 s90, s90, 13\n\t" \
  "v_add_f32 v160, v160, v144\n\t" \
  "v_add_f32 v161, v161, v145\n\t" \
  "v_add_f32 v162, v162, v146\n\t" \
  "v_add_f32 v163, v163, v147\n\t" \
  "v_add_f32 v164, v164, v148\n\t" \
  "v_add_f32 v165, v165, v149\n\t" \
  "v_add_f32 v166, v166, v150\n\t" \
  "v_add_f32 v167, v167, v151\n\t" \
  "v_cvt_pk_bf16_f32 v168, v160, v161\n\t" \
  "v_cvt_pk_bf16_f32 v169, v162, v163\n\t" \
  "v_cvt_pk_bf16_f32 v170, v164, v165\n\t" \
  "v_cvt_pk_bf16_f32 v171, v166, v167\n\t" \
  "buffer_store_dwordx4 v[168:171], v172, %[rt], s90 offen sc1\n\t" \
  "s_add_u32 s86, s86, 1\n\t" \
  "s_add_u32 s87, s87, 1\n\t" \
  "s_waitcnt lgkmcnt(0)\n\t" \
  "s_setprio 1\n\t" \
  "v_mfma_f32_16x16x32_bf16 v[128:131], v[176:179], v[64:67], v[128:131]\n\t" \
  "v_mfma_f32_16x16x32_bf16 v[136:139], v[176:179], v[68:71], v[136:139]\n\t" \
  "v_mfma_f32_16x16x32_bf16 v[132:135], v[180:183], v[64:67], v[132:135]\n\t" \
  "v_mfma_f32_16x16x32_bf16 v[140:143], v[180:183], v[68:71], v[140:143]\n\t" \
  "v_mfma_f32_16x16x32_bf16 v[128:131], v[184:187], v[72:75], v[128:131]\n\t" \
  "v_mfma_f32_16x16x32_bf16 v[136:139], v[184:187], v[76:79], v[136:139]\n\t" \
  "v_mfma_f32_16x16x32_bf16 v[132:135], v[188:191], v[72:75], v[132:135]\n\t" \
  "v_mfma_f32_16x16x32_bf16 v[140:143], v[188:191], v[76:79], v[140:143]\n\t" \
  "v_mfma_f32_16x16x32_bf16 v[128:131], v[192:195], v[80:83], v[128:131]\n\t" \
  "v_mfma_f32_16x16x32_bf16 v[136:139], v[192:195], v[84:87], v[136:139]\n\t" \
  "v_mfma_f32_16x16x32_bf16 v[132:135], v[196:199], v[80:83], v[132:135]\n\t" \
  "v_mfma_f32_16x16x32_bf16 v[140:143], v[196:199], v[84:87], v[140:143]\n\t" \
  "v_mfma_f32_16x16x32_bf16 v[128:131], v[200:203], v[88:91], v[128:131]\n\t" \
  "v_mfma_f32_16x16x32_bf16 v[136:139], v[200:203], v[92:95], v[136:139]\n\t" \
  "v_mfma_f32_16x16x32_bf16 v[132:135], v[204:207], v[88:91], v[132:135]\n\t" \
  "v_mfma_f32_16x16x32_bf16 v[140:143], v[204:207], v[92:95], v[140:143]\n\t" \
  "v_mfma_f32_16x16x32_bf16 v[128:131], v[208:211], v[96:99], v[128:131]\n\t" \
  "v_mfma_f32_16x16x32_bf16 v[136:139], v[208:211], v[100:103], v[136:139]\n\t" \
  "v_mfma_f32_16x16x32_bf16 v[132:135], v[212:215], v[96:99], v[132:135]\n\t" \
  "v_mfma_f32_16x16x32_bf16 v[140:143], v[212:215], v[100:103], v[140:143]\n\t" \
  "v_mfma_f32_16x16x32_bf16 v[128:131], v[216:219], v[104:107], v[128:131]\n\t" \
  "v_mfma_f32_16x16x32_bf16 v[136:139], v[216:219], v[108:111], v[136:139]\n\t" \
  "v_mfma_f32_16x16x32_bf16 v[132:135], v[220:223], v[104:107], v[132:135]\n\t" \
  "v_mfma_f32_16x16x32_bf16 v[140:143], v[220:223], v[108:111], v[140:143]\n\t" \
  "v_mfma_f32_16x16x32_bf16 v[128:131], v[224:227], v[112:115], v[128:131]\n\t" \
  "v_mfma_f32_16x16x32_bf16 v[136:139], v[224:227], v[116:119], v[136:139]\n\t" \
  "v_mfma_f32_16x16x32_bf16 v[132:135], v[228:231], v[112:115], v[132:135]\n\t" \
  "v_mfma_f32_16x16x32_bf16 v[140:143], v[228:231], v[116:119], v[140:143]\n\t" \
  "v_mfma_f32_16x16x32_bf16 v[128:131], v[232:235], v[120:123], v[128:131]\n\t" \
  "v_mfma_f32_16x16x32_bf16 v[136:139], v[232:235], v[124:127], v[136:139]\n\t" \
  "v_mfma_f32_16x16x32_bf16 v[132:135], v[236:239], v[120:123], v[132:135]\n\t" \
  "v_mfma_f32_16x16x32_bf16 v[140:143], v[236:239], v[124:127], v[140:143]\n\t" \
  "s_setprio 0\n\t" \
  "s_waitcnt vmcnt(9)\n\t" \
  "s_barrier\n\t" \
  "s_add_u32 s92, s92, 512\n\t" \
  "s_addc_u32 s93, s93, 0\n\t" \
  "ds_read_b128 v[176:179], v244 offset:0\n\t" \
  "ds_read_b128 v[180:183], v244 offset:16384\n\t" \
  "ds_read_b128 v[184:187], v245 offset:0\n\t" \
  "ds_read_b128 v[188:191], v245 offset:16384\n\t" \
  "s_add_u32 m0, %[lw], 32768\n\t" \
  "s_nop 0\n\t" \
  "global_load_lds_dwordx4 v248, s[92:93] sc1\n\t" \
  "ds_read_b128 v[192:195], v246 offset:0\n\t" \
  "ds_read_b128 v[196:199], v246 offset:16384\n\t" \
  "ds_read_b128 v[200:203], v247 offset:0\n\t" \
  "ds_read_b128 v[204:207], v247 offset:16384\n\t" \
  "s_add_u32 m0, %[lw], 33792\n\t" \
  "s_nop 0\n\t" \
  "global_load_lds_dwordx4 v249, s[92:93] sc1\n\t" \
  "ds_read_b128 v[208:211], v244 offset:256\n\t" \
  "ds_read_b128 v[212:215], v244 offset:16640\n\t" \
  "ds_read_b128 v[216:219], v245 offset:256\n\t" \
  "ds_read_b128 v[220:223], v245 offset:16640\n\t" \
  "s_add_u32 m0, %[lw], 34816\n\t" \
  "s_nop 0\n\t" \
  "global_load_lds_dwordx4 v250, s[92:93] sc1\n\t" \
  "ds_read_b128 v[224:227], v246 offset:256\n\t" \
  "ds_read_b128 v[228:231], v246 offset:16640\n\t" \
  "ds_read_b128 v[232:235], v247 offset:256\n\t" \
  "ds_read_b128 v[236:239], v247 offset:16640\n\t" \
  "s_add_u32 m0, %[lw], 35840\n\t" \
  "s_nop 0\n\t" \
  "global_load_lds_dwordx4 v251, s[92:93] sc1\n\t" \
  "ds_write_b128 v252, v[136:139] offset:0\n\t" \
  "ds_write_b128 v252, v[140:143] offset:1024\n\t" \
  "s_waitcnt lgkmcnt(0)\n\t" \
  "s_setprio 1\n\t" \
  "v_mfma_f32_16x16x32_bf16 v[144:147], v[176:179], v[0:3], 0\n\t" \
  "v_mfma_f32_16x16x32_bf16 v[152:155], v[176:179], v[4:7], 0\n\t" \
  "v_mfma_f32_16x16x32_bf16 v[148:151], v[180:183], v[0:3], 0\n\t" \
  "v_mfma_f32_16x16x32_bf16 v[156:159], v[180:183], v[4:7], 0\n\t" \
  "v_mfma_f32_16x16x32_bf16 v[144:147], v[184:187], v[8:11], v[144:147]\n\t" \
  "v_mfma_f32_16x16x32_bf16 v[152:155], v[184:187], v[12:15], v[152:155]\n\t" \
  "v_mfma_f32_16x16x32_bf16 v[148:151], v[188:191], v[8:11], v[148:151]\n\t" \
  "v_mfma_f32_16x16x32_bf16 v[156:159], v[188:191], v[12:15], v[156:159]\n\t" \
  "v_mfma_f32_16x16x32_bf16 v[144:147], v[192:195], v[16:19], v[144:147]\n\t" \
  "v_mfma_f32_16x16x32_bf16 v[152:155], v[192:195], v[20:23], v[152:155]\n\t" \
  "v_mfma_f32_16x16x32_bf16 v[148:151], v[196:199], v[16:19], v[148:151]\n\t" \
  "v_mfma_f32_16x16x32_bf16 v[156:159], v[196:199], v[20:23], v[156:159]\n\t" \
  "v_mfma_f32_16x16x32_bf16 v[144:147], v[200:203], v[24:27], v[144:147]\n\t" \
  "v_mfma_f32_16x16x32_bf16 v[152:155], v[200:203], v[28:31], v[152:155]\n\t" \
  "v_mfma_f32_16x16x32_bf16 v[148:151], v[204:207], v[24:27], v[148:151]\n\t" \
  "v_mfma_f32_16x16x32_bf16 v[156:159], v[204:207], v[28:31], v[156:159]\n\t" \
  "v_mfma_f32_16x16x32_bf16 v[144:147], v[208:211], v[32:35], v[144:147]\n\t" \
  "v_mfma_f32_16x16x32_bf16 v[152:155], v[208:211], v[36:39], v[152:155]\n\t" \
  "v_mfma_f32_16x16x32_bf16 v[148:151], v[212:215], v[32:35], v[148:151]\n\t" \
  "v_mfma_f32_16x16x32_bf16 v[156:159], v[212:215], v[36:39], v[156:159]\n\t" \
  "v_mfma_f32_16x16x32_bf16 v[144:147], v[216:219], v[40:43], v[144:147]\n\t" \
  "v_mfma_f32_16x16x32_bf16 v[152:155], v[216:219], v[44:47], v[152:155]\n\t" \
  "v_mfma_f32_16x16x32_bf16 v[148:151], v[220:223], v[40:43], v[148:151]\n\t" \
  "v_mfma_f32_16x16x32_bf16 v[156:159], v[220:223], v[44:47], v[156:159]\n\t" \
  "v_mfma_f32_16x16x32_bf16 v[144:147], v[224:227], v[48:51], v[144:147]\n\t" \
  "v_mfma_f32_16x16x32_bf16 v[152:155], v[224:227], v[52:55], v[152:155]\n\t" \
  "v_mfma_f32_16x16x32_bf16 v[148:151], v[228:231], v[48:51], v[148:151]\n\t" \
  "v_mfma_f32_16x16x32_bf16 v[156:159], v[228:231], v[52:55], v[156:159]\n\t" \
  "v_mfma_f32_16x16x32_bf16 v[144:147], v[232:235], v[56:59], v[144:147]\n\t" \
  "v_mfma_f32_16x16x32_bf16 v[152:155], v[232:235], v[60:63], v[152:155]\n\t" \
  "v_mfma_f32_16x16x32_bf16 v[148:151], v[236:239], v[56:59], v[148:151]\n\t" \
  "v_mfma_f32_16x16x32_bf16 v[156:159], v[236:239], v[60:63], v[156:159]\n\t" \
  "s_setprio 0\n\t" \
  "s_waitcnt vmcnt(4)\n\t" \
  "s_barrier\n\t" \
  "s_add_u32 s92, s92, 65024\n\t" \
  "s_addc_u32 s93, s93, 0\n\t" \
  "ds_read_b128 v[160:163], v253 offset:0\n\t" \
  "ds_read_b128 v[164:167], v253 offset:1024\n\t" \
  "ds_read_b128 v[176:179], v244 offset:32768\n\t" \
  "ds_read_b128 v[180:183], v244 offset:49152\n\t" \
  "ds_read_b128 v[184:187], v245 offset:32768\n\t" \
  "ds_read_b128 v[188:191], v245 offset:49152\n\t" \
  "s_add_u32 m0, %[lw], 65536\n\t" \
  "s_nop 0\n\t" \
  "global_load_lds_dwordx4 v248, s[92:93] sc1\n\t" \
  "ds_read_b128 v[192:195], v246 offset:32768\n\t" \
  "ds_read_b128 v[196:199], v246 offset:49152\n\t" \
  "ds_read_b128 v[200:203], v247 offset:32768\n\t" \
  "ds_read_b128 v[204:207], v247 offset:49152\n\t" \
  "s_add_u32 m0, %[lw], 66560\n\t" \
  "s_nop 0\n\t" \
  "global_load_lds_dwordx4 v249, s[92:93] sc1\n\t" \
  "ds_read_b128 v[208:211], v244 offset:33024\n\t" \
  "ds_read_b128 v[212:215], v244 offset:49408\n\t" \
  "ds_read_b128 v[216:219], v245 offset:33024\n\t" \
  "ds_read_b128 v[220:223], v245 offset:49408\n\t" \
  "s_add_u32 m0, %[lw], 67584\n\t" \
  "s_nop 0\n\t" \
  "global_load_lds_dwordx4 v250, s[92:93] sc1\n\t" \
  "ds_read_b128 v[224:227], v246 offset:33024\n\t" \
  "ds_read_b128 v[228:231], v246 offset:49408\n\t" \
  "ds_read_b128 v[232:235], v247 offset:33024\n\t" \
  "ds_read_b128 v[236:239], v247 offset:49408\n\t" \
  "s_add_u32 m0, %[lw], 68608\n\t" \
  "s_nop 0\n\t" \
  "global_load_lds_dwordx4 v251, s[92:93] sc1\n\t" \
  "s_waitcnt lgkmcnt(15)\n\t" \
  "s_waitcnt lgkmcnt(12)\n\t" \
  "s_lshr_b32 s90, s87, 7\n\t" \
  "s_xor_b32 s90, s90, %[s0]\n\t" \
  "s_and_b32 s90, s90, 1\n\t" \
  "s_lshl1_add_u32 s90, s87, s90\n\t" \
  "s_lshl_b32 s90, s90, 13\n\t" \
  "v_add_f32 v160, v160, v128\n\t" \
  "v_add_f32 v161, v161, v129\n\t" \
  "v_add_f32 v162, v162, v130\n\t" \
  "v_add_f32 v163, v163, v131\n\t" \
  "v_add_f32 v164, v164, v132\n\t" \
  "v_add_f32 v165, v165, v133\n\t" \
  "v_add_f32 v166, v166, v134\n\t" \
  "v_add_f32 v167, v167, v135\n\t" \
  "v_cvt_pk_bf16_f32 v168, v160, v161\n\t" \
  "v_cvt_pk_bf16_f32 v169, v162, v163\n\t" \
  "v_cvt_pk_bf16_f32 v170, v164, v165\n\t" \
  "v_cvt_pk_bf16_f32 v171, v166, v167\n\t" \
  "buffer_store_dwordx4 v[168:171], v172, %[rt], s90 offen sc1\n\t" \
  "s_add_u32 s86, s86, 1\n\t" \
  "s_add_u32 s87, s87, 1\n\t" \
  "s_waitcnt lgkmcnt(0)\n\t" \
  "s_setprio 1\n\t" \
  "v_mfma_f32_16x16x32_bf16 v[144:147], v[176:179], v[64:67], v[144:147]\n\t" \
  "v_mfma_f32_16x16x32_bf16 v[152:155], v[176:179], v[68:71], v[152:155]\n\t" \
  "v_mfma_f32_16x16x32_bf16 v[148:151], v[180:183], v[64:67], v[148:151]\n\t" \
  "v_mfma_f32_16x16x32_bf16 v[156:159], v[180:183], v[68:71], v[156:159]\n\t" \
  "v_mfma_f32_16x16x32_bf16 v[144:147], v[184:187], v[72:75], v[144:147]\n\t" \
  "v_mfma_f32_16x16x32_bf16 v[152:155], v[184:187], v[76:79], v[152:155]\n\t" \
  "v_mfma_f32_16x16x32_bf16 v[148:151], v[188:191], v[72:75], v[148:151]\n\t" \
  "v_mfma_f32_16x16x32_bf16 v[156:159], v[188:191], v[76:79], v[156:159]\n\t" \
  "v_mfma_f32_16x16x32_bf16 v[144:147], v[192:195], v[80:83], v[144:147]\n\t" \
  "v_mfma_f32_16x16x32_bf16 v[152:155], v[192:195], v[84:87], v[152:155]\n\t" \
  "v_mfma_f32_16x16x32_bf16 v[148:151], v[196:199], v[80:83], v[148:151]\n\t" \
  "v_mfma_f32_16x16x32_bf16 v[156:159], v[196:199], v[84:87], v[156:159]\n\t" \
  "v_mfma_f32_16x16x32_bf16 v[144:147], v[200:203], v[88:91], v[144:147]\n\t" \
  "v_mfma_f32_16x16x32_bf16 v[152:155], v[200:203], v[92:95], v[152:155]\n\t" \
  "v_mfma_f32_16x16x32_bf16 v[148:151], v[204:207], v[88:91], v[148:151]\n\t" \
  "v_mfma_f32_16x16x32_bf16 v[156:159], v[204:207], v[92:95], v[156:159]\n\t" \
  "v_mfma_f32_16x16x32_bf16 v[144:147], v[208:211], v[96:99], v[144:147]\n\t" \
  "v_mfma_f32_16x16x32_bf16 v[152:155], v[208:211], v[100:103], v[152:155]\n\t" \
  "v_mfma_f32_16x16x32_bf16 v[148:151], v[212:215], v[96:99], v[148:151]\n\t" \
  "v_mfma_f32_16x16x32_bf16 v[156:159], v[212:215], v[100:103], v[156:159]\n\t" \
  "v_mfma_f32_16x16x32_bf16 v[144:147], v[216:219], v[104:107], v[144:147]\n\t" \
  "v_mfma_f32_16x16x32_bf16 v[152:155], v[216:219], v[108:111], v[152:155]\n\t" \
  "v_mfma_f32_16x16x32_bf16 v[148:151], v[220:223], v[104:107], v[148:151]\n\t" \
  "v_mfma_f32_16x16x32_bf16 v[156:159], v[220:223], v[108:111], v[156:159]\n\t" \
  "v_mfma_f32_16x16x32_bf16 v[144:147], v[224:227], v[112:115], v[144:147]\n\t" \
  "v_mfma_f32_16x16x32_bf16 v[152:155], v[224:227], v[116:119], v[152:155]\n\t" \
  "v_mfma_f32_16x16x32_bf16 v[148:151], v[228:231], v[112:115], v[148:151]\n\t" \
  "v_mfma_f32_16x16x32_bf16 v[156:159], v[228:231], v[116:119], v[156:159]\n\t" \
  "v_mfma_f32_16x16x32_bf16 v[144:147], v[232:235], v[120:123], v[144:147]\n\t" \
  "v_mfma_f32_16x16x32_bf16 v[152:155], v[232:235], v[124:127], v[152:155]\n\t" \
  "v_mfma_f32_16x16x32_bf16 v[148:151], v[236:239], v[120:123], v[148:151]\n\t" \
  "v_mfma_f32_16x16x32_bf16 v[156:159], v[236:239], v[124:127], v[156:159]\n\t" \
  "s_setprio 0\n\t" \
  "s_cmp_lt_u32 s86, 64\n\t" \
  "s_cbranch_scc1 .Lp0_%=\n\t" \
  "s_barrier\n\t" \
  "s_nop 1\n\t" \
  "ds_write_b128 v252, v[152:155] offset:0\n\t" \
  "s_nop 0\n\t" \
  "ds_write_b128 v252, v[156:159] offset:1024\n\t" \
  "s_waitcnt lgkmcnt(0)\n\t" \
  "s_barrier\n\t" \
  "ds_read_b128 v[160:163], v253 offset:0\n\t" \
  "ds_read_b128 v[164:167], v253 offset:1024\n\t" \
  "s_waitcnt lgkmcnt(0)\n\t" \
  "s_lshr_b32 s90, s87, 7\n\t" \
  "s_xor_b32 s90, s90, %[s0]\n\t" \
  "s_and_b32 s90, s90, 1\n\t" \
  "s_lshl1_add_u32 s90, s87, s90\n\t" \
  "s_lshl_b32 s90, s90, 13\n\t" \
  "v_add_f32 v160, v160, v144\n\t" \
  "v_add_f32 v161, v161, v145\n\t" \
  "v_add_f32 v162, v162, v146\n\t" \
  "v_add_f32 v163, v163, v147\n\t" \
  "v_add_f32 v164, v164, v148\n\t" \
  "v_add_f32 v165, v165, v149\n\t" \
  "v_add_f32 v166, v166, v150\n\t" \
  "v_add_f32 v167, v167, v151\n\t" \
  "v_cvt_pk_bf16_f32 v168, v160, v161\n\t" \
  "v_cvt_pk_bf16_f32 v169, v162, v163\n\t" \
  "v_cvt_pk_bf16_f32 v170, v164, v165\n\t" \
  "v_cvt_pk_bf16_f32 v171, v166, v167\n\t" \
  "buffer_store_dwordx4 v[168:171], v172, %[rt], s90 offen sc1\n\t" \
  "s_waitcnt vmcnt(0)\n\t" \
  "s_barrier\n\t" \
  "s_mov_b32 m0, s94\n\t" \
  "s_branch .Le_%=\n\t" \
  ".Lk_%=:\n\t" \
  "s_mov_b32 s94, m0\n\t" \
  "s_mov_b64 s[92:93], %[x0]\n\t" \
  "s_mov_b32 s86, %[g0]\n\t" \
  "s_add_u32 s87, s86, %[t0]\n\t" \
  "s_add_u32 m0, %[lw], 67584\n\t" \
  "s_nop 0\n\t" \
  "global_load_lds_dwordx4 v250, s[92:93] sc1\n\t" \
  "s_add_u32 m0, %[lw], 68608\n\t" \
  "s_nop 0\n\t" \
  "global_load_lds_dwordx4 v251, s[92:93] sc1\n\t" \
  "s_barrier\n\t" \
  "s_branch .Ls1_%=\n\t" \
  ".Lp1_%=:\n\t" \
  "s_waitcnt vmcnt(10)\n\t" \
  "s_barrier\n\t" \
  "s_setprio 2\n\t" \
  "v_mfma_f32_16x16x32_bf16 v[144:147], v[176:179], v[64:67], v[144:147]\n\t" \
  "v_mfma_f32_16x16x32_bf16 v[152:155], v[176:179], v[68:71], v[152:155]\n\t" \
  "v_mfma_f32_16x16x32_bf16 v[148:151], v[180:183], v[64:67], v[148:151]\n\t" \
  "v_mfma_f32_16x16x32_bf16 v[156:159], v[180:183], v[68:71], v[156:159]\n\t" \
  "v_mfma_f32_16x16x32_bf16 v[144:147], v[184:187], v[72:75], v[144:147]\n\t" \
  "v_mfma_f32_16x16x32_bf16 v[152:155], v[184:187], v[76:79], v[152:155]\n\t" \
  "v_mfma_f32_16x16x32_bf16 v[148:151], v[188:191], v[72:75], v[148:151]\n\t" \
  "v_mfma_f32_16x16x32_bf16 v[156:159], v[188:191], v[76:79], v[156:159]\n\t" \
  "v_mfma_f32_16x16x32_bf16 v[144:147], v[192:195], v[80:83], v[144:147]\n\t" \
  "v_mfma_f32_16x16x32_bf16 v[152:155], v[192:195], v[84:87], v[152:155]\n\t" \
  "v_mfma_f32_16x16x32_bf16 v[148:151], v[196:199], v[80:83], v[148:151]\n\t" \
  "v_mfma_f32_16x16x32_bf16 v[156:159], v[196:199], v[84:87], v[156:159]\n\t" \
  "v_mfma_f32_16x16x32_bf16 v[144:147], v[200:203], v[88:91], v[144:147]\n\t" \
  "v_mfma_f32_16x16x32_bf16 v[152:155], v[200:203], v[92:95], v[152:155]\n\t" \
  "v_mfma_f32_16x16x32_bf16 v[148:151], v[204:207], v[88:91], v[148:151]\n\t" \
  "v_mfma_f32_16x16x32_bf16 v[156:159], v[204:207], v[92:95], v[156:159]\n\t" \
  "v_mfma_f32_16x16x32_bf16 v[144:147], v[208:211], v[96:99], v[144:147]\n\t" \
  "v_mfma_f32_16x16x32_bf16 v[152:155], v[208:211], v[100:103], v[152:155]\n\t" \
  "v_mfma_f32_16x16x32_bf16 v[148:151], v[212:215], v[96:99], v[148:151]\n\t" \
  "v_mfma_f32_16x16x32_bf16 v[156:159], v[212:215], v[100:103], v[156:159]\n\t" \
  "v_mfma_f32_16x16x32_bf16 v[144:147], v[216:219], v[104:107], v[144:147]\n\t" \
  "v_mfma_f32_16x16x32_bf16 v[152:155], v[216:219], v[108:111], v[152:155]\n\t" \
  "v_mfma_f32_16x16x32_bf16 v[148:151], v[220:223], v[104:107], v[148:151]\n\t" \
  "v_mfma_f32_16x16x32_bf16 v[156:159], v[220:223], v[108:111], v[156:159]\n\t" \
  "v_mfma_f32_16x16x32_bf16 v[144:147], v[224:227], v[112:115], v[144:147]\n\t" \
  "v_mfma_f32_16x16x32_bf16 v[152:155], v[224:227], v[116:119], v[152:155]\n\t" \
  "v_mfma_f32_16x16x32_bf16 v[148:151], v[228:231], v[112:115], v[148:151]\n\t" \
  "v_mfma_f32_16x16x32_bf16 v[156:159], v[228:231], v[116:119], v[156:159]\n\t" \
  "v_mfma_f32_16x16x32_bf16 v[144:147], v[232:235], v[120:123], v[144:147]\n\t" \
  "v_mfma_f32_16x16x32_bf16 v[152:155], v[232:235], v[124:127], v[152:155]\n\t" \
  "v_mfma_f32_16x16x32_bf16 v[148:151], v[236:239], v[120:123], v[148:151]\n\t" \
  "v_mfma_f32_16x16x32_bf16 v[156:159], v[236:239], v[124:127], v[156:159]\n\t" \
  "s_setprio 0\n\t" \
  ".Ls1_%=:\n\t" \
  "s_add_u32 s92, s92, 512\n\t" \
  "s_addc_u32 s93, s93, 0\n\t" \
  "ds_read_b128 v[176:179], v240 offset:0\n\t" \
  "ds_read_b128 v[180:183], v240 offset:16384\n\t" \
  "ds_read_b128 v[184:187], v241 offset:0\n\t" \
  "ds_read_b128 v[188:191], v241 offset:16384\n\t" \
  "s_add_u32 m0, %[lw], 98304\n\t" \
  "s_nop 0\n\t" \
  "global_load_lds_dwordx4 v248, s[92:93] sc1\n\t" \
  "ds_read_b128 v[192:195], v242 offset:0\n\t" \
  "ds_read_b128 v[196:199], v242 offset:16384\n\t" \
  "ds_read_b128 v[200:203], v243 offset:0\n\t" \
  "ds_read_b128 v[204:207], v243 offset:16384\n\t" \
  "s_add_u32 m0, %[lw], 99328\n\t" \
  "s_nop 0\n\t" \
  "global_load_lds_dwordx4 v249, s[92:93] sc1\n\t" \
  "ds_read_b128 v[208:211], v240 offset:256\n\t" \
  "ds_read_b128 v[212:215], v240 offset:16640\n\t" \
  "ds_read_b128 v[216:219], v241 offset:256\n\t" \
  "ds_read_b128 v[220:223], v241 offset:16640\n\t" \
  "s_add_u32 m0, %[lw], 100352\n\t" \
  "s_nop 0\n\t" \
  "global_load_lds_dwordx4 v250, s[92:93] sc1\n\t" \
  "ds_read_b128 v[224:227], v242 offset:256\n\t" \
  "ds_read_b128 v[228:231], v242 offset:16640\n\t" \
  "ds_read_b128 v[232:235], v243 offset:256\n\t" \
  "ds_read_b128 v[236:239], v243 offset:16640\n\t" \
  "s_add_u32 m0, %[lw], 101376\n\t" \
  "s_nop 0\n\t" \
  "global_load_lds_dwordx4 v251, s[92:93] sc1\n\t" \
  "ds_write_b128 v252, v[152:155] offset:0\n\t" \
  "ds_write_b128 v252, v[156:159] offset:1024\n\t" \
  "s_waitcnt lgkmcnt(0)\n\t" \
  "s_waitcnt vmcnt(4)\n\t" \
  "s_barrier\n\t" \
  "s_setprio 2\n\t" \
  "v_mfma_f32_16x16x32_bf16 v[128:131], v[176:179], v[0:3], 0\n\t" \
  "v_mfma_f32_16x16x32_bf16 v[136:139], v[176:179], v[4:7], 0\n\t" \
  "v_mfma_f32_16x16x32_bf16 v[132:135], v[180:183], v[0:3], 0\n\t" \
  "v_mfma_f32_16x16x32_bf16 v[140:143], v[180:183], v[4:7], 0\n\t" \
  "v_mfma_f32_16x16x32_bf16 v[128:131], v[184:187], v[8:11], v[128:131]\n\t" \
  "v_mfma_f32_16x16x32_bf16 v[136:139], v[184:187], v[12:15], v[136:139]\n\t" \
  "v_mfma_f32_16x16x32_bf16 v[132:135], v[188:191], v[8:11], v[132:135]\n\t" \
  "v_mfma_f32_16x16x32_bf16 v[140:143], v[188:191], v[12:15], v[140:143]\n\t" \
  "v_mfma_f32_16x16x32_bf16 v[128:131], v[192:195], v[16:19], v[128:131]\n\t" \
  "v_mfma_f32_16x16x32_bf16 v[136:139], v[192:195], v[20:23], v[136:139]\n\t" \
  "v_mfma_f32_16x16x32_bf16 v[132:135], v[196:199], v[16:19], v[132:135]\n\t" \
  "v_mfma_f32_16x16x32_bf16 v[140:143], v[196:199], v[20:23], v[140:143]\n\t" \
  "v_mfma_f32_16x16x32_bf16 v[128:131], v[200:203], v[24:27], v[128:131]\n\t" \
  "v_mfma_f32_16x16x32_bf16 v[136:139], v[200:203], v[28:31], v[136:139]\n\t" \
  "v_mfma_f32_16x16x32_bf16 v[132:135], v[204:207], v[24:27], v[132:135]\n\t" \
  "v_mfma_f32_16x16x32_bf16 v[140:143], v[204:207], v[28:31], v[140:143]\n\t" \
  "v_mfma_f32_16x16x32_bf16 v[128:131], v[208:211], v[32:35], v[128:131]\n\t" \
  "v_mfma_f32_16x16x32_bf16 v[136:139], v[208:211], v[36:39], v[136:139]\n\t" \
  "v_mfma_f32_16x16x32_bf16 v[132:135], v[212:215], v[32:35], v[132:135]\n\t" \
  "v_mfma_f32_16x16x32_bf16 v[140:143], v[212:215], v[36:39], v[140:143]\n\t" \
  "v_mfma_f32_16x16x32_bf16 v[128:131], v[216:219], v[40:43], v[128:131]\n\t" \
  "v_mfma_f32_16x16x32_bf16 v[136:139], v[216:219], v[44:47], v[136:139]\n\t" \
  "v_mfma_f32_16x16x32_bf16 v[132:135], v[220:223], v[40:43], v[132:135]\n\t" \
  "v_mfma_f32_16x16x32_bf16 v[140:143], v[220:223], v[44:47], v[140:143]\n\t" \
  "v_mfma_f32_16x16x32_bf16 v[128:131], v[224:227], v[48:51], v[128:131]\n\t" \
  "v_mfma_f32_16x16x32_bf16 v[136:139], v[224:227], v[52:55], v[136:139]\n\t" \
  "v_mfma_f32_16x16x32_bf16 v[132:135], v[228:231], v[48:51], v[132:135]\n\t" \
  "v_mfma_f32_16x16x32_bf16 v[140:143], v[228:231], v[52:55], v[140:143]\n\t" \
  "v_mfma_f32_16x16x32_bf16 v[128:131], v[232:235], v[56:59], v[128:131]\n\t" \
  "v_mfma_f32_16x16x32_bf16 v[136:139], v[232:235], v[60:63], v[136:139]\n\t" \
  "v_mfma_f32_16x16x32_bf16 v[132:135], v[236:239], v[56:59], v[132:135]\n\t" \
  "v_mfma_f32_16x16x32_bf16 v[140:143], v[236:239], v[60:63], v[140:143]\n\t" \
  "s_setprio 0\n\t" \
  "s_add_u32 s92, s92, 65024\n\t" \
  "s_addc_u32 s93, s93, 0\n\t" \
  "ds_read_b128 v[160:163], v253 offset:0\n\t" \
  "ds_read_b128 v[164:167], v253 offset:1024\n\t" \
  "ds_read_b128 v[176:179], v240 offset:32768\n\t" \
  "ds_read_b128 v[180:183], v240 offset:49152\n\t" \
  "ds_read_b128 v[184:187], v241 offset:32768\n\t" \
  "ds_read_b128 v[188:191], v241 offset:49152\n\t" \
  "s_add_u32 m0, %[lw], 0\n\t" \
  "s_nop 0\n\t" \
  "global_load_lds_dwordx4 v248, s[92:93] sc1\n\t" \
  "ds_read_b128 v[192:195], v242 offset:32768\n\t" \
  "ds_read_b128 v[196:199], v242 offset:49152\n\t" \
  "ds_read_b128 v[200:203], v243 offset:32768\n\t" \
  "ds_read_b128 v[204:207], v243 offset:49152\n\t" \
  "s_add_u32 m0, %[lw], 1024\n\t" \
  "s_nop 0\n\t" \
  "global_load_lds_dwordx4 v249, s[92:93] sc1\n\t" \
  "ds_read_b128 v[208:211], v240 offset:33024\n\t" \
  "ds_read_b128 v[212:215], v240 offset:49408\n\t" \
  "ds_read_b128 v[216:219], v241 offset:33024\n\t" \
  "ds_read_b128 v[220:223], v241 offset:49408\n\t" \
  "s_add_u32 m0, %[lw], 2048\n\t" \
  "s_nop 0\n\t" \
  "global_load_lds_dwordx4 v250, s[92:93] sc1\n\t" \
  "ds_read_b128 v[224:227], v242 offset:33024\n\t" \
  "ds_read_b128 v[228:231], v242 offset:49408\n\t" \
  "ds_read_b128 v[232:235], v243 offset:33024\n\t" \
  "ds_read_b128 v[236:239], v243 offset:49408\n\t" \
  "s_add_u32 m0, %[lw], 3072\n\t" \
  "s_nop 0\n\t" \
  "global_load_lds_dwordx4 v251, s[92:93] sc1\n\t" \
  "s_waitcnt lgkmcnt(15)\n\t" \
  "s_waitcnt lgkmcnt(12)\n\t" \
  "s_sub_i32 s91, s86, 2\n\t" \
  "v_mov_b32 v173, s91\n\t" \
  "buffer_store_dword v173, v174, %[rs], 0 offen sc1\n\t" \
  "s_lshr_b32 s90, s87, 7\n\t" \
  "s_xor_b32 s90, s90, %[s0]\n\t" \
  "s_and_b32 s90, s90, 1\n\t" \
  "s_lshl1_add_u32 s90, s87, s90\n\t" \
  "s_lshl_b32 s90, s90, 13\n\t" \
  "v_add_f32 v160, v160, v144\n\t" \
  "v_add_f32 v161, v161, v145\n\t" \
  "v_add_f32 v162, v162, v146\n\t" \
  "v_add_f32 v163, v163, v147\n\t" \
  "v_add_f32 v164, v164, v148\n\t" \
  "v_add_f32 v165, v165, v149\n\t" \
  "v_add_f32 v166, v166, v150\n\t" \
  "v_add_f32 v167, v167, v151\n\t" \
  "v_cvt_pk_bf16_f32 v168, v160, v161\n\t" \
  "v_cvt_pk_bf16_f32 v169, v162, v163\n\t" \
  "v_cvt_pk_bf16_f32 v170, v164, v165\n\t" \
  "v_cvt_pk_bf16_f32 v171, v166, v167\n\t" \
  "buffer_store_dwordx4 v[168:171], v172, %[rt], s90 offen sc1\n\t" \
  "s_add_u32 s86, s86, 1\n\t" \
  "s_add_u32 s87, s87, 1\n\t" \
  "s_waitcnt lgkmcnt(0)\n\t" \
  "s_waitcnt vmcnt(10)\n\t" \
  "s_barrier\n\t" \
  "s_setprio 2\n\t" \
  "v_mfma_f32_16x16x32_bf16 v[128:131], v[176:179], v[64:67], v[128:131]\n\t" \
  "v_mfma_f32_16x16x32_bf16 v[136:139], v[176:179], v[68:71], v[136:139]\n\t" \
  "v_mfma_f32_16x16x32_bf16 v[132:135], v[180:183], v[64:67], v[132:135]\n\t" \
  "v_mfma_f32_16x16x32_bf16 v[140:143], v[180:183], v[68:71], v[140:143]\n\t" \
  "v_mfma_f32_16x16x32_bf16 v[128:131], v[184:187], v[72:75], v[128:131]\n\t" \
  "v_mfma_f32_16x16x32_bf16 v[136:139], v[184:187], v[76:79], v[136:139]\n\t" \
  "v_mfma_f32_16x16x32_bf16 v[132:135], v[188:191], v[72:75], v[132:135]\n\t" \
  "v_mfma_f32_16x16x32_bf16 v[140:143], v[188:191], v[76:79], v[140:143]\n\t" \
  "v_mfma_f32_16x16x32_bf16 v[128:131], v[192:195], v[80:83], v[128:131]\n\t" \
  "v_mfma_f32_16x16x32_bf16 v[136:139], v[192:195], v[84:87], v[136:139]\n\t" \
  "v_mfma_f32_16x16x32_bf16 v[132:135], v[196:199], v[80:83], v[132:135]\n\t" \
  "v_mfma_f32_16x16x32_bf16 v[140:143], v[196:199], v[84:87], v[140:143]\n\t" \
  "v_mfma_f32_16x16x32_bf16 v[128:131], v[200:203], v[88:91], v[128:131]\n\t" \
  "v_mfma_f32_16x16x32_bf16 v[136:139], v[200:203], v[92:95], v[136:139]\n\t" \
  "v_mfma_f32_16x16x32_bf16 v[132:135], v[204:207], v[88:91], v[132:135]\n\t" \
  "v_mfma_f32_16x16x32_bf16 v[140:143], v[204:207], v[92:95], v[140:143]\n\t" \
  "v_mfma_f32_16x16x32_bf16 v[128:131], v[208:211], v[96:99], v[128:131]\n\t" \
  "v_mfma_f32_16x16x32_bf16 v[136:139], v[208:211], v[100:103], v[136:139]\n\t" \
  "v_mfma_f32_16x16x32_bf16 v[132:135], v[212:215], v[96:99], v[132:135]\n\t" \
  "v_mfma_f32_16x16x32_bf16 v[140:143], v[212:215], v[100:103], v[140:143]\n\t" \
  "v_mfma_f32_16x16x32_bf16 v[128:131], v[216:219], v[104:107], v[128:131]\n\t" \
  "v_mfma_f32_16x16x32_bf16 v[136:139], v[216:219], v[108:111], v[136:139]\n\t" \
  "v_mfma_f32_16x16x32_bf16 v[132:135], v[220:223], v[104:107], v[132:135]\n\t" \
  "v_mfma_f32_16x16x32_bf16 v[140:143], v[220:223], v[108:111], v[140:143]\n\t" \
  "v_mfma_f32_16x16x32_bf16 v[128:131], v[224:227], v[112:115], v[128:131]\n\t" \
  "v_mfma_f32_16x16x32_bf16 v[136:139], v[224:227], v[116:119], v[136:139]\n\t" \
  "v_mfma_f32_16x16x32_bf16 v[132:135], v[228:231], v[112:115], v[132:135]\n\t" \
  "v_mfma_f32_16x16x32_bf16 v[140:143], v[228:231], v[116:119], v[140:143]\n\t" \
  "v_mfma_f32_16x16x32_bf16 v[128:131], v[232:235], v[120:123], v[128:131]\n\t" \
  "v_mfma_f32_16x16x32_bf16 v[136:139], v[232:235], v[124:127], v[136:139]\n\t" \
  "v_mfma_f32_16x16x32_bf16 v[132:135], v[236:239], v[120:123], v[132:135]\n\t" \
  "v_mfma_f32_16x16x32_bf16 v[140:143], v[236:239], v[124:127], v[140:143]\n\t" \
  "s_setprio 0\n\t" \
  "s_add_u32 s92, s92, 512\n\t" \
  "s_addc_u32 s93, s93, 0\n\t" \
  "ds_read_b128 v[176:179], v244 offset:0\n\t" \
  "ds_read_b128 v[180:183], v244 offset:16384\n\t" \
  "ds_read_b128 v[184:187], v245 offset:0\n\t" \
  "ds_read_b128 v[188:191], v245 offset:16384\n\t" \
  "s_add_u32 m0, %[lw], 32768\n\t" \
  "s_nop 0\n\t" \
  "global_load_lds_dwordx4 v248, s[92:93] sc1\n\t" \
  "ds_read_b128 v[192:195], v246 offset:0\n\t" \
  "ds_read_b128 v[196:199], v246 offset:16384\n\t" \
  "ds_read_b128 v[200:203], v247 offset:0\n\t" \
  "ds_read_b128 v[204:207], v247 offset:16384\n\t" \
  "s_add_u32 m0, %[lw], 33792\n\t" \
  "s_nop 0\n\t" \
  "global_load_lds_dwordx4 v249, s[92:93] sc1\n\t" \
  "ds_read_b128 v[208:211], v244 offset:256\n\t" \
  "ds_read_b128 v[212:215], v244 offset:16640\n\t" \
  "ds_read_b128 v[216:219], v245 offset:256\n\t" \
  "ds_read_b128 v[220:223], v245 offset:16640\n\t" \
  "s_add_u32 m0, %[lw], 34816\n\t" \
  "s_nop 0\n\t" \
  "global_load_lds_dwordx4 v250, s[92:93] sc1\n\t" \
  "ds_read_b128 v[224:227], v246 offset:256\n\t" \
  "ds_read_b128 v[228:231], v246 offset:16640\n\t" \
  "ds_read_b128 v[232:235], v247 offset:256\n\t" \
  "ds_read_b128 v[236:239], v247 offset:16640\n\t" \
  "s_add_u32 m0, %[lw], 35840\n\t" \
  "s_nop 0\n\t" \
  "global_load_lds_dwordx4 v251, s[92:93] sc1\n\t" \
  "ds_write_b128 v252, v[136:139] offset:0\n\t" \
  "ds_write_b128 v252, v[140:143] offset:1024\n\t" \
  "s_waitcnt lgkmcnt(0)\n\t" \
  "s_waitcnt vmcnt(4)\n\t" \
  "s_barrier\n\t" \
  "s_setprio 2\n\t" \
  "v_mfma_f32_16x16x32_bf16 v[144:147], v[176:179], v[0:3], 0\n\t" \
  "v_mfma_f32_16x16x32_bf16 v[152:155], v[176:179], v[4:7], 0\n\t" \
  "v_mfma_f32_16x16x32_bf16 v[148:151], v[180:183], v[0:3], 0\n\t" \
  "v_mfma_f32_16x16x32_bf16 v[156:159], v[180:183], v[4:7], 0\n\t" \
  "v_mfma_f32_16x16x32_bf16 v[144:147], v[184:187], v[8:11], v[144:147]\n\t" \
  "v_mfma_f32_16x16x32_bf16 v[152:155], v[184:187], v[12:15], v[152:155]\n\t" \
  "v_mfma_f32_16x16x32_bf16 v[148:151], v[188:191], v[8:11], v[148:151]\n\t" \
  "v_mfma_f32_16x16x32_bf16 v[156:159], v[188:191], v[12:15], v[156:159]\n\t" \
  "v_mfma_f32_16x16x32_bf16 v[144:147], v[192:195], v[16:19], v[144:147]\n\t" \
  "v_mfma_f32_16x16x32_bf16 v[152:155], v[192:195], v[20:23], v[152:155]\n\t" \
  "v_mfma_f32_16x16x32_bf16 v[148:151], v[196:199], v[16:19], v[148:151]\n\t" \
  "v_mfma_f32_16x16x32_bf16 v[156:159], v[196:199], v[20:23], v[156:159]\n\t" \
  "v_mfma_f32_16x16x32_bf16 v[144:147], v[200:203], v[24:27], v[144:147]\n\t" \
  "v_mfma_f32_16x16x32_bf16 v[152:155], v[200:203], v[28:31], v[152:155]\n\t" \
  "v_mfma_f32_16x16x32_bf16 v[148:151], v[204:207], v[24:27], v[148:151]\n\t" \
  "v_mfma_f32_16x16x32_bf16 v[156:159], v[204:207], v[28:31], v[156:159]\n\t" \
  "v_mfma_f32_16x16x32_bf16 v[144:147], v[208:211], v[32:35], v[144:147]\n\t" \
  "v_mfma_f32_16x16x32_bf16 v[152:155], v[208:211], v[36:39], v[152:155]\n\t" \
  "v_mfma_f32_16x16x32_bf16 v[148:151], v[212:215], v[32:35], v[148:151]\n\t" \
  "v_mfma_f32_16x16x32_bf16 v[156:159], v[212:215], v[36:39], v[156:159]\n\t" \
  "v_mfma_f32_16x16x32_bf16 v[144:147], v[216:219], v[40:43], v[144:147]\n\t" \
  "v_mfma_f32_16x16x32_bf16 v[152:155], v[216:219], v[44:47], v[152:155]\n\t" \
  "v_mfma_f32_16x16x32_bf16 v[148:151], v[220:223], v[40:43], v[148:151]\n\t" \
  "v_mfma_f32_16x16x32_bf16 v[156:159], v[220:223], v[44:47], v[156:159]\n\t" \
  "v_mfma_f32_16x16x32_bf16 v[144:147], v[224:227], v[48:51], v[144:147]\n\t" \
  "v_mfma_f32_16x16x32_bf16 v[152:155], v[224:227], v[52:55], v[152:155]\n\t" \
  "v_mfma_f32_16x16x32_bf16 v[148:151], v[228:231], v[48:51], v[148:151]\n\t" \
  "v_mfma_f32_16x16x32_bf16 v[156:159], v[228:231], v[52:55], v[156:159]\n\t" \
  "v_mfma_f32_16x16x32_bf16 v[144:147], v[232:235], v[56:59], v[144:147]\n\t" \
  "v_mfma_f32_16x16x32_bf16 v[152:155], v[232:235], v[60:63], v[152:155]\n\t" \
  "v_mfma_f32_16x16x32_bf16 v[148:151], v[236:239], v[56:59], v[148:151]\n\t" \
  "v_mfma_f32_16x16x32_bf16 v[156:159], v[236:239], v[60:63], v[156:159]\n\t" \
  "s_setprio 0\n\t" \
  "s_add_u32 s92, s92, 65024\n\t" \
  "s_addc_u32 s93, s93, 0\n\t" \
  "ds_read_b128 v[160:163], v253 offset:0\n\t" \
  "ds_read_b128 v[164:167], v253 offset:1024\n\t" \
  "ds_read_b128 v[176:179], v244 offset:32768\n\t" \
  "ds_read_b128 v[180:183], v244 offset:49152\n\t" \
  "ds_read_b128 v[184:187], v245 offset:32768\n\t" \
  "ds_read_b128 v[188:191], v245 offset:49152\n\t" \
  "s_add_u32 m0, %[lw], 65536\n\t" \
  "s_nop 0\n\t" \
  "global_load_lds_dwordx4 v248, s[92:93] sc1\n\t" \
  "ds_read_b128 v[192:195], v246 offset:32768\n\t" \
  "ds_read_b128 v[196:199], v246 offset:49152\n\t" \
  "ds_read_b128 v[200:203], v247 offset:32768\n\t" \
  "ds_read_b128 v[204:207], v247 offset:49152\n\t" \
  "s_add_u32 m0, %[lw], 66560\n\t" \
  "s_nop 0\n\t" \
  "global_load_lds_dwordx4 v249, s[92:93] sc1\n\t" \
  "ds_read_b128 v[208:211], v244 offset:33024\n\t" \
  "ds_read_b128 v[212:215], v244 offset:49408\n\t" \
  "ds_read_b128 v[216:219], v245 offset:33024\n\t" \
  "ds_read_b128 v[220:223], v245 offset:49408\n\t" \
  "s_add_u32 m0, %[lw], 67584\n\t" \
  "s_nop 0\n\t" \
  "global_load_lds_dwordx4 v250, s[92:93] sc1\n\t" \
  "ds_read_b128 v[224:227], v246 offset:33024\n\t" \
  "ds_read_b128 v[228:231], v246 offset:49408\n\t" \
  "ds_read_b128 v[232:235], v247 offset:33024\n\t" \
  "ds_read_b128 v[236:239], v247 offset:49408\n\t" \
  "s_add_u32 m0, %[lw], 68608\n\t" \
  "s_nop 0\n\t" \
  "global_load_lds_dwordx4 v251, s[92:93] sc1\n\t" \
  "s_waitcnt lgkmcnt(15)\n\t" \
  "s_waitcnt lgkmcnt(12)\n\t" \
  "s_sub_i32 s91, s86, 2\n\t" \
  "v_mov_b32 v173, s91\n\t" \
  "buffer_store_dword v173, v174, %[rs], 0 offen sc1\n\t" \
  "s_lshr_b32 s90, s87, 7\n\t" \
  "s_xor_b32 s90, s90, %[s0]\n\t" \
  "s_and_b32 s90, s90, 1\n\t" \
  "s_lshl1_add_u32 s90, s87, s90\n\t" \
  "s_lshl_b32 s90, s90, 13\n\t" \
  "v_add_f32 v160, v160, v128\n\t" \
  "v_add_f32 v161, v161, v129\n\t" \
  "v_add_f32 v162, v162, v130\n\t" \
  "v_add_f32 v163, v163, v131\n\t" \
  "v_add_f32 v164, v164, v132\n\t" \
  "v_add_f32 v165, v165, v133\n\t" \
  "v_add_f32 v166, v166, v134\n\t" \
  "v_add_f32 v167, v167, v135\n\t" \
  "v_cvt_pk_bf16_f32 v168, v160, v161\n\t" \
  "v_cvt_pk_bf16_f32 v169, v162, v163\n\t" \
  "v_cvt_pk_bf16_f32 v170, v164, v165\n\t" \
  "v_cvt_pk_bf16_f32 v171, v166, v167\n\t" \
  "buffer_store_dwordx4 v[168:171], v172, %[rt], s90 offen sc1\n\t" \
  "s_add_u32 s86, s86, 1\n\t" \
  "s_add_u32 s87, s87, 1\n\t" \
  "s_waitcnt lgkmcnt(0)\n\t" \
  "s_cmp_lt_u32 s86, 64\n\t" \
  "s_cbranch_scc1 .Lp1_%=\n\t" \
  "s_barrier\n\t" \
  "s_setprio 2\n\t" \
  "v_mfma_f32_16x16x32_bf16 v[144:147], v[176:179], v[64:67], v[144:147]\n\t" \
  "v_mfma_f32_16x16x32_bf16 v[152:155], v[176:179], v[68:71], v[152:155]\n\t" \
  "v_mfma_f32_16x16x32_bf16 v[148:151], v[180:183], v[64:67], v[148:151]\n\t" \
  "v_mfma_f32_16x16x32_bf16 v[156:159], v[180:183], v[68:71], v[156:159]\n\t" \
  "v_mfma_f32_16x16x32_bf16 v[144:147], v[184:187], v[72:75], v[144:147]\n\t" \
  "v_mfma_f32_16x16x32_bf16 v[152:155], v[184:187], v[76:79], v[152:155]\n\t" \
  "v_mfma_f32_16x16x32_bf16 v[148:151], v[188:191], v[72:75], v[148:151]\n\t" \
  "v_mfma_f32_16x16x32_bf16 v[156:159], v[188:191], v[76:79], v[156:159]\n\t" \
  "v_mfma_f32_16x16x32_bf16 v[144:147], v[192:195], v[80:83], v[144:147]\n\t" \
  "v_mfma_f32_16x16x32_bf16 v[152:155], v[192:195], v[84:87], v[152:155]\n\t" \
  "v_mfma_f32_16x16x32_bf16 v[148:151], v[196:199], v[80:83], v[148:151]\n\t" \
  "v_mfma_f32_16x16x32_bf16 v[156:159], v[196:199], v[84:87], v[156:159]\n\t" \
  "v_mfma_f32_16x16x32_bf16 v[144:147], v[200:203], v[88:91], v[144:147]\n\t" \
  "v_mfma_f32_16x16x32_bf16 v[152:155], v[200:203], v[92:95], v[152:155]\n\t" \
  "v_mfma_f32_16x16x32_bf16 v[148:151], v[204:207], v[88:91], v[148:151]\n\t" \
  "v_mfma_f32_16x16x32_bf16 v[156:159], v[204:207], v[92:95], v[156:159]\n\t" \
  "v_mfma_f32_16x16x32_bf16 v[144:147], v[208:211], v[96:99], v[144:147]\n\t" \
  "v_mfma_f32_16x16x32_bf16 v[152:155], v[208:211], v[100:103], v[152:155]\n\t" \
  "v_mfma_f32_16x16x32_bf16 v[148:151], v[212:215], v[96:99], v[148:151]\n\t" \
  "v_mfma_f32_16x16x32_bf16 v[156:159], v[212:215], v[100:103], v[156:159]\n\t" \
  "v_mfma_f32_16x16x32_bf16 v[144:147], v[216:219], v[104:107], v[144:147]\n\t" \
  "v_mfma_f32_16x16x32_bf16 v[152:155], v[216:219], v[108:111], v[152:155]\n\t" \
  "v_mfma_f32_16x16x32_bf16 v[148:151], v[220:223], v[104:107], v[148:151]\n\t" \
  "v_mfma_f32_16x16x32_bf16 v[156:159], v[220:223], v[108:111], v[156:159]\n\t" \
  "v_mfma_f32_16x16x32_bf16 v[144:147], v[224:227], v[112:115], v[144:147]\n\t" \
  "v_mfma_f32_16x16x32_bf16 v[152:155], v[224:227], v[116:119], v[152:155]\n\t" \
  "v_mfma_f32_16x16x32_bf16 v[148:151], v[228:231], v[112:115], v[148:151]\n\t" \
  "v_mfma_f32_16x16x32_bf16 v[156:159], v[228:231], v[116:119], v[156:159]\n\t" \
  "v_mfma_f32_16x16x32_bf16 v[144:147], v[232:235], v[120:123], v[144:147]\n\t" \
  "v_mfma_f32_16x16x32_bf16 v[152:155], v[232:235], v[124:127], v[152:155]\n\t" \
  "v_mfma_f32_16x16x32_bf16 v[148:151], v[236:239], v[120:123], v[148:151]\n\t" \
  "v_mfma_f32_16x16x32_bf16 v[156:159], v[236:239], v[124:127], v[156:159]\n\t" \
  "s_setprio 0\n\t" \
  "s_nop 4\n\t" \
  "ds_write_b128 v252, v[152:155] offset:0\n\t" \
  "s_nop 0\n\t" \
  "ds_write_b128 v252, v[156:159] offset:1024\n\t" \
  "s_waitcnt lgkmcnt(0)\n\t" \
  "s_barrier\n\t" \
  "ds_read_b128 v[160:163], v253 offset:0\n\t" \
  "ds_read_b128 v[164:167], v253 offset:1024\n\t" \
  "s_waitcnt lgkmcnt(0)\n\t" \
  "s_lshr_b32 s90, s87, 7\n\t" \
  "s_xor_b32 s90, s90, %[s0]\n\t" \
  "s_and_b32 s90, s90, 1\n\t" \
  "s_lshl1_add_u32 s90, s87, s90\n\t" \
  "s_lshl_b32 s90, s90, 13\n\t" \
  "v_add_f32 v160, v160, v144\n\t" \
  "v_add_f32 v161, v161, v145\n\t" \
  "v_add_f32 v162, v162, v146\n\t" \
  "v_add_f32 v163, v163, v147\n\t" \
  "v_add_f32 v164, v164, v148\n\t" \
  "v_add_f32 v165, v165, v149\n\t" \
  "v_add_f32 v166, v166, v150\n\t" \
  "v_add_f32 v167, v167, v151\n\t" \
  "v_cvt_pk_bf16_f32 v168, v160, v161\n\t" \
  "v_cvt_pk_bf16_f32 v169, v162, v163\n\t" \
  "v_cvt_pk_bf16_f32 v170, v164, v165\n\t" \
  "v_cvt_pk_bf16_f32 v171, v166, v167\n\t" \
  "buffer_store_dwordx4 v[168:171], v172, %[rt], s90 offen sc1\n\t" \
  "s_waitcnt vmcnt(0)\n\t" \
  "s_barrier\n\t" \
  "s_sub_i32 s91, s86, 1\n\t" \
  "v_mov_b32 v173, s91\n\t" \
  "buffer_store_dword v173, v174, %[rs], 0 offen sc1\n\t" \
  "s_mov_b32 m0, s94\n\t" \
  ".Le_%=:\n\t"
constexpr int W_X = 131072, W_PM = W_X + 16384;
template <bool ISV>
__device__ __forceinline__ void job_proj(Args& p, int g, int which, int h, char* smem, __amdgpu_buffer_rsrc_t rsy) {
  const int tid = threadIdx.x, lane = tid & 63, r = lane & 15, q = lane >> 4;
  const int w = __builtin_amdgcn_readfirstlane(tid >> 6), kh = w >> 2, cg = w & 3;
  u32x32 W[4];
  const unsigned lw = lds_off(smem) + w * 4096;
  u32x4 vx;
  u32x2 tag = {0u, 0u};
  const char* xn = p.ws + WS_XN;
  auto row = [&](int t, int i) {
    const char* src = xn + (size_t)(min(t, 127) >> 1) * (SEG * XLD * 2) + (t & 1) * 512;
    unsigned keep;
    asm volatile("s_mov_b32 %0, m0\n\ts_mov_b32 m0, %3\n\ts_nop 0\n\tglobal_load_lds_dwordx4 %1, %2 sc1\n\ts_mov_b32 m0, %0"
                 : "=&s"(keep) : "v"(vx[i]), "s"(src), "s"(lw + (t & 3) * 32768 + i * 1024) : "memory");
  };
  {
    const u16* Wm = which == 0 ? p.Wk : p.Wq;
    asm volatile("" : "+s"(Wm));
    if (ISV) Wm = p.Wv;
    const unsigned vo = (tid >> 4) * (HD * 2) + 16 * ((lane & 15) ^ swzB((tid >> 4) & 15));
    auto stage = [&](int j) {
      const char* src = (const char*)(Wm + (size_t)(WREV ? 7 - j : j) * 64 * HD + h * DH);
      const unsigned dst = lds_off(smem) + (j & 3) * 32768 + w * 1024;
      glds2(src, 32 * HD * 2, vo, dst);
      glds2(src + 512 * HD * 2, 32 * HD * 2, vo, dst + 16384);
    };
    stage(0); stage(1); stage(2); stage(3);
#pragma unroll
    for (int i = 0; i < 4; ++i) vx[i] = ((4 * w + i) ^ (ISV ? 0 : (lane >> 1) & 16)) * (XLD * 2) + (lane >> 5) * 1024 + (((lane & 31) ^ ((4 * w + i) & 15)) << 4);
    Boot bt;
#pragma unroll
    for (int j = 0; j < 8; ++j) {
      if (j < 5) WAIT_V(12); else if (j == 5) WAIT_V(8); else if (j == 6) WAIT_V(4); else WAIT_V(10);
      if (j == 6 && w == 0) { lds_at(smem + W_PM)[0] = boot_done(p.ws, bt); tag = next_tag(bt.e); }
      LBAR();
      const char* sb = smem + (j & 3) * 32768 + kh * 16384;
#pragma unroll
      for (int k = 0; k < 4; ++k) {
        const int ks = k >> 1, a = k & 1;
        const unsigned ch = ISV ? 2 * (2 * cg + (a ^ kh)) + ((r & 3) >> 1) : 4 * cg + (r & 3), by = ISV ? 8 * (r & 1) : 8 * a;
        s16x4 lo = __builtin_amdgcn_ds_read_tr16_b64_v4i16((lds_s16x4_ptr)(sb + offB(32 * ks + 8 * q + (r >> 2), ch) + by));
        s16x4 hi = __builtin_amdgcn_ds_read_tr16_b64_v4i16((lds_s16x4_ptr)(sb + offB(32 * ks + 8 * q + 4 + (r >> 2), ch) + by));
        const int f = 4 * (WREV ? 7 - j : j) + k;
        put4(W[f >> 3], f & 7, (bf16x8){lo[0], lo[1], lo[2], lo[3], hi[0], hi[1], hi[2], hi[3]});
      }
      LBAR();
      if (j == 3 && w == 0) boot_ask(p.ws, bt);
      if (j + 4 < 8) stage(j + 4);
      if (j == 6) {
#pragma unroll
        for (int i = 0; i < 10; ++i) row(i >> 2, i & 3);
      }
    }
    p.s0 = uni(lds_at(smem + W_PM)[0] >> 31);
  }
  typedef __attribute__((ext_vector_type(8))) unsigned u32x8;
  u32x8 la;
#pragma unroll
  for (int i = 0; i < 8; ++i) la[i] = (i >> 2) * 65536 + r * 1024 + kh * 512 + (((4 * (i & 3) + q) ^ r) << 4);
  const unsigned xw = W_X + w * 2048 + lane * 16;
  u32x32 acc;
  asm volatile("" : "={v[128:159]}"(acc));
  WAIT_V(6);
  LBAR();
  {
    const int g0 = 0;
#define PR_LOOP(TXT)                                                                                                              \
    asm volatile(TXT                                                                                                              \
        : "+{v[128:159]}"(acc)                                                                                                    \
        : W4_IN, "{v[208:215]}"(la), "{v[216:219]}"(vx), "{v220}"(xw), "{v221}"(xw ^ 8192),                                       \
          "{v172}"((ISV ? 2 * cg + kh : 4 * kh + cg) * 1024 + lane * 16), "{v174}"(tid == 256 ? fkq(h, which) : 0x7F000000),      \
          [rt] "s"(raw_rsrc(p.ws + WS_T, T_BYTES)), [rs] "s"(raw_rsrc(p.ws + WS_SY, SY_TOTAL)), [s0] "s"(p.s0 ^ TF_KQV),          \
          [lw] "s"(lw), [kh] "s"(kh), [g0] "s"(g0), [end] "s"(g0 ? NSEG : PR_G1), [x0] "s"(xn + (g0 + 1) * (SEG * XLD * 2)),                       \
          [t0] "s"(which * T_Q + kseg(0, h) - 1)                                                                                  \
        : CLB8(160, 161, 162, 163, 164, 165, 166, 167), "v168", "v169", "v170", "v171", "v173", CLB8(176, 177, 178, 179, 180, 181, 182, 183), \
          CLB8(184, 185, 186, 187, 188, 189, 190, 191), CLB8(192, 193, 194, 195, 196, 197, 198, 199),                             \
          CLB8(200, 201, 202, 203, 204, 205, 206, 207), "s86", "s87", "s88", "s90", "s91", "s92", "s93", "s94", "scc", "memory")
    if (ISV) PR_LOOP(PR_ASM_1); else PR_LOOP(PR_ASM_0);
    if (w == 0 && g0 == 0) wait_rows(p.ws, tag);
  }
#define PP_LOOP(TXT)                                                                                                              \
  asm volatile(TXT                                                                                                                \
      : "+{v[128:159]}"(acc)                                                                                                      \
      : W4_IN, "{v[240:247]}"(la), "{v[248:251]}"(vx), "{v252}"(xw), "{v253}"(xw ^ 8192),                                         \
        "{v172}"((ISV ? 2 * cg + kh : 4 * kh + cg) * 1024 + lane * 16), "{v174}"(tid == 256 ? fkq(h, which) : 0x7F000000),        \
        [rt] "s"(raw_rsrc(p.ws + WS_T, T_BYTES)), [rs] "s"(raw_rsrc(p.ws + WS_SY, SY_TOTAL)), [s0] "s"(p.s0 ^ TF_KQV),            \
        [lw] "s"(lw), [kh] "s"(kh), [g0] "n"(PR_G1), [x0] "s"(xn + (PR_G1 + 1) * (SEG * XLD * 2)),                                \
        [t0] "s"(which * T_Q + kseg(0, h) - 1)                                                                                    \
      : CLB8(160, 161, 162, 163, 164, 165, 166, 167), "v168", "v169", "v170", "v171", "v173", CLB8(176, 177, 178, 179, 180, 181, 182, 183), \
        CLB8(184, 185, 186, 187, 188, 189, 190, 191), CLB32(192), CLB8(224, 225, 226, 227, 228, 229, 230, 231),                   \
        CLB8(232, 233, 234, 235, 236, 237, 238, 239), "s86", "s87", "s90", "s91", "s92", "s93", "s94", "scc", "memory")
  if (ISV) PP_LOOP(PP_ASM_1); else PP_LOOP(PP_ASM_0);
}

enum { J_NONE = 0, J_GU = 3, J_PR = 5, J_CB = 8, J_DN = 14 };
struct BigJob { int type, panel, idx; };
struct BigPtr { const char *a, *b0, *b1; };
constexpr int G_LDA = XLD * 2, G_LDB = FF * 2;

__device__ __forceinline__ BigPtr big_ptrs(const Args& p, const BigJob& j) {
  BigPtr r;
  r.a = p.ws + WS_XN + (size_t)j.panel * 128 * G_LDA;
  r.b0 = (const char*)(p.wg + j.idx * 128); r.b1 = (const char*)(p.wu + j.idx * 128);
  return r;
}
__device__ __forceinline__ void big_publish(const Args& p, const BigJob& j) {
  int tid = threadIdx.x;
  asm volatile("" : "+v"(tid));
  if (j.type == J_GU && tid == 0)
    __hip_atomic_fetch_add((gu32*)(p.ws + WS_SY + SY_CH) + j.panel * 16, 1u, __ATOMIC_RELAXED, __HIP_MEMORY_SCOPE_AGENT);
}
typedef f32x4 Acc[2][2][4][2];
__device__ __forceinline__ void big_epilogue(const Args& p, const BigJob& j, Acc& acc, char* smem) {
  int tid = threadIdx.x;
  asm volatile("" : "+v"(tid));
  const int lane = tid & 63;
  const int wid = __builtin_amdgcn_readfirstlane(tid >> 6);
  const int wr = wid >> 2, wc = wid & 3;
  {
    const __amdgpu_buffer_rsrc_t ro = mkrsrc(p.ws + WS_HF, S_LEN * 16384);
#pragma unroll
    for (int i = 0; i < 8; ++i) {
      f32x4 hh[2];
#pragma unroll
      for (int n = 0; n < 2; ++n)
#pragma unroll
        for (int jj = 0; jj < 4; ++jj) {
          const float g = acc[i >> 2][0][i & 3][n][jj], u = acc[i >> 2][1][i & 3][n][jj];
          hh[n][jj] = __builtin_amdgcn_rcpf(1.0f + __builtin_amdgcn_exp2f(g * -1.4426950408889634f)) * u;
        }
      st_sc1(ro, h_off(p, j.panel + (i >> 2)) + (j.idx * 4 + wc) * 16384 + (wr * 4 + (i & 3)) * 1024 + lane * 16,
             ((u32x4){cvtpk(hh[0][0], hh[0][1]), cvtpk(hh[0][2], hh[0][3]), cvtpk(hh[1][0], hh[1][1]), cvtpk(hh[1][2], hh[1][3])}));
    }
  }
}
__device__ __forceinline__ void gemm_role(Args& p, int g, char* smem) {
  const __amdgpu_buffer_rsrc_t rsy = mkrsrc(p.ws + WS_SY, SY_TOTAL);
  const int x = g & 3, k = g >> 2;
  volatile unsigned* slot = reinterpret_cast<volatile unsigned*>(smem + 163824);
  int rnd = k < 24 ? 2 : -1;
  auto claim = [&]() {
    ++rnd;
    const int n = k < 24 ? 32 + k : rnd * 8 + k - 24, pn = (n * 37) >> 8;
    BigJob b;
    b.type = rnd < 4 ? J_GU : (rnd == 4 ? J_DN : (rnd == 5 && k >= 24 && k - 24 < 14 - NFUSE ? J_CB : J_NONE));
    b.panel = rnd < 4 ? 14 - 2 * pn : (rnd == 4 ? 4 * ((2 - (k >> 3)) & 3) + x : NFUSE + k - 24);
    b.idx = rnd < 4 ? x + 4 * (n - 7 * pn) : k & 7;
    return b;
  };
  if (k < 24) {
    if (k >= 16) job_proj<true>(p, g, 2, x * 8 + (k & 7), smem, rsy);
    else job_proj<false>(p, g, k >> 3, x * 8 + (k & 7), smem, rsy);
  } else {
    u32x2 tag;
#pragma unroll 1
    for (int u = 0; u < 4; ++u) {
      tag = start_rows(p, g - 96 + u * 32);
      if (u == 0 || u == 3) {
        WAIT_V(0);
        LBAR();
        report_rows(p, (u ? 32 : 0) + g - 96, tag);
      }
    }
    for (int z = 0; z < PF_T0; ++z) __builtin_amdgcn_s_sleep(16);
#pragma unroll 1
    for (int i = 0; i < PF_N; ++i) {
      const int m = i % 3, o = ((i / 3) * 32 + g - 96) * 448 + (int)threadIdx.x;
      if (threadIdx.x < 448) touch_line((const char*)(m == 0 ? p.wg : (m == 1 ? p.wu : p.wd)) + (size_t)o * 128, smem + (threadIdx.x >> 6) * 256);
      WAIT_V(1);
    }
    WAIT_V(0);
    if (threadIdx.x < 64) {
      wait_rows(p.ws, tag);
      Boot bt;
      boot_ask(p.ws, bt);
      WAIT_V(0);
      *slot = boot_done(p.ws, bt, true, tag);
    }
    LBAR();
    p.s0 = *slot >> 31;
  }
  p.s0 = uni(p.s0);
  BigJob cur = claim();
  while (cur.type != J_NONE) {
    if (cur.type != J_GU) {
      if (cur.type == J_DN) job_down(p, cur.panel, cur.idx, smem, rsy);
      else job_comb(p, cur.panel, x, rsy);
      cur = claim();
      continue;
    }
    int tid = threadIdx.x;
    asm volatile("" : "+v"(tid));
    const int lane = tid & 63;
    const int wid = __builtin_amdgcn_readfirstlane(tid >> 6);
    const int wr = wid >> 2, wc = wid & 3;
    const unsigned ra = tid >> 3, ca = 16 * ((tid & 7) ^ ((tid >> 3) & 7)), rb = tid >> 4, cb = 16 * ((tid & 15) ^ swzB((tid >> 4) & 15));
    const unsigned lw = lds_off(smem) + wid * 1024;
#define B8_SA(b, h) (((b) * 2 + (h)) * 16384)
#define B8_SB(b, h) ((4 + (b) * 2 + (h)) * 16384)
#define B8_STA(b, h, P, kt) glds2<true>((P).a + (size_t)(h) * 128 * G_LDA + (kt) * 128, 64 * G_LDA, ra * G_LDA + ca, lw + B8_SA(b, h))
#define B8_STB(b, h, P, kt) glds2(((h) ? (P).b1 : (P).b0) + (size_t)(kt) * 64 * G_LDB, 32 * G_LDB, rb * G_LDB + cb, lw + B8_SB(b, h))
#define B8_LDA(b, h) do { _Pragma("unroll") for (int m = 0; m < 4; ++m) _Pragma("unroll") for (int kk = 0; kk < 2; ++kk) \
      At[m][kk] = ldsf(smem + B8_SA(b, h) + offA(wr * 64 + m * 16 + (lane & 15), kk * 4 + (lane >> 4))); } while (0)
#define B8_LDB(dst, b, h) do { _Pragma("unroll") for (int n = 0; n < 2; ++n) _Pragma("unroll") for (int kk = 0; kk < 2; ++kk) \
      dst[n][kk] = ld_bfrag(smem + B8_SB(b, h), kk, wc * 2 + n, lane); } while (0)
#define B8_MMA(ai, bj, Bt) do { _Pragma("unroll") for (int m = 0; m < 4; ++m) _Pragma("unroll") for (int n = 0; n < 2; ++n) \
      _Pragma("unroll") for (int kk = 0; kk < 2; ++kk) acc[ai][bj][m][n] = MFMA(Bt[n][kk], At[m][kk], acc[ai][bj][m][n]); } while (0)
#define WAIT_L(n) asm volatile("s_waitcnt lgkmcnt(%0)" :: "n"(n) : "memory")
#define BAR() __builtin_amdgcn_s_barrier()
#define BAR_1() do { SCHED(); asm volatile("s_setprio 1\n\ts_cmp_eq_u32 %0, 0\n\ts_cbranch_scc1 .Lb_%=\n\ts_setprio 2\n\ts_barrier\n.Lb_%=:" :: "s"(wr) : "memory", "scc"); SCHED(); } while (0)
#define BAR_0() do { SCHED(); asm volatile("s_setprio 0\n\ts_cmp_lg_u32 %0, 0\n\ts_cbranch_scc1 .Lb_%=\n\ts_barrier\n.Lb_%=:" :: "s"(wr) : "memory", "scc"); SCHED(); } while (0)
#define WAIT_RX(c) asm volatile("s_waitcnt vmcnt(16)\n\ts_cmp_lg_u32 %0, 0\n\ts_cbranch_scc1 .Lrx_%=\n\ts_waitcnt vmcnt(8)\n.Lrx_%=:" :: "s"(c) : "memory", "scc")
    Acc acc;
    bf16x8 At[4][2], B0[2][2], B1[2][2];
    BigPtr cp = big_ptrs(p, cur);
    BigJob pend; pend.type = J_NONE; pend.panel = 0; pend.idx = 0;
    LBAR();
    B8_STB(0, 0, cp, 0); B8_STB(0, 1, cp, 0); B8_STA(0, 0, cp, 0); B8_STA(0, 1, cp, 0);
    B8_STB(1, 0, cp, 1); B8_STA(1, 0, cp, 1); B8_STB(1, 1, cp, 1);
    WAIT_V(8); BAR();
    BigJob nx = pend;
    for (;;) {
      bool chain = false;
      BigPtr np = cp;
      constexpr int nt = DM / 64;
#pragma unroll
      for (int i = 0; i < 32; ++i) acc[i >> 4][(i >> 3) & 1][(i >> 1) & 3][i & 1] = (f32x4){0.f, 0.f, 0.f, 0.f};
#pragma unroll 1
      for (int t = 0; t < nt; t += 2) {
        const bool last = t == nt - 2;
        if (last) {
          nx = claim();
          chain = nx.type == J_GU;
          if (chain) np = big_ptrs(p, nx);
        }
        const BigPtr sp = np;
        const int t2 = last ? 0 : t + 2;
        if (t == 2) big_publish(p, pend);
        const int rx = __builtin_amdgcn_readfirstlane(t == 0 && pend.type != J_NONE);
        B8_LDB(B0, 0, 0); B8_LDB(B1, 0, 1); SCHED(); B8_LDA(0, 0); B8_STA(1, 1, cp, t + 1);
        WAIT_RX(rx);
        WAIT_L(0); BAR_1(); B8_MMA(0, 0, B0); B8_MMA(0, 1, B1); BAR_0();
        B8_LDA(0, 1); B8_STB(0, 0, sp, t2); B8_STB(0, 1, sp, t2); B8_STA(0, 0, sp, t2);
        WAIT_RX(rx);
        WAIT_L(0); BAR_1(); B8_MMA(1, 0, B0); B8_MMA(1, 1, B1); BAR_0();
        B8_LDB(B0, 1, 0); B8_LDB(B1, 1, 1); SCHED(); B8_LDA(1, 0); B8_STA(0, 1, sp, t2);
        WAIT_V(8); WAIT_L(0); BAR_1(); B8_MMA(0, 0, B0); B8_MMA(0, 1, B1); BAR_0();
        B8_LDA(1, 1); B8_STB(1, 0, sp, t2 + 1); B8_STB(1, 1, sp, t2 + 1); B8_STA(1, 0, sp, t2 + 1);
        WAIT_V(8); WAIT_L(0); BAR_1(); B8_MMA(1, 0, B0); B8_MMA(1, 1, B1); BAR_0();
      }
      big_epilogue(p, cur, acc, smem);
      if (!chain) break;
      pend = cur; cur = nx; cp = np;
    }
    WAIT_V(0);
    BAR();
    asm volatile(".Lcode_end:");
    big_publish(p, cur);
    cur = nx;
  }
}


__device__ __forceinline__ unsigned now_q() { return (unsigned)__builtin_amdgcn_s_memrealtime() * 4u; }
__device__ __forceinline__ void pace_until(unsigned t) {
#pragma unroll 1
  for (int i = 0; i < 256; ++i) {
    const int d = (int)(t - now_q());
    if (d <= 0) break;
    if (d > 32) __builtin_amdgcn_s_sleep(1);
  }
}
#define CLB_E "s76", "s77", "s78", "s79", "s80", "s81", "s82", "s83", "s84", "s85", "s86", "s87", "s88", "s89"
#define EDGE_ASM \
  ".p2align 5\n\t" \
  "s_memtime s[76:77]\n\t" \
  "s_memrealtime s[78:79]\n\t" \
  "s_nop 3\n\t" \
  "s_memrealtime s[80:81]\n\t" \
  "s_nop 3\n\t" \
  "s_memrealtime s[82:83]\n\t" \
  "s_nop 3\n\t" \
  "s_memrealtime s[84:85]\n\t" \
  "s_nop 3\n\t" \
  "s_memrealtime s[86:87]\n\t" \
  "s_nop 3\n\t" \
  "s_memrealtime s[88:89]\n\t" \
  "s_mov_b32 s93, 0\n\t" \
  "s_waitcnt lgkmcnt(0)\n\t" \
  "s_cmp_eq_u32 s80, s78\n\t" \
  "s_addc_u32 s93, s93, 0\n\t" \
  "s_cmp_eq_u32 s82, s78\n\t" \
  "s_addc_u32 s93, s93, 0\n\t" \
  "s_cmp_eq_u32 s84, s78\n\t" \
  "s_addc_u32 s93, s93, 0\n\t" \
  "s_cmp_eq_u32 s86, s78\n\t" \
  "s_addc_u32 s93, s93, 0\n\t" \
  "s_cmp_eq_u32 s88, s78\n\t" \
  "s_addc_u32 s93, s93, 0\n\t" \
  "s_mul_i32 s93, s93, 21\n\t" \
  "s_add_u32 s76, s76, 15\n\t" \
  "s_add_u32 %[kc], s76, s93\n\t" \
  "s_lshl2_add_u32 %[kq], s78, 16\n\t"
__device__ __forceinline__ unsigned time_of(unsigned cyc) {
  unsigned c, q;
  asm volatile(EDGE_ASM : [kc] "=&s"(c), [kq] "=&s"(q) :: CLB_E, "s93", "scc");
  return q - (((c - cyc) * 11900u) >> 16);
}
#define DS_RED 16
#define DS_MEM 11
#define LEAD_DN 2
#define LEAD_UP 6
__device__ __forceinline__ u32x2 ld2_sc1(__amdgpu_buffer_rsrc_t rs, int off, int soff = 0) {
  return __builtin_amdgcn_raw_buffer_load_b64(rs, off, soff, 16);
}
__device__ __forceinline__ void st2_sc1(__amdgpu_buffer_rsrc_t rs, int off, u32x2 v) {
  __builtin_amdgcn_raw_buffer_store_b64(v, rs, off, 0, 16);
}
__device__ __forceinline__ int row_max(int v) {
  v = max(v, __builtin_amdgcn_update_dpp(0, v, 0x128, 0xf, 0xf, false));
  v = max(v, __builtin_amdgcn_update_dpp(0, v, 0x124, 0xf, 0xf, false));
  v = max(v, __builtin_amdgcn_update_dpp(0, v, 0x122, 0xf, 0xf, false));
  v = max(v, __builtin_amdgcn_update_dpp(0, v, 0x121, 0xf, 0xf, false));
  return v;
}
__device__ __forceinline__ int wave_max(int v) {
  v = row_max(v);
  return max(max(__builtin_amdgcn_readlane(v, 0), __builtin_amdgcn_readlane(v, 16)),
             max(__builtin_amdgcn_readlane(v, 32), __builtin_amdgcn_readlane(v, 48)));
}
__device__ __forceinline__ int track(int est, int x, int up, int dn, int out) {
  const int d = x - est;
  return est + (d > out ? 4 : max(min(d, up), -dn));
}
#define WSL 12
#define PLAN_SLEEP 2
#define DS_N0 3
#define DS_X0 20
#define REF_W 3
#define REF_G 60
#define RL0 310
__device__ __forceinline__ void watch(__amdgpu_buffer_rsrc_t rsy, int s, int lane, unsigned t0, const unsigned* tgt_, gu32* abortw, int hs) {
  const int rl = (int)__builtin_amdgcn_raw_buffer_load_b32(rsy, lane * 128, SY_BR + hs, 16);
  u32x2 ra;
  for (unsigned spins = 0;;) {
    asm volatile("" ::: "memory");
    ra = ld2_sc1(rsy, (lane & 31) * 128, SY_BA + hs);
    const bool all = __all((int)ra[0] >= s);
    if (all) break;
    if (spins < 3) __builtin_amdgcn_s_sleep(PLAN_SLEEP); else __builtin_amdgcn_s_sleep(WSL);
    if (spin_fail(spins, abortw)) break;
  }
  int d = ra[0] == (unsigned)s ? (int)(ra[1] - t0) : 0;
  d = max(d, __builtin_amdgcn_update_dpp(0, d, 0xB1, 0xf, 0xf, false));
  d = max(d, __builtin_amdgcn_update_dpp(0, d, 0x4E, 0xf, 0xf, false));
  int sum = d + __builtin_amdgcn_update_dpp(0, d, 0x128, 0xf, 0xf, false);
  sum += __builtin_amdgcn_update_dpp(0, sum, 0x124, 0xf, 0xf, false);
  const int mx = wave_max(d), mean = (__builtin_amdgcn_readlane(sum, 0) + __builtin_amdgcn_readlane(sum, 16)) >> 3;
  const unsigned t = t0 + mx - min(((mx - mean) * REF_W) >> 2, REF_G) + wave_max(rl < 0 ? RL0 : rl) + (DS_RED + DS_MEM) + (s < DS_N0 ? DS_X0 : 0);
  lds_vu_ptr tgt = lds_at(tgt_);
  if (lane < 2) tgt[lane] = lane ? t : s + 1;
}
__device__ __forceinline__ unsigned ld_flag(__amdgpu_buffer_rsrc_t rs, int off) {
  return __builtin_amdgcn_raw_buffer_load_b32(rs, off, 0, 16);
}

constexpr unsigned long long QK_MAP[8] = {0xbaf9876d543210ecull, 0xa98f76d543210bceull, 0x98f76d543210ecbaull, 0x876fd543210bcea9ull, 0x76fd543210ecba98ull, 0x65df43210bcea987ull, 0x5df43210ecba9876ull, 0xd43f210bcea98765ull};
#define QK_ASM_0 \
  "s_waitcnt lgkmcnt(0)\n\t" \
  "v_subrev_u32 v244, 0x8000, v250\n\t" \
  "ds_read_b128 v[216:219], v248 offset:32768\n\t" \
  "ds_read_b128 v[220:223], v248 offset:33792\n\t" \
  "ds_read_b128 v[224:227], v248 offset:34816\n\t" \
  "ds_read_b128 v[228:231], v248 offset:35840\n\t" \
  "ds_read_b128 v[208:211], v244 offset:0\n\t" \
  "ds_read_b128 v[212:215], v244 offset:1024\n\t" \
  "ds_read_b128 v[236:239], v244 offset:2048\n\t" \
  "ds_read_b128 v[240:243], v244 offset:3072\n\t" \
  "v_mov_b64 v[192:193], 0\n\t" \
  "v_mov_b64 v[194:195], 0\n\t" \
  "v_mov_b64 v[196:197], 0\n\t" \
  "v_mov_b64 v[198:199], 0\n\t" \
  ".p2align 5\n\t" \
  "s_nop 0\n\t" \
  "s_nop 0\n\t" \
  "s_nop 0\n\t" \
  "s_nop 0\n\t" \
  "s_movk_i32 s92, 0x2000\n\t" \
  ".Lwt_%=:\n\t" \
  "ds_read_b64 v[200:201], v248 offset:30720\n\t" \
  "s_memrealtime s[90:91]\n\t" \
  "s_sub_u32 s92, s92, 1\n\t" \
  "s_waitcnt lgkmcnt(0)\n\t" \
  "v_readfirstlane_b32 s93, v200\n\t" \
  "v_readfirstlane_b32 %[tgt], v201\n\t" \
  "s_cbranch_scc1 .Lgo_%=\n\t" \
  "s_cmp_eq_u32 s93, %[step]\n\t" \
  "s_cbranch_scc1 .Lwx_%=\n\t" \
  "s_sleep 1\n\t" \
  "s_branch .Lwt_%=\n\t" \
  ".Lwx_%=:\n\t" \
  "s_lshl_b32 s93, s90, 2\n\t" \
  "s_sub_i32 s93, %[tgt], s93\n\t" \
  "s_cmp_le_i32 s93, 0\n\t" \
  "s_cbranch_scc1 .Lgo_%=\n\t" \
  "s_min_i32 s93, s93, 8000\n\t" \
  "s_mov_b32 s94, s93\n\t" \
  ".Lsl_%=:\n\t" \
  "s_sub_i32 s94, s94, 18\n\t" \
  "s_cmp_lt_i32 s94, 30\n\t" \
  "s_cbranch_scc1 .Lsx_%=\n\t" \
  "s_sleep 1\n\t" \
  "s_branch .Lsl_%=\n\t" \
  ".Lsx_%=:\n\t" \
  "s_sub_i32 s93, s93, 12\n\t" \
  "s_max_i32 s93, s93, 4\n\t" \
  "s_lshr_b32 s93, s93, 2\n\t" \
  "s_add_u32 s93, s93, s90\n\t" \
  "s_memrealtime s[90:91]\n\t" \
  ".Lpace_%=:\n\t" \
  "s_memrealtime s[94:95]\n\t" \
  "s_waitcnt lgkmcnt(1)\n\t" \
  "s_sub_i32 s92, s90, s93\n\t" \
  "s_cmp_ge_i32 s92, 0\n\t" \
  "s_cbranch_scc1 .Le_%=\n\t" \
  "s_memrealtime s[90:91]\n\t" \
  "s_waitcnt lgkmcnt(1)\n\t" \
  "s_sub_i32 s92, s94, s93\n\t" \
  "s_cmp_lt_i32 s92, 0\n\t" \
  "s_cbranch_scc1 .Lpace_%=\n\t" \
  "s_nop 4\n\t" \
  ".Le_%=:\n\t" \
  "s_add_u32 s92, s92, s93\n\t" \
  "s_lshl_b32 s92, s92, 2\n\t" \
  "s_sub_i32 s92, %[tgt], s92\n\t" \
  "s_max_i32 s92, s92, 0\n\t" \
  "s_bitcmp0_b32 s92, 3\n\t" \
  "s_cbranch_scc1 .Lf3_%=\n\t" \
  "s_nop 15\n\t" \
  ".Lf3_%=:\n\t" \
  "s_bitcmp0_b32 s92, 2\n\t" \
  "s_cbranch_scc1 .Lf2_%=\n\t" \
  "s_nop 10\n\t" \
  ".Lf2_%=:\n\t" \
  ".Lgo_%=:\n\t" \
  "s_mov_b32 s92, 0\n\t" \
  ".Lpoll_%=:\n\t" \
  "buffer_load_dwordx4 v[200:203], v248, %[rs], %[som] offen sc1\n\t" \
  "buffer_load_dwordx4 v[204:207], v248, %[rs], %[som2] offen sc1\n\t" \
  ".p2align 5\n\t" \
  "s_waitcnt vmcnt(1) lgkmcnt(0)\n\t" \
  "ds_write_b128 v249, v[200:203]\n\t" \
  "v_smfmac_f32_16x16x64_bf16 v[192:195], v[200:203], v[208:215], v251\n\t" \
  "v_max3_u32 v244, v200, v201, v202\n\t" \
  "v_smfmac_f32_16x16x64_bf16 v[196:199], v[200:203], v[236:243], v251\n\t" \
  "s_waitcnt vmcnt(0)\n\t" \
  "ds_write_b128 v249, v[204:207] offset:8192\n\t" \
  "v_smfmac_f32_16x16x64_bf16 v[192:195], v[204:207], v[216:223], v251\n\t" \
  "v_max3_u32 v244, v244, v203, v204\n\t" \
  "v_max3_u32 v244, v244, v205, v206\n\t" \
  "v_smfmac_f32_16x16x64_bf16 v[196:199], v[204:207], v[224:231], v251\n\t" \
  "v_max_u32 v244, v244, v207\n\t" \
  "v_cmp_le_u32_e64 vcc, %[sent], v244\n\t" \
  "s_cbranch_vccnz .Lslow_%=\n\t" \
  ".Lok_%=:\n\t" \
  "ds_read_b128 v[216:219], v250 offset:32768\n\t" \
  "ds_read_b128 v[220:223], v250 offset:33792\n\t" \
  "ds_read_b128 v[224:227], v250 offset:34816\n\t" \
  "ds_read_b128 v[228:231], v250 offset:35840\n\t" \
  "s_waitcnt lgkmcnt(4)\n\t" \
  "s_barrier\n\t" \
  "ds_read_b128 v[208:211], v248 offset:1024\n\t" \
  "ds_read_b128 v[212:215], v248 offset:2048\n\t" \
  "ds_read_b128 v[236:239], v248 offset:3072\n\t" \
  "ds_read_b128 v[240:243], v248 offset:4096\n\t" \
  "s_cmp_eq_u32 %[step], 63\n\t" \
  "s_cbranch_scc1 .Lls_%=\n\t" \
  "s_waitcnt lgkmcnt(3)\n\t" \
  "ds_read_b128 v[244:247], v248 offset:5120\n\t" \
  "v_smfmac_f32_16x16x64_bf16 v[192:195], v[208:211], v[216:223], v251\n\t" \
  "ds_read_b128 v[200:203], v248 offset:13312\n\t" \
  "v_smfmac_f32_16x16x64_bf16 v[196:199], v[208:211], v[224:231], v251\n\t" \
  "s_waitcnt lgkmcnt(4)\n\t" \
  "ds_read_b128 v[216:219], v250 offset:0\n\t" \
  "v_smfmac_f32_16x16x64_bf16 v[192:195], v[212:215], v[0:7], v251\n\t" \
  "ds_read_b128 v[220:223], v250 offset:1024\n\t" \
  "v_smfmac_f32_16x16x64_bf16 v[196:199], v[212:215], v[8:15], v251\n\t" \
  "s_waitcnt lgkmcnt(5)\n\t" \
  "ds_read_b128 v[224:227], v250 offset:2048\n\t" \
  "v_smfmac_f32_16x16x64_bf16 v[192:195], v[236:239], v[16:23], v251\n\t" \
  "ds_read_b128 v[228:231], v250 offset:3072\n\t" \
  "v_smfmac_f32_16x16x64_bf16 v[196:199], v[236:239], v[24:31], v251\n\t" \
  "s_waitcnt lgkmcnt(5)\n\t" \
  "ds_read_b128 v[204:207], v248 offset:6144\n\t" \
  "v_smfmac_f32_16x16x64_bf16 v[192:195], v[240:243], v[32:39], v251\n\t" \
  "ds_read_b128 v[208:211], v248 offset:7168\n\t" \
  "v_smfmac_f32_16x16x64_bf16 v[196:199], v[240:243], v[40:47], v251\n\t" \
  "ds_read_b128 v[212:215], v248 offset:9216\n\t" \
  "v_smfmac_f32_16x16x64_bf16 v[192:195], v[244:247], v[48:55], v251\n\t" \
  "ds_read_b128 v[236:239], v248 offset:10240\n\t" \
  "v_smfmac_f32_16x16x64_bf16 v[196:199], v[244:247], v[56:63], v251\n\t" \
  "s_waitcnt lgkmcnt(6)\n\t" \
  "v_smfmac_f32_16x16x64_bf16 v[192:195], v[200:203], v[216:223], v251\n\t" \
  "s_waitcnt lgkmcnt(4)\n\t" \
  "v_smfmac_f32_16x16x64_bf16 v[196:199], v[200:203], v[224:231], v251\n\t" \
  "s_waitcnt lgkmcnt(3)\n\t" \
  "ds_read_b128 v[240:243], v248 offset:11264\n\t" \
  "v_smfmac_f32_16x16x64_bf16 v[192:195], v[204:207], v[64:71], v251\n\t" \
  "ds_read_b128 v[244:247], v248 offset:12288\n\t" \
  "v_smfmac_f32_16x16x64_bf16 v[196:199], v[204:207], v[72:79], v251\n\t" \
  "s_waitcnt lgkmcnt(3)\n\t" \
  "v_smfmac_f32_16x16x64_bf16 v[192:195], v[208:211], v[80:87], v251\n\t" \
  "ds_read_b128 v[200:203], v248 offset:14336\n\t" \
  "v_smfmac_f32_16x16x64_bf16 v[196:199], v[208:211], v[88:95], v251\n\t" \
  "ds_read_b128 v[204:207], v248 offset:15360\n\t" \
  "v_smfmac_f32_16x16x64_bf16 v[192:195], v[212:215], v[96:103], v251\n\t" \
  "v_smfmac_f32_16x16x64_bf16 v[196:199], v[212:215], v[104:111], v251\n\t" \
  "s_waitcnt lgkmcnt(3)\n\t" \
  "v_smfmac_f32_16x16x64_bf16 v[192:195], v[236:239], v[112:119], v251\n\t" \
  "v_smfmac_f32_16x16x64_bf16 v[196:199], v[236:239], v[120:127], v251\n\t" \
  "v_smfmac_f32_16x16x64_bf16 v[192:195], v[240:243], v[128:135], v251\n\t" \
  "v_smfmac_f32_16x16x64_bf16 v[196:199], v[240:243], v[136:143], v251\n\t" \
  "s_waitcnt lgkmcnt(0)\n\t" \
  "v_smfmac_f32_16x16x64_bf16 v[192:195], v[244:247], v[144:151], v251\n\t" \
  "v_smfmac_f32_16x16x64_bf16 v[196:199], v[244:247], v[152:159], v251\n\t" \
  "v_smfmac_f32_16x16x64_bf16 v[192:195], v[200:203], v[160:167], v251\n\t" \
  "v_smfmac_f32_16x16x64_bf16 v[196:199], v[200:203], v[168:175], v251\n\t" \
  "v_smfmac_f32_16x16x64_bf16 v[192:195], v[204:207], v[176:183], v251\n\t" \
  "v_smfmac_f32_16x16x64_bf16 v[196:199], v[204:207], v[184:191], v251\n\t" \
  "ds_read_b128 v[200:203], v248 offset:16384\n\t" \
  "ds_read_b128 v[204:207], v248 offset:20480\n\t" \
  "v_mov_b32 v250, v232\n\t" \
  "v_mov_b32 v249, v233\n\t" \
  "s_nop 2\n\t" \
  "v_add_f32 v192, v192, v194\n\t" \
  "v_add_f32 v193, v193, v195\n\t" \
  "v_add_f32 v196, v196, v198\n\t" \
  "v_add_f32_e64 v197, v197, v199\n\t" \
  "v_cvt_pk_bf16_f32 v192, v192, v193\n\t" \
  "v_cvt_pk_bf16_f32 v196, v196, v197\n\t" \
  "ds_write2_b32 v252, v192, v196 offset1:64\n\t" \
  "v_mov_b32_e64 v251, v234\n\t" \
  "ds_read_b64_tr_b16 v[232:233], v250 offset:0\n\t" \
  "ds_read_b64_tr_b16 v[234:235], v250 offset:64\n\t" \
  "v_mov_b32_e64 v212, %[step]\n\t" \
  "s_mov_b64 exec, 1\n\t" \
  "ds_write_b32 v248, v212 offset:28672\n\t" \
  "s_mov_b64 exec, -1\n\t" \
  "buffer_load_dwordx4 v[192:195], v248, %[rt], %[svt] offen offset:0 sc1\n\t" \
  "buffer_load_dwordx4 v[196:199], v248, %[rt], %[svt] offen offset:1024 sc1\n\t" \
  "s_waitcnt lgkmcnt(1)\n\t" \
  "v_mfma_f32_16x16x32_bf16 v[200:203], v[200:203], v[232:235], 0\n\t" \
  "v_mfma_f32_16x16x32_bf16 v[204:207], v[204:207], v[232:235], 0\n\t" \
  "s_nop 7\n\t" \
  "v_cndmask_b32 v216, v200, v204, %[nh]\n\t" \
  "v_cndmask_b32 v217, v201, v205, %[nh]\n\t" \
  "v_cndmask_b32 v218, v202, v206, %[nh]\n\t" \
  "v_cndmask_b32 v219, v203, v207, %[nh]\n\t" \
  "ds_write_b128 v248, v[216:219] offset:16384\n\t" \
  "buffer_load_dword v252, v249, %[rs], %[sv] offen offset:0 sc1\n\t" \
  "buffer_load_dword v253, v249, %[rs], %[sv] offen offset:256 sc1\n\t" \
  "ds_read_b32 v212, v248 offset:28672\n\t" \
  "ds_read_b64_tr_b16 v[236:237], v250 offset:512\n\t" \
  "ds_read_b64_tr_b16 v[238:239], v250 offset:576\n\t" \
  "ds_read_b64_tr_b16 v[240:241], v250 offset:1024\n\t" \
  "ds_read_b64_tr_b16 v[242:243], v250 offset:1088\n\t" \
  "ds_read_b64_tr_b16 v[244:245], v250 offset:1536\n\t" \
  "ds_read_b64_tr_b16 v[246:247], v250 offset:1600\n\t" \
  "s_setprio 3\n\t" \
  "s_waitcnt lgkmcnt(0)\n\t" \
  "v_cmp_ne_u32 vcc, %[step], v212\n\t" \
  "s_and_b32 s93, vcc_lo, 0xf\n\t" \
  "s_barrier\n\t" \
  "s_cbranch_scc1 .Lre_%=\n\t" \
  ".Lrb_%=:\n\t" \
  "ds_read_b64_tr_b16 v[200:201], v250 offset:2048\n\t" \
  "ds_read_b64_tr_b16 v[202:203], v250 offset:2112\n\t" \
  "ds_read_b64_tr_b16 v[204:205], v250 offset:2560\n\t" \
  "ds_read_b64_tr_b16 v[206:207], v250 offset:2624\n\t" \
  "ds_read_b64_tr_b16 v[208:209], v250 offset:3072\n\t" \
  "ds_read_b64_tr_b16 v[210:211], v250 offset:3136\n\t" \
  "ds_read_b64_tr_b16 v[212:213], v250 offset:3584\n\t" \
  "ds_read_b64_tr_b16 v[214:215], v250 offset:3648\n\t" \
  "ds_read_b128 v[220:223], v248 offset:17408\n\t" \
  "ds_read_b128 v[224:227], v248 offset:18432\n\t" \
  "ds_read_b128 v[228:231], v248 offset:19456\n\t" \
  "s_waitcnt lgkmcnt(9)\n\t" \
  "v_mfma_f32_16x16x32_bf16 v[200:203], v[200:203], v[232:235], 0\n\t" \
  "s_waitcnt lgkmcnt(7)\n\t" \
  "v_mfma_f32_16x16x32_bf16 v[200:203], v[204:207], v[236:239], v[200:203]\n\t" \
  "v_mov_b32 v233, 0\n\t" \
  "s_waitcnt lgkmcnt(5)\n\t" \
  "v_mfma_f32_16x16x32_bf16 v[200:203], v[208:211], v[240:243], v[200:203]\n\t" \
  "v_mov_b32 v234, 0\n\t" \
  "s_waitcnt lgkmcnt(3)\n\t" \
  "v_mfma_f32_16x16x32_bf16 v[200:203], v[212:215], v[244:247], v[200:203]\n\t" \
  "s_waitcnt lgkmcnt(2)\n\t" \
  "v_pk_add_f32 v[216:217], v[216:217], v[220:221]\n\t" \
  "v_pk_add_f32 v[218:219], v[218:219], v[222:223]\n\t" \
  "s_waitcnt lgkmcnt(0)\n\t" \
  "v_pk_add_f32 v[224:225], v[224:225], v[228:229]\n\t" \
  "v_pk_add_f32 v[226:227], v[226:227], v[230:231]\n\t" \
  "v_pk_add_f32 v[216:217], v[216:217], v[224:225]\n\t" \
  "v_pk_add_f32 v[218:219], v[218:219], v[226:227]\n\t" \
  "v_max3_f32 v224, v216, v217, v218\n\t" \
  "v_max_f32 v224, v224, v219\n\t" \
  "v_max3_f32 v231, v200, v201, v202\n\t" \
  "v_max_f32 v231, v231, v203\n\t" \
  "v_max_f32_dpp v225, v224, v224 row_ror:8 row_mask:0xf bank_mask:0xf\n\t" \
  "v_max_f32_dpp v226, v224, v224 row_ror:8 row_mask:0xf bank_mask:0xf\n\t" \
  "v_cndmask_b32 v200, v200, v202, %[up]\n\t" \
  "v_cndmask_b32 v201, v201, v203, %[up]\n\t" \
  "v_permlane32_swap_b32_e64 v225, v226\n\t" \
  "v_max3_f32 v205, v225, v226, v231\n\t" \
  "v_max3_f32 v206, v225, v226, v231\n\t" \
  "v_mov_b64 v[220:221], %[one]\n\t" \
  "v_mov_b64 v[222:223], %[one]\n\t" \
  "v_permlane16_swap_b32 v205, v206\n\t" \
  "v_max_f32 v204, v205, v206\n\t" \
  "v_mul_f32_e64 v204, %[negc], v204\n\t" \
  "v_fma_f32 v200, v200, %[c], v204\n\t" \
  "v_fma_f32 v201, v201, %[c], v204\n\t" \
  "v_exp_f32 v200, v200\n\t" \
  "v_fma_f32 v216, v216, %[c], v204\n\t" \
  "v_exp_f32 v201, v201\n\t" \
  "v_fma_f32 v217, v217, %[c], v204\n\t" \
  "v_exp_f32_e64 v216, v216\n\t" \
  "v_fma_f32 v218, v218, %[c], v204\n\t" \
  "v_cvt_pk_bf16_f32 v232, v200, v201\n\t" \
  "v_cvt_pk_bf16_f32 v235, v200, v201\n\t" \
  "v_exp_f32 v217, v217\n\t" \
  "v_fma_f32 v219, v219, %[c], v204\n\t" \
  "v_mfma_f32_16x16x16_bf16 v[212:215], v[220:221], v[232:233], 0\n\t" \
  "v_exp_f32 v218, v218\n\t" \
  "s_waitcnt vmcnt(0)\n\t" \
  "v_mfma_f32_16x16x16_bf16 v[224:227], v[252:253], v[232:233], 0\n\t" \
  "v_cvt_pk_bf16_f32 v210, v216, v217\n\t" \
  "v_cvt_pk_bf16_f32 v208, v216, v217\n\t" \
  "v_exp_f32 v219, v219\n\t" \
  "v_mfma_f32_16x16x16_bf16 v[228:231], v[252:253], v[234:235], 0\n\t" \
  "v_cvt_pk_bf16_f32 v211, v218, v219\n\t" \
  "v_cvt_pk_bf16_f32 v209, v218, v219\n\t" \
  "v_mov_b32_dpp v208, v210 row_ror:8 row_mask:0xf bank_mask:0xc\n\t" \
  "v_mov_b32_dpp v210, v210 row_ror:8 row_mask:0xf bank_mask:0x3\n\t" \
  "v_mov_b32_dpp v209, v211 row_ror:8 row_mask:0xf bank_mask:0xc\n\t" \
  "v_mov_b32_dpp v211, v211 row_ror:8 row_mask:0xf bank_mask:0x3\n\t" \
  "v_max_u32 v204, v252, v253\n\t" \
  "v_cmp_le_u32 vcc, %[sent], v204\n\t" \
  "v_mfma_f32_16x16x32_bf16 v[212:215], v[220:223], v[208:211], v[212:215]\n\t" \
  ".Lpv_%=:\n\t" \
  "v_mfma_f32_16x16x32_bf16 v[224:227], v[192:195], v[208:211], v[224:227]\n\t" \
  "v_mfma_f32_16x16x32_bf16 v[228:231], v[196:199], v[208:211], v[228:231]\n\t" \
  "s_cbranch_vccnz .Lvs_%=\n\t" \
  ".Lvok_%=:\n\t" \
  "s_nop 4\n\t" \
  "v_rcp_f32_e64 v204, v212\n\t" \
  "v_cndmask_b32 v224, v224, v226, %[nh]\n\t" \
  "v_cndmask_b32 v225, v225, v227, %[nh]\n\t" \
  "v_cndmask_b32 v228, v228, v230, %[nh]\n\t" \
  "v_cndmask_b32 v229, v229, v231, %[nh]\n\t" \
  "v_pk_mul_f32 v[224:225], v[224:225], v[204:205] op_sel_hi:[1,0]\n\t" \
  "v_pk_mul_f32 v[228:229], v[228:229], v[204:205] op_sel_hi:[1,0]\n\t" \
  "v_cvt_pk_bf16_f32 v224, v224, v225\n\t" \
  "v_cvt_pk_bf16_f32 v225, v228, v229\n\t" \
  "buffer_store_dwordx2 v[224:225], v251, %[rs], %[so] offen sc1\n\t" \
  "s_memtime s[90:91]\n\t" \
  "s_setprio 0\n\t" \
  "s_waitcnt lgkmcnt(0)\n\t" \
  "s_mov_b32 %[tout], s90\n\t" \
  "s_mov_b32 %[spins], s92\n\t" \
  "s_branch .Lend_%=\n\t" \
  ".Lls_%=:\n\t" \
  "s_waitcnt lgkmcnt(0)\n\t" \
  "s_barrier\n\t" \
  "s_mov_b32 %[spins], 0\n\t" \
  "s_branch .Lend_%=\n\t" \
  ".Lre_%=:\n\t" \
  "ds_read_b64_tr_b16 v[236:237], v250 offset:512\n\t" \
  "ds_read_b64_tr_b16 v[238:239], v250 offset:576\n\t" \
  "ds_read_b64_tr_b16 v[240:241], v250 offset:1024\n\t" \
  "ds_read_b64_tr_b16 v[242:243], v250 offset:1088\n\t" \
  "ds_read_b64_tr_b16 v[244:245], v250 offset:1536\n\t" \
  "ds_read_b64_tr_b16 v[246:247], v250 offset:1600\n\t" \
  "s_waitcnt lgkmcnt(0)\n\t" \
  "s_branch .Lrb_%=\n\t" \
  ".Lvs_%=:\n\t" \
  "v_cmp_eq_u32 vcc, %[sent], v252\n\t" \
  "v_cmp_eq_u32 s[94:95], %[sent], v253\n\t" \
  "s_or_b64 s[94:95], s[94:95], vcc\n\t" \
  "s_cbranch_scc0 .Lvok_%=\n\t" \
  "s_add_u32 s92, s92, 1\n\t" \
  "s_sleep 2\n\t" \
  "s_and_b32 s93, s92, 63\n\t" \
  "s_cbranch_scc1 .Lvr_%=\n\t" \
  "buffer_load_dword v204, off, %[rs], %[sab] sc1\n\t" \
  "s_waitcnt vmcnt(0)\n\t" \
  "v_readfirstlane_b32 s93, v204\n\t" \
  "s_nop 3\n\t" \
  "s_cmp_eq_u32 s93, 1\n\t" \
  "s_cbranch_scc1 .Lvok_%=\n\t" \
  "s_cmp_lt_u32 s92, 0x80000\n\t" \
  "s_cbranch_scc1 .Lvr_%=\n\t" \
  "v_mov_b32 v204, 1\n\t" \
  "buffer_store_dword v204, off, %[rs], %[sab] sc1\n\t" \
  "s_branch .Lvok_%=\n\t" \
  ".Lvr_%=:\n\t" \
  "buffer_load_dword v252, v249, %[rs], %[sv] offen offset:0 sc1\n\t" \
  "buffer_load_dword v253, v249, %[rs], %[sv] offen offset:256 sc1\n\t" \
  "s_waitcnt vmcnt(0)\n\t" \
  "v_mfma_f32_16x16x16_bf16 v[224:227], v[252:253], v[232:233], 0\n\t" \
  "v_mfma_f32_16x16x16_bf16 v[228:231], v[252:253], v[234:235], 0\n\t" \
  "v_max_u32 v204, v252, v253\n\t" \
  "v_cmp_le_u32 vcc, %[sent], v204\n\t" \
  "s_nop 4\n\t" \
  "s_branch .Lpv_%=\n\t" \
  "s_branch .Lend_%=\n\t" \
  ".Lslow_%=:\n\t" \
  "s_mov_b64 s[94:95], 0\n\t" \
  "v_cmp_eq_u32 vcc, %[sent], v200\n\t" \
  "s_or_b64 s[94:95], s[94:95], vcc\n\t" \
  "v_cmp_eq_u32 vcc, %[sent], v201\n\t" \
  "s_or_b64 s[94:95], s[94:95], vcc\n\t" \
  "v_cmp_eq_u32 vcc, %[sent], v202\n\t" \
  "s_or_b64 s[94:95], s[94:95], vcc\n\t" \
  "v_cmp_eq_u32 vcc, %[sent], v203\n\t" \
  "s_or_b64 s[94:95], s[94:95], vcc\n\t" \
  "v_cmp_eq_u32 vcc, %[sent], v204\n\t" \
  "s_or_b64 s[94:95], s[94:95], vcc\n\t" \
  "v_cmp_eq_u32 vcc, %[sent], v205\n\t" \
  "s_or_b64 s[94:95], s[94:95], vcc\n\t" \
  "v_cmp_eq_u32 vcc, %[sent], v206\n\t" \
  "s_or_b64 s[94:95], s[94:95], vcc\n\t" \
  "v_cmp_eq_u32 vcc, %[sent], v207\n\t" \
  "s_or_b64 s[94:95], s[94:95], vcc\n\t" \
  "s_cmp_eq_u64 s[94:95], 0\n\t" \
  "s_cbranch_scc1 .Lok_%=\n\t" \
  "v_mov_b64 v[192:193], 0\n\t" \
  "v_mov_b64 v[194:195], 0\n\t" \
  "v_mov_b64 v[196:197], 0\n\t" \
  "v_mov_b64 v[198:199], 0\n\t" \
  "s_add_u32 s92, s92, 1\n\t" \
  "s_cmp_eq_u32 s92, 1\n\t" \
  "s_cbranch_scc1 .Lpoll_%=\n\t" \
  "s_sleep 2\n\t" \
  "s_and_b32 s93, s92, 63\n\t" \
  "s_cmp_lg_u32 s93, 0\n\t" \
  "s_cbranch_scc1 .Lpoll_%=\n\t" \
  "v_mov_b32 v245, 0\n\t" \
  "buffer_load_dword v244, v245, %[rs], %[sab] offen sc1\n\t" \
  "s_waitcnt vmcnt(0)\n\t" \
  "v_readfirstlane_b32 s93, v244\n\t" \
  "s_nop 3\n\t" \
  "s_cmp_eq_u32 s93, 1\n\t" \
  "s_cbranch_scc1 .Lok_%=\n\t" \
  "s_cmp_lt_u32 s92, 0x80000\n\t" \
  "s_cbranch_scc1 .Lpoll_%=\n\t" \
  "v_mov_b32 v244, 1\n\t" \
  "buffer_store_dword v244, v245, %[rs], %[sab] offen sc1\n\t" \
  "s_branch .Lok_%=\n\t" \
  ".Lend_%=:\n\t"
#define QK_ASM_1 \
  "s_waitcnt lgkmcnt(0)\n\t" \
  "v_subrev_u32 v244, 0x8000, v250\n\t" \
  "ds_read_b128 v[216:219], v248 offset:36864\n\t" \
  "ds_read_b128 v[220:223], v248 offset:37888\n\t" \
  "ds_read_b128 v[224:227], v248 offset:38912\n\t" \
  "ds_read_b128 v[228:231], v248 offset:39936\n\t" \
  "ds_read_b128 v[208:211], v244 offset:0\n\t" \
  "ds_read_b128 v[212:215], v244 offset:1024\n\t" \
  "ds_read_b128 v[236:239], v244 offset:2048\n\t" \
  "ds_read_b128 v[240:243], v244 offset:3072\n\t" \
  "v_mov_b64 v[192:193], 0\n\t" \
  "v_mov_b64 v[194:195], 0\n\t" \
  "v_mov_b64 v[196:197], 0\n\t" \
  "v_mov_b64 v[198:199], 0\n\t" \
  ".p2align 5\n\t" \
  "s_nop 0\n\t" \
  "s_nop 0\n\t" \
  "s_nop 0\n\t" \
  "s_nop 0\n\t" \
  "s_movk_i32 s92, 0x2000\n\t" \
  ".Lwt_%=:\n\t" \
  "ds_read_b64 v[200:201], v248 offset:30720\n\t" \
  "s_memrealtime s[90:91]\n\t" \
  "s_sub_u32 s92, s92, 1\n\t" \
  "s_waitcnt lgkmcnt(0)\n\t" \
  "v_readfirstlane_b32 s93, v200\n\t" \
  "v_readfirstlane_b32 %[tgt], v201\n\t" \
  "s_cbranch_scc1 .Lgo_%=\n\t" \
  "s_cmp_eq_u32 s93, %[step]\n\t" \
  "s_cbranch_scc1 .Lwx_%=\n\t" \
  "s_sleep 1\n\t" \
  "s_branch .Lwt_%=\n\t" \
  ".Lwx_%=:\n\t" \
  "s_lshl_b32 s93, s90, 2\n\t" \
  "s_sub_i32 s93, %[tgt], s93\n\t" \
  "s_cmp_le_i32 s93, 0\n\t" \
  "s_cbranch_scc1 .Lgo_%=\n\t" \
  "s_min_i32 s93, s93, 8000\n\t" \
  "s_mov_b32 s94, s93\n\t" \
  ".Lsl_%=:\n\t" \
  "s_sub_i32 s94, s94, 18\n\t" \
  "s_cmp_lt_i32 s94, 30\n\t" \
  "s_cbranch_scc1 .Lsx_%=\n\t" \
  "s_sleep 1\n\t" \
  "s_branch .Lsl_%=\n\t" \
  ".Lsx_%=:\n\t" \
  "s_sub_i32 s93, s93, 12\n\t" \
  "s_max_i32 s93, s93, 4\n\t" \
  "s_lshr_b32 s93, s93, 2\n\t" \
  "s_add_u32 s93, s93, s90\n\t" \
  "s_memrealtime s[90:91]\n\t" \
  ".Lpace_%=:\n\t" \
  "s_memrealtime s[94:95]\n\t" \
  "s_waitcnt lgkmcnt(1)\n\t" \
  "s_sub_i32 s92, s90, s93\n\t" \
  "s_cmp_ge_i32 s92, 0\n\t" \
  "s_cbranch_scc1 .Le_%=\n\t" \
  "s_memrealtime s[90:91]\n\t" \
  "s_waitcnt lgkmcnt(1)\n\t" \
  "s_sub_i32 s92, s94, s93\n\t" \
  "s_cmp_lt_i32 s92, 0\n\t" \
  "s_cbranch_scc1 .Lpace_%=\n\t" \
  "s_nop 4\n\t" \
  ".Le_%=:\n\t" \
  "s_add_u32 s92, s92, s93\n\t" \
  "s_lshl_b32 s92, s92, 2\n\t" \
  "s_sub_i32 s92, %[tgt], s92\n\t" \
  "s_max_i32 s92, s92, 0\n\t" \
  "s_bitcmp0_b32 s92, 3\n\t" \
  "s_cbranch_scc1 .Lf3_%=\n\t" \
  "s_nop 15\n\t" \
  ".Lf3_%=:\n\t" \
  "s_bitcmp0_b32 s92, 2\n\t" \
  "s_cbranch_scc1 .Lf2_%=\n\t" \
  "s_nop 10\n\t" \
  ".Lf2_%=:\n\t" \
  ".Lgo_%=:\n\t" \
  "s_mov_b32 s92, 0\n\t" \
  ".Lpoll_%=:\n\t" \
  "buffer_load_dwordx4 v[200:203], v248, %[rs], %[som] offen sc1\n\t" \
  "buffer_load_dwordx4 v[204:207], v248, %[rs], %[som2] offen sc1\n\t" \
  ".p2align 5\n\t" \
  "s_waitcnt vmcnt(1) lgkmcnt(0)\n\t" \
  "ds_write_b128 v249, v[200:203]\n\t" \
  "v_smfmac_f32_16x16x64_bf16 v[192:195], v[200:203], v[208:215], v251\n\t" \
  "v_max3_u32 v244, v200, v201, v202\n\t" \
  "v_smfmac_f32_16x16x64_bf16 v[196:199], v[200:203], v[236:243], v251\n\t" \
  "s_waitcnt vmcnt(0)\n\t" \
  "ds_write_b128 v249, v[204:207] offset:8192\n\t" \
  "v_smfmac_f32_16x16x64_bf16 v[192:195], v[204:207], v[216:223], v251\n\t" \
  "v_max3_u32 v244, v244, v203, v204\n\t" \
  "v_max3_u32 v244, v244, v205, v206\n\t" \
  "v_smfmac_f32_16x16x64_bf16 v[196:199], v[204:207], v[224:231], v251\n\t" \
  "v_max_u32 v244, v244, v207\n\t" \
  "v_cmp_le_u32_e64 vcc, %[sent], v244\n\t" \
  "s_cbranch_vccnz .Lslow_%=\n\t" \
  ".Lok_%=:\n\t" \
  "ds_read_b128 v[216:219], v250 offset:32768\n\t" \
  "ds_read_b128 v[220:223], v250 offset:33792\n\t" \
  "ds_read_b128 v[224:227], v250 offset:34816\n\t" \
  "ds_read_b128 v[228:231], v250 offset:35840\n\t" \
  "s_waitcnt lgkmcnt(4)\n\t" \
  "s_barrier\n\t" \
  "ds_read_b128 v[208:211], v248 offset:0\n\t" \
  "ds_read_b128 v[212:215], v248 offset:3072\n\t" \
  "ds_read_b128 v[236:239], v248 offset:4096\n\t" \
  "ds_read_b128 v[240:243], v248 offset:5120\n\t" \
  "s_cmp_eq_u32 %[step], 63\n\t" \
  "s_cbranch_scc1 .Lls_%=\n\t" \
  "s_waitcnt lgkmcnt(3)\n\t" \
  "ds_read_b128 v[244:247], v248 offset:6144\n\t" \
  "v_smfmac_f32_16x16x64_bf16 v[192:195], v[208:211], v[216:223], v251\n\t" \
  "ds_read_b128 v[200:203], v248 offset:12288\n\t" \
  "v_smfmac_f32_16x16x64_bf16 v[196:199], v[208:211], v[224:231], v251\n\t" \
  "s_waitcnt lgkmcnt(4)\n\t" \
  "ds_read_b128 v[216:219], v250 offset:0\n\t" \
  "v_smfmac_f32_16x16x64_bf16 v[192:195], v[212:215], v[0:7], v251\n\t" \
  "ds_read_b128 v[220:223], v250 offset:1024\n\t" \
  "v_smfmac_f32_16x16x64_bf16 v[196:199], v[212:215], v[8:15], v251\n\t" \
  "s_waitcnt lgkmcnt(5)\n\t" \
  "ds_read_b128 v[224:227], v250 offset:2048\n\t" \
  "v_smfmac_f32_16x16x64_bf16 v[192:195], v[236:239], v[16:23], v251\n\t" \
  "ds_read_b128 v[228:231], v250 offset:3072\n\t" \
  "v_smfmac_f32_16x16x64_bf16 v[196:199], v[236:239], v[24:31], v251\n\t" \
  "s_waitcnt lgkmcnt(5)\n\t" \
  "ds_read_b128 v[204:207], v248 offset:7168\n\t" \
  "v_smfmac_f32_16x16x64_bf16 v[192:195], v[240:243], v[32:39], v251\n\t" \
  "ds_read_b128 v[208:211], v248 offset:8192\n\t" \
  "v_smfmac_f32_16x16x64_bf16 v[196:199], v[240:243], v[40:47], v251\n\t" \
  "ds_read_b128 v[212:215], v248 offset:10240\n\t" \
  "v_smfmac_f32_16x16x64_bf16 v[192:195], v[244:247], v[48:55], v251\n\t" \
  "ds_read_b128 v[236:239], v248 offset:11264\n\t" \
  "v_smfmac_f32_16x16x64_bf16 v[196:199], v[244:247], v[56:63], v251\n\t" \
  "s_waitcnt lgkmcnt(6)\n\t" \
  "v_smfmac_f32_16x16x64_bf16 v[192:195], v[200:203], v[216:223], v251\n\t" \
  "s_waitcnt lgkmcnt(4)\n\t" \
  "v_smfmac_f32_16x16x64_bf16 v[196:199], v[200:203], v[224:231], v251\n\t" \
  "s_waitcnt lgkmcnt(3)\n\t" \
  "ds_read_b128 v[240:243], v248 offset:13312\n\t" \
  "v_smfmac_f32_16x16x64_bf16 v[192:195], v[204:207], v[64:71], v251\n\t" \
  "ds_read_b128 v[244:247], v248 offset:14336\n\t" \
  "v_smfmac_f32_16x16x64_bf16 v[196:199], v[204:207], v[72:79], v251\n\t" \
  "s_waitcnt lgkmcnt(3)\n\t" \
  "v_smfmac_f32_16x16x64_bf16 v[192:195], v[208:211], v[80:87], v251\n\t" \
  "ds_read_b128 v[200:203], v248 offset:15360\n\t" \
  "v_smfmac_f32_16x16x64_bf16 v[196:199], v[208:211], v[88:95], v251\n\t" \
  "ds_read_b128 v[204:207], v248 offset:2048\n\t" \
  "v_smfmac_f32_16x16x64_bf16 v[192:195], v[212:215], v[96:103], v251\n\t" \
  "v_smfmac_f32_16x16x64_bf16 v[196:199], v[212:215], v[104:111], v251\n\t" \
  "s_waitcnt lgkmcnt(3)\n\t" \
  "v_smfmac_f32_16x16x64_bf16 v[192:195], v[236:239], v[112:119], v251\n\t" \
  "v_smfmac_f32_16x16x64_bf16 v[196:199], v[236:239], v[120:127], v251\n\t" \
  "v_smfmac_f32_16x16x64_bf16 v[192:195], v[240:243], v[128:135], v251\n\t" \
  "v_smfmac_f32_16x16x64_bf16 v[196:199], v[240:243], v[136:143], v251\n\t" \
  "s_waitcnt lgkmcnt(0)\n\t" \
  "v_smfmac_f32_16x16x64_bf16 v[192:195], v[244:247], v[144:151], v251\n\t" \
  "v_smfmac_f32_16x16x64_bf16 v[196:199], v[244:247], v[152:159], v251\n\t" \
  "v_smfmac_f32_16x16x64_bf16 v[192:195], v[200:203], v[160:167], v251\n\t" \
  "v_smfmac_f32_16x16x64_bf16 v[196:199], v[200:203], v[168:175], v251\n\t" \
  "v_smfmac_f32_16x16x64_bf16 v[192:195], v[204:207], v[176:183], v251\n\t" \
  "v_smfmac_f32_16x16x64_bf16 v[196:199], v[204:207], v[184:191], v251\n\t" \
  "ds_read_b128 v[200:203], v248 offset:17408\n\t" \
  "ds_read_b128 v[204:207], v248 offset:21504\n\t" \
  "v_mov_b32 v250, v232\n\t" \
  "v_mov_b32 v249, v233\n\t" \
  "s_nop 2\n\t" \
  "v_add_f32 v192, v192, v194\n\t" \
  "v_add_f32 v193, v193, v195\n\t" \
  "v_add_f32 v196, v196, v198\n\t" \
  "v_add_f32_e64 v197, v197, v199\n\t" \
  "v_cvt_pk_bf16_f32 v192, v192, v193\n\t" \
  "v_cvt_pk_bf16_f32 v196, v196, v197\n\t" \
  "ds_write2_b32 v252, v192, v196 offset1:64\n\t" \
  "v_mov_b32_e64 v251, v234\n\t" \
  "ds_read_b64_tr_b16 v[236:237], v250 offset:512\n\t" \
  "ds_read_b64_tr_b16 v[238:239], v250 offset:576\n\t" \
  "v_mov_b32_e64 v212, %[step]\n\t" \
  "s_mov_b64 exec, 1\n\t" \
  "ds_write_b32 v248, v212 offset:28688\n\t" \
  "s_mov_b64 exec, -1\n\t" \
  "buffer_load_dwordx4 v[192:195], v248, %[rt], %[svt] offen offset:0 sc1\n\t" \
  "buffer_load_dwordx4 v[196:199], v248, %[rt], %[svt] offen offset:1024 sc1\n\t" \
  "s_waitcnt lgkmcnt(1)\n\t" \
  "v_mfma_f32_16x16x32_bf16 v[200:203], v[200:203], v[236:239], 0\n\t" \
  "v_mfma_f32_16x16x32_bf16 v[204:207], v[204:207], v[236:239], 0\n\t" \
  "s_nop 7\n\t" \
  "v_cndmask_b32 v216, v200, v204, %[nh]\n\t" \
  "v_cndmask_b32 v217, v201, v205, %[nh]\n\t" \
  "v_cndmask_b32 v218, v202, v206, %[nh]\n\t" \
  "v_cndmask_b32 v219, v203, v207, %[nh]\n\t" \
  "ds_write_b128 v248, v[216:219] offset:17408\n\t" \
  "buffer_load_dword v252, v249, %[rs], %[sv] offen offset:0 sc1\n\t" \
  "buffer_load_dword v253, v249, %[rs], %[sv] offen offset:256 sc1\n\t" \
  "ds_read_b32 v212, v248 offset:28672\n\t" \
  "ds_read_b64_tr_b16 v[240:241], v250 offset:1024\n\t" \
  "ds_read_b64_tr_b16 v[242:243], v250 offset:1088\n\t" \
  "ds_read_b64_tr_b16 v[244:245], v250 offset:1536\n\t" \
  "ds_read_b64_tr_b16 v[246:247], v250 offset:1600\n\t" \
  "ds_read_b64_tr_b16 v[232:233], v250 offset:0\n\t" \
  "ds_read_b64_tr_b16 v[234:235], v250 offset:64\n\t" \
  "s_setprio 3\n\t" \
  "s_waitcnt lgkmcnt(0)\n\t" \
  "v_cmp_ne_u32 vcc, %[step], v212\n\t" \
  "s_and_b32 s93, vcc_lo, 0xf\n\t" \
  "s_barrier\n\t" \
  "s_cbranch_scc1 .Lre_%=\n\t" \
  ".Lrb_%=:\n\t" \
  "ds_read_b64_tr_b16 v[204:205], v250 offset:2560\n\t" \
  "ds_read_b64_tr_b16 v[206:207], v250 offset:2624\n\t" \
  "ds_read_b64_tr_b16 v[208:209], v250 offset:3072\n\t" \
  "ds_read_b64_tr_b16 v[210:211], v250 offset:3136\n\t" \
  "ds_read_b64_tr_b16 v[212:213], v250 offset:3584\n\t" \
  "ds_read_b64_tr_b16 v[214:215], v250 offset:3648\n\t" \
  "ds_read_b64_tr_b16 v[200:201], v250 offset:2048\n\t" \
  "ds_read_b64_tr_b16 v[202:203], v250 offset:2112\n\t" \
  "ds_read_b128 v[220:223], v248 offset:18432\n\t" \
  "ds_read_b128 v[224:227], v248 offset:19456\n\t" \
  "ds_read_b128 v[228:231], v248 offset:16384\n\t" \
  "s_waitcnt lgkmcnt(9)\n\t" \
  "v_mfma_f32_16x16x32_bf16 v[204:207], v[204:207], v[236:239], 0\n\t" \
  "s_waitcnt lgkmcnt(7)\n\t" \
  "v_mfma_f32_16x16x32_bf16 v[204:207], v[208:211], v[240:243], v[204:207]\n\t" \
  "v_mov_b32 v237, 0\n\t" \
  "s_waitcnt lgkmcnt(5)\n\t" \
  "v_mfma_f32_16x16x32_bf16 v[204:207], v[212:215], v[244:247], v[204:207]\n\t" \
  "v_mov_b32 v238, 0\n\t" \
  "s_waitcnt lgkmcnt(3)\n\t" \
  "v_mfma_f32_16x16x32_bf16 v[204:207], v[200:203], v[232:235], v[204:207]\n\t" \
  "s_waitcnt lgkmcnt(2)\n\t" \
  "v_pk_add_f32 v[216:217], v[216:217], v[220:221]\n\t" \
  "v_pk_add_f32 v[218:219], v[218:219], v[222:223]\n\t" \
  "s_waitcnt lgkmcnt(0)\n\t" \
  "v_pk_add_f32 v[224:225], v[224:225], v[228:229]\n\t" \
  "v_pk_add_f32 v[226:227], v[226:227], v[230:231]\n\t" \
  "v_pk_add_f32 v[216:217], v[216:217], v[224:225]\n\t" \
  "v_pk_add_f32 v[218:219], v[218:219], v[226:227]\n\t" \
  "v_max3_f32 v224, v216, v217, v218\n\t" \
  "v_max_f32 v224, v224, v219\n\t" \
  "v_max3_f32 v231, v204, v205, v206\n\t" \
  "v_max_f32 v231, v231, v207\n\t" \
  "v_max_f32_dpp v225, v224, v224 row_ror:8 row_mask:0xf bank_mask:0xf\n\t" \
  "v_max_f32_dpp v226, v224, v224 row_ror:8 row_mask:0xf bank_mask:0xf\n\t" \
  "v_cndmask_b32 v204, v204, v206, %[up]\n\t" \
  "v_cndmask_b32 v205, v205, v207, %[up]\n\t" \
  "v_permlane32_swap_b32_e64 v225, v226\n\t" \
  "v_max3_f32 v209, v225, v226, v231\n\t" \
  "v_max3_f32 v210, v225, v226, v231\n\t" \
  "v_mov_b64 v[220:221], %[one]\n\t" \
  "v_mov_b64 v[222:223], %[one]\n\t" \
  "v_permlane16_swap_b32 v209, v210\n\t" \
  "v_max_f32 v208, v209, v210\n\t" \
  "v_mul_f32_e64 v208, %[negc], v208\n\t" \
  "v_fma_f32 v204, v204, %[c], v208\n\t" \
  "v_fma_f32 v205, v205, %[c], v208\n\t" \
  "v_exp_f32 v204, v204\n\t" \
  "v_fma_f32 v216, v216, %[c], v208\n\t" \
  "v_exp_f32 v205, v205\n\t" \
  "v_fma_f32 v217, v217, %[c], v208\n\t" \
  "v_exp_f32_e64 v216, v216\n\t" \
  "v_fma_f32 v218, v218, %[c], v208\n\t" \
  "v_cvt_pk_bf16_f32 v236, v204, v205\n\t" \
  "v_cvt_pk_bf16_f32 v239, v204, v205\n\t" \
  "v_exp_f32 v217, v217\n\t" \
  "v_fma_f32 v219, v219, %[c], v208\n\t" \
  "v_mfma_f32_16x16x16_bf16 v[200:203], v[220:221], v[236:237], 0\n\t" \
  "v_exp_f32 v218, v218\n\t" \
  "s_waitcnt vmcnt(0)\n\t" \
  "v_mfma_f32_16x16x16_bf16 v[224:227], v[252:253], v[236:237], 0\n\t" \
  "v_cvt_pk_bf16_f32 v214, v216, v217\n\t" \
  "v_cvt_pk_bf16_f32 v212, v216, v217\n\t" \
  "v_exp_f32 v219, v219\n\t" \
  "v_mfma_f32_16x16x16_bf16 v[228:231], v[252:253], v[238:239], 0\n\t" \
  "v_cvt_pk_bf16_f32 v215, v218, v219\n\t" \
  "v_cvt_pk_bf16_f32 v213, v218, v219\n\t" \
  "v_mov_b32_dpp v212, v214 row_ror:8 row_mask:0xf bank_mask:0xc\n\t" \
  "v_mov_b32_dpp v214, v214 row_ror:8 row_mask:0xf bank_mask:0x3\n\t" \
  "v_mov_b32_dpp v213, v215 row_ror:8 row_mask:0xf bank_mask:0xc\n\t" \
  "v_mov_b32_dpp v215, v215 row_ror:8 row_mask:0xf bank_mask:0x3\n\t" \
  "v_max_u32 v208, v252, v253\n\t" \
  "v_cmp_le_u32 vcc, %[sent], v208\n\t" \
  "v_mfma_f32_16x16x32_bf16 v[200:203], v[220:223], v[212:215], v[200:203]\n\t" \
  ".Lpv_%=:\n\t" \
  "v_mfma_f32_16x16x32_bf16 v[224:227], v[192:195], v[212:215], v[224:227]\n\t" \
  "v_mfma_f32_16x16x32_bf16 v[228:231], v[196:199], v[212:215], v[228:231]\n\t" \
  "s_cbranch_vccnz .Lvs_%=\n\t" \
  ".Lvok_%=:\n\t" \
  "s_nop 4\n\t" \
  "v_rcp_f32_e64 v208, v200\n\t" \
  "v_cndmask_b32 v224, v224, v226, %[nh]\n\t" \
  "v_cndmask_b32 v225, v225, v227, %[nh]\n\t" \
  "v_cndmask_b32 v228, v228, v230, %[nh]\n\t" \
  "v_cndmask_b32 v229, v229, v231, %[nh]\n\t" \
  "v_pk_mul_f32 v[224:225], v[224:225], v[208:209] op_sel_hi:[1,0]\n\t" \
  "v_pk_mul_f32 v[228:229], v[228:229], v[208:209] op_sel_hi:[1,0]\n\t" \
  "v_cvt_pk_bf16_f32 v224, v224, v225\n\t" \
  "v_cvt_pk_bf16_f32 v225, v228, v229\n\t" \
  "buffer_store_dwordx2 v[224:225], v251, %[rs], %[so] offen sc1\n\t" \
  "s_memtime s[90:91]\n\t" \
  "s_setprio 0\n\t" \
  "s_waitcnt lgkmcnt(0)\n\t" \
  "s_mov_b32 %[tout], s90\n\t" \
  "s_mov_b32 %[spins], s92\n\t" \
  "s_branch .Lend_%=\n\t" \
  ".Lls_%=:\n\t" \
  "s_waitcnt lgkmcnt(0)\n\t" \
  "s_barrier\n\t" \
  "s_mov_b32 %[spins], 0\n\t" \
  "s_branch .Lend_%=\n\t" \
  ".Lre_%=:\n\t" \
  "ds_read_b64_tr_b16 v[240:241], v250 offset:1024\n\t" \
  "ds_read_b64_tr_b16 v[242:243], v250 offset:1088\n\t" \
  "ds_read_b64_tr_b16 v[244:245], v250 offset:1536\n\t" \
  "ds_read_b64_tr_b16 v[246:247], v250 offset:1600\n\t" \
  "ds_read_b64_tr_b16 v[232:233], v250 offset:0\n\t" \
  "ds_read_b64_tr_b16 v[234:235], v250 offset:64\n\t" \
  "s_waitcnt lgkmcnt(0)\n\t" \
  "s_branch .Lrb_%=\n\t" \
  ".Lvs_%=:\n\t" \
  "v_cmp_eq_u32 vcc, %[sent], v252\n\t" \
  "v_cmp_eq_u32 s[94:95], %[sent], v253\n\t" \
  "s_or_b64 s[94:95], s[94:95], vcc\n\t" \
  "s_cbranch_scc0 .Lvok_%=\n\t" \
  "s_add_u32 s92, s92, 1\n\t" \
  "s_sleep 2\n\t" \
  "s_and_b32 s93, s92, 63\n\t" \
  "s_cbranch_scc1 .Lvr_%=\n\t" \
  "buffer_load_dword v208, off, %[rs], %[sab] sc1\n\t" \
  "s_waitcnt vmcnt(0)\n\t" \
  "v_readfirstlane_b32 s93, v208\n\t" \
  "s_nop 3\n\t" \
  "s_cmp_eq_u32 s93, 1\n\t" \
  "s_cbranch_scc1 .Lvok_%=\n\t" \
  "s_cmp_lt_u32 s92, 0x80000\n\t" \
  "s_cbranch_scc1 .Lvr_%=\n\t" \
  "v_mov_b32 v208, 1\n\t" \
  "buffer_store_dword v208, off, %[rs], %[sab] sc1\n\t" \
  "s_branch .Lvok_%=\n\t" \
  ".Lvr_%=:\n\t" \
  "buffer_load_dword v252, v249, %[rs], %[sv] offen offset:0 sc1\n\t" \
  "buffer_load_dword v253, v249, %[rs], %[sv] offen offset:256 sc1\n\t" \
  "s_waitcnt vmcnt(0)\n\t" \
  "v_mfma_f32_16x16x16_bf16 v[224:227], v[252:253], v[236:237], 0\n\t" \
  "v_mfma_f32_16x16x16_bf16 v[228:231], v[252:253], v[238:239], 0\n\t" \
  "v_max_u32 v208, v252, v253\n\t" \
  "v_cmp_le_u32 vcc, %[sent], v208\n\t" \
  "s_nop 4\n\t" \
  "s_branch .Lpv_%=\n\t" \
  "s_branch .Lend_%=\n\t" \
  ".Lslow_%=:\n\t" \
  "s_mov_b64 s[94:95], 0\n\t" \
  "v_cmp_eq_u32 vcc, %[sent], v200\n\t" \
  "s_or_b64 s[94:95], s[94:95], vcc\n\t" \
  "v_cmp_eq_u32 vcc, %[sent], v201\n\t" \
  "s_or_b64 s[94:95], s[94:95], vcc\n\t" \
  "v_cmp_eq_u32 vcc, %[sent], v202\n\t" \
  "s_or_b64 s[94:95], s[94:95], vcc\n\t" \
  "v_cmp_eq_u32 vcc, %[sent], v203\n\t" \
  "s_or_b64 s[94:95], s[94:95], vcc\n\t" \
  "v_cmp_eq_u32 vcc, %[sent], v204\n\t" \
  "s_or_b64 s[94:95], s[94:95], vcc\n\t" \
  "v_cmp_eq_u32 vcc, %[sent], v205\n\t" \
  "s_or_b64 s[94:95], s[94:95], vcc\n\t" \
  "v_cmp_eq_u32 vcc, %[sent], v206\n\t" \
  "s_or_b64 s[94:95], s[94:95], vcc\n\t" \
  "v_cmp_eq_u32 vcc, %[sent], v207\n\t" \
  "s_or_b64 s[94:95], s[94:95], vcc\n\t" \
  "s_cmp_eq_u64 s[94:95], 0\n\t" \
  "s_cbranch_scc1 .Lok_%=\n\t" \
  "v_mov_b64 v[192:193], 0\n\t" \
  "v_mov_b64 v[194:195], 0\n\t" \
  "v_mov_b64 v[196:197], 0\n\t" \
  "v_mov_b64 v[198:199], 0\n\t" \
  "s_add_u32 s92, s92, 1\n\t" \
  "s_cmp_eq_u32 s92, 1\n\t" \
  "s_cbranch_scc1 .Lpoll_%=\n\t" \
  "s_sleep 2\n\t" \
  "s_and_b32 s93, s92, 63\n\t" \
  "s_cmp_lg_u32 s93, 0\n\t" \
  "s_cbranch_scc1 .Lpoll_%=\n\t" \
  "v_mov_b32 v245, 0\n\t" \
  "buffer_load_dword v244, v245, %[rs], %[sab] offen sc1\n\t" \
  "s_waitcnt vmcnt(0)\n\t" \
  "v_readfirstlane_b32 s93, v244\n\t" \
  "s_nop 3\n\t" \
  "s_cmp_eq_u32 s93, 1\n\t" \
  "s_cbranch_scc1 .Lok_%=\n\t" \
  "s_cmp_lt_u32 s92, 0x80000\n\t" \
  "s_cbranch_scc1 .Lpoll_%=\n\t" \
  "v_mov_b32 v244, 1\n\t" \
  "buffer_store_dword v244, v245, %[rs], %[sab] offen sc1\n\t" \
  "s_branch .Lok_%=\n\t" \
  ".Lend_%=:\n\t"
#define QK_ASM_2 \
  "s_waitcnt lgkmcnt(0)\n\t" \
  "v_subrev_u32 v244, 0x8000, v250\n\t" \
  "ds_read_b128 v[216:219], v248 offset:40960\n\t" \
  "ds_read_b128 v[220:223], v248 offset:41984\n\t" \
  "ds_read_b128 v[224:227], v248 offset:43008\n\t" \
  "ds_read_b128 v[228:231], v248 offset:44032\n\t" \
  "ds_read_b128 v[208:211], v244 offset:0\n\t" \
  "ds_read_b128 v[212:215], v244 offset:1024\n\t" \
  "ds_read_b128 v[236:239], v244 offset:2048\n\t" \
  "ds_read_b128 v[240:243], v244 offset:3072\n\t" \
  "v_mov_b64 v[192:193], 0\n\t" \
  "v_mov_b64 v[194:195], 0\n\t" \
  "v_mov_b64 v[196:197], 0\n\t" \
  "v_mov_b64 v[198:199], 0\n\t" \
  ".p2align 5\n\t" \
  "s_nop 0\n\t" \
  "s_nop 0\n\t" \
  "s_nop 0\n\t" \
  "s_nop 0\n\t" \
  "s_movk_i32 s92, 0x2000\n\t" \
  ".Lwt_%=:\n\t" \
  "ds_read_b64 v[200:201], v248 offset:30720\n\t" \
  "s_memrealtime s[90:91]\n\t" \
  "s_sub_u32 s92, s92, 1\n\t" \
  "s_waitcnt lgkmcnt(0)\n\t" \
  "v_readfirstlane_b32 s93, v200\n\t" \
  "v_readfirstlane_b32 %[tgt], v201\n\t" \
  "s_cbranch_scc1 .Lgo_%=\n\t" \
  "s_cmp_eq_u32 s93, %[step]\n\t" \
  "s_cbranch_scc1 .Lwx_%=\n\t" \
  "s_sleep 1\n\t" \
  "s_branch .Lwt_%=\n\t" \
  ".Lwx_%=:\n\t" \
  "s_lshl_b32 s93, s90, 2\n\t" \
  "s_sub_i32 s93, %[tgt], s93\n\t" \
  "s_cmp_le_i32 s93, 0\n\t" \
  "s_cbranch_scc1 .Lgo_%=\n\t" \
  "s_min_i32 s93, s93, 8000\n\t" \
  "s_mov_b32 s94, s93\n\t" \
  ".Lsl_%=:\n\t" \
  "s_sub_i32 s94, s94, 18\n\t" \
  "s_cmp_lt_i32 s94, 30\n\t" \
  "s_cbranch_scc1 .Lsx_%=\n\t" \
  "s_sleep 1\n\t" \
  "s_branch .Lsl_%=\n\t" \
  ".Lsx_%=:\n\t" \
  "s_sub_i32 s93, s93, 12\n\t" \
  "s_max_i32 s93, s93, 4\n\t" \
  "s_lshr_b32 s93, s93, 2\n\t" \
  "s_add_u32 s93, s93, s90\n\t" \
  "s_memrealtime s[90:91]\n\t" \
  ".Lpace_%=:\n\t" \
  "s_memrealtime s[94:95]\n\t" \
  "s_waitcnt lgkmcnt(1)\n\t" \
  "s_sub_i32 s92, s90, s93\n\t" \
  "s_cmp_ge_i32 s92, 0\n\t" \
  "s_cbranch_scc1 .Le_%=\n\t" \
  "s_memrealtime s[90:91]\n\t" \
  "s_waitcnt lgkmcnt(1)\n\t" \
  "s_sub_i32 s92, s94, s93\n\t" \
  "s_cmp_lt_i32 s92, 0\n\t" \
  "s_cbranch_scc1 .Lpace_%=\n\t" \
  "s_nop 4\n\t" \
  ".Le_%=:\n\t" \
  "s_add_u32 s92, s92, s93\n\t" \
  "s_lshl_b32 s92, s92, 2\n\t" \
  "s_sub_i32 s92, %[tgt], s92\n\t" \
  "s_max_i32 s92, s92, 0\n\t" \
  "s_bitcmp0_b32 s92, 3\n\t" \
  "s_cbranch_scc1 .Lf3_%=\n\t" \
  "s_nop 15\n\t" \
  ".Lf3_%=:\n\t" \
  "s_bitcmp0_b32 s92, 2\n\t" \
  "s_cbranch_scc1 .Lf2_%=\n\t" \
  "s_nop 10\n\t" \
  ".Lf2_%=:\n\t" \
  ".Lgo_%=:\n\t" \
  "s_mov_b32 s92, 0\n\t" \
  ".Lpoll_%=:\n\t" \
  "buffer_load_dwordx4 v[200:203], v248, %[rs], %[som] offen sc1\n\t" \
  "buffer_load_dwordx4 v[204:207], v248, %[rs], %[som2] offen sc1\n\t" \
  ".p2align 5\n\t" \
  "s_waitcnt vmcnt(1) lgkmcnt(0)\n\t" \
  "ds_write_b128 v249, v[200:203]\n\t" \
  "v_smfmac_f32_16x16x64_bf16 v[192:195], v[200:203], v[208:215], v251\n\t" \
  "v_max3_u32 v244, v200, v201, v202\n\t" \
  "v_smfmac_f32_16x16x64_bf16 v[196:199], v[200:203], v[236:243], v251\n\t" \
  "s_waitcnt vmcnt(0)\n\t" \
  "ds_write_b128 v249, v[204:207] offset:8192\n\t" \
  "v_smfmac_f32_16x16x64_bf16 v[192:195], v[204:207], v[216:223], v251\n\t" \
  "v_max3_u32 v244, v244, v203, v204\n\t" \
  "v_max3_u32 v244, v244, v205, v206\n\t" \
  "v_smfmac_f32_16x16x64_bf16 v[196:199], v[204:207], v[224:231], v251\n\t" \
  "v_max_u32 v244, v244, v207\n\t" \
  "v_cmp_le_u32_e64 vcc, %[sent], v244\n\t" \
  "s_cbranch_vccnz .Lslow_%=\n\t" \
  ".Lok_%=:\n\t" \
  "ds_read_b128 v[216:219], v250 offset:32768\n\t" \
  "ds_read_b128 v[220:223], v250 offset:33792\n\t" \
  "ds_read_b128 v[224:227], v250 offset:34816\n\t" \
  "ds_read_b128 v[228:231], v250 offset:35840\n\t" \
  "s_waitcnt lgkmcnt(4)\n\t" \
  "s_barrier\n\t" \
  "ds_read_b128 v[208:211], v248 offset:3072\n\t" \
  "ds_read_b128 v[212:215], v248 offset:4096\n\t" \
  "ds_read_b128 v[236:239], v248 offset:5120\n\t" \
  "ds_read_b128 v[240:243], v248 offset:6144\n\t" \
  "s_cmp_eq_u32 %[step], 63\n\t" \
  "s_cbranch_scc1 .Lls_%=\n\t" \
  "s_waitcnt lgkmcnt(3)\n\t" \
  "ds_read_b128 v[244:247], v248 offset:7168\n\t" \
  "v_smfmac_f32_16x16x64_bf16 v[192:195], v[208:211], v[216:223], v251\n\t" \
  "ds_read_b128 v[200:203], v248 offset:13312\n\t" \
  "v_smfmac_f32_16x16x64_bf16 v[196:199], v[208:211], v[224:231], v251\n\t" \
  "s_waitcnt lgkmcnt(4)\n\t" \
  "ds_read_b128 v[216:219], v250 offset:0\n\t" \
  "v_smfmac_f32_16x16x64_bf16 v[192:195], v[212:215], v[0:7], v251\n\t" \
  "ds_read_b128 v[220:223], v250 offset:1024\n\t" \
  "v_smfmac_f32_16x16x64_bf16 v[196:199], v[212:215], v[8:15], v251\n\t" \
  "s_waitcnt lgkmcnt(5)\n\t" \
  "ds_read_b128 v[224:227], v250 offset:2048\n\t" \
  "v_smfmac_f32_16x16x64_bf16 v[192:195], v[236:239], v[16:23], v251\n\t" \
  "ds_read_b128 v[228:231], v250 offset:3072\n\t" \
  "v_smfmac_f32_16x16x64_bf16 v[196:199], v[236:239], v[24:31], v251\n\t" \
  "s_waitcnt lgkmcnt(5)\n\t" \
  "ds_read_b128 v[204:207], v248 offset:8192\n\t" \
  "v_smfmac_f32_16x16x64_bf16 v[192:195], v[240:243], v[32:39], v251\n\t" \
  "ds_read_b128 v[208:211], v248 offset:9216\n\t" \
  "v_smfmac_f32_16x16x64_bf16 v[196:199], v[240:243], v[40:47], v251\n\t" \
  "ds_read_b128 v[212:215], v248 offset:11264\n\t" \
  "v_smfmac_f32_16x16x64_bf16 v[192:195], v[244:247], v[48:55], v251\n\t" \
  "ds_read_b128 v[236:239], v248 offset:12288\n\t" \
  "v_smfmac_f32_16x16x64_bf16 v[196:199], v[244:247], v[56:63], v251\n\t" \
  "s_waitcnt lgkmcnt(6)\n\t" \
  "v_smfmac_f32_16x16x64_bf16 v[192:195], v[200:203], v[216:223], v251\n\t" \
  "s_waitcnt lgkmcnt(4)\n\t" \
  "v_smfmac_f32_16x16x64_bf16 v[196:199], v[200:203], v[224:231], v251\n\t" \
  "s_waitcnt lgkmcnt(3)\n\t" \
  "ds_read_b128 v[240:243], v248 offset:14336\n\t" \
  "v_smfmac_f32_16x16x64_bf16 v[192:195], v[204:207], v[64:71], v251\n\t" \
  "ds_read_b128 v[244:247], v248 offset:15360\n\t" \
  "v_smfmac_f32_16x16x64_bf16 v[196:199], v[204:207], v[72:79], v251\n\t" \
  "s_waitcnt lgkmcnt(3)\n\t" \
  "v_smfmac_f32_16x16x64_bf16 v[192:195], v[208:211], v[80:87], v251\n\t" \
  "ds_read_b128 v[200:203], v248 offset:0\n\t" \
  "v_smfmac_f32_16x16x64_bf16 v[196:199], v[208:211], v[88:95], v251\n\t" \
  "ds_read_b128 v[204:207], v248 offset:1024\n\t" \
  "v_smfmac_f32_16x16x64_bf16 v[192:195], v[212:215], v[96:103], v251\n\t" \
  "v_smfmac_f32_16x16x64_bf16 v[196:199], v[212:215], v[104:111], v251\n\t" \
  "s_waitcnt lgkmcnt(3)\n\t" \
  "v_smfmac_f32_16x16x64_bf16 v[192:195], v[236:239], v[112:119], v251\n\t" \
  "v_smfmac_f32_16x16x64_bf16 v[196:199], v[236:239], v[120:127], v251\n\t" \
  "v_smfmac_f32_16x16x64_bf16 v[192:195], v[240:243], v[128:135], v251\n\t" \
  "v_smfmac_f32_16x16x64_bf16 v[196:199], v[240:243], v[136:143], v251\n\t" \
  "s_waitcnt lgkmcnt(0)\n\t" \
  "v_smfmac_f32_16x16x64_bf16 v[192:195], v[244:247], v[144:151], v251\n\t" \
  "v_smfmac_f32_16x16x64_bf16 v[196:199], v[244:247], v[152:159], v251\n\t" \
  "v_smfmac_f32_16x16x64_bf16 v[192:195], v[200:203], v[160:167], v251\n\t" \
  "v_smfmac_f32_16x16x64_bf16 v[196:199], v[200:203], v[168:175], v251\n\t" \
  "v_smfmac_f32_16x16x64_bf16 v[192:195], v[204:207], v[176:183], v251\n\t" \
  "v_smfmac_f32_16x16x64_bf16 v[196:199], v[204:207], v[184:191], v251\n\t" \
  "ds_read_b128 v[200:203], v248 offset:18432\n\t" \
  "ds_read_b128 v[204:207], v248 offset:22528\n\t" \
  "v_mov_b32 v250, v232\n\t" \
  "v_mov_b32 v249, v233\n\t" \
  "s_nop 2\n\t" \
  "v_add_f32 v192, v192, v194\n\t" \
  "v_add_f32 v193, v193, v195\n\t" \
  "v_add_f32 v196, v196, v198\n\t" \
  "v_add_f32_e64 v197, v197, v199\n\t" \
  "v_cvt_pk_bf16_f32 v192, v192, v193\n\t" \
  "v_cvt_pk_bf16_f32 v196, v196, v197\n\t" \
  "ds_write2_b32 v252, v192, v196 offset1:64\n\t" \
  "v_mov_b32_e64 v251, v234\n\t" \
  "ds_read_b64_tr_b16 v[240:241], v250 offset:1024\n\t" \
  "ds_read_b64_tr_b16 v[242:243], v250 offset:1088\n\t" \
  "v_mov_b32_e64 v212, %[step]\n\t" \
  "s_mov_b64 exec, 1\n\t" \
  "ds_write_b32 v248, v212 offset:28704\n\t" \
  "s_mov_b64 exec, -1\n\t" \
  "buffer_load_dwordx4 v[192:195], v248, %[rt], %[svt] offen offset:0 sc1\n\t" \
  "buffer_load_dwordx4 v[196:199], v248, %[rt], %[svt] offen offset:1024 sc1\n\t" \
  "s_waitcnt lgkmcnt(1)\n\t" \
  "v_mfma_f32_16x16x32_bf16 v[200:203], v[200:203], v[240:243], 0\n\t" \
  "v_mfma_f32_16x16x32_bf16 v[204:207], v[204:207], v[240:243], 0\n\t" \
  "s_nop 7\n\t" \
  "v_cndmask_b32 v216, v200, v204, %[nh]\n\t" \
  "v_cndmask_b32 v217, v201, v205, %[nh]\n\t" \
  "v_cndmask_b32 v218, v202, v206, %[nh]\n\t" \
  "v_cndmask_b32 v219, v203, v207, %[nh]\n\t" \
  "ds_write_b128 v248, v[216:219] offset:18432\n\t" \
  "buffer_load_dword v252, v249, %[rs], %[sv] offen offset:0 sc1\n\t" \
  "buffer_load_dword v253, v249, %[rs], %[sv] offen offset:256 sc1\n\t" \
  "ds_read_b32 v212, v248 offset:28672\n\t" \
  "ds_read_b64_tr_b16 v[244:245], v250 offset:1536\n\t" \
  "ds_read_b64_tr_b16 v[246:247], v250 offset:1600\n\t" \
  "ds_read_b64_tr_b16 v[232:233], v250 offset:0\n\t" \
  "ds_read_b64_tr_b16 v[234:235], v250 offset:64\n\t" \
  "ds_read_b64_tr_b16 v[236:237], v250 offset:512\n\t" \
  "ds_read_b64_tr_b16 v[238:239], v250 offset:576\n\t" \
  "s_setprio 3\n\t" \
  "s_waitcnt lgkmcnt(0)\n\t" \
  "v_cmp_ne_u32 vcc, %[step], v212\n\t" \
  "s_and_b32 s93, vcc_lo, 0xf\n\t" \
  "s_barrier\n\t" \
  "s_cbranch_scc1 .Lre_%=\n\t" \
  ".Lrb_%=:\n\t" \
  "ds_read_b64_tr_b16 v[208:209], v250 offset:3072\n\t" \
  "ds_read_b64_tr_b16 v[210:211], v250 offset:3136\n\t" \
  "ds_read_b64_tr_b16 v[212:213], v250 offset:3584\n\t" \
  "ds_read_b64_tr_b16 v[214:215], v250 offset:3648\n\t" \
  "ds_read_b64_tr_b16 v[200:201], v250 offset:2048\n\t" \
  "ds_read_b64_tr_b16 v[202:203], v250 offset:2112\n\t" \
  "ds_read_b64_tr_b16 v[204:205], v250 offset:2560\n\t" \
  "ds_read_b64_tr_b16 v[206:207], v250 offset:2624\n\t" \
  "ds_read_b128 v[220:223], v248 offset:19456\n\t" \
  "ds_read_b128 v[224:227], v248 offset:16384\n\t" \
  "ds_read_b128 v[228:231], v248 offset:17408\n\t" \
  "s_waitcnt lgkmcnt(9)\n\t" \
  "v_mfma_f32_16x16x32_bf16 v[208:211], v[208:211], v[240:243], 0\n\t" \
  "s_waitcnt lgkmcnt(7)\n\t" \
  "v_mfma_f32_16x16x32_bf16 v[208:211], v[212:215], v[244:247], v[208:211]\n\t" \
  "v_mov_b32 v241, 0\n\t" \
  "s_waitcnt lgkmcnt(5)\n\t" \
  "v_mfma_f32_16x16x32_bf16 v[208:211], v[200:203], v[232:235], v[208:211]\n\t" \
  "v_mov_b32 v242, 0\n\t" \
  "s_waitcnt lgkmcnt(3)\n\t" \
  "v_mfma_f32_16x16x32_bf16 v[208:211], v[204:207], v[236:239], v[208:211]\n\t" \
  "s_waitcnt lgkmcnt(2)\n\t" \
  "v_pk_add_f32 v[216:217], v[216:217], v[220:221]\n\t" \
  "v_pk_add_f32 v[218:219], v[218:219], v[222:223]\n\t" \
  "s_waitcnt lgkmcnt(0)\n\t" \
  "v_pk_add_f32 v[224:225], v[224:225], v[228:229]\n\t" \
  "v_pk_add_f32 v[226:227], v[226:227], v[230:231]\n\t" \
  "v_pk_add_f32 v[216:217], v[216:217], v[224:225]\n\t" \
  "v_pk_add_f32 v[218:219], v[218:219], v[226:227]\n\t" \
  "v_max3_f32 v224, v216, v217, v218\n\t" \
  "v_max_f32 v224, v224, v219\n\t" \
  "v_max3_f32 v231, v208, v209, v210\n\t" \
  "v_max_f32 v231, v231, v211\n\t" \
  "v_max_f32_dpp v225, v224, v224 row_ror:8 row_mask:0xf bank_mask:0xf\n\t" \
  "v_max_f32_dpp v226, v224, v224 row_ror:8 row_mask:0xf bank_mask:0xf\n\t" \
  "v_cndmask_b32 v208, v208, v210, %[up]\n\t" \
  "v_cndmask_b32 v209, v209, v211, %[up]\n\t" \
  "v_permlane32_swap_b32_e64 v225, v226\n\t" \
  "v_max3_f32 v213, v225, v226, v231\n\t" \
  "v_max3_f32 v214, v225, v226, v231\n\t" \
  "v_mov_b64 v[220:221], %[one]\n\t" \
  "v_mov_b64 v[222:223], %[one]\n\t" \
  "v_permlane16_swap_b32 v213, v214\n\t" \
  "v_max_f32 v212, v213, v214\n\t" \
  "v_mul_f32_e64 v212, %[negc], v212\n\t" \
  "v_fma_f32 v208, v208, %[c], v212\n\t" \
  "v_fma_f32 v209, v209, %[c], v212\n\t" \
  "v_exp_f32 v208, v208\n\t" \
  "v_fma_f32 v216, v216, %[c], v212\n\t" \
  "v_exp_f32 v209, v209\n\t" \
  "v_fma_f32 v217, v217, %[c], v212\n\t" \
  "v_exp_f32_e64 v216, v216\n\t" \
  "v_fma_f32 v218, v218, %[c], v212\n\t" \
  "v_cvt_pk_bf16_f32 v240, v208, v209\n\t" \
  "v_cvt_pk_bf16_f32 v243, v208, v209\n\t" \
  "v_exp_f32 v217, v217\n\t" \
  "v_fma_f32 v219, v219, %[c], v212\n\t" \
  "v_mfma_f32_16x16x16_bf16 v[204:207], v[220:221], v[240:241], 0\n\t" \
  "v_exp_f32 v218, v218\n\t" \
  "s_waitcnt vmcnt(0)\n\t" \
  "v_mfma_f32_16x16x16_bf16 v[224:227], v[252:253], v[240:241], 0\n\t" \
  "v_cvt_pk_bf16_f32 v202, v216, v217\n\t" \
  "v_cvt_pk_bf16_f32 v200, v216, v217\n\t" \
  "v_exp_f32 v219, v219\n\t" \
  "v_mfma_f32_16x16x16_bf16 v[228:231], v[252:253], v[242:243], 0\n\t" \
  "v_cvt_pk_bf16_f32 v203, v218, v219\n\t" \
  "v_cvt_pk_bf16_f32 v201, v218, v219\n\t" \
  "v_mov_b32_dpp v200, v202 row_ror:8 row_mask:0xf bank_mask:0xc\n\t" \
  "v_mov_b32_dpp v202, v202 row_ror:8 row_mask:0xf bank_mask:0x3\n\t" \
  "v_mov_b32_dpp v201, v203 row_ror:8 row_mask:0xf bank_mask:0xc\n\t" \
  "v_mov_b32_dpp v203, v203 row_ror:8 row_mask:0xf bank_mask:0x3\n\t" \
  "v_max_u32 v212, v252, v253\n\t" \
  "v_cmp_le_u32 vcc, %[sent], v212\n\t" \
  "v_mfma_f32_16x16x32_bf16 v[204:207], v[220:223], v[200:203], v[204:207]\n\t" \
  ".Lpv_%=:\n\t" \
  "v_mfma_f32_16x16x32_bf16 v[224:227], v[192:195], v[200:203], v[224:227]\n\t" \
  "v_mfma_f32_16x16x32_bf16 v[228:231], v[196:199], v[200:203], v[228:231]\n\t" \
  "s_cbranch_vccnz .Lvs_%=\n\t" \
  ".Lvok_%=:\n\t" \
  "s_nop 4\n\t" \
  "v_rcp_f32_e64 v212, v204\n\t" \
  "v_cndmask_b32 v224, v224, v226, %[nh]\n\t" \
  "v_cndmask_b32 v225, v225, v227, %[nh]\n\t" \
  "v_cndmask_b32 v228, v228, v230, %[nh]\n\t" \
  "v_cndmask_b32 v229, v229, v231, %[nh]\n\t" \
  "v_pk_mul_f32 v[224:225], v[224:225], v[212:213] op_sel_hi:[1,0]\n\t" \
  "v_pk_mul_f32 v[228:229], v[228:229], v[212:213] op_sel_hi:[1,0]\n\t" \
  "v_cvt_pk_bf16_f32 v224, v224, v225\n\t" \
  "v_cvt_pk_bf16_f32 v225, v228, v229\n\t" \
  "buffer_store_dwordx2 v[224:225], v251, %[rs], %[so] offen sc1\n\t" \
  "s_memtime s[90:91]\n\t" \
  "s_setprio 0\n\t" \
  "s_waitcnt lgkmcnt(0)\n\t" \
  "s_mov_b32 %[tout], s90\n\t" \
  "s_mov_b32 %[spins], s92\n\t" \
  "s_branch .Lend_%=\n\t" \
  ".Lls_%=:\n\t" \
  "s_waitcnt lgkmcnt(0)\n\t" \
  "s_barrier\n\t" \
  "s_mov_b32 %[spins], 0\n\t" \
  "s_branch .Lend_%=\n\t" \
  ".Lre_%=:\n\t" \
  "ds_read_b64_tr_b16 v[244:245], v250 offset:1536\n\t" \
  "ds_read_b64_tr_b16 v[246:247], v250 offset:1600\n\t" \
  "ds_read_b64_tr_b16 v[232:233], v250 offset:0\n\t" \
  "ds_read_b64_tr_b16 v[234:235], v250 offset:64\n\t" \
  "ds_read_b64_tr_b16 v[236:237], v250 offset:512\n\t" \
  "ds_read_b64_tr_b16 v[238:239], v250 offset:576\n\t" \
  "s_waitcnt lgkmcnt(0)\n\t" \
  "s_branch .Lrb_%=\n\t" \
  ".Lvs_%=:\n\t" \
  "v_cmp_eq_u32 vcc, %[sent], v252\n\t" \
  "v_cmp_eq_u32 s[94:95], %[sent], v253\n\t" \
  "s_or_b64 s[94:95], s[94:95], vcc\n\t" \
  "s_cbranch_scc0 .Lvok_%=\n\t" \
  "s_add_u32 s92, s92, 1\n\t" \
  "s_sleep 2\n\t" \
  "s_and_b32 s93, s92, 63\n\t" \
  "s_cbranch_scc1 .Lvr_%=\n\t" \
  "buffer_load_dword v212, off, %[rs], %[sab] sc1\n\t" \
  "s_waitcnt vmcnt(0)\n\t" \
  "v_readfirstlane_b32 s93, v212\n\t" \
  "s_nop 3\n\t" \
  "s_cmp_eq_u32 s93, 1\n\t" \
  "s_cbranch_scc1 .Lvok_%=\n\t" \
  "s_cmp_lt_u32 s92, 0x80000\n\t" \
  "s_cbranch_scc1 .Lvr_%=\n\t" \
  "v_mov_b32 v212, 1\n\t" \
  "buffer_store_dword v212, off, %[rs], %[sab] sc1\n\t" \
  "s_branch .Lvok_%=\n\t" \
  ".Lvr_%=:\n\t" \
  "buffer_load_dword v252, v249, %[rs], %[sv] offen offset:0 sc1\n\t" \
  "buffer_load_dword v253, v249, %[rs], %[sv] offen offset:256 sc1\n\t" \
  "s_waitcnt vmcnt(0)\n\t" \
  "v_mfma_f32_16x16x16_bf16 v[224:227], v[252:253], v[240:241], 0\n\t" \
  "v_mfma_f32_16x16x16_bf16 v[228:231], v[252:253], v[242:243], 0\n\t" \
  "v_max_u32 v212, v252, v253\n\t" \
  "v_cmp_le_u32 vcc, %[sent], v212\n\t" \
  "s_nop 4\n\t" \
  "s_branch .Lpv_%=\n\t" \
  "s_branch .Lend_%=\n\t" \
  ".Lslow_%=:\n\t" \
  "s_mov_b64 s[94:95], 0\n\t" \
  "v_cmp_eq_u32 vcc, %[sent], v200\n\t" \
  "s_or_b64 s[94:95], s[94:95], vcc\n\t" \
  "v_cmp_eq_u32 vcc, %[sent], v201\n\t" \
  "s_or_b64 s[94:95], s[94:95], vcc\n\t" \
  "v_cmp_eq_u32 vcc, %[sent], v202\n\t" \
  "s_or_b64 s[94:95], s[94:95], vcc\n\t" \
  "v_cmp_eq_u32 vcc, %[sent], v203\n\t" \
  "s_or_b64 s[94:95], s[94:95], vcc\n\t" \
  "v_cmp_eq_u32 vcc, %[sent], v204\n\t" \
  "s_or_b64 s[94:95], s[94:95], vcc\n\t" \
  "v_cmp_eq_u32 vcc, %[sent], v205\n\t" \
  "s_or_b64 s[94:95], s[94:95], vcc\n\t" \
  "v_cmp_eq_u32 vcc, %[sent], v206\n\t" \
  "s_or_b64 s[94:95], s[94:95], vcc\n\t" \
  "v_cmp_eq_u32 vcc, %[sent], v207\n\t" \
  "s_or_b64 s[94:95], s[94:95], vcc\n\t" \
  "s_cmp_eq_u64 s[94:95], 0\n\t" \
  "s_cbranch_scc1 .Lok_%=\n\t" \
  "v_mov_b64 v[192:193], 0\n\t" \
  "v_mov_b64 v[194:195], 0\n\t" \
  "v_mov_b64 v[196:197], 0\n\t" \
  "v_mov_b64 v[198:199], 0\n\t" \
  "s_add_u32 s92, s92, 1\n\t" \
  "s_cmp_eq_u32 s92, 1\n\t" \
  "s_cbranch_scc1 .Lpoll_%=\n\t" \
  "s_sleep 2\n\t" \
  "s_and_b32 s93, s92, 63\n\t" \
  "s_cmp_lg_u32 s93, 0\n\t" \
  "s_cbranch_scc1 .Lpoll_%=\n\t" \
  "v_mov_b32 v245, 0\n\t" \
  "buffer_load_dword v244, v245, %[rs], %[sab] offen sc1\n\t" \
  "s_waitcnt vmcnt(0)\n\t" \
  "v_readfirstlane_b32 s93, v244\n\t" \
  "s_nop 3\n\t" \
  "s_cmp_eq_u32 s93, 1\n\t" \
  "s_cbranch_scc1 .Lok_%=\n\t" \
  "s_cmp_lt_u32 s92, 0x80000\n\t" \
  "s_cbranch_scc1 .Lpoll_%=\n\t" \
  "v_mov_b32 v244, 1\n\t" \
  "buffer_store_dword v244, v245, %[rs], %[sab] offen sc1\n\t" \
  "s_branch .Lok_%=\n\t" \
  ".Lend_%=:\n\t"
#define QK_ASM_3 \
  "s_waitcnt lgkmcnt(0)\n\t" \
  "v_subrev_u32 v244, 0x8000, v250\n\t" \
  "ds_read_b128 v[216:219], v248 offset:45056\n\t" \
  "ds_read_b128 v[220:223], v248 offset:46080\n\t" \
  "ds_read_b128 v[224:227], v248 offset:47104\n\t" \
  "ds_read_b128 v[228:231], v248 offset:48128\n\t" \
  "ds_read_b128 v[208:211], v244 offset:0\n\t" \
  "ds_read_b128 v[212:215], v244 offset:1024\n\t" \
  "ds_read_b128 v[236:239], v244 offset:2048\n\t" \
  "ds_read_b128 v[240:243], v244 offset:3072\n\t" \
  "v_mov_b64 v[192:193], 0\n\t" \
  "v_mov_b64 v[194:195], 0\n\t" \
  "v_mov_b64 v[196:197], 0\n\t" \
  "v_mov_b64 v[198:199], 0\n\t" \
  ".p2align 5\n\t" \
  "s_nop 0\n\t" \
  "s_nop 0\n\t" \
  "s_nop 0\n\t" \
  "s_nop 0\n\t" \
  "s_movk_i32 s92, 0x2000\n\t" \
  ".Lwt_%=:\n\t" \
  "ds_read_b64 v[200:201], v248 offset:30720\n\t" \
  "s_memrealtime s[90:91]\n\t" \
  "s_sub_u32 s92, s92, 1\n\t" \
  "s_waitcnt lgkmcnt(0)\n\t" \
  "v_readfirstlane_b32 s93, v200\n\t" \
  "v_readfirstlane_b32 %[tgt], v201\n\t" \
  "s_cbranch_scc1 .Lgo_%=\n\t" \
  "s_cmp_eq_u32 s93, %[step]\n\t" \
  "s_cbranch_scc1 .Lwx_%=\n\t" \
  "s_sleep 1\n\t" \
  "s_branch .Lwt_%=\n\t" \
  ".Lwx_%=:\n\t" \
  "s_lshl_b32 s93, s90, 2\n\t" \
  "s_sub_i32 s93, %[tgt], s93\n\t" \
  "s_cmp_le_i32 s93, 0\n\t" \
  "s_cbranch_scc1 .Lgo_%=\n\t" \
  "s_min_i32 s93, s93, 8000\n\t" \
  "s_mov_b32 s94, s93\n\t" \
  ".Lsl_%=:\n\t" \
  "s_sub_i32 s94, s94, 18\n\t" \
  "s_cmp_lt_i32 s94, 30\n\t" \
  "s_cbranch_scc1 .Lsx_%=\n\t" \
  "s_sleep 1\n\t" \
  "s_branch .Lsl_%=\n\t" \
  ".Lsx_%=:\n\t" \
  "s_sub_i32 s93, s93, 12\n\t" \
  "s_max_i32 s93, s93, 4\n\t" \
  "s_lshr_b32 s93, s93, 2\n\t" \
  "s_add_u32 s93, s93, s90\n\t" \
  "s_memrealtime s[90:91]\n\t" \
  ".Lpace_%=:\n\t" \
  "s_memrealtime s[94:95]\n\t" \
  "s_waitcnt lgkmcnt(1)\n\t" \
  "s_sub_i32 s92, s90, s93\n\t" \
  "s_cmp_ge_i32 s92, 0\n\t" \
  "s_cbranch_scc1 .Le_%=\n\t" \
  "s_memrealtime s[90:91]\n\t" \
  "s_waitcnt lgkmcnt(1)\n\t" \
  "s_sub_i32 s92, s94, s93\n\t" \
  "s_cmp_lt_i32 s92, 0\n\t" \
  "s_cbranch_scc1 .Lpace_%=\n\t" \
  "s_nop 4\n\t" \
  ".Le_%=:\n\t" \
  "s_add_u32 s92, s92, s93\n\t" \
  "s_lshl_b32 s92, s92, 2\n\t" \
  "s_sub_i32 s92, %[tgt], s92\n\t" \
  "s_max_i32 s92, s92, 0\n\t" \
  "s_bitcmp0_b32 s92, 3\n\t" \
  "s_cbranch_scc1 .Lf3_%=\n\t" \
  "s_nop 15\n\t" \
  ".Lf3_%=:\n\t" \
  "s_bitcmp0_b32 s92, 2\n\t" \
  "s_cbranch_scc1 .Lf2_%=\n\t" \
  "s_nop 10\n\t" \
  ".Lf2_%=:\n\t" \
  ".Lgo_%=:\n\t" \
  "s_mov_b32 s92, 0\n\t" \
  ".Lpoll_%=:\n\t" \
  "buffer_load_dwordx4 v[200:203], v248, %[rs], %[som] offen sc1\n\t" \
  "buffer_load_dwordx4 v[204:207], v248, %[rs], %[som2] offen sc1\n\t" \
  ".p2align 5\n\t" \
  "s_waitcnt vmcnt(1) lgkmcnt(0)\n\t" \
  "ds_write_b128 v249, v[200:203]\n\t" \
  "v_smfmac_f32_16x16x64_bf16 v[192:195], v[200:203], v[208:215], v251\n\t" \
  "v_max3_u32 v244, v200, v201, v202\n\t" \
  "v_smfmac_f32_16x16x64_bf16 v[196:199], v[200:203], v[236:243], v251\n\t" \
  "s_waitcnt vmcnt(0)\n\t" \
  "ds_write_b128 v249, v[204:207] offset:8192\n\t" \
  "v_smfmac_f32_16x16x64_bf16 v[192:195], v[204:207], v[216:223], v251\n\t" \
  "v_max3_u32 v244, v244, v203, v204\n\t" \
  "v_max3_u32 v244, v244, v205, v206\n\t" \
  "v_smfmac_f32_16x16x64_bf16 v[196:199], v[204:207], v[224:231], v251\n\t" \
  "v_max_u32 v244, v244, v207\n\t" \
  "v_cmp_le_u32_e64 vcc, %[sent], v244\n\t" \
  "s_cbranch_vccnz .Lslow_%=\n\t" \
  ".Lok_%=:\n\t" \
  "ds_read_b128 v[216:219], v250 offset:32768\n\t" \
  "ds_read_b128 v[220:223], v250 offset:33792\n\t" \
  "ds_read_b128 v[224:227], v250 offset:34816\n\t" \
  "ds_read_b128 v[228:231], v250 offset:35840\n\t" \
  "s_waitcnt lgkmcnt(4)\n\t" \
  "s_barrier\n\t" \
  "ds_read_b128 v[208:211], v248 offset:2048\n\t" \
  "ds_read_b128 v[212:215], v248 offset:5120\n\t" \
  "ds_read_b128 v[236:239], v248 offset:6144\n\t" \
  "ds_read_b128 v[240:243], v248 offset:7168\n\t" \
  "s_cmp_eq_u32 %[step], 63\n\t" \
  "s_cbranch_scc1 .Lls_%=\n\t" \
  "s_waitcnt lgkmcnt(3)\n\t" \
  "ds_read_b128 v[244:247], v248 offset:8192\n\t" \
  "v_smfmac_f32_16x16x64_bf16 v[192:195], v[208:211], v[216:223], v251\n\t" \
  "ds_read_b128 v[200:203], v248 offset:12288\n\t" \
  "v_smfmac_f32_16x16x64_bf16 v[196:199], v[208:211], v[224:231], v251\n\t" \
  "s_waitcnt lgkmcnt(4)\n\t" \
  "ds_read_b128 v[216:219], v250 offset:0\n\t" \
  "v_smfmac_f32_16x16x64_bf16 v[192:195], v[212:215], v[0:7], v251\n\t" \
  "ds_read_b128 v[220:223], v250 offset:1024\n\t" \
  "v_smfmac_f32_16x16x64_bf16 v[196:199], v[212:215], v[8:15], v251\n\t" \
  "s_waitcnt lgkmcnt(5)\n\t" \
  "ds_read_b128 v[224:227], v250 offset:2048\n\t" \
  "v_smfmac_f32_16x16x64_bf16 v[192:195], v[236:239], v[16:23], v251\n\t" \
  "ds_read_b128 v[228:231], v250 offset:3072\n\t" \
  "v_smfmac_f32_16x16x64_bf16 v[196:199], v[236:239], v[24:31], v251\n\t" \
  "s_waitcnt lgkmcnt(5)\n\t" \
  "ds_read_b128 v[204:207], v248 offset:9216\n\t" \
  "v_smfmac_f32_16x16x64_bf16 v[192:195], v[240:243], v[32:39], v251\n\t" \
  "ds_read_b128 v[208:211], v248 offset:10240\n\t" \
  "v_smfmac_f32_16x16x64_bf16 v[196:199], v[240:243], v[40:47], v251\n\t" \
  "ds_read_b128 v[212:215], v248 offset:13312\n\t" \
  "v_smfmac_f32_16x16x64_bf16 v[192:195], v[244:247], v[48:55], v251\n\t" \
  "ds_read_b128 v[236:239], v248 offset:14336\n\t" \
  "v_smfmac_f32_16x16x64_bf16 v[196:199], v[244:247], v[56:63], v251\n\t" \
  "s_waitcnt lgkmcnt(6)\n\t" \
  "v_smfmac_f32_16x16x64_bf16 v[192:195], v[200:203], v[216:223], v251\n\t" \
  "s_waitcnt lgkmcnt(4)\n\t" \
  "v_smfmac_f32_16x16x64_bf16 v[196:199], v[200:203], v[224:231], v251\n\t" \
  "s_waitcnt lgkmcnt(3)\n\t" \
  "ds_read_b128 v[240:243], v248 offset:15360\n\t" \
  "v_smfmac_f32_16x16x64_bf16 v[192:195], v[204:207], v[64:71], v251\n\t" \
  "ds_read_b128 v[244:247], v248 offset:0\n\t" \
  "v_smfmac_f32_16x16x64_bf16 v[196:199], v[204:207], v[72:79], v251\n\t" \
  "s_waitcnt lgkmcnt(3)\n\t" \
  "v_smfmac_f32_16x16x64_bf16 v[192:195], v[208:211], v[80:87], v251\n\t" \
  "ds_read_b128 v[200:203], v248 offset:1024\n\t" \
  "v_smfmac_f32_16x16x64_bf16 v[196:199], v[208:211], v[88:95], v251\n\t" \
  "ds_read_b128 v[204:207], v248 offset:4096\n\t" \
  "v_smfmac_f32_16x16x64_bf16 v[192:195], v[212:215], v[96:103], v251\n\t" \
  "v_smfmac_f32_16x16x64_bf16 v[196:199], v[212:215], v[104:111], v251\n\t" \
  "s_waitcnt lgkmcnt(3)\n\t" \
  "v_smfmac_f32_16x16x64_bf16 v[192:195], v[236:239], v[112:119], v251\n\t" \
  "v_smfmac_f32_16x16x64_bf16 v[196:199], v[236:239], v[120:127], v251\n\t" \
  "v_smfmac_f32_16x16x64_bf16 v[192:195], v[240:243], v[128:135], v251\n\t" \
  "v_smfmac_f32_16x16x64_bf16 v[196:199], v[240:243], v[136:143], v251\n\t" \
  "s_waitcnt lgkmcnt(0)\n\t" \
  "v_smfmac_f32_16x16x64_bf16 v[192:195], v[244:247], v[144:151], v251\n\t" \
  "v_smfmac_f32_16x16x64_bf16 v[196:199], v[244:247], v[152:159], v251\n\t" \
  "v_smfmac_f32_16x16x64_bf16 v[192:195], v[200:203], v[160:167], v251\n\t" \
  "v_smfmac_f32_16x16x64_bf16 v[196:199], v[200:203], v[168:175], v251\n\t" \
  "v_smfmac_f32_16x16x64_bf16 v[192:195], v[204:207], v[176:183], v251\n\t" \
  "v_smfmac_f32_16x16x64_bf16 v[196:199], v[204:207], v[184:191], v251\n\t" \
  "ds_read_b128 v[200:203], v248 offset:19456\n\t" \
  "ds_read_b128 v[204:207], v248 offset:23552\n\t" \
  "v_mov_b32 v250, v232\n\t" \
  "v_mov_b32 v249, v233\n\t" \
  "s_nop 2\n\t" \
  "v_add_f32 v192, v192, v194\n\t" \
  "v_add_f32 v193, v193, v195\n\t" \
  "v_add_f32 v196, v196, v198\n\t" \
  "v_add_f32_e64 v197, v197, v199\n\t" \
  "v_cvt_pk_bf16_f32 v192, v192, v193\n\t" \
  "v_cvt_pk_bf16_f32 v196, v196, v197\n\t" \
  "ds_write2_b32 v252, v192, v196 offset1:64\n\t" \
  "v_mov_b32_e64 v251, v234\n\t" \
  "ds_read_b64_tr_b16 v[244:245], v250 offset:1536\n\t" \
  "ds_read_b64_tr_b16 v[246:247], v250 offset:1600\n\t" \
  "v_mov_b32_e64 v212, %[step]\n\t" \
  "s_mov_b64 exec, 1\n\t" \
  "ds_write_b32 v248, v212 offset:28720\n\t" \
  "s_mov_b64 exec, -1\n\t" \
  "buffer_load_dwordx4 v[192:195], v248, %[rt], %[svt] offen offset:0 sc1\n\t" \
  "buffer_load_dwordx4 v[196:199], v248, %[rt], %[svt] offen offset:1024 sc1\n\t" \
  "s_waitcnt lgkmcnt(1)\n\t" \
  "v_mfma_f32_16x16x32_bf16 v[200:203], v[200:203], v[244:247], 0\n\t" \
  "v_mfma_f32_16x16x32_bf16 v[204:207], v[204:207], v[244:247], 0\n\t" \
  "s_nop 7\n\t" \
  "v_cndmask_b32 v216, v200, v204, %[nh]\n\t" \
  "v_cndmask_b32 v217, v201, v205, %[nh]\n\t" \
  "v_cndmask_b32 v218, v202, v206, %[nh]\n\t" \
  "v_cndmask_b32 v219, v203, v207, %[nh]\n\t" \
  "ds_write_b128 v248, v[216:219] offset:19456\n\t" \
  "buffer_load_dword v252, v249, %[rs], %[sv] offen offset:0 sc1\n\t" \
  "buffer_load_dword v253, v249, %[rs], %[sv] offen offset:256 sc1\n\t" \
  "ds_read_b32 v212, v248 offset:28672\n\t" \
  "ds_read_b64_tr_b16 v[232:233], v250 offset:0\n\t" \
  "ds_read_b64_tr_b16 v[234:235], v250 offset:64\n\t" \
  "ds_read_b64_tr_b16 v[236:237], v250 offset:512\n\t" \
  "ds_read_b64_tr_b16 v[238:239], v250 offset:576\n\t" \
  "ds_read_b64_tr_b16 v[240:241], v250 offset:1024\n\t" \
  "ds_read_b64_tr_b16 v[242:243], v250 offset:1088\n\t" \
  "s_setprio 3\n\t" \
  "s_waitcnt lgkmcnt(0)\n\t" \
  "v_cmp_ne_u32 vcc, %[step], v212\n\t" \
  "s_and_b32 s93, vcc_lo, 0xf\n\t" \
  "s_barrier\n\t" \
  "s_cbranch_scc1 .Lre_%=\n\t" \
  ".Lrb_%=:\n\t" \
  "ds_read_b64_tr_b16 v[212:213], v250 offset:3584\n\t" \
  "ds_read_b64_tr_b16 v[214:215], v250 offset:3648\n\t" \
  "ds_read_b64_tr_b16 v[200:201], v250 offset:2048\n\t" \
  "ds_read_b64_tr_b16 v[202:203], v250 offset:2112\n\t" \
  "ds_read_b64_tr_b16 v[204:205], v250 offset:2560\n\t" \
  "ds_read_b64_tr_b16 v[206:207], v250 offset:2624\n\t" \
  "ds_read_b64_tr_b16 v[208:209], v250 offset:3072\n\t" \
  "ds_read_b64_tr_b16 v[210:211], v250 offset:3136\n\t" \
  "ds_read_b128 v[220:223], v248 offset:16384\n\t" \
  "ds_read_b128 v[224:227], v248 offset:17408\n\t" \
  "ds_read_b128 v[228:231], v248 offset:18432\n\t" \
  "s_waitcnt lgkmcnt(9)\n\t" \
  "v_mfma_f32_16x16x32_bf16 v[212:215], v[212:215], v[244:247], 0\n\t" \
  "s_waitcnt lgkmcnt(7)\n\t" \
  "v_mfma_f32_16x16x32_bf16 v[212:215], v[200:203], v[232:235], v[212:215]\n\t" \
  "v_mov_b32 v245, 0\n\t" \
  "s_waitcnt lgkmcnt(5)\n\t" \
  "v_mfma_f32_16x16x32_bf16 v[212:215], v[204:207], v[236:239], v[212:215]\n\t" \
  "v_mov_b32 v246, 0\n\t" \
  "s_waitcnt lgkmcnt(3)\n\t" \
  "v_mfma_f32_16x16x32_bf16 v[212:215], v[208:211], v[240:243], v[212:215]\n\t" \
  "s_waitcnt lgkmcnt(2)\n\t" \
  "v_pk_add_f32 v[216:217], v[216:217], v[220:221]\n\t" \
  "v_pk_add_f32 v[218:219], v[218:219], v[222:223]\n\t" \
  "s_waitcnt lgkmcnt(0)\n\t" \
  "v_pk_add_f32 v[224:225], v[224:225], v[228:229]\n\t" \
  "v_pk_add_f32 v[226:227], v[226:227], v[230:231]\n\t" \
  "v_pk_add_f32 v[216:217], v[216:217], v[224:225]\n\t" \
  "v_pk_add_f32 v[218:219], v[218:219], v[226:227]\n\t" \
  "v_max3_f32 v224, v216, v217, v218\n\t" \
  "v_max_f32 v224, v224, v219\n\t" \
  "v_max3_f32 v231, v212, v213, v214\n\t" \
  "v_max_f32 v231, v231, v215\n\t" \
  "v_max_f32_dpp v225, v224, v224 row_ror:8 row_mask:0xf bank_mask:0xf\n\t" \
  "v_max_f32_dpp v226, v224, v224 row_ror:8 row_mask:0xf bank_mask:0xf\n\t" \
  "v_cndmask_b32 v212, v212, v214, %[up]\n\t" \
  "v_cndmask_b32 v213, v213, v215, %[up]\n\t" \
  "v_permlane32_swap_b32_e64 v225, v226\n\t" \
  "v_max3_f32 v201, v225, v226, v231\n\t" \
  "v_max3_f32 v202, v225, v226, v231\n\t" \
  "v_mov_b64 v[220:221], %[one]\n\t" \
  "v_mov_b64 v[222:223], %[one]\n\t" \
  "v_permlane16_swap_b32 v201, v202\n\t" \
  "v_max_f32 v200, v201, v202\n\t" \
  "v_mul_f32_e64 v200, %[negc], v200\n\t" \
  "v_fma_f32 v212, v212, %[c], v200\n\t" \
  "v_fma_f32 v213, v213, %[c], v200\n\t" \
  "v_exp_f32 v212, v212\n\t" \
  "v_fma_f32 v216, v216, %[c], v200\n\t" \
  "v_exp_f32 v213, v213\n\t" \
  "v_fma_f32 v217, v217, %[c], v200\n\t" \
  "v_exp_f32_e64 v216, v216\n\t" \
  "v_fma_f32 v218, v218, %[c], v200\n\t" \
  "v_cvt_pk_bf16_f32 v244, v212, v213\n\t" \
  "v_cvt_pk_bf16_f32 v247, v212, v213\n\t" \
  "v_exp_f32 v217, v217\n\t" \
  "v_fma_f32 v219, v219, %[c], v200\n\t" \
  "v_mfma_f32_16x16x16_bf16 v[208:211], v[220:221], v[244:245], 0\n\t" \
  "v_exp_f32 v218, v218\n\t" \
  "s_waitcnt vmcnt(0)\n\t" \
  "v_mfma_f32_16x16x16_bf16 v[224:227], v[252:253], v[244:245], 0\n\t" \
  "v_cvt_pk_bf16_f32 v206, v216, v217\n\t" \
  "v_cvt_pk_bf16_f32 v204, v216, v217\n\t" \
  "v_exp_f32 v219, v219\n\t" \
  "v_mfma_f32_16x16x16_bf16 v[228:231], v[252:253], v[246:247], 0\n\t" \
  "v_cvt_pk_bf16_f32 v207, v218, v219\n\t" \
  "v_cvt_pk_bf16_f32 v205, v218, v219\n\t" \
  "v_mov_b32_dpp v204, v206 row_ror:8 row_mask:0xf bank_mask:0xc\n\t" \
  "v_mov_b32_dpp v206, v206 row_ror:8 row_mask:0xf bank_mask:0x3\n\t" \
  "v_mov_b32_dpp v205, v207 row_ror:8 row_mask:0xf bank_mask:0xc\n\t" \
  "v_mov_b32_dpp v207, v207 row_ror:8 row_mask:0xf bank_mask:0x3\n\t" \
  "v_max_u32 v200, v252, v253\n\t" \
  "v_cmp_le_u32 vcc, %[sent], v200\n\t" \
  "v_mfma_f32_16x16x32_bf16 v[208:211], v[220:223], v[204:207], v[208:211]\n\t" \
  ".Lpv_%=:\n\t" \
  "v_mfma_f32_16x16x32_bf16 v[224:227], v[192:195], v[204:207], v[224:227]\n\t" \
  "v_mfma_f32_16x16x32_bf16 v[228:231], v[196:199], v[204:207], v[228:231]\n\t" \
  "s_cbranch_vccnz .Lvs_%=\n\t" \
  ".Lvok_%=:\n\t" \
  "s_nop 4\n\t" \
  "v_rcp_f32_e64 v200, v208\n\t" \
  "v_cndmask_b32 v224, v224, v226, %[nh]\n\t" \
  "v_cndmask_b32 v225, v225, v227, %[nh]\n\t" \
  "v_cndmask_b32 v228, v228, v230, %[nh]\n\t" \
  "v_cndmask_b32 v229, v229, v231, %[nh]\n\t" \
  "v_pk_mul_f32 v[224:225], v[224:225], v[200:201] op_sel_hi:[1,0]\n\t" \
  "v_pk_mul_f32 v[228:229], v[228:229], v[200:201] op_sel_hi:[1,0]\n\t" \
  "v_cvt_pk_bf16_f32 v224, v224, v225\n\t" \
  "v_cvt_pk_bf16_f32 v225, v228, v229\n\t" \
  "buffer_store_dwordx2 v[224:225], v251, %[rs], %[so] offen sc1\n\t" \
  "s_memtime s[90:91]\n\t" \
  "s_setprio 0\n\t" \
  "s_waitcnt lgkmcnt(0)\n\t" \
  "s_mov_b32 %[tout], s90\n\t" \
  "s_mov_b32 %[spins], s92\n\t" \
  "s_branch .Lend_%=\n\t" \
  ".Lls_%=:\n\t" \
  "s_waitcnt lgkmcnt(0)\n\t" \
  "s_barrier\n\t" \
  "s_mov_b32 %[spins], 0\n\t" \
  "s_branch .Lend_%=\n\t" \
  ".Lre_%=:\n\t" \
  "ds_read_b64_tr_b16 v[232:233], v250 offset:0\n\t" \
  "ds_read_b64_tr_b16 v[234:235], v250 offset:64\n\t" \
  "ds_read_b64_tr_b16 v[236:237], v250 offset:512\n\t" \
  "ds_read_b64_tr_b16 v[238:239], v250 offset:576\n\t" \
  "ds_read_b64_tr_b16 v[240:241], v250 offset:1024\n\t" \
  "ds_read_b64_tr_b16 v[242:243], v250 offset:1088\n\t" \
  "s_waitcnt lgkmcnt(0)\n\t" \
  "s_branch .Lrb_%=\n\t" \
  ".Lvs_%=:\n\t" \
  "v_cmp_eq_u32 vcc, %[sent], v252\n\t" \
  "v_cmp_eq_u32 s[94:95], %[sent], v253\n\t" \
  "s_or_b64 s[94:95], s[94:95], vcc\n\t" \
  "s_cbranch_scc0 .Lvok_%=\n\t" \
  "s_add_u32 s92, s92, 1\n\t" \
  "s_sleep 2\n\t" \
  "s_and_b32 s93, s92, 63\n\t" \
  "s_cbranch_scc1 .Lvr_%=\n\t" \
  "buffer_load_dword v200, off, %[rs], %[sab] sc1\n\t" \
  "s_waitcnt vmcnt(0)\n\t" \
  "v_readfirstlane_b32 s93, v200\n\t" \
  "s_nop 3\n\t" \
  "s_cmp_eq_u32 s93, 1\n\t" \
  "s_cbranch_scc1 .Lvok_%=\n\t" \
  "s_cmp_lt_u32 s92, 0x80000\n\t" \
  "s_cbranch_scc1 .Lvr_%=\n\t" \
  "v_mov_b32 v200, 1\n\t" \
  "buffer_store_dword v200, off, %[rs], %[sab] sc1\n\t" \
  "s_branch .Lvok_%=\n\t" \
  ".Lvr_%=:\n\t" \
  "buffer_load_dword v252, v249, %[rs], %[sv] offen offset:0 sc1\n\t" \
  "buffer_load_dword v253, v249, %[rs], %[sv] offen offset:256 sc1\n\t" \
  "s_waitcnt vmcnt(0)\n\t" \
  "v_mfma_f32_16x16x16_bf16 v[224:227], v[252:253], v[244:245], 0\n\t" \
  "v_mfma_f32_16x16x16_bf16 v[228:231], v[252:253], v[246:247], 0\n\t" \
  "v_max_u32 v200, v252, v253\n\t" \
  "v_cmp_le_u32 vcc, %[sent], v200\n\t" \
  "s_nop 4\n\t" \
  "s_branch .Lpv_%=\n\t" \
  "s_branch .Lend_%=\n\t" \
  ".Lslow_%=:\n\t" \
  "s_mov_b64 s[94:95], 0\n\t" \
  "v_cmp_eq_u32 vcc, %[sent], v200\n\t" \
  "s_or_b64 s[94:95], s[94:95], vcc\n\t" \
  "v_cmp_eq_u32 vcc, %[sent], v201\n\t" \
  "s_or_b64 s[94:95], s[94:95], vcc\n\t" \
  "v_cmp_eq_u32 vcc, %[sent], v202\n\t" \
  "s_or_b64 s[94:95], s[94:95], vcc\n\t" \
  "v_cmp_eq_u32 vcc, %[sent], v203\n\t" \
  "s_or_b64 s[94:95], s[94:95], vcc\n\t" \
  "v_cmp_eq_u32 vcc, %[sent], v204\n\t" \
  "s_or_b64 s[94:95], s[94:95], vcc\n\t" \
  "v_cmp_eq_u32 vcc, %[sent], v205\n\t" \
  "s_or_b64 s[94:95], s[94:95], vcc\n\t" \
  "v_cmp_eq_u32 vcc, %[sent], v206\n\t" \
  "s_or_b64 s[94:95], s[94:95], vcc\n\t" \
  "v_cmp_eq_u32 vcc, %[sent], v207\n\t" \
  "s_or_b64 s[94:95], s[94:95], vcc\n\t" \
  "s_cmp_eq_u64 s[94:95], 0\n\t" \
  "s_cbranch_scc1 .Lok_%=\n\t" \
  "v_mov_b64 v[192:193], 0\n\t" \
  "v_mov_b64 v[194:195], 0\n\t" \
  "v_mov_b64 v[196:197], 0\n\t" \
  "v_mov_b64 v[198:199], 0\n\t" \
  "s_add_u32 s92, s92, 1\n\t" \
  "s_cmp_eq_u32 s92, 1\n\t" \
  "s_cbranch_scc1 .Lpoll_%=\n\t" \
  "s_sleep 2\n\t" \
  "s_and_b32 s93, s92, 63\n\t" \
  "s_cmp_lg_u32 s93, 0\n\t" \
  "s_cbranch_scc1 .Lpoll_%=\n\t" \
  "v_mov_b32 v245, 0\n\t" \
  "buffer_load_dword v244, v245, %[rs], %[sab] offen sc1\n\t" \
  "s_waitcnt vmcnt(0)\n\t" \
  "v_readfirstlane_b32 s93, v244\n\t" \
  "s_nop 3\n\t" \
  "s_cmp_eq_u32 s93, 1\n\t" \
  "s_cbranch_scc1 .Lok_%=\n\t" \
  "s_cmp_lt_u32 s92, 0x80000\n\t" \
  "s_cbranch_scc1 .Lpoll_%=\n\t" \
  "v_mov_b32 v244, 1\n\t" \
  "buffer_store_dword v244, v245, %[rs], %[sab] offen sc1\n\t" \
  "s_branch .Lok_%=\n\t" \
  ".Lend_%=:\n\t"
#define QK_ASM_4 \
  "s_waitcnt lgkmcnt(0)\n\t" \
  "v_subrev_u32 v244, 0x8000, v250\n\t" \
  "ds_read_b128 v[216:219], v248 offset:49152\n\t" \
  "ds_read_b128 v[220:223], v248 offset:50176\n\t" \
  "ds_read_b128 v[224:227], v248 offset:51200\n\t" \
  "ds_read_b128 v[228:231], v248 offset:52224\n\t" \
  "ds_read_b128 v[208:211], v244 offset:0\n\t" \
  "ds_read_b128 v[212:215], v244 offset:1024\n\t" \
  "ds_read_b128 v[236:239], v244 offset:2048\n\t" \
  "ds_read_b128 v[240:243], v244 offset:3072\n\t" \
  "v_mov_b64 v[192:193], 0\n\t" \
  "v_mov_b64 v[194:195], 0\n\t" \
  "v_mov_b64 v[196:197], 0\n\t" \
  "v_mov_b64 v[198:199], 0\n\t" \
  ".p2align 5\n\t" \
  "s_nop 0\n\t" \
  "s_nop 0\n\t" \
  "s_nop 0\n\t" \
  "s_nop 0\n\t" \
  "s_movk_i32 s92, 0x2000\n\t" \
  ".Lwt_%=:\n\t" \
  "ds_read_b64 v[200:201], v248 offset:30720\n\t" \
  "s_memrealtime s[90:91]\n\t" \
  "s_sub_u32 s92, s92, 1\n\t" \
  "s_waitcnt lgkmcnt(0)\n\t" \
  "v_readfirstlane_b32 s93, v200\n\t" \
  "v_readfirstlane_b32 %[tgt], v201\n\t" \
  "s_cbranch_scc1 .Lgo_%=\n\t" \
  "s_cmp_eq_u32 s93, %[step]\n\t" \
  "s_cbranch_scc1 .Lwx_%=\n\t" \
  "s_sleep 1\n\t" \
  "s_branch .Lwt_%=\n\t" \
  ".Lwx_%=:\n\t" \
  "s_lshl_b32 s93, s90, 2\n\t" \
  "s_sub_i32 s93, %[tgt], s93\n\t" \
  "s_cmp_le_i32 s93, 0\n\t" \
  "s_cbranch_scc1 .Lgo_%=\n\t" \
  "s_min_i32 s93, s93, 8000\n\t" \
  "s_mov_b32 s94, s93\n\t" \
  ".Lsl_%=:\n\t" \
  "s_sub_i32 s94, s94, 18\n\t" \
  "s_cmp_lt_i32 s94, 30\n\t" \
  "s_cbranch_scc1 .Lsx_%=\n\t" \
  "s_sleep 1\n\t" \
  "s_branch .Lsl_%=\n\t" \
  ".Lsx_%=:\n\t" \
  "s_sub_i32 s93, s93, 12\n\t" \
  "s_max_i32 s93, s93, 4\n\t" \
  "s_lshr_b32 s93, s93, 2\n\t" \
  "s_add_u32 s93, s93, s90\n\t" \
  "s_memrealtime s[90:91]\n\t" \
  ".Lpace_%=:\n\t" \
  "s_memrealtime s[94:95]\n\t" \
  "s_waitcnt lgkmcnt(1)\n\t" \
  "s_sub_i32 s92, s90, s93\n\t" \
  "s_cmp_ge_i32 s92, 0\n\t" \
  "s_cbranch_scc1 .Le_%=\n\t" \
  "s_memrealtime s[90:91]\n\t" \
  "s_waitcnt lgkmcnt(1)\n\t" \
  "s_sub_i32 s92, s94, s93\n\t" \
  "s_cmp_lt_i32 s92, 0\n\t" \
  "s_cbranch_scc1 .Lpace_%=\n\t" \
  "s_nop 4\n\t" \
  ".Le_%=:\n\t" \
  "s_add_u32 s92, s92, s93\n\t" \
  "s_lshl_b32 s92, s92, 2\n\t" \
  "s_sub_i32 s92, %[tgt], s92\n\t" \
  "s_max_i32 s92, s92, 0\n\t" \
  "s_bitcmp0_b32 s92, 3\n\t" \
  "s_cbranch_scc1 .Lf3_%=\n\t" \
  "s_nop 15\n\t" \
  ".Lf3_%=:\n\t" \
  "s_bitcmp0_b32 s92, 2\n\t" \
  "s_cbranch_scc1 .Lf2_%=\n\t" \
  "s_nop 10\n\t" \
  ".Lf2_%=:\n\t" \
  ".Lgo_%=:\n\t" \
  "s_mov_b32 s92, 0\n\t" \
  ".Lpoll_%=:\n\t" \
  "buffer_load_dwordx4 v[200:203], v248, %[rs], %[som] offen sc1\n\t" \
  "buffer_load_dwordx4 v[204:207], v248, %[rs], %[som2] offen sc1\n\t" \
  ".p2align 5\n\t" \
  "s_nop 0\n\t" \
  "s_nop 0\n\t" \
  "s_nop 0\n\t" \
  "s_nop 0\n\t" \
  "s_nop 0\n\t" \
  "s_nop 0\n\t" \
  "s_nop 0\n\t" \
  "s_waitcnt vmcnt(1) lgkmcnt(0)\n\t" \
  "ds_write_b128 v249, v[200:203]\n\t" \
  "v_smfmac_f32_16x16x64_bf16 v[192:195], v[200:203], v[208:215], v251\n\t" \
  "v_max3_u32 v244, v200, v201, v202\n\t" \
  "v_smfmac_f32_16x16x64_bf16 v[196:199], v[200:203], v[236:243], v251\n\t" \
  "s_waitcnt vmcnt(0)\n\t" \
  "ds_write_b128 v249, v[204:207] offset:8192\n\t" \
  "v_smfmac_f32_16x16x64_bf16 v[192:195], v[204:207], v[216:223], v251\n\t" \
  "v_max3_u32 v244, v244, v203, v204\n\t" \
  "v_max3_u32 v244, v244, v205, v206\n\t" \
  "v_smfmac_f32_16x16x64_bf16 v[196:199], v[204:207], v[224:231], v251\n\t" \
  "v_max_u32 v244, v244, v207\n\t" \
  "v_cmp_le_u32 vcc, %[sent], v244\n\t" \
  "s_cbranch_vccnz .Lslow_%=\n\t" \
  ".Lok_%=:\n\t" \
  "s_waitcnt lgkmcnt(0)\n\t" \
  "s_barrier\n\t" \
  "ds_read_b128 v[208:211], v248 offset:6144\n\t" \
  "ds_read_b128 v[212:215], v248 offset:7168\n\t" \
  "ds_read_b128 v[236:239], v248 offset:8192\n\t" \
  "ds_read_b128 v[240:243], v248 offset:9216\n\t" \
  "ds_read_b128 v[244:247], v248 offset:10240\n\t" \
  "s_waitcnt lgkmcnt(4)\n\t" \
  "ds_read_b128 v[200:203], v248 offset:5120\n\t" \
  "v_smfmac_f32_16x16x64_bf16 v[192:195], v[208:211], v[0:7], v251\n\t" \
  "ds_read_b128 v[216:219], v250 offset:32768\n\t" \
  "v_smfmac_f32_16x16x64_bf16 v[196:199], v[208:211], v[8:15], v251\n\t" \
  "s_waitcnt lgkmcnt(5)\n\t" \
  "ds_read_b128 v[220:223], v250 offset:33792\n\t" \
  "v_smfmac_f32_16x16x64_bf16 v[192:195], v[212:215], v[16:23], v251\n\t" \
  "ds_read_b128 v[224:227], v250 offset:34816\n\t" \
  "v_smfmac_f32_16x16x64_bf16 v[196:199], v[212:215], v[24:31], v251\n\t" \
  "s_waitcnt lgkmcnt(6)\n\t" \
  "ds_read_b128 v[228:231], v250 offset:35840\n\t" \
  "v_smfmac_f32_16x16x64_bf16 v[192:195], v[236:239], v[32:39], v251\n\t" \
  "ds_read_b128 v[204:207], v248 offset:11264\n\t" \
  "v_smfmac_f32_16x16x64_bf16 v[196:199], v[236:239], v[40:47], v251\n\t" \
  "s_waitcnt lgkmcnt(7)\n\t" \
  "ds_read_b128 v[208:211], v248 offset:14336\n\t" \
  "v_smfmac_f32_16x16x64_bf16 v[192:195], v[240:243], v[48:55], v251\n\t" \
  "ds_read_b128 v[212:215], v248 offset:15360\n\t" \
  "v_smfmac_f32_16x16x64_bf16 v[196:199], v[240:243], v[56:63], v251\n\t" \
  "s_waitcnt lgkmcnt(7)\n\t" \
  "v_smfmac_f32_16x16x64_bf16 v[192:195], v[244:247], v[64:71], v251\n\t" \
  "ds_read_b128 v[236:239], v248 offset:0\n\t" \
  "v_smfmac_f32_16x16x64_bf16 v[196:199], v[244:247], v[72:79], v251\n\t" \
  "s_waitcnt lgkmcnt(6)\n\t" \
  "v_smfmac_f32_16x16x64_bf16 v[192:195], v[200:203], v[216:223], v251\n\t" \
  "s_waitcnt lgkmcnt(4)\n\t" \
  "ds_read_b128 v[240:243], v248 offset:13312\n\t" \
  "v_smfmac_f32_16x16x64_bf16 v[196:199], v[200:203], v[224:231], v251\n\t" \
  "s_waitcnt lgkmcnt(4)\n\t" \
  "ds_read_b128 v[216:219], v250 offset:0\n\t" \
  "v_smfmac_f32_16x16x64_bf16 v[192:195], v[204:207], v[80:87], v251\n\t" \
  "ds_read_b128 v[220:223], v250 offset:1024\n\t" \
  "v_smfmac_f32_16x16x64_bf16 v[196:199], v[204:207], v[88:95], v251\n\t" \
  "s_waitcnt lgkmcnt(4)\n\t" \
  "ds_read_b128 v[224:227], v250 offset:2048\n\t" \
  "v_smfmac_f32_16x16x64_bf16 v[192:195], v[208:211], v[96:103], v251\n\t" \
  "ds_read_b128 v[228:231], v250 offset:3072\n\t" \
  "v_smfmac_f32_16x16x64_bf16 v[196:199], v[208:211], v[104:111], v251\n\t" \
  "ds_read_b128 v[244:247], v248 offset:1024\n\t" \
  "v_smfmac_f32_16x16x64_bf16 v[192:195], v[212:215], v[112:119], v251\n\t" \
  "ds_read_b128 v[200:203], v248 offset:2048\n\t" \
  "v_smfmac_f32_16x16x64_bf16 v[196:199], v[212:215], v[120:127], v251\n\t" \
  "s_waitcnt lgkmcnt(6)\n\t" \
  "v_smfmac_f32_16x16x64_bf16 v[192:195], v[236:239], v[128:135], v251\n\t" \
  "ds_read_b128 v[204:207], v248 offset:3072\n\t" \
  "v_smfmac_f32_16x16x64_bf16 v[196:199], v[236:239], v[136:143], v251\n\t" \
  "s_waitcnt lgkmcnt(5)\n\t" \
  "v_smfmac_f32_16x16x64_bf16 v[192:195], v[240:243], v[216:223], v251\n\t" \
  "s_waitcnt lgkmcnt(3)\n\t" \
  "v_smfmac_f32_16x16x64_bf16 v[196:199], v[240:243], v[224:231], v251\n\t" \
  "s_waitcnt lgkmcnt(2)\n\t" \
  "v_smfmac_f32_16x16x64_bf16 v[192:195], v[244:247], v[144:151], v251\n\t" \
  "v_smfmac_f32_16x16x64_bf16 v[196:199], v[244:247], v[152:159], v251\n\t" \
  "s_waitcnt lgkmcnt(1)\n\t" \
  "v_smfmac_f32_16x16x64_bf16 v[192:195], v[200:203], v[160:167], v251\n\t" \
  "v_smfmac_f32_16x16x64_bf16 v[196:199], v[200:203], v[168:175], v251\n\t" \
  "s_waitcnt lgkmcnt(0)\n\t" \
  "v_smfmac_f32_16x16x64_bf16 v[192:195], v[204:207], v[176:183], v251\n\t" \
  "v_smfmac_f32_16x16x64_bf16 v[196:199], v[204:207], v[184:191], v251\n\t" \
  "s_nop 6\n\t" \
  "v_pk_add_f32 v[192:193], v[192:193], v[194:195]\n\t" \
  "v_pk_add_f32 v[196:197], v[196:197], v[198:199]\n\t" \
  "v_cvt_pk_bf16_f32 v192, v192, v193\n\t" \
  "v_cvt_pk_bf16_f32 v196, v196, v197\n\t" \
  "ds_write2_b32 v252, v192, v196 offset1:64\n\t" \
  "s_waitcnt lgkmcnt(0)\n\t" \
  "s_barrier\n\t" \
  "buffer_store_dword v192, v235, %[rs], %[sk] offen sc1\n\t" \
  "buffer_store_dword v196, v235, %[rs], %[sk] offen offset:256 sc1\n\t" \
  "s_mov_b32 %[spins], s92\n\t" \
  "s_branch .Lend_%=\n\t" \
  ".Lslow_%=:\n\t" \
  "s_mov_b64 s[94:95], 0\n\t" \
  "v_cmp_eq_u32 vcc, %[sent], v200\n\t" \
  "s_or_b64 s[94:95], s[94:95], vcc\n\t" \
  "v_cmp_eq_u32 vcc, %[sent], v201\n\t" \
  "s_or_b64 s[94:95], s[94:95], vcc\n\t" \
  "v_cmp_eq_u32 vcc, %[sent], v202\n\t" \
  "s_or_b64 s[94:95], s[94:95], vcc\n\t" \
  "v_cmp_eq_u32 vcc, %[sent], v203\n\t" \
  "s_or_b64 s[94:95], s[94:95], vcc\n\t" \
  "v_cmp_eq_u32 vcc, %[sent], v204\n\t" \
  "s_or_b64 s[94:95], s[94:95], vcc\n\t" \
  "v_cmp_eq_u32 vcc, %[sent], v205\n\t" \
  "s_or_b64 s[94:95], s[94:95], vcc\n\t" \
  "v_cmp_eq_u32 vcc, %[sent], v206\n\t" \
  "s_or_b64 s[94:95], s[94:95], vcc\n\t" \
  "v_cmp_eq_u32 vcc, %[sent], v207\n\t" \
  "s_or_b64 s[94:95], s[94:95], vcc\n\t" \
  "s_cmp_eq_u64 s[94:95], 0\n\t" \
  "s_cbranch_scc1 .Lok_%=\n\t" \
  "v_mov_b64 v[192:193], 0\n\t" \
  "v_mov_b64 v[194:195], 0\n\t" \
  "v_mov_b64 v[196:197], 0\n\t" \
  "v_mov_b64 v[198:199], 0\n\t" \
  "s_add_u32 s92, s92, 1\n\t" \
  "s_cmp_eq_u32 s92, 1\n\t" \
  "s_cbranch_scc1 .Lpoll_%=\n\t" \
  "s_sleep 2\n\t" \
  "s_and_b32 s93, s92, 63\n\t" \
  "s_cmp_lg_u32 s93, 0\n\t" \
  "s_cbranch_scc1 .Lpoll_%=\n\t" \
  "v_mov_b32 v245, 0\n\t" \
  "buffer_load_dword v244, v245, %[rs], %[sab] offen sc1\n\t" \
  "s_waitcnt vmcnt(0)\n\t" \
  "v_readfirstlane_b32 s93, v244\n\t" \
  "s_nop 3\n\t" \
  "s_cmp_eq_u32 s93, 1\n\t" \
  "s_cbranch_scc1 .Lok_%=\n\t" \
  "s_cmp_lt_u32 s92, 0x80000\n\t" \
  "s_cbranch_scc1 .Lpoll_%=\n\t" \
  "v_mov_b32 v244, 1\n\t" \
  "buffer_store_dword v244, v245, %[rs], %[sab] offen sc1\n\t" \
  "s_branch .Lok_%=\n\t" \
  ".Lend_%=:\n\t"
#define QK_ASM_5 \
  "s_waitcnt lgkmcnt(0)\n\t" \
  "v_subrev_u32 v244, 0x8000, v250\n\t" \
  "ds_read_b128 v[216:219], v248 offset:53248\n\t" \
  "ds_read_b128 v[220:223], v248 offset:54272\n\t" \
  "ds_read_b128 v[224:227], v248 offset:55296\n\t" \
  "ds_read_b128 v[228:231], v248 offset:56320\n\t" \
  "ds_read_b128 v[208:211], v244 offset:0\n\t" \
  "ds_read_b128 v[212:215], v244 offset:1024\n\t" \
  "ds_read_b128 v[236:239], v244 offset:2048\n\t" \
  "ds_read_b128 v[240:243], v244 offset:3072\n\t" \
  "v_mov_b64 v[192:193], 0\n\t" \
  "v_mov_b64 v[194:195], 0\n\t" \
  "v_mov_b64 v[196:197], 0\n\t" \
  "v_mov_b64 v[198:199], 0\n\t" \
  ".p2align 5\n\t" \
  "s_nop 0\n\t" \
  "s_nop 0\n\t" \
  "s_nop 0\n\t" \
  "s_nop 0\n\t" \
  "s_movk_i32 s92, 0x2000\n\t" \
  ".Lwt_%=:\n\t" \
  "ds_read_b64 v[200:201], v248 offset:30720\n\t" \
  "s_memrealtime s[90:91]\n\t" \
  "s_sub_u32 s92, s92, 1\n\t" \
  "s_waitcnt lgkmcnt(0)\n\t" \
  "v_readfirstlane_b32 s93, v200\n\t" \
  "v_readfirstlane_b32 %[tgt], v201\n\t" \
  "s_cbranch_scc1 .Lgo_%=\n\t" \
  "s_cmp_eq_u32 s93, %[step]\n\t" \
  "s_cbranch_scc1 .Lwx_%=\n\t" \
  "s_sleep 1\n\t" \
  "s_branch .Lwt_%=\n\t" \
  ".Lwx_%=:\n\t" \
  "s_lshl_b32 s93, s90, 2\n\t" \
  "s_sub_i32 s93, %[tgt], s93\n\t" \
  "s_cmp_le_i32 s93, 0\n\t" \
  "s_cbranch_scc1 .Lgo_%=\n\t" \
  "s_min_i32 s93, s93, 8000\n\t" \
  "s_mov_b32 s94, s93\n\t" \
  ".Lsl_%=:\n\t" \
  "s_sub_i32 s94, s94, 18\n\t" \
  "s_cmp_lt_i32 s94, 30\n\t" \
  "s_cbranch_scc1 .Lsx_%=\n\t" \
  "s_sleep 1\n\t" \
  "s_branch .Lsl_%=\n\t" \
  ".Lsx_%=:\n\t" \
  "s_sub_i32 s93, s93, 12\n\t" \
  "s_max_i32 s93, s93, 4\n\t" \
  "s_lshr_b32 s93, s93, 2\n\t" \
  "s_add_u32 s93, s93, s90\n\t" \
  "s_memrealtime s[90:91]\n\t" \
  ".Lpace_%=:\n\t" \
  "s_memrealtime s[94:95]\n\t" \
  "s_waitcnt lgkmcnt(1)\n\t" \
  "s_sub_i32 s92, s90, s93\n\t" \
  "s_cmp_ge_i32 s92, 0\n\t" \
  "s_cbranch_scc1 .Le_%=\n\t" \
  "s_memrealtime s[90:91]\n\t" \
  "s_waitcnt lgkmcnt(1)\n\t" \
  "s_sub_i32 s92, s94, s93\n\t" \
  "s_cmp_lt_i32 s92, 0\n\t" \
  "s_cbranch_scc1 .Lpace_%=\n\t" \
  "s_nop 4\n\t" \
  ".Le_%=:\n\t" \
  "s_add_u32 s92, s92, s93\n\t" \
  "s_lshl_b32 s92, s92, 2\n\t" \
  "s_sub_i32 s92, %[tgt], s92\n\t" \
  "s_max_i32 s92, s92, 0\n\t" \
  "s_bitcmp0_b32 s92, 3\n\t" \
  "s_cbranch_scc1 .Lf3_%=\n\t" \
  "s_nop 15\n\t" \
  ".Lf3_%=:\n\t" \
  "s_bitcmp0_b32 s92, 2\n\t" \
  "s_cbranch_scc1 .Lf2_%=\n\t" \
  "s_nop 10\n\t" \
  ".Lf2_%=:\n\t" \
  ".Lgo_%=:\n\t" \
  "s_mov_b32 s92, 0\n\t" \
  ".Lpoll_%=:\n\t" \
  "buffer_load_dwordx4 v[200:203], v248, %[rs], %[som] offen sc1\n\t" \
  "buffer_load_dwordx4 v[204:207], v248, %[rs], %[som2] offen sc1\n\t" \
  ".p2align 5\n\t" \
  "s_nop 0\n\t" \
  "s_nop 0\n\t" \
  "s_nop 0\n\t" \
  "s_nop 0\n\t" \
  "s_nop 0\n\t" \
  "s_nop 0\n\t" \
  "s_nop 0\n\t" \
  "s_waitcnt vmcnt(1) lgkmcnt(0)\n\t" \
  "ds_write_b128 v249, v[200:203]\n\t" \
  "v_smfmac_f32_16x16x64_bf16 v[192:195], v[200:203], v[208:215], v251\n\t" \
  "v_max3_u32 v244, v200, v201, v202\n\t" \
  "v_smfmac_f32_16x16x64_bf16 v[196:199], v[200:203], v[236:243], v251\n\t" \
  "s_waitcnt vmcnt(0)\n\t" \
  "ds_write_b128 v249, v[204:207] offset:8192\n\t" \
  "v_smfmac_f32_16x16x64_bf16 v[192:195], v[204:207], v[216:223], v251\n\t" \
  "v_max3_u32 v244, v244, v203, v204\n\t" \
  "v_max3_u32 v244, v244, v205, v206\n\t" \
  "v_smfmac_f32_16x16x64_bf16 v[196:199], v[204:207], v[224:231], v251\n\t" \
  "v_max_u32 v244, v244, v207\n\t" \
  "v_cmp_le_u32 vcc, %[sent], v244\n\t" \
  "s_cbranch_vccnz .Lslow_%=\n\t" \
  ".Lok_%=:\n\t" \
  "s_waitcnt lgkmcnt(0)\n\t" \
  "s_barrier\n\t" \
  "ds_read_b128 v[208:211], v248 offset:7168\n\t" \
  "ds_read_b128 v[212:215], v248 offset:8192\n\t" \
  "ds_read_b128 v[236:239], v248 offset:9216\n\t" \
  "ds_read_b128 v[240:243], v248 offset:10240\n\t" \
  "ds_read_b128 v[244:247], v248 offset:11264\n\t" \
  "s_waitcnt lgkmcnt(4)\n\t" \
  "ds_read_b128 v[200:203], v248 offset:4096\n\t" \
  "v_smfmac_f32_16x16x64_bf16 v[192:195], v[208:211], v[0:7], v251\n\t" \
  "ds_read_b128 v[216:219], v250 offset:32768\n\t" \
  "v_smfmac_f32_16x16x64_bf16 v[196:199], v[208:211], v[8:15], v251\n\t" \
  "s_waitcnt lgkmcnt(5)\n\t" \
  "ds_read_b128 v[220:223], v250 offset:33792\n\t" \
  "v_smfmac_f32_16x16x64_bf16 v[192:195], v[212:215], v[16:23], v251\n\t" \
  "ds_read_b128 v[224:227], v250 offset:34816\n\t" \
  "v_smfmac_f32_16x16x64_bf16 v[196:199], v[212:215], v[24:31], v251\n\t" \
  "s_waitcnt lgkmcnt(6)\n\t" \
  "ds_read_b128 v[228:231], v250 offset:35840\n\t" \
  "v_smfmac_f32_16x16x64_bf16 v[192:195], v[236:239], v[32:39], v251\n\t" \
  "ds_read_b128 v[204:207], v248 offset:14336\n\t" \
  "v_smfmac_f32_16x16x64_bf16 v[196:199], v[236:239], v[40:47], v251\n\t" \
  "s_waitcnt lgkmcnt(7)\n\t" \
  "ds_read_b128 v[208:211], v248 offset:15360\n\t" \
  "v_smfmac_f32_16x16x64_bf16 v[192:195], v[240:243], v[48:55], v251\n\t" \
  "ds_read_b128 v[212:215], v248 offset:0\n\t" \
  "v_smfmac_f32_16x16x64_bf16 v[196:199], v[240:243], v[56:63], v251\n\t" \
  "s_waitcnt lgkmcnt(7)\n\t" \
  "v_smfmac_f32_16x16x64_bf16 v[192:195], v[244:247], v[64:71], v251\n\t" \
  "ds_read_b128 v[236:239], v248 offset:1024\n\t" \
  "v_smfmac_f32_16x16x64_bf16 v[196:199], v[244:247], v[72:79], v251\n\t" \
  "s_waitcnt lgkmcnt(6)\n\t" \
  "v_smfmac_f32_16x16x64_bf16 v[192:195], v[200:203], v[216:223], v251\n\t" \
  "s_waitcnt lgkmcnt(4)\n\t" \
  "ds_read_b128 v[240:243], v248 offset:12288\n\t" \
  "v_smfmac_f32_16x16x64_bf16 v[196:199], v[200:203], v[224:231], v251\n\t" \
  "s_waitcnt lgkmcnt(4)\n\t" \
  "ds_read_b128 v[216:219], v250 offset:0\n\t" \
  "v_smfmac_f32_16x16x64_bf16 v[192:195], v[204:207], v[80:87], v251\n\t" \
  "ds_read_b128 v[220:223], v250 offset:1024\n\t" \
  "v_smfmac_f32_16x16x64_bf16 v[196:199], v[204:207], v[88:95], v251\n\t" \
  "s_waitcnt lgkmcnt(4)\n\t" \
  "ds_read_b128 v[224:227], v250 offset:2048\n\t" \
  "v_smfmac_f32_16x16x64_bf16 v[192:195], v[208:211], v[96:103], v251\n\t" \
  "ds_read_b128 v[228:231], v250 offset:3072\n\t" \
  "v_smfmac_f32_16x16x64_bf16 v[196:199], v[208:211], v[104:111], v251\n\t" \
  "ds_read_b128 v[244:247], v248 offset:2048\n\t" \
  "v_smfmac_f32_16x16x64_bf16 v[192:195], v[212:215], v[112:119], v251\n\t" \
  "ds_read_b128 v[200:203], v248 offset:3072\n\t" \
  "v_smfmac_f32_16x16x64_bf16 v[196:199], v[212:215], v[120:127], v251\n\t" \
  "s_waitcnt lgkmcnt(6)\n\t" \
  "v_smfmac_f32_16x16x64_bf16 v[192:195], v[236:239], v[128:135], v251\n\t" \
  "ds_read_b128 v[204:207], v248 offset:6144\n\t" \
  "v_smfmac_f32_16x16x64_bf16 v[196:199], v[236:239], v[136:143], v251\n\t" \
  "s_waitcnt lgkmcnt(5)\n\t" \
  "v_smfmac_f32_16x16x64_bf16 v[192:195], v[240:243], v[216:223], v251\n\t" \
  "s_waitcnt lgkmcnt(3)\n\t" \
  "v_smfmac_f32_16x16x64_bf16 v[196:199], v[240:243], v[224:231], v251\n\t" \
  "s_waitcnt lgkmcnt(2)\n\t" \
  "v_smfmac_f32_16x16x64_bf16 v[192:195], v[244:247], v[144:151], v251\n\t" \
  "v_smfmac_f32_16x16x64_bf16 v[196:199], v[244:247], v[152:159], v251\n\t" \
  "s_waitcnt lgkmcnt(1)\n\t" \
  "v_smfmac_f32_16x16x64_bf16 v[192:195], v[200:203], v[160:167], v251\n\t" \
  "v_smfmac_f32_16x16x64_bf16 v[196:199], v[200:203], v[168:175], v251\n\t" \
  "s_waitcnt lgkmcnt(0)\n\t" \
  "v_smfmac_f32_16x16x64_bf16 v[192:195], v[204:207], v[176:183], v251\n\t" \
  "v_smfmac_f32_16x16x64_bf16 v[196:199], v[204:207], v[184:191], v251\n\t" \
  "s_nop 6\n\t" \
  "v_pk_add_f32 v[192:193], v[192:193], v[194:195]\n\t" \
  "v_pk_add_f32 v[196:197], v[196:197], v[198:199]\n\t" \
  "v_cvt_pk_bf16_f32 v192, v192, v193\n\t" \
  "v_cvt_pk_bf16_f32 v196, v196, v197\n\t" \
  "ds_write2_b32 v252, v192, v196 offset1:64\n\t" \
  "s_waitcnt lgkmcnt(0)\n\t" \
  "s_barrier\n\t" \
  "buffer_store_dword v192, v235, %[rs], %[sk] offen sc1\n\t" \
  "buffer_store_dword v196, v235, %[rs], %[sk] offen offset:256 sc1\n\t" \
  "s_mov_b32 %[spins], s92\n\t" \
  "s_branch .Lend_%=\n\t" \
  ".Lslow_%=:\n\t" \
  "s_mov_b64 s[94:95], 0\n\t" \
  "v_cmp_eq_u32 vcc, %[sent], v200\n\t" \
  "s_or_b64 s[94:95], s[94:95], vcc\n\t" \
  "v_cmp_eq_u32 vcc, %[sent], v201\n\t" \
  "s_or_b64 s[94:95], s[94:95], vcc\n\t" \
  "v_cmp_eq_u32 vcc, %[sent], v202\n\t" \
  "s_or_b64 s[94:95], s[94:95], vcc\n\t" \
  "v_cmp_eq_u32 vcc, %[sent], v203\n\t" \
  "s_or_b64 s[94:95], s[94:95], vcc\n\t" \
  "v_cmp_eq_u32 vcc, %[sent], v204\n\t" \
  "s_or_b64 s[94:95], s[94:95], vcc\n\t" \
  "v_cmp_eq_u32 vcc, %[sent], v205\n\t" \
  "s_or_b64 s[94:95], s[94:95], vcc\n\t" \
  "v_cmp_eq_u32 vcc, %[sent], v206\n\t" \
  "s_or_b64 s[94:95], s[94:95], vcc\n\t" \
  "v_cmp_eq_u32 vcc, %[sent], v207\n\t" \
  "s_or_b64 s[94:95], s[94:95], vcc\n\t" \
  "s_cmp_eq_u64 s[94:95], 0\n\t" \
  "s_cbranch_scc1 .Lok_%=\n\t" \
  "v_mov_b64 v[192:193], 0\n\t" \
  "v_mov_b64 v[194:195], 0\n\t" \
  "v_mov_b64 v[196:197], 0\n\t" \
  "v_mov_b64 v[198:199], 0\n\t" \
  "s_add_u32 s92, s92, 1\n\t" \
  "s_cmp_eq_u32 s92, 1\n\t" \
  "s_cbranch_scc1 .Lpoll_%=\n\t" \
  "s_sleep 2\n\t" \
  "s_and_b32 s93, s92, 63\n\t" \
  "s_cmp_lg_u32 s93, 0\n\t" \
  "s_cbranch_scc1 .Lpoll_%=\n\t" \
  "v_mov_b32 v245, 0\n\t" \
  "buffer_load_dword v244, v245, %[rs], %[sab] offen sc1\n\t" \
  "s_waitcnt vmcnt(0)\n\t" \
  "v_readfirstlane_b32 s93, v244\n\t" \
  "s_nop 3\n\t" \
  "s_cmp_eq_u32 s93, 1\n\t" \
  "s_cbranch_scc1 .Lok_%=\n\t" \
  "s_cmp_lt_u32 s92, 0x80000\n\t" \
  "s_cbranch_scc1 .Lpoll_%=\n\t" \
  "v_mov_b32 v244, 1\n\t" \
  "buffer_store_dword v244, v245, %[rs], %[sab] offen sc1\n\t" \
  "s_branch .Lok_%=\n\t" \
  ".Lend_%=:\n\t"
#define QK_ASM_6 \
  "s_waitcnt lgkmcnt(0)\n\t" \
  "v_subrev_u32 v244, 0x8000, v250\n\t" \
  "ds_read_b128 v[216:219], v248 offset:57344\n\t" \
  "ds_read_b128 v[220:223], v248 offset:58368\n\t" \
  "ds_read_b128 v[224:227], v248 offset:59392\n\t" \
  "ds_read_b128 v[228:231], v248 offset:60416\n\t" \
  "ds_read_b128 v[208:211], v244 offset:0\n\t" \
  "ds_read_b128 v[212:215], v244 offset:1024\n\t" \
  "ds_read_b128 v[236:239], v244 offset:2048\n\t" \
  "ds_read_b128 v[240:243], v244 offset:3072\n\t" \
  "v_mov_b64 v[192:193], 0\n\t" \
  "v_mov_b64 v[194:195], 0\n\t" \
  "v_mov_b64 v[196:197], 0\n\t" \
  "v_mov_b64 v[198:199], 0\n\t" \
  ".p2align 5\n\t" \
  "s_nop 0\n\t" \
  "s_nop 0\n\t" \
  "s_nop 0\n\t" \
  "s_nop 0\n\t" \
  "s_movk_i32 s92, 0x2000\n\t" \
  ".Lwt_%=:\n\t" \
  "ds_read_b64 v[200:201], v248 offset:30720\n\t" \
  "s_memrealtime s[90:91]\n\t" \
  "s_sub_u32 s92, s92, 1\n\t" \
  "s_waitcnt lgkmcnt(0)\n\t" \
  "v_readfirstlane_b32 s93, v200\n\t" \
  "v_readfirstlane_b32 %[tgt], v201\n\t" \
  "s_cbranch_scc1 .Lgo_%=\n\t" \
  "s_cmp_eq_u32 s93, %[step]\n\t" \
  "s_cbranch_scc1 .Lwx_%=\n\t" \
  "s_sleep 1\n\t" \
  "s_branch .Lwt_%=\n\t" \
  ".Lwx_%=:\n\t" \
  "s_lshl_b32 s93, s90, 2\n\t" \
  "s_sub_i32 s93, %[tgt], s93\n\t" \
  "s_cmp_le_i32 s93, 0\n\t" \
  "s_cbranch_scc1 .Lgo_%=\n\t" \
  "s_min_i32 s93, s93, 8000\n\t" \
  "s_mov_b32 s94, s93\n\t" \
  ".Lsl_%=:\n\t" \
  "s_sub_i32 s94, s94, 18\n\t" \
  "s_cmp_lt_i32 s94, 30\n\t" \
  "s_cbranch_scc1 .Lsx_%=\n\t" \
  "s_sleep 1\n\t" \
  "s_branch .Lsl_%=\n\t" \
  ".Lsx_%=:\n\t" \
  "s_sub_i32 s93, s93, 12\n\t" \
  "s_max_i32 s93, s93, 4\n\t" \
  "s_lshr_b32 s93, s93, 2\n\t" \
  "s_add_u32 s93, s93, s90\n\t" \
  "s_memrealtime s[90:91]\n\t" \
  ".Lpace_%=:\n\t" \
  "s_memrealtime s[94:95]\n\t" \
  "s_waitcnt lgkmcnt(1)\n\t" \
  "s_sub_i32 s92, s90, s93\n\t" \
  "s_cmp_ge_i32 s92, 0\n\t" \
  "s_cbranch_scc1 .Le_%=\n\t" \
  "s_memrealtime s[90:91]\n\t" \
  "s_waitcnt lgkmcnt(1)\n\t" \
  "s_sub_i32 s92, s94, s93\n\t" \
  "s_cmp_lt_i32 s92, 0\n\t" \
  "s_cbranch_scc1 .Lpace_%=\n\t" \
  "s_nop 4\n\t" \
  ".Le_%=:\n\t" \
  "s_add_u32 s92, s92, s93\n\t" \
  "s_lshl_b32 s92, s92, 2\n\t" \
  "s_sub_i32 s92, %[tgt], s92\n\t" \
  "s_max_i32 s92, s92, 0\n\t" \
  "s_bitcmp0_b32 s92, 3\n\t" \
  "s_cbranch_scc1 .Lf3_%=\n\t" \
  "s_nop 15\n\t" \
  ".Lf3_%=:\n\t" \
  "s_bitcmp0_b32 s92, 2\n\t" \
  "s_cbranch_scc1 .Lf2_%=\n\t" \
  "s_nop 10\n\t" \
  ".Lf2_%=:\n\t" \
  ".Lgo_%=:\n\t" \
  "s_mov_b32 s92, 0\n\t" \
  ".Lpoll_%=:\n\t" \
  "buffer_load_dwordx4 v[200:203], v248, %[rs], %[som] offen sc1\n\t" \
  "buffer_load_dwordx4 v[204:207], v248, %[rs], %[som2] offen sc1\n\t" \
  ".p2align 5\n\t" \
  "s_nop 0\n\t" \
  "s_nop 0\n\t" \
  "s_nop 0\n\t" \
  "s_nop 0\n\t" \
  "s_nop 0\n\t" \
  "s_nop 0\n\t" \
  "s_nop 0\n\t" \
  "s_waitcnt vmcnt(1) lgkmcnt(0)\n\t" \
  "ds_write_b128 v249, v[200:203]\n\t" \
  "v_smfmac_f32_16x16x64_bf16 v[192:195], v[200:203], v[208:215], v251\n\t" \
  "v_max3_u32 v244, v200, v201, v202\n\t" \
  "v_smfmac_f32_16x16x64_bf16 v[196:199], v[200:203], v[236:243], v251\n\t" \
  "s_waitcnt vmcnt(0)\n\t" \
  "ds_write_b128 v249, v[204:207] offset:8192\n\t" \
  "v_smfmac_f32_16x16x64_bf16 v[192:195], v[204:207], v[216:223], v251\n\t" \
  "v_max3_u32 v244, v244, v203, v204\n\t" \
  "v_max3_u32 v244, v244, v205, v206\n\t" \
  "v_smfmac_f32_16x16x64_bf16 v[196:199], v[204:207], v[224:231], v251\n\t" \
  "v_max_u32 v244, v244, v207\n\t" \
  "v_cmp_le_u32 vcc, %[sent], v244\n\t" \
  "s_cbranch_vccnz .Lslow_%=\n\t" \
  ".Lok_%=:\n\t" \
  "s_waitcnt lgkmcnt(0)\n\t" \
  "s_barrier\n\t" \
  "ds_read_b128 v[208:211], v248 offset:8192\n\t" \
  "ds_read_b128 v[212:215], v248 offset:9216\n\t" \
  "ds_read_b128 v[236:239], v248 offset:10240\n\t" \
  "ds_read_b128 v[240:243], v248 offset:11264\n\t" \
  "ds_read_b128 v[244:247], v248 offset:12288\n\t" \
  "s_waitcnt lgkmcnt(4)\n\t" \
  "ds_read_b128 v[200:203], v248 offset:7168\n\t" \
  "v_smfmac_f32_16x16x64_bf16 v[192:195], v[208:211], v[0:7], v251\n\t" \
  "ds_read_b128 v[216:219], v250 offset:32768\n\t" \
  "v_smfmac_f32_16x16x64_bf16 v[196:199], v[208:211], v[8:15], v251\n\t" \
  "s_waitcnt lgkmcnt(5)\n\t" \
  "ds_read_b128 v[220:223], v250 offset:33792\n\t" \
  "v_smfmac_f32_16x16x64_bf16 v[192:195], v[212:215], v[16:23], v251\n\t" \
  "ds_read_b128 v[224:227], v250 offset:34816\n\t" \
  "v_smfmac_f32_16x16x64_bf16 v[196:199], v[212:215], v[24:31], v251\n\t" \
  "s_waitcnt lgkmcnt(6)\n\t" \
  "ds_read_b128 v[228:231], v250 offset:35840\n\t" \
  "v_smfmac_f32_16x16x64_bf16 v[192:195], v[236:239], v[32:39], v251\n\t" \
  "ds_read_b128 v[204:207], v248 offset:15360\n\t" \
  "v_smfmac_f32_16x16x64_bf16 v[196:199], v[236:239], v[40:47], v251\n\t" \
  "s_waitcnt lgkmcnt(7)\n\t" \
  "ds_read_b128 v[208:211], v248 offset:0\n\t" \
  "v_smfmac_f32_16x16x64_bf16 v[192:195], v[240:243], v[48:55], v251\n\t" \
  "ds_read_b128 v[212:215], v248 offset:1024\n\t" \
  "v_smfmac_f32_16x16x64_bf16 v[196:199], v[240:243], v[56:63], v251\n\t" \
  "s_waitcnt lgkmcnt(7)\n\t" \
  "v_smfmac_f32_16x16x64_bf16 v[192:195], v[244:247], v[64:71], v251\n\t" \
  "ds_read_b128 v[236:239], v248 offset:2048\n\t" \
  "v_smfmac_f32_16x16x64_bf16 v[196:199], v[244:247], v[72:79], v251\n\t" \
  "s_waitcnt lgkmcnt(6)\n\t" \
  "v_smfmac_f32_16x16x64_bf16 v[192:195], v[200:203], v[216:223], v251\n\t" \
  "s_waitcnt lgkmcnt(4)\n\t" \
  "ds_read_b128 v[240:243], v248 offset:13312\n\t" \
  "v_smfmac_f32_16x16x64_bf16 v[196:199], v[200:203], v[224:231], v251\n\t" \
  "s_waitcnt lgkmcnt(4)\n\t" \
  "ds_read_b128 v[216:219], v250 offset:0\n\t" \
  "v_smfmac_f32_16x16x64_bf16 v[192:195], v[204:207], v[80:87], v251\n\t" \
  "ds_read_b128 v[220:223], v250 offset:1024\n\t" \
  "v_smfmac_f32_16x16x64_bf16 v[196:199], v[204:207], v[88:95], v251\n\t" \
  "s_waitcnt lgkmcnt(4)\n\t" \
  "ds_read_b128 v[224:227], v250 offset:2048\n\t" \
  "v_smfmac_f32_16x16x64_bf16 v[192:195], v[208:211], v[96:103], v251\n\t" \
  "ds_read_b128 v[228:231], v250 offset:3072\n\t" \
  "v_smfmac_f32_16x16x64_bf16 v[196:199], v[208:211], v[104:111], v251\n\t" \
  "ds_read_b128 v[244:247], v248 offset:3072\n\t" \
  "v_smfmac_f32_16x16x64_bf16 v[192:195], v[212:215], v[112:119], v251\n\t" \
  "ds_read_b128 v[200:203], v248 offset:4096\n\t" \
  "v_smfmac_f32_16x16x64_bf16 v[196:199], v[212:215], v[120:127], v251\n\t" \
  "s_waitcnt lgkmcnt(6)\n\t" \
  "v_smfmac_f32_16x16x64_bf16 v[192:195], v[236:239], v[128:135], v251\n\t" \
  "ds_read_b128 v[204:207], v248 offset:5120\n\t" \
  "v_smfmac_f32_16x16x64_bf16 v[196:199], v[236:239], v[136:143], v251\n\t" \
  "s_waitcnt lgkmcnt(5)\n\t" \
  "v_smfmac_f32_16x16x64_bf16 v[192:195], v[240:243], v[216:223], v251\n\t" \
  "s_waitcnt lgkmcnt(3)\n\t" \
  "v_smfmac_f32_16x16x64_bf16 v[196:199], v[240:243], v[224:231], v251\n\t" \
  "s_waitcnt lgkmcnt(2)\n\t" \
  "v_smfmac_f32_16x16x64_bf16 v[192:195], v[244:247], v[144:151], v251\n\t" \
  "v_smfmac_f32_16x16x64_bf16 v[196:199], v[244:247], v[152:159], v251\n\t" \
  "s_waitcnt lgkmcnt(1)\n\t" \
  "v_smfmac_f32_16x16x64_bf16 v[192:195], v[200:203], v[160:167], v251\n\t" \
  "v_smfmac_f32_16x16x64_bf16 v[196:199], v[200:203], v[168:175], v251\n\t" \
  "s_waitcnt lgkmcnt(0)\n\t" \
  "v_smfmac_f32_16x16x64_bf16 v[192:195], v[204:207], v[176:183], v251\n\t" \
  "v_smfmac_f32_16x16x64_bf16 v[196:199], v[204:207], v[184:191], v251\n\t" \
  "s_nop 6\n\t" \
  "v_pk_add_f32 v[192:193], v[192:193], v[194:195]\n\t" \
  "v_pk_add_f32 v[196:197], v[196:197], v[198:199]\n\t" \
  "v_cvt_pk_bf16_f32 v192, v192, v193\n\t" \
  "v_cvt_pk_bf16_f32 v196, v196, v197\n\t" \
  "ds_write2_b32 v252, v192, v196 offset1:64\n\t" \
  "s_waitcnt lgkmcnt(0)\n\t" \
  "s_barrier\n\t" \
  "buffer_store_dword v192, v235, %[rs], %[sk] offen sc1\n\t" \
  "buffer_store_dword v196, v235, %[rs], %[sk] offen offset:256 sc1\n\t" \
  "s_mov_b32 %[spins], s92\n\t" \
  "s_branch .Lend_%=\n\t" \
  ".Lslow_%=:\n\t" \
  "s_mov_b64 s[94:95], 0\n\t" \
  "v_cmp_eq_u32 vcc, %[sent], v200\n\t" \
  "s_or_b64 s[94:95], s[94:95], vcc\n\t" \
  "v_cmp_eq_u32 vcc, %[sent], v201\n\t" \
  "s_or_b64 s[94:95], s[94:95], vcc\n\t" \
  "v_cmp_eq_u32 vcc, %[sent], v202\n\t" \
  "s_or_b64 s[94:95], s[94:95], vcc\n\t" \
  "v_cmp_eq_u32 vcc, %[sent], v203\n\t" \
  "s_or_b64 s[94:95], s[94:95], vcc\n\t" \
  "v_cmp_eq_u32 vcc, %[sent], v204\n\t" \
  "s_or_b64 s[94:95], s[94:95], vcc\n\t" \
  "v_cmp_eq_u32 vcc, %[sent], v205\n\t" \
  "s_or_b64 s[94:95], s[94:95], vcc\n\t" \
  "v_cmp_eq_u32 vcc, %[sent], v206\n\t" \
  "s_or_b64 s[94:95], s[94:95], vcc\n\t" \
  "v_cmp_eq_u32 vcc, %[sent], v207\n\t" \
  "s_or_b64 s[94:95], s[94:95], vcc\n\t" \
  "s_cmp_eq_u64 s[94:95], 0\n\t" \
  "s_cbranch_scc1 .Lok_%=\n\t" \
  "v_mov_b64 v[192:193], 0\n\t" \
  "v_mov_b64 v[194:195], 0\n\t" \
  "v_mov_b64 v[196:197], 0\n\t" \
  "v_mov_b64 v[198:199], 0\n\t" \
  "s_add_u32 s92, s92, 1\n\t" \
  "s_cmp_eq_u32 s92, 1\n\t" \
  "s_cbranch_scc1 .Lpoll_%=\n\t" \
  "s_sleep 2\n\t" \
  "s_and_b32 s93, s92, 63\n\t" \
  "s_cmp_lg_u32 s93, 0\n\t" \
  "s_cbranch_scc1 .Lpoll_%=\n\t" \
  "v_mov_b32 v245, 0\n\t" \
  "buffer_load_dword v244, v245, %[rs], %[sab] offen sc1\n\t" \
  "s_waitcnt vmcnt(0)\n\t" \
  "v_readfirstlane_b32 s93, v244\n\t" \
  "s_nop 3\n\t" \
  "s_cmp_eq_u32 s93, 1\n\t" \
  "s_cbranch_scc1 .Lok_%=\n\t" \
  "s_cmp_lt_u32 s92, 0x80000\n\t" \
  "s_cbranch_scc1 .Lpoll_%=\n\t" \
  "v_mov_b32 v244, 1\n\t" \
  "buffer_store_dword v244, v245, %[rs], %[sab] offen sc1\n\t" \
  "s_branch .Lok_%=\n\t" \
  ".Lend_%=:\n\t"
#define QK_ASM_7 \
  "s_waitcnt lgkmcnt(0)\n\t" \
  "v_subrev_u32 v244, 0x8000, v250\n\t" \
  "ds_read_b128 v[216:219], v248 offset:61440\n\t" \
  "ds_read_b128 v[220:223], v248 offset:62464\n\t" \
  "ds_read_b128 v[224:227], v248 offset:63488\n\t" \
  "ds_read_b128 v[228:231], v248 offset:64512\n\t" \
  "ds_read_b128 v[208:211], v244 offset:0\n\t" \
  "ds_read_b128 v[212:215], v244 offset:1024\n\t" \
  "ds_read_b128 v[236:239], v244 offset:2048\n\t" \
  "ds_read_b128 v[240:243], v244 offset:3072\n\t" \
  "v_mov_b64 v[192:193], 0\n\t" \
  "v_mov_b64 v[194:195], 0\n\t" \
  "v_mov_b64 v[196:197], 0\n\t" \
  "v_mov_b64 v[198:199], 0\n\t" \
  ".p2align 5\n\t" \
  "s_nop 0\n\t" \
  "s_nop 0\n\t" \
  "s_nop 0\n\t" \
  "s_nop 0\n\t" \
  "s_movk_i32 s92, 0x2000\n\t" \
  ".Lwt_%=:\n\t" \
  "ds_read_b64 v[200:201], v248 offset:30720\n\t" \
  "s_memrealtime s[90:91]\n\t" \
  "s_sub_u32 s92, s92, 1\n\t" \
  "s_waitcnt lgkmcnt(0)\n\t" \
  "v_readfirstlane_b32 s93, v200\n\t" \
  "v_readfirstlane_b32 %[tgt], v201\n\t" \
  "s_cbranch_scc1 .Lgo_%=\n\t" \
  "s_cmp_eq_u32 s93, %[step]\n\t" \
  "s_cbranch_scc1 .Lwx_%=\n\t" \
  "s_sleep 1\n\t" \
  "s_branch .Lwt_%=\n\t" \
  ".Lwx_%=:\n\t" \
  "s_lshl_b32 s93, s90, 2\n\t" \
  "s_sub_i32 s93, %[tgt], s93\n\t" \
  "s_cmp_le_i32 s93, 0\n\t" \
  "s_cbranch_scc1 .Lgo_%=\n\t" \
  "s_min_i32 s93, s93, 8000\n\t" \
  "s_mov_b32 s94, s93\n\t" \
  ".Lsl_%=:\n\t" \
  "s_sub_i32 s94, s94, 18\n\t" \
  "s_cmp_lt_i32 s94, 30\n\t" \
  "s_cbranch_scc1 .Lsx_%=\n\t" \
  "s_sleep 1\n\t" \
  "s_branch .Lsl_%=\n\t" \
  ".Lsx_%=:\n\t" \
  "s_sub_i32 s93, s93, 12\n\t" \
  "s_max_i32 s93, s93, 4\n\t" \
  "s_lshr_b32 s93, s93, 2\n\t" \
  "s_add_u32 s93, s93, s90\n\t" \
  "s_memrealtime s[90:91]\n\t" \
  ".Lpace_%=:\n\t" \
  "s_memrealtime s[94:95]\n\t" \
  "s_waitcnt lgkmcnt(1)\n\t" \
  "s_sub_i32 s92, s90, s93\n\t" \
  "s_cmp_ge_i32 s92, 0\n\t" \
  "s_cbranch_scc1 .Le_%=\n\t" \
  "s_memrealtime s[90:91]\n\t" \
  "s_waitcnt lgkmcnt(1)\n\t" \
  "s_sub_i32 s92, s94, s93\n\t" \
  "s_cmp_lt_i32 s92, 0\n\t" \
  "s_cbranch_scc1 .Lpace_%=\n\t" \
  "s_nop 4\n\t" \
  ".Le_%=:\n\t" \
  "s_add_u32 s92, s92, s93\n\t" \
  "s_lshl_b32 s92, s92, 2\n\t" \
  "s_sub_i32 s92, %[tgt], s92\n\t" \
  "s_max_i32 s92, s92, 0\n\t" \
  "s_bitcmp0_b32 s92, 3\n\t" \
  "s_cbranch_scc1 .Lf3_%=\n\t" \
  "s_nop 15\n\t" \
  ".Lf3_%=:\n\t" \
  "s_bitcmp0_b32 s92, 2\n\t" \
  "s_cbranch_scc1 .Lf2_%=\n\t" \
  "s_nop 10\n\t" \
  ".Lf2_%=:\n\t" \
  ".Lgo_%=:\n\t" \
  "s_mov_b32 s92, 0\n\t" \
  "s_memtime s[88:89]\n\t" \
  ".Lpoll_%=:\n\t" \
  "buffer_load_dwordx4 v[200:203], v248, %[rs], %[som] offen sc1\n\t" \
  "buffer_load_dwordx4 v[204:207], v248, %[rs], %[som2] offen sc1\n\t" \
  ".p2align 5\n\t" \
  "s_nop 0\n\t" \
  "s_nop 0\n\t" \
  "s_nop 0\n\t" \
  "s_nop 0\n\t" \
  "s_nop 0\n\t" \
  "s_nop 0\n\t" \
  "s_nop 0\n\t" \
  "s_waitcnt vmcnt(1) lgkmcnt(0)\n\t" \
  "ds_write_b128 v249, v[200:203]\n\t" \
  "v_smfmac_f32_16x16x64_bf16 v[192:195], v[200:203], v[208:215], v251\n\t" \
  "v_max3_u32 v244, v200, v201, v202\n\t" \
  "v_smfmac_f32_16x16x64_bf16 v[196:199], v[200:203], v[236:243], v251\n\t" \
  "s_waitcnt vmcnt(0)\n\t" \
  "ds_write_b128 v249, v[204:207] offset:8192\n\t" \
  "v_smfmac_f32_16x16x64_bf16 v[192:195], v[204:207], v[216:223], v251\n\t" \
  "v_max3_u32 v244, v244, v203, v204\n\t" \
  "v_max3_u32 v244, v244, v205, v206\n\t" \
  "v_smfmac_f32_16x16x64_bf16 v[196:199], v[204:207], v[224:231], v251\n\t" \
  "v_max_u32 v244, v244, v207\n\t" \
  "v_cmp_le_u32 vcc, %[sent], v244\n\t" \
  "s_cbranch_vccnz .Lslow_%=\n\t" \
  ".Lok_%=:\n\t" \
  "s_waitcnt lgkmcnt(0)\n\t" \
  "s_setprio 3\n\t" \
  "s_barrier\n\t" \
  "s_memrealtime s[90:91]\n\t" \
  "s_memtime s[94:95]\n\t" \
  "v_mov_b32 v232, %[step]\n\t" \
  "v_lshrrev_b32_e64 v234, 1, v248\n\t" \
  "s_mov_b64 exec, 0xffff\n\t" \
  "s_waitcnt lgkmcnt(0)\n\t" \
  "s_lshl_b32 s90, s90, 2\n\t" \
  "s_sub_u32 s94, s94, s88\n\t" \
  "s_mul_i32 s94, s94, 12200\n\t" \
  "s_lshr_b32 s94, s94, 16\n\t" \
  "s_add_u32 s94, s94, %[tgt]\n\t" \
  "s_sub_i32 s93, s90, s94\n\t" \
  "s_sub_i32 s93, s93, 30\n\t" \
  "s_max_i32 s93, s93, 0\n\t" \
  "s_add_u32 s90, s94, s93\n\t" \
  "s_sub_i32 s93, s90, %[tgt]\n\t" \
  "s_cmp_gt_i32 s93, 270\n\t" \
  "s_cselect_b32 s93, 70, 0\n\t" \
  "s_add_u32 s93, s93, s90\n\t" \
  "s_add_u32 s93, s93, %[lead]\n\t" \
  "v_mov_b32 v233, s93\n\t" \
  "buffer_store_dwordx2 v[232:233], v234, %[rs], %[sbc] offen sc1\n\t" \
  "s_mov_b64 exec, -1\n\t" \
  "s_mov_b32 %[tb1], s90\n\t" \
  "s_setprio 0\n\t" \
  "ds_read_b128 v[208:211], v248 offset:9216\n\t" \
  "ds_read_b128 v[212:215], v248 offset:10240\n\t" \
  "ds_read_b128 v[236:239], v248 offset:11264\n\t" \
  "ds_read_b128 v[240:243], v248 offset:13312\n\t" \
  "ds_read_b128 v[244:247], v248 offset:14336\n\t" \
  "s_waitcnt lgkmcnt(4)\n\t" \
  "ds_read_b128 v[200:203], v248 offset:6144\n\t" \
  "v_smfmac_f32_16x16x64_bf16 v[192:195], v[208:211], v[0:7], v251\n\t" \
  "ds_read_b128 v[216:219], v250 offset:32768\n\t" \
  "v_smfmac_f32_16x16x64_bf16 v[196:199], v[208:211], v[8:15], v251\n\t" \
  "s_waitcnt lgkmcnt(5)\n\t" \
  "ds_read_b128 v[220:223], v250 offset:33792\n\t" \
  "v_smfmac_f32_16x16x64_bf16 v[192:195], v[212:215], v[16:23], v251\n\t" \
  "ds_read_b128 v[224:227], v250 offset:34816\n\t" \
  "v_smfmac_f32_16x16x64_bf16 v[196:199], v[212:215], v[24:31], v251\n\t" \
  "s_waitcnt lgkmcnt(6)\n\t" \
  "ds_read_b128 v[228:231], v250 offset:35840\n\t" \
  "v_smfmac_f32_16x16x64_bf16 v[192:195], v[236:239], v[32:39], v251\n\t" \
  "ds_read_b128 v[204:207], v248 offset:0\n\t" \
  "v_smfmac_f32_16x16x64_bf16 v[196:199], v[236:239], v[40:47], v251\n\t" \
  "s_waitcnt lgkmcnt(7)\n\t" \
  "ds_read_b128 v[208:211], v248 offset:1024\n\t" \
  "v_smfmac_f32_16x16x64_bf16 v[192:195], v[240:243], v[48:55], v251\n\t" \
  "ds_read_b128 v[212:215], v248 offset:2048\n\t" \
  "v_smfmac_f32_16x16x64_bf16 v[196:199], v[240:243], v[56:63], v251\n\t" \
  "s_waitcnt lgkmcnt(7)\n\t" \
  "v_smfmac_f32_16x16x64_bf16 v[192:195], v[244:247], v[64:71], v251\n\t" \
  "ds_read_b128 v[236:239], v248 offset:3072\n\t" \
  "v_smfmac_f32_16x16x64_bf16 v[196:199], v[244:247], v[72:79], v251\n\t" \
  "s_waitcnt lgkmcnt(6)\n\t" \
  "v_smfmac_f32_16x16x64_bf16 v[192:195], v[200:203], v[216:223], v251\n\t" \
  "s_waitcnt lgkmcnt(4)\n\t" \
  "ds_read_b128 v[240:243], v248 offset:12288\n\t" \
  "v_smfmac_f32_16x16x64_bf16 v[196:199], v[200:203], v[224:231], v251\n\t" \
  "s_waitcnt lgkmcnt(4)\n\t" \
  "ds_read_b128 v[216:219], v250 offset:0\n\t" \
  "v_smfmac_f32_16x16x64_bf16 v[192:195], v[204:207], v[80:87], v251\n\t" \
  "ds_read_b128 v[220:223], v250 offset:1024\n\t" \
  "v_smfmac_f32_16x16x64_bf16 v[196:199], v[204:207], v[88:95], v251\n\t" \
  "s_waitcnt lgkmcnt(4)\n\t" \
  "ds_read_b128 v[224:227], v250 offset:2048\n\t" \
  "v_smfmac_f32_16x16x64_bf16 v[192:195], v[208:211], v[96:103], v251\n\t" \
  "ds_read_b128 v[228:231], v250 offset:3072\n\t" \
  "v_smfmac_f32_16x16x64_bf16 v[196:199], v[208:211], v[104:111], v251\n\t" \
  "ds_read_b128 v[244:247], v248 offset:4096\n\t" \
  "v_smfmac_f32_16x16x64_bf16 v[192:195], v[212:215], v[112:119], v251\n\t" \
  "ds_read_b128 v[200:203], v248 offset:5120\n\t" \
  "v_smfmac_f32_16x16x64_bf16 v[196:199], v[212:215], v[120:127], v251\n\t" \
  "s_waitcnt lgkmcnt(6)\n\t" \
  "v_smfmac_f32_16x16x64_bf16 v[192:195], v[236:239], v[128:135], v251\n\t" \
  "ds_read_b128 v[204:207], v248 offset:8192\n\t" \
  "v_smfmac_f32_16x16x64_bf16 v[196:199], v[236:239], v[136:143], v251\n\t" \
  "s_waitcnt lgkmcnt(5)\n\t" \
  "v_smfmac_f32_16x16x64_bf16 v[192:195], v[240:243], v[216:223], v251\n\t" \
  "s_waitcnt lgkmcnt(3)\n\t" \
  "v_smfmac_f32_16x16x64_bf16 v[196:199], v[240:243], v[224:231], v251\n\t" \
  "s_waitcnt lgkmcnt(2)\n\t" \
  "v_smfmac_f32_16x16x64_bf16 v[192:195], v[244:247], v[144:151], v251\n\t" \
  "v_smfmac_f32_16x16x64_bf16 v[196:199], v[244:247], v[152:159], v251\n\t" \
  "s_waitcnt lgkmcnt(1)\n\t" \
  "v_smfmac_f32_16x16x64_bf16 v[192:195], v[200:203], v[160:167], v251\n\t" \
  "v_smfmac_f32_16x16x64_bf16 v[196:199], v[200:203], v[168:175], v251\n\t" \
  "s_waitcnt lgkmcnt(0)\n\t" \
  "v_smfmac_f32_16x16x64_bf16 v[192:195], v[204:207], v[176:183], v251\n\t" \
  "v_smfmac_f32_16x16x64_bf16 v[196:199], v[204:207], v[184:191], v251\n\t" \
  "s_nop 6\n\t" \
  "v_pk_add_f32 v[192:193], v[192:193], v[194:195]\n\t" \
  "v_pk_add_f32 v[196:197], v[196:197], v[198:199]\n\t" \
  "v_cvt_pk_bf16_f32 v192, v192, v193\n\t" \
  "v_cvt_pk_bf16_f32 v196, v196, v197\n\t" \
  "ds_write2_b32 v252, v192, v196 offset1:64\n\t" \
  "s_waitcnt lgkmcnt(0)\n\t" \
  "s_barrier\n\t" \
  "buffer_store_dword v192, v235, %[rs], %[sk] offen sc1\n\t" \
  "buffer_store_dword v196, v235, %[rs], %[sk] offen offset:256 sc1\n\t" \
  "s_mov_b32 %[spins], s92\n\t" \
  "s_branch .Lend_%=\n\t" \
  ".Lslow_%=:\n\t" \
  "s_mov_b64 s[94:95], 0\n\t" \
  "v_cmp_eq_u32 vcc, %[sent], v200\n\t" \
  "s_or_b64 s[94:95], s[94:95], vcc\n\t" \
  "v_cmp_eq_u32 vcc, %[sent], v201\n\t" \
  "s_or_b64 s[94:95], s[94:95], vcc\n\t" \
  "v_cmp_eq_u32 vcc, %[sent], v202\n\t" \
  "s_or_b64 s[94:95], s[94:95], vcc\n\t" \
  "v_cmp_eq_u32 vcc, %[sent], v203\n\t" \
  "s_or_b64 s[94:95], s[94:95], vcc\n\t" \
  "v_cmp_eq_u32 vcc, %[sent], v204\n\t" \
  "s_or_b64 s[94:95], s[94:95], vcc\n\t" \
  "v_cmp_eq_u32 vcc, %[sent], v205\n\t" \
  "s_or_b64 s[94:95], s[94:95], vcc\n\t" \
  "v_cmp_eq_u32 vcc, %[sent], v206\n\t" \
  "s_or_b64 s[94:95], s[94:95], vcc\n\t" \
  "v_cmp_eq_u32 vcc, %[sent], v207\n\t" \
  "s_or_b64 s[94:95], s[94:95], vcc\n\t" \
  "s_cmp_eq_u64 s[94:95], 0\n\t" \
  "s_cbranch_scc1 .Lok_%=\n\t" \
  "v_mov_b64 v[192:193], 0\n\t" \
  "v_mov_b64 v[194:195], 0\n\t" \
  "v_mov_b64 v[196:197], 0\n\t" \
  "v_mov_b64 v[198:199], 0\n\t" \
  "s_add_u32 s92, s92, 1\n\t" \
  "s_cmp_eq_u32 s92, 1\n\t" \
  "s_cbranch_scc1 .Lpoll_%=\n\t" \
  "s_sleep 2\n\t" \
  "s_and_b32 s93, s92, 63\n\t" \
  "s_cmp_lg_u32 s93, 0\n\t" \
  "s_cbranch_scc1 .Lpoll_%=\n\t" \
  "v_mov_b32 v245, 0\n\t" \
  "buffer_load_dword v244, v245, %[rs], %[sab] offen sc1\n\t" \
  "s_waitcnt vmcnt(0)\n\t" \
  "v_readfirstlane_b32 s93, v244\n\t" \
  "s_nop 3\n\t" \
  "s_cmp_eq_u32 s93, 1\n\t" \
  "s_cbranch_scc1 .Lok_%=\n\t" \
  "s_cmp_lt_u32 s92, 0x80000\n\t" \
  "s_cbranch_scc1 .Lpoll_%=\n\t" \
  "v_mov_b32 v244, 1\n\t" \
  "buffer_store_dword v244, v245, %[rs], %[sab] offen sc1\n\t" \
  "s_branch .Lok_%=\n\t" \
  ".Lend_%=:\n\t"
#define VV_ASM \
  "s_waitcnt lgkmcnt(0)\n\t" \
  "v_mov_b64 v[128:129], 0\n\t" \
  "v_mov_b64 v[130:131], 0\n\t" \
  ".p2align 5\n\t" \
  "s_nop 0\n\t" \
  "s_nop 0\n\t" \
  "s_nop 0\n\t" \
  "s_nop 0\n\t" \
  "s_movk_i32 s92, 0x2000\n\t" \
  ".Lwt_%=:\n\t" \
  "ds_read_b64 v[132:133], v200 offset:30720\n\t" \
  "s_memrealtime s[90:91]\n\t" \
  "s_sub_u32 s92, s92, 1\n\t" \
  "s_waitcnt lgkmcnt(0)\n\t" \
  "v_readfirstlane_b32 s93, v132\n\t" \
  "v_readfirstlane_b32 %[tgt], v133\n\t" \
  "s_cbranch_scc1 .Lgo_%=\n\t" \
  "s_cmp_eq_u32 s93, %[step]\n\t" \
  "s_cbranch_scc1 .Lwx_%=\n\t" \
  "s_sleep 1\n\t" \
  "s_branch .Lwt_%=\n\t" \
  ".Lwx_%=:\n\t" \
  "s_lshl_b32 s93, s90, 2\n\t" \
  "s_sub_i32 s93, %[tgt], s93\n\t" \
  "s_cmp_le_i32 s93, 0\n\t" \
  "s_cbranch_scc1 .Lgo_%=\n\t" \
  "s_min_i32 s93, s93, 8000\n\t" \
  "s_mov_b32 s94, s93\n\t" \
  ".Lsl_%=:\n\t" \
  "s_sub_i32 s94, s94, 18\n\t" \
  "s_cmp_lt_i32 s94, 30\n\t" \
  "s_cbranch_scc1 .Lsx_%=\n\t" \
  "s_sleep 1\n\t" \
  "s_branch .Lsl_%=\n\t" \
  ".Lsx_%=:\n\t" \
  "s_sub_i32 s93, s93, 12\n\t" \
  "s_max_i32 s93, s93, 4\n\t" \
  "s_lshr_b32 s93, s93, 2\n\t" \
  "s_add_u32 s93, s93, s90\n\t" \
  "s_memrealtime s[90:91]\n\t" \
  ".Lpace_%=:\n\t" \
  "s_memrealtime s[94:95]\n\t" \
  "s_waitcnt lgkmcnt(1)\n\t" \
  "s_sub_i32 s92, s90, s93\n\t" \
  "s_cmp_ge_i32 s92, 0\n\t" \
  "s_cbranch_scc1 .Le_%=\n\t" \
  "s_memrealtime s[90:91]\n\t" \
  "s_waitcnt lgkmcnt(1)\n\t" \
  "s_sub_i32 s92, s94, s93\n\t" \
  "s_cmp_lt_i32 s92, 0\n\t" \
  "s_cbranch_scc1 .Lpace_%=\n\t" \
  "s_nop 4\n\t" \
  ".Le_%=:\n\t" \
  "s_add_u32 s92, s92, s93\n\t" \
  "s_lshl_b32 s92, s92, 2\n\t" \
  "s_sub_i32 s92, %[tgt], s92\n\t" \
  "s_max_i32 s92, s92, 0\n\t" \
  "s_bitcmp0_b32 s92, 3\n\t" \
  "s_cbranch_scc1 .Lf3_%=\n\t" \
  "s_nop 15\n\t" \
  ".Lf3_%=:\n\t" \
  "s_bitcmp0_b32 s92, 2\n\t" \
  "s_cbranch_scc1 .Lf2_%=\n\t" \
  "s_nop 10\n\t" \
  ".Lf2_%=:\n\t" \
  ".Lgo_%=:\n\t" \
  "s_mov_b32 s92, 0\n\t" \
  ".Lpoll_%=:\n\t" \
  "buffer_load_dwordx4 v[132:135], v200, %[rs], %[som] offen sc1\n\t" \
  "buffer_load_dwordx4 v[136:139], v200, %[rs], %[som2] offen sc1\n\t" \
  ".p2align 5\n\t" \
  "s_waitcnt vmcnt(1) lgkmcnt(0)\n\t" \
  "ds_write_b128 v232, v[132:135]\n\t" \
  "v_smfmac_f32_16x16x64_bf16 v[128:131], v[132:135], v[0:7], v202\n\t" \
  "v_max3_u32 v196, v132, v133, v134\n\t" \
  "s_waitcnt vmcnt(0)\n\t" \
  "ds_write_b128 v232, v[136:139] offset:8192\n\t" \
  "v_smfmac_f32_16x16x64_bf16 v[128:131], v[136:139], v[64:71], v202\n\t" \
  "v_max3_u32 v196, v196, v135, v136\n\t" \
  "v_max3_u32 v196, v196, v137, v138\n\t" \
  "v_max_u32 v196, v196, v139\n\t" \
  "v_cmp_le_u32_e64 vcc, %[sent], v196\n\t" \
  "s_cbranch_vccnz .Lslow_%=\n\t" \
  ".Lok_%=:\n\t" \
  "s_waitcnt lgkmcnt(0)\n\t" \
  "s_barrier\n\t" \
  "ds_read_b128 v[164:167], v233 offset:0\n\t" \
  "ds_read_b128 v[140:143], v234 offset:0\n\t" \
  "ds_read_b128 v[144:147], v235 offset:0\n\t" \
  "ds_read_b128 v[148:151], v236 offset:0\n\t" \
  "ds_read_b128 v[152:155], v237 offset:0\n\t" \
  "s_waitcnt lgkmcnt(4)\n\t" \
  "ds_read_b128 v[156:159], v238 offset:0\n\t" \
  "v_smfmac_f32_16x16x64_bf16 v[128:131], v[164:167], v[8:15], v202\n\t" \
  "s_waitcnt lgkmcnt(4)\n\t" \
  "ds_read_b128 v[160:163], v239 offset:0\n\t" \
  "v_smfmac_f32_16x16x64_bf16 v[128:131], v[140:143], v[16:23], v202\n\t" \
  "s_waitcnt lgkmcnt(4)\n\t" \
  "ds_read_b128 v[168:171], v233 offset:8192\n\t" \
  "v_smfmac_f32_16x16x64_bf16 v[128:131], v[144:147], v[24:31], v202\n\t" \
  "s_waitcnt lgkmcnt(4)\n\t" \
  "ds_read_b128 v[172:175], v234 offset:8192\n\t" \
  "v_smfmac_f32_16x16x64_bf16 v[128:131], v[148:151], v[32:39], v202\n\t" \
  "s_waitcnt lgkmcnt(4)\n\t" \
  "ds_read_b128 v[176:179], v235 offset:8192\n\t" \
  "v_smfmac_f32_16x16x64_bf16 v[128:131], v[152:155], v[40:47], v202\n\t" \
  "s_waitcnt lgkmcnt(4)\n\t" \
  "ds_read_b128 v[180:183], v236 offset:8192\n\t" \
  "v_smfmac_f32_16x16x64_bf16 v[128:131], v[156:159], v[48:55], v202\n\t" \
  "s_waitcnt lgkmcnt(4)\n\t" \
  "ds_read_b128 v[184:187], v237 offset:8192\n\t" \
  "v_smfmac_f32_16x16x64_bf16 v[128:131], v[160:163], v[56:63], v202\n\t" \
  "s_waitcnt lgkmcnt(4)\n\t" \
  "ds_read_b128 v[188:191], v238 offset:8192\n\t" \
  "v_smfmac_f32_16x16x64_bf16 v[128:131], v[168:171], v[72:79], v202\n\t" \
  "s_waitcnt lgkmcnt(4)\n\t" \
  "ds_read_b128 v[192:195], v239 offset:8192\n\t" \
  "v_smfmac_f32_16x16x64_bf16 v[128:131], v[172:175], v[80:87], v202\n\t" \
  "s_waitcnt lgkmcnt(4)\n\t" \
  "v_smfmac_f32_16x16x64_bf16 v[128:131], v[176:179], v[88:95], v202\n\t" \
  "s_waitcnt lgkmcnt(3)\n\t" \
  "v_smfmac_f32_16x16x64_bf16 v[128:131], v[180:183], v[96:103], v202\n\t" \
  "s_waitcnt lgkmcnt(2)\n\t" \
  "v_smfmac_f32_16x16x64_bf16 v[128:131], v[184:187], v[104:111], v202\n\t" \
  "s_waitcnt lgkmcnt(1)\n\t" \
  "v_smfmac_f32_16x16x64_bf16 v[128:131], v[188:191], v[112:119], v202\n\t" \
  "s_waitcnt lgkmcnt(0)\n\t" \
  "v_smfmac_f32_16x16x64_bf16 v[128:131], v[192:195], v[120:127], v202\n\t" \
  "s_nop 7\n\t" \
  "v_add_f32 v128, v128, v130\n\t" \
  "v_add_f32_e64 v129, v129, v131\n\t" \
  "v_cvt_pk_bf16_f32 v128, v128, v129\n\t" \
  "buffer_store_dword v128, v203, %[rs], %[sv] offen sc1\n\t" \
  "ds_write_b32 v204, v128\n\t" \
  "s_waitcnt lgkmcnt(0)\n\t" \
  "s_barrier\n\t" \
  "s_mov_b32 %[spins], s92\n\t" \
  "s_branch .Lend_%=\n\t" \
  ".Lslow_%=:\n\t" \
  "s_mov_b64 s[94:95], 0\n\t" \
  "v_cmp_eq_u32 vcc, %[sent], v132\n\t" \
  "s_or_b64 s[94:95], s[94:95], vcc\n\t" \
  "v_cmp_eq_u32 vcc, %[sent], v133\n\t" \
  "s_or_b64 s[94:95], s[94:95], vcc\n\t" \
  "v_cmp_eq_u32 vcc, %[sent], v134\n\t" \
  "s_or_b64 s[94:95], s[94:95], vcc\n\t" \
  "v_cmp_eq_u32 vcc, %[sent], v135\n\t" \
  "s_or_b64 s[94:95], s[94:95], vcc\n\t" \
  "v_cmp_eq_u32 vcc, %[sent], v136\n\t" \
  "s_or_b64 s[94:95], s[94:95], vcc\n\t" \
  "v_cmp_eq_u32 vcc, %[sent], v137\n\t" \
  "s_or_b64 s[94:95], s[94:95], vcc\n\t" \
  "v_cmp_eq_u32 vcc, %[sent], v138\n\t" \
  "s_or_b64 s[94:95], s[94:95], vcc\n\t" \
  "v_cmp_eq_u32 vcc, %[sent], v139\n\t" \
  "s_or_b64 s[94:95], s[94:95], vcc\n\t" \
  "s_cmp_eq_u64 s[94:95], 0\n\t" \
  "s_cbranch_scc1 .Lok_%=\n\t" \
  "v_mov_b64 v[128:129], 0\n\t" \
  "v_mov_b64 v[130:131], 0\n\t" \
  "s_add_u32 s92, s92, 1\n\t" \
  "s_cmp_eq_u32 s92, 1\n\t" \
  "s_cbranch_scc1 .Lpoll_%=\n\t" \
  "s_sleep 2\n\t" \
  "s_and_b32 s93, s92, 63\n\t" \
  "s_cmp_lg_u32 s93, 0\n\t" \
  "s_cbranch_scc1 .Lpoll_%=\n\t" \
  "v_mov_b32 v197, 0\n\t" \
  "buffer_load_dword v196, v197, %[rs], %[sab] offen sc1\n\t" \
  "s_waitcnt vmcnt(0)\n\t" \
  "v_readfirstlane_b32 s93, v196\n\t" \
  "s_nop 3\n\t" \
  "s_cmp_eq_u32 s93, 1\n\t" \
  "s_cbranch_scc1 .Lok_%=\n\t" \
  "s_cmp_lt_u32 s92, 0x80000\n\t" \
  "s_cbranch_scc1 .Lpoll_%=\n\t" \
  "v_mov_b32 v196, 1\n\t" \
  "buffer_store_dword v196, v197, %[rs], %[sab] offen sc1\n\t" \
  "s_branch .Lok_%=\n\t" \
  ".Lend_%=:\n\t"
#define TK_ASM \
  "buffer_load_dwordx4 v[224:227], v200, %[rs], %[sk] offen offset:0 sc1\n\t" \
  "buffer_load_dwordx4 v[228:231], v200, %[rs], %[sk] offen offset:1024 sc1\n\t" \
  "s_memrealtime s[88:89]\n\t" \
  "v_add_u32 v205, %[tl], v200\n\t" \
  "v_add_u32 v206, %[tq], v205\n\t" \
  "v_add_u32 v207, %[tv], v205\n\t" \
  "s_waitcnt lgkmcnt(0)\n\t" \
  "ds_read_b128 v[128:131], v206 offset:8192\n\t" \
  "ds_read_b128 v[144:147], v205 offset:0\n\t" \
  "ds_read_b128 v[160:163], v205 offset:4096\n\t" \
  "ds_read_b128 v[132:135], v206 offset:9216\n\t" \
  "ds_read_b128 v[148:151], v205 offset:1024\n\t" \
  "ds_read_b128 v[164:167], v205 offset:5120\n\t" \
  "ds_read_b128 v[136:139], v206 offset:10240\n\t" \
  "ds_read_b128 v[152:155], v205 offset:2048\n\t" \
  "ds_read_b128 v[168:171], v205 offset:6144\n\t" \
  "ds_read_b128 v[140:143], v206 offset:11264\n\t" \
  "ds_read_b128 v[156:159], v205 offset:3072\n\t" \
  "ds_read_b128 v[172:175], v205 offset:7168\n\t" \
  "v_add_u32 v206, %[vp], v218\n\t" \
  "s_lshl_b32 s88, s88, 2\n\t" \
  "s_add_u32 s89, s88, %[od]\n\t" \
  "s_waitcnt lgkmcnt(10)\n\t" \
  "v_mfma_f32_16x16x32_bf16 v[196:199], v[144:147], v[128:131], 0\n\t" \
  "s_waitcnt lgkmcnt(9)\n\t" \
  "v_mfma_f32_16x16x32_bf16 v[144:147], v[160:163], v[128:131], 0\n\t" \
  "s_waitcnt lgkmcnt(7)\n\t" \
  "v_mfma_f32_16x16x32_bf16 v[196:199], v[148:151], v[132:135], v[196:199]\n\t" \
  "s_waitcnt lgkmcnt(6)\n\t" \
  "v_mfma_f32_16x16x32_bf16 v[144:147], v[164:167], v[132:135], v[144:147]\n\t" \
  "s_waitcnt lgkmcnt(4)\n\t" \
  "v_mfma_f32_16x16x32_bf16 v[196:199], v[152:155], v[136:139], v[196:199]\n\t" \
  "s_waitcnt lgkmcnt(3)\n\t" \
  "v_mfma_f32_16x16x32_bf16 v[144:147], v[168:171], v[136:139], v[144:147]\n\t" \
  "s_waitcnt lgkmcnt(1)\n\t" \
  "v_mfma_f32_16x16x32_bf16 v[196:199], v[156:159], v[140:143], v[196:199]\n\t" \
  "s_waitcnt lgkmcnt(0)\n\t" \
  "v_mfma_f32_16x16x32_bf16 v[144:147], v[172:175], v[140:143], v[144:147]\n\t" \
  "ds_read_b128 v[176:179], v207 offset:16384\n\t" \
  "ds_read_b128 v[180:183], v207 offset:17408\n\t" \
  "ds_read_b128 v[184:187], v207 offset:18432\n\t" \
  "ds_read_b128 v[188:191], v207 offset:19456\n\t" \
  "ds_read2st64_b32 v[192:193], v206 offset0:0 offset1:1\n\t" \
  "ds_read2st64_b32 v[194:195], v206 offset0:2 offset1:3\n\t" \
  "v_cvt_pk_bf16_f32 v197, v196, v197\n\t" \
  "v_cvt_pk_bf16_f32 v199, v198, v199\n\t" \
  "v_cvt_pk_bf16_f32 v145, v144, v145\n\t" \
  "v_cvt_pk_bf16_f32 v147, v146, v147\n\t" \
  "v_lshlrev_b32 v196, 16, v197\n\t" \
  "v_lshlrev_b32 v198, 16, v199\n\t" \
  "v_lshlrev_b32 v144, 16, v145\n\t" \
  "v_lshlrev_b32 v146, 16, v147\n\t" \
  "v_and_b32 v197, 0xffff0000, v197\n\t" \
  "v_and_b32 v199, 0xffff0000, v199\n\t" \
  "v_and_b32 v145, 0xffff0000, v145\n\t" \
  "v_and_b32 v147, 0xffff0000, v147\n\t" \
  "v_pk_mul_f32 v[196:197], v[196:197], %[sc] op_sel_hi:[1,0]\n\t" \
  "v_pk_mul_f32 v[198:199], v[198:199], %[sc] op_sel_hi:[1,0]\n\t" \
  "v_pk_mul_f32 v[144:145], v[144:145], %[sc] op_sel_hi:[1,0]\n\t" \
  "v_pk_mul_f32 v[146:147], v[146:147], %[sc] op_sel_hi:[1,0]\n\t" \
  "v_cvt_pk_bf16_f32 v197, v196, v197\n\t" \
  "v_cvt_pk_bf16_f32 v199, v198, v199\n\t" \
  "v_cvt_pk_bf16_f32 v145, v144, v145\n\t" \
  "v_cvt_pk_bf16_f32 v147, v146, v147\n\t" \
  "v_lshlrev_b32 v196, 16, v197\n\t" \
  "v_lshlrev_b32 v198, 16, v199\n\t" \
  "v_lshlrev_b32 v144, 16, v145\n\t" \
  "v_lshlrev_b32 v146, 16, v147\n\t" \
  "v_and_b32 v197, 0xffff0000, v197\n\t" \
  "v_and_b32 v199, 0xffff0000, v199\n\t" \
  "v_and_b32 v145, 0xffff0000, v145\n\t" \
  "v_and_b32 v147, 0xffff0000, v147\n\t" \
  "v_min_f32 v196, v196, v208\n\t" \
  "v_min_f32 v197, v197, v209\n\t" \
  "v_min_f32 v198, v198, v210\n\t" \
  "v_min_f32 v199, v199, v211\n\t" \
  "v_min_f32 v144, v144, v212\n\t" \
  "v_min_f32 v145, v145, v213\n\t" \
  "v_min_f32 v146, v146, v214\n\t" \
  "v_min_f32 v147, v147, v215\n\t" \
  "v_max3_f32 v148, v196, v197, v198\n\t" \
  "v_max3_f32 v149, v199, v144, v145\n\t" \
  "v_max3_f32 v148, v148, v146, v147\n\t" \
  "v_max_f32 v150, v148, v149\n\t" \
  "v_max_f32 v151, v148, v149\n\t" \
  "s_nop 1\n\t" \
  "v_permlane32_swap_b32 v150, v151\n\t" \
  "v_max_f32 v150, v150, v151\n\t" \
  "s_mov_b32 s92, 0\n\t" \
  ".Ltake_%=:\n\t" \
  "s_waitcnt vmcnt(1) lgkmcnt(0)\n\t" \
  "ds_write_b128 v216, v[224:227]\n\t" \
  "v_max3_u32 v148, v224, v225, v226\n\t" \
  "s_waitcnt vmcnt(0)\n\t" \
  "ds_write_b128 v216, v[228:231] offset:1024\n\t" \
  "ds_read_b64_tr_b16 v[160:161], v217 offset:0\n\t" \
  "ds_read_b64_tr_b16 v[162:163], v217 offset:64\n\t" \
  "ds_read_b64_tr_b16 v[164:165], v217 offset:512\n\t" \
  "ds_read_b64_tr_b16 v[166:167], v217 offset:576\n\t" \
  "ds_read_b64_tr_b16 v[168:169], v217 offset:1024\n\t" \
  "ds_read_b64_tr_b16 v[170:171], v217 offset:1088\n\t" \
  "ds_read_b64_tr_b16 v[172:173], v217 offset:1536\n\t" \
  "ds_read_b64_tr_b16 v[174:175], v217 offset:1600\n\t" \
  "v_max3_u32 v148, v148, v227, v228\n\t" \
  "v_max3_u32 v148, v148, v229, v230\n\t" \
  "v_max_u32 v148, v148, v231\n\t" \
  "v_cmp_le_u32 vcc, %[sent], v148\n\t" \
  "s_cbranch_vccnz .Lslow_%=\n\t" \
  ".Lok_%=:\n\t" \
  "s_waitcnt lgkmcnt(6)\n\t" \
  "v_mfma_f32_16x16x32_bf16 v[152:155], v[160:163], v[128:131], 0\n\t" \
  "s_waitcnt lgkmcnt(4)\n\t" \
  "v_mfma_f32_16x16x32_bf16 v[152:155], v[164:167], v[132:135], v[152:155]\n\t" \
  "s_waitcnt lgkmcnt(2)\n\t" \
  "v_mfma_f32_16x16x32_bf16 v[152:155], v[168:171], v[136:139], v[152:155]\n\t" \
  "s_waitcnt lgkmcnt(0)\n\t" \
  "v_mfma_f32_16x16x32_bf16 v[152:155], v[172:175], v[140:143], v[152:155]\n\t" \
  "v_mov_b32 v157, 0\n\t" \
  "v_mov_b32 v158, 0\n\t" \
  "s_nop 5\n\t" \
  "v_cvt_pk_bf16_f32 v153, v152, v153\n\t" \
  "v_cvt_pk_bf16_f32 v155, v154, v155\n\t" \
  "v_lshlrev_b32 v152, 16, v153\n\t" \
  "v_lshlrev_b32 v154, 16, v155\n\t" \
  "v_and_b32 v153, 0xffff0000, v153\n\t" \
  "v_and_b32 v155, 0xffff0000, v155\n\t" \
  "v_pk_mul_f32 v[152:153], v[152:153], %[sc] op_sel_hi:[1,0]\n\t" \
  "v_pk_mul_f32 v[154:155], v[154:155], %[sc] op_sel_hi:[1,0]\n\t" \
  "v_cvt_pk_bf16_f32 v153, v152, v153\n\t" \
  "v_cvt_pk_bf16_f32 v155, v154, v155\n\t" \
  "v_lshlrev_b32 v152, 16, v153\n\t" \
  "v_lshlrev_b32 v154, 16, v155\n\t" \
  "v_and_b32 v153, 0xffff0000, v153\n\t" \
  "v_and_b32 v155, 0xffff0000, v155\n\t" \
  "v_max3_f32 v148, v152, v153, v150\n\t" \
  "v_max3_f32 v149, v148, v154, v155\n\t" \
  "v_max3_f32 v150, v148, v154, v155\n\t" \
  "v_cndmask_b32 v152, v152, v154, %[up]\n\t" \
  "v_cndmask_b32 v153, v153, v155, %[up]\n\t" \
  "v_permlane16_swap_b32 v149, v150\n\t" \
  "v_max_f32 v148, v149, v150\n\t" \
  "v_mul_f32 v148, %[negc], v148\n\t" \
  "v_fma_f32 v152, v152, %[c], v148\n\t" \
  "v_fma_f32 v153, v153, %[c], v148\n\t" \
  "v_exp_f32 v152, v152\n\t" \
  "v_fma_f32 v196, v196, %[c], v148\n\t" \
  "v_exp_f32 v153, v153\n\t" \
  "v_fma_f32 v197, v197, %[c], v148\n\t" \
  "v_exp_f32 v196, v196\n\t" \
  "v_fma_f32 v198, v198, %[c], v148\n\t" \
  "v_exp_f32 v197, v197\n\t" \
  "v_fma_f32 v199, v199, %[c], v148\n\t" \
  "v_exp_f32 v198, v198\n\t" \
  "v_fma_f32 v144, v144, %[c], v148\n\t" \
  "v_pk_add_f32 v[132:133], v[152:153], v[196:197]\n\t" \
  "v_exp_f32 v199, v199\n\t" \
  "v_fma_f32 v145, v145, %[c], v148\n\t" \
  "v_exp_f32 v144, v144\n\t" \
  "v_fma_f32 v146, v146, %[c], v148\n\t" \
  "v_pk_add_f32 v[132:133], v[132:133], v[198:199]\n\t" \
  "v_exp_f32 v145, v145\n\t" \
  "v_fma_f32 v147, v147, %[c], v148\n\t" \
  "v_exp_f32 v146, v146\n\t" \
  "v_pk_add_f32 v[132:133], v[132:133], v[144:145]\n\t" \
  "v_exp_f32 v147, v147\n\t" \
  "s_nop 0\n\t" \
  "v_pk_add_f32 v[132:133], v[132:133], v[146:147]\n\t" \
  "v_add_f32 v134, v132, v133\n\t" \
  "v_add_f32 v135, v132, v133\n\t" \
  "s_nop 1\n\t" \
  "v_permlane16_swap_b32 v134, v135\n\t" \
  "v_add_f32 v132, v134, v135\n\t" \
  "v_add_f32 v133, v134, v135\n\t" \
  "s_nop 1\n\t" \
  "v_permlane32_swap_b32 v132, v133\n\t" \
  "v_add_f32 v132, v132, v133\n\t" \
  "v_rcp_f32 v148, v132\n\t" \
  "s_nop 0\n\t" \
  "v_pk_mul_f32 v[152:153], v[152:153], v[148:149] op_sel_hi:[1,0]\n\t" \
  "v_pk_mul_f32 v[196:197], v[196:197], v[148:149] op_sel_hi:[1,0]\n\t" \
  "v_pk_mul_f32 v[198:199], v[198:199], v[148:149] op_sel_hi:[1,0]\n\t" \
  "v_pk_mul_f32 v[144:145], v[144:145], v[148:149] op_sel_hi:[1,0]\n\t" \
  "v_pk_mul_f32 v[146:147], v[146:147], v[148:149] op_sel_hi:[1,0]\n\t" \
  "v_cvt_pk_bf16_f32 v156, v152, v153\n\t" \
  "v_cvt_pk_bf16_f32 v159, v152, v153\n\t" \
  "v_cvt_pk_bf16_f32 v128, v196, v197\n\t" \
  "v_cvt_pk_bf16_f32 v129, v198, v199\n\t" \
  "v_mfma_f32_16x16x16_bf16 v[160:163], v[192:193], v[156:157], 0\n\t" \
  "v_cvt_pk_bf16_f32 v130, v144, v145\n\t" \
  "v_mfma_f32_16x16x16_bf16 v[164:167], v[192:193], v[158:159], 0\n\t" \
  "v_cvt_pk_bf16_f32 v131, v146, v147\n\t" \
  "v_mfma_f32_16x16x16_bf16 v[168:171], v[194:195], v[156:157], 0\n\t" \
  "v_mfma_f32_16x16x16_bf16 v[172:175], v[194:195], v[158:159], 0\n\t" \
  "s_nop 0\n\t" \
  "v_mfma_f32_16x16x32_bf16 v[160:163], v[176:179], v[128:131], v[160:163]\n\t" \
  "s_nop 0\n\t" \
  "v_mfma_f32_16x16x32_bf16 v[164:167], v[180:183], v[128:131], v[164:167]\n\t" \
  "s_nop 0\n\t" \
  "v_mfma_f32_16x16x32_bf16 v[168:171], v[184:187], v[128:131], v[168:171]\n\t" \
  "v_mfma_f32_16x16x32_bf16 v[172:175], v[188:191], v[128:131], v[172:175]\n\t" \
  "s_nop 2\n\t" \
  "v_cvt_pk_bf16_f32 v136, v160, v161\n\t" \
  "v_cvt_pk_bf16_f32 v137, v162, v163\n\t" \
  "v_cvt_pk_bf16_f32 v138, v164, v165\n\t" \
  "v_cvt_pk_bf16_f32 v139, v166, v167\n\t" \
  "v_cvt_pk_bf16_f32 v140, v168, v169\n\t" \
  "v_cvt_pk_bf16_f32 v141, v170, v171\n\t" \
  "v_cvt_pk_bf16_f32 v142, v172, v173\n\t" \
  "v_cvt_pk_bf16_f32 v143, v174, v175\n\t" \
  "s_memrealtime s[90:91]\n\t" \
  "s_waitcnt lgkmcnt(0)\n\t" \
  "s_lshl_b32 s93, s90, 2\n\t" \
  "s_sub_i32 s93, s89, s93\n\t" \
  "s_cmp_le_i32 s93, 0\n\t" \
  "s_cbranch_scc1 .Lgoo_%=\n\t" \
  "s_min_i32 s93, s93, 8000\n\t" \
  "s_mov_b32 s94, s93\n\t" \
  ".Lslo_%=:\n\t" \
  "s_sub_i32 s94, s94, 18\n\t" \
  "s_cmp_lt_i32 s94, 30\n\t" \
  "s_cbranch_scc1 .Lsxo_%=\n\t" \
  "s_sleep 1\n\t" \
  "s_branch .Lslo_%=\n\t" \
  ".Lsxo_%=:\n\t" \
  "s_add_u32 s93, s93, 3\n\t" \
  "s_lshr_b32 s93, s93, 2\n\t" \
  "s_add_u32 s93, s93, s90\n\t" \
  "s_memrealtime s[90:91]\n\t" \
  ".Lpaceo_%=:\n\t" \
  "s_memrealtime s[94:95]\n\t" \
  "s_waitcnt lgkmcnt(1)\n\t" \
  "s_sub_i32 s92, s90, s93\n\t" \
  "s_cmp_ge_i32 s92, 0\n\t" \
  "s_cbranch_scc1 .Leo_%=\n\t" \
  "s_memrealtime s[90:91]\n\t" \
  "s_waitcnt lgkmcnt(1)\n\t" \
  "s_sub_i32 s92, s94, s93\n\t" \
  "s_cmp_lt_i32 s92, 0\n\t" \
  "s_cbranch_scc1 .Lpaceo_%=\n\t" \
  ".Leo_%=:\n\t" \
  ".Lgoo_%=:\n\t" \
  "buffer_store_dwordx4 v[136:139], v200, %[rt], %[so] offen offset:0 sc1\n\t" \
  "buffer_store_dwordx4 v[140:143], v200, %[rt], %[so] offen offset:1024 sc1\n\t" \
  "s_waitcnt lgkmcnt(0)\n\t" \
  "s_branch .Lend_%=\n\t" \
  ".Lpoll_%=:\n\t" \
  "buffer_load_dwordx4 v[224:227], v200, %[rs], %[sk] offen offset:0 sc1\n\t" \
  "buffer_load_dwordx4 v[228:231], v200, %[rs], %[sk] offen offset:1024 sc1\n\t" \
  "s_branch .Ltake_%=\n\t" \
  "s_branch .Lend_%=\n\t" \
  ".Lslow_%=:\n\t" \
  "s_mov_b64 s[94:95], 0\n\t" \
  "v_cmp_eq_u32 vcc, %[sent], v224\n\t" \
  "s_or_b64 s[94:95], s[94:95], vcc\n\t" \
  "v_cmp_eq_u32 vcc, %[sent], v225\n\t" \
  "s_or_b64 s[94:95], s[94:95], vcc\n\t" \
  "v_cmp_eq_u32 vcc, %[sent], v226\n\t" \
  "s_or_b64 s[94:95], s[94:95], vcc\n\t" \
  "v_cmp_eq_u32 vcc, %[sent], v227\n\t" \
  "s_or_b64 s[94:95], s[94:95], vcc\n\t" \
  "v_cmp_eq_u32 vcc, %[sent], v228\n\t" \
  "s_or_b64 s[94:95], s[94:95], vcc\n\t" \
  "v_cmp_eq_u32 vcc, %[sent], v229\n\t" \
  "s_or_b64 s[94:95], s[94:95], vcc\n\t" \
  "v_cmp_eq_u32 vcc, %[sent], v230\n\t" \
  "s_or_b64 s[94:95], s[94:95], vcc\n\t" \
  "v_cmp_eq_u32 vcc, %[sent], v231\n\t" \
  "s_or_b64 s[94:95], s[94:95], vcc\n\t" \
  "s_cmp_eq_u64 s[94:95], 0\n\t" \
  "s_cbranch_scc1 .Lok_%=\n\t" \
  "s_add_u32 s92, s92, 1\n\t" \
  "s_cmp_eq_u32 s92, 1\n\t" \
  "s_cbranch_scc1 .Lpoll_%=\n\t" \
  "s_sleep 2\n\t" \
  "s_and_b32 s93, s92, 63\n\t" \
  "s_cmp_lg_u32 s93, 0\n\t" \
  "s_cbranch_scc1 .Lpoll_%=\n\t" \
  "v_mov_b32 v149, 0\n\t" \
  "buffer_load_dword v148, v149, %[rs], %[sab] offen sc1\n\t" \
  "s_waitcnt vmcnt(0)\n\t" \
  "v_readfirstlane_b32 s93, v148\n\t" \
  "s_nop 3\n\t" \
  "s_cmp_eq_u32 s93, 1\n\t" \
  "s_cbranch_scc1 .Lok_%=\n\t" \
  "s_cmp_lt_u32 s92, 0x80000\n\t" \
  "s_cbranch_scc1 .Lpoll_%=\n\t" \
  "v_mov_b32 v148, 1\n\t" \
  "buffer_store_dword v148, v149, %[rs], %[sab] offen sc1\n\t" \
  "s_branch .Lok_%=\n\t" \
  ".Lend_%=:\n\t"
constexpr int TLAG = 2, R4 = 0;
#define RED_ASM \
  "s_mov_b32 %[step], 0\n\t" \
  "s_lshl_b32 %[sh], %[step], 15\n\t" \
  "s_lshr_b32 s93, %[step], 4\n\t" \
  "s_lshr_b32 s93, %[pm], s93\n\t" \
  "s_and_b32 s93, s93, 4\n\t" \
  "s_lshl_b32 s93, s93, 11\n\t" \
  "s_lshl2_add_u32 %[so], %[sh], s93\n\t" \
  "s_sub_i32 s93, %[step], 2\n\t" \
  "s_lshl_b32 %[st], s93, 19\n\t" \
  "s_lshl_b32 s94, s93, 11\n\t" \
  "s_xor_b32 s94, s94, %[s0]\n\t" \
  "s_and_b32 s94, s94, 0x2000\n\t" \
  "s_or_b32 %[st], %[st], s94\n\t" \
  "s_cmp_lt_i32 s93, 0\n\t" \
  "s_cselect_b32 %[st], 0x70000000, %[st]\n\t" \
  "s_add_u32 %[st4], %[st], 4096\n\t" \
  "buffer_load_dwordx2 v[166:167], v173, %[rs], 0 offen sc1\n\t" \
  "v_mov_b64 v[144:145], 0\n\t" \
  "v_mov_b64 v[146:147], 0\n\t" \
  "v_mov_b64 v[148:149], 0\n\t" \
  "v_mov_b64 v[150:151], 0\n\t" \
  "v_mov_b64 v[152:153], 0\n\t" \
  "v_mov_b64 v[154:155], 0\n\t" \
  "v_mov_b64 v[156:157], 0\n\t" \
  "v_mov_b64 v[158:159], 0\n\t" \
  ".Lstep_%=:\n\t" \
  "s_setprio 3\n\t" \
  "s_mov_b32 s92, 0\n\t" \
  ".Lcal_%=:\n\t" \
  "s_waitcnt vmcnt(0)\n\t" \
  "s_memrealtime s[76:77]\n\t" \
  "v_cmp_ne_u32 vcc, %[step], v166\n\t" \
  "v_readlane_b32 s90, v167, 0\n\t" \
  "v_readlane_b32 s91, v167, 1\n\t" \
  "v_readlane_b32 s94, v167, 2\n\t" \
  "v_readlane_b32 s95, v167, 3\n\t" \
  "s_sub_i32 s90, s90, %[base]\n\t" \
  "s_sub_i32 s91, s91, %[base]\n\t" \
  "s_sub_i32 s94, s94, %[base]\n\t" \
  "s_sub_i32 s95, s95, %[base]\n\t" \
  "s_max_i32 s90, s90, s91\n\t" \
  "s_max_i32 s94, s94, s95\n\t" \
  "s_max_i32 s90, s90, s94\n\t" \
  "s_add_i32 s90, s90, %[ds]\n\t" \
  "s_waitcnt lgkmcnt(0)\n\t" \
  "s_add_u32 %[E], s90, %[base]\n\t" \
  "s_and_b32 s93, vcc_lo, 0xf\n\t" \
  "s_cbranch_scc1 .Lbl_%=\n\t" \
  ".Lgo_%=:\n\t" \
  "v_mov_b32 v166, 7\n\t" \
  "s_mov_b64 exec, 1\n\t" \
  "v_mov_b32 v164, s90\n\t" \
  "ds_max_u32 v168, v164 offset:4100\n\t" \
  "ds_add_u32 v168, v164 offset:4096\n\t" \
  "s_mov_b64 exec, -1\n\t" \
  "s_memrealtime s[90:91]\n\t" \
  ".p2align 5\n\t" \
  "s_waitcnt lgkmcnt(0)\n\t" \
  "s_lshl_b32 s93, s90, 2\n\t" \
  "s_sub_i32 s93, %[E], s93\n\t" \
  "s_cmp_le_i32 s93, 0\n\t" \
  "s_cbranch_scc1 .Lgop_%=\n\t" \
  "s_min_i32 s93, s93, 8000\n\t" \
  "s_mov_b32 s94, s93\n\t" \
  ".Lslp_%=:\n\t" \
  "s_sub_i32 s94, s94, 18\n\t" \
  "s_cmp_lt_i32 s94, 30\n\t" \
  "s_cbranch_scc1 .Lsxp_%=\n\t" \
  "s_sleep 1\n\t" \
  "s_branch .Lslp_%=\n\t" \
  ".Lsxp_%=:\n\t" \
  "s_sub_i32 s93, s93, 12\n\t" \
  "s_max_i32 s93, s93, 4\n\t" \
  "s_lshr_b32 s93, s93, 2\n\t" \
  "s_add_u32 s93, s93, s90\n\t" \
  "s_memrealtime s[90:91]\n\t" \
  ".Lpacep_%=:\n\t" \
  "s_memrealtime s[94:95]\n\t" \
  "s_waitcnt lgkmcnt(1)\n\t" \
  "s_sub_i32 s92, s90, s93\n\t" \
  "s_cmp_ge_i32 s92, 0\n\t" \
  "s_cbranch_scc1 .Lep_%=\n\t" \
  "s_memrealtime s[90:91]\n\t" \
  "s_waitcnt lgkmcnt(1)\n\t" \
  "s_sub_i32 s92, s94, s93\n\t" \
  "s_cmp_lt_i32 s92, 0\n\t" \
  "s_cbranch_scc1 .Lpacep_%=\n\t" \
  "s_nop 4\n\t" \
  ".Lep_%=:\n\t" \
  "s_add_u32 s92, s92, s93\n\t" \
  "s_lshl_b32 s92, s92, 2\n\t" \
  "s_sub_i32 s92, %[E], s92\n\t" \
  "s_max_i32 s92, s92, 0\n\t" \
  "s_bitcmp0_b32 s92, 3\n\t" \
  "s_cbranch_scc1 .Lf3p_%=\n\t" \
  "s_nop 15\n\t" \
  ".Lf3p_%=:\n\t" \
  "s_bitcmp0_b32 s92, 2\n\t" \
  "s_cbranch_scc1 .Lf2p_%=\n\t" \
  "s_nop 10\n\t" \
  ".Lf2p_%=:\n\t" \
  ".Lgop_%=:\n\t" \
  "s_mov_b32 s92, 0\n\t" \
  "buffer_load_dwordx4 v[128:131], v234, %[rs], %[so] offen sc1\n\t" \
  "s_nop 15\n\t" \
  "s_nop 1\n\t" \
  "buffer_load_dwordx4 v[132:135], v235, %[rs], %[so] offen sc1\n\t" \
  "s_nop 15\n\t" \
  "s_nop 1\n\t" \
  "buffer_load_dwordx4 v[136:139], v236, %[rs], %[so] offen sc1\n\t" \
  "s_nop 15\n\t" \
  "s_nop 1\n\t" \
  "buffer_load_dwordx4 v[140:143], v237, %[rs], %[so] offen sc1\n\t" \
  ".p2align 5\n\t" \
  "s_nop 0\n\t" \
  "s_nop 0\n\t" \
  ".Lw_%=:\n\t" \
  "s_waitcnt lgkmcnt(0)\n\t" \
  "s_waitcnt vmcnt(3)\n\t" \
  "v_smfmac_f32_16x16x64_bf16 v[144:147], v[128:131], v[0:7], v172\n\t" \
  "v_max3_u32 v164, v128, v129, 0\n\t" \
  "v_smfmac_f32_16x16x64_bf16 v[148:151], v[128:131], v[8:15], v172\n\t" \
  "v_max3_u32 v164, v130, v131, v164\n\t" \
  "v_smfmac_f32_16x16x64_bf16 v[152:155], v[128:131], v[16:23], v172\n\t" \
  "buffer_load_dwordx4 v[176:179], v238, %[rt], %[st] offen offset:0 sc1\n\t" \
  "v_smfmac_f32_16x16x64_bf16 v[156:159], v[128:131], v[24:31], v172\n\t" \
  "s_waitcnt vmcnt(3)\n\t" \
  "v_smfmac_f32_16x16x64_bf16 v[144:147], v[132:135], v[32:39], v172\n\t" \
  "v_max3_u32 v164, v132, v133, v164\n\t" \
  "v_smfmac_f32_16x16x64_bf16 v[148:151], v[132:135], v[40:47], v172\n\t" \
  "v_max3_u32 v164, v134, v135, v164\n\t" \
  "v_smfmac_f32_16x16x64_bf16 v[152:155], v[132:135], v[48:55], v172\n\t" \
  "buffer_load_dwordx4 v[180:183], v238, %[rt], %[st] offen offset:1024 sc1\n\t" \
  "v_smfmac_f32_16x16x64_bf16 v[156:159], v[132:135], v[56:63], v172\n\t" \
  "s_waitcnt vmcnt(3)\n\t" \
  "v_smfmac_f32_16x16x64_bf16 v[144:147], v[136:139], v[64:71], v172\n\t" \
  "v_max3_u32 v164, v136, v137, v164\n\t" \
  "v_smfmac_f32_16x16x64_bf16 v[148:151], v[136:139], v[72:79], v172\n\t" \
  "v_max3_u32 v164, v138, v139, v164\n\t" \
  "v_smfmac_f32_16x16x64_bf16 v[152:155], v[136:139], v[80:87], v172\n\t" \
  "buffer_load_dwordx4 v[184:187], v238, %[rt], %[st4] offen offset:0 sc1\n\t" \
  "v_smfmac_f32_16x16x64_bf16 v[156:159], v[136:139], v[88:95], v172\n\t" \
  "s_waitcnt vmcnt(3)\n\t" \
  "v_smfmac_f32_16x16x64_bf16 v[144:147], v[140:143], v[96:103], v172\n\t" \
  "v_max3_u32 v164, v140, v141, v164\n\t" \
  "v_smfmac_f32_16x16x64_bf16 v[148:151], v[140:143], v[104:111], v172\n\t" \
  "v_max3_u32 v164, v142, v143, v164\n\t" \
  "v_smfmac_f32_16x16x64_bf16 v[152:155], v[140:143], v[112:119], v172\n\t" \
  "v_cmp_le_u32 vcc, %[sent], v164\n\t" \
  "buffer_load_dwordx4 v[188:191], v238, %[rt], %[st4] offen offset:1024 sc1\n\t" \
  "v_smfmac_f32_16x16x64_bf16 v[156:159], v[140:143], v[120:127], v172\n\t" \
  "s_cbranch_vccnz .Lslow_%=\n\t" \
  ".Lok_%=:\n\t" \
  "v_add_f32_dpp v160, v144, v146 quad_perm:[0,1,2,3] row_mask:0x3 bank_mask:0xf\n\t" \
  "v_add_f32_dpp v161, v145, v147 quad_perm:[0,1,2,3] row_mask:0x3 bank_mask:0xf\n\t" \
  "v_add_f32_dpp v162, v148, v150 quad_perm:[0,1,2,3] row_mask:0x3 bank_mask:0xf\n\t" \
  "v_add_f32_dpp v163, v149, v151 quad_perm:[0,1,2,3] row_mask:0x3 bank_mask:0xf\n\t" \
  "v_add_f32_dpp v160, v152, v154 quad_perm:[0,1,2,3] row_mask:0xc bank_mask:0xf\n\t" \
  "v_add_f32_dpp v161, v153, v155 quad_perm:[0,1,2,3] row_mask:0xc bank_mask:0xf\n\t" \
  "s_nop 0\n\t" \
  "v_add_f32_dpp v162, v156, v158 quad_perm:[0,1,2,3] row_mask:0xc bank_mask:0xf\n\t" \
  "v_add_f32_dpp v163, v157, v159 quad_perm:[0,1,2,3] row_mask:0xc bank_mask:0xf\n\t" \
  "s_nop 0\n\t" \
  "v_permlane32_swap_b32 v160, v162\n\t" \
  "v_permlane32_swap_b32 v161, v163\n\t" \
  "v_add_f32 v160, v160, v162\n\t" \
  "v_add_f32_e64 v161, v161, v163\n\t" \
  "ds_write_b64 v169, v[160:161]\n\t" \
  "s_mov_b64 exec, 1\n\t" \
  "ds_inc_rtn_u32 v164, v168, v166 offset:4104\n\t" \
  "s_mov_b64 exec, -1\n\t" \
  "ds_read_b64 v[128:129], v170 offset:0\n\t" \
  "ds_read_b64 v[130:131], v170 offset:512\n\t" \
  "ds_read_b64 v[132:133], v170 offset:1024\n\t" \
  "ds_read_b64 v[134:135], v170 offset:1536\n\t" \
  "ds_read_b64 v[136:137], v170 offset:2048\n\t" \
  "ds_read_b64 v[138:139], v170 offset:2560\n\t" \
  "ds_read_b64 v[140:141], v170 offset:3072\n\t" \
  "ds_read_b64 v[142:143], v170 offset:3584\n\t" \
  "s_waitcnt lgkmcnt(8)\n\t" \
  "v_readfirstlane_b32 s93, v164\n\t" \
  "s_cmp_lg_u32 s93, 7\n\t" \
  "s_cbranch_scc1 .Lb1_%=\n\t" \
  "s_waitcnt lgkmcnt(4)\n\t" \
  "v_pk_add_f32 v[128:129], v[128:129], v[130:131]\n\t" \
  "v_pk_add_f32 v[132:133], v[132:133], v[134:135]\n\t" \
  "s_waitcnt lgkmcnt(0)\n\t" \
  "v_pk_add_f32 v[136:137], v[136:137], v[138:139]\n\t" \
  "v_pk_add_f32 v[140:141], v[140:141], v[142:143]\n\t" \
  "v_pk_add_f32 v[128:129], v[128:129], v[132:133]\n\t" \
  "v_pk_add_f32 v[136:137], v[136:137], v[140:141]\n\t" \
  "v_pk_add_f32 v[128:129], v[128:129], v[136:137]\n\t" \
  "v_cvt_pk_bf16_f32 v128, v128, v129\n\t" \
  "s_nop 1\n\t" \
  "v_mov_b32_dpp v129, v128 quad_perm:[1,0,3,2] row_mask:0xf bank_mask:0xf\n\t" \
  "v_perm_b32 v128, v129, v128, v174\n\t" \
  "buffer_store_dword v128, v171, %[rs], %[sh] offen sc1\n\t" \
  "s_memtime s[90:91]\n\t" \
  "s_barrier\n\t" \
  "s_memtime s[76:77]\n\t" \
  "s_memrealtime s[78:79]\n\t" \
  "s_nop 3\n\t" \
  "s_memrealtime s[80:81]\n\t" \
  "s_nop 3\n\t" \
  "s_memrealtime s[82:83]\n\t" \
  "s_nop 3\n\t" \
  "s_memrealtime s[84:85]\n\t" \
  "s_nop 3\n\t" \
  "s_memrealtime s[86:87]\n\t" \
  "s_nop 3\n\t" \
  "s_memrealtime s[88:89]\n\t" \
  "s_mov_b32 s93, 0\n\t" \
  "s_waitcnt lgkmcnt(0)\n\t" \
  "s_cmp_eq_u32 s80, s78\n\t" \
  "s_addc_u32 s93, s93, 0\n\t" \
  "s_cmp_eq_u32 s82, s78\n\t" \
  "s_addc_u32 s93, s93, 0\n\t" \
  "s_cmp_eq_u32 s84, s78\n\t" \
  "s_addc_u32 s93, s93, 0\n\t" \
  "s_cmp_eq_u32 s86, s78\n\t" \
  "s_addc_u32 s93, s93, 0\n\t" \
  "s_cmp_eq_u32 s88, s78\n\t" \
  "s_addc_u32 s93, s93, 0\n\t" \
  "s_mul_i32 s93, s93, 21\n\t" \
  "s_add_u32 s76, s76, 15\n\t" \
  "s_add_u32 s76, s76, s93\n\t" \
  "s_lshl2_add_u32 s78, s78, 16\n\t" \
  "s_mov_b64 exec, 1\n\t" \
  "s_sub_i32 s90, s76, s90\n\t" \
  "s_mul_i32 s90, s90, 11900\n\t" \
  "s_ashr_i32 s90, s90, 16\n\t" \
  "s_sub_u32 s90, s78, s90\n\t" \
  "s_sub_u32 s90, s90, %[base]\n\t" \
  "v_mov_b32 v164, s90\n\t" \
  "ds_write_b32 v168, v164 offset:4108\n\t" \
  "s_mov_b64 exec, -1\n\t" \
  "s_waitcnt vmcnt(1)\n\t" \
  ".Lb2_%=:\n\t" \
  "s_setprio 0\n\t" \
  "s_bitcmp0_b64 %[am], %[step]\n\t" \
  "s_cbranch_scc1 .La1_%=\n\t" \
  "buffer_store_dwordx4 v[224:227], v228, %[ra], 0 offen sc1\n\t" \
  ".La1_%=:\n\t" \
  "buffer_load_dwordx4 v[192:195], v239, %[rt], %[st] offen offset:0 sc1\n\t" \
  "buffer_load_dwordx4 v[196:199], v239, %[rt], %[st] offen offset:1024 sc1\n\t" \
  "v_mfma_f32_16x16x32_bf16 v[128:131], v[0:3], v[176:179], 0\n\t" \
  "v_mfma_f32_16x16x32_bf16 v[136:139], v[8:11], v[176:179], 0\n\t" \
  "ds_read2st64_b32 v[144:145], v231 offset0:0 offset1:8\n\t" \
  "ds_read2st64_b32 v[146:147], v231 offset0:16 offset1:24\n\t" \
  "ds_read2st64_b32 v[148:149], v231 offset0:32 offset1:40\n\t" \
  "ds_read2st64_b32 v[150:151], v231 offset0:48 offset1:56\n\t" \
  "buffer_load_dwordx4 v[200:203], v239, %[rt], %[st4] offen offset:0 sc1\n\t" \
  "buffer_load_dwordx4 v[204:207], v239, %[rt], %[st4] offen offset:1024 sc1\n\t" \
  "v_mfma_f32_16x16x32_bf16 v[132:135], v[4:7], v[180:183], 0\n\t" \
  "v_mfma_f32_16x16x32_bf16 v[140:143], v[12:15], v[180:183], 0\n\t" \
  "s_waitcnt lgkmcnt(0)\n\t" \
  "v_add_f32 v144, v144, v145\n\t" \
  "v_add_f32 v146, v146, v147\n\t" \
  "v_add_f32 v148, v148, v149\n\t" \
  "v_add_f32 v150, v150, v151\n\t" \
  "v_add_f32 v144, v144, v146\n\t" \
  "v_add_f32 v148, v148, v150\n\t" \
  "buffer_load_dwordx4 v[208:211], v240, %[rt], %[st] offen offset:0 sc1\n\t" \
  "buffer_load_dwordx4 v[212:215], v240, %[rt], %[st] offen offset:1024 sc1\n\t" \
  "v_mfma_f32_16x16x32_bf16 v[128:131], v[16:19], v[184:187], v[128:131]\n\t" \
  "v_mfma_f32_16x16x32_bf16 v[136:139], v[24:27], v[184:187], v[136:139]\n\t" \
  "v_add_f32 v144, v144, v148\n\t" \
  "v_cvt_pk_bf16_f32 v144, v144, v144\n\t" \
  "ds_write_b16 v232, v144\n\t" \
  "ds_read_b128 v[224:227], v229\n\t" \
  "v_add_u32 v228, %[ainc], v228\n\t" \
  "v_add_u32 v229, %[rinc], v229\n\t" \
  "v_add_u32 v232, 1024, v232\n\t" \
  "v_xor_b32 v231, 0x4000, v231\n\t" \
  "buffer_load_dwordx4 v[216:219], v240, %[rt], %[st4] offen offset:0 sc1\n\t" \
  "buffer_load_dwordx4 v[220:223], v240, %[rt], %[st4] offen offset:1024 sc1\n\t" \
  "v_mfma_f32_16x16x32_bf16 v[132:135], v[20:23], v[188:191], v[132:135]\n\t" \
  "v_mfma_f32_16x16x32_bf16 v[140:143], v[28:31], v[188:191], v[140:143]\n\t" \
  "buffer_load_dwordx4 v[176:179], v241, %[rt], %[st] offen offset:0 sc1\n\t" \
  "s_waitcnt vmcnt(8)\n\t" \
  "v_mfma_f32_16x16x32_bf16 v[128:131], v[32:35], v[192:195], v[128:131]\n\t" \
  "v_mfma_f32_16x16x32_bf16 v[136:139], v[40:43], v[192:195], v[136:139]\n\t" \
  "buffer_load_dwordx4 v[180:183], v241, %[rt], %[st] offen offset:1024 sc1\n\t" \
  "s_waitcnt vmcnt(8)\n\t" \
  "v_mfma_f32_16x16x32_bf16 v[132:135], v[36:39], v[196:199], v[132:135]\n\t" \
  "v_mfma_f32_16x16x32_bf16 v[140:143], v[44:47], v[196:199], v[140:143]\n\t" \
  "buffer_load_dwordx4 v[184:187], v241, %[rt], %[st4] offen offset:0 sc1\n\t" \
  "s_waitcnt vmcnt(8)\n\t" \
  "v_mfma_f32_16x16x32_bf16 v[128:131], v[48:51], v[200:203], v[128:131]\n\t" \
  "v_mfma_f32_16x16x32_bf16 v[136:139], v[56:59], v[200:203], v[136:139]\n\t" \
  "ds_read_b128 v[160:163], v168 offset:4096\n\t" \
  "buffer_load_dwordx4 v[188:191], v241, %[rt], %[st4] offen offset:1024 sc1\n\t" \
  "s_waitcnt vmcnt(8)\n\t" \
  "v_mfma_f32_16x16x32_bf16 v[132:135], v[52:55], v[204:207], v[132:135]\n\t" \
  "v_mfma_f32_16x16x32_bf16 v[140:143], v[60:63], v[204:207], v[140:143]\n\t" \
  "s_waitcnt lgkmcnt(0)\n\t" \
  "v_readfirstlane_b32 s90, v161\n\t" \
  "v_readfirstlane_b32 s91, v163\n\t" \
  "v_readfirstlane_b32 s94, v160\n\t" \
  "s_sub_u32 s93, s94, %[sm]\n\t" \
  "s_mov_b32 %[sm], s94\n\t" \
  "s_lshr_b32 s93, s93, 3\n\t" \
  "s_sub_i32 s93, s90, s93\n\t" \
  "s_mul_i32 s93, s93, %[wm]\n\t" \
  "s_ashr_i32 s93, s93, 2\n\t" \
  "s_min_u32 s93, s93, %[gm]\n\t" \
  "s_sub_i32 s90, s90, s93\n\t" \
  "s_add_u32 s93, s90, %[rl]\n\t" \
  "s_add_u32 s93, s93, %[base]\n\t" \
  "s_add_u32 %[E], s93, %[lk]\n\t" \
  "s_sub_i32 s91, s91, s90\n\t" \
  "s_sub_i32 s91, s91, %[rl]\n\t" \
  "s_min_i32 s93, s91, %[up]\n\t" \
  "s_max_i32 s93, s93, %[dn]\n\t" \
  "s_cmp_gt_i32 s91, 60\n\t" \
  "s_cselect_b32 s93, 2, s93\n\t" \
  "s_add_i32 %[rl], %[rl], s93\n\t" \
  "v_mov_b32 v160, %[rl]\n\t" \
  "buffer_store_dword v160, v175, %[rs], 0 offen sc1\n\t" \
  "s_waitcnt vmcnt(8)\n\t" \
  "v_mfma_f32_16x16x32_bf16 v[128:131], v[64:67], v[208:211], v[128:131]\n\t" \
  "v_mfma_f32_16x16x32_bf16 v[136:139], v[72:75], v[208:211], v[136:139]\n\t" \
  "s_add_u32 %[step], %[step], 1\n\t" \
  "s_lshl_b32 %[sh], %[step], 15\n\t" \
  "s_lshr_b32 s93, %[step], 4\n\t" \
  "s_lshr_b32 s93, %[pm], s93\n\t" \
  "s_and_b32 s93, s93, 4\n\t" \
  "s_lshl_b32 s93, s93, 11\n\t" \
  "s_lshl2_add_u32 %[so], %[sh], s93\n\t" \
  "s_waitcnt vmcnt(7)\n\t" \
  "v_mfma_f32_16x16x32_bf16 v[132:135], v[68:71], v[212:215], v[132:135]\n\t" \
  "v_mfma_f32_16x16x32_bf16 v[140:143], v[76:79], v[212:215], v[140:143]\n\t" \
  "s_sub_i32 s93, %[step], 2\n\t" \
  "s_lshl_b32 %[st], s93, 19\n\t" \
  "s_lshl_b32 s94, s93, 11\n\t" \
  "s_xor_b32 s94, s94, %[s0]\n\t" \
  "s_and_b32 s94, s94, 0x2000\n\t" \
  "s_or_b32 %[st], %[st], s94\n\t" \
  "s_cmp_lt_i32 s93, 0\n\t" \
  "s_cselect_b32 %[st], 0x70000000, %[st]\n\t" \
  "s_add_u32 %[st4], %[st], 4096\n\t" \
  "s_waitcnt vmcnt(6)\n\t" \
  "v_mfma_f32_16x16x32_bf16 v[128:131], v[80:83], v[216:219], v[128:131]\n\t" \
  "v_mfma_f32_16x16x32_bf16 v[136:139], v[88:91], v[216:219], v[136:139]\n\t" \
  "s_waitcnt vmcnt(5)\n\t" \
  "v_mfma_f32_16x16x32_bf16 v[132:135], v[84:87], v[220:223], v[132:135]\n\t" \
  "v_mfma_f32_16x16x32_bf16 v[140:143], v[92:95], v[220:223], v[140:143]\n\t" \
  "s_cmp_eq_u32 %[step], 63\n\t" \
  "s_cbranch_scc1 .Llast_%=\n\t" \
  "s_movk_i32 s93, 600\n\t" \
  ".Lpacel_%=:\n\t" \
  "s_memrealtime s[90:91]\n\t" \
  "s_waitcnt lgkmcnt(0)\n\t" \
  "s_lshl_b32 s90, s90, 2\n\t" \
  "s_sub_i32 s91, %[E], s90\n\t" \
  "s_cmp_le_i32 s91, 0\n\t" \
  "s_cbranch_scc1 .Lgol_%=\n\t" \
  "s_sub_u32 s93, s93, 1\n\t" \
  "s_cmp_lg_u32 s93, 0\n\t" \
  "s_cbranch_scc1 .Lpacel_%=\n\t" \
  ".Lgol_%=:\n\t" \
  "buffer_load_dwordx2 v[166:167], v173, %[rs], 0 offen sc1\n\t" \
  "s_waitcnt vmcnt(5)\n\t" \
  "v_mfma_f32_16x16x32_bf16 v[128:131], v[96:99], v[176:179], v[128:131]\n\t" \
  "v_mfma_f32_16x16x32_bf16 v[136:139], v[104:107], v[176:179], v[136:139]\n\t" \
  "s_waitcnt vmcnt(4)\n\t" \
  "v_mfma_f32_16x16x32_bf16 v[132:135], v[100:103], v[180:183], v[132:135]\n\t" \
  "v_mfma_f32_16x16x32_bf16 v[140:143], v[108:111], v[180:183], v[140:143]\n\t" \
  "s_waitcnt vmcnt(3)\n\t" \
  "v_mfma_f32_16x16x32_bf16 v[128:131], v[112:115], v[184:187], v[128:131]\n\t" \
  "v_mfma_f32_16x16x32_bf16 v[136:139], v[120:123], v[184:187], v[136:139]\n\t" \
  "s_waitcnt vmcnt(2)\n\t" \
  "v_mfma_f32_16x16x32_bf16 v[132:135], v[116:119], v[188:191], v[132:135]\n\t" \
  "v_mfma_f32_16x16x32_bf16 v[140:143], v[124:127], v[188:191], v[140:143]\n\t" \
  "v_mov_b64 v[144:145], 0\n\t" \
  "v_mov_b64 v[146:147], 0\n\t" \
  "v_mov_b64 v[148:149], 0\n\t" \
  "v_mov_b64 v[150:151], 0\n\t" \
  "v_mov_b64 v[152:153], 0\n\t" \
  "v_mov_b64 v[154:155], 0\n\t" \
  "v_mov_b64 v[156:157], 0\n\t" \
  "v_mov_b64 v[158:159], 0\n\t" \
  "v_pk_add_f32 v[128:129], v[128:129], v[132:133]\n\t" \
  "v_pk_add_f32 v[130:131], v[130:131], v[134:135]\n\t" \
  "ds_write_b128 v230, v[128:131] offset:0\n\t" \
  "v_pk_add_f32 v[136:137], v[136:137], v[140:141]\n\t" \
  "v_pk_add_f32 v[138:139], v[138:139], v[142:143]\n\t" \
  "ds_write_b128 v230, v[136:139] offset:1024\n\t" \
  "v_xor_b32 v230, 0x4000, v230\n\t" \
  "s_cmp_lg_u32 %[step], 63\n\t" \
  "s_cbranch_scc1 .Lstep_%=\n\t" \
  ".Lfc_%=:\n\t" \
  "s_waitcnt vmcnt(0)\n\t" \
  "v_cmp_eq_u32 vcc, -1, v166\n\t" \
  "s_cbranch_vccnz .Lfw_%=\n\t" \
  ".Lfo_%=:\n\t" \
  "v_add_u32 v242, 16384, v242\n\t" \
  "buffer_load_dwordx4 v[176:179], v238, %[rt], %[st] offen offset:0 sc1\n\t" \
  "buffer_load_dwordx4 v[180:183], v238, %[rt], %[st] offen offset:1024 sc1\n\t" \
  "buffer_load_dwordx4 v[184:187], v238, %[rt], %[st4] offen offset:0 sc1\n\t" \
  "buffer_load_dwordx4 v[188:191], v238, %[rt], %[st4] offen offset:1024 sc1\n\t" \
  "buffer_load_dwordx4 v[192:195], v239, %[rt], %[st] offen offset:0 sc1\n\t" \
  "buffer_load_dwordx4 v[196:199], v239, %[rt], %[st] offen offset:1024 sc1\n\t" \
  "buffer_load_dwordx4 v[200:203], v239, %[rt], %[st4] offen offset:0 sc1\n\t" \
  "buffer_load_dwordx4 v[204:207], v239, %[rt], %[st4] offen offset:1024 sc1\n\t" \
  "v_subrev_u32 v230, 98304, v230\n\t" \
  ".Lt_%=:\n\t" \
  "s_waitcnt vmcnt(7)\n\t" \
  "v_mfma_f32_16x16x32_bf16 v[128:131], v[0:3], v[176:179], 0\n\t" \
  "v_mfma_f32_16x16x32_bf16 v[136:139], v[8:11], v[176:179], 0\n\t" \
  "buffer_load_dwordx4 v[176:179], v240, %[rt], %[st] offen offset:0 sc1\n\t" \
  "s_waitcnt vmcnt(7)\n\t" \
  "v_mfma_f32_16x16x32_bf16 v[132:135], v[4:7], v[180:183], 0\n\t" \
  "v_mfma_f32_16x16x32_bf16 v[140:143], v[12:15], v[180:183], 0\n\t" \
  "buffer_load_dwordx4 v[180:183], v240, %[rt], %[st] offen offset:1024 sc1\n\t" \
  "s_waitcnt vmcnt(7)\n\t" \
  "v_mfma_f32_16x16x32_bf16 v[128:131], v[16:19], v[184:187], v[128:131]\n\t" \
  "v_mfma_f32_16x16x32_bf16 v[136:139], v[24:27], v[184:187], v[136:139]\n\t" \
  "buffer_load_dwordx4 v[184:187], v240, %[rt], %[st4] offen offset:0 sc1\n\t" \
  "s_waitcnt vmcnt(7)\n\t" \
  "v_mfma_f32_16x16x32_bf16 v[132:135], v[20:23], v[188:191], v[132:135]\n\t" \
  "v_mfma_f32_16x16x32_bf16 v[140:143], v[28:31], v[188:191], v[140:143]\n\t" \
  "buffer_load_dwordx4 v[188:191], v240, %[rt], %[st4] offen offset:1024 sc1\n\t" \
  "buffer_load_dword v166, v242, %[rs], 0 offen sc1\n\t" \
  "s_waitcnt vmcnt(8)\n\t" \
  "v_mfma_f32_16x16x32_bf16 v[128:131], v[32:35], v[192:195], v[128:131]\n\t" \
  "v_mfma_f32_16x16x32_bf16 v[136:139], v[40:43], v[192:195], v[136:139]\n\t" \
  "buffer_load_dwordx4 v[192:195], v241, %[rt], %[st] offen offset:0 sc1\n\t" \
  "s_waitcnt vmcnt(8)\n\t" \
  "v_mfma_f32_16x16x32_bf16 v[132:135], v[36:39], v[196:199], v[132:135]\n\t" \
  "v_mfma_f32_16x16x32_bf16 v[140:143], v[44:47], v[196:199], v[140:143]\n\t" \
  "buffer_load_dwordx4 v[196:199], v241, %[rt], %[st] offen offset:1024 sc1\n\t" \
  "s_waitcnt vmcnt(8)\n\t" \
  "v_mfma_f32_16x16x32_bf16 v[128:131], v[48:51], v[200:203], v[128:131]\n\t" \
  "v_mfma_f32_16x16x32_bf16 v[136:139], v[56:59], v[200:203], v[136:139]\n\t" \
  "buffer_load_dwordx4 v[200:203], v241, %[rt], %[st4] offen offset:0 sc1\n\t" \
  "s_waitcnt vmcnt(8)\n\t" \
  "v_mfma_f32_16x16x32_bf16 v[132:135], v[52:55], v[204:207], v[132:135]\n\t" \
  "v_mfma_f32_16x16x32_bf16 v[140:143], v[60:63], v[204:207], v[140:143]\n\t" \
  "buffer_load_dwordx4 v[204:207], v241, %[rt], %[st4] offen offset:1024 sc1\n\t" \
  "s_waitcnt vmcnt(8)\n\t" \
  "v_mfma_f32_16x16x32_bf16 v[128:131], v[64:67], v[176:179], v[128:131]\n\t" \
  "v_mfma_f32_16x16x32_bf16 v[136:139], v[72:75], v[176:179], v[136:139]\n\t" \
  "s_waitcnt vmcnt(4)\n\t" \
  ".Ltc_%=:\n\t" \
  "v_cmp_eq_u32 vcc, -1, v166\n\t" \
  "s_cbranch_vccnz .Ltw_%=\n\t" \
  ".Lto_%=:\n\t" \
  "s_cmp_eq_u32 %[step], 65\n\t" \
  "s_cbranch_scc1 .Ltl_%=\n\t" \
  "s_add_u32 %[step], %[step], 1\n\t" \
  "s_sub_i32 s93, %[step], 2\n\t" \
  "s_lshl_b32 %[st], s93, 19\n\t" \
  "s_lshl_b32 s94, s93, 11\n\t" \
  "s_xor_b32 s94, s94, %[s0]\n\t" \
  "s_and_b32 s94, s94, 0x2000\n\t" \
  "s_or_b32 %[st], %[st], s94\n\t" \
  "s_cmp_lt_i32 s93, 0\n\t" \
  "s_cselect_b32 %[st], 0x70000000, %[st]\n\t" \
  "s_add_u32 %[st4], %[st], 4096\n\t" \
  "v_add_u32 v242, 16384, v242\n\t" \
  "s_cmp_eq_u32 %[step], 65\n\t" \
  "s_cbranch_scc0 .Ltn_%=\n\t" \
  "v_mov_b32 v242, %[fg]\n\t" \
  ".Ltn_%=:\n\t" \
  "buffer_load_dwordx4 v[176:179], v238, %[rt], %[st] offen offset:0 sc1\n\t" \
  "s_waitcnt vmcnt(7)\n\t" \
  "v_mfma_f32_16x16x32_bf16 v[132:135], v[68:71], v[180:183], v[132:135]\n\t" \
  "v_mfma_f32_16x16x32_bf16 v[140:143], v[76:79], v[180:183], v[140:143]\n\t" \
  "buffer_load_dwordx4 v[180:183], v238, %[rt], %[st] offen offset:1024 sc1\n\t" \
  "s_waitcnt vmcnt(7)\n\t" \
  "v_mfma_f32_16x16x32_bf16 v[128:131], v[80:83], v[184:187], v[128:131]\n\t" \
  "v_mfma_f32_16x16x32_bf16 v[136:139], v[88:91], v[184:187], v[136:139]\n\t" \
  "buffer_load_dwordx4 v[184:187], v238, %[rt], %[st4] offen offset:0 sc1\n\t" \
  "s_waitcnt vmcnt(7)\n\t" \
  "v_mfma_f32_16x16x32_bf16 v[132:135], v[84:87], v[188:191], v[132:135]\n\t" \
  "v_mfma_f32_16x16x32_bf16 v[140:143], v[92:95], v[188:191], v[140:143]\n\t" \
  "buffer_load_dwordx4 v[188:191], v238, %[rt], %[st4] offen offset:1024 sc1\n\t" \
  "s_waitcnt vmcnt(7)\n\t" \
  "v_mfma_f32_16x16x32_bf16 v[128:131], v[96:99], v[192:195], v[128:131]\n\t" \
  "v_mfma_f32_16x16x32_bf16 v[136:139], v[104:107], v[192:195], v[136:139]\n\t" \
  "buffer_load_dwordx4 v[192:195], v239, %[rt], %[st] offen offset:0 sc1\n\t" \
  "s_waitcnt vmcnt(7)\n\t" \
  "v_mfma_f32_16x16x32_bf16 v[132:135], v[100:103], v[196:199], v[132:135]\n\t" \
  "v_mfma_f32_16x16x32_bf16 v[140:143], v[108:111], v[196:199], v[140:143]\n\t" \
  "buffer_load_dwordx4 v[196:199], v239, %[rt], %[st] offen offset:1024 sc1\n\t" \
  "s_waitcnt vmcnt(7)\n\t" \
  "v_mfma_f32_16x16x32_bf16 v[128:131], v[112:115], v[200:203], v[128:131]\n\t" \
  "v_mfma_f32_16x16x32_bf16 v[136:139], v[120:123], v[200:203], v[136:139]\n\t" \
  "buffer_load_dwordx4 v[200:203], v239, %[rt], %[st4] offen offset:0 sc1\n\t" \
  "s_waitcnt vmcnt(7)\n\t" \
  "v_mfma_f32_16x16x32_bf16 v[132:135], v[116:119], v[204:207], v[132:135]\n\t" \
  "v_mfma_f32_16x16x32_bf16 v[140:143], v[124:127], v[204:207], v[140:143]\n\t" \
  "buffer_load_dwordx4 v[204:207], v239, %[rt], %[st4] offen offset:1024 sc1\n\t" \
  "s_nop 5\n\t" \
  "v_pk_add_f32 v[128:129], v[128:129], v[132:133]\n\t" \
  "v_pk_add_f32 v[130:131], v[130:131], v[134:135]\n\t" \
  "ds_write_b128 v230, v[128:131] offset:0\n\t" \
  "v_pk_add_f32 v[136:137], v[136:137], v[140:141]\n\t" \
  "v_pk_add_f32 v[138:139], v[138:139], v[142:143]\n\t" \
  "ds_write_b128 v230, v[136:139] offset:1024\n\t" \
  "v_add_u32 v230, 16384, v230\n\t" \
  "s_branch .Lt_%=\n\t" \
  ".Ltl_%=:\n\t" \
  "buffer_load_dwordx4 v[224:227], v243, %[rg], 0 offen sc1\n\t" \
  "s_waitcnt vmcnt(7)\n\t" \
  "v_mfma_f32_16x16x32_bf16 v[132:135], v[68:71], v[180:183], v[132:135]\n\t" \
  "v_mfma_f32_16x16x32_bf16 v[140:143], v[76:79], v[180:183], v[140:143]\n\t" \
  "s_waitcnt vmcnt(6)\n\t" \
  "v_mfma_f32_16x16x32_bf16 v[128:131], v[80:83], v[184:187], v[128:131]\n\t" \
  "v_mfma_f32_16x16x32_bf16 v[136:139], v[88:91], v[184:187], v[136:139]\n\t" \
  "s_waitcnt vmcnt(5)\n\t" \
  "v_mfma_f32_16x16x32_bf16 v[132:135], v[84:87], v[188:191], v[132:135]\n\t" \
  "v_mfma_f32_16x16x32_bf16 v[140:143], v[92:95], v[188:191], v[140:143]\n\t" \
  "s_waitcnt vmcnt(4)\n\t" \
  "v_mfma_f32_16x16x32_bf16 v[128:131], v[96:99], v[192:195], v[128:131]\n\t" \
  "v_mfma_f32_16x16x32_bf16 v[136:139], v[104:107], v[192:195], v[136:139]\n\t" \
  "s_waitcnt vmcnt(3)\n\t" \
  "v_mfma_f32_16x16x32_bf16 v[132:135], v[100:103], v[196:199], v[132:135]\n\t" \
  "v_mfma_f32_16x16x32_bf16 v[140:143], v[108:111], v[196:199], v[140:143]\n\t" \
  "s_waitcnt vmcnt(2)\n\t" \
  "v_mfma_f32_16x16x32_bf16 v[128:131], v[112:115], v[200:203], v[128:131]\n\t" \
  "v_mfma_f32_16x16x32_bf16 v[136:139], v[120:123], v[200:203], v[136:139]\n\t" \
  "s_waitcnt vmcnt(1)\n\t" \
  "v_mfma_f32_16x16x32_bf16 v[132:135], v[116:119], v[204:207], v[132:135]\n\t" \
  "v_mfma_f32_16x16x32_bf16 v[140:143], v[124:127], v[204:207], v[140:143]\n\t" \
  "s_nop 6\n\t" \
  "v_pk_add_f32 v[128:129], v[128:129], v[132:133]\n\t" \
  "v_pk_add_f32 v[130:131], v[130:131], v[134:135]\n\t" \
  "ds_write_b128 v230, v[128:131] offset:0\n\t" \
  "v_pk_add_f32 v[136:137], v[136:137], v[140:141]\n\t" \
  "v_pk_add_f32 v[138:139], v[138:139], v[142:143]\n\t" \
  "ds_write_b128 v230, v[136:139] offset:1024\n\t" \
  "s_waitcnt vmcnt(0) lgkmcnt(0)\n\t" \
  "s_branch .Lend_%=\n\t" \
  ".Ltw_%=:\n\t" \
  "s_add_u32 s92, s92, 1\n\t" \
  "s_sleep 2\n\t" \
  "s_and_b32 s93, s92, 63\n\t" \
  "s_cbranch_scc1 .Ltr_%=\n\t" \
  "buffer_load_dword v164, off, %[rs], %[sab] sc1\n\t" \
  "s_waitcnt vmcnt(0)\n\t" \
  "v_readfirstlane_b32 s93, v164\n\t" \
  "s_nop 3\n\t" \
  "s_cmp_eq_u32 s93, 1\n\t" \
  "s_cbranch_scc1 .Lto_%=\n\t" \
  "s_cmp_lt_u32 s92, 0x80000\n\t" \
  "s_cbranch_scc1 .Ltr_%=\n\t" \
  "v_mov_b32 v164, 1\n\t" \
  "buffer_store_dword v164, off, %[rs], %[sab] sc1\n\t" \
  "s_waitcnt vmcnt(0)\n\t" \
  "s_branch .Lto_%=\n\t" \
  ".Ltr_%=:\n\t" \
  "buffer_load_dword v166, v242, %[rs], 0 offen sc1\n\t" \
  "s_waitcnt vmcnt(0)\n\t" \
  "s_branch .Ltc_%=\n\t" \
  ".Llast_%=:\n\t" \
  "v_mov_b32 v173, v242\n\t" \
  "s_branch .Lgol_%=\n\t" \
  ".Lfw_%=:\n\t" \
  "s_add_u32 s92, s92, 1\n\t" \
  "s_sleep 2\n\t" \
  "s_and_b32 s93, s92, 63\n\t" \
  "s_cbranch_scc1 .Lfr_%=\n\t" \
  "buffer_load_dword v164, off, %[rs], %[sab] sc1\n\t" \
  "s_waitcnt vmcnt(0)\n\t" \
  "v_readfirstlane_b32 s93, v164\n\t" \
  "s_nop 3\n\t" \
  "s_cmp_eq_u32 s93, 1\n\t" \
  "s_cbranch_scc1 .Lfo_%=\n\t" \
  "s_cmp_lt_u32 s92, 0x80000\n\t" \
  "s_cbranch_scc1 .Lfr_%=\n\t" \
  "v_mov_b32 v164, 1\n\t" \
  "buffer_store_dword v164, off, %[rs], %[sab] sc1\n\t" \
  "s_waitcnt vmcnt(0)\n\t" \
  "s_branch .Lfo_%=\n\t" \
  ".Lfr_%=:\n\t" \
  "buffer_load_dwordx2 v[166:167], v173, %[rs], 0 offen sc1\n\t" \
  "s_branch .Lfc_%=\n\t" \
  ".Lb1_%=:\n\t" \
  "s_waitcnt vmcnt(0) lgkmcnt(0)\n\t" \
  "s_barrier\n\t" \
  "s_branch .Lb2_%=\n\t" \
  ".Lbl_%=:\n\t" \
  "s_bitset1_b64 %[ps], %[step]\n\t" \
  "s_cmp_lt_u32 s92, 3\n\t" \
  "s_cbranch_scc1 .Lbm_%=\n\t" \
  "s_sleep 12\n\t" \
  ".Lbm_%=:\n\t" \
  "s_add_u32 s92, s92, 1\n\t" \
  "s_sleep 2\n\t" \
  "s_and_b32 s93, s92, 63\n\t" \
  "s_cbranch_scc1 .Lba_%=\n\t" \
  "buffer_load_dword v164, off, %[rs], %[sab] sc1\n\t" \
  "s_waitcnt vmcnt(0)\n\t" \
  "v_readfirstlane_b32 s93, v164\n\t" \
  "s_nop 3\n\t" \
  "s_cmp_eq_u32 s93, 1\n\t" \
  "s_cbranch_scc1 .Lgo_%=\n\t" \
  "s_cmp_lt_u32 s92, 0x80000\n\t" \
  "s_cbranch_scc1 .Lba_%=\n\t" \
  "v_mov_b32 v164, 1\n\t" \
  "buffer_store_dword v164, off, %[rs], %[sab] sc1\n\t" \
  "s_waitcnt vmcnt(0)\n\t" \
  "s_branch .Lgo_%=\n\t" \
  ".Lba_%=:\n\t" \
  "buffer_load_dwordx2 v[166:167], v173, %[rs], 0 offen sc1\n\t" \
  "s_branch .Lcal_%=\n\t" \
  ".Lslow_%=:\n\t" \
  "s_mov_b64 s[94:95], 0\n\t" \
  "v_cmp_eq_u32 vcc, %[sent], v128\n\t" \
  "s_or_b64 s[94:95], s[94:95], vcc\n\t" \
  "v_cmp_eq_u32 vcc, %[sent], v129\n\t" \
  "s_or_b64 s[94:95], s[94:95], vcc\n\t" \
  "v_cmp_eq_u32 vcc, %[sent], v130\n\t" \
  "s_or_b64 s[94:95], s[94:95], vcc\n\t" \
  "v_cmp_eq_u32 vcc, %[sent], v131\n\t" \
  "s_or_b64 s[94:95], s[94:95], vcc\n\t" \
  "v_cmp_eq_u32 vcc, %[sent], v132\n\t" \
  "s_or_b64 s[94:95], s[94:95], vcc\n\t" \
  "v_cmp_eq_u32 vcc, %[sent], v133\n\t" \
  "s_or_b64 s[94:95], s[94:95], vcc\n\t" \
  "v_cmp_eq_u32 vcc, %[sent], v134\n\t" \
  "s_or_b64 s[94:95], s[94:95], vcc\n\t" \
  "v_cmp_eq_u32 vcc, %[sent], v135\n\t" \
  "s_or_b64 s[94:95], s[94:95], vcc\n\t" \
  "v_cmp_eq_u32 vcc, %[sent], v136\n\t" \
  "s_or_b64 s[94:95], s[94:95], vcc\n\t" \
  "v_cmp_eq_u32 vcc, %[sent], v137\n\t" \
  "s_or_b64 s[94:95], s[94:95], vcc\n\t" \
  "v_cmp_eq_u32 vcc, %[sent], v138\n\t" \
  "s_or_b64 s[94:95], s[94:95], vcc\n\t" \
  "v_cmp_eq_u32 vcc, %[sent], v139\n\t" \
  "s_or_b64 s[94:95], s[94:95], vcc\n\t" \
  "v_cmp_eq_u32 vcc, %[sent], v140\n\t" \
  "s_or_b64 s[94:95], s[94:95], vcc\n\t" \
  "v_cmp_eq_u32 vcc, %[sent], v141\n\t" \
  "s_or_b64 s[94:95], s[94:95], vcc\n\t" \
  "v_cmp_eq_u32 vcc, %[sent], v142\n\t" \
  "s_or_b64 s[94:95], s[94:95], vcc\n\t" \
  "v_cmp_eq_u32 vcc, %[sent], v143\n\t" \
  "s_or_b64 s[94:95], s[94:95], vcc\n\t" \
  "s_cbranch_scc0 .Lok_%=\n\t" \
  "s_bitset1_b64 %[fm], %[step]\n\t" \
  "s_add_u32 s92, s92, 1\n\t" \
  "s_sleep 1\n\t" \
  "s_and_b32 s93, s92, 63\n\t" \
  "s_cbranch_scc1 .Lre_%=\n\t" \
  "buffer_load_dword v164, off, %[rs], %[sab] sc1\n\t" \
  "s_waitcnt vmcnt(0)\n\t" \
  "v_readfirstlane_b32 s93, v164\n\t" \
  "s_nop 3\n\t" \
  "s_cmp_eq_u32 s93, 1\n\t" \
  "s_cbranch_scc1 .Lok_%=\n\t" \
  "s_cmp_lt_u32 s92, 0x80000\n\t" \
  "s_cbranch_scc1 .Lre_%=\n\t" \
  "v_mov_b32 v164, 1\n\t" \
  "buffer_store_dword v164, off, %[rs], %[sab] sc1\n\t" \
  "s_waitcnt vmcnt(0)\n\t" \
  "s_branch .Lok_%=\n\t" \
  ".Lre_%=:\n\t" \
  "buffer_load_dwordx4 v[128:131], v234, %[rs], %[so] offen sc1\n\t" \
  "s_nop 15\n\t" \
  "s_nop 1\n\t" \
  "buffer_load_dwordx4 v[132:135], v235, %[rs], %[so] offen sc1\n\t" \
  "s_nop 15\n\t" \
  "s_nop 1\n\t" \
  "buffer_load_dwordx4 v[136:139], v236, %[rs], %[so] offen sc1\n\t" \
  "s_nop 15\n\t" \
  "s_nop 1\n\t" \
  "buffer_load_dwordx4 v[140:143], v237, %[rs], %[so] offen sc1\n\t" \
  "v_mov_b64 v[144:145], 0\n\t" \
  "v_mov_b64 v[146:147], 0\n\t" \
  "v_mov_b64 v[148:149], 0\n\t" \
  "v_mov_b64 v[150:151], 0\n\t" \
  "v_mov_b64 v[152:153], 0\n\t" \
  "v_mov_b64 v[154:155], 0\n\t" \
  "v_mov_b64 v[156:157], 0\n\t" \
  "v_mov_b64 v[158:159], 0\n\t" \
  "s_waitcnt vmcnt(0)\n\t" \
  "s_branch .Lw_%=\n\t" \
  ".Lend_%=:\n\t"


__device__ __forceinline__ int look_prog(__amdgpu_buffer_rsrc_t rs, int off) {
  asm volatile("" ::: "memory");
  const int v = (int)ld_flag(rs, off);
  return min(__builtin_amdgcn_readlane(v, 0), __builtin_amdgcn_readlane(v, 1));
}
__device__ __forceinline__ int wait_prog(__amdgpu_buffer_rsrc_t rs, int off, int need, gu32* abortw) {
  int v;
  for (unsigned spins = 0;;) {
    v = look_prog(rs, off);
    if (v >= need) break;
    __builtin_amdgcn_s_sleep(4);
    if (spin_fail(spins, abortw)) break;
  }
  asm volatile("" ::: "memory");
  return v;
}
#define CLB_S "s90", "s91", "s92", "s93", "s94", "s95", "vcc", "scc", "memory"
#define W_IN "{v[0:31]}"(W[0]), "{v[32:63]}"(W[1]), "{v[64:95]}"(W[2]), "{v[96:127]}"(W[3]), "{v[128:159]}"(W[4]), "{v[160:191]}"(W[5])

#define LEAD0 396
#define LEAD_N0 4
#define LEAD_SH 1
#define LEAD_X0 8
#define LEAD_U0 24
#define LEAD_D0 0
#define T0_BACK 1000
#define QBJ 3
constexpr int L_MEM = 0, L_KT = 16384, L_QX = 24576, L_KX = 26624, L_VX = 16384, L_MISC = 30720, L_WL = 32768;

#define QK_STEP(TXT)                                                                                                        \
  asm volatile(TXT                                                                                                          \
      : [tout] "=&s"(tout), [spins] "=&s"(spins), [tb1] "=&s"(tb1), [tgt] "=&s"(tgt), "+{v248}"(aL16), "+{v249}"(aMW), "+{v250}"(aWL),                            \
        "+{v251}"(aIDX), "+{v252}"(aQW), "+{v[232:235]}"(QF)                                                                 \
      : W_IN, [rs] "s"(rs4), [som] "s"(o_mem(pm, s) + WV * 1024), [som2] "s"(o_mem(pm, s) + WV * 1024 + 16384), [sent] "s"(DSENT), [sab] "s"(SY_ABORT), \
        [lead] "s"(lead), [step] "s"(s), [sbc] "s"(SY_BA + side(pm, 1) + h * 128), [nh] "s"(0xFF00FF00FF00FF00ull), [one] "s"(0x3F803F803F803F80ull), [up] "s"(0xFFFFFFFF00000000ull), [negc] "s"(-cexp),      \
        [c] "s"(cexp), [so] "s"(o_o(pm, s, h) + WV * 512), [sk] "s"(o_v(pm, s, h) + 2048 + (WV & 3) * 512),                    \
        [sv] "s"(o_v(pm, s, h) + WV * 512), [rt] "s"(rt4), [svt] "s"(t_off(p, T_V + kseg(s, h)) + WV * 2048)                  \
      : CLB8(192, 193, 194, 195, 196, 197, 198, 199), CLB8(200, 201, 202, 203, 204, 205, 206, 207),                          \
        CLB8(208, 209, 210, 211, 212, 213, 214, 215), CLB8(216, 217, 218, 219, 220, 221, 222, 223),                          \
        CLB8(224, 225, 226, 227, 228, 229, 230, 231), CLB8(236, 237, 238, 239, 240, 241, 242, 243), "v244", "v245", "v246", "v247", "v253", "s88", "s89", CLB_S)

template <int WV>
__device__ __forceinline__ void qk_wave(Args& p, int h, char* smem) {
  const int tid = threadIdx.x;
  unsigned pm = 0;
  int have = -1;
  u32x32 W[6];
  {
    int lane = tid & 63;
    asm volatile("" : "+v"(lane));
    const unsigned vo = (WV * 4 + (lane >> 4)) * (HD * 2) + 16 * ((lane & 15) ^ swzB((WV * 4 + (lane >> 4)) & 15));
    auto stage = [&](int j) {
#pragma unroll
      for (int mm = 0; mm < 2; ++mm) {
        const char* src = (const char*)((mm ? p.Wk : p.Wq) + (size_t)j * 128 * HD + h * DH);
        const unsigned dst = lds_off(smem) + (~j & 1) * 65536 + mm * 32768 + WV * 1024;
        glds2(src, 32 * HD * 2, vo, dst, true);
        if (TH_Q < 63) HOLD_V((h & 3) ^ 1, TH_Q);
        glds2(src + 64 * HD * 2, 32 * HD * 2, vo, dst + 16384, true);
        if (TH_Q < 63) HOLD_V((h & 3) ^ 1, TH_Q);
      }
    };
    bf16x8 keep[4][4];
    int f0 = -1;
    auto place = [&](int g) {
#pragma unroll
      for (int i = 0; i < 4; ++i)
        *reinterpret_cast<bf16x8*>(smem + (g == 0 ? 65536 : g == 1 ? 32768 : g == 2 ? 131072 : 98304) + WV * 4096 + i * 1024 + lane * 16) = keep[g][i];
    };
    stage(0); stage(1);
    Boot bt;
#pragma unroll
    for (int j = 0; j < 8; ++j) {
      if (j < 7) WAIT_V(8); else WAIT_V(0);
      if (WV == 7 && j == QBJ + 1) pm = boot_done(p.ws, bt);
      if ((WV == 4 || WV == 5) && j == 7) {
        asm volatile("" : "+v"(f0));
        have = min(__builtin_amdgcn_readlane(f0, 0), __builtin_amdgcn_readlane(f0, 1));
      }
      LBAR();
#pragma unroll
      for (int k = 0; k < 8; ++k) {
        const int P = (QK_MAP[WV] >> (4 * (2 * j + (k >> 2)))) & 15;
        const bf16x8 fr = ld_bfrag(smem + (~j & 1) * 65536 + (WV >> 2) * 32768 + (k >> 2) * 16384, k & 1, 2 * (WV & 3) + ((k >> 1) & 1), lane);
        const int f = P * 4 + (k & 3);
        if (P < 12) put4(W[f >> 3], f & 7, fr);
        else keep[P - 12][k & 3] = fr;
      }
      if (j == (WV >> 1)) place(2);
      LBAR();
      if (WV == 7 && j == QBJ) boot_ask(p.ws, bt);
      if (j + 2 < 8) stage(j + 2);
      if (j == 6) {
        if (WV == 4 || WV == 5)
          asm volatile("buffer_load_dword %0, %1, %2, 0 offen sc1" : "=&v"(f0) : "v"(fkq(h, (lane & 1) * 2)), "s"(raw_rsrc(p.ws + WS_SY, SY_TOTAL)) : "memory");
        place(0); place(3);
      }
    }
    place(1);
    if (WV == 7 && lane < 8) lds_at(smem + L_MISC)[lane] = lane == 7 ? pm : (lane == 1 ? now_q() - T0_BACK : 0);
    if (WV == 6) lds_at(smem + 28672)[lane] = ~0u;
    LBAR();
    pm = uni(lds_at(smem + L_MISC)[7]);
    p.s0 = pm >> 31;
    if (WV == 4 || WV == 5) {
      if (have < 0) have = wait_prog(mkrsrc(p.ws + WS_SY, SY_TOTAL), fkq(h, (lane & 1) * 2), 0, (gu32*)(p.ws + WS_SY + SY_ABORT));
      const char* src = p.ws + WS_T + t_off(p, kseg(0, h)) + (WV - 4) * 4096 + lane * 16;
#pragma unroll
      for (int i = 0; i < 4; ++i) glds16_sc1(src + i * 1024, smem + L_KT + (WV - 4) * 4096 + i * 1024);
    }
  }
  const __amdgpu_buffer_rsrc_t rsy = mkrsrc(p.ws + WS_SY, SY_TOTAL);
  const u32x4 rs4 = raw_rsrc(p.ws + WS_SY, SY_TOTAL);
  const u32x4 rt4 = raw_rsrc(p.ws + WS_T, T_BYTES);
  gu32* abortw = (gu32*)(p.ws + WS_SY + SY_ABORT);
  const float cexp = 0.08838834764831845f * 1.4426950408889634f;
  constexpr int m = WV >> 2, b = WV & 3;
  int lead = LEAD0;
  unsigned tb1 = 0;
  unsigned long long fm = 0, ps = 0; (void)fm; (void)ps;
  for (int s = 0; s < NSEG; ++s) {
    int lane = tid & 63;
    asm volatile("" : "+v"(lane));
    const int r = lane & 15, q = lane >> 4;
    unsigned aL16 = lane * 16, aMW = aL16 + WV * 1024, aWL = aL16 + 98304 + WV * 4096;
    unsigned aIDX = (lane & 2) ? 0xEEEE : 0x4444;
    unsigned aQW = (32 * b + r) * 16 + q * 4 + (m ? L_KX : L_QX);
    u32x4 QF;
    asm volatile("" : "={v[232:235]}"(QF));
    if (WV < 4) {
      QF[0] = L_QX + (q * 8 + (r >> 2)) * 16 + (r & 1) * 8;
      QF[1] = ((2 * (q & 1) + (q >> 1)) * 16 + r) * 4;
      QF[2] = ((r >> 2) & 1) * 65536 + ((r >> 1) & 1) * 64 + (r >> 3) * 32 + (r & 1) * 16 + (q >> 1) * 256 + (q & 1) * 8;
    } else QF[3] = r * 16 + q * 4;
    unsigned tout, spins, tgt;
    if (WV == 0) QK_STEP(QK_ASM_0); else if (WV == 1) QK_STEP(QK_ASM_1); else if (WV == 2) QK_STEP(QK_ASM_2);
    else if (WV == 3) QK_STEP(QK_ASM_3); else if (WV == 4) QK_STEP(QK_ASM_4); else if (WV == 5) QK_STEP(QK_ASM_5);
    else if (WV == 6) QK_STEP(QK_ASM_6); else QK_STEP(QK_ASM_7);
    tout = __builtin_amdgcn_readfirstlane(tout);
    tb1 = __builtin_amdgcn_readfirstlane(tb1);
    lane = tid & 63;
    asm volatile("" : "+v"(lane));
    if (WV < 4) {
      lds_at(smem + L_MISC + 8)[WV] = s + 1;
      if (WV == 3) { lds_at(smem + L_MISC)[6] = time_of(tout); lds_at(smem + 28672)[2] = s; }
    }
    if (WV == 6) {
      if (s + 1 < NSEG) watch(rsy, s, lane, now_q(), (const unsigned*)(smem + L_MISC), abortw, side(pm, 1));
    } else if (WV >= 4 && s + 1 < NSEG) {
      unsigned t;
      do {
        __builtin_amdgcn_s_sleep(1);
        t = lds_at(smem + L_MISC + 8)[lane & 3];
      } while (!__all(t == (unsigned)(s + 1)));
      if (WV <= 5) {
        if (s + 2 < NSEG) {
          if (have < s + 1) have = wait_prog(rsy, fkq(h, (lane & 1) * 2), s + 1, abortw);
          const char* src = p.ws + WS_T + t_off(p, kseg(s + 1, h)) + (WV - 4) * 4096 + lane * 16;
#pragma unroll
          for (int i = 0; i < 4; ++i) glds16_sc1(src + i * 1024, smem + L_KT + (WV - 4) * 4096 + i * 1024);
          if (have < s + 2) have = look_prog(rsy, fkq(h, (lane & 1) * 2));
        }
      } else {
        do __builtin_amdgcn_s_sleep(2); while (__builtin_amdgcn_readfirstlane(lds_at(smem + 28672)[2]) != (unsigned)s);
        const int x = (int)(__builtin_amdgcn_readfirstlane(lds_at(smem + L_MISC)[6]) - tb1);
        lead = __builtin_amdgcn_readfirstlane(track(lead, x + (s < LEAD_N0 ? LEAD_X0 : 0), s < LEAD_N0 ? LEAD_U0 : LEAD_UP, s < LEAD_N0 ? LEAD_D0 : (s < 3 * LEAD_N0 ? min(max(LEAD_DN, (lead - x) >> LEAD_SH), 16) : LEAD_DN), 60));
      }
    }
  }
}

__device__ __forceinline__ void qk_role(Args& p, int h, char* smem) {
  switch (__builtin_amdgcn_readfirstlane(threadIdx.x >> 6)) {
    case 0: qk_wave<0>(p, h, smem); break;
    case 1: qk_wave<1>(p, h, smem); break;
    case 2: qk_wave<2>(p, h, smem); break;
    case 3: qk_wave<3>(p, h, smem); break;
    case 4: qk_wave<4>(p, h, smem); break;
    case 5: qk_wave<5>(p, h, smem); break;
    case 6: qk_wave<6>(p, h, smem); break;
    default: qk_wave<7>(p, h, smem); break;
  }
}

#define K_DUE 50
#define K_DUE2 500
#define T_SL 2
#define O_DUE 400

constexpr int V_VM = 16384, V_T = 32768, V_KI = 131072;
#define TK_CALL(sg, od_) \
  asm volatile(TK_ASM \
            : \
            : "{v200}"(lane * 16), "{v[208:215]}"(bias), "{v[216:219]}"(ta), \
              [rs] "s"(rs4), [rt] "s"(raw_rsrc(p.ws + WS_T, T_BYTES)), [sent] "s"(DSENT), [sab] "s"(SY_ABORT), [sk] "s"(o_v(pm, sg, h) + 2048), \
              [tl] "s"(V_T + (sg & 3) * 24576), [vp] "s"((sg & 1) * 2048), [tq] "s"((w & 1) * 4096), [tv] "s"((w & 2) * 2048), \
              [so] "s"(t_off_o(p, T_O + (sg * 2 + (w & 1)) * 16 + (h >> 1)) + (h & 1) * 4096 + (w & 2) * 1024), \
              [up] "s"(0xFFFFFFFF00000000ull), [c] "s"(1.4426950408889634f), [negc] "s"(-1.4426950408889634f), \
              [sc] "s"((unsigned long long)__float_as_uint(0.08838834764831845f)), [od] "s"(od_) \
            : CLB32(128), CLB32(160), CLB8(192, 193, 194, 195, 196, 197, 198, 199), CLB8(224, 225, 226, 227, 228, 229, 230, 231), \
              "v205", "v206", "v207", "s88", "s89", CLB_S)
__device__ __forceinline__ void vu_role(Args& p, int h, char* smem) {
  const int tid = threadIdx.x, lane = tid & 63;
  const int w = __builtin_amdgcn_readfirstlane(tid >> 6);
  u32x32 W[4];
  {
    const unsigned vo = (tid >> 4) * (HD * 2) + 16 * ((lane & 15) ^ swzB((tid >> 4) & 15));
    auto stage = [&](int j) {
      const char* src = (const char*)(p.Wv + (size_t)j * 128 * HD + h * DH);
      const unsigned dst = lds_off(smem) + L_WL + (j & 3) * 32768 + w * 1024;
      glds2(src, 32 * HD * 2, vo, dst, true);
      glds2(src + 64 * HD * 2, 32 * HD * 2, vo, dst + 16384, true);
    };
    start_sy(p.ws, p.mem0, h, smem);
    Boot bt;
#pragma unroll
    for (int u = 0; u < 2; ++u) {
#pragma unroll
      for (int j = 0; j < 4; ++j) stage(4 * u + j);
      if (u && w == 7) boot_ask(p.ws, bt);
      WAIT_V(0);
      if (u && w == 7) {
        const unsigned m = boot_done(p.ws, bt);
        if (lane < 8) lds_at(smem + L_MISC)[lane] = lane == 7 ? m : (lane == 1 ? now_q() : 0);
      }
      LBAR();
#pragma unroll
      for (int k = 0; k < 16; ++k) put4(W[2 * u + (k >> 3)], k & 7, ld_bfrag(smem + L_WL + ((k >> 1) ^ w) * 16384, k & 1, w, lane));
      LBAR();
    }
  }
  const __amdgpu_buffer_rsrc_t rsy = mkrsrc(p.ws + WS_SY, SY_TOTAL);
  const u32x4 rs4 = raw_rsrc(p.ws + WS_SY, SY_TOTAL);
  gu32* abortw = (gu32*)(p.ws + WS_SY + SY_ABORT);
  const unsigned pm = uni(lds_at(smem + L_MISC)[7]);
  p.s0 = pm >> 31;
  unsigned long long fm = 0; (void)fm;
  int have = -1;
  auto fetch = [&](int sg) {
    if (have < sg) have = wait_prog(rsy, fkq(h, w - 4), sg, abortw);
    const char* src = p.ws + WS_T + t_off(p, (w - 4) * T_Q + kseg(sg, h)) + lane * 16;
#pragma unroll
    for (int i = 0; i < 8; ++i) glds16_sc1(src + i * 1024, smem + V_T + (sg & 3) * 24576 + (w - 4) * 8192 + i * 1024);
  };
  const int fo = SY_FO + ((w & 3) * NH + h) * 128;
  typedef __attribute__((ext_vector_type(8))) float f32x8;
  f32x8 bias;
  typedef __attribute__((ext_vector_type(8))) unsigned u32x8;
  u32x8 ca;
#pragma unroll
  for (int i = 0; i < 8; ++i) ca[i] = lane * 16 + (i ^ w) * 1024;
  u32x4 ta;
  {
    const int r = lane & 15, q = lane >> 4;
#pragma unroll
    for (int j = 0; j < 8; ++j) bias[j] = (j >> 2) * 16 + 4 * q + (j & 3) <= 16 * (w & 1) + r ? INFINITY : -INFINITY;
    ta[0] = V_KI + w * 2048 + lane * 16;
    ta[1] = V_KI + w * 2048 + (q * 8 + (r >> 2)) * 16 + (r & 1) * 8;
    ta[2] = V_VM + (w & 2) * 512 + ((2 * (q & 1) + (q >> 1)) * 16 + r) * 4;
    ta[3] = 0;
  }
#pragma unroll 1
  for (int s = 0; s < NSEG; ++s) {
    unsigned spins, tgt;
    asm volatile(VV_ASM
        : [spins] "=&s"(spins), [tgt] "=&s"(tgt)
        : W4_IN, "{v200}"(lane * 16), "{v[232:239]}"(ca), "{v202}"((lane & 2) ? 0xEEEE : 0x4444), "{v203}"(lane * 4),
          "{v204}"(tid * 4 + V_VM + (s & 1) * 2048), [rs] "s"(rs4), [som] "s"(o_mem(pm, s) + w * 1024), [som2] "s"(o_mem(pm, s) + w * 1024 + 16384), [step] "s"(s),
          [sent] "s"(DSENT), [sab] "s"(SY_ABORT), [sv] "s"(o_v(pm, s, h) + w * 256)
        : CLB32(128), CLB32(160), CLB8(192, 193, 194, 195, 196, 197, 198, 199), CLB_S);
    if (w < 4 || s == NSEG - 1) {
      const int sg = s - (w < 4);
      if (w >= 4) {
        if (lane == 0) __builtin_amdgcn_raw_buffer_store_b32(1u, rsy, fo + 4 * NH * 128, 0, 16);
        pace_until(now_q() + K_DUE);
      }
      if (sg >= 0 && sg != NSEG - 2) {
        TK_CALL(sg, s < NSEG - 1 ? O_DUE : 0);
        if (sg >= NSEG - 3) {
          static_assert(TLAG == 2, "");
          WAIT_V(0);
          if (lane == 0) __builtin_amdgcn_raw_buffer_store_b32(1u, rsy, fo + (sg - NSEG + 3) * 4 * NH * 128, 0, 16);
        }
      }
    } else if (w == 7) {
      watch(rsy, s, lane, now_q(), (const unsigned*)(smem + L_MISC), abortw, side(pm, 1));
    } else if (w >= 4) {
      __builtin_amdgcn_s_sleep(T_SL);
      const int s1 = w == 5 && s < NSEG - 3 ? s : s + 1;
#pragma unroll 1
      for (int sg = (w == 5 ? s < NSEG - 2 : s == 0) ? s : s + 1; sg <= s1; ++sg) fetch(sg);
      if (have < s1 + 1 && s1 + 1 < NSEG) have = look_prog(rsy, fkq(h, w - 4));
      if (w == 5 && s + NPRE < NSEG) {
#pragma unroll
        for (int i = 0; i < 4; ++i) st_sc1(rsy, o_v(pm, s + NPRE, h) + i * 1024 + lane * 16, (u32x4){DSENT, DSENT, DSENT, DSENT});
      }
    }
    if (s == NSEG - 2 && w >= 4) {
      pace_until(tgt + K_DUE2);
      TK_CALL(s, 0);
    }
  }
}

#define RL_UP 6
#define RL_DN 4
#define RED_MIN 40
#define BA_LOOK 360
constexpr int R_SI = 32768, R_PART = 98304;

__device__ __forceinline__ void red_role(Args& p, int j, char* smem) {
  const int tid = threadIdx.x;
  int lane = tid & 63;
  const int w = __builtin_amdgcn_readfirstlane(tid >> 6);
  const int c = (j & 2) * 8 + (j >> 2), rh = j & 1;
  const int hg = (w + (j >> 2)) & 7;
  const __amdgpu_buffer_rsrc_t rsy = mkrsrc(p.ws + WS_SY, SY_TOTAL);
  const u32x4 rs4 = raw_rsrc(p.ws + WS_SY, SY_TOTAL);
  const __amdgpu_buffer_rsrc_t rt = mkrsrc(p.ws + WS_T, T_BYTES);
  gu32* abortw = (gu32*)(p.ws + WS_SY + SY_ABORT);
  u32x32 W[4];
  u32x2 tag = {0u, 0u};
  {
    const u16* Wo = p.Wo + (size_t)(4 * hg * DH) * DM + 32 * c;
    const int ro = (lane >> 2) * 64 + (lane & 3) * 8;
    Boot bt;
    load_blocks<16>(smem + 32768 + w * 16384,
        [&](int i, char* slot) {
          if (i == 12 && w == 0) boot_ask(p.ws, bt);
          if ((i < 2 || i == 4 || i == 8) && (c & 14) != 6 && lane < 10) {
            const int x = i < 2 ? i : 1 + (i >> 2), n = min(((c & 15) - ((c & 8) >> 2)) * 10 + lane, 127);
            touch_line((const char*)p.Wo + (size_t)(w * 512 + (x >> 1) * 256 + (x & 1) * 64 + (n & 64) + n) * (DM * 2) + (c & 16) * 64 + 384, smem + w * 256);
          }
          const int x = i >> 2;
          blk_dma(Wo + (size_t)((x >> 1) * 256 + (x & 1) * 64 + ((i >> 1) & 1) * 128 + (i & 1) * 32) * DM, DM, slot, lane);
        },
        [&](int i, char* slot) {
#pragma unroll
          for (int t = 0; t < 2; ++t) {
            s16x4 lo = __builtin_amdgcn_ds_read_tr16_b64_v4i16((lds_s16x4_ptr)(slot + ro + t * 32));
            s16x4 hi = __builtin_amdgcn_ds_read_tr16_b64_v4i16((lds_s16x4_ptr)(slot + ro + t * 32 + 1024));
            put4(W[i >> 2], (i & 2) * 2 + 2 * t + (i & 1), (bf16x8){lo[0], lo[1], lo[2], lo[3], hi[0], hi[1], hi[2], hi[3]});
          }
        });
    if (w == 0) { lds_at(smem + 4128)[0] = boot_done(p.ws, bt); tag = next_tag(bt.e); }
    LBAR();
  }
  const unsigned pm = uni(lds_at(smem + 4128)[0]);
  p.s0 = pm >> 31;
  const int hs = side(pm, 1);
  {
    const u32x4 ds = {DSENT, DSENT, DSENT, DSENT};
#pragma unroll
    for (int i = 0; i < 2; ++i) {
      const int s = (w * 2 + i) * 4 + (lane >> 4);
      if (s >= 2) st_sc1(rsy, o_mem(pm, s) + (c >> 4) * 16384 + (c & 15) * 512 + rh * 128 + ((lane >> 3) & 1) * 256 + (lane & 7) * 16, ds);
    }
  }
  if (tid < 4) { reinterpret_cast<unsigned*>(smem + 4096)[tid] = tid == 3 ? now_q() - 0x100000u : 0; reinterpret_cast<unsigned*>(smem + 4112)[tid] = DSENT; }
  if (tid < 256) reinterpret_cast<int*>(smem + 5120)[tid] = ((tid >> 2) - TLAG - 6) >> 2;
  LBAR();
  const unsigned base = __builtin_amdgcn_readfirstlane(lds_at(smem + 4096)[3]);
  unsigned long long fm = 0, ps = 0;
  const unsigned aL = lane * 16;
  const int e = ((56 + w) * SEG + rh * 16 + (lane >> 2)) * DM + c * 32 + (lane & 3) * 8;
  u32x4 xv = *reinterpret_cast<const u32x4*>(p.x + e), av = {DSENT, DSENT, DSENT, DSENT};
  asm volatile("" : "+v"(xv));
  {
    const int r = lane & 15, q = lane >> 4;
    const int sm0 = o_mem(pm, 0) + (c >> 4) * 16384 + (c & 15) * 512 + rh * 128;
    u32x4 VO, VT;
    unsigned fo = SY_FO + ((rh + (lane & 4) / 2) * NH + 4 * hg + (lane & 3)) * 128;
    unsigned ao = w == 1 ? (c * S_LEN + rh * 16 - (TLAG + 3) * SEG) * 64 + lane * 16
                         : (w >= 4 ? SY_O + NPRE * 131072 + side(pm, w - 2) + (j >> 3) * 16384 + (j & 7) * 1024 + lane * 16
                                   : SY_FS + (2 * c + rh) * 128 + (lane < 8 ? lane * 16 : 0x70000000));
    unsigned ar = w == 1 ? R_SI - (TLAG + 2) * 1024 + lane * 16 : (w == 2 ? 5120 : 4112);
    const unsigned long long am = w == 1 ? ((1ull << 56) - 1) << (TLAG + 3) : (w == 2 ? 0x11111111111111ull << (TLAG + 7) : (w >= 4 ? (0xFFFFull << (16 * (w - 4))) >> NPRE : 0));
    unsigned pw = R_PART + w * 2048 + lane * 16, pr = R_PART + 16384 + tid * 4;
    unsigned sw = R_SI - (TLAG + 1) * 1024 + (lane >> 2) * 64 + w * 8 + (lane & 3) * 2, stm = 8192 + w * 512;
#pragma unroll
    for (int l = 0; l < 4; ++l) {
      const int x = l;
      VO[l] = o_o(0, 0, 4 * hg) + rh * 65536 + x * 1024 + aL;
      VT[l] = (T_O + rh * 16 + 2 * hg + (x >> 1)) * 16384 + (x & 1) * 2048 + aL;
    }
    unsigned ba = lane < 4 ? SY_BA + hs + (4 * hg + lane) * 128 : 0x7F000000u;
    unsigned sm = 0;
    int rl = RL0;
    unsigned step, E, so, st, st4, sh;
    asm volatile(RED_ASM
        : [step] "=&s"(step), [E] "=&s"(E), [so] "=&s"(so), [st] "=&s"(st), [st4] "=&s"(st4), [sh] "=&s"(sh),
          [rl] "+s"(rl), [sm] "+s"(sm), [fm] "+s"(fm), [ps] "+s"(ps),
          "+{v[224:227]}"(av), "+{v228}"(ao), "+{v229}"(ar), "+{v230}"(pw), "+{v231}"(pr), "+{v232}"(sw), "+{v233}"(stm), "+{v242}"(fo), "+{v173}"(ba)
        : W4_IN, "{v168}"(aL), "{v169}"(R4 ? (w >> 1) * 1024 + lane * 16 + (w & 1) * 8 : w * 512 + lane * 8), "{v170}"(lane * 8), "{v174}"((r & 1) ? 0x03020706u : 0x05040100u),
          "{v171}"(sm0 + 32768 + (q >> 1) * 256 + (q & 1) * 64 + ((r >> 1) & 1) * 32 + (r & 1) * 16 + (r >> 2) * 4),
          "{v172}"((lane & 2) ? 0xEEEE : 0x4444),
          "{v175}"(w == 3 && lane < 32 ? SY_BR + hs + j * 128 + lane * 4 : 0x7F000000u), "{v[234:237]}"(VO), "{v[238:241]}"(VT), "{v243}"(e * 2),
          [rg] "s"(raw_rsrc(p.ws + WS_G, S_LEN * DM * 2)), [fg] "s"(SY_FG + ((14 + (w >> 2)) * 8 + (c >> 2)) * 128),
          [rs] "s"(rs4), [ra] "s"(w == 1 ? raw_rsrc(p.ws + WS_S, S_LEN * DM * 2) : rs4), [rt] "s"(raw_rsrc(p.ws + WS_T, T_BYTES)),
          [ds] "n"(DS_RED), [wm] "n"(REF_W), [gm] "n"(REF_G), [mn] "n"(RED_MIN), [lk] "n"(DS_MEM + BA_LOOK), [up] "n"(RL_UP), [dn] "n"(-RL_DN), [sent] "s"(DSENT), [sab] "s"(SY_ABORT),
          [base] "s"(base), [pm] "s"(pm), [s0] "s"((p.s0 ^ TF_O) << 13), [am] "s"(am), [ainc] "s"(w == 1 ? SEG * 64 : (w >= 4 ? 131072 : 0)),
          [rinc] "s"(w == 1 ? 1024 : (w == 2 ? 16 : 0))
        : CLB32(128), CLB8(160, 161, 162, 163, 164, 165, 166, 167), CLB8(176, 177, 178, 179, 180, 181, 182, 183),
          CLB8(184, 185, 186, 187, 188, 189, 190, 191), CLB8(192, 193, 194, 195, 196, 197, 198, 199), CLB8(200, 201, 202, 203, 204, 205, 206, 207),
          CLB8(208, 209, 210, 211, 212, 213, 214, 215), CLB8(216, 217, 218, 219, 220, 221, 222, 223), CLB_E, "s96", CLB_S);
  }
  LBAR();
  u32x4 sv;
  if (w < 6 - TLAG) sv = *reinterpret_cast<const u32x4*>(smem + R_SI + (56 + w) * 1024 + lane * 16);
  else {
    const f32x4* part = reinterpret_cast<const f32x4*>(smem + (w == 6 - TLAG ? R_PART : (w - 6 + TLAG) * 16384)) +
                        (lane & 2) * 32 + (lane & 1) * 32 + (lane >> 2);
    f32x4 x[8][2];
#pragma unroll
    for (int ww = 0; ww < 8; ++ww) { x[ww][0] = part[ww * 128]; x[ww][1] = part[ww * 128 + 16]; }
#pragma unroll
    for (int u = 0; u < 2; ++u) {
      const f32x4 a = ((x[0][u] + x[1][u]) + (x[2][u] + x[3][u])) + ((x[4][u] + x[5][u]) + (x[6][u] + x[7][u]));
      sv[2 * u] = cvtpk(a[0], a[1]); sv[2 * u + 1] = cvtpk(a[2], a[3]);
    }
  }
  st_out(p, e, combine(sv, av, xv));
  if (j == 0 && tid == 0) st2_sc1(rsy, SY_EP, tag);
}


__global__ __launch_bounds__(512, 2) void main_kernel(Args p) {
  char* smem = g_smem;
  p.ws = ws_align(p.ws);
  const int bx = blockIdx.x & 7, bk = blockIdx.x >> 3;
  const bool isg = (bx ^ XPROBE) & 4;
  const int b = (isg ? NCHAIN : 0) + bk * 4 + (bx & 3);
  if (threadIdx.x < 64) {
    unsigned long long pc; unsigned len, keep;
    asm volatile("s_getpc_b64 %0\n.Lpc_%=:\n\ts_mov_b32 %1, .Lcode_end-.Lpc_%=" : "=s"(pc), "=s"(len));
    asm volatile("s_mov_b32 %0, m0\n\ts_mov_b32 m0, %3\n\ts_nop 0\n\tbuffer_load_dword %1, %2, 0 offen lds\n\ts_mov_b32 m0, %0"
                 : "=&s"(keep) : "v"((bk * 40 + threadIdx.x) * 128), "s"(raw_rsrc((const void*)pc, len)), "s"((b >> 6) == 1 ? 2048 : 163328) : "memory");
  }
  if ((bk >> 3) == (isg ? 3 : 1) && (threadIdx.x >> 6) == 1) {
    constexpr int NI = TLB_NI + 1, NW = (int)(WS_END / TLB_WS);
    const int n = (threadIdx.x & 63) * 8 + (bk & 7), m = n / NI, i = n % NI;
    const u16* q = m == 0 ? p.Wq : m == 1 ? p.Wk : m == 2 ? p.Wv : m == 3 ? p.Wo : m == 4 ? p.wg : m == 5 ? p.wu : m == 6 ? p.wd : m == 7 ? p.x : p.out;
    static_assert(9 * NI + NW <= 512, "");
    if (n < 9 * NI + NW)
      touch_line(m < 9 ? (const char*)q + min(i * (DM * HD * 2 / TLB_NI), (m < 4 ? HD : m < 7 ? FF : S_LEN) * DM * 2 - 128)
                       : p.ws + (size_t)(n - 9 * NI + 1) * TLB_WS - 16384, smem + 163072);
  }
  if (b >= NCHAIN) gemm_role(p, b - NCHAIN, smem);
  else if (b < 32) qk_role(p, (b & 3) * 8 + bk, smem);
  else if (b < 64) vu_role(p, (b & 3) * 8 + bk - 8, smem);
  else red_role(p, b - 64, smem);
}

extern "C" void kernel_launch(void* const* d_in, const int* in_sizes, int n_in,
                              void* d_out, int out_size, void* d_ws, size_t ws_size,
                              hipStream_t stream) {
  Args a;
  a.x = (const u16*)d_in[0]; a.mem0 = (const u16*)d_in[1];
  a.Wq = (const u16*)d_in[2]; a.Wk = (const u16*)d_in[3]; a.Wv = (const u16*)d_in[4]; a.Wo = (const u16*)d_in[5];
  a.wg = (const u16*)d_in[8]; a.wu = (const u16*)d_in[9]; a.wd = (const u16*)d_in[10];
  a.ws = (char*)d_ws; a.out = (u16*)d_out; a.s0 = 0; a.pad1 = 0;
  main_kernel<<<256, 512, 0, stream>>>(a);
}
```

```cpp
#include <hip/hip_runtime.h>
#include <hip/hip_bf16.h>
#include <stdint.h>

constexpr int S_LEN = 2048, DM = 1024, NH = 32, DH = 128, NMEM = 8, SEG = 32;
constexpr int NSEG = S_LEN / SEG;
constexpr int HD = NH * DH;
constexpr int FF = 3584;

typedef unsigned short u16;
typedef __attribute__((ext_vector_type(8))) short bf16x8;
typedef __attribute__((ext_vector_type(4))) short s16x4;
typedef __attribute__((ext_vector_type(4))) float f32x4;
typedef __attribute__((ext_vector_type(4))) unsigned int u32x4;
typedef __attribute__((ext_vector_type(2))) unsigned int u32x2;
typedef __attribute__((address_space(3))) s16x4* lds_s16x4_ptr;
typedef __attribute__((address_space(3))) void* lds_void_ptr;
typedef __attribute__((address_space(1))) unsigned gu32;
typedef __bf16 bf16x2_t __attribute__((ext_vector_type(2)));
typedef float f32x2_t __attribute__((ext_vector_type(2)));

__device__ __forceinline__ unsigned cvtpk(float lo, float hi) {
  bf16x2_t r = __builtin_convertvector((f32x2_t){lo, hi}, bf16x2_t);
  return __builtin_bit_cast(unsigned, r);
}
__device__ __forceinline__ float lo_f(unsigned v) { return __uint_as_float(v << 16); }
__device__ __forceinline__ float hi_f(unsigned v) { return __uint_as_float(v & 0xffff0000u); }
__device__ __forceinline__ float rbf(float f) { return lo_f(cvtpk(f, f)); }
__device__ __forceinline__ bf16x8 as_bf16x8(u32x4 v) { return __builtin_bit_cast(bf16x8, v); }
#define MFMA(a, b, c) __builtin_amdgcn_mfma_f32_16x16x32_bf16((a), (b), (c), 0, 0, 0)

#define KSTR 65
#define XLD 1024
constexpr int PAGE = 2 << 20, NPAGE = 14;
constexpr int T_K = 0, T_Q = 2304, T_V = 4608, T_O = 6912, T_END = T_O + 2048;
constexpr unsigned T_BYTES = T_END * 16384u;
constexpr size_t WS_SY = 0;
constexpr size_t WS_T = (size_t)NPAGE * PAGE;
constexpr size_t WS_XN = WS_T + T_BYTES;
constexpr size_t WS_HF = WS_XN + (size_t)S_LEN * XLD * 2;
constexpr size_t WS_G = WS_HF + (size_t)S_LEN * 16384;
constexpr size_t WS_S = WS_G + (size_t)S_LEN * DM * 2;
constexpr size_t WS_END = WS_S + (size_t)S_LEN * DM * 2;
constexpr unsigned SENT = 0xFFFFFFFFu;
constexpr unsigned DSENT = 0xFFBFFFBFu;
constexpr int SY_MEM = 0;
constexpr int SY_O = 2 * PAGE;
constexpr int SY_V = 6 * PAGE;
constexpr int SY_BR = PAGE, SY_BA = SY_BR + 16384;
constexpr int SY_FKQ = SY_BR + 32768;
constexpr int SY_FG = SY_FKQ + NH * 4 * 128;
constexpr int SY_FO = SY_FG + 16 * 8 * 128;
constexpr int SY_CH = SY_FO + 384 * 128;
constexpr int SY_ABORT = SY_CH + 8 * 128;
constexpr int SY_FS = SY_ABORT + 256;
constexpr int SY_FEND = SY_FS + 64 * 128;
constexpr int SY_EP = SY_FEND, SY_BT = SY_EP + 128;
constexpr int SY_TOTAL = NPAGE * PAGE;
__device__ __forceinline__ int side(unsigned pm, int page) { return ((pm >> page) & 1u) << 13; }
__device__ __forceinline__ int o_mem(unsigned pm, int s) { return SY_MEM + s * 32768 + side(pm, 0); }
__device__ __forceinline__ int o_o(unsigned pm, int s, int h) {
  return SY_O + s * 131072 + (h >> 3) * 16384 + ((h >> 1) & 3) * 2048 + (h & 1) * 128 + side(pm, 2 + (s >> 4));
}
__device__ __forceinline__ int o_v(unsigned pm, int s, int h) { return SY_V + s * 262144 + (h >> 1) * 16384 + (h & 1) * 4096 + side(pm, 6 + (s >> 3)); }
__device__ __forceinline__ char* ws_align(char* ws) { return ws + ((PAGE - (unsigned)(uintptr_t)ws) & (PAGE - 1)); }
#define NFUSE 12
constexpr int NPRE = 4;
static_assert(WS_END + PAGE <= (size_t)256 << 20 && NH * KSTR <= T_Q && NPAGE % 2 == 0, "");
constexpr int NCHAIN = 128;
__host__ __device__ constexpr int kseg(int sg, int h) { return h * KSTR + sg; }
__host__ __device__ constexpr int fkq(int h, int kind) { return SY_FKQ + (h * 4 + kind) * 128; }

#define TLB_NI 16
#define TLB_WS (2 << 20)
#define XPROBE 0
struct Args {
  const u16 *x, *mem0, *Wq, *Wk, *Wv, *Wo, *wg, *wu, *wd;
  char* ws;
  u16* out;
  int s0, pad1;
};
__device__ __forceinline__ int h_off(const Args& p, int rblk) { return rblk * PAGE + (((p.s0 ^ rblk ^ 1) & 1) << 13); }
static_assert(WS_HF % (2 * PAGE) == 0, "");
#define TF_KQV 1
#define TF_O 0
__device__ __forceinline__ int t_off(const Args& p, int t) { return t * 16384 + (((p.s0 ^ (t >> 7) ^ (TF_KQV == 2 ? t : TF_KQV)) & 1) << 13); }
__device__ __forceinline__ int t_off_o(const Args& p, int t) { return t * 16384 + (((p.s0 ^ (t >> 7) ^ (TF_O == 2 ? t : TF_O)) & 1) << 13); }

__device__ __forceinline__ float wave_sum(float v) {
#pragma unroll
  for (int i = 0; i < 4; ++i) {
    const int x = (int)__float_as_uint(v);
    v += __uint_as_float(i == 0 ? __builtin_amdgcn_update_dpp(0, x, 0x128, 0xf, 0xf, false) : i == 1 ? __builtin_amdgcn_update_dpp(0, x, 0x124, 0xf, 0xf, false)
                         : i == 2 ? __builtin_amdgcn_update_dpp(0, x, 0x122, 0xf, 0xf, false) : __builtin_amdgcn_update_dpp(0, x, 0x121, 0xf, 0xf, false));
  }
  auto r = __builtin_amdgcn_permlane16_swap(__float_as_uint(v), __float_as_uint(v), false, false);
  v = __uint_as_float(r[0]) + __uint_as_float(r[1]);
  r = __builtin_amdgcn_permlane32_swap(__float_as_uint(v), __float_as_uint(v), false, false);
  return __uint_as_float(r[0]) + __uint_as_float(r[1]);
}

__shared__ __attribute__((aligned(16))) char g_smem[163840];
__device__ __forceinline__ unsigned lds_off(const void* p) {
  return (unsigned)(uintptr_t)(lds_void_ptr)g_smem + (unsigned)((const char*)p - g_smem);
}
__device__ __forceinline__ __amdgpu_buffer_rsrc_t mkrsrc(const void* p, unsigned bytes) {
  return __builtin_amdgcn_make_buffer_rsrc((void*)p, 0, bytes, 0x00020000);
}
__device__ __forceinline__ u32x4 ld_sc1(__amdgpu_buffer_rsrc_t rs, int off) {
  return __builtin_amdgcn_raw_buffer_load_b128(rs, off, 0, 16);
}
__device__ __forceinline__ void st_sc1(__amdgpu_buffer_rsrc_t rs, int off, u32x4 v) {
  __builtin_amdgcn_raw_buffer_store_b128(v, rs, off, 0, 16);
}
__device__ __forceinline__ void swap16(unsigned& x, unsigned& y) {
  auto r = __builtin_amdgcn_permlane16_swap(x, y, false, false);
  x = r[0]; y = r[1];
}
__device__ __forceinline__ void swap32(unsigned& x, unsigned& y) {
  auto r = __builtin_amdgcn_permlane32_swap(x, y, false, false);
  x = r[0]; y = r[1];
}
__device__ __forceinline__ bool spin_fail(unsigned& spins, gu32* abortw) {
  ++spins;
  if ((spins & 63u) == 0u) {
    const __amdgpu_buffer_rsrc_t ra = mkrsrc((const void*)abortw, 4);
    unsigned z = 0;
    asm volatile("" : "+v"(z));
    if (__builtin_amdgcn_readfirstlane(__builtin_amdgcn_raw_buffer_load_b32(ra, z, 0, 16)) == 1u) return true;
    if (spins > (1u << 19)) {
      __builtin_amdgcn_raw_buffer_store_b32(z + 1u, ra, z, 0, 16);
      return true;
    }
  }
  return false;
}
__device__ __forceinline__ bf16x8 ldsf(const char* p) { return *reinterpret_cast<const bf16x8*>(p); }
#define SCHED() __builtin_amdgcn_sched_barrier(0)
#define LBAR() do { asm volatile("s_waitcnt lgkmcnt(0)" ::: "memory"); __builtin_amdgcn_s_barrier(); asm volatile("" ::: "memory"); } while (0)
#define WAIT_V(n) asm volatile("s_waitcnt vmcnt(%0)" :: "n"(n) : "memory")

#define HOLD_V(c, n) asm volatile("s_cmp_eq_u32 %0, 0\n\ts_cbranch_scc1 .Lh_%=\n\ts_waitcnt vmcnt(%1)\n.Lh_%=:" :: "s"((int)(c)), "n"(n) : "memory", "scc")
#define TH_Q 2
__device__ __forceinline__ void wait_flags(__amdgpu_buffer_rsrc_t rs, int off, gu32* abortw) {
  for (unsigned spins = 0;;) {
    asm volatile("" ::: "memory");
    const unsigned v = __builtin_amdgcn_raw_buffer_load_b32(rs, off, 0, 16);
    if (__all(v != SENT)) break;
    __builtin_amdgcn_s_sleep(4);
    if (spin_fail(spins, abortw)) break;
  }
  asm volatile("" ::: "memory");
}

__device__ __forceinline__ unsigned offA(unsigned row, unsigned ch) {
  return row * 128u + 16u * (ch ^ (row & 7u));
}
__device__ __forceinline__ unsigned swzB(unsigned row) { return ((row & 3u) << 2) | ((row >> 2) & 3u); }
__device__ __forceinline__ unsigned offB(unsigned row, unsigned ch) {
  return 256u * row + 16u * (ch ^ swzB(row));
}
__device__ __forceinline__ void glds16(const u16* src, char* dst) {
  const unsigned d = __builtin_amdgcn_readfirstlane(lds_off(dst));
  unsigned keep;
  asm volatile("s_mov_b32 %0, m0\n\ts_mov_b32 m0, %2\n\ts_nop 0\n\tglobal_load_lds_dwordx4 %1, off\n\ts_mov_b32 m0, %0"
               : "=&s"(keep) : "v"(src), "s"(d) : "memory");
}
__device__ __forceinline__ bf16x8 ld_bfrag(const char* sb, int ks, int c, int lane) {
  const unsigned g = lane >> 4, qq = (lane & 15) >> 2, pp = lane & 3;
  s16x4 lo = __builtin_amdgcn_ds_read_tr16_b64_v4i16(
      (lds_s16x4_ptr)(sb + offB(32 * ks + 8 * g + qq, 2 * c + (pp >> 1)) + 8 * (pp & 1)));
  s16x4 hi = __builtin_amdgcn_ds_read_tr16_b64_v4i16(
      (lds_s16x4_ptr)(sb + offB(32 * ks + 8 * g + 4 + qq, 2 * c + (pp >> 1)) + 8 * (pp & 1)));
  return (bf16x8){lo[0], lo[1], lo[2], lo[3], hi[0], hi[1], hi[2], hi[3]};
}

__device__ __forceinline__ void blk_dma(const u16* src, size_t ld, char* slot, int lane) {
  const u16* s = src + (size_t)(lane >> 2) * ld + (lane & 3) * 8;
  glds16(s, slot);
  glds16(s + 16 * ld, slot + 1024);
}
template <int NB, typename ISSUE, typename SINK>
__device__ __forceinline__ void load_blocks(char* slice, ISSUE&& issue, SINK&& sink) {
#pragma unroll
  for (int i = 0; i < 8; ++i) issue(i, slice + i * 2048);
#pragma unroll
  for (int g = 0; g < NB / 4; ++g) {
    if (g + 1 < NB / 4) WAIT_V(8); else WAIT_V(0);
#pragma unroll
    for (int j = 0; j < 4; ++j) sink(4 * g + j, slice + ((4 * g + j) & 7) * 2048);
    asm volatile("s_waitcnt lgkmcnt(0)" ::: "memory");
    if (g + 2 < NB / 4) {
#pragma unroll
      for (int j = 0; j < 4; ++j) issue(4 * g + 8 + j, slice + ((4 * g + j) & 7) * 2048);
    }
  }
}

__device__ __forceinline__ u32x4 widen32(unsigned a0, unsigned a1, unsigned b0, unsigned b1) {
  swap32(a0, b0); swap16(a0, b0);
  swap32(a1, b1); swap16(a1, b1);
  return (u32x4){a0, a1, b0, b1};
}

__device__ __forceinline__ void publish_flags(__amdgpu_buffer_rsrc_t rs, int off, int stride, int n) {
  WAIT_V(0);
  LBAR();
  int tid = threadIdx.x;
  asm volatile("" : "+v"(tid));
  if (tid < n) __builtin_amdgcn_raw_buffer_store_b32(1u, rs, off + tid * stride, 0, 16);
}

__device__ __forceinline__ void wait_s(const Args& p, __amdgpu_buffer_rsrc_t rsy, int rblk, int c0, int n, bool need) {
  if (need) {
    if (threadIdx.x < 64) {
      for (unsigned spins = 0;;) {
        asm volatile("" ::: "memory");
        const int f = (int)__builtin_amdgcn_raw_buffer_load_b32(rsy, SY_FS + (2 * c0 + (threadIdx.x & (2 * n - 1))) * 128, 0, 16);
        if (__all(f >= rblk)) break;
        __builtin_amdgcn_s_sleep(32);
        if (spin_fail(spins, (gu32*)(p.ws + WS_SY + SY_ABORT))) break;
      }
    }
    LBAR();
  }
}
__device__ __forceinline__ u32x4 combine(u32x4 sv, u32x4 g, u32x4 x) {
  u32x4 o;
#pragma unroll
  for (int j = 0; j < 4; ++j) {
    const unsigned t = cvtpk(lo_f(sv[j]) * lo_f(g[j]), hi_f(sv[j]) * hi_f(g[j]));
    o[j] = cvtpk(lo_f(x[j]) + lo_f(t), hi_f(x[j]) + hi_f(t));
  }
  return o;
}
#define OUT_AUX 0
__device__ __forceinline__ void st_out(const Args& p, int e, u32x4 v) {
  __builtin_amdgcn_raw_buffer_store_b128(v, mkrsrc(p.out, S_LEN * DM * 2), e * 2, 0, OUT_AUX);
}
struct SX { u32x4 s, x; };
__device__ __forceinline__ SX get_sx(const Args& p, int rblk, int e) {
  SX r; r.s = (u32x4){0u, 0u, 0u, 0u}; r.x = r.s;
  if (rblk < NFUSE) {
    r.s = ld_sc1(mkrsrc(p.ws + WS_S, S_LEN * DM * 2), (((e & 1023) >> 5) * S_LEN + (e >> 10)) * 64 + (e & 31) * 2);
    r.x = *reinterpret_cast<const u32x4*>(p.x + e);
  }
  return r;
}
__device__ __forceinline__ void put_g(const Args& p, __amdgpu_buffer_rsrc_t rg, int rblk, int e, u32x4 g, const SX& v) {
  if (rblk < NFUSE) {
    st_out(p, e, combine(v.s, g, v.x));
  } else st_sc1(rg, e * 2, g);
}

__device__ __forceinline__ void job_comb(const Args& p, int rblk, int ct, __amdgpu_buffer_rsrc_t rsy) {
  int tid = threadIdx.x;
  asm volatile("" : "+v"(tid));
  u32x4 xv[8];
#pragma unroll
  for (int i = 0; i < 8; ++i) xv[i] = *reinterpret_cast<const u32x4*>(p.x + (rblk * 128 + i * 16 + (tid >> 5)) * DM + ct * 256 + (tid & 31) * 8);
  if (tid < 64) wait_flags(rsy, SY_FG + (rblk * 8 + 2 * ct + (tid & 1)) * 128, (gu32*)(p.ws + WS_SY + SY_ABORT));
  wait_s(p, rsy, rblk, ct * 8, 8, true);
  u32x4 sv[8], gv[8];
#pragma unroll
  for (int i = 0; i < 8; ++i) {
    const int e = (rblk * 128 + i * 16 + (tid >> 5)) * DM + ct * 256 + (tid & 31) * 8;
    sv[i] = ld_sc1(mkrsrc(p.ws + WS_S, S_LEN * DM * 2), (((e & 1023) >> 5) * S_LEN + (e >> 10)) * 64 + (e & 31) * 2);
    gv[i] = ld_sc1(mkrsrc(p.ws + WS_G, S_LEN * DM * 2), e * 2);
  }
#pragma unroll
  for (int i = 0; i < 8; ++i)
    st_out(p, (rblk * 128 + i * 16 + (tid >> 5)) * DM + ct * 256 + (tid & 31) * 8, combine(sv[i], gv[i], xv[i]));
}

__device__ __forceinline__ u32x4 raw_rsrc(const void* p, unsigned bytes) {
  const unsigned long long a = (unsigned long long)p;
  return (u32x4){(unsigned)a, (unsigned)(a >> 32) & 0xffffu, bytes, 0x00020000u};
}
__device__ __forceinline__ u32x2 next_tag(u32x2 e) { return (u32x2){e[0] + 1u, e[1] + (e[0] == 0xFFFFFFFFu)}; }
struct Boot { u32x2 e; u32x4 c; };
__device__ __forceinline__ void boot_ask(const char* ws, Boot& r) {
  const unsigned o = (threadIdx.x & 63) * 128;
  asm volatile("buffer_load_dwordx2 %0, off, %3, %4 sc1\n\tbuffer_load_dwordx4 %1, %2, %3, %5 offen sc1"
               : "=&v"(r.e), "=&v"(r.c) : "v"(o), "s"(raw_rsrc(ws, SY_TOTAL)), "s"(SY_EP), "s"(SY_BT) : "memory");
}
__device__ __forceinline__ void wait_rows(const char* ws, u32x2 tag) {
  const __amdgpu_buffer_rsrc_t rs = mkrsrc(ws, SY_TOTAL);
  int o = (threadIdx.x & 63) * 128;
  asm volatile("" : "+v"(o));
#pragma unroll 1
  for (int i = 0; i < (1 << 17); ++i) {
    asm volatile("" ::: "memory");
    const u32x2 a = __builtin_amdgcn_raw_buffer_load_b64(rs, o, SY_BT + 32 * 128, 16);
    if (__all(a[0] == tag[0] && a[1] == tag[1])) break;
    __builtin_amdgcn_s_sleep(8);
  }
  asm volatile("" ::: "memory");
}
__device__ __forceinline__ unsigned boot_done(const char* ws, Boot& r, bool own = false, u32x2 tag = (u32x2){0u, 0u}) {
#pragma unroll 1
  for (int i = 0; i < (1 << 17); ++i) {
    asm volatile("" : "+v"(r.e), "+v"(r.c));
    if (!own) tag = next_tag(r.e);
    if (__all(r.c[0] == tag[0] && r.c[1] == tag[1] && (r.c[2] ^ r.c[3]) == tag[0])) break;
    __builtin_amdgcn_s_sleep(8);
    boot_ask(ws, r);
    WAIT_V(0);
  }
  unsigned q0[NPAGE], q1[NPAGE], ga = 0xFFFFu, gb = 0xFFFFu;
#pragma unroll
  for (int i = 0; i < NPAGE; ++i) {
    const unsigned x = __builtin_amdgcn_readlane(r.c[2], i), y = __builtin_amdgcn_readlane(r.c[2], i + NPAGE);
    q0[i] = min(x >> 16, y >> 16); q1[i] = min(x & 0xFFFFu, y & 0xFFFFu);
    ga = min(ga, (i & 1) ? q1[i] : q0[i]); gb = min(gb, (i & 1) ? q0[i] : q1[i]);
  }
  unsigned m = gb < ga ? 0x80001555u : 0x2AAAu;
  const unsigned thr = min(ga, gb) + 110;
#pragma unroll
  for (int i = 0; i < NPAGE; ++i) {
    const unsigned lo = min(q0[i], q1[i]), hi = max(q0[i], q1[i]);
    const unsigned keep = (lo < thr ? hi : 0u) > lo + 100 ? ~(1u << i) : ~0u;
    m = (m & keep) | ((q1[i] < q0[i] ? ~0u : 0u) & ~keep);
  }
  return m;
}
typedef __attribute__((address_space(3))) volatile unsigned* lds_vu_ptr;
__device__ __forceinline__ lds_vu_ptr lds_at(const void* p) {
  unsigned a = lds_off(p);
  asm volatile("" : "+v"(a));
  return (lds_vu_ptr)(uintptr_t)a;
}
__device__ __forceinline__ unsigned uni(unsigned v) {
  unsigned r;
  asm volatile("s_nop 1\n\tv_readfirstlane_b32 %0, %1\n\ts_nop 3" : "=s"(r) : "v"(v));
  return r;
}
__device__ __forceinline__ void start_sy(char* ws, const u16* mem0, int i, char* smem) {
  const int tid = threadIdx.x, lane = tid & 63, w = __builtin_amdgcn_readfirstlane(tid >> 6);
  const __amdgpu_buffer_rsrc_t rsy = mkrsrc(ws, SY_TOTAL);
  const __amdgpu_buffer_rsrc_t rs = mkrsrc(ws + (size_t)(i % NPAGE) * PAGE + (32 + (i * 5 + w * 11) % 95) * 16384 + ((i * 3 + w * 5) & 31) * 256, 16384);
  const u32x2 e = __builtin_amdgcn_raw_buffer_load_b64(rsy, 0, SY_EP, 16);
  const u32x4 ds = {DSENT, DSENT, DSENT, DSENT};
  auto arm = [&]() {
    static_assert(NPRE == 4 && SY_FEND - SY_BR <= 32 * 6144, "");
#pragma unroll
    for (int u = 0; u < 6; ++u)
      st_sc1(rsy, (u < 4 ? SY_V + (i * 4 + u) * 8192 : SY_O + (i * 2 + u - 4) * 8192) + tid * 16, ds);
    if (w == 7) {
      const int n = i * 64 + lane, j = (n >> 10) * 512 + (n & 511);
      const unsigned* m0 = reinterpret_cast<const unsigned*>(mem0 + (2 * ((j >> 2) & 3) + (j & 1)) * DM + (j >> 4) * 16) + ((j >> 1) & 1);
      u32x4 m;
#pragma unroll
      for (int k = 0; k < 4; ++k) { const unsigned t = m0[2 * k]; m[k] = t == DSENT ? 0xFFFFFFFFu : t; }
      st_sc1(rsy, n * 16, m);
    } else if (w == 6) st_sc1(rsy, 32768 + i * 1024 + lane * 16, ds);
    else {
      const int o = SY_BR + i * 6144 + tid * 16;
      st_sc1(rsy, o < SY_FEND ? o : 0x7F000000, (u32x4){SENT, SENT, SENT, SENT});
    }
  };
  int ro = SY_BT + i * 128 + lane * 16;
  asm volatile("" : "+v"(ro));
  unsigned m0 = 30000u, m1 = 30000u;
  {
    if (lane < 32) st_sc1(rs, (lane >> 4) * 8192 + (lane & 15) * 16, ds);
    arm();
    unsigned t[4];
#pragma unroll
    for (int k = 0; k < 4; ++k) {
      unsigned long long c0, c1; unsigned v, off = ((k ^ (k >> 1)) & 1) * 8192 + (k >> 1) * 128;
      asm volatile("s_waitcnt vmcnt(0)\n\ts_barrier\n\ts_memtime %0\n\ts_waitcnt lgkmcnt(0)\n\tbuffer_load_dword %2, %3, %4, 0 offen sc1\n\ts_waitcnt vmcnt(0)\n\t"
                   "s_memtime %1\n\ts_waitcnt lgkmcnt(0)" : "=&s"(c0), "=&s"(c1), "=&v"(v) : "v"(off), "s"(rs) : "memory");
      t[k] = (unsigned)(c1 - c0);
    }
    lds_vu_ptr tm = lds_at(smem);
    if (lane == 0) { tm[w] = min(t[0], t[3]); tm[8 + w] = min(t[1], t[2]); }
    LBAR();
    const unsigned x = tm[lane & 15];
#pragma unroll
    for (int k = 0; k < 8; ++k) { m0 = min(m0, (unsigned)__builtin_amdgcn_readlane(x, k)); m1 = min(m1, (unsigned)__builtin_amdgcn_readlane(x, 8 + k)); }
  }
  if (threadIdx.x < 8) {
    const u32x2 tag = next_tag(e);
    const unsigned v = m0 << 16 | m1;
    st_sc1(rsy, ro, (u32x4){tag[0], tag[1], v, v ^ tag[0]});
  }
}
__device__ __forceinline__ u32x2 start_rows(const Args& p, int g) {
  const int tid = threadIdx.x, lane = tid & 63, row0 = g * 16 + (tid >> 6) * 2;
  u32x4 a[4];
#pragma unroll
  for (int i = 0; i < 4; ++i) a[i] = *reinterpret_cast<const u32x4*>(p.x + row0 * DM + i * 512 + lane * 8);
  const u32x2 e = __builtin_amdgcn_raw_buffer_load_b64(mkrsrc(p.ws, SY_TOTAL), 0, SY_EP, 16);
  const __amdgpu_buffer_rsrc_t ro = mkrsrc(p.ws + WS_XN, S_LEN * XLD * 2);
#pragma unroll
  for (int u = 0; u < 2; ++u) {
    float v[16];
#pragma unroll
    for (int i = 0; i < 8; ++i) { v[2 * i] = lo_f(a[2 * u + (i >> 2)][i & 3]); v[2 * i + 1] = hi_f(a[2 * u + (i >> 2)][i & 3]); }
    float s = 0.f;
#pragma unroll
    for (int i = 0; i < 16; ++i) s += v[i];
    const float mean = wave_sum(s) * (1.0f / DM);
    float q = 0.f;
#pragma unroll
    for (int i = 0; i < 16; ++i) { float d = v[i] - mean; q += d * d; }
    const float var = rbf(wave_sum(q) * (1.0f / DM));
    const float mb = rbf(mean);
    const float inv = 1.0f / rbf(sqrtf(rbf(var + 1e-5f)));
#pragma unroll
    for (int k = 0; k < 2; ++k) {
      u32x4 o;
#pragma unroll
      for (int i = 0; i < 4; ++i) {
        const unsigned t = cvtpk(v[8 * k + 2 * i] - mb, v[8 * k + 2 * i + 1] - mb);
        o[i] = cvtpk(lo_f(t) * inv, hi_f(t) * inv);
      }
      st_sc1(ro, (row0 + u) * (XLD * 2) + k * 1024 + lane * 16, o);
    }
  }
  return next_tag(e);
}
__device__ __forceinline__ void report_rows(const Args& p, int g, u32x2 tag) {
  if (threadIdx.x < 8) st_sc1(mkrsrc(p.ws + WS_SY, SY_TOTAL), SY_BT + (32 + g) * 128 + threadIdx.x * 16, (u32x4){tag[0], tag[1], 0u, tag[0]});
}

typedef __attribute__((ext_vector_type(32))) unsigned u32x32;
__device__ __forceinline__ void put4(u32x32& W, int i, bf16x8 f) {
  const u32x4 v = __builtin_bit_cast(u32x4, f);
#pragma unroll
  for (int j = 0; j < 4; ++j) W[4 * i + j] = v[j];
}
#define CLB8(a, b, c, d, e, f, g, h) "v" #a, "v" #b, "v" #c, "v" #d, "v" #e, "v" #f, "v" #g, "v" #h
#define CLB32(a) CLB32_##a
#define CLB32_128 CLB8(128, 129, 130, 131, 132, 133, 134, 135), CLB8(136, 137, 138, 139, 140, 141, 142, 143), CLB8(144, 145, 146, 147, 148, 149, 150, 151), CLB8(152, 153, 154, 155, 156, 157, 158, 159)
#define CLB32_160 CLB8(160, 161, 162, 163, 164, 165, 166, 167), CLB8(168, 169, 170, 171, 172, 173, 174, 175), CLB8(176, 177, 178, 179, 180, 181, 182, 183), CLB8(184, 185, 186, 187, 188, 189, 190, 191)
#define W4_IN "{v[0:31]}"(W[0]), "{v[32:63]}"(W[1]), "{v[64:95]}"(W[2]), "{v[96:127]}"(W[3])
#define CLB32_192 CLB8(192, 193, 194, 195, 196, 197, 198, 199), CLB8(200, 201, 202, 203, 204, 205, 206, 207), CLB8(208, 209, 210, 211, 212, 213, 214, 215), CLB8(216, 217, 218, 219, 220, 221, 222, 223)
__device__ __forceinline__ unsigned ld_flag0(__amdgpu_buffer_rsrc_t rs, int off) {
  return __builtin_amdgcn_raw_buffer_load_b32(rs, off, 0, 16);
}
__device__ __forceinline__ void glds16_sc1(const char* src, char* dst) {
  const unsigned d = __builtin_amdgcn_readfirstlane(lds_off(dst));
  unsigned keep;
  asm volatile("s_mov_b32 %0, m0\n\ts_mov_b32 m0, %2\n\ts_nop 0\n\tglobal_load_lds_dwordx4 %1, off sc1\n\ts_mov_b32 m0, %0"
               : "=&s"(keep) : "v"(src), "s"(d) : "memory");
}

__device__ __forceinline__ void touch_line(const char* src, char* dst) {
  const unsigned d = __builtin_amdgcn_readfirstlane(lds_off(dst));
  unsigned keep;
  asm volatile("s_mov_b32 %0, m0\n\ts_mov_b32 m0, %2\n\ts_nop 0\n\tglobal_load_lds_dword %1, off sc1\n\ts_mov_b32 m0, %0"
               : "=&s"(keep) : "v"(src), "s"(d) : "memory");
}

#define PF_N 12
#define PR_G1 8
#define PF_T0 0
#define WREV 1
template <bool DEV = false>
__device__ __forceinline__ void glds2(const char* base, int step, unsigned voff, unsigned dst, bool nt = false) {
  unsigned keep;
  const char* base2 = base + step;
  if (nt)
    asm volatile("s_mov_b32 %0, m0\n\ts_mov_b32 m0, %4\n\ts_nop 0\n\tglobal_load_lds_dwordx4 %1, %2 nt\n\t"
                 "s_add_u32 m0, m0, 0x2000\n\ts_nop 0\n\tglobal_load_lds_dwordx4 %1, %3 nt\n\ts_mov_b32 m0, %0"
                 : "=&s"(keep) : "v"(voff), "s"(base), "s"(base2), "s"(dst) : "memory", "scc");
  else
  if (DEV)
    asm volatile("s_mov_b32 %0, m0\n\ts_mov_b32 m0, %4\n\ts_nop 0\n\tglobal_load_lds_dwordx4 %1, %2 sc1\n\t"
                 "s_add_u32 m0, m0, 0x2000\n\ts_nop 0\n\tglobal_load_lds_dwordx4 %1, %3 sc1\n\ts_mov_b32 m0, %0"
                 : "=&s"(keep) : "v"(voff), "s"(base), "s"(base2), "s"(dst) : "memory", "scc");
  else
  asm volatile("s_mov_b32 %0, m0\n\ts_mov_b32 m0, %4\n\ts_nop 0\n\tglobal_load_lds_dwordx4 %1, %2\n\t"
               "s_add_u32 m0, m0, 0x2000\n\ts_nop 0\n\tglobal_load_lds_dwordx4 %1, %3\n\ts_mov_b32 m0, %0"
               : "=&s"(keep) : "v"(voff), "s"(base), "s"(base2), "s"(dst) : "memory", "scc");
}

#define DN_ASM \
  "s_mov_b32 s94, m0\n\t" \
  "s_mov_b64 s[84:85], %[pa]\n\t" \
  "s_mov_b64 s[86:87], %[pb]\n\t" \
  "s_movk_i32 s88, 0x4000\n\t" \
  "s_mov_b32 s89, 0x10000\n\t" \
  "s_movk_i32 s90, 13\n\t" \
  "v_mov_b64 v[0:1], 0\n\t" \
  "v_mov_b64 v[2:3], 0\n\t" \
  "v_mov_b64 v[4:5], 0\n\t" \
  "v_mov_b64 v[6:7], 0\n\t" \
  "v_mov_b64 v[8:9], 0\n\t" \
  "v_mov_b64 v[10:11], 0\n\t" \
  "v_mov_b64 v[12:13], 0\n\t" \
  "v_mov_b64 v[14:15], 0\n\t" \
  "v_mov_b64 v[16:17], 0\n\t" \
  "v_mov_b64 v[18:19], 0\n\t" \
  "v_mov_b64 v[20:21], 0\n\t" \
  "v_mov_b64 v[22:23], 0\n\t" \
  "v_mov_b64 v[24:25], 0\n\t" \
  "v_mov_b64 v[26:27], 0\n\t" \
  "v_mov_b64 v[28:29], 0\n\t" \
  "v_mov_b64 v[30:31], 0\n\t" \
  "s_cmp_lg_u32 %[hf], 0\n\t" \
  "s_cbranch_scc1 .Lk_%=\n\t" \
  "ds_read_b128 v[32:35], v80 offset:0\n\t" \
  "ds_read_b128 v[36:39], v80 offset:1024\n\t" \
  "ds_read_b128 v[40:43], v80 offset:2048\n\t" \
  "ds_read_b128 v[44:47], v80 offset:3072\n\t" \
  "ds_read_b64_tr_b16 v[64:65], v82 offset:0\n\t" \
  "ds_read_b64_tr_b16 v[66:67], v83 offset:0\n\t" \
  "ds_read_b128 v[48:51], v80 offset:8192\n\t" \
  "ds_read_b64_tr_b16 v[68:69], v84 offset:0\n\t" \
  "ds_read_b64_tr_b16 v[70:71], v85 offset:0\n\t" \
  "ds_read_b128 v[52:55], v80 offset:9216\n\t" \
  "ds_read_b128 v[56:59], v80 offset:10240\n\t" \
  "ds_read_b128 v[60:63], v80 offset:11264\n\t" \
  ".Lp0_%=:\n\t" \
  "s_waitcnt vmcnt(4)\n\t" \
  "s_barrier\n\t" \
  "s_waitcnt lgkmcnt(3)\n\t" \
  "v_mfma_f32_16x16x32_bf16 v[0:3], v[64:67], v[32:35], v[0:3]\n\t" \
  "ds_read_b64_tr_b16 v[72:73], v82 offset:8192\n\t" \
  "v_mfma_f32_16x16x32_bf16 v[4:7], v[68:71], v[32:35], v[4:7]\n\t" \
  "ds_read_b64_tr_b16 v[74:75], v83 offset:8192\n\t" \
  "ds_read_b128 v[32:35], v80 offset:32768\n\t" \
  "s_add_u32 m0, %[lw], 98304\n\t" \
  "v_mfma_f32_16x16x32_bf16 v[8:11], v[64:67], v[36:39], v[8:11]\n\t" \
  "ds_read_b64_tr_b16 v[76:77], v84 offset:8192\n\t" \
  "global_load_lds_dwordx4 v90, s[84:85] sc1\n\t" \
  "s_add_u32 s84, s84, s88\n\t" \
  "s_addc_u32 s85, s85, 0\n\t" \
  "v_mfma_f32_16x16x32_bf16 v[12:15], v[68:71], v[36:39], v[12:15]\n\t" \
  "ds_read_b64_tr_b16 v[78:79], v85 offset:8192\n\t" \
  "ds_read_b128 v[36:39], v80 offset:33792\n\t" \
  "v_mfma_f32_16x16x32_bf16 v[16:19], v[64:67], v[40:43], v[16:19]\n\t" \
  "v_mfma_f32_16x16x32_bf16 v[20:23], v[68:71], v[40:43], v[20:23]\n\t" \
  "ds_read_b128 v[40:43], v80 offset:34816\n\t" \
  "s_add_u32 m0, %[lw], 114688\n\t" \
  "v_mfma_f32_16x16x32_bf16 v[24:27], v[64:67], v[44:47], v[24:27]\n\t" \
  "global_load_lds_dwordx4 v91, s[86:87]\n\t" \
  "s_add_u32 s86, s86, s89\n\t" \
  "s_addc_u32 s87, s87, 0\n\t" \
  "v_mfma_f32_16x16x32_bf16 v[28:31], v[68:71], v[44:47], v[28:31]\n\t" \
  "ds_read_b128 v[44:47], v80 offset:35840\n\t" \
  "s_waitcnt lgkmcnt(3)\n\t" \
  "v_mfma_f32_16x16x32_bf16 v[0:3], v[72:75], v[48:51], v[0:3]\n\t" \
  "ds_read_b64_tr_b16 v[64:65], v82 offset:32768\n\t" \
  "v_mfma_f32_16x16x32_bf16 v[4:7], v[76:79], v[48:51], v[4:7]\n\t" \
  "ds_read_b64_tr_b16 v[66:67], v83 offset:32768\n\t" \
  "ds_read_b128 v[48:51], v80 offset:40960\n\t" \
  "s_add_u32 m0, %[lw], 106496\n\t" \
  "v_mfma_f32_16x16x32_bf16 v[8:11], v[72:75], v[52:55], v[8:11]\n\t" \
  "ds_read_b64_tr_b16 v[68:69], v84 offset:32768\n\t" \
  "global_load_lds_dwordx4 v90, s[84:85] sc1\n\t" \
  "s_add_u32 s84, s84, s88\n\t" \
  "s_addc_u32 s85, s85, 0\n\t" \
  "v_mfma_f32_16x16x32_bf16 v[12:15], v[76:79], v[52:55], v[12:15]\n\t" \
  "ds_read_b64_tr_b16 v[70:71], v85 offset:32768\n\t" \
  "ds_read_b128 v[52:55], v80 offset:41984\n\t" \
  "v_mfma_f32_16x16x32_bf16 v[16:19], v[72:75], v[56:59], v[16:19]\n\t" \
  "v_mfma_f32_16x16x32_bf16 v[20:23], v[76:79], v[56:59], v[20:23]\n\t" \
  "ds_read_b128 v[56:59], v80 offset:43008\n\t" \
  "s_add_u32 m0, %[lw], 122880\n\t" \
  "v_mfma_f32_16x16x32_bf16 v[24:27], v[72:75], v[60:63], v[24:27]\n\t" \
  "global_load_lds_dwordx4 v91, s[86:87]\n\t" \
  "s_add_u32 s86, s86, s89\n\t" \
  "s_addc_u32 s87, s87, 0\n\t" \
  "v_mfma_f32_16x16x32_bf16 v[28:31], v[76:79], v[60:63], v[28:31]\n\t" \
  "ds_read_b128 v[60:63], v80 offset:44032\n\t" \
  "s_sub_u32 s90, s90, 1\n\t" \
  "s_cselect_b32 s88, 0, s88\n\t" \
  "s_cselect_b32 s89, 0, s89\n\t" \
  "s_cselect_b32 s91, 16384, 0\n\t" \
  "s_sub_u32 s84, s84, s91\n\t" \
  "s_subb_u32 s85, s85, 0\n\t" \
  "s_lshl_b32 s91, s91, 2\n\t" \
  "s_sub_u32 s86, s86, s91\n\t" \
  "s_subb_u32 s87, s87, 0\n\t" \
  "s_waitcnt vmcnt(4)\n\t" \
  "s_barrier\n\t" \
  "s_waitcnt lgkmcnt(3)\n\t" \
  "v_mfma_f32_16x16x32_bf16 v[0:3], v[64:67], v[32:35], v[0:3]\n\t" \
  "ds_read_b64_tr_b16 v[72:73], v82 offset:40960\n\t" \
  "v_mfma_f32_16x16x32_bf16 v[4:7], v[68:71], v[32:35], v[4:7]\n\t" \
  "ds_read_b64_tr_b16 v[74:75], v83 offset:40960\n\t" \
  "ds_read_b128 v[32:35], v81 offset:0\n\t" \
  "s_add_u32 m0, %[lw], 0\n\t" \
  "v_mfma_f32_16x16x32_bf16 v[8:11], v[64:67], v[36:39], v[8:11]\n\t" \
  "ds_read_b64_tr_b16 v[76:77], v84 offset:40960\n\t" \
  "global_load_lds_dwordx4 v90, s[84:85] sc1\n\t" \
  "s_add_u32 s84, s84, s88\n\t" \
  "s_addc_u32 s85, s85, 0\n\t" \
  "v_mfma_f32_16x16x32_bf16 v[12:15], v[68:71], v[36:39], v[12:15]\n\t" \
  "ds_read_b64_tr_b16 v[78:79], v85 offset:40960\n\t" \
  "ds_read_b128 v[36:39], v81 offset:1024\n\t" \
  "v_mfma_f32_16x16x32_bf16 v[16:19], v[64:67], v[40:43], v[16:19]\n\t" \
  "v_mfma_f32_16x16x32_bf16 v[20:23], v[68:71], v[40:43], v[20:23]\n\t" \
  "ds_read_b128 v[40:43], v81 offset:2048\n\t" \
  "s_add_u32 m0, %[lw], 16384\n\t" \
  "v_mfma_f32_16x16x32_bf16 v[24:27], v[64:67], v[44:47], v[24:27]\n\t" \
  "global_load_lds_dwordx4 v91, s[86:87]\n\t" \
  "s_add_u32 s86, s86, s89\n\t" \
  "s_addc_u32 s87, s87, 0\n\t" \
  "v_mfma_f32_16x16x32_bf16 v[28:31], v[68:71], v[44:47], v[28:31]\n\t" \
  "ds_read_b128 v[44:47], v81 offset:3072\n\t" \
  "s_waitcnt lgkmcnt(3)\n\t" \
  "v_mfma_f32_16x16x32_bf16 v[0:3], v[72:75], v[48:51], v[0:3]\n\t" \
  "ds_read_b64_tr_b16 v[64:65], v86 offset:0\n\t" \
  "v_mfma_f32_16x16x32_bf16 v[4:7], v[76:79], v[48:51], v[4:7]\n\t" \
  "ds_read_b64_tr_b16 v[66:67], v87 offset:0\n\t" \
  "ds_read_b128 v[48:51], v81 offset:8192\n\t" \
  "s_add_u32 m0, %[lw], 8192\n\t" \
  "v_mfma_f32_16x16x32_bf16 v[8:11], v[72:75], v[52:55], v[8:11]\n\t" \
  "ds_read_b64_tr_b16 v[68:69], v88 offset:0\n\t" \
  "global_load_lds_dwordx4 v90, s[84:85] sc1\n\t" \
  "s_add_u32 s84, s84, s88\n\t" \
  "s_addc_u32 s85, s85, 0\n\t" \
  "v_mfma_f32_16x16x32_bf16 v[12:15], v[76:79], v[52:55], v[12:15]\n\t" \
  "ds_read_b64_tr_b16 v[70:71], v89 offset:0\n\t" \
  "ds_read_b128 v[52:55], v81 offset:9216\n\t" \
  "v_mfma_f32_16x16x32_bf16 v[16:19], v[72:75], v[56:59], v[16:19]\n\t" \
  "v_mfma_f32_16x16x32_bf16 v[20:23], v[76:79], v[56:59], v[20:23]\n\t" \
  "ds_read_b128 v[56:59], v81 offset:10240\n\t" \
  "s_add_u32 m0, %[lw], 24576\n\t" \
  "v_mfma_f32_16x16x32_bf16 v[24:27], v[72:75], v[60:63], v[24:27]\n\t" \
  "global_load_lds_dwordx4 v91, s[86:87]\n\t" \
  "s_add_u32 s86, s86, s89\n\t" \
  "s_addc_u32 s87, s87, 0\n\t" \
  "v_mfma_f32_16x16x32_bf16 v[28:31], v[76:79], v[60:63], v[28:31]\n\t" \
  "ds_read_b128 v[60:63], v81 offset:11264\n\t" \
  "s_waitcnt vmcnt(4)\n\t" \
  "s_barrier\n\t" \
  "s_waitcnt lgkmcnt(3)\n\t" \
  "v_mfma_f32_16x16x32_bf16 v[0:3], v[64:67], v[32:35], v[0:3]\n\t" \
  "ds_read_b64_tr_b16 v[72:73], v86 offset:8192\n\t" \
  "v_mfma_f32_16x16x32_bf16 v[4:7], v[68:71], v[32:35], v[4:7]\n\t" \
  "ds_read_b64_tr_b16 v[74:75], v87 offset:8192\n\t" \
  "ds_read_b128 v[32:35], v81 offset:32768\n\t" \
  "s_add_u32 m0, %[lw], 32768\n\t" \
  "v_mfma_f32_16x16x32_bf16 v[8:11], v[64:67], v[36:39], v[8:11]\n\t" \
  "ds_read_b64_tr_b16 v[76:77], v88 offset:8192\n\t" \
  "global_load_lds_dwordx4 v90, s[84:85] sc1\n\t" \
  "s_add_u32 s84, s84, s88\n\t" \
  "s_addc_u32 s85, s85, 0\n\t" \
  "v_mfma_f32_16x16x32_bf16 v[12:15], v[68:71], v[36:39], v[12:15]\n\t" \
  "ds_read_b64_tr_b16 v[78:79], v89 offset:8192\n\t" \
  "ds_read_b128 v[36:39], v81 offset:33792\n\t" \
  "v_mfma_f32_16x16x32_bf16 v[16:19], v[64:67], v[40:43], v[16:19]\n\t" \
  "v_mfma_f32_16x16x32_bf16 v[20:23], v[68:71], v[40:43], v[20:23]\n\t" \
  "ds_read_b128 v[40:43], v81 offset:34816\n\t" \
  "s_add_u32 m0, %[lw], 49152\n\t" \
  "v_mfma_f32_16x16x32_bf16 v[24:27], v[64:67], v[44:47], v[24:27]\n\t" \
  "global_load_lds_dwordx4 v91, s[86:87]\n\t" \
  "s_add_u32 s86, s86, s89\n\t" \
  "s_addc_u32 s87, s87, 0\n\t" \
  "v_mfma_f32_16x16x32_bf16 v[28:31], v[68:71], v[44:47], v[28:31]\n\t" \
  "ds_read_b128 v[44:47], v81 offset:35840\n\t" \
  "s_waitcnt lgkmcnt(3)\n\t" \
  "v_mfma_f32_16x16x32_bf16 v[0:3], v[72:75], v[48:51], v[0:3]\n\t" \
  "ds_read_b64_tr_b16 v[64:65], v86 offset:32768\n\t" \
  "v_mfma_f32_16x16x32_bf16 v[4:7], v[76:79], v[48:51], v[4:7]\n\t" \
  "ds_read_b64_tr_b16 v[66:67], v87 offset:32768\n\t" \
  "ds_read_b128 v[48:51], v81 offset:40960\n\t" \
  "s_add_u32 m0, %[lw], 40960\n\t" \
  "v_mfma_f32_16x16x32_bf16 v[8:11], v[72:75], v[52:55], v[8:11]\n\t" \
  "ds_read_b64_tr_b16 v[68:69], v88 offset:32768\n\t" \
  "global_load_lds_dwordx4 v90, s[84:85] sc1\n\t" \
  "s_add_u32 s84, s84, s88\n\t" \
  "s_addc_u32 s85, s85, 0\n\t" \
  "v_mfma_f32_16x16x32_bf16 v[12:15], v[76:79], v[52:55], v[12:15]\n\t" \
  "ds_read_b64_tr_b16 v[70:71], v89 offset:32768\n\t" \
  "ds_read_b128 v[52:55], v81 offset:41984\n\t" \
  "v_mfma_f32_16x16x32_bf16 v[16:19], v[72:75], v[56:59], v[16:19]\n\t" \
  "v_mfma_f32_16x16x32_bf16 v[20:23], v[76:79], v[56:59], v[20:23]\n\t" \
  "ds_read_b128 v[56:59], v81 offset:43008\n\t" \
  "s_add_u32 m0, %[lw], 57344\n\t" \
  "v_mfma_f32_16x16x32_bf16 v[24:27], v[72:75], v[60:63], v[24:27]\n\t" \
  "global_load_lds_dwordx4 v91, s[86:87]\n\t" \
  "s_add_u32 s86, s86, s89\n\t" \
  "s_addc_u32 s87, s87, 0\n\t" \
  "v_mfma_f32_16x16x32_bf16 v[28:31], v[76:79], v[60:63], v[28:31]\n\t" \
  "ds_read_b128 v[60:63], v81 offset:44032\n\t" \
  "s_waitcnt vmcnt(4)\n\t" \
  "s_barrier\n\t" \
  "s_waitcnt lgkmcnt(3)\n\t" \
  "v_mfma_f32_16x16x32_bf16 v[0:3], v[64:67], v[32:35], v[0:3]\n\t" \
  "ds_read_b64_tr_b16 v[72:73], v86 offset:40960\n\t" \
  "v_mfma_f32_16x16x32_bf16 v[4:7], v[68:71], v[32:35], v[4:7]\n\t" \
  "ds_read_b64_tr_b16 v[74:75], v87 offset:40960\n\t" \
  "ds_read_b128 v[32:35], v80 offset:0\n\t" \
  "s_add_u32 m0, %[lw], 65536\n\t" \
  "v_mfma_f32_16x16x32_bf16 v[8:11], v[64:67], v[36:39], v[8:11]\n\t" \
  "ds_read_b64_tr_b16 v[76:77], v88 offset:40960\n\t" \
  "global_load_lds_dwordx4 v90, s[84:85] sc1\n\t" \
  "s_add_u32 s84, s84, s88\n\t" \
  "s_addc_u32 s85, s85, 0\n\t" \
  "v_mfma_f32_16x16x32_bf16 v[12:15], v[68:71], v[36:39], v[12:15]\n\t" \
  "ds_read_b64_tr_b16 v[78:79], v89 offset:40960\n\t" \
  "ds_read_b128 v[36:39], v80 offset:1024\n\t" \
  "v_mfma_f32_16x16x32_bf16 v[16:19], v[64:67], v[40:43], v[16:19]\n\t" \
  "v_mfma_f32_16x16x32_bf16 v[20:23], v[68:71], v[40:43], v[20:23]\n\t" \
  "ds_read_b128 v[40:43], v80 offset:2048\n\t" \
  "s_add_u32 m0, %[lw], 81920\n\t" \
  "v_mfma_f32_16x16x32_bf16 v[24:27], v[64:67], v[44:47], v[24:27]\n\t" \
  "global_load_lds_dwordx4 v91, s[86:87]\n\t" \
  "s_add_u32 s86, s86, s89\n\t" \
  "s_addc_u32 s87, s87, 0\n\t" \
  "v_mfma_f32_16x16x32_bf16 v[28:31], v[68:71], v[44:47], v[28:31]\n\t" \
  "ds_read_b128 v[44:47], v80 offset:3072\n\t" \
  "s_waitcnt lgkmcnt(3)\n\t" \
  "v_mfma_f32_16x16x32_bf16 v[0:3], v[72:75], v[48:51], v[0:3]\n\t" \
  "ds_read_b64_tr_b16 v[64:65], v82 offset:0\n\t" \
  "v_mfma_f32_16x16x32_bf16 v[4:7], v[76:79], v[48:51], v[4:7]\n\t" \
  "ds_read_b64_tr_b16 v[66:67], v83 offset:0\n\t" \
  "ds_read_b128 v[48:51], v80 offset:8192\n\t" \
  "s_add_u32 m0, %[lw], 73728\n\t" \
  "v_mfma_f32_16x16x32_bf16 v[8:11], v[72:75], v[52:55], v[8:11]\n\t" \
  "ds_read_b64_tr_b16 v[68:69], v84 offset:0\n\t" \
  "global_load_lds_dwordx4 v90, s[84:85] sc1\n\t" \
  "s_add_u32 s84, s84, s88\n\t" \
  "s_addc_u32 s85, s85, 0\n\t" \
  "v_mfma_f32_16x16x32_bf16 v[12:15], v[76:79], v[52:55], v[12:15]\n\t" \
  "ds_read_b64_tr_b16 v[70:71], v85 offset:0\n\t" \
  "ds_read_b128 v[52:55], v80 offset:9216\n\t" \
  "v_mfma_f32_16x16x32_bf16 v[16:19], v[72:75], v[56:59], v[16:19]\n\t" \
  "v_mfma_f32_16x16x32_bf16 v[20:23], v[76:79], v[56:59], v[20:23]\n\t" \
  "ds_read_b128 v[56:59], v80 offset:10240\n\t" \
  "s_add_u32 m0, %[lw], 90112\n\t" \
  "v_mfma_f32_16x16x32_bf16 v[24:27], v[72:75], v[60:63], v[24:27]\n\t" \
  "global_load_lds_dwordx4 v91, s[86:87]\n\t" \
  "s_add_u32 s86, s86, s89\n\t" \
  "s_addc_u32 s87, s87, 0\n\t" \
  "v_mfma_f32_16x16x32_bf16 v[28:31], v[76:79], v[60:63], v[28:31]\n\t" \
  "ds_read_b128 v[60:63], v80 offset:11264\n\t" \
  "s_cmp_lg_u32 s88, 0\n\t" \
  "s_cbranch_scc1 .Lp0_%=\n\t" \
  "s_branch .Le_%=\n\t" \
  ".Lk_%=:\n\t" \
  "ds_read_b128 v[32:35], v80 offset:0\n\t" \
  "ds_read_b128 v[36:39], v80 offset:1024\n\t" \
  "ds_read_b128 v[40:43], v80 offset:2048\n\t" \
  "ds_read_b128 v[44:47], v80 offset:3072\n\t" \
  "ds_read_b64_tr_b16 v[64:65], v82 offset:0\n\t" \
  "ds_read_b64_tr_b16 v[66:67], v83 offset:0\n\t" \
  "ds_read_b128 v[48:51], v80 offset:8192\n\t" \
  "ds_read_b64_tr_b16 v[68:69], v84 offset:0\n\t" \
  "ds_read_b64_tr_b16 v[70:71], v85 offset:0\n\t" \
  "ds_read_b128 v[52:55], v80 offset:9216\n\t" \
  "ds_read_b128 v[56:59], v80 offset:10240\n\t" \
  "ds_read_b128 v[60:63], v80 offset:11264\n\t" \
  ".Lp1_%=:\n\t" \
  "s_waitcnt vmcnt(4)\n\t" \
  "s_barrier\n\t" \
  "s_waitcnt lgkmcnt(3)\n\t" \
  "v_mfma_f32_16x16x32_bf16 v[0:3], v[64:67], v[32:35], v[0:3]\n\t" \
  "ds_read_b64_tr_b16 v[72:73], v82 offset:8192\n\t" \
  "v_mfma_f32_16x16x32_bf16 v[4:7], v[68:71], v[32:35], v[4:7]\n\t" \
  "ds_read_b64_tr_b16 v[74:75], v83 offset:8192\n\t" \
  "ds_read_b128 v[32:35], v80 offset:32768\n\t" \
  "v_mfma_f32_16x16x32_bf16 v[8:11], v[64:67], v[36:39], v[8:11]\n\t" \
  "ds_read_b64_tr_b16 v[76:77], v84 offset:8192\n\t" \
  "v_mfma_f32_16x16x32_bf16 v[12:15], v[68:71], v[36:39], v[12:15]\n\t" \
  "ds_read_b64_tr_b16 v[78:79], v85 offset:8192\n\t" \
  "ds_read_b128 v[36:39], v80 offset:33792\n\t" \
  "s_add_u32 m0, %[lw], 98304\n\t" \
  "v_mfma_f32_16x16x32_bf16 v[16:19], v[64:67], v[40:43], v[16:19]\n\t" \
  "global_load_lds_dwordx4 v90, s[84:85] sc1\n\t" \
  "s_add_u32 s84, s84, s88\n\t" \
  "s_addc_u32 s85, s85, 0\n\t" \
  "v_mfma_f32_16x16x32_bf16 v[20:23], v[68:71], v[40:43], v[20:23]\n\t" \
  "ds_read_b128 v[40:43], v80 offset:34816\n\t" \
  "v_mfma_f32_16x16x32_bf16 v[24:27], v[64:67], v[44:47], v[24:27]\n\t" \
  "s_add_u32 m0, %[lw], 114688\n\t" \
  "v_mfma_f32_16x16x32_bf16 v[28:31], v[68:71], v[44:47], v[28:31]\n\t" \
  "ds_read_b128 v[44:47], v80 offset:35840\n\t" \
  "global_load_lds_dwordx4 v91, s[86:87]\n\t" \
  "s_add_u32 s86, s86, s89\n\t" \
  "s_addc_u32 s87, s87, 0\n\t" \
  "s_waitcnt lgkmcnt(3)\n\t" \
  "v_mfma_f32_16x16x32_bf16 v[0:3], v[72:75], v[48:51], v[0:3]\n\t" \
  "ds_read_b64_tr_b16 v[64:65], v82 offset:32768\n\t" \
  "v_mfma_f32_16x16x32_bf16 v[4:7], v[76:79], v[48:51], v[4:7]\n\t" \
  "ds_read_b64_tr_b16 v[66:67], v83 offset:32768\n\t" \
  "ds_read_b128 v[48:51], v80 offset:40960\n\t" \
  "v_mfma_f32_16x16x32_bf16 v[8:11], v[72:75], v[52:55], v[8:11]\n\t" \
  "ds_read_b64_tr_b16 v[68:69], v84 offset:32768\n\t" \
  "v_mfma_f32_16x16x32_bf16 v[12:15], v[76:79], v[52:55], v[12:15]\n\t" \
  "ds_read_b64_tr_b16 v[70:71], v85 offset:32768\n\t" \
  "ds_read_b128 v[52:55], v80 offset:41984\n\t" \
  "s_add_u32 m0, %[lw], 106496\n\t" \
  "v_mfma_f32_16x16x32_bf16 v[16:19], v[72:75], v[56:59], v[16:19]\n\t" \
  "global_load_lds_dwordx4 v90, s[84:85] sc1\n\t" \
  "s_add_u32 s84, s84, s88\n\t" \
  "s_addc_u32 s85, s85, 0\n\t" \
  "v_mfma_f32_16x16x32_bf16 v[20:23], v[76:79], v[56:59], v[20:23]\n\t" \
  "ds_read_b128 v[56:59], v80 offset:43008\n\t" \
  "v_mfma_f32_16x16x32_bf16 v[24:27], v[72:75], v[60:63], v[24:27]\n\t" \
  "s_add_u32 m0, %[lw], 122880\n\t" \
  "v_mfma_f32_16x16x32_bf16 v[28:31], v[76:79], v[60:63], v[28:31]\n\t" \
  "ds_read_b128 v[60:63], v80 offset:44032\n\t" \
  "global_load_lds_dwordx4 v91, s[86:87]\n\t" \
  "s_add_u32 s86, s86, s89\n\t" \
  "s_addc_u32 s87, s87, 0\n\t" \
  "s_sub_u32 s90, s90, 1\n\t" \
  "s_cselect_b32 s88, 0, s88\n\t" \
  "s_cselect_b32 s89, 0, s89\n\t" \
  "s_cselect_b32 s91, 16384, 0\n\t" \
  "s_sub_u32 s84, s84, s91\n\t" \
  "s_subb_u32 s85, s85, 0\n\t" \
  "s_lshl_b32 s91, s91, 2\n\t" \
  "s_sub_u32 s86, s86, s91\n\t" \
  "s_subb_u32 s87, s87, 0\n\t" \
  "s_waitcnt vmcnt(4)\n\t" \
  "s_barrier\n\t" \
  "s_waitcnt lgkmcnt(3)\n\t" \
  "v_mfma_f32_16x16x32_bf16 v[0:3], v[64:67], v[32:35], v[0:3]\n\t" \
  "ds_read_b64_tr_b16 v[72:73], v82 offset:40960\n\t" \
  "v_mfma_f32_16x16x32_bf16 v[4:7], v[68:71], v[32:35], v[4:7]\n\t" \
  "ds_read_b64_tr_b16 v[74:75], v83 offset:40960\n\t" \
  "ds_read_b128 v[32:35], v81 offset:0\n\t" \
  "v_mfma_f32_16x16x32_bf16 v[8:11], v[64:67], v[36:39], v[8:11]\n\t" \
  "ds_read_b64_tr_b16 v[76:77], v84 offset:40960\n\t" \
  "v_mfma_f32_16x16x32_bf16 v[12:15], v[68:71], v[36:39], v[12:15]\n\t" \
  "ds_read_b64_tr_b16 v[78:79], v85 offset:40960\n\t" \
  "ds_read_b128 v[36:39], v81 offset:1024\n\t" \
  "s_add_u32 m0, %[lw], 0\n\t" \
  "v_mfma_f32_16x16x32_bf16 v[16:19], v[64:67], v[40:43], v[16:19]\n\t" \
  "global_load_lds_dwordx4 v90, s[84:85] sc1\n\t" \
  "s_add_u32 s84, s84, s88\n\t" \
  "s_addc_u32 s85, s85, 0\n\t" \
  "v_mfma_f32_16x16x32_bf16 v[20:23], v[68:71], v[40:43], v[20:23]\n\t" \
  "ds_read_b128 v[40:43], v81 offset:2048\n\t" \
  "v_mfma_f32_16x16x32_bf16 v[24:27], v[64:67], v[44:47], v[24:27]\n\t" \
  "s_add_u32 m0, %[lw], 16384\n\t" \
  "v_mfma_f32_16x16x32_bf16 v[28:31], v[68:71], v[44:47], v[28:31]\n\t" \
  "ds_read_b128 v[44:47], v81 offset:3072\n\t" \
  "global_load_lds_dwordx4 v91, s[86:87]\n\t" \
  "s_add_u32 s86, s86, s89\n\t" \
  "s_addc_u32 s87, s87, 0\n\t" \
  "s_waitcnt lgkmcnt(3)\n\t" \
  "v_mfma_f32_16x16x32_bf16 v[0:3], v[72:75], v[48:51], v[0:3]\n\t" \
  "ds_read_b64_tr_b16 v[64:65], v86 offset:0\n\t" \
  "v_mfma_f32_16x16x32_bf16 v[4:7], v[76:79], v[48:51], v[4:7]\n\t" \
  "ds_read_b64_tr_b16 v[66:67], v87 offset:0\n\t" \
  "ds_read_b128 v[48:51], v81 offset:8192\n\t" \
  "v_mfma_f32_16x16x32_bf16 v[8:11], v[72:75], v[52:55], v[8:11]\n\t" \
  "ds_read_b64_tr_b16 v[68:69], v88 offset:0\n\t" \
  "v_mfma_f32_16x16x32_bf16 v[12:15], v[76:79], v[52:55], v[12:15]\n\t" \
  "ds_read_b64_tr_b16 v[70:71], v89 offset:0\n\t" \
  "ds_read_b128 v[52:55], v81 offset:9216\n\t" \
  "s_add_u32 m0, %[lw], 8192\n\t" \
  "v_mfma_f32_16x16x32_bf16 v[16:19], v[72:75], v[56:59], v[16:19]\n\t" \
  "global_load_lds_dwordx4 v90, s[84:85] sc1\n\t" \
  "s_add_u32 s84, s84, s88\n\t" \
  "s_addc_u32 s85, s85, 0\n\t" \
  "v_mfma_f32_16x16x32_bf16 v[20:23], v[76:79], v[56:59], v[20:23]\n\t" \
  "ds_read_b128 v[56:59], v81 offset:10240\n\t" \
  "v_mfma_f32_16x16x32_bf16 v[24:27], v[72:75], v[60:63], v[24:27]\n\t" \
  "s_add_u32 m0, %[lw], 24576\n\t" \
  "v_mfma_f32_16x16x32_bf16 v[28:31], v[76:79], v[60:63], v[28:31]\n\t" \
  "ds_read_b128 v[60:63], v81 offset:11264\n\t" \
  "global_load_lds_dwordx4 v91, s[86:87]\n\t" \
  "s_add_u32 s86, s86, s89\n\t" \
  "s_addc_u32 s87, s87, 0\n\t" \
  "s_waitcnt vmcnt(4)\n\t" \
  "s_barrier\n\t" \
  "s_waitcnt lgkmcnt(3)\n\t" \
  "v_mfma_f32_16x16x32_bf16 v[0:3], v[64:67], v[32:35], v[0:3]\n\t" \
  "ds_read_b64_tr_b16 v[72:73], v86 offset:8192\n\t" \
  "v_mfma_f32_16x16x32_bf16 v[4:7], v[68:71], v[32:35], v[4:7]\n\t" \
  "ds_read_b64_tr_b16 v[74:75], v87 offset:8192\n\t" \
  "ds_read_b128 v[32:35], v81 offset:32768\n\t" \
  "v_mfma_f32_16x16x32_bf16 v[8:11], v[64:67], v[36:39], v[8:11]\n\t" \
  "ds_read_b64_tr_b16 v[76:77], v88 offset:8192\n\t" \
  "v_mfma_f32_16x16x32_bf16 v[12:15], v[68:71], v[36:39], v[12:15]\n\t" \
  "ds_read_b64_tr_b16 v[78:79], v89 offset:8192\n\t" \
  "ds_read_b128 v[36:39], v81 offset:33792\n\t" \
  "s_add_u32 m0, %[lw], 32768\n\t" \
  "v_mfma_f32_16x16x32_bf16 v[16:19], v[64:67], v[40:43], v[16:19]\n\t" \
  "global_load_lds_dwordx4 v90, s[84:85] sc1\n\t" \
  "s_add_u32 s84, s84, s88\n\t" \
  "s_addc_u32 s85, s85, 0\n\t" \
  "v_mfma_f32_16x16x32_bf16 v[20:23], v[68:71], v[40:43], v[20:23]\n\t" \
  "ds_read_b128 v[40:43], v81 offset:34816\n\t" \
  "v_mfma_f32_16x16x32_bf16 v[24:27], v[64:67], v[44:47], v[24:27]\n\t" \
  "s_add_u32 m0, %[lw], 49152\n\t" \
  "v_mfma_f32_16x16x32_bf16 v[28:31], v[68:71], v[44:47], v[28:31]\n\t" \
  "ds_read_b128 v[44:47], v81 offset:35840\n\t" \
  "global_load_lds_dwordx4 v91, s[86:87]\n\t" \
  "s_add_u32 s86, s86, s89\n\t" \
  "s_addc_u32 s87, s87, 0\n\t" \
  "s_waitcnt lgkmcnt(3)\n\t" \
  "v_mfma_f32_16x16x32_bf16 v[0:3], v[72:75], v[48:51], v[0:3]\n\t" \
  "ds_read_b64_tr_b16 v[64:65], v86 offset:32768\n\t" \
  "v_mfma_f32_16x16x32_bf16 v[4:7], v[76:79], v[48:51], v[4:7]\n\t" \
  "ds_read_b64_tr_b16 v[66:67], v87 offset:32768\n\t" \
  "ds_read_b128 v[48:51], v81 offset:40960\n\t" \
  "v_mfma_f32_16x16x32_bf16 v[8:11], v[72:75], v[52:55], v[8:11]\n\t" \
  "ds_read_b64_tr_b16 v[68:69], v88 offset:32768\n\t" \
  "v_mfma_f32_16x16x32_bf16 v[12:15], v[76:79], v[52:55], v[12:15]\n\t" \
  "ds_read_b64_tr_b16 v[70:71], v89 offset:32768\n\t" \
  "ds_read_b128 v[52:55], v81 offset:41984\n\t" \
  "s_add_u32 m0, %[lw], 40960\n\t" \
  "v_mfma_f32_16x16x32_bf16 v[16:19], v[72:75], v[56:59], v[16:19]\n\t" \
  "global_load_lds_dwordx4 v90, s[84:85] sc1\n\t" \
  "s_add_u32 s84, s84, s88\n\t" \
  "s_addc_u32 s85, s85, 0\n\t" \
  "v_mfma_f32_16x16x32_bf16 v[20:23], v[76:79], v[56:59], v[20:23]\n\t" \
  "ds_read_b128 v[56:59], v81 offset:43008\n\t" \
  "v_mfma_f32_16x16x32_bf16 v[24:27], v[72:75], v[60:63], v[24:27]\n\t" \
  "s_add_u32 m0, %[lw], 57344\n\t" \
  "v_mfma_f32_16x16x32_bf16 v[28:31], v[76:79], v[60:63], v[28:31]\n\t" \
  "ds_read_b128 v[60:63], v81 offset:44032\n\t" \
  "global_load_lds_dwordx4 v91, s[86:87]\n\t" \
  "s_add_u32 s86, s86, s89\n\t" \
  "s_addc_u32 s87, s87, 0\n\t" \
  "s_waitcnt vmcnt(4)\n\t" \
  "s_barrier\n\t" \
  "s_waitcnt lgkmcnt(3)\n\t" \
  "v_mfma_f32_16x16x32_bf16 v[0:3], v[64:67], v[32:35], v[0:3]\n\t" \
  "ds_read_b64_tr_b16 v[72:73], v86 offset:40960\n\t" \
  "v_mfma_f32_16x16x32_bf16 v[4:7], v[68:71], v[32:35], v[4:7]\n\t" \
  "ds_read_b64_tr_b16 v[74:75], v87 offset:40960\n\t" \
  "ds_read_b128 v[32:35], v80 offset:0\n\t" \
  "v_mfma_f32_16x16x32_bf16 v[8:11], v[64:67], v[36:39], v[8:11]\n\t" \
  "ds_read_b64_tr_b16 v[76:77], v88 offset:40960\n\t" \
  "v_mfma_f32_16x16x32_bf16 v[12:15], v[68:71], v[36:39], v[12:15]\n\t" \
  "ds_read_b64_tr_b16 v[78:79], v89 offset:40960\n\t" \
  "ds_read_b128 v[36:39], v80 offset:1024\n\t" \
  "s_add_u32 m0, %[lw], 65536\n\t" \
  "v_mfma_f32_16x16x32_bf16 v[16:19], v[64:67], v[40:43], v[16:19]\n\t" \
  "global_load_lds_dwordx4 v90, s[84:85] sc1\n\t" \
  "s_add_u32 s84, s84, s88\n\t" \
  "s_addc_u32 s85, s85, 0\n\t" \
  "v_mfma_f32_16x16x32_bf16 v[20:23], v[68:71], v[40:43], v[20:23]\n\t" \
  "ds_read_b128 v[40:43], v80 offset:2048\n\t" \
  "v_mfma_f32_16x16x32_bf16 v[24:27], v[64:67], v[44:47], v[24:27]\n\t" \
  "s_add_u32 m0, %[lw], 81920\n\t" \
  "v_mfma_f32_16x16x32_bf16 v[28:31], v[68:71], v[44:47], v[28:31]\n\t" \
  "ds_read_b128 v[44:47], v80 offset:3072\n\t" \
  "global_load_lds_dwordx4 v91, s[86:87]\n\t" \
  "s_add_u32 s86, s86, s89\n\t" \
  "s_addc_u32 s87, s87, 0\n\t" \
  "s_waitcnt lgkmcnt(3)\n\t" \
  "v_mfma_f32_16x16x32_bf16 v[0:3], v[72:75], v[48:51], v[0:3]\n\t" \
  "ds_read_b64_tr_b16 v[64:65], v82 offset:0\n\t" \
  "v_mfma_f32_16x16x32_bf16 v[4:7], v[76:79], v[48:51], v[4:7]\n\t" \
  "ds_read_b64_tr_b16 v[66:67], v83 offset:0\n\t" \
  "ds_read_b128 v[48:51], v80 offset:8192\n\t" \
  "v_mfma_f32_16x16x32_bf16 v[8:11], v[72:75], v[52:55], v[8:11]\n\t" \
  "ds_read_b64_tr_b16 v[68:69], v84 offset:0\n\t" \
  "v_mfma_f32_16x16x32_bf16 v[12:15], v[76:79], v[52:55], v[12:15]\n\t" \
  "ds_read_b64_tr_b16 v[70:71], v85 offset:0\n\t" \
  "ds_read_b128 v[52:55], v80 offset:9216\n\t" \
  "s_add_u32 m0, %[lw], 73728\n\t" \
  "v_mfma_f32_16x16x32_bf16 v[16:19], v[72:75], v[56:59], v[16:19]\n\t" \
  "global_load_lds_dwordx4 v90, s[84:85] sc1\n\t" \
  "s_add_u32 s84, s84, s88\n\t" \
  "s_addc_u32 s85, s85, 0\n\t" \
  "v_mfma_f32_16x16x32_bf16 v[20:23], v[76:79], v[56:59], v[20:23]\n\t" \
  "ds_read_b128 v[56:59], v80 offset:10240\n\t" \
  "v_mfma_f32_16x16x32_bf16 v[24:27], v[72:75], v[60:63], v[24:27]\n\t" \
  "s_add_u32 m0, %[lw], 90112\n\t" \
  "v_mfma_f32_16x16x32_bf16 v[28:31], v[76:79], v[60:63], v[28:31]\n\t" \
  "ds_read_b128 v[60:63], v80 offset:11264\n\t" \
  "global_load_lds_dwordx4 v91, s[86:87]\n\t" \
  "s_add_u32 s86, s86, s89\n\t" \
  "s_addc_u32 s87, s87, 0\n\t" \
  "s_cmp_lg_u32 s88, 0\n\t" \
  "s_cbranch_scc1 .Lp1_%=\n\t" \
  ".Le_%=:\n\t" \
  "s_waitcnt vmcnt(0) lgkmcnt(0)\n\t" \
  "s_mov_b32 m0, s94\n\t" \
  "s_setprio 0\n\t" \
  "s_nop 7\n\t"
#define HSL 16
__device__ __forceinline__ void job_down(const Args& p, int rblk, int ct, char* smem, __amdgpu_buffer_rsrc_t rsy) {
  if (threadIdx.x < 64) {
    for (unsigned spins = 0;;) {
      asm volatile("" ::: "memory");
      if (__builtin_amdgcn_readfirstlane((int)ld_flag0(rsy, SY_CH + (rblk >> 1) * 128)) >= 27) break;
      __builtin_amdgcn_s_sleep(HSL);
      if (spin_fail(spins, (gu32*)(p.ws + WS_SY + SY_ABORT))) break;
    }
  }
  int tid = threadIdx.x;
  asm volatile("" : "+v"(tid));
  const int lane = tid & 63, r = lane & 15, q = lane >> 4;
  const int wid = __builtin_amdgcn_readfirstlane(tid >> 6), wr = wid >> 2, wc = wid & 3;
  typedef __attribute__((ext_vector_type(32))) float f32x32;
  typedef __attribute__((ext_vector_type(8))) unsigned u32x8;
  f32x32 acc;
  {
    const char* A = p.ws + WS_HF + h_off(p, rblk);
    const char* B = (const char*)(p.wd + ct * 128);
    const unsigned rb = tid >> 4, va = tid * 16, vb = ((rb & 4) * 4 + (rb >> 3) * 4 + (rb & 3)) * (DM * 2) + 16 * ((tid & 15) ^ swzB(rb & 15));
    u32x8 lb;
#pragma unroll
    for (int i = 0; i < 8; ++i)
      lb[i] = (i >> 2) * 65536 + 16384 + offB(8 * q + 4 * (i & 1) + (r >> 2), 2 * (wc * 2 + ((i >> 1) & 1)) + ((r & 3) >> 1)) + 8 * (r & 1);
    LBAR();
#pragma unroll
    for (int t = 0; t < 3; ++t) {
      glds2<true>(A + t * 32768, 16384, va, wid * 1024 + t * 32768);
      glds2(B + t * 64 * DM * 2, 32 * DM * 2, vb, wid * 1024 + t * 32768 + 16384);
    }
    WAIT_V(8);
    LBAR();
    asm volatile(DN_ASM
        : "={v[0:31]}"(acc)
        : "{v[80:81]}"((u32x2){wr * 4096u + lane * 16u, 65536u + wr * 4096u + lane * 16u}), "{v[82:89]}"(lb), "{v90}"(va), "{v91}"(vb),
          [pa] "s"(A + 3 * 32768), [pb] "s"(B + 3 * 64 * DM * 2), [lw] "s"(wid * 1024), [hf] "s"(wr)
        : CLB8(32, 33, 34, 35, 36, 37, 38, 39), CLB8(40, 41, 42, 43, 44, 45, 46, 47), CLB8(48, 49, 50, 51, 52, 53, 54, 55),
          CLB8(56, 57, 58, 59, 60, 61, 62, 63), CLB8(64, 65, 66, 67, 68, 69, 70, 71), CLB8(72, 73, 74, 75, 76, 77, 78, 79),
          "s84", "s85", "s86", "s87", "s88", "s89", "s90", "s91", "s94", "scc", "memory");
  }
  wait_s(p, rsy, rblk, ct * 4, 4, rblk < NFUSE);
  const int e0 = (rblk * 128 + wr * 64 + r) * DM + ct * 128 + wc * 32 + q * 8;
  SX sx[4];
#pragma unroll
  for (int m = 0; m < 4; ++m) sx[m] = get_sx(p, rblk, e0 + m * 16 * DM);
  const __amdgpu_buffer_rsrc_t ro = mkrsrc(p.ws + WS_G, S_LEN * DM * 2);
#pragma unroll
  for (int m = 0; m < 4; ++m) {
    float rr[8];
#pragma unroll
    for (int j = 0; j < 8; ++j) { float d = rbf(acc[8 * m + j]); rr[j] = d * __builtin_amdgcn_rcpf(1.0f + __expf(-d)); }
    put_g(p, ro, rblk, e0 + m * 16 * DM, widen32(cvtpk(rr[0], rr[1]), cvtpk(rr[2], rr[3]), cvtpk(rr[4], rr[5]), cvtpk(rr[6], rr[7])), sx[m]);
  }
  if (rblk >= NFUSE) publish_flags(rsy, SY_FG + (rblk * 8 + ct) * 128, 0, 1);
}

#define PR_ASM_0 \
  "s_cmp_lg_u32 %[kh], 0\n\t" \
  "s_cbranch_scc1 .Lk_%=\n\t" \
  "s_mov_b32 s94, m0\n\t" \
  "s_mov_b64 s[92:93], %[x0]\n\t" \
  "s_mov_b32 s86, %[g0]\n\t" \
  "s_add_u32 s87, s86, %[t0]\n\t" \
  "s_sub_i32 s88, s86, 3\n\t" \
  "ds_read_b128 v[176:179], v208 offset:0\n\t" \
  "ds_read_b128 v[180:183], v208 offset:16384\n\t" \
  "ds_read_b128 v[184:187], v209 offset:0\n\t" \
  "ds_read_b128 v[188:191], v209 offset:16384\n\t" \
  "ds_read_b128 v[192:195], v210 offset:0\n\t" \
  "ds_read_b128 v[196:199], v210 offset:16384\n\t" \
  "ds_read_b128 v[200:203], v211 offset:0\n\t" \
  "ds_read_b128 v[204:207], v211 offset:16384\n\t" \
  "s_branch .Lfb00_%=\n\t" \
  ".Lp0_%=:\n\t" \
  "s_cmp_le_u32 s86, 5\n\t" \
  "s_cbranch_scc1 .Lf00_%=\n\t" \
  ".Lfb00_%=:\n\t" \
  "s_waitcnt lgkmcnt(6)\n\t" \
  "v_mfma_f32_16x16x32_bf16 v[128:131], v[0:3], v[176:179], 0\n\t" \
  "v_mfma_f32_16x16x32_bf16 v[132:135], v[4:7], v[176:179], 0\n\t" \
  "ds_read_b128 v[176:179], v208 offset:256\n\t" \
  "s_add_u32 m0, %[lw], 67584\n\t" \
  "v_mfma_f32_16x16x32_bf16 v[136:139], v[0:3], v[180:183], 0\n\t" \
  "v_mfma_f32_16x16x32_bf16 v[140:143], v[4:7], v[180:183], 0\n\t" \
  "ds_read_b128 v[180:183], v208 offset:16640\n\t" \
  "global_load_lds_dwordx4 v218, s[92:93] sc1\n\t" \
  "s_waitcnt lgkmcnt(6)\n\t" \
  "v_mfma_f32_16x16x32_bf16 v[128:131], v[8:11], v[184:187], v[128:131]\n\t" \
  "v_mfma_f32_16x16x32_bf16 v[132:135], v[12:15], v[184:187], v[132:135]\n\t" \
  "ds_read_b128 v[184:187], v209 offset:256\n\t" \
  "v_mfma_f32_16x16x32_bf16 v[136:139], v[8:11], v[188:191], v[136:139]\n\t" \
  "v_mfma_f32_16x16x32_bf16 v[140:143], v[12:15], v[188:191], v[140:143]\n\t" \
  "ds_read_b128 v[188:191], v209 offset:16640\n\t" \
  "ds_write_b128 v220, v[152:155] offset:0\n\t" \
  "ds_write_b128 v220, v[156:159] offset:1024\n\t" \
  "s_waitcnt lgkmcnt(8)\n\t" \
  "v_mfma_f32_16x16x32_bf16 v[128:131], v[16:19], v[192:195], v[128:131]\n\t" \
  "v_mfma_f32_16x16x32_bf16 v[132:135], v[20:23], v[192:195], v[132:135]\n\t" \
  "ds_read_b128 v[192:195], v210 offset:256\n\t" \
  "s_add_u32 m0, %[lw], 68608\n\t" \
  "v_mfma_f32_16x16x32_bf16 v[136:139], v[16:19], v[196:199], v[136:139]\n\t" \
  "v_mfma_f32_16x16x32_bf16 v[140:143], v[20:23], v[196:199], v[140:143]\n\t" \
  "ds_read_b128 v[196:199], v210 offset:16640\n\t" \
  "global_load_lds_dwordx4 v219, s[92:93] sc1\n\t" \
  "s_lshr_b32 s90, s87, 7\n\t" \
  "s_xor_b32 s90, s90, %[s0]\n\t" \
  "s_and_b32 s90, s90, 1\n\t" \
  "s_lshl1_add_u32 s90, s87, s90\n\t" \
  "s_lshl_b32 s90, s90, 13\n\t" \
  "s_cmp_lt_i32 s86, 6\n\t" \
  "s_cselect_b32 s90, 0x70000000, s90\n\t" \
  "s_waitcnt lgkmcnt(8)\n\t" \
  "v_mfma_f32_16x16x32_bf16 v[128:131], v[24:27], v[200:203], v[128:131]\n\t" \
  "v_mfma_f32_16x16x32_bf16 v[132:135], v[28:31], v[200:203], v[132:135]\n\t" \
  "ds_read_b128 v[200:203], v211 offset:256\n\t" \
  "v_mfma_f32_16x16x32_bf16 v[136:139], v[24:27], v[204:207], v[136:139]\n\t" \
  "v_mfma_f32_16x16x32_bf16 v[140:143], v[28:31], v[204:207], v[140:143]\n\t" \
  "ds_read_b128 v[204:207], v211 offset:16640\n\t" \
  "s_waitcnt lgkmcnt(4)\n\t" \
  "s_cmp_lt_u32 s86, 6\n\t" \
  "s_cbranch_scc1 .Lw00_%=\n\t" \
  "s_waitcnt vmcnt(4)\n\t" \
  "s_barrier\n\t" \
  ".Lg00_%=:\n\t" \
  "s_add_u32 s92, s92, 512\n\t" \
  "s_addc_u32 s93, s93, 0\n\t" \
  "v_mfma_f32_16x16x32_bf16 v[128:131], v[32:35], v[176:179], v[128:131]\n\t" \
  "v_mfma_f32_16x16x32_bf16 v[132:135], v[36:39], v[176:179], v[132:135]\n\t" \
  "ds_read_b128 v[176:179], v208 offset:32768\n\t" \
  "s_add_u32 m0, %[lw], 98304\n\t" \
  "v_mfma_f32_16x16x32_bf16 v[136:139], v[32:35], v[180:183], v[136:139]\n\t" \
  "v_mfma_f32_16x16x32_bf16 v[140:143], v[36:39], v[180:183], v[140:143]\n\t" \
  "ds_read_b128 v[180:183], v208 offset:49152\n\t" \
  "global_load_lds_dwordx4 v216, s[92:93] sc1\n\t" \
  "ds_read_b128 v[160:163], v221 offset:0\n\t" \
  "ds_read_b128 v[164:167], v221 offset:1024\n\t" \
  "v_mfma_f32_16x16x32_bf16 v[128:131], v[40:43], v[184:187], v[128:131]\n\t" \
  "v_mfma_f32_16x16x32_bf16 v[132:135], v[44:47], v[184:187], v[132:135]\n\t" \
  "ds_read_b128 v[184:187], v209 offset:32768\n\t" \
  "v_mfma_f32_16x16x32_bf16 v[136:139], v[40:43], v[188:191], v[136:139]\n\t" \
  "v_mfma_f32_16x16x32_bf16 v[140:143], v[44:47], v[188:191], v[140:143]\n\t" \
  "ds_read_b128 v[188:191], v209 offset:49152\n\t" \
  "s_waitcnt lgkmcnt(8)\n\t" \
  "v_mfma_f32_16x16x32_bf16 v[128:131], v[48:51], v[192:195], v[128:131]\n\t" \
  "v_mfma_f32_16x16x32_bf16 v[132:135], v[52:55], v[192:195], v[132:135]\n\t" \
  "ds_read_b128 v[192:195], v210 offset:32768\n\t" \
  "s_add_u32 m0, %[lw], 99328\n\t" \
  "v_mfma_f32_16x16x32_bf16 v[136:139], v[48:51], v[196:199], v[136:139]\n\t" \
  "v_mfma_f32_16x16x32_bf16 v[140:143], v[52:55], v[196:199], v[140:143]\n\t" \
  "ds_read_b128 v[196:199], v210 offset:49152\n\t" \
  "global_load_lds_dwordx4 v217, s[92:93] sc1\n\t" \
  "s_waitcnt lgkmcnt(4)\n\t" \
  "v_add_f32 v160, v160, v144\n\t" \
  "v_add_f32 v161, v161, v145\n\t" \
  "v_add_f32 v162, v162, v146\n\t" \
  "v_add_f32 v163, v163, v147\n\t" \
  "v_add_f32 v164, v164, v148\n\t" \
  "v_add_f32 v165, v165, v149\n\t" \
  "v_add_f32 v166, v166, v150\n\t" \
  "v_add_f32 v167, v167, v151\n\t" \
  "v_mfma_f32_16x16x32_bf16 v[128:131], v[56:59], v[200:203], v[128:131]\n\t" \
  "v_mfma_f32_16x16x32_bf16 v[132:135], v[60:63], v[200:203], v[132:135]\n\t" \
  "ds_read_b128 v[200:203], v211 offset:32768\n\t" \
  "v_mfma_f32_16x16x32_bf16 v[136:139], v[56:59], v[204:207], v[136:139]\n\t" \
  "v_mfma_f32_16x16x32_bf16 v[140:143], v[60:63], v[204:207], v[140:143]\n\t" \
  "ds_read_b128 v[204:207], v211 offset:49152\n\t" \
  "v_cvt_pk_bf16_f32 v168, v160, v161\n\t" \
  "v_cvt_pk_bf16_f32 v169, v162, v163\n\t" \
  "v_cvt_pk_bf16_f32 v170, v164, v165\n\t" \
  "v_cvt_pk_bf16_f32 v171, v166, v167\n\t" \
  "buffer_store_dwordx4 v[168:171], v172, %[rt], s90 offen sc1\n\t" \
  "v_mfma_f32_16x16x32_bf16 v[128:131], v[64:67], v[176:179], v[128:131]\n\t" \
  "v_mfma_f32_16x16x32_bf16 v[132:135], v[68:71], v[176:179], v[132:135]\n\t" \
  "ds_read_b128 v[176:179], v208 offset:33024\n\t" \
  "s_add_u32 m0, %[lw], 100352\n\t" \
  "v_mfma_f32_16x16x32_bf16 v[136:139], v[64:67], v[180:183], v[136:139]\n\t" \
  "v_mfma_f32_16x16x32_bf16 v[140:143], v[68:71], v[180:183], v[140:143]\n\t" \
  "ds_read_b128 v[180:183], v208 offset:49408\n\t" \
  "global_load_lds_dwordx4 v218, s[92:93] sc1\n\t" \
  "s_waitcnt lgkmcnt(6)\n\t" \
  "v_mfma_f32_16x16x32_bf16 v[128:131], v[72:75], v[184:187], v[128:131]\n\t" \
  "v_mfma_f32_16x16x32_bf16 v[132:135], v[76:79], v[184:187], v[132:135]\n\t" \
  "ds_read_b128 v[184:187], v209 offset:33024\n\t" \
  "v_mfma_f32_16x16x32_bf16 v[136:139], v[72:75], v[188:191], v[136:139]\n\t" \
  "v_mfma_f32_16x16x32_bf16 v[140:143], v[76:79], v[188:191], v[140:143]\n\t" \
  "ds_read_b128 v[188:191], v209 offset:49408\n\t" \
  "s_waitcnt lgkmcnt(6)\n\t" \
  "v_mfma_f32_16x16x32_bf16 v[128:131], v[80:83], v[192:195], v[128:131]\n\t" \
  "v_mfma_f32_16x16x32_bf16 v[132:135], v[84:87], v[192:195], v[132:135]\n\t" \
  "ds_read_b128 v[192:195], v210 offset:33024\n\t" \
  "s_add_u32 m0, %[lw], 101376\n\t" \
  "v_mfma_f32_16x16x32_bf16 v[136:139], v[80:83], v[196:199], v[136:139]\n\t" \
  "v_mfma_f32_16x16x32_bf16 v[140:143], v[84:87], v[196:199], v[140:143]\n\t" \
  "ds_read_b128 v[196:199], v210 offset:49408\n\t" \
  "global_load_lds_dwordx4 v219, s[92:93] sc1\n\t" \
  "s_waitcnt lgkmcnt(6)\n\t" \
  "v_mfma_f32_16x16x32_bf16 v[128:131], v[88:91], v[200:203], v[128:131]\n\t" \
  "v_mfma_f32_16x16x32_bf16 v[132:135], v[92:95], v[200:203], v[132:135]\n\t" \
  "ds_read_b128 v[200:203], v211 offset:33024\n\t" \
  "v_mfma_f32_16x16x32_bf16 v[136:139], v[88:91], v[204:207], v[136:139]\n\t" \
  "v_mfma_f32_16x16x32_bf16 v[140:143], v[92:95], v[204:207], v[140:143]\n\t" \
  "ds_read_b128 v[204:207], v211 offset:49408\n\t" \
  "s_cmp_lt_u32 s86, 8\n\t" \
  "s_cbranch_scc1 .Lw10_%=\n\t" \
  "s_waitcnt vmcnt(5)\n\t" \
  "s_barrier\n\t" \
  ".Lg10_%=:\n\t" \
  "s_add_u32 s92, s92, 65024\n\t" \
  "s_addc_u32 s93, s93, 0\n\t" \
  "s_waitcnt lgkmcnt(6)\n\t" \
  "v_mfma_f32_16x16x32_bf16 v[128:131], v[96:99], v[176:179], v[128:131]\n\t" \
  "v_mfma_f32_16x16x32_bf16 v[132:135], v[100:103], v[176:179], v[132:135]\n\t" \
  "ds_read_b128 v[176:179], v212 offset:0\n\t" \
  "s_add_u32 m0, %[lw], 0\n\t" \
  "v_mfma_f32_16x16x32_bf16 v[136:139], v[96:99], v[180:183], v[136:139]\n\t" \
  "v_mfma_f32_16x16x32_bf16 v[140:143], v[100:103], v[180:183], v[140:143]\n\t" \
  "ds_read_b128 v[180:183], v212 offset:16384\n\t" \
  "global_load_lds_dwordx4 v216, s[92:93] sc1\n\t" \
  "s_waitcnt lgkmcnt(6)\n\t" \
  "v_mfma_f32_16x16x32_bf16 v[128:131], v[104:107], v[184:187], v[128:131]\n\t" \
  "v_mfma_f32_16x16x32_bf16 v[132:135], v[108:111], v[184:187], v[132:135]\n\t" \
  "ds_read_b128 v[184:187], v213 offset:0\n\t" \
  "v_mfma_f32_16x16x32_bf16 v[136:139], v[104:107], v[188:191], v[136:139]\n\t" \
  "v_mfma_f32_16x16x32_bf16 v[140:143], v[108:111], v[188:191], v[140:143]\n\t" \
  "ds_read_b128 v[188:191], v213 offset:16384\n\t" \
  "s_waitcnt lgkmcnt(6)\n\t" \
  "v_mfma_f32_16x16x32_bf16 v[128:131], v[112:115], v[192:195], v[128:131]\n\t" \
  "v_mfma_f32_16x16x32_bf16 v[132:135], v[116:119], v[192:195], v[132:135]\n\t" \
  "ds_read_b128 v[192:195], v214 offset:0\n\t" \
  "s_add_u32 m0, %[lw], 1024\n\t" \
  "v_mfma_f32_16x16x32_bf16 v[136:139], v[112:115], v[196:199], v[136:139]\n\t" \
  "v_mfma_f32_16x16x32_bf16 v[140:143], v[116:119], v[196:199], v[140:143]\n\t" \
  "ds_read_b128 v[196:199], v214 offset:16384\n\t" \
  "global_load_lds_dwordx4 v217, s[92:93] sc1\n\t" \
  "s_waitcnt lgkmcnt(6)\n\t" \
  "v_mfma_f32_16x16x32_bf16 v[128:131], v[120:123], v[200:203], v[128:131]\n\t" \
  "v_mfma_f32_16x16x32_bf16 v[132:135], v[124:127], v[200:203], v[132:135]\n\t" \
  "ds_read_b128 v[200:203], v215 offset:0\n\t" \
  "v_mfma_f32_16x16x32_bf16 v[136:139], v[120:123], v[204:207], v[136:139]\n\t" \
  "v_mfma_f32_16x16x32_bf16 v[140:143], v[124:127], v[204:207], v[140:143]\n\t" \
  "ds_read_b128 v[204:207], v215 offset:16384\n\t" \
  "s_add_u32 s86, s86, 1\n\t" \
  "s_add_u32 s87, s87, 1\n\t" \
  "s_cmp_le_u32 s86, 5\n\t" \
  "s_cbranch_scc1 .Lf10_%=\n\t" \
  ".Lfb10_%=:\n\t" \
  "s_waitcnt lgkmcnt(6)\n\t" \
  "v_mfma_f32_16x16x32_bf16 v[144:147], v[0:3], v[176:179], 0\n\t" \
  "v_mfma_f32_16x16x32_bf16 v[148:151], v[4:7], v[176:179], 0\n\t" \
  "ds_read_b128 v[176:179], v212 offset:256\n\t" \
  "s_add_u32 m0, %[lw], 2048\n\t" \
  "v_mfma_f32_16x16x32_bf16 v[152:155], v[0:3], v[180:183], 0\n\t" \
  "v_mfma_f32_16x16x32_bf16 v[156:159], v[4:7], v[180:183], 0\n\t" \
  "ds_read_b128 v[180:183], v212 offset:16640\n\t" \
  "global_load_lds_dwordx4 v218, s[92:93] sc1\n\t" \
  "s_waitcnt lgkmcnt(6)\n\t" \
  "v_mfma_f32_16x16x32_bf16 v[144:147], v[8:11], v[184:187], v[144:147]\n\t" \
  "v_mfma_f32_16x16x32_bf16 v[148:151], v[12:15], v[184:187], v[148:151]\n\t" \
  "ds_read_b128 v[184:187], v213 offset:256\n\t" \
  "v_mfma_f32_16x16x32_bf16 v[152:155], v[8:11], v[188:191], v[152:155]\n\t" \
  "v_mfma_f32_16x16x32_bf16 v[156:159], v[12:15], v[188:191], v[156:159]\n\t" \
  "ds_read_b128 v[188:191], v213 offset:16640\n\t" \
  "ds_write_b128 v220, v[136:139] offset:0\n\t" \
  "ds_write_b128 v220, v[140:143] offset:1024\n\t" \
  "s_waitcnt lgkmcnt(8)\n\t" \
  "v_mfma_f32_16x16x32_bf16 v[144:147], v[16:19], v[192:195], v[144:147]\n\t" \
  "v_mfma_f32_16x16x32_bf16 v[148:151], v[20:23], v[192:195], v[148:151]\n\t" \
  "ds_read_b128 v[192:195], v214 offset:256\n\t" \
  "s_add_u32 m0, %[lw], 3072\n\t" \
  "v_mfma_f32_16x16x32_bf16 v[152:155], v[16:19], v[196:199], v[152:155]\n\t" \
  "v_mfma_f32_16x16x32_bf16 v[156:159], v[20:23], v[196:199], v[156:159]\n\t" \
  "ds_read_b128 v[196:199], v214 offset:16640\n\t" \
  "global_load_lds_dwordx4 v219, s[92:93] sc1\n\t" \
  "s_lshr_b32 s90, s87, 7\n\t" \
  "s_xor_b32 s90, s90, %[s0]\n\t" \
  "s_and_b32 s90, s90, 1\n\t" \
  "s_lshl1_add_u32 s90, s87, s90\n\t" \
  "s_lshl_b32 s90, s90, 13\n\t" \
  "s_cmp_lt_i32 s86, 6\n\t" \
  "s_cselect_b32 s90, 0x70000000, s90\n\t" \
  "s_waitcnt lgkmcnt(8)\n\t" \
  "v_mfma_f32_16x16x32_bf16 v[144:147], v[24:27], v[200:203], v[144:147]\n\t" \
  "v_mfma_f32_16x16x32_bf16 v[148:151], v[28:31], v[200:203], v[148:151]\n\t" \
  "ds_read_b128 v[200:203], v215 offset:256\n\t" \
  "v_mfma_f32_16x16x32_bf16 v[152:155], v[24:27], v[204:207], v[152:155]\n\t" \
  "v_mfma_f32_16x16x32_bf16 v[156:159], v[28:31], v[204:207], v[156:159]\n\t" \
  "ds_read_b128 v[204:207], v215 offset:16640\n\t" \
  "s_waitcnt lgkmcnt(4)\n\t" \
  "s_cmp_lt_u32 s86, 6\n\t" \
  "s_cbranch_scc1 .Lw20_%=\n\t" \
  "s_waitcnt vmcnt(4)\n\t" \
  "s_barrier\n\t" \
  ".Lg20_%=:\n\t" \
  "s_add_u32 s92, s92, 512\n\t" \
  "s_addc_u32 s93, s93, 0\n\t" \
  "v_mfma_f32_16x16x32_bf16 v[144:147], v[32:35], v[176:179], v[144:147]\n\t" \
  "v_mfma_f32_16x16x32_bf16 v[148:151], v[36:39], v[176:179], v[148:151]\n\t" \
  "ds_read_b128 v[176:179], v212 offset:32768\n\t" \
  "s_add_u32 m0, %[lw], 32768\n\t" \
  "v_mfma_f32_16x16x32_bf16 v[152:155], v[32:35], v[180:183], v[152:155]\n\t" \
  "v_mfma_f32_16x16x32_bf16 v[156:159], v[36:39], v[180:183], v[156:159]\n\t" \
  "ds_read_b128 v[180:183], v212 offset:49152\n\t" \
  "global_load_lds_dwordx4 v216, s[92:93] sc1\n\t" \
  "ds_read_b128 v[160:163], v221 offset:0\n\t" \
  "ds_read_b128 v[164:167], v221 offset:1024\n\t" \
  "v_mfma_f32_16x16x32_bf16 v[144:147], v[40:43], v[184:187], v[144:147]\n\t" \
  "v_mfma_f32_16x16x32_bf16 v[148:151], v[44:47], v[184:187], v[148:151]\n\t" \
  "ds_read_b128 v[184:187], v213 offset:32768\n\t" \
  "v_mfma_f32_16x16x32_bf16 v[152:155], v[40:43], v[188:191], v[152:155]\n\t" \
  "v_mfma_f32_16x16x32_bf16 v[156:159], v[44:47], v[188:191], v[156:159]\n\t" \
  "ds_read_b128 v[188:191], v213 offset:49152\n\t" \
  "s_waitcnt lgkmcnt(8)\n\t" \
  "v_mfma_f32_16x16x32_bf16 v[144:147], v[48:51], v[192:195], v[144:147]\n\t" \
  "v_mfma_f32_16x16x32_bf16 v[148:151], v[52:55], v[192:195], v[148:151]\n\t" \
  "ds_read_b128 v[192:195], v214 offset:32768\n\t" \
  "s_add_u32 m0, %[lw], 33792\n\t" \
  "v_mfma_f32_16x16x32_bf16 v[152:155], v[48:51], v[196:199], v[152:155]\n\t" \
  "v_mfma_f32_16x16x32_bf16 v[156:159], v[52:55], v[196:199], v[156:159]\n\t" \
  "ds_read_b128 v[196:199], v214 offset:49152\n\t" \
  "global_load_lds_dwordx4 v217, s[92:93] sc1\n\t" \
  "s_waitcnt lgkmcnt(4)\n\t" \
  "v_add_f32 v160, v160, v128\n\t" \
  "v_add_f32 v161, v161, v129\n\t" \
  "v_add_f32 v162, v162, v130\n\t" \
  "v_add_f32 v163, v163, v131\n\t" \
  "v_add_f32 v164, v164, v132\n\t" \
  "v_add_f32 v165, v165, v133\n\t" \
  "v_add_f32 v166, v166, v134\n\t" \
  "v_add_f32 v167, v167, v135\n\t" \
  "v_mfma_f32_16x16x32_bf16 v[144:147], v[56:59], v[200:203], v[144:147]\n\t" \
  "v_mfma_f32_16x16x32_bf16 v[148:151], v[60:63], v[200:203], v[148:151]\n\t" \
  "ds_read_b128 v[200:203], v215 offset:32768\n\t" \
  "v_mfma_f32_16x16x32_bf16 v[152:155], v[56:59], v[204:207], v[152:155]\n\t" \
  "v_mfma_f32_16x16x32_bf16 v[156:159], v[60:63], v[204:207], v[156:159]\n\t" \
  "ds_read_b128 v[204:207], v215 offset:49152\n\t" \
  "v_cvt_pk_bf16_f32 v168, v160, v161\n\t" \
  "v_cvt_pk_bf16_f32 v169, v162, v163\n\t" \
  "v_cvt_pk_bf16_f32 v170, v164, v165\n\t" \
  "v_cvt_pk_bf16_f32 v171, v166, v167\n\t" \
  "buffer_store_dwordx4 v[168:171], v172, %[rt], s90 offen sc1\n\t" \
  "v_mfma_f32_16x16x32_bf16 v[144:147], v[64:67], v[176:179], v[144:147]\n\t" \
  "v_mfma_f32_16x16x32_bf16 v[148:151], v[68:71], v[176:179], v[148:151]\n\t" \
  "ds_read_b128 v[176:179], v212 offset:33024\n\t" \
  "s_add_u32 m0, %[lw], 34816\n\t" \
  "v_mfma_f32_16x16x32_bf16 v[152:155], v[64:67], v[180:183], v[152:155]\n\t" \
  "v_mfma_f32_16x16x32_bf16 v[156:159], v[68:71], v[180:183], v[156:159]\n\t" \
  "ds_read_b128 v[180:183], v212 offset:49408\n\t" \
  "global_load_lds_dwordx4 v218, s[92:93] sc1\n\t" \
  "s_waitcnt lgkmcnt(6)\n\t" \
  "v_mfma_f32_16x16x32_bf16 v[144:147], v[72:75], v[184:187], v[144:147]\n\t" \
  "v_mfma_f32_16x16x32_bf16 v[148:151], v[76:79], v[184:187], v[148:151]\n\t" \
  "ds_read_b128 v[184:187], v213 offset:33024\n\t" \
  "v_mfma_f32_16x16x32_bf16 v[152:155], v[72:75], v[188:191], v[152:155]\n\t" \
  "v_mfma_f32_16x16x32_bf16 v[156:159], v[76:79], v[188:191], v[156:159]\n\t" \
  "ds_read_b128 v[188:191], v213 offset:49408\n\t" \
  "s_waitcnt lgkmcnt(6)\n\t" \
  "v_mfma_f32_16x16x32_bf16 v[144:147], v[80:83], v[192:195], v[144:147]\n\t" \
  "v_mfma_f32_16x16x32_bf16 v[148:151], v[84:87], v[192:195], v[148:151]\n\t" \
  "ds_read_b128 v[192:195], v214 offset:33024\n\t" \
  "s_add_u32 m0, %[lw], 35840\n\t" \
  "v_mfma_f32_16x16x32_bf16 v[152:155], v[80:83], v[196:199], v[152:155]\n\t" \
  "v_mfma_f32_16x16x32_bf16 v[156:159], v[84:87], v[196:199], v[156:159]\n\t" \
  "ds_read_b128 v[196:199], v214 offset:49408\n\t" \
  "global_load_lds_dwordx4 v219, s[92:93] sc1\n\t" \
  "s_waitcnt lgkmcnt(6)\n\t" \
  "v_mfma_f32_16x16x32_bf16 v[144:147], v[88:91], v[200:203], v[144:147]\n\t" \
  "v_mfma_f32_16x16x32_bf16 v[148:151], v[92:95], v[200:203], v[148:151]\n\t" \
  "ds_read_b128 v[200:203], v215 offset:33024\n\t" \
  "v_mfma_f32_16x16x32_bf16 v[152:155], v[88:91], v[204:207], v[152:155]\n\t" \
  "v_mfma_f32_16x16x32_bf16 v[156:159], v[92:95], v[204:207], v[156:159]\n\t" \
  "ds_read_b128 v[204:207], v215 offset:49408\n\t" \
  "s_cmp_lt_u32 s86, 8\n\t" \
  "s_cbranch_scc1 .Lw30_%=\n\t" \
  "s_waitcnt vmcnt(5)\n\t" \
  "s_barrier\n\t" \
  ".Lg30_%=:\n\t" \
  "s_add_u32 s92, s92, 65024\n\t" \
  "s_addc_u32 s93, s93, 0\n\t" \
  "s_waitcnt lgkmcnt(6)\n\t" \
  "v_mfma_f32_16x16x32_bf16 v[144:147], v[96:99], v[176:179], v[144:147]\n\t" \
  "v_mfma_f32_16x16x32_bf16 v[148:151], v[100:103], v[176:179], v[148:151]\n\t" \
  "ds_read_b128 v[176:179], v208 offset:0\n\t" \
  "s_add_u32 m0, %[lw], 65536\n\t" \
  "v_mfma_f32_16x16x32_bf16 v[152:155], v[96:99], v[180:183], v[152:155]\n\t" \
  "v_mfma_f32_16x16x32_bf16 v[156:159], v[100:103], v[180:183], v[156:159]\n\t" \
  "ds_read_b128 v[180:183], v208 offset:16384\n\t" \
  "global_load_lds_dwordx4 v216, s[92:93] sc1\n\t" \
  "s_waitcnt lgkmcnt(6)\n\t" \
  "v_mfma_f32_16x16x32_bf16 v[144:147], v[104:107], v[184:187], v[144:147]\n\t" \
  "v_mfma_f32_16x16x32_bf16 v[148:151], v[108:111], v[184:187], v[148:151]\n\t" \
  "ds_read_b128 v[184:187], v209 offset:0\n\t" \
  "v_mfma_f32_16x16x32_bf16 v[152:155], v[104:107], v[188:191], v[152:155]\n\t" \
  "v_mfma_f32_16x16x32_bf16 v[156:159], v[108:111], v[188:191], v[156:159]\n\t" \
  "ds_read_b128 v[188:191], v209 offset:16384\n\t" \
  "s_waitcnt lgkmcnt(6)\n\t" \
  "v_mfma_f32_16x16x32_bf16 v[144:147], v[112:115], v[192:195], v[144:147]\n\t" \
  "v_mfma_f32_16x16x32_bf16 v[148:151], v[116:119], v[192:195], v[148:151]\n\t" \
  "ds_read_b128 v[192:195], v210 offset:0\n\t" \
  "s_add_u32 m0, %[lw], 66560\n\t" \
  "v_mfma_f32_16x16x32_bf16 v[152:155], v[112:115], v[196:199], v[152:155]\n\t" \
  "v_mfma_f32_16x16x32_bf16 v[156:159], v[116:119], v[196:199], v[156:159]\n\t" \
  "ds_read_b128 v[196:199], v210 offset:16384\n\t" \
  "global_load_lds_dwordx4 v217, s[92:93] sc1\n\t" \
  "s_waitcnt lgkmcnt(6)\n\t" \
  "v_mfma_f32_16x16x32_bf16 v[144:147], v[120:123], v[200:203], v[144:147]\n\t" \
  "v_mfma_f32_16x16x32_bf16 v[148:151], v[124:127], v[200:203], v[148:151]\n\t" \
  "ds_read_b128 v[200:203], v211 offset:0\n\t" \
  "v_mfma_f32_16x16x32_bf16 v[152:155], v[120:123], v[204:207], v[152:155]\n\t" \
  "v_mfma_f32_16x16x32_bf16 v[156:159], v[124:127], v[204:207], v[156:159]\n\t" \
  "ds_read_b128 v[204:207], v211 offset:16384\n\t" \
  "s_add_u32 s86, s86, 1\n\t" \
  "s_add_u32 s87, s87, 1\n\t" \
  "s_cmp_lt_u32 s86, %[end]\n\t" \
  "s_cbranch_scc1 .Lp0_%=\n\t" \
  "s_branch .Lx0_%=\n\t" \
  ".Lf00_%=:\n\t" \
  "s_nop 7\n\t" \
  "ds_write_b128 v220, v[152:155] offset:0\n\t" \
  "ds_write_b128 v220, v[156:159] offset:1024\n\t" \
  "s_lshr_b32 s90, s87, 7\n\t" \
  "s_xor_b32 s90, s90, %[s0]\n\t" \
  "s_and_b32 s90, s90, 1\n\t" \
  "s_lshl1_add_u32 s90, s87, s90\n\t" \
  "s_lshl_b32 s90, s90, 13\n\t" \
  "s_cmp_lt_i32 s86, 1\n\t" \
  "s_cselect_b32 s90, 0x70000000, s90\n\t" \
  "s_waitcnt lgkmcnt(0)\n\t" \
  "s_barrier\n\t" \
  "ds_read_b128 v[160:163], v221 offset:0\n\t" \
  "ds_read_b128 v[164:167], v221 offset:1024\n\t" \
  "s_waitcnt lgkmcnt(0)\n\t" \
  "v_add_f32 v160, v160, v144\n\t" \
  "v_add_f32 v161, v161, v145\n\t" \
  "v_add_f32 v162, v162, v146\n\t" \
  "v_add_f32 v163, v163, v147\n\t" \
  "v_add_f32 v164, v164, v148\n\t" \
  "v_add_f32 v165, v165, v149\n\t" \
  "v_add_f32 v166, v166, v150\n\t" \
  "v_add_f32 v167, v167, v151\n\t" \
  "v_cvt_pk_bf16_f32 v168, v160, v161\n\t" \
  "v_cvt_pk_bf16_f32 v169, v162, v163\n\t" \
  "v_cvt_pk_bf16_f32 v170, v164, v165\n\t" \
  "v_cvt_pk_bf16_f32 v171, v166, v167\n\t" \
  "buffer_store_dwordx4 v[168:171], v172, %[rt], s90 offen sc1\n\t" \
  "s_branch .Lfb00_%=\n\t" \
  ".Lf10_%=:\n\t" \
  "s_nop 7\n\t" \
  "ds_write_b128 v220, v[136:139] offset:0\n\t" \
  "ds_write_b128 v220, v[140:143] offset:1024\n\t" \
  "s_lshr_b32 s90, s87, 7\n\t" \
  "s_xor_b32 s90, s90, %[s0]\n\t" \
  "s_and_b32 s90, s90, 1\n\t" \
  "s_lshl1_add_u32 s90, s87, s90\n\t" \
  "s_lshl_b32 s90, s90, 13\n\t" \
  "s_cmp_lt_i32 s86, 1\n\t" \
  "s_cselect_b32 s90, 0x70000000, s90\n\t" \
  "s_waitcnt lgkmcnt(0)\n\t" \
  "s_barrier\n\t" \
  "ds_read_b128 v[160:163], v221 offset:0\n\t" \
  "ds_read_b128 v[164:167], v221 offset:1024\n\t" \
  "s_waitcnt lgkmcnt(0)\n\t" \
  "v_add_f32 v160, v160, v128\n\t" \
  "v_add_f32 v161, v161, v129\n\t" \
  "v_add_f32 v162, v162, v130\n\t" \
  "v_add_f32 v163, v163, v131\n\t" \
  "v_add_f32 v164, v164, v132\n\t" \
  "v_add_f32 v165, v165, v133\n\t" \
  "v_add_f32 v166, v166, v134\n\t" \
  "v_add_f32 v167, v167, v135\n\t" \
  "v_cvt_pk_bf16_f32 v168, v160, v161\n\t" \
  "v_cvt_pk_bf16_f32 v169, v162, v163\n\t" \
  "v_cvt_pk_bf16_f32 v170, v164, v165\n\t" \
  "v_cvt_pk_bf16_f32 v171, v166, v167\n\t" \
  "buffer_store_dwordx4 v[168:171], v172, %[rt], s90 offen sc1\n\t" \
  "s_branch .Lfb10_%=\n\t" \
  ".Lw00_%=:\n\t" \
  "s_waitcnt vmcnt(2)\n\t" \
  "s_barrier\n\t" \
  "s_branch .Lg00_%=\n\t" \
  ".Lw10_%=:\n\t" \
  "s_waitcnt vmcnt(2)\n\t" \
  "s_barrier\n\t" \
  "s_branch .Lg10_%=\n\t" \
  ".Lw20_%=:\n\t" \
  "s_waitcnt vmcnt(2)\n\t" \
  "s_barrier\n\t" \
  "s_branch .Lg20_%=\n\t" \
  ".Lw30_%=:\n\t" \
  "s_waitcnt vmcnt(2)\n\t" \
  "s_barrier\n\t" \
  "s_branch .Lg30_%=\n\t" \
  ".Lx0_%=:\n\t" \
  "s_waitcnt lgkmcnt(0)\n\t" \
  "s_setprio 0\n\t" \
  "s_mov_b32 m0, s94\n\t" \
  "s_branch .Le_%=\n\t" \
  ".Lk_%=:\n\t" \
  "s_mov_b32 s94, m0\n\t" \
  "s_mov_b64 s[92:93], %[x0]\n\t" \
  "s_mov_b32 s86, %[g0]\n\t" \
  "s_add_u32 s87, s86, %[t0]\n\t" \
  "s_sub_i32 s88, s86, 3\n\t" \
  "ds_read_b128 v[176:179], v208 offset:0\n\t" \
  "ds_read_b128 v[180:183], v208 offset:16384\n\t" \
  "ds_read_b128 v[184:187], v209 offset:0\n\t" \
  "ds_read_b128 v[188:191], v209 offset:16384\n\t" \
  "ds_read_b128 v[192:195], v210 offset:0\n\t" \
  "ds_read_b128 v[196:199], v210 offset:16384\n\t" \
  "ds_read_b128 v[200:203], v211 offset:0\n\t" \
  "ds_read_b128 v[204:207], v211 offset:16384\n\t" \
  "s_branch .Lfb01_%=\n\t" \
  ".Lp1_%=:\n\t" \
  "s_cmp_le_u32 s86, 5\n\t" \
  "s_cbranch_scc1 .Lf01_%=\n\t" \
  ".Lfb01_%=:\n\t" \
  "s_waitcnt lgkmcnt(6)\n\t" \
  "v_mfma_f32_16x16x32_bf16 v[128:131], v[0:3], v[176:179], 0\n\t" \
  "v_mfma_f32_16x16x32_bf16 v[132:135], v[4:7], v[176:179], 0\n\t" \
  "ds_read_b128 v[176:179], v208 offset:256\n\t" \
  "v_mfma_f32_16x16x32_bf16 v[136:139], v[0:3], v[180:183], 0\n\t" \
  "v_mfma_f32_16x16x32_bf16 v[140:143], v[4:7], v[180:183], 0\n\t" \
  "ds_read_b128 v[180:183], v208 offset:16640\n\t" \
  "s_waitcnt lgkmcnt(6)\n\t" \
  "v_mfma_f32_16x16x32_bf16 v[128:131], v[8:11], v[184:187], v[128:131]\n\t" \
  "v_mfma_f32_16x16x32_bf16 v[132:135], v[12:15], v[184:187], v[132:135]\n\t" \
  "ds_read_b128 v[184:187], v209 offset:256\n\t" \
  "s_add_u32 m0, %[lw], 67584\n\t" \
  "v_mfma_f32_16x16x32_bf16 v[136:139], v[8:11], v[188:191], v[136:139]\n\t" \
  "v_mfma_f32_16x16x32_bf16 v[140:143], v[12:15], v[188:191], v[140:143]\n\t" \
  "ds_read_b128 v[188:191], v209 offset:16640\n\t" \
  "global_load_lds_dwordx4 v218, s[92:93] sc1\n\t" \
  "ds_write_b128 v220, v[152:155] offset:0\n\t" \
  "ds_write_b128 v220, v[156:159] offset:1024\n\t" \
  "s_waitcnt lgkmcnt(8)\n\t" \
  "v_mfma_f32_16x16x32_bf16 v[128:131], v[16:19], v[192:195], v[128:131]\n\t" \
  "v_mfma_f32_16x16x32_bf16 v[132:135], v[20:23], v[192:195], v[132:135]\n\t" \
  "ds_read_b128 v[192:195], v210 offset:256\n\t" \
  "v_mfma_f32_16x16x32_bf16 v[136:139], v[16:19], v[196:199], v[136:139]\n\t" \
  "v_mfma_f32_16x16x32_bf16 v[140:143], v[20:23], v[196:199], v[140:143]\n\t" \
  "ds_read_b128 v[196:199], v210 offset:16640\n\t" \
  "s_lshr_b32 s90, s87, 7\n\t" \
  "s_xor_b32 s90, s90, %[s0]\n\t" \
  "s_and_b32 s90, s90, 1\n\t" \
  "s_lshl1_add_u32 s90, s87, s90\n\t" \
  "s_lshl_b32 s90, s90, 13\n\t" \
  "s_cmp_lt_i32 s86, 6\n\t" \
  "s_cselect_b32 s90, 0x70000000, s90\n\t" \
  "s_waitcnt lgkmcnt(8)\n\t" \
  "v_mfma_f32_16x16x32_bf16 v[128:131], v[24:27], v[200:203], v[128:131]\n\t" \
  "v_mfma_f32_16x16x32_bf16 v[132:135], v[28:31], v[200:203], v[132:135]\n\t" \
  "ds_read_b128 v[200:203], v211 offset:256\n\t" \
  "s_add_u32 m0, %[lw], 68608\n\t" \
  "v_mfma_f32_16x16x32_bf16 v[136:139], v[24:27], v[204:207], v[136:139]\n\t" \
  "v_mfma_f32_16x16x32_bf16 v[140:143], v[28:31], v[204:207], v[140:143]\n\t" \
  "ds_read_b128 v[204:207], v211 offset:16640\n\t" \
  "global_load_lds_dwordx4 v219, s[92:93] sc1\n\t" \
  "s_waitcnt lgkmcnt(4)\n\t" \
  "s_cmp_lt_u32 s86, 6\n\t" \
  "s_cbranch_scc1 .Lw01_%=\n\t" \
  "s_waitcnt vmcnt(4)\n\t" \
  "s_barrier\n\t" \
  ".Lg01_%=:\n\t" \
  "s_add_u32 s92, s92, 512\n\t" \
  "s_addc_u32 s93, s93, 0\n\t" \
  "v_mfma_f32_16x16x32_bf16 v[128:131], v[32:35], v[176:179], v[128:131]\n\t" \
  "v_mfma_f32_16x16x32_bf16 v[132:135], v[36:39], v[176:179], v[132:135]\n\t" \
  "ds_read_b128 v[176:179], v208 offset:32768\n\t" \
  "v_mfma_f32_16x16x32_bf16 v[136:139], v[32:35], v[180:183], v[136:139]\n\t" \
  "v_mfma_f32_16x16x32_bf16 v[140:143], v[36:39], v[180:183], v[140:143]\n\t" \
  "ds_read_b128 v[180:183], v208 offset:49152\n\t" \
  "ds_read_b128 v[160:163], v221 offset:0\n\t" \
  "ds_read_b128 v[164:167], v221 offset:1024\n\t" \
  "s_sub_i32 s91, s86, 2\n\t" \
  "s_max_i32 s88, s88, s91\n\t" \
  "v_mov_b32 v173, s88\n\t" \
  "buffer_store_dword v173, v174, %[rs], 0 offen sc1\n\t" \
  "v_mfma_f32_16x16x32_bf16 v[128:131], v[40:43], v[184:187], v[128:131]\n\t" \
  "v_mfma_f32_16x16x32_bf16 v[132:135], v[44:47], v[184:187], v[132:135]\n\t" \
  "ds_read_b128 v[184:187], v209 offset:32768\n\t" \
  "s_add_u32 m0, %[lw], 98304\n\t" \
  "v_mfma_f32_16x16x32_bf16 v[136:139], v[40:43], v[188:191], v[136:139]\n\t" \
  "v_mfma_f32_16x16x32_bf16 v[140:143], v[44:47], v[188:191], v[140:143]\n\t" \
  "ds_read_b128 v[188:191], v209 offset:49152\n\t" \
  "global_load_lds_dwordx4 v216, s[92:93] sc1\n\t" \
  "s_waitcnt lgkmcnt(8)\n\t" \
  "v_mfma_f32_16x16x32_bf16 v[128:131], v[48:51], v[192:195], v[128:131]\n\t" \
  "v_mfma_f32_16x16x32_bf16 v[132:135], v[52:55], v[192:195], v[132:135]\n\t" \
  "ds_read_b128 v[192:195], v210 offset:32768\n\t" \
  "v_mfma_f32_16x16x32_bf16 v[136:139], v[48:51], v[196:199], v[136:139]\n\t" \
  "v_mfma_f32_16x16x32_bf16 v[140:143], v[52:55], v[196:199], v[140:143]\n\t" \
  "ds_read_b128 v[196:199], v210 offset:49152\n\t" \
  "s_waitcnt lgkmcnt(4)\n\t" \
  "v_add_f32 v160, v160, v144\n\t" \
  "v_add_f32 v161, v161, v145\n\t" \
  "v_add_f32 v162, v162, v146\n\t" \
  "v_add_f32 v163, v163, v147\n\t" \
  "v_add_f32 v164, v164, v148\n\t" \
  "v_add_f32 v165, v165, v149\n\t" \
  "v_add_f32 v166, v166, v150\n\t" \
  "v_add_f32 v167, v167, v151\n\t" \
  "v_mfma_f32_16x16x32_bf16 v[128:131], v[56:59], v[200:203], v[128:131]\n\t" \
  "v_mfma_f32_16x16x32_bf16 v[132:135], v[60:63], v[200:203], v[132:135]\n\t" \
  "ds_read_b128 v[200:203], v211 offset:32768\n\t" \
  "s_add_u32 m0, %[lw], 99328\n\t" \
  "v_mfma_f32_16x16x32_bf16 v[136:139], v[56:59], v[204:207], v[136:139]\n\t" \
  "v_mfma_f32_16x16x32_bf16 v[140:143], v[60:63], v[204:207], v[140:143]\n\t" \
  "ds_read_b128 v[204:207], v211 offset:49152\n\t" \
  "global_load_lds_dwordx4 v217, s[92:93] sc1\n\t" \
  "v_cvt_pk_bf16_f32 v168, v160, v161\n\t" \
  "v_cvt_pk_bf16_f32 v169, v162, v163\n\t" \
  "v_cvt_pk_bf16_f32 v170, v164, v165\n\t" \
  "v_cvt_pk_bf16_f32 v171, v166, v167\n\t" \
  "buffer_store_dwordx4 v[168:171], v172, %[rt], s90 offen sc1\n\t" \
  "v_mfma_f32_16x16x32_bf16 v[128:131], v[64:67], v[176:179], v[128:131]\n\t" \
  "v_mfma_f32_16x16x32_bf16 v[132:135], v[68:71], v[176:179], v[132:135]\n\t" \
  "ds_read_b128 v[176:179], v208 offset:33024\n\t" \
  "v_mfma_f32_16x16x32_bf16 v[136:139], v[64:67], v[180:183], v[136:139]\n\t" \
  "v_mfma_f32_16x16x32_bf16 v[140:143], v[68:71], v[180:183], v[140:143]\n\t" \
  "ds_read_b128 v[180:183], v208 offset:49408\n\t" \
  "s_waitcnt lgkmcnt(6)\n\t" \
  "v_mfma_f32_16x16x32_bf16 v[128:131], v[72:75], v[184:187], v[128:131]\n\t" \
  "v_mfma_f32_16x16x32_bf16 v[132:135], v[76:79], v[184:187], v[132:135]\n\t" \
  "ds_read_b128 v[184:187], v209 offset:33024\n\t" \
  "s_add_u32 m0, %[lw], 100352\n\t" \
  "v_mfma_f32_16x16x32_bf16 v[136:139], v[72:75], v[188:191], v[136:139]\n\t" \
  "v_mfma_f32_16x16x32_bf16 v[140:143], v[76:79], v[188:191], v[140:143]\n\t" \
  "ds_read_b128 v[188:191], v209 offset:49408\n\t" \
  "global_load_lds_dwordx4 v218, s[92:93] sc1\n\t" \
  "s_waitcnt lgkmcnt(6)\n\t" \
  "v_mfma_f32_16x16x32_bf16 v[128:131], v[80:83], v[192:195], v[128:131]\n\t" \
  "v_mfma_f32_16x16x32_bf16 v[132:135], v[84:87], v[192:195], v[132:135]\n\t" \
  "ds_read_b128 v[192:195], v210 offset:33024\n\t" \
  "v_mfma_f32_16x16x32_bf16 v[136:139], v[80:83], v[196:199], v[136:139]\n\t" \
  "v_mfma_f32_16x16x32_bf16 v[140:143], v[84:87], v[196:199], v[140:143]\n\t" \
  "ds_read_b128 v[196:199], v210 offset:49408\n\t" \
  "s_waitcnt lgkmcnt(6)\n\t" \
  "v_mfma_f32_16x16x32_bf16 v[128:131], v[88:91], v[200:203], v[128:131]\n\t" \
  "v_mfma_f32_16x16x32_bf16 v[132:135], v[92:95], v[200:203], v[132:135]\n\t" \
  "ds_read_b128 v[200:203], v211 offset:33024\n\t" \
  "s_add_u32 m0, %[lw], 101376\n\t" \
  "v_mfma_f32_16x16x32_bf16 v[136:139], v[88:91], v[204:207], v[136:139]\n\t" \
  "v_mfma_f32_16x16x32_bf16 v[140:143], v[92:95], v[204:207], v[140:143]\n\t" \
  "ds_read_b128 v[204:207], v211 offset:49408\n\t" \
  "global_load_lds_dwordx4 v219, s[92:93] sc1\n\t" \
  "s_cmp_lt_u32 s86, 8\n\t" \
  "s_cbranch_scc1 .Lw11_%=\n\t" \
  "s_waitcnt vmcnt(6)\n\t" \
  "s_barrier\n\t" \
  ".Lg11_%=:\n\t" \
  "s_add_u32 s92, s92, 65024\n\t" \
  "s_addc_u32 s93, s93, 0\n\t" \
  "s_waitcnt lgkmcnt(6)\n\t" \
  "v_mfma_f32_16x16x32_bf16 v[128:131], v[96:99], v[176:179], v[128:131]\n\t" \
  "v_mfma_f32_16x16x32_bf16 v[132:135], v[100:103], v[176:179], v[132:135]\n\t" \
  "ds_read_b128 v[176:179], v212 offset:0\n\t" \
  "v_mfma_f32_16x16x32_bf16 v[136:139], v[96:99], v[180:183], v[136:139]\n\t" \
  "v_mfma_f32_16x16x32_bf16 v[140:143], v[100:103], v[180:183], v[140:143]\n\t" \
  "ds_read_b128 v[180:183], v212 offset:16384\n\t" \
  "s_waitcnt lgkmcnt(6)\n\t" \
  "v_mfma_f32_16x16x32_bf16 v[128:131], v[104:107], v[184:187], v[128:131]\n\t" \
  "v_mfma_f32_16x16x32_bf16 v[132:135], v[108:111], v[184:187], v[132:135]\n\t" \
  "ds_read_b128 v[184:187], v213 offset:0\n\t" \
  "s_add_u32 m0, %[lw], 0\n\t" \
  "v_mfma_f32_16x16x32_bf16 v[136:139], v[104:107], v[188:191], v[136:139]\n\t" \
  "v_mfma_f32_16x16x32_bf16 v[140:143], v[108:111], v[188:191], v[140:143]\n\t" \
  "ds_read_b128 v[188:191], v213 offset:16384\n\t" \
  "global_load_lds_dwordx4 v216, s[92:93] sc1\n\t" \
  "s_waitcnt lgkmcnt(6)\n\t" \
  "v_mfma_f32_16x16x32_bf16 v[128:131], v[112:115], v[192:195], v[128:131]\n\t" \
  "v_mfma_f32_16x16x32_bf16 v[132:135], v[116:119], v[192:195], v[132:135]\n\t" \
  "ds_read_b128 v[192:195], v214 offset:0\n\t" \
  "v_mfma_f32_16x16x32_bf16 v[136:139], v[112:115], v[196:199], v[136:139]\n\t" \
  "v_mfma_f32_16x16x32_bf16 v[140:143], v[116:119], v[196:199], v[140:143]\n\t" \
  "ds_read_b128 v[196:199], v214 offset:16384\n\t" \
  "s_waitcnt lgkmcnt(6)\n\t" \
  "v_mfma_f32_16x16x32_bf16 v[128:131], v[120:123], v[200:203], v[128:131]\n\t" \
  "v_mfma_f32_16x16x32_bf16 v[132:135], v[124:127], v[200:203], v[132:135]\n\t" \
  "ds_read_b128 v[200:203], v215 offset:0\n\t" \
  "s_add_u32 m0, %[lw], 1024\n\t" \
  "v_mfma_f32_16x16x32_bf16 v[136:139], v[120:123], v[204:207], v[136:139]\n\t" \
  "v_mfma_f32_16x16x32_bf16 v[140:143], v[124:127], v[204:207], v[140:143]\n\t" \
  "ds_read_b128 v[204:207], v215 offset:16384\n\t" \
  "global_load_lds_dwordx4 v217, s[92:93] sc1\n\t" \
  "s_add_u32 s86, s86, 1\n\t" \
  "s_add_u32 s87, s87, 1\n\t" \
  "s_cmp_le_u32 s86, 5\n\t" \
  "s_cbranch_scc1 .Lf11_%=\n\t" \
  ".Lfb11_%=:\n\t" \
  "s_waitcnt lgkmcnt(6)\n\t" \
  "v_mfma_f32_16x16x32_bf16 v[144:147], v[0:3], v[176:179], 0\n\t" \
  "v_mfma_f32_16x16x32_bf16 v[148:151], v[4:7], v[176:179], 0\n\t" \
  "ds_read_b128 v[176:179], v212 offset:256\n\t" \
  "v_mfma_f32_16x16x32_bf16 v[152:155], v[0:3], v[180:183], 0\n\t" \
  "v_mfma_f32_16x16x32_bf16 v[156:159], v[4:7], v[180:183], 0\n\t" \
  "ds_read_b128 v[180:183], v212 offset:16640\n\t" \
  "s_waitcnt lgkmcnt(6)\n\t" \
  "v_mfma_f32_16x16x32_bf16 v[144:147], v[8:11], v[184:187], v[144:147]\n\t" \
  "v_mfma_f32_16x16x32_bf16 v[148:151], v[12:15], v[184:187], v[148:151]\n\t" \
  "ds_read_b128 v[184:187], v213 offset:256\n\t" \
  "s_add_u32 m0, %[lw], 2048\n\t" \
  "v_mfma_f32_16x16x32_bf16 v[152:155], v[8:11], v[188:191], v[152:155]\n\t" \
  "v_mfma_f32_16x16x32_bf16 v[156:159], v[12:15], v[188:191], v[156:159]\n\t" \
  "ds_read_b128 v[188:191], v213 offset:16640\n\t" \
  "global_load_lds_dwordx4 v218, s[92:93] sc1\n\t" \
  "ds_write_b128 v220, v[136:139] offset:0\n\t" \
  "ds_write_b128 v220, v[140:143] offset:1024\n\t" \
  "s_waitcnt lgkmcnt(8)\n\t" \
  "v_mfma_f32_16x16x32_bf16 v[144:147], v[16:19], v[192:195], v[144:147]\n\t" \
  "v_mfma_f32_16x16x32_bf16 v[148:151], v[20:23], v[192:195], v[148:151]\n\t" \
  "ds_read_b128 v[192:195], v214 offset:256\n\t" \
  "v_mfma_f32_16x16x32_bf16 v[152:155], v[16:19], v[196:199], v[152:155]\n\t" \
  "v_mfma_f32_16x16x32_bf16 v[156:159], v[20:23], v[196:199], v[156:159]\n\t" \
  "ds_read_b128 v[196:199], v214 offset:16640\n\t" \
  "s_lshr_b32 s90, s87, 7\n\t" \
  "s_xor_b32 s90, s90, %[s0]\n\t" \
  "s_and_b32 s90, s90, 1\n\t" \
  "s_lshl1_add_u32 s90, s87, s90\n\t" \
  "s_lshl_b32 s90, s90, 13\n\t" \
  "s_cmp_lt_i32 s86, 6\n\t" \
  "s_cselect_b32 s90, 0x70000000, s90\n\t" \
  "s_waitcnt lgkmcnt(8)\n\t" \
  "v_mfma_f32_16x16x32_bf16 v[144:147], v[24:27], v[200:203], v[144:147]\n\t" \
  "v_mfma_f32_16x16x32_bf16 v[148:151], v[28:31], v[200:203], v[148:151]\n\t" \
  "ds_read_b128 v[200:203], v215 offset:256\n\t" \
  "s_add_u32 m0, %[lw], 3072\n\t" \
  "v_mfma_f32_16x16x32_bf16 v[152:155], v[24:27], v[204:207], v[152:155]\n\t" \
  "v_mfma_f32_16x16x32_bf16 v[156:159], v[28:31], v[204:207], v[156:159]\n\t" \
  "ds_read_b128 v[204:207], v215 offset:16640\n\t" \
  "global_load_lds_dwordx4 v219, s[92:93] sc1\n\t" \
  "s_waitcnt lgkmcnt(4)\n\t" \
  "s_cmp_lt_u32 s86, 6\n\t" \
  "s_cbranch_scc1 .Lw21_%=\n\t" \
  "s_waitcnt vmcnt(4)\n\t" \
  "s_barrier\n\t" \
  ".Lg21_%=:\n\t" \
  "s_add_u32 s92, s92, 512\n\t" \
  "s_addc_u32 s93, s93, 0\n\t" \
  "v_mfma_f32_16x16x32_bf16 v[144:147], v[32:35], v[176:179], v[144:147]\n\t" \
  "v_mfma_f32_16x16x32_bf16 v[148:151], v[36:39], v[176:179], v[148:151]\n\t" \
  "ds_read_b128 v[176:179], v212 offset:32768\n\t" \
  "v_mfma_f32_16x16x32_bf16 v[152:155], v[32:35], v[180:183], v[152:155]\n\t" \
  "v_mfma_f32_16x16x32_bf16 v[156:159], v[36:39], v[180:183], v[156:159]\n\t" \
  "ds_read_b128 v[180:183], v212 offset:49152\n\t" \
  "ds_read_b128 v[160:163], v221 offset:0\n\t" \
  "ds_read_b128 v[164:167], v221 offset:1024\n\t" \
  "s_sub_i32 s91, s86, 2\n\t" \
  "s_max_i32 s88, s88, s91\n\t" \
  "v_mov_b32 v173, s88\n\t" \
  "buffer_store_dword v173, v174, %[rs], 0 offen sc1\n\t" \
  "v_mfma_f32_16x16x32_bf16 v[144:147], v[40:43], v[184:187], v[144:147]\n\t" \
  "v_mfma_f32_16x16x32_bf16 v[148:151], v[44:47], v[184:187], v[148:151]\n\t" \
  "ds_read_b128 v[184:187], v213 offset:32768\n\t" \
  "s_add_u32 m0, %[lw], 32768\n\t" \
  "v_mfma_f32_16x16x32_bf16 v[152:155], v[40:43], v[188:191], v[152:155]\n\t" \
  "v_mfma_f32_16x16x32_bf16 v[156:159], v[44:47], v[188:191], v[156:159]\n\t" \
  "ds_read_b128 v[188:191], v213 offset:49152\n\t" \
  "global_load_lds_dwordx4 v216, s[92:93] sc1\n\t" \
  "s_waitcnt lgkmcnt(8)\n\t" \
  "v_mfma_f32_16x16x32_bf16 v[144:147], v[48:51], v[192:195], v[144:147]\n\t" \
  "v_mfma_f32_16x16x32_bf16 v[148:151], v[52:55], v[192:195], v[148:151]\n\t" \
  "ds_read_b128 v[192:195], v214 offset:32768\n\t" \
  "v_mfma_f32_16x16x32_bf16 v[152:155], v[48:51], v[196:199], v[152:155]\n\t" \
  "v_mfma_f32_16x16x32_bf16 v[156:159], v[52:55], v[196:199], v[156:159]\n\t" \
  "ds_read_b128 v[196:199], v214 offset:49152\n\t" \
  "s_waitcnt lgkmcnt(4)\n\t" \
  "v_add_f32 v160, v160, v128\n\t" \
  "v_add_f32 v161, v161, v129\n\t" \
  "v_add_f32 v162, v162, v130\n\t" \
  "v_add_f32 v163, v163, v131\n\t" \
  "v_add_f32 v164, v164, v132\n\t" \
  "v_add_f32 v165, v165, v133\n\t" \
  "v_add_f32 v166, v166, v134\n\t" \
  "v_add_f32 v167, v167, v135\n\t" \
  "v_mfma_f32_16x16x32_bf16 v[144:147], v[56:59], v[200:203], v[144:147]\n\t" \
  "v_mfma_f32_16x16x32_bf16 v[148:151], v[60:63], v[200:203], v[148:151]\n\t" \
  "ds_read_b128 v[200:203], v215 offset:32768\n\t" \
  "s_add_u32 m0, %[lw], 33792\n\t" \
  "v_mfma_f32_16x16x32_bf16 v[152:155], v[56:59], v[204:207], v[152:155]\n\t" \
  "v_mfma_f32_16x16x32_bf16 v[156:159], v[60:63], v[204:207], v[156:159]\n\t" \
  "ds_read_b128 v[204:207], v215 offset:49152\n\t" \
  "global_load_lds_dwordx4 v217, s[92:93] sc1\n\t" \
  "v_cvt_pk_bf16_f32 v168, v160, v161\n\t" \
  "v_cvt_pk_bf16_f32 v169, v162, v163\n\t" \
  "v_cvt_pk_bf16_f32 v170, v164, v165\n\t" \
  "v_cvt_pk_bf16_f32 v171, v166, v167\n\t" \
  "buffer_store_dwordx4 v[168:171], v172, %[rt], s90 offen sc1\n\t" \
  "v_mfma_f32_16x16x32_bf16 v[144:147], v[64:67], v[176:179], v[144:147]\n\t" \
  "v_mfma_f32_16x16x32_bf16 v[148:151], v[68:71], v[176:179], v[148:151]\n\t" \
  "ds_read_b128 v[176:179], v212 offset:33024\n\t" \
  "v_mfma_f32_16x16x32_bf16 v[152:155], v[64:67], v[180:183], v[152:155]\n\t" \
  "v_mfma_f32_16x16x32_bf16 v[156:159], v[68:71], v[180:183], v[156:159]\n\t" \
  "ds_read_b128 v[180:183], v212 offset:49408\n\t" \
  "s_waitcnt lgkmcnt(6)\n\t" \
  "v_mfma_f32_16x16x32_bf16 v[144:147], v[72:75], v[184:187], v[144:147]\n\t" \
  "v_mfma_f32_16x16x32_bf16 v[148:151], v[76:79], v[184:187], v[148:151]\n\t" \
  "ds_read_b128 v[184:187], v213 offset:33024\n\t" \
  "s_add_u32 m0, %[lw], 34816\n\t" \
  "v_mfma_f32_16x16x32_bf16 v[152:155], v[72:75], v[188:191], v[152:155]\n\t" \
  "v_mfma_f32_16x16x32_bf16 v[156:159], v[76:79], v[188:191], v[156:159]\n\t" \
  "ds_read_b128 v[188:191], v213 offset:49408\n\t" \
  "global_load_lds_dwordx4 v218, s[92:93] sc1\n\t" \
  "s_waitcnt lgkmcnt(6)\n\t" \
  "v_mfma_f32_16x16x32_bf16 v[144:147], v[80:83], v[192:195], v[144:147]\n\t" \
  "v_mfma_f32_16x16x32_bf16 v[148:151], v[84:87], v[192:195], v[148:151]\n\t" \
  "ds_read_b128 v[192:195], v214 offset:33024\n\t" \
  "v_mfma_f32_16x16x32_bf16 v[152:155], v[80:83], v[196:199], v[152:155]\n\t" \
  "v_mfma_f32_16x16x32_bf16 v[156:159], v[84:87], v[196:199], v[156:159]\n\t" \
  "ds_read_b128 v[196:199], v214 offset:49408\n\t" \
  "s_waitcnt lgkmcnt(6)\n\t" \
  "v_mfma_f32_16x16x32_bf16 v[144:147], v[88:91], v[200:203], v[144:147]\n\t" \
  "v_mfma_f32_16x16x32_bf16 v[148:151], v[92:95], v[200:203], v[148:151]\n\t" \
  "ds_read_b128 v[200:203], v215 offset:33024\n\t" \
  "s_add_u32 m0, %[lw], 35840\n\t" \
  "v_mfma_f32_16x16x32_bf16 v[152:155], v[88:91], v[204:207], v[152:155]\n\t" \
  "v_mfma_f32_16x16x32_bf16 v[156:159], v[92:95], v[204:207], v[156:159]\n\t" \
  "ds_read_b128 v[204:207], v215 offset:49408\n\t" \
  "global_load_lds_dwordx4 v219, s[92:93] sc1\n\t" \
  "s_cmp_lt_u32 s86, 8\n\t" \
  "s_cbranch_scc1 .Lw31_%=\n\t" \
  "s_waitcnt vmcnt(6)\n\t" \
  "s_barrier\n\t" \
  ".Lg31_%=:\n\t" \
  "s_add_u32 s92, s92, 65024\n\t" \
  "s_addc_u32 s93, s93, 0\n\t" \
  "s_waitcnt lgkmcnt(6)\n\t" \
  "v_mfma_f32_16x16x32_bf16 v[144:147], v[96:99], v[176:179], v[144:147]\n\t" \
  "v_mfma_f32_16x16x32_bf16 v[148:151], v[100:103], v[176:179], v[148:151]\n\t" \
  "ds_read_b128 v[176:179], v208 offset:0\n\t" \
  "v_mfma_f32_16x16x32_bf16 v[152:155], v[96:99], v[180:183], v[152:155]\n\t" \
  "v_mfma_f32_16x16x32_bf16 v[156:159], v[100:103], v[180:183], v[156:159]\n\t" \
  "ds_read_b128 v[180:183], v208 offset:16384\n\t" \
  "s_waitcnt lgkmcnt(6)\n\t" \
  "v_mfma_f32_16x16x32_bf16 v[144:147], v[104:107], v[184:187], v[144:147]\n\t" \
  "v_mfma_f32_16x16x32_bf16 v[148:151], v[108:111], v[184:187], v[148:151]\n\t" \
  "ds_read_b128 v[184:187], v209 offset:0\n\t" \
  "s_add_u32 m0, %[lw], 65536\n\t" \
  "v_mfma_f32_16x16x32_bf16 v[152:155], v[104:107], v[188:191], v[152:155]\n\t" \
  "v_mfma_f32_16x16x32_bf16 v[156:159], v[108:111], v[188:191], v[156:159]\n\t" \
  "ds_read_b128 v[188:191], v209 offset:16384\n\t" \
  "global_load_lds_dwordx4 v216, s[92:93] sc1\n\t" \
  "s_waitcnt lgkmcnt(6)\n\t" \
  "v_mfma_f32_16x16x32_bf16 v[144:147], v[112:115], v[192:195], v[144:147]\n\t" \
  "v_mfma_f32_16x16x32_bf16 v[148:151], v[116:119], v[192:195], v[148:151]\n\t" \
  "ds_read_b128 v[192:195], v210 offset:0\n\t" \
  "v_mfma_f32_16x16x32_bf16 v[152:155], v[112:115], v[196:199], v[152:155]\n\t" \
  "v_mfma_f32_16x16x32_bf16 v[156:159], v[116:119], v[196:199], v[156:159]\n\t" \
  "ds_read_b128 v[196:199], v210 offset:16384\n\t" \
  "s_waitcnt lgkmcnt(6)\n\t" \
  "v_mfma_f32_16x16x32_bf16 v[144:147], v[120:123], v[200:203], v[144:147]\n\t" \
  "v_mfma_f32_16x16x32_bf16 v[148:151], v[124:127], v[200:203], v[148:151]\n\t" \
  "ds_read_b128 v[200:203], v211 offset:0\n\t" \
  "s_add_u32 m0, %[lw], 66560\n\t" \
  "v_mfma_f32_16x16x32_bf16 v[152:155], v[120:123], v[204:207], v[152:155]\n\t" \
  "v_mfma_f32_16x16x32_bf16 v[156:159], v[124:127], v[204:207], v[156:159]\n\t" \
  "ds_read_b128 v[204:207], v211 offset:16384\n\t" \
  "global_load_lds_dwordx4 v217, s[92:93] sc1\n\t" \
  "s_add_u32 s86, s86, 1\n\t" \
  "s_add_u32 s87, s87, 1\n\t" \
  "s_cmp_lt_u32 s86, %[end]\n\t" \
  "s_cbranch_scc1 .Lp1_%=\n\t" \
  "s_branch .Lx1_%=\n\t" \
  ".Lf01_%=:\n\t" \
  "s_nop 7\n\t" \
  "ds_write_b128 v220, v[152:155] offset:0\n\t" \
  "ds_write_b128 v220, v[156:159] offset:1024\n\t" \
  "s_lshr_b32 s90, s87, 7\n\t" \
  "s_xor_b32 s90, s90, %[s0]\n\t" \
  "s_and_b32 s90, s90, 1\n\t" \
  "s_lshl1_add_u32 s90, s87, s90\n\t" \
  "s_lshl_b32 s90, s90, 13\n\t" \
  "s_cmp_lt_i32 s86, 1\n\t" \
  "s_cselect_b32 s90, 0x70000000, s90\n\t" \
  "s_waitcnt lgkmcnt(0)\n\t" \
  "s_barrier\n\t" \
  "ds_read_b128 v[160:163], v221 offset:0\n\t" \
  "ds_read_b128 v[164:167], v221 offset:1024\n\t" \
  "s_waitcnt lgkmcnt(0)\n\t" \
  "v_add_f32 v160, v160, v144\n\t" \
  "v_add_f32 v161, v161, v145\n\t" \
  "v_add_f32 v162, v162, v146\n\t" \
  "v_add_f32 v163, v163, v147\n\t" \
  "v_add_f32 v164, v164, v148\n\t" \
  "v_add_f32 v165, v165, v149\n\t" \
  "v_add_f32 v166, v166, v150\n\t" \
  "v_add_f32 v167, v167, v151\n\t" \
  "v_cvt_pk_bf16_f32 v168, v160, v161\n\t" \
  "v_cvt_pk_bf16_f32 v169, v162, v163\n\t" \
  "v_cvt_pk_bf16_f32 v170, v164, v165\n\t" \
  "v_cvt_pk_bf16_f32 v171, v166, v167\n\t" \
  "buffer_store_dwordx4 v[168:171], v172, %[rt], s90 offen sc1\n\t" \
  "s_branch .Lfb01_%=\n\t" \
  ".Lf11_%=:\n\t" \
  "s_nop 7\n\t" \
  "ds_write_b128 v220, v[136:139] offset:0\n\t" \
  "ds_write_b128 v220, v[140:143] offset:1024\n\t" \
  "s_lshr_b32 s90, s87, 7\n\t" \
  "s_xor_b32 s90, s90, %[s0]\n\t" \
  "s_and_b32 s90, s90, 1\n\t" \
  "s_lshl1_add_u32 s90, s87, s90\n\t" \
  "s_lshl_b32 s90, s90, 13\n\t" \
  "s_cmp_lt_i32 s86, 1\n\t" \
  "s_cselect_b32 s90, 0x70000000, s90\n\t" \
  "s_waitcnt lgkmcnt(0)\n\t" \
  "s_barrier\n\t" \
  "ds_read_b128 v[160:163], v221 offset:0\n\t" \
  "ds_read_b128 v[164:167], v221 offset:1024\n\t" \
  "s_waitcnt lgkmcnt(0)\n\t" \
  "v_add_f32 v160, v160, v128\n\t" \
  "v_add_f32 v161, v161, v129\n\t" \
  "v_add_f32 v162, v162, v130\n\t" \
  "v_add_f32 v163, v163, v131\n\t" \
  "v_add_f32 v164, v164, v132\n\t" \
  "v_add_f32 v165, v165, v133\n\t" \
  "v_add_f32 v166, v166, v134\n\t" \
  "v_add_f32 v167, v167, v135\n\t" \
  "v_cvt_pk_bf16_f32 v168, v160, v161\n\t" \
  "v_cvt_pk_bf16_f32 v169, v162, v163\n\t" \
  "v_cvt_pk_bf16_f32 v170, v164, v165\n\t" \
  "v_cvt_pk_bf16_f32 v171, v166, v167\n\t" \
  "buffer_store_dwordx4 v[168:171], v172, %[rt], s90 offen sc1\n\t" \
  "s_branch .Lfb11_%=\n\t" \
  ".Lw01_%=:\n\t" \
  "s_waitcnt vmcnt(2)\n\t" \
  "s_barrier\n\t" \
  "s_sub_i32 s91, s86, 1\n\t" \
  "s_max_i32 s88, s88, s91\n\t" \
  "v_mov_b32 v173, s88\n\t" \
  "buffer_store_dword v173, v174, %[rs], 0 offen sc1\n\t" \
  "s_branch .Lg01_%=\n\t" \
  ".Lw11_%=:\n\t" \
  "s_waitcnt vmcnt(2)\n\t" \
  "s_barrier\n\t" \
  "s_sub_i32 s91, s86, 1\n\t" \
  "s_max_i32 s88, s88, s91\n\t" \
  "v_mov_b32 v173, s88\n\t" \
  "buffer_store_dword v173, v174, %[rs], 0 offen sc1\n\t" \
  "s_branch .Lg11_%=\n\t" \
  ".Lw21_%=:\n\t" \
  "s_waitcnt vmcnt(2)\n\t" \
  "s_barrier\n\t" \
  "s_sub_i32 s91, s86, 1\n\t" \
  "s_max_i32 s88, s88, s91\n\t" \
  "v_mov_b32 v173, s88\n\t" \
  "buffer_store_dword v173, v174, %[rs], 0 offen sc1\n\t" \
  "s_branch .Lg21_%=\n\t" \
  ".Lw31_%=:\n\t" \
  "s_waitcnt vmcnt(2)\n\t" \
  "s_barrier\n\t" \
  "s_sub_i32 s91, s86, 1\n\t" \
  "s_max_i32 s88, s88, s91\n\t" \
  "v_mov_b32 v173, s88\n\t" \
  "buffer_store_dword v173, v174, %[rs], 0 offen sc1\n\t" \
  "s_branch .Lg31_%=\n\t" \
  ".Lx1_%=:\n\t" \
  "s_waitcnt lgkmcnt(0)\n\t" \
  "s_setprio 0\n\t" \
  "s_mov_b32 m0, s94\n\t" \
  ".Le_%=:\n\t"
#define PR_ASM_1 \
  "s_cmp_lg_u32 %[kh], 0\n\t" \
  "s_cbranch_scc1 .Lk_%=\n\t" \
  "s_mov_b32 s94, m0\n\t" \
  "s_mov_b64 s[92:93], %[x0]\n\t" \
  "s_mov_b32 s86, %[g0]\n\t" \
  "s_add_u32 s87, s86, %[t0]\n\t" \
  "s_sub_i32 s88, s86, 3\n\t" \
  "ds_read_b128 v[176:179], v208 offset:0\n\t" \
  "ds_read_b128 v[180:183], v208 offset:16384\n\t" \
  "ds_read_b128 v[184:187], v209 offset:0\n\t" \
  "ds_read_b128 v[188:191], v209 offset:16384\n\t" \
  "ds_read_b128 v[192:195], v210 offset:0\n\t" \
  "ds_read_b128 v[196:199], v210 offset:16384\n\t" \
  "ds_read_b128 v[200:203], v211 offset:0\n\t" \
  "ds_read_b128 v[204:207], v211 offset:16384\n\t" \
  "s_branch .Lfb00_%=\n\t" \
  ".Lp0_%=:\n\t" \
  "s_cmp_le_u32 s86, 5\n\t" \
  "s_cbranch_scc1 .Lf00_%=\n\t" \
  ".Lfb00_%=:\n\t" \
  "s_waitcnt lgkmcnt(6)\n\t" \
  "v_mfma_f32_16x16x32_bf16 v[128:131], v[176:179], v[0:3], 0\n\t" \
  "v_mfma_f32_16x16x32_bf16 v[136:139], v[176:179], v[4:7], 0\n\t" \
  "ds_read_b128 v[176:179], v208 offset:256\n\t" \
  "s_add_u32 m0, %[lw], 67584\n\t" \
  "v_mfma_f32_16x16x32_bf16 v[132:135], v[180:183], v[0:3], 0\n\t" \
  "v_mfma_f32_16x16x32_bf16 v[140:143], v[180:183], v[4:7], 0\n\t" \
  "ds_read_b128 v[180:183], v208 offset:16640\n\t" \
  "global_load_lds_dwordx4 v218, s[92:93] sc1\n\t" \
  "s_waitcnt lgkmcnt(6)\n\t" \
  "v_mfma_f32_16x16x32_bf16 v[128:131], v[184:187], v[8:11], v[128:131]\n\t" \
  "v_mfma_f32_16x16x32_bf16 v[136:139], v[184:187], v[12:15], v[136:139]\n\t" \
  "ds_read_b128 v[184:187], v209 offset:256\n\t" \
  "v_mfma_f32_16x16x32_bf16 v[132:135], v[188:191], v[8:11], v[132:135]\n\t" \
  "v_mfma_f32_16x16x32_bf16 v[140:143], v[188:191], v[12:15], v[140:143]\n\t" \
  "ds_read_b128 v[188:191], v209 offset:16640\n\t" \
  "ds_write_b128 v220, v[152:155] offset:0\n\t" \
  "ds_write_b128 v220, v[156:159] offset:1024\n\t" \
  "s_waitcnt lgkmcnt(8)\n\t" \
  "v_mfma_f32_16x16x32_bf16 v[128:131], v[192:195], v[16:19], v[128:131]\n\t" \
  "v_mfma_f32_16x16x32_bf16 v[136:139], v[192:195], v[20:23], v[136:139]\n\t" \
  "ds_read_b128 v[192:195], v210 offset:256\n\t" \
  "s_add_u32 m0, %[lw], 68608\n\t" \
  "v_mfma_f32_16x16x32_bf16 v[132:135], v[196:199], v[16:19], v[132:135]\n\t" \
  "v_mfma_f32_16x16x32_bf16 v[140:143], v[196:199], v[20:23], v[140:143]\n\t" \
  "ds_read_b128 v[196:199], v210 offset:16640\n\t" \
  "global_load_lds_dwordx4 v219, s[92:93] sc1\n\t" \
  "s_lshr_b32 s90, s87, 7\n\t" \
  "s_xor_b32 s90, s90, %[s0]\n\t" \
  "s_and_b32 s90, s90, 1\n\t" \
  "s_lshl1_add_u32 s90, s87, s90\n\t" \
  "s_lshl_b32 s90, s90, 13\n\t" \
  "s_cmp_lt_i32 s86, 6\n\t" \
  "s_cselect_b32 s90, 0x70000000, s90\n\t" \
  "s_waitcnt lgkmcnt(8)\n\t" \
  "v_mfma_f32_16x16x32_bf16 v[128:131], v[200:203], v[24:27], v[128:131]\n\t" \
  "v_mfma_f32_16x16x32_bf16 v[136:139], v[200:203], v[28:31], v[136:139]\n\t" \
  "ds_read_b128 v[200:203], v211 offset:256\n\t" \
  "v_mfma_f32_16x16x32_bf16 v[132:135], v[204:207], v[24:27], v[132:135]\n\t" \
  "v_mfma_f32_16x16x32_bf16 v[140:143], v[204:207], v[28:31], v[140:143]\n\t" \
  "ds_read_b128 v[204:207], v211 offset:16640\n\t" \
  "s_waitcnt lgkmcnt(4)\n\t" \
  "s_cmp_lt_u32 s86, 6\n\t" \
  "s_cbranch_scc1 .Lw00_%=\n\t" \
  "s_waitcnt vmcnt(4)\n\t" \
  "s_barrier\n\t" \
  ".Lg00_%=:\n\t" \
  "s_add_u32 s92, s92, 512\n\t" \
  "s_addc_u32 s93, s93, 0\n\t" \
  "v_mfma_f32_16x16x32_bf16 v[128:131], v[176:179], v[32:35], v[128:131]\n\t" \
  "v_mfma_f32_16x16x32_bf16 v[136:139], v[176:179], v[36:39], v[136:139]\n\t" \
  "ds_read_b128 v[176:179], v208 offset:32768\n\t" \
  "s_add_u32 m0, %[lw], 98304\n\t" \
  "v_mfma_f32_16x16x32_bf16 v[132:135], v[180:183], v[32:35], v[132:135]\n\t" \
  "v_mfma_f32_16x16x32_bf16 v[140:143], v[180:183], v[36:39], v[140:143]\n\t" \
  "ds_read_b128 v[180:183], v208 offset:49152\n\t" \
  "global_load_lds_dwordx4 v216, s[92:93] sc1\n\t" \
  "ds_read_b128 v[160:163], v221 offset:0\n\t" \
  "ds_read_b128 v[164:167], v221 offset:1024\n\t" \
  "v_mfma_f32_16x16x32_bf16 v[128:131], v[184:187], v[40:43], v[128:131]\n\t" \
  "v_mfma_f32_16x16x32_bf16 v[136:139], v[184:187], v[44:47], v[136:139]\n\t" \
  "ds_read_b128 v[184:187], v209 offset:32768\n\t" \
  "v_mfma_f32_16x16x32_bf16 v[132:135], v[188:191], v[40:43], v[132:135]\n\t" \
  "v_mfma_f32_16x16x32_bf16 v[140:143], v[188:191], v[44:47], v[140:143]\n\t" \
  "ds_read_b128 v[188:191], v209 offset:49152\n\t" \
  "s_waitcnt lgkmcnt(8)\n\t" \
  "v_mfma_f32_16x16x32_bf16 v[128:131], v[192:195], v[48:51], v[128:131]\n\t" \
  "v_mfma_f32_16x16x32_bf16 v[136:139], v[192:195], v[52:55], v[136:139]\n\t" \
  "ds_read_b128 v[192:195], v210 offset:32768\n\t" \
  "s_add_u32 m0, %[lw], 99328\n\t" \
  "v_mfma_f32_16x16x32_bf16 v[132:135], v[196:199], v[48:51], v[132:135]\n\t" \
  "v_mfma_f32_16x16x32_bf16 v[140:143], v[196:199], v[52:55], v[140:143]\n\t" \
  "ds_read_b128 v[196:199], v210 offset:49152\n\t" \
  "global_load_lds_dwordx4 v217, s[92:93] sc1\n\t" \
  "s_waitcnt lgkmcnt(4)\n\t" \
  "v_add_f32 v160, v160, v144\n\t" \
  "v_add_f32 v161, v161, v145\n\t" \
  "v_add_f32 v162, v162, v146\n\t" \
  "v_add_f32 v163, v163, v147\n\t" \
  "v_add_f32 v164, v164, v148\n\t" \
  "v_add_f32 v165, v165, v149\n\t" \
  "v_add_f32 v166, v166, v150\n\t" \
  "v_add_f32 v167, v167, v151\n\t" \
  "v_mfma_f32_16x16x32_bf16 v[128:131], v[200:203], v[56:59], v[128:131]\n\t" \
  "v_mfma_f32_16x16x32_bf16 v[136:139], v[200:203], v[60:63], v[136:139]\n\t" \
  "ds_read_b128 v[200:203], v211 offset:32768\n\t" \
  "v_mfma_f32_16x16x32_bf16 v[132:135], v[204:207], v[56:59], v[132:135]\n\t" \
  "v_mfma_f32_16x16x32_bf16 v[140:143], v[204:207], v[60:63], v[140:143]\n\t" \
  "ds_read_b128 v[204:207], v211 offset:49152\n\t" \
  "v_cvt_pk_bf16_f32 v168, v160, v161\n\t" \
  "v_cvt_pk_bf16_f32 v169, v162, v163\n\t" \
  "v_cvt_pk_bf16_f32 v170, v164, v165\n\t" \
  "v_cvt_pk_bf16_f32 v171, v166, v167\n\t" \
  "buffer_store_dwordx4 v[168:171], v172, %[rt], s90 offen sc1\n\t" \
  "v_mfma_f32_16x16x32_bf16 v[128:131], v[176:179], v[64:67], v[128:131]\n\t" \
  "v_mfma_f32_16x16x32_bf16 v[136:139], v[176:179], v[68:71], v[136:139]\n\t" \
  "ds_read_b128 v[176:179], v208 offset:33024\n\t" \
  "s_add_u32 m0, %[lw], 100352\n\t" \
  "v_mfma_f32_16x16x32_bf16 v[132:135], v[180:183], v[64:67], v[132:135]\n\t" \
  "v_mfma_f32_16x16x32_bf16 v[140:143], v[180:183], v[68:71], v[140:143]\n\t" \
  "ds_read_b128 v[180:183], v208 offset:49408\n\t" \
  "global_load_lds_dwordx4 v218, s[92:93] sc1\n\t" \
  "s_waitcnt lgkmcnt(6)\n\t" \
  "v_mfma_f32_16x16x32_bf16 v[128:131], v[184:187], v[72:75], v[128:131]\n\t" \
  "v_mfma_f32_16x16x32_bf16 v[136:139], v[184:187], v[76:79], v[136:139]\n\t" \
  "ds_read_b128 v[184:187], v209 offset:33024\n\t" \
  "v_mfma_f32_16x16x32_bf16 v[132:135], v[188:191], v[72:75], v[132:135]\n\t" \
  "v_mfma_f32_16x16x32_bf16 v[140:143], v[188:191], v[76:79], v[140:143]\n\t" \
  "ds_read_b128 v[188:191], v209 offset:49408\n\t" \
  "s_waitcnt lgkmcnt(6)\n\t" \
  "v_mfma_f32_16x16x32_bf16 v[128:131], v[192:195], v[80:83], v[128:131]\n\t" \
  "v_mfma_f32_16x16x32_bf16 v[136:139], v[192:195], v[84:87], v[136:139]\n\t" \
  "ds_read_b128 v[192:195], v210 offset:33024\n\t" \
  "s_add_u32 m0, %[lw], 101376\n\t" \
  "v_mfma_f32_16x16x32_bf16 v[132:135], v[196:199], v[80:83], v[132:135]\n\t" \
  "v_mfma_f32_16x16x32_bf16 v[140:143], v[196:199], v[84:87], v[140:143]\n\t" \
  "ds_read_b128 v[196:199], v210 offset:49408\n\t" \
  "global_load_lds_dwordx4 v219, s[92:93] sc1\n\t" \
  "s_waitcnt lgkmcnt(6)\n\t" \
  "v_mfma_f32_16x16x32_bf16 v[128:131], v[200:203], v[88:91], v[128:131]\n\t" \
  "v_mfma_f32_16x16x32_bf16 v[136:139], v[200:203], v[92:95], v[136:139]\n\t" \
  "ds_read_b128 v[200:203], v211 offset:33024\n\t" \
  "v_mfma_f32_16x16x32_bf16 v[132:135], v[204:207], v[88:91], v[132:135]\n\t" \
  "v_mfma_f32_16x16x32_bf16 v[140:143], v[204:207], v[92:95], v[140:143]\n\t" \
  "ds_read_b128 v[204:207], v211 offset:49408\n\t" \
  "s_cmp_lt_u32 s86, 8\n\t" \
  "s_cbranch_scc1 .Lw10_%=\n\t" \
  "s_waitcnt vmcnt(5)\n\t" \
  "s_barrier\n\t" \
  ".Lg10_%=:\n\t" \
  "s_add_u32 s92, s92, 65024\n\t" \
  "s_addc_u32 s93, s93, 0\n\t" \
  "s_waitcnt lgkmcnt(6)\n\t" \
  "v_mfma_f32_16x16x32_bf16 v[128:131], v[176:179], v[96:99], v[128:131]\n\t" \
  "v_mfma_f32_16x16x32_bf16 v[136:139], v[176:179], v[100:103], v[136:139]\n\t" \
  "ds_read_b128 v[176:179], v212 offset:0\n\t" \
  "s_add_u32 m0, %[lw], 0\n\t" \
  "v_mfma_f32_16x16x32_bf16 v[132:135], v[180:183], v[96:99], v[132:135]\n\t" \
  "v_mfma_f32_16x16x32_bf16 v[140:143], v[180:183], v[100:103], v[140:143]\n\t" \
  "ds_read_b128 v[180:183], v212 offset:16384\n\t" \
  "global_load_lds_dwordx4 v216, s[92:93] sc1\n\t" \
  "s_waitcnt lgkmcnt(6)\n\t" \
  "v_mfma_f32_16x16x32_bf16 v[128:131], v[184:187], v[104:107], v[128:131]\n\t" \
  "v_mfma_f32_16x16x32_bf16 v[136:139], v[184:187], v[108:111], v[136:139]\n\t" \
  "ds_read_b128 v[184:187], v213 offset:0\n\t" \
  "v_mfma_f32_16x16x32_bf16 v[132:135], v[188:191], v[104:107], v[132:135]\n\t" \
  "v_mfma_f32_16x16x32_bf16 v[140:143], v[188:191], v[108:111], v[140:143]\n\t" \
  "ds_read_b128 v[188:191], v213 offset:16384\n\t" \
  "s_waitcnt lgkmcnt(6)\n\t" \
  "v_mfma_f32_16x16x32_bf16 v[128:131], v[192:195], v[112:115], v[128:131]\n\t" \
  "v_mfma_f32_16x16x32_bf16 v[136:139], v[192:195], v[116:119], v[136:139]\n\t" \
  "ds_read_b128 v[192:195], v214 offset:0\n\t" \
  "s_add_u32 m0, %[lw], 1024\n\t" \
  "v_mfma_f32_16x16x32_bf16 v[132:135], v[196:199], v[112:115], v[132:135]\n\t" \
  "v_mfma_f32_16x16x32_bf16 v[140:143], v[196:199], v[116:119], v[140:143]\n\t" \
  "ds_read_b128 v[196:199], v214 offset:16384\n\t" \
  "global_load_lds_dwordx4 v217, s[92:93] sc1\n\t" \
  "s_waitcnt lgkmcnt(6)\n\t" \
  "v_mfma_f32_16x16x32_bf16 v[128:131], v[200:203], v[120:123], v[128:131]\n\t" \
  "v_mfma_f32_16x16x32_bf16 v[136:139], v[200:203], v[124:127], v[136:139]\n\t" \
  "ds_read_b128 v[200:203], v215 offset:0\n\t" \
  "v_mfma_f32_16x16x32_bf16 v[132:135], v[204:207], v[120:123], v[132:135]\n\t" \
  "v_mfma_f32_16x16x32_bf16 v[140:143], v[204:207], v[124:127], v[140:143]\n\t" \
  "ds_read_b128 v[204:207], v215 offset:16384\n\t" \
  "s_add_u32 s86, s86, 1\n\t" \
  "s_add_u32 s87, s87, 1\n\t" \
  "s_cmp_le_u32 s86, 5\n\t" \
  "s_cbranch_scc1 .Lf10_%=\n\t" \
  ".Lfb10_%=:\n\t" \
  "s_waitcnt lgkmcnt(6)\n\t" \
  "v_mfma_f32_16x16x32_bf16 v[144:147], v[176:179], v[0:3], 0\n\t" \
  "v_mfma_f32_16x16x32_bf16 v[152:155], v[176:179], v[4:7], 0\n\t" \
  "ds_read_b128 v[176:179], v212 offset:256\n\t" \
  "s_add_u32 m0, %[lw], 2048\n\t" \
  "v_mfma_f32_16x16x32_bf16 v[148:151], v[180:183], v[0:3], 0\n\t" \
  "v_mfma_f32_16x16x32_bf16 v[156:159], v[180:183], v[4:7], 0\n\t" \
  "ds_read_b128 v[180:183], v212 offset:16640\n\t" \
  "global_load_lds_dwordx4 v218, s[92:93] sc1\n\t" \
  "s_waitcnt lgkmcnt(6)\n\t" \
  "v_mfma_f32_16x16x32_bf16 v[144:147], v[184:187], v[8:11], v[144:147]\n\t" \
  "v_mfma_f32_16x16x32_bf16 v[152:155], v[184:187], v[12:15], v[152:155]\n\t" \
  "ds_read_b128 v[184:187], v213 offset:256\n\t" \
  "v_mfma_f32_16x16x32_bf16 v[148:151], v[188:191], v[8:11], v[148:151]\n\t" \
  "v_mfma_f32_16x16x32_bf16 v[156:159], v[188:191], v[12:15], v[156:159]\n\t" \
  "ds_read_b128 v[188:191], v213 offset:16640\n\t" \
  "ds_write_b128 v220, v[136:139] offset:0\n\t" \
  "ds_write_b128 v220, v[140:143] offset:1024\n\t" \
  "s_waitcnt lgkmcnt(8)\n\t" \
  "v_mfma_f32_16x16x32_bf16 v[144:147], v[192:195], v[16:19], v[144:147]\n\t" \
  "v_mfma_f32_16x16x32_bf16 v[152:155], v[192:195], v[20:23], v[152:155]\n\t" \
  "ds_read_b128 v[192:195], v214 offset:256\n\t" \
  "s_add_u32 m0, %[lw], 3072\n\t" \
  "v_mfma_f32_16x16x32_bf16 v[148:151], v[196:199], v[16:19], v[148:151]\n\t" \
  "v_mfma_f32_16x16x32_bf16 v[156:159], v[196:199], v[20:23], v[156:159]\n\t" \
  "ds_read_b128 v[196:199], v214 offset:16640\n\t" \
  "global_load_lds_dwordx4 v219, s[92:93] sc1\n\t" \
  "s_lshr_b32 s90, s87, 7\n\t" \
  "s_xor_b32 s90, s90, %[s0]\n\t" \
  "s_and_b32 s90, s90, 1\n\t" \
  "s_lshl1_add_u32 s90, s87, s90\n\t" \
  "s_lshl_b32 s90, s90, 13\n\t" \
  "s_cmp_lt_i32 s86, 6\n\t" \
  "s_cselect_b32 s90, 0x70000000, s90\n\t" \
  "s_waitcnt lgkmcnt(8)\n\t" \
  "v_mfma_f32_16x16x32_bf16 v[144:147], v[200:203], v[24:27], v[144:147]\n\t" \
  "v_mfma_f32_16x16x32_bf16 v[152:155], v[200:203], v[28:31], v[152:155]\n\t" \
  "ds_read_b128 v[200:203], v215 offset:256\n\t" \
  "v_mfma_f32_16x16x32_bf16 v[148:151], v[204:207], v[24:27], v[148:151]\n\t" \
  "v_mfma_f32_16x16x32_bf16 v[156:159], v[204:207], v[28:31], v[156:159]\n\t" \
  "ds_read_b128 v[204:207], v215 offset:16640\n\t" \
  "s_waitcnt lgkmcnt(4)\n\t" \
  "s_cmp_lt_u32 s86, 6\n\t" \
  "s_cbranch_scc1 .Lw20_%=\n\t" \
  "s_waitcnt vmcnt(4)\n\t" \
  "s_barrier\n\t" \
  ".Lg20_%=:\n\t" \
  "s_add_u32 s92, s92, 512\n\t" \
  "s_addc_u32 s93, s93, 0\n\t" \
  "v_mfma_f32_16x16x32_bf16 v[144:147], v[176:179], v[32:35], v[144:147]\n\t" \
  "v_mfma_f32_16x16x32_bf16 v[152:155], v[176:179], v[36:39], v[152:155]\n\t" \
  "ds_read_b128 v[176:179], v212 offset:32768\n\t" \
  "s_add_u32 m0, %[lw], 32768\n\t" \
  "v_mfma_f32_16x16x32_bf16 v[148:151], v[180:183], v[32:35], v[148:151]\n\t" \
  "v_mfma_f32_16x16x32_bf16 v[156:159], v[180:183], v[36:39], v[156:159]\n\t" \
  "ds_read_b128 v[180:183], v212 offset:49152\n\t" \
  "global_load_lds_dwordx4 v216, s[92:93] sc1\n\t" \
  "ds_read_b128 v[160:163], v221 offset:0\n\t" \
  "ds_read_b128 v[164:167], v221 offset:1024\n\t" \
  "v_mfma_f32_16x16x32_bf16 v[144:147], v[184:187], v[40:43], v[144:147]\n\t" \
  "v_mfma_f32_16x16x32_bf16 v[152:155], v[184:187], v[44:47], v[152:155]\n\t" \
  "ds_read_b128 v[184:187], v213 offset:32768\n\t" \
  "v_mfma_f32_16x16x32_bf16 v[148:151], v[188:191], v[40:43], v[148:151]\n\t" \
  "v_mfma_f32_16x16x32_bf16 v[156:159], v[188:191], v[44:47], v[156:159]\n\t" \
  "ds_read_b128 v[188:191], v213 offset:49152\n\t" \
  "s_waitcnt lgkmcnt(8)\n\t" \
  "v_mfma_f32_16x16x32_bf16 v[144:147], v[192:195], v[48:51], v[144:147]\n\t" \
  "v_mfma_f32_16x16x32_bf16 v[152:155], v[192:195], v[52:55], v[152:155]\n\t" \
  "ds_read_b128 v[192:195], v214 offset:32768\n\t" \
  "s_add_u32 m0, %[lw], 33792\n\t" \
  "v_mfma_f32_16x16x32_bf16 v[148:151], v[196:199], v[48:51], v[148:151]\n\t" \
  "v_mfma_f32_16x16x32_bf16 v[156:159], v[196:199], v[52:55], v[156:159]\n\t" \
  "ds_read_b128 v[196:199], v214 offset:49152\n\t" \
  "global_load_lds_dwordx4 v217, s[92:93] sc1\n\t" \
  "s_waitcnt lgkmcnt(4)\n\t" \
  "v_add_f32 v160, v160, v128\n\t" \
  "v_add_f32 v161, v161, v129\n\t" \
  "v_add_f32 v162, v162, v130\n\t" \
  "v_add_f32 v163, v163, v131\n\t" \
  "v_add_f32 v164, v164, v132\n\t" \
  "v_add_f32 v165, v165, v133\n\t" \
  "v_add_f32 v166, v166, v134\n\t" \
  "v_add_f32 v167, v167, v135\n\t" \
  "v_mfma_f32_16x16x32_bf16 v[144:147], v[200:203], v[56:59], v[144:147]\n\t" \
  "v_mfma_f32_16x16x32_bf16 v[152:155], v[200:203], v[60:63], v[152:155]\n\t" \
  "ds_read_b128 v[200:203], v215 offset:32768\n\t" \
  "v_mfma_f32_16x16x32_bf16 v[148:151], v[204:207], v[56:59], v[148:151]\n\t" \
  "v_mfma_f32_16x16x32_bf16 v[156:159], v[204:207], v[60:63], v[156:159]\n\t" \
  "ds_read_b128 v[204:207], v215 offset:49152\n\t" \
  "v_cvt_pk_bf16_f32 v168, v160, v161\n\t" \
  "v_cvt_pk_bf16_f32 v169, v162, v163\n\t" \
  "v_cvt_pk_bf16_f32 v170, v164, v165\n\t" \
  "v_cvt_pk_bf16_f32 v171, v166, v167\n\t" \
  "buffer_store_dwordx4 v[168:171], v172, %[rt], s90 offen sc1\n\t" \
  "v_mfma_f32_16x16x32_bf16 v[144:147], v[176:179], v[64:67], v[144:147]\n\t" \
  "v_mfma_f32_16x16x32_bf16 v[152:155], v[176:179], v[68:71], v[152:155]\n\t" \
  "ds_read_b128 v[176:179], v212 offset:33024\n\t" \
  "s_add_u32 m0, %[lw], 34816\n\t" \
  "v_mfma_f32_16x16x32_bf16 v[148:151], v[180:183], v[64:67], v[148:151]\n\t" \
  "v_mfma_f32_16x16x32_bf16 v[156:159], v[180:183], v[68:71], v[156:159]\n\t" \
  "ds_read_b128 v[180:183], v212 offset:49408\n\t" \
  "global_load_lds_dwordx4 v218, s[92:93] sc1\n\t" \
  "s_waitcnt lgkmcnt(6)\n\t" \
  "v_mfma_f32_16x16x32_bf16 v[144:147], v[184:187], v[72:75], v[144:147]\n\t" \
  "v_mfma_f32_16x16x32_bf16 v[152:155], v[184:187], v[76:79], v[152:155]\n\t" \
  "ds_read_b128 v[184:187], v213 offset:33024\n\t" \
  "v_mfma_f32_16x16x32_bf16 v[148:151], v[188:191], v[72:75], v[148:151]\n\t" \
  "v_mfma_f32_16x16x32_bf16 v[156:159], v[188:191], v[76:79], v[156:159]\n\t" \
  "ds_read_b128 v[188:191], v213 offset:49408\n\t" \
  "s_waitcnt lgkmcnt(6)\n\t" \
  "v_mfma_f32_16x16x32_bf16 v[144:147], v[192:195], v[80:83], v[144:147]\n\t" \
  "v_mfma_f32_16x16x32_bf16 v[152:155], v[192:195], v[84:87], v[152:155]\n\t" \
  "ds_read_b128 v[192:195], v214 offset:33024\n\t" \
  "s_add_u32 m0, %[lw], 35840\n\t" \
  "v_mfma_f32_16x16x32_bf16 v[148:151], v[196:199], v[80:83], v[148:151]\n\t" \
  "v_mfma_f32_16x16x32_bf16 v[156:159], v[196:199], v[84:87], v[156:159]\n\t" \
  "ds_read_b128 v[196:199], v214 offset:49408\n\t" \
  "global_load_lds_dwordx4 v219, s[92:93] sc1\n\t" \
  "s_waitcnt lgkmcnt(6)\n\t" \
  "v_mfma_f32_16x16x32_bf16 v[144:147], v[200:203], v[88:91], v[144:147]\n\t" \
  "v_mfma_f32_16x16x32_bf16 v[152:155], v[200:203], v[92:95], v[152:155]\n\t" \
  "ds_read_b128 v[200:203], v215 offset:33024\n\t" \
  "v_mfma_f32_16x16x32_bf16 v[148:151], v[204:207], v[88:91], v[148:151]\n\t" \
  "v_mfma_f32_16x16x32_bf16 v[156:159], v[204:207], v[92:95], v[156:159]\n\t" \
  "ds_read_b128 v[204:207], v215 offset:49408\n\t" \
  "s_cmp_lt_u32 s86, 8\n\t" \
  "s_cbranch_scc1 .Lw30_%=\n\t" \
  "s_waitcnt vmcnt(5)\n\t" \
  "s_barrier\n\t" \
  ".Lg30_%=:\n\t" \
  "s_add_u32 s92, s92, 65024\n\t" \
  "s_addc_u32 s93, s93, 0\n\t" \
  "s_waitcnt lgkmcnt(6)\n\t" \
  "v_mfma_f32_16x16x32_bf16 v[144:147], v[176:179], v[96:99], v[144:147]\n\t" \
  "v_mfma_f32_16x16x32_bf16 v[152:155], v[176:179], v[100:103], v[152:155]\n\t" \
  "ds_read_b128 v[176:179], v208 offset:0\n\t" \
  "s_add_u32 m0, %[lw], 65536\n\t" \
  "v_mfma_f32_16x16x32_bf16 v[148:151], v[180:183], v[96:99], v[148:151]\n\t" \
  "v_mfma_f32_16x16x32_bf16 v[156:159], v[180:183], v[100:103], v[156:159]\n\t" \
  "ds_read_b128 v[180:183], v208 offset:16384\n\t" \
  "global_load_lds_dwordx4 v216, s[92:93] sc1\n\t" \
  "s_waitcnt lgkmcnt(6)\n\t" \
  "v_mfma_f32_16x16x32_bf16 v[144:147], v[184:187], v[104:107], v[144:147]\n\t" \
  "v_mfma_f32_16x16x32_bf16 v[152:155], v[184:187], v[108:111], v[152:155]\n\t" \
  "ds_read_b128 v[184:187], v209 offset:0\n\t" \
  "v_mfma_f32_16x16x32_bf16 v[148:151], v[188:191], v[104:107], v[148:151]\n\t" \
  "v_mfma_f32_16x16x32_bf16 v[156:159], v[188:191], v[108:111], v[156:159]\n\t" \
  "ds_read_b128 v[188:191], v209 offset:16384\n\t" \
  "s_waitcnt lgkmcnt(6)\n\t" \
  "v_mfma_f32_16x16x32_bf16 v[144:147], v[192:195], v[112:115], v[144:147]\n\t" \
  "v_mfma_f32_16x16x32_bf16 v[152:155], v[192:195], v[116:119], v[152:155]\n\t" \
  "ds_read_b128 v[192:195], v210 offset:0\n\t" \
  "s_add_u32 m0, %[lw], 66560\n\t" \
  "v_mfma_f32_16x16x32_bf16 v[148:151], v[196:199], v[112:115], v[148:151]\n\t" \
  "v_mfma_f32_16x16x32_bf16 v[156:159], v[196:199], v[116:119], v[156:159]\n\t" \
  "ds_read_b128 v[196:199], v210 offset:16384\n\t" \
  "global_load_lds_dwordx4 v217, s[92:93] sc1\n\t" \
  "s_waitcnt lgkmcnt(6)\n\t" \
  "v_mfma_f32_16x16x32_bf16 v[144:147], v[200:203], v[120:123], v[144:147]\n\t" \
  "v_mfma_f32_16x16x32_bf16 v[152:155], v[200:203], v[124:127], v[152:155]\n\t" \
  "ds_read_b128 v[200:203], v211 offset:0\n\t" \
  "v_mfma_f32_16x16x32_bf16 v[148:151], v[204:207], v[120:123], v[148:151]\n\t" \
  "v_mfma_f32_16x16x32_bf16 v[156:159], v[204:207], v[124:127], v[156:159]\n\t" \
  "ds_read_b128 v[204:207], v211 offset:16384\n\t" \
  "s_add_u32 s86, s86, 1\n\t" \
  "s_add_u32 s87, s87, 1\n\t" \
  "s_cmp_lt_u32 s86, %[end]\n\t" \
  "s_cbranch_scc1 .Lp0_%=\n\t" \
  "s_branch .Lx0_%=\n\t" \
  ".Lf00_%=:\n\t" \
  "s_nop 7\n\t" \
  "ds_write_b128 v220, v[152:155] offset:0\n\t" \
  "ds_write_b128 v220, v[156:159] offset:1024\n\t" \
  "s_lshr_b32 s90, s87, 7\n\t" \
  "s_xor_b32 s90, s90, %[s0]\n\t" \
  "s_and_b32 s90, s90, 1\n\t" \
  "s_lshl1_add_u32 s90, s87, s90\n\t" \
  "s_lshl_b32 s90, s90, 13\n\t" \
  "s_cmp_lt_i32 s86, 1\n\t" \
  "s_cselect_b32 s90, 0x70000000, s90\n\t" \
  "s_waitcnt lgkmcnt(0)\n\t" \
  "s_barrier\n\t" \
  "ds_read_b128 v[160:163], v221 offset:0\n\t" \
  "ds_read_b128 v[164:167], v221 offset:1024\n\t" \
  "s_waitcnt lgkmcnt(0)\n\t" \
  "v_add_f32 v160, v160, v144\n\t" \
  "v_add_f32 v161, v161, v145\n\t" \
  "v_add_f32 v162, v162, v146\n\t" \
  "v_add_f32 v163, v163, v147\n\t" \
  "v_add_f32 v164, v164, v148\n\t" \
  "v_add_f32 v165, v165, v149\n\t" \
  "v_add_f32 v166, v166, v150\n\t" \
  "v_add_f32 v167, v167, v151\n\t" \
  "v_cvt_pk_bf16_f32 v168, v160, v161\n\t" \
  "v_cvt_pk_bf16_f32 v169, v162, v163\n\t" \
  "v_cvt_pk_bf16_f32 v170, v164, v165\n\t" \
  "v_cvt_pk_bf16_f32 v171, v166, v167\n\t" \
  "buffer_store_dwordx4 v[168:171], v172, %[rt], s90 offen sc1\n\t" \
  "s_branch .Lfb00_%=\n\t" \
  ".Lf10_%=:\n\t" \
  "s_nop 7\n\t" \
  "ds_write_b128 v220, v[136:139] offset:0\n\t" \
  "ds_write_b128 v220, v[140:143] offset:1024\n\t" \
  "s_lshr_b32 s90, s87, 7\n\t" \
  "s_xor_b32 s90, s90, %[s0]\n\t" \
  "s_and_b32 s90, s90, 1\n\t" \
  "s_lshl1_add_u32 s90, s87, s90\n\t" \
  "s_lshl_b32 s90, s90, 13\n\t" \
  "s_cmp_lt_i32 s86, 1\n\t" \
  "s_cselect_b32 s90, 0x70000000, s90\n\t" \
  "s_waitcnt lgkmcnt(0)\n\t" \
  "s_barrier\n\t" \
  "ds_read_b128 v[160:163], v221 offset:0\n\t" \
  "ds_read_b128 v[164:167], v221 offset:1024\n\t" \
  "s_waitcnt lgkmcnt(0)\n\t" \
  "v_add_f32 v160, v160, v128\n\t" \
  "v_add_f32 v161, v161, v129\n\t" \
  "v_add_f32 v162, v162, v130\n\t" \
  "v_add_f32 v163, v163, v131\n\t" \
  "v_add_f32 v164, v164, v132\n\t" \
  "v_add_f32 v165, v165, v133\n\t" \
  "v_add_f32 v166, v166, v134\n\t" \
  "v_add_f32 v167, v167, v135\n\t" \
  "v_cvt_pk_bf16_f32 v168, v160, v161\n\t" \
  "v_cvt_pk_bf16_f32 v169, v162, v163\n\t" \
  "v_cvt_pk_bf16_f32 v170, v164, v165\n\t" \
  "v_cvt_pk_bf16_f32 v171, v166, v167\n\t" \
  "buffer_store_dwordx4 v[168:171], v172, %[rt], s90 offen sc1\n\t" \
  "s_branch .Lfb10_%=\n\t" \
  ".Lw00_%=:\n\t" \
  "s_waitcnt vmcnt(2)\n\t" \
  "s_barrier\n\t" \
  "s_branch .Lg00_%=\n\t" \
  ".Lw10_%=:\n\t" \
  "s_waitcnt vmcnt(2)\n\t" \
  "s_barrier\n\t" \
  "s_branch .Lg10_%=\n\t" \
  ".Lw20_%=:\n\t" \
  "s_waitcnt vmcnt(2)\n\t" \
  "s_barrier\n\t" \
  "s_branch .Lg20_%=\n\t" \
  ".Lw30_%=:\n\t" \
  "s_waitcnt vmcnt(2)\n\t" \
  "s_barrier\n\t" \
  "s_branch .Lg30_%=\n\t" \
  ".Lx0_%=:\n\t" \
  "s_waitcnt lgkmcnt(0)\n\t" \
  "s_setprio 0\n\t" \
  "s_mov_b32 m0, s94\n\t" \
  "s_branch .Le_%=\n\t" \
  ".Lk_%=:\n\t" \
  "s_mov_b32 s94, m0\n\t" \
  "s_mov_b64 s[92:93], %[x0]\n\t" \
  "s_mov_b32 s86, %[g0]\n\t" \
  "s_add_u32 s87, s86, %[t0]\n\t" \
  "s_sub_i32 s88, s86, 3\n\t" \
  "ds_read_b128 v[176:179], v208 offset:0\n\t" \
  "ds_read_b128 v[180:183], v208 offset:16384\n\t" \
  "ds_read_b128 v[184:187], v209 offset:0\n\t" \
  "ds_read_b128 v[188:191], v209 offset:16384\n\t" \
  "ds_read_b128 v[192:195], v210 offset:0\n\t" \
  "ds_read_b128 v[196:199], v210 offset:16384\n\t" \
  "ds_read_b128 v[200:203], v211 offset:0\n\t" \
  "ds_read_b128 v[204:207], v211 offset:16384\n\t" \
  "s_branch .Lfb01_%=\n\t" \
  ".Lp1_%=:\n\t" \
  "s_cmp_le_u32 s86, 5\n\t" \
  "s_cbranch_scc1 .Lf01_%=\n\t" \
  ".Lfb01_%=:\n\t" \
  "s_waitcnt lgkmcnt(6)\n\t" \
  "v_mfma_f32_16x16x32_bf16 v[128:131], v[176:179], v[0:3], 0\n\t" \
  "v_mfma_f32_16x16x32_bf16 v[136:139], v[176:179], v[4:7], 0\n\t" \
  "ds_read_b128 v[176:179], v208 offset:256\n\t" \
  "v_mfma_f32_16x16x32_bf16 v[132:135], v[180:183], v[0:3], 0\n\t" \
  "v_mfma_f32_16x16x32_bf16 v[140:143], v[180:183], v[4:7], 0\n\t" \
  "ds_read_b128 v[180:183], v208 offset:16640\n\t" \
  "s_waitcnt lgkmcnt(6)\n\t" \
  "v_mfma_f32_16x16x32_bf16 v[128:131], v[184:187], v[8:11], v[128:131]\n\t" \
  "v_mfma_f32_16x16x32_bf16 v[136:139], v[184:187], v[12:15], v[136:139]\n\t" \
  "ds_read_b128 v[184:187], v209 offset:256\n\t" \
  "s_add_u32 m0, %[lw], 67584\n\t" \
  "v_mfma_f32_16x16x32_bf16 v[132:135], v[188:191], v[8:11], v[132:135]\n\t" \
  "v_mfma_f32_16x16x32_bf16 v[140:143], v[188:191], v[12:15], v[140:143]\n\t" \
  "ds_read_b128 v[188:191], v209 offset:16640\n\t" \
  "global_load_lds_dwordx4 v218, s[92:93] sc1\n\t" \
  "ds_write_b128 v220, v[152:155] offset:0\n\t" \
  "ds_write_b128 v220, v[156:159] offset:1024\n\t" \
  "s_waitcnt lgkmcnt(8)\n\t" \
  "v_mfma_f32_16x16x32_bf16 v[128:131], v[192:195], v[16:19], v[128:131]\n\t" \
  "v_mfma_f32_16x16x32_bf16 v[136:139], v[192:195], v[20:23], v[136:139]\n\t" \
  "ds_read_b128 v[192:195], v210 offset:256\n\t" \
  "v_mfma_f32_16x16x32_bf16 v[132:135], v[196:199], v[16:19], v[132:135]\n\t" \
  "v_mfma_f32_16x16x32_bf16 v[140:143], v[196:199], v[20:23], v[140:143]\n\t" \
  "ds_read_b128 v[196:199], v210 offset:16640\n\t" \
  "s_lshr_b32 s90, s87, 7\n\t" \
  "s_xor_b32 s90, s90, %[s0]\n\t" \
  "s_and_b32 s90, s90, 1\n\t" \
  "s_lshl1_add_u32 s90, s87, s90\n\t" \
  "s_lshl_b32 s90, s90, 13\n\t" \
  "s_cmp_lt_i32 s86, 6\n\t" \
  "s_cselect_b32 s90, 0x70000000, s90\n\t" \
  "s_waitcnt lgkmcnt(8)\n\t" \
  "v_mfma_f32_16x16x32_bf16 v[128:131], v[200:203], v[24:27], v[128:131]\n\t" \
  "v_mfma_f32_16x16x32_bf16 v[136:139], v[200:203], v[28:31], v[136:139]\n\t" \
  "ds_read_b128 v[200:203], v211 offset:256\n\t" \
  "s_add_u32 m0, %[lw], 68608\n\t" \
  "v_mfma_f32_16x16x32_bf16 v[132:135], v[204:207], v[24:27], v[132:135]\n\t" \
  "v_mfma_f32_16x16x32_bf16 v[140:143], v[204:207], v[28:31], v[140:143]\n\t" \
  "ds_read_b128 v[204:207], v211 offset:16640\n\t" \
  "global_load_lds_dwordx4 v219, s[92:93] sc1\n\t" \
  "s_waitcnt lgkmcnt(4)\n\t" \
  "s_cmp_lt_u32 s86, 6\n\t" \
  "s_cbranch_scc1 .Lw01_%=\n\t" \
  "s_waitcnt vmcnt(4)\n\t" \
  "s_barrier\n\t" \
  ".Lg01_%=:\n\t" \
  "s_add_u32 s92, s92, 512\n\t" \
  "s_addc_u32 s93, s93, 0\n\t" \
  "v_mfma_f32_16x16x32_bf16 v[128:131], v[176:179], v[32:35], v[128:131]\n\t" \
  "v_mfma_f32_16x16x32_bf16 v[136:139], v[176:179], v[36:39], v[136:139]\n\t" \
  "ds_read_b128 v[176:179], v208 offset:32768\n\t" \
  "v_mfma_f32_16x16x32_bf16 v[132:135], v[180:183], v[32:35], v[132:135]\n\t" \
  "v_mfma_f32_16x16x32_bf16 v[140:143], v[180:183], v[36:39], v[140:143]\n\t" \
  "ds_read_b128 v[180:183], v208 offset:49152\n\t" \
  "ds_read_b128 v[160:163], v221 offset:0\n\t" \
  "ds_read_b128 v[164:167], v221 offset:1024\n\t" \
  "s_sub_i32 s91, s86, 2\n\t" \
  "s_max_i32 s88, s88, s91\n\t" \
  "v_mov_b32 v173, s88\n\t" \
  "buffer_store_dword v173, v174, %[rs], 0 offen sc1\n\t" \
  "v_mfma_f32_16x16x32_bf16 v[128:131], v[184:187], v[40:43], v[128:131]\n\t" \
  "v_mfma_f32_16x16x32_bf16 v[136:139], v[184:187], v[44:47], v[136:139]\n\t" \
  "ds_read_b128 v[184:187], v209 offset:32768\n\t" \
  "s_add_u32 m0, %[lw], 98304\n\t" \
  "v_mfma_f32_16x16x32_bf16 v[132:135], v[188:191], v[40:43], v[132:135]\n\t" \
  "v_mfma_f32_16x16x32_bf16 v[140:143], v[188:191], v[44:47], v[140:143]\n\t" \
  "ds_read_b128 v[188:191], v209 offset:49152\n\t" \
  "global_load_lds_dwordx4 v216, s[92:93] sc1\n\t" \
  "s_waitcnt lgkmcnt(8)\n\t" \
  "v_mfma_f32_16x16x32_bf16 v[128:131], v[192:195], v[48:51], v[128:131]\n\t" \
  "v_mfma_f32_16x16x32_bf16 v[136:139], v[192:195], v[52:55], v[136:139]\n\t" \
  "ds_read_b128 v[192:195], v210 offset:32768\n\t" \
  "v_mfma_f32_16x16x32_bf16 v[132:135], v[196:199], v[48:51], v[132:135]\n\t" \
  "v_mfma_f32_16x16x32_bf16 v[140:143], v[196:199], v[52:55], v[140:143]\n\t" \
  "ds_read_b128 v[196:199], v210 offset:49152\n\t" \
  "s_waitcnt lgkmcnt(4)\n\t" \
  "v_add_f32 v160, v160, v144\n\t" \
  "v_add_f32 v161, v161, v145\n\t" \
  "v_add_f32 v162, v162, v146\n\t" \
  "v_add_f32 v163, v163, v147\n\t" \
  "v_add_f32 v164, v164, v148\n\t" \
  "v_add_f32 v165, v165, v149\n\t" \
  "v_add_f32 v166, v166, v150\n\t" \
  "v_add_f32 v167, v167, v151\n\t" \
  "v_mfma_f32_16x16x32_bf16 v[128:131], v[200:203], v[56:59], v[128:131]\n\t" \
  "v_mfma_f32_16x16x32_bf16 v[136:139], v[200:203], v[60:63], v[136:139]\n\t" \
  "ds_read_b128 v[200:203], v211 offset:32768\n\t" \
  "s_add_u32 m0, %[lw], 99328\n\t" \
  "v_mfma_f32_16x16x32_bf16 v[132:135], v[204:207], v[56:59], v[132:135]\n\t" \
  "v_mfma_f32_16x16x32_bf16 v[140:143], v[204:207], v[60:63], v[140:143]\n\t" \
  "ds_read_b128 v[204:207], v211 offset:49152\n\t" \
  "global_load_lds_dwordx4 v217, s[92:93] sc1\n\t" \
  "v_cvt_pk_bf16_f32 v168, v160, v161\n\t" \
  "v_cvt_pk_bf16_f32 v169, v162, v163\n\t" \
  "v_cvt_pk_bf16_f32 v170, v164, v165\n\t" \
  "v_cvt_pk_bf16_f32 v171, v166, v167\n\t" \
  "buffer_store_dwordx4 v[168:171], v172, %[rt], s90 offen sc1\n\t" \
  "v_mfma_f32_16x16x32_bf16 v[128:131], v[176:179], v[64:67], v[128:131]\n\t" \
  "v_mfma_f32_16x16x32_bf16 v[136:139], v[176:179], v[68:71], v[136:139]\n\t" \
  "ds_read_b128 v[176:179], v208 offset:33024\n\t" \
  "v_mfma_f32_16x16x32_bf16 v[132:135], v[180:183], v[64:67], v[132:135]\n\t" \
  "v_mfma_f32_16x16x32_bf16 v[140:143], v[180:183], v[68:71], v[140:143]\n\t" \
  "ds_read_b128 v[180:183], v208 offset:49408\n\t" \
  "s_waitcnt lgkmcnt(6)\n\t" \
  "v_mfma_f32_16x16x32_bf16 v[128:131], v[184:187], v[72:75], v[128:131]\n\t" \
  "v_mfma_f32_16x16x32_bf16 v[136:139], v[184:187], v[76:79], v[136:139]\n\t" \
  "ds_read_b128 v[184:187], v209 offset:33024\n\t" \
  "s_add_u32 m0, %[lw], 100352\n\t" \
  "v_mfma_f32_16x16x32_bf16 v[132:135], v[188:191], v[72:75], v[132:135]\n\t" \
  "v_mfma_f32_16x16x32_bf16 v[140:143], v[188:191], v[76:79], v[140:143]\n\t" \
  "ds_read_b128 v[188:191], v209 offset:49408\n\t" \
  "global_load_lds_dwordx4 v218, s[92:93] sc1\n\t" \
  "s_waitcnt lgkmcnt(6)\n\t" \
  "v_mfma_f32_16x16x32_bf16 v[128:131], v[192:195], v[80:83], v[128:131]\n\t" \
  "v_mfma_f32_16x16x32_bf16 v[136:139], v[192:195], v[84:87], v[136:139]\n\t" \
  "ds_read_b128 v[192:195], v210 offset:33024\n\t" \
  "v_mfma_f32_16x16x32_bf16 v[132:135], v[196:199], v[80:83], v[132:135]\n\t" \
  "v_mfma_f32_16x16x32_bf16 v[140:143], v[196:199], v[84:87], v[140:143]\n\t" \
  "ds_read_b128 v[196:199], v210 offset:49408\n\t" \
  "s_waitcnt lgkmcnt(6)\n\t" \
  "v_mfma_f32_16x16x32_bf16 v[128:131], v[200:203], v[88:91], v[128:131]\n\t" \
  "v_mfma_f32_16x16x32_bf16 v[136:139], v[200:203], v[92:95], v[136:139]\n\t" \
  "ds_read_b128 v[200:203], v211 offset:33024\n\t" \
  "s_add_u32 m0, %[lw], 101376\n\t" \
  "v_mfma_f32_16x16x32_bf16 v[132:135], v[204:207], v[88:91], v[132:135]\n\t" \
  "v_mfma_f32_16x16x32_bf16 v[140:143], v[204:207], v[92:95], v[140:143]\n\t" \
  "ds_read_b128 v[204:207], v211 offset:49408\n\t" \
  "global_load_lds_dwordx4 v219, s[92:93] sc1\n\t" \
  "s_cmp_lt_u32 s86, 8\n\t" \
  "s_cbranch_scc1 .Lw11_%=\n\t" \
  "s_waitcnt vmcnt(6)\n\t" \
  "s_barrier\n\t" \
  ".Lg11_%=:\n\t" \
  "s_add_u32 s92, s92, 65024\n\t" \
  "s_addc_u32 s93, s93, 0\n\t" \
  "s_waitcnt lgkmcnt(6)\n\t" \
  "v_mfma_f32_16x16x32_bf16 v[128:131], v[176:179], v[96:99], v[128:131]\n\t" \
  "v_mfma_f32_16x16x32_bf16 v[136:139], v[176:179], v[100:103], v[136:139]\n\t" \
  "ds_read_b128 v[176:179], v212 offset:0\n\t" \
  "v_mfma_f32_16x16x32_bf16 v[132:135], v[180:183], v[96:99], v[132:135]\n\t" \
  "v_mfma_f32_16x16x32_bf16 v[140:143], v[180:183], v[100:103], v[140:143]\n\t" \
  "ds_read_b128 v[180:183], v212 offset:16384\n\t" \
  "s_waitcnt lgkmcnt(6)\n\t" \
  "v_mfma_f32_16x16x32_bf16 v[128:131], v[184:187], v[104:107], v[128:131]\n\t" \
  "v_mfma_f32_16x16x32_bf16 v[136:139], v[184:187], v[108:111], v[136:139]\n\t" \
  "ds_read_b128 v[184:187], v213 offset:0\n\t" \
  "s_add_u32 m0, %[lw], 0\n\t" \
  "v_mfma_f32_16x16x32_bf16 v[132:135], v[188:191], v[104:107], v[132:135]\n\t" \
  "v_mfma_f32_16x16x32_bf16 v[140:143], v[188:191], v[108:111], v[140:143]\n\t" \
  "ds_read_b128 v[188:191], v213 offset:16384\n\t" \
  "global_load_lds_dwordx4 v216, s[92:93] sc1\n\t" \
  "s_waitcnt lgkmcnt(6)\n\t" \
  "v_mfma_f32_16x16x32_bf16 v[128:131], v[192:195], v[112:115], v[128:131]\n\t" \
  "v_mfma_f32_16x16x32_bf16 v[136:139], v[192:195], v[116:119], v[136:139]\n\t" \
  "ds_read_b128 v[192:195], v214 offset:0\n\t" \
  "v_mfma_f32_16x16x32_bf16 v[132:135], v[196:199], v[112:115], v[132:135]\n\t" \
  "v_mfma_f32_16x16x32_bf16 v[140:143], v[196:199], v[116:119], v[140:143]\n\t" \
  "ds_read_b128 v[196:199], v214 offset:16384\n\t" \
  "s_waitcnt lgkmcnt(6)\n\t" \
  "v_mfma_f32_16x16x32_bf16 v[128:131], v[200:203], v[120:123], v[128:131]\n\t" \
  "v_mfma_f32_16x16x32_bf16 v[136:139], v[200:203], v[124:127], v[136:139]\n\t" \
  "ds_read_b128 v[200:203], v215 offset:0\n\t" \
  "s_add_u32 m0, %[lw], 1024\n\t" \
  "v_mfma_f32_16x16x32_bf16 v[132:135], v[204:207], v[120:123], v[132:135]\n\t" \
  "v_mfma_f32_16x16x32_bf16 v[140:143], v[204:207], v[124:127], v[140:143]\n\t" \
  "ds_read_b128 v[204:207], v215 offset:16384\n\t" \
  "global_load_lds_dwordx4 v217, s[92:93] sc1\n\t" \
  "s_add_u32 s86, s86, 1\n\t" \
  "s_add_u32 s87, s87, 1\n\t" \
  "s_cmp_le_u32 s86, 5\n\t" \
  "s_cbranch_scc1 .Lf11_%=\n\t" \
  ".Lfb11_%=:\n\t" \
  "s_waitcnt lgkmcnt(6)\n\t" \
  "v_mfma_f32_16x16x32_bf16 v[144:147], v[176:179], v[0:3], 0\n\t" \
  "v_mfma_f32_16x16x32_bf16 v[152:155], v[176:179], v[4:7], 0\n\t" \
  "ds_read_b128 v[176:179], v212 offset:256\n\t" \
  "v_mfma_f32_16x16x32_bf16 v[148:151], v[180:183], v[0:3], 0\n\t" \
  "v_mfma_f32_16x16x32_bf16 v[156:159], v[180:183], v[4:7], 0\n\t" \
  "ds_read_b128 v[180:183], v212 offset:16640\n\t" \
  "s_waitcnt lgkmcnt(6)\n\t" \
  "v_mfma_f32_16x16x32_bf16 v[144:147], v[184:187], v[8:11], v[144:147]\n\t" \
  "v_mfma_f32_16x16x32_bf16 v[152:155], v[184:187], v[12:15], v[152:155]\n\t" \
  "ds_read_b128 v[184:187], v213 offset:256\n\t" \
  "s_add_u32 m0, %[lw], 2048\n\t" \
  "v_mfma_f32_16x16x32_bf16 v[148:151], v[188:191], v[8:11], v[148:151]\n\t" \
  "v_mfma_f32_16x16x32_bf16 v[156:159], v[188:191], v[12:15], v[156:159]\n\t" \
  "ds_read_b128 v[188:191], v213 offset:16640\n\t" \
  "global_load_lds_dwordx4 v218, s[92:93] sc1\n\t" \
  "ds_write_b128 v220, v[136:139] offset:0\n\t" \
  "ds_write_b128 v220, v[140:143] offset:1024\n\t" \
  "s_waitcnt lgkmcnt(8)\n\t" \
  "v_mfma_f32_16x16x32_bf16 v[144:147], v[192:195], v[16:19], v[144:147]\n\t" \
  "v_mfma_f32_16x16x32_bf16 v[152:155], v[192:195], v[20:23], v[152:155]\n\t" \
  "ds_read_b128 v[192:195], v214 offset:256\n\t" \
  "v_mfma_f32_16x16x32_bf16 v[148:151], v[196:199], v[16:19], v[148:151]\n\t" \
  "v_mfma_f32_16x16x32_bf16 v[156:159], v[196:199], v[20:23], v[156:159]\n\t" \
  "ds_read_b128 v[196:199], v214 offset:16640\n\t" \
  "s_lshr_b32 s90, s87, 7\n\t" \
  "s_xor_b32 s90, s90, %[s0]\n\t" \
  "s_and_b32 s90, s90, 1\n\t" \
  "s_lshl1_add_u32 s90, s87, s90\n\t" \
  "s_lshl_b32 s90, s90, 13\n\t" \
  "s_cmp_lt_i32 s86, 6\n\t" \
  "s_cselect_b32 s90, 0x70000000, s90\n\t" \
  "s_waitcnt lgkmcnt(8)\n\t" \
  "v_mfma_f32_16x16x32_bf16 v[144:147], v[200:203], v[24:27], v[144:147]\n\t" \
  "v_mfma_f32_16x16x32_bf16 v[152:155], v[200:203], v[28:31], v[152:155]\n\t" \
  "ds_read_b128 v[200:203], v215 offset:256\n\t" \
  "s_add_u32 m0, %[lw], 3072\n\t" \
  "v_mfma_f32_16x16x32_bf16 v[148:151], v[204:207], v[24:27], v[148:151]\n\t" \
  "v_mfma_f32_16x16x32_bf16 v[156:159], v[204:207], v[28:31], v[156:159]\n\t" \
  "ds_read_b128 v[204:207], v215 offset:16640\n\t" \
  "global_load_lds_dwordx4 v219, s[92:93] sc1\n\t" \
  "s_waitcnt lgkmcnt(4)\n\t" \
  "s_cmp_lt_u32 s86, 6\n\t" \
  "s_cbranch_scc1 .Lw21_%=\n\t" \
  "s_waitcnt vmcnt(4)\n\t" \
  "s_barrier\n\t" \
  ".Lg21_%=:\n\t" \
  "s_add_u32 s92, s92, 512\n\t" \
  "s_addc_u32 s93, s93, 0\n\t" \
  "v_mfma_f32_16x16x32_bf16 v[144:147], v[176:179], v[32:35], v[144:147]\n\t" \
  "v_mfma_f32_16x16x32_bf16 v[152:155], v[176:179], v[36:39], v[152:155]\n\t" \
  "ds_read_b128 v[176:179], v212 offset:32768\n\t" \
  "v_mfma_f32_16x16x32_bf16 v[148:151], v[180:183], v[32:35], v[148:151]\n\t" \
  "v_mfma_f32_16x16x32_bf16 v[156:159], v[180:183], v[36:39], v[156:159]\n\t" \
  "ds_read_b128 v[180:183], v212 offset:49152\n\t" \
  "ds_read_b128 v[160:163], v221 offset:0\n\t" \
  "ds_read_b128 v[164:167], v221 offset:1024\n\t" \
  "s_sub_i32 s91, s86, 2\n\t" \
  "s_max_i32 s88, s88, s91\n\t" \
  "v_mov_b32 v173, s88\n\t" \
  "buffer_store_dword v173, v174, %[rs], 0 offen sc1\n\t" \
  "v_mfma_f32_16x16x32_bf16 v[144:147], v[184:187], v[40:43], v[144:147]\n\t" \
  "v_mfma_f32_16x16x32_bf16 v[152:155], v[184:187], v[44:47], v[152:155]\n\t" \
  "ds_read_b128 v[184:187], v213 offset:32768\n\t" \
  "s_add_u32 m0, %[lw], 32768\n\t" \
  "v_mfma_f32_16x16x32_bf16 v[148:151], v[188:191], v[40:43], v[148:151]\n\t" \
  "v_mfma_f32_16x16x32_bf16 v[156:159], v[188:191], v[44:47], v[156:159]\n\t" \
  "ds_read_b128 v[188:191], v213 offset:49152\n\t" \
  "global_load_lds_dwordx4 v216, s[92:93] sc1\n\t" \
  "s_waitcnt lgkmcnt(8)\n\t" \
  "v_mfma_f32_16x16x32_bf16 v[144:147], v[192:195], v[48:51], v[144:147]\n\t" \
  "v_mfma_f32_16x16x32_bf16 v[152:155], v[192:195], v[52:55], v[152:155]\n\t" \
  "ds_read_b128 v[192:195], v214 offset:32768\n\t" \
  "v_mfma_f32_16x16x32_bf16 v[148:151], v[196:199], v[48:51], v[148:151]\n\t" \
  "v_mfma_f32_16x16x32_bf16 v[156:159], v[196:199], v[52:55], v[156:159]\n\t" \
  "ds_read_b128 v[196:199], v214 offset:49152\n\t" \
  "s_waitcnt lgkmcnt(4)\n\t" \
  "v_add_f32 v160, v160, v128\n\t" \
  "v_add_f32 v161, v161, v129\n\t" \
  "v_add_f32 v162, v162, v130\n\t" \
  "v_add_f32 v163, v163, v131\n\t" \
  "v_add_f32 v164, v164, v132\n\t" \
  "v_add_f32 v165, v165, v133\n\t" \
  "v_add_f32 v166, v166, v134\n\t" \
  "v_add_f32 v167, v167, v135\n\t" \
  "v_mfma_f32_16x16x32_bf16 v[144:147], v[200:203], v[56:59], v[144:147]\n\t" \
  "v_mfma_f32_16x16x32_bf16 v[152:155], v[200:203], v[60:63], v[152:155]\n\t" \
  "ds_read_b128 v[200:203], v215 offset:32768\n\t" \
  "s_add_u32 m0, %[lw], 33792\n\t" \
  "v_mfma_f32_16x16x32_bf16 v[148:151], v[204:207], v[56:59], v[148:151]\n\t" \
  "v_mfma_f32_16x16x32_bf16 v[156:159], v[204:207], v[60:63], v[156:159]\n\t" \
  "ds_read_b128 v[204:207], v215 offset:49152\n\t" \
  "global_load_lds_dwordx4 v217, s[92:93] sc1\n\t" \
  "v_cvt_pk_bf16_f32 v168, v160, v161\n\t" \
  "v_cvt_pk_bf16_f32 v169, v162, v163\n\t" \
  "v_cvt_pk_bf16_f32 v170, v164, v165\n\t" \
  "v_cvt_pk_bf16_f32 v171, v166, v167\n\t" \
  "buffer_store_dwordx4 v[168:171], v172, %[rt], s90 offen sc1\n\t" \
  "v_mfma_f32_16x16x32_bf16 v[144:147], v[176:179], v[64:67], v[144:147]\n\t" \
  "v_mfma_f32_16x16x32_bf16 v[152:155], v[176:179], v[68:71], v[152:155]\n\t" \
  "ds_read_b128 v[176:179], v212 offset:33024\n\t" \
  "v_mfma_f32_16x16x32_bf16 v[148:151], v[180:183], v[64:67], v[148:151]\n\t" \
  "v_mfma_f32_16x16x32_bf16 v[156:159], v[180:183], v[68:71], v[156:159]\n\t" \
  "ds_read_b128 v[180:183], v212 offset:49408\n\t" \
  "s_waitcnt lgkmcnt(6)\n\t" \
  "v_mfma_f32_16x16x32_bf16 v[144:147], v[184:187], v[72:75], v[144:147]\n\t" \
  "v_mfma_f32_16x16x32_bf16 v[152:155], v[184:187], v[76:79], v[152:155]\n\t" \
  "ds_read_b128 v[184:187], v213 offset:33024\n\t" \
  "s_add_u32 m0, %[lw], 34816\n\t" \
  "v_mfma_f32_16x16x32_bf16 v[148:151], v[188:191], v[72:75], v[148:151]\n\t" \
  "v_mfma_f32_16x16x32_bf16 v[156:159], v[188:191], v[76:79], v[156:159]\n\t" \
  "ds_read_b128 v[188:191], v213 offset:49408\n\t" \
  "global_load_lds_dwordx4 v218, s[92:93] sc1\n\t" \
  "s_waitcnt lgkmcnt(6)\n\t" \
  "v_mfma_f32_16x16x32_bf16 v[144:147], v[192:195], v[80:83], v[144:147]\n\t" \
  "v_mfma_f32_16x16x32_bf16 v[152:155], v[192:195], v[84:87], v[152:155]\n\t" \
  "ds_read_b128 v[192:195], v214 offset:33024\n\t" \
  "v_mfma_f32_16x16x32_bf16 v[148:151], v[196:199], v[80:83], v[148:151]\n\t" \
  "v_mfma_f32_16x16x32_bf16 v[156:159], v[196:199], v[84:87], v[156:159]\n\t" \
  "ds_read_b128 v[196:199], v214 offset:49408\n\t" \
  "s_waitcnt lgkmcnt(6)\n\t" \
  "v_mfma_f32_16x16x32_bf16 v[144:147], v[200:203], v[88:91], v[144:147]\n\t" \
  "v_mfma_f32_16x16x32_bf16 v[152:155], v[200:203], v[92:95], v[152:155]\n\t" \
  "ds_read_b128 v[200:203], v215 offset:33024\n\t" \
  "s_add_u32 m0, %[lw], 35840\n\t" \
  "v_mfma_f32_16x16x32_bf16 v[148:151], v[204:207], v[88:91], v[148:151]\n\t" \
  "v_mfma_f32_16x16x32_bf16 v[156:159], v[204:207], v[92:95], v[156:159]\n\t" \
  "ds_read_b128 v[204:207], v215 offset:49408\n\t" \
  "global_load_lds_dwordx4 v219, s[92:93] sc1\n\t" \
  "s_cmp_lt_u32 s86, 8\n\t" \
  "s_cbranch_scc1 .Lw31_%=\n\t" \
  "s_waitcnt vmcnt(6)\n\t" \
  "s_barrier\n\t" \
  ".Lg31_%=:\n\t" \
  "s_add_u32 s92, s92, 65024\n\t" \
  "s_addc_u32 s93, s93, 0\n\t" \
  "s_waitcnt lgkmcnt(6)\n\t" \
  "v_mfma_f32_16x16x32_bf16 v[144:147], v[176:179], v[96:99], v[144:147]\n\t" \
  "v_mfma_f32_16x16x32_bf16 v[152:155], v[176:179], v[100:103], v[152:155]\n\t" \
  "ds_read_b128 v[176:179], v208 offset:0\n\t" \
  "v_mfma_f32_16x16x32_bf16 v[148:151], v[180:183], v[96:99], v[148:151]\n\t" \
  "v_mfma_f32_16x16x32_bf16 v[156:159], v[180:183], v[100:103], v[156:159]\n\t" \
  "ds_read_b128 v[180:183], v208 offset:16384\n\t" \
  "s_waitcnt lgkmcnt(6)\n\t" \
  "v_mfma_f32_16x16x32_bf16 v[144:147], v[184:187], v[104:107], v[144:147]\n\t" \
  "v_mfma_f32_16x16x32_bf16 v[152:155], v[184:187], v[108:111], v[152:155]\n\t" \
  "ds_read_b128 v[184:187], v209 offset:0\n\t" \
  "s_add_u32 m0, %[lw], 65536\n\t" \
  "v_mfma_f32_16x16x32_bf16 v[148:151], v[188:191], v[104:107], v[148:151]\n\t" \
  "v_mfma_f32_16x16x32_bf16 v[156:159], v[188:191], v[108:111], v[156:159]\n\t" \
  "ds_read_b128 v[188:191], v209 offset:16384\n\t" \
  "global_load_lds_dwordx4 v216, s[92:93] sc1\n\t" \
  "s_waitcnt lgkmcnt(6)\n\t" \
  "v_mfma_f32_16x16x32_bf16 v[144:147], v[192:195], v[112:115], v[144:147]\n\t" \
  "v_mfma_f32_16x16x32_bf16 v[152:155], v[192:195], v[116:119], v[152:155]\n\t" \
  "ds_read_b128 v[192:195], v210 offset:0\n\t" \
  "v_mfma_f32_16x16x32_bf16 v[148:151], v[196:199], v[112:115], v[148:151]\n\t" \
  "v_mfma_f32_16x16x32_bf16 v[156:159], v[196:199], v[116:119], v[156:159]\n\t" \
  "ds_read_b128 v[196:199], v210 offset:16384\n\t" \
  "s_waitcnt lgkmcnt(6)\n\t" \
  "v_mfma_f32_16x16x32_bf16 v[144:147], v[200:203], v[120:123], v[144:147]\n\t" \
  "v_mfma_f32_16x16x32_bf16 v[152:155], v[200:203], v[124:127], v[152:155]\n\t" \
  "ds_read_b128 v[200:203], v211 offset:0\n\t" \
  "s_add_u32 m0, %[lw], 66560\n\t" \
  "v_mfma_f32_16x16x32_bf16 v[148:151], v[204:207], v[120:123], v[148:151]\n\t" \
  "v_mfma_f32_16x16x32_bf16 v[156:159], v[204:207], v[124:127], v[156:159]\n\t" \
  "ds_read_b128 v[204:207], v211 offset:16384\n\t" \
  "global_load_lds_dwordx4 v217, s[92:93] sc1\n\t" \
  "s_add_u32 s86, s86, 1\n\t" \
  "s_add_u32 s87, s87, 1\n\t" \
  "s_cmp_lt_u32 s86, %[end]\n\t" \
  "s_cbranch_scc1 .Lp1_%=\n\t" \
  "s_branch .Lx1_%=\n\t" \
  ".Lf01_%=:\n\t" \
  "s_nop 7\n\t" \
  "ds_write_b128 v220, v[152:155] offset:0\n\t" \
  "ds_write_b128 v220, v[156:159] offset:1024\n\t" \
  "s_lshr_b32 s90, s87, 7\n\t" \
  "s_xor_b32 s90, s90, %[s0]\n\t" \
  "s_and_b32 s90, s90, 1\n\t" \
  "s_lshl1_add_u32 s90, s87, s90\n\t" \
  "s_lshl_b32 s90, s90, 13\n\t" \
  "s_cmp_lt_i32 s86, 1\n\t" \
  "s_cselect_b32 s90, 0x70000000, s90\n\t" \
  "s_waitcnt lgkmcnt(0)\n\t" \
  "s_barrier\n\t" \
  "ds_read_b128 v[160:163], v221 offset:0\n\t" \
  "ds_read_b128 v[164:167], v221 offset:1024\n\t" \
  "s_waitcnt lgkmcnt(0)\n\t" \
  "v_add_f32 v160, v160, v144\n\t" \
  "v_add_f32 v161, v161, v145\n\t" \
  "v_add_f32 v162, v162, v146\n\t" \
  "v_add_f32 v163, v163, v147\n\t" \
  "v_add_f32 v164, v164, v148\n\t" \
  "v_add_f32 v165, v165, v149\n\t" \
  "v_add_f32 v166, v166, v150\n\t" \
  "v_add_f32 v167, v167, v151\n\t" \
  "v_cvt_pk_bf16_f32 v168, v160, v161\n\t" \
  "v_cvt_pk_bf16_f32 v169, v162, v163\n\t" \
  "v_cvt_pk_bf16_f32 v170, v164, v165\n\t" \
  "v_cvt_pk_bf16_f32 v171, v166, v167\n\t" \
  "buffer_store_dwordx4 v[168:171], v172, %[rt], s90 offen sc1\n\t" \
  "s_branch .Lfb01_%=\n\t" \
  ".Lf11_%=:\n\t" \
  "s_nop 7\n\t" \
  "ds_write_b128 v220, v[136:139] offset:0\n\t" \
  "ds_write_b128 v220, v[140:143] offset:1024\n\t" \
  "s_lshr_b32 s90, s87, 7\n\t" \
  "s_xor_b32 s90, s90, %[s0]\n\t" \
  "s_and_b32 s90, s90, 1\n\t" \
  "s_lshl1_add_u32 s90, s87, s90\n\t" \
  "s_lshl_b32 s90, s90, 13\n\t" \
  "s_cmp_lt_i32 s86, 1\n\t" \
  "s_cselect_b32 s90, 0x70000000, s90\n\t" \
  "s_waitcnt lgkmcnt(0)\n\t" \
  "s_barrier\n\t" \
  "ds_read_b128 v[160:163], v221 offset:0\n\t" \
  "ds_read_b128 v[164:167], v221 offset:1024\n\t" \
  "s_waitcnt lgkmcnt(0)\n\t" \
  "v_add_f32 v160, v160, v128\n\t" \
  "v_add_f32 v161, v161, v129\n\t" \
  "v_add_f32 v162, v162, v130\n\t" \
  "v_add_f32 v163, v163, v131\n\t" \
  "v_add_f32 v164, v164, v132\n\t" \
  "v_add_f32 v165, v165, v133\n\t" \
  "v_add_f32 v166, v166, v134\n\t" \
  "v_add_f32 v167, v167, v135\n\t" \
  "v_cvt_pk_bf16_f32 v168, v160, v161\n\t" \
  "v_cvt_pk_bf16_f32 v169, v162, v163\n\t" \
  "v_cvt_pk_bf16_f32 v170, v164, v165\n\t" \
  "v_cvt_pk_bf16_f32 v171, v166, v167\n\t" \
  "buffer_store_dwordx4 v[168:171], v172, %[rt], s90 offen sc1\n\t" \
  "s_branch .Lfb11_%=\n\t" \
  ".Lw01_%=:\n\t" \
  "s_waitcnt vmcnt(2)\n\t" \
  "s_barrier\n\t" \
  "s_sub_i32 s91, s86, 1\n\t" \
  "s_max_i32 s88, s88, s91\n\t" \
  "v_mov_b32 v173, s88\n\t" \
  "buffer_store_dword v173, v174, %[rs], 0 offen sc1\n\t" \
  "s_branch .Lg01_%=\n\t" \
  ".Lw11_%=:\n\t" \
  "s_waitcnt vmcnt(2)\n\t" \
  "s_barrier\n\t" \
  "s_sub_i32 s91, s86, 1\n\t" \
  "s_max_i32 s88, s88, s91\n\t" \
  "v_mov_b32 v173, s88\n\t" \
  "buffer_store_dword v173, v174, %[rs], 0 offen sc1\n\t" \
  "s_branch .Lg11_%=\n\t" \
  ".Lw21_%=:\n\t" \
  "s_waitcnt vmcnt(2)\n\t" \
  "s_barrier\n\t" \
  "s_sub_i32 s91, s86, 1\n\t" \
  "s_max_i32 s88, s88, s91\n\t" \
  "v_mov_b32 v173, s88\n\t" \
  "buffer_store_dword v173, v174, %[rs], 0 offen sc1\n\t" \
  "s_branch .Lg21_%=\n\t" \
  ".Lw31_%=:\n\t" \
  "s_waitcnt vmcnt(2)\n\t" \
  "s_barrier\n\t" \
  "s_sub_i32 s91, s86, 1\n\t" \
  "s_max_i32 s88, s88, s91\n\t" \
  "v_mov_b32 v173, s88\n\t" \
  "buffer_store_dword v173, v174, %[rs], 0 offen sc1\n\t" \
  "s_branch .Lg31_%=\n\t" \
  ".Lx1_%=:\n\t" \
  "s_waitcnt lgkmcnt(0)\n\t" \
  "s_setprio 0\n\t" \
  "s_mov_b32 m0, s94\n\t" \
  ".Le_%=:\n\t"
#define PP_ASM_0 \
  "s_cmp_lg_u32 %[kh], 0\n\t" \
  "s_cbranch_scc1 .Lk_%=\n\t" \
  "s_mov_b32 s94, m0\n\t" \
  "s_mov_b64 s[92:93], %[x0]\n\t" \
  "s_mov_b32 s86, %[g0]\n\t" \
  "s_add_u32 s87, s86, %[t0]\n\t" \
  "s_add_u32 m0, %[lw], 67584\n\t" \
  "s_nop 0\n\t" \
  "global_load_lds_dwordx4 v250, s[92:93] sc1\n\t" \
  "s_add_u32 m0, %[lw], 68608\n\t" \
  "s_nop 0\n\t" \
  "global_load_lds_dwordx4 v251, s[92:93] sc1\n\t" \
  "s_barrier\n\t" \
  "s_branch .Ls0_%=\n\t" \
  ".Lp0_%=:\n\t" \
  "s_waitcnt vmcnt(9)\n\t" \
  "s_barrier\n\t" \
  ".Ls0_%=:\n\t" \
  "s_add_u32 s92, s92, 512\n\t" \
  "s_addc_u32 s93, s93, 0\n\t" \
  "ds_read_b128 v[176:179], v240 offset:0\n\t" \
  "ds_read_b128 v[180:183], v240 offset:16384\n\t" \
  "ds_read_b128 v[184:187], v241 offset:0\n\t" \
  "ds_read_b128 v[188:191], v241 offset:16384\n\t" \
  "s_add_u32 m0, %[lw], 98304\n\t" \
  "s_nop 0\n\t" \
  "global_load_lds_dwordx4 v248, s[92:93] sc1\n\t" \
  "ds_read_b128 v[192:195], v242 offset:0\n\t" \
  "ds_read_b128 v[196:199], v242 offset:16384\n\t" \
  "ds_read_b128 v[200:203], v243 offset:0\n\t" \
  "ds_read_b128 v[204:207], v243 offset:16384\n\t" \
  "s_add_u32 m0, %[lw], 99328\n\t" \
  "s_nop 0\n\t" \
  "global_load_lds_dwordx4 v249, s[92:93] sc1\n\t" \
  "ds_read_b128 v[208:211], v240 offset:256\n\t" \
  "ds_read_b128 v[212:215], v240 offset:16640\n\t" \
  "ds_read_b128 v[216:219], v241 offset:256\n\t" \
  "ds_read_b128 v[220:223], v241 offset:16640\n\t" \
  "s_add_u32 m0, %[lw], 100352\n\t" \
  "s_nop 0\n\t" \
  "global_load_lds_dwordx4 v250, s[92:93] sc1\n\t" \
  "ds_read_b128 v[224:227], v242 offset:256\n\t" \
  "ds_read_b128 v[228:231], v242 offset:16640\n\t" \
  "ds_read_b128 v[232:235], v243 offset:256\n\t" \
  "ds_read_b128 v[236:239], v243 offset:16640\n\t" \
  "s_add_u32 m0, %[lw], 101376\n\t" \
  "s_nop 0\n\t" \
  "global_load_lds_dwordx4 v251, s[92:93] sc1\n\t" \
  "ds_write_b128 v252, v[152:155] offset:0\n\t" \
  "ds_write_b128 v252, v[156:159] offset:1024\n\t" \
  "s_waitcnt lgkmcnt(0)\n\t" \
  "s_setprio 1\n\t" \
  "v_mfma_f32_16x16x32_bf16 v[128:131], v[0:3], v[176:179], 0\n\t" \
  "v_mfma_f32_16x16x32_bf16 v[132:135], v[4:7], v[176:179], 0\n\t" \
  "v_mfma_f32_16x16x32_bf16 v[136:139], v[0:3], v[180:183], 0\n\t" \
  "v_mfma_f32_16x16x32_bf16 v[140:143], v[4:7], v[180:183], 0\n\t" \
  "v_mfma_f32_16x16x32_bf16 v[128:131], v[8:11], v[184:187], v[128:131]\n\t" \
  "v_mfma_f32_16x16x32_bf16 v[132:135], v[12:15], v[184:187], v[132:135]\n\t" \
  "v_mfma_f32_16x16x32_bf16 v[136:139], v[8:11], v[188:191], v[136:139]\n\t" \
  "v_mfma_f32_16x16x32_bf16 v[140:143], v[12:15], v[188:191], v[140:143]\n\t" \
  "v_mfma_f32_16x16x32_bf16 v[128:131], v[16:19], v[192:195], v[128:131]\n\t" \
  "v_mfma_f32_16x16x32_bf16 v[132:135], v[20:23], v[192:195], v[132:135]\n\t" \
  "v_mfma_f32_16x16x32_bf16 v[136:139], v[16:19], v[196:199], v[136:139]\n\t" \
  "v_mfma_f32_16x16x32_bf16 v[140:143], v[20:23], v[196:199], v[140:143]\n\t" \
  "v_mfma_f32_16x16x32_bf16 v[128:131], v[24:27], v[200:203], v[128:131]\n\t" \
  "v_mfma_f32_16x16x32_bf16 v[132:135], v[28:31], v[200:203], v[132:135]\n\t" \
  "v_mfma_f32_16x16x32_bf16 v[136:139], v[24:27], v[204:207], v[136:139]\n\t" \
  "v_mfma_f32_16x16x32_bf16 v[140:143], v[28:31], v[204:207], v[140:143]\n\t" \
  "v_mfma_f32_16x16x32_bf16 v[128:131], v[32:35], v[208:211], v[128:131]\n\t" \
  "v_mfma_f32_16x16x32_bf16 v[132:135], v[36:39], v[208:211], v[132:135]\n\t" \
  "v_mfma_f32_16x16x32_bf16 v[136:139], v[32:35], v[212:215], v[136:139]\n\t" \
  "v_mfma_f32_16x16x32_bf16 v[140:143], v[36:39], v[212:215], v[140:143]\n\t" \
  "v_mfma_f32_16x16x32_bf16 v[128:131], v[40:43], v[216:219], v[128:131]\n\t" \
  "v_mfma_f32_16x16x32_bf16 v[132:135], v[44:47], v[216:219], v[132:135]\n\t" \
  "v_mfma_f32_16x16x32_bf16 v[136:139], v[40:43], v[220:223], v[136:139]\n\t" \
  "v_mfma_f32_16x16x32_bf16 v[140:143], v[44:47], v[220:223], v[140:143]\n\t" \
  "v_mfma_f32_16x16x32_bf16 v[128:131], v[48:51], v[224:227], v[128:131]\n\t" \
  "v_mfma_f32_16x16x32_bf16 v[132:135], v[52:55], v[224:227], v[132:135]\n\t" \
  "v_mfma_f32_16x16x32_bf16 v[136:139], v[48:51], v[228:231], v[136:139]\n\t" \
  "v_mfma_f32_16x16x32_bf16 v[140:143], v[52:55], v[228:231], v[140:143]\n\t" \
  "v_mfma_f32_16x16x32_bf16 v[128:131], v[56:59], v[232:235], v[128:131]\n\t" \
  "v_mfma_f32_16x16x32_bf16 v[132:135], v[60:63], v[232:235], v[132:135]\n\t" \
  "v_mfma_f32_16x16x32_bf16 v[136:139], v[56:59], v[236:239], v[136:139]\n\t" \
  "v_mfma_f32_16x16x32_bf16 v[140:143], v[60:63], v[236:239], v[140:143]\n\t" \
  "s_setprio 0\n\t" \
  "s_waitcnt vmcnt(4)\n\t" \
  "s_barrier\n\t" \
  "s_add_u32 s92, s92, 65024\n\t" \
  "s_addc_u32 s93, s93, 0\n\t" \
  "ds_read_b128 v[160:163], v253 offset:0\n\t" \
  "ds_read_b128 v[164:167], v253 offset:1024\n\t" \
  "ds_read_b128 v[176:179], v240 offset:32768\n\t" \
  "ds_read_b128 v[180:183], v240 offset:49152\n\t" \
  "ds_read_b128 v[184:187], v241 offset:32768\n\t" \
  "ds_read_b128 v[188:191], v241 offset:49152\n\t" \
  "s_add_u32 m0, %[lw], 0\n\t" \
  "s_nop 0\n\t" \
  "global_load_lds_dwordx4 v248, s[92:93] sc1\n\t" \
  "ds_read_b128 v[192:195], v242 offset:32768\n\t" \
  "ds_read_b128 v[196:199], v242 offset:49152\n\t" \
  "ds_read_b128 v[200:203], v243 offset:32768\n\t" \
  "ds_read_b128 v[204:207], v243 offset:49152\n\t" \
  "s_add_u32 m0, %[lw], 1024\n\t" \
  "s_nop 0\n\t" \
  "global_load_lds_dwordx4 v249, s[92:93] sc1\n\t" \
  "ds_read_b128 v[208:211], v240 offset:33024\n\t" \
  "ds_read_b128 v[212:215], v240 offset:49408\n\t" \
  "ds_read_b128 v[216:219], v241 offset:33024\n\t" \
  "ds_read_b128 v[220:223], v241 offset:49408\n\t" \
  "s_add_u32 m0, %[lw], 2048\n\t" \
  "s_nop 0\n\t" \
  "global_load_lds_dwordx4 v250, s[92:93] sc1\n\t" \
  "ds_read_b128 v[224:227], v242 offset:33024\n\t" \
  "ds_read_b128 v[228:231], v242 offset:49408\n\t" \
  "ds_read_b128 v[232:235], v243 offset:33024\n\t" \
  "ds_read_b128 v[236:239], v243 offset:49408\n\t" \
  "s_add_u32 m0, %[lw], 3072\n\t" \
  "s_nop 0\n\t" \
  "global_load_lds_dwordx4 v251, s[92:93] sc1\n\t" \
  "s_waitcnt lgkmcnt(15)\n\t" \
  "s_waitcnt lgkmcnt(12)\n\t" \
  "s_lshr_b32 s90, s87, 7\n\t" \
  "s_xor_b32 s90, s90, %[s0]\n\t" \
  "s_and_b32 s90, s90, 1\n\t" \
  "s_lshl1_add_u32 s90, s87, s90\n\t" \
  "s_lshl_b32 s90, s90, 13\n\t" \
  "v_add_f32 v160, v160, v144\n\t" \
  "v_add_f32 v161, v161, v145\n\t" \
  "v_add_f32 v162, v162, v146\n\t" \
  "v_add_f32 v163, v163, v147\n\t" \
  "v_add_f32 v164, v164, v148\n\t" \
  "v_add_f32 v165, v165, v149\n\t" \
  "v_add_f32 v166, v166, v150\n\t" \
  "v_add_f32 v167, v167, v151\n\t" \
  "v_cvt_pk_bf16_f32 v168, v160, v161\n\t" \
  "v_cvt_pk_bf16_f32 v169, v162, v163\n\t" \
  "v_cvt_pk_bf16_f32 v170, v164, v165\n\t" \
  "v_cvt_pk_bf16_f32 v171, v166, v167\n\t" \
  "buffer_store_dwordx4 v[168:171], v172, %[rt], s90 offen sc1\n\t" \
  "s_add_u32 s86, s86, 1\n\t" \
  "s_add_u32 s87, s87, 1\n\t" \
  "s_waitcnt lgkmcnt(0)\n\t" \
  "s_setprio 1\n\t" \
  "v_mfma_f32_16x16x32_bf16 v[128:131], v[64:67], v[176:179], v[128:131]\n\t" \
  "v_mfma_f32_16x16x32_bf16 v[132:135], v[68:71], v[176:179], v[132:135]\n\t" \
  "v_mfma_f32_16x16x32_bf16 v[136:139], v[64:67], v[180:183], v[136:139]\n\t" \
  "v_mfma_f32_16x16x32_bf16 v[140:143], v[68:71], v[180:183], v[140:143]\n\t" \
  "v_mfma_f32_16x16x32_bf16 v[128:131], v[72:75], v[184:187], v[128:131]\n\t" \
  "v_mfma_f32_16x16x32_bf16 v[132:135], v[76:79], v[184:187], v[132:135]\n\t" \
  "v_mfma_f32_16x16x32_bf16 v[136:139], v[72:75], v[188:191], v[136:139]\n\t" \
  "v_mfma_f32_16x16x32_bf16 v[140:143], v[76:79], v[188:191], v[140:143]\n\t" \
  "v_mfma_f32_16x16x32_bf16 v[128:131], v[80:83], v[192:195], v[128:131]\n\t" \
  "v_mfma_f32_16x16x32_bf16 v[132:135], v[84:87], v[192:195], v[132:135]\n\t" \
  "v_mfma_f32_16x16x32_bf16 v[136:139], v[80:83], v[196:199], v[136:139]\n\t" \
  "v_mfma_f32_16x16x32_bf16 v[140:143], v[84:87], v[196:199], v[140:143]\n\t" \
  "v_mfma_f32_16x16x32_bf16 v[128:131], v[88:91], v[200:203], v[128:131]\n\t" \
  "v_mfma_f32_16x16x32_bf16 v[132:135], v[92:95], v[200:203], v[132:135]\n\t" \
  "v_mfma_f32_16x16x32_bf16 v[136:139], v[88:91], v[204:207], v[136:139]\n\t" \
  "v_mfma_f32_16x16x32_bf16 v[140:143], v[92:95], v[204:207], v[140:143]\n\t" \
  "v_mfma_f32_16x16x32_bf16 v[128:131], v[96:99], v[208:211], v[128:131]\n\t" \
  "v_mfma_f32_16x16x32_bf16 v[132:135], v[100:103], v[208:211], v[132:135]\n\t" \
  "v_mfma_f32_16x16x32_bf16 v[136:139], v[96:99], v[212:215], v[136:139]\n\t" \
  "v_mfma_f32_16x16x32_bf16 v[140:143], v[100:103], v[212:215], v[140:143]\n\t" \
  "v_mfma_f32_16x16x32_bf16 v[128:131], v[104:107], v[216:219], v[128:131]\n\t" \
  "v_mfma_f32_16x16x32_bf16 v[132:135], v[108:111], v[216:219], v[132:135]\n\t" \
  "v_mfma_f32_16x16x32_bf16 v[136:139], v[104:107], v[220:223], v[136:139]\n\t" \
  "v_mfma_f32_16x16x32_bf16 v[140:143], v[108:111], v[220:223], v[140:143]\n\t" \
  "v_mfma_f32_16x16x32_bf16 v[128:131], v[112:115], v[224:227], v[128:131]\n\t" \
  "v_mfma_f32_16x16x32_bf16 v[132:135], v[116:119], v[224:227], v[132:135]\n\t" \
  "v_mfma_f32_16x16x32_bf16 v[136:139], v[112:115], v[228:231], v[136:139]\n\t" \
  "v_mfma_f32_16x16x32_bf16 v[140:143], v[116:119], v[228:231], v[140:143]\n\t" \
  "v_mfma_f32_16x16x32_bf16 v[128:131], v[120:123], v[232:235], v[128:131]\n\t" \
  "v_mfma_f32_16x16x32_bf16 v[132:135], v[124:127], v[232:235], v[132:135]\n\t" \
  "v_mfma_f32_16x16x32_bf16 v[136:139], v[120:123], v[236:239], v[136:139]\n\t" \
  "v_mfma_f32_16x16x32_bf16 v[140:143], v[124:127], v[236:239], v[140:143]\n\t" \
  "s_setprio 0\n\t" \
  "s_waitcnt vmcnt(9)\n\t" \
  "s_barrier\n\t" \
  "s_add_u32 s92, s92, 512\n\t" \
  "s_addc_u32 s93, s93, 0\n\t" \
  "ds_read_b128 v[176:179], v244 offset:0\n\t" \
  "ds_read_b128 v[180:183], v244 offset:16384\n\t" \
  "ds_read_b128 v[184:187], v245 offset:0\n\t" \
  "ds_read_b128 v[188:191], v245 offset:16384\n\t" \
  "s_add_u32 m0, %[lw], 32768\n\t" \
  "s_nop 0\n\t" \
  "global_load_lds_dwordx4 v248, s[92:93] sc1\n\t" \
  "ds_read_b128 v[192:195], v246 offset:0\n\t" \
  "ds_read_b128 v[196:199], v246 offset:16384\n\t" \
  "ds_read_b128 v[200:203], v247 offset:0\n\t" \
  "ds_read_b128 v[204:207], v247 offset:16384\n\t" \
  "s_add_u32 m0, %[lw], 33792\n\t" \
  "s_nop 0\n\t" \
  "global_load_lds_dwordx4 v249, s[92:93] sc1\n\t" \
  "ds_read_b128 v[208:211], v244 offset:256\n\t" \
  "ds_read_b128 v[212:215], v244 offset:16640\n\t" \
  "ds_read_b128 v[216:219], v245 offset:256\n\t" \
  "ds_read_b128 v[220:223], v245 offset:16640\n\t" \
  "s_add_u32 m0, %[lw], 34816\n\t" \
  "s_nop 0\n\t" \
  "global_load_lds_dwordx4 v250, s[92:93] sc1\n\t" \
  "ds_read_b128 v[224:227], v246 offset:256\n\t" \
  "ds_read_b128 v[228:231], v246 offset:16640\n\t" \
  "ds_read_b128 v[232:235], v247 offset:256\n\t" \
  "ds_read_b128 v[236:239], v247 offset:16640\n\t" \
  "s_add_u32 m0, %[lw], 35840\n\t" \
  "s_nop 0\n\t" \
  "global_load_lds_dwordx4 v251, s[92:93] sc1\n\t" \
  "ds_write_b128 v252, v[136:139] offset:0\n\t" \
  "ds_write_b128 v252, v[140:143] offset:1024\n\t" \
  "s_waitcnt lgkmcnt(0)\n\t" \
  "s_setprio 1\n\t" \
  "v_mfma_f32_16x16x32_bf16 v[144:147], v[0:3], v[176:179], 0\n\t" \
  "v_mfma_f32_16x16x32_bf16 v[148:151], v[4:7], v[176:179], 0\n\t" \
  "v_mfma_f32_16x16x32_bf16 v[152:155], v[0:3], v[180:183], 0\n\t" \
  "v_mfma_f32_16x16x32_bf16 v[156:159], v[4:7], v[180:183], 0\n\t" \
  "v_mfma_f32_16x16x32_bf16 v[144:147], v[8:11], v[184:187], v[144:147]\n\t" \
  "v_mfma_f32_16x16x32_bf16 v[148:151], v[12:15], v[184:187], v[148:151]\n\t" \
  "v_mfma_f32_16x16x32_bf16 v[152:155], v[8:11], v[188:191], v[152:155]\n\t" \
  "v_mfma_f32_16x16x32_bf16 v[156:159], v[12:15], v[188:191], v[156:159]\n\t" \
  "v_mfma_f32_16x16x32_bf16 v[144:147], v[16:19], v[192:195], v[144:147]\n\t" \
  "v_mfma_f32_16x16x32_bf16 v[148:151], v[20:23], v[192:195], v[148:151]\n\t" \
  "v_mfma_f32_16x16x32_bf16 v[152:155], v[16:19], v[196:199], v[152:155]\n\t" \
  "v_mfma_f32_16x16x32_bf16 v[156:159], v[20:23], v[196:199], v[156:159]\n\t" \
  "v_mfma_f32_16x16x32_bf16 v[144:147], v[24:27], v[200:203], v[144:147]\n\t" \
  "v_mfma_f32_16x16x32_bf16 v[148:151], v[28:31], v[200:203], v[148:151]\n\t" \
  "v_mfma_f32_16x16x32_bf16 v[152:155], v[24:27], v[204:207], v[152:155]\n\t" \
  "v_mfma_f32_16x16x32_bf16 v[156:159], v[28:31], v[204:207], v[156:159]\n\t" \
  "v_mfma_f32_16x16x32_bf16 v[144:147], v[32:35], v[208:211], v[144:147]\n\t" \
  "v_mfma_f32_16x16x32_bf16 v[148:151], v[36:39], v[208:211], v[148:151]\n\t" \
  "v_mfma_f32_16x16x32_bf16 v[152:155], v[32:35], v[212:215], v[152:155]\n\t" \
  "v_mfma_f32_16x16x32_bf16 v[156:159], v[36:39], v[212:215], v[156:159]\n\t" \
  "v_mfma_f32_16x16x32_bf16 v[144:147], v[40:43], v[216:219], v[144:147]\n\t" \
  "v_mfma_f32_16x16x32_bf16 v[148:151], v[44:47], v[216:219], v[148:151]\n\t" \
  "v_mfma_f32_16x16x32_bf16 v[152:155], v[40:43], v[220:223], v[152:155]\n\t" \
  "v_mfma_f32_16x16x32_bf16 v[156:159], v[44:47], v[220:223], v[156:159]\n\t" \
  "v_mfma_f32_16x16x32_bf16 v[144:147], v[48:51], v[224:227], v[144:147]\n\t" \
  "v_mfma_f32_16x16x32_bf16 v[148:151], v[52:55], v[224:227], v[148:151]\n\t" \
  "v_mfma_f32_16x16x32_bf16 v[152:155], v[48:51], v[228:231], v[152:155]\n\t" \
  "v_mfma_f32_16x16x32_bf16 v[156:159], v[52:55], v[228:231], v[156:159]\n\t" \
  "v_mfma_f32_16x16x32_bf16 v[144:147], v[56:59], v[232:235], v[144:147]\n\t" \
  "v_mfma_f32_16x16x32_bf16 v[148:151], v[60:63], v[232:235], v[148:151]\n\t" \
  "v_mfma_f32_16x16x32_bf16 v[152:155], v[56:59], v[236:239], v[152:155]\n\t" \
  "v_mfma_f32_16x16x32_bf16 v[156:159], v[60:63], v[236:239], v[156:159]\n\t" \
  "s_setprio 0\n\t" \
  "s_waitcnt vmcnt(4)\n\t" \
  "s_barrier\n\t" \
  "s_add_u32 s92, s92, 65024\n\t" \
  "s_addc_u32 s93, s93, 0\n\t" \
  "ds_read_b128 v[160:163], v253 offset:0\n\t" \
  "ds_read_b128 v[164:167], v253 offset:1024\n\t" \
  "ds_read_b128 v[176:179], v244 offset:32768\n\t" \
  "ds_read_b128 v[180:183], v244 offset:49152\n\t" \
  "ds_read_b128 v[184:187], v245 offset:32768\n\t" \
  "ds_read_b128 v[188:191], v245 offset:49152\n\t" \
  "s_add_u32 m0, %[lw], 65536\n\t" \
  "s_nop 0\n\t" \
  "global_load_lds_dwordx4 v248, s[92:93] sc1\n\t" \
  "ds_read_b128 v[192:195], v246 offset:32768\n\t" \
  "ds_read_b128 v[196:199], v246 offset:49152\n\t" \
  "ds_read_b128 v[200:203], v247 offset:32768\n\t" \
  "ds_read_b128 v[204:207], v247 offset:49152\n\t" \
  "s_add_u32 m0, %[lw], 66560\n\t" \
  "s_nop 0\n\t" \
  "global_load_lds_dwordx4 v249, s[92:93] sc1\n\t" \
  "ds_read_b128 v[208:211], v244 offset:33024\n\t" \
  "ds_read_b128 v[212:215], v244 offset:49408\n\t" \
  "ds_read_b128 v[216:219], v245 offset:33024\n\t" \
  "ds_read_b128 v[220:223], v245 offset:49408\n\t" \
  "s_add_u32 m0, %[lw], 67584\n\t" \
  "s_nop 0\n\t" \
  "global_load_lds_dwordx4 v250, s[92:93] sc1\n\t" \
  "ds_read_b128 v[224:227], v246 offset:33024\n\t" \
  "ds_read_b128 v[228:231], v246 offset:49408\n\t" \
  "ds_read_b128 v[232:235], v247 offset:33024\n\t" \
  "ds_read_b128 v[236:239], v247 offset:49408\n\t" \
  "s_add_u32 m0, %[lw], 68608\n\t" \
  "s_nop 0\n\t" \
  "global_load_lds_dwordx4 v251, s[92:93] sc1\n\t" \
  "s_waitcnt lgkmcnt(15)\n\t" \
  "s_waitcnt lgkmcnt(12)\n\t" \
  "s_lshr_b32 s90, s87, 7\n\t" \
  "s_xor_b32 s90, s90, %[s0]\n\t" \
  "s_and_b32 s90, s90, 1\n\t" \
  "s_lshl1_add_u32 s90, s87, s90\n\t" \
  "s_lshl_b32 s90, s90, 13\n\t" \
  "v_add_f32 v160, v160, v128\n\t" \
  "v_add_f32 v161, v161, v129\n\t" \
  "v_add_f32 v162, v162, v130\n\t" \
  "v_add_f32 v163, v163, v131\n\t" \
  "v_add_f32 v164, v164, v132\n\t" \
  "v_add_f32 v165, v165, v133\n\t" \
  "v_add_f32 v166, v166, v134\n\t" \
  "v_add_f32 v167, v167, v135\n\t" \
  "v_cvt_pk_bf16_f32 v168, v160, v161\n\t" \
  "v_cvt_pk_bf16_f32 v169, v162, v163\n\t" \
  "v_cvt_pk_bf16_f32 v170, v164, v165\n\t" \
  "v_cvt_pk_bf16_f32 v171, v166, v167\n\t" \
  "buffer_store_dwordx4 v[168:171], v172, %[rt], s90 offen sc1\n\t" \
  "s_add_u32 s86, s86, 1\n\t" \
  "s_add_u32 s87, s87, 1\n\t" \
  "s_waitcnt lgkmcnt(0)\n\t" \
  "s_setprio 1\n\t" \
  "v_mfma_f32_16x16x32_bf16 v[144:147], v[64:67], v[176:179], v[144:147]\n\t" \
  "v_mfma_f32_16x16x32_bf16 v[148:151], v[68:71], v[176:179], v[148:151]\n\t" \
  "v_mfma_f32_16x16x32_bf16 v[152:155], v[64:67], v[180:183], v[152:155]\n\t" \
  "v_mfma_f32_16x16x32_bf16 v[156:159], v[68:71], v[180:183], v[156:159]\n\t" \
  "v_mfma_f32_16x16x32_bf16 v[144:147], v[72:75], v[184:187], v[144:147]\n\t" \
  "v_mfma_f32_16x16x32_bf16 v[148:151], v[76:79], v[184:187], v[148:151]\n\t" \
  "v_mfma_f32_16x16x32_bf16 v[152:155], v[72:75], v[188:191], v[152:155]\n\t" \
  "v_mfma_f32_16x16x32_bf16 v[156:159], v[76:79], v[188:191], v[156:159]\n\t" \
  "v_mfma_f32_16x16x32_bf16 v[144:147], v[80:83], v[192:195], v[144:147]\n\t" \
  "v_mfma_f32_16x16x32_bf16 v[148:151], v[84:87], v[192:195], v[148:151]\n\t" \
  "v_mfma_f32_16x16x32_bf16 v[152:155], v[80:83], v[196:199], v[152:155]\n\t" \
  "v_mfma_f32_16x16x32_bf16 v[156:159], v[84:87], v[196:199], v[156:159]\n\t" \
  "v_mfma_f32_16x16x32_bf16 v[144:147], v[88:91], v[200:203], v[144:147]\n\t" \
  "v_mfma_f32_16x16x32_bf16 v[148:151], v[92:95], v[200:203], v[148:151]\n\t" \
  "v_mfma_f32_16x16x32_bf16 v[152:155], v[88:91], v[204:207], v[152:155]\n\t" \
  "v_mfma_f32_16x16x32_bf16 v[156:159], v[92:95], v[204:207], v[156:159]\n\t" \
  "v_mfma_f32_16x16x32_bf16 v[144:147], v[96:99], v[208:211], v[144:147]\n\t" \
  "v_mfma_f32_16x16x32_bf16 v[148:151], v[100:103], v[208:211], v[148:151]\n\t" \
  "v_mfma_f32_16x16x32_bf16 v[152:155], v[96:99], v[212:215], v[152:155]\n\t" \
  "v_mfma_f32_16x16x32_bf16 v[156:159], v[100:103], v[212:215], v[156:159]\n\t" \
  "v_mfma_f32_16x16x32_bf16 v[144:147], v[104:107], v[216:219], v[144:147]\n\t" \
  "v_mfma_f32_16x16x32_bf16 v[148:151], v[108:111], v[216:219], v[148:151]\n\t" \
  "v_mfma_f32_16x16x32_bf16 v[152:155], v[104:107], v[220:223], v[152:155]\n\t" \
  "v_mfma_f32_16x16x32_bf16 v[156:159], v[108:111], v[220:223], v[156:159]\n\t" \
  "v_mfma_f32_16x16x32_bf16 v[144:147], v[112:115], v[224:227], v[144:147]\n\t" \
  "v_mfma_f32_16x16x32_bf16 v[148:151], v[116:119], v[224:227], v[148:151]\n\t" \
  "v_mfma_f32_16x16x32_bf16 v[152:155], v[112:115], v[228:231], v[152:155]\n\t" \
  "v_mfma_f32_16x16x32_bf16 v[156:159], v[116:119], v[228:231], v[156:159]\n\t" \
  "v_mfma_f32_16x16x32_bf16 v[144:147], v[120:123], v[232:235], v[144:147]\n\t" \
  "v_mfma_f32_16x16x32_bf16 v[148:151], v[124:127], v[232:235], v[148:151]\n\t" \
  "v_mfma_f32_16x16x32_bf16 v[152:155], v[120:123], v[236:239], v[152:155]\n\t" \
  "v_mfma_f32_16x16x32_bf16 v[156:159], v[124:127], v[236:239], v[156:159]\n\t" \
  "s_setprio 0\n\t" \
  "s_cmp_lt_u32 s86, 64\n\t" \
  "s_cbranch_scc1 .Lp0_%=\n\t" \
  "s_barrier\n\t" \
  "s_nop 2\n\t" \
  "ds_write_b128 v252, v[152:155] offset:0\n\t" \
  "ds_write_b128 v252, v[156:159] offset:1024\n\t" \
  "s_waitcnt lgkmcnt(0)\n\t" \
  "s_barrier\n\t" \
  "ds_read_b128 v[160:163], v253 offset:0\n\t" \
  "ds_read_b128 v[164:167], v253 offset:1024\n\t" \
  "s_waitcnt lgkmcnt(0)\n\t" \
  "s_lshr_b32 s90, s87, 7\n\t" \
  "s_xor_b32 s90, s90, %[s0]\n\t" \
  "s_and_b32 s90, s90, 1\n\t" \
  "s_lshl1_add_u32 s90, s87, s90\n\t" \
  "s_lshl_b32 s90, s90, 13\n\t" \
  "v_add_f32 v160, v160, v144\n\t" \
  "v_add_f32 v161, v161, v145\n\t" \
  "v_add_f32 v162, v162, v146\n\t" \
  "v_add_f32 v163, v163, v147\n\t" \
  "v_add_f32 v164, v164, v148\n\t" \
  "v_add_f32 v165, v165, v149\n\t" \
  "v_add_f32 v166, v166, v150\n\t" \
  "v_add_f32 v167, v167, v151\n\t" \
  "v_cvt_pk_bf16_f32 v168, v160, v161\n\t" \
  "v_cvt_pk_bf16_f32 v169, v162, v163\n\t" \
  "v_cvt_pk_bf16_f32 v170, v164, v165\n\t" \
  "v_cvt_pk_bf16_f32 v171, v166, v167\n\t" \
  "buffer_store_dwordx4 v[168:171], v172, %[rt], s90 offen sc1\n\t" \
  "s_waitcnt vmcnt(0)\n\t" \
  "s_barrier\n\t" \
  "s_mov_b32 m0, s94\n\t" \
  "s_branch .Le_%=\n\t" \
  ".Lk_%=:\n\t" \
  "s_mov_b32 s94, m0\n\t" \
  "s_mov_b64 s[92:93], %[x0]\n\t" \
  "s_mov_b32 s86, %[g0]\n\t" \
  "s_add_u32 s87, s86, %[t0]\n\t" \
  "s_add_u32 m0, %[lw], 67584\n\t" \
  "s_nop 0\n\t" \
  "global_load_lds_dwordx4 v250, s[92:93] sc1\n\t" \
  "s_add_u32 m0, %[lw], 68608\n\t" \
  "s_nop 0\n\t" \
  "global_load_lds_dwordx4 v251, s[92:93] sc1\n\t" \
  "s_barrier\n\t" \
  "s_branch .Ls1_%=\n\t" \
  ".Lp1_%=:\n\t" \
  "s_waitcnt vmcnt(10)\n\t" \
  "s_barrier\n\t" \
  "s_setprio 2\n\t" \
  "v_mfma_f32_16x16x32_bf16 v[144:147], v[64:67], v[176:179], v[144:147]\n\t" \
  "v_mfma_f32_16x16x32_bf16 v[148:151], v[68:71], v[176:179], v[148:151]\n\t" \
  "v_mfma_f32_16x16x32_bf16 v[152:155], v[64:67], v[180:183], v[152:155]\n\t" \
  "v_mfma_f32_16x16x32_bf16 v[156:159], v[68:71], v[180:183], v[156:159]\n\t" \
  "v_mfma_f32_16x16x32_bf16 v[144:147], v[72:75], v[184:187], v[144:147]\n\t" \
  "v_mfma_f32_16x16x32_bf16 v[148:151], v[76:79], v[184:187], v[148:151]\n\t" \
  "v_mfma_f32_16x16x32_bf16 v[152:155], v[72:75], v[188:191], v[152:155]\n\t" \
  "v_mfma_f32_16x16x32_bf16 v[156:159], v[76:79], v[188:191], v[156:159]\n\t" \
  "v_mfma_f32_16x16x32_bf16 v[144:147], v[80:83], v[192:195], v[144:147]\n\t" \
  "v_mfma_f32_16x16x32_bf16 v[148:151], v[84:87], v[192:195], v[148:151]\n\t" \
  "v_mfma_f32_16x16x32_bf16 v[152:155], v[80:83], v[196:199], v[152:155]\n\t" \
  "v_mfma_f32_16x16x32_bf16 v[156:159], v[84:87], v[196:199], v[156:159]\n\t" \
  "v_mfma_f32_16x16x32_bf16 v[144:147], v[88:91], v[200:203], v[144:147]\n\t" \
  "v_mfma_f32_16x16x32_bf16 v[148:151], v[92:95], v[200:203], v[148:151]\n\t" \
  "v_mfma_f32_16x16x32_bf16 v[152:155], v[88:91], v[204:207], v[152:155]\n\t" \
  "v_mfma_f32_16x16x32_bf16 v[156:159], v[92:95], v[204:207], v[156:159]\n\t" \
  "v_mfma_f32_16x16x32_bf16 v[144:147], v[96:99], v[208:211], v[144:147]\n\t" \
  "v_mfma_f32_16x16x32_bf16 v[148:151], v[100:103], v[208:211], v[148:151]\n\t" \
  "v_mfma_f32_16x16x32_bf16 v[152:155], v[96:99], v[212:215], v[152:155]\n\t" \
  "v_mfma_f32_16x16x32_bf16 v[156:159], v[100:103], v[212:215], v[156:159]\n\t" \
  "v_mfma_f32_16x16x32_bf16 v[144:147], v[104:107], v[216:219], v[144:147]\n\t" \
  "v_mfma_f32_16x16x32_bf16 v[148:151], v[108:111], v[216:219], v[148:151]\n\t" \
  "v_mfma_f32_16x16x32_bf16 v[152:155], v[104:107], v[220:223], v[152:155]\n\t" \
  "v_mfma_f32_16x16x32_bf16 v[156:159], v[108:111], v[220:223], v[156:159]\n\t" \
  "v_mfma_f32_16x16x32_bf16 v[144:147], v[112:115], v[224:227], v[144:147]\n\t" \
  "v_mfma_f32_16x16x32_bf16 v[148:151], v[116:119], v[224:227], v[148:151]\n\t" \
  "v_mfma_f32_16x16x32_bf16 v[152:155], v[112:115], v[228:231], v[152:155]\n\t" \
  "v_mfma_f32_16x16x32_bf16 v[156:159], v[116:119], v[228:231], v[156:159]\n\t" \
  "v_mfma_f32_16x16x32_bf16 v[144:147], v[120:123], v[232:235], v[144:147]\n\t" \
  "v_mfma_f32_16x16x32_bf16 v[148:151], v[124:127], v[232:235], v[148:151]\n\t" \
  "v_mfma_f32_16x16x32_bf16 v[152:155], v[120:123], v[236:239], v[152:155]\n\t" \
  "v_mfma_f32_16x16x32_bf16 v[156:159], v[124:127], v[236:239], v[156:159]\n\t" \
  "s_setprio 0\n\t" \
  ".Ls1_%=:\n\t" \
  "s_add_u32 s92, s92, 512\n\t" \
  "s_addc_u32 s93, s93, 0\n\t" \
  "ds_read_b128 v[176:179], v240 offset:0\n\t" \
  "ds_read_b128 v[180:183], v240 offset:16384\n\t" \
  "ds_read_b128 v[184:187], v241 offset:0\n\t" \
  "ds_read_b128 v[188:191], v241 offset:16384\n\t" \
  "s_add_u32 m0, %[lw], 98304\n\t" \
  "s_nop 0\n\t" \
  "global_load_lds_dwordx4 v248, s[92:93] sc1\n\t" \
  "ds_read_b128 v[192:195], v242 offset:0\n\t" \
  "ds_read_b128 v[196:199], v242 offset:16384\n\t" \
  "ds_read_b128 v[200:203], v243 offset:0\n\t" \
  "ds_read_b128 v[204:207], v243 offset:16384\n\t" \
  "s_add_u32 m0, %[lw], 99328\n\t" \
  "s_nop 0\n\t" \
  "global_load_lds_dwordx4 v249, s[92:93] sc1\n\t" \
  "ds_read_b128 v[208:211], v240 offset:256\n\t" \
  "ds_read_b128 v[212:215], v240 offset:16640\n\t" \
  "ds_read_b128 v[216:219], v241 offset:256\n\t" \
  "ds_read_b128 v[220:223], v241 offset:16640\n\t" \
  "s_add_u32 m0, %[lw], 100352\n\t" \
  "s_nop 0\n\t" \
  "global_load_lds_dwordx4 v250, s[92:93] sc1\n\t" \
  "ds_read_b128 v[224:227], v242 offset:256\n\t" \
  "ds_read_b128 v[228:231], v242 offset:16640\n\t" \
  "ds_read_b128 v[232:235], v243 offset:256\n\t" \
  "ds_read_b128 v[236:239], v243 offset:16640\n\t" \
  "s_add_u32 m0, %[lw], 101376\n\t" \
  "s_nop 0\n\t" \
  "global_load_lds_dwordx4 v251, s[92:93] sc1\n\t" \
  "ds_write_b128 v252, v[152:155] offset:0\n\t" \
  "ds_write_b128 v252, v[156:159] offset:1024\n\t" \
  "s_waitcnt lgkmcnt(0)\n\t" \
  "s_waitcnt vmcnt(4)\n\t" \
  "s_barrier\n\t" \
  "s_setprio 2\n\t" \
  "v_mfma_f32_16x16x32_bf16 v[128:131], v[0:3], v[176:179], 0\n\t" \
  "v_mfma_f32_16x16x32_bf16 v[132:135], v[4:7], v[176:179], 0\n\t" \
  "v_mfma_f32_16x16x32_bf16 v[136:139], v[0:3], v[180:183], 0\n\t" \
  "v_mfma_f32_16x16x32_bf16 v[140:143], v[4:7], v[180:183], 0\n\t" \
  "v_mfma_f32_16x16x32_bf16 v[128:131], v[8:11], v[184:187], v[128:131]\n\t" \
  "v_mfma_f32_16x16x32_bf16 v[132:135], v[12:15], v[184:187], v[132:135]\n\t" \
  "v_mfma_f32_16x16x32_bf16 v[136:139], v[8:11], v[188:191], v[136:139]\n\t" \
  "v_mfma_f32_16x16x32_bf16 v[140:143], v[12:15], v[188:191], v[140:143]\n\t" \
  "v_mfma_f32_16x16x32_bf16 v[128:131], v[16:19], v[192:195], v[128:131]\n\t" \
  "v_mfma_f32_16x16x32_bf16 v[132:135], v[20:23], v[192:195], v[132:135]\n\t" \
  "v_mfma_f32_16x16x32_bf16 v[136:139], v[16:19], v[196:199], v[136:139]\n\t" \
  "v_mfma_f32_16x16x32_bf16 v[140:143], v[20:23], v[196:199], v[140:143]\n\t" \
  "v_mfma_f32_16x16x32_bf16 v[128:131], v[24:27], v[200:203], v[128:131]\n\t" \
  "v_mfma_f32_16x16x32_bf16 v[132:135], v[28:31], v[200:203], v[132:135]\n\t" \
  "v_mfma_f32_16x16x32_bf16 v[136:139], v[24:27], v[204:207], v[136:139]\n\t" \
  "v_mfma_f32_16x16x32_bf16 v[140:143], v[28:31], v[204:207], v[140:143]\n\t" \
  "v_mfma_f32_16x16x32_bf16 v[128:131], v[32:35], v[208:211], v[128:131]\n\t" \
  "v_mfma_f32_16x16x32_bf16 v[132:135], v[36:39], v[208:211], v[132:135]\n\t" \
  "v_mfma_f32_16x16x32_bf16 v[136:139], v[32:35], v[212:215], v[136:139]\n\t" \
  "v_mfma_f32_16x16x32_bf16 v[140:143], v[36:39], v[212:215], v[140:143]\n\t" \
  "v_mfma_f32_16x16x32_bf16 v[128:131], v[40:43], v[216:219], v[128:131]\n\t" \
  "v_mfma_f32_16x16x32_bf16 v[132:135], v[44:47], v[216:219], v[132:135]\n\t" \
  "v_mfma_f32_16x16x32_bf16 v[136:139], v[40:43], v[220:223], v[136:139]\n\t" \
  "v_mfma_f32_16x16x32_bf16 v[140:143], v[44:47], v[220:223], v[140:143]\n\t" \
  "v_mfma_f32_16x16x32_bf16 v[128:131], v[48:51], v[224:227], v[128:131]\n\t" \
  "v_mfma_f32_16x16x32_bf16 v[132:135], v[52:55], v[224:227], v[132:135]\n\t" \
  "v_mfma_f32_16x16x32_bf16 v[136:139], v[48:51], v[228:231], v[136:139]\n\t" \
  "v_mfma_f32_16x16x32_bf16 v[140:143], v[52:55], v[228:231], v[140:143]\n\t" \
  "v_mfma_f32_16x16x32_bf16 v[128:131], v[56:59], v[232:235], v[128:131]\n\t" \
  "v_mfma_f32_16x16x32_bf16 v[132:135], v[60:63], v[232:235], v[132:135]\n\t" \
  "v_mfma_f32_16x16x32_bf16 v[136:139], v[56:59], v[236:239], v[136:139]\n\t" \
  "v_mfma_f32_16x16x32_bf16 v[140:143], v[60:63], v[236:239], v[140:143]\n\t" \
  "s_setprio 0\n\t" \
  "s_add_u32 s92, s92, 65024\n\t" \
  "s_addc_u32 s93, s93, 0\n\t" \
  "ds_read_b128 v[160:163], v253 offset:0\n\t" \
  "ds_read_b128 v[164:167], v253 offset:1024\n\t" \
  "ds_read_b128 v[176:179], v240 offset:32768\n\t" \
  "ds_read_b128 v[180:183], v240 offset:49152\n\t" \
  "ds_read_b128 v[184:187], v241 offset:32768\n\t" \
  "ds_read_b128 v[188:191], v241 offset:49152\n\t" \
  "s_add_u32 m0, %[lw], 0\n\t" \
  "s_nop 0\n\t" \
  "global_load_lds_dwordx4 v248, s[92:93] sc1\n\t" \
  "ds_read_b128 v[192:195], v242 offset:32768\n\t" \
  "ds_read_b128 v[196:199], v242 offset:49152\n\t" \
  "ds_read_b128 v[200:203], v243 offset:32768\n\t" \
  "ds_read_b128 v[204:207], v243 offset:49152\n\t" \
  "s_add_u32 m0, %[lw], 1024\n\t" \
  "s_nop 0\n\t" \
  "global_load_lds_dwordx4 v249, s[92:93] sc1\n\t" \
  "ds_read_b128 v[208:211], v240 offset:33024\n\t" \
  "ds_read_b128 v[212:215], v240 offset:49408\n\t" \
  "ds_read_b128 v[216:219], v241 offset:33024\n\t" \
  "ds_read_b128 v[220:223], v241 offset:49408\n\t" \
  "s_add_u32 m0, %[lw], 2048\n\t" \
  "s_nop 0\n\t" \
  "global_load_lds_dwordx4 v250, s[92:93] sc1\n\t" \
  "ds_read_b128 v[224:227], v242 offset:33024\n\t" \
  "ds_read_b128 v[228:231], v242 offset:49408\n\t" \
  "ds_read_b128 v[232:235], v243 offset:33024\n\t" \
  "ds_read_b128 v[236:239], v243 offset:49408\n\t" \
  "s_add_u32 m0, %[lw], 3072\n\t" \
  "s_nop 0\n\t" \
  "global_load_lds_dwordx4 v251, s[92:93] sc1\n\t" \
  "s_waitcnt lgkmcnt(15)\n\t" \
  "s_waitcnt lgkmcnt(12)\n\t" \
  "s_sub_i32 s91, s86, 2\n\t" \
  "v_mov_b32 v173, s91\n\t" \
  "buffer_store_dword v173, v174, %[rs], 0 offen sc1\n\t" \
  "s_lshr_b32 s90, s87, 7\n\t" \
  "s_xor_b32 s90, s90, %[s0]\n\t" \
  "s_and_b32 s90, s90, 1\n\t" \
  "s_lshl1_add_u32 s90, s87, s90\n\t" \
  "s_lshl_b32 s90, s90, 13\n\t" \
  "v_add_f32 v160, v160, v144\n\t" \
  "v_add_f32 v161, v161, v145\n\t" \
  "v_add_f32 v162, v162, v146\n\t" \
  "v_add_f32 v163, v163, v147\n\t" \
  "v_add_f32 v164, v164, v148\n\t" \
  "v_add_f32 v165, v165, v149\n\t" \
  "v_add_f32 v166, v166, v150\n\t" \
  "v_add_f32 v167, v167, v151\n\t" \
  "v_cvt_pk_bf16_f32 v168, v160, v161\n\t" \
  "v_cvt_pk_bf16_f32 v169, v162, v163\n\t" \
  "v_cvt_pk_bf16_f32 v170, v164, v165\n\t" \
  "v_cvt_pk_bf16_f32 v171, v166, v167\n\t" \
  "buffer_store_dwordx4 v[168:171], v172, %[rt], s90 offen sc1\n\t" \
  "s_add_u32 s86, s86, 1\n\t" \
  "s_add_u32 s87, s87, 1\n\t" \
  "s_waitcnt lgkmcnt(0)\n\t" \
  "s_waitcnt vmcnt(10)\n\t" \
  "s_barrier\n\t" \
  "s_setprio 2\n\t" \
  "v_mfma_f32_16x16x32_bf16 v[128:131], v[64:67], v[176:179], v[128:131]\n\t" \
  "v_mfma_f32_16x16x32_bf16 v[132:135], v[68:71], v[176:179], v[132:135]\n\t" \
  "v_mfma_f32_16x16x32_bf16 v[136:139], v[64:67], v[180:183], v[136:139]\n\t" \
  "v_mfma_f32_16x16x32_bf16 v[140:143], v[68:71], v[180:183], v[140:143]\n\t" \
  "v_mfma_f32_16x16x32_bf16 v[128:131], v[72:75], v[184:187], v[128:131]\n\t" \
  "v_mfma_f32_16x16x32_bf16 v[132:135], v[76:79], v[184:187], v[132:135]\n\t" \
  "v_mfma_f32_16x16x32_bf16 v[136:139], v[72:75], v[188:191], v[136:139]\n\t" \
  "v_mfma_f32_16x16x32_bf16 v[140:143], v[76:79], v[188:191], v[140:143]\n\t" \
  "v_mfma_f32_16x16x32_bf16 v[128:131], v[80:83], v[192:195], v[128:131]\n\t" \
  "v_mfma_f32_16x16x32_bf16 v[132:135], v[84:87], v[192:195], v[132:135]\n\t" \
  "v_mfma_f32_16x16x32_bf16 v[136:139], v[80:83], v[196:199], v[136:139]\n\t" \
  "v_mfma_f32_16x16x32_bf16 v[140:143], v[84:87], v[196:199], v[140:143]\n\t" \
  "v_mfma_f32_16x16x32_bf16 v[128:131], v[88:91], v[200:203], v[128:131]\n\t" \
  "v_mfma_f32_16x16x32_bf16 v[132:135], v[92:95], v[200:203], v[132:135]\n\t" \
  "v_mfma_f32_16x16x32_bf16 v[136:139], v[88:91], v[204:207], v[136:139]\n\t" \
  "v_mfma_f32_16x16x32_bf16 v[140:143], v[92:95], v[204:207], v[140:143]\n\t" \
  "v_mfma_f32_16x16x32_bf16 v[128:131], v[96:99], v[208:211], v[128:131]\n\t" \
  "v_mfma_f32_16x16x32_bf16 v[132:135], v[100:103], v[208:211], v[132:135]\n\t" \
  "v_mfma_f32_16x16x32_bf16 v[136:139], v[96:99], v[212:215], v[136:139]\n\t" \
  "v_mfma_f32_16x16x32_bf16 v[140:143], v[100:103], v[212:215], v[140:143]\n\t" \
  "v_mfma_f32_16x16x32_bf16 v[128:131], v[104:107], v[216:219], v[128:131]\n\t" \
  "v_mfma_f32_16x16x32_bf16 v[132:135], v[108:111], v[216:219], v[132:135]\n\t" \
  "v_mfma_f32_16x16x32_bf16 v[136:139], v[104:107], v[220:223], v[136:139]\n\t" \
  "v_mfma_f32_16x16x32_bf16 v[140:143], v[108:111], v[220:223], v[140:143]\n\t" \
  "v_mfma_f32_16x16x32_bf16 v[128:131], v[112:115], v[224:227], v[128:131]\n\t" \
  "v_mfma_f32_16x16x32_bf16 v[132:135], v[116:119], v[224:227], v[132:135]\n\t" \
  "v_mfma_f32_16x16x32_bf16 v[136:139], v[112:115], v[228:231], v[136:139]\n\t" \
  "v_mfma_f32_16x16x32_bf16 v[140:143], v[116:119], v[228:231], v[140:143]\n\t" \
  "v_mfma_f32_16x16x32_bf16 v[128:131], v[120:123], v[232:235], v[128:131]\n\t" \
  "v_mfma_f32_16x16x32_bf16 v[132:135], v[124:127], v[232:235], v[132:135]\n\t" \
  "v_mfma_f32_16x16x32_bf16 v[136:139], v[120:123], v[236:239], v[136:139]\n\t" \
  "v_mfma_f32_16x16x32_bf16 v[140:143], v[124:127], v[236:239], v[140:143]\n\t" \
  "s_setprio 0\n\t" \
  "s_add_u32 s92, s92, 512\n\t" \
  "s_addc_u32 s93, s93, 0\n\t" \
  "ds_read_b128 v[176:179], v244 offset:0\n\t" \
  "ds_read_b128 v[180:183], v244 offset:16384\n\t" \
  "ds_read_b128 v[184:187], v245 offset:0\n\t" \
  "ds_read_b128 v[188:191], v245 offset:16384\n\t" \
  "s_add_u32 m0, %[lw], 32768\n\t" \
  "s_nop 0\n\t" \
  "global_load_lds_dwordx4 v248, s[92:93] sc1\n\t" \
  "ds_read_b128 v[192:195], v246 offset:0\n\t" \
  "ds_read_b128 v[196:199], v246 offset:16384\n\t" \
  "ds_read_b128 v[200:203], v247 offset:0\n\t" \
  "ds_read_b128 v[204:207], v247 offset:16384\n\t" \
  "s_add_u32 m0, %[lw], 33792\n\t" \
  "s_nop 0\n\t" \
  "global_load_lds_dwordx4 v249, s[92:93] sc1\n\t" \
  "ds_read_b128 v[208:211], v244 offset:256\n\t" \
  "ds_read_b128 v[212:215], v244 offset:16640\n\t" \
  "ds_read_b128 v[216:219], v245 offset:256\n\t" \
  "ds_read_b128 v[220:223], v245 offset:16640\n\t" \
  "s_add_u32 m0, %[lw], 34816\n\t" \
  "s_nop 0\n\t" \
  "global_load_lds_dwordx4 v250, s[92:93] sc1\n\t" \
  "ds_read_b128 v[224:227], v246 offset:256\n\t" \
  "ds_read_b128 v[228:231], v246 offset:16640\n\t" \
  "ds_read_b128 v[232:235], v247 offset:256\n\t" \
  "ds_read_b128 v[236:239], v247 offset:16640\n\t" \
  "s_add_u32 m0, %[lw], 35840\n\t" \
  "s_nop 0\n\t" \
  "global_load_lds_dwordx4 v251, s[92:93] sc1\n\t" \
  "ds_write_b128 v252, v[136:139] offset:0\n\t" \
  "ds_write_b128 v252, v[140:143] offset:1024\n\t" \
  "s_waitcnt lgkmcnt(0)\n\t" \
  "s_waitcnt vmcnt(4)\n\t" \
  "s_barrier\n\t" \
  "s_setprio 2\n\t" \
  "v_mfma_f32_16x16x32_bf16 v[144:147], v[0:3], v[176:179], 0\n\t" \
  "v_mfma_f32_16x16x32_bf16 v[148:151], v[4:7], v[176:179], 0\n\t" \
  "v_mfma_f32_16x16x32_bf16 v[152:155], v[0:3], v[180:183], 0\n\t" \
  "v_mfma_f32_16x16x32_bf16 v[156:159], v[4:7], v[180:183], 0\n\t" \
  "v_mfma_f32_16x16x32_bf16 v[144:147], v[8:11], v[184:187], v[144:147]\n\t" \
  "v_mfma_f32_16x16x32_bf16 v[148:151], v[12:15], v[184:187], v[148:151]\n\t" \
  "v_mfma_f32_16x16x32_bf16 v[152:155], v[8:11], v[188:191], v[152:155]\n\t" \
  "v_mfma_f32_16x16x32_bf16 v[156:159], v[12:15], v[188:191], v[156:159]\n\t" \
  "v_mfma_f32_16x16x32_bf16 v[144:147], v[16:19], v[192:195], v[144:147]\n\t" \
  "v_mfma_f32_16x16x32_bf16 v[148:151], v[20:23], v[192:195], v[148:151]\n\t" \
  "v_mfma_f32_16x16x32_bf16 v[152:155], v[16:19], v[196:199], v[152:155]\n\t" \
  "v_mfma_f32_16x16x32_bf16 v[156:159], v[20:23], v[196:199], v[156:159]\n\t" \
  "v_mfma_f32_16x16x32_bf16 v[144:147], v[24:27], v[200:203], v[144:147]\n\t" \
  "v_mfma_f32_16x16x32_bf16 v[148:151], v[28:31], v[200:203], v[148:151]\n\t" \
  "v_mfma_f32_16x16x32_bf16 v[152:155], v[24:27], v[204:207], v[152:155]\n\t" \
  "v_mfma_f32_16x16x32_bf16 v[156:159], v[28:31], v[204:207], v[156:159]\n\t" \
  "v_mfma_f32_16x16x32_bf16 v[144:147], v[32:35], v[208:211], v[144:147]\n\t" \
  "v_mfma_f32_16x16x32_bf16 v[148:151], v[36:39], v[208:211], v[148:151]\n\t" \
  "v_mfma_f32_16x16x32_bf16 v[152:155], v[32:35], v[212:215], v[152:155]\n\t" \
  "v_mfma_f32_16x16x32_bf16 v[156:159], v[36:39], v[212:215], v[156:159]\n\t" \
  "v_mfma_f32_16x16x32_bf16 v[144:147], v[40:43], v[216:219], v[144:147]\n\t" \
  "v_mfma_f32_16x16x32_bf16 v[148:151], v[44:47], v[216:219], v[148:151]\n\t" \
  "v_mfma_f32_16x16x32_bf16 v[152:155], v[40:43], v[220:223], v[152:155]\n\t" \
  "v_mfma_f32_16x16x32_bf16 v[156:159], v[44:47], v[220:223], v[156:159]\n\t" \
  "v_mfma_f32_16x16x32_bf16 v[144:147], v[48:51], v[224:227], v[144:147]\n\t" \
  "v_mfma_f32_16x16x32_bf16 v[148:151], v[52:55], v[224:227], v[148:151]\n\t" \
  "v_mfma_f32_16x16x32_bf16 v[152:155], v[48:51], v[228:231], v[152:155]\n\t" \
  "v_mfma_f32_16x16x32_bf16 v[156:159], v[52:55], v[228:231], v[156:159]\n\t" \
  "v_mfma_f32_16x16x32_bf16 v[144:147], v[56:59], v[232:235], v[144:147]\n\t" \
  "v_mfma_f32_16x16x32_bf16 v[148:151], v[60:63], v[232:235], v[148:151]\n\t" \
  "v_mfma_f32_16x16x32_bf16 v[152:155], v[56:59], v[236:239], v[152:155]\n\t" \
  "v_mfma_f32_16x16x32_bf16 v[156:159], v[60:63], v[236:239], v[156:159]\n\t" \
  "s_setprio 0\n\t" \
  "s_add_u32 s92, s92, 65024\n\t" \
  "s_addc_u32 s93, s93, 0\n\t" \
  "ds_read_b128 v[160:163], v253 offset:0\n\t" \
  "ds_read_b128 v[164:167], v253 offset:1024\n\t" \
  "ds_read_b128 v[176:179], v244 offset:32768\n\t" \
  "ds_read_b128 v[180:183], v244 offset:49152\n\t" \
  "ds_read_b128 v[184:187], v245 offset:32768\n\t" \
  "ds_read_b128 v[188:191], v245 offset:49152\n\t" \
  "s_add_u32 m0, %[lw], 65536\n\t" \
  "s_nop 0\n\t" \
  "global_load_lds_dwordx4 v248, s[92:93] sc1\n\t" \
  "ds_read_b128 v[192:195], v246 offset:32768\n\t" \
  "ds_read_b128 v[196:199], v246 offset:49152\n\t" \
  "ds_read_b128 v[200:203], v247 offset:32768\n\t" \
  "ds_read_b128 v[204:207], v247 offset:49152\n\t" \
  "s_add_u32 m0, %[lw], 66560\n\t" \
  "s_nop 0\n\t" \
  "global_load_lds_dwordx4 v249, s[92:93] sc1\n\t" \
  "ds_read_b128 v[208:211], v244 offset:33024\n\t" \
  "ds_read_b128 v[212:215], v244 offset:49408\n\t" \
  "ds_read_b128 v[216:219], v245 offset:33024\n\t" \
  "ds_read_b128 v[220:223], v245 offset:49408\n\t" \
  "s_add_u32 m0, %[lw], 67584\n\t" \
  "s_nop 0\n\t" \
  "global_load_lds_dwordx4 v250, s[92:93] sc1\n\t" \
  "ds_read_b128 v[224:227], v246 offset:33024\n\t" \
  "ds_read_b128 v[228:231], v246 offset:49408\n\t" \
  "ds_read_b128 v[232:235], v247 offset:33024\n\t" \
  "ds_read_b128 v[236:239], v247 offset:49408\n\t" \
  "s_add_u32 m0, %[lw], 68608\n\t" \
  "s_nop 0\n\t" \
  "global_load_lds_dwordx4 v251, s[92:93] sc1\n\t" \
  "s_waitcnt lgkmcnt(15)\n\t" \
  "s_waitcnt lgkmcnt(12)\n\t" \
  "s_sub_i32 s91, s86, 2\n\t" \
  "v_mov_b32 v173, s91\n\t" \
  "buffer_store_dword v173, v174, %[rs], 0 offen sc1\n\t" \
  "s_lshr_b32 s90, s87, 7\n\t" \
  "s_xor_b32 s90, s90, %[s0]\n\t" \
  "s_and_b32 s90, s90, 1\n\t" \
  "s_lshl1_add_u32 s90, s87, s90\n\t" \
  "s_lshl_b32 s90, s90, 13\n\t" \
  "v_add_f32 v160, v160, v128\n\t" \
  "v_add_f32 v161, v161, v129\n\t" \
  "v_add_f32 v162, v162, v130\n\t" \
  "v_add_f32 v163, v163, v131\n\t" \
  "v_add_f32 v164, v164, v132\n\t" \
  "v_add_f32 v165, v165, v133\n\t" \
  "v_add_f32 v166, v166, v134\n\t" \
  "v_add_f32 v167, v167, v135\n\t" \
  "v_cvt_pk_bf16_f32 v168, v160, v161\n\t" \
  "v_cvt_pk_bf16_f32 v169, v162, v163\n\t" \
  "v_cvt_pk_bf16_f32 v170, v164, v165\n\t" \
  "v_cvt_pk_bf16_f32 v171, v166, v167\n\t" \
  "buffer_store_dwordx4 v[168:171], v172, %[rt], s90 offen sc1\n\t" \
  "s_add_u32 s86, s86, 1\n\t" \
  "s_add_u32 s87, s87, 1\n\t" \
  "s_waitcnt lgkmcnt(0)\n\t" \
  "s_cmp_lt_u32 s86, 64\n\t" \
  "s_cbranch_scc1 .Lp1_%=\n\t" \
  "s_barrier\n\t" \
  "s_setprio 2\n\t" \
  "v_mfma_f32_16x16x32_bf16 v[144:147], v[64:67], v[176:179], v[144:147]\n\t" \
  "v_mfma_f32_16x16x32_bf16 v[148:151], v[68:71], v[176:179], v[148:151]\n\t" \
  "v_mfma_f32_16x16x32_bf16 v[152:155], v[64:67], v[180:183], v[152:155]\n\t" \
  "v_mfma_f32_16x16x32_bf16 v[156:159], v[68:71], v[180:183], v[156:159]\n\t" \
  "v_mfma_f32_16x16x32_bf16 v[144:147], v[72:75], v[184:187], v[144:147]\n\t" \
  "v_mfma_f32_16x16x32_bf16 v[148:151], v[76:79], v[184:187], v[148:151]\n\t" \
  "v_mfma_f32_16x16x32_bf16 v[152:155], v[72:75], v[188:191], v[152:155]\n\t" \
  "v_mfma_f32_16x16x32_bf16 v[156:159], v[76:79], v[188:191], v[156:159]\n\t" \
  "v_mfma_f32_16x16x32_bf16 v[144:147], v[80:83], v[192:195], v[144:147]\n\t" \
  "v_mfma_f32_16x16x32_bf16 v[148:151], v[84:87], v[192:195], v[148:151]\n\t" \
  "v_mfma_f32_16x16x32_bf16 v[152:155], v[80:83], v[196:199], v[152:155]\n\t" \
  "v_mfma_f32_16x16x32_bf16 v[156:159], v[84:87], v[196:199], v[156:159]\n\t" \
  "v_mfma_f32_16x16x32_bf16 v[144:147], v[88:91], v[200:203], v[144:147]\n\t" \
  "v_mfma_f32_16x16x32_bf16 v[148:151], v[92:95], v[200:203], v[148:151]\n\t" \
  "v_mfma_f32_16x16x32_bf16 v[152:155], v[88:91], v[204:207], v[152:155]\n\t" \
  "v_mfma_f32_16x16x32_bf16 v[156:159], v[92:95], v[204:207], v[156:159]\n\t" \
  "v_mfma_f32_16x16x32_bf16 v[144:147], v[96:99], v[208:211], v[144:147]\n\t" \
  "v_mfma_f32_16x16x32_bf16 v[148:151], v[100:103], v[208:211], v[148:151]\n\t" \
  "v_mfma_f32_16x16x32_bf16 v[152:155], v[96:99], v[212:215], v[152:155]\n\t" \
  "v_mfma_f32_16x16x32_bf16 v[156:159], v[100:103], v[212:215], v[156:159]\n\t" \
  "v_mfma_f32_16x16x32_bf16 v[144:147], v[104:107], v[216:219], v[144:147]\n\t" \
  "v_mfma_f32_16x16x32_bf16 v[148:151], v[108:111], v[216:219], v[148:151]\n\t" \
  "v_mfma_f32_16x16x32_bf16 v[152:155], v[104:107], v[220:223], v[152:155]\n\t" \
  "v_mfma_f32_16x16x32_bf16 v[156:159], v[108:111], v[220:223], v[156:159]\n\t" \
  "v_mfma_f32_16x16x32_bf16 v[144:147], v[112:115], v[224:227], v[144:147]\n\t" \
  "v_mfma_f32_16x16x32_bf16 v[148:151], v[116:119], v[224:227], v[148:151]\n\t" \
  "v_mfma_f32_16x16x32_bf16 v[152:155], v[112:115], v[228:231], v[152:155]\n\t" \
  "v_mfma_f32_16x16x32_bf16 v[156:159], v[116:119], v[228:231], v[156:159]\n\t" \
  "v_mfma_f32_16x16x32_bf16 v[144:147], v[120:123], v[232:235], v[144:147]\n\t" \
  "v_mfma_f32_16x16x32_bf16 v[148:151], v[124:127], v[232:235], v[148:151]\n\t" \
  "v_mfma_f32_16x16x32_bf16 v[152:155], v[120:123], v[236:239], v[152:155]\n\t" \
  "v_mfma_f32_16x16x32_bf16 v[156:159], v[124:127], v[236:239], v[156:159]\n\t" \
  "s_setprio 0\n\t" \
  "s_nop 5\n\t" \
  "ds_write_b128 v252, v[152:155] offset:0\n\t" \
  "ds_write_b128 v252, v[156:159] offset:1024\n\t" \
  "s_waitcnt lgkmcnt(0)\n\t" \
  "s_barrier\n\t" \
  "ds_read_b128 v[160:163], v253 offset:0\n\t" \
  "ds_read_b128 v[164:167], v253 offset:1024\n\t" \
  "s_waitcnt lgkmcnt(0)\n\t" \
  "s_lshr_b32 s90, s87, 7\n\t" \
  "s_xor_b32 s90, s90, %[s0]\n\t" \
  "s_and_b32 s90, s90, 1\n\t" \
  "s_lshl1_add_u32 s90, s87, s90\n\t" \
  "s_lshl_b32 s90, s90, 13\n\t" \
  "v_add_f32 v160, v160, v144\n\t" \
  "v_add_f32 v161, v161, v145\n\t" \
  "v_add_f32 v162, v162, v146\n\t" \
  "v_add_f32 v163, v163, v147\n\t" \
  "v_add_f32 v164, v164, v148\n\t" \
  "v_add_f32 v165, v165, v149\n\t" \
  "v_add_f32 v166, v166, v150\n\t" \
  "v_add_f32 v167, v167, v151\n\t" \
  "v_cvt_pk_bf16_f32 v168, v160, v161\n\t" \
  "v_cvt_pk_bf16_f32 v169, v162, v163\n\t" \
  "v_cvt_pk_bf16_f32 v170, v164, v165\n\t" \
  "v_cvt_pk_bf16_f32 v171, v166, v167\n\t" \
  "buffer_store_dwordx4 v[168:171], v172, %[rt], s90 offen sc1\n\t" \
  "s_waitcnt vmcnt(0)\n\t" \
  "s_barrier\n\t" \
  "s_sub_i32 s91, s86, 1\n\t" \
  "v_mov_b32 v173, s91\n\t" \
  "buffer_store_dword v173, v174, %[rs], 0 offen sc1\n\t" \
  "s_mov_b32 m0, s94\n\t" \
  ".Le_%=:\n\t"
#define PP_ASM_1 \
  "s_cmp_lg_u32 %[kh], 0\n\t" \
  "s_cbranch_scc1 .Lk_%=\n\t" \
  "s_mov_b32 s94, m0\n\t" \
  "s_mov_b64 s[92:93], %[x0]\n\t" \
  "s_mov_b32 s86, %[g0]\n\t" \
  "s_add_u32 s87, s86, %[t0]\n\t" \
  "s_add_u32 m0, %[lw], 67584\n\t" \
  "s_nop 0\n\t" \
  "global_load_lds_dwordx4 v250, s[92:93] sc1\n\t" \
  "s_add_u32 m0, %[lw], 68608\n\t" \
  "s_nop 0\n\t" \
  "global_load_lds_dwordx4 v251, s[92:93] sc1\n\t" \
  "s_barrier\n\t" \
  "s_branch .Ls0_%=\n\t" \
  ".Lp0_%=:\n\t" \
  "s_waitcnt vmcnt(9)\n\t" \
  "s_barrier\n\t" \
  ".Ls0_%=:\n\t" \
  "s_add_u32 s92, s92, 512\n\t" \
  "s_addc_u32 s93, s93, 0\n\t" \
  "ds_read_b128 v[176:179], v240 offset:0\n\t" \
  "ds_read_b128 v[180:183], v240 offset:16384\n\t" \
  "ds_read_b128 v[184:187], v241 offset:0\n\t" \
  "ds_read_b128 v[188:191], v241 offset:16384\n\t" \
  "s_add_u32 m0, %[lw], 98304\n\t" \
  "s_nop 0\n\t" \
  "global_load_lds_dwordx4 v248, s[92:93] sc1\n\t" \
  "ds_read_b128 v[192:195], v242 offset:0\n\t" \
  "ds_read_b128 v[196:199], v242 offset:16384\n\t" \
  "ds_read_b128 v[200:203], v243 offset:0\n\t" \
  "ds_read_b128 v[204:207], v243 offset:16384\n\t" \
  "s_add_u32 m0, %[lw], 99328\n\t" \
  "s_nop 0\n\t" \
  "global_load_lds_dwordx4 v249, s[92:93] sc1\n\t" \
  "ds_read_b128 v[208:211], v240 offset:256\n\t" \
  "ds_read_b128 v[212:215], v240 offset:16640\n\t" \
  "ds_read_b128 v[216:219], v241 offset:256\n\t" \
  "ds_read_b128 v[220:223], v241 offset:16640\n\t" \
  "s_add_u32 m0, %[lw], 100352\n\t" \
  "s_nop 0\n\t" \
  "global_load_lds_dwordx4 v250, s[92:93] sc1\n\t" \
  "ds_read_b128 v[224:227], v242 offset:256\n\t" \
  "ds_read_b128 v[228:231], v242 offset:16640\n\t" \
  "ds_read_b128 v[232:235], v243 offset:256\n\t" \
  "ds_read_b128 v[236:239], v243 offset:16640\n\t" \
  "s_add_u32 m0, %[lw], 101376\n\t" \
  "s_nop 0\n\t" \
  "global_load_lds_dwordx4 v251, s[92:93] sc1\n\t" \
  "ds_write_b128 v252, v[152:155] offset:0\n\t" \
  "ds_write_b128 v252, v[156:159] offset:1024\n\t" \
  "s_waitcnt lgkmcnt(0)\n\t" \
  "s_setprio 1\n\t" \
  "v_mfma_f32_16x16x32_bf16 v[128:131], v[176:179], v[0:3], 0\n\t" \
  "v_mfma_f32_16x16x32_bf16 v[136:139], v[176:179], v[4:7], 0\n\t" \
  "v_mfma_f32_16x16x32_bf16 v[132:135], v[180:183], v[0:3], 0\n\t" \
  "v_mfma_f32_16x16x32_bf16 v[140:143], v[180:183], v[4:7], 0\n\t" \
  "v_mfma_f32_16x16x32_bf16 v[128:131], v[184:187], v[8:11], v[128:131]\n\t" \
  "v_mfma_f32_16x16x32_bf16 v[136:139], v[184:187], v[12:15], v[136:139]\n\t" \
  "v_mfma_f32_16x16x32_bf16 v[132:135], v[188:191], v[8:11], v[132:135]\n\t" \
  "v_mfma_f32_16x16x32_bf16 v[140:143], v[188:191], v[12:15], v[140:143]\n\t" \
  "v_mfma_f32_16x16x32_bf16 v[128:131], v[192:195], v[16:19], v[128:131]\n\t" \
  "v_mfma_f32_16x16x32_bf16 v[136:139], v[192:195], v[20:23], v[136:139]\n\t" \
  "v_mfma_f32_16x16x32_bf16 v[132:135], v[196:199], v[16:19], v[132:135]\n\t" \
  "v_mfma_f32_16x16x32_bf16 v[140:143], v[196:199], v[20:23], v[140:143]\n\t" \
  "v_mfma_f32_16x16x32_bf16 v[128:131], v[200:203], v[24:27], v[128:131]\n\t" \
  "v_mfma_f32_16x16x32_bf16 v[136:139], v[200:203], v[28:31], v[136:139]\n\t" \
  "v_mfma_f32_16x16x32_bf16 v[132:135], v[204:207], v[24:27], v[132:135]\n\t" \
  "v_mfma_f32_16x16x32_bf16 v[140:143], v[204:207], v[28:31], v[140:143]\n\t" \
  "v_mfma_f32_16x16x32_bf16 v[128:131], v[208:211], v[32:35], v[128:131]\n\t" \
  "v_mfma_f32_16x16x32_bf16 v[136:139], v[208:211], v[36:39], v[136:139]\n\t" \
  "v_mfma_f32_16x16x32_bf16 v[132:135], v[212:215], v[32:35], v[132:135]\n\t" \
  "v_mfma_f32_16x16x32_bf16 v[140:143], v[212:215], v[36:39], v[140:143]\n\t" \
  "v_mfma_f32_16x16x32_bf16 v[128:131], v[216:219], v[40:43], v[128:131]\n\t" \
  "v_mfma_f32_16x16x32_bf16 v[136:139], v[216:219], v[44:47], v[136:139]\n\t" \
  "v_mfma_f32_16x16x32_bf16 v[132:135], v[220:223], v[40:43], v[132:135]\n\t" \
  "v_mfma_f32_16x16x32_bf16 v[140:143], v[220:223], v[44:47], v[140:143]\n\t" \
  "v_mfma_f32_16x16x32_bf16 v[128:131], v[224:227], v[48:51], v[128:131]\n\t" \
  "v_mfma_f32_16x16x32_bf16 v[136:139], v[224:227], v[52:55], v[136:139]\n\t" \
  "v_mfma_f32_16x16x32_bf16 v[132:135], v[228:231], v[48:51], v[132:135]\n\t" \
  "v_mfma_f32_16x16x32_bf16 v[140:143], v[228:231], v[52:55], v[140:143]\n\t" \
  "v_mfma_f32_16x16x32_bf16 v[128:131], v[232:235], v[56:59], v[128:131]\n\t" \
  "v_mfma_f32_16x16x32_bf16 v[136:139], v[232:235], v[60:63], v[136:139]\n\t" \
  "v_mfma_f32_16x16x32_bf16 v[132:135], v[236:239], v[56:59], v[132:135]\n\t" \
  "v_mfma_f32_16x16x32_bf16 v[140:143], v[236:239], v[60:63], v[140:143]\n\t" \
  "s_setprio 0\n\t" \
  "s_waitcnt vmcnt(4)\n\t" \
  "s_barrier\n\t" \
  "s_add_u32 s92, s92, 65024\n\t" \
  "s_addc_u32 s93, s93, 0\n\t" \
  "ds_read_b128 v[160:163], v253 offset:0\n\t" \
  "ds_read_b128 v[164:167], v253 offset:1024\n\t" \
  "ds_read_b128 v[176:179], v240 offset:32768\n\t" \
  "ds_read_b128 v[180:183], v240 offset:49152\n\t" \
  "ds_read_b128 v[184:187], v241 offset:32768\n\t" \
  "ds_read_b128 v[188:191], v241 offset:49152\n\t" \
  "s_add_u32 m0, %[lw], 0\n\t" \
  "s_nop 0\n\t" \
  "global_load_lds_dwordx4 v248, s[92:93] sc1\n\t" \
  "ds_read_b128 v[192:195], v242 offset:32768\n\t" \
  "ds_read_b128 v[196:199], v242 offset:49152\n\t" \
  "ds_read_b128 v[200:203], v243 offset:32768\n\t" \
  "ds_read_b128 v[204:207], v243 offset:49152\n\t" \
  "s_add_u32 m0, %[lw], 1024\n\t" \
  "s_nop 0\n\t" \
  "global_load_lds_dwordx4 v249, s[92:93] sc1\n\t" \
  "ds_read_b128 v[208:211], v240 offset:33024\n\t" \
  "ds_read_b128 v[212:215], v240 offset:49408\n\t" \
  "ds_read_b128 v[216:219], v241 offset:33024\n\t" \
  "ds_read_b128 v[220:223], v241 offset:49408\n\t" \
  "s_add_u32 m0, %[lw], 2048\n\t" \
  "s_nop 0\n\t" \
  "global_load_lds_dwordx4 v250, s[92:93] sc1\n\t" \
  "ds_read_b128 v[224:227], v242 offset:33024\n\t" \
  "ds_read_b128 v[228:231], v242 offset:49408\n\t" \
  "ds_read_b128 v[232:235], v243 offset:33024\n\t" \
  "ds_read_b128 v[236:239], v243 offset:49408\n\t" \
  "s_add_u32 m0, %[lw], 3072\n\t" \
  "s_nop 0\n\t" \
  "global_load_lds_dwordx4 v251, s[92:93] sc1\n\t" \
  "s_waitcnt lgkmcnt(15)\n\t" \
  "s_waitcnt lgkmcnt(12)\n\t" \
  "s_lshr_b32 s90, s87, 7\n\t" \
  "s_xor_b32 s90, s90, %[s0]\n\t" \
  "s_and_b32 s90, s90, 1\n\t" \
  "s_lshl1_add_u32 s90, s87, s90\n\t" \
  "s_lshl_b32 s90, s90, 13\n\t" \
  "v_add_f32 v160, v160, v144\n\t" \
  "v_add_f32 v161, v161, v145\n\t" \
  "v_add_f32 v162, v162, v146\n\t" \
  "v_add_f32 v163, v163, v147\n\t" \
  "v_add_f32 v164, v164, v148\n\t" \
  "v_add_f32 v165, v165, v149\n\t" \
  "v_add_f32 v166, v166, v150\n\t" \
  "v_add_f32 v167, v167, v151\n\t" \
  "v_cvt_pk_bf16_f32 v168, v160, v161\n\t" \
  "v_cvt_pk_bf16_f32 v169, v162, v163\n\t" \
  "v_cvt_pk_bf16_f32 v170, v164, v165\n\t" \
  "v_cvt_pk_bf16_f32 v171, v166, v167\n\t" \
  "buffer_store_dwordx4 v[168:171], v172, %[rt], s90 offen sc1\n\t" \
  "s_add_u32 s86, s86, 1\n\t" \
  "s_add_u32 s87, s87, 1\n\t" \
  "s_waitcnt lgkmcnt(0)\n\t" \
  "s_setprio 1\n\t" \
  "v_mfma_f32_16x16x32_bf16 v[128:131], v[176:179], v[64:67], v[128:131]\n\t" \
  "v_mfma_f32_16x16x32_bf16 v[136:139], v[176:179], v[68:71], v[136:139]\n\t" \
  "v_mfma_f32_16x16x32_bf16 v[132:135], v[180:183], v[64:67], v[132:135]\n\t" \
  "v_mfma_f32_16x16x32_bf16 v[140:143], v[180:183], v[68:71], v[140:143]\n\t" \
  "v_mfma_f32_16x16x32_bf16 v[128:131], v[184:187], v[72:75], v[128:131]\n\t" \
  "v_mfma_f32_16x16x32_bf16 v[136:139], v[184:187], v[76:79], v[136:139]\n\t" \
  "v_mfma_f32_16x16x32_bf16 v[132:135], v[188:191], v[72:75], v[132:135]\n\t" \
  "v_mfma_f32_16x16x32_bf16 v[140:143], v[188:191], v[76:79], v[140:143]\n\t" \
  "v_mfma_f32_16x16x32_bf16 v[128:131], v[192:195], v[80:83], v[128:131]\n\t" \
  "v_mfma_f32_16x16x32_bf16 v[136:139], v[192:195], v[84:87], v[136:139]\n\t" \
  "v_mfma_f32_16x16x32_bf16 v[132:135], v[196:199], v[80:83], v[132:135]\n\t" \
  "v_mfma_f32_16x16x32_bf16 v[140:143], v[196:199], v[84:87], v[140:143]\n\t" \
  "v_mfma_f32_16x16x32_bf16 v[128:131], v[200:203], v[88:91], v[128:131]\n\t" \
  "v_mfma_f32_16x16x32_bf16 v[136:139], v[200:203], v[92:95], v[136:139]\n\t" \
  "v_mfma_f32_16x16x32_bf16 v[132:135], v[204:207], v[88:91], v[132:135]\n\t" \
  "v_mfma_f32_16x16x32_bf16 v[140:143], v[204:207], v[92:95], v[140:143]\n\t" \
  "v_mfma_f32_16x16x32_bf16 v[128:131], v[208:211], v[96:99], v[128:131]\n\t" \
  "v_mfma_f32_16x16x32_bf16 v[136:139], v[208:211], v[100:103], v[136:139]\n\t" \
  "v_mfma_f32_16x16x32_bf16 v[132:135], v[212:215], v[96:99], v[132:135]\n\t" \
  "v_mfma_f32_16x16x32_bf16 v[140:143], v[212:215], v[100:103], v[140:143]\n\t" \
  "v_mfma_f32_16x16x32_bf16 v[128:131], v[216:219], v[104:107], v[128:131]\n\t" \
  "v_mfma_f32_16x16x32_bf16 v[136:139], v[216:219], v[108:111], v[136:139]\n\t" \
  "v_mfma_f32_16x16x32_bf16 v[132:135], v[220:223], v[104:107], v[132:135]\n\t" \
  "v_mfma_f32_16x16x32_bf16 v[140:143], v[220:223], v[108:111], v[140:143]\n\t" \
  "v_mfma_f32_16x16x32_bf16 v[128:131], v[224:227], v[112:115], v[128:131]\n\t" \
  "v_mfma_f32_16x16x32_bf16 v[136:139], v[224:227], v[116:119], v[136:139]\n\t" \
  "v_mfma_f32_16x16x32_bf16 v[132:135], v[228:231], v[112:115], v[132:135]\n\t" \
  "v_mfma_f32_16x16x32_bf16 v[140:143], v[228:231], v[116:119], v[140:143]\n\t" \
  "v_mfma_f32_16x16x32_bf16 v[128:131], v[232:235], v[120:123], v[128:131]\n\t" \
  "v_mfma_f32_16x16x32_bf16 v[136:139], v[232:235], v[124:127], v[136:139]\n\t" \
  "v_mfma_f32_16x16x32_bf16 v[132:135], v[236:239], v[120:123], v[132:135]\n\t" \
  "v_mfma_f32_16x16x32_bf16 v[140:143], v[236:239], v[124:127], v[140:143]\n\t" \
  "s_setprio 0\n\t" \
  "s_waitcnt vmcnt(9)\n\t" \
  "s_barrier\n\t" \
  "s_add_u32 s92, s92, 512\n\t" \
  "s_addc_u32 s93, s93, 0\n\t" \
  "ds_read_b128 v[176:179], v244 offset:0\n\t" \
  "ds_read_b128 v[180:183], v244 offset:16384\n\t" \
  "ds_read_b128 v[184:187], v245 offset:0\n\t" \
  "ds_read_b128 v[188:191], v245 offset:16384\n\t" \
  "s_add_u32 m0, %[lw], 32768\n\t" \
  "s_nop 0\n\t" \
  "global_load_lds_dwordx4 v248, s[92:93] sc1\n\t" \
  "ds_read_b128 v[192:195], v246 offset:0\n\t" \
  "ds_read_b128 v[196:199], v246 offset:16384\n\t" \
  "ds_read_b128 v[200:203], v247 offset:0\n\t" \
  "ds_read_b128 v[204:207], v247 offset:16384\n\t" \
  "s_add_u32 m0, %[lw], 33792\n\t" \
  "s_nop 0\n\t" \
  "global_load_lds_dwordx4 v249, s[92:93] sc1\n\t" \
  "ds_read_b128 v[208:211], v244 offset:256\n\t" \
  "ds_read_b128 v[212:215], v244 offset:16640\n\t" \
  "ds_read_b128 v[216:219], v245 offset:256\n\t" \
  "ds_read_b128 v[220:223], v245 offset:16640\n\t" \
  "s_add_u32 m0, %[lw], 34816\n\t" \
  "s_nop 0\n\t" \
  "global_load_lds_dwordx4 v250, s[92:93] sc1\n\t" \
  "ds_read_b128 v[224:227], v246 offset:256\n\t" \
  "ds_read_b128 v[228:231], v246 offset:16640\n\t" \
  "ds_read_b128 v[232:235], v247 offset:256\n\t" \
  "ds_read_b128 v[236:239], v247 offset:16640\n\t" \
  "s_add_u32 m0, %[lw], 35840\n\t" \
  "s_nop 0\n\t" \
  "global_load_lds_dwordx4 v251, s[92:93] sc1\n\t" \
  "ds_write_b128 v252, v[136:139] offset:0\n\t" \
  "ds_write_b128 v252, v[140:143] offset:1024\n\t" \
  "s_waitcnt lgkmcnt(0)\n\t" \
  "s_setprio 1\n\t" \
  "v_mfma_f32_16x16x32_bf16 v[144:147], v[176:179], v[0:3], 0\n\t" \
  "v_mfma_f32_16x16x32_bf16 v[152:155], v[176:179], v[4:7], 0\n\t" \
  "v_mfma_f32_16x16x32_bf16 v[148:151], v[180:183], v[0:3], 0\n\t" \
  "v_mfma_f32_16x16x32_bf16 v[156:159], v[180:183], v[4:7], 0\n\t" \
  "v_mfma_f32_16x16x32_bf16 v[144:147], v[184:187], v[8:11], v[144:147]\n\t" \
  "v_mfma_f32_16x16x32_bf16 v[152:155], v[184:187], v[12:15], v[152:155]\n\t" \
  "v_mfma_f32_16x16x32_bf16 v[148:151], v[188:191], v[8:11], v[148:151]\n\t" \
  "v_mfma_f32_16x16x32_bf16 v[156:159], v[188:191], v[12:15], v[156:159]\n\t" \
  "v_mfma_f32_16x16x32_bf16 v[144:147], v[192:195], v[16:19], v[144:147]\n\t" \
  "v_mfma_f32_16x16x32_bf16 v[152:155], v[192:195], v[20:23], v[152:155]\n\t" \
  "v_mfma_f32_16x16x32_bf16 v[148:151], v[196:199], v[16:19], v[148:151]\n\t" \
  "v_mfma_f32_16x16x32_bf16 v[156:159], v[196:199], v[20:23], v[156:159]\n\t" \
  "v_mfma_f32_16x16x32_bf16 v[144:147], v[200:203], v[24:27], v[144:147]\n\t" \
  "v_mfma_f32_16x16x32_bf16 v[152:155], v[200:203], v[28:31], v[152:155]\n\t" \
  "v_mfma_f32_16x16x32_bf16 v[148:151], v[204:207], v[24:27], v[148:151]\n\t" \
  "v_mfma_f32_16x16x32_bf16 v[156:159], v[204:207], v[28:31], v[156:159]\n\t" \
  "v_mfma_f32_16x16x32_bf16 v[144:147], v[208:211], v[32:35], v[144:147]\n\t" \
  "v_mfma_f32_16x16x32_bf16 v[152:155], v[208:211], v[36:39], v[152:155]\n\t" \
  "v_mfma_f32_16x16x32_bf16 v[148:151], v[212:215], v[32:35], v[148:151]\n\t" \
  "v_mfma_f32_16x16x32_bf16 v[156:159], v[212:215], v[36:39], v[156:159]\n\t" \
  "v_mfma_f32_16x16x32_bf16 v[144:147], v[216:219], v[40:43], v[144:147]\n\t" \
  "v_mfma_f32_16x16x32_bf16 v[152:155], v[216:219], v[44:47], v[152:155]\n\t" \
  "v_mfma_f32_16x16x32_bf16 v[148:151], v[220:223], v[40:43], v[148:151]\n\t" \
  "v_mfma_f32_16x16x32_bf16 v[156:159], v[220:223], v[44:47], v[156:159]\n\t" \
  "v_mfma_f32_16x16x32_bf16 v[144:147], v[224:227], v[48:51], v[144:147]\n\t" \
  "v_mfma_f32_16x16x32_bf16 v[152:155], v[224:227], v[52:55], v[152:155]\n\t" \
  "v_mfma_f32_16x16x32_bf16 v[148:151], v[228:231], v[48:51], v[148:151]\n\t" \
  "v_mfma_f32_16x16x32_bf16 v[156:159], v[228:231], v[52:55], v[156:159]\n\t" \
  "v_mfma_f32_16x16x32_bf16 v[144:147], v[232:235], v[56:59], v[144:147]\n\t" \
  "v_mfma_f32_16x16x32_bf16 v[152:155], v[232:235], v[60:63], v[152:155]\n\t" \
  "v_mfma_f32_16x16x32_bf16 v[148:151], v[236:239], v[56:59], v[148:151]\n\t" \
  "v_mfma_f32_16x16x32_bf16 v[156:159], v[236:239], v[60:63], v[156:159]\n\t" \
  "s_setprio 0\n\t" \
  "s_waitcnt vmcnt(4)\n\t" \
  "s_barrier\n\t" \
  "s_add_u32 s92, s92, 65024\n\t" \
  "s_addc_u32 s93, s93, 0\n\t" \
  "ds_read_b128 v[160:163], v253 offset:0\n\t" \
  "ds_read_b128 v[164:167], v253 offset:1024\n\t" \
  "ds_read_b128 v[176:179], v244 offset:32768\n\t" \
  "ds_read_b128 v[180:183], v244 offset:49152\n\t" \
  "ds_read_b128 v[184:187], v245 offset:32768\n\t" \
  "ds_read_b128 v[188:191], v245 offset:49152\n\t" \
  "s_add_u32 m0, %[lw], 65536\n\t" \
  "s_nop 0\n\t" \
  "global_load_lds_dwordx4 v248, s[92:93] sc1\n\t" \
  "ds_read_b128 v[192:195], v246 offset:32768\n\t" \
  "ds_read_b128 v[196:199], v246 offset:49152\n\t" \
  "ds_read_b128 v[200:203], v247 offset:32768\n\t" \
  "ds_read_b128 v[204:207], v247 offset:49152\n\t" \
  "s_add_u32 m0, %[lw], 66560\n\t" \
  "s_nop 0\n\t" \
  "global_load_lds_dwordx4 v249, s[92:93] sc1\n\t" \
  "ds_read_b128 v[208:211], v244 offset:33024\n\t" \
  "ds_read_b128 v[212:215], v244 offset:49408\n\t" \
  "ds_read_b128 v[216:219], v245 offset:33024\n\t" \
  "ds_read_b128 v[220:223], v245 offset:49408\n\t" \
  "s_add_u32 m0, %[lw], 67584\n\t" \
  "s_nop 0\n\t" \
  "global_load_lds_dwordx4 v250, s[92:93] sc1\n\t" \
  "ds_read_b128 v[224:227], v246 offset:33024\n\t" \
  "ds_read_b128 v[228:231], v246 offset:49408\n\t" \
  "ds_read_b128 v[232:235], v247 offset:33024\n\t" \
  "ds_read_b128 v[236:239], v247 offset:49408\n\t" \
  "s_add_u32 m0, %[lw], 68608\n\t" \
  "s_nop 0\n\t" \
  "global_load_lds_dwordx4 v251, s[92:93] sc1\n\t" \
  "s_waitcnt lgkmcnt(15)\n\t" \
  "s_waitcnt lgkmcnt(12)\n\t" \
  "s_lshr_b32 s90, s87, 7\n\t" \
  "s_xor_b32 s90, s90, %[s0]\n\t" \
  "s_and_b32 s90, s90, 1\n\t" \
  "s_lshl1_add_u32 s90, s87, s90\n\t" \
  "s_lshl_b32 s90, s90, 13\n\t" \
  "v_add_f32 v160, v160, v128\n\t" \
  "v_add_f32 v161, v161, v129\n\t" \
  "v_add_f32 v162, v162, v130\n\t" \
  "v_add_f32 v163, v163, v131\n\t" \
  "v_add_f32 v164, v164, v132\n\t" \
  "v_add_f32 v165, v165, v133\n\t" \
  "v_add_f32 v166, v166, v134\n\t" \
  "v_add_f32 v167, v167, v135\n\t" \
  "v_cvt_pk_bf16_f32 v168, v160, v161\n\t" \
  "v_cvt_pk_bf16_f32 v169, v162, v163\n\t" \
  "v_cvt_pk_bf16_f32 v170, v164, v165\n\t" \
  "v_cvt_pk_bf16_f32 v171, v166, v167\n\t" \
  "buffer_store_dwordx4 v[168:171], v172, %[rt], s90 offen sc1\n\t" \
  "s_add_u32 s86, s86, 1\n\t" \
  "s_add_u32 s87, s87, 1\n\t" \
  "s_waitcnt lgkmcnt(0)\n\t" \
  "s_setprio 1\n\t" \
  "v_mfma_f32_16x16x32_bf16 v[144:147], v[176:179], v[64:67], v[144:147]\n\t" \
  "v_mfma_f32_16x16x32_bf16 v[152:155], v[176:179], v[68:71], v[152:155]\n\t" \
  "v_mfma_f32_16x16x32_bf16 v[148:151], v[180:183], v[64:67], v[148:151]\n\t" \
  "v_mfma_f32_16x16x32_bf16 v[156:159], v[180:183], v[68:71], v[156:159]\n\t" \
  "v_mfma_f32_16x16x32_bf16 v[144:147], v[184:187], v[72:75], v[144:147]\n\t" \
  "v_mfma_f32_16x16x32_bf16 v[152:155], v[184:187], v[76:79], v[152:155]\n\t" \
  "v_mfma_f32_16x16x32_bf16 v[148:151], v[188:191], v[72:75], v[148:151]\n\t" \
  "v_mfma_f32_16x16x32_bf16 v[156:159], v[188:191], v[76:79], v[156:159]\n\t" \
  "v_mfma_f32_16x16x32_bf16 v[144:147], v[192:195], v[80:83], v[144:147]\n\t" \
  "v_mfma_f32_16x16x32_bf16 v[152:155], v[192:195], v[84:87], v[152:155]\n\t" \
  "v_mfma_f32_16x16x32_bf16 v[148:151], v[196:199], v[80:83], v[148:151]\n\t" \
  "v_mfma_f32_16x16x32_bf16 v[156:159], v[196:199], v[84:87], v[156:159]\n\t" \
  "v_mfma_f32_16x16x32_bf16 v[144:147], v[200:203], v[88:91], v[144:147]\n\t" \
  "v_mfma_f32_16x16x32_bf16 v[152:155], v[200:203], v[92:95], v[152:155]\n\t" \
  "v_mfma_f32_16x16x32_bf16 v[148:151], v[204:207], v[88:91], v[148:151]\n\t" \
  "v_mfma_f32_16x16x32_bf16 v[156:159], v[204:207], v[92:95], v[156:159]\n\t" \
  "v_mfma_f32_16x16x32_bf16 v[144:147], v[208:211], v[96:99], v[144:147]\n\t" \
  "v_mfma_f32_16x16x32_bf16 v[152:155], v[208:211], v[100:103], v[152:155]\n\t" \
  "v_mfma_f32_16x16x32_bf16 v[148:151], v[212:215], v[96:99], v[148:151]\n\t" \
  "v_mfma_f32_16x16x32_bf16 v[156:159], v[212:215], v[100:103], v[156:159]\n\t" \
  "v_mfma_f32_16x16x32_bf16 v[144:147], v[216:219], v[104:107], v[144:147]\n\t" \
  "v_mfma_f32_16x16x32_bf16 v[152:155], v[216:219], v[108:111], v[152:155]\n\t" \
  "v_mfma_f32_16x16x32_bf16 v[148:151], v[220:223], v[104:107], v[148:151]\n\t" \
  "v_mfma_f32_16x16x32_bf16 v[156:159], v[220:223], v[108:111], v[156:159]\n\t" \
  "v_mfma_f32_16x16x32_bf16 v[144:147], v[224:227], v[112:115], v[144:147]\n\t" \
  "v_mfma_f32_16x16x32_bf16 v[152:155], v[224:227], v[116:119], v[152:155]\n\t" \
  "v_mfma_f32_16x16x32_bf16 v[148:151], v[228:231], v[112:115], v[148:151]\n\t" \
  "v_mfma_f32_16x16x32_bf16 v[156:159], v[228:231], v[116:119], v[156:159]\n\t" \
  "v_mfma_f32_16x16x32_bf16 v[144:147], v[232:235], v[120:123], v[144:147]\n\t" \
  "v_mfma_f32_16x16x32_bf16 v[152:155], v[232:235], v[124:127], v[152:155]\n\t" \
  "v_mfma_f32_16x16x32_bf16 v[148:151], v[236:239], v[120:123], v[148:151]\n\t" \
  "v_mfma_f32_16x16x32_bf16 v[156:159], v[236:239], v[124:127], v[156:159]\n\t" \
  "s_setprio 0\n\t" \
  "s_cmp_lt_u32 s86, 64\n\t" \
  "s_cbranch_scc1 .Lp0_%=\n\t" \
  "s_barrier\n\t" \
  "s_nop 1\n\t" \
  "ds_write_b128 v252, v[152:155] offset:0\n\t" \
  "s_nop 0\n\t" \
  "ds_write_b128 v252, v[156:159] offset:1024\n\t" \
  "s_waitcnt lgkmcnt(0)\n\t" \
  "s_barrier\n\t" \
  "ds_read_b128 v[160:163], v253 offset:0\n\t" \
  "ds_read_b128 v[164:167], v253 offset:1024\n\t" \
  "s_waitcnt lgkmcnt(0)\n\t" \
  "s_lshr_b32 s90, s87, 7\n\t" \
  "s_xor_b32 s90, s90, %[s0]\n\t" \
  "s_and_b32 s90, s90, 1\n\t" \
  "s_lshl1_add_u32 s90, s87, s90\n\t" \
  "s_lshl_b32 s90, s90, 13\n\t" \
  "v_add_f32 v160, v160, v144\n\t" \
  "v_add_f32 v161, v161, v145\n\t" \
  "v_add_f32 v162, v162, v146\n\t" \
  "v_add_f32 v163, v163, v147\n\t" \
  "v_add_f32 v164, v164, v148\n\t" \
  "v_add_f32 v165, v165, v149\n\t" \
  "v_add_f32 v166, v166, v150\n\t" \
  "v_add_f32 v167, v167, v151\n\t" \
  "v_cvt_pk_bf16_f32 v168, v160, v161\n\t" \
  "v_cvt_pk_bf16_f32 v169, v162, v163\n\t" \
  "v_cvt_pk_bf16_f32 v170, v164, v165\n\t" \
  "v_cvt_pk_bf16_f32 v171, v166, v167\n\t" \
  "buffer_store_dwordx4 v[168:171], v172, %[rt], s90 offen sc1\n\t" \
  "s_waitcnt vmcnt(0)\n\t" \
  "s_barrier\n\t" \
  "s_mov_b32 m0, s94\n\t" \
  "s_branch .Le_%=\n\t" \
  ".Lk_%=:\n\t" \
  "s_mov_b32 s94, m0\n\t" \
  "s_mov_b64 s[92:93], %[x0]\n\t" \
  "s_mov_b32 s86, %[g0]\n\t" \
  "s_add_u32 s87, s86, %[t0]\n\t" \
  "s_add_u32 m0, %[lw], 67584\n\t" \
  "s_nop 0\n\t" \
  "global_load_lds_dwordx4 v250, s[92:93] sc1\n\t" \
  "s_add_u32 m0, %[lw], 68608\n\t" \
  "s_nop 0\n\t" \
  "global_load_lds_dwordx4 v251, s[92:93] sc1\n\t" \
  "s_barrier\n\t" \
  "s_branch .Ls1_%=\n\t" \
  ".Lp1_%=:\n\t" \
  "s_waitcnt vmcnt(10)\n\t" \
  "s_barrier\n\t" \
  "s_setprio 2\n\t" \
  "v_mfma_f32_16x16x32_bf16 v[144:147], v[176:179], v[64:67], v[144:147]\n\t" \
  "v_mfma_f32_16x16x32_bf16 v[152:155], v[176:179], v[68:71], v[152:155]\n\t" \
  "v_mfma_f32_16x16x32_bf16 v[148:151], v[180:183], v[64:67], v[148:151]\n\t" \
  "v_mfma_f32_16x16x32_bf16 v[156:159], v[180:183], v[68:71], v[156:159]\n\t" \
  "v_mfma_f32_16x16x32_bf16 v[144:147], v[184:187], v[72:75], v[144:147]\n\t" \
  "v_mfma_f32_16x16x32_bf16 v[152:155], v[184:187], v[76:79], v[152:155]\n\t" \
  "v_mfma_f32_16x16x32_bf16 v[148:151], v[188:191], v[72:75], v[148:151]\n\t" \
  "v_mfma_f32_16x16x32_bf16 v[156:159], v[188:191], v[76:79], v[156:159]\n\t" \
  "v_mfma_f32_16x16x32_bf16 v[144:147], v[192:195], v[80:83], v[144:147]\n\t" \
  "v_mfma_f32_16x16x32_bf16 v[152:155], v[192:195], v[84:87], v[152:155]\n\t" \
  "v_mfma_f32_16x16x32_bf16 v[148:151], v[196:199], v[80:83], v[148:151]\n\t" \
  "v_mfma_f32_16x16x32_bf16 v[156:159], v[196:199], v[84:87], v[156:159]\n\t" \
  "v_mfma_f32_16x16x32_bf16 v[144:147], v[200:203], v[88:91], v[144:147]\n\t" \
  "v_mfma_f32_16x16x32_bf16 v[152:155], v[200:203], v[92:95], v[152:155]\n\t" \
  "v_mfma_f32_16x16x32_bf16 v[148:151], v[204:207], v[88:91], v[148:151]\n\t" \
  "v_mfma_f32_16x16x32_bf16 v[156:159], v[204:207], v[92:95], v[156:159]\n\t" \
  "v_mfma_f32_16x16x32_bf16 v[144:147], v[208:211], v[96:99], v[144:147]\n\t" \
  "v_mfma_f32_16x16x32_bf16 v[152:155], v[208:211], v[100:103], v[152:155]\n\t" \
  "v_mfma_f32_16x16x32_bf16 v[148:151], v[212:215], v[96:99], v[148:151]\n\t" \
  "v_mfma_f32_16x16x32_bf16 v[156:159], v[212:215], v[100:103], v[156:159]\n\t" \
  "v_mfma_f32_16x16x32_bf16 v[144:147], v[216:219], v[104:107], v[144:147]\n\t" \
  "v_mfma_f32_16x16x32_bf16 v[152:155], v[216:219], v[108:111], v[152:155]\n\t" \
  "v_mfma_f32_16x16x32_bf16 v[148:151], v[220:223], v[104:107], v[148:151]\n\t" \
  "v_mfma_f32_16x16x32_bf16 v[156:159], v[220:223], v[108:111], v[156:159]\n\t" \
  "v_mfma_f32_16x16x32_bf16 v[144:147], v[224:227], v[112:115], v[144:147]\n\t" \
  "v_mfma_f32_16x16x32_bf16 v[152:155], v[224:227], v[116:119], v[152:155]\n\t" \
  "v_mfma_f32_16x16x32_bf16 v[148:151], v[228:231], v[112:115], v[148:151]\n\t" \
  "v_mfma_f32_16x16x32_bf16 v[156:159], v[228:231], v[116:119], v[156:159]\n\t" \
  "v_mfma_f32_16x16x32_bf16 v[144:147], v[232:235], v[120:123], v[144:147]\n\t" \
  "v_mfma_f32_16x16x32_bf16 v[152:155], v[232:235], v[124:127], v[152:155]\n\t" \
  "v_mfma_f32_16x16x32_bf16 v[148:151], v[236:239], v[120:123], v[148:151]\n\t" \
  "v_mfma_f32_16x16x32_bf16 v[156:159], v[236:239], v[124:127], v[156:159]\n\t" \
  "s_setprio 0\n\t" \
  ".Ls1_%=:\n\t" \
  "s_add_u32 s92, s92, 512\n\t" \
  "s_addc_u32 s93, s93, 0\n\t" \
  "ds_read_b128 v[176:179], v240 offset:0\n\t" \
  "ds_read_b128 v[180:183], v240 offset:16384\n\t" \
  "ds_read_b128 v[184:187], v241 offset:0\n\t" \
  "ds_read_b128 v[188:191], v241 offset:16384\n\t" \
  "s_add_u32 m0, %[lw], 98304\n\t" \
  "s_nop 0\n\t" \
  "global_load_lds_dwordx4 v248, s[92:93] sc1\n\t" \
  "ds_read_b128 v[192:195], v242 offset:0\n\t" \
  "ds_read_b128 v[196:199], v242 offset:16384\n\t" \
  "ds_read_b128 v[200:203], v243 offset:0\n\t" \
  "ds_read_b128 v[204:207], v243 offset:16384\n\t" \
  "s_add_u32 m0, %[lw], 99328\n\t" \
  "s_nop 0\n\t" \
  "global_load_lds_dwordx4 v249, s[92:93] sc1\n\t" \
  "ds_read_b128 v[208:211], v240 offset:256\n\t" \
  "ds_read_b128 v[212:215], v240 offset:16640\n\t" \
  "ds_read_b128 v[216:219], v241 offset:256\n\t" \
  "ds_read_b128 v[220:223], v241 offset:16640\n\t" \
  "s_add_u32 m0, %[lw], 100352\n\t" \
  "s_nop 0\n\t" \
  "global_load_lds_dwordx4 v250, s[92:93] sc1\n\t" \
  "ds_read_b128 v[224:227], v242 offset:256\n\t" \
  "ds_read_b128 v[228:231], v242 offset:16640\n\t" \
  "ds_read_b128 v[232:235], v243 offset:256\n\t" \
  "ds_read_b128 v[236:239], v243 offset:16640\n\t" \
  "s_add_u32 m0, %[lw], 101376\n\t" \
  "s_nop 0\n\t" \
  "global_load_lds_dwordx4 v251, s[92:93] sc1\n\t" \
  "ds_write_b128 v252, v[152:155] offset:0\n\t" \
  "ds_write_b128 v252, v[156:159] offset:1024\n\t" \
  "s_waitcnt lgkmcnt(0)\n\t" \
  "s_waitcnt vmcnt(4)\n\t" \
  "s_barrier\n\t" \
  "s_setprio 2\n\t" \
  "v_mfma_f32_16x16x32_bf16 v[128:131], v[176:179], v[0:3], 0\n\t" \
  "v_mfma_f32_16x16x32_bf16 v[136:139], v[176:179], v[4:7], 0\n\t" \
  "v_mfma_f32_16x16x32_bf16 v[132:135], v[180:183], v[0:3], 0\n\t" \
  "v_mfma_f32_16x16x32_bf16 v[140:143], v[180:183], v[4:7], 0\n\t" \
  "v_mfma_f32_16x16x32_bf16 v[128:131], v[184:187], v[8:11], v[128:131]\n\t" \
  "v_mfma_f32_16x16x32_bf16 v[136:139], v[184:187], v[12:15], v[136:139]\n\t" \
  "v_mfma_f32_16x16x32_bf16 v[132:135], v[188:191], v[8:11], v[132:135]\n\t" \
  "v_mfma_f32_16x16x32_bf16 v[140:143], v[188:191], v[12:15], v[140:143]\n\t" \
  "v_mfma_f32_16x16x32_bf16 v[128:131], v[192:195], v[16:19], v[128:131]\n\t" \
  "v_mfma_f32_16x16x32_bf16 v[136:139], v[192:195], v[20:23], v[136:139]\n\t" \
  "v_mfma_f32_16x16x32_bf16 v[132:135], v[196:199], v[16:19], v[132:135]\n\t" \
  "v_mfma_f32_16x16x32_bf16 v[140:143], v[196:199], v[20:23], v[140:143]\n\t" \
  "v_mfma_f32_16x16x32_bf16 v[128:131], v[200:203], v[24:27], v[128:131]\n\t" \
  "v_mfma_f32_16x16x32_bf16 v[136:139], v[200:203], v[28:31], v[136:139]\n\t" \
  "v_mfma_f32_16x16x32_bf16 v[132:135], v[204:207], v[24:27], v[132:135]\n\t" \
  "v_mfma_f32_16x16x32_bf16 v[140:143], v[204:207], v[28:31], v[140:143]\n\t" \
  "v_mfma_f32_16x16x32_bf16 v[128:131], v[208:211], v[32:35], v[128:131]\n\t" \
  "v_mfma_f32_16x16x32_bf16 v[136:139], v[208:211], v[36:39], v[136:139]\n\t" \
  "v_mfma_f32_16x16x32_bf16 v[132:135], v[212:215], v[32:35], v[132:135]\n\t" \
  "v_mfma_f32_16x16x32_bf16 v[140:143], v[212:215], v[36:39], v[140:143]\n\t" \
  "v_mfma_f32_16x16x32_bf16 v[128:131], v[216:219], v[40:43], v[128:131]\n\t" \
  "v_mfma_f32_16x16x32_bf16 v[136:139], v[216:219], v[44:47], v[136:139]\n\t" \
  "v_mfma_f32_16x16x32_bf16 v[132:135], v[220:223], v[40:43], v[132:135]\n\t" \
  "v_mfma_f32_16x16x32_bf16 v[140:143], v[220:223], v[44:47], v[140:143]\n\t" \
  "v_mfma_f32_16x16x32_bf16 v[128:131], v[224:227], v[48:51], v[128:131]\n\t" \
  "v_mfma_f32_16x16x32_bf16 v[136:139], v[224:227], v[52:55], v[136:139]\n\t" \
  "v_mfma_f32_16x16x32_bf16 v[132:135], v[228:231], v[48:51], v[132:135]\n\t" \
  "v_mfma_f32_16x16x32_bf16 v[140:143], v[228:231], v[52:55], v[140:143]\n\t" \
  "v_mfma_f32_16x16x32_bf16 v[128:131], v[232:235], v[56:59], v[128:131]\n\t" \
  "v_mfma_f32_16x16x32_bf16 v[136:139], v[232:235], v[60:63], v[136:139]\n\t" \
  "v_mfma_f32_16x16x32_bf16 v[132:135], v[236:239], v[56:59], v[132:135]\n\t" \
  "v_mfma_f32_16x16x32_bf16 v[140:143], v[236:239], v[60:63], v[140:143]\n\t" \
  "s_setprio 0\n\t" \
  "s_add_u32 s92, s92, 65024\n\t" \
  "s_addc_u32 s93, s93, 0\n\t" \
  "ds_read_b128 v[160:163], v253 offset:0\n\t" \
  "ds_read_b128 v[164:167], v253 offset:1024\n\t" \
  "ds_read_b128 v[176:179], v240 offset:32768\n\t" \
  "ds_read_b128 v[180:183], v240 offset:49152\n\t" \
  "ds_read_b128 v[184:187], v241 offset:32768\n\t" \
  "ds_read_b128 v[188:191], v241 offset:49152\n\t" \
  "s_add_u32 m0, %[lw], 0\n\t" \
  "s_nop 0\n\t" \
  "global_load_lds_dwordx4 v248, s[92:93] sc1\n\t" \
  "ds_read_b128 v[192:195], v242 offset:32768\n\t" \
  "ds_read_b128 v[196:199], v242 offset:49152\n\t" \
  "ds_read_b128 v[200:203], v243 offset:32768\n\t" \
  "ds_read_b128 v[204:207], v243 offset:49152\n\t" \
  "s_add_u32 m0, %[lw], 1024\n\t" \
  "s_nop 0\n\t" \
  "global_load_lds_dwordx4 v249, s[92:93] sc1\n\t" \
  "ds_read_b128 v[208:211], v240 offset:33024\n\t" \
  "ds_read_b128 v[212:215], v240 offset:49408\n\t" \
  "ds_read_b128 v[216:219], v241 offset:33024\n\t" \
  "ds_read_b128 v[220:223], v241 offset:49408\n\t" \
  "s_add_u32 m0, %[lw], 2048\n\t" \
  "s_nop 0\n\t" \
  "global_load_lds_dwordx4 v250, s[92:93] sc1\n\t" \
  "ds_read_b128 v[224:227], v242 offset:33024\n\t" \
  "ds_read_b128 v[228:231], v242 offset:49408\n\t" \
  "ds_read_b128 v[232:235], v243 offset:33024\n\t" \
  "ds_read_b128 v[236:239], v243 offset:49408\n\t" \
  "s_add_u32 m0, %[lw], 3072\n\t" \
  "s_nop 0\n\t" \
  "global_load_lds_dwordx4 v251, s[92:93] sc1\n\t" \
  "s_waitcnt lgkmcnt(15)\n\t" \
  "s_waitcnt lgkmcnt(12)\n\t" \
  "s_sub_i32 s91, s86, 2\n\t" \
  "v_mov_b32 v173, s91\n\t" \
  "buffer_store_dword v173, v174, %[rs], 0 offen sc1\n\t" \
  "s_lshr_b32 s90, s87, 7\n\t" \
  "s_xor_b32 s90, s90, %[s0]\n\t" \
  "s_and_b32 s90, s90, 1\n\t" \
  "s_lshl1_add_u32 s90, s87, s90\n\t" \
  "s_lshl_b32 s90, s90, 13\n\t" \
  "v_add_f32 v160, v160, v144\n\t" \
  "v_add_f32 v161, v161, v145\n\t" \
  "v_add_f32 v162, v162, v146\n\t" \
  "v_add_f32 v163, v163, v147\n\t" \
  "v_add_f32 v164, v164, v148\n\t" \
  "v_add_f32 v165, v165, v149\n\t" \
  "v_add_f32 v166, v166, v150\n\t" \
  "v_add_f32 v167, v167, v151\n\t" \
  "v_cvt_pk_bf16_f32 v168, v160, v161\n\t" \
  "v_cvt_pk_bf16_f32 v169, v162, v163\n\t" \
  "v_cvt_pk_bf16_f32 v170, v164, v165\n\t" \
  "v_cvt_pk_bf16_f32 v171, v166, v167\n\t" \
  "buffer_store_dwordx4 v[168:171], v172, %[rt], s90 offen sc1\n\t" \
  "s_add_u32 s86, s86, 1\n\t" \
  "s_add_u32 s87, s87, 1\n\t" \
  "s_waitcnt lgkmcnt(0)\n\t" \
  "s_waitcnt vmcnt(10)\n\t" \
  "s_barrier\n\t" \
  "s_setprio 2\n\t" \
  "v_mfma_f32_16x16x32_bf16 v[128:131], v[176:179], v[64:67], v[128:131]\n\t" \
  "v_mfma_f32_16x16x32_bf16 v[136:139], v[176:179], v[68:71], v[136:139]\n\t" \
  "v_mfma_f32_16x16x32_bf16 v[132:135], v[180:183], v[64:67], v[132:135]\n\t" \
  "v_mfma_f32_16x16x32_bf16 v[140:143], v[180:183], v[68:71], v[140:143]\n\t" \
  "v_mfma_f32_16x16x32_bf16 v[128:131], v[184:187], v[72:75], v[128:131]\n\t" \
  "v_mfma_f32_16x16x32_bf16 v[136:139], v[184:187], v[76:79], v[136:139]\n\t" \
  "v_mfma_f32_16x16x32_bf16 v[132:135], v[188:191], v[72:75], v[132:135]\n\t" \
  "v_mfma_f32_16x16x32_bf16 v[140:143], v[188:191], v[76:79], v[140:143]\n\t" \
  "v_mfma_f32_16x16x32_bf16 v[128:131], v[192:195], v[80:83], v[128:131]\n\t" \
  "v_mfma_f32_16x16x32_bf16 v[136:139], v[192:195], v[84:87], v[136:139]\n\t" \
  "v_mfma_f32_16x16x32_bf16 v[132:135], v[196:199], v[80:83], v[132:135]\n\t" \
  "v_mfma_f32_16x16x32_bf16 v[140:143], v[196:199], v[84:87], v[140:143]\n\t" \
  "v_mfma_f32_16x16x32_bf16 v[128:131], v[200:203], v[88:91], v[128:131]\n\t" \
  "v_mfma_f32_16x16x32_bf16 v[136:139], v[200:203], v[92:95], v[136:139]\n\t" \
  "v_mfma_f32_16x16x32_bf16 v[132:135], v[204:207], v[88:91], v[132:135]\n\t" \
  "v_mfma_f32_16x16x32_bf16 v[140:143], v[204:207], v[92:95], v[140:143]\n\t" \
  "v_mfma_f32_16x16x32_bf16 v[128:131], v[208:211], v[96:99], v[128:131]\n\t" \
  "v_mfma_f32_16x16x32_bf16 v[136:139], v[208:211], v[100:103], v[136:139]\n\t" \
  "v_mfma_f32_16x16x32_bf16 v[132:135], v[212:215], v[96:99], v[132:135]\n\t" \
  "v_mfma_f32_16x16x32_bf16 v[140:143], v[212:215], v[100:103], v[140:143]\n\t" \
  "v_mfma_f32_16x16x32_bf16 v[128:131], v[216:219], v[104:107], v[128:131]\n\t" \
  "v_mfma_f32_16x16x32_bf16 v[136:139], v[216:219], v[108:111], v[136:139]\n\t" \
  "v_mfma_f32_16x16x32_bf16 v[132:135], v[220:223], v[104:107], v[132:135]\n\t" \
  "v_mfma_f32_16x16x32_bf16 v[140:143], v[220:223], v[108:111], v[140:143]\n\t" \
  "v_mfma_f32_16x16x32_bf16 v[128:131], v[224:227], v[112:115], v[128:131]\n\t" \
  "v_mfma_f32_16x16x32_bf16 v[136:139], v[224:227], v[116:119], v[136:139]\n\t" \
  "v_mfma_f32_16x16x32_bf16 v[132:135], v[228:231], v[112:115], v[132:135]\n\t" \
  "v_mfma_f32_16x16x32_bf16 v[140:143], v[228:231], v[116:119], v[140:143]\n\t" \
  "v_mfma_f32_16x16x32_bf16 v[128:131], v[232:235], v[120:123], v[128:131]\n\t" \
  "v_mfma_f32_16x16x32_bf16 v[136:139], v[232:235], v[124:127], v[136:139]\n\t" \
  "v_mfma_f32_16x16x32_bf16 v[132:135], v[236:239], v[120:123], v[132:135]\n\t" \
  "v_mfma_f32_16x16x32_bf16 v[140:143], v[236:239], v[124:127], v[140:143]\n\t" \
  "s_setprio 0\n\t" \
  "s_add_u32 s92, s92, 512\n\t" \
  "s_addc_u32 s93, s93, 0\n\t" \
  "ds_read_b128 v[176:179], v244 offset:0\n\t" \
  "ds_read_b128 v[180:183], v244 offset:16384\n\t" \
  "ds_read_b128 v[184:187], v245 offset:0\n\t" \
  "ds_read_b128 v[188:191], v245 offset:16384\n\t" \
  "s_add_u32 m0, %[lw], 32768\n\t" \
  "s_nop 0\n\t" \
  "global_load_lds_dwordx4 v248, s[92:93] sc1\n\t" \
  "ds_read_b128 v[192:195], v246 offset:0\n\t" \
  "ds_read_b128 v[196:199], v246 offset:16384\n\t" \
  "ds_read_b128 v[200:203], v247 offset:0\n\t" \
  "ds_read_b128 v[204:207], v247 offset:16384\n\t" \
  "s_add_u32 m0, %[lw], 33792\n\t" \
  "s_nop 0\n\t" \
  "global_load_lds_dwordx4 v249, s[92:93] sc1\n\t" \
  "ds_read_b128 v[208:211], v244 offset:256\n\t" \
  "ds_read_b128 v[212:215], v244 offset:16640\n\t" \
  "ds_read_b128 v[216:219], v245 offset:256\n\t" \
  "ds_read_b128 v[220:223], v245 offset:16640\n\t" \
  "s_add_u32 m0, %[lw], 34816\n\t" \
  "s_nop 0\n\t" \
  "global_load_lds_dwordx4 v250, s[92:93] sc1\n\t" \
  "ds_read_b128 v[224:227], v246 offset:256\n\t" \
  "ds_read_b128 v[228:231], v246 offset:16640\n\t" \
  "ds_read_b128 v[232:235], v247 offset:256\n\t" \
  "ds_read_b128 v[236:239], v247 offset:16640\n\t" \
  "s_add_u32 m0, %[lw], 35840\n\t" \
  "s_nop 0\n\t" \
  "global_load_lds_dwordx4 v251, s[92:93] sc1\n\t" \
  "ds_write_b128 v252, v[136:139] offset:0\n\t" \
  "ds_write_b128 v252, v[140:143] offset:1024\n\t" \
  "s_waitcnt lgkmcnt(0)\n\t" \
  "s_waitcnt vmcnt(4)\n\t" \
  "s_barrier\n\t" \
  "s_setprio 2\n\t" \
  "v_mfma_f32_16x16x32_bf16 v[144:147], v[176:179], v[0:3], 0\n\t" \
  "v_mfma_f32_16x16x32_bf16 v[152:155], v[176:179], v[4:7], 0\n\t" \
  "v_mfma_f32_16x16x32_bf16 v[148:151], v[180:183], v[0:3], 0\n\t" \
  "v_mfma_f32_16x16x32_bf16 v[156:159], v[180:183], v[4:7], 0\n\t" \
  "v_mfma_f32_16x16x32_bf16 v[144:147], v[184:187], v[8:11], v[144:147]\n\t" \
  "v_mfma_f32_16x16x32_bf16 v[152:155], v[184:187], v[12:15], v[152:155]\n\t" \
  "v_mfma_f32_16x16x32_bf16 v[148:151], v[188:191], v[8:11], v[148:151]\n\t" \
  "v_mfma_f32_16x16x32_bf16 v[156:159], v[188:191], v[12:15], v[156:159]\n\t" \
  "v_mfma_f32_16x16x32_bf16 v[144:147], v[192:195], v[16:19], v[144:147]\n\t" \
  "v_mfma_f32_16x16x32_bf16 v[152:155], v[192:195], v[20:23], v[152:155]\n\t" \
  "v_mfma_f32_16x16x32_bf16 v[148:151], v[196:199], v[16:19], v[148:151]\n\t" \
  "v_mfma_f32_16x16x32_bf16 v[156:159], v[196:199], v[20:23], v[156:159]\n\t" \
  "v_mfma_f32_16x16x32_bf16 v[144:147], v[200:203], v[24:27], v[144:147]\n\t" \
  "v_mfma_f32_16x16x32_bf16 v[152:155], v[200:203], v[28:31], v[152:155]\n\t" \
  "v_mfma_f32_16x16x32_bf16 v[148:151], v[204:207], v[24:27], v[148:151]\n\t" \
  "v_mfma_f32_16x16x32_bf16 v[156:159], v[204:207], v[28:31], v[156:159]\n\t" \
  "v_mfma_f32_16x16x32_bf16 v[144:147], v[208:211], v[32:35], v[144:147]\n\t" \
  "v_mfma_f32_16x16x32_bf16 v[152:155], v[208:211], v[36:39], v[152:155]\n\t" \
  "v_mfma_f32_16x16x32_bf16 v[148:151], v[212:215], v[32:35], v[148:151]\n\t" \
  "v_mfma_f32_16x16x32_bf16 v[156:159], v[212:215], v[36:39], v[156:159]\n\t" \
  "v_mfma_f32_16x16x32_bf16 v[144:147], v[216:219], v[40:43], v[144:147]\n\t" \
  "v_mfma_f32_16x16x32_bf16 v[152:155], v[216:219], v[44:47], v[152:155]\n\t" \
  "v_mfma_f32_16x16x32_bf16 v[148:151], v[220:223], v[40:43], v[148:151]\n\t" \
  "v_mfma_f32_16x16x32_bf16 v[156:159], v[220:223], v[44:47], v[156:159]\n\t" \
  "v_mfma_f32_16x16x32_bf16 v[144:147], v[224:227], v[48:51], v[144:147]\n\t" \
  "v_mfma_f32_16x16x32_bf16 v[152:155], v[224:227], v[52:55], v[152:155]\n\t" \
  "v_mfma_f32_16x16x32_bf16 v[148:151], v[228:231], v[48:51], v[148:151]\n\t" \
  "v_mfma_f32_16x16x32_bf16 v[156:159], v[228:231], v[52:55], v[156:159]\n\t" \
  "v_mfma_f32_16x16x32_bf16 v[144:147], v[232:235], v[56:59], v[144:147]\n\t" \
  "v_mfma_f32_16x16x32_bf16 v[152:155], v[232:235], v[60:63], v[152:155]\n\t" \
  "v_mfma_f32_16x16x32_bf16 v[148:151], v[236:239], v[56:59], v[148:151]\n\t" \
  "v_mfma_f32_16x16x32_bf16 v[156:159], v[236:239], v[60:63], v[156:159]\n\t" \
  "s_setprio 0\n\t" \
  "s_add_u32 s92, s92, 65024\n\t" \
  "s_addc_u32 s93, s93, 0\n\t" \
  "ds_read_b128 v[160:163], v253 offset:0\n\t" \
  "ds_read_b128 v[164:167], v253 offset:1024\n\t" \
  "ds_read_b128 v[176:179], v244 offset:32768\n\t" \
  "ds_read_b128 v[180:183], v244 offset:49152\n\t" \
  "ds_read_b128 v[184:187], v245 offset:32768\n\t" \
  "ds_read_b128 v[188:191], v245 offset:49152\n\t" \
  "s_add_u32 m0, %[lw], 65536\n\t" \
  "s_nop 0\n\t" \
  "global_load_lds_dwordx4 v248, s[92:93] sc1\n\t" \
  "ds_read_b128 v[192:195], v246 offset:32768\n\t" \
  "ds_read_b128 v[196:199], v246 offset:49152\n\t" \
  "ds_read_b128 v[200:203], v247 offset:32768\n\t" \
  "ds_read_b128 v[204:207], v247 offset:49152\n\t" \
  "s_add_u32 m0, %[lw], 66560\n\t" \
  "s_nop 0\n\t" \
  "global_load_lds_dwordx4 v249, s[92:93] sc1\n\t" \
  "ds_read_b128 v[208:211], v244 offset:33024\n\t" \
  "ds_read_b128 v[212:215], v244 offset:49408\n\t" \
  "ds_read_b128 v[216:219], v245 offset:33024\n\t" \
  "ds_read_b128 v[220:223], v245 offset:49408\n\t" \
  "s_add_u32 m0, %[lw], 67584\n\t" \
  "s_nop 0\n\t" \
  "global_load_lds_dwordx4 v250, s[92:93] sc1\n\t" \
  "ds_read_b128 v[224:227], v246 offset:33024\n\t" \
  "ds_read_b128 v[228:231], v246 offset:49408\n\t" \
  "ds_read_b128 v[232:235], v247 offset:33024\n\t" \
  "ds_read_b128 v[236:239], v247 offset:49408\n\t" \
  "s_add_u32 m0, %[lw], 68608\n\t" \
  "s_nop 0\n\t" \
  "global_load_lds_dwordx4 v251, s[92:93] sc1\n\t" \
  "s_waitcnt lgkmcnt(15)\n\t" \
  "s_waitcnt lgkmcnt(12)\n\t" \
  "s_sub_i32 s91, s86, 2\n\t" \
  "v_mov_b32 v173, s91\n\t" \
  "buffer_store_dword v173, v174, %[rs], 0 offen sc1\n\t" \
  "s_lshr_b32 s90, s87, 7\n\t" \
  "s_xor_b32 s90, s90, %[s0]\n\t" \
  "s_and_b32 s90, s90, 1\n\t" \
  "s_lshl1_add_u32 s90, s87, s90\n\t" \
  "s_lshl_b32 s90, s90, 13\n\t" \
  "v_add_f32 v160, v160, v128\n\t" \
  "v_add_f32 v161, v161, v129\n\t" \
  "v_add_f32 v162, v162, v130\n\t" \
  "v_add_f32 v163, v163, v131\n\t" \
  "v_add_f32 v164, v164, v132\n\t" \
  "v_add_f32 v165, v165, v133\n\t" \
  "v_add_f32 v166, v166, v134\n\t" \
  "v_add_f32 v167, v167, v135\n\t" \
  "v_cvt_pk_bf16_f32 v168, v160, v161\n\t" \
  "v_cvt_pk_bf16_f32 v169, v162, v163\n\t" \
  "v_cvt_pk_bf16_f32 v170, v164, v165\n\t" \
  "v_cvt_pk_bf16_f32 v171, v166, v167\n\t" \
  "buffer_store_dwordx4 v[168:171], v172, %[rt], s90 offen sc1\n\t" \
  "s_add_u32 s86, s86, 1\n\t" \
  "s_add_u32 s87, s87, 1\n\t" \
  "s_waitcnt lgkmcnt(0)\n\t" \
  "s_cmp_lt_u32 s86, 64\n\t" \
  "s_cbranch_scc1 .Lp1_%=\n\t" \
  "s_barrier\n\t" \
  "s_setprio 2\n\t" \
  "v_mfma_f32_16x16x32_bf16 v[144:147], v[176:179], v[64:67], v[144:147]\n\t" \
  "v_mfma_f32_16x16x32_bf16 v[152:155], v[176:179], v[68:71], v[152:155]\n\t" \
  "v_mfma_f32_16x16x32_bf16 v[148:151], v[180:183], v[64:67], v[148:151]\n\t" \
  "v_mfma_f32_16x16x32_bf16 v[156:159], v[180:183], v[68:71], v[156:159]\n\t" \
  "v_mfma_f32_16x16x32_bf16 v[144:147], v[184:187], v[72:75], v[144:147]\n\t" \
  "v_mfma_f32_16x16x32_bf16 v[152:155], v[184:187], v[76:79], v[152:155]\n\t" \
  "v_mfma_f32_16x16x32_bf16 v[148:151], v[188:191], v[72:75], v[148:151]\n\t" \
  "v_mfma_f32_16x16x32_bf16 v[156:159], v[188:191], v[76:79], v[156:159]\n\t" \
  "v_mfma_f32_16x16x32_bf16 v[144:147], v[192:195], v[80:83], v[144:147]\n\t" \
  "v_mfma_f32_16x16x32_bf16 v[152:155], v[192:195], v[84:87], v[152:155]\n\t" \
  "v_mfma_f32_16x16x32_bf16 v[148:151], v[196:199], v[80:83], v[148:151]\n\t" \
  "v_mfma_f32_16x16x32_bf16 v[156:159], v[196:199], v[84:87], v[156:159]\n\t" \
  "v_mfma_f32_16x16x32_bf16 v[144:147], v[200:203], v[88:91], v[144:147]\n\t" \
  "v_mfma_f32_16x16x32_bf16 v[152:155], v[200:203], v[92:95], v[152:155]\n\t" \
  "v_mfma_f32_16x16x32_bf16 v[148:151], v[204:207], v[88:91], v[148:151]\n\t" \
  "v_mfma_f32_16x16x32_bf16 v[156:159], v[204:207], v[92:95], v[156:159]\n\t" \
  "v_mfma_f32_16x16x32_bf16 v[144:147], v[208:211], v[96:99], v[144:147]\n\t" \
  "v_mfma_f32_16x16x32_bf16 v[152:155], v[208:211], v[100:103], v[152:155]\n\t" \
  "v_mfma_f32_16x16x32_bf16 v[148:151], v[212:215], v[96:99], v[148:151]\n\t" \
  "v_mfma_f32_16x16x32_bf16 v[156:159], v[212:215], v[100:103], v[156:159]\n\t" \
  "v_mfma_f32_16x16x32_bf16 v[144:147], v[216:219], v[104:107], v[144:147]\n\t" \
  "v_mfma_f32_16x16x32_bf16 v[152:155], v[216:219], v[108:111], v[152:155]\n\t" \
  "v_mfma_f32_16x16x32_bf16 v[148:151], v[220:223], v[104:107], v[148:151]\n\t" \
  "v_mfma_f32_16x16x32_bf16 v[156:159], v[220:223], v[108:111], v[156:159]\n\t" \
  "v_mfma_f32_16x16x32_bf16 v[144:147], v[224:227], v[112:115], v[144:147]\n\t" \
  "v_mfma_f32_16x16x32_bf16 v[152:155], v[224:227], v[116:119], v[152:155]\n\t" \
  "v_mfma_f32_16x16x32_bf16 v[148:151], v[228:231], v[112:115], v[148:151]\n\t" \
  "v_mfma_f32_16x16x32_bf16 v[156:159], v[228:231], v[116:119], v[156:159]\n\t" \
  "v_mfma_f32_16x16x32_bf16 v[144:147], v[232:235], v[120:123], v[144:147]\n\t" \
  "v_mfma_f32_16x16x32_bf16 v[152:155], v[232:235], v[124:127], v[152:155]\n\t" \
  "v_mfma_f32_16x16x32_bf16 v[148:151], v[236:239], v[120:123], v[148:151]\n\t" \
  "v_mfma_f32_16x16x32_bf16 v[156:159], v[236:239], v[124:127], v[156:159]\n\t" \
  "s_setprio 0\n\t" \
  "s_nop 4\n\t" \
  "ds_write_b128 v252, v[152:155] offset:0\n\t" \
  "s_nop 0\n\t" \
  "ds_write_b128 v252, v[156:159] offset:1024\n\t" \
  "s_waitcnt lgkmcnt(0)\n\t" \
  "s_barrier\n\t" \
  "ds_read_b128 v[160:163], v253 offset:0\n\t" \
  "ds_read_b128 v[164:167], v253 offset:1024\n\t" \
  "s_waitcnt lgkmcnt(0)\n\t" \
  "s_lshr_b32 s90, s87, 7\n\t" \
  "s_xor_b32 s90, s90, %[s0]\n\t" \
  "s_and_b32 s90, s90, 1\n\t" \
  "s_lshl1_add_u32 s90, s87, s90\n\t" \
  "s_lshl_b32 s90, s90, 13\n\t" \
  "v_add_f32 v160, v160, v144\n\t" \
  "v_add_f32 v161, v161, v145\n\t" \
  "v_add_f32 v162, v162, v146\n\t" \
  "v_add_f32 v163, v163, v147\n\t" \
  "v_add_f32 v164, v164, v148\n\t" \
  "v_add_f32 v165, v165, v149\n\t" \
  "v_add_f32 v166, v166, v150\n\t" \
  "v_add_f32 v167, v167, v151\n\t" \
  "v_cvt_pk_bf16_f32 v168, v160, v161\n\t" \
  "v_cvt_pk_bf16_f32 v169, v162, v163\n\t" \
  "v_cvt_pk_bf16_f32 v170, v164, v165\n\t" \
  "v_cvt_pk_bf16_f32 v171, v166, v167\n\t" \
  "buffer_store_dwordx4 v[168:171], v172, %[rt], s90 offen sc1\n\t" \
  "s_waitcnt vmcnt(0)\n\t" \
  "s_barrier\n\t" \
  "s_sub_i32 s91, s86, 1\n\t" \
  "v_mov_b32 v173, s91\n\t" \
  "buffer_store_dword v173, v174, %[rs], 0 offen sc1\n\t" \
  "s_mov_b32 m0, s94\n\t" \
  ".Le_%=:\n\t"
constexpr int W_X = 131072, W_PM = W_X + 16384;
template <bool ISV>
__device__ __forceinline__ void job_proj(Args& p, int g, int which, int h, char* smem, __amdgpu_buffer_rsrc_t rsy) {
  const int tid = threadIdx.x, lane = tid & 63, r = lane & 15, q = lane >> 4;
  const int w = __builtin_amdgcn_readfirstlane(tid >> 6), kh = w >> 2, cg = w & 3;
  u32x32 W[4];
  const unsigned lw = lds_off(smem) + w * 4096;
  u32x4 vx;
  u32x2 tag = {0u, 0u};
  const char* xn = p.ws + WS_XN;
  auto row = [&](int t, int i) {
    const char* src = xn + (size_t)(min(t, 127) >> 1) * (SEG * XLD * 2) + (t & 1) * 512;
    unsigned keep;
    asm volatile("s_mov_b32 %0, m0\n\ts_mov_b32 m0, %3\n\ts_nop 0\n\tglobal_load_lds_dwordx4 %1, %2 sc1\n\ts_mov_b32 m0, %0"
                 : "=&s"(keep) : "v"(vx[i]), "s"(src), "s"(lw + (t & 3) * 32768 + i * 1024) : "memory");
  };
  {
    const u16* Wm = which == 0 ? p.Wk : p.Wq;
    asm volatile("" : "+s"(Wm));
    if (ISV) Wm = p.Wv;
    const unsigned vo = (tid >> 4) * (HD * 2) + 16 * ((lane & 15) ^ swzB((tid >> 4) & 15));
    auto stage = [&](int j) {
      const char* src = (const char*)(Wm + (size_t)(WREV ? 7 - j : j) * 64 * HD + h * DH);
      const unsigned dst = lds_off(smem) + (j & 3) * 32768 + w * 1024;
      glds2(src, 32 * HD * 2, vo, dst);
      glds2(src + 512 * HD * 2, 32 * HD * 2, vo, dst + 16384);
    };
    stage(0); stage(1); stage(2); stage(3);
#pragma unroll
    for (int i = 0; i < 4; ++i) vx[i] = ((4 * w + i) ^ (ISV ? 0 : (lane >> 1) & 16)) * (XLD * 2) + (lane >> 5) * 1024 + (((lane & 31) ^ ((4 * w + i) & 15)) << 4);
    Boot bt;
#pragma unroll
    for (int j = 0; j < 8; ++j) {
      if (j < 5) WAIT_V(12); else if (j == 5) WAIT_V(8); else if (j == 6) WAIT_V(4); else WAIT_V(10);
      if (j == 6 && w == 0) { lds_at(smem + W_PM)[0] = boot_done(p.ws, bt); tag = next_tag(bt.e); }
      LBAR();
      const char* sb = smem + (j & 3) * 32768 + kh * 16384;
#pragma unroll
      for (int k = 0; k < 4; ++k) {
        const int ks = k >> 1, a = k & 1;
        const unsigned ch = ISV ? 2 * (2 * cg + (a ^ kh)) + ((r & 3) >> 1) : 4 * cg + (r & 3), by = ISV ? 8 * (r & 1) : 8 * a;
        s16x4 lo = __builtin_amdgcn_ds_read_tr16_b64_v4i16((lds_s16x4_ptr)(sb + offB(32 * ks + 8 * q + (r >> 2), ch) + by));
        s16x4 hi = __builtin_amdgcn_ds_read_tr16_b64_v4i16((lds_s16x4_ptr)(sb + offB(32 * ks + 8 * q + 4 + (r >> 2), ch) + by));
        const int f = 4 * (WREV ? 7 - j : j) + k;
        put4(W[f >> 3], f & 7, (bf16x8){lo[0], lo[1], lo[2], lo[3], hi[0], hi[1], hi[2], hi[3]});
      }
      LBAR();
      if (j == 3 && w == 0) boot_ask(p.ws, bt);
      if (j + 4 < 8) stage(j + 4);
      if (j == 6) {
#pragma unroll
        for (int i = 0; i < 10; ++i) row(i >> 2, i & 3);
      }
    }
    p.s0 = uni(lds_at(smem + W_PM)[0] >> 31);
  }
  typedef __attribute__((ext_vector_type(8))) unsigned u32x8;
  u32x8 la;
#pragma unroll
  for (int i = 0; i < 8; ++i) la[i] = (i >> 2) * 65536 + r * 1024 + kh * 512 + (((4 * (i & 3) + q) ^ r) << 4);
  const unsigned xw = W_X + w * 2048 + lane * 16;
  u32x32 acc;
  asm volatile("" : "={v[128:159]}"(acc));
  WAIT_V(6);
  LBAR();
  {
    const int g0 = 0;
#define PR_LOOP(TXT)                                                                                                              \
    asm volatile(TXT                                                                                                              \
        : "+{v[128:159]}"(acc)                                                                                                    \
        : W4_IN, "{v[208:215]}"(la), "{v[216:219]}"(vx), "{v220}"(xw), "{v221}"(xw ^ 8192),                                       \
          "{v172}"((ISV ? 2 * cg + kh : 4 * kh + cg) * 1024 + lane * 16), "{v174}"(tid == 256 ? fkq(h, which) : 0x7F000000),      \
          [rt] "s"(raw_rsrc(p.ws + WS_T, T_BYTES)), [rs] "s"(raw_rsrc(p.ws + WS_SY, SY_TOTAL)), [s0] "s"(p.s0 ^ TF_KQV),          \
          [lw] "s"(lw), [kh] "s"(kh), [g0] "s"(g0), [end] "s"(g0 ? NSEG : PR_G1), [x0] "s"(xn + (g0 + 1) * (SEG * XLD * 2)),                       \
          [t0] "s"(which * T_Q + kseg(0, h) - 1)                                                                                  \
        : CLB8(160, 161, 162, 163, 164, 165, 166, 167), "v168", "v169", "v170", "v171", "v173", CLB8(176, 177, 178, 179, 180, 181, 182, 183), \
          CLB8(184, 185, 186, 187, 188, 189, 190, 191), CLB8(192, 193, 194, 195, 196, 197, 198, 199),                             \
          CLB8(200, 201, 202, 203, 204, 205, 206, 207), "s86", "s87", "s88", "s90", "s91", "s92", "s93", "s94", "scc", "memory")
    if (ISV) PR_LOOP(PR_ASM_1); else PR_LOOP(PR_ASM_0);
    if (w == 0 && g0 == 0) wait_rows(p.ws, tag);
  }
#define PP_LOOP(TXT)                                                                                                              \
  asm volatile(TXT                                                                                                                \
      : "+{v[128:159]}"(acc)                                                                                                      \
      : W4_IN, "{v[240:247]}"(la), "{v[248:251]}"(vx), "{v252}"(xw), "{v253}"(xw ^ 8192),                                         \
        "{v172}"((ISV ? 2 * cg + kh : 4 * kh + cg) * 1024 + lane * 16), "{v174}"(tid == 256 ? fkq(h, which) : 0x7F000000),        \
        [rt] "s"(raw_rsrc(p.ws + WS_T, T_BYTES)), [rs] "s"(raw_rsrc(p.ws + WS_SY, SY_TOTAL)), [s0] "s"(p.s0 ^ TF_KQV),            \
        [lw] "s"(lw), [kh] "s"(kh), [g0] "n"(PR_G1), [x0] "s"(xn + (PR_G1 + 1) * (SEG * XLD * 2)),                                \
        [t0] "s"(which * T_Q + kseg(0, h) - 1)                                                                                    \
      : CLB8(160, 161, 162, 163, 164, 165, 166, 167), "v168", "v169", "v170", "v171", "v173", CLB8(176, 177, 178, 179, 180, 181, 182, 183), \
        CLB8(184, 185, 186, 187, 188, 189, 190, 191), CLB32(192), CLB8(224, 225, 226, 227, 228, 229, 230, 231),                   \
        CLB8(232, 233, 234, 235, 236, 237, 238, 239), "s86", "s87", "s90", "s91", "s92", "s93", "s94", "scc", "memory")
  if (ISV) PP_LOOP(PP_ASM_1); else PP_LOOP(PP_ASM_0);
}

enum { J_NONE = 0, J_GU = 3, J_PR = 5, J_CB = 8, J_DN = 14 };
struct BigJob { int type, panel, idx; };
struct BigPtr { const char *a, *b0, *b1; };
constexpr int G_LDA = XLD * 2, G_LDB = FF * 2;

__device__ __forceinline__ BigPtr big_ptrs(const Args& p, const BigJob& j) {
  BigPtr r;
  r.a = p.ws + WS_XN + (size_t)j.panel * 128 * G_LDA;
  r.b0 = (const char*)(p.wg + j.idx * 128); r.b1 = (const char*)(p.wu + j.idx * 128);
  return r;
}
__device__ __forceinline__ void big_publish(const Args& p, const BigJob& j) {
  int tid = threadIdx.x;
  asm volatile("" : "+v"(tid));
  if (j.type == J_GU && tid == 0)
    __hip_atomic_fetch_add((gu32*)(p.ws + WS_SY + SY_CH) + j.panel * 16, 1u, __ATOMIC_RELAXED, __HIP_MEMORY_SCOPE_AGENT);
}
typedef f32x4 Acc[2][2][4][2];
__device__ __forceinline__ void big_epilogue(const Args& p, const BigJob& j, Acc& acc, char* smem) {
  int tid = threadIdx.x;
  asm volatile("" : "+v"(tid));
  const int lane = tid & 63;
  const int wid = __builtin_amdgcn_readfirstlane(tid >> 6);
  const int wr = wid >> 2, wc = wid & 3;
  {
    const __amdgpu_buffer_rsrc_t ro = mkrsrc(p.ws + WS_HF, S_LEN * 16384);
#pragma unroll
    for (int i = 0; i < 8; ++i) {
      f32x4 hh[2];
#pragma unroll
      for (int n = 0; n < 2; ++n)
#pragma unroll
        for (int jj = 0; jj < 4; ++jj) {
          const float g = acc[i >> 2][0][i & 3][n][jj], u = acc[i >> 2][1][i & 3][n][jj];
          hh[n][jj] = __builtin_amdgcn_rcpf(1.0f + __builtin_amdgcn_exp2f(g * -1.4426950408889634f)) * u;
        }
      st_sc1(ro, h_off(p, j.panel + (i >> 2)) + (j.idx * 4 + wc) * 16384 + (wr * 4 + (i & 3)) * 1024 + lane * 16,
             ((u32x4){cvtpk(hh[0][0], hh[0][1]), cvtpk(hh[0][2], hh[0][3]), cvtpk(hh[1][0], hh[1][1]), cvtpk(hh[1][2], hh[1][3])}));
    }
  }
}
__device__ __forceinline__ void gemm_role(Args& p, int g, char* smem) {
  const __amdgpu_buffer_rsrc_t rsy = mkrsrc(p.ws + WS_SY, SY_TOTAL);
  const int x = g & 3, k = g >> 2;
  volatile unsigned* slot = reinterpret_cast<volatile unsigned*>(smem + 163824);
  int rnd = k < 24 ? 2 : -1;
  auto claim = [&]() {
    ++rnd;
    const int n = k < 24 ? 32 + k : rnd * 8 + k - 24, pn = (n * 37) >> 8;
    BigJob b;
    b.type = rnd < 4 ? J_GU : (rnd == 4 ? J_DN : (rnd == 5 && k >= 24 && k - 24 < 14 - NFUSE ? J_CB : J_NONE));
    b.panel = rnd < 4 ? 14 - 2 * pn : (rnd == 4 ? 4 * ((2 - (k >> 3)) & 3) + x : NFUSE + k - 24);
    b.idx = rnd < 4 ? x + 4 * (n - 7 * pn) : k & 7;
    return b;
  };
  if (k < 24) {
    if (k >= 16) job_proj<true>(p, g, 2, x * 8 + (k & 7), smem, rsy);
    else job_proj<false>(p, g, k >> 3, x * 8 + (k & 7), smem, rsy);
  } else {
    u32x2 tag;
#pragma unroll 1
    for (int u = 0; u < 4; ++u) {
      tag = start_rows(p, g - 96 + u * 32);
      if (u == 0 || u == 3) {
        WAIT_V(0);
        LBAR();
        report_rows(p, (u ? 32 : 0) + g - 96, tag);
      }
    }
    for (int z = 0; z < PF_T0; ++z) __builtin_amdgcn_s_sleep(16);
#pragma unroll 1
    for (int i = 0; i < PF_N; ++i) {
      const int m = i % 3, o = ((i / 3) * 32 + g - 96) * 448 + (int)threadIdx.x;
      if (threadIdx.x < 448) touch_line((const char*)(m == 0 ? p.wg : (m == 1 ? p.wu : p.wd)) + (size_t)o * 128, smem + (threadIdx.x >> 6) * 256);
      WAIT_V(1);
    }
    WAIT_V(0);
    if (threadIdx.x < 64) {
      wait_rows(p.ws, tag);
      Boot bt;
      boot_ask(p.ws, bt);
      WAIT_V(0);
      *slot = boot_done(p.ws, bt, true, tag);
    }
    LBAR();
    p.s0 = *slot >> 31;
  }
  p.s0 = uni(p.s0);
  BigJob cur = claim();
  while (cur.type != J_NONE) {
    if (cur.type != J_GU) {
      if (cur.type == J_DN) job_down(p, cur.panel, cur.idx, smem, rsy);
      else job_comb(p, cur.panel, x, rsy);
      cur = claim();
      continue;
    }
    int tid = threadIdx.x;
    asm volatile("" : "+v"(tid));
    const int lane = tid & 63;
    const int wid = __builtin_amdgcn_readfirstlane(tid >> 6);
    const int wr = wid >> 2, wc = wid & 3;
    const unsigned ra = tid >> 3, ca = 16 * ((tid & 7) ^ ((tid >> 3) & 7)), rb = tid >> 4, cb = 16 * ((tid & 15) ^ swzB((tid >> 4) & 15));
    const unsigned lw = lds_off(smem) + wid * 1024;
#define B8_SA(b, h) (((b) * 2 + (h)) * 16384)
#define B8_SB(b, h) ((4 + (b) * 2 + (h)) * 16384)
#define B8_STA(b, h, P, kt) glds2<true>((P).a + (size_t)(h) * 128 * G_LDA + (kt) * 128, 64 * G_LDA, ra * G_LDA + ca, lw + B8_SA(b, h))
#define B8_STB(b, h, P, kt) glds2(((h) ? (P).b1 : (P).b0) + (size_t)(kt) * 64 * G_LDB, 32 * G_LDB, rb * G_LDB + cb, lw + B8_SB(b, h))
#define B8_LDA(b, h) do { _Pragma("unroll") for (int m = 0; m < 4; ++m) _Pragma("unroll") for (int kk = 0; kk < 2; ++kk) \
      At[m][kk] = ldsf(smem + B8_SA(b, h) + offA(wr * 64 + m * 16 + (lane & 15), kk * 4 + (lane >> 4))); } while (0)
#define B8_LDB(dst, b, h) do { _Pragma("unroll") for (int n = 0; n < 2; ++n) _Pragma("unroll") for (int kk = 0; kk < 2; ++kk) \
      dst[n][kk] = ld_bfrag(smem + B8_SB(b, h), kk, wc * 2 + n, lane); } while (0)
#define B8_MMA(ai, bj, Bt) do { _Pragma("unroll") for (int m = 0; m < 4; ++m) _Pragma("unroll") for (int n = 0; n < 2; ++n) \
      _Pragma("unroll") for (int kk = 0; kk < 2; ++kk) acc[ai][bj][m][n] = MFMA(Bt[n][kk], At[m][kk], acc[ai][bj][m][n]); } while (0)
#define WAIT_L(n) asm volatile("s_waitcnt lgkmcnt(%0)" :: "n"(n) : "memory")
#define BAR() __builtin_amdgcn_s_barrier()
#define BAR_1() do { SCHED(); asm volatile("s_setprio 1\n\ts_cmp_eq_u32 %0, 0\n\ts_cbranch_scc1 .Lb_%=\n\ts_setprio 2\n\ts_barrier\n.Lb_%=:" :: "s"(wr) : "memory", "scc"); SCHED(); } while (0)
#define BAR_0() do { SCHED(); asm volatile("s_setprio 0\n\ts_cmp_lg_u32 %0, 0\n\ts_cbranch_scc1 .Lb_%=\n\ts_barrier\n.Lb_%=:" :: "s"(wr) : "memory", "scc"); SCHED(); } while (0)
#define WAIT_RX(c) asm volatile("s_waitcnt vmcnt(16)\n\ts_cmp_lg_u32 %0, 0\n\ts_cbranch_scc1 .Lrx_%=\n\ts_waitcnt vmcnt(8)\n.Lrx_%=:" :: "s"(c) : "memory", "scc")
    Acc acc;
    bf16x8 At[4][2], B0[2][2], B1[2][2];
    BigPtr cp = big_ptrs(p, cur);
    BigJob pend; pend.type = J_NONE; pend.panel = 0; pend.idx = 0;
    LBAR();
    B8_STB(0, 0, cp, 0); B8_STB(0, 1, cp, 0); B8_STA(0, 0, cp, 0); B8_STA(0, 1, cp, 0);
    B8_STB(1, 0, cp, 1); B8_STA(1, 0, cp, 1); B8_STB(1, 1, cp, 1);
    WAIT_V(8); BAR();
    BigJob nx = pend;
    for (;;) {
      bool chain = false;
      BigPtr np = cp;
      constexpr int nt = DM / 64;
#pragma unroll
      for (int i = 0; i < 32; ++i) acc[i >> 4][(i >> 3) & 1][(i >> 1) & 3][i & 1] = (f32x4){0.f, 0.f, 0.f, 0.f};
#pragma unroll 1
      for (int t = 0; t < nt; t += 2) {
        const bool last = t == nt - 2;
        if (last) {
          nx = claim();
          chain = nx.type == J_GU;
          if (chain) np = big_ptrs(p, nx);
        }
        const BigPtr sp = np;
        const int t2 = last ? 0 : t + 2;
        if (t == 2) big_publish(p, pend);
        const int rx = __builtin_amdgcn_readfirstlane(t == 0 && pend.type != J_NONE);
        B8_LDB(B0, 0, 0); B8_LDB(B1, 0, 1); SCHED(); B8_LDA(0, 0); B8_STA(1, 1, cp, t + 1);
        WAIT_RX(rx);
        WAIT_L(0); BAR_1(); B8_MMA(0, 0, B0); B8_MMA(0, 1, B1); BAR_0();
        B8_LDA(0, 1); B8_STB(0, 0, sp, t2); B8_STB(0, 1, sp, t2); B8_STA(0, 0, sp, t2);
        WAIT_RX(rx);
        WAIT_L(0); BAR_1(); B8_MMA(1, 0, B0); B8_MMA(1, 1, B1); BAR_0();
        B8_LDB(B0, 1, 0); B8_LDB(B1, 1, 1); SCHED(); B8_LDA(1, 0); B8_STA(0, 1, sp, t2);
        WAIT_V(8); WAIT_L(0); BAR_1(); B8_MMA(0, 0, B0); B8_MMA(0, 1, B1); BAR_0();
        B8_LDA(1, 1); B8_STB(1, 0, sp, t2 + 1); B8_STB(1, 1, sp, t2 + 1); B8_STA(1, 0, sp, t2 + 1);
        WAIT_V(8); WAIT_L(0); BAR_1(); B8_MMA(1, 0, B0); B8_MMA(1, 1, B1); BAR_0();
      }
      big_epilogue(p, cur, acc, smem);
      if (!chain) break;
      pend = cur; cur = nx; cp = np;
    }
    WAIT_V(0);
    BAR();
    asm volatile(".Lcode_end:");
    big_publish(p, cur);
    cur = nx;
  }
}


__device__ __forceinline__ unsigned now_q() { return (unsigned)__builtin_amdgcn_s_memrealtime() * 4u; }
__device__ __forceinline__ void pace_until(unsigned t) {
#pragma unroll 1
  for (int i = 0; i < 256; ++i) {
    const int d = (int)(t - now_q());
    if (d <= 0) break;
    if (d > 32) __builtin_amdgcn_s_sleep(1);
  }
}
#define CLB_E "s76", "s77", "s78", "s79", "s80", "s81", "s82", "s83", "s84", "s85", "s86", "s87", "s88", "s89"
#define EDGE_ASM \
  ".p2align 5\n\t" \
  "s_memtime s[76:77]\n\t" \
  "s_memrealtime s[78:79]\n\t" \
  "s_nop 3\n\t" \
  "s_memrealtime s[80:81]\n\t" \
  "s_nop 3\n\t" \
  "s_memrealtime s[82:83]\n\t" \
  "s_nop 3\n\t" \
  "s_memrealtime s[84:85]\n\t" \
  "s_nop 3\n\t" \
  "s_memrealtime s[86:87]\n\t" \
  "s_nop 3\n\t" \
  "s_memrealtime s[88:89]\n\t" \
  "s_mov_b32 s93, 0\n\t" \
  "s_waitcnt lgkmcnt(0)\n\t" \
  "s_cmp_eq_u32 s80, s78\n\t" \
  "s_addc_u32 s93, s93, 0\n\t" \
  "s_cmp_eq_u32 s82, s78\n\t" \
  "s_addc_u32 s93, s93, 0\n\t" \
  "s_cmp_eq_u32 s84, s78\n\t" \
  "s_addc_u32 s93, s93, 0\n\t" \
  "s_cmp_eq_u32 s86, s78\n\t" \
  "s_addc_u32 s93, s93, 0\n\t" \
  "s_cmp_eq_u32 s88, s78\n\t" \
  "s_addc_u32 s93, s93, 0\n\t" \
  "s_mul_i32 s93, s93, 21\n\t" \
  "s_add_u32 s76, s76, 15\n\t" \
  "s_add_u32 %[kc], s76, s93\n\t" \
  "s_lshl2_add_u32 %[kq], s78, 16\n\t"
__device__ __forceinline__ unsigned time_of(unsigned cyc) {
  unsigned c, q;
  asm volatile(EDGE_ASM : [kc] "=&s"(c), [kq] "=&s"(q) :: CLB_E, "s93", "scc");
  return q - (((c - cyc) * 11900u) >> 16);
}
#define DS_RED 16
#define DS_MEM 11
#define LEAD_DN 2
#define LEAD_UP 6
__device__ __forceinline__ u32x2 ld2_sc1(__amdgpu_buffer_rsrc_t rs, int off, int soff = 0) {
  return __builtin_amdgcn_raw_buffer_load_b64(rs, off, soff, 16);
}
__device__ __forceinline__ void st2_sc1(__amdgpu_buffer_rsrc_t rs, int off, u32x2 v) {
  __builtin_amdgcn_raw_buffer_store_b64(v, rs, off, 0, 16);
}
__device__ __forceinline__ int row_max(int v) {
  v = max(v, __builtin_amdgcn_update_dpp(0, v, 0x128, 0xf, 0xf, false));
  v = max(v, __builtin_amdgcn_update_dpp(0, v, 0x124, 0xf, 0xf, false));
  v = max(v, __builtin_amdgcn_update_dpp(0, v, 0x122, 0xf, 0xf, false));
  v = max(v, __builtin_amdgcn_update_dpp(0, v, 0x121, 0xf, 0xf, false));
  return v;
}
__device__ __forceinline__ int wave_max(int v) {
  v = row_max(v);
  return max(max(__builtin_amdgcn_readlane(v, 0), __builtin_amdgcn_readlane(v, 16)),
             max(__builtin_amdgcn_readlane(v, 32), __builtin_amdgcn_readlane(v, 48)));
}
__device__ __forceinline__ int track(int est, int x, int up, int dn, int out) {
  const int d = x - est;
  return est + (d > out ? 4 : max(min(d, up), -dn));
}
#define WSL 12
#define PLAN_SLEEP 2
#define DS_N0 3
#define DS_X0 20
#define REF_W 3
#define REF_G 60
#define RL0 310
__device__ __forceinline__ void watch(__amdgpu_buffer_rsrc_t rsy, int s, int lane, unsigned t0, const unsigned* tgt_, gu32* abortw, int hs) {
  const int rl = (int)__builtin_amdgcn_raw_buffer_load_b32(rsy, lane * 128, SY_BR + hs, 16);
  u32x2 ra;
  for (unsigned spins = 0;;) {
    asm volatile("" ::: "memory");
    ra = ld2_sc1(rsy, (lane & 31) * 128, SY_BA + hs);
    const bool all = __all((int)ra[0] >= s);
    if (all) break;
    if (spins < 3) __builtin_amdgcn_s_sleep(PLAN_SLEEP); else __builtin_amdgcn_s_sleep(WSL);
    if (spin_fail(spins, abortw)) break;
  }
  int d = ra[0] == (unsigned)s ? (int)(ra[1] - t0) : 0;
  d = max(d, __builtin_amdgcn_update_dpp(0, d, 0xB1, 0xf, 0xf, false));
  d = max(d, __builtin_amdgcn_update_dpp(0, d, 0x4E, 0xf, 0xf, false));
  int sum = d + __builtin_amdgcn_update_dpp(0, d, 0x128, 0xf, 0xf, false);
  sum += __builtin_amdgcn_update_dpp(0, sum, 0x124, 0xf, 0xf, false);
  const int mx = wave_max(d), mean = (__builtin_amdgcn_readlane(sum, 0) + __builtin_amdgcn_readlane(sum, 16)) >> 3;
  const unsigned t = t0 + mx - min(((mx - mean) * REF_W) >> 2, REF_G) + wave_max(rl < 0 ? RL0 : rl) + (DS_RED + DS_MEM) + (s < DS_N0 ? DS_X0 : 0);
  lds_vu_ptr tgt = lds_at(tgt_);
  if (lane < 2) tgt[lane] = lane ? t : s + 1;
}
__device__ __forceinline__ unsigned ld_flag(__amdgpu_buffer_rsrc_t rs, int off) {
  return __builtin_amdgcn_raw_buffer_load_b32(rs, off, 0, 16);
}

constexpr unsigned long long QK_MAP[8] = {0xbaf9876d543210ecull, 0xa98f76d543210bceull, 0x98f76d543210ecbaull, 0x876fd543210bcea9ull, 0x76fd543210ecba98ull, 0x65df43210bcea987ull, 0x5df43210ecba9876ull, 0xd43f210bcea98765ull};
#define QK_ASM_0 \
  "s_waitcnt lgkmcnt(0)\n\t" \
  "v_subrev_u32 v244, 0x8000, v250\n\t" \
  "ds_read_b128 v[216:219], v248 offset:32768\n\t" \
  "ds_read_b128 v[220:223], v248 offset:33792\n\t" \
  "ds_read_b128 v[224:227], v248 offset:34816\n\t" \
  "ds_read_b128 v[228:231], v248 offset:35840\n\t" \
  "ds_read_b128 v[208:211], v244 offset:0\n\t" \
  "ds_read_b128 v[212:215], v244 offset:1024\n\t" \
  "ds_read_b128 v[236:239], v244 offset:2048\n\t" \
  "ds_read_b128 v[240:243], v244 offset:3072\n\t" \
  "v_mov_b64 v[192:193], 0\n\t" \
  "v_mov_b64 v[194:195], 0\n\t" \
  "v_mov_b64 v[196:197], 0\n\t" \
  "v_mov_b64 v[198:199], 0\n\t" \
  ".p2align 5\n\t" \
  "s_nop 0\n\t" \
  "s_nop 0\n\t" \
  "s_nop 0\n\t" \
  "s_nop 0\n\t" \
  "s_movk_i32 s92, 0x2000\n\t" \
  ".Lwt_%=:\n\t" \
  "ds_read_b64 v[200:201], v248 offset:30720\n\t" \
  "s_memrealtime s[90:91]\n\t" \
  "s_sub_u32 s92, s92, 1\n\t" \
  "s_waitcnt lgkmcnt(0)\n\t" \
  "v_readfirstlane_b32 s93, v200\n\t" \
  "v_readfirstlane_b32 %[tgt], v201\n\t" \
  "s_cbranch_scc1 .Lgo_%=\n\t" \
  "s_cmp_eq_u32 s93, %[step]\n\t" \
  "s_cbranch_scc1 .Lwx_%=\n\t" \
  "s_sleep 1\n\t" \
  "s_branch .Lwt_%=\n\t" \
  ".Lwx_%=:\n\t" \
  "s_lshl_b32 s93, s90, 2\n\t" \
  "s_sub_i32 s93, %[tgt], s93\n\t" \
  "s_cmp_le_i32 s93, 0\n\t" \
  "s_cbranch_scc1 .Lgo_%=\n\t" \
  "s_min_i32 s93, s93, 8000\n\t" \
  "s_mov_b32 s94, s93\n\t" \
  ".Lsl_%=:\n\t" \
  "s_sub_i32 s94, s94, 18\n\t" \
  "s_cmp_lt_i32 s94, 30\n\t" \
  "s_cbranch_scc1 .Lsx_%=\n\t" \
  "s_sleep 1\n\t" \
  "s_branch .Lsl_%=\n\t" \
  ".Lsx_%=:\n\t" \
  "s_sub_i32 s93, s93, 12\n\t" \
  "s_max_i32 s93, s93, 4\n\t" \
  "s_lshr_b32 s93, s93, 2\n\t" \
  "s_add_u32 s93, s93, s90\n\t" \
  "s_memrealtime s[90:91]\n\t" \
  ".Lpace_%=:\n\t" \
  "s_memrealtime s[94:95]\n\t" \
  "s_waitcnt lgkmcnt(1)\n\t" \
  "s_sub_i32 s92, s90, s93\n\t" \
  "s_cmp_ge_i32 s92, 0\n\t" \
  "s_cbranch_scc1 .Le_%=\n\t" \
  "s_memrealtime s[90:91]\n\t" \
  "s_waitcnt lgkmcnt(1)\n\t" \
  "s_sub_i32 s92, s94, s93\n\t" \
  "s_cmp_lt_i32 s92, 0\n\t" \
  "s_cbranch_scc1 .Lpace_%=\n\t" \
  "s_nop 4\n\t" \
  ".Le_%=:\n\t" \
  "s_add_u32 s92, s92, s93\n\t" \
  "s_lshl_b32 s92, s92, 2\n\t" \
  "s_sub_i32 s92, %[tgt], s92\n\t" \
  "s_max_i32 s92, s92, 0\n\t" \
  "s_bitcmp0_b32 s92, 3\n\t" \
  "s_cbranch_scc1 .Lf3_%=\n\t" \
  "s_nop 15\n\t" \
  ".Lf3_%=:\n\t" \
  "s_bitcmp0_b32 s92, 2\n\t" \
  "s_cbranch_scc1 .Lf2_%=\n\t" \
  "s_nop 10\n\t" \
  ".Lf2_%=:\n\t" \
  ".Lgo_%=:\n\t" \
  "s_mov_b32 s92, 0\n\t" \
  ".Lpoll_%=:\n\t" \
  "buffer_load_dwordx4 v[200:203], v248, %[rs], %[som] offen sc1\n\t" \
  "buffer_load_dwordx4 v[204:207], v248, %[rs], %[som2] offen sc1\n\t" \
  ".p2align 5\n\t" \
  "s_waitcnt vmcnt(1) lgkmcnt(0)\n\t" \
  "ds_write_b128 v249, v[200:203]\n\t" \
  "v_smfmac_f32_16x16x64_bf16 v[192:195], v[200:203], v[208:215], v251\n\t" \
  "v_max3_u32 v244, v200, v201, v202\n\t" \
  "v_smfmac_f32_16x16x64_bf16 v[196:199], v[200:203], v[236:243], v251\n\t" \
  "s_waitcnt vmcnt(0)\n\t" \
  "ds_write_b128 v249, v[204:207] offset:8192\n\t" \
  "v_smfmac_f32_16x16x64_bf16 v[192:195], v[204:207], v[216:223], v251\n\t" \
  "v_max3_u32 v244, v244, v203, v204\n\t" \
  "v_max3_u32 v244, v244, v205, v206\n\t" \
  "v_smfmac_f32_16x16x64_bf16 v[196:199], v[204:207], v[224:231], v251\n\t" \
  "v_max_u32 v244, v244, v207\n\t" \
  "v_cmp_le_u32_e64 vcc, %[sent], v244\n\t" \
  "s_cbranch_vccnz .Lslow_%=\n\t" \
  ".Lok_%=:\n\t" \
  "ds_read_b128 v[216:219], v250 offset:32768\n\t" \
  "ds_read_b128 v[220:223], v250 offset:33792\n\t" \
  "ds_read_b128 v[224:227], v250 offset:34816\n\t" \
  "ds_read_b128 v[228:231], v250 offset:35840\n\t" \
  "s_waitcnt lgkmcnt(4)\n\t" \
  "s_barrier\n\t" \
  "ds_read_b128 v[208:211], v248 offset:1024\n\t" \
  "ds_read_b128 v[212:215], v248 offset:2048\n\t" \
  "ds_read_b128 v[236:239], v248 offset:3072\n\t" \
  "ds_read_b128 v[240:243], v248 offset:4096\n\t" \
  "s_cmp_eq_u32 %[step], 63\n\t" \
  "s_cbranch_scc1 .Lls_%=\n\t" \
  "s_waitcnt lgkmcnt(3)\n\t" \
  "ds_read_b128 v[244:247], v248 offset:5120\n\t" \
  "v_smfmac_f32_16x16x64_bf16 v[192:195], v[208:211], v[216:223], v251\n\t" \
  "ds_read_b128 v[200:203], v248 offset:13312\n\t" \
  "v_smfmac_f32_16x16x64_bf16 v[196:199], v[208:211], v[224:231], v251\n\t" \
  "s_waitcnt lgkmcnt(4)\n\t" \
  "ds_read_b128 v[216:219], v250 offset:0\n\t" \
  "v_smfmac_f32_16x16x64_bf16 v[192:195], v[212:215], v[0:7], v251\n\t" \
  "ds_read_b128 v[220:223], v250 offset:1024\n\t" \
  "v_smfmac_f32_16x16x64_bf16 v[196:199], v[212:215], v[8:15], v251\n\t" \
  "s_waitcnt lgkmcnt(5)\n\t" \
  "ds_read_b128 v[224:227], v250 offset:2048\n\t" \
  "v_smfmac_f32_16x16x64_bf16 v[192:195], v[236:239], v[16:23], v251\n\t" \
  "ds_read_b128 v[228:231], v250 offset:3072\n\t" \
  "v_smfmac_f32_16x16x64_bf16 v[196:199], v[236:239], v[24:31], v251\n\t" \
  "s_waitcnt lgkmcnt(5)\n\t" \
  "ds_read_b128 v[204:207], v248 offset:6144\n\t" \
  "v_smfmac_f32_16x16x64_bf16 v[192:195], v[240:243], v[32:39], v251\n\t" \
  "ds_read_b128 v[208:211], v248 offset:7168\n\t" \
  "v_smfmac_f32_16x16x64_bf16 v[196:199], v[240:243], v[40:47], v251\n\t" \
  "ds_read_b128 v[212:215], v248 offset:9216\n\t" \
  "v_smfmac_f32_16x16x64_bf16 v[192:195], v[244:247], v[48:55], v251\n\t" \
  "ds_read_b128 v[236:239], v248 offset:10240\n\t" \
  "v_smfmac_f32_16x16x64_bf16 v[196:199], v[244:247], v[56:63], v251\n\t" \
  "s_waitcnt lgkmcnt(6)\n\t" \
  "v_smfmac_f32_16x16x64_bf16 v[192:195], v[200:203], v[216:223], v251\n\t" \
  "s_waitcnt lgkmcnt(4)\n\t" \
  "v_smfmac_f32_16x16x64_bf16 v[196:199], v[200:203], v[224:231], v251\n\t" \
  "s_waitcnt lgkmcnt(3)\n\t" \
  "ds_read_b128 v[240:243], v248 offset:11264\n\t" \
  "v_smfmac_f32_16x16x64_bf16 v[192:195], v[204:207], v[64:71], v251\n\t" \
  "ds_read_b128 v[244:247], v248 offset:12288\n\t" \
  "v_smfmac_f32_16x16x64_bf16 v[196:199], v[204:207], v[72:79], v251\n\t" \
  "s_waitcnt lgkmcnt(3)\n\t" \
  "v_smfmac_f32_16x16x64_bf16 v[192:195], v[208:211], v[80:87], v251\n\t" \
  "ds_read_b128 v[200:203], v248 offset:14336\n\t" \
  "v_smfmac_f32_16x16x64_bf16 v[196:199], v[208:211], v[88:95], v251\n\t" \
  "ds_read_b128 v[204:207], v248 offset:15360\n\t" \
  "v_smfmac_f32_16x16x64_bf16 v[192:195], v[212:215], v[96:103], v251\n\t" \
  "v_smfmac_f32_16x16x64_bf16 v[196:199], v[212:215], v[104:111], v251\n\t" \
  "s_waitcnt lgkmcnt(3)\n\t" \
  "v_smfmac_f32_16x16x64_bf16 v[192:195], v[236:239], v[112:119], v251\n\t" \
  "v_smfmac_f32_16x16x64_bf16 v[196:199], v[236:239], v[120:127], v251\n\t" \
  "v_smfmac_f32_16x16x64_bf16 v[192:195], v[240:243], v[128:135], v251\n\t" \
  "v_smfmac_f32_16x16x64_bf16 v[196:199], v[240:243], v[136:143], v251\n\t" \
  "s_waitcnt lgkmcnt(0)\n\t" \
  "v_smfmac_f32_16x16x64_bf16 v[192:195], v[244:247], v[144:151], v251\n\t" \
  "v_smfmac_f32_16x16x64_bf16 v[196:199], v[244:247], v[152:159], v251\n\t" \
  "v_smfmac_f32_16x16x64_bf16 v[192:195], v[200:203], v[160:167], v251\n\t" \
  "v_smfmac_f32_16x16x64_bf16 v[196:199], v[200:203], v[168:175], v251\n\t" \
  "v_smfmac_f32_16x16x64_bf16 v[192:195], v[204:207], v[176:183], v251\n\t" \
  "v_smfmac_f32_16x16x64_bf16 v[196:199], v[204:207], v[184:191], v251\n\t" \
  "ds_read_b128 v[200:203], v248 offset:16384\n\t" \
  "ds_read_b128 v[204:207], v248 offset:20480\n\t" \
  "v_mov_b32 v250, v232\n\t" \
  "v_mov_b32 v249, v233\n\t" \
  "s_nop 2\n\t" \
  "v_add_f32 v192, v192, v194\n\t" \
  "v_add_f32 v193, v193, v195\n\t" \
  "v_add_f32 v196, v196, v198\n\t" \
  "v_add_f32_e64 v197, v197, v199\n\t" \
  "v_cvt_pk_bf16_f32 v192, v192, v193\n\t" \
  "v_cvt_pk_bf16_f32 v196, v196, v197\n\t" \
  "ds_write2_b32 v252, v192, v196 offset1:64\n\t" \
  "v_mov_b32_e64 v251, v234\n\t" \
  "ds_read_b64_tr_b16 v[232:233], v250 offset:0\n\t" \
  "ds_read_b64_tr_b16 v[234:235], v250 offset:64\n\t" \
  "v_mov_b32_e64 v212, %[step]\n\t" \
  "s_mov_b64 exec, 1\n\t" \
  "ds_write_b32 v248, v212 offset:28672\n\t" \
  "s_mov_b64 exec, -1\n\t" \
  "buffer_load_dwordx4 v[192:195], v248, %[rt], %[svt] offen offset:0 sc1\n\t" \
  "buffer_load_dwordx4 v[196:199], v248, %[rt], %[svt] offen offset:1024 sc1\n\t" \
  "s_waitcnt lgkmcnt(1)\n\t" \
  "v_mfma_f32_16x16x32_bf16 v[200:203], v[200:203], v[232:235], 0\n\t" \
  "v_mfma_f32_16x16x32_bf16 v[204:207], v[204:207], v[232:235], 0\n\t" \
  "s_nop 7\n\t" \
  "v_cndmask_b32 v216, v200, v204, %[nh]\n\t" \
  "v_cndmask_b32 v217, v201, v205, %[nh]\n\t" \
  "v_cndmask_b32 v218, v202, v206, %[nh]\n\t" \
  "v_cndmask_b32 v219, v203, v207, %[nh]\n\t" \
  "ds_write_b128 v248, v[216:219] offset:16384\n\t" \
  "buffer_load_dword v252, v249, %[rs], %[sv] offen offset:0 sc1\n\t" \
  "buffer_load_dword v253, v249, %[rs], %[sv] offen offset:256 sc1\n\t" \
  "ds_read_b32 v212, v248 offset:28672\n\t" \
  "ds_read_b64_tr_b16 v[236:237], v250 offset:512\n\t" \
  "ds_read_b64_tr_b16 v[238:239], v250 offset:576\n\t" \
  "ds_read_b64_tr_b16 v[240:241], v250 offset:1024\n\t" \
  "ds_read_b64_tr_b16 v[242:243], v250 offset:1088\n\t" \
  "ds_read_b64_tr_b16 v[244:245], v250 offset:1536\n\t" \
  "ds_read_b64_tr_b16 v[246:247], v250 offset:1600\n\t" \
  "s_setprio 3\n\t" \
  "s_waitcnt lgkmcnt(0)\n\t" \
  "v_cmp_ne_u32 vcc, %[step], v212\n\t" \
  "s_and_b32 s93, vcc_lo, 0xf\n\t" \
  "s_barrier\n\t" \
  "s_cbranch_scc1 .Lre_%=\n\t" \
  ".Lrb_%=:\n\t" \
  "ds_read_b64_tr_b16 v[200:201], v250 offset:2048\n\t" \
  "ds_read_b64_tr_b16 v[202:203], v250 offset:2112\n\t" \
  "ds_read_b64_tr_b16 v[204:205], v250 offset:2560\n\t" \
  "ds_read_b64_tr_b16 v[206:207], v250 offset:2624\n\t" \
  "ds_read_b64_tr_b16 v[208:209], v250 offset:3072\n\t" \
  "ds_read_b64_tr_b16 v[210:211], v250 offset:3136\n\t" \
  "ds_read_b64_tr_b16 v[212:213], v250 offset:3584\n\t" \
  "ds_read_b64_tr_b16 v[214:215], v250 offset:3648\n\t" \
  "ds_read_b128 v[220:223], v248 offset:17408\n\t" \
  "ds_read_b128 v[224:227], v248 offset:18432\n\t" \
  "ds_read_b128 v[228:231], v248 offset:19456\n\t" \
  "s_waitcnt lgkmcnt(9)\n\t" \
  "v_mfma_f32_16x16x32_bf16 v[200:203], v[200:203], v[232:235], 0\n\t" \
  "s_waitcnt lgkmcnt(7)\n\t" \
  "v_mfma_f32_16x16x32_bf16 v[200:203], v[204:207], v[236:239], v[200:203]\n\t" \
  "v_mov_b32 v233, 0\n\t" \
  "s_waitcnt lgkmcnt(5)\n\t" \
  "v_mfma_f32_16x16x32_bf16 v[200:203], v[208:211], v[240:243], v[200:203]\n\t" \
  "v_mov_b32 v234, 0\n\t" \
  "s_waitcnt lgkmcnt(3)\n\t" \
  "v_mfma_f32_16x16x32_bf16 v[200:203], v[212:215], v[244:247], v[200:203]\n\t" \
  "s_waitcnt lgkmcnt(2)\n\t" \
  "v_pk_add_f32 v[216:217], v[216:217], v[220:221]\n\t" \
  "v_pk_add_f32 v[218:219], v[218:219], v[222:223]\n\t" \
  "s_waitcnt lgkmcnt(0)\n\t" \
  "v_pk_add_f32 v[224:225], v[224:225], v[228:229]\n\t" \
  "v_pk_add_f32 v[226:227], v[226:227], v[230:231]\n\t" \
  "v_pk_add_f32 v[216:217], v[216:217], v[224:225]\n\t" \
  "v_pk_add_f32 v[218:219], v[218:219], v[226:227]\n\t" \
  "v_max3_f32 v224, v216, v217, v218\n\t" \
  "v_max_f32 v224, v224, v219\n\t" \
  "v_max3_f32 v231, v200, v201, v202\n\t" \
  "v_max_f32 v231, v231, v203\n\t" \
  "v_max_f32_dpp v225, v224, v224 row_ror:8 row_mask:0xf bank_mask:0xf\n\t" \
  "v_max_f32_dpp v226, v224, v224 row_ror:8 row_mask:0xf bank_mask:0xf\n\t" \
  "v_cndmask_b32 v200, v200, v202, %[up]\n\t" \
  "v_cndmask_b32 v201, v201, v203, %[up]\n\t" \
  "v_permlane32_swap_b32_e64 v225, v226\n\t" \
  "v_max3_f32 v205, v225, v226, v231\n\t" \
  "v_max3_f32 v206, v225, v226, v231\n\t" \
  "v_mov_b64 v[220:221], %[one]\n\t" \
  "v_mov_b64 v[222:223], %[one]\n\t" \
  "v_permlane16_swap_b32 v205, v206\n\t" \
  "v_max_f32 v204, v205, v206\n\t" \
  "v_mul_f32_e64 v204, %[negc], v204\n\t" \
  "v_fma_f32 v200, v200, %[c], v204\n\t" \
  "v_fma_f32 v201, v201, %[c], v204\n\t" \
  "v_exp_f32 v200, v200\n\t" \
  "v_fma_f32 v216, v216, %[c], v204\n\t" \
  "v_exp_f32 v201, v201\n\t" \
  "v_fma_f32 v217, v217, %[c], v204\n\t" \
  "v_exp_f32_e64 v216, v216\n\t" \
  "v_fma_f32 v218, v218, %[c], v204\n\t" \
  "v_cvt_pk_bf16_f32 v232, v200, v201\n\t" \
  "v_cvt_pk_bf16_f32 v235, v200, v201\n\t" \
  "v_exp_f32 v217, v217\n\t" \
  "v_fma_f32 v219, v219, %[c], v204\n\t" \
  "v_mfma_f32_16x16x16_bf16 v[212:215], v[220:221], v[232:233], 0\n\t" \
  "v_exp_f32 v218, v218\n\t" \
  "s_waitcnt vmcnt(0)\n\t" \
  "v_mfma_f32_16x16x16_bf16 v[224:227], v[252:253], v[232:233], 0\n\t" \
  "v_cvt_pk_bf16_f32 v210, v216, v217\n\t" \
  "v_cvt_pk_bf16_f32 v208, v216, v217\n\t" \
  "v_exp_f32 v219, v219\n\t" \
  "v_mfma_f32_16x16x16_bf16 v[228:231], v[252:253], v[234:235], 0\n\t" \
  "v_cvt_pk_bf16_f32 v211, v218, v219\n\t" \
  "v_cvt_pk_bf16_f32 v209, v218, v219\n\t" \
  "v_mov_b32_dpp v208, v210 row_ror:8 row_mask:0xf bank_mask:0xc\n\t" \
  "v_mov_b32_dpp v210, v210 row_ror:8 row_mask:0xf bank_mask:0x3\n\t" \
  "v_mov_b32_dpp v209, v211 row_ror:8 row_mask:0xf bank_mask:0xc\n\t" \
  "v_mov_b32_dpp v211, v211 row_ror:8 row_mask:0xf bank_mask:0x3\n\t" \
  "v_max_u32 v204, v252, v253\n\t" \
  "v_cmp_le_u32 vcc, %[sent], v204\n\t" \
  "v_mfma_f32_16x16x32_bf16 v[212:215], v[220:223], v[208:211], v[212:215]\n\t" \
  ".Lpv_%=:\n\t" \
  "v_mfma_f32_16x16x32_bf16 v[224:227], v[192:195], v[208:211], v[224:227]\n\t" \
  "v_mfma_f32_16x16x32_bf16 v[228:231], v[196:199], v[208:211], v[228:231]\n\t" \
  "s_cbranch_vccnz .Lvs_%=\n\t" \
  ".Lvok_%=:\n\t" \
  "s_nop 4\n\t" \
  "v_rcp_f32_e64 v204, v212\n\t" \
  "v_cndmask_b32 v224, v224, v226, %[nh]\n\t" \
  "v_cndmask_b32 v225, v225, v227, %[nh]\n\t" \
  "v_cndmask_b32 v228, v228, v230, %[nh]\n\t" \
  "v_cndmask_b32 v229, v229, v231, %[nh]\n\t" \
  "v_pk_mul_f32 v[224:225], v[224:225], v[204:205] op_sel_hi:[1,0]\n\t" \
  "v_pk_mul_f32 v[228:229], v[228:229], v[204:205] op_sel_hi:[1,0]\n\t" \
  "v_cvt_pk_bf16_f32 v224, v224, v225\n\t" \
  "v_cvt_pk_bf16_f32 v225, v228, v229\n\t" \
  "buffer_store_dwordx2 v[224:225], v251, %[rs], %[so] offen sc1\n\t" \
  "s_memtime s[90:91]\n\t" \
  "s_setprio 0\n\t" \
  "s_waitcnt lgkmcnt(0)\n\t" \
  "s_mov_b32 %[tout], s90\n\t" \
  "s_mov_b32 %[spins], s92\n\t" \
  "s_branch .Lend_%=\n\t" \
  ".Lls_%=:\n\t" \
  "s_waitcnt lgkmcnt(0)\n\t" \
  "s_barrier\n\t" \
  "s_mov_b32 %[spins], 0\n\t" \
  "s_branch .Lend_%=\n\t" \
  ".Lre_%=:\n\t" \
  "ds_read_b64_tr_b16 v[236:237], v250 offset:512\n\t" \
  "ds_read_b64_tr_b16 v[238:239], v250 offset:576\n\t" \
  "ds_read_b64_tr_b16 v[240:241], v250 offset:1024\n\t" \
  "ds_read_b64_tr_b16 v[242:243], v250 offset:1088\n\t" \
  "ds_read_b64_tr_b16 v[244:245], v250 offset:1536\n\t" \
  "ds_read_b64_tr_b16 v[246:247], v250 offset:1600\n\t" \
  "s_waitcnt lgkmcnt(0)\n\t" \
  "s_branch .Lrb_%=\n\t" \
  ".Lvs_%=:\n\t" \
  "v_cmp_eq_u32 vcc, %[sent], v252\n\t" \
  "v_cmp_eq_u32 s[94:95], %[sent], v253\n\t" \
  "s_or_b64 s[94:95], s[94:95], vcc\n\t" \
  "s_cbranch_scc0 .Lvok_%=\n\t" \
  "s_add_u32 s92, s92, 1\n\t" \
  "s_sleep 2\n\t" \
  "s_and_b32 s93, s92, 63\n\t" \
  "s_cbranch_scc1 .Lvr_%=\n\t" \
  "buffer_load_dword v204, off, %[rs], %[sab] sc1\n\t" \
  "s_waitcnt vmcnt(0)\n\t" \
  "v_readfirstlane_b32 s93, v204\n\t" \
  "s_nop 3\n\t" \
  "s_cmp_eq_u32 s93, 1\n\t" \
  "s_cbranch_scc1 .Lvok_%=\n\t" \
  "s_cmp_lt_u32 s92, 0x80000\n\t" \
  "s_cbranch_scc1 .Lvr_%=\n\t" \
  "v_mov_b32 v204, 1\n\t" \
  "buffer_store_dword v204, off, %[rs], %[sab] sc1\n\t" \
  "s_branch .Lvok_%=\n\t" \
  ".Lvr_%=:\n\t" \
  "buffer_load_dword v252, v249, %[rs], %[sv] offen offset:0 sc1\n\t" \
  "buffer_load_dword v253, v249, %[rs], %[sv] offen offset:256 sc1\n\t" \
  "s_waitcnt vmcnt(0)\n\t" \
  "v_mfma_f32_16x16x16_bf16 v[224:227], v[252:253], v[232:233], 0\n\t" \
  "v_mfma_f32_16x16x16_bf16 v[228:231], v[252:253], v[234:235], 0\n\t" \
  "v_max_u32 v204, v252, v253\n\t" \
  "v_cmp_le_u32 vcc, %[sent], v204\n\t" \
  "s_nop 4\n\t" \
  "s_branch .Lpv_%=\n\t" \
  "s_branch .Lend_%=\n\t" \
  ".Lslow_%=:\n\t" \
  "s_mov_b64 s[94:95], 0\n\t" \
  "v_cmp_eq_u32 vcc, %[sent], v200\n\t" \
  "s_or_b64 s[94:95], s[94:95], vcc\n\t" \
  "v_cmp_eq_u32 vcc, %[sent], v201\n\t" \
  "s_or_b64 s[94:95], s[94:95], vcc\n\t" \
  "v_cmp_eq_u32 vcc, %[sent], v202\n\t" \
  "s_or_b64 s[94:95], s[94:95], vcc\n\t" \
  "v_cmp_eq_u32 vcc, %[sent], v203\n\t" \
  "s_or_b64 s[94:95], s[94:95], vcc\n\t" \
  "v_cmp_eq_u32 vcc, %[sent], v204\n\t" \
  "s_or_b64 s[94:95], s[94:95], vcc\n\t" \
  "v_cmp_eq_u32 vcc, %[sent], v205\n\t" \
  "s_or_b64 s[94:95], s[94:95], vcc\n\t" \
  "v_cmp_eq_u32 vcc, %[sent], v206\n\t" \
  "s_or_b64 s[94:95], s[94:95], vcc\n\t" \
  "v_cmp_eq_u32 vcc, %[sent], v207\n\t" \
  "s_or_b64 s[94:95], s[94:95], vcc\n\t" \
  "s_cmp_eq_u64 s[94:95], 0\n\t" \
  "s_cbranch_scc1 .Lok_%=\n\t" \
  "v_mov_b64 v[192:193], 0\n\t" \
  "v_mov_b64 v[194:195], 0\n\t" \
  "v_mov_b64 v[196:197], 0\n\t" \
  "v_mov_b64 v[198:199], 0\n\t" \
  "s_add_u32 s92, s92, 1\n\t" \
  "s_cmp_eq_u32 s92, 1\n\t" \
  "s_cbranch_scc1 .Lpoll_%=\n\t" \
  "s_sleep 2\n\t" \
  "s_and_b32 s93, s92, 63\n\t" \
  "s_cmp_lg_u32 s93, 0\n\t" \
  "s_cbranch_scc1 .Lpoll_%=\n\t" \
  "v_mov_b32 v245, 0\n\t" \
  "buffer_load_dword v244, v245, %[rs], %[sab] offen sc1\n\t" \
  "s_waitcnt vmcnt(0)\n\t" \
  "v_readfirstlane_b32 s93, v244\n\t" \
  "s_nop 3\n\t" \
  "s_cmp_eq_u32 s93, 1\n\t" \
  "s_cbranch_scc1 .Lok_%=\n\t" \
  "s_cmp_lt_u32 s92, 0x80000\n\t" \
  "s_cbranch_scc1 .Lpoll_%=\n\t" \
  "v_mov_b32 v244, 1\n\t" \
  "buffer_store_dword v244, v245, %[rs], %[sab] offen sc1\n\t" \
  "s_branch .Lok_%=\n\t" \
  ".Lend_%=:\n\t"
#define QK_ASM_1 \
  "s_waitcnt lgkmcnt(0)\n\t" \
  "v_subrev_u32 v244, 0x8000, v250\n\t" \
  "ds_read_b128 v[216:219], v248 offset:36864\n\t" \
  "ds_read_b128 v[220:223], v248 offset:37888\n\t" \
  "ds_read_b128 v[224:227], v248 offset:38912\n\t" \
  "ds_read_b128 v[228:231], v248 offset:39936\n\t" \
  "ds_read_b128 v[208:211], v244 offset:0\n\t" \
  "ds_read_b128 v[212:215], v244 offset:1024\n\t" \
  "ds_read_b128 v[236:239], v244 offset:2048\n\t" \
  "ds_read_b128 v[240:243], v244 offset:3072\n\t" \
  "v_mov_b64 v[192:193], 0\n\t" \
  "v_mov_b64 v[194:195], 0\n\t" \
  "v_mov_b64 v[196:197], 0\n\t" \
  "v_mov_b64 v[198:199], 0\n\t" \
  ".p2align 5\n\t" \
  "s_nop 0\n\t" \
  "s_nop 0\n\t" \
  "s_nop 0\n\t" \
  "s_nop 0\n\t" \
  "s_movk_i32 s92, 0x2000\n\t" \
  ".Lwt_%=:\n\t" \
  "ds_read_b64 v[200:201], v248 offset:30720\n\t" \
  "s_memrealtime s[90:91]\n\t" \
  "s_sub_u32 s92, s92, 1\n\t" \
  "s_waitcnt lgkmcnt(0)\n\t" \
  "v_readfirstlane_b32 s93, v200\n\t" \
  "v_readfirstlane_b32 %[tgt], v201\n\t" \
  "s_cbranch_scc1 .Lgo_%=\n\t" \
  "s_cmp_eq_u32 s93, %[step]\n\t" \
  "s_cbranch_scc1 .Lwx_%=\n\t" \
  "s_sleep 1\n\t" \
  "s_branch .Lwt_%=\n\t" \
  ".Lwx_%=:\n\t" \
  "s_lshl_b32 s93, s90, 2\n\t" \
  "s_sub_i32 s93, %[tgt], s93\n\t" \
  "s_cmp_le_i32 s93, 0\n\t" \
  "s_cbranch_scc1 .Lgo_%=\n\t" \
  "s_min_i32 s93, s93, 8000\n\t" \
  "s_mov_b32 s94, s93\n\t" \
  ".Lsl_%=:\n\t" \
  "s_sub_i32 s94, s94, 18\n\t" \
  "s_cmp_lt_i32 s94, 30\n\t" \
  "s_cbranch_scc1 .Lsx_%=\n\t" \
  "s_sleep 1\n\t" \
  "s_branch .Lsl_%=\n\t" \
  ".Lsx_%=:\n\t" \
  "s_sub_i32 s93, s93, 12\n\t" \
  "s_max_i32 s93, s93, 4\n\t" \
  "s_lshr_b32 s93, s93, 2\n\t" \
  "s_add_u32 s93, s93, s90\n\t" \
  "s_memrealtime s[90:91]\n\t" \
  ".Lpace_%=:\n\t" \
  "s_memrealtime s[94:95]\n\t" \
  "s_waitcnt lgkmcnt(1)\n\t" \
  "s_sub_i32 s92, s90, s93\n\t" \
  "s_cmp_ge_i32 s92, 0\n\t" \
  "s_cbranch_scc1 .Le_%=\n\t" \
  "s_memrealtime s[90:91]\n\t" \
  "s_waitcnt lgkmcnt(1)\n\t" \
  "s_sub_i32 s92, s94, s93\n\t" \
  "s_cmp_lt_i32 s92, 0\n\t" \
  "s_cbranch_scc1 .Lpace_%=\n\t" \
  "s_nop 4\n\t" \
  ".Le_%=:\n\t" \
  "s_add_u32 s92, s92, s93\n\t" \
  "s_lshl_b32 s92, s92, 2\n\t" \
  "s_sub_i32 s92, %[tgt], s92\n\t" \
  "s_max_i32 s92, s92, 0\n\t" \
  "s_bitcmp0_b32 s92, 3\n\t" \
  "s_cbranch_scc1 .Lf3_%=\n\t" \
  "s_nop 15\n\t" \
  ".Lf3_%=:\n\t" \
  "s_bitcmp0_b32 s92, 2\n\t" \
  "s_cbranch_scc1 .Lf2_%=\n\t" \
  "s_nop 10\n\t" \
  ".Lf2_%=:\n\t" \
  ".Lgo_%=:\n\t" \
  "s_mov_b32 s92, 0\n\t" \
  ".Lpoll_%=:\n\t" \
  "buffer_load_dwordx4 v[200:203], v248, %[rs], %[som] offen sc1\n\t" \
  "buffer_load_dwordx4 v[204:207], v248, %[rs], %[som2] offen sc1\n\t" \
  ".p2align 5\n\t" \
  "s_waitcnt vmcnt(1) lgkmcnt(0)\n\t" \
  "ds_write_b128 v249, v[200:203]\n\t" \
  "v_smfmac_f32_16x16x64_bf16 v[192:195], v[200:203], v[208:215], v251\n\t" \
  "v_max3_u32 v244, v200, v201, v202\n\t" \
  "v_smfmac_f32_16x16x64_bf16 v[196:199], v[200:203], v[236:243], v251\n\t" \
  "s_waitcnt vmcnt(0)\n\t" \
  "ds_write_b128 v249, v[204:207] offset:8192\n\t" \
  "v_smfmac_f32_16x16x64_bf16 v[192:195], v[204:207], v[216:223], v251\n\t" \
  "v_max3_u32 v244, v244, v203, v204\n\t" \
  "v_max3_u32 v244, v244, v205, v206\n\t" \
  "v_smfmac_f32_16x16x64_bf16 v[196:199], v[204:207], v[224:231], v251\n\t" \
  "v_max_u32 v244, v244, v207\n\t" \
  "v_cmp_le_u32_e64 vcc, %[sent], v244\n\t" \
  "s_cbranch_vccnz .Lslow_%=\n\t" \
  ".Lok_%=:\n\t" \
  "ds_read_b128 v[216:219], v250 offset:32768\n\t" \
  "ds_read_b128 v[220:223], v250 offset:33792\n\t" \
  "ds_read_b128 v[224:227], v250 offset:34816\n\t" \
  "ds_read_b128 v[228:231], v250 offset:35840\n\t" \
  "s_waitcnt lgkmcnt(4)\n\t" \
  "s_barrier\n\t" \
  "ds_read_b128 v[208:211], v248 offset:0\n\t" \
  "ds_read_b128 v[212:215], v248 offset:3072\n\t" \
  "ds_read_b128 v[236:239], v248 offset:4096\n\t" \
  "ds_read_b128 v[240:243], v248 offset:5120\n\t" \
  "s_cmp_eq_u32 %[step], 63\n\t" \
  "s_cbranch_scc1 .Lls_%=\n\t" \
  "s_waitcnt lgkmcnt(3)\n\t" \
  "ds_read_b128 v[244:247], v248 offset:6144\n\t" \
  "v_smfmac_f32_16x16x64_bf16 v[192:195], v[208:211], v[216:223], v251\n\t" \
  "ds_read_b128 v[200:203], v248 offset:12288\n\t" \
  "v_smfmac_f32_16x16x64_bf16 v[196:199], v[208:211], v[224:231], v251\n\t" \
  "s_waitcnt lgkmcnt(4)\n\t" \
  "ds_read_b128 v[216:219], v250 offset:0\n\t" \
  "v_smfmac_f32_16x16x64_bf16 v[192:195], v[212:215], v[0:7], v251\n\t" \
  "ds_read_b128 v[220:223], v250 offset:1024\n\t" \
  "v_smfmac_f32_16x16x64_bf16 v[196:199], v[212:215], v[8:15], v251\n\t" \
  "s_waitcnt lgkmcnt(5)\n\t" \
  "ds_read_b128 v[224:227], v250 offset:2048\n\t" \
  "v_smfmac_f32_16x16x64_bf16 v[192:195], v[236:239], v[16:23], v251\n\t" \
  "ds_read_b128 v[228:231], v250 offset:3072\n\t" \
  "v_smfmac_f32_16x16x64_bf16 v[196:199], v[236:239], v[24:31], v251\n\t" \
  "s_waitcnt lgkmcnt(5)\n\t" \
  "ds_read_b128 v[204:207], v248 offset:7168\n\t" \
  "v_smfmac_f32_16x16x64_bf16 v[192:195], v[240:243], v[32:39], v251\n\t" \
  "ds_read_b128 v[208:211], v248 offset:8192\n\t" \
  "v_smfmac_f32_16x16x64_bf16 v[196:199], v[240:243], v[40:47], v251\n\t" \
  "ds_read_b128 v[212:215], v248 offset:10240\n\t" \
  "v_smfmac_f32_16x16x64_bf16 v[192:195], v[244:247], v[48:55], v251\n\t" \
  "ds_read_b128 v[236:239], v248 offset:11264\n\t" \
  "v_smfmac_f32_16x16x64_bf16 v[196:199], v[244:247], v[56:63], v251\n\t" \
  "s_waitcnt lgkmcnt(6)\n\t" \
  "v_smfmac_f32_16x16x64_bf16 v[192:195], v[200:203], v[216:223], v251\n\t" \
  "s_waitcnt lgkmcnt(4)\n\t" \
  "v_smfmac_f32_16x16x64_bf16 v[196:199], v[200:203], v[224:231], v251\n\t" \
  "s_waitcnt lgkmcnt(3)\n\t" \
  "ds_read_b128 v[240:243], v248 offset:13312\n\t" \
  "v_smfmac_f32_16x16x64_bf16 v[192:195], v[204:207], v[64:71], v251\n\t" \
  "ds_read_b128 v[244:247], v248 offset:14336\n\t" \
  "v_smfmac_f32_16x16x64_bf16 v[196:199], v[204:207], v[72:79], v251\n\t" \
  "s_waitcnt lgkmcnt(3)\n\t" \
  "v_smfmac_f32_16x16x64_bf16 v[192:195], v[208:211], v[80:87], v251\n\t" \
  "ds_read_b128 v[200:203], v248 offset:15360\n\t" \
  "v_smfmac_f32_16x16x64_bf16 v[196:199], v[208:211], v[88:95], v251\n\t" \
  "ds_read_b128 v[204:207], v248 offset:2048\n\t" \
  "v_smfmac_f32_16x16x64_bf16 v[192:195], v[212:215], v[96:103], v251\n\t" \
  "v_smfmac_f32_16x16x64_bf16 v[196:199], v[212:215], v[104:111], v251\n\t" \
  "s_waitcnt lgkmcnt(3)\n\t" \
  "v_smfmac_f32_16x16x64_bf16 v[192:195], v[236:239], v[112:119], v251\n\t" \
  "v_smfmac_f32_16x16x64_bf16 v[196:199], v[236:239], v[120:127], v251\n\t" \
  "v_smfmac_f32_16x16x64_bf16 v[192:195], v[240:243], v[128:135], v251\n\t" \
  "v_smfmac_f32_16x16x64_bf16 v[196:199], v[240:243], v[136:143], v251\n\t" \
  "s_waitcnt lgkmcnt(0)\n\t" \
  "v_smfmac_f32_16x16x64_bf16 v[192:195], v[244:247], v[144:151], v251\n\t" \
  "v_smfmac_f32_16x16x64_bf16 v[196:199], v[244:247], v[152:159], v251\n\t" \
  "v_smfmac_f32_16x16x64_bf16 v[192:195], v[200:203], v[160:167], v251\n\t" \
  "v_smfmac_f32_16x16x64_bf16 v[196:199], v[200:203], v[168:175], v251\n\t" \
  "v_smfmac_f32_16x16x64_bf16 v[192:195], v[204:207], v[176:183], v251\n\t" \
  "v_smfmac_f32_16x16x64_bf16 v[196:199], v[204:207], v[184:191], v251\n\t" \
  "ds_read_b128 v[200:203], v248 offset:17408\n\t" \
  "ds_read_b128 v[204:207], v248 offset:21504\n\t" \
  "v_mov_b32 v250, v232\n\t" \
  "v_mov_b32 v249, v233\n\t" \
  "s_nop 2\n\t" \
  "v_add_f32 v192, v192, v194\n\t" \
  "v_add_f32 v193, v193, v195\n\t" \
  "v_add_f32 v196, v196, v198\n\t" \
  "v_add_f32_e64 v197, v197, v199\n\t" \
  "v_cvt_pk_bf16_f32 v192, v192, v193\n\t" \
  "v_cvt_pk_bf16_f32 v196, v196, v197\n\t" \
  "ds_write2_b32 v252, v192, v196 offset1:64\n\t" \
  "v_mov_b32_e64 v251, v234\n\t" \
  "ds_read_b64_tr_b16 v[236:237], v250 offset:512\n\t" \
  "ds_read_b64_tr_b16 v[238:239], v250 offset:576\n\t" \
  "v_mov_b32_e64 v212, %[step]\n\t" \
  "s_mov_b64 exec, 1\n\t" \
  "ds_write_b32 v248, v212 offset:28688\n\t" \
  "s_mov_b64 exec, -1\n\t" \
  "buffer_load_dwordx4 v[192:195], v248, %[rt], %[svt] offen offset:0 sc1\n\t" \
  "buffer_load_dwordx4 v[196:199], v248, %[rt], %[svt] offen offset:1024 sc1\n\t" \
  "s_waitcnt lgkmcnt(1)\n\t" \
  "v_mfma_f32_16x16x32_bf16 v[200:203], v[200:203], v[236:239], 0\n\t" \
  "v_mfma_f32_16x16x32_bf16 v[204:207], v[204:207], v[236:239], 0\n\t" \
  "s_nop 7\n\t" \
  "v_cndmask_b32 v216, v200, v204, %[nh]\n\t" \
  "v_cndmask_b32 v217, v201, v205, %[nh]\n\t" \
  "v_cndmask_b32 v218, v202, v206, %[nh]\n\t" \
  "v_cndmask_b32 v219, v203, v207, %[nh]\n\t" \
  "ds_write_b128 v248, v[216:219] offset:17408\n\t" \
  "buffer_load_dword v252, v249, %[rs], %[sv] offen offset:0 sc1\n\t" \
  "buffer_load_dword v253, v249, %[rs], %[sv] offen offset:256 sc1\n\t" \
  "ds_read_b32 v212, v248 offset:28672\n\t" \
  "ds_read_b64_tr_b16 v[240:241], v250 offset:1024\n\t" \
  "ds_read_b64_tr_b16 v[242:243], v250 offset:1088\n\t" \
  "ds_read_b64_tr_b16 v[244:245], v250 offset:1536\n\t" \
  "ds_read_b64_tr_b16 v[246:247], v250 offset:1600\n\t" \
  "ds_read_b64_tr_b16 v[232:233], v250 offset:0\n\t" \
  "ds_read_b64_tr_b16 v[234:235], v250 offset:64\n\t" \
  "s_setprio 3\n\t" \
  "s_waitcnt lgkmcnt(0)\n\t" \
  "v_cmp_ne_u32 vcc, %[step], v212\n\t" \
  "s_and_b32 s93, vcc_lo, 0xf\n\t" \
  "s_barrier\n\t" \
  "s_cbranch_scc1 .Lre_%=\n\t" \
  ".Lrb_%=:\n\t" \
  "ds_read_b64_tr_b16 v[204:205], v250 offset:2560\n\t" \
  "ds_read_b64_tr_b16 v[206:207], v250 offset:2624\n\t" \
  "ds_read_b64_tr_b16 v[208:209], v250 offset:3072\n\t" \
  "ds_read_b64_tr_b16 v[210:211], v250 offset:3136\n\t" \
  "ds_read_b64_tr_b16 v[212:213], v250 offset:3584\n\t" \
  "ds_read_b64_tr_b16 v[214:215], v250 offset:3648\n\t" \
  "ds_read_b64_tr_b16 v[200:201], v250 offset:2048\n\t" \
  "ds_read_b64_tr_b16 v[202:203], v250 offset:2112\n\t" \
  "ds_read_b128 v[220:223], v248 offset:18432\n\t" \
  "ds_read_b128 v[224:227], v248 offset:19456\n\t" \
  "ds_read_b128 v[228:231], v248 offset:16384\n\t" \
  "s_waitcnt lgkmcnt(9)\n\t" \
  "v_mfma_f32_16x16x32_bf16 v[204:207], v[204:207], v[236:239], 0\n\t" \
  "s_waitcnt lgkmcnt(7)\n\t" \
  "v_mfma_f32_16x16x32_bf16 v[204:207], v[208:211], v[240:243], v[204:207]\n\t" \
  "v_mov_b32 v237, 0\n\t" \
  "s_waitcnt lgkmcnt(5)\n\t" \
  "v_mfma_f32_16x16x32_bf16 v[204:207], v[212:215], v[244:247], v[204:207]\n\t" \
  "v_mov_b32 v238, 0\n\t" \
  "s_waitcnt lgkmcnt(3)\n\t" \
  "v_mfma_f32_16x16x32_bf16 v[204:207], v[200:203], v[232:235], v[204:207]\n\t" \
  "s_waitcnt lgkmcnt(2)\n\t" \
  "v_pk_add_f32 v[216:217], v[216:217], v[220:221]\n\t" \
  "v_pk_add_f32 v[218:219], v[218:219], v[222:223]\n\t" \
  "s_waitcnt lgkmcnt(0)\n\t" \
  "v_pk_add_f32 v[224:225], v[224:225], v[228:229]\n\t" \
  "v_pk_add_f32 v[226:227], v[226:227], v[230:231]\n\t" \
  "v_pk_add_f32 v[216:217], v[216:217], v[224:225]\n\t" \
  "v_pk_add_f32 v[218:219], v[218:219], v[226:227]\n\t" \
  "v_max3_f32 v224, v216, v217, v218\n\t" \
  "v_max_f32 v224, v224, v219\n\t" \
  "v_max3_f32 v231, v204, v205, v206\n\t" \
  "v_max_f32 v231, v231, v207\n\t" \
  "v_max_f32_dpp v225, v224, v224 row_ror:8 row_mask:0xf bank_mask:0xf\n\t" \
  "v_max_f32_dpp v226, v224, v224 row_ror:8 row_mask:0xf bank_mask:0xf\n\t" \
  "v_cndmask_b32 v204, v204, v206, %[up]\n\t" \
  "v_cndmask_b32 v205, v205, v207, %[up]\n\t" \
  "v_permlane32_swap_b32_e64 v225, v226\n\t" \
  "v_max3_f32 v209, v225, v226, v231\n\t" \
  "v_max3_f32 v210, v225, v226, v231\n\t" \
  "v_mov_b64 v[220:221], %[one]\n\t" \
  "v_mov_b64 v[222:223], %[one]\n\t" \
  "v_permlane16_swap_b32 v209, v210\n\t" \
  "v_max_f32 v208, v209, v210\n\t" \
  "v_mul_f32_e64 v208, %[negc], v208\n\t" \
  "v_fma_f32 v204, v204, %[c], v208\n\t" \
  "v_fma_f32 v205, v205, %[c], v208\n\t" \
  "v_exp_f32 v204, v204\n\t" \
  "v_fma_f32 v216, v216, %[c], v208\n\t" \
  "v_exp_f32 v205, v205\n\t" \
  "v_fma_f32 v217, v217, %[c], v208\n\t" \
  "v_exp_f32_e64 v216, v216\n\t" \
  "v_fma_f32 v218, v218, %[c], v208\n\t" \
  "v_cvt_pk_bf16_f32 v236, v204, v205\n\t" \
  "v_cvt_pk_bf16_f32 v239, v204, v205\n\t" \
  "v_exp_f32 v217, v217\n\t" \
  "v_fma_f32 v219, v219, %[c], v208\n\t" \
  "v_mfma_f32_16x16x16_bf16 v[200:203], v[220:221], v[236:237], 0\n\t" \
  "v_exp_f32 v218, v218\n\t" \
  "s_waitcnt vmcnt(0)\n\t" \
  "v_mfma_f32_16x16x16_bf16 v[224:227], v[252:253], v[236:237], 0\n\t" \
  "v_cvt_pk_bf16_f32 v214, v216, v217\n\t" \
  "v_cvt_pk_bf16_f32 v212, v216, v217\n\t" \
  "v_exp_f32 v219, v219\n\t" \
  "v_mfma_f32_16x16x16_bf16 v[228:231], v[252:253], v[238:239], 0\n\t" \
  "v_cvt_pk_bf16_f32 v215, v218, v219\n\t" \
  "v_cvt_pk_bf16_f32 v213, v218, v219\n\t" \
  "v_mov_b32_dpp v212, v214 row_ror:8 row_mask:0xf bank_mask:0xc\n\t" \
  "v_mov_b32_dpp v214, v214 row_ror:8 row_mask:0xf bank_mask:0x3\n\t" \
  "v_mov_b32_dpp v213, v215 row_ror:8 row_mask:0xf bank_mask:0xc\n\t" \
  "v_mov_b32_dpp v215, v215 row_ror:8 row_mask:0xf bank_mask:0x3\n\t" \
  "v_max_u32 v208, v252, v253\n\t" \
  "v_cmp_le_u32 vcc, %[sent], v208\n\t" \
  "v_mfma_f32_16x16x32_bf16 v[200:203], v[220:223], v[212:215], v[200:203]\n\t" \
  ".Lpv_%=:\n\t" \
  "v_mfma_f32_16x16x32_bf16 v[224:227], v[192:195], v[212:215], v[224:227]\n\t" \
  "v_mfma_f32_16x16x32_bf16 v[228:231], v[196:199], v[212:215], v[228:231]\n\t" \
  "s_cbranch_vccnz .Lvs_%=\n\t" \
  ".Lvok_%=:\n\t" \
  "s_nop 4\n\t" \
  "v_rcp_f32_e64 v208, v200\n\t" \
  "v_cndmask_b32 v224, v224, v226, %[nh]\n\t" \
  "v_cndmask_b32 v225, v225, v227, %[nh]\n\t" \
  "v_cndmask_b32 v228, v228, v230, %[nh]\n\t" \
  "v_cndmask_b32 v229, v229, v231, %[nh]\n\t" \
  "v_pk_mul_f32 v[224:225], v[224:225], v[208:209] op_sel_hi:[1,0]\n\t" \
  "v_pk_mul_f32 v[228:229], v[228:229], v[208:209] op_sel_hi:[1,0]\n\t" \
  "v_cvt_pk_bf16_f32 v224, v224, v225\n\t" \
  "v_cvt_pk_bf16_f32 v225, v228, v229\n\t" \
  "buffer_store_dwordx2 v[224:225], v251, %[rs], %[so] offen sc1\n\t" \
  "s_memtime s[90:91]\n\t" \
  "s_setprio 0\n\t" \
  "s_waitcnt lgkmcnt(0)\n\t" \
  "s_mov_b32 %[tout], s90\n\t" \
  "s_mov_b32 %[spins], s92\n\t" \
  "s_branch .Lend_%=\n\t" \
  ".Lls_%=:\n\t" \
  "s_waitcnt lgkmcnt(0)\n\t" \
  "s_barrier\n\t" \
  "s_mov_b32 %[spins], 0\n\t" \
  "s_branch .Lend_%=\n\t" \
  ".Lre_%=:\n\t" \
  "ds_read_b64_tr_b16 v[240:241], v250 offset:1024\n\t" \
  "ds_read_b64_tr_b16 v[242:243], v250 offset:1088\n\t" \
  "ds_read_b64_tr_b16 v[244:245], v250 offset:1536\n\t" \
  "ds_read_b64_tr_b16 v[246:247], v250 offset:1600\n\t" \
  "ds_read_b64_tr_b16 v[232:233], v250 offset:0\n\t" \
  "ds_read_b64_tr_b16 v[234:235], v250 offset:64\n\t" \
  "s_waitcnt lgkmcnt(0)\n\t" \
  "s_branch .Lrb_%=\n\t" \
  ".Lvs_%=:\n\t" \
  "v_cmp_eq_u32 vcc, %[sent], v252\n\t" \
  "v_cmp_eq_u32 s[94:95], %[sent], v253\n\t" \
  "s_or_b64 s[94:95], s[94:95], vcc\n\t" \
  "s_cbranch_scc0 .Lvok_%=\n\t" \
  "s_add_u32 s92, s92, 1\n\t" \
  "s_sleep 2\n\t" \
  "s_and_b32 s93, s92, 63\n\t" \
  "s_cbranch_scc1 .Lvr_%=\n\t" \
  "buffer_load_dword v208, off, %[rs], %[sab] sc1\n\t" \
  "s_waitcnt vmcnt(0)\n\t" \
  "v_readfirstlane_b32 s93, v208\n\t" \
  "s_nop 3\n\t" \
  "s_cmp_eq_u32 s93, 1\n\t" \
  "s_cbranch_scc1 .Lvok_%=\n\t" \
  "s_cmp_lt_u32 s92, 0x80000\n\t" \
  "s_cbranch_scc1 .Lvr_%=\n\t" \
  "v_mov_b32 v208, 1\n\t" \
  "buffer_store_dword v208, off, %[rs], %[sab] sc1\n\t" \
  "s_branch .Lvok_%=\n\t" \
  ".Lvr_%=:\n\t" \
  "buffer_load_dword v252, v249, %[rs], %[sv] offen offset:0 sc1\n\t" \
  "buffer_load_dword v253, v249, %[rs], %[sv] offen offset:256 sc1\n\t" \
  "s_waitcnt vmcnt(0)\n\t" \
  "v_mfma_f32_16x16x16_bf16 v[224:227], v[252:253], v[236:237], 0\n\t" \
  "v_mfma_f32_16x16x16_bf16 v[228:231], v[252:253], v[238:239], 0\n\t" \
  "v_max_u32 v208, v252, v253\n\t" \
  "v_cmp_le_u32 vcc, %[sent], v208\n\t" \
  "s_nop 4\n\t" \
  "s_branch .Lpv_%=\n\t" \
  "s_branch .Lend_%=\n\t" \
  ".Lslow_%=:\n\t" \
  "s_mov_b64 s[94:95], 0\n\t" \
  "v_cmp_eq_u32 vcc, %[sent], v200\n\t" \
  "s_or_b64 s[94:95], s[94:95], vcc\n\t" \
  "v_cmp_eq_u32 vcc, %[sent], v201\n\t" \
  "s_or_b64 s[94:95], s[94:95], vcc\n\t" \
  "v_cmp_eq_u32 vcc, %[sent], v202\n\t" \
  "s_or_b64 s[94:95], s[94:95], vcc\n\t" \
  "v_cmp_eq_u32 vcc, %[sent], v203\n\t" \
  "s_or_b64 s[94:95], s[94:95], vcc\n\t" \
  "v_cmp_eq_u32 vcc, %[sent], v204\n\t" \
  "s_or_b64 s[94:95], s[94:95], vcc\n\t" \
  "v_cmp_eq_u32 vcc, %[sent], v205\n\t" \
  "s_or_b64 s[94:95], s[94:95], vcc\n\t" \
  "v_cmp_eq_u32 vcc, %[sent], v206\n\t" \
  "s_or_b64 s[94:95], s[94:95], vcc\n\t" \
  "v_cmp_eq_u32 vcc, %[sent], v207\n\t" \
  "s_or_b64 s[94:95], s[94:95], vcc\n\t" \
  "s_cmp_eq_u64 s[94:95], 0\n\t" \
  "s_cbranch_scc1 .Lok_%=\n\t" \
  "v_mov_b64 v[192:193], 0\n\t" \
  "v_mov_b64 v[194:195], 0\n\t" \
  "v_mov_b64 v[196:197], 0\n\t" \
  "v_mov_b64 v[198:199], 0\n\t" \
  "s_add_u32 s92, s92, 1\n\t" \
  "s_cmp_eq_u32 s92, 1\n\t" \
  "s_cbranch_scc1 .Lpoll_%=\n\t" \
  "s_sleep 2\n\t" \
  "s_and_b32 s93, s92, 63\n\t" \
  "s_cmp_lg_u32 s93, 0\n\t" \
  "s_cbranch_scc1 .Lpoll_%=\n\t" \
  "v_mov_b32 v245, 0\n\t" \
  "buffer_load_dword v244, v245, %[rs], %[sab] offen sc1\n\t" \
  "s_waitcnt vmcnt(0)\n\t" \
  "v_readfirstlane_b32 s93, v244\n\t" \
  "s_nop 3\n\t" \
  "s_cmp_eq_u32 s93, 1\n\t" \
  "s_cbranch_scc1 .Lok_%=\n\t" \
  "s_cmp_lt_u32 s92, 0x80000\n\t" \
  "s_cbranch_scc1 .Lpoll_%=\n\t" \
  "v_mov_b32 v244, 1\n\t" \
  "buffer_store_dword v244, v245, %[rs], %[sab] offen sc1\n\t" \
  "s_branch .Lok_%=\n\t" \
  ".Lend_%=:\n\t"
#define QK_ASM_2 \
  "s_waitcnt lgkmcnt(0)\n\t" \
  "v_subrev_u32 v244, 0x8000, v250\n\t" \
  "ds_read_b128 v[216:219], v248 offset:40960\n\t" \
  "ds_read_b128 v[220:223], v248 offset:41984\n\t" \
  "ds_read_b128 v[224:227], v248 offset:43008\n\t" \
  "ds_read_b128 v[228:231], v248 offset:44032\n\t" \
  "ds_read_b128 v[208:211], v244 offset:0\n\t" \
  "ds_read_b128 v[212:215], v244 offset:1024\n\t" \
  "ds_read_b128 v[236:239], v244 offset:2048\n\t" \
  "ds_read_b128 v[240:243], v244 offset:3072\n\t" \
  "v_mov_b64 v[192:193], 0\n\t" \
  "v_mov_b64 v[194:195], 0\n\t" \
  "v_mov_b64 v[196:197], 0\n\t" \
  "v_mov_b64 v[198:199], 0\n\t" \
  ".p2align 5\n\t" \
  "s_nop 0\n\t" \
  "s_nop 0\n\t" \
  "s_nop 0\n\t" \
  "s_nop 0\n\t" \
  "s_movk_i32 s92, 0x2000\n\t" \
  ".Lwt_%=:\n\t" \
  "ds_read_b64 v[200:201], v248 offset:30720\n\t" \
  "s_memrealtime s[90:91]\n\t" \
  "s_sub_u32 s92, s92, 1\n\t" \
  "s_waitcnt lgkmcnt(0)\n\t" \
  "v_readfirstlane_b32 s93, v200\n\t" \
  "v_readfirstlane_b32 %[tgt], v201\n\t" \
  "s_cbranch_scc1 .Lgo_%=\n\t" \
  "s_cmp_eq_u32 s93, %[step]\n\t" \
  "s_cbranch_scc1 .Lwx_%=\n\t" \
  "s_sleep 1\n\t" \
  "s_branch .Lwt_%=\n\t" \
  ".Lwx_%=:\n\t" \
  "s_lshl_b32 s93, s90, 2\n\t" \
  "s_sub_i32 s93, %[tgt], s93\n\t" \
  "s_cmp_le_i32 s93, 0\n\t" \
  "s_cbranch_scc1 .Lgo_%=\n\t" \
  "s_min_i32 s93, s93, 8000\n\t" \
  "s_mov_b32 s94, s93\n\t" \
  ".Lsl_%=:\n\t" \
  "s_sub_i32 s94, s94, 18\n\t" \
  "s_cmp_lt_i32 s94, 30\n\t" \
  "s_cbranch_scc1 .Lsx_%=\n\t" \
  "s_sleep 1\n\t" \
  "s_branch .Lsl_%=\n\t" \
  ".Lsx_%=:\n\t" \
  "s_sub_i32 s93, s93, 12\n\t" \
  "s_max_i32 s93, s93, 4\n\t" \
  "s_lshr_b32 s93, s93, 2\n\t" \
  "s_add_u32 s93, s93, s90\n\t" \
  "s_memrealtime s[90:91]\n\t" \
  ".Lpace_%=:\n\t" \
  "s_memrealtime s[94:95]\n\t" \
  "s_waitcnt lgkmcnt(1)\n\t" \
  "s_sub_i32 s92, s90, s93\n\t" \
  "s_cmp_ge_i32 s92, 0\n\t" \
  "s_cbranch_scc1 .Le_%=\n\t" \
  "s_memrealtime s[90:91]\n\t" \
  "s_waitcnt lgkmcnt(1)\n\t" \
  "s_sub_i32 s92, s94, s93\n\t" \
  "s_cmp_lt_i32 s92, 0\n\t" \
  "s_cbranch_scc1 .Lpace_%=\n\t" \
  "s_nop 4\n\t" \
  ".Le_%=:\n\t" \
  "s_add_u32 s92, s92, s93\n\t" \
  "s_lshl_b32 s92, s92, 2\n\t" \
  "s_sub_i32 s92, %[tgt], s92\n\t" \
  "s_max_i32 s92, s92, 0\n\t" \
  "s_bitcmp0_b32 s92, 3\n\t" \
  "s_cbranch_scc1 .Lf3_%=\n\t" \
  "s_nop 15\n\t" \
  ".Lf3_%=:\n\t" \
  "s_bitcmp0_b32 s92, 2\n\t" \
  "s_cbranch_scc1 .Lf2_%=\n\t" \
  "s_nop 10\n\t" \
  ".Lf2_%=:\n\t" \
  ".Lgo_%=:\n\t" \
  "s_mov_b32 s92, 0\n\t" \
  ".Lpoll_%=:\n\t" \
  "buffer_load_dwordx4 v[200:203], v248, %[rs], %[som] offen sc1\n\t" \
  "buffer_load_dwordx4 v[204:207], v248, %[rs], %[som2] offen sc1\n\t" \
  ".p2align 5\n\t" \
  "s_waitcnt vmcnt(1) lgkmcnt(0)\n\t" \
  "ds_write_b128 v249, v[200:203]\n\t" \
  "v_smfmac_f32_16x16x64_bf16 v[192:195], v[200:203], v[208:215], v251\n\t" \
  "v_max3_u32 v244, v200, v201, v202\n\t" \
  "v_smfmac_f32_16x16x64_bf16 v[196:199], v[200:203], v[236:243], v251\n\t" \
  "s_waitcnt vmcnt(0)\n\t" \
  "ds_write_b128 v249, v[204:207] offset:8192\n\t" \
  "v_smfmac_f32_16x16x64_bf16 v[192:195], v[204:207], v[216:223], v251\n\t" \
  "v_max3_u32 v244, v244, v203, v204\n\t" \
  "v_max3_u32 v244, v244, v205, v206\n\t" \
  "v_smfmac_f32_16x16x64_bf16 v[196:199], v[204:207], v[224:231], v251\n\t" \
  "v_max_u32 v244, v244, v207\n\t" \
  "v_cmp_le_u32_e64 vcc, %[sent], v244\n\t" \
  "s_cbranch_vccnz .Lslow_%=\n\t" \
  ".Lok_%=:\n\t" \
  "ds_read_b128 v[216:219], v250 offset:32768\n\t" \
  "ds_read_b128 v[220:223], v250 offset:33792\n\t" \
  "ds_read_b128 v[224:227], v250 offset:34816\n\t" \
  "ds_read_b128 v[228:231], v250 offset:35840\n\t" \
  "s_waitcnt lgkmcnt(4)\n\t" \
  "s_barrier\n\t" \
  "ds_read_b128 v[208:211], v248 offset:3072\n\t" \
  "ds_read_b128 v[212:215], v248 offset:4096\n\t" \
  "ds_read_b128 v[236:239], v248 offset:5120\n\t" \
  "ds_read_b128 v[240:243], v248 offset:6144\n\t" \
  "s_cmp_eq_u32 %[step], 63\n\t" \
  "s_cbranch_scc1 .Lls_%=\n\t" \
  "s_waitcnt lgkmcnt(3)\n\t" \
  "ds_read_b128 v[244:247], v248 offset:7168\n\t" \
  "v_smfmac_f32_16x16x64_bf16 v[192:195], v[208:211], v[216:223], v251\n\t" \
  "ds_read_b128 v[200:203], v248 offset:13312\n\t" \
  "v_smfmac_f32_16x16x64_bf16 v[196:199], v[208:211], v[224:231], v251\n\t" \
  "s_waitcnt lgkmcnt(4)\n\t" \
  "ds_read_b128 v[216:219], v250 offset:0\n\t" \
  "v_smfmac_f32_16x16x64_bf16 v[192:195], v[212:215], v[0:7], v251\n\t" \
  "ds_read_b128 v[220:223], v250 offset:1024\n\t" \
  "v_smfmac_f32_16x16x64_bf16 v[196:199], v[212:215], v[8:15], v251\n\t" \
  "s_waitcnt lgkmcnt(5)\n\t" \
  "ds_read_b128 v[224:227], v250 offset:2048\n\t" \
  "v_smfmac_f32_16x16x64_bf16 v[192:195], v[236:239], v[16:23], v251\n\t" \
  "ds_read_b128 v[228:231], v250 offset:3072\n\t" \
  "v_smfmac_f32_16x16x64_bf16 v[196:199], v[236:239], v[24:31], v251\n\t" \
  "s_waitcnt lgkmcnt(5)\n\t" \
  "ds_read_b128 v[204:207], v248 offset:8192\n\t" \
  "v_smfmac_f32_16x16x64_bf16 v[192:195], v[240:243], v[32:39], v251\n\t" \
  "ds_read_b128 v[208:211], v248 offset:9216\n\t" \
  "v_smfmac_f32_16x16x64_bf16 v[196:199], v[240:243], v[40:47], v251\n\t" \
  "ds_read_b128 v[212:215], v248 offset:11264\n\t" \
  "v_smfmac_f32_16x16x64_bf16 v[192:195], v[244:247], v[48:55], v251\n\t" \
  "ds_read_b128 v[236:239], v248 offset:12288\n\t" \
  "v_smfmac_f32_16x16x64_bf16 v[196:199], v[244:247], v[56:63], v251\n\t" \
  "s_waitcnt lgkmcnt(6)\n\t" \
  "v_smfmac_f32_16x16x64_bf16 v[192:195], v[200:203], v[216:223], v251\n\t" \
  "s_waitcnt lgkmcnt(4)\n\t" \
  "v_smfmac_f32_16x16x64_bf16 v[196:199], v[200:203], v[224:231], v251\n\t" \
  "s_waitcnt lgkmcnt(3)\n\t" \
  "ds_read_b128 v[240:243], v248 offset:14336\n\t" \
  "v_smfmac_f32_16x16x64_bf16 v[192:195], v[204:207], v[64:71], v251\n\t" \
  "ds_read_b128 v[244:247], v248 offset:15360\n\t" \
  "v_smfmac_f32_16x16x64_bf16 v[196:199], v[204:207], v[72:79], v251\n\t" \
  "s_waitcnt lgkmcnt(3)\n\t" \
  "v_smfmac_f32_16x16x64_bf16 v[192:195], v[208:211], v[80:87], v251\n\t" \
  "ds_read_b128 v[200:203], v248 offset:0\n\t" \
  "v_smfmac_f32_16x16x64_bf16 v[196:199], v[208:211], v[88:95], v251\n\t" \
  "ds_read_b128 v[204:207], v248 offset:1024\n\t" \
  "v_smfmac_f32_16x16x64_bf16 v[192:195], v[212:215], v[96:103], v251\n\t" \
  "v_smfmac_f32_16x16x64_bf16 v[196:199], v[212:215], v[104:111], v251\n\t" \
  "s_waitcnt lgkmcnt(3)\n\t" \
  "v_smfmac_f32_16x16x64_bf16 v[192:195], v[236:239], v[112:119], v251\n\t" \
  "v_smfmac_f32_16x16x64_bf16 v[196:199], v[236:239], v[120:127], v251\n\t" \
  "v_smfmac_f32_16x16x64_bf16 v[192:195], v[240:243], v[128:135], v251\n\t" \
  "v_smfmac_f32_16x16x64_bf16 v[196:199], v[240:243], v[136:143], v251\n\t" \
  "s_waitcnt lgkmcnt(0)\n\t" \
  "v_smfmac_f32_16x16x64_bf16 v[192:195], v[244:247], v[144:151], v251\n\t" \
  "v_smfmac_f32_16x16x64_bf16 v[196:199], v[244:247], v[152:159], v251\n\t" \
  "v_smfmac_f32_16x16x64_bf16 v[192:195], v[200:203], v[160:167], v251\n\t" \
  "v_smfmac_f32_16x16x64_bf16 v[196:199], v[200:203], v[168:175], v251\n\t" \
  "v_smfmac_f32_16x16x64_bf16 v[192:195], v[204:207], v[176:183], v251\n\t" \
  "v_smfmac_f32_16x16x64_bf16 v[196:199], v[204:207], v[184:191], v251\n\t" \
  "ds_read_b128 v[200:203], v248 offset:18432\n\t" \
  "ds_read_b128 v[204:207], v248 offset:22528\n\t" \
  "v_mov_b32 v250, v232\n\t" \
  "v_mov_b32 v249, v233\n\t" \
  "s_nop 2\n\t" \
  "v_add_f32 v192, v192, v194\n\t" \
  "v_add_f32 v193, v193, v195\n\t" \
  "v_add_f32 v196, v196, v198\n\t" \
  "v_add_f32_e64 v197, v197, v199\n\t" \
  "v_cvt_pk_bf16_f32 v192, v192, v193\n\t" \
  "v_cvt_pk_bf16_f32 v196, v196, v197\n\t" \
  "ds_write2_b32 v252, v192, v196 offset1:64\n\t" \
  "v_mov_b32_e64 v251, v234\n\t" \
  "ds_read_b64_tr_b16 v[240:241], v250 offset:1024\n\t" \
  "ds_read_b64_tr_b16 v[242:243], v250 offset:1088\n\t" \
  "v_mov_b32_e64 v212, %[step]\n\t" \
  "s_mov_b64 exec, 1\n\t" \
  "ds_write_b32 v248, v212 offset:28704\n\t" \
  "s_mov_b64 exec, -1\n\t" \
  "buffer_load_dwordx4 v[192:195], v248, %[rt], %[svt] offen offset:0 sc1\n\t" \
  "buffer_load_dwordx4 v[196:199], v248, %[rt], %[svt] offen offset:1024 sc1\n\t" \
  "s_waitcnt lgkmcnt(1)\n\t" \
  "v_mfma_f32_16x16x32_bf16 v[200:203], v[200:203], v[240:243], 0\n\t" \
  "v_mfma_f32_16x16x32_bf16 v[204:207], v[204:207], v[240:243], 0\n\t" \
  "s_nop 7\n\t" \
  "v_cndmask_b32 v216, v200, v204, %[nh]\n\t" \
  "v_cndmask_b32 v217, v201, v205, %[nh]\n\t" \
  "v_cndmask_b32 v218, v202, v206, %[nh]\n\t" \
  "v_cndmask_b32 v219, v203, v207, %[nh]\n\t" \
  "ds_write_b128 v248, v[216:219] offset:18432\n\t" \
  "buffer_load_dword v252, v249, %[rs], %[sv] offen offset:0 sc1\n\t" \
  "buffer_load_dword v253, v249, %[rs], %[sv] offen offset:256 sc1\n\t" \
  "ds_read_b32 v212, v248 offset:28672\n\t" \
  "ds_read_b64_tr_b16 v[244:245], v250 offset:1536\n\t" \
  "ds_read_b64_tr_b16 v[246:247], v250 offset:1600\n\t" \
  "ds_read_b64_tr_b16 v[232:233], v250 offset:0\n\t" \
  "ds_read_b64_tr_b16 v[234:235], v250 offset:64\n\t" \
  "ds_read_b64_tr_b16 v[236:237], v250 offset:512\n\t" \
  "ds_read_b64_tr_b16 v[238:239], v250 offset:576\n\t" \
  "s_setprio 3\n\t" \
  "s_waitcnt lgkmcnt(0)\n\t" \
  "v_cmp_ne_u32 vcc, %[step], v212\n\t" \
  "s_and_b32 s93, vcc_lo, 0xf\n\t" \
  "s_barrier\n\t" \
  "s_cbranch_scc1 .Lre_%=\n\t" \
  ".Lrb_%=:\n\t" \
  "ds_read_b64_tr_b16 v[208:209], v250 offset:3072\n\t" \
  "ds_read_b64_tr_b16 v[210:211], v250 offset:3136\n\t" \
  "ds_read_b64_tr_b16 v[212:213], v250 offset:3584\n\t" \
  "ds_read_b64_tr_b16 v[214:215], v250 offset:3648\n\t" \
  "ds_read_b64_tr_b16 v[200:201], v250 offset:2048\n\t" \
  "ds_read_b64_tr_b16 v[202:203], v250 offset:2112\n\t" \
  "ds_read_b64_tr_b16 v[204:205], v250 offset:2560\n\t" \
  "ds_read_b64_tr_b16 v[206:207], v250 offset:2624\n\t" \
  "ds_read_b128 v[220:223], v248 offset:19456\n\t" \
  "ds_read_b128 v[224:227], v248 offset:16384\n\t" \
  "ds_read_b128 v[228:231], v248 offset:17408\n\t" \
  "s_waitcnt lgkmcnt(9)\n\t" \
  "v_mfma_f32_16x16x32_bf16 v[208:211], v[208:211], v[240:243], 0\n\t" \
  "s_waitcnt lgkmcnt(7)\n\t" \
  "v_mfma_f32_16x16x32_bf16 v[208:211], v[212:215], v[244:247], v[208:211]\n\t" \
  "v_mov_b32 v241, 0\n\t" \
  "s_waitcnt lgkmcnt(5)\n\t" \
  "v_mfma_f32_16x16x32_bf16 v[208:211], v[200:203], v[232:235], v[208:211]\n\t" \
  "v_mov_b32 v242, 0\n\t" \
  "s_waitcnt lgkmcnt(3)\n\t" \
  "v_mfma_f32_16x16x32_bf16 v[208:211], v[204:207], v[236:239], v[208:211]\n\t" \
  "s_waitcnt lgkmcnt(2)\n\t" \
  "v_pk_add_f32 v[216:217], v[216:217], v[220:221]\n\t" \
  "v_pk_add_f32 v[218:219], v[218:219], v[222:223]\n\t" \
  "s_waitcnt lgkmcnt(0)\n\t" \
  "v_pk_add_f32 v[224:225], v[224:225], v[228:229]\n\t" \
  "v_pk_add_f32 v[226:227], v[226:227], v[230:231]\n\t" \
  "v_pk_add_f32 v[216:217], v[216:217], v[224:225]\n\t" \
  "v_pk_add_f32 v[218:219], v[218:219], v[226:227]\n\t" \
  "v_max3_f32 v224, v216, v217, v218\n\t" \
  "v_max_f32 v224, v224, v219\n\t" \
  "v_max3_f32 v231, v208, v209, v210\n\t" \
  "v_max_f32 v231, v231, v211\n\t" \
  "v_max_f32_dpp v225, v224, v224 row_ror:8 row_mask:0xf bank_mask:0xf\n\t" \
  "v_max_f32_dpp v226, v224, v224 row_ror:8 row_mask:0xf bank_mask:0xf\n\t" \
  "v_cndmask_b32 v208, v208, v210, %[up]\n\t" \
  "v_cndmask_b32 v209, v209, v211, %[up]\n\t" \
  "v_permlane32_swap_b32_e64 v225, v226\n\t" \
  "v_max3_f32 v213, v225, v226, v231\n\t" \
  "v_max3_f32 v214, v225, v226, v231\n\t" \
  "v_mov_b64 v[220:221], %[one]\n\t" \
  "v_mov_b64 v[222:223], %[one]\n\t" \
  "v_permlane16_swap_b32 v213, v214\n\t" \
  "v_max_f32 v212, v213, v214\n\t" \
  "v_mul_f32_e64 v212, %[negc], v212\n\t" \
  "v_fma_f32 v208, v208, %[c], v212\n\t" \
  "v_fma_f32 v209, v209, %[c], v212\n\t" \
  "v_exp_f32 v208, v208\n\t" \
  "v_fma_f32 v216, v216, %[c], v212\n\t" \
  "v_exp_f32 v209, v209\n\t" \
  "v_fma_f32 v217, v217, %[c], v212\n\t" \
  "v_exp_f32_e64 v216, v216\n\t" \
  "v_fma_f32 v218, v218, %[c], v212\n\t" \
  "v_cvt_pk_bf16_f32 v240, v208, v209\n\t" \
  "v_cvt_pk_bf16_f32 v243, v208, v209\n\t" \
  "v_exp_f32 v217, v217\n\t" \
  "v_fma_f32 v219, v219, %[c], v212\n\t" \
  "v_mfma_f32_16x16x16_bf16 v[204:207], v[220:221], v[240:241], 0\n\t" \
  "v_exp_f32 v218, v218\n\t" \
  "s_waitcnt vmcnt(0)\n\t" \
  "v_mfma_f32_16x16x16_bf16 v[224:227], v[252:253], v[240:241], 0\n\t" \
  "v_cvt_pk_bf16_f32 v202, v216, v217\n\t" \
  "v_cvt_pk_bf16_f32 v200, v216, v217\n\t" \
  "v_exp_f32 v219, v219\n\t" \
  "v_mfma_f32_16x16x16_bf16 v[228:231], v[252:253], v[242:243], 0\n\t" \
  "v_cvt_pk_bf16_f32 v203, v218, v219\n\t" \
  "v_cvt_pk_bf16_f32 v201, v218, v219\n\t" \
  "v_mov_b32_dpp v200, v202 row_ror:8 row_mask:0xf bank_mask:0xc\n\t" \
  "v_mov_b32_dpp v202, v202 row_ror:8 row_mask:0xf bank_mask:0x3\n\t" \
  "v_mov_b32_dpp v201, v203 row_ror:8 row_mask:0xf bank_mask:0xc\n\t" \
  "v_mov_b32_dpp v203, v203 row_ror:8 row_mask:0xf bank_mask:0x3\n\t" \
  "v_max_u32 v212, v252, v253\n\t" \
  "v_cmp_le_u32 vcc, %[sent], v212\n\t" \
  "v_mfma_f32_16x16x32_bf16 v[204:207], v[220:223], v[200:203], v[204:207]\n\t" \
  ".Lpv_%=:\n\t" \
  "v_mfma_f32_16x16x32_bf16 v[224:227], v[192:195], v[200:203], v[224:227]\n\t" \
  "v_mfma_f32_16x16x32_bf16 v[228:231], v[196:199], v[200:203], v[228:231]\n\t" \
  "s_cbranch_vccnz .Lvs_%=\n\t" \
  ".Lvok_%=:\n\t" \
  "s_nop 4\n\t" \
  "v_rcp_f32_e64 v212, v204\n\t" \
  "v_cndmask_b32 v224, v224, v226, %[nh]\n\t" \
  "v_cndmask_b32 v225, v225, v227, %[nh]\n\t" \
  "v_cndmask_b32 v228, v228, v230, %[nh]\n\t" \
  "v_cndmask_b32 v229, v229, v231, %[nh]\n\t" \
  "v_pk_mul_f32 v[224:225], v[224:225], v[212:213] op_sel_hi:[1,0]\n\t" \
  "v_pk_mul_f32 v[228:229], v[228:229], v[212:213] op_sel_hi:[1,0]\n\t" \
  "v_cvt_pk_bf16_f32 v224, v224, v225\n\t" \
  "v_cvt_pk_bf16_f32 v225, v228, v229\n\t" \
  "buffer_store_dwordx2 v[224:225], v251, %[rs], %[so] offen sc1\n\t" \
  "s_memtime s[90:91]\n\t" \
  "s_setprio 0\n\t" \
  "s_waitcnt lgkmcnt(0)\n\t" \
  "s_mov_b32 %[tout], s90\n\t" \
  "s_mov_b32 %[spins], s92\n\t" \
  "s_branch .Lend_%=\n\t" \
  ".Lls_%=:\n\t" \
  "s_waitcnt lgkmcnt(0)\n\t" \
  "s_barrier\n\t" \
  "s_mov_b32 %[spins], 0\n\t" \
  "s_branch .Lend_%=\n\t" \
  ".Lre_%=:\n\t" \
  "ds_read_b64_tr_b16 v[244:245], v250 offset:1536\n\t" \
  "ds_read_b64_tr_b16 v[246:247], v250 offset:1600\n\t" \
  "ds_read_b64_tr_b16 v[232:233], v250 offset:0\n\t" \
  "ds_read_b64_tr_b16 v[234:235], v250 offset:64\n\t" \
  "ds_read_b64_tr_b16 v[236:237], v250 offset:512\n\t" \
  "ds_read_b64_tr_b16 v[238:239], v250 offset:576\n\t" \
  "s_waitcnt lgkmcnt(0)\n\t" \
  "s_branch .Lrb_%=\n\t" \
  ".Lvs_%=:\n\t" \
  "v_cmp_eq_u32 vcc, %[sent], v252\n\t" \
  "v_cmp_eq_u32 s[94:95], %[sent], v253\n\t" \
  "s_or_b64 s[94:95], s[94:95], vcc\n\t" \
  "s_cbranch_scc0 .Lvok_%=\n\t" \
  "s_add_u32 s92, s92, 1\n\t" \
  "s_sleep 2\n\t" \
  "s_and_b32 s93, s92, 63\n\t" \
  "s_cbranch_scc1 .Lvr_%=\n\t" \
  "buffer_load_dword v212, off, %[rs], %[sab] sc1\n\t" \
  "s_waitcnt vmcnt(0)\n\t" \
  "v_readfirstlane_b32 s93, v212\n\t" \
  "s_nop 3\n\t" \
  "s_cmp_eq_u32 s93, 1\n\t" \
  "s_cbranch_scc1 .Lvok_%=\n\t" \
  "s_cmp_lt_u32 s92, 0x80000\n\t" \
  "s_cbranch_scc1 .Lvr_%=\n\t" \
  "v_mov_b32 v212, 1\n\t" \
  "buffer_store_dword v212, off, %[rs], %[sab] sc1\n\t" \
  "s_branch .Lvok_%=\n\t" \
  ".Lvr_%=:\n\t" \
  "buffer_load_dword v252, v249, %[rs], %[sv] offen offset:0 sc1\n\t" \
  "buffer_load_dword v253, v249, %[rs], %[sv] offen offset:256 sc1\n\t" \
  "s_waitcnt vmcnt(0)\n\t" \
  "v_mfma_f32_16x16x16_bf16 v[224:227], v[252:253], v[240:241], 0\n\t" \
  "v_mfma_f32_16x16x16_bf16 v[228:231], v[252:253], v[242:243], 0\n\t" \
  "v_max_u32 v212, v252, v253\n\t" \
  "v_cmp_le_u32 vcc, %[sent], v212\n\t" \
  "s_nop 4\n\t" \
  "s_branch .Lpv_%=\n\t" \
  "s_branch .Lend_%=\n\t" \
  ".Lslow_%=:\n\t" \
  "s_mov_b64 s[94:95], 0\n\t" \
  "v_cmp_eq_u32 vcc, %[sent], v200\n\t" \
  "s_or_b64 s[94:95], s[94:95], vcc\n\t" \
  "v_cmp_eq_u32 vcc, %[sent], v201\n\t" \
  "s_or_b64 s[94:95], s[94:95], vcc\n\t" \
  "v_cmp_eq_u32 vcc, %[sent], v202\n\t" \
  "s_or_b64 s[94:95], s[94:95], vcc\n\t" \
  "v_cmp_eq_u32 vcc, %[sent], v203\n\t" \
  "s_or_b64 s[94:95], s[94:95], vcc\n\t" \
  "v_cmp_eq_u32 vcc, %[sent], v204\n\t" \
  "s_or_b64 s[94:95], s[94:95], vcc\n\t" \
  "v_cmp_eq_u32 vcc, %[sent], v205\n\t" \
  "s_or_b64 s[94:95], s[94:95], vcc\n\t" \
  "v_cmp_eq_u32 vcc, %[sent], v206\n\t" \
  "s_or_b64 s[94:95], s[94:95], vcc\n\t" \
  "v_cmp_eq_u32 vcc, %[sent], v207\n\t" \
  "s_or_b64 s[94:95], s[94:95], vcc\n\t" \
  "s_cmp_eq_u64 s[94:95], 0\n\t" \
  "s_cbranch_scc1 .Lok_%=\n\t" \
  "v_mov_b64 v[192:193], 0\n\t" \
  "v_mov_b64 v[194:195], 0\n\t" \
  "v_mov_b64 v[196:197], 0\n\t" \
  "v_mov_b64 v[198:199], 0\n\t" \
  "s_add_u32 s92, s92, 1\n\t" \
  "s_cmp_eq_u32 s92, 1\n\t" \
  "s_cbranch_scc1 .Lpoll_%=\n\t" \
  "s_sleep 2\n\t" \
  "s_and_b32 s93, s92, 63\n\t" \
  "s_cmp_lg_u32 s93, 0\n\t" \
  "s_cbranch_scc1 .Lpoll_%=\n\t" \
  "v_mov_b32 v245, 0\n\t" \
  "buffer_load_dword v244, v245, %[rs], %[sab] offen sc1\n\t" \
  "s_waitcnt vmcnt(0)\n\t" \
  "v_readfirstlane_b32 s93, v244\n\t" \
  "s_nop 3\n\t" \
  "s_cmp_eq_u32 s93, 1\n\t" \
  "s_cbranch_scc1 .Lok_%=\n\t" \
  "s_cmp_lt_u32 s92, 0x80000\n\t" \
  "s_cbranch_scc1 .Lpoll_%=\n\t" \
  "v_mov_b32 v244, 1\n\t" \
  "buffer_store_dword v244, v245, %[rs], %[sab] offen sc1\n\t" \
  "s_branch .Lok_%=\n\t" \
  ".Lend_%=:\n\t"
#define QK_ASM_3 \
  "s_waitcnt lgkmcnt(0)\n\t" \
  "v_subrev_u32 v244, 0x8000, v250\n\t" \
  "ds_read_b128 v[216:219], v248 offset:45056\n\t" \
  "ds_read_b128 v[220:223], v248 offset:46080\n\t" \
  "ds_read_b128 v[224:227], v248 offset:47104\n\t" \
  "ds_read_b128 v[228:231], v248 offset:48128\n\t" \
  "ds_read_b128 v[208:211], v244 offset:0\n\t" \
  "ds_read_b128 v[212:215], v244 offset:1024\n\t" \
  "ds_read_b128 v[236:239], v244 offset:2048\n\t" \
  "ds_read_b128 v[240:243], v244 offset:3072\n\t" \
  "v_mov_b64 v[192:193], 0\n\t" \
  "v_mov_b64 v[194:195], 0\n\t" \
  "v_mov_b64 v[196:197], 0\n\t" \
  "v_mov_b64 v[198:199], 0\n\t" \
  ".p2align 5\n\t" \
  "s_nop 0\n\t" \
  "s_nop 0\n\t" \
  "s_nop 0\n\t" \
  "s_nop 0\n\t" \
  "s_movk_i32 s92, 0x2000\n\t" \
  ".Lwt_%=:\n\t" \
  "ds_read_b64 v[200:201], v248 offset:30720\n\t" \
  "s_memrealtime s[90:91]\n\t" \
  "s_sub_u32 s92, s92, 1\n\t" \
  "s_waitcnt lgkmcnt(0)\n\t" \
  "v_readfirstlane_b32 s93, v200\n\t" \
  "v_readfirstlane_b32 %[tgt], v201\n\t" \
  "s_cbranch_scc1 .Lgo_%=\n\t" \
  "s_cmp_eq_u32 s93, %[step]\n\t" \
  "s_cbranch_scc1 .Lwx_%=\n\t" \
  "s_sleep 1\n\t" \
  "s_branch .Lwt_%=\n\t" \
  ".Lwx_%=:\n\t" \
  "s_lshl_b32 s93, s90, 2\n\t" \
  "s_sub_i32 s93, %[tgt], s93\n\t" \
  "s_cmp_le_i32 s93, 0\n\t" \
  "s_cbranch_scc1 .Lgo_%=\n\t" \
  "s_min_i32 s93, s93, 8000\n\t" \
  "s_mov_b32 s94, s93\n\t" \
  ".Lsl_%=:\n\t" \
  "s_sub_i32 s94, s94, 18\n\t" \
  "s_cmp_lt_i32 s94, 30\n\t" \
  "s_cbranch_scc1 .Lsx_%=\n\t" \
  "s_sleep 1\n\t" \
  "s_branch .Lsl_%=\n\t" \
  ".Lsx_%=:\n\t" \
  "s_sub_i32 s93, s93, 12\n\t" \
  "s_max_i32 s93, s93, 4\n\t" \
  "s_lshr_b32 s93, s93, 2\n\t" \
  "s_add_u32 s93, s93, s90\n\t" \
  "s_memrealtime s[90:91]\n\t" \
  ".Lpace_%=:\n\t" \
  "s_memrealtime s[94:95]\n\t" \
  "s_waitcnt lgkmcnt(1)\n\t" \
  "s_sub_i32 s92, s90, s93\n\t" \
  "s_cmp_ge_i32 s92, 0\n\t" \
  "s_cbranch_scc1 .Le_%=\n\t" \
  "s_memrealtime s[90:91]\n\t" \
  "s_waitcnt lgkmcnt(1)\n\t" \
  "s_sub_i32 s92, s94, s93\n\t" \
  "s_cmp_lt_i32 s92, 0\n\t" \
  "s_cbranch_scc1 .Lpace_%=\n\t" \
  "s_nop 4\n\t" \
  ".Le_%=:\n\t" \
  "s_add_u32 s92, s92, s93\n\t" \
  "s_lshl_b32 s92, s92, 2\n\t" \
  "s_sub_i32 s92, %[tgt], s92\n\t" \
  "s_max_i32 s92, s92, 0\n\t" \
  "s_bitcmp0_b32 s92, 3\n\t" \
  "s_cbranch_scc1 .Lf3_%=\n\t" \
  "s_nop 15\n\t" \
  ".Lf3_%=:\n\t" \
  "s_bitcmp0_b32 s92, 2\n\t" \
  "s_cbranch_scc1 .Lf2_%=\n\t" \
  "s_nop 10\n\t" \
  ".Lf2_%=:\n\t" \
  ".Lgo_%=:\n\t" \
  "s_mov_b32 s92, 0\n\t" \
  ".Lpoll_%=:\n\t" \
  "buffer_load_dwordx4 v[200:203], v248, %[rs], %[som] offen sc1\n\t" \
  "buffer_load_dwordx4 v[204:207], v248, %[rs], %[som2] offen sc1\n\t" \
  ".p2align 5\n\t" \
  "s_waitcnt vmcnt(1) lgkmcnt(0)\n\t" \
  "ds_write_b128 v249, v[200:203]\n\t" \
  "v_smfmac_f32_16x16x64_bf16 v[192:195], v[200:203], v[208:215], v251\n\t" \
  "v_max3_u32 v244, v200, v201, v202\n\t" \
  "v_smfmac_f32_16x16x64_bf16 v[196:199], v[200:203], v[236:243], v251\n\t" \
  "s_waitcnt vmcnt(0)\n\t" \
  "ds_write_b128 v249, v[204:207] offset:8192\n\t" \
  "v_smfmac_f32_16x16x64_bf16 v[192:195], v[204:207], v[216:223], v251\n\t" \
  "v_max3_u32 v244, v244, v203, v204\n\t" \
  "v_max3_u32 v244, v244, v205, v206\n\t" \
  "v_smfmac_f32_16x16x64_bf16 v[196:199], v[204:207], v[224:231], v251\n\t" \
  "v_max_u32 v244, v244, v207\n\t" \
  "v_cmp_le_u32_e64 vcc, %[sent], v244\n\t" \
  "s_cbranch_vccnz .Lslow_%=\n\t" \
  ".Lok_%=:\n\t" \
  "ds_read_b128 v[216:219], v250 offset:32768\n\t" \
  "ds_read_b128 v[220:223], v250 offset:33792\n\t" \
  "ds_read_b128 v[224:227], v250 offset:34816\n\t" \
  "ds_read_b128 v[228:231], v250 offset:35840\n\t" \
  "s_waitcnt lgkmcnt(4)\n\t" \
  "s_barrier\n\t" \
  "ds_read_b128 v[208:211], v248 offset:2048\n\t" \
  "ds_read_b128 v[212:215], v248 offset:5120\n\t" \
  "ds_read_b128 v[236:239], v248 offset:6144\n\t" \
  "ds_read_b128 v[240:243], v248 offset:7168\n\t" \
  "s_cmp_eq_u32 %[step], 63\n\t" \
  "s_cbranch_scc1 .Lls_%=\n\t" \
  "s_waitcnt lgkmcnt(3)\n\t" \
  "ds_read_b128 v[244:247], v248 offset:8192\n\t" \
  "v_smfmac_f32_16x16x64_bf16 v[192:195], v[208:211], v[216:223], v251\n\t" \
  "ds_read_b128 v[200:203], v248 offset:12288\n\t" \
  "v_smfmac_f32_16x16x64_bf16 v[196:199], v[208:211], v[224:231], v251\n\t" \
  "s_waitcnt lgkmcnt(4)\n\t" \
  "ds_read_b128 v[216:219], v250 offset:0\n\t" \
  "v_smfmac_f32_16x16x64_bf16 v[192:195], v[212:215], v[0:7], v251\n\t" \
  "ds_read_b128 v[220:223], v250 offset:1024\n\t" \
  "v_smfmac_f32_16x16x64_bf16 v[196:199], v[212:215], v[8:15], v251\n\t" \
  "s_waitcnt lgkmcnt(5)\n\t" \
  "ds_read_b128 v[224:227], v250 offset:2048\n\t" \
  "v_smfmac_f32_16x16x64_bf16 v[192:195], v[236:239], v[16:23], v251\n\t" \
  "ds_read_b128 v[228:231], v250 offset:3072\n\t" \
  "v_smfmac_f32_16x16x64_bf16 v[196:199], v[236:239], v[24:31], v251\n\t" \
  "s_waitcnt lgkmcnt(5)\n\t" \
  "ds_read_b128 v[204:207], v248 offset:9216\n\t" \
  "v_smfmac_f32_16x16x64_bf16 v[192:195], v[240:243], v[32:39], v251\n\t" \
  "ds_read_b128 v[208:211], v248 offset:10240\n\t" \
  "v_smfmac_f32_16x16x64_bf16 v[196:199], v[240:243], v[40:47], v251\n\t" \
  "ds_read_b128 v[212:215], v248 offset:13312\n\t" \
  "v_smfmac_f32_16x16x64_bf16 v[192:195], v[244:247], v[48:55], v251\n\t" \
  "ds_read_b128 v[236:239], v248 offset:14336\n\t" \
  "v_smfmac_f32_16x16x64_bf16 v[196:199], v[244:247], v[56:63], v251\n\t" \
  "s_waitcnt lgkmcnt(6)\n\t" \
  "v_smfmac_f32_16x16x64_bf16 v[192:195], v[200:203], v[216:223], v251\n\t" \
  "s_waitcnt lgkmcnt(4)\n\t" \
  "v_smfmac_f32_16x16x64_bf16 v[196:199], v[200:203], v[224:231], v251\n\t" \
  "s_waitcnt lgkmcnt(3)\n\t" \
  "ds_read_b128 v[240:243], v248 offset:15360\n\t" \
  "v_smfmac_f32_16x16x64_bf16 v[192:195], v[204:207], v[64:71], v251\n\t" \
  "ds_read_b128 v[244:247], v248 offset:0\n\t" \
  "v_smfmac_f32_16x16x64_bf16 v[196:199], v[204:207], v[72:79], v251\n\t" \
  "s_waitcnt lgkmcnt(3)\n\t" \
  "v_smfmac_f32_16x16x64_bf16 v[192:195], v[208:211], v[80:87], v251\n\t" \
  "ds_read_b128 v[200:203], v248 offset:1024\n\t" \
  "v_smfmac_f32_16x16x64_bf16 v[196:199], v[208:211], v[88:95], v251\n\t" \
  "ds_read_b128 v[204:207], v248 offset:4096\n\t" \
  "v_smfmac_f32_16x16x64_bf16 v[192:195], v[212:215], v[96:103], v251\n\t" \
  "v_smfmac_f32_16x16x64_bf16 v[196:199], v[212:215], v[104:111], v251\n\t" \
  "s_waitcnt lgkmcnt(3)\n\t" \
  "v_smfmac_f32_16x16x64_bf16 v[192:195], v[236:239], v[112:119], v251\n\t" \
  "v_smfmac_f32_16x16x64_bf16 v[196:199], v[236:239], v[120:127], v251\n\t" \
  "v_smfmac_f32_16x16x64_bf16 v[192:195], v[240:243], v[128:135], v251\n\t" \
  "v_smfmac_f32_16x16x64_bf16 v[196:199], v[240:243], v[136:143], v251\n\t" \
  "s_waitcnt lgkmcnt(0)\n\t" \
  "v_smfmac_f32_16x16x64_bf16 v[192:195], v[244:247], v[144:151], v251\n\t" \
  "v_smfmac_f32_16x16x64_bf16 v[196:199], v[244:247], v[152:159], v251\n\t" \
  "v_smfmac_f32_16x16x64_bf16 v[192:195], v[200:203], v[160:167], v251\n\t" \
  "v_smfmac_f32_16x16x64_bf16 v[196:199], v[200:203], v[168:175], v251\n\t" \
  "v_smfmac_f32_16x16x64_bf16 v[192:195], v[204:207], v[176:183], v251\n\t" \
  "v_smfmac_f32_16x16x64_bf16 v[196:199], v[204:207], v[184:191], v251\n\t" \
  "ds_read_b128 v[200:203], v248 offset:19456\n\t" \
  "ds_read_b128 v[204:207], v248 offset:23552\n\t" \
  "v_mov_b32 v250, v232\n\t" \
  "v_mov_b32 v249, v233\n\t" \
  "s_nop 2\n\t" \
  "v_add_f32 v192, v192, v194\n\t" \
  "v_add_f32 v193, v193, v195\n\t" \
  "v_add_f32 v196, v196, v198\n\t" \
  "v_add_f32_e64 v197, v197, v199\n\t" \
  "v_cvt_pk_bf16_f32 v192, v192, v193\n\t" \
  "v_cvt_pk_bf16_f32 v196, v196, v197\n\t" \
  "ds_write2_b32 v252, v192, v196 offset1:64\n\t" \
  "v_mov_b32_e64 v251, v234\n\t" \
  "ds_read_b64_tr_b16 v[244:245], v250 offset:1536\n\t" \
  "ds_read_b64_tr_b16 v[246:247], v250 offset:1600\n\t" \
  "v_mov_b32_e64 v212, %[step]\n\t" \
  "s_mov_b64 exec, 1\n\t" \
  "ds_write_b32 v248, v212 offset:28720\n\t" \
  "s_mov_b64 exec, -1\n\t" \
  "buffer_load_dwordx4 v[192:195], v248, %[rt], %[svt] offen offset:0 sc1\n\t" \
  "buffer_load_dwordx4 v[196:199], v248, %[rt], %[svt] offen offset:1024 sc1\n\t" \
  "s_waitcnt lgkmcnt(1)\n\t" \
  "v_mfma_f32_16x16x32_bf16 v[200:203], v[200:203], v[244:247], 0\n\t" \
  "v_mfma_f32_16x16x32_bf16 v[204:207], v[204:207], v[244:247], 0\n\t" \
  "s_nop 7\n\t" \
  "v_cndmask_b32 v216, v200, v204, %[nh]\n\t" \
  "v_cndmask_b32 v217, v201, v205, %[nh]\n\t" \
  "v_cndmask_b32 v218, v202, v206, %[nh]\n\t" \
  "v_cndmask_b32 v219, v203, v207, %[nh]\n\t" \
  "ds_write_b128 v248, v[216:219] offset:19456\n\t" \
  "buffer_load_dword v252, v249, %[rs], %[sv] offen offset:0 sc1\n\t" \
  "buffer_load_dword v253, v249, %[rs], %[sv] offen offset:256 sc1\n\t" \
  "ds_read_b32 v212, v248 offset:28672\n\t" \
  "ds_read_b64_tr_b16 v[232:233], v250 offset:0\n\t" \
  "ds_read_b64_tr_b16 v[234:235], v250 offset:64\n\t" \
  "ds_read_b64_tr_b16 v[236:237], v250 offset:512\n\t" \
  "ds_read_b64_tr_b16 v[238:239], v250 offset:576\n\t" \
  "ds_read_b64_tr_b16 v[240:241], v250 offset:1024\n\t" \
  "ds_read_b64_tr_b16 v[242:243], v250 offset:1088\n\t" \
  "s_setprio 3\n\t" \
  "s_waitcnt lgkmcnt(0)\n\t" \
  "v_cmp_ne_u32 vcc, %[step], v212\n\t" \
  "s_and_b32 s93, vcc_lo, 0xf\n\t" \
  "s_barrier\n\t" \
  "s_cbranch_scc1 .Lre_%=\n\t" \
  ".Lrb_%=:\n\t" \
  "ds_read_b64_tr_b16 v[212:213], v250 offset:3584\n\t" \
  "ds_read_b64_tr_b16 v[214:215], v250 offset:3648\n\t" \
  "ds_read_b64_tr_b16 v[200:201], v250 offset:2048\n\t" \
  "ds_read_b64_tr_b16 v[202:203], v250 offset:2112\n\t" \
  "ds_read_b64_tr_b16 v[204:205], v250 offset:2560\n\t" \
  "ds_read_b64_tr_b16 v[206:207], v250 offset:2624\n\t" \
  "ds_read_b64_tr_b16 v[208:209], v250 offset:3072\n\t" \
  "ds_read_b64_tr_b16 v[210:211], v250 offset:3136\n\t" \
  "ds_read_b128 v[220:223], v248 offset:16384\n\t" \
  "ds_read_b128 v[224:227], v248 offset:17408\n\t" \
  "ds_read_b128 v[228:231], v248 offset:18432\n\t" \
  "s_waitcnt lgkmcnt(9)\n\t" \
  "v_mfma_f32_16x16x32_bf16 v[212:215], v[212:215], v[244:247], 0\n\t" \
  "s_waitcnt lgkmcnt(7)\n\t" \
  "v_mfma_f32_16x16x32_bf16 v[212:215], v[200:203], v[232:235], v[212:215]\n\t" \
  "v_mov_b32 v245, 0\n\t" \
  "s_waitcnt lgkmcnt(5)\n\t" \
  "v_mfma_f32_16x16x32_bf16 v[212:215], v[204:207], v[236:239], v[212:215]\n\t" \
  "v_mov_b32 v246, 0\n\t" \
  "s_waitcnt lgkmcnt(3)\n\t" \
  "v_mfma_f32_16x16x32_bf16 v[212:215], v[208:211], v[240:243], v[212:215]\n\t" \
  "s_waitcnt lgkmcnt(2)\n\t" \
  "v_pk_add_f32 v[216:217], v[216:217], v[220:221]\n\t" \
  "v_pk_add_f32 v[218:219], v[218:219], v[222:223]\n\t" \
  "s_waitcnt lgkmcnt(0)\n\t" \
  "v_pk_add_f32 v[224:225], v[224:225], v[228:229]\n\t" \
  "v_pk_add_f32 v[226:227], v[226:227], v[230:231]\n\t" \
  "v_pk_add_f32 v[216:217], v[216:217], v[224:225]\n\t" \
  "v_pk_add_f32 v[218:219], v[218:219], v[226:227]\n\t" \
  "v_max3_f32 v224, v216, v217, v218\n\t" \
  "v_max_f32 v224, v224, v219\n\t" \
  "v_max3_f32 v231, v212, v213, v214\n\t" \
  "v_max_f32 v231, v231, v215\n\t" \
  "v_max_f32_dpp v225, v224, v224 row_ror:8 row_mask:0xf bank_mask:0xf\n\t" \
  "v_max_f32_dpp v226, v224, v224 row_ror:8 row_mask:0xf bank_mask:0xf\n\t" \
  "v_cndmask_b32 v212, v212, v214, %[up]\n\t" \
  "v_cndmask_b32 v213, v213, v215, %[up]\n\t" \
  "v_permlane32_swap_b32_e64 v225, v226\n\t" \
  "v_max3_f32 v201, v225, v226, v231\n\t" \
  "v_max3_f32 v202, v225, v226, v231\n\t" \
  "v_mov_b64 v[220:221], %[one]\n\t" \
  "v_mov_b64 v[222:223], %[one]\n\t" \
  "v_permlane16_swap_b32 v201, v202\n\t" \
  "v_max_f32 v200, v201, v202\n\t" \
  "v_mul_f32_e64 v200, %[negc], v200\n\t" \
  "v_fma_f32 v212, v212, %[c], v200\n\t" \
  "v_fma_f32 v213, v213, %[c], v200\n\t" \
  "v_exp_f32 v212, v212\n\t" \
  "v_fma_f32 v216, v216, %[c], v200\n\t" \
  "v_exp_f32 v213, v213\n\t" \
  "v_fma_f32 v217, v217, %[c], v200\n\t" \
  "v_exp_f32_e64 v216, v216\n\t" \
  "v_fma_f32 v218, v218, %[c], v200\n\t" \
  "v_cvt_pk_bf16_f32 v244, v212, v213\n\t" \
  "v_cvt_pk_bf16_f32 v247, v212, v213\n\t" \
  "v_exp_f32 v217, v217\n\t" \
  "v_fma_f32 v219, v219, %[c], v200\n\t" \
  "v_mfma_f32_16x16x16_bf16 v[208:211], v[220:221], v[244:245], 0\n\t" \
  "v_exp_f32 v218, v218\n\t" \
  "s_waitcnt vmcnt(0)\n\t" \
  "v_mfma_f32_16x16x16_bf16 v[224:227], v[252:253], v[244:245], 0\n\t" \
  "v_cvt_pk_bf16_f32 v206, v216, v217\n\t" \
  "v_cvt_pk_bf16_f32 v204, v216, v217\n\t" \
  "v_exp_f32 v219, v219\n\t" \
  "v_mfma_f32_16x16x16_bf16 v[228:231], v[252:253], v[246:247], 0\n\t" \
  "v_cvt_pk_bf16_f32 v207, v218, v219\n\t" \
  "v_cvt_pk_bf16_f32 v205, v218, v219\n\t" \
  "v_mov_b32_dpp v204, v206 row_ror:8 row_mask:0xf bank_mask:0xc\n\t" \
  "v_mov_b32_dpp v206, v206 row_ror:8 row_mask:0xf bank_mask:0x3\n\t" \
  "v_mov_b32_dpp v205, v207 row_ror:8 row_mask:0xf bank_mask:0xc\n\t" \
  "v_mov_b32_dpp v207, v207 row_ror:8 row_mask:0xf bank_mask:0x3\n\t" \
  "v_max_u32 v200, v252, v253\n\t" \
  "v_cmp_le_u32 vcc, %[sent], v200\n\t" \
  "v_mfma_f32_16x16x32_bf16 v[208:211], v[220:223], v[204:207], v[208:211]\n\t" \
  ".Lpv_%=:\n\t" \
  "v_mfma_f32_16x16x32_bf16 v[224:227], v[192:195], v[204:207], v[224:227]\n\t" \
  "v_mfma_f32_16x16x32_bf16 v[228:231], v[196:199], v[204:207], v[228:231]\n\t" \
  "s_cbranch_vccnz .Lvs_%=\n\t" \
  ".Lvok_%=:\n\t" \
  "s_nop 4\n\t" \
  "v_rcp_f32_e64 v200, v208\n\t" \
  "v_cndmask_b32 v224, v224, v226, %[nh]\n\t" \
  "v_cndmask_b32 v225, v225, v227, %[nh]\n\t" \
  "v_cndmask_b32 v228, v228, v230, %[nh]\n\t" \
  "v_cndmask_b32 v229, v229, v231, %[nh]\n\t" \
  "v_pk_mul_f32 v[224:225], v[224:225], v[200:201] op_sel_hi:[1,0]\n\t" \
  "v_pk_mul_f32 v[228:229], v[228:229], v[200:201] op_sel_hi:[1,0]\n\t" \
  "v_cvt_pk_bf16_f32 v224, v224, v225\n\t" \
  "v_cvt_pk_bf16_f32 v225, v228, v229\n\t" \
  "buffer_store_dwordx2 v[224:225], v251, %[rs], %[so] offen sc1\n\t" \
  "s_memtime s[90:91]\n\t" \
  "s_setprio 0\n\t" \
  "s_waitcnt lgkmcnt(0)\n\t" \
  "s_mov_b32 %[tout], s90\n\t" \
  "s_mov_b32 %[spins], s92\n\t" \
  "s_branch .Lend_%=\n\t" \
  ".Lls_%=:\n\t" \
  "s_waitcnt lgkmcnt(0)\n\t" \
  "s_barrier\n\t" \
  "s_mov_b32 %[spins], 0\n\t" \
  "s_branch .Lend_%=\n\t" \
  ".Lre_%=:\n\t" \
  "ds_read_b64_tr_b16 v[232:233], v250 offset:0\n\t" \
  "ds_read_b64_tr_b16 v[234:235], v250 offset:64\n\t" \
  "ds_read_b64_tr_b16 v[236:237], v250 offset:512\n\t" \
  "ds_read_b64_tr_b16 v[238:239], v250 offset:576\n\t" \
  "ds_read_b64_tr_b16 v[240:241], v250 offset:1024\n\t" \
  "ds_read_b64_tr_b16 v[242:243], v250 offset:1088\n\t" \
  "s_waitcnt lgkmcnt(0)\n\t" \
  "s_branch .Lrb_%=\n\t" \
  ".Lvs_%=:\n\t" \
  "v_cmp_eq_u32 vcc, %[sent], v252\n\t" \
  "v_cmp_eq_u32 s[94:95], %[sent], v253\n\t" \
  "s_or_b64 s[94:95], s[94:95], vcc\n\t" \
  "s_cbranch_scc0 .Lvok_%=\n\t" \
  "s_add_u32 s92, s92, 1\n\t" \
  "s_sleep 2\n\t" \
  "s_and_b32 s93, s92, 63\n\t" \
  "s_cbranch_scc1 .Lvr_%=\n\t" \
  "buffer_load_dword v200, off, %[rs], %[sab] sc1\n\t" \
  "s_waitcnt vmcnt(0)\n\t" \
  "v_readfirstlane_b32 s93, v200\n\t" \
  "s_nop 3\n\t" \
  "s_cmp_eq_u32 s93, 1\n\t" \
  "s_cbranch_scc1 .Lvok_%=\n\t" \
  "s_cmp_lt_u32 s92, 0x80000\n\t" \
  "s_cbranch_scc1 .Lvr_%=\n\t" \
  "v_mov_b32 v200, 1\n\t" \
  "buffer_store_dword v200, off, %[rs], %[sab] sc1\n\t" \
  "s_branch .Lvok_%=\n\t" \
  ".Lvr_%=:\n\t" \
  "buffer_load_dword v252, v249, %[rs], %[sv] offen offset:0 sc1\n\t" \
  "buffer_load_dword v253, v249, %[rs], %[sv] offen offset:256 sc1\n\t" \
  "s_waitcnt vmcnt(0)\n\t" \
  "v_mfma_f32_16x16x16_bf16 v[224:227], v[252:253], v[244:245], 0\n\t" \
  "v_mfma_f32_16x16x16_bf16 v[228:231], v[252:253], v[246:247], 0\n\t" \
  "v_max_u32 v200, v252, v253\n\t" \
  "v_cmp_le_u32 vcc, %[sent], v200\n\t" \
  "s_nop 4\n\t" \
  "s_branch .Lpv_%=\n\t" \
  "s_branch .Lend_%=\n\t" \
  ".Lslow_%=:\n\t" \
  "s_mov_b64 s[94:95], 0\n\t" \
  "v_cmp_eq_u32 vcc, %[sent], v200\n\t" \
  "s_or_b64 s[94:95], s[94:95], vcc\n\t" \
  "v_cmp_eq_u32 vcc, %[sent], v201\n\t" \
  "s_or_b64 s[94:95], s[94:95], vcc\n\t" \
  "v_cmp_eq_u32 vcc, %[sent], v202\n\t" \
  "s_or_b64 s[94:95], s[94:95], vcc\n\t" \
  "v_cmp_eq_u32 vcc, %[sent], v203\n\t" \
  "s_or_b64 s[94:95], s[94:95], vcc\n\t" \
  "v_cmp_eq_u32 vcc, %[sent], v204\n\t" \
  "s_or_b64 s[94:95], s[94:95], vcc\n\t" \
  "v_cmp_eq_u32 vcc, %[sent], v205\n\t" \
  "s_or_b64 s[94:95], s[94:95], vcc\n\t" \
  "v_cmp_eq_u32 vcc, %[sent], v206\n\t" \
  "s_or_b64 s[94:95], s[94:95], vcc\n\t" \
  "v_cmp_eq_u32 vcc, %[sent], v207\n\t" \
  "s_or_b64 s[94:95], s[94:95], vcc\n\t" \
  "s_cmp_eq_u64 s[94:95], 0\n\t" \
  "s_cbranch_scc1 .Lok_%=\n\t" \
  "v_mov_b64 v[192:193], 0\n\t" \
  "v_mov_b64 v[194:195], 0\n\t" \
  "v_mov_b64 v[196:197], 0\n\t" \
  "v_mov_b64 v[198:199], 0\n\t" \
  "s_add_u32 s92, s92, 1\n\t" \
  "s_cmp_eq_u32 s92, 1\n\t" \
  "s_cbranch_scc1 .Lpoll_%=\n\t" \
  "s_sleep 2\n\t" \
  "s_and_b32 s93, s92, 63\n\t" \
  "s_cmp_lg_u32 s93, 0\n\t" \
  "s_cbranch_scc1 .Lpoll_%=\n\t" \
  "v_mov_b32 v245, 0\n\t" \
  "buffer_load_dword v244, v245, %[rs], %[sab] offen sc1\n\t" \
  "s_waitcnt vmcnt(0)\n\t" \
  "v_readfirstlane_b32 s93, v244\n\t" \
  "s_nop 3\n\t" \
  "s_cmp_eq_u32 s93, 1\n\t" \
  "s_cbranch_scc1 .Lok_%=\n\t" \
  "s_cmp_lt_u32 s92, 0x80000\n\t" \
  "s_cbranch_scc1 .Lpoll_%=\n\t" \
  "v_mov_b32 v244, 1\n\t" \
  "buffer_store_dword v244, v245, %[rs], %[sab] offen sc1\n\t" \
  "s_branch .Lok_%=\n\t" \
  ".Lend_%=:\n\t"
#define QK_ASM_4 \
  "s_waitcnt lgkmcnt(0)\n\t" \
  "v_subrev_u32 v244, 0x8000, v250\n\t" \
  "ds_read_b128 v[216:219], v248 offset:49152\n\t" \
  "ds_read_b128 v[220:223], v248 offset:50176\n\t" \
  "ds_read_b128 v[224:227], v248 offset:51200\n\t" \
  "ds_read_b128 v[228:231], v248 offset:52224\n\t" \
  "ds_read_b128 v[208:211], v244 offset:0\n\t" \
  "ds_read_b128 v[212:215], v244 offset:1024\n\t" \
  "ds_read_b128 v[236:239], v244 offset:2048\n\t" \
  "ds_read_b128 v[240:243], v244 offset:3072\n\t" \
  "v_mov_b64 v[192:193], 0\n\t" \
  "v_mov_b64 v[194:195], 0\n\t" \
  "v_mov_b64 v[196:197], 0\n\t" \
  "v_mov_b64 v[198:199], 0\n\t" \
  ".p2align 5\n\t" \
  "s_nop 0\n\t" \
  "s_nop 0\n\t" \
  "s_nop 0\n\t" \
  "s_nop 0\n\t" \
  "s_movk_i32 s92, 0x2000\n\t" \
  ".Lwt_%=:\n\t" \
  "ds_read_b64 v[200:201], v248 offset:30720\n\t" \
  "s_memrealtime s[90:91]\n\t" \
  "s_sub_u32 s92, s92, 1\n\t" \
  "s_waitcnt lgkmcnt(0)\n\t" \
  "v_readfirstlane_b32 s93, v200\n\t" \
  "v_readfirstlane_b32 %[tgt], v201\n\t" \
  "s_cbranch_scc1 .Lgo_%=\n\t" \
  "s_cmp_eq_u32 s93, %[step]\n\t" \
  "s_cbranch_scc1 .Lwx_%=\n\t" \
  "s_sleep 1\n\t" \
  "s_branch .Lwt_%=\n\t" \
  ".Lwx_%=:\n\t" \
  "s_lshl_b32 s93, s90, 2\n\t" \
  "s_sub_i32 s93, %[tgt], s93\n\t" \
  "s_cmp_le_i32 s93, 0\n\t" \
  "s_cbranch_scc1 .Lgo_%=\n\t" \
  "s_min_i32 s93, s93, 8000\n\t" \
  "s_mov_b32 s94, s93\n\t" \
  ".Lsl_%=:\n\t" \
  "s_sub_i32 s94, s94, 18\n\t" \
  "s_cmp_lt_i32 s94, 30\n\t" \
  "s_cbranch_scc1 .Lsx_%=\n\t" \
  "s_sleep 1\n\t" \
  "s_branch .Lsl_%=\n\t" \
  ".Lsx_%=:\n\t" \
  "s_sub_i32 s93, s93, 12\n\t" \
  "s_max_i32 s93, s93, 4\n\t" \
  "s_lshr_b32 s93, s93, 2\n\t" \
  "s_add_u32 s93, s93, s90\n\t" \
  "s_memrealtime s[90:91]\n\t" \
  ".Lpace_%=:\n\t" \
  "s_memrealtime s[94:95]\n\t" \
  "s_waitcnt lgkmcnt(1)\n\t" \
  "s_sub_i32 s92, s90, s93\n\t" \
  "s_cmp_ge_i32 s92, 0\n\t" \
  "s_cbranch_scc1 .Le_%=\n\t" \
  "s_memrealtime s[90:91]\n\t" \
  "s_waitcnt lgkmcnt(1)\n\t" \
  "s_sub_i32 s92, s94, s93\n\t" \
  "s_cmp_lt_i32 s92, 0\n\t" \
  "s_cbranch_scc1 .Lpace_%=\n\t" \
  "s_nop 4\n\t" \
  ".Le_%=:\n\t" \
  "s_add_u32 s92, s92, s93\n\t" \
  "s_lshl_b32 s92, s92, 2\n\t" \
  "s_sub_i32 s92, %[tgt], s92\n\t" \
  "s_max_i32 s92, s92, 0\n\t" \
  "s_bitcmp0_b32 s92, 3\n\t" \
  "s_cbranch_scc1 .Lf3_%=\n\t" \
  "s_nop 15\n\t" \
  ".Lf3_%=:\n\t" \
  "s_bitcmp0_b32 s92, 2\n\t" \
  "s_cbranch_scc1 .Lf2_%=\n\t" \
  "s_nop 10\n\t" \
  ".Lf2_%=:\n\t" \
  ".Lgo_%=:\n\t" \
  "s_mov_b32 s92, 0\n\t" \
  ".Lpoll_%=:\n\t" \
  "buffer_load_dwordx4 v[200:203], v248, %[rs], %[som] offen sc1\n\t" \
  "buffer_load_dwordx4 v[204:207], v248, %[rs], %[som2] offen sc1\n\t" \
  ".p2align 5\n\t" \
  "s_nop 0\n\t" \
  "s_nop 0\n\t" \
  "s_nop 0\n\t" \
  "s_nop 0\n\t" \
  "s_nop 0\n\t" \
  "s_nop 0\n\t" \
  "s_nop 0\n\t" \
  "s_waitcnt vmcnt(1) lgkmcnt(0)\n\t" \
  "ds_write_b128 v249, v[200:203]\n\t" \
  "v_smfmac_f32_16x16x64_bf16 v[192:195], v[200:203], v[208:215], v251\n\t" \
  "v_max3_u32 v244, v200, v201, v202\n\t" \
  "v_smfmac_f32_16x16x64_bf16 v[196:199], v[200:203], v[236:243], v251\n\t" \
  "s_waitcnt vmcnt(0)\n\t" \
  "ds_write_b128 v249, v[204:207] offset:8192\n\t" \
  "v_smfmac_f32_16x16x64_bf16 v[192:195], v[204:207], v[216:223], v251\n\t" \
  "v_max3_u32 v244, v244, v203, v204\n\t" \
  "v_max3_u32 v244, v244, v205, v206\n\t" \
  "v_smfmac_f32_16x16x64_bf16 v[196:199], v[204:207], v[224:231], v251\n\t" \
  "v_max_u32 v244, v244, v207\n\t" \
  "v_cmp_le_u32 vcc, %[sent], v244\n\t" \
  "s_cbranch_vccnz .Lslow_%=\n\t" \
  ".Lok_%=:\n\t" \
  "s_waitcnt lgkmcnt(0)\n\t" \
  "s_barrier\n\t" \
  "ds_read_b128 v[208:211], v248 offset:6144\n\t" \
  "ds_read_b128 v[212:215], v248 offset:7168\n\t" \
  "ds_read_b128 v[236:239], v248 offset:8192\n\t" \
  "ds_read_b128 v[240:243], v248 offset:9216\n\t" \
  "ds_read_b128 v[244:247], v248 offset:10240\n\t" \
  "s_waitcnt lgkmcnt(4)\n\t" \
  "ds_read_b128 v[200:203], v248 offset:5120\n\t" \
  "v_smfmac_f32_16x16x64_bf16 v[192:195], v[208:211], v[0:7], v251\n\t" \
  "ds_read_b128 v[216:219], v250 offset:32768\n\t" \
  "v_smfmac_f32_16x16x64_bf16 v[196:199], v[208:211], v[8:15], v251\n\t" \
  "s_waitcnt lgkmcnt(5)\n\t" \
  "ds_read_b128 v[220:223], v250 offset:33792\n\t" \
  "v_smfmac_f32_16x16x64_bf16 v[192:195], v[212:215], v[16:23], v251\n\t" \
  "ds_read_b128 v[224:227], v250 offset:34816\n\t" \
  "v_smfmac_f32_16x16x64_bf16 v[196:199], v[212:215], v[24:31], v251\n\t" \
  "s_waitcnt lgkmcnt(6)\n\t" \
  "ds_read_b128 v[228:231], v250 offset:35840\n\t" \
  "v_smfmac_f32_16x16x64_bf16 v[192:195], v[236:239], v[32:39], v251\n\t" \
  "ds_read_b128 v[204:207], v248 offset:11264\n\t" \
  "v_smfmac_f32_16x16x64_bf16 v[196:199], v[236:239], v[40:47], v251\n\t" \
  "s_waitcnt lgkmcnt(7)\n\t" \
  "ds_read_b128 v[208:211], v248 offset:14336\n\t" \
  "v_smfmac_f32_16x16x64_bf16 v[192:195], v[240:243], v[48:55], v251\n\t" \
  "ds_read_b128 v[212:215], v248 offset:15360\n\t" \
  "v_smfmac_f32_16x16x64_bf16 v[196:199], v[240:243], v[56:63], v251\n\t" \
  "s_waitcnt lgkmcnt(7)\n\t" \
  "v_smfmac_f32_16x16x64_bf16 v[192:195], v[244:247], v[64:71], v251\n\t" \
  "ds_read_b128 v[236:239], v248 offset:0\n\t" \
  "v_smfmac_f32_16x16x64_bf16 v[196:199], v[244:247], v[72:79], v251\n\t" \
  "s_waitcnt lgkmcnt(6)\n\t" \
  "v_smfmac_f32_16x16x64_bf16 v[192:195], v[200:203], v[216:223], v251\n\t" \
  "s_waitcnt lgkmcnt(4)\n\t" \
  "ds_read_b128 v[240:243], v248 offset:13312\n\t" \
  "v_smfmac_f32_16x16x64_bf16 v[196:199], v[200:203], v[224:231], v251\n\t" \
  "s_waitcnt lgkmcnt(4)\n\t" \
  "ds_read_b128 v[216:219], v250 offset:0\n\t" \
  "v_smfmac_f32_16x16x64_bf16 v[192:195], v[204:207], v[80:87], v251\n\t" \
  "ds_read_b128 v[220:223], v250 offset:1024\n\t" \
  "v_smfmac_f32_16x16x64_bf16 v[196:199], v[204:207], v[88:95], v251\n\t" \
  "s_waitcnt lgkmcnt(4)\n\t" \
  "ds_read_b128 v[224:227], v250 offset:2048\n\t" \
  "v_smfmac_f32_16x16x64_bf16 v[192:195], v[208:211], v[96:103], v251\n\t" \
  "ds_read_b128 v[228:231], v250 offset:3072\n\t" \
  "v_smfmac_f32_16x16x64_bf16 v[196:199], v[208:211], v[104:111], v251\n\t" \
  "ds_read_b128 v[244:247], v248 offset:1024\n\t" \
  "v_smfmac_f32_16x16x64_bf16 v[192:195], v[212:215], v[112:119], v251\n\t" \
  "ds_read_b128 v[200:203], v248 offset:2048\n\t" \
  "v_smfmac_f32_16x16x64_bf16 v[196:199], v[212:215], v[120:127], v251\n\t" \
  "s_waitcnt lgkmcnt(6)\n\t" \
  "v_smfmac_f32_16x16x64_bf16 v[192:195], v[236:239], v[128:135], v251\n\t" \
  "ds_read_b128 v[204:207], v248 offset:3072\n\t" \
  "v_smfmac_f32_16x16x64_bf16 v[196:199], v[236:239], v[136:143], v251\n\t" \
  "s_waitcnt lgkmcnt(5)\n\t" \
  "v_smfmac_f32_16x16x64_bf16 v[192:195], v[240:243], v[216:223], v251\n\t" \
  "s_waitcnt lgkmcnt(3)\n\t" \
  "v_smfmac_f32_16x16x64_bf16 v[196:199], v[240:243], v[224:231], v251\n\t" \
  "s_waitcnt lgkmcnt(2)\n\t" \
  "v_smfmac_f32_16x16x64_bf16 v[192:195], v[244:247], v[144:151], v251\n\t" \
  "v_smfmac_f32_16x16x64_bf16 v[196:199], v[244:247], v[152:159], v251\n\t" \
  "s_waitcnt lgkmcnt(1)\n\t" \
  "v_smfmac_f32_16x16x64_bf16 v[192:195], v[200:203], v[160:167], v251\n\t" \
  "v_smfmac_f32_16x16x64_bf16 v[196:199], v[200:203], v[168:175], v251\n\t" \
  "s_waitcnt lgkmcnt(0)\n\t" \
  "v_smfmac_f32_16x16x64_bf16 v[192:195], v[204:207], v[176:183], v251\n\t" \
  "v_smfmac_f32_16x16x64_bf16 v[196:199], v[204:207], v[184:191], v251\n\t" \
  "s_nop 6\n\t" \
  "v_pk_add_f32 v[192:193], v[192:193], v[194:195]\n\t" \
  "v_pk_add_f32 v[196:197], v[196:197], v[198:199]\n\t" \
  "v_cvt_pk_bf16_f32 v192, v192, v193\n\t" \
  "v_cvt_pk_bf16_f32 v196, v196, v197\n\t" \
  "ds_write2_b32 v252, v192, v196 offset1:64\n\t" \
  "s_waitcnt lgkmcnt(0)\n\t" \
  "s_barrier\n\t" \
  "buffer_store_dword v192, v235, %[rs], %[sk] offen sc1\n\t" \
  "buffer_store_dword v196, v235, %[rs], %[sk] offen offset:256 sc1\n\t" \
  "s_mov_b32 %[spins], s92\n\t" \
  "s_branch .Lend_%=\n\t" \
  ".Lslow_%=:\n\t" \
  "s_mov_b64 s[94:95], 0\n\t" \
  "v_cmp_eq_u32 vcc, %[sent], v200\n\t" \
  "s_or_b64 s[94:95], s[94:95], vcc\n\t" \
  "v_cmp_eq_u32 vcc, %[sent], v201\n\t" \
  "s_or_b64 s[94:95], s[94:95], vcc\n\t" \
  "v_cmp_eq_u32 vcc, %[sent], v202\n\t" \
  "s_or_b64 s[94:95], s[94:95], vcc\n\t" \
  "v_cmp_eq_u32 vcc, %[sent], v203\n\t" \
  "s_or_b64 s[94:95], s[94:95], vcc\n\t" \
  "v_cmp_eq_u32 vcc, %[sent], v204\n\t" \
  "s_or_b64 s[94:95], s[94:95], vcc\n\t" \
  "v_cmp_eq_u32 vcc, %[sent], v205\n\t" \
  "s_or_b64 s[94:95], s[94:95], vcc\n\t" \
  "v_cmp_eq_u32 vcc, %[sent], v206\n\t" \
  "s_or_b64 s[94:95], s[94:95], vcc\n\t" \
  "v_cmp_eq_u32 vcc, %[sent], v207\n\t" \
  "s_or_b64 s[94:95], s[94:95], vcc\n\t" \
  "s_cmp_eq_u64 s[94:95], 0\n\t" \
  "s_cbranch_scc1 .Lok_%=\n\t" \
  "v_mov_b64 v[192:193], 0\n\t" \
  "v_mov_b64 v[194:195], 0\n\t" \
  "v_mov_b64 v[196:197], 0\n\t" \
  "v_mov_b64 v[198:199], 0\n\t" \
  "s_add_u32 s92, s92, 1\n\t" \
  "s_cmp_eq_u32 s92, 1\n\t" \
  "s_cbranch_scc1 .Lpoll_%=\n\t" \
  "s_sleep 2\n\t" \
  "s_and_b32 s93, s92, 63\n\t" \
  "s_cmp_lg_u32 s93, 0\n\t" \
  "s_cbranch_scc1 .Lpoll_%=\n\t" \
  "v_mov_b32 v245, 0\n\t" \
  "buffer_load_dword v244, v245, %[rs], %[sab] offen sc1\n\t" \
  "s_waitcnt vmcnt(0)\n\t" \
  "v_readfirstlane_b32 s93, v244\n\t" \
  "s_nop 3\n\t" \
  "s_cmp_eq_u32 s93, 1\n\t" \
  "s_cbranch_scc1 .Lok_%=\n\t" \
  "s_cmp_lt_u32 s92, 0x80000\n\t" \
  "s_cbranch_scc1 .Lpoll_%=\n\t" \
  "v_mov_b32 v244, 1\n\t" \
  "buffer_store_dword v244, v245, %[rs], %[sab] offen sc1\n\t" \
  "s_branch .Lok_%=\n\t" \
  ".Lend_%=:\n\t"
#define QK_ASM_5 \
  "s_waitcnt lgkmcnt(0)\n\t" \
  "v_subrev_u32 v244, 0x8000, v250\n\t" \
  "ds_read_b128 v[216:219], v248 offset:53248\n\t" \
  "ds_read_b128 v[220:223], v248 offset:54272\n\t" \
  "ds_read_b128 v[224:227], v248 offset:55296\n\t" \
  "ds_read_b128 v[228:231], v248 offset:56320\n\t" \
  "ds_read_b128 v[208:211], v244 offset:0\n\t" \
  "ds_read_b128 v[212:215], v244 offset:1024\n\t" \
  "ds_read_b128 v[236:239], v244 offset:2048\n\t" \
  "ds_read_b128 v[240:243], v244 offset:3072\n\t" \
  "v_mov_b64 v[192:193], 0\n\t" \
  "v_mov_b64 v[194:195], 0\n\t" \
  "v_mov_b64 v[196:197], 0\n\t" \
  "v_mov_b64 v[198:199], 0\n\t" \
  ".p2align 5\n\t" \
  "s_nop 0\n\t" \
  "s_nop 0\n\t" \
  "s_nop 0\n\t" \
  "s_nop 0\n\t" \
  "s_movk_i32 s92, 0x2000\n\t" \
  ".Lwt_%=:\n\t" \
  "ds_read_b64 v[200:201], v248 offset:30720\n\t" \
  "s_memrealtime s[90:91]\n\t" \
  "s_sub_u32 s92, s92, 1\n\t" \
  "s_waitcnt lgkmcnt(0)\n\t" \
  "v_readfirstlane_b32 s93, v200\n\t" \
  "v_readfirstlane_b32 %[tgt], v201\n\t" \
  "s_cbranch_scc1 .Lgo_%=\n\t" \
  "s_cmp_eq_u32 s93, %[step]\n\t" \
  "s_cbranch_scc1 .Lwx_%=\n\t" \
  "s_sleep 1\n\t" \
  "s_branch .Lwt_%=\n\t" \
  ".Lwx_%=:\n\t" \
  "s_lshl_b32 s93, s90, 2\n\t" \
  "s_sub_i32 s93, %[tgt], s93\n\t" \
  "s_cmp_le_i32 s93, 0\n\t" \
  "s_cbranch_scc1 .Lgo_%=\n\t" \
  "s_min_i32 s93, s93, 8000\n\t" \
  "s_mov_b32 s94, s93\n\t" \
  ".Lsl_%=:\n\t" \
  "s_sub_i32 s94, s94, 18\n\t" \
  "s_cmp_lt_i32 s94, 30\n\t" \
  "s_cbranch_scc1 .Lsx_%=\n\t" \
  "s_sleep 1\n\t" \
  "s_branch .Lsl_%=\n\t" \
  ".Lsx_%=:\n\t" \
  "s_sub_i32 s93, s93, 12\n\t" \
  "s_max_i32 s93, s93, 4\n\t" \
  "s_lshr_b32 s93, s93, 2\n\t" \
  "s_add_u32 s93, s93, s90\n\t" \
  "s_memrealtime s[90:91]\n\t" \
  ".Lpace_%=:\n\t" \
  "s_memrealtime s[94:95]\n\t" \
  "s_waitcnt lgkmcnt(1)\n\t" \
  "s_sub_i32 s92, s90, s93\n\t" \
  "s_cmp_ge_i32 s92, 0\n\t" \
  "s_cbranch_scc1 .Le_%=\n\t" \
  "s_memrealtime s[90:91]\n\t" \
  "s_waitcnt lgkmcnt(1)\n\t" \
  "s_sub_i32 s92, s94, s93\n\t" \
  "s_cmp_lt_i32 s92, 0\n\t" \
  "s_cbranch_scc1 .Lpace_%=\n\t" \
  "s_nop 4\n\t" \
  ".Le_%=:\n\t" \
  "s_add_u32 s92, s92, s93\n\t" \
  "s_lshl_b32 s92, s92, 2\n\t" \
  "s_sub_i32 s92, %[tgt], s92\n\t" \
  "s_max_i32 s92, s92, 0\n\t" \
  "s_bitcmp0_b32 s92, 3\n\t" \
  "s_cbranch_scc1 .Lf3_%=\n\t" \
  "s_nop 15\n\t" \
  ".Lf3_%=:\n\t" \
  "s_bitcmp0_b32 s92, 2\n\t" \
  "s_cbranch_scc1 .Lf2_%=\n\t" \
  "s_nop 10\n\t" \
  ".Lf2_%=:\n\t" \
  ".Lgo_%=:\n\t" \
  "s_mov_b32 s92, 0\n\t" \
  ".Lpoll_%=:\n\t" \
  "buffer_load_dwordx4 v[200:203], v248, %[rs], %[som] offen sc1\n\t" \
  "buffer_load_dwordx4 v[204:207], v248, %[rs], %[som2] offen sc1\n\t" \
  ".p2align 5\n\t" \
  "s_nop 0\n\t" \
  "s_nop 0\n\t" \
  "s_nop 0\n\t" \
  "s_nop 0\n\t" \
  "s_nop 0\n\t" \
  "s_nop 0\n\t" \
  "s_nop 0\n\t" \
  "s_waitcnt vmcnt(1) lgkmcnt(0)\n\t" \
  "ds_write_b128 v249, v[200:203]\n\t" \
  "v_smfmac_f32_16x16x64_bf16 v[192:195], v[200:203], v[208:215], v251\n\t" \
  "v_max3_u32 v244, v200, v201, v202\n\t" \
  "v_smfmac_f32_16x16x64_bf16 v[196:199], v[200:203], v[236:243], v251\n\t" \
  "s_waitcnt vmcnt(0)\n\t" \
  "ds_write_b128 v249, v[204:207] offset:8192\n\t" \
  "v_smfmac_f32_16x16x64_bf16 v[192:195], v[204:207], v[216:223], v251\n\t" \
  "v_max3_u32 v244, v244, v203, v204\n\t" \
  "v_max3_u32 v244, v244, v205, v206\n\t" \
  "v_smfmac_f32_16x16x64_bf16 v[196:199], v[204:207], v[224:231], v251\n\t" \
  "v_max_u32 v244, v244, v207\n\t" \
  "v_cmp_le_u32 vcc, %[sent], v244\n\t" \
  "s_cbranch_vccnz .Lslow_%=\n\t" \
  ".Lok_%=:\n\t" \
  "s_waitcnt lgkmcnt(0)\n\t" \
  "s_barrier\n\t" \
  "ds_read_b128 v[208:211], v248 offset:7168\n\t" \
  "ds_read_b128 v[212:215], v248 offset:8192\n\t" \
  "ds_read_b128 v[236:239], v248 offset:9216\n\t" \
  "ds_read_b128 v[240:243], v248 offset:10240\n\t" \
  "ds_read_b128 v[244:247], v248 offset:11264\n\t" \
  "s_waitcnt lgkmcnt(4)\n\t" \
  "ds_read_b128 v[200:203], v248 offset:4096\n\t" \
  "v_smfmac_f32_16x16x64_bf16 v[192:195], v[208:211], v[0:7], v251\n\t" \
  "ds_read_b128 v[216:219], v250 offset:32768\n\t" \
  "v_smfmac_f32_16x16x64_bf16 v[196:199], v[208:211], v[8:15], v251\n\t" \
  "s_waitcnt lgkmcnt(5)\n\t" \
  "ds_read_b128 v[220:223], v250 offset:33792\n\t" \
  "v_smfmac_f32_16x16x64_bf16 v[192:195], v[212:215], v[16:23], v251\n\t" \
  "ds_read_b128 v[224:227], v250 offset:34816\n\t" \
  "v_smfmac_f32_16x16x64_bf16 v[196:199], v[212:215], v[24:31], v251\n\t" \
  "s_waitcnt lgkmcnt(6)\n\t" \
  "ds_read_b128 v[228:231], v250 offset:35840\n\t" \
  "v_smfmac_f32_16x16x64_bf16 v[192:195], v[236:239], v[32:39], v251\n\t" \
  "ds_read_b128 v[204:207], v248 offset:14336\n\t" \
  "v_smfmac_f32_16x16x64_bf16 v[196:199], v[236:239], v[40:47], v251\n\t" \
  "s_waitcnt lgkmcnt(7)\n\t" \
  "ds_read_b128 v[208:211], v248 offset:15360\n\t" \
  "v_smfmac_f32_16x16x64_bf16 v[192:195], v[240:243], v[48:55], v251\n\t" \
  "ds_read_b128 v[212:215], v248 offset:0\n\t" \
  "v_smfmac_f32_16x16x64_bf16 v[196:199], v[240:243], v[56:63], v251\n\t" \
  "s_waitcnt lgkmcnt(7)\n\t" \
  "v_smfmac_f32_16x16x64_bf16 v[192:195], v[244:247], v[64:71], v251\n\t" \
  "ds_read_b128 v[236:239], v248 offset:1024\n\t" \
  "v_smfmac_f32_16x16x64_bf16 v[196:199], v[244:247], v[72:79], v251\n\t" \
  "s_waitcnt lgkmcnt(6)\n\t" \
  "v_smfmac_f32_16x16x64_bf16 v[192:195], v[200:203], v[216:223], v251\n\t" \
  "s_waitcnt lgkmcnt(4)\n\t" \
  "ds_read_b128 v[240:243], v248 offset:12288\n\t" \
  "v_smfmac_f32_16x16x64_bf16 v[196:199], v[200:203], v[224:231], v251\n\t" \
  "s_waitcnt lgkmcnt(4)\n\t" \
  "ds_read_b128 v[216:219], v250 offset:0\n\t" \
  "v_smfmac_f32_16x16x64_bf16 v[192:195], v[204:207], v[80:87], v251\n\t" \
  "ds_read_b128 v[220:223], v250 offset:1024\n\t" \
  "v_smfmac_f32_16x16x64_bf16 v[196:199], v[204:207], v[88:95], v251\n\t" \
  "s_waitcnt lgkmcnt(4)\n\t" \
  "ds_read_b128 v[224:227], v250 offset:2048\n\t" \
  "v_smfmac_f32_16x16x64_bf16 v[192:195], v[208:211], v[96:103], v251\n\t" \
  "ds_read_b128 v[228:231], v250 offset:3072\n\t" \
  "v_smfmac_f32_16x16x64_bf16 v[196:199], v[208:211], v[104:111], v251\n\t" \
  "ds_read_b128 v[244:247], v248 offset:2048\n\t" \
  "v_smfmac_f32_16x16x64_bf16 v[192:195], v[212:215], v[112:119], v251\n\t" \
  "ds_read_b128 v[200:203], v248 offset:3072\n\t" \
  "v_smfmac_f32_16x16x64_bf16 v[196:199], v[212:215], v[120:127], v251\n\t" \
  "s_waitcnt lgkmcnt(6)\n\t" \
  "v_smfmac_f32_16x16x64_bf16 v[192:195], v[236:239], v[128:135], v251\n\t" \
  "ds_read_b128 v[204:207], v248 offset:6144\n\t" \
  "v_smfmac_f32_16x16x64_bf16 v[196:199], v[236:239], v[136:143], v251\n\t" \
  "s_waitcnt lgkmcnt(5)\n\t" \
  "v_smfmac_f32_16x16x64_bf16 v[192:195], v[240:243], v[216:223], v251\n\t" \
  "s_waitcnt lgkmcnt(3)\n\t" \
  "v_smfmac_f32_16x16x64_bf16 v[196:199], v[240:243], v[224:231], v251\n\t" \
  "s_waitcnt lgkmcnt(2)\n\t" \
  "v_smfmac_f32_16x16x64_bf16 v[192:195], v[244:247], v[144:151], v251\n\t" \
  "v_smfmac_f32_16x16x64_bf16 v[196:199], v[244:247], v[152:159], v251\n\t" \
  "s_waitcnt lgkmcnt(1)\n\t" \
  "v_smfmac_f32_16x16x64_bf16 v[192:195], v[200:203], v[160:167], v251\n\t" \
  "v_smfmac_f32_16x16x64_bf16 v[196:199], v[200:203], v[168:175], v251\n\t" \
  "s_waitcnt lgkmcnt(0)\n\t" \
  "v_smfmac_f32_16x16x64_bf16 v[192:195], v[204:207], v[176:183], v251\n\t" \
  "v_smfmac_f32_16x16x64_bf16 v[196:199], v[204:207], v[184:191], v251\n\t" \
  "s_nop 6\n\t" \
  "v_pk_add_f32 v[192:193], v[192:193], v[194:195]\n\t" \
  "v_pk_add_f32 v[196:197], v[196:197], v[198:199]\n\t" \
  "v_cvt_pk_bf16_f32 v192, v192, v193\n\t" \
  "v_cvt_pk_bf16_f32 v196, v196, v197\n\t" \
  "ds_write2_b32 v252, v192, v196 offset1:64\n\t" \
  "s_waitcnt lgkmcnt(0)\n\t" \
  "s_barrier\n\t" \
  "buffer_store_dword v192, v235, %[rs], %[sk] offen sc1\n\t" \
  "buffer_store_dword v196, v235, %[rs], %[sk] offen offset:256 sc1\n\t" \
  "s_mov_b32 %[spins], s92\n\t" \
  "s_branch .Lend_%=\n\t" \
  ".Lslow_%=:\n\t" \
  "s_mov_b64 s[94:95], 0\n\t" \
  "v_cmp_eq_u32 vcc, %[sent], v200\n\t" \
  "s_or_b64 s[94:95], s[94:95], vcc\n\t" \
  "v_cmp_eq_u32 vcc, %[sent], v201\n\t" \
  "s_or_b64 s[94:95], s[94:95], vcc\n\t" \
  "v_cmp_eq_u32 vcc, %[sent], v202\n\t" \
  "s_or_b64 s[94:95], s[94:95], vcc\n\t" \
  "v_cmp_eq_u32 vcc, %[sent], v203\n\t" \
  "s_or_b64 s[94:95], s[94:95], vcc\n\t" \
  "v_cmp_eq_u32 vcc, %[sent], v204\n\t" \
  "s_or_b64 s[94:95], s[94:95], vcc\n\t" \
  "v_cmp_eq_u32 vcc, %[sent], v205\n\t" \
  "s_or_b64 s[94:95], s[94:95], vcc\n\t" \
  "v_cmp_eq_u32 vcc, %[sent], v206\n\t" \
  "s_or_b64 s[94:95], s[94:95], vcc\n\t" \
  "v_cmp_eq_u32 vcc, %[sent], v207\n\t" \
  "s_or_b64 s[94:95], s[94:95], vcc\n\t" \
  "s_cmp_eq_u64 s[94:95], 0\n\t" \
  "s_cbranch_scc1 .Lok_%=\n\t" \
  "v_mov_b64 v[192:193], 0\n\t" \
  "v_mov_b64 v[194:195], 0\n\t" \
  "v_mov_b64 v[196:197], 0\n\t" \
  "v_mov_b64 v[198:199], 0\n\t" \
  "s_add_u32 s92, s92, 1\n\t" \
  "s_cmp_eq_u32 s92, 1\n\t" \
  "s_cbranch_scc1 .Lpoll_%=\n\t" \
  "s_sleep 2\n\t" \
  "s_and_b32 s93, s92, 63\n\t" \
  "s_cmp_lg_u32 s93, 0\n\t" \
  "s_cbranch_scc1 .Lpoll_%=\n\t" \
  "v_mov_b32 v245, 0\n\t" \
  "buffer_load_dword v244, v245, %[rs], %[sab] offen sc1\n\t" \
  "s_waitcnt vmcnt(0)\n\t" \
  "v_readfirstlane_b32 s93, v244\n\t" \
  "s_nop 3\n\t" \
  "s_cmp_eq_u32 s93, 1\n\t" \
  "s_cbranch_scc1 .Lok_%=\n\t" \
  "s_cmp_lt_u32 s92, 0x80000\n\t" \
  "s_cbranch_scc1 .Lpoll_%=\n\t" \
  "v_mov_b32 v244, 1\n\t" \
  "buffer_store_dword v244, v245, %[rs], %[sab] offen sc1\n\t" \
  "s_branch .Lok_%=\n\t" \
  ".Lend_%=:\n\t"
#define QK_ASM_6 \
  "s_waitcnt lgkmcnt(0)\n\t" \
  "v_subrev_u32 v244, 0x8000, v250\n\t" \
  "ds_read_b128 v[216:219], v248 offset:57344\n\t" \
  "ds_read_b128 v[220:223], v248 offset:58368\n\t" \
  "ds_read_b128 v[224:227], v248 offset:59392\n\t" \
  "ds_read_b128 v[228:231], v248 offset:60416\n\t" \
  "ds_read_b128 v[208:211], v244 offset:0\n\t" \
  "ds_read_b128 v[212:215], v244 offset:1024\n\t" \
  "ds_read_b128 v[236:239], v244 offset:2048\n\t" \
  "ds_read_b128 v[240:243], v244 offset:3072\n\t" \
  "v_mov_b64 v[192:193], 0\n\t" \
  "v_mov_b64 v[194:195], 0\n\t" \
  "v_mov_b64 v[196:197], 0\n\t" \
  "v_mov_b64 v[198:199], 0\n\t" \
  ".p2align 5\n\t" \
  "s_nop 0\n\t" \
  "s_nop 0\n\t" \
  "s_nop 0\n\t" \
  "s_nop 0\n\t" \
  "s_movk_i32 s92, 0x2000\n\t" \
  ".Lwt_%=:\n\t" \
  "ds_read_b64 v[200:201], v248 offset:30720\n\t" \
  "s_memrealtime s[90:91]\n\t" \
  "s_sub_u32 s92, s92, 1\n\t" \
  "s_waitcnt lgkmcnt(0)\n\t" \
  "v_readfirstlane_b32 s93, v200\n\t" \
  "v_readfirstlane_b32 %[tgt], v201\n\t" \
  "s_cbranch_scc1 .Lgo_%=\n\t" \
  "s_cmp_eq_u32 s93, %[step]\n\t" \
  "s_cbranch_scc1 .Lwx_%=\n\t" \
  "s_sleep 1\n\t" \
  "s_branch .Lwt_%=\n\t" \
  ".Lwx_%=:\n\t" \
  "s_lshl_b32 s93, s90, 2\n\t" \
  "s_sub_i32 s93, %[tgt], s93\n\t" \
  "s_cmp_le_i32 s93, 0\n\t" \
  "s_cbranch_scc1 .Lgo_%=\n\t" \
  "s_min_i32 s93, s93, 8000\n\t" \
  "s_mov_b32 s94, s93\n\t" \
  ".Lsl_%=:\n\t" \
  "s_sub_i32 s94, s94, 18\n\t" \
  "s_cmp_lt_i32 s94, 30\n\t" \
  "s_cbranch_scc1 .Lsx_%=\n\t" \
  "s_sleep 1\n\t" \
  "s_branch .Lsl_%=\n\t" \
  ".Lsx_%=:\n\t" \
  "s_sub_i32 s93, s93, 12\n\t" \
  "s_max_i32 s93, s93, 4\n\t" \
  "s_lshr_b32 s93, s93, 2\n\t" \
  "s_add_u32 s93, s93, s90\n\t" \
  "s_memrealtime s[90:91]\n\t" \
  ".Lpace_%=:\n\t" \
  "s_memrealtime s[94:95]\n\t" \
  "s_waitcnt lgkmcnt(1)\n\t" \
  "s_sub_i32 s92, s90, s93\n\t" \
  "s_cmp_ge_i32 s92, 0\n\t" \
  "s_cbranch_scc1 .Le_%=\n\t" \
  "s_memrealtime s[90:91]\n\t" \
  "s_waitcnt lgkmcnt(1)\n\t" \
  "s_sub_i32 s92, s94, s93\n\t" \
  "s_cmp_lt_i32 s92, 0\n\t" \
  "s_cbranch_scc1 .Lpace_%=\n\t" \
  "s_nop 4\n\t" \
  ".Le_%=:\n\t" \
  "s_add_u32 s92, s92, s93\n\t" \
  "s_lshl_b32 s92, s92, 2\n\t" \
  "s_sub_i32 s92, %[tgt], s92\n\t" \
  "s_max_i32 s92, s92, 0\n\t" \
  "s_bitcmp0_b32 s92, 3\n\t" \
  "s_cbranch_scc1 .Lf3_%=\n\t" \
  "s_nop 15\n\t" \
  ".Lf3_%=:\n\t" \
  "s_bitcmp0_b32 s92, 2\n\t" \
  "s_cbranch_scc1 .Lf2_%=\n\t" \
  "s_nop 10\n\t" \
  ".Lf2_%=:\n\t" \
  ".Lgo_%=:\n\t" \
  "s_mov_b32 s92, 0\n\t" \
  ".Lpoll_%=:\n\t" \
  "buffer_load_dwordx4 v[200:203], v248, %[rs], %[som] offen sc1\n\t" \
  "buffer_load_dwordx4 v[204:207], v248, %[rs], %[som2] offen sc1\n\t" \
  ".p2align 5\n\t" \
  "s_nop 0\n\t" \
  "s_nop 0\n\t" \
  "s_nop 0\n\t" \
  "s_nop 0\n\t" \
  "s_nop 0\n\t" \
  "s_nop 0\n\t" \
  "s_nop 0\n\t" \
  "s_waitcnt vmcnt(1) lgkmcnt(0)\n\t" \
  "ds_write_b128 v249, v[200:203]\n\t" \
  "v_smfmac_f32_16x16x64_bf16 v[192:195], v[200:203], v[208:215], v251\n\t" \
  "v_max3_u32 v244, v200, v201, v202\n\t" \
  "v_smfmac_f32_16x16x64_bf16 v[196:199], v[200:203], v[236:243], v251\n\t" \
  "s_waitcnt vmcnt(0)\n\t" \
  "ds_write_b128 v249, v[204:207] offset:8192\n\t" \
  "v_smfmac_f32_16x16x64_bf16 v[192:195], v[204:207], v[216:223], v251\n\t" \
  "v_max3_u32 v244, v244, v203, v204\n\t" \
  "v_max3_u32 v244, v244, v205, v206\n\t" \
  "v_smfmac_f32_16x16x64_bf16 v[196:199], v[204:207], v[224:231], v251\n\t" \
  "v_max_u32 v244, v244, v207\n\t" \
  "v_cmp_le_u32 vcc, %[sent], v244\n\t" \
  "s_cbranch_vccnz .Lslow_%=\n\t" \
  ".Lok_%=:\n\t" \
  "s_waitcnt lgkmcnt(0)\n\t" \
  "s_barrier\n\t" \
  "ds_read_b128 v[208:211], v248 offset:8192\n\t" \
  "ds_read_b128 v[212:215], v248 offset:9216\n\t" \
  "ds_read_b128 v[236:239], v248 offset:10240\n\t" \
  "ds_read_b128 v[240:243], v248 offset:11264\n\t" \
  "ds_read_b128 v[244:247], v248 offset:12288\n\t" \
  "s_waitcnt lgkmcnt(4)\n\t" \
  "ds_read_b128 v[200:203], v248 offset:7168\n\t" \
  "v_smfmac_f32_16x16x64_bf16 v[192:195], v[208:211], v[0:7], v251\n\t" \
  "ds_read_b128 v[216:219], v250 offset:32768\n\t" \
  "v_smfmac_f32_16x16x64_bf16 v[196:199], v[208:211], v[8:15], v251\n\t" \
  "s_waitcnt lgkmcnt(5)\n\t" \
  "ds_read_b128 v[220:223], v250 offset:33792\n\t" \
  "v_smfmac_f32_16x16x64_bf16 v[192:195], v[212:215], v[16:23], v251\n\t" \
  "ds_read_b128 v[224:227], v250 offset:34816\n\t" \
  "v_smfmac_f32_16x16x64_bf16 v[196:199], v[212:215], v[24:31], v251\n\t" \
  "s_waitcnt lgkmcnt(6)\n\t" \
  "ds_read_b128 v[228:231], v250 offset:35840\n\t" \
  "v_smfmac_f32_16x16x64_bf16 v[192:195], v[236:239], v[32:39], v251\n\t" \
  "ds_read_b128 v[204:207], v248 offset:15360\n\t" \
  "v_smfmac_f32_16x16x64_bf16 v[196:199], v[236:239], v[40:47], v251\n\t" \
  "s_waitcnt lgkmcnt(7)\n\t" \
  "ds_read_b128 v[208:211], v248 offset:0\n\t" \
  "v_smfmac_f32_16x16x64_bf16 v[192:195], v[240:243], v[48:55], v251\n\t" \
  "ds_read_b128 v[212:215], v248 offset:1024\n\t" \
  "v_smfmac_f32_16x16x64_bf16 v[196:199], v[240:243], v[56:63], v251\n\t" \
  "s_waitcnt lgkmcnt(7)\n\t" \
  "v_smfmac_f32_16x16x64_bf16 v[192:195], v[244:247], v[64:71], v251\n\t" \
  "ds_read_b128 v[236:239], v248 offset:2048\n\t" \
  "v_smfmac_f32_16x16x64_bf16 v[196:199], v[244:247], v[72:79], v251\n\t" \
  "s_waitcnt lgkmcnt(6)\n\t" \
  "v_smfmac_f32_16x16x64_bf16 v[192:195], v[200:203], v[216:223], v251\n\t" \
  "s_waitcnt lgkmcnt(4)\n\t" \
  "ds_read_b128 v[240:243], v248 offset:13312\n\t" \
  "v_smfmac_f32_16x16x64_bf16 v[196:199], v[200:203], v[224:231], v251\n\t" \
  "s_waitcnt lgkmcnt(4)\n\t" \
  "ds_read_b128 v[216:219], v250 offset:0\n\t" \
  "v_smfmac_f32_16x16x64_bf16 v[192:195], v[204:207], v[80:87], v251\n\t" \
  "ds_read_b128 v[220:223], v250 offset:1024\n\t" \
  "v_smfmac_f32_16x16x64_bf16 v[196:199], v[204:207], v[88:95], v251\n\t" \
  "s_waitcnt lgkmcnt(4)\n\t" \
  "ds_read_b128 v[224:227], v250 offset:2048\n\t" \
  "v_smfmac_f32_16x16x64_bf16 v[192:195], v[208:211], v[96:103], v251\n\t" \
  "ds_read_b128 v[228:231], v250 offset:3072\n\t" \
  "v_smfmac_f32_16x16x64_bf16 v[196:199], v[208:211], v[104:111], v251\n\t" \
  "ds_read_b128 v[244:247], v248 offset:3072\n\t" \
  "v_smfmac_f32_16x16x64_bf16 v[192:195], v[212:215], v[112:119], v251\n\t" \
  "ds_read_b128 v[200:203], v248 offset:4096\n\t" \
  "v_smfmac_f32_16x16x64_bf16 v[196:199], v[212:215], v[120:127], v251\n\t" \
  "s_waitcnt lgkmcnt(6)\n\t" \
  "v_smfmac_f32_16x16x64_bf16 v[192:195], v[236:239], v[128:135], v251\n\t" \
  "ds_read_b128 v[204:207], v248 offset:5120\n\t" \
  "v_smfmac_f32_16x16x64_bf16 v[196:199], v[236:239], v[136:143], v251\n\t" \
  "s_waitcnt lgkmcnt(5)\n\t" \
  "v_smfmac_f32_16x16x64_bf16 v[192:195], v[240:243], v[216:223], v251\n\t" \
  "s_waitcnt lgkmcnt(3)\n\t" \
  "v_smfmac_f32_16x16x64_bf16 v[196:199], v[240:243], v[224:231], v251\n\t" \
  "s_waitcnt lgkmcnt(2)\n\t" \
  "v_smfmac_f32_16x16x64_bf16 v[192:195], v[244:247], v[144:151], v251\n\t" \
  "v_smfmac_f32_16x16x64_bf16 v[196:199], v[244:247], v[152:159], v251\n\t" \
  "s_waitcnt lgkmcnt(1)\n\t" \
  "v_smfmac_f32_16x16x64_bf16 v[192:195], v[200:203], v[160:167], v251\n\t" \
  "v_smfmac_f32_16x16x64_bf16 v[196:199], v[200:203], v[168:175], v251\n\t" \
  "s_waitcnt lgkmcnt(0)\n\t" \
  "v_smfmac_f32_16x16x64_bf16 v[192:195], v[204:207], v[176:183], v251\n\t" \
  "v_smfmac_f32_16x16x64_bf16 v[196:199], v[204:207], v[184:191], v251\n\t" \
  "s_nop 6\n\t" \
  "v_pk_add_f32 v[192:193], v[192:193], v[194:195]\n\t" \
  "v_pk_add_f32 v[196:197], v[196:197], v[198:199]\n\t" \
  "v_cvt_pk_bf16_f32 v192, v192, v193\n\t" \
  "v_cvt_pk_bf16_f32 v196, v196, v197\n\t" \
  "ds_write2_b32 v252, v192, v196 offset1:64\n\t" \
  "s_waitcnt lgkmcnt(0)\n\t" \
  "s_barrier\n\t" \
  "buffer_store_dword v192, v235, %[rs], %[sk] offen sc1\n\t" \
  "buffer_store_dword v196, v235, %[rs], %[sk] offen offset:256 sc1\n\t" \
  "s_mov_b32 %[spins], s92\n\t" \
  "s_branch .Lend_%=\n\t" \
  ".Lslow_%=:\n\t" \
  "s_mov_b64 s[94:95], 0\n\t" \
  "v_cmp_eq_u32 vcc, %[sent], v200\n\t" \
  "s_or_b64 s[94:95], s[94:95], vcc\n\t" \
  "v_cmp_eq_u32 vcc, %[sent], v201\n\t" \
  "s_or_b64 s[94:95], s[94:95], vcc\n\t" \
  "v_cmp_eq_u32 vcc, %[sent], v202\n\t" \
  "s_or_b64 s[94:95], s[94:95], vcc\n\t" \
  "v_cmp_eq_u32 vcc, %[sent], v203\n\t" \
  "s_or_b64 s[94:95], s[94:95], vcc\n\t" \
  "v_cmp_eq_u32 vcc, %[sent], v204\n\t" \
  "s_or_b64 s[94:95], s[94:95], vcc\n\t" \
  "v_cmp_eq_u32 vcc, %[sent], v205\n\t" \
  "s_or_b64 s[94:95], s[94:95], vcc\n\t" \
  "v_cmp_eq_u32 vcc, %[sent], v206\n\t" \
  "s_or_b64 s[94:95], s[94:95], vcc\n\t" \
  "v_cmp_eq_u32 vcc, %[sent], v207\n\t" \
  "s_or_b64 s[94:95], s[94:95], vcc\n\t" \
  "s_cmp_eq_u64 s[94:95], 0\n\t" \
  "s_cbranch_scc1 .Lok_%=\n\t" \
  "v_mov_b64 v[192:193], 0\n\t" \
  "v_mov_b64 v[194:195], 0\n\t" \
  "v_mov_b64 v[196:197], 0\n\t" \
  "v_mov_b64 v[198:199], 0\n\t" \
  "s_add_u32 s92, s92, 1\n\t" \
  "s_cmp_eq_u32 s92, 1\n\t" \
  "s_cbranch_scc1 .Lpoll_%=\n\t" \
  "s_sleep 2\n\t" \
  "s_and_b32 s93, s92, 63\n\t" \
  "s_cmp_lg_u32 s93, 0\n\t" \
  "s_cbranch_scc1 .Lpoll_%=\n\t" \
  "v_mov_b32 v245, 0\n\t" \
  "buffer_load_dword v244, v245, %[rs], %[sab] offen sc1\n\t" \
  "s_waitcnt vmcnt(0)\n\t" \
  "v_readfirstlane_b32 s93, v244\n\t" \
  "s_nop 3\n\t" \
  "s_cmp_eq_u32 s93, 1\n\t" \
  "s_cbranch_scc1 .Lok_%=\n\t" \
  "s_cmp_lt_u32 s92, 0x80000\n\t" \
  "s_cbranch_scc1 .Lpoll_%=\n\t" \
  "v_mov_b32 v244, 1\n\t" \
  "buffer_store_dword v244, v245, %[rs], %[sab] offen sc1\n\t" \
  "s_branch .Lok_%=\n\t" \
  ".Lend_%=:\n\t"
#define QK_ASM_7 \
  "s_waitcnt lgkmcnt(0)\n\t" \
  "v_subrev_u32 v244, 0x8000, v250\n\t" \
  "ds_read_b128 v[216:219], v248 offset:61440\n\t" \
  "ds_read_b128 v[220:223], v248 offset:62464\n\t" \
  "ds_read_b128 v[224:227], v248 offset:63488\n\t" \
  "ds_read_b128 v[228:231], v248 offset:64512\n\t" \
  "ds_read_b128 v[208:211], v244 offset:0\n\t" \
  "ds_read_b128 v[212:215], v244 offset:1024\n\t" \
  "ds_read_b128 v[236:239], v244 offset:2048\n\t" \
  "ds_read_b128 v[240:243], v244 offset:3072\n\t" \
  "v_mov_b64 v[192:193], 0\n\t" \
  "v_mov_b64 v[194:195], 0\n\t" \
  "v_mov_b64 v[196:197], 0\n\t" \
  "v_mov_b64 v[198:199], 0\n\t" \
  ".p2align 5\n\t" \
  "s_nop 0\n\t" \
  "s_nop 0\n\t" \
  "s_nop 0\n\t" \
  "s_nop 0\n\t" \
  "s_movk_i32 s92, 0x2000\n\t" \
  ".Lwt_%=:\n\t" \
  "ds_read_b64 v[200:201], v248 offset:30720\n\t" \
  "s_memrealtime s[90:91]\n\t" \
  "s_sub_u32 s92, s92, 1\n\t" \
  "s_waitcnt lgkmcnt(0)\n\t" \
  "v_readfirstlane_b32 s93, v200\n\t" \
  "v_readfirstlane_b32 %[tgt], v201\n\t" \
  "s_cbranch_scc1 .Lgo_%=\n\t" \
  "s_cmp_eq_u32 s93, %[step]\n\t" \
  "s_cbranch_scc1 .Lwx_%=\n\t" \
  "s_sleep 1\n\t" \
  "s_branch .Lwt_%=\n\t" \
  ".Lwx_%=:\n\t" \
  "s_lshl_b32 s93, s90, 2\n\t" \
  "s_sub_i32 s93, %[tgt], s93\n\t" \
  "s_cmp_le_i32 s93, 0\n\t" \
  "s_cbranch_scc1 .Lgo_%=\n\t" \
  "s_min_i32 s93, s93, 8000\n\t" \
  "s_mov_b32 s94, s93\n\t" \
  ".Lsl_%=:\n\t" \
  "s_sub_i32 s94, s94, 18\n\t" \
  "s_cmp_lt_i32 s94, 30\n\t" \
  "s_cbranch_scc1 .Lsx_%=\n\t" \
  "s_sleep 1\n\t" \
  "s_branch .Lsl_%=\n\t" \
  ".Lsx_%=:\n\t" \
  "s_sub_i32 s93, s93, 12\n\t" \
  "s_max_i32 s93, s93, 4\n\t" \
  "s_lshr_b32 s93, s93, 2\n\t" \
  "s_add_u32 s93, s93, s90\n\t" \
  "s_memrealtime s[90:91]\n\t" \
  ".Lpace_%=:\n\t" \
  "s_memrealtime s[94:95]\n\t" \
  "s_waitcnt lgkmcnt(1)\n\t" \
  "s_sub_i32 s92, s90, s93\n\t" \
  "s_cmp_ge_i32 s92, 0\n\t" \
  "s_cbranch_scc1 .Le_%=\n\t" \
  "s_memrealtime s[90:91]\n\t" \
  "s_waitcnt lgkmcnt(1)\n\t" \
  "s_sub_i32 s92, s94, s93\n\t" \
  "s_cmp_lt_i32 s92, 0\n\t" \
  "s_cbranch_scc1 .Lpace_%=\n\t" \
  "s_nop 4\n\t" \
  ".Le_%=:\n\t" \
  "s_add_u32 s92, s92, s93\n\t" \
  "s_lshl_b32 s92, s92, 2\n\t" \
  "s_sub_i32 s92, %[tgt], s92\n\t" \
  "s_max_i32 s92, s92, 0\n\t" \
  "s_bitcmp0_b32 s92, 3\n\t" \
  "s_cbranch_scc1 .Lf3_%=\n\t" \
  "s_nop 15\n\t" \
  ".Lf3_%=:\n\t" \
  "s_bitcmp0_b32 s92, 2\n\t" \
  "s_cbranch_scc1 .Lf2_%=\n\t" \
  "s_nop 10\n\t" \
  ".Lf2_%=:\n\t" \
  ".Lgo_%=:\n\t" \
  "s_mov_b32 s92, 0\n\t" \
  "s_memtime s[88:89]\n\t" \
  ".Lpoll_%=:\n\t" \
  "buffer_load_dwordx4 v[200:203], v248, %[rs], %[som] offen sc1\n\t" \
  "buffer_load_dwordx4 v[204:207], v248, %[rs], %[som2] offen sc1\n\t" \
  ".p2align 5\n\t" \
  "s_nop 0\n\t" \
  "s_nop 0\n\t" \
  "s_nop 0\n\t" \
  "s_nop 0\n\t" \
  "s_nop 0\n\t" \
  "s_nop 0\n\t" \
  "s_nop 0\n\t" \
  "s_waitcnt vmcnt(1) lgkmcnt(0)\n\t" \
  "ds_write_b128 v249, v[200:203]\n\t" \
  "v_smfmac_f32_16x16x64_bf16 v[192:195], v[200:203], v[208:215], v251\n\t" \
  "v_max3_u32 v244, v200, v201, v202\n\t" \
  "v_smfmac_f32_16x16x64_bf16 v[196:199], v[200:203], v[236:243], v251\n\t" \
  "s_waitcnt vmcnt(0)\n\t" \
  "ds_write_b128 v249, v[204:207] offset:8192\n\t" \
  "v_smfmac_f32_16x16x64_bf16 v[192:195], v[204:207], v[216:223], v251\n\t" \
  "v_max3_u32 v244, v244, v203, v204\n\t" \
  "v_max3_u32 v244, v244, v205, v206\n\t" \
  "v_smfmac_f32_16x16x64_bf16 v[196:199], v[204:207], v[224:231], v251\n\t" \
  "v_max_u32 v244, v244, v207\n\t" \
  "v_cmp_le_u32 vcc, %[sent], v244\n\t" \
  "s_cbranch_vccnz .Lslow_%=\n\t" \
  ".Lok_%=:\n\t" \
  "s_waitcnt lgkmcnt(0)\n\t" \
  "s_setprio 3\n\t" \
  "s_barrier\n\t" \
  "s_memrealtime s[90:91]\n\t" \
  "s_memtime s[94:95]\n\t" \
  "v_mov_b32 v232, %[step]\n\t" \
  "v_lshrrev_b32_e64 v234, 1, v248\n\t" \
  "s_mov_b64 exec, 0xffff\n\t" \
  "s_waitcnt lgkmcnt(0)\n\t" \
  "s_lshl_b32 s90, s90, 2\n\t" \
  "s_sub_u32 s94, s94, s88\n\t" \
  "s_mul_i32 s94, s94, 12200\n\t" \
  "s_lshr_b32 s94, s94, 16\n\t" \
  "s_add_u32 s94, s94, %[tgt]\n\t" \
  "s_sub_i32 s93, s90, s94\n\t" \
  "s_sub_i32 s93, s93, 30\n\t" \
  "s_max_i32 s93, s93, 0\n\t" \
  "s_add_u32 s90, s94, s93\n\t" \
  "s_sub_i32 s93, s90, %[tgt]\n\t" \
  "s_cmp_gt_i32 s93, 270\n\t" \
  "s_cselect_b32 s93, 70, 0\n\t" \
  "s_add_u32 s93, s93, s90\n\t" \
  "s_add_u32 s93, s93, %[lead]\n\t" \
  "v_mov_b32 v233, s93\n\t" \
  "buffer_store_dwordx2 v[232:233], v234, %[rs], %[sbc] offen sc1\n\t" \
  "s_mov_b64 exec, -1\n\t" \
  "s_mov_b32 %[tb1], s90\n\t" \
  "s_setprio 0\n\t" \
  "ds_read_b128 v[208:211], v248 offset:9216\n\t" \
  "ds_read_b128 v[212:215], v248 offset:10240\n\t" \
  "ds_read_b128 v[236:239], v248 offset:11264\n\t" \
  "ds_read_b128 v[240:243], v248 offset:13312\n\t" \
  "ds_read_b128 v[244:247], v248 offset:14336\n\t" \
  "s_waitcnt lgkmcnt(4)\n\t" \
  "ds_read_b128 v[200:203], v248 offset:6144\n\t" \
  "v_smfmac_f32_16x16x64_bf16 v[192:195], v[208:211], v[0:7], v251\n\t" \
  "ds_read_b128 v[216:219], v250 offset:32768\n\t" \
  "v_smfmac_f32_16x16x64_bf16 v[196:199], v[208:211], v[8:15], v251\n\t" \
  "s_waitcnt lgkmcnt(5)\n\t" \
  "ds_read_b128 v[220:223], v250 offset:33792\n\t" \
  "v_smfmac_f32_16x16x64_bf16 v[192:195], v[212:215], v[16:23], v251\n\t" \
  "ds_read_b128 v[224:227], v250 offset:34816\n\t" \
  "v_smfmac_f32_16x16x64_bf16 v[196:199], v[212:215], v[24:31], v251\n\t" \
  "s_waitcnt lgkmcnt(6)\n\t" \
  "ds_read_b128 v[228:231], v250 offset:35840\n\t" \
  "v_smfmac_f32_16x16x64_bf16 v[192:195], v[236:239], v[32:39], v251\n\t" \
  "ds_read_b128 v[204:207], v248 offset:0\n\t" \
  "v_smfmac_f32_16x16x64_bf16 v[196:199], v[236:239], v[40:47], v251\n\t" \
  "s_waitcnt lgkmcnt(7)\n\t" \
  "ds_read_b128 v[208:211], v248 offset:1024\n\t" \
  "v_smfmac_f32_16x16x64_bf16 v[192:195], v[240:243], v[48:55], v251\n\t" \
  "ds_read_b128 v[212:215], v248 offset:2048\n\t" \
  "v_smfmac_f32_16x16x64_bf16 v[196:199], v[240:243], v[56:63], v251\n\t" \
  "s_waitcnt lgkmcnt(7)\n\t" \
  "v_smfmac_f32_16x16x64_bf16 v[192:195], v[244:247], v[64:71], v251\n\t" \
  "ds_read_b128 v[236:239], v248 offset:3072\n\t" \
  "v_smfmac_f32_16x16x64_bf16 v[196:199], v[244:247], v[72:79], v251\n\t" \
  "s_waitcnt lgkmcnt(6)\n\t" \
  "v_smfmac_f32_16x16x64_bf16 v[192:195], v[200:203], v[216:223], v251\n\t" \
  "s_waitcnt lgkmcnt(4)\n\t" \
  "ds_read_b128 v[240:243], v248 offset:12288\n\t" \
  "v_smfmac_f32_16x16x64_bf16 v[196:199], v[200:203], v[224:231], v251\n\t" \
  "s_waitcnt lgkmcnt(4)\n\t" \
  "ds_read_b128 v[216:219], v250 offset:0\n\t" \
  "v_smfmac_f32_16x16x64_bf16 v[192:195], v[204:207], v[80:87], v251\n\t" \
  "ds_read_b128 v[220:223], v250 offset:1024\n\t" \
  "v_smfmac_f32_16x16x64_bf16 v[196:199], v[204:207], v[88:95], v251\n\t" \
  "s_waitcnt lgkmcnt(4)\n\t" \
  "ds_read_b128 v[224:227], v250 offset:2048\n\t" \
  "v_smfmac_f32_16x16x64_bf16 v[192:195], v[208:211], v[96:103], v251\n\t" \
  "ds_read_b128 v[228:231], v250 offset:3072\n\t" \
  "v_smfmac_f32_16x16x64_bf16 v[196:199], v[208:211], v[104:111], v251\n\t" \
  "ds_read_b128 v[244:247], v248 offset:4096\n\t" \
  "v_smfmac_f32_16x16x64_bf16 v[192:195], v[212:215], v[112:119], v251\n\t" \
  "ds_read_b128 v[200:203], v248 offset:5120\n\t" \
  "v_smfmac_f32_16x16x64_bf16 v[196:199], v[212:215], v[120:127], v251\n\t" \
  "s_waitcnt lgkmcnt(6)\n\t" \
  "v_smfmac_f32_16x16x64_bf16 v[192:195], v[236:239], v[128:135], v251\n\t" \
  "ds_read_b128 v[204:207], v248 offset:8192\n\t" \
  "v_smfmac_f32_16x16x64_bf16 v[196:199], v[236:239], v[136:143], v251\n\t" \
  "s_waitcnt lgkmcnt(5)\n\t" \
  "v_smfmac_f32_16x16x64_bf16 v[192:195], v[240:243], v[216:223], v251\n\t" \
  "s_waitcnt lgkmcnt(3)\n\t" \
  "v_smfmac_f32_16x16x64_bf16 v[196:199], v[240:243], v[224:231], v251\n\t" \
  "s_waitcnt lgkmcnt(2)\n\t" \
  "v_smfmac_f32_16x16x64_bf16 v[192:195], v[244:247], v[144:151], v251\n\t" \
  "v_smfmac_f32_16x16x64_bf16 v[196:199], v[244:247], v[152:159], v251\n\t" \
  "s_waitcnt lgkmcnt(1)\n\t" \
  "v_smfmac_f32_16x16x64_bf16 v[192:195], v[200:203], v[160:167], v251\n\t" \
  "v_smfmac_f32_16x16x64_bf16 v[196:199], v[200:203], v[168:175], v251\n\t" \
  "s_waitcnt lgkmcnt(0)\n\t" \
  "v_smfmac_f32_16x16x64_bf16 v[192:195], v[204:207], v[176:183], v251\n\t" \
  "v_smfmac_f32_16x16x64_bf16 v[196:199], v[204:207], v[184:191], v251\n\t" \
  "s_nop 6\n\t" \
  "v_pk_add_f32 v[192:193], v[192:193], v[194:195]\n\t" \
  "v_pk_add_f32 v[196:197], v[196:197], v[198:199]\n\t" \
  "v_cvt_pk_bf16_f32 v192, v192, v193\n\t" \
  "v_cvt_pk_bf16_f32 v196, v196, v197\n\t" \
  "ds_write2_b32 v252, v192, v196 offset1:64\n\t" \
  "s_waitcnt lgkmcnt(0)\n\t" \
  "s_barrier\n\t" \
  "buffer_store_dword v192, v235, %[rs], %[sk] offen sc1\n\t" \
  "buffer_store_dword v196, v235, %[rs], %[sk] offen offset:256 sc1\n\t" \
  "s_mov_b32 %[spins], s92\n\t" \
  "s_branch .Lend_%=\n\t" \
  ".Lslow_%=:\n\t" \
  "s_mov_b64 s[94:95], 0\n\t" \
  "v_cmp_eq_u32 vcc, %[sent], v200\n\t" \
  "s_or_b64 s[94:95], s[94:95], vcc\n\t" \
  "v_cmp_eq_u32 vcc, %[sent], v201\n\t" \
  "s_or_b64 s[94:95], s[94:95], vcc\n\t" \
  "v_cmp_eq_u32 vcc, %[sent], v202\n\t" \
  "s_or_b64 s[94:95], s[94:95], vcc\n\t" \
  "v_cmp_eq_u32 vcc, %[sent], v203\n\t" \
  "s_or_b64 s[94:95], s[94:95], vcc\n\t" \
  "v_cmp_eq_u32 vcc, %[sent], v204\n\t" \
  "s_or_b64 s[94:95], s[94:95], vcc\n\t" \
  "v_cmp_eq_u32 vcc, %[sent], v205\n\t" \
  "s_or_b64 s[94:95], s[94:95], vcc\n\t" \
  "v_cmp_eq_u32 vcc, %[sent], v206\n\t" \
  "s_or_b64 s[94:95], s[94:95], vcc\n\t" \
  "v_cmp_eq_u32 vcc, %[sent], v207\n\t" \
  "s_or_b64 s[94:95], s[94:95], vcc\n\t" \
  "s_cmp_eq_u64 s[94:95], 0\n\t" \
  "s_cbranch_scc1 .Lok_%=\n\t" \
  "v_mov_b64 v[192:193], 0\n\t" \
  "v_mov_b64 v[194:195], 0\n\t" \
  "v_mov_b64 v[196:197], 0\n\t" \
  "v_mov_b64 v[198:199], 0\n\t" \
  "s_add_u32 s92, s92, 1\n\t" \
  "s_cmp_eq_u32 s92, 1\n\t" \
  "s_cbranch_scc1 .Lpoll_%=\n\t" \
  "s_sleep 2\n\t" \
  "s_and_b32 s93, s92, 63\n\t" \
  "s_cmp_lg_u32 s93, 0\n\t" \
  "s_cbranch_scc1 .Lpoll_%=\n\t" \
  "v_mov_b32 v245, 0\n\t" \
  "buffer_load_dword v244, v245, %[rs], %[sab] offen sc1\n\t" \
  "s_waitcnt vmcnt(0)\n\t" \
  "v_readfirstlane_b32 s93, v244\n\t" \
  "s_nop 3\n\t" \
  "s_cmp_eq_u32 s93, 1\n\t" \
  "s_cbranch_scc1 .Lok_%=\n\t" \
  "s_cmp_lt_u32 s92, 0x80000\n\t" \
  "s_cbranch_scc1 .Lpoll_%=\n\t" \
  "v_mov_b32 v244, 1\n\t" \
  "buffer_store_dword v244, v245, %[rs], %[sab] offen sc1\n\t" \
  "s_branch .Lok_%=\n\t" \
  ".Lend_%=:\n\t"
#define VV_ASM \
  "s_waitcnt lgkmcnt(0)\n\t" \
  "v_mov_b64 v[128:129], 0\n\t" \
  "v_mov_b64 v[130:131], 0\n\t" \
  ".p2align 5\n\t" \
  "s_nop 0\n\t" \
  "s_nop 0\n\t" \
  "s_nop 0\n\t" \
  "s_nop 0\n\t" \
  "s_movk_i32 s92, 0x2000\n\t" \
  ".Lwt_%=:\n\t" \
  "ds_read_b64 v[132:133], v200 offset:30720\n\t" \
  "s_memrealtime s[90:91]\n\t" \
  "s_sub_u32 s92, s92, 1\n\t" \
  "s_waitcnt lgkmcnt(0)\n\t" \
  "v_readfirstlane_b32 s93, v132\n\t" \
  "v_readfirstlane_b32 %[tgt], v133\n\t" \
  "s_cbranch_scc1 .Lgo_%=\n\t" \
  "s_cmp_eq_u32 s93, %[step]\n\t" \
  "s_cbranch_scc1 .Lwx_%=\n\t" \
  "s_sleep 1\n\t" \
  "s_branch .Lwt_%=\n\t" \
  ".Lwx_%=:\n\t" \
  "s_lshl_b32 s93, s90, 2\n\t" \
  "s_sub_i32 s93, %[tgt], s93\n\t" \
  "s_cmp_le_i32 s93, 0\n\t" \
  "s_cbranch_scc1 .Lgo_%=\n\t" \
  "s_min_i32 s93, s93, 8000\n\t" \
  "s_mov_b32 s94, s93\n\t" \
  ".Lsl_%=:\n\t" \
  "s_sub_i32 s94, s94, 18\n\t" \
  "s_cmp_lt_i32 s94, 30\n\t" \
  "s_cbranch_scc1 .Lsx_%=\n\t" \
  "s_sleep 1\n\t" \
  "s_branch .Lsl_%=\n\t" \
  ".Lsx_%=:\n\t" \
  "s_sub_i32 s93, s93, 12\n\t" \
  "s_max_i32 s93, s93, 4\n\t" \
  "s_lshr_b32 s93, s93, 2\n\t" \
  "s_add_u32 s93, s93, s90\n\t" \
  "s_memrealtime s[90:91]\n\t" \
  ".Lpace_%=:\n\t" \
  "s_memrealtime s[94:95]\n\t" \
  "s_waitcnt lgkmcnt(1)\n\t" \
  "s_sub_i32 s92, s90, s93\n\t" \
  "s_cmp_ge_i32 s92, 0\n\t" \
  "s_cbranch_scc1 .Le_%=\n\t" \
  "s_memrealtime s[90:91]\n\t" \
  "s_waitcnt lgkmcnt(1)\n\t" \
  "s_sub_i32 s92, s94, s93\n\t" \
  "s_cmp_lt_i32 s92, 0\n\t" \
  "s_cbranch_scc1 .Lpace_%=\n\t" \
  "s_nop 4\n\t" \
  ".Le_%=:\n\t" \
  "s_add_u32 s92, s92, s93\n\t" \
  "s_lshl_b32 s92, s92, 2\n\t" \
  "s_sub_i32 s92, %[tgt], s92\n\t" \
  "s_max_i32 s92, s92, 0\n\t" \
  "s_bitcmp0_b32 s92, 3\n\t" \
  "s_cbranch_scc1 .Lf3_%=\n\t" \
  "s_nop 15\n\t" \
  ".Lf3_%=:\n\t" \
  "s_bitcmp0_b32 s92, 2\n\t" \
  "s_cbranch_scc1 .Lf2_%=\n\t" \
  "s_nop 10\n\t" \
  ".Lf2_%=:\n\t" \
  ".Lgo_%=:\n\t" \
  "s_mov_b32 s92, 0\n\t" \
  ".Lpoll_%=:\n\t" \
  "buffer_load_dwordx4 v[132:135], v200, %[rs], %[som] offen sc1\n\t" \
  "buffer_load_dwordx4 v[136:139], v200, %[rs], %[som2] offen sc1\n\t" \
  ".p2align 5\n\t" \
  "s_waitcnt vmcnt(1) lgkmcnt(0)\n\t" \
  "ds_write_b128 v232, v[132:135]\n\t" \
  "v_smfmac_f32_16x16x64_bf16 v[128:131], v[132:135], v[0:7], v202\n\t" \
  "v_max3_u32 v196, v132, v133, v134\n\t" \
  "s_waitcnt vmcnt(0)\n\t" \
  "ds_write_b128 v232, v[136:139] offset:8192\n\t" \
  "v_smfmac_f32_16x16x64_bf16 v[128:131], v[136:139], v[64:71], v202\n\t" \
  "v_max3_u32 v196, v196, v135, v136\n\t" \
  "v_max3_u32 v196, v196, v137, v138\n\t" \
  "v_max_u32 v196, v196, v139\n\t" \
  "v_cmp_le_u32_e64 vcc, %[sent], v196\n\t" \
  "s_cbranch_vccnz .Lslow_%=\n\t" \
  ".Lok_%=:\n\t" \
  "s_waitcnt lgkmcnt(0)\n\t" \
  "s_barrier\n\t" \
  "ds_read_b128 v[164:167], v233 offset:0\n\t" \
  "ds_read_b128 v[140:143], v234 offset:0\n\t" \
  "ds_read_b128 v[144:147], v235 offset:0\n\t" \
  "ds_read_b128 v[148:151], v236 offset:0\n\t" \
  "ds_read_b128 v[152:155], v237 offset:0\n\t" \
  "s_waitcnt lgkmcnt(4)\n\t" \
  "ds_read_b128 v[156:159], v238 offset:0\n\t" \
  "v_smfmac_f32_16x16x64_bf16 v[128:131], v[164:167], v[8:15], v202\n\t" \
  "s_waitcnt lgkmcnt(4)\n\t" \
  "ds_read_b128 v[160:163], v239 offset:0\n\t" \
  "v_smfmac_f32_16x16x64_bf16 v[128:131], v[140:143], v[16:23], v202\n\t" \
  "s_waitcnt lgkmcnt(4)\n\t" \
  "ds_read_b128 v[168:171], v233 offset:8192\n\t" \
  "v_smfmac_f32_16x16x64_bf16 v[128:131], v[144:147], v[24:31], v202\n\t" \
  "s_waitcnt lgkmcnt(4)\n\t" \
  "ds_read_b128 v[172:175], v234 offset:8192\n\t" \
  "v_smfmac_f32_16x16x64_bf16 v[128:131], v[148:151], v[32:39], v202\n\t" \
  "s_waitcnt lgkmcnt(4)\n\t" \
  "ds_read_b128 v[176:179], v235 offset:8192\n\t" \
  "v_smfmac_f32_16x16x64_bf16 v[128:131], v[152:155], v[40:47], v202\n\t" \
  "s_waitcnt lgkmcnt(4)\n\t" \
  "ds_read_b128 v[180:183], v236 offset:8192\n\t" \
  "v_smfmac_f32_16x16x64_bf16 v[128:131], v[156:159], v[48:55], v202\n\t" \
  "s_waitcnt lgkmcnt(4)\n\t" \
  "ds_read_b128 v[184:187], v237 offset:8192\n\t" \
  "v_smfmac_f32_16x16x64_bf16 v[128:131], v[160:163], v[56:63], v202\n\t" \
  "s_waitcnt lgkmcnt(4)\n\t" \
  "ds_read_b128 v[188:191], v238 offset:8192\n\t" \
  "v_smfmac_f32_16x16x64_bf16 v[128:131], v[168:171], v[72:79], v202\n\t" \
  "s_waitcnt lgkmcnt(4)\n\t" \
  "ds_read_b128 v[192:195], v239 offset:8192\n\t" \
  "v_smfmac_f32_16x16x64_bf16 v[128:131], v[172:175], v[80:87], v202\n\t" \
  "s_waitcnt lgkmcnt(4)\n\t" \
  "v_smfmac_f32_16x16x64_bf16 v[128:131], v[176:179], v[88:95], v202\n\t" \
  "s_waitcnt lgkmcnt(3)\n\t" \
  "v_smfmac_f32_16x16x64_bf16 v[128:131], v[180:183], v[96:103], v202\n\t" \
  "s_waitcnt lgkmcnt(2)\n\t" \
  "v_smfmac_f32_16x16x64_bf16 v[128:131], v[184:187], v[104:111], v202\n\t" \
  "s_waitcnt lgkmcnt(1)\n\t" \
  "v_smfmac_f32_16x16x64_bf16 v[128:131], v[188:191], v[112:119], v202\n\t" \
  "s_waitcnt lgkmcnt(0)\n\t" \
  "v_smfmac_f32_16x16x64_bf16 v[128:131], v[192:195], v[120:127], v202\n\t" \
  "s_nop 7\n\t" \
  "v_add_f32 v128, v128, v130\n\t" \
  "v_add_f32_e64 v129, v129, v131\n\t" \
  "v_cvt_pk_bf16_f32 v128, v128, v129\n\t" \
  "buffer_store_dword v128, v203, %[rs], %[sv] offen sc1\n\t" \
  "ds_write_b32 v204, v128\n\t" \
  "s_waitcnt lgkmcnt(0)\n\t" \
  "s_barrier\n\t" \
  "s_mov_b32 %[spins], s92\n\t" \
  "s_branch .Lend_%=\n\t" \
  ".Lslow_%=:\n\t" \
  "s_mov_b64 s[94:95], 0\n\t" \
  "v_cmp_eq_u32 vcc, %[sent], v132\n\t" \
  "s_or_b64 s[94:95], s[94:95], vcc\n\t" \
  "v_cmp_eq_u32 vcc, %[sent], v133\n\t" \
  "s_or_b64 s[94:95], s[94:95], vcc\n\t" \
  "v_cmp_eq_u32 vcc, %[sent], v134\n\t" \
  "s_or_b64 s[94:95], s[94:95], vcc\n\t" \
  "v_cmp_eq_u32 vcc, %[sent], v135\n\t" \
  "s_or_b64 s[94:95], s[94:95], vcc\n\t" \
  "v_cmp_eq_u32 vcc, %[sent], v136\n\t" \
  "s_or_b64 s[94:95], s[94:95], vcc\n\t" \
  "v_cmp_eq_u32 vcc, %[sent], v137\n\t" \
  "s_or_b64 s[94:95], s[94:95], vcc\n\t" \
  "v_cmp_eq_u32 vcc, %[sent], v138\n\t" \
  "s_or_b64 s[94:95], s[94:95], vcc\n\t" \
  "v_cmp_eq_u32 vcc, %[sent], v139\n\t" \
  "s_or_b64 s[94:95], s[94:95], vcc\n\t" \
  "s_cmp_eq_u64 s[94:95], 0\n\t" \
  "s_cbranch_scc1 .Lok_%=\n\t" \
  "v_mov_b64 v[128:129], 0\n\t" \
  "v_mov_b64 v[130:131], 0\n\t" \
  "s_add_u32 s92, s92, 1\n\t" \
  "s_cmp_eq_u32 s92, 1\n\t" \
  "s_cbranch_scc1 .Lpoll_%=\n\t" \
  "s_sleep 2\n\t" \
  "s_and_b32 s93, s92, 63\n\t" \
  "s_cmp_lg_u32 s93, 0\n\t" \
  "s_cbranch_scc1 .Lpoll_%=\n\t" \
  "v_mov_b32 v197, 0\n\t" \
  "buffer_load_dword v196, v197, %[rs], %[sab] offen sc1\n\t" \
  "s_waitcnt vmcnt(0)\n\t" \
  "v_readfirstlane_b32 s93, v196\n\t" \
  "s_nop 3\n\t" \
  "s_cmp_eq_u32 s93, 1\n\t" \
  "s_cbranch_scc1 .Lok_%=\n\t" \
  "s_cmp_lt_u32 s92, 0x80000\n\t" \
  "s_cbranch_scc1 .Lpoll_%=\n\t" \
  "v_mov_b32 v196, 1\n\t" \
  "buffer_store_dword v196, v197, %[rs], %[sab] offen sc1\n\t" \
  "s_branch .Lok_%=\n\t" \
  ".Lend_%=:\n\t"
#define TK_ASM \
  "buffer_load_dwordx4 v[224:227], v200, %[rs], %[sk] offen offset:0 sc1\n\t" \
  "buffer_load_dwordx4 v[228:231], v200, %[rs], %[sk] offen offset:1024 sc1\n\t" \
  "s_memrealtime s[88:89]\n\t" \
  "v_add_u32 v205, %[tl], v200\n\t" \
  "v_add_u32 v206, %[tq], v205\n\t" \
  "v_add_u32 v207, %[tv], v205\n\t" \
  "s_waitcnt lgkmcnt(0)\n\t" \
  "ds_read_b128 v[128:131], v206 offset:8192\n\t" \
  "ds_read_b128 v[144:147], v205 offset:0\n\t" \
  "ds_read_b128 v[160:163], v205 offset:4096\n\t" \
  "ds_read_b128 v[132:135], v206 offset:9216\n\t" \
  "ds_read_b128 v[148:151], v205 offset:1024\n\t" \
  "ds_read_b128 v[164:167], v205 offset:5120\n\t" \
  "ds_read_b128 v[136:139], v206 offset:10240\n\t" \
  "ds_read_b128 v[152:155], v205 offset:2048\n\t" \
  "ds_read_b128 v[168:171], v205 offset:6144\n\t" \
  "ds_read_b128 v[140:143], v206 offset:11264\n\t" \
  "ds_read_b128 v[156:159], v205 offset:3072\n\t" \
  "ds_read_b128 v[172:175], v205 offset:7168\n\t" \
  "v_add_u32 v206, %[vp], v218\n\t" \
  "s_lshl_b32 s88, s88, 2\n\t" \
  "s_add_u32 s89, s88, %[od]\n\t" \
  "s_waitcnt lgkmcnt(10)\n\t" \
  "v_mfma_f32_16x16x32_bf16 v[196:199], v[144:147], v[128:131], 0\n\t" \
  "s_waitcnt lgkmcnt(9)\n\t" \
  "v_mfma_f32_16x16x32_bf16 v[144:147], v[160:163], v[128:131], 0\n\t" \
  "s_waitcnt lgkmcnt(7)\n\t" \
  "v_mfma_f32_16x16x32_bf16 v[196:199], v[148:151], v[132:135], v[196:199]\n\t" \
  "s_waitcnt lgkmcnt(6)\n\t" \
  "v_mfma_f32_16x16x32_bf16 v[144:147], v[164:167], v[132:135], v[144:147]\n\t" \
  "s_waitcnt lgkmcnt(4)\n\t" \
  "v_mfma_f32_16x16x32_bf16 v[196:199], v[152:155], v[136:139], v[196:199]\n\t" \
  "s_waitcnt lgkmcnt(3)\n\t" \
  "v_mfma_f32_16x16x32_bf16 v[144:147], v[168:171], v[136:139], v[144:147]\n\t" \
  "s_waitcnt lgkmcnt(1)\n\t" \
  "v_mfma_f32_16x16x32_bf16 v[196:199], v[156:159], v[140:143], v[196:199]\n\t" \
  "s_waitcnt lgkmcnt(0)\n\t" \
  "v_mfma_f32_16x16x32_bf16 v[144:147], v[172:175], v[140:143], v[144:147]\n\t" \
  "ds_read_b128 v[176:179], v207 offset:16384\n\t" \
  "ds_read_b128 v[180:183], v207 offset:17408\n\t" \
  "ds_read_b128 v[184:187], v207 offset:18432\n\t" \
  "ds_read_b128 v[188:191], v207 offset:19456\n\t" \
  "ds_read2st64_b32 v[192:193], v206 offset0:0 offset1:1\n\t" \
  "ds_read2st64_b32 v[194:195], v206 offset0:2 offset1:3\n\t" \
  "v_cvt_pk_bf16_f32 v197, v196, v197\n\t" \
  "v_cvt_pk_bf16_f32 v199, v198, v199\n\t" \
  "v_cvt_pk_bf16_f32 v145, v144, v145\n\t" \
  "v_cvt_pk_bf16_f32 v147, v146, v147\n\t" \
  "v_lshlrev_b32 v196, 16, v197\n\t" \
  "v_lshlrev_b32 v198, 16, v199\n\t" \
  "v_lshlrev_b32 v144, 16, v145\n\t" \
  "v_lshlrev_b32 v146, 16, v147\n\t" \
  "v_and_b32 v197, 0xffff0000, v197\n\t" \
  "v_and_b32 v199, 0xffff0000, v199\n\t" \
  "v_and_b32 v145, 0xffff0000, v145\n\t" \
  "v_and_b32 v147, 0xffff0000, v147\n\t" \
  "v_pk_mul_f32 v[196:197], v[196:197], %[sc] op_sel_hi:[1,0]\n\t" \
  "v_pk_mul_f32 v[198:199], v[198:199], %[sc] op_sel_hi:[1,0]\n\t" \
  "v_pk_mul_f32 v[144:145], v[144:145], %[sc] op_sel_hi:[1,0]\n\t" \
  "v_pk_mul_f32 v[146:147], v[146:147], %[sc] op_sel_hi:[1,0]\n\t" \
  "v_cvt_pk_bf16_f32 v197, v196, v197\n\t" \
  "v_cvt_pk_bf16_f32 v199, v198, v199\n\t" \
  "v_cvt_pk_bf16_f32 v145, v144, v145\n\t" \
  "v_cvt_pk_bf16_f32 v147, v146, v147\n\t" \
  "v_lshlrev_b32 v196, 16, v197\n\t" \
  "v_lshlrev_b32 v198, 16, v199\n\t" \
  "v_lshlrev_b32 v144, 16, v145\n\t" \
  "v_lshlrev_b32 v146, 16, v147\n\t" \
  "v_and_b32 v197, 0xffff0000, v197\n\t" \
  "v_and_b32 v199, 0xffff0000, v199\n\t" \
  "v_and_b32 v145, 0xffff0000, v145\n\t" \
  "v_and_b32 v147, 0xffff0000, v147\n\t" \
  "v_min_f32 v196, v196, v208\n\t" \
  "v_min_f32 v197, v197, v209\n\t" \
  "v_min_f32 v198, v198, v210\n\t" \
  "v_min_f32 v199, v199, v211\n\t" \
  "v_min_f32 v144, v144, v212\n\t" \
  "v_min_f32 v145, v145, v213\n\t" \
  "v_min_f32 v146, v146, v214\n\t" \
  "v_min_f32 v147, v147, v215\n\t" \
  "v_max3_f32 v148, v196, v197, v198\n\t" \
  "v_max3_f32 v149, v199, v144, v145\n\t" \
  "v_max3_f32 v148, v148, v146, v147\n\t" \
  "v_max_f32 v150, v148, v149\n\t" \
  "v_max_f32 v151, v148, v149\n\t" \
  "s_nop 1\n\t" \
  "v_permlane32_swap_b32 v150, v151\n\t" \
  "v_max_f32 v150, v150, v151\n\t" \
  "s_mov_b32 s92, 0\n\t" \
  ".Ltake_%=:\n\t" \
  "s_waitcnt vmcnt(1) lgkmcnt(0)\n\t" \
  "ds_write_b128 v216, v[224:227]\n\t" \
  "v_max3_u32 v148, v224, v225, v226\n\t" \
  "s_waitcnt vmcnt(0)\n\t" \
  "ds_write_b128 v216, v[228:231] offset:1024\n\t" \
  "ds_read_b64_tr_b16 v[160:161], v217 offset:0\n\t" \
  "ds_read_b64_tr_b16 v[162:163], v217 offset:64\n\t" \
  "ds_read_b64_tr_b16 v[164:165], v217 offset:512\n\t" \
  "ds_read_b64_tr_b16 v[166:167], v217 offset:576\n\t" \
  "ds_read_b64_tr_b16 v[168:169], v217 offset:1024\n\t" \
  "ds_read_b64_tr_b16 v[170:171], v217 offset:1088\n\t" \
  "ds_read_b64_tr_b16 v[172:173], v217 offset:1536\n\t" \
  "ds_read_b64_tr_b16 v[174:175], v217 offset:1600\n\t" \
  "v_max3_u32 v148, v148, v227, v228\n\t" \
  "v_max3_u32 v148, v148, v229, v230\n\t" \
  "v_max_u32 v148, v148, v231\n\t" \
  "v_cmp_le_u32 vcc, %[sent], v148\n\t" \
  "s_cbranch_vccnz .Lslow_%=\n\t" \
  ".Lok_%=:\n\t" \
  "s_waitcnt lgkmcnt(6)\n\t" \
  "v_mfma_f32_16x16x32_bf16 v[152:155], v[160:163], v[128:131], 0\n\t" \
  "s_waitcnt lgkmcnt(4)\n\t" \
  "v_mfma_f32_16x16x32_bf16 v[152:155], v[164:167], v[132:135], v[152:155]\n\t" \
  "s_waitcnt lgkmcnt(2)\n\t" \
  "v_mfma_f32_16x16x32_bf16 v[152:155], v[168:171], v[136:139], v[152:155]\n\t" \
  "s_waitcnt lgkmcnt(0)\n\t" \
  "v_mfma_f32_16x16x32_bf16 v[152:155], v[172:175], v[140:143], v[152:155]\n\t" \
  "v_mov_b32 v157, 0\n\t" \
  "v_mov_b32 v158, 0\n\t" \
  "s_nop 5\n\t" \
  "v_cvt_pk_bf16_f32 v153, v152, v153\n\t" \
  "v_cvt_pk_bf16_f32 v155, v154, v155\n\t" \
  "v_lshlrev_b32 v152, 16, v153\n\t" \
  "v_lshlrev_b32 v154, 16, v155\n\t" \
  "v_and_b32 v153, 0xffff0000, v153\n\t" \
  "v_and_b32 v155, 0xffff0000, v155\n\t" \
  "v_pk_mul_f32 v[152:153], v[152:153], %[sc] op_sel_hi:[1,0]\n\t" \
  "v_pk_mul_f32 v[154:155], v[154:155], %[sc] op_sel_hi:[1,0]\n\t" \
  "v_cvt_pk_bf16_f32 v153, v152, v153\n\t" \
  "v_cvt_pk_bf16_f32 v155, v154, v155\n\t" \
  "v_lshlrev_b32 v152, 16, v153\n\t" \
  "v_lshlrev_b32 v154, 16, v155\n\t" \
  "v_and_b32 v153, 0xffff0000, v153\n\t" \
  "v_and_b32 v155, 0xffff0000, v155\n\t" \
  "v_max3_f32 v148, v152, v153, v150\n\t" \
  "v_max3_f32 v149, v148, v154, v155\n\t" \
  "v_max3_f32 v150, v148, v154, v155\n\t" \
  "v_cndmask_b32 v152, v152, v154, %[up]\n\t" \
  "v_cndmask_b32 v153, v153, v155, %[up]\n\t" \
  "v_permlane16_swap_b32 v149, v150\n\t" \
  "v_max_f32 v148, v149, v150\n\t" \
  "v_mul_f32 v148, %[negc], v148\n\t" \
  "v_fma_f32 v152, v152, %[c], v148\n\t" \
  "v_fma_f32 v153, v153, %[c], v148\n\t" \
  "v_exp_f32 v152, v152\n\t" \
  "v_fma_f32 v196, v196, %[c], v148\n\t" \
  "v_exp_f32 v153, v153\n\t" \
  "v_fma_f32 v197, v197, %[c], v148\n\t" \
  "v_exp_f32 v196, v196\n\t" \
  "v_fma_f32 v198, v198, %[c], v148\n\t" \
  "v_exp_f32 v197, v197\n\t" \
  "v_fma_f32 v199, v199, %[c], v148\n\t" \
  "v_exp_f32 v198, v198\n\t" \
  "v_fma_f32 v144, v144, %[c], v148\n\t" \
  "v_pk_add_f32 v[132:133], v[152:153], v[196:197]\n\t" \
  "v_exp_f32 v199, v199\n\t" \
  "v_fma_f32 v145, v145, %[c], v148\n\t" \
  "v_exp_f32 v144, v144\n\t" \
  "v_fma_f32 v146, v146, %[c], v148\n\t" \
  "v_pk_add_f32 v[132:133], v[132:133], v[198:199]\n\t" \
  "v_exp_f32 v145, v145\n\t" \
  "v_fma_f32 v147, v147, %[c], v148\n\t" \
  "v_exp_f32 v146, v146\n\t" \
  "v_pk_add_f32 v[132:133], v[132:133], v[144:145]\n\t" \
  "v_exp_f32 v147, v147\n\t" \
  "s_nop 0\n\t" \
  "v_pk_add_f32 v[132:133], v[132:133], v[146:147]\n\t" \
  "v_add_f32 v134, v132, v133\n\t" \
  "v_add_f32 v135, v132, v133\n\t" \
  "s_nop 1\n\t" \
  "v_permlane16_swap_b32 v134, v135\n\t" \
  "v_add_f32 v132, v134, v135\n\t" \
  "v_add_f32 v133, v134, v135\n\t" \
  "s_nop 1\n\t" \
  "v_permlane32_swap_b32 v132, v133\n\t" \
  "v_add_f32 v132, v132, v133\n\t" \
  "v_rcp_f32 v148, v132\n\t" \
  "s_nop 0\n\t" \
  "v_pk_mul_f32 v[152:153], v[152:153], v[148:149] op_sel_hi:[1,0]\n\t" \
  "v_pk_mul_f32 v[196:197], v[196:197], v[148:149] op_sel_hi:[1,0]\n\t" \
  "v_pk_mul_f32 v[198:199], v[198:199], v[148:149] op_sel_hi:[1,0]\n\t" \
  "v_pk_mul_f32 v[144:145], v[144:145], v[148:149] op_sel_hi:[1,0]\n\t" \
  "v_pk_mul_f32 v[146:147], v[146:147], v[148:149] op_sel_hi:[1,0]\n\t" \
  "v_cvt_pk_bf16_f32 v156, v152, v153\n\t" \
  "v_cvt_pk_bf16_f32 v159, v152, v153\n\t" \
  "v_cvt_pk_bf16_f32 v128, v196, v197\n\t" \
  "v_cvt_pk_bf16_f32 v129, v198, v199\n\t" \
  "v_mfma_f32_16x16x16_bf16 v[160:163], v[192:193], v[156:157], 0\n\t" \
  "v_cvt_pk_bf16_f32 v130, v144, v145\n\t" \
  "v_mfma_f32_16x16x16_bf16 v[164:167], v[192:193], v[158:159], 0\n\t" \
  "v_cvt_pk_bf16_f32 v131, v146, v147\n\t" \
  "v_mfma_f32_16x16x16_bf16 v[168:171], v[194:195], v[156:157], 0\n\t" \
  "v_mfma_f32_16x16x16_bf16 v[172:175], v[194:195], v[158:159], 0\n\t" \
  "s_nop 0\n\t" \
  "v_mfma_f32_16x16x32_bf16 v[160:163], v[176:179], v[128:131], v[160:163]\n\t" \
  "s_nop 0\n\t" \
  "v_mfma_f32_16x16x32_bf16 v[164:167], v[180:183], v[128:131], v[164:167]\n\t" \
  "s_nop 0\n\t" \
  "v_mfma_f32_16x16x32_bf16 v[168:171], v[184:187], v[128:131], v[168:171]\n\t" \
  "v_mfma_f32_16x16x32_bf16 v[172:175], v[188:191], v[128:131], v[172:175]\n\t" \
  "s_nop 2\n\t" \
  "v_cvt_pk_bf16_f32 v136, v160, v161\n\t" \
  "v_cvt_pk_bf16_f32 v137, v162, v163\n\t" \
  "v_cvt_pk_bf16_f32 v138, v164, v165\n\t" \
  "v_cvt_pk_bf16_f32 v139, v166, v167\n\t" \
  "v_cvt_pk_bf16_f32 v140, v168, v169\n\t" \
  "v_cvt_pk_bf16_f32 v141, v170, v171\n\t" \
  "v_cvt_pk_bf16_f32 v142, v172, v173\n\t" \
  "v_cvt_pk_bf16_f32 v143, v174, v175\n\t" \
  "s_memrealtime s[90:91]\n\t" \
  "s_waitcnt lgkmcnt(0)\n\t" \
  "s_lshl_b32 s93, s90, 2\n\t" \
  "s_sub_i32 s93, s89, s93\n\t" \
  "s_cmp_le_i32 s93, 0\n\t" \
  "s_cbranch_scc1 .Lgoo_%=\n\t" \
  "s_min_i32 s93, s93, 8000\n\t" \
  "s_mov_b32 s94, s93\n\t" \
  ".Lslo_%=:\n\t" \
  "s_sub_i32 s94, s94, 18\n\t" \
  "s_cmp_lt_i32 s94, 30\n\t" \
  "s_cbranch_scc1 .Lsxo_%=\n\t" \
  "s_sleep 1\n\t" \
  "s_branch .Lslo_%=\n\t" \
  ".Lsxo_%=:\n\t" \
  "s_add_u32 s93, s93, 3\n\t" \
  "s_lshr_b32 s93, s93, 2\n\t" \
  "s_add_u32 s93, s93, s90\n\t" \
  "s_memrealtime s[90:91]\n\t" \
  ".Lpaceo_%=:\n\t" \
  "s_memrealtime s[94:95]\n\t" \
  "s_waitcnt lgkmcnt(1)\n\t" \
  "s_sub_i32 s92, s90, s93\n\t" \
  "s_cmp_ge_i32 s92, 0\n\t" \
  "s_cbranch_scc1 .Leo_%=\n\t" \
  "s_memrealtime s[90:91]\n\t" \
  "s_waitcnt lgkmcnt(1)\n\t" \
  "s_sub_i32 s92, s94, s93\n\t" \
  "s_cmp_lt_i32 s92, 0\n\t" \
  "s_cbranch_scc1 .Lpaceo_%=\n\t" \
  ".Leo_%=:\n\t" \
  ".Lgoo_%=:\n\t" \
  "buffer_store_dwordx4 v[136:139], v200, %[rt], %[so] offen offset:0 sc1\n\t" \
  "buffer_store_dwordx4 v[140:143], v200, %[rt], %[so] offen offset:1024 sc1\n\t" \
  "s_waitcnt lgkmcnt(0)\n\t" \
  "s_branch .Lend_%=\n\t" \
  ".Lpoll_%=:\n\t" \
  "buffer_load_dwordx4 v[224:227], v200, %[rs], %[sk] offen offset:0 sc1\n\t" \
  "buffer_load_dwordx4 v[228:231], v200, %[rs], %[sk] offen offset:1024 sc1\n\t" \
  "s_branch .Ltake_%=\n\t" \
  "s_branch .Lend_%=\n\t" \
  ".Lslow_%=:\n\t" \
  "s_mov_b64 s[94:95], 0\n\t" \
  "v_cmp_eq_u32 vcc, %[sent], v224\n\t" \
  "s_or_b64 s[94:95], s[94:95], vcc\n\t" \
  "v_cmp_eq_u32 vcc, %[sent], v225\n\t" \
  "s_or_b64 s[94:95], s[94:95], vcc\n\t" \
  "v_cmp_eq_u32 vcc, %[sent], v226\n\t" \
  "s_or_b64 s[94:95], s[94:95], vcc\n\t" \
  "v_cmp_eq_u32 vcc, %[sent], v227\n\t" \
  "s_or_b64 s[94:95], s[94:95], vcc\n\t" \
  "v_cmp_eq_u32 vcc, %[sent], v228\n\t" \
  "s_or_b64 s[94:95], s[94:95], vcc\n\t" \
  "v_cmp_eq_u32 vcc, %[sent], v229\n\t" \
  "s_or_b64 s[94:95], s[94:95], vcc\n\t" \
  "v_cmp_eq_u32 vcc, %[sent], v230\n\t" \
  "s_or_b64 s[94:95], s[94:95], vcc\n\t" \
  "v_cmp_eq_u32 vcc, %[sent], v231\n\t" \
  "s_or_b64 s[94:95], s[94:95], vcc\n\t" \
  "s_cmp_eq_u64 s[94:95], 0\n\t" \
  "s_cbranch_scc1 .Lok_%=\n\t" \
  "s_add_u32 s92, s92, 1\n\t" \
  "s_cmp_eq_u32 s92, 1\n\t" \
  "s_cbranch_scc1 .Lpoll_%=\n\t" \
  "s_sleep 2\n\t" \
  "s_and_b32 s93, s92, 63\n\t" \
  "s_cmp_lg_u32 s93, 0\n\t" \
  "s_cbranch_scc1 .Lpoll_%=\n\t" \
  "v_mov_b32 v149, 0\n\t" \
  "buffer_load_dword v148, v149, %[rs], %[sab] offen sc1\n\t" \
  "s_waitcnt vmcnt(0)\n\t" \
  "v_readfirstlane_b32 s93, v148\n\t" \
  "s_nop 3\n\t" \
  "s_cmp_eq_u32 s93, 1\n\t" \
  "s_cbranch_scc1 .Lok_%=\n\t" \
  "s_cmp_lt_u32 s92, 0x80000\n\t" \
  "s_cbranch_scc1 .Lpoll_%=\n\t" \
  "v_mov_b32 v148, 1\n\t" \
  "buffer_store_dword v148, v149, %[rs], %[sab] offen sc1\n\t" \
  "s_branch .Lok_%=\n\t" \
  ".Lend_%=:\n\t"
constexpr int TLAG = 2, R4 = 0;
#define RED_ASM \
  "s_mov_b32 %[step], 0\n\t" \
  "s_lshl_b32 %[sh], %[step], 15\n\t" \
  "s_lshr_b32 s93, %[step], 4\n\t" \
  "s_lshr_b32 s93, %[pm], s93\n\t" \
  "s_and_b32 s93, s93, 4\n\t" \
  "s_lshl_b32 s93, s93, 11\n\t" \
  "s_lshl2_add_u32 %[so], %[sh], s93\n\t" \
  "s_sub_i32 s93, %[step], 2\n\t" \
  "s_lshl_b32 %[st], s93, 19\n\t" \
  "s_lshl_b32 s94, s93, 11\n\t" \
  "s_xor_b32 s94, s94, %[s0]\n\t" \
  "s_and_b32 s94, s94, 0x2000\n\t" \
  "s_or_b32 %[st], %[st], s94\n\t" \
  "s_cmp_lt_i32 s93, 0\n\t" \
  "s_cselect_b32 %[st], 0x70000000, %[st]\n\t" \
  "s_add_u32 %[st4], %[st], 4096\n\t" \
  "buffer_load_dwordx2 v[166:167], v173, %[rs], 0 offen sc1\n\t" \
  "v_mov_b64 v[144:145], 0\n\t" \
  "v_mov_b64 v[146:147], 0\n\t" \
  "v_mov_b64 v[148:149], 0\n\t" \
  "v_mov_b64 v[150:151], 0\n\t" \
  "v_mov_b64 v[152:153], 0\n\t" \
  "v_mov_b64 v[154:155], 0\n\t" \
  "v_mov_b64 v[156:157], 0\n\t" \
  "v_mov_b64 v[158:159], 0\n\t" \
  ".Lstep_%=:\n\t" \
  "s_setprio 3\n\t" \
  "s_mov_b32 s92, 0\n\t" \
  ".Lcal_%=:\n\t" \
  "s_waitcnt vmcnt(0)\n\t" \
  "s_memrealtime s[76:77]\n\t" \
  "v_cmp_ne_u32 vcc, %[step], v166\n\t" \
  "v_readlane_b32 s90, v167, 0\n\t" \
  "v_readlane_b32 s91, v167, 1\n\t" \
  "v_readlane_b32 s94, v167, 2\n\t" \
  "v_readlane_b32 s95, v167, 3\n\t" \
  "s_sub_i32 s90, s90, %[base]\n\t" \
  "s_sub_i32 s91, s91, %[base]\n\t" \
  "s_sub_i32 s94, s94, %[base]\n\t" \
  "s_sub_i32 s95, s95, %[base]\n\t" \
  "s_max_i32 s90, s90, s91\n\t" \
  "s_max_i32 s94, s94, s95\n\t" \
  "s_max_i32 s90, s90, s94\n\t" \
  "s_add_i32 s90, s90, %[ds]\n\t" \
  "s_waitcnt lgkmcnt(0)\n\t" \
  "s_add_u32 %[E], s90, %[base]\n\t" \
  "s_and_b32 s93, vcc_lo, 0xf\n\t" \
  "s_cbranch_scc1 .Lbl_%=\n\t" \
  ".Lgo_%=:\n\t" \
  "v_mov_b32 v166, 7\n\t" \
  "s_mov_b64 exec, 1\n\t" \
  "v_mov_b32 v164, s90\n\t" \
  "ds_max_u32 v168, v164 offset:4100\n\t" \
  "ds_add_u32 v168, v164 offset:4096\n\t" \
  "s_mov_b64 exec, -1\n\t" \
  "s_memrealtime s[90:91]\n\t" \
  ".p2align 5\n\t" \
  "s_waitcnt lgkmcnt(0)\n\t" \
  "s_lshl_b32 s93, s90, 2\n\t" \
  "s_sub_i32 s93, %[E], s93\n\t" \
  "s_cmp_le_i32 s93, 0\n\t" \
  "s_cbranch_scc1 .Lgop_%=\n\t" \
  "s_min_i32 s93, s93, 8000\n\t" \
  "s_mov_b32 s94, s93\n\t" \
  ".Lslp_%=:\n\t" \
  "s_sub_i32 s94, s94, 18\n\t" \
  "s_cmp_lt_i32 s94, 30\n\t" \
  "s_cbranch_scc1 .Lsxp_%=\n\t" \
  "s_sleep 1\n\t" \
  "s_branch .Lslp_%=\n\t" \
  ".Lsxp_%=:\n\t" \
  "s_sub_i32 s93, s93, 12\n\t" \
  "s_max_i32 s93, s93, 4\n\t" \
  "s_lshr_b32 s93, s93, 2\n\t" \
  "s_add_u32 s93, s93, s90\n\t" \
  "s_memrealtime s[90:91]\n\t" \
  ".Lpacep_%=:\n\t" \
  "s_memrealtime s[94:95]\n\t" \
  "s_waitcnt lgkmcnt(1)\n\t" \
  "s_sub_i32 s92, s90, s93\n\t" \
  "s_cmp_ge_i32 s92, 0\n\t" \
  "s_cbranch_scc1 .Lep_%=\n\t" \
  "s_memrealtime s[90:91]\n\t" \
  "s_waitcnt lgkmcnt(1)\n\t" \
  "s_sub_i32 s92, s94, s93\n\t" \
  "s_cmp_lt_i32 s92, 0\n\t" \
  "s_cbranch_scc1 .Lpacep_%=\n\t" \
  "s_nop 4\n\t" \
  ".Lep_%=:\n\t" \
  "s_add_u32 s92, s92, s93\n\t" \
  "s_lshl_b32 s92, s92, 2\n\t" \
  "s_sub_i32 s92, %[E], s92\n\t" \
  "s_max_i32 s92, s92, 0\n\t" \
  "s_bitcmp0_b32 s92, 3\n\t" \
  "s_cbranch_scc1 .Lf3p_%=\n\t" \
  "s_nop 15\n\t" \
  ".Lf3p_%=:\n\t" \
  "s_bitcmp0_b32 s92, 2\n\t" \
  "s_cbranch_scc1 .Lf2p_%=\n\t" \
  "s_nop 10\n\t" \
  ".Lf2p_%=:\n\t" \
  ".Lgop_%=:\n\t" \
  "s_mov_b32 s92, 0\n\t" \
  "buffer_load_dwordx4 v[128:131], v234, %[rs], %[so] offen sc1\n\t" \
  "s_nop 15\n\t" \
  "s_nop 1\n\t" \
  "buffer_load_dwordx4 v[132:135], v235, %[rs], %[so] offen sc1\n\t" \
  "s_nop 15\n\t" \
  "s_nop 1\n\t" \
  "buffer_load_dwordx4 v[136:139], v236, %[rs], %[so] offen sc1\n\t" \
  "s_nop 15\n\t" \
  "s_nop 1\n\t" \
  "buffer_load_dwordx4 v[140:143], v237, %[rs], %[so] offen sc1\n\t" \
  ".p2align 5\n\t" \
  "s_nop 0\n\t" \
  "s_nop 0\n\t" \
  ".Lw_%=:\n\t" \
  "s_waitcnt lgkmcnt(0)\n\t" \
  "s_waitcnt vmcnt(3)\n\t" \
  "v_smfmac_f32_16x16x64_bf16 v[144:147], v[128:131], v[0:7], v172\n\t" \
  "v_max3_u32 v164, v128, v129, 0\n\t" \
  "v_smfmac_f32_16x16x64_bf16 v[148:151], v[128:131], v[8:15], v172\n\t" \
  "v_max3_u32 v164, v130, v131, v164\n\t" \
  "v_smfmac_f32_16x16x64_bf16 v[152:155], v[128:131], v[16:23], v172\n\t" \
  "buffer_load_dwordx4 v[176:179], v238, %[rt], %[st] offen offset:0 sc1\n\t" \
  "v_smfmac_f32_16x16x64_bf16 v[156:159], v[128:131], v[24:31], v172\n\t" \
  "s_waitcnt vmcnt(3)\n\t" \
  "v_smfmac_f32_16x16x64_bf16 v[144:147], v[132:135], v[32:39], v172\n\t" \
  "v_max3_u32 v164, v132, v133, v164\n\t" \
  "v_smfmac_f32_16x16x64_bf16 v[148:151], v[132:135], v[40:47], v172\n\t" \
  "v_max3_u32 v164, v134, v135, v164\n\t" \
  "v_smfmac_f32_16x16x64_bf16 v[152:155], v[132:135], v[48:55], v172\n\t" \
  "buffer_load_dwordx4 v[180:183], v238, %[rt], %[st] offen offset:1024 sc1\n\t" \
  "v_smfmac_f32_16x16x64_bf16 v[156:159], v[132:135], v[56:63], v172\n\t" \
  "s_waitcnt vmcnt(3)\n\t" \
  "v_smfmac_f32_16x16x64_bf16 v[144:147], v[136:139], v[64:71], v172\n\t" \
  "v_max3_u32 v164, v136, v137, v164\n\t" \
  "v_smfmac_f32_16x16x64_bf16 v[148:151], v[136:139], v[72:79], v172\n\t" \
  "v_max3_u32 v164, v138, v139, v164\n\t" \
  "v_smfmac_f32_16x16x64_bf16 v[152:155], v[136:139], v[80:87], v172\n\t" \
  "buffer_load_dwordx4 v[184:187], v238, %[rt], %[st4] offen offset:0 sc1\n\t" \
  "v_smfmac_f32_16x16x64_bf16 v[156:159], v[136:139], v[88:95], v172\n\t" \
  "s_waitcnt vmcnt(3)\n\t" \
  "v_smfmac_f32_16x16x64_bf16 v[144:147], v[140:143], v[96:103], v172\n\t" \
  "v_max3_u32 v164, v140, v141, v164\n\t" \
  "v_smfmac_f32_16x16x64_bf16 v[148:151], v[140:143], v[104:111], v172\n\t" \
  "v_max3_u32 v164, v142, v143, v164\n\t" \
  "v_smfmac_f32_16x16x64_bf16 v[152:155], v[140:143], v[112:119], v172\n\t" \
  "v_cmp_le_u32 vcc, %[sent], v164\n\t" \
  "buffer_load_dwordx4 v[188:191], v238, %[rt], %[st4] offen offset:1024 sc1\n\t" \
  "v_smfmac_f32_16x16x64_bf16 v[156:159], v[140:143], v[120:127], v172\n\t" \
  "s_cbranch_vccnz .Lslow_%=\n\t" \
  ".Lok_%=:\n\t" \
  "v_add_f32_dpp v160, v144, v146 quad_perm:[0,1,2,3] row_mask:0x3 bank_mask:0xf\n\t" \
  "v_add_f32_dpp v161, v145, v147 quad_perm:[0,1,2,3] row_mask:0x3 bank_mask:0xf\n\t" \
  "v_add_f32_dpp v162, v148, v150 quad_perm:[0,1,2,3] row_mask:0x3 bank_mask:0xf\n\t" \
  "v_add_f32_dpp v163, v149, v151 quad_perm:[0,1,2,3] row_mask:0x3 bank_mask:0xf\n\t" \
  "v_add_f32_dpp v160, v152, v154 quad_perm:[0,1,2,3] row_mask:0xc bank_mask:0xf\n\t" \
  "v_add_f32_dpp v161, v153, v155 quad_perm:[0,1,2,3] row_mask:0xc bank_mask:0xf\n\t" \
  "s_nop 0\n\t" \
  "v_add_f32_dpp v162, v156, v158 quad_perm:[0,1,2,3] row_mask:0xc bank_mask:0xf\n\t" \
  "v_add_f32_dpp v163, v157, v159 quad_perm:[0,1,2,3] row_mask:0xc bank_mask:0xf\n\t" \
  "s_nop 0\n\t" \
  "v_permlane32_swap_b32 v160, v162\n\t" \
  "v_permlane32_swap_b32 v161, v163\n\t" \
  "v_add_f32 v160, v160, v162\n\t" \
  "v_add_f32_e64 v161, v161, v163\n\t" \
  "ds_write_b64 v169, v[160:161]\n\t" \
  "s_mov_b64 exec, 1\n\t" \
  "ds_inc_rtn_u32 v164, v168, v166 offset:4104\n\t" \
  "s_mov_b64 exec, -1\n\t" \
  "ds_read_b64 v[128:129], v170 offset:0\n\t" \
  "ds_read_b64 v[130:131], v170 offset:512\n\t" \
  "ds_read_b64 v[132:133], v170 offset:1024\n\t" \
  "ds_read_b64 v[134:135], v170 offset:1536\n\t" \
  "ds_read_b64 v[136:137], v170 offset:2048\n\t" \
  "ds_read_b64 v[138:139], v170 offset:2560\n\t" \
  "ds_read_b64 v[140:141], v170 offset:3072\n\t" \
  "ds_read_b64 v[142:143], v170 offset:3584\n\t" \
  "s_waitcnt lgkmcnt(8)\n\t" \
  "v_readfirstlane_b32 s93, v164\n\t" \
  "s_cmp_lg_u32 s93, 7\n\t" \
  "s_cbranch_scc1 .Lb1_%=\n\t" \
  "s_waitcnt lgkmcnt(4)\n\t" \
  "v_pk_add_f32 v[128:129], v[128:129], v[130:131]\n\t" \
  "v_pk_add_f32 v[132:133], v[132:133], v[134:135]\n\t" \
  "s_waitcnt lgkmcnt(0)\n\t" \
  "v_pk_add_f32 v[136:137], v[136:137], v[138:139]\n\t" \
  "v_pk_add_f32 v[140:141], v[140:141], v[142:143]\n\t" \
  "v_pk_add_f32 v[128:129], v[128:129], v[132:133]\n\t" \
  "v_pk_add_f32 v[136:137], v[136:137], v[140:141]\n\t" \
  "v_pk_add_f32 v[128:129], v[128:129], v[136:137]\n\t" \
  "v_cvt_pk_bf16_f32 v128, v128, v129\n\t" \
  "s_nop 1\n\t" \
  "v_mov_b32_dpp v129, v128 quad_perm:[1,0,3,2] row_mask:0xf bank_mask:0xf\n\t" \
  "v_perm_b32 v128, v129, v128, v174\n\t" \
  "buffer_store_dword v128, v171, %[rs], %[sh] offen sc1\n\t" \
  "s_memtime s[90:91]\n\t" \
  "s_barrier\n\t" \
  "s_memtime s[76:77]\n\t" \
  "s_memrealtime s[78:79]\n\t" \
  "s_nop 3\n\t" \
  "s_memrealtime s[80:81]\n\t" \
  "s_nop 3\n\t" \
  "s_memrealtime s[82:83]\n\t" \
  "s_nop 3\n\t" \
  "s_memrealtime s[84:85]\n\t" \
  "s_nop 3\n\t" \
  "s_memrealtime s[86:87]\n\t" \
  "s_nop 3\n\t" \
  "s_memrealtime s[88:89]\n\t" \
  "s_mov_b32 s93, 0\n\t" \
  "s_waitcnt lgkmcnt(0)\n\t" \
  "s_cmp_eq_u32 s80, s78\n\t" \
  "s_addc_u32 s93, s93, 0\n\t" \
  "s_cmp_eq_u32 s82, s78\n\t" \
  "s_addc_u32 s93, s93, 0\n\t" \
  "s_cmp_eq_u32 s84, s78\n\t" \
  "s_addc_u32 s93, s93, 0\n\t" \
  "s_cmp_eq_u32 s86, s78\n\t" \
  "s_addc_u32 s93, s93, 0\n\t" \
  "s_cmp_eq_u32 s88, s78\n\t" \
  "s_addc_u32 s93, s93, 0\n\t" \
  "s_mul_i32 s93, s93, 21\n\t" \
  "s_add_u32 s76, s76, 15\n\t" \
  "s_add_u32 s76, s76, s93\n\t" \
  "s_lshl2_add_u32 s78, s78, 16\n\t" \
  "s_mov_b64 exec, 1\n\t" \
  "s_sub_i32 s90, s76, s90\n\t" \
  "s_mul_i32 s90, s90, 11900\n\t" \
  "s_ashr_i32 s90, s90, 16\n\t" \
  "s_sub_u32 s90, s78, s90\n\t" \
  "s_sub_u32 s90, s90, %[base]\n\t" \
  "v_mov_b32 v164, s90\n\t" \
  "ds_write_b32 v168, v164 offset:4108\n\t" \
  "s_mov_b64 exec, -1\n\t" \
  "s_waitcnt vmcnt(1)\n\t" \
  ".Lb2_%=:\n\t" \
  "s_setprio 0\n\t" \
  "s_bitcmp0_b64 %[am], %[step]\n\t" \
  "s_cbranch_scc1 .La1_%=\n\t" \
  "buffer_store_dwordx4 v[224:227], v228, %[ra], 0 offen sc1\n\t" \
  ".La1_%=:\n\t" \
  "buffer_load_dwordx4 v[192:195], v239, %[rt], %[st] offen offset:0 sc1\n\t" \
  "buffer_load_dwordx4 v[196:199], v239, %[rt], %[st] offen offset:1024 sc1\n\t" \
  "v_mfma_f32_16x16x32_bf16 v[128:131], v[0:3], v[176:179], 0\n\t" \
  "v_mfma_f32_16x16x32_bf16 v[136:139], v[8:11], v[176:179], 0\n\t" \
  "ds_read2st64_b32 v[144:145], v231 offset0:0 offset1:8\n\t" \
  "ds_read2st64_b32 v[146:147], v231 offset0:16 offset1:24\n\t" \
  "ds_read2st64_b32 v[148:149], v231 offset0:32 offset1:40\n\t" \
  "ds_read2st64_b32 v[150:151], v231 offset0:48 offset1:56\n\t" \
  "buffer_load_dwordx4 v[200:203], v239, %[rt], %[st4] offen offset:0 sc1\n\t" \
  "buffer_load_dwordx4 v[204:207], v239, %[rt], %[st4] offen offset:1024 sc1\n\t" \
  "v_mfma_f32_16x16x32_bf16 v[132:135], v[4:7], v[180:183], 0\n\t" \
  "v_mfma_f32_16x16x32_bf16 v[140:143], v[12:15], v[180:183], 0\n\t" \
  "s_waitcnt lgkmcnt(0)\n\t" \
  "v_add_f32 v144, v144, v145\n\t" \
  "v_add_f32 v146, v146, v147\n\t" \
  "v_add_f32 v148, v148, v149\n\t" \
  "v_add_f32 v150, v150, v151\n\t" \
  "v_add_f32 v144, v144, v146\n\t" \
  "v_add_f32 v148, v148, v150\n\t" \
  "buffer_load_dwordx4 v[208:211], v240, %[rt], %[st] offen offset:0 sc1\n\t" \
  "buffer_load_dwordx4 v[212:215], v240, %[rt], %[st] offen offset:1024 sc1\n\t" \
  "v_mfma_f32_16x16x32_bf16 v[128:131], v[16:19], v[184:187], v[128:131]\n\t" \
  "v_mfma_f32_16x16x32_bf16 v[136:139], v[24:27], v[184:187], v[136:139]\n\t" \
  "v_add_f32 v144, v144, v148\n\t" \
  "v_cvt_pk_bf16_f32 v144, v144, v144\n\t" \
  "ds_write_b16 v232, v144\n\t" \
  "ds_read_b128 v[224:227], v229\n\t" \
  "v_add_u32 v228, %[ainc], v228\n\t" \
  "v_add_u32 v229, %[rinc], v229\n\t" \
  "v_add_u32 v232, 1024, v232\n\t" \
  "v_xor_b32 v231, 0x4000, v231\n\t" \
  "buffer_load_dwordx4 v[216:219], v240, %[rt], %[st4] offen offset:0 sc1\n\t" \
  "buffer_load_dwordx4 v[220:223], v240, %[rt], %[st4] offen offset:1024 sc1\n\t" \
  "v_mfma_f32_16x16x32_bf16 v[132:135], v[20:23], v[188:191], v[132:135]\n\t" \
  "v_mfma_f32_16x16x32_bf16 v[140:143], v[28:31], v[188:191], v[140:143]\n\t" \
  "buffer_load_dwordx4 v[176:179], v241, %[rt], %[st] offen offset:0 sc1\n\t" \
  "s_waitcnt vmcnt(8)\n\t" \
  "v_mfma_f32_16x16x32_bf16 v[128:131], v[32:35], v[192:195], v[128:131]\n\t" \
  "v_mfma_f32_16x16x32_bf16 v[136:139], v[40:43], v[192:195], v[136:139]\n\t" \
  "buffer_load_dwordx4 v[180:183], v241, %[rt], %[st] offen offset:1024 sc1\n\t" \
  "s_waitcnt vmcnt(8)\n\t" \
  "v_mfma_f32_16x16x32_bf16 v[132:135], v[36:39], v[196:199], v[132:135]\n\t" \
  "v_mfma_f32_16x16x32_bf16 v[140:143], v[44:47], v[196:199], v[140:143]\n\t" \
  "buffer_load_dwordx4 v[184:187], v241, %[rt], %[st4] offen offset:0 sc1\n\t" \
  "s_waitcnt vmcnt(8)\n\t" \
  "v_mfma_f32_16x16x32_bf16 v[128:131], v[48:51], v[200:203], v[128:131]\n\t" \
  "v_mfma_f32_16x16x32_bf16 v[136:139], v[56:59], v[200:203], v[136:139]\n\t" \
  "ds_read_b128 v[160:163], v168 offset:4096\n\t" \
  "buffer_load_dwordx4 v[188:191], v241, %[rt], %[st4] offen offset:1024 sc1\n\t" \
  "s_waitcnt vmcnt(8)\n\t" \
  "v_mfma_f32_16x16x32_bf16 v[132:135], v[52:55], v[204:207], v[132:135]\n\t" \
  "v_mfma_f32_16x16x32_bf16 v[140:143], v[60:63], v[204:207], v[140:143]\n\t" \
  "s_waitcnt lgkmcnt(0)\n\t" \
  "v_readfirstlane_b32 s90, v161\n\t" \
  "v_readfirstlane_b32 s91, v163\n\t" \
  "v_readfirstlane_b32 s94, v160\n\t" \
  "s_sub_u32 s93, s94, %[sm]\n\t" \
  "s_mov_b32 %[sm], s94\n\t" \
  "s_lshr_b32 s93, s93, 3\n\t" \
  "s_sub_i32 s93, s90, s93\n\t" \
  "s_mul_i32 s93, s93, %[wm]\n\t" \
  "s_ashr_i32 s93, s93, 2\n\t" \
  "s_min_u32 s93, s93, %[gm]\n\t" \
  "s_sub_i32 s90, s90, s93\n\t" \
  "s_add_u32 s93, s90, %[rl]\n\t" \
  "s_add_u32 s93, s93, %[base]\n\t" \
  "s_add_u32 %[E], s93, %[lk]\n\t" \
  "s_sub_i32 s91, s91, s90\n\t" \
  "s_sub_i32 s91, s91, %[rl]\n\t" \
  "s_min_i32 s93, s91, %[up]\n\t" \
  "s_max_i32 s93, s93, %[dn]\n\t" \
  "s_cmp_gt_i32 s91, 60\n\t" \
  "s_cselect_b32 s93, 2, s93\n\t" \
  "s_add_i32 %[rl], %[rl], s93\n\t" \
  "v_mov_b32 v160, %[rl]\n\t" \
  "buffer_store_dword v160, v175, %[rs], 0 offen sc1\n\t" \
  "s_waitcnt vmcnt(8)\n\t" \
  "v_mfma_f32_16x16x32_bf16 v[128:131], v[64:67], v[208:211], v[128:131]\n\t" \
  "v_mfma_f32_16x16x32_bf16 v[136:139], v[72:75], v[208:211], v[136:139]\n\t" \
  "s_add_u32 %[step], %[step], 1\n\t" \
  "s_lshl_b32 %[sh], %[step], 15\n\t" \
  "s_lshr_b32 s93, %[step], 4\n\t" \
  "s_lshr_b32 s93, %[pm], s93\n\t" \
  "s_and_b32 s93, s93, 4\n\t" \
  "s_lshl_b32 s93, s93, 11\n\t" \
  "s_lshl2_add_u32 %[so], %[sh], s93\n\t" \
  "s_waitcnt vmcnt(7)\n\t" \
  "v_mfma_f32_16x16x32_bf16 v[132:135], v[68:71], v[212:215], v[132:135]\n\t" \
  "v_mfma_f32_16x16x32_bf16 v[140:143], v[76:79], v[212:215], v[140:143]\n\t" \
  "s_sub_i32 s93, %[step], 2\n\t" \
  "s_lshl_b32 %[st], s93, 19\n\t" \
  "s_lshl_b32 s94, s93, 11\n\t" \
  "s_xor_b32 s94, s94, %[s0]\n\t" \
  "s_and_b32 s94, s94, 0x2000\n\t" \
  "s_or_b32 %[st], %[st], s94\n\t" \
  "s_cmp_lt_i32 s93, 0\n\t" \
  "s_cselect_b32 %[st], 0x70000000, %[st]\n\t" \
  "s_add_u32 %[st4], %[st], 4096\n\t" \
  "s_waitcnt vmcnt(6)\n\t" \
  "v_mfma_f32_16x16x32_bf16 v[128:131], v[80:83], v[216:219], v[128:131]\n\t" \
  "v_mfma_f32_16x16x32_bf16 v[136:139], v[88:91], v[216:219], v[136:139]\n\t" \
  "s_waitcnt vmcnt(5)\n\t" \
  "v_mfma_f32_16x16x32_bf16 v[132:135], v[84:87], v[220:223], v[132:135]\n\t" \
  "v_mfma_f32_16x16x32_bf16 v[140:143], v[92:95], v[220:223], v[140:143]\n\t" \
  "s_cmp_eq_u32 %[step], 63\n\t" \
  "s_cbranch_scc1 .Llast_%=\n\t" \
  "s_movk_i32 s93, 600\n\t" \
  ".Lpacel_%=:\n\t" \
  "s_memrealtime s[90:91]\n\t" \
  "s_waitcnt lgkmcnt(0)\n\t" \
  "s_lshl_b32 s90, s90, 2\n\t" \
  "s_sub_i32 s91, %[E], s90\n\t" \
  "s_cmp_le_i32 s91, 0\n\t" \
  "s_cbranch_scc1 .Lgol_%=\n\t" \
  "s_sub_u32 s93, s93, 1\n\t" \
  "s_cmp_lg_u32 s93, 0\n\t" \
  "s_cbranch_scc1 .Lpacel_%=\n\t" \
  ".Lgol_%=:\n\t" \
  "buffer_load_dwordx2 v[166:167], v173, %[rs], 0 offen sc1\n\t" \
  "s_waitcnt vmcnt(5)\n\t" \
  "v_mfma_f32_16x16x32_bf16 v[128:131], v[96:99], v[176:179], v[128:131]\n\t" \
  "v_mfma_f32_16x16x32_bf16 v[136:139], v[104:107], v[176:179], v[136:139]\n\t" \
  "s_waitcnt vmcnt(4)\n\t" \
  "v_mfma_f32_16x16x32_bf16 v[132:135], v[100:103], v[180:183], v[132:135]\n\t" \
  "v_mfma_f32_16x16x32_bf16 v[140:143], v[108:111], v[180:183], v[140:143]\n\t" \
  "s_waitcnt vmcnt(3)\n\t" \
  "v_mfma_f32_16x16x32_bf16 v[128:131], v[112:115], v[184:187], v[128:131]\n\t" \
  "v_mfma_f32_16x16x32_bf16 v[136:139], v[120:123], v[184:187], v[136:139]\n\t" \
  "s_waitcnt vmcnt(2)\n\t" \
  "v_mfma_f32_16x16x32_bf16 v[132:135], v[116:119], v[188:191], v[132:135]\n\t" \
  "v_mfma_f32_16x16x32_bf16 v[140:143], v[124:127], v[188:191], v[140:143]\n\t" \
  "v_mov_b64 v[144:145], 0\n\t" \
  "v_mov_b64 v[146:147], 0\n\t" \
  "v_mov_b64 v[148:149], 0\n\t" \
  "v_mov_b64 v[150:151], 0\n\t" \
  "v_mov_b64 v[152:153], 0\n\t" \
  "v_mov_b64 v[154:155], 0\n\t" \
  "v_mov_b64 v[156:157], 0\n\t" \
  "v_mov_b64 v[158:159], 0\n\t" \
  "v_pk_add_f32 v[128:129], v[128:129], v[132:133]\n\t" \
  "v_pk_add_f32 v[130:131], v[130:131], v[134:135]\n\t" \
  "ds_write_b128 v230, v[128:131] offset:0\n\t" \
  "v_pk_add_f32 v[136:137], v[136:137], v[140:141]\n\t" \
  "v_pk_add_f32 v[138:139], v[138:139], v[142:143]\n\t" \
  "ds_write_b128 v230, v[136:139] offset:1024\n\t" \
  "v_xor_b32 v230, 0x4000, v230\n\t" \
  "s_cmp_lg_u32 %[step], 63\n\t" \
  "s_cbranch_scc1 .Lstep_%=\n\t" \
  ".Lfc_%=:\n\t" \
  "s_waitcnt vmcnt(0)\n\t" \
  "v_cmp_eq_u32 vcc, -1, v166\n\t" \
  "s_cbranch_vccnz .Lfw_%=\n\t" \
  ".Lfo_%=:\n\t" \
  "v_add_u32 v242, 16384, v242\n\t" \
  "buffer_load_dwordx4 v[176:179], v238, %[rt], %[st] offen offset:0 sc1\n\t" \
  "buffer_load_dwordx4 v[180:183], v238, %[rt], %[st] offen offset:1024 sc1\n\t" \
  "buffer_load_dwordx4 v[184:187], v238, %[rt], %[st4] offen offset:0 sc1\n\t" \
  "buffer_load_dwordx4 v[188:191], v238, %[rt], %[st4] offen offset:1024 sc1\n\t" \
  "buffer_load_dwordx4 v[192:195], v239, %[rt], %[st] offen offset:0 sc1\n\t" \
  "buffer_load_dwordx4 v[196:199], v239, %[rt], %[st] offen offset:1024 sc1\n\t" \
  "buffer_load_dwordx4 v[200:203], v239, %[rt], %[st4] offen offset:0 sc1\n\t" \
  "buffer_load_dwordx4 v[204:207], v239, %[rt], %[st4] offen offset:1024 sc1\n\t" \
  "v_subrev_u32 v230, 98304, v230\n\t" \
  ".Lt_%=:\n\t" \
  "s_waitcnt vmcnt(7)\n\t" \
  "v_mfma_f32_16x16x32_bf16 v[128:131], v[0:3], v[176:179], 0\n\t" \
  "v_mfma_f32_16x16x32_bf16 v[136:139], v[8:11], v[176:179], 0\n\t" \
  "buffer_load_dwordx4 v[176:179], v240, %[rt], %[st] offen offset:0 sc1\n\t" \
  "s_waitcnt vmcnt(7)\n\t" \
  "v_mfma_f32_16x16x32_bf16 v[132:135], v[4:7], v[180:183], 0\n\t" \
  "v_mfma_f32_16x16x32_bf16 v[140:143], v[12:15], v[180:183], 0\n\t" \
  "buffer_load_dwordx4 v[180:183], v240, %[rt], %[st] offen offset:1024 sc1\n\t" \
  "s_waitcnt vmcnt(7)\n\t" \
  "v_mfma_f32_16x16x32_bf16 v[128:131], v[16:19], v[184:187], v[128:131]\n\t" \
  "v_mfma_f32_16x16x32_bf16 v[136:139], v[24:27], v[184:187], v[136:139]\n\t" \
  "buffer_load_dwordx4 v[184:187], v240, %[rt], %[st4] offen offset:0 sc1\n\t" \
  "s_waitcnt vmcnt(7)\n\t" \
  "v_mfma_f32_16x16x32_bf16 v[132:135], v[20:23], v[188:191], v[132:135]\n\t" \
  "v_mfma_f32_16x16x32_bf16 v[140:143], v[28:31], v[188:191], v[140:143]\n\t" \
  "buffer_load_dwordx4 v[188:191], v240, %[rt], %[st4] offen offset:1024 sc1\n\t" \
  "buffer_load_dword v166, v242, %[rs], 0 offen sc1\n\t" \
  "s_waitcnt vmcnt(8)\n\t" \
  "v_mfma_f32_16x16x32_bf16 v[128:131], v[32:35], v[192:195], v[128:131]\n\t" \
  "v_mfma_f32_16x16x32_bf16 v[136:139], v[40:43], v[192:195], v[136:139]\n\t" \
  "buffer_load_dwordx4 v[192:195], v241, %[rt], %[st] offen offset:0 sc1\n\t" \
  "s_waitcnt vmcnt(8)\n\t" \
  "v_mfma_f32_16x16x32_bf16 v[132:135], v[36:39], v[196:199], v[132:135]\n\t" \
  "v_mfma_f32_16x16x32_bf16 v[140:143], v[44:47], v[196:199], v[140:143]\n\t" \
  "buffer_load_dwordx4 v[196:199], v241, %[rt], %[st] offen offset:1024 sc1\n\t" \
  "s_waitcnt vmcnt(8)\n\t" \
  "v_mfma_f32_16x16x32_bf16 v[128:131], v[48:51], v[200:203], v[128:131]\n\t" \
  "v_mfma_f32_16x16x32_bf16 v[136:139], v[56:59], v[200:203], v[136:139]\n\t" \
  "buffer_load_dwordx4 v[200:203], v241, %[rt], %[st4] offen offset:0 sc1\n\t" \
  "s_waitcnt vmcnt(8)\n\t" \
  "v_mfma_f32_16x16x32_bf16 v[132:135], v[52:55], v[204:207], v[132:135]\n\t" \
  "v_mfma_f32_16x16x32_bf16 v[140:143], v[60:63], v[204:207], v[140:143]\n\t" \
  "buffer_load_dwordx4 v[204:207], v241, %[rt], %[st4] offen offset:1024 sc1\n\t" \
  "s_waitcnt vmcnt(8)\n\t" \
  "v_mfma_f32_16x16x32_bf16 v[128:131], v[64:67], v[176:179], v[128:131]\n\t" \
  "v_mfma_f32_16x16x32_bf16 v[136:139], v[72:75], v[176:179], v[136:139]\n\t" \
  "s_waitcnt vmcnt(4)\n\t" \
  ".Ltc_%=:\n\t" \
  "v_cmp_eq_u32 vcc, -1, v166\n\t" \
  "s_cbranch_vccnz .Ltw_%=\n\t" \
  ".Lto_%=:\n\t" \
  "s_cmp_eq_u32 %[step], 65\n\t" \
  "s_cbranch_scc1 .Ltl_%=\n\t" \
  "s_add_u32 %[step], %[step], 1\n\t" \
  "s_sub_i32 s93, %[step], 2\n\t" \
  "s_lshl_b32 %[st], s93, 19\n\t" \
  "s_lshl_b32 s94, s93, 11\n\t" \
  "s_xor_b32 s94, s94, %[s0]\n\t" \
  "s_and_b32 s94, s94, 0x2000\n\t" \
  "s_or_b32 %[st], %[st], s94\n\t" \
  "s_cmp_lt_i32 s93, 0\n\t" \
  "s_cselect_b32 %[st], 0x70000000, %[st]\n\t" \
  "s_add_u32 %[st4], %[st], 4096\n\t" \
  "v_add_u32 v242, 16384, v242\n\t" \
  "s_cmp_eq_u32 %[step], 65\n\t" \
  "s_cbranch_scc0 .Ltn_%=\n\t" \
  "v_mov_b32 v242, %[fg]\n\t" \
  ".Ltn_%=:\n\t" \
  "buffer_load_dwordx4 v[176:179], v238, %[rt], %[st] offen offset:0 sc1\n\t" \
  "s_waitcnt vmcnt(7)\n\t" \
  "v_mfma_f32_16x16x32_bf16 v[132:135], v[68:71], v[180:183], v[132:135]\n\t" \
  "v_mfma_f32_16x16x32_bf16 v[140:143], v[76:79], v[180:183], v[140:143]\n\t" \
  "buffer_load_dwordx4 v[180:183], v238, %[rt], %[st] offen offset:1024 sc1\n\t" \
  "s_waitcnt vmcnt(7)\n\t" \
  "v_mfma_f32_16x16x32_bf16 v[128:131], v[80:83], v[184:187], v[128:131]\n\t" \
  "v_mfma_f32_16x16x32_bf16 v[136:139], v[88:91], v[184:187], v[136:139]\n\t" \
  "buffer_load_dwordx4 v[184:187], v238, %[rt], %[st4] offen offset:0 sc1\n\t" \
  "s_waitcnt vmcnt(7)\n\t" \
  "v_mfma_f32_16x16x32_bf16 v[132:135], v[84:87], v[188:191], v[132:135]\n\t" \
  "v_mfma_f32_16x16x32_bf16 v[140:143], v[92:95], v[188:191], v[140:143]\n\t" \
  "buffer_load_dwordx4 v[188:191], v238, %[rt], %[st4] offen offset:1024 sc1\n\t" \
  "s_waitcnt vmcnt(7)\n\t" \
  "v_mfma_f32_16x16x32_bf16 v[128:131], v[96:99], v[192:195], v[128:131]\n\t" \
  "v_mfma_f32_16x16x32_bf16 v[136:139], v[104:107], v[192:195], v[136:139]\n\t" \
  "buffer_load_dwordx4 v[192:195], v239, %[rt], %[st] offen offset:0 sc1\n\t" \
  "s_waitcnt vmcnt(7)\n\t" \
  "v_mfma_f32_16x16x32_bf16 v[132:135], v[100:103], v[196:199], v[132:135]\n\t" \
  "v_mfma_f32_16x16x32_bf16 v[140:143], v[108:111], v[196:199], v[140:143]\n\t" \
  "buffer_load_dwordx4 v[196:199], v239, %[rt], %[st] offen offset:1024 sc1\n\t" \
  "s_waitcnt vmcnt(7)\n\t" \
  "v_mfma_f32_16x16x32_bf16 v[128:131], v[112:115], v[200:203], v[128:131]\n\t" \
  "v_mfma_f32_16x16x32_bf16 v[136:139], v[120:123], v[200:203], v[136:139]\n\t" \
  "buffer_load_dwordx4 v[200:203], v239, %[rt], %[st4] offen offset:0 sc1\n\t" \
  "s_waitcnt vmcnt(7)\n\t" \
  "v_mfma_f32_16x16x32_bf16 v[132:135], v[116:119], v[204:207], v[132:135]\n\t" \
  "v_mfma_f32_16x16x32_bf16 v[140:143], v[124:127], v[204:207], v[140:143]\n\t" \
  "buffer_load_dwordx4 v[204:207], v239, %[rt], %[st4] offen offset:1024 sc1\n\t" \
  "s_nop 5\n\t" \
  "v_pk_add_f32 v[128:129], v[128:129], v[132:133]\n\t" \
  "v_pk_add_f32 v[130:131], v[130:131], v[134:135]\n\t" \
  "ds_write_b128 v230, v[128:131] offset:0\n\t" \
  "v_pk_add_f32 v[136:137], v[136:137], v[140:141]\n\t" \
  "v_pk_add_f32 v[138:139], v[138:139], v[142:143]\n\t" \
  "ds_write_b128 v230, v[136:139] offset:1024\n\t" \
  "v_add_u32 v230, 16384, v230\n\t" \
  "s_branch .Lt_%=\n\t" \
  ".Ltl_%=:\n\t" \
  "buffer_load_dwordx4 v[224:227], v243, %[rg], 0 offen sc1\n\t" \
  "s_waitcnt vmcnt(7)\n\t" \
  "v_mfma_f32_16x16x32_bf16 v[132:135], v[68:71], v[180:183], v[132:135]\n\t" \
  "v_mfma_f32_16x16x32_bf16 v[140:143], v[76:79], v[180:183], v[140:143]\n\t" \
  "s_waitcnt vmcnt(6)\n\t" \
  "v_mfma_f32_16x16x32_bf16 v[128:131], v[80:83], v[184:187], v[128:131]\n\t" \
  "v_mfma_f32_16x16x32_bf16 v[136:139], v[88:91], v[184:187], v[136:139]\n\t" \
  "s_waitcnt vmcnt(5)\n\t" \
  "v_mfma_f32_16x16x32_bf16 v[132:135], v[84:87], v[188:191], v[132:135]\n\t" \
  "v_mfma_f32_16x16x32_bf16 v[140:143], v[92:95], v[188:191], v[140:143]\n\t" \
  "s_waitcnt vmcnt(4)\n\t" \
  "v_mfma_f32_16x16x32_bf16 v[128:131], v[96:99], v[192:195], v[128:131]\n\t" \
  "v_mfma_f32_16x16x32_bf16 v[136:139], v[104:107], v[192:195], v[136:139]\n\t" \
  "s_waitcnt vmcnt(3)\n\t" \
  "v_mfma_f32_16x16x32_bf16 v[132:135], v[100:103], v[196:199], v[132:135]\n\t" \
  "v_mfma_f32_16x16x32_bf16 v[140:143], v[108:111], v[196:199], v[140:143]\n\t" \
  "s_waitcnt vmcnt(2)\n\t" \
  "v_mfma_f32_16x16x32_bf16 v[128:131], v[112:115], v[200:203], v[128:131]\n\t" \
  "v_mfma_f32_16x16x32_bf16 v[136:139], v[120:123], v[200:203], v[136:139]\n\t" \
  "s_waitcnt vmcnt(1)\n\t" \
  "v_mfma_f32_16x16x32_bf16 v[132:135], v[116:119], v[204:207], v[132:135]\n\t" \
  "v_mfma_f32_16x16x32_bf16 v[140:143], v[124:127], v[204:207], v[140:143]\n\t" \
  "s_nop 6\n\t" \
  "v_pk_add_f32 v[128:129], v[128:129], v[132:133]\n\t" \
  "v_pk_add_f32 v[130:131], v[130:131], v[134:135]\n\t" \
  "ds_write_b128 v230, v[128:131] offset:0\n\t" \
  "v_pk_add_f32 v[136:137], v[136:137], v[140:141]\n\t" \
  "v_pk_add_f32 v[138:139], v[138:139], v[142:143]\n\t" \
  "ds_write_b128 v230, v[136:139] offset:1024\n\t" \
  "s_waitcnt vmcnt(0) lgkmcnt(0)\n\t" \
  "s_branch .Lend_%=\n\t" \
  ".Ltw_%=:\n\t" \
  "s_add_u32 s92, s92, 1\n\t" \
  "s_sleep 2\n\t" \
  "s_and_b32 s93, s92, 63\n\t" \
  "s_cbranch_scc1 .Ltr_%=\n\t" \
  "buffer_load_dword v164, off, %[rs], %[sab] sc1\n\t" \
  "s_waitcnt vmcnt(0)\n\t" \
  "v_readfirstlane_b32 s93, v164\n\t" \
  "s_nop 3\n\t" \
  "s_cmp_eq_u32 s93, 1\n\t" \
  "s_cbranch_scc1 .Lto_%=\n\t" \
  "s_cmp_lt_u32 s92, 0x80000\n\t" \
  "s_cbranch_scc1 .Ltr_%=\n\t" \
  "v_mov_b32 v164, 1\n\t" \
  "buffer_store_dword v164, off, %[rs], %[sab] sc1\n\t" \
  "s_waitcnt vmcnt(0)\n\t" \
  "s_branch .Lto_%=\n\t" \
  ".Ltr_%=:\n\t" \
  "buffer_load_dword v166, v242, %[rs], 0 offen sc1\n\t" \
  "s_waitcnt vmcnt(0)\n\t" \
  "s_branch .Ltc_%=\n\t" \
  ".Llast_%=:\n\t" \
  "v_mov_b32 v173, v242\n\t" \
  "s_branch .Lgol_%=\n\t" \
  ".Lfw_%=:\n\t" \
  "s_add_u32 s92, s92, 1\n\t" \
  "s_sleep 2\n\t" \
  "s_and_b32 s93, s92, 63\n\t" \
  "s_cbranch_scc1 .Lfr_%=\n\t" \
  "buffer_load_dword v164, off, %[rs], %[sab] sc1\n\t" \
  "s_waitcnt vmcnt(0)\n\t" \
  "v_readfirstlane_b32 s93, v164\n\t" \
  "s_nop 3\n\t" \
  "s_cmp_eq_u32 s93, 1\n\t" \
  "s_cbranch_scc1 .Lfo_%=\n\t" \
  "s_cmp_lt_u32 s92, 0x80000\n\t" \
  "s_cbranch_scc1 .Lfr_%=\n\t" \
  "v_mov_b32 v164, 1\n\t" \
  "buffer_store_dword v164, off, %[rs], %[sab] sc1\n\t" \
  "s_waitcnt vmcnt(0)\n\t" \
  "s_branch .Lfo_%=\n\t" \
  ".Lfr_%=:\n\t" \
  "buffer_load_dwordx2 v[166:167], v173, %[rs], 0 offen sc1\n\t" \
  "s_branch .Lfc_%=\n\t" \
  ".Lb1_%=:\n\t" \
  "s_waitcnt vmcnt(0) lgkmcnt(0)\n\t" \
  "s_barrier\n\t" \
  "s_branch .Lb2_%=\n\t" \
  ".Lbl_%=:\n\t" \
  "s_bitset1_b64 %[ps], %[step]\n\t" \
  "s_cmp_lt_u32 s92, 3\n\t" \
  "s_cbranch_scc1 .Lbm_%=\n\t" \
  "s_sleep 12\n\t" \
  ".Lbm_%=:\n\t" \
  "s_add_u32 s92, s92, 1\n\t" \
  "s_sleep 2\n\t" \
  "s_and_b32 s93, s92, 63\n\t" \
  "s_cbranch_scc1 .Lba_%=\n\t" \
  "buffer_load_dword v164, off, %[rs], %[sab] sc1\n\t" \
  "s_waitcnt vmcnt(0)\n\t" \
  "v_readfirstlane_b32 s93, v164\n\t" \
  "s_nop 3\n\t" \
  "s_cmp_eq_u32 s93, 1\n\t" \
  "s_cbranch_scc1 .Lgo_%=\n\t" \
  "s_cmp_lt_u32 s92, 0x80000\n\t" \
  "s_cbranch_scc1 .Lba_%=\n\t" \
  "v_mov_b32 v164, 1\n\t" \
  "buffer_store_dword v164, off, %[rs], %[sab] sc1\n\t" \
  "s_waitcnt vmcnt(0)\n\t" \
  "s_branch .Lgo_%=\n\t" \
  ".Lba_%=:\n\t" \
  "buffer_load_dwordx2 v[166:167], v173, %[rs], 0 offen sc1\n\t" \
  "s_branch .Lcal_%=\n\t" \
  ".Lslow_%=:\n\t" \
  "s_mov_b64 s[94:95], 0\n\t" \
  "v_cmp_eq_u32 vcc, %[sent], v128\n\t" \
  "s_or_b64 s[94:95], s[94:95], vcc\n\t" \
  "v_cmp_eq_u32 vcc, %[sent], v129\n\t" \
  "s_or_b64 s[94:95], s[94:95], vcc\n\t" \
  "v_cmp_eq_u32 vcc, %[sent], v130\n\t" \
  "s_or_b64 s[94:95], s[94:95], vcc\n\t" \
  "v_cmp_eq_u32 vcc, %[sent], v131\n\t" \
  "s_or_b64 s[94:95], s[94:95], vcc\n\t" \
  "v_cmp_eq_u32 vcc, %[sent], v132\n\t" \
  "s_or_b64 s[94:95], s[94:95], vcc\n\t" \
  "v_cmp_eq_u32 vcc, %[sent], v133\n\t" \
  "s_or_b64 s[94:95], s[94:95], vcc\n\t" \
  "v_cmp_eq_u32 vcc, %[sent], v134\n\t" \
  "s_or_b64 s[94:95], s[94:95], vcc\n\t" \
  "v_cmp_eq_u32 vcc, %[sent], v135\n\t" \
  "s_or_b64 s[94:95], s[94:95], vcc\n\t" \
  "v_cmp_eq_u32 vcc, %[sent], v136\n\t" \
  "s_or_b64 s[94:95], s[94:95], vcc\n\t" \
  "v_cmp_eq_u32 vcc, %[sent], v137\n\t" \
  "s_or_b64 s[94:95], s[94:95], vcc\n\t" \
  "v_cmp_eq_u32 vcc, %[sent], v138\n\t" \
  "s_or_b64 s[94:95], s[94:95], vcc\n\t" \
  "v_cmp_eq_u32 vcc, %[sent], v139\n\t" \
  "s_or_b64 s[94:95], s[94:95], vcc\n\t" \
  "v_cmp_eq_u32 vcc, %[sent], v140\n\t" \
  "s_or_b64 s[94:95], s[94:95], vcc\n\t" \
  "v_cmp_eq_u32 vcc, %[sent], v141\n\t" \
  "s_or_b64 s[94:95], s[94:95], vcc\n\t" \
  "v_cmp_eq_u32 vcc, %[sent], v142\n\t" \
  "s_or_b64 s[94:95], s[94:95], vcc\n\t" \
  "v_cmp_eq_u32 vcc, %[sent], v143\n\t" \
  "s_or_b64 s[94:95], s[94:95], vcc\n\t" \
  "s_cbranch_scc0 .Lok_%=\n\t" \
  "s_bitset1_b64 %[fm], %[step]\n\t" \
  "s_add_u32 s92, s92, 1\n\t" \
  "s_sleep 1\n\t" \
  "s_and_b32 s93, s92, 63\n\t" \
  "s_cbranch_scc1 .Lre_%=\n\t" \
  "buffer_load_dword v164, off, %[rs], %[sab] sc1\n\t" \
  "s_waitcnt vmcnt(0)\n\t" \
  "v_readfirstlane_b32 s93, v164\n\t" \
  "s_nop 3\n\t" \
  "s_cmp_eq_u32 s93, 1\n\t" \
  "s_cbranch_scc1 .Lok_%=\n\t" \
  "s_cmp_lt_u32 s92, 0x80000\n\t" \
  "s_cbranch_scc1 .Lre_%=\n\t" \
  "v_mov_b32 v164, 1\n\t" \
  "buffer_store_dword v164, off, %[rs], %[sab] sc1\n\t" \
  "s_waitcnt vmcnt(0)\n\t" \
  "s_branch .Lok_%=\n\t" \
  ".Lre_%=:\n\t" \
  "buffer_load_dwordx4 v[128:131], v234, %[rs], %[so] offen sc1\n\t" \
  "s_nop 15\n\t" \
  "s_nop 1\n\t" \
  "buffer_load_dwordx4 v[132:135], v235, %[rs], %[so] offen sc1\n\t" \
  "s_nop 15\n\t" \
  "s_nop 1\n\t" \
  "buffer_load_dwordx4 v[136:139], v236, %[rs], %[so] offen sc1\n\t" \
  "s_nop 15\n\t" \
  "s_nop 1\n\t" \
  "buffer_load_dwordx4 v[140:143], v237, %[rs], %[so] offen sc1\n\t" \
  "v_mov_b64 v[144:145], 0\n\t" \
  "v_mov_b64 v[146:147], 0\n\t" \
  "v_mov_b64 v[148:149], 0\n\t" \
  "v_mov_b64 v[150:151], 0\n\t" \
  "v_mov_b64 v[152:153], 0\n\t" \
  "v_mov_b64 v[154:155], 0\n\t" \
  "v_mov_b64 v[156:157], 0\n\t" \
  "v_mov_b64 v[158:159], 0\n\t" \
  "s_waitcnt vmcnt(0)\n\t" \
  "s_branch .Lw_%=\n\t" \
  ".Lend_%=:\n\t"


__device__ __forceinline__ int look_prog(__amdgpu_buffer_rsrc_t rs, int off) {
  asm volatile("" ::: "memory");
  const int v = (int)ld_flag(rs, off);
  return min(__builtin_amdgcn_readlane(v, 0), __builtin_amdgcn_readlane(v, 1));
}
__device__ __forceinline__ int wait_prog(__amdgpu_buffer_rsrc_t rs, int off, int need, gu32* abortw) {
  int v;
  for (unsigned spins = 0;;) {
    v = look_prog(rs, off);
    if (v >= need) break;
    __builtin_amdgcn_s_sleep(4);
    if (spin_fail(spins, abortw)) break;
  }
  asm volatile("" ::: "memory");
  return v;
}
#define CLB_S "s90", "s91", "s92", "s93", "s94", "s95", "vcc", "scc", "memory"
#define W_IN "{v[0:31]}"(W[0]), "{v[32:63]}"(W[1]), "{v[64:95]}"(W[2]), "{v[96:127]}"(W[3]), "{v[128:159]}"(W[4]), "{v[160:191]}"(W[5])

#define LEAD0 396
#define LEAD_N0 4
#define LEAD_SH 1
#define LEAD_X0 8
#define LEAD_U0 24
#define LEAD_D0 0
#define T0_BACK 1000
#define QBJ 3
constexpr int L_MEM = 0, L_KT = 16384, L_QX = 24576, L_KX = 26624, L_VX = 16384, L_MISC = 30720, L_WL = 32768;

#define QK_STEP(TXT)                                                                                                        \
  asm volatile(TXT                                                                                                          \
      : [tout] "=&s"(tout), [spins] "=&s"(spins), [tb1] "=&s"(tb1), [tgt] "=&s"(tgt), "+{v248}"(aL16), "+{v249}"(aMW), "+{v250}"(aWL),                            \
        "+{v251}"(aIDX), "+{v252}"(aQW), "+{v[232:235]}"(QF)                                                                 \
      : W_IN, [rs] "s"(rs4), [som] "s"(o_mem(pm, s) + WV * 1024), [som2] "s"(o_mem(pm, s) + WV * 1024 + 16384), [sent] "s"(DSENT), [sab] "s"(SY_ABORT), \
        [lead] "s"(lead), [step] "s"(s), [sbc] "s"(SY_BA + side(pm, 1) + h * 128), [nh] "s"(0xFF00FF00FF00FF00ull), [one] "s"(0x3F803F803F803F80ull), [up] "s"(0xFFFFFFFF00000000ull), [negc] "s"(-cexp),      \
        [c] "s"(cexp), [so] "s"(o_o(pm, s, h) + WV * 512), [sk] "s"(o_v(pm, s, h) + 2048 + (WV & 3) * 512),                    \
        [sv] "s"(o_v(pm, s, h) + WV * 512), [rt] "s"(rt4), [svt] "s"(t_off(p, T_V + kseg(s, h)) + WV * 2048)                  \
      : CLB8(192, 193, 194, 195, 196, 197, 198, 199), CLB8(200, 201, 202, 203, 204, 205, 206, 207),                          \
        CLB8(208, 209, 210, 211, 212, 213, 214, 215), CLB8(216, 217, 218, 219, 220, 221, 222, 223),                          \
        CLB8(224, 225, 226, 227, 228, 229, 230, 231), CLB8(236, 237, 238, 239, 240, 241, 242, 243), "v244", "v245", "v246", "v247", "v253", "s88", "s89", CLB_S)

template <int WV>
__device__ __forceinline__ void qk_wave(Args& p, int h, char* smem) {
  const int tid = threadIdx.x;
  unsigned pm = 0;
  int have = -1;
  u32x32 W[6];
  {
    int lane = tid & 63;
    asm volatile("" : "+v"(lane));
    const unsigned vo = (WV * 4 + (lane >> 4)) * (HD * 2) + 16 * ((lane & 15) ^ swzB((WV * 4 + (lane >> 4)) & 15));
    auto stage = [&](int j) {
#pragma unroll
      for (int mm = 0; mm < 2; ++mm) {
        const char* src = (const char*)((mm ? p.Wk : p.Wq) + (size_t)j * 128 * HD + h * DH);
        const unsigned dst = lds_off(smem) + (~j & 1) * 65536 + mm * 32768 + WV * 1024;
        glds2(src, 32 * HD * 2, vo, dst, true);
        if (TH_Q < 63) HOLD_V((h & 3) ^ 1, TH_Q);
        glds2(src + 64 * HD * 2, 32 * HD * 2, vo, dst + 16384, true);
        if (TH_Q < 63) HOLD_V((h & 3) ^ 1, TH_Q);
      }
    };
    bf16x8 keep[4][4];
    int f0 = -1;
    auto place = [&](int g) {
#pragma unroll
      for (int i = 0; i < 4; ++i)
        *reinterpret_cast<bf16x8*>(smem + (g == 0 ? 65536 : g == 1 ? 32768 : g == 2 ? 131072 : 98304) + WV * 4096 + i * 1024 + lane * 16) = keep[g][i];
    };
    stage(0); stage(1);
    Boot bt;
#pragma unroll
    for (int j = 0; j < 8; ++j) {
      if (j < 7) WAIT_V(8); else WAIT_V(0);
      if (WV == 7 && j == QBJ + 1) pm = boot_done(p.ws, bt);
      if ((WV == 4 || WV == 5) && j == 7) {
        asm volatile("" : "+v"(f0));
        have = min(__builtin_amdgcn_readlane(f0, 0), __builtin_amdgcn_readlane(f0, 1));
      }
      LBAR();
#pragma unroll
      for (int k = 0; k < 8; ++k) {
        const int P = (QK_MAP[WV] >> (4 * (2 * j + (k >> 2)))) & 15;
        const bf16x8 fr = ld_bfrag(smem + (~j & 1) * 65536 + (WV >> 2) * 32768 + (k >> 2) * 16384, k & 1, 2 * (WV & 3) + ((k >> 1) & 1), lane);
        const int f = P * 4 + (k & 3);
        if (P < 12) put4(W[f >> 3], f & 7, fr);
        else keep[P - 12][k & 3] = fr;
      }
      if (j == (WV >> 1)) place(2);
      LBAR();
      if (WV == 7 && j == QBJ) boot_ask(p.ws, bt);
      if (j + 2 < 8) stage(j + 2);
      if (j == 6) {
        if (WV == 4 || WV == 5)
          asm volatile("buffer_load_dword %0, %1, %2, 0 offen sc1" : "=&v"(f0) : "v"(fkq(h, (lane & 1) * 2)), "s"(raw_rsrc(p.ws + WS_SY, SY_TOTAL)) : "memory");
        place(0); place(3);
      }
    }
    place(1);
    if (WV == 7 && lane < 8) lds_at(smem + L_MISC)[lane] = lane == 7 ? pm : (lane == 1 ? now_q() - T0_BACK : 0);
    if (WV == 6) lds_at(smem + 28672)[lane] = ~0u;
    LBAR();
    pm = uni(lds_at(smem + L_MISC)[7]);
    p.s0 = pm >> 31;
    if (WV == 4 || WV == 5) {
      if (have < 0) have = wait_prog(mkrsrc(p.ws + WS_SY, SY_TOTAL), fkq(h, (lane & 1) * 2), 0, (gu32*)(p.ws + WS_SY + SY_ABORT));
      const char* src = p.ws + WS_T + t_off(p, kseg(0, h)) + (WV - 4) * 4096 + lane * 16;
#pragma unroll
      for (int i = 0; i < 4; ++i) glds16_sc1(src + i * 1024, smem + L_KT + (WV - 4) * 4096 + i * 1024);
    }
  }
  const __amdgpu_buffer_rsrc_t rsy = mkrsrc(p.ws + WS_SY, SY_TOTAL);
  const u32x4 rs4 = raw_rsrc(p.ws + WS_SY, SY_TOTAL);
  const u32x4 rt4 = raw_rsrc(p.ws + WS_T, T_BYTES);
  gu32* abortw = (gu32*)(p.ws + WS_SY + SY_ABORT);
  const float cexp = 0.08838834764831845f * 1.4426950408889634f;
  constexpr int m = WV >> 2, b = WV & 3;
  int lead = LEAD0;
  unsigned tb1 = 0;
  unsigned long long fm = 0, ps = 0; (void)fm; (void)ps;
  for (int s = 0; s < NSEG; ++s) {
    int lane = tid & 63;
    asm volatile("" : "+v"(lane));
    const int r = lane & 15, q = lane >> 4;
    unsigned aL16 = lane * 16, aMW = aL16 + WV * 1024, aWL = aL16 + 98304 + WV * 4096;
    unsigned aIDX = (lane & 2) ? 0xEEEE : 0x4444;
    unsigned aQW = (32 * b + r) * 16 + q * 4 + (m ? L_KX : L_QX);
    u32x4 QF;
    asm volatile("" : "={v[232:235]}"(QF));
    if (WV < 4) {
      QF[0] = L_QX + (q * 8 + (r >> 2)) * 16 + (r & 1) * 8;
      QF[1] = ((2 * (q & 1) + (q >> 1)) * 16 + r) * 4;
      QF[2] = ((r >> 2) & 1) * 65536 + ((r >> 1) & 1) * 64 + (r >> 3) * 32 + (r & 1) * 16 + (q >> 1) * 256 + (q & 1) * 8;
    } else QF[3] = r * 16 + q * 4;
    unsigned tout, spins, tgt;
    if (WV == 0) QK_STEP(QK_ASM_0); else if (WV == 1) QK_STEP(QK_ASM_1); else if (WV == 2) QK_STEP(QK_ASM_2);
    else if (WV == 3) QK_STEP(QK_ASM_3); else if (WV == 4) QK_STEP(QK_ASM_4); else if (WV == 5) QK_STEP(QK_ASM_5);
    else if (WV == 6) QK_STEP(QK_ASM_6); else QK_STEP(QK_ASM_7);
    tout = __builtin_amdgcn_readfirstlane(tout);
    tb1 = __builtin_amdgcn_readfirstlane(tb1);
    lane = tid & 63;
    asm volatile("" : "+v"(lane));
    if (WV < 4) {
      lds_at(smem + L_MISC + 8)[WV] = s + 1;
      if (WV == 3) { lds_at(smem + L_MISC)[6] = time_of(tout); lds_at(smem + 28672)[2] = s; }
    }
    if (WV == 6) {
      if (s + 1 < NSEG) watch(rsy, s, lane, now_q(), (const unsigned*)(smem + L_MISC), abortw, side(pm, 1));
    } else if (WV >= 4 && s + 1 < NSEG) {
      unsigned t;
      do {
        __builtin_amdgcn_s_sleep(1);
        t = lds_at(smem + L_MISC + 8)[lane & 3];
      } while (!__all(t == (unsigned)(s + 1)));
      if (WV <= 5) {
        if (s + 2 < NSEG) {
          if (have < s + 1) have = wait_prog(rsy, fkq(h, (lane & 1) * 2), s + 1, abortw);
          const char* src = p.ws + WS_T + t_off(p, kseg(s + 1, h)) + (WV - 4) * 4096 + lane * 16;
#pragma unroll
          for (int i = 0; i < 4; ++i) glds16_sc1(src + i * 1024, smem + L_KT + (WV - 4) * 4096 + i * 1024);
          if (have < s + 2) have = look_prog(rsy, fkq(h, (lane & 1) * 2));
        }
      } else {
        do __builtin_amdgcn_s_sleep(2); while (__builtin_amdgcn_readfirstlane(lds_at(smem + 28672)[2]) != (unsigned)s);
        const int x = (int)(__builtin_amdgcn_readfirstlane(lds_at(smem + L_MISC)[6]) - tb1);
        lead = __builtin_amdgcn_readfirstlane(track(lead, x + (s < LEAD_N0 ? LEAD_X0 : 0), s < LEAD_N0 ? LEAD_U0 : LEAD_UP, s < LEAD_N0 ? LEAD_D0 : (s < 3 * LEAD_N0 ? min(max(LEAD_DN, (lead - x) >> LEAD_SH), 16) : LEAD_DN), 60));
      }
    }
  }
}

__device__ __forceinline__ void qk_role(Args& p, int h, char* smem) {
  switch (__builtin_amdgcn_readfirstlane(threadIdx.x >> 6)) {
    case 0: qk_wave<0>(p, h, smem); break;
    case 1: qk_wave<1>(p, h, smem); break;
    case 2: qk_wave<2>(p, h, smem); break;
    case 3: qk_wave<3>(p, h, smem); break;
    case 4: qk_wave<4>(p, h, smem); break;
    case 5: qk_wave<5>(p, h, smem); break;
    case 6: qk_wave<6>(p, h, smem); break;
    default: qk_wave<7>(p, h, smem); break;
  }
}

#define K_DUE 50
#define K_DUE2 500
#define T_SL 2
#define O_DUE 400

constexpr int V_VM = 16384, V_T = 32768, V_KI = 131072;
#define TK_CALL(sg, od_) \
  asm volatile(TK_ASM \
            : \
            : "{v200}"(lane * 16), "{v[208:215]}"(bias), "{v[216:219]}"(ta), \
              [rs] "s"(rs4), [rt] "s"(raw_rsrc(p.ws + WS_T, T_BYTES)), [sent] "s"(DSENT), [sab] "s"(SY_ABORT), [sk] "s"(o_v(pm, sg, h) + 2048), \
              [tl] "s"(V_T + (sg & 3) * 24576), [vp] "s"((sg & 1) * 2048), [tq] "s"((w & 1) * 4096), [tv] "s"((w & 2) * 2048), \
              [so] "s"(t_off_o(p, T_O + (sg * 2 + (w & 1)) * 16 + (h >> 1)) + (h & 1) * 4096 + (w & 2) * 1024), \
              [up] "s"(0xFFFFFFFF00000000ull), [c] "s"(1.4426950408889634f), [negc] "s"(-1.4426950408889634f), \
              [sc] "s"((unsigned long long)__float_as_uint(0.08838834764831845f)), [od] "s"(od_) \
            : CLB32(128), CLB32(160), CLB8(192, 193, 194, 195, 196, 197, 198, 199), CLB8(224, 225, 226, 227, 228, 229, 230, 231), \
              "v205", "v206", "v207", "s88", "s89", CLB_S)
__device__ __forceinline__ void vu_role(Args& p, int h, char* smem) {
  const int tid = threadIdx.x, lane = tid & 63;
  const int w = __builtin_amdgcn_readfirstlane(tid >> 6);
  u32x32 W[4];
  {
    const unsigned vo = (tid >> 4) * (HD * 2) + 16 * ((lane & 15) ^ swzB((tid >> 4) & 15));
    auto stage = [&](int j) {
      const char* src = (const char*)(p.Wv + (size_t)j * 128 * HD + h * DH);
      const unsigned dst = lds_off(smem) + L_WL + (j & 3) * 32768 + w * 1024;
      glds2(src, 32 * HD * 2, vo, dst, true);
      glds2(src + 64 * HD * 2, 32 * HD * 2, vo, dst + 16384, true);
    };
    start_sy(p.ws, p.mem0, h, smem);
    Boot bt;
#pragma unroll
    for (int u = 0; u < 2; ++u) {
#pragma unroll
      for (int j = 0; j < 4; ++j) stage(4 * u + j);
      if (u && w == 7) boot_ask(p.ws, bt);
      WAIT_V(0);
      if (u && w == 7) {
        const unsigned m = boot_done(p.ws, bt);
        if (lane < 8) lds_at(smem + L_MISC)[lane] = lane == 7 ? m : (lane == 1 ? now_q() : 0);
      }
      LBAR();
#pragma unroll
      for (int k = 0; k < 16; ++k) put4(W[2 * u + (k >> 3)], k & 7, ld_bfrag(smem + L_WL + ((k >> 1) ^ w) * 16384, k & 1, w, lane));
      LBAR();
    }
  }
  const __amdgpu_buffer_rsrc_t rsy = mkrsrc(p.ws + WS_SY, SY_TOTAL);
  const u32x4 rs4 = raw_rsrc(p.ws + WS_SY, SY_TOTAL);
  gu32* abortw = (gu32*)(p.ws + WS_SY + SY_ABORT);
  const unsigned pm = uni(lds_at(smem + L_MISC)[7]);
  p.s0 = pm >> 31;
  unsigned long long fm = 0; (void)fm;
  int have = -1;
  auto fetch = [&](int sg) {
    if (have < sg) have = wait_prog(rsy, fkq(h, w - 4), sg, abortw);
    const char* src = p.ws + WS_T + t_off(p, (w - 4) * T_Q + kseg(sg, h)) + lane * 16;
#pragma unroll
    for (int i = 0; i < 8; ++i) glds16_sc1(src + i * 1024, smem + V_T + (sg & 3) * 24576 + (w - 4) * 8192 + i * 1024);
  };
  const int fo = SY_FO + ((w & 3) * NH + h) * 128;
  typedef __attribute__((ext_vector_type(8))) float f32x8;
  f32x8 bias;
  typedef __attribute__((ext_vector_type(8))) unsigned u32x8;
  u32x8 ca;
#pragma unroll
  for (int i = 0; i < 8; ++i) ca[i] = lane * 16 + (i ^ w) * 1024;
  u32x4 ta;
  {
    const int r = lane & 15, q = lane >> 4;
#pragma unroll
    for (int j = 0; j < 8; ++j) bias[j] = (j >> 2) * 16 + 4 * q + (j & 3) <= 16 * (w & 1) + r ? INFINITY : -INFINITY;
    ta[0] = V_KI + w * 2048 + lane * 16;
    ta[1] = V_KI + w * 2048 + (q * 8 + (r >> 2)) * 16 + (r & 1) * 8;
    ta[2] = V_VM + (w & 2) * 512 + ((2 * (q & 1) + (q >> 1)) * 16 + r) * 4;
    ta[3] = 0;
  }
#pragma unroll 1
  for (int s = 0; s < NSEG; ++s) {
    unsigned spins, tgt;
    asm volatile(VV_ASM
        : [spins] "=&s"(spins), [tgt] "=&s"(tgt)
        : W4_IN, "{v200}"(lane * 16), "{v[232:239]}"(ca), "{v202}"((lane & 2) ? 0xEEEE : 0x4444), "{v203}"(lane * 4),
          "{v204}"(tid * 4 + V_VM + (s & 1) * 2048), [rs] "s"(rs4), [som] "s"(o_mem(pm, s) + w * 1024), [som2] "s"(o_mem(pm, s) + w * 1024 + 16384), [step] "s"(s),
          [sent] "s"(DSENT), [sab] "s"(SY_ABORT), [sv] "s"(o_v(pm, s, h) + w * 256)
        : CLB32(128), CLB32(160), CLB8(192, 193, 194, 195, 196, 197, 198, 199), CLB_S);
    if (w < 4 || s == NSEG - 1) {
      const int sg = s - (w < 4);
      if (w >= 4) {
        if (lane == 0) __builtin_amdgcn_raw_buffer_store_b32(1u, rsy, fo + 4 * NH * 128, 0, 16);
        pace_until(now_q() + K_DUE);
      }
      if (sg >= 0 && sg != NSEG - 2) {
        TK_CALL(sg, s < NSEG - 1 ? O_DUE : 0);
        if (sg >= NSEG - 3) {
          static_assert(TLAG == 2, "");
          WAIT_V(0);
          if (lane == 0) __builtin_amdgcn_raw_buffer_store_b32(1u, rsy, fo + (sg - NSEG + 3) * 4 * NH * 128, 0, 16);
        }
      }
    } else if (w == 7) {
      watch(rsy, s, lane, now_q(), (const unsigned*)(smem + L_MISC), abortw, side(pm, 1));
    } else if (w >= 4) {
      __builtin_amdgcn_s_sleep(T_SL);
      const int s1 = w == 5 && s < NSEG - 3 ? s : s + 1;
#pragma unroll 1
      for (int sg = (w == 5 ? s < NSEG - 2 : s == 0) ? s : s + 1; sg <= s1; ++sg) fetch(sg);
      if (have < s1 + 1 && s1 + 1 < NSEG) have = look_prog(rsy, fkq(h, w - 4));
      if (w == 5 && s + NPRE < NSEG) {
#pragma unroll
        for (int i = 0; i < 4; ++i) st_sc1(rsy, o_v(pm, s + NPRE, h) + i * 1024 + lane * 16, (u32x4){DSENT, DSENT, DSENT, DSENT});
      }
    }
    if (s == NSEG - 2 && w >= 4) {
      pace_until(tgt + K_DUE2);
      TK_CALL(s, 0);
    }
  }
}

#define RL_UP 6
#define RL_DN 4
#define RED_MIN 40
#define BA_LOOK 360
constexpr int R_SI = 32768, R_PART = 98304;

__device__ __forceinline__ void red_role(Args& p, int j, char* smem) {
  const int tid = threadIdx.x;
  int lane = tid & 63;
  const int w = __builtin_amdgcn_readfirstlane(tid >> 6);
  const int c = (j & 2) * 8 + (j >> 2), rh = j & 1;
  const int hg = (w + (j >> 2)) & 7;
  const __amdgpu_buffer_rsrc_t rsy = mkrsrc(p.ws + WS_SY, SY_TOTAL);
  const u32x4 rs4 = raw_rsrc(p.ws + WS_SY, SY_TOTAL);
  const __amdgpu_buffer_rsrc_t rt = mkrsrc(p.ws + WS_T, T_BYTES);
  gu32* abortw = (gu32*)(p.ws + WS_SY + SY_ABORT);
  u32x32 W[4];
  u32x2 tag = {0u, 0u};
  {
    const u16* Wo = p.Wo + (size_t)(4 * hg * DH) * DM + 32 * c;
    const int ro = (lane >> 2) * 64 + (lane & 3) * 8;
    Boot bt;
    load_blocks<16>(smem + 32768 + w * 16384,
        [&](int i, char* slot) {
          if (i == 12 && w == 0) boot_ask(p.ws, bt);
          if ((i < 2 || i == 4 || i == 8) && (c & 14) != 6 && lane < 10) {
            const int x = i < 2 ? i : 1 + (i >> 2), n = min(((c & 15) - ((c & 8) >> 2)) * 10 + lane, 127);
            touch_line((const char*)p.Wo + (size_t)(w * 512 + (x >> 1) * 256 + (x & 1) * 64 + (n & 64) + n) * (DM * 2) + (c & 16) * 64 + 384, smem + w * 256);
          }
          const int x = i >> 2;
          blk_dma(Wo + (size_t)((x >> 1) * 256 + (x & 1) * 64 + ((i >> 1) & 1) * 128 + (i & 1) * 32) * DM, DM, slot, lane);
        },
        [&](int i, char* slot) {
#pragma unroll
          for (int t = 0; t < 2; ++t) {
            s16x4 lo = __builtin_amdgcn_ds_read_tr16_b64_v4i16((lds_s16x4_ptr)(slot + ro + t * 32));
            s16x4 hi = __builtin_amdgcn_ds_read_tr16_b64_v4i16((lds_s16x4_ptr)(slot + ro + t * 32 + 1024));
            put4(W[i >> 2], (i & 2) * 2 + 2 * t + (i & 1), (bf16x8){lo[0], lo[1], lo[2], lo[3], hi[0], hi[1], hi[2], hi[3]});
          }
        });
    if (w == 0) { lds_at(smem + 4128)[0] = boot_done(p.ws, bt); tag = next_tag(bt.e); }
    LBAR();
  }
  const unsigned pm = uni(lds_at(smem + 4128)[0]);
  p.s0 = pm >> 31;
  const int hs = side(pm, 1);
  {
    const u32x4 ds = {DSENT, DSENT, DSENT, DSENT};
#pragma unroll
    for (int i = 0; i < 2; ++i) {
      const int s = (w * 2 + i) * 4 + (lane >> 4);
      if (s >= 2) st_sc1(rsy, o_mem(pm, s) + (c >> 4) * 16384 + (c & 15) * 512 + rh * 128 + ((lane >> 3) & 1) * 256 + (lane & 7) * 16, ds);
    }
  }
  if (tid < 4) { reinterpret_cast<unsigned*>(smem + 4096)[tid] = tid == 3 ? now_q() - 0x100000u : 0; reinterpret_cast<unsigned*>(smem + 4112)[tid] = DSENT; }
  if (tid < 256) reinterpret_cast<int*>(smem + 5120)[tid] = ((tid >> 2) - TLAG - 6) >> 2;
  LBAR();
  const unsigned base = __builtin_amdgcn_readfirstlane(lds_at(smem + 4096)[3]);
  unsigned long long fm = 0, ps = 0;
  const unsigned aL = lane * 16;
  const int e = ((56 + w) * SEG + rh * 16 + (lane >> 2)) * DM + c * 32 + (lane & 3) * 8;
  u32x4 xv = *reinterpret_cast<const u32x4*>(p.x + e), av = {DSENT, DSENT, DSENT, DSENT};
  asm volatile("" : "+v"(xv));
  {
    const int r = lane & 15, q = lane >> 4;
    const int sm0 = o_mem(pm, 0) + (c >> 4) * 16384 + (c & 15) * 512 + rh * 128;
    u32x4 VO, VT;
    unsigned fo = SY_FO + ((rh + (lane & 4) / 2) * NH + 4 * hg + (lane & 3)) * 128;
    unsigned ao = w == 1 ? (c * S_LEN + rh * 16 - (TLAG + 3) * SEG) * 64 + lane * 16
                         : (w >= 4 ? SY_O + NPRE * 131072 + side(pm, w - 2) + (j >> 3) * 16384 + (j & 7) * 1024 + lane * 16
                                   : SY_FS + (2 * c + rh) * 128 + (lane < 8 ? lane * 16 : 0x70000000));
    unsigned ar = w == 1 ? R_SI - (TLAG + 2) * 1024 + lane * 16 : (w == 2 ? 5120 : 4112);
    const unsigned long long am = w == 1 ? ((1ull << 56) - 1) << (TLAG + 3) : (w == 2 ? 0x11111111111111ull << (TLAG + 7) : (w >= 4 ? (0xFFFFull << (16 * (w - 4))) >> NPRE : 0));
    unsigned pw = R_PART + w * 2048 + lane * 16, pr = R_PART + 16384 + tid * 4;
    unsigned sw = R_SI - (TLAG + 1) * 1024 + (lane >> 2) * 64 + w * 8 + (lane & 3) * 2, stm = 8192 + w * 512;
#pragma unroll
    for (int l = 0; l < 4; ++l) {
      const int x = l;
      VO[l] = o_o(0, 0, 4 * hg) + rh * 65536 + x * 1024 + aL;
      VT[l] = (T_O + rh * 16 + 2 * hg + (x >> 1)) * 16384 + (x & 1) * 2048 + aL;
    }
    unsigned ba = lane < 4 ? SY_BA + hs + (4 * hg + lane) * 128 : 0x7F000000u;
    unsigned sm = 0;
    int rl = RL0;
    unsigned step, E, so, st, st4, sh;
    asm volatile(RED_ASM
        : [step] "=&s"(step), [E] "=&s"(E), [so] "=&s"(so), [st] "=&s"(st), [st4] "=&s"(st4), [sh] "=&s"(sh),
          [rl] "+s"(rl), [sm] "+s"(sm), [fm] "+s"(fm), [ps] "+s"(ps),
          "+{v[224:227]}"(av), "+{v228}"(ao), "+{v229}"(ar), "+{v230}"(pw), "+{v231}"(pr), "+{v232}"(sw), "+{v233}"(stm), "+{v242}"(fo), "+{v173}"(ba)
        : W4_IN, "{v168}"(aL), "{v169}"(R4 ? (w >> 1) * 1024 + lane * 16 + (w & 1) * 8 : w * 512 + lane * 8), "{v170}"(lane * 8), "{v174}"((r & 1) ? 0x03020706u : 0x05040100u),
          "{v171}"(sm0 + 32768 + (q >> 1) * 256 + (q & 1) * 64 + ((r >> 1) & 1) * 32 + (r & 1) * 16 + (r >> 2) * 4),
          "{v172}"((lane & 2) ? 0xEEEE : 0x4444),
          "{v175}"(w == 3 && lane < 32 ? SY_BR + hs + j * 128 + lane * 4 : 0x7F000000u), "{v[234:237]}"(VO), "{v[238:241]}"(VT), "{v243}"(e * 2),
          [rg] "s"(raw_rsrc(p.ws + WS_G, S_LEN * DM * 2)), [fg] "s"(SY_FG + ((14 + (w >> 2)) * 8 + (c >> 2)) * 128),
          [rs] "s"(rs4), [ra] "s"(w == 1 ? raw_rsrc(p.ws + WS_S, S_LEN * DM * 2) : rs4), [rt] "s"(raw_rsrc(p.ws + WS_T, T_BYTES)),
          [ds] "n"(DS_RED), [wm] "n"(REF_W), [gm] "n"(REF_G), [mn] "n"(RED_MIN), [lk] "n"(DS_MEM + BA_LOOK), [up] "n"(RL_UP), [dn] "n"(-RL_DN), [sent] "s"(DSENT), [sab] "s"(SY_ABORT),
          [base] "s"(base), [pm] "s"(pm), [s0] "s"((p.s0 ^ TF_O) << 13), [am] "s"(am), [ainc] "s"(w == 1 ? SEG * 64 : (w >= 4 ? 131072 : 0)),
          [rinc] "s"(w == 1 ? 1024 : (w == 2 ? 16 : 0))
        : CLB32(128), CLB8(160, 161, 162, 163, 164, 165, 166, 167), CLB8(176, 177, 178, 179, 180, 181, 182, 183),
          CLB8(184, 185, 186, 187, 188, 189, 190, 191), CLB8(192, 193, 194, 195, 196, 197, 198, 199), CLB8(200, 201, 202, 203, 204, 205, 206, 207),
          CLB8(208, 209, 210, 211, 212, 213, 214, 215), CLB8(216, 217, 218, 219, 220, 221, 222, 223), CLB_E, "s96", CLB_S);
  }
  LBAR();
  u32x4 sv;
  if (w < 6 - TLAG) sv = *reinterpret_cast<const u32x4*>(smem + R_SI + (56 + w) * 1024 + lane * 16);
  else {
    const f32x4* part = reinterpret_cast<const f32x4*>(smem + (w == 6 - TLAG ? R_PART : (w - 6 + TLAG) * 16384)) +
                        (lane & 2) * 32 + (lane & 1) * 32 + (lane >> 2);
    f32x4 x[8][2];
#pragma unroll
    for (int ww = 0; ww < 8; ++ww) { x[ww][0] = part[ww * 128]; x[ww][1] = part[ww * 128 + 16]; }
#pragma unroll
    for (int u = 0; u < 2; ++u) {
      const f32x4 a = ((x[0][u] + x[1][u]) + (x[2][u] + x[3][u])) + ((x[4][u] + x[5][u]) + (x[6][u] + x[7][u]));
      sv[2 * u] = cvtpk(a[0], a[1]); sv[2 * u + 1] = cvtpk(a[2], a[3]);
    }
  }
  st_out(p, e, combine(sv, av, xv));
  if (j == 0 && tid == 0) st2_sc1(rsy, SY_EP, tag);
}


__global__ __launch_bounds__(512, 2) void main_kernel(Args p) {
  char* smem = g_smem;
  p.ws = ws_align(p.ws);
  const int bx = blockIdx.x & 7, bk = blockIdx.x >> 3;
  const bool isg = (bx ^ XPROBE) & 4;
  const int b = (isg ? NCHAIN : 0) + bk * 4 + (bx & 3);
  if (threadIdx.x < 64) {
    unsigned long long pc; unsigned len, keep;
    asm volatile("s_getpc_b64 %0\n.Lpc_%=:\n\ts_mov_b32 %1, .Lcode_end-.Lpc_%=" : "=s"(pc), "=s"(len));
    asm volatile("s_mov_b32 %0, m0\n\ts_mov_b32 m0, %3\n\ts_nop 0\n\tbuffer_load_dword %1, %2, 0 offen lds\n\ts_mov_b32 m0, %0"
                 : "=&s"(keep) : "v"((bk * 40 + threadIdx.x) * 128), "s"(raw_rsrc((const void*)pc, len)), "s"((b >> 6) == 1 ? 2048 : 163328) : "memory");
  }
  if ((bk >> 3) == (isg ? 3 : 1) && (threadIdx.x >> 6) == 1) {
    constexpr int NI = TLB_NI + 1, NW = (int)(WS_END / TLB_WS);
    const int n = (threadIdx.x & 63) * 8 + (bk & 7), m = n / NI, i = n % NI;
    const u16* q = m == 0 ? p.Wq : m == 1 ? p.Wk : m == 2 ? p.Wv : m == 3 ? p.Wo : m == 4 ? p.wg : m == 5 ? p.wu : m == 6 ? p.wd : m == 7 ? p.x : p.out;
    static_assert(9 * NI + NW <= 512, "");
    if (n < 9 * NI + NW)
      touch_line(m < 9 ? (const char*)q + min(i * (DM * HD * 2 / TLB_NI), (m < 4 ? HD : m < 7 ? FF : S_LEN) * DM * 2 - 128)
                       : p.ws + (size_t)(n - 9 * NI + 1) * TLB_WS - 16384, smem + 163072);
  }
  if (b >= NCHAIN) gemm_role(p, b - NCHAIN, smem);
  else if (b < 32) qk_role(p, (b & 3) * 8 + bk, smem);
  else if (b < 64) vu_role(p, (b & 3) * 8 + bk - 8, smem);
  else red_role(p, b - 64, smem);
}

extern "C" void kernel_launch(void* const* d_in, const int* in_sizes, int n_in,
                              void* d_out, int out_size, void* d_ws, size_t ws_size,
                              hipStream_t stream) {
  Args a;
  a.x = (const u16*)d_in[0]; a.mem0 = (const u16*)d_in[1];
  a.Wq = (const u16*)d_in[2]; a.Wk = (const u16*)d_in[3]; a.Wv = (const u16*)d_in[4]; a.Wo = (const u16*)d_in[5];
  a.wg = (const u16*)d_in[8]; a.wu = (const u16*)d_in[9]; a.wd = (const u16*)d_in[10];
  a.ws = (char*)d_ws; a.out = (u16*)d_out; a.s0 = 0; a.pad1 = 0;
  main_kernel<<<256, 512, 0, stream>>>(a);
}
```

```cpp
#include <hip/hip_runtime.h>
#include <hip/hip_bf16.h>
#include <stdint.h>

constexpr int S_LEN = 2048, DM = 1024, NH = 32, DH = 128, NMEM = 8, SEG = 32;
constexpr int NSEG = S_LEN / SEG;
constexpr int HD = NH * DH;
constexpr int FF = 3584;

typedef unsigned short u16;
typedef __attribute__((ext_vector_type(8))) short bf16x8;
typedef __attribute__((ext_vector_type(4))) short s16x4;
typedef __attribute__((ext_vector_type(4))) float f32x4;
typedef __attribute__((ext_vector_type(4))) unsigned int u32x4;
typedef __attribute__((ext_vector_type(2))) unsigned int u32x2;
typedef __attribute__((address_space(3))) s16x4* lds_s16x4_ptr;
typedef __attribute__((address_space(3))) void* lds_void_ptr;
typedef __attribute__((address_space(1))) unsigned gu32;
typedef __bf16 bf16x2_t __attribute__((ext_vector_type(2)));
typedef float f32x2_t __attribute__((ext_vector_type(2)));

__device__ __forceinline__ unsigned cvtpk(float lo, float hi) {
  bf16x2_t r = __builtin_convertvector((f32x2_t){lo, hi}, bf16x2_t);
  return __builtin_bit_cast(unsigned, r);
}
__device__ __forceinline__ float lo_f(unsigned v) { return __uint_as_float(v << 16); }
__device__ __forceinline__ float hi_f(unsigned v) { return __uint_as_float(v & 0xffff0000u); }
__device__ __forceinline__ float rbf(float f) { return lo_f(cvtpk(f, f)); }
__device__ __forceinline__ bf16x8 as_bf16x8(u32x4 v) { return __builtin_bit_cast(bf16x8, v); }
#define MFMA(a, b, c) __builtin_amdgcn_mfma_f32_16x16x32_bf16((a), (b), (c), 0, 0, 0)

#define KSTR 65
#define XLD 1024
constexpr int PAGE = 2 << 20, NPAGE = 14;
constexpr int T_K = 0, T_Q = 2304, T_V = 4608, T_O = 6912, T_END = T_O + 2048;
constexpr unsigned T_BYTES = T_END * 16384u;
constexpr size_t WS_SY = 0;
constexpr size_t WS_T = (size_t)NPAGE * PAGE;
constexpr size_t WS_XN = WS_T + T_BYTES;
constexpr size_t WS_HF = WS_XN + (size_t)S_LEN * XLD * 2;
constexpr size_t WS_G = WS_HF + (size_t)S_LEN * 16384;
constexpr size_t WS_S = WS_G + (size_t)S_LEN * DM * 2;
constexpr size_t WS_END = WS_S + (size_t)S_LEN * DM * 2;
constexpr unsigned SENT = 0xFFFFFFFFu;
constexpr unsigned DSENT = 0xFFBFFFBFu;
constexpr int SY_MEM = 0;
constexpr int SY_O = 2 * PAGE;
constexpr int SY_V = 6 * PAGE;
constexpr int SY_BR = PAGE, SY_BA = SY_BR + 16384;
constexpr int SY_FKQ = SY_BR + 32768;
constexpr int SY_FG = SY_FKQ + NH * 4 * 128;
constexpr int SY_FO = SY_FG + 16 * 8 * 128;
constexpr int SY_CH = SY_FO + 384 * 128;
constexpr int SY_ABORT = SY_CH + 8 * 128;
constexpr int SY_FS = SY_ABORT + 256;
constexpr int SY_FEND = SY_FS + 64 * 128;
constexpr int SY_EP = SY_FEND, SY_BT = SY_EP + 128;
constexpr int SY_TOTAL = NPAGE * PAGE;
__device__ __forceinline__ int side(unsigned pm, int page) { return ((pm >> page) & 1u) << 13; }
__device__ __forceinline__ int o_mem(unsigned pm, int s) { return SY_MEM + s * 32768 + side(pm, 0); }
__device__ __forceinline__ int o_o(unsigned pm, int s, int h) {
  return SY_O + s * 131072 + (h >> 3) * 16384 + ((h >> 1) & 3) * 2048 + (h & 1) * 128 + side(pm, 2 + (s >> 4));
}
__device__ __forceinline__ int o_v(unsigned pm, int s, int h) { return SY_V + s * 262144 + (h >> 1) * 16384 + (h & 1) * 4096 + side(pm, 6 + (s >> 3)); }
__device__ __forceinline__ char* ws_align(char* ws) { return ws + ((PAGE - (unsigned)(uintptr_t)ws) & (PAGE - 1)); }
#define NFUSE 12
constexpr int NPRE = 4;
static_assert(WS_END + PAGE <= (size_t)256 << 20 && NH * KSTR <= T_Q && NPAGE % 2 == 0, "");
constexpr int NCHAIN = 128;
__host__ __device__ constexpr int kseg(int sg, int h) { return h * KSTR + sg; }
__host__ __device__ constexpr int fkq(int h, int kind) { return SY_FKQ + (h * 4 + kind) * 128; }

#define TLB_NI 16
#define TLB_WS (2 << 20)
#define XPROBE 0
struct Args {
  const u16 *x, *mem0, *Wq, *Wk, *Wv, *Wo, *wg, *wu, *wd;
  char* ws;
  u16* out;
  int s0, pad1;
};
__device__ __forceinline__ int h_off(const Args& p, int rblk) { return rblk * PAGE + (((p.s0 ^ rblk ^ 1) & 1) << 13); }
static_assert(WS_HF % (2 * PAGE) == 0, "");
#define TF_KQV 1
#define TF_O 0
__device__ __forceinline__ int t_off(const Args& p, int t) { return t * 16384 + (((p.s0 ^ (t >> 7) ^ (TF_KQV == 2 ? t : TF_KQV)) & 1) << 13); }
__device__ __forceinline__ int t_off_o(const Args& p, int t) { return t * 16384 + (((p.s0 ^ (t >> 7) ^ (TF_O == 2 ? t : TF_O)) & 1) << 13); }

__device__ __forceinline__ float wave_sum(float v) {
#pragma unroll
  for (int i = 0; i < 4; ++i) {
    const int x = (int)__float_as_uint(v);
    v += __uint_as_float(i == 0 ? __builtin_amdgcn_update_dpp(0, x, 0x128, 0xf, 0xf, false) : i == 1 ? __builtin_amdgcn_update_dpp(0, x, 0x124, 0xf, 0xf, false)
                         : i == 2 ? __builtin_amdgcn_update_dpp(0, x, 0x122, 0xf, 0xf, false) : __builtin_amdgcn_update_dpp(0, x, 0x121, 0xf, 0xf, false));
  }
  auto r = __builtin_amdgcn_permlane16_swap(__float_as_uint(v), __float_as_uint(v), false, false);
  v = __uint_as_float(r[0]) + __uint_as_float(r[1]);
  r = __builtin_amdgcn_permlane32_swap(__float_as_uint(v), __float_as_uint(v), false, false);
  return __uint_as_float(r[0]) + __uint_as_float(r[1]);
}

__shared__ __attribute__((aligned(16))) char g_smem[163840];
__device__ __forceinline__ unsigned lds_off(const void* p) {
  return (unsigned)(uintptr_t)(lds_void_ptr)g_smem + (unsigned)((const char*)p - g_smem);
}
__device__ __forceinline__ __amdgpu_buffer_rsrc_t mkrsrc(const void* p, unsigned bytes) {
  return __builtin_amdgcn_make_buffer_rsrc((void*)p, 0, bytes, 0x00020000);
}
__device__ __forceinline__ u32x4 ld_sc1(__amdgpu_buffer_rsrc_t rs, int off) {
  return __builtin_amdgcn_raw_buffer_load_b128(rs, off, 0, 16);
}
__device__ __forceinline__ void st_sc1(__amdgpu_buffer_rsrc_t rs, int off, u32x4 v) {
  __builtin_amdgcn_raw_buffer_store_b128(v, rs, off, 0, 16);
}
__device__ __forceinline__ void swap16(unsigned& x, unsigned& y) {
  auto r = __builtin_amdgcn_permlane16_swap(x, y, false, false);
  x = r[0]; y = r[1];
}
__device__ __forceinline__ void swap32(unsigned& x, unsigned& y) {
  auto r = __builtin_amdgcn_permlane32_swap(x, y, false, false);
  x = r[0]; y = r[1];
}
__device__ __forceinline__ bool spin_fail(unsigned& spins, gu32* abortw) {
  ++spins;
  if ((spins & 63u) == 0u) {
    const __amdgpu_buffer_rsrc_t ra = mkrsrc((const void*)abortw, 4);
    unsigned z = 0;
    asm volatile("" : "+v"(z));
    if (__builtin_amdgcn_readfirstlane(__builtin_amdgcn_raw_buffer_load_b32(ra, z, 0, 16)) == 1u) return true;
    if (spins > (1u << 19)) {
      __builtin_amdgcn_raw_buffer_store_b32(z + 1u, ra, z, 0, 16);
      return true;
    }
  }
  return false;
}
__device__ __forceinline__ bf16x8 ldsf(const char* p) { return *reinterpret_cast<const bf16x8*>(p); }
#define SCHED() __builtin_amdgcn_sched_barrier(0)
#define LBAR() do { asm volatile("s_waitcnt lgkmcnt(0)" ::: "memory"); __builtin_amdgcn_s_barrier(); asm volatile("" ::: "memory"); } while (0)
#define WAIT_V(n) asm volatile("s_waitcnt vmcnt(%0)" :: "n"(n) : "memory")

#define HOLD_V(c, n) asm volatile("s_cmp_eq_u32 %0, 0\n\ts_cbranch_scc1 .Lh_%=\n\ts_waitcnt vmcnt(%1)\n.Lh_%=:" :: "s"((int)(c)), "n"(n) : "memory", "scc")
#define TH_Q 3
__device__ __forceinline__ void wait_flags(__amdgpu_buffer_rsrc_t rs, int off, gu32* abortw) {
  for (unsigned spins = 0;;) {
    asm volatile("" ::: "memory");
    const unsigned v = __builtin_amdgcn_raw_buffer_load_b32(rs, off, 0, 16);
    if (__all(v != SENT)) break;
    __builtin_amdgcn_s_sleep(4);
    if (spin_fail(spins, abortw)) break;
  }
  asm volatile("" ::: "memory");
}

__device__ __forceinline__ unsigned offA(unsigned row, unsigned ch) {
  return row * 128u + 16u * (ch ^ (row & 7u));
}
__device__ __forceinline__ unsigned swzB(unsigned row) { return ((row & 3u) << 2) | ((row >> 2) & 3u); }
__device__ __forceinline__ unsigned offB(unsigned row, unsigned ch) {
  return 256u * row + 16u * (ch ^ swzB(row));
}
__device__ __forceinline__ void glds16(const u16* src, char* dst) {
  const unsigned d = __builtin_amdgcn_readfirstlane(lds_off(dst));
  unsigned keep;
  asm volatile("s_mov_b32 %0, m0\n\ts_mov_b32 m0, %2\n\ts_nop 0\n\tglobal_load_lds_dwordx4 %1, off\n\ts_mov_b32 m0, %0"
               : "=&s"(keep) : "v"(src), "s"(d) : "memory");
}
__device__ __forceinline__ bf16x8 ld_bfrag(const char* sb, int ks, int c, int lane) {
  const unsigned g = lane >> 4, qq = (lane & 15) >> 2, pp = lane & 3;
  s16x4 lo = __builtin_amdgcn_ds_read_tr16_b64_v4i16(
      (lds_s16x4_ptr)(sb + offB(32 * ks + 8 * g + qq, 2 * c + (pp >> 1)) + 8 * (pp & 1)));
  s16x4 hi = __builtin_amdgcn_ds_read_tr16_b64_v4i16(
      (lds_s16x4_ptr)(sb + offB(32 * ks + 8 * g + 4 + qq, 2 * c + (pp >> 1)) + 8 * (pp & 1)));
  return (bf16x8){lo[0], lo[1], lo[2], lo[3], hi[0], hi[1], hi[2], hi[3]};
}

__device__ __forceinline__ void blk_dma(const u16* src, size_t ld, char* slot, int lane) {
  const u16* s = src + (size_t)(lane >> 2) * ld + (lane & 3) * 8;
  glds16(s, slot);
  glds16(s + 16 * ld, slot + 1024);
}
template <int NB, typename ISSUE, typename SINK>
__device__ __forceinline__ void load_blocks(char* slice, ISSUE&& issue, SINK&& sink) {
#pragma unroll
  for (int i = 0; i < 8; ++i) issue(i, slice + i * 2048);
#pragma unroll
  for (int g = 0; g < NB / 4; ++g) {
    if (g + 1 < NB / 4) WAIT_V(8); else WAIT_V(0);
#pragma unroll
    for (int j = 0; j < 4; ++j) sink(4 * g + j, slice + ((4 * g + j) & 7) * 2048);
    asm volatile("s_waitcnt lgkmcnt(0)" ::: "memory");
    if (g + 2 < NB / 4) {
#pragma unroll
      for (int j = 0; j < 4; ++j) issue(4 * g + 8 + j, slice + ((4 * g + j) & 7) * 2048);
    }
  }
}

__device__ __forceinline__ u32x4 widen32(unsigned a0, unsigned a1, unsigned b0, unsigned b1) {
  swap32(a0, b0); swap16(a0, b0);
  swap32(a1, b1); swap16(a1, b1);
  return (u32x4){a0, a1, b0, b1};
}

__device__ __forceinline__ void publish_flags(__amdgpu_buffer_rsrc_t rs, int off, int stride, int n) {
  WAIT_V(0);
  LBAR();
  int tid = threadIdx.x;
  asm volatile("" : "+v"(tid));
  if (tid < n) __builtin_amdgcn_raw_buffer_store_b32(1u, rs, off + tid * stride, 0, 16);
}

__device__ __forceinline__ void wait_s(const Args& p, __amdgpu_buffer_rsrc_t rsy, int rblk, int c0, int n, bool need) {
  if (need) {
    if (threadIdx.x < 64) {
      for (unsigned spins = 0;;) {
        asm volatile("" ::: "memory");
        const int f = (int)__builtin_amdgcn_raw_buffer_load_b32(rsy, SY_FS + (2 * c0 + (threadIdx.x & (2 * n - 1))) * 128, 0, 16);
        if (__all(f >= rblk)) break;
        __builtin_amdgcn_s_sleep(32);
        if (spin_fail(spins, (gu32*)(p.ws + WS_SY + SY_ABORT))) break;
      }
    }
    LBAR();
  }
}
__device__ __forceinline__ u32x4 combine(u32x4 sv, u32x4 g, u32x4 x) {
  u32x4 o;
#pragma unroll
  for (int j = 0; j < 4; ++j) {
    const unsigned t = cvtpk(lo_f(sv[j]) * lo_f(g[j]), hi_f(sv[j]) * hi_f(g[j]));
    o[j] = cvtpk(lo_f(x[j]) + lo_f(t), hi_f(x[j]) + hi_f(t));
  }
  return o;
}
#define OUT_AUX 0
__device__ __forceinline__ void st_out(const Args& p, int e, u32x4 v) {
  __builtin_amdgcn_raw_buffer_store_b128(v, mkrsrc(p.out, S_LEN * DM * 2), e * 2, 0, OUT_AUX);
}
struct SX { u32x4 s, x; };
__device__ __forceinline__ SX get_sx(const Args& p, int rblk, int e) {
  SX r; r.s = (u32x4){0u, 0u, 0u, 0u}; r.x = r.s;
  if (rblk < NFUSE) {
    r.s = ld_sc1(mkrsrc(p.ws + WS_S, S_LEN * DM * 2), (((e & 1023) >> 5) * S_LEN + (e >> 10)) * 64 + (e & 31) * 2);
    r.x = *reinterpret_cast<const u32x4*>(p.x + e);
  }
  return r;
}
__device__ __forceinline__ void put_g(const Args& p, __amdgpu_buffer_rsrc_t rg, int rblk, int e, u32x4 g, const SX& v) {
  if (rblk < NFUSE) {
    st_out(p, e, combine(v.s, g, v.x));
  } else st_sc1(rg, e * 2, g);
}

__device__ __forceinline__ void job_comb(const Args& p, int rblk, int ct, __amdgpu_buffer_rsrc_t rsy) {
  int tid = threadIdx.x;
  asm volatile("" : "+v"(tid));
  u32x4 xv[8];
#pragma unroll
  for (int i = 0; i < 8; ++i) xv[i] = *reinterpret_cast<const u32x4*>(p.x + (rblk * 128 + i * 16 + (tid >> 5)) * DM + ct * 256 + (tid & 31) * 8);
  if (tid < 64) wait_flags(rsy, SY_FG + (rblk * 8 + 2 * ct + (tid & 1)) * 128, (gu32*)(p.ws + WS_SY + SY_ABORT));
  wait_s(p, rsy, rblk, ct * 8, 8, true);
  u32x4 sv[8], gv[8];
#pragma unroll
  for (int i = 0; i < 8; ++i) {
    const int e = (rblk * 128 + i * 16 + (tid >> 5)) * DM + ct * 256 + (tid & 31) * 8;
    sv[i] = ld_sc1(mkrsrc(p.ws + WS_S, S_LEN * DM * 2), (((e & 1023) >> 5) * S_LEN + (e >> 10)) * 64 + (e & 31) * 2);
    gv[i] = ld_sc1(mkrsrc(p.ws + WS_G, S_LEN * DM * 2), e * 2);
  }
#pragma unroll
  for (int i = 0; i < 8; ++i)
    st_out(p, (rblk * 128 + i * 16 + (tid >> 5)) * DM + ct * 256 + (tid & 31) * 8, combine(sv[i], gv[i], xv[i]));
}

__device__ __forceinline__ u32x4 raw_rsrc(const void* p, unsigned bytes) {
  const unsigned long long a = (unsigned long long)p;
  return (u32x4){(unsigned)a, (unsigned)(a >> 32) & 0xffffu, bytes, 0x00020000u};
}
__device__ __forceinline__ u32x2 next_tag(u32x2 e) { return (u32x2){e[0] + 1u, e[1] + (e[0] == 0xFFFFFFFFu)}; }
struct Boot { u32x2 e; u32x4 c; };
__device__ __forceinline__ void boot_ask(const char* ws, Boot& r) {
  const unsigned o = (threadIdx.x & 63) * 128;
  asm volatile("buffer_load_dwordx2 %0, off, %3, %4 sc1\n\tbuffer_load_dwordx4 %1, %2, %3, %5 offen sc1"
               : "=&v"(r.e), "=&v"(r.c) : "v"(o), "s"(raw_rsrc(ws, SY_TOTAL)), "s"(SY_EP), "s"(SY_BT) : "memory");
}
__device__ __forceinline__ void wait_rows(const char* ws, u32x2 tag) {
  const __amdgpu_buffer_rsrc_t rs = mkrsrc(ws, SY_TOTAL);
  int o = (threadIdx.x & 63) * 128;
  asm volatile("" : "+v"(o));
#pragma unroll 1
  for (int i = 0; i < (1 << 17); ++i) {
    asm volatile("" ::: "memory");
    const u32x2 a = __builtin_amdgcn_raw_buffer_load_b64(rs, o, SY_BT + 32 * 128, 16);
    if (__all(a[0] == tag[0] && a[1] == tag[1])) break;
    __builtin_amdgcn_s_sleep(8);
  }
  asm volatile("" ::: "memory");
}
__device__ __forceinline__ unsigned boot_done(const char* ws, Boot& r, bool own = false, u32x2 tag = (u32x2){0u, 0u}) {
#pragma unroll 1
  for (int i = 0; i < (1 << 17); ++i) {
    asm volatile("" : "+v"(r.e), "+v"(r.c));
    if (!own) tag = next_tag(r.e);
    if (__all(r.c[0] == tag[0] && r.c[1] == tag[1] && (r.c[2] ^ r.c[3]) == tag[0])) break;
    __builtin_amdgcn_s_sleep(8);
    boot_ask(ws, r);
    WAIT_V(0);
  }
  unsigned q0[NPAGE], q1[NPAGE], ga = 0xFFFFu, gb = 0xFFFFu;
#pragma unroll
  for (int i = 0; i < NPAGE; ++i) {
    const unsigned x = __builtin_amdgcn_readlane(r.c[2], i), y = __builtin_amdgcn_readlane(r.c[2], i + NPAGE);
    q0[i] = min(x >> 16, y >> 16); q1[i] = min(x & 0xFFFFu, y & 0xFFFFu);
    ga = min(ga, (i & 1) ? q1[i] : q0[i]); gb = min(gb, (i & 1) ? q0[i] : q1[i]);
  }
  unsigned m = gb < ga ? 0x80001555u : 0x2AAAu;
  const unsigned thr = min(ga, gb) + 110;
#pragma unroll
  for (int i = 0; i < NPAGE; ++i) {
    const unsigned lo = min(q0[i], q1[i]), hi = max(q0[i], q1[i]);
    const unsigned keep = (lo < thr ? hi : 0u) > lo + 100 ? ~(1u << i) : ~0u;
    m = (m & keep) | ((q1[i] < q0[i] ? ~0u : 0u) & ~keep);
  }
  return m;
}
typedef __attribute__((address_space(3))) volatile unsigned* lds_vu_ptr;
__device__ __forceinline__ lds_vu_ptr lds_at(const void* p) {
  unsigned a = lds_off(p);
  asm volatile("" : "+v"(a));
  return (lds_vu_ptr)(uintptr_t)a;
}
__device__ __forceinline__ unsigned uni(unsigned v) {
  unsigned r;
  asm volatile("s_nop 1\n\tv_readfirstlane_b32 %0, %1\n\ts_nop 3" : "=s"(r) : "v"(v));
  return r;
}
__device__ __forceinline__ void start_sy(char* ws, const u16* mem0, int i, char* smem) {
  const int tid = threadIdx.x, lane = tid & 63, w = __builtin_amdgcn_readfirstlane(tid >> 6);
  const __amdgpu_buffer_rsrc_t rsy = mkrsrc(ws, SY_TOTAL);
  const __amdgpu_buffer_rsrc_t rs = mkrsrc(ws + (size_t)(i % NPAGE) * PAGE + (32 + (i * 5 + w * 11) % 95) * 16384 + ((i * 3 + w * 5) & 31) * 256, 16384);
  const u32x2 e = __builtin_amdgcn_raw_buffer_load_b64(rsy, 0, SY_EP, 16);
  const u32x4 ds = {DSENT, DSENT, DSENT, DSENT};
  auto arm = [&]() {
    static_assert(NPRE == 4 && SY_FEND - SY_BR <= 32 * 6144, "");
#pragma unroll
    for (int u = 0; u < 6; ++u)
      st_sc1(rsy, (u < 4 ? SY_V + (i * 4 + u) * 8192 : SY_O + (i * 2 + u - 4) * 8192) + tid * 16, ds);
    if (w == 7) {
      const int n = i * 64 + lane, j = (n >> 10) * 512 + (n & 511);
      const unsigned* m0 = reinterpret_cast<const unsigned*>(mem0 + (2 * ((j >> 2) & 3) + (j & 1)) * DM + (j >> 4) * 16) + ((j >> 1) & 1);
      u32x4 m;
#pragma unroll
      for (int k = 0; k < 4; ++k) { const unsigned t = m0[2 * k]; m[k] = t == DSENT ? 0xFFFFFFFFu : t; }
      st_sc1(rsy, n * 16, m);
    } else if (w == 6) st_sc1(rsy, 32768 + i * 1024 + lane * 16, ds);
    else {
      const int o = SY_BR + i * 6144 + tid * 16;
      st_sc1(rsy, o < SY_FEND ? o : 0x7F000000, (u32x4){SENT, SENT, SENT, SENT});
    }
  };
  int ro = SY_BT + i * 128 + lane * 16;
  asm volatile("" : "+v"(ro));
  unsigned m0 = 30000u, m1 = 30000u;
  {
    if (lane < 32) st_sc1(rs, (lane >> 4) * 8192 + (lane & 15) * 16, ds);
    arm();
    unsigned t[4];
#pragma unroll
    for (int k = 0; k < 4; ++k) {
      unsigned long long c0, c1; unsigned v, off = ((k ^ (k >> 1)) & 1) * 8192 + (k >> 1) * 128;
      asm volatile("s_waitcnt vmcnt(0)\n\ts_barrier\n\ts_memtime %0\n\ts_waitcnt lgkmcnt(0)\n\tbuffer_load_dword %2, %3, %4, 0 offen sc1\n\ts_waitcnt vmcnt(0)\n\t"
                   "s_memtime %1\n\ts_waitcnt lgkmcnt(0)" : "=&s"(c0), "=&s"(c1), "=&v"(v) : "v"(off), "s"(rs) : "memory");
      t[k] = (unsigned)(c1 - c0);
    }
    lds_vu_ptr tm = lds_at(smem);
    if (lane == 0) { tm[w] = min(t[0], t[3]); tm[8 + w] = min(t[1], t[2]); }
    LBAR();
    const unsigned x = tm[lane & 15];
#pragma unroll
    for (int k = 0; k < 8; ++k) { m0 = min(m0, (unsigned)__builtin_amdgcn_readlane(x, k)); m1 = min(m1, (unsigned)__builtin_amdgcn_readlane(x, 8 + k)); }
  }
  if (threadIdx.x < 8) {
    const u32x2 tag = next_tag(e);
    const unsigned v = m0 << 16 | m1;
    st_sc1(rsy, ro, (u32x4){tag[0], tag[1], v, v ^ tag[0]});
  }
}
__device__ __forceinline__ u32x2 start_rows(const Args& p, int g) {
  const int tid = threadIdx.x, lane = tid & 63, row0 = g * 16 + (tid >> 6) * 2;
  u32x4 a[4];
#pragma unroll
  for (int i = 0; i < 4; ++i) a[i] = *reinterpret_cast<const u32x4*>(p.x + row0 * DM + i * 512 + lane * 8);
  const u32x2 e = __builtin_amdgcn_raw_buffer_load_b64(mkrsrc(p.ws, SY_TOTAL), 0, SY_EP, 16);
  const __amdgpu_buffer_rsrc_t ro = mkrsrc(p.ws + WS_XN, S_LEN * XLD * 2);
#pragma unroll
  for (int u = 0; u < 2; ++u) {
    float v[16];
#pragma unroll
    for (int i = 0; i < 8; ++i) { v[2 * i] = lo_f(a[2 * u + (i >> 2)][i & 3]); v[2 * i + 1] = hi_f(a[2 * u + (i >> 2)][i & 3]); }
    float s = 0.f;
#pragma unroll
    for (int i = 0; i < 16; ++i) s += v[i];
    const float mean = wave_sum(s) * (1.0f / DM);
    float q = 0.f;
#pragma unroll
    for (int i = 0; i < 16; ++i) { float d = v[i] - mean; q += d * d; }
    const float var = rbf(wave_sum(q) * (1.0f / DM));
    const float mb = rbf(mean);
    const float inv = 1.0f / rbf(sqrtf(rbf(var + 1e-5f)));
#pragma unroll
    for (int k = 0; k < 2; ++k) {
      u32x4 o;
#pragma unroll
      for (int i = 0; i < 4; ++i) {
        const unsigned t = cvtpk(v[8 * k + 2 * i] - mb, v[8 * k + 2 * i + 1] - mb);
        o[i] = cvtpk(lo_f(t) * inv, hi_f(t) * inv);
      }
      st_sc1(ro, (row0 + u) * (XLD * 2) + k * 1024 + lane * 16, o);
    }
  }
  return next_tag(e);
}
__device__ __forceinline__ void report_rows(const Args& p, int g, u32x2 tag) {
  if (threadIdx.x < 8) st_sc1(mkrsrc(p.ws + WS_SY, SY_TOTAL), SY_BT + (32 + g) * 128 + threadIdx.x * 16, (u32x4){tag[0], tag[1], 0u, tag[0]});
}

typedef __attribute__((ext_vector_type(32))) unsigned u32x32;
__device__ __forceinline__ void put4(u32x32& W, int i, bf16x8 f) {
  const u32x4 v = __builtin_bit_cast(u32x4, f);
#pragma unroll
  for (int j = 0; j < 4; ++j) W[4 * i + j] = v[j];
}
#define CLB8(a, b, c, d, e, f, g, h) "v" #a, "v" #b, "v" #c, "v" #d, "v" #e, "v" #f, "v" #g, "v" #h
#define CLB32(a) CLB32_##a
#define CLB32_128 CLB8(128, 129, 130, 131, 132, 133, 134, 135), CLB8(136, 137, 138, 139, 140, 141, 142, 143), CLB8(144, 145, 146, 147, 148, 149, 150, 151), CLB8(152, 153, 154, 155, 156, 157, 158, 159)
#define CLB32_160 CLB8(160, 161, 162, 163, 164, 165, 166, 167), CLB8(168, 169, 170, 171, 172, 173, 174, 175), CLB8(176, 177, 178, 179, 180, 181, 182, 183), CLB8(184, 185, 186, 187, 188, 189, 190, 191)
#define W4_IN "{v[0:31]}"(W[0]), "{v[32:63]}"(W[1]), "{v[64:95]}"(W[2]), "{v[96:127]}"(W[3])
#define CLB32_192 CLB8(192, 193, 194, 195, 196, 197, 198, 199), CLB8(200, 201, 202, 203, 204, 205, 206, 207), CLB8(208, 209, 210, 211, 212, 213, 214, 215), CLB8(216, 217, 218, 219, 220, 221, 222, 223)
__device__ __forceinline__ unsigned ld_flag0(__amdgpu_buffer_rsrc_t rs, int off) {
  return __builtin_amdgcn_raw_buffer_load_b32(rs, off, 0, 16);
}
__device__ __forceinline__ void glds16_sc1(const char* src, char* dst) {
  const unsigned d = __builtin_amdgcn_readfirstlane(lds_off(dst));
  unsigned keep;
  asm volatile("s_mov_b32 %0, m0\n\ts_mov_b32 m0, %2\n\ts_nop 0\n\tglobal_load_lds_dwordx4 %1, off sc1\n\ts_mov_b32 m0, %0"
               : "=&s"(keep) : "v"(src), "s"(d) : "memory");
}

__device__ __forceinline__ void touch_line(const char* src, char* dst) {
  const unsigned d = __builtin_amdgcn_readfirstlane(lds_off(dst));
  unsigned keep;
  asm volatile("s_mov_b32 %0, m0\n\ts_mov_b32 m0, %2\n\ts_nop 0\n\tglobal_load_lds_dword %1, off sc1\n\ts_mov_b32 m0, %0"
               : "=&s"(keep) : "v"(src), "s"(d) : "memory");
}

#define PF_N 12
#define PR_G1 8
#define PF_T0 0
#define WREV 1
template <bool DEV = false>
__device__ __forceinline__ void glds2(const char* base, int step, unsigned voff, unsigned dst, bool nt = false) {
  unsigned keep;
  const char* base2 = base + step;
  if (nt)
    asm volatile("s_mov_b32 %0, m0\n\ts_mov_b32 m0, %4\n\ts_nop 0\n\tglobal_load_lds_dwordx4 %1, %2 nt\n\t"
                 "s_add_u32 m0, m0, 0x2000\n\ts_nop 0\n\tglobal_load_lds_dwordx4 %1, %3 nt\n\ts_mov_b32 m0, %0"
                 : "=&s"(keep) : "v"(voff), "s"(base), "s"(base2), "s"(dst) : "memory", "scc");
  else
  if (DEV)
    asm volatile("s_mov_b32 %0, m0\n\ts_mov_b32 m0, %4\n\ts_nop 0\n\tglobal_load_lds_dwordx4 %1, %2 sc1\n\t"
                 "s_add_u32 m0, m0, 0x2000\n\ts_nop 0\n\tglobal_load_lds_dwordx4 %1, %3 sc1\n\ts_mov_b32 m0, %0"
                 : "=&s"(keep) : "v"(voff), "s"(base), "s"(base2), "s"(dst) : "memory", "scc");
  else
  asm volatile("s_mov_b32 %0, m0\n\ts_mov_b32 m0, %4\n\ts_nop 0\n\tglobal_load_lds_dwordx4 %1, %2\n\t"
               "s_add_u32 m0, m0, 0x2000\n\ts_nop 0\n\tglobal_load_lds_dwordx4 %1, %3\n\ts_mov_b32 m0, %0"
               : "=&s"(keep) : "v"(voff), "s"(base), "s"(base2), "s"(dst) : "memory", "scc");
}

#define DN_ASM \
  "s_mov_b32 s94, m0\n\t" \
  "s_mov_b64 s[84:85], %[pa]\n\t" \
  "s_mov_b64 s[86:87], %[pb]\n\t" \
  "s_movk_i32 s88, 0x4000\n\t" \
  "s_mov_b32 s89, 0x10000\n\t" \
  "s_movk_i32 s90, 13\n\t" \
  "v_mov_b64 v[0:1], 0\n\t" \
  "v_mov_b64 v[2:3], 0\n\t" \
  "v_mov_b64 v[4:5], 0\n\t" \
  "v_mov_b64 v[6:7], 0\n\t" \
  "v_mov_b64 v[8:9], 0\n\t" \
  "v_mov_b64 v[10:11], 0\n\t" \
  "v_mov_b64 v[12:13], 0\n\t" \
  "v_mov_b64 v[14:15], 0\n\t" \
  "v_mov_b64 v[16:17], 0\n\t" \
  "v_mov_b64 v[18:19], 0\n\t" \
  "v_mov_b64 v[20:21], 0\n\t" \
  "v_mov_b64 v[22:23], 0\n\t" \
  "v_mov_b64 v[24:25], 0\n\t" \
  "v_mov_b64 v[26:27], 0\n\t" \
  "v_mov_b64 v[28:29], 0\n\t" \
  "v_mov_b64 v[30:31], 0\n\t" \
  "s_cmp_lg_u32 %[hf], 0\n\t" \
  "s_cbranch_scc1 .Lk_%=\n\t" \
  "ds_read_b128 v[32:35], v80 offset:0\n\t" \
  "ds_read_b128 v[36:39], v80 offset:1024\n\t" \
  "ds_read_b128 v[40:43], v80 offset:2048\n\t" \
  "ds_read_b128 v[44:47], v80 offset:3072\n\t" \
  "ds_read_b64_tr_b16 v[64:65], v82 offset:0\n\t" \
  "ds_read_b64_tr_b16 v[66:67], v83 offset:0\n\t" \
  "ds_read_b128 v[48:51], v80 offset:8192\n\t" \
  "ds_read_b64_tr_b16 v[68:69], v84 offset:0\n\t" \
  "ds_read_b64_tr_b16 v[70:71], v85 offset:0\n\t" \
  "ds_read_b128 v[52:55], v80 offset:9216\n\t" \
  "ds_read_b128 v[56:59], v80 offset:10240\n\t" \
  "ds_read_b128 v[60:63], v80 offset:11264\n\t" \
  ".Lp0_%=:\n\t" \
  "s_waitcnt vmcnt(4)\n\t" \
  "s_barrier\n\t" \
  "s_waitcnt lgkmcnt(3)\n\t" \
  "v_mfma_f32_16x16x32_bf16 v[0:3], v[64:67], v[32:35], v[0:3]\n\t" \
  "ds_read_b64_tr_b16 v[72:73], v82 offset:8192\n\t" \
  "v_mfma_f32_16x16x32_bf16 v[4:7], v[68:71], v[32:35], v[4:7]\n\t" \
  "ds_read_b64_tr_b16 v[74:75], v83 offset:8192\n\t" \
  "ds_read_b128 v[32:35], v80 offset:32768\n\t" \
  "s_add_u32 m0, %[lw], 98304\n\t" \
  "v_mfma_f32_16x16x32_bf16 v[8:11], v[64:67], v[36:39], v[8:11]\n\t" \
  "ds_read_b64_tr_b16 v[76:77], v84 offset:8192\n\t" \
  "global_load_lds_dwordx4 v90, s[84:85] sc1\n\t" \
  "s_add_u32 s84, s84, s88\n\t" \
  "s_addc_u32 s85, s85, 0\n\t" \
  "v_mfma_f32_16x16x32_bf16 v[12:15], v[68:71], v[36:39], v[12:15]\n\t" \
  "ds_read_b64_tr_b16 v[78:79], v85 offset:8192\n\t" \
  "ds_read_b128 v[36:39], v80 offset:33792\n\t" \
  "v_mfma_f32_16x16x32_bf16 v[16:19], v[64:67], v[40:43], v[16:19]\n\t" \
  "v_mfma_f32_16x16x32_bf16 v[20:23], v[68:71], v[40:43], v[20:23]\n\t" \
  "ds_read_b128 v[40:43], v80 offset:34816\n\t" \
  "s_add_u32 m0, %[lw], 114688\n\t" \
  "v_mfma_f32_16x16x32_bf16 v[24:27], v[64:67], v[44:47], v[24:27]\n\t" \
  "global_load_lds_dwordx4 v91, s[86:87]\n\t" \
  "s_add_u32 s86, s86, s89\n\t" \
  "s_addc_u32 s87, s87, 0\n\t" \
  "v_mfma_f32_16x16x32_bf16 v[28:31], v[68:71], v[44:47], v[28:31]\n\t" \
  "ds_read_b128 v[44:47], v80 offset:35840\n\t" \
  "s_waitcnt lgkmcnt(3)\n\t" \
  "v_mfma_f32_16x16x32_bf16 v[0:3], v[72:75], v[48:51], v[0:3]\n\t" \
  "ds_read_b64_tr_b16 v[64:65], v82 offset:32768\n\t" \
  "v_mfma_f32_16x16x32_bf16 v[4:7], v[76:79], v[48:51], v[4:7]\n\t" \
  "ds_read_b64_tr_b16 v[66:67], v83 offset:32768\n\t" \
  "ds_read_b128 v[48:51], v80 offset:40960\n\t" \
  "s_add_u32 m0, %[lw], 106496\n\t" \
  "v_mfma_f32_16x16x32_bf16 v[8:11], v[72:75], v[52:55], v[8:11]\n\t" \
  "ds_read_b64_tr_b16 v[68:69], v84 offset:32768\n\t" \
  "global_load_lds_dwordx4 v90, s[84:85] sc1\n\t" \
  "s_add_u32 s84, s84, s88\n\t" \
  "s_addc_u32 s85, s85, 0\n\t" \
  "v_mfma_f32_16x16x32_bf16 v[12:15], v[76:79], v[52:55], v[12:15]\n\t" \
  "ds_read_b64_tr_b16 v[70:71], v85 offset:32768\n\t" \
  "ds_read_b128 v[52:55], v80 offset:41984\n\t" \
  "v_mfma_f32_16x16x32_bf16 v[16:19], v[72:75], v[56:59], v[16:19]\n\t" \
  "v_mfma_f32_16x16x32_bf16 v[20:23], v[76:79], v[56:59], v[20:23]\n\t" \
  "ds_read_b128 v[56:59], v80 offset:43008\n\t" \
  "s_add_u32 m0, %[lw], 122880\n\t" \
  "v_mfma_f32_16x16x32_bf16 v[24:27], v[72:75], v[60:63], v[24:27]\n\t" \
  "global_load_lds_dwordx4 v91, s[86:87]\n\t" \
  "s_add_u32 s86, s86, s89\n\t" \
  "s_addc_u32 s87, s87, 0\n\t" \
  "v_mfma_f32_16x16x32_bf16 v[28:31], v[76:79], v[60:63], v[28:31]\n\t" \
  "ds_read_b128 v[60:63], v80 offset:44032\n\t" \
  "s_sub_u32 s90, s90, 1\n\t" \
  "s_cselect_b32 s88, 0, s88\n\t" \
  "s_cselect_b32 s89, 0, s89\n\t" \
  "s_cselect_b32 s91, 16384, 0\n\t" \
  "s_sub_u32 s84, s84, s91\n\t" \
  "s_subb_u32 s85, s85, 0\n\t" \
  "s_lshl_b32 s91, s91, 2\n\t" \
  "s_sub_u32 s86, s86, s91\n\t" \
  "s_subb_u32 s87, s87, 0\n\t" \
  "s_waitcnt vmcnt(4)\n\t" \
  "s_barrier\n\t" \
  "s_waitcnt lgkmcnt(3)\n\t" \
  "v_mfma_f32_16x16x32_bf16 v[0:3], v[64:67], v[32:35], v[0:3]\n\t" \
  "ds_read_b64_tr_b16 v[72:73], v82 offset:40960\n\t" \
  "v_mfma_f32_16x16x32_bf16 v[4:7], v[68:71], v[32:35], v[4:7]\n\t" \
  "ds_read_b64_tr_b16 v[74:75], v83 offset:40960\n\t" \
  "ds_read_b128 v[32:35], v81 offset:0\n\t" \
  "s_add_u32 m0, %[lw], 0\n\t" \
  "v_mfma_f32_16x16x32_bf16 v[8:11], v[64:67], v[36:39], v[8:11]\n\t" \
  "ds_read_b64_tr_b16 v[76:77], v84 offset:40960\n\t" \
  "global_load_lds_dwordx4 v90, s[84:85] sc1\n\t" \
  "s_add_u32 s84, s84, s88\n\t" \
  "s_addc_u32 s85, s85, 0\n\t" \
  "v_mfma_f32_16x16x32_bf16 v[12:15], v[68:71], v[36:39], v[12:15]\n\t" \
  "ds_read_b64_tr_b16 v[78:79], v85 offset:40960\n\t" \
  "ds_read_b128 v[36:39], v81 offset:1024\n\t" \
  "v_mfma_f32_16x16x32_bf16 v[16:19], v[64:67], v[40:43], v[16:19]\n\t" \
  "v_mfma_f32_16x16x32_bf16 v[20:23], v[68:71], v[40:43], v[20:23]\n\t" \
  "ds_read_b128 v[40:43], v81 offset:2048\n\t" \
  "s_add_u32 m0, %[lw], 16384\n\t" \
  "v_mfma_f32_16x16x32_bf16 v[24:27], v[64:67], v[44:47], v[24:27]\n\t" \
  "global_load_lds_dwordx4 v91, s[86:87]\n\t" \
  "s_add_u32 s86, s86, s89\n\t" \
  "s_addc_u32 s87, s87, 0\n\t" \
  "v_mfma_f32_16x16x32_bf16 v[28:31], v[68:71], v[44:47], v[28:31]\n\t" \
  "ds_read_b128 v[44:47], v81 offset:3072\n\t" \
  "s_waitcnt lgkmcnt(3)\n\t" \
  "v_mfma_f32_16x16x32_bf16 v[0:3], v[72:75], v[48:51], v[0:3]\n\t" \
  "ds_read_b64_tr_b16 v[64:65], v86 offset:0\n\t" \
  "v_mfma_f32_16x16x32_bf16 v[4:7], v[76:79], v[48:51], v[4:7]\n\t" \
  "ds_read_b64_tr_b16 v[66:67], v87 offset:0\n\t" \
  "ds_read_b128 v[48:51], v81 offset:8192\n\t" \
  "s_add_u32 m0, %[lw], 8192\n\t" \
  "v_mfma_f32_16x16x32_bf16 v[8:11], v[72:75], v[52:55], v[8:11]\n\t" \
  "ds_read_b64_tr_b16 v[68:69], v88 offset:0\n\t" \
  "global_load_lds_dwordx4 v90, s[84:85] sc1\n\t" \
  "s_add_u32 s84, s84, s88\n\t" \
  "s_addc_u32 s85, s85, 0\n\t" \
  "v_mfma_f32_16x16x32_bf16 v[12:15], v[76:79], v[52:55], v[12:15]\n\t" \
  "ds_read_b64_tr_b16 v[70:71], v89 offset:0\n\t" \
  "ds_read_b128 v[52:55], v81 offset:9216\n\t" \
  "v_mfma_f32_16x16x32_bf16 v[16:19], v[72:75], v[56:59], v[16:19]\n\t" \
  "v_mfma_f32_16x16x32_bf16 v[20:23], v[76:79], v[56:59], v[20:23]\n\t" \
  "ds_read_b128 v[56:59], v81 offset:10240\n\t" \
  "s_add_u32 m0, %[lw], 24576\n\t" \
  "v_mfma_f32_16x16x32_bf16 v[24:27], v[72:75], v[60:63], v[24:27]\n\t" \
  "global_load_lds_dwordx4 v91, s[86:87]\n\t" \
  "s_add_u32 s86, s86, s89\n\t" \
  "s_addc_u32 s87, s87, 0\n\t" \
  "v_mfma_f32_16x16x32_bf16 v[28:31], v[76:79], v[60:63], v[28:31]\n\t" \
  "ds_read_b128 v[60:63], v81 offset:11264\n\t" \
  "s_waitcnt vmcnt(4)\n\t" \
  "s_barrier\n\t" \
  "s_waitcnt lgkmcnt(3)\n\t" \
  "v_mfma_f32_16x16x32_bf16 v[0:3], v[64:67], v[32:35], v[0:3]\n\t" \
  "ds_read_b64_tr_b16 v[72:73], v86 offset:8192\n\t" \
  "v_mfma_f32_16x16x32_bf16 v[4:7], v[68:71], v[32:35], v[4:7]\n\t" \
  "ds_read_b64_tr_b16 v[74:75], v87 offset:8192\n\t" \
  "ds_read_b128 v[32:35], v81 offset:32768\n\t" \
  "s_add_u32 m0, %[lw], 32768\n\t" \
  "v_mfma_f32_16x16x32_bf16 v[8:11], v[64:67], v[36:39], v[8:11]\n\t" \
  "ds_read_b64_tr_b16 v[76:77], v88 offset:8192\n\t" \
  "global_load_lds_dwordx4 v90, s[84:85] sc1\n\t" \
  "s_add_u32 s84, s84, s88\n\t" \
  "s_addc_u32 s85, s85, 0\n\t" \
  "v_mfma_f32_16x16x32_bf16 v[12:15], v[68:71], v[36:39], v[12:15]\n\t" \
  "ds_read_b64_tr_b16 v[78:79], v89 offset:8192\n\t" \
  "ds_read_b128 v[36:39], v81 offset:33792\n\t" \
  "v_mfma_f32_16x16x32_bf16 v[16:19], v[64:67], v[40:43], v[16:19]\n\t" \
  "v_mfma_f32_16x16x32_bf16 v[20:23], v[68:71], v[40:43], v[20:23]\n\t" \
  "ds_read_b128 v[40:43], v81 offset:34816\n\t" \
  "s_add_u32 m0, %[lw], 49152\n\t" \
  "v_mfma_f32_16x16x32_bf16 v[24:27], v[64:67], v[44:47], v[24:27]\n\t" \
  "global_load_lds_dwordx4 v91, s[86:87]\n\t" \
  "s_add_u32 s86, s86, s89\n\t" \
  "s_addc_u32 s87, s87, 0\n\t" \
  "v_mfma_f32_16x16x32_bf16 v[28:31], v[68:71], v[44:47], v[28:31]\n\t" \
  "ds_read_b128 v[44:47], v81 offset:35840\n\t" \
  "s_waitcnt lgkmcnt(3)\n\t" \
  "v_mfma_f32_16x16x32_bf16 v[0:3], v[72:75], v[48:51], v[0:3]\n\t" \
  "ds_read_b64_tr_b16 v[64:65], v86 offset:32768\n\t" \
  "v_mfma_f32_16x16x32_bf16 v[4:7], v[76:79], v[48:51], v[4:7]\n\t" \
  "ds_read_b64_tr_b16 v[66:67], v87 offset:32768\n\t" \
  "ds_read_b128 v[48:51], v81 offset:40960\n\t" \
  "s_add_u32 m0, %[lw], 40960\n\t" \
  "v_mfma_f32_16x16x32_bf16 v[8:11], v[72:75], v[52:55], v[8:11]\n\t" \
  "ds_read_b64_tr_b16 v[68:69], v88 offset:32768\n\t" \
  "global_load_lds_dwordx4 v90, s[84:85] sc1\n\t" \
  "s_add_u32 s84, s84, s88\n\t" \
  "s_addc_u32 s85, s85, 0\n\t" \
  "v_mfma_f32_16x16x32_bf16 v[12:15], v[76:79], v[52:55], v[12:15]\n\t" \
  "ds_read_b64_tr_b16 v[70:71], v89 offset:32768\n\t" \
  "ds_read_b128 v[52:55], v81 offset:41984\n\t" \
  "v_mfma_f32_16x16x32_bf16 v[16:19], v[72:75], v[56:59], v[16:19]\n\t" \
  "v_mfma_f32_16x16x32_bf16 v[20:23], v[76:79], v[56:59], v[20:23]\n\t" \
  "ds_read_b128 v[56:59], v81 offset:43008\n\t" \
  "s_add_u32 m0, %[lw], 57344\n\t" \
  "v_mfma_f32_16x16x32_bf16 v[24:27], v[72:75], v[60:63], v[24:27]\n\t" \
  "global_load_lds_dwordx4 v91, s[86:87]\n\t" \
  "s_add_u32 s86, s86, s89\n\t" \
  "s_addc_u32 s87, s87, 0\n\t" \
  "v_mfma_f32_16x16x32_bf16 v[28:31], v[76:79], v[60:63], v[28:31]\n\t" \
  "ds_read_b128 v[60:63], v81 offset:44032\n\t" \
  "s_waitcnt vmcnt(4)\n\t" \
  "s_barrier\n\t" \
  "s_waitcnt lgkmcnt(3)\n\t" \
  "v_mfma_f32_16x16x32_bf16 v[0:3], v[64:67], v[32:35], v[0:3]\n\t" \
  "ds_read_b64_tr_b16 v[72:73], v86 offset:40960\n\t" \
  "v_mfma_f32_16x16x32_bf16 v[4:7], v[68:71], v[32:35], v[4:7]\n\t" \
  "ds_read_b64_tr_b16 v[74:75], v87 offset:40960\n\t" \
  "ds_read_b128 v[32:35], v80 offset:0\n\t" \
  "s_add_u32 m0, %[lw], 65536\n\t" \
  "v_mfma_f32_16x16x32_bf16 v[8:11], v[64:67], v[36:39], v[8:11]\n\t" \
  "ds_read_b64_tr_b16 v[76:77], v88 offset:40960\n\t" \
  "global_load_lds_dwordx4 v90, s[84:85] sc1\n\t" \
  "s_add_u32 s84, s84, s88\n\t" \
  "s_addc_u32 s85, s85, 0\n\t" \
  "v_mfma_f32_16x16x32_bf16 v[12:15], v[68:71], v[36:39], v[12:15]\n\t" \
  "ds_read_b64_tr_b16 v[78:79], v89 offset:40960\n\t" \
  "ds_read_b128 v[36:39], v80 offset:1024\n\t" \
  "v_mfma_f32_16x16x32_bf16 v[16:19], v[64:67], v[40:43], v[16:19]\n\t" \
  "v_mfma_f32_16x16x32_bf16 v[20:23], v[68:71], v[40:43], v[20:23]\n\t" \
  "ds_read_b128 v[40:43], v80 offset:2048\n\t" \
  "s_add_u32 m0, %[lw], 81920\n\t" \
  "v_mfma_f32_16x16x32_bf16 v[24:27], v[64:67], v[44:47], v[24:27]\n\t" \
  "global_load_lds_dwordx4 v91, s[86:87]\n\t" \
  "s_add_u32 s86, s86, s89\n\t" \
  "s_addc_u32 s87, s87, 0\n\t" \
  "v_mfma_f32_16x16x32_bf16 v[28:31], v[68:71], v[44:47], v[28:31]\n\t" \
  "ds_read_b128 v[44:47], v80 offset:3072\n\t" \
  "s_waitcnt lgkmcnt(3)\n\t" \
  "v_mfma_f32_16x16x32_bf16 v[0:3], v[72:75], v[48:51], v[0:3]\n\t" \
  "ds_read_b64_tr_b16 v[64:65], v82 offset:0\n\t" \
  "v_mfma_f32_16x16x32_bf16 v[4:7], v[76:79], v[48:51], v[4:7]\n\t" \
  "ds_read_b64_tr_b16 v[66:67], v83 offset:0\n\t" \
  "ds_read_b128 v[48:51], v80 offset:8192\n\t" \
  "s_add_u32 m0, %[lw], 73728\n\t" \
  "v_mfma_f32_16x16x32_bf16 v[8:11], v[72:75], v[52:55], v[8:11]\n\t" \
  "ds_read_b64_tr_b16 v[68:69], v84 offset:0\n\t" \
  "global_load_lds_dwordx4 v90, s[84:85] sc1\n\t" \
  "s_add_u32 s84, s84, s88\n\t" \
  "s_addc_u32 s85, s85, 0\n\t" \
  "v_mfma_f32_16x16x32_bf16 v[12:15], v[76:79], v[52:55], v[12:15]\n\t" \
  "ds_read_b64_tr_b16 v[70:71], v85 offset:0\n\t" \
  "ds_read_b128 v[52:55], v80 offset:9216\n\t" \
  "v_mfma_f32_16x16x32_bf16 v[16:19], v[72:75], v[56:59], v[16:19]\n\t" \
  "v_mfma_f32_16x16x32_bf16 v[20:23], v[76:79], v[56:59], v[20:23]\n\t" \
  "ds_read_b128 v[56:59], v80 offset:10240\n\t" \
  "s_add_u32 m0, %[lw], 90112\n\t" \
  "v_mfma_f32_16x16x32_bf16 v[24:27], v[72:75], v[60:63], v[24:27]\n\t" \
  "global_load_lds_dwordx4 v91, s[86:87]\n\t" \
  "s_add_u32 s86, s86, s89\n\t" \
  "s_addc_u32 s87, s87, 0\n\t" \
  "v_mfma_f32_16x16x32_bf16 v[28:31], v[76:79], v[60:63], v[28:31]\n\t" \
  "ds_read_b128 v[60:63], v80 offset:11264\n\t" \
  "s_cmp_lg_u32 s88, 0\n\t" \
  "s_cbranch_scc1 .Lp0_%=\n\t" \
  "s_branch .Le_%=\n\t" \
  ".Lk_%=:\n\t" \
  "ds_read_b128 v[32:35], v80 offset:0\n\t" \
  "ds_read_b128 v[36:39], v80 offset:1024\n\t" \
  "ds_read_b128 v[40:43], v80 offset:2048\n\t" \
  "ds_read_b128 v[44:47], v80 offset:3072\n\t" \
  "ds_read_b64_tr_b16 v[64:65], v82 offset:0\n\t" \
  "ds_read_b64_tr_b16 v[66:67], v83 offset:0\n\t" \
  "ds_read_b128 v[48:51], v80 offset:8192\n\t" \
  "ds_read_b64_tr_b16 v[68:69], v84 offset:0\n\t" \
  "ds_read_b64_tr_b16 v[70:71], v85 offset:0\n\t" \
  "ds_read_b128 v[52:55], v80 offset:9216\n\t" \
  "ds_read_b128 v[56:59], v80 offset:10240\n\t" \
  "ds_read_b128 v[60:63], v80 offset:11264\n\t" \
  ".Lp1_%=:\n\t" \
  "s_waitcnt vmcnt(4)\n\t" \
  "s_barrier\n\t" \
  "s_waitcnt lgkmcnt(3)\n\t" \
  "v_mfma_f32_16x16x32_bf16 v[0:3], v[64:67], v[32:35], v[0:3]\n\t" \
  "ds_read_b64_tr_b16 v[72:73], v82 offset:8192\n\t" \
  "v_mfma_f32_16x16x32_bf16 v[4:7], v[68:71], v[32:35], v[4:7]\n\t" \
  "ds_read_b64_tr_b16 v[74:75], v83 offset:8192\n\t" \
  "ds_read_b128 v[32:35], v80 offset:32768\n\t" \
  "v_mfma_f32_16x16x32_bf16 v[8:11], v[64:67], v[36:39], v[8:11]\n\t" \
  "ds_read_b64_tr_b16 v[76:77], v84 offset:8192\n\t" \
  "v_mfma_f32_16x16x32_bf16 v[12:15], v[68:71], v[36:39], v[12:15]\n\t" \
  "ds_read_b64_tr_b16 v[78:79], v85 offset:8192\n\t" \
  "ds_read_b128 v[36:39], v80 offset:33792\n\t" \
  "s_add_u32 m0, %[lw], 98304\n\t" \
  "v_mfma_f32_16x16x32_bf16 v[16:19], v[64:67], v[40:43], v[16:19]\n\t" \
  "global_load_lds_dwordx4 v90, s[84:85] sc1\n\t" \
  "s_add_u32 s84, s84, s88\n\t" \
  "s_addc_u32 s85, s85, 0\n\t" \
  "v_mfma_f32_16x16x32_bf16 v[20:23], v[68:71], v[40:43], v[20:23]\n\t" \
  "ds_read_b128 v[40:43], v80 offset:34816\n\t" \
  "v_mfma_f32_16x16x32_bf16 v[24:27], v[64:67], v[44:47], v[24:27]\n\t" \
  "s_add_u32 m0, %[lw], 114688\n\t" \
  "v_mfma_f32_16x16x32_bf16 v[28:31], v[68:71], v[44:47], v[28:31]\n\t" \
  "ds_read_b128 v[44:47], v80 offset:35840\n\t" \
  "global_load_lds_dwordx4 v91, s[86:87]\n\t" \
  "s_add_u32 s86, s86, s89\n\t" \
  "s_addc_u32 s87, s87, 0\n\t" \
  "s_waitcnt lgkmcnt(3)\n\t" \
  "v_mfma_f32_16x16x32_bf16 v[0:3], v[72:75], v[48:51], v[0:3]\n\t" \
  "ds_read_b64_tr_b16 v[64:65], v82 offset:32768\n\t" \
  "v_mfma_f32_16x16x32_bf16 v[4:7], v[76:79], v[48:51], v[4:7]\n\t" \
  "ds_read_b64_tr_b16 v[66:67], v83 offset:32768\n\t" \
  "ds_read_b128 v[48:51], v80 offset:40960\n\t" \
  "v_mfma_f32_16x16x32_bf16 v[8:11], v[72:75], v[52:55], v[8:11]\n\t" \
  "ds_read_b64_tr_b16 v[68:69], v84 offset:32768\n\t" \
  "v_mfma_f32_16x16x32_bf16 v[12:15], v[76:79], v[52:55], v[12:15]\n\t" \
  "ds_read_b64_tr_b16 v[70:71], v85 offset:32768\n\t" \
  "ds_read_b128 v[52:55], v80 offset:41984\n\t" \
  "s_add_u32 m0, %[lw], 106496\n\t" \
  "v_mfma_f32_16x16x32_bf16 v[16:19], v[72:75], v[56:59], v[16:19]\n\t" \
  "global_load_lds_dwordx4 v90, s[84:85] sc1\n\t" \
  "s_add_u32 s84, s84, s88\n\t" \
  "s_addc_u32 s85, s85, 0\n\t" \
  "v_mfma_f32_16x16x32_bf16 v[20:23], v[76:79], v[56:59], v[20:23]\n\t" \
  "ds_read_b128 v[56:59], v80 offset:43008\n\t" \
  "v_mfma_f32_16x16x32_bf16 v[24:27], v[72:75], v[60:63], v[24:27]\n\t" \
  "s_add_u32 m0, %[lw], 122880\n\t" \
  "v_mfma_f32_16x16x32_bf16 v[28:31], v[76:79], v[60:63], v[28:31]\n\t" \
  "ds_read_b128 v[60:63], v80 offset:44032\n\t" \
  "global_load_lds_dwordx4 v91, s[86:87]\n\t" \
  "s_add_u32 s86, s86, s89\n\t" \
  "s_addc_u32 s87, s87, 0\n\t" \
  "s_sub_u32 s90, s90, 1\n\t" \
  "s_cselect_b32 s88, 0, s88\n\t" \
  "s_cselect_b32 s89, 0, s89\n\t" \
  "s_cselect_b32 s91, 16384, 0\n\t" \
  "s_sub_u32 s84, s84, s91\n\t" \
  "s_subb_u32 s85, s85, 0\n\t" \
  "s_lshl_b32 s91, s91, 2\n\t" \
  "s_sub_u32 s86, s86, s91\n\t" \
  "s_subb_u32 s87, s87, 0\n\t" \
  "s_waitcnt vmcnt(4)\n\t" \
  "s_barrier\n\t" \
  "s_waitcnt lgkmcnt(3)\n\t" \
  "v_mfma_f32_16x16x32_bf16 v[0:3], v[64:67], v[32:35], v[0:3]\n\t" \
  "ds_read_b64_tr_b16 v[72:73], v82 offset:40960\n\t" \
  "v_mfma_f32_16x16x32_bf16 v[4:7], v[68:71], v[32:35], v[4:7]\n\t" \
  "ds_read_b64_tr_b16 v[74:75], v83 offset:40960\n\t" \
  "ds_read_b128 v[32:35], v81 offset:0\n\t" \
  "v_mfma_f32_16x16x32_bf16 v[8:11], v[64:67], v[36:39], v[8:11]\n\t" \
  "ds_read_b64_tr_b16 v[76:77], v84 offset:40960\n\t" \
  "v_mfma_f32_16x16x32_bf16 v[12:15], v[68:71], v[36:39], v[12:15]\n\t" \
  "ds_read_b64_tr_b16 v[78:79], v85 offset:40960\n\t" \
  "ds_read_b128 v[36:39], v81 offset:1024\n\t" \
  "s_add_u32 m0, %[lw], 0\n\t" \
  "v_mfma_f32_16x16x32_bf16 v[16:19], v[64:67], v[40:43], v[16:19]\n\t" \
  "global_load_lds_dwordx4 v90, s[84:85] sc1\n\t" \
  "s_add_u32 s84, s84, s88\n\t" \
  "s_addc_u32 s85, s85, 0\n\t" \
  "v_mfma_f32_16x16x32_bf16 v[20:23], v[68:71], v[40:43], v[20:23]\n\t" \
  "ds_read_b128 v[40:43], v81 offset:2048\n\t" \
  "v_mfma_f32_16x16x32_bf16 v[24:27], v[64:67], v[44:47], v[24:27]\n\t" \
  "s_add_u32 m0, %[lw], 16384\n\t" \
  "v_mfma_f32_16x16x32_bf16 v[28:31], v[68:71], v[44:47], v[28:31]\n\t" \
  "ds_read_b128 v[44:47], v81 offset:3072\n\t" \
  "global_load_lds_dwordx4 v91, s[86:87]\n\t" \
  "s_add_u32 s86, s86, s89\n\t" \
  "s_addc_u32 s87, s87, 0\n\t" \
  "s_waitcnt lgkmcnt(3)\n\t" \
  "v_mfma_f32_16x16x32_bf16 v[0:3], v[72:75], v[48:51], v[0:3]\n\t" \
  "ds_read_b64_tr_b16 v[64:65], v86 offset:0\n\t" \
  "v_mfma_f32_16x16x32_bf16 v[4:7], v[76:79], v[48:51], v[4:7]\n\t" \
  "ds_read_b64_tr_b16 v[66:67], v87 offset:0\n\t" \
  "ds_read_b128 v[48:51], v81 offset:8192\n\t" \
  "v_mfma_f32_16x16x32_bf16 v[8:11], v[72:75], v[52:55], v[8:11]\n\t" \
  "ds_read_b64_tr_b16 v[68:69], v88 offset:0\n\t" \
  "v_mfma_f32_16x16x32_bf16 v[12:15], v[76:79], v[52:55], v[12:15]\n\t" \
  "ds_read_b64_tr_b16 v[70:71], v89 offset:0\n\t" \
  "ds_read_b128 v[52:55], v81 offset:9216\n\t" \
  "s_add_u32 m0, %[lw], 8192\n\t" \
  "v_mfma_f32_16x16x32_bf16 v[16:19], v[72:75], v[56:59], v[16:19]\n\t" \
  "global_load_lds_dwordx4 v90, s[84:85] sc1\n\t" \
  "s_add_u32 s84, s84, s88\n\t" \
  "s_addc_u32 s85, s85, 0\n\t" \
  "v_mfma_f32_16x16x32_bf16 v[20:23], v[76:79], v[56:59], v[20:23]\n\t" \
  "ds_read_b128 v[56:59], v81 offset:10240\n\t" \
  "v_mfma_f32_16x16x32_bf16 v[24:27], v[72:75], v[60:63], v[24:27]\n\t" \
  "s_add_u32 m0, %[lw], 24576\n\t" \
  "v_mfma_f32_16x16x32_bf16 v[28:31], v[76:79], v[60:63], v[28:31]\n\t" \
  "ds_read_b128 v[60:63], v81 offset:11264\n\t" \
  "global_load_lds_dwordx4 v91, s[86:87]\n\t" \
  "s_add_u32 s86, s86, s89\n\t" \
  "s_addc_u32 s87, s87, 0\n\t" \
  "s_waitcnt vmcnt(4)\n\t" \
  "s_barrier\n\t" \
  "s_waitcnt lgkmcnt(3)\n\t" \
  "v_mfma_f32_16x16x32_bf16 v[0:3], v[64:67], v[32:35], v[0:3]\n\t" \
  "ds_read_b64_tr_b16 v[72:73], v86 offset:8192\n\t" \
  "v_mfma_f32_16x16x32_bf16 v[4:7], v[68:71], v[32:35], v[4:7]\n\t" \
  "ds_read_b64_tr_b16 v[74:75], v87 offset:8192\n\t" \
  "ds_read_b128 v[32:35], v81 offset:32768\n\t" \
  "v_mfma_f32_16x16x32_bf16 v[8:11], v[64:67], v[36:39], v[8:11]\n\t" \
  "ds_read_b64_tr_b16 v[76:77], v88 offset:8192\n\t" \
  "v_mfma_f32_16x16x32_bf16 v[12:15], v[68:71], v[36:39], v[12:15]\n\t" \
  "ds_read_b64_tr_b16 v[78:79], v89 offset:8192\n\t" \
  "ds_read_b128 v[36:39], v81 offset:33792\n\t" \
  "s_add_u32 m0, %[lw], 32768\n\t" \
  "v_mfma_f32_16x16x32_bf16 v[16:19], v[64:67], v[40:43], v[16:19]\n\t" \
  "global_load_lds_dwordx4 v90, s[84:85] sc1\n\t" \
  "s_add_u32 s84, s84, s88\n\t" \
  "s_addc_u32 s85, s85, 0\n\t" \
  "v_mfma_f32_16x16x32_bf16 v[20:23], v[68:71], v[40:43], v[20:23]\n\t" \
  "ds_read_b128 v[40:43], v81 offset:34816\n\t" \
  "v_mfma_f32_16x16x32_bf16 v[24:27], v[64:67], v[44:47], v[24:27]\n\t" \
  "s_add_u32 m0, %[lw], 49152\n\t" \
  "v_mfma_f32_16x16x32_bf16 v[28:31], v[68:71], v[44:47], v[28:31]\n\t" \
  "ds_read_b128 v[44:47], v81 offset:35840\n\t" \
  "global_load_lds_dwordx4 v91, s[86:87]\n\t" \
  "s_add_u32 s86, s86, s89\n\t" \
  "s_addc_u32 s87, s87, 0\n\t" \
  "s_waitcnt lgkmcnt(3)\n\t" \
  "v_mfma_f32_16x16x32_bf16 v[0:3], v[72:75], v[48:51], v[0:3]\n\t" \
  "ds_read_b64_tr_b16 v[64:65], v86 offset:32768\n\t" \
  "v_mfma_f32_16x16x32_bf16 v[4:7], v[76:79], v[48:51], v[4:7]\n\t" \
  "ds_read_b64_tr_b16 v[66:67], v87 offset:32768\n\t" \
  "ds_read_b128 v[48:51], v81 offset:40960\n\t" \
  "v_mfma_f32_16x16x32_bf16 v[8:11], v[72:75], v[52:55], v[8:11]\n\t" \
  "ds_read_b64_tr_b16 v[68:69], v88 offset:32768\n\t" \
  "v_mfma_f32_16x16x32_bf16 v[12:15], v[76:79], v[52:55], v[12:15]\n\t" \
  "ds_read_b64_tr_b16 v[70:71], v89 offset:32768\n\t" \
  "ds_read_b128 v[52:55], v81 offset:41984\n\t" \
  "s_add_u32 m0, %[lw], 40960\n\t" \
  "v_mfma_f32_16x16x32_bf16 v[16:19], v[72:75], v[56:59], v[16:19]\n\t" \
  "global_load_lds_dwordx4 v90, s[84:85] sc1\n\t" \
  "s_add_u32 s84, s84, s88\n\t" \
  "s_addc_u32 s85, s85, 0\n\t" \
  "v_mfma_f32_16x16x32_bf16 v[20:23], v[76:79], v[56:59], v[20:23]\n\t" \
  "ds_read_b128 v[56:59], v81 offset:43008\n\t" \
  "v_mfma_f32_16x16x32_bf16 v[24:27], v[72:75], v[60:63], v[24:27]\n\t" \
  "s_add_u32 m0, %[lw], 57344\n\t" \
  "v_mfma_f32_16x16x32_bf16 v[28:31], v[76:79], v[60:63], v[28:31]\n\t" \
  "ds_read_b128 v[60:63], v81 offset:44032\n\t" \
  "global_load_lds_dwordx4 v91, s[86:87]\n\t" \
  "s_add_u32 s86, s86, s89\n\t" \
  "s_addc_u32 s87, s87, 0\n\t" \
  "s_waitcnt vmcnt(4)\n\t" \
  "s_barrier\n\t" \
  "s_waitcnt lgkmcnt(3)\n\t" \
  "v_mfma_f32_16x16x32_bf16 v[0:3], v[64:67], v[32:35], v[0:3]\n\t" \
  "ds_read_b64_tr_b16 v[72:73], v86 offset:40960\n\t" \
  "v_mfma_f32_16x16x32_bf16 v[4:7], v[68:71], v[32:35], v[4:7]\n\t" \
  "ds_read_b64_tr_b16 v[74:75], v87 offset:40960\n\t" \
  "ds_read_b128 v[32:35], v80 offset:0\n\t" \
  "v_mfma_f32_16x16x32_bf16 v[8:11], v[64:67], v[36:39], v[8:11]\n\t" \
  "ds_read_b64_tr_b16 v[76:77], v88 offset:40960\n\t" \
  "v_mfma_f32_16x16x32_bf16 v[12:15], v[68:71], v[36:39], v[12:15]\n\t" \
  "ds_read_b64_tr_b16 v[78:79], v89 offset:40960\n\t" \
  "ds_read_b128 v[36:39], v80 offset:1024\n\t" \
  "s_add_u32 m0, %[lw], 65536\n\t" \
  "v_mfma_f32_16x16x32_bf16 v[16:19], v[64:67], v[40:43], v[16:19]\n\t" \
  "global_load_lds_dwordx4 v90, s[84:85] sc1\n\t" \
  "s_add_u32 s84, s84, s88\n\t" \
  "s_addc_u32 s85, s85, 0\n\t" \
  "v_mfma_f32_16x16x32_bf16 v[20:23], v[68:71], v[40:43], v[20:23]\n\t" \
  "ds_read_b128 v[40:43], v80 offset:2048\n\t" \
  "v_mfma_f32_16x16x32_bf16 v[24:27], v[64:67], v[44:47], v[24:27]\n\t" \
  "s_add_u32 m0, %[lw], 81920\n\t" \
  "v_mfma_f32_16x16x32_bf16 v[28:31], v[68:71], v[44:47], v[28:31]\n\t" \
  "ds_read_b128 v[44:47], v80 offset:3072\n\t" \
  "global_load_lds_dwordx4 v91, s[86:87]\n\t" \
  "s_add_u32 s86, s86, s89\n\t" \
  "s_addc_u32 s87, s87, 0\n\t" \
  "s_waitcnt lgkmcnt(3)\n\t" \
  "v_mfma_f32_16x16x32_bf16 v[0:3], v[72:75], v[48:51], v[0:3]\n\t" \
  "ds_read_b64_tr_b16 v[64:65], v82 offset:0\n\t" \
  "v_mfma_f32_16x16x32_bf16 v[4:7], v[76:79], v[48:51], v[4:7]\n\t" \
  "ds_read_b64_tr_b16 v[66:67], v83 offset:0\n\t" \
  "ds_read_b128 v[48:51], v80 offset:8192\n\t" \
  "v_mfma_f32_16x16x32_bf16 v[8:11], v[72:75], v[52:55], v[8:11]\n\t" \
  "ds_read_b64_tr_b16 v[68:69], v84 offset:0\n\t" \
  "v_mfma_f32_16x16x32_bf16 v[12:15], v[76:79], v[52:55], v[12:15]\n\t" \
  "ds_read_b64_tr_b16 v[70:71], v85 offset:0\n\t" \
  "ds_read_b128 v[52:55], v80 offset:9216\n\t" \
  "s_add_u32 m0, %[lw], 73728\n\t" \
  "v_mfma_f32_16x16x32_bf16 v[16:19], v[72:75], v[56:59], v[16:19]\n\t" \
  "global_load_lds_dwordx4 v90, s[84:85] sc1\n\t" \
  "s_add_u32 s84, s84, s88\n\t" \
  "s_addc_u32 s85, s85, 0\n\t" \
  "v_mfma_f32_16x16x32_bf16 v[20:23], v[76:79], v[56:59], v[20:23]\n\t" \
  "ds_read_b128 v[56:59], v80 offset:10240\n\t" \
  "v_mfma_f32_16x16x32_bf16 v[24:27], v[72:75], v[60:63], v[24:27]\n\t" \
  "s_add_u32 m0, %[lw], 90112\n\t" \
  "v_mfma_f32_16x16x32_bf16 v[28:31], v[76:79], v[60:63], v[28:31]\n\t" \
  "ds_read_b128 v[60:63], v80 offset:11264\n\t" \
  "global_load_lds_dwordx4 v91, s[86:87]\n\t" \
  "s_add_u32 s86, s86, s89\n\t" \
  "s_addc_u32 s87, s87, 0\n\t" \
  "s_cmp_lg_u32 s88, 0\n\t" \
  "s_cbranch_scc1 .Lp1_%=\n\t" \
  ".Le_%=:\n\t" \
  "s_waitcnt vmcnt(0) lgkmcnt(0)\n\t" \
  "s_mov_b32 m0, s94\n\t" \
  "s_setprio 0\n\t" \
  "s_nop 7\n\t"
#define HSL 16
__device__ __forceinline__ void job_down(const Args& p, int rblk, int ct, char* smem, __amdgpu_buffer_rsrc_t rsy) {
  if (threadIdx.x < 64) {
    for (unsigned spins = 0;;) {
      asm volatile("" ::: "memory");
      if (__builtin_amdgcn_readfirstlane((int)ld_flag0(rsy, SY_CH + (rblk >> 1) * 128)) >= 27) break;
      __builtin_amdgcn_s_sleep(HSL);
      if (spin_fail(spins, (gu32*)(p.ws + WS_SY + SY_ABORT))) break;
    }
  }
  int tid = threadIdx.x;
  asm volatile("" : "+v"(tid));
  const int lane = tid & 63, r = lane & 15, q = lane >> 4;
  const int wid = __builtin_amdgcn_readfirstlane(tid >> 6), wr = wid >> 2, wc = wid & 3;
  typedef __attribute__((ext_vector_type(32))) float f32x32;
  typedef __attribute__((ext_vector_type(8))) unsigned u32x8;
  f32x32 acc;
  {
    const char* A = p.ws + WS_HF + h_off(p, rblk);
    const char* B = (const char*)(p.wd + ct * 128);
    const unsigned rb = tid >> 4, va = tid * 16, vb = ((rb & 4) * 4 + (rb >> 3) * 4 + (rb & 3)) * (DM * 2) + 16 * ((tid & 15) ^ swzB(rb & 15));
    u32x8 lb;
#pragma unroll
    for (int i = 0; i < 8; ++i)
      lb[i] = (i >> 2) * 65536 + 16384 + offB(8 * q + 4 * (i & 1) + (r >> 2), 2 * (wc * 2 + ((i >> 1) & 1)) + ((r & 3) >> 1)) + 8 * (r & 1);
    LBAR();
#pragma unroll
    for (int t = 0; t < 3; ++t) {
      glds2<true>(A + t * 32768, 16384, va, wid * 1024 + t * 32768);
      glds2(B + t * 64 * DM * 2, 32 * DM * 2, vb, wid * 1024 + t * 32768 + 16384);
    }
    WAIT_V(8);
    LBAR();
    asm volatile(DN_ASM
        : "={v[0:31]}"(acc)
        : "{v[80:81]}"((u32x2){wr * 4096u + lane * 16u, 65536u + wr * 4096u + lane * 16u}), "{v[82:89]}"(lb), "{v90}"(va), "{v91}"(vb),
          [pa] "s"(A + 3 * 32768), [pb] "s"(B + 3 * 64 * DM * 2), [lw] "s"(wid * 1024), [hf] "s"(wr)
        : CLB8(32, 33, 34, 35, 36, 37, 38, 39), CLB8(40, 41, 42, 43, 44, 45, 46, 47), CLB8(48, 49, 50, 51, 52, 53, 54, 55),
          CLB8(56, 57, 58, 59, 60, 61, 62, 63), CLB8(64, 65, 66, 67, 68, 69, 70, 71), CLB8(72, 73, 74, 75, 76, 77, 78, 79),
          "s84", "s85", "s86", "s87", "s88", "s89", "s90", "s91", "s94", "scc", "memory");
  }
  wait_s(p, rsy, rblk, ct * 4, 4, rblk < NFUSE);
  const int e0 = (rblk * 128 + wr * 64 + r) * DM + ct * 128 + wc * 32 + q * 8;
  SX sx[4];
#pragma unroll
  for (int m = 0; m < 4; ++m) sx[m] = get_sx(p, rblk, e0 + m * 16 * DM);
  const __amdgpu_buffer_rsrc_t ro = mkrsrc(p.ws + WS_G, S_LEN * DM * 2);
#pragma unroll
  for (int m = 0; m < 4; ++m) {
    float rr[8];
#pragma unroll
    for (int j = 0; j < 8; ++j) { float d = rbf(acc[8 * m + j]); rr[j] = d * __builtin_amdgcn_rcpf(1.0f + __expf(-d)); }
    put_g(p, ro, rblk, e0 + m * 16 * DM, widen32(cvtpk(rr[0], rr[1]), cvtpk(rr[2], rr[3]), cvtpk(rr[4], rr[5]), cvtpk(rr[6], rr[7])), sx[m]);
  }
  if (rblk >= NFUSE) publish_flags(rsy, SY_FG + (rblk * 8 + ct) * 128, 0, 1);
}

#define PR_ASM_0 \
  "s_cmp_lg_u32 %[kh], 0\n\t" \
  "s_cbranch_scc1 .Lk_%=\n\t" \
  "s_mov_b32 s94, m0\n\t" \
  "s_mov_b64 s[92:93], %[x0]\n\t" \
  "s_mov_b32 s86, %[g0]\n\t" \
  "s_add_u32 s87, s86, %[t0]\n\t" \
  "s_sub_i32 s88, s86, 3\n\t" \
  "ds_read_b128 v[176:179], v208 offset:0\n\t" \
  "ds_read_b128 v[180:183], v208 offset:16384\n\t" \
  "ds_read_b128 v[184:187], v209 offset:0\n\t" \
  "ds_read_b128 v[188:191], v209 offset:16384\n\t" \
  "ds_read_b128 v[192:195], v210 offset:0\n\t" \
  "ds_read_b128 v[196:199], v210 offset:16384\n\t" \
  "ds_read_b128 v[200:203], v211 offset:0\n\t" \
  "ds_read_b128 v[204:207], v211 offset:16384\n\t" \
  "s_branch .Lfb00_%=\n\t" \
  ".Lp0_%=:\n\t" \
  "s_cmp_le_u32 s86, 5\n\t" \
  "s_cbranch_scc1 .Lf00_%=\n\t" \
  ".Lfb00_%=:\n\t" \
  "s_waitcnt lgkmcnt(6)\n\t" \
  "v_mfma_f32_16x16x32_bf16 v[128:131], v[0:3], v[176:179], 0\n\t" \
  "v_mfma_f32_16x16x32_bf16 v[132:135], v[4:7], v[176:179], 0\n\t" \
  "ds_read_b128 v[176:179], v208 offset:256\n\t" \
  "s_add_u32 m0, %[lw], 67584\n\t" \
  "v_mfma_f32_16x16x32_bf16 v[136:139], v[0:3], v[180:183], 0\n\t" \
  "v_mfma_f32_16x16x32_bf16 v[140:143], v[4:7], v[180:183], 0\n\t" \
  "ds_read_b128 v[180:183], v208 offset:16640\n\t" \
  "global_load_lds_dwordx4 v218, s[92:93] sc1\n\t" \
  "s_waitcnt lgkmcnt(6)\n\t" \
  "v_mfma_f32_16x16x32_bf16 v[128:131], v[8:11], v[184:187], v[128:131]\n\t" \
  "v_mfma_f32_16x16x32_bf16 v[132:135], v[12:15], v[184:187], v[132:135]\n\t" \
  "ds_read_b128 v[184:187], v209 offset:256\n\t" \
  "v_mfma_f32_16x16x32_bf16 v[136:139], v[8:11], v[188:191], v[136:139]\n\t" \
  "v_mfma_f32_16x16x32_bf16 v[140:143], v[12:15], v[188:191], v[140:143]\n\t" \
  "ds_read_b128 v[188:191], v209 offset:16640\n\t" \
  "ds_write_b128 v220, v[152:155] offset:0\n\t" \
  "ds_write_b128 v220, v[156:159] offset:1024\n\t" \
  "s_waitcnt lgkmcnt(8)\n\t" \
  "v_mfma_f32_16x16x32_bf16 v[128:131], v[16:19], v[192:195], v[128:131]\n\t" \
  "v_mfma_f32_16x16x32_bf16 v[132:135], v[20:23], v[192:195], v[132:135]\n\t" \
  "ds_read_b128 v[192:195], v210 offset:256\n\t" \
  "s_add_u32 m0, %[lw], 68608\n\t" \
  "v_mfma_f32_16x16x32_bf16 v[136:139], v[16:19], v[196:199], v[136:139]\n\t" \
  "v_mfma_f32_16x16x32_bf16 v[140:143], v[20:23], v[196:199], v[140:143]\n\t" \
  "ds_read_b128 v[196:199], v210 offset:16640\n\t" \
  "global_load_lds_dwordx4 v219, s[92:93] sc1\n\t" \
  "s_lshr_b32 s90, s87, 7\n\t" \
  "s_xor_b32 s90, s90, %[s0]\n\t" \
  "s_and_b32 s90, s90, 1\n\t" \
  "s_lshl1_add_u32 s90, s87, s90\n\t" \
  "s_lshl_b32 s90, s90, 13\n\t" \
  "s_cmp_lt_i32 s86, 6\n\t" \
  "s_cselect_b32 s90, 0x70000000, s90\n\t" \
  "s_waitcnt lgkmcnt(8)\n\t" \
  "v_mfma_f32_16x16x32_bf16 v[128:131], v[24:27], v[200:203], v[128:131]\n\t" \
  "v_mfma_f32_16x16x32_bf16 v[132:135], v[28:31], v[200:203], v[132:135]\n\t" \
  "ds_read_b128 v[200:203], v211 offset:256\n\t" \
  "v_mfma_f32_16x16x32_bf16 v[136:139], v[24:27], v[204:207], v[136:139]\n\t" \
  "v_mfma_f32_16x16x32_bf16 v[140:143], v[28:31], v[204:207], v[140:143]\n\t" \
  "ds_read_b128 v[204:207], v211 offset:16640\n\t" \
  "s_waitcnt lgkmcnt(4)\n\t" \
  "s_cmp_lt_u32 s86, 6\n\t" \
  "s_cbranch_scc1 .Lw00_%=\n\t" \
  "s_waitcnt vmcnt(4)\n\t" \
  "s_barrier\n\t" \
  ".Lg00_%=:\n\t" \
  "s_add_u32 s92, s92, 512\n\t" \
  "s_addc_u32 s93, s93, 0\n\t" \
  "v_mfma_f32_16x16x32_bf16 v[128:131], v[32:35], v[176:179], v[128:131]\n\t" \
  "v_mfma_f32_16x16x32_bf16 v[132:135], v[36:39], v[176:179], v[132:135]\n\t" \
  "ds_read_b128 v[176:179], v208 offset:32768\n\t" \
  "s_add_u32 m0, %[lw], 98304\n\t" \
  "v_mfma_f32_16x16x32_bf16 v[136:139], v[32:35], v[180:183], v[136:139]\n\t" \
  "v_mfma_f32_16x16x32_bf16 v[140:143], v[36:39], v[180:183], v[140:143]\n\t" \
  "ds_read_b128 v[180:183], v208 offset:49152\n\t" \
  "global_load_lds_dwordx4 v216, s[92:93] sc1\n\t" \
  "ds_read_b128 v[160:163], v221 offset:0\n\t" \
  "ds_read_b128 v[164:167], v221 offset:1024\n\t" \
  "v_mfma_f32_16x16x32_bf16 v[128:131], v[40:43], v[184:187], v[128:131]\n\t" \
  "v_mfma_f32_16x16x32_bf16 v[132:135], v[44:47], v[184:187], v[132:135]\n\t" \
  "ds_read_b128 v[184:187], v209 offset:32768\n\t" \
  "v_mfma_f32_16x16x32_bf16 v[136:139], v[40:43], v[188:191], v[136:139]\n\t" \
  "v_mfma_f32_16x16x32_bf16 v[140:143], v[44:47], v[188:191], v[140:143]\n\t" \
  "ds_read_b128 v[188:191], v209 offset:49152\n\t" \
  "s_waitcnt lgkmcnt(8)\n\t" \
  "v_mfma_f32_16x16x32_bf16 v[128:131], v[48:51], v[192:195], v[128:131]\n\t" \
  "v_mfma_f32_16x16x32_bf16 v[132:135], v[52:55], v[192:195], v[132:135]\n\t" \
  "ds_read_b128 v[192:195], v210 offset:32768\n\t" \
  "s_add_u32 m0, %[lw], 99328\n\t" \
  "v_mfma_f32_16x16x32_bf16 v[136:139], v[48:51], v[196:199], v[136:139]\n\t" \
  "v_mfma_f32_16x16x32_bf16 v[140:143], v[52:55], v[196:199], v[140:143]\n\t" \
  "ds_read_b128 v[196:199], v210 offset:49152\n\t" \
  "global_load_lds_dwordx4 v217, s[92:93] sc1\n\t" \
  "s_waitcnt lgkmcnt(4)\n\t" \
  "v_add_f32 v160, v160, v144\n\t" \
  "v_add_f32 v161, v161, v145\n\t" \
  "v_add_f32 v162, v162, v146\n\t" \
  "v_add_f32 v163, v163, v147\n\t" \
  "v_add_f32 v164, v164, v148\n\t" \
  "v_add_f32 v165, v165, v149\n\t" \
  "v_add_f32 v166, v166, v150\n\t" \
  "v_add_f32 v167, v167, v151\n\t" \
  "v_mfma_f32_16x16x32_bf16 v[128:131], v[56:59], v[200:203], v[128:131]\n\t" \
  "v_mfma_f32_16x16x32_bf16 v[132:135], v[60:63], v[200:203], v[132:135]\n\t" \
  "ds_read_b128 v[200:203], v211 offset:32768\n\t" \
  "v_mfma_f32_16x16x32_bf16 v[136:139], v[56:59], v[204:207], v[136:139]\n\t" \
  "v_mfma_f32_16x16x32_bf16 v[140:143], v[60:63], v[204:207], v[140:143]\n\t" \
  "ds_read_b128 v[204:207], v211 offset:49152\n\t" \
  "v_cvt_pk_bf16_f32 v168, v160, v161\n\t" \
  "v_cvt_pk_bf16_f32 v169, v162, v163\n\t" \
  "v_cvt_pk_bf16_f32 v170, v164, v165\n\t" \
  "v_cvt_pk_bf16_f32 v171, v166, v167\n\t" \
  "buffer_store_dwordx4 v[168:171], v172, %[rt], s90 offen sc1\n\t" \
  "v_mfma_f32_16x16x32_bf16 v[128:131], v[64:67], v[176:179], v[128:131]\n\t" \
  "v_mfma_f32_16x16x32_bf16 v[132:135], v[68:71], v[176:179], v[132:135]\n\t" \
  "ds_read_b128 v[176:179], v208 offset:33024\n\t" \
  "s_add_u32 m0, %[lw], 100352\n\t" \
  "v_mfma_f32_16x16x32_bf16 v[136:139], v[64:67], v[180:183], v[136:139]\n\t" \
  "v_mfma_f32_16x16x32_bf16 v[140:143], v[68:71], v[180:183], v[140:143]\n\t" \
  "ds_read_b128 v[180:183], v208 offset:49408\n\t" \
  "global_load_lds_dwordx4 v218, s[92:93] sc1\n\t" \
  "s_waitcnt lgkmcnt(6)\n\t" \
  "v_mfma_f32_16x16x32_bf16 v[128:131], v[72:75], v[184:187], v[128:131]\n\t" \
  "v_mfma_f32_16x16x32_bf16 v[132:135], v[76:79], v[184:187], v[132:135]\n\t" \
  "ds_read_b128 v[184:187], v209 offset:33024\n\t" \
  "v_mfma_f32_16x16x32_bf16 v[136:139], v[72:75], v[188:191], v[136:139]\n\t" \
  "v_mfma_f32_16x16x32_bf16 v[140:143], v[76:79], v[188:191], v[140:143]\n\t" \
  "ds_read_b128 v[188:191], v209 offset:49408\n\t" \
  "s_waitcnt lgkmcnt(6)\n\t" \
  "v_mfma_f32_16x16x32_bf16 v[128:131], v[80:83], v[192:195], v[128:131]\n\t" \
  "v_mfma_f32_16x16x32_bf16 v[132:135], v[84:87], v[192:195], v[132:135]\n\t" \
  "ds_read_b128 v[192:195], v210 offset:33024\n\t" \
  "s_add_u32 m0, %[lw], 101376\n\t" \
  "v_mfma_f32_16x16x32_bf16 v[136:139], v[80:83], v[196:199], v[136:139]\n\t" \
  "v_mfma_f32_16x16x32_bf16 v[140:143], v[84:87], v[196:199], v[140:143]\n\t" \
  "ds_read_b128 v[196:199], v210 offset:49408\n\t" \
  "global_load_lds_dwordx4 v219, s[92:93] sc1\n\t" \
  "s_waitcnt lgkmcnt(6)\n\t" \
  "v_mfma_f32_16x16x32_bf16 v[128:131], v[88:91], v[200:203], v[128:131]\n\t" \
  "v_mfma_f32_16x16x32_bf16 v[132:135], v[92:95], v[200:203], v[132:135]\n\t" \
  "ds_read_b128 v[200:203], v211 offset:33024\n\t" \
  "v_mfma_f32_16x16x32_bf16 v[136:139], v[88:91], v[204:207], v[136:139]\n\t" \
  "v_mfma_f32_16x16x32_bf16 v[140:143], v[92:95], v[204:207], v[140:143]\n\t" \
  "ds_read_b128 v[204:207], v211 offset:49408\n\t" \
  "s_cmp_lt_u32 s86, 8\n\t" \
  "s_cbranch_scc1 .Lw10_%=\n\t" \
  "s_waitcnt vmcnt(5)\n\t" \
  "s_barrier\n\t" \
  ".Lg10_%=:\n\t" \
  "s_add_u32 s92, s92, 65024\n\t" \
  "s_addc_u32 s93, s93, 0\n\t" \
  "s_waitcnt lgkmcnt(6)\n\t" \
  "v_mfma_f32_16x16x32_bf16 v[128:131], v[96:99], v[176:179], v[128:131]\n\t" \
  "v_mfma_f32_16x16x32_bf16 v[132:135], v[100:103], v[176:179], v[132:135]\n\t" \
  "ds_read_b128 v[176:179], v212 offset:0\n\t" \
  "s_add_u32 m0, %[lw], 0\n\t" \
  "v_mfma_f32_16x16x32_bf16 v[136:139], v[96:99], v[180:183], v[136:139]\n\t" \
  "v_mfma_f32_16x16x32_bf16 v[140:143], v[100:103], v[180:183], v[140:143]\n\t" \
  "ds_read_b128 v[180:183], v212 offset:16384\n\t" \
  "global_load_lds_dwordx4 v216, s[92:93] sc1\n\t" \
  "s_waitcnt lgkmcnt(6)\n\t" \
  "v_mfma_f32_16x16x32_bf16 v[128:131], v[104:107], v[184:187], v[128:131]\n\t" \
  "v_mfma_f32_16x16x32_bf16 v[132:135], v[108:111], v[184:187], v[132:135]\n\t" \
  "ds_read_b128 v[184:187], v213 offset:0\n\t" \
  "v_mfma_f32_16x16x32_bf16 v[136:139], v[104:107], v[188:191], v[136:139]\n\t" \
  "v_mfma_f32_16x16x32_bf16 v[140:143], v[108:111], v[188:191], v[140:143]\n\t" \
  "ds_read_b128 v[188:191], v213 offset:16384\n\t" \
  "s_waitcnt lgkmcnt(6)\n\t" \
  "v_mfma_f32_16x16x32_bf16 v[128:131], v[112:115], v[192:195], v[128:131]\n\t" \
  "v_mfma_f32_16x16x32_bf16 v[132:135], v[116:119], v[192:195], v[132:135]\n\t" \
  "ds_read_b128 v[192:195], v214 offset:0\n\t" \
  "s_add_u32 m0, %[lw], 1024\n\t" \
  "v_mfma_f32_16x16x32_bf16 v[136:139], v[112:115], v[196:199], v[136:139]\n\t" \
  "v_mfma_f32_16x16x32_bf16 v[140:143], v[116:119], v[196:199], v[140:143]\n\t" \
  "ds_read_b128 v[196:199], v214 offset:16384\n\t" \
  "global_load_lds_dwordx4 v217, s[92:93] sc1\n\t" \
  "s_waitcnt lgkmcnt(6)\n\t" \
  "v_mfma_f32_16x16x32_bf16 v[128:131], v[120:123], v[200:203], v[128:131]\n\t" \
  "v_mfma_f32_16x16x32_bf16 v[132:135], v[124:127], v[200:203], v[132:135]\n\t" \
  "ds_read_b128 v[200:203], v215 offset:0\n\t" \
  "v_mfma_f32_16x16x32_bf16 v[136:139], v[120:123], v[204:207], v[136:139]\n\t" \
  "v_mfma_f32_16x16x32_bf16 v[140:143], v[124:127], v[204:207], v[140:143]\n\t" \
  "ds_read_b128 v[204:207], v215 offset:16384\n\t" \
  "s_add_u32 s86, s86, 1\n\t" \
  "s_add_u32 s87, s87, 1\n\t" \
  "s_cmp_le_u32 s86, 5\n\t" \
  "s_cbranch_scc1 .Lf10_%=\n\t" \
  ".Lfb10_%=:\n\t" \
  "s_waitcnt lgkmcnt(6)\n\t" \
  "v_mfma_f32_16x16x32_bf16 v[144:147], v[0:3], v[176:179], 0\n\t" \
  "v_mfma_f32_16x16x32_bf16 v[148:151], v[4:7], v[176:179], 0\n\t" \
  "ds_read_b128 v[176:179], v212 offset:256\n\t" \
  "s_add_u32 m0, %[lw], 2048\n\t" \
  "v_mfma_f32_16x16x32_bf16 v[152:155], v[0:3], v[180:183], 0\n\t" \
  "v_mfma_f32_16x16x32_bf16 v[156:159], v[4:7], v[180:183], 0\n\t" \
  "ds_read_b128 v[180:183], v212 offset:16640\n\t" \
  "global_load_lds_dwordx4 v218, s[92:93] sc1\n\t" \
  "s_waitcnt lgkmcnt(6)\n\t" \
  "v_mfma_f32_16x16x32_bf16 v[144:147], v[8:11], v[184:187], v[144:147]\n\t" \
  "v_mfma_f32_16x16x32_bf16 v[148:151], v[12:15], v[184:187], v[148:151]\n\t" \
  "ds_read_b128 v[184:187], v213 offset:256\n\t" \
  "v_mfma_f32_16x16x32_bf16 v[152:155], v[8:11], v[188:191], v[152:155]\n\t" \
  "v_mfma_f32_16x16x32_bf16 v[156:159], v[12:15], v[188:191], v[156:159]\n\t" \
  "ds_read_b128 v[188:191], v213 offset:16640\n\t" \
  "ds_write_b128 v220, v[136:139] offset:0\n\t" \
  "ds_write_b128 v220, v[140:143] offset:1024\n\t" \
  "s_waitcnt lgkmcnt(8)\n\t" \
  "v_mfma_f32_16x16x32_bf16 v[144:147], v[16:19], v[192:195], v[144:147]\n\t" \
  "v_mfma_f32_16x16x32_bf16 v[148:151], v[20:23], v[192:195], v[148:151]\n\t" \
  "ds_read_b128 v[192:195], v214 offset:256\n\t" \
  "s_add_u32 m0, %[lw], 3072\n\t" \
  "v_mfma_f32_16x16x32_bf16 v[152:155], v[16:19], v[196:199], v[152:155]\n\t" \
  "v_mfma_f32_16x16x32_bf16 v[156:159], v[20:23], v[196:199], v[156:159]\n\t" \
  "ds_read_b128 v[196:199], v214 offset:16640\n\t" \
  "global_load_lds_dwordx4 v219, s[92:93] sc1\n\t" \
  "s_lshr_b32 s90, s87, 7\n\t" \
  "s_xor_b32 s90, s90, %[s0]\n\t" \
  "s_and_b32 s90, s90, 1\n\t" \
  "s_lshl1_add_u32 s90, s87, s90\n\t" \
  "s_lshl_b32 s90, s90, 13\n\t" \
  "s_cmp_lt_i32 s86, 6\n\t" \
  "s_cselect_b32 s90, 0x70000000, s90\n\t" \
  "s_waitcnt lgkmcnt(8)\n\t" \
  "v_mfma_f32_16x16x32_bf16 v[144:147], v[24:27], v[200:203], v[144:147]\n\t" \
  "v_mfma_f32_16x16x32_bf16 v[148:151], v[28:31], v[200:203], v[148:151]\n\t" \
  "ds_read_b128 v[200:203], v215 offset:256\n\t" \
  "v_mfma_f32_16x16x32_bf16 v[152:155], v[24:27], v[204:207], v[152:155]\n\t" \
  "v_mfma_f32_16x16x32_bf16 v[156:159], v[28:31], v[204:207], v[156:159]\n\t" \
  "ds_read_b128 v[204:207], v215 offset:16640\n\t" \
  "s_waitcnt lgkmcnt(4)\n\t" \
  "s_cmp_lt_u32 s86, 6\n\t" \
  "s_cbranch_scc1 .Lw20_%=\n\t" \
  "s_waitcnt vmcnt(4)\n\t" \
  "s_barrier\n\t" \
  ".Lg20_%=:\n\t" \
  "s_add_u32 s92, s92, 512\n\t" \
  "s_addc_u32 s93, s93, 0\n\t" \
  "v_mfma_f32_16x16x32_bf16 v[144:147], v[32:35], v[176:179], v[144:147]\n\t" \
  "v_mfma_f32_16x16x32_bf16 v[148:151], v[36:39], v[176:179], v[148:151]\n\t" \
  "ds_read_b128 v[176:179], v212 offset:32768\n\t" \
  "s_add_u32 m0, %[lw], 32768\n\t" \
  "v_mfma_f32_16x16x32_bf16 v[152:155], v[32:35], v[180:183], v[152:155]\n\t" \
  "v_mfma_f32_16x16x32_bf16 v[156:159], v[36:39], v[180:183], v[156:159]\n\t" \
  "ds_read_b128 v[180:183], v212 offset:49152\n\t" \
  "global_load_lds_dwordx4 v216, s[92:93] sc1\n\t" \
  "ds_read_b128 v[160:163], v221 offset:0\n\t" \
  "ds_read_b128 v[164:167], v221 offset:1024\n\t" \
  "v_mfma_f32_16x16x32_bf16 v[144:147], v[40:43], v[184:187], v[144:147]\n\t" \
  "v_mfma_f32_16x16x32_bf16 v[148:151], v[44:47], v[184:187], v[148:151]\n\t" \
  "ds_read_b128 v[184:187], v213 offset:32768\n\t" \
  "v_mfma_f32_16x16x32_bf16 v[152:155], v[40:43], v[188:191], v[152:155]\n\t" \
  "v_mfma_f32_16x16x32_bf16 v[156:159], v[44:47], v[188:191], v[156:159]\n\t" \
  "ds_read_b128 v[188:191], v213 offset:49152\n\t" \
  "s_waitcnt lgkmcnt(8)\n\t" \
  "v_mfma_f32_16x16x32_bf16 v[144:147], v[48:51], v[192:195], v[144:147]\n\t" \
  "v_mfma_f32_16x16x32_bf16 v[148:151], v[52:55], v[192:195], v[148:151]\n\t" \
  "ds_read_b128 v[192:195], v214 offset:32768\n\t" \
  "s_add_u32 m0, %[lw], 33792\n\t" \
  "v_mfma_f32_16x16x32_bf16 v[152:155], v[48:51], v[196:199], v[152:155]\n\t" \
  "v_mfma_f32_16x16x32_bf16 v[156:159], v[52:55], v[196:199], v[156:159]\n\t" \
  "ds_read_b128 v[196:199], v214 offset:49152\n\t" \
  "global_load_lds_dwordx4 v217, s[92:93] sc1\n\t" \
  "s_waitcnt lgkmcnt(4)\n\t" \
  "v_add_f32 v160, v160, v128\n\t" \
  "v_add_f32 v161, v161, v129\n\t" \
  "v_add_f32 v162, v162, v130\n\t" \
  "v_add_f32 v163, v163, v131\n\t" \
  "v_add_f32 v164, v164, v132\n\t" \
  "v_add_f32 v165, v165, v133\n\t" \
  "v_add_f32 v166, v166, v134\n\t" \
  "v_add_f32 v167, v167, v135\n\t" \
  "v_mfma_f32_16x16x32_bf16 v[144:147], v[56:59], v[200:203], v[144:147]\n\t" \
  "v_mfma_f32_16x16x32_bf16 v[148:151], v[60:63], v[200:203], v[148:151]\n\t" \
  "ds_read_b128 v[200:203], v215 offset:32768\n\t" \
  "v_mfma_f32_16x16x32_bf16 v[152:155], v[56:59], v[204:207], v[152:155]\n\t" \
  "v_mfma_f32_16x16x32_bf16 v[156:159], v[60:63], v[204:207], v[156:159]\n\t" \
  "ds_read_b128 v[204:207], v215 offset:49152\n\t" \
  "v_cvt_pk_bf16_f32 v168, v160, v161\n\t" \
  "v_cvt_pk_bf16_f32 v169, v162, v163\n\t" \
  "v_cvt_pk_bf16_f32 v170, v164, v165\n\t" \
  "v_cvt_pk_bf16_f32 v171, v166, v167\n\t" \
  "buffer_store_dwordx4 v[168:171], v172, %[rt], s90 offen sc1\n\t" \
  "v_mfma_f32_16x16x32_bf16 v[144:147], v[64:67], v[176:179], v[144:147]\n\t" \
  "v_mfma_f32_16x16x32_bf16 v[148:151], v[68:71], v[176:179], v[148:151]\n\t" \
  "ds_read_b128 v[176:179], v212 offset:33024\n\t" \
  "s_add_u32 m0, %[lw], 34816\n\t" \
  "v_mfma_f32_16x16x32_bf16 v[152:155], v[64:67], v[180:183], v[152:155]\n\t" \
  "v_mfma_f32_16x16x32_bf16 v[156:159], v[68:71], v[180:183], v[156:159]\n\t" \
  "ds_read_b128 v[180:183], v212 offset:49408\n\t" \
  "global_load_lds_dwordx4 v218, s[92:93] sc1\n\t" \
  "s_waitcnt lgkmcnt(6)\n\t" \
  "v_mfma_f32_16x16x32_bf16 v[144:147], v[72:75], v[184:187], v[144:147]\n\t" \
  "v_mfma_f32_16x16x32_bf16 v[148:151], v[76:79], v[184:187], v[148:151]\n\t" \
  "ds_read_b128 v[184:187], v213 offset:33024\n\t" \
  "v_mfma_f32_16x16x32_bf16 v[152:155], v[72:75], v[188:191], v[152:155]\n\t" \
  "v_mfma_f32_16x16x32_bf16 v[156:159], v[76:79], v[188:191], v[156:159]\n\t" \
  "ds_read_b128 v[188:191], v213 offset:49408\n\t" \
  "s_waitcnt lgkmcnt(6)\n\t" \
  "v_mfma_f32_16x16x32_bf16 v[144:147], v[80:83], v[192:195], v[144:147]\n\t" \
  "v_mfma_f32_16x16x32_bf16 v[148:151], v[84:87], v[192:195], v[148:151]\n\t" \
  "ds_read_b128 v[192:195], v214 offset:33024\n\t" \
  "s_add_u32 m0, %[lw], 35840\n\t" \
  "v_mfma_f32_16x16x32_bf16 v[152:155], v[80:83], v[196:199], v[152:155]\n\t" \
  "v_mfma_f32_16x16x32_bf16 v[156:159], v[84:87], v[196:199], v[156:159]\n\t" \
  "ds_read_b128 v[196:199], v214 offset:49408\n\t" \
  "global_load_lds_dwordx4 v219, s[92:93] sc1\n\t" \
  "s_waitcnt lgkmcnt(6)\n\t" \
  "v_mfma_f32_16x16x32_bf16 v[144:147], v[88:91], v[200:203], v[144:147]\n\t" \
  "v_mfma_f32_16x16x32_bf16 v[148:151], v[92:95], v[200:203], v[148:151]\n\t" \
  "ds_read_b128 v[200:203], v215 offset:33024\n\t" \
  "v_mfma_f32_16x16x32_bf16 v[152:155], v[88:91], v[204:207], v[152:155]\n\t" \
  "v_mfma_f32_16x16x32_bf16 v[156:159], v[92:95], v[204:207], v[156:159]\n\t" \
  "ds_read_b128 v[204:207], v215 offset:49408\n\t" \
  "s_cmp_lt_u32 s86, 8\n\t" \
  "s_cbranch_scc1 .Lw30_%=\n\t" \
  "s_waitcnt vmcnt(5)\n\t" \
  "s_barrier\n\t" \
  ".Lg30_%=:\n\t" \
  "s_add_u32 s92, s92, 65024\n\t" \
  "s_addc_u32 s93, s93, 0\n\t" \
  "s_waitcnt lgkmcnt(6)\n\t" \
  "v_mfma_f32_16x16x32_bf16 v[144:147], v[96:99], v[176:179], v[144:147]\n\t" \
  "v_mfma_f32_16x16x32_bf16 v[148:151], v[100:103], v[176:179], v[148:151]\n\t" \
  "ds_read_b128 v[176:179], v208 offset:0\n\t" \
  "s_add_u32 m0, %[lw], 65536\n\t" \
  "v_mfma_f32_16x16x32_bf16 v[152:155], v[96:99], v[180:183], v[152:155]\n\t" \
  "v_mfma_f32_16x16x32_bf16 v[156:159], v[100:103], v[180:183], v[156:159]\n\t" \
  "ds_read_b128 v[180:183], v208 offset:16384\n\t" \
  "global_load_lds_dwordx4 v216, s[92:93] sc1\n\t" \
  "s_waitcnt lgkmcnt(6)\n\t" \
  "v_mfma_f32_16x16x32_bf16 v[144:147], v[104:107], v[184:187], v[144:147]\n\t" \
  "v_mfma_f32_16x16x32_bf16 v[148:151], v[108:111], v[184:187], v[148:151]\n\t" \
  "ds_read_b128 v[184:187], v209 offset:0\n\t" \
  "v_mfma_f32_16x16x32_bf16 v[152:155], v[104:107], v[188:191], v[152:155]\n\t" \
  "v_mfma_f32_16x16x32_bf16 v[156:159], v[108:111], v[188:191], v[156:159]\n\t" \
  "ds_read_b128 v[188:191], v209 offset:16384\n\t" \
  "s_waitcnt lgkmcnt(6)\n\t" \
  "v_mfma_f32_16x16x32_bf16 v[144:147], v[112:115], v[192:195], v[144:147]\n\t" \
  "v_mfma_f32_16x16x32_bf16 v[148:151], v[116:119], v[192:195], v[148:151]\n\t" \
  "ds_read_b128 v[192:195], v210 offset:0\n\t" \
  "s_add_u32 m0, %[lw], 66560\n\t" \
  "v_mfma_f32_16x16x32_bf16 v[152:155], v[112:115], v[196:199], v[152:155]\n\t" \
  "v_mfma_f32_16x16x32_bf16 v[156:159], v[116:119], v[196:199], v[156:159]\n\t" \
  "ds_read_b128 v[196:199], v210 offset:16384\n\t" \
  "global_load_lds_dwordx4 v217, s[92:93] sc1\n\t" \
  "s_waitcnt lgkmcnt(6)\n\t" \
  "v_mfma_f32_16x16x32_bf16 v[144:147], v[120:123], v[200:203], v[144:147]\n\t" \
  "v_mfma_f32_16x16x32_bf16 v[148:151], v[124:127], v[200:203], v[148:151]\n\t" \
  "ds_read_b128 v[200:203], v211 offset:0\n\t" \
  "v_mfma_f32_16x16x32_bf16 v[152:155], v[120:123], v[204:207], v[152:155]\n\t" \
  "v_mfma_f32_16x16x32_bf16 v[156:159], v[124:127], v[204:207], v[156:159]\n\t" \
  "ds_read_b128 v[204:207], v211 offset:16384\n\t" \
  "s_add_u32 s86, s86, 1\n\t" \
  "s_add_u32 s87, s87, 1\n\t" \
  "s_cmp_lt_u32 s86, %[end]\n\t" \
  "s_cbranch_scc1 .Lp0_%=\n\t" \
  "s_branch .Lx0_%=\n\t" \
  ".Lf00_%=:\n\t" \
  "s_nop 7\n\t" \
  "ds_write_b128 v220, v[152:155] offset:0\n\t" \
  "ds_write_b128 v220, v[156:159] offset:1024\n\t" \
  "s_lshr_b32 s90, s87, 7\n\t" \
  "s_xor_b32 s90, s90, %[s0]\n\t" \
  "s_and_b32 s90, s90, 1\n\t" \
  "s_lshl1_add_u32 s90, s87, s90\n\t" \
  "s_lshl_b32 s90, s90, 13\n\t" \
  "s_cmp_lt_i32 s86, 1\n\t" \
  "s_cselect_b32 s90, 0x70000000, s90\n\t" \
  "s_waitcnt lgkmcnt(0)\n\t" \
  "s_barrier\n\t" \
  "ds_read_b128 v[160:163], v221 offset:0\n\t" \
  "ds_read_b128 v[164:167], v221 offset:1024\n\t" \
  "s_waitcnt lgkmcnt(0)\n\t" \
  "v_add_f32 v160, v160, v144\n\t" \
  "v_add_f32 v161, v161, v145\n\t" \
  "v_add_f32 v162, v162, v146\n\t" \
  "v_add_f32 v163, v163, v147\n\t" \
  "v_add_f32 v164, v164, v148\n\t" \
  "v_add_f32 v165, v165, v149\n\t" \
  "v_add_f32 v166, v166, v150\n\t" \
  "v_add_f32 v167, v167, v151\n\t" \
  "v_cvt_pk_bf16_f32 v168, v160, v161\n\t" \
  "v_cvt_pk_bf16_f32 v169, v162, v163\n\t" \
  "v_cvt_pk_bf16_f32 v170, v164, v165\n\t" \
  "v_cvt_pk_bf16_f32 v171, v166, v167\n\t" \
  "buffer_store_dwordx4 v[168:171], v172, %[rt], s90 offen sc1\n\t" \
  "s_branch .Lfb00_%=\n\t" \
  ".Lf10_%=:\n\t" \
  "s_nop 7\n\t" \
  "ds_write_b128 v220, v[136:139] offset:0\n\t" \
  "ds_write_b128 v220, v[140:143] offset:1024\n\t" \
  "s_lshr_b32 s90, s87, 7\n\t" \
  "s_xor_b32 s90, s90, %[s0]\n\t" \
  "s_and_b32 s90, s90, 1\n\t" \
  "s_lshl1_add_u32 s90, s87, s90\n\t" \
  "s_lshl_b32 s90, s90, 13\n\t" \
  "s_cmp_lt_i32 s86, 1\n\t" \
  "s_cselect_b32 s90, 0x70000000, s90\n\t" \
  "s_waitcnt lgkmcnt(0)\n\t" \
  "s_barrier\n\t" \
  "ds_read_b128 v[160:163], v221 offset:0\n\t" \
  "ds_read_b128 v[164:167], v221 offset:1024\n\t" \
  "s_waitcnt lgkmcnt(0)\n\t" \
  "v_add_f32 v160, v160, v128\n\t" \
  "v_add_f32 v161, v161, v129\n\t" \
  "v_add_f32 v162, v162, v130\n\t" \
  "v_add_f32 v163, v163, v131\n\t" \
  "v_add_f32 v164, v164, v132\n\t" \
  "v_add_f32 v165, v165, v133\n\t" \
  "v_add_f32 v166, v166, v134\n\t" \
  "v_add_f32 v167, v167, v135\n\t" \
  "v_cvt_pk_bf16_f32 v168, v160, v161\n\t" \
  "v_cvt_pk_bf16_f32 v169, v162, v163\n\t" \
  "v_cvt_pk_bf16_f32 v170, v164, v165\n\t" \
  "v_cvt_pk_bf16_f32 v171, v166, v167\n\t" \
  "buffer_store_dwordx4 v[168:171], v172, %[rt], s90 offen sc1\n\t" \
  "s_branch .Lfb10_%=\n\t" \
  ".Lw00_%=:\n\t" \
  "s_waitcnt vmcnt(2)\n\t" \
  "s_barrier\n\t" \
  "s_branch .Lg00_%=\n\t" \
  ".Lw10_%=:\n\t" \
  "s_waitcnt vmcnt(2)\n\t" \
  "s_barrier\n\t" \
  "s_branch .Lg10_%=\n\t" \
  ".Lw20_%=:\n\t" \
  "s_waitcnt vmcnt(2)\n\t" \
  "s_barrier\n\t" \
  "s_branch .Lg20_%=\n\t" \
  ".Lw30_%=:\n\t" \
  "s_waitcnt vmcnt(2)\n\t" \
  "s_barrier\n\t" \
  "s_branch .Lg30_%=\n\t" \
  ".Lx0_%=:\n\t" \
  "s_waitcnt lgkmcnt(0)\n\t" \
  "s_setprio 0\n\t" \
  "s_mov_b32 m0, s94\n\t" \
  "s_branch .Le_%=\n\t" \
  ".Lk_%=:\n\t" \
  "s_mov_b32 s94, m0\n\t" \
  "s_mov_b64 s[92:93], %[x0]\n\t" \
  "s_mov_b32 s86, %[g0]\n\t" \
  "s_add_u32 s87, s86, %[t0]\n\t" \
  "s_sub_i32 s88, s86, 3\n\t" \
  "ds_read_b128 v[176:179], v208 offset:0\n\t" \
  "ds_read_b128 v[180:183], v208 offset:16384\n\t" \
  "ds_read_b128 v[184:187], v209 offset:0\n\t" \
  "ds_read_b128 v[188:191], v209 offset:16384\n\t" \
  "ds_read_b128 v[192:195], v210 offset:0\n\t" \
  "ds_read_b128 v[196:199], v210 offset:16384\n\t" \
  "ds_read_b128 v[200:203], v211 offset:0\n\t" \
  "ds_read_b128 v[204:207], v211 offset:16384\n\t" \
  "s_branch .Lfb01_%=\n\t" \
  ".Lp1_%=:\n\t" \
  "s_cmp_le_u32 s86, 5\n\t" \
  "s_cbranch_scc1 .Lf01_%=\n\t" \
  ".Lfb01_%=:\n\t" \
  "s_waitcnt lgkmcnt(6)\n\t" \
  "v_mfma_f32_16x16x32_bf16 v[128:131], v[0:3], v[176:179], 0\n\t" \
  "v_mfma_f32_16x16x32_bf16 v[132:135], v[4:7], v[176:179], 0\n\t" \
  "ds_read_b128 v[176:179], v208 offset:256\n\t" \
  "v_mfma_f32_16x16x32_bf16 v[136:139], v[0:3], v[180:183], 0\n\t" \
  "v_mfma_f32_16x16x32_bf16 v[140:143], v[4:7], v[180:183], 0\n\t" \
  "ds_read_b128 v[180:183], v208 offset:16640\n\t" \
  "s_waitcnt lgkmcnt(6)\n\t" \
  "v_mfma_f32_16x16x32_bf16 v[128:131], v[8:11], v[184:187], v[128:131]\n\t" \
  "v_mfma_f32_16x16x32_bf16 v[132:135], v[12:15], v[184:187], v[132:135]\n\t" \
  "ds_read_b128 v[184:187], v209 offset:256\n\t" \
  "s_add_u32 m0, %[lw], 67584\n\t" \
  "v_mfma_f32_16x16x32_bf16 v[136:139], v[8:11], v[188:191], v[136:139]\n\t" \
  "v_mfma_f32_16x16x32_bf16 v[140:143], v[12:15], v[188:191], v[140:143]\n\t" \
  "ds_read_b128 v[188:191], v209 offset:16640\n\t" \
  "global_load_lds_dwordx4 v218, s[92:93] sc1\n\t" \
  "ds_write_b128 v220, v[152:155] offset:0\n\t" \
  "ds_write_b128 v220, v[156:159] offset:1024\n\t" \
  "s_waitcnt lgkmcnt(8)\n\t" \
  "v_mfma_f32_16x16x32_bf16 v[128:131], v[16:19], v[192:195], v[128:131]\n\t" \
  "v_mfma_f32_16x16x32_bf16 v[132:135], v[20:23], v[192:195], v[132:135]\n\t" \
  "ds_read_b128 v[192:195], v210 offset:256\n\t" \
  "v_mfma_f32_16x16x32_bf16 v[136:139], v[16:19], v[196:199], v[136:139]\n\t" \
  "v_mfma_f32_16x16x32_bf16 v[140:143], v[20:23], v[196:199], v[140:143]\n\t" \
  "ds_read_b128 v[196:199], v210 offset:16640\n\t" \
  "s_lshr_b32 s90, s87, 7\n\t" \
  "s_xor_b32 s90, s90, %[s0]\n\t" \
  "s_and_b32 s90, s90, 1\n\t" \
  "s_lshl1_add_u32 s90, s87, s90\n\t" \
  "s_lshl_b32 s90, s90, 13\n\t" \
  "s_cmp_lt_i32 s86, 6\n\t" \
  "s_cselect_b32 s90, 0x70000000, s90\n\t" \
  "s_waitcnt lgkmcnt(8)\n\t" \
  "v_mfma_f32_16x16x32_bf16 v[128:131], v[24:27], v[200:203], v[128:131]\n\t" \
  "v_mfma_f32_16x16x32_bf16 v[132:135], v[28:31], v[200:203], v[132:135]\n\t" \
  "ds_read_b128 v[200:203], v211 offset:256\n\t" \
  "s_add_u32 m0, %[lw], 68608\n\t" \
  "v_mfma_f32_16x16x32_bf16 v[136:139], v[24:27], v[204:207], v[136:139]\n\t" \
  "v_mfma_f32_16x16x32_bf16 v[140:143], v[28:31], v[204:207], v[140:143]\n\t" \
  "ds_read_b128 v[204:207], v211 offset:16640\n\t" \
  "global_load_lds_dwordx4 v219, s[92:93] sc1\n\t" \
  "s_waitcnt lgkmcnt(4)\n\t" \
  "s_cmp_lt_u32 s86, 6\n\t" \
  "s_cbranch_scc1 .Lw01_%=\n\t" \
  "s_waitcnt vmcnt(4)\n\t" \
  "s_barrier\n\t" \
  ".Lg01_%=:\n\t" \
  "s_add_u32 s92, s92, 512\n\t" \
  "s_addc_u32 s93, s93, 0\n\t" \
  "v_mfma_f32_16x16x32_bf16 v[128:131], v[32:35], v[176:179], v[128:131]\n\t" \
  "v_mfma_f32_16x16x32_bf16 v[132:135], v[36:39], v[176:179], v[132:135]\n\t" \
  "ds_read_b128 v[176:179], v208 offset:32768\n\t" \
  "v_mfma_f32_16x16x32_bf16 v[136:139], v[32:35], v[180:183], v[136:139]\n\t" \
  "v_mfma_f32_16x16x32_bf16 v[140:143], v[36:39], v[180:183], v[140:143]\n\t" \
  "ds_read_b128 v[180:183], v208 offset:49152\n\t" \
  "ds_read_b128 v[160:163], v221 offset:0\n\t" \
  "ds_read_b128 v[164:167], v221 offset:1024\n\t" \
  "s_sub_i32 s91, s86, 2\n\t" \
  "s_max_i32 s88, s88, s91\n\t" \
  "v_mov_b32 v173, s88\n\t" \
  "buffer_store_dword v173, v174, %[rs], 0 offen sc1\n\t" \
  "v_mfma_f32_16x16x32_bf16 v[128:131], v[40:43], v[184:187], v[128:131]\n\t" \
  "v_mfma_f32_16x16x32_bf16 v[132:135], v[44:47], v[184:187], v[132:135]\n\t" \
  "ds_read_b128 v[184:187], v209 offset:32768\n\t" \
  "s_add_u32 m0, %[lw], 98304\n\t" \
  "v_mfma_f32_16x16x32_bf16 v[136:139], v[40:43], v[188:191], v[136:139]\n\t" \
  "v_mfma_f32_16x16x32_bf16 v[140:143], v[44:47], v[188:191], v[140:143]\n\t" \
  "ds_read_b128 v[188:191], v209 offset:49152\n\t" \
  "global_load_lds_dwordx4 v216, s[92:93] sc1\n\t" \
  "s_waitcnt lgkmcnt(8)\n\t" \
  "v_mfma_f32_16x16x32_bf16 v[128:131], v[48:51], v[192:195], v[128:131]\n\t" \
  "v_mfma_f32_16x16x32_bf16 v[132:135], v[52:55], v[192:195], v[132:135]\n\t" \
  "ds_read_b128 v[192:195], v210 offset:32768\n\t" \
  "v_mfma_f32_16x16x32_bf16 v[136:139], v[48:51], v[196:199], v[136:139]\n\t" \
  "v_mfma_f32_16x16x32_bf16 v[140:143], v[52:55], v[196:199], v[140:143]\n\t" \
  "ds_read_b128 v[196:199], v210 offset:49152\n\t" \
  "s_waitcnt lgkmcnt(4)\n\t" \
  "v_add_f32 v160, v160, v144\n\t" \
  "v_add_f32 v161, v161, v145\n\t" \
  "v_add_f32 v162, v162, v146\n\t" \
  "v_add_f32 v163, v163, v147\n\t" \
  "v_add_f32 v164, v164, v148\n\t" \
  "v_add_f32 v165, v165, v149\n\t" \
  "v_add_f32 v166, v166, v150\n\t" \
  "v_add_f32 v167, v167, v151\n\t" \
  "v_mfma_f32_16x16x32_bf16 v[128:131], v[56:59], v[200:203], v[128:131]\n\t" \
  "v_mfma_f32_16x16x32_bf16 v[132:135], v[60:63], v[200:203], v[132:135]\n\t" \
  "ds_read_b128 v[200:203], v211 offset:32768\n\t" \
  "s_add_u32 m0, %[lw], 99328\n\t" \
  "v_mfma_f32_16x16x32_bf16 v[136:139], v[56:59], v[204:207], v[136:139]\n\t" \
  "v_mfma_f32_16x16x32_bf16 v[140:143], v[60:63], v[204:207], v[140:143]\n\t" \
  "ds_read_b128 v[204:207], v211 offset:49152\n\t" \
  "global_load_lds_dwordx4 v217, s[92:93] sc1\n\t" \
  "v_cvt_pk_bf16_f32 v168, v160, v161\n\t" \
  "v_cvt_pk_bf16_f32 v169, v162, v163\n\t" \
  "v_cvt_pk_bf16_f32 v170, v164, v165\n\t" \
  "v_cvt_pk_bf16_f32 v171, v166, v167\n\t" \
  "buffer_store_dwordx4 v[168:171], v172, %[rt], s90 offen sc1\n\t" \
  "v_mfma_f32_16x16x32_bf16 v[128:131], v[64:67], v[176:179], v[128:131]\n\t" \
  "v_mfma_f32_16x16x32_bf16 v[132:135], v[68:71], v[176:179], v[132:135]\n\t" \
  "ds_read_b128 v[176:179], v208 offset:33024\n\t" \
  "v_mfma_f32_16x16x32_bf16 v[136:139], v[64:67], v[180:183], v[136:139]\n\t" \
  "v_mfma_f32_16x16x32_bf16 v[140:143], v[68:71], v[180:183], v[140:143]\n\t" \
  "ds_read_b128 v[180:183], v208 offset:49408\n\t" \
  "s_waitcnt lgkmcnt(6)\n\t" \
  "v_mfma_f32_16x16x32_bf16 v[128:131], v[72:75], v[184:187], v[128:131]\n\t" \
  "v_mfma_f32_16x16x32_bf16 v[132:135], v[76:79], v[184:187], v[132:135]\n\t" \
  "ds_read_b128 v[184:187], v209 offset:33024\n\t" \
  "s_add_u32 m0, %[lw], 100352\n\t" \
  "v_mfma_f32_16x16x32_bf16 v[136:139], v[72:75], v[188:191], v[136:139]\n\t" \
  "v_mfma_f32_16x16x32_bf16 v[140:143], v[76:79], v[188:191], v[140:143]\n\t" \
  "ds_read_b128 v[188:191], v209 offset:49408\n\t" \
  "global_load_lds_dwordx4 v218, s[92:93] sc1\n\t" \
  "s_waitcnt lgkmcnt(6)\n\t" \
  "v_mfma_f32_16x16x32_bf16 v[128:131], v[80:83], v[192:195], v[128:131]\n\t" \
  "v_mfma_f32_16x16x32_bf16 v[132:135], v[84:87], v[192:195], v[132:135]\n\t" \
  "ds_read_b128 v[192:195], v210 offset:33024\n\t" \
  "v_mfma_f32_16x16x32_bf16 v[136:139], v[80:83], v[196:199], v[136:139]\n\t" \
  "v_mfma_f32_16x16x32_bf16 v[140:143], v[84:87], v[196:199], v[140:143]\n\t" \
  "ds_read_b128 v[196:199], v210 offset:49408\n\t" \
  "s_waitcnt lgkmcnt(6)\n\t" \
  "v_mfma_f32_16x16x32_bf16 v[128:131], v[88:91], v[200:203], v[128:131]\n\t" \
  "v_mfma_f32_16x16x32_bf16 v[132:135], v[92:95], v[200:203], v[132:135]\n\t" \
  "ds_read_b128 v[200:203], v211 offset:33024\n\t" \
  "s_add_u32 m0, %[lw], 101376\n\t" \
  "v_mfma_f32_16x16x32_bf16 v[136:139], v[88:91], v[204:207], v[136:139]\n\t" \
  "v_mfma_f32_16x16x32_bf16 v[140:143], v[92:95], v[204:207], v[140:143]\n\t" \
  "ds_read_b128 v[204:207], v211 offset:49408\n\t" \
  "global_load_lds_dwordx4 v219, s[92:93] sc1\n\t" \
  "s_cmp_lt_u32 s86, 8\n\t" \
  "s_cbranch_scc1 .Lw11_%=\n\t" \
  "s_waitcnt vmcnt(6)\n\t" \
  "s_barrier\n\t" \
  ".Lg11_%=:\n\t" \
  "s_add_u32 s92, s92, 65024\n\t" \
  "s_addc_u32 s93, s93, 0\n\t" \
  "s_waitcnt lgkmcnt(6)\n\t" \
  "v_mfma_f32_16x16x32_bf16 v[128:131], v[96:99], v[176:179], v[128:131]\n\t" \
  "v_mfma_f32_16x16x32_bf16 v[132:135], v[100:103], v[176:179], v[132:135]\n\t" \
  "ds_read_b128 v[176:179], v212 offset:0\n\t" \
  "v_mfma_f32_16x16x32_bf16 v[136:139], v[96:99], v[180:183], v[136:139]\n\t" \
  "v_mfma_f32_16x16x32_bf16 v[140:143], v[100:103], v[180:183], v[140:143]\n\t" \
  "ds_read_b128 v[180:183], v212 offset:16384\n\t" \
  "s_waitcnt lgkmcnt(6)\n\t" \
  "v_mfma_f32_16x16x32_bf16 v[128:131], v[104:107], v[184:187], v[128:131]\n\t" \
  "v_mfma_f32_16x16x32_bf16 v[132:135], v[108:111], v[184:187], v[132:135]\n\t" \
  "ds_read_b128 v[184:187], v213 offset:0\n\t" \
  "s_add_u32 m0, %[lw], 0\n\t" \
  "v_mfma_f32_16x16x32_bf16 v[136:139], v[104:107], v[188:191], v[136:139]\n\t" \
  "v_mfma_f32_16x16x32_bf16 v[140:143], v[108:111], v[188:191], v[140:143]\n\t" \
  "ds_read_b128 v[188:191], v213 offset:16384\n\t" \
  "global_load_lds_dwordx4 v216, s[92:93] sc1\n\t" \
  "s_waitcnt lgkmcnt(6)\n\t" \
  "v_mfma_f32_16x16x32_bf16 v[128:131], v[112:115], v[192:195], v[128:131]\n\t" \
  "v_mfma_f32_16x16x32_bf16 v[132:135], v[116:119], v[192:195], v[132:135]\n\t" \
  "ds_read_b128 v[192:195], v214 offset:0\n\t" \
  "v_mfma_f32_16x16x32_bf16 v[136:139], v[112:115], v[196:199], v[136:139]\n\t" \
  "v_mfma_f32_16x16x32_bf16 v[140:143], v[116:119], v[196:199], v[140:143]\n\t" \
  "ds_read_b128 v[196:199], v214 offset:16384\n\t" \
  "s_waitcnt lgkmcnt(6)\n\t" \
  "v_mfma_f32_16x16x32_bf16 v[128:131], v[120:123], v[200:203], v[128:131]\n\t" \
  "v_mfma_f32_16x16x32_bf16 v[132:135], v[124:127], v[200:203], v[132:135]\n\t" \
  "ds_read_b128 v[200:203], v215 offset:0\n\t" \
  "s_add_u32 m0, %[lw], 1024\n\t" \
  "v_mfma_f32_16x16x32_bf16 v[136:139], v[120:123], v[204:207], v[136:139]\n\t" \
  "v_mfma_f32_16x16x32_bf16 v[140:143], v[124:127], v[204:207], v[140:143]\n\t" \
  "ds_read_b128 v[204:207], v215 offset:16384\n\t" \
  "global_load_lds_dwordx4 v217, s[92:93] sc1\n\t" \
  "s_add_u32 s86, s86, 1\n\t" \
  "s_add_u32 s87, s87, 1\n\t" \
  "s_cmp_le_u32 s86, 5\n\t" \
  "s_cbranch_scc1 .Lf11_%=\n\t" \
  ".Lfb11_%=:\n\t" \
  "s_waitcnt lgkmcnt(6)\n\t" \
  "v_mfma_f32_16x16x32_bf16 v[144:147], v[0:3], v[176:179], 0\n\t" \
  "v_mfma_f32_16x16x32_bf16 v[148:151], v[4:7], v[176:179], 0\n\t" \
  "ds_read_b128 v[176:179], v212 offset:256\n\t" \
  "v_mfma_f32_16x16x32_bf16 v[152:155], v[0:3], v[180:183], 0\n\t" \
  "v_mfma_f32_16x16x32_bf16 v[156:159], v[4:7], v[180:183], 0\n\t" \
  "ds_read_b128 v[180:183], v212 offset:16640\n\t" \
  "s_waitcnt lgkmcnt(6)\n\t" \
  "v_mfma_f32_16x16x32_bf16 v[144:147], v[8:11], v[184:187], v[144:147]\n\t" \
  "v_mfma_f32_16x16x32_bf16 v[148:151], v[12:15], v[184:187], v[148:151]\n\t" \
  "ds_read_b128 v[184:187], v213 offset:256\n\t" \
  "s_add_u32 m0, %[lw], 2048\n\t" \
  "v_mfma_f32_16x16x32_bf16 v[152:155], v[8:11], v[188:191], v[152:155]\n\t" \
  "v_mfma_f32_16x16x32_bf16 v[156:159], v[12:15], v[188:191], v[156:159]\n\t" \
  "ds_read_b128 v[188:191], v213 offset:16640\n\t" \
  "global_load_lds_dwordx4 v218, s[92:93] sc1\n\t" \
  "ds_write_b128 v220, v[136:139] offset:0\n\t" \
  "ds_write_b128 v220, v[140:143] offset:1024\n\t" \
  "s_waitcnt lgkmcnt(8)\n\t" \
  "v_mfma_f32_16x16x32_bf16 v[144:147], v[16:19], v[192:195], v[144:147]\n\t" \
  "v_mfma_f32_16x16x32_bf16 v[148:151], v[20:23], v[192:195], v[148:151]\n\t" \
  "ds_read_b128 v[192:195], v214 offset:256\n\t" \
  "v_mfma_f32_16x16x32_bf16 v[152:155], v[16:19], v[196:199], v[152:155]\n\t" \
  "v_mfma_f32_16x16x32_bf16 v[156:159], v[20:23], v[196:199], v[156:159]\n\t" \
  "ds_read_b128 v[196:199], v214 offset:16640\n\t" \
  "s_lshr_b32 s90, s87, 7\n\t" \
  "s_xor_b32 s90, s90, %[s0]\n\t" \
  "s_and_b32 s90, s90, 1\n\t" \
  "s_lshl1_add_u32 s90, s87, s90\n\t" \
  "s_lshl_b32 s90, s90, 13\n\t" \
  "s_cmp_lt_i32 s86, 6\n\t" \
  "s_cselect_b32 s90, 0x70000000, s90\n\t" \
  "s_waitcnt lgkmcnt(8)\n\t" \
  "v_mfma_f32_16x16x32_bf16 v[144:147], v[24:27], v[200:203], v[144:147]\n\t" \
  "v_mfma_f32_16x16x32_bf16 v[148:151], v[28:31], v[200:203], v[148:151]\n\t" \
  "ds_read_b128 v[200:203], v215 offset:256\n\t" \
  "s_add_u32 m0, %[lw], 3072\n\t" \
  "v_mfma_f32_16x16x32_bf16 v[152:155], v[24:27], v[204:207], v[152:155]\n\t" \
  "v_mfma_f32_16x16x32_bf16 v[156:159], v[28:31], v[204:207], v[156:159]\n\t" \
  "ds_read_b128 v[204:207], v215 offset:16640\n\t" \
  "global_load_lds_dwordx4 v219, s[92:93] sc1\n\t" \
  "s_waitcnt lgkmcnt(4)\n\t" \
  "s_cmp_lt_u32 s86, 6\n\t" \
  "s_cbranch_scc1 .Lw21_%=\n\t" \
  "s_waitcnt vmcnt(4)\n\t" \
  "s_barrier\n\t" \
  ".Lg21_%=:\n\t" \
  "s_add_u32 s92, s92, 512\n\t" \
  "s_addc_u32 s93, s93, 0\n\t" \
  "v_mfma_f32_16x16x32_bf16 v[144:147], v[32:35], v[176:179], v[144:147]\n\t" \
  "v_mfma_f32_16x16x32_bf16 v[148:151], v[36:39], v[176:179], v[148:151]\n\t" \
  "ds_read_b128 v[176:179], v212 offset:32768\n\t" \
  "v_mfma_f32_16x16x32_bf16 v[152:155], v[32:35], v[180:183], v[152:155]\n\t" \
  "v_mfma_f32_16x16x32_bf16 v[156:159], v[36:39], v[180:183], v[156:159]\n\t" \
  "ds_read_b128 v[180:183], v212 offset:49152\n\t" \
  "ds_read_b128 v[160:163], v221 offset:0\n\t" \
  "ds_read_b128 v[164:167], v221 offset:1024\n\t" \
  "s_sub_i32 s91, s86, 2\n\t" \
  "s_max_i32 s88, s88, s91\n\t" \
  "v_mov_b32 v173, s88\n\t" \
  "buffer_store_dword v173, v174, %[rs], 0 offen sc1\n\t" \
  "v_mfma_f32_16x16x32_bf16 v[144:147], v[40:43], v[184:187], v[144:147]\n\t" \
  "v_mfma_f32_16x16x32_bf16 v[148:151], v[44:47], v[184:187], v[148:151]\n\t" \
  "ds_read_b128 v[184:187], v213 offset:32768\n\t" \
  "s_add_u32 m0, %[lw], 32768\n\t" \
  "v_mfma_f32_16x16x32_bf16 v[152:155], v[40:43], v[188:191], v[152:155]\n\t" \
  "v_mfma_f32_16x16x32_bf16 v[156:159], v[44:47], v[188:191], v[156:159]\n\t" \
  "ds_read_b128 v[188:191], v213 offset:49152\n\t" \
  "global_load_lds_dwordx4 v216, s[92:93] sc1\n\t" \
  "s_waitcnt lgkmcnt(8)\n\t" \
  "v_mfma_f32_16x16x32_bf16 v[144:147], v[48:51], v[192:195], v[144:147]\n\t" \
  "v_mfma_f32_16x16x32_bf16 v[148:151], v[52:55], v[192:195], v[148:151]\n\t" \
  "ds_read_b128 v[192:195], v214 offset:32768\n\t" \
  "v_mfma_f32_16x16x32_bf16 v[152:155], v[48:51], v[196:199], v[152:155]\n\t" \
  "v_mfma_f32_16x16x32_bf16 v[156:159], v[52:55], v[196:199], v[156:159]\n\t" \
  "ds_read_b128 v[196:199], v214 offset:49152\n\t" \
  "s_waitcnt lgkmcnt(4)\n\t" \
  "v_add_f32 v160, v160, v128\n\t" \
  "v_add_f32 v161, v161, v129\n\t" \
  "v_add_f32 v162, v162, v130\n\t" \
  "v_add_f32 v163, v163, v131\n\t" \
  "v_add_f32 v164, v164, v132\n\t" \
  "v_add_f32 v165, v165, v133\n\t" \
  "v_add_f32 v166, v166, v134\n\t" \
  "v_add_f32 v167, v167, v135\n\t" \
  "v_mfma_f32_16x16x32_bf16 v[144:147], v[56:59], v[200:203], v[144:147]\n\t" \
  "v_mfma_f32_16x16x32_bf16 v[148:151], v[60:63], v[200:203], v[148:151]\n\t" \
  "ds_read_b128 v[200:203], v215 offset:32768\n\t" \
  "s_add_u32 m0, %[lw], 33792\n\t" \
  "v_mfma_f32_16x16x32_bf16 v[152:155], v[56:59], v[204:207], v[152:155]\n\t" \
  "v_mfma_f32_16x16x32_bf16 v[156:159], v[60:63], v[204:207], v[156:159]\n\t" \
  "ds_read_b128 v[204:207], v215 offset:49152\n\t" \
  "global_load_lds_dwordx4 v217, s[92:93] sc1\n\t" \
  "v_cvt_pk_bf16_f32 v168, v160, v161\n\t" \
  "v_cvt_pk_bf16_f32 v169, v162, v163\n\t" \
  "v_cvt_pk_bf16_f32 v170, v164, v165\n\t" \
  "v_cvt_pk_bf16_f32 v171, v166, v167\n\t" \
  "buffer_store_dwordx4 v[168:171], v172, %[rt], s90 offen sc1\n\t" \
  "v_mfma_f32_16x16x32_bf16 v[144:147], v[64:67], v[176:179], v[144:147]\n\t" \
  "v_mfma_f32_16x16x32_bf16 v[148:151], v[68:71], v[176:179], v[148:151]\n\t" \
  "ds_read_b128 v[176:179], v212 offset:33024\n\t" \
  "v_mfma_f32_16x16x32_bf16 v[152:155], v[64:67], v[180:183], v[152:155]\n\t" \
  "v_mfma_f32_16x16x32_bf16 v[156:159], v[68:71], v[180:183], v[156:159]\n\t" \
  "ds_read_b128 v[180:183], v212 offset:49408\n\t" \
  "s_waitcnt lgkmcnt(6)\n\t" \
  "v_mfma_f32_16x16x32_bf16 v[144:147], v[72:75], v[184:187], v[144:147]\n\t" \
  "v_mfma_f32_16x16x32_bf16 v[148:151], v[76:79], v[184:187], v[148:151]\n\t" \
  "ds_read_b128 v[184:187], v213 offset:33024\n\t" \
  "s_add_u32 m0, %[lw], 34816\n\t" \
  "v_mfma_f32_16x16x32_bf16 v[152:155], v[72:75], v[188:191], v[152:155]\n\t" \
  "v_mfma_f32_16x16x32_bf16 v[156:159], v[76:79], v[188:191], v[156:159]\n\t" \
  "ds_read_b128 v[188:191], v213 offset:49408\n\t" \
  "global_load_lds_dwordx4 v218, s[92:93] sc1\n\t" \
  "s_waitcnt lgkmcnt(6)\n\t" \
  "v_mfma_f32_16x16x32_bf16 v[144:147], v[80:83], v[192:195], v[144:147]\n\t" \
  "v_mfma_f32_16x16x32_bf16 v[148:151], v[84:87], v[192:195], v[148:151]\n\t" \
  "ds_read_b128 v[192:195], v214 offset:33024\n\t" \
  "v_mfma_f32_16x16x32_bf16 v[152:155], v[80:83], v[196:199], v[152:155]\n\t" \
  "v_mfma_f32_16x16x32_bf16 v[156:159], v[84:87], v[196:199], v[156:159]\n\t" \
  "ds_read_b128 v[196:199], v214 offset:49408\n\t" \
  "s_waitcnt lgkmcnt(6)\n\t" \
  "v_mfma_f32_16x16x32_bf16 v[144:147], v[88:91], v[200:203], v[144:147]\n\t" \
  "v_mfma_f32_16x16x32_bf16 v[148:151], v[92:95], v[200:203], v[148:151]\n\t" \
  "ds_read_b128 v[200:203], v215 offset:33024\n\t" \
  "s_add_u32 m0, %[lw], 35840\n\t" \
  "v_mfma_f32_16x16x32_bf16 v[152:155], v[88:91], v[204:207], v[152:155]\n\t" \
  "v_mfma_f32_16x16x32_bf16 v[156:159], v[92:95], v[204:207], v[156:159]\n\t" \
  "ds_read_b128 v[204:207], v215 offset:49408\n\t" \
  "global_load_lds_dwordx4 v219, s[92:93] sc1\n\t" \
  "s_cmp_lt_u32 s86, 8\n\t" \
  "s_cbranch_scc1 .Lw31_%=\n\t" \
  "s_waitcnt vmcnt(6)\n\t" \
  "s_barrier\n\t" \
  ".Lg31_%=:\n\t" \
  "s_add_u32 s92, s92, 65024\n\t" \
  "s_addc_u32 s93, s93, 0\n\t" \
  "s_waitcnt lgkmcnt(6)\n\t" \
  "v_mfma_f32_16x16x32_bf16 v[144:147], v[96:99], v[176:179], v[144:147]\n\t" \
  "v_mfma_f32_16x16x32_bf16 v[148:151], v[100:103], v[176:179], v[148:151]\n\t" \
  "ds_read_b128 v[176:179], v208 offset:0\n\t" \
  "v_mfma_f32_16x16x32_bf16 v[152:155], v[96:99], v[180:183], v[152:155]\n\t" \
  "v_mfma_f32_16x16x32_bf16 v[156:159], v[100:103], v[180:183], v[156:159]\n\t" \
  "ds_read_b128 v[180:183], v208 offset:16384\n\t" \
  "s_waitcnt lgkmcnt(6)\n\t" \
  "v_mfma_f32_16x16x32_bf16 v[144:147], v[104:107], v[184:187], v[144:147]\n\t" \
  "v_mfma_f32_16x16x32_bf16 v[148:151], v[108:111], v[184:187], v[148:151]\n\t" \
  "ds_read_b128 v[184:187], v209 offset:0\n\t" \
  "s_add_u32 m0, %[lw], 65536\n\t" \
  "v_mfma_f32_16x16x32_bf16 v[152:155], v[104:107], v[188:191], v[152:155]\n\t" \
  "v_mfma_f32_16x16x32_bf16 v[156:159], v[108:111], v[188:191], v[156:159]\n\t" \
  "ds_read_b128 v[188:191], v209 offset:16384\n\t" \
  "global_load_lds_dwordx4 v216, s[92:93] sc1\n\t" \
  "s_waitcnt lgkmcnt(6)\n\t" \
  "v_mfma_f32_16x16x32_bf16 v[144:147], v[112:115], v[192:195], v[144:147]\n\t" \
  "v_mfma_f32_16x16x32_bf16 v[148:151], v[116:119], v[192:195], v[148:151]\n\t" \
  "ds_read_b128 v[192:195], v210 offset:0\n\t" \
  "v_mfma_f32_16x16x32_bf16 v[152:155], v[112:115], v[196:199], v[152:155]\n\t" \
  "v_mfma_f32_16x16x32_bf16 v[156:159], v[116:119], v[196:199], v[156:159]\n\t" \
  "ds_read_b128 v[196:199], v210 offset:16384\n\t" \
  "s_waitcnt lgkmcnt(6)\n\t" \
  "v_mfma_f32_16x16x32_bf16 v[144:147], v[120:123], v[200:203], v[144:147]\n\t" \
  "v_mfma_f32_16x16x32_bf16 v[148:151], v[124:127], v[200:203], v[148:151]\n\t" \
  "ds_read_b128 v[200:203], v211 offset:0\n\t" \
  "s_add_u32 m0, %[lw], 66560\n\t" \
  "v_mfma_f32_16x16x32_bf16 v[152:155], v[120:123], v[204:207], v[152:155]\n\t" \
  "v_mfma_f32_16x16x32_bf16 v[156:159], v[124:127], v[204:207], v[156:159]\n\t" \
  "ds_read_b128 v[204:207], v211 offset:16384\n\t" \
  "global_load_lds_dwordx4 v217, s[92:93] sc1\n\t" \
  "s_add_u32 s86, s86, 1\n\t" \
  "s_add_u32 s87, s87, 1\n\t" \
  "s_cmp_lt_u32 s86, %[end]\n\t" \
  "s_cbranch_scc1 .Lp1_%=\n\t" \
  "s_branch .Lx1_%=\n\t" \
  ".Lf01_%=:\n\t" \
  "s_nop 7\n\t" \
  "ds_write_b128 v220, v[152:155] offset:0\n\t" \
  "ds_write_b128 v220, v[156:159] offset:1024\n\t" \
  "s_lshr_b32 s90, s87, 7\n\t" \
  "s_xor_b32 s90, s90, %[s0]\n\t" \
  "s_and_b32 s90, s90, 1\n\t" \
  "s_lshl1_add_u32 s90, s87, s90\n\t" \
  "s_lshl_b32 s90, s90, 13\n\t" \
  "s_cmp_lt_i32 s86, 1\n\t" \
  "s_cselect_b32 s90, 0x70000000, s90\n\t" \
  "s_waitcnt lgkmcnt(0)\n\t" \
  "s_barrier\n\t" \
  "ds_read_b128 v[160:163], v221 offset:0\n\t" \
  "ds_read_b128 v[164:167], v221 offset:1024\n\t" \
  "s_waitcnt lgkmcnt(0)\n\t" \
  "v_add_f32 v160, v160, v144\n\t" \
  "v_add_f32 v161, v161, v145\n\t" \
  "v_add_f32 v162, v162, v146\n\t" \
  "v_add_f32 v163, v163, v147\n\t" \
  "v_add_f32 v164, v164, v148\n\t" \
  "v_add_f32 v165, v165, v149\n\t" \
  "v_add_f32 v166, v166, v150\n\t" \
  "v_add_f32 v167, v167, v151\n\t" \
  "v_cvt_pk_bf16_f32 v168, v160, v161\n\t" \
  "v_cvt_pk_bf16_f32 v169, v162, v163\n\t" \
  "v_cvt_pk_bf16_f32 v170, v164, v165\n\t" \
  "v_cvt_pk_bf16_f32 v171, v166, v167\n\t" \
  "buffer_store_dwordx4 v[168:171], v172, %[rt], s90 offen sc1\n\t" \
  "s_branch .Lfb01_%=\n\t" \
  ".Lf11_%=:\n\t" \
  "s_nop 7\n\t" \
  "ds_write_b128 v220, v[136:139] offset:0\n\t" \
  "ds_write_b128 v220, v[140:143] offset:1024\n\t" \
  "s_lshr_b32 s90, s87, 7\n\t" \
  "s_xor_b32 s90, s90, %[s0]\n\t" \
  "s_and_b32 s90, s90, 1\n\t" \
  "s_lshl1_add_u32 s90, s87, s90\n\t" \
  "s_lshl_b32 s90, s90, 13\n\t" \
  "s_cmp_lt_i32 s86, 1\n\t" \
  "s_cselect_b32 s90, 0x70000000, s90\n\t" \
  "s_waitcnt lgkmcnt(0)\n\t" \
  "s_barrier\n\t" \
  "ds_read_b128 v[160:163], v221 offset:0\n\t" \
  "ds_read_b128 v[164:167], v221 offset:1024\n\t" \
  "s_waitcnt lgkmcnt(0)\n\t" \
  "v_add_f32 v160, v160, v128\n\t" \
  "v_add_f32 v161, v161, v129\n\t" \
  "v_add_f32 v162, v162, v130\n\t" \
  "v_add_f32 v163, v163, v131\n\t" \
  "v_add_f32 v164, v164, v132\n\t" \
  "v_add_f32 v165, v165, v133\n\t" \
  "v_add_f32 v166, v166, v134\n\t" \
  "v_add_f32 v167, v167, v135\n\t" \
  "v_cvt_pk_bf16_f32 v168, v160, v161\n\t" \
  "v_cvt_pk_bf16_f32 v169, v162, v163\n\t" \
  "v_cvt_pk_bf16_f32 v170, v164, v165\n\t" \
  "v_cvt_pk_bf16_f32 v171, v166, v167\n\t" \
  "buffer_store_dwordx4 v[168:171], v172, %[rt], s90 offen sc1\n\t" \
  "s_branch .Lfb11_%=\n\t" \
  ".Lw01_%=:\n\t" \
  "s_waitcnt vmcnt(2)\n\t" \
  "s_barrier\n\t" \
  "s_sub_i32 s91, s86, 1\n\t" \
  "s_max_i32 s88, s88, s91\n\t" \
  "v_mov_b32 v173, s88\n\t" \
  "buffer_store_dword v173, v174, %[rs], 0 offen sc1\n\t" \
  "s_branch .Lg01_%=\n\t" \
  ".Lw11_%=:\n\t" \
  "s_waitcnt vmcnt(2)\n\t" \
  "s_barrier\n\t" \
  "s_sub_i32 s91, s86, 1\n\t" \
  "s_max_i32 s88, s88, s91\n\t" \
  "v_mov_b32 v173, s88\n\t" \
  "buffer_store_dword v173, v174, %[rs], 0 offen sc1\n\t" \
  "s_branch .Lg11_%=\n\t" \
  ".Lw21_%=:\n\t" \
  "s_waitcnt vmcnt(2)\n\t" \
  "s_barrier\n\t" \
  "s_sub_i32 s91, s86, 1\n\t" \
  "s_max_i32 s88, s88, s91\n\t" \
  "v_mov_b32 v173, s88\n\t" \
  "buffer_store_dword v173, v174, %[rs], 0 offen sc1\n\t" \
  "s_branch .Lg21_%=\n\t" \
  ".Lw31_%=:\n\t" \
  "s_waitcnt vmcnt(2)\n\t" \
  "s_barrier\n\t" \
  "s_sub_i32 s91, s86, 1\n\t" \
  "s_max_i32 s88, s88, s91\n\t" \
  "v_mov_b32 v173, s88\n\t" \
  "buffer_store_dword v173, v174, %[rs], 0 offen sc1\n\t" \
  "s_branch .Lg31_%=\n\t" \
  ".Lx1_%=:\n\t" \
  "s_waitcnt lgkmcnt(0)\n\t" \
  "s_setprio 0\n\t" \
  "s_mov_b32 m0, s94\n\t" \
  ".Le_%=:\n\t"
#define PR_ASM_1 \
  "s_cmp_lg_u32 %[kh], 0\n\t" \
  "s_cbranch_scc1 .Lk_%=\n\t" \
  "s_mov_b32 s94, m0\n\t" \
  "s_mov_b64 s[92:93], %[x0]\n\t" \
  "s_mov_b32 s86, %[g0]\n\t" \
  "s_add_u32 s87, s86, %[t0]\n\t" \
  "s_sub_i32 s88, s86, 3\n\t" \
  "ds_read_b128 v[176:179], v208 offset:0\n\t" \
  "ds_read_b128 v[180:183], v208 offset:16384\n\t" \
  "ds_read_b128 v[184:187], v209 offset:0\n\t" \
  "ds_read_b128 v[188:191], v209 offset:16384\n\t" \
  "ds_read_b128 v[192:195], v210 offset:0\n\t" \
  "ds_read_b128 v[196:199], v210 offset:16384\n\t" \
  "ds_read_b128 v[200:203], v211 offset:0\n\t" \
  "ds_read_b128 v[204:207], v211 offset:16384\n\t" \
  "s_branch .Lfb00_%=\n\t" \
  ".Lp0_%=:\n\t" \
  "s_cmp_le_u32 s86, 5\n\t" \
  "s_cbranch_scc1 .Lf00_%=\n\t" \
  ".Lfb00_%=:\n\t" \
  "s_waitcnt lgkmcnt(6)\n\t" \
  "v_mfma_f32_16x16x32_bf16 v[128:131], v[176:179], v[0:3], 0\n\t" \
  "v_mfma_f32_16x16x32_bf16 v[136:139], v[176:179], v[4:7], 0\n\t" \
  "ds_read_b128 v[176:179], v208 offset:256\n\t" \
  "s_add_u32 m0, %[lw], 67584\n\t" \
  "v_mfma_f32_16x16x32_bf16 v[132:135], v[180:183], v[0:3], 0\n\t" \
  "v_mfma_f32_16x16x32_bf16 v[140:143], v[180:183], v[4:7], 0\n\t" \
  "ds_read_b128 v[180:183], v208 offset:16640\n\t" \
  "global_load_lds_dwordx4 v218, s[92:93] sc1\n\t" \
  "s_waitcnt lgkmcnt(6)\n\t" \
  "v_mfma_f32_16x16x32_bf16 v[128:131], v[184:187], v[8:11], v[128:131]\n\t" \
  "v_mfma_f32_16x16x32_bf16 v[136:139], v[184:187], v[12:15], v[136:139]\n\t" \
  "ds_read_b128 v[184:187], v209 offset:256\n\t" \
  "v_mfma_f32_16x16x32_bf16 v[132:135], v[188:191], v[8:11], v[132:135]\n\t" \
  "v_mfma_f32_16x16x32_bf16 v[140:143], v[188:191], v[12:15], v[140:143]\n\t" \
  "ds_read_b128 v[188:191], v209 offset:16640\n\t" \
  "ds_write_b128 v220, v[152:155] offset:0\n\t" \
  "ds_write_b128 v220, v[156:159] offset:1024\n\t" \
  "s_waitcnt lgkmcnt(8)\n\t" \
  "v_mfma_f32_16x16x32_bf16 v[128:131], v[192:195], v[16:19], v[128:131]\n\t" \
  "v_mfma_f32_16x16x32_bf16 v[136:139], v[192:195], v[20:23], v[136:139]\n\t" \
  "ds_read_b128 v[192:195], v210 offset:256\n\t" \
  "s_add_u32 m0, %[lw], 68608\n\t" \
  "v_mfma_f32_16x16x32_bf16 v[132:135], v[196:199], v[16:19], v[132:135]\n\t" \
  "v_mfma_f32_16x16x32_bf16 v[140:143], v[196:199], v[20:23], v[140:143]\n\t" \
  "ds_read_b128 v[196:199], v210 offset:16640\n\t" \
  "global_load_lds_dwordx4 v219, s[92:93] sc1\n\t" \
  "s_lshr_b32 s90, s87, 7\n\t" \
  "s_xor_b32 s90, s90, %[s0]\n\t" \
  "s_and_b32 s90, s90, 1\n\t" \
  "s_lshl1_add_u32 s90, s87, s90\n\t" \
  "s_lshl_b32 s90, s90, 13\n\t" \
  "s_cmp_lt_i32 s86, 6\n\t" \
  "s_cselect_b32 s90, 0x70000000, s90\n\t" \
  "s_waitcnt lgkmcnt(8)\n\t" \
  "v_mfma_f32_16x16x32_bf16 v[128:131], v[200:203], v[24:27], v[128:131]\n\t" \
  "v_mfma_f32_16x16x32_bf16 v[136:139], v[200:203], v[28:31], v[136:139]\n\t" \
  "ds_read_b128 v[200:203], v211 offset:256\n\t" \
  "v_mfma_f32_16x16x32_bf16 v[132:135], v[204:207], v[24:27], v[132:135]\n\t" \
  "v_mfma_f32_16x16x32_bf16 v[140:143], v[204:207], v[28:31], v[140:143]\n\t" \
  "ds_read_b128 v[204:207], v211 offset:16640\n\t" \
  "s_waitcnt lgkmcnt(4)\n\t" \
  "s_cmp_lt_u32 s86, 6\n\t" \
  "s_cbranch_scc1 .Lw00_%=\n\t" \
  "s_waitcnt vmcnt(4)\n\t" \
  "s_barrier\n\t" \
  ".Lg00_%=:\n\t" \
  "s_add_u32 s92, s92, 512\n\t" \
  "s_addc_u32 s93, s93, 0\n\t" \
  "v_mfma_f32_16x16x32_bf16 v[128:131], v[176:179], v[32:35], v[128:131]\n\t" \
  "v_mfma_f32_16x16x32_bf16 v[136:139], v[176:179], v[36:39], v[136:139]\n\t" \
  "ds_read_b128 v[176:179], v208 offset:32768\n\t" \
  "s_add_u32 m0, %[lw], 98304\n\t" \
  "v_mfma_f32_16x16x32_bf16 v[132:135], v[180:183], v[32:35], v[132:135]\n\t" \
  "v_mfma_f32_16x16x32_bf16 v[140:143], v[180:183], v[36:39], v[140:143]\n\t" \
  "ds_read_b128 v[180:183], v208 offset:49152\n\t" \
  "global_load_lds_dwordx4 v216, s[92:93] sc1\n\t" \
  "ds_read_b128 v[160:163], v221 offset:0\n\t" \
  "ds_read_b128 v[164:167], v221 offset:1024\n\t" \
  "v_mfma_f32_16x16x32_bf16 v[128:131], v[184:187], v[40:43], v[128:131]\n\t" \
  "v_mfma_f32_16x16x32_bf16 v[136:139], v[184:187], v[44:47], v[136:139]\n\t" \
  "ds_read_b128 v[184:187], v209 offset:32768\n\t" \
  "v_mfma_f32_16x16x32_bf16 v[132:135], v[188:191], v[40:43], v[132:135]\n\t" \
  "v_mfma_f32_16x16x32_bf16 v[140:143], v[188:191], v[44:47], v[140:143]\n\t" \
  "ds_read_b128 v[188:191], v209 offset:49152\n\t" \
  "s_waitcnt lgkmcnt(8)\n\t" \
  "v_mfma_f32_16x16x32_bf16 v[128:131], v[192:195], v[48:51], v[128:131]\n\t" \
  "v_mfma_f32_16x16x32_bf16 v[136:139], v[192:195], v[52:55], v[136:139]\n\t" \
  "ds_read_b128 v[192:195], v210 offset:32768\n\t" \
  "s_add_u32 m0, %[lw], 99328\n\t" \
  "v_mfma_f32_16x16x32_bf16 v[132:135], v[196:199], v[48:51], v[132:135]\n\t" \
  "v_mfma_f32_16x16x32_bf16 v[140:143], v[196:199], v[52:55], v[140:143]\n\t" \
  "ds_read_b128 v[196:199], v210 offset:49152\n\t" \
  "global_load_lds_dwordx4 v217, s[92:93] sc1\n\t" \
  "s_waitcnt lgkmcnt(4)\n\t" \
  "v_add_f32 v160, v160, v144\n\t" \
  "v_add_f32 v161, v161, v145\n\t" \
  "v_add_f32 v162, v162, v146\n\t" \
  "v_add_f32 v163, v163, v147\n\t" \
  "v_add_f32 v164, v164, v148\n\t" \
  "v_add_f32 v165, v165, v149\n\t" \
  "v_add_f32 v166, v166, v150\n\t" \
  "v_add_f32 v167, v167, v151\n\t" \
  "v_mfma_f32_16x16x32_bf16 v[128:131], v[200:203], v[56:59], v[128:131]\n\t" \
  "v_mfma_f32_16x16x32_bf16 v[136:139], v[200:203], v[60:63], v[136:139]\n\t" \
  "ds_read_b128 v[200:203], v211 offset:32768\n\t" \
  "v_mfma_f32_16x16x32_bf16 v[132:135], v[204:207], v[56:59], v[132:135]\n\t" \
  "v_mfma_f32_16x16x32_bf16 v[140:143], v[204:207], v[60:63], v[140:143]\n\t" \
  "ds_read_b128 v[204:207], v211 offset:49152\n\t" \
  "v_cvt_pk_bf16_f32 v168, v160, v161\n\t" \
  "v_cvt_pk_bf16_f32 v169, v162, v163\n\t" \
  "v_cvt_pk_bf16_f32 v170, v164, v165\n\t" \
  "v_cvt_pk_bf16_f32 v171, v166, v167\n\t" \
  "buffer_store_dwordx4 v[168:171], v172, %[rt], s90 offen sc1\n\t" \
  "v_mfma_f32_16x16x32_bf16 v[128:131], v[176:179], v[64:67], v[128:131]\n\t" \
  "v_mfma_f32_16x16x32_bf16 v[136:139], v[176:179], v[68:71], v[136:139]\n\t" \
  "ds_read_b128 v[176:179], v208 offset:33024\n\t" \
  "s_add_u32 m0, %[lw], 100352\n\t" \
  "v_mfma_f32_16x16x32_bf16 v[132:135], v[180:183], v[64:67], v[132:135]\n\t" \
  "v_mfma_f32_16x16x32_bf16 v[140:143], v[180:183], v[68:71], v[140:143]\n\t" \
  "ds_read_b128 v[180:183], v208 offset:49408\n\t" \
  "global_load_lds_dwordx4 v218, s[92:93] sc1\n\t" \
  "s_waitcnt lgkmcnt(6)\n\t" \
  "v_mfma_f32_16x16x32_bf16 v[128:131], v[184:187], v[72:75], v[128:131]\n\t" \
  "v_mfma_f32_16x16x32_bf16 v[136:139], v[184:187], v[76:79], v[136:139]\n\t" \
  "ds_read_b128 v[184:187], v209 offset:33024\n\t" \
  "v_mfma_f32_16x16x32_bf16 v[132:135], v[188:191], v[72:75], v[132:135]\n\t" \
  "v_mfma_f32_16x16x32_bf16 v[140:143], v[188:191], v[76:79], v[140:143]\n\t" \
  "ds_read_b128 v[188:191], v209 offset:49408\n\t" \
  "s_waitcnt lgkmcnt(6)\n\t" \
  "v_mfma_f32_16x16x32_bf16 v[128:131], v[192:195], v[80:83], v[128:131]\n\t" \
  "v_mfma_f32_16x16x32_bf16 v[136:139], v[192:195], v[84:87], v[136:139]\n\t" \
  "ds_read_b128 v[192:195], v210 offset:33024\n\t" \
  "s_add_u32 m0, %[lw], 101376\n\t" \
  "v_mfma_f32_16x16x32_bf16 v[132:135], v[196:199], v[80:83], v[132:135]\n\t" \
  "v_mfma_f32_16x16x32_bf16 v[140:143], v[196:199], v[84:87], v[140:143]\n\t" \
  "ds_read_b128 v[196:199], v210 offset:49408\n\t" \
  "global_load_lds_dwordx4 v219, s[92:93] sc1\n\t" \
  "s_waitcnt lgkmcnt(6)\n\t" \
  "v_mfma_f32_16x16x32_bf16 v[128:131], v[200:203], v[88:91], v[128:131]\n\t" \
  "v_mfma_f32_16x16x32_bf16 v[136:139], v[200:203], v[92:95], v[136:139]\n\t" \
  "ds_read_b128 v[200:203], v211 offset:33024\n\t" \
  "v_mfma_f32_16x16x32_bf16 v[132:135], v[204:207], v[88:91], v[132:135]\n\t" \
  "v_mfma_f32_16x16x32_bf16 v[140:143], v[204:207], v[92:95], v[140:143]\n\t" \
  "ds_read_b128 v[204:207], v211 offset:49408\n\t" \
  "s_cmp_lt_u32 s86, 8\n\t" \
  "s_cbranch_scc1 .Lw10_%=\n\t" \
  "s_waitcnt vmcnt(5)\n\t" \
  "s_barrier\n\t" \
  ".Lg10_%=:\n\t" \
  "s_add_u32 s92, s92, 65024\n\t" \
  "s_addc_u32 s93, s93, 0\n\t" \
  "s_waitcnt lgkmcnt(6)\n\t" \
  "v_mfma_f32_16x16x32_bf16 v[128:131], v[176:179], v[96:99], v[128:131]\n\t" \
  "v_mfma_f32_16x16x32_bf16 v[136:139], v[176:179], v[100:103], v[136:139]\n\t" \
  "ds_read_b128 v[176:179], v212 offset:0\n\t" \
  "s_add_u32 m0, %[lw], 0\n\t" \
  "v_mfma_f32_16x16x32_bf16 v[132:135], v[180:183], v[96:99], v[132:135]\n\t" \
  "v_mfma_f32_16x16x32_bf16 v[140:143], v[180:183], v[100:103], v[140:143]\n\t" \
  "ds_read_b128 v[180:183], v212 offset:16384\n\t" \
  "global_load_lds_dwordx4 v216, s[92:93] sc1\n\t" \
  "s_waitcnt lgkmcnt(6)\n\t" \
  "v_mfma_f32_16x16x32_bf16 v[128:131], v[184:187], v[104:107], v[128:131]\n\t" \
  "v_mfma_f32_16x16x32_bf16 v[136:139], v[184:187], v[108:111], v[136:139]\n\t" \
  "ds_read_b128 v[184:187], v213 offset:0\n\t" \
  "v_mfma_f32_16x16x32_bf16 v[132:135], v[188:191], v[104:107], v[132:135]\n\t" \
  "v_mfma_f32_16x16x32_bf16 v[140:143], v[188:191], v[108:111], v[140:143]\n\t" \
  "ds_read_b128 v[188:191], v213 offset:16384\n\t" \
  "s_waitcnt lgkmcnt(6)\n\t" \
  "v_mfma_f32_16x16x32_bf16 v[128:131], v[192:195], v[112:115], v[128:131]\n\t" \
  "v_mfma_f32_16x16x32_bf16 v[136:139], v[192:195], v[116:119], v[136:139]\n\t" \
  "ds_read_b128 v[192:195], v214 offset:0\n\t" \
  "s_add_u32 m0, %[lw], 1024\n\t" \
  "v_mfma_f32_16x16x32_bf16 v[132:135], v[196:199], v[112:115], v[132:135]\n\t" \
  "v_mfma_f32_16x16x32_bf16 v[140:143], v[196:199], v[116:119], v[140:143]\n\t" \
  "ds_read_b128 v[196:199], v214 offset:16384\n\t" \
  "global_load_lds_dwordx4 v217, s[92:93] sc1\n\t" \
  "s_waitcnt lgkmcnt(6)\n\t" \
  "v_mfma_f32_16x16x32_bf16 v[128:131], v[200:203], v[120:123], v[128:131]\n\t" \
  "v_mfma_f32_16x16x32_bf16 v[136:139], v[200:203], v[124:127], v[136:139]\n\t" \
  "ds_read_b128 v[200:203], v215 offset:0\n\t" \
  "v_mfma_f32_16x16x32_bf16 v[132:135], v[204:207], v[120:123], v[132:135]\n\t" \
  "v_mfma_f32_16x16x32_bf16 v[140:143], v[204:207], v[124:127], v[140:143]\n\t" \
  "ds_read_b128 v[204:207], v215 offset:16384\n\t" \
  "s_add_u32 s86, s86, 1\n\t" \
  "s_add_u32 s87, s87, 1\n\t" \
  "s_cmp_le_u32 s86, 5\n\t" \
  "s_cbranch_scc1 .Lf10_%=\n\t" \
  ".Lfb10_%=:\n\t" \
  "s_waitcnt lgkmcnt(6)\n\t" \
  "v_mfma_f32_16x16x32_bf16 v[144:147], v[176:179], v[0:3], 0\n\t" \
  "v_mfma_f32_16x16x32_bf16 v[152:155], v[176:179], v[4:7], 0\n\t" \
  "ds_read_b128 v[176:179], v212 offset:256\n\t" \
  "s_add_u32 m0, %[lw], 2048\n\t" \
  "v_mfma_f32_16x16x32_bf16 v[148:151], v[180:183], v[0:3], 0\n\t" \
  "v_mfma_f32_16x16x32_bf16 v[156:159], v[180:183], v[4:7], 0\n\t" \
  "ds_read_b128 v[180:183], v212 offset:16640\n\t" \
  "global_load_lds_dwordx4 v218, s[92:93] sc1\n\t" \
  "s_waitcnt lgkmcnt(6)\n\t" \
  "v_mfma_f32_16x16x32_bf16 v[144:147], v[184:187], v[8:11], v[144:147]\n\t" \
  "v_mfma_f32_16x16x32_bf16 v[152:155], v[184:187], v[12:15], v[152:155]\n\t" \
  "ds_read_b128 v[184:187], v213 offset:256\n\t" \
  "v_mfma_f32_16x16x32_bf16 v[148:151], v[188:191], v[8:11], v[148:151]\n\t" \
  "v_mfma_f32_16x16x32_bf16 v[156:159], v[188:191], v[12:15], v[156:159]\n\t" \
  "ds_read_b128 v[188:191], v213 offset:16640\n\t" \
  "ds_write_b128 v220, v[136:139] offset:0\n\t" \
  "ds_write_b128 v220, v[140:143] offset:1024\n\t" \
  "s_waitcnt lgkmcnt(8)\n\t" \
  "v_mfma_f32_16x16x32_bf16 v[144:147], v[192:195], v[16:19], v[144:147]\n\t" \
  "v_mfma_f32_16x16x32_bf16 v[152:155], v[192:195], v[20:23], v[152:155]\n\t" \
  "ds_read_b128 v[192:195], v214 offset:256\n\t" \
  "s_add_u32 m0, %[lw], 3072\n\t" \
  "v_mfma_f32_16x16x32_bf16 v[148:151], v[196:199], v[16:19], v[148:151]\n\t" \
  "v_mfma_f32_16x16x32_bf16 v[156:159], v[196:199], v[20:23], v[156:159]\n\t" \
  "ds_read_b128 v[196:199], v214 offset:16640\n\t" \
  "global_load_lds_dwordx4 v219, s[92:93] sc1\n\t" \
  "s_lshr_b32 s90, s87, 7\n\t" \
  "s_xor_b32 s90, s90, %[s0]\n\t" \
  "s_and_b32 s90, s90, 1\n\t" \
  "s_lshl1_add_u32 s90, s87, s90\n\t" \
  "s_lshl_b32 s90, s90, 13\n\t" \
  "s_cmp_lt_i32 s86, 6\n\t" \
  "s_cselect_b32 s90, 0x70000000, s90\n\t" \
  "s_waitcnt lgkmcnt(8)\n\t" \
  "v_mfma_f32_16x16x32_bf16 v[144:147], v[200:203], v[24:27], v[144:147]\n\t" \
  "v_mfma_f32_16x16x32_bf16 v[152:155], v[200:203], v[28:31], v[152:155]\n\t" \
  "ds_read_b128 v[200:203], v215 offset:256\n\t" \
  "v_mfma_f32_16x16x32_bf16 v[148:151], v[204:207], v[24:27], v[148:151]\n\t" \
  "v_mfma_f32_16x16x32_bf16 v[156:159], v[204:207], v[28:31], v[156:159]\n\t" \
  "ds_read_b128 v[204:207], v215 offset:16640\n\t" \
  "s_waitcnt lgkmcnt(4)\n\t" \
  "s_cmp_lt_u32 s86, 6\n\t" \
  "s_cbranch_scc1 .Lw20_%=\n\t" \
  "s_waitcnt vmcnt(4)\n\t" \
  "s_barrier\n\t" \
  ".Lg20_%=:\n\t" \
  "s_add_u32 s92, s92, 512\n\t" \
  "s_addc_u32 s93, s93, 0\n\t" \
  "v_mfma_f32_16x16x32_bf16 v[144:147], v[176:179], v[32:35], v[144:147]\n\t" \
  "v_mfma_f32_16x16x32_bf16 v[152:155], v[176:179], v[36:39], v[152:155]\n\t" \
  "ds_read_b128 v[176:179], v212 offset:32768\n\t" \
  "s_add_u32 m0, %[lw], 32768\n\t" \
  "v_mfma_f32_16x16x32_bf16 v[148:151], v[180:183], v[32:35], v[148:151]\n\t" \
  "v_mfma_f32_16x16x32_bf16 v[156:159], v[180:183], v[36:39], v[156:159]\n\t" \
  "ds_read_b128 v[180:183], v212 offset:49152\n\t" \
  "global_load_lds_dwordx4 v216, s[92:93] sc1\n\t" \
  "ds_read_b128 v[160:163], v221 offset:0\n\t" \
  "ds_read_b128 v[164:167], v221 offset:1024\n\t" \
  "v_mfma_f32_16x16x32_bf16 v[144:147], v[184:187], v[40:43], v[144:147]\n\t" \
  "v_mfma_f32_16x16x32_bf16 v[152:155], v[184:187], v[44:47], v[152:155]\n\t" \
  "ds_read_b128 v[184:187], v213 offset:32768\n\t" \
  "v_mfma_f32_16x16x32_bf16 v[148:151], v[188:191], v[40:43], v[148:151]\n\t" \
  "v_mfma_f32_16x16x32_bf16 v[156:159], v[188:191], v[44:47], v[156:159]\n\t" \
  "ds_read_b128 v[188:191], v213 offset:49152\n\t" \
  "s_waitcnt lgkmcnt(8)\n\t" \
  "v_mfma_f32_16x16x32_bf16 v[144:147], v[192:195], v[48:51], v[144:147]\n\t" \
  "v_mfma_f32_16x16x32_bf16 v[152:155], v[192:195], v[52:55], v[152:155]\n\t" \
  "ds_read_b128 v[192:195], v214 offset:32768\n\t" \
  "s_add_u32 m0, %[lw], 33792\n\t" \
  "v_mfma_f32_16x16x32_bf16 v[148:151], v[196:199], v[48:51], v[148:151]\n\t" \
  "v_mfma_f32_16x16x32_bf16 v[156:159], v[196:199], v[52:55], v[156:159]\n\t" \
  "ds_read_b128 v[196:199], v214 offset:49152\n\t" \
  "global_load_lds_dwordx4 v217, s[92:93] sc1\n\t" \
  "s_waitcnt lgkmcnt(4)\n\t" \
  "v_add_f32 v160, v160, v128\n\t" \
  "v_add_f32 v161, v161, v129\n\t" \
  "v_add_f32 v162, v162, v130\n\t" \
  "v_add_f32 v163, v163, v131\n\t" \
  "v_add_f32 v164, v164, v132\n\t" \
  "v_add_f32 v165, v165, v133\n\t" \
  "v_add_f32 v166, v166, v134\n\t" \
  "v_add_f32 v167, v167, v135\n\t" \
  "v_mfma_f32_16x16x32_bf16 v[144:147], v[200:203], v[56:59], v[144:147]\n\t" \
  "v_mfma_f32_16x16x32_bf16 v[152:155], v[200:203], v[60:63], v[152:155]\n\t" \
  "ds_read_b128 v[200:203], v215 offset:32768\n\t" \
  "v_mfma_f32_16x16x32_bf16 v[148:151], v[204:207], v[56:59], v[148:151]\n\t" \
  "v_mfma_f32_16x16x32_bf16 v[156:159], v[204:207], v[60:63], v[156:159]\n\t" \
  "ds_read_b128 v[204:207], v215 offset:49152\n\t" \
  "v_cvt_pk_bf16_f32 v168, v160, v161\n\t" \
  "v_cvt_pk_bf16_f32 v169, v162, v163\n\t" \
  "v_cvt_pk_bf16_f32 v170, v164, v165\n\t" \
  "v_cvt_pk_bf16_f32 v171, v166, v167\n\t" \
  "buffer_store_dwordx4 v[168:171], v172, %[rt], s90 offen sc1\n\t" \
  "v_mfma_f32_16x16x32_bf16 v[144:147], v[176:179], v[64:67], v[144:147]\n\t" \
  "v_mfma_f32_16x16x32_bf16 v[152:155], v[176:179], v[68:71], v[152:155]\n\t" \
  "ds_read_b128 v[176:179], v212 offset:33024\n\t" \
  "s_add_u32 m0, %[lw], 34816\n\t" \
  "v_mfma_f32_16x16x32_bf16 v[148:151], v[180:183], v[64:67], v[148:151]\n\t" \
  "v_mfma_f32_16x16x32_bf16 v[156:159], v[180:183], v[68:71], v[156:159]\n\t" \
  "ds_read_b128 v[180:183], v212 offset:49408\n\t" \
  "global_load_lds_dwordx4 v218, s[92:93] sc1\n\t" \
  "s_waitcnt lgkmcnt(6)\n\t" \
  "v_mfma_f32_16x16x32_bf16 v[144:147], v[184:187], v[72:75], v[144:147]\n\t" \
  "v_mfma_f32_16x16x32_bf16 v[152:155], v[184:187], v[76:79], v[152:155]\n\t" \
  "ds_read_b128 v[184:187], v213 offset:33024\n\t" \
  "v_mfma_f32_16x16x32_bf16 v[148:151], v[188:191], v[72:75], v[148:151]\n\t" \
  "v_mfma_f32_16x16x32_bf16 v[156:159], v[188:191], v[76:79], v[156:159]\n\t" \
  "ds_read_b128 v[188:191], v213 offset:49408\n\t" \
  "s_waitcnt lgkmcnt(6)\n\t" \
  "v_mfma_f32_16x16x32_bf16 v[144:147], v[192:195], v[80:83], v[144:147]\n\t" \
  "v_mfma_f32_16x16x32_bf16 v[152:155], v[192:195], v[84:87], v[152:155]\n\t" \
  "ds_read_b128 v[192:195], v214 offset:33024\n\t" \
  "s_add_u32 m0, %[lw], 35840\n\t" \
  "v_mfma_f32_16x16x32_bf16 v[148:151], v[196:199], v[80:83], v[148:151]\n\t" \
  "v_mfma_f32_16x16x32_bf16 v[156:159], v[196:199], v[84:87], v[156:159]\n\t" \
  "ds_read_b128 v[196:199], v214 offset:49408\n\t" \
  "global_load_lds_dwordx4 v219, s[92:93] sc1\n\t" \
  "s_waitcnt lgkmcnt(6)\n\t" \
  "v_mfma_f32_16x16x32_bf16 v[144:147], v[200:203], v[88:91], v[144:147]\n\t" \
  "v_mfma_f32_16x16x32_bf16 v[152:155], v[200:203], v[92:95], v[152:155]\n\t" \
  "ds_read_b128 v[200:203], v215 offset:33024\n\t" \
  "v_mfma_f32_16x16x32_bf16 v[148:151], v[204:207], v[88:91], v[148:151]\n\t" \
  "v_mfma_f32_16x16x32_bf16 v[156:159], v[204:207], v[92:95], v[156:159]\n\t" \
  "ds_read_b128 v[204:207], v215 offset:49408\n\t" \
  "s_cmp_lt_u32 s86, 8\n\t" \
  "s_cbranch_scc1 .Lw30_%=\n\t" \
  "s_waitcnt vmcnt(5)\n\t" \
  "s_barrier\n\t" \
  ".Lg30_%=:\n\t" \
  "s_add_u32 s92, s92, 65024\n\t" \
  "s_addc_u32 s93, s93, 0\n\t" \
  "s_waitcnt lgkmcnt(6)\n\t" \
  "v_mfma_f32_16x16x32_bf16 v[144:147], v[176:179], v[96:99], v[144:147]\n\t" \
  "v_mfma_f32_16x16x32_bf16 v[152:155], v[176:179], v[100:103], v[152:155]\n\t" \
  "ds_read_b128 v[176:179], v208 offset:0\n\t" \
  "s_add_u32 m0, %[lw], 65536\n\t" \
  "v_mfma_f32_16x16x32_bf16 v[148:151], v[180:183], v[96:99], v[148:151]\n\t" \
  "v_mfma_f32_16x16x32_bf16 v[156:159], v[180:183], v[100:103], v[156:159]\n\t" \
  "ds_read_b128 v[180:183], v208 offset:16384\n\t" \
  "global_load_lds_dwordx4 v216, s[92:93] sc1\n\t" \
  "s_waitcnt lgkmcnt(6)\n\t" \
  "v_mfma_f32_16x16x32_bf16 v[144:147], v[184:187], v[104:107], v[144:147]\n\t" \
  "v_mfma_f32_16x16x32_bf16 v[152:155], v[184:187], v[108:111], v[152:155]\n\t" \
  "ds_read_b128 v[184:187], v209 offset:0\n\t" \
  "v_mfma_f32_16x16x32_bf16 v[148:151], v[188:191], v[104:107], v[148:151]\n\t" \
  "v_mfma_f32_16x16x32_bf16 v[156:159], v[188:191], v[108:111], v[156:159]\n\t" \
  "ds_read_b128 v[188:191], v209 offset:16384\n\t" \
  "s_waitcnt lgkmcnt(6)\n\t" \
  "v_mfma_f32_16x16x32_bf16 v[144:147], v[192:195], v[112:115], v[144:147]\n\t" \
  "v_mfma_f32_16x16x32_bf16 v[152:155], v[192:195], v[116:119], v[152:155]\n\t" \
  "ds_read_b128 v[192:195], v210 offset:0\n\t" \
  "s_add_u32 m0, %[lw], 66560\n\t" \
  "v_mfma_f32_16x16x32_bf16 v[148:151], v[196:199], v[112:115], v[148:151]\n\t" \
  "v_mfma_f32_16x16x32_bf16 v[156:159], v[196:199], v[116:119], v[156:159]\n\t" \
  "ds_read_b128 v[196:199], v210 offset:16384\n\t" \
  "global_load_lds_dwordx4 v217, s[92:93] sc1\n\t" \
  "s_waitcnt lgkmcnt(6)\n\t" \
  "v_mfma_f32_16x16x32_bf16 v[144:147], v[200:203], v[120:123], v[144:147]\n\t" \
  "v_mfma_f32_16x16x32_bf16 v[152:155], v[200:203], v[124:127], v[152:155]\n\t" \
  "ds_read_b128 v[200:203], v211 offset:0\n\t" \
  "v_mfma_f32_16x16x32_bf16 v[148:151], v[204:207], v[120:123], v[148:151]\n\t" \
  "v_mfma_f32_16x16x32_bf16 v[156:159], v[204:207], v[124:127], v[156:159]\n\t" \
  "ds_read_b128 v[204:207], v211 offset:16384\n\t" \
  "s_add_u32 s86, s86, 1\n\t" \
  "s_add_u32 s87, s87, 1\n\t" \
  "s_cmp_lt_u32 s86, %[end]\n\t" \
  "s_cbranch_scc1 .Lp0_%=\n\t" \
  "s_branch .Lx0_%=\n\t" \
  ".Lf00_%=:\n\t" \
  "s_nop 7\n\t" \
  "ds_write_b128 v220, v[152:155] offset:0\n\t" \
  "ds_write_b128 v220, v[156:159] offset:1024\n\t" \
  "s_lshr_b32 s90, s87, 7\n\t" \
  "s_xor_b32 s90, s90, %[s0]\n\t" \
  "s_and_b32 s90, s90, 1\n\t" \
  "s_lshl1_add_u32 s90, s87, s90\n\t" \
  "s_lshl_b32 s90, s90, 13\n\t" \
  "s_cmp_lt_i32 s86, 1\n\t" \
  "s_cselect_b32 s90, 0x70000000, s90\n\t" \
  "s_waitcnt lgkmcnt(0)\n\t" \
  "s_barrier\n\t" \
  "ds_read_b128 v[160:163], v221 offset:0\n\t" \
  "ds_read_b128 v[164:167], v221 offset:1024\n\t" \
  "s_waitcnt lgkmcnt(0)\n\t" \
  "v_add_f32 v160, v160, v144\n\t" \
  "v_add_f32 v161, v161, v145\n\t" \
  "v_add_f32 v162, v162, v146\n\t" \
  "v_add_f32 v163, v163, v147\n\t" \
  "v_add_f32 v164, v164, v148\n\t" \
  "v_add_f32 v165, v165, v149\n\t" \
  "v_add_f32 v166, v166, v150\n\t" \
  "v_add_f32 v167, v167, v151\n\t" \
  "v_cvt_pk_bf16_f32 v168, v160, v161\n\t" \
  "v_cvt_pk_bf16_f32 v169, v162, v163\n\t" \
  "v_cvt_pk_bf16_f32 v170, v164, v165\n\t" \
  "v_cvt_pk_bf16_f32 v171, v166, v167\n\t" \
  "buffer_store_dwordx4 v[168:171], v172, %[rt], s90 offen sc1\n\t" \
  "s_branch .Lfb00_%=\n\t" \
  ".Lf10_%=:\n\t" \
  "s_nop 7\n\t" \
  "ds_write_b128 v220, v[136:139] offset:0\n\t" \
  "ds_write_b128 v220, v[140:143] offset:1024\n\t" \
  "s_lshr_b32 s90, s87, 7\n\t" \
  "s_xor_b32 s90, s90, %[s0]\n\t" \
  "s_and_b32 s90, s90, 1\n\t" \
  "s_lshl1_add_u32 s90, s87, s90\n\t" \
  "s_lshl_b32 s90, s90, 13\n\t" \
  "s_cmp_lt_i32 s86, 1\n\t" \
  "s_cselect_b32 s90, 0x70000000, s90\n\t" \
  "s_waitcnt lgkmcnt(0)\n\t" \
  "s_barrier\n\t" \
  "ds_read_b128 v[160:163], v221 offset:0\n\t" \
  "ds_read_b128 v[164:167], v221 offset:1024\n\t" \
  "s_waitcnt lgkmcnt(0)\n\t" \
  "v_add_f32 v160, v160, v128\n\t" \
  "v_add_f32 v161, v161, v129\n\t" \
  "v_add_f32 v162, v162, v130\n\t" \
  "v_add_f32 v163, v163, v131\n\t" \
  "v_add_f32 v164, v164, v132\n\t" \
  "v_add_f32 v165, v165, v133\n\t" \
  "v_add_f32 v166, v166, v134\n\t" \
  "v_add_f32 v167, v167, v135\n\t" \
  "v_cvt_pk_bf16_f32 v168, v160, v161\n\t" \
  "v_cvt_pk_bf16_f32 v169, v162, v163\n\t" \
  "v_cvt_pk_bf16_f32 v170, v164, v165\n\t" \
  "v_cvt_pk_bf16_f32 v171, v166, v167\n\t" \
  "buffer_store_dwordx4 v[168:171], v172, %[rt], s90 offen sc1\n\t" \
  "s_branch .Lfb10_%=\n\t" \
  ".Lw00_%=:\n\t" \
  "s_waitcnt vmcnt(2)\n\t" \
  "s_barrier\n\t" \
  "s_branch .Lg00_%=\n\t" \
  ".Lw10_%=:\n\t" \
  "s_waitcnt vmcnt(2)\n\t" \
  "s_barrier\n\t" \
  "s_branch .Lg10_%=\n\t" \
  ".Lw20_%=:\n\t" \
  "s_waitcnt vmcnt(2)\n\t" \
  "s_barrier\n\t" \
  "s_branch .Lg20_%=\n\t" \
  ".Lw30_%=:\n\t" \
  "s_waitcnt vmcnt(2)\n\t" \
  "s_barrier\n\t" \
  "s_branch .Lg30_%=\n\t" \
  ".Lx0_%=:\n\t" \
  "s_waitcnt lgkmcnt(0)\n\t" \
  "s_setprio 0\n\t" \
  "s_mov_b32 m0, s94\n\t" \
  "s_branch .Le_%=\n\t" \
  ".Lk_%=:\n\t" \
  "s_mov_b32 s94, m0\n\t" \
  "s_mov_b64 s[92:93], %[x0]\n\t" \
  "s_mov_b32 s86, %[g0]\n\t" \
  "s_add_u32 s87, s86, %[t0]\n\t" \
  "s_sub_i32 s88, s86, 3\n\t" \
  "ds_read_b128 v[176:179], v208 offset:0\n\t" \
  "ds_read_b128 v[180:183], v208 offset:16384\n\t" \
  "ds_read_b128 v[184:187], v209 offset:0\n\t" \
  "ds_read_b128 v[188:191], v209 offset:16384\n\t" \
  "ds_read_b128 v[192:195], v210 offset:0\n\t" \
  "ds_read_b128 v[196:199], v210 offset:16384\n\t" \
  "ds_read_b128 v[200:203], v211 offset:0\n\t" \
  "ds_read_b128 v[204:207], v211 offset:16384\n\t" \
  "s_branch .Lfb01_%=\n\t" \
  ".Lp1_%=:\n\t" \
  "s_cmp_le_u32 s86, 5\n\t" \
  "s_cbranch_scc1 .Lf01_%=\n\t" \
  ".Lfb01_%=:\n\t" \
  "s_waitcnt lgkmcnt(6)\n\t" \
  "v_mfma_f32_16x16x32_bf16 v[128:131], v[176:179], v[0:3], 0\n\t" \
  "v_mfma_f32_16x16x32_bf16 v[136:139], v[176:179], v[4:7], 0\n\t" \
  "ds_read_b128 v[176:179], v208 offset:256\n\t" \
  "v_mfma_f32_16x16x32_bf16 v[132:135], v[180:183], v[0:3], 0\n\t" \
  "v_mfma_f32_16x16x32_bf16 v[140:143], v[180:183], v[4:7], 0\n\t" \
  "ds_read_b128 v[180:183], v208 offset:16640\n\t" \
  "s_waitcnt lgkmcnt(6)\n\t" \
  "v_mfma_f32_16x16x32_bf16 v[128:131], v[184:187], v[8:11], v[128:131]\n\t" \
  "v_mfma_f32_16x16x32_bf16 v[136:139], v[184:187], v[12:15], v[136:139]\n\t" \
  "ds_read_b128 v[184:187], v209 offset:256\n\t" \
  "s_add_u32 m0, %[lw], 67584\n\t" \
  "v_mfma_f32_16x16x32_bf16 v[132:135], v[188:191], v[8:11], v[132:135]\n\t" \
  "v_mfma_f32_16x16x32_bf16 v[140:143], v[188:191], v[12:15], v[140:143]\n\t" \
  "ds_read_b128 v[188:191], v209 offset:16640\n\t" \
  "global_load_lds_dwordx4 v218, s[92:93] sc1\n\t" \
  "ds_write_b128 v220, v[152:155] offset:0\n\t" \
  "ds_write_b128 v220, v[156:159] offset:1024\n\t" \
  "s_waitcnt lgkmcnt(8)\n\t" \
  "v_mfma_f32_16x16x32_bf16 v[128:131], v[192:195], v[16:19], v[128:131]\n\t" \
  "v_mfma_f32_16x16x32_bf16 v[136:139], v[192:195], v[20:23], v[136:139]\n\t" \
  "ds_read_b128 v[192:195], v210 offset:256\n\t" \
  "v_mfma_f32_16x16x32_bf16 v[132:135], v[196:199], v[16:19], v[132:135]\n\t" \
  "v_mfma_f32_16x16x32_bf16 v[140:143], v[196:199], v[20:23], v[140:143]\n\t" \
  "ds_read_b128 v[196:199], v210 offset:16640\n\t" \
  "s_lshr_b32 s90, s87, 7\n\t" \
  "s_xor_b32 s90, s90, %[s0]\n\t" \
  "s_and_b32 s90, s90, 1\n\t" \
  "s_lshl1_add_u32 s90, s87, s90\n\t" \
  "s_lshl_b32 s90, s90, 13\n\t" \
  "s_cmp_lt_i32 s86, 6\n\t" \
  "s_cselect_b32 s90, 0x70000000, s90\n\t" \
  "s_waitcnt lgkmcnt(8)\n\t" \
  "v_mfma_f32_16x16x32_bf16 v[128:131], v[200:203], v[24:27], v[128:131]\n\t" \
  "v_mfma_f32_16x16x32_bf16 v[136:139], v[200:203], v[28:31], v[136:139]\n\t" \
  "ds_read_b128 v[200:203], v211 offset:256\n\t" \
  "s_add_u32 m0, %[lw], 68608\n\t" \
  "v_mfma_f32_16x16x32_bf16 v[132:135], v[204:207], v[24:27], v[132:135]\n\t" \
  "v_mfma_f32_16x16x32_bf16 v[140:143], v[204:207], v[28:31], v[140:143]\n\t" \
  "ds_read_b128 v[204:207], v211 offset:16640\n\t" \
  "global_load_lds_dwordx4 v219, s[92:93] sc1\n\t" \
  "s_waitcnt lgkmcnt(4)\n\t" \
  "s_cmp_lt_u32 s86, 6\n\t" \
  "s_cbranch_scc1 .Lw01_%=\n\t" \
  "s_waitcnt vmcnt(4)\n\t" \
  "s_barrier\n\t" \
  ".Lg01_%=:\n\t" \
  "s_add_u32 s92, s92, 512\n\t" \
  "s_addc_u32 s93, s93, 0\n\t" \
  "v_mfma_f32_16x16x32_bf16 v[128:131], v[176:179], v[32:35], v[128:131]\n\t" \
  "v_mfma_f32_16x16x32_bf16 v[136:139], v[176:179], v[36:39], v[136:139]\n\t" \
  "ds_read_b128 v[176:179], v208 offset:32768\n\t" \
  "v_mfma_f32_16x16x32_bf16 v[132:135], v[180:183], v[32:35], v[132:135]\n\t" \
  "v_mfma_f32_16x16x32_bf16 v[140:143], v[180:183], v[36:39], v[140:143]\n\t" \
  "ds_read_b128 v[180:183], v208 offset:49152\n\t" \
  "ds_read_b128 v[160:163], v221 offset:0\n\t" \
  "ds_read_b128 v[164:167], v221 offset:1024\n\t" \
  "s_sub_i32 s91, s86, 2\n\t" \
  "s_max_i32 s88, s88, s91\n\t" \
  "v_mov_b32 v173, s88\n\t" \
  "buffer_store_dword v173, v174, %[rs], 0 offen sc1\n\t" \
  "v_mfma_f32_16x16x32_bf16 v[128:131], v[184:187], v[40:43], v[128:131]\n\t" \
  "v_mfma_f32_16x16x32_bf16 v[136:139], v[184:187], v[44:47], v[136:139]\n\t" \
  "ds_read_b128 v[184:187], v209 offset:32768\n\t" \
  "s_add_u32 m0, %[lw], 98304\n\t" \
  "v_mfma_f32_16x16x32_bf16 v[132:135], v[188:191], v[40:43], v[132:135]\n\t" \
  "v_mfma_f32_16x16x32_bf16 v[140:143], v[188:191], v[44:47], v[140:143]\n\t" \
  "ds_read_b128 v[188:191], v209 offset:49152\n\t" \
  "global_load_lds_dwordx4 v216, s[92:93] sc1\n\t" \
  "s_waitcnt lgkmcnt(8)\n\t" \
  "v_mfma_f32_16x16x32_bf16 v[128:131], v[192:195], v[48:51], v[128:131]\n\t" \
  "v_mfma_f32_16x16x32_bf16 v[136:139], v[192:195], v[52:55], v[136:139]\n\t" \
  "ds_read_b128 v[192:195], v210 offset:32768\n\t" \
  "v_mfma_f32_16x16x32_bf16 v[132:135], v[196:199], v[48:51], v[132:135]\n\t" \
  "v_mfma_f32_16x16x32_bf16 v[140:143], v[196:199], v[52:55], v[140:143]\n\t" \
  "ds_read_b128 v[196:199], v210 offset:49152\n\t" \
  "s_waitcnt lgkmcnt(4)\n\t" \
  "v_add_f32 v160, v160, v144\n\t" \
  "v_add_f32 v161, v161, v145\n\t" \
  "v_add_f32 v162, v162, v146\n\t" \
  "v_add_f32 v163, v163, v147\n\t" \
  "v_add_f32 v164, v164, v148\n\t" \
  "v_add_f32 v165, v165, v149\n\t" \
  "v_add_f32 v166, v166, v150\n\t" \
  "v_add_f32 v167, v167, v151\n\t" \
  "v_mfma_f32_16x16x32_bf16 v[128:131], v[200:203], v[56:59], v[128:131]\n\t" \
  "v_mfma_f32_16x16x32_bf16 v[136:139], v[200:203], v[60:63], v[136:139]\n\t" \
  "ds_read_b128 v[200:203], v211 offset:32768\n\t" \
  "s_add_u32 m0, %[lw], 99328\n\t" \
  "v_mfma_f32_16x16x32_bf16 v[132:135], v[204:207], v[56:59], v[132:135]\n\t" \
  "v_mfma_f32_16x16x32_bf16 v[140:143], v[204:207], v[60:63], v[140:143]\n\t" \
  "ds_read_b128 v[204:207], v211 offset:49152\n\t" \
  "global_load_lds_dwordx4 v217, s[92:93] sc1\n\t" \
  "v_cvt_pk_bf16_f32 v168, v160, v161\n\t" \
  "v_cvt_pk_bf16_f32 v169, v162, v163\n\t" \
  "v_cvt_pk_bf16_f32 v170, v164, v165\n\t" \
  "v_cvt_pk_bf16_f32 v171, v166, v167\n\t" \
  "buffer_store_dwordx4 v[168:171], v172, %[rt], s90 offen sc1\n\t" \
  "v_mfma_f32_16x16x32_bf16 v[128:131], v[176:179], v[64:67], v[128:131]\n\t" \
  "v_mfma_f32_16x16x32_bf16 v[136:139], v[176:179], v[68:71], v[136:139]\n\t" \
  "ds_read_b128 v[176:179], v208 offset:33024\n\t" \
  "v_mfma_f32_16x16x32_bf16 v[132:135], v[180:183], v[64:67], v[132:135]\n\t" \
  "v_mfma_f32_16x16x32_bf16 v[140:143], v[180:183], v[68:71], v[140:143]\n\t" \
  "ds_read_b128 v[180:183], v208 offset:49408\n\t" \
  "s_waitcnt lgkmcnt(6)\n\t" \
  "v_mfma_f32_16x16x32_bf16 v[128:131], v[184:187], v[72:75], v[128:131]\n\t" \
  "v_mfma_f32_16x16x32_bf16 v[136:139], v[184:187], v[76:79], v[136:139]\n\t" \
  "ds_read_b128 v[184:187], v209 offset:33024\n\t" \
  "s_add_u32 m0, %[lw], 100352\n\t" \
  "v_mfma_f32_16x16x32_bf16 v[132:135], v[188:191], v[72:75], v[132:135]\n\t" \
  "v_mfma_f32_16x16x32_bf16 v[140:143], v[188:191], v[76:79], v[140:143]\n\t" \
  "ds_read_b128 v[188:191], v209 offset:49408\n\t" \
  "global_load_lds_dwordx4 v218, s[92:93] sc1\n\t" \
  "s_waitcnt lgkmcnt(6)\n\t" \
  "v_mfma_f32_16x16x32_bf16 v[128:131], v[192:195], v[80:83], v[128:131]\n\t" \
  "v_mfma_f32_16x16x32_bf16 v[136:139], v[192:195], v[84:87], v[136:139]\n\t" \
  "ds_read_b128 v[192:195], v210 offset:33024\n\t" \
  "v_mfma_f32_16x16x32_bf16 v[132:135], v[196:199], v[80:83], v[132:135]\n\t" \
  "v_mfma_f32_16x16x32_bf16 v[140:143], v[196:199], v[84:87], v[140:143]\n\t" \
  "ds_read_b128 v[196:199], v210 offset:49408\n\t" \
  "s_waitcnt lgkmcnt(6)\n\t" \
  "v_mfma_f32_16x16x32_bf16 v[128:131], v[200:203], v[88:91], v[128:131]\n\t" \
  "v_mfma_f32_16x16x32_bf16 v[136:139], v[200:203], v[92:95], v[136:139]\n\t" \
  "ds_read_b128 v[200:203], v211 offset:33024\n\t" \
  "s_add_u32 m0, %[lw], 101376\n\t" \
  "v_mfma_f32_16x16x32_bf16 v[132:135], v[204:207], v[88:91], v[132:135]\n\t" \
  "v_mfma_f32_16x16x32_bf16 v[140:143], v[204:207], v[92:95], v[140:143]\n\t" \
  "ds_read_b128 v[204:207], v211 offset:49408\n\t" \
  "global_load_lds_dwordx4 v219, s[92:93] sc1\n\t" \
  "s_cmp_lt_u32 s86, 8\n\t" \
  "s_cbranch_scc1 .Lw11_%=\n\t" \
  "s_waitcnt vmcnt(6)\n\t" \
  "s_barrier\n\t" \
  ".Lg11_%=:\n\t" \
  "s_add_u32 s92, s92, 65024\n\t" \
  "s_addc_u32 s93, s93, 0\n\t" \
  "s_waitcnt lgkmcnt(6)\n\t" \
  "v_mfma_f32_16x16x32_bf16 v[128:131], v[176:179], v[96:99], v[128:131]\n\t" \
  "v_mfma_f32_16x16x32_bf16 v[136:139], v[176:179], v[100:103], v[136:139]\n\t" \
  "ds_read_b128 v[176:179], v212 offset:0\n\t" \
  "v_mfma_f32_16x16x32_bf16 v[132:135], v[180:183], v[96:99], v[132:135]\n\t" \
  "v_mfma_f32_16x16x32_bf16 v[140:143], v[180:183], v[100:103], v[140:143]\n\t" \
  "ds_read_b128 v[180:183], v212 offset:16384\n\t" \
  "s_waitcnt lgkmcnt(6)\n\t" \
  "v_mfma_f32_16x16x32_bf16 v[128:131], v[184:187], v[104:107], v[128:131]\n\t" \
  "v_mfma_f32_16x16x32_bf16 v[136:139], v[184:187], v[108:111], v[136:139]\n\t" \
  "ds_read_b128 v[184:187], v213 offset:0\n\t" \
  "s_add_u32 m0, %[lw], 0\n\t" \
  "v_mfma_f32_16x16x32_bf16 v[132:135], v[188:191], v[104:107], v[132:135]\n\t" \
  "v_mfma_f32_16x16x32_bf16 v[140:143], v[188:191], v[108:111], v[140:143]\n\t" \
  "ds_read_b128 v[188:191], v213 offset:16384\n\t" \
  "global_load_lds_dwordx4 v216, s[92:93] sc1\n\t" \
  "s_waitcnt lgkmcnt(6)\n\t" \
  "v_mfma_f32_16x16x32_bf16 v[128:131], v[192:195], v[112:115], v[128:131]\n\t" \
  "v_mfma_f32_16x16x32_bf16 v[136:139], v[192:195], v[116:119], v[136:139]\n\t" \
  "ds_read_b128 v[192:195], v214 offset:0\n\t" \
  "v_mfma_f32_16x16x32_bf16 v[132:135], v[196:199], v[112:115], v[132:135]\n\t" \
  "v_mfma_f32_16x16x32_bf16 v[140:143], v[196:199], v[116:119], v[140:143]\n\t" \
  "ds_read_b128 v[196:199], v214 offset:16384\n\t" \
  "s_waitcnt lgkmcnt(6)\n\t" \
  "v_mfma_f32_16x16x32_bf16 v[128:131], v[200:203], v[120:123], v[128:131]\n\t" \
  "v_mfma_f32_16x16x32_bf16 v[136:139], v[200:203], v[124:127], v[136:139]\n\t" \
  "ds_read_b128 v[200:203], v215 offset:0\n\t" \
  "s_add_u32 m0, %[lw], 1024\n\t" \
  "v_mfma_f32_16x16x32_bf16 v[132:135], v[204:207], v[120:123], v[132:135]\n\t" \
  "v_mfma_f32_16x16x32_bf16 v[140:143], v[204:207], v[124:127], v[140:143]\n\t" \
  "ds_read_b128 v[204:207], v215 offset:16384\n\t" \
  "global_load_lds_dwordx4 v217, s[92:93] sc1\n\t" \
  "s_add_u32 s86, s86, 1\n\t" \
  "s_add_u32 s87, s87, 1\n\t" \
  "s_cmp_le_u32 s86, 5\n\t" \
  "s_cbranch_scc1 .Lf11_%=\n\t" \
  ".Lfb11_%=:\n\t" \
  "s_waitcnt lgkmcnt(6)\n\t" \
  "v_mfma_f32_16x16x32_bf16 v[144:147], v[176:179], v[0:3], 0\n\t" \
  "v_mfma_f32_16x16x32_bf16 v[152:155], v[176:179], v[4:7], 0\n\t" \
  "ds_read_b128 v[176:179], v212 offset:256\n\t" \
  "v_mfma_f32_16x16x32_bf16 v[148:151], v[180:183], v[0:3], 0\n\t" \
  "v_mfma_f32_16x16x32_bf16 v[156:159], v[180:183], v[4:7], 0\n\t" \
  "ds_read_b128 v[180:183], v212 offset:16640\n\t" \
  "s_waitcnt lgkmcnt(6)\n\t" \
  "v_mfma_f32_16x16x32_bf16 v[144:147], v[184:187], v[8:11], v[144:147]\n\t" \
  "v_mfma_f32_16x16x32_bf16 v[152:155], v[184:187], v[12:15], v[152:155]\n\t" \
  "ds_read_b128 v[184:187], v213 offset:256\n\t" \
  "s_add_u32 m0, %[lw], 2048\n\t" \
  "v_mfma_f32_16x16x32_bf16 v[148:151], v[188:191], v[8:11], v[148:151]\n\t" \
  "v_mfma_f32_16x16x32_bf16 v[156:159], v[188:191], v[12:15], v[156:159]\n\t" \
  "ds_read_b128 v[188:191], v213 offset:16640\n\t" \
  "global_load_lds_dwordx4 v218, s[92:93] sc1\n\t" \
  "ds_write_b128 v220, v[136:139] offset:0\n\t" \
  "ds_write_b128 v220, v[140:143] offset:1024\n\t" \
  "s_waitcnt lgkmcnt(8)\n\t" \
  "v_mfma_f32_16x16x32_bf16 v[144:147], v[192:195], v[16:19], v[144:147]\n\t" \
  "v_mfma_f32_16x16x32_bf16 v[152:155], v[192:195], v[20:23], v[152:155]\n\t" \
  "ds_read_b128 v[192:195], v214 offset:256\n\t" \
  "v_mfma_f32_16x16x32_bf16 v[148:151], v[196:199], v[16:19], v[148:151]\n\t" \
  "v_mfma_f32_16x16x32_bf16 v[156:159], v[196:199], v[20:23], v[156:159]\n\t" \
  "ds_read_b128 v[196:199], v214 offset:16640\n\t" \
  "s_lshr_b32 s90, s87, 7\n\t" \
  "s_xor_b32 s90, s90, %[s0]\n\t" \
  "s_and_b32 s90, s90, 1\n\t" \
  "s_lshl1_add_u32 s90, s87, s90\n\t" \
  "s_lshl_b32 s90, s90, 13\n\t" \
  "s_cmp_lt_i32 s86, 6\n\t" \
  "s_cselect_b32 s90, 0x70000000, s90\n\t" \
  "s_waitcnt lgkmcnt(8)\n\t" \
  "v_mfma_f32_16x16x32_bf16 v[144:147], v[200:203], v[24:27], v[144:147]\n\t" \
  "v_mfma_f32_16x16x32_bf16 v[152:155], v[200:203], v[28:31], v[152:155]\n\t" \
  "ds_read_b128 v[200:203], v215 offset:256\n\t" \
  "s_add_u32 m0, %[lw], 3072\n\t" \
  "v_mfma_f32_16x16x32_bf16 v[148:151], v[204:207], v[24:27], v[148:151]\n\t" \
  "v_mfma_f32_16x16x32_bf16 v[156:159], v[204:207], v[28:31], v[156:159]\n\t" \
  "ds_read_b128 v[204:207], v215 offset:16640\n\t" \
  "global_load_lds_dwordx4 v219, s[92:93] sc1\n\t" \
  "s_waitcnt lgkmcnt(4)\n\t" \
  "s_cmp_lt_u32 s86, 6\n\t" \
  "s_cbranch_scc1 .Lw21_%=\n\t" \
  "s_waitcnt vmcnt(4)\n\t" \
  "s_barrier\n\t" \
  ".Lg21_%=:\n\t" \
  "s_add_u32 s92, s92, 512\n\t" \
  "s_addc_u32 s93, s93, 0\n\t" \
  "v_mfma_f32_16x16x32_bf16 v[144:147], v[176:179], v[32:35], v[144:147]\n\t" \
  "v_mfma_f32_16x16x32_bf16 v[152:155], v[176:179], v[36:39], v[152:155]\n\t" \
  "ds_read_b128 v[176:179], v212 offset:32768\n\t" \
  "v_mfma_f32_16x16x32_bf16 v[148:151], v[180:183], v[32:35], v[148:151]\n\t" \
  "v_mfma_f32_16x16x32_bf16 v[156:159], v[180:183], v[36:39], v[156:159]\n\t" \
  "ds_read_b128 v[180:183], v212 offset:49152\n\t" \
  "ds_read_b128 v[160:163], v221 offset:0\n\t" \
  "ds_read_b128 v[164:167], v221 offset:1024\n\t" \
  "s_sub_i32 s91, s86, 2\n\t" \
  "s_max_i32 s88, s88, s91\n\t" \
  "v_mov_b32 v173, s88\n\t" \
  "buffer_store_dword v173, v174, %[rs], 0 offen sc1\n\t" \
  "v_mfma_f32_16x16x32_bf16 v[144:147], v[184:187], v[40:43], v[144:147]\n\t" \
  "v_mfma_f32_16x16x32_bf16 v[152:155], v[184:187], v[44:47], v[152:155]\n\t" \
  "ds_read_b128 v[184:187], v213 offset:32768\n\t" \
  "s_add_u32 m0, %[lw], 32768\n\t" \
  "v_mfma_f32_16x16x32_bf16 v[148:151], v[188:191], v[40:43], v[148:151]\n\t" \
  "v_mfma_f32_16x16x32_bf16 v[156:159], v[188:191], v[44:47], v[156:159]\n\t" \
  "ds_read_b128 v[188:191], v213 offset:49152\n\t" \
  "global_load_lds_dwordx4 v216, s[92:93] sc1\n\t" \
  "s_waitcnt lgkmcnt(8)\n\t" \
  "v_mfma_f32_16x16x32_bf16 v[144:147], v[192:195], v[48:51], v[144:147]\n\t" \
  "v_mfma_f32_16x16x32_bf16 v[152:155], v[192:195], v[52:55], v[152:155]\n\t" \
  "ds_read_b128 v[192:195], v214 offset:32768\n\t" \
  "v_mfma_f32_16x16x32_bf16 v[148:151], v[196:199], v[48:51], v[148:151]\n\t" \
  "v_mfma_f32_16x16x32_bf16 v[156:159], v[196:199], v[52:55], v[156:159]\n\t" \
  "ds_read_b128 v[196:199], v214 offset:49152\n\t" \
  "s_waitcnt lgkmcnt(4)\n\t" \
  "v_add_f32 v160, v160, v128\n\t" \
  "v_add_f32 v161, v161, v129\n\t" \
  "v_add_f32 v162, v162, v130\n\t" \
  "v_add_f32 v163, v163, v131\n\t" \
  "v_add_f32 v164, v164, v132\n\t" \
  "v_add_f32 v165, v165, v133\n\t" \
  "v_add_f32 v166, v166, v134\n\t" \
  "v_add_f32 v167, v167, v135\n\t" \
  "v_mfma_f32_16x16x32_bf16 v[144:147], v[200:203], v[56:59], v[144:147]\n\t" \
  "v_mfma_f32_16x16x32_bf16 v[152:155], v[200:203], v[60:63], v[152:155]\n\t" \
  "ds_read_b128 v[200:203], v215 offset:32768\n\t" \
  "s_add_u32 m0, %[lw], 33792\n\t" \
  "v_mfma_f32_16x16x32_bf16 v[148:151], v[204:207], v[56:59], v[148:151]\n\t" \
  "v_mfma_f32_16x16x32_bf16 v[156:159], v[204:207], v[60:63], v[156:159]\n\t" \
  "ds_read_b128 v[204:207], v215 offset:49152\n\t" \
  "global_load_lds_dwordx4 v217, s[92:93] sc1\n\t" \
  "v_cvt_pk_bf16_f32 v168, v160, v161\n\t" \
  "v_cvt_pk_bf16_f32 v169, v162, v163\n\t" \
  "v_cvt_pk_bf16_f32 v170, v164, v165\n\t" \
  "v_cvt_pk_bf16_f32 v171, v166, v167\n\t" \
  "buffer_store_dwordx4 v[168:171], v172, %[rt], s90 offen sc1\n\t" \
  "v_mfma_f32_16x16x32_bf16 v[144:147], v[176:179], v[64:67], v[144:147]\n\t" \
  "v_mfma_f32_16x16x32_bf16 v[152:155], v[176:179], v[68:71], v[152:155]\n\t" \
  "ds_read_b128 v[176:179], v212 offset:33024\n\t" \
  "v_mfma_f32_16x16x32_bf16 v[148:151], v[180:183], v[64:67], v[148:151]\n\t" \
  "v_mfma_f32_16x16x32_bf16 v[156:159], v[180:183], v[68:71], v[156:159]\n\t" \
  "ds_read_b128 v[180:183], v212 offset:49408\n\t" \
  "s_waitcnt lgkmcnt(6)\n\t" \
  "v_mfma_f32_16x16x32_bf16 v[144:147], v[184:187], v[72:75], v[144:147]\n\t" \
  "v_mfma_f32_16x16x32_bf16 v[152:155], v[184:187], v[76:79], v[152:155]\n\t" \
  "ds_read_b128 v[184:187], v213 offset:33024\n\t" \
  "s_add_u32 m0, %[lw], 34816\n\t" \
  "v_mfma_f32_16x16x32_bf16 v[148:151], v[188:191], v[72:75], v[148:151]\n\t" \
  "v_mfma_f32_16x16x32_bf16 v[156:159], v[188:191], v[76:79], v[156:159]\n\t" \
  "ds_read_b128 v[188:191], v213 offset:49408\n\t" \
  "global_load_lds_dwordx4 v218, s[92:93] sc1\n\t" \
  "s_waitcnt lgkmcnt(6)\n\t" \
  "v_mfma_f32_16x16x32_bf16 v[144:147], v[192:195], v[80:83], v[144:147]\n\t" \
  "v_mfma_f32_16x16x32_bf16 v[152:155], v[192:195], v[84:87], v[152:155]\n\t" \
  "ds_read_b128 v[192:195], v214 offset:33024\n\t" \
  "v_mfma_f32_16x16x32_bf16 v[148:151], v[196:199], v[80:83], v[148:151]\n\t" \
  "v_mfma_f32_16x16x32_bf16 v[156:159], v[196:199], v[84:87], v[156:159]\n\t" \
  "ds_read_b128 v[196:199], v214 offset:49408\n\t" \
  "s_waitcnt lgkmcnt(6)\n\t" \
  "v_mfma_f32_16x16x32_bf16 v[144:147], v[200:203], v[88:91], v[144:147]\n\t" \
  "v_mfma_f32_16x16x32_bf16 v[152:155], v[200:203], v[92:95], v[152:155]\n\t" \
  "ds_read_b128 v[200:203], v215 offset:33024\n\t" \
  "s_add_u32 m0, %[lw], 35840\n\t" \
  "v_mfma_f32_16x16x32_bf16 v[148:151], v[204:207], v[88:91], v[148:151]\n\t" \
  "v_mfma_f32_16x16x32_bf16 v[156:159], v[204:207], v[92:95], v[156:159]\n\t" \
  "ds_read_b128 v[204:207], v215 offset:49408\n\t" \
  "global_load_lds_dwordx4 v219, s[92:93] sc1\n\t" \
  "s_cmp_lt_u32 s86, 8\n\t" \
  "s_cbranch_scc1 .Lw31_%=\n\t" \
  "s_waitcnt vmcnt(6)\n\t" \
  "s_barrier\n\t" \
  ".Lg31_%=:\n\t" \
  "s_add_u32 s92, s92, 65024\n\t" \
  "s_addc_u32 s93, s93, 0\n\t" \
  "s_waitcnt lgkmcnt(6)\n\t" \
  "v_mfma_f32_16x16x32_bf16 v[144:147], v[176:179], v[96:99], v[144:147]\n\t" \
  "v_mfma_f32_16x16x32_bf16 v[152:155], v[176:179], v[100:103], v[152:155]\n\t" \
  "ds_read_b128 v[176:179], v208 offset:0\n\t" \
  "v_mfma_f32_16x16x32_bf16 v[148:151], v[180:183], v[96:99], v[148:151]\n\t" \
  "v_mfma_f32_16x16x32_bf16 v[156:159], v[180:183], v[100:103], v[156:159]\n\t" \
  "ds_read_b128 v[180:183], v208 offset:16384\n\t" \
  "s_waitcnt lgkmcnt(6)\n\t" \
  "v_mfma_f32_16x16x32_bf16 v[144:147], v[184:187], v[104:107], v[144:147]\n\t" \
  "v_mfma_f32_16x16x32_bf16 v[152:155], v[184:187], v[108:111], v[152:155]\n\t" \
  "ds_read_b128 v[184:187], v209 offset:0\n\t" \
  "s_add_u32 m0, %[lw], 65536\n\t" \
  "v_mfma_f32_16x16x32_bf16 v[148:151], v[188:191], v[104:107], v[148:151]\n\t" \
  "v_mfma_f32_16x16x32_bf16 v[156:159], v[188:191], v[108:111], v[156:159]\n\t" \
  "ds_read_b128 v[188:191], v209 offset:16384\n\t" \
  "global_load_lds_dwordx4 v216, s[92:93] sc1\n\t" \
  "s_waitcnt lgkmcnt(6)\n\t" \
  "v_mfma_f32_16x16x32_bf16 v[144:147], v[192:195], v[112:115], v[144:147]\n\t" \
  "v_mfma_f32_16x16x32_bf16 v[152:155], v[192:195], v[116:119], v[152:155]\n\t" \
  "ds_read_b128 v[192:195], v210 offset:0\n\t" \
  "v_mfma_f32_16x16x32_bf16 v[148:151], v[196:199], v[112:115], v[148:151]\n\t" \
  "v_mfma_f32_16x16x32_bf16 v[156:159], v[196:199], v[116:119], v[156:159]\n\t" \
  "ds_read_b128 v[196:199], v210 offset:16384\n\t" \
  "s_waitcnt lgkmcnt(6)\n\t" \
  "v_mfma_f32_16x16x32_bf16 v[144:147], v[200:203], v[120:123], v[144:147]\n\t" \
  "v_mfma_f32_16x16x32_bf16 v[152:155], v[200:203], v[124:127], v[152:155]\n\t" \
  "ds_read_b128 v[200:203], v211 offset:0\n\t" \
  "s_add_u32 m0, %[lw], 66560\n\t" \
  "v_mfma_f32_16x16x32_bf16 v[148:151], v[204:207], v[120:123], v[148:151]\n\t" \
  "v_mfma_f32_16x16x32_bf16 v[156:159], v[204:207], v[124:127], v[156:159]\n\t" \
  "ds_read_b128 v[204:207], v211 offset:16384\n\t" \
  "global_load_lds_dwordx4 v217, s[92:93] sc1\n\t" \
  "s_add_u32 s86, s86, 1\n\t" \
  "s_add_u32 s87, s87, 1\n\t" \
  "s_cmp_lt_u32 s86, %[end]\n\t" \
  "s_cbranch_scc1 .Lp1_%=\n\t" \
  "s_branch .Lx1_%=\n\t" \
  ".Lf01_%=:\n\t" \
  "s_nop 7\n\t" \
  "ds_write_b128 v220, v[152:155] offset:0\n\t" \
  "ds_write_b128 v220, v[156:159] offset:1024\n\t" \
  "s_lshr_b32 s90, s87, 7\n\t" \
  "s_xor_b32 s90, s90, %[s0]\n\t" \
  "s_and_b32 s90, s90, 1\n\t" \
  "s_lshl1_add_u32 s90, s87, s90\n\t" \
  "s_lshl_b32 s90, s90, 13\n\t" \
  "s_cmp_lt_i32 s86, 1\n\t" \
  "s_cselect_b32 s90, 0x70000000, s90\n\t" \
  "s_waitcnt lgkmcnt(0)\n\t" \
  "s_barrier\n\t" \
  "ds_read_b128 v[160:163], v221 offset:0\n\t" \
  "ds_read_b128 v[164:167], v221 offset:1024\n\t" \
  "s_waitcnt lgkmcnt(0)\n\t" \
  "v_add_f32 v160, v160, v144\n\t" \
  "v_add_f32 v161, v161, v145\n\t" \
  "v_add_f32 v162, v162, v146\n\t" \
  "v_add_f32 v163, v163, v147\n\t" \
  "v_add_f32 v164, v164, v148\n\t" \
  "v_add_f32 v165, v165, v149\n\t" \
  "v_add_f32 v166, v166, v150\n\t" \
  "v_add_f32 v167, v167, v151\n\t" \
  "v_cvt_pk_bf16_f32 v168, v160, v161\n\t" \
  "v_cvt_pk_bf16_f32 v169, v162, v163\n\t" \
  "v_cvt_pk_bf16_f32 v170, v164, v165\n\t" \
  "v_cvt_pk_bf16_f32 v171, v166, v167\n\t" \
  "buffer_store_dwordx4 v[168:171], v172, %[rt], s90 offen sc1\n\t" \
  "s_branch .Lfb01_%=\n\t" \
  ".Lf11_%=:\n\t" \
  "s_nop 7\n\t" \
  "ds_write_b128 v220, v[136:139] offset:0\n\t" \
  "ds_write_b128 v220, v[140:143] offset:1024\n\t" \
  "s_lshr_b32 s90, s87, 7\n\t" \
  "s_xor_b32 s90, s90, %[s0]\n\t" \
  "s_and_b32 s90, s90, 1\n\t" \
  "s_lshl1_add_u32 s90, s87, s90\n\t" \
  "s_lshl_b32 s90, s90, 13\n\t" \
  "s_cmp_lt_i32 s86, 1\n\t" \
  "s_cselect_b32 s90, 0x70000000, s90\n\t" \
  "s_waitcnt lgkmcnt(0)\n\t" \
  "s_barrier\n\t" \
  "ds_read_b128 v[160:163], v221 offset:0\n\t" \
  "ds_read_b128 v[164:167], v221 offset:1024\n\t" \
  "s_waitcnt lgkmcnt(0)\n\t" \
  "v_add_f32 v160, v160, v128\n\t" \
  "v_add_f32 v161, v161, v129\n\t" \
  "v_add_f32 v162, v162, v130\n\t" \
  "v_add_f32 v163, v163, v131\n\t" \
  "v_add_f32 v164, v164, v132\n\t" \
  "v_add_f32 v165, v165, v133\n\t" \
  "v_add_f32 v166, v166, v134\n\t" \
  "v_add_f32 v167, v167, v135\n\t" \
  "v_cvt_pk_bf16_f32 v168, v160, v161\n\t" \
  "v_cvt_pk_bf16_f32 v169, v162, v163\n\t" \
  "v_cvt_pk_bf16_f32 v170, v164, v165\n\t" \
  "v_cvt_pk_bf16_f32 v171, v166, v167\n\t" \
  "buffer_store_dwordx4 v[168:171], v172, %[rt], s90 offen sc1\n\t" \
  "s_branch .Lfb11_%=\n\t" \
  ".Lw01_%=:\n\t" \
  "s_waitcnt vmcnt(2)\n\t" \
  "s_barrier\n\t" \
  "s_sub_i32 s91, s86, 1\n\t" \
  "s_max_i32 s88, s88, s91\n\t" \
  "v_mov_b32 v173, s88\n\t" \
  "buffer_store_dword v173, v174, %[rs], 0 offen sc1\n\t" \
  "s_branch .Lg01_%=\n\t" \
  ".Lw11_%=:\n\t" \
  "s_waitcnt vmcnt(2)\n\t" \
  "s_barrier\n\t" \
  "s_sub_i32 s91, s86, 1\n\t" \
  "s_max_i32 s88, s88, s91\n\t" \
  "v_mov_b32 v173, s88\n\t" \
  "buffer_store_dword v173, v174, %[rs], 0 offen sc1\n\t" \
  "s_branch .Lg11_%=\n\t" \
  ".Lw21_%=:\n\t" \
  "s_waitcnt vmcnt(2)\n\t" \
  "s_barrier\n\t" \
  "s_sub_i32 s91, s86, 1\n\t" \
  "s_max_i32 s88, s88, s91\n\t" \
  "v_mov_b32 v173, s88\n\t" \
  "buffer_store_dword v173, v174, %[rs], 0 offen sc1\n\t" \
  "s_branch .Lg21_%=\n\t" \
  ".Lw31_%=:\n\t" \
  "s_waitcnt vmcnt(2)\n\t" \
  "s_barrier\n\t" \
  "s_sub_i32 s91, s86, 1\n\t" \
  "s_max_i32 s88, s88, s91\n\t" \
  "v_mov_b32 v173, s88\n\t" \
  "buffer_store_dword v173, v174, %[rs], 0 offen sc1\n\t" \
  "s_branch .Lg31_%=\n\t" \
  ".Lx1_%=:\n\t" \
  "s_waitcnt lgkmcnt(0)\n\t" \
  "s_setprio 0\n\t" \
  "s_mov_b32 m0, s94\n\t" \
  ".Le_%=:\n\t"
#define PP_ASM_0 \
  "s_cmp_lg_u32 %[kh], 0\n\t" \
  "s_cbranch_scc1 .Lk_%=\n\t" \
  "s_mov_b32 s94, m0\n\t" \
  "s_mov_b64 s[92:93], %[x0]\n\t" \
  "s_mov_b32 s86, %[g0]\n\t" \
  "s_add_u32 s87, s86, %[t0]\n\t" \
  "s_add_u32 m0, %[lw], 67584\n\t" \
  "s_nop 0\n\t" \
  "global_load_lds_dwordx4 v250, s[92:93] sc1\n\t" \
  "s_add_u32 m0, %[lw], 68608\n\t" \
  "s_nop 0\n\t" \
  "global_load_lds_dwordx4 v251, s[92:93] sc1\n\t" \
  "s_barrier\n\t" \
  "s_branch .Ls0_%=\n\t" \
  ".Lp0_%=:\n\t" \
  "s_waitcnt vmcnt(9)\n\t" \
  "s_barrier\n\t" \
  ".Ls0_%=:\n\t" \
  "s_add_u32 s92, s92, 512\n\t" \
  "s_addc_u32 s93, s93, 0\n\t" \
  "ds_read_b128 v[176:179], v240 offset:0\n\t" \
  "ds_read_b128 v[180:183], v240 offset:16384\n\t" \
  "ds_read_b128 v[184:187], v241 offset:0\n\t" \
  "ds_read_b128 v[188:191], v241 offset:16384\n\t" \
  "s_add_u32 m0, %[lw], 98304\n\t" \
  "s_nop 0\n\t" \
  "global_load_lds_dwordx4 v248, s[92:93] sc1\n\t" \
  "ds_read_b128 v[192:195], v242 offset:0\n\t" \
  "ds_read_b128 v[196:199], v242 offset:16384\n\t" \
  "ds_read_b128 v[200:203], v243 offset:0\n\t" \
  "ds_read_b128 v[204:207], v243 offset:16384\n\t" \
  "s_add_u32 m0, %[lw], 99328\n\t" \
  "s_nop 0\n\t" \
  "global_load_lds_dwordx4 v249, s[92:93] sc1\n\t" \
  "ds_read_b128 v[208:211], v240 offset:256\n\t" \
  "ds_read_b128 v[212:215], v240 offset:16640\n\t" \
  "ds_read_b128 v[216:219], v241 offset:256\n\t" \
  "ds_read_b128 v[220:223], v241 offset:16640\n\t" \
  "s_add_u32 m0, %[lw], 100352\n\t" \
  "s_nop 0\n\t" \
  "global_load_lds_dwordx4 v250, s[92:93] sc1\n\t" \
  "ds_read_b128 v[224:227], v242 offset:256\n\t" \
  "ds_read_b128 v[228:231], v242 offset:16640\n\t" \
  "ds_read_b128 v[232:235], v243 offset:256\n\t" \
  "ds_read_b128 v[236:239], v243 offset:16640\n\t" \
  "s_add_u32 m0, %[lw], 101376\n\t" \
  "s_nop 0\n\t" \
  "global_load_lds_dwordx4 v251, s[92:93] sc1\n\t" \
  "ds_write_b128 v252, v[152:155] offset:0\n\t" \
  "ds_write_b128 v252, v[156:159] offset:1024\n\t" \
  "s_waitcnt lgkmcnt(0)\n\t" \
  "s_setprio 1\n\t" \
  "v_mfma_f32_16x16x32_bf16 v[128:131], v[0:3], v[176:179], 0\n\t" \
  "v_mfma_f32_16x16x32_bf16 v[132:135], v[4:7], v[176:179], 0\n\t" \
  "v_mfma_f32_16x16x32_bf16 v[136:139], v[0:3], v[180:183], 0\n\t" \
  "v_mfma_f32_16x16x32_bf16 v[140:143], v[4:7], v[180:183], 0\n\t" \
  "v_mfma_f32_16x16x32_bf16 v[128:131], v[8:11], v[184:187], v[128:131]\n\t" \
  "v_mfma_f32_16x16x32_bf16 v[132:135], v[12:15], v[184:187], v[132:135]\n\t" \
  "v_mfma_f32_16x16x32_bf16 v[136:139], v[8:11], v[188:191], v[136:139]\n\t" \
  "v_mfma_f32_16x16x32_bf16 v[140:143], v[12:15], v[188:191], v[140:143]\n\t" \
  "v_mfma_f32_16x16x32_bf16 v[128:131], v[16:19], v[192:195], v[128:131]\n\t" \
  "v_mfma_f32_16x16x32_bf16 v[132:135], v[20:23], v[192:195], v[132:135]\n\t" \
  "v_mfma_f32_16x16x32_bf16 v[136:139], v[16:19], v[196:199], v[136:139]\n\t" \
  "v_mfma_f32_16x16x32_bf16 v[140:143], v[20:23], v[196:199], v[140:143]\n\t" \
  "v_mfma_f32_16x16x32_bf16 v[128:131], v[24:27], v[200:203], v[128:131]\n\t" \
  "v_mfma_f32_16x16x32_bf16 v[132:135], v[28:31], v[200:203], v[132:135]\n\t" \
  "v_mfma_f32_16x16x32_bf16 v[136:139], v[24:27], v[204:207], v[136:139]\n\t" \
  "v_mfma_f32_16x16x32_bf16 v[140:143], v[28:31], v[204:207], v[140:143]\n\t" \
  "v_mfma_f32_16x16x32_bf16 v[128:131], v[32:35], v[208:211], v[128:131]\n\t" \
  "v_mfma_f32_16x16x32_bf16 v[132:135], v[36:39], v[208:211], v[132:135]\n\t" \
  "v_mfma_f32_16x16x32_bf16 v[136:139], v[32:35], v[212:215], v[136:139]\n\t" \
  "v_mfma_f32_16x16x32_bf16 v[140:143], v[36:39], v[212:215], v[140:143]\n\t" \
  "v_mfma_f32_16x16x32_bf16 v[128:131], v[40:43], v[216:219], v[128:131]\n\t" \
  "v_mfma_f32_16x16x32_bf16 v[132:135], v[44:47], v[216:219], v[132:135]\n\t" \
  "v_mfma_f32_16x16x32_bf16 v[136:139], v[40:43], v[220:223], v[136:139]\n\t" \
  "v_mfma_f32_16x16x32_bf16 v[140:143], v[44:47], v[220:223], v[140:143]\n\t" \
  "v_mfma_f32_16x16x32_bf16 v[128:131], v[48:51], v[224:227], v[128:131]\n\t" \
  "v_mfma_f32_16x16x32_bf16 v[132:135], v[52:55], v[224:227], v[132:135]\n\t" \
  "v_mfma_f32_16x16x32_bf16 v[136:139], v[48:51], v[228:231], v[136:139]\n\t" \
  "v_mfma_f32_16x16x32_bf16 v[140:143], v[52:55], v[228:231], v[140:143]\n\t" \
  "v_mfma_f32_16x16x32_bf16 v[128:131], v[56:59], v[232:235], v[128:131]\n\t" \
  "v_mfma_f32_16x16x32_bf16 v[132:135], v[60:63], v[232:235], v[132:135]\n\t" \
  "v_mfma_f32_16x16x32_bf16 v[136:139], v[56:59], v[236:239], v[136:139]\n\t" \
  "v_mfma_f32_16x16x32_bf16 v[140:143], v[60:63], v[236:239], v[140:143]\n\t" \
  "s_setprio 0\n\t" \
  "s_waitcnt vmcnt(4)\n\t" \
  "s_barrier\n\t" \
  "s_add_u32 s92, s92, 65024\n\t" \
  "s_addc_u32 s93, s93, 0\n\t" \
  "ds_read_b128 v[160:163], v253 offset:0\n\t" \
  "ds_read_b128 v[164:167], v253 offset:1024\n\t" \
  "ds_read_b128 v[176:179], v240 offset:32768\n\t" \
  "ds_read_b128 v[180:183], v240 offset:49152\n\t" \
  "ds_read_b128 v[184:187], v241 offset:32768\n\t" \
  "ds_read_b128 v[188:191], v241 offset:49152\n\t" \
  "s_add_u32 m0, %[lw], 0\n\t" \
  "s_nop 0\n\t" \
  "global_load_lds_dwordx4 v248, s[92:93] sc1\n\t" \
  "ds_read_b128 v[192:195], v242 offset:32768\n\t" \
  "ds_read_b128 v[196:199], v242 offset:49152\n\t" \
  "ds_read_b128 v[200:203], v243 offset:32768\n\t" \
  "ds_read_b128 v[204:207], v243 offset:49152\n\t" \
  "s_add_u32 m0, %[lw], 1024\n\t" \
  "s_nop 0\n\t" \
  "global_load_lds_dwordx4 v249, s[92:93] sc1\n\t" \
  "ds_read_b128 v[208:211], v240 offset:33024\n\t" \
  "ds_read_b128 v[212:215], v240 offset:49408\n\t" \
  "ds_read_b128 v[216:219], v241 offset:33024\n\t" \
  "ds_read_b128 v[220:223], v241 offset:49408\n\t" \
  "s_add_u32 m0, %[lw], 2048\n\t" \
  "s_nop 0\n\t" \
  "global_load_lds_dwordx4 v250, s[92:93] sc1\n\t" \
  "ds_read_b128 v[224:227], v242 offset:33024\n\t" \
  "ds_read_b128 v[228:231], v242 offset:49408\n\t" \
  "ds_read_b128 v[232:235], v243 offset:33024\n\t" \
  "ds_read_b128 v[236:239], v243 offset:49408\n\t" \
  "s_add_u32 m0, %[lw], 3072\n\t" \
  "s_nop 0\n\t" \
  "global_load_lds_dwordx4 v251, s[92:93] sc1\n\t" \
  "s_waitcnt lgkmcnt(15)\n\t" \
  "s_waitcnt lgkmcnt(12)\n\t" \
  "s_lshr_b32 s90, s87, 7\n\t" \
  "s_xor_b32 s90, s90, %[s0]\n\t" \
  "s_and_b32 s90, s90, 1\n\t" \
  "s_lshl1_add_u32 s90, s87, s90\n\t" \
  "s_lshl_b32 s90, s90, 13\n\t" \
  "v_add_f32 v160, v160, v144\n\t" \
  "v_add_f32 v161, v161, v145\n\t" \
  "v_add_f32 v162, v162, v146\n\t" \
  "v_add_f32 v163, v163, v147\n\t" \
  "v_add_f32 v164, v164, v148\n\t" \
  "v_add_f32 v165, v165, v149\n\t" \
  "v_add_f32 v166, v166, v150\n\t" \
  "v_add_f32 v167, v167, v151\n\t" \
  "v_cvt_pk_bf16_f32 v168, v160, v161\n\t" \
  "v_cvt_pk_bf16_f32 v169, v162, v163\n\t" \
  "v_cvt_pk_bf16_f32 v170, v164, v165\n\t" \
  "v_cvt_pk_bf16_f32 v171, v166, v167\n\t" \
  "buffer_store_dwordx4 v[168:171], v172, %[rt], s90 offen sc1\n\t" \
  "s_add_u32 s86, s86, 1\n\t" \
  "s_add_u32 s87, s87, 1\n\t" \
  "s_waitcnt lgkmcnt(0)\n\t" \
  "s_setprio 1\n\t" \
  "v_mfma_f32_16x16x32_bf16 v[128:131], v[64:67], v[176:179], v[128:131]\n\t" \
  "v_mfma_f32_16x16x32_bf16 v[132:135], v[68:71], v[176:179], v[132:135]\n\t" \
  "v_mfma_f32_16x16x32_bf16 v[136:139], v[64:67], v[180:183], v[136:139]\n\t" \
  "v_mfma_f32_16x16x32_bf16 v[140:143], v[68:71], v[180:183], v[140:143]\n\t" \
  "v_mfma_f32_16x16x32_bf16 v[128:131], v[72:75], v[184:187], v[128:131]\n\t" \
  "v_mfma_f32_16x16x32_bf16 v[132:135], v[76:79], v[184:187], v[132:135]\n\t" \
  "v_mfma_f32_16x16x32_bf16 v[136:139], v[72:75], v[188:191], v[136:139]\n\t" \
  "v_mfma_f32_16x16x32_bf16 v[140:143], v[76:79], v[188:191], v[140:143]\n\t" \
  "v_mfma_f32_16x16x32_bf16 v[128:131], v[80:83], v[192:195], v[128:131]\n\t" \
  "v_mfma_f32_16x16x32_bf16 v[132:135], v[84:87], v[192:195], v[132:135]\n\t" \
  "v_mfma_f32_16x16x32_bf16 v[136:139], v[80:83], v[196:199], v[136:139]\n\t" \
  "v_mfma_f32_16x16x32_bf16 v[140:143], v[84:87], v[196:199], v[140:143]\n\t" \
  "v_mfma_f32_16x16x32_bf16 v[128:131], v[88:91], v[200:203], v[128:131]\n\t" \
  "v_mfma_f32_16x16x32_bf16 v[132:135], v[92:95], v[200:203], v[132:135]\n\t" \
  "v_mfma_f32_16x16x32_bf16 v[136:139], v[88:91], v[204:207], v[136:139]\n\t" \
  "v_mfma_f32_16x16x32_bf16 v[140:143], v[92:95], v[204:207], v[140:143]\n\t" \
  "v_mfma_f32_16x16x32_bf16 v[128:131], v[96:99], v[208:211], v[128:131]\n\t" \
  "v_mfma_f32_16x16x32_bf16 v[132:135], v[100:103], v[208:211], v[132:135]\n\t" \
  "v_mfma_f32_16x16x32_bf16 v[136:139], v[96:99], v[212:215], v[136:139]\n\t" \
  "v_mfma_f32_16x16x32_bf16 v[140:143], v[100:103], v[212:215], v[140:143]\n\t" \
  "v_mfma_f32_16x16x32_bf16 v[128:131], v[104:107], v[216:219], v[128:131]\n\t" \
  "v_mfma_f32_16x16x32_bf16 v[132:135], v[108:111], v[216:219], v[132:135]\n\t" \
  "v_mfma_f32_16x16x32_bf16 v[136:139], v[104:107], v[220:223], v[136:139]\n\t" \
  "v_mfma_f32_16x16x32_bf16 v[140:143], v[108:111], v[220:223], v[140:143]\n\t" \
  "v_mfma_f32_16x16x32_bf16 v[128:131], v[112:115], v[224:227], v[128:131]\n\t" \
  "v_mfma_f32_16x16x32_bf16 v[132:135], v[116:119], v[224:227], v[132:135]\n\t" \
  "v_mfma_f32_16x16x32_bf16 v[136:139], v[112:115], v[228:231], v[136:139]\n\t" \
  "v_mfma_f32_16x16x32_bf16 v[140:143], v[116:119], v[228:231], v[140:143]\n\t" \
  "v_mfma_f32_16x16x32_bf16 v[128:131], v[120:123], v[232:235], v[128:131]\n\t" \
  "v_mfma_f32_16x16x32_bf16 v[132:135], v[124:127], v[232:235], v[132:135]\n\t" \
  "v_mfma_f32_16x16x32_bf16 v[136:139], v[120:123], v[236:239], v[136:139]\n\t" \
  "v_mfma_f32_16x16x32_bf16 v[140:143], v[124:127], v[236:239], v[140:143]\n\t" \
  "s_setprio 0\n\t" \
  "s_waitcnt vmcnt(9)\n\t" \
  "s_barrier\n\t" \
  "s_add_u32 s92, s92, 512\n\t" \
  "s_addc_u32 s93, s93, 0\n\t" \
  "ds_read_b128 v[176:179], v244 offset:0\n\t" \
  "ds_read_b128 v[180:183], v244 offset:16384\n\t" \
  "ds_read_b128 v[184:187], v245 offset:0\n\t" \
  "ds_read_b128 v[188:191], v245 offset:16384\n\t" \
  "s_add_u32 m0, %[lw], 32768\n\t" \
  "s_nop 0\n\t" \
  "global_load_lds_dwordx4 v248, s[92:93] sc1\n\t" \
  "ds_read_b128 v[192:195], v246 offset:0\n\t" \
  "ds_read_b128 v[196:199], v246 offset:16384\n\t" \
  "ds_read_b128 v[200:203], v247 offset:0\n\t" \
  "ds_read_b128 v[204:207], v247 offset:16384\n\t" \
  "s_add_u32 m0, %[lw], 33792\n\t" \
  "s_nop 0\n\t" \
  "global_load_lds_dwordx4 v249, s[92:93] sc1\n\t" \
  "ds_read_b128 v[208:211], v244 offset:256\n\t" \
  "ds_read_b128 v[212:215], v244 offset:16640\n\t" \
  "ds_read_b128 v[216:219], v245 offset:256\n\t" \
  "ds_read_b128 v[220:223], v245 offset:16640\n\t" \
  "s_add_u32 m0, %[lw], 34816\n\t" \
  "s_nop 0\n\t" \
  "global_load_lds_dwordx4 v250, s[92:93] sc1\n\t" \
  "ds_read_b128 v[224:227], v246 offset:256\n\t" \
  "ds_read_b128 v[228:231], v246 offset:16640\n\t" \
  "ds_read_b128 v[232:235], v247 offset:256\n\t" \
  "ds_read_b128 v[236:239], v247 offset:16640\n\t" \
  "s_add_u32 m0, %[lw], 35840\n\t" \
  "s_nop 0\n\t" \
  "global_load_lds_dwordx4 v251, s[92:93] sc1\n\t" \
  "ds_write_b128 v252, v[136:139] offset:0\n\t" \
  "ds_write_b128 v252, v[140:143] offset:1024\n\t" \
  "s_waitcnt lgkmcnt(0)\n\t" \
  "s_setprio 1\n\t" \
  "v_mfma_f32_16x16x32_bf16 v[144:147], v[0:3], v[176:179], 0\n\t" \
  "v_mfma_f32_16x16x32_bf16 v[148:151], v[4:7], v[176:179], 0\n\t" \
  "v_mfma_f32_16x16x32_bf16 v[152:155], v[0:3], v[180:183], 0\n\t" \
  "v_mfma_f32_16x16x32_bf16 v[156:159], v[4:7], v[180:183], 0\n\t" \
  "v_mfma_f32_16x16x32_bf16 v[144:147], v[8:11], v[184:187], v[144:147]\n\t" \
  "v_mfma_f32_16x16x32_bf16 v[148:151], v[12:15], v[184:187], v[148:151]\n\t" \
  "v_mfma_f32_16x16x32_bf16 v[152:155], v[8:11], v[188:191], v[152:155]\n\t" \
  "v_mfma_f32_16x16x32_bf16 v[156:159], v[12:15], v[188:191], v[156:159]\n\t" \
  "v_mfma_f32_16x16x32_bf16 v[144:147], v[16:19], v[192:195], v[144:147]\n\t" \
  "v_mfma_f32_16x16x32_bf16 v[148:151], v[20:23], v[192:195], v[148:151]\n\t" \
  "v_mfma_f32_16x16x32_bf16 v[152:155], v[16:19], v[196:199], v[152:155]\n\t" \
  "v_mfma_f32_16x16x32_bf16 v[156:159], v[20:23], v[196:199], v[156:159]\n\t" \
  "v_mfma_f32_16x16x32_bf16 v[144:147], v[24:27], v[200:203], v[144:147]\n\t" \
  "v_mfma_f32_16x16x32_bf16 v[148:151], v[28:31], v[200:203], v[148:151]\n\t" \
  "v_mfma_f32_16x16x32_bf16 v[152:155], v[24:27], v[204:207], v[152:155]\n\t" \
  "v_mfma_f32_16x16x32_bf16 v[156:159], v[28:31], v[204:207], v[156:159]\n\t" \
  "v_mfma_f32_16x16x32_bf16 v[144:147], v[32:35], v[208:211], v[144:147]\n\t" \
  "v_mfma_f32_16x16x32_bf16 v[148:151], v[36:39], v[208:211], v[148:151]\n\t" \
  "v_mfma_f32_16x16x32_bf16 v[152:155], v[32:35], v[212:215], v[152:155]\n\t" \
  "v_mfma_f32_16x16x32_bf16 v[156:159], v[36:39], v[212:215], v[156:159]\n\t" \
  "v_mfma_f32_16x16x32_bf16 v[144:147], v[40:43], v[216:219], v[144:147]\n\t" \
  "v_mfma_f32_16x16x32_bf16 v[148:151], v[44:47], v[216:219], v[148:151]\n\t" \
  "v_mfma_f32_16x16x32_bf16 v[152:155], v[40:43], v[220:223], v[152:155]\n\t" \
  "v_mfma_f32_16x16x32_bf16 v[156:159], v[44:47], v[220:223], v[156:159]\n\t" \
  "v_mfma_f32_16x16x32_bf16 v[144:147], v[48:51], v[224:227], v[144:147]\n\t" \
  "v_mfma_f32_16x16x32_bf16 v[148:151], v[52:55], v[224:227], v[148:151]\n\t" \
  "v_mfma_f32_16x16x32_bf16 v[152:155], v[48:51], v[228:231], v[152:155]\n\t" \
  "v_mfma_f32_16x16x32_bf16 v[156:159], v[52:55], v[228:231], v[156:159]\n\t" \
  "v_mfma_f32_16x16x32_bf16 v[144:147], v[56:59], v[232:235], v[144:147]\n\t" \
  "v_mfma_f32_16x16x32_bf16 v[148:151], v[60:63], v[232:235], v[148:151]\n\t" \
  "v_mfma_f32_16x16x32_bf16 v[152:155], v[56:59], v[236:239], v[152:155]\n\t" \
  "v_mfma_f32_16x16x32_bf16 v[156:159], v[60:63], v[236:239], v[156:159]\n\t" \
  "s_setprio 0\n\t" \
  "s_waitcnt vmcnt(4)\n\t" \
  "s_barrier\n\t" \
  "s_add_u32 s92, s92, 65024\n\t" \
  "s_addc_u32 s93, s93, 0\n\t" \
  "ds_read_b128 v[160:163], v253 offset:0\n\t" \
  "ds_read_b128 v[164:167], v253 offset:1024\n\t" \
  "ds_read_b128 v[176:179], v244 offset:32768\n\t" \
  "ds_read_b128 v[180:183], v244 offset:49152\n\t" \
  "ds_read_b128 v[184:187], v245 offset:32768\n\t" \
  "ds_read_b128 v[188:191], v245 offset:49152\n\t" \
  "s_add_u32 m0, %[lw], 65536\n\t" \
  "s_nop 0\n\t" \
  "global_load_lds_dwordx4 v248, s[92:93] sc1\n\t" \
  "ds_read_b128 v[192:195], v246 offset:32768\n\t" \
  "ds_read_b128 v[196:199], v246 offset:49152\n\t" \
  "ds_read_b128 v[200:203], v247 offset:32768\n\t" \
  "ds_read_b128 v[204:207], v247 offset:49152\n\t" \
  "s_add_u32 m0, %[lw], 66560\n\t" \
  "s_nop 0\n\t" \
  "global_load_lds_dwordx4 v249, s[92:93] sc1\n\t" \
  "ds_read_b128 v[208:211], v244 offset:33024\n\t" \
  "ds_read_b128 v[212:215], v244 offset:49408\n\t" \
  "ds_read_b128 v[216:219], v245 offset:33024\n\t" \
  "ds_read_b128 v[220:223], v245 offset:49408\n\t" \
  "s_add_u32 m0, %[lw], 67584\n\t" \
  "s_nop 0\n\t" \
  "global_load_lds_dwordx4 v250, s[92:93] sc1\n\t" \
  "ds_read_b128 v[224:227], v246 offset:33024\n\t" \
  "ds_read_b128 v[228:231], v246 offset:49408\n\t" \
  "ds_read_b128 v[232:235], v247 offset:33024\n\t" \
  "ds_read_b128 v[236:239], v247 offset:49408\n\t" \
  "s_add_u32 m0, %[lw], 68608\n\t" \
  "s_nop 0\n\t" \
  "global_load_lds_dwordx4 v251, s[92:93] sc1\n\t" \
  "s_waitcnt lgkmcnt(15)\n\t" \
  "s_waitcnt lgkmcnt(12)\n\t" \
  "s_lshr_b32 s90, s87, 7\n\t" \
  "s_xor_b32 s90, s90, %[s0]\n\t" \
  "s_and_b32 s90, s90, 1\n\t" \
  "s_lshl1_add_u32 s90, s87, s90\n\t" \
  "s_lshl_b32 s90, s90, 13\n\t" \
  "v_add_f32 v160, v160, v128\n\t" \
  "v_add_f32 v161, v161, v129\n\t" \
  "v_add_f32 v162, v162, v130\n\t" \
  "v_add_f32 v163, v163, v131\n\t" \
  "v_add_f32 v164, v164, v132\n\t" \
  "v_add_f32 v165, v165, v133\n\t" \
  "v_add_f32 v166, v166, v134\n\t" \
  "v_add_f32 v167, v167, v135\n\t" \
  "v_cvt_pk_bf16_f32 v168, v160, v161\n\t" \
  "v_cvt_pk_bf16_f32 v169, v162, v163\n\t" \
  "v_cvt_pk_bf16_f32 v170, v164, v165\n\t" \
  "v_cvt_pk_bf16_f32 v171, v166, v167\n\t" \
  "buffer_store_dwordx4 v[168:171], v172, %[rt], s90 offen sc1\n\t" \
  "s_add_u32 s86, s86, 1\n\t" \
  "s_add_u32 s87, s87, 1\n\t" \
  "s_waitcnt lgkmcnt(0)\n\t" \
  "s_setprio 1\n\t" \
  "v_mfma_f32_16x16x32_bf16 v[144:147], v[64:67], v[176:179], v[144:147]\n\t" \
  "v_mfma_f32_16x16x32_bf16 v[148:151], v[68:71], v[176:179], v[148:151]\n\t" \
  "v_mfma_f32_16x16x32_bf16 v[152:155], v[64:67], v[180:183], v[152:155]\n\t" \
  "v_mfma_f32_16x16x32_bf16 v[156:159], v[68:71], v[180:183], v[156:159]\n\t" \
  "v_mfma_f32_16x16x32_bf16 v[144:147], v[72:75], v[184:187], v[144:147]\n\t" \
  "v_mfma_f32_16x16x32_bf16 v[148:151], v[76:79], v[184:187], v[148:151]\n\t" \
  "v_mfma_f32_16x16x32_bf16 v[152:155], v[72:75], v[188:191], v[152:155]\n\t" \
  "v_mfma_f32_16x16x32_bf16 v[156:159], v[76:79], v[188:191], v[156:159]\n\t" \
  "v_mfma_f32_16x16x32_bf16 v[144:147], v[80:83], v[192:195], v[144:147]\n\t" \
  "v_mfma_f32_16x16x32_bf16 v[148:151], v[84:87], v[192:195], v[148:151]\n\t" \
  "v_mfma_f32_16x16x32_bf16 v[152:155], v[80:83], v[196:199], v[152:155]\n\t" \
  "v_mfma_f32_16x16x32_bf16 v[156:159], v[84:87], v[196:199], v[156:159]\n\t" \
  "v_mfma_f32_16x16x32_bf16 v[144:147], v[88:91], v[200:203], v[144:147]\n\t" \
  "v_mfma_f32_16x16x32_bf16 v[148:151], v[92:95], v[200:203], v[148:151]\n\t" \
  "v_mfma_f32_16x16x32_bf16 v[152:155], v[88:91], v[204:207], v[152:155]\n\t" \
  "v_mfma_f32_16x16x32_bf16 v[156:159], v[92:95], v[204:207], v[156:159]\n\t" \
  "v_mfma_f32_16x16x32_bf16 v[144:147], v[96:99], v[208:211], v[144:147]\n\t" \
  "v_mfma_f32_16x16x32_bf16 v[148:151], v[100:103], v[208:211], v[148:151]\n\t" \
  "v_mfma_f32_16x16x32_bf16 v[152:155], v[96:99], v[212:215], v[152:155]\n\t" \
  "v_mfma_f32_16x16x32_bf16 v[156:159], v[100:103], v[212:215], v[156:159]\n\t" \
  "v_mfma_f32_16x16x32_bf16 v[144:147], v[104:107], v[216:219], v[144:147]\n\t" \
  "v_mfma_f32_16x16x32_bf16 v[148:151], v[108:111], v[216:219], v[148:151]\n\t" \
  "v_mfma_f32_16x16x32_bf16 v[152:155], v[104:107], v[220:223], v[152:155]\n\t" \
  "v_mfma_f32_16x16x32_bf16 v[156:159], v[108:111], v[220:223], v[156:159]\n\t" \
  "v_mfma_f32_16x16x32_bf16 v[144:147], v[112:115], v[224:227], v[144:147]\n\t" \
  "v_mfma_f32_16x16x32_bf16 v[148:151], v[116:119], v[224:227], v[148:151]\n\t" \
  "v_mfma_f32_16x16x32_bf16 v[152:155], v[112:115], v[228:231], v[152:155]\n\t" \
  "v_mfma_f32_16x16x32_bf16 v[156:159], v[116:119], v[228:231], v[156:159]\n\t" \
  "v_mfma_f32_16x16x32_bf16 v[144:147], v[120:123], v[232:235], v[144:147]\n\t" \
  "v_mfma_f32_16x16x32_bf16 v[148:151], v[124:127], v[232:235], v[148:151]\n\t" \
  "v_mfma_f32_16x16x32_bf16 v[152:155], v[120:123], v[236:239], v[152:155]\n\t" \
  "v_mfma_f32_16x16x32_bf16 v[156:159], v[124:127], v[236:239], v[156:159]\n\t" \
  "s_setprio 0\n\t" \
  "s_cmp_lt_u32 s86, 64\n\t" \
  "s_cbranch_scc1 .Lp0_%=\n\t" \
  "s_barrier\n\t" \
  "s_nop 2\n\t" \
  "ds_write_b128 v252, v[152:155] offset:0\n\t" \
  "ds_write_b128 v252, v[156:159] offset:1024\n\t" \
  "s_waitcnt lgkmcnt(0)\n\t" \
  "s_barrier\n\t" \
  "ds_read_b128 v[160:163], v253 offset:0\n\t" \
  "ds_read_b128 v[164:167], v253 offset:1024\n\t" \
  "s_waitcnt lgkmcnt(0)\n\t" \
  "s_lshr_b32 s90, s87, 7\n\t" \
  "s_xor_b32 s90, s90, %[s0]\n\t" \
  "s_and_b32 s90, s90, 1\n\t" \
  "s_lshl1_add_u32 s90, s87, s90\n\t" \
  "s_lshl_b32 s90, s90, 13\n\t" \
  "v_add_f32 v160, v160, v144\n\t" \
  "v_add_f32 v161, v161, v145\n\t" \
  "v_add_f32 v162, v162, v146\n\t" \
  "v_add_f32 v163, v163, v147\n\t" \
  "v_add_f32 v164, v164, v148\n\t" \
  "v_add_f32 v165, v165, v149\n\t" \
  "v_add_f32 v166, v166, v150\n\t" \
  "v_add_f32 v167, v167, v151\n\t" \
  "v_cvt_pk_bf16_f32 v168, v160, v161\n\t" \
  "v_cvt_pk_bf16_f32 v169, v162, v163\n\t" \
  "v_cvt_pk_bf16_f32 v170, v164, v165\n\t" \
  "v_cvt_pk_bf16_f32 v171, v166, v167\n\t" \
  "buffer_store_dwordx4 v[168:171], v172, %[rt], s90 offen sc1\n\t" \
  "s_waitcnt vmcnt(0)\n\t" \
  "s_barrier\n\t" \
  "s_mov_b32 m0, s94\n\t" \
  "s_branch .Le_%=\n\t" \
  ".Lk_%=:\n\t" \
  "s_mov_b32 s94, m0\n\t" \
  "s_mov_b64 s[92:93], %[x0]\n\t" \
  "s_mov_b32 s86, %[g0]\n\t" \
  "s_add_u32 s87, s86, %[t0]\n\t" \
  "s_add_u32 m0, %[lw], 67584\n\t" \
  "s_nop 0\n\t" \
  "global_load_lds_dwordx4 v250, s[92:93] sc1\n\t" \
  "s_add_u32 m0, %[lw], 68608\n\t" \
  "s_nop 0\n\t" \
  "global_load_lds_dwordx4 v251, s[92:93] sc1\n\t" \
  "s_barrier\n\t" \
  "s_branch .Ls1_%=\n\t" \
  ".Lp1_%=:\n\t" \
  "s_waitcnt vmcnt(10)\n\t" \
  "s_barrier\n\t" \
  "s_setprio 2\n\t" \
  "v_mfma_f32_16x16x32_bf16 v[144:147], v[64:67], v[176:179], v[144:147]\n\t" \
  "v_mfma_f32_16x16x32_bf16 v[148:151], v[68:71], v[176:179], v[148:151]\n\t" \
  "v_mfma_f32_16x16x32_bf16 v[152:155], v[64:67], v[180:183], v[152:155]\n\t" \
  "v_mfma_f32_16x16x32_bf16 v[156:159], v[68:71], v[180:183], v[156:159]\n\t" \
  "v_mfma_f32_16x16x32_bf16 v[144:147], v[72:75], v[184:187], v[144:147]\n\t" \
  "v_mfma_f32_16x16x32_bf16 v[148:151], v[76:79], v[184:187], v[148:151]\n\t" \
  "v_mfma_f32_16x16x32_bf16 v[152:155], v[72:75], v[188:191], v[152:155]\n\t" \
  "v_mfma_f32_16x16x32_bf16 v[156:159], v[76:79], v[188:191], v[156:159]\n\t" \
  "v_mfma_f32_16x16x32_bf16 v[144:147], v[80:83], v[192:195], v[144:147]\n\t" \
  "v_mfma_f32_16x16x32_bf16 v[148:151], v[84:87], v[192:195], v[148:151]\n\t" \
  "v_mfma_f32_16x16x32_bf16 v[152:155], v[80:83], v[196:199], v[152:155]\n\t" \
  "v_mfma_f32_16x16x32_bf16 v[156:159], v[84:87], v[196:199], v[156:159]\n\t" \
  "v_mfma_f32_16x16x32_bf16 v[144:147], v[88:91], v[200:203], v[144:147]\n\t" \
  "v_mfma_f32_16x16x32_bf16 v[148:151], v[92:95], v[200:203], v[148:151]\n\t" \
  "v_mfma_f32_16x16x32_bf16 v[152:155], v[88:91], v[204:207], v[152:155]\n\t" \
  "v_mfma_f32_16x16x32_bf16 v[156:159], v[92:95], v[204:207], v[156:159]\n\t" \
  "v_mfma_f32_16x16x32_bf16 v[144:147], v[96:99], v[208:211], v[144:147]\n\t" \
  "v_mfma_f32_16x16x32_bf16 v[148:151], v[100:103], v[208:211], v[148:151]\n\t" \
  "v_mfma_f32_16x16x32_bf16 v[152:155], v[96:99], v[212:215], v[152:155]\n\t" \
  "v_mfma_f32_16x16x32_bf16 v[156:159], v[100:103], v[212:215], v[156:159]\n\t" \
  "v_mfma_f32_16x16x32_bf16 v[144:147], v[104:107], v[216:219], v[144:147]\n\t" \
  "v_mfma_f32_16x16x32_bf16 v[148:151], v[108:111], v[216:219], v[148:151]\n\t" \
  "v_mfma_f32_16x16x32_bf16 v[152:155], v[104:107], v[220:223], v[152:155]\n\t" \
  "v_mfma_f32_16x16x32_bf16 v[156:159], v[108:111], v[220:223], v[156:159]\n\t" \
  "v_mfma_f32_16x16x32_bf16 v[144:147], v[112:115], v[224:227], v[144:147]\n\t" \
  "v_mfma_f32_16x16x32_bf16 v[148:151], v[116:119], v[224:227], v[148:151]\n\t" \
  "v_mfma_f32_16x16x32_bf16 v[152:155], v[112:115], v[228:231], v[152:155]\n\t" \
  "v_mfma_f32_16x16x32_bf16 v[156:159], v[116:119], v[228:231], v[156:159]\n\t" \
  "v_mfma_f32_16x16x32_bf16 v[144:147], v[120:123], v[232:235], v[144:147]\n\t" \
  "v_mfma_f32_16x16x32_bf16 v[148:151], v[124:127], v[232:235], v[148:151]\n\t" \
  "v_mfma_f32_16x16x32_bf16 v[152:155], v[120:123], v[236:239], v[152:155]\n\t" \
  "v_mfma_f32_16x16x32_bf16 v[156:159], v[124:127], v[236:239], v[156:159]\n\t" \
  "s_setprio 0\n\t" \
  ".Ls1_%=:\n\t" \
  "s_add_u32 s92, s92, 512\n\t" \
  "s_addc_u32 s93, s93, 0\n\t" \
  "ds_read_b128 v[176:179], v240 offset:0\n\t" \
  "ds_read_b128 v[180:183], v240 offset:16384\n\t" \
  "ds_read_b128 v[184:187], v241 offset:0\n\t" \
  "ds_read_b128 v[188:191], v241 offset:16384\n\t" \
  "s_add_u32 m0, %[lw], 98304\n\t" \
  "s_nop 0\n\t" \
  "global_load_lds_dwordx4 v248, s[92:93] sc1\n\t" \
  "ds_read_b128 v[192:195], v242 offset:0\n\t" \
  "ds_read_b128 v[196:199], v242 offset:16384\n\t" \
  "ds_read_b128 v[200:203], v243 offset:0\n\t" \
  "ds_read_b128 v[204:207], v243 offset:16384\n\t" \
  "s_add_u32 m0, %[lw], 99328\n\t" \
  "s_nop 0\n\t" \
  "global_load_lds_dwordx4 v249, s[92:93] sc1\n\t" \
  "ds_read_b128 v[208:211], v240 offset:256\n\t" \
  "ds_read_b128 v[212:215], v240 offset:16640\n\t" \
  "ds_read_b128 v[216:219], v241 offset:256\n\t" \
  "ds_read_b128 v[220:223], v241 offset:16640\n\t" \
  "s_add_u32 m0, %[lw], 100352\n\t" \
  "s_nop 0\n\t" \
  "global_load_lds_dwordx4 v250, s[92:93] sc1\n\t" \
  "ds_read_b128 v[224:227], v242 offset:256\n\t" \
  "ds_read_b128 v[228:231], v242 offset:16640\n\t" \
  "ds_read_b128 v[232:235], v243 offset:256\n\t" \
  "ds_read_b128 v[236:239], v243 offset:16640\n\t" \
  "s_add_u32 m0, %[lw], 101376\n\t" \
  "s_nop 0\n\t" \
  "global_load_lds_dwordx4 v251, s[92:93] sc1\n\t" \
  "ds_write_b128 v252, v[152:155] offset:0\n\t" \
  "ds_write_b128 v252, v[156:159] offset:1024\n\t" \
  "s_waitcnt lgkmcnt(0)\n\t" \
  "s_waitcnt vmcnt(4)\n\t" \
  "s_barrier\n\t" \
  "s_setprio 2\n\t" \
  "v_mfma_f32_16x16x32_bf16 v[128:131], v[0:3], v[176:179], 0\n\t" \
  "v_mfma_f32_16x16x32_bf16 v[132:135], v[4:7], v[176:179], 0\n\t" \
  "v_mfma_f32_16x16x32_bf16 v[136:139], v[0:3], v[180:183], 0\n\t" \
  "v_mfma_f32_16x16x32_bf16 v[140:143], v[4:7], v[180:183], 0\n\t" \
  "v_mfma_f32_16x16x32_bf16 v[128:131], v[8:11], v[184:187], v[128:131]\n\t" \
  "v_mfma_f32_16x16x32_bf16 v[132:135], v[12:15], v[184:187], v[132:135]\n\t" \
  "v_mfma_f32_16x16x32_bf16 v[136:139], v[8:11], v[188:191], v[136:139]\n\t" \
  "v_mfma_f32_16x16x32_bf16 v[140:143], v[12:15], v[188:191], v[140:143]\n\t" \
  "v_mfma_f32_16x16x32_bf16 v[128:131], v[16:19], v[192:195], v[128:131]\n\t" \
  "v_mfma_f32_16x16x32_bf16 v[132:135], v[20:23], v[192:195], v[132:135]\n\t" \
  "v_mfma_f32_16x16x32_bf16 v[136:139], v[16:19], v[196:199], v[136:139]\n\t" \
  "v_mfma_f32_16x16x32_bf16 v[140:143], v[20:23], v[196:199], v[140:143]\n\t" \
  "v_mfma_f32_16x16x32_bf16 v[128:131], v[24:27], v[200:203], v[128:131]\n\t" \
  "v_mfma_f32_16x16x32_bf16 v[132:135], v[28:31], v[200:203], v[132:135]\n\t" \
  "v_mfma_f32_16x16x32_bf16 v[136:139], v[24:27], v[204:207], v[136:139]\n\t" \
  "v_mfma_f32_16x16x32_bf16 v[140:143], v[28:31], v[204:207], v[140:143]\n\t" \
  "v_mfma_f32_16x16x32_bf16 v[128:131], v[32:35], v[208:211], v[128:131]\n\t" \
  "v_mfma_f32_16x16x32_bf16 v[132:135], v[36:39], v[208:211], v[132:135]\n\t" \
  "v_mfma_f32_16x16x32_bf16 v[136:139], v[32:35], v[212:215], v[136:139]\n\t" \
  "v_mfma_f32_16x16x32_bf16 v[140:143], v[36:39], v[212:215], v[140:143]\n\t" \
  "v_mfma_f32_16x16x32_bf16 v[128:131], v[40:43], v[216:219], v[128:131]\n\t" \
  "v_mfma_f32_16x16x32_bf16 v[132:135], v[44:47], v[216:219], v[132:135]\n\t" \
  "v_mfma_f32_16x16x32_bf16 v[136:139], v[40:43], v[220:223], v[136:139]\n\t" \
  "v_mfma_f32_16x16x32_bf16 v[140:143], v[44:47], v[220:223], v[140:143]\n\t" \
  "v_mfma_f32_16x16x32_bf16 v[128:131], v[48:51], v[224:227], v[128:131]\n\t" \
  "v_mfma_f32_16x16x32_bf16 v[132:135], v[52:55], v[224:227], v[132:135]\n\t" \
  "v_mfma_f32_16x16x32_bf16 v[136:139], v[48:51], v[228:231], v[136:139]\n\t" \
  "v_mfma_f32_16x16x32_bf16 v[140:143], v[52:55], v[228:231], v[140:143]\n\t" \
  "v_mfma_f32_16x16x32_bf16 v[128:131], v[56:59], v[232:235], v[128:131]\n\t" \
  "v_mfma_f32_16x16x32_bf16 v[132:135], v[60:63], v[232:235], v[132:135]\n\t" \
  "v_mfma_f32_16x16x32_bf16 v[136:139], v[56:59], v[236:239], v[136:139]\n\t" \
  "v_mfma_f32_16x16x32_bf16 v[140:143], v[60:63], v[236:239], v[140:143]\n\t" \
  "s_setprio 0\n\t" \
  "s_add_u32 s92, s92, 65024\n\t" \
  "s_addc_u32 s93, s93, 0\n\t" \
  "ds_read_b128 v[160:163], v253 offset:0\n\t" \
  "ds_read_b128 v[164:167], v253 offset:1024\n\t" \
  "ds_read_b128 v[176:179], v240 offset:32768\n\t" \
  "ds_read_b128 v[180:183], v240 offset:49152\n\t" \
  "ds_read_b128 v[184:187], v241 offset:32768\n\t" \
  "ds_read_b128 v[188:191], v241 offset:49152\n\t" \
  "s_add_u32 m0, %[lw], 0\n\t" \
  "s_nop 0\n\t" \
  "global_load_lds_dwordx4 v248, s[92:93] sc1\n\t" \
  "ds_read_b128 v[192:195], v242 offset:32768\n\t" \
  "ds_read_b128 v[196:199], v242 offset:49152\n\t" \
  "ds_read_b128 v[200:203], v243 offset:32768\n\t" \
  "ds_read_b128 v[204:207], v243 offset:49152\n\t" \
  "s_add_u32 m0, %[lw], 1024\n\t" \
  "s_nop 0\n\t" \
  "global_load_lds_dwordx4 v249, s[92:93] sc1\n\t" \
  "ds_read_b128 v[208:211], v240 offset:33024\n\t" \
  "ds_read_b128 v[212:215], v240 offset:49408\n\t" \
  "ds_read_b128 v[216:219], v241 offset:33024\n\t" \
  "ds_read_b128 v[220:223], v241 offset:49408\n\t" \
  "s_add_u32 m0, %[lw], 2048\n\t" \
  "s_nop 0\n\t" \
  "global_load_lds_dwordx4 v250, s[92:93] sc1\n\t" \
  "ds_read_b128 v[224:227], v242 offset:33024\n\t" \
  "ds_read_b128 v[228:231], v242 offset:49408\n\t" \
  "ds_read_b128 v[232:235], v243 offset:33024\n\t" \
  "ds_read_b128 v[236:239], v243 offset:49408\n\t" \
  "s_add_u32 m0, %[lw], 3072\n\t" \
  "s_nop 0\n\t" \
  "global_load_lds_dwordx4 v251, s[92:93] sc1\n\t" \
  "s_waitcnt lgkmcnt(15)\n\t" \
  "s_waitcnt lgkmcnt(12)\n\t" \
  "s_sub_i32 s91, s86, 2\n\t" \
  "v_mov_b32 v173, s91\n\t" \
  "buffer_store_dword v173, v174, %[rs], 0 offen sc1\n\t" \
  "s_lshr_b32 s90, s87, 7\n\t" \
  "s_xor_b32 s90, s90, %[s0]\n\t" \
  "s_and_b32 s90, s90, 1\n\t" \
  "s_lshl1_add_u32 s90, s87, s90\n\t" \
  "s_lshl_b32 s90, s90, 13\n\t" \
  "v_add_f32 v160, v160, v144\n\t" \
  "v_add_f32 v161, v161, v145\n\t" \
  "v_add_f32 v162, v162, v146\n\t" \
  "v_add_f32 v163, v163, v147\n\t" \
  "v_add_f32 v164, v164, v148\n\t" \
  "v_add_f32 v165, v165, v149\n\t" \
  "v_add_f32 v166, v166, v150\n\t" \
  "v_add_f32 v167, v167, v151\n\t" \
  "v_cvt_pk_bf16_f32 v168, v160, v161\n\t" \
  "v_cvt_pk_bf16_f32 v169, v162, v163\n\t" \
  "v_cvt_pk_bf16_f32 v170, v164, v165\n\t" \
  "v_cvt_pk_bf16_f32 v171, v166, v167\n\t" \
  "buffer_store_dwordx4 v[168:171], v172, %[rt], s90 offen sc1\n\t" \
  "s_add_u32 s86, s86, 1\n\t" \
  "s_add_u32 s87, s87, 1\n\t" \
  "s_waitcnt lgkmcnt(0)\n\t" \
  "s_waitcnt vmcnt(10)\n\t" \
  "s_barrier\n\t" \
  "s_setprio 2\n\t" \
  "v_mfma_f32_16x16x32_bf16 v[128:131], v[64:67], v[176:179], v[128:131]\n\t" \
  "v_mfma_f32_16x16x32_bf16 v[132:135], v[68:71], v[176:179], v[132:135]\n\t" \
  "v_mfma_f32_16x16x32_bf16 v[136:139], v[64:67], v[180:183], v[136:139]\n\t" \
  "v_mfma_f32_16x16x32_bf16 v[140:143], v[68:71], v[180:183], v[140:143]\n\t" \
  "v_mfma_f32_16x16x32_bf16 v[128:131], v[72:75], v[184:187], v[128:131]\n\t" \
  "v_mfma_f32_16x16x32_bf16 v[132:135], v[76:79], v[184:187], v[132:135]\n\t" \
  "v_mfma_f32_16x16x32_bf16 v[136:139], v[72:75], v[188:191], v[136:139]\n\t" \
  "v_mfma_f32_16x16x32_bf16 v[140:143], v[76:79], v[188:191], v[140:143]\n\t" \
  "v_mfma_f32_16x16x32_bf16 v[128:131], v[80:83], v[192:195], v[128:131]\n\t" \
  "v_mfma_f32_16x16x32_bf16 v[132:135], v[84:87], v[192:195], v[132:135]\n\t" \
  "v_mfma_f32_16x16x32_bf16 v[136:139], v[80:83], v[196:199], v[136:139]\n\t" \
  "v_mfma_f32_16x16x32_bf16 v[140:143], v[84:87], v[196:199], v[140:143]\n\t" \
  "v_mfma_f32_16x16x32_bf16 v[128:131], v[88:91], v[200:203], v[128:131]\n\t" \
  "v_mfma_f32_16x16x32_bf16 v[132:135], v[92:95], v[200:203], v[132:135]\n\t" \
  "v_mfma_f32_16x16x32_bf16 v[136:139], v[88:91], v[204:207], v[136:139]\n\t" \
  "v_mfma_f32_16x16x32_bf16 v[140:143], v[92:95], v[204:207], v[140:143]\n\t" \
  "v_mfma_f32_16x16x32_bf16 v[128:131], v[96:99], v[208:211], v[128:131]\n\t" \
  "v_mfma_f32_16x16x32_bf16 v[132:135], v[100:103], v[208:211], v[132:135]\n\t" \
  "v_mfma_f32_16x16x32_bf16 v[136:139], v[96:99], v[212:215], v[136:139]\n\t" \
  "v_mfma_f32_16x16x32_bf16 v[140:143], v[100:103], v[212:215], v[140:143]\n\t" \
  "v_mfma_f32_16x16x32_bf16 v[128:131], v[104:107], v[216:219], v[128:131]\n\t" \
  "v_mfma_f32_16x16x32_bf16 v[132:135], v[108:111], v[216:219], v[132:135]\n\t" \
  "v_mfma_f32_16x16x32_bf16 v[136:139], v[104:107], v[220:223], v[136:139]\n\t" \
  "v_mfma_f32_16x16x32_bf16 v[140:143], v[108:111], v[220:223], v[140:143]\n\t" \
  "v_mfma_f32_16x16x32_bf16 v[128:131], v[112:115], v[224:227], v[128:131]\n\t" \
  "v_mfma_f32_16x16x32_bf16 v[132:135], v[116:119], v[224:227], v[132:135]\n\t" \
  "v_mfma_f32_16x16x32_bf16 v[136:139], v[112:115], v[228:231], v[136:139]\n\t" \
  "v_mfma_f32_16x16x32_bf16 v[140:143], v[116:119], v[228:231], v[140:143]\n\t" \
  "v_mfma_f32_16x16x32_bf16 v[128:131], v[120:123], v[232:235], v[128:131]\n\t" \
  "v_mfma_f32_16x16x32_bf16 v[132:135], v[124:127], v[232:235], v[132:135]\n\t" \
  "v_mfma_f32_16x16x32_bf16 v[136:139], v[120:123], v[236:239], v[136:139]\n\t" \
  "v_mfma_f32_16x16x32_bf16 v[140:143], v[124:127], v[236:239], v[140:143]\n\t" \
  "s_setprio 0\n\t" \
  "s_add_u32 s92, s92, 512\n\t" \
  "s_addc_u32 s93, s93, 0\n\t" \
  "ds_read_b128 v[176:179], v244 offset:0\n\t" \
  "ds_read_b128 v[180:183], v244 offset:16384\n\t" \
  "ds_read_b128 v[184:187], v245 offset:0\n\t" \
  "ds_read_b128 v[188:191], v245 offset:16384\n\t" \
  "s_add_u32 m0, %[lw], 32768\n\t" \
  "s_nop 0\n\t" \
  "global_load_lds_dwordx4 v248, s[92:93] sc1\n\t" \
  "ds_read_b128 v[192:195], v246 offset:0\n\t" \
  "ds_read_b128 v[196:199], v246 offset:16384\n\t" \
  "ds_read_b128 v[200:203], v247 offset:0\n\t" \
  "ds_read_b128 v[204:207], v247 offset:16384\n\t" \
  "s_add_u32 m0, %[lw], 33792\n\t" \
  "s_nop 0\n\t" \
  "global_load_lds_dwordx4 v249, s[92:93] sc1\n\t" \
  "ds_read_b128 v[208:211], v244 offset:256\n\t" \
  "ds_read_b128 v[212:215], v244 offset:16640\n\t" \
  "ds_read_b128 v[216:219], v245 offset:256\n\t" \
  "ds_read_b128 v[220:223], v245 offset:16640\n\t" \
  "s_add_u32 m0, %[lw], 34816\n\t" \
  "s_nop 0\n\t" \
  "global_load_lds_dwordx4 v250, s[92:93] sc1\n\t" \
  "ds_read_b128 v[224:227], v246 offset:256\n\t" \
  "ds_read_b128 v[228:231], v246 offset:16640\n\t" \
  "ds_read_b128 v[232:235], v247 offset:256\n\t" \
  "ds_read_b128 v[236:239], v247 offset:16640\n\t" \
  "s_add_u32 m0, %[lw], 35840\n\t" \
  "s_nop 0\n\t" \
  "global_load_lds_dwordx4 v251, s[92:93] sc1\n\t" \
  "ds_write_b128 v252, v[136:139] offset:0\n\t" \
  "ds_write_b128 v252, v[140:143] offset:1024\n\t" \
  "s_waitcnt lgkmcnt(0)\n\t" \
  "s_waitcnt vmcnt(4)\n\t" \
  "s_barrier\n\t" \
  "s_setprio 2\n\t" \
  "v_mfma_f32_16x16x32_bf16 v[144:147], v[0:3], v[176:179], 0\n\t" \
  "v_mfma_f32_16x16x32_bf16 v[148:151], v[4:7], v[176:179], 0\n\t" \
  "v_mfma_f32_16x16x32_bf16 v[152:155], v[0:3], v[180:183], 0\n\t" \
  "v_mfma_f32_16x16x32_bf16 v[156:159], v[4:7], v[180:183], 0\n\t" \
  "v_mfma_f32_16x16x32_bf16 v[144:147], v[8:11], v[184:187], v[144:147]\n\t" \
  "v_mfma_f32_16x16x32_bf16 v[148:151], v[12:15], v[184:187], v[148:151]\n\t" \
  "v_mfma_f32_16x16x32_bf16 v[152:155], v[8:11], v[188:191], v[152:155]\n\t" \
  "v_mfma_f32_16x16x32_bf16 v[156:159], v[12:15], v[188:191], v[156:159]\n\t" \
  "v_mfma_f32_16x16x32_bf16 v[144:147], v[16:19], v[192:195], v[144:147]\n\t" \
  "v_mfma_f32_16x16x32_bf16 v[148:151], v[20:23], v[192:195], v[148:151]\n\t" \
  "v_mfma_f32_16x16x32_bf16 v[152:155], v[16:19], v[196:199], v[152:155]\n\t" \
  "v_mfma_f32_16x16x32_bf16 v[156:159], v[20:23], v[196:199], v[156:159]\n\t" \
  "v_mfma_f32_16x16x32_bf16 v[144:147], v[24:27], v[200:203], v[144:147]\n\t" \
  "v_mfma_f32_16x16x32_bf16 v[148:151], v[28:31], v[200:203], v[148:151]\n\t" \
  "v_mfma_f32_16x16x32_bf16 v[152:155], v[24:27], v[204:207], v[152:155]\n\t" \
  "v_mfma_f32_16x16x32_bf16 v[156:159], v[28:31], v[204:207], v[156:159]\n\t" \
  "v_mfma_f32_16x16x32_bf16 v[144:147], v[32:35], v[208:211], v[144:147]\n\t" \
  "v_mfma_f32_16x16x32_bf16 v[148:151], v[36:39], v[208:211], v[148:151]\n\t" \
  "v_mfma_f32_16x16x32_bf16 v[152:155], v[32:35], v[212:215], v[152:155]\n\t" \
  "v_mfma_f32_16x16x32_bf16 v[156:159], v[36:39], v[212:215], v[156:159]\n\t" \
  "v_mfma_f32_16x16x32_bf16 v[144:147], v[40:43], v[216:219], v[144:147]\n\t" \
  "v_mfma_f32_16x16x32_bf16 v[148:151], v[44:47], v[216:219], v[148:151]\n\t" \
  "v_mfma_f32_16x16x32_bf16 v[152:155], v[40:43], v[220:223], v[152:155]\n\t" \
  "v_mfma_f32_16x16x32_bf16 v[156:159], v[44:47], v[220:223], v[156:159]\n\t" \
  "v_mfma_f32_16x16x32_bf16 v[144:147], v[48:51], v[224:227], v[144:147]\n\t" \
  "v_mfma_f32_16x16x32_bf16 v[148:151], v[52:55], v[224:227], v[148:151]\n\t" \
  "v_mfma_f32_16x16x32_bf16 v[152:155], v[48:51], v[228:231], v[152:155]\n\t" \
  "v_mfma_f32_16x16x32_bf16 v[156:159], v[52:55], v[228:231], v[156:159]\n\t" \
  "v_mfma_f32_16x16x32_bf16 v[144:147], v[56:59], v[232:235], v[144:147]\n\t" \
  "v_mfma_f32_16x16x32_bf16 v[148:151], v[60:63], v[232:235], v[148:151]\n\t" \
  "v_mfma_f32_16x16x32_bf16 v[152:155], v[56:59], v[236:239], v[152:155]\n\t" \
  "v_mfma_f32_16x16x32_bf16 v[156:159], v[60:63], v[236:239], v[156:159]\n\t" \
  "s_setprio 0\n\t" \
  "s_add_u32 s92, s92, 65024\n\t" \
  "s_addc_u32 s93, s93, 0\n\t" \
  "ds_read_b128 v[160:163], v253 offset:0\n\t" \
  "ds_read_b128 v[164:167], v253 offset:1024\n\t" \
  "ds_read_b128 v[176:179], v244 offset:32768\n\t" \
  "ds_read_b128 v[180:183], v244 offset:49152\n\t" \
  "ds_read_b128 v[184:187], v245 offset:32768\n\t" \
  "ds_read_b128 v[188:191], v245 offset:49152\n\t" \
  "s_add_u32 m0, %[lw], 65536\n\t" \
  "s_nop 0\n\t" \
  "global_load_lds_dwordx4 v248, s[92:93] sc1\n\t" \
  "ds_read_b128 v[192:195], v246 offset:32768\n\t" \
  "ds_read_b128 v[196:199], v246 offset:49152\n\t" \
  "ds_read_b128 v[200:203], v247 offset:32768\n\t" \
  "ds_read_b128 v[204:207], v247 offset:49152\n\t" \
  "s_add_u32 m0, %[lw], 66560\n\t" \
  "s_nop 0\n\t" \
  "global_load_lds_dwordx4 v249, s[92:93] sc1\n\t" \
  "ds_read_b128 v[208:211], v244 offset:33024\n\t" \
  "ds_read_b128 v[212:215], v244 offset:49408\n\t" \
  "ds_read_b128 v[216:219], v245 offset:33024\n\t" \
  "ds_read_b128 v[220:223], v245 offset:49408\n\t" \
  "s_add_u32 m0, %[lw], 67584\n\t" \
  "s_nop 0\n\t" \
  "global_load_lds_dwordx4 v250, s[92:93] sc1\n\t" \
  "ds_read_b128 v[224:227], v246 offset:33024\n\t" \
  "ds_read_b128 v[228:231], v246 offset:49408\n\t" \
  "ds_read_b128 v[232:235], v247 offset:33024\n\t" \
  "ds_read_b128 v[236:239], v247 offset:49408\n\t" \
  "s_add_u32 m0, %[lw], 68608\n\t" \
  "s_nop 0\n\t" \
  "global_load_lds_dwordx4 v251, s[92:93] sc1\n\t" \
  "s_waitcnt lgkmcnt(15)\n\t" \
  "s_waitcnt lgkmcnt(12)\n\t" \
  "s_sub_i32 s91, s86, 2\n\t" \
  "v_mov_b32 v173, s91\n\t" \
  "buffer_store_dword v173, v174, %[rs], 0 offen sc1\n\t" \
  "s_lshr_b32 s90, s87, 7\n\t" \
  "s_xor_b32 s90, s90, %[s0]\n\t" \
  "s_and_b32 s90, s90, 1\n\t" \
  "s_lshl1_add_u32 s90, s87, s90\n\t" \
  "s_lshl_b32 s90, s90, 13\n\t" \
  "v_add_f32 v160, v160, v128\n\t" \
  "v_add_f32 v161, v161, v129\n\t" \
  "v_add_f32 v162, v162, v130\n\t" \
  "v_add_f32 v163, v163, v131\n\t" \
  "v_add_f32 v164, v164, v132\n\t" \
  "v_add_f32 v165, v165, v133\n\t" \
  "v_add_f32 v166, v166, v134\n\t" \
  "v_add_f32 v167, v167, v135\n\t" \
  "v_cvt_pk_bf16_f32 v168, v160, v161\n\t" \
  "v_cvt_pk_bf16_f32 v169, v162, v163\n\t" \
  "v_cvt_pk_bf16_f32 v170, v164, v165\n\t" \
  "v_cvt_pk_bf16_f32 v171, v166, v167\n\t" \
  "buffer_store_dwordx4 v[168:171], v172, %[rt], s90 offen sc1\n\t" \
  "s_add_u32 s86, s86, 1\n\t" \
  "s_add_u32 s87, s87, 1\n\t" \
  "s_waitcnt lgkmcnt(0)\n\t" \
  "s_cmp_lt_u32 s86, 64\n\t" \
  "s_cbranch_scc1 .Lp1_%=\n\t" \
  "s_barrier\n\t" \
  "s_setprio 2\n\t" \
  "v_mfma_f32_16x16x32_bf16 v[144:147], v[64:67], v[176:179], v[144:147]\n\t" \
  "v_mfma_f32_16x16x32_bf16 v[148:151], v[68:71], v[176:179], v[148:151]\n\t" \
  "v_mfma_f32_16x16x32_bf16 v[152:155], v[64:67], v[180:183], v[152:155]\n\t" \
  "v_mfma_f32_16x16x32_bf16 v[156:159], v[68:71], v[180:183], v[156:159]\n\t" \
  "v_mfma_f32_16x16x32_bf16 v[144:147], v[72:75], v[184:187], v[144:147]\n\t" \
  "v_mfma_f32_16x16x32_bf16 v[148:151], v[76:79], v[184:187], v[148:151]\n\t" \
  "v_mfma_f32_16x16x32_bf16 v[152:155], v[72:75], v[188:191], v[152:155]\n\t" \
  "v_mfma_f32_16x16x32_bf16 v[156:159], v[76:79], v[188:191], v[156:159]\n\t" \
  "v_mfma_f32_16x16x32_bf16 v[144:147], v[80:83], v[192:195], v[144:147]\n\t" \
  "v_mfma_f32_16x16x32_bf16 v[148:151], v[84:87], v[192:195], v[148:151]\n\t" \
  "v_mfma_f32_16x16x32_bf16 v[152:155], v[80:83], v[196:199], v[152:155]\n\t" \
  "v_mfma_f32_16x16x32_bf16 v[156:159], v[84:87], v[196:199], v[156:159]\n\t" \
  "v_mfma_f32_16x16x32_bf16 v[144:147], v[88:91], v[200:203], v[144:147]\n\t" \
  "v_mfma_f32_16x16x32_bf16 v[148:151], v[92:95], v[200:203], v[148:151]\n\t" \
  "v_mfma_f32_16x16x32_bf16 v[152:155], v[88:91], v[204:207], v[152:155]\n\t" \
  "v_mfma_f32_16x16x32_bf16 v[156:159], v[92:95], v[204:207], v[156:159]\n\t" \
  "v_mfma_f32_16x16x32_bf16 v[144:147], v[96:99], v[208:211], v[144:147]\n\t" \
  "v_mfma_f32_16x16x32_bf16 v[148:151], v[100:103], v[208:211], v[148:151]\n\t" \
  "v_mfma_f32_16x16x32_bf16 v[152:155], v[96:99], v[212:215], v[152:155]\n\t" \
  "v_mfma_f32_16x16x32_bf16 v[156:159], v[100:103], v[212:215], v[156:159]\n\t" \
  "v_mfma_f32_16x16x32_bf16 v[144:147], v[104:107], v[216:219], v[144:147]\n\t" \
  "v_mfma_f32_16x16x32_bf16 v[148:151], v[108:111], v[216:219], v[148:151]\n\t" \
  "v_mfma_f32_16x16x32_bf16 v[152:155], v[104:107], v[220:223], v[152:155]\n\t" \
  "v_mfma_f32_16x16x32_bf16 v[156:159], v[108:111], v[220:223], v[156:159]\n\t" \
  "v_mfma_f32_16x16x32_bf16 v[144:147], v[112:115], v[224:227], v[144:147]\n\t" \
  "v_mfma_f32_16x16x32_bf16 v[148:151], v[116:119], v[224:227], v[148:151]\n\t" \
  "v_mfma_f32_16x16x32_bf16 v[152:155], v[112:115], v[228:231], v[152:155]\n\t" \
  "v_mfma_f32_16x16x32_bf16 v[156:159], v[116:119], v[228:231], v[156:159]\n\t" \
  "v_mfma_f32_16x16x32_bf16 v[144:147], v[120:123], v[232:235], v[144:147]\n\t" \
  "v_mfma_f32_16x16x32_bf16 v[148:151], v[124:127], v[232:235], v[148:151]\n\t" \
  "v_mfma_f32_16x16x32_bf16 v[152:155], v[120:123], v[236:239], v[152:155]\n\t" \
  "v_mfma_f32_16x16x32_bf16 v[156:159], v[124:127], v[236:239], v[156:159]\n\t" \
  "s_setprio 0\n\t" \
  "s_nop 5\n\t" \
  "ds_write_b128 v252, v[152:155] offset:0\n\t" \
  "ds_write_b128 v252, v[156:159] offset:1024\n\t" \
  "s_waitcnt lgkmcnt(0)\n\t" \
  "s_barrier\n\t" \
  "ds_read_b128 v[160:163], v253 offset:0\n\t" \
  "ds_read_b128 v[164:167], v253 offset:1024\n\t" \
  "s_waitcnt lgkmcnt(0)\n\t" \
  "s_lshr_b32 s90, s87, 7\n\t" \
  "s_xor_b32 s90, s90, %[s0]\n\t" \
  "s_and_b32 s90, s90, 1\n\t" \
  "s_lshl1_add_u32 s90, s87, s90\n\t" \
  "s_lshl_b32 s90, s90, 13\n\t" \
  "v_add_f32 v160, v160, v144\n\t" \
  "v_add_f32 v161, v161, v145\n\t" \
  "v_add_f32 v162, v162, v146\n\t" \
  "v_add_f32 v163, v163, v147\n\t" \
  "v_add_f32 v164, v164, v148\n\t" \
  "v_add_f32 v165, v165, v149\n\t" \
  "v_add_f32 v166, v166, v150\n\t" \
  "v_add_f32 v167, v167, v151\n\t" \
  "v_cvt_pk_bf16_f32 v168, v160, v161\n\t" \
  "v_cvt_pk_bf16_f32 v169, v162, v163\n\t" \
  "v_cvt_pk_bf16_f32 v170, v164, v165\n\t" \
  "v_cvt_pk_bf16_f32 v171, v166, v167\n\t" \
  "buffer_store_dwordx4 v[168:171], v172, %[rt], s90 offen sc1\n\t" \
  "s_waitcnt vmcnt(0)\n\t" \
  "s_barrier\n\t" \
  "s_sub_i32 s91, s86, 1\n\t" \
  "v_mov_b32 v173, s91\n\t" \
  "buffer_store_dword v173, v174, %[rs], 0 offen sc1\n\t" \
  "s_mov_b32 m0, s94\n\t" \
  ".Le_%=:\n\t"
#define PP_ASM_1 \
  "s_cmp_lg_u32 %[kh], 0\n\t" \
  "s_cbranch_scc1 .Lk_%=\n\t" \
  "s_mov_b32 s94, m0\n\t" \
  "s_mov_b64 s[92:93], %[x0]\n\t" \
  "s_mov_b32 s86, %[g0]\n\t" \
  "s_add_u32 s87, s86, %[t0]\n\t" \
  "s_add_u32 m0, %[lw], 67584\n\t" \
  "s_nop 0\n\t" \
  "global_load_lds_dwordx4 v250, s[92:93] sc1\n\t" \
  "s_add_u32 m0, %[lw], 68608\n\t" \
  "s_nop 0\n\t" \
  "global_load_lds_dwordx4 v251, s[92:93] sc1\n\t" \
  "s_barrier\n\t" \
  "s_branch .Ls0_%=\n\t" \
  ".Lp0_%=:\n\t" \
  "s_waitcnt vmcnt(9)\n\t" \
  "s_barrier\n\t" \
  ".Ls0_%=:\n\t" \
  "s_add_u32 s92, s92, 512\n\t" \
  "s_addc_u32 s93, s93, 0\n\t" \
  "ds_read_b128 v[176:179], v240 offset:0\n\t" \
  "ds_read_b128 v[180:183], v240 offset:16384\n\t" \
  "ds_read_b128 v[184:187], v241 offset:0\n\t" \
  "ds_read_b128 v[188:191], v241 offset:16384\n\t" \
  "s_add_u32 m0, %[lw], 98304\n\t" \
  "s_nop 0\n\t" \
  "global_load_lds_dwordx4 v248, s[92:93] sc1\n\t" \
  "ds_read_b128 v[192:195], v242 offset:0\n\t" \
  "ds_read_b128 v[196:199], v242 offset:16384\n\t" \
  "ds_read_b128 v[200:203], v243 offset:0\n\t" \
  "ds_read_b128 v[204:207], v243 offset:16384\n\t" \
  "s_add_u32 m0, %[lw], 99328\n\t" \
  "s_nop 0\n\t" \
  "global_load_lds_dwordx4 v249, s[92:93] sc1\n\t" \
  "ds_read_b128 v[208:211], v240 offset:256\n\t" \
  "ds_read_b128 v[212:215], v240 offset:16640\n\t" \
  "ds_read_b128 v[216:219], v241 offset:256\n\t" \
  "ds_read_b128 v[220:223], v241 offset:16640\n\t" \
  "s_add_u32 m0, %[lw], 100352\n\t" \
  "s_nop 0\n\t" \
  "global_load_lds_dwordx4 v250, s[92:93] sc1\n\t" \
  "ds_read_b128 v[224:227], v242 offset:256\n\t" \
  "ds_read_b128 v[228:231], v242 offset:16640\n\t" \
  "ds_read_b128 v[232:235], v243 offset:256\n\t" \
  "ds_read_b128 v[236:239], v243 offset:16640\n\t" \
  "s_add_u32 m0, %[lw], 101376\n\t" \
  "s_nop 0\n\t" \
  "global_load_lds_dwordx4 v251, s[92:93] sc1\n\t" \
  "ds_write_b128 v252, v[152:155] offset:0\n\t" \
  "ds_write_b128 v252, v[156:159] offset:1024\n\t" \
  "s_waitcnt lgkmcnt(0)\n\t" \
  "s_setprio 1\n\t" \
  "v_mfma_f32_16x16x32_bf16 v[128:131], v[176:179], v[0:3], 0\n\t" \
  "v_mfma_f32_16x16x32_bf16 v[136:139], v[176:179], v[4:7], 0\n\t" \
  "v_mfma_f32_16x16x32_bf16 v[132:135], v[180:183], v[0:3], 0\n\t" \
  "v_mfma_f32_16x16x32_bf16 v[140:143], v[180:183], v[4:7], 0\n\t" \
  "v_mfma_f32_16x16x32_bf16 v[128:131], v[184:187], v[8:11], v[128:131]\n\t" \
  "v_mfma_f32_16x16x32_bf16 v[136:139], v[184:187], v[12:15], v[136:139]\n\t" \
  "v_mfma_f32_16x16x32_bf16 v[132:135], v[188:191], v[8:11], v[132:135]\n\t" \
  "v_mfma_f32_16x16x32_bf16 v[140:143], v[188:191], v[12:15], v[140:143]\n\t" \
  "v_mfma_f32_16x16x32_bf16 v[128:131], v[192:195], v[16:19], v[128:131]\n\t" \
  "v_mfma_f32_16x16x32_bf16 v[136:139], v[192:195], v[20:23], v[136:139]\n\t" \
  "v_mfma_f32_16x16x32_bf16 v[132:135], v[196:199], v[16:19], v[132:135]\n\t" \
  "v_mfma_f32_16x16x32_bf16 v[140:143], v[196:199], v[20:23], v[140:143]\n\t" \
  "v_mfma_f32_16x16x32_bf16 v[128:131], v[200:203], v[24:27], v[128:131]\n\t" \
  "v_mfma_f32_16x16x32_bf16 v[136:139], v[200:203], v[28:31], v[136:139]\n\t" \
  "v_mfma_f32_16x16x32_bf16 v[132:135], v[204:207], v[24:27], v[132:135]\n\t" \
  "v_mfma_f32_16x16x32_bf16 v[140:143], v[204:207], v[28:31], v[140:143]\n\t" \
  "v_mfma_f32_16x16x32_bf16 v[128:131], v[208:211], v[32:35], v[128:131]\n\t" \
  "v_mfma_f32_16x16x32_bf16 v[136:139], v[208:211], v[36:39], v[136:139]\n\t" \
  "v_mfma_f32_16x16x32_bf16 v[132:135], v[212:215], v[32:35], v[132:135]\n\t" \
  "v_mfma_f32_16x16x32_bf16 v[140:143], v[212:215], v[36:39], v[140:143]\n\t" \
  "v_mfma_f32_16x16x32_bf16 v[128:131], v[216:219], v[40:43], v[128:131]\n\t" \
  "v_mfma_f32_16x16x32_bf16 v[136:139], v[216:219], v[44:47], v[136:139]\n\t" \
  "v_mfma_f32_16x16x32_bf16 v[132:135], v[220:223], v[40:43], v[132:135]\n\t" \
  "v_mfma_f32_16x16x32_bf16 v[140:143], v[220:223], v[44:47], v[140:143]\n\t" \
  "v_mfma_f32_16x16x32_bf16 v[128:131], v[224:227], v[48:51], v[128:131]\n\t" \
  "v_mfma_f32_16x16x32_bf16 v[136:139], v[224:227], v[52:55], v[136:139]\n\t" \
  "v_mfma_f32_16x16x32_bf16 v[132:135], v[228:231], v[48:51], v[132:135]\n\t" \
  "v_mfma_f32_16x16x32_bf16 v[140:143], v[228:231], v[52:55], v[140:143]\n\t" \
  "v_mfma_f32_16x16x32_bf16 v[128:131], v[232:235], v[56:59], v[128:131]\n\t" \
  "v_mfma_f32_16x16x32_bf16 v[136:139], v[232:235], v[60:63], v[136:139]\n\t" \
  "v_mfma_f32_16x16x32_bf16 v[132:135], v[236:239], v[56:59], v[132:135]\n\t" \
  "v_mfma_f32_16x16x32_bf16 v[140:143], v[236:239], v[60:63], v[140:143]\n\t" \
  "s_setprio 0\n\t" \
  "s_waitcnt vmcnt(4)\n\t" \
  "s_barrier\n\t" \
  "s_add_u32 s92, s92, 65024\n\t" \
  "s_addc_u32 s93, s93, 0\n\t" \
  "ds_read_b128 v[160:163], v253 offset:0\n\t" \
  "ds_read_b128 v[164:167], v253 offset:1024\n\t" \
  "ds_read_b128 v[176:179], v240 offset:32768\n\t" \
  "ds_read_b128 v[180:183], v240 offset:49152\n\t" \
  "ds_read_b128 v[184:187], v241 offset:32768\n\t" \
  "ds_read_b128 v[188:191], v241 offset:49152\n\t" \
  "s_add_u32 m0, %[lw], 0\n\t" \
  "s_nop 0\n\t" \
  "global_load_lds_dwordx4 v248, s[92:93] sc1\n\t" \
  "ds_read_b128 v[192:195], v242 offset:32768\n\t" \
  "ds_read_b128 v[196:199], v242 offset:49152\n\t" \
  "ds_read_b128 v[200:203], v243 offset:32768\n\t" \
  "ds_read_b128 v[204:207], v243 offset:49152\n\t" \
  "s_add_u32 m0, %[lw], 1024\n\t" \
  "s_nop 0\n\t" \
  "global_load_lds_dwordx4 v249, s[92:93] sc1\n\t" \
  "ds_read_b128 v[208:211], v240 offset:33024\n\t" \
  "ds_read_b128 v[212:215], v240 offset:49408\n\t" \
  "ds_read_b128 v[216:219], v241 offset:33024\n\t" \
  "ds_read_b128 v[220:223], v241 offset:49408\n\t" \
  "s_add_u32 m0, %[lw], 2048\n\t" \
  "s_nop 0\n\t" \
  "global_load_lds_dwordx4 v250, s[92:93] sc1\n\t" \
  "ds_read_b128 v[224:227], v242 offset:33024\n\t" \
  "ds_read_b128 v[228:231], v242 offset:49408\n\t" \
  "ds_read_b128 v[232:235], v243 offset:33024\n\t" \
  "ds_read_b128 v[236:239], v243 offset:49408\n\t" \
  "s_add_u32 m0, %[lw], 3072\n\t" \
  "s_nop 0\n\t" \
  "global_load_lds_dwordx4 v251, s[92:93] sc1\n\t" \
  "s_waitcnt lgkmcnt(15)\n\t" \
  "s_waitcnt lgkmcnt(12)\n\t" \
  "s_lshr_b32 s90, s87, 7\n\t" \
  "s_xor_b32 s90, s90, %[s0]\n\t" \
  "s_and_b32 s90, s90, 1\n\t" \
  "s_lshl1_add_u32 s90, s87, s90\n\t" \
  "s_lshl_b32 s90, s90, 13\n\t" \
  "v_add_f32 v160, v160, v144\n\t" \
  "v_add_f32 v161, v161, v145\n\t" \
  "v_add_f32 v162, v162, v146\n\t" \
  "v_add_f32 v163, v163, v147\n\t" \
  "v_add_f32 v164, v164, v148\n\t" \
  "v_add_f32 v165, v165, v149\n\t" \
  "v_add_f32 v166, v166, v150\n\t" \
  "v_add_f32 v167, v167, v151\n\t" \
  "v_cvt_pk_bf16_f32 v168, v160, v161\n\t" \
  "v_cvt_pk_bf16_f32 v169, v162, v163\n\t" \
  "v_cvt_pk_bf16_f32 v170, v164, v165\n\t" \
  "v_cvt_pk_bf16_f32 v171, v166, v167\n\t" \
  "buffer_store_dwordx4 v[168:171], v172, %[rt], s90 offen sc1\n\t" \
  "s_add_u32 s86, s86, 1\n\t" \
  "s_add_u32 s87, s87, 1\n\t" \
  "s_waitcnt lgkmcnt(0)\n\t" \
  "s_setprio 1\n\t" \
  "v_mfma_f32_16x16x32_bf16 v[128:131], v[176:179], v[64:67], v[128:131]\n\t" \
  "v_mfma_f32_16x16x32_bf16 v[136:139], v[176:179], v[68:71], v[136:139]\n\t" \
  "v_mfma_f32_16x16x32_bf16 v[132:135], v[180:183], v[64:67], v[132:135]\n\t" \
  "v_mfma_f32_16x16x32_bf16 v[140:143], v[180:183], v[68:71], v[140:143]\n\t" \
  "v_mfma_f32_16x16x32_bf16 v[128:131], v[184:187], v[72:75], v[128:131]\n\t" \
  "v_mfma_f32_16x16x32_bf16 v[136:139], v[184:187], v[76:79], v[136:139]\n\t" \
  "v_mfma_f32_16x16x32_bf16 v[132:135], v[188:191], v[72:75], v[132:135]\n\t" \
  "v_mfma_f32_16x16x32_bf16 v[140:143], v[188:191], v[76:79], v[140:143]\n\t" \
  "v_mfma_f32_16x16x32_bf16 v[128:131], v[192:195], v[80:83], v[128:131]\n\t" \
  "v_mfma_f32_16x16x32_bf16 v[136:139], v[192:195], v[84:87], v[136:139]\n\t" \
  "v_mfma_f32_16x16x32_bf16 v[132:135], v[196:199], v[80:83], v[132:135]\n\t" \
  "v_mfma_f32_16x16x32_bf16 v[140:143], v[196:199], v[84:87], v[140:143]\n\t" \
  "v_mfma_f32_16x16x32_bf16 v[128:131], v[200:203], v[88:91], v[128:131]\n\t" \
  "v_mfma_f32_16x16x32_bf16 v[136:139], v[200:203], v[92:95], v[136:139]\n\t" \
  "v_mfma_f32_16x16x32_bf16 v[132:135], v[204:207], v[88:91], v[132:135]\n\t" \
  "v_mfma_f32_16x16x32_bf16 v[140:143], v[204:207], v[92:95], v[140:143]\n\t" \
  "v_mfma_f32_16x16x32_bf16 v[128:131], v[208:211], v[96:99], v[128:131]\n\t" \
  "v_mfma_f32_16x16x32_bf16 v[136:139], v[208:211], v[100:103], v[136:139]\n\t" \
  "v_mfma_f32_16x16x32_bf16 v[132:135], v[212:215], v[96:99], v[132:135]\n\t" \
  "v_mfma_f32_16x16x32_bf16 v[140:143], v[212:215], v[100:103], v[140:143]\n\t" \
  "v_mfma_f32_16x16x32_bf16 v[128:131], v[216:219], v[104:107], v[128:131]\n\t" \
  "v_mfma_f32_16x16x32_bf16 v[136:139], v[216:219], v[108:111], v[136:139]\n\t" \
  "v_mfma_f32_16x16x32_bf16 v[132:135], v[220:223], v[104:107], v[132:135]\n\t" \
  "v_mfma_f32_16x16x32_bf16 v[140:143], v[220:223], v[108:111], v[140:143]\n\t" \
  "v_mfma_f32_16x16x32_bf16 v[128:131], v[224:227], v[112:115], v[128:131]\n\t" \
  "v_mfma_f32_16x16x32_bf16 v[136:139], v[224:227], v[116:119], v[136:139]\n\t" \
  "v_mfma_f32_16x16x32_bf16 v[132:135], v[228:231], v[112:115], v[132:135]\n\t" \
  "v_mfma_f32_16x16x32_bf16 v[140:143], v[228:231], v[116:119], v[140:143]\n\t" \
  "v_mfma_f32_16x16x32_bf16 v[128:131], v[232:235], v[120:123], v[128:131]\n\t" \
  "v_mfma_f32_16x16x32_bf16 v[136:139], v[232:235], v[124:127], v[136:139]\n\t" \
  "v_mfma_f32_16x16x32_bf16 v[132:135], v[236:239], v[120:123], v[132:135]\n\t" \
  "v_mfma_f32_16x16x32_bf16 v[140:143], v[236:239], v[124:127], v[140:143]\n\t" \
  "s_setprio 0\n\t" \
  "s_waitcnt vmcnt(9)\n\t" \
  "s_barrier\n\t" \
  "s_add_u32 s92, s92, 512\n\t" \
  "s_addc_u32 s93, s93, 0\n\t" \
  "ds_read_b128 v[176:179], v244 offset:0\n\t" \
  "ds_read_b128 v[180:183], v244 offset:16384\n\t" \
  "ds_read_b128 v[184:187], v245 offset:0\n\t" \
  "ds_read_b128 v[188:191], v245 offset:16384\n\t" \
  "s_add_u32 m0, %[lw], 32768\n\t" \
  "s_nop 0\n\t" \
  "global_load_lds_dwordx4 v248, s[92:93] sc1\n\t" \
  "ds_read_b128 v[192:195], v246 offset:0\n\t" \
  "ds_read_b128 v[196:199], v246 offset:16384\n\t" \
  "ds_read_b128 v[200:203], v247 offset:0\n\t" \
  "ds_read_b128 v[204:207], v247 offset:16384\n\t" \
  "s_add_u32 m0, %[lw], 33792\n\t" \
  "s_nop 0\n\t" \
  "global_load_lds_dwordx4 v249, s[92:93] sc1\n\t" \
  "ds_read_b128 v[208:211], v244 offset:256\n\t" \
  "ds_read_b128 v[212:215], v244 offset:16640\n\t" \
  "ds_read_b128 v[216:219], v245 offset:256\n\t" \
  "ds_read_b128 v[220:223], v245 offset:16640\n\t" \
  "s_add_u32 m0, %[lw], 34816\n\t" \
  "s_nop 0\n\t" \
  "global_load_lds_dwordx4 v250, s[92:93] sc1\n\t" \
  "ds_read_b128 v[224:227], v246 offset:256\n\t" \
  "ds_read_b128 v[228:231], v246 offset:16640\n\t" \
  "ds_read_b128 v[232:235], v247 offset:256\n\t" \
  "ds_read_b128 v[236:239], v247 offset:16640\n\t" \
  "s_add_u32 m0, %[lw], 35840\n\t" \
  "s_nop 0\n\t" \
  "global_load_lds_dwordx4 v251, s[92:93] sc1\n\t" \
  "ds_write_b128 v252, v[136:139] offset:0\n\t" \
  "ds_write_b128 v252, v[140:143] offset:1024\n\t" \
  "s_waitcnt lgkmcnt(0)\n\t" \
  "s_setprio 1\n\t" \
  "v_mfma_f32_16x16x32_bf16 v[144:147], v[176:179], v[0:3], 0\n\t" \
  "v_mfma_f32_16x16x32_bf16 v[152:155], v[176:179], v[4:7], 0\n\t" \
  "v_mfma_f32_16x16x32_bf16 v[148:151], v[180:183], v[0:3], 0\n\t" \
  "v_mfma_f32_16x16x32_bf16 v[156:159], v[180:183], v[4:7], 0\n\t" \
  "v_mfma_f32_16x16x32_bf16 v[144:147], v[184:187], v[8:11], v[144:147]\n\t" \
  "v_mfma_f32_16x16x32_bf16 v[152:155], v[184:187], v[12:15], v[152:155]\n\t" \
  "v_mfma_f32_16x16x32_bf16 v[148:151], v[188:191], v[8:11], v[148:151]\n\t" \
  "v_mfma_f32_16x16x32_bf16 v[156:159], v[188:191], v[12:15], v[156:159]\n\t" \
  "v_mfma_f32_16x16x32_bf16 v[144:147], v[192:195], v[16:19], v[144:147]\n\t" \
  "v_mfma_f32_16x16x32_bf16 v[152:155], v[192:195], v[20:23], v[152:155]\n\t" \
  "v_mfma_f32_16x16x32_bf16 v[148:151], v[196:199], v[16:19], v[148:151]\n\t" \
  "v_mfma_f32_16x16x32_bf16 v[156:159], v[196:199], v[20:23], v[156:159]\n\t" \
  "v_mfma_f32_16x16x32_bf16 v[144:147], v[200:203], v[24:27], v[144:147]\n\t" \
  "v_mfma_f32_16x16x32_bf16 v[152:155], v[200:203], v[28:31], v[152:155]\n\t" \
  "v_mfma_f32_16x16x32_bf16 v[148:151], v[204:207], v[24:27], v[148:151]\n\t" \
  "v_mfma_f32_16x16x32_bf16 v[156:159], v[204:207], v[28:31], v[156:159]\n\t" \
  "v_mfma_f32_16x16x32_bf16 v[144:147], v[208:211], v[32:35], v[144:147]\n\t" \
  "v_mfma_f32_16x16x32_bf16 v[152:155], v[208:211], v[36:39], v[152:155]\n\t" \
  "v_mfma_f32_16x16x32_bf16 v[148:151], v[212:215], v[32:35], v[148:151]\n\t" \
  "v_mfma_f32_16x16x32_bf16 v[156:159], v[212:215], v[36:39], v[156:159]\n\t" \
  "v_mfma_f32_16x16x32_bf16 v[144:147], v[216:219], v[40:43], v[144:147]\n\t" \
  "v_mfma_f32_16x16x32_bf16 v[152:155], v[216:219], v[44:47], v[152:155]\n\t" \
  "v_mfma_f32_16x16x32_bf16 v[148:151], v[220:223], v[40:43], v[148:151]\n\t" \
  "v_mfma_f32_16x16x32_bf16 v[156:159], v[220:223], v[44:47], v[156:159]\n\t" \
  "v_mfma_f32_16x16x32_bf16 v[144:147], v[224:227], v[48:51], v[144:147]\n\t" \
  "v_mfma_f32_16x16x32_bf16 v[152:155], v[224:227], v[52:55], v[152:155]\n\t" \
  "v_mfma_f32_16x16x32_bf16 v[148:151], v[228:231], v[48:51], v[148:151]\n\t" \
  "v_mfma_f32_16x16x32_bf16 v[156:159], v[228:231], v[52:55], v[156:159]\n\t" \
  "v_mfma_f32_16x16x32_bf16 v[144:147], v[232:235], v[56:59], v[144:147]\n\t" \
  "v_mfma_f32_16x16x32_bf16 v[152:155], v[232:235], v[60:63], v[152:155]\n\t" \
  "v_mfma_f32_16x16x32_bf16 v[148:151], v[236:239], v[56:59], v[148:151]\n\t" \
  "v_mfma_f32_16x16x32_bf16 v[156:159], v[236:239], v[60:63], v[156:159]\n\t" \
  "s_setprio 0\n\t" \
  "s_waitcnt vmcnt(4)\n\t" \
  "s_barrier\n\t" \
  "s_add_u32 s92, s92, 65024\n\t" \
  "s_addc_u32 s93, s93, 0\n\t" \
  "ds_read_b128 v[160:163], v253 offset:0\n\t" \
  "ds_read_b128 v[164:167], v253 offset:1024\n\t" \
  "ds_read_b128 v[176:179], v244 offset:32768\n\t" \
  "ds_read_b128 v[180:183], v244 offset:49152\n\t" \
  "ds_read_b128 v[184:187], v245 offset:32768\n\t" \
  "ds_read_b128 v[188:191], v245 offset:49152\n\t" \
  "s_add_u32 m0, %[lw], 65536\n\t" \
  "s_nop 0\n\t" \
  "global_load_lds_dwordx4 v248, s[92:93] sc1\n\t" \
  "ds_read_b128 v[192:195], v246 offset:32768\n\t" \
  "ds_read_b128 v[196:199], v246 offset:49152\n\t" \
  "ds_read_b128 v[200:203], v247 offset:32768\n\t" \
  "ds_read_b128 v[204:207], v247 offset:49152\n\t" \
  "s_add_u32 m0, %[lw], 66560\n\t" \
  "s_nop 0\n\t" \
  "global_load_lds_dwordx4 v249, s[92:93] sc1\n\t" \
  "ds_read_b128 v[208:211], v244 offset:33024\n\t" \
  "ds_read_b128 v[212:215], v244 offset:49408\n\t" \
  "ds_read_b128 v[216:219], v245 offset:33024\n\t" \
  "ds_read_b128 v[220:223], v245 offset:49408\n\t" \
  "s_add_u32 m0, %[lw], 67584\n\t" \
  "s_nop 0\n\t" \
  "global_load_lds_dwordx4 v250, s[92:93] sc1\n\t" \
  "ds_read_b128 v[224:227], v246 offset:33024\n\t" \
  "ds_read_b128 v[228:231], v246 offset:49408\n\t" \
  "ds_read_b128 v[232:235], v247 offset:33024\n\t" \
  "ds_read_b128 v[236:239], v247 offset:49408\n\t" \
  "s_add_u32 m0, %[lw], 68608\n\t" \
  "s_nop 0\n\t" \
  "global_load_lds_dwordx4 v251, s[92:93] sc1\n\t" \
  "s_waitcnt lgkmcnt(15)\n\t" \
  "s_waitcnt lgkmcnt(12)\n\t" \
  "s_lshr_b32 s90, s87, 7\n\t" \
  "s_xor_b32 s90, s90, %[s0]\n\t" \
  "s_and_b32 s90, s90, 1\n\t" \
  "s_lshl1_add_u32 s90, s87, s90\n\t" \
  "s_lshl_b32 s90, s90, 13\n\t" \
  "v_add_f32 v160, v160, v128\n\t" \
  "v_add_f32 v161, v161, v129\n\t" \
  "v_add_f32 v162, v162, v130\n\t" \
  "v_add_f32 v163, v163, v131\n\t" \
  "v_add_f32 v164, v164, v132\n\t" \
  "v_add_f32 v165, v165, v133\n\t" \
  "v_add_f32 v166, v166, v134\n\t" \
  "v_add_f32 v167, v167, v135\n\t" \
  "v_cvt_pk_bf16_f32 v168, v160, v161\n\t" \
  "v_cvt_pk_bf16_f32 v169, v162, v163\n\t" \
  "v_cvt_pk_bf16_f32 v170, v164, v165\n\t" \
  "v_cvt_pk_bf16_f32 v171, v166, v167\n\t" \
  "buffer_store_dwordx4 v[168:171], v172, %[rt], s90 offen sc1\n\t" \
  "s_add_u32 s86, s86, 1\n\t" \
  "s_add_u32 s87, s87, 1\n\t" \
  "s_waitcnt lgkmcnt(0)\n\t" \
  "s_setprio 1\n\t" \
  "v_mfma_f32_16x16x32_bf16 v[144:147], v[176:179], v[64:67], v[144:147]\n\t" \
  "v_mfma_f32_16x16x32_bf16 v[152:155], v[176:179], v[68:71], v[152:155]\n\t" \
  "v_mfma_f32_16x16x32_bf16 v[148:151], v[180:183], v[64:67], v[148:151]\n\t" \
  "v_mfma_f32_16x16x32_bf16 v[156:159], v[180:183], v[68:71], v[156:159]\n\t" \
  "v_mfma_f32_16x16x32_bf16 v[144:147], v[184:187], v[72:75], v[144:147]\n\t" \
  "v_mfma_f32_16x16x32_bf16 v[152:155], v[184:187], v[76:79], v[152:155]\n\t" \
  "v_mfma_f32_16x16x32_bf16 v[148:151], v[188:191], v[72:75], v[148:151]\n\t" \
  "v_mfma_f32_16x16x32_bf16 v[156:159], v[188:191], v[76:79], v[156:159]\n\t" \
  "v_mfma_f32_16x16x32_bf16 v[144:147], v[192:195], v[80:83], v[144:147]\n\t" \
  "v_mfma_f32_16x16x32_bf16 v[152:155], v[192:195], v[84:87], v[152:155]\n\t" \
  "v_mfma_f32_16x16x32_bf16 v[148:151], v[196:199], v[80:83], v[148:151]\n\t" \
  "v_mfma_f32_16x16x32_bf16 v[156:159], v[196:199], v[84:87], v[156:159]\n\t" \
  "v_mfma_f32_16x16x32_bf16 v[144:147], v[200:203], v[88:91], v[144:147]\n\t" \
  "v_mfma_f32_16x16x32_bf16 v[152:155], v[200:203], v[92:95], v[152:155]\n\t" \
  "v_mfma_f32_16x16x32_bf16 v[148:151], v[204:207], v[88:91], v[148:151]\n\t" \
  "v_mfma_f32_16x16x32_bf16 v[156:159], v[204:207], v[92:95], v[156:159]\n\t" \
  "v_mfma_f32_16x16x32_bf16 v[144:147], v[208:211], v[96:99], v[144:147]\n\t" \
  "v_mfma_f32_16x16x32_bf16 v[152:155], v[208:211], v[100:103], v[152:155]\n\t" \
  "v_mfma_f32_16x16x32_bf16 v[148:151], v[212:215], v[96:99], v[148:151]\n\t" \
  "v_mfma_f32_16x16x32_bf16 v[156:159], v[212:215], v[100:103], v[156:159]\n\t" \
  "v_mfma_f32_16x16x32_bf16 v[144:147], v[216:219], v[104:107], v[144:147]\n\t" \
  "v_mfma_f32_16x16x32_bf16 v[152:155], v[216:219], v[108:111], v[152:155]\n\t" \
  "v_mfma_f32_16x16x32_bf16 v[148:151], v[220:223], v[104:107], v[148:151]\n\t" \
  "v_mfma_f32_16x16x32_bf16 v[156:159], v[220:223], v[108:111], v[156:159]\n\t" \
  "v_mfma_f32_16x16x32_bf16 v[144:147], v[224:227], v[112:115], v[144:147]\n\t" \
  "v_mfma_f32_16x16x32_bf16 v[152:155], v[224:227], v[116:119], v[152:155]\n\t" \
  "v_mfma_f32_16x16x32_bf16 v[148:151], v[228:231], v[112:115], v[148:151]\n\t" \
  "v_mfma_f32_16x16x32_bf16 v[156:159], v[228:231], v[116:119], v[156:159]\n\t" \
  "v_mfma_f32_16x16x32_bf16 v[144:147], v[232:235], v[120:123], v[144:147]\n\t" \
  "v_mfma_f32_16x16x32_bf16 v[152:155], v[232:235], v[124:127], v[152:155]\n\t" \
  "v_mfma_f32_16x16x32_bf16 v[148:151], v[236:239], v[120:123], v[148:151]\n\t" \
  "v_mfma_f32_16x16x32_bf16 v[156:159], v[236:239], v[124:127], v[156:159]\n\t" \
  "s_setprio 0\n\t" \
  "s_cmp_lt_u32 s86, 64\n\t" \
  "s_cbranch_scc1 .Lp0_%=\n\t" \
  "s_barrier\n\t" \
  "s_nop 1\n\t" \
  "ds_write_b128 v252, v[152:155] offset:0\n\t" \
  "s_nop 0\n\t" \
  "ds_write_b128 v252, v[156:159] offset:1024\n\t" \
  "s_waitcnt lgkmcnt(0)\n\t" \
  "s_barrier\n\t" \
  "ds_read_b128 v[160:163], v253 offset:0\n\t" \
  "ds_read_b128 v[164:167], v253 offset:1024\n\t" \
  "s_waitcnt lgkmcnt(0)\n\t" \
  "s_lshr_b32 s90, s87, 7\n\t" \
  "s_xor_b32 s90, s90, %[s0]\n\t" \
  "s_and_b32 s90, s90, 1\n\t" \
  "s_lshl1_add_u32 s90, s87, s90\n\t" \
  "s_lshl_b32 s90, s90, 13\n\t" \
  "v_add_f32 v160, v160, v144\n\t" \
  "v_add_f32 v161, v161, v145\n\t" \
  "v_add_f32 v162, v162, v146\n\t" \
  "v_add_f32 v163, v163, v147\n\t" \
  "v_add_f32 v164, v164, v148\n\t" \
  "v_add_f32 v165, v165, v149\n\t" \
  "v_add_f32 v166, v166, v150\n\t" \
  "v_add_f32 v167, v167, v151\n\t" \
  "v_cvt_pk_bf16_f32 v168, v160, v161\n\t" \
  "v_cvt_pk_bf16_f32 v169, v162, v163\n\t" \
  "v_cvt_pk_bf16_f32 v170, v164, v165\n\t" \
  "v_cvt_pk_bf16_f32 v171, v166, v167\n\t" \
  "buffer_store_dwordx4 v[168:171], v172, %[rt], s90 offen sc1\n\t" \
  "s_waitcnt vmcnt(0)\n\t" \
  "s_barrier\n\t" \
  "s_mov_b32 m0, s94\n\t" \
  "s_branch .Le_%=\n\t" \
  ".Lk_%=:\n\t" \
  "s_mov_b32 s94, m0\n\t" \
  "s_mov_b64 s[92:93], %[x0]\n\t" \
  "s_mov_b32 s86, %[g0]\n\t" \
  "s_add_u32 s87, s86, %[t0]\n\t" \
  "s_add_u32 m0, %[lw], 67584\n\t" \
  "s_nop 0\n\t" \
  "global_load_lds_dwordx4 v250, s[92:93] sc1\n\t" \
  "s_add_u32 m0, %[lw], 68608\n\t" \
  "s_nop 0\n\t" \
  "global_load_lds_dwordx4 v251, s[92:93] sc1\n\t" \
  "s_barrier\n\t" \
  "s_branch .Ls1_%=\n\t" \
  ".Lp1_%=:\n\t" \
  "s_waitcnt vmcnt(10)\n\t" \
  "s_barrier\n\t" \
  "s_setprio 2\n\t" \
  "v_mfma_f32_16x16x32_bf16 v[144:147], v[176:179], v[64:67], v[144:147]\n\t" \
  "v_mfma_f32_16x16x32_bf16 v[152:155], v[176:179], v[68:71], v[152:155]\n\t" \
  "v_mfma_f32_16x16x32_bf16 v[148:151], v[180:183], v[64:67], v[148:151]\n\t" \
  "v_mfma_f32_16x16x32_bf16 v[156:159], v[180:183], v[68:71], v[156:159]\n\t" \
  "v_mfma_f32_16x16x32_bf16 v[144:147], v[184:187], v[72:75], v[144:147]\n\t" \
  "v_mfma_f32_16x16x32_bf16 v[152:155], v[184:187], v[76:79], v[152:155]\n\t" \
  "v_mfma_f32_16x16x32_bf16 v[148:151], v[188:191], v[72:75], v[148:151]\n\t" \
  "v_mfma_f32_16x16x32_bf16 v[156:159], v[188:191], v[76:79], v[156:159]\n\t" \
  "v_mfma_f32_16x16x32_bf16 v[144:147], v[192:195], v[80:83], v[144:147]\n\t" \
  "v_mfma_f32_16x16x32_bf16 v[152:155], v[192:195], v[84:87], v[152:155]\n\t" \
  "v_mfma_f32_16x16x32_bf16 v[148:151], v[196:199], v[80:83], v[148:151]\n\t" \
  "v_mfma_f32_16x16x32_bf16 v[156:159], v[196:199], v[84:87], v[156:159]\n\t" \
  "v_mfma_f32_16x16x32_bf16 v[144:147], v[200:203], v[88:91], v[144:147]\n\t" \
  "v_mfma_f32_16x16x32_bf16 v[152:155], v[200:203], v[92:95], v[152:155]\n\t" \
  "v_mfma_f32_16x16x32_bf16 v[148:151], v[204:207], v[88:91], v[148:151]\n\t" \
  "v_mfma_f32_16x16x32_bf16 v[156:159], v[204:207], v[92:95], v[156:159]\n\t" \
  "v_mfma_f32_16x16x32_bf16 v[144:147], v[208:211], v[96:99], v[144:147]\n\t" \
  "v_mfma_f32_16x16x32_bf16 v[152:155], v[208:211], v[100:103], v[152:155]\n\t" \
  "v_mfma_f32_16x16x32_bf16 v[148:151], v[212:215], v[96:99], v[148:151]\n\t" \
  "v_mfma_f32_16x16x32_bf16 v[156:159], v[212:215], v[100:103], v[156:159]\n\t" \
  "v_mfma_f32_16x16x32_bf16 v[144:147], v[216:219], v[104:107], v[144:147]\n\t" \
  "v_mfma_f32_16x16x32_bf16 v[152:155], v[216:219], v[108:111], v[152:155]\n\t" \
  "v_mfma_f32_16x16x32_bf16 v[148:151], v[220:223], v[104:107], v[148:151]\n\t" \
  "v_mfma_f32_16x16x32_bf16 v[156:159], v[220:223], v[108:111], v[156:159]\n\t" \
  "v_mfma_f32_16x16x32_bf16 v[144:147], v[224:227], v[112:115], v[144:147]\n\t" \
  "v_mfma_f32_16x16x32_bf16 v[152:155], v[224:227], v[116:119], v[152:155]\n\t" \
  "v_mfma_f32_16x16x32_bf16 v[148:151], v[228:231], v[112:115], v[148:151]\n\t" \
  "v_mfma_f32_16x16x32_bf16 v[156:159], v[228:231], v[116:119], v[156:159]\n\t" \
  "v_mfma_f32_16x16x32_bf16 v[144:147], v[232:235], v[120:123], v[144:147]\n\t" \
  "v_mfma_f32_16x16x32_bf16 v[152:155], v[232:235], v[124:127], v[152:155]\n\t" \
  "v_mfma_f32_16x16x32_bf16 v[148:151], v[236:239], v[120:123], v[148:151]\n\t" \
  "v_mfma_f32_16x16x32_bf16 v[156:159], v[236:239], v[124:127], v[156:159]\n\t" \
  "s_setprio 0\n\t" \
  ".Ls1_%=:\n\t" \
  "s_add_u32 s92, s92, 512\n\t" \
  "s_addc_u32 s93, s93, 0\n\t" \
  "ds_read_b128 v[176:179], v240 offset:0\n\t" \
  "ds_read_b128 v[180:183], v240 offset:16384\n\t" \
  "ds_read_b128 v[184:187], v241 offset:0\n\t" \
  "ds_read_b128 v[188:191], v241 offset:16384\n\t" \
  "s_add_u32 m0, %[lw], 98304\n\t" \
  "s_nop 0\n\t" \
  "global_load_lds_dwordx4 v248, s[92:93] sc1\n\t" \
  "ds_read_b128 v[192:195], v242 offset:0\n\t" \
  "ds_read_b128 v[196:199], v242 offset:16384\n\t" \
  "ds_read_b128 v[200:203], v243 offset:0\n\t" \
  "ds_read_b128 v[204:207], v243 offset:16384\n\t" \
  "s_add_u32 m0, %[lw], 99328\n\t" \
  "s_nop 0\n\t" \
  "global_load_lds_dwordx4 v249, s[92:93] sc1\n\t" \
  "ds_read_b128 v[208:211], v240 offset:256\n\t" \
  "ds_read_b128 v[212:215], v240 offset:16640\n\t" \
  "ds_read_b128 v[216:219], v241 offset:256\n\t" \
  "ds_read_b128 v[220:223], v241 offset:16640\n\t" \
  "s_add_u32 m0, %[lw], 100352\n\t" \
  "s_nop 0\n\t" \
  "global_load_lds_dwordx4 v250, s[92:93] sc1\n\t" \
  "ds_read_b128 v[224:227], v242 offset:256\n\t" \
  "ds_read_b128 v[228:231], v242 offset:16640\n\t" \
  "ds_read_b128 v[232:235], v243 offset:256\n\t" \
  "ds_read_b128 v[236:239], v243 offset:16640\n\t" \
  "s_add_u32 m0, %[lw], 101376\n\t" \
  "s_nop 0\n\t" \
  "global_load_lds_dwordx4 v251, s[92:93] sc1\n\t" \
  "ds_write_b128 v252, v[152:155] offset:0\n\t" \
  "ds_write_b128 v252, v[156:159] offset:1024\n\t" \
  "s_waitcnt lgkmcnt(0)\n\t" \
  "s_waitcnt vmcnt(4)\n\t" \
  "s_barrier\n\t" \
  "s_setprio 2\n\t" \
  "v_mfma_f32_16x16x32_bf16 v[128:131], v[176:179], v[0:3], 0\n\t" \
  "v_mfma_f32_16x16x32_bf16 v[136:139], v[176:179], v[4:7], 0\n\t" \
  "v_mfma_f32_16x16x32_bf16 v[132:135], v[180:183], v[0:3], 0\n\t" \
  "v_mfma_f32_16x16x32_bf16 v[140:143], v[180:183], v[4:7], 0\n\t" \
  "v_mfma_f32_16x16x32_bf16 v[128:131], v[184:187], v[8:11], v[128:131]\n\t" \
  "v_mfma_f32_16x16x32_bf16 v[136:139], v[184:187], v[12:15], v[136:139]\n\t" \
  "v_mfma_f32_16x16x32_bf16 v[132:135], v[188:191], v[8:11], v[132:135]\n\t" \
  "v_mfma_f32_16x16x32_bf16 v[140:143], v[188:191], v[12:15], v[140:143]\n\t" \
  "v_mfma_f32_16x16x32_bf16 v[128:131], v[192:195], v[16:19], v[128:131]\n\t" \
  "v_mfma_f32_16x16x32_bf16 v[136:139], v[192:195], v[20:23], v[136:139]\n\t" \
  "v_mfma_f32_16x16x32_bf16 v[132:135], v[196:199], v[16:19], v[132:135]\n\t" \
  "v_mfma_f32_16x16x32_bf16 v[140:143], v[196:199], v[20:23], v[140:143]\n\t" \
  "v_mfma_f32_16x16x32_bf16 v[128:131], v[200:203], v[24:27], v[128:131]\n\t" \
  "v_mfma_f32_16x16x32_bf16 v[136:139], v[200:203], v[28:31], v[136:139]\n\t" \
  "v_mfma_f32_16x16x32_bf16 v[132:135], v[204:207], v[24:27], v[132:135]\n\t" \
  "v_mfma_f32_16x16x32_bf16 v[140:143], v[204:207], v[28:31], v[140:143]\n\t" \
  "v_mfma_f32_16x16x32_bf16 v[128:131], v[208:211], v[32:35], v[128:131]\n\t" \
  "v_mfma_f32_16x16x32_bf16 v[136:139], v[208:211], v[36:39], v[136:139]\n\t" \
  "v_mfma_f32_16x16x32_bf16 v[132:135], v[212:215], v[32:35], v[132:135]\n\t" \
  "v_mfma_f32_16x16x32_bf16 v[140:143], v[212:215], v[36:39], v[140:143]\n\t" \
  "v_mfma_f32_16x16x32_bf16 v[128:131], v[216:219], v[40:43], v[128:131]\n\t" \
  "v_mfma_f32_16x16x32_bf16 v[136:139], v[216:219], v[44:47], v[136:139]\n\t" \
  "v_mfma_f32_16x16x32_bf16 v[132:135], v[220:223], v[40:43], v[132:135]\n\t" \
  "v_mfma_f32_16x16x32_bf16 v[140:143], v[220:223], v[44:47], v[140:143]\n\t" \
  "v_mfma_f32_16x16x32_bf16 v[128:131], v[224:227], v[48:51], v[128:131]\n\t" \
  "v_mfma_f32_16x16x32_bf16 v[136:139], v[224:227], v[52:55], v[136:139]\n\t" \
  "v_mfma_f32_16x16x32_bf16 v[132:135], v[228:231], v[48:51], v[132:135]\n\t" \
  "v_mfma_f32_16x16x32_bf16 v[140:143], v[228:231], v[52:55], v[140:143]\n\t" \
  "v_mfma_f32_16x16x32_bf16 v[128:131], v[232:235], v[56:59], v[128:131]\n\t" \
  "v_mfma_f32_16x16x32_bf16 v[136:139], v[232:235], v[60:63], v[136:139]\n\t" \
  "v_mfma_f32_16x16x32_bf16 v[132:135], v[236:239], v[56:59], v[132:135]\n\t" \
  "v_mfma_f32_16x16x32_bf16 v[140:143], v[236:239], v[60:63], v[140:143]\n\t" \
  "s_setprio 0\n\t" \
  "s_add_u32 s92, s92, 65024\n\t" \
  "s_addc_u32 s93, s93, 0\n\t" \
  "ds_read_b128 v[160:163], v253 offset:0\n\t" \
  "ds_read_b128 v[164:167], v253 offset:1024\n\t" \
  "ds_read_b128 v[176:179], v240 offset:32768\n\t" \
  "ds_read_b128 v[180:183], v240 offset:49152\n\t" \
  "ds_read_b128 v[184:187], v241 offset:32768\n\t" \
  "ds_read_b128 v[188:191], v241 offset:49152\n\t" \
  "s_add_u32 m0, %[lw], 0\n\t" \
  "s_nop 0\n\t" \
  "global_load_lds_dwordx4 v248, s[92:93] sc1\n\t" \
  "ds_read_b128 v[192:195], v242 offset:32768\n\t" \
  "ds_read_b128 v[196:199], v242 offset:49152\n\t" \
  "ds_read_b128 v[200:203], v243 offset:32768\n\t" \
  "ds_read_b128 v[204:207], v243 offset:49152\n\t" \
  "s_add_u32 m0, %[lw], 1024\n\t" \
  "s_nop 0\n\t" \
  "global_load_lds_dwordx4 v249, s[92:93] sc1\n\t" \
  "ds_read_b128 v[208:211], v240 offset:33024\n\t" \
  "ds_read_b128 v[212:215], v240 offset:49408\n\t" \
  "ds_read_b128 v[216:219], v241 offset:33024\n\t" \
  "ds_read_b128 v[220:223], v241 offset:49408\n\t" \
  "s_add_u32 m0, %[lw], 2048\n\t" \
  "s_nop 0\n\t" \
  "global_load_lds_dwordx4 v250, s[92:93] sc1\n\t" \
  "ds_read_b128 v[224:227], v242 offset:33024\n\t" \
  "ds_read_b128 v[228:231], v242 offset:49408\n\t" \
  "ds_read_b128 v[232:235], v243 offset:33024\n\t" \
  "ds_read_b128 v[236:239], v243 offset:49408\n\t" \
  "s_add_u32 m0, %[lw], 3072\n\t" \
  "s_nop 0\n\t" \
  "global_load_lds_dwordx4 v251, s[92:93] sc1\n\t" \
  "s_waitcnt lgkmcnt(15)\n\t" \
  "s_waitcnt lgkmcnt(12)\n\t" \
  "s_sub_i32 s91, s86, 2\n\t" \
  "v_mov_b32 v173, s91\n\t" \
  "buffer_store_dword v173, v174, %[rs], 0 offen sc1\n\t" \
  "s_lshr_b32 s90, s87, 7\n\t" \
  "s_xor_b32 s90, s90, %[s0]\n\t" \
  "s_and_b32 s90, s90, 1\n\t" \
  "s_lshl1_add_u32 s90, s87, s90\n\t" \
  "s_lshl_b32 s90, s90, 13\n\t" \
  "v_add_f32 v160, v160, v144\n\t" \
  "v_add_f32 v161, v161, v145\n\t" \
  "v_add_f32 v162, v162, v146\n\t" \
  "v_add_f32 v163, v163, v147\n\t" \
  "v_add_f32 v164, v164, v148\n\t" \
  "v_add_f32 v165, v165, v149\n\t" \
  "v_add_f32 v166, v166, v150\n\t" \
  "v_add_f32 v167, v167, v151\n\t" \
  "v_cvt_pk_bf16_f32 v168, v160, v161\n\t" \
  "v_cvt_pk_bf16_f32 v169, v162, v163\n\t" \
  "v_cvt_pk_bf16_f32 v170, v164, v165\n\t" \
  "v_cvt_pk_bf16_f32 v171, v166, v167\n\t" \
  "buffer_store_dwordx4 v[168:171], v172, %[rt], s90 offen sc1\n\t" \
  "s_add_u32 s86, s86, 1\n\t" \
  "s_add_u32 s87, s87, 1\n\t" \
  "s_waitcnt lgkmcnt(0)\n\t" \
  "s_waitcnt vmcnt(10)\n\t" \
  "s_barrier\n\t" \
  "s_setprio 2\n\t" \
  "v_mfma_f32_16x16x32_bf16 v[128:131], v[176:179], v[64:67], v[128:131]\n\t" \
  "v_mfma_f32_16x16x32_bf16 v[136:139], v[176:179], v[68:71], v[136:139]\n\t" \
  "v_mfma_f32_16x16x32_bf16 v[132:135], v[180:183], v[64:67], v[132:135]\n\t" \
  "v_mfma_f32_16x16x32_bf16 v[140:143], v[180:183], v[68:71], v[140:143]\n\t" \
  "v_mfma_f32_16x16x32_bf16 v[128:131], v[184:187], v[72:75], v[128:131]\n\t" \
  "v_mfma_f32_16x16x32_bf16 v[136:139], v[184:187], v[76:79], v[136:139]\n\t" \
  "v_mfma_f32_16x16x32_bf16 v[132:135], v[188:191], v[72:75], v[132:135]\n\t" \
  "v_mfma_f32_16x16x32_bf16 v[140:143], v[188:191], v[76:79], v[140:143]\n\t" \
  "v_mfma_f32_16x16x32_bf16 v[128:131], v[192:195], v[80:83], v[128:131]\n\t" \
  "v_mfma_f32_16x16x32_bf16 v[136:139], v[192:195], v[84:87], v[136:139]\n\t" \
  "v_mfma_f32_16x16x32_bf16 v[132:135], v[196:199], v[80:83], v[132:135]\n\t" \
  "v_mfma_f32_16x16x32_bf16 v[140:143], v[196:199], v[84:87], v[140:143]\n\t" \
  "v_mfma_f32_16x16x32_bf16 v[128:131], v[200:203], v[88:91], v[128:131]\n\t" \
  "v_mfma_f32_16x16x32_bf16 v[136:139], v[200:203], v[92:95], v[136:139]\n\t" \
  "v_mfma_f32_16x16x32_bf16 v[132:135], v[204:207], v[88:91], v[132:135]\n\t" \
  "v_mfma_f32_16x16x32_bf16 v[140:143], v[204:207], v[92:95], v[140:143]\n\t" \
  "v_mfma_f32_16x16x32_bf16 v[128:131], v[208:211], v[96:99], v[128:131]\n\t" \
  "v_mfma_f32_16x16x32_bf16 v[136:139], v[208:211], v[100:103], v[136:139]\n\t" \
  "v_mfma_f32_16x16x32_bf16 v[132:135], v[212:215], v[96:99], v[132:135]\n\t" \
  "v_mfma_f32_16x16x32_bf16 v[140:143], v[212:215], v[100:103], v[140:143]\n\t" \
  "v_mfma_f32_16x16x32_bf16 v[128:131], v[216:219], v[104:107], v[128:131]\n\t" \
  "v_mfma_f32_16x16x32_bf16 v[136:139], v[216:219], v[108:111], v[136:139]\n\t" \
  "v_mfma_f32_16x16x32_bf16 v[132:135], v[220:223], v[104:107], v[132:135]\n\t" \
  "v_mfma_f32_16x16x32_bf16 v[140:143], v[220:223], v[108:111], v[140:143]\n\t" \
  "v_mfma_f32_16x16x32_bf16 v[128:131], v[224:227], v[112:115], v[128:131]\n\t" \
  "v_mfma_f32_16x16x32_bf16 v[136:139], v[224:227], v[116:119], v[136:139]\n\t" \
  "v_mfma_f32_16x16x32_bf16 v[132:135], v[228:231], v[112:115], v[132:135]\n\t" \
  "v_mfma_f32_16x16x32_bf16 v[140:143], v[228:231], v[116:119], v[140:143]\n\t" \
  "v_mfma_f32_16x16x32_bf16 v[128:131], v[232:235], v[120:123], v[128:131]\n\t" \
  "v_mfma_f32_16x16x32_bf16 v[136:139], v[232:235], v[124:127], v[136:139]\n\t" \
  "v_mfma_f32_16x16x32_bf16 v[132:135], v[236:239], v[120:123], v[132:135]\n\t" \
  "v_mfma_f32_16x16x32_bf16 v[140:143], v[236:239], v[124:127], v[140:143]\n\t" \
  "s_setprio 0\n\t" \
  "s_add_u32 s92, s92, 512\n\t" \
  "s_addc_u32 s93, s93, 0\n\t" \
  "ds_read_b128 v[176:179], v244 offset:0\n\t" \
  "ds_read_b128 v[180:183], v244 offset:16384\n\t" \
  "ds_read_b128 v[184:187], v245 offset:0\n\t" \
  "ds_read_b128 v[188:191], v245 offset:16384\n\t" \
  "s_add_u32 m0, %[lw], 32768\n\t" \
  "s_nop 0\n\t" \
  "global_load_lds_dwordx4 v248, s[92:93] sc1\n\t" \
  "ds_read_b128 v[192:195], v246 offset:0\n\t" \
  "ds_read_b128 v[196:199], v246 offset:16384\n\t" \
  "ds_read_b128 v[200:203], v247 offset:0\n\t" \
  "ds_read_b128 v[204:207], v247 offset:16384\n\t" \
  "s_add_u32 m0, %[lw], 33792\n\t" \
  "s_nop 0\n\t" \
  "global_load_lds_dwordx4 v249, s[92:93] sc1\n\t" \
  "ds_read_b128 v[208:211], v244 offset:256\n\t" \
  "ds_read_b128 v[212:215], v244 offset:16640\n\t" \
  "ds_read_b128 v[216:219], v245 offset:256\n\t" \
  "ds_read_b128 v[220:223], v245 offset:16640\n\t" \
  "s_add_u32 m0, %[lw], 34816\n\t" \
  "s_nop 0\n\t" \
  "global_load_lds_dwordx4 v250, s[92:93] sc1\n\t" \
  "ds_read_b128 v[224:227], v246 offset:256\n\t" \
  "ds_read_b128 v[228:231], v246 offset:16640\n\t" \
  "ds_read_b128 v[232:235], v247 offset:256\n\t" \
  "ds_read_b128 v[236:239], v247 offset:16640\n\t" \
  "s_add_u32 m0, %[lw], 35840\n\t" \
  "s_nop 0\n\t" \
  "global_load_lds_dwordx4 v251, s[92:93] sc1\n\t" \
  "ds_write_b128 v252, v[136:139] offset:0\n\t" \
  "ds_write_b128 v252, v[140:143] offset:1024\n\t" \
  "s_waitcnt lgkmcnt(0)\n\t" \
  "s_waitcnt vmcnt(4)\n\t" \
  "s_barrier\n\t" \
  "s_setprio 2\n\t" \
  "v_mfma_f32_16x16x32_bf16 v[144:147], v[176:179], v[0:3], 0\n\t" \
  "v_mfma_f32_16x16x32_bf16 v[152:155], v[176:179], v[4:7], 0\n\t" \
  "v_mfma_f32_16x16x32_bf16 v[148:151], v[180:183], v[0:3], 0\n\t" \
  "v_mfma_f32_16x16x32_bf16 v[156:159], v[180:183], v[4:7], 0\n\t" \
  "v_mfma_f32_16x16x32_bf16 v[144:147], v[184:187], v[8:11], v[144:147]\n\t" \
  "v_mfma_f32_16x16x32_bf16 v[152:155], v[184:187], v[12:15], v[152:155]\n\t" \
  "v_mfma_f32_16x16x32_bf16 v[148:151], v[188:191], v[8:11], v[148:151]\n\t" \
  "v_mfma_f32_16x16x32_bf16 v[156:159], v[188:191], v[12:15], v[156:159]\n\t" \
  "v_mfma_f32_16x16x32_bf16 v[144:147], v[192:195], v[16:19], v[144:147]\n\t" \
  "v_mfma_f32_16x16x32_bf16 v[152:155], v[192:195], v[20:23], v[152:155]\n\t" \
  "v_mfma_f32_16x16x32_bf16 v[148:151], v[196:199], v[16:19], v[148:151]\n\t" \
  "v_mfma_f32_16x16x32_bf16 v[156:159], v[196:199], v[20:23], v[156:159]\n\t" \
  "v_mfma_f32_16x16x32_bf16 v[144:147], v[200:203], v[24:27], v[144:147]\n\t" \
  "v_mfma_f32_16x16x32_bf16 v[152:155], v[200:203], v[28:31], v[152:155]\n\t" \
  "v_mfma_f32_16x16x32_bf16 v[148:151], v[204:207], v[24:27], v[148:151]\n\t" \
  "v_mfma_f32_16x16x32_bf16 v[156:159], v[204:207], v[28:31], v[156:159]\n\t" \
  "v_mfma_f32_16x16x32_bf16 v[144:147], v[208:211], v[32:35], v[144:147]\n\t" \
  "v_mfma_f32_16x16x32_bf16 v[152:155], v[208:211], v[36:39], v[152:155]\n\t" \
  "v_mfma_f32_16x16x32_bf16 v[148:151], v[212:215], v[32:35], v[148:151]\n\t" \
  "v_mfma_f32_16x16x32_bf16 v[156:159], v[212:215], v[36:39], v[156:159]\n\t" \
  "v_mfma_f32_16x16x32_bf16 v[144:147], v[216:219], v[40:43], v[144:147]\n\t" \
  "v_mfma_f32_16x16x32_bf16 v[152:155], v[216:219], v[44:47], v[152:155]\n\t" \
  "v_mfma_f32_16x16x32_bf16 v[148:151], v[220:223], v[40:43], v[148:151]\n\t" \
  "v_mfma_f32_16x16x32_bf16 v[156:159], v[220:223], v[44:47], v[156:159]\n\t" \
  "v_mfma_f32_16x16x32_bf16 v[144:147], v[224:227], v[48:51], v[144:147]\n\t" \
  "v_mfma_f32_16x16x32_bf16 v[152:155], v[224:227], v[52:55], v[152:155]\n\t" \
  "v_mfma_f32_16x16x32_bf16 v[148:151], v[228:231], v[48:51], v[148:151]\n\t" \
  "v_mfma_f32_16x16x32_bf16 v[156:159], v[228:231], v[52:55], v[156:159]\n\t" \
  "v_mfma_f32_16x16x32_bf16 v[144:147], v[232:235], v[56:59], v[144:147]\n\t" \
  "v_mfma_f32_16x16x32_bf16 v[152:155], v[232:235], v[60:63], v[152:155]\n\t" \
  "v_mfma_f32_16x16x32_bf16 v[148:151], v[236:239], v[56:59], v[148:151]\n\t" \
  "v_mfma_f32_16x16x32_bf16 v[156:159], v[236:239], v[60:63], v[156:159]\n\t" \
  "s_setprio 0\n\t" \
  "s_add_u32 s92, s92, 65024\n\t" \
  "s_addc_u32 s93, s93, 0\n\t" \
  "ds_read_b128 v[160:163], v253 offset:0\n\t" \
  "ds_read_b128 v[164:167], v253 offset:1024\n\t" \
  "ds_read_b128 v[176:179], v244 offset:32768\n\t" \
  "ds_read_b128 v[180:183], v244 offset:49152\n\t" \
  "ds_read_b128 v[184:187], v245 offset:32768\n\t" \
  "ds_read_b128 v[188:191], v245 offset:49152\n\t" \
  "s_add_u32 m0, %[lw], 65536\n\t" \
  "s_nop 0\n\t" \
  "global_load_lds_dwordx4 v248, s[92:93] sc1\n\t" \
  "ds_read_b128 v[192:195], v246 offset:32768\n\t" \
  "ds_read_b128 v[196:199], v246 offset:49152\n\t" \
  "ds_read_b128 v[200:203], v247 offset:32768\n\t" \
  "ds_read_b128 v[204:207], v247 offset:49152\n\t" \
  "s_add_u32 m0, %[lw], 66560\n\t" \
  "s_nop 0\n\t" \
  "global_load_lds_dwordx4 v249, s[92:93] sc1\n\t" \
  "ds_read_b128 v[208:211], v244 offset:33024\n\t" \
  "ds_read_b128 v[212:215], v244 offset:49408\n\t" \
  "ds_read_b128 v[216:219], v245 offset:33024\n\t" \
  "ds_read_b128 v[220:223], v245 offset:49408\n\t" \
  "s_add_u32 m0, %[lw], 67584\n\t" \
  "s_nop 0\n\t" \
  "global_load_lds_dwordx4 v250, s[92:93] sc1\n\t" \
  "ds_read_b128 v[224:227], v246 offset:33024\n\t" \
  "ds_read_b128 v[228:231], v246 offset:49408\n\t" \
  "ds_read_b128 v[232:235], v247 offset:33024\n\t" \
  "ds_read_b128 v[236:239], v247 offset:49408\n\t" \
  "s_add_u32 m0, %[lw], 68608\n\t" \
  "s_nop 0\n\t" \
  "global_load_lds_dwordx4 v251, s[92:93] sc1\n\t" \
  "s_waitcnt lgkmcnt(15)\n\t" \
  "s_waitcnt lgkmcnt(12)\n\t" \
  "s_sub_i32 s91, s86, 2\n\t" \
  "v_mov_b32 v173, s91\n\t" \
  "buffer_store_dword v173, v174, %[rs], 0 offen sc1\n\t" \
  "s_lshr_b32 s90, s87, 7\n\t" \
  "s_xor_b32 s90, s90, %[s0]\n\t" \
  "s_and_b32 s90, s90, 1\n\t" \
  "s_lshl1_add_u32 s90, s87, s90\n\t" \
  "s_lshl_b32 s90, s90, 13\n\t" \
  "v_add_f32 v160, v160, v128\n\t" \
  "v_add_f32 v161, v161, v129\n\t" \
  "v_add_f32 v162, v162, v130\n\t" \
  "v_add_f32 v163, v163, v131\n\t" \
  "v_add_f32 v164, v164, v132\n\t" \
  "v_add_f32 v165, v165, v133\n\t" \
  "v_add_f32 v166, v166, v134\n\t" \
  "v_add_f32 v167, v167, v135\n\t" \
  "v_cvt_pk_bf16_f32 v168, v160, v161\n\t" \
  "v_cvt_pk_bf16_f32 v169, v162, v163\n\t" \
  "v_cvt_pk_bf16_f32 v170, v164, v165\n\t" \
  "v_cvt_pk_bf16_f32 v171, v166, v167\n\t" \
  "buffer_store_dwordx4 v[168:171], v172, %[rt], s90 offen sc1\n\t" \
  "s_add_u32 s86, s86, 1\n\t" \
  "s_add_u32 s87, s87, 1\n\t" \
  "s_waitcnt lgkmcnt(0)\n\t" \
  "s_cmp_lt_u32 s86, 64\n\t" \
  "s_cbranch_scc1 .Lp1_%=\n\t" \
  "s_barrier\n\t" \
  "s_setprio 2\n\t" \
  "v_mfma_f32_16x16x32_bf16 v[144:147], v[176:179], v[64:67], v[144:147]\n\t" \
  "v_mfma_f32_16x16x32_bf16 v[152:155], v[176:179], v[68:71], v[152:155]\n\t" \
  "v_mfma_f32_16x16x32_bf16 v[148:151], v[180:183], v[64:67], v[148:151]\n\t" \
  "v_mfma_f32_16x16x32_bf16 v[156:159], v[180:183], v[68:71], v[156:159]\n\t" \
  "v_mfma_f32_16x16x32_bf16 v[144:147], v[184:187], v[72:75], v[144:147]\n\t" \
  "v_mfma_f32_16x16x32_bf16 v[152:155], v[184:187], v[76:79], v[152:155]\n\t" \
  "v_mfma_f32_16x16x32_bf16 v[148:151], v[188:191], v[72:75], v[148:151]\n\t" \
  "v_mfma_f32_16x16x32_bf16 v[156:159], v[188:191], v[76:79], v[156:159]\n\t" \
  "v_mfma_f32_16x16x32_bf16 v[144:147], v[192:195], v[80:83], v[144:147]\n\t" \
  "v_mfma_f32_16x16x32_bf16 v[152:155], v[192:195], v[84:87], v[152:155]\n\t" \
  "v_mfma_f32_16x16x32_bf16 v[148:151], v[196:199], v[80:83], v[148:151]\n\t" \
  "v_mfma_f32_16x16x32_bf16 v[156:159], v[196:199], v[84:87], v[156:159]\n\t" \
  "v_mfma_f32_16x16x32_bf16 v[144:147], v[200:203], v[88:91], v[144:147]\n\t" \
  "v_mfma_f32_16x16x32_bf16 v[152:155], v[200:203], v[92:95], v[152:155]\n\t" \
  "v_mfma_f32_16x16x32_bf16 v[148:151], v[204:207], v[88:91], v[148:151]\n\t" \
  "v_mfma_f32_16x16x32_bf16 v[156:159], v[204:207], v[92:95], v[156:159]\n\t" \
  "v_mfma_f32_16x16x32_bf16 v[144:147], v[208:211], v[96:99], v[144:147]\n\t" \
  "v_mfma_f32_16x16x32_bf16 v[152:155], v[208:211], v[100:103], v[152:155]\n\t" \
  "v_mfma_f32_16x16x32_bf16 v[148:151], v[212:215], v[96:99], v[148:151]\n\t" \
  "v_mfma_f32_16x16x32_bf16 v[156:159], v[212:215], v[100:103], v[156:159]\n\t" \
  "v_mfma_f32_16x16x32_bf16 v[144:147], v[216:219], v[104:107], v[144:147]\n\t" \
  "v_mfma_f32_16x16x32_bf16 v[152:155], v[216:219], v[108:111], v[152:155]\n\t" \
  "v_mfma_f32_16x16x32_bf16 v[148:151], v[220:223], v[104:107], v[148:151]\n\t" \
  "v_mfma_f32_16x16x32_bf16 v[156:159], v[220:223], v[108:111], v[156:159]\n\t" \
  "v_mfma_f32_16x16x32_bf16 v[144:147], v[224:227], v[112:115], v[144:147]\n\t" \
  "v_mfma_f32_16x16x32_bf16 v[152:155], v[224:227], v[116:119], v[152:155]\n\t" \
  "v_mfma_f32_16x16x32_bf16 v[148:151], v[228:231], v[112:115], v[148:151]\n\t" \
  "v_mfma_f32_16x16x32_bf16 v[156:159], v[228:231], v[116:119], v[156:159]\n\t" \
  "v_mfma_f32_16x16x32_bf16 v[144:147], v[232:235], v[120:123], v[144:147]\n\t" \
  "v_mfma_f32_16x16x32_bf16 v[152:155], v[232:235], v[124:127], v[152:155]\n\t" \
  "v_mfma_f32_16x16x32_bf16 v[148:151], v[236:239], v[120:123], v[148:151]\n\t" \
  "v_mfma_f32_16x16x32_bf16 v[156:159], v[236:239], v[124:127], v[156:159]\n\t" \
  "s_setprio 0\n\t" \
  "s_nop 4\n\t" \
  "ds_write_b128 v252, v[152:155] offset:0\n\t" \
  "s_nop 0\n\t" \
  "ds_write_b128 v252, v[156:159] offset:1024\n\t" \
  "s_waitcnt lgkmcnt(0)\n\t" \
  "s_barrier\n\t" \
  "ds_read_b128 v[160:163], v253 offset:0\n\t" \
  "ds_read_b128 v[164:167], v253 offset:1024\n\t" \
  "s_waitcnt lgkmcnt(0)\n\t" \
  "s_lshr_b32 s90, s87, 7\n\t" \
  "s_xor_b32 s90, s90, %[s0]\n\t" \
  "s_and_b32 s90, s90, 1\n\t" \
  "s_lshl1_add_u32 s90, s87, s90\n\t" \
  "s_lshl_b32 s90, s90, 13\n\t" \
  "v_add_f32 v160, v160, v144\n\t" \
  "v_add_f32 v161, v161, v145\n\t" \
  "v_add_f32 v162, v162, v146\n\t" \
  "v_add_f32 v163, v163, v147\n\t" \
  "v_add_f32 v164, v164, v148\n\t" \
  "v_add_f32 v165, v165, v149\n\t" \
  "v_add_f32 v166, v166, v150\n\t" \
  "v_add_f32 v167, v167, v151\n\t" \
  "v_cvt_pk_bf16_f32 v168, v160, v161\n\t" \
  "v_cvt_pk_bf16_f32 v169, v162, v163\n\t" \
  "v_cvt_pk_bf16_f32 v170, v164, v165\n\t" \
  "v_cvt_pk_bf16_f32 v171, v166, v167\n\t" \
  "buffer_store_dwordx4 v[168:171], v172, %[rt], s90 offen sc1\n\t" \
  "s_waitcnt vmcnt(0)\n\t" \
  "s_barrier\n\t" \
  "s_sub_i32 s91, s86, 1\n\t" \
  "v_mov_b32 v173, s91\n\t" \
  "buffer_store_dword v173, v174, %[rs], 0 offen sc1\n\t" \
  "s_mov_b32 m0, s94\n\t" \
  ".Le_%=:\n\t"
constexpr int W_X = 131072, W_PM = W_X + 16384;
template <bool ISV>
__device__ __forceinline__ void job_proj(Args& p, int g, int which, int h, char* smem, __amdgpu_buffer_rsrc_t rsy) {
  const int tid = threadIdx.x, lane = tid & 63, r = lane & 15, q = lane >> 4;
  const int w = __builtin_amdgcn_readfirstlane(tid >> 6), kh = w >> 2, cg = w & 3;
  u32x32 W[4];
  const unsigned lw = lds_off(smem) + w * 4096;
  u32x4 vx;
  u32x2 tag = {0u, 0u};
  const char* xn = p.ws + WS_XN;
  auto row = [&](int t, int i) {
    const char* src = xn + (size_t)(min(t, 127) >> 1) * (SEG * XLD * 2) + (t & 1) * 512;
    unsigned keep;
    asm volatile("s_mov_b32 %0, m0\n\ts_mov_b32 m0, %3\n\ts_nop 0\n\tglobal_load_lds_dwordx4 %1, %2 sc1\n\ts_mov_b32 m0, %0"
                 : "=&s"(keep) : "v"(vx[i]), "s"(src), "s"(lw + (t & 3) * 32768 + i * 1024) : "memory");
  };
  {
    const u16* Wm = which == 0 ? p.Wk : p.Wq;
    asm volatile("" : "+s"(Wm));
    if (ISV) Wm = p.Wv;
    const unsigned vo = (tid >> 4) * (HD * 2) + 16 * ((lane & 15) ^ swzB((tid >> 4) & 15));
    auto stage = [&](int j) {
      const char* src = (const char*)(Wm + (size_t)(WREV ? 7 - j : j) * 64 * HD + h * DH);
      const unsigned dst = lds_off(smem) + (j & 3) * 32768 + w * 1024;
      glds2(src, 32 * HD * 2, vo, dst);
      glds2(src + 512 * HD * 2, 32 * HD * 2, vo, dst + 16384);
    };
    stage(0); stage(1); stage(2); stage(3);
#pragma unroll
    for (int i = 0; i < 4; ++i) vx[i] = ((4 * w + i) ^ (ISV ? 0 : (lane >> 1) & 16)) * (XLD * 2) + (lane >> 5) * 1024 + (((lane & 31) ^ ((4 * w + i) & 15)) << 4);
    Boot bt;
#pragma unroll
    for (int j = 0; j < 8; ++j) {
      if (j < 5) WAIT_V(12); else if (j == 5) WAIT_V(8); else if (j == 6) WAIT_V(4); else WAIT_V(10);
      if (j == 6 && w == 0) { lds_at(smem + W_PM)[0] = boot_done(p.ws, bt); tag = next_tag(bt.e); }
      LBAR();
      const char* sb = smem + (j & 3) * 32768 + kh * 16384;
#pragma unroll
      for (int k = 0; k < 4; ++k) {
        const int ks = k >> 1, a = k & 1;
        const unsigned ch = ISV ? 2 * (2 * cg + (a ^ kh)) + ((r & 3) >> 1) : 4 * cg + (r & 3), by = ISV ? 8 * (r & 1) : 8 * a;
        s16x4 lo = __builtin_amdgcn_ds_read_tr16_b64_v4i16((lds_s16x4_ptr)(sb + offB(32 * ks + 8 * q + (r >> 2), ch) + by));
        s16x4 hi = __builtin_amdgcn_ds_read_tr16_b64_v4i16((lds_s16x4_ptr)(sb + offB(32 * ks + 8 * q + 4 + (r >> 2), ch) + by));
        const int f = 4 * (WREV ? 7 - j : j) + k;
        put4(W[f >> 3], f & 7, (bf16x8){lo[0], lo[1], lo[2], lo[3], hi[0], hi[1], hi[2], hi[3]});
      }
      LBAR();
      if (j == 3 && w == 0) boot_ask(p.ws, bt);
      if (j + 4 < 8) stage(j + 4);
      if (j == 6) {
#pragma unroll
        for (int i = 0; i < 10; ++i) row(i >> 2, i & 3);
      }
    }
    p.s0 = uni(lds_at(smem + W_PM)[0] >> 31);
  }
  typedef __attribute__((ext_vector_type(8))) unsigned u32x8;
  u32x8 la;
#pragma unroll
  for (int i = 0; i < 8; ++i) la[i] = (i >> 2) * 65536 + r * 1024 + kh * 512 + (((4 * (i & 3) + q) ^ r) << 4);
  const unsigned xw = W_X + w * 2048 + lane * 16;
  u32x32 acc;
  asm volatile("" : "={v[128:159]}"(acc));
  WAIT_V(6);
  LBAR();
  {
    const int g0 = 0;
#define PR_LOOP(TXT)                                                                                                              \
    asm volatile(TXT                                                                                                              \
        : "+{v[128:159]}"(acc)                                                                                                    \
        : W4_IN, "{v[208:215]}"(la), "{v[216:219]}"(vx), "{v220}"(xw), "{v221}"(xw ^ 8192),                                       \
          "{v172}"((ISV ? 2 * cg + kh : 4 * kh + cg) * 1024 + lane * 16), "{v174}"(tid == 256 ? fkq(h, which) : 0x7F000000),      \
          [rt] "s"(raw_rsrc(p.ws + WS_T, T_BYTES)), [rs] "s"(raw_rsrc(p.ws + WS_SY, SY_TOTAL)), [s0] "s"(p.s0 ^ TF_KQV),          \
          [lw] "s"(lw), [kh] "s"(kh), [g0] "s"(g0), [end] "s"(g0 ? NSEG : PR_G1), [x0] "s"(xn + (g0 + 1) * (SEG * XLD * 2)),                       \
          [t0] "s"(which * T_Q + kseg(0, h) - 1)                                                                                  \
        : CLB8(160, 161, 162, 163, 164, 165, 166, 167), "v168", "v169", "v170", "v171", "v173", CLB8(176, 177, 178, 179, 180, 181, 182, 183), \
          CLB8(184, 185, 186, 187, 188, 189, 190, 191), CLB8(192, 193, 194, 195, 196, 197, 198, 199),                             \
          CLB8(200, 201, 202, 203, 204, 205, 206, 207), "s86", "s87", "s88", "s90", "s91", "s92", "s93", "s94", "scc", "memory")
    if (ISV) PR_LOOP(PR_ASM_1); else PR_LOOP(PR_ASM_0);
    if (w == 0 && g0 == 0) wait_rows(p.ws, tag);
  }
#define PP_LOOP(TXT)                                                                                                              \
  asm volatile(TXT                                                                                                                \
      : "+{v[128:159]}"(acc)                                                                                                      \
      : W4_IN, "{v[240:247]}"(la), "{v[248:251]}"(vx), "{v252}"(xw), "{v253}"(xw ^ 8192),                                         \
        "{v172}"((ISV ? 2 * cg + kh : 4 * kh + cg) * 1024 + lane * 16), "{v174}"(tid == 256 ? fkq(h, which) : 0x7F000000),        \
        [rt] "s"(raw_rsrc(p.ws + WS_T, T_BYTES)), [rs] "s"(raw_rsrc(p.ws + WS_SY, SY_TOTAL)), [s0] "s"(p.s0 ^ TF_KQV),            \
        [lw] "s"(lw), [kh] "s"(kh), [g0] "n"(PR_G1), [x0] "s"(xn + (PR_G1 + 1) * (SEG * XLD * 2)),                                \
        [t0] "s"(which * T_Q + kseg(0, h) - 1)                                                                                    \
      : CLB8(160, 161, 162, 163, 164, 165, 166, 167), "v168", "v169", "v170", "v171", "v173", CLB8(176, 177, 178, 179, 180, 181, 182, 183), \
        CLB8(184, 185, 186, 187, 188, 189, 190, 191), CLB32(192), CLB8(224, 225, 226, 227, 228, 229, 230, 231),                   \
        CLB8(232, 233, 234, 235, 236, 237, 238, 239), "s86", "s87", "s90", "s91", "s92", "s93", "s94", "scc", "memory")
  if (ISV) PP_LOOP(PP_ASM_1); else PP_LOOP(PP_ASM_0);
}

enum { J_NONE = 0, J_GU = 3, J_PR = 5, J_CB = 8, J_DN = 14 };
struct BigJob { int type, panel, idx; };
struct BigPtr { const char *a, *b0, *b1; };
constexpr int G_LDA = XLD * 2, G_LDB = FF * 2;

__device__ __forceinline__ BigPtr big_ptrs(const Args& p, const BigJob& j) {
  BigPtr r;
  r.a = p.ws + WS_XN + (size_t)j.panel * 128 * G_LDA;
  r.b0 = (const char*)(p.wg + j.idx * 128); r.b1 = (const char*)(p.wu + j.idx * 128);
  return r;
}
__device__ __forceinline__ void big_publish(const Args& p, const BigJob& j) {
  int tid = threadIdx.x;
  asm volatile("" : "+v"(tid));
  if (j.type == J_GU && tid == 0)
    __hip_atomic_fetch_add((gu32*)(p.ws + WS_SY + SY_CH) + j.panel * 16, 1u, __ATOMIC_RELAXED, __HIP_MEMORY_SCOPE_AGENT);
}
typedef f32x4 Acc[2][2][4][2];
__device__ __forceinline__ void big_epilogue(const Args& p, const BigJob& j, Acc& acc, char* smem) {
  int tid = threadIdx.x;
  asm volatile("" : "+v"(tid));
  const int lane = tid & 63;
  const int wid = __builtin_amdgcn_readfirstlane(tid >> 6);
  const int wr = wid >> 2, wc = wid & 3;
  {
    const __amdgpu_buffer_rsrc_t ro = mkrsrc(p.ws + WS_HF, S_LEN * 16384);
#pragma unroll
    for (int i = 0; i < 8; ++i) {
      f32x4 hh[2];
#pragma unroll
      for (int n = 0; n < 2; ++n)
#pragma unroll
        for (int jj = 0; jj < 4; ++jj) {
          const float g = acc[i >> 2][0][i & 3][n][jj], u = acc[i >> 2][1][i & 3][n][jj];
          hh[n][jj] = __builtin_amdgcn_rcpf(1.0f + __builtin_amdgcn_exp2f(g * -1.4426950408889634f)) * u;
        }
      st_sc1(ro, h_off(p, j.panel + (i >> 2)) + (j.idx * 4 + wc) * 16384 + (wr * 4 + (i & 3)) * 1024 + lane * 16,
             ((u32x4){cvtpk(hh[0][0], hh[0][1]), cvtpk(hh[0][2], hh[0][3]), cvtpk(hh[1][0], hh[1][1]), cvtpk(hh[1][2], hh[1][3])}));
    }
  }
}
__device__ __forceinline__ void gemm_role(Args& p, int g, char* smem) {
  const __amdgpu_buffer_rsrc_t rsy = mkrsrc(p.ws + WS_SY, SY_TOTAL);
  const int x = g & 3, k = g >> 2;
  volatile unsigned* slot = reinterpret_cast<volatile unsigned*>(smem + 163824);
  int rnd = k < 24 ? 2 : -1;
  auto claim = [&]() {
    ++rnd;
    const int n = k < 24 ? 32 + k : rnd * 8 + k - 24, pn = (n * 37) >> 8;
    BigJob b;
    b.type = rnd < 4 ? J_GU : (rnd == 4 ? J_DN : (rnd == 5 && k >= 24 && k - 24 < 14 - NFUSE ? J_CB : J_NONE));
    b.panel = rnd < 4 ? 14 - 2 * pn : (rnd == 4 ? 4 * ((2 - (k >> 3)) & 3) + x : NFUSE + k - 24);
    b.idx = rnd < 4 ? x + 4 * (n - 7 * pn) : k & 7;
    return b;
  };
  if (k < 24) {
    if (k >= 16) job_proj<true>(p, g, 2, x * 8 + (k & 7), smem, rsy);
    else job_proj<false>(p, g, k >> 3, x * 8 + (k & 7), smem, rsy);
  } else {
    u32x2 tag;
#pragma unroll 1
    for (int u = 0; u < 4; ++u) {
      tag = start_rows(p, g - 96 + u * 32);
      if (u == 0 || u == 3) {
        WAIT_V(0);
        LBAR();
        report_rows(p, (u ? 32 : 0) + g - 96, tag);
      }
    }
    for (int z = 0; z < PF_T0; ++z) __builtin_amdgcn_s_sleep(16);
#pragma unroll 1
    for (int i = 0; i < PF_N; ++i) {
      const int m = i % 3, o = ((i / 3) * 32 + g - 96) * 448 + (int)threadIdx.x;
      if (threadIdx.x < 448) touch_line((const char*)(m == 0 ? p.wg : (m == 1 ? p.wu : p.wd)) + (size_t)o * 128, smem + (threadIdx.x >> 6) * 256);
      WAIT_V(1);
    }
    WAIT_V(0);
    if (threadIdx.x < 64) {
      wait_rows(p.ws, tag);
      Boot bt;
      boot_ask(p.ws, bt);
      WAIT_V(0);
      *slot = boot_done(p.ws, bt, true, tag);
    }
    LBAR();
    p.s0 = *slot >> 31;
  }
  p.s0 = uni(p.s0);
  BigJob cur = claim();
  while (cur.type != J_NONE) {
    if (cur.type != J_GU) {
      if (cur.type == J_DN) job_down(p, cur.panel, cur.idx, smem, rsy);
      else job_comb(p, cur.panel, x, rsy);
      cur = claim();
      continue;
    }
    int tid = threadIdx.x;
    asm volatile("" : "+v"(tid));
    const int lane = tid & 63;
    const int wid = __builtin_amdgcn_readfirstlane(tid >> 6);
    const int wr = wid >> 2, wc = wid & 3;
    const unsigned ra = tid >> 3, ca = 16 * ((tid & 7) ^ ((tid >> 3) & 7)), rb = tid >> 4, cb = 16 * ((tid & 15) ^ swzB((tid >> 4) & 15));
    const unsigned lw = lds_off(smem) + wid * 1024;
#define B8_SA(b, h) (((b) * 2 + (h)) * 16384)
#define B8_SB(b, h) ((4 + (b) * 2 + (h)) * 16384)
#define B8_STA(b, h, P, kt) glds2<true>((P).a + (size_t)(h) * 128 * G_LDA + (kt) * 128, 64 * G_LDA, ra * G_LDA + ca, lw + B8_SA(b, h))
#define B8_STB(b, h, P, kt) glds2(((h) ? (P).b1 : (P).b0) + (size_t)(kt) * 64 * G_LDB, 32 * G_LDB, rb * G_LDB + cb, lw + B8_SB(b, h))
#define B8_LDA(b, h) do { _Pragma("unroll") for (int m = 0; m < 4; ++m) _Pragma("unroll") for (int kk = 0; kk < 2; ++kk) \
      At[m][kk] = ldsf(smem + B8_SA(b, h) + offA(wr * 64 + m * 16 + (lane & 15), kk * 4 + (lane >> 4))); } while (0)
#define B8_LDB(dst, b, h) do { _Pragma("unroll") for (int n = 0; n < 2; ++n) _Pragma("unroll") for (int kk = 0; kk < 2; ++kk) \
      dst[n][kk] = ld_bfrag(smem + B8_SB(b, h), kk, wc * 2 + n, lane); } while (0)
#define B8_MMA(ai, bj, Bt) do { _Pragma("unroll") for (int m = 0; m < 4; ++m) _Pragma("unroll") for (int n = 0; n < 2; ++n) \
      _Pragma("unroll") for (int kk = 0; kk < 2; ++kk) acc[ai][bj][m][n] = MFMA(Bt[n][kk], At[m][kk], acc[ai][bj][m][n]); } while (0)
#define WAIT_L(n) asm volatile("s_waitcnt lgkmcnt(%0)" :: "n"(n) : "memory")
#define BAR() __builtin_amdgcn_s_barrier()
#define BAR_1() do { SCHED(); asm volatile("s_setprio 1\n\ts_cmp_eq_u32 %0, 0\n\ts_cbranch_scc1 .Lb_%=\n\ts_setprio 2\n\ts_barrier\n.Lb_%=:" :: "s"(wr) : "memory", "scc"); SCHED(); } while (0)
#define BAR_0() do { SCHED(); asm volatile("s_setprio 0\n\ts_cmp_lg_u32 %0, 0\n\ts_cbranch_scc1 .Lb_%=\n\ts_barrier\n.Lb_%=:" :: "s"(wr) : "memory", "scc"); SCHED(); } while (0)
#define WAIT_RX(c) asm volatile("s_waitcnt vmcnt(16)\n\ts_cmp_lg_u32 %0, 0\n\ts_cbranch_scc1 .Lrx_%=\n\ts_waitcnt vmcnt(8)\n.Lrx_%=:" :: "s"(c) : "memory", "scc")
    Acc acc;
    bf16x8 At[4][2], B0[2][2], B1[2][2];
    BigPtr cp = big_ptrs(p, cur);
    BigJob pend; pend.type = J_NONE; pend.panel = 0; pend.idx = 0;
    LBAR();
    B8_STB(0, 0, cp, 0); B8_STB(0, 1, cp, 0); B8_STA(0, 0, cp, 0); B8_STA(0, 1, cp, 0);
    B8_STB(1, 0, cp, 1); B8_STA(1, 0, cp, 1); B8_STB(1, 1, cp, 1);
    WAIT_V(8); BAR();
    BigJob nx = pend;
    for (;;) {
      bool chain = false;
      BigPtr np = cp;
      constexpr int nt = DM / 64;
#pragma unroll
      for (int i = 0; i < 32; ++i) acc[i >> 4][(i >> 3) & 1][(i >> 1) & 3][i & 1] = (f32x4){0.f, 0.f, 0.f, 0.f};
#pragma unroll 1
      for (int t = 0; t < nt; t += 2) {
        const bool last = t == nt - 2;
        if (last) {
          nx = claim();
          chain = nx.type == J_GU;
          if (chain) np = big_ptrs(p, nx);
        }
        const BigPtr sp = np;
        const int t2 = last ? 0 : t + 2;
        if (t == 2) big_publish(p, pend);
        const int rx = __builtin_amdgcn_readfirstlane(t == 0 && pend.type != J_NONE);
        B8_LDB(B0, 0, 0); B8_LDB(B1, 0, 1); SCHED(); B8_LDA(0, 0); B8_STA(1, 1, cp, t + 1);
        WAIT_RX(rx);
        WAIT_L(0); BAR_1(); B8_MMA(0, 0, B0); B8_MMA(0, 1, B1); BAR_0();
        B8_LDA(0, 1); B8_STB(0, 0, sp, t2); B8_STB(0, 1, sp, t2); B8_STA(0, 0, sp, t2);
        WAIT_RX(rx);
        WAIT_L(0); BAR_1(); B8_MMA(1, 0, B0); B8_MMA(1, 1, B1); BAR_0();
        B8_LDB(B0, 1, 0); B8_LDB(B1, 1, 1); SCHED(); B8_LDA(1, 0); B8_STA(0, 1, sp, t2);
        WAIT_V(8); WAIT_L(0); BAR_1(); B8_MMA(0, 0, B0); B8_MMA(0, 1, B1); BAR_0();
        B8_LDA(1, 1); B8_STB(1, 0, sp, t2 + 1); B8_STB(1, 1, sp, t2 + 1); B8_STA(1, 0, sp, t2 + 1);
        WAIT_V(8); WAIT_L(0); BAR_1(); B8_MMA(1, 0, B0); B8_MMA(1, 1, B1); BAR_0();
      }
      big_epilogue(p, cur, acc, smem);
      if (!chain) break;
      pend = cur; cur = nx; cp = np;
    }
    WAIT_V(0);
    BAR();
    asm volatile(".Lcode_end:");
    big_publish(p, cur);
    cur = nx;
  }
}


__device__ __forceinline__ unsigned now_q() { return (unsigned)__builtin_amdgcn_s_memrealtime() * 4u; }
__device__ __forceinline__ void pace_until(unsigned t) {
#pragma unroll 1
  for (int i = 0; i < 256; ++i) {
    const int d = (int)(t - now_q());
    if (d <= 0) break;
    if (d > 32) __builtin_amdgcn_s_sleep(1);
  }
}
#define CLB_E "s76", "s77", "s78", "s79", "s80", "s81", "s82", "s83", "s84", "s85", "s86", "s87", "s88", "s89"
#define EDGE_ASM \
  ".p2align 5\n\t" \
  "s_memtime s[76:77]\n\t" \
  "s_memrealtime s[78:79]\n\t" \
  "s_nop 3\n\t" \
  "s_memrealtime s[80:81]\n\t" \
  "s_nop 3\n\t" \
  "s_memrealtime s[82:83]\n\t" \
  "s_nop 3\n\t" \
  "s_memrealtime s[84:85]\n\t" \
  "s_nop 3\n\t" \
  "s_memrealtime s[86:87]\n\t" \
  "s_nop 3\n\t" \
  "s_memrealtime s[88:89]\n\t" \
  "s_mov_b32 s93, 0\n\t" \
  "s_waitcnt lgkmcnt(0)\n\t" \
  "s_cmp_eq_u32 s80, s78\n\t" \
  "s_addc_u32 s93, s93, 0\n\t" \
  "s_cmp_eq_u32 s82, s78\n\t" \
  "s_addc_u32 s93, s93, 0\n\t" \
  "s_cmp_eq_u32 s84, s78\n\t" \
  "s_addc_u32 s93, s93, 0\n\t" \
  "s_cmp_eq_u32 s86, s78\n\t" \
  "s_addc_u32 s93, s93, 0\n\t" \
  "s_cmp_eq_u32 s88, s78\n\t" \
  "s_addc_u32 s93, s93, 0\n\t" \
  "s_mul_i32 s93, s93, 21\n\t" \
  "s_add_u32 s76, s76, 15\n\t" \
  "s_add_u32 %[kc], s76, s93\n\t" \
  "s_lshl2_add_u32 %[kq], s78, 16\n\t"
__device__ __forceinline__ unsigned time_of(unsigned cyc) {
  unsigned c, q;
  asm volatile(EDGE_ASM : [kc] "=&s"(c), [kq] "=&s"(q) :: CLB_E, "s93", "scc");
  return q - (((c - cyc) * 11900u) >> 16);
}
#define DS_RED 17
#define DS_MEM 12
#define LEAD_DN 1
#define LEAD_UP 12
__device__ __forceinline__ u32x2 ld2_sc1(__amdgpu_buffer_rsrc_t rs, int off, int soff = 0) {
  return __builtin_amdgcn_raw_buffer_load_b64(rs, off, soff, 16);
}
__device__ __forceinline__ void st2_sc1(__amdgpu_buffer_rsrc_t rs, int off, u32x2 v) {
  __builtin_amdgcn_raw_buffer_store_b64(v, rs, off, 0, 16);
}
__device__ __forceinline__ int row_max(int v) {
  v = max(v, __builtin_amdgcn_update_dpp(0, v, 0x128, 0xf, 0xf, false));
  v = max(v, __builtin_amdgcn_update_dpp(0, v, 0x124, 0xf, 0xf, false));
  v = max(v, __builtin_amdgcn_update_dpp(0, v, 0x122, 0xf, 0xf, false));
  v = max(v, __builtin_amdgcn_update_dpp(0, v, 0x121, 0xf, 0xf, false));
  return v;
}
__device__ __forceinline__ int wave_max(int v) {
  v = row_max(v);
  return max(max(__builtin_amdgcn_readlane(v, 0), __builtin_amdgcn_readlane(v, 16)),
             max(__builtin_amdgcn_readlane(v, 32), __builtin_amdgcn_readlane(v, 48)));
}
__device__ __forceinline__ int track(int est, int x, int up, int dn, int out) {
  const int d = x - est;
  return est + (d > out ? 4 : max(min(d, up), -dn));
}
#define WSL 12
#define PLAN_SLEEP 2
#define DS_N0 3
#define DS_X0 20
#define REF_W 3
#define REF_G 60
#define RL0 310
__device__ __forceinline__ void watch(__amdgpu_buffer_rsrc_t rsy, int s, int lane, unsigned t0, const unsigned* tgt_, gu32* abortw, int hs) {
  const int rl = (int)__builtin_amdgcn_raw_buffer_load_b32(rsy, lane * 128, SY_BR + hs, 16);
  u32x2 ra;
  for (unsigned spins = 0;;) {
    asm volatile("" ::: "memory");
    ra = ld2_sc1(rsy, (lane & 31) * 128, SY_BA + hs);
    const bool all = __all((int)ra[0] >= s);
    if (all) break;
    if (spins < 3) __builtin_amdgcn_s_sleep(PLAN_SLEEP); else __builtin_amdgcn_s_sleep(WSL);
    if (spin_fail(spins, abortw)) break;
  }
  int d = ra[0] == (unsigned)s ? (int)(ra[1] - t0) : 0;
  d = max(d, __builtin_amdgcn_update_dpp(0, d, 0xB1, 0xf, 0xf, false));
  d = max(d, __builtin_amdgcn_update_dpp(0, d, 0x4E, 0xf, 0xf, false));
  int sum = d + __builtin_amdgcn_update_dpp(0, d, 0x128, 0xf, 0xf, false);
  sum += __builtin_amdgcn_update_dpp(0, sum, 0x124, 0xf, 0xf, false);
  const int mx = wave_max(d), mean = (__builtin_amdgcn_readlane(sum, 0) + __builtin_amdgcn_readlane(sum, 16)) >> 3;
  const unsigned t = t0 + mx - min(((mx - mean) * REF_W) >> 2, REF_G) + wave_max(rl < 0 ? RL0 : rl) + (DS_RED + DS_MEM) + (s < DS_N0 ? DS_X0 : 0);
  lds_vu_ptr tgt = lds_at(tgt_);
  if (lane < 2) tgt[lane] = lane ? t : s + 1;
}
__device__ __forceinline__ unsigned ld_flag(__amdgpu_buffer_rsrc_t rs, int off) {
  return __builtin_amdgcn_raw_buffer_load_b32(rs, off, 0, 16);
}

constexpr unsigned long long QK_MAP[8] = {0xbaf9876d543210ecull, 0xa98f76d543210bceull, 0x98f76d543210ecbaull, 0x876fd543210bcea9ull, 0x76fd543210ecba98ull, 0x65df43210bcea987ull, 0x5df43210ecba9876ull, 0xd43f210bcea98765ull};
#define QK_ASM_0 \
  "s_waitcnt lgkmcnt(0)\n\t" \
  "v_subrev_u32 v244, 0x8000, v250\n\t" \
  "ds_read_b128 v[216:219], v248 offset:32768\n\t" \
  "ds_read_b128 v[220:223], v248 offset:33792\n\t" \
  "ds_read_b128 v[224:227], v248 offset:34816\n\t" \
  "ds_read_b128 v[228:231], v248 offset:35840\n\t" \
  "ds_read_b128 v[208:211], v244 offset:0\n\t" \
  "ds_read_b128 v[212:215], v244 offset:1024\n\t" \
  "ds_read_b128 v[236:239], v244 offset:2048\n\t" \
  "ds_read_b128 v[240:243], v244 offset:3072\n\t" \
  "v_mov_b64 v[192:193], 0\n\t" \
  "v_mov_b64 v[194:195], 0\n\t" \
  "v_mov_b64 v[196:197], 0\n\t" \
  "v_mov_b64 v[198:199], 0\n\t" \
  ".p2align 5\n\t" \
  "s_nop 0\n\t" \
  "s_nop 0\n\t" \
  "s_nop 0\n\t" \
  "s_nop 0\n\t" \
  "s_movk_i32 s92, 0x2000\n\t" \
  ".Lwt_%=:\n\t" \
  "ds_read_b64 v[200:201], v248 offset:30720\n\t" \
  "s_memrealtime s[90:91]\n\t" \
  "s_sub_u32 s92, s92, 1\n\t" \
  "s_waitcnt lgkmcnt(0)\n\t" \
  "v_readfirstlane_b32 s93, v200\n\t" \
  "v_readfirstlane_b32 %[tgt], v201\n\t" \
  "s_cbranch_scc1 .Lgo_%=\n\t" \
  "s_cmp_eq_u32 s93, %[step]\n\t" \
  "s_cbranch_scc1 .Lwx_%=\n\t" \
  "s_sleep 1\n\t" \
  "s_branch .Lwt_%=\n\t" \
  ".Lwx_%=:\n\t" \
  "s_lshl_b32 s93, s90, 2\n\t" \
  "s_sub_i32 s93, %[tgt], s93\n\t" \
  "s_cmp_le_i32 s93, 0\n\t" \
  "s_cbranch_scc1 .Lgo_%=\n\t" \
  "s_min_i32 s93, s93, 8000\n\t" \
  "s_mov_b32 s94, s93\n\t" \
  ".Lsl_%=:\n\t" \
  "s_sub_i32 s94, s94, 18\n\t" \
  "s_cmp_lt_i32 s94, 30\n\t" \
  "s_cbranch_scc1 .Lsx_%=\n\t" \
  "s_sleep 1\n\t" \
  "s_branch .Lsl_%=\n\t" \
  ".Lsx_%=:\n\t" \
  "s_sub_i32 s93, s93, 12\n\t" \
  "s_max_i32 s93, s93, 4\n\t" \
  "s_lshr_b32 s93, s93, 2\n\t" \
  "s_add_u32 s93, s93, s90\n\t" \
  "s_memrealtime s[90:91]\n\t" \
  ".Lpace_%=:\n\t" \
  "s_memrealtime s[94:95]\n\t" \
  "s_waitcnt lgkmcnt(1)\n\t" \
  "s_sub_i32 s92, s90, s93\n\t" \
  "s_cmp_ge_i32 s92, 0\n\t" \
  "s_cbranch_scc1 .Le_%=\n\t" \
  "s_memrealtime s[90:91]\n\t" \
  "s_waitcnt lgkmcnt(1)\n\t" \
  "s_sub_i32 s92, s94, s93\n\t" \
  "s_cmp_lt_i32 s92, 0\n\t" \
  "s_cbranch_scc1 .Lpace_%=\n\t" \
  "s_nop 4\n\t" \
  ".Le_%=:\n\t" \
  "s_add_u32 s92, s92, s93\n\t" \
  "s_lshl_b32 s92, s92, 2\n\t" \
  "s_sub_i32 s92, %[tgt], s92\n\t" \
  "s_max_i32 s92, s92, 0\n\t" \
  "s_bitcmp0_b32 s92, 3\n\t" \
  "s_cbranch_scc1 .Lf3_%=\n\t" \
  "s_nop 15\n\t" \
  ".Lf3_%=:\n\t" \
  "s_bitcmp0_b32 s92, 2\n\t" \
  "s_cbranch_scc1 .Lf2_%=\n\t" \
  "s_nop 10\n\t" \
  ".Lf2_%=:\n\t" \
  ".Lgo_%=:\n\t" \
  "s_mov_b32 s92, 0\n\t" \
  ".Lpoll_%=:\n\t" \
  "buffer_load_dwordx4 v[200:203], v248, %[rs], %[som] offen sc1\n\t" \
  "buffer_load_dwordx4 v[204:207], v248, %[rs], %[som2] offen sc1\n\t" \
  ".p2align 5\n\t" \
  "s_waitcnt vmcnt(1) lgkmcnt(0)\n\t" \
  "ds_write_b128 v249, v[200:203]\n\t" \
  "v_smfmac_f32_16x16x64_bf16 v[192:195], v[200:203], v[208:215], v251\n\t" \
  "v_max3_u32 v244, v200, v201, v202\n\t" \
  "v_smfmac_f32_16x16x64_bf16 v[196:199], v[200:203], v[236:243], v251\n\t" \
  "s_waitcnt vmcnt(0)\n\t" \
  "ds_write_b128 v249, v[204:207] offset:8192\n\t" \
  "v_smfmac_f32_16x16x64_bf16 v[192:195], v[204:207], v[216:223], v251\n\t" \
  "v_max3_u32 v244, v244, v203, v204\n\t" \
  "v_max3_u32 v244, v244, v205, v206\n\t" \
  "v_smfmac_f32_16x16x64_bf16 v[196:199], v[204:207], v[224:231], v251\n\t" \
  "v_max_u32 v244, v244, v207\n\t" \
  "v_cmp_le_u32_e64 vcc, %[sent], v244\n\t" \
  "s_cbranch_vccnz .Lslow_%=\n\t" \
  ".Lok_%=:\n\t" \
  "ds_read_b128 v[216:219], v250 offset:32768\n\t" \
  "ds_read_b128 v[220:223], v250 offset:33792\n\t" \
  "ds_read_b128 v[224:227], v250 offset:34816\n\t" \
  "ds_read_b128 v[228:231], v250 offset:35840\n\t" \
  "s_waitcnt lgkmcnt(4)\n\t" \
  "s_barrier\n\t" \
  "ds_read_b128 v[208:211], v248 offset:1024\n\t" \
  "ds_read_b128 v[212:215], v248 offset:2048\n\t" \
  "ds_read_b128 v[236:239], v248 offset:3072\n\t" \
  "ds_read_b128 v[240:243], v248 offset:4096\n\t" \
  "s_cmp_eq_u32 %[step], 63\n\t" \
  "s_cbranch_scc1 .Lls_%=\n\t" \
  "s_waitcnt lgkmcnt(3)\n\t" \
  "ds_read_b128 v[244:247], v248 offset:5120\n\t" \
  "v_smfmac_f32_16x16x64_bf16 v[192:195], v[208:211], v[216:223], v251\n\t" \
  "ds_read_b128 v[200:203], v248 offset:13312\n\t" \
  "v_smfmac_f32_16x16x64_bf16 v[196:199], v[208:211], v[224:231], v251\n\t" \
  "s_waitcnt lgkmcnt(4)\n\t" \
  "ds_read_b128 v[216:219], v250 offset:0\n\t" \
  "v_smfmac_f32_16x16x64_bf16 v[192:195], v[212:215], v[0:7], v251\n\t" \
  "ds_read_b128 v[220:223], v250 offset:1024\n\t" \
  "v_smfmac_f32_16x16x64_bf16 v[196:199], v[212:215], v[8:15], v251\n\t" \
  "s_waitcnt lgkmcnt(5)\n\t" \
  "ds_read_b128 v[224:227], v250 offset:2048\n\t" \
  "v_smfmac_f32_16x16x64_bf16 v[192:195], v[236:239], v[16:23], v251\n\t" \
  "ds_read_b128 v[228:231], v250 offset:3072\n\t" \
  "v_smfmac_f32_16x16x64_bf16 v[196:199], v[236:239], v[24:31], v251\n\t" \
  "s_waitcnt lgkmcnt(5)\n\t" \
  "ds_read_b128 v[204:207], v248 offset:6144\n\t" \
  "v_smfmac_f32_16x16x64_bf16 v[192:195], v[240:243], v[32:39], v251\n\t" \
  "ds_read_b128 v[208:211], v248 offset:7168\n\t" \
  "v_smfmac_f32_16x16x64_bf16 v[196:199], v[240:243], v[40:47], v251\n\t" \
  "ds_read_b128 v[212:215], v248 offset:9216\n\t" \
  "v_smfmac_f32_16x16x64_bf16 v[192:195], v[244:247], v[48:55], v251\n\t" \
  "ds_read_b128 v[236:239], v248 offset:10240\n\t" \
  "v_smfmac_f32_16x16x64_bf16 v[196:199], v[244:247], v[56:63], v251\n\t" \
  "s_waitcnt lgkmcnt(6)\n\t" \
  "v_smfmac_f32_16x16x64_bf16 v[192:195], v[200:203], v[216:223], v251\n\t" \
  "s_waitcnt lgkmcnt(4)\n\t" \
  "v_smfmac_f32_16x16x64_bf16 v[196:199], v[200:203], v[224:231], v251\n\t" \
  "s_waitcnt lgkmcnt(3)\n\t" \
  "ds_read_b128 v[240:243], v248 offset:11264\n\t" \
  "v_smfmac_f32_16x16x64_bf16 v[192:195], v[204:207], v[64:71], v251\n\t" \
  "ds_read_b128 v[244:247], v248 offset:12288\n\t" \
  "v_smfmac_f32_16x16x64_bf16 v[196:199], v[204:207], v[72:79], v251\n\t" \
  "s_waitcnt lgkmcnt(3)\n\t" \
  "v_smfmac_f32_16x16x64_bf16 v[192:195], v[208:211], v[80:87], v251\n\t" \
  "ds_read_b128 v[200:203], v248 offset:14336\n\t" \
  "v_smfmac_f32_16x16x64_bf16 v[196:199], v[208:211], v[88:95], v251\n\t" \
  "ds_read_b128 v[204:207], v248 offset:15360\n\t" \
  "v_smfmac_f32_16x16x64_bf16 v[192:195], v[212:215], v[96:103], v251\n\t" \
  "v_smfmac_f32_16x16x64_bf16 v[196:199], v[212:215], v[104:111], v251\n\t" \
  "s_waitcnt lgkmcnt(3)\n\t" \
  "v_smfmac_f32_16x16x64_bf16 v[192:195], v[236:239], v[112:119], v251\n\t" \
  "v_smfmac_f32_16x16x64_bf16 v[196:199], v[236:239], v[120:127], v251\n\t" \
  "v_smfmac_f32_16x16x64_bf16 v[192:195], v[240:243], v[128:135], v251\n\t" \
  "v_smfmac_f32_16x16x64_bf16 v[196:199], v[240:243], v[136:143], v251\n\t" \
  "s_waitcnt lgkmcnt(0)\n\t" \
  "v_smfmac_f32_16x16x64_bf16 v[192:195], v[244:247], v[144:151], v251\n\t" \
  "v_smfmac_f32_16x16x64_bf16 v[196:199], v[244:247], v[152:159], v251\n\t" \
  "v_smfmac_f32_16x16x64_bf16 v[192:195], v[200:203], v[160:167], v251\n\t" \
  "v_smfmac_f32_16x16x64_bf16 v[196:199], v[200:203], v[168:175], v251\n\t" \
  "v_smfmac_f32_16x16x64_bf16 v[192:195], v[204:207], v[176:183], v251\n\t" \
  "v_smfmac_f32_16x16x64_bf16 v[196:199], v[204:207], v[184:191], v251\n\t" \
  "ds_read_b128 v[200:203], v248 offset:16384\n\t" \
  "ds_read_b128 v[204:207], v248 offset:20480\n\t" \
  "v_mov_b32 v250, v232\n\t" \
  "v_mov_b32 v249, v233\n\t" \
  "s_nop 2\n\t" \
  "v_add_f32 v192, v192, v194\n\t" \
  "v_add_f32 v193, v193, v195\n\t" \
  "v_add_f32 v196, v196, v198\n\t" \
  "v_add_f32_e64 v197, v197, v199\n\t" \
  "v_cvt_pk_bf16_f32 v192, v192, v193\n\t" \
  "v_cvt_pk_bf16_f32 v196, v196, v197\n\t" \
  "ds_write2_b32 v252, v192, v196 offset1:64\n\t" \
  "v_mov_b32_e64 v251, v234\n\t" \
  "ds_read_b64_tr_b16 v[232:233], v250 offset:0\n\t" \
  "ds_read_b64_tr_b16 v[234:235], v250 offset:64\n\t" \
  "v_mov_b32_e64 v212, %[step]\n\t" \
  "s_mov_b64 exec, 1\n\t" \
  "ds_write_b32 v248, v212 offset:28672\n\t" \
  "s_mov_b64 exec, -1\n\t" \
  "buffer_load_dwordx4 v[192:195], v248, %[rt], %[svt] offen offset:0 sc1\n\t" \
  "buffer_load_dwordx4 v[196:199], v248, %[rt], %[svt] offen offset:1024 sc1\n\t" \
  "s_waitcnt lgkmcnt(1)\n\t" \
  "v_mfma_f32_16x16x32_bf16 v[200:203], v[200:203], v[232:235], 0\n\t" \
  "v_mfma_f32_16x16x32_bf16 v[204:207], v[204:207], v[232:235], 0\n\t" \
  "s_nop 7\n\t" \
  "v_cndmask_b32 v216, v200, v204, %[nh]\n\t" \
  "v_cndmask_b32 v217, v201, v205, %[nh]\n\t" \
  "v_cndmask_b32 v218, v202, v206, %[nh]\n\t" \
  "v_cndmask_b32 v219, v203, v207, %[nh]\n\t" \
  "ds_write_b128 v248, v[216:219] offset:16384\n\t" \
  "buffer_load_dword v252, v249, %[rs], %[sv] offen offset:0 sc1\n\t" \
  "buffer_load_dword v253, v249, %[rs], %[sv] offen offset:256 sc1\n\t" \
  "ds_read_b32 v212, v248 offset:28672\n\t" \
  "ds_read_b64_tr_b16 v[236:237], v250 offset:512\n\t" \
  "ds_read_b64_tr_b16 v[238:239], v250 offset:576\n\t" \
  "ds_read_b64_tr_b16 v[240:241], v250 offset:1024\n\t" \
  "ds_read_b64_tr_b16 v[242:243], v250 offset:1088\n\t" \
  "ds_read_b64_tr_b16 v[244:245], v250 offset:1536\n\t" \
  "ds_read_b64_tr_b16 v[246:247], v250 offset:1600\n\t" \
  "s_setprio 3\n\t" \
  "s_waitcnt lgkmcnt(0)\n\t" \
  "v_cmp_ne_u32 vcc, %[step], v212\n\t" \
  "s_and_b32 s93, vcc_lo, 0xf\n\t" \
  "s_barrier\n\t" \
  "s_cbranch_scc1 .Lre_%=\n\t" \
  ".Lrb_%=:\n\t" \
  "ds_read_b64_tr_b16 v[200:201], v250 offset:2048\n\t" \
  "ds_read_b64_tr_b16 v[202:203], v250 offset:2112\n\t" \
  "ds_read_b64_tr_b16 v[204:205], v250 offset:2560\n\t" \
  "ds_read_b64_tr_b16 v[206:207], v250 offset:2624\n\t" \
  "ds_read_b64_tr_b16 v[208:209], v250 offset:3072\n\t" \
  "ds_read_b64_tr_b16 v[210:211], v250 offset:3136\n\t" \
  "ds_read_b64_tr_b16 v[212:213], v250 offset:3584\n\t" \
  "ds_read_b64_tr_b16 v[214:215], v250 offset:3648\n\t" \
  "ds_read_b128 v[220:223], v248 offset:17408\n\t" \
  "ds_read_b128 v[224:227], v248 offset:18432\n\t" \
  "ds_read_b128 v[228:231], v248 offset:19456\n\t" \
  "s_waitcnt lgkmcnt(9)\n\t" \
  "v_mfma_f32_16x16x32_bf16 v[200:203], v[200:203], v[232:235], 0\n\t" \
  "s_waitcnt lgkmcnt(7)\n\t" \
  "v_mfma_f32_16x16x32_bf16 v[200:203], v[204:207], v[236:239], v[200:203]\n\t" \
  "v_mov_b32 v233, 0\n\t" \
  "s_waitcnt lgkmcnt(5)\n\t" \
  "v_mfma_f32_16x16x32_bf16 v[200:203], v[208:211], v[240:243], v[200:203]\n\t" \
  "v_mov_b32 v234, 0\n\t" \
  "s_waitcnt lgkmcnt(3)\n\t" \
  "v_mfma_f32_16x16x32_bf16 v[200:203], v[212:215], v[244:247], v[200:203]\n\t" \
  "s_waitcnt lgkmcnt(2)\n\t" \
  "v_pk_add_f32 v[216:217], v[216:217], v[220:221]\n\t" \
  "v_pk_add_f32 v[218:219], v[218:219], v[222:223]\n\t" \
  "s_waitcnt lgkmcnt(0)\n\t" \
  "v_pk_add_f32 v[224:225], v[224:225], v[228:229]\n\t" \
  "v_pk_add_f32 v[226:227], v[226:227], v[230:231]\n\t" \
  "v_pk_add_f32 v[216:217], v[216:217], v[224:225]\n\t" \
  "v_pk_add_f32 v[218:219], v[218:219], v[226:227]\n\t" \
  "v_max3_f32 v224, v216, v217, v218\n\t" \
  "v_max_f32 v224, v224, v219\n\t" \
  "v_max3_f32 v231, v200, v201, v202\n\t" \
  "v_max_f32 v231, v231, v203\n\t" \
  "v_max_f32_dpp v225, v224, v224 row_ror:8 row_mask:0xf bank_mask:0xf\n\t" \
  "v_max_f32_dpp v226, v224, v224 row_ror:8 row_mask:0xf bank_mask:0xf\n\t" \
  "v_cndmask_b32 v200, v200, v202, %[up]\n\t" \
  "v_cndmask_b32 v201, v201, v203, %[up]\n\t" \
  "v_permlane32_swap_b32_e64 v225, v226\n\t" \
  "v_max3_f32 v205, v225, v226, v231\n\t" \
  "v_max3_f32 v206, v225, v226, v231\n\t" \
  "v_mov_b64 v[220:221], %[one]\n\t" \
  "v_mov_b64 v[222:223], %[one]\n\t" \
  "v_permlane16_swap_b32 v205, v206\n\t" \
  "v_max_f32 v204, v205, v206\n\t" \
  "v_mul_f32_e64 v204, %[negc], v204\n\t" \
  "v_fma_f32 v200, v200, %[c], v204\n\t" \
  "v_fma_f32 v201, v201, %[c], v204\n\t" \
  "v_exp_f32 v200, v200\n\t" \
  "v_fma_f32 v216, v216, %[c], v204\n\t" \
  "v_exp_f32 v201, v201\n\t" \
  "v_fma_f32 v217, v217, %[c], v204\n\t" \
  "v_exp_f32_e64 v216, v216\n\t" \
  "v_fma_f32 v218, v218, %[c], v204\n\t" \
  "v_cvt_pk_bf16_f32 v232, v200, v201\n\t" \
  "v_cvt_pk_bf16_f32 v235, v200, v201\n\t" \
  "v_exp_f32 v217, v217\n\t" \
  "v_fma_f32 v219, v219, %[c], v204\n\t" \
  "v_mfma_f32_16x16x16_bf16 v[212:215], v[220:221], v[232:233], 0\n\t" \
  "v_exp_f32 v218, v218\n\t" \
  "s_waitcnt vmcnt(0)\n\t" \
  "v_mfma_f32_16x16x16_bf16 v[224:227], v[252:253], v[232:233], 0\n\t" \
  "v_cvt_pk_bf16_f32 v210, v216, v217\n\t" \
  "v_cvt_pk_bf16_f32 v208, v216, v217\n\t" \
  "v_exp_f32 v219, v219\n\t" \
  "v_mfma_f32_16x16x16_bf16 v[228:231], v[252:253], v[234:235], 0\n\t" \
  "v_cvt_pk_bf16_f32 v211, v218, v219\n\t" \
  "v_cvt_pk_bf16_f32 v209, v218, v219\n\t" \
  "v_mov_b32_dpp v208, v210 row_ror:8 row_mask:0xf bank_mask:0xc\n\t" \
  "v_mov_b32_dpp v210, v210 row_ror:8 row_mask:0xf bank_mask:0x3\n\t" \
  "v_mov_b32_dpp v209, v211 row_ror:8 row_mask:0xf bank_mask:0xc\n\t" \
  "v_mov_b32_dpp v211, v211 row_ror:8 row_mask:0xf bank_mask:0x3\n\t" \
  "v_max_u32 v204, v252, v253\n\t" \
  "v_cmp_le_u32 vcc, %[sent], v204\n\t" \
  "v_mfma_f32_16x16x32_bf16 v[212:215], v[220:223], v[208:211], v[212:215]\n\t" \
  ".Lpv_%=:\n\t" \
  "v_mfma_f32_16x16x32_bf16 v[224:227], v[192:195], v[208:211], v[224:227]\n\t" \
  "v_mfma_f32_16x16x32_bf16 v[228:231], v[196:199], v[208:211], v[228:231]\n\t" \
  "s_cbranch_vccnz .Lvs_%=\n\t" \
  ".Lvok_%=:\n\t" \
  "s_nop 4\n\t" \
  "v_rcp_f32_e64 v204, v212\n\t" \
  "v_cndmask_b32 v224, v224, v226, %[nh]\n\t" \
  "v_cndmask_b32 v225, v225, v227, %[nh]\n\t" \
  "v_cndmask_b32 v228, v228, v230, %[nh]\n\t" \
  "v_cndmask_b32 v229, v229, v231, %[nh]\n\t" \
  "v_pk_mul_f32 v[224:225], v[224:225], v[204:205] op_sel_hi:[1,0]\n\t" \
  "v_pk_mul_f32 v[228:229], v[228:229], v[204:205] op_sel_hi:[1,0]\n\t" \
  "v_cvt_pk_bf16_f32 v224, v224, v225\n\t" \
  "v_cvt_pk_bf16_f32 v225, v228, v229\n\t" \
  "buffer_store_dwordx2 v[224:225], v251, %[rs], %[so] offen sc1\n\t" \
  "s_memtime s[90:91]\n\t" \
  "s_setprio 0\n\t" \
  "s_waitcnt lgkmcnt(0)\n\t" \
  "s_mov_b32 %[tout], s90\n\t" \
  "s_mov_b32 %[spins], s92\n\t" \
  "s_branch .Lend_%=\n\t" \
  ".Lls_%=:\n\t" \
  "s_waitcnt lgkmcnt(0)\n\t" \
  "s_barrier\n\t" \
  "s_mov_b32 %[spins], 0\n\t" \
  "s_branch .Lend_%=\n\t" \
  ".Lre_%=:\n\t" \
  "ds_read_b64_tr_b16 v[236:237], v250 offset:512\n\t" \
  "ds_read_b64_tr_b16 v[238:239], v250 offset:576\n\t" \
  "ds_read_b64_tr_b16 v[240:241], v250 offset:1024\n\t" \
  "ds_read_b64_tr_b16 v[242:243], v250 offset:1088\n\t" \
  "ds_read_b64_tr_b16 v[244:245], v250 offset:1536\n\t" \
  "ds_read_b64_tr_b16 v[246:247], v250 offset:1600\n\t" \
  "s_waitcnt lgkmcnt(0)\n\t" \
  "s_branch .Lrb_%=\n\t" \
  ".Lvs_%=:\n\t" \
  "v_cmp_eq_u32 vcc, %[sent], v252\n\t" \
  "v_cmp_eq_u32 s[94:95], %[sent], v253\n\t" \
  "s_or_b64 s[94:95], s[94:95], vcc\n\t" \
  "s_cbranch_scc0 .Lvok_%=\n\t" \
  "s_add_u32 s92, s92, 1\n\t" \
  "s_sleep 2\n\t" \
  "s_and_b32 s93, s92, 63\n\t" \
  "s_cbranch_scc1 .Lvr_%=\n\t" \
  "buffer_load_dword v204, off, %[rs], %[sab] sc1\n\t" \
  "s_waitcnt vmcnt(0)\n\t" \
  "v_readfirstlane_b32 s93, v204\n\t" \
  "s_nop 3\n\t" \
  "s_cmp_eq_u32 s93, 1\n\t" \
  "s_cbranch_scc1 .Lvok_%=\n\t" \
  "s_cmp_lt_u32 s92, 0x80000\n\t" \
  "s_cbranch_scc1 .Lvr_%=\n\t" \
  "v_mov_b32 v204, 1\n\t" \
  "buffer_store_dword v204, off, %[rs], %[sab] sc1\n\t" \
  "s_branch .Lvok_%=\n\t" \
  ".Lvr_%=:\n\t" \
  "buffer_load_dword v252, v249, %[rs], %[sv] offen offset:0 sc1\n\t" \
  "buffer_load_dword v253, v249, %[rs], %[sv] offen offset:256 sc1\n\t" \
  "s_waitcnt vmcnt(0)\n\t" \
  "v_mfma_f32_16x16x16_bf16 v[224:227], v[252:253], v[232:233], 0\n\t" \
  "v_mfma_f32_16x16x16_bf16 v[228:231], v[252:253], v[234:235], 0\n\t" \
  "v_max_u32 v204, v252, v253\n\t" \
  "v_cmp_le_u32 vcc, %[sent], v204\n\t" \
  "s_nop 4\n\t" \
  "s_branch .Lpv_%=\n\t" \
  "s_branch .Lend_%=\n\t" \
  ".Lslow_%=:\n\t" \
  "s_mov_b64 s[94:95], 0\n\t" \
  "v_cmp_eq_u32 vcc, %[sent], v200\n\t" \
  "s_or_b64 s[94:95], s[94:95], vcc\n\t" \
  "v_cmp_eq_u32 vcc, %[sent], v201\n\t" \
  "s_or_b64 s[94:95], s[94:95], vcc\n\t" \
  "v_cmp_eq_u32 vcc, %[sent], v202\n\t" \
  "s_or_b64 s[94:95], s[94:95], vcc\n\t" \
  "v_cmp_eq_u32 vcc, %[sent], v203\n\t" \
  "s_or_b64 s[94:95], s[94:95], vcc\n\t" \
  "v_cmp_eq_u32 vcc, %[sent], v204\n\t" \
  "s_or_b64 s[94:95], s[94:95], vcc\n\t" \
  "v_cmp_eq_u32 vcc, %[sent], v205\n\t" \
  "s_or_b64 s[94:95], s[94:95], vcc\n\t" \
  "v_cmp_eq_u32 vcc, %[sent], v206\n\t" \
  "s_or_b64 s[94:95], s[94:95], vcc\n\t" \
  "v_cmp_eq_u32 vcc, %[sent], v207\n\t" \
  "s_or_b64 s[94:95], s[94:95], vcc\n\t" \
  "s_cmp_eq_u64 s[94:95], 0\n\t" \
  "s_cbranch_scc1 .Lok_%=\n\t" \
  "v_mov_b64 v[192:193], 0\n\t" \
  "v_mov_b64 v[194:195], 0\n\t" \
  "v_mov_b64 v[196:197], 0\n\t" \
  "v_mov_b64 v[198:199], 0\n\t" \
  "s_add_u32 s92, s92, 1\n\t" \
  "s_cmp_eq_u32 s92, 1\n\t" \
  "s_cbranch_scc1 .Lpoll_%=\n\t" \
  "s_sleep 2\n\t" \
  "s_and_b32 s93, s92, 63\n\t" \
  "s_cmp_lg_u32 s93, 0\n\t" \
  "s_cbranch_scc1 .Lpoll_%=\n\t" \
  "v_mov_b32 v245, 0\n\t" \
  "buffer_load_dword v244, v245, %[rs], %[sab] offen sc1\n\t" \
  "s_waitcnt vmcnt(0)\n\t" \
  "v_readfirstlane_b32 s93, v244\n\t" \
  "s_nop 3\n\t" \
  "s_cmp_eq_u32 s93, 1\n\t" \
  "s_cbranch_scc1 .Lok_%=\n\t" \
  "s_cmp_lt_u32 s92, 0x80000\n\t" \
  "s_cbranch_scc1 .Lpoll_%=\n\t" \
  "v_mov_b32 v244, 1\n\t" \
  "buffer_store_dword v244, v245, %[rs], %[sab] offen sc1\n\t" \
  "s_branch .Lok_%=\n\t" \
  ".Lend_%=:\n\t"
#define QK_ASM_1 \
  "s_waitcnt lgkmcnt(0)\n\t" \
  "v_subrev_u32 v244, 0x8000, v250\n\t" \
  "ds_read_b128 v[216:219], v248 offset:36864\n\t" \
  "ds_read_b128 v[220:223], v248 offset:37888\n\t" \
  "ds_read_b128 v[224:227], v248 offset:38912\n\t" \
  "ds_read_b128 v[228:231], v248 offset:39936\n\t" \
  "ds_read_b128 v[208:211], v244 offset:0\n\t" \
  "ds_read_b128 v[212:215], v244 offset:1024\n\t" \
  "ds_read_b128 v[236:239], v244 offset:2048\n\t" \
  "ds_read_b128 v[240:243], v244 offset:3072\n\t" \
  "v_mov_b64 v[192:193], 0\n\t" \
  "v_mov_b64 v[194:195], 0\n\t" \
  "v_mov_b64 v[196:197], 0\n\t" \
  "v_mov_b64 v[198:199], 0\n\t" \
  ".p2align 5\n\t" \
  "s_nop 0\n\t" \
  "s_nop 0\n\t" \
  "s_nop 0\n\t" \
  "s_nop 0\n\t" \
  "s_movk_i32 s92, 0x2000\n\t" \
  ".Lwt_%=:\n\t" \
  "ds_read_b64 v[200:201], v248 offset:30720\n\t" \
  "s_memrealtime s[90:91]\n\t" \
  "s_sub_u32 s92, s92, 1\n\t" \
  "s_waitcnt lgkmcnt(0)\n\t" \
  "v_readfirstlane_b32 s93, v200\n\t" \
  "v_readfirstlane_b32 %[tgt], v201\n\t" \
  "s_cbranch_scc1 .Lgo_%=\n\t" \
  "s_cmp_eq_u32 s93, %[step]\n\t" \
  "s_cbranch_scc1 .Lwx_%=\n\t" \
  "s_sleep 1\n\t" \
  "s_branch .Lwt_%=\n\t" \
  ".Lwx_%=:\n\t" \
  "s_lshl_b32 s93, s90, 2\n\t" \
  "s_sub_i32 s93, %[tgt], s93\n\t" \
  "s_cmp_le_i32 s93, 0\n\t" \
  "s_cbranch_scc1 .Lgo_%=\n\t" \
  "s_min_i32 s93, s93, 8000\n\t" \
  "s_mov_b32 s94, s93\n\t" \
  ".Lsl_%=:\n\t" \
  "s_sub_i32 s94, s94, 18\n\t" \
  "s_cmp_lt_i32 s94, 30\n\t" \
  "s_cbranch_scc1 .Lsx_%=\n\t" \
  "s_sleep 1\n\t" \
  "s_branch .Lsl_%=\n\t" \
  ".Lsx_%=:\n\t" \
  "s_sub_i32 s93, s93, 12\n\t" \
  "s_max_i32 s93, s93, 4\n\t" \
  "s_lshr_b32 s93, s93, 2\n\t" \
  "s_add_u32 s93, s93, s90\n\t" \
  "s_memrealtime s[90:91]\n\t" \
  ".Lpace_%=:\n\t" \
  "s_memrealtime s[94:95]\n\t" \
  "s_waitcnt lgkmcnt(1)\n\t" \
  "s_sub_i32 s92, s90, s93\n\t" \
  "s_cmp_ge_i32 s92, 0\n\t" \
  "s_cbranch_scc1 .Le_%=\n\t" \
  "s_memrealtime s[90:91]\n\t" \
  "s_waitcnt lgkmcnt(1)\n\t" \
  "s_sub_i32 s92, s94, s93\n\t" \
  "s_cmp_lt_i32 s92, 0\n\t" \
  "s_cbranch_scc1 .Lpace_%=\n\t" \
  "s_nop 4\n\t" \
  ".Le_%=:\n\t" \
  "s_add_u32 s92, s92, s93\n\t" \
  "s_lshl_b32 s92, s92, 2\n\t" \
  "s_sub_i32 s92, %[tgt], s92\n\t" \
  "s_max_i32 s92, s92, 0\n\t" \
  "s_bitcmp0_b32 s92, 3\n\t" \
  "s_cbranch_scc1 .Lf3_%=\n\t" \
  "s_nop 15\n\t" \
  ".Lf3_%=:\n\t" \
  "s_bitcmp0_b32 s92, 2\n\t" \
  "s_cbranch_scc1 .Lf2_%=\n\t" \
  "s_nop 10\n\t" \
  ".Lf2_%=:\n\t" \
  ".Lgo_%=:\n\t" \
  "s_mov_b32 s92, 0\n\t" \
  ".Lpoll_%=:\n\t" \
  "buffer_load_dwordx4 v[200:203], v248, %[rs], %[som] offen sc1\n\t" \
  "buffer_load_dwordx4 v[204:207], v248, %[rs], %[som2] offen sc1\n\t" \
  ".p2align 5\n\t" \
  "s_waitcnt vmcnt(1) lgkmcnt(0)\n\t" \
  "ds_write_b128 v249, v[200:203]\n\t" \
  "v_smfmac_f32_16x16x64_bf16 v[192:195], v[200:203], v[208:215], v251\n\t" \
  "v_max3_u32 v244, v200, v201, v202\n\t" \
  "v_smfmac_f32_16x16x64_bf16 v[196:199], v[200:203], v[236:243], v251\n\t" \
  "s_waitcnt vmcnt(0)\n\t" \
  "ds_write_b128 v249, v[204:207] offset:8192\n\t" \
  "v_smfmac_f32_16x16x64_bf16 v[192:195], v[204:207], v[216:223], v251\n\t" \
  "v_max3_u32 v244, v244, v203, v204\n\t" \
  "v_max3_u32 v244, v244, v205, v206\n\t" \
  "v_smfmac_f32_16x16x64_bf16 v[196:199], v[204:207], v[224:231], v251\n\t" \
  "v_max_u32 v244, v244, v207\n\t" \
  "v_cmp_le_u32_e64 vcc, %[sent], v244\n\t" \
  "s_cbranch_vccnz .Lslow_%=\n\t" \
  ".Lok_%=:\n\t" \
  "ds_read_b128 v[216:219], v250 offset:32768\n\t" \
  "ds_read_b128 v[220:223], v250 offset:33792\n\t" \
  "ds_read_b128 v[224:227], v250 offset:34816\n\t" \
  "ds_read_b128 v[228:231], v250 offset:35840\n\t" \
  "s_waitcnt lgkmcnt(4)\n\t" \
  "s_barrier\n\t" \
  "ds_read_b128 v[208:211], v248 offset:0\n\t" \
  "ds_read_b128 v[212:215], v248 offset:3072\n\t" \
  "ds_read_b128 v[236:239], v248 offset:4096\n\t" \
  "ds_read_b128 v[240:243], v248 offset:5120\n\t" \
  "s_cmp_eq_u32 %[step], 63\n\t" \
  "s_cbranch_scc1 .Lls_%=\n\t" \
  "s_waitcnt lgkmcnt(3)\n\t" \
  "ds_read_b128 v[244:247], v248 offset:6144\n\t" \
  "v_smfmac_f32_16x16x64_bf16 v[192:195], v[208:211], v[216:223], v251\n\t" \
  "ds_read_b128 v[200:203], v248 offset:12288\n\t" \
  "v_smfmac_f32_16x16x64_bf16 v[196:199], v[208:211], v[224:231], v251\n\t" \
  "s_waitcnt lgkmcnt(4)\n\t" \
  "ds_read_b128 v[216:219], v250 offset:0\n\t" \
  "v_smfmac_f32_16x16x64_bf16 v[192:195], v[212:215], v[0:7], v251\n\t" \
  "ds_read_b128 v[220:223], v250 offset:1024\n\t" \
  "v_smfmac_f32_16x16x64_bf16 v[196:199], v[212:215], v[8:15], v251\n\t" \
  "s_waitcnt lgkmcnt(5)\n\t" \
  "ds_read_b128 v[224:227], v250 offset:2048\n\t" \
  "v_smfmac_f32_16x16x64_bf16 v[192:195], v[236:239], v[16:23], v251\n\t" \
  "ds_read_b128 v[228:231], v250 offset:3072\n\t" \
  "v_smfmac_f32_16x16x64_bf16 v[196:199], v[236:239], v[24:31], v251\n\t" \
  "s_waitcnt lgkmcnt(5)\n\t" \
  "ds_read_b128 v[204:207], v248 offset:7168\n\t" \
  "v_smfmac_f32_16x16x64_bf16 v[192:195], v[240:243], v[32:39], v251\n\t" \
  "ds_read_b128 v[208:211], v248 offset:8192\n\t" \
  "v_smfmac_f32_16x16x64_bf16 v[196:199], v[240:243], v[40:47], v251\n\t" \
  "ds_read_b128 v[212:215], v248 offset:10240\n\t" \
  "v_smfmac_f32_16x16x64_bf16 v[192:195], v[244:247], v[48:55], v251\n\t" \
  "ds_read_b128 v[236:239], v248 offset:11264\n\t" \
  "v_smfmac_f32_16x16x64_bf16 v[196:199], v[244:247], v[56:63], v251\n\t" \
  "s_waitcnt lgkmcnt(6)\n\t" \
  "v_smfmac_f32_16x16x64_bf16 v[192:195], v[200:203], v[216:223], v251\n\t" \
  "s_waitcnt lgkmcnt(4)\n\t" \
  "v_smfmac_f32_16x16x64_bf16 v[196:199], v[200:203], v[224:231], v251\n\t" \
  "s_waitcnt lgkmcnt(3)\n\t" \
  "ds_read_b128 v[240:243], v248 offset:13312\n\t" \
  "v_smfmac_f32_16x16x64_bf16 v[192:195], v[204:207], v[64:71], v251\n\t" \
  "ds_read_b128 v[244:247], v248 offset:14336\n\t" \
  "v_smfmac_f32_16x16x64_bf16 v[196:199], v[204:207], v[72:79], v251\n\t" \
  "s_waitcnt lgkmcnt(3)\n\t" \
  "v_smfmac_f32_16x16x64_bf16 v[192:195], v[208:211], v[80:87], v251\n\t" \
  "ds_read_b128 v[200:203], v248 offset:15360\n\t" \
  "v_smfmac_f32_16x16x64_bf16 v[196:199], v[208:211], v[88:95], v251\n\t" \
  "ds_read_b128 v[204:207], v248 offset:2048\n\t" \
  "v_smfmac_f32_16x16x64_bf16 v[192:195], v[212:215], v[96:103], v251\n\t" \
  "v_smfmac_f32_16x16x64_bf16 v[196:199], v[212:215], v[104:111], v251\n\t" \
  "s_waitcnt lgkmcnt(3)\n\t" \
  "v_smfmac_f32_16x16x64_bf16 v[192:195], v[236:239], v[112:119], v251\n\t" \
  "v_smfmac_f32_16x16x64_bf16 v[196:199], v[236:239], v[120:127], v251\n\t" \
  "v_smfmac_f32_16x16x64_bf16 v[192:195], v[240:243], v[128:135], v251\n\t" \
  "v_smfmac_f32_16x16x64_bf16 v[196:199], v[240:243], v[136:143], v251\n\t" \
  "s_waitcnt lgkmcnt(0)\n\t" \
  "v_smfmac_f32_16x16x64_bf16 v[192:195], v[244:247], v[144:151], v251\n\t" \
  "v_smfmac_f32_16x16x64_bf16 v[196:199], v[244:247], v[152:159], v251\n\t" \
  "v_smfmac_f32_16x16x64_bf16 v[192:195], v[200:203], v[160:167], v251\n\t" \
  "v_smfmac_f32_16x16x64_bf16 v[196:199], v[200:203], v[168:175], v251\n\t" \
  "v_smfmac_f32_16x16x64_bf16 v[192:195], v[204:207], v[176:183], v251\n\t" \
  "v_smfmac_f32_16x16x64_bf16 v[196:199], v[204:207], v[184:191], v251\n\t" \
  "ds_read_b128 v[200:203], v248 offset:17408\n\t" \
  "ds_read_b128 v[204:207], v248 offset:21504\n\t" \
  "v_mov_b32 v250, v232\n\t" \
  "v_mov_b32 v249, v233\n\t" \
  "s_nop 2\n\t" \
  "v_add_f32 v192, v192, v194\n\t" \
  "v_add_f32 v193, v193, v195\n\t" \
  "v_add_f32 v196, v196, v198\n\t" \
  "v_add_f32_e64 v197, v197, v199\n\t" \
  "v_cvt_pk_bf16_f32 v192, v192, v193\n\t" \
  "v_cvt_pk_bf16_f32 v196, v196, v197\n\t" \
  "ds_write2_b32 v252, v192, v196 offset1:64\n\t" \
  "v_mov_b32_e64 v251, v234\n\t" \
  "ds_read_b64_tr_b16 v[236:237], v250 offset:512\n\t" \
  "ds_read_b64_tr_b16 v[238:239], v250 offset:576\n\t" \
  "v_mov_b32_e64 v212, %[step]\n\t" \
  "s_mov_b64 exec, 1\n\t" \
  "ds_write_b32 v248, v212 offset:28688\n\t" \
  "s_mov_b64 exec, -1\n\t" \
  "buffer_load_dwordx4 v[192:195], v248, %[rt], %[svt] offen offset:0 sc1\n\t" \
  "buffer_load_dwordx4 v[196:199], v248, %[rt], %[svt] offen offset:1024 sc1\n\t" \
  "s_waitcnt lgkmcnt(1)\n\t" \
  "v_mfma_f32_16x16x32_bf16 v[200:203], v[200:203], v[236:239], 0\n\t" \
  "v_mfma_f32_16x16x32_bf16 v[204:207], v[204:207], v[236:239], 0\n\t" \
  "s_nop 7\n\t" \
  "v_cndmask_b32 v216, v200, v204, %[nh]\n\t" \
  "v_cndmask_b32 v217, v201, v205, %[nh]\n\t" \
  "v_cndmask_b32 v218, v202, v206, %[nh]\n\t" \
  "v_cndmask_b32 v219, v203, v207, %[nh]\n\t" \
  "ds_write_b128 v248, v[216:219] offset:17408\n\t" \
  "buffer_load_dword v252, v249, %[rs], %[sv] offen offset:0 sc1\n\t" \
  "buffer_load_dword v253, v249, %[rs], %[sv] offen offset:256 sc1\n\t" \
  "ds_read_b32 v212, v248 offset:28672\n\t" \
  "ds_read_b64_tr_b16 v[240:241], v250 offset:1024\n\t" \
  "ds_read_b64_tr_b16 v[242:243], v250 offset:1088\n\t" \
  "ds_read_b64_tr_b16 v[244:245], v250 offset:1536\n\t" \
  "ds_read_b64_tr_b16 v[246:247], v250 offset:1600\n\t" \
  "ds_read_b64_tr_b16 v[232:233], v250 offset:0\n\t" \
  "ds_read_b64_tr_b16 v[234:235], v250 offset:64\n\t" \
  "s_setprio 3\n\t" \
  "s_waitcnt lgkmcnt(0)\n\t" \
  "v_cmp_ne_u32 vcc, %[step], v212\n\t" \
  "s_and_b32 s93, vcc_lo, 0xf\n\t" \
  "s_barrier\n\t" \
  "s_cbranch_scc1 .Lre_%=\n\t" \
  ".Lrb_%=:\n\t" \
  "ds_read_b64_tr_b16 v[204:205], v250 offset:2560\n\t" \
  "ds_read_b64_tr_b16 v[206:207], v250 offset:2624\n\t" \
  "ds_read_b64_tr_b16 v[208:209], v250 offset:3072\n\t" \
  "ds_read_b64_tr_b16 v[210:211], v250 offset:3136\n\t" \
  "ds_read_b64_tr_b16 v[212:213], v250 offset:3584\n\t" \
  "ds_read_b64_tr_b16 v[214:215], v250 offset:3648\n\t" \
  "ds_read_b64_tr_b16 v[200:201], v250 offset:2048\n\t" \
  "ds_read_b64_tr_b16 v[202:203], v250 offset:2112\n\t" \
  "ds_read_b128 v[220:223], v248 offset:18432\n\t" \
  "ds_read_b128 v[224:227], v248 offset:19456\n\t" \
  "ds_read_b128 v[228:231], v248 offset:16384\n\t" \
  "s_waitcnt lgkmcnt(9)\n\t" \
  "v_mfma_f32_16x16x32_bf16 v[204:207], v[204:207], v[236:239], 0\n\t" \
  "s_waitcnt lgkmcnt(7)\n\t" \
  "v_mfma_f32_16x16x32_bf16 v[204:207], v[208:211], v[240:243], v[204:207]\n\t" \
  "v_mov_b32 v237, 0\n\t" \
  "s_waitcnt lgkmcnt(5)\n\t" \
  "v_mfma_f32_16x16x32_bf16 v[204:207], v[212:215], v[244:247], v[204:207]\n\t" \
  "v_mov_b32 v238, 0\n\t" \
  "s_waitcnt lgkmcnt(3)\n\t" \
  "v_mfma_f32_16x16x32_bf16 v[204:207], v[200:203], v[232:235], v[204:207]\n\t" \
  "s_waitcnt lgkmcnt(2)\n\t" \
  "v_pk_add_f32 v[216:217], v[216:217], v[220:221]\n\t" \
  "v_pk_add_f32 v[218:219], v[218:219], v[222:223]\n\t" \
  "s_waitcnt lgkmcnt(0)\n\t" \
  "v_pk_add_f32 v[224:225], v[224:225], v[228:229]\n\t" \
  "v_pk_add_f32 v[226:227], v[226:227], v[230:231]\n\t" \
  "v_pk_add_f32 v[216:217], v[216:217], v[224:225]\n\t" \
  "v_pk_add_f32 v[218:219], v[218:219], v[226:227]\n\t" \
  "v_max3_f32 v224, v216, v217, v218\n\t" \
  "v_max_f32 v224, v224, v219\n\t" \
  "v_max3_f32 v231, v204, v205, v206\n\t" \
  "v_max_f32 v231, v231, v207\n\t" \
  "v_max_f32_dpp v225, v224, v224 row_ror:8 row_mask:0xf bank_mask:0xf\n\t" \
  "v_max_f32_dpp v226, v224, v224 row_ror:8 row_mask:0xf bank_mask:0xf\n\t" \
  "v_cndmask_b32 v204, v204, v206, %[up]\n\t" \
  "v_cndmask_b32 v205, v205, v207, %[up]\n\t" \
  "v_permlane32_swap_b32_e64 v225, v226\n\t" \
  "v_max3_f32 v209, v225, v226, v231\n\t" \
  "v_max3_f32 v210, v225, v226, v231\n\t" \
  "v_mov_b64 v[220:221], %[one]\n\t" \
  "v_mov_b64 v[222:223], %[one]\n\t" \
  "v_permlane16_swap_b32 v209, v210\n\t" \
  "v_max_f32 v208, v209, v210\n\t" \
  "v_mul_f32_e64 v208, %[negc], v208\n\t" \
  "v_fma_f32 v204, v204, %[c], v208\n\t" \
  "v_fma_f32 v205, v205, %[c], v208\n\t" \
  "v_exp_f32 v204, v204\n\t" \
  "v_fma_f32 v216, v216, %[c], v208\n\t" \
  "v_exp_f32 v205, v205\n\t" \
  "v_fma_f32 v217, v217, %[c], v208\n\t" \
  "v_exp_f32_e64 v216, v216\n\t" \
  "v_fma_f32 v218, v218, %[c], v208\n\t" \
  "v_cvt_pk_bf16_f32 v236, v204, v205\n\t" \
  "v_cvt_pk_bf16_f32 v239, v204, v205\n\t" \
  "v_exp_f32 v217, v217\n\t" \
  "v_fma_f32 v219, v219, %[c], v208\n\t" \
  "v_mfma_f32_16x16x16_bf16 v[200:203], v[220:221], v[236:237], 0\n\t" \
  "v_exp_f32 v218, v218\n\t" \
  "s_waitcnt vmcnt(0)\n\t" \
  "v_mfma_f32_16x16x16_bf16 v[224:227], v[252:253], v[236:237], 0\n\t" \
  "v_cvt_pk_bf16_f32 v214, v216, v217\n\t" \
  "v_cvt_pk_bf16_f32 v212, v216, v217\n\t" \
  "v_exp_f32 v219, v219\n\t" \
  "v_mfma_f32_16x16x16_bf16 v[228:231], v[252:253], v[238:239], 0\n\t" \
  "v_cvt_pk_bf16_f32 v215, v218, v219\n\t" \
  "v_cvt_pk_bf16_f32 v213, v218, v219\n\t" \
  "v_mov_b32_dpp v212, v214 row_ror:8 row_mask:0xf bank_mask:0xc\n\t" \
  "v_mov_b32_dpp v214, v214 row_ror:8 row_mask:0xf bank_mask:0x3\n\t" \
  "v_mov_b32_dpp v213, v215 row_ror:8 row_mask:0xf bank_mask:0xc\n\t" \
  "v_mov_b32_dpp v215, v215 row_ror:8 row_mask:0xf bank_mask:0x3\n\t" \
  "v_max_u32 v208, v252, v253\n\t" \
  "v_cmp_le_u32 vcc, %[sent], v208\n\t" \
  "v_mfma_f32_16x16x32_bf16 v[200:203], v[220:223], v[212:215], v[200:203]\n\t" \
  ".Lpv_%=:\n\t" \
  "v_mfma_f32_16x16x32_bf16 v[224:227], v[192:195], v[212:215], v[224:227]\n\t" \
  "v_mfma_f32_16x16x32_bf16 v[228:231], v[196:199], v[212:215], v[228:231]\n\t" \
  "s_cbranch_vccnz .Lvs_%=\n\t" \
  ".Lvok_%=:\n\t" \
  "s_nop 4\n\t" \
  "v_rcp_f32_e64 v208, v200\n\t" \
  "v_cndmask_b32 v224, v224, v226, %[nh]\n\t" \
  "v_cndmask_b32 v225, v225, v227, %[nh]\n\t" \
  "v_cndmask_b32 v228, v228, v230, %[nh]\n\t" \
  "v_cndmask_b32 v229, v229, v231, %[nh]\n\t" \
  "v_pk_mul_f32 v[224:225], v[224:225], v[208:209] op_sel_hi:[1,0]\n\t" \
  "v_pk_mul_f32 v[228:229], v[228:229], v[208:209] op_sel_hi:[1,0]\n\t" \
  "v_cvt_pk_bf16_f32 v224, v224, v225\n\t" \
  "v_cvt_pk_bf16_f32 v225, v228, v229\n\t" \
  "buffer_store_dwordx2 v[224:225], v251, %[rs], %[so] offen sc1\n\t" \
  "s_memtime s[90:91]\n\t" \
  "s_setprio 0\n\t" \
  "s_waitcnt lgkmcnt(0)\n\t" \
  "s_mov_b32 %[tout], s90\n\t" \
  "s_mov_b32 %[spins], s92\n\t" \
  "s_branch .Lend_%=\n\t" \
  ".Lls_%=:\n\t" \
  "s_waitcnt lgkmcnt(0)\n\t" \
  "s_barrier\n\t" \
  "s_mov_b32 %[spins], 0\n\t" \
  "s_branch .Lend_%=\n\t" \
  ".Lre_%=:\n\t" \
  "ds_read_b64_tr_b16 v[240:241], v250 offset:1024\n\t" \
  "ds_read_b64_tr_b16 v[242:243], v250 offset:1088\n\t" \
  "ds_read_b64_tr_b16 v[244:245], v250 offset:1536\n\t" \
  "ds_read_b64_tr_b16 v[246:247], v250 offset:1600\n\t" \
  "ds_read_b64_tr_b16 v[232:233], v250 offset:0\n\t" \
  "ds_read_b64_tr_b16 v[234:235], v250 offset:64\n\t" \
  "s_waitcnt lgkmcnt(0)\n\t" \
  "s_branch .Lrb_%=\n\t" \
  ".Lvs_%=:\n\t" \
  "v_cmp_eq_u32 vcc, %[sent], v252\n\t" \
  "v_cmp_eq_u32 s[94:95], %[sent], v253\n\t" \
  "s_or_b64 s[94:95], s[94:95], vcc\n\t" \
  "s_cbranch_scc0 .Lvok_%=\n\t" \
  "s_add_u32 s92, s92, 1\n\t" \
  "s_sleep 2\n\t" \
  "s_and_b32 s93, s92, 63\n\t" \
  "s_cbranch_scc1 .Lvr_%=\n\t" \
  "buffer_load_dword v208, off, %[rs], %[sab] sc1\n\t" \
  "s_waitcnt vmcnt(0)\n\t" \
  "v_readfirstlane_b32 s93, v208\n\t" \
  "s_nop 3\n\t" \
  "s_cmp_eq_u32 s93, 1\n\t" \
  "s_cbranch_scc1 .Lvok_%=\n\t" \
  "s_cmp_lt_u32 s92, 0x80000\n\t" \
  "s_cbranch_scc1 .Lvr_%=\n\t" \
  "v_mov_b32 v208, 1\n\t" \
  "buffer_store_dword v208, off, %[rs], %[sab] sc1\n\t" \
  "s_branch .Lvok_%=\n\t" \
  ".Lvr_%=:\n\t" \
  "buffer_load_dword v252, v249, %[rs], %[sv] offen offset:0 sc1\n\t" \
  "buffer_load_dword v253, v249, %[rs], %[sv] offen offset:256 sc1\n\t" \
  "s_waitcnt vmcnt(0)\n\t" \
  "v_mfma_f32_16x16x16_bf16 v[224:227], v[252:253], v[236:237], 0\n\t" \
  "v_mfma_f32_16x16x16_bf16 v[228:231], v[252:253], v[238:239], 0\n\t" \
  "v_max_u32 v208, v252, v253\n\t" \
  "v_cmp_le_u32 vcc, %[sent], v208\n\t" \
  "s_nop 4\n\t" \
  "s_branch .Lpv_%=\n\t" \
  "s_branch .Lend_%=\n\t" \
  ".Lslow_%=:\n\t" \
  "s_mov_b64 s[94:95], 0\n\t" \
  "v_cmp_eq_u32 vcc, %[sent], v200\n\t" \
  "s_or_b64 s[94:95], s[94:95], vcc\n\t" \
  "v_cmp_eq_u32 vcc, %[sent], v201\n\t" \
  "s_or_b64 s[94:95], s[94:95], vcc\n\t" \
  "v_cmp_eq_u32 vcc, %[sent], v202\n\t" \
  "s_or_b64 s[94:95], s[94:95], vcc\n\t" \
  "v_cmp_eq_u32 vcc, %[sent], v203\n\t" \
  "s_or_b64 s[94:95], s[94:95], vcc\n\t" \
  "v_cmp_eq_u32 vcc, %[sent], v204\n\t" \
  "s_or_b64 s[94:95], s[94:95], vcc\n\t" \
  "v_cmp_eq_u32 vcc, %[sent], v205\n\t" \
  "s_or_b64 s[94:95], s[94:95], vcc\n\t" \
  "v_cmp_eq_u32 vcc, %[sent], v206\n\t" \
  "s_or_b64 s[94:95], s[94:95], vcc\n\t" \
  "v_cmp_eq_u32 vcc, %[sent], v207\n\t" \
  "s_or_b64 s[94:95], s[94:95], vcc\n\t" \
  "s_cmp_eq_u64 s[94:95], 0\n\t" \
  "s_cbranch_scc1 .Lok_%=\n\t" \
  "v_mov_b64 v[192:193], 0\n\t" \
  "v_mov_b64 v[194:195], 0\n\t" \
  "v_mov_b64 v[196:197], 0\n\t" \
  "v_mov_b64 v[198:199], 0\n\t" \
  "s_add_u32 s92, s92, 1\n\t" \
  "s_cmp_eq_u32 s92, 1\n\t" \
  "s_cbranch_scc1 .Lpoll_%=\n\t" \
  "s_sleep 2\n\t" \
  "s_and_b32 s93, s92, 63\n\t" \
  "s_cmp_lg_u32 s93, 0\n\t" \
  "s_cbranch_scc1 .Lpoll_%=\n\t" \
  "v_mov_b32 v245, 0\n\t" \
  "buffer_load_dword v244, v245, %[rs], %[sab] offen sc1\n\t" \
  "s_waitcnt vmcnt(0)\n\t" \
  "v_readfirstlane_b32 s93, v244\n\t" \
  "s_nop 3\n\t" \
  "s_cmp_eq_u32 s93, 1\n\t" \
  "s_cbranch_scc1 .Lok_%=\n\t" \
  "s_cmp_lt_u32 s92, 0x80000\n\t" \
  "s_cbranch_scc1 .Lpoll_%=\n\t" \
  "v_mov_b32 v244, 1\n\t" \
  "buffer_store_dword v244, v245, %[rs], %[sab] offen sc1\n\t" \
  "s_branch .Lok_%=\n\t" \
  ".Lend_%=:\n\t"
#define QK_ASM_2 \
  "s_waitcnt lgkmcnt(0)\n\t" \
  "v_subrev_u32 v244, 0x8000, v250\n\t" \
  "ds_read_b128 v[216:219], v248 offset:40960\n\t" \
  "ds_read_b128 v[220:223], v248 offset:41984\n\t" \
  "ds_read_b128 v[224:227], v248 offset:43008\n\t" \
  "ds_read_b128 v[228:231], v248 offset:44032\n\t" \
  "ds_read_b128 v[208:211], v244 offset:0\n\t" \
  "ds_read_b128 v[212:215], v244 offset:1024\n\t" \
  "ds_read_b128 v[236:239], v244 offset:2048\n\t" \
  "ds_read_b128 v[240:243], v244 offset:3072\n\t" \
  "v_mov_b64 v[192:193], 0\n\t" \
  "v_mov_b64 v[194:195], 0\n\t" \
  "v_mov_b64 v[196:197], 0\n\t" \
  "v_mov_b64 v[198:199], 0\n\t" \
  ".p2align 5\n\t" \
  "s_nop 0\n\t" \
  "s_nop 0\n\t" \
  "s_nop 0\n\t" \
  "s_nop 0\n\t" \
  "s_movk_i32 s92, 0x2000\n\t" \
  ".Lwt_%=:\n\t" \
  "ds_read_b64 v[200:201], v248 offset:30720\n\t" \
  "s_memrealtime s[90:91]\n\t" \
  "s_sub_u32 s92, s92, 1\n\t" \
  "s_waitcnt lgkmcnt(0)\n\t" \
  "v_readfirstlane_b32 s93, v200\n\t" \
  "v_readfirstlane_b32 %[tgt], v201\n\t" \
  "s_cbranch_scc1 .Lgo_%=\n\t" \
  "s_cmp_eq_u32 s93, %[step]\n\t" \
  "s_cbranch_scc1 .Lwx_%=\n\t" \
  "s_sleep 1\n\t" \
  "s_branch .Lwt_%=\n\t" \
  ".Lwx_%=:\n\t" \
  "s_lshl_b32 s93, s90, 2\n\t" \
  "s_sub_i32 s93, %[tgt], s93\n\t" \
  "s_cmp_le_i32 s93, 0\n\t" \
  "s_cbranch_scc1 .Lgo_%=\n\t" \
  "s_min_i32 s93, s93, 8000\n\t" \
  "s_mov_b32 s94, s93\n\t" \
  ".Lsl_%=:\n\t" \
  "s_sub_i32 s94, s94, 18\n\t" \
  "s_cmp_lt_i32 s94, 30\n\t" \
  "s_cbranch_scc1 .Lsx_%=\n\t" \
  "s_sleep 1\n\t" \
  "s_branch .Lsl_%=\n\t" \
  ".Lsx_%=:\n\t" \
  "s_sub_i32 s93, s93, 12\n\t" \
  "s_max_i32 s93, s93, 4\n\t" \
  "s_lshr_b32 s93, s93, 2\n\t" \
  "s_add_u32 s93, s93, s90\n\t" \
  "s_memrealtime s[90:91]\n\t" \
  ".Lpace_%=:\n\t" \
  "s_memrealtime s[94:95]\n\t" \
  "s_waitcnt lgkmcnt(1)\n\t" \
  "s_sub_i32 s92, s90, s93\n\t" \
  "s_cmp_ge_i32 s92, 0\n\t" \
  "s_cbranch_scc1 .Le_%=\n\t" \
  "s_memrealtime s[90:91]\n\t" \
  "s_waitcnt lgkmcnt(1)\n\t" \
  "s_sub_i32 s92, s94, s93\n\t" \
  "s_cmp_lt_i32 s92, 0\n\t" \
  "s_cbranch_scc1 .Lpace_%=\n\t" \
  "s_nop 4\n\t" \
  ".Le_%=:\n\t" \
  "s_add_u32 s92, s92, s93\n\t" \
  "s_lshl_b32 s92, s92, 2\n\t" \
  "s_sub_i32 s92, %[tgt], s92\n\t" \
  "s_max_i32 s92, s92, 0\n\t" \
  "s_bitcmp0_b32 s92, 3\n\t" \
  "s_cbranch_scc1 .Lf3_%=\n\t" \
  "s_nop 15\n\t" \
  ".Lf3_%=:\n\t" \
  "s_bitcmp0_b32 s92, 2\n\t" \
  "s_cbranch_scc1 .Lf2_%=\n\t" \
  "s_nop 10\n\t" \
  ".Lf2_%=:\n\t" \
  ".Lgo_%=:\n\t" \
  "s_mov_b32 s92, 0\n\t" \
  ".Lpoll_%=:\n\t" \
  "buffer_load_dwordx4 v[200:203], v248, %[rs], %[som] offen sc1\n\t" \
  "buffer_load_dwordx4 v[204:207], v248, %[rs], %[som2] offen sc1\n\t" \
  ".p2align 5\n\t" \
  "s_waitcnt vmcnt(1) lgkmcnt(0)\n\t" \
  "ds_write_b128 v249, v[200:203]\n\t" \
  "v_smfmac_f32_16x16x64_bf16 v[192:195], v[200:203], v[208:215], v251\n\t" \
  "v_max3_u32 v244, v200, v201, v202\n\t" \
  "v_smfmac_f32_16x16x64_bf16 v[196:199], v[200:203], v[236:243], v251\n\t" \
  "s_waitcnt vmcnt(0)\n\t" \
  "ds_write_b128 v249, v[204:207] offset:8192\n\t" \
  "v_smfmac_f32_16x16x64_bf16 v[192:195], v[204:207], v[216:223], v251\n\t" \
  "v_max3_u32 v244, v244, v203, v204\n\t" \
  "v_max3_u32 v244, v244, v205, v206\n\t" \
  "v_smfmac_f32_16x16x64_bf16 v[196:199], v[204:207], v[224:231], v251\n\t" \
  "v_max_u32 v244, v244, v207\n\t" \
  "v_cmp_le_u32_e64 vcc, %[sent], v244\n\t" \
  "s_cbranch_vccnz .Lslow_%=\n\t" \
  ".Lok_%=:\n\t" \
  "ds_read_b128 v[216:219], v250 offset:32768\n\t" \
  "ds_read_b128 v[220:223], v250 offset:33792\n\t" \
  "ds_read_b128 v[224:227], v250 offset:34816\n\t" \
  "ds_read_b128 v[228:231], v250 offset:35840\n\t" \
  "s_waitcnt lgkmcnt(4)\n\t" \
  "s_barrier\n\t" \
  "ds_read_b128 v[208:211], v248 offset:3072\n\t" \
  "ds_read_b128 v[212:215], v248 offset:4096\n\t" \
  "ds_read_b128 v[236:239], v248 offset:5120\n\t" \
  "ds_read_b128 v[240:243], v248 offset:6144\n\t" \
  "s_cmp_eq_u32 %[step], 63\n\t" \
  "s_cbranch_scc1 .Lls_%=\n\t" \
  "s_waitcnt lgkmcnt(3)\n\t" \
  "ds_read_b128 v[244:247], v248 offset:7168\n\t" \
  "v_smfmac_f32_16x16x64_bf16 v[192:195], v[208:211], v[216:223], v251\n\t" \
  "ds_read_b128 v[200:203], v248 offset:13312\n\t" \
  "v_smfmac_f32_16x16x64_bf16 v[196:199], v[208:211], v[224:231], v251\n\t" \
  "s_waitcnt lgkmcnt(4)\n\t" \
  "ds_read_b128 v[216:219], v250 offset:0\n\t" \
  "v_smfmac_f32_16x16x64_bf16 v[192:195], v[212:215], v[0:7], v251\n\t" \
  "ds_read_b128 v[220:223], v250 offset:1024\n\t" \
  "v_smfmac_f32_16x16x64_bf16 v[196:199], v[212:215], v[8:15], v251\n\t" \
  "s_waitcnt lgkmcnt(5)\n\t" \
  "ds_read_b128 v[224:227], v250 offset:2048\n\t" \
  "v_smfmac_f32_16x16x64_bf16 v[192:195], v[236:239], v[16:23], v251\n\t" \
  "ds_read_b128 v[228:231], v250 offset:3072\n\t" \
  "v_smfmac_f32_16x16x64_bf16 v[196:199], v[236:239], v[24:31], v251\n\t" \
  "s_waitcnt lgkmcnt(5)\n\t" \
  "ds_read_b128 v[204:207], v248 offset:8192\n\t" \
  "v_smfmac_f32_16x16x64_bf16 v[192:195], v[240:243], v[32:39], v251\n\t" \
  "ds_read_b128 v[208:211], v248 offset:9216\n\t" \
  "v_smfmac_f32_16x16x64_bf16 v[196:199], v[240:243], v[40:47], v251\n\t" \
  "ds_read_b128 v[212:215], v248 offset:11264\n\t" \
  "v_smfmac_f32_16x16x64_bf16 v[192:195], v[244:247], v[48:55], v251\n\t" \
  "ds_read_b128 v[236:239], v248 offset:12288\n\t" \
  "v_smfmac_f32_16x16x64_bf16 v[196:199], v[244:247], v[56:63], v251\n\t" \
  "s_waitcnt lgkmcnt(6)\n\t" \
  "v_smfmac_f32_16x16x64_bf16 v[192:195], v[200:203], v[216:223], v251\n\t" \
  "s_waitcnt lgkmcnt(4)\n\t" \
  "v_smfmac_f32_16x16x64_bf16 v[196:199], v[200:203], v[224:231], v251\n\t" \
  "s_waitcnt lgkmcnt(3)\n\t" \
  "ds_read_b128 v[240:243], v248 offset:14336\n\t" \
  "v_smfmac_f32_16x16x64_bf16 v[192:195], v[204:207], v[64:71], v251\n\t" \
  "ds_read_b128 v[244:247], v248 offset:15360\n\t" \
  "v_smfmac_f32_16x16x64_bf16 v[196:199], v[204:207], v[72:79], v251\n\t" \
  "s_waitcnt lgkmcnt(3)\n\t" \
  "v_smfmac_f32_16x16x64_bf16 v[192:195], v[208:211], v[80:87], v251\n\t" \
  "ds_read_b128 v[200:203], v248 offset:0\n\t" \
  "v_smfmac_f32_16x16x64_bf16 v[196:199], v[208:211], v[88:95], v251\n\t" \
  "ds_read_b128 v[204:207], v248 offset:1024\n\t" \
  "v_smfmac_f32_16x16x64_bf16 v[192:195], v[212:215], v[96:103], v251\n\t" \
  "v_smfmac_f32_16x16x64_bf16 v[196:199], v[212:215], v[104:111], v251\n\t" \
  "s_waitcnt lgkmcnt(3)\n\t" \
  "v_smfmac_f32_16x16x64_bf16 v[192:195], v[236:239], v[112:119], v251\n\t" \
  "v_smfmac_f32_16x16x64_bf16 v[196:199], v[236:239], v[120:127], v251\n\t" \
  "v_smfmac_f32_16x16x64_bf16 v[192:195], v[240:243], v[128:135], v251\n\t" \
  "v_smfmac_f32_16x16x64_bf16 v[196:199], v[240:243], v[136:143], v251\n\t" \
  "s_waitcnt lgkmcnt(0)\n\t" \
  "v_smfmac_f32_16x16x64_bf16 v[192:195], v[244:247], v[144:151], v251\n\t" \
  "v_smfmac_f32_16x16x64_bf16 v[196:199], v[244:247], v[152:159], v251\n\t" \
  "v_smfmac_f32_16x16x64_bf16 v[192:195], v[200:203], v[160:167], v251\n\t" \
  "v_smfmac_f32_16x16x64_bf16 v[196:199], v[200:203], v[168:175], v251\n\t" \
  "v_smfmac_f32_16x16x64_bf16 v[192:195], v[204:207], v[176:183], v251\n\t" \
  "v_smfmac_f32_16x16x64_bf16 v[196:199], v[204:207], v[184:191], v251\n\t" \
  "ds_read_b128 v[200:203], v248 offset:18432\n\t" \
  "ds_read_b128 v[204:207], v248 offset:22528\n\t" \
  "v_mov_b32 v250, v232\n\t" \
  "v_mov_b32 v249, v233\n\t" \
  "s_nop 2\n\t" \
  "v_add_f32 v192, v192, v194\n\t" \
  "v_add_f32 v193, v193, v195\n\t" \
  "v_add_f32 v196, v196, v198\n\t" \
  "v_add_f32_e64 v197, v197, v199\n\t" \
  "v_cvt_pk_bf16_f32 v192, v192, v193\n\t" \
  "v_cvt_pk_bf16_f32 v196, v196, v197\n\t" \
  "ds_write2_b32 v252, v192, v196 offset1:64\n\t" \
  "v_mov_b32_e64 v251, v234\n\t" \
  "ds_read_b64_tr_b16 v[240:241], v250 offset:1024\n\t" \
  "ds_read_b64_tr_b16 v[242:243], v250 offset:1088\n\t" \
  "v_mov_b32_e64 v212, %[step]\n\t" \
  "s_mov_b64 exec, 1\n\t" \
  "ds_write_b32 v248, v212 offset:28704\n\t" \
  "s_mov_b64 exec, -1\n\t" \
  "buffer_load_dwordx4 v[192:195], v248, %[rt], %[svt] offen offset:0 sc1\n\t" \
  "buffer_load_dwordx4 v[196:199], v248, %[rt], %[svt] offen offset:1024 sc1\n\t" \
  "s_waitcnt lgkmcnt(1)\n\t" \
  "v_mfma_f32_16x16x32_bf16 v[200:203], v[200:203], v[240:243], 0\n\t" \
  "v_mfma_f32_16x16x32_bf16 v[204:207], v[204:207], v[240:243], 0\n\t" \
  "s_nop 7\n\t" \
  "v_cndmask_b32 v216, v200, v204, %[nh]\n\t" \
  "v_cndmask_b32 v217, v201, v205, %[nh]\n\t" \
  "v_cndmask_b32 v218, v202, v206, %[nh]\n\t" \
  "v_cndmask_b32 v219, v203, v207, %[nh]\n\t" \
  "ds_write_b128 v248, v[216:219] offset:18432\n\t" \
  "buffer_load_dword v252, v249, %[rs], %[sv] offen offset:0 sc1\n\t" \
  "buffer_load_dword v253, v249, %[rs], %[sv] offen offset:256 sc1\n\t" \
  "ds_read_b32 v212, v248 offset:28672\n\t" \
  "ds_read_b64_tr_b16 v[244:245], v250 offset:1536\n\t" \
  "ds_read_b64_tr_b16 v[246:247], v250 offset:1600\n\t" \
  "ds_read_b64_tr_b16 v[232:233], v250 offset:0\n\t" \
  "ds_read_b64_tr_b16 v[234:235], v250 offset:64\n\t" \
  "ds_read_b64_tr_b16 v[236:237], v250 offset:512\n\t" \
  "ds_read_b64_tr_b16 v[238:239], v250 offset:576\n\t" \
  "s_setprio 3\n\t" \
  "s_waitcnt lgkmcnt(0)\n\t" \
  "v_cmp_ne_u32 vcc, %[step], v212\n\t" \
  "s_and_b32 s93, vcc_lo, 0xf\n\t" \
  "s_barrier\n\t" \
  "s_cbranch_scc1 .Lre_%=\n\t" \
  ".Lrb_%=:\n\t" \
  "ds_read_b64_tr_b16 v[208:209], v250 offset:3072\n\t" \
  "ds_read_b64_tr_b16 v[210:211], v250 offset:3136\n\t" \
  "ds_read_b64_tr_b16 v[212:213], v250 offset:3584\n\t" \
  "ds_read_b64_tr_b16 v[214:215], v250 offset:3648\n\t" \
  "ds_read_b64_tr_b16 v[200:201], v250 offset:2048\n\t" \
  "ds_read_b64_tr_b16 v[202:203], v250 offset:2112\n\t" \
  "ds_read_b64_tr_b16 v[204:205], v250 offset:2560\n\t" \
  "ds_read_b64_tr_b16 v[206:207], v250 offset:2624\n\t" \
  "ds_read_b128 v[220:223], v248 offset:19456\n\t" \
  "ds_read_b128 v[224:227], v248 offset:16384\n\t" \
  "ds_read_b128 v[228:231], v248 offset:17408\n\t" \
  "s_waitcnt lgkmcnt(9)\n\t" \
  "v_mfma_f32_16x16x32_bf16 v[208:211], v[208:211], v[240:243], 0\n\t" \
  "s_waitcnt lgkmcnt(7)\n\t" \
  "v_mfma_f32_16x16x32_bf16 v[208:211], v[212:215], v[244:247], v[208:211]\n\t" \
  "v_mov_b32 v241, 0\n\t" \
  "s_waitcnt lgkmcnt(5)\n\t" \
  "v_mfma_f32_16x16x32_bf16 v[208:211], v[200:203], v[232:235], v[208:211]\n\t" \
  "v_mov_b32 v242, 0\n\t" \
  "s_waitcnt lgkmcnt(3)\n\t" \
  "v_mfma_f32_16x16x32_bf16 v[208:211], v[204:207], v[236:239], v[208:211]\n\t" \
  "s_waitcnt lgkmcnt(2)\n\t" \
  "v_pk_add_f32 v[216:217], v[216:217], v[220:221]\n\t" \
  "v_pk_add_f32 v[218:219], v[218:219], v[222:223]\n\t" \
  "s_waitcnt lgkmcnt(0)\n\t" \
  "v_pk_add_f32 v[224:225], v[224:225], v[228:229]\n\t" \
  "v_pk_add_f32 v[226:227], v[226:227], v[230:231]\n\t" \
  "v_pk_add_f32 v[216:217], v[216:217], v[224:225]\n\t" \
  "v_pk_add_f32 v[218:219], v[218:219], v[226:227]\n\t" \
  "v_max3_f32 v224, v216, v217, v218\n\t" \
  "v_max_f32 v224, v224, v219\n\t" \
  "v_max3_f32 v231, v208, v209, v210\n\t" \
  "v_max_f32 v231, v231, v211\n\t" \
  "v_max_f32_dpp v225, v224, v224 row_ror:8 row_mask:0xf bank_mask:0xf\n\t" \
  "v_max_f32_dpp v226, v224, v224 row_ror:8 row_mask:0xf bank_mask:0xf\n\t" \
  "v_cndmask_b32 v208, v208, v210, %[up]\n\t" \
  "v_cndmask_b32 v209, v209, v211, %[up]\n\t" \
  "v_permlane32_swap_b32_e64 v225, v226\n\t" \
  "v_max3_f32 v213, v225, v226, v231\n\t" \
  "v_max3_f32 v214, v225, v226, v231\n\t" \
  "v_mov_b64 v[220:221], %[one]\n\t" \
  "v_mov_b64 v[222:223], %[one]\n\t" \
  "v_permlane16_swap_b32 v213, v214\n\t" \
  "v_max_f32 v212, v213, v214\n\t" \
  "v_mul_f32_e64 v212, %[negc], v212\n\t" \
  "v_fma_f32 v208, v208, %[c], v212\n\t" \
  "v_fma_f32 v209, v209, %[c], v212\n\t" \
  "v_exp_f32 v208, v208\n\t" \
  "v_fma_f32 v216, v216, %[c], v212\n\t" \
  "v_exp_f32 v209, v209\n\t" \
  "v_fma_f32 v217, v217, %[c], v212\n\t" \
  "v_exp_f32_e64 v216, v216\n\t" \
  "v_fma_f32 v218, v218, %[c], v212\n\t" \
  "v_cvt_pk_bf16_f32 v240, v208, v209\n\t" \
  "v_cvt_pk_bf16_f32 v243, v208, v209\n\t" \
  "v_exp_f32 v217, v217\n\t" \
  "v_fma_f32 v219, v219, %[c], v212\n\t" \
  "v_mfma_f32_16x16x16_bf16 v[204:207], v[220:221], v[240:241], 0\n\t" \
  "v_exp_f32 v218, v218\n\t" \
  "s_waitcnt vmcnt(0)\n\t" \
  "v_mfma_f32_16x16x16_bf16 v[224:227], v[252:253], v[240:241], 0\n\t" \
  "v_cvt_pk_bf16_f32 v202, v216, v217\n\t" \
  "v_cvt_pk_bf16_f32 v200, v216, v217\n\t" \
  "v_exp_f32 v219, v219\n\t" \
  "v_mfma_f32_16x16x16_bf16 v[228:231], v[252:253], v[242:243], 0\n\t" \
  "v_cvt_pk_bf16_f32 v203, v218, v219\n\t" \
  "v_cvt_pk_bf16_f32 v201, v218, v219\n\t" \
  "v_mov_b32_dpp v200, v202 row_ror:8 row_mask:0xf bank_mask:0xc\n\t" \
  "v_mov_b32_dpp v202, v202 row_ror:8 row_mask:0xf bank_mask:0x3\n\t" \
  "v_mov_b32_dpp v201, v203 row_ror:8 row_mask:0xf bank_mask:0xc\n\t" \
  "v_mov_b32_dpp v203, v203 row_ror:8 row_mask:0xf bank_mask:0x3\n\t" \
  "v_max_u32 v212, v252, v253\n\t" \
  "v_cmp_le_u32 vcc, %[sent], v212\n\t" \
  "v_mfma_f32_16x16x32_bf16 v[204:207], v[220:223], v[200:203], v[204:207]\n\t" \
  ".Lpv_%=:\n\t" \
  "v_mfma_f32_16x16x32_bf16 v[224:227], v[192:195], v[200:203], v[224:227]\n\t" \
  "v_mfma_f32_16x16x32_bf16 v[228:231], v[196:199], v[200:203], v[228:231]\n\t" \
  "s_cbranch_vccnz .Lvs_%=\n\t" \
  ".Lvok_%=:\n\t" \
  "s_nop 4\n\t" \
  "v_rcp_f32_e64 v212, v204\n\t" \
  "v_cndmask_b32 v224, v224, v226, %[nh]\n\t" \
  "v_cndmask_b32 v225, v225, v227, %[nh]\n\t" \
  "v_cndmask_b32 v228, v228, v230, %[nh]\n\t" \
  "v_cndmask_b32 v229, v229, v231, %[nh]\n\t" \
  "v_pk_mul_f32 v[224:225], v[224:225], v[212:213] op_sel_hi:[1,0]\n\t" \
  "v_pk_mul_f32 v[228:229], v[228:229], v[212:213] op_sel_hi:[1,0]\n\t" \
  "v_cvt_pk_bf16_f32 v224, v224, v225\n\t" \
  "v_cvt_pk_bf16_f32 v225, v228, v229\n\t" \
  "buffer_store_dwordx2 v[224:225], v251, %[rs], %[so] offen sc1\n\t" \
  "s_memtime s[90:91]\n\t" \
  "s_setprio 0\n\t" \
  "s_waitcnt lgkmcnt(0)\n\t" \
  "s_mov_b32 %[tout], s90\n\t" \
  "s_mov_b32 %[spins], s92\n\t" \
  "s_branch .Lend_%=\n\t" \
  ".Lls_%=:\n\t" \
  "s_waitcnt lgkmcnt(0)\n\t" \
  "s_barrier\n\t" \
  "s_mov_b32 %[spins], 0\n\t" \
  "s_branch .Lend_%=\n\t" \
  ".Lre_%=:\n\t" \
  "ds_read_b64_tr_b16 v[244:245], v250 offset:1536\n\t" \
  "ds_read_b64_tr_b16 v[246:247], v250 offset:1600\n\t" \
  "ds_read_b64_tr_b16 v[232:233], v250 offset:0\n\t" \
  "ds_read_b64_tr_b16 v[234:235], v250 offset:64\n\t" \
  "ds_read_b64_tr_b16 v[236:237], v250 offset:512\n\t" \
  "ds_read_b64_tr_b16 v[238:239], v250 offset:576\n\t" \
  "s_waitcnt lgkmcnt(0)\n\t" \
  "s_branch .Lrb_%=\n\t" \
  ".Lvs_%=:\n\t" \
  "v_cmp_eq_u32 vcc, %[sent], v252\n\t" \
  "v_cmp_eq_u32 s[94:95], %[sent], v253\n\t" \
  "s_or_b64 s[94:95], s[94:95], vcc\n\t" \
  "s_cbranch_scc0 .Lvok_%=\n\t" \
  "s_add_u32 s92, s92, 1\n\t" \
  "s_sleep 2\n\t" \
  "s_and_b32 s93, s92, 63\n\t" \
  "s_cbranch_scc1 .Lvr_%=\n\t" \
  "buffer_load_dword v212, off, %[rs], %[sab] sc1\n\t" \
  "s_waitcnt vmcnt(0)\n\t" \
  "v_readfirstlane_b32 s93, v212\n\t" \
  "s_nop 3\n\t" \
  "s_cmp_eq_u32 s93, 1\n\t" \
  "s_cbranch_scc1 .Lvok_%=\n\t" \
  "s_cmp_lt_u32 s92, 0x80000\n\t" \
  "s_cbranch_scc1 .Lvr_%=\n\t" \
  "v_mov_b32 v212, 1\n\t" \
  "buffer_store_dword v212, off, %[rs], %[sab] sc1\n\t" \
  "s_branch .Lvok_%=\n\t" \
  ".Lvr_%=:\n\t" \
  "buffer_load_dword v252, v249, %[rs], %[sv] offen offset:0 sc1\n\t" \
  "buffer_load_dword v253, v249, %[rs], %[sv] offen offset:256 sc1\n\t" \
  "s_waitcnt vmcnt(0)\n\t" \
  "v_mfma_f32_16x16x16_bf16 v[224:227], v[252:253], v[240:241], 0\n\t" \
  "v_mfma_f32_16x16x16_bf16 v[228:231], v[252:253], v[242:243], 0\n\t" \
  "v_max_u32 v212, v252, v253\n\t" \
  "v_cmp_le_u32 vcc, %[sent], v212\n\t" \
  "s_nop 4\n\t" \
  "s_branch .Lpv_%=\n\t" \
  "s_branch .Lend_%=\n\t" \
  ".Lslow_%=:\n\t" \
  "s_mov_b64 s[94:95], 0\n\t" \
  "v_cmp_eq_u32 vcc, %[sent], v200\n\t" \
  "s_or_b64 s[94:95], s[94:95], vcc\n\t" \
  "v_cmp_eq_u32 vcc, %[sent], v201\n\t" \
  "s_or_b64 s[94:95], s[94:95], vcc\n\t" \
  "v_cmp_eq_u32 vcc, %[sent], v202\n\t" \
  "s_or_b64 s[94:95], s[94:95], vcc\n\t" \
  "v_cmp_eq_u32 vcc, %[sent], v203\n\t" \
  "s_or_b64 s[94:95], s[94:95], vcc\n\t" \
  "v_cmp_eq_u32 vcc, %[sent], v204\n\t" \
  "s_or_b64 s[94:95], s[94:95], vcc\n\t" \
  "v_cmp_eq_u32 vcc, %[sent], v205\n\t" \
  "s_or_b64 s[94:95], s[94:95], vcc\n\t" \
  "v_cmp_eq_u32 vcc, %[sent], v206\n\t" \
  "s_or_b64 s[94:95], s[94:95], vcc\n\t" \
  "v_cmp_eq_u32 vcc, %[sent], v207\n\t" \
  "s_or_b64 s[94:95], s[94:95], vcc\n\t" \
  "s_cmp_eq_u64 s[94:95], 0\n\t" \
  "s_cbranch_scc1 .Lok_%=\n\t" \
  "v_mov_b64 v[192:193], 0\n\t" \
  "v_mov_b64 v[194:195], 0\n\t" \
  "v_mov_b64 v[196:197], 0\n\t" \
  "v_mov_b64 v[198:199], 0\n\t" \
  "s_add_u32 s92, s92, 1\n\t" \
  "s_cmp_eq_u32 s92, 1\n\t" \
  "s_cbranch_scc1 .Lpoll_%=\n\t" \
  "s_sleep 2\n\t" \
  "s_and_b32 s93, s92, 63\n\t" \
  "s_cmp_lg_u32 s93, 0\n\t" \
  "s_cbranch_scc1 .Lpoll_%=\n\t" \
  "v_mov_b32 v245, 0\n\t" \
  "buffer_load_dword v244, v245, %[rs], %[sab] offen sc1\n\t" \
  "s_waitcnt vmcnt(0)\n\t" \
  "v_readfirstlane_b32 s93, v244\n\t" \
  "s_nop 3\n\t" \
  "s_cmp_eq_u32 s93, 1\n\t" \
  "s_cbranch_scc1 .Lok_%=\n\t" \
  "s_cmp_lt_u32 s92, 0x80000\n\t" \
  "s_cbranch_scc1 .Lpoll_%=\n\t" \
  "v_mov_b32 v244, 1\n\t" \
  "buffer_store_dword v244, v245, %[rs], %[sab] offen sc1\n\t" \
  "s_branch .Lok_%=\n\t" \
  ".Lend_%=:\n\t"
#define QK_ASM_3 \
  "s_waitcnt lgkmcnt(0)\n\t" \
  "v_subrev_u32 v244, 0x8000, v250\n\t" \
  "ds_read_b128 v[216:219], v248 offset:45056\n\t" \
  "ds_read_b128 v[220:223], v248 offset:46080\n\t" \
  "ds_read_b128 v[224:227], v248 offset:47104\n\t" \
  "ds_read_b128 v[228:231], v248 offset:48128\n\t" \
  "ds_read_b128 v[208:211], v244 offset:0\n\t" \
  "ds_read_b128 v[212:215], v244 offset:1024\n\t" \
  "ds_read_b128 v[236:239], v244 offset:2048\n\t" \
  "ds_read_b128 v[240:243], v244 offset:3072\n\t" \
  "v_mov_b64 v[192:193], 0\n\t" \
  "v_mov_b64 v[194:195], 0\n\t" \
  "v_mov_b64 v[196:197], 0\n\t" \
  "v_mov_b64 v[198:199], 0\n\t" \
  ".p2align 5\n\t" \
  "s_nop 0\n\t" \
  "s_nop 0\n\t" \
  "s_nop 0\n\t" \
  "s_nop 0\n\t" \
  "s_movk_i32 s92, 0x2000\n\t" \
  ".Lwt_%=:\n\t" \
  "ds_read_b64 v[200:201], v248 offset:30720\n\t" \
  "s_memrealtime s[90:91]\n\t" \
  "s_sub_u32 s92, s92, 1\n\t" \
  "s_waitcnt lgkmcnt(0)\n\t" \
  "v_readfirstlane_b32 s93, v200\n\t" \
  "v_readfirstlane_b32 %[tgt], v201\n\t" \
  "s_cbranch_scc1 .Lgo_%=\n\t" \
  "s_cmp_eq_u32 s93, %[step]\n\t" \
  "s_cbranch_scc1 .Lwx_%=\n\t" \
  "s_sleep 1\n\t" \
  "s_branch .Lwt_%=\n\t" \
  ".Lwx_%=:\n\t" \
  "s_lshl_b32 s93, s90, 2\n\t" \
  "s_sub_i32 s93, %[tgt], s93\n\t" \
  "s_cmp_le_i32 s93, 0\n\t" \
  "s_cbranch_scc1 .Lgo_%=\n\t" \
  "s_min_i32 s93, s93, 8000\n\t" \
  "s_mov_b32 s94, s93\n\t" \
  ".Lsl_%=:\n\t" \
  "s_sub_i32 s94, s94, 18\n\t" \
  "s_cmp_lt_i32 s94, 30\n\t" \
  "s_cbranch_scc1 .Lsx_%=\n\t" \
  "s_sleep 1\n\t" \
  "s_branch .Lsl_%=\n\t" \
  ".Lsx_%=:\n\t" \
  "s_sub_i32 s93, s93, 12\n\t" \
  "s_max_i32 s93, s93, 4\n\t" \
  "s_lshr_b32 s93, s93, 2\n\t" \
  "s_add_u32 s93, s93, s90\n\t" \
  "s_memrealtime s[90:91]\n\t" \
  ".Lpace_%=:\n\t" \
  "s_memrealtime s[94:95]\n\t" \
  "s_waitcnt lgkmcnt(1)\n\t" \
  "s_sub_i32 s92, s90, s93\n\t" \
  "s_cmp_ge_i32 s92, 0\n\t" \
  "s_cbranch_scc1 .Le_%=\n\t" \
  "s_memrealtime s[90:91]\n\t" \
  "s_waitcnt lgkmcnt(1)\n\t" \
  "s_sub_i32 s92, s94, s93\n\t" \
  "s_cmp_lt_i32 s92, 0\n\t" \
  "s_cbranch_scc1 .Lpace_%=\n\t" \
  "s_nop 4\n\t" \
  ".Le_%=:\n\t" \
  "s_add_u32 s92, s92, s93\n\t" \
  "s_lshl_b32 s92, s92, 2\n\t" \
  "s_sub_i32 s92, %[tgt], s92\n\t" \
  "s_max_i32 s92, s92, 0\n\t" \
  "s_bitcmp0_b32 s92, 3\n\t" \
  "s_cbranch_scc1 .Lf3_%=\n\t" \
  "s_nop 15\n\t" \
  ".Lf3_%=:\n\t" \
  "s_bitcmp0_b32 s92, 2\n\t" \
  "s_cbranch_scc1 .Lf2_%=\n\t" \
  "s_nop 10\n\t" \
  ".Lf2_%=:\n\t" \
  ".Lgo_%=:\n\t" \
  "s_mov_b32 s92, 0\n\t" \
  ".Lpoll_%=:\n\t" \
  "buffer_load_dwordx4 v[200:203], v248, %[rs], %[som] offen sc1\n\t" \
  "buffer_load_dwordx4 v[204:207], v248, %[rs], %[som2] offen sc1\n\t" \
  ".p2align 5\n\t" \
  "s_waitcnt vmcnt(1) lgkmcnt(0)\n\t" \
  "ds_write_b128 v249, v[200:203]\n\t" \
  "v_smfmac_f32_16x16x64_bf16 v[192:195], v[200:203], v[208:215], v251\n\t" \
  "v_max3_u32 v244, v200, v201, v202\n\t" \
  "v_smfmac_f32_16x16x64_bf16 v[196:199], v[200:203], v[236:243], v251\n\t" \
  "s_waitcnt vmcnt(0)\n\t" \
  "ds_write_b128 v249, v[204:207] offset:8192\n\t" \
  "v_smfmac_f32_16x16x64_bf16 v[192:195], v[204:207], v[216:223], v251\n\t" \
  "v_max3_u32 v244, v244, v203, v204\n\t" \
  "v_max3_u32 v244, v244, v205, v206\n\t" \
  "v_smfmac_f32_16x16x64_bf16 v[196:199], v[204:207], v[224:231], v251\n\t" \
  "v_max_u32 v244, v244, v207\n\t" \
  "v_cmp_le_u32_e64 vcc, %[sent], v244\n\t" \
  "s_cbranch_vccnz .Lslow_%=\n\t" \
  ".Lok_%=:\n\t" \
  "ds_read_b128 v[216:219], v250 offset:32768\n\t" \
  "ds_read_b128 v[220:223], v250 offset:33792\n\t" \
  "ds_read_b128 v[224:227], v250 offset:34816\n\t" \
  "ds_read_b128 v[228:231], v250 offset:35840\n\t" \
  "s_waitcnt lgkmcnt(4)\n\t" \
  "s_barrier\n\t" \
  "ds_read_b128 v[208:211], v248 offset:2048\n\t" \
  "ds_read_b128 v[212:215], v248 offset:5120\n\t" \
  "ds_read_b128 v[236:239], v248 offset:6144\n\t" \
  "ds_read_b128 v[240:243], v248 offset:7168\n\t" \
  "s_cmp_eq_u32 %[step], 63\n\t" \
  "s_cbranch_scc1 .Lls_%=\n\t" \
  "s_waitcnt lgkmcnt(3)\n\t" \
  "ds_read_b128 v[244:247], v248 offset:8192\n\t" \
  "v_smfmac_f32_16x16x64_bf16 v[192:195], v[208:211], v[216:223], v251\n\t" \
  "ds_read_b128 v[200:203], v248 offset:12288\n\t" \
  "v_smfmac_f32_16x16x64_bf16 v[196:199], v[208:211], v[224:231], v251\n\t" \
  "s_waitcnt lgkmcnt(4)\n\t" \
  "ds_read_b128 v[216:219], v250 offset:0\n\t" \
  "v_smfmac_f32_16x16x64_bf16 v[192:195], v[212:215], v[0:7], v251\n\t" \
  "ds_read_b128 v[220:223], v250 offset:1024\n\t" \
  "v_smfmac_f32_16x16x64_bf16 v[196:199], v[212:215], v[8:15], v251\n\t" \
  "s_waitcnt lgkmcnt(5)\n\t" \
  "ds_read_b128 v[224:227], v250 offset:2048\n\t" \
  "v_smfmac_f32_16x16x64_bf16 v[192:195], v[236:239], v[16:23], v251\n\t" \
  "ds_read_b128 v[228:231], v250 offset:3072\n\t" \
  "v_smfmac_f32_16x16x64_bf16 v[196:199], v[236:239], v[24:31], v251\n\t" \
  "s_waitcnt lgkmcnt(5)\n\t" \
  "ds_read_b128 v[204:207], v248 offset:9216\n\t" \
  "v_smfmac_f32_16x16x64_bf16 v[192:195], v[240:243], v[32:39], v251\n\t" \
  "ds_read_b128 v[208:211], v248 offset:10240\n\t" \
  "v_smfmac_f32_16x16x64_bf16 v[196:199], v[240:243], v[40:47], v251\n\t" \
  "ds_read_b128 v[212:215], v248 offset:13312\n\t" \
  "v_smfmac_f32_16x16x64_bf16 v[192:195], v[244:247], v[48:55], v251\n\t" \
  "ds_read_b128 v[236:239], v248 offset:14336\n\t" \
  "v_smfmac_f32_16x16x64_bf16 v[196:199], v[244:247], v[56:63], v251\n\t" \
  "s_waitcnt lgkmcnt(6)\n\t" \
  "v_smfmac_f32_16x16x64_bf16 v[192:195], v[200:203], v[216:223], v251\n\t" \
  "s_waitcnt lgkmcnt(4)\n\t" \
  "v_smfmac_f32_16x16x64_bf16 v[196:199], v[200:203], v[224:231], v251\n\t" \
  "s_waitcnt lgkmcnt(3)\n\t" \
  "ds_read_b128 v[240:243], v248 offset:15360\n\t" \
  "v_smfmac_f32_16x16x64_bf16 v[192:195], v[204:207], v[64:71], v251\n\t" \
  "ds_read_b128 v[244:247], v248 offset:0\n\t" \
  "v_smfmac_f32_16x16x64_bf16 v[196:199], v[204:207], v[72:79], v251\n\t" \
  "s_waitcnt lgkmcnt(3)\n\t" \
  "v_smfmac_f32_16x16x64_bf16 v[192:195], v[208:211], v[80:87], v251\n\t" \
  "ds_read_b128 v[200:203], v248 offset:1024\n\t" \
  "v_smfmac_f32_16x16x64_bf16 v[196:199], v[208:211], v[88:95], v251\n\t" \
  "ds_read_b128 v[204:207], v248 offset:4096\n\t" \
  "v_smfmac_f32_16x16x64_bf16 v[192:195], v[212:215], v[96:103], v251\n\t" \
  "v_smfmac_f32_16x16x64_bf16 v[196:199], v[212:215], v[104:111], v251\n\t" \
  "s_waitcnt lgkmcnt(3)\n\t" \
  "v_smfmac_f32_16x16x64_bf16 v[192:195], v[236:239], v[112:119], v251\n\t" \
  "v_smfmac_f32_16x16x64_bf16 v[196:199], v[236:239], v[120:127], v251\n\t" \
  "v_smfmac_f32_16x16x64_bf16 v[192:195], v[240:243], v[128:135], v251\n\t" \
  "v_smfmac_f32_16x16x64_bf16 v[196:199], v[240:243], v[136:143], v251\n\t" \
  "s_waitcnt lgkmcnt(0)\n\t" \
  "v_smfmac_f32_16x16x64_bf16 v[192:195], v[244:247], v[144:151], v251\n\t" \
  "v_smfmac_f32_16x16x64_bf16 v[196:199], v[244:247], v[152:159], v251\n\t" \
  "v_smfmac_f32_16x16x64_bf16 v[192:195], v[200:203], v[160:167], v251\n\t" \
  "v_smfmac_f32_16x16x64_bf16 v[196:199], v[200:203], v[168:175], v251\n\t" \
  "v_smfmac_f32_16x16x64_bf16 v[192:195], v[204:207], v[176:183], v251\n\t" \
  "v_smfmac_f32_16x16x64_bf16 v[196:199], v[204:207], v[184:191], v251\n\t" \
  "ds_read_b128 v[200:203], v248 offset:19456\n\t" \
  "ds_read_b128 v[204:207], v248 offset:23552\n\t" \
  "v_mov_b32 v250, v232\n\t" \
  "v_mov_b32 v249, v233\n\t" \
  "s_nop 2\n\t" \
  "v_add_f32 v192, v192, v194\n\t" \
  "v_add_f32 v193, v193, v195\n\t" \
  "v_add_f32 v196, v196, v198\n\t" \
  "v_add_f32_e64 v197, v197, v199\n\t" \
  "v_cvt_pk_bf16_f32 v192, v192, v193\n\t" \
  "v_cvt_pk_bf16_f32 v196, v196, v197\n\t" \
  "ds_write2_b32 v252, v192, v196 offset1:64\n\t" \
  "v_mov_b32_e64 v251, v234\n\t" \
  "ds_read_b64_tr_b16 v[244:245], v250 offset:1536\n\t" \
  "ds_read_b64_tr_b16 v[246:247], v250 offset:1600\n\t" \
  "v_mov_b32_e64 v212, %[step]\n\t" \
  "s_mov_b64 exec, 1\n\t" \
  "ds_write_b32 v248, v212 offset:28720\n\t" \
  "s_mov_b64 exec, -1\n\t" \
  "buffer_load_dwordx4 v[192:195], v248, %[rt], %[svt] offen offset:0 sc1\n\t" \
  "buffer_load_dwordx4 v[196:199], v248, %[rt], %[svt] offen offset:1024 sc1\n\t" \
  "s_waitcnt lgkmcnt(1)\n\t" \
  "v_mfma_f32_16x16x32_bf16 v[200:203], v[200:203], v[244:247], 0\n\t" \
  "v_mfma_f32_16x16x32_bf16 v[204:207], v[204:207], v[244:247], 0\n\t" \
  "s_nop 7\n\t" \
  "v_cndmask_b32 v216, v200, v204, %[nh]\n\t" \
  "v_cndmask_b32 v217, v201, v205, %[nh]\n\t" \
  "v_cndmask_b32 v218, v202, v206, %[nh]\n\t" \
  "v_cndmask_b32 v219, v203, v207, %[nh]\n\t" \
  "ds_write_b128 v248, v[216:219] offset:19456\n\t" \
  "buffer_load_dword v252, v249, %[rs], %[sv] offen offset:0 sc1\n\t" \
  "buffer_load_dword v253, v249, %[rs], %[sv] offen offset:256 sc1\n\t" \
  "ds_read_b32 v212, v248 offset:28672\n\t" \
  "ds_read_b64_tr_b16 v[232:233], v250 offset:0\n\t" \
  "ds_read_b64_tr_b16 v[234:235], v250 offset:64\n\t" \
  "ds_read_b64_tr_b16 v[236:237], v250 offset:512\n\t" \
  "ds_read_b64_tr_b16 v[238:239], v250 offset:576\n\t" \
  "ds_read_b64_tr_b16 v[240:241], v250 offset:1024\n\t" \
  "ds_read_b64_tr_b16 v[242:243], v250 offset:1088\n\t" \
  "s_setprio 3\n\t" \
  "s_waitcnt lgkmcnt(0)\n\t" \
  "v_cmp_ne_u32 vcc, %[step], v212\n\t" \
  "s_and_b32 s93, vcc_lo, 0xf\n\t" \
  "s_barrier\n\t" \
  "s_cbranch_scc1 .Lre_%=\n\t" \
  ".Lrb_%=:\n\t" \
  "ds_read_b64_tr_b16 v[212:213], v250 offset:3584\n\t" \
  "ds_read_b64_tr_b16 v[214:215], v250 offset:3648\n\t" \
  "ds_read_b64_tr_b16 v[200:201], v250 offset:2048\n\t" \
  "ds_read_b64_tr_b16 v[202:203], v250 offset:2112\n\t" \
  "ds_read_b64_tr_b16 v[204:205], v250 offset:2560\n\t" \
  "ds_read_b64_tr_b16 v[206:207], v250 offset:2624\n\t" \
  "ds_read_b64_tr_b16 v[208:209], v250 offset:3072\n\t" \
  "ds_read_b64_tr_b16 v[210:211], v250 offset:3136\n\t" \
  "ds_read_b128 v[220:223], v248 offset:16384\n\t" \
  "ds_read_b128 v[224:227], v248 offset:17408\n\t" \
  "ds_read_b128 v[228:231], v248 offset:18432\n\t" \
  "s_waitcnt lgkmcnt(9)\n\t" \
  "v_mfma_f32_16x16x32_bf16 v[212:215], v[212:215], v[244:247], 0\n\t" \
  "s_waitcnt lgkmcnt(7)\n\t" \
  "v_mfma_f32_16x16x32_bf16 v[212:215], v[200:203], v[232:235], v[212:215]\n\t" \
  "v_mov_b32 v245, 0\n\t" \
  "s_waitcnt lgkmcnt(5)\n\t" \
  "v_mfma_f32_16x16x32_bf16 v[212:215], v[204:207], v[236:239], v[212:215]\n\t" \
  "v_mov_b32 v246, 0\n\t" \
  "s_waitcnt lgkmcnt(3)\n\t" \
  "v_mfma_f32_16x16x32_bf16 v[212:215], v[208:211], v[240:243], v[212:215]\n\t" \
  "s_waitcnt lgkmcnt(2)\n\t" \
  "v_pk_add_f32 v[216:217], v[216:217], v[220:221]\n\t" \
  "v_pk_add_f32 v[218:219], v[218:219], v[222:223]\n\t" \
  "s_waitcnt lgkmcnt(0)\n\t" \
  "v_pk_add_f32 v[224:225], v[224:225], v[228:229]\n\t" \
  "v_pk_add_f32 v[226:227], v[226:227], v[230:231]\n\t" \
  "v_pk_add_f32 v[216:217], v[216:217], v[224:225]\n\t" \
  "v_pk_add_f32 v[218:219], v[218:219], v[226:227]\n\t" \
  "v_max3_f32 v224, v216, v217, v218\n\t" \
  "v_max_f32 v224, v224, v219\n\t" \
  "v_max3_f32 v231, v212, v213, v214\n\t" \
  "v_max_f32 v231, v231, v215\n\t" \
  "v_max_f32_dpp v225, v224, v224 row_ror:8 row_mask:0xf bank_mask:0xf\n\t" \
  "v_max_f32_dpp v226, v224, v224 row_ror:8 row_mask:0xf bank_mask:0xf\n\t" \
  "v_cndmask_b32 v212, v212, v214, %[up]\n\t" \
  "v_cndmask_b32 v213, v213, v215, %[up]\n\t" \
  "v_permlane32_swap_b32_e64 v225, v226\n\t" \
  "v_max3_f32 v201, v225, v226, v231\n\t" \
  "v_max3_f32 v202, v225, v226, v231\n\t" \
  "v_mov_b64 v[220:221], %[one]\n\t" \
  "v_mov_b64 v[222:223], %[one]\n\t" \
  "v_permlane16_swap_b32 v201, v202\n\t" \
  "v_max_f32 v200, v201, v202\n\t" \
  "v_mul_f32_e64 v200, %[negc], v200\n\t" \
  "v_fma_f32 v212, v212, %[c], v200\n\t" \
  "v_fma_f32 v213, v213, %[c], v200\n\t" \
  "v_exp_f32 v212, v212\n\t" \
  "v_fma_f32 v216, v216, %[c], v200\n\t" \
  "v_exp_f32 v213, v213\n\t" \
  "v_fma_f32 v217, v217, %[c], v200\n\t" \
  "v_exp_f32_e64 v216, v216\n\t" \
  "v_fma_f32 v218, v218, %[c], v200\n\t" \
  "v_cvt_pk_bf16_f32 v244, v212, v213\n\t" \
  "v_cvt_pk_bf16_f32 v247, v212, v213\n\t" \
  "v_exp_f32 v217, v217\n\t" \
  "v_fma_f32 v219, v219, %[c], v200\n\t" \
  "v_mfma_f32_16x16x16_bf16 v[208:211], v[220:221], v[244:245], 0\n\t" \
  "v_exp_f32 v218, v218\n\t" \
  "s_waitcnt vmcnt(0)\n\t" \
  "v_mfma_f32_16x16x16_bf16 v[224:227], v[252:253], v[244:245], 0\n\t" \
  "v_cvt_pk_bf16_f32 v206, v216, v217\n\t" \
  "v_cvt_pk_bf16_f32 v204, v216, v217\n\t" \
  "v_exp_f32 v219, v219\n\t" \
  "v_mfma_f32_16x16x16_bf16 v[228:231], v[252:253], v[246:247], 0\n\t" \
  "v_cvt_pk_bf16_f32 v207, v218, v219\n\t" \
  "v_cvt_pk_bf16_f32 v205, v218, v219\n\t" \
  "v_mov_b32_dpp v204, v206 row_ror:8 row_mask:0xf bank_mask:0xc\n\t" \
  "v_mov_b32_dpp v206, v206 row_ror:8 row_mask:0xf bank_mask:0x3\n\t" \
  "v_mov_b32_dpp v205, v207 row_ror:8 row_mask:0xf bank_mask:0xc\n\t" \
  "v_mov_b32_dpp v207, v207 row_ror:8 row_mask:0xf bank_mask:0x3\n\t" \
  "v_max_u32 v200, v252, v253\n\t" \
  "v_cmp_le_u32 vcc, %[sent], v200\n\t" \
  "v_mfma_f32_16x16x32_bf16 v[208:211], v[220:223], v[204:207], v[208:211]\n\t" \
  ".Lpv_%=:\n\t" \
  "v_mfma_f32_16x16x32_bf16 v[224:227], v[192:195], v[204:207], v[224:227]\n\t" \
  "v_mfma_f32_16x16x32_bf16 v[228:231], v[196:199], v[204:207], v[228:231]\n\t" \
  "s_cbranch_vccnz .Lvs_%=\n\t" \
  ".Lvok_%=:\n\t" \
  "s_nop 4\n\t" \
  "v_rcp_f32_e64 v200, v208\n\t" \
  "v_cndmask_b32 v224, v224, v226, %[nh]\n\t" \
  "v_cndmask_b32 v225, v225, v227, %[nh]\n\t" \
  "v_cndmask_b32 v228, v228, v230, %[nh]\n\t" \
  "v_cndmask_b32 v229, v229, v231, %[nh]\n\t" \
  "v_pk_mul_f32 v[224:225], v[224:225], v[200:201] op_sel_hi:[1,0]\n\t" \
  "v_pk_mul_f32 v[228:229], v[228:229], v[200:201] op_sel_hi:[1,0]\n\t" \
  "v_cvt_pk_bf16_f32 v224, v224, v225\n\t" \
  "v_cvt_pk_bf16_f32 v225, v228, v229\n\t" \
  "buffer_store_dwordx2 v[224:225], v251, %[rs], %[so] offen sc1\n\t" \
  "s_memtime s[90:91]\n\t" \
  "s_setprio 0\n\t" \
  "s_waitcnt lgkmcnt(0)\n\t" \
  "s_mov_b32 %[tout], s90\n\t" \
  "s_mov_b32 %[spins], s92\n\t" \
  "s_branch .Lend_%=\n\t" \
  ".Lls_%=:\n\t" \
  "s_waitcnt lgkmcnt(0)\n\t" \
  "s_barrier\n\t" \
  "s_mov_b32 %[spins], 0\n\t" \
  "s_branch .Lend_%=\n\t" \
  ".Lre_%=:\n\t" \
  "ds_read_b64_tr_b16 v[232:233], v250 offset:0\n\t" \
  "ds_read_b64_tr_b16 v[234:235], v250 offset:64\n\t" \
  "ds_read_b64_tr_b16 v[236:237], v250 offset:512\n\t" \
  "ds_read_b64_tr_b16 v[238:239], v250 offset:576\n\t" \
  "ds_read_b64_tr_b16 v[240:241], v250 offset:1024\n\t" \
  "ds_read_b64_tr_b16 v[242:243], v250 offset:1088\n\t" \
  "s_waitcnt lgkmcnt(0)\n\t" \
  "s_branch .Lrb_%=\n\t" \
  ".Lvs_%=:\n\t" \
  "v_cmp_eq_u32 vcc, %[sent], v252\n\t" \
  "v_cmp_eq_u32 s[94:95], %[sent], v253\n\t" \
  "s_or_b64 s[94:95], s[94:95], vcc\n\t" \
  "s_cbranch_scc0 .Lvok_%=\n\t" \
  "s_add_u32 s92, s92, 1\n\t" \
  "s_sleep 2\n\t" \
  "s_and_b32 s93, s92, 63\n\t" \
  "s_cbranch_scc1 .Lvr_%=\n\t" \
  "buffer_load_dword v200, off, %[rs], %[sab] sc1\n\t" \
  "s_waitcnt vmcnt(0)\n\t" \
  "v_readfirstlane_b32 s93, v200\n\t" \
  "s_nop 3\n\t" \
  "s_cmp_eq_u32 s93, 1\n\t" \
  "s_cbranch_scc1 .Lvok_%=\n\t" \
  "s_cmp_lt_u32 s92, 0x80000\n\t" \
  "s_cbranch_scc1 .Lvr_%=\n\t" \
  "v_mov_b32 v200, 1\n\t" \
  "buffer_store_dword v200, off, %[rs], %[sab] sc1\n\t" \
  "s_branch .Lvok_%=\n\t" \
  ".Lvr_%=:\n\t" \
  "buffer_load_dword v252, v249, %[rs], %[sv] offen offset:0 sc1\n\t" \
  "buffer_load_dword v253, v249, %[rs], %[sv] offen offset:256 sc1\n\t" \
  "s_waitcnt vmcnt(0)\n\t" \
  "v_mfma_f32_16x16x16_bf16 v[224:227], v[252:253], v[244:245], 0\n\t" \
  "v_mfma_f32_16x16x16_bf16 v[228:231], v[252:253], v[246:247], 0\n\t" \
  "v_max_u32 v200, v252, v253\n\t" \
  "v_cmp_le_u32 vcc, %[sent], v200\n\t" \
  "s_nop 4\n\t" \
  "s_branch .Lpv_%=\n\t" \
  "s_branch .Lend_%=\n\t" \
  ".Lslow_%=:\n\t" \
  "s_mov_b64 s[94:95], 0\n\t" \
  "v_cmp_eq_u32 vcc, %[sent], v200\n\t" \
  "s_or_b64 s[94:95], s[94:95], vcc\n\t" \
  "v_cmp_eq_u32 vcc, %[sent], v201\n\t" \
  "s_or_b64 s[94:95], s[94:95], vcc\n\t" \
  "v_cmp_eq_u32 vcc, %[sent], v202\n\t" \
  "s_or_b64 s[94:95], s[94:95], vcc\n\t" \
  "v_cmp_eq_u32 vcc, %[sent], v203\n\t" \
  "s_or_b64 s[94:95], s[94:95], vcc\n\t" \
  "v_cmp_eq_u32 vcc, %[sent], v204\n\t" \
  "s_or_b64 s[94:95], s[94:95], vcc\n\t" \
  "v_cmp_eq_u32 vcc, %[sent], v205\n\t" \
  "s_or_b64 s[94:95], s[94:95], vcc\n\t" \
  "v_cmp_eq_u32 vcc, %[sent], v206\n\t" \
  "s_or_b64 s[94:95], s[94:95], vcc\n\t" \
  "v_cmp_eq_u32 vcc, %[sent], v207\n\t" \
  "s_or_b64 s[94:95], s[94:95], vcc\n\t" \
  "s_cmp_eq_u64 s[94:95], 0\n\t" \
  "s_cbranch_scc1 .Lok_%=\n\t" \
  "v_mov_b64 v[192:193], 0\n\t" \
  "v_mov_b64 v[194:195], 0\n\t" \
  "v_mov_b64 v[196:197], 0\n\t" \
  "v_mov_b64 v[198:199], 0\n\t" \
  "s_add_u32 s92, s92, 1\n\t" \
  "s_cmp_eq_u32 s92, 1\n\t" \
  "s_cbranch_scc1 .Lpoll_%=\n\t" \
  "s_sleep 2\n\t" \
  "s_and_b32 s93, s92, 63\n\t" \
  "s_cmp_lg_u32 s93, 0\n\t" \
  "s_cbranch_scc1 .Lpoll_%=\n\t" \
  "v_mov_b32 v245, 0\n\t" \
  "buffer_load_dword v244, v245, %[rs], %[sab] offen sc1\n\t" \
  "s_waitcnt vmcnt(0)\n\t" \
  "v_readfirstlane_b32 s93, v244\n\t" \
  "s_nop 3\n\t" \
  "s_cmp_eq_u32 s93, 1\n\t" \
  "s_cbranch_scc1 .Lok_%=\n\t" \
  "s_cmp_lt_u32 s92, 0x80000\n\t" \
  "s_cbranch_scc1 .Lpoll_%=\n\t" \
  "v_mov_b32 v244, 1\n\t" \
  "buffer_store_dword v244, v245, %[rs], %[sab] offen sc1\n\t" \
  "s_branch .Lok_%=\n\t" \
  ".Lend_%=:\n\t"
#define QK_ASM_4 \
  "s_waitcnt lgkmcnt(0)\n\t" \
  "v_subrev_u32 v244, 0x8000, v250\n\t" \
  "ds_read_b128 v[216:219], v248 offset:49152\n\t" \
  "ds_read_b128 v[220:223], v248 offset:50176\n\t" \
  "ds_read_b128 v[224:227], v248 offset:51200\n\t" \
  "ds_read_b128 v[228:231], v248 offset:52224\n\t" \
  "ds_read_b128 v[208:211], v244 offset:0\n\t" \
  "ds_read_b128 v[212:215], v244 offset:1024\n\t" \
  "ds_read_b128 v[236:239], v244 offset:2048\n\t" \
  "ds_read_b128 v[240:243], v244 offset:3072\n\t" \
  "v_mov_b64 v[192:193], 0\n\t" \
  "v_mov_b64 v[194:195], 0\n\t" \
  "v_mov_b64 v[196:197], 0\n\t" \
  "v_mov_b64 v[198:199], 0\n\t" \
  ".p2align 5\n\t" \
  "s_nop 0\n\t" \
  "s_nop 0\n\t" \
  "s_nop 0\n\t" \
  "s_nop 0\n\t" \
  "s_movk_i32 s92, 0x2000\n\t" \
  ".Lwt_%=:\n\t" \
  "ds_read_b64 v[200:201], v248 offset:30720\n\t" \
  "s_memrealtime s[90:91]\n\t" \
  "s_sub_u32 s92, s92, 1\n\t" \
  "s_waitcnt lgkmcnt(0)\n\t" \
  "v_readfirstlane_b32 s93, v200\n\t" \
  "v_readfirstlane_b32 %[tgt], v201\n\t" \
  "s_cbranch_scc1 .Lgo_%=\n\t" \
  "s_cmp_eq_u32 s93, %[step]\n\t" \
  "s_cbranch_scc1 .Lwx_%=\n\t" \
  "s_sleep 1\n\t" \
  "s_branch .Lwt_%=\n\t" \
  ".Lwx_%=:\n\t" \
  "s_lshl_b32 s93, s90, 2\n\t" \
  "s_sub_i32 s93, %[tgt], s93\n\t" \
  "s_cmp_le_i32 s93, 0\n\t" \
  "s_cbranch_scc1 .Lgo_%=\n\t" \
  "s_min_i32 s93, s93, 8000\n\t" \
  "s_mov_b32 s94, s93\n\t" \
  ".Lsl_%=:\n\t" \
  "s_sub_i32 s94, s94, 18\n\t" \
  "s_cmp_lt_i32 s94, 30\n\t" \
  "s_cbranch_scc1 .Lsx_%=\n\t" \
  "s_sleep 1\n\t" \
  "s_branch .Lsl_%=\n\t" \
  ".Lsx_%=:\n\t" \
  "s_sub_i32 s93, s93, 12\n\t" \
  "s_max_i32 s93, s93, 4\n\t" \
  "s_lshr_b32 s93, s93, 2\n\t" \
  "s_add_u32 s93, s93, s90\n\t" \
  "s_memrealtime s[90:91]\n\t" \
  ".Lpace_%=:\n\t" \
  "s_memrealtime s[94:95]\n\t" \
  "s_waitcnt lgkmcnt(1)\n\t" \
  "s_sub_i32 s92, s90, s93\n\t" \
  "s_cmp_ge_i32 s92, 0\n\t" \
  "s_cbranch_scc1 .Le_%=\n\t" \
  "s_memrealtime s[90:91]\n\t" \
  "s_waitcnt lgkmcnt(1)\n\t" \
  "s_sub_i32 s92, s94, s93\n\t" \
  "s_cmp_lt_i32 s92, 0\n\t" \
  "s_cbranch_scc1 .Lpace_%=\n\t" \
  "s_nop 4\n\t" \
  ".Le_%=:\n\t" \
  "s_add_u32 s92, s92, s93\n\t" \
  "s_lshl_b32 s92, s92, 2\n\t" \
  "s_sub_i32 s92, %[tgt], s92\n\t" \
  "s_max_i32 s92, s92, 0\n\t" \
  "s_bitcmp0_b32 s92, 3\n\t" \
  "s_cbranch_scc1 .Lf3_%=\n\t" \
  "s_nop 15\n\t" \
  ".Lf3_%=:\n\t" \
  "s_bitcmp0_b32 s92, 2\n\t" \
  "s_cbranch_scc1 .Lf2_%=\n\t" \
  "s_nop 10\n\t" \
  ".Lf2_%=:\n\t" \
  ".Lgo_%=:\n\t" \
  "s_mov_b32 s92, 0\n\t" \
  ".Lpoll_%=:\n\t" \
  "buffer_load_dwordx4 v[200:203], v248, %[rs], %[som] offen sc1\n\t" \
  "buffer_load_dwordx4 v[204:207], v248, %[rs], %[som2] offen sc1\n\t" \
  ".p2align 5\n\t" \
  "s_nop 0\n\t" \
  "s_nop 0\n\t" \
  "s_nop 0\n\t" \
  "s_nop 0\n\t" \
  "s_nop 0\n\t" \
  "s_nop 0\n\t" \
  "s_nop 0\n\t" \
  "s_waitcnt vmcnt(1) lgkmcnt(0)\n\t" \
  "ds_write_b128 v249, v[200:203]\n\t" \
  "v_smfmac_f32_16x16x64_bf16 v[192:195], v[200:203], v[208:215], v251\n\t" \
  "v_max3_u32 v244, v200, v201, v202\n\t" \
  "v_smfmac_f32_16x16x64_bf16 v[196:199], v[200:203], v[236:243], v251\n\t" \
  "s_waitcnt vmcnt(0)\n\t" \
  "ds_write_b128 v249, v[204:207] offset:8192\n\t" \
  "v_smfmac_f32_16x16x64_bf16 v[192:195], v[204:207], v[216:223], v251\n\t" \
  "v_max3_u32 v244, v244, v203, v204\n\t" \
  "v_max3_u32 v244, v244, v205, v206\n\t" \
  "v_smfmac_f32_16x16x64_bf16 v[196:199], v[204:207], v[224:231], v251\n\t" \
  "v_max_u32 v244, v244, v207\n\t" \
  "v_cmp_le_u32 vcc, %[sent], v244\n\t" \
  "s_cbranch_vccnz .Lslow_%=\n\t" \
  ".Lok_%=:\n\t" \
  "s_waitcnt lgkmcnt(0)\n\t" \
  "s_barrier\n\t" \
  "ds_read_b128 v[208:211], v248 offset:6144\n\t" \
  "ds_read_b128 v[212:215], v248 offset:7168\n\t" \
  "ds_read_b128 v[236:239], v248 offset:8192\n\t" \
  "ds_read_b128 v[240:243], v248 offset:9216\n\t" \
  "ds_read_b128 v[244:247], v248 offset:10240\n\t" \
  "s_waitcnt lgkmcnt(4)\n\t" \
  "ds_read_b128 v[200:203], v248 offset:5120\n\t" \
  "v_smfmac_f32_16x16x64_bf16 v[192:195], v[208:211], v[0:7], v251\n\t" \
  "ds_read_b128 v[216:219], v250 offset:32768\n\t" \
  "v_smfmac_f32_16x16x64_bf16 v[196:199], v[208:211], v[8:15], v251\n\t" \
  "s_waitcnt lgkmcnt(5)\n\t" \
  "ds_read_b128 v[220:223], v250 offset:33792\n\t" \
  "v_smfmac_f32_16x16x64_bf16 v[192:195], v[212:215], v[16:23], v251\n\t" \
  "ds_read_b128 v[224:227], v250 offset:34816\n\t" \
  "v_smfmac_f32_16x16x64_bf16 v[196:199], v[212:215], v[24:31], v251\n\t" \
  "s_waitcnt lgkmcnt(6)\n\t" \
  "ds_read_b128 v[228:231], v250 offset:35840\n\t" \
  "v_smfmac_f32_16x16x64_bf16 v[192:195], v[236:239], v[32:39], v251\n\t" \
  "ds_read_b128 v[204:207], v248 offset:11264\n\t" \
  "v_smfmac_f32_16x16x64_bf16 v[196:199], v[236:239], v[40:47], v251\n\t" \
  "s_waitcnt lgkmcnt(7)\n\t" \
  "ds_read_b128 v[208:211], v248 offset:14336\n\t" \
  "v_smfmac_f32_16x16x64_bf16 v[192:195], v[240:243], v[48:55], v251\n\t" \
  "ds_read_b128 v[212:215], v248 offset:15360\n\t" \
  "v_smfmac_f32_16x16x64_bf16 v[196:199], v[240:243], v[56:63], v251\n\t" \
  "s_waitcnt lgkmcnt(7)\n\t" \
  "v_smfmac_f32_16x16x64_bf16 v[192:195], v[244:247], v[64:71], v251\n\t" \
  "ds_read_b128 v[236:239], v248 offset:0\n\t" \
  "v_smfmac_f32_16x16x64_bf16 v[196:199], v[244:247], v[72:79], v251\n\t" \
  "s_waitcnt lgkmcnt(6)\n\t" \
  "v_smfmac_f32_16x16x64_bf16 v[192:195], v[200:203], v[216:223], v251\n\t" \
  "s_waitcnt lgkmcnt(4)\n\t" \
  "ds_read_b128 v[240:243], v248 offset:13312\n\t" \
  "v_smfmac_f32_16x16x64_bf16 v[196:199], v[200:203], v[224:231], v251\n\t" \
  "s_waitcnt lgkmcnt(4)\n\t" \
  "ds_read_b128 v[216:219], v250 offset:0\n\t" \
  "v_smfmac_f32_16x16x64_bf16 v[192:195], v[204:207], v[80:87], v251\n\t" \
  "ds_read_b128 v[220:223], v250 offset:1024\n\t" \
  "v_smfmac_f32_16x16x64_bf16 v[196:199], v[204:207], v[88:95], v251\n\t" \
  "s_waitcnt lgkmcnt(4)\n\t" \
  "ds_read_b128 v[224:227], v250 offset:2048\n\t" \
  "v_smfmac_f32_16x16x64_bf16 v[192:195], v[208:211], v[96:103], v251\n\t" \
  "ds_read_b128 v[228:231], v250 offset:3072\n\t" \
  "v_smfmac_f32_16x16x64_bf16 v[196:199], v[208:211], v[104:111], v251\n\t" \
  "ds_read_b128 v[244:247], v248 offset:1024\n\t" \
  "v_smfmac_f32_16x16x64_bf16 v[192:195], v[212:215], v[112:119], v251\n\t" \
  "ds_read_b128 v[200:203], v248 offset:2048\n\t" \
  "v_smfmac_f32_16x16x64_bf16 v[196:199], v[212:215], v[120:127], v251\n\t" \
  "s_waitcnt lgkmcnt(6)\n\t" \
  "v_smfmac_f32_16x16x64_bf16 v[192:195], v[236:239], v[128:135], v251\n\t" \
  "ds_read_b128 v[204:207], v248 offset:3072\n\t" \
  "v_smfmac_f32_16x16x64_bf16 v[196:199], v[236:239], v[136:143], v251\n\t" \
  "s_waitcnt lgkmcnt(5)\n\t" \
  "v_smfmac_f32_16x16x64_bf16 v[192:195], v[240:243], v[216:223], v251\n\t" \
  "s_waitcnt lgkmcnt(3)\n\t" \
  "v_smfmac_f32_16x16x64_bf16 v[196:199], v[240:243], v[224:231], v251\n\t" \
  "s_waitcnt lgkmcnt(2)\n\t" \
  "v_smfmac_f32_16x16x64_bf16 v[192:195], v[244:247], v[144:151], v251\n\t" \
  "v_smfmac_f32_16x16x64_bf16 v[196:199], v[244:247], v[152:159], v251\n\t" \
  "s_waitcnt lgkmcnt(1)\n\t" \
  "v_smfmac_f32_16x16x64_bf16 v[192:195], v[200:203], v[160:167], v251\n\t" \
  "v_smfmac_f32_16x16x64_bf16 v[196:199], v[200:203], v[168:175], v251\n\t" \
  "s_waitcnt lgkmcnt(0)\n\t" \
  "v_smfmac_f32_16x16x64_bf16 v[192:195], v[204:207], v[176:183], v251\n\t" \
  "v_smfmac_f32_16x16x64_bf16 v[196:199], v[204:207], v[184:191], v251\n\t" \
  "s_nop 6\n\t" \
  "v_pk_add_f32 v[192:193], v[192:193], v[194:195]\n\t" \
  "v_pk_add_f32 v[196:197], v[196:197], v[198:199]\n\t" \
  "v_cvt_pk_bf16_f32 v192, v192, v193\n\t" \
  "v_cvt_pk_bf16_f32 v196, v196, v197\n\t" \
  "ds_write2_b32 v252, v192, v196 offset1:64\n\t" \
  "s_waitcnt lgkmcnt(0)\n\t" \
  "s_barrier\n\t" \
  "buffer_store_dword v192, v235, %[rs], %[sk] offen sc1\n\t" \
  "buffer_store_dword v196, v235, %[rs], %[sk] offen offset:256 sc1\n\t" \
  "s_mov_b32 %[spins], s92\n\t" \
  "s_branch .Lend_%=\n\t" \
  ".Lslow_%=:\n\t" \
  "s_mov_b64 s[94:95], 0\n\t" \
  "v_cmp_eq_u32 vcc, %[sent], v200\n\t" \
  "s_or_b64 s[94:95], s[94:95], vcc\n\t" \
  "v_cmp_eq_u32 vcc, %[sent], v201\n\t" \
  "s_or_b64 s[94:95], s[94:95], vcc\n\t" \
  "v_cmp_eq_u32 vcc, %[sent], v202\n\t" \
  "s_or_b64 s[94:95], s[94:95], vcc\n\t" \
  "v_cmp_eq_u32 vcc, %[sent], v203\n\t" \
  "s_or_b64 s[94:95], s[94:95], vcc\n\t" \
  "v_cmp_eq_u32 vcc, %[sent], v204\n\t" \
  "s_or_b64 s[94:95], s[94:95], vcc\n\t" \
  "v_cmp_eq_u32 vcc, %[sent], v205\n\t" \
  "s_or_b64 s[94:95], s[94:95], vcc\n\t" \
  "v_cmp_eq_u32 vcc, %[sent], v206\n\t" \
  "s_or_b64 s[94:95], s[94:95], vcc\n\t" \
  "v_cmp_eq_u32 vcc, %[sent], v207\n\t" \
  "s_or_b64 s[94:95], s[94:95], vcc\n\t" \
  "s_cmp_eq_u64 s[94:95], 0\n\t" \
  "s_cbranch_scc1 .Lok_%=\n\t" \
  "v_mov_b64 v[192:193], 0\n\t" \
  "v_mov_b64 v[194:195], 0\n\t" \
  "v_mov_b64 v[196:197], 0\n\t" \
  "v_mov_b64 v[198:199], 0\n\t" \
  "s_add_u32 s92, s92, 1\n\t" \
  "s_cmp_eq_u32 s92, 1\n\t" \
  "s_cbranch_scc1 .Lpoll_%=\n\t" \
  "s_sleep 2\n\t" \
  "s_and_b32 s93, s92, 63\n\t" \
  "s_cmp_lg_u32 s93, 0\n\t" \
  "s_cbranch_scc1 .Lpoll_%=\n\t" \
  "v_mov_b32 v245, 0\n\t" \
  "buffer_load_dword v244, v245, %[rs], %[sab] offen sc1\n\t" \
  "s_waitcnt vmcnt(0)\n\t" \
  "v_readfirstlane_b32 s93, v244\n\t" \
  "s_nop 3\n\t" \
  "s_cmp_eq_u32 s93, 1\n\t" \
  "s_cbranch_scc1 .Lok_%=\n\t" \
  "s_cmp_lt_u32 s92, 0x80000\n\t" \
  "s_cbranch_scc1 .Lpoll_%=\n\t" \
  "v_mov_b32 v244, 1\n\t" \
  "buffer_store_dword v244, v245, %[rs], %[sab] offen sc1\n\t" \
  "s_branch .Lok_%=\n\t" \
  ".Lend_%=:\n\t"
#define QK_ASM_5 \
  "s_waitcnt lgkmcnt(0)\n\t" \
  "v_subrev_u32 v244, 0x8000, v250\n\t" \
  "ds_read_b128 v[216:219], v248 offset:53248\n\t" \
  "ds_read_b128 v[220:223], v248 offset:54272\n\t" \
  "ds_read_b128 v[224:227], v248 offset:55296\n\t" \
  "ds_read_b128 v[228:231], v248 offset:56320\n\t" \
  "ds_read_b128 v[208:211], v244 offset:0\n\t" \
  "ds_read_b128 v[212:215], v244 offset:1024\n\t" \
  "ds_read_b128 v[236:239], v244 offset:2048\n\t" \
  "ds_read_b128 v[240:243], v244 offset:3072\n\t" \
  "v_mov_b64 v[192:193], 0\n\t" \
  "v_mov_b64 v[194:195], 0\n\t" \
  "v_mov_b64 v[196:197], 0\n\t" \
  "v_mov_b64 v[198:199], 0\n\t" \
  ".p2align 5\n\t" \
  "s_nop 0\n\t" \
  "s_nop 0\n\t" \
  "s_nop 0\n\t" \
  "s_nop 0\n\t" \
  "s_movk_i32 s92, 0x2000\n\t" \
  ".Lwt_%=:\n\t" \
  "ds_read_b64 v[200:201], v248 offset:30720\n\t" \
  "s_memrealtime s[90:91]\n\t" \
  "s_sub_u32 s92, s92, 1\n\t" \
  "s_waitcnt lgkmcnt(0)\n\t" \
  "v_readfirstlane_b32 s93, v200\n\t" \
  "v_readfirstlane_b32 %[tgt], v201\n\t" \
  "s_cbranch_scc1 .Lgo_%=\n\t" \
  "s_cmp_eq_u32 s93, %[step]\n\t" \
  "s_cbranch_scc1 .Lwx_%=\n\t" \
  "s_sleep 1\n\t" \
  "s_branch .Lwt_%=\n\t" \
  ".Lwx_%=:\n\t" \
  "s_lshl_b32 s93, s90, 2\n\t" \
  "s_sub_i32 s93, %[tgt], s93\n\t" \
  "s_cmp_le_i32 s93, 0\n\t" \
  "s_cbranch_scc1 .Lgo_%=\n\t" \
  "s_min_i32 s93, s93, 8000\n\t" \
  "s_mov_b32 s94, s93\n\t" \
  ".Lsl_%=:\n\t" \
  "s_sub_i32 s94, s94, 18\n\t" \
  "s_cmp_lt_i32 s94, 30\n\t" \
  "s_cbranch_scc1 .Lsx_%=\n\t" \
  "s_sleep 1\n\t" \
  "s_branch .Lsl_%=\n\t" \
  ".Lsx_%=:\n\t" \
  "s_sub_i32 s93, s93, 12\n\t" \
  "s_max_i32 s93, s93, 4\n\t" \
  "s_lshr_b32 s93, s93, 2\n\t" \
  "s_add_u32 s93, s93, s90\n\t" \
  "s_memrealtime s[90:91]\n\t" \
  ".Lpace_%=:\n\t" \
  "s_memrealtime s[94:95]\n\t" \
  "s_waitcnt lgkmcnt(1)\n\t" \
  "s_sub_i32 s92, s90, s93\n\t" \
  "s_cmp_ge_i32 s92, 0\n\t" \
  "s_cbranch_scc1 .Le_%=\n\t" \
  "s_memrealtime s[90:91]\n\t" \
  "s_waitcnt lgkmcnt(1)\n\t" \
  "s_sub_i32 s92, s94, s93\n\t" \
  "s_cmp_lt_i32 s92, 0\n\t" \
  "s_cbranch_scc1 .Lpace_%=\n\t" \
  "s_nop 4\n\t" \
  ".Le_%=:\n\t" \
  "s_add_u32 s92, s92, s93\n\t" \
  "s_lshl_b32 s92, s92, 2\n\t" \
  "s_sub_i32 s92, %[tgt], s92\n\t" \
  "s_max_i32 s92, s92, 0\n\t" \
  "s_bitcmp0_b32 s92, 3\n\t" \
  "s_cbranch_scc1 .Lf3_%=\n\t" \
  "s_nop 15\n\t" \
  ".Lf3_%=:\n\t" \
  "s_bitcmp0_b32 s92, 2\n\t" \
  "s_cbranch_scc1 .Lf2_%=\n\t" \
  "s_nop 10\n\t" \
  ".Lf2_%=:\n\t" \
  ".Lgo_%=:\n\t" \
  "s_mov_b32 s92, 0\n\t" \
  ".Lpoll_%=:\n\t" \
  "buffer_load_dwordx4 v[200:203], v248, %[rs], %[som] offen sc1\n\t" \
  "buffer_load_dwordx4 v[204:207], v248, %[rs], %[som2] offen sc1\n\t" \
  ".p2align 5\n\t" \
  "s_nop 0\n\t" \
  "s_nop 0\n\t" \
  "s_nop 0\n\t" \
  "s_nop 0\n\t" \
  "s_nop 0\n\t" \
  "s_nop 0\n\t" \
  "s_nop 0\n\t" \
  "s_waitcnt vmcnt(1) lgkmcnt(0)\n\t" \
  "ds_write_b128 v249, v[200:203]\n\t" \
  "v_smfmac_f32_16x16x64_bf16 v[192:195], v[200:203], v[208:215], v251\n\t" \
  "v_max3_u32 v244, v200, v201, v202\n\t" \
  "v_smfmac_f32_16x16x64_bf16 v[196:199], v[200:203], v[236:243], v251\n\t" \
  "s_waitcnt vmcnt(0)\n\t" \
  "ds_write_b128 v249, v[204:207] offset:8192\n\t" \
  "v_smfmac_f32_16x16x64_bf16 v[192:195], v[204:207], v[216:223], v251\n\t" \
  "v_max3_u32 v244, v244, v203, v204\n\t" \
  "v_max3_u32 v244, v244, v205, v206\n\t" \
  "v_smfmac_f32_16x16x64_bf16 v[196:199], v[204:207], v[224:231], v251\n\t" \
  "v_max_u32 v244, v244, v207\n\t" \
  "v_cmp_le_u32 vcc, %[sent], v244\n\t" \
  "s_cbranch_vccnz .Lslow_%=\n\t" \
  ".Lok_%=:\n\t" \
  "s_waitcnt lgkmcnt(0)\n\t" \
  "s_barrier\n\t" \
  "ds_read_b128 v[208:211], v248 offset:7168\n\t" \
  "ds_read_b128 v[212:215], v248 offset:8192\n\t" \
  "ds_read_b128 v[236:239], v248 offset:9216\n\t" \
  "ds_read_b128 v[240:243], v248 offset:10240\n\t" \
  "ds_read_b128 v[244:247], v248 offset:11264\n\t" \
  "s_waitcnt lgkmcnt(4)\n\t" \
  "ds_read_b128 v[200:203], v248 offset:4096\n\t" \
  "v_smfmac_f32_16x16x64_bf16 v[192:195], v[208:211], v[0:7], v251\n\t" \
  "ds_read_b128 v[216:219], v250 offset:32768\n\t" \
  "v_smfmac_f32_16x16x64_bf16 v[196:199], v[208:211], v[8:15], v251\n\t" \
  "s_waitcnt lgkmcnt(5)\n\t" \
  "ds_read_b128 v[220:223], v250 offset:33792\n\t" \
  "v_smfmac_f32_16x16x64_bf16 v[192:195], v[212:215], v[16:23], v251\n\t" \
  "ds_read_b128 v[224:227], v250 offset:34816\n\t" \
  "v_smfmac_f32_16x16x64_bf16 v[196:199], v[212:215], v[24:31], v251\n\t" \
  "s_waitcnt lgkmcnt(6)\n\t" \
  "ds_read_b128 v[228:231], v250 offset:35840\n\t" \
  "v_smfmac_f32_16x16x64_bf16 v[192:195], v[236:239], v[32:39], v251\n\t" \
  "ds_read_b128 v[204:207], v248 offset:14336\n\t" \
  "v_smfmac_f32_16x16x64_bf16 v[196:199], v[236:239], v[40:47], v251\n\t" \
  "s_waitcnt lgkmcnt(7)\n\t" \
  "ds_read_b128 v[208:211], v248 offset:15360\n\t" \
  "v_smfmac_f32_16x16x64_bf16 v[192:195], v[240:243], v[48:55], v251\n\t" \
  "ds_read_b128 v[212:215], v248 offset:0\n\t" \
  "v_smfmac_f32_16x16x64_bf16 v[196:199], v[240:243], v[56:63], v251\n\t" \
  "s_waitcnt lgkmcnt(7)\n\t" \
  "v_smfmac_f32_16x16x64_bf16 v[192:195], v[244:247], v[64:71], v251\n\t" \
  "ds_read_b128 v[236:239], v248 offset:1024\n\t" \
  "v_smfmac_f32_16x16x64_bf16 v[196:199], v[244:247], v[72:79], v251\n\t" \
  "s_waitcnt lgkmcnt(6)\n\t" \
  "v_smfmac_f32_16x16x64_bf16 v[192:195], v[200:203], v[216:223], v251\n\t" \
  "s_waitcnt lgkmcnt(4)\n\t" \
  "ds_read_b128 v[240:243], v248 offset:12288\n\t" \
  "v_smfmac_f32_16x16x64_bf16 v[196:199], v[200:203], v[224:231], v251\n\t" \
  "s_waitcnt lgkmcnt(4)\n\t" \
  "ds_read_b128 v[216:219], v250 offset:0\n\t" \
  "v_smfmac_f32_16x16x64_bf16 v[192:195], v[204:207], v[80:87], v251\n\t" \
  "ds_read_b128 v[220:223], v250 offset:1024\n\t" \
  "v_smfmac_f32_16x16x64_bf16 v[196:199], v[204:207], v[88:95], v251\n\t" \
  "s_waitcnt lgkmcnt(4)\n\t" \
  "ds_read_b128 v[224:227], v250 offset:2048\n\t" \
  "v_smfmac_f32_16x16x64_bf16 v[192:195], v[208:211], v[96:103], v251\n\t" \
  "ds_read_b128 v[228:231], v250 offset:3072\n\t" \
  "v_smfmac_f32_16x16x64_bf16 v[196:199], v[208:211], v[104:111], v251\n\t" \
  "ds_read_b128 v[244:247], v248 offset:2048\n\t" \
  "v_smfmac_f32_16x16x64_bf16 v[192:195], v[212:215], v[112:119], v251\n\t" \
  "ds_read_b128 v[200:203], v248 offset:3072\n\t" \
  "v_smfmac_f32_16x16x64_bf16 v[196:199], v[212:215], v[120:127], v251\n\t" \
  "s_waitcnt lgkmcnt(6)\n\t" \
  "v_smfmac_f32_16x16x64_bf16 v[192:195], v[236:239], v[128:135], v251\n\t" \
  "ds_read_b128 v[204:207], v248 offset:6144\n\t" \
  "v_smfmac_f32_16x16x64_bf16 v[196:199], v[236:239], v[136:143], v251\n\t" \
  "s_waitcnt lgkmcnt(5)\n\t" \
  "v_smfmac_f32_16x16x64_bf16 v[192:195], v[240:243], v[216:223], v251\n\t" \
  "s_waitcnt lgkmcnt(3)\n\t" \
  "v_smfmac_f32_16x16x64_bf16 v[196:199], v[240:243], v[224:231], v251\n\t" \
  "s_waitcnt lgkmcnt(2)\n\t" \
  "v_smfmac_f32_16x16x64_bf16 v[192:195], v[244:247], v[144:151], v251\n\t" \
  "v_smfmac_f32_16x16x64_bf16 v[196:199], v[244:247], v[152:159], v251\n\t" \
  "s_waitcnt lgkmcnt(1)\n\t" \
  "v_smfmac_f32_16x16x64_bf16 v[192:195], v[200:203], v[160:167], v251\n\t" \
  "v_smfmac_f32_16x16x64_bf16 v[196:199], v[200:203], v[168:175], v251\n\t" \
  "s_waitcnt lgkmcnt(0)\n\t" \
  "v_smfmac_f32_16x16x64_bf16 v[192:195], v[204:207], v[176:183], v251\n\t" \
  "v_smfmac_f32_16x16x64_bf16 v[196:199], v[204:207], v[184:191], v251\n\t" \
  "s_nop 6\n\t" \
  "v_pk_add_f32 v[192:193], v[192:193], v[194:195]\n\t" \
  "v_pk_add_f32 v[196:197], v[196:197], v[198:199]\n\t" \
  "v_cvt_pk_bf16_f32 v192, v192, v193\n\t" \
  "v_cvt_pk_bf16_f32 v196, v196, v197\n\t" \
  "ds_write2_b32 v252, v192, v196 offset1:64\n\t" \
  "s_waitcnt lgkmcnt(0)\n\t" \
  "s_barrier\n\t" \
  "buffer_store_dword v192, v235, %[rs], %[sk] offen sc1\n\t" \
  "buffer_store_dword v196, v235, %[rs], %[sk] offen offset:256 sc1\n\t" \
  "s_mov_b32 %[spins], s92\n\t" \
  "s_branch .Lend_%=\n\t" \
  ".Lslow_%=:\n\t" \
  "s_mov_b64 s[94:95], 0\n\t" \
  "v_cmp_eq_u32 vcc, %[sent], v200\n\t" \
  "s_or_b64 s[94:95], s[94:95], vcc\n\t" \
  "v_cmp_eq_u32 vcc, %[sent], v201\n\t" \
  "s_or_b64 s[94:95], s[94:95], vcc\n\t" \
  "v_cmp_eq_u32 vcc, %[sent], v202\n\t" \
  "s_or_b64 s[94:95], s[94:95], vcc\n\t" \
  "v_cmp_eq_u32 vcc, %[sent], v203\n\t" \
  "s_or_b64 s[94:95], s[94:95], vcc\n\t" \
  "v_cmp_eq_u32 vcc, %[sent], v204\n\t" \
  "s_or_b64 s[94:95], s[94:95], vcc\n\t" \
  "v_cmp_eq_u32 vcc, %[sent], v205\n\t" \
  "s_or_b64 s[94:95], s[94:95], vcc\n\t" \
  "v_cmp_eq_u32 vcc, %[sent], v206\n\t" \
  "s_or_b64 s[94:95], s[94:95], vcc\n\t" \
  "v_cmp_eq_u32 vcc, %[sent], v207\n\t" \
  "s_or_b64 s[94:95], s[94:95], vcc\n\t" \
  "s_cmp_eq_u64 s[94:95], 0\n\t" \
  "s_cbranch_scc1 .Lok_%=\n\t" \
  "v_mov_b64 v[192:193], 0\n\t" \
  "v_mov_b64 v[194:195], 0\n\t" \
  "v_mov_b64 v[196:197], 0\n\t" \
  "v_mov_b64 v[198:199], 0\n\t" \
  "s_add_u32 s92, s92, 1\n\t" \
  "s_cmp_eq_u32 s92, 1\n\t" \
  "s_cbranch_scc1 .Lpoll_%=\n\t" \
  "s_sleep 2\n\t" \
  "s_and_b32 s93, s92, 63\n\t" \
  "s_cmp_lg_u32 s93, 0\n\t" \
  "s_cbranch_scc1 .Lpoll_%=\n\t" \
  "v_mov_b32 v245, 0\n\t" \
  "buffer_load_dword v244, v245, %[rs], %[sab] offen sc1\n\t" \
  "s_waitcnt vmcnt(0)\n\t" \
  "v_readfirstlane_b32 s93, v244\n\t" \
  "s_nop 3\n\t" \
  "s_cmp_eq_u32 s93, 1\n\t" \
  "s_cbranch_scc1 .Lok_%=\n\t" \
  "s_cmp_lt_u32 s92, 0x80000\n\t" \
  "s_cbranch_scc1 .Lpoll_%=\n\t" \
  "v_mov_b32 v244, 1\n\t" \
  "buffer_store_dword v244, v245, %[rs], %[sab] offen sc1\n\t" \
  "s_branch .Lok_%=\n\t" \
  ".Lend_%=:\n\t"
#define QK_ASM_6 \
  "s_waitcnt lgkmcnt(0)\n\t" \
  "v_subrev_u32 v244, 0x8000, v250\n\t" \
  "ds_read_b128 v[216:219], v248 offset:57344\n\t" \
  "ds_read_b128 v[220:223], v248 offset:58368\n\t" \
  "ds_read_b128 v[224:227], v248 offset:59392\n\t" \
  "ds_read_b128 v[228:231], v248 offset:60416\n\t" \
  "ds_read_b128 v[208:211], v244 offset:0\n\t" \
  "ds_read_b128 v[212:215], v244 offset:1024\n\t" \
  "ds_read_b128 v[236:239], v244 offset:2048\n\t" \
  "ds_read_b128 v[240:243], v244 offset:3072\n\t" \
  "v_mov_b64 v[192:193], 0\n\t" \
  "v_mov_b64 v[194:195], 0\n\t" \
  "v_mov_b64 v[196:197], 0\n\t" \
  "v_mov_b64 v[198:199], 0\n\t" \
  ".p2align 5\n\t" \
  "s_nop 0\n\t" \
  "s_nop 0\n\t" \
  "s_nop 0\n\t" \
  "s_nop 0\n\t" \
  "s_movk_i32 s92, 0x2000\n\t" \
  ".Lwt_%=:\n\t" \
  "ds_read_b64 v[200:201], v248 offset:30720\n\t" \
  "s_memrealtime s[90:91]\n\t" \
  "s_sub_u32 s92, s92, 1\n\t" \
  "s_waitcnt lgkmcnt(0)\n\t" \
  "v_readfirstlane_b32 s93, v200\n\t" \
  "v_readfirstlane_b32 %[tgt], v201\n\t" \
  "s_cbranch_scc1 .Lgo_%=\n\t" \
  "s_cmp_eq_u32 s93, %[step]\n\t" \
  "s_cbranch_scc1 .Lwx_%=\n\t" \
  "s_sleep 1\n\t" \
  "s_branch .Lwt_%=\n\t" \
  ".Lwx_%=:\n\t" \
  "s_lshl_b32 s93, s90, 2\n\t" \
  "s_sub_i32 s93, %[tgt], s93\n\t" \
  "s_cmp_le_i32 s93, 0\n\t" \
  "s_cbranch_scc1 .Lgo_%=\n\t" \
  "s_min_i32 s93, s93, 8000\n\t" \
  "s_mov_b32 s94, s93\n\t" \
  ".Lsl_%=:\n\t" \
  "s_sub_i32 s94, s94, 18\n\t" \
  "s_cmp_lt_i32 s94, 30\n\t" \
  "s_cbranch_scc1 .Lsx_%=\n\t" \
  "s_sleep 1\n\t" \
  "s_branch .Lsl_%=\n\t" \
  ".Lsx_%=:\n\t" \
  "s_sub_i32 s93, s93, 12\n\t" \
  "s_max_i32 s93, s93, 4\n\t" \
  "s_lshr_b32 s93, s93, 2\n\t" \
  "s_add_u32 s93, s93, s90\n\t" \
  "s_memrealtime s[90:91]\n\t" \
  ".Lpace_%=:\n\t" \
  "s_memrealtime s[94:95]\n\t" \
  "s_waitcnt lgkmcnt(1)\n\t" \
  "s_sub_i32 s92, s90, s93\n\t" \
  "s_cmp_ge_i32 s92, 0\n\t" \
  "s_cbranch_scc1 .Le_%=\n\t" \
  "s_memrealtime s[90:91]\n\t" \
  "s_waitcnt lgkmcnt(1)\n\t" \
  "s_sub_i32 s92, s94, s93\n\t" \
  "s_cmp_lt_i32 s92, 0\n\t" \
  "s_cbranch_scc1 .Lpace_%=\n\t" \
  "s_nop 4\n\t" \
  ".Le_%=:\n\t" \
  "s_add_u32 s92, s92, s93\n\t" \
  "s_lshl_b32 s92, s92, 2\n\t" \
  "s_sub_i32 s92, %[tgt], s92\n\t" \
  "s_max_i32 s92, s92, 0\n\t" \
  "s_bitcmp0_b32 s92, 3\n\t" \
  "s_cbranch_scc1 .Lf3_%=\n\t" \
  "s_nop 15\n\t" \
  ".Lf3_%=:\n\t" \
  "s_bitcmp0_b32 s92, 2\n\t" \
  "s_cbranch_scc1 .Lf2_%=\n\t" \
  "s_nop 10\n\t" \
  ".Lf2_%=:\n\t" \
  ".Lgo_%=:\n\t" \
  "s_mov_b32 s92, 0\n\t" \
  ".Lpoll_%=:\n\t" \
  "buffer_load_dwordx4 v[200:203], v248, %[rs], %[som] offen sc1\n\t" \
  "buffer_load_dwordx4 v[204:207], v248, %[rs], %[som2] offen sc1\n\t" \
  ".p2align 5\n\t" \
  "s_nop 0\n\t" \
  "s_nop 0\n\t" \
  "s_nop 0\n\t" \
  "s_nop 0\n\t" \
  "s_nop 0\n\t" \
  "s_nop 0\n\t" \
  "s_nop 0\n\t" \
  "s_waitcnt vmcnt(1) lgkmcnt(0)\n\t" \
  "ds_write_b128 v249, v[200:203]\n\t" \
  "v_smfmac_f32_16x16x64_bf16 v[192:195], v[200:203], v[208:215], v251\n\t" \
  "v_max3_u32 v244, v200, v201, v202\n\t" \
  "v_smfmac_f32_16x16x64_bf16 v[196:199], v[200:203], v[236:243], v251\n\t" \
  "s_waitcnt vmcnt(0)\n\t" \
  "ds_write_b128 v249, v[204:207] offset:8192\n\t" \
  "v_smfmac_f32_16x16x64_bf16 v[192:195], v[204:207], v[216:223], v251\n\t" \
  "v_max3_u32 v244, v244, v203, v204\n\t" \
  "v_max3_u32 v244, v244, v205, v206\n\t" \
  "v_smfmac_f32_16x16x64_bf16 v[196:199], v[204:207], v[224:231], v251\n\t" \
  "v_max_u32 v244, v244, v207\n\t" \
  "v_cmp_le_u32 vcc, %[sent], v244\n\t" \
  "s_cbranch_vccnz .Lslow_%=\n\t" \
  ".Lok_%=:\n\t" \
  "s_waitcnt lgkmcnt(0)\n\t" \
  "s_barrier\n\t" \
  "ds_read_b128 v[208:211], v248 offset:8192\n\t" \
  "ds_read_b128 v[212:215], v248 offset:9216\n\t" \
  "ds_read_b128 v[236:239], v248 offset:10240\n\t" \
  "ds_read_b128 v[240:243], v248 offset:11264\n\t" \
  "ds_read_b128 v[244:247], v248 offset:12288\n\t" \
  "s_waitcnt lgkmcnt(4)\n\t" \
  "ds_read_b128 v[200:203], v248 offset:7168\n\t" \
  "v_smfmac_f32_16x16x64_bf16 v[192:195], v[208:211], v[0:7], v251\n\t" \
  "ds_read_b128 v[216:219], v250 offset:32768\n\t" \
  "v_smfmac_f32_16x16x64_bf16 v[196:199], v[208:211], v[8:15], v251\n\t" \
  "s_waitcnt lgkmcnt(5)\n\t" \
  "ds_read_b128 v[220:223], v250 offset:33792\n\t" \
  "v_smfmac_f32_16x16x64_bf16 v[192:195], v[212:215], v[16:23], v251\n\t" \
  "ds_read_b128 v[224:227], v250 offset:34816\n\t" \
  "v_smfmac_f32_16x16x64_bf16 v[196:199], v[212:215], v[24:31], v251\n\t" \
  "s_waitcnt lgkmcnt(6)\n\t" \
  "ds_read_b128 v[228:231], v250 offset:35840\n\t" \
  "v_smfmac_f32_16x16x64_bf16 v[192:195], v[236:239], v[32:39], v251\n\t" \
  "ds_read_b128 v[204:207], v248 offset:15360\n\t" \
  "v_smfmac_f32_16x16x64_bf16 v[196:199], v[236:239], v[40:47], v251\n\t" \
  "s_waitcnt lgkmcnt(7)\n\t" \
  "ds_read_b128 v[208:211], v248 offset:0\n\t" \
  "v_smfmac_f32_16x16x64_bf16 v[192:195], v[240:243], v[48:55], v251\n\t" \
  "ds_read_b128 v[212:215], v248 offset:1024\n\t" \
  "v_smfmac_f32_16x16x64_bf16 v[196:199], v[240:243], v[56:63], v251\n\t" \
  "s_waitcnt lgkmcnt(7)\n\t" \
  "v_smfmac_f32_16x16x64_bf16 v[192:195], v[244:247], v[64:71], v251\n\t" \
  "ds_read_b128 v[236:239], v248 offset:2048\n\t" \
  "v_smfmac_f32_16x16x64_bf16 v[196:199], v[244:247], v[72:79], v251\n\t" \
  "s_waitcnt lgkmcnt(6)\n\t" \
  "v_smfmac_f32_16x16x64_bf16 v[192:195], v[200:203], v[216:223], v251\n\t" \
  "s_waitcnt lgkmcnt(4)\n\t" \
  "ds_read_b128 v[240:243], v248 offset:13312\n\t" \
  "v_smfmac_f32_16x16x64_bf16 v[196:199], v[200:203], v[224:231], v251\n\t" \
  "s_waitcnt lgkmcnt(4)\n\t" \
  "ds_read_b128 v[216:219], v250 offset:0\n\t" \
  "v_smfmac_f32_16x16x64_bf16 v[192:195], v[204:207], v[80:87], v251\n\t" \
  "ds_read_b128 v[220:223], v250 offset:1024\n\t" \
  "v_smfmac_f32_16x16x64_bf16 v[196:199], v[204:207], v[88:95], v251\n\t" \
  "s_waitcnt lgkmcnt(4)\n\t" \
  "ds_read_b128 v[224:227], v250 offset:2048\n\t" \
  "v_smfmac_f32_16x16x64_bf16 v[192:195], v[208:211], v[96:103], v251\n\t" \
  "ds_read_b128 v[228:231], v250 offset:3072\n\t" \
  "v_smfmac_f32_16x16x64_bf16 v[196:199], v[208:211], v[104:111], v251\n\t" \
  "ds_read_b128 v[244:247], v248 offset:3072\n\t" \
  "v_smfmac_f32_16x16x64_bf16 v[192:195], v[212:215], v[112:119], v251\n\t" \
  "ds_read_b128 v[200:203], v248 offset:4096\n\t" \
  "v_smfmac_f32_16x16x64_bf16 v[196:199], v[212:215], v[120:127], v251\n\t" \
  "s_waitcnt lgkmcnt(6)\n\t" \
  "v_smfmac_f32_16x16x64_bf16 v[192:195], v[236:239], v[128:135], v251\n\t" \
  "ds_read_b128 v[204:207], v248 offset:5120\n\t" \
  "v_smfmac_f32_16x16x64_bf16 v[196:199], v[236:239], v[136:143], v251\n\t" \
  "s_waitcnt lgkmcnt(5)\n\t" \
  "v_smfmac_f32_16x16x64_bf16 v[192:195], v[240:243], v[216:223], v251\n\t" \
  "s_waitcnt lgkmcnt(3)\n\t" \
  "v_smfmac_f32_16x16x64_bf16 v[196:199], v[240:243], v[224:231], v251\n\t" \
  "s_waitcnt lgkmcnt(2)\n\t" \
  "v_smfmac_f32_16x16x64_bf16 v[192:195], v[244:247], v[144:151], v251\n\t" \
  "v_smfmac_f32_16x16x64_bf16 v[196:199], v[244:247], v[152:159], v251\n\t" \
  "s_waitcnt lgkmcnt(1)\n\t" \
  "v_smfmac_f32_16x16x64_bf16 v[192:195], v[200:203], v[160:167], v251\n\t" \
  "v_smfmac_f32_16x16x64_bf16 v[196:199], v[200:203], v[168:175], v251\n\t" \
  "s_waitcnt lgkmcnt(0)\n\t" \
  "v_smfmac_f32_16x16x64_bf16 v[192:195], v[204:207], v[176:183], v251\n\t" \
  "v_smfmac_f32_16x16x64_bf16 v[196:199], v[204:207], v[184:191], v251\n\t" \
  "s_nop 6\n\t" \
  "v_pk_add_f32 v[192:193], v[192:193], v[194:195]\n\t" \
  "v_pk_add_f32 v[196:197], v[196:197], v[198:199]\n\t" \
  "v_cvt_pk_bf16_f32 v192, v192, v193\n\t" \
  "v_cvt_pk_bf16_f32 v196, v196, v197\n\t" \
  "ds_write2_b32 v252, v192, v196 offset1:64\n\t" \
  "s_waitcnt lgkmcnt(0)\n\t" \
  "s_barrier\n\t" \
  "buffer_store_dword v192, v235, %[rs], %[sk] offen sc1\n\t" \
  "buffer_store_dword v196, v235, %[rs], %[sk] offen offset:256 sc1\n\t" \
  "s_mov_b32 %[spins], s92\n\t" \
  "s_branch .Lend_%=\n\t" \
  ".Lslow_%=:\n\t" \
  "s_mov_b64 s[94:95], 0\n\t" \
  "v_cmp_eq_u32 vcc, %[sent], v200\n\t" \
  "s_or_b64 s[94:95], s[94:95], vcc\n\t" \
  "v_cmp_eq_u32 vcc, %[sent], v201\n\t" \
  "s_or_b64 s[94:95], s[94:95], vcc\n\t" \
  "v_cmp_eq_u32 vcc, %[sent], v202\n\t" \
  "s_or_b64 s[94:95], s[94:95], vcc\n\t" \
  "v_cmp_eq_u32 vcc, %[sent], v203\n\t" \
  "s_or_b64 s[94:95], s[94:95], vcc\n\t" \
  "v_cmp_eq_u32 vcc, %[sent], v204\n\t" \
  "s_or_b64 s[94:95], s[94:95], vcc\n\t" \
  "v_cmp_eq_u32 vcc, %[sent], v205\n\t" \
  "s_or_b64 s[94:95], s[94:95], vcc\n\t" \
  "v_cmp_eq_u32 vcc, %[sent], v206\n\t" \
  "s_or_b64 s[94:95], s[94:95], vcc\n\t" \
  "v_cmp_eq_u32 vcc, %[sent], v207\n\t" \
  "s_or_b64 s[94:95], s[94:95], vcc\n\t" \
  "s_cmp_eq_u64 s[94:95], 0\n\t" \
  "s_cbranch_scc1 .Lok_%=\n\t" \
  "v_mov_b64 v[192:193], 0\n\t" \
  "v_mov_b64 v[194:195], 0\n\t" \
  "v_mov_b64 v[196:197], 0\n\t" \
  "v_mov_b64 v[198:199], 0\n\t" \
  "s_add_u32 s92, s92, 1\n\t" \
  "s_cmp_eq_u32 s92, 1\n\t" \
  "s_cbranch_scc1 .Lpoll_%=\n\t" \
  "s_sleep 2\n\t" \
  "s_and_b32 s93, s92, 63\n\t" \
  "s_cmp_lg_u32 s93, 0\n\t" \
  "s_cbranch_scc1 .Lpoll_%=\n\t" \
  "v_mov_b32 v245, 0\n\t" \
  "buffer_load_dword v244, v245, %[rs], %[sab] offen sc1\n\t" \
  "s_waitcnt vmcnt(0)\n\t" \
  "v_readfirstlane_b32 s93, v244\n\t" \
  "s_nop 3\n\t" \
  "s_cmp_eq_u32 s93, 1\n\t" \
  "s_cbranch_scc1 .Lok_%=\n\t" \
  "s_cmp_lt_u32 s92, 0x80000\n\t" \
  "s_cbranch_scc1 .Lpoll_%=\n\t" \
  "v_mov_b32 v244, 1\n\t" \
  "buffer_store_dword v244, v245, %[rs], %[sab] offen sc1\n\t" \
  "s_branch .Lok_%=\n\t" \
  ".Lend_%=:\n\t"
#define QK_ASM_7 \
  "s_waitcnt lgkmcnt(0)\n\t" \
  "v_subrev_u32 v244, 0x8000, v250\n\t" \
  "ds_read_b128 v[216:219], v248 offset:61440\n\t" \
  "ds_read_b128 v[220:223], v248 offset:62464\n\t" \
  "ds_read_b128 v[224:227], v248 offset:63488\n\t" \
  "ds_read_b128 v[228:231], v248 offset:64512\n\t" \
  "ds_read_b128 v[208:211], v244 offset:0\n\t" \
  "ds_read_b128 v[212:215], v244 offset:1024\n\t" \
  "ds_read_b128 v[236:239], v244 offset:2048\n\t" \
  "ds_read_b128 v[240:243], v244 offset:3072\n\t" \
  "v_mov_b64 v[192:193], 0\n\t" \
  "v_mov_b64 v[194:195], 0\n\t" \
  "v_mov_b64 v[196:197], 0\n\t" \
  "v_mov_b64 v[198:199], 0\n\t" \
  ".p2align 5\n\t" \
  "s_nop 0\n\t" \
  "s_nop 0\n\t" \
  "s_nop 0\n\t" \
  "s_nop 0\n\t" \
  "s_movk_i32 s92, 0x2000\n\t" \
  ".Lwt_%=:\n\t" \
  "ds_read_b64 v[200:201], v248 offset:30720\n\t" \
  "s_memrealtime s[90:91]\n\t" \
  "s_sub_u32 s92, s92, 1\n\t" \
  "s_waitcnt lgkmcnt(0)\n\t" \
  "v_readfirstlane_b32 s93, v200\n\t" \
  "v_readfirstlane_b32 %[tgt], v201\n\t" \
  "s_cbranch_scc1 .Lgo_%=\n\t" \
  "s_cmp_eq_u32 s93, %[step]\n\t" \
  "s_cbranch_scc1 .Lwx_%=\n\t" \
  "s_sleep 1\n\t" \
  "s_branch .Lwt_%=\n\t" \
  ".Lwx_%=:\n\t" \
  "s_lshl_b32 s93, s90, 2\n\t" \
  "s_sub_i32 s93, %[tgt], s93\n\t" \
  "s_cmp_le_i32 s93, 0\n\t" \
  "s_cbranch_scc1 .Lgo_%=\n\t" \
  "s_min_i32 s93, s93, 8000\n\t" \
  "s_mov_b32 s94, s93\n\t" \
  ".Lsl_%=:\n\t" \
  "s_sub_i32 s94, s94, 18\n\t" \
  "s_cmp_lt_i32 s94, 30\n\t" \
  "s_cbranch_scc1 .Lsx_%=\n\t" \
  "s_sleep 1\n\t" \
  "s_branch .Lsl_%=\n\t" \
  ".Lsx_%=:\n\t" \
  "s_sub_i32 s93, s93, 12\n\t" \
  "s_max_i32 s93, s93, 4\n\t" \
  "s_lshr_b32 s93, s93, 2\n\t" \
  "s_add_u32 s93, s93, s90\n\t" \
  "s_memrealtime s[90:91]\n\t" \
  ".Lpace_%=:\n\t" \
  "s_memrealtime s[94:95]\n\t" \
  "s_waitcnt lgkmcnt(1)\n\t" \
  "s_sub_i32 s92, s90, s93\n\t" \
  "s_cmp_ge_i32 s92, 0\n\t" \
  "s_cbranch_scc1 .Le_%=\n\t" \
  "s_memrealtime s[90:91]\n\t" \
  "s_waitcnt lgkmcnt(1)\n\t" \
  "s_sub_i32 s92, s94, s93\n\t" \
  "s_cmp_lt_i32 s92, 0\n\t" \
  "s_cbranch_scc1 .Lpace_%=\n\t" \
  "s_nop 4\n\t" \
  ".Le_%=:\n\t" \
  "s_add_u32 s92, s92, s93\n\t" \
  "s_lshl_b32 s92, s92, 2\n\t" \
  "s_sub_i32 s92, %[tgt], s92\n\t" \
  "s_max_i32 s92, s92, 0\n\t" \
  "s_bitcmp0_b32 s92, 3\n\t" \
  "s_cbranch_scc1 .Lf3_%=\n\t" \
  "s_nop 15\n\t" \
  ".Lf3_%=:\n\t" \
  "s_bitcmp0_b32 s92, 2\n\t" \
  "s_cbranch_scc1 .Lf2_%=\n\t" \
  "s_nop 10\n\t" \
  ".Lf2_%=:\n\t" \
  ".Lgo_%=:\n\t" \
  "s_mov_b32 s92, 0\n\t" \
  "s_memtime s[88:89]\n\t" \
  ".Lpoll_%=:\n\t" \
  "buffer_load_dwordx4 v[200:203], v248, %[rs], %[som] offen sc1\n\t" \
  "buffer_load_dwordx4 v[204:207], v248, %[rs], %[som2] offen sc1\n\t" \
  ".p2align 5\n\t" \
  "s_nop 0\n\t" \
  "s_nop 0\n\t" \
  "s_nop 0\n\t" \
  "s_nop 0\n\t" \
  "s_nop 0\n\t" \
  "s_nop 0\n\t" \
  "s_nop 0\n\t" \
  "s_waitcnt vmcnt(1) lgkmcnt(0)\n\t" \
  "ds_write_b128 v249, v[200:203]\n\t" \
  "v_smfmac_f32_16x16x64_bf16 v[192:195], v[200:203], v[208:215], v251\n\t" \
  "v_max3_u32 v244, v200, v201, v202\n\t" \
  "v_smfmac_f32_16x16x64_bf16 v[196:199], v[200:203], v[236:243], v251\n\t" \
  "s_waitcnt vmcnt(0)\n\t" \
  "ds_write_b128 v249, v[204:207] offset:8192\n\t" \
  "v_smfmac_f32_16x16x64_bf16 v[192:195], v[204:207], v[216:223], v251\n\t" \
  "v_max3_u32 v244, v244, v203, v204\n\t" \
  "v_max3_u32 v244, v244, v205, v206\n\t" \
  "v_smfmac_f32_16x16x64_bf16 v[196:199], v[204:207], v[224:231], v251\n\t" \
  "v_max_u32 v244, v244, v207\n\t" \
  "v_cmp_le_u32 vcc, %[sent], v244\n\t" \
  "s_cbranch_vccnz .Lslow_%=\n\t" \
  ".Lok_%=:\n\t" \
  "s_waitcnt lgkmcnt(0)\n\t" \
  "s_setprio 3\n\t" \
  "s_barrier\n\t" \
  "s_memrealtime s[90:91]\n\t" \
  "s_memtime s[94:95]\n\t" \
  "v_mov_b32 v232, %[step]\n\t" \
  "v_lshrrev_b32_e64 v234, 1, v248\n\t" \
  "s_mov_b64 exec, 0xffff\n\t" \
  "s_waitcnt lgkmcnt(0)\n\t" \
  "s_lshl_b32 s90, s90, 2\n\t" \
  "s_sub_u32 s94, s94, s88\n\t" \
  "s_mul_i32 s94, s94, 12200\n\t" \
  "s_lshr_b32 s94, s94, 16\n\t" \
  "s_add_u32 s94, s94, %[tgt]\n\t" \
  "s_sub_i32 s93, s90, s94\n\t" \
  "s_sub_i32 s93, s93, 30\n\t" \
  "s_max_i32 s93, s93, 0\n\t" \
  "s_add_u32 s90, s94, s93\n\t" \
  "s_sub_i32 s93, s90, %[tgt]\n\t" \
  "s_cmp_gt_i32 s93, 270\n\t" \
  "s_cselect_b32 s93, 70, 0\n\t" \
  "s_add_u32 s93, s93, s90\n\t" \
  "s_add_u32 s93, s93, %[lead]\n\t" \
  "v_mov_b32 v233, s93\n\t" \
  "buffer_store_dwordx2 v[232:233], v234, %[rs], %[sbc] offen sc1\n\t" \
  "s_mov_b64 exec, -1\n\t" \
  "s_mov_b32 %[tb1], s90\n\t" \
  "s_setprio 0\n\t" \
  "ds_read_b128 v[208:211], v248 offset:9216\n\t" \
  "ds_read_b128 v[212:215], v248 offset:10240\n\t" \
  "ds_read_b128 v[236:239], v248 offset:11264\n\t" \
  "ds_read_b128 v[240:243], v248 offset:13312\n\t" \
  "ds_read_b128 v[244:247], v248 offset:14336\n\t" \
  "s_waitcnt lgkmcnt(4)\n\t" \
  "ds_read_b128 v[200:203], v248 offset:6144\n\t" \
  "v_smfmac_f32_16x16x64_bf16 v[192:195], v[208:211], v[0:7], v251\n\t" \
  "ds_read_b128 v[216:219], v250 offset:32768\n\t" \
  "v_smfmac_f32_16x16x64_bf16 v[196:199], v[208:211], v[8:15], v251\n\t" \
  "s_waitcnt lgkmcnt(5)\n\t" \
  "ds_read_b128 v[220:223], v250 offset:33792\n\t" \
  "v_smfmac_f32_16x16x64_bf16 v[192:195], v[212:215], v[16:23], v251\n\t" \
  "ds_read_b128 v[224:227], v250 offset:34816\n\t" \
  "v_smfmac_f32_16x16x64_bf16 v[196:199], v[212:215], v[24:31], v251\n\t" \
  "s_waitcnt lgkmcnt(6)\n\t" \
  "ds_read_b128 v[228:231], v250 offset:35840\n\t" \
  "v_smfmac_f32_16x16x64_bf16 v[192:195], v[236:239], v[32:39], v251\n\t" \
  "ds_read_b128 v[204:207], v248 offset:0\n\t" \
  "v_smfmac_f32_16x16x64_bf16 v[196:199], v[236:239], v[40:47], v251\n\t" \
  "s_waitcnt lgkmcnt(7)\n\t" \
  "ds_read_b128 v[208:211], v248 offset:1024\n\t" \
  "v_smfmac_f32_16x16x64_bf16 v[192:195], v[240:243], v[48:55], v251\n\t" \
  "ds_read_b128 v[212:215], v248 offset:2048\n\t" \
  "v_smfmac_f32_16x16x64_bf16 v[196:199], v[240:243], v[56:63], v251\n\t" \
  "s_waitcnt lgkmcnt(7)\n\t" \
  "v_smfmac_f32_16x16x64_bf16 v[192:195], v[244:247], v[64:71], v251\n\t" \
  "ds_read_b128 v[236:239], v248 offset:3072\n\t" \
  "v_smfmac_f32_16x16x64_bf16 v[196:199], v[244:247], v[72:79], v251\n\t" \
  "s_waitcnt lgkmcnt(6)\n\t" \
  "v_smfmac_f32_16x16x64_bf16 v[192:195], v[200:203], v[216:223], v251\n\t" \
  "s_waitcnt lgkmcnt(4)\n\t" \
  "ds_read_b128 v[240:243], v248 offset:12288\n\t" \
  "v_smfmac_f32_16x16x64_bf16 v[196:199], v[200:203], v[224:231], v251\n\t" \
  "s_waitcnt lgkmcnt(4)\n\t" \
  "ds_read_b128 v[216:219], v250 offset:0\n\t" \
  "v_smfmac_f32_16x16x64_bf16 v[192:195], v[204:207], v[80:87], v251\n\t" \
  "ds_read_b128 v[220:223], v250 offset:1024\n\t" \
  "v_smfmac_f32_16x16x64_bf16 v[196:199], v[204:207], v[88:95], v251\n\t" \
  "s_waitcnt lgkmcnt(4)\n\t" \
  "ds_read_b128 v[224:227], v250 offset:2048\n\t" \
  "v_smfmac_f32_16x16x64_bf16 v[192:195], v[208:211], v[96:103], v251\n\t" \
  "ds_read_b128 v[228:231], v250 offset:3072\n\t" \
  "v_smfmac_f32_16x16x64_bf16 v[196:199], v[208:211], v[104:111], v251\n\t" \
  "ds_read_b128 v[244:247], v248 offset:4096\n\t" \
  "v_smfmac_f32_16x16x64_bf16 v[192:195], v[212:215], v[112:119], v251\n\t" \
  "ds_read_b128 v[200:203], v248 offset:5120\n\t" \
  "v_smfmac_f32_16x16x64_bf16 v[196:199], v[212:215], v[120:127], v251\n\t" \
  "s_waitcnt lgkmcnt(6)\n\t" \
  "v_smfmac_f32_16x16x64_bf16 v[192:195], v[236:239], v[128:135], v251\n\t" \
  "ds_read_b128 v[204:207], v248 offset:8192\n\t" \
  "v_smfmac_f32_16x16x64_bf16 v[196:199], v[236:239], v[136:143], v251\n\t" \
  "s_waitcnt lgkmcnt(5)\n\t" \
  "v_smfmac_f32_16x16x64_bf16 v[192:195], v[240:243], v[216:223], v251\n\t" \
  "s_waitcnt lgkmcnt(3)\n\t" \
  "v_smfmac_f32_16x16x64_bf16 v[196:199], v[240:243], v[224:231], v251\n\t" \
  "s_waitcnt lgkmcnt(2)\n\t" \
  "v_smfmac_f32_16x16x64_bf16 v[192:195], v[244:247], v[144:151], v251\n\t" \
  "v_smfmac_f32_16x16x64_bf16 v[196:199], v[244:247], v[152:159], v251\n\t" \
  "s_waitcnt lgkmcnt(1)\n\t" \
  "v_smfmac_f32_16x16x64_bf16 v[192:195], v[200:203], v[160:167], v251\n\t" \
  "v_smfmac_f32_16x16x64_bf16 v[196:199], v[200:203], v[168:175], v251\n\t" \
  "s_waitcnt lgkmcnt(0)\n\t" \
  "v_smfmac_f32_16x16x64_bf16 v[192:195], v[204:207], v[176:183], v251\n\t" \
  "v_smfmac_f32_16x16x64_bf16 v[196:199], v[204:207], v[184:191], v251\n\t" \
  "s_nop 6\n\t" \
  "v_pk_add_f32 v[192:193], v[192:193], v[194:195]\n\t" \
  "v_pk_add_f32 v[196:197], v[196:197], v[198:199]\n\t" \
  "v_cvt_pk_bf16_f32 v192, v192, v193\n\t" \
  "v_cvt_pk_bf16_f32 v196, v196, v197\n\t" \
  "ds_write2_b32 v252, v192, v196 offset1:64\n\t" \
  "s_waitcnt lgkmcnt(0)\n\t" \
  "s_barrier\n\t" \
  "buffer_store_dword v192, v235, %[rs], %[sk] offen sc1\n\t" \
  "buffer_store_dword v196, v235, %[rs], %[sk] offen offset:256 sc1\n\t" \
  "s_mov_b32 %[spins], s92\n\t" \
  "s_branch .Lend_%=\n\t" \
  ".Lslow_%=:\n\t" \
  "s_mov_b64 s[94:95], 0\n\t" \
  "v_cmp_eq_u32 vcc, %[sent], v200\n\t" \
  "s_or_b64 s[94:95], s[94:95], vcc\n\t" \
  "v_cmp_eq_u32 vcc, %[sent], v201\n\t" \
  "s_or_b64 s[94:95], s[94:95], vcc\n\t" \
  "v_cmp_eq_u32 vcc, %[sent], v202\n\t" \
  "s_or_b64 s[94:95], s[94:95], vcc\n\t" \
  "v_cmp_eq_u32 vcc, %[sent], v203\n\t" \
  "s_or_b64 s[94:95], s[94:95], vcc\n\t" \
  "v_cmp_eq_u32 vcc, %[sent], v204\n\t" \
  "s_or_b64 s[94:95], s[94:95], vcc\n\t" \
  "v_cmp_eq_u32 vcc, %[sent], v205\n\t" \
  "s_or_b64 s[94:95], s[94:95], vcc\n\t" \
  "v_cmp_eq_u32 vcc, %[sent], v206\n\t" \
  "s_or_b64 s[94:95], s[94:95], vcc\n\t" \
  "v_cmp_eq_u32 vcc, %[sent], v207\n\t" \
  "s_or_b64 s[94:95], s[94:95], vcc\n\t" \
  "s_cmp_eq_u64 s[94:95], 0\n\t" \
  "s_cbranch_scc1 .Lok_%=\n\t" \
  "v_mov_b64 v[192:193], 0\n\t" \
  "v_mov_b64 v[194:195], 0\n\t" \
  "v_mov_b64 v[196:197], 0\n\t" \
  "v_mov_b64 v[198:199], 0\n\t" \
  "s_add_u32 s92, s92, 1\n\t" \
  "s_cmp_eq_u32 s92, 1\n\t" \
  "s_cbranch_scc1 .Lpoll_%=\n\t" \
  "s_sleep 2\n\t" \
  "s_and_b32 s93, s92, 63\n\t" \
  "s_cmp_lg_u32 s93, 0\n\t" \
  "s_cbranch_scc1 .Lpoll_%=\n\t" \
  "v_mov_b32 v245, 0\n\t" \
  "buffer_load_dword v244, v245, %[rs], %[sab] offen sc1\n\t" \
  "s_waitcnt vmcnt(0)\n\t" \
  "v_readfirstlane_b32 s93, v244\n\t" \
  "s_nop 3\n\t" \
  "s_cmp_eq_u32 s93, 1\n\t" \
  "s_cbranch_scc1 .Lok_%=\n\t" \
  "s_cmp_lt_u32 s92, 0x80000\n\t" \
  "s_cbranch_scc1 .Lpoll_%=\n\t" \
  "v_mov_b32 v244, 1\n\t" \
  "buffer_store_dword v244, v245, %[rs], %[sab] offen sc1\n\t" \
  "s_branch .Lok_%=\n\t" \
  ".Lend_%=:\n\t"
#define VV_ASM \
  "s_waitcnt lgkmcnt(0)\n\t" \
  "v_mov_b64 v[128:129], 0\n\t" \
  "v_mov_b64 v[130:131], 0\n\t" \
  ".p2align 5\n\t" \
  "s_nop 0\n\t" \
  "s_nop 0\n\t" \
  "s_nop 0\n\t" \
  "s_nop 0\n\t" \
  "s_movk_i32 s92, 0x2000\n\t" \
  ".Lwt_%=:\n\t" \
  "ds_read_b64 v[132:133], v200 offset:30720\n\t" \
  "s_memrealtime s[90:91]\n\t" \
  "s_sub_u32 s92, s92, 1\n\t" \
  "s_waitcnt lgkmcnt(0)\n\t" \
  "v_readfirstlane_b32 s93, v132\n\t" \
  "v_readfirstlane_b32 %[tgt], v133\n\t" \
  "s_cbranch_scc1 .Lgo_%=\n\t" \
  "s_cmp_eq_u32 s93, %[step]\n\t" \
  "s_cbranch_scc1 .Lwx_%=\n\t" \
  "s_sleep 1\n\t" \
  "s_branch .Lwt_%=\n\t" \
  ".Lwx_%=:\n\t" \
  "s_lshl_b32 s93, s90, 2\n\t" \
  "s_sub_i32 s93, %[tgt], s93\n\t" \
  "s_cmp_le_i32 s93, 0\n\t" \
  "s_cbranch_scc1 .Lgo_%=\n\t" \
  "s_min_i32 s93, s93, 8000\n\t" \
  "s_mov_b32 s94, s93\n\t" \
  ".Lsl_%=:\n\t" \
  "s_sub_i32 s94, s94, 18\n\t" \
  "s_cmp_lt_i32 s94, 30\n\t" \
  "s_cbranch_scc1 .Lsx_%=\n\t" \
  "s_sleep 1\n\t" \
  "s_branch .Lsl_%=\n\t" \
  ".Lsx_%=:\n\t" \
  "s_sub_i32 s93, s93, 12\n\t" \
  "s_max_i32 s93, s93, 4\n\t" \
  "s_lshr_b32 s93, s93, 2\n\t" \
  "s_add_u32 s93, s93, s90\n\t" \
  "s_memrealtime s[90:91]\n\t" \
  ".Lpace_%=:\n\t" \
  "s_memrealtime s[94:95]\n\t" \
  "s_waitcnt lgkmcnt(1)\n\t" \
  "s_sub_i32 s92, s90, s93\n\t" \
  "s_cmp_ge_i32 s92, 0\n\t" \
  "s_cbranch_scc1 .Le_%=\n\t" \
  "s_memrealtime s[90:91]\n\t" \
  "s_waitcnt lgkmcnt(1)\n\t" \
  "s_sub_i32 s92, s94, s93\n\t" \
  "s_cmp_lt_i32 s92, 0\n\t" \
  "s_cbranch_scc1 .Lpace_%=\n\t" \
  "s_nop 4\n\t" \
  ".Le_%=:\n\t" \
  "s_add_u32 s92, s92, s93\n\t" \
  "s_lshl_b32 s92, s92, 2\n\t" \
  "s_sub_i32 s92, %[tgt], s92\n\t" \
  "s_max_i32 s92, s92, 0\n\t" \
  "s_bitcmp0_b32 s92, 3\n\t" \
  "s_cbranch_scc1 .Lf3_%=\n\t" \
  "s_nop 15\n\t" \
  ".Lf3_%=:\n\t" \
  "s_bitcmp0_b32 s92, 2\n\t" \
  "s_cbranch_scc1 .Lf2_%=\n\t" \
  "s_nop 10\n\t" \
  ".Lf2_%=:\n\t" \
  ".Lgo_%=:\n\t" \
  "s_mov_b32 s92, 0\n\t" \
  ".Lpoll_%=:\n\t" \
  "buffer_load_dwordx4 v[132:135], v200, %[rs], %[som] offen sc1\n\t" \
  "buffer_load_dwordx4 v[136:139], v200, %[rs], %[som2] offen sc1\n\t" \
  ".p2align 5\n\t" \
  "s_waitcnt vmcnt(1) lgkmcnt(0)\n\t" \
  "ds_write_b128 v232, v[132:135]\n\t" \
  "v_smfmac_f32_16x16x64_bf16 v[128:131], v[132:135], v[0:7], v202\n\t" \
  "v_max3_u32 v196, v132, v133, v134\n\t" \
  "s_waitcnt vmcnt(0)\n\t" \
  "ds_write_b128 v232, v[136:139] offset:8192\n\t" \
  "v_smfmac_f32_16x16x64_bf16 v[128:131], v[136:139], v[64:71], v202\n\t" \
  "v_max3_u32 v196, v196, v135, v136\n\t" \
  "v_max3_u32 v196, v196, v137, v138\n\t" \
  "v_max_u32 v196, v196, v139\n\t" \
  "v_cmp_le_u32_e64 vcc, %[sent], v196\n\t" \
  "s_cbranch_vccnz .Lslow_%=\n\t" \
  ".Lok_%=:\n\t" \
  "s_waitcnt lgkmcnt(0)\n\t" \
  "s_barrier\n\t" \
  "ds_read_b128 v[164:167], v233 offset:0\n\t" \
  "ds_read_b128 v[140:143], v234 offset:0\n\t" \
  "ds_read_b128 v[144:147], v235 offset:0\n\t" \
  "ds_read_b128 v[148:151], v236 offset:0\n\t" \
  "ds_read_b128 v[152:155], v237 offset:0\n\t" \
  "s_waitcnt lgkmcnt(4)\n\t" \
  "ds_read_b128 v[156:159], v238 offset:0\n\t" \
  "v_smfmac_f32_16x16x64_bf16 v[128:131], v[164:167], v[8:15], v202\n\t" \
  "s_waitcnt lgkmcnt(4)\n\t" \
  "ds_read_b128 v[160:163], v239 offset:0\n\t" \
  "v_smfmac_f32_16x16x64_bf16 v[128:131], v[140:143], v[16:23], v202\n\t" \
  "s_waitcnt lgkmcnt(4)\n\t" \
  "ds_read_b128 v[168:171], v233 offset:8192\n\t" \
  "v_smfmac_f32_16x16x64_bf16 v[128:131], v[144:147], v[24:31], v202\n\t" \
  "s_waitcnt lgkmcnt(4)\n\t" \
  "ds_read_b128 v[172:175], v234 offset:8192\n\t" \
  "v_smfmac_f32_16x16x64_bf16 v[128:131], v[148:151], v[32:39], v202\n\t" \
  "s_waitcnt lgkmcnt(4)\n\t" \
  "ds_read_b128 v[176:179], v235 offset:8192\n\t" \
  "v_smfmac_f32_16x16x64_bf16 v[128:131], v[152:155], v[40:47], v202\n\t" \
  "s_waitcnt lgkmcnt(4)\n\t" \
  "ds_read_b128 v[180:183], v236 offset:8192\n\t" \
  "v_smfmac_f32_16x16x64_bf16 v[128:131], v[156:159], v[48:55], v202\n\t" \
  "s_waitcnt lgkmcnt(4)\n\t" \
  "ds_read_b128 v[184:187], v237 offset:8192\n\t" \
  "v_smfmac_f32_16x16x64_bf16 v[128:131], v[160:163], v[56:63], v202\n\t" \
  "s_waitcnt lgkmcnt(4)\n\t" \
  "ds_read_b128 v[188:191], v238 offset:8192\n\t" \
  "v_smfmac_f32_16x16x64_bf16 v[128:131], v[168:171], v[72:79], v202\n\t" \
  "s_waitcnt lgkmcnt(4)\n\t" \
  "ds_read_b128 v[192:195], v239 offset:8192\n\t" \
  "v_smfmac_f32_16x16x64_bf16 v[128:131], v[172:175], v[80:87], v202\n\t" \
  "s_waitcnt lgkmcnt(4)\n\t" \
  "v_smfmac_f32_16x16x64_bf16 v[128:131], v[176:179], v[88:95], v202\n\t" \
  "s_waitcnt lgkmcnt(3)\n\t" \
  "v_smfmac_f32_16x16x64_bf16 v[128:131], v[180:183], v[96:103], v202\n\t" \
  "s_waitcnt lgkmcnt(2)\n\t" \
  "v_smfmac_f32_16x16x64_bf16 v[128:131], v[184:187], v[104:111], v202\n\t" \
  "s_waitcnt lgkmcnt(1)\n\t" \
  "v_smfmac_f32_16x16x64_bf16 v[128:131], v[188:191], v[112:119], v202\n\t" \
  "s_waitcnt lgkmcnt(0)\n\t" \
  "v_smfmac_f32_16x16x64_bf16 v[128:131], v[192:195], v[120:127], v202\n\t" \
  "s_nop 7\n\t" \
  "v_add_f32 v128, v128, v130\n\t" \
  "v_add_f32_e64 v129, v129, v131\n\t" \
  "v_cvt_pk_bf16_f32 v128, v128, v129\n\t" \
  "buffer_store_dword v128, v203, %[rs], %[sv] offen sc1\n\t" \
  "ds_write_b32 v204, v128\n\t" \
  "s_waitcnt lgkmcnt(0)\n\t" \
  "s_barrier\n\t" \
  "s_mov_b32 %[spins], s92\n\t" \
  "s_branch .Lend_%=\n\t" \
  ".Lslow_%=:\n\t" \
  "s_mov_b64 s[94:95], 0\n\t" \
  "v_cmp_eq_u32 vcc, %[sent], v132\n\t" \
  "s_or_b64 s[94:95], s[94:95], vcc\n\t" \
  "v_cmp_eq_u32 vcc, %[sent], v133\n\t" \
  "s_or_b64 s[94:95], s[94:95], vcc\n\t" \
  "v_cmp_eq_u32 vcc, %[sent], v134\n\t" \
  "s_or_b64 s[94:95], s[94:95], vcc\n\t" \
  "v_cmp_eq_u32 vcc, %[sent], v135\n\t" \
  "s_or_b64 s[94:95], s[94:95], vcc\n\t" \
  "v_cmp_eq_u32 vcc, %[sent], v136\n\t" \
  "s_or_b64 s[94:95], s[94:95], vcc\n\t" \
  "v_cmp_eq_u32 vcc, %[sent], v137\n\t" \
  "s_or_b64 s[94:95], s[94:95], vcc\n\t" \
  "v_cmp_eq_u32 vcc, %[sent], v138\n\t" \
  "s_or_b64 s[94:95], s[94:95], vcc\n\t" \
  "v_cmp_eq_u32 vcc, %[sent], v139\n\t" \
  "s_or_b64 s[94:95], s[94:95], vcc\n\t" \
  "s_cmp_eq_u64 s[94:95], 0\n\t" \
  "s_cbranch_scc1 .Lok_%=\n\t" \
  "v_mov_b64 v[128:129], 0\n\t" \
  "v_mov_b64 v[130:131], 0\n\t" \
  "s_add_u32 s92, s92, 1\n\t" \
  "s_cmp_eq_u32 s92, 1\n\t" \
  "s_cbranch_scc1 .Lpoll_%=\n\t" \
  "s_sleep 2\n\t" \
  "s_and_b32 s93, s92, 63\n\t" \
  "s_cmp_lg_u32 s93, 0\n\t" \
  "s_cbranch_scc1 .Lpoll_%=\n\t" \
  "v_mov_b32 v197, 0\n\t" \
  "buffer_load_dword v196, v197, %[rs], %[sab] offen sc1\n\t" \
  "s_waitcnt vmcnt(0)\n\t" \
  "v_readfirstlane_b32 s93, v196\n\t" \
  "s_nop 3\n\t" \
  "s_cmp_eq_u32 s93, 1\n\t" \
  "s_cbranch_scc1 .Lok_%=\n\t" \
  "s_cmp_lt_u32 s92, 0x80000\n\t" \
  "s_cbranch_scc1 .Lpoll_%=\n\t" \
  "v_mov_b32 v196, 1\n\t" \
  "buffer_store_dword v196, v197, %[rs], %[sab] offen sc1\n\t" \
  "s_branch .Lok_%=\n\t" \
  ".Lend_%=:\n\t"
#define TK_ASM \
  "buffer_load_dwordx4 v[224:227], v200, %[rs], %[sk] offen offset:0 sc1\n\t" \
  "buffer_load_dwordx4 v[228:231], v200, %[rs], %[sk] offen offset:1024 sc1\n\t" \
  "s_memrealtime s[88:89]\n\t" \
  "v_add_u32 v205, %[tl], v200\n\t" \
  "v_add_u32 v206, %[tq], v205\n\t" \
  "v_add_u32 v207, %[tv], v205\n\t" \
  "s_waitcnt lgkmcnt(0)\n\t" \
  "ds_read_b128 v[128:131], v206 offset:8192\n\t" \
  "ds_read_b128 v[144:147], v205 offset:0\n\t" \
  "ds_read_b128 v[160:163], v205 offset:4096\n\t" \
  "ds_read_b128 v[132:135], v206 offset:9216\n\t" \
  "ds_read_b128 v[148:151], v205 offset:1024\n\t" \
  "ds_read_b128 v[164:167], v205 offset:5120\n\t" \
  "ds_read_b128 v[136:139], v206 offset:10240\n\t" \
  "ds_read_b128 v[152:155], v205 offset:2048\n\t" \
  "ds_read_b128 v[168:171], v205 offset:6144\n\t" \
  "ds_read_b128 v[140:143], v206 offset:11264\n\t" \
  "ds_read_b128 v[156:159], v205 offset:3072\n\t" \
  "ds_read_b128 v[172:175], v205 offset:7168\n\t" \
  "v_add_u32 v206, %[vp], v218\n\t" \
  "s_lshl_b32 s88, s88, 2\n\t" \
  "s_add_u32 s89, s88, %[od]\n\t" \
  "s_waitcnt lgkmcnt(10)\n\t" \
  "v_mfma_f32_16x16x32_bf16 v[196:199], v[144:147], v[128:131], 0\n\t" \
  "s_waitcnt lgkmcnt(9)\n\t" \
  "v_mfma_f32_16x16x32_bf16 v[144:147], v[160:163], v[128:131], 0\n\t" \
  "s_waitcnt lgkmcnt(7)\n\t" \
  "v_mfma_f32_16x16x32_bf16 v[196:199], v[148:151], v[132:135], v[196:199]\n\t" \
  "s_waitcnt lgkmcnt(6)\n\t" \
  "v_mfma_f32_16x16x32_bf16 v[144:147], v[164:167], v[132:135], v[144:147]\n\t" \
  "s_waitcnt lgkmcnt(4)\n\t" \
  "v_mfma_f32_16x16x32_bf16 v[196:199], v[152:155], v[136:139], v[196:199]\n\t" \
  "s_waitcnt lgkmcnt(3)\n\t" \
  "v_mfma_f32_16x16x32_bf16 v[144:147], v[168:171], v[136:139], v[144:147]\n\t" \
  "s_waitcnt lgkmcnt(1)\n\t" \
  "v_mfma_f32_16x16x32_bf16 v[196:199], v[156:159], v[140:143], v[196:199]\n\t" \
  "s_waitcnt lgkmcnt(0)\n\t" \
  "v_mfma_f32_16x16x32_bf16 v[144:147], v[172:175], v[140:143], v[144:147]\n\t" \
  "ds_read_b128 v[176:179], v207 offset:16384\n\t" \
  "ds_read_b128 v[180:183], v207 offset:17408\n\t" \
  "ds_read_b128 v[184:187], v207 offset:18432\n\t" \
  "ds_read_b128 v[188:191], v207 offset:19456\n\t" \
  "ds_read2st64_b32 v[192:193], v206 offset0:0 offset1:1\n\t" \
  "ds_read2st64_b32 v[194:195], v206 offset0:2 offset1:3\n\t" \
  "v_cvt_pk_bf16_f32 v197, v196, v197\n\t" \
  "v_cvt_pk_bf16_f32 v199, v198, v199\n\t" \
  "v_cvt_pk_bf16_f32 v145, v144, v145\n\t" \
  "v_cvt_pk_bf16_f32 v147, v146, v147\n\t" \
  "v_lshlrev_b32 v196, 16, v197\n\t" \
  "v_lshlrev_b32 v198, 16, v199\n\t" \
  "v_lshlrev_b32 v144, 16, v145\n\t" \
  "v_lshlrev_b32 v146, 16, v147\n\t" \
  "v_and_b32 v197, 0xffff0000, v197\n\t" \
  "v_and_b32 v199, 0xffff0000, v199\n\t" \
  "v_and_b32 v145, 0xffff0000, v145\n\t" \
  "v_and_b32 v147, 0xffff0000, v147\n\t" \
  "v_pk_mul_f32 v[196:197], v[196:197], %[sc] op_sel_hi:[1,0]\n\t" \
  "v_pk_mul_f32 v[198:199], v[198:199], %[sc] op_sel_hi:[1,0]\n\t" \
  "v_pk_mul_f32 v[144:145], v[144:145], %[sc] op_sel_hi:[1,0]\n\t" \
  "v_pk_mul_f32 v[146:147], v[146:147], %[sc] op_sel_hi:[1,0]\n\t" \
  "v_cvt_pk_bf16_f32 v197, v196, v197\n\t" \
  "v_cvt_pk_bf16_f32 v199, v198, v199\n\t" \
  "v_cvt_pk_bf16_f32 v145, v144, v145\n\t" \
  "v_cvt_pk_bf16_f32 v147, v146, v147\n\t" \
  "v_lshlrev_b32 v196, 16, v197\n\t" \
  "v_lshlrev_b32 v198, 16, v199\n\t" \
  "v_lshlrev_b32 v144, 16, v145\n\t" \
  "v_lshlrev_b32 v146, 16, v147\n\t" \
  "v_and_b32 v197, 0xffff0000, v197\n\t" \
  "v_and_b32 v199, 0xffff0000, v199\n\t" \
  "v_and_b32 v145, 0xffff0000, v145\n\t" \
  "v_and_b32 v147, 0xffff0000, v147\n\t" \
  "v_min_f32 v196, v196, v208\n\t" \
  "v_min_f32 v197, v197, v209\n\t" \
  "v_min_f32 v198, v198, v210\n\t" \
  "v_min_f32 v199, v199, v211\n\t" \
  "v_min_f32 v144, v144, v212\n\t" \
  "v_min_f32 v145, v145, v213\n\t" \
  "v_min_f32 v146, v146, v214\n\t" \
  "v_min_f32 v147, v147, v215\n\t" \
  "v_max3_f32 v148, v196, v197, v198\n\t" \
  "v_max3_f32 v149, v199, v144, v145\n\t" \
  "v_max3_f32 v148, v148, v146, v147\n\t" \
  "v_max_f32 v150, v148, v149\n\t" \
  "v_max_f32 v151, v148, v149\n\t" \
  "s_nop 1\n\t" \
  "v_permlane32_swap_b32 v150, v151\n\t" \
  "v_max_f32 v150, v150, v151\n\t" \
  "s_mov_b32 s92, 0\n\t" \
  ".Ltake_%=:\n\t" \
  "s_waitcnt vmcnt(1) lgkmcnt(0)\n\t" \
  "ds_write_b128 v216, v[224:227]\n\t" \
  "v_max3_u32 v148, v224, v225, v226\n\t" \
  "s_waitcnt vmcnt(0)\n\t" \
  "ds_write_b128 v216, v[228:231] offset:1024\n\t" \
  "ds_read_b64_tr_b16 v[160:161], v217 offset:0\n\t" \
  "ds_read_b64_tr_b16 v[162:163], v217 offset:64\n\t" \
  "ds_read_b64_tr_b16 v[164:165], v217 offset:512\n\t" \
  "ds_read_b64_tr_b16 v[166:167], v217 offset:576\n\t" \
  "ds_read_b64_tr_b16 v[168:169], v217 offset:1024\n\t" \
  "ds_read_b64_tr_b16 v[170:171], v217 offset:1088\n\t" \
  "ds_read_b64_tr_b16 v[172:173], v217 offset:1536\n\t" \
  "ds_read_b64_tr_b16 v[174:175], v217 offset:1600\n\t" \
  "v_max3_u32 v148, v148, v227, v228\n\t" \
  "v_max3_u32 v148, v148, v229, v230\n\t" \
  "v_max_u32 v148, v148, v231\n\t" \
  "v_cmp_le_u32 vcc, %[sent], v148\n\t" \
  "s_cbranch_vccnz .Lslow_%=\n\t" \
  ".Lok_%=:\n\t" \
  "s_waitcnt lgkmcnt(6)\n\t" \
  "v_mfma_f32_16x16x32_bf16 v[152:155], v[160:163], v[128:131], 0\n\t" \
  "s_waitcnt lgkmcnt(4)\n\t" \
  "v_mfma_f32_16x16x32_bf16 v[152:155], v[164:167], v[132:135], v[152:155]\n\t" \
  "s_waitcnt lgkmcnt(2)\n\t" \
  "v_mfma_f32_16x16x32_bf16 v[152:155], v[168:171], v[136:139], v[152:155]\n\t" \
  "s_waitcnt lgkmcnt(0)\n\t" \
  "v_mfma_f32_16x16x32_bf16 v[152:155], v[172:175], v[140:143], v[152:155]\n\t" \
  "v_mov_b32 v157, 0\n\t" \
  "v_mov_b32 v158, 0\n\t" \
  "s_nop 5\n\t" \
  "v_cvt_pk_bf16_f32 v153, v152, v153\n\t" \
  "v_cvt_pk_bf16_f32 v155, v154, v155\n\t" \
  "v_lshlrev_b32 v152, 16, v153\n\t" \
  "v_lshlrev_b32 v154, 16, v155\n\t" \
  "v_and_b32 v153, 0xffff0000, v153\n\t" \
  "v_and_b32 v155, 0xffff0000, v155\n\t" \
  "v_pk_mul_f32 v[152:153], v[152:153], %[sc] op_sel_hi:[1,0]\n\t" \
  "v_pk_mul_f32 v[154:155], v[154:155], %[sc] op_sel_hi:[1,0]\n\t" \
  "v_cvt_pk_bf16_f32 v153, v152, v153\n\t" \
  "v_cvt_pk_bf16_f32 v155, v154, v155\n\t" \
  "v_lshlrev_b32 v152, 16, v153\n\t" \
  "v_lshlrev_b32 v154, 16, v155\n\t" \
  "v_and_b32 v153, 0xffff0000, v153\n\t" \
  "v_and_b32 v155, 0xffff0000, v155\n\t" \
  "v_max3_f32 v148, v152, v153, v150\n\t" \
  "v_max3_f32 v149, v148, v154, v155\n\t" \
  "v_max3_f32 v150, v148, v154, v155\n\t" \
  "v_cndmask_b32 v152, v152, v154, %[up]\n\t" \
  "v_cndmask_b32 v153, v153, v155, %[up]\n\t" \
  "v_permlane16_swap_b32 v149, v150\n\t" \
  "v_max_f32 v148, v149, v150\n\t" \
  "v_mul_f32 v148, %[negc], v148\n\t" \
  "v_fma_f32 v152, v152, %[c], v148\n\t" \
  "v_fma_f32 v153, v153, %[c], v148\n\t" \
  "v_exp_f32 v152, v152\n\t" \
  "v_fma_f32 v196, v196, %[c], v148\n\t" \
  "v_exp_f32 v153, v153\n\t" \
  "v_fma_f32 v197, v197, %[c], v148\n\t" \
  "v_exp_f32 v196, v196\n\t" \
  "v_fma_f32 v198, v198, %[c], v148\n\t" \
  "v_exp_f32 v197, v197\n\t" \
  "v_fma_f32 v199, v199, %[c], v148\n\t" \
  "v_exp_f32 v198, v198\n\t" \
  "v_fma_f32 v144, v144, %[c], v148\n\t" \
  "v_pk_add_f32 v[132:133], v[152:153], v[196:197]\n\t" \
  "v_exp_f32 v199, v199\n\t" \
  "v_fma_f32 v145, v145, %[c], v148\n\t" \
  "v_exp_f32 v144, v144\n\t" \
  "v_fma_f32 v146, v146, %[c], v148\n\t" \
  "v_pk_add_f32 v[132:133], v[132:133], v[198:199]\n\t" \
  "v_exp_f32 v145, v145\n\t" \
  "v_fma_f32 v147, v147, %[c], v148\n\t" \
  "v_exp_f32 v146, v146\n\t" \
  "v_pk_add_f32 v[132:133], v[132:133], v[144:145]\n\t" \
  "v_exp_f32 v147, v147\n\t" \
  "s_nop 0\n\t" \
  "v_pk_add_f32 v[132:133], v[132:133], v[146:147]\n\t" \
  "v_add_f32 v134, v132, v133\n\t" \
  "v_add_f32 v135, v132, v133\n\t" \
  "s_nop 1\n\t" \
  "v_permlane16_swap_b32 v134, v135\n\t" \
  "v_add_f32 v132, v134, v135\n\t" \
  "v_add_f32 v133, v134, v135\n\t" \
  "s_nop 1\n\t" \
  "v_permlane32_swap_b32 v132, v133\n\t" \
  "v_add_f32 v132, v132, v133\n\t" \
  "v_rcp_f32 v148, v132\n\t" \
  "s_nop 0\n\t" \
  "v_pk_mul_f32 v[152:153], v[152:153], v[148:149] op_sel_hi:[1,0]\n\t" \
  "v_pk_mul_f32 v[196:197], v[196:197], v[148:149] op_sel_hi:[1,0]\n\t" \
  "v_pk_mul_f32 v[198:199], v[198:199], v[148:149] op_sel_hi:[1,0]\n\t" \
  "v_pk_mul_f32 v[144:145], v[144:145], v[148:149] op_sel_hi:[1,0]\n\t" \
  "v_pk_mul_f32 v[146:147], v[146:147], v[148:149] op_sel_hi:[1,0]\n\t" \
  "v_cvt_pk_bf16_f32 v156, v152, v153\n\t" \
  "v_cvt_pk_bf16_f32 v159, v152, v153\n\t" \
  "v_cvt_pk_bf16_f32 v128, v196, v197\n\t" \
  "v_cvt_pk_bf16_f32 v129, v198, v199\n\t" \
  "v_mfma_f32_16x16x16_bf16 v[160:163], v[192:193], v[156:157], 0\n\t" \
  "v_cvt_pk_bf16_f32 v130, v144, v145\n\t" \
  "v_mfma_f32_16x16x16_bf16 v[164:167], v[192:193], v[158:159], 0\n\t" \
  "v_cvt_pk_bf16_f32 v131, v146, v147\n\t" \
  "v_mfma_f32_16x16x16_bf16 v[168:171], v[194:195], v[156:157], 0\n\t" \
  "v_mfma_f32_16x16x16_bf16 v[172:175], v[194:195], v[158:159], 0\n\t" \
  "s_nop 0\n\t" \
  "v_mfma_f32_16x16x32_bf16 v[160:163], v[176:179], v[128:131], v[160:163]\n\t" \
  "s_nop 0\n\t" \
  "v_mfma_f32_16x16x32_bf16 v[164:167], v[180:183], v[128:131], v[164:167]\n\t" \
  "s_nop 0\n\t" \
  "v_mfma_f32_16x16x32_bf16 v[168:171], v[184:187], v[128:131], v[168:171]\n\t" \
  "v_mfma_f32_16x16x32_bf16 v[172:175], v[188:191], v[128:131], v[172:175]\n\t" \
  "s_nop 2\n\t" \
  "v_cvt_pk_bf16_f32 v136, v160, v161\n\t" \
  "v_cvt_pk_bf16_f32 v137, v162, v163\n\t" \
  "v_cvt_pk_bf16_f32 v138, v164, v165\n\t" \
  "v_cvt_pk_bf16_f32 v139, v166, v167\n\t" \
  "v_cvt_pk_bf16_f32 v140, v168, v169\n\t" \
  "v_cvt_pk_bf16_f32 v141, v170, v171\n\t" \
  "v_cvt_pk_bf16_f32 v142, v172, v173\n\t" \
  "v_cvt_pk_bf16_f32 v143, v174, v175\n\t" \
  "s_memrealtime s[90:91]\n\t" \
  "s_waitcnt lgkmcnt(0)\n\t" \
  "s_lshl_b32 s93, s90, 2\n\t" \
  "s_sub_i32 s93, s89, s93\n\t" \
  "s_cmp_le_i32 s93, 0\n\t" \
  "s_cbranch_scc1 .Lgoo_%=\n\t" \
  "s_min_i32 s93, s93, 8000\n\t" \
  "s_mov_b32 s94, s93\n\t" \
  ".Lslo_%=:\n\t" \
  "s_sub_i32 s94, s94, 18\n\t" \
  "s_cmp_lt_i32 s94, 30\n\t" \
  "s_cbranch_scc1 .Lsxo_%=\n\t" \
  "s_sleep 1\n\t" \
  "s_branch .Lslo_%=\n\t" \
  ".Lsxo_%=:\n\t" \
  "s_add_u32 s93, s93, 3\n\t" \
  "s_lshr_b32 s93, s93, 2\n\t" \
  "s_add_u32 s93, s93, s90\n\t" \
  "s_memrealtime s[90:91]\n\t" \
  ".Lpaceo_%=:\n\t" \
  "s_memrealtime s[94:95]\n\t" \
  "s_waitcnt lgkmcnt(1)\n\t" \
  "s_sub_i32 s92, s90, s93\n\t" \
  "s_cmp_ge_i32 s92, 0\n\t" \
  "s_cbranch_scc1 .Leo_%=\n\t" \
  "s_memrealtime s[90:91]\n\t" \
  "s_waitcnt lgkmcnt(1)\n\t" \
  "s_sub_i32 s92, s94, s93\n\t" \
  "s_cmp_lt_i32 s92, 0\n\t" \
  "s_cbranch_scc1 .Lpaceo_%=\n\t" \
  ".Leo_%=:\n\t" \
  ".Lgoo_%=:\n\t" \
  "buffer_store_dwordx4 v[136:139], v200, %[rt], %[so] offen offset:0 sc1\n\t" \
  "buffer_store_dwordx4 v[140:143], v200, %[rt], %[so] offen offset:1024 sc1\n\t" \
  "s_waitcnt lgkmcnt(0)\n\t" \
  "s_branch .Lend_%=\n\t" \
  ".Lpoll_%=:\n\t" \
  "buffer_load_dwordx4 v[224:227], v200, %[rs], %[sk] offen offset:0 sc1\n\t" \
  "buffer_load_dwordx4 v[228:231], v200, %[rs], %[sk] offen offset:1024 sc1\n\t" \
  "s_branch .Ltake_%=\n\t" \
  "s_branch .Lend_%=\n\t" \
  ".Lslow_%=:\n\t" \
  "s_mov_b64 s[94:95], 0\n\t" \
  "v_cmp_eq_u32 vcc, %[sent], v224\n\t" \
  "s_or_b64 s[94:95], s[94:95], vcc\n\t" \
  "v_cmp_eq_u32 vcc, %[sent], v225\n\t" \
  "s_or_b64 s[94:95], s[94:95], vcc\n\t" \
  "v_cmp_eq_u32 vcc, %[sent], v226\n\t" \
  "s_or_b64 s[94:95], s[94:95], vcc\n\t" \
  "v_cmp_eq_u32 vcc, %[sent], v227\n\t" \
  "s_or_b64 s[94:95], s[94:95], vcc\n\t" \
  "v_cmp_eq_u32 vcc, %[sent], v228\n\t" \
  "s_or_b64 s[94:95], s[94:95], vcc\n\t" \
  "v_cmp_eq_u32 vcc, %[sent], v229\n\t" \
  "s_or_b64 s[94:95], s[94:95], vcc\n\t" \
  "v_cmp_eq_u32 vcc, %[sent], v230\n\t" \
  "s_or_b64 s[94:95], s[94:95], vcc\n\t" \
  "v_cmp_eq_u32 vcc, %[sent], v231\n\t" \
  "s_or_b64 s[94:95], s[94:95], vcc\n\t" \
  "s_cmp_eq_u64 s[94:95], 0\n\t" \
  "s_cbranch_scc1 .Lok_%=\n\t" \
  "s_add_u32 s92, s92, 1\n\t" \
  "s_cmp_eq_u32 s92, 1\n\t" \
  "s_cbranch_scc1 .Lpoll_%=\n\t" \
  "s_sleep 2\n\t" \
  "s_and_b32 s93, s92, 63\n\t" \
  "s_cmp_lg_u32 s93, 0\n\t" \
  "s_cbranch_scc1 .Lpoll_%=\n\t" \
  "v_mov_b32 v149, 0\n\t" \
  "buffer_load_dword v148, v149, %[rs], %[sab] offen sc1\n\t" \
  "s_waitcnt vmcnt(0)\n\t" \
  "v_readfirstlane_b32 s93, v148\n\t" \
  "s_nop 3\n\t" \
  "s_cmp_eq_u32 s93, 1\n\t" \
  "s_cbranch_scc1 .Lok_%=\n\t" \
  "s_cmp_lt_u32 s92, 0x80000\n\t" \
  "s_cbranch_scc1 .Lpoll_%=\n\t" \
  "v_mov_b32 v148, 1\n\t" \
  "buffer_store_dword v148, v149, %[rs], %[sab] offen sc1\n\t" \
  "s_branch .Lok_%=\n\t" \
  ".Lend_%=:\n\t"
constexpr int TLAG = 2, R4 = 0;
#define RED_ASM \
  "s_mov_b32 %[step], 0\n\t" \
  "s_lshl_b32 %[sh], %[step], 15\n\t" \
  "s_lshr_b32 s93, %[step], 4\n\t" \
  "s_lshr_b32 s93, %[pm], s93\n\t" \
  "s_and_b32 s93, s93, 4\n\t" \
  "s_lshl_b32 s93, s93, 11\n\t" \
  "s_lshl2_add_u32 %[so], %[sh], s93\n\t" \
  "s_sub_i32 s93, %[step], 2\n\t" \
  "s_lshl_b32 %[st], s93, 19\n\t" \
  "s_lshl_b32 s94, s93, 11\n\t" \
  "s_xor_b32 s94, s94, %[s0]\n\t" \
  "s_and_b32 s94, s94, 0x2000\n\t" \
  "s_or_b32 %[st], %[st], s94\n\t" \
  "s_cmp_lt_i32 s93, 0\n\t" \
  "s_cselect_b32 %[st], 0x70000000, %[st]\n\t" \
  "s_add_u32 %[st4], %[st], 4096\n\t" \
  "buffer_load_dwordx2 v[166:167], v173, %[rs], 0 offen sc1\n\t" \
  "v_mov_b64 v[144:145], 0\n\t" \
  "v_mov_b64 v[146:147], 0\n\t" \
  "v_mov_b64 v[148:149], 0\n\t" \
  "v_mov_b64 v[150:151], 0\n\t" \
  "v_mov_b64 v[152:153], 0\n\t" \
  "v_mov_b64 v[154:155], 0\n\t" \
  "v_mov_b64 v[156:157], 0\n\t" \
  "v_mov_b64 v[158:159], 0\n\t" \
  ".Lstep_%=:\n\t" \
  "s_setprio 3\n\t" \
  "s_mov_b32 s92, 0\n\t" \
  ".Lcal_%=:\n\t" \
  "s_waitcnt vmcnt(0)\n\t" \
  "s_memrealtime s[76:77]\n\t" \
  "v_cmp_ne_u32 vcc, %[step], v166\n\t" \
  "v_readlane_b32 s90, v167, 0\n\t" \
  "v_readlane_b32 s91, v167, 1\n\t" \
  "v_readlane_b32 s94, v167, 2\n\t" \
  "v_readlane_b32 s95, v167, 3\n\t" \
  "s_sub_i32 s90, s90, %[base]\n\t" \
  "s_sub_i32 s91, s91, %[base]\n\t" \
  "s_sub_i32 s94, s94, %[base]\n\t" \
  "s_sub_i32 s95, s95, %[base]\n\t" \
  "s_max_i32 s90, s90, s91\n\t" \
  "s_max_i32 s94, s94, s95\n\t" \
  "s_max_i32 s90, s90, s94\n\t" \
  "s_add_i32 s90, s90, %[ds]\n\t" \
  "s_waitcnt lgkmcnt(0)\n\t" \
  "s_add_u32 %[E], s90, %[base]\n\t" \
  "s_and_b32 s93, vcc_lo, 0xf\n\t" \
  "s_cbranch_scc1 .Lbl_%=\n\t" \
  ".Lgo_%=:\n\t" \
  "v_mov_b32 v166, 7\n\t" \
  "s_mov_b64 exec, 1\n\t" \
  "v_mov_b32 v164, s90\n\t" \
  "ds_max_u32 v168, v164 offset:4100\n\t" \
  "ds_add_u32 v168, v164 offset:4096\n\t" \
  "s_mov_b64 exec, -1\n\t" \
  "s_memrealtime s[90:91]\n\t" \
  ".p2align 5\n\t" \
  "s_waitcnt lgkmcnt(0)\n\t" \
  "s_lshl_b32 s93, s90, 2\n\t" \
  "s_sub_i32 s93, %[E], s93\n\t" \
  "s_cmp_le_i32 s93, 0\n\t" \
  "s_cbranch_scc1 .Lgop_%=\n\t" \
  "s_min_i32 s93, s93, 8000\n\t" \
  "s_mov_b32 s94, s93\n\t" \
  ".Lslp_%=:\n\t" \
  "s_sub_i32 s94, s94, 18\n\t" \
  "s_cmp_lt_i32 s94, 30\n\t" \
  "s_cbranch_scc1 .Lsxp_%=\n\t" \
  "s_sleep 1\n\t" \
  "s_branch .Lslp_%=\n\t" \
  ".Lsxp_%=:\n\t" \
  "s_sub_i32 s93, s93, 12\n\t" \
  "s_max_i32 s93, s93, 4\n\t" \
  "s_lshr_b32 s93, s93, 2\n\t" \
  "s_add_u32 s93, s93, s90\n\t" \
  "s_memrealtime s[90:91]\n\t" \
  ".Lpacep_%=:\n\t" \
  "s_memrealtime s[94:95]\n\t" \
  "s_waitcnt lgkmcnt(1)\n\t" \
  "s_sub_i32 s92, s90, s93\n\t" \
  "s_cmp_ge_i32 s92, 0\n\t" \
  "s_cbranch_scc1 .Lep_%=\n\t" \
  "s_memrealtime s[90:91]\n\t" \
  "s_waitcnt lgkmcnt(1)\n\t" \
  "s_sub_i32 s92, s94, s93\n\t" \
  "s_cmp_lt_i32 s92, 0\n\t" \
  "s_cbranch_scc1 .Lpacep_%=\n\t" \
  "s_nop 4\n\t" \
  ".Lep_%=:\n\t" \
  "s_add_u32 s92, s92, s93\n\t" \
  "s_lshl_b32 s92, s92, 2\n\t" \
  "s_sub_i32 s92, %[E], s92\n\t" \
  "s_max_i32 s92, s92, 0\n\t" \
  "s_bitcmp0_b32 s92, 3\n\t" \
  "s_cbranch_scc1 .Lf3p_%=\n\t" \
  "s_nop 15\n\t" \
  ".Lf3p_%=:\n\t" \
  "s_bitcmp0_b32 s92, 2\n\t" \
  "s_cbranch_scc1 .Lf2p_%=\n\t" \
  "s_nop 10\n\t" \
  ".Lf2p_%=:\n\t" \
  ".Lgop_%=:\n\t" \
  "s_mov_b32 s92, 0\n\t" \
  "buffer_load_dwordx4 v[128:131], v234, %[rs], %[so] offen sc1\n\t" \
  "s_nop 15\n\t" \
  "s_nop 1\n\t" \
  "buffer_load_dwordx4 v[132:135], v235, %[rs], %[so] offen sc1\n\t" \
  "s_nop 15\n\t" \
  "s_nop 1\n\t" \
  "buffer_load_dwordx4 v[136:139], v236, %[rs], %[so] offen sc1\n\t" \
  "s_nop 15\n\t" \
  "s_nop 1\n\t" \
  "buffer_load_dwordx4 v[140:143], v237, %[rs], %[so] offen sc1\n\t" \
  ".p2align 5\n\t" \
  "s_nop 0\n\t" \
  "s_nop 0\n\t" \
  ".Lw_%=:\n\t" \
  "s_waitcnt lgkmcnt(0)\n\t" \
  "s_waitcnt vmcnt(3)\n\t" \
  "v_smfmac_f32_16x16x64_bf16 v[144:147], v[128:131], v[0:7], v172\n\t" \
  "v_max3_u32 v164, v128, v129, 0\n\t" \
  "v_smfmac_f32_16x16x64_bf16 v[148:151], v[128:131], v[8:15], v172\n\t" \
  "v_max3_u32 v164, v130, v131, v164\n\t" \
  "v_smfmac_f32_16x16x64_bf16 v[152:155], v[128:131], v[16:23], v172\n\t" \
  "buffer_load_dwordx4 v[176:179], v238, %[rt], %[st] offen offset:0 sc1\n\t" \
  "v_smfmac_f32_16x16x64_bf16 v[156:159], v[128:131], v[24:31], v172\n\t" \
  "s_waitcnt vmcnt(3)\n\t" \
  "v_smfmac_f32_16x16x64_bf16 v[144:147], v[132:135], v[32:39], v172\n\t" \
  "v_max3_u32 v164, v132, v133, v164\n\t" \
  "v_smfmac_f32_16x16x64_bf16 v[148:151], v[132:135], v[40:47], v172\n\t" \
  "v_max3_u32 v164, v134, v135, v164\n\t" \
  "v_smfmac_f32_16x16x64_bf16 v[152:155], v[132:135], v[48:55], v172\n\t" \
  "buffer_load_dwordx4 v[180:183], v238, %[rt], %[st] offen offset:1024 sc1\n\t" \
  "v_smfmac_f32_16x16x64_bf16 v[156:159], v[132:135], v[56:63], v172\n\t" \
  "s_waitcnt vmcnt(3)\n\t" \
  "v_smfmac_f32_16x16x64_bf16 v[144:147], v[136:139], v[64:71], v172\n\t" \
  "v_max3_u32 v164, v136, v137, v164\n\t" \
  "v_smfmac_f32_16x16x64_bf16 v[148:151], v[136:139], v[72:79], v172\n\t" \
  "v_max3_u32 v164, v138, v139, v164\n\t" \
  "v_smfmac_f32_16x16x64_bf16 v[152:155], v[136:139], v[80:87], v172\n\t" \
  "buffer_load_dwordx4 v[184:187], v238, %[rt], %[st4] offen offset:0 sc1\n\t" \
  "v_smfmac_f32_16x16x64_bf16 v[156:159], v[136:139], v[88:95], v172\n\t" \
  "s_waitcnt vmcnt(3)\n\t" \
  "v_smfmac_f32_16x16x64_bf16 v[144:147], v[140:143], v[96:103], v172\n\t" \
  "v_max3_u32 v164, v140, v141, v164\n\t" \
  "v_smfmac_f32_16x16x64_bf16 v[148:151], v[140:143], v[104:111], v172\n\t" \
  "v_max3_u32 v164, v142, v143, v164\n\t" \
  "v_smfmac_f32_16x16x64_bf16 v[152:155], v[140:143], v[112:119], v172\n\t" \
  "v_cmp_le_u32 vcc, %[sent], v164\n\t" \
  "buffer_load_dwordx4 v[188:191], v238, %[rt], %[st4] offen offset:1024 sc1\n\t" \
  "v_smfmac_f32_16x16x64_bf16 v[156:159], v[140:143], v[120:127], v172\n\t" \
  "s_cbranch_vccnz .Lslow_%=\n\t" \
  ".Lok_%=:\n\t" \
  "v_add_f32_dpp v160, v144, v146 quad_perm:[0,1,2,3] row_mask:0x3 bank_mask:0xf\n\t" \
  "v_add_f32_dpp v161, v145, v147 quad_perm:[0,1,2,3] row_mask:0x3 bank_mask:0xf\n\t" \
  "v_add_f32_dpp v162, v148, v150 quad_perm:[0,1,2,3] row_mask:0x3 bank_mask:0xf\n\t" \
  "v_add_f32_dpp v163, v149, v151 quad_perm:[0,1,2,3] row_mask:0x3 bank_mask:0xf\n\t" \
  "v_add_f32_dpp v160, v152, v154 quad_perm:[0,1,2,3] row_mask:0xc bank_mask:0xf\n\t" \
  "v_add_f32_dpp v161, v153, v155 quad_perm:[0,1,2,3] row_mask:0xc bank_mask:0xf\n\t" \
  "s_nop 0\n\t" \
  "v_add_f32_dpp v162, v156, v158 quad_perm:[0,1,2,3] row_mask:0xc bank_mask:0xf\n\t" \
  "v_add_f32_dpp v163, v157, v159 quad_perm:[0,1,2,3] row_mask:0xc bank_mask:0xf\n\t" \
  "s_nop 0\n\t" \
  "v_permlane32_swap_b32 v160, v162\n\t" \
  "v_permlane32_swap_b32 v161, v163\n\t" \
  "v_add_f32 v160, v160, v162\n\t" \
  "v_add_f32_e64 v161, v161, v163\n\t" \
  "ds_write_b64 v169, v[160:161]\n\t" \
  "s_mov_b64 exec, 1\n\t" \
  "ds_inc_rtn_u32 v164, v168, v166 offset:4104\n\t" \
  "s_mov_b64 exec, -1\n\t" \
  "ds_read_b64 v[128:129], v170 offset:0\n\t" \
  "ds_read_b64 v[130:131], v170 offset:512\n\t" \
  "ds_read_b64 v[132:133], v170 offset:1024\n\t" \
  "ds_read_b64 v[134:135], v170 offset:1536\n\t" \
  "ds_read_b64 v[136:137], v170 offset:2048\n\t" \
  "ds_read_b64 v[138:139], v170 offset:2560\n\t" \
  "ds_read_b64 v[140:141], v170 offset:3072\n\t" \
  "ds_read_b64 v[142:143], v170 offset:3584\n\t" \
  "s_waitcnt lgkmcnt(8)\n\t" \
  "v_readfirstlane_b32 s93, v164\n\t" \
  "s_cmp_lg_u32 s93, 7\n\t" \
  "s_cbranch_scc1 .Lb1_%=\n\t" \
  "s_waitcnt lgkmcnt(4)\n\t" \
  "v_pk_add_f32 v[128:129], v[128:129], v[130:131]\n\t" \
  "v_pk_add_f32 v[132:133], v[132:133], v[134:135]\n\t" \
  "s_waitcnt lgkmcnt(0)\n\t" \
  "v_pk_add_f32 v[136:137], v[136:137], v[138:139]\n\t" \
  "v_pk_add_f32 v[140:141], v[140:141], v[142:143]\n\t" \
  "v_pk_add_f32 v[128:129], v[128:129], v[132:133]\n\t" \
  "v_pk_add_f32 v[136:137], v[136:137], v[140:141]\n\t" \
  "v_pk_add_f32 v[128:129], v[128:129], v[136:137]\n\t" \
  "v_cvt_pk_bf16_f32 v128, v128, v129\n\t" \
  "s_nop 1\n\t" \
  "v_mov_b32_dpp v129, v128 quad_perm:[1,0,3,2] row_mask:0xf bank_mask:0xf\n\t" \
  "v_perm_b32 v128, v129, v128, v174\n\t" \
  "buffer_store_dword v128, v171, %[rs], %[sh] offen sc1\n\t" \
  "s_memtime s[90:91]\n\t" \
  "s_barrier\n\t" \
  "s_memtime s[76:77]\n\t" \
  "s_memrealtime s[78:79]\n\t" \
  "s_nop 3\n\t" \
  "s_memrealtime s[80:81]\n\t" \
  "s_nop 3\n\t" \
  "s_memrealtime s[82:83]\n\t" \
  "s_nop 3\n\t" \
  "s_memrealtime s[84:85]\n\t" \
  "s_nop 3\n\t" \
  "s_memrealtime s[86:87]\n\t" \
  "s_nop 3\n\t" \
  "s_memrealtime s[88:89]\n\t" \
  "s_mov_b32 s93, 0\n\t" \
  "s_waitcnt lgkmcnt(0)\n\t" \
  "s_cmp_eq_u32 s80, s78\n\t" \
  "s_addc_u32 s93, s93, 0\n\t" \
  "s_cmp_eq_u32 s82, s78\n\t" \
  "s_addc_u32 s93, s93, 0\n\t" \
  "s_cmp_eq_u32 s84, s78\n\t" \
  "s_addc_u32 s93, s93, 0\n\t" \
  "s_cmp_eq_u32 s86, s78\n\t" \
  "s_addc_u32 s93, s93, 0\n\t" \
  "s_cmp_eq_u32 s88, s78\n\t" \
  "s_addc_u32 s93, s93, 0\n\t" \
  "s_mul_i32 s93, s93, 21\n\t" \
  "s_add_u32 s76, s76, 15\n\t" \
  "s_add_u32 s76, s76, s93\n\t" \
  "s_lshl2_add_u32 s78, s78, 16\n\t" \
  "s_mov_b64 exec, 1\n\t" \
  "s_sub_i32 s90, s76, s90\n\t" \
  "s_mul_i32 s90, s90, 11900\n\t" \
  "s_ashr_i32 s90, s90, 16\n\t" \
  "s_sub_u32 s90, s78, s90\n\t" \
  "s_sub_u32 s90, s90, %[base]\n\t" \
  "v_mov_b32 v164, s90\n\t" \
  "ds_write_b32 v168, v164 offset:4108\n\t" \
  "s_mov_b64 exec, -1\n\t" \
  "s_waitcnt vmcnt(1)\n\t" \
  ".Lb2_%=:\n\t" \
  "s_setprio 0\n\t" \
  "s_bitcmp0_b64 %[am], %[step]\n\t" \
  "s_cbranch_scc1 .La1_%=\n\t" \
  "buffer_store_dwordx4 v[224:227], v228, %[ra], 0 offen sc1\n\t" \
  ".La1_%=:\n\t" \
  "buffer_load_dwordx4 v[192:195], v239, %[rt], %[st] offen offset:0 sc1\n\t" \
  "buffer_load_dwordx4 v[196:199], v239, %[rt], %[st] offen offset:1024 sc1\n\t" \
  "v_mfma_f32_16x16x32_bf16 v[128:131], v[0:3], v[176:179], 0\n\t" \
  "v_mfma_f32_16x16x32_bf16 v[136:139], v[8:11], v[176:179], 0\n\t" \
  "ds_read2st64_b32 v[144:145], v231 offset0:0 offset1:8\n\t" \
  "ds_read2st64_b32 v[146:147], v231 offset0:16 offset1:24\n\t" \
  "ds_read2st64_b32 v[148:149], v231 offset0:32 offset1:40\n\t" \
  "ds_read2st64_b32 v[150:151], v231 offset0:48 offset1:56\n\t" \
  "buffer_load_dwordx4 v[200:203], v239, %[rt], %[st4] offen offset:0 sc1\n\t" \
  "buffer_load_dwordx4 v[204:207], v239, %[rt], %[st4] offen offset:1024 sc1\n\t" \
  "v_mfma_f32_16x16x32_bf16 v[132:135], v[4:7], v[180:183], 0\n\t" \
  "v_mfma_f32_16x16x32_bf16 v[140:143], v[12:15], v[180:183], 0\n\t" \
  "s_waitcnt lgkmcnt(0)\n\t" \
  "v_add_f32 v144, v144, v145\n\t" \
  "v_add_f32 v146, v146, v147\n\t" \
  "v_add_f32 v148, v148, v149\n\t" \
  "v_add_f32 v150, v150, v151\n\t" \
  "v_add_f32 v144, v144, v146\n\t" \
  "v_add_f32 v148, v148, v150\n\t" \
  "buffer_load_dwordx4 v[208:211], v240, %[rt], %[st] offen offset:0 sc1\n\t" \
  "buffer_load_dwordx4 v[212:215], v240, %[rt], %[st] offen offset:1024 sc1\n\t" \
  "v_mfma_f32_16x16x32_bf16 v[128:131], v[16:19], v[184:187], v[128:131]\n\t" \
  "v_mfma_f32_16x16x32_bf16 v[136:139], v[24:27], v[184:187], v[136:139]\n\t" \
  "v_add_f32 v144, v144, v148\n\t" \
  "v_cvt_pk_bf16_f32 v144, v144, v144\n\t" \
  "ds_write_b16 v232, v144\n\t" \
  "ds_read_b128 v[224:227], v229\n\t" \
  "v_add_u32 v228, %[ainc], v228\n\t" \
  "v_add_u32 v229, %[rinc], v229\n\t" \
  "v_add_u32 v232, 1024, v232\n\t" \
  "v_xor_b32 v231, 0x4000, v231\n\t" \
  "buffer_load_dwordx4 v[216:219], v240, %[rt], %[st4] offen offset:0 sc1\n\t" \
  "buffer_load_dwordx4 v[220:223], v240, %[rt], %[st4] offen offset:1024 sc1\n\t" \
  "v_mfma_f32_16x16x32_bf16 v[132:135], v[20:23], v[188:191], v[132:135]\n\t" \
  "v_mfma_f32_16x16x32_bf16 v[140:143], v[28:31], v[188:191], v[140:143]\n\t" \
  "buffer_load_dwordx4 v[176:179], v241, %[rt], %[st] offen offset:0 sc1\n\t" \
  "s_waitcnt vmcnt(8)\n\t" \
  "v_mfma_f32_16x16x32_bf16 v[128:131], v[32:35], v[192:195], v[128:131]\n\t" \
  "v_mfma_f32_16x16x32_bf16 v[136:139], v[40:43], v[192:195], v[136:139]\n\t" \
  "buffer_load_dwordx4 v[180:183], v241, %[rt], %[st] offen offset:1024 sc1\n\t" \
  "s_waitcnt vmcnt(8)\n\t" \
  "v_mfma_f32_16x16x32_bf16 v[132:135], v[36:39], v[196:199], v[132:135]\n\t" \
  "v_mfma_f32_16x16x32_bf16 v[140:143], v[44:47], v[196:199], v[140:143]\n\t" \
  "buffer_load_dwordx4 v[184:187], v241, %[rt], %[st4] offen offset:0 sc1\n\t" \
  "s_waitcnt vmcnt(8)\n\t" \
  "v_mfma_f32_16x16x32_bf16 v[128:131], v[48:51], v[200:203], v[128:131]\n\t" \
  "v_mfma_f32_16x16x32_bf16 v[136:139], v[56:59], v[200:203], v[136:139]\n\t" \
  "ds_read_b128 v[160:163], v168 offset:4096\n\t" \
  "buffer_load_dwordx4 v[188:191], v241, %[rt], %[st4] offen offset:1024 sc1\n\t" \
  "s_waitcnt vmcnt(8)\n\t" \
  "v_mfma_f32_16x16x32_bf16 v[132:135], v[52:55], v[204:207], v[132:135]\n\t" \
  "v_mfma_f32_16x16x32_bf16 v[140:143], v[60:63], v[204:207], v[140:143]\n\t" \
  "s_waitcnt lgkmcnt(0)\n\t" \
  "v_readfirstlane_b32 s90, v161\n\t" \
  "v_readfirstlane_b32 s91, v163\n\t" \
  "v_readfirstlane_b32 s94, v160\n\t" \
  "s_sub_u32 s93, s94, %[sm]\n\t" \
  "s_mov_b32 %[sm], s94\n\t" \
  "s_lshr_b32 s93, s93, 3\n\t" \
  "s_sub_i32 s93, s90, s93\n\t" \
  "s_mul_i32 s93, s93, %[wm]\n\t" \
  "s_ashr_i32 s93, s93, 2\n\t" \
  "s_min_u32 s93, s93, %[gm]\n\t" \
  "s_sub_i32 s90, s90, s93\n\t" \
  "s_add_u32 s93, s90, %[rl]\n\t" \
  "s_add_u32 s93, s93, %[base]\n\t" \
  "s_add_u32 %[E], s93, %[lk]\n\t" \
  "s_sub_i32 s91, s91, s90\n\t" \
  "s_sub_i32 s91, s91, %[rl]\n\t" \
  "s_min_i32 s93, s91, %[up]\n\t" \
  "s_max_i32 s93, s93, %[dn]\n\t" \
  "s_cmp_gt_i32 s91, 60\n\t" \
  "s_cselect_b32 s93, 2, s93\n\t" \
  "s_add_i32 %[rl], %[rl], s93\n\t" \
  "v_mov_b32 v160, %[rl]\n\t" \
  "buffer_store_dword v160, v175, %[rs], 0 offen sc1\n\t" \
  "s_waitcnt vmcnt(8)\n\t" \
  "v_mfma_f32_16x16x32_bf16 v[128:131], v[64:67], v[208:211], v[128:131]\n\t" \
  "v_mfma_f32_16x16x32_bf16 v[136:139], v[72:75], v[208:211], v[136:139]\n\t" \
  "s_add_u32 %[step], %[step], 1\n\t" \
  "s_lshl_b32 %[sh], %[step], 15\n\t" \
  "s_lshr_b32 s93, %[step], 4\n\t" \
  "s_lshr_b32 s93, %[pm], s93\n\t" \
  "s_and_b32 s93, s93, 4\n\t" \
  "s_lshl_b32 s93, s93, 11\n\t" \
  "s_lshl2_add_u32 %[so], %[sh], s93\n\t" \
  "s_waitcnt vmcnt(7)\n\t" \
  "v_mfma_f32_16x16x32_bf16 v[132:135], v[68:71], v[212:215], v[132:135]\n\t" \
  "v_mfma_f32_16x16x32_bf16 v[140:143], v[76:79], v[212:215], v[140:143]\n\t" \
  "s_sub_i32 s93, %[step], 2\n\t" \
  "s_lshl_b32 %[st], s93, 19\n\t" \
  "s_lshl_b32 s94, s93, 11\n\t" \
  "s_xor_b32 s94, s94, %[s0]\n\t" \
  "s_and_b32 s94, s94, 0x2000\n\t" \
  "s_or_b32 %[st], %[st], s94\n\t" \
  "s_cmp_lt_i32 s93, 0\n\t" \
  "s_cselect_b32 %[st], 0x70000000, %[st]\n\t" \
  "s_add_u32 %[st4], %[st], 4096\n\t" \
  "s_waitcnt vmcnt(6)\n\t" \
  "v_mfma_f32_16x16x32_bf16 v[128:131], v[80:83], v[216:219], v[128:131]\n\t" \
  "v_mfma_f32_16x16x32_bf16 v[136:139], v[88:91], v[216:219], v[136:139]\n\t" \
  "s_waitcnt vmcnt(5)\n\t" \
  "v_mfma_f32_16x16x32_bf16 v[132:135], v[84:87], v[220:223], v[132:135]\n\t" \
  "v_mfma_f32_16x16x32_bf16 v[140:143], v[92:95], v[220:223], v[140:143]\n\t" \
  "s_cmp_eq_u32 %[step], 63\n\t" \
  "s_cbranch_scc1 .Llast_%=\n\t" \
  "s_movk_i32 s93, 600\n\t" \
  ".Lpacel_%=:\n\t" \
  "s_memrealtime s[90:91]\n\t" \
  "s_waitcnt lgkmcnt(0)\n\t" \
  "s_lshl_b32 s90, s90, 2\n\t" \
  "s_sub_i32 s91, %[E], s90\n\t" \
  "s_cmp_le_i32 s91, 0\n\t" \
  "s_cbranch_scc1 .Lgol_%=\n\t" \
  "s_sub_u32 s93, s93, 1\n\t" \
  "s_cmp_lg_u32 s93, 0\n\t" \
  "s_cbranch_scc1 .Lpacel_%=\n\t" \
  ".Lgol_%=:\n\t" \
  "buffer_load_dwordx2 v[166:167], v173, %[rs], 0 offen sc1\n\t" \
  "s_waitcnt vmcnt(5)\n\t" \
  "v_mfma_f32_16x16x32_bf16 v[128:131], v[96:99], v[176:179], v[128:131]\n\t" \
  "v_mfma_f32_16x16x32_bf16 v[136:139], v[104:107], v[176:179], v[136:139]\n\t" \
  "s_waitcnt vmcnt(4)\n\t" \
  "v_mfma_f32_16x16x32_bf16 v[132:135], v[100:103], v[180:183], v[132:135]\n\t" \
  "v_mfma_f32_16x16x32_bf16 v[140:143], v[108:111], v[180:183], v[140:143]\n\t" \
  "s_waitcnt vmcnt(3)\n\t" \
  "v_mfma_f32_16x16x32_bf16 v[128:131], v[112:115], v[184:187], v[128:131]\n\t" \
  "v_mfma_f32_16x16x32_bf16 v[136:139], v[120:123], v[184:187], v[136:139]\n\t" \
  "s_waitcnt vmcnt(2)\n\t" \
  "v_mfma_f32_16x16x32_bf16 v[132:135], v[116:119], v[188:191], v[132:135]\n\t" \
  "v_mfma_f32_16x16x32_bf16 v[140:143], v[124:127], v[188:191], v[140:143]\n\t" \
  "v_mov_b64 v[144:145], 0\n\t" \
  "v_mov_b64 v[146:147], 0\n\t" \
  "v_mov_b64 v[148:149], 0\n\t" \
  "v_mov_b64 v[150:151], 0\n\t" \
  "v_mov_b64 v[152:153], 0\n\t" \
  "v_mov_b64 v[154:155], 0\n\t" \
  "v_mov_b64 v[156:157], 0\n\t" \
  "v_mov_b64 v[158:159], 0\n\t" \
  "v_pk_add_f32 v[128:129], v[128:129], v[132:133]\n\t" \
  "v_pk_add_f32 v[130:131], v[130:131], v[134:135]\n\t" \
  "ds_write_b128 v230, v[128:131] offset:0\n\t" \
  "v_pk_add_f32 v[136:137], v[136:137], v[140:141]\n\t" \
  "v_pk_add_f32 v[138:139], v[138:139], v[142:143]\n\t" \
  "ds_write_b128 v230, v[136:139] offset:1024\n\t" \
  "v_xor_b32 v230, 0x4000, v230\n\t" \
  "s_cmp_lg_u32 %[step], 63\n\t" \
  "s_cbranch_scc1 .Lstep_%=\n\t" \
  ".Lfc_%=:\n\t" \
  "s_waitcnt vmcnt(0)\n\t" \
  "v_cmp_eq_u32 vcc, -1, v166\n\t" \
  "s_cbranch_vccnz .Lfw_%=\n\t" \
  ".Lfo_%=:\n\t" \
  "v_add_u32 v242, 16384, v242\n\t" \
  "buffer_load_dwordx4 v[176:179], v238, %[rt], %[st] offen offset:0 sc1\n\t" \
  "buffer_load_dwordx4 v[180:183], v238, %[rt], %[st] offen offset:1024 sc1\n\t" \
  "buffer_load_dwordx4 v[184:187], v238, %[rt], %[st4] offen offset:0 sc1\n\t" \
  "buffer_load_dwordx4 v[188:191], v238, %[rt], %[st4] offen offset:1024 sc1\n\t" \
  "buffer_load_dwordx4 v[192:195], v239, %[rt], %[st] offen offset:0 sc1\n\t" \
  "buffer_load_dwordx4 v[196:199], v239, %[rt], %[st] offen offset:1024 sc1\n\t" \
  "buffer_load_dwordx4 v[200:203], v239, %[rt], %[st4] offen offset:0 sc1\n\t" \
  "buffer_load_dwordx4 v[204:207], v239, %[rt], %[st4] offen offset:1024 sc1\n\t" \
  "v_subrev_u32 v230, 98304, v230\n\t" \
  ".Lt_%=:\n\t" \
  "s_waitcnt vmcnt(7)\n\t" \
  "v_mfma_f32_16x16x32_bf16 v[128:131], v[0:3], v[176:179], 0\n\t" \
  "v_mfma_f32_16x16x32_bf16 v[136:139], v[8:11], v[176:179], 0\n\t" \
  "buffer_load_dwordx4 v[176:179], v240, %[rt], %[st] offen offset:0 sc1\n\t" \
  "s_waitcnt vmcnt(7)\n\t" \
  "v_mfma_f32_16x16x32_bf16 v[132:135], v[4:7], v[180:183], 0\n\t" \
  "v_mfma_f32_16x16x32_bf16 v[140:143], v[12:15], v[180:183], 0\n\t" \
  "buffer_load_dwordx4 v[180:183], v240, %[rt], %[st] offen offset:1024 sc1\n\t" \
  "s_waitcnt vmcnt(7)\n\t" \
  "v_mfma_f32_16x16x32_bf16 v[128:131], v[16:19], v[184:187], v[128:131]\n\t" \
  "v_mfma_f32_16x16x32_bf16 v[136:139], v[24:27], v[184:187], v[136:139]\n\t" \
  "buffer_load_dwordx4 v[184:187], v240, %[rt], %[st4] offen offset:0 sc1\n\t" \
  "s_waitcnt vmcnt(7)\n\t" \
  "v_mfma_f32_16x16x32_bf16 v[132:135], v[20:23], v[188:191], v[132:135]\n\t" \
  "v_mfma_f32_16x16x32_bf16 v[140:143], v[28:31], v[188:191], v[140:143]\n\t" \
  "buffer_load_dwordx4 v[188:191], v240, %[rt], %[st4] offen offset:1024 sc1\n\t" \
  "buffer_load_dword v166, v242, %[rs], 0 offen sc1\n\t" \
  "s_waitcnt vmcnt(8)\n\t" \
  "v_mfma_f32_16x16x32_bf16 v[128:131], v[32:35], v[192:195], v[128:131]\n\t" \
  "v_mfma_f32_16x16x32_bf16 v[136:139], v[40:43], v[192:195], v[136:139]\n\t" \
  "buffer_load_dwordx4 v[192:195], v241, %[rt], %[st] offen offset:0 sc1\n\t" \
  "s_waitcnt vmcnt(8)\n\t" \
  "v_mfma_f32_16x16x32_bf16 v[132:135], v[36:39], v[196:199], v[132:135]\n\t" \
  "v_mfma_f32_16x16x32_bf16 v[140:143], v[44:47], v[196:199], v[140:143]\n\t" \
  "buffer_load_dwordx4 v[196:199], v241, %[rt], %[st] offen offset:1024 sc1\n\t" \
  "s_waitcnt vmcnt(8)\n\t" \
  "v_mfma_f32_16x16x32_bf16 v[128:131], v[48:51], v[200:203], v[128:131]\n\t" \
  "v_mfma_f32_16x16x32_bf16 v[136:139], v[56:59], v[200:203], v[136:139]\n\t" \
  "buffer_load_dwordx4 v[200:203], v241, %[rt], %[st4] offen offset:0 sc1\n\t" \
  "s_waitcnt vmcnt(8)\n\t" \
  "v_mfma_f32_16x16x32_bf16 v[132:135], v[52:55], v[204:207], v[132:135]\n\t" \
  "v_mfma_f32_16x16x32_bf16 v[140:143], v[60:63], v[204:207], v[140:143]\n\t" \
  "buffer_load_dwordx4 v[204:207], v241, %[rt], %[st4] offen offset:1024 sc1\n\t" \
  "s_waitcnt vmcnt(8)\n\t" \
  "v_mfma_f32_16x16x32_bf16 v[128:131], v[64:67], v[176:179], v[128:131]\n\t" \
  "v_mfma_f32_16x16x32_bf16 v[136:139], v[72:75], v[176:179], v[136:139]\n\t" \
  "s_waitcnt vmcnt(4)\n\t" \
  ".Ltc_%=:\n\t" \
  "v_cmp_eq_u32 vcc, -1, v166\n\t" \
  "s_cbranch_vccnz .Ltw_%=\n\t" \
  ".Lto_%=:\n\t" \
  "s_cmp_eq_u32 %[step], 65\n\t" \
  "s_cbranch_scc1 .Ltl_%=\n\t" \
  "s_add_u32 %[step], %[step], 1\n\t" \
  "s_sub_i32 s93, %[step], 2\n\t" \
  "s_lshl_b32 %[st], s93, 19\n\t" \
  "s_lshl_b32 s94, s93, 11\n\t" \
  "s_xor_b32 s94, s94, %[s0]\n\t" \
  "s_and_b32 s94, s94, 0x2000\n\t" \
  "s_or_b32 %[st], %[st], s94\n\t" \
  "s_cmp_lt_i32 s93, 0\n\t" \
  "s_cselect_b32 %[st], 0x70000000, %[st]\n\t" \
  "s_add_u32 %[st4], %[st], 4096\n\t" \
  "v_add_u32 v242, 16384, v242\n\t" \
  "s_cmp_eq_u32 %[step], 65\n\t" \
  "s_cbranch_scc0 .Ltn_%=\n\t" \
  "v_mov_b32 v242, %[fg]\n\t" \
  ".Ltn_%=:\n\t" \
  "buffer_load_dwordx4 v[176:179], v238, %[rt], %[st] offen offset:0 sc1\n\t" \
  "s_waitcnt vmcnt(7)\n\t" \
  "v_mfma_f32_16x16x32_bf16 v[132:135], v[68:71], v[180:183], v[132:135]\n\t" \
  "v_mfma_f32_16x16x32_bf16 v[140:143], v[76:79], v[180:183], v[140:143]\n\t" \
  "buffer_load_dwordx4 v[180:183], v238, %[rt], %[st] offen offset:1024 sc1\n\t" \
  "s_waitcnt vmcnt(7)\n\t" \
  "v_mfma_f32_16x16x32_bf16 v[128:131], v[80:83], v[184:187], v[128:131]\n\t" \
  "v_mfma_f32_16x16x32_bf16 v[136:139], v[88:91], v[184:187], v[136:139]\n\t" \
  "buffer_load_dwordx4 v[184:187], v238, %[rt], %[st4] offen offset:0 sc1\n\t" \
  "s_waitcnt vmcnt(7)\n\t" \
  "v_mfma_f32_16x16x32_bf16 v[132:135], v[84:87], v[188:191], v[132:135]\n\t" \
  "v_mfma_f32_16x16x32_bf16 v[140:143], v[92:95], v[188:191], v[140:143]\n\t" \
  "buffer_load_dwordx4 v[188:191], v238, %[rt], %[st4] offen offset:1024 sc1\n\t" \
  "s_waitcnt vmcnt(7)\n\t" \
  "v_mfma_f32_16x16x32_bf16 v[128:131], v[96:99], v[192:195], v[128:131]\n\t" \
  "v_mfma_f32_16x16x32_bf16 v[136:139], v[104:107], v[192:195], v[136:139]\n\t" \
  "buffer_load_dwordx4 v[192:195], v239, %[rt], %[st] offen offset:0 sc1\n\t" \
  "s_waitcnt vmcnt(7)\n\t" \
  "v_mfma_f32_16x16x32_bf16 v[132:135], v[100:103], v[196:199], v[132:135]\n\t" \
  "v_mfma_f32_16x16x32_bf16 v[140:143], v[108:111], v[196:199], v[140:143]\n\t" \
  "buffer_load_dwordx4 v[196:199], v239, %[rt], %[st] offen offset:1024 sc1\n\t" \
  "s_waitcnt vmcnt(7)\n\t" \
  "v_mfma_f32_16x16x32_bf16 v[128:131], v[112:115], v[200:203], v[128:131]\n\t" \
  "v_mfma_f32_16x16x32_bf16 v[136:139], v[120:123], v[200:203], v[136:139]\n\t" \
  "buffer_load_dwordx4 v[200:203], v239, %[rt], %[st4] offen offset:0 sc1\n\t" \
  "s_waitcnt vmcnt(7)\n\t" \
  "v_mfma_f32_16x16x32_bf16 v[132:135], v[116:119], v[204:207], v[132:135]\n\t" \
  "v_mfma_f32_16x16x32_bf16 v[140:143], v[124:127], v[204:207], v[140:143]\n\t" \
  "buffer_load_dwordx4 v[204:207], v239, %[rt], %[st4] offen offset:1024 sc1\n\t" \
  "s_nop 5\n\t" \
  "v_pk_add_f32 v[128:129], v[128:129], v[132:133]\n\t" \
  "v_pk_add_f32 v[130:131], v[130:131], v[134:135]\n\t" \
  "ds_write_b128 v230, v[128:131] offset:0\n\t" \
  "v_pk_add_f32 v[136:137], v[136:137], v[140:141]\n\t" \
  "v_pk_add_f32 v[138:139], v[138:139], v[142:143]\n\t" \
  "ds_write_b128 v230, v[136:139] offset:1024\n\t" \
  "v_add_u32 v230, 16384, v230\n\t" \
  "s_branch .Lt_%=\n\t" \
  ".Ltl_%=:\n\t" \
  "buffer_load_dwordx4 v[224:227], v243, %[rg], 0 offen sc1\n\t" \
  "s_waitcnt vmcnt(7)\n\t" \
  "v_mfma_f32_16x16x32_bf16 v[132:135], v[68:71], v[180:183], v[132:135]\n\t" \
  "v_mfma_f32_16x16x32_bf16 v[140:143], v[76:79], v[180:183], v[140:143]\n\t" \
  "s_waitcnt vmcnt(6)\n\t" \
  "v_mfma_f32_16x16x32_bf16 v[128:131], v[80:83], v[184:187], v[128:131]\n\t" \
  "v_mfma_f32_16x16x32_bf16 v[136:139], v[88:91], v[184:187], v[136:139]\n\t" \
  "s_waitcnt vmcnt(5)\n\t" \
  "v_mfma_f32_16x16x32_bf16 v[132:135], v[84:87], v[188:191], v[132:135]\n\t" \
  "v_mfma_f32_16x16x32_bf16 v[140:143], v[92:95], v[188:191], v[140:143]\n\t" \
  "s_waitcnt vmcnt(4)\n\t" \
  "v_mfma_f32_16x16x32_bf16 v[128:131], v[96:99], v[192:195], v[128:131]\n\t" \
  "v_mfma_f32_16x16x32_bf16 v[136:139], v[104:107], v[192:195], v[136:139]\n\t" \
  "s_waitcnt vmcnt(3)\n\t" \
  "v_mfma_f32_16x16x32_bf16 v[132:135], v[100:103], v[196:199], v[132:135]\n\t" \
  "v_mfma_f32_16x16x32_bf16 v[140:143], v[108:111], v[196:199], v[140:143]\n\t" \
  "s_waitcnt vmcnt(2)\n\t" \
  "v_mfma_f32_16x16x32_bf16 v[128:131], v[112:115], v[200:203], v[128:131]\n\t" \
  "v_mfma_f32_16x16x32_bf16 v[136:139], v[120:123], v[200:203], v[136:139]\n\t" \
  "s_waitcnt vmcnt(1)\n\t" \
  "v_mfma_f32_16x16x32_bf16 v[132:135], v[116:119], v[204:207], v[132:135]\n\t" \
  "v_mfma_f32_16x16x32_bf16 v[140:143], v[124:127], v[204:207], v[140:143]\n\t" \
  "s_nop 6\n\t" \
  "v_pk_add_f32 v[128:129], v[128:129], v[132:133]\n\t" \
  "v_pk_add_f32 v[130:131], v[130:131], v[134:135]\n\t" \
  "ds_write_b128 v230, v[128:131] offset:0\n\t" \
  "v_pk_add_f32 v[136:137], v[136:137], v[140:141]\n\t" \
  "v_pk_add_f32 v[138:139], v[138:139], v[142:143]\n\t" \
  "ds_write_b128 v230, v[136:139] offset:1024\n\t" \
  "s_waitcnt vmcnt(0) lgkmcnt(0)\n\t" \
  "s_branch .Lend_%=\n\t" \
  ".Ltw_%=:\n\t" \
  "s_add_u32 s92, s92, 1\n\t" \
  "s_sleep 2\n\t" \
  "s_and_b32 s93, s92, 63\n\t" \
  "s_cbranch_scc1 .Ltr_%=\n\t" \
  "buffer_load_dword v164, off, %[rs], %[sab] sc1\n\t" \
  "s_waitcnt vmcnt(0)\n\t" \
  "v_readfirstlane_b32 s93, v164\n\t" \
  "s_nop 3\n\t" \
  "s_cmp_eq_u32 s93, 1\n\t" \
  "s_cbranch_scc1 .Lto_%=\n\t" \
  "s_cmp_lt_u32 s92, 0x80000\n\t" \
  "s_cbranch_scc1 .Ltr_%=\n\t" \
  "v_mov_b32 v164, 1\n\t" \
  "buffer_store_dword v164, off, %[rs], %[sab] sc1\n\t" \
  "s_waitcnt vmcnt(0)\n\t" \
  "s_branch .Lto_%=\n\t" \
  ".Ltr_%=:\n\t" \
  "buffer_load_dword v166, v242, %[rs], 0 offen sc1\n\t" \
  "s_waitcnt vmcnt(0)\n\t" \
  "s_branch .Ltc_%=\n\t" \
  ".Llast_%=:\n\t" \
  "v_mov_b32 v173, v242\n\t" \
  "s_branch .Lgol_%=\n\t" \
  ".Lfw_%=:\n\t" \
  "s_add_u32 s92, s92, 1\n\t" \
  "s_sleep 2\n\t" \
  "s_and_b32 s93, s92, 63\n\t" \
  "s_cbranch_scc1 .Lfr_%=\n\t" \
  "buffer_load_dword v164, off, %[rs], %[sab] sc1\n\t" \
  "s_waitcnt vmcnt(0)\n\t" \
  "v_readfirstlane_b32 s93, v164\n\t" \
  "s_nop 3\n\t" \
  "s_cmp_eq_u32 s93, 1\n\t" \
  "s_cbranch_scc1 .Lfo_%=\n\t" \
  "s_cmp_lt_u32 s92, 0x80000\n\t" \
  "s_cbranch_scc1 .Lfr_%=\n\t" \
  "v_mov_b32 v164, 1\n\t" \
  "buffer_store_dword v164, off, %[rs], %[sab] sc1\n\t" \
  "s_waitcnt vmcnt(0)\n\t" \
  "s_branch .Lfo_%=\n\t" \
  ".Lfr_%=:\n\t" \
  "buffer_load_dwordx2 v[166:167], v173, %[rs], 0 offen sc1\n\t" \
  "s_branch .Lfc_%=\n\t" \
  ".Lb1_%=:\n\t" \
  "s_waitcnt vmcnt(0) lgkmcnt(0)\n\t" \
  "s_barrier\n\t" \
  "s_branch .Lb2_%=\n\t" \
  ".Lbl_%=:\n\t" \
  "s_bitset1_b64 %[ps], %[step]\n\t" \
  "s_cmp_lt_u32 s92, 3\n\t" \
  "s_cbranch_scc1 .Lbm_%=\n\t" \
  "s_sleep 12\n\t" \
  ".Lbm_%=:\n\t" \
  "s_add_u32 s92, s92, 1\n\t" \
  "s_sleep 2\n\t" \
  "s_and_b32 s93, s92, 63\n\t" \
  "s_cbranch_scc1 .Lba_%=\n\t" \
  "buffer_load_dword v164, off, %[rs], %[sab] sc1\n\t" \
  "s_waitcnt vmcnt(0)\n\t" \
  "v_readfirstlane_b32 s93, v164\n\t" \
  "s_nop 3\n\t" \
  "s_cmp_eq_u32 s93, 1\n\t" \
  "s_cbranch_scc1 .Lgo_%=\n\t" \
  "s_cmp_lt_u32 s92, 0x80000\n\t" \
  "s_cbranch_scc1 .Lba_%=\n\t" \
  "v_mov_b32 v164, 1\n\t" \
  "buffer_store_dword v164, off, %[rs], %[sab] sc1\n\t" \
  "s_waitcnt vmcnt(0)\n\t" \
  "s_branch .Lgo_%=\n\t" \
  ".Lba_%=:\n\t" \
  "buffer_load_dwordx2 v[166:167], v173, %[rs], 0 offen sc1\n\t" \
  "s_branch .Lcal_%=\n\t" \
  ".Lslow_%=:\n\t" \
  "s_mov_b64 s[94:95], 0\n\t" \
  "v_cmp_eq_u32 vcc, %[sent], v128\n\t" \
  "s_or_b64 s[94:95], s[94:95], vcc\n\t" \
  "v_cmp_eq_u32 vcc, %[sent], v129\n\t" \
  "s_or_b64 s[94:95], s[94:95], vcc\n\t" \
  "v_cmp_eq_u32 vcc, %[sent], v130\n\t" \
  "s_or_b64 s[94:95], s[94:95], vcc\n\t" \
  "v_cmp_eq_u32 vcc, %[sent], v131\n\t" \
  "s_or_b64 s[94:95], s[94:95], vcc\n\t" \
  "v_cmp_eq_u32 vcc, %[sent], v132\n\t" \
  "s_or_b64 s[94:95], s[94:95], vcc\n\t" \
  "v_cmp_eq_u32 vcc, %[sent], v133\n\t" \
  "s_or_b64 s[94:95], s[94:95], vcc\n\t" \
  "v_cmp_eq_u32 vcc, %[sent], v134\n\t" \
  "s_or_b64 s[94:95], s[94:95], vcc\n\t" \
  "v_cmp_eq_u32 vcc, %[sent], v135\n\t" \
  "s_or_b64 s[94:95], s[94:95], vcc\n\t" \
  "v_cmp_eq_u32 vcc, %[sent], v136\n\t" \
  "s_or_b64 s[94:95], s[94:95], vcc\n\t" \
  "v_cmp_eq_u32 vcc, %[sent], v137\n\t" \
  "s_or_b64 s[94:95], s[94:95], vcc\n\t" \
  "v_cmp_eq_u32 vcc, %[sent], v138\n\t" \
  "s_or_b64 s[94:95], s[94:95], vcc\n\t" \
  "v_cmp_eq_u32 vcc, %[sent], v139\n\t" \
  "s_or_b64 s[94:95], s[94:95], vcc\n\t" \
  "v_cmp_eq_u32 vcc, %[sent], v140\n\t" \
  "s_or_b64 s[94:95], s[94:95], vcc\n\t" \
  "v_cmp_eq_u32 vcc, %[sent], v141\n\t" \
  "s_or_b64 s[94:95], s[94:95], vcc\n\t" \
  "v_cmp_eq_u32 vcc, %[sent], v142\n\t" \
  "s_or_b64 s[94:95], s[94:95], vcc\n\t" \
  "v_cmp_eq_u32 vcc, %[sent], v143\n\t" \
  "s_or_b64 s[94:95], s[94:95], vcc\n\t" \
  "s_cbranch_scc0 .Lok_%=\n\t" \
  "s_bitset1_b64 %[fm], %[step]\n\t" \
  "s_add_u32 s92, s92, 1\n\t" \
  "s_sleep 1\n\t" \
  "s_and_b32 s93, s92, 63\n\t" \
  "s_cbranch_scc1 .Lre_%=\n\t" \
  "buffer_load_dword v164, off, %[rs], %[sab] sc1\n\t" \
  "s_waitcnt vmcnt(0)\n\t" \
  "v_readfirstlane_b32 s93, v164\n\t" \
  "s_nop 3\n\t" \
  "s_cmp_eq_u32 s93, 1\n\t" \
  "s_cbranch_scc1 .Lok_%=\n\t" \
  "s_cmp_lt_u32 s92, 0x80000\n\t" \
  "s_cbranch_scc1 .Lre_%=\n\t" \
  "v_mov_b32 v164, 1\n\t" \
  "buffer_store_dword v164, off, %[rs], %[sab] sc1\n\t" \
  "s_waitcnt vmcnt(0)\n\t" \
  "s_branch .Lok_%=\n\t" \
  ".Lre_%=:\n\t" \
  "buffer_load_dwordx4 v[128:131], v234, %[rs], %[so] offen sc1\n\t" \
  "s_nop 15\n\t" \
  "s_nop 1\n\t" \
  "buffer_load_dwordx4 v[132:135], v235, %[rs], %[so] offen sc1\n\t" \
  "s_nop 15\n\t" \
  "s_nop 1\n\t" \
  "buffer_load_dwordx4 v[136:139], v236, %[rs], %[so] offen sc1\n\t" \
  "s_nop 15\n\t" \
  "s_nop 1\n\t" \
  "buffer_load_dwordx4 v[140:143], v237, %[rs], %[so] offen sc1\n\t" \
  "v_mov_b64 v[144:145], 0\n\t" \
  "v_mov_b64 v[146:147], 0\n\t" \
  "v_mov_b64 v[148:149], 0\n\t" \
  "v_mov_b64 v[150:151], 0\n\t" \
  "v_mov_b64 v[152:153], 0\n\t" \
  "v_mov_b64 v[154:155], 0\n\t" \
  "v_mov_b64 v[156:157], 0\n\t" \
  "v_mov_b64 v[158:159], 0\n\t" \
  "s_waitcnt vmcnt(0)\n\t" \
  "s_branch .Lw_%=\n\t" \
  ".Lend_%=:\n\t"


__device__ __forceinline__ int look_prog(__amdgpu_buffer_rsrc_t rs, int off) {
  asm volatile("" ::: "memory");
  const int v = (int)ld_flag(rs, off);
  return min(__builtin_amdgcn_readlane(v, 0), __builtin_amdgcn_readlane(v, 1));
}
__device__ __forceinline__ int wait_prog(__amdgpu_buffer_rsrc_t rs, int off, int need, gu32* abortw) {
  int v;
  for (unsigned spins = 0;;) {
    v = look_prog(rs, off);
    if (v >= need) break;
    __builtin_amdgcn_s_sleep(4);
    if (spin_fail(spins, abortw)) break;
  }
  asm volatile("" ::: "memory");
  return v;
}
#define CLB_S "s90", "s91", "s92", "s93", "s94", "s95", "vcc", "scc", "memory"
#define W_IN "{v[0:31]}"(W[0]), "{v[32:63]}"(W[1]), "{v[64:95]}"(W[2]), "{v[96:127]}"(W[3]), "{v[128:159]}"(W[4]), "{v[160:191]}"(W[5])

#define LEAD0 396
#define LEAD_N0 4
#define LEAD_SH 1
#define LEAD_X0 8
#define LEAD_U0 24
#define LEAD_D0 0
#define T0_BACK 1000
#define QBJ 3
constexpr int L_MEM = 0, L_KT = 16384, L_QX = 24576, L_KX = 26624, L_VX = 16384, L_MISC = 30720, L_WL = 32768;

#define QK_STEP(TXT)                                                                                                        \
  asm volatile(TXT                                                                                                          \
      : [tout] "=&s"(tout), [spins] "=&s"(spins), [tb1] "=&s"(tb1), [tgt] "=&s"(tgt), "+{v248}"(aL16), "+{v249}"(aMW), "+{v250}"(aWL),                            \
        "+{v251}"(aIDX), "+{v252}"(aQW), "+{v[232:235]}"(QF)                                                                 \
      : W_IN, [rs] "s"(rs4), [som] "s"(o_mem(pm, s) + WV * 1024), [som2] "s"(o_mem(pm, s) + WV * 1024 + 16384), [sent] "s"(DSENT), [sab] "s"(SY_ABORT), \
        [lead] "s"(lead), [step] "s"(s), [sbc] "s"(SY_BA + side(pm, 1) + h * 128), [nh] "s"(0xFF00FF00FF00FF00ull), [one] "s"(0x3F803F803F803F80ull), [up] "s"(0xFFFFFFFF00000000ull), [negc] "s"(-cexp),      \
        [c] "s"(cexp), [so] "s"(o_o(pm, s, h) + WV * 512), [sk] "s"(o_v(pm, s, h) + 2048 + (WV & 3) * 512),                    \
        [sv] "s"(o_v(pm, s, h) + WV * 512), [rt] "s"(rt4), [svt] "s"(t_off(p, T_V + kseg(s, h)) + WV * 2048)                  \
      : CLB8(192, 193, 194, 195, 196, 197, 198, 199), CLB8(200, 201, 202, 203, 204, 205, 206, 207),                          \
        CLB8(208, 209, 210, 211, 212, 213, 214, 215), CLB8(216, 217, 218, 219, 220, 221, 222, 223),                          \
        CLB8(224, 225, 226, 227, 228, 229, 230, 231), CLB8(236, 237, 238, 239, 240, 241, 242, 243), "v244", "v245", "v246", "v247", "v253", "s88", "s89", CLB_S)

template <int WV>
__device__ __forceinline__ void qk_wave(Args& p, int h, char* smem) {
  const int tid = threadIdx.x;
  unsigned pm = 0;
  int have = -1;
  u32x32 W[6];
  {
    int lane = tid & 63;
    asm volatile("" : "+v"(lane));
    const unsigned vo = (WV * 4 + (lane >> 4)) * (HD * 2) + 16 * ((lane & 15) ^ swzB((WV * 4 + (lane >> 4)) & 15));
    auto stage = [&](int j) {
#pragma unroll
      for (int mm = 0; mm < 2; ++mm) {
        const char* src = (const char*)((mm ? p.Wk : p.Wq) + (size_t)j * 128 * HD + h * DH);
        const unsigned dst = lds_off(smem) + (~j & 1) * 65536 + mm * 32768 + WV * 1024;
        glds2(src, 32 * HD * 2, vo, dst, true);
        if (TH_Q < 63) HOLD_V((h & 3) ^ 1, TH_Q);
        glds2(src + 64 * HD * 2, 32 * HD * 2, vo, dst + 16384, true);
        if (TH_Q < 63) HOLD_V((h & 3) ^ 1, TH_Q);
      }
    };
    bf16x8 keep[4][4];
    int f0 = -1;
    auto place = [&](int g) {
#pragma unroll
      for (int i = 0; i < 4; ++i)
        *reinterpret_cast<bf16x8*>(smem + (g == 0 ? 65536 : g == 1 ? 32768 : g == 2 ? 131072 : 98304) + WV * 4096 + i * 1024 + lane * 16) = keep[g][i];
    };
    stage(0); stage(1);
    Boot bt;
#pragma unroll
    for (int j = 0; j < 8; ++j) {
      if (j < 7) WAIT_V(8); else WAIT_V(0);
      if (WV == 7 && j == QBJ + 1) pm = boot_done(p.ws, bt);
      if ((WV == 4 || WV == 5) && j == 7) {
        asm volatile("" : "+v"(f0));
        have = min(__builtin_amdgcn_readlane(f0, 0), __builtin_amdgcn_readlane(f0, 1));
      }
      LBAR();
#pragma unroll
      for (int k = 0; k < 8; ++k) {
        const int P = (QK_MAP[WV] >> (4 * (2 * j + (k >> 2)))) & 15;
        const bf16x8 fr = ld_bfrag(smem + (~j & 1) * 65536 + (WV >> 2) * 32768 + (k >> 2) * 16384, k & 1, 2 * (WV & 3) + ((k >> 1) & 1), lane);
        const int f = P * 4 + (k & 3);
        if (P < 12) put4(W[f >> 3], f & 7, fr);
        else keep[P - 12][k & 3] = fr;
      }
      if (j == (WV >> 1)) place(2);
      LBAR();
      if (WV == 7 && j == QBJ) boot_ask(p.ws, bt);
      if (j + 2 < 8) stage(j + 2);
      if (j == 6) {
        if (WV == 4 || WV == 5)
          asm volatile("buffer_load_dword %0, %1, %2, 0 offen sc1" : "=&v"(f0) : "v"(fkq(h, (lane & 1) * 2)), "s"(raw_rsrc(p.ws + WS_SY, SY_TOTAL)) : "memory");
        place(0); place(3);
      }
    }
    place(1);
    if (WV == 7 && lane < 8) lds_at(smem + L_MISC)[lane] = lane == 7 ? pm : (lane == 1 ? now_q() - T0_BACK : 0);
    if (WV == 6) lds_at(smem + 28672)[lane] = ~0u;
    LBAR();
    pm = uni(lds_at(smem + L_MISC)[7]);
    p.s0 = pm >> 31;
    if (WV == 4 || WV == 5) {
      if (have < 0) have = wait_prog(mkrsrc(p.ws + WS_SY, SY_TOTAL), fkq(h, (lane & 1) * 2), 0, (gu32*)(p.ws + WS_SY + SY_ABORT));
      const char* src = p.ws + WS_T + t_off(p, kseg(0, h)) + (WV - 4) * 4096 + lane * 16;
#pragma unroll
      for (int i = 0; i < 4; ++i) glds16_sc1(src + i * 1024, smem + L_KT + (WV - 4) * 4096 + i * 1024);
    }
  }
  const __amdgpu_buffer_rsrc_t rsy = mkrsrc(p.ws + WS_SY, SY_TOTAL);
  const u32x4 rs4 = raw_rsrc(p.ws + WS_SY, SY_TOTAL);
  const u32x4 rt4 = raw_rsrc(p.ws + WS_T, T_BYTES);
  gu32* abortw = (gu32*)(p.ws + WS_SY + SY_ABORT);
  const float cexp = 0.08838834764831845f * 1.4426950408889634f;
  constexpr int m = WV >> 2, b = WV & 3;
  int lead = LEAD0;
  unsigned tb1 = 0;
  unsigned long long fm = 0, ps = 0; (void)fm; (void)ps;
  for (int s = 0; s < NSEG; ++s) {
    int lane = tid & 63;
    asm volatile("" : "+v"(lane));
    const int r = lane & 15, q = lane >> 4;
    unsigned aL16 = lane * 16, aMW = aL16 + WV * 1024, aWL = aL16 + 98304 + WV * 4096;
    unsigned aIDX = (lane & 2) ? 0xEEEE : 0x4444;
    unsigned aQW = (32 * b + r) * 16 + q * 4 + (m ? L_KX : L_QX);
    u32x4 QF;
    asm volatile("" : "={v[232:235]}"(QF));
    if (WV < 4) {
      QF[0] = L_QX + (q * 8 + (r >> 2)) * 16 + (r & 1) * 8;
      QF[1] = ((2 * (q & 1) + (q >> 1)) * 16 + r) * 4;
      QF[2] = ((r >> 2) & 1) * 65536 + ((r >> 1) & 1) * 64 + (r >> 3) * 32 + (r & 1) * 16 + (q >> 1) * 256 + (q & 1) * 8;
    } else QF[3] = r * 16 + q * 4;
    unsigned tout, spins, tgt;
    if (WV == 0) QK_STEP(QK_ASM_0); else if (WV == 1) QK_STEP(QK_ASM_1); else if (WV == 2) QK_STEP(QK_ASM_2);
    else if (WV == 3) QK_STEP(QK_ASM_3); else if (WV == 4) QK_STEP(QK_ASM_4); else if (WV == 5) QK_STEP(QK_ASM_5);
    else if (WV == 6) QK_STEP(QK_ASM_6); else QK_STEP(QK_ASM_7);
    tout = __builtin_amdgcn_readfirstlane(tout);
    tb1 = __builtin_amdgcn_readfirstlane(tb1);
    lane = tid & 63;
    asm volatile("" : "+v"(lane));
    if (WV < 4) {
      lds_at(smem + L_MISC + 8)[WV] = s + 1;
      if (WV == 3) { lds_at(smem + L_MISC)[6] = time_of(tout); lds_at(smem + 28672)[2] = s; }
    }
    if (WV == 6) {
      if (s + 1 < NSEG) watch(rsy, s, lane, now_q(), (const unsigned*)(smem + L_MISC), abortw, side(pm, 1));
    } else if (WV >= 4 && s + 1 < NSEG) {
      unsigned t;
      do {
        __builtin_amdgcn_s_sleep(1);
        t = lds_at(smem + L_MISC + 8)[lane & 3];
      } while (!__all(t == (unsigned)(s + 1)));
      if (WV <= 5) {
        if (s + 2 < NSEG) {
          if (have < s + 1) have = wait_prog(rsy, fkq(h, (lane & 1) * 2), s + 1, abortw);
          const char* src = p.ws + WS_T + t_off(p, kseg(s + 1, h)) + (WV - 4) * 4096 + lane * 16;
#pragma unroll
          for (int i = 0; i < 4; ++i) glds16_sc1(src + i * 1024, smem + L_KT + (WV - 4) * 4096 + i * 1024);
          if (have < s + 2) have = look_prog(rsy, fkq(h, (lane & 1) * 2));
        }
      } else {
        do __builtin_amdgcn_s_sleep(2); while (__builtin_amdgcn_readfirstlane(lds_at(smem + 28672)[2]) != (unsigned)s);
        const int x = (int)(__builtin_amdgcn_readfirstlane(lds_at(smem + L_MISC)[6]) - tb1);
        lead = __builtin_amdgcn_readfirstlane(track(lead, x + (s < LEAD_N0 ? LEAD_X0 : 0), s < LEAD_N0 ? LEAD_U0 : LEAD_UP, s < LEAD_N0 ? LEAD_D0 : (s < 3 * LEAD_N0 ? min(max(LEAD_DN, (lead - x) >> LEAD_SH), 16) : LEAD_DN), 60));
      }
    }
  }
}

__device__ __forceinline__ void qk_role(Args& p, int h, char* smem) {
  switch (__builtin_amdgcn_readfirstlane(threadIdx.x >> 6)) {
    case 0: qk_wave<0>(p, h, smem); break;
    case 1: qk_wave<1>(p, h, smem); break;
    case 2: qk_wave<2>(p, h, smem); break;
    case 3: qk_wave<3>(p, h, smem); break;
    case 4: qk_wave<4>(p, h, smem); break;
    case 5: qk_wave<5>(p, h, smem); break;
    case 6: qk_wave<6>(p, h, smem); break;
    default: qk_wave<7>(p, h, smem); break;
  }
}

#define K_DUE 50
#define K_DUE2 500
#define T_SL 2
#define O_DUE 400

constexpr int V_VM = 16384, V_T = 32768, V_KI = 131072;
#define TK_CALL(sg, od_) \
  asm volatile(TK_ASM \
            : \
            : "{v200}"(lane * 16), "{v[208:215]}"(bias), "{v[216:219]}"(ta), \
              [rs] "s"(rs4), [rt] "s"(raw_rsrc(p.ws + WS_T, T_BYTES)), [sent] "s"(DSENT), [sab] "s"(SY_ABORT), [sk] "s"(o_v(pm, sg, h) + 2048), \
              [tl] "s"(V_T + (sg & 3) * 24576), [vp] "s"((sg & 1) * 2048), [tq] "s"((w & 1) * 4096), [tv] "s"((w & 2) * 2048), \
              [so] "s"(t_off_o(p, T_O + (sg * 2 + (w & 1)) * 16 + (h >> 1)) + (h & 1) * 4096 + (w & 2) * 1024), \
              [up] "s"(0xFFFFFFFF00000000ull), [c] "s"(1.4426950408889634f), [negc] "s"(-1.4426950408889634f), \
              [sc] "s"((unsigned long long)__float_as_uint(0.08838834764831845f)), [od] "s"(od_) \
            : CLB32(128), CLB32(160), CLB8(192, 193, 194, 195, 196, 197, 198, 199), CLB8(224, 225, 226, 227, 228, 229, 230, 231), \
              "v205", "v206", "v207", "s88", "s89", CLB_S)
__device__ __forceinline__ void vu_role(Args& p, int h, char* smem) {
  const int tid = threadIdx.x, lane = tid & 63;
  const int w = __builtin_amdgcn_readfirstlane(tid >> 6);
  u32x32 W[4];
  {
    const unsigned vo = (tid >> 4) * (HD * 2) + 16 * ((lane & 15) ^ swzB((tid >> 4) & 15));
    auto stage = [&](int j) {
      const char* src = (const char*)(p.Wv + (size_t)j * 128 * HD + h * DH);
      const unsigned dst = lds_off(smem) + L_WL + (j & 3) * 32768 + w * 1024;
      glds2(src, 32 * HD * 2, vo, dst, true);
      glds2(src + 64 * HD * 2, 32 * HD * 2, vo, dst + 16384, true);
    };
    Boot bt;
#pragma unroll
    for (int u = 0; u < 2; ++u) {
#pragma unroll
      for (int j = 0; j < 4; ++j) stage(4 * u + j);
      if (u && w == 7) boot_ask(p.ws, bt);
      WAIT_V(0);
      if (u && w == 7) {
        const unsigned m = boot_done(p.ws, bt);
        if (lane < 8) lds_at(smem + L_MISC)[lane] = lane == 7 ? m : (lane == 1 ? now_q() : 0);
      }
      LBAR();
#pragma unroll
      for (int k = 0; k < 16; ++k) put4(W[2 * u + (k >> 3)], k & 7, ld_bfrag(smem + L_WL + ((k >> 1) ^ w) * 16384, k & 1, w, lane));
      LBAR();
    }
  }
  const __amdgpu_buffer_rsrc_t rsy = mkrsrc(p.ws + WS_SY, SY_TOTAL);
  const u32x4 rs4 = raw_rsrc(p.ws + WS_SY, SY_TOTAL);
  gu32* abortw = (gu32*)(p.ws + WS_SY + SY_ABORT);
  const unsigned pm = uni(lds_at(smem + L_MISC)[7]);
  p.s0 = pm >> 31;
  unsigned long long fm = 0; (void)fm;
  int have = -1;
  auto fetch = [&](int sg) {
    if (have < sg) have = wait_prog(rsy, fkq(h, w - 4), sg, abortw);
    const char* src = p.ws + WS_T + t_off(p, (w - 4) * T_Q + kseg(sg, h)) + lane * 16;
#pragma unroll
    for (int i = 0; i < 8; ++i) glds16_sc1(src + i * 1024, smem + V_T + (sg & 3) * 24576 + (w - 4) * 8192 + i * 1024);
  };
  const int fo = SY_FO + ((w & 3) * NH + h) * 128;
  typedef __attribute__((ext_vector_type(8))) float f32x8;
  f32x8 bias;
  typedef __attribute__((ext_vector_type(8))) unsigned u32x8;
  u32x8 ca;
#pragma unroll
  for (int i = 0; i < 8; ++i) ca[i] = lane * 16 + (i ^ w) * 1024;
  u32x4 ta;
  {
    const int r = lane & 15, q = lane >> 4;
#pragma unroll
    for (int j = 0; j < 8; ++j) bias[j] = (j >> 2) * 16 + 4 * q + (j & 3) <= 16 * (w & 1) + r ? INFINITY : -INFINITY;
    ta[0] = V_KI + w * 2048 + lane * 16;
    ta[1] = V_KI + w * 2048 + (q * 8 + (r >> 2)) * 16 + (r & 1) * 8;
    ta[2] = V_VM + (w & 2) * 512 + ((2 * (q & 1) + (q >> 1)) * 16 + r) * 4;
    ta[3] = 0;
  }
#pragma unroll 1
  for (int s = 0; s < NSEG; ++s) {
    unsigned spins, tgt;
    asm volatile(VV_ASM
        : [spins] "=&s"(spins), [tgt] "=&s"(tgt)
        : W4_IN, "{v200}"(lane * 16), "{v[232:239]}"(ca), "{v202}"((lane & 2) ? 0xEEEE : 0x4444), "{v203}"(lane * 4),
          "{v204}"(tid * 4 + V_VM + (s & 1) * 2048), [rs] "s"(rs4), [som] "s"(o_mem(pm, s) + w * 1024), [som2] "s"(o_mem(pm, s) + w * 1024 + 16384), [step] "s"(s),
          [sent] "s"(DSENT), [sab] "s"(SY_ABORT), [sv] "s"(o_v(pm, s, h) + w * 256)
        : CLB32(128), CLB32(160), CLB8(192, 193, 194, 195, 196, 197, 198, 199), CLB_S);
    if (w < 4 || s == NSEG - 1) {
      const int sg = s - (w < 4);
      if (w >= 4) {
        if (lane == 0) __builtin_amdgcn_raw_buffer_store_b32(1u, rsy, fo + 4 * NH * 128, 0, 16);
        pace_until(now_q() + K_DUE);
      }
      if (sg >= 0 && sg != NSEG - 2) {
        TK_CALL(sg, s < NSEG - 1 ? O_DUE : 0);
        if (sg >= NSEG - 3) {
          static_assert(TLAG == 2, "");
          WAIT_V(0);
          if (lane == 0) __builtin_amdgcn_raw_buffer_store_b32(1u, rsy, fo + (sg - NSEG + 3) * 4 * NH * 128, 0, 16);
        }
      }
    } else if (w == 7) {
      watch(rsy, s, lane, now_q(), (const unsigned*)(smem + L_MISC), abortw, side(pm, 1));
    } else if (w >= 4) {
      __builtin_amdgcn_s_sleep(T_SL);
      const int s1 = w == 5 && s < NSEG - 3 ? s : s + 1;
#pragma unroll 1
      for (int sg = (w == 5 ? s < NSEG - 2 : s == 0) ? s : s + 1; sg <= s1; ++sg) fetch(sg);
      if (have < s1 + 1 && s1 + 1 < NSEG) have = look_prog(rsy, fkq(h, w - 4));
      if (w == 5 && s + NPRE < NSEG) {
#pragma unroll
        for (int i = 0; i < 4; ++i) st_sc1(rsy, o_v(pm, s + NPRE, h) + i * 1024 + lane * 16, (u32x4){DSENT, DSENT, DSENT, DSENT});
      }
    }
    if (s == NSEG - 2 && w >= 4) {
      pace_until(tgt + K_DUE2);
      TK_CALL(s, 0);
    }
  }
}

#define RL_UP 6
#define RL_DN 4
#define RED_MIN 40
#define BA_LOOK 360
constexpr int R_SI = 32768, R_PART = 98304;

__device__ __forceinline__ void red_role(Args& p, int j, char* smem) {
  const int tid = threadIdx.x;
  int lane = tid & 63;
  const int w = __builtin_amdgcn_readfirstlane(tid >> 6);
  const int c = (j & 2) * 8 + (j >> 2), rh = j & 1;
  const int hg = (w + (j >> 2)) & 7;
  const __amdgpu_buffer_rsrc_t rsy = mkrsrc(p.ws + WS_SY, SY_TOTAL);
  const u32x4 rs4 = raw_rsrc(p.ws + WS_SY, SY_TOTAL);
  const __amdgpu_buffer_rsrc_t rt = mkrsrc(p.ws + WS_T, T_BYTES);
  gu32* abortw = (gu32*)(p.ws + WS_SY + SY_ABORT);
  u32x32 W[4];
  u32x2 tag = {0u, 0u};
  {
    const u16* Wo = p.Wo + (size_t)(4 * hg * DH) * DM + 32 * c;
    const int ro = (lane >> 2) * 64 + (lane & 3) * 8;
    Boot bt;
    if (c & 1) { start_sy(p.ws, p.mem0, (c & 30) + rh, smem); LBAR(); }
    load_blocks<16>(smem + 32768 + w * 16384,
        [&](int i, char* slot) {
          if (i == 12 && w == 0) boot_ask(p.ws, bt);
          if ((i < 2 || i == 4 || i == 8) && (c & 14) != 6 && !(c & 1) && lane < 19) {
            const int x = i < 2 ? i : 1 + (i >> 2), n = min((((c & 15) >> 1) - ((c & 8) >> 3)) * 19 + lane, 127);
            touch_line((const char*)p.Wo + (size_t)(w * 512 + (x >> 1) * 256 + (x & 1) * 64 + (n & 64) + n) * (DM * 2) + (c & 16) * 64 + 384, smem + w * 256);
          }
          const int x = i >> 2;
          blk_dma(Wo + (size_t)((x >> 1) * 256 + (x & 1) * 64 + ((i >> 1) & 1) * 128 + (i & 1) * 32) * DM, DM, slot, lane);
        },
        [&](int i, char* slot) {
#pragma unroll
          for (int t = 0; t < 2; ++t) {
            s16x4 lo = __builtin_amdgcn_ds_read_tr16_b64_v4i16((lds_s16x4_ptr)(slot + ro + t * 32));
            s16x4 hi = __builtin_amdgcn_ds_read_tr16_b64_v4i16((lds_s16x4_ptr)(slot + ro + t * 32 + 1024));
            put4(W[i >> 2], (i & 2) * 2 + 2 * t + (i & 1), (bf16x8){lo[0], lo[1], lo[2], lo[3], hi[0], hi[1], hi[2], hi[3]});
          }
        });
    if (w == 0) { lds_at(smem + 4128)[0] = boot_done(p.ws, bt); tag = next_tag(bt.e); }
    LBAR();
  }
  const unsigned pm = uni(lds_at(smem + 4128)[0]);
  p.s0 = pm >> 31;
  const int hs = side(pm, 1);
  {
    const u32x4 ds = {DSENT, DSENT, DSENT, DSENT};
#pragma unroll
    for (int i = 0; i < 2; ++i) {
      const int s = (w * 2 + i) * 4 + (lane >> 4);
      if (s >= 2) st_sc1(rsy, o_mem(pm, s) + (c >> 4) * 16384 + (c & 15) * 512 + rh * 128 + ((lane >> 3) & 1) * 256 + (lane & 7) * 16, ds);
    }
  }
  if (tid < 4) { reinterpret_cast<unsigned*>(smem + 4096)[tid] = tid == 3 ? now_q() - 0x100000u : 0; reinterpret_cast<unsigned*>(smem + 4112)[tid] = DSENT; }
  if (tid < 256) reinterpret_cast<int*>(smem + 5120)[tid] = ((tid >> 2) - TLAG - 6) >> 2;
  LBAR();
  const unsigned base = __builtin_amdgcn_readfirstlane(lds_at(smem + 4096)[3]);
  unsigned long long fm = 0, ps = 0;
  const unsigned aL = lane * 16;
  const int e = ((56 + w) * SEG + rh * 16 + (lane >> 2)) * DM + c * 32 + (lane & 3) * 8;
  u32x4 xv = *reinterpret_cast<const u32x4*>(p.x + e), av = {DSENT, DSENT, DSENT, DSENT};
  asm volatile("" : "+v"(xv));
  {
    const int r = lane & 15, q = lane >> 4;
    const int sm0 = o_mem(pm, 0) + (c >> 4) * 16384 + (c & 15) * 512 + rh * 128;
    u32x4 VO, VT;
    unsigned fo = SY_FO + ((rh + (lane & 4) / 2) * NH + 4 * hg + (lane & 3)) * 128;
    unsigned ao = w == 1 ? (c * S_LEN + rh * 16 - (TLAG + 3) * SEG) * 64 + lane * 16
                         : (w >= 4 ? SY_O + NPRE * 131072 + side(pm, w - 2) + (j >> 3) * 16384 + (j & 7) * 1024 + lane * 16
                                   : SY_FS + (2 * c + rh) * 128 + (lane < 8 ? lane * 16 : 0x70000000));
    unsigned ar = w == 1 ? R_SI - (TLAG + 2) * 1024 + lane * 16 : (w == 2 ? 5120 : 4112);
    const unsigned long long am = w == 1 ? ((1ull << 56) - 1) << (TLAG + 3) : (w == 2 ? 0x11111111111111ull << (TLAG + 7) : (w >= 4 ? (0xFFFFull << (16 * (w - 4))) >> NPRE : 0));
    unsigned pw = R_PART + w * 2048 + lane * 16, pr = R_PART + 16384 + tid * 4;
    unsigned sw = R_SI - (TLAG + 1) * 1024 + (lane >> 2) * 64 + w * 8 + (lane & 3) * 2, stm = 8192 + w * 512;
#pragma unroll
    for (int l = 0; l < 4; ++l) {
      const int x = l;
      VO[l] = o_o(0, 0, 4 * hg) + rh * 65536 + x * 1024 + aL;
      VT[l] = (T_O + rh * 16 + 2 * hg + (x >> 1)) * 16384 + (x & 1) * 2048 + aL;
    }
    unsigned ba = lane < 4 ? SY_BA + hs + (4 * hg + lane) * 128 : 0x7F000000u;
    unsigned sm = 0;
    int rl = RL0;
    unsigned step, E, so, st, st4, sh;
    asm volatile(RED_ASM
        : [step] "=&s"(step), [E] "=&s"(E), [so] "=&s"(so), [st] "=&s"(st), [st4] "=&s"(st4), [sh] "=&s"(sh),
          [rl] "+s"(rl), [sm] "+s"(sm), [fm] "+s"(fm), [ps] "+s"(ps),
          "+{v[224:227]}"(av), "+{v228}"(ao), "+{v229}"(ar), "+{v230}"(pw), "+{v231}"(pr), "+{v232}"(sw), "+{v233}"(stm), "+{v242}"(fo), "+{v173}"(ba)
        : W4_IN, "{v168}"(aL), "{v169}"(R4 ? (w >> 1) * 1024 + lane * 16 + (w & 1) * 8 : w * 512 + lane * 8), "{v170}"(lane * 8), "{v174}"((r & 1) ? 0x03020706u : 0x05040100u),
          "{v171}"(sm0 + 32768 + (q >> 1) * 256 + (q & 1) * 64 + ((r >> 1) & 1) * 32 + (r & 1) * 16 + (r >> 2) * 4),
          "{v172}"((lane & 2) ? 0xEEEE : 0x4444),
          "{v175}"(w == 3 && lane < 32 ? SY_BR + hs + j * 128 + lane * 4 : 0x7F000000u), "{v[234:237]}"(VO), "{v[238:241]}"(VT), "{v243}"(e * 2),
          [rg] "s"(raw_rsrc(p.ws + WS_G, S_LEN * DM * 2)), [fg] "s"(SY_FG + ((14 + (w >> 2)) * 8 + (c >> 2)) * 128),
          [rs] "s"(rs4), [ra] "s"(w == 1 ? raw_rsrc(p.ws + WS_S, S_LEN * DM * 2) : rs4), [rt] "s"(raw_rsrc(p.ws + WS_T, T_BYTES)),
          [ds] "n"(DS_RED), [wm] "n"(REF_W), [gm] "n"(REF_G), [mn] "n"(RED_MIN), [lk] "n"(DS_MEM + BA_LOOK), [up] "n"(RL_UP), [dn] "n"(-RL_DN), [sent] "s"(DSENT), [sab] "s"(SY_ABORT),
          [base] "s"(base), [pm] "s"(pm), [s0] "s"((p.s0 ^ TF_O) << 13), [am] "s"(am), [ainc] "s"(w == 1 ? SEG * 64 : (w >= 4 ? 131072 : 0)),
          [rinc] "s"(w == 1 ? 1024 : (w == 2 ? 16 : 0))
        : CLB32(128), CLB8(160, 161, 162, 163, 164, 165, 166, 167), CLB8(176, 177, 178, 179, 180, 181, 182, 183),
          CLB8(184, 185, 186, 187, 188, 189, 190, 191), CLB8(192, 193, 194, 195, 196, 197, 198, 199), CLB8(200, 201, 202, 203, 204, 205, 206, 207),
          CLB8(208, 209, 210, 211, 212, 213, 214, 215), CLB8(216, 217, 218, 219, 220, 221, 222, 223), CLB_E, "s96", CLB_S);
  }
  LBAR();
  u32x4 sv;
  if (w < 6 - TLAG) sv = *reinterpret_cast<const u32x4*>(smem + R_SI + (56 + w) * 1024 + lane * 16);
  else {
    const f32x4* part = reinterpret_cast<const f32x4*>(smem + (w == 6 - TLAG ? R_PART : (w - 6 + TLAG) * 16384)) +
                        (lane & 2) * 32 + (lane & 1) * 32 + (lane >> 2);
    f32x4 x[8][2];
#pragma unroll
    for (int ww = 0; ww < 8; ++ww) { x[ww][0] = part[ww * 128]; x[ww][1] = part[ww * 128 + 16]; }
#pragma unroll
    for (int u = 0; u < 2; ++u) {
      const f32x4 a = ((x[0][u] + x[1][u]) + (x[2][u] + x[3][u])) + ((x[4][u] + x[5][u]) + (x[6][u] + x[7][u]));
      sv[2 * u] = cvtpk(a[0], a[1]); sv[2 * u + 1] = cvtpk(a[2], a[3]);
    }
  }
  st_out(p, e, combine(sv, av, xv));
  if (j == 0 && tid == 0) st2_sc1(rsy, SY_EP, tag);
}


__global__ __launch_bounds__(512, 2) void main_kernel(Args p) {
  char* smem = g_smem;
  p.ws = ws_align(p.ws);
  const int bx = blockIdx.x & 7, bk = blockIdx.x >> 3;
  const bool isg = (bx ^ XPROBE) & 4;
  const int b = (isg ? NCHAIN : 0) + bk * 4 + (bx & 3);
  if (threadIdx.x < 64) {
    unsigned long long pc; unsigned len, keep;
    asm volatile("s_getpc_b64 %0\n.Lpc_%=:\n\ts_mov_b32 %1, .Lcode_end-.Lpc_%=" : "=s"(pc), "=s"(len));
    asm volatile("s_mov_b32 %0, m0\n\ts_mov_b32 m0, %3\n\ts_nop 0\n\tbuffer_load_dword %1, %2, 0 offen lds\n\ts_mov_b32 m0, %0"
                 : "=&s"(keep) : "v"((bk * 40 + threadIdx.x) * 128), "s"(raw_rsrc((const void*)pc, len)), "s"(b >= 32 && b < 128 ? 2048 : 163328) : "memory");
  }
  if ((isg ? bk >= 24 : (bk & 17) == 17) && (threadIdx.x >> 6) == 1) {
    const int bq = isg ? bk : bk >> 1;
    constexpr int NI = TLB_NI + 1, NW = (int)(WS_END / TLB_WS);
    const int n = (threadIdx.x & 63) * 8 + (bq & 7), m = n / NI, i = n % NI;
    const u16* q = m == 0 ? p.Wq : m == 1 ? p.Wk : m == 2 ? p.Wv : m == 3 ? p.Wo : m == 4 ? p.wg : m == 5 ? p.wu : m == 6 ? p.wd : m == 7 ? p.x : p.out;
    static_assert(9 * NI + NW <= 512, "");
    if (n < 9 * NI + NW)
      touch_line(m < 9 ? (const char*)q + min(i * (DM * HD * 2 / TLB_NI), (m < 4 ? HD : m < 7 ? FF : S_LEN) * DM * 2 - 128)
                       : p.ws + (size_t)(n - 9 * NI + 1) * TLB_WS - 16384, smem + 163072);
  }
  if (b >= NCHAIN) gemm_role(p, b - NCHAIN, smem);
  else if (b < 32) qk_role(p, (b & 3) * 8 + bk, smem);
  else if (b < 64) vu_role(p, (b & 3) * 8 + bk - 8, smem);
  else red_role(p, b - 64, smem);
}

extern "C" void kernel_launch(void* const* d_in, const int* in_sizes, int n_in,
                              void* d_out, int out_size, void* d_ws, size_t ws_size,
                              hipStream_t stream) {
  Args a;
  a.x = (const u16*)d_in[0]; a.mem0 = (const u16*)d_in[1];
  a.Wq = (const u16*)d_in[2]; a.Wk = (const u16*)d_in[3]; a.Wv = (const u16*)d_in[4]; a.Wo = (const u16*)d_in[5];
  a.wg = (const u16*)d_in[8]; a.wu = (const u16*)d_in[9]; a.wd = (const u16*)d_in[10];
  a.ws = (char*)d_ws; a.out = (u16*)d_out; a.s0 = 0; a.pad1 = 0;
  main_kernel<<<256, 512, 0, stream>>>(a);
}
```
